```python
import torch

def _softmax(x, axis=-1):
    return torch.softmax(x, dim=axis)
def _gelu(x):
    return torch.nn.functional.gelu(x, approximate="tanh")
def _silu(x):
    return torch.nn.functional.silu(x)
def _sigmoid(x):
    return torch.sigmoid(x)
def _layernorm(x, axis=-1, eps=1e-5):
    m = x.mean(dim=axis, keepdim=True)
    v = x.var(dim=axis, keepdim=True, unbiased=False)
    return (x - m) / torch.sqrt(v + eps)
def _scatter_add(tgt, idx, src, axis=0):
    return tgt.clone().index_add_(axis, idx, src)
def _segment_sum(src, seg, n):
    out = torch.zeros((n,) + src.shape[1:], dtype=src.dtype)
    return out.index_add_(0, seg, src)
def _pad(x, pw):
    flat = tuple(v for pair in reversed(pw) for v in pair)
    return torch.nn.functional.pad(x, flat)

kernel_name = 'standalone__gqa_block_cached__near_ridge'

def setup_inputs():

    _w = (lambda a: (lambda t: t.to(torch.bfloat16) if t.is_floating_point() else t)(torch.as_tensor(a)))
    B, Sq, D = 16, 240, 640
    Hq, Hkv, Sk, Dh = 32, 32, 1024, 128
    assert Hq % Hkv == 0, (Hq, Hkv)
    return {
        "x": _w((lambda *s: torch.randn(*s, dtype=torch.bfloat16) * 0.8)(B, Sq, D)),
        "k_cache": _w((lambda *s: torch.randn(*s, dtype=torch.bfloat16) * 0.8)(B, Hkv, Sk, Dh)),
        "v_cache": _w((lambda *s: torch.randn(*s, dtype=torch.bfloat16) * 0.05)(B, Hkv, Sk, Dh)),
        "wq": _w((lambda *s: torch.randn(*s, dtype=torch.bfloat16) * 0.8)(D, Hq * Dh) * (D ** -0.5)),
        "wo": _w((lambda *s: torch.randn(*s, dtype=torch.bfloat16) * 0.05)(Hq * Dh, D) * ((Hq * Dh) ** -0.5)),
    }

def reference(x, k_cache, v_cache, wq, wo):
    B, Sq, D = x.shape
    Hq, Dh = 32, 128
    Hkv = k_cache.shape[1]
    rep = Hq // Hkv
    q = (lambda x, *d: x.permute(*d))((x @ wq).reshape(B, Sq, Hq, Dh), 0, 2, 1, 3)
    k = (lambda x, n, axis: torch.repeat_interleave(x, n, dim=axis))(k_cache, rep, axis=1)
    v = (lambda x, n, axis: torch.repeat_interleave(x, n, dim=axis))(v_cache, rep, axis=1)
    scores = torch.einsum("bhqd,bhkd->bhqk", q, k) * (Dh ** -0.5)
    p = _softmax(scores, axis=-1)
    o = torch.einsum("bhqk,bhkd->bhqd", p, v)
    return (lambda x, *d: x.permute(*d))(o, 0, 2, 1, 3).reshape(B, Sq, Hq * Dh) @ wo
```

```cpp
#include <hip/hip_runtime.h>
#include <hip/hip_bf16.h>

constexpr int LDS_ATT = 163840;

__global__ __launch_bounds__(256, 1) __attribute__((aligned(4096))) void qattn_kernel(const void* __restrict__ x, const void* __restrict__ kc, const void* __restrict__ vc,
                                                       const void* __restrict__ wq, void* __restrict__ ws, const void* __restrict__ wo,
                                                       void* __restrict__ out) {
  const unsigned blk = blockIdx.x;
  const unsigned wid = __builtin_amdgcn_readfirstlane(threadIdx.x >> 6);
  auto ka = __builtin_amdgcn_kernarg_segment_ptr();
  asm volatile(
R"A(s_load_dwordx8 s[4:11],%0,0x0
s_load_dwordx4 s[12:15],%0,0x20
s_load_dwordx2 s[16:17],%0,0x30
v_mbcnt_lo_u32_b32 v23,-1,0
v_mbcnt_hi_u32_b32 v23,-1,v23
s_mov_b32 s60,%2
s_and_b32 s66,%1,7
s_lshr_b32 s67,%1,3
s_getpc_b64 s[20:21]
L_pc_%=:
s_and_b32 s20,s20,0xffffff80
s_and_b32 s21,s21,0xffff
s_mov_b32 s22,L_hot_%=-L_pc_%=
s_mov_b32 s23,0x20000
s_lshl_b32 s68,s67,2
s_add_u32 s68,s68,s60
s_mul_i32 s68,s68,896
v_lshlrev_b32 v67,7,v23
s_mov_b64 exec,127
buffer_load_dword v8,v67,s[20:23],s68 offen
s_mov_b64 exec,-1
s_lshr_b32 s61,s66,2
s_lshl_b32 s61,s61,3
s_lshr_b32 s68,s67,2
s_add_u32 s61,s61,s68
s_and_b32 s62,s66,1
s_lshl_b32 s62,s62,2
s_and_b32 s68,s66,2
s_add_u32 s62,s62,s68
s_lshl_b32 s62,s62,1
s_and_b32 s68,s67,3
s_add_u32 s62,s62,s68
s_lshl_b32 s83,s62,9
s_mov_b32 s84,0
s_lshl_b32 s89,s60,13
s_add_u32 s85,s89,65536
s_add_u32 s88,s89,131072
v_and_b32 v20,3,v23
v_bfe_u32 v21,v23,2,2
v_lshrrev_b32 v22,4,v23
v_lshrrev_b32 v75,3,v23
s_lshl_b32 s66,s60,6
v_add_u32 v75,s66,v75
v_mul_u32_u24 v75,1280,v75
v_and_b32 v76,7,v23
v_or_b32 v77,0,v22
v_xor_b32 v77,v77,v76
v_lshl_add_u32 v77,v77,4,v75
v_add_u32 v32,0,v77
v_add_u32 v34,18432,v77
v_add_u32 v36,40960,v77
v_add_u32 v38,59392,v77
v_or_b32 v77,4,v22
v_xor_b32 v77,v77,v76
v_lshl_add_u32 v77,v77,4,v75
v_add_u32 v33,9216,v77
v_add_u32 v35,27648,v77
v_add_u32 v37,50176,v77
v_add_u32 v39,68608,v77
s_lshl_b32 s66,s60,17
v_lshlrev_b32 v75,13,v21
v_lshl_add_u32 v75,v22,6,v75
v_lshl_add_u32 v75,v20,4,v75
v_add_u32 v75,s66,v75
v_add_u32 v40,0,v75
v_add_u32 v41,256,v75
v_add_u32 v42,32768,v75
v_add_u32 v43,33024,v75
v_add_u32 v44,65536,v75
v_add_u32 v45,65792,v75
v_add_u32 v46,98304,v75
v_add_u32 v47,98560,v75
s_mul_i32 s66,s61,307200
s_waitcnt lgkmcnt(0)
s_add_u32 s28,s4,s66
s_addc_u32 s29,s5,0
s_and_b32 s29,s29,0xffff
s_mov_b32 s30,307200
s_mov_b32 s31,0x20000
s_mov_b32 s36,s10
s_and_b32 s37,s11,0xffff
s_mov_b32 s38,5242880
s_mov_b32 s39,0x20000
s_add_u32 m0,s85,0
s_nop 0
buffer_load_dwordx4 v32,s[28:31],s84 offen lds
buffer_load_dwordx4 v33,s[28:31],s84 offen offset:1024 lds
buffer_load_dwordx4 v34,s[28:31],s84 offen offset:2048 lds
buffer_load_dwordx4 v35,s[28:31],s84 offen offset:3072 lds
s_add_u32 m0,s85,4096
s_nop 0
buffer_load_dwordx4 v36,s[28:31],s84 offen lds
buffer_load_dwordx4 v37,s[28:31],s84 offen offset:1024 lds
buffer_load_dwordx4 v38,s[28:31],s84 offen offset:2048 lds
buffer_load_dwordx4 v39,s[28:31],s84 offen offset:3072 lds
s_add_u32 s84,s84,128
s_xor_b32 s85,s85,98304
s_add_u32 m0,s88,0
s_nop 0
buffer_load_dwordx4 v40,s[36:39],s83 offen lds
s_add_u32 m0,s88,1024
s_nop 0
buffer_load_dwordx4 v41,s[36:39],s83 offen lds
s_add_u32 m0,s88,2048
s_nop 0
buffer_load_dwordx4 v42,s[36:39],s83 offen lds
s_add_u32 m0,s88,3072
s_nop 0
buffer_load_dwordx4 v43,s[36:39],s83 offen lds
s_add_u32 m0,s88,4096
s_nop 0
buffer_load_dwordx4 v44,s[36:39],s83 offen lds
s_add_u32 m0,s88,5120
s_nop 0
buffer_load_dwordx4 v45,s[36:39],s83 offen lds
s_add_u32 m0,s88,6144
s_nop 0
buffer_load_dwordx4 v46,s[36:39],s83 offen lds
s_add_u32 m0,s88,7168
s_nop 0
buffer_load_dwordx4 v47,s[36:39],s83 offen lds
s_add_u32 s83,s83,524288
s_lshl_b32 s66,s61,12
s_add_u32 s66,s66,104857600
s_add_u32 s44,s12,s66
s_addc_u32 s45,s13,0
s_and_b32 s45,s45,0xffff
s_mov_b32 s46,4096
s_mov_b32 s47,0x20000
v_mov_b32 v10,0
buffer_load_dword v9,v10,s[44:47],0 offen sc1
v_lshlrev_b32 v6,4,v23
v_lshlrev_b32 v72,12,v22
v_lshl_add_u32 v72,v21,6,v72
v_lshl_add_u32 v72,v20,4,v72
v_subrev_u32 v51,s89,v72
v_add_u32 v50,131072,v72
v_and_b32 v74,15,v23
v_lshrrev_b32 v76,1,v74
v_lshlrev_b32 v75,7,v74
s_add_u32 s66,s89,65536
v_add_u32 v75,s66,v75
v_or_b32 v77,0,v22
v_xor_b32 v77,v77,v76
v_lshl_add_u32 v48,v77,4,v75
v_or_b32 v77,4,v22
v_xor_b32 v77,v77,v76
v_lshl_add_u32 v49,v77,4,v75
s_add_u32 s86,s89,131072
s_add_u32 s87,s89,98304
s_mov_b64 exec,0
s_cmp_eq_u32 s60,0
s_cbranch_scc1 L_pd1_%=
s_cmp_eq_u32 s60,1
s_cbranch_scc1 L_pd2_%=
s_cmp_eq_u32 s60,2
s_cbranch_scc1 L_pd3_%=
s_branch L_pd4_%=
L_trt0_%=:
s_mov_b64 exec,-1
s_mov_b32 s88,s87
s_add_u32 m0,s88,0
s_nop 0
buffer_load_dwordx4 v40,s[36:39],s83 offen lds
s_add_u32 m0,s88,1024
s_nop 0
buffer_load_dwordx4 v41,s[36:39],s83 offen lds
s_add_u32 m0,s88,2048
s_nop 0
buffer_load_dwordx4 v42,s[36:39],s83 offen lds
s_add_u32 m0,s88,3072
s_nop 0
buffer_load_dwordx4 v43,s[36:39],s83 offen lds
s_add_u32 m0,s88,4096
s_nop 0
buffer_load_dwordx4 v44,s[36:39],s83 offen lds
s_add_u32 m0,s88,5120
s_nop 0
buffer_load_dwordx4 v45,s[36:39],s83 offen lds
s_add_u32 m0,s88,6144
s_nop 0
buffer_load_dwordx4 v46,s[36:39],s83 offen lds
s_add_u32 m0,s88,7168
s_nop 0
buffer_load_dwordx4 v47,s[36:39],s83 offen lds
s_add_u32 s83,s83,524288
s_add_u32 s88,s89,0
s_mov_b64 exec,0
s_cmp_eq_u32 s60,0
s_cbranch_scc1 L_pd9_%=
s_cmp_eq_u32 s60,1
s_cbranch_scc1 L_pd10_%=
s_cmp_eq_u32 s60,2
s_cbranch_scc1 L_pd11_%=
s_branch L_pd12_%=
L_t1b_%=:
s_cmp_ge_u32 s62,8
s_cbranch_scc1 L_pd20_%=
s_branch L_pd14_%=
L_t1c_%=:
s_cmp_ge_u32 s62,8
s_cbranch_scc1 L_pd21_%=
s_branch L_pd15_%=
s_nop 0
s_nop 0
s_nop 0
s_nop 0
s_nop 0
s_nop 0
L_trt1_%=:
s_mov_b64 exec,-1
s_add_u32 m0,s85,0
s_nop 0
buffer_load_dwordx4 v32,s[28:31],s84 offen lds
buffer_load_dwordx4 v33,s[28:31],s84 offen offset:1024 lds
buffer_load_dwordx4 v34,s[28:31],s84 offen offset:2048 lds
buffer_load_dwordx4 v35,s[28:31],s84 offen offset:3072 lds
s_add_u32 m0,s85,4096
s_nop 0
buffer_load_dwordx4 v36,s[28:31],s84 offen lds
buffer_load_dwordx4 v37,s[28:31],s84 offen offset:1024 lds
buffer_load_dwordx4 v38,s[28:31],s84 offen offset:2048 lds
buffer_load_dwordx4 v39,s[28:31],s84 offen offset:3072 lds
s_add_u32 s84,s84,128
s_xor_b32 s85,s85,98304
s_branch L_pacx_%=
L_pace_%=:
s_sub_u32 s5,s4,1
s_max_i32 s5,s5,0
L_pacl_%=:
s_nop 3
s_sub_u32 s4,s4,1
s_cmp_lg_u32 s4,0
s_cbranch_scc1 L_pacl_%=
s_setpc_b64 s[92:93]
L_pacx_%=:
s_waitcnt vmcnt(25)
ds_read_b128 a[192:195],v48
ds_read_b128 a[196:199],v49
ds_read_b128 a[200:203],v48 offset:2048
ds_read_b128 a[204:207],v49 offset:2048
ds_read_b128 a[208:211],v48 offset:4096
ds_read_b128 a[212:215],v49 offset:4096
ds_read_b128 a[216:219],v48 offset:6144
ds_read_b128 a[220:223],v49 offset:6144
s_waitcnt vmcnt(17)
s_barrier
ds_read_b64_tr_b16 a[64:65],v50
ds_read_b64_tr_b16 a[66:67],v50 offset:2048
ds_read_b64_tr_b16 a[68:69],v50 offset:16384
ds_read_b64_tr_b16 a[70:71],v50 offset:18432
ds_read_b64_tr_b16 a[72:73],v50 offset:8
ds_read_b64_tr_b16 a[74:75],v50 offset:2056
ds_read_b64_tr_b16 a[76:77],v50 offset:16392
ds_read_b64_tr_b16 a[78:79],v50 offset:18440
s_waitcnt lgkmcnt(6)
v_mfma_f32_16x16x32_bf16 a[0:3],a[64:67],a[192:195],0
s_add_u32 m0,s88,0
ds_read_b64_tr_b16 a[80:81],v50 offset:256
v_xor_b32 v48,98304,v48
s_waitcnt lgkmcnt(5)
v_mfma_f32_16x16x32_bf16 a[0:3],a[68:71],a[196:199],a[0:3]
s_mov_b32 s69,s86
buffer_load_dwordx4 v40,s[36:39],s83 offen lds
ds_read_b64_tr_b16 a[82:83],v50 offset:2304
v_mfma_f32_16x16x32_bf16 a[16:19],a[68:71],a[204:207],0
s_mov_b32 s86,s87
ds_read_b64_tr_b16 a[84:85],v50 offset:16640
v_xor_b32 v49,98304,v49
v_mfma_f32_16x16x32_bf16 a[16:19],a[64:67],a[200:203],a[16:19]
s_mov_b32 s87,s88
ds_read_b64_tr_b16 a[86:87],v50 offset:18688
v_mfma_f32_16x16x32_bf16 a[32:35],a[64:67],a[208:211],0
v_mfma_f32_16x16x32_bf16 a[32:35],a[68:71],a[212:215],a[32:35]
ds_read_b64_tr_b16 a[88:89],v50 offset:264
v_mfma_f32_16x16x32_bf16 a[48:51],a[68:71],a[220:223],0
s_add_u32 m0,s88,1024
ds_read_b64_tr_b16 a[90:91],v50 offset:2312
v_mfma_f32_16x16x32_bf16 a[48:51],a[64:67],a[216:219],a[48:51]
s_waitcnt lgkmcnt(6)
ds_read_b64_tr_b16 a[92:93],v50 offset:16648
v_mfma_f32_16x16x32_bf16 a[4:7],a[72:75],a[192:195],0
s_lshl_b32 s67,s60,12
buffer_load_dwordx4 v41,s[36:39],s83 offen lds
v_cmp_lt_u32 vcc,6,v23
v_mfma_f32_16x16x32_bf16 a[4:7],a[76:79],a[196:199],a[4:7]
s_lshl_b32 s65,s61,5
ds_read_b64_tr_b16 a[94:95],v50 offset:18696
v_lshlrev_b32 v19,3,v6
v_mfma_f32_16x16x32_bf16 a[20:23],a[76:79],a[204:207],0
s_lshl_b32 s66,s62,1
ds_read_b64_tr_b16 a[96:97],v50 offset:512
v_cndmask_b32_e64 v19,v19,-1,vcc
v_mfma_f32_16x16x32_bf16 a[20:23],a[72:75],a[200:203],a[20:23]
s_add_u32 s65,s65,s66
ds_read_b64_tr_b16 a[98:99],v50 offset:2560
v_lshrrev_b32 v18,1,v19
v_mfma_f32_16x16x32_bf16 a[36:39],a[72:75],a[208:211],0
s_lshl_b32 s66,s65,18
v_min_u32 v19,v19,v19
v_cndmask_b32_e64 v19,v19,v18,vcc
v_mfma_f32_16x16x32_bf16 a[36:39],a[76:79],a[212:215],a[36:39]
s_add_u32 m0,s88,2048
ds_read_b64_tr_b16 a[100:101],v50 offset:16896
v_lshlrev_b32 v11,8,v21
v_mfma_f32_16x16x32_bf16 a[52:55],a[76:79],a[220:223],0
s_lshr_b32 s63,s62,2
ds_read_b64_tr_b16 a[102:103],v50 offset:18944
v_lshl_add_u32 v11,v22,6,v11
v_mfma_f32_16x16x32_bf16 a[52:55],a[72:75],a[216:219],a[52:55]
s_mov_b32 s22,524288
buffer_load_dwordx4 v42,s[36:39],s83 offen lds
ds_read_b64_tr_b16 a[104:105],v50 offset:520
s_waitcnt lgkmcnt(6)
v_mfma_f32_16x16x32_bf16 a[8:11],a[80:83],a[192:195],0
s_mov_b32 s23,0x20000
v_add_u32 v11,s67,v11
v_lshl_add_u32 v4,v20,4,v11
v_mfma_f32_16x16x32_bf16 a[8:11],a[84:87],a[196:199],a[8:11]
s_mov_b32 s26,524288
ds_read_b64_tr_b16 a[106:107],v50 offset:2568
v_xor_b32 v12,2,v20
v_mfma_f32_16x16x32_bf16 a[24:27],a[84:87],a[204:207],0
s_mov_b32 s27,0x20000
ds_read_b64_tr_b16 a[108:109],v50 offset:16904
v_lshl_add_u32 v5,v12,4,v11
v_mfma_f32_16x16x32_bf16 a[24:27],a[80:83],a[200:203],a[24:27]
s_mov_b32 s42,131072
ds_read_b64_tr_b16 a[110:111],v50 offset:18952
v_lshlrev_b32 v11,10,v21
v_mfma_f32_16x16x32_bf16 a[40:43],a[80:83],a[208:211],0
s_add_u32 m0,s88,3072
v_lshl_add_u32 v11,v20,6,v11
v_and_b32 v12,2,v21
v_mfma_f32_16x16x32_bf16 a[40:43],a[84:87],a[212:215],a[40:43]
s_mov_b32 s43,0x20000
ds_read_b64_tr_b16 a[112:113],v50 offset:768
v_xor_b32 v12,v12,v22
v_mfma_f32_16x16x32_bf16 a[56:59],a[84:87],a[220:223],0
s_mov_b32 s48,s14
buffer_load_dwordx4 v43,s[36:39],s83 offen lds
ds_read_b64_tr_b16 a[114:115],v50 offset:2816
v_mfma_f32_16x16x32_bf16 a[56:59],a[80:83],a[216:219],a[56:59]
s_and_b32 s49,s15,0xffff
ds_read_b64_tr_b16 a[116:117],v50 offset:17152
v_lshl_add_u32 v11,v12,4,v11
v_mfma_f32_16x16x32_bf16 a[12:15],a[88:91],a[192:195],0
s_mov_b32 s50,5242880
v_add_u32 v1,98304,v11
v_lshlrev_b32 v11,10,v22
s_waitcnt lgkmcnt(6)
v_mfma_f32_16x16x32_bf16 a[12:15],a[92:95],a[196:199],a[12:15]
s_mov_b32 s51,0x20000
ds_read_b64_tr_b16 a[118:119],v50 offset:19200
v_lshl_add_u32 v11,v21,6,v11
v_mfma_f32_16x16x32_bf16 a[28:31],a[92:95],a[204:207],0
s_add_u32 s76,s67,98304
ds_read_b64_tr_b16 a[120:121],v50 offset:776
v_lshl_add_u32 v11,v20,3,v11
v_mfma_f32_16x16x32_bf16 a[28:31],a[88:91],a[200:203],a[28:31]
s_add_u32 m0,s88,4096
ds_read_b64_tr_b16 a[122:123],v50 offset:2824
v_add_u32 v11,114688,v11
v_mfma_f32_16x16x32_bf16 a[44:47],a[88:91],a[208:211],0
s_mul_i32 s74,s61,5
v_and_b32 v12,1,v22
v_lshl_add_u32 v2,v12,5,v11
v_mfma_f32_16x16x32_bf16 a[44:47],a[92:95],a[212:215],a[44:47]
s_add_u32 s74,s74,s62
buffer_load_dwordx4 v44,s[36:39],s83 offen lds
ds_read_b64_tr_b16 a[124:125],v50 offset:17160
v_mfma_f32_16x16x32_bf16 a[60:63],a[92:95],a[220:223],0
s_and_b32 s74,s74,15
ds_read_b64_tr_b16 a[126:127],v50 offset:19208
v_xor_b32 v12,1,v12
v_mfma_f32_16x16x32_bf16 a[60:63],a[88:91],a[216:219],a[60:63]
s_lshl_b32 s74,s74,14
ds_read_b64_tr_b16 a[64:65],v50 offset:1024
v_lshl_add_u32 v3,v12,5,v11
v_mfma_f32_16x16x32_bf16 v[64:67],a[96:99],a[192:195],0
s_mov_b32 s75,0
v_add_u32 v11,0,v23
v_mul_u32_u24 v12,3277,v11
v_mfma_f32_16x16x32_bf16 v[64:67],a[100:103],a[196:199],v[64:67]
s_mov_b32 s77,98304
ds_read_b64_tr_b16 a[66:67],v50 offset:3072
v_lshrrev_b32 v12,16,v12
v_mfma_f32_16x16x32_bf16 v[80:83],a[100:103],a[204:207],0
s_add_u32 m0,s88,5120
ds_read_b64_tr_b16 a[68:69],v50 offset:17408
v_mul_u32_u24 v13,20,v12
v_mfma_f32_16x16x32_bf16 v[80:83],a[96:99],a[200:203],v[80:83]
s_mov_b32 s71,0x3e0293ee
ds_read_b64_tr_b16 a[70:71],v50 offset:19456
v_sub_u32 v13,v11,v13
v_mfma_f32_16x16x32_bf16 v[96:99],a[96:99],a[208:211],0
s_mov_b32 s73,0x53800000
buffer_load_dwordx4 v45,s[36:39],s83 offen lds
v_bfe_u32 v14,v12,2,1
v_mfma_f32_16x16x32_bf16 v[96:99],a[100:103],a[212:215],v[96:99]
s_lshl_b32 s79,s60,14
ds_read_b64_tr_b16 a[72:73],v50 offset:1032
)A"
R"A(v_lshlrev_b32 v14,1,v14
v_mfma_f32_16x16x32_bf16 v[112:115],a[100:103],a[220:223],0
s_add_u32 s80,s79,4096
ds_read_b64_tr_b16 a[74:75],v50 offset:3080
v_xor_b32 v13,v13,v14
v_mfma_f32_16x16x32_bf16 v[112:115],a[96:99],a[216:219],v[112:115]
s_add_u32 s81,s79,8192
ds_read_b64_tr_b16 a[76:77],v50 offset:17416
v_lshrrev_b32 v14,2,v13
s_waitcnt lgkmcnt(6)
v_mfma_f32_16x16x32_bf16 v[68:71],a[104:107],a[192:195],0
s_add_u32 s20,s6,s66
s_addc_u32 s21,s7,0
v_cmp_eq_u32 vcc,3,v14
v_mfma_f32_16x16x32_bf16 v[68:71],a[108:111],a[196:199],v[68:71]
s_add_u32 m0,s88,6144
ds_read_b64_tr_b16 a[78:79],v50 offset:19464
v_min_u32 v15,3,v14
v_mfma_f32_16x16x32_bf16 v[84:87],a[108:111],a[204:207],0
s_add_u32 s24,s8,s66
s_addc_u32 s25,s9,0
ds_read_b64_tr_b16 a[80:81],v50 offset:1280
v_mfma_f32_16x16x32_bf16 v[84:87],a[104:107],a[200:203],v[84:87]
s_lshl_b32 s66,s65,16
buffer_load_dwordx4 v46,s[36:39],s83 offen lds
ds_read_b64_tr_b16 a[82:83],v50 offset:3328
v_mfma_f32_16x16x32_bf16 v[100:103],a[104:107],a[208:211],0
s_add_u32 s40,s12,s66
s_addc_u32 s41,s13,0
s_mul_i32 s66,s60,40960
v_mfma_f32_16x16x32_bf16 v[100:103],a[108:111],a[212:215],v[100:103]
s_add_u32 s65,s63,1
ds_read_b64_tr_b16 a[84:85],v50 offset:17664
v_add_u32 v15,s65,v15
v_mfma_f32_16x16x32_bf16 v[116:119],a[108:111],a[220:223],0
s_and_b32 s21,s21,0xffff
ds_read_b64_tr_b16 a[86:87],v50 offset:19712
v_and_b32 v15,3,v15
v_mfma_f32_16x16x32_bf16 v[116:119],a[104:107],a[216:219],v[116:119]
s_and_b32 s25,s25,0xffff
ds_read_b64_tr_b16 a[88:89],v50 offset:1288
v_cndmask_b32_e64 v15,v15,4,vcc
v_mfma_f32_16x16x32_bf16 v[72:75],a[112:115],a[192:195],0
s_add_u32 m0,s88,7168
v_and_b32 v13,3,v13
v_lshl_add_u32 v13,v15,2,v13
v_mfma_f32_16x16x32_bf16 v[72:75],a[116:119],a[196:199],v[72:75]
s_mov_b32 s88,s69
ds_read_b64_tr_b16 a[90:91],v50 offset:3336
v_mul_u32_u24 v12,1280,v12
v_mfma_f32_16x16x32_bf16 v[88:91],a[116:119],a[204:207],0
s_and_b32 s41,s41,0xffff
buffer_load_dwordx4 v47,s[36:39],s83 offen lds
ds_read_b64_tr_b16 a[92:93],v50 offset:17672
v_mfma_f32_16x16x32_bf16 v[88:91],a[112:115],a[200:203],v[88:91]
s_add_u32 s83,s83,524288
ds_read_b64_tr_b16 a[94:95],v50 offset:19720
v_lshl_add_u32 v12,v13,4,v12
v_mfma_f32_16x16x32_bf16 v[104:107],a[112:115],a[208:211],0
s_add_u32 s82,s79,12288
v_add_u32 v12,s66,v12
v_subrev_u32 v24,0,v12
v_mfma_f32_16x16x32_bf16 v[104:107],a[116:119],a[212:215],v[104:107]
s_and_b32 s64,s62,3
ds_read_b64_tr_b16 a[96:97],v50 offset:1536
v_add_u32 v11,64,v23
v_mfma_f32_16x16x32_bf16 v[120:123],a[116:119],a[220:223],0
s_mov_b32 s72,0xff800000
ds_read_b64_tr_b16 a[98:99],v50 offset:3584
v_mul_u32_u24 v12,3277,v11
v_mfma_f32_16x16x32_bf16 v[120:123],a[112:115],a[216:219],v[120:123]
s_add_u32 m0,s85,0
ds_read_b64_tr_b16 a[100:101],v50 offset:17920
v_lshrrev_b32 v12,16,v12
v_mfma_f32_16x16x32_bf16 v[76:79],a[120:123],a[192:195],0
s_lshl_b32 s18,s60,24
v_mul_u32_u24 v13,20,v12
v_sub_u32 v13,v11,v13
v_mfma_f32_16x16x32_bf16 v[76:79],a[124:127],a[196:199],v[76:79]
s_lshl_b32 s68,s62,6
buffer_load_dwordx4 v32,s[28:31],s84 offen lds
ds_read_b64_tr_b16 a[102:103],v50 offset:19968
v_mfma_f32_16x16x32_bf16 v[92:95],a[124:127],a[204:207],0
s_add_u32 s18,s18,s68
ds_read_b64_tr_b16 a[104:105],v50 offset:1544
v_bfe_u32 v14,v12,2,1
v_mfma_f32_16x16x32_bf16 v[92:95],a[120:123],a[200:203],v[92:95]
s_lshl_b32 s90,%1,2
ds_read_b64_tr_b16 a[106:107],v50 offset:3592
v_lshlrev_b32 v14,1,v14
v_mfma_f32_16x16x32_bf16 v[108:111],a[120:123],a[208:211],0
s_add_u32 s90,s90,s60
v_xor_b32 v13,v13,v14
v_lshrrev_b32 v14,2,v13
v_mfma_f32_16x16x32_bf16 v[108:111],a[124:127],a[212:215],v[108:111]
s_mul_i32 s90,s90,5120
ds_read_b64_tr_b16 a[108:109],v50 offset:17928
v_cmp_eq_u32 vcc,3,v14
v_mfma_f32_16x16x32_bf16 v[124:127],a[124:127],a[220:223],0
ds_read_b64_tr_b16 a[110:111],v50 offset:19976
v_min_u32 v15,3,v14
v_mfma_f32_16x16x32_bf16 v[124:127],a[120:123],a[216:219],v[124:127]
s_waitcnt lgkmcnt(5)
ds_read_b64_tr_b16 a[112:113],v50 offset:1792
v_add_u32 v15,s65,v15
v_mfma_f32_16x16x32_bf16 v[128:131],a[64:67],a[192:195],0
buffer_load_dwordx4 v33,s[28:31],s84 offen offset:1024 lds
v_and_b32 v15,3,v15
v_mfma_f32_16x16x32_bf16 v[128:131],a[68:71],a[196:199],v[128:131]
ds_read_b64_tr_b16 a[114:115],v50 offset:3840
v_cndmask_b32_e64 v15,v15,4,vcc
v_mfma_f32_16x16x32_bf16 v[160:163],a[68:71],a[204:207],0
ds_read_b64_tr_b16 a[116:117],v50 offset:18176
v_and_b32 v13,3,v13
v_mfma_f32_16x16x32_bf16 v[160:163],a[64:67],a[200:203],v[160:163]
ds_read_b64_tr_b16 a[118:119],v50 offset:20224
v_lshl_add_u32 v13,v15,2,v13
v_mfma_f32_16x16x32_bf16 v[192:195],a[64:67],a[208:211],0
v_mul_u32_u24 v12,1280,v12
v_lshl_add_u32 v12,v13,4,v12
v_mfma_f32_16x16x32_bf16 v[192:195],a[68:71],a[212:215],v[192:195]
ds_read_b64_tr_b16 a[120:121],v50 offset:1800
v_add_u32 v12,s66,v12
v_mfma_f32_16x16x32_bf16 v[224:227],a[68:71],a[220:223],0
ds_read_b64_tr_b16 a[122:123],v50 offset:3848
v_subrev_u32 v25,1024,v12
v_mfma_f32_16x16x32_bf16 v[224:227],a[64:67],a[216:219],v[224:227]
buffer_load_dwordx4 v34,s[28:31],s84 offen offset:2048 lds
ds_read_b64_tr_b16 a[124:125],v50 offset:18184
v_mfma_f32_16x16x32_bf16 v[132:135],a[72:75],a[192:195],0
v_add_u32 v11,128,v23
v_mul_u32_u24 v12,3277,v11
v_mfma_f32_16x16x32_bf16 v[132:135],a[76:79],a[196:199],v[132:135]
ds_read_b64_tr_b16 a[126:127],v50 offset:20232
v_add_u32 v50,s86,v51
v_mfma_f32_16x16x32_bf16 v[164:167],a[76:79],a[204:207],0
v_lshrrev_b32 v12,16,v12
v_mul_u32_u24 v13,20,v12
v_mfma_f32_16x16x32_bf16 v[164:167],a[72:75],a[200:203],v[164:167]
v_sub_u32 v13,v11,v13
v_bfe_u32 v14,v12,2,1
v_mfma_f32_16x16x32_bf16 v[196:199],a[72:75],a[208:211],0
v_lshlrev_b32 v14,1,v14
v_xor_b32 v13,v13,v14
v_mfma_f32_16x16x32_bf16 v[196:199],a[76:79],a[212:215],v[196:199]
v_lshrrev_b32 v14,2,v13
v_cmp_eq_u32 vcc,3,v14
v_mfma_f32_16x16x32_bf16 v[228:231],a[76:79],a[220:223],0
buffer_load_dwordx4 v35,s[28:31],s84 offen offset:3072 lds
v_min_u32 v15,3,v14
v_mfma_f32_16x16x32_bf16 v[228:231],a[72:75],a[216:219],v[228:231]
v_add_u32 v15,s65,v15
v_and_b32 v15,3,v15
v_mfma_f32_16x16x32_bf16 v[136:139],a[80:83],a[192:195],0
v_cndmask_b32_e64 v15,v15,4,vcc
v_and_b32 v13,3,v13
v_mfma_f32_16x16x32_bf16 v[136:139],a[84:87],a[196:199],v[136:139]
v_lshl_add_u32 v13,v15,2,v13
v_mul_u32_u24 v12,1280,v12
v_mfma_f32_16x16x32_bf16 v[168:171],a[84:87],a[204:207],0
v_lshl_add_u32 v12,v13,4,v12
v_add_u32 v12,s66,v12
v_mfma_f32_16x16x32_bf16 v[168:171],a[80:83],a[200:203],v[168:171]
s_add_u32 m0,s85,4096
s_xor_b32 s85,s85,98304
v_subrev_u32 v26,2048,v12
v_mfma_f32_16x16x32_bf16 v[200:203],a[80:83],a[208:211],0
v_add_u32 v11,192,v23
v_mul_u32_u24 v12,3277,v11
v_mfma_f32_16x16x32_bf16 v[200:203],a[84:87],a[212:215],v[200:203]
buffer_load_dwordx4 v36,s[28:31],s84 offen lds
v_lshrrev_b32 v12,16,v12
v_mfma_f32_16x16x32_bf16 v[232:235],a[84:87],a[220:223],0
v_mul_u32_u24 v13,20,v12
v_sub_u32 v13,v11,v13
v_mfma_f32_16x16x32_bf16 v[232:235],a[80:83],a[216:219],v[232:235]
v_bfe_u32 v14,v12,2,1
v_lshlrev_b32 v14,1,v14
v_mfma_f32_16x16x32_bf16 v[140:143],a[88:91],a[192:195],0
v_xor_b32 v13,v13,v14
v_lshrrev_b32 v14,2,v13
v_mfma_f32_16x16x32_bf16 v[140:143],a[92:95],a[196:199],v[140:143]
v_cmp_eq_u32 vcc,3,v14
v_min_u32 v15,3,v14
v_mfma_f32_16x16x32_bf16 v[172:175],a[92:95],a[204:207],0
v_add_u32 v15,s65,v15
v_and_b32 v15,3,v15
v_mfma_f32_16x16x32_bf16 v[172:175],a[88:91],a[200:203],v[172:175]
v_cndmask_b32_e64 v15,v15,4,vcc
v_and_b32 v13,3,v13
v_mfma_f32_16x16x32_bf16 v[204:207],a[88:91],a[208:211],0
buffer_load_dwordx4 v37,s[28:31],s84 offen offset:1024 lds
v_lshl_add_u32 v13,v15,2,v13
v_mfma_f32_16x16x32_bf16 v[204:207],a[92:95],a[212:215],v[204:207]
v_mul_u32_u24 v12,1280,v12
v_lshl_add_u32 v12,v13,4,v12
v_mfma_f32_16x16x32_bf16 v[236:239],a[92:95],a[220:223],0
v_add_u32 v12,s66,v12
v_subrev_u32 v27,3072,v12
v_mfma_f32_16x16x32_bf16 v[236:239],a[88:91],a[216:219],v[236:239]
v_add_u32 v11,256,v23
v_mul_u32_u24 v12,3277,v11
s_waitcnt vmcnt(22) lgkmcnt(0)
s_barrier
v_mfma_f32_16x16x32_bf16 v[144:147],a[96:99],a[192:195],0
s_waitcnt vmcnt(14)
ds_read_b128 a[224:227],v48
v_lshrrev_b32 v12,16,v12
v_mfma_f32_16x16x32_bf16 v[144:147],a[100:103],a[196:199],v[144:147]
ds_read_b128 a[228:231],v49
v_mul_u32_u24 v13,20,v12
v_mfma_f32_16x16x32_bf16 v[176:179],a[100:103],a[204:207],0
buffer_load_dwordx4 v38,s[28:31],s84 offen offset:2048 lds
ds_read_b128 a[232:235],v48 offset:2048
v_mfma_f32_16x16x32_bf16 v[176:179],a[96:99],a[200:203],v[176:179]
ds_read_b128 a[236:239],v49 offset:2048
v_sub_u32 v13,v11,v13
v_mfma_f32_16x16x32_bf16 v[208:211],a[96:99],a[208:211],0
v_bfe_u32 v14,v12,2,1
v_lshlrev_b32 v14,1,v14
v_mfma_f32_16x16x32_bf16 v[208:211],a[100:103],a[212:215],v[208:211]
ds_read_b128 a[240:243],v48 offset:4096
v_xor_b32 v13,v13,v14
v_mfma_f32_16x16x32_bf16 v[240:243],a[100:103],a[220:223],0
ds_read_b128 a[244:247],v49 offset:4096
v_lshrrev_b32 v14,2,v13
v_mfma_f32_16x16x32_bf16 v[240:243],a[96:99],a[216:219],v[240:243]
ds_read_b128 a[248:251],v48 offset:6144
v_cmp_eq_u32 vcc,3,v14
v_mfma_f32_16x16x32_bf16 v[148:151],a[104:107],a[192:195],0
s_cmp_eq_u32 s60,3
s_cselect_b32 s72,0x7149f2ca,s72
v_readfirstlane_b32 s52,v9
v_mfma_f32_16x16x32_bf16 v[148:151],a[108:111],a[196:199],v[148:151]
s_add_u32 s52,s52,1
buffer_load_dwordx4 v39,s[28:31],s84 offen offset:3072 lds
ds_read_b128 a[252:255],v49 offset:6144
s_nop 0
L_pd1_%=:
s_cbranch_execz L_pd5_%=
v_mfma_f32_16x16x32_bf16 v[180:183],a[108:111],a[204:207],0
s_add_u32 s84,s84,128
s_xor_b32 s53,s52,0x9e3779b9
ds_read_b64_tr_b16 a[64:65],v50
v_mfma_f32_16x16x32_bf16 v[180:183],a[104:107],a[200:203],v[180:183]
ds_read_b64_tr_b16 a[66:67],v50 offset:2048
v_mov_b32 v28,0x3f803f80
v_mfma_f32_16x16x32_bf16 v[212:215],a[104:107],a[208:211],0
v_mov_b32 v29,0x3f803f80
v_mov_b32 v30,0x3f803f80
v_mfma_f32_16x16x32_bf16 v[212:215],a[108:111],a[212:215],v[212:215]
ds_read_b64_tr_b16 a[68:69],v50 offset:16384
v_mov_b32 v31,0x3f803f80
v_mfma_f32_16x16x32_bf16 v[244:247],a[108:111],a[220:223],0
ds_read_b64_tr_b16 a[70:71],v50 offset:18432
v_min_u32 v15,3,v14
v_mfma_f32_16x16x32_bf16 v[244:247],a[104:107],a[216:219],v[244:247]
ds_read_b64_tr_b16 a[72:73],v50 offset:8
v_add_u32 v15,s65,v15
v_mfma_f32_16x16x32_bf16 v[152:155],a[112:115],a[192:195],0
v_and_b32 v15,3,v15
v_cndmask_b32_e64 v15,v15,4,vcc
v_mfma_f32_16x16x32_bf16 v[152:155],a[116:119],a[196:199],v[152:155]
ds_read_b64_tr_b16 a[74:75],v50 offset:2056
v_and_b32 v13,3,v13
v_mfma_f32_16x16x32_bf16 v[184:187],a[116:119],a[204:207],0
ds_read_b64_tr_b16 a[76:77],v50 offset:16392
v_lshl_add_u32 v13,v15,2,v13
v_mfma_f32_16x16x32_bf16 v[184:187],a[112:115],a[200:203],v[184:187]
ds_read_b64_tr_b16 a[78:79],v50 offset:18440
v_mul_u32_u24 v12,1280,v12
v_mfma_f32_16x16x32_bf16 v[216:219],a[112:115],a[208:211],0
v_lshl_add_u32 v12,v13,4,v12
v_add_u32 v12,s66,v12
v_mfma_f32_16x16x32_bf16 v[216:219],a[116:119],a[212:215],v[216:219]
ds_read_b64_tr_b16 a[80:81],v50 offset:256
v_subrev_u32 v7,0,v12
v_mfma_f32_16x16x32_bf16 v[248:251],a[116:119],a[220:223],0
ds_read_b64_tr_b16 a[82:83],v50 offset:2304
v_mfma_f32_16x16x32_bf16 v[248:251],a[112:115],a[216:219],v[248:251]
ds_read_b64_tr_b16 a[84:85],v50 offset:16640
v_mfma_f32_16x16x32_bf16 v[156:159],a[120:123],a[192:195],0
v_mfma_f32_16x16x32_bf16 v[156:159],a[124:127],a[196:199],v[156:159]
ds_read_b64_tr_b16 a[86:87],v50 offset:18688
v_mfma_f32_16x16x32_bf16 v[188:191],a[124:127],a[204:207],0
ds_read_b64_tr_b16 a[88:89],v50 offset:264
v_mfma_f32_16x16x32_bf16 v[188:191],a[120:123],a[200:203],v[188:191]
ds_read_b64_tr_b16 a[90:91],v50 offset:2312
v_mfma_f32_16x16x32_bf16 v[220:223],a[120:123],a[208:211],0
v_mfma_f32_16x16x32_bf16 v[220:223],a[124:127],a[212:215],v[220:223]
ds_read_b64_tr_b16 a[92:93],v50 offset:16648
v_mfma_f32_16x16x32_bf16 v[252:255],a[124:127],a[220:223],0
ds_read_b64_tr_b16 a[94:95],v50 offset:18696
v_mfma_f32_16x16x32_bf16 v[252:255],a[120:123],a[216:219],v[252:255]
s_mov_b32 s70,3
L_qp_%=:
s_waitcnt lgkmcnt(5)
v_mfma_f32_16x16x32_bf16 a[0:3],a[64:67],a[224:227],a[0:3]
s_add_u32 m0,s88,0
ds_read_b64_tr_b16 a[96:97],v50 offset:512
v_xor_b32 v48,98304,v48
v_mfma_f32_16x16x32_bf16 a[0:3],a[68:71],a[228:231],a[0:3]
)A"
R"A(s_mov_b32 s69,s86
buffer_load_dwordx4 v40,s[36:39],s83 offen lds
ds_read_b64_tr_b16 a[98:99],v50 offset:2560
v_mfma_f32_16x16x32_bf16 a[16:19],a[68:71],a[236:239],a[16:19]
s_mov_b32 s86,s87
ds_read_b64_tr_b16 a[100:101],v50 offset:16896
v_xor_b32 v49,98304,v49
v_mfma_f32_16x16x32_bf16 a[16:19],a[64:67],a[232:235],a[16:19]
s_mov_b32 s87,s88
ds_read_b64_tr_b16 a[102:103],v50 offset:18944
v_mfma_f32_16x16x32_bf16 a[32:35],a[64:67],a[240:243],a[32:35]
s_cmp_eq_u32 s70,1
s_cselect_b32 s65,256,0
s_cselect_b32 s66,524288,0
v_mfma_f32_16x16x32_bf16 a[32:35],a[68:71],a[244:247],a[32:35]
ds_read_b64_tr_b16 a[104:105],v50 offset:520
v_add_u32 v40,s65,v40
v_mfma_f32_16x16x32_bf16 a[48:51],a[68:71],a[252:255],a[48:51]
s_add_u32 m0,s88,1024
ds_read_b64_tr_b16 a[106:107],v50 offset:2568
v_mfma_f32_16x16x32_bf16 a[48:51],a[64:67],a[248:251],a[48:51]
ds_read_b64_tr_b16 a[108:109],v50 offset:16904
v_mfma_f32_16x16x32_bf16 a[4:7],a[72:75],a[224:227],a[4:7]
buffer_load_dwordx4 v41,s[36:39],s83 offen lds
v_add_u32 v41,s66,v41
v_mfma_f32_16x16x32_bf16 a[4:7],a[76:79],a[228:231],a[4:7]
ds_read_b64_tr_b16 a[110:111],v50 offset:18952
v_mfma_f32_16x16x32_bf16 a[20:23],a[76:79],a[236:239],a[20:23]
ds_read_b64_tr_b16 a[112:113],v50 offset:768
v_mfma_f32_16x16x32_bf16 a[20:23],a[72:75],a[232:235],a[20:23]
ds_read_b64_tr_b16 a[114:115],v50 offset:2816
v_mfma_f32_16x16x32_bf16 a[36:39],a[72:75],a[240:243],a[36:39]
v_mfma_f32_16x16x32_bf16 a[36:39],a[76:79],a[244:247],a[36:39]
s_add_u32 m0,s88,2048
ds_read_b64_tr_b16 a[116:117],v50 offset:17152
v_mfma_f32_16x16x32_bf16 a[52:55],a[76:79],a[252:255],a[52:55]
ds_read_b64_tr_b16 a[118:119],v50 offset:19200
v_mfma_f32_16x16x32_bf16 a[52:55],a[72:75],a[248:251],a[52:55]
buffer_load_dwordx4 v42,s[36:39],s83 offen lds
ds_read_b64_tr_b16 a[120:121],v50 offset:776
v_mfma_f32_16x16x32_bf16 a[8:11],a[80:83],a[224:227],a[8:11]
s_waitcnt lgkmcnt(6)
v_add_u32 v42,s65,v42
v_mfma_f32_16x16x32_bf16 a[8:11],a[84:87],a[228:231],a[8:11]
ds_read_b64_tr_b16 a[122:123],v50 offset:2824
v_mfma_f32_16x16x32_bf16 a[24:27],a[84:87],a[236:239],a[24:27]
ds_read_b64_tr_b16 a[124:125],v50 offset:17160
v_mfma_f32_16x16x32_bf16 a[24:27],a[80:83],a[232:235],a[24:27]
ds_read_b64_tr_b16 a[126:127],v50 offset:19208
v_mfma_f32_16x16x32_bf16 a[40:43],a[80:83],a[240:243],a[40:43]
s_add_u32 m0,s88,3072
v_mfma_f32_16x16x32_bf16 a[40:43],a[84:87],a[244:247],a[40:43]
ds_read_b64_tr_b16 a[64:65],v50 offset:1024
v_mfma_f32_16x16x32_bf16 a[56:59],a[84:87],a[252:255],a[56:59]
buffer_load_dwordx4 v43,s[36:39],s83 offen lds
ds_read_b64_tr_b16 a[66:67],v50 offset:3072
v_mfma_f32_16x16x32_bf16 a[56:59],a[80:83],a[248:251],a[56:59]
ds_read_b64_tr_b16 a[68:69],v50 offset:17408
v_add_u32 v43,s66,v43
v_mfma_f32_16x16x32_bf16 a[12:15],a[88:91],a[224:227],a[12:15]
v_mfma_f32_16x16x32_bf16 a[12:15],a[92:95],a[228:231],a[12:15]
ds_read_b64_tr_b16 a[70:71],v50 offset:19456
v_mfma_f32_16x16x32_bf16 a[28:31],a[92:95],a[236:239],a[28:31]
ds_read_b64_tr_b16 a[72:73],v50 offset:1032
v_mfma_f32_16x16x32_bf16 a[28:31],a[88:91],a[232:235],a[28:31]
s_add_u32 m0,s88,4096
ds_read_b64_tr_b16 a[74:75],v50 offset:3080
v_mfma_f32_16x16x32_bf16 a[44:47],a[88:91],a[240:243],a[44:47]
v_mfma_f32_16x16x32_bf16 a[44:47],a[92:95],a[244:247],a[44:47]
buffer_load_dwordx4 v44,s[36:39],s83 offen lds
ds_read_b64_tr_b16 a[76:77],v50 offset:17416
v_mfma_f32_16x16x32_bf16 a[60:63],a[92:95],a[252:255],a[60:63]
ds_read_b64_tr_b16 a[78:79],v50 offset:19464
v_add_u32 v44,s65,v44
v_mfma_f32_16x16x32_bf16 a[60:63],a[88:91],a[248:251],a[60:63]
ds_read_b64_tr_b16 a[80:81],v50 offset:1280
v_mfma_f32_16x16x32_bf16 v[64:67],a[96:99],a[224:227],v[64:67]
v_mfma_f32_16x16x32_bf16 v[64:67],a[100:103],a[228:231],v[64:67]
ds_read_b64_tr_b16 a[82:83],v50 offset:3328
v_mfma_f32_16x16x32_bf16 v[80:83],a[100:103],a[236:239],v[80:83]
s_add_u32 m0,s88,5120
ds_read_b64_tr_b16 a[84:85],v50 offset:17664
v_mfma_f32_16x16x32_bf16 v[80:83],a[96:99],a[232:235],v[80:83]
ds_read_b64_tr_b16 a[86:87],v50 offset:19712
v_mfma_f32_16x16x32_bf16 v[96:99],a[96:99],a[240:243],v[96:99]
buffer_load_dwordx4 v45,s[36:39],s83 offen lds
v_add_u32 v45,s66,v45
v_mfma_f32_16x16x32_bf16 v[96:99],a[100:103],a[244:247],v[96:99]
ds_read_b64_tr_b16 a[88:89],v50 offset:1288
v_mfma_f32_16x16x32_bf16 v[112:115],a[100:103],a[252:255],v[112:115]
ds_read_b64_tr_b16 a[90:91],v50 offset:3336
v_mfma_f32_16x16x32_bf16 v[112:115],a[96:99],a[248:251],v[112:115]
ds_read_b64_tr_b16 a[92:93],v50 offset:17672
v_mfma_f32_16x16x32_bf16 v[68:71],a[104:107],a[224:227],v[68:71]
s_waitcnt lgkmcnt(6)
v_mfma_f32_16x16x32_bf16 v[68:71],a[108:111],a[228:231],v[68:71]
s_add_u32 m0,s88,6144
ds_read_b64_tr_b16 a[94:95],v50 offset:19720
v_mfma_f32_16x16x32_bf16 v[84:87],a[108:111],a[236:239],v[84:87]
ds_read_b64_tr_b16 a[96:97],v50 offset:1536
v_mfma_f32_16x16x32_bf16 v[84:87],a[104:107],a[232:235],v[84:87]
buffer_load_dwordx4 v46,s[36:39],s83 offen lds
ds_read_b64_tr_b16 a[98:99],v50 offset:3584
v_mfma_f32_16x16x32_bf16 v[100:103],a[104:107],a[240:243],v[100:103]
v_add_u32 v46,s65,v46
v_mfma_f32_16x16x32_bf16 v[100:103],a[108:111],a[244:247],v[100:103]
ds_read_b64_tr_b16 a[100:101],v50 offset:17920
v_mfma_f32_16x16x32_bf16 v[116:119],a[108:111],a[252:255],v[116:119]
ds_read_b64_tr_b16 a[102:103],v50 offset:19968
v_mfma_f32_16x16x32_bf16 v[116:119],a[104:107],a[248:251],v[116:119]
ds_read_b64_tr_b16 a[104:105],v50 offset:1544
v_mfma_f32_16x16x32_bf16 v[72:75],a[112:115],a[224:227],v[72:75]
s_add_u32 m0,s88,7168
s_mov_b32 s88,s69
s_mov_b32 s69,s86
v_mfma_f32_16x16x32_bf16 v[72:75],a[116:119],a[228:231],v[72:75]
ds_read_b64_tr_b16 a[106:107],v50 offset:3592
v_mfma_f32_16x16x32_bf16 v[88:91],a[116:119],a[236:239],v[88:91]
buffer_load_dwordx4 v47,s[36:39],s83 offen lds
ds_read_b64_tr_b16 a[108:109],v50 offset:17928
v_mfma_f32_16x16x32_bf16 v[88:91],a[112:115],a[232:235],v[88:91]
s_add_u32 s83,s83,524288
ds_read_b64_tr_b16 a[110:111],v50 offset:19976
v_add_u32 v47,s66,v47
v_mfma_f32_16x16x32_bf16 v[104:107],a[112:115],a[240:243],v[104:107]
v_mfma_f32_16x16x32_bf16 v[104:107],a[116:119],a[244:247],v[104:107]
v_mfma_f32_16x16x32_bf16 v[120:123],a[116:119],a[252:255],v[120:123]
ds_read_b64_tr_b16 a[116:117],v50 offset:18176
v_mfma_f32_16x16x32_bf16 v[120:123],a[112:115],a[248:251],v[120:123]
s_add_u32 m0,s85,0
ds_read_b64_tr_b16 a[112:113],v50 offset:1792
v_mfma_f32_16x16x32_bf16 v[76:79],a[120:123],a[224:227],v[76:79]
ds_read_b64_tr_b16 a[114:115],v50 offset:3840
v_mfma_f32_16x16x32_bf16 v[76:79],a[124:127],a[228:231],v[76:79]
buffer_load_dwordx4 v32,s[28:31],s84 offen lds
ds_read_b64_tr_b16 a[118:119],v50 offset:20224
v_mfma_f32_16x16x32_bf16 v[92:95],a[124:127],a[236:239],v[92:95]
v_mfma_f32_16x16x32_bf16 v[92:95],a[120:123],a[232:235],v[92:95]
v_mfma_f32_16x16x32_bf16 v[108:111],a[120:123],a[240:243],v[108:111]
v_mfma_f32_16x16x32_bf16 v[108:111],a[124:127],a[244:247],v[108:111]
v_mfma_f32_16x16x32_bf16 v[124:127],a[124:127],a[252:255],v[124:127]
ds_read_b64_tr_b16 a[124:125],v50 offset:18184
v_mfma_f32_16x16x32_bf16 v[124:127],a[120:123],a[248:251],v[124:127]
ds_read_b64_tr_b16 a[120:121],v50 offset:1800
v_mfma_f32_16x16x32_bf16 v[128:131],a[64:67],a[224:227],v[128:131]
buffer_load_dwordx4 v33,s[28:31],s84 offen offset:1024 lds
ds_read_b64_tr_b16 a[122:123],v50 offset:3848
v_mfma_f32_16x16x32_bf16 v[128:131],a[68:71],a[228:231],v[128:131]
ds_read_b64_tr_b16 a[126:127],v50 offset:20232
v_add_u32 v50,s86,v51
v_mfma_f32_16x16x32_bf16 v[160:163],a[68:71],a[236:239],v[160:163]
s_mov_b32 s86,s87
s_mov_b32 s87,s88
v_mfma_f32_16x16x32_bf16 v[160:163],a[64:67],a[232:235],v[160:163]
v_mfma_f32_16x16x32_bf16 v[192:195],a[64:67],a[240:243],v[192:195]
v_mfma_f32_16x16x32_bf16 v[192:195],a[68:71],a[244:247],v[192:195]
v_mfma_f32_16x16x32_bf16 v[224:227],a[68:71],a[252:255],v[224:227]
v_mfma_f32_16x16x32_bf16 v[224:227],a[64:67],a[248:251],v[224:227]
buffer_load_dwordx4 v34,s[28:31],s84 offen offset:2048 lds
v_mfma_f32_16x16x32_bf16 v[132:135],a[72:75],a[224:227],v[132:135]
v_mfma_f32_16x16x32_bf16 v[132:135],a[76:79],a[228:231],v[132:135]
v_mfma_f32_16x16x32_bf16 v[164:167],a[76:79],a[236:239],v[164:167]
v_mfma_f32_16x16x32_bf16 v[164:167],a[72:75],a[232:235],v[164:167]
v_mfma_f32_16x16x32_bf16 v[196:199],a[72:75],a[240:243],v[196:199]
v_mfma_f32_16x16x32_bf16 v[196:199],a[76:79],a[244:247],v[196:199]
v_mfma_f32_16x16x32_bf16 v[228:231],a[76:79],a[252:255],v[228:231]
buffer_load_dwordx4 v35,s[28:31],s84 offen offset:3072 lds
v_mfma_f32_16x16x32_bf16 v[228:231],a[72:75],a[248:251],v[228:231]
s_waitcnt lgkmcnt(0)
v_mfma_f32_16x16x32_bf16 v[136:139],a[80:83],a[224:227],v[136:139]
v_mfma_f32_16x16x32_bf16 v[136:139],a[84:87],a[228:231],v[136:139]
v_mfma_f32_16x16x32_bf16 v[168:171],a[84:87],a[236:239],v[168:171]
v_mfma_f32_16x16x32_bf16 v[168:171],a[80:83],a[232:235],v[168:171]
s_add_u32 m0,s85,4096
s_xor_b32 s85,s85,98304
v_mfma_f32_16x16x32_bf16 v[200:203],a[80:83],a[240:243],v[200:203]
v_mfma_f32_16x16x32_bf16 v[200:203],a[84:87],a[244:247],v[200:203]
buffer_load_dwordx4 v36,s[28:31],s84 offen lds
v_mfma_f32_16x16x32_bf16 v[232:235],a[84:87],a[252:255],v[232:235]
v_mfma_f32_16x16x32_bf16 v[232:235],a[80:83],a[248:251],v[232:235]
v_mfma_f32_16x16x32_bf16 v[140:143],a[88:91],a[224:227],v[140:143]
v_mfma_f32_16x16x32_bf16 v[140:143],a[92:95],a[228:231],v[140:143]
v_mfma_f32_16x16x32_bf16 v[172:175],a[92:95],a[236:239],v[172:175]
v_mfma_f32_16x16x32_bf16 v[172:175],a[88:91],a[232:235],v[172:175]
v_mfma_f32_16x16x32_bf16 v[204:207],a[88:91],a[240:243],v[204:207]
buffer_load_dwordx4 v37,s[28:31],s84 offen offset:1024 lds
v_mfma_f32_16x16x32_bf16 v[204:207],a[92:95],a[244:247],v[204:207]
v_mfma_f32_16x16x32_bf16 v[236:239],a[92:95],a[252:255],v[236:239]
v_mfma_f32_16x16x32_bf16 v[236:239],a[88:91],a[248:251],v[236:239]
s_waitcnt vmcnt(22)
s_barrier
v_mfma_f32_16x16x32_bf16 v[144:147],a[96:99],a[224:227],v[144:147]
s_waitcnt vmcnt(14)
ds_read_b128 a[192:195],v48
v_mfma_f32_16x16x32_bf16 v[144:147],a[100:103],a[228:231],v[144:147]
ds_read_b128 a[196:199],v49
v_mfma_f32_16x16x32_bf16 v[176:179],a[100:103],a[236:239],v[176:179]
buffer_load_dwordx4 v38,s[28:31],s84 offen offset:2048 lds
ds_read_b128 a[200:203],v48 offset:2048
v_mfma_f32_16x16x32_bf16 v[176:179],a[96:99],a[232:235],v[176:179]
ds_read_b128 a[204:207],v49 offset:2048
v_mfma_f32_16x16x32_bf16 v[208:211],a[96:99],a[240:243],v[208:211]
v_mfma_f32_16x16x32_bf16 v[208:211],a[100:103],a[244:247],v[208:211]
ds_read_b128 a[208:211],v48 offset:4096
v_mfma_f32_16x16x32_bf16 v[240:243],a[100:103],a[252:255],v[240:243]
ds_read_b128 a[212:215],v49 offset:4096
v_mfma_f32_16x16x32_bf16 v[240:243],a[96:99],a[248:251],v[240:243]
ds_read_b128 a[216:219],v48 offset:6144
v_xor_b32 v48,98304,v48
v_mfma_f32_16x16x32_bf16 v[148:151],a[104:107],a[224:227],v[148:151]
v_mfma_f32_16x16x32_bf16 v[148:151],a[108:111],a[228:231],v[148:151]
buffer_load_dwordx4 v39,s[28:31],s84 offen offset:3072 lds
ds_read_b128 a[220:223],v49 offset:6144
v_mfma_f32_16x16x32_bf16 v[180:183],a[108:111],a[236:239],v[180:183]
s_add_u32 s84,s84,128
ds_read_b64_tr_b16 a[64:65],v50
v_xor_b32 v49,98304,v49
v_mfma_f32_16x16x32_bf16 v[180:183],a[104:107],a[232:235],v[180:183]
ds_read_b64_tr_b16 a[66:67],v50 offset:2048
v_mfma_f32_16x16x32_bf16 v[212:215],a[104:107],a[240:243],v[212:215]
v_mfma_f32_16x16x32_bf16 v[212:215],a[108:111],a[244:247],v[212:215]
ds_read_b64_tr_b16 a[68:69],v50 offset:16384
v_mfma_f32_16x16x32_bf16 v[244:247],a[108:111],a[252:255],v[244:247]
ds_read_b64_tr_b16 a[70:71],v50 offset:18432
v_mfma_f32_16x16x32_bf16 v[244:247],a[104:107],a[248:251],v[244:247]
ds_read_b64_tr_b16 a[72:73],v50 offset:8
v_mfma_f32_16x16x32_bf16 v[152:155],a[112:115],a[224:227],v[152:155]
buffer_load_dword v9,v19,s[48:51],s90 offen
v_mfma_f32_16x16x32_bf16 v[152:155],a[116:119],a[228:231],v[152:155]
s_add_u32 s90,s90,896
ds_read_b64_tr_b16 a[74:75],v50 offset:2056
v_mfma_f32_16x16x32_bf16 v[184:187],a[116:119],a[236:239],v[184:187]
ds_read_b64_tr_b16 a[76:77],v50 offset:16392
v_mfma_f32_16x16x32_bf16 v[184:187],a[112:115],a[232:235],v[184:187]
ds_read_b64_tr_b16 a[78:79],v50 offset:18440
)A"
R"A(v_mfma_f32_16x16x32_bf16 v[216:219],a[112:115],a[240:243],v[216:219]
v_mfma_f32_16x16x32_bf16 v[216:219],a[116:119],a[244:247],v[216:219]
ds_read_b64_tr_b16 a[80:81],v50 offset:256
v_mfma_f32_16x16x32_bf16 v[248:251],a[116:119],a[252:255],v[248:251]
ds_read_b64_tr_b16 a[82:83],v50 offset:2304
v_mfma_f32_16x16x32_bf16 v[248:251],a[112:115],a[248:251],v[248:251]
ds_read_b64_tr_b16 a[84:85],v50 offset:16640
v_mfma_f32_16x16x32_bf16 v[156:159],a[120:123],a[224:227],v[156:159]
v_mfma_f32_16x16x32_bf16 v[156:159],a[124:127],a[228:231],v[156:159]
ds_read_b64_tr_b16 a[86:87],v50 offset:18688
v_mfma_f32_16x16x32_bf16 v[188:191],a[124:127],a[236:239],v[188:191]
ds_read_b64_tr_b16 a[88:89],v50 offset:264
v_mfma_f32_16x16x32_bf16 v[188:191],a[120:123],a[232:235],v[188:191]
ds_read_b64_tr_b16 a[90:91],v50 offset:2312
v_mfma_f32_16x16x32_bf16 v[220:223],a[120:123],a[240:243],v[220:223]
v_mfma_f32_16x16x32_bf16 v[220:223],a[124:127],a[244:247],v[220:223]
ds_read_b64_tr_b16 a[92:93],v50 offset:16648
v_mfma_f32_16x16x32_bf16 v[252:255],a[124:127],a[252:255],v[252:255]
ds_read_b64_tr_b16 a[94:95],v50 offset:18696
v_mfma_f32_16x16x32_bf16 v[252:255],a[120:123],a[248:251],v[252:255]
s_add_u32 m0,s88,0
ds_read_b64_tr_b16 a[96:97],v50 offset:512
s_waitcnt lgkmcnt(6)
v_mfma_f32_16x16x32_bf16 a[0:3],a[64:67],a[192:195],a[0:3]
v_mfma_f32_16x16x32_bf16 a[0:3],a[68:71],a[196:199],a[0:3]
buffer_load_dwordx4 v40,s[36:39],s83 offen lds
ds_read_b64_tr_b16 a[98:99],v50 offset:2560
v_mfma_f32_16x16x32_bf16 a[16:19],a[68:71],a[204:207],a[16:19]
ds_read_b64_tr_b16 a[100:101],v50 offset:16896
v_mfma_f32_16x16x32_bf16 a[16:19],a[64:67],a[200:203],a[16:19]
ds_read_b64_tr_b16 a[102:103],v50 offset:18944
v_mfma_f32_16x16x32_bf16 a[32:35],a[64:67],a[208:211],a[32:35]
v_mfma_f32_16x16x32_bf16 a[32:35],a[68:71],a[212:215],a[32:35]
ds_read_b64_tr_b16 a[104:105],v50 offset:520
v_mfma_f32_16x16x32_bf16 a[48:51],a[68:71],a[220:223],a[48:51]
s_add_u32 m0,s88,1024
ds_read_b64_tr_b16 a[106:107],v50 offset:2568
v_mfma_f32_16x16x32_bf16 a[48:51],a[64:67],a[216:219],a[48:51]
ds_read_b64_tr_b16 a[108:109],v50 offset:16904
v_mfma_f32_16x16x32_bf16 a[4:7],a[72:75],a[192:195],a[4:7]
buffer_load_dwordx4 v41,s[36:39],s83 offen lds
v_mfma_f32_16x16x32_bf16 a[4:7],a[76:79],a[196:199],a[4:7]
ds_read_b64_tr_b16 a[110:111],v50 offset:18952
v_mfma_f32_16x16x32_bf16 a[20:23],a[76:79],a[204:207],a[20:23]
ds_read_b64_tr_b16 a[112:113],v50 offset:768
v_mfma_f32_16x16x32_bf16 a[20:23],a[72:75],a[200:203],a[20:23]
ds_read_b64_tr_b16 a[114:115],v50 offset:2816
v_mfma_f32_16x16x32_bf16 a[36:39],a[72:75],a[208:211],a[36:39]
v_mfma_f32_16x16x32_bf16 a[36:39],a[76:79],a[212:215],a[36:39]
s_add_u32 m0,s88,2048
ds_read_b64_tr_b16 a[116:117],v50 offset:17152
v_mfma_f32_16x16x32_bf16 a[52:55],a[76:79],a[220:223],a[52:55]
ds_read_b64_tr_b16 a[118:119],v50 offset:19200
v_mfma_f32_16x16x32_bf16 a[52:55],a[72:75],a[216:219],a[52:55]
buffer_load_dwordx4 v42,s[36:39],s83 offen lds
ds_read_b64_tr_b16 a[120:121],v50 offset:776
v_mfma_f32_16x16x32_bf16 a[8:11],a[80:83],a[192:195],a[8:11]
s_waitcnt lgkmcnt(6)
v_mfma_f32_16x16x32_bf16 a[8:11],a[84:87],a[196:199],a[8:11]
ds_read_b64_tr_b16 a[122:123],v50 offset:2824
v_mfma_f32_16x16x32_bf16 a[24:27],a[84:87],a[204:207],a[24:27]
ds_read_b64_tr_b16 a[124:125],v50 offset:17160
v_mfma_f32_16x16x32_bf16 a[24:27],a[80:83],a[200:203],a[24:27]
ds_read_b64_tr_b16 a[126:127],v50 offset:19208
v_mfma_f32_16x16x32_bf16 a[40:43],a[80:83],a[208:211],a[40:43]
s_add_u32 m0,s88,3072
v_mfma_f32_16x16x32_bf16 a[40:43],a[84:87],a[212:215],a[40:43]
ds_read_b64_tr_b16 a[64:65],v50 offset:1024
v_mfma_f32_16x16x32_bf16 a[56:59],a[84:87],a[220:223],a[56:59]
buffer_load_dwordx4 v43,s[36:39],s83 offen lds
ds_read_b64_tr_b16 a[66:67],v50 offset:3072
v_mfma_f32_16x16x32_bf16 a[56:59],a[80:83],a[216:219],a[56:59]
ds_read_b64_tr_b16 a[68:69],v50 offset:17408
v_mfma_f32_16x16x32_bf16 a[12:15],a[88:91],a[192:195],a[12:15]
v_mfma_f32_16x16x32_bf16 a[12:15],a[92:95],a[196:199],a[12:15]
ds_read_b64_tr_b16 a[70:71],v50 offset:19456
v_mfma_f32_16x16x32_bf16 a[28:31],a[92:95],a[204:207],a[28:31]
ds_read_b64_tr_b16 a[72:73],v50 offset:1032
v_mfma_f32_16x16x32_bf16 a[28:31],a[88:91],a[200:203],a[28:31]
s_add_u32 m0,s88,4096
ds_read_b64_tr_b16 a[74:75],v50 offset:3080
v_mfma_f32_16x16x32_bf16 a[44:47],a[88:91],a[208:211],a[44:47]
v_mfma_f32_16x16x32_bf16 a[44:47],a[92:95],a[212:215],a[44:47]
buffer_load_dwordx4 v44,s[36:39],s83 offen lds
ds_read_b64_tr_b16 a[76:77],v50 offset:17416
v_mfma_f32_16x16x32_bf16 a[60:63],a[92:95],a[220:223],a[60:63]
ds_read_b64_tr_b16 a[78:79],v50 offset:19464
v_mfma_f32_16x16x32_bf16 a[60:63],a[88:91],a[216:219],a[60:63]
ds_read_b64_tr_b16 a[80:81],v50 offset:1280
v_mfma_f32_16x16x32_bf16 v[64:67],a[96:99],a[192:195],v[64:67]
v_mfma_f32_16x16x32_bf16 v[64:67],a[100:103],a[196:199],v[64:67]
ds_read_b64_tr_b16 a[82:83],v50 offset:3328
v_mfma_f32_16x16x32_bf16 v[80:83],a[100:103],a[204:207],v[80:83]
s_add_u32 m0,s88,5120
ds_read_b64_tr_b16 a[84:85],v50 offset:17664
v_mfma_f32_16x16x32_bf16 v[80:83],a[96:99],a[200:203],v[80:83]
ds_read_b64_tr_b16 a[86:87],v50 offset:19712
v_mfma_f32_16x16x32_bf16 v[96:99],a[96:99],a[208:211],v[96:99]
buffer_load_dwordx4 v45,s[36:39],s83 offen lds
v_mfma_f32_16x16x32_bf16 v[96:99],a[100:103],a[212:215],v[96:99]
ds_read_b64_tr_b16 a[88:89],v50 offset:1288
v_mfma_f32_16x16x32_bf16 v[112:115],a[100:103],a[220:223],v[112:115]
ds_read_b64_tr_b16 a[90:91],v50 offset:3336
v_mfma_f32_16x16x32_bf16 v[112:115],a[96:99],a[216:219],v[112:115]
ds_read_b64_tr_b16 a[92:93],v50 offset:17672
v_mfma_f32_16x16x32_bf16 v[68:71],a[104:107],a[192:195],v[68:71]
s_waitcnt lgkmcnt(6)
v_mfma_f32_16x16x32_bf16 v[68:71],a[108:111],a[196:199],v[68:71]
s_add_u32 m0,s88,6144
ds_read_b64_tr_b16 a[94:95],v50 offset:19720
v_mfma_f32_16x16x32_bf16 v[84:87],a[108:111],a[204:207],v[84:87]
ds_read_b64_tr_b16 a[96:97],v50 offset:1536
v_mfma_f32_16x16x32_bf16 v[84:87],a[104:107],a[200:203],v[84:87]
buffer_load_dwordx4 v46,s[36:39],s83 offen lds
ds_read_b64_tr_b16 a[98:99],v50 offset:3584
v_mfma_f32_16x16x32_bf16 v[100:103],a[104:107],a[208:211],v[100:103]
v_mfma_f32_16x16x32_bf16 v[100:103],a[108:111],a[212:215],v[100:103]
ds_read_b64_tr_b16 a[100:101],v50 offset:17920
v_mfma_f32_16x16x32_bf16 v[116:119],a[108:111],a[220:223],v[116:119]
ds_read_b64_tr_b16 a[102:103],v50 offset:19968
v_mfma_f32_16x16x32_bf16 v[116:119],a[104:107],a[216:219],v[116:119]
ds_read_b64_tr_b16 a[104:105],v50 offset:1544
v_mfma_f32_16x16x32_bf16 v[72:75],a[112:115],a[192:195],v[72:75]
s_add_u32 m0,s88,7168
s_mov_b32 s88,s69
v_mfma_f32_16x16x32_bf16 v[72:75],a[116:119],a[196:199],v[72:75]
ds_read_b64_tr_b16 a[106:107],v50 offset:3592
v_mfma_f32_16x16x32_bf16 v[88:91],a[116:119],a[204:207],v[88:91]
buffer_load_dwordx4 v47,s[36:39],s83 offen lds
ds_read_b64_tr_b16 a[108:109],v50 offset:17928
v_mfma_f32_16x16x32_bf16 v[88:91],a[112:115],a[200:203],v[88:91]
s_add_u32 s83,s83,524288
ds_read_b64_tr_b16 a[110:111],v50 offset:19976
v_mfma_f32_16x16x32_bf16 v[104:107],a[112:115],a[208:211],v[104:107]
v_mfma_f32_16x16x32_bf16 v[104:107],a[116:119],a[212:215],v[104:107]
v_mfma_f32_16x16x32_bf16 v[120:123],a[116:119],a[220:223],v[120:123]
ds_read_b64_tr_b16 a[116:117],v50 offset:18176
v_mfma_f32_16x16x32_bf16 v[120:123],a[112:115],a[216:219],v[120:123]
s_add_u32 m0,s85,0
ds_read_b64_tr_b16 a[112:113],v50 offset:1792
v_mfma_f32_16x16x32_bf16 v[76:79],a[120:123],a[192:195],v[76:79]
ds_read_b64_tr_b16 a[114:115],v50 offset:3840
v_mfma_f32_16x16x32_bf16 v[76:79],a[124:127],a[196:199],v[76:79]
buffer_load_dwordx4 v32,s[28:31],s84 offen lds
ds_read_b64_tr_b16 a[118:119],v50 offset:20224
v_mfma_f32_16x16x32_bf16 v[92:95],a[124:127],a[204:207],v[92:95]
v_mfma_f32_16x16x32_bf16 v[92:95],a[120:123],a[200:203],v[92:95]
v_mfma_f32_16x16x32_bf16 v[108:111],a[120:123],a[208:211],v[108:111]
v_mfma_f32_16x16x32_bf16 v[108:111],a[124:127],a[212:215],v[108:111]
v_mfma_f32_16x16x32_bf16 v[124:127],a[124:127],a[220:223],v[124:127]
ds_read_b64_tr_b16 a[124:125],v50 offset:18184
v_mfma_f32_16x16x32_bf16 v[124:127],a[120:123],a[216:219],v[124:127]
ds_read_b64_tr_b16 a[120:121],v50 offset:1800
v_mfma_f32_16x16x32_bf16 v[128:131],a[64:67],a[192:195],v[128:131]
buffer_load_dwordx4 v33,s[28:31],s84 offen offset:1024 lds
ds_read_b64_tr_b16 a[122:123],v50 offset:3848
v_mfma_f32_16x16x32_bf16 v[128:131],a[68:71],a[196:199],v[128:131]
ds_read_b64_tr_b16 a[126:127],v50 offset:20232
v_add_u32 v50,s86,v51
v_mfma_f32_16x16x32_bf16 v[160:163],a[68:71],a[204:207],v[160:163]
v_mfma_f32_16x16x32_bf16 v[160:163],a[64:67],a[200:203],v[160:163]
v_mfma_f32_16x16x32_bf16 v[192:195],a[64:67],a[208:211],v[192:195]
v_mfma_f32_16x16x32_bf16 v[192:195],a[68:71],a[212:215],v[192:195]
v_mfma_f32_16x16x32_bf16 v[224:227],a[68:71],a[220:223],v[224:227]
v_mfma_f32_16x16x32_bf16 v[224:227],a[64:67],a[216:219],v[224:227]
buffer_load_dwordx4 v34,s[28:31],s84 offen offset:2048 lds
v_mfma_f32_16x16x32_bf16 v[132:135],a[72:75],a[192:195],v[132:135]
v_mfma_f32_16x16x32_bf16 v[132:135],a[76:79],a[196:199],v[132:135]
v_mfma_f32_16x16x32_bf16 v[164:167],a[76:79],a[204:207],v[164:167]
v_mfma_f32_16x16x32_bf16 v[164:167],a[72:75],a[200:203],v[164:167]
v_mfma_f32_16x16x32_bf16 v[196:199],a[72:75],a[208:211],v[196:199]
v_mfma_f32_16x16x32_bf16 v[196:199],a[76:79],a[212:215],v[196:199]
v_mfma_f32_16x16x32_bf16 v[228:231],a[76:79],a[220:223],v[228:231]
buffer_load_dwordx4 v35,s[28:31],s84 offen offset:3072 lds
v_mfma_f32_16x16x32_bf16 v[228:231],a[72:75],a[216:219],v[228:231]
s_waitcnt lgkmcnt(0)
v_mfma_f32_16x16x32_bf16 v[136:139],a[80:83],a[192:195],v[136:139]
v_mfma_f32_16x16x32_bf16 v[136:139],a[84:87],a[196:199],v[136:139]
v_mfma_f32_16x16x32_bf16 v[168:171],a[84:87],a[204:207],v[168:171]
v_mfma_f32_16x16x32_bf16 v[168:171],a[80:83],a[200:203],v[168:171]
s_add_u32 m0,s85,4096
s_xor_b32 s85,s85,98304
v_mfma_f32_16x16x32_bf16 v[200:203],a[80:83],a[208:211],v[200:203]
v_mfma_f32_16x16x32_bf16 v[200:203],a[84:87],a[212:215],v[200:203]
buffer_load_dwordx4 v36,s[28:31],s84 offen lds
v_mfma_f32_16x16x32_bf16 v[232:235],a[84:87],a[220:223],v[232:235]
v_mfma_f32_16x16x32_bf16 v[232:235],a[80:83],a[216:219],v[232:235]
v_mfma_f32_16x16x32_bf16 v[140:143],a[88:91],a[192:195],v[140:143]
v_mfma_f32_16x16x32_bf16 v[140:143],a[92:95],a[196:199],v[140:143]
v_mfma_f32_16x16x32_bf16 v[172:175],a[92:95],a[204:207],v[172:175]
v_mfma_f32_16x16x32_bf16 v[172:175],a[88:91],a[200:203],v[172:175]
v_mfma_f32_16x16x32_bf16 v[204:207],a[88:91],a[208:211],v[204:207]
buffer_load_dwordx4 v37,s[28:31],s84 offen offset:1024 lds
v_mfma_f32_16x16x32_bf16 v[204:207],a[92:95],a[212:215],v[204:207]
v_mfma_f32_16x16x32_bf16 v[236:239],a[92:95],a[220:223],v[236:239]
v_mfma_f32_16x16x32_bf16 v[236:239],a[88:91],a[216:219],v[236:239]
s_waitcnt vmcnt(23)
s_barrier
v_mfma_f32_16x16x32_bf16 v[144:147],a[96:99],a[192:195],v[144:147]
s_waitcnt vmcnt(15)
ds_read_b128 a[224:227],v48
v_mfma_f32_16x16x32_bf16 v[144:147],a[100:103],a[196:199],v[144:147]
ds_read_b128 a[228:231],v49
v_mfma_f32_16x16x32_bf16 v[176:179],a[100:103],a[204:207],v[176:179]
buffer_load_dwordx4 v38,s[28:31],s84 offen offset:2048 lds
ds_read_b128 a[232:235],v48 offset:2048
v_mfma_f32_16x16x32_bf16 v[176:179],a[96:99],a[200:203],v[176:179]
ds_read_b128 a[236:239],v49 offset:2048
v_mfma_f32_16x16x32_bf16 v[208:211],a[96:99],a[208:211],v[208:211]
v_mfma_f32_16x16x32_bf16 v[208:211],a[100:103],a[212:215],v[208:211]
ds_read_b128 a[240:243],v48 offset:4096
v_mfma_f32_16x16x32_bf16 v[240:243],a[100:103],a[220:223],v[240:243]
ds_read_b128 a[244:247],v49 offset:4096
v_mfma_f32_16x16x32_bf16 v[240:243],a[96:99],a[216:219],v[240:243]
ds_read_b128 a[248:251],v48 offset:6144
v_mfma_f32_16x16x32_bf16 v[148:151],a[104:107],a[192:195],v[148:151]
v_mfma_f32_16x16x32_bf16 v[148:151],a[108:111],a[196:199],v[148:151]
buffer_load_dwordx4 v39,s[28:31],s84 offen offset:3072 lds
ds_read_b128 a[252:255],v49 offset:6144
v_mfma_f32_16x16x32_bf16 v[180:183],a[108:111],a[204:207],v[180:183]
s_add_u32 s84,s84,128
ds_read_b64_tr_b16 a[64:65],v50
v_mfma_f32_16x16x32_bf16 v[180:183],a[104:107],a[200:203],v[180:183]
)A"
R"A(ds_read_b64_tr_b16 a[66:67],v50 offset:2048
v_mfma_f32_16x16x32_bf16 v[212:215],a[104:107],a[208:211],v[212:215]
v_mfma_f32_16x16x32_bf16 v[212:215],a[108:111],a[212:215],v[212:215]
ds_read_b64_tr_b16 a[68:69],v50 offset:16384
v_mfma_f32_16x16x32_bf16 v[244:247],a[108:111],a[220:223],v[244:247]
ds_read_b64_tr_b16 a[70:71],v50 offset:18432
v_mfma_f32_16x16x32_bf16 v[244:247],a[104:107],a[216:219],v[244:247]
ds_read_b64_tr_b16 a[72:73],v50 offset:8
v_mfma_f32_16x16x32_bf16 v[152:155],a[112:115],a[192:195],v[152:155]
buffer_load_dword v8,v19,s[48:51],s90 offen
v_mfma_f32_16x16x32_bf16 v[152:155],a[116:119],a[196:199],v[152:155]
s_add_u32 s90,s90,896
ds_read_b64_tr_b16 a[74:75],v50 offset:2056
v_mfma_f32_16x16x32_bf16 v[184:187],a[116:119],a[204:207],v[184:187]
s_nop 0
L_pd2_%=:
s_cbranch_execz L_pd6_%=
ds_read_b64_tr_b16 a[76:77],v50 offset:16392
v_mfma_f32_16x16x32_bf16 v[184:187],a[112:115],a[200:203],v[184:187]
ds_read_b64_tr_b16 a[78:79],v50 offset:18440
v_mfma_f32_16x16x32_bf16 v[216:219],a[112:115],a[208:211],v[216:219]
v_mfma_f32_16x16x32_bf16 v[216:219],a[116:119],a[212:215],v[216:219]
ds_read_b64_tr_b16 a[80:81],v50 offset:256
v_mfma_f32_16x16x32_bf16 v[248:251],a[116:119],a[220:223],v[248:251]
ds_read_b64_tr_b16 a[82:83],v50 offset:2304
v_mfma_f32_16x16x32_bf16 v[248:251],a[112:115],a[216:219],v[248:251]
ds_read_b64_tr_b16 a[84:85],v50 offset:16640
v_mfma_f32_16x16x32_bf16 v[156:159],a[120:123],a[192:195],v[156:159]
v_mfma_f32_16x16x32_bf16 v[156:159],a[124:127],a[196:199],v[156:159]
ds_read_b64_tr_b16 a[86:87],v50 offset:18688
v_mfma_f32_16x16x32_bf16 v[188:191],a[124:127],a[204:207],v[188:191]
ds_read_b64_tr_b16 a[88:89],v50 offset:264
v_mfma_f32_16x16x32_bf16 v[188:191],a[120:123],a[200:203],v[188:191]
ds_read_b64_tr_b16 a[90:91],v50 offset:2312
v_mfma_f32_16x16x32_bf16 v[220:223],a[120:123],a[208:211],v[220:223]
v_mfma_f32_16x16x32_bf16 v[220:223],a[124:127],a[212:215],v[220:223]
ds_read_b64_tr_b16 a[92:93],v50 offset:16648
v_mfma_f32_16x16x32_bf16 v[252:255],a[124:127],a[220:223],v[252:255]
ds_read_b64_tr_b16 a[94:95],v50 offset:18696
v_mfma_f32_16x16x32_bf16 v[252:255],a[120:123],a[216:219],v[252:255]
s_sub_u32 s70,s70,1
s_cmp_lg_u32 s70,0
s_cbranch_scc1 L_qp_%=
s_waitcnt lgkmcnt(5)
v_mfma_f32_16x16x32_bf16 a[0:3],a[64:67],a[224:227],a[0:3]
s_add_u32 m0,s85,0
ds_read_b64_tr_b16 a[96:97],v50 offset:512
v_subrev_u32 v40,256,v40
v_mfma_f32_16x16x32_bf16 a[0:3],a[68:71],a[228:231],a[0:3]
s_sub_u32 s83,s83,524288
buffer_load_dwordx4 v32,s[28:31],s84 offen lds
ds_read_b64_tr_b16 a[98:99],v50 offset:2560
v_mfma_f32_16x16x32_bf16 a[16:19],a[68:71],a[236:239],a[16:19]
s_mov_b32 s69,s86
ds_read_b64_tr_b16 a[100:101],v50 offset:16896
v_subrev_u32 v41,256,v41
v_mfma_f32_16x16x32_bf16 a[16:19],a[64:67],a[232:235],a[16:19]
s_mov_b32 s86,s87
ds_read_b64_tr_b16 a[102:103],v50 offset:18944
v_subrev_u32 v42,256,v42
v_mfma_f32_16x16x32_bf16 a[32:35],a[64:67],a[240:243],a[32:35]
s_mov_b32 s87,s88
v_subrev_u32 v43,256,v43
v_subrev_u32 v44,256,v44
v_mfma_f32_16x16x32_bf16 a[32:35],a[68:71],a[244:247],a[32:35]
s_add_u32 s75,s75,16384
ds_read_b64_tr_b16 a[104:105],v50 offset:520
v_subrev_u32 v45,256,v45
v_mfma_f32_16x16x32_bf16 a[48:51],a[68:71],a[252:255],a[48:51]
s_sub_u32 s78,s77,32768
ds_read_b64_tr_b16 a[106:107],v50 offset:2568
v_subrev_u32 v46,256,v46
v_mfma_f32_16x16x32_bf16 a[48:51],a[64:67],a[248:251],a[48:51]
s_and_b32 s78,s78,0x1ffff
buffer_load_dwordx4 v33,s[28:31],s84 offen offset:1024 lds
ds_read_b64_tr_b16 a[108:109],v50 offset:16904
v_mfma_f32_16x16x32_bf16 a[4:7],a[72:75],a[224:227],a[4:7]
s_sub_u32 s78,s78,s77
v_subrev_u32 v47,256,v47
v_xor_b32 v48,98304,v48
v_mfma_f32_16x16x32_bf16 a[4:7],a[76:79],a[228:231],a[4:7]
s_mov_b32 s65,262144
ds_read_b64_tr_b16 a[110:111],v50 offset:18952
v_xor_b32 v49,98304,v49
v_mfma_f32_16x16x32_bf16 a[20:23],a[76:79],a[236:239],a[20:23]
s_add_u32 s77,s77,s78
ds_read_b64_tr_b16 a[112:113],v50 offset:768
v_add_u32 v52,s87,v51
v_mfma_f32_16x16x32_bf16 a[20:23],a[72:75],a[232:235],a[20:23]
s_mov_b32 s6,0
s_mov_b32 s5,0
ds_read_b64_tr_b16 a[114:115],v50 offset:2816
v_mfma_f32_16x16x32_bf16 a[36:39],a[72:75],a[240:243],a[36:39]
v_mfma_f32_16x16x32_bf16 a[36:39],a[76:79],a[244:247],a[36:39]
buffer_load_dwordx4 v34,s[28:31],s84 offen offset:2048 lds
ds_read_b64_tr_b16 a[116:117],v50 offset:17152
v_mfma_f32_16x16x32_bf16 a[52:55],a[76:79],a[252:255],a[52:55]
ds_read_b64_tr_b16 a[118:119],v50 offset:19200
v_mfma_f32_16x16x32_bf16 a[52:55],a[72:75],a[248:251],a[52:55]
ds_read_b64_tr_b16 a[120:121],v50 offset:776
v_mfma_f32_16x16x32_bf16 a[8:11],a[80:83],a[224:227],a[8:11]
s_waitcnt lgkmcnt(6)
v_mfma_f32_16x16x32_bf16 a[8:11],a[84:87],a[228:231],a[8:11]
ds_read_b64_tr_b16 a[122:123],v50 offset:2824
v_mfma_f32_16x16x32_bf16 a[24:27],a[84:87],a[236:239],a[24:27]
ds_read_b64_tr_b16 a[124:125],v50 offset:17160
v_mfma_f32_16x16x32_bf16 a[24:27],a[80:83],a[232:235],a[24:27]
buffer_load_dwordx4 v35,s[28:31],s84 offen offset:3072 lds
ds_read_b64_tr_b16 a[126:127],v50 offset:19208
v_mfma_f32_16x16x32_bf16 a[40:43],a[80:83],a[240:243],a[40:43]
v_mfma_f32_16x16x32_bf16 a[40:43],a[84:87],a[244:247],a[40:43]
ds_read_b64_tr_b16 a[64:65],v50 offset:1024
v_mfma_f32_16x16x32_bf16 a[56:59],a[84:87],a[252:255],a[56:59]
ds_read_b64_tr_b16 a[66:67],v50 offset:3072
v_mfma_f32_16x16x32_bf16 a[56:59],a[80:83],a[248:251],a[56:59]
s_add_u32 m0,s85,4096
s_xor_b32 s85,s85,98304
ds_read_b64_tr_b16 a[68:69],v50 offset:17408
v_mfma_f32_16x16x32_bf16 a[12:15],a[88:91],a[224:227],a[12:15]
v_mfma_f32_16x16x32_bf16 a[12:15],a[92:95],a[228:231],a[12:15]
buffer_load_dwordx4 v36,s[28:31],s84 offen lds
ds_read_b64_tr_b16 a[70:71],v50 offset:19456
v_mfma_f32_16x16x32_bf16 a[28:31],a[92:95],a[236:239],a[28:31]
ds_read_b64_tr_b16 a[72:73],v50 offset:1032
v_mfma_f32_16x16x32_bf16 a[28:31],a[88:91],a[232:235],a[28:31]
ds_read_b64_tr_b16 a[74:75],v50 offset:3080
v_mfma_f32_16x16x32_bf16 a[44:47],a[88:91],a[240:243],a[44:47]
v_mfma_f32_16x16x32_bf16 a[44:47],a[92:95],a[244:247],a[44:47]
ds_read_b64_tr_b16 a[76:77],v50 offset:17416
v_mfma_f32_16x16x32_bf16 a[60:63],a[92:95],a[252:255],a[60:63]
ds_read_b64_tr_b16 a[78:79],v50 offset:19464
v_mfma_f32_16x16x32_bf16 a[60:63],a[88:91],a[248:251],a[60:63]
buffer_load_dwordx4 v37,s[28:31],s84 offen offset:1024 lds
ds_read_b64_tr_b16 a[80:81],v50 offset:1280
v_mfma_f32_16x16x32_bf16 v[64:67],a[96:99],a[224:227],v[64:67]
v_mfma_f32_16x16x32_bf16 v[64:67],a[100:103],a[228:231],v[64:67]
ds_read_b64_tr_b16 a[82:83],v50 offset:3328
v_mfma_f32_16x16x32_bf16 v[80:83],a[100:103],a[236:239],v[80:83]
ds_read_b64_tr_b16 a[84:85],v50 offset:17664
v_mfma_f32_16x16x32_bf16 v[80:83],a[96:99],a[232:235],v[80:83]
ds_read_b64_tr_b16 a[86:87],v50 offset:19712
v_mfma_f32_16x16x32_bf16 v[96:99],a[96:99],a[240:243],v[96:99]
v_mfma_f32_16x16x32_bf16 v[96:99],a[100:103],a[244:247],v[96:99]
buffer_load_dwordx4 v38,s[28:31],s84 offen offset:2048 lds
ds_read_b64_tr_b16 a[88:89],v50 offset:1288
v_mfma_f32_16x16x32_bf16 v[112:115],a[100:103],a[252:255],v[112:115]
ds_read_b64_tr_b16 a[90:91],v50 offset:3336
v_mfma_f32_16x16x32_bf16 v[112:115],a[96:99],a[248:251],v[112:115]
ds_read_b64_tr_b16 a[92:93],v50 offset:17672
v_mfma_f32_16x16x32_bf16 v[68:71],a[104:107],a[224:227],v[68:71]
s_waitcnt lgkmcnt(6)
v_mfma_f32_16x16x32_bf16 v[68:71],a[108:111],a[228:231],v[68:71]
ds_read_b64_tr_b16 a[94:95],v50 offset:19720
v_mfma_f32_16x16x32_bf16 v[84:87],a[108:111],a[236:239],v[84:87]
ds_read_b64_tr_b16 a[96:97],v50 offset:1536
v_mfma_f32_16x16x32_bf16 v[84:87],a[104:107],a[232:235],v[84:87]
buffer_load_dwordx4 v39,s[28:31],s84 offen offset:3072 lds
ds_read_b64_tr_b16 a[98:99],v50 offset:3584
v_mfma_f32_16x16x32_bf16 v[100:103],a[104:107],a[240:243],v[100:103]
s_add_u32 s84,s84,128
v_mfma_f32_16x16x32_bf16 v[100:103],a[108:111],a[244:247],v[100:103]
ds_read_b64_tr_b16 a[100:101],v50 offset:17920
v_mfma_f32_16x16x32_bf16 v[116:119],a[108:111],a[252:255],v[116:119]
ds_read_b64_tr_b16 a[102:103],v50 offset:19968
v_mfma_f32_16x16x32_bf16 v[116:119],a[104:107],a[248:251],v[116:119]
s_add_u32 m0,s88,0
ds_read_b64_tr_b16 a[104:105],v50 offset:1544
v_mfma_f32_16x16x32_bf16 v[72:75],a[112:115],a[224:227],v[72:75]
v_mfma_f32_16x16x32_bf16 v[72:75],a[116:119],a[228:231],v[72:75]
buffer_load_dwordx4 v40,s[36:39],s83 offen lds
ds_read_b64_tr_b16 a[106:107],v50 offset:3592
v_mfma_f32_16x16x32_bf16 v[88:91],a[116:119],a[236:239],v[88:91]
ds_read_b64_tr_b16 a[108:109],v50 offset:17928
v_mfma_f32_16x16x32_bf16 v[88:91],a[112:115],a[232:235],v[88:91]
ds_read_b64_tr_b16 a[110:111],v50 offset:19976
v_mfma_f32_16x16x32_bf16 v[104:107],a[112:115],a[240:243],v[104:107]
v_mfma_f32_16x16x32_bf16 v[104:107],a[116:119],a[244:247],v[104:107]
s_add_u32 m0,s88,1024
v_mfma_f32_16x16x32_bf16 v[120:123],a[116:119],a[252:255],v[120:123]
ds_read_b64_tr_b16 a[116:117],v50 offset:18176
v_mfma_f32_16x16x32_bf16 v[120:123],a[112:115],a[248:251],v[120:123]
buffer_load_dwordx4 v41,s[36:39],s83 offen lds
ds_read_b64_tr_b16 a[112:113],v50 offset:1792
v_mfma_f32_16x16x32_bf16 v[76:79],a[120:123],a[224:227],v[76:79]
ds_read_b64_tr_b16 a[114:115],v50 offset:3840
v_mfma_f32_16x16x32_bf16 v[76:79],a[124:127],a[228:231],v[76:79]
ds_read_b64_tr_b16 a[118:119],v50 offset:20224
v_mfma_f32_16x16x32_bf16 v[92:95],a[124:127],a[236:239],v[92:95]
v_mfma_f32_16x16x32_bf16 v[92:95],a[120:123],a[232:235],v[92:95]
s_add_u32 m0,s88,2048
v_mfma_f32_16x16x32_bf16 v[108:111],a[120:123],a[240:243],v[108:111]
v_mfma_f32_16x16x32_bf16 v[108:111],a[124:127],a[244:247],v[108:111]
buffer_load_dwordx4 v42,s[36:39],s83 offen lds
v_mfma_f32_16x16x32_bf16 v[124:127],a[124:127],a[252:255],v[124:127]
ds_read_b64_tr_b16 a[124:125],v50 offset:18184
v_mfma_f32_16x16x32_bf16 v[124:127],a[120:123],a[248:251],v[124:127]
ds_read_b64_tr_b16 a[120:121],v50 offset:1800
v_mfma_f32_16x16x32_bf16 v[128:131],a[64:67],a[224:227],v[128:131]
ds_read_b64_tr_b16 a[122:123],v50 offset:3848
v_mfma_f32_16x16x32_bf16 v[128:131],a[68:71],a[228:231],v[128:131]
s_add_u32 m0,s88,3072
ds_read_b64_tr_b16 a[126:127],v50 offset:20232
v_add_u32 v50,s86,v51
v_mfma_f32_16x16x32_bf16 v[160:163],a[68:71],a[236:239],v[160:163]
v_mfma_f32_16x16x32_bf16 v[160:163],a[64:67],a[232:235],v[160:163]
buffer_load_dwordx4 v43,s[36:39],s83 offen lds
v_mfma_f32_16x16x32_bf16 v[192:195],a[64:67],a[240:243],v[192:195]
v_mfma_f32_16x16x32_bf16 v[192:195],a[68:71],a[244:247],v[192:195]
v_mfma_f32_16x16x32_bf16 v[224:227],a[68:71],a[252:255],v[224:227]
v_mfma_f32_16x16x32_bf16 v[224:227],a[64:67],a[248:251],v[224:227]
s_add_u32 m0,s88,4096
v_mfma_f32_16x16x32_bf16 v[132:135],a[72:75],a[224:227],v[132:135]
v_mfma_f32_16x16x32_bf16 v[132:135],a[76:79],a[228:231],v[132:135]
buffer_load_dwordx4 v44,s[36:39],s83 offen lds
v_mfma_f32_16x16x32_bf16 v[164:167],a[76:79],a[236:239],v[164:167]
v_mfma_f32_16x16x32_bf16 v[164:167],a[72:75],a[232:235],v[164:167]
v_mfma_f32_16x16x32_bf16 v[196:199],a[72:75],a[240:243],v[196:199]
v_mfma_f32_16x16x32_bf16 v[196:199],a[76:79],a[244:247],v[196:199]
s_add_u32 m0,s88,5120
v_mfma_f32_16x16x32_bf16 v[228:231],a[76:79],a[252:255],v[228:231]
v_mfma_f32_16x16x32_bf16 v[228:231],a[72:75],a[248:251],v[228:231]
s_waitcnt lgkmcnt(0)
buffer_load_dwordx4 v45,s[36:39],s83 offen lds
v_mfma_f32_16x16x32_bf16 v[136:139],a[80:83],a[224:227],v[136:139]
v_mfma_f32_16x16x32_bf16 v[136:139],a[84:87],a[228:231],v[136:139]
v_mfma_f32_16x16x32_bf16 v[168:171],a[84:87],a[236:239],v[168:171]
v_mfma_f32_16x16x32_bf16 v[168:171],a[80:83],a[232:235],v[168:171]
s_add_u32 m0,s88,6144
v_mfma_f32_16x16x32_bf16 v[200:203],a[80:83],a[240:243],v[200:203]
v_mfma_f32_16x16x32_bf16 v[200:203],a[84:87],a[244:247],v[200:203]
buffer_load_dwordx4 v46,s[36:39],s83 offen lds
v_mfma_f32_16x16x32_bf16 v[232:235],a[84:87],a[252:255],v[232:235]
v_mfma_f32_16x16x32_bf16 v[232:235],a[80:83],a[248:251],v[232:235]
v_mfma_f32_16x16x32_bf16 v[140:143],a[88:91],a[224:227],v[140:143]
v_mfma_f32_16x16x32_bf16 v[140:143],a[92:95],a[228:231],v[140:143]
s_add_u32 m0,s88,7168
s_mov_b32 s88,s69
s_and_b32 s69,s75,0xfffc0000
v_mfma_f32_16x16x32_bf16 v[172:175],a[92:95],a[236:239],v[172:175]
)A"
R"A(s_add_u32 s75,s75,16384
v_mfma_f32_16x16x32_bf16 v[172:175],a[88:91],a[232:235],v[172:175]
buffer_load_dwordx4 v47,s[36:39],s83 offen lds
v_mfma_f32_16x16x32_bf16 v[204:207],a[88:91],a[240:243],v[204:207]
s_add_u32 s83,s83,524288
v_mfma_f32_16x16x32_bf16 v[204:207],a[92:95],a[244:247],v[204:207]
v_mfma_f32_16x16x32_bf16 v[236:239],a[92:95],a[252:255],v[236:239]
v_mfma_f32_16x16x32_bf16 v[236:239],a[88:91],a[248:251],v[236:239]
s_waitcnt vmcnt(25)
s_barrier
v_mfma_f32_16x16x32_bf16 v[144:147],a[96:99],a[224:227],v[144:147]
s_add_u32 m0,s76,0
s_waitcnt vmcnt(17)
ds_read_b128 a[192:195],v48
v_mfma_f32_16x16x32_bf16 v[144:147],a[100:103],a[228:231],v[144:147]
ds_read_b128 a[196:199],v49
v_mfma_f32_16x16x32_bf16 v[176:179],a[100:103],a[236:239],v[176:179]
buffer_load_dwordx4 v4,s[20:23],s74 offen nt lds
ds_read_b128 a[200:203],v48 offset:2048
v_mfma_f32_16x16x32_bf16 v[176:179],a[96:99],a[232:235],v[176:179]
ds_read_b128 a[204:207],v49 offset:2048
v_mfma_f32_16x16x32_bf16 v[208:211],a[96:99],a[240:243],v[208:211]
v_mfma_f32_16x16x32_bf16 v[208:211],a[100:103],a[244:247],v[208:211]
ds_read_b128 a[208:211],v48 offset:4096
v_mfma_f32_16x16x32_bf16 v[240:243],a[100:103],a[252:255],v[240:243]
ds_read_b128 a[212:215],v49 offset:4096
v_mfma_f32_16x16x32_bf16 v[240:243],a[96:99],a[248:251],v[240:243]
ds_read_b128 a[216:219],v48 offset:6144
v_xor_b32 v48,98304,v48
v_mfma_f32_16x16x32_bf16 v[148:151],a[104:107],a[224:227],v[148:151]
v_mfma_f32_16x16x32_bf16 v[148:151],a[108:111],a[228:231],v[148:151]
ds_read_b128 a[220:223],v49 offset:6144
v_xor_b32 v49,98304,v49
v_mfma_f32_16x16x32_bf16 v[180:183],a[108:111],a[236:239],v[180:183]
ds_read_b64_tr_b16 a[64:65],v50
v_mfma_f32_16x16x32_bf16 v[180:183],a[104:107],a[232:235],v[180:183]
ds_read_b64_tr_b16 a[66:67],v50 offset:2048
v_mfma_f32_16x16x32_bf16 v[212:215],a[104:107],a[240:243],v[212:215]
v_mfma_f32_16x16x32_bf16 v[212:215],a[108:111],a[244:247],v[212:215]
ds_read_b64_tr_b16 a[68:69],v50 offset:16384
v_mfma_f32_16x16x32_bf16 v[244:247],a[108:111],a[252:255],v[244:247]
ds_read_b64_tr_b16 a[70:71],v50 offset:18432
v_mfma_f32_16x16x32_bf16 v[244:247],a[104:107],a[248:251],v[244:247]
ds_read_b64_tr_b16 a[72:73],v50 offset:8
v_mfma_f32_16x16x32_bf16 v[152:155],a[112:115],a[224:227],v[152:155]
v_mfma_f32_16x16x32_bf16 v[152:155],a[116:119],a[228:231],v[152:155]
ds_read_b64_tr_b16 a[74:75],v50 offset:2056
v_mfma_f32_16x16x32_bf16 v[184:187],a[116:119],a[236:239],v[184:187]
buffer_load_dwordx4 v4,s[20:23],s74 offen offset:1024 nt lds
ds_read_b64_tr_b16 a[76:77],v50 offset:16392
v_mfma_f32_16x16x32_bf16 v[184:187],a[112:115],a[232:235],v[184:187]
ds_read_b64_tr_b16 a[78:79],v50 offset:18440
v_mfma_f32_16x16x32_bf16 v[216:219],a[112:115],a[240:243],v[216:219]
v_mfma_f32_16x16x32_bf16 v[216:219],a[116:119],a[244:247],v[216:219]
ds_read_b64_tr_b16 a[80:81],v50 offset:1024
v_mfma_f32_16x16x32_bf16 v[248:251],a[116:119],a[252:255],v[248:251]
ds_read_b64_tr_b16 a[82:83],v50 offset:3072
v_mfma_f32_16x16x32_bf16 v[248:251],a[112:115],a[248:251],v[248:251]
ds_read_b64_tr_b16 a[84:85],v50 offset:17408
v_mfma_f32_16x16x32_bf16 v[156:159],a[120:123],a[224:227],v[156:159]
v_mfma_f32_16x16x32_bf16 v[156:159],a[124:127],a[228:231],v[156:159]
ds_read_b64_tr_b16 a[86:87],v50 offset:19456
v_mfma_f32_16x16x32_bf16 v[188:191],a[124:127],a[236:239],v[188:191]
ds_read_b64_tr_b16 a[88:89],v50 offset:1032
v_mfma_f32_16x16x32_bf16 v[188:191],a[120:123],a[232:235],v[188:191]
ds_read_b64_tr_b16 a[90:91],v50 offset:3080
v_mfma_f32_16x16x32_bf16 v[220:223],a[120:123],a[240:243],v[220:223]
v_mfma_f32_16x16x32_bf16 v[220:223],a[124:127],a[244:247],v[220:223]
ds_read_b64_tr_b16 a[92:93],v50 offset:17416
v_mfma_f32_16x16x32_bf16 v[252:255],a[124:127],a[252:255],v[252:255]
ds_read_b64_tr_b16 a[94:95],v50 offset:19464
v_mfma_f32_16x16x32_bf16 v[252:255],a[120:123],a[248:251],v[252:255]
s_waitcnt vmcnt(10) lgkmcnt(5)
ds_read_b128 a[224:227],v48
v_mfma_f32_16x16x32_bf16 v[128:131],a[64:67],a[192:195],v[128:131]
v_mfma_f32_16x16x32_bf16 v[128:131],a[68:71],a[196:199],v[128:131]
ds_read_b128 a[228:231],v49
v_mfma_f32_16x16x32_bf16 v[160:163],a[68:71],a[204:207],v[160:163]
buffer_load_dwordx4 v5,s[20:23],s74 offen offset:2048 nt lds
ds_read_b128 a[236:239],v49 offset:2048
v_mfma_f32_16x16x32_bf16 v[160:163],a[64:67],a[200:203],v[160:163]
ds_read_b128 a[232:235],v48 offset:2048
v_mfma_f32_16x16x32_bf16 v[192:195],a[64:67],a[208:211],v[192:195]
v_mfma_f32_16x16x32_bf16 v[192:195],a[68:71],a[212:215],v[192:195]
ds_read_b128 a[240:243],v48 offset:4096
v_mfma_f32_16x16x32_bf16 v[224:227],a[68:71],a[220:223],v[224:227]
ds_read_b128 a[244:247],v49 offset:4096
v_mfma_f32_16x16x32_bf16 v[224:227],a[64:67],a[216:219],v[224:227]
ds_read_b128 a[252:255],v49 offset:6144
v_mfma_f32_16x16x32_bf16 v[132:135],a[72:75],a[192:195],v[132:135]
v_mfma_f32_16x16x32_bf16 v[132:135],a[76:79],a[196:199],v[132:135]
ds_read_b128 a[248:251],v48 offset:6144
v_mfma_f32_16x16x32_bf16 v[164:167],a[76:79],a[204:207],v[164:167]
ds_read_b64_tr_b16 a[96:97],v50 offset:256
v_mfma_f32_16x16x32_bf16 v[164:167],a[72:75],a[200:203],v[164:167]
ds_read_b64_tr_b16 a[98:99],v50 offset:2304
v_mfma_f32_16x16x32_bf16 v[196:199],a[72:75],a[208:211],v[196:199]
v_mfma_f32_16x16x32_bf16 v[196:199],a[76:79],a[212:215],v[196:199]
ds_read_b64_tr_b16 a[100:101],v50 offset:16640
v_mfma_f32_16x16x32_bf16 v[228:231],a[76:79],a[220:223],v[228:231]
ds_read_b64_tr_b16 a[102:103],v50 offset:18688
v_mfma_f32_16x16x32_bf16 v[228:231],a[72:75],a[216:219],v[228:231]
s_waitcnt lgkmcnt(5)
ds_read_b64_tr_b16 a[104:105],v50 offset:1280
v_mfma_f32_16x16x32_bf16 v[128:131],a[80:83],a[224:227],v[128:131]
v_mfma_f32_16x16x32_bf16 v[128:131],a[84:87],a[228:231],v[128:131]
ds_read_b64_tr_b16 a[106:107],v50 offset:3328
v_mfma_f32_16x16x32_bf16 v[160:163],a[84:87],a[236:239],v[160:163]
buffer_load_dwordx4 v5,s[20:23],s74 offen offset:3072 lds
ds_read_b64_tr_b16 a[108:109],v50 offset:17664
v_mfma_f32_16x16x32_bf16 v[160:163],a[80:83],a[232:235],v[160:163]
ds_read_b64_tr_b16 a[110:111],v50 offset:19712
v_mfma_f32_16x16x32_bf16 v[192:195],a[80:83],a[240:243],v[192:195]
v_mfma_f32_16x16x32_bf16 v[192:195],a[84:87],a[244:247],v[192:195]
ds_read_b64_tr_b16 a[112:113],v50 offset:264
v_mul_f32 v128,s71,v128
v_mfma_f32_16x16x32_bf16 v[224:227],a[84:87],a[252:255],v[224:227]
s_waitcnt lgkmcnt(5)
ds_read_b64_tr_b16 a[114:115],v50 offset:2312
v_mul_f32 v129,s71,v129
v_mfma_f32_16x16x32_bf16 v[224:227],a[80:83],a[248:251],v[224:227]
ds_read_b64_tr_b16 a[116:117],v50 offset:16648
v_mul_f32 v130,s71,v130
v_mfma_f32_16x16x32_bf16 v[132:135],a[88:91],a[224:227],v[132:135]
v_mul_f32 v131,s71,v131
v_cvt_pk_bf16_f32 v128,v128,v129
v_mfma_f32_16x16x32_bf16 v[132:135],a[92:95],a[228:231],v[132:135]
ds_read_b64_tr_b16 a[118:119],v50 offset:18696
v_cvt_pk_bf16_f32 v129,v130,v131
v_mfma_f32_16x16x32_bf16 v[164:167],a[92:95],a[236:239],v[164:167]
ds_read_b64_tr_b16 a[120:121],v50 offset:1288
v_mul_f32 v192,s71,v192
v_mfma_f32_16x16x32_bf16 v[164:167],a[88:91],a[232:235],v[164:167]
ds_read_b64_tr_b16 a[122:123],v50 offset:3336
v_mul_f32 v193,s71,v193
v_mfma_f32_16x16x32_bf16 v[196:199],a[88:91],a[240:243],v[196:199]
v_mul_f32 v132,s71,v132
v_mul_f32 v133,s71,v133
v_mfma_f32_16x16x32_bf16 v[196:199],a[92:95],a[244:247],v[196:199]
ds_read_b64_tr_b16 a[124:125],v50 offset:17672
v_mul_f32 v134,s71,v134
v_mfma_f32_16x16x32_bf16 v[228:231],a[92:95],a[252:255],v[228:231]
ds_read_b64_tr_b16 a[126:127],v50 offset:19720
v_mul_f32 v135,s71,v135
v_mfma_f32_16x16x32_bf16 v[228:231],a[88:91],a[248:251],v[228:231]
ds_read_b64_tr_b16 a[128:129],v50 offset:512
v_cvt_pk_bf16_f32 v130,v132,v133
v_mfma_f32_16x16x32_bf16 v[136:139],a[96:99],a[192:195],v[136:139]
s_add_u32 m0,s76,16384
s_sub_u32 s76,s76,32768
s_and_b32 s76,s76,0x1ffff
v_mfma_f32_16x16x32_bf16 v[136:139],a[100:103],a[196:199],v[136:139]
s_waitcnt lgkmcnt(5)
ds_read_b64_tr_b16 a[130:131],v50 offset:2560
v_cvt_pk_bf16_f32 v131,v134,v135
v_mfma_f32_16x16x32_bf16 v[136:139],a[104:107],a[224:227],v[136:139]
buffer_load_dwordx4 v4,s[24:27],s74 offen lds
ds_read_b64_tr_b16 a[132:133],v50 offset:16896
v_mfma_f32_16x16x32_bf16 v[136:139],a[108:111],a[228:231],v[136:139]
ds_read_b64_tr_b16 a[134:135],v50 offset:18944
v_mul_f32 v194,s71,v194
v_mfma_f32_16x16x32_bf16 v[168:171],a[108:111],a[236:239],v[168:171]
v_mul_f32 v195,s71,v195
v_mul_f32 v196,s71,v196
v_mfma_f32_16x16x32_bf16 v[168:171],a[104:107],a[232:235],v[168:171]
ds_read_b64_tr_b16 a[136:137],v50 offset:1536
v_mul_f32 v197,s71,v197
v_mfma_f32_16x16x32_bf16 v[168:171],a[100:103],a[204:207],v[168:171]
ds_read_b64_tr_b16 a[138:139],v50 offset:3584
v_mul_f32 v198,s71,v198
v_mfma_f32_16x16x32_bf16 v[168:171],a[96:99],a[200:203],v[168:171]
ds_read_b64_tr_b16 a[140:141],v50 offset:17920
v_mul_f32 v199,s71,v199
v_mfma_f32_16x16x32_bf16 v[200:203],a[96:99],a[208:211],v[200:203]
v_cvt_pk_bf16_f32 v192,v192,v193
v_cvt_pk_bf16_f32 v193,v194,v195
v_mfma_f32_16x16x32_bf16 v[200:203],a[100:103],a[212:215],v[200:203]
ds_read_b64_tr_b16 a[142:143],v50 offset:19968
v_cvt_pk_bf16_f32 v194,v196,v197
v_mfma_f32_16x16x32_bf16 v[200:203],a[104:107],a[240:243],v[200:203]
ds_read_b64_tr_b16 a[144:145],v50 offset:520
v_cvt_pk_bf16_f32 v195,v198,v199
v_mfma_f32_16x16x32_bf16 v[200:203],a[108:111],a[244:247],v[200:203]
ds_read_b64_tr_b16 a[146:147],v50 offset:2568
v_mul_f32 v136,s71,v136
v_mfma_f32_16x16x32_bf16 v[232:235],a[108:111],a[252:255],v[232:235]
v_mul_f32 v137,s71,v137
v_mul_f32 v138,s71,v138
v_mfma_f32_16x16x32_bf16 v[232:235],a[104:107],a[248:251],v[232:235]
ds_read_b64_tr_b16 a[148:149],v50 offset:16904
v_mul_f32 v139,s71,v139
v_mfma_f32_16x16x32_bf16 v[232:235],a[100:103],a[220:223],v[232:235]
ds_read_b64_tr_b16 a[150:151],v50 offset:18952
v_cvt_pk_bf16_f32 v136,v136,v137
v_mfma_f32_16x16x32_bf16 v[232:235],a[96:99],a[216:219],v[232:235]
ds_read_b64_tr_b16 a[152:153],v50 offset:1544
v_cvt_pk_bf16_f32 v137,v138,v139
v_mfma_f32_16x16x32_bf16 v[140:143],a[112:115],a[192:195],v[140:143]
v_mul_f32 v160,s71,v160
v_mul_f32 v161,s71,v161
v_mfma_f32_16x16x32_bf16 v[140:143],a[116:119],a[196:199],v[140:143]
s_waitcnt lgkmcnt(5)
ds_read_b64_tr_b16 a[154:155],v50 offset:3592
v_mul_f32 v162,s71,v162
v_mfma_f32_16x16x32_bf16 v[140:143],a[120:123],a[224:227],v[140:143]
buffer_load_dwordx4 v5,s[24:27],s74 offen offset:1024 lds
ds_read_b64_tr_b16 a[156:157],v50 offset:17928
v_mfma_f32_16x16x32_bf16 v[140:143],a[124:127],a[228:231],v[140:143]
ds_read_b64_tr_b16 a[158:159],v50 offset:19976
v_mul_f32 v163,s71,v163
v_mfma_f32_16x16x32_bf16 v[172:175],a[124:127],a[236:239],v[172:175]
v_mul_f32 v164,s71,v164
v_mul_f32 v165,s71,v165
v_mfma_f32_16x16x32_bf16 v[172:175],a[120:123],a[232:235],v[172:175]
ds_read_b64_tr_b16 a[160:161],v50 offset:768
v_mul_f32 v166,s71,v166
v_mfma_f32_16x16x32_bf16 v[172:175],a[116:119],a[204:207],v[172:175]
ds_read_b64_tr_b16 a[162:163],v50 offset:2816
v_mul_f32 v140,s71,v140
v_mfma_f32_16x16x32_bf16 v[172:175],a[112:115],a[200:203],v[172:175]
ds_read_b64_tr_b16 a[164:165],v50 offset:17152
v_mul_f32 v141,s71,v141
v_mfma_f32_16x16x32_bf16 v[204:207],a[112:115],a[208:211],v[204:207]
v_mul_f32 v142,s71,v142
v_mul_f32 v143,s71,v143
v_mfma_f32_16x16x32_bf16 v[204:207],a[116:119],a[212:215],v[204:207]
ds_read_b64_tr_b16 a[166:167],v50 offset:19200
v_cvt_pk_bf16_f32 v138,v140,v141
v_mfma_f32_16x16x32_bf16 v[204:207],a[120:123],a[240:243],v[204:207]
ds_read_b64_tr_b16 a[168:169],v50 offset:1792
v_cvt_pk_bf16_f32 v139,v142,v143
v_mfma_f32_16x16x32_bf16 v[204:207],a[124:127],a[244:247],v[204:207]
ds_read_b64_tr_b16 a[170:171],v50 offset:3840
v_mul_f32 v167,s71,v167
v_mfma_f32_16x16x32_bf16 v[236:239],a[124:127],a[252:255],v[236:239]
v_cvt_pk_bf16_f32 v160,v160,v161
v_cvt_pk_bf16_f32 v161,v162,v163
v_mfma_f32_16x16x32_bf16 v[236:239],a[120:123],a[248:251],v[236:239]
ds_read_b64_tr_b16 a[172:173],v50 offset:18176
v_cvt_pk_bf16_f32 v162,v164,v165
v_mfma_f32_16x16x32_bf16 v[236:239],a[116:119],a[220:223],v[236:239]
ds_read_b64_tr_b16 a[174:175],v50 offset:20224
v_cvt_pk_bf16_f32 v163,v166,v167
v_mfma_f32_16x16x32_bf16 v[236:239],a[112:115],a[216:219],v[236:239]
)A"
R"A(ds_read_b64_tr_b16 a[176:177],v50 offset:776
v_mul_f32 v224,s71,v224
v_mfma_f32_16x16x32_bf16 v[144:147],a[128:131],a[192:195],v[144:147]
v_mul_f32 v225,s71,v225
v_mul_f32 v226,s71,v226
v_mfma_f32_16x16x32_bf16 v[144:147],a[132:135],a[196:199],v[144:147]
ds_read_b64_tr_b16 a[178:179],v50 offset:2824
v_mul_f32 v227,s71,v227
v_mfma_f32_16x16x32_bf16 v[144:147],a[136:139],a[224:227],v[144:147]
buffer_load_dwordx4 v4,s[24:27],s74 offen offset:2048 lds
ds_read_b64_tr_b16 a[180:181],v50 offset:17160
v_mfma_f32_16x16x32_bf16 v[144:147],a[140:143],a[228:231],v[144:147]
ds_read_b64_tr_b16 a[182:183],v50 offset:19208
v_mul_f32 v228,s71,v228
v_mfma_f32_16x16x32_bf16 v[176:179],a[140:143],a[236:239],v[176:179]
v_mul_f32 v229,s71,v229
v_mul_f32 v230,s71,v230
v_mfma_f32_16x16x32_bf16 v[176:179],a[136:139],a[232:235],v[176:179]
ds_read_b64_tr_b16 a[184:185],v50 offset:1800
v_mul_f32 v231,s71,v231
v_mfma_f32_16x16x32_bf16 v[176:179],a[132:135],a[204:207],v[176:179]
ds_read_b64_tr_b16 a[186:187],v50 offset:3848
v_mul_f32 v144,s71,v144
v_mfma_f32_16x16x32_bf16 v[176:179],a[128:131],a[200:203],v[176:179]
ds_read_b64_tr_b16 a[188:189],v50 offset:18184
v_mul_f32 v145,s71,v145
v_mfma_f32_16x16x32_bf16 v[208:211],a[128:131],a[208:211],v[208:211]
v_mul_f32 v146,s71,v146
v_mul_f32 v147,s71,v147
v_mfma_f32_16x16x32_bf16 v[208:211],a[132:135],a[212:215],v[208:211]
ds_read_b64_tr_b16 a[190:191],v50 offset:20232
v_cvt_pk_bf16_f32 v144,v144,v145
v_mfma_f32_16x16x32_bf16 v[208:211],a[136:139],a[240:243],v[208:211]
v_cvt_pk_bf16_f32 v145,v146,v147
v_cvt_pk_bf16_f32 v224,v224,v225
v_mfma_f32_16x16x32_bf16 v[208:211],a[140:143],a[244:247],v[208:211]
v_cvt_pk_bf16_f32 v225,v226,v227
v_cvt_pk_bf16_f32 v226,v228,v229
v_mfma_f32_16x16x32_bf16 v[240:243],a[140:143],a[252:255],v[240:243]
v_cvt_pk_bf16_f32 v227,v230,v231
v_mul_f32 v168,s71,v168
v_mfma_f32_16x16x32_bf16 v[240:243],a[136:139],a[248:251],v[240:243]
v_mul_f32 v169,s71,v169
v_mul_f32 v170,s71,v170
v_mfma_f32_16x16x32_bf16 v[240:243],a[132:135],a[220:223],v[240:243]
v_mul_f32 v171,s71,v171
v_mul_f32 v172,s71,v172
v_mfma_f32_16x16x32_bf16 v[240:243],a[128:131],a[216:219],v[240:243]
v_mul_f32 v173,s71,v173
v_mul_f32 v174,s71,v174
s_waitcnt vmcnt(7)
s_barrier
s_waitcnt lgkmcnt(1)
v_mfma_f32_16x16x32_bf16 v[148:151],a[144:147],a[192:195],v[148:151]
ds_read_b64_tr_b16 a[64:65],v52
v_mul_f32 v175,s71,v175
v_mfma_f32_16x16x32_bf16 v[148:151],a[148:151],a[196:199],v[148:151]
buffer_load_dwordx4 v5,s[24:27],s74 offen offset:3072 lds
ds_read_b64_tr_b16 a[66:67],v52 offset:2048
v_mfma_f32_16x16x32_bf16 v[148:151],a[152:155],a[224:227],v[148:151]
s_add_u32 s74,s74,16384
s_and_b32 s74,s74,0x3ffff
s_or_b32 s74,s74,s69
v_mfma_f32_16x16x32_bf16 v[148:151],a[156:159],a[228:231],v[148:151]
s_and_b32 s69,s75,0xfffc0000
ds_read_b64_tr_b16 a[68:69],v52 offset:16384
v_cvt_pk_bf16_f32 v168,v168,v169
v_mfma_f32_16x16x32_bf16 v[180:183],a[156:159],a[236:239],v[180:183]
s_add_u32 s75,s75,16384
ds_read_b64_tr_b16 a[70:71],v52 offset:18432
v_cvt_pk_bf16_f32 v169,v170,v171
v_mfma_f32_16x16x32_bf16 v[180:183],a[152:155],a[232:235],v[180:183]
ds_read_b64_tr_b16 a[72:73],v52 offset:1024
v_mul_f32 v148,s71,v148
v_mfma_f32_16x16x32_bf16 v[180:183],a[148:151],a[204:207],v[180:183]
ds_read_b64_tr_b16 a[74:75],v52 offset:3072
v_mul_f32 v149,s71,v149
v_mfma_f32_16x16x32_bf16 v[180:183],a[144:147],a[200:203],v[180:183]
v_mul_f32 v150,s71,v150
v_mul_f32 v151,s71,v151
v_mfma_f32_16x16x32_bf16 v[212:215],a[144:147],a[208:211],v[212:215]
ds_read_b64_tr_b16 a[76:77],v52 offset:17408
v_cvt_pk_bf16_f32 v146,v148,v149
v_mfma_f32_16x16x32_bf16 v[212:215],a[148:151],a[212:215],v[212:215]
ds_read_b64_tr_b16 a[78:79],v52 offset:19456
v_cvt_pk_bf16_f32 v147,v150,v151
v_mfma_f32_16x16x32_bf16 v[212:215],a[152:155],a[240:243],v[212:215]
ds_read_b64_tr_b16 a[80:81],v52 offset:8
v_cvt_pk_bf16_f32 v170,v172,v173
s_nop 0
L_pd3_%=:
s_cbranch_execz L_pd7_%=
v_mfma_f32_16x16x32_bf16 v[212:215],a[156:159],a[244:247],v[212:215]
v_cvt_pk_bf16_f32 v171,v174,v175
v_mul_f32 v176,s71,v176
v_mfma_f32_16x16x32_bf16 v[244:247],a[156:159],a[252:255],v[244:247]
ds_read_b64_tr_b16 a[82:83],v52 offset:2056
v_mul_f32 v177,s71,v177
v_mfma_f32_16x16x32_bf16 v[244:247],a[152:155],a[248:251],v[244:247]
ds_read_b64_tr_b16 a[84:85],v52 offset:16392
v_mul_f32 v178,s71,v178
v_mfma_f32_16x16x32_bf16 v[244:247],a[148:151],a[220:223],v[244:247]
ds_read_b64_tr_b16 a[86:87],v52 offset:18440
v_mul_f32 v179,s71,v179
v_mfma_f32_16x16x32_bf16 v[244:247],a[144:147],a[216:219],v[244:247]
v_mul_f32 v180,s71,v180
v_mul_f32 v181,s71,v181
v_mfma_f32_16x16x32_bf16 v[152:155],a[160:163],a[192:195],v[152:155]
s_add_u32 m0,s76,0
ds_read_b64_tr_b16 a[88:89],v52 offset:1032
v_mul_f32 v182,s71,v182
v_mfma_f32_16x16x32_bf16 v[152:155],a[164:167],a[196:199],v[152:155]
ds_read_b64_tr_b16 a[90:91],v52 offset:3080
v_mul_f32 v183,s71,v183
v_mfma_f32_16x16x32_bf16 v[152:155],a[168:171],a[224:227],v[152:155]
buffer_load_dwordx4 v4,s[20:23],s74 offen nt lds
ds_read_b64_tr_b16 a[92:93],v52 offset:17416
v_mfma_f32_16x16x32_bf16 v[152:155],a[172:175],a[228:231],v[152:155]
v_cvt_pk_bf16_f32 v176,v176,v177
v_cvt_pk_bf16_f32 v177,v178,v179
v_mfma_f32_16x16x32_bf16 v[184:187],a[172:175],a[236:239],v[184:187]
ds_read_b64_tr_b16 a[94:95],v52 offset:19464
v_cvt_pk_bf16_f32 v178,v180,v181
v_mfma_f32_16x16x32_bf16 v[184:187],a[168:171],a[232:235],v[184:187]
ds_read_b64_tr_b16 a[96:97],v52 offset:256
v_cvt_pk_bf16_f32 v179,v182,v183
v_mfma_f32_16x16x32_bf16 v[184:187],a[164:167],a[204:207],v[184:187]
ds_read_b64_tr_b16 a[98:99],v52 offset:2304
v_mul_f32 v152,s71,v152
v_mfma_f32_16x16x32_bf16 v[184:187],a[160:163],a[200:203],v[184:187]
v_mul_f32 v153,s71,v153
v_mul_f32 v154,s71,v154
v_mfma_f32_16x16x32_bf16 v[216:219],a[160:163],a[208:211],v[216:219]
ds_read_b64_tr_b16 a[100:101],v52 offset:16640
v_mul_f32 v155,s71,v155
v_mfma_f32_16x16x32_bf16 v[216:219],a[164:167],a[212:215],v[216:219]
ds_read_b64_tr_b16 a[102:103],v52 offset:18688
v_cvt_pk_bf16_f32 v152,v152,v153
v_mfma_f32_16x16x32_bf16 v[216:219],a[168:171],a[240:243],v[216:219]
ds_read_b64_tr_b16 a[104:105],v52 offset:1280
v_cvt_pk_bf16_f32 v153,v154,v155
v_mfma_f32_16x16x32_bf16 v[216:219],a[172:175],a[244:247],v[216:219]
v_mul_f32 v184,s71,v184
v_mul_f32 v185,s71,v185
v_mfma_f32_16x16x32_bf16 v[248:251],a[172:175],a[252:255],v[248:251]
ds_read_b64_tr_b16 a[106:107],v52 offset:3328
v_mul_f32 v186,s71,v186
v_mfma_f32_16x16x32_bf16 v[248:251],a[168:171],a[248:251],v[248:251]
ds_read_b64_tr_b16 a[108:109],v52 offset:17664
v_mul_f32 v187,s71,v187
v_mfma_f32_16x16x32_bf16 v[248:251],a[164:167],a[220:223],v[248:251]
ds_read_b64_tr_b16 a[110:111],v52 offset:19712
v_cvt_pk_bf16_f32 v184,v184,v185
v_mfma_f32_16x16x32_bf16 v[248:251],a[160:163],a[216:219],v[248:251]
v_cvt_pk_bf16_f32 v185,v186,v187
v_mul_f32 v200,s71,v200
v_mfma_f32_16x16x32_bf16 v[156:159],a[176:179],a[192:195],v[156:159]
ds_read_b64_tr_b16 a[112:113],v52 offset:264
v_mul_f32 v201,s71,v201
v_mfma_f32_16x16x32_bf16 v[156:159],a[180:183],a[196:199],v[156:159]
buffer_load_dwordx4 v4,s[20:23],s74 offen offset:1024 nt lds
ds_read_b64_tr_b16 a[114:115],v52 offset:2312
v_mfma_f32_16x16x32_bf16 v[156:159],a[184:187],a[224:227],v[156:159]
s_waitcnt lgkmcnt(5)
ds_read_b64_tr_b16 a[116:117],v52 offset:16648
v_mul_f32 v202,s71,v202
v_mfma_f32_16x16x32_bf16 v[156:159],a[188:191],a[228:231],v[156:159]
v_mul_f32 v203,s71,v203
v_mul_f32 v204,s71,v204
v_mfma_f32_16x16x32_bf16 v[188:191],a[188:191],a[236:239],v[188:191]
ds_read_b64_tr_b16 a[118:119],v52 offset:18696
v_mul_f32 v205,s71,v205
v_mfma_f32_16x16x32_bf16 v[188:191],a[184:187],a[232:235],v[188:191]
ds_read_b64_tr_b16 a[120:121],v52 offset:1288
v_mul_f32 v206,s71,v206
v_mfma_f32_16x16x32_bf16 v[188:191],a[180:183],a[204:207],v[188:191]
ds_read_b64_tr_b16 a[122:123],v52 offset:3336
v_mul_f32 v156,s71,v156
v_mfma_f32_16x16x32_bf16 v[188:191],a[176:179],a[200:203],v[188:191]
v_mul_f32 v157,s71,v157
v_mul_f32 v158,s71,v158
v_mfma_f32_16x16x32_bf16 v[220:223],a[176:179],a[208:211],v[220:223]
ds_read_b64_tr_b16 a[124:125],v52 offset:17672
v_mul_f32 v159,s71,v159
v_mfma_f32_16x16x32_bf16 v[220:223],a[180:183],a[212:215],v[220:223]
ds_read_b64_tr_b16 a[126:127],v52 offset:19720
v_cvt_pk_bf16_f32 v154,v156,v157
v_mfma_f32_16x16x32_bf16 v[220:223],a[184:187],a[240:243],v[220:223]
v_cvt_pk_bf16_f32 v155,v158,v159
v_mul_f32 v188,s71,v188
v_mfma_f32_16x16x32_bf16 v[220:223],a[188:191],a[244:247],v[220:223]
v_mul_f32 v189,s71,v189
v_mul_f32 v190,s71,v190
v_mfma_f32_16x16x32_bf16 v[252:255],a[188:191],a[252:255],v[252:255]
v_mul_f32 v191,s71,v191
v_cvt_pk_bf16_f32 v186,v188,v189
v_mfma_f32_16x16x32_bf16 v[252:255],a[184:187],a[248:251],v[252:255]
v_cvt_pk_bf16_f32 v187,v190,v191
v_mul_f32 v207,s71,v207
v_mfma_f32_16x16x32_bf16 v[252:255],a[180:183],a[220:223],v[252:255]
v_cvt_pk_bf16_f32 v200,v200,v201
v_cvt_pk_bf16_f32 v201,v202,v203
v_mfma_f32_16x16x32_bf16 v[252:255],a[176:179],a[216:219],v[252:255]
v_cvt_pk_bf16_f32 v202,v204,v205
v_cvt_pk_bf16_f32 v203,v206,v207
v_mfma_f32_16x16x32_bf16 a[0:3],a[64:67],a[192:195],a[0:3]
buffer_load_dwordx4 v5,s[20:23],s74 offen offset:2048 nt lds
v_mul_f32 v208,s71,v208
v_mfma_f32_16x16x32_bf16 a[0:3],a[68:71],a[196:199],a[0:3]
v_mul_f32 v209,s71,v209
v_mul_f32 v210,s71,v210
v_mfma_f32_16x16x32_bf16 a[0:3],a[72:75],a[224:227],a[0:3]
v_mul_f32 v211,s71,v211
v_mul_f32 v212,s71,v212
v_mfma_f32_16x16x32_bf16 a[0:3],a[76:79],a[228:231],a[0:3]
v_mul_f32 v213,s71,v213
v_mul_f32 v214,s71,v214
v_mfma_f32_16x16x32_bf16 a[16:19],a[76:79],a[236:239],a[16:19]
v_mul_f32 v215,s71,v215
v_cvt_pk_bf16_f32 v208,v208,v209
v_mfma_f32_16x16x32_bf16 a[16:19],a[72:75],a[232:235],a[16:19]
v_cvt_pk_bf16_f32 v209,v210,v211
v_cvt_pk_bf16_f32 v210,v212,v213
v_mfma_f32_16x16x32_bf16 a[16:19],a[68:71],a[204:207],a[16:19]
v_accvgpr_read_b32 v8,a0
v_accvgpr_read_b32 v9,a1
v_mfma_f32_16x16x32_bf16 a[16:19],a[64:67],a[200:203],a[16:19]
v_accvgpr_read_b32 v10,a2
v_accvgpr_read_b32 v11,a3
v_mfma_f32_16x16x32_bf16 a[32:35],a[64:67],a[208:211],a[32:35]
v_mul_f32 v8,s71,v8
v_mul_f32 v9,s71,v9
v_mfma_f32_16x16x32_bf16 a[32:35],a[68:71],a[212:215],a[32:35]
v_mul_f32 v10,s71,v10
v_mul_f32 v11,s71,v11
v_mfma_f32_16x16x32_bf16 a[32:35],a[72:75],a[240:243],a[32:35]
v_cvt_pk_bf16_f32 v132,v8,v9
v_cvt_pk_bf16_f32 v133,v10,v11
v_mfma_f32_16x16x32_bf16 a[32:35],a[76:79],a[244:247],a[32:35]
v_accvgpr_read_b32 v16,a16
v_accvgpr_read_b32 v17,a17
v_mfma_f32_16x16x32_bf16 a[48:51],a[76:79],a[252:255],a[48:51]
ds_read_b64_tr_b16 a[76:77],v52 offset:17920
v_accvgpr_read_b32 v18,a18
v_mfma_f32_16x16x32_bf16 a[48:51],a[72:75],a[248:251],a[48:51]
ds_read_b64_tr_b16 a[72:73],v52 offset:1536
v_accvgpr_read_b32 v19,a19
v_mfma_f32_16x16x32_bf16 a[48:51],a[68:71],a[220:223],a[48:51]
ds_read_b64_tr_b16 a[68:69],v52 offset:16896
v_accvgpr_read_b32 v8,a32
v_mfma_f32_16x16x32_bf16 a[48:51],a[64:67],a[216:219],a[48:51]
buffer_load_dwordx4 v5,s[20:23],s74 offen offset:3072 lds
ds_read_b64_tr_b16 a[64:65],v52 offset:512
v_mfma_f32_16x16x32_bf16 a[4:7],a[80:83],a[192:195],a[4:7]
v_accvgpr_read_b32 v9,a33
v_accvgpr_read_b32 v10,a34
v_mfma_f32_16x16x32_bf16 a[4:7],a[84:87],a[196:199],a[4:7]
ds_read_b64_tr_b16 a[66:67],v52 offset:2560
v_accvgpr_read_b32 v11,a35
v_mfma_f32_16x16x32_bf16 a[4:7],a[88:91],a[224:227],a[4:7]
ds_read_b64_tr_b16 a[70:71],v52 offset:18944
v_mul_f32 v8,s71,v8
v_mfma_f32_16x16x32_bf16 a[4:7],a[92:95],a[228:231],a[4:7]
ds_read_b64_tr_b16 a[74:75],v52 offset:3584
v_mul_f32 v9,s71,v9
v_mfma_f32_16x16x32_bf16 a[20:23],a[92:95],a[236:239],a[20:23]
v_mul_f32 v10,s71,v10
v_mul_f32 v11,s71,v11
v_mfma_f32_16x16x32_bf16 a[20:23],a[88:91],a[232:235],a[20:23]
ds_read_b64_tr_b16 a[78:79],v52 offset:19968
v_cvt_pk_bf16_f32 v196,v8,v9
v_mfma_f32_16x16x32_bf16 a[20:23],a[84:87],a[204:207],a[20:23]
v_accvgpr_read_b32 v12,a4
v_accvgpr_read_b32 v13,a5
v_mfma_f32_16x16x32_bf16 a[20:23],a[80:83],a[200:203],a[20:23]
v_accvgpr_read_b32 v14,a6
v_accvgpr_read_b32 v15,a7
v_mfma_f32_16x16x32_bf16 a[36:39],a[80:83],a[208:211],a[36:39]
v_mul_f32 v12,s71,v12
v_mul_f32 v13,s71,v13
)A"
R"A(v_mfma_f32_16x16x32_bf16 a[36:39],a[84:87],a[212:215],a[36:39]
v_mul_f32 v14,s71,v14
v_mul_f32 v15,s71,v15
v_mfma_f32_16x16x32_bf16 a[36:39],a[88:91],a[240:243],a[36:39]
v_cvt_pk_bf16_f32 v134,v12,v13
v_cvt_pk_bf16_f32 v135,v14,v15
v_mfma_f32_16x16x32_bf16 a[36:39],a[92:95],a[244:247],a[36:39]
v_cvt_pk_bf16_f32 v197,v10,v11
v_accvgpr_read_b32 v20,a20
v_mfma_f32_16x16x32_bf16 a[52:55],a[92:95],a[252:255],a[52:55]
s_add_u32 m0,s76,16384
s_sub_u32 s76,s76,32768
ds_read_b64_tr_b16 a[92:93],v52 offset:17928
v_mfma_f32_16x16x32_bf16 a[52:55],a[88:91],a[248:251],a[52:55]
s_and_b32 s76,s76,0x1ffff
ds_read_b64_tr_b16 a[88:89],v52 offset:1544
v_accvgpr_read_b32 v21,a21
v_mfma_f32_16x16x32_bf16 a[52:55],a[84:87],a[220:223],a[52:55]
buffer_load_dwordx4 v4,s[24:27],s74 offen lds
ds_read_b64_tr_b16 a[84:85],v52 offset:16904
v_mfma_f32_16x16x32_bf16 a[52:55],a[80:83],a[216:219],a[52:55]
ds_read_b64_tr_b16 a[80:81],v52 offset:520
v_accvgpr_read_b32 v12,a36
v_mfma_f32_16x16x32_bf16 a[8:11],a[96:99],a[192:195],a[8:11]
v_accvgpr_read_b32 v13,a37
v_accvgpr_read_b32 v14,a38
v_mfma_f32_16x16x32_bf16 a[8:11],a[100:103],a[196:199],a[8:11]
s_waitcnt lgkmcnt(4)
ds_read_b64_tr_b16 a[82:83],v52 offset:2568
v_accvgpr_read_b32 v15,a39
v_mfma_f32_16x16x32_bf16 a[8:11],a[104:107],a[224:227],a[8:11]
ds_read_b64_tr_b16 a[86:87],v52 offset:18952
v_mul_f32 v12,s71,v12
v_mfma_f32_16x16x32_bf16 a[8:11],a[108:111],a[228:231],a[8:11]
ds_read_b64_tr_b16 a[90:91],v52 offset:3592
v_mul_f32 v13,s71,v13
v_mfma_f32_16x16x32_bf16 a[24:27],a[108:111],a[236:239],a[24:27]
v_mul_f32 v14,s71,v14
v_mul_f32 v15,s71,v15
v_mfma_f32_16x16x32_bf16 a[24:27],a[104:107],a[232:235],a[24:27]
ds_read_b64_tr_b16 a[94:95],v52 offset:19976
v_cvt_pk_bf16_f32 v198,v12,v13
v_mfma_f32_16x16x32_bf16 a[24:27],a[100:103],a[204:207],a[24:27]
v_cvt_pk_bf16_f32 v199,v14,v15
v_accvgpr_read_b32 v8,a8
v_mfma_f32_16x16x32_bf16 a[24:27],a[96:99],a[200:203],a[24:27]
v_accvgpr_read_b32 v9,a9
v_accvgpr_read_b32 v10,a10
v_mfma_f32_16x16x32_bf16 a[40:43],a[96:99],a[208:211],a[40:43]
v_accvgpr_read_b32 v11,a11
v_mul_f32 v8,s71,v8
v_mfma_f32_16x16x32_bf16 a[40:43],a[100:103],a[212:215],a[40:43]
v_mul_f32 v9,s71,v9
v_mul_f32 v10,s71,v10
v_mfma_f32_16x16x32_bf16 a[40:43],a[104:107],a[240:243],a[40:43]
v_mul_f32 v11,s71,v11
v_cvt_pk_bf16_f32 v140,v8,v9
v_mfma_f32_16x16x32_bf16 a[40:43],a[108:111],a[244:247],a[40:43]
v_cvt_pk_bf16_f32 v141,v10,v11
v_accvgpr_read_b32 v22,a22
v_mfma_f32_16x16x32_bf16 a[56:59],a[108:111],a[252:255],a[56:59]
ds_read_b64_tr_b16 a[108:109],v52 offset:18176
v_accvgpr_read_b32 v23,a23
v_mfma_f32_16x16x32_bf16 a[56:59],a[104:107],a[248:251],a[56:59]
buffer_load_dwordx4 v5,s[24:27],s74 offen offset:1024 lds
ds_read_b64_tr_b16 a[104:105],v52 offset:1792
v_mfma_f32_16x16x32_bf16 a[56:59],a[100:103],a[220:223],a[56:59]
ds_read_b64_tr_b16 a[100:101],v52 offset:17152
v_mul_f32 v16,s71,v16
v_mfma_f32_16x16x32_bf16 a[56:59],a[96:99],a[216:219],a[56:59]
ds_read_b64_tr_b16 a[96:97],v52 offset:768
v_mul_f32 v17,s71,v17
v_mfma_f32_16x16x32_bf16 a[12:15],a[112:115],a[192:195],a[12:15]
v_mul_f32 v18,s71,v18
v_mul_f32 v19,s71,v19
v_mfma_f32_16x16x32_bf16 a[12:15],a[116:119],a[196:199],a[12:15]
ds_read_b64_tr_b16 a[98:99],v52 offset:2816
v_mul_f32 v20,s71,v20
v_mfma_f32_16x16x32_bf16 a[12:15],a[120:123],a[224:227],a[12:15]
ds_read_b64_tr_b16 a[102:103],v52 offset:19200
v_mul_f32 v21,s71,v21
v_mfma_f32_16x16x32_bf16 a[12:15],a[124:127],a[228:231],a[12:15]
ds_read_b64_tr_b16 a[106:107],v52 offset:3840
v_mul_f32 v22,s71,v22
v_mfma_f32_16x16x32_bf16 a[28:31],a[124:127],a[236:239],a[28:31]
v_mul_f32 v23,s71,v23
v_cvt_pk_bf16_f32 v164,v16,v17
v_mfma_f32_16x16x32_bf16 a[28:31],a[120:123],a[232:235],a[28:31]
ds_read_b64_tr_b16 a[110:111],v52 offset:20224
v_cvt_pk_bf16_f32 v165,v18,v19
v_mfma_f32_16x16x32_bf16 a[28:31],a[116:119],a[204:207],a[28:31]
v_accvgpr_read_b32 v12,a12
v_accvgpr_read_b32 v13,a13
v_mfma_f32_16x16x32_bf16 a[28:31],a[112:115],a[200:203],a[28:31]
v_accvgpr_read_b32 v14,a14
v_accvgpr_read_b32 v15,a15
v_mfma_f32_16x16x32_bf16 a[44:47],a[112:115],a[208:211],a[44:47]
v_mul_f32 v12,s71,v12
v_mul_f32 v13,s71,v13
v_mfma_f32_16x16x32_bf16 a[44:47],a[116:119],a[212:215],a[44:47]
v_mul_f32 v14,s71,v14
v_mul_f32 v15,s71,v15
v_mfma_f32_16x16x32_bf16 a[44:47],a[120:123],a[240:243],a[44:47]
v_cvt_pk_bf16_f32 v142,v12,v13
v_cvt_pk_bf16_f32 v143,v14,v15
v_mfma_f32_16x16x32_bf16 a[44:47],a[124:127],a[244:247],a[44:47]
v_cvt_pk_bf16_f32 v166,v20,v21
v_cvt_pk_bf16_f32 v167,v22,v23
v_mfma_f32_16x16x32_bf16 a[60:63],a[124:127],a[252:255],a[60:63]
buffer_load_dwordx4 v4,s[24:27],s74 offen offset:2048 lds
ds_read_b64_tr_b16 a[124:125],v52 offset:18184
v_mfma_f32_16x16x32_bf16 a[60:63],a[120:123],a[248:251],a[60:63]
ds_read_b64_tr_b16 a[120:121],v52 offset:1800
v_accvgpr_read_b32 v16,a48
v_mfma_f32_16x16x32_bf16 a[60:63],a[116:119],a[220:223],a[60:63]
ds_read_b64_tr_b16 a[116:117],v52 offset:17160
v_accvgpr_read_b32 v17,a49
v_mfma_f32_16x16x32_bf16 a[60:63],a[112:115],a[216:219],a[60:63]
ds_read_b64_tr_b16 a[112:113],v52 offset:776
v_accvgpr_read_b32 v18,a50
v_mfma_f32_16x16x32_bf16 v[64:67],a[64:67],a[192:195],v[64:67]
v_accvgpr_read_b32 v19,a51
v_accvgpr_read_b32 v20,a52
v_mfma_f32_16x16x32_bf16 v[64:67],a[68:71],a[196:199],v[64:67]
ds_read_b64_tr_b16 a[114:115],v52 offset:2824
v_accvgpr_read_b32 v21,a53
v_mfma_f32_16x16x32_bf16 v[64:67],a[72:75],a[224:227],v[64:67]
ds_read_b64_tr_b16 a[118:119],v52 offset:19208
v_accvgpr_read_b32 v22,a54
v_mfma_f32_16x16x32_bf16 v[64:67],a[76:79],a[228:231],v[64:67]
ds_read_b64_tr_b16 a[122:123],v52 offset:3848
v_accvgpr_read_b32 v23,a55
v_mfma_f32_16x16x32_bf16 v[80:83],a[76:79],a[236:239],v[80:83]
v_mul_f32 v16,s71,v16
v_mul_f32 v17,s71,v17
v_mfma_f32_16x16x32_bf16 v[80:83],a[72:75],a[232:235],v[80:83]
ds_read_b64_tr_b16 a[126:127],v52 offset:20232
v_mul_f32 v18,s71,v18
v_mfma_f32_16x16x32_bf16 v[80:83],a[68:71],a[204:207],v[80:83]
v_mul_f32 v64,s71,v64
v_mul_f32 v65,s71,v65
v_mfma_f32_16x16x32_bf16 v[80:83],a[64:67],a[200:203],v[80:83]
v_mul_f32 v66,s71,v66
v_mul_f32 v67,s71,v67
v_mfma_f32_16x16x32_bf16 v[96:99],a[64:67],a[208:211],v[96:99]
v_cvt_pk_bf16_f32 v148,v64,v65
v_cvt_pk_bf16_f32 v149,v66,v67
v_mfma_f32_16x16x32_bf16 v[96:99],a[68:71],a[212:215],v[96:99]
v_mul_f32 v19,s71,v19
v_mul_f32 v20,s71,v20
v_mfma_f32_16x16x32_bf16 v[96:99],a[72:75],a[240:243],v[96:99]
v_mul_f32 v21,s71,v21
v_mul_f32 v22,s71,v22
v_mfma_f32_16x16x32_bf16 v[96:99],a[76:79],a[244:247],v[96:99]
buffer_load_dwordx4 v5,s[24:27],s74 offen offset:3072 lds
v_mul_f32 v23,s71,v23
v_mfma_f32_16x16x32_bf16 v[112:115],a[76:79],a[252:255],v[112:115]
s_add_u32 s74,s74,16384
s_and_b32 s74,s74,0x3ffff
s_or_b32 s74,s74,s69
v_mfma_f32_16x16x32_bf16 v[112:115],a[72:75],a[248:251],v[112:115]
s_and_b32 s69,s75,0xfffc0000
v_cvt_pk_bf16_f32 v228,v16,v17
v_cvt_pk_bf16_f32 v229,v18,v19
v_mfma_f32_16x16x32_bf16 v[112:115],a[68:71],a[220:223],v[112:115]
s_add_u32 s75,s75,16384
v_cvt_pk_bf16_f32 v230,v20,v21
v_cvt_pk_bf16_f32 v231,v22,v23
v_mfma_f32_16x16x32_bf16 v[112:115],a[64:67],a[216:219],v[112:115]
v_accvgpr_read_b32 v16,a24
v_accvgpr_read_b32 v17,a25
s_waitcnt vmcnt(8) lgkmcnt(0)
s_barrier
v_mfma_f32_16x16x32_bf16 v[68:71],a[80:83],a[192:195],v[68:71]
ds_read_b128 a[128:131],v1
v_accvgpr_read_b32 v18,a26
v_mfma_f32_16x16x32_bf16 v[68:71],a[84:87],a[196:199],v[68:71]
ds_read_b128 a[132:135],v1 offset:256
v_accvgpr_read_b32 v19,a27
v_mfma_f32_16x16x32_bf16 v[68:71],a[88:91],a[224:227],v[68:71]
ds_read_b128 a[136:139],v1 offset:512
v_accvgpr_read_b32 v20,a28
v_mfma_f32_16x16x32_bf16 v[68:71],a[92:95],a[228:231],v[68:71]
ds_read_b128 a[140:143],v1 offset:768
v_accvgpr_read_b32 v21,a29
v_mfma_f32_16x16x32_bf16 v[84:87],a[92:95],a[236:239],v[84:87]
v_accvgpr_read_b32 v22,a30
v_accvgpr_read_b32 v23,a31
v_mfma_f32_16x16x32_bf16 v[84:87],a[88:91],a[232:235],v[84:87]
v_mul_f32 v16,s71,v16
v_mul_f32 v17,s71,v17
v_mfma_f32_16x16x32_bf16 v[84:87],a[84:87],a[204:207],v[84:87]
v_mul_f32 v68,s71,v68
v_mul_f32 v69,s71,v69
v_mfma_f32_16x16x32_bf16 v[84:87],a[80:83],a[200:203],v[84:87]
v_mul_f32 v70,s71,v70
v_mul_f32 v71,s71,v71
v_mfma_f32_16x16x32_bf16 v[100:103],a[80:83],a[208:211],v[100:103]
s_add_u32 m0,s76,0
v_cvt_pk_bf16_f32 v150,v68,v69
v_cvt_pk_bf16_f32 v151,v70,v71
v_mfma_f32_16x16x32_bf16 v[100:103],a[84:87],a[212:215],v[100:103]
v_mul_f32 v18,s71,v18
v_mul_f32 v19,s71,v19
v_mfma_f32_16x16x32_bf16 v[100:103],a[88:91],a[240:243],v[100:103]
buffer_load_dwordx4 v4,s[20:23],s74 offen nt lds
v_mul_f32 v20,s71,v20
v_mfma_f32_16x16x32_bf16 v[100:103],a[92:95],a[244:247],v[100:103]
v_mul_f32 v21,s71,v21
v_mul_f32 v22,s71,v22
v_mfma_f32_16x16x32_bf16 v[116:119],a[92:95],a[252:255],v[116:119]
v_mul_f32 v23,s71,v23
v_cvt_pk_bf16_f32 v172,v16,v17
v_mfma_f32_16x16x32_bf16 v[116:119],a[88:91],a[248:251],v[116:119]
v_cvt_pk_bf16_f32 v173,v18,v19
v_cvt_pk_bf16_f32 v174,v20,v21
v_mfma_f32_16x16x32_bf16 v[116:119],a[84:87],a[220:223],v[116:119]
v_cvt_pk_bf16_f32 v175,v22,v23
v_mul_f32 v80,s71,v80
v_mfma_f32_16x16x32_bf16 v[116:119],a[80:83],a[216:219],v[116:119]
v_mul_f32 v81,s71,v81
v_mul_f32 v82,s71,v82
v_mfma_f32_16x16x32_bf16 v[72:75],a[96:99],a[192:195],v[72:75]
v_mul_f32 v83,s71,v83
v_mul_f32 v84,s71,v84
v_mfma_f32_16x16x32_bf16 v[72:75],a[100:103],a[196:199],v[72:75]
v_mul_f32 v85,s71,v85
v_mul_f32 v86,s71,v86
v_mfma_f32_16x16x32_bf16 v[72:75],a[104:107],a[224:227],v[72:75]
v_mul_f32 v87,s71,v87
v_cvt_pk_bf16_f32 v180,v80,v81
v_mfma_f32_16x16x32_bf16 v[72:75],a[108:111],a[228:231],v[72:75]
v_cvt_pk_bf16_f32 v181,v82,v83
v_cvt_pk_bf16_f32 v182,v84,v85
v_mfma_f32_16x16x32_bf16 v[88:91],a[108:111],a[236:239],v[88:91]
v_cvt_pk_bf16_f32 v183,v86,v87
v_accvgpr_read_b32 v8,a40
v_mfma_f32_16x16x32_bf16 v[88:91],a[104:107],a[232:235],v[88:91]
v_accvgpr_read_b32 v9,a41
v_accvgpr_read_b32 v10,a42
v_mfma_f32_16x16x32_bf16 v[88:91],a[100:103],a[204:207],v[88:91]
v_mul_f32 v72,s71,v72
v_mul_f32 v73,s71,v73
v_mfma_f32_16x16x32_bf16 v[88:91],a[96:99],a[200:203],v[88:91]
v_mul_f32 v74,s71,v74
v_mul_f32 v75,s71,v75
v_mfma_f32_16x16x32_bf16 v[104:107],a[96:99],a[208:211],v[104:107]
buffer_load_dwordx4 v4,s[20:23],s74 offen offset:1024 nt lds
v_cvt_pk_bf16_f32 v156,v72,v73
v_mfma_f32_16x16x32_bf16 v[104:107],a[100:103],a[212:215],v[104:107]
v_cvt_pk_bf16_f32 v157,v74,v75
v_accvgpr_read_b32 v11,a43
v_mfma_f32_16x16x32_bf16 v[104:107],a[104:107],a[240:243],v[104:107]
v_mul_f32 v88,s71,v88
v_mul_f32 v89,s71,v89
v_mfma_f32_16x16x32_bf16 v[104:107],a[108:111],a[244:247],v[104:107]
v_mul_f32 v90,s71,v90
v_mul_f32 v91,s71,v91
v_mfma_f32_16x16x32_bf16 v[120:123],a[108:111],a[252:255],v[120:123]
v_cvt_pk_bf16_f32 v188,v88,v89
v_cvt_pk_bf16_f32 v189,v90,v91
v_mfma_f32_16x16x32_bf16 v[120:123],a[104:107],a[248:251],v[120:123]
v_accvgpr_read_b32 v12,a44
v_accvgpr_read_b32 v13,a45
v_mfma_f32_16x16x32_bf16 v[120:123],a[100:103],a[220:223],v[120:123]
v_accvgpr_read_b32 v14,a46
v_accvgpr_read_b32 v15,a47
v_mfma_f32_16x16x32_bf16 v[120:123],a[96:99],a[216:219],v[120:123]
v_mul_f32 v8,s71,v8
v_mul_f32 v9,s71,v9
v_mfma_f32_16x16x32_bf16 v[76:79],a[112:115],a[192:195],v[76:79]
v_mul_f32 v10,s71,v10
v_mul_f32 v11,s71,v11
v_mfma_f32_16x16x32_bf16 v[76:79],a[116:119],a[196:199],v[76:79]
v_mul_f32 v12,s71,v12
v_mul_f32 v13,s71,v13
v_mfma_f32_16x16x32_bf16 v[76:79],a[120:123],a[224:227],v[76:79]
v_mul_f32 v14,s71,v14
v_mul_f32 v15,s71,v15
v_mfma_f32_16x16x32_bf16 v[76:79],a[124:127],a[228:231],v[76:79]
v_cvt_pk_bf16_f32 v204,v8,v9
v_cvt_pk_bf16_f32 v205,v10,v11
v_mfma_f32_16x16x32_bf16 v[92:95],a[124:127],a[236:239],v[92:95]
v_cvt_pk_bf16_f32 v206,v12,v13
v_cvt_pk_bf16_f32 v207,v14,v15
v_mfma_f32_16x16x32_bf16 v[92:95],a[120:123],a[232:235],v[92:95]
ds_read_b128 a[156:159],v1 offset:4864
v_cvt_pk_bf16_f32 v211,v214,v215
v_mfma_f32_16x16x32_bf16 v[92:95],a[116:119],a[204:207],v[92:95]
buffer_load_dwordx4 v5,s[20:23],s74 offen offset:2048 nt lds
v_mul_f32 v76,s71,v76
v_mfma_f32_16x16x32_bf16 v[92:95],a[112:115],a[200:203],v[92:95]
v_mul_f32 v77,s71,v77
v_mul_f32 v78,s71,v78
v_mfma_f32_16x16x32_bf16 v[108:111],a[112:115],a[208:211],v[108:111]
)A"
R"A(v_mul_f32 v79,s71,v79
v_cvt_pk_bf16_f32 v158,v76,v77
v_mfma_f32_16x16x32_bf16 v[108:111],a[116:119],a[212:215],v[108:111]
ds_read_b128 a[152:155],v1 offset:4608
v_cvt_pk_bf16_f32 v159,v78,v79
v_mfma_f32_16x16x32_bf16 v[108:111],a[120:123],a[240:243],v[108:111]
v_mul_f32 v92,s71,v92
v_mul_f32 v93,s71,v93
v_mfma_f32_16x16x32_bf16 v[108:111],a[124:127],a[244:247],v[108:111]
v_mul_f32 v94,s71,v94
v_mul_f32 v95,s71,v95
v_mfma_f32_16x16x32_bf16 v[124:127],a[124:127],a[252:255],v[124:127]
v_cvt_pk_bf16_f32 v190,v92,v93
v_cvt_pk_bf16_f32 v191,v94,v95
v_mfma_f32_16x16x32_bf16 v[124:127],a[120:123],a[248:251],v[124:127]
v_mul_f32 v96,s71,v96
v_mul_f32 v97,s71,v97
v_mfma_f32_16x16x32_bf16 v[124:127],a[116:119],a[220:223],v[124:127]
v_mul_f32 v98,s71,v98
v_mul_f32 v99,s71,v99
v_mfma_f32_16x16x32_bf16 v[124:127],a[112:115],a[216:219],v[124:127]
v_mul_f32 v100,s71,v100
v_mul_f32 v101,s71,v101
s_waitcnt lgkmcnt(1)
v_mfma_f32_16x16x32_bf16 v[64:67],a[128:131],v[132:135],0
v_mul_f32 v102,s71,v102
v_mul_f32 v103,s71,v103
v_mfma_f32_16x16x32_bf16 v[64:67],a[132:135],v[140:143],v[64:67]
v_cvt_pk_bf16_f32 v212,v96,v97
v_cvt_pk_bf16_f32 v213,v98,v99
v_mfma_f32_16x16x32_bf16 v[64:67],a[136:139],v[148:151],v[64:67]
v_cvt_pk_bf16_f32 v214,v100,v101
v_cvt_pk_bf16_f32 v215,v102,v103
v_mfma_f32_16x16x32_bf16 v[64:67],a[140:143],v[156:159],v[64:67]
buffer_load_dwordx4 v5,s[20:23],s74 offen offset:3072 lds
v_mul_f32 v216,s71,v216
v_mfma_f32_16x16x32_bf16 v[80:83],a[128:131],v[164:167],0
v_mul_f32 v217,s71,v217
v_mul_f32 v218,s71,v218
v_mfma_f32_16x16x32_bf16 v[80:83],a[132:135],v[172:175],v[80:83]
v_mul_f32 v219,s71,v219
v_mul_f32 v220,s71,v220
v_mfma_f32_16x16x32_bf16 v[80:83],a[136:139],v[180:183],v[80:83]
v_max3_f32 v64,v64,v65,v66
v_max_f32 v64,v64,v67
v_mov_b32 v65,v64
v_mfma_f32_16x16x32_bf16 v[80:83],a[140:143],v[188:191],v[80:83]
v_mul_f32 v221,s71,v221
v_mul_f32 v222,s71,v222
v_mfma_f32_16x16x32_bf16 v[96:99],a[128:131],v[196:199],0
v_permlane32_swap_b32 v64,v65
v_max_f32 v64,v64,v65
v_mov_b32 v65,v64
v_mul_f32 v223,s71,v223
v_cvt_pk_bf16_f32 v216,v216,v217
v_cvt_pk_bf16_f32 v217,v218,v219
v_cvt_pk_bf16_f32 v218,v220,v221
v_cvt_pk_bf16_f32 v219,v222,v223
v_mul_f32 v104,s71,v104
v_mul_f32 v105,s71,v105
v_mul_f32 v106,s71,v106
v_mul_f32 v107,s71,v107
v_mul_f32 v108,s71,v108
v_mul_f32 v109,s71,v109
v_mul_f32 v110,s71,v110
v_mul_f32 v111,s71,v111
v_cvt_pk_bf16_f32 v220,v104,v105
v_cvt_pk_bf16_f32 v221,v106,v107
v_cvt_pk_bf16_f32 v222,v108,v109
v_cvt_pk_bf16_f32 v223,v110,v111
v_mfma_f32_16x16x32_bf16 v[96:99],a[132:135],v[204:207],v[96:99]
ds_read_b128 a[148:151],v1 offset:4352
v_max3_f32 v80,v80,v81,v82
v_mfma_f32_16x16x32_bf16 v[96:99],a[136:139],v[212:215],v[96:99]
ds_read_b128 a[144:147],v1 offset:4096
v_permlane16_swap_b32 v64,v65
v_max_f32 v64,v64,v65
v_sub_f32 v35,0,v64
v_sub_f32 v34,0,v64
v_sub_f32 v33,0,v64
v_sub_f32 v32,0,v64
v_mfma_f32_16x16x32_bf16 v[96:99],a[140:143],v[220:223],v[96:99]
ds_read_b128 a[160:163],v1 offset:8192
v_max_f32 v80,v80,v83
v_mfma_f32_16x16x32_bf16 v[64:67],a[128:131],v[132:135],v[32:35]
ds_read_b128 a[164:167],v1 offset:8448
v_mov_b32 v81,v80
v_mfma_f32_16x16x32_bf16 v[64:67],a[132:135],v[140:143],v[64:67]
v_mul_f32 v232,s71,v232
v_mul_f32 v233,s71,v233
v_mfma_f32_16x16x32_bf16 v[64:67],a[136:139],v[148:151],v[64:67]
ds_read_b128 a[168:171],v1 offset:8704
v_max3_f32 v96,v96,v97,v98
v_mfma_f32_16x16x32_bf16 v[64:67],a[140:143],v[156:159],v[64:67]
s_add_u32 m0,s76,16384
ds_read_b128 a[172:175],v1 offset:8960
v_max_f32 v96,v96,v99
v_mfma_f32_16x16x32_bf16 v[68:71],a[156:159],v[156:159],v[32:35]
s_sub_u32 s76,s76,32768
ds_read_b128 a[188:191],v1 offset:13056
v_mov_b32 v97,v96
s_waitcnt lgkmcnt(7)
v_mfma_f32_16x16x32_bf16 v[68:71],a[152:155],v[148:151],v[68:71]
s_and_b32 s76,s76,0x1ffff
buffer_load_dwordx4 v4,s[24:27],s74 offen lds
v_mul_f32 v234,s71,v234
s_waitcnt lgkmcnt(6)
v_mfma_f32_16x16x32_bf16 v[68:71],a[148:151],v[140:143],v[68:71]
s_waitcnt lgkmcnt(5)
ds_read_b128 a[184:187],v1 offset:12800
v_mul_f32 v235,s71,v235
v_mfma_f32_16x16x32_bf16 v[68:71],a[144:147],v[132:135],v[68:71]
s_waitcnt lgkmcnt(5)
ds_read_b128 a[180:183],v1 offset:12544
v_mul_f32 v236,s71,v236
v_mfma_f32_16x16x32_bf16 v[72:75],a[160:163],v[132:135],v[32:35]
s_waitcnt lgkmcnt(5)
ds_read_b128 a[176:179],v1 offset:12288
v_mul_f32 v237,s71,v237
v_mfma_f32_16x16x32_bf16 v[72:75],a[164:167],v[140:143],v[72:75]
s_waitcnt lgkmcnt(5)
v_permlane32_swap_b32 v80,v81
s_nop 0
L_pd4_%=:
s_cbranch_execz L_pd8_%=
v_mfma_f32_16x16x32_bf16 v[72:75],a[168:171],v[148:151],v[72:75]
s_waitcnt lgkmcnt(4)
v_max_f32 v80,v80,v81
v_mov_b32 v81,v80
v_mfma_f32_16x16x32_bf16 v[72:75],a[172:175],v[156:159],v[72:75]
s_waitcnt lgkmcnt(3)
v_mul_f32 v238,s71,v238
v_mul_f32 v239,s71,v239
v_mfma_f32_16x16x32_bf16 v[76:79],a[188:191],v[156:159],v[32:35]
s_waitcnt lgkmcnt(2)
v_permlane16_swap_b32 v80,v81
v_mfma_f32_16x16x32_bf16 v[76:79],a[184:187],v[148:151],v[76:79]
s_waitcnt lgkmcnt(1)
v_max_f32 v80,v80,v81
v_sub_f32 v39,0,v80
v_sub_f32 v38,0,v80
v_sub_f32 v37,0,v80
v_sub_f32 v36,0,v80
v_mfma_f32_16x16x32_bf16 v[76:79],a[180:183],v[140:143],v[76:79]
s_waitcnt lgkmcnt(0)
v_cvt_pk_bf16_f32 v232,v232,v233
v_cvt_pk_bf16_f32 v233,v234,v235
v_mfma_f32_16x16x32_bf16 v[76:79],a[176:179],v[132:135],v[76:79]
v_cvt_pk_bf16_f32 v234,v236,v237
v_cvt_pk_bf16_f32 v235,v238,v239
v_mfma_f32_16x16x32_bf16 v[80:83],a[128:131],v[164:167],v[36:39]
v_accvgpr_read_b32 v16,a56
v_accvgpr_read_b32 v17,a57
v_mfma_f32_16x16x32_bf16 v[80:83],a[132:135],v[172:175],v[80:83]
v_accvgpr_read_b32 v18,a58
v_accvgpr_read_b32 v19,a59
v_mfma_f32_16x16x32_bf16 v[80:83],a[136:139],v[180:183],v[80:83]
v_accvgpr_read_b32 v20,a60
v_accvgpr_read_b32 v21,a61
v_mfma_f32_16x16x32_bf16 v[80:83],a[140:143],v[188:191],v[80:83]
buffer_load_dwordx4 v5,s[24:27],s74 offen offset:1024 lds
v_accvgpr_read_b32 v22,a62
v_mfma_f32_16x16x32_bf16 v[84:87],a[156:159],v[188:191],v[36:39]
v_accvgpr_read_b32 v23,a63
v_mul_f32 v16,s71,v16
v_mfma_f32_16x16x32_bf16 v[84:87],a[152:155],v[180:183],v[84:87]
v_mul_f32 v17,s71,v17
v_mul_f32 v18,s71,v18
v_mfma_f32_16x16x32_bf16 v[84:87],a[148:151],v[172:175],v[84:87]
v_mul_f32 v19,s71,v19
v_mul_f32 v20,s71,v20
v_mfma_f32_16x16x32_bf16 v[84:87],a[144:147],v[164:167],v[84:87]
v_mul_f32 v21,s71,v21
v_mul_f32 v22,s71,v22
v_mfma_f32_16x16x32_bf16 v[88:91],a[160:163],v[164:167],v[36:39]
v_mul_f32 v23,s71,v23
v_cvt_pk_bf16_f32 v236,v16,v17
v_mfma_f32_16x16x32_bf16 v[88:91],a[164:167],v[172:175],v[88:91]
v_cvt_pk_bf16_f32 v237,v18,v19
v_cvt_pk_bf16_f32 v238,v20,v21
v_mfma_f32_16x16x32_bf16 v[88:91],a[168:171],v[180:183],v[88:91]
v_cvt_pk_bf16_f32 v239,v22,v23
v_mul_f32 v240,s71,v240
v_mfma_f32_16x16x32_bf16 v[88:91],a[172:175],v[188:191],v[88:91]
v_mul_f32 v241,s71,v241
v_mul_f32 v242,s71,v242
v_mfma_f32_16x16x32_bf16 v[92:95],a[188:191],v[188:191],v[36:39]
v_mul_f32 v243,s71,v243
v_mul_f32 v244,s71,v244
v_mfma_f32_16x16x32_bf16 v[92:95],a[184:187],v[180:183],v[92:95]
v_mul_f32 v245,s71,v245
v_mul_f32 v246,s71,v246
v_mfma_f32_16x16x32_bf16 v[92:95],a[180:183],v[172:175],v[92:95]
v_mul_f32 v247,s71,v247
v_cvt_pk_bf16_f32 v240,v240,v241
v_mfma_f32_16x16x32_bf16 v[92:95],a[176:179],v[164:167],v[92:95]
v_cvt_pk_bf16_f32 v241,v242,v243
v_cvt_pk_bf16_f32 v242,v244,v245
v_cvt_pk_bf16_f32 v243,v246,v247
v_mul_f32 v112,s71,v112
v_mul_f32 v113,s71,v113
v_mul_f32 v114,s71,v114
v_mul_f32 v115,s71,v115
v_mul_f32 v116,s71,v116
v_mul_f32 v117,s71,v117
v_mul_f32 v118,s71,v118
v_mul_f32 v119,s71,v119
v_cvt_pk_bf16_f32 v244,v112,v113
v_cvt_pk_bf16_f32 v245,v114,v115
v_cvt_pk_bf16_f32 v246,v116,v117
v_cvt_pk_bf16_f32 v247,v118,v119
v_permlane32_swap_b32 v96,v97
v_max_f32 v96,v96,v97
v_mov_b32 v97,v96
v_mfma_f32_16x16x32_bf16 v[112:115],a[128:131],v[228:231],0
v_mul_f32 v248,s71,v248
v_mul_f32 v249,s71,v249
v_mul_f32 v250,s71,v250
v_mul_f32 v251,s71,v251
v_mul_f32 v252,s71,v252
v_mul_f32 v253,s71,v253
v_mul_f32 v254,s71,v254
v_mul_f32 v255,s71,v255
v_cvt_pk_bf16_f32 v248,v248,v249
v_cvt_pk_bf16_f32 v249,v250,v251
v_cvt_pk_bf16_f32 v250,v252,v253
v_cvt_pk_bf16_f32 v251,v254,v255
v_mul_f32 v120,s71,v120
v_mul_f32 v121,s71,v121
v_mul_f32 v122,s71,v122
v_mul_f32 v123,s71,v123
v_mul_f32 v124,s71,v124
v_mul_f32 v125,s71,v125
v_mul_f32 v126,s71,v126
v_mul_f32 v127,s71,v127
v_cvt_pk_bf16_f32 v252,v120,v121
v_cvt_pk_bf16_f32 v253,v122,v123
v_cvt_pk_bf16_f32 v254,v124,v125
v_cvt_pk_bf16_f32 v255,v126,v127
v_mfma_f32_16x16x32_bf16 v[112:115],a[132:135],v[236:239],v[112:115]
buffer_load_dwordx4 v4,s[24:27],s74 offen offset:2048 lds
v_permlane16_swap_b32 v96,v97
v_max_f32 v96,v96,v97
v_sub_f32 v43,0,v96
v_sub_f32 v42,0,v96
v_sub_f32 v41,0,v96
v_sub_f32 v40,0,v96
v_mfma_f32_16x16x32_bf16 v[112:115],a[136:139],v[244:247],v[112:115]
ds_read_b64_tr_b16 a[192:193],v2
ds_read_b64_tr_b16 a[194:195],v2 offset:4096
v_mfma_f32_16x16x32_bf16 v[112:115],a[140:143],v[252:255],v[112:115]
ds_read_b64_tr_b16 a[196:197],v2 offset:8192
ds_read_b64_tr_b16 a[198:199],v2 offset:12288
v_mfma_f32_16x16x32_bf16 v[96:99],a[128:131],v[196:199],v[40:43]
ds_read_b64_tr_b16 a[200:201],v3
ds_read_b64_tr_b16 a[202:203],v3 offset:4096
v_mfma_f32_16x16x32_bf16 v[96:99],a[132:135],v[204:207],v[96:99]
ds_read_b64_tr_b16 a[204:205],v3 offset:8192
ds_read_b64_tr_b16 a[206:207],v3 offset:12288
v_mfma_f32_16x16x32_bf16 v[96:99],a[136:139],v[212:215],v[96:99]
ds_read_b64_tr_b16 a[208:209],v2 offset:256
v_max3_f32 v112,v112,v113,v114
v_mfma_f32_16x16x32_bf16 v[96:99],a[140:143],v[220:223],v[96:99]
ds_read_b64_tr_b16 a[210:211],v2 offset:4352
ds_read_b64_tr_b16 a[212:213],v2 offset:8448
v_mfma_f32_16x16x32_bf16 v[100:103],a[156:159],v[220:223],v[40:43]
ds_read_b64_tr_b16 a[214:215],v2 offset:12544
v_max_f32 v112,v112,v115
v_mfma_f32_16x16x32_bf16 v[100:103],a[152:155],v[212:215],v[100:103]
ds_read_b64_tr_b16 a[216:217],v3 offset:256
ds_read_b64_tr_b16 a[218:219],v3 offset:4352
v_mfma_f32_16x16x32_bf16 v[100:103],a[148:151],v[204:207],v[100:103]
v_exp_f32 v64,v64
v_mfma_f32_16x16x32_bf16 v[100:103],a[144:147],v[196:199],v[100:103]
v_exp_f32 v65,v65
v_mfma_f32_16x16x32_bf16 v[104:107],a[160:163],v[196:199],v[40:43]
v_exp_f32 v66,v66
v_mfma_f32_16x16x32_bf16 v[104:107],a[164:167],v[204:207],v[104:107]
v_exp_f32 v67,v67
v_mfma_f32_16x16x32_bf16 v[104:107],a[168:171],v[212:215],v[104:107]
v_exp_f32 v68,v68
v_mfma_f32_16x16x32_bf16 v[104:107],a[172:175],v[220:223],v[104:107]
buffer_load_dwordx4 v5,s[24:27],s74 offen offset:3072 lds
ds_read_b64_tr_b16 a[220:221],v3 offset:8448
v_mfma_f32_16x16x32_bf16 v[108:111],a[188:191],v[220:223],v[40:43]
v_exp_f32 v69,v69
v_exp_f32 v70,v70
v_exp_f32 v71,v71
v_mfma_f32_16x16x32_bf16 v[108:111],a[184:187],v[212:215],v[108:111]
ds_read_b64_tr_b16 a[222:223],v3 offset:12544
v_cvt_pk_bf16_f32 v64,v64,v65
v_cvt_pk_bf16_f32 v65,v66,v67
v_cvt_pk_bf16_f32 v66,v68,v69
v_cvt_pk_bf16_f32 v67,v70,v71
v_exp_f32 v72,v72
v_exp_f32 v73,v73
v_exp_f32 v74,v74
v_exp_f32 v75,v75
v_exp_f32 v76,v76
v_exp_f32 v77,v77
v_exp_f32 v78,v78
v_exp_f32 v79,v79
v_mfma_f32_16x16x32_bf16 v[108:111],a[180:183],v[204:207],v[108:111]
ds_read_b64_tr_b16 a[224:225],v2 offset:512
ds_read_b64_tr_b16 a[226:227],v2 offset:4608
v_cvt_pk_bf16_f32 v72,v72,v73
v_cvt_pk_bf16_f32 v73,v74,v75
v_cvt_pk_bf16_f32 v74,v76,v77
v_cvt_pk_bf16_f32 v75,v78,v79
v_mfma_f32_16x16x32_bf16 v[108:111],a[176:179],v[196:199],v[108:111]
s_add_u32 s74,s74,16384
ds_read_b64_tr_b16 a[228:229],v2 offset:8704
ds_read_b64_tr_b16 a[230:231],v2 offset:12800
s_waitcnt lgkmcnt(6)
v_mfma_f32_16x16x32_bf16 a[0:3],a[192:195],v[64:67],0
s_and_b32 s74,s74,0x3ffff
ds_read_b64_tr_b16 a[232:233],v3 offset:512
ds_read_b64_tr_b16 a[234:235],v3 offset:4608
v_mfma_f32_16x16x32_bf16 a[0:3],a[196:199],v[72:75],a[0:3]
s_or_b32 s74,s74,s69
ds_read_b64_tr_b16 a[236:237],v3 offset:8704
ds_read_b64_tr_b16 a[238:239],v3 offset:12800
v_mfma_f32_16x16x32_bf16 a[4:7],a[200:203],v[64:67],0
s_and_b32 s69,s75,0xfffc0000
ds_read_b64_tr_b16 a[240:241],v2 offset:768
v_mov_b32 v113,v112
v_mfma_f32_16x16x32_bf16 a[4:7],a[204:207],v[72:75],a[4:7]
s_add_u32 s75,s75,16384
ds_read_b64_tr_b16 a[242:243],v2 offset:4864
)A"
R"A(ds_read_b64_tr_b16 a[244:245],v2 offset:8960
v_mfma_f32_16x16x32_bf16 a[8:11],a[208:211],v[64:67],0
ds_read_b64_tr_b16 a[246:247],v2 offset:13056
v_add_u32 v1,s78,v1
v_mfma_f32_16x16x32_bf16 a[8:11],a[212:215],v[72:75],a[8:11]
ds_read_b64_tr_b16 a[248:249],v3 offset:768
ds_read_b64_tr_b16 a[250:251],v3 offset:4864
v_mfma_f32_16x16x32_bf16 a[12:15],a[216:219],v[64:67],0
s_waitcnt lgkmcnt(12)
ds_read_b64_tr_b16 a[252:253],v3 offset:8960
v_add_u32 v2,s78,v2
v_mfma_f32_16x16x32_bf16 a[12:15],a[220:223],v[72:75],a[12:15]
ds_read_b64_tr_b16 a[254:255],v3 offset:13056
v_add_u32 v3,s78,v3
v_mfma_f32_16x16x32_bf16 a[16:19],a[224:227],v[64:67],0
s_sub_u32 s78,s77,32768
v_permlane32_swap_b32 v112,v113
s_waitcnt lgkmcnt(10)
v_mfma_f32_16x16x32_bf16 a[16:19],a[228:231],v[72:75],a[16:19]
s_and_b32 s78,s78,0x1ffff
v_max_f32 v112,v112,v113
v_mov_b32 v113,v112
v_mfma_f32_16x16x32_bf16 a[20:23],a[232:235],v[64:67],0
s_sub_u32 s78,s78,s77
v_exp_f32 v80,v80
s_waitcnt lgkmcnt(7)
v_mfma_f32_16x16x32_bf16 a[20:23],a[236:239],v[72:75],a[20:23]
s_add_u32 s77,s77,s78
v_permlane16_swap_b32 v112,v113
s_waitcnt lgkmcnt(5)
v_mfma_f32_16x16x32_bf16 a[24:27],a[240:243],v[64:67],0
s_waitcnt lgkmcnt(4)
v_max_f32 v112,v112,v113
v_max_f32 v112,s72,v112
v_mfma_f32_16x16x32_bf16 a[24:27],a[244:247],v[72:75],a[24:27]
s_waitcnt lgkmcnt(2)
v_sub_f32 v47,0,v112
v_sub_f32 v46,0,v112
v_mfma_f32_16x16x32_bf16 a[28:31],a[248:251],v[64:67],0
s_waitcnt lgkmcnt(0)
v_sub_f32 v45,0,v112
v_sub_f32 v44,0,v112
v_mfma_f32_16x16x32_bf16 a[28:31],a[252:255],v[72:75],a[28:31]
v_exp_f32 v81,v81
v_mfma_f32_16x16x32_bf16 v[48:51],v[28:31],v[64:67],0
v_exp_f32 v82,v82
v_mfma_f32_16x16x32_bf16 v[48:51],v[28:31],v[72:75],v[48:51]
v_exp_f32 v83,v83
s_waitcnt vmcnt(8)
s_barrier
v_mfma_f32_16x16x32_bf16 v[112:115],a[128:131],v[228:231],v[44:47]
s_add_u32 m0,s76,0
v_exp_f32 v84,v84
v_mfma_f32_16x16x32_bf16 v[112:115],a[132:135],v[236:239],v[112:115]
v_exp_f32 v85,v85
v_mfma_f32_16x16x32_bf16 v[112:115],a[136:139],v[244:247],v[112:115]
buffer_load_dwordx4 v4,s[20:23],s74 offen nt lds
v_cvt_pk_bf16_f32 v80,v80,v81
v_mfma_f32_16x16x32_bf16 v[112:115],a[140:143],v[252:255],v[112:115]
v_exp_f32 v86,v86
v_mfma_f32_16x16x32_bf16 v[116:119],a[156:159],v[252:255],v[44:47]
v_exp_f32 v87,v87
v_mfma_f32_16x16x32_bf16 v[116:119],a[152:155],v[244:247],v[116:119]
v_cvt_pk_bf16_f32 v81,v82,v83
v_cvt_pk_bf16_f32 v82,v84,v85
v_mfma_f32_16x16x32_bf16 v[116:119],a[148:151],v[236:239],v[116:119]
ds_read_b128 a[128:131],v1
v_cvt_pk_bf16_f32 v83,v86,v87
v_mfma_f32_16x16x32_bf16 v[116:119],a[144:147],v[228:231],v[116:119]
v_exp_f32 v88,v88
v_mfma_f32_16x16x32_bf16 v[120:123],a[160:163],v[228:231],v[44:47]
v_exp_f32 v89,v89
v_mfma_f32_16x16x32_bf16 v[120:123],a[164:167],v[236:239],v[120:123]
v_exp_f32 v90,v90
v_mfma_f32_16x16x32_bf16 v[120:123],a[168:171],v[244:247],v[120:123]
v_exp_f32 v91,v91
v_mfma_f32_16x16x32_bf16 v[120:123],a[172:175],v[252:255],v[120:123]
v_exp_f32 v92,v92
v_mfma_f32_16x16x32_bf16 v[124:127],a[188:191],v[252:255],v[44:47]
v_exp_f32 v93,v93
v_mfma_f32_16x16x32_bf16 v[124:127],a[184:187],v[244:247],v[124:127]
v_exp_f32 v94,v94
v_exp_f32 v95,v95
v_mfma_f32_16x16x32_bf16 v[124:127],a[180:183],v[236:239],v[124:127]
v_cvt_pk_bf16_f32 v88,v88,v89
v_cvt_pk_bf16_f32 v89,v90,v91
v_cvt_pk_bf16_f32 v90,v92,v93
v_cvt_pk_bf16_f32 v91,v94,v95
v_mfma_f32_16x16x32_bf16 v[124:127],a[176:179],v[228:231],v[124:127]
v_exp_f32 v96,v96
v_mfma_f32_16x16x32_bf16 a[32:35],a[192:195],v[80:83],0
buffer_load_dwordx4 v4,s[20:23],s74 offen offset:1024 nt lds
ds_read_b128 a[132:135],v1 offset:256
v_mfma_f32_16x16x32_bf16 a[32:35],a[196:199],v[88:91],a[32:35]
v_exp_f32 v97,v97
v_mfma_f32_16x16x32_bf16 a[36:39],a[200:203],v[80:83],0
ds_read_b128 a[136:139],v1 offset:512
v_cvt_pk_bf16_f32 v96,v96,v97
v_mfma_f32_16x16x32_bf16 a[36:39],a[204:207],v[88:91],a[36:39]
v_exp_f32 v98,v98
v_mfma_f32_16x16x32_bf16 a[40:43],a[208:211],v[80:83],0
v_exp_f32 v99,v99
v_mfma_f32_16x16x32_bf16 a[40:43],a[212:215],v[88:91],a[40:43]
ds_read_b128 a[140:143],v1 offset:768
v_cvt_pk_bf16_f32 v97,v98,v99
v_mfma_f32_16x16x32_bf16 a[44:47],a[216:219],v[80:83],0
v_exp_f32 v100,v100
v_mfma_f32_16x16x32_bf16 a[44:47],a[220:223],v[88:91],a[44:47]
v_exp_f32 v101,v101
v_mfma_f32_16x16x32_bf16 a[48:51],a[224:227],v[80:83],0
ds_read_b128 a[156:159],v1 offset:4864
v_cvt_pk_bf16_f32 v98,v100,v101
v_mfma_f32_16x16x32_bf16 a[48:51],a[228:231],v[88:91],a[48:51]
v_exp_f32 v102,v102
v_mfma_f32_16x16x32_bf16 a[52:55],a[232:235],v[80:83],0
v_exp_f32 v103,v103
v_mfma_f32_16x16x32_bf16 a[52:55],a[236:239],v[88:91],a[52:55]
ds_read_b128 a[152:155],v1 offset:4608
v_cvt_pk_bf16_f32 v99,v102,v103
v_mfma_f32_16x16x32_bf16 a[56:59],a[240:243],v[80:83],0
ds_read_b128 a[148:151],v1 offset:4352
v_mfma_f32_16x16x32_bf16 a[56:59],a[244:247],v[88:91],a[56:59]
ds_read_b128 a[144:147],v1 offset:4096
v_mfma_f32_16x16x32_bf16 a[60:63],a[248:251],v[80:83],0
buffer_load_dwordx4 v5,s[20:23],s74 offen offset:2048 nt lds
ds_read_b128 a[160:163],v1 offset:8192
v_mfma_f32_16x16x32_bf16 a[60:63],a[252:255],v[88:91],a[60:63]
v_exp_f32 v104,v104
v_mfma_f32_16x16x32_bf16 v[52:55],v[28:31],v[80:83],0
ds_read_b128 a[164:167],v1 offset:8448
v_mfma_f32_16x16x32_bf16 v[52:55],v[28:31],v[88:91],v[52:55]
s_waitcnt lgkmcnt(5)
ds_read_b128 a[168:171],v1 offset:8704
v_mfma_f32_16x16x32_bf16 v[64:67],a[128:131],v[132:135],v[32:35]
ds_read_b128 a[172:175],v1 offset:8960
v_mfma_f32_16x16x32_bf16 v[64:67],a[132:135],v[140:143],v[64:67]
v_exp_f32 v105,v105
v_mfma_f32_16x16x32_bf16 v[64:67],a[136:139],v[148:151],v[64:67]
ds_read_b128 a[188:191],v1 offset:13056
v_cvt_pk_bf16_f32 v104,v104,v105
v_mfma_f32_16x16x32_bf16 v[64:67],a[140:143],v[156:159],v[64:67]
ds_read_b128 a[184:187],v1 offset:12800
v_mfma_f32_16x16x32_bf16 v[68:71],a[156:159],v[156:159],v[32:35]
s_waitcnt lgkmcnt(5)
ds_read_b128 a[180:183],v1 offset:12544
v_mfma_f32_16x16x32_bf16 v[68:71],a[152:155],v[148:151],v[68:71]
v_exp_f32 v106,v106
v_mfma_f32_16x16x32_bf16 v[68:71],a[148:151],v[140:143],v[68:71]
ds_read_b128 a[176:179],v1 offset:12288
v_add_u32 v1,s78,v1
v_mfma_f32_16x16x32_bf16 v[68:71],a[144:147],v[132:135],v[68:71]
v_exp_f32 v107,v107
v_mfma_f32_16x16x32_bf16 v[72:75],a[160:163],v[132:135],v[32:35]
s_waitcnt lgkmcnt(4)
v_exp_f32 v108,v108
v_mfma_f32_16x16x32_bf16 v[72:75],a[164:167],v[140:143],v[72:75]
v_exp_f32 v109,v109
v_mfma_f32_16x16x32_bf16 v[72:75],a[168:171],v[148:151],v[72:75]
buffer_load_dwordx4 v5,s[20:23],s74 offen offset:3072 lds
v_cvt_pk_bf16_f32 v105,v106,v107
v_mfma_f32_16x16x32_bf16 v[72:75],a[172:175],v[156:159],v[72:75]
s_waitcnt lgkmcnt(2)
v_exp_f32 v110,v110
v_mfma_f32_16x16x32_bf16 v[76:79],a[188:191],v[156:159],v[32:35]
v_exp_f32 v111,v111
v_mfma_f32_16x16x32_bf16 v[76:79],a[184:187],v[148:151],v[76:79]
s_waitcnt lgkmcnt(1)
v_cvt_pk_bf16_f32 v106,v108,v109
v_cvt_pk_bf16_f32 v107,v110,v111
v_mfma_f32_16x16x32_bf16 v[76:79],a[180:183],v[140:143],v[76:79]
s_waitcnt lgkmcnt(0)
v_exp_f32 v112,v112
v_mfma_f32_16x16x32_bf16 v[76:79],a[176:179],v[132:135],v[76:79]
v_exp_f32 v113,v113
v_mfma_f32_16x16x32_bf16 a[64:67],a[192:195],v[96:99],0
v_exp_f32 v114,v114
v_mfma_f32_16x16x32_bf16 a[64:67],a[196:199],v[104:107],a[64:67]
v_exp_f32 v115,v115
v_mfma_f32_16x16x32_bf16 a[68:71],a[200:203],v[96:99],0
v_exp_f32 v116,v116
v_mfma_f32_16x16x32_bf16 a[68:71],a[204:207],v[104:107],a[68:71]
v_exp_f32 v117,v117
v_mfma_f32_16x16x32_bf16 a[72:75],a[208:211],v[96:99],0
v_exp_f32 v118,v118
v_mfma_f32_16x16x32_bf16 a[72:75],a[212:215],v[104:107],a[72:75]
v_exp_f32 v119,v119
v_mfma_f32_16x16x32_bf16 a[76:79],a[216:219],v[96:99],0
s_add_u32 m0,s76,16384
v_cvt_pk_bf16_f32 v112,v112,v113
v_cvt_pk_bf16_f32 v113,v114,v115
v_mfma_f32_16x16x32_bf16 a[76:79],a[220:223],v[104:107],a[76:79]
s_sub_u32 s76,s76,32768
v_cvt_pk_bf16_f32 v114,v116,v117
v_cvt_pk_bf16_f32 v115,v118,v119
v_mfma_f32_16x16x32_bf16 a[80:83],a[224:227],v[96:99],0
s_and_b32 s76,s76,0x1ffff
buffer_load_dwordx4 v4,s[24:27],s74 offen lds
v_mfma_f32_16x16x32_bf16 a[80:83],a[228:231],v[104:107],a[80:83]
v_exp_f32 v120,v120
v_mfma_f32_16x16x32_bf16 a[84:87],a[232:235],v[96:99],0
v_exp_f32 v121,v121
v_mfma_f32_16x16x32_bf16 a[84:87],a[236:239],v[104:107],a[84:87]
v_exp_f32 v122,v122
v_mfma_f32_16x16x32_bf16 a[88:91],a[240:243],v[96:99],0
v_exp_f32 v123,v123
v_mfma_f32_16x16x32_bf16 a[88:91],a[244:247],v[104:107],a[88:91]
v_exp_f32 v124,v124
v_mfma_f32_16x16x32_bf16 a[92:95],a[248:251],v[96:99],0
v_exp_f32 v125,v125
v_mfma_f32_16x16x32_bf16 a[92:95],a[252:255],v[104:107],a[92:95]
v_exp_f32 v126,v126
v_mfma_f32_16x16x32_bf16 v[56:59],v[28:31],v[96:99],0
v_exp_f32 v127,v127
v_mfma_f32_16x16x32_bf16 v[56:59],v[28:31],v[104:107],v[56:59]
v_cvt_pk_bf16_f32 v120,v120,v121
v_cvt_pk_bf16_f32 v121,v122,v123
v_mfma_f32_16x16x32_bf16 v[80:83],a[128:131],v[164:167],v[36:39]
v_cvt_pk_bf16_f32 v122,v124,v125
v_cvt_pk_bf16_f32 v123,v126,v127
v_mfma_f32_16x16x32_bf16 v[80:83],a[132:135],v[172:175],v[80:83]
v_exp_f32 v64,v64
v_mfma_f32_16x16x32_bf16 v[80:83],a[136:139],v[180:183],v[80:83]
v_exp_f32 v65,v65
v_mfma_f32_16x16x32_bf16 v[80:83],a[140:143],v[188:191],v[80:83]
v_exp_f32 v66,v66
v_mfma_f32_16x16x32_bf16 v[84:87],a[156:159],v[188:191],v[36:39]
v_exp_f32 v67,v67
v_mfma_f32_16x16x32_bf16 v[84:87],a[152:155],v[180:183],v[84:87]
buffer_load_dwordx4 v5,s[24:27],s74 offen offset:1024 lds
v_cvt_pk_bf16_f32 v64,v64,v65
v_mfma_f32_16x16x32_bf16 v[84:87],a[148:151],v[172:175],v[84:87]
v_exp_f32 v68,v68
v_mfma_f32_16x16x32_bf16 v[84:87],a[144:147],v[164:167],v[84:87]
v_exp_f32 v69,v69
v_mfma_f32_16x16x32_bf16 v[88:91],a[160:163],v[164:167],v[36:39]
v_exp_f32 v70,v70
v_mfma_f32_16x16x32_bf16 v[88:91],a[164:167],v[172:175],v[88:91]
v_exp_f32 v71,v71
v_mfma_f32_16x16x32_bf16 v[88:91],a[168:171],v[180:183],v[88:91]
v_cvt_pk_bf16_f32 v65,v66,v67
v_cvt_pk_bf16_f32 v66,v68,v69
v_mfma_f32_16x16x32_bf16 v[88:91],a[172:175],v[188:191],v[88:91]
v_exp_f32 v72,v72
v_mfma_f32_16x16x32_bf16 v[92:95],a[188:191],v[188:191],v[36:39]
v_exp_f32 v73,v73
v_mfma_f32_16x16x32_bf16 v[92:95],a[184:187],v[180:183],v[92:95]
v_cvt_pk_bf16_f32 v67,v70,v71
v_cvt_pk_bf16_f32 v72,v72,v73
v_mfma_f32_16x16x32_bf16 v[92:95],a[180:183],v[172:175],v[92:95]
v_exp_f32 v74,v74
v_mfma_f32_16x16x32_bf16 v[92:95],a[176:179],v[164:167],v[92:95]
v_exp_f32 v75,v75
v_mfma_f32_16x16x32_bf16 a[96:99],a[192:195],v[112:115],0
ds_read_b64_tr_b16 a[192:193],v2
ds_read_b64_tr_b16 a[194:195],v2 offset:4096
v_mfma_f32_16x16x32_bf16 a[96:99],a[196:199],v[120:123],a[96:99]
ds_read_b64_tr_b16 a[196:197],v2 offset:8192
ds_read_b64_tr_b16 a[198:199],v2 offset:12288
v_mfma_f32_16x16x32_bf16 a[100:103],a[200:203],v[112:115],0
ds_read_b64_tr_b16 a[200:201],v3
ds_read_b64_tr_b16 a[202:203],v3 offset:4096
v_mfma_f32_16x16x32_bf16 a[100:103],a[204:207],v[120:123],a[100:103]
buffer_load_dwordx4 v4,s[24:27],s74 offen offset:2048 lds
ds_read_b64_tr_b16 a[204:205],v3 offset:8192
v_mfma_f32_16x16x32_bf16 a[104:107],a[208:211],v[112:115],0
ds_read_b64_tr_b16 a[206:207],v3 offset:12288
ds_read_b64_tr_b16 a[208:209],v2 offset:256
v_mfma_f32_16x16x32_bf16 a[104:107],a[212:215],v[120:123],a[104:107]
ds_read_b64_tr_b16 a[210:211],v2 offset:4352
ds_read_b64_tr_b16 a[212:213],v2 offset:8448
v_mfma_f32_16x16x32_bf16 a[108:111],a[216:219],v[112:115],0
ds_read_b64_tr_b16 a[214:215],v2 offset:12544
v_cvt_pk_bf16_f32 v73,v74,v75
v_mfma_f32_16x16x32_bf16 a[108:111],a[220:223],v[120:123],a[108:111]
ds_read_b64_tr_b16 a[216:217],v3 offset:256
ds_read_b64_tr_b16 a[218:219],v3 offset:4352
v_mfma_f32_16x16x32_bf16 a[112:115],a[224:227],v[112:115],0
v_exp_f32 v76,v76
v_mfma_f32_16x16x32_bf16 a[112:115],a[228:231],v[120:123],a[112:115]
v_exp_f32 v77,v77
v_mfma_f32_16x16x32_bf16 a[116:119],a[232:235],v[112:115],0
v_exp_f32 v78,v78
v_mfma_f32_16x16x32_bf16 a[116:119],a[236:239],v[120:123],a[116:119]
v_exp_f32 v79,v79
v_mfma_f32_16x16x32_bf16 a[120:123],a[240:243],v[112:115],0
ds_read_b64_tr_b16 a[220:221],v3 offset:8448
ds_read_b64_tr_b16 a[222:223],v3 offset:12544
v_mfma_f32_16x16x32_bf16 a[120:123],a[244:247],v[120:123],a[120:123]
v_cvt_pk_bf16_f32 v74,v76,v77
)A"
R"A(v_cvt_pk_bf16_f32 v75,v78,v79
v_mfma_f32_16x16x32_bf16 a[124:127],a[248:251],v[112:115],0
ds_read_b64_tr_b16 a[224:225],v2 offset:512
ds_read_b64_tr_b16 a[226:227],v2 offset:4608
v_mfma_f32_16x16x32_bf16 a[124:127],a[252:255],v[120:123],a[124:127]
ds_read_b64_tr_b16 a[228:229],v2 offset:8704
ds_read_b64_tr_b16 a[230:231],v2 offset:12800
v_mfma_f32_16x16x32_bf16 v[60:63],v[28:31],v[112:115],0
ds_read_b64_tr_b16 a[232:233],v3 offset:512
ds_read_b64_tr_b16 a[234:235],v3 offset:4608
v_mfma_f32_16x16x32_bf16 v[60:63],v[28:31],v[120:123],v[60:63]
buffer_load_dwordx4 v5,s[24:27],s74 offen offset:3072 lds
ds_read_b64_tr_b16 a[236:237],v3 offset:8704
v_mfma_f32_16x16x32_bf16 v[96:99],a[128:131],v[196:199],v[40:43]
s_add_u32 s74,s74,16384
s_and_b32 s74,s74,0x3ffff
s_or_b32 s74,s74,s69
s_lshl_b32 s69,s60,13
s_add_u32 s69,s69,131072
v_add_u32 v8,s69,v6
v_mfma_f32_16x16x32_bf16 v[96:99],a[132:135],v[204:207],v[96:99]
ds_write_b128 v8,v[24:27] offset:4096
ds_write_b32 v8,v7 offset:5120
v_mfma_f32_16x16x32_bf16 v[96:99],a[136:139],v[212:215],v[96:99]
s_lshl_b32 s69,s60,13
ds_read_b64_tr_b16 a[238:239],v3 offset:12800
ds_read_b64_tr_b16 a[240:241],v2 offset:768
v_mfma_f32_16x16x32_bf16 v[96:99],a[140:143],v[220:223],v[96:99]
v_bfe_u32 v24,v6,4,4
v_lshrrev_b32 v7,8,v6
v_lshlrev_b32 v7,4,v7
v_lshl_add_u32 v7,v24,8,v7
v_mfma_f32_16x16x32_bf16 v[100:103],a[156:159],v[220:223],v[40:43]
s_add_u32 s69,s69,131072
buffer_load_dwordx4 v[8:11],v7,s[20:23],s65 offen nt
ds_read_b64_tr_b16 a[242:243],v2 offset:4864
v_mfma_f32_16x16x32_bf16 v[100:103],a[152:155],v[212:215],v[100:103]
ds_read_b64_tr_b16 a[244:245],v2 offset:8960
ds_read_b64_tr_b16 a[246:247],v2 offset:13056
v_mfma_f32_16x16x32_bf16 v[100:103],a[148:151],v[204:207],v[100:103]
ds_read_b64_tr_b16 a[248:249],v3 offset:768
ds_read_b64_tr_b16 a[250:251],v3 offset:4864
v_mfma_f32_16x16x32_bf16 v[100:103],a[144:147],v[196:199],v[100:103]
ds_read_b64_tr_b16 a[252:253],v3 offset:8960
ds_read_b64_tr_b16 a[254:255],v3 offset:13056
v_mfma_f32_16x16x32_bf16 v[104:107],a[160:163],v[196:199],v[40:43]
v_exp_f32 v80,v80
v_mfma_f32_16x16x32_bf16 v[104:107],a[164:167],v[204:207],v[104:107]
buffer_load_dwordx4 v[12:15],v7,s[20:23],s65 offen offset:64 nt
v_add_u32 v2,s78,v2
v_mfma_f32_16x16x32_bf16 v[104:107],a[168:171],v[212:215],v[104:107]
v_exp_f32 v81,v81
v_mfma_f32_16x16x32_bf16 v[104:107],a[172:175],v[220:223],v[104:107]
s_add_u32 m0,s76,0
v_exp_f32 v82,v82
v_mfma_f32_16x16x32_bf16 v[108:111],a[188:191],v[220:223],v[40:43]
v_exp_f32 v83,v83
v_mfma_f32_16x16x32_bf16 v[108:111],a[184:187],v[212:215],v[108:111]
v_exp_f32 v84,v84
v_mfma_f32_16x16x32_bf16 v[108:111],a[180:183],v[204:207],v[108:111]
buffer_load_dwordx4 v[16:19],v7,s[20:23],s65 offen offset:128 nt
v_cvt_pk_bf16_f32 v80,v80,v81
v_mfma_f32_16x16x32_bf16 v[108:111],a[176:179],v[196:199],v[108:111]
s_waitcnt lgkmcnt(0)
v_exp_f32 v85,v85
v_mfma_f32_16x16x32_bf16 a[0:3],a[192:195],v[64:67],a[0:3]
v_exp_f32 v86,v86
v_mfma_f32_16x16x32_bf16 a[0:3],a[196:199],v[72:75],a[0:3]
v_exp_f32 v87,v87
v_mfma_f32_16x16x32_bf16 a[4:7],a[200:203],v[64:67],a[4:7]
v_cvt_pk_bf16_f32 v81,v82,v83
v_cvt_pk_bf16_f32 v82,v84,v85
v_mfma_f32_16x16x32_bf16 a[4:7],a[204:207],v[72:75],a[4:7]
buffer_load_dwordx4 v[20:23],v7,s[20:23],s65 offen offset:192 nt
v_cvt_pk_bf16_f32 v83,v86,v87
v_mfma_f32_16x16x32_bf16 a[8:11],a[208:211],v[64:67],a[8:11]
v_exp_f32 v88,v88
v_mfma_f32_16x16x32_bf16 a[8:11],a[212:215],v[72:75],a[8:11]
v_exp_f32 v89,v89
v_mfma_f32_16x16x32_bf16 a[12:15],a[216:219],v[64:67],a[12:15]
v_exp_f32 v90,v90
v_mfma_f32_16x16x32_bf16 a[12:15],a[220:223],v[72:75],a[12:15]
v_exp_f32 v91,v91
v_mfma_f32_16x16x32_bf16 a[16:19],a[224:227],v[64:67],a[16:19]
v_exp_f32 v92,v92
v_mfma_f32_16x16x32_bf16 a[16:19],a[228:231],v[72:75],a[16:19]
buffer_load_dwordx4 v4,s[20:23],s74 offen nt lds
v_cvt_pk_bf16_f32 v88,v88,v89
v_mfma_f32_16x16x32_bf16 a[20:23],a[232:235],v[64:67],a[20:23]
v_exp_f32 v93,v93
v_mfma_f32_16x16x32_bf16 a[20:23],a[236:239],v[72:75],a[20:23]
v_exp_f32 v94,v94
v_mfma_f32_16x16x32_bf16 a[24:27],a[240:243],v[64:67],a[24:27]
v_exp_f32 v95,v95
v_mfma_f32_16x16x32_bf16 a[24:27],a[244:247],v[72:75],a[24:27]
v_cvt_pk_bf16_f32 v89,v90,v91
v_cvt_pk_bf16_f32 v90,v92,v93
v_mfma_f32_16x16x32_bf16 a[28:31],a[248:251],v[64:67],a[28:31]
v_cvt_pk_bf16_f32 v91,v94,v95
v_add_u32 v7,s69,v6
v_mfma_f32_16x16x32_bf16 a[28:31],a[252:255],v[72:75],a[28:31]
s_and_b32 s69,s75,0xfffc0000
v_exp_f32 v96,v96
v_mfma_f32_16x16x32_bf16 v[48:51],v[28:31],v[64:67],v[48:51]
s_add_u32 s75,s75,16384
v_exp_f32 v97,v97
v_mfma_f32_16x16x32_bf16 v[48:51],v[28:31],v[72:75],v[48:51]
v_exp_f32 v98,v98
s_sleep 4
s_waitcnt vmcnt(13)
s_barrier
v_mfma_f32_16x16x32_bf16 v[112:115],a[128:131],v[228:231],v[44:47]
s_add_u32 m0,s76,0
ds_read_b128 a[128:131],v1
v_cvt_pk_bf16_f32 v96,v96,v97
v_mfma_f32_16x16x32_bf16 v[112:115],a[132:135],v[236:239],v[112:115]
ds_read_b128 a[132:135],v1 offset:256
v_add_u32 v3,s78,v3
v_mfma_f32_16x16x32_bf16 v[112:115],a[136:139],v[244:247],v[112:115]
s_sub_u32 s78,s77,32768
buffer_load_dwordx4 v4,s[20:23],s74 offen offset:1024 nt lds
ds_read_b128 a[136:139],v1 offset:512
v_mfma_f32_16x16x32_bf16 v[112:115],a[140:143],v[252:255],v[112:115]
s_and_b32 s78,s78,0x1ffff
v_exp_f32 v99,v99
v_mfma_f32_16x16x32_bf16 v[116:119],a[156:159],v[252:255],v[44:47]
L_pd5_%=:
s_cbranch_execz L_trt0_%=
s_sub_u32 s78,s78,s77
ds_read_b128 a[140:143],v1 offset:768
v_cvt_pk_bf16_f32 v97,v98,v99
v_mfma_f32_16x16x32_bf16 v[116:119],a[152:155],v[244:247],v[116:119]
s_add_u32 s77,s77,s78
v_exp_f32 v100,v100
v_mfma_f32_16x16x32_bf16 v[116:119],a[148:151],v[236:239],v[116:119]
v_exp_f32 v101,v101
v_mfma_f32_16x16x32_bf16 v[116:119],a[144:147],v[228:231],v[116:119]
ds_read_b128 a[156:159],v1 offset:4864
v_cvt_pk_bf16_f32 v98,v100,v101
v_mfma_f32_16x16x32_bf16 v[120:123],a[160:163],v[228:231],v[44:47]
v_exp_f32 v102,v102
v_mfma_f32_16x16x32_bf16 v[120:123],a[164:167],v[236:239],v[120:123]
v_exp_f32 v103,v103
v_mfma_f32_16x16x32_bf16 v[120:123],a[168:171],v[244:247],v[120:123]
ds_read_b128 a[152:155],v1 offset:4608
v_cvt_pk_bf16_f32 v99,v102,v103
v_mfma_f32_16x16x32_bf16 v[120:123],a[172:175],v[252:255],v[120:123]
v_exp_f32 v104,v104
v_mfma_f32_16x16x32_bf16 v[124:127],a[188:191],v[252:255],v[44:47]
v_exp_f32 v105,v105
v_mfma_f32_16x16x32_bf16 v[124:127],a[184:187],v[244:247],v[124:127]
ds_read_b128 a[148:151],v1 offset:4352
v_cvt_pk_bf16_f32 v104,v104,v105
v_mfma_f32_16x16x32_bf16 v[124:127],a[180:183],v[236:239],v[124:127]
v_exp_f32 v106,v106
v_mfma_f32_16x16x32_bf16 v[124:127],a[176:179],v[228:231],v[124:127]
v_exp_f32 v107,v107
v_mfma_f32_16x16x32_bf16 a[32:35],a[192:195],v[80:83],a[32:35]
buffer_load_dwordx4 v5,s[20:23],s74 offen offset:2048 nt lds
ds_read_b128 a[144:147],v1 offset:4096
v_mfma_f32_16x16x32_bf16 a[32:35],a[196:199],v[88:91],a[32:35]
ds_read_b128 a[160:163],v1 offset:8192
v_cvt_pk_bf16_f32 v105,v106,v107
v_mfma_f32_16x16x32_bf16 a[36:39],a[200:203],v[80:83],a[36:39]
v_exp_f32 v108,v108
v_mfma_f32_16x16x32_bf16 a[36:39],a[204:207],v[88:91],a[36:39]
v_exp_f32 v109,v109
v_mfma_f32_16x16x32_bf16 a[40:43],a[208:211],v[80:83],a[40:43]
ds_read_b128 a[164:167],v1 offset:8448
v_cvt_pk_bf16_f32 v106,v108,v109
v_mfma_f32_16x16x32_bf16 a[40:43],a[212:215],v[88:91],a[40:43]
v_exp_f32 v110,v110
v_mfma_f32_16x16x32_bf16 a[44:47],a[216:219],v[80:83],a[44:47]
v_exp_f32 v111,v111
v_mfma_f32_16x16x32_bf16 a[44:47],a[220:223],v[88:91],a[44:47]
ds_read_b128 a[168:171],v1 offset:8704
v_cvt_pk_bf16_f32 v107,v110,v111
v_mfma_f32_16x16x32_bf16 a[48:51],a[224:227],v[80:83],a[48:51]
v_exp_f32 v112,v112
v_mfma_f32_16x16x32_bf16 a[48:51],a[228:231],v[88:91],a[48:51]
v_exp_f32 v113,v113
v_mfma_f32_16x16x32_bf16 a[52:55],a[232:235],v[80:83],a[52:55]
ds_read_b128 a[172:175],v1 offset:8960
v_cvt_pk_bf16_f32 v112,v112,v113
v_mfma_f32_16x16x32_bf16 a[52:55],a[236:239],v[88:91],a[52:55]
v_exp_f32 v114,v114
v_mfma_f32_16x16x32_bf16 a[56:59],a[240:243],v[80:83],a[56:59]
v_exp_f32 v115,v115
v_mfma_f32_16x16x32_bf16 a[56:59],a[244:247],v[88:91],a[56:59]
ds_read_b128 a[188:191],v1 offset:13056
v_cvt_pk_bf16_f32 v113,v114,v115
v_mfma_f32_16x16x32_bf16 a[60:63],a[248:251],v[80:83],a[60:63]
buffer_load_dwordx4 v5,s[20:23],s74 offen offset:3072 lds
ds_read_b128 a[184:187],v1 offset:12800
v_mfma_f32_16x16x32_bf16 a[60:63],a[252:255],v[88:91],a[60:63]
v_exp_f32 v116,v116
v_mfma_f32_16x16x32_bf16 v[52:55],v[28:31],v[80:83],v[52:55]
v_exp_f32 v117,v117
v_mfma_f32_16x16x32_bf16 v[52:55],v[28:31],v[88:91],v[52:55]
s_waitcnt lgkmcnt(3)
ds_read_b128 a[180:183],v1 offset:12544
v_cvt_pk_bf16_f32 v114,v116,v117
v_mfma_f32_16x16x32_bf16 v[64:67],a[128:131],v[132:135],v[32:35]
ds_read_b128 a[176:179],v1 offset:12288
v_add_u32 v1,s78,v1
v_mfma_f32_16x16x32_bf16 v[64:67],a[132:135],v[140:143],v[64:67]
v_exp_f32 v118,v118
v_mfma_f32_16x16x32_bf16 v[64:67],a[136:139],v[148:151],v[64:67]
v_exp_f32 v119,v119
v_mfma_f32_16x16x32_bf16 v[64:67],a[140:143],v[156:159],v[64:67]
v_exp_f32 v120,v120
v_mfma_f32_16x16x32_bf16 v[68:71],a[156:159],v[156:159],v[32:35]
v_exp_f32 v121,v121
v_mfma_f32_16x16x32_bf16 v[68:71],a[152:155],v[148:151],v[68:71]
v_cvt_pk_bf16_f32 v115,v118,v119
v_cvt_pk_bf16_f32 v120,v120,v121
v_mfma_f32_16x16x32_bf16 v[68:71],a[148:151],v[140:143],v[68:71]
v_exp_f32 v122,v122
v_mfma_f32_16x16x32_bf16 v[68:71],a[144:147],v[132:135],v[68:71]
v_exp_f32 v123,v123
v_mfma_f32_16x16x32_bf16 v[72:75],a[160:163],v[132:135],v[32:35]
s_add_u32 m0,s76,16384
v_exp_f32 v124,v124
v_mfma_f32_16x16x32_bf16 v[72:75],a[164:167],v[140:143],v[72:75]
s_sub_u32 s76,s76,32768
v_exp_f32 v125,v125
v_mfma_f32_16x16x32_bf16 v[72:75],a[168:171],v[148:151],v[72:75]
s_and_b32 s76,s76,0x1ffff
buffer_load_dwordx4 v4,s[24:27],s74 offen lds
v_cvt_pk_bf16_f32 v121,v122,v123
s_waitcnt lgkmcnt(0)
v_mfma_f32_16x16x32_bf16 v[72:75],a[172:175],v[156:159],v[72:75]
v_exp_f32 v126,v126
v_mfma_f32_16x16x32_bf16 v[76:79],a[188:191],v[156:159],v[32:35]
v_exp_f32 v127,v127
v_mfma_f32_16x16x32_bf16 v[76:79],a[184:187],v[148:151],v[76:79]
v_cvt_pk_bf16_f32 v122,v124,v125
v_cvt_pk_bf16_f32 v123,v126,v127
v_mfma_f32_16x16x32_bf16 v[76:79],a[180:183],v[140:143],v[76:79]
v_exp_f32 v64,v64
v_mfma_f32_16x16x32_bf16 v[76:79],a[176:179],v[132:135],v[76:79]
v_exp_f32 v65,v65
v_mfma_f32_16x16x32_bf16 a[64:67],a[192:195],v[96:99],a[64:67]
v_exp_f32 v66,v66
v_mfma_f32_16x16x32_bf16 a[64:67],a[196:199],v[104:107],a[64:67]
v_exp_f32 v67,v67
v_mfma_f32_16x16x32_bf16 a[68:71],a[200:203],v[96:99],a[68:71]
v_exp_f32 v68,v68
v_mfma_f32_16x16x32_bf16 a[68:71],a[204:207],v[104:107],a[68:71]
v_exp_f32 v69,v69
v_mfma_f32_16x16x32_bf16 a[72:75],a[208:211],v[96:99],a[72:75]
v_exp_f32 v70,v70
v_mfma_f32_16x16x32_bf16 a[72:75],a[212:215],v[104:107],a[72:75]
v_exp_f32 v71,v71
v_mfma_f32_16x16x32_bf16 a[76:79],a[216:219],v[96:99],a[76:79]
v_cvt_pk_bf16_f32 v64,v64,v65
v_cvt_pk_bf16_f32 v65,v66,v67
v_mfma_f32_16x16x32_bf16 a[76:79],a[220:223],v[104:107],a[76:79]
v_cvt_pk_bf16_f32 v66,v68,v69
v_cvt_pk_bf16_f32 v67,v70,v71
v_mfma_f32_16x16x32_bf16 a[80:83],a[224:227],v[96:99],a[80:83]
buffer_load_dwordx4 v5,s[24:27],s74 offen offset:1024 lds
v_mfma_f32_16x16x32_bf16 a[80:83],a[228:231],v[104:107],a[80:83]
v_exp_f32 v72,v72
v_mfma_f32_16x16x32_bf16 a[84:87],a[232:235],v[96:99],a[84:87]
v_exp_f32 v73,v73
v_mfma_f32_16x16x32_bf16 a[84:87],a[236:239],v[104:107],a[84:87]
v_exp_f32 v74,v74
v_mfma_f32_16x16x32_bf16 a[88:91],a[240:243],v[96:99],a[88:91]
v_exp_f32 v75,v75
v_mfma_f32_16x16x32_bf16 a[88:91],a[244:247],v[104:107],a[88:91]
v_exp_f32 v76,v76
v_mfma_f32_16x16x32_bf16 a[92:95],a[248:251],v[96:99],a[92:95]
v_exp_f32 v77,v77
v_mfma_f32_16x16x32_bf16 a[92:95],a[252:255],v[104:107],a[92:95]
v_exp_f32 v78,v78
v_mfma_f32_16x16x32_bf16 v[56:59],v[28:31],v[96:99],v[56:59]
v_exp_f32 v79,v79
v_mfma_f32_16x16x32_bf16 v[56:59],v[28:31],v[104:107],v[56:59]
v_cvt_pk_bf16_f32 v72,v72,v73
v_cvt_pk_bf16_f32 v73,v74,v75
v_mfma_f32_16x16x32_bf16 v[80:83],a[128:131],v[164:167],v[36:39]
v_cvt_pk_bf16_f32 v74,v76,v77
v_cvt_pk_bf16_f32 v75,v78,v79
v_mfma_f32_16x16x32_bf16 v[80:83],a[132:135],v[172:175],v[80:83]
)A"
R"A(v_mfma_f32_16x16x32_bf16 v[80:83],a[136:139],v[180:183],v[80:83]
v_mfma_f32_16x16x32_bf16 v[80:83],a[140:143],v[188:191],v[80:83]
v_mfma_f32_16x16x32_bf16 v[84:87],a[156:159],v[188:191],v[36:39]
v_mfma_f32_16x16x32_bf16 v[84:87],a[152:155],v[180:183],v[84:87]
buffer_load_dwordx4 v4,s[24:27],s74 offen offset:2048 lds
v_mfma_f32_16x16x32_bf16 v[84:87],a[148:151],v[172:175],v[84:87]
v_mfma_f32_16x16x32_bf16 v[84:87],a[144:147],v[164:167],v[84:87]
v_mfma_f32_16x16x32_bf16 v[88:91],a[160:163],v[164:167],v[36:39]
v_mfma_f32_16x16x32_bf16 v[88:91],a[164:167],v[172:175],v[88:91]
v_mfma_f32_16x16x32_bf16 v[88:91],a[168:171],v[180:183],v[88:91]
v_exp_f32 v80,v80
v_mfma_f32_16x16x32_bf16 v[88:91],a[172:175],v[188:191],v[88:91]
v_exp_f32 v81,v81
v_mfma_f32_16x16x32_bf16 v[92:95],a[188:191],v[188:191],v[36:39]
v_exp_f32 v82,v82
v_mfma_f32_16x16x32_bf16 v[92:95],a[184:187],v[180:183],v[92:95]
v_exp_f32 v83,v83
v_mfma_f32_16x16x32_bf16 v[92:95],a[180:183],v[172:175],v[92:95]
v_exp_f32 v84,v84
v_mfma_f32_16x16x32_bf16 v[92:95],a[176:179],v[164:167],v[92:95]
v_exp_f32 v85,v85
v_mfma_f32_16x16x32_bf16 a[96:99],a[192:195],v[112:115],a[96:99]
ds_read_b64_tr_b16 a[192:193],v2
ds_read_b64_tr_b16 a[194:195],v2 offset:4096
v_mfma_f32_16x16x32_bf16 a[96:99],a[196:199],v[120:123],a[96:99]
ds_read_b64_tr_b16 a[196:197],v2 offset:8192
ds_read_b64_tr_b16 a[198:199],v2 offset:12288
v_mfma_f32_16x16x32_bf16 a[100:103],a[200:203],v[112:115],a[100:103]
ds_read_b64_tr_b16 a[200:201],v3
ds_read_b64_tr_b16 a[202:203],v3 offset:4096
v_mfma_f32_16x16x32_bf16 a[100:103],a[204:207],v[120:123],a[100:103]
buffer_load_dwordx4 v5,s[24:27],s74 offen offset:3072 lds
ds_read_b64_tr_b16 a[204:205],v3 offset:8192
v_mfma_f32_16x16x32_bf16 a[104:107],a[208:211],v[112:115],a[104:107]
s_add_u32 s74,s74,16384
s_and_b32 s74,s74,0x3ffff
s_or_b32 s74,s74,s69
v_mfma_f32_16x16x32_bf16 a[104:107],a[212:215],v[120:123],a[104:107]
s_and_b32 s69,s75,0xfffc0000
ds_read_b64_tr_b16 a[206:207],v3 offset:12288
ds_read_b64_tr_b16 a[208:209],v2 offset:256
v_mfma_f32_16x16x32_bf16 a[108:111],a[216:219],v[112:115],a[108:111]
ds_read_b64_tr_b16 a[210:211],v2 offset:4352
ds_read_b64_tr_b16 a[212:213],v2 offset:8448
v_mfma_f32_16x16x32_bf16 a[108:111],a[220:223],v[120:123],a[108:111]
ds_read_b64_tr_b16 a[214:215],v2 offset:12544
ds_read_b64_tr_b16 a[216:217],v3 offset:256
v_mfma_f32_16x16x32_bf16 a[112:115],a[224:227],v[112:115],a[112:115]
ds_read_b64_tr_b16 a[218:219],v3 offset:4352
ds_read_b64_tr_b16 a[220:221],v3 offset:8448
v_mfma_f32_16x16x32_bf16 a[112:115],a[228:231],v[120:123],a[112:115]
ds_read_b64_tr_b16 a[222:223],v3 offset:12544
v_cvt_pk_bf16_f32 v80,v80,v81
v_mfma_f32_16x16x32_bf16 a[116:119],a[232:235],v[112:115],a[116:119]
ds_read_b64_tr_b16 a[224:225],v2 offset:512
ds_read_b64_tr_b16 a[226:227],v2 offset:4608
v_mfma_f32_16x16x32_bf16 a[116:119],a[236:239],v[120:123],a[116:119]
ds_read_b64_tr_b16 a[228:229],v2 offset:8704
v_cvt_pk_bf16_f32 v81,v82,v83
v_mfma_f32_16x16x32_bf16 a[120:123],a[240:243],v[112:115],a[120:123]
ds_read_b64_tr_b16 a[230:231],v2 offset:12800
ds_read_b64_tr_b16 a[232:233],v3 offset:512
v_mfma_f32_16x16x32_bf16 a[120:123],a[244:247],v[120:123],a[120:123]
ds_read_b64_tr_b16 a[234:235],v3 offset:4608
v_cvt_pk_bf16_f32 v82,v84,v85
v_mfma_f32_16x16x32_bf16 a[124:127],a[248:251],v[112:115],a[124:127]
ds_read_b64_tr_b16 a[236:237],v3 offset:8704
ds_read_b64_tr_b16 a[238:239],v3 offset:12800
v_mfma_f32_16x16x32_bf16 a[124:127],a[252:255],v[120:123],a[124:127]
s_add_u32 m0,s76,0
v_exp_f32 v86,v86
v_mfma_f32_16x16x32_bf16 v[60:63],v[28:31],v[112:115],v[60:63]
ds_read_b64_tr_b16 a[240:241],v2 offset:768
ds_read_b64_tr_b16 a[242:243],v2 offset:4864
v_mfma_f32_16x16x32_bf16 v[60:63],v[28:31],v[120:123],v[60:63]
buffer_load_dwordx4 v4,s[20:23],s74 offen nt lds
ds_read_b64_tr_b16 a[244:245],v2 offset:8960
v_mfma_f32_16x16x32_bf16 v[96:99],a[128:131],v[196:199],v[40:43]
ds_read_b64_tr_b16 a[246:247],v2 offset:13056
ds_read_b64_tr_b16 a[248:249],v3 offset:768
v_mfma_f32_16x16x32_bf16 v[96:99],a[132:135],v[204:207],v[96:99]
ds_read_b64_tr_b16 a[250:251],v3 offset:4864
ds_read_b64_tr_b16 a[252:253],v3 offset:8960
v_mfma_f32_16x16x32_bf16 v[96:99],a[136:139],v[212:215],v[96:99]
ds_read_b64_tr_b16 a[254:255],v3 offset:13056
v_add_u32 v2,s78,v2
v_mfma_f32_16x16x32_bf16 v[96:99],a[140:143],v[220:223],v[96:99]
v_exp_f32 v87,v87
v_mfma_f32_16x16x32_bf16 v[100:103],a[156:159],v[220:223],v[40:43]
v_cvt_pk_bf16_f32 v83,v86,v87
v_add_u32 v3,s78,v3
v_mfma_f32_16x16x32_bf16 v[100:103],a[152:155],v[212:215],v[100:103]
s_sub_u32 s78,s77,32768
v_exp_f32 v88,v88
v_mfma_f32_16x16x32_bf16 v[100:103],a[148:151],v[204:207],v[100:103]
s_and_b32 s78,s78,0x1ffff
v_exp_f32 v89,v89
v_mfma_f32_16x16x32_bf16 v[100:103],a[144:147],v[196:199],v[100:103]
s_sub_u32 s78,s78,s77
v_exp_f32 v90,v90
v_mfma_f32_16x16x32_bf16 v[104:107],a[160:163],v[196:199],v[40:43]
s_add_u32 s77,s77,s78
v_exp_f32 v91,v91
v_mfma_f32_16x16x32_bf16 v[104:107],a[164:167],v[204:207],v[104:107]
v_exp_f32 v92,v92
v_mfma_f32_16x16x32_bf16 v[104:107],a[168:171],v[212:215],v[104:107]
v_exp_f32 v93,v93
v_mfma_f32_16x16x32_bf16 v[104:107],a[172:175],v[220:223],v[104:107]
v_exp_f32 v94,v94
v_mfma_f32_16x16x32_bf16 v[108:111],a[188:191],v[220:223],v[40:43]
v_exp_f32 v95,v95
v_mfma_f32_16x16x32_bf16 v[108:111],a[184:187],v[212:215],v[108:111]
buffer_load_dwordx4 v4,s[20:23],s74 offen offset:1024 nt lds
v_cvt_pk_bf16_f32 v88,v88,v89
v_mfma_f32_16x16x32_bf16 v[108:111],a[180:183],v[204:207],v[108:111]
v_cvt_pk_bf16_f32 v89,v90,v91
v_cvt_pk_bf16_f32 v90,v92,v93
v_mfma_f32_16x16x32_bf16 v[108:111],a[176:179],v[196:199],v[108:111]
s_waitcnt lgkmcnt(0)
v_exp_f32 v96,v96
v_mfma_f32_16x16x32_bf16 a[0:3],a[192:195],v[64:67],a[0:3]
v_exp_f32 v97,v97
v_mfma_f32_16x16x32_bf16 a[0:3],a[196:199],v[72:75],a[0:3]
v_cvt_pk_bf16_f32 v91,v94,v95
v_cvt_pk_bf16_f32 v96,v96,v97
v_mfma_f32_16x16x32_bf16 a[4:7],a[200:203],v[64:67],a[4:7]
v_exp_f32 v98,v98
v_mfma_f32_16x16x32_bf16 a[4:7],a[204:207],v[72:75],a[4:7]
v_exp_f32 v99,v99
v_mfma_f32_16x16x32_bf16 a[8:11],a[208:211],v[64:67],a[8:11]
v_exp_f32 v100,v100
v_mfma_f32_16x16x32_bf16 a[8:11],a[212:215],v[72:75],a[8:11]
v_exp_f32 v101,v101
v_mfma_f32_16x16x32_bf16 a[12:15],a[216:219],v[64:67],a[12:15]
v_exp_f32 v102,v102
v_mfma_f32_16x16x32_bf16 a[12:15],a[220:223],v[72:75],a[12:15]
v_exp_f32 v103,v103
v_mfma_f32_16x16x32_bf16 a[16:19],a[224:227],v[64:67],a[16:19]
v_cvt_pk_bf16_f32 v97,v98,v99
v_cvt_pk_bf16_f32 v98,v100,v101
v_mfma_f32_16x16x32_bf16 a[16:19],a[228:231],v[72:75],a[16:19]
v_exp_f32 v104,v104
v_mfma_f32_16x16x32_bf16 a[20:23],a[232:235],v[64:67],a[20:23]
s_getreg_b32 s92,hwreg(HW_REG_IB_STS,0,4)
s_min_u32 s4,s92,s6
s_mov_b32 s6,s92
v_mfma_f32_16x16x32_bf16 a[20:23],a[236:239],v[72:75],a[20:23]
s_cmp_ge_u32 s4,9
s_cselect_b32 s4,14,s5
v_cvt_pk_bf16_f32 v99,v102,v103
v_mfma_f32_16x16x32_bf16 a[24:27],a[240:243],v[64:67],a[24:27]
v_exp_f32 v105,v105
v_mfma_f32_16x16x32_bf16 a[24:27],a[244:247],v[72:75],a[24:27]
v_exp_f32 v106,v106
v_mfma_f32_16x16x32_bf16 a[28:31],a[248:251],v[64:67],a[28:31]
v_exp_f32 v107,v107
v_mfma_f32_16x16x32_bf16 a[28:31],a[252:255],v[72:75],a[28:31]
s_getpc_b64 s[92:93]
s_cmp_lg_u32 s4,0
s_cbranch_scc1 L_pace_%=
v_mfma_f32_16x16x32_bf16 v[48:51],v[28:31],v[64:67],v[48:51]
v_exp_f32 v108,v108
v_mfma_f32_16x16x32_bf16 v[48:51],v[28:31],v[72:75],v[48:51]
v_exp_f32 v109,v109
s_sleep 4
s_waitcnt vmcnt(10)
s_barrier
v_mfma_f32_16x16x32_bf16 v[24:27],v[8:11],v[128:131],0
s_add_u32 m0,s76,0
v_exp_f32 v110,v110
v_mfma_f32_16x16x32_bf16 v[24:27],v[12:15],v[136:139],v[24:27]
v_exp_f32 v111,v111
v_mfma_f32_16x16x32_bf16 v[24:27],v[16:19],v[144:147],v[24:27]
buffer_load_dwordx4 v5,s[20:23],s74 offen offset:2048 nt lds
v_cvt_pk_bf16_f32 v104,v104,v105
v_mfma_f32_16x16x32_bf16 v[24:27],v[20:23],v[152:155],v[24:27]
v_cvt_pk_bf16_f32 v105,v106,v107
v_cvt_pk_bf16_f32 v106,v108,v109
v_mfma_f32_16x16x32_bf16 v[112:115],a[128:131],v[228:231],v[44:47]
ds_read_b128 a[128:131],v1
v_cvt_pk_bf16_f32 v107,v110,v111
v_mfma_f32_16x16x32_bf16 v[112:115],a[132:135],v[236:239],v[112:115]
ds_read_b128 a[132:135],v1 offset:256
v_mfma_f32_16x16x32_bf16 v[112:115],a[136:139],v[244:247],v[112:115]
v_max3_f32 v24,v24,v25,v26
v_max_f32 v24,v24,v27
v_mfma_f32_16x16x32_bf16 v[112:115],a[140:143],v[252:255],v[112:115]
ds_read_b128 a[136:139],v1 offset:512
v_mov_b32 v25,v24
v_mfma_f32_16x16x32_bf16 v[116:119],a[156:159],v[252:255],v[44:47]
ds_read_b128 a[140:143],v1 offset:768
v_mfma_f32_16x16x32_bf16 v[116:119],a[152:155],v[244:247],v[116:119]
v_permlane32_swap_b32 v24,v25
v_mfma_f32_16x16x32_bf16 v[116:119],a[148:151],v[236:239],v[116:119]
v_max_f32 v24,v24,v25
v_mov_b32 v25,v24
v_mfma_f32_16x16x32_bf16 v[116:119],a[144:147],v[228:231],v[116:119]
v_exp_f32 v112,v112
v_mfma_f32_16x16x32_bf16 v[120:123],a[160:163],v[228:231],v[44:47]
v_permlane16_swap_b32 v24,v25
v_mfma_f32_16x16x32_bf16 v[120:123],a[164:167],v[236:239],v[120:123]
v_max_f32 v24,v24,v25
v_sub_f32 v27,0,v24
v_mfma_f32_16x16x32_bf16 v[120:123],a[168:171],v[244:247],v[120:123]
v_sub_f32 v26,0,v24
v_sub_f32 v25,0,v24
v_mfma_f32_16x16x32_bf16 v[120:123],a[172:175],v[252:255],v[120:123]
ds_read_b128 a[156:159],v1 offset:4864
v_sub_f32 v24,0,v24
v_mfma_f32_16x16x32_bf16 v[124:127],a[188:191],v[252:255],v[44:47]
buffer_load_dwordx4 v5,s[20:23],s74 offen offset:3072 lds
ds_read_b128 a[152:155],v1 offset:4608
v_mfma_f32_16x16x32_bf16 v[124:127],a[184:187],v[244:247],v[124:127]
ds_write_b128 v7,v[24:27]
v_mfma_f32_16x16x32_bf16 v[124:127],a[180:183],v[236:239],v[124:127]
v_exp_f32 v113,v113
v_mfma_f32_16x16x32_bf16 v[124:127],a[176:179],v[228:231],v[124:127]
ds_read_b128 a[148:151],v1 offset:4352
v_cvt_pk_bf16_f32 v112,v112,v113
v_mfma_f32_16x16x32_bf16 v[24:27],v[8:11],v[160:163],0
v_exp_f32 v114,v114
v_mfma_f32_16x16x32_bf16 v[24:27],v[12:15],v[168:171],v[24:27]
v_exp_f32 v115,v115
v_mfma_f32_16x16x32_bf16 v[24:27],v[16:19],v[176:179],v[24:27]
ds_read_b128 a[144:147],v1 offset:4096
v_cvt_pk_bf16_f32 v113,v114,v115
v_mfma_f32_16x16x32_bf16 v[24:27],v[20:23],v[184:187],v[24:27]
v_exp_f32 v116,v116
v_mfma_f32_16x16x32_bf16 a[32:35],a[192:195],v[80:83],a[32:35]
v_exp_f32 v117,v117
v_mfma_f32_16x16x32_bf16 a[32:35],a[196:199],v[88:91],a[32:35]
ds_read_b128 a[160:163],v1 offset:8192
v_cvt_pk_bf16_f32 v114,v116,v117
v_mfma_f32_16x16x32_bf16 a[36:39],a[200:203],v[80:83],a[36:39]
v_exp_f32 v118,v118
v_mfma_f32_16x16x32_bf16 a[36:39],a[204:207],v[88:91],a[36:39]
v_max3_f32 v24,v24,v25,v26
v_max_f32 v24,v24,v27
v_mfma_f32_16x16x32_bf16 a[40:43],a[208:211],v[80:83],a[40:43]
s_add_u32 m0,s76,16384
ds_read_b128 a[164:167],v1 offset:8448
v_mov_b32 v25,v24
v_mfma_f32_16x16x32_bf16 a[40:43],a[212:215],v[88:91],a[40:43]
s_sub_u32 s76,s76,32768
v_exp_f32 v119,v119
v_mfma_f32_16x16x32_bf16 a[44:47],a[216:219],v[80:83],a[44:47]
s_and_b32 s76,s76,0x1ffff
buffer_load_dwordx4 v4,s[24:27],s74 offen lds
ds_read_b128 a[168:171],v1 offset:8704
v_mfma_f32_16x16x32_bf16 a[44:47],a[220:223],v[88:91],a[44:47]
v_permlane32_swap_b32 v24,v25
v_mfma_f32_16x16x32_bf16 a[48:51],a[224:227],v[80:83],a[48:51]
v_max_f32 v24,v24,v25
v_mov_b32 v25,v24
v_mfma_f32_16x16x32_bf16 a[48:51],a[228:231],v[88:91],a[48:51]
ds_read_b128 a[172:175],v1 offset:8960
v_cvt_pk_bf16_f32 v115,v118,v119
v_mfma_f32_16x16x32_bf16 a[52:55],a[232:235],v[80:83],a[52:55]
v_permlane16_swap_b32 v24,v25
v_mfma_f32_16x16x32_bf16 a[52:55],a[236:239],v[88:91],a[52:55]
v_max_f32 v24,v24,v25
v_sub_f32 v27,0,v24
v_mfma_f32_16x16x32_bf16 a[56:59],a[240:243],v[80:83],a[56:59]
v_sub_f32 v26,0,v24
v_sub_f32 v25,0,v24
v_mfma_f32_16x16x32_bf16 a[56:59],a[244:247],v[88:91],a[56:59]
ds_read_b128 a[188:191],v1 offset:13056
v_sub_f32 v24,0,v24
v_mfma_f32_16x16x32_bf16 a[60:63],a[248:251],v[80:83],a[60:63]
ds_write_b128 v7,v[24:27] offset:1024
v_mfma_f32_16x16x32_bf16 a[60:63],a[252:255],v[88:91],a[60:63]
v_exp_f32 v120,v120
v_mfma_f32_16x16x32_bf16 v[52:55],v[28:31],v[80:83],v[52:55]
v_exp_f32 v121,v121
v_mfma_f32_16x16x32_bf16 v[52:55],v[28:31],v[88:91],v[52:55]
ds_read_b128 a[184:187],v1 offset:12800
v_cvt_pk_bf16_f32 v120,v120,v121
)A"
R"A(v_mfma_f32_16x16x32_bf16 v[24:27],v[8:11],v[192:195],0
v_exp_f32 v122,v122
v_mfma_f32_16x16x32_bf16 v[24:27],v[12:15],v[200:203],v[24:27]
v_exp_f32 v123,v123
v_mfma_f32_16x16x32_bf16 v[24:27],v[16:19],v[208:211],v[24:27]
ds_read_b128 a[180:183],v1 offset:12544
v_cvt_pk_bf16_f32 v121,v122,v123
v_mfma_f32_16x16x32_bf16 v[24:27],v[20:23],v[216:219],v[24:27]
s_waitcnt lgkmcnt(3)
buffer_load_dwordx4 v5,s[24:27],s74 offen offset:1024 lds
ds_read_b128 a[176:179],v1 offset:12288
v_mfma_f32_16x16x32_bf16 v[64:67],a[128:131],v[132:135],v[32:35]
v_exp_f32 v124,v124
v_mfma_f32_16x16x32_bf16 v[64:67],a[132:135],v[140:143],v[64:67]
v_exp_f32 v125,v125
v_mfma_f32_16x16x32_bf16 v[64:67],a[136:139],v[148:151],v[64:67]
v_exp_f32 v126,v126
v_mfma_f32_16x16x32_bf16 v[64:67],a[140:143],v[156:159],v[64:67]
v_max3_f32 v24,v24,v25,v26
v_max_f32 v24,v24,v27
v_mfma_f32_16x16x32_bf16 v[68:71],a[156:159],v[156:159],v[32:35]
v_mov_b32 v25,v24
v_cvt_pk_bf16_f32 v122,v124,v125
v_mfma_f32_16x16x32_bf16 v[68:71],a[152:155],v[148:151],v[68:71]
v_exp_f32 v127,v127
v_mfma_f32_16x16x32_bf16 v[68:71],a[148:151],v[140:143],v[68:71]
v_permlane32_swap_b32 v24,v25
v_mfma_f32_16x16x32_bf16 v[68:71],a[144:147],v[132:135],v[68:71]
v_max_f32 v24,v24,v25
v_mov_b32 v25,v24
v_mfma_f32_16x16x32_bf16 v[72:75],a[160:163],v[132:135],v[32:35]
v_cvt_pk_bf16_f32 v123,v126,v127
v_add_u32 v1,s78,v1
v_mfma_f32_16x16x32_bf16 v[72:75],a[164:167],v[140:143],v[72:75]
v_permlane16_swap_b32 v24,v25
v_mfma_f32_16x16x32_bf16 v[72:75],a[168:171],v[148:151],v[72:75]
v_max_f32 v24,v24,v25
v_sub_f32 v27,0,v24
v_mfma_f32_16x16x32_bf16 v[72:75],a[172:175],v[156:159],v[72:75]
v_sub_f32 v26,0,v24
v_sub_f32 v25,0,v24
v_mfma_f32_16x16x32_bf16 v[76:79],a[188:191],v[156:159],v[32:35]
s_waitcnt lgkmcnt(0)
v_exp_f32 v64,v64
v_mfma_f32_16x16x32_bf16 v[76:79],a[184:187],v[148:151],v[76:79]
buffer_load_dwordx4 v4,s[24:27],s74 offen offset:2048 lds
v_sub_f32 v24,0,v24
v_mfma_f32_16x16x32_bf16 v[76:79],a[180:183],v[140:143],v[76:79]
ds_write_b128 v7,v[24:27] offset:2048
v_mfma_f32_16x16x32_bf16 v[76:79],a[176:179],v[132:135],v[76:79]
v_exp_f32 v65,v65
v_mfma_f32_16x16x32_bf16 a[64:67],a[192:195],v[96:99],a[64:67]
v_exp_f32 v66,v66
v_mfma_f32_16x16x32_bf16 a[64:67],a[196:199],v[104:107],a[64:67]
v_exp_f32 v67,v67
v_mfma_f32_16x16x32_bf16 a[68:71],a[200:203],v[96:99],a[68:71]
v_exp_f32 v68,v68
v_mfma_f32_16x16x32_bf16 a[68:71],a[204:207],v[104:107],a[68:71]
v_exp_f32 v69,v69
v_mfma_f32_16x16x32_bf16 a[72:75],a[208:211],v[96:99],a[72:75]
v_exp_f32 v70,v70
v_mfma_f32_16x16x32_bf16 a[72:75],a[212:215],v[104:107],a[72:75]
v_exp_f32 v71,v71
v_mfma_f32_16x16x32_bf16 a[76:79],a[216:219],v[96:99],a[76:79]
v_cvt_pk_bf16_f32 v64,v64,v65
v_cvt_pk_bf16_f32 v65,v66,v67
v_mfma_f32_16x16x32_bf16 a[76:79],a[220:223],v[104:107],a[76:79]
v_cvt_pk_bf16_f32 v66,v68,v69
v_cvt_pk_bf16_f32 v67,v70,v71
v_mfma_f32_16x16x32_bf16 a[80:83],a[224:227],v[96:99],a[80:83]
v_exp_f32 v72,v72
v_mfma_f32_16x16x32_bf16 a[80:83],a[228:231],v[104:107],a[80:83]
v_exp_f32 v73,v73
v_mfma_f32_16x16x32_bf16 a[84:87],a[232:235],v[96:99],a[84:87]
v_exp_f32 v74,v74
v_mfma_f32_16x16x32_bf16 a[84:87],a[236:239],v[104:107],a[84:87]
v_exp_f32 v75,v75
v_mfma_f32_16x16x32_bf16 a[88:91],a[240:243],v[96:99],a[88:91]
buffer_load_dwordx4 v5,s[24:27],s74 offen offset:3072 lds
v_cvt_pk_bf16_f32 v72,v72,v73
v_mfma_f32_16x16x32_bf16 a[88:91],a[244:247],v[104:107],a[88:91]
s_add_u32 s74,s74,16384
s_and_b32 s74,s74,0x3ffff
s_or_b32 s74,s74,s69
v_mfma_f32_16x16x32_bf16 a[92:95],a[248:251],v[96:99],a[92:95]
v_exp_f32 v76,v76
v_mfma_f32_16x16x32_bf16 a[92:95],a[252:255],v[104:107],a[92:95]
v_exp_f32 v77,v77
v_mfma_f32_16x16x32_bf16 v[56:59],v[28:31],v[96:99],v[56:59]
v_exp_f32 v78,v78
v_mfma_f32_16x16x32_bf16 v[56:59],v[28:31],v[104:107],v[56:59]
v_exp_f32 v79,v79
v_mfma_f32_16x16x32_bf16 v[24:27],v[8:11],v[224:227],0
v_cvt_pk_bf16_f32 v73,v74,v75
v_cvt_pk_bf16_f32 v74,v76,v77
v_mfma_f32_16x16x32_bf16 v[24:27],v[12:15],v[232:235],v[24:27]
v_cvt_pk_bf16_f32 v75,v78,v79
v_mfma_f32_16x16x32_bf16 v[24:27],v[16:19],v[240:243],v[24:27]
v_mfma_f32_16x16x32_bf16 v[24:27],v[20:23],v[248:251],v[24:27]
v_mfma_f32_16x16x32_bf16 v[80:83],a[128:131],v[164:167],v[36:39]
v_mfma_f32_16x16x32_bf16 v[80:83],a[132:135],v[172:175],v[80:83]
v_mfma_f32_16x16x32_bf16 v[80:83],a[136:139],v[180:183],v[80:83]
s_add_u32 m0,s76,0
v_mfma_f32_16x16x32_bf16 v[80:83],a[140:143],v[188:191],v[80:83]
v_mfma_f32_16x16x32_bf16 v[84:87],a[156:159],v[188:191],v[36:39]
buffer_load_dwordx4 v4,s[20:23],s74 offen nt lds
v_mfma_f32_16x16x32_bf16 v[84:87],a[152:155],v[180:183],v[84:87]
v_max3_f32 v24,v24,v25,v26
v_max_f32 v24,v24,v27
v_mfma_f32_16x16x32_bf16 v[84:87],a[148:151],v[172:175],v[84:87]
v_mov_b32 v25,v24
v_mfma_f32_16x16x32_bf16 v[84:87],a[144:147],v[164:167],v[84:87]
v_exp_f32 v80,v80
v_mfma_f32_16x16x32_bf16 v[88:91],a[160:163],v[164:167],v[36:39]
v_permlane32_swap_b32 v24,v25
v_mfma_f32_16x16x32_bf16 v[88:91],a[164:167],v[172:175],v[88:91]
v_max_f32 v24,v24,v25
v_mov_b32 v25,v24
v_mfma_f32_16x16x32_bf16 v[88:91],a[168:171],v[180:183],v[88:91]
v_exp_f32 v81,v81
v_mfma_f32_16x16x32_bf16 v[88:91],a[172:175],v[188:191],v[88:91]
v_permlane16_swap_b32 v24,v25
v_mfma_f32_16x16x32_bf16 v[92:95],a[188:191],v[188:191],v[36:39]
v_max_f32 v24,v24,v25
v_max_f32 v24,s72,v24
v_mfma_f32_16x16x32_bf16 v[92:95],a[184:187],v[180:183],v[92:95]
v_sub_f32 v27,0,v24
v_sub_f32 v26,0,v24
v_mfma_f32_16x16x32_bf16 v[92:95],a[180:183],v[172:175],v[92:95]
v_sub_f32 v25,0,v24
v_sub_f32 v24,0,v24
v_mfma_f32_16x16x32_bf16 v[92:95],a[176:179],v[164:167],v[92:95]
v_exp_f32 v82,v82
v_mfma_f32_16x16x32_bf16 a[96:99],a[192:195],v[112:115],a[96:99]
ds_read_b64_tr_b16 a[192:193],v2
ds_read_b64_tr_b16 a[194:195],v2 offset:4096
v_mfma_f32_16x16x32_bf16 a[96:99],a[196:199],v[120:123],a[96:99]
ds_read_b64_tr_b16 a[196:197],v2 offset:8192
ds_read_b64_tr_b16 a[198:199],v2 offset:12288
v_mfma_f32_16x16x32_bf16 a[100:103],a[200:203],v[112:115],a[100:103]
ds_read_b64_tr_b16 a[200:201],v3
ds_read_b64_tr_b16 a[202:203],v3 offset:4096
v_mfma_f32_16x16x32_bf16 a[100:103],a[204:207],v[120:123],a[100:103]
buffer_load_dwordx4 v4,s[20:23],s74 offen offset:1024 nt lds
ds_read_b64_tr_b16 a[204:205],v3 offset:8192
v_mfma_f32_16x16x32_bf16 a[104:107],a[208:211],v[112:115],a[104:107]
ds_read_b64_tr_b16 a[206:207],v3 offset:12288
ds_read_b64_tr_b16 a[208:209],v2 offset:256
v_mfma_f32_16x16x32_bf16 a[104:107],a[212:215],v[120:123],a[104:107]
ds_read_b64_tr_b16 a[210:211],v2 offset:4352
ds_read_b64_tr_b16 a[212:213],v2 offset:8448
v_mfma_f32_16x16x32_bf16 a[108:111],a[216:219],v[112:115],a[108:111]
ds_read_b64_tr_b16 a[214:215],v2 offset:12544
v_cvt_pk_bf16_f32 v80,v80,v81
v_mfma_f32_16x16x32_bf16 a[108:111],a[220:223],v[120:123],a[108:111]
ds_read_b64_tr_b16 a[216:217],v3 offset:256
ds_read_b64_tr_b16 a[218:219],v3 offset:4352
v_mfma_f32_16x16x32_bf16 a[112:115],a[224:227],v[112:115],a[112:115]
v_exp_f32 v83,v83
v_mfma_f32_16x16x32_bf16 a[112:115],a[228:231],v[120:123],a[112:115]
ds_read_b64_tr_b16 a[220:221],v3 offset:8448
ds_read_b64_tr_b16 a[222:223],v3 offset:12544
v_mfma_f32_16x16x32_bf16 a[116:119],a[232:235],v[112:115],a[116:119]
ds_read_b64_tr_b16 a[224:225],v2 offset:512
ds_read_b64_tr_b16 a[226:227],v2 offset:4608
v_mfma_f32_16x16x32_bf16 a[116:119],a[236:239],v[120:123],a[116:119]
ds_read_b64_tr_b16 a[228:229],v2 offset:8704
ds_read_b64_tr_b16 a[230:231],v2 offset:12800
v_mfma_f32_16x16x32_bf16 a[120:123],a[240:243],v[112:115],a[120:123]
ds_read_b64_tr_b16 a[232:233],v3 offset:512
s_nop 0
L_pd6_%=:
s_cbranch_execz L_trt0_%=
v_cvt_pk_bf16_f32 v81,v82,v83
v_mfma_f32_16x16x32_bf16 a[120:123],a[244:247],v[120:123],a[120:123]
ds_read_b64_tr_b16 a[234:235],v3 offset:4608
ds_read_b64_tr_b16 a[236:237],v3 offset:8704
v_mfma_f32_16x16x32_bf16 a[124:127],a[248:251],v[112:115],a[124:127]
v_exp_f32 v84,v84
v_mfma_f32_16x16x32_bf16 a[124:127],a[252:255],v[120:123],a[124:127]
ds_read_b64_tr_b16 a[238:239],v3 offset:12800
ds_read_b64_tr_b16 a[240:241],v2 offset:768
v_mfma_f32_16x16x32_bf16 v[60:63],v[28:31],v[112:115],v[60:63]
ds_read_b64_tr_b16 a[242:243],v2 offset:4864
ds_read_b64_tr_b16 a[244:245],v2 offset:8960
v_mfma_f32_16x16x32_bf16 v[60:63],v[28:31],v[120:123],v[60:63]
buffer_load_dwordx4 v5,s[20:23],s74 offen offset:2048 nt lds
ds_read_b64_tr_b16 a[246:247],v2 offset:13056
v_mfma_f32_16x16x32_bf16 v[96:99],a[128:131],v[196:199],v[40:43]
ds_read_b64_tr_b16 a[248:249],v3 offset:768
ds_read_b64_tr_b16 a[250:251],v3 offset:4864
v_mfma_f32_16x16x32_bf16 v[96:99],a[132:135],v[204:207],v[96:99]
ds_read_b64_tr_b16 a[252:253],v3 offset:8960
ds_read_b64_tr_b16 a[254:255],v3 offset:13056
v_mfma_f32_16x16x32_bf16 v[96:99],a[136:139],v[212:215],v[96:99]
v_exp_f32 v85,v85
v_mfma_f32_16x16x32_bf16 v[96:99],a[140:143],v[220:223],v[96:99]
v_exp_f32 v86,v86
v_mfma_f32_16x16x32_bf16 v[100:103],a[156:159],v[220:223],v[40:43]
v_exp_f32 v87,v87
v_mfma_f32_16x16x32_bf16 v[100:103],a[152:155],v[212:215],v[100:103]
v_exp_f32 v88,v88
v_mfma_f32_16x16x32_bf16 v[100:103],a[148:151],v[204:207],v[100:103]
v_exp_f32 v89,v89
v_mfma_f32_16x16x32_bf16 v[100:103],a[144:147],v[196:199],v[100:103]
v_exp_f32 v90,v90
v_mfma_f32_16x16x32_bf16 v[104:107],a[160:163],v[196:199],v[40:43]
v_exp_f32 v91,v91
v_mfma_f32_16x16x32_bf16 v[104:107],a[164:167],v[204:207],v[104:107]
v_exp_f32 v92,v92
v_mfma_f32_16x16x32_bf16 v[104:107],a[168:171],v[212:215],v[104:107]
v_exp_f32 v93,v93
v_mfma_f32_16x16x32_bf16 v[104:107],a[172:175],v[220:223],v[104:107]
v_exp_f32 v94,v94
v_mfma_f32_16x16x32_bf16 v[108:111],a[188:191],v[220:223],v[40:43]
v_exp_f32 v95,v95
v_mfma_f32_16x16x32_bf16 v[108:111],a[184:187],v[212:215],v[108:111]
v_exp_f32 v96,v96
v_mfma_f32_16x16x32_bf16 v[108:111],a[180:183],v[204:207],v[108:111]
buffer_load_dwordx4 v5,s[20:23],s74 offen offset:3072 lds
v_cvt_pk_bf16_f32 v82,v84,v85
v_mfma_f32_16x16x32_bf16 v[108:111],a[176:179],v[196:199],v[108:111]
s_waitcnt lgkmcnt(0)
v_exp_f32 v97,v97
v_mfma_f32_16x16x32_bf16 a[0:3],a[192:195],v[64:67],a[0:3]
v_exp_f32 v98,v98
v_mfma_f32_16x16x32_bf16 a[0:3],a[196:199],v[72:75],a[0:3]
v_exp_f32 v99,v99
v_mfma_f32_16x16x32_bf16 a[4:7],a[200:203],v[64:67],a[4:7]
v_exp_f32 v100,v100
v_mfma_f32_16x16x32_bf16 a[4:7],a[204:207],v[72:75],a[4:7]
v_exp_f32 v101,v101
v_mfma_f32_16x16x32_bf16 a[8:11],a[208:211],v[64:67],a[8:11]
v_exp_f32 v102,v102
v_mfma_f32_16x16x32_bf16 a[8:11],a[212:215],v[72:75],a[8:11]
v_exp_f32 v103,v103
v_mfma_f32_16x16x32_bf16 a[12:15],a[216:219],v[64:67],a[12:15]
v_exp_f32 v104,v104
v_mfma_f32_16x16x32_bf16 a[12:15],a[220:223],v[72:75],a[12:15]
v_exp_f32 v105,v105
v_mfma_f32_16x16x32_bf16 a[16:19],a[224:227],v[64:67],a[16:19]
v_exp_f32 v106,v106
v_mfma_f32_16x16x32_bf16 a[16:19],a[228:231],v[72:75],a[16:19]
v_exp_f32 v107,v107
v_mfma_f32_16x16x32_bf16 a[20:23],a[232:235],v[64:67],a[20:23]
s_getreg_b32 s92,hwreg(HW_REG_IB_STS,0,4)
s_min_u32 s4,s92,s6
s_mov_b32 s6,s92
v_mfma_f32_16x16x32_bf16 a[20:23],a[236:239],v[72:75],a[20:23]
s_cmp_ge_u32 s4,9
s_cselect_b32 s4,14,s5
v_cvt_pk_bf16_f32 v83,v86,v87
v_mfma_f32_16x16x32_bf16 a[24:27],a[240:243],v[64:67],a[24:27]
v_exp_f32 v108,v108
v_mfma_f32_16x16x32_bf16 a[24:27],a[244:247],v[72:75],a[24:27]
v_exp_f32 v109,v109
v_mfma_f32_16x16x32_bf16 a[28:31],a[248:251],v[64:67],a[28:31]
ds_write_b128 v7,v[24:27] offset:3072
v_mfma_f32_16x16x32_bf16 a[28:31],a[252:255],v[72:75],a[28:31]
s_getpc_b64 s[92:93]
s_cmp_lg_u32 s4,0
s_cbranch_scc1 L_pace_%=
v_mfma_f32_16x16x32_bf16 v[48:51],v[28:31],v[64:67],v[48:51]
v_cvt_pk_bf16_f32 v88,v88,v89
v_cvt_pk_bf16_f32 v89,v90,v91
v_mfma_f32_16x16x32_bf16 v[48:51],v[28:31],v[72:75],v[48:51]
v_cvt_pk_bf16_f32 v90,v92,v93
v_cvt_pk_bf16_f32 v91,v94,v95
s_sleep 3
v_cvt_pk_bf16_f32 v96,v96,v97
v_cvt_pk_bf16_f32 v97,v98,v99
v_cvt_pk_bf16_f32 v98,v100,v101
v_cvt_pk_bf16_f32 v99,v102,v103
v_cvt_pk_bf16_f32 v104,v104,v105
v_cvt_pk_bf16_f32 v105,v106,v107
v_cvt_pk_bf16_f32 v106,v108,v109
s_mov_b32 s70,12
L_a0_%=:
s_waitcnt vmcnt(12)
s_barrier
v_mfma_f32_16x16x32_bf16 v[112:115],a[128:131],v[228:231],v[44:47]
s_add_u32 m0,s76,16384
s_sub_u32 s76,s76,32768
)A"
R"A(ds_read_b128 a[128:131],v1
v_mfma_f32_16x16x32_bf16 v[112:115],a[132:135],v[236:239],v[112:115]
s_and_b32 s76,s76,0x1ffff
v_exp_f32 v110,v110
v_mfma_f32_16x16x32_bf16 v[112:115],a[136:139],v[244:247],v[112:115]
s_add_u32 s75,s75,16384
buffer_load_dwordx4 v4,s[24:27],s74 offen lds
ds_read_b128 a[132:135],v1 offset:256
v_mfma_f32_16x16x32_bf16 v[112:115],a[140:143],v[252:255],v[112:115]
s_and_b32 s69,s75,0xfffc0000
v_exp_f32 v111,v111
v_mfma_f32_16x16x32_bf16 v[116:119],a[156:159],v[252:255],v[44:47]
ds_read_b128 a[136:139],v1 offset:512
v_cvt_pk_bf16_f32 v107,v110,v111
v_mfma_f32_16x16x32_bf16 v[116:119],a[152:155],v[244:247],v[116:119]
v_add_u32 v2,s78,v2
v_add_u32 v3,s78,v3
v_mfma_f32_16x16x32_bf16 v[116:119],a[148:151],v[236:239],v[116:119]
s_sub_u32 s78,s77,32768
v_exp_f32 v112,v112
v_mfma_f32_16x16x32_bf16 v[116:119],a[144:147],v[228:231],v[116:119]
s_and_b32 s78,s78,0x1ffff
v_exp_f32 v113,v113
v_mfma_f32_16x16x32_bf16 v[120:123],a[160:163],v[228:231],v[44:47]
s_sub_u32 s78,s78,s77
ds_read_b128 a[140:143],v1 offset:768
v_cvt_pk_bf16_f32 v112,v112,v113
v_mfma_f32_16x16x32_bf16 v[120:123],a[164:167],v[236:239],v[120:123]
s_add_u32 s77,s77,s78
v_exp_f32 v114,v114
v_mfma_f32_16x16x32_bf16 v[120:123],a[168:171],v[244:247],v[120:123]
v_exp_f32 v115,v115
v_mfma_f32_16x16x32_bf16 v[120:123],a[172:175],v[252:255],v[120:123]
ds_read_b128 a[156:159],v1 offset:4864
v_cvt_pk_bf16_f32 v113,v114,v115
v_mfma_f32_16x16x32_bf16 v[124:127],a[188:191],v[252:255],v[44:47]
v_exp_f32 v116,v116
v_mfma_f32_16x16x32_bf16 v[124:127],a[184:187],v[244:247],v[124:127]
v_exp_f32 v117,v117
v_mfma_f32_16x16x32_bf16 v[124:127],a[180:183],v[236:239],v[124:127]
ds_read_b128 a[152:155],v1 offset:4608
v_cvt_pk_bf16_f32 v114,v116,v117
v_mfma_f32_16x16x32_bf16 v[124:127],a[176:179],v[228:231],v[124:127]
v_exp_f32 v118,v118
v_mfma_f32_16x16x32_bf16 a[32:35],a[192:195],v[80:83],a[32:35]
v_exp_f32 v119,v119
v_mfma_f32_16x16x32_bf16 a[32:35],a[196:199],v[88:91],a[32:35]
ds_read_b128 a[148:151],v1 offset:4352
v_cvt_pk_bf16_f32 v115,v118,v119
v_mfma_f32_16x16x32_bf16 a[36:39],a[200:203],v[80:83],a[36:39]
buffer_load_dwordx4 v5,s[24:27],s74 offen offset:1024 lds
v_mfma_f32_16x16x32_bf16 a[36:39],a[204:207],v[88:91],a[36:39]
v_exp_f32 v120,v120
v_mfma_f32_16x16x32_bf16 a[40:43],a[208:211],v[80:83],a[40:43]
v_exp_f32 v121,v121
v_mfma_f32_16x16x32_bf16 a[40:43],a[212:215],v[88:91],a[40:43]
ds_read_b128 a[144:147],v1 offset:4096
v_cvt_pk_bf16_f32 v120,v120,v121
v_mfma_f32_16x16x32_bf16 a[44:47],a[216:219],v[80:83],a[44:47]
v_exp_f32 v122,v122
v_mfma_f32_16x16x32_bf16 a[44:47],a[220:223],v[88:91],a[44:47]
v_exp_f32 v123,v123
v_mfma_f32_16x16x32_bf16 a[48:51],a[224:227],v[80:83],a[48:51]
ds_read_b128 a[160:163],v1 offset:8192
v_cvt_pk_bf16_f32 v121,v122,v123
v_mfma_f32_16x16x32_bf16 a[48:51],a[228:231],v[88:91],a[48:51]
v_exp_f32 v124,v124
v_mfma_f32_16x16x32_bf16 a[52:55],a[232:235],v[80:83],a[52:55]
v_exp_f32 v125,v125
v_mfma_f32_16x16x32_bf16 a[52:55],a[236:239],v[88:91],a[52:55]
ds_read_b128 a[164:167],v1 offset:8448
v_cvt_pk_bf16_f32 v122,v124,v125
v_mfma_f32_16x16x32_bf16 a[56:59],a[240:243],v[80:83],a[56:59]
v_exp_f32 v126,v126
v_mfma_f32_16x16x32_bf16 a[56:59],a[244:247],v[88:91],a[56:59]
v_exp_f32 v127,v127
v_mfma_f32_16x16x32_bf16 a[60:63],a[248:251],v[80:83],a[60:63]
ds_read_b128 a[168:171],v1 offset:8704
v_cvt_pk_bf16_f32 v123,v126,v127
v_mfma_f32_16x16x32_bf16 a[60:63],a[252:255],v[88:91],a[60:63]
v_mfma_f32_16x16x32_bf16 v[52:55],v[28:31],v[80:83],v[52:55]
ds_read_b128 a[172:175],v1 offset:8960
v_mfma_f32_16x16x32_bf16 v[52:55],v[28:31],v[88:91],v[52:55]
s_waitcnt lgkmcnt(3)
v_mfma_f32_16x16x32_bf16 v[64:67],a[128:131],v[132:135],v[32:35]
buffer_load_dwordx4 v4,s[24:27],s74 offen offset:2048 lds
ds_read_b128 a[188:191],v1 offset:13056
v_mfma_f32_16x16x32_bf16 v[64:67],a[132:135],v[140:143],v[64:67]
v_mfma_f32_16x16x32_bf16 v[64:67],a[136:139],v[148:151],v[64:67]
ds_read_b128 a[184:187],v1 offset:12800
v_mfma_f32_16x16x32_bf16 v[64:67],a[140:143],v[156:159],v[64:67]
v_mfma_f32_16x16x32_bf16 v[68:71],a[156:159],v[156:159],v[32:35]
ds_read_b128 a[180:183],v1 offset:12544
v_mfma_f32_16x16x32_bf16 v[68:71],a[152:155],v[148:151],v[68:71]
v_mfma_f32_16x16x32_bf16 v[68:71],a[148:151],v[140:143],v[68:71]
ds_read_b128 a[176:179],v1 offset:12288
v_add_u32 v1,s78,v1
v_mfma_f32_16x16x32_bf16 v[68:71],a[144:147],v[132:135],v[68:71]
v_mfma_f32_16x16x32_bf16 v[72:75],a[160:163],v[132:135],v[32:35]
s_waitcnt lgkmcnt(3)
v_exp_f32 v64,v64
v_mfma_f32_16x16x32_bf16 v[72:75],a[164:167],v[140:143],v[72:75]
v_exp_f32 v65,v65
v_mfma_f32_16x16x32_bf16 v[72:75],a[168:171],v[148:151],v[72:75]
v_exp_f32 v66,v66
v_mfma_f32_16x16x32_bf16 v[72:75],a[172:175],v[156:159],v[72:75]
v_exp_f32 v67,v67
v_mfma_f32_16x16x32_bf16 v[76:79],a[188:191],v[156:159],v[32:35]
s_waitcnt lgkmcnt(1)
v_exp_f32 v68,v68
v_mfma_f32_16x16x32_bf16 v[76:79],a[184:187],v[148:151],v[76:79]
v_exp_f32 v69,v69
v_mfma_f32_16x16x32_bf16 v[76:79],a[180:183],v[140:143],v[76:79]
s_waitcnt lgkmcnt(0)
v_exp_f32 v70,v70
v_mfma_f32_16x16x32_bf16 v[76:79],a[176:179],v[132:135],v[76:79]
v_exp_f32 v71,v71
v_mfma_f32_16x16x32_bf16 a[64:67],a[192:195],v[96:99],a[64:67]
buffer_load_dwordx4 v5,s[24:27],s74 offen offset:3072 lds
v_cvt_pk_bf16_f32 v64,v64,v65
v_mfma_f32_16x16x32_bf16 a[64:67],a[196:199],v[104:107],a[64:67]
s_add_u32 s74,s74,16384
s_and_b32 s74,s74,0x3ffff
s_or_b32 s74,s74,s69
v_mfma_f32_16x16x32_bf16 a[68:71],a[200:203],v[96:99],a[68:71]
v_cvt_pk_bf16_f32 v65,v66,v67
v_cvt_pk_bf16_f32 v66,v68,v69
v_mfma_f32_16x16x32_bf16 a[68:71],a[204:207],v[104:107],a[68:71]
v_exp_f32 v72,v72
v_mfma_f32_16x16x32_bf16 a[72:75],a[208:211],v[96:99],a[72:75]
v_exp_f32 v73,v73
v_mfma_f32_16x16x32_bf16 a[72:75],a[212:215],v[104:107],a[72:75]
v_cvt_pk_bf16_f32 v67,v70,v71
v_cvt_pk_bf16_f32 v72,v72,v73
v_mfma_f32_16x16x32_bf16 a[76:79],a[216:219],v[96:99],a[76:79]
v_exp_f32 v74,v74
v_mfma_f32_16x16x32_bf16 a[76:79],a[220:223],v[104:107],a[76:79]
v_exp_f32 v75,v75
v_mfma_f32_16x16x32_bf16 a[80:83],a[224:227],v[96:99],a[80:83]
v_exp_f32 v76,v76
v_mfma_f32_16x16x32_bf16 a[80:83],a[228:231],v[104:107],a[80:83]
v_exp_f32 v77,v77
v_mfma_f32_16x16x32_bf16 a[84:87],a[232:235],v[96:99],a[84:87]
v_exp_f32 v78,v78
v_mfma_f32_16x16x32_bf16 a[84:87],a[236:239],v[104:107],a[84:87]
v_exp_f32 v79,v79
v_mfma_f32_16x16x32_bf16 a[88:91],a[240:243],v[96:99],a[88:91]
v_cvt_pk_bf16_f32 v73,v74,v75
v_cvt_pk_bf16_f32 v74,v76,v77
v_mfma_f32_16x16x32_bf16 a[88:91],a[244:247],v[104:107],a[88:91]
v_cvt_pk_bf16_f32 v75,v78,v79
v_mfma_f32_16x16x32_bf16 a[92:95],a[248:251],v[96:99],a[92:95]
s_add_u32 m0,s76,0
v_mfma_f32_16x16x32_bf16 a[92:95],a[252:255],v[104:107],a[92:95]
v_mfma_f32_16x16x32_bf16 v[56:59],v[28:31],v[96:99],v[56:59]
buffer_load_dwordx4 v4,s[20:23],s74 offen nt lds
v_mfma_f32_16x16x32_bf16 v[56:59],v[28:31],v[104:107],v[56:59]
v_mfma_f32_16x16x32_bf16 v[80:83],a[128:131],v[164:167],v[36:39]
v_mfma_f32_16x16x32_bf16 v[80:83],a[132:135],v[172:175],v[80:83]
v_mfma_f32_16x16x32_bf16 v[80:83],a[136:139],v[180:183],v[80:83]
v_mfma_f32_16x16x32_bf16 v[80:83],a[140:143],v[188:191],v[80:83]
v_mfma_f32_16x16x32_bf16 v[84:87],a[156:159],v[188:191],v[36:39]
v_mfma_f32_16x16x32_bf16 v[84:87],a[152:155],v[180:183],v[84:87]
v_mfma_f32_16x16x32_bf16 v[84:87],a[148:151],v[172:175],v[84:87]
v_mfma_f32_16x16x32_bf16 v[84:87],a[144:147],v[164:167],v[84:87]
v_mfma_f32_16x16x32_bf16 v[88:91],a[160:163],v[164:167],v[36:39]
v_mfma_f32_16x16x32_bf16 v[88:91],a[164:167],v[172:175],v[88:91]
v_mfma_f32_16x16x32_bf16 v[88:91],a[168:171],v[180:183],v[88:91]
v_mfma_f32_16x16x32_bf16 v[88:91],a[172:175],v[188:191],v[88:91]
v_exp_f32 v80,v80
v_mfma_f32_16x16x32_bf16 v[92:95],a[188:191],v[188:191],v[36:39]
v_exp_f32 v81,v81
v_mfma_f32_16x16x32_bf16 v[92:95],a[184:187],v[180:183],v[92:95]
v_exp_f32 v82,v82
v_mfma_f32_16x16x32_bf16 v[92:95],a[180:183],v[172:175],v[92:95]
buffer_load_dwordx4 v4,s[20:23],s74 offen offset:1024 nt lds
v_cvt_pk_bf16_f32 v80,v80,v81
v_mfma_f32_16x16x32_bf16 v[92:95],a[176:179],v[164:167],v[92:95]
v_exp_f32 v83,v83
v_mfma_f32_16x16x32_bf16 a[96:99],a[192:195],v[112:115],a[96:99]
ds_read_b64_tr_b16 a[192:193],v2
ds_read_b64_tr_b16 a[194:195],v2 offset:4096
v_mfma_f32_16x16x32_bf16 a[96:99],a[196:199],v[120:123],a[96:99]
ds_read_b64_tr_b16 a[196:197],v2 offset:8192
v_cvt_pk_bf16_f32 v81,v82,v83
v_mfma_f32_16x16x32_bf16 a[100:103],a[200:203],v[112:115],a[100:103]
v_exp_f32 v84,v84
v_mfma_f32_16x16x32_bf16 a[100:103],a[204:207],v[120:123],a[100:103]
ds_read_b64_tr_b16 a[198:199],v2 offset:12288
ds_read_b64_tr_b16 a[200:201],v3
v_mfma_f32_16x16x32_bf16 a[104:107],a[208:211],v[112:115],a[104:107]
v_exp_f32 v85,v85
v_mfma_f32_16x16x32_bf16 a[104:107],a[212:215],v[120:123],a[104:107]
ds_read_b64_tr_b16 a[202:203],v3 offset:4096
ds_read_b64_tr_b16 a[204:205],v3 offset:8192
v_mfma_f32_16x16x32_bf16 a[108:111],a[216:219],v[112:115],a[108:111]
ds_read_b64_tr_b16 a[206:207],v3 offset:12288
v_cvt_pk_bf16_f32 v82,v84,v85
v_mfma_f32_16x16x32_bf16 a[108:111],a[220:223],v[120:123],a[108:111]
v_exp_f32 v86,v86
v_mfma_f32_16x16x32_bf16 a[112:115],a[224:227],v[112:115],a[112:115]
ds_read_b64_tr_b16 a[208:209],v2 offset:256
ds_read_b64_tr_b16 a[210:211],v2 offset:4352
v_mfma_f32_16x16x32_bf16 a[112:115],a[228:231],v[120:123],a[112:115]
v_exp_f32 v87,v87
v_mfma_f32_16x16x32_bf16 a[116:119],a[232:235],v[112:115],a[116:119]
ds_read_b64_tr_b16 a[212:213],v2 offset:8448
ds_read_b64_tr_b16 a[214:215],v2 offset:12544
v_mfma_f32_16x16x32_bf16 a[116:119],a[236:239],v[120:123],a[116:119]
ds_read_b64_tr_b16 a[216:217],v3 offset:256
v_cvt_pk_bf16_f32 v83,v86,v87
v_mfma_f32_16x16x32_bf16 a[120:123],a[240:243],v[112:115],a[120:123]
v_exp_f32 v88,v88
v_mfma_f32_16x16x32_bf16 a[120:123],a[244:247],v[120:123],a[120:123]
ds_read_b64_tr_b16 a[218:219],v3 offset:4352
ds_read_b64_tr_b16 a[220:221],v3 offset:8448
v_mfma_f32_16x16x32_bf16 a[124:127],a[248:251],v[112:115],a[124:127]
buffer_load_dwordx4 v5,s[20:23],s74 offen offset:2048 nt lds
ds_read_b64_tr_b16 a[222:223],v3 offset:12544
v_mfma_f32_16x16x32_bf16 a[124:127],a[252:255],v[120:123],a[124:127]
v_exp_f32 v89,v89
v_mfma_f32_16x16x32_bf16 v[60:63],v[28:31],v[112:115],v[60:63]
ds_read_b64_tr_b16 a[224:225],v2 offset:512
ds_read_b64_tr_b16 a[226:227],v2 offset:4608
v_mfma_f32_16x16x32_bf16 v[60:63],v[28:31],v[120:123],v[60:63]
ds_read_b64_tr_b16 a[228:229],v2 offset:8704
v_cvt_pk_bf16_f32 v88,v88,v89
v_mfma_f32_16x16x32_bf16 v[96:99],a[128:131],v[196:199],v[40:43]
v_exp_f32 v90,v90
v_mfma_f32_16x16x32_bf16 v[96:99],a[132:135],v[204:207],v[96:99]
ds_read_b64_tr_b16 a[230:231],v2 offset:12800
ds_read_b64_tr_b16 a[232:233],v3 offset:512
v_mfma_f32_16x16x32_bf16 v[96:99],a[136:139],v[212:215],v[96:99]
v_exp_f32 v91,v91
v_mfma_f32_16x16x32_bf16 v[96:99],a[140:143],v[220:223],v[96:99]
ds_read_b64_tr_b16 a[234:235],v3 offset:4608
ds_read_b64_tr_b16 a[236:237],v3 offset:8704
v_mfma_f32_16x16x32_bf16 v[100:103],a[156:159],v[220:223],v[40:43]
ds_read_b64_tr_b16 a[238:239],v3 offset:12800
v_cvt_pk_bf16_f32 v89,v90,v91
v_mfma_f32_16x16x32_bf16 v[100:103],a[152:155],v[212:215],v[100:103]
v_exp_f32 v92,v92
v_mfma_f32_16x16x32_bf16 v[100:103],a[148:151],v[204:207],v[100:103]
ds_read_b64_tr_b16 a[240:241],v2 offset:768
ds_read_b64_tr_b16 a[242:243],v2 offset:4864
v_mfma_f32_16x16x32_bf16 v[100:103],a[144:147],v[196:199],v[100:103]
v_exp_f32 v93,v93
v_mfma_f32_16x16x32_bf16 v[104:107],a[160:163],v[196:199],v[40:43]
ds_read_b64_tr_b16 a[244:245],v2 offset:8960
ds_read_b64_tr_b16 a[246:247],v2 offset:13056
v_mfma_f32_16x16x32_bf16 v[104:107],a[164:167],v[204:207],v[104:107]
ds_read_b64_tr_b16 a[248:249],v3 offset:768
v_cvt_pk_bf16_f32 v90,v92,v93
v_mfma_f32_16x16x32_bf16 v[104:107],a[168:171],v[212:215],v[104:107]
v_exp_f32 v94,v94
v_mfma_f32_16x16x32_bf16 v[104:107],a[172:175],v[220:223],v[104:107]
ds_read_b64_tr_b16 a[250:251],v3 offset:4864
ds_read_b64_tr_b16 a[252:253],v3 offset:8960
v_mfma_f32_16x16x32_bf16 v[108:111],a[188:191],v[220:223],v[40:43]
buffer_load_dwordx4 v5,s[20:23],s74 offen offset:3072 lds
ds_read_b64_tr_b16 a[254:255],v3 offset:13056
)A"
R"A(v_mfma_f32_16x16x32_bf16 v[108:111],a[184:187],v[212:215],v[108:111]
v_exp_f32 v95,v95
v_mfma_f32_16x16x32_bf16 v[108:111],a[180:183],v[204:207],v[108:111]
v_exp_f32 v96,v96
v_mfma_f32_16x16x32_bf16 v[108:111],a[176:179],v[196:199],v[108:111]
s_waitcnt lgkmcnt(6)
v_exp_f32 v97,v97
v_mfma_f32_16x16x32_bf16 a[0:3],a[192:195],v[64:67],a[0:3]
v_exp_f32 v98,v98
v_mfma_f32_16x16x32_bf16 a[0:3],a[196:199],v[72:75],a[0:3]
v_exp_f32 v99,v99
v_mfma_f32_16x16x32_bf16 a[4:7],a[200:203],v[64:67],a[4:7]
v_exp_f32 v100,v100
v_mfma_f32_16x16x32_bf16 a[4:7],a[204:207],v[72:75],a[4:7]
v_exp_f32 v101,v101
v_mfma_f32_16x16x32_bf16 a[8:11],a[208:211],v[64:67],a[8:11]
v_exp_f32 v102,v102
v_mfma_f32_16x16x32_bf16 a[8:11],a[212:215],v[72:75],a[8:11]
v_exp_f32 v103,v103
v_mfma_f32_16x16x32_bf16 a[12:15],a[216:219],v[64:67],a[12:15]
v_exp_f32 v104,v104
v_mfma_f32_16x16x32_bf16 a[12:15],a[220:223],v[72:75],a[12:15]
v_exp_f32 v105,v105
v_mfma_f32_16x16x32_bf16 a[16:19],a[224:227],v[64:67],a[16:19]
v_exp_f32 v106,v106
v_mfma_f32_16x16x32_bf16 a[16:19],a[228:231],v[72:75],a[16:19]
v_exp_f32 v107,v107
v_mfma_f32_16x16x32_bf16 a[20:23],a[232:235],v[64:67],a[20:23]
s_getreg_b32 s92,hwreg(HW_REG_IB_STS,0,4)
s_min_u32 s4,s92,s6
s_mov_b32 s6,s92
v_mfma_f32_16x16x32_bf16 a[20:23],a[236:239],v[72:75],a[20:23]
s_cmp_ge_u32 s4,8
s_cselect_b32 s4,14,s5
v_cvt_pk_bf16_f32 v91,v94,v95
v_mfma_f32_16x16x32_bf16 a[24:27],a[240:243],v[64:67],a[24:27]
s_waitcnt lgkmcnt(0)
v_exp_f32 v108,v108
v_mfma_f32_16x16x32_bf16 a[24:27],a[244:247],v[72:75],a[24:27]
v_exp_f32 v109,v109
v_mfma_f32_16x16x32_bf16 a[28:31],a[248:251],v[64:67],a[28:31]
v_cvt_pk_bf16_f32 v96,v96,v97
v_cvt_pk_bf16_f32 v97,v98,v99
v_mfma_f32_16x16x32_bf16 a[28:31],a[252:255],v[72:75],a[28:31]
s_getpc_b64 s[92:93]
s_cmp_lg_u32 s4,0
s_cbranch_scc1 L_pace_%=
v_mfma_f32_16x16x32_bf16 v[48:51],v[28:31],v[64:67],v[48:51]
v_cvt_pk_bf16_f32 v98,v100,v101
v_cvt_pk_bf16_f32 v99,v102,v103
v_mfma_f32_16x16x32_bf16 v[48:51],v[28:31],v[72:75],v[48:51]
v_cvt_pk_bf16_f32 v104,v104,v105
v_cvt_pk_bf16_f32 v105,v106,v107
s_sleep 2
v_cvt_pk_bf16_f32 v106,v108,v109
s_sub_u32 s70,s70,1
s_cmp_lg_u32 s70,0
s_cbranch_scc1 L_a0_%=
s_waitcnt vmcnt(12)
s_barrier
v_mfma_f32_16x16x32_bf16 v[112:115],a[128:131],v[228:231],v[44:47]
s_add_u32 m0,s76,16384
s_sub_u32 s76,s76,32768
ds_read_b128 v[32:35],v7
v_mfma_f32_16x16x32_bf16 v[112:115],a[132:135],v[236:239],v[112:115]
s_and_b32 s76,s76,0x1ffff
v_exp_f32 v110,v110
v_mfma_f32_16x16x32_bf16 v[112:115],a[136:139],v[244:247],v[112:115]
s_add_u32 s75,s75,16384
buffer_load_dwordx4 v4,s[24:27],s74 offen lds
ds_read_b128 a[128:131],v1
v_mfma_f32_16x16x32_bf16 v[112:115],a[140:143],v[252:255],v[112:115]
s_and_b32 s69,s75,0xfffc0000
v_exp_f32 v111,v111
v_mfma_f32_16x16x32_bf16 v[116:119],a[156:159],v[252:255],v[44:47]
ds_read_b128 a[132:135],v1 offset:256
v_cvt_pk_bf16_f32 v107,v110,v111
v_mfma_f32_16x16x32_bf16 v[116:119],a[152:155],v[244:247],v[116:119]
v_add_u32 v2,s78,v2
v_add_u32 v3,s78,v3
v_mfma_f32_16x16x32_bf16 v[116:119],a[148:151],v[236:239],v[116:119]
s_sub_u32 s78,s77,32768
ds_read_b128 a[136:139],v1 offset:512
v_accvgpr_read_b32 v8,a0
v_mfma_f32_16x16x32_bf16 v[116:119],a[144:147],v[228:231],v[116:119]
s_and_b32 s78,s78,0x1ffff
v_exp_f32 v112,v112
v_mfma_f32_16x16x32_bf16 v[120:123],a[160:163],v[228:231],v[44:47]
s_sub_u32 s78,s78,s77
ds_read_b128 a[140:143],v1 offset:768
v_accvgpr_read_b32 v9,a1
v_mfma_f32_16x16x32_bf16 v[120:123],a[164:167],v[236:239],v[120:123]
s_add_u32 s77,s77,s78
v_exp_f32 v113,v113
v_mfma_f32_16x16x32_bf16 v[120:123],a[168:171],v[244:247],v[120:123]
ds_read_b128 a[156:159],v1 offset:4864
v_cvt_pk_bf16_f32 v112,v112,v113
v_mfma_f32_16x16x32_bf16 v[120:123],a[172:175],v[252:255],v[120:123]
v_exp_f32 v114,v114
v_mfma_f32_16x16x32_bf16 v[124:127],a[188:191],v[252:255],v[44:47]
ds_read_b128 a[152:155],v1 offset:4608
v_accvgpr_read_b32 v10,a2
v_mfma_f32_16x16x32_bf16 v[124:127],a[184:187],v[244:247],v[124:127]
v_exp_f32 v115,v115
v_mfma_f32_16x16x32_bf16 v[124:127],a[180:183],v[236:239],v[124:127]
ds_read_b128 a[148:151],v1 offset:4352
v_cvt_pk_bf16_f32 v113,v114,v115
v_mfma_f32_16x16x32_bf16 v[124:127],a[176:179],v[228:231],v[124:127]
v_exp_f32 v116,v116
v_mfma_f32_16x16x32_bf16 v[52:55],v[28:31],v[80:83],v[52:55]
buffer_load_dwordx4 v5,s[24:27],s74 offen offset:1024 lds
ds_read_b128 a[144:147],v1 offset:4096
v_mfma_f32_16x16x32_bf16 v[52:55],v[28:31],v[88:91],v[52:55]
v_exp_f32 v117,v117
v_mfma_f32_16x16x32_bf16 a[32:35],a[192:195],v[80:83],a[32:35]
ds_read_b128 a[160:163],v1 offset:8192
v_cvt_pk_bf16_f32 v114,v116,v117
v_mfma_f32_16x16x32_bf16 a[32:35],a[196:199],v[88:91],a[32:35]
v_exp_f32 v118,v118
v_mfma_f32_16x16x32_bf16 a[36:39],a[200:203],v[80:83],a[36:39]
ds_read_b128 a[164:167],v1 offset:8448
v_accvgpr_read_b32 v11,a3
v_mfma_f32_16x16x32_bf16 a[36:39],a[204:207],v[88:91],a[36:39]
v_exp_f32 v119,v119
v_mfma_f32_16x16x32_bf16 a[40:43],a[208:211],v[80:83],a[40:43]
ds_read_b128 a[168:171],v1 offset:8704
v_cvt_pk_bf16_f32 v115,v118,v119
v_mfma_f32_16x16x32_bf16 a[40:43],a[212:215],v[88:91],a[40:43]
v_exp_f32 v120,v120
v_mfma_f32_16x16x32_bf16 a[44:47],a[216:219],v[80:83],a[44:47]
ds_read_b128 a[172:175],v1 offset:8960
v_accvgpr_read_b32 v12,a4
v_mfma_f32_16x16x32_bf16 a[44:47],a[220:223],v[88:91],a[44:47]
v_exp_f32 v121,v121
v_mfma_f32_16x16x32_bf16 a[48:51],a[224:227],v[80:83],a[48:51]
ds_read_b128 a[188:191],v1 offset:13056
v_cvt_pk_bf16_f32 v120,v120,v121
v_mfma_f32_16x16x32_bf16 a[48:51],a[228:231],v[88:91],a[48:51]
v_exp_f32 v122,v122
v_mfma_f32_16x16x32_bf16 a[52:55],a[232:235],v[80:83],a[52:55]
ds_read_b128 a[184:187],v1 offset:12800
v_accvgpr_read_b32 v13,a5
v_mfma_f32_16x16x32_bf16 a[52:55],a[236:239],v[88:91],a[52:55]
v_exp_f32 v123,v123
v_mfma_f32_16x16x32_bf16 a[56:59],a[240:243],v[80:83],a[56:59]
buffer_load_dwordx4 v4,s[24:27],s74 offen offset:2048 lds
ds_read_b128 a[180:183],v1 offset:12544
v_mfma_f32_16x16x32_bf16 a[56:59],a[244:247],v[88:91],a[56:59]
v_exp_f32 v124,v124
v_mfma_f32_16x16x32_bf16 a[60:63],a[248:251],v[80:83],a[60:63]
ds_read_b128 a[176:179],v1 offset:12288
v_cvt_pk_bf16_f32 v121,v122,v123
v_mfma_f32_16x16x32_bf16 a[60:63],a[252:255],v[88:91],a[60:63]
s_waitcnt lgkmcnt(4)
v_exp_f32 v125,v125
v_mfma_f32_16x16x32_bf16 v[64:67],a[128:131],v[128:131],v[32:35]
ds_read_b128 v[36:39],v7 offset:1024
v_cvt_pk_bf16_f32 v122,v124,v125
v_mfma_f32_16x16x32_bf16 v[64:67],a[132:135],v[136:139],v[64:67]
v_exp_f32 v126,v126
v_mfma_f32_16x16x32_bf16 v[64:67],a[136:139],v[144:147],v[64:67]
v_exp_f32 v127,v127
v_mfma_f32_16x16x32_bf16 v[64:67],a[140:143],v[152:155],v[64:67]
ds_read_b128 v[40:43],v7 offset:2048
v_cvt_pk_bf16_f32 v123,v126,v127
v_mfma_f32_16x16x32_bf16 v[68:71],a[156:159],v[152:155],v[32:35]
v_accvgpr_read_b32 v14,a6
v_accvgpr_read_b32 v15,a7
v_mfma_f32_16x16x32_bf16 v[68:71],a[152:155],v[144:147],v[68:71]
v_max_f32 v24,0x00800000,v48
v_accvgpr_read_b32 v16,a8
v_mfma_f32_16x16x32_bf16 v[68:71],a[148:151],v[136:139],v[68:71]
v_exp_f32 v64,v64
v_mfma_f32_16x16x32_bf16 v[68:71],a[144:147],v[128:131],v[68:71]
v_exp_f32 v65,v65
v_mfma_f32_16x16x32_bf16 v[72:75],a[160:163],v[128:131],v[32:35]
v_exp_f32 v66,v66
v_mfma_f32_16x16x32_bf16 v[72:75],a[164:167],v[136:139],v[72:75]
v_exp_f32 v67,v67
v_mfma_f32_16x16x32_bf16 v[72:75],a[168:171],v[144:147],v[72:75]
v_cvt_pk_bf16_f32 v64,v64,v65
v_cvt_pk_bf16_f32 v65,v66,v67
v_mfma_f32_16x16x32_bf16 v[72:75],a[172:175],v[152:155],v[72:75]
s_waitcnt lgkmcnt(0)
buffer_load_dwordx4 v5,s[24:27],s74 offen offset:3072 lds
v_accvgpr_read_b32 v17,a9
v_mfma_f32_16x16x32_bf16 v[76:79],a[188:191],v[152:155],v[32:35]
s_add_u32 s74,s74,16384
s_and_b32 s74,s74,0x3ffff
s_or_b32 s74,s74,s69
v_mfma_f32_16x16x32_bf16 v[76:79],a[184:187],v[144:147],v[76:79]
v_exp_f32 v68,v68
v_mfma_f32_16x16x32_bf16 v[76:79],a[180:183],v[136:139],v[76:79]
v_exp_f32 v69,v69
v_mfma_f32_16x16x32_bf16 v[76:79],a[176:179],v[128:131],v[76:79]
v_exp_f32 v70,v70
v_mfma_f32_16x16x32_bf16 v[56:59],v[28:31],v[96:99],v[56:59]
v_exp_f32 v71,v71
v_mfma_f32_16x16x32_bf16 v[56:59],v[28:31],v[104:107],v[56:59]
v_cvt_pk_bf16_f32 v66,v68,v69
v_cvt_pk_bf16_f32 v67,v70,v71
v_mfma_f32_16x16x32_bf16 a[64:67],a[192:195],v[96:99],a[64:67]
v_exp_f32 v72,v72
v_mfma_f32_16x16x32_bf16 a[64:67],a[196:199],v[104:107],a[64:67]
v_exp_f32 v73,v73
v_mfma_f32_16x16x32_bf16 a[68:71],a[200:203],v[96:99],a[68:71]
v_exp_f32 v74,v74
v_mfma_f32_16x16x32_bf16 a[68:71],a[204:207],v[104:107],a[68:71]
v_exp_f32 v75,v75
v_mfma_f32_16x16x32_bf16 a[72:75],a[208:211],v[96:99],a[72:75]
v_exp_f32 v76,v76
v_mfma_f32_16x16x32_bf16 a[72:75],a[212:215],v[104:107],a[72:75]
s_add_u32 m0,s76,0
v_exp_f32 v77,v77
v_mfma_f32_16x16x32_bf16 a[76:79],a[216:219],v[96:99],a[76:79]
v_exp_f32 v78,v78
v_mfma_f32_16x16x32_bf16 a[76:79],a[220:223],v[104:107],a[76:79]
buffer_load_dwordx4 v4,s[20:23],s74 offen nt lds
v_cvt_pk_bf16_f32 v72,v72,v73
v_mfma_f32_16x16x32_bf16 a[80:83],a[224:227],v[96:99],a[80:83]
v_exp_f32 v79,v79
v_mfma_f32_16x16x32_bf16 a[80:83],a[228:231],v[104:107],a[80:83]
v_cvt_pk_bf16_f32 v73,v74,v75
v_cvt_pk_bf16_f32 v74,v76,v77
v_mfma_f32_16x16x32_bf16 a[84:87],a[232:235],v[96:99],a[84:87]
v_cvt_pk_bf16_f32 v75,v78,v79
v_accvgpr_read_b32 v18,a10
s_nop 0
L_pd7_%=:
s_cbranch_execz L_trt0_%=
v_mfma_f32_16x16x32_bf16 a[84:87],a[236:239],v[104:107],a[84:87]
v_rcp_f32 v24,v24
v_mfma_f32_16x16x32_bf16 a[88:91],a[240:243],v[96:99],a[88:91]
v_accvgpr_read_b32 v19,a11
v_mul_f32 v8,v8,v24
v_mfma_f32_16x16x32_bf16 a[88:91],a[244:247],v[104:107],a[88:91]
v_mul_f32 v9,v9,v24
v_mul_f32 v10,v10,v24
v_mfma_f32_16x16x32_bf16 a[92:95],a[248:251],v[96:99],a[92:95]
v_mul_f32 v11,v11,v24
v_mul_f32 v12,v12,v24
v_mfma_f32_16x16x32_bf16 a[92:95],a[252:255],v[104:107],a[92:95]
v_mul_f32 v13,v13,v24
v_mul_f32 v14,v14,v24
v_mfma_f32_16x16x32_bf16 v[80:83],a[128:131],v[160:163],v[36:39]
v_mul_f32 v15,v15,v24
v_cvt_pk_bf16_f32 v8,v8,v9
v_mfma_f32_16x16x32_bf16 v[80:83],a[132:135],v[168:171],v[80:83]
v_cvt_pk_bf16_f32 v9,v10,v11
v_cvt_pk_bf16_f32 v10,v12,v13
v_mfma_f32_16x16x32_bf16 v[80:83],a[136:139],v[176:179],v[80:83]
v_cvt_pk_bf16_f32 v11,v14,v15
v_accvgpr_read_b32 v20,a12
v_mfma_f32_16x16x32_bf16 v[80:83],a[140:143],v[184:187],v[80:83]
ds_write_b128 v7,v[8:11] offset:6144
v_mfma_f32_16x16x32_bf16 v[84:87],a[156:159],v[184:187],v[36:39]
v_accvgpr_read_b32 v21,a13
v_accvgpr_read_b32 v22,a14
v_mfma_f32_16x16x32_bf16 v[84:87],a[152:155],v[176:179],v[84:87]
v_accvgpr_read_b32 v23,a15
v_accvgpr_read_b32 v8,a16
v_mfma_f32_16x16x32_bf16 v[84:87],a[148:151],v[168:171],v[84:87]
buffer_load_dwordx4 v4,s[20:23],s74 offen offset:1024 nt lds
v_accvgpr_read_b32 v9,a17
v_mfma_f32_16x16x32_bf16 v[84:87],a[144:147],v[160:163],v[84:87]
v_accvgpr_read_b32 v10,a18
v_accvgpr_read_b32 v11,a19
v_mfma_f32_16x16x32_bf16 v[88:91],a[160:163],v[160:163],v[36:39]
v_mul_f32 v16,v16,v24
v_mul_f32 v17,v17,v24
v_mfma_f32_16x16x32_bf16 v[88:91],a[164:167],v[168:171],v[88:91]
v_mul_f32 v18,v18,v24
v_mul_f32 v19,v19,v24
v_mfma_f32_16x16x32_bf16 v[88:91],a[168:171],v[176:179],v[88:91]
v_mul_f32 v20,v20,v24
v_mul_f32 v21,v21,v24
v_mfma_f32_16x16x32_bf16 v[88:91],a[172:175],v[184:187],v[88:91]
v_mul_f32 v22,v22,v24
v_mul_f32 v23,v23,v24
v_mfma_f32_16x16x32_bf16 v[92:95],a[188:191],v[184:187],v[36:39]
v_cvt_pk_bf16_f32 v16,v16,v17
v_cvt_pk_bf16_f32 v17,v18,v19
v_mfma_f32_16x16x32_bf16 v[92:95],a[184:187],v[176:179],v[92:95]
v_cvt_pk_bf16_f32 v18,v20,v21
v_cvt_pk_bf16_f32 v19,v22,v23
v_mfma_f32_16x16x32_bf16 v[92:95],a[180:183],v[168:171],v[92:95]
ds_write_b128 v7,v[16:19] offset:7168
v_mfma_f32_16x16x32_bf16 v[92:95],a[176:179],v[160:163],v[92:95]
v_accvgpr_read_b32 v12,a20
v_accvgpr_read_b32 v13,a21
v_mfma_f32_16x16x32_bf16 v[60:63],v[28:31],v[112:115],v[60:63]
v_accvgpr_read_b32 v14,a22
v_accvgpr_read_b32 v15,a23
v_mfma_f32_16x16x32_bf16 v[60:63],v[28:31],v[120:123],v[60:63]
v_accvgpr_read_b32 v16,a24
v_accvgpr_read_b32 v17,a25
v_mfma_f32_16x16x32_bf16 a[96:99],a[192:195],v[112:115],a[96:99]
ds_read_b64_tr_b16 a[192:193],v2
ds_read_b64_tr_b16 a[194:195],v2 offset:4096
v_mfma_f32_16x16x32_bf16 a[96:99],a[196:199],v[120:123],a[96:99]
)A"
R"A(ds_read_b64_tr_b16 a[196:197],v2 offset:8192
v_accvgpr_read_b32 v18,a26
v_mfma_f32_16x16x32_bf16 a[100:103],a[200:203],v[112:115],a[100:103]
buffer_load_dwordx4 v5,s[20:23],s74 offen offset:2048 nt lds
ds_read_b64_tr_b16 a[198:199],v2 offset:12288
v_mfma_f32_16x16x32_bf16 a[100:103],a[204:207],v[120:123],a[100:103]
ds_read_b64_tr_b16 a[200:201],v3
v_accvgpr_read_b32 v19,a27
v_mfma_f32_16x16x32_bf16 a[104:107],a[208:211],v[112:115],a[104:107]
ds_read_b64_tr_b16 a[202:203],v3 offset:4096
ds_read_b64_tr_b16 a[204:205],v3 offset:8192
v_mfma_f32_16x16x32_bf16 a[104:107],a[212:215],v[120:123],a[104:107]
ds_read_b64_tr_b16 a[206:207],v3 offset:12288
v_accvgpr_read_b32 v20,a28
v_mfma_f32_16x16x32_bf16 a[108:111],a[216:219],v[112:115],a[108:111]
ds_read_b64_tr_b16 a[208:209],v2 offset:256
v_accvgpr_read_b32 v21,a29
v_mfma_f32_16x16x32_bf16 a[108:111],a[220:223],v[120:123],a[108:111]
ds_read_b64_tr_b16 a[210:211],v2 offset:4352
v_accvgpr_read_b32 v22,a30
v_mfma_f32_16x16x32_bf16 a[112:115],a[224:227],v[112:115],a[112:115]
ds_read_b64_tr_b16 a[212:213],v2 offset:8448
ds_read_b64_tr_b16 a[214:215],v2 offset:12544
v_mfma_f32_16x16x32_bf16 a[112:115],a[228:231],v[120:123],a[112:115]
ds_read_b64_tr_b16 a[216:217],v3 offset:256
v_accvgpr_read_b32 v23,a31
v_mfma_f32_16x16x32_bf16 a[116:119],a[232:235],v[112:115],a[116:119]
ds_read_b64_tr_b16 a[218:219],v3 offset:4352
v_cmp_nlt_f32 s[54:55],v48,s73
v_mfma_f32_16x16x32_bf16 a[116:119],a[236:239],v[120:123],a[116:119]
ds_read_b64_tr_b16 a[220:221],v3 offset:8448
v_cmp_nlt_f32 s[56:57],v52,s73
v_mfma_f32_16x16x32_bf16 a[120:123],a[240:243],v[112:115],a[120:123]
s_or_b64 s[54:55],s[54:55],s[56:57]
ds_read_b64_tr_b16 a[222:223],v3 offset:12544
ds_read_b64_tr_b16 a[224:225],v2 offset:512
v_mfma_f32_16x16x32_bf16 a[120:123],a[244:247],v[120:123],a[120:123]
ds_read_b64_tr_b16 a[226:227],v2 offset:4608
v_cmp_nlt_f32 s[56:57],v56,s73
v_mfma_f32_16x16x32_bf16 a[124:127],a[248:251],v[112:115],a[124:127]
s_or_b64 s[54:55],s[54:55],s[56:57]
ds_read_b64_tr_b16 a[228:229],v2 offset:8704
v_mul_f32 v8,v8,v24
v_mfma_f32_16x16x32_bf16 a[124:127],a[252:255],v[120:123],a[124:127]
ds_read_b64_tr_b16 a[230:231],v2 offset:12800
v_mul_f32 v9,v9,v24
v_mfma_f32_16x16x32_bf16 v[96:99],a[128:131],v[192:195],v[40:43]
ds_read_b64_tr_b16 a[232:233],v3 offset:512
ds_read_b64_tr_b16 a[234:235],v3 offset:4608
v_mfma_f32_16x16x32_bf16 v[96:99],a[132:135],v[200:203],v[96:99]
buffer_load_dwordx4 v5,s[20:23],s74 offen offset:3072 lds
ds_read_b64_tr_b16 a[236:237],v3 offset:8704
v_mfma_f32_16x16x32_bf16 v[96:99],a[136:139],v[208:211],v[96:99]
ds_read_b64_tr_b16 a[238:239],v3 offset:12800
v_mul_f32 v10,v10,v24
v_mfma_f32_16x16x32_bf16 v[96:99],a[140:143],v[216:219],v[96:99]
ds_read_b64_tr_b16 a[240:241],v2 offset:768
v_mul_f32 v11,v11,v24
v_mfma_f32_16x16x32_bf16 v[100:103],a[156:159],v[216:219],v[40:43]
ds_read_b64_tr_b16 a[242:243],v2 offset:4864
ds_read_b64_tr_b16 a[244:245],v2 offset:8960
v_mfma_f32_16x16x32_bf16 v[100:103],a[152:155],v[208:211],v[100:103]
ds_read_b64_tr_b16 a[246:247],v2 offset:13056
v_mul_f32 v12,v12,v24
v_mfma_f32_16x16x32_bf16 v[100:103],a[148:151],v[200:203],v[100:103]
ds_read_b64_tr_b16 a[248:249],v3 offset:768
v_mul_f32 v13,v13,v24
v_mfma_f32_16x16x32_bf16 v[100:103],a[144:147],v[192:195],v[100:103]
ds_read_b64_tr_b16 a[250:251],v3 offset:4864
v_mul_f32 v14,v14,v24
v_mfma_f32_16x16x32_bf16 v[104:107],a[160:163],v[192:195],v[40:43]
ds_read_b64_tr_b16 a[252:253],v3 offset:8960
ds_read_b64_tr_b16 a[254:255],v3 offset:13056
v_mfma_f32_16x16x32_bf16 v[104:107],a[164:167],v[200:203],v[104:107]
v_mul_f32 v15,v15,v24
v_cvt_pk_bf16_f32 v8,v8,v9
v_mfma_f32_16x16x32_bf16 v[104:107],a[168:171],v[208:211],v[104:107]
v_cvt_pk_bf16_f32 v9,v10,v11
v_cvt_pk_bf16_f32 v10,v12,v13
v_mfma_f32_16x16x32_bf16 v[104:107],a[172:175],v[216:219],v[104:107]
v_cvt_pk_bf16_f32 v11,v14,v15
v_mul_f32 v16,v16,v24
v_mfma_f32_16x16x32_bf16 v[108:111],a[188:191],v[216:219],v[40:43]
v_mul_f32 v17,v17,v24
v_mul_f32 v18,v18,v24
v_mfma_f32_16x16x32_bf16 v[108:111],a[184:187],v[208:211],v[108:111]
s_add_u32 m0,s76,16384
v_mul_f32 v19,v19,v24
v_mul_f32 v20,v20,v24
v_mfma_f32_16x16x32_bf16 v[108:111],a[180:183],v[200:203],v[108:111]
v_mul_f32 v21,v21,v24
v_mul_f32 v22,v22,v24
v_mfma_f32_16x16x32_bf16 v[108:111],a[176:179],v[192:195],v[108:111]
s_waitcnt lgkmcnt(2)
buffer_load_dwordx4 v4,s[24:27],s74 offen lds
v_mul_f32 v23,v23,v24
v_mfma_f32_16x16x32_bf16 a[0:3],a[192:195],v[64:67],0
v_cvt_pk_bf16_f32 v16,v16,v17
v_cvt_pk_bf16_f32 v17,v18,v19
v_mfma_f32_16x16x32_bf16 a[0:3],a[196:199],v[72:75],a[0:3]
v_cvt_pk_bf16_f32 v18,v20,v21
v_cvt_pk_bf16_f32 v19,v22,v23
v_mfma_f32_16x16x32_bf16 a[4:7],a[200:203],v[64:67],0
v_exp_f32 v80,v80
v_mfma_f32_16x16x32_bf16 a[4:7],a[204:207],v[72:75],a[4:7]
v_exp_f32 v81,v81
v_mfma_f32_16x16x32_bf16 a[8:11],a[208:211],v[64:67],0
v_exp_f32 v82,v82
v_mfma_f32_16x16x32_bf16 a[8:11],a[212:215],v[72:75],a[8:11]
v_exp_f32 v83,v83
v_mfma_f32_16x16x32_bf16 a[12:15],a[216:219],v[64:67],0
v_exp_f32 v84,v84
v_mfma_f32_16x16x32_bf16 a[12:15],a[220:223],v[72:75],a[12:15]
v_exp_f32 v85,v85
v_mfma_f32_16x16x32_bf16 a[16:19],a[224:227],v[64:67],0
v_exp_f32 v86,v86
v_mfma_f32_16x16x32_bf16 a[16:19],a[228:231],v[72:75],a[16:19]
v_exp_f32 v87,v87
v_mfma_f32_16x16x32_bf16 a[20:23],a[232:235],v[64:67],0
s_getreg_b32 s92,hwreg(HW_REG_IB_STS,0,4)
s_min_u32 s4,s92,s6
s_mov_b32 s6,s92
v_mfma_f32_16x16x32_bf16 a[20:23],a[236:239],v[72:75],a[20:23]
s_cmp_ge_u32 s4,9
s_cselect_b32 s4,14,s5
v_cmp_nlt_f32 s[56:57],v60,s73
v_mfma_f32_16x16x32_bf16 a[24:27],a[240:243],v[64:67],0
s_or_b64 s[54:55],s[54:55],s[56:57]
v_exp_f32 v88,v88
v_mfma_f32_16x16x32_bf16 a[24:27],a[244:247],v[72:75],a[24:27]
v_exp_f32 v89,v89
v_mfma_f32_16x16x32_bf16 a[28:31],a[248:251],v[64:67],0
s_waitcnt lgkmcnt(0)
buffer_load_dwordx4 v5,s[24:27],s74 offen offset:1024 lds
v_add_u32 v1,s78,v1
v_mfma_f32_16x16x32_bf16 a[28:31],a[252:255],v[72:75],a[28:31]
s_getpc_b64 s[92:93]
s_cmp_lg_u32 s4,0
s_cbranch_scc1 L_pace_%=
v_mfma_f32_16x16x32_bf16 v[48:51],v[28:31],v[64:67],0
ds_write_b128 v7,v[8:11]
v_mfma_f32_16x16x32_bf16 v[48:51],v[28:31],v[72:75],v[48:51]
v_max_f32 v25,0x00800000,v52
v_max_f32 v26,0x00800000,v56
s_sleep 2
ds_write_b128 v7,v[16:19] offset:1024
ds_read_b128 v[44:47],v7 offset:3072
v_rcp_f32 v25,v25
v_rcp_f32 v26,v26
v_cvt_pk_bf16_f32 v80,v80,v81
v_cvt_pk_bf16_f32 v81,v82,v83
v_cvt_pk_bf16_f32 v82,v84,v85
v_cvt_pk_bf16_f32 v83,v86,v87
v_cvt_pk_bf16_f32 v88,v88,v89
v_max_f32 v27,0x00800000,v60
v_rcp_f32 v27,v27
s_cmp_lg_u64 s[54:55],0
s_cbranch_scc1 L_slow0_%=
L_ok0_%=:
s_waitcnt vmcnt(14)
s_barrier
s_waitcnt lgkmcnt(0)
v_mfma_f32_16x16x32_bf16 v[112:115],a[128:131],v[224:227],v[44:47]
s_add_u32 m0,s76,16384
v_exp_f32 v90,v90
v_mfma_f32_16x16x32_bf16 v[112:115],a[132:135],v[232:235],v[112:115]
s_sub_u32 s76,s76,32768
v_exp_f32 v91,v91
v_mfma_f32_16x16x32_bf16 v[112:115],a[136:139],v[240:243],v[112:115]
s_and_b32 s76,s76,0x1ffff
buffer_load_dwordx4 v4,s[24:27],s74 offen offset:2048 lds
v_cvt_pk_bf16_f32 v89,v90,v91
v_mfma_f32_16x16x32_bf16 v[112:115],a[140:143],v[248:251],v[112:115]
s_add_u32 s75,s75,16384
v_exp_f32 v92,v92
v_mfma_f32_16x16x32_bf16 v[116:119],a[156:159],v[248:251],v[44:47]
s_and_b32 s69,s75,0xfffc0000
v_exp_f32 v93,v93
v_mfma_f32_16x16x32_bf16 v[116:119],a[152:155],v[240:243],v[116:119]
s_add_u32 s75,s75,16384
v_exp_f32 v94,v94
v_mfma_f32_16x16x32_bf16 v[116:119],a[148:151],v[232:235],v[116:119]
v_exp_f32 v95,v95
v_mfma_f32_16x16x32_bf16 v[116:119],a[144:147],v[224:227],v[116:119]
v_cvt_pk_bf16_f32 v90,v92,v93
v_cvt_pk_bf16_f32 v91,v94,v95
v_mfma_f32_16x16x32_bf16 v[120:123],a[160:163],v[224:227],v[44:47]
ds_read_b128 a[128:131],v1
v_accvgpr_read_b32 v16,a32
v_mfma_f32_16x16x32_bf16 v[120:123],a[164:167],v[232:235],v[120:123]
v_exp_f32 v96,v96
v_mfma_f32_16x16x32_bf16 v[120:123],a[168:171],v[240:243],v[120:123]
ds_read_b128 a[132:135],v1 offset:256
v_accvgpr_read_b32 v17,a33
v_mfma_f32_16x16x32_bf16 v[120:123],a[172:175],v[248:251],v[120:123]
v_exp_f32 v97,v97
v_mfma_f32_16x16x32_bf16 v[124:127],a[188:191],v[248:251],v[44:47]
ds_read_b128 a[136:139],v1 offset:512
v_cvt_pk_bf16_f32 v96,v96,v97
v_mfma_f32_16x16x32_bf16 v[124:127],a[184:187],v[240:243],v[124:127]
v_exp_f32 v98,v98
v_mfma_f32_16x16x32_bf16 v[124:127],a[180:183],v[232:235],v[124:127]
ds_read_b128 a[140:143],v1 offset:768
v_accvgpr_read_b32 v18,a34
v_mfma_f32_16x16x32_bf16 v[124:127],a[176:179],v[224:227],v[124:127]
v_exp_f32 v99,v99
v_mfma_f32_16x16x32_bf16 v[132:135],a[192:195],v[80:83],0
buffer_load_dwordx4 v5,s[24:27],s74 offen offset:3072 lds
ds_read_b128 a[156:159],v1 offset:4864
v_mfma_f32_16x16x32_bf16 v[132:135],a[196:199],v[88:91],v[132:135]
s_add_u32 s74,s74,16384
s_and_b32 s74,s74,0x3ffff
s_or_b32 s74,s74,s69
v_mfma_f32_16x16x32_bf16 v[140:143],a[200:203],v[80:83],0
s_and_b32 s69,s75,0xfffc0000
ds_read_b128 a[152:155],v1 offset:4608
v_cvt_pk_bf16_f32 v97,v98,v99
v_mfma_f32_16x16x32_bf16 v[140:143],a[204:207],v[88:91],v[140:143]
s_add_u32 s75,s75,16384
v_exp_f32 v100,v100
v_mfma_f32_16x16x32_bf16 v[148:151],a[208:211],v[80:83],0
ds_read_b128 a[148:151],v1 offset:4352
v_accvgpr_read_b32 v19,a35
v_mfma_f32_16x16x32_bf16 v[148:151],a[212:215],v[88:91],v[148:151]
v_exp_f32 v101,v101
v_mfma_f32_16x16x32_bf16 v[156:159],a[216:219],v[80:83],0
ds_read_b128 a[144:147],v1 offset:4096
v_cvt_pk_bf16_f32 v98,v100,v101
v_mfma_f32_16x16x32_bf16 v[156:159],a[220:223],v[88:91],v[156:159]
v_exp_f32 v102,v102
v_mfma_f32_16x16x32_bf16 v[164:167],a[224:227],v[80:83],0
ds_read_b128 a[160:163],v1 offset:8192
v_accvgpr_read_b32 v20,a36
v_mfma_f32_16x16x32_bf16 v[164:167],a[228:231],v[88:91],v[164:167]
v_exp_f32 v103,v103
v_mfma_f32_16x16x32_bf16 v[172:175],a[232:235],v[80:83],0
ds_read_b128 a[164:167],v1 offset:8448
v_cvt_pk_bf16_f32 v99,v102,v103
v_mfma_f32_16x16x32_bf16 v[172:175],a[236:239],v[88:91],v[172:175]
v_exp_f32 v104,v104
v_mfma_f32_16x16x32_bf16 v[180:183],a[240:243],v[80:83],0
s_add_u32 m0,s76,0
ds_read_b128 a[168:171],v1 offset:8704
v_accvgpr_read_b32 v21,a37
v_mfma_f32_16x16x32_bf16 v[180:183],a[244:247],v[88:91],v[180:183]
v_exp_f32 v105,v105
v_mfma_f32_16x16x32_bf16 v[188:191],a[248:251],v[80:83],0
buffer_load_dwordx4 v4,s[20:23],s74 offen nt lds
ds_read_b128 a[172:175],v1 offset:8960
v_mfma_f32_16x16x32_bf16 v[188:191],a[252:255],v[88:91],v[188:191]
v_exp_f32 v106,v106
v_mfma_f32_16x16x32_bf16 v[52:55],v[28:31],v[80:83],0
ds_read_b128 a[188:191],v1 offset:13056
v_cvt_pk_bf16_f32 v104,v104,v105
v_mfma_f32_16x16x32_bf16 v[52:55],v[28:31],v[88:91],v[52:55]
s_waitcnt lgkmcnt(4)
v_exp_f32 v107,v107
v_mfma_f32_16x16x32_bf16 v[64:67],a[128:131],v[128:131],v[32:35]
ds_read_b128 a[184:187],v1 offset:12800
v_cvt_pk_bf16_f32 v105,v106,v107
v_mfma_f32_16x16x32_bf16 v[64:67],a[132:135],v[136:139],v[64:67]
v_exp_f32 v108,v108
v_mfma_f32_16x16x32_bf16 v[64:67],a[136:139],v[144:147],v[64:67]
ds_read_b128 a[180:183],v1 offset:12544
v_accvgpr_read_b32 v22,a38
v_mfma_f32_16x16x32_bf16 v[64:67],a[140:143],v[152:155],v[64:67]
v_exp_f32 v109,v109
v_mfma_f32_16x16x32_bf16 v[68:71],a[156:159],v[152:155],v[32:35]
ds_read_b128 a[176:179],v1 offset:12288
v_cvt_pk_bf16_f32 v106,v108,v109
v_mfma_f32_16x16x32_bf16 v[68:71],a[152:155],v[144:147],v[68:71]
v_exp_f32 v110,v110
v_mfma_f32_16x16x32_bf16 v[68:71],a[148:151],v[136:139],v[68:71]
v_exp_f32 v111,v111
v_mfma_f32_16x16x32_bf16 v[68:71],a[144:147],v[128:131],v[68:71]
v_cvt_pk_bf16_f32 v107,v110,v111
v_accvgpr_read_b32 v23,a39
v_mfma_f32_16x16x32_bf16 v[72:75],a[160:163],v[128:131],v[32:35]
s_waitcnt lgkmcnt(2)
v_exp_f32 v112,v112
v_mfma_f32_16x16x32_bf16 v[72:75],a[164:167],v[136:139],v[72:75]
v_exp_f32 v113,v113
v_mfma_f32_16x16x32_bf16 v[72:75],a[168:171],v[144:147],v[72:75]
buffer_load_dwordx4 v4,s[20:23],s74 offen offset:1024 nt lds
v_cvt_pk_bf16_f32 v112,v112,v113
v_mfma_f32_16x16x32_bf16 v[72:75],a[172:175],v[152:155],v[72:75]
v_exp_f32 v114,v114
v_mfma_f32_16x16x32_bf16 v[76:79],a[188:191],v[152:155],v[32:35]
v_exp_f32 v115,v115
v_mfma_f32_16x16x32_bf16 v[76:79],a[184:187],v[144:147],v[76:79]
s_waitcnt lgkmcnt(0)
)A"
R"A(v_exp_f32 v116,v116
v_mfma_f32_16x16x32_bf16 v[76:79],a[180:183],v[136:139],v[76:79]
v_exp_f32 v117,v117
v_mfma_f32_16x16x32_bf16 v[76:79],a[176:179],v[128:131],v[76:79]
v_exp_f32 v118,v118
v_mfma_f32_16x16x32_bf16 v[196:199],a[192:195],v[96:99],0
v_exp_f32 v119,v119
v_mfma_f32_16x16x32_bf16 v[196:199],a[196:199],v[104:107],v[196:199]
v_cvt_pk_bf16_f32 v113,v114,v115
v_cvt_pk_bf16_f32 v114,v116,v117
v_mfma_f32_16x16x32_bf16 v[204:207],a[200:203],v[96:99],0
v_cvt_pk_bf16_f32 v115,v118,v119
v_mul_f32 v16,v16,v25
v_mfma_f32_16x16x32_bf16 v[204:207],a[204:207],v[104:107],v[204:207]
v_exp_f32 v120,v120
v_mfma_f32_16x16x32_bf16 v[212:215],a[208:211],v[96:99],0
v_exp_f32 v121,v121
v_mfma_f32_16x16x32_bf16 v[212:215],a[212:215],v[104:107],v[212:215]
v_exp_f32 v122,v122
v_mfma_f32_16x16x32_bf16 v[220:223],a[216:219],v[96:99],0
v_exp_f32 v123,v123
v_mfma_f32_16x16x32_bf16 v[220:223],a[220:223],v[104:107],v[220:223]
v_exp_f32 v124,v124
v_mfma_f32_16x16x32_bf16 v[228:231],a[224:227],v[96:99],0
buffer_load_dwordx4 v5,s[20:23],s74 offen offset:2048 nt lds
v_cvt_pk_bf16_f32 v120,v120,v121
v_mfma_f32_16x16x32_bf16 v[228:231],a[228:231],v[104:107],v[228:231]
v_exp_f32 v125,v125
v_mfma_f32_16x16x32_bf16 v[236:239],a[232:235],v[96:99],0
v_exp_f32 v126,v126
v_mfma_f32_16x16x32_bf16 v[236:239],a[236:239],v[104:107],v[236:239]
v_exp_f32 v127,v127
v_mfma_f32_16x16x32_bf16 v[244:247],a[240:243],v[96:99],0
v_cvt_pk_bf16_f32 v121,v122,v123
v_cvt_pk_bf16_f32 v122,v124,v125
v_mfma_f32_16x16x32_bf16 v[244:247],a[244:247],v[104:107],v[244:247]
v_cvt_pk_bf16_f32 v123,v126,v127
v_mul_f32 v17,v17,v25
v_mfma_f32_16x16x32_bf16 v[252:255],a[248:251],v[96:99],0
v_mul_f32 v18,v18,v25
v_mul_f32 v19,v19,v25
v_mfma_f32_16x16x32_bf16 v[252:255],a[252:255],v[104:107],v[252:255]
v_mul_f32 v20,v20,v25
v_mul_f32 v21,v21,v25
v_mfma_f32_16x16x32_bf16 v[56:59],v[28:31],v[96:99],0
v_mul_f32 v22,v22,v25
v_mul_f32 v23,v23,v25
v_mfma_f32_16x16x32_bf16 v[56:59],v[28:31],v[104:107],v[56:59]
v_cvt_pk_bf16_f32 v16,v16,v17
v_cvt_pk_bf16_f32 v17,v18,v19
v_mfma_f32_16x16x32_bf16 v[80:83],a[128:131],v[160:163],v[36:39]
v_cvt_pk_bf16_f32 v18,v20,v21
v_cvt_pk_bf16_f32 v19,v22,v23
v_mfma_f32_16x16x32_bf16 v[80:83],a[132:135],v[168:171],v[80:83]
ds_write_b128 v7,v[16:19] offset:2048
v_mfma_f32_16x16x32_bf16 v[80:83],a[136:139],v[176:179],v[80:83]
v_accvgpr_read_b32 v20,a44
v_accvgpr_read_b32 v21,a45
v_mfma_f32_16x16x32_bf16 v[80:83],a[140:143],v[184:187],v[80:83]
v_accvgpr_read_b32 v16,a40
v_accvgpr_read_b32 v17,a41
v_mfma_f32_16x16x32_bf16 v[84:87],a[156:159],v[184:187],v[36:39]
v_accvgpr_read_b32 v18,a42
v_accvgpr_read_b32 v19,a43
v_mfma_f32_16x16x32_bf16 v[84:87],a[152:155],v[176:179],v[84:87]
buffer_load_dwordx4 v5,s[20:23],s74 offen offset:3072 lds
v_accvgpr_read_b32 v22,a46
v_mfma_f32_16x16x32_bf16 v[84:87],a[148:151],v[168:171],v[84:87]
v_accvgpr_read_b32 v23,a47
v_accvgpr_read_b32 v8,a48
v_mfma_f32_16x16x32_bf16 v[84:87],a[144:147],v[160:163],v[84:87]
v_accvgpr_read_b32 v9,a49
v_accvgpr_read_b32 v10,a50
v_mfma_f32_16x16x32_bf16 v[88:91],a[160:163],v[160:163],v[36:39]
v_accvgpr_read_b32 v11,a51
v_mul_f32 v16,v16,v25
v_mfma_f32_16x16x32_bf16 v[88:91],a[164:167],v[168:171],v[88:91]
v_mul_f32 v17,v17,v25
v_mul_f32 v18,v18,v25
v_mfma_f32_16x16x32_bf16 v[88:91],a[168:171],v[176:179],v[88:91]
v_mul_f32 v19,v19,v25
v_mul_f32 v20,v20,v25
v_mfma_f32_16x16x32_bf16 v[88:91],a[172:175],v[184:187],v[88:91]
v_mul_f32 v21,v21,v25
v_mul_f32 v22,v22,v25
v_mfma_f32_16x16x32_bf16 v[92:95],a[188:191],v[184:187],v[36:39]
v_mul_f32 v23,v23,v25
v_cvt_pk_bf16_f32 v16,v16,v17
v_mfma_f32_16x16x32_bf16 v[92:95],a[184:187],v[176:179],v[92:95]
v_cvt_pk_bf16_f32 v17,v18,v19
v_cvt_pk_bf16_f32 v18,v20,v21
v_mfma_f32_16x16x32_bf16 v[92:95],a[180:183],v[168:171],v[92:95]
v_cvt_pk_bf16_f32 v19,v22,v23
v_accvgpr_read_b32 v12,a52
v_mfma_f32_16x16x32_bf16 v[92:95],a[176:179],v[160:163],v[92:95]
ds_write_b128 v7,v[16:19] offset:3072
v_mfma_f32_16x16x32_bf16 a[32:35],a[192:195],v[112:115],0
v_accvgpr_read_b32 v13,a53
v_accvgpr_read_b32 v14,a54
v_mfma_f32_16x16x32_bf16 a[32:35],a[196:199],v[120:123],a[32:35]
s_add_u32 m0,s76,16384
v_accvgpr_read_b32 v15,a55
v_accvgpr_read_b32 v16,a56
v_mfma_f32_16x16x32_bf16 a[36:39],a[200:203],v[112:115],0
v_accvgpr_read_b32 v17,a57
v_accvgpr_read_b32 v18,a58
v_mfma_f32_16x16x32_bf16 a[36:39],a[204:207],v[120:123],a[36:39]
buffer_load_dwordx4 v4,s[24:27],s74 offen lds
v_accvgpr_read_b32 v19,a59
v_mfma_f32_16x16x32_bf16 a[40:43],a[208:211],v[112:115],0
v_accvgpr_read_b32 v20,a60
v_accvgpr_read_b32 v21,a61
v_mfma_f32_16x16x32_bf16 a[40:43],a[212:215],v[120:123],a[40:43]
v_accvgpr_read_b32 v22,a62
v_accvgpr_read_b32 v23,a63
v_mfma_f32_16x16x32_bf16 a[44:47],a[216:219],v[112:115],0
v_add_u32 v2,s78,v2
v_add_u32 v3,s78,v3
v_mfma_f32_16x16x32_bf16 a[44:47],a[220:223],v[120:123],a[44:47]
s_sub_u32 s78,s77,32768
ds_read_b64_tr_b16 a[192:193],v2
ds_read_b64_tr_b16 a[194:195],v2 offset:4096
v_mfma_f32_16x16x32_bf16 a[48:51],a[224:227],v[112:115],0
s_and_b32 s78,s78,0x1ffff
s_sub_u32 s78,s78,s77
ds_read_b64_tr_b16 a[196:197],v2 offset:8192
v_mfma_f32_16x16x32_bf16 a[48:51],a[228:231],v[120:123],a[48:51]
s_add_u32 s77,s77,s78
ds_read_b64_tr_b16 a[198:199],v2 offset:12288
v_add_u32 v1,s78,v1
v_mfma_f32_16x16x32_bf16 a[52:55],a[232:235],v[112:115],0
ds_read_b64_tr_b16 a[200:201],v3
v_mul_f32 v8,v8,v25
v_mfma_f32_16x16x32_bf16 a[52:55],a[236:239],v[120:123],a[52:55]
ds_read_b64_tr_b16 a[202:203],v3 offset:4096
ds_read_b64_tr_b16 a[204:205],v3 offset:8192
v_mfma_f32_16x16x32_bf16 a[56:59],a[240:243],v[112:115],0
ds_read_b64_tr_b16 a[206:207],v3 offset:12288
v_mul_f32 v9,v9,v25
v_mfma_f32_16x16x32_bf16 a[56:59],a[244:247],v[120:123],a[56:59]
ds_read_b64_tr_b16 a[208:209],v2 offset:256
v_mul_f32 v10,v10,v25
v_mfma_f32_16x16x32_bf16 a[60:63],a[248:251],v[112:115],0
ds_read_b64_tr_b16 a[210:211],v2 offset:4352
v_mul_f32 v11,v11,v25
v_mfma_f32_16x16x32_bf16 a[60:63],a[252:255],v[120:123],a[60:63]
ds_read_b64_tr_b16 a[212:213],v2 offset:8448
ds_read_b64_tr_b16 a[214:215],v2 offset:12544
v_mfma_f32_16x16x32_bf16 v[60:63],v[28:31],v[112:115],0
v_exp_f32 v64,v64
v_mfma_f32_16x16x32_bf16 v[60:63],v[28:31],v[120:123],v[60:63]
buffer_load_dwordx4 v5,s[24:27],s74 offen offset:1024 lds
ds_read_b64_tr_b16 a[216:217],v3 offset:256
v_mfma_f32_16x16x32_bf16 v[96:99],a[128:131],v[192:195],v[40:43]
v_exp_f32 v65,v65
v_mfma_f32_16x16x32_bf16 v[96:99],a[132:135],v[200:203],v[96:99]
v_exp_f32 v66,v66
v_mfma_f32_16x16x32_bf16 v[96:99],a[136:139],v[208:211],v[96:99]
v_exp_f32 v67,v67
v_mfma_f32_16x16x32_bf16 v[96:99],a[140:143],v[216:219],v[96:99]
v_exp_f32 v68,v68
v_mfma_f32_16x16x32_bf16 v[100:103],a[156:159],v[216:219],v[40:43]
v_exp_f32 v69,v69
v_mfma_f32_16x16x32_bf16 v[100:103],a[152:155],v[208:211],v[100:103]
v_exp_f32 v70,v70
v_mfma_f32_16x16x32_bf16 v[100:103],a[148:151],v[200:203],v[100:103]
v_exp_f32 v71,v71
v_mfma_f32_16x16x32_bf16 v[100:103],a[144:147],v[192:195],v[100:103]
ds_read_b64_tr_b16 a[218:219],v3 offset:4352
v_cvt_pk_bf16_f32 v64,v64,v65
v_mfma_f32_16x16x32_bf16 v[104:107],a[160:163],v[192:195],v[40:43]
v_cvt_pk_bf16_f32 v65,v66,v67
v_cvt_pk_bf16_f32 v66,v68,v69
v_mfma_f32_16x16x32_bf16 v[104:107],a[164:167],v[200:203],v[104:107]
ds_read_b64_tr_b16 a[220:221],v3 offset:8448
v_cvt_pk_bf16_f32 v67,v70,v71
v_mfma_f32_16x16x32_bf16 v[104:107],a[168:171],v[208:211],v[104:107]
v_exp_f32 v72,v72
v_mfma_f32_16x16x32_bf16 v[104:107],a[172:175],v[216:219],v[104:107]
v_exp_f32 v73,v73
v_mfma_f32_16x16x32_bf16 v[108:111],a[188:191],v[216:219],v[40:43]
v_exp_f32 v74,v74
v_mfma_f32_16x16x32_bf16 v[108:111],a[184:187],v[208:211],v[108:111]
buffer_load_dwordx4 v4,s[24:27],s74 offen offset:2048 lds
v_exp_f32 v75,v75
v_exp_f32 v76,v76
v_exp_f32 v77,v77
v_exp_f32 v78,v78
v_exp_f32 v79,v79
v_mfma_f32_16x16x32_bf16 v[108:111],a[180:183],v[200:203],v[108:111]
ds_read_b64_tr_b16 a[222:223],v3 offset:12544
v_cvt_pk_bf16_f32 v72,v72,v73
v_cvt_pk_bf16_f32 v73,v74,v75
v_cvt_pk_bf16_f32 v74,v76,v77
v_cvt_pk_bf16_f32 v75,v78,v79
v_mfma_f32_16x16x32_bf16 v[108:111],a[176:179],v[192:195],v[108:111]
s_waitcnt lgkmcnt(2)
ds_read_b64_tr_b16 a[224:225],v2 offset:512
ds_read_b64_tr_b16 a[226:227],v2 offset:4608
v_mfma_f32_16x16x32_bf16 a[0:3],a[192:195],v[64:67],a[0:3]
ds_read_b64_tr_b16 a[228:229],v2 offset:8704
v_mul_f32 v12,v12,v25
v_mfma_f32_16x16x32_bf16 a[0:3],a[196:199],v[72:75],a[0:3]
ds_read_b64_tr_b16 a[230:231],v2 offset:12800
v_mul_f32 v13,v13,v25
v_mfma_f32_16x16x32_bf16 a[4:7],a[200:203],v[64:67],a[4:7]
ds_read_b64_tr_b16 a[232:233],v3 offset:512
v_mul_f32 v14,v14,v25
v_mfma_f32_16x16x32_bf16 a[4:7],a[204:207],v[72:75],a[4:7]
ds_read_b64_tr_b16 a[234:235],v3 offset:4608
ds_read_b64_tr_b16 a[236:237],v3 offset:8704
v_mfma_f32_16x16x32_bf16 a[8:11],a[208:211],v[64:67],a[8:11]
ds_read_b64_tr_b16 a[238:239],v3 offset:12800
v_mul_f32 v15,v15,v25
v_mfma_f32_16x16x32_bf16 a[8:11],a[212:215],v[72:75],a[8:11]
ds_read_b64_tr_b16 a[240:241],v2 offset:768
v_cvt_pk_bf16_f32 v8,v8,v9
v_mfma_f32_16x16x32_bf16 a[12:15],a[216:219],v[64:67],a[12:15]
s_waitcnt lgkmcnt(9)
ds_read_b64_tr_b16 a[242:243],v2 offset:4864
v_cvt_pk_bf16_f32 v9,v10,v11
v_mfma_f32_16x16x32_bf16 a[12:15],a[220:223],v[72:75],a[12:15]
s_waitcnt lgkmcnt(8)
ds_read_b64_tr_b16 a[244:245],v2 offset:8960
ds_read_b64_tr_b16 a[246:247],v2 offset:13056
v_mfma_f32_16x16x32_bf16 a[16:19],a[224:227],v[64:67],a[16:19]
s_waitcnt lgkmcnt(8)
ds_read_b64_tr_b16 a[248:249],v3 offset:768
v_add_u32 v2,s78,v2
v_mfma_f32_16x16x32_bf16 a[16:19],a[228:231],v[72:75],a[16:19]
s_waitcnt lgkmcnt(7)
s_nop 0
L_pd8_%=:
s_cbranch_execz L_trt0_%=
ds_read_b64_tr_b16 a[250:251],v3 offset:4864
v_cvt_pk_bf16_f32 v10,v12,v13
v_mfma_f32_16x16x32_bf16 a[20:23],a[232:235],v[64:67],a[20:23]
s_getreg_b32 s92,hwreg(HW_REG_IB_STS,0,4)
s_min_u32 s4,s92,s6
ds_read_b64_tr_b16 a[252:253],v3 offset:8960
s_waitcnt lgkmcnt(7)
v_mfma_f32_16x16x32_bf16 a[20:23],a[236:239],v[72:75],a[20:23]
s_mov_b32 s6,s92
ds_read_b64_tr_b16 a[254:255],v3 offset:13056
v_add_u32 v3,s78,v3
s_waitcnt lgkmcnt(6)
v_mfma_f32_16x16x32_bf16 a[24:27],a[240:243],v[64:67],a[24:27]
s_cmp_ge_u32 s4,9
s_cselect_b32 s4,14,s5
s_sub_u32 s78,s77,32768
s_waitcnt lgkmcnt(4)
v_mfma_f32_16x16x32_bf16 a[24:27],a[244:247],v[72:75],a[24:27]
s_and_b32 s78,s78,0x1ffff
v_exp_f32 v80,v80
s_waitcnt lgkmcnt(2)
v_mfma_f32_16x16x32_bf16 a[28:31],a[248:251],v[64:67],a[28:31]
s_sub_u32 s78,s78,s77
v_exp_f32 v81,v81
s_waitcnt lgkmcnt(0)
v_mfma_f32_16x16x32_bf16 a[28:31],a[252:255],v[72:75],a[28:31]
s_getpc_b64 s[92:93]
s_cmp_lg_u32 s4,0
s_cbranch_scc1 L_pace_%=
v_mfma_f32_16x16x32_bf16 v[48:51],v[28:31],v[64:67],v[48:51]
s_add_u32 s77,s77,s78
v_exp_f32 v82,v82
v_mfma_f32_16x16x32_bf16 v[48:51],v[28:31],v[72:75],v[48:51]
v_exp_f32 v83,v83
s_sleep 2
s_waitcnt vmcnt(15)
s_barrier
v_mfma_f32_16x16x32_bf16 v[112:115],a[128:131],v[224:227],v[44:47]
s_add_u32 m0,s76,16384
v_exp_f32 v84,v84
v_mfma_f32_16x16x32_bf16 v[112:115],a[132:135],v[232:235],v[112:115]
s_sub_u32 s76,s76,32768
v_exp_f32 v85,v85
v_mfma_f32_16x16x32_bf16 v[112:115],a[136:139],v[240:243],v[112:115]
s_and_b32 s76,s76,0x1ffff
buffer_load_dwordx4 v5,s[24:27],s74 offen offset:3072 lds
v_cvt_pk_bf16_f32 v80,v80,v81
v_mfma_f32_16x16x32_bf16 v[112:115],a[140:143],v[248:251],v[112:115]
s_add_u32 s74,s74,16384
v_exp_f32 v86,v86
v_mfma_f32_16x16x32_bf16 v[116:119],a[156:159],v[248:251],v[44:47]
s_and_b32 s74,s74,0x3ffff
v_exp_f32 v87,v87
v_mfma_f32_16x16x32_bf16 v[116:119],a[152:155],v[240:243],v[116:119]
s_or_b32 s74,s74,s69
v_cvt_pk_bf16_f32 v81,v82,v83
v_cvt_pk_bf16_f32 v82,v84,v85
v_mfma_f32_16x16x32_bf16 v[116:119],a[148:151],v[232:235],v[116:119]
s_and_b32 s69,s75,0xfffc0000
ds_read_b128 a[128:131],v1
v_cvt_pk_bf16_f32 v83,v86,v87
v_mfma_f32_16x16x32_bf16 v[116:119],a[144:147],v[224:227],v[116:119]
s_add_u32 s75,s75,16384
v_exp_f32 v88,v88
v_mfma_f32_16x16x32_bf16 v[120:123],a[160:163],v[224:227],v[44:47]
v_exp_f32 v89,v89
v_mfma_f32_16x16x32_bf16 v[120:123],a[164:167],v[232:235],v[120:123]
v_exp_f32 v90,v90
v_mfma_f32_16x16x32_bf16 v[120:123],a[168:171],v[240:243],v[120:123]
)A"
R"A(v_exp_f32 v91,v91
v_mfma_f32_16x16x32_bf16 v[120:123],a[172:175],v[248:251],v[120:123]
v_exp_f32 v92,v92
v_mfma_f32_16x16x32_bf16 v[124:127],a[188:191],v[248:251],v[44:47]
v_exp_f32 v93,v93
v_mfma_f32_16x16x32_bf16 v[124:127],a[184:187],v[240:243],v[124:127]
v_exp_f32 v94,v94
v_exp_f32 v95,v95
v_mfma_f32_16x16x32_bf16 v[124:127],a[180:183],v[232:235],v[124:127]
v_cvt_pk_bf16_f32 v88,v88,v89
v_cvt_pk_bf16_f32 v89,v90,v91
v_cvt_pk_bf16_f32 v90,v92,v93
v_cvt_pk_bf16_f32 v91,v94,v95
v_mfma_f32_16x16x32_bf16 v[124:127],a[176:179],v[224:227],v[124:127]
s_add_u32 m0,s76,0
ds_read_b128 a[132:135],v1 offset:256
v_cvt_pk_bf16_f32 v11,v14,v15
v_mfma_f32_16x16x32_bf16 v[132:135],a[192:195],v[80:83],v[132:135]
buffer_load_dwordx4 v4,s[20:23],s74 offen nt lds
v_mul_f32 v16,v16,v25
v_mfma_f32_16x16x32_bf16 v[132:135],a[196:199],v[88:91],v[132:135]
ds_read_b128 a[136:139],v1 offset:512
v_mul_f32 v17,v17,v25
v_mfma_f32_16x16x32_bf16 v[140:143],a[200:203],v[80:83],v[140:143]
v_exp_f32 v96,v96
v_mfma_f32_16x16x32_bf16 v[140:143],a[204:207],v[88:91],v[140:143]
ds_read_b128 a[140:143],v1 offset:768
v_mul_f32 v18,v18,v25
v_mfma_f32_16x16x32_bf16 v[148:151],a[208:211],v[80:83],v[148:151]
v_exp_f32 v97,v97
v_mfma_f32_16x16x32_bf16 v[148:151],a[212:215],v[88:91],v[148:151]
ds_read_b128 a[156:159],v1 offset:4864
v_cvt_pk_bf16_f32 v96,v96,v97
v_mfma_f32_16x16x32_bf16 v[156:159],a[216:219],v[80:83],v[156:159]
v_exp_f32 v98,v98
v_mfma_f32_16x16x32_bf16 v[156:159],a[220:223],v[88:91],v[156:159]
ds_read_b128 a[152:155],v1 offset:4608
v_mul_f32 v19,v19,v25
v_mfma_f32_16x16x32_bf16 v[164:167],a[224:227],v[80:83],v[164:167]
v_exp_f32 v99,v99
v_mfma_f32_16x16x32_bf16 v[164:167],a[228:231],v[88:91],v[164:167]
ds_read_b128 a[148:151],v1 offset:4352
v_cvt_pk_bf16_f32 v97,v98,v99
v_mfma_f32_16x16x32_bf16 v[172:175],a[232:235],v[80:83],v[172:175]
v_exp_f32 v100,v100
v_mfma_f32_16x16x32_bf16 v[172:175],a[236:239],v[88:91],v[172:175]
ds_read_b128 a[144:147],v1 offset:4096
v_mul_f32 v20,v20,v25
v_mfma_f32_16x16x32_bf16 v[180:183],a[240:243],v[80:83],v[180:183]
v_exp_f32 v101,v101
v_mfma_f32_16x16x32_bf16 v[180:183],a[244:247],v[88:91],v[180:183]
ds_read_b128 a[160:163],v1 offset:8192
v_cvt_pk_bf16_f32 v98,v100,v101
v_mfma_f32_16x16x32_bf16 v[188:191],a[248:251],v[80:83],v[188:191]
buffer_load_dwordx4 v4,s[20:23],s74 offen offset:1024 nt lds
v_mul_f32 v21,v21,v25
v_mfma_f32_16x16x32_bf16 v[188:191],a[252:255],v[88:91],v[188:191]
ds_read_b128 a[164:167],v1 offset:8448
v_mul_f32 v22,v22,v25
v_mfma_f32_16x16x32_bf16 v[52:55],v[28:31],v[80:83],v[52:55]
v_exp_f32 v102,v102
v_mfma_f32_16x16x32_bf16 v[52:55],v[28:31],v[88:91],v[52:55]
s_waitcnt lgkmcnt(3)
ds_read_b128 a[168:171],v1 offset:8704
v_mul_f32 v23,v23,v25
v_mfma_f32_16x16x32_bf16 v[64:67],a[128:131],v[128:131],v[32:35]
v_exp_f32 v103,v103
v_mfma_f32_16x16x32_bf16 v[64:67],a[132:135],v[136:139],v[64:67]
ds_read_b128 a[172:175],v1 offset:8960
v_cvt_pk_bf16_f32 v99,v102,v103
v_mfma_f32_16x16x32_bf16 v[64:67],a[136:139],v[144:147],v[64:67]
v_exp_f32 v104,v104
v_mfma_f32_16x16x32_bf16 v[64:67],a[140:143],v[152:155],v[64:67]
ds_read_b128 a[188:191],v1 offset:13056
v_cvt_pk_bf16_f32 v12,v16,v17
v_mfma_f32_16x16x32_bf16 v[68:71],a[156:159],v[152:155],v[32:35]
v_exp_f32 v105,v105
v_mfma_f32_16x16x32_bf16 v[68:71],a[152:155],v[144:147],v[68:71]
ds_read_b128 a[184:187],v1 offset:12800
v_cvt_pk_bf16_f32 v104,v104,v105
v_mfma_f32_16x16x32_bf16 v[68:71],a[148:151],v[136:139],v[68:71]
s_waitcnt lgkmcnt(4)
v_exp_f32 v106,v106
v_mfma_f32_16x16x32_bf16 v[68:71],a[144:147],v[128:131],v[68:71]
ds_read_b128 a[180:183],v1 offset:12544
v_cvt_pk_bf16_f32 v13,v18,v19
v_mfma_f32_16x16x32_bf16 v[72:75],a[160:163],v[128:131],v[32:35]
v_exp_f32 v107,v107
v_mfma_f32_16x16x32_bf16 v[72:75],a[164:167],v[136:139],v[72:75]
s_waitcnt lgkmcnt(3)
ds_read_b128 a[176:179],v1 offset:12288
v_cvt_pk_bf16_f32 v105,v106,v107
v_mfma_f32_16x16x32_bf16 v[72:75],a[168:171],v[144:147],v[72:75]
buffer_load_dwordx4 v5,s[20:23],s74 offen offset:2048 nt lds
v_add_u32 v1,s78,v1
v_mfma_f32_16x16x32_bf16 v[72:75],a[172:175],v[152:155],v[72:75]
s_waitcnt lgkmcnt(3)
v_exp_f32 v108,v108
v_mfma_f32_16x16x32_bf16 v[76:79],a[188:191],v[152:155],v[32:35]
s_waitcnt lgkmcnt(2)
v_exp_f32 v109,v109
v_mfma_f32_16x16x32_bf16 v[76:79],a[184:187],v[144:147],v[76:79]
s_waitcnt lgkmcnt(1)
v_exp_f32 v110,v110
v_exp_f32 v111,v111
v_mfma_f32_16x16x32_bf16 v[76:79],a[180:183],v[136:139],v[76:79]
s_waitcnt lgkmcnt(0)
v_cvt_pk_bf16_f32 v106,v108,v109
v_cvt_pk_bf16_f32 v107,v110,v111
v_mfma_f32_16x16x32_bf16 v[76:79],a[176:179],v[128:131],v[76:79]
v_exp_f32 v112,v112
v_mfma_f32_16x16x32_bf16 v[196:199],a[192:195],v[96:99],v[196:199]
v_exp_f32 v113,v113
v_mfma_f32_16x16x32_bf16 v[196:199],a[196:199],v[104:107],v[196:199]
v_exp_f32 v114,v114
v_mfma_f32_16x16x32_bf16 v[204:207],a[200:203],v[96:99],v[204:207]
v_exp_f32 v115,v115
v_mfma_f32_16x16x32_bf16 v[204:207],a[204:207],v[104:107],v[204:207]
v_exp_f32 v116,v116
v_mfma_f32_16x16x32_bf16 v[212:215],a[208:211],v[96:99],v[212:215]
v_exp_f32 v117,v117
v_mfma_f32_16x16x32_bf16 v[212:215],a[212:215],v[104:107],v[212:215]
v_exp_f32 v118,v118
v_mfma_f32_16x16x32_bf16 v[220:223],a[216:219],v[96:99],v[220:223]
v_exp_f32 v119,v119
v_mfma_f32_16x16x32_bf16 v[220:223],a[220:223],v[104:107],v[220:223]
v_cvt_pk_bf16_f32 v112,v112,v113
v_cvt_pk_bf16_f32 v113,v114,v115
v_mfma_f32_16x16x32_bf16 v[228:231],a[224:227],v[96:99],v[228:231]
buffer_load_dwordx4 v5,s[20:23],s74 offen offset:3072 lds
v_cvt_pk_bf16_f32 v114,v116,v117
v_mfma_f32_16x16x32_bf16 v[228:231],a[228:231],v[104:107],v[228:231]
v_cvt_pk_bf16_f32 v115,v118,v119
v_cvt_pk_bf16_f32 v14,v20,v21
v_mfma_f32_16x16x32_bf16 v[236:239],a[232:235],v[96:99],v[236:239]
v_exp_f32 v120,v120
v_mfma_f32_16x16x32_bf16 v[236:239],a[236:239],v[104:107],v[236:239]
v_exp_f32 v121,v121
v_mfma_f32_16x16x32_bf16 v[244:247],a[240:243],v[96:99],v[244:247]
v_exp_f32 v122,v122
v_mfma_f32_16x16x32_bf16 v[244:247],a[244:247],v[104:107],v[244:247]
v_exp_f32 v123,v123
v_mfma_f32_16x16x32_bf16 v[252:255],a[248:251],v[96:99],v[252:255]
v_exp_f32 v124,v124
v_mfma_f32_16x16x32_bf16 v[252:255],a[252:255],v[104:107],v[252:255]
v_exp_f32 v125,v125
v_mfma_f32_16x16x32_bf16 v[56:59],v[28:31],v[96:99],v[56:59]
v_exp_f32 v126,v126
v_mfma_f32_16x16x32_bf16 v[56:59],v[28:31],v[104:107],v[56:59]
v_exp_f32 v127,v127
v_mfma_f32_16x16x32_bf16 v[80:83],a[128:131],v[160:163],v[36:39]
v_cvt_pk_bf16_f32 v120,v120,v121
v_cvt_pk_bf16_f32 v121,v122,v123
v_mfma_f32_16x16x32_bf16 v[80:83],a[132:135],v[168:171],v[80:83]
v_cvt_pk_bf16_f32 v122,v124,v125
v_cvt_pk_bf16_f32 v123,v126,v127
v_mfma_f32_16x16x32_bf16 v[80:83],a[136:139],v[176:179],v[80:83]
v_exp_f32 v64,v64
v_mfma_f32_16x16x32_bf16 v[80:83],a[140:143],v[184:187],v[80:83]
s_add_u32 m0,s76,16384
v_exp_f32 v65,v65
v_mfma_f32_16x16x32_bf16 v[84:87],a[156:159],v[184:187],v[36:39]
s_sub_u32 s76,s76,32768
v_exp_f32 v66,v66
v_mfma_f32_16x16x32_bf16 v[84:87],a[152:155],v[176:179],v[84:87]
s_and_b32 s76,s76,0x1ffff
buffer_load_dwordx4 v4,s[24:27],s74 offen lds
v_cvt_pk_bf16_f32 v64,v64,v65
v_mfma_f32_16x16x32_bf16 v[84:87],a[148:151],v[168:171],v[84:87]
v_exp_f32 v67,v67
v_mfma_f32_16x16x32_bf16 v[84:87],a[144:147],v[160:163],v[84:87]
v_exp_f32 v68,v68
v_mfma_f32_16x16x32_bf16 v[88:91],a[160:163],v[160:163],v[36:39]
v_exp_f32 v69,v69
v_mfma_f32_16x16x32_bf16 v[88:91],a[164:167],v[168:171],v[88:91]
v_exp_f32 v70,v70
v_mfma_f32_16x16x32_bf16 v[88:91],a[168:171],v[176:179],v[88:91]
v_exp_f32 v71,v71
v_mfma_f32_16x16x32_bf16 v[88:91],a[172:175],v[184:187],v[88:91]
v_cvt_pk_bf16_f32 v65,v66,v67
v_cvt_pk_bf16_f32 v66,v68,v69
v_mfma_f32_16x16x32_bf16 v[92:95],a[188:191],v[184:187],v[36:39]
v_cvt_pk_bf16_f32 v67,v70,v71
v_cvt_pk_bf16_f32 v15,v22,v23
v_mfma_f32_16x16x32_bf16 v[92:95],a[184:187],v[176:179],v[92:95]
v_exp_f32 v72,v72
v_mfma_f32_16x16x32_bf16 v[92:95],a[180:183],v[168:171],v[92:95]
v_exp_f32 v73,v73
v_mfma_f32_16x16x32_bf16 v[92:95],a[176:179],v[160:163],v[92:95]
v_exp_f32 v74,v74
v_mfma_f32_16x16x32_bf16 a[32:35],a[192:195],v[112:115],a[32:35]
ds_read_b64_tr_b16 a[192:193],v2
ds_read_b64_tr_b16 a[194:195],v2 offset:4096
v_mfma_f32_16x16x32_bf16 a[32:35],a[196:199],v[120:123],a[32:35]
ds_read_b64_tr_b16 a[196:197],v2 offset:8192
v_cvt_pk_bf16_f32 v72,v72,v73
v_mfma_f32_16x16x32_bf16 a[36:39],a[200:203],v[112:115],a[36:39]
ds_read_b64_tr_b16 a[198:199],v2 offset:12288
v_accvgpr_read_b32 v16,a112
v_mfma_f32_16x16x32_bf16 a[36:39],a[204:207],v[120:123],a[36:39]
buffer_load_dwordx4 v5,s[24:27],s74 offen offset:1024 lds
ds_read_b64_tr_b16 a[200:201],v3
v_mfma_f32_16x16x32_bf16 a[40:43],a[208:211],v[112:115],a[40:43]
ds_read_b64_tr_b16 a[202:203],v3 offset:4096
ds_read_b64_tr_b16 a[204:205],v3 offset:8192
v_mfma_f32_16x16x32_bf16 a[40:43],a[212:215],v[120:123],a[40:43]
ds_read_b64_tr_b16 a[206:207],v3 offset:12288
v_accvgpr_read_b32 v17,a113
v_mfma_f32_16x16x32_bf16 a[44:47],a[216:219],v[112:115],a[44:47]
ds_read_b64_tr_b16 a[208:209],v2 offset:256
v_accvgpr_read_b32 v18,a114
v_mfma_f32_16x16x32_bf16 a[44:47],a[220:223],v[120:123],a[44:47]
ds_read_b64_tr_b16 a[210:211],v2 offset:4352
v_accvgpr_read_b32 v19,a115
v_mfma_f32_16x16x32_bf16 a[48:51],a[224:227],v[112:115],a[48:51]
ds_read_b64_tr_b16 a[212:213],v2 offset:8448
ds_read_b64_tr_b16 a[214:215],v2 offset:12544
v_mfma_f32_16x16x32_bf16 a[48:51],a[228:231],v[120:123],a[48:51]
ds_read_b64_tr_b16 a[216:217],v3 offset:256
v_accvgpr_read_b32 v20,a116
v_mfma_f32_16x16x32_bf16 a[52:55],a[232:235],v[112:115],a[52:55]
ds_read_b64_tr_b16 a[218:219],v3 offset:4352
v_accvgpr_read_b32 v21,a117
v_mfma_f32_16x16x32_bf16 a[52:55],a[236:239],v[120:123],a[52:55]
v_exp_f32 v75,v75
v_mfma_f32_16x16x32_bf16 a[56:59],a[240:243],v[112:115],a[56:59]
v_exp_f32 v76,v76
v_mfma_f32_16x16x32_bf16 a[56:59],a[244:247],v[120:123],a[56:59]
v_exp_f32 v77,v77
v_mfma_f32_16x16x32_bf16 a[60:63],a[248:251],v[112:115],a[60:63]
v_exp_f32 v78,v78
v_mfma_f32_16x16x32_bf16 a[60:63],a[252:255],v[120:123],a[60:63]
v_exp_f32 v79,v79
v_mfma_f32_16x16x32_bf16 v[60:63],v[28:31],v[112:115],v[60:63]
ds_read_b64_tr_b16 a[220:221],v3 offset:8448
ds_read_b64_tr_b16 a[222:223],v3 offset:12544
v_mfma_f32_16x16x32_bf16 v[60:63],v[28:31],v[120:123],v[60:63]
buffer_load_dwordx4 v4,s[24:27],s74 offen offset:2048 lds
v_cvt_pk_bf16_f32 v73,v74,v75
v_mfma_f32_16x16x32_bf16 v[96:99],a[128:131],v[192:195],v[40:43]
v_cvt_pk_bf16_f32 v74,v76,v77
v_cvt_pk_bf16_f32 v75,v78,v79
v_mfma_f32_16x16x32_bf16 v[96:99],a[132:135],v[200:203],v[96:99]
ds_read_b64_tr_b16 a[224:225],v2 offset:512
ds_read_b64_tr_b16 a[226:227],v2 offset:4608
v_mfma_f32_16x16x32_bf16 v[96:99],a[136:139],v[208:211],v[96:99]
ds_read_b64_tr_b16 a[228:229],v2 offset:8704
v_accvgpr_read_b32 v22,a118
v_mfma_f32_16x16x32_bf16 v[96:99],a[140:143],v[216:219],v[96:99]
ds_read_b64_tr_b16 a[230:231],v2 offset:12800
v_accvgpr_read_b32 v23,a119
v_mfma_f32_16x16x32_bf16 v[100:103],a[156:159],v[216:219],v[40:43]
ds_read_b64_tr_b16 a[232:233],v3 offset:512
v_mul_f32 v16,v16,v27
v_mfma_f32_16x16x32_bf16 v[100:103],a[152:155],v[208:211],v[100:103]
ds_read_b64_tr_b16 a[234:235],v3 offset:4608
ds_read_b64_tr_b16 a[236:237],v3 offset:8704
v_mfma_f32_16x16x32_bf16 v[100:103],a[148:151],v[200:203],v[100:103]
ds_read_b64_tr_b16 a[238:239],v3 offset:12800
v_mul_f32 v17,v17,v27
v_mfma_f32_16x16x32_bf16 v[100:103],a[144:147],v[192:195],v[100:103]
ds_read_b64_tr_b16 a[240:241],v2 offset:768
v_mul_f32 v18,v18,v27
v_mfma_f32_16x16x32_bf16 v[104:107],a[160:163],v[192:195],v[40:43]
ds_read_b64_tr_b16 a[242:243],v2 offset:4864
v_mul_f32 v19,v19,v27
v_mfma_f32_16x16x32_bf16 v[104:107],a[164:167],v[200:203],v[104:107]
ds_read_b64_tr_b16 a[244:245],v2 offset:8960
ds_read_b64_tr_b16 a[246:247],v2 offset:13056
v_mfma_f32_16x16x32_bf16 v[104:107],a[168:171],v[208:211],v[104:107]
ds_read_b64_tr_b16 a[248:249],v3 offset:768
v_add_u32 v2,s78,v2
v_mfma_f32_16x16x32_bf16 v[104:107],a[172:175],v[216:219],v[104:107]
ds_read_b64_tr_b16 a[250:251],v3 offset:4864
v_mul_f32 v20,v20,v27
v_mfma_f32_16x16x32_bf16 v[108:111],a[188:191],v[216:219],v[40:43]
)A"
R"A(ds_read_b64_tr_b16 a[252:253],v3 offset:8960
v_mul_f32 v21,v21,v27
v_mfma_f32_16x16x32_bf16 v[108:111],a[184:187],v[208:211],v[108:111]
buffer_load_dwordx4 v5,s[24:27],s74 offen offset:3072 lds
ds_read_b64_tr_b16 a[254:255],v3 offset:13056
v_mfma_f32_16x16x32_bf16 v[108:111],a[180:183],v[200:203],v[108:111]
s_add_u32 s74,s74,16384
s_and_b32 s74,s74,0x3ffff
s_or_b32 s74,s74,s69
v_mfma_f32_16x16x32_bf16 v[108:111],a[176:179],v[192:195],v[108:111]
s_and_b32 s69,s75,0xfffc0000
v_exp_f32 v80,v80
s_waitcnt lgkmcnt(7)
v_mfma_f32_16x16x32_bf16 a[0:3],a[192:195],v[64:67],a[0:3]
s_add_u32 s75,s75,16384
v_exp_f32 v81,v81
v_mfma_f32_16x16x32_bf16 a[0:3],a[196:199],v[72:75],a[0:3]
v_exp_f32 v82,v82
v_mfma_f32_16x16x32_bf16 a[4:7],a[200:203],v[64:67],a[4:7]
v_exp_f32 v83,v83
v_mfma_f32_16x16x32_bf16 a[4:7],a[204:207],v[72:75],a[4:7]
v_exp_f32 v84,v84
v_mfma_f32_16x16x32_bf16 a[8:11],a[208:211],v[64:67],a[8:11]
v_exp_f32 v85,v85
v_mfma_f32_16x16x32_bf16 a[8:11],a[212:215],v[72:75],a[8:11]
v_exp_f32 v86,v86
v_mfma_f32_16x16x32_bf16 a[12:15],a[216:219],v[64:67],a[12:15]
v_exp_f32 v87,v87
v_mfma_f32_16x16x32_bf16 a[12:15],a[220:223],v[72:75],a[12:15]
v_cvt_pk_bf16_f32 v80,v80,v81
v_cvt_pk_bf16_f32 v81,v82,v83
v_mfma_f32_16x16x32_bf16 a[16:19],a[224:227],v[64:67],a[16:19]
v_cvt_pk_bf16_f32 v82,v84,v85
v_cvt_pk_bf16_f32 v83,v86,v87
v_mfma_f32_16x16x32_bf16 a[16:19],a[228:231],v[72:75],a[16:19]
v_exp_f32 v88,v88
v_mfma_f32_16x16x32_bf16 a[20:23],a[232:235],v[64:67],a[20:23]
s_getreg_b32 s92,hwreg(HW_REG_IB_STS,0,4)
s_min_u32 s4,s92,s6
s_mov_b32 s6,s92
v_mfma_f32_16x16x32_bf16 a[20:23],a[236:239],v[72:75],a[20:23]
s_cmp_ge_u32 s4,9
s_cselect_b32 s4,14,s5
v_add_u32 v3,s78,v3
s_waitcnt lgkmcnt(0)
v_mfma_f32_16x16x32_bf16 a[24:27],a[240:243],v[64:67],a[24:27]
s_sub_u32 s78,s77,32768
v_exp_f32 v89,v89
v_mfma_f32_16x16x32_bf16 a[24:27],a[244:247],v[72:75],a[24:27]
s_and_b32 s78,s78,0x1ffff
v_exp_f32 v90,v90
v_mfma_f32_16x16x32_bf16 a[28:31],a[248:251],v[64:67],a[28:31]
s_sub_u32 s78,s78,s77
v_exp_f32 v91,v91
v_mfma_f32_16x16x32_bf16 a[28:31],a[252:255],v[72:75],a[28:31]
s_getpc_b64 s[92:93]
s_cmp_lg_u32 s4,0
s_cbranch_scc1 L_pace_%=
v_mfma_f32_16x16x32_bf16 v[48:51],v[28:31],v[64:67],v[48:51]
s_add_u32 s77,s77,s78
v_exp_f32 v92,v92
v_mfma_f32_16x16x32_bf16 v[48:51],v[28:31],v[72:75],v[48:51]
v_exp_f32 v93,v93
s_sleep 1
s_waitcnt vmcnt(16)
s_barrier
v_mfma_f32_16x16x32_bf16 v[112:115],a[128:131],v[224:227],v[44:47]
s_add_u32 m0,s76,0
v_exp_f32 v94,v94
v_mfma_f32_16x16x32_bf16 v[112:115],a[132:135],v[232:235],v[112:115]
v_exp_f32 v95,v95
v_mfma_f32_16x16x32_bf16 v[112:115],a[136:139],v[240:243],v[112:115]
buffer_load_dwordx4 v4,s[20:23],s74 offen nt lds
v_cvt_pk_bf16_f32 v88,v88,v89
v_mfma_f32_16x16x32_bf16 v[112:115],a[140:143],v[248:251],v[112:115]
v_cvt_pk_bf16_f32 v89,v90,v91
v_cvt_pk_bf16_f32 v90,v92,v93
v_mfma_f32_16x16x32_bf16 v[116:119],a[156:159],v[248:251],v[44:47]
ds_read_b128 a[128:131],v1
v_cvt_pk_bf16_f32 v91,v94,v95
v_mfma_f32_16x16x32_bf16 v[116:119],a[152:155],v[240:243],v[116:119]
v_exp_f32 v96,v96
v_mfma_f32_16x16x32_bf16 v[116:119],a[148:151],v[232:235],v[116:119]
ds_read_b128 a[132:135],v1 offset:256
v_mul_f32 v22,v22,v27
v_mfma_f32_16x16x32_bf16 v[116:119],a[144:147],v[224:227],v[116:119]
v_exp_f32 v97,v97
v_mfma_f32_16x16x32_bf16 v[120:123],a[160:163],v[224:227],v[44:47]
ds_read_b128 a[136:139],v1 offset:512
v_cvt_pk_bf16_f32 v96,v96,v97
v_mfma_f32_16x16x32_bf16 v[120:123],a[164:167],v[232:235],v[120:123]
v_exp_f32 v98,v98
v_mfma_f32_16x16x32_bf16 v[120:123],a[168:171],v[240:243],v[120:123]
ds_read_b128 a[140:143],v1 offset:768
v_mul_f32 v23,v23,v27
v_mfma_f32_16x16x32_bf16 v[120:123],a[172:175],v[248:251],v[120:123]
v_exp_f32 v99,v99
v_mfma_f32_16x16x32_bf16 v[124:127],a[188:191],v[248:251],v[44:47]
ds_read_b128 a[156:159],v1 offset:4864
v_cvt_pk_bf16_f32 v97,v98,v99
v_mfma_f32_16x16x32_bf16 v[124:127],a[184:187],v[240:243],v[124:127]
v_exp_f32 v100,v100
v_mfma_f32_16x16x32_bf16 v[124:127],a[180:183],v[232:235],v[124:127]
ds_read_b128 a[152:155],v1 offset:4608
v_cvt_pk_bf16_f32 v16,v16,v17
v_mfma_f32_16x16x32_bf16 v[124:127],a[176:179],v[224:227],v[124:127]
v_exp_f32 v101,v101
v_mfma_f32_16x16x32_bf16 v[132:135],a[192:195],v[80:83],v[132:135]
ds_read_b128 a[148:151],v1 offset:4352
v_cvt_pk_bf16_f32 v98,v100,v101
v_mfma_f32_16x16x32_bf16 v[132:135],a[196:199],v[88:91],v[132:135]
v_exp_f32 v102,v102
v_mfma_f32_16x16x32_bf16 v[140:143],a[200:203],v[80:83],v[140:143]
buffer_load_dwordx4 v4,s[20:23],s74 offen offset:1024 nt lds
ds_read_b128 a[144:147],v1 offset:4096
v_mfma_f32_16x16x32_bf16 v[140:143],a[204:207],v[88:91],v[140:143]
v_exp_f32 v103,v103
v_mfma_f32_16x16x32_bf16 v[148:151],a[208:211],v[80:83],v[148:151]
ds_read_b128 a[160:163],v1 offset:8192
v_cvt_pk_bf16_f32 v99,v102,v103
v_mfma_f32_16x16x32_bf16 v[148:151],a[212:215],v[88:91],v[148:151]
v_exp_f32 v104,v104
v_mfma_f32_16x16x32_bf16 v[156:159],a[216:219],v[80:83],v[156:159]
ds_read_b128 a[164:167],v1 offset:8448
v_cvt_pk_bf16_f32 v17,v18,v19
v_mfma_f32_16x16x32_bf16 v[156:159],a[220:223],v[88:91],v[156:159]
v_exp_f32 v105,v105
v_mfma_f32_16x16x32_bf16 v[164:167],a[224:227],v[80:83],v[164:167]
ds_read_b128 a[168:171],v1 offset:8704
v_cvt_pk_bf16_f32 v104,v104,v105
v_mfma_f32_16x16x32_bf16 v[164:167],a[228:231],v[88:91],v[164:167]
v_exp_f32 v106,v106
v_mfma_f32_16x16x32_bf16 v[172:175],a[232:235],v[80:83],v[172:175]
ds_read_b128 a[172:175],v1 offset:8960
v_cvt_pk_bf16_f32 v18,v20,v21
v_mfma_f32_16x16x32_bf16 v[172:175],a[236:239],v[88:91],v[172:175]
v_exp_f32 v107,v107
v_mfma_f32_16x16x32_bf16 v[180:183],a[240:243],v[80:83],v[180:183]
ds_read_b128 a[188:191],v1 offset:13056
v_cvt_pk_bf16_f32 v105,v106,v107
v_mfma_f32_16x16x32_bf16 v[180:183],a[244:247],v[88:91],v[180:183]
v_exp_f32 v108,v108
v_mfma_f32_16x16x32_bf16 v[188:191],a[248:251],v[80:83],v[188:191]
ds_read_b128 a[184:187],v1 offset:12800
v_cvt_pk_bf16_f32 v19,v22,v23
v_mfma_f32_16x16x32_bf16 v[188:191],a[252:255],v[88:91],v[188:191]
v_exp_f32 v109,v109
v_mfma_f32_16x16x32_bf16 v[52:55],v[28:31],v[80:83],v[52:55]
ds_read_b128 a[180:183],v1 offset:12544
v_cvt_pk_bf16_f32 v106,v108,v109
v_mfma_f32_16x16x32_bf16 v[52:55],v[28:31],v[88:91],v[52:55]
s_waitcnt lgkmcnt(4)
v_exp_f32 v110,v110
v_mfma_f32_16x16x32_bf16 v[64:67],a[128:131],v[128:131],v[32:35]
buffer_load_dwordx4 v5,s[20:23],s74 offen offset:2048 nt lds
ds_read_b128 a[176:179],v1 offset:12288
v_mfma_f32_16x16x32_bf16 v[64:67],a[132:135],v[136:139],v[64:67]
v_exp_f32 v111,v111
v_mfma_f32_16x16x32_bf16 v[64:67],a[136:139],v[144:147],v[64:67]
v_cvt_pk_bf16_f32 v107,v110,v111
v_add_u32 v1,s78,v1
v_mfma_f32_16x16x32_bf16 v[64:67],a[140:143],v[152:155],v[64:67]
v_exp_f32 v112,v112
v_mfma_f32_16x16x32_bf16 v[68:71],a[156:159],v[152:155],v[32:35]
v_exp_f32 v113,v113
v_mfma_f32_16x16x32_bf16 v[68:71],a[152:155],v[144:147],v[68:71]
v_exp_f32 v114,v114
v_mfma_f32_16x16x32_bf16 v[68:71],a[148:151],v[136:139],v[68:71]
v_exp_f32 v115,v115
v_mfma_f32_16x16x32_bf16 v[68:71],a[144:147],v[128:131],v[68:71]
v_exp_f32 v116,v116
v_mfma_f32_16x16x32_bf16 v[72:75],a[160:163],v[128:131],v[32:35]
v_exp_f32 v117,v117
v_mfma_f32_16x16x32_bf16 v[72:75],a[164:167],v[136:139],v[72:75]
v_exp_f32 v118,v118
v_mfma_f32_16x16x32_bf16 v[72:75],a[168:171],v[144:147],v[72:75]
s_waitcnt lgkmcnt(0)
v_exp_f32 v119,v119
v_mfma_f32_16x16x32_bf16 v[72:75],a[172:175],v[152:155],v[72:75]
v_cvt_pk_bf16_f32 v112,v112,v113
v_cvt_pk_bf16_f32 v113,v114,v115
v_mfma_f32_16x16x32_bf16 v[76:79],a[188:191],v[152:155],v[32:35]
v_cvt_pk_bf16_f32 v114,v116,v117
v_cvt_pk_bf16_f32 v115,v118,v119
v_mfma_f32_16x16x32_bf16 v[76:79],a[184:187],v[144:147],v[76:79]
v_exp_f32 v120,v120
v_mfma_f32_16x16x32_bf16 v[76:79],a[180:183],v[136:139],v[76:79]
v_exp_f32 v121,v121
v_mfma_f32_16x16x32_bf16 v[76:79],a[176:179],v[128:131],v[76:79]
v_exp_f32 v122,v122
v_mfma_f32_16x16x32_bf16 v[196:199],a[192:195],v[96:99],v[196:199]
buffer_load_dwordx4 v5,s[20:23],s74 offen offset:3072 lds
v_cvt_pk_bf16_f32 v120,v120,v121
v_mfma_f32_16x16x32_bf16 v[196:199],a[196:199],v[104:107],v[196:199]
v_exp_f32 v123,v123
v_mfma_f32_16x16x32_bf16 v[204:207],a[200:203],v[96:99],v[204:207]
v_exp_f32 v124,v124
v_mfma_f32_16x16x32_bf16 v[204:207],a[204:207],v[104:107],v[204:207]
v_exp_f32 v125,v125
v_mfma_f32_16x16x32_bf16 v[212:215],a[208:211],v[96:99],v[212:215]
v_exp_f32 v126,v126
v_mfma_f32_16x16x32_bf16 v[212:215],a[212:215],v[104:107],v[212:215]
v_exp_f32 v127,v127
v_mfma_f32_16x16x32_bf16 v[220:223],a[216:219],v[96:99],v[220:223]
v_cvt_pk_bf16_f32 v121,v122,v123
v_cvt_pk_bf16_f32 v122,v124,v125
v_mfma_f32_16x16x32_bf16 v[220:223],a[220:223],v[104:107],v[220:223]
v_cvt_pk_bf16_f32 v123,v126,v127
v_accvgpr_write_b32 a112,v16
v_mfma_f32_16x16x32_bf16 v[228:231],a[224:227],v[96:99],v[228:231]
v_exp_f32 v64,v64
v_mfma_f32_16x16x32_bf16 v[228:231],a[228:231],v[104:107],v[228:231]
v_exp_f32 v65,v65
v_mfma_f32_16x16x32_bf16 v[236:239],a[232:235],v[96:99],v[236:239]
v_exp_f32 v66,v66
v_mfma_f32_16x16x32_bf16 v[236:239],a[236:239],v[104:107],v[236:239]
v_exp_f32 v67,v67
v_mfma_f32_16x16x32_bf16 v[244:247],a[240:243],v[96:99],v[244:247]
v_exp_f32 v68,v68
v_mfma_f32_16x16x32_bf16 v[244:247],a[244:247],v[104:107],v[244:247]
v_exp_f32 v69,v69
v_mfma_f32_16x16x32_bf16 v[252:255],a[248:251],v[96:99],v[252:255]
s_add_u32 m0,s76,16384
v_exp_f32 v70,v70
v_mfma_f32_16x16x32_bf16 v[252:255],a[252:255],v[104:107],v[252:255]
s_sub_u32 s76,s76,32768
v_exp_f32 v71,v71
v_mfma_f32_16x16x32_bf16 v[56:59],v[28:31],v[96:99],v[56:59]
s_and_b32 s76,s76,0x1ffff
buffer_load_dwordx4 v4,s[24:27],s74 offen lds
v_cvt_pk_bf16_f32 v64,v64,v65
v_mfma_f32_16x16x32_bf16 v[56:59],v[28:31],v[104:107],v[56:59]
v_cvt_pk_bf16_f32 v65,v66,v67
v_cvt_pk_bf16_f32 v66,v68,v69
v_mfma_f32_16x16x32_bf16 v[80:83],a[128:131],v[160:163],v[36:39]
v_cvt_pk_bf16_f32 v67,v70,v71
v_accvgpr_write_b32 a113,v17
v_mfma_f32_16x16x32_bf16 v[80:83],a[132:135],v[168:171],v[80:83]
v_exp_f32 v72,v72
v_mfma_f32_16x16x32_bf16 v[80:83],a[136:139],v[176:179],v[80:83]
v_exp_f32 v73,v73
v_mfma_f32_16x16x32_bf16 v[80:83],a[140:143],v[184:187],v[80:83]
v_exp_f32 v74,v74
v_mfma_f32_16x16x32_bf16 v[84:87],a[156:159],v[184:187],v[36:39]
v_exp_f32 v75,v75
v_mfma_f32_16x16x32_bf16 v[84:87],a[152:155],v[176:179],v[84:87]
v_exp_f32 v76,v76
v_mfma_f32_16x16x32_bf16 v[84:87],a[148:151],v[168:171],v[84:87]
v_exp_f32 v77,v77
v_mfma_f32_16x16x32_bf16 v[84:87],a[144:147],v[160:163],v[84:87]
v_exp_f32 v78,v78
v_mfma_f32_16x16x32_bf16 v[88:91],a[160:163],v[160:163],v[36:39]
v_exp_f32 v79,v79
v_mfma_f32_16x16x32_bf16 v[88:91],a[164:167],v[168:171],v[88:91]
v_cvt_pk_bf16_f32 v72,v72,v73
v_cvt_pk_bf16_f32 v73,v74,v75
v_mfma_f32_16x16x32_bf16 v[88:91],a[168:171],v[176:179],v[88:91]
v_cvt_pk_bf16_f32 v74,v76,v77
v_cvt_pk_bf16_f32 v75,v78,v79
v_mfma_f32_16x16x32_bf16 v[88:91],a[172:175],v[184:187],v[88:91]
v_exp_f32 v80,v80
v_mfma_f32_16x16x32_bf16 v[92:95],a[188:191],v[184:187],v[36:39]
v_exp_f32 v81,v81
v_mfma_f32_16x16x32_bf16 v[92:95],a[184:187],v[176:179],v[92:95]
v_exp_f32 v82,v82
s_nop 0
L_pd9_%=:
s_cbranch_execz L_pd13_%=
v_mfma_f32_16x16x32_bf16 v[92:95],a[180:183],v[168:171],v[92:95]
buffer_load_dwordx4 v5,s[24:27],s74 offen offset:1024 lds
v_cvt_pk_bf16_f32 v80,v80,v81
v_mfma_f32_16x16x32_bf16 v[92:95],a[176:179],v[160:163],v[92:95]
v_exp_f32 v83,v83
v_mfma_f32_16x16x32_bf16 a[32:35],a[192:195],v[112:115],a[32:35]
ds_read_b64_tr_b16 a[192:193],v2
ds_read_b64_tr_b16 a[194:195],v2 offset:4096
v_mfma_f32_16x16x32_bf16 a[32:35],a[196:199],v[120:123],a[32:35]
ds_read_b64_tr_b16 a[196:197],v2 offset:8192
v_cvt_pk_bf16_f32 v81,v82,v83
v_mfma_f32_16x16x32_bf16 a[36:39],a[200:203],v[112:115],a[36:39]
ds_read_b64_tr_b16 a[198:199],v2 offset:12288
v_accvgpr_write_b32 a114,v18
v_mfma_f32_16x16x32_bf16 a[36:39],a[204:207],v[120:123],a[36:39]
ds_read_b64_tr_b16 a[200:201],v3
v_accvgpr_write_b32 a115,v19
v_mfma_f32_16x16x32_bf16 a[40:43],a[208:211],v[112:115],a[40:43]
ds_read_b64_tr_b16 a[202:203],v3 offset:4096
ds_read_b64_tr_b16 a[204:205],v3 offset:8192
v_mfma_f32_16x16x32_bf16 a[40:43],a[212:215],v[120:123],a[40:43]
)A"
R"A(ds_read_b64_tr_b16 a[206:207],v3 offset:12288
v_accvgpr_write_b32 a116,v8
v_mfma_f32_16x16x32_bf16 a[44:47],a[216:219],v[112:115],a[44:47]
ds_read_b64_tr_b16 a[208:209],v2 offset:256
v_accvgpr_write_b32 a117,v9
v_mfma_f32_16x16x32_bf16 a[44:47],a[220:223],v[120:123],a[44:47]
ds_read_b64_tr_b16 a[210:211],v2 offset:4352
v_accvgpr_write_b32 a118,v10
v_mfma_f32_16x16x32_bf16 a[48:51],a[224:227],v[112:115],a[48:51]
ds_read_b64_tr_b16 a[212:213],v2 offset:8448
ds_read_b64_tr_b16 a[214:215],v2 offset:12544
v_mfma_f32_16x16x32_bf16 a[48:51],a[228:231],v[120:123],a[48:51]
ds_read_b64_tr_b16 a[216:217],v3 offset:256
v_accvgpr_write_b32 a119,v11
v_mfma_f32_16x16x32_bf16 a[52:55],a[232:235],v[112:115],a[52:55]
ds_read_b64_tr_b16 a[218:219],v3 offset:4352
v_accvgpr_read_b32 v16,a120
v_mfma_f32_16x16x32_bf16 a[52:55],a[236:239],v[120:123],a[52:55]
ds_read_b64_tr_b16 a[220:221],v3 offset:8448
v_accvgpr_read_b32 v17,a121
v_mfma_f32_16x16x32_bf16 a[56:59],a[240:243],v[112:115],a[56:59]
ds_read_b64_tr_b16 a[222:223],v3 offset:12544
ds_read_b64_tr_b16 a[224:225],v2 offset:512
v_mfma_f32_16x16x32_bf16 a[56:59],a[244:247],v[120:123],a[56:59]
ds_read_b64_tr_b16 a[226:227],v2 offset:4608
v_accvgpr_read_b32 v18,a122
v_mfma_f32_16x16x32_bf16 a[60:63],a[248:251],v[112:115],a[60:63]
buffer_load_dwordx4 v4,s[24:27],s74 offen offset:2048 lds
ds_read_b64_tr_b16 a[228:229],v2 offset:8704
v_mfma_f32_16x16x32_bf16 a[60:63],a[252:255],v[120:123],a[60:63]
ds_read_b64_tr_b16 a[230:231],v2 offset:12800
v_accvgpr_read_b32 v19,a123
v_mfma_f32_16x16x32_bf16 v[60:63],v[28:31],v[112:115],v[60:63]
ds_read_b64_tr_b16 a[232:233],v3 offset:512
ds_read_b64_tr_b16 a[234:235],v3 offset:4608
v_mfma_f32_16x16x32_bf16 v[60:63],v[28:31],v[120:123],v[60:63]
ds_read_b64_tr_b16 a[236:237],v3 offset:8704
v_accvgpr_read_b32 v20,a124
v_mfma_f32_16x16x32_bf16 v[96:99],a[128:131],v[192:195],v[40:43]
ds_read_b64_tr_b16 a[238:239],v3 offset:12800
v_accvgpr_read_b32 v21,a125
v_mfma_f32_16x16x32_bf16 v[96:99],a[132:135],v[200:203],v[96:99]
ds_read_b64_tr_b16 a[240:241],v2 offset:768
v_accvgpr_read_b32 v22,a126
v_mfma_f32_16x16x32_bf16 v[96:99],a[136:139],v[208:211],v[96:99]
ds_read_b64_tr_b16 a[242:243],v2 offset:4864
ds_read_b64_tr_b16 a[244:245],v2 offset:8960
v_mfma_f32_16x16x32_bf16 v[96:99],a[140:143],v[216:219],v[96:99]
ds_read_b64_tr_b16 a[246:247],v2 offset:13056
v_add_u32 v2,s78,v2
v_mfma_f32_16x16x32_bf16 v[100:103],a[156:159],v[216:219],v[40:43]
ds_read_b64_tr_b16 a[248:249],v3 offset:768
v_accvgpr_read_b32 v23,a127
v_mfma_f32_16x16x32_bf16 v[100:103],a[152:155],v[208:211],v[100:103]
ds_read_b64_tr_b16 a[250:251],v3 offset:4864
v_mul_f32 v16,v16,v27
v_mfma_f32_16x16x32_bf16 v[100:103],a[148:151],v[200:203],v[100:103]
ds_read_b64_tr_b16 a[252:253],v3 offset:8960
ds_read_b64_tr_b16 a[254:255],v3 offset:13056
v_mfma_f32_16x16x32_bf16 v[100:103],a[144:147],v[192:195],v[100:103]
v_exp_f32 v84,v84
v_mfma_f32_16x16x32_bf16 v[104:107],a[160:163],v[192:195],v[40:43]
v_exp_f32 v85,v85
v_mfma_f32_16x16x32_bf16 v[104:107],a[164:167],v[200:203],v[104:107]
v_exp_f32 v86,v86
v_mfma_f32_16x16x32_bf16 v[104:107],a[168:171],v[208:211],v[104:107]
v_exp_f32 v87,v87
v_mfma_f32_16x16x32_bf16 v[104:107],a[172:175],v[216:219],v[104:107]
v_cvt_pk_bf16_f32 v82,v84,v85
v_cvt_pk_bf16_f32 v83,v86,v87
v_mfma_f32_16x16x32_bf16 v[108:111],a[188:191],v[216:219],v[40:43]
buffer_load_dwordx4 v5,s[24:27],s74 offen offset:3072 lds
v_add_u32 v3,s78,v3
v_mfma_f32_16x16x32_bf16 v[108:111],a[184:187],v[208:211],v[108:111]
s_add_u32 s74,s74,16384
s_and_b32 s74,s74,0x3ffff
s_or_b32 s74,s74,s69
v_mfma_f32_16x16x32_bf16 v[108:111],a[180:183],v[200:203],v[108:111]
s_and_b32 s69,s75,0xfffc0000
v_exp_f32 v88,v88
v_mfma_f32_16x16x32_bf16 v[108:111],a[176:179],v[192:195],v[108:111]
s_sub_u32 s78,s77,32768
v_exp_f32 v89,v89
s_waitcnt lgkmcnt(0)
v_mfma_f32_16x16x32_bf16 a[0:3],a[192:195],v[64:67],a[0:3]
s_and_b32 s78,s78,0x1ffff
v_exp_f32 v90,v90
v_mfma_f32_16x16x32_bf16 a[0:3],a[196:199],v[72:75],a[0:3]
s_sub_u32 s78,s78,s77
v_exp_f32 v91,v91
v_mfma_f32_16x16x32_bf16 a[4:7],a[200:203],v[64:67],a[4:7]
s_add_u32 s75,s75,16384
v_exp_f32 v92,v92
v_mfma_f32_16x16x32_bf16 a[4:7],a[204:207],v[72:75],a[4:7]
s_add_u32 s77,s77,s78
v_exp_f32 v93,v93
v_mfma_f32_16x16x32_bf16 a[8:11],a[208:211],v[64:67],a[8:11]
v_exp_f32 v94,v94
v_mfma_f32_16x16x32_bf16 a[8:11],a[212:215],v[72:75],a[8:11]
v_exp_f32 v95,v95
v_mfma_f32_16x16x32_bf16 a[12:15],a[216:219],v[64:67],a[12:15]
v_cvt_pk_bf16_f32 v88,v88,v89
v_cvt_pk_bf16_f32 v89,v90,v91
v_mfma_f32_16x16x32_bf16 a[12:15],a[220:223],v[72:75],a[12:15]
v_cvt_pk_bf16_f32 v90,v92,v93
v_cvt_pk_bf16_f32 v91,v94,v95
v_mfma_f32_16x16x32_bf16 a[16:19],a[224:227],v[64:67],a[16:19]
v_exp_f32 v96,v96
v_mfma_f32_16x16x32_bf16 a[16:19],a[228:231],v[72:75],a[16:19]
v_exp_f32 v97,v97
v_mfma_f32_16x16x32_bf16 a[20:23],a[232:235],v[64:67],a[20:23]
s_getreg_b32 s92,hwreg(HW_REG_IB_STS,0,4)
s_min_u32 s4,s92,s6
s_mov_b32 s6,s92
v_mfma_f32_16x16x32_bf16 a[20:23],a[236:239],v[72:75],a[20:23]
s_cmp_ge_u32 s4,8
s_cselect_b32 s4,14,s5
v_cvt_pk_bf16_f32 v96,v96,v97
v_mfma_f32_16x16x32_bf16 a[24:27],a[240:243],v[64:67],a[24:27]
v_exp_f32 v98,v98
v_mfma_f32_16x16x32_bf16 a[24:27],a[244:247],v[72:75],a[24:27]
v_exp_f32 v99,v99
v_mfma_f32_16x16x32_bf16 a[28:31],a[248:251],v[64:67],a[28:31]
v_exp_f32 v100,v100
v_mfma_f32_16x16x32_bf16 a[28:31],a[252:255],v[72:75],a[28:31]
s_getpc_b64 s[92:93]
s_cmp_lg_u32 s4,0
s_cbranch_scc1 L_pace_%=
v_mfma_f32_16x16x32_bf16 v[48:51],v[28:31],v[64:67],v[48:51]
v_exp_f32 v101,v101
v_mfma_f32_16x16x32_bf16 v[48:51],v[28:31],v[72:75],v[48:51]
v_exp_f32 v102,v102
s_sleep 1
s_waitcnt vmcnt(16)
s_barrier
v_mfma_f32_16x16x32_bf16 v[112:115],a[128:131],v[224:227],v[44:47]
s_add_u32 m0,s76,0
ds_read_b128 a[128:131],v1
v_cvt_pk_bf16_f32 v97,v98,v99
v_mfma_f32_16x16x32_bf16 v[112:115],a[132:135],v[232:235],v[112:115]
v_exp_f32 v103,v103
v_mfma_f32_16x16x32_bf16 v[112:115],a[136:139],v[240:243],v[112:115]
buffer_load_dwordx4 v4,s[20:23],s74 offen nt lds
ds_read_b128 a[132:135],v1 offset:256
v_mfma_f32_16x16x32_bf16 v[112:115],a[140:143],v[248:251],v[112:115]
v_cvt_pk_bf16_f32 v98,v100,v101
v_cvt_pk_bf16_f32 v99,v102,v103
v_mfma_f32_16x16x32_bf16 v[116:119],a[156:159],v[248:251],v[44:47]
ds_read_b128 a[136:139],v1 offset:512
v_mul_f32 v17,v17,v27
v_mfma_f32_16x16x32_bf16 v[116:119],a[152:155],v[240:243],v[116:119]
v_exp_f32 v104,v104
v_mfma_f32_16x16x32_bf16 v[116:119],a[148:151],v[232:235],v[116:119]
ds_read_b128 a[140:143],v1 offset:768
v_mul_f32 v18,v18,v27
v_mfma_f32_16x16x32_bf16 v[116:119],a[144:147],v[224:227],v[116:119]
v_exp_f32 v105,v105
v_mfma_f32_16x16x32_bf16 v[120:123],a[160:163],v[224:227],v[44:47]
ds_read_b128 a[156:159],v1 offset:4864
v_cvt_pk_bf16_f32 v104,v104,v105
v_mfma_f32_16x16x32_bf16 v[120:123],a[164:167],v[232:235],v[120:123]
v_exp_f32 v106,v106
v_mfma_f32_16x16x32_bf16 v[120:123],a[168:171],v[240:243],v[120:123]
ds_read_b128 a[152:155],v1 offset:4608
v_mul_f32 v19,v19,v27
v_mfma_f32_16x16x32_bf16 v[120:123],a[172:175],v[248:251],v[120:123]
v_exp_f32 v107,v107
v_mfma_f32_16x16x32_bf16 v[124:127],a[188:191],v[248:251],v[44:47]
ds_read_b128 a[148:151],v1 offset:4352
v_cvt_pk_bf16_f32 v105,v106,v107
v_mfma_f32_16x16x32_bf16 v[124:127],a[184:187],v[240:243],v[124:127]
v_exp_f32 v108,v108
v_mfma_f32_16x16x32_bf16 v[124:127],a[180:183],v[232:235],v[124:127]
ds_read_b128 a[144:147],v1 offset:4096
v_mul_f32 v20,v20,v27
v_mfma_f32_16x16x32_bf16 v[124:127],a[176:179],v[224:227],v[124:127]
v_exp_f32 v109,v109
v_mfma_f32_16x16x32_bf16 v[132:135],a[192:195],v[80:83],v[132:135]
ds_read_b128 a[160:163],v1 offset:8192
v_cvt_pk_bf16_f32 v106,v108,v109
v_mfma_f32_16x16x32_bf16 v[132:135],a[196:199],v[88:91],v[132:135]
v_exp_f32 v110,v110
v_mfma_f32_16x16x32_bf16 v[140:143],a[200:203],v[80:83],v[140:143]
buffer_load_dwordx4 v4,s[20:23],s74 offen offset:1024 nt lds
ds_read_b128 a[164:167],v1 offset:8448
v_mfma_f32_16x16x32_bf16 v[140:143],a[204:207],v[88:91],v[140:143]
v_exp_f32 v111,v111
v_mfma_f32_16x16x32_bf16 v[148:151],a[208:211],v[80:83],v[148:151]
ds_read_b128 a[168:171],v1 offset:8704
v_cvt_pk_bf16_f32 v107,v110,v111
v_mfma_f32_16x16x32_bf16 v[148:151],a[212:215],v[88:91],v[148:151]
v_exp_f32 v112,v112
v_mfma_f32_16x16x32_bf16 v[156:159],a[216:219],v[80:83],v[156:159]
ds_read_b128 a[172:175],v1 offset:8960
v_mul_f32 v21,v21,v27
v_mfma_f32_16x16x32_bf16 v[156:159],a[220:223],v[88:91],v[156:159]
v_exp_f32 v113,v113
v_mfma_f32_16x16x32_bf16 v[164:167],a[224:227],v[80:83],v[164:167]
ds_read_b128 a[188:191],v1 offset:13056
v_cvt_pk_bf16_f32 v112,v112,v113
v_mfma_f32_16x16x32_bf16 v[164:167],a[228:231],v[88:91],v[164:167]
v_exp_f32 v114,v114
v_mfma_f32_16x16x32_bf16 v[172:175],a[232:235],v[80:83],v[172:175]
ds_read_b128 a[184:187],v1 offset:12800
v_mul_f32 v22,v22,v27
v_mfma_f32_16x16x32_bf16 v[172:175],a[236:239],v[88:91],v[172:175]
v_exp_f32 v115,v115
v_mfma_f32_16x16x32_bf16 v[180:183],a[240:243],v[80:83],v[180:183]
ds_read_b128 a[180:183],v1 offset:12544
v_cvt_pk_bf16_f32 v113,v114,v115
v_mfma_f32_16x16x32_bf16 v[180:183],a[244:247],v[88:91],v[180:183]
v_exp_f32 v116,v116
v_mfma_f32_16x16x32_bf16 v[188:191],a[248:251],v[80:83],v[188:191]
ds_read_b128 a[176:179],v1 offset:12288
v_add_u32 v1,s78,v1
v_mfma_f32_16x16x32_bf16 v[188:191],a[252:255],v[88:91],v[188:191]
v_exp_f32 v117,v117
v_mfma_f32_16x16x32_bf16 v[52:55],v[28:31],v[80:83],v[52:55]
v_exp_f32 v118,v118
v_mfma_f32_16x16x32_bf16 v[52:55],v[28:31],v[88:91],v[52:55]
s_waitcnt lgkmcnt(3)
v_exp_f32 v119,v119
v_mfma_f32_16x16x32_bf16 v[64:67],a[128:131],v[128:131],v[32:35]
buffer_load_dwordx4 v5,s[20:23],s74 offen offset:2048 nt lds
v_cvt_pk_bf16_f32 v114,v116,v117
v_mfma_f32_16x16x32_bf16 v[64:67],a[132:135],v[136:139],v[64:67]
v_cvt_pk_bf16_f32 v115,v118,v119
v_mul_f32 v23,v23,v27
v_mfma_f32_16x16x32_bf16 v[64:67],a[136:139],v[144:147],v[64:67]
v_exp_f32 v120,v120
v_mfma_f32_16x16x32_bf16 v[64:67],a[140:143],v[152:155],v[64:67]
v_exp_f32 v121,v121
v_mfma_f32_16x16x32_bf16 v[68:71],a[156:159],v[152:155],v[32:35]
v_exp_f32 v122,v122
v_mfma_f32_16x16x32_bf16 v[68:71],a[152:155],v[144:147],v[68:71]
v_exp_f32 v123,v123
v_mfma_f32_16x16x32_bf16 v[68:71],a[148:151],v[136:139],v[68:71]
v_exp_f32 v124,v124
v_mfma_f32_16x16x32_bf16 v[68:71],a[144:147],v[128:131],v[68:71]
v_exp_f32 v125,v125
v_mfma_f32_16x16x32_bf16 v[72:75],a[160:163],v[128:131],v[32:35]
v_exp_f32 v126,v126
v_mfma_f32_16x16x32_bf16 v[72:75],a[164:167],v[136:139],v[72:75]
v_exp_f32 v127,v127
v_mfma_f32_16x16x32_bf16 v[72:75],a[168:171],v[144:147],v[72:75]
v_cvt_pk_bf16_f32 v120,v120,v121
v_cvt_pk_bf16_f32 v121,v122,v123
v_mfma_f32_16x16x32_bf16 v[72:75],a[172:175],v[152:155],v[72:75]
v_cvt_pk_bf16_f32 v122,v124,v125
v_cvt_pk_bf16_f32 v123,v126,v127
v_mfma_f32_16x16x32_bf16 v[76:79],a[188:191],v[152:155],v[32:35]
s_waitcnt lgkmcnt(0)
v_exp_f32 v64,v64
v_mfma_f32_16x16x32_bf16 v[76:79],a[184:187],v[144:147],v[76:79]
v_exp_f32 v65,v65
v_mfma_f32_16x16x32_bf16 v[76:79],a[180:183],v[136:139],v[76:79]
v_exp_f32 v66,v66
v_mfma_f32_16x16x32_bf16 v[76:79],a[176:179],v[128:131],v[76:79]
v_exp_f32 v67,v67
v_mfma_f32_16x16x32_bf16 v[196:199],a[192:195],v[96:99],v[196:199]
buffer_load_dwordx4 v5,s[20:23],s74 offen offset:3072 lds
v_cvt_pk_bf16_f32 v64,v64,v65
v_mfma_f32_16x16x32_bf16 v[196:199],a[196:199],v[104:107],v[196:199]
v_exp_f32 v68,v68
v_mfma_f32_16x16x32_bf16 v[204:207],a[200:203],v[96:99],v[204:207]
v_exp_f32 v69,v69
v_mfma_f32_16x16x32_bf16 v[204:207],a[204:207],v[104:107],v[204:207]
v_exp_f32 v70,v70
v_mfma_f32_16x16x32_bf16 v[212:215],a[208:211],v[96:99],v[212:215]
v_exp_f32 v71,v71
v_mfma_f32_16x16x32_bf16 v[212:215],a[212:215],v[104:107],v[212:215]
v_cvt_pk_bf16_f32 v65,v66,v67
v_cvt_pk_bf16_f32 v66,v68,v69
v_mfma_f32_16x16x32_bf16 v[220:223],a[216:219],v[96:99],v[220:223]
v_cvt_pk_bf16_f32 v67,v70,v71
v_cvt_pk_bf16_f32 v16,v16,v17
v_mfma_f32_16x16x32_bf16 v[220:223],a[220:223],v[104:107],v[220:223]
v_exp_f32 v72,v72
v_mfma_f32_16x16x32_bf16 v[228:231],a[224:227],v[96:99],v[228:231]
v_exp_f32 v73,v73
v_mfma_f32_16x16x32_bf16 v[228:231],a[228:231],v[104:107],v[228:231]
)A"
R"A(v_exp_f32 v74,v74
v_mfma_f32_16x16x32_bf16 v[236:239],a[232:235],v[96:99],v[236:239]
v_exp_f32 v75,v75
v_mfma_f32_16x16x32_bf16 v[236:239],a[236:239],v[104:107],v[236:239]
v_exp_f32 v76,v76
v_mfma_f32_16x16x32_bf16 v[244:247],a[240:243],v[96:99],v[244:247]
v_exp_f32 v77,v77
v_mfma_f32_16x16x32_bf16 v[244:247],a[244:247],v[104:107],v[244:247]
v_exp_f32 v78,v78
v_mfma_f32_16x16x32_bf16 v[252:255],a[248:251],v[96:99],v[252:255]
s_add_u32 m0,s76,16384
v_exp_f32 v79,v79
v_mfma_f32_16x16x32_bf16 v[252:255],a[252:255],v[104:107],v[252:255]
s_sub_u32 s76,s76,32768
v_cvt_pk_bf16_f32 v72,v72,v73
v_cvt_pk_bf16_f32 v73,v74,v75
v_mfma_f32_16x16x32_bf16 v[56:59],v[28:31],v[96:99],v[56:59]
s_and_b32 s76,s76,0x1ffff
buffer_load_dwordx4 v4,s[24:27],s74 offen lds
v_cvt_pk_bf16_f32 v74,v76,v77
v_mfma_f32_16x16x32_bf16 v[56:59],v[28:31],v[104:107],v[56:59]
v_cvt_pk_bf16_f32 v75,v78,v79
v_cvt_pk_bf16_f32 v17,v18,v19
v_mfma_f32_16x16x32_bf16 v[80:83],a[128:131],v[160:163],v[36:39]
v_cvt_pk_bf16_f32 v18,v20,v21
v_cvt_pk_bf16_f32 v19,v22,v23
v_mfma_f32_16x16x32_bf16 v[80:83],a[132:135],v[168:171],v[80:83]
v_accvgpr_write_b32 a120,v16
v_accvgpr_write_b32 a121,v17
v_mfma_f32_16x16x32_bf16 v[80:83],a[136:139],v[176:179],v[80:83]
v_accvgpr_write_b32 a122,v18
v_accvgpr_write_b32 a123,v19
v_mfma_f32_16x16x32_bf16 v[80:83],a[140:143],v[184:187],v[80:83]
v_accvgpr_write_b32 a124,v12
v_accvgpr_write_b32 a125,v13
v_mfma_f32_16x16x32_bf16 v[84:87],a[156:159],v[184:187],v[36:39]
v_accvgpr_write_b32 a126,v14
v_accvgpr_write_b32 a127,v15
v_mfma_f32_16x16x32_bf16 v[84:87],a[152:155],v[176:179],v[84:87]
v_accvgpr_read_b32 v8,a96
v_accvgpr_read_b32 v9,a97
v_mfma_f32_16x16x32_bf16 v[84:87],a[148:151],v[168:171],v[84:87]
v_exp_f32 v80,v80
v_mfma_f32_16x16x32_bf16 v[84:87],a[144:147],v[160:163],v[84:87]
v_exp_f32 v81,v81
v_mfma_f32_16x16x32_bf16 v[88:91],a[160:163],v[160:163],v[36:39]
v_exp_f32 v82,v82
v_mfma_f32_16x16x32_bf16 v[88:91],a[164:167],v[168:171],v[88:91]
v_exp_f32 v83,v83
v_mfma_f32_16x16x32_bf16 v[88:91],a[168:171],v[176:179],v[88:91]
v_cvt_pk_bf16_f32 v80,v80,v81
v_cvt_pk_bf16_f32 v81,v82,v83
v_mfma_f32_16x16x32_bf16 v[88:91],a[172:175],v[184:187],v[88:91]
v_exp_f32 v84,v84
v_mfma_f32_16x16x32_bf16 v[92:95],a[188:191],v[184:187],v[36:39]
v_exp_f32 v85,v85
v_mfma_f32_16x16x32_bf16 v[92:95],a[184:187],v[176:179],v[92:95]
v_exp_f32 v86,v86
v_mfma_f32_16x16x32_bf16 v[92:95],a[180:183],v[168:171],v[92:95]
buffer_load_dwordx4 v5,s[24:27],s74 offen offset:1024 lds
v_cvt_pk_bf16_f32 v82,v84,v85
v_mfma_f32_16x16x32_bf16 v[92:95],a[176:179],v[160:163],v[92:95]
v_exp_f32 v87,v87
v_mfma_f32_16x16x32_bf16 a[32:35],a[192:195],v[112:115],a[32:35]
ds_read_b64_tr_b16 a[192:193],v2
ds_read_b64_tr_b16 a[194:195],v2 offset:4096
v_mfma_f32_16x16x32_bf16 a[32:35],a[196:199],v[120:123],a[32:35]
ds_read_b64_tr_b16 a[196:197],v2 offset:8192
v_cvt_pk_bf16_f32 v83,v86,v87
v_mfma_f32_16x16x32_bf16 a[36:39],a[200:203],v[112:115],a[36:39]
ds_read_b64_tr_b16 a[198:199],v2 offset:12288
v_accvgpr_read_b32 v10,a98
v_mfma_f32_16x16x32_bf16 a[36:39],a[204:207],v[120:123],a[36:39]
ds_read_b64_tr_b16 a[200:201],v3
v_accvgpr_read_b32 v11,a99
v_mfma_f32_16x16x32_bf16 a[40:43],a[208:211],v[112:115],a[40:43]
ds_read_b64_tr_b16 a[202:203],v3 offset:4096
ds_read_b64_tr_b16 a[204:205],v3 offset:8192
v_mfma_f32_16x16x32_bf16 a[40:43],a[212:215],v[120:123],a[40:43]
ds_read_b64_tr_b16 a[206:207],v3 offset:12288
v_accvgpr_read_b32 v12,a100
v_mfma_f32_16x16x32_bf16 a[44:47],a[216:219],v[112:115],a[44:47]
ds_read_b64_tr_b16 a[208:209],v2 offset:256
v_accvgpr_read_b32 v13,a101
v_mfma_f32_16x16x32_bf16 a[44:47],a[220:223],v[120:123],a[44:47]
ds_read_b64_tr_b16 a[210:211],v2 offset:4352
v_accvgpr_read_b32 v14,a102
v_mfma_f32_16x16x32_bf16 a[48:51],a[224:227],v[112:115],a[48:51]
ds_read_b64_tr_b16 a[212:213],v2 offset:8448
ds_read_b64_tr_b16 a[214:215],v2 offset:12544
v_mfma_f32_16x16x32_bf16 a[48:51],a[228:231],v[120:123],a[48:51]
ds_read_b64_tr_b16 a[216:217],v3 offset:256
v_accvgpr_read_b32 v15,a103
v_mfma_f32_16x16x32_bf16 a[52:55],a[232:235],v[112:115],a[52:55]
ds_read_b64_tr_b16 a[218:219],v3 offset:4352
v_mul_f32 v8,v8,v27
v_mfma_f32_16x16x32_bf16 a[52:55],a[236:239],v[120:123],a[52:55]
ds_read_b64_tr_b16 a[220:221],v3 offset:8448
v_mul_f32 v9,v9,v27
v_mfma_f32_16x16x32_bf16 a[56:59],a[240:243],v[112:115],a[56:59]
ds_read_b64_tr_b16 a[222:223],v3 offset:12544
ds_read_b64_tr_b16 a[224:225],v2 offset:512
v_mfma_f32_16x16x32_bf16 a[56:59],a[244:247],v[120:123],a[56:59]
ds_read_b64_tr_b16 a[226:227],v2 offset:4608
v_mul_f32 v10,v10,v27
v_mfma_f32_16x16x32_bf16 a[60:63],a[248:251],v[112:115],a[60:63]
buffer_load_dwordx4 v4,s[24:27],s74 offen offset:2048 lds
ds_read_b64_tr_b16 a[228:229],v2 offset:8704
v_mfma_f32_16x16x32_bf16 a[60:63],a[252:255],v[120:123],a[60:63]
ds_read_b64_tr_b16 a[230:231],v2 offset:12800
v_mul_f32 v11,v11,v27
v_mfma_f32_16x16x32_bf16 v[60:63],v[28:31],v[112:115],v[60:63]
ds_read_b64_tr_b16 a[232:233],v3 offset:512
ds_read_b64_tr_b16 a[234:235],v3 offset:4608
v_mfma_f32_16x16x32_bf16 v[60:63],v[28:31],v[120:123],v[60:63]
ds_read_b64_tr_b16 a[236:237],v3 offset:8704
v_mul_f32 v12,v12,v27
v_mfma_f32_16x16x32_bf16 v[96:99],a[128:131],v[192:195],v[40:43]
ds_read_b64_tr_b16 a[238:239],v3 offset:12800
v_mul_f32 v13,v13,v27
v_mfma_f32_16x16x32_bf16 v[96:99],a[132:135],v[200:203],v[96:99]
ds_read_b64_tr_b16 a[240:241],v2 offset:768
v_mul_f32 v14,v14,v27
v_mfma_f32_16x16x32_bf16 v[96:99],a[136:139],v[208:211],v[96:99]
ds_read_b64_tr_b16 a[242:243],v2 offset:4864
ds_read_b64_tr_b16 a[244:245],v2 offset:8960
v_mfma_f32_16x16x32_bf16 v[96:99],a[140:143],v[216:219],v[96:99]
ds_read_b64_tr_b16 a[246:247],v2 offset:13056
v_add_u32 v2,s78,v2
v_mfma_f32_16x16x32_bf16 v[100:103],a[156:159],v[216:219],v[40:43]
ds_read_b64_tr_b16 a[248:249],v3 offset:768
v_mul_f32 v15,v15,v27
v_mfma_f32_16x16x32_bf16 v[100:103],a[152:155],v[208:211],v[100:103]
ds_read_b64_tr_b16 a[250:251],v3 offset:4864
v_cvt_pk_bf16_f32 v8,v8,v9
v_mfma_f32_16x16x32_bf16 v[100:103],a[148:151],v[200:203],v[100:103]
ds_read_b64_tr_b16 a[252:253],v3 offset:8960
ds_read_b64_tr_b16 a[254:255],v3 offset:13056
v_mfma_f32_16x16x32_bf16 v[100:103],a[144:147],v[192:195],v[100:103]
v_exp_f32 v88,v88
v_mfma_f32_16x16x32_bf16 v[104:107],a[160:163],v[192:195],v[40:43]
v_exp_f32 v89,v89
v_mfma_f32_16x16x32_bf16 v[104:107],a[164:167],v[200:203],v[104:107]
v_exp_f32 v90,v90
v_mfma_f32_16x16x32_bf16 v[104:107],a[168:171],v[208:211],v[104:107]
v_exp_f32 v91,v91
v_mfma_f32_16x16x32_bf16 v[104:107],a[172:175],v[216:219],v[104:107]
v_exp_f32 v92,v92
v_mfma_f32_16x16x32_bf16 v[108:111],a[188:191],v[216:219],v[40:43]
buffer_load_dwordx4 v5,s[24:27],s74 offen offset:3072 lds
v_cvt_pk_bf16_f32 v88,v88,v89
v_mfma_f32_16x16x32_bf16 v[108:111],a[184:187],v[208:211],v[108:111]
s_add_u32 s74,s74,16384
s_and_b32 s74,s74,0x3ffff
s_or_b32 s74,s74,s69
v_mfma_f32_16x16x32_bf16 v[108:111],a[180:183],v[200:203],v[108:111]
s_and_b32 s69,s75,0xfffc0000
v_exp_f32 v93,v93
v_mfma_f32_16x16x32_bf16 v[108:111],a[176:179],v[192:195],v[108:111]
s_add_u32 s75,s75,16384
v_exp_f32 v94,v94
s_waitcnt lgkmcnt(0)
v_mfma_f32_16x16x32_bf16 a[0:3],a[192:195],v[64:67],a[0:3]
v_exp_f32 v95,v95
v_mfma_f32_16x16x32_bf16 a[0:3],a[196:199],v[72:75],a[0:3]
v_cvt_pk_bf16_f32 v89,v90,v91
v_cvt_pk_bf16_f32 v90,v92,v93
v_mfma_f32_16x16x32_bf16 a[4:7],a[200:203],v[64:67],a[4:7]
v_cvt_pk_bf16_f32 v91,v94,v95
v_add_u32 v3,s78,v3
v_mfma_f32_16x16x32_bf16 a[4:7],a[204:207],v[72:75],a[4:7]
s_sub_u32 s78,s77,32768
v_exp_f32 v96,v96
v_mfma_f32_16x16x32_bf16 a[8:11],a[208:211],v[64:67],a[8:11]
s_and_b32 s78,s78,0x1ffff
v_exp_f32 v97,v97
v_mfma_f32_16x16x32_bf16 a[8:11],a[212:215],v[72:75],a[8:11]
s_sub_u32 s78,s78,s77
v_exp_f32 v98,v98
v_mfma_f32_16x16x32_bf16 a[12:15],a[216:219],v[64:67],a[12:15]
s_add_u32 s77,s77,s78
v_exp_f32 v99,v99
v_mfma_f32_16x16x32_bf16 a[12:15],a[220:223],v[72:75],a[12:15]
v_exp_f32 v100,v100
v_mfma_f32_16x16x32_bf16 a[16:19],a[224:227],v[64:67],a[16:19]
v_exp_f32 v101,v101
v_mfma_f32_16x16x32_bf16 a[16:19],a[228:231],v[72:75],a[16:19]
v_exp_f32 v102,v102
v_mfma_f32_16x16x32_bf16 a[20:23],a[232:235],v[64:67],a[20:23]
s_getreg_b32 s92,hwreg(HW_REG_IB_STS,0,4)
s_min_u32 s4,s92,s6
s_mov_b32 s6,s92
v_mfma_f32_16x16x32_bf16 a[20:23],a[236:239],v[72:75],a[20:23]
s_cmp_ge_u32 s4,8
s_cselect_b32 s4,14,s5
v_cvt_pk_bf16_f32 v96,v96,v97
v_mfma_f32_16x16x32_bf16 a[24:27],a[240:243],v[64:67],a[24:27]
v_exp_f32 v103,v103
v_mfma_f32_16x16x32_bf16 a[24:27],a[244:247],v[72:75],a[24:27]
v_cvt_pk_bf16_f32 v97,v98,v99
v_cvt_pk_bf16_f32 v98,v100,v101
v_mfma_f32_16x16x32_bf16 a[28:31],a[248:251],v[64:67],a[28:31]
v_cvt_pk_bf16_f32 v99,v102,v103
v_cvt_pk_bf16_f32 v9,v10,v11
v_mfma_f32_16x16x32_bf16 a[28:31],a[252:255],v[72:75],a[28:31]
s_getpc_b64 s[92:93]
s_cmp_lg_u32 s4,0
s_cbranch_scc1 L_pace_%=
v_mfma_f32_16x16x32_bf16 v[48:51],v[28:31],v[64:67],v[48:51]
v_exp_f32 v104,v104
v_mfma_f32_16x16x32_bf16 v[48:51],v[28:31],v[72:75],v[48:51]
v_exp_f32 v105,v105
s_sleep 1
s_waitcnt vmcnt(16)
s_barrier
v_mfma_f32_16x16x32_bf16 v[112:115],a[128:131],v[224:227],v[44:47]
s_add_u32 m0,s76,0
ds_read_b128 a[128:131],v1
v_cvt_pk_bf16_f32 v104,v104,v105
v_mfma_f32_16x16x32_bf16 v[112:115],a[132:135],v[232:235],v[112:115]
v_exp_f32 v106,v106
v_mfma_f32_16x16x32_bf16 v[112:115],a[136:139],v[240:243],v[112:115]
buffer_load_dwordx4 v4,s[20:23],s74 offen nt lds
ds_read_b128 a[132:135],v1 offset:256
v_mfma_f32_16x16x32_bf16 v[112:115],a[140:143],v[248:251],v[112:115]
v_exp_f32 v107,v107
v_mfma_f32_16x16x32_bf16 v[116:119],a[156:159],v[248:251],v[44:47]
ds_read_b128 a[136:139],v1 offset:512
v_cvt_pk_bf16_f32 v105,v106,v107
v_mfma_f32_16x16x32_bf16 v[116:119],a[152:155],v[240:243],v[116:119]
v_exp_f32 v108,v108
v_mfma_f32_16x16x32_bf16 v[116:119],a[148:151],v[232:235],v[116:119]
ds_read_b128 a[140:143],v1 offset:768
v_cvt_pk_bf16_f32 v10,v12,v13
v_mfma_f32_16x16x32_bf16 v[116:119],a[144:147],v[224:227],v[116:119]
v_exp_f32 v109,v109
v_mfma_f32_16x16x32_bf16 v[120:123],a[160:163],v[224:227],v[44:47]
ds_read_b128 a[156:159],v1 offset:4864
v_cvt_pk_bf16_f32 v106,v108,v109
v_mfma_f32_16x16x32_bf16 v[120:123],a[164:167],v[232:235],v[120:123]
v_exp_f32 v110,v110
v_mfma_f32_16x16x32_bf16 v[120:123],a[168:171],v[240:243],v[120:123]
ds_read_b128 a[152:155],v1 offset:4608
v_cvt_pk_bf16_f32 v11,v14,v15
v_mfma_f32_16x16x32_bf16 v[120:123],a[172:175],v[248:251],v[120:123]
v_exp_f32 v111,v111
v_mfma_f32_16x16x32_bf16 v[124:127],a[188:191],v[248:251],v[44:47]
ds_read_b128 a[148:151],v1 offset:4352
v_cvt_pk_bf16_f32 v107,v110,v111
v_mfma_f32_16x16x32_bf16 v[124:127],a[184:187],v[240:243],v[124:127]
v_exp_f32 v112,v112
v_mfma_f32_16x16x32_bf16 v[124:127],a[180:183],v[232:235],v[124:127]
ds_read_b128 a[144:147],v1 offset:4096
v_accvgpr_write_b32 a96,v8
v_mfma_f32_16x16x32_bf16 v[124:127],a[176:179],v[224:227],v[124:127]
v_exp_f32 v113,v113
v_mfma_f32_16x16x32_bf16 v[132:135],a[192:195],v[80:83],v[132:135]
ds_read_b128 a[160:163],v1 offset:8192
v_cvt_pk_bf16_f32 v112,v112,v113
v_mfma_f32_16x16x32_bf16 v[132:135],a[196:199],v[88:91],v[132:135]
v_exp_f32 v114,v114
v_mfma_f32_16x16x32_bf16 v[140:143],a[200:203],v[80:83],v[140:143]
buffer_load_dwordx4 v4,s[20:23],s74 offen offset:1024 nt lds
ds_read_b128 a[164:167],v1 offset:8448
v_mfma_f32_16x16x32_bf16 v[140:143],a[204:207],v[88:91],v[140:143]
v_exp_f32 v115,v115
v_mfma_f32_16x16x32_bf16 v[148:151],a[208:211],v[80:83],v[148:151]
ds_read_b128 a[168:171],v1 offset:8704
v_cvt_pk_bf16_f32 v113,v114,v115
v_mfma_f32_16x16x32_bf16 v[148:151],a[212:215],v[88:91],v[148:151]
v_exp_f32 v116,v116
v_mfma_f32_16x16x32_bf16 v[156:159],a[216:219],v[80:83],v[156:159]
ds_read_b128 a[172:175],v1 offset:8960
v_accvgpr_write_b32 a97,v9
v_mfma_f32_16x16x32_bf16 v[156:159],a[220:223],v[88:91],v[156:159]
v_exp_f32 v117,v117
v_mfma_f32_16x16x32_bf16 v[164:167],a[224:227],v[80:83],v[164:167]
ds_read_b128 a[188:191],v1 offset:13056
v_cvt_pk_bf16_f32 v114,v116,v117
v_mfma_f32_16x16x32_bf16 v[164:167],a[228:231],v[88:91],v[164:167]
v_exp_f32 v118,v118
v_mfma_f32_16x16x32_bf16 v[172:175],a[232:235],v[80:83],v[172:175]
)A"
R"A(ds_read_b128 a[184:187],v1 offset:12800
v_accvgpr_write_b32 a98,v10
v_mfma_f32_16x16x32_bf16 v[172:175],a[236:239],v[88:91],v[172:175]
v_exp_f32 v119,v119
v_mfma_f32_16x16x32_bf16 v[180:183],a[240:243],v[80:83],v[180:183]
s_nop 0
L_pd10_%=:
s_cbranch_execz L_t1b_%=
ds_read_b128 a[180:183],v1 offset:12544
v_cvt_pk_bf16_f32 v115,v118,v119
v_mfma_f32_16x16x32_bf16 v[180:183],a[244:247],v[88:91],v[180:183]
v_exp_f32 v120,v120
v_mfma_f32_16x16x32_bf16 v[188:191],a[248:251],v[80:83],v[188:191]
ds_read_b128 a[176:179],v1 offset:12288
v_add_u32 v1,s78,v1
v_mfma_f32_16x16x32_bf16 v[188:191],a[252:255],v[88:91],v[188:191]
v_exp_f32 v121,v121
v_mfma_f32_16x16x32_bf16 v[52:55],v[28:31],v[80:83],v[52:55]
v_exp_f32 v122,v122
v_mfma_f32_16x16x32_bf16 v[52:55],v[28:31],v[88:91],v[52:55]
s_waitcnt lgkmcnt(3)
v_exp_f32 v123,v123
v_mfma_f32_16x16x32_bf16 v[64:67],a[128:131],v[128:131],v[32:35]
buffer_load_dwordx4 v5,s[20:23],s74 offen offset:2048 nt lds
v_cvt_pk_bf16_f32 v120,v120,v121
v_mfma_f32_16x16x32_bf16 v[64:67],a[132:135],v[136:139],v[64:67]
v_exp_f32 v124,v124
v_mfma_f32_16x16x32_bf16 v[64:67],a[136:139],v[144:147],v[64:67]
v_exp_f32 v125,v125
v_mfma_f32_16x16x32_bf16 v[64:67],a[140:143],v[152:155],v[64:67]
v_exp_f32 v126,v126
v_mfma_f32_16x16x32_bf16 v[68:71],a[156:159],v[152:155],v[32:35]
v_exp_f32 v127,v127
v_mfma_f32_16x16x32_bf16 v[68:71],a[152:155],v[144:147],v[68:71]
v_cvt_pk_bf16_f32 v121,v122,v123
v_cvt_pk_bf16_f32 v122,v124,v125
v_mfma_f32_16x16x32_bf16 v[68:71],a[148:151],v[136:139],v[68:71]
v_cvt_pk_bf16_f32 v123,v126,v127
v_accvgpr_write_b32 a99,v11
v_mfma_f32_16x16x32_bf16 v[68:71],a[144:147],v[128:131],v[68:71]
v_exp_f32 v64,v64
v_mfma_f32_16x16x32_bf16 v[72:75],a[160:163],v[128:131],v[32:35]
v_exp_f32 v65,v65
v_mfma_f32_16x16x32_bf16 v[72:75],a[164:167],v[136:139],v[72:75]
v_exp_f32 v66,v66
v_mfma_f32_16x16x32_bf16 v[72:75],a[168:171],v[144:147],v[72:75]
v_exp_f32 v67,v67
v_mfma_f32_16x16x32_bf16 v[72:75],a[172:175],v[152:155],v[72:75]
v_exp_f32 v68,v68
v_mfma_f32_16x16x32_bf16 v[76:79],a[188:191],v[152:155],v[32:35]
s_waitcnt lgkmcnt(0)
v_exp_f32 v69,v69
v_mfma_f32_16x16x32_bf16 v[76:79],a[184:187],v[144:147],v[76:79]
v_exp_f32 v70,v70
v_mfma_f32_16x16x32_bf16 v[76:79],a[180:183],v[136:139],v[76:79]
v_exp_f32 v71,v71
v_mfma_f32_16x16x32_bf16 v[76:79],a[176:179],v[128:131],v[76:79]
v_cvt_pk_bf16_f32 v64,v64,v65
v_cvt_pk_bf16_f32 v65,v66,v67
v_mfma_f32_16x16x32_bf16 v[196:199],a[192:195],v[96:99],v[196:199]
buffer_load_dwordx4 v5,s[20:23],s74 offen offset:3072 lds
v_cvt_pk_bf16_f32 v66,v68,v69
v_mfma_f32_16x16x32_bf16 v[196:199],a[196:199],v[104:107],v[196:199]
v_cvt_pk_bf16_f32 v67,v70,v71
v_accvgpr_read_b32 v16,a104
v_mfma_f32_16x16x32_bf16 v[204:207],a[200:203],v[96:99],v[204:207]
v_exp_f32 v72,v72
v_mfma_f32_16x16x32_bf16 v[204:207],a[204:207],v[104:107],v[204:207]
v_exp_f32 v73,v73
v_mfma_f32_16x16x32_bf16 v[212:215],a[208:211],v[96:99],v[212:215]
v_exp_f32 v74,v74
v_mfma_f32_16x16x32_bf16 v[212:215],a[212:215],v[104:107],v[212:215]
v_exp_f32 v75,v75
v_mfma_f32_16x16x32_bf16 v[220:223],a[216:219],v[96:99],v[220:223]
v_exp_f32 v76,v76
v_mfma_f32_16x16x32_bf16 v[220:223],a[220:223],v[104:107],v[220:223]
v_exp_f32 v77,v77
v_mfma_f32_16x16x32_bf16 v[228:231],a[224:227],v[96:99],v[228:231]
v_exp_f32 v78,v78
v_mfma_f32_16x16x32_bf16 v[228:231],a[228:231],v[104:107],v[228:231]
v_exp_f32 v79,v79
v_mfma_f32_16x16x32_bf16 v[236:239],a[232:235],v[96:99],v[236:239]
v_cvt_pk_bf16_f32 v72,v72,v73
v_cvt_pk_bf16_f32 v73,v74,v75
v_mfma_f32_16x16x32_bf16 v[236:239],a[236:239],v[104:107],v[236:239]
v_cvt_pk_bf16_f32 v74,v76,v77
v_cvt_pk_bf16_f32 v75,v78,v79
v_mfma_f32_16x16x32_bf16 v[244:247],a[240:243],v[96:99],v[244:247]
v_accvgpr_read_b32 v17,a105
v_accvgpr_read_b32 v18,a106
v_mfma_f32_16x16x32_bf16 v[244:247],a[244:247],v[104:107],v[244:247]
v_accvgpr_read_b32 v19,a107
v_accvgpr_read_b32 v20,a108
v_mfma_f32_16x16x32_bf16 v[252:255],a[248:251],v[96:99],v[252:255]
s_add_u32 m0,s76,16384
s_sub_u32 s76,s76,32768
s_and_b32 s76,s76,0x1ffff
v_mfma_f32_16x16x32_bf16 v[252:255],a[252:255],v[104:107],v[252:255]
v_accvgpr_read_b32 v21,a109
v_accvgpr_read_b32 v22,a110
v_mfma_f32_16x16x32_bf16 v[56:59],v[28:31],v[96:99],v[56:59]
buffer_load_dwordx4 v4,s[24:27],s74 offen lds
v_accvgpr_read_b32 v23,a111
v_mfma_f32_16x16x32_bf16 v[56:59],v[28:31],v[104:107],v[56:59]
v_mul_f32 v16,v16,v27
v_mul_f32 v17,v17,v27
v_mfma_f32_16x16x32_bf16 v[80:83],a[128:131],v[160:163],v[36:39]
v_mul_f32 v18,v18,v27
v_mul_f32 v19,v19,v27
v_mfma_f32_16x16x32_bf16 v[80:83],a[132:135],v[168:171],v[80:83]
v_mul_f32 v20,v20,v27
v_mul_f32 v21,v21,v27
v_mfma_f32_16x16x32_bf16 v[80:83],a[136:139],v[176:179],v[80:83]
v_mul_f32 v22,v22,v27
v_mul_f32 v23,v23,v27
v_mfma_f32_16x16x32_bf16 v[80:83],a[140:143],v[184:187],v[80:83]
v_cvt_pk_bf16_f32 v16,v16,v17
v_cvt_pk_bf16_f32 v17,v18,v19
v_mfma_f32_16x16x32_bf16 v[84:87],a[156:159],v[184:187],v[36:39]
v_cvt_pk_bf16_f32 v18,v20,v21
v_cvt_pk_bf16_f32 v19,v22,v23
v_mfma_f32_16x16x32_bf16 v[84:87],a[152:155],v[176:179],v[84:87]
v_accvgpr_write_b32 a104,v16
v_accvgpr_write_b32 a105,v17
v_mfma_f32_16x16x32_bf16 v[84:87],a[148:151],v[168:171],v[84:87]
v_exp_f32 v80,v80
v_mfma_f32_16x16x32_bf16 v[84:87],a[144:147],v[160:163],v[84:87]
v_exp_f32 v81,v81
v_mfma_f32_16x16x32_bf16 v[88:91],a[160:163],v[160:163],v[36:39]
v_exp_f32 v82,v82
v_mfma_f32_16x16x32_bf16 v[88:91],a[164:167],v[168:171],v[88:91]
v_exp_f32 v83,v83
v_mfma_f32_16x16x32_bf16 v[88:91],a[168:171],v[176:179],v[88:91]
v_cvt_pk_bf16_f32 v80,v80,v81
v_cvt_pk_bf16_f32 v81,v82,v83
v_mfma_f32_16x16x32_bf16 v[88:91],a[172:175],v[184:187],v[88:91]
v_exp_f32 v84,v84
v_mfma_f32_16x16x32_bf16 v[92:95],a[188:191],v[184:187],v[36:39]
v_exp_f32 v85,v85
v_mfma_f32_16x16x32_bf16 v[92:95],a[184:187],v[176:179],v[92:95]
v_exp_f32 v86,v86
v_mfma_f32_16x16x32_bf16 v[92:95],a[180:183],v[168:171],v[92:95]
buffer_load_dwordx4 v5,s[24:27],s74 offen offset:1024 lds
v_cvt_pk_bf16_f32 v82,v84,v85
v_mfma_f32_16x16x32_bf16 v[92:95],a[176:179],v[160:163],v[92:95]
v_exp_f32 v87,v87
v_mfma_f32_16x16x32_bf16 a[32:35],a[192:195],v[112:115],a[32:35]
ds_read_b64_tr_b16 a[192:193],v2
ds_read_b64_tr_b16 a[194:195],v2 offset:4096
v_mfma_f32_16x16x32_bf16 a[32:35],a[196:199],v[120:123],a[32:35]
ds_read_b64_tr_b16 a[196:197],v2 offset:8192
v_cvt_pk_bf16_f32 v83,v86,v87
v_mfma_f32_16x16x32_bf16 a[36:39],a[200:203],v[112:115],a[36:39]
ds_read_b64_tr_b16 a[198:199],v2 offset:12288
v_accvgpr_write_b32 a106,v18
v_mfma_f32_16x16x32_bf16 a[36:39],a[204:207],v[120:123],a[36:39]
ds_read_b64_tr_b16 a[200:201],v3
v_accvgpr_write_b32 a107,v19
v_mfma_f32_16x16x32_bf16 a[40:43],a[208:211],v[112:115],a[40:43]
ds_read_b64_tr_b16 a[202:203],v3 offset:4096
ds_read_b64_tr_b16 a[204:205],v3 offset:8192
v_mfma_f32_16x16x32_bf16 a[40:43],a[212:215],v[120:123],a[40:43]
ds_read_b64_tr_b16 a[206:207],v3 offset:12288
v_accvgpr_read_b32 v8,a64
v_mfma_f32_16x16x32_bf16 a[44:47],a[216:219],v[112:115],a[44:47]
ds_read_b64_tr_b16 a[208:209],v2 offset:256
v_accvgpr_read_b32 v9,a65
v_mfma_f32_16x16x32_bf16 a[44:47],a[220:223],v[120:123],a[44:47]
ds_read_b64_tr_b16 a[210:211],v2 offset:4352
v_accvgpr_read_b32 v10,a66
v_mfma_f32_16x16x32_bf16 a[48:51],a[224:227],v[112:115],a[48:51]
ds_read_b64_tr_b16 a[212:213],v2 offset:8448
ds_read_b64_tr_b16 a[214:215],v2 offset:12544
v_mfma_f32_16x16x32_bf16 a[48:51],a[228:231],v[120:123],a[48:51]
ds_read_b64_tr_b16 a[216:217],v3 offset:256
v_accvgpr_read_b32 v11,a67
v_mfma_f32_16x16x32_bf16 a[52:55],a[232:235],v[112:115],a[52:55]
ds_read_b64_tr_b16 a[218:219],v3 offset:4352
v_accvgpr_read_b32 v12,a68
v_mfma_f32_16x16x32_bf16 a[52:55],a[236:239],v[120:123],a[52:55]
ds_read_b64_tr_b16 a[220:221],v3 offset:8448
v_accvgpr_read_b32 v13,a69
v_mfma_f32_16x16x32_bf16 a[56:59],a[240:243],v[112:115],a[56:59]
ds_read_b64_tr_b16 a[222:223],v3 offset:12544
ds_read_b64_tr_b16 a[224:225],v2 offset:512
v_mfma_f32_16x16x32_bf16 a[56:59],a[244:247],v[120:123],a[56:59]
ds_read_b64_tr_b16 a[226:227],v2 offset:4608
v_accvgpr_read_b32 v14,a70
v_mfma_f32_16x16x32_bf16 a[60:63],a[248:251],v[112:115],a[60:63]
buffer_load_dwordx4 v4,s[24:27],s74 offen offset:2048 lds
ds_read_b64_tr_b16 a[228:229],v2 offset:8704
v_mfma_f32_16x16x32_bf16 a[60:63],a[252:255],v[120:123],a[60:63]
ds_read_b64_tr_b16 a[230:231],v2 offset:12800
v_accvgpr_read_b32 v15,a71
v_mfma_f32_16x16x32_bf16 v[60:63],v[28:31],v[112:115],v[60:63]
ds_read_b64_tr_b16 a[232:233],v3 offset:512
ds_read_b64_tr_b16 a[234:235],v3 offset:4608
v_mfma_f32_16x16x32_bf16 v[60:63],v[28:31],v[120:123],v[60:63]
ds_read_b64_tr_b16 a[236:237],v3 offset:8704
v_mul_f32 v8,v8,v26
v_mfma_f32_16x16x32_bf16 v[96:99],a[128:131],v[192:195],v[40:43]
ds_read_b64_tr_b16 a[238:239],v3 offset:12800
v_mul_f32 v9,v9,v26
v_mfma_f32_16x16x32_bf16 v[96:99],a[132:135],v[200:203],v[96:99]
ds_read_b64_tr_b16 a[240:241],v2 offset:768
v_mul_f32 v10,v10,v26
v_mfma_f32_16x16x32_bf16 v[96:99],a[136:139],v[208:211],v[96:99]
ds_read_b64_tr_b16 a[242:243],v2 offset:4864
ds_read_b64_tr_b16 a[244:245],v2 offset:8960
v_mfma_f32_16x16x32_bf16 v[96:99],a[140:143],v[216:219],v[96:99]
ds_read_b64_tr_b16 a[246:247],v2 offset:13056
v_add_u32 v2,s78,v2
v_mfma_f32_16x16x32_bf16 v[100:103],a[156:159],v[216:219],v[40:43]
ds_read_b64_tr_b16 a[248:249],v3 offset:768
v_mul_f32 v11,v11,v26
v_mfma_f32_16x16x32_bf16 v[100:103],a[152:155],v[208:211],v[100:103]
ds_read_b64_tr_b16 a[250:251],v3 offset:4864
v_mul_f32 v12,v12,v26
v_mfma_f32_16x16x32_bf16 v[100:103],a[148:151],v[200:203],v[100:103]
ds_read_b64_tr_b16 a[252:253],v3 offset:8960
ds_read_b64_tr_b16 a[254:255],v3 offset:13056
v_mfma_f32_16x16x32_bf16 v[100:103],a[144:147],v[192:195],v[100:103]
v_exp_f32 v88,v88
v_mfma_f32_16x16x32_bf16 v[104:107],a[160:163],v[192:195],v[40:43]
v_exp_f32 v89,v89
v_mfma_f32_16x16x32_bf16 v[104:107],a[164:167],v[200:203],v[104:107]
v_exp_f32 v90,v90
v_mfma_f32_16x16x32_bf16 v[104:107],a[168:171],v[208:211],v[104:107]
v_exp_f32 v91,v91
v_mfma_f32_16x16x32_bf16 v[104:107],a[172:175],v[216:219],v[104:107]
v_exp_f32 v92,v92
v_mfma_f32_16x16x32_bf16 v[108:111],a[188:191],v[216:219],v[40:43]
buffer_load_dwordx4 v5,s[24:27],s74 offen offset:3072 lds
v_cvt_pk_bf16_f32 v88,v88,v89
v_mfma_f32_16x16x32_bf16 v[108:111],a[184:187],v[208:211],v[108:111]
s_add_u32 s74,s74,16384
s_and_b32 s74,s74,0x3ffff
s_or_b32 s74,s74,s69
v_mfma_f32_16x16x32_bf16 v[108:111],a[180:183],v[200:203],v[108:111]
s_and_b32 s69,s75,0xfffc0000
v_exp_f32 v93,v93
v_mfma_f32_16x16x32_bf16 v[108:111],a[176:179],v[192:195],v[108:111]
s_waitcnt lgkmcnt(0)
v_exp_f32 v94,v94
v_mfma_f32_16x16x32_bf16 a[0:3],a[192:195],v[64:67],a[0:3]
v_exp_f32 v95,v95
v_mfma_f32_16x16x32_bf16 a[0:3],a[196:199],v[72:75],a[0:3]
v_cvt_pk_bf16_f32 v89,v90,v91
v_cvt_pk_bf16_f32 v90,v92,v93
v_mfma_f32_16x16x32_bf16 a[4:7],a[200:203],v[64:67],a[4:7]
v_cvt_pk_bf16_f32 v91,v94,v95
v_add_u32 v3,s78,v3
v_mfma_f32_16x16x32_bf16 a[4:7],a[204:207],v[72:75],a[4:7]
s_sub_u32 s78,s77,32768
v_exp_f32 v96,v96
v_mfma_f32_16x16x32_bf16 a[8:11],a[208:211],v[64:67],a[8:11]
s_and_b32 s78,s78,0x1ffff
v_exp_f32 v97,v97
v_mfma_f32_16x16x32_bf16 a[8:11],a[212:215],v[72:75],a[8:11]
s_sub_u32 s78,s78,s77
v_exp_f32 v98,v98
v_mfma_f32_16x16x32_bf16 a[12:15],a[216:219],v[64:67],a[12:15]
s_add_u32 s77,s77,s78
v_exp_f32 v99,v99
v_mfma_f32_16x16x32_bf16 a[12:15],a[220:223],v[72:75],a[12:15]
v_exp_f32 v100,v100
v_mfma_f32_16x16x32_bf16 a[16:19],a[224:227],v[64:67],a[16:19]
v_exp_f32 v101,v101
v_mfma_f32_16x16x32_bf16 a[16:19],a[228:231],v[72:75],a[16:19]
v_exp_f32 v102,v102
v_mfma_f32_16x16x32_bf16 a[20:23],a[232:235],v[64:67],a[20:23]
s_getreg_b32 s92,hwreg(HW_REG_IB_STS,0,4)
s_min_u32 s4,s92,s6
s_mov_b32 s6,s92
v_mfma_f32_16x16x32_bf16 a[20:23],a[236:239],v[72:75],a[20:23]
s_cmp_ge_u32 s4,8
s_cselect_b32 s4,14,s5
v_cvt_pk_bf16_f32 v96,v96,v97
v_mfma_f32_16x16x32_bf16 a[24:27],a[240:243],v[64:67],a[24:27]
v_exp_f32 v103,v103
v_mfma_f32_16x16x32_bf16 a[24:27],a[244:247],v[72:75],a[24:27]
v_cvt_pk_bf16_f32 v97,v98,v99
v_cvt_pk_bf16_f32 v98,v100,v101
v_mfma_f32_16x16x32_bf16 a[28:31],a[248:251],v[64:67],a[28:31]
)A"
R"A(v_cvt_pk_bf16_f32 v99,v102,v103
v_mul_f32 v13,v13,v26
v_mfma_f32_16x16x32_bf16 a[28:31],a[252:255],v[72:75],a[28:31]
s_getpc_b64 s[92:93]
s_cmp_lg_u32 s4,0
s_cbranch_scc1 L_pace_%=
v_mfma_f32_16x16x32_bf16 v[48:51],v[28:31],v[64:67],v[48:51]
v_exp_f32 v104,v104
v_mfma_f32_16x16x32_bf16 v[48:51],v[28:31],v[72:75],v[48:51]
v_exp_f32 v105,v105
s_waitcnt vmcnt(16)
s_barrier
v_mfma_f32_16x16x32_bf16 v[112:115],a[128:131],v[224:227],v[44:47]
s_add_u32 m0,s76,0
ds_read_b128 a[128:131],v1
v_cvt_pk_bf16_f32 v104,v104,v105
v_mfma_f32_16x16x32_bf16 v[112:115],a[132:135],v[232:235],v[112:115]
v_exp_f32 v106,v106
v_mfma_f32_16x16x32_bf16 v[112:115],a[136:139],v[240:243],v[112:115]
buffer_load_dwordx4 v4,s[20:23],s74 offen nt lds
ds_read_b128 a[132:135],v1 offset:256
v_mfma_f32_16x16x32_bf16 v[112:115],a[140:143],v[248:251],v[112:115]
v_exp_f32 v107,v107
v_mfma_f32_16x16x32_bf16 v[116:119],a[156:159],v[248:251],v[44:47]
ds_read_b128 a[136:139],v1 offset:512
v_cvt_pk_bf16_f32 v105,v106,v107
v_mfma_f32_16x16x32_bf16 v[116:119],a[152:155],v[240:243],v[116:119]
v_exp_f32 v108,v108
v_mfma_f32_16x16x32_bf16 v[116:119],a[148:151],v[232:235],v[116:119]
ds_read_b128 a[140:143],v1 offset:768
v_mul_f32 v14,v14,v26
v_mfma_f32_16x16x32_bf16 v[116:119],a[144:147],v[224:227],v[116:119]
v_exp_f32 v109,v109
v_mfma_f32_16x16x32_bf16 v[120:123],a[160:163],v[224:227],v[44:47]
ds_read_b128 a[156:159],v1 offset:4864
v_cvt_pk_bf16_f32 v106,v108,v109
v_mfma_f32_16x16x32_bf16 v[120:123],a[164:167],v[232:235],v[120:123]
v_exp_f32 v110,v110
v_mfma_f32_16x16x32_bf16 v[120:123],a[168:171],v[240:243],v[120:123]
ds_read_b128 a[152:155],v1 offset:4608
v_mul_f32 v15,v15,v26
v_mfma_f32_16x16x32_bf16 v[120:123],a[172:175],v[248:251],v[120:123]
v_exp_f32 v111,v111
v_mfma_f32_16x16x32_bf16 v[124:127],a[188:191],v[248:251],v[44:47]
ds_read_b128 a[148:151],v1 offset:4352
v_cvt_pk_bf16_f32 v107,v110,v111
v_mfma_f32_16x16x32_bf16 v[124:127],a[184:187],v[240:243],v[124:127]
v_exp_f32 v112,v112
v_mfma_f32_16x16x32_bf16 v[124:127],a[180:183],v[232:235],v[124:127]
ds_read_b128 a[144:147],v1 offset:4096
v_cvt_pk_bf16_f32 v8,v8,v9
v_mfma_f32_16x16x32_bf16 v[124:127],a[176:179],v[224:227],v[124:127]
v_exp_f32 v113,v113
v_mfma_f32_16x16x32_bf16 v[132:135],a[192:195],v[80:83],v[132:135]
ds_read_b128 a[160:163],v1 offset:8192
v_cvt_pk_bf16_f32 v112,v112,v113
v_mfma_f32_16x16x32_bf16 v[132:135],a[196:199],v[88:91],v[132:135]
v_exp_f32 v114,v114
v_mfma_f32_16x16x32_bf16 v[140:143],a[200:203],v[80:83],v[140:143]
buffer_load_dwordx4 v4,s[20:23],s74 offen offset:1024 nt lds
ds_read_b128 a[164:167],v1 offset:8448
v_mfma_f32_16x16x32_bf16 v[140:143],a[204:207],v[88:91],v[140:143]
v_exp_f32 v115,v115
v_mfma_f32_16x16x32_bf16 v[148:151],a[208:211],v[80:83],v[148:151]
ds_read_b128 a[168:171],v1 offset:8704
v_cvt_pk_bf16_f32 v113,v114,v115
v_mfma_f32_16x16x32_bf16 v[148:151],a[212:215],v[88:91],v[148:151]
v_exp_f32 v116,v116
v_mfma_f32_16x16x32_bf16 v[156:159],a[216:219],v[80:83],v[156:159]
ds_read_b128 a[172:175],v1 offset:8960
v_cvt_pk_bf16_f32 v9,v10,v11
v_mfma_f32_16x16x32_bf16 v[156:159],a[220:223],v[88:91],v[156:159]
v_exp_f32 v117,v117
v_mfma_f32_16x16x32_bf16 v[164:167],a[224:227],v[80:83],v[164:167]
ds_read_b128 a[188:191],v1 offset:13056
v_cvt_pk_bf16_f32 v114,v116,v117
v_mfma_f32_16x16x32_bf16 v[164:167],a[228:231],v[88:91],v[164:167]
v_exp_f32 v118,v118
v_mfma_f32_16x16x32_bf16 v[172:175],a[232:235],v[80:83],v[172:175]
ds_read_b128 a[184:187],v1 offset:12800
v_cvt_pk_bf16_f32 v10,v12,v13
v_mfma_f32_16x16x32_bf16 v[172:175],a[236:239],v[88:91],v[172:175]
v_exp_f32 v119,v119
v_mfma_f32_16x16x32_bf16 v[180:183],a[240:243],v[80:83],v[180:183]
ds_read_b128 a[180:183],v1 offset:12544
v_cvt_pk_bf16_f32 v115,v118,v119
v_mfma_f32_16x16x32_bf16 v[180:183],a[244:247],v[88:91],v[180:183]
v_exp_f32 v120,v120
v_mfma_f32_16x16x32_bf16 v[188:191],a[248:251],v[80:83],v[188:191]
ds_read_b128 a[176:179],v1 offset:12288
v_cvt_pk_bf16_f32 v11,v14,v15
v_mfma_f32_16x16x32_bf16 v[188:191],a[252:255],v[88:91],v[188:191]
v_exp_f32 v121,v121
v_mfma_f32_16x16x32_bf16 v[52:55],v[28:31],v[80:83],v[52:55]
v_exp_f32 v122,v122
v_mfma_f32_16x16x32_bf16 v[52:55],v[28:31],v[88:91],v[52:55]
s_waitcnt lgkmcnt(3)
v_exp_f32 v123,v123
v_mfma_f32_16x16x32_bf16 v[64:67],a[128:131],v[128:131],v[32:35]
buffer_load_dwordx4 v5,s[20:23],s74 offen offset:2048 nt lds
v_cvt_pk_bf16_f32 v120,v120,v121
v_mfma_f32_16x16x32_bf16 v[64:67],a[132:135],v[136:139],v[64:67]
v_exp_f32 v124,v124
v_mfma_f32_16x16x32_bf16 v[64:67],a[136:139],v[144:147],v[64:67]
v_exp_f32 v125,v125
v_mfma_f32_16x16x32_bf16 v[64:67],a[140:143],v[152:155],v[64:67]
v_exp_f32 v126,v126
v_mfma_f32_16x16x32_bf16 v[68:71],a[156:159],v[152:155],v[32:35]
v_exp_f32 v127,v127
v_mfma_f32_16x16x32_bf16 v[68:71],a[152:155],v[144:147],v[68:71]
v_cvt_pk_bf16_f32 v121,v122,v123
v_cvt_pk_bf16_f32 v122,v124,v125
v_mfma_f32_16x16x32_bf16 v[68:71],a[148:151],v[136:139],v[68:71]
v_cvt_pk_bf16_f32 v123,v126,v127
v_accvgpr_write_b32 a64,v8
v_mfma_f32_16x16x32_bf16 v[68:71],a[144:147],v[128:131],v[68:71]
v_exp_f32 v64,v64
v_mfma_f32_16x16x32_bf16 v[72:75],a[160:163],v[128:131],v[32:35]
v_exp_f32 v65,v65
v_mfma_f32_16x16x32_bf16 v[72:75],a[164:167],v[136:139],v[72:75]
v_exp_f32 v66,v66
v_mfma_f32_16x16x32_bf16 v[72:75],a[168:171],v[144:147],v[72:75]
v_exp_f32 v67,v67
v_mfma_f32_16x16x32_bf16 v[72:75],a[172:175],v[152:155],v[72:75]
v_exp_f32 v68,v68
v_mfma_f32_16x16x32_bf16 v[76:79],a[188:191],v[152:155],v[32:35]
s_waitcnt lgkmcnt(0)
v_exp_f32 v69,v69
v_mfma_f32_16x16x32_bf16 v[76:79],a[184:187],v[144:147],v[76:79]
v_exp_f32 v70,v70
v_mfma_f32_16x16x32_bf16 v[76:79],a[180:183],v[136:139],v[76:79]
v_exp_f32 v71,v71
v_mfma_f32_16x16x32_bf16 v[76:79],a[176:179],v[128:131],v[76:79]
v_cvt_pk_bf16_f32 v64,v64,v65
v_cvt_pk_bf16_f32 v65,v66,v67
v_mfma_f32_16x16x32_bf16 v[196:199],a[192:195],v[96:99],v[196:199]
buffer_load_dwordx4 v5,s[20:23],s74 offen offset:3072 lds
v_cvt_pk_bf16_f32 v66,v68,v69
v_mfma_f32_16x16x32_bf16 v[196:199],a[196:199],v[104:107],v[196:199]
v_cvt_pk_bf16_f32 v67,v70,v71
v_accvgpr_write_b32 a65,v9
v_mfma_f32_16x16x32_bf16 v[204:207],a[200:203],v[96:99],v[204:207]
v_exp_f32 v72,v72
v_mfma_f32_16x16x32_bf16 v[204:207],a[204:207],v[104:107],v[204:207]
v_exp_f32 v73,v73
v_mfma_f32_16x16x32_bf16 v[212:215],a[208:211],v[96:99],v[212:215]
v_exp_f32 v74,v74
v_mfma_f32_16x16x32_bf16 v[212:215],a[212:215],v[104:107],v[212:215]
v_exp_f32 v75,v75
v_mfma_f32_16x16x32_bf16 v[220:223],a[216:219],v[96:99],v[220:223]
v_exp_f32 v76,v76
v_mfma_f32_16x16x32_bf16 v[220:223],a[220:223],v[104:107],v[220:223]
v_exp_f32 v77,v77
v_mfma_f32_16x16x32_bf16 v[228:231],a[224:227],v[96:99],v[228:231]
v_exp_f32 v78,v78
v_mfma_f32_16x16x32_bf16 v[228:231],a[228:231],v[104:107],v[228:231]
v_exp_f32 v79,v79
v_mfma_f32_16x16x32_bf16 v[236:239],a[232:235],v[96:99],v[236:239]
v_cvt_pk_bf16_f32 v72,v72,v73
v_cvt_pk_bf16_f32 v73,v74,v75
v_mfma_f32_16x16x32_bf16 v[236:239],a[236:239],v[104:107],v[236:239]
v_cvt_pk_bf16_f32 v74,v76,v77
v_cvt_pk_bf16_f32 v75,v78,v79
v_mfma_f32_16x16x32_bf16 v[244:247],a[240:243],v[96:99],v[244:247]
v_accvgpr_write_b32 a66,v10
v_accvgpr_write_b32 a67,v11
v_mfma_f32_16x16x32_bf16 v[244:247],a[244:247],v[104:107],v[244:247]
v_accvgpr_read_b32 v16,a72
v_accvgpr_read_b32 v17,a73
v_mfma_f32_16x16x32_bf16 v[252:255],a[248:251],v[96:99],v[252:255]
s_add_u32 m0,s76,16384
v_accvgpr_read_b32 v18,a74
v_accvgpr_read_b32 v19,a75
v_mfma_f32_16x16x32_bf16 v[252:255],a[252:255],v[104:107],v[252:255]
s_sub_u32 s76,s76,32768
v_accvgpr_read_b32 v20,a76
v_accvgpr_read_b32 v21,a77
v_mfma_f32_16x16x32_bf16 v[56:59],v[28:31],v[96:99],v[56:59]
s_and_b32 s76,s76,0x1ffff
buffer_load_dwordx4 v4,s[24:27],s74 offen lds
v_accvgpr_read_b32 v22,a78
v_mfma_f32_16x16x32_bf16 v[56:59],v[28:31],v[104:107],v[56:59]
v_accvgpr_read_b32 v23,a79
v_mul_f32 v16,v16,v26
v_mfma_f32_16x16x32_bf16 v[80:83],a[128:131],v[160:163],v[36:39]
v_mul_f32 v17,v17,v26
v_mul_f32 v18,v18,v26
v_mfma_f32_16x16x32_bf16 v[80:83],a[132:135],v[168:171],v[80:83]
v_mul_f32 v19,v19,v26
v_mul_f32 v20,v20,v26
v_mfma_f32_16x16x32_bf16 v[80:83],a[136:139],v[176:179],v[80:83]
v_mul_f32 v21,v21,v26
v_mul_f32 v22,v22,v26
v_mfma_f32_16x16x32_bf16 v[80:83],a[140:143],v[184:187],v[80:83]
v_mul_f32 v23,v23,v26
v_cvt_pk_bf16_f32 v16,v16,v17
v_mfma_f32_16x16x32_bf16 v[84:87],a[156:159],v[184:187],v[36:39]
v_cvt_pk_bf16_f32 v17,v18,v19
v_cvt_pk_bf16_f32 v18,v20,v21
v_mfma_f32_16x16x32_bf16 v[84:87],a[152:155],v[176:179],v[84:87]
v_cvt_pk_bf16_f32 v19,v22,v23
v_accvgpr_write_b32 a72,v16
v_mfma_f32_16x16x32_bf16 v[84:87],a[148:151],v[168:171],v[84:87]
v_accvgpr_write_b32 a73,v17
v_accvgpr_write_b32 a74,v18
v_mfma_f32_16x16x32_bf16 v[84:87],a[144:147],v[160:163],v[84:87]
v_accvgpr_write_b32 a75,v19
v_accvgpr_read_b32 v8,a80
v_mfma_f32_16x16x32_bf16 v[88:91],a[160:163],v[160:163],v[36:39]
v_accvgpr_read_b32 v9,a81
v_accvgpr_read_b32 v10,a82
v_mfma_f32_16x16x32_bf16 v[88:91],a[164:167],v[168:171],v[88:91]
v_accvgpr_read_b32 v11,a83
v_accvgpr_read_b32 v12,a84
v_mfma_f32_16x16x32_bf16 v[88:91],a[168:171],v[176:179],v[88:91]
v_accvgpr_read_b32 v13,a85
v_accvgpr_read_b32 v14,a86
v_mfma_f32_16x16x32_bf16 v[88:91],a[172:175],v[184:187],v[88:91]
v_accvgpr_read_b32 v15,a87
v_mul_f32 v8,v8,v26
v_mfma_f32_16x16x32_bf16 v[92:95],a[188:191],v[184:187],v[36:39]
v_mul_f32 v9,v9,v26
v_mul_f32 v10,v10,v26
v_mfma_f32_16x16x32_bf16 v[92:95],a[184:187],v[176:179],v[92:95]
v_mul_f32 v11,v11,v26
v_mul_f32 v12,v12,v26
v_mfma_f32_16x16x32_bf16 v[92:95],a[180:183],v[168:171],v[92:95]
buffer_load_dwordx4 v5,s[24:27],s74 offen offset:1024 lds
v_mul_f32 v13,v13,v26
v_mfma_f32_16x16x32_bf16 v[92:95],a[176:179],v[160:163],v[92:95]
v_mul_f32 v14,v14,v26
v_mul_f32 v15,v15,v26
v_mfma_f32_16x16x32_bf16 a[32:35],a[192:195],v[112:115],a[32:35]
ds_read_b64_tr_b16 a[192:193],v2
ds_read_b64_tr_b16 a[194:195],v2 offset:4096
v_mfma_f32_16x16x32_bf16 a[32:35],a[196:199],v[120:123],a[32:35]
ds_read_b64_tr_b16 a[196:197],v2 offset:8192
v_cvt_pk_bf16_f32 v8,v8,v9
v_mfma_f32_16x16x32_bf16 a[36:39],a[200:203],v[112:115],a[36:39]
ds_read_b64_tr_b16 a[198:199],v2 offset:12288
v_accvgpr_write_b32 a80,v8
v_mfma_f32_16x16x32_bf16 a[36:39],a[204:207],v[120:123],a[36:39]
ds_read_b64_tr_b16 a[200:201],v3
v_accvgpr_read_b32 v16,a88
v_mfma_f32_16x16x32_bf16 a[40:43],a[208:211],v[112:115],a[40:43]
ds_read_b64_tr_b16 a[202:203],v3 offset:4096
ds_read_b64_tr_b16 a[204:205],v3 offset:8192
v_mfma_f32_16x16x32_bf16 a[40:43],a[212:215],v[120:123],a[40:43]
ds_read_b64_tr_b16 a[206:207],v3 offset:12288
v_accvgpr_read_b32 v17,a89
v_mfma_f32_16x16x32_bf16 a[44:47],a[216:219],v[112:115],a[44:47]
ds_read_b64_tr_b16 a[208:209],v2 offset:256
v_accvgpr_read_b32 v18,a90
v_mfma_f32_16x16x32_bf16 a[44:47],a[220:223],v[120:123],a[44:47]
ds_read_b64_tr_b16 a[210:211],v2 offset:4352
v_accvgpr_read_b32 v19,a91
v_mfma_f32_16x16x32_bf16 a[48:51],a[224:227],v[112:115],a[48:51]
ds_read_b64_tr_b16 a[212:213],v2 offset:8448
ds_read_b64_tr_b16 a[214:215],v2 offset:12544
v_mfma_f32_16x16x32_bf16 a[48:51],a[228:231],v[120:123],a[48:51]
ds_read_b64_tr_b16 a[216:217],v3 offset:256
v_accvgpr_read_b32 v20,a92
v_mfma_f32_16x16x32_bf16 a[52:55],a[232:235],v[112:115],a[52:55]
ds_read_b64_tr_b16 a[218:219],v3 offset:4352
v_accvgpr_read_b32 v21,a93
v_mfma_f32_16x16x32_bf16 a[52:55],a[236:239],v[120:123],a[52:55]
ds_read_b64_tr_b16 a[220:221],v3 offset:8448
v_accvgpr_read_b32 v22,a94
v_mfma_f32_16x16x32_bf16 a[56:59],a[240:243],v[112:115],a[56:59]
ds_read_b64_tr_b16 a[222:223],v3 offset:12544
ds_read_b64_tr_b16 a[224:225],v2 offset:512
v_mfma_f32_16x16x32_bf16 a[56:59],a[244:247],v[120:123],a[56:59]
ds_read_b64_tr_b16 a[226:227],v2 offset:4608
v_accvgpr_read_b32 v23,a95
v_mfma_f32_16x16x32_bf16 a[60:63],a[248:251],v[112:115],a[60:63]
buffer_load_dwordx4 v4,s[24:27],s74 offen offset:2048 lds
ds_read_b64_tr_b16 a[228:229],v2 offset:8704
v_mfma_f32_16x16x32_bf16 a[60:63],a[252:255],v[120:123],a[60:63]
ds_read_b64_tr_b16 a[230:231],v2 offset:12800
v_mul_f32 v16,v16,v26
v_mfma_f32_16x16x32_bf16 v[60:63],v[28:31],v[112:115],v[60:63]
)A"
R"A(ds_read_b64_tr_b16 a[232:233],v3 offset:512
ds_read_b64_tr_b16 a[234:235],v3 offset:4608
v_mfma_f32_16x16x32_bf16 v[60:63],v[28:31],v[120:123],v[60:63]
ds_read_b64_tr_b16 a[236:237],v3 offset:8704
v_mul_f32 v17,v17,v26
v_mfma_f32_16x16x32_bf16 v[96:99],a[128:131],v[192:195],v[40:43]
ds_read_b64_tr_b16 a[238:239],v3 offset:12800
v_mul_f32 v18,v18,v26
v_mfma_f32_16x16x32_bf16 v[96:99],a[132:135],v[200:203],v[96:99]
ds_read_b64_tr_b16 a[240:241],v2 offset:768
v_mul_f32 v19,v19,v26
v_mfma_f32_16x16x32_bf16 v[96:99],a[136:139],v[208:211],v[96:99]
ds_read_b64_tr_b16 a[242:243],v2 offset:4864
ds_read_b64_tr_b16 a[244:245],v2 offset:8960
v_mfma_f32_16x16x32_bf16 v[96:99],a[140:143],v[216:219],v[96:99]
ds_read_b64_tr_b16 a[246:247],v2 offset:13056
v_mul_f32 v20,v20,v26
v_mfma_f32_16x16x32_bf16 v[100:103],a[156:159],v[216:219],v[40:43]
ds_read_b64_tr_b16 a[248:249],v3 offset:768
v_mul_f32 v21,v21,v26
v_mfma_f32_16x16x32_bf16 v[100:103],a[152:155],v[208:211],v[100:103]
ds_read_b64_tr_b16 a[250:251],v3 offset:4864
v_mul_f32 v22,v22,v26
v_mfma_f32_16x16x32_bf16 v[100:103],a[148:151],v[200:203],v[100:103]
ds_read_b64_tr_b16 a[252:253],v3 offset:8960
ds_read_b64_tr_b16 a[254:255],v3 offset:13056
v_mfma_f32_16x16x32_bf16 v[100:103],a[144:147],v[192:195],v[100:103]
v_mul_f32 v23,v23,v26
v_mov_b32 v8,v7
v_mfma_f32_16x16x32_bf16 v[104:107],a[160:163],v[192:195],v[40:43]
L_pd11_%=:
s_cbranch_execz L_t1c_%=
v_exp_f32 v80,v80
v_mfma_f32_16x16x32_bf16 v[104:107],a[164:167],v[200:203],v[104:107]
v_exp_f32 v81,v81
v_mfma_f32_16x16x32_bf16 v[104:107],a[168:171],v[208:211],v[104:107]
v_exp_f32 v82,v82
v_mfma_f32_16x16x32_bf16 v[104:107],a[172:175],v[216:219],v[104:107]
v_exp_f32 v83,v83
v_mfma_f32_16x16x32_bf16 v[108:111],a[188:191],v[216:219],v[40:43]
buffer_load_dwordx4 v5,s[24:27],s74 offen offset:3072 lds
ds_read_b128 a[100:103],v7 offset:2048
v_mfma_f32_16x16x32_bf16 v[108:111],a[184:187],v[208:211],v[108:111]
s_add_u32 s74,s74,16384
v_exp_f32 v84,v84
v_mfma_f32_16x16x32_bf16 v[108:111],a[180:183],v[200:203],v[108:111]
s_and_b32 s74,s74,0x3ffff
v_exp_f32 v85,v85
v_mfma_f32_16x16x32_bf16 v[108:111],a[176:179],v[192:195],v[108:111]
s_or_b32 s74,s74,s69
v_exp_f32 v86,v86
s_waitcnt lgkmcnt(1)
v_mfma_f32_16x16x32_bf16 a[0:3],a[192:195],v[64:67],a[0:3]
v_exp_f32 v87,v87
v_mfma_f32_16x16x32_bf16 a[0:3],a[196:199],v[72:75],a[0:3]
v_exp_f32 v88,v88
v_mfma_f32_16x16x32_bf16 a[4:7],a[200:203],v[64:67],a[4:7]
v_exp_f32 v89,v89
v_mfma_f32_16x16x32_bf16 a[4:7],a[204:207],v[72:75],a[4:7]
ds_read_b128 a[108:111],v7 offset:3072
v_cvt_pk_bf16_f32 v9,v10,v11
v_mfma_f32_16x16x32_bf16 a[8:11],a[208:211],v[64:67],a[8:11]
v_cvt_pk_bf16_f32 v10,v12,v13
v_cvt_pk_bf16_f32 v11,v14,v15
v_mfma_f32_16x16x32_bf16 a[8:11],a[212:215],v[72:75],a[8:11]
ds_read_b128 a[68:71],v7 offset:6144
v_accvgpr_write_b32 a81,v9
v_mfma_f32_16x16x32_bf16 a[12:15],a[216:219],v[64:67],a[12:15]
v_accvgpr_write_b32 a82,v10
v_accvgpr_write_b32 a83,v11
v_mfma_f32_16x16x32_bf16 a[12:15],a[220:223],v[72:75],a[12:15]
ds_read_b128 a[76:79],v7 offset:7168
v_cvt_pk_bf16_f32 v16,v16,v17
v_mfma_f32_16x16x32_bf16 a[16:19],a[224:227],v[64:67],a[16:19]
v_cvt_pk_bf16_f32 v17,v18,v19
v_cvt_pk_bf16_f32 v18,v20,v21
v_mfma_f32_16x16x32_bf16 a[16:19],a[228:231],v[72:75],a[16:19]
ds_read_b128 a[84:87],v7
v_cvt_pk_bf16_f32 v19,v22,v23
v_mfma_f32_16x16x32_bf16 a[20:23],a[232:235],v[64:67],a[20:23]
s_getreg_b32 s92,hwreg(HW_REG_IB_STS,0,4)
s_min_u32 s4,s92,s6
s_mov_b32 s6,s92
v_mfma_f32_16x16x32_bf16 a[20:23],a[236:239],v[72:75],a[20:23]
s_cmp_ge_u32 s4,8
s_cselect_b32 s4,14,s5
v_accvgpr_write_b32 a88,v16
v_mfma_f32_16x16x32_bf16 a[24:27],a[240:243],v[64:67],a[24:27]
v_accvgpr_write_b32 a89,v17
v_accvgpr_write_b32 a90,v18
v_mfma_f32_16x16x32_bf16 a[24:27],a[244:247],v[72:75],a[24:27]
ds_read_b128 a[92:95],v7 offset:1024
v_accvgpr_write_b32 a91,v19
v_mfma_f32_16x16x32_bf16 a[28:31],a[248:251],v[64:67],a[28:31]
ds_read_b32 v7,v8 offset:5120
v_add_u32 v1,s78,v1
v_mfma_f32_16x16x32_bf16 a[28:31],a[252:255],v[72:75],a[28:31]
s_getpc_b64 s[92:93]
s_cmp_lg_u32 s4,0
s_cbranch_scc1 L_pace_%=
v_mfma_f32_16x16x32_bf16 v[48:51],v[28:31],v[64:67],v[48:51]
ds_read_b128 v[24:27],v8 offset:4096
v_cvt_pk_bf16_f32 v80,v80,v81
v_mfma_f32_16x16x32_bf16 v[48:51],v[28:31],v[72:75],v[48:51]
v_cvt_pk_bf16_f32 v81,v82,v83
v_cvt_pk_bf16_f32 v82,v84,v85
v_cvt_pk_bf16_f32 v83,v86,v87
v_cvt_pk_bf16_f32 v88,v88,v89
s_mov_b32 s70,6
L_a1_%=:
s_waitcnt vmcnt(16)
s_barrier
v_mfma_f32_16x16x32_bf16 v[112:115],a[128:131],v[224:227],v[44:47]
s_add_u32 m0,s76,0
v_exp_f32 v90,v90
v_mfma_f32_16x16x32_bf16 v[112:115],a[132:135],v[232:235],v[112:115]
s_add_u32 s75,s75,16384
v_exp_f32 v91,v91
v_mfma_f32_16x16x32_bf16 v[112:115],a[136:139],v[240:243],v[112:115]
s_and_b32 s69,s75,0xfffc0000
buffer_load_dwordx4 v4,s[20:23],s74 offen nt lds
v_cvt_pk_bf16_f32 v89,v90,v91
v_mfma_f32_16x16x32_bf16 v[112:115],a[140:143],v[248:251],v[112:115]
v_exp_f32 v92,v92
v_mfma_f32_16x16x32_bf16 v[116:119],a[156:159],v[248:251],v[44:47]
v_exp_f32 v93,v93
v_mfma_f32_16x16x32_bf16 v[116:119],a[152:155],v[240:243],v[116:119]
v_exp_f32 v94,v94
v_mfma_f32_16x16x32_bf16 v[116:119],a[148:151],v[232:235],v[116:119]
v_exp_f32 v95,v95
v_mfma_f32_16x16x32_bf16 v[116:119],a[144:147],v[224:227],v[116:119]
v_cvt_pk_bf16_f32 v90,v92,v93
v_cvt_pk_bf16_f32 v91,v94,v95
v_mfma_f32_16x16x32_bf16 v[120:123],a[160:163],v[224:227],v[44:47]
ds_read_b128 a[128:131],v1
v_add_u32 v2,s78,v2
v_mfma_f32_16x16x32_bf16 v[120:123],a[164:167],v[232:235],v[120:123]
v_exp_f32 v96,v96
v_mfma_f32_16x16x32_bf16 v[120:123],a[168:171],v[240:243],v[120:123]
ds_read_b128 a[132:135],v1 offset:256
v_add_u32 v3,s78,v3
v_mfma_f32_16x16x32_bf16 v[120:123],a[172:175],v[248:251],v[120:123]
s_sub_u32 s78,s77,32768
v_exp_f32 v97,v97
v_mfma_f32_16x16x32_bf16 v[124:127],a[188:191],v[248:251],v[44:47]
s_and_b32 s78,s78,0x1ffff
ds_read_b128 a[136:139],v1 offset:512
v_cvt_pk_bf16_f32 v96,v96,v97
v_mfma_f32_16x16x32_bf16 v[124:127],a[184:187],v[240:243],v[124:127]
s_sub_u32 s78,s78,s77
v_exp_f32 v98,v98
v_mfma_f32_16x16x32_bf16 v[124:127],a[180:183],v[232:235],v[124:127]
s_add_u32 s77,s77,s78
v_exp_f32 v99,v99
v_mfma_f32_16x16x32_bf16 v[124:127],a[176:179],v[224:227],v[124:127]
ds_read_b128 a[140:143],v1 offset:768
v_cvt_pk_bf16_f32 v97,v98,v99
v_mfma_f32_16x16x32_bf16 v[132:135],a[192:195],v[80:83],v[132:135]
v_exp_f32 v100,v100
v_mfma_f32_16x16x32_bf16 v[132:135],a[196:199],v[88:91],v[132:135]
v_exp_f32 v101,v101
v_mfma_f32_16x16x32_bf16 v[140:143],a[200:203],v[80:83],v[140:143]
buffer_load_dwordx4 v4,s[20:23],s74 offen offset:1024 nt lds
ds_read_b128 a[156:159],v1 offset:4864
v_mfma_f32_16x16x32_bf16 v[140:143],a[204:207],v[88:91],v[140:143]
v_exp_f32 v102,v102
v_mfma_f32_16x16x32_bf16 v[148:151],a[208:211],v[80:83],v[148:151]
ds_read_b128 a[152:155],v1 offset:4608
v_cvt_pk_bf16_f32 v98,v100,v101
v_mfma_f32_16x16x32_bf16 v[148:151],a[212:215],v[88:91],v[148:151]
v_exp_f32 v103,v103
v_mfma_f32_16x16x32_bf16 v[156:159],a[216:219],v[80:83],v[156:159]
ds_read_b128 a[148:151],v1 offset:4352
v_cvt_pk_bf16_f32 v99,v102,v103
v_mfma_f32_16x16x32_bf16 v[156:159],a[220:223],v[88:91],v[156:159]
v_exp_f32 v104,v104
v_mfma_f32_16x16x32_bf16 v[164:167],a[224:227],v[80:83],v[164:167]
v_exp_f32 v105,v105
v_mfma_f32_16x16x32_bf16 v[164:167],a[228:231],v[88:91],v[164:167]
ds_read_b128 a[144:147],v1 offset:4096
v_cvt_pk_bf16_f32 v104,v104,v105
v_mfma_f32_16x16x32_bf16 v[172:175],a[232:235],v[80:83],v[172:175]
v_exp_f32 v106,v106
v_mfma_f32_16x16x32_bf16 v[172:175],a[236:239],v[88:91],v[172:175]
v_exp_f32 v107,v107
v_mfma_f32_16x16x32_bf16 v[180:183],a[240:243],v[80:83],v[180:183]
ds_read_b128 a[160:163],v1 offset:8192
v_cvt_pk_bf16_f32 v105,v106,v107
v_mfma_f32_16x16x32_bf16 v[180:183],a[244:247],v[88:91],v[180:183]
v_exp_f32 v108,v108
v_mfma_f32_16x16x32_bf16 v[188:191],a[248:251],v[80:83],v[188:191]
v_exp_f32 v109,v109
v_mfma_f32_16x16x32_bf16 v[188:191],a[252:255],v[88:91],v[188:191]
ds_read_b128 a[164:167],v1 offset:8448
v_cvt_pk_bf16_f32 v106,v108,v109
v_mfma_f32_16x16x32_bf16 v[52:55],v[28:31],v[80:83],v[52:55]
v_exp_f32 v110,v110
v_mfma_f32_16x16x32_bf16 v[52:55],v[28:31],v[88:91],v[52:55]
s_waitcnt lgkmcnt(2)
ds_read_b128 a[168:171],v1 offset:8704
v_mfma_f32_16x16x32_bf16 v[64:67],a[128:131],v[128:131],v[32:35]
buffer_load_dwordx4 v5,s[20:23],s74 offen offset:2048 nt lds
v_mfma_f32_16x16x32_bf16 v[64:67],a[132:135],v[136:139],v[64:67]
ds_read_b128 a[172:175],v1 offset:8960
v_mfma_f32_16x16x32_bf16 v[64:67],a[136:139],v[144:147],v[64:67]
v_exp_f32 v111,v111
v_mfma_f32_16x16x32_bf16 v[64:67],a[140:143],v[152:155],v[64:67]
ds_read_b128 a[188:191],v1 offset:13056
v_cvt_pk_bf16_f32 v107,v110,v111
v_mfma_f32_16x16x32_bf16 v[68:71],a[156:159],v[152:155],v[32:35]
v_exp_f32 v112,v112
v_mfma_f32_16x16x32_bf16 v[68:71],a[152:155],v[144:147],v[68:71]
ds_read_b128 a[184:187],v1 offset:12800
v_mfma_f32_16x16x32_bf16 v[68:71],a[148:151],v[136:139],v[68:71]
v_exp_f32 v113,v113
v_mfma_f32_16x16x32_bf16 v[68:71],a[144:147],v[128:131],v[68:71]
s_waitcnt lgkmcnt(3)
ds_read_b128 a[180:183],v1 offset:12544
v_cvt_pk_bf16_f32 v112,v112,v113
v_mfma_f32_16x16x32_bf16 v[72:75],a[160:163],v[128:131],v[32:35]
v_exp_f32 v114,v114
v_mfma_f32_16x16x32_bf16 v[72:75],a[164:167],v[136:139],v[72:75]
ds_read_b128 a[176:179],v1 offset:12288
v_add_u32 v1,s78,v1
v_mfma_f32_16x16x32_bf16 v[72:75],a[168:171],v[144:147],v[72:75]
s_waitcnt lgkmcnt(4)
v_exp_f32 v115,v115
v_mfma_f32_16x16x32_bf16 v[72:75],a[172:175],v[152:155],v[72:75]
s_waitcnt lgkmcnt(3)
v_exp_f32 v116,v116
v_mfma_f32_16x16x32_bf16 v[76:79],a[188:191],v[152:155],v[32:35]
s_waitcnt lgkmcnt(2)
v_exp_f32 v117,v117
v_mfma_f32_16x16x32_bf16 v[76:79],a[184:187],v[144:147],v[76:79]
s_waitcnt lgkmcnt(1)
v_exp_f32 v118,v118
v_mfma_f32_16x16x32_bf16 v[76:79],a[180:183],v[136:139],v[76:79]
s_waitcnt lgkmcnt(0)
v_exp_f32 v119,v119
v_mfma_f32_16x16x32_bf16 v[76:79],a[176:179],v[128:131],v[76:79]
v_cvt_pk_bf16_f32 v113,v114,v115
v_cvt_pk_bf16_f32 v114,v116,v117
v_mfma_f32_16x16x32_bf16 v[196:199],a[192:195],v[96:99],v[196:199]
buffer_load_dwordx4 v5,s[20:23],s74 offen offset:3072 lds
v_cvt_pk_bf16_f32 v115,v118,v119
v_mfma_f32_16x16x32_bf16 v[196:199],a[196:199],v[104:107],v[196:199]
v_exp_f32 v120,v120
v_mfma_f32_16x16x32_bf16 v[204:207],a[200:203],v[96:99],v[204:207]
v_exp_f32 v121,v121
v_mfma_f32_16x16x32_bf16 v[204:207],a[204:207],v[104:107],v[204:207]
v_exp_f32 v122,v122
v_mfma_f32_16x16x32_bf16 v[212:215],a[208:211],v[96:99],v[212:215]
v_exp_f32 v123,v123
v_mfma_f32_16x16x32_bf16 v[212:215],a[212:215],v[104:107],v[212:215]
v_exp_f32 v124,v124
v_mfma_f32_16x16x32_bf16 v[220:223],a[216:219],v[96:99],v[220:223]
v_exp_f32 v125,v125
v_mfma_f32_16x16x32_bf16 v[220:223],a[220:223],v[104:107],v[220:223]
v_exp_f32 v126,v126
v_mfma_f32_16x16x32_bf16 v[228:231],a[224:227],v[96:99],v[228:231]
v_exp_f32 v127,v127
v_mfma_f32_16x16x32_bf16 v[228:231],a[228:231],v[104:107],v[228:231]
v_cvt_pk_bf16_f32 v120,v120,v121
v_cvt_pk_bf16_f32 v121,v122,v123
v_mfma_f32_16x16x32_bf16 v[236:239],a[232:235],v[96:99],v[236:239]
v_cvt_pk_bf16_f32 v122,v124,v125
v_cvt_pk_bf16_f32 v123,v126,v127
v_mfma_f32_16x16x32_bf16 v[236:239],a[236:239],v[104:107],v[236:239]
v_exp_f32 v64,v64
v_mfma_f32_16x16x32_bf16 v[244:247],a[240:243],v[96:99],v[244:247]
v_exp_f32 v65,v65
v_mfma_f32_16x16x32_bf16 v[244:247],a[244:247],v[104:107],v[244:247]
v_exp_f32 v66,v66
v_mfma_f32_16x16x32_bf16 v[252:255],a[248:251],v[96:99],v[252:255]
s_add_u32 m0,s76,16384
v_exp_f32 v67,v67
v_mfma_f32_16x16x32_bf16 v[252:255],a[252:255],v[104:107],v[252:255]
s_sub_u32 s76,s76,32768
v_exp_f32 v68,v68
v_mfma_f32_16x16x32_bf16 v[56:59],v[28:31],v[96:99],v[56:59]
s_and_b32 s76,s76,0x1ffff
buffer_load_dwordx4 v4,s[24:27],s74 offen lds
v_cvt_pk_bf16_f32 v64,v64,v65
v_mfma_f32_16x16x32_bf16 v[56:59],v[28:31],v[104:107],v[56:59]
v_exp_f32 v69,v69
v_mfma_f32_16x16x32_bf16 v[80:83],a[128:131],v[160:163],v[36:39]
v_exp_f32 v70,v70
v_mfma_f32_16x16x32_bf16 v[80:83],a[132:135],v[168:171],v[80:83]
v_exp_f32 v71,v71
v_mfma_f32_16x16x32_bf16 v[80:83],a[136:139],v[176:179],v[80:83]
)A"
R"A(v_cvt_pk_bf16_f32 v65,v66,v67
v_cvt_pk_bf16_f32 v66,v68,v69
v_mfma_f32_16x16x32_bf16 v[80:83],a[140:143],v[184:187],v[80:83]
v_exp_f32 v72,v72
v_mfma_f32_16x16x32_bf16 v[84:87],a[156:159],v[184:187],v[36:39]
v_exp_f32 v73,v73
v_mfma_f32_16x16x32_bf16 v[84:87],a[152:155],v[176:179],v[84:87]
v_cvt_pk_bf16_f32 v67,v70,v71
v_cvt_pk_bf16_f32 v72,v72,v73
v_mfma_f32_16x16x32_bf16 v[84:87],a[148:151],v[168:171],v[84:87]
v_exp_f32 v74,v74
v_mfma_f32_16x16x32_bf16 v[84:87],a[144:147],v[160:163],v[84:87]
v_exp_f32 v75,v75
v_mfma_f32_16x16x32_bf16 v[88:91],a[160:163],v[160:163],v[36:39]
v_exp_f32 v76,v76
v_mfma_f32_16x16x32_bf16 v[88:91],a[164:167],v[168:171],v[88:91]
v_exp_f32 v77,v77
v_mfma_f32_16x16x32_bf16 v[88:91],a[168:171],v[176:179],v[88:91]
v_exp_f32 v78,v78
v_mfma_f32_16x16x32_bf16 v[88:91],a[172:175],v[184:187],v[88:91]
v_exp_f32 v79,v79
v_mfma_f32_16x16x32_bf16 v[92:95],a[188:191],v[184:187],v[36:39]
v_cvt_pk_bf16_f32 v73,v74,v75
v_cvt_pk_bf16_f32 v74,v76,v77
v_mfma_f32_16x16x32_bf16 v[92:95],a[184:187],v[176:179],v[92:95]
v_exp_f32 v80,v80
v_mfma_f32_16x16x32_bf16 v[92:95],a[180:183],v[168:171],v[92:95]
buffer_load_dwordx4 v5,s[24:27],s74 offen offset:1024 lds
v_cvt_pk_bf16_f32 v75,v78,v79
v_mfma_f32_16x16x32_bf16 v[92:95],a[176:179],v[160:163],v[92:95]
v_exp_f32 v81,v81
v_mfma_f32_16x16x32_bf16 a[32:35],a[192:195],v[112:115],a[32:35]
ds_read_b64_tr_b16 a[192:193],v2
ds_read_b64_tr_b16 a[194:195],v2 offset:4096
v_mfma_f32_16x16x32_bf16 a[32:35],a[196:199],v[120:123],a[32:35]
ds_read_b64_tr_b16 a[196:197],v2 offset:8192
v_cvt_pk_bf16_f32 v80,v80,v81
v_mfma_f32_16x16x32_bf16 a[36:39],a[200:203],v[112:115],a[36:39]
v_exp_f32 v82,v82
v_mfma_f32_16x16x32_bf16 a[36:39],a[204:207],v[120:123],a[36:39]
ds_read_b64_tr_b16 a[198:199],v2 offset:12288
ds_read_b64_tr_b16 a[200:201],v3
v_mfma_f32_16x16x32_bf16 a[40:43],a[208:211],v[112:115],a[40:43]
v_exp_f32 v83,v83
v_mfma_f32_16x16x32_bf16 a[40:43],a[212:215],v[120:123],a[40:43]
ds_read_b64_tr_b16 a[202:203],v3 offset:4096
ds_read_b64_tr_b16 a[204:205],v3 offset:8192
v_mfma_f32_16x16x32_bf16 a[44:47],a[216:219],v[112:115],a[44:47]
ds_read_b64_tr_b16 a[206:207],v3 offset:12288
v_cvt_pk_bf16_f32 v81,v82,v83
v_mfma_f32_16x16x32_bf16 a[44:47],a[220:223],v[120:123],a[44:47]
v_exp_f32 v84,v84
v_mfma_f32_16x16x32_bf16 a[48:51],a[224:227],v[112:115],a[48:51]
ds_read_b64_tr_b16 a[208:209],v2 offset:256
ds_read_b64_tr_b16 a[210:211],v2 offset:4352
v_mfma_f32_16x16x32_bf16 a[48:51],a[228:231],v[120:123],a[48:51]
v_exp_f32 v85,v85
v_mfma_f32_16x16x32_bf16 a[52:55],a[232:235],v[112:115],a[52:55]
ds_read_b64_tr_b16 a[212:213],v2 offset:8448
ds_read_b64_tr_b16 a[214:215],v2 offset:12544
v_mfma_f32_16x16x32_bf16 a[52:55],a[236:239],v[120:123],a[52:55]
ds_read_b64_tr_b16 a[216:217],v3 offset:256
v_cvt_pk_bf16_f32 v82,v84,v85
v_mfma_f32_16x16x32_bf16 a[56:59],a[240:243],v[112:115],a[56:59]
v_exp_f32 v86,v86
v_mfma_f32_16x16x32_bf16 a[56:59],a[244:247],v[120:123],a[56:59]
ds_read_b64_tr_b16 a[218:219],v3 offset:4352
ds_read_b64_tr_b16 a[220:221],v3 offset:8448
v_mfma_f32_16x16x32_bf16 a[60:63],a[248:251],v[112:115],a[60:63]
buffer_load_dwordx4 v4,s[24:27],s74 offen offset:2048 lds
ds_read_b64_tr_b16 a[222:223],v3 offset:12544
v_mfma_f32_16x16x32_bf16 a[60:63],a[252:255],v[120:123],a[60:63]
v_exp_f32 v87,v87
v_mfma_f32_16x16x32_bf16 v[60:63],v[28:31],v[112:115],v[60:63]
ds_read_b64_tr_b16 a[224:225],v2 offset:512
ds_read_b64_tr_b16 a[226:227],v2 offset:4608
v_mfma_f32_16x16x32_bf16 v[60:63],v[28:31],v[120:123],v[60:63]
ds_read_b64_tr_b16 a[228:229],v2 offset:8704
v_cvt_pk_bf16_f32 v83,v86,v87
v_mfma_f32_16x16x32_bf16 v[96:99],a[128:131],v[192:195],v[40:43]
v_exp_f32 v88,v88
v_mfma_f32_16x16x32_bf16 v[96:99],a[132:135],v[200:203],v[96:99]
ds_read_b64_tr_b16 a[230:231],v2 offset:12800
ds_read_b64_tr_b16 a[232:233],v3 offset:512
v_mfma_f32_16x16x32_bf16 v[96:99],a[136:139],v[208:211],v[96:99]
v_exp_f32 v89,v89
v_mfma_f32_16x16x32_bf16 v[96:99],a[140:143],v[216:219],v[96:99]
ds_read_b64_tr_b16 a[234:235],v3 offset:4608
ds_read_b64_tr_b16 a[236:237],v3 offset:8704
v_mfma_f32_16x16x32_bf16 v[100:103],a[156:159],v[216:219],v[40:43]
ds_read_b64_tr_b16 a[238:239],v3 offset:12800
v_cvt_pk_bf16_f32 v88,v88,v89
v_mfma_f32_16x16x32_bf16 v[100:103],a[152:155],v[208:211],v[100:103]
ds_read_b64_tr_b16 a[240:241],v2 offset:768
v_mfma_f32_16x16x32_bf16 v[100:103],a[148:151],v[200:203],v[100:103]
ds_read_b64_tr_b16 a[242:243],v2 offset:4864
v_mfma_f32_16x16x32_bf16 v[100:103],a[144:147],v[192:195],v[100:103]
ds_read_b64_tr_b16 a[244:245],v2 offset:8960
ds_read_b64_tr_b16 a[246:247],v2 offset:13056
v_mfma_f32_16x16x32_bf16 v[104:107],a[160:163],v[192:195],v[40:43]
ds_read_b64_tr_b16 a[248:249],v3 offset:768
v_mfma_f32_16x16x32_bf16 v[104:107],a[164:167],v[200:203],v[104:107]
ds_read_b64_tr_b16 a[250:251],v3 offset:4864
v_mfma_f32_16x16x32_bf16 v[104:107],a[168:171],v[208:211],v[104:107]
ds_read_b64_tr_b16 a[252:253],v3 offset:8960
v_mfma_f32_16x16x32_bf16 v[104:107],a[172:175],v[216:219],v[104:107]
ds_read_b64_tr_b16 a[254:255],v3 offset:13056
v_mfma_f32_16x16x32_bf16 v[108:111],a[188:191],v[216:219],v[40:43]
buffer_load_dwordx4 v5,s[24:27],s74 offen offset:3072 lds
v_mfma_f32_16x16x32_bf16 v[108:111],a[184:187],v[208:211],v[108:111]
s_add_u32 s74,s74,16384
s_and_b32 s74,s74,0x3ffff
s_or_b32 s74,s74,s69
v_mfma_f32_16x16x32_bf16 v[108:111],a[180:183],v[200:203],v[108:111]
v_mfma_f32_16x16x32_bf16 v[108:111],a[176:179],v[192:195],v[108:111]
s_waitcnt lgkmcnt(4)
v_mfma_f32_16x16x32_bf16 a[0:3],a[192:195],v[64:67],a[0:3]
v_mfma_f32_16x16x32_bf16 a[0:3],a[196:199],v[72:75],a[0:3]
v_mfma_f32_16x16x32_bf16 a[4:7],a[200:203],v[64:67],a[4:7]
v_mfma_f32_16x16x32_bf16 a[4:7],a[204:207],v[72:75],a[4:7]
v_mfma_f32_16x16x32_bf16 a[8:11],a[208:211],v[64:67],a[8:11]
v_mfma_f32_16x16x32_bf16 a[8:11],a[212:215],v[72:75],a[8:11]
v_mfma_f32_16x16x32_bf16 a[12:15],a[216:219],v[64:67],a[12:15]
v_mfma_f32_16x16x32_bf16 a[12:15],a[220:223],v[72:75],a[12:15]
v_mfma_f32_16x16x32_bf16 a[16:19],a[224:227],v[64:67],a[16:19]
v_mfma_f32_16x16x32_bf16 a[16:19],a[228:231],v[72:75],a[16:19]
v_mfma_f32_16x16x32_bf16 a[20:23],a[232:235],v[64:67],a[20:23]
s_getreg_b32 s92,hwreg(HW_REG_IB_STS,0,4)
s_min_u32 s4,s92,s6
s_mov_b32 s6,s92
v_mfma_f32_16x16x32_bf16 a[20:23],a[236:239],v[72:75],a[20:23]
s_cmp_ge_u32 s4,8
s_cselect_b32 s4,14,s5
v_mfma_f32_16x16x32_bf16 a[24:27],a[240:243],v[64:67],a[24:27]
v_mfma_f32_16x16x32_bf16 a[24:27],a[244:247],v[72:75],a[24:27]
s_waitcnt lgkmcnt(0)
v_mfma_f32_16x16x32_bf16 a[28:31],a[248:251],v[64:67],a[28:31]
v_mfma_f32_16x16x32_bf16 a[28:31],a[252:255],v[72:75],a[28:31]
s_getpc_b64 s[92:93]
s_cmp_lg_u32 s4,0
s_cbranch_scc1 L_pace_%=
v_mfma_f32_16x16x32_bf16 v[48:51],v[28:31],v[64:67],v[48:51]
v_mfma_f32_16x16x32_bf16 v[48:51],v[28:31],v[72:75],v[48:51]
s_sub_u32 s70,s70,1
s_cmp_lg_u32 s70,0
s_cbranch_scc1 L_a1_%=
s_waitcnt vmcnt(16)
s_barrier
v_mfma_f32_16x16x32_bf16 v[112:115],a[128:131],v[224:227],v[44:47]
s_mul_i32 s86,s60,10240
s_mul_i32 s85,s63,1310720
s_mul_i32 s66,s64,320
v_mfma_f32_16x16x32_bf16 v[112:115],a[132:135],v[232:235],v[112:115]
s_add_u32 s85,s85,s66
s_add_u32 s83,s64,0
s_and_b32 s83,s83,3
v_mfma_f32_16x16x32_bf16 v[112:115],a[136:139],v[240:243],v[112:115]
s_lshl_b32 s83,s83,1
s_mul_i32 s83,s83,163840
s_add_u32 s83,s83,s85
v_mfma_f32_16x16x32_bf16 v[112:115],a[140:143],v[248:251],v[112:115]
s_cmp_eq_u32 s60,3
s_cselect_b32 s69,0x40000000,0
v_add_u32 v14,s69,v6
v_mfma_f32_16x16x32_bf16 v[116:119],a[156:159],v[248:251],v[44:47]
s_add_u32 s84,s83,20480
v_exp_f32 v90,v90
v_mfma_f32_16x16x32_bf16 v[116:119],a[152:155],v[240:243],v[116:119]
s_add_u32 m0,s86,122880
v_exp_f32 v91,v91
v_mfma_f32_16x16x32_bf16 v[116:119],a[148:151],v[232:235],v[116:119]
s_getpc_b64 s[8:9]
L_pcg_%=:
v_exp_f32 v92,v92
v_mfma_f32_16x16x32_bf16 v[116:119],a[144:147],v[224:227],v[116:119]
s_mov_b32 s6,L_gbF_%=-L_pcg_%=
buffer_load_dwordx4 v24,s[48:51],s83 offen lds
v_cvt_pk_bf16_f32 v89,v90,v91
v_mfma_f32_16x16x32_bf16 v[120:123],a[160:163],v[224:227],v[44:47]
s_mov_b32 s10,L_geF_%=-L_gbF_%=
v_exp_f32 v93,v93
v_mfma_f32_16x16x32_bf16 v[120:123],a[164:167],v[232:235],v[120:123]
s_mov_b32 s11,0x20000
buffer_store_dwordx4 a[112:115],v14,s[40:43],s81 offen offset:3072 sc1
v_mfma_f32_16x16x32_bf16 v[120:123],a[168:171],v[240:243],v[120:123]
s_lshr_b32 s7,%1,3
v_exp_f32 v94,v94
v_mfma_f32_16x16x32_bf16 v[120:123],a[172:175],v[248:251],v[120:123]
s_lshl_b32 s7,s7,2
v_exp_f32 v95,v95
v_mfma_f32_16x16x32_bf16 v[124:127],a[188:191],v[248:251],v[44:47]
s_add_u32 s7,s7,s60
v_cvt_pk_bf16_f32 v90,v92,v93
v_cvt_pk_bf16_f32 v91,v94,v95
v_mfma_f32_16x16x32_bf16 v[124:127],a[184:187],v[240:243],v[124:127]
s_lshl_b32 s7,s7,7
ds_read_b128 a[128:131],v1
v_add_u32 v2,s78,v2
v_mfma_f32_16x16x32_bf16 v[124:127],a[180:183],v[232:235],v[124:127]
s_lshl_b32 s66,s61,5
v_exp_f32 v96,v96
v_mfma_f32_16x16x32_bf16 v[124:127],a[176:179],v[224:227],v[124:127]
s_lshl_b32 s67,s63,3
ds_read_b128 a[132:135],v1 offset:256
v_add_u32 v3,s78,v3
v_mfma_f32_16x16x32_bf16 v[132:135],a[192:195],v[80:83],v[132:135]
s_sub_u32 s78,s77,32768
v_exp_f32 v97,v97
v_mfma_f32_16x16x32_bf16 v[132:135],a[196:199],v[88:91],v[132:135]
s_and_b32 s78,s78,0x1ffff
buffer_load_dwordx4 v25,s[48:51],s83 offen offset:1024 lds
ds_read_b128 a[136:139],v1 offset:512
v_mfma_f32_16x16x32_bf16 v[140:143],a[200:203],v[80:83],v[140:143]
s_sub_u32 s78,s78,s77
v_exp_f32 v98,v98
v_mfma_f32_16x16x32_bf16 v[140:143],a[204:207],v[88:91],v[140:143]
s_add_u32 s77,s77,s78
ds_read_b128 a[140:143],v1 offset:768
v_cvt_pk_bf16_f32 v96,v96,v97
v_mfma_f32_16x16x32_bf16 v[148:151],a[208:211],v[80:83],v[148:151]
s_add_u32 s66,s66,s67
v_exp_f32 v99,v99
v_mfma_f32_16x16x32_bf16 v[148:151],a[212:215],v[88:91],v[148:151]
s_lshl_b32 s66,s66,16
ds_read_b128 a[156:159],v1 offset:4864
v_cvt_pk_bf16_f32 v97,v98,v99
v_mfma_f32_16x16x32_bf16 v[156:159],a[216:219],v[80:83],v[156:159]
s_lshl_b32 s67,s60,14
v_exp_f32 v100,v100
v_mfma_f32_16x16x32_bf16 v[156:159],a[220:223],v[88:91],v[156:159]
s_add_u32 s66,s66,s67
ds_read_b128 a[152:155],v1 offset:4608
v_lshlrev_b32 v4,10,v6
v_mfma_f32_16x16x32_bf16 v[164:167],a[224:227],v[80:83],v[164:167]
s_mov_b32 s30,1523712
v_exp_f32 v101,v101
v_mfma_f32_16x16x32_bf16 v[164:167],a[228:231],v[88:91],v[164:167]
s_mov_b32 s31,0x20000
ds_read_b128 a[148:151],v1 offset:4352
v_cvt_pk_bf16_f32 v98,v100,v101
v_mfma_f32_16x16x32_bf16 v[172:175],a[232:235],v[80:83],v[172:175]
buffer_store_dwordx4 a[116:119],v6,s[40:43],s81 offen offset:1024 sc1
v_mfma_f32_16x16x32_bf16 v[172:175],a[236:239],v[88:91],v[172:175]
ds_read_b128 a[144:147],v1 offset:4096
v_mov_b32 v8,s52
v_mfma_f32_16x16x32_bf16 v[180:183],a[240:243],v[80:83],v[180:183]
buffer_load_dwordx4 v26,s[48:51],s83 offen offset:2048 lds
v_mov_b32 v9,s53
v_mfma_f32_16x16x32_bf16 v[180:183],a[244:247],v[88:91],v[180:183]
ds_read_b128 a[160:163],v1 offset:8192
v_lshlrev_b32 v10,20,v6
v_mfma_f32_16x16x32_bf16 v[188:191],a[248:251],v[80:83],v[188:191]
v_exp_f32 v102,v102
v_mfma_f32_16x16x32_bf16 v[188:191],a[252:255],v[88:91],v[188:191]
s_add_u32 s28,s12,s66
s_addc_u32 s29,s13,0
ds_read_b128 a[164:167],v1 offset:8448
v_mfma_f32_16x16x32_bf16 v[52:55],v[28:31],v[80:83],v[52:55]
s_and_b32 s29,s29,0xffff
v_exp_f32 v103,v103
v_mfma_f32_16x16x32_bf16 v[52:55],v[28:31],v[88:91],v[52:55]
s_waitcnt lgkmcnt(3)
ds_read_b128 a[168:171],v1 offset:8704
v_cvt_pk_bf16_f32 v99,v102,v103
v_mfma_f32_16x16x32_bf16 v[64:67],a[128:131],v[128:131],v[32:35]
v_exp_f32 v104,v104
v_mfma_f32_16x16x32_bf16 v[64:67],a[132:135],v[136:139],v[64:67]
ds_read_b128 a[172:175],v1 offset:8960
v_mfma_f32_16x16x32_bf16 v[64:67],a[136:139],v[144:147],v[64:67]
v_exp_f32 v105,v105
v_mfma_f32_16x16x32_bf16 v[64:67],a[140:143],v[152:155],v[64:67]
buffer_load_dwordx4 v27,s[48:51],s83 offen offset:3072 lds
ds_read_b128 a[188:191],v1 offset:13056
v_mfma_f32_16x16x32_bf16 v[68:71],a[156:159],v[152:155],v[32:35]
v_exp_f32 v106,v106
v_mfma_f32_16x16x32_bf16 v[68:71],a[152:155],v[144:147],v[68:71]
ds_read_b128 a[184:187],v1 offset:12800
v_cvt_pk_bf16_f32 v104,v104,v105
)A"
R"A(v_mfma_f32_16x16x32_bf16 v[68:71],a[148:151],v[136:139],v[68:71]
s_waitcnt lgkmcnt(4)
v_exp_f32 v107,v107
v_mfma_f32_16x16x32_bf16 v[68:71],a[144:147],v[128:131],v[68:71]
ds_read_b128 a[180:183],v1 offset:12544
v_cvt_pk_bf16_f32 v105,v106,v107
v_mfma_f32_16x16x32_bf16 v[72:75],a[160:163],v[128:131],v[32:35]
v_exp_f32 v108,v108
v_mfma_f32_16x16x32_bf16 v[72:75],a[164:167],v[136:139],v[72:75]
s_waitcnt lgkmcnt(3)
buffer_store_dwordx4 a[120:123],v14,s[40:43],s82 offen offset:3072 sc1
v_mfma_f32_16x16x32_bf16 v[72:75],a[168:171],v[144:147],v[72:75]
ds_read_b128 a[176:179],v1 offset:12288
v_add_u32 v1,s78,v1
v_mfma_f32_16x16x32_bf16 v[72:75],a[172:175],v[152:155],v[72:75]
s_add_u32 m0,s86,126976
v_exp_f32 v109,v109
s_waitcnt lgkmcnt(3)
v_mfma_f32_16x16x32_bf16 v[76:79],a[188:191],v[152:155],v[32:35]
s_waitcnt lgkmcnt(2)
v_exp_f32 v110,v110
v_mfma_f32_16x16x32_bf16 v[76:79],a[184:187],v[144:147],v[76:79]
s_waitcnt lgkmcnt(1)
buffer_load_dwordx4 v7,s[48:51],s83 offen lds
v_exp_f32 v111,v111
v_mfma_f32_16x16x32_bf16 v[76:79],a[180:183],v[136:139],v[76:79]
s_add_u32 s83,s64,0
v_cvt_pk_bf16_f32 v106,v108,v109
v_cvt_pk_bf16_f32 v107,v110,v111
s_waitcnt lgkmcnt(0)
v_mfma_f32_16x16x32_bf16 v[76:79],a[176:179],v[128:131],v[76:79]
s_and_b32 s83,s83,3
v_exp_f32 v112,v112
v_mfma_f32_16x16x32_bf16 v[196:199],a[192:195],v[96:99],v[196:199]
s_lshl_b32 s83,s83,1
v_exp_f32 v113,v113
v_mfma_f32_16x16x32_bf16 v[196:199],a[196:199],v[104:107],v[196:199]
s_or_b32 s83,s83,1
v_exp_f32 v114,v114
v_mfma_f32_16x16x32_bf16 v[204:207],a[200:203],v[96:99],v[204:207]
s_mul_i32 s83,s83,163840
v_exp_f32 v115,v115
v_mfma_f32_16x16x32_bf16 v[204:207],a[204:207],v[104:107],v[204:207]
s_add_u32 s83,s83,s85
v_exp_f32 v116,v116
v_mfma_f32_16x16x32_bf16 v[212:215],a[208:211],v[96:99],v[212:215]
v_exp_f32 v117,v117
v_mfma_f32_16x16x32_bf16 v[212:215],a[212:215],v[104:107],v[212:215]
s_add_u32 m0,s86,128000
v_exp_f32 v118,v118
v_mfma_f32_16x16x32_bf16 v[220:223],a[216:219],v[96:99],v[220:223]
v_exp_f32 v119,v119
v_mfma_f32_16x16x32_bf16 v[220:223],a[220:223],v[104:107],v[220:223]
buffer_load_dwordx4 v24,s[48:51],s84 offen lds
v_cvt_pk_bf16_f32 v112,v112,v113
v_mfma_f32_16x16x32_bf16 v[228:231],a[224:227],v[96:99],v[228:231]
v_cvt_pk_bf16_f32 v113,v114,v115
v_cvt_pk_bf16_f32 v114,v116,v117
v_mfma_f32_16x16x32_bf16 v[228:231],a[228:231],v[104:107],v[228:231]
v_exp_f32 v120,v120
v_mfma_f32_16x16x32_bf16 v[236:239],a[232:235],v[96:99],v[236:239]
buffer_store_dwordx4 a[124:127],v6,s[40:43],s82 offen offset:1024 sc1
v_mfma_f32_16x16x32_bf16 v[236:239],a[236:239],v[104:107],v[236:239]
v_exp_f32 v121,v121
v_mfma_f32_16x16x32_bf16 v[244:247],a[240:243],v[96:99],v[244:247]
v_cvt_pk_bf16_f32 v115,v118,v119
v_cvt_pk_bf16_f32 v120,v120,v121
v_mfma_f32_16x16x32_bf16 v[244:247],a[244:247],v[104:107],v[244:247]
v_exp_f32 v122,v122
v_mfma_f32_16x16x32_bf16 v[252:255],a[248:251],v[96:99],v[252:255]
v_exp_f32 v123,v123
v_mfma_f32_16x16x32_bf16 v[252:255],a[252:255],v[104:107],v[252:255]
v_exp_f32 v124,v124
v_mfma_f32_16x16x32_bf16 v[56:59],v[28:31],v[96:99],v[56:59]
v_exp_f32 v125,v125
L_pd12_%=:
s_cbranch_execz L_trt1_%=
v_mfma_f32_16x16x32_bf16 v[56:59],v[28:31],v[104:107],v[56:59]
buffer_load_dwordx4 v25,s[48:51],s84 offen offset:1024 lds
v_cvt_pk_bf16_f32 v121,v122,v123
v_mfma_f32_16x16x32_bf16 v[80:83],a[128:131],v[160:163],v[36:39]
v_exp_f32 v126,v126
v_mfma_f32_16x16x32_bf16 v[80:83],a[132:135],v[168:171],v[80:83]
v_exp_f32 v127,v127
v_mfma_f32_16x16x32_bf16 v[80:83],a[136:139],v[176:179],v[80:83]
v_cvt_pk_bf16_f32 v122,v124,v125
v_cvt_pk_bf16_f32 v123,v126,v127
v_mfma_f32_16x16x32_bf16 v[80:83],a[140:143],v[184:187],v[80:83]
v_exp_f32 v64,v64
v_mfma_f32_16x16x32_bf16 v[84:87],a[156:159],v[184:187],v[36:39]
v_exp_f32 v65,v65
v_mfma_f32_16x16x32_bf16 v[84:87],a[152:155],v[176:179],v[84:87]
v_exp_f32 v66,v66
v_mfma_f32_16x16x32_bf16 v[84:87],a[148:151],v[168:171],v[84:87]
v_exp_f32 v67,v67
v_mfma_f32_16x16x32_bf16 v[84:87],a[144:147],v[160:163],v[84:87]
v_exp_f32 v68,v68
v_mfma_f32_16x16x32_bf16 v[88:91],a[160:163],v[160:163],v[36:39]
v_exp_f32 v69,v69
v_mfma_f32_16x16x32_bf16 v[88:91],a[164:167],v[168:171],v[88:91]
buffer_store_dwordx4 a[96:99],v14,s[40:43],s79 offen offset:3072 sc1
v_mfma_f32_16x16x32_bf16 v[88:91],a[168:171],v[176:179],v[88:91]
v_exp_f32 v70,v70
v_mfma_f32_16x16x32_bf16 v[88:91],a[172:175],v[184:187],v[88:91]
buffer_load_dwordx4 v26,s[48:51],s84 offen offset:2048 lds
v_cvt_pk_bf16_f32 v64,v64,v65
v_mfma_f32_16x16x32_bf16 v[92:95],a[188:191],v[184:187],v[36:39]
v_exp_f32 v71,v71
v_mfma_f32_16x16x32_bf16 v[92:95],a[184:187],v[176:179],v[92:95]
v_cvt_pk_bf16_f32 v65,v66,v67
v_cvt_pk_bf16_f32 v66,v68,v69
v_mfma_f32_16x16x32_bf16 v[92:95],a[180:183],v[168:171],v[92:95]
v_exp_f32 v72,v72
v_mfma_f32_16x16x32_bf16 v[92:95],a[176:179],v[160:163],v[92:95]
v_exp_f32 v73,v73
v_mfma_f32_16x16x32_bf16 a[32:35],a[192:195],v[112:115],a[32:35]
ds_read_b64_tr_b16 a[192:193],v2
ds_read_b64_tr_b16 a[194:195],v2 offset:4096
v_mfma_f32_16x16x32_bf16 a[32:35],a[196:199],v[120:123],a[32:35]
ds_read_b64_tr_b16 a[196:197],v2 offset:8192
v_cvt_pk_bf16_f32 v67,v70,v71
v_mfma_f32_16x16x32_bf16 a[36:39],a[200:203],v[112:115],a[36:39]
ds_read_b64_tr_b16 a[198:199],v2 offset:12288
v_cvt_pk_bf16_f32 v72,v72,v73
v_mfma_f32_16x16x32_bf16 a[36:39],a[204:207],v[120:123],a[36:39]
buffer_load_dwordx4 v27,s[48:51],s84 offen offset:3072 lds
ds_read_b64_tr_b16 a[200:201],v3
v_mfma_f32_16x16x32_bf16 a[40:43],a[208:211],v[112:115],a[40:43]
ds_read_b64_tr_b16 a[202:203],v3 offset:4096
ds_read_b64_tr_b16 a[204:205],v3 offset:8192
v_mfma_f32_16x16x32_bf16 a[40:43],a[212:215],v[120:123],a[40:43]
v_exp_f32 v74,v74
v_mfma_f32_16x16x32_bf16 a[44:47],a[216:219],v[112:115],a[44:47]
ds_read_b64_tr_b16 a[206:207],v3 offset:12288
ds_read_b64_tr_b16 a[208:209],v2 offset:256
v_mfma_f32_16x16x32_bf16 a[44:47],a[220:223],v[120:123],a[44:47]
v_exp_f32 v75,v75
v_mfma_f32_16x16x32_bf16 a[48:51],a[224:227],v[112:115],a[48:51]
ds_read_b64_tr_b16 a[210:211],v2 offset:4352
ds_read_b64_tr_b16 a[212:213],v2 offset:8448
v_mfma_f32_16x16x32_bf16 a[48:51],a[228:231],v[120:123],a[48:51]
ds_read_b64_tr_b16 a[214:215],v2 offset:12544
v_cvt_pk_bf16_f32 v73,v74,v75
v_mfma_f32_16x16x32_bf16 a[52:55],a[232:235],v[112:115],a[52:55]
buffer_store_dwordx4 a[100:103],v6,s[40:43],s79 offen offset:1024 sc1
v_mfma_f32_16x16x32_bf16 a[52:55],a[236:239],v[120:123],a[52:55]
s_add_u32 m0,s86,132096
ds_read_b64_tr_b16 a[216:217],v3 offset:256
ds_read_b64_tr_b16 a[218:219],v3 offset:4352
v_mfma_f32_16x16x32_bf16 a[56:59],a[240:243],v[112:115],a[56:59]
v_exp_f32 v76,v76
v_mfma_f32_16x16x32_bf16 a[56:59],a[244:247],v[120:123],a[56:59]
buffer_load_dwordx4 v7,s[48:51],s84 offen lds
ds_read_b64_tr_b16 a[220:221],v3 offset:8448
v_mfma_f32_16x16x32_bf16 a[60:63],a[248:251],v[112:115],a[60:63]
s_add_u32 s84,s83,20480
v_exp_f32 v77,v77
v_mfma_f32_16x16x32_bf16 a[60:63],a[252:255],v[120:123],a[60:63]
v_exp_f32 v78,v78
v_mfma_f32_16x16x32_bf16 v[60:63],v[28:31],v[112:115],v[60:63]
v_exp_f32 v79,v79
v_mfma_f32_16x16x32_bf16 v[60:63],v[28:31],v[120:123],v[60:63]
ds_read_b64_tr_b16 a[222:223],v3 offset:12544
v_cvt_pk_bf16_f32 v74,v76,v77
v_mfma_f32_16x16x32_bf16 v[96:99],a[128:131],v[192:195],v[40:43]
ds_read_b64_tr_b16 a[224:225],v2 offset:512
v_cvt_pk_bf16_f32 v75,v78,v79
v_mfma_f32_16x16x32_bf16 v[96:99],a[132:135],v[200:203],v[96:99]
ds_read_b64_tr_b16 a[226:227],v2 offset:4608
ds_read_b64_tr_b16 a[228:229],v2 offset:8704
v_mfma_f32_16x16x32_bf16 v[96:99],a[136:139],v[208:211],v[96:99]
v_exp_f32 v80,v80
v_mfma_f32_16x16x32_bf16 v[96:99],a[140:143],v[216:219],v[96:99]
ds_read_b64_tr_b16 a[230:231],v2 offset:12800
ds_read_b64_tr_b16 a[232:233],v3 offset:512
v_mfma_f32_16x16x32_bf16 v[100:103],a[156:159],v[216:219],v[40:43]
v_exp_f32 v81,v81
v_mfma_f32_16x16x32_bf16 v[100:103],a[152:155],v[208:211],v[100:103]
ds_read_b64_tr_b16 a[234:235],v3 offset:4608
ds_read_b64_tr_b16 a[236:237],v3 offset:8704
v_mfma_f32_16x16x32_bf16 v[100:103],a[148:151],v[200:203],v[100:103]
ds_read_b64_tr_b16 a[238:239],v3 offset:12800
v_cvt_pk_bf16_f32 v80,v80,v81
v_mfma_f32_16x16x32_bf16 v[100:103],a[144:147],v[192:195],v[100:103]
v_exp_f32 v82,v82
v_mfma_f32_16x16x32_bf16 v[104:107],a[160:163],v[192:195],v[40:43]
ds_read_b64_tr_b16 a[240:241],v2 offset:768
ds_read_b64_tr_b16 a[242:243],v2 offset:4864
v_mfma_f32_16x16x32_bf16 v[104:107],a[164:167],v[200:203],v[104:107]
buffer_store_dwordx4 a[104:107],v14,s[40:43],s80 offen offset:3072 sc1
v_mfma_f32_16x16x32_bf16 v[104:107],a[168:171],v[208:211],v[104:107]
ds_read_b64_tr_b16 a[244:245],v2 offset:8960
ds_read_b64_tr_b16 a[246:247],v2 offset:13056
v_mfma_f32_16x16x32_bf16 v[104:107],a[172:175],v[216:219],v[104:107]
ds_read_b64_tr_b16 a[248:249],v3 offset:768
v_add_u32 v2,s78,v2
v_mfma_f32_16x16x32_bf16 v[108:111],a[188:191],v[216:219],v[40:43]
v_exp_f32 v83,v83
v_mfma_f32_16x16x32_bf16 v[108:111],a[184:187],v[208:211],v[108:111]
ds_read_b64_tr_b16 a[250:251],v3 offset:4864
ds_read_b64_tr_b16 a[252:253],v3 offset:8960
v_mfma_f32_16x16x32_bf16 v[108:111],a[180:183],v[200:203],v[108:111]
ds_read_b64_tr_b16 a[254:255],v3 offset:13056
v_cvt_pk_bf16_f32 v81,v82,v83
v_mfma_f32_16x16x32_bf16 v[108:111],a[176:179],v[192:195],v[108:111]
s_waitcnt lgkmcnt(8)
v_exp_f32 v84,v84
v_mfma_f32_16x16x32_bf16 a[0:3],a[192:195],v[64:67],a[0:3]
v_exp_f32 v85,v85
v_mfma_f32_16x16x32_bf16 a[0:3],a[196:199],v[72:75],a[0:3]
v_exp_f32 v86,v86
v_mfma_f32_16x16x32_bf16 a[4:7],a[200:203],v[64:67],a[4:7]
v_exp_f32 v87,v87
v_mfma_f32_16x16x32_bf16 a[4:7],a[204:207],v[72:75],a[4:7]
v_cvt_pk_bf16_f32 v82,v84,v85
v_cvt_pk_bf16_f32 v83,v86,v87
v_mfma_f32_16x16x32_bf16 a[8:11],a[208:211],v[64:67],a[8:11]
v_exp_f32 v88,v88
v_mfma_f32_16x16x32_bf16 a[8:11],a[212:215],v[72:75],a[8:11]
v_exp_f32 v89,v89
v_mfma_f32_16x16x32_bf16 a[12:15],a[216:219],v[64:67],a[12:15]
v_exp_f32 v90,v90
v_mfma_f32_16x16x32_bf16 a[12:15],a[220:223],v[72:75],a[12:15]
v_exp_f32 v91,v91
v_mfma_f32_16x16x32_bf16 a[16:19],a[224:227],v[64:67],a[16:19]
v_exp_f32 v92,v92
v_mfma_f32_16x16x32_bf16 a[16:19],a[228:231],v[72:75],a[16:19]
v_exp_f32 v93,v93
v_mfma_f32_16x16x32_bf16 a[20:23],a[232:235],v[64:67],a[20:23]
buffer_store_dwordx4 a[108:111],v6,s[40:43],s80 offen offset:1024 sc1
v_mfma_f32_16x16x32_bf16 a[20:23],a[236:239],v[72:75],a[20:23]
s_waitcnt lgkmcnt(0)
v_exp_f32 v94,v94
v_mfma_f32_16x16x32_bf16 a[24:27],a[240:243],v[64:67],a[24:27]
v_exp_f32 v95,v95
v_mfma_f32_16x16x32_bf16 a[24:27],a[244:247],v[72:75],a[24:27]
v_cvt_pk_bf16_f32 v88,v88,v89
v_cvt_pk_bf16_f32 v89,v90,v91
v_mfma_f32_16x16x32_bf16 a[28:31],a[248:251],v[64:67],a[28:31]
v_cvt_pk_bf16_f32 v90,v92,v93
v_cvt_pk_bf16_f32 v91,v94,v95
v_mfma_f32_16x16x32_bf16 a[28:31],a[252:255],v[72:75],a[28:31]
v_exp_f32 v96,v96
v_mfma_f32_16x16x32_bf16 v[48:51],v[28:31],v[64:67],v[48:51]
v_exp_f32 v97,v97
v_mfma_f32_16x16x32_bf16 v[48:51],v[28:31],v[72:75],v[48:51]
v_exp_f32 v98,v98
s_waitcnt vmcnt(26)
s_barrier
v_mfma_f32_16x16x32_bf16 v[112:115],a[128:131],v[224:227],v[44:47]
ds_read_b128 a[128:131],v1
v_cvt_pk_bf16_f32 v96,v96,v97
v_mfma_f32_16x16x32_bf16 v[112:115],a[132:135],v[232:235],v[112:115]
v_exp_f32 v99,v99
v_mfma_f32_16x16x32_bf16 v[112:115],a[136:139],v[240:243],v[112:115]
ds_read_b128 a[132:135],v1 offset:256
v_cvt_pk_bf16_f32 v97,v98,v99
v_mfma_f32_16x16x32_bf16 v[112:115],a[140:143],v[248:251],v[112:115]
v_exp_f32 v100,v100
v_mfma_f32_16x16x32_bf16 v[116:119],a[156:159],v[248:251],v[44:47]
ds_read_b128 a[136:139],v1 offset:512
v_add_u32 v3,s78,v3
v_mfma_f32_16x16x32_bf16 v[116:119],a[152:155],v[240:243],v[116:119]
s_add_u32 m0,s86,81920
v_exp_f32 v101,v101
v_mfma_f32_16x16x32_bf16 v[116:119],a[148:151],v[232:235],v[116:119]
s_sub_u32 s78,s77,32768
ds_read_b128 a[140:143],v1 offset:768
v_cvt_pk_bf16_f32 v98,v100,v101
v_mfma_f32_16x16x32_bf16 v[116:119],a[144:147],v[224:227],v[116:119]
s_and_b32 s78,s78,0x1ffff
s_sub_u32 s78,s78,s77
buffer_load_dwordx4 v24,s[48:51],s83 offen lds
v_mfma_f32_16x16x32_bf16 v[120:123],a[160:163],v[224:227],v[44:47]
s_add_u32 s77,s77,s78
v_exp_f32 v102,v102
v_mfma_f32_16x16x32_bf16 v[120:123],a[164:167],v[232:235],v[120:123]
)A"
R"A(buffer_store_dwordx4 a[64:67],v6,s[40:43],s79 offen offset:2048 sc1
v_mfma_f32_16x16x32_bf16 v[120:123],a[168:171],v[240:243],v[120:123]
v_exp_f32 v103,v103
v_mfma_f32_16x16x32_bf16 v[120:123],a[172:175],v[248:251],v[120:123]
ds_read_b128 a[156:159],v1 offset:4864
v_cvt_pk_bf16_f32 v99,v102,v103
v_mfma_f32_16x16x32_bf16 v[124:127],a[188:191],v[248:251],v[44:47]
v_exp_f32 v104,v104
v_mfma_f32_16x16x32_bf16 v[124:127],a[184:187],v[240:243],v[124:127]
v_exp_f32 v105,v105
v_mfma_f32_16x16x32_bf16 v[124:127],a[180:183],v[232:235],v[124:127]
ds_read_b128 a[152:155],v1 offset:4608
v_cvt_pk_bf16_f32 v104,v104,v105
v_mfma_f32_16x16x32_bf16 v[124:127],a[176:179],v[224:227],v[124:127]
v_exp_f32 v106,v106
v_mfma_f32_16x16x32_bf16 v[132:135],a[192:195],v[80:83],v[132:135]
v_exp_f32 v107,v107
v_mfma_f32_16x16x32_bf16 v[132:135],a[196:199],v[88:91],v[132:135]
buffer_load_dwordx4 v25,s[48:51],s83 offen offset:1024 lds
ds_read_b128 a[148:151],v1 offset:4352
v_mfma_f32_16x16x32_bf16 v[140:143],a[200:203],v[80:83],v[140:143]
v_exp_f32 v108,v108
v_mfma_f32_16x16x32_bf16 v[140:143],a[204:207],v[88:91],v[140:143]
ds_read_b128 a[144:147],v1 offset:4096
v_cvt_pk_bf16_f32 v105,v106,v107
v_mfma_f32_16x16x32_bf16 v[148:151],a[208:211],v[80:83],v[148:151]
v_exp_f32 v109,v109
v_mfma_f32_16x16x32_bf16 v[148:151],a[212:215],v[88:91],v[148:151]
ds_read_b128 a[160:163],v1 offset:8192
v_cvt_pk_bf16_f32 v106,v108,v109
v_mfma_f32_16x16x32_bf16 v[156:159],a[216:219],v[80:83],v[156:159]
v_exp_f32 v110,v110
v_mfma_f32_16x16x32_bf16 v[156:159],a[220:223],v[88:91],v[156:159]
v_exp_f32 v111,v111
v_mfma_f32_16x16x32_bf16 v[164:167],a[224:227],v[80:83],v[164:167]
ds_read_b128 a[164:167],v1 offset:8448
v_cvt_pk_bf16_f32 v107,v110,v111
v_mfma_f32_16x16x32_bf16 v[164:167],a[228:231],v[88:91],v[164:167]
buffer_store_dwordx4 a[68:71],v6,s[40:43],s79 offen sc1
v_mfma_f32_16x16x32_bf16 v[172:175],a[232:235],v[80:83],v[172:175]
s_add_u32 s79,s79,65536
v_exp_f32 v112,v112
v_mfma_f32_16x16x32_bf16 v[172:175],a[236:239],v[88:91],v[172:175]
buffer_load_dwordx4 v26,s[48:51],s83 offen offset:2048 lds
ds_read_b128 a[168:171],v1 offset:8704
v_mfma_f32_16x16x32_bf16 v[180:183],a[240:243],v[80:83],v[180:183]
v_exp_f32 v113,v113
v_mfma_f32_16x16x32_bf16 v[180:183],a[244:247],v[88:91],v[180:183]
ds_read_b128 a[172:175],v1 offset:8960
v_cvt_pk_bf16_f32 v112,v112,v113
v_mfma_f32_16x16x32_bf16 v[188:191],a[248:251],v[80:83],v[188:191]
v_exp_f32 v114,v114
v_mfma_f32_16x16x32_bf16 v[188:191],a[252:255],v[88:91],v[188:191]
v_exp_f32 v115,v115
v_mfma_f32_16x16x32_bf16 v[52:55],v[28:31],v[80:83],v[52:55]
ds_read_b128 a[188:191],v1 offset:13056
v_cvt_pk_bf16_f32 v113,v114,v115
v_mfma_f32_16x16x32_bf16 v[52:55],v[28:31],v[88:91],v[52:55]
s_waitcnt lgkmcnt(3)
v_exp_f32 v116,v116
v_mfma_f32_16x16x32_bf16 v[64:67],a[128:131],v[128:131],v[32:35]
v_exp_f32 v117,v117
v_mfma_f32_16x16x32_bf16 v[64:67],a[132:135],v[136:139],v[64:67]
ds_read_b128 a[184:187],v1 offset:12800
v_cvt_pk_bf16_f32 v114,v116,v117
v_mfma_f32_16x16x32_bf16 v[64:67],a[136:139],v[144:147],v[64:67]
v_exp_f32 v118,v118
v_mfma_f32_16x16x32_bf16 v[64:67],a[140:143],v[152:155],v[64:67]
buffer_load_dwordx4 v27,s[48:51],s83 offen offset:3072 lds
ds_read_b128 a[180:183],v1 offset:12544
v_mfma_f32_16x16x32_bf16 v[68:71],a[156:159],v[152:155],v[32:35]
v_exp_f32 v119,v119
v_mfma_f32_16x16x32_bf16 v[68:71],a[152:155],v[144:147],v[68:71]
ds_read_b128 a[176:179],v1 offset:12288
v_cvt_pk_bf16_f32 v115,v118,v119
v_mfma_f32_16x16x32_bf16 v[68:71],a[148:151],v[136:139],v[68:71]
v_exp_f32 v120,v120
v_mfma_f32_16x16x32_bf16 v[68:71],a[144:147],v[128:131],v[68:71]
v_exp_f32 v121,v121
v_mfma_f32_16x16x32_bf16 v[72:75],a[160:163],v[128:131],v[32:35]
buffer_store_dwordx4 a[72:75],v6,s[40:43],s80 offen offset:2048 sc1
v_mfma_f32_16x16x32_bf16 v[72:75],a[164:167],v[136:139],v[72:75]
s_waitcnt lgkmcnt(2)
v_exp_f32 v122,v122
v_mfma_f32_16x16x32_bf16 v[72:75],a[168:171],v[144:147],v[72:75]
v_exp_f32 v123,v123
v_mfma_f32_16x16x32_bf16 v[72:75],a[172:175],v[152:155],v[72:75]
s_add_u32 m0,s86,86016
v_exp_f32 v124,v124
v_mfma_f32_16x16x32_bf16 v[76:79],a[188:191],v[152:155],v[32:35]
v_exp_f32 v125,v125
v_mfma_f32_16x16x32_bf16 v[76:79],a[184:187],v[144:147],v[76:79]
s_waitcnt lgkmcnt(0)
buffer_load_dwordx4 v7,s[48:51],s83 offen lds
v_cvt_pk_bf16_f32 v120,v120,v121
v_mfma_f32_16x16x32_bf16 v[76:79],a[180:183],v[136:139],v[76:79]
s_add_u32 s83,s64,1
v_exp_f32 v126,v126
v_mfma_f32_16x16x32_bf16 v[76:79],a[176:179],v[128:131],v[76:79]
s_and_b32 s83,s83,3
v_exp_f32 v127,v127
v_mfma_f32_16x16x32_bf16 v[196:199],a[192:195],v[96:99],v[196:199]
s_lshl_b32 s83,s83,1
v_cvt_pk_bf16_f32 v121,v122,v123
v_cvt_pk_bf16_f32 v122,v124,v125
v_mfma_f32_16x16x32_bf16 v[196:199],a[196:199],v[104:107],v[196:199]
s_mul_i32 s83,s83,163840
s_add_u32 s83,s83,s85
v_cvt_pk_bf16_f32 v123,v126,v127
v_mfma_f32_16x16x32_bf16 v[204:207],a[200:203],v[96:99],v[204:207]
v_exp_f32 v64,v64
v_mfma_f32_16x16x32_bf16 v[204:207],a[204:207],v[104:107],v[204:207]
v_exp_f32 v65,v65
v_mfma_f32_16x16x32_bf16 v[212:215],a[208:211],v[96:99],v[212:215]
v_exp_f32 v66,v66
v_mfma_f32_16x16x32_bf16 v[212:215],a[212:215],v[104:107],v[212:215]
s_add_u32 m0,s86,87040
v_exp_f32 v67,v67
v_mfma_f32_16x16x32_bf16 v[220:223],a[216:219],v[96:99],v[220:223]
v_exp_f32 v68,v68
v_mfma_f32_16x16x32_bf16 v[220:223],a[220:223],v[104:107],v[220:223]
buffer_load_dwordx4 v24,s[48:51],s84 offen lds
v_cvt_pk_bf16_f32 v64,v64,v65
v_mfma_f32_16x16x32_bf16 v[228:231],a[224:227],v[96:99],v[228:231]
v_exp_f32 v69,v69
v_mfma_f32_16x16x32_bf16 v[228:231],a[228:231],v[104:107],v[228:231]
buffer_store_dwordx4 a[76:79],v6,s[40:43],s80 offen sc1
v_mfma_f32_16x16x32_bf16 v[236:239],a[232:235],v[96:99],v[236:239]
s_add_u32 s80,s80,65536
v_exp_f32 v70,v70
v_mfma_f32_16x16x32_bf16 v[236:239],a[236:239],v[104:107],v[236:239]
v_exp_f32 v71,v71
v_mfma_f32_16x16x32_bf16 v[244:247],a[240:243],v[96:99],v[244:247]
v_cvt_pk_bf16_f32 v65,v66,v67
v_cvt_pk_bf16_f32 v66,v68,v69
v_mfma_f32_16x16x32_bf16 v[244:247],a[244:247],v[104:107],v[244:247]
v_cvt_pk_bf16_f32 v67,v70,v71
v_add_u32 v1,s78,v1
v_mfma_f32_16x16x32_bf16 v[252:255],a[248:251],v[96:99],v[252:255]
v_exp_f32 v72,v72
v_mfma_f32_16x16x32_bf16 v[252:255],a[252:255],v[104:107],v[252:255]
v_exp_f32 v73,v73
v_mfma_f32_16x16x32_bf16 v[56:59],v[28:31],v[96:99],v[56:59]
v_exp_f32 v74,v74
v_mfma_f32_16x16x32_bf16 v[56:59],v[28:31],v[104:107],v[56:59]
buffer_load_dwordx4 v25,s[48:51],s84 offen offset:1024 lds
v_cvt_pk_bf16_f32 v72,v72,v73
v_mfma_f32_16x16x32_bf16 v[80:83],a[128:131],v[160:163],v[36:39]
v_exp_f32 v75,v75
v_mfma_f32_16x16x32_bf16 v[80:83],a[132:135],v[168:171],v[80:83]
v_exp_f32 v76,v76
v_mfma_f32_16x16x32_bf16 v[80:83],a[136:139],v[176:179],v[80:83]
v_exp_f32 v77,v77
v_mfma_f32_16x16x32_bf16 v[80:83],a[140:143],v[184:187],v[80:83]
v_exp_f32 v78,v78
v_mfma_f32_16x16x32_bf16 v[84:87],a[156:159],v[184:187],v[36:39]
v_exp_f32 v79,v79
v_mfma_f32_16x16x32_bf16 v[84:87],a[152:155],v[176:179],v[84:87]
v_cvt_pk_bf16_f32 v73,v74,v75
v_cvt_pk_bf16_f32 v74,v76,v77
v_mfma_f32_16x16x32_bf16 v[84:87],a[148:151],v[168:171],v[84:87]
v_cvt_pk_bf16_f32 v75,v78,v79
v_mfma_f32_16x16x32_bf16 v[84:87],a[144:147],v[160:163],v[84:87]
v_exp_f32 v80,v80
v_mfma_f32_16x16x32_bf16 v[88:91],a[160:163],v[160:163],v[36:39]
buffer_store_dwordx4 a[80:83],v6,s[40:43],s81 offen offset:2048 sc1
v_mfma_f32_16x16x32_bf16 v[88:91],a[164:167],v[168:171],v[88:91]
v_exp_f32 v81,v81
v_mfma_f32_16x16x32_bf16 v[88:91],a[168:171],v[176:179],v[88:91]
buffer_load_dwordx4 v26,s[48:51],s84 offen offset:2048 lds
v_cvt_pk_bf16_f32 v80,v80,v81
v_mfma_f32_16x16x32_bf16 v[88:91],a[172:175],v[184:187],v[88:91]
v_exp_f32 v82,v82
v_mfma_f32_16x16x32_bf16 v[92:95],a[188:191],v[184:187],v[36:39]
v_exp_f32 v83,v83
v_mfma_f32_16x16x32_bf16 v[92:95],a[184:187],v[176:179],v[92:95]
v_exp_f32 v84,v84
v_mfma_f32_16x16x32_bf16 v[92:95],a[180:183],v[168:171],v[92:95]
v_exp_f32 v85,v85
v_mfma_f32_16x16x32_bf16 v[92:95],a[176:179],v[160:163],v[92:95]
v_exp_f32 v86,v86
v_mfma_f32_16x16x32_bf16 a[32:35],a[192:195],v[112:115],a[32:35]
ds_read_b64_tr_b16 a[192:193],v2
ds_read_b64_tr_b16 a[194:195],v2 offset:4096
v_mfma_f32_16x16x32_bf16 a[32:35],a[196:199],v[120:123],a[32:35]
ds_read_b64_tr_b16 a[196:197],v2 offset:8192
v_cvt_pk_bf16_f32 v81,v82,v83
v_mfma_f32_16x16x32_bf16 a[36:39],a[200:203],v[112:115],a[36:39]
ds_read_b64_tr_b16 a[198:199],v2 offset:12288
v_cvt_pk_bf16_f32 v82,v84,v85
v_mfma_f32_16x16x32_bf16 a[36:39],a[204:207],v[120:123],a[36:39]
buffer_load_dwordx4 v27,s[48:51],s84 offen offset:3072 lds
ds_read_b64_tr_b16 a[200:201],v3
v_mfma_f32_16x16x32_bf16 a[40:43],a[208:211],v[112:115],a[40:43]
ds_read_b64_tr_b16 a[202:203],v3 offset:4096
ds_read_b64_tr_b16 a[204:205],v3 offset:8192
v_mfma_f32_16x16x32_bf16 a[40:43],a[212:215],v[120:123],a[40:43]
v_exp_f32 v87,v87
v_mfma_f32_16x16x32_bf16 a[44:47],a[216:219],v[112:115],a[44:47]
ds_read_b64_tr_b16 a[206:207],v3 offset:12288
ds_read_b64_tr_b16 a[208:209],v2 offset:256
v_mfma_f32_16x16x32_bf16 a[44:47],a[220:223],v[120:123],a[44:47]
ds_read_b64_tr_b16 a[210:211],v2 offset:4352
v_cvt_pk_bf16_f32 v83,v86,v87
v_mfma_f32_16x16x32_bf16 a[48:51],a[224:227],v[112:115],a[48:51]
v_exp_f32 v88,v88
v_mfma_f32_16x16x32_bf16 a[48:51],a[228:231],v[120:123],a[48:51]
buffer_store_dwordx4 a[84:87],v6,s[40:43],s81 offen sc1
v_mfma_f32_16x16x32_bf16 a[52:55],a[232:235],v[112:115],a[52:55]
s_add_u32 s81,s81,65536
ds_read_b64_tr_b16 a[212:213],v2 offset:8448
ds_read_b64_tr_b16 a[214:215],v2 offset:12544
v_mfma_f32_16x16x32_bf16 a[52:55],a[236:239],v[120:123],a[52:55]
s_add_u32 m0,s86,91136
v_exp_f32 v89,v89
v_mfma_f32_16x16x32_bf16 a[56:59],a[240:243],v[112:115],a[56:59]
ds_read_b64_tr_b16 a[216:217],v3 offset:256
ds_read_b64_tr_b16 a[218:219],v3 offset:4352
v_mfma_f32_16x16x32_bf16 a[56:59],a[244:247],v[120:123],a[56:59]
buffer_load_dwordx4 v7,s[48:51],s84 offen lds
ds_read_b64_tr_b16 a[220:221],v3 offset:8448
v_mfma_f32_16x16x32_bf16 a[60:63],a[248:251],v[112:115],a[60:63]
s_add_u32 s84,s83,20480
ds_read_b64_tr_b16 a[222:223],v3 offset:12544
v_cvt_pk_bf16_f32 v88,v88,v89
v_mfma_f32_16x16x32_bf16 a[60:63],a[252:255],v[120:123],a[60:63]
v_exp_f32 v90,v90
v_mfma_f32_16x16x32_bf16 v[60:63],v[28:31],v[112:115],v[60:63]
ds_read_b64_tr_b16 a[224:225],v2 offset:512
ds_read_b64_tr_b16 a[226:227],v2 offset:4608
v_mfma_f32_16x16x32_bf16 v[60:63],v[28:31],v[120:123],v[60:63]
v_exp_f32 v91,v91
v_mfma_f32_16x16x32_bf16 v[96:99],a[128:131],v[192:195],v[40:43]
ds_read_b64_tr_b16 a[228:229],v2 offset:8704
ds_read_b64_tr_b16 a[230:231],v2 offset:12800
v_mfma_f32_16x16x32_bf16 v[96:99],a[132:135],v[200:203],v[96:99]
ds_read_b64_tr_b16 a[232:233],v3 offset:512
v_cvt_pk_bf16_f32 v89,v90,v91
v_mfma_f32_16x16x32_bf16 v[96:99],a[136:139],v[208:211],v[96:99]
v_exp_f32 v92,v92
v_mfma_f32_16x16x32_bf16 v[96:99],a[140:143],v[216:219],v[96:99]
ds_read_b64_tr_b16 a[234:235],v3 offset:4608
ds_read_b64_tr_b16 a[236:237],v3 offset:8704
v_mfma_f32_16x16x32_bf16 v[100:103],a[156:159],v[216:219],v[40:43]
v_exp_f32 v93,v93
v_mfma_f32_16x16x32_bf16 v[100:103],a[152:155],v[208:211],v[100:103]
ds_read_b64_tr_b16 a[238:239],v3 offset:12800
ds_read_b64_tr_b16 a[240:241],v2 offset:768
v_mfma_f32_16x16x32_bf16 v[100:103],a[148:151],v[200:203],v[100:103]
ds_read_b64_tr_b16 a[242:243],v2 offset:4864
v_cvt_pk_bf16_f32 v90,v92,v93
v_mfma_f32_16x16x32_bf16 v[100:103],a[144:147],v[192:195],v[100:103]
v_exp_f32 v94,v94
v_mfma_f32_16x16x32_bf16 v[104:107],a[160:163],v[192:195],v[40:43]
buffer_store_dwordx4 a[88:91],v6,s[40:43],s82 offen offset:2048 sc1
v_mfma_f32_16x16x32_bf16 v[104:107],a[164:167],v[200:203],v[104:107]
ds_read_b64_tr_b16 a[244:245],v2 offset:8960
ds_read_b64_tr_b16 a[246:247],v2 offset:13056
v_mfma_f32_16x16x32_bf16 v[104:107],a[168:171],v[208:211],v[104:107]
ds_read_b64_tr_b16 a[248:249],v3 offset:768
v_add_u32 v2,s78,v2
v_mfma_f32_16x16x32_bf16 v[104:107],a[172:175],v[216:219],v[104:107]
v_exp_f32 v95,v95
v_mfma_f32_16x16x32_bf16 v[108:111],a[188:191],v[216:219],v[40:43]
ds_read_b64_tr_b16 a[250:251],v3 offset:4864
)A"
R"A(ds_read_b64_tr_b16 a[252:253],v3 offset:8960
v_mfma_f32_16x16x32_bf16 v[108:111],a[184:187],v[208:211],v[108:111]
ds_read_b64_tr_b16 a[254:255],v3 offset:13056
v_cvt_pk_bf16_f32 v91,v94,v95
v_mfma_f32_16x16x32_bf16 v[108:111],a[180:183],v[200:203],v[108:111]
s_cmp_ge_u32 s63,2
s_cselect_b32 s6,L_gbS_%=-L_pcg_%=,s6
s_cselect_b32 s10,L_geS_%=-L_gbS_%=,s10
v_mfma_f32_16x16x32_bf16 v[108:111],a[176:179],v[192:195],v[108:111]
s_add_u32 s8,s8,s6
s_addc_u32 s9,s9,0
s_and_b32 s8,s8,0xffffff80
s_waitcnt lgkmcnt(6)
v_mfma_f32_16x16x32_bf16 a[0:3],a[192:195],v[64:67],a[0:3]
s_and_b32 s9,s9,0xffff
v_exp_f32 v96,v96
v_mfma_f32_16x16x32_bf16 a[0:3],a[196:199],v[72:75],a[0:3]
v_exp_f32 v97,v97
v_mfma_f32_16x16x32_bf16 a[4:7],a[200:203],v[64:67],a[4:7]
v_exp_f32 v98,v98
v_mfma_f32_16x16x32_bf16 a[4:7],a[204:207],v[72:75],a[4:7]
v_exp_f32 v99,v99
v_mfma_f32_16x16x32_bf16 a[8:11],a[208:211],v[64:67],a[8:11]
v_exp_f32 v100,v100
v_mfma_f32_16x16x32_bf16 a[8:11],a[212:215],v[72:75],a[8:11]
v_exp_f32 v101,v101
v_mfma_f32_16x16x32_bf16 a[12:15],a[216:219],v[64:67],a[12:15]
v_exp_f32 v102,v102
v_mfma_f32_16x16x32_bf16 a[12:15],a[220:223],v[72:75],a[12:15]
v_exp_f32 v103,v103
v_mfma_f32_16x16x32_bf16 a[16:19],a[224:227],v[64:67],a[16:19]
v_cvt_pk_bf16_f32 v96,v96,v97
v_cvt_pk_bf16_f32 v97,v98,v99
v_mfma_f32_16x16x32_bf16 a[16:19],a[228:231],v[72:75],a[16:19]
buffer_store_dwordx4 a[92:95],v6,s[40:43],s82 offen sc1
v_mfma_f32_16x16x32_bf16 a[20:23],a[232:235],v[64:67],a[20:23]
s_add_u32 s82,s82,65536
v_cvt_pk_bf16_f32 v98,v100,v101
v_cvt_pk_bf16_f32 v99,v102,v103
v_mfma_f32_16x16x32_bf16 a[20:23],a[236:239],v[72:75],a[20:23]
v_exp_f32 v104,v104
v_mfma_f32_16x16x32_bf16 a[24:27],a[240:243],v[64:67],a[24:27]
s_waitcnt lgkmcnt(0)
v_exp_f32 v105,v105
v_mfma_f32_16x16x32_bf16 a[24:27],a[244:247],v[72:75],a[24:27]
v_exp_f32 v106,v106
v_mfma_f32_16x16x32_bf16 a[28:31],a[248:251],v[64:67],a[28:31]
v_exp_f32 v107,v107
v_mfma_f32_16x16x32_bf16 a[28:31],a[252:255],v[72:75],a[28:31]
v_exp_f32 v108,v108
v_mfma_f32_16x16x32_bf16 v[48:51],v[28:31],v[64:67],v[48:51]
v_exp_f32 v109,v109
v_mfma_f32_16x16x32_bf16 v[48:51],v[28:31],v[72:75],v[48:51]
v_exp_f32 v110,v110
s_waitcnt vmcnt(36)
s_barrier
v_mfma_f32_16x16x32_bf16 v[112:115],a[128:131],v[224:227],v[44:47]
ds_read_b128 a[128:131],v1
v_cvt_pk_bf16_f32 v104,v104,v105
v_mfma_f32_16x16x32_bf16 v[112:115],a[132:135],v[232:235],v[112:115]
v_exp_f32 v111,v111
v_mfma_f32_16x16x32_bf16 v[112:115],a[136:139],v[240:243],v[112:115]
ds_read_b128 a[132:135],v1 offset:256
v_cvt_pk_bf16_f32 v105,v106,v107
v_mfma_f32_16x16x32_bf16 v[112:115],a[140:143],v[248:251],v[112:115]
v_cvt_pk_bf16_f32 v106,v108,v109
v_cvt_pk_bf16_f32 v107,v110,v111
v_mfma_f32_16x16x32_bf16 v[116:119],a[156:159],v[248:251],v[44:47]
ds_read_b128 a[136:139],v1 offset:512
v_add_u32 v3,s78,v3
v_mfma_f32_16x16x32_bf16 v[116:119],a[152:155],v[240:243],v[116:119]
s_add_u32 m0,s86,40960
s_sub_u32 s78,s77,32768
s_and_b32 s78,s78,0x1ffff
v_mfma_f32_16x16x32_bf16 v[116:119],a[148:151],v[232:235],v[116:119]
s_sub_u32 s78,s78,s77
v_exp_f32 v112,v112
v_mfma_f32_16x16x32_bf16 v[116:119],a[144:147],v[224:227],v[116:119]
s_add_u32 s77,s77,s78
buffer_load_dwordx4 v24,s[48:51],s83 offen lds
ds_read_b128 a[140:143],v1 offset:768
v_mfma_f32_16x16x32_bf16 v[120:123],a[160:163],v[224:227],v[44:47]
v_exp_f32 v113,v113
v_mfma_f32_16x16x32_bf16 v[120:123],a[164:167],v[232:235],v[120:123]
ds_read_b128 a[156:159],v1 offset:4864
v_cvt_pk_bf16_f32 v112,v112,v113
v_mfma_f32_16x16x32_bf16 v[120:123],a[168:171],v[240:243],v[120:123]
v_exp_f32 v114,v114
v_mfma_f32_16x16x32_bf16 v[120:123],a[172:175],v[248:251],v[120:123]
v_exp_f32 v115,v115
v_mfma_f32_16x16x32_bf16 v[124:127],a[188:191],v[248:251],v[44:47]
ds_read_b128 a[152:155],v1 offset:4608
v_cvt_pk_bf16_f32 v113,v114,v115
v_mfma_f32_16x16x32_bf16 v[124:127],a[184:187],v[240:243],v[124:127]
v_exp_f32 v116,v116
v_mfma_f32_16x16x32_bf16 v[124:127],a[180:183],v[232:235],v[124:127]
v_exp_f32 v117,v117
v_mfma_f32_16x16x32_bf16 v[124:127],a[176:179],v[224:227],v[124:127]
ds_read_b128 a[148:151],v1 offset:4352
v_cvt_pk_bf16_f32 v114,v116,v117
v_mfma_f32_16x16x32_bf16 v[132:135],a[192:195],v[80:83],v[132:135]
v_exp_f32 v118,v118
v_mfma_f32_16x16x32_bf16 v[132:135],a[196:199],v[88:91],v[132:135]
buffer_load_dwordx4 v25,s[48:51],s83 offen offset:1024 lds
ds_read_b128 a[144:147],v1 offset:4096
v_mfma_f32_16x16x32_bf16 v[140:143],a[200:203],v[80:83],v[140:143]
v_exp_f32 v119,v119
v_mfma_f32_16x16x32_bf16 v[140:143],a[204:207],v[88:91],v[140:143]
ds_read_b128 a[160:163],v1 offset:8192
v_cvt_pk_bf16_f32 v115,v118,v119
v_mfma_f32_16x16x32_bf16 v[148:151],a[208:211],v[80:83],v[148:151]
v_exp_f32 v120,v120
v_mfma_f32_16x16x32_bf16 v[148:151],a[212:215],v[88:91],v[148:151]
v_exp_f32 v121,v121
v_mfma_f32_16x16x32_bf16 v[156:159],a[216:219],v[80:83],v[156:159]
ds_read_b128 a[164:167],v1 offset:8448
v_cvt_pk_bf16_f32 v120,v120,v121
v_mfma_f32_16x16x32_bf16 v[156:159],a[220:223],v[88:91],v[156:159]
v_exp_f32 v122,v122
v_mfma_f32_16x16x32_bf16 v[164:167],a[224:227],v[80:83],v[164:167]
buffer_load_dword v5,v4,s[8:11],s7 offen
ds_read_b128 a[168:171],v1 offset:8704
s_nop 0
L_pd13_%=:
s_cbranch_execz L_trt1_%=
v_mfma_f32_16x16x32_bf16 v[164:167],a[228:231],v[88:91],v[164:167]
v_exp_f32 v123,v123
v_mfma_f32_16x16x32_bf16 v[172:175],a[232:235],v[80:83],v[172:175]
ds_read_b128 a[172:175],v1 offset:8960
v_cvt_pk_bf16_f32 v121,v122,v123
v_mfma_f32_16x16x32_bf16 v[172:175],a[236:239],v[88:91],v[172:175]
v_exp_f32 v124,v124
v_mfma_f32_16x16x32_bf16 v[180:183],a[240:243],v[80:83],v[180:183]
v_exp_f32 v125,v125
v_mfma_f32_16x16x32_bf16 v[180:183],a[244:247],v[88:91],v[180:183]
ds_read_b128 a[188:191],v1 offset:13056
v_cvt_pk_bf16_f32 v122,v124,v125
v_mfma_f32_16x16x32_bf16 v[188:191],a[248:251],v[80:83],v[188:191]
buffer_load_dwordx4 v26,s[48:51],s83 offen offset:2048 lds
v_mfma_f32_16x16x32_bf16 v[188:191],a[252:255],v[88:91],v[188:191]
v_exp_f32 v126,v126
v_mfma_f32_16x16x32_bf16 v[52:55],v[28:31],v[80:83],v[52:55]
v_exp_f32 v127,v127
v_mfma_f32_16x16x32_bf16 v[52:55],v[28:31],v[88:91],v[52:55]
s_waitcnt lgkmcnt(2)
ds_read_b128 a[184:187],v1 offset:12800
v_cvt_pk_bf16_f32 v123,v126,v127
v_mfma_f32_16x16x32_bf16 v[64:67],a[128:131],v[128:131],v[32:35]
v_mfma_f32_16x16x32_bf16 v[64:67],a[132:135],v[136:139],v[64:67]
ds_read_b128 a[180:183],v1 offset:12544
v_mfma_f32_16x16x32_bf16 v[64:67],a[136:139],v[144:147],v[64:67]
v_mfma_f32_16x16x32_bf16 v[64:67],a[140:143],v[152:155],v[64:67]
buffer_load_dwordx4 v27,s[48:51],s83 offen offset:3072 lds
ds_read_b128 a[176:179],v1 offset:12288
v_mfma_f32_16x16x32_bf16 v[68:71],a[156:159],v[152:155],v[32:35]
v_add_u32 v1,s78,v1
v_lshrrev_b32 v1,6,v6
v_mfma_f32_16x16x32_bf16 v[68:71],a[152:155],v[144:147],v[68:71]
v_mul_u32_u24 v1,320,v1
v_mfma_f32_16x16x32_bf16 v[68:71],a[148:151],v[136:139],v[68:71]
v_exp_f32 v64,v64
v_mfma_f32_16x16x32_bf16 v[68:71],a[144:147],v[128:131],v[68:71]
v_exp_f32 v65,v65
v_mfma_f32_16x16x32_bf16 v[72:75],a[160:163],v[128:131],v[32:35]
v_exp_f32 v66,v66
v_mfma_f32_16x16x32_bf16 v[72:75],a[164:167],v[136:139],v[72:75]
v_exp_f32 v67,v67
v_mfma_f32_16x16x32_bf16 v[72:75],a[168:171],v[144:147],v[72:75]
s_waitcnt lgkmcnt(1)
v_cvt_pk_bf16_f32 v64,v64,v65
v_cvt_pk_bf16_f32 v65,v66,v67
v_mfma_f32_16x16x32_bf16 v[72:75],a[172:175],v[152:155],v[72:75]
s_add_u32 m0,s86,45056
v_exp_f32 v68,v68
v_mfma_f32_16x16x32_bf16 v[76:79],a[188:191],v[152:155],v[32:35]
v_exp_f32 v69,v69
v_mfma_f32_16x16x32_bf16 v[76:79],a[184:187],v[144:147],v[76:79]
buffer_load_dwordx4 v7,s[48:51],s83 offen lds
v_cvt_pk_bf16_f32 v66,v68,v69
v_mfma_f32_16x16x32_bf16 v[76:79],a[180:183],v[136:139],v[76:79]
s_add_u32 s83,s64,2
v_exp_f32 v70,v70
s_waitcnt lgkmcnt(0)
v_mfma_f32_16x16x32_bf16 v[76:79],a[176:179],v[128:131],v[76:79]
s_and_b32 s83,s83,3
v_exp_f32 v71,v71
v_mfma_f32_16x16x32_bf16 v[196:199],a[192:195],v[96:99],v[196:199]
s_lshl_b32 s83,s83,1
v_exp_f32 v72,v72
v_mfma_f32_16x16x32_bf16 v[196:199],a[196:199],v[104:107],v[196:199]
s_mul_i32 s83,s83,163840
v_exp_f32 v73,v73
v_mfma_f32_16x16x32_bf16 v[204:207],a[200:203],v[96:99],v[204:207]
s_add_u32 s83,s83,s85
v_cvt_pk_bf16_f32 v67,v70,v71
v_cvt_pk_bf16_f32 v72,v72,v73
v_mfma_f32_16x16x32_bf16 v[204:207],a[204:207],v[104:107],v[204:207]
v_exp_f32 v74,v74
v_mfma_f32_16x16x32_bf16 v[212:215],a[208:211],v[96:99],v[212:215]
v_exp_f32 v75,v75
v_mfma_f32_16x16x32_bf16 v[212:215],a[212:215],v[104:107],v[212:215]
s_add_u32 m0,s86,46080
v_exp_f32 v76,v76
v_mfma_f32_16x16x32_bf16 v[220:223],a[216:219],v[96:99],v[220:223]
v_exp_f32 v77,v77
v_mfma_f32_16x16x32_bf16 v[220:223],a[220:223],v[104:107],v[220:223]
buffer_load_dwordx4 v24,s[48:51],s84 offen lds
v_cvt_pk_bf16_f32 v73,v74,v75
v_mfma_f32_16x16x32_bf16 v[228:231],a[224:227],v[96:99],v[228:231]
v_exp_f32 v78,v78
v_mfma_f32_16x16x32_bf16 v[228:231],a[228:231],v[104:107],v[228:231]
v_exp_f32 v79,v79
v_mfma_f32_16x16x32_bf16 v[236:239],a[232:235],v[96:99],v[236:239]
v_cvt_pk_bf16_f32 v74,v76,v77
v_cvt_pk_bf16_f32 v75,v78,v79
v_mfma_f32_16x16x32_bf16 v[236:239],a[236:239],v[104:107],v[236:239]
v_mfma_f32_16x16x32_bf16 v[244:247],a[240:243],v[96:99],v[244:247]
v_mfma_f32_16x16x32_bf16 v[244:247],a[244:247],v[104:107],v[244:247]
v_mfma_f32_16x16x32_bf16 v[252:255],a[248:251],v[96:99],v[252:255]
v_mfma_f32_16x16x32_bf16 v[252:255],a[252:255],v[104:107],v[252:255]
v_mfma_f32_16x16x32_bf16 v[56:59],v[28:31],v[96:99],v[56:59]
v_mfma_f32_16x16x32_bf16 v[56:59],v[28:31],v[104:107],v[56:59]
buffer_load_dwordx4 v25,s[48:51],s84 offen offset:1024 lds
v_mfma_f32_16x16x32_bf16 v[80:83],a[128:131],v[160:163],v[36:39]
v_mfma_f32_16x16x32_bf16 v[80:83],a[132:135],v[168:171],v[80:83]
v_mfma_f32_16x16x32_bf16 v[80:83],a[136:139],v[176:179],v[80:83]
v_mfma_f32_16x16x32_bf16 v[80:83],a[140:143],v[184:187],v[80:83]
v_mfma_f32_16x16x32_bf16 v[84:87],a[156:159],v[184:187],v[36:39]
v_mfma_f32_16x16x32_bf16 v[84:87],a[152:155],v[176:179],v[84:87]
v_mfma_f32_16x16x32_bf16 v[84:87],a[148:151],v[168:171],v[84:87]
v_mfma_f32_16x16x32_bf16 v[84:87],a[144:147],v[160:163],v[84:87]
v_mfma_f32_16x16x32_bf16 v[88:91],a[160:163],v[160:163],v[36:39]
v_mfma_f32_16x16x32_bf16 v[88:91],a[164:167],v[168:171],v[88:91]
buffer_load_dwordx4 v26,s[48:51],s84 offen offset:2048 lds
v_mfma_f32_16x16x32_bf16 v[88:91],a[168:171],v[176:179],v[88:91]
v_exp_f32 v80,v80
v_mfma_f32_16x16x32_bf16 v[88:91],a[172:175],v[184:187],v[88:91]
v_exp_f32 v81,v81
v_mfma_f32_16x16x32_bf16 v[92:95],a[188:191],v[184:187],v[36:39]
v_exp_f32 v82,v82
v_mfma_f32_16x16x32_bf16 v[92:95],a[184:187],v[176:179],v[92:95]
v_exp_f32 v83,v83
v_mfma_f32_16x16x32_bf16 v[92:95],a[180:183],v[168:171],v[92:95]
v_exp_f32 v84,v84
v_mfma_f32_16x16x32_bf16 v[92:95],a[176:179],v[160:163],v[92:95]
v_exp_f32 v85,v85
v_mfma_f32_16x16x32_bf16 a[32:35],a[192:195],v[112:115],a[32:35]
ds_read_b64_tr_b16 a[192:193],v2
ds_read_b64_tr_b16 a[194:195],v2 offset:4096
v_mfma_f32_16x16x32_bf16 a[32:35],a[196:199],v[120:123],a[32:35]
ds_read_b64_tr_b16 a[196:197],v2 offset:8192
v_cvt_pk_bf16_f32 v80,v80,v81
v_mfma_f32_16x16x32_bf16 a[36:39],a[200:203],v[112:115],a[36:39]
ds_read_b64_tr_b16 a[198:199],v2 offset:12288
v_cvt_pk_bf16_f32 v81,v82,v83
v_mfma_f32_16x16x32_bf16 a[36:39],a[204:207],v[120:123],a[36:39]
buffer_load_dwordx4 v27,s[48:51],s84 offen offset:3072 lds
ds_read_b64_tr_b16 a[200:201],v3
v_mfma_f32_16x16x32_bf16 a[40:43],a[208:211],v[112:115],a[40:43]
ds_read_b64_tr_b16 a[202:203],v3 offset:4096
ds_read_b64_tr_b16 a[204:205],v3 offset:8192
v_mfma_f32_16x16x32_bf16 a[40:43],a[212:215],v[120:123],a[40:43]
ds_read_b64_tr_b16 a[206:207],v3 offset:12288
v_cvt_pk_bf16_f32 v82,v84,v85
v_mfma_f32_16x16x32_bf16 a[44:47],a[216:219],v[112:115],a[44:47]
v_exp_f32 v86,v86
v_mfma_f32_16x16x32_bf16 a[44:47],a[220:223],v[120:123],a[44:47]
ds_read_b64_tr_b16 a[208:209],v2 offset:256
ds_read_b64_tr_b16 a[210:211],v2 offset:4352
v_mfma_f32_16x16x32_bf16 a[48:51],a[224:227],v[112:115],a[48:51]
v_exp_f32 v87,v87
v_mfma_f32_16x16x32_bf16 a[48:51],a[228:231],v[120:123],a[48:51]
ds_read_b64_tr_b16 a[212:213],v2 offset:8448
)A"
R"A(ds_read_b64_tr_b16 a[214:215],v2 offset:12544
v_mfma_f32_16x16x32_bf16 a[52:55],a[232:235],v[112:115],a[52:55]
ds_read_b64_tr_b16 a[216:217],v3 offset:256
v_cvt_pk_bf16_f32 v83,v86,v87
v_mfma_f32_16x16x32_bf16 a[52:55],a[236:239],v[120:123],a[52:55]
s_add_u32 m0,s86,50176
v_exp_f32 v88,v88
v_mfma_f32_16x16x32_bf16 a[56:59],a[240:243],v[112:115],a[56:59]
ds_read_b64_tr_b16 a[218:219],v3 offset:4352
ds_read_b64_tr_b16 a[220:221],v3 offset:8448
v_mfma_f32_16x16x32_bf16 a[56:59],a[244:247],v[120:123],a[56:59]
buffer_load_dwordx4 v7,s[48:51],s84 offen lds
ds_read_b64_tr_b16 a[222:223],v3 offset:12544
v_mfma_f32_16x16x32_bf16 a[60:63],a[248:251],v[112:115],a[60:63]
s_add_u32 s84,s83,20480
v_exp_f32 v89,v89
v_mfma_f32_16x16x32_bf16 a[60:63],a[252:255],v[120:123],a[60:63]
ds_read_b64_tr_b16 a[224:225],v2 offset:512
ds_read_b64_tr_b16 a[226:227],v2 offset:4608
v_mfma_f32_16x16x32_bf16 v[60:63],v[28:31],v[112:115],v[60:63]
ds_read_b64_tr_b16 a[228:229],v2 offset:8704
v_cvt_pk_bf16_f32 v88,v88,v89
v_mfma_f32_16x16x32_bf16 v[60:63],v[28:31],v[120:123],v[60:63]
v_exp_f32 v90,v90
v_mfma_f32_16x16x32_bf16 v[96:99],a[128:131],v[192:195],v[40:43]
ds_read_b64_tr_b16 a[230:231],v2 offset:12800
ds_read_b64_tr_b16 a[232:233],v3 offset:512
v_mfma_f32_16x16x32_bf16 v[96:99],a[132:135],v[200:203],v[96:99]
v_exp_f32 v91,v91
v_mfma_f32_16x16x32_bf16 v[96:99],a[136:139],v[208:211],v[96:99]
ds_read_b64_tr_b16 a[234:235],v3 offset:4608
ds_read_b64_tr_b16 a[236:237],v3 offset:8704
v_mfma_f32_16x16x32_bf16 v[96:99],a[140:143],v[216:219],v[96:99]
ds_read_b64_tr_b16 a[238:239],v3 offset:12800
v_cvt_pk_bf16_f32 v89,v90,v91
v_mfma_f32_16x16x32_bf16 v[100:103],a[156:159],v[216:219],v[40:43]
v_exp_f32 v92,v92
v_mfma_f32_16x16x32_bf16 v[100:103],a[152:155],v[208:211],v[100:103]
ds_read_b64_tr_b16 a[240:241],v2 offset:768
ds_read_b64_tr_b16 a[242:243],v2 offset:4864
v_mfma_f32_16x16x32_bf16 v[100:103],a[148:151],v[200:203],v[100:103]
v_exp_f32 v93,v93
v_mfma_f32_16x16x32_bf16 v[100:103],a[144:147],v[192:195],v[100:103]
ds_read_b64_tr_b16 a[244:245],v2 offset:8960
ds_read_b64_tr_b16 a[246:247],v2 offset:13056
v_mfma_f32_16x16x32_bf16 v[104:107],a[160:163],v[192:195],v[40:43]
ds_read_b64_tr_b16 a[248:249],v3 offset:768
v_cvt_pk_bf16_f32 v90,v92,v93
v_mfma_f32_16x16x32_bf16 v[104:107],a[164:167],v[200:203],v[104:107]
ds_read_b64_tr_b16 a[250:251],v3 offset:4864
v_bfe_u32 v2,v6,4,2
v_mfma_f32_16x16x32_bf16 v[104:107],a[168:171],v[208:211],v[104:107]
ds_read_b64_tr_b16 a[252:253],v3 offset:8960
v_lshl_add_u32 v1,v2,3,v1
v_mfma_f32_16x16x32_bf16 v[104:107],a[172:175],v[216:219],v[104:107]
ds_read_b64_tr_b16 a[254:255],v3 offset:13056
v_add_u32 v1,122880,v1
v_mfma_f32_16x16x32_bf16 v[108:111],a[188:191],v[216:219],v[40:43]
v_exp_f32 v94,v94
v_mfma_f32_16x16x32_bf16 v[108:111],a[184:187],v[208:211],v[108:111]
v_exp_f32 v95,v95
v_mfma_f32_16x16x32_bf16 v[108:111],a[180:183],v[200:203],v[108:111]
v_cvt_pk_bf16_f32 v91,v94,v95
v_bfe_u32 v2,v6,8,1
v_mfma_f32_16x16x32_bf16 v[108:111],a[176:179],v[192:195],v[108:111]
s_waitcnt lgkmcnt(4)
v_exp_f32 v96,v96
v_mfma_f32_16x16x32_bf16 a[0:3],a[192:195],v[64:67],a[0:3]
v_exp_f32 v97,v97
v_mfma_f32_16x16x32_bf16 a[0:3],a[196:199],v[72:75],a[0:3]
v_exp_f32 v98,v98
v_mfma_f32_16x16x32_bf16 a[4:7],a[200:203],v[64:67],a[4:7]
v_exp_f32 v99,v99
v_mfma_f32_16x16x32_bf16 a[4:7],a[204:207],v[72:75],a[4:7]
v_exp_f32 v100,v100
v_mfma_f32_16x16x32_bf16 a[8:11],a[208:211],v[64:67],a[8:11]
v_exp_f32 v101,v101
v_mfma_f32_16x16x32_bf16 a[8:11],a[212:215],v[72:75],a[8:11]
v_exp_f32 v102,v102
v_mfma_f32_16x16x32_bf16 a[12:15],a[216:219],v[64:67],a[12:15]
v_exp_f32 v103,v103
v_mfma_f32_16x16x32_bf16 a[12:15],a[220:223],v[72:75],a[12:15]
v_cvt_pk_bf16_f32 v96,v96,v97
v_cvt_pk_bf16_f32 v97,v98,v99
v_mfma_f32_16x16x32_bf16 a[16:19],a[224:227],v[64:67],a[16:19]
v_cvt_pk_bf16_f32 v98,v100,v101
v_cvt_pk_bf16_f32 v99,v102,v103
v_mfma_f32_16x16x32_bf16 a[16:19],a[228:231],v[72:75],a[16:19]
v_exp_f32 v104,v104
v_mfma_f32_16x16x32_bf16 a[20:23],a[232:235],v[64:67],a[20:23]
v_exp_f32 v105,v105
v_mfma_f32_16x16x32_bf16 a[20:23],a[236:239],v[72:75],a[20:23]
v_exp_f32 v106,v106
v_mfma_f32_16x16x32_bf16 a[24:27],a[240:243],v[64:67],a[24:27]
v_exp_f32 v107,v107
v_mfma_f32_16x16x32_bf16 a[24:27],a[244:247],v[72:75],a[24:27]
s_waitcnt lgkmcnt(0)
v_exp_f32 v108,v108
v_mfma_f32_16x16x32_bf16 a[28:31],a[248:251],v[64:67],a[28:31]
v_exp_f32 v109,v109
v_mfma_f32_16x16x32_bf16 a[28:31],a[252:255],v[72:75],a[28:31]
v_exp_f32 v110,v110
v_mfma_f32_16x16x32_bf16 v[48:51],v[28:31],v[64:67],v[48:51]
v_exp_f32 v111,v111
v_mfma_f32_16x16x32_bf16 v[48:51],v[28:31],v[72:75],v[48:51]
v_cvt_pk_bf16_f32 v104,v104,v105
v_cvt_pk_bf16_f32 v105,v106,v107
s_waitcnt vmcnt(11)
s_barrier
v_mfma_f32_16x16x32_bf16 v[112:115],a[128:131],v[224:227],v[44:47]
buffer_store_dwordx2 v[8:9],v10,s[44:47],s18 offen offset:256 sc1
v_cvt_pk_bf16_f32 v106,v108,v109
v_mfma_f32_16x16x32_bf16 v[112:115],a[132:135],v[232:235],v[112:115]
v_cvt_pk_bf16_f32 v107,v110,v111
v_lshl_add_u32 v1,v2,5,v1
v_mfma_f32_16x16x32_bf16 v[112:115],a[136:139],v[240:243],v[112:115]
v_cmp_nlt_f32 s[54:55],v48,s73
v_max_f32 v49,0x00800000,v48
v_mfma_f32_16x16x32_bf16 v[112:115],a[140:143],v[248:251],v[112:115]
s_add_u32 m0,s86,0
v_rcp_f32 v49,v49
v_mfma_f32_16x16x32_bf16 v[116:119],a[156:159],v[248:251],v[44:47]
v_lshlrev_b32 v2,6,v2
v_sub_u32 v2,v1,v2
v_mfma_f32_16x16x32_bf16 v[116:119],a[152:155],v[240:243],v[116:119]
v_accvgpr_read_b32 v8,a0
v_accvgpr_read_b32 v9,a1
v_mfma_f32_16x16x32_bf16 v[116:119],a[148:151],v[232:235],v[116:119]
buffer_load_dwordx4 v24,s[48:51],s83 offen lds
v_accvgpr_read_b32 v10,a2
v_mfma_f32_16x16x32_bf16 v[116:119],a[144:147],v[224:227],v[116:119]
v_exp_f32 v112,v112
v_mfma_f32_16x16x32_bf16 v[120:123],a[160:163],v[224:227],v[44:47]
v_exp_f32 v113,v113
v_mfma_f32_16x16x32_bf16 v[120:123],a[164:167],v[232:235],v[120:123]
v_exp_f32 v114,v114
v_mfma_f32_16x16x32_bf16 v[120:123],a[168:171],v[240:243],v[120:123]
v_exp_f32 v115,v115
v_mfma_f32_16x16x32_bf16 v[120:123],a[172:175],v[248:251],v[120:123]
v_exp_f32 v116,v116
v_mfma_f32_16x16x32_bf16 v[124:127],a[188:191],v[248:251],v[44:47]
buffer_load_dwordx4 v25,s[48:51],s83 offen offset:1024 lds
v_cvt_pk_bf16_f32 v112,v112,v113
v_mfma_f32_16x16x32_bf16 v[124:127],a[184:187],v[240:243],v[124:127]
v_exp_f32 v117,v117
v_mfma_f32_16x16x32_bf16 v[124:127],a[180:183],v[232:235],v[124:127]
v_exp_f32 v118,v118
v_mfma_f32_16x16x32_bf16 v[124:127],a[176:179],v[224:227],v[124:127]
v_exp_f32 v119,v119
v_mfma_f32_16x16x32_bf16 v[52:55],v[28:31],v[80:83],v[52:55]
v_cvt_pk_bf16_f32 v113,v114,v115
v_cvt_pk_bf16_f32 v114,v116,v117
v_mfma_f32_16x16x32_bf16 v[52:55],v[28:31],v[88:91],v[52:55]
v_cvt_pk_bf16_f32 v115,v118,v119
v_accvgpr_read_b32 v11,a3
v_mfma_f32_16x16x32_bf16 v[132:135],a[192:195],v[80:83],v[132:135]
buffer_load_dwordx4 v26,s[48:51],s83 offen offset:2048 lds
v_accvgpr_read_b32 v12,a4
v_mfma_f32_16x16x32_bf16 v[132:135],a[196:199],v[88:91],v[132:135]
v_exp_f32 v120,v120
v_mfma_f32_16x16x32_bf16 v[140:143],a[200:203],v[80:83],v[140:143]
v_exp_f32 v121,v121
v_mfma_f32_16x16x32_bf16 v[140:143],a[204:207],v[88:91],v[140:143]
v_exp_f32 v122,v122
v_mfma_f32_16x16x32_bf16 v[148:151],a[208:211],v[80:83],v[148:151]
v_exp_f32 v123,v123
v_mfma_f32_16x16x32_bf16 v[148:151],a[212:215],v[88:91],v[148:151]
v_exp_f32 v124,v124
v_mfma_f32_16x16x32_bf16 v[156:159],a[216:219],v[80:83],v[156:159]
buffer_load_dwordx4 v27,s[48:51],s83 offen offset:3072 lds
v_cvt_pk_bf16_f32 v120,v120,v121
v_mfma_f32_16x16x32_bf16 v[156:159],a[220:223],v[88:91],v[156:159]
v_exp_f32 v125,v125
v_mfma_f32_16x16x32_bf16 v[164:167],a[224:227],v[80:83],v[164:167]
v_exp_f32 v126,v126
v_mfma_f32_16x16x32_bf16 v[164:167],a[228:231],v[88:91],v[164:167]
s_add_u32 m0,s86,4096
v_exp_f32 v127,v127
v_mfma_f32_16x16x32_bf16 v[172:175],a[232:235],v[80:83],v[172:175]
v_cvt_pk_bf16_f32 v121,v122,v123
v_cvt_pk_bf16_f32 v122,v124,v125
v_mfma_f32_16x16x32_bf16 v[172:175],a[236:239],v[88:91],v[172:175]
v_cvt_pk_bf16_f32 v123,v126,v127
v_cmp_nlt_f32 s[56:57],v52,s73
v_mfma_f32_16x16x32_bf16 v[180:183],a[240:243],v[80:83],v[180:183]
s_or_b64 s[54:55],s[54:55],s[56:57]
buffer_load_dwordx4 v7,s[48:51],s83 offen lds
v_accvgpr_read_b32 v13,a5
v_mfma_f32_16x16x32_bf16 v[180:183],a[244:247],v[88:91],v[180:183]
v_accvgpr_read_b32 v14,a6
v_accvgpr_read_b32 v15,a7
v_mfma_f32_16x16x32_bf16 v[188:191],a[248:251],v[80:83],v[188:191]
v_mul_f32 v8,v8,v49
v_mul_f32 v9,v9,v49
v_mfma_f32_16x16x32_bf16 v[188:191],a[252:255],v[88:91],v[188:191]
s_add_u32 m0,s86,5120
v_mul_f32 v10,v10,v49
v_mul_f32 v11,v11,v49
v_mfma_f32_16x16x32_bf16 v[56:59],v[28:31],v[96:99],v[56:59]
v_mul_f32 v12,v12,v49
v_mul_f32 v13,v13,v49
v_mfma_f32_16x16x32_bf16 v[56:59],v[28:31],v[104:107],v[56:59]
v_mul_f32 v14,v14,v49
v_mul_f32 v15,v15,v49
v_mfma_f32_16x16x32_bf16 v[196:199],a[192:195],v[96:99],v[196:199]
buffer_load_dwordx4 v24,s[48:51],s84 offen lds
v_cvt_pk_bf16_f32 v32,v8,v9
v_mfma_f32_16x16x32_bf16 v[196:199],a[196:199],v[104:107],v[196:199]
v_cvt_pk_bf16_f32 v33,v10,v11
v_cvt_pk_bf16_f32 v34,v12,v13
v_mfma_f32_16x16x32_bf16 v[204:207],a[200:203],v[96:99],v[204:207]
v_cmp_nlt_f32 s[56:57],v56,s73
v_cvt_pk_bf16_f32 v35,v14,v15
v_mfma_f32_16x16x32_bf16 v[204:207],a[204:207],v[104:107],v[204:207]
s_or_b64 s[54:55],s[54:55],s[56:57]
v_accvgpr_read_b32 v16,a8
v_accvgpr_read_b32 v17,a9
v_mfma_f32_16x16x32_bf16 v[212:215],a[208:211],v[96:99],v[212:215]
v_accvgpr_read_b32 v18,a10
v_accvgpr_read_b32 v19,a11
v_mfma_f32_16x16x32_bf16 v[212:215],a[212:215],v[104:107],v[212:215]
v_accvgpr_read_b32 v20,a12
v_accvgpr_read_b32 v21,a13
v_mfma_f32_16x16x32_bf16 v[220:223],a[216:219],v[96:99],v[220:223]
buffer_load_dwordx4 v25,s[48:51],s84 offen offset:1024 lds
v_accvgpr_read_b32 v22,a14
v_mfma_f32_16x16x32_bf16 v[220:223],a[220:223],v[104:107],v[220:223]
v_accvgpr_read_b32 v23,a15
v_mul_f32 v16,v16,v49
v_mfma_f32_16x16x32_bf16 v[228:231],a[224:227],v[96:99],v[228:231]
v_mul_f32 v17,v17,v49
v_mul_f32 v18,v18,v49
v_mfma_f32_16x16x32_bf16 v[228:231],a[228:231],v[104:107],v[228:231]
v_mul_f32 v19,v19,v49
v_mul_f32 v20,v20,v49
v_mfma_f32_16x16x32_bf16 v[236:239],a[232:235],v[96:99],v[236:239]
v_mul_f32 v21,v21,v49
v_mul_f32 v22,v22,v49
v_mfma_f32_16x16x32_bf16 v[236:239],a[236:239],v[104:107],v[236:239]
v_mul_f32 v23,v23,v49
v_cvt_pk_bf16_f32 v36,v16,v17
v_mfma_f32_16x16x32_bf16 v[244:247],a[240:243],v[96:99],v[244:247]
buffer_load_dwordx4 v26,s[48:51],s84 offen offset:2048 lds
v_cvt_pk_bf16_f32 v37,v18,v19
v_mfma_f32_16x16x32_bf16 v[244:247],a[244:247],v[104:107],v[244:247]
v_cvt_pk_bf16_f32 v38,v20,v21
v_cvt_pk_bf16_f32 v39,v22,v23
v_mfma_f32_16x16x32_bf16 v[252:255],a[248:251],v[96:99],v[252:255]
v_accvgpr_read_b32 v8,a16
v_accvgpr_read_b32 v9,a17
v_mfma_f32_16x16x32_bf16 v[252:255],a[252:255],v[104:107],v[252:255]
v_accvgpr_read_b32 v10,a18
v_accvgpr_read_b32 v11,a19
v_mfma_f32_16x16x32_bf16 v[60:63],v[28:31],v[112:115],v[60:63]
v_accvgpr_read_b32 v12,a20
v_accvgpr_read_b32 v13,a21
v_mfma_f32_16x16x32_bf16 v[60:63],v[28:31],v[120:123],v[60:63]
v_accvgpr_read_b32 v14,a22
v_accvgpr_read_b32 v15,a23
v_mfma_f32_16x16x32_bf16 a[32:35],a[192:195],v[112:115],a[32:35]
buffer_load_dwordx4 v27,s[48:51],s84 offen offset:3072 lds
v_mul_f32 v8,v8,v49
v_mfma_f32_16x16x32_bf16 a[32:35],a[196:199],v[120:123],a[32:35]
v_mul_f32 v9,v9,v49
v_mul_f32 v10,v10,v49
v_mfma_f32_16x16x32_bf16 a[36:39],a[200:203],v[112:115],a[36:39]
v_cmp_nlt_f32 s[56:57],v60,s73
v_mul_f32 v11,v11,v49
v_mfma_f32_16x16x32_bf16 a[36:39],a[204:207],v[120:123],a[36:39]
s_add_u32 m0,s86,9216
v_mul_f32 v12,v12,v49
v_mul_f32 v13,v13,v49
v_mfma_f32_16x16x32_bf16 a[40:43],a[208:211],v[112:115],a[40:43]
s_or_b64 s[54:55],s[54:55],s[56:57]
v_mul_f32 v14,v14,v49
v_mul_f32 v15,v15,v49
v_mfma_f32_16x16x32_bf16 a[40:43],a[212:215],v[120:123],a[40:43]
v_cvt_pk_bf16_f32 v40,v8,v9
v_cvt_pk_bf16_f32 v41,v10,v11
v_mfma_f32_16x16x32_bf16 a[44:47],a[216:219],v[112:115],a[44:47]
buffer_load_dwordx4 v7,s[48:51],s84 offen lds
v_cvt_pk_bf16_f32 v42,v12,v13
v_mfma_f32_16x16x32_bf16 a[44:47],a[220:223],v[120:123],a[44:47]
)A"
R"A(ds_read_b64_tr_b16 v[64:65],v1
v_cvt_pk_bf16_f32 v43,v14,v15
v_mfma_f32_16x16x32_bf16 a[48:51],a[224:227],v[112:115],a[48:51]
ds_read_b64_tr_b16 v[66:67],v1 offset:5120
ds_read_b64_tr_b16 v[68:69],v1 offset:10240
v_mfma_f32_16x16x32_bf16 a[48:51],a[228:231],v[120:123],a[48:51]
ds_read_b64_tr_b16 v[70:71],v1 offset:15360
v_max_f32 v53,0x00800000,v52
v_mfma_f32_16x16x32_bf16 a[52:55],a[232:235],v[112:115],a[52:55]
ds_read_b64_tr_b16 v[72:73],v1 offset:20480
v_max_f32 v57,0x00800000,v56
v_mfma_f32_16x16x32_bf16 a[52:55],a[236:239],v[120:123],a[52:55]
ds_read_b64_tr_b16 v[74:75],v1 offset:25600
v_max_f32 v61,0x00800000,v60
v_mfma_f32_16x16x32_bf16 a[56:59],a[240:243],v[112:115],a[56:59]
ds_read_b64_tr_b16 v[76:77],v1 offset:30720
ds_read_b64_tr_b16 v[78:79],v1 offset:35840
v_mfma_f32_16x16x32_bf16 a[56:59],a[244:247],v[120:123],a[56:59]
v_rcp_f32 v53,v53
v_mfma_f32_16x16x32_bf16 a[60:63],a[248:251],v[112:115],a[60:63]
ds_read_b64_tr_b16 v[80:81],v2 offset:32
ds_read_b64_tr_b16 v[82:83],v2 offset:5152
v_mfma_f32_16x16x32_bf16 a[60:63],a[252:255],v[120:123],a[60:63]
ds_read_b64_tr_b16 v[84:85],v2 offset:10272
v_rcp_f32 v57,v57
v_rcp_f32 v61,v61
ds_read_b64_tr_b16 v[86:87],v2 offset:15392
ds_read_b64_tr_b16 v[88:89],v2 offset:20512
ds_read_b64_tr_b16 v[90:91],v2 offset:25632
ds_read_b64_tr_b16 v[92:93],v2 offset:30752
ds_read_b64_tr_b16 v[94:95],v2 offset:35872
s_cmp_lg_u64 s[54:55],0
s_cbranch_scc1 L_slow1_%=
L_ok1_%=:
s_cmp_ge_u32 s63,2
s_cbranch_scc1 L_GS_%=
L_gbF_%=:
s_waitcnt lgkmcnt(14)
v_mfma_f32_16x16x32_bf16 a[192:195],v[64:67],a[68:71],0
s_lshl_b32 s91,s63,8
buffer_store_dwordx4 v[32:35],v6,s[40:43],s79 offen sc1
s_waitcnt lgkmcnt(12)
v_mfma_f32_16x16x32_bf16 a[192:195],v[68:71],a[76:79],a[192:195]
s_add_u32 s87,s64,1
v_bfe_u32 v3,v6,4,2
v_min_u32 v3,2,v3
s_waitcnt lgkmcnt(10)
v_mfma_f32_16x16x32_bf16 a[192:195],v[72:75],a[84:87],a[192:195]
s_and_b32 s87,s87,3
v_add3_u32 v3,v3,s64,1
v_and_b32 v3,3,v3
s_waitcnt lgkmcnt(8)
v_mfma_f32_16x16x32_bf16 a[192:195],v[76:79],a[92:95],a[192:195]
s_lshl_b32 s87,s87,1
ds_read_b64_tr_b16 v[96:97],v1 offset:64
v_lshlrev_b32 v3,6,v3
v_mfma_f32_16x16x32_bf16 a[196:199],v[76:79],a[124:127],0
s_lshl_b32 s87,s87,16
ds_read_b64_tr_b16 v[98:99],v1 offset:5184
ds_read_b64_tr_b16 v[100:101],v1 offset:10304
v_mfma_f32_16x16x32_bf16 a[196:199],v[72:75],a[116:119],a[196:199]
s_add_u32 s88,s87,4096
ds_read_b64_tr_b16 v[102:103],v1 offset:15424
v_lshlrev_b32 v29,10,v6
v_mfma_f32_16x16x32_bf16 a[196:199],v[68:71],a[108:111],a[196:199]
s_add_u32 s89,s87,8192
ds_read_b64_tr_b16 v[104:105],v1 offset:20544
v_accvgpr_read_b32 v8,a24
v_mfma_f32_16x16x32_bf16 a[196:199],v[64:67],a[100:103],a[196:199]
s_add_u32 s90,s87,12288
ds_read_b64_tr_b16 v[106:107],v1 offset:25664
v_accvgpr_read_b32 v9,a25
v_mfma_f32_16x16x32_bf16 a[200:203],v[64:67],a[64:67],0
s_add_u32 m0,s86,122880
ds_read_b64_tr_b16 v[108:109],v1 offset:30784
ds_read_b64_tr_b16 v[110:111],v1 offset:35904
v_mfma_f32_16x16x32_bf16 a[200:203],v[68:71],a[72:75],a[200:203]
s_add_u32 s83,s64,3
ds_read_b64_tr_b16 v[112:113],v2 offset:96
v_accvgpr_read_b32 v10,a26
v_mfma_f32_16x16x32_bf16 a[200:203],v[72:75],a[80:83],a[200:203]
s_and_b32 s83,s83,3
ds_read_b64_tr_b16 v[114:115],v2 offset:5216
v_accvgpr_read_b32 v11,a27
v_mfma_f32_16x16x32_bf16 a[200:203],v[76:79],a[88:91],a[200:203]
s_lshl_b32 s83,s83,1
ds_read_b64_tr_b16 v[116:117],v2 offset:10336
v_accvgpr_read_b32 v12,a28
v_mfma_f32_16x16x32_bf16 a[204:207],v[76:79],a[120:123],0
s_mul_i32 s83,s83,163840
ds_read_b64_tr_b16 v[118:119],v2 offset:15456
ds_read_b64_tr_b16 v[120:121],v2 offset:20576
v_mfma_f32_16x16x32_bf16 a[204:207],v[72:75],a[112:115],a[204:207]
s_add_u32 s83,s83,s85
ds_read_b64_tr_b16 v[122:123],v2 offset:25696
v_accvgpr_read_b32 v13,a29
v_mfma_f32_16x16x32_bf16 a[204:207],v[68:71],a[104:107],a[204:207]
s_add_u32 s84,s83,20480
ds_read_b64_tr_b16 v[124:125],v2 offset:30816
v_accvgpr_read_b32 v14,a30
v_mfma_f32_16x16x32_bf16 a[204:207],v[64:67],a[96:99],a[204:207]
s_lshl_b32 s66,s61,2
buffer_store_dwordx4 v[36:39],v6,s[40:43],s80 offen sc1
s_waitcnt lgkmcnt(9)
v_mfma_f32_16x16x32_bf16 a[208:211],v[80:83],a[68:71],0
s_add_u32 s66,s66,s64
ds_read_b64_tr_b16 v[126:127],v2 offset:35936
ds_read_b64_tr_b16 v[64:65],v1 offset:128
v_mfma_f32_16x16x32_bf16 a[208:211],v[84:87],a[76:79],a[208:211]
s_mul_i32 s66,s66,262144
ds_read_b64_tr_b16 v[66:67],v1 offset:5248
v_accvgpr_read_b32 v15,a31
v_mfma_f32_16x16x32_bf16 a[208:211],v[88:91],a[84:87],a[208:211]
s_add_u32 s66,s66,134217728
ds_read_b64_tr_b16 v[68:69],v1 offset:10368
v_mul_f32 v8,v8,v49
v_mfma_f32_16x16x32_bf16 a[208:211],v[92:95],a[92:95],a[208:211]
s_mov_b32 s38,262144
ds_read_b64_tr_b16 v[70:71],v1 offset:15488
v_mul_f32 v9,v9,v49
v_mfma_f32_16x16x32_bf16 a[212:215],v[92:95],a[124:127],0
s_mov_b32 s39,0x20000
s_waitcnt vmcnt(21)
buffer_load_dwordx2 v[4:5],v3,s[44:47],s91 offen offset:256 sc1
ds_read_b64_tr_b16 v[72:73],v1 offset:20608
v_mfma_f32_16x16x32_bf16 a[212:215],v[88:91],a[116:119],a[212:215]
s_add_u32 s74,s63,1
ds_read_b64_tr_b16 v[74:75],v1 offset:25728
ds_read_b64_tr_b16 v[76:77],v1 offset:30848
v_mfma_f32_16x16x32_bf16 a[212:215],v[84:87],a[108:111],a[212:215]
s_and_b32 s74,s74,3
ds_read_b64_tr_b16 v[78:79],v1 offset:35968
v_mul_f32 v10,v10,v49
v_mfma_f32_16x16x32_bf16 a[212:215],v[80:83],a[100:103],a[212:215]
s_lshl_b32 s74,s74,2
v_mul_f32 v11,v11,v49
v_mul_f32 v12,v12,v49
v_mfma_f32_16x16x32_bf16 a[216:219],v[80:83],a[64:67],0
s_add_u32 s74,s74,s60
v_mul_f32 v13,v13,v49
v_mul_f32 v14,v14,v49
v_mfma_f32_16x16x32_bf16 a[216:219],v[84:87],a[72:75],a[216:219]
s_mul_i32 s74,s74,16384
v_mul_f32 v15,v15,v49
v_cvt_pk_bf16_f32 v44,v8,v9
v_mfma_f32_16x16x32_bf16 a[216:219],v[88:91],a[80:83],a[216:219]
s_add_u32 s74,s74,8192
v_cvt_pk_bf16_f32 v45,v10,v11
v_cvt_pk_bf16_f32 v46,v12,v13
v_mfma_f32_16x16x32_bf16 a[216:219],v[92:95],a[88:91],a[216:219]
s_add_u32 s75,s63,2
v_cvt_pk_bf16_f32 v47,v14,v15
v_mul_f32 v132,v132,v53
v_mfma_f32_16x16x32_bf16 a[220:223],v[92:95],a[120:123],0
s_and_b32 s75,s75,3
ds_read_b64_tr_b16 v[92:93],v2 offset:30880
ds_read_b64_tr_b16 v[94:95],v2 offset:36000
v_mfma_f32_16x16x32_bf16 a[220:223],v[88:91],a[112:115],a[220:223]
s_lshl_b32 s75,s75,2
ds_read_b64_tr_b16 v[88:89],v2 offset:20640
v_mul_f32 v133,v133,v53
s_nop 0
L_pd14_%=:
s_cbranch_execz L_trt1_%=
v_mfma_f32_16x16x32_bf16 a[220:223],v[84:87],a[104:107],a[220:223]
s_add_u32 s75,s75,s60
buffer_store_dwordx4 v[40:43],v6,s[40:43],s81 offen sc1
v_mfma_f32_16x16x32_bf16 a[220:223],v[80:83],a[96:99],a[220:223]
s_mul_i32 s75,s75,16384
ds_read_b64_tr_b16 v[80:81],v2 offset:160
ds_read_b64_tr_b16 v[82:83],v2 offset:5280
v_mfma_f32_16x16x32_bf16 a[224:227],v[96:99],a[68:71],0
s_add_u32 s75,s75,4096
ds_read_b64_tr_b16 v[84:85],v2 offset:10400
v_mul_f32 v134,v134,v53
v_mfma_f32_16x16x32_bf16 a[224:227],v[100:103],a[76:79],a[224:227]
s_add_u32 s76,s63,3
ds_read_b64_tr_b16 v[86:87],v2 offset:15520
v_mul_f32 v135,v135,v53
v_mfma_f32_16x16x32_bf16 a[224:227],v[104:107],a[84:87],a[224:227]
s_and_b32 s76,s76,3
ds_read_b64_tr_b16 v[90:91],v2 offset:25760
v_mul_f32 v140,v140,v53
s_waitcnt lgkmcnt(8)
v_mfma_f32_16x16x32_bf16 a[224:227],v[108:111],a[92:95],a[224:227]
s_lshl_b32 s76,s76,2
v_mul_f32 v141,v141,v53
v_mul_f32 v142,v142,v53
v_mfma_f32_16x16x32_bf16 a[228:231],v[108:111],a[124:127],0
s_add_u32 s76,s76,s60
v_mul_f32 v143,v143,v53
v_cvt_pk_bf16_f32 v132,v132,v133
v_mfma_f32_16x16x32_bf16 a[228:231],v[104:107],a[116:119],a[228:231]
s_mul_i32 s76,s76,16384
v_cvt_pk_bf16_f32 v133,v134,v135
v_cvt_pk_bf16_f32 v134,v140,v141
v_mfma_f32_16x16x32_bf16 a[228:231],v[100:103],a[108:111],a[228:231]
s_add_u32 s76,s76,0
v_cvt_pk_bf16_f32 v135,v142,v143
v_mul_f32 v148,v148,v53
v_mfma_f32_16x16x32_bf16 a[228:231],v[96:99],a[100:103],a[228:231]
s_lshl_b32 s67,s63,10
v_mul_f32 v149,v149,v53
v_mul_f32 v150,v150,v53
v_mfma_f32_16x16x32_bf16 a[232:235],v[96:99],a[64:67],0
s_mov_b32 s26,307200
v_mul_f32 v151,v151,v53
v_mul_f32 v156,v156,v53
v_mfma_f32_16x16x32_bf16 a[232:235],v[100:103],a[72:75],a[232:235]
s_mov_b32 s27,0x20000
v_mul_f32 v157,v157,v53
v_mul_f32 v158,v158,v53
v_mfma_f32_16x16x32_bf16 a[232:235],v[104:107],a[80:83],a[232:235]
s_lshl_b32 s77,s63,2
v_mul_f32 v159,v159,v53
v_cvt_pk_bf16_f32 v148,v148,v149
v_mfma_f32_16x16x32_bf16 a[232:235],v[108:111],a[88:91],a[232:235]
s_add_u32 s77,s77,s60
v_cvt_pk_bf16_f32 v149,v150,v151
v_cvt_pk_bf16_f32 v150,v156,v157
v_mfma_f32_16x16x32_bf16 a[236:239],v[108:111],a[120:123],0
s_mul_i32 s77,s77,16384
ds_read_b64_tr_b16 v[108:109],v1 offset:30912
ds_read_b64_tr_b16 v[110:111],v1 offset:36032
v_mfma_f32_16x16x32_bf16 a[236:239],v[104:107],a[112:115],a[236:239]
s_add_u32 s65,s77,0
buffer_store_dwordx4 v[44:47],v6,s[40:43],s82 offen sc1
v_mfma_f32_16x16x32_bf16 a[236:239],v[100:103],a[104:107],a[236:239]
s_add_u32 s68,s77,12288
ds_read_b64_tr_b16 v[100:101],v1 offset:10432
ds_read_b64_tr_b16 v[102:103],v1 offset:15552
v_mfma_f32_16x16x32_bf16 a[236:239],v[96:99],a[96:99],a[236:239]
ds_read_b64_tr_b16 v[96:97],v1 offset:192
v_cvt_pk_bf16_f32 v151,v158,v159
v_mfma_f32_16x16x32_bf16 a[240:243],v[112:115],a[68:71],0
ds_read_b64_tr_b16 v[98:99],v1 offset:5312
v_mul_f32 v164,v164,v53
v_mfma_f32_16x16x32_bf16 a[240:243],v[116:119],a[76:79],a[240:243]
ds_read_b64_tr_b16 v[104:105],v1 offset:20672
v_mul_f32 v165,v165,v53
v_mfma_f32_16x16x32_bf16 a[240:243],v[120:123],a[84:87],a[240:243]
ds_read_b64_tr_b16 v[106:107],v1 offset:25792
v_mul_f32 v166,v166,v53
v_mfma_f32_16x16x32_bf16 a[240:243],v[124:127],a[92:95],a[240:243]
v_mul_f32 v167,v167,v53
v_mul_f32 v172,v172,v53
v_mfma_f32_16x16x32_bf16 a[244:247],v[124:127],a[124:127],0
v_mul_f32 v173,v173,v53
v_mul_f32 v174,v174,v53
v_mfma_f32_16x16x32_bf16 a[244:247],v[120:123],a[116:119],a[244:247]
v_mul_f32 v175,v175,v53
v_cvt_pk_bf16_f32 v164,v164,v165
v_mfma_f32_16x16x32_bf16 a[244:247],v[116:119],a[108:111],a[244:247]
v_cvt_pk_bf16_f32 v165,v166,v167
v_cvt_pk_bf16_f32 v166,v172,v173
v_mfma_f32_16x16x32_bf16 a[244:247],v[112:115],a[100:103],a[244:247]
v_cvt_pk_bf16_f32 v167,v174,v175
v_mul_f32 v180,v180,v53
v_mfma_f32_16x16x32_bf16 a[248:251],v[112:115],a[64:67],0
v_mul_f32 v181,v181,v53
v_mul_f32 v182,v182,v53
v_mfma_f32_16x16x32_bf16 a[248:251],v[116:119],a[72:75],a[248:251]
v_mul_f32 v183,v183,v53
v_mul_f32 v188,v188,v53
v_mfma_f32_16x16x32_bf16 a[248:251],v[120:123],a[80:83],a[248:251]
v_mul_f32 v189,v189,v53
v_mul_f32 v190,v190,v53
v_mfma_f32_16x16x32_bf16 a[248:251],v[124:127],a[88:91],a[248:251]
v_mul_f32 v191,v191,v53
v_cvt_pk_bf16_f32 v180,v180,v181
v_mfma_f32_16x16x32_bf16 a[252:255],v[124:127],a[120:123],0
buffer_load_dword v30,v29,s[8:11],s7 offen
ds_read_b64_tr_b16 v[124:125],v2 offset:30944
v_mfma_f32_16x16x32_bf16 a[252:255],v[120:123],a[112:115],a[252:255]
ds_read_b64_tr_b16 v[120:121],v2 offset:20704
ds_read_b64_tr_b16 v[122:123],v2 offset:25824
v_mfma_f32_16x16x32_bf16 a[252:255],v[116:119],a[104:107],a[252:255]
buffer_store_dwordx4 v[132:135],v6,s[40:43],s79 offen offset:1024 sc1
v_mfma_f32_16x16x32_bf16 a[252:255],v[112:115],a[96:99],a[252:255]
ds_read_b64_tr_b16 v[112:113],v2 offset:224
ds_read_b64_tr_b16 v[114:115],v2 offset:5344
v_mfma_f32_16x16x32_bf16 a[0:3],v[64:67],a[68:71],0
ds_read_b64_tr_b16 v[116:117],v2 offset:10464
v_cvt_pk_bf16_f32 v181,v182,v183
v_mfma_f32_16x16x32_bf16 a[0:3],v[68:71],a[76:79],a[0:3]
ds_read_b64_tr_b16 v[118:119],v2 offset:15584
v_cvt_pk_bf16_f32 v182,v188,v189
v_mfma_f32_16x16x32_bf16 a[0:3],v[72:75],a[84:87],a[0:3]
ds_read_b64_tr_b16 v[126:127],v2 offset:36064
v_cvt_pk_bf16_f32 v183,v190,v191
v_mfma_f32_16x16x32_bf16 a[0:3],v[76:79],a[92:95],a[0:3]
v_mul_f32 v196,v196,v57
v_mul_f32 v197,v197,v57
v_mfma_f32_16x16x32_bf16 a[4:7],v[76:79],a[124:127],0
v_mul_f32 v198,v198,v57
v_mul_f32 v199,v199,v57
v_mfma_f32_16x16x32_bf16 a[4:7],v[72:75],a[116:119],a[4:7]
v_mul_f32 v204,v204,v57
v_mul_f32 v205,v205,v57
v_mfma_f32_16x16x32_bf16 a[4:7],v[68:71],a[108:111],a[4:7]
v_mul_f32 v206,v206,v57
v_mul_f32 v207,v207,v57
v_mfma_f32_16x16x32_bf16 a[4:7],v[64:67],a[100:103],a[4:7]
v_cvt_pk_bf16_f32 v196,v196,v197
v_cvt_pk_bf16_f32 v197,v198,v199
)A"
R"A(v_mfma_f32_16x16x32_bf16 a[8:11],v[64:67],a[64:67],0
v_cvt_pk_bf16_f32 v198,v204,v205
v_cvt_pk_bf16_f32 v199,v206,v207
v_mfma_f32_16x16x32_bf16 a[8:11],v[68:71],a[72:75],a[8:11]
v_mul_f32 v212,v212,v57
v_mul_f32 v213,v213,v57
v_mfma_f32_16x16x32_bf16 a[8:11],v[72:75],a[80:83],a[8:11]
v_mul_f32 v214,v214,v57
v_mul_f32 v215,v215,v57
v_mfma_f32_16x16x32_bf16 a[8:11],v[76:79],a[88:91],a[8:11]
v_mul_f32 v220,v220,v57
v_mul_f32 v221,v221,v57
v_mfma_f32_16x16x32_bf16 a[12:15],v[76:79],a[120:123],0
ds_read_b64_tr_b16 v[76:77],v1 offset:30976
ds_read_b64_tr_b16 v[78:79],v1 offset:36096
v_mfma_f32_16x16x32_bf16 a[12:15],v[72:75],a[112:115],a[12:15]
buffer_store_dwordx4 v[148:151],v6,s[40:43],s80 offen offset:1024 sc1
v_mfma_f32_16x16x32_bf16 a[12:15],v[68:71],a[104:107],a[12:15]
ds_read_b64_tr_b16 v[68:69],v1 offset:10496
ds_read_b64_tr_b16 v[70:71],v1 offset:15616
v_mfma_f32_16x16x32_bf16 a[12:15],v[64:67],a[96:99],a[12:15]
s_waitcnt lgkmcnt(4)
ds_read_b64_tr_b16 v[64:65],v1 offset:256
v_mul_f32 v222,v222,v57
v_mfma_f32_16x16x32_bf16 a[16:19],v[80:83],a[68:71],0
ds_read_b64_tr_b16 v[66:67],v1 offset:5376
v_mul_f32 v223,v223,v57
v_mfma_f32_16x16x32_bf16 a[16:19],v[84:87],a[76:79],a[16:19]
ds_read_b64_tr_b16 v[72:73],v1 offset:20736
v_cvt_pk_bf16_f32 v212,v212,v213
v_mfma_f32_16x16x32_bf16 a[16:19],v[88:91],a[84:87],a[16:19]
ds_read_b64_tr_b16 v[74:75],v1 offset:25856
v_add_u32 v1,0xffff6000,v1
v_mfma_f32_16x16x32_bf16 a[16:19],v[92:95],a[92:95],a[16:19]
v_cvt_pk_bf16_f32 v213,v214,v215
v_cvt_pk_bf16_f32 v214,v220,v221
v_mfma_f32_16x16x32_bf16 a[20:23],v[92:95],a[124:127],0
v_cvt_pk_bf16_f32 v215,v222,v223
v_mul_f32 v228,v228,v57
v_mfma_f32_16x16x32_bf16 a[20:23],v[88:91],a[116:119],a[20:23]
v_mul_f32 v229,v229,v57
v_mul_f32 v230,v230,v57
v_mfma_f32_16x16x32_bf16 a[20:23],v[84:87],a[108:111],a[20:23]
v_mul_f32 v231,v231,v57
v_mul_f32 v236,v236,v57
v_mfma_f32_16x16x32_bf16 a[20:23],v[80:83],a[100:103],a[20:23]
v_mul_f32 v237,v237,v57
v_mul_f32 v238,v238,v57
v_mfma_f32_16x16x32_bf16 a[24:27],v[80:83],a[64:67],0
v_mul_f32 v239,v239,v57
v_cvt_pk_bf16_f32 v228,v228,v229
v_mfma_f32_16x16x32_bf16 a[24:27],v[84:87],a[72:75],a[24:27]
v_cvt_pk_bf16_f32 v229,v230,v231
v_cvt_pk_bf16_f32 v230,v236,v237
v_mfma_f32_16x16x32_bf16 a[24:27],v[88:91],a[80:83],a[24:27]
s_waitcnt vmcnt(5)
v_cmp_ne_u32 s[56:57],s52,v4
v_cmp_ne_u32 s[58:59],s53,v5
v_mfma_f32_16x16x32_bf16 a[24:27],v[92:95],a[88:91],a[24:27]
s_or_b64 s[56:57],s[56:57],s[58:59]
s_cbranch_scc1 L_fr1_%=
L_fo1_%=:
v_cvt_pk_bf16_f32 v231,v238,v239
v_mfma_f32_16x16x32_bf16 a[28:31],v[92:95],a[120:123],0
s_lshl_b32 s91,s63,8
buffer_store_dwordx4 v[164:167],v6,s[40:43],s81 offen offset:1024 sc1
v_mfma_f32_16x16x32_bf16 a[28:31],v[88:91],a[112:115],a[28:31]
ds_read_b64_tr_b16 v[88:89],v2 offset:20768
ds_read_b64_tr_b16 v[90:91],v2 offset:25888
v_mfma_f32_16x16x32_bf16 a[28:31],v[84:87],a[104:107],a[28:31]
buffer_load_dwordx4 a[128:131],v6,s[28:31],s87 offen sc1
ds_read_b64_tr_b16 v[84:85],v2 offset:10528
v_mfma_f32_16x16x32_bf16 a[28:31],v[80:83],a[96:99],a[28:31]
ds_read_b64_tr_b16 v[80:81],v2 offset:288
v_mul_f32 v244,v244,v57
v_mfma_f32_16x16x32_bf16 v[128:131],v[96:99],a[68:71],0
ds_read_b64_tr_b16 v[82:83],v2 offset:5408
v_mul_f32 v245,v245,v57
v_mfma_f32_16x16x32_bf16 v[128:131],v[100:103],a[76:79],v[128:131]
ds_read_b64_tr_b16 v[86:87],v2 offset:15648
ds_read_b64_tr_b16 v[92:93],v2 offset:31008
v_mfma_f32_16x16x32_bf16 v[128:131],v[104:107],a[84:87],v[128:131]
ds_read_b64_tr_b16 v[94:95],v2 offset:36128
v_add_u32 v2,0xffff6000,v2
v_mfma_f32_16x16x32_bf16 v[128:131],v[108:111],a[92:95],v[128:131]
v_mul_f32 v246,v246,v57
v_mul_f32 v247,v247,v57
v_mfma_f32_16x16x32_bf16 v[136:139],v[108:111],a[124:127],0
v_mul_f32 v252,v252,v57
v_mul_f32 v253,v253,v57
v_mfma_f32_16x16x32_bf16 v[136:139],v[104:107],a[116:119],v[136:139]
v_mul_f32 v254,v254,v57
v_mul_f32 v255,v255,v57
v_mfma_f32_16x16x32_bf16 v[136:139],v[100:103],a[108:111],v[136:139]
v_cvt_pk_bf16_f32 v244,v244,v245
v_cvt_pk_bf16_f32 v245,v246,v247
v_mfma_f32_16x16x32_bf16 v[136:139],v[96:99],a[100:103],v[136:139]
v_cvt_pk_bf16_f32 v246,v252,v253
v_cvt_pk_bf16_f32 v247,v254,v255
v_mfma_f32_16x16x32_bf16 v[144:147],v[96:99],a[64:67],0
v_accvgpr_read_b32 v8,a40
v_accvgpr_read_b32 v9,a41
v_mfma_f32_16x16x32_bf16 v[144:147],v[100:103],a[72:75],v[144:147]
buffer_load_dwordx4 a[144:147],v6,s[28:31],s88 offen sc1
v_accvgpr_read_b32 v10,a42
v_mfma_f32_16x16x32_bf16 v[144:147],v[104:107],a[80:83],v[144:147]
v_accvgpr_read_b32 v11,a43
v_accvgpr_read_b32 v12,a44
v_mfma_f32_16x16x32_bf16 v[144:147],v[108:111],a[88:91],v[144:147]
buffer_store_dwordx4 v[180:183],v6,s[40:43],s82 offen offset:1024 sc1
v_mfma_f32_16x16x32_bf16 v[152:155],v[108:111],a[120:123],0
ds_read_b64_tr_b16 v[108:109],v1 offset:30720
ds_read_b64_tr_b16 v[110:111],v1 offset:35840
v_mfma_f32_16x16x32_bf16 v[152:155],v[104:107],a[112:115],v[152:155]
ds_read_b64_tr_b16 v[104:105],v1 offset:20480
v_accvgpr_read_b32 v13,a45
v_mfma_f32_16x16x32_bf16 v[152:155],v[100:103],a[104:107],v[152:155]
ds_read_b64_tr_b16 v[100:101],v1 offset:10240
v_accvgpr_read_b32 v14,a46
v_mfma_f32_16x16x32_bf16 v[152:155],v[96:99],a[96:99],v[152:155]
ds_read_b64_tr_b16 v[96:97],v1
v_accvgpr_read_b32 v15,a47
v_mfma_f32_16x16x32_bf16 v[160:163],v[112:115],a[68:71],0
ds_read_b64_tr_b16 v[98:99],v1 offset:5120
ds_read_b64_tr_b16 v[102:103],v1 offset:15360
v_mfma_f32_16x16x32_bf16 v[160:163],v[116:119],a[76:79],v[160:163]
ds_read_b64_tr_b16 v[106:107],v1 offset:25600
v_mul_f32 v8,v8,v61
v_mfma_f32_16x16x32_bf16 v[160:163],v[120:123],a[84:87],v[160:163]
v_mul_f32 v9,v9,v61
v_mul_f32 v10,v10,v61
v_mfma_f32_16x16x32_bf16 v[160:163],v[124:127],a[92:95],v[160:163]
v_mul_f32 v11,v11,v61
v_mul_f32 v12,v12,v61
v_mfma_f32_16x16x32_bf16 v[168:171],v[124:127],a[124:127],0
v_mul_f32 v13,v13,v61
v_mul_f32 v14,v14,v61
v_mfma_f32_16x16x32_bf16 v[168:171],v[120:123],a[116:119],v[168:171]
v_mul_f32 v15,v15,v61
v_cvt_pk_bf16_f32 v156,v8,v9
v_mfma_f32_16x16x32_bf16 v[168:171],v[116:119],a[108:111],v[168:171]
buffer_load_dwordx4 a[160:163],v6,s[28:31],s89 offen sc1
v_cvt_pk_bf16_f32 v157,v10,v11
v_mfma_f32_16x16x32_bf16 v[168:171],v[112:115],a[100:103],v[168:171]
v_cvt_pk_bf16_f32 v158,v12,v13
v_cvt_pk_bf16_f32 v159,v14,v15
v_mfma_f32_16x16x32_bf16 v[176:179],v[112:115],a[64:67],0
v_accvgpr_read_b32 v8,a56
v_accvgpr_read_b32 v9,a57
v_mfma_f32_16x16x32_bf16 v[176:179],v[116:119],a[72:75],v[176:179]
v_accvgpr_read_b32 v10,a58
v_accvgpr_read_b32 v11,a59
v_mfma_f32_16x16x32_bf16 v[176:179],v[120:123],a[80:83],v[176:179]
buffer_store_dwordx4 v[196:199],v6,s[40:43],s79 offen offset:2048 sc1
v_mfma_f32_16x16x32_bf16 v[176:179],v[124:127],a[88:91],v[176:179]
v_accvgpr_read_b32 v12,a60
v_accvgpr_read_b32 v13,a61
v_mfma_f32_16x16x32_bf16 v[184:187],v[124:127],a[120:123],0
v_accvgpr_read_b32 v14,a62
v_accvgpr_read_b32 v15,a63
v_mfma_f32_16x16x32_bf16 v[184:187],v[120:123],a[112:115],v[184:187]
ds_read_b64_tr_b16 v[120:121],v2 offset:20512
ds_read_b64_tr_b16 v[122:123],v2 offset:25632
v_mfma_f32_16x16x32_bf16 v[184:187],v[116:119],a[104:107],v[184:187]
ds_read_b64_tr_b16 v[116:117],v2 offset:10272
v_mul_f32 v8,v8,v61
v_mfma_f32_16x16x32_bf16 v[184:187],v[112:115],a[96:99],v[184:187]
s_waitcnt lgkmcnt(3)
ds_read_b64_tr_b16 v[112:113],v2 offset:32
v_mul_f32 v9,v9,v61
v_mfma_f32_16x16x32_bf16 v[192:195],v[64:67],a[68:71],0
ds_read_b64_tr_b16 v[114:115],v2 offset:5152
v_mul_f32 v10,v10,v61
v_mfma_f32_16x16x32_bf16 v[192:195],v[68:71],a[76:79],v[192:195]
ds_read_b64_tr_b16 v[118:119],v2 offset:15392
v_mul_f32 v11,v11,v61
v_mfma_f32_16x16x32_bf16 v[192:195],v[72:75],a[84:87],v[192:195]
v_mul_f32 v12,v12,v61
v_mul_f32 v13,v13,v61
v_mfma_f32_16x16x32_bf16 v[192:195],v[76:79],a[92:95],v[192:195]
buffer_load_dwordx4 a[176:179],v6,s[28:31],s90 offen sc1
v_mul_f32 v14,v14,v61
v_mfma_f32_16x16x32_bf16 v[200:203],v[76:79],a[124:127],0
v_mul_f32 v15,v15,v61
v_cvt_pk_bf16_f32 v188,v8,v9
v_mfma_f32_16x16x32_bf16 v[200:203],v[72:75],a[116:119],v[200:203]
v_cvt_pk_bf16_f32 v189,v10,v11
v_cvt_pk_bf16_f32 v190,v12,v13
v_mfma_f32_16x16x32_bf16 v[200:203],v[68:71],a[108:111],v[200:203]
ds_read_b64_tr_b16 v[124:125],v2 offset:30752
v_cvt_pk_bf16_f32 v191,v14,v15
v_mfma_f32_16x16x32_bf16 v[200:203],v[64:67],a[100:103],v[200:203]
ds_read_b64_tr_b16 v[126:127],v2 offset:35872
v_accvgpr_read_b32 v16,a32
v_mfma_f32_16x16x32_bf16 v[208:211],v[64:67],a[64:67],0
v_accvgpr_read_b32 v17,a33
v_accvgpr_read_b32 v18,a34
v_mfma_f32_16x16x32_bf16 v[208:211],v[68:71],a[72:75],v[208:211]
buffer_store_dwordx4 v[212:215],v6,s[40:43],s80 offen offset:2048 sc1
v_mfma_f32_16x16x32_bf16 v[208:211],v[72:75],a[80:83],v[208:211]
v_accvgpr_read_b32 v19,a35
v_accvgpr_read_b32 v20,a36
v_mfma_f32_16x16x32_bf16 v[208:211],v[76:79],a[88:91],v[208:211]
v_accvgpr_read_b32 v21,a37
v_accvgpr_read_b32 v22,a38
v_mfma_f32_16x16x32_bf16 v[216:219],v[76:79],a[120:123],0
v_accvgpr_read_b32 v23,a39
v_mul_f32 v16,v16,v61
v_mfma_f32_16x16x32_bf16 v[216:219],v[72:75],a[112:115],v[216:219]
v_mul_f32 v17,v17,v61
v_mul_f32 v18,v18,v61
v_mfma_f32_16x16x32_bf16 v[216:219],v[68:71],a[104:107],v[216:219]
v_mul_f32 v19,v19,v61
v_mul_f32 v20,v20,v61
v_mfma_f32_16x16x32_bf16 v[216:219],v[64:67],a[96:99],v[216:219]
v_mul_f32 v21,v21,v61
v_mul_f32 v22,v22,v61
v_mfma_f32_16x16x32_bf16 v[224:227],v[80:83],a[68:71],0
buffer_load_dwordx4 a[180:183],v6,s[28:31],s90 offen offset:1024 sc1
v_mul_f32 v23,v23,v61
v_mfma_f32_16x16x32_bf16 v[224:227],v[84:87],a[76:79],v[224:227]
v_cvt_pk_bf16_f32 v140,v16,v17
v_cvt_pk_bf16_f32 v141,v18,v19
v_mfma_f32_16x16x32_bf16 v[224:227],v[88:91],a[84:87],v[224:227]
v_cvt_pk_bf16_f32 v142,v20,v21
v_cvt_pk_bf16_f32 v143,v22,v23
v_mfma_f32_16x16x32_bf16 v[224:227],v[92:95],a[92:95],v[224:227]
v_accvgpr_read_b32 v16,a48
v_accvgpr_read_b32 v17,a49
v_mfma_f32_16x16x32_bf16 v[232:235],v[92:95],a[124:127],0
v_accvgpr_read_b32 v18,a50
v_accvgpr_read_b32 v19,a51
v_mfma_f32_16x16x32_bf16 v[232:235],v[88:91],a[116:119],v[232:235]
v_accvgpr_read_b32 v20,a52
v_accvgpr_read_b32 v21,a53
v_mfma_f32_16x16x32_bf16 v[232:235],v[84:87],a[108:111],v[232:235]
v_accvgpr_read_b32 v22,a54
v_accvgpr_read_b32 v23,a55
v_mfma_f32_16x16x32_bf16 v[232:235],v[80:83],a[100:103],v[232:235]
v_mul_f32 v16,v16,v61
v_mul_f32 v17,v17,v61
v_mfma_f32_16x16x32_bf16 v[240:243],v[80:83],a[64:67],0
buffer_store_dwordx4 v[228:231],v6,s[40:43],s81 offen offset:2048 sc1
v_mfma_f32_16x16x32_bf16 v[240:243],v[84:87],a[72:75],v[240:243]
v_mul_f32 v18,v18,v61
v_mul_f32 v19,v19,v61
v_mfma_f32_16x16x32_bf16 v[240:243],v[88:91],a[80:83],v[240:243]
v_mul_f32 v20,v20,v61
v_mul_f32 v21,v21,v61
v_mfma_f32_16x16x32_bf16 v[240:243],v[92:95],a[88:91],v[240:243]
v_mul_f32 v22,v22,v61
v_mul_f32 v23,v23,v61
v_mfma_f32_16x16x32_bf16 v[248:251],v[92:95],a[120:123],0
v_cvt_pk_bf16_f32 v172,v16,v17
v_cvt_pk_bf16_f32 v173,v18,v19
v_mfma_f32_16x16x32_bf16 v[248:251],v[88:91],a[112:115],v[248:251]
buffer_load_dwordx4 a[164:167],v6,s[28:31],s89 offen offset:1024 sc1
v_cvt_pk_bf16_f32 v174,v20,v21
v_mfma_f32_16x16x32_bf16 v[248:251],v[84:87],a[104:107],v[248:251]
ds_read_b64_tr_b16 v[64:65],v1 offset:64
v_cvt_pk_bf16_f32 v175,v22,v23
v_mfma_f32_16x16x32_bf16 v[248:251],v[80:83],a[96:99],v[248:251]
ds_read_b64_tr_b16 v[66:67],v1 offset:5184
ds_read_b64_tr_b16 v[68:69],v1 offset:10304
v_mfma_f32_16x16x32_bf16 a[192:195],v[96:99],v[32:35],a[192:195]
s_cmp_eq_u32 s60,3
s_cselect_b32 s69,0x40000000,0
ds_read_b64_tr_b16 v[70:71],v1 offset:15424
v_mfma_f32_16x16x32_bf16 a[192:195],v[100:103],v[36:39],a[192:195]
ds_read_b64_tr_b16 v[72:73],v1 offset:20544
v_add_u32 v28,s69,v6
v_mfma_f32_16x16x32_bf16 a[192:195],v[104:107],v[40:43],a[192:195]
s_lshl_b32 s69,s63,2
ds_read_b64_tr_b16 v[74:75],v1 offset:25664
v_mov_b32 v8,s52
v_mfma_f32_16x16x32_bf16 a[192:195],v[108:111],v[44:47],a[192:195]
ds_read_b64_tr_b16 v[76:77],v1 offset:30784
ds_read_b64_tr_b16 v[78:79],v1 offset:35904
v_mfma_f32_16x16x32_bf16 a[196:199],v[108:111],v[180:183],a[196:199]
ds_read_b64_tr_b16 v[80:81],v2 offset:96
v_mov_b32 v9,s53
v_mfma_f32_16x16x32_bf16 a[196:199],v[104:107],v[164:167],a[196:199]
)A"
R"A(ds_read_b64_tr_b16 v[82:83],v2 offset:5216
v_lshlrev_b32 v10,20,v6
v_mfma_f32_16x16x32_bf16 a[196:199],v[100:103],v[148:151],a[196:199]
ds_read_b64_tr_b16 v[84:85],v2 offset:10336
v_bfe_u32 v3,v6,4,2
v_mfma_f32_16x16x32_bf16 a[196:199],v[96:99],v[132:135],a[196:199]
buffer_store_dwordx4 v[244:247],v6,s[40:43],s82 offen offset:2048 sc1
v_mfma_f32_16x16x32_bf16 a[200:203],v[96:99],v[196:199],a[200:203]
ds_read_b64_tr_b16 v[86:87],v2 offset:15456
ds_read_b64_tr_b16 v[88:89],v2 offset:20576
v_mfma_f32_16x16x32_bf16 a[200:203],v[100:103],v[212:215],a[200:203]
ds_read_b64_tr_b16 v[90:91],v2 offset:25696
v_min_u32 v3,2,v3
v_mfma_f32_16x16x32_bf16 a[200:203],v[104:107],v[228:231],a[200:203]
buffer_load_dwordx4 a[148:151],v6,s[28:31],s88 offen offset:1024 sc1
ds_read_b64_tr_b16 v[92:93],v2 offset:30816
v_mfma_f32_16x16x32_bf16 a[200:203],v[108:111],v[244:247],a[200:203]
ds_read_b64_tr_b16 v[94:95],v2 offset:35936
v_add3_u32 v3,v3,s64,1
v_mfma_f32_16x16x32_bf16 a[204:207],v[108:111],v[188:191],a[204:207]
ds_read_b64_tr_b16 v[108:109],v1 offset:30848
ds_read_b64_tr_b16 v[110:111],v1 offset:35968
v_mfma_f32_16x16x32_bf16 a[204:207],v[104:107],v[172:175],a[204:207]
ds_read_b64_tr_b16 v[104:105],v1 offset:20608
v_and_b32 v3,3,v3
v_mfma_f32_16x16x32_bf16 a[204:207],v[100:103],v[156:159],a[204:207]
ds_read_b64_tr_b16 v[100:101],v1 offset:10368
v_lshlrev_b32 v3,6,v3
v_mfma_f32_16x16x32_bf16 a[204:207],v[96:99],v[140:143],a[204:207]
s_waitcnt lgkmcnt(9)
ds_read_b64_tr_b16 v[96:97],v1 offset:128
v_bfe_u32 v56,v6,6,2
v_mfma_f32_16x16x32_bf16 a[208:211],v[112:115],v[32:35],a[208:211]
ds_read_b64_tr_b16 v[98:99],v1 offset:5248
ds_read_b64_tr_b16 v[102:103],v1 offset:15488
v_mfma_f32_16x16x32_bf16 a[208:211],v[116:119],v[36:39],a[208:211]
ds_read_b64_tr_b16 v[106:107],v1 offset:25728
v_lshrrev_b32 v57,8,v6
v_mfma_f32_16x16x32_bf16 a[208:211],v[120:123],v[40:43],a[208:211]
v_cmp_eq_u32 s[56:57],v56,v57
v_bfe_u32 v56,v6,4,1
v_mfma_f32_16x16x32_bf16 a[208:211],v[124:127],v[44:47],a[208:211]
v_lshlrev_b32 v56,4,v56
v_mov_b32 v57,0x3f80
v_mfma_f32_16x16x32_bf16 a[212:215],v[124:127],v[180:183],a[212:215]
v_lshlrev_b32 v56,v56,v57
v_cndmask_b32 v56,0,v56,s[56:57]
v_mfma_f32_16x16x32_bf16 a[212:215],v[120:123],v[164:167],a[212:215]
v_bfe_u32 v57,v6,5,1
v_cmp_eq_u32 s[56:57],0,v57
v_mfma_f32_16x16x32_bf16 a[212:215],v[116:119],v[148:151],a[212:215]
buffer_store_dwordx4 v[140:143],v28,s[40:43],s79 offen offset:3072 sc1
v_mfma_f32_16x16x32_bf16 a[212:215],v[112:115],v[132:135],a[212:215]
v_cndmask_b32 v48,0,v56,s[56:57]
v_cndmask_b32 v49,v56,0,s[56:57]
v_mfma_f32_16x16x32_bf16 a[216:219],v[112:115],v[196:199],a[216:219]
s_add_u32 s36,s12,s66
s_addc_u32 s37,s13,0
buffer_load_dwordx4 a[132:135],v6,s[28:31],s87 offen offset:1024 sc1
v_mfma_f32_16x16x32_bf16 a[216:219],v[116:119],v[212:215],a[216:219]
s_and_b32 s37,s37,0xffff
v_mov_b32 v58,s52
v_mov_b32 v59,s53
v_mfma_f32_16x16x32_bf16 a[216:219],v[120:123],v[228:231],a[216:219]
s_mul_i32 s66,s61,307200
v_lshlrev_b32 v60,20,v6
v_bfe_u32 v61,v6,4,1
v_mfma_f32_16x16x32_bf16 a[216:219],v[124:127],v[244:247],a[216:219]
s_add_u32 s24,s16,s66
s_addc_u32 s25,s17,0
s_mul_i32 s66,s60,16384
v_mfma_f32_16x16x32_bf16 a[220:223],v[124:127],v[188:191],a[220:223]
s_add_u32 s66,s66,s67
ds_read_b64_tr_b16 v[124:125],v2 offset:30880
ds_read_b64_tr_b16 v[126:127],v2 offset:36000
v_mfma_f32_16x16x32_bf16 a[220:223],v[120:123],v[172:175],a[220:223]
s_add_u32 s79,s66,12288
ds_read_b64_tr_b16 v[120:121],v2 offset:20640
v_add3_u32 v61,v61,s63,2
v_mfma_f32_16x16x32_bf16 a[220:223],v[116:119],v[156:159],a[220:223]
s_and_b32 s25,s25,0xffff
ds_read_b64_tr_b16 v[116:117],v2 offset:10400
v_and_b32 v61,3,v61
v_mfma_f32_16x16x32_bf16 a[220:223],v[112:115],v[140:143],a[220:223]
s_add_u32 s67,s77,8192
ds_read_b64_tr_b16 v[112:113],v2 offset:160
v_lshlrev_b32 v61,8,v61
v_mfma_f32_16x16x32_bf16 a[224:227],v[64:67],v[32:35],a[224:227]
ds_read_b64_tr_b16 v[114:115],v2 offset:5280
ds_read_b64_tr_b16 v[118:119],v2 offset:15520
v_mfma_f32_16x16x32_bf16 a[224:227],v[68:71],v[36:39],a[224:227]
ds_read_b64_tr_b16 v[122:123],v2 offset:25760
v_mov_b32 v50,0
v_mfma_f32_16x16x32_bf16 a[224:227],v[72:75],v[40:43],a[224:227]
v_mov_b32 v51,0
v_mov_b32 v52,0
v_mfma_f32_16x16x32_bf16 a[224:227],v[76:79],v[44:47],a[224:227]
v_mov_b32 v53,0
v_mov_b32 v54,v48
v_mfma_f32_16x16x32_bf16 a[228:231],v[76:79],v[180:183],a[228:231]
buffer_load_dwordx4 a[136:139],v6,s[28:31],s87 offen offset:2048 sc1
v_mov_b32 v55,v49
v_mfma_f32_16x16x32_bf16 a[228:231],v[72:75],v[164:167],a[228:231]
v_bfe_u32 v56,v6,4,4
v_mul_u32_u24 v56,1280,v56
v_mfma_f32_16x16x32_bf16 a[228:231],v[68:71],v[148:151],a[228:231]
buffer_store_dwordx4 v[156:159],v28,s[40:43],s80 offen offset:3072 sc1
v_mfma_f32_16x16x32_bf16 a[228:231],v[64:67],v[132:135],a[228:231]
s_add_u32 s80,s66,77824
v_bfe_u32 v57,v6,8,1
v_lshl_add_u32 v56,v57,5,v56
v_mfma_f32_16x16x32_bf16 a[232:235],v[64:67],v[196:199],a[232:235]
v_lshrrev_b32 v57,9,v6
v_lshl_add_u32 v56,v57,4,v56
v_mfma_f32_16x16x32_bf16 a[232:235],v[68:71],v[212:215],a[232:235]
v_mfma_f32_16x16x32_bf16 a[232:235],v[72:75],v[228:231],a[232:235]
v_mfma_f32_16x16x32_bf16 a[232:235],v[76:79],v[244:247],a[232:235]
v_mfma_f32_16x16x32_bf16 a[236:239],v[76:79],v[188:191],a[236:239]
ds_read_b64_tr_b16 v[76:77],v1 offset:30912
ds_read_b64_tr_b16 v[78:79],v1 offset:36032
v_mfma_f32_16x16x32_bf16 a[236:239],v[72:75],v[172:175],a[236:239]
ds_read_b64_tr_b16 v[72:73],v1 offset:20672
v_mfma_f32_16x16x32_bf16 a[236:239],v[68:71],v[156:159],a[236:239]
ds_read_b64_tr_b16 v[68:69],v1 offset:10432
v_mfma_f32_16x16x32_bf16 a[236:239],v[64:67],v[140:143],a[236:239]
ds_read_b64_tr_b16 v[64:65],v1 offset:192
v_mfma_f32_16x16x32_bf16 a[240:243],v[80:83],v[32:35],a[240:243]
s_waitcnt lgkmcnt(5)
ds_read_b64_tr_b16 v[66:67],v1 offset:5312
ds_read_b64_tr_b16 v[70:71],v1 offset:15552
v_mfma_f32_16x16x32_bf16 a[240:243],v[84:87],v[36:39],a[240:243]
buffer_load_dwordx4 a[152:155],v6,s[28:31],s88 offen offset:2048 sc1
ds_read_b64_tr_b16 v[74:75],v1 offset:25792
v_mfma_f32_16x16x32_bf16 a[240:243],v[88:91],v[40:43],a[240:243]
v_mfma_f32_16x16x32_bf16 a[240:243],v[92:95],v[44:47],a[240:243]
v_mfma_f32_16x16x32_bf16 a[244:247],v[92:95],v[180:183],a[244:247]
v_mfma_f32_16x16x32_bf16 a[244:247],v[88:91],v[164:167],a[244:247]
buffer_store_dwordx4 v[172:175],v28,s[40:43],s81 offen offset:3072 sc1
v_mfma_f32_16x16x32_bf16 a[244:247],v[84:87],v[148:151],a[244:247]
s_add_u32 s81,s66,143360
v_mfma_f32_16x16x32_bf16 a[244:247],v[80:83],v[132:135],a[244:247]
v_mfma_f32_16x16x32_bf16 a[248:251],v[80:83],v[196:199],a[248:251]
v_mfma_f32_16x16x32_bf16 a[248:251],v[84:87],v[212:215],a[248:251]
v_mfma_f32_16x16x32_bf16 a[248:251],v[88:91],v[228:231],a[248:251]
v_mfma_f32_16x16x32_bf16 a[248:251],v[92:95],v[244:247],a[248:251]
v_mfma_f32_16x16x32_bf16 a[252:255],v[92:95],v[188:191],a[252:255]
ds_read_b64_tr_b16 v[92:93],v2 offset:30944
ds_read_b64_tr_b16 v[94:95],v2 offset:36064
v_mfma_f32_16x16x32_bf16 a[252:255],v[88:91],v[172:175],a[252:255]
ds_read_b64_tr_b16 v[88:89],v2 offset:20704
v_mfma_f32_16x16x32_bf16 a[252:255],v[84:87],v[156:159],a[252:255]
buffer_load_dwordx4 a[168:171],v6,s[28:31],s89 offen offset:2048 sc1
ds_read_b64_tr_b16 v[84:85],v2 offset:10464
v_mfma_f32_16x16x32_bf16 a[252:255],v[80:83],v[140:143],a[252:255]
ds_read_b64_tr_b16 v[80:81],v2 offset:224
v_mfma_f32_16x16x32_bf16 a[0:3],v[96:99],v[32:35],a[0:3]
ds_read_b64_tr_b16 v[82:83],v2 offset:5344
ds_read_b64_tr_b16 v[86:87],v2 offset:15584
v_mfma_f32_16x16x32_bf16 a[0:3],v[100:103],v[36:39],a[0:3]
ds_read_b64_tr_b16 v[90:91],v2 offset:25824
v_mfma_f32_16x16x32_bf16 a[0:3],v[104:107],v[40:43],a[0:3]
L_pd15_%=:
s_cbranch_execz L_trt1_%=
v_mfma_f32_16x16x32_bf16 a[0:3],v[108:111],v[44:47],a[0:3]
v_mfma_f32_16x16x32_bf16 a[4:7],v[108:111],v[180:183],a[4:7]
buffer_store_dwordx4 v[188:191],v28,s[40:43],s82 offen offset:3072 sc1
v_mfma_f32_16x16x32_bf16 a[4:7],v[104:107],v[164:167],a[4:7]
s_add_u32 s82,s66,208896
s_mul_i32 s66,s64,320
v_mfma_f32_16x16x32_bf16 a[4:7],v[100:103],v[148:151],a[4:7]
v_mfma_f32_16x16x32_bf16 a[4:7],v[96:99],v[132:135],a[4:7]
v_mfma_f32_16x16x32_bf16 a[8:11],v[96:99],v[196:199],a[8:11]
v_mfma_f32_16x16x32_bf16 a[8:11],v[100:103],v[212:215],a[8:11]
v_mfma_f32_16x16x32_bf16 a[8:11],v[104:107],v[228:231],a[8:11]
v_mfma_f32_16x16x32_bf16 a[8:11],v[108:111],v[244:247],a[8:11]
buffer_load_dwordx4 a[184:187],v6,s[28:31],s90 offen offset:2048 sc1
v_mfma_f32_16x16x32_bf16 a[12:15],v[108:111],v[188:191],a[12:15]
ds_read_b64_tr_b16 v[108:109],v1 offset:30976
ds_read_b64_tr_b16 v[110:111],v1 offset:36096
v_mfma_f32_16x16x32_bf16 a[12:15],v[104:107],v[172:175],a[12:15]
ds_read_b64_tr_b16 v[104:105],v1 offset:20736
v_mfma_f32_16x16x32_bf16 a[12:15],v[100:103],v[156:159],a[12:15]
ds_read_b64_tr_b16 v[100:101],v1 offset:10496
v_mfma_f32_16x16x32_bf16 a[12:15],v[96:99],v[140:143],a[12:15]
ds_read_b64_tr_b16 v[96:97],v1 offset:256
v_mfma_f32_16x16x32_bf16 a[16:19],v[112:115],v[32:35],a[16:19]
ds_read_b64_tr_b16 v[98:99],v1 offset:5376
ds_read_b64_tr_b16 v[102:103],v1 offset:15616
v_mfma_f32_16x16x32_bf16 a[16:19],v[116:119],v[36:39],a[16:19]
ds_read_b64_tr_b16 v[106:107],v1 offset:25856
v_add_u32 v1,0xffff6000,v1
v_mfma_f32_16x16x32_bf16 a[16:19],v[120:123],v[40:43],a[16:19]
v_mfma_f32_16x16x32_bf16 a[16:19],v[124:127],v[44:47],a[16:19]
v_mfma_f32_16x16x32_bf16 a[20:23],v[124:127],v[180:183],a[20:23]
v_mfma_f32_16x16x32_bf16 a[20:23],v[120:123],v[164:167],a[20:23]
v_mfma_f32_16x16x32_bf16 a[20:23],v[116:119],v[148:151],a[20:23]
v_mfma_f32_16x16x32_bf16 a[20:23],v[112:115],v[132:135],a[20:23]
v_mfma_f32_16x16x32_bf16 a[24:27],v[112:115],v[196:199],a[24:27]
buffer_load_dwordx4 a[188:191],v28,s[28:31],s90 offen offset:3072 sc1
v_mfma_f32_16x16x32_bf16 a[24:27],v[116:119],v[212:215],a[24:27]
v_mfma_f32_16x16x32_bf16 a[24:27],v[120:123],v[228:231],a[24:27]
v_mfma_f32_16x16x32_bf16 a[24:27],v[124:127],v[244:247],a[24:27]
v_mfma_f32_16x16x32_bf16 a[28:31],v[124:127],v[188:191],a[28:31]
ds_read_b64_tr_b16 v[124:125],v2 offset:31008
ds_read_b64_tr_b16 v[126:127],v2 offset:36128
v_mfma_f32_16x16x32_bf16 a[28:31],v[120:123],v[172:175],a[28:31]
ds_read_b64_tr_b16 v[120:121],v2 offset:20768
v_mfma_f32_16x16x32_bf16 a[28:31],v[116:119],v[156:159],a[28:31]
ds_read_b64_tr_b16 v[116:117],v2 offset:10528
v_mfma_f32_16x16x32_bf16 a[28:31],v[112:115],v[140:143],a[28:31]
s_waitcnt lgkmcnt(4)
ds_read_b64_tr_b16 v[112:113],v2 offset:288
v_mfma_f32_16x16x32_bf16 v[128:131],v[64:67],v[32:35],v[128:131]
ds_read_b64_tr_b16 v[114:115],v2 offset:5408
ds_read_b64_tr_b16 v[118:119],v2 offset:15648
v_mfma_f32_16x16x32_bf16 v[128:131],v[68:71],v[36:39],v[128:131]
ds_read_b64_tr_b16 v[122:123],v2 offset:25888
v_add_u32 v2,0xffff6000,v2
v_mfma_f32_16x16x32_bf16 v[128:131],v[72:75],v[40:43],v[128:131]
v_mfma_f32_16x16x32_bf16 v[128:131],v[76:79],v[44:47],v[128:131]
v_mfma_f32_16x16x32_bf16 v[136:139],v[76:79],v[180:183],v[136:139]
v_mfma_f32_16x16x32_bf16 v[136:139],v[72:75],v[164:167],v[136:139]
buffer_load_dwordx4 a[172:175],v28,s[28:31],s89 offen offset:3072 sc1
v_mfma_f32_16x16x32_bf16 v[136:139],v[68:71],v[148:151],v[136:139]
v_mfma_f32_16x16x32_bf16 v[136:139],v[64:67],v[132:135],v[136:139]
v_mfma_f32_16x16x32_bf16 v[144:147],v[64:67],v[196:199],v[144:147]
v_mfma_f32_16x16x32_bf16 v[144:147],v[68:71],v[212:215],v[144:147]
v_mfma_f32_16x16x32_bf16 v[144:147],v[72:75],v[228:231],v[144:147]
v_mfma_f32_16x16x32_bf16 v[144:147],v[76:79],v[244:247],v[144:147]
v_mfma_f32_16x16x32_bf16 v[152:155],v[76:79],v[188:191],v[152:155]
v_mfma_f32_16x16x32_bf16 v[152:155],v[72:75],v[172:175],v[152:155]
v_mfma_f32_16x16x32_bf16 v[152:155],v[68:71],v[156:159],v[152:155]
v_mfma_f32_16x16x32_bf16 v[152:155],v[64:67],v[140:143],v[152:155]
s_mov_b64 exec,0
s_cmp_eq_u32 s60,0
s_cbranch_scc1 L_pd16_%=
s_cmp_eq_u32 s60,1
s_cbranch_scc1 L_pd17_%=
s_cmp_eq_u32 s60,2
s_cbranch_scc1 L_pd18_%=
s_branch L_pd19_%=
L_trtg2F_%=:
s_mov_b64 exec,-1
s_waitcnt lgkmcnt(0)
s_barrier
v_mfma_f32_16x16x32_bf16 v[160:163],v[80:83],v[32:35],v[160:163]
buffer_load_dwordx4 v24,s[48:51],s83 offen lds
)A"
R"A(ds_read_b64_tr_b16 v[64:65],v1
v_mfma_f32_16x16x32_bf16 v[160:163],v[84:87],v[36:39],v[160:163]
ds_read_b64_tr_b16 v[66:67],v1 offset:5120
ds_read_b64_tr_b16 v[68:69],v1 offset:10240
v_mfma_f32_16x16x32_bf16 v[160:163],v[88:91],v[40:43],v[160:163]
ds_read_b64_tr_b16 v[70:71],v1 offset:15360
v_mfma_f32_16x16x32_bf16 v[160:163],v[92:95],v[44:47],v[160:163]
ds_read_b64_tr_b16 v[72:73],v1 offset:20480
v_mfma_f32_16x16x32_bf16 v[168:171],v[92:95],v[180:183],v[168:171]
ds_read_b64_tr_b16 v[74:75],v1 offset:25600
v_mfma_f32_16x16x32_bf16 v[168:171],v[88:91],v[164:167],v[168:171]
buffer_load_dwordx4 a[156:159],v28,s[28:31],s88 offen offset:3072 sc1
ds_read_b64_tr_b16 v[76:77],v1 offset:30720
v_mfma_f32_16x16x32_bf16 v[168:171],v[84:87],v[148:151],v[168:171]
ds_read_b64_tr_b16 v[78:79],v1 offset:35840
v_mfma_f32_16x16x32_bf16 v[168:171],v[80:83],v[132:135],v[168:171]
v_mfma_f32_16x16x32_bf16 v[176:179],v[80:83],v[196:199],v[176:179]
v_mfma_f32_16x16x32_bf16 v[176:179],v[84:87],v[212:215],v[176:179]
v_mfma_f32_16x16x32_bf16 v[176:179],v[88:91],v[228:231],v[176:179]
v_mfma_f32_16x16x32_bf16 v[176:179],v[92:95],v[244:247],v[176:179]
buffer_load_dwordx4 v25,s[48:51],s83 offen offset:1024 lds
v_mfma_f32_16x16x32_bf16 v[184:187],v[92:95],v[188:191],v[184:187]
ds_read_b64_tr_b16 v[92:93],v2 offset:30752
ds_read_b64_tr_b16 v[94:95],v2 offset:35872
v_mfma_f32_16x16x32_bf16 v[184:187],v[88:91],v[172:175],v[184:187]
ds_read_b64_tr_b16 v[88:89],v2 offset:20512
v_mfma_f32_16x16x32_bf16 v[184:187],v[84:87],v[156:159],v[184:187]
ds_read_b64_tr_b16 v[84:85],v2 offset:10272
v_mfma_f32_16x16x32_bf16 v[184:187],v[80:83],v[140:143],v[184:187]
ds_read_b64_tr_b16 v[80:81],v2 offset:32
v_mfma_f32_16x16x32_bf16 v[192:195],v[96:99],v[32:35],v[192:195]
buffer_load_dwordx4 a[140:143],v28,s[28:31],s87 offen offset:3072 sc1
ds_read_b64_tr_b16 v[82:83],v2 offset:5152
v_mfma_f32_16x16x32_bf16 v[192:195],v[100:103],v[36:39],v[192:195]
s_add_u32 s87,s64,2
s_and_b32 s87,s87,3
s_lshl_b32 s87,s87,1
v_mfma_f32_16x16x32_bf16 v[192:195],v[104:107],v[40:43],v[192:195]
s_lshl_b32 s87,s87,16
s_add_u32 s88,s87,4096
s_add_u32 s89,s87,8192
v_mfma_f32_16x16x32_bf16 v[192:195],v[108:111],v[44:47],v[192:195]
s_add_u32 s90,s87,12288
ds_read_b64_tr_b16 v[86:87],v2 offset:15392
ds_read_b64_tr_b16 v[90:91],v2 offset:25632
v_mfma_f32_16x16x32_bf16 v[200:203],v[108:111],v[180:183],v[200:203]
v_mfma_f32_16x16x32_bf16 v[200:203],v[104:107],v[164:167],v[200:203]
buffer_load_dwordx4 a[68:71],v6,s[28:31],s87 offen sc1
v_mfma_f32_16x16x32_bf16 v[200:203],v[100:103],v[148:151],v[200:203]
v_mfma_f32_16x16x32_bf16 v[200:203],v[96:99],v[132:135],v[200:203]
v_mfma_f32_16x16x32_bf16 v[208:211],v[96:99],v[196:199],v[208:211]
v_mfma_f32_16x16x32_bf16 v[208:211],v[100:103],v[212:215],v[208:211]
v_mfma_f32_16x16x32_bf16 v[208:211],v[104:107],v[228:231],v[208:211]
buffer_load_dwordx4 a[76:79],v6,s[28:31],s88 offen sc1
v_mfma_f32_16x16x32_bf16 v[208:211],v[108:111],v[244:247],v[208:211]
v_mfma_f32_16x16x32_bf16 v[216:219],v[108:111],v[188:191],v[216:219]
ds_read_b64_tr_b16 v[108:109],v1 offset:30784
ds_read_b64_tr_b16 v[110:111],v1 offset:35904
v_mfma_f32_16x16x32_bf16 v[216:219],v[104:107],v[172:175],v[216:219]
ds_read_b64_tr_b16 v[104:105],v1 offset:20544
v_mfma_f32_16x16x32_bf16 v[216:219],v[100:103],v[156:159],v[216:219]
ds_read_b64_tr_b16 v[100:101],v1 offset:10304
v_mfma_f32_16x16x32_bf16 v[216:219],v[96:99],v[140:143],v[216:219]
buffer_load_dwordx4 a[84:87],v6,s[28:31],s89 offen sc1
ds_read_b64_tr_b16 v[96:97],v1 offset:64
v_mfma_f32_16x16x32_bf16 v[224:227],v[112:115],v[32:35],v[224:227]
ds_read_b64_tr_b16 v[98:99],v1 offset:5184
ds_read_b64_tr_b16 v[102:103],v1 offset:15424
v_mfma_f32_16x16x32_bf16 v[224:227],v[116:119],v[36:39],v[224:227]
ds_read_b64_tr_b16 v[106:107],v1 offset:25664
v_mfma_f32_16x16x32_bf16 v[224:227],v[120:123],v[40:43],v[224:227]
v_mfma_f32_16x16x32_bf16 v[224:227],v[124:127],v[44:47],v[224:227]
v_mfma_f32_16x16x32_bf16 v[232:235],v[124:127],v[180:183],v[232:235]
buffer_load_dwordx4 a[92:95],v6,s[28:31],s90 offen sc1
v_mfma_f32_16x16x32_bf16 v[232:235],v[120:123],v[164:167],v[232:235]
v_mfma_f32_16x16x32_bf16 v[232:235],v[116:119],v[148:151],v[232:235]
v_mfma_f32_16x16x32_bf16 v[232:235],v[112:115],v[132:135],v[232:235]
v_mfma_f32_16x16x32_bf16 v[240:243],v[112:115],v[196:199],v[240:243]
v_mfma_f32_16x16x32_bf16 v[240:243],v[116:119],v[212:215],v[240:243]
buffer_load_dwordx4 a[124:127],v6,s[28:31],s90 offen offset:1024 sc1
v_mfma_f32_16x16x32_bf16 v[240:243],v[120:123],v[228:231],v[240:243]
v_mfma_f32_16x16x32_bf16 v[240:243],v[124:127],v[244:247],v[240:243]
v_mfma_f32_16x16x32_bf16 v[248:251],v[124:127],v[188:191],v[248:251]
ds_read_b64_tr_b16 v[124:125],v2 offset:30816
ds_read_b64_tr_b16 v[126:127],v2 offset:35936
v_mfma_f32_16x16x32_bf16 v[248:251],v[120:123],v[172:175],v[248:251]
ds_read_b64_tr_b16 v[120:121],v2 offset:20576
v_mfma_f32_16x16x32_bf16 v[248:251],v[116:119],v[156:159],v[248:251]
buffer_load_dwordx4 a[116:119],v6,s[28:31],s89 offen offset:1024 sc1
ds_read_b64_tr_b16 v[116:117],v2 offset:10336
v_mfma_f32_16x16x32_bf16 v[248:251],v[112:115],v[140:143],v[248:251]
s_waitcnt vmcnt(31) lgkmcnt(4)
ds_read_b64_tr_b16 v[112:113],v2 offset:96
v_mfma_f32_16x16x32_bf16 a[192:195],v[64:67],a[128:131],a[192:195]
ds_read_b64_tr_b16 v[114:115],v2 offset:5216
ds_read_b64_tr_b16 v[118:119],v2 offset:15456
v_mfma_f32_16x16x32_bf16 a[192:195],v[68:71],a[144:147],a[192:195]
s_waitcnt vmcnt(29)
ds_read_b64_tr_b16 v[122:123],v2 offset:25696
v_mfma_f32_16x16x32_bf16 a[192:195],v[72:75],a[160:163],a[192:195]
s_waitcnt vmcnt(27)
v_mfma_f32_16x16x32_bf16 a[192:195],v[76:79],a[176:179],a[192:195]
s_waitcnt vmcnt(25)
buffer_load_dwordx4 a[108:111],v6,s[28:31],s88 offen offset:1024 sc1
v_mfma_f32_16x16x32_bf16 a[196:199],v[76:79],a[180:183],a[196:199]
s_waitcnt vmcnt(24)
v_mfma_f32_16x16x32_bf16 a[196:199],v[72:75],a[164:167],a[196:199]
s_waitcnt vmcnt(22)
v_mfma_f32_16x16x32_bf16 a[196:199],v[68:71],a[148:151],a[196:199]
s_waitcnt vmcnt(19)
v_mfma_f32_16x16x32_bf16 a[196:199],v[64:67],a[132:135],a[196:199]
v_mfma_f32_16x16x32_bf16 a[200:203],v[64:67],a[136:139],a[200:203]
s_waitcnt vmcnt(17)
buffer_load_dwordx4 a[100:103],v6,s[28:31],s87 offen offset:1024 sc1
v_mfma_f32_16x16x32_bf16 a[200:203],v[68:71],a[152:155],a[200:203]
s_waitcnt vmcnt(16)
v_mfma_f32_16x16x32_bf16 a[200:203],v[72:75],a[168:171],a[200:203]
s_waitcnt vmcnt(14)
v_mfma_f32_16x16x32_bf16 a[200:203],v[76:79],a[184:187],a[200:203]
s_waitcnt vmcnt(13)
v_mfma_f32_16x16x32_bf16 a[204:207],v[76:79],a[188:191],a[204:207]
s_waitcnt vmcnt(12)
ds_read_b64_tr_b16 v[76:77],v1 offset:30848
ds_read_b64_tr_b16 v[78:79],v1 offset:35968
v_mfma_f32_16x16x32_bf16 a[204:207],v[72:75],a[172:175],a[204:207]
s_waitcnt vmcnt(10)
buffer_load_dwordx4 a[64:67],v6,s[28:31],s87 offen offset:2048 sc1
ds_read_b64_tr_b16 v[72:73],v1 offset:20608
v_mfma_f32_16x16x32_bf16 a[204:207],v[68:71],a[156:159],a[204:207]
s_waitcnt vmcnt(9)
ds_read_b64_tr_b16 v[68:69],v1 offset:10368
v_mfma_f32_16x16x32_bf16 a[204:207],v[64:67],a[140:143],a[204:207]
ds_read_b64_tr_b16 v[64:65],v1 offset:128
v_mfma_f32_16x16x32_bf16 a[208:211],v[80:83],a[128:131],a[208:211]
ds_read_b64_tr_b16 v[66:67],v1 offset:5248
ds_read_b64_tr_b16 v[70:71],v1 offset:15488
v_mfma_f32_16x16x32_bf16 a[208:211],v[84:87],a[144:147],a[208:211]
ds_read_b64_tr_b16 v[74:75],v1 offset:25728
v_mfma_f32_16x16x32_bf16 a[208:211],v[88:91],a[160:163],a[208:211]
v_mfma_f32_16x16x32_bf16 a[208:211],v[92:95],a[176:179],a[208:211]
buffer_load_dwordx4 v26,s[48:51],s83 offen offset:2048 lds
v_mfma_f32_16x16x32_bf16 a[212:215],v[92:95],a[180:183],a[212:215]
v_mfma_f32_16x16x32_bf16 a[212:215],v[88:91],a[164:167],a[212:215]
v_mfma_f32_16x16x32_bf16 a[212:215],v[84:87],a[148:151],a[212:215]
v_mfma_f32_16x16x32_bf16 a[212:215],v[80:83],a[132:135],a[212:215]
v_mfma_f32_16x16x32_bf16 a[216:219],v[80:83],a[136:139],a[216:219]
buffer_load_dwordx4 a[72:75],v6,s[28:31],s88 offen offset:2048 sc1
v_mfma_f32_16x16x32_bf16 a[216:219],v[84:87],a[152:155],a[216:219]
v_mfma_f32_16x16x32_bf16 a[216:219],v[88:91],a[168:171],a[216:219]
v_mfma_f32_16x16x32_bf16 a[216:219],v[92:95],a[184:187],a[216:219]
v_mfma_f32_16x16x32_bf16 a[220:223],v[92:95],a[188:191],a[220:223]
ds_read_b64_tr_b16 v[92:93],v2 offset:30880
ds_read_b64_tr_b16 v[94:95],v2 offset:36000
v_mfma_f32_16x16x32_bf16 a[220:223],v[88:91],a[172:175],a[220:223]
ds_read_b64_tr_b16 v[88:89],v2 offset:20640
v_mfma_f32_16x16x32_bf16 a[220:223],v[84:87],a[156:159],a[220:223]
buffer_load_dwordx4 v27,s[48:51],s83 offen offset:3072 lds
ds_read_b64_tr_b16 v[84:85],v2 offset:10400
v_mfma_f32_16x16x32_bf16 a[220:223],v[80:83],a[140:143],a[220:223]
s_add_u32 m0,s86,126976
ds_read_b64_tr_b16 v[80:81],v2 offset:160
s_barrier
v_mfma_f32_16x16x32_bf16 a[224:227],v[96:99],a[128:131],a[224:227]
ds_read_b64_tr_b16 v[82:83],v2 offset:5280
ds_read_b64_tr_b16 v[86:87],v2 offset:15520
v_mfma_f32_16x16x32_bf16 a[224:227],v[100:103],a[144:147],a[224:227]
ds_read_b64_tr_b16 v[90:91],v2 offset:25760
v_mfma_f32_16x16x32_bf16 a[224:227],v[104:107],a[160:163],a[224:227]
buffer_store_dwordx2 v[8:9],v10,s[44:47],s18 offen offset:1280 sc1
v_mfma_f32_16x16x32_bf16 a[224:227],v[108:111],a[176:179],a[224:227]
v_mfma_f32_16x16x32_bf16 a[228:231],v[108:111],a[180:183],a[228:231]
v_mfma_f32_16x16x32_bf16 a[228:231],v[104:107],a[164:167],a[228:231]
v_mfma_f32_16x16x32_bf16 a[228:231],v[100:103],a[148:151],a[228:231]
v_mfma_f32_16x16x32_bf16 a[228:231],v[96:99],a[132:135],a[228:231]
buffer_load_dwordx4 a[80:83],v6,s[28:31],s89 offen offset:2048 sc1
v_mfma_f32_16x16x32_bf16 a[232:235],v[96:99],a[136:139],a[232:235]
v_mfma_f32_16x16x32_bf16 a[232:235],v[100:103],a[152:155],a[232:235]
v_mfma_f32_16x16x32_bf16 a[232:235],v[104:107],a[168:171],a[232:235]
v_mfma_f32_16x16x32_bf16 a[232:235],v[108:111],a[184:187],a[232:235]
v_mfma_f32_16x16x32_bf16 a[236:239],v[108:111],a[188:191],a[236:239]
ds_read_b64_tr_b16 v[108:109],v1 offset:30912
ds_read_b64_tr_b16 v[110:111],v1 offset:36032
v_mfma_f32_16x16x32_bf16 a[236:239],v[104:107],a[172:175],a[236:239]
buffer_load_dwordx4 v7,s[48:51],s83 offen lds
ds_read_b64_tr_b16 v[104:105],v1 offset:20672
v_mfma_f32_16x16x32_bf16 a[236:239],v[100:103],a[156:159],a[236:239]
s_add_u32 m0,s86,128000
s_add_u32 s83,s64,1
ds_read_b64_tr_b16 v[100:101],v1 offset:10432
v_mfma_f32_16x16x32_bf16 a[236:239],v[96:99],a[140:143],a[236:239]
s_and_b32 s83,s83,3
s_lshl_b32 s83,s83,1
ds_read_b64_tr_b16 v[96:97],v1 offset:192
s_waitcnt lgkmcnt(5)
v_mfma_f32_16x16x32_bf16 a[240:243],v[112:115],a[128:131],a[240:243]
s_or_b32 s83,s83,1
ds_read_b64_tr_b16 v[98:99],v1 offset:5312
ds_read_b64_tr_b16 v[102:103],v1 offset:15552
v_mfma_f32_16x16x32_bf16 a[240:243],v[116:119],a[144:147],a[240:243]
s_mul_i32 s83,s83,163840
s_add_u32 s83,s83,s85
ds_read_b64_tr_b16 v[106:107],v1 offset:25792
v_mfma_f32_16x16x32_bf16 a[240:243],v[120:123],a[160:163],a[240:243]
buffer_load_dwordx4 a[88:91],v6,s[28:31],s90 offen offset:2048 sc1
v_mfma_f32_16x16x32_bf16 a[240:243],v[124:127],a[176:179],a[240:243]
v_mfma_f32_16x16x32_bf16 a[244:247],v[124:127],a[180:183],a[244:247]
v_mfma_f32_16x16x32_bf16 a[244:247],v[120:123],a[164:167],a[244:247]
v_mfma_f32_16x16x32_bf16 a[244:247],v[116:119],a[148:151],a[244:247]
v_mfma_f32_16x16x32_bf16 a[244:247],v[112:115],a[132:135],a[244:247]
v_mfma_f32_16x16x32_bf16 a[248:251],v[112:115],a[136:139],a[248:251]
buffer_load_dwordx4 v24,s[48:51],s84 offen lds
v_mfma_f32_16x16x32_bf16 a[248:251],v[116:119],a[152:155],a[248:251]
v_mfma_f32_16x16x32_bf16 a[248:251],v[120:123],a[168:171],a[248:251]
v_mfma_f32_16x16x32_bf16 a[248:251],v[124:127],a[184:187],a[248:251]
v_mfma_f32_16x16x32_bf16 a[252:255],v[124:127],a[188:191],a[252:255]
ds_read_b64_tr_b16 v[124:125],v2 offset:30944
ds_read_b64_tr_b16 v[126:127],v2 offset:36064
v_mfma_f32_16x16x32_bf16 a[252:255],v[120:123],a[172:175],a[252:255]
buffer_load_dwordx4 a[120:123],v28,s[28:31],s90 offen offset:3072 sc1
ds_read_b64_tr_b16 v[120:121],v2 offset:20704
v_mfma_f32_16x16x32_bf16 a[252:255],v[116:119],a[156:159],a[252:255]
)A"
R"A(ds_read_b64_tr_b16 v[116:117],v2 offset:10464
v_mfma_f32_16x16x32_bf16 a[252:255],v[112:115],a[140:143],a[252:255]
ds_read_b64_tr_b16 v[112:113],v2 offset:224
v_mfma_f32_16x16x32_bf16 a[0:3],v[64:67],a[128:131],a[0:3]
ds_read_b64_tr_b16 v[114:115],v2 offset:5344
ds_read_b64_tr_b16 v[118:119],v2 offset:15584
v_mfma_f32_16x16x32_bf16 a[0:3],v[68:71],a[144:147],a[0:3]
ds_read_b64_tr_b16 v[122:123],v2 offset:25824
v_mfma_f32_16x16x32_bf16 a[0:3],v[72:75],a[160:163],a[0:3]
v_mfma_f32_16x16x32_bf16 a[0:3],v[76:79],a[176:179],a[0:3]
buffer_load_dwordx4 v25,s[48:51],s84 offen offset:1024 lds
v_mfma_f32_16x16x32_bf16 a[4:7],v[76:79],a[180:183],a[4:7]
v_mfma_f32_16x16x32_bf16 a[4:7],v[72:75],a[164:167],a[4:7]
v_mfma_f32_16x16x32_bf16 a[4:7],v[68:71],a[148:151],a[4:7]
v_mfma_f32_16x16x32_bf16 a[4:7],v[64:67],a[132:135],a[4:7]
v_mfma_f32_16x16x32_bf16 a[8:11],v[64:67],a[136:139],a[8:11]
buffer_load_dwordx4 a[112:115],v28,s[28:31],s89 offen offset:3072 sc1
v_mfma_f32_16x16x32_bf16 a[8:11],v[68:71],a[152:155],a[8:11]
v_mfma_f32_16x16x32_bf16 a[8:11],v[72:75],a[168:171],a[8:11]
v_mfma_f32_16x16x32_bf16 a[8:11],v[76:79],a[184:187],a[8:11]
v_mfma_f32_16x16x32_bf16 a[12:15],v[76:79],a[188:191],a[12:15]
ds_read_b64_tr_b16 v[76:77],v1 offset:30976
ds_read_b64_tr_b16 v[78:79],v1 offset:36096
v_mfma_f32_16x16x32_bf16 a[12:15],v[72:75],a[172:175],a[12:15]
buffer_load_dwordx4 a[104:107],v28,s[28:31],s88 offen offset:3072 sc1
ds_read_b64_tr_b16 v[72:73],v1 offset:20736
v_mfma_f32_16x16x32_bf16 a[12:15],v[68:71],a[156:159],a[12:15]
ds_read_b64_tr_b16 v[68:69],v1 offset:10496
v_mfma_f32_16x16x32_bf16 a[12:15],v[64:67],a[140:143],a[12:15]
ds_read_b64_tr_b16 v[64:65],v1 offset:256
v_mfma_f32_16x16x32_bf16 a[16:19],v[80:83],a[128:131],a[16:19]
ds_read_b64_tr_b16 v[66:67],v1 offset:5376
ds_read_b64_tr_b16 v[70:71],v1 offset:15616
v_mfma_f32_16x16x32_bf16 a[16:19],v[84:87],a[144:147],a[16:19]
ds_read_b64_tr_b16 v[74:75],v1 offset:25856
v_add_u32 v1,0xffff6000,v1
v_mfma_f32_16x16x32_bf16 a[16:19],v[88:91],a[160:163],a[16:19]
v_mfma_f32_16x16x32_bf16 a[16:19],v[92:95],a[176:179],a[16:19]
buffer_load_dwordx4 v26,s[48:51],s84 offen offset:2048 lds
v_mfma_f32_16x16x32_bf16 a[20:23],v[92:95],a[180:183],a[20:23]
v_mfma_f32_16x16x32_bf16 a[20:23],v[88:91],a[164:167],a[20:23]
v_mfma_f32_16x16x32_bf16 a[20:23],v[84:87],a[148:151],a[20:23]
v_mfma_f32_16x16x32_bf16 a[20:23],v[80:83],a[132:135],a[20:23]
v_mfma_f32_16x16x32_bf16 a[24:27],v[80:83],a[136:139],a[24:27]
buffer_load_dwordx4 a[96:99],v28,s[28:31],s87 offen offset:3072 sc1
v_mfma_f32_16x16x32_bf16 a[24:27],v[84:87],a[152:155],a[24:27]
s_add_u32 s87,s64,3
s_and_b32 s87,s87,3
s_lshl_b32 s87,s87,1
v_mfma_f32_16x16x32_bf16 a[24:27],v[88:91],a[168:171],a[24:27]
s_lshl_b32 s87,s87,16
s_add_u32 s88,s87,4096
s_add_u32 s89,s87,8192
v_mfma_f32_16x16x32_bf16 a[24:27],v[92:95],a[184:187],a[24:27]
s_add_u32 s90,s87,12288
v_mfma_f32_16x16x32_bf16 a[28:31],v[92:95],a[188:191],a[28:31]
ds_read_b64_tr_b16 v[92:93],v2 offset:31008
ds_read_b64_tr_b16 v[94:95],v2 offset:36128
v_mfma_f32_16x16x32_bf16 a[28:31],v[88:91],a[172:175],a[28:31]
ds_read_b64_tr_b16 v[88:89],v2 offset:20768
v_mfma_f32_16x16x32_bf16 a[28:31],v[84:87],a[156:159],a[28:31]
buffer_load_dwordx4 v27,s[48:51],s84 offen offset:3072 lds
ds_read_b64_tr_b16 v[84:85],v2 offset:10528
v_mfma_f32_16x16x32_bf16 a[28:31],v[80:83],a[140:143],a[28:31]
s_add_u32 m0,s86,132096
ds_read_b64_tr_b16 v[80:81],v2 offset:288
s_waitcnt lgkmcnt(5)
v_mfma_f32_16x16x32_bf16 v[128:131],v[96:99],a[128:131],v[128:131]
ds_read_b64_tr_b16 v[82:83],v2 offset:5408
ds_read_b64_tr_b16 v[86:87],v2 offset:15648
v_mfma_f32_16x16x32_bf16 v[128:131],v[100:103],a[144:147],v[128:131]
ds_read_b64_tr_b16 v[90:91],v2 offset:25888
v_add_u32 v2,0xffff6000,v2
v_mfma_f32_16x16x32_bf16 v[128:131],v[104:107],a[160:163],v[128:131]
v_mfma_f32_16x16x32_bf16 v[128:131],v[108:111],a[176:179],v[128:131]
v_mfma_f32_16x16x32_bf16 v[136:139],v[108:111],a[180:183],v[136:139]
buffer_load_dwordx4 v7,s[48:51],s84 offen lds
v_mfma_f32_16x16x32_bf16 v[136:139],v[104:107],a[164:167],v[136:139]
s_add_u32 m0,s86,81920
s_add_u32 s84,s83,20480
v_mfma_f32_16x16x32_bf16 v[136:139],v[100:103],a[148:151],v[136:139]
v_mfma_f32_16x16x32_bf16 v[136:139],v[96:99],a[132:135],v[136:139]
v_mfma_f32_16x16x32_bf16 v[144:147],v[96:99],a[136:139],v[144:147]
v_mfma_f32_16x16x32_bf16 v[144:147],v[100:103],a[152:155],v[144:147]
v_mfma_f32_16x16x32_bf16 v[144:147],v[104:107],a[168:171],v[144:147]
buffer_load_dwordx4 v24,s[48:51],s83 offen lds
v_mfma_f32_16x16x32_bf16 v[144:147],v[108:111],a[184:187],v[144:147]
v_mfma_f32_16x16x32_bf16 v[152:155],v[108:111],a[188:191],v[152:155]
ds_read_b64_tr_b16 v[108:109],v1 offset:30720
ds_read_b64_tr_b16 v[110:111],v1 offset:35840
v_mfma_f32_16x16x32_bf16 v[152:155],v[104:107],a[172:175],v[152:155]
ds_read_b64_tr_b16 v[104:105],v1 offset:20480
v_mfma_f32_16x16x32_bf16 v[152:155],v[100:103],a[156:159],v[152:155]
ds_read_b64_tr_b16 v[100:101],v1 offset:10240
v_mfma_f32_16x16x32_bf16 v[152:155],v[96:99],a[140:143],v[152:155]
ds_read_b64_tr_b16 v[96:97],v1
v_mfma_f32_16x16x32_bf16 v[160:163],v[112:115],a[128:131],v[160:163]
buffer_load_dwordx4 v25,s[48:51],s83 offen offset:1024 lds
ds_read_b64_tr_b16 v[98:99],v1 offset:5120
v_mfma_f32_16x16x32_bf16 v[160:163],v[116:119],a[144:147],v[160:163]
ds_read_b64_tr_b16 v[102:103],v1 offset:15360
ds_read_b64_tr_b16 v[106:107],v1 offset:25600
v_mfma_f32_16x16x32_bf16 v[160:163],v[120:123],a[160:163],v[160:163]
v_mfma_f32_16x16x32_bf16 v[160:163],v[124:127],a[176:179],v[160:163]
v_mfma_f32_16x16x32_bf16 v[168:171],v[124:127],a[180:183],v[168:171]
v_mfma_f32_16x16x32_bf16 v[168:171],v[120:123],a[164:167],v[168:171]
v_mfma_f32_16x16x32_bf16 v[168:171],v[116:119],a[148:151],v[168:171]
buffer_load_dwordx4 v26,s[48:51],s83 offen offset:2048 lds
v_mfma_f32_16x16x32_bf16 v[168:171],v[112:115],a[132:135],v[168:171]
v_mfma_f32_16x16x32_bf16 v[176:179],v[112:115],a[136:139],v[176:179]
v_mfma_f32_16x16x32_bf16 v[176:179],v[116:119],a[152:155],v[176:179]
v_mfma_f32_16x16x32_bf16 v[176:179],v[120:123],a[168:171],v[176:179]
v_mfma_f32_16x16x32_bf16 v[176:179],v[124:127],a[184:187],v[176:179]
v_mfma_f32_16x16x32_bf16 v[184:187],v[124:127],a[188:191],v[184:187]
buffer_load_dwordx4 v27,s[48:51],s83 offen offset:3072 lds
ds_read_b64_tr_b16 v[124:125],v2 offset:30752
v_mfma_f32_16x16x32_bf16 v[184:187],v[120:123],a[172:175],v[184:187]
s_add_u32 m0,s86,86016
ds_read_b64_tr_b16 v[120:121],v2 offset:20512
ds_read_b64_tr_b16 v[122:123],v2 offset:25632
v_mfma_f32_16x16x32_bf16 v[184:187],v[116:119],a[156:159],v[184:187]
ds_read_b64_tr_b16 v[116:117],v2 offset:10272
v_mfma_f32_16x16x32_bf16 v[184:187],v[112:115],a[140:143],v[184:187]
ds_read_b64_tr_b16 v[112:113],v2 offset:32
v_mfma_f32_16x16x32_bf16 v[192:195],v[64:67],a[128:131],v[192:195]
ds_read_b64_tr_b16 v[114:115],v2 offset:5152
v_mfma_f32_16x16x32_bf16 v[192:195],v[68:71],a[144:147],v[192:195]
ds_read_b64_tr_b16 v[118:119],v2 offset:15392
ds_read_b64_tr_b16 v[126:127],v2 offset:35872
v_mfma_f32_16x16x32_bf16 v[192:195],v[72:75],a[160:163],v[192:195]
buffer_load_dwordx4 v7,s[48:51],s83 offen lds
v_mfma_f32_16x16x32_bf16 v[192:195],v[76:79],a[176:179],v[192:195]
s_add_u32 m0,s86,87040
s_add_u32 s83,s64,2
s_and_b32 s83,s83,3
v_mfma_f32_16x16x32_bf16 v[200:203],v[76:79],a[180:183],v[200:203]
s_lshl_b32 s83,s83,1
s_or_b32 s83,s83,1
s_mul_i32 s83,s83,163840
v_mfma_f32_16x16x32_bf16 v[200:203],v[72:75],a[164:167],v[200:203]
s_add_u32 s83,s83,s85
v_mfma_f32_16x16x32_bf16 v[200:203],v[68:71],a[148:151],v[200:203]
v_mfma_f32_16x16x32_bf16 v[200:203],v[64:67],a[132:135],v[200:203]
v_mfma_f32_16x16x32_bf16 v[208:211],v[64:67],a[136:139],v[208:211]
buffer_load_dwordx4 v24,s[48:51],s84 offen lds
v_mfma_f32_16x16x32_bf16 v[208:211],v[68:71],a[152:155],v[208:211]
v_mfma_f32_16x16x32_bf16 v[208:211],v[72:75],a[168:171],v[208:211]
v_mfma_f32_16x16x32_bf16 v[208:211],v[76:79],a[184:187],v[208:211]
v_mfma_f32_16x16x32_bf16 v[216:219],v[76:79],a[188:191],v[216:219]
ds_read_b64_tr_b16 v[76:77],v1 offset:30784
ds_read_b64_tr_b16 v[78:79],v1 offset:35904
v_mfma_f32_16x16x32_bf16 v[216:219],v[72:75],a[172:175],v[216:219]
ds_read_b64_tr_b16 v[72:73],v1 offset:20544
v_mfma_f32_16x16x32_bf16 v[216:219],v[68:71],a[156:159],v[216:219]
buffer_load_dwordx2 v[4:5],v3,s[44:47],s91 offen offset:1280 sc1
ds_read_b64_tr_b16 v[68:69],v1 offset:10304
v_mfma_f32_16x16x32_bf16 v[216:219],v[64:67],a[140:143],v[216:219]
s_waitcnt lgkmcnt(4)
ds_read_b64_tr_b16 v[64:65],v1 offset:64
v_mfma_f32_16x16x32_bf16 v[224:227],v[80:83],a[128:131],v[224:227]
ds_read_b64_tr_b16 v[66:67],v1 offset:5184
ds_read_b64_tr_b16 v[70:71],v1 offset:15424
v_mfma_f32_16x16x32_bf16 v[224:227],v[84:87],a[144:147],v[224:227]
ds_read_b64_tr_b16 v[74:75],v1 offset:25664
v_mfma_f32_16x16x32_bf16 v[224:227],v[88:91],a[160:163],v[224:227]
v_mfma_f32_16x16x32_bf16 v[224:227],v[92:95],a[176:179],v[224:227]
v_mfma_f32_16x16x32_bf16 v[232:235],v[92:95],a[180:183],v[232:235]
buffer_load_dwordx4 v25,s[48:51],s84 offen offset:1024 lds
v_mfma_f32_16x16x32_bf16 v[232:235],v[88:91],a[164:167],v[232:235]
v_mfma_f32_16x16x32_bf16 v[232:235],v[84:87],a[148:151],v[232:235]
v_mfma_f32_16x16x32_bf16 v[232:235],v[80:83],a[132:135],v[232:235]
v_mfma_f32_16x16x32_bf16 v[240:243],v[80:83],a[136:139],v[240:243]
v_mfma_f32_16x16x32_bf16 v[240:243],v[84:87],a[152:155],v[240:243]
v_mfma_f32_16x16x32_bf16 v[240:243],v[88:91],a[168:171],v[240:243]
buffer_load_dwordx4 v26,s[48:51],s84 offen offset:2048 lds
v_mfma_f32_16x16x32_bf16 v[240:243],v[92:95],a[184:187],v[240:243]
v_mfma_f32_16x16x32_bf16 v[248:251],v[92:95],a[188:191],v[248:251]
ds_read_b64_tr_b16 v[92:93],v2 offset:30816
ds_read_b64_tr_b16 v[94:95],v2 offset:35936
v_mfma_f32_16x16x32_bf16 v[248:251],v[88:91],a[172:175],v[248:251]
ds_read_b64_tr_b16 v[88:89],v2 offset:20576
v_mfma_f32_16x16x32_bf16 v[248:251],v[84:87],a[156:159],v[248:251]
ds_read_b64_tr_b16 v[84:85],v2 offset:10336
v_mfma_f32_16x16x32_bf16 v[248:251],v[80:83],a[140:143],v[248:251]
s_waitcnt vmcnt(25)
ds_read_b64_tr_b16 v[80:81],v2 offset:96
v_mfma_f32_16x16x32_bf16 a[192:195],v[96:99],a[68:71],a[192:195]
buffer_load_dwordx4 v27,s[48:51],s84 offen offset:3072 lds
ds_read_b64_tr_b16 v[82:83],v2 offset:5216
v_mfma_f32_16x16x32_bf16 a[192:195],v[100:103],a[76:79],a[192:195]
s_add_u32 m0,s86,91136
ds_read_b64_tr_b16 v[86:87],v2 offset:15456
ds_read_b64_tr_b16 v[90:91],v2 offset:25696
v_mfma_f32_16x16x32_bf16 a[192:195],v[104:107],a[84:87],a[192:195]
v_mfma_f32_16x16x32_bf16 a[192:195],v[108:111],a[92:95],a[192:195]
v_mfma_f32_16x16x32_bf16 a[196:199],v[108:111],a[124:127],a[196:199]
v_mfma_f32_16x16x32_bf16 a[196:199],v[104:107],a[116:119],a[196:199]
v_mfma_f32_16x16x32_bf16 a[196:199],v[100:103],a[108:111],a[196:199]
buffer_load_dwordx4 v7,s[48:51],s84 offen lds
v_mfma_f32_16x16x32_bf16 a[196:199],v[96:99],a[100:103],a[196:199]
s_add_u32 m0,s86,40960
s_add_u32 s84,s83,20480
v_mfma_f32_16x16x32_bf16 a[200:203],v[96:99],a[64:67],a[200:203]
s_waitcnt vmcnt(25)
v_mfma_f32_16x16x32_bf16 a[200:203],v[100:103],a[72:75],a[200:203]
s_waitcnt vmcnt(22)
v_mfma_f32_16x16x32_bf16 a[200:203],v[104:107],a[80:83],a[200:203]
s_waitcnt vmcnt(20)
v_mfma_f32_16x16x32_bf16 a[200:203],v[108:111],a[88:91],a[200:203]
s_waitcnt vmcnt(18)
buffer_load_dwordx4 v[32:35],v6,s[28:31],s87 offen sc1
v_mfma_f32_16x16x32_bf16 a[204:207],v[108:111],a[120:123],a[204:207]
s_waitcnt vmcnt(17)
ds_read_b64_tr_b16 v[108:109],v1 offset:30848
ds_read_b64_tr_b16 v[110:111],v1 offset:35968
v_mfma_f32_16x16x32_bf16 a[204:207],v[104:107],a[112:115],a[204:207]
s_waitcnt vmcnt(16)
ds_read_b64_tr_b16 v[104:105],v1 offset:20608
v_mfma_f32_16x16x32_bf16 a[204:207],v[100:103],a[104:107],a[204:207]
s_waitcnt vmcnt(14)
ds_read_b64_tr_b16 v[100:101],v1 offset:10368
v_mfma_f32_16x16x32_bf16 a[204:207],v[96:99],a[96:99],a[204:207]
ds_read_b64_tr_b16 v[96:97],v1 offset:128
v_mfma_f32_16x16x32_bf16 a[208:211],v[112:115],a[68:71],a[208:211]
buffer_load_dwordx4 v[132:135],v6,s[28:31],s87 offen offset:1024 sc1
ds_read_b64_tr_b16 v[98:99],v1 offset:5248
)A"
R"A(v_mfma_f32_16x16x32_bf16 a[208:211],v[116:119],a[76:79],a[208:211]
ds_read_b64_tr_b16 v[102:103],v1 offset:15488
ds_read_b64_tr_b16 v[106:107],v1 offset:25728
v_mfma_f32_16x16x32_bf16 a[208:211],v[120:123],a[84:87],a[208:211]
v_mfma_f32_16x16x32_bf16 a[208:211],v[124:127],a[92:95],a[208:211]
v_mfma_f32_16x16x32_bf16 a[212:215],v[124:127],a[124:127],a[212:215]
v_mfma_f32_16x16x32_bf16 a[212:215],v[120:123],a[116:119],a[212:215]
buffer_load_dwordx4 v[196:199],v6,s[28:31],s87 offen offset:2048 sc1
v_mfma_f32_16x16x32_bf16 a[212:215],v[116:119],a[108:111],a[212:215]
s_nop 0
L_pd16_%=:
s_cbranch_execz L_trtg2F_%=
v_mfma_f32_16x16x32_bf16 a[212:215],v[112:115],a[100:103],a[212:215]
v_mfma_f32_16x16x32_bf16 a[216:219],v[112:115],a[64:67],a[216:219]
v_mfma_f32_16x16x32_bf16 a[216:219],v[116:119],a[72:75],a[216:219]
v_mfma_f32_16x16x32_bf16 a[216:219],v[120:123],a[80:83],a[216:219]
buffer_load_dwordx4 v[36:39],v6,s[28:31],s88 offen sc1
v_mfma_f32_16x16x32_bf16 a[216:219],v[124:127],a[88:91],a[216:219]
v_mfma_f32_16x16x32_bf16 a[220:223],v[124:127],a[120:123],a[220:223]
ds_read_b64_tr_b16 v[124:125],v2 offset:30880
ds_read_b64_tr_b16 v[126:127],v2 offset:36000
v_mfma_f32_16x16x32_bf16 a[220:223],v[120:123],a[112:115],a[220:223]
ds_read_b64_tr_b16 v[120:121],v2 offset:20640
v_mfma_f32_16x16x32_bf16 a[220:223],v[116:119],a[104:107],a[220:223]
ds_read_b64_tr_b16 v[116:117],v2 offset:10400
v_mfma_f32_16x16x32_bf16 a[220:223],v[112:115],a[96:99],a[220:223]
s_waitcnt lgkmcnt(4)
buffer_load_dwordx4 v[140:143],v28,s[28:31],s87 offen offset:3072 sc1
ds_read_b64_tr_b16 v[112:113],v2 offset:160
v_mfma_f32_16x16x32_bf16 a[224:227],v[64:67],a[68:71],a[224:227]
s_add_u32 s87,s64,1
s_and_b32 s87,s87,3
s_lshl_b32 s87,s87,1
v_mfma_f32_16x16x32_bf16 a[224:227],v[68:71],a[76:79],a[224:227]
s_or_b32 s87,s87,1
s_lshl_b32 s87,s87,16
ds_read_b64_tr_b16 v[114:115],v2 offset:5280
v_mfma_f32_16x16x32_bf16 a[224:227],v[72:75],a[84:87],a[224:227]
ds_read_b64_tr_b16 v[118:119],v2 offset:15520
ds_read_b64_tr_b16 v[122:123],v2 offset:25760
v_mfma_f32_16x16x32_bf16 a[224:227],v[76:79],a[92:95],a[224:227]
v_mfma_f32_16x16x32_bf16 a[228:231],v[76:79],a[124:127],a[228:231]
buffer_load_dwordx4 v[212:215],v6,s[28:31],s88 offen offset:2048 sc1
v_mfma_f32_16x16x32_bf16 a[228:231],v[72:75],a[116:119],a[228:231]
v_mfma_f32_16x16x32_bf16 a[228:231],v[68:71],a[108:111],a[228:231]
v_mfma_f32_16x16x32_bf16 a[228:231],v[64:67],a[100:103],a[228:231]
v_mfma_f32_16x16x32_bf16 a[232:235],v[64:67],a[64:67],a[232:235]
v_mfma_f32_16x16x32_bf16 a[232:235],v[68:71],a[72:75],a[232:235]
buffer_load_dwordx4 v[148:151],v6,s[28:31],s88 offen offset:1024 sc1
v_mfma_f32_16x16x32_bf16 a[232:235],v[72:75],a[80:83],a[232:235]
s_waitcnt vmcnt(11)
v_cmp_ne_u32 s[56:57],s52,v4
v_cmp_ne_u32 s[58:59],s53,v5
v_mfma_f32_16x16x32_bf16 a[232:235],v[76:79],a[88:91],a[232:235]
s_or_b64 s[56:57],s[56:57],s[58:59]
s_cbranch_scc1 L_fr2_%=
L_fo2_%=:
v_mov_b32 v3,0
v_mfma_f32_16x16x32_bf16 a[236:239],v[76:79],a[120:123],a[236:239]
s_add_u32 s91,s64,3
ds_read_b64_tr_b16 v[76:77],v1 offset:30912
ds_read_b64_tr_b16 v[78:79],v1 offset:36032
v_mfma_f32_16x16x32_bf16 a[236:239],v[72:75],a[112:115],a[236:239]
s_and_b32 s91,s91,3
s_add_u32 s91,s91,s69
ds_read_b64_tr_b16 v[72:73],v1 offset:20672
v_mfma_f32_16x16x32_bf16 a[236:239],v[68:71],a[104:107],a[236:239]
s_add_u32 s91,s91,8
buffer_load_dwordx4 v[40:43],v6,s[28:31],s89 offen sc1
ds_read_b64_tr_b16 v[68:69],v1 offset:10432
v_mfma_f32_16x16x32_bf16 a[236:239],v[64:67],a[96:99],a[236:239]
s_lshl_b32 s91,s91,6
s_and_b32 s69,s63,s60
ds_read_b64_tr_b16 v[64:65],v1 offset:192
v_mfma_f32_16x16x32_bf16 a[240:243],v[80:83],a[68:71],a[240:243]
ds_read_b64_tr_b16 v[66:67],v1 offset:5312
ds_read_b64_tr_b16 v[70:71],v1 offset:15552
v_mfma_f32_16x16x32_bf16 a[240:243],v[84:87],a[76:79],a[240:243]
s_cmp_eq_u32 s69,3
s_cselect_b32 s69,0x40000000,0
ds_read_b64_tr_b16 v[74:75],v1 offset:25792
v_mfma_f32_16x16x32_bf16 a[240:243],v[88:91],a[84:87],a[240:243]
s_add_u32 s68,s68,s69
v_mfma_f32_16x16x32_bf16 a[240:243],v[92:95],a[92:95],a[240:243]
buffer_load_dwordx4 a[128:131],v6,s[28:31],s87 offen sc1
v_mfma_f32_16x16x32_bf16 a[244:247],v[92:95],a[124:127],a[244:247]
v_mfma_f32_16x16x32_bf16 a[244:247],v[88:91],a[116:119],a[244:247]
v_mfma_f32_16x16x32_bf16 a[244:247],v[84:87],a[108:111],a[244:247]
v_mfma_f32_16x16x32_bf16 a[244:247],v[80:83],a[100:103],a[244:247]
v_mfma_f32_16x16x32_bf16 a[248:251],v[80:83],a[64:67],a[248:251]
buffer_load_dwordx4 v[156:159],v28,s[28:31],s88 offen offset:3072 sc1
v_mfma_f32_16x16x32_bf16 a[248:251],v[84:87],a[72:75],a[248:251]
s_add_u32 s88,s87,4096
v_mfma_f32_16x16x32_bf16 a[248:251],v[88:91],a[80:83],a[248:251]
v_mfma_f32_16x16x32_bf16 a[248:251],v[92:95],a[88:91],a[248:251]
v_mfma_f32_16x16x32_bf16 a[252:255],v[92:95],a[120:123],a[252:255]
ds_read_b64_tr_b16 v[92:93],v2 offset:30944
ds_read_b64_tr_b16 v[94:95],v2 offset:36064
v_mfma_f32_16x16x32_bf16 a[252:255],v[88:91],a[112:115],a[252:255]
buffer_load_dwordx4 v[228:231],v6,s[28:31],s89 offen offset:2048 sc1
ds_read_b64_tr_b16 v[88:89],v2 offset:20704
v_mfma_f32_16x16x32_bf16 a[252:255],v[84:87],a[104:107],a[252:255]
ds_read_b64_tr_b16 v[84:85],v2 offset:10464
v_mfma_f32_16x16x32_bf16 a[252:255],v[80:83],a[96:99],a[252:255]
ds_read_b64_tr_b16 v[80:81],v2 offset:224
v_mfma_f32_16x16x32_bf16 a[0:3],v[96:99],a[68:71],a[0:3]
ds_read_b64_tr_b16 v[82:83],v2 offset:5344
ds_read_b64_tr_b16 v[86:87],v2 offset:15584
v_mfma_f32_16x16x32_bf16 a[0:3],v[100:103],a[76:79],a[0:3]
ds_read_b64_tr_b16 v[90:91],v2 offset:25824
v_mfma_f32_16x16x32_bf16 a[0:3],v[104:107],a[84:87],a[0:3]
buffer_load_dwordx4 v[164:167],v6,s[28:31],s89 offen offset:1024 sc1
v_mfma_f32_16x16x32_bf16 a[0:3],v[108:111],a[92:95],a[0:3]
v_mfma_f32_16x16x32_bf16 a[4:7],v[108:111],a[124:127],a[4:7]
v_mfma_f32_16x16x32_bf16 a[4:7],v[104:107],a[116:119],a[4:7]
v_mfma_f32_16x16x32_bf16 a[4:7],v[100:103],a[108:111],a[4:7]
v_mfma_f32_16x16x32_bf16 a[4:7],v[96:99],a[100:103],a[4:7]
buffer_load_dwordx4 v[44:47],v6,s[28:31],s90 offen sc1
v_mfma_f32_16x16x32_bf16 a[8:11],v[96:99],a[64:67],a[8:11]
v_mfma_f32_16x16x32_bf16 a[8:11],v[100:103],a[72:75],a[8:11]
v_mfma_f32_16x16x32_bf16 a[8:11],v[104:107],a[80:83],a[8:11]
v_mfma_f32_16x16x32_bf16 a[8:11],v[108:111],a[88:91],a[8:11]
v_mfma_f32_16x16x32_bf16 a[12:15],v[108:111],a[120:123],a[12:15]
buffer_load_dwordx4 a[144:147],v6,s[28:31],s88 offen sc1
ds_read_b64_tr_b16 v[108:109],v1 offset:30976
v_mfma_f32_16x16x32_bf16 a[12:15],v[104:107],a[112:115],a[12:15]
ds_read_b64_tr_b16 v[104:105],v1 offset:20736
ds_read_b64_tr_b16 v[106:107],v1 offset:25856
v_mfma_f32_16x16x32_bf16 a[12:15],v[100:103],a[104:107],a[12:15]
ds_read_b64_tr_b16 v[100:101],v1 offset:10496
v_mfma_f32_16x16x32_bf16 a[12:15],v[96:99],a[96:99],a[12:15]
s_waitcnt lgkmcnt(4)
ds_read_b64_tr_b16 v[96:97],v1 offset:256
v_mfma_f32_16x16x32_bf16 a[16:19],v[112:115],a[68:71],a[16:19]
ds_read_b64_tr_b16 v[98:99],v1 offset:5376
v_mfma_f32_16x16x32_bf16 a[16:19],v[116:119],a[76:79],a[16:19]
buffer_load_dwordx4 v[172:175],v28,s[28:31],s89 offen offset:3072 sc1
ds_read_b64_tr_b16 v[102:103],v1 offset:15616
v_mfma_f32_16x16x32_bf16 a[16:19],v[120:123],a[84:87],a[16:19]
s_add_u32 s89,s87,8192
ds_read_b64_tr_b16 v[110:111],v1 offset:36096
v_add_u32 v1,0x1e000,v1
v_mfma_f32_16x16x32_bf16 a[16:19],v[124:127],a[92:95],a[16:19]
v_mfma_f32_16x16x32_bf16 a[20:23],v[124:127],a[124:127],a[20:23]
v_mfma_f32_16x16x32_bf16 a[20:23],v[120:123],a[116:119],a[20:23]
v_mfma_f32_16x16x32_bf16 a[20:23],v[116:119],a[108:111],a[20:23]
buffer_load_dwordx4 v[180:183],v6,s[28:31],s90 offen offset:1024 sc1
v_mfma_f32_16x16x32_bf16 a[20:23],v[112:115],a[100:103],a[20:23]
v_mfma_f32_16x16x32_bf16 a[24:27],v[112:115],a[64:67],a[24:27]
v_mfma_f32_16x16x32_bf16 a[24:27],v[116:119],a[72:75],a[24:27]
v_mfma_f32_16x16x32_bf16 a[24:27],v[120:123],a[80:83],a[24:27]
v_mfma_f32_16x16x32_bf16 a[24:27],v[124:127],a[88:91],a[24:27]
buffer_load_dwordx4 a[160:163],v6,s[28:31],s89 offen sc1
v_mfma_f32_16x16x32_bf16 a[28:31],v[124:127],a[120:123],a[28:31]
ds_read_b64_tr_b16 v[124:125],v2 offset:31008
ds_read_b64_tr_b16 v[126:127],v2 offset:36128
v_mfma_f32_16x16x32_bf16 a[28:31],v[120:123],a[112:115],a[28:31]
ds_read_b64_tr_b16 v[120:121],v2 offset:20768
v_mfma_f32_16x16x32_bf16 a[28:31],v[116:119],a[104:107],a[28:31]
ds_read_b64_tr_b16 v[116:117],v2 offset:10528
v_mfma_f32_16x16x32_bf16 a[28:31],v[112:115],a[96:99],a[28:31]
ds_read_b64_tr_b16 v[112:113],v2 offset:288
v_mfma_f32_16x16x32_bf16 v[128:131],v[64:67],a[68:71],v[128:131]
buffer_load_dwordx4 v[244:247],v6,s[28:31],s90 offen offset:2048 sc1
ds_read_b64_tr_b16 v[114:115],v2 offset:5408
v_mfma_f32_16x16x32_bf16 v[128:131],v[68:71],a[76:79],v[128:131]
ds_read_b64_tr_b16 v[118:119],v2 offset:15648
ds_read_b64_tr_b16 v[122:123],v2 offset:25888
v_mfma_f32_16x16x32_bf16 v[128:131],v[72:75],a[84:87],v[128:131]
v_add_u32 v2,0x1e000,v2
v_mfma_f32_16x16x32_bf16 v[128:131],v[76:79],a[92:95],v[128:131]
v_mfma_f32_16x16x32_bf16 v[136:139],v[76:79],a[124:127],v[136:139]
v_mfma_f32_16x16x32_bf16 v[136:139],v[72:75],a[116:119],v[136:139]
buffer_load_dwordx4 v[188:191],v28,s[28:31],s90 offen offset:3072 sc1
v_mfma_f32_16x16x32_bf16 v[136:139],v[68:71],a[108:111],v[136:139]
s_add_u32 s90,s87,12288
v_mfma_f32_16x16x32_bf16 v[136:139],v[64:67],a[100:103],v[136:139]
v_mfma_f32_16x16x32_bf16 v[144:147],v[64:67],a[64:67],v[144:147]
v_mfma_f32_16x16x32_bf16 v[144:147],v[68:71],a[72:75],v[144:147]
v_mfma_f32_16x16x32_bf16 v[144:147],v[72:75],a[80:83],v[144:147]
buffer_load_dwordx4 a[176:179],v6,s[28:31],s90 offen sc1
v_mfma_f32_16x16x32_bf16 v[144:147],v[76:79],a[88:91],v[144:147]
v_mfma_f32_16x16x32_bf16 v[152:155],v[76:79],a[120:123],v[152:155]
v_mfma_f32_16x16x32_bf16 v[152:155],v[72:75],a[112:115],v[152:155]
v_mfma_f32_16x16x32_bf16 v[152:155],v[68:71],a[104:107],v[152:155]
v_mfma_f32_16x16x32_bf16 v[152:155],v[64:67],a[96:99],v[152:155]
buffer_load_dwordx4 a[180:183],v6,s[28:31],s90 offen offset:1024 sc1
s_waitcnt vmcnt(21) lgkmcnt(0)
s_barrier
v_mfma_f32_16x16x32_bf16 v[160:163],v[80:83],a[68:71],v[160:163]
ds_read_b64_tr_b16 v[64:65],v1
ds_read_b64_tr_b16 v[66:67],v1 offset:5120
v_mfma_f32_16x16x32_bf16 v[160:163],v[84:87],a[76:79],v[160:163]
ds_read_b64_tr_b16 v[68:69],v1 offset:10240
v_mfma_f32_16x16x32_bf16 v[160:163],v[88:91],a[84:87],v[160:163]
ds_read_b64_tr_b16 v[70:71],v1 offset:15360
v_mfma_f32_16x16x32_bf16 v[160:163],v[92:95],a[92:95],v[160:163]
buffer_load_dwordx4 a[164:167],v6,s[28:31],s89 offen offset:1024 sc1
ds_read_b64_tr_b16 v[72:73],v1 offset:20480
v_mfma_f32_16x16x32_bf16 v[168:171],v[92:95],a[124:127],v[168:171]
ds_read_b64_tr_b16 v[74:75],v1 offset:25600
ds_read_b64_tr_b16 v[76:77],v1 offset:30720
v_mfma_f32_16x16x32_bf16 v[168:171],v[88:91],a[116:119],v[168:171]
ds_read_b64_tr_b16 v[78:79],v1 offset:35840
v_mfma_f32_16x16x32_bf16 v[168:171],v[84:87],a[108:111],v[168:171]
v_mfma_f32_16x16x32_bf16 v[168:171],v[80:83],a[100:103],v[168:171]
v_mfma_f32_16x16x32_bf16 v[176:179],v[80:83],a[64:67],v[176:179]
v_mfma_f32_16x16x32_bf16 v[176:179],v[84:87],a[72:75],v[176:179]
buffer_load_dwordx4 v24,s[48:51],s83 offen lds
v_mfma_f32_16x16x32_bf16 v[176:179],v[88:91],a[80:83],v[176:179]
v_mfma_f32_16x16x32_bf16 v[176:179],v[92:95],a[88:91],v[176:179]
v_mfma_f32_16x16x32_bf16 v[184:187],v[92:95],a[120:123],v[184:187]
ds_read_b64_tr_b16 v[92:93],v2 offset:30752
ds_read_b64_tr_b16 v[94:95],v2 offset:35872
v_mfma_f32_16x16x32_bf16 v[184:187],v[88:91],a[112:115],v[184:187]
ds_read_b64_tr_b16 v[88:89],v2 offset:20512
v_mfma_f32_16x16x32_bf16 v[184:187],v[84:87],a[104:107],v[184:187]
buffer_load_dwordx4 a[148:151],v6,s[28:31],s88 offen offset:1024 sc1
ds_read_b64_tr_b16 v[84:85],v2 offset:10272
v_mfma_f32_16x16x32_bf16 v[184:187],v[80:83],a[96:99],v[184:187]
ds_read_b64_tr_b16 v[80:81],v2 offset:32
v_mfma_f32_16x16x32_bf16 v[192:195],v[96:99],a[68:71],v[192:195]
ds_read_b64_tr_b16 v[82:83],v2 offset:5152
ds_read_b64_tr_b16 v[86:87],v2 offset:15392
v_mfma_f32_16x16x32_bf16 v[192:195],v[100:103],a[76:79],v[192:195]
)A"
R"A(ds_read_b64_tr_b16 v[90:91],v2 offset:25632
v_mfma_f32_16x16x32_bf16 v[192:195],v[104:107],a[84:87],v[192:195]
v_mfma_f32_16x16x32_bf16 v[192:195],v[108:111],a[92:95],v[192:195]
v_mfma_f32_16x16x32_bf16 v[200:203],v[108:111],a[124:127],v[200:203]
buffer_load_dwordx4 v25,s[48:51],s83 offen offset:1024 lds
v_mfma_f32_16x16x32_bf16 v[200:203],v[104:107],a[116:119],v[200:203]
v_mfma_f32_16x16x32_bf16 v[200:203],v[100:103],a[108:111],v[200:203]
v_mfma_f32_16x16x32_bf16 v[200:203],v[96:99],a[100:103],v[200:203]
v_mfma_f32_16x16x32_bf16 v[208:211],v[96:99],a[64:67],v[208:211]
v_mfma_f32_16x16x32_bf16 v[208:211],v[100:103],a[72:75],v[208:211]
buffer_load_dwordx4 a[132:135],v6,s[28:31],s87 offen offset:1024 sc1
v_mfma_f32_16x16x32_bf16 v[208:211],v[104:107],a[80:83],v[208:211]
v_mfma_f32_16x16x32_bf16 v[208:211],v[108:111],a[88:91],v[208:211]
v_mfma_f32_16x16x32_bf16 v[216:219],v[108:111],a[120:123],v[216:219]
ds_read_b64_tr_b16 v[108:109],v1 offset:30784
ds_read_b64_tr_b16 v[110:111],v1 offset:35904
v_mfma_f32_16x16x32_bf16 v[216:219],v[104:107],a[112:115],v[216:219]
ds_read_b64_tr_b16 v[104:105],v1 offset:20544
v_mfma_f32_16x16x32_bf16 v[216:219],v[100:103],a[104:107],v[216:219]
ds_read_b64_tr_b16 v[100:101],v1 offset:10304
v_mfma_f32_16x16x32_bf16 v[216:219],v[96:99],a[96:99],v[216:219]
buffer_load_dwordx4 v26,s[48:51],s83 offen offset:2048 lds
ds_read_b64_tr_b16 v[96:97],v1 offset:64
v_mfma_f32_16x16x32_bf16 v[224:227],v[112:115],a[68:71],v[224:227]
ds_read_b64_tr_b16 v[98:99],v1 offset:5184
ds_read_b64_tr_b16 v[102:103],v1 offset:15424
v_mfma_f32_16x16x32_bf16 v[224:227],v[116:119],a[76:79],v[224:227]
ds_read_b64_tr_b16 v[106:107],v1 offset:25664
v_mfma_f32_16x16x32_bf16 v[224:227],v[120:123],a[84:87],v[224:227]
v_mfma_f32_16x16x32_bf16 v[224:227],v[124:127],a[92:95],v[224:227]
v_mfma_f32_16x16x32_bf16 v[232:235],v[124:127],a[124:127],v[232:235]
v_mfma_f32_16x16x32_bf16 v[232:235],v[120:123],a[116:119],v[232:235]
buffer_load_dwordx4 a[136:139],v6,s[28:31],s87 offen offset:2048 sc1
v_mfma_f32_16x16x32_bf16 v[232:235],v[116:119],a[108:111],v[232:235]
v_mfma_f32_16x16x32_bf16 v[232:235],v[112:115],a[100:103],v[232:235]
v_mfma_f32_16x16x32_bf16 v[240:243],v[112:115],a[64:67],v[240:243]
v_mfma_f32_16x16x32_bf16 v[240:243],v[116:119],a[72:75],v[240:243]
v_mfma_f32_16x16x32_bf16 v[240:243],v[120:123],a[80:83],v[240:243]
v_mfma_f32_16x16x32_bf16 v[240:243],v[124:127],a[88:91],v[240:243]
buffer_load_dwordx4 v27,s[48:51],s83 offen offset:3072 lds
v_mfma_f32_16x16x32_bf16 v[248:251],v[124:127],a[120:123],v[248:251]
s_add_u32 m0,s86,45056
ds_read_b64_tr_b16 v[124:125],v2 offset:30816
ds_read_b64_tr_b16 v[126:127],v2 offset:35936
v_mfma_f32_16x16x32_bf16 v[248:251],v[120:123],a[112:115],v[248:251]
ds_read_b64_tr_b16 v[120:121],v2 offset:20576
v_mfma_f32_16x16x32_bf16 v[248:251],v[116:119],a[104:107],v[248:251]
ds_read_b64_tr_b16 v[116:117],v2 offset:10336
v_mfma_f32_16x16x32_bf16 v[248:251],v[112:115],a[96:99],v[248:251]
s_waitcnt vmcnt(21) lgkmcnt(4)
ds_read_b64_tr_b16 v[112:113],v2 offset:96
v_mfma_f32_16x16x32_bf16 a[192:195],v[64:67],v[32:35],a[192:195]
ds_read_b64_tr_b16 v[114:115],v2 offset:5216
ds_read_b64_tr_b16 v[118:119],v2 offset:15456
v_mfma_f32_16x16x32_bf16 a[192:195],v[68:71],v[36:39],a[192:195]
buffer_load_dwordx4 v7,s[48:51],s83 offen lds
ds_read_b64_tr_b16 v[122:123],v2 offset:25696
v_mfma_f32_16x16x32_bf16 a[192:195],v[72:75],v[40:43],a[192:195]
s_add_u32 m0,s86,46080
s_add_u32 s83,s64,3
s_and_b32 s83,s83,3
s_waitcnt vmcnt(17)
v_mfma_f32_16x16x32_bf16 a[192:195],v[76:79],v[44:47],a[192:195]
s_lshl_b32 s83,s83,1
s_or_b32 s83,s83,17
s_mul_i32 s83,s83,163840
s_waitcnt vmcnt(14)
v_mfma_f32_16x16x32_bf16 a[196:199],v[76:79],v[180:183],a[196:199]
s_add_u32 s83,s83,s85
v_mfma_f32_16x16x32_bf16 a[196:199],v[72:75],v[164:167],a[196:199]
v_mfma_f32_16x16x32_bf16 a[196:199],v[68:71],v[148:151],a[196:199]
buffer_load_dwordx4 a[152:155],v6,s[28:31],s88 offen offset:2048 sc1
v_mfma_f32_16x16x32_bf16 a[196:199],v[64:67],v[132:135],a[196:199]
v_mfma_f32_16x16x32_bf16 a[200:203],v[64:67],v[196:199],a[200:203]
v_mfma_f32_16x16x32_bf16 a[200:203],v[68:71],v[212:215],a[200:203]
v_mfma_f32_16x16x32_bf16 a[200:203],v[72:75],v[228:231],a[200:203]
s_waitcnt vmcnt(13)
v_mfma_f32_16x16x32_bf16 a[200:203],v[76:79],v[244:247],a[200:203]
s_waitcnt vmcnt(12)
v_mfma_f32_16x16x32_bf16 a[204:207],v[76:79],v[188:191],a[204:207]
buffer_load_dwordx4 v24,s[48:51],s84 offen lds
ds_read_b64_tr_b16 v[76:77],v1 offset:30848
v_mfma_f32_16x16x32_bf16 a[204:207],v[72:75],v[172:175],a[204:207]
ds_read_b64_tr_b16 v[72:73],v1 offset:20608
ds_read_b64_tr_b16 v[74:75],v1 offset:25728
v_mfma_f32_16x16x32_bf16 a[204:207],v[68:71],v[156:159],a[204:207]
ds_read_b64_tr_b16 v[68:69],v1 offset:10368
v_mfma_f32_16x16x32_bf16 a[204:207],v[64:67],v[140:143],a[204:207]
ds_read_b64_tr_b16 v[64:65],v1 offset:128
v_mfma_f32_16x16x32_bf16 a[208:211],v[80:83],v[32:35],a[208:211]
ds_read_b64_tr_b16 v[66:67],v1 offset:5248
v_mfma_f32_16x16x32_bf16 a[208:211],v[84:87],v[36:39],a[208:211]
buffer_load_dwordx4 a[168:171],v6,s[28:31],s89 offen offset:2048 sc1
ds_read_b64_tr_b16 v[70:71],v1 offset:15488
v_mfma_f32_16x16x32_bf16 a[208:211],v[88:91],v[40:43],a[208:211]
ds_read_b64_tr_b16 v[78:79],v1 offset:35968
v_mfma_f32_16x16x32_bf16 a[208:211],v[92:95],v[44:47],a[208:211]
v_mfma_f32_16x16x32_bf16 a[212:215],v[92:95],v[180:183],a[212:215]
v_mfma_f32_16x16x32_bf16 a[212:215],v[88:91],v[164:167],a[212:215]
v_mfma_f32_16x16x32_bf16 a[212:215],v[84:87],v[148:151],a[212:215]
v_mfma_f32_16x16x32_bf16 a[212:215],v[80:83],v[132:135],a[212:215]
buffer_load_dwordx4 v25,s[48:51],s84 offen offset:1024 lds
v_mfma_f32_16x16x32_bf16 a[216:219],v[80:83],v[196:199],a[216:219]
v_mfma_f32_16x16x32_bf16 a[216:219],v[84:87],v[212:215],a[216:219]
v_mfma_f32_16x16x32_bf16 a[216:219],v[88:91],v[228:231],a[216:219]
v_mfma_f32_16x16x32_bf16 a[216:219],v[92:95],v[244:247],a[216:219]
v_mfma_f32_16x16x32_bf16 a[220:223],v[92:95],v[188:191],a[220:223]
buffer_load_dwordx4 a[184:187],v6,s[28:31],s90 offen offset:2048 sc1
ds_read_b64_tr_b16 v[92:93],v2 offset:30880
v_mfma_f32_16x16x32_bf16 a[220:223],v[88:91],v[172:175],a[220:223]
ds_read_b64_tr_b16 v[88:89],v2 offset:20640
ds_read_b64_tr_b16 v[90:91],v2 offset:25760
v_mfma_f32_16x16x32_bf16 a[220:223],v[84:87],v[156:159],a[220:223]
ds_read_b64_tr_b16 v[84:85],v2 offset:10400
v_mfma_f32_16x16x32_bf16 a[220:223],v[80:83],v[140:143],a[220:223]
ds_read_b64_tr_b16 v[80:81],v2 offset:160
v_mfma_f32_16x16x32_bf16 a[224:227],v[96:99],v[32:35],a[224:227]
ds_read_b64_tr_b16 v[82:83],v2 offset:5280
v_mfma_f32_16x16x32_bf16 a[224:227],v[100:103],v[36:39],a[224:227]
ds_read_b64_tr_b16 v[86:87],v2 offset:15520
ds_read_b64_tr_b16 v[94:95],v2 offset:36000
v_mfma_f32_16x16x32_bf16 a[224:227],v[104:107],v[40:43],a[224:227]
buffer_load_dwordx4 v26,s[48:51],s84 offen offset:2048 lds
v_mfma_f32_16x16x32_bf16 a[224:227],v[108:111],v[44:47],a[224:227]
v_mfma_f32_16x16x32_bf16 a[228:231],v[108:111],v[180:183],a[228:231]
v_mfma_f32_16x16x32_bf16 a[228:231],v[104:107],v[164:167],a[228:231]
v_mfma_f32_16x16x32_bf16 a[228:231],v[100:103],v[148:151],a[228:231]
v_mfma_f32_16x16x32_bf16 a[228:231],v[96:99],v[132:135],a[228:231]
v_mfma_f32_16x16x32_bf16 a[232:235],v[96:99],v[196:199],a[232:235]
buffer_load_dwordx4 v27,s[48:51],s84 offen offset:3072 lds
v_mfma_f32_16x16x32_bf16 a[232:235],v[100:103],v[212:215],a[232:235]
s_add_u32 m0,s86,50176
v_mfma_f32_16x16x32_bf16 a[232:235],v[104:107],v[228:231],a[232:235]
v_mfma_f32_16x16x32_bf16 a[232:235],v[108:111],v[244:247],a[232:235]
v_mfma_f32_16x16x32_bf16 a[236:239],v[108:111],v[188:191],a[236:239]
ds_read_b64_tr_b16 v[108:109],v1 offset:30912
ds_read_b64_tr_b16 v[110:111],v1 offset:36032
v_mfma_f32_16x16x32_bf16 a[236:239],v[104:107],v[172:175],a[236:239]
buffer_load_dwordx4 a[188:191],v28,s[28:31],s90 offen offset:3072 sc1
ds_read_b64_tr_b16 v[104:105],v1 offset:20672
v_mfma_f32_16x16x32_bf16 a[236:239],v[100:103],v[156:159],a[236:239]
ds_read_b64_tr_b16 v[100:101],v1 offset:10432
v_mfma_f32_16x16x32_bf16 a[236:239],v[96:99],v[140:143],a[236:239]
s_waitcnt lgkmcnt(4)
ds_read_b64_tr_b16 v[96:97],v1 offset:192
v_mfma_f32_16x16x32_bf16 a[240:243],v[112:115],v[32:35],a[240:243]
ds_read_b64_tr_b16 v[98:99],v1 offset:5312
ds_read_b64_tr_b16 v[102:103],v1 offset:15552
v_mfma_f32_16x16x32_bf16 a[240:243],v[116:119],v[36:39],a[240:243]
ds_read_b64_tr_b16 v[106:107],v1 offset:25792
v_mfma_f32_16x16x32_bf16 a[240:243],v[120:123],v[40:43],a[240:243]
v_mfma_f32_16x16x32_bf16 a[240:243],v[124:127],v[44:47],a[240:243]
buffer_load_dwordx4 v7,s[48:51],s84 offen lds
v_mfma_f32_16x16x32_bf16 a[244:247],v[124:127],v[180:183],a[244:247]
s_add_u32 m0,s86,0
s_add_u32 s84,s83,20480
v_mfma_f32_16x16x32_bf16 a[244:247],v[120:123],v[164:167],a[244:247]
v_mfma_f32_16x16x32_bf16 a[244:247],v[116:119],v[148:151],a[244:247]
v_mfma_f32_16x16x32_bf16 a[244:247],v[112:115],v[132:135],a[244:247]
v_mfma_f32_16x16x32_bf16 a[248:251],v[112:115],v[196:199],a[248:251]
buffer_load_dwordx4 a[172:175],v28,s[28:31],s89 offen offset:3072 sc1
v_mfma_f32_16x16x32_bf16 a[248:251],v[116:119],v[212:215],a[248:251]
v_mfma_f32_16x16x32_bf16 a[248:251],v[120:123],v[228:231],a[248:251]
v_mfma_f32_16x16x32_bf16 a[248:251],v[124:127],v[244:247],a[248:251]
v_mfma_f32_16x16x32_bf16 a[252:255],v[124:127],v[188:191],a[252:255]
ds_read_b64_tr_b16 v[124:125],v2 offset:30944
ds_read_b64_tr_b16 v[126:127],v2 offset:36064
v_mfma_f32_16x16x32_bf16 a[252:255],v[120:123],v[172:175],a[252:255]
ds_read_b64_tr_b16 v[120:121],v2 offset:20704
v_mfma_f32_16x16x32_bf16 a[252:255],v[116:119],v[156:159],a[252:255]
buffer_load_dwordx4 v24,s[48:51],s83 offen lds
ds_read_b64_tr_b16 v[116:117],v2 offset:10464
v_mfma_f32_16x16x32_bf16 a[252:255],v[112:115],v[140:143],a[252:255]
ds_read_b64_tr_b16 v[112:113],v2 offset:224
v_mfma_f32_16x16x32_bf16 a[0:3],v[64:67],v[32:35],a[0:3]
ds_read_b64_tr_b16 v[114:115],v2 offset:5344
ds_read_b64_tr_b16 v[118:119],v2 offset:15584
v_mfma_f32_16x16x32_bf16 a[0:3],v[68:71],v[36:39],a[0:3]
ds_read_b64_tr_b16 v[122:123],v2 offset:25824
v_mfma_f32_16x16x32_bf16 a[0:3],v[72:75],v[40:43],a[0:3]
v_mfma_f32_16x16x32_bf16 a[0:3],v[76:79],v[44:47],a[0:3]
buffer_load_dwordx4 a[156:159],v28,s[28:31],s88 offen offset:3072 sc1
v_mfma_f32_16x16x32_bf16 a[4:7],v[76:79],v[180:183],a[4:7]
v_mfma_f32_16x16x32_bf16 a[4:7],v[72:75],v[164:167],a[4:7]
v_mfma_f32_16x16x32_bf16 a[4:7],v[68:71],v[148:151],a[4:7]
v_mfma_f32_16x16x32_bf16 a[4:7],v[64:67],v[132:135],a[4:7]
v_mfma_f32_16x16x32_bf16 a[8:11],v[64:67],v[196:199],a[8:11]
v_mfma_f32_16x16x32_bf16 a[8:11],v[68:71],v[212:215],a[8:11]
buffer_load_dwordx4 v25,s[48:51],s83 offen offset:1024 lds
v_mfma_f32_16x16x32_bf16 a[8:11],v[72:75],v[228:231],a[8:11]
v_mfma_f32_16x16x32_bf16 a[8:11],v[76:79],v[244:247],a[8:11]
v_mfma_f32_16x16x32_bf16 a[12:15],v[76:79],v[188:191],a[12:15]
ds_read_b64_tr_b16 v[76:77],v1 offset:30976
ds_read_b64_tr_b16 v[78:79],v1 offset:36096
v_mfma_f32_16x16x32_bf16 a[12:15],v[72:75],v[172:175],a[12:15]
ds_read_b64_tr_b16 v[72:73],v1 offset:20736
v_mfma_f32_16x16x32_bf16 a[12:15],v[68:71],v[156:159],a[12:15]
ds_read_b64_tr_b16 v[68:69],v1 offset:10496
v_mfma_f32_16x16x32_bf16 a[12:15],v[64:67],v[140:143],a[12:15]
buffer_load_dwordx4 v26,s[48:51],s83 offen offset:2048 lds
ds_read_b64_tr_b16 v[64:65],v1 offset:256
v_mfma_f32_16x16x32_bf16 a[16:19],v[80:83],v[32:35],a[16:19]
ds_read_b64_tr_b16 v[66:67],v1 offset:5376
ds_read_b64_tr_b16 v[70:71],v1 offset:15616
v_mfma_f32_16x16x32_bf16 a[16:19],v[84:87],v[36:39],a[16:19]
ds_read_b64_tr_b16 v[74:75],v1 offset:25856
v_add_u32 v1,0xffff6000,v1
v_mfma_f32_16x16x32_bf16 a[16:19],v[88:91],v[40:43],a[16:19]
v_mfma_f32_16x16x32_bf16 a[16:19],v[92:95],v[44:47],a[16:19]
v_mfma_f32_16x16x32_bf16 a[20:23],v[92:95],v[180:183],a[20:23]
buffer_load_dwordx4 a[140:143],v28,s[28:31],s87 offen offset:3072 sc1
v_mfma_f32_16x16x32_bf16 a[20:23],v[88:91],v[164:167],a[20:23]
s_add_u32 s87,s64,2
s_and_b32 s87,s87,3
s_lshl_b32 s87,s87,1
v_mfma_f32_16x16x32_bf16 a[20:23],v[84:87],v[148:151],a[20:23]
)A"
R"A(s_or_b32 s87,s87,1
s_lshl_b32 s87,s87,16
s_add_u32 s88,s87,4096
v_mfma_f32_16x16x32_bf16 a[20:23],v[80:83],v[132:135],a[20:23]
s_add_u32 s89,s87,8192
s_add_u32 s90,s87,12288
v_mfma_f32_16x16x32_bf16 a[24:27],v[80:83],v[196:199],a[24:27]
v_mfma_f32_16x16x32_bf16 a[24:27],v[84:87],v[212:215],a[24:27]
v_mfma_f32_16x16x32_bf16 a[24:27],v[88:91],v[228:231],a[24:27]
buffer_load_dwordx4 v27,s[48:51],s83 offen offset:3072 lds
v_mfma_f32_16x16x32_bf16 a[24:27],v[92:95],v[244:247],a[24:27]
s_add_u32 m0,s86,4096
v_mfma_f32_16x16x32_bf16 a[28:31],v[92:95],v[188:191],a[28:31]
ds_read_b64_tr_b16 v[92:93],v2 offset:31008
ds_read_b64_tr_b16 v[94:95],v2 offset:36128
v_mfma_f32_16x16x32_bf16 a[28:31],v[88:91],v[172:175],a[28:31]
ds_read_b64_tr_b16 v[88:89],v2 offset:20768
v_mfma_f32_16x16x32_bf16 a[28:31],v[84:87],v[156:159],a[28:31]
ds_read_b64_tr_b16 v[84:85],v2 offset:10528
v_mfma_f32_16x16x32_bf16 a[28:31],v[80:83],v[140:143],a[28:31]
s_waitcnt lgkmcnt(4)
ds_read_b64_tr_b16 v[80:81],v2 offset:288
v_mfma_f32_16x16x32_bf16 v[128:131],v[96:99],v[32:35],v[128:131]
buffer_load_dwordx4 v7,s[48:51],s83 offen lds
ds_read_b64_tr_b16 v[82:83],v2 offset:5408
v_mfma_f32_16x16x32_bf16 v[128:131],v[100:103],v[36:39],v[128:131]
s_add_u32 m0,s86,5120
ds_read_b64_tr_b16 v[86:87],v2 offset:15648
ds_read_b64_tr_b16 v[90:91],v2 offset:25888
v_mfma_f32_16x16x32_bf16 v[128:131],v[104:107],v[40:43],v[128:131]
s_add_u32 s83,s64,3
s_and_b32 s83,s83,3
v_add_u32 v2,0xffff6000,v2
v_mfma_f32_16x16x32_bf16 v[128:131],v[108:111],v[44:47],v[128:131]
s_lshl_b32 s83,s83,1
s_or_b32 s83,s83,1
s_mul_i32 s83,s83,163840
v_mfma_f32_16x16x32_bf16 v[136:139],v[108:111],v[180:183],v[136:139]
s_add_u32 s83,s83,s85
v_mfma_f32_16x16x32_bf16 v[136:139],v[104:107],v[164:167],v[136:139]
v_mfma_f32_16x16x32_bf16 v[136:139],v[100:103],v[148:151],v[136:139]
buffer_load_dwordx4 v24,s[48:51],s84 offen lds
v_mfma_f32_16x16x32_bf16 v[136:139],v[96:99],v[132:135],v[136:139]
v_mfma_f32_16x16x32_bf16 v[144:147],v[96:99],v[196:199],v[144:147]
v_mfma_f32_16x16x32_bf16 v[144:147],v[100:103],v[212:215],v[144:147]
v_mfma_f32_16x16x32_bf16 v[144:147],v[104:107],v[228:231],v[144:147]
v_mfma_f32_16x16x32_bf16 v[144:147],v[108:111],v[244:247],v[144:147]
v_mfma_f32_16x16x32_bf16 v[152:155],v[108:111],v[188:191],v[152:155]
buffer_load_dwordx4 v25,s[48:51],s84 offen offset:1024 lds
ds_read_b64_tr_b16 v[108:109],v1 offset:30720
v_mfma_f32_16x16x32_bf16 v[152:155],v[104:107],v[172:175],v[152:155]
ds_read_b64_tr_b16 v[104:105],v1 offset:20480
ds_read_b64_tr_b16 v[106:107],v1 offset:25600
v_mfma_f32_16x16x32_bf16 v[152:155],v[100:103],v[156:159],v[152:155]
ds_read_b64_tr_b16 v[100:101],v1 offset:10240
v_mfma_f32_16x16x32_bf16 v[152:155],v[96:99],v[140:143],v[152:155]
ds_read_b64_tr_b16 v[96:97],v1
v_mfma_f32_16x16x32_bf16 v[160:163],v[112:115],v[32:35],v[160:163]
ds_read_b64_tr_b16 v[98:99],v1 offset:5120
v_mfma_f32_16x16x32_bf16 v[160:163],v[116:119],v[36:39],v[160:163]
ds_read_b64_tr_b16 v[102:103],v1 offset:15360
ds_read_b64_tr_b16 v[110:111],v1 offset:35840
v_mfma_f32_16x16x32_bf16 v[160:163],v[120:123],v[40:43],v[160:163]
buffer_load_dwordx4 v26,s[48:51],s84 offen offset:2048 lds
v_mfma_f32_16x16x32_bf16 v[160:163],v[124:127],v[44:47],v[160:163]
v_mfma_f32_16x16x32_bf16 v[168:171],v[124:127],v[180:183],v[168:171]
v_mfma_f32_16x16x32_bf16 v[168:171],v[120:123],v[164:167],v[168:171]
v_mfma_f32_16x16x32_bf16 v[168:171],v[116:119],v[148:151],v[168:171]
v_mfma_f32_16x16x32_bf16 v[168:171],v[112:115],v[132:135],v[168:171]
v_mfma_f32_16x16x32_bf16 v[176:179],v[112:115],v[196:199],v[176:179]
buffer_load_dwordx4 v27,s[48:51],s84 offen offset:3072 lds
v_mfma_f32_16x16x32_bf16 v[176:179],v[116:119],v[212:215],v[176:179]
s_add_u32 m0,s86,9216
v_mfma_f32_16x16x32_bf16 v[176:179],v[120:123],v[228:231],v[176:179]
v_mfma_f32_16x16x32_bf16 v[176:179],v[124:127],v[244:247],v[176:179]
v_mfma_f32_16x16x32_bf16 v[184:187],v[124:127],v[188:191],v[184:187]
ds_read_b64_tr_b16 v[124:125],v2 offset:30752
ds_read_b64_tr_b16 v[126:127],v2 offset:35872
v_mfma_f32_16x16x32_bf16 v[184:187],v[120:123],v[172:175],v[184:187]
ds_read_b64_tr_b16 v[120:121],v2 offset:20512
v_mfma_f32_16x16x32_bf16 v[184:187],v[116:119],v[156:159],v[184:187]
buffer_load_dwordx4 v7,s[48:51],s84 offen lds
ds_read_b64_tr_b16 v[116:117],v2 offset:10272
v_mfma_f32_16x16x32_bf16 v[184:187],v[112:115],v[140:143],v[184:187]
s_add_u32 m0,s86,122880
s_add_u32 s84,s83,20480
ds_read_b64_tr_b16 v[112:113],v2 offset:32
v_mfma_f32_16x16x32_bf16 v[192:195],v[64:67],v[32:35],v[192:195]
ds_read_b64_tr_b16 v[114:115],v2 offset:5152
ds_read_b64_tr_b16 v[118:119],v2 offset:15392
v_mfma_f32_16x16x32_bf16 v[192:195],v[68:71],v[36:39],v[192:195]
ds_read_b64_tr_b16 v[122:123],v2 offset:25632
v_mfma_f32_16x16x32_bf16 v[192:195],v[72:75],v[40:43],v[192:195]
v_mfma_f32_16x16x32_bf16 v[192:195],v[76:79],v[44:47],v[192:195]
buffer_load_dwordx4 a[68:71],v6,s[28:31],s87 offen sc1
v_mfma_f32_16x16x32_bf16 v[200:203],v[76:79],v[180:183],v[200:203]
v_mfma_f32_16x16x32_bf16 v[200:203],v[72:75],v[164:167],v[200:203]
v_mfma_f32_16x16x32_bf16 v[200:203],v[68:71],v[148:151],v[200:203]
v_mfma_f32_16x16x32_bf16 v[200:203],v[64:67],v[132:135],v[200:203]
v_mfma_f32_16x16x32_bf16 v[208:211],v[64:67],v[196:199],v[208:211]
buffer_load_dwordx4 a[100:103],v6,s[28:31],s87 offen offset:1024 sc1
v_mfma_f32_16x16x32_bf16 v[208:211],v[68:71],v[212:215],v[208:211]
v_mfma_f32_16x16x32_bf16 v[208:211],v[72:75],v[228:231],v[208:211]
v_mfma_f32_16x16x32_bf16 v[208:211],v[76:79],v[244:247],v[208:211]
v_mfma_f32_16x16x32_bf16 v[216:219],v[76:79],v[188:191],v[216:219]
ds_read_b64_tr_b16 v[76:77],v1 offset:30784
L_pd17_%=:
s_cbranch_execz L_trtg2F_%=
ds_read_b64_tr_b16 v[78:79],v1 offset:35904
v_mfma_f32_16x16x32_bf16 v[216:219],v[72:75],v[172:175],v[216:219]
buffer_load_dwordx2 v[4:5],v3,s[44:47],s91 offen offset:1280 sc1
ds_read_b64_tr_b16 v[72:73],v1 offset:20544
v_mfma_f32_16x16x32_bf16 v[216:219],v[68:71],v[156:159],v[216:219]
ds_read_b64_tr_b16 v[68:69],v1 offset:10304
v_mfma_f32_16x16x32_bf16 v[216:219],v[64:67],v[140:143],v[216:219]
s_waitcnt lgkmcnt(4)
ds_read_b64_tr_b16 v[64:65],v1 offset:64
v_mfma_f32_16x16x32_bf16 v[224:227],v[80:83],v[32:35],v[224:227]
ds_read_b64_tr_b16 v[66:67],v1 offset:5184
ds_read_b64_tr_b16 v[70:71],v1 offset:15424
v_mfma_f32_16x16x32_bf16 v[224:227],v[84:87],v[36:39],v[224:227]
ds_read_b64_tr_b16 v[74:75],v1 offset:25664
v_mfma_f32_16x16x32_bf16 v[224:227],v[88:91],v[40:43],v[224:227]
buffer_load_dwordx4 a[64:67],v6,s[28:31],s87 offen offset:2048 sc1
v_mfma_f32_16x16x32_bf16 v[224:227],v[92:95],v[44:47],v[224:227]
v_mfma_f32_16x16x32_bf16 v[232:235],v[92:95],v[180:183],v[232:235]
v_mfma_f32_16x16x32_bf16 v[232:235],v[88:91],v[164:167],v[232:235]
v_mfma_f32_16x16x32_bf16 v[232:235],v[84:87],v[148:151],v[232:235]
v_mfma_f32_16x16x32_bf16 v[232:235],v[80:83],v[132:135],v[232:235]
buffer_load_dwordx4 a[76:79],v6,s[28:31],s88 offen sc1
v_mfma_f32_16x16x32_bf16 v[240:243],v[80:83],v[196:199],v[240:243]
v_mfma_f32_16x16x32_bf16 v[240:243],v[84:87],v[212:215],v[240:243]
v_mfma_f32_16x16x32_bf16 v[240:243],v[88:91],v[228:231],v[240:243]
v_mfma_f32_16x16x32_bf16 v[240:243],v[92:95],v[244:247],v[240:243]
v_mfma_f32_16x16x32_bf16 v[248:251],v[92:95],v[188:191],v[248:251]
buffer_load_dwordx4 a[96:99],v28,s[28:31],s87 offen offset:3072 sc1
ds_read_b64_tr_b16 v[92:93],v2 offset:30816
v_mfma_f32_16x16x32_bf16 v[248:251],v[88:91],v[172:175],v[248:251]
s_add_u32 s87,s64,3
ds_read_b64_tr_b16 v[88:89],v2 offset:20576
ds_read_b64_tr_b16 v[90:91],v2 offset:25696
v_mfma_f32_16x16x32_bf16 v[248:251],v[84:87],v[156:159],v[248:251]
s_and_b32 s87,s87,3
s_lshl_b32 s87,s87,1
ds_read_b64_tr_b16 v[84:85],v2 offset:10336
v_mfma_f32_16x16x32_bf16 v[248:251],v[80:83],v[140:143],v[248:251]
s_or_b32 s87,s87,17
s_lshl_b32 s87,s87,16
ds_read_b64_tr_b16 v[80:81],v2 offset:96
v_mfma_f32_16x16x32_bf16 a[192:195],v[96:99],a[128:131],a[192:195]
ds_read_b64_tr_b16 v[82:83],v2 offset:5216
v_mfma_f32_16x16x32_bf16 a[192:195],v[100:103],a[144:147],a[192:195]
buffer_load_dwordx4 a[72:75],v6,s[28:31],s88 offen offset:2048 sc1
ds_read_b64_tr_b16 v[86:87],v2 offset:15456
v_mfma_f32_16x16x32_bf16 a[192:195],v[104:107],a[160:163],a[192:195]
s_waitcnt vmcnt(37)
ds_read_b64_tr_b16 v[94:95],v2 offset:35936
v_mfma_f32_16x16x32_bf16 a[192:195],v[108:111],a[176:179],a[192:195]
v_mfma_f32_16x16x32_bf16 a[196:199],v[108:111],a[180:183],a[196:199]
v_mfma_f32_16x16x32_bf16 a[196:199],v[104:107],a[164:167],a[196:199]
s_waitcnt vmcnt(35)
v_mfma_f32_16x16x32_bf16 a[196:199],v[100:103],a[148:151],a[196:199]
s_waitcnt vmcnt(33)
buffer_load_dwordx4 a[108:111],v6,s[28:31],s88 offen offset:1024 sc1
v_mfma_f32_16x16x32_bf16 a[196:199],v[96:99],a[132:135],a[196:199]
s_waitcnt vmcnt(32)
v_mfma_f32_16x16x32_bf16 a[200:203],v[96:99],a[136:139],a[200:203]
s_waitcnt vmcnt(29)
v_mfma_f32_16x16x32_bf16 a[200:203],v[100:103],a[152:155],a[200:203]
s_waitcnt vmcnt(27)
v_mfma_f32_16x16x32_bf16 a[200:203],v[104:107],a[168:171],a[200:203]
s_waitcnt vmcnt(25)
v_mfma_f32_16x16x32_bf16 a[200:203],v[108:111],a[184:187],a[200:203]
s_waitcnt vmcnt(22)
buffer_load_dwordx4 a[84:87],v6,s[28:31],s89 offen sc1
v_mfma_f32_16x16x32_bf16 a[204:207],v[108:111],a[188:191],a[204:207]
s_waitcnt vmcnt(21)
ds_read_b64_tr_b16 v[108:109],v1 offset:30848
ds_read_b64_tr_b16 v[110:111],v1 offset:35968
v_mfma_f32_16x16x32_bf16 a[204:207],v[104:107],a[172:175],a[204:207]
s_waitcnt vmcnt(19)
ds_read_b64_tr_b16 v[104:105],v1 offset:20608
v_mfma_f32_16x16x32_bf16 a[204:207],v[100:103],a[156:159],a[204:207]
s_waitcnt vmcnt(16)
ds_read_b64_tr_b16 v[100:101],v1 offset:10368
v_mfma_f32_16x16x32_bf16 a[204:207],v[96:99],a[140:143],a[204:207]
ds_read_b64_tr_b16 v[96:97],v1 offset:128
v_mfma_f32_16x16x32_bf16 a[208:211],v[112:115],a[128:131],a[208:211]
buffer_load_dwordx4 a[104:107],v28,s[28:31],s88 offen offset:3072 sc1
ds_read_b64_tr_b16 v[98:99],v1 offset:5248
v_mfma_f32_16x16x32_bf16 a[208:211],v[116:119],a[144:147],a[208:211]
s_add_u32 s88,s87,4096
ds_read_b64_tr_b16 v[102:103],v1 offset:15488
ds_read_b64_tr_b16 v[106:107],v1 offset:25728
v_mfma_f32_16x16x32_bf16 a[208:211],v[120:123],a[160:163],a[208:211]
v_mfma_f32_16x16x32_bf16 a[208:211],v[124:127],a[176:179],a[208:211]
v_mfma_f32_16x16x32_bf16 a[212:215],v[124:127],a[180:183],a[212:215]
v_mfma_f32_16x16x32_bf16 a[212:215],v[120:123],a[164:167],a[212:215]
buffer_load_dwordx4 a[80:83],v6,s[28:31],s89 offen offset:2048 sc1
v_mfma_f32_16x16x32_bf16 a[212:215],v[116:119],a[148:151],a[212:215]
v_mfma_f32_16x16x32_bf16 a[212:215],v[112:115],a[132:135],a[212:215]
v_mfma_f32_16x16x32_bf16 a[216:219],v[112:115],a[136:139],a[216:219]
v_mfma_f32_16x16x32_bf16 a[216:219],v[116:119],a[152:155],a[216:219]
v_mfma_f32_16x16x32_bf16 a[216:219],v[120:123],a[168:171],a[216:219]
buffer_load_dwordx4 a[116:119],v6,s[28:31],s89 offen offset:1024 sc1
v_mfma_f32_16x16x32_bf16 a[216:219],v[124:127],a[184:187],a[216:219]
v_mfma_f32_16x16x32_bf16 a[220:223],v[124:127],a[188:191],a[220:223]
ds_read_b64_tr_b16 v[124:125],v2 offset:30880
ds_read_b64_tr_b16 v[126:127],v2 offset:36000
v_mfma_f32_16x16x32_bf16 a[220:223],v[120:123],a[172:175],a[220:223]
ds_read_b64_tr_b16 v[120:121],v2 offset:20640
v_mfma_f32_16x16x32_bf16 a[220:223],v[116:119],a[156:159],a[220:223]
ds_read_b64_tr_b16 v[116:117],v2 offset:10400
v_mfma_f32_16x16x32_bf16 a[220:223],v[112:115],a[140:143],a[220:223]
s_waitcnt lgkmcnt(4)
buffer_load_dwordx4 a[92:95],v6,s[28:31],s90 offen sc1
ds_read_b64_tr_b16 v[112:113],v2 offset:160
v_mfma_f32_16x16x32_bf16 a[224:227],v[64:67],a[128:131],a[224:227]
ds_read_b64_tr_b16 v[114:115],v2 offset:5280
ds_read_b64_tr_b16 v[118:119],v2 offset:15520
v_mfma_f32_16x16x32_bf16 a[224:227],v[68:71],a[144:147],a[224:227]
ds_read_b64_tr_b16 v[122:123],v2 offset:25760
v_mfma_f32_16x16x32_bf16 a[224:227],v[72:75],a[160:163],a[224:227]
v_mfma_f32_16x16x32_bf16 a[224:227],v[76:79],a[176:179],a[224:227]
v_mfma_f32_16x16x32_bf16 a[228:231],v[76:79],a[180:183],a[228:231]
buffer_load_dwordx4 a[112:115],v28,s[28:31],s89 offen offset:3072 sc1
)A"
R"A(v_mfma_f32_16x16x32_bf16 a[228:231],v[72:75],a[164:167],a[228:231]
s_add_u32 s89,s87,8192
v_mfma_f32_16x16x32_bf16 a[228:231],v[68:71],a[148:151],a[228:231]
v_mfma_f32_16x16x32_bf16 a[228:231],v[64:67],a[132:135],a[228:231]
v_mfma_f32_16x16x32_bf16 a[232:235],v[64:67],a[136:139],a[232:235]
v_mfma_f32_16x16x32_bf16 a[232:235],v[68:71],a[152:155],a[232:235]
buffer_load_dwordx4 a[124:127],v6,s[28:31],s90 offen offset:1024 sc1
v_mfma_f32_16x16x32_bf16 a[232:235],v[72:75],a[168:171],a[232:235]
s_waitcnt vmcnt(12)
v_cmp_ne_u32 s[56:57],s52,v4
v_cmp_ne_u32 s[58:59],s53,v5
v_mfma_f32_16x16x32_bf16 a[232:235],v[76:79],a[184:187],a[232:235]
s_or_b64 s[56:57],s[56:57],s[58:59]
s_cbranch_scc1 L_fr3_%=
L_fo3_%=:
s_lshl_b32 s91,s64,6
v_mfma_f32_16x16x32_bf16 a[236:239],v[76:79],a[188:191],a[236:239]
ds_read_b64_tr_b16 v[76:77],v1 offset:30912
ds_read_b64_tr_b16 v[78:79],v1 offset:36032
v_mfma_f32_16x16x32_bf16 a[236:239],v[72:75],a[172:175],a[236:239]
ds_read_b64_tr_b16 v[72:73],v1 offset:20672
v_bfe_u32 v3,v6,4,2
v_mfma_f32_16x16x32_bf16 a[236:239],v[68:71],a[156:159],a[236:239]
buffer_load_dwordx4 v[32:35],v6,s[28:31],s87 offen sc1
ds_read_b64_tr_b16 v[68:69],v1 offset:10432
v_mfma_f32_16x16x32_bf16 a[236:239],v[64:67],a[140:143],a[236:239]
ds_read_b64_tr_b16 v[64:65],v1 offset:192
v_min_u32 v3,2,v3
v_mfma_f32_16x16x32_bf16 a[240:243],v[80:83],a[128:131],a[240:243]
ds_read_b64_tr_b16 v[66:67],v1 offset:5312
ds_read_b64_tr_b16 v[70:71],v1 offset:15552
v_mfma_f32_16x16x32_bf16 a[240:243],v[84:87],a[144:147],a[240:243]
ds_read_b64_tr_b16 v[74:75],v1 offset:25792
v_add3_u32 v3,v3,s63,1
v_mfma_f32_16x16x32_bf16 a[240:243],v[88:91],a[160:163],a[240:243]
v_and_b32 v3,3,v3
v_lshlrev_b32 v3,8,v3
v_mfma_f32_16x16x32_bf16 a[240:243],v[92:95],a[176:179],a[240:243]
buffer_load_dwordx4 a[88:91],v6,s[28:31],s90 offen offset:2048 sc1
v_mfma_f32_16x16x32_bf16 a[244:247],v[92:95],a[180:183],a[244:247]
v_mfma_f32_16x16x32_bf16 a[244:247],v[88:91],a[164:167],a[244:247]
v_mfma_f32_16x16x32_bf16 a[244:247],v[84:87],a[148:151],a[244:247]
v_mfma_f32_16x16x32_bf16 a[244:247],v[80:83],a[132:135],a[244:247]
v_mfma_f32_16x16x32_bf16 a[248:251],v[80:83],a[136:139],a[248:251]
buffer_load_dwordx4 v[36:39],v6,s[28:31],s88 offen sc1
v_mfma_f32_16x16x32_bf16 a[248:251],v[84:87],a[152:155],a[248:251]
v_mfma_f32_16x16x32_bf16 a[248:251],v[88:91],a[168:171],a[248:251]
v_mfma_f32_16x16x32_bf16 a[248:251],v[92:95],a[184:187],a[248:251]
v_mfma_f32_16x16x32_bf16 a[252:255],v[92:95],a[188:191],a[252:255]
ds_read_b64_tr_b16 v[92:93],v2 offset:30944
ds_read_b64_tr_b16 v[94:95],v2 offset:36064
v_mfma_f32_16x16x32_bf16 a[252:255],v[88:91],a[172:175],a[252:255]
buffer_load_dwordx4 a[120:123],v28,s[28:31],s90 offen offset:3072 sc1
ds_read_b64_tr_b16 v[88:89],v2 offset:20704
v_mfma_f32_16x16x32_bf16 a[252:255],v[84:87],a[156:159],a[252:255]
s_add_u32 s90,s87,12288
ds_read_b64_tr_b16 v[84:85],v2 offset:10464
v_mfma_f32_16x16x32_bf16 a[252:255],v[80:83],a[140:143],a[252:255]
ds_read_b64_tr_b16 v[80:81],v2 offset:224
v_mfma_f32_16x16x32_bf16 a[0:3],v[96:99],a[128:131],a[0:3]
ds_read_b64_tr_b16 v[82:83],v2 offset:5344
ds_read_b64_tr_b16 v[86:87],v2 offset:15584
v_mfma_f32_16x16x32_bf16 a[0:3],v[100:103],a[144:147],a[0:3]
ds_read_b64_tr_b16 v[90:91],v2 offset:25824
v_mfma_f32_16x16x32_bf16 a[0:3],v[104:107],a[160:163],a[0:3]
buffer_load_dwordx4 v[40:43],v6,s[28:31],s89 offen sc1
v_mfma_f32_16x16x32_bf16 a[0:3],v[108:111],a[176:179],a[0:3]
v_mfma_f32_16x16x32_bf16 a[4:7],v[108:111],a[180:183],a[4:7]
v_mfma_f32_16x16x32_bf16 a[4:7],v[104:107],a[164:167],a[4:7]
v_mfma_f32_16x16x32_bf16 a[4:7],v[100:103],a[148:151],a[4:7]
v_mfma_f32_16x16x32_bf16 a[4:7],v[96:99],a[132:135],a[4:7]
v_mfma_f32_16x16x32_bf16 a[8:11],v[96:99],a[136:139],a[8:11]
v_mfma_f32_16x16x32_bf16 a[8:11],v[100:103],a[152:155],a[8:11]
v_mfma_f32_16x16x32_bf16 a[8:11],v[104:107],a[168:171],a[8:11]
v_mfma_f32_16x16x32_bf16 a[8:11],v[108:111],a[184:187],a[8:11]
v_mfma_f32_16x16x32_bf16 a[12:15],v[108:111],a[188:191],a[12:15]
ds_read_b64_tr_b16 v[108:109],v1 offset:30976
ds_read_b64_tr_b16 v[110:111],v1 offset:36096
v_mfma_f32_16x16x32_bf16 a[12:15],v[104:107],a[172:175],a[12:15]
buffer_load_dwordx4 v[44:47],v6,s[28:31],s90 offen sc1
ds_read_b64_tr_b16 v[104:105],v1 offset:20736
v_mfma_f32_16x16x32_bf16 a[12:15],v[100:103],a[156:159],a[12:15]
ds_read_b64_tr_b16 v[100:101],v1 offset:10496
v_mfma_f32_16x16x32_bf16 a[12:15],v[96:99],a[140:143],a[12:15]
s_waitcnt lgkmcnt(4)
ds_read_b64_tr_b16 v[96:97],v1 offset:256
v_mfma_f32_16x16x32_bf16 a[16:19],v[112:115],a[128:131],a[16:19]
ds_read_b64_tr_b16 v[98:99],v1 offset:5376
ds_read_b64_tr_b16 v[102:103],v1 offset:15616
v_mfma_f32_16x16x32_bf16 a[16:19],v[116:119],a[144:147],a[16:19]
ds_read_b64_tr_b16 v[106:107],v1 offset:25856
v_add_u32 v1,0xffff6000,v1
v_mfma_f32_16x16x32_bf16 a[16:19],v[120:123],a[160:163],a[16:19]
v_mfma_f32_16x16x32_bf16 a[16:19],v[124:127],a[176:179],a[16:19]
v_mfma_f32_16x16x32_bf16 a[20:23],v[124:127],a[180:183],a[20:23]
v_mfma_f32_16x16x32_bf16 a[20:23],v[120:123],a[164:167],a[20:23]
v_mfma_f32_16x16x32_bf16 a[20:23],v[116:119],a[148:151],a[20:23]
v_mfma_f32_16x16x32_bf16 a[20:23],v[112:115],a[132:135],a[20:23]
v_mfma_f32_16x16x32_bf16 a[24:27],v[112:115],a[136:139],a[24:27]
buffer_load_dwordx4 v[180:183],v6,s[28:31],s90 offen offset:1024 sc1
v_mfma_f32_16x16x32_bf16 a[24:27],v[116:119],a[152:155],a[24:27]
v_mfma_f32_16x16x32_bf16 a[24:27],v[120:123],a[168:171],a[24:27]
v_mfma_f32_16x16x32_bf16 a[24:27],v[124:127],a[184:187],a[24:27]
v_mfma_f32_16x16x32_bf16 a[28:31],v[124:127],a[188:191],a[28:31]
ds_read_b64_tr_b16 v[124:125],v2 offset:31008
ds_read_b64_tr_b16 v[126:127],v2 offset:36128
v_mfma_f32_16x16x32_bf16 a[28:31],v[120:123],a[172:175],a[28:31]
ds_read_b64_tr_b16 v[120:121],v2 offset:20768
v_mfma_f32_16x16x32_bf16 a[28:31],v[116:119],a[156:159],a[28:31]
ds_read_b64_tr_b16 v[116:117],v2 offset:10528
v_mfma_f32_16x16x32_bf16 a[28:31],v[112:115],a[140:143],a[28:31]
ds_read_b64_tr_b16 v[112:113],v2 offset:288
v_mfma_f32_16x16x32_bf16 v[128:131],v[64:67],a[128:131],v[128:131]
ds_read_b64_tr_b16 v[114:115],v2 offset:5408
ds_read_b64_tr_b16 v[118:119],v2 offset:15648
v_mfma_f32_16x16x32_bf16 v[128:131],v[68:71],a[144:147],v[128:131]
ds_read_b64_tr_b16 v[122:123],v2 offset:25888
v_add_u32 v2,0xffff6000,v2
v_mfma_f32_16x16x32_bf16 v[128:131],v[72:75],a[160:163],v[128:131]
v_mfma_f32_16x16x32_bf16 v[128:131],v[76:79],a[176:179],v[128:131]
buffer_load_dwordx4 v[164:167],v6,s[28:31],s89 offen offset:1024 sc1
v_mfma_f32_16x16x32_bf16 v[136:139],v[76:79],a[180:183],v[136:139]
v_mfma_f32_16x16x32_bf16 v[136:139],v[72:75],a[164:167],v[136:139]
v_mfma_f32_16x16x32_bf16 v[136:139],v[68:71],a[148:151],v[136:139]
v_mfma_f32_16x16x32_bf16 v[136:139],v[64:67],a[132:135],v[136:139]
v_mfma_f32_16x16x32_bf16 v[144:147],v[64:67],a[136:139],v[144:147]
v_mfma_f32_16x16x32_bf16 v[144:147],v[68:71],a[152:155],v[144:147]
v_mfma_f32_16x16x32_bf16 v[144:147],v[72:75],a[168:171],v[144:147]
v_mfma_f32_16x16x32_bf16 v[144:147],v[76:79],a[184:187],v[144:147]
v_mfma_f32_16x16x32_bf16 v[152:155],v[76:79],a[188:191],v[152:155]
v_mfma_f32_16x16x32_bf16 v[152:155],v[72:75],a[172:175],v[152:155]
v_mfma_f32_16x16x32_bf16 v[152:155],v[68:71],a[156:159],v[152:155]
buffer_load_dwordx4 v[148:151],v6,s[28:31],s88 offen offset:1024 sc1
v_mfma_f32_16x16x32_bf16 v[152:155],v[64:67],a[140:143],v[152:155]
s_barrier
v_mfma_f32_16x16x32_bf16 v[160:163],v[80:83],a[128:131],v[160:163]
ds_read_b64_tr_b16 v[64:65],v1
ds_read_b64_tr_b16 v[66:67],v1 offset:5120
v_mfma_f32_16x16x32_bf16 v[160:163],v[84:87],a[144:147],v[160:163]
ds_read_b64_tr_b16 v[68:69],v1 offset:10240
v_mfma_f32_16x16x32_bf16 v[160:163],v[88:91],a[160:163],v[160:163]
ds_read_b64_tr_b16 v[70:71],v1 offset:15360
v_mfma_f32_16x16x32_bf16 v[160:163],v[92:95],a[176:179],v[160:163]
buffer_load_dwordx4 v24,s[48:51],s83 offen lds
ds_read_b64_tr_b16 v[72:73],v1 offset:20480
v_mfma_f32_16x16x32_bf16 v[168:171],v[92:95],a[180:183],v[168:171]
ds_read_b64_tr_b16 v[74:75],v1 offset:25600
ds_read_b64_tr_b16 v[76:77],v1 offset:30720
v_mfma_f32_16x16x32_bf16 v[168:171],v[88:91],a[164:167],v[168:171]
ds_read_b64_tr_b16 v[78:79],v1 offset:35840
v_mfma_f32_16x16x32_bf16 v[168:171],v[84:87],a[148:151],v[168:171]
v_mfma_f32_16x16x32_bf16 v[168:171],v[80:83],a[132:135],v[168:171]
v_mfma_f32_16x16x32_bf16 v[176:179],v[80:83],a[136:139],v[176:179]
buffer_load_dwordx4 v[132:135],v6,s[28:31],s87 offen offset:1024 sc1
v_mfma_f32_16x16x32_bf16 v[176:179],v[84:87],a[152:155],v[176:179]
v_mfma_f32_16x16x32_bf16 v[176:179],v[88:91],a[168:171],v[176:179]
v_mfma_f32_16x16x32_bf16 v[176:179],v[92:95],a[184:187],v[176:179]
v_mfma_f32_16x16x32_bf16 v[184:187],v[92:95],a[188:191],v[184:187]
ds_read_b64_tr_b16 v[92:93],v2 offset:30752
ds_read_b64_tr_b16 v[94:95],v2 offset:35872
v_mfma_f32_16x16x32_bf16 v[184:187],v[88:91],a[172:175],v[184:187]
ds_read_b64_tr_b16 v[88:89],v2 offset:20512
v_mfma_f32_16x16x32_bf16 v[184:187],v[84:87],a[156:159],v[184:187]
buffer_load_dwordx4 v25,s[48:51],s83 offen offset:1024 lds
ds_read_b64_tr_b16 v[84:85],v2 offset:10272
v_mfma_f32_16x16x32_bf16 v[184:187],v[80:83],a[140:143],v[184:187]
s_waitcnt lgkmcnt(4)
ds_read_b64_tr_b16 v[80:81],v2 offset:32
v_mfma_f32_16x16x32_bf16 v[192:195],v[96:99],a[128:131],v[192:195]
ds_read_b64_tr_b16 v[82:83],v2 offset:5152
ds_read_b64_tr_b16 v[86:87],v2 offset:15392
v_mfma_f32_16x16x32_bf16 v[192:195],v[100:103],a[144:147],v[192:195]
ds_read_b64_tr_b16 v[90:91],v2 offset:25632
v_mfma_f32_16x16x32_bf16 v[192:195],v[104:107],a[160:163],v[192:195]
v_mfma_f32_16x16x32_bf16 v[192:195],v[108:111],a[176:179],v[192:195]
v_mfma_f32_16x16x32_bf16 v[200:203],v[108:111],a[180:183],v[200:203]
buffer_load_dwordx4 v26,s[48:51],s83 offen offset:2048 lds
v_mfma_f32_16x16x32_bf16 v[200:203],v[104:107],a[164:167],v[200:203]
v_mfma_f32_16x16x32_bf16 v[200:203],v[100:103],a[148:151],v[200:203]
v_mfma_f32_16x16x32_bf16 v[200:203],v[96:99],a[132:135],v[200:203]
v_mfma_f32_16x16x32_bf16 v[208:211],v[96:99],a[136:139],v[208:211]
v_mfma_f32_16x16x32_bf16 v[208:211],v[100:103],a[152:155],v[208:211]
buffer_load_dwordx4 v[196:199],v6,s[28:31],s87 offen offset:2048 sc1
v_mfma_f32_16x16x32_bf16 v[208:211],v[104:107],a[168:171],v[208:211]
v_mfma_f32_16x16x32_bf16 v[208:211],v[108:111],a[184:187],v[208:211]
v_mfma_f32_16x16x32_bf16 v[216:219],v[108:111],a[188:191],v[216:219]
ds_read_b64_tr_b16 v[108:109],v1 offset:30784
ds_read_b64_tr_b16 v[110:111],v1 offset:35904
v_mfma_f32_16x16x32_bf16 v[216:219],v[104:107],a[172:175],v[216:219]
ds_read_b64_tr_b16 v[104:105],v1 offset:20544
v_mfma_f32_16x16x32_bf16 v[216:219],v[100:103],a[156:159],v[216:219]
ds_read_b64_tr_b16 v[100:101],v1 offset:10304
v_mfma_f32_16x16x32_bf16 v[216:219],v[96:99],a[140:143],v[216:219]
buffer_load_dwordx4 v27,s[48:51],s83 offen offset:3072 lds
ds_read_b64_tr_b16 v[96:97],v1 offset:64
v_mfma_f32_16x16x32_bf16 v[224:227],v[112:115],a[128:131],v[224:227]
s_add_u32 m0,s86,126976
ds_read_b64_tr_b16 v[98:99],v1 offset:5184
ds_read_b64_tr_b16 v[102:103],v1 offset:15424
v_mfma_f32_16x16x32_bf16 v[224:227],v[116:119],a[144:147],v[224:227]
ds_read_b64_tr_b16 v[106:107],v1 offset:25664
v_mfma_f32_16x16x32_bf16 v[224:227],v[120:123],a[160:163],v[224:227]
v_mfma_f32_16x16x32_bf16 v[224:227],v[124:127],a[176:179],v[224:227]
v_mfma_f32_16x16x32_bf16 v[232:235],v[124:127],a[180:183],v[232:235]
buffer_load_dwordx4 v[212:215],v6,s[28:31],s88 offen offset:2048 sc1
v_mfma_f32_16x16x32_bf16 v[232:235],v[120:123],a[164:167],v[232:235]
v_mfma_f32_16x16x32_bf16 v[232:235],v[116:119],a[148:151],v[232:235]
v_mfma_f32_16x16x32_bf16 v[232:235],v[112:115],a[132:135],v[232:235]
v_mfma_f32_16x16x32_bf16 v[240:243],v[112:115],a[136:139],v[240:243]
v_mfma_f32_16x16x32_bf16 v[240:243],v[116:119],a[152:155],v[240:243]
v_mfma_f32_16x16x32_bf16 v[240:243],v[120:123],a[168:171],v[240:243]
buffer_load_dwordx4 v7,s[48:51],s83 offen lds
v_mfma_f32_16x16x32_bf16 v[240:243],v[124:127],a[184:187],v[240:243]
s_add_u32 m0,s86,128000
s_mul_i32 s83,s63,20480
v_mfma_f32_16x16x32_bf16 v[248:251],v[124:127],a[188:191],v[248:251]
)A"
R"A(ds_read_b64_tr_b16 v[124:125],v2 offset:30816
ds_read_b64_tr_b16 v[126:127],v2 offset:35936
v_mfma_f32_16x16x32_bf16 v[248:251],v[120:123],a[172:175],v[248:251]
ds_read_b64_tr_b16 v[120:121],v2 offset:20576
v_mfma_f32_16x16x32_bf16 v[248:251],v[116:119],a[156:159],v[248:251]
ds_read_b64_tr_b16 v[116:117],v2 offset:10336
v_mfma_f32_16x16x32_bf16 v[248:251],v[112:115],a[140:143],v[248:251]
buffer_load_dwordx4 v[228:231],v6,s[28:31],s89 offen offset:2048 sc1
ds_read_b64_tr_b16 v[112:113],v2 offset:96
v_mfma_f32_16x16x32_bf16 a[192:195],v[64:67],a[68:71],a[192:195]
s_waitcnt vmcnt(17)
ds_read_b64_tr_b16 v[114:115],v2 offset:5216
ds_read_b64_tr_b16 v[118:119],v2 offset:15456
v_mfma_f32_16x16x32_bf16 a[192:195],v[68:71],a[76:79],a[192:195]
ds_read_b64_tr_b16 v[122:123],v2 offset:25696
v_mfma_f32_16x16x32_bf16 a[192:195],v[72:75],a[84:87],a[192:195]
v_mfma_f32_16x16x32_bf16 a[192:195],v[76:79],a[92:95],a[192:195]
v_mfma_f32_16x16x32_bf16 a[196:199],v[76:79],a[124:127],a[196:199]
v_mfma_f32_16x16x32_bf16 a[196:199],v[72:75],a[116:119],a[196:199]
buffer_load_dwordx4 v24,s[48:51],s84 offen lds
v_mfma_f32_16x16x32_bf16 a[196:199],v[68:71],a[108:111],a[196:199]
v_mfma_f32_16x16x32_bf16 a[196:199],v[64:67],a[100:103],a[196:199]
v_mfma_f32_16x16x32_bf16 a[200:203],v[64:67],a[64:67],a[200:203]
v_mfma_f32_16x16x32_bf16 a[200:203],v[68:71],a[72:75],a[200:203]
v_mfma_f32_16x16x32_bf16 a[200:203],v[72:75],a[80:83],a[200:203]
s_waitcnt vmcnt(16)
buffer_load_dwordx4 v[244:247],v6,s[28:31],s90 offen offset:2048 sc1
v_mfma_f32_16x16x32_bf16 a[200:203],v[76:79],a[88:91],a[200:203]
s_waitcnt vmcnt(16)
v_mfma_f32_16x16x32_bf16 a[204:207],v[76:79],a[120:123],a[204:207]
ds_read_b64_tr_b16 v[76:77],v1 offset:30848
ds_read_b64_tr_b16 v[78:79],v1 offset:35968
v_mfma_f32_16x16x32_bf16 a[204:207],v[72:75],a[112:115],a[204:207]
ds_read_b64_tr_b16 v[72:73],v1 offset:20608
v_mfma_f32_16x16x32_bf16 a[204:207],v[68:71],a[104:107],a[204:207]
ds_read_b64_tr_b16 v[68:69],v1 offset:10368
v_mfma_f32_16x16x32_bf16 a[204:207],v[64:67],a[96:99],a[204:207]
s_waitcnt lgkmcnt(4)
ds_read_b64_tr_b16 v[64:65],v1 offset:128
v_mfma_f32_16x16x32_bf16 a[208:211],v[80:83],a[68:71],a[208:211]
buffer_load_dwordx4 v25,s[48:51],s84 offen offset:1024 lds
ds_read_b64_tr_b16 v[66:67],v1 offset:5248
v_mfma_f32_16x16x32_bf16 a[208:211],v[84:87],a[76:79],a[208:211]
ds_read_b64_tr_b16 v[70:71],v1 offset:15488
ds_read_b64_tr_b16 v[74:75],v1 offset:25728
v_mfma_f32_16x16x32_bf16 a[208:211],v[88:91],a[84:87],a[208:211]
v_mfma_f32_16x16x32_bf16 a[208:211],v[92:95],a[92:95],a[208:211]
v_mfma_f32_16x16x32_bf16 a[212:215],v[92:95],a[124:127],a[212:215]
v_mfma_f32_16x16x32_bf16 a[212:215],v[88:91],a[116:119],a[212:215]
buffer_load_dwordx4 v[188:191],v28,s[28:31],s90 offen offset:3072 sc1
v_mfma_f32_16x16x32_bf16 a[212:215],v[84:87],a[108:111],a[212:215]
v_mfma_f32_16x16x32_bf16 a[212:215],v[80:83],a[100:103],a[212:215]
v_mfma_f32_16x16x32_bf16 a[216:219],v[80:83],a[64:67],a[216:219]
v_mfma_f32_16x16x32_bf16 a[216:219],v[84:87],a[72:75],a[216:219]
v_mfma_f32_16x16x32_bf16 a[216:219],v[88:91],a[80:83],a[216:219]
v_mfma_f32_16x16x32_bf16 a[216:219],v[92:95],a[88:91],a[216:219]
buffer_load_dwordx4 v26,s[48:51],s84 offen offset:2048 lds
v_mfma_f32_16x16x32_bf16 a[220:223],v[92:95],a[120:123],a[220:223]
ds_read_b64_tr_b16 v[92:93],v2 offset:30880
ds_read_b64_tr_b16 v[94:95],v2 offset:36000
v_mfma_f32_16x16x32_bf16 a[220:223],v[88:91],a[112:115],a[220:223]
ds_read_b64_tr_b16 v[88:89],v2 offset:20640
v_mfma_f32_16x16x32_bf16 a[220:223],v[84:87],a[104:107],a[220:223]
ds_read_b64_tr_b16 v[84:85],v2 offset:10400
v_mfma_f32_16x16x32_bf16 a[220:223],v[80:83],a[96:99],a[220:223]
ds_read_b64_tr_b16 v[80:81],v2 offset:160
v_mfma_f32_16x16x32_bf16 a[224:227],v[96:99],a[68:71],a[224:227]
ds_read_b64_tr_b16 v[82:83],v2 offset:5280
ds_read_b64_tr_b16 v[86:87],v2 offset:15520
v_mfma_f32_16x16x32_bf16 a[224:227],v[100:103],a[76:79],a[224:227]
buffer_load_dwordx4 v27,s[48:51],s84 offen offset:3072 lds
ds_read_b64_tr_b16 v[90:91],v2 offset:25760
v_mfma_f32_16x16x32_bf16 a[224:227],v[104:107],a[84:87],a[224:227]
s_add_u32 m0,s86,132096
v_mfma_f32_16x16x32_bf16 a[224:227],v[108:111],a[92:95],a[224:227]
v_mfma_f32_16x16x32_bf16 a[228:231],v[108:111],a[124:127],a[228:231]
v_mfma_f32_16x16x32_bf16 a[228:231],v[104:107],a[116:119],a[228:231]
v_mfma_f32_16x16x32_bf16 a[228:231],v[100:103],a[108:111],a[228:231]
buffer_load_dwordx4 v[172:175],v28,s[28:31],s89 offen offset:3072 sc1
v_mfma_f32_16x16x32_bf16 a[228:231],v[96:99],a[100:103],a[228:231]
v_mfma_f32_16x16x32_bf16 a[232:235],v[96:99],a[64:67],a[232:235]
v_mfma_f32_16x16x32_bf16 a[232:235],v[100:103],a[72:75],a[232:235]
v_mfma_f32_16x16x32_bf16 a[232:235],v[104:107],a[80:83],a[232:235]
v_mfma_f32_16x16x32_bf16 a[232:235],v[108:111],a[88:91],a[232:235]
v_mfma_f32_16x16x32_bf16 a[236:239],v[108:111],a[120:123],a[236:239]
buffer_load_dwordx4 v7,s[48:51],s84 offen lds
ds_read_b64_tr_b16 v[108:109],v1 offset:30912
v_mfma_f32_16x16x32_bf16 a[236:239],v[104:107],a[112:115],a[236:239]
ds_read_b64_tr_b16 v[104:105],v1 offset:20672
ds_read_b64_tr_b16 v[106:107],v1 offset:25792
v_mfma_f32_16x16x32_bf16 a[236:239],v[100:103],a[104:107],a[236:239]
ds_read_b64_tr_b16 v[100:101],v1 offset:10432
v_mfma_f32_16x16x32_bf16 a[236:239],v[96:99],a[96:99],a[236:239]
ds_read_b64_tr_b16 v[96:97],v1 offset:192
v_mfma_f32_16x16x32_bf16 a[240:243],v[112:115],a[68:71],a[240:243]
ds_read_b64_tr_b16 v[98:99],v1 offset:5312
v_mfma_f32_16x16x32_bf16 a[240:243],v[116:119],a[76:79],a[240:243]
buffer_load_dwordx4 v[156:159],v28,s[28:31],s88 offen offset:3072 sc1
ds_read_b64_tr_b16 v[102:103],v1 offset:15552
v_mfma_f32_16x16x32_bf16 a[240:243],v[120:123],a[84:87],a[240:243]
ds_read_b64_tr_b16 v[110:111],v1 offset:36032
v_add_u32 v1,0xffff6000,v1
v_mfma_f32_16x16x32_bf16 a[240:243],v[124:127],a[92:95],a[240:243]
v_mfma_f32_16x16x32_bf16 a[244:247],v[124:127],a[124:127],a[244:247]
v_mfma_f32_16x16x32_bf16 a[244:247],v[120:123],a[116:119],a[244:247]
v_mfma_f32_16x16x32_bf16 a[244:247],v[116:119],a[108:111],a[244:247]
buffer_load_dwordx4 v[140:143],v28,s[28:31],s87 offen offset:3072 sc1
v_mfma_f32_16x16x32_bf16 a[244:247],v[112:115],a[100:103],a[244:247]
s_add_u32 s87,s64,3
s_and_b32 s87,s87,3
s_lshl_b32 s87,s87,1
s_or_b32 s87,s87,1
s_lshl_b32 s87,s87,16
s_add_u32 s88,s87,4096
s_add_u32 s89,s87,8192
s_add_u32 s90,s87,12288
v_mfma_f32_16x16x32_bf16 a[248:251],v[112:115],a[64:67],a[248:251]
v_mfma_f32_16x16x32_bf16 a[248:251],v[116:119],a[72:75],a[248:251]
v_mfma_f32_16x16x32_bf16 a[248:251],v[120:123],a[80:83],a[248:251]
v_mfma_f32_16x16x32_bf16 a[248:251],v[124:127],a[88:91],a[248:251]
buffer_load_dwordx4 a[128:131],v6,s[28:31],s87 offen sc1
v_mfma_f32_16x16x32_bf16 a[252:255],v[124:127],a[120:123],a[252:255]
ds_read_b64_tr_b16 v[124:125],v2 offset:30944
ds_read_b64_tr_b16 v[126:127],v2 offset:36064
v_mfma_f32_16x16x32_bf16 a[252:255],v[120:123],a[112:115],a[252:255]
ds_read_b64_tr_b16 v[120:121],v2 offset:20704
v_mfma_f32_16x16x32_bf16 a[252:255],v[116:119],a[104:107],a[252:255]
ds_read_b64_tr_b16 v[116:117],v2 offset:10464
v_mfma_f32_16x16x32_bf16 a[252:255],v[112:115],a[96:99],a[252:255]
s_waitcnt lgkmcnt(4)
ds_read_b64_tr_b16 v[112:113],v2 offset:224
v_mfma_f32_16x16x32_bf16 a[0:3],v[64:67],a[68:71],a[0:3]
buffer_load_dwordx4 a[144:147],v6,s[28:31],s88 offen sc1
ds_read_b64_tr_b16 v[114:115],v2 offset:5344
v_mfma_f32_16x16x32_bf16 a[0:3],v[68:71],a[76:79],a[0:3]
ds_read_b64_tr_b16 v[118:119],v2 offset:15584
ds_read_b64_tr_b16 v[122:123],v2 offset:25824
v_mfma_f32_16x16x32_bf16 a[0:3],v[72:75],a[84:87],a[0:3]
v_add_u32 v2,0xffff6000,v2
v_mfma_f32_16x16x32_bf16 a[0:3],v[76:79],a[92:95],a[0:3]
v_mfma_f32_16x16x32_bf16 a[4:7],v[76:79],a[124:127],a[4:7]
v_mfma_f32_16x16x32_bf16 a[4:7],v[72:75],a[116:119],a[4:7]
buffer_load_dwordx4 a[160:163],v6,s[28:31],s89 offen sc1
v_mfma_f32_16x16x32_bf16 a[4:7],v[68:71],a[108:111],a[4:7]
v_mfma_f32_16x16x32_bf16 a[4:7],v[64:67],a[100:103],a[4:7]
v_mfma_f32_16x16x32_bf16 a[8:11],v[64:67],a[64:67],a[8:11]
v_mfma_f32_16x16x32_bf16 a[8:11],v[68:71],a[72:75],a[8:11]
v_mfma_f32_16x16x32_bf16 a[8:11],v[72:75],a[80:83],a[8:11]
buffer_load_dwordx4 a[176:179],v6,s[28:31],s90 offen sc1
v_mfma_f32_16x16x32_bf16 a[8:11],v[76:79],a[88:91],a[8:11]
v_mfma_f32_16x16x32_bf16 a[12:15],v[76:79],a[120:123],a[12:15]
ds_read_b64_tr_b16 v[76:77],v1 offset:30720
ds_read_b64_tr_b16 v[78:79],v1 offset:35840
v_mfma_f32_16x16x32_bf16 a[12:15],v[72:75],a[112:115],a[12:15]
ds_read_b64_tr_b16 v[72:73],v1 offset:20480
v_mfma_f32_16x16x32_bf16 a[12:15],v[68:71],a[104:107],a[12:15]
ds_read_b64_tr_b16 v[68:69],v1 offset:10240
v_mfma_f32_16x16x32_bf16 a[12:15],v[64:67],a[96:99],a[12:15]
buffer_load_dwordx4 a[180:183],v6,s[28:31],s90 offen offset:1024 sc1
ds_read_b64_tr_b16 v[64:65],v1
v_mfma_f32_16x16x32_bf16 a[16:19],v[80:83],a[68:71],a[16:19]
ds_read_b64_tr_b16 v[66:67],v1 offset:5120
ds_read_b64_tr_b16 v[70:71],v1 offset:15360
v_mfma_f32_16x16x32_bf16 a[16:19],v[84:87],a[76:79],a[16:19]
ds_read_b64_tr_b16 v[74:75],v1 offset:25600
v_add_u32 v1,0x1e000,v1
v_mfma_f32_16x16x32_bf16 a[16:19],v[88:91],a[84:87],a[16:19]
v_mfma_f32_16x16x32_bf16 a[16:19],v[92:95],a[92:95],a[16:19]
v_mfma_f32_16x16x32_bf16 a[20:23],v[92:95],a[124:127],a[20:23]
buffer_load_dwordx4 a[164:167],v6,s[28:31],s89 offen offset:1024 sc1
v_mfma_f32_16x16x32_bf16 a[20:23],v[88:91],a[116:119],a[20:23]
v_mfma_f32_16x16x32_bf16 a[20:23],v[84:87],a[108:111],a[20:23]
v_mfma_f32_16x16x32_bf16 a[20:23],v[80:83],a[100:103],a[20:23]
v_mfma_f32_16x16x32_bf16 a[24:27],v[80:83],a[64:67],a[24:27]
v_mfma_f32_16x16x32_bf16 a[24:27],v[84:87],a[72:75],a[24:27]
buffer_load_dwordx4 a[148:151],v6,s[28:31],s88 offen offset:1024 sc1
v_mfma_f32_16x16x32_bf16 a[24:27],v[88:91],a[80:83],a[24:27]
v_mfma_f32_16x16x32_bf16 a[24:27],v[92:95],a[88:91],a[24:27]
v_mfma_f32_16x16x32_bf16 a[28:31],v[92:95],a[120:123],a[28:31]
ds_read_b64_tr_b16 v[92:93],v2 offset:30752
ds_read_b64_tr_b16 v[94:95],v2 offset:35872
v_mfma_f32_16x16x32_bf16 a[28:31],v[88:91],a[112:115],a[28:31]
ds_read_b64_tr_b16 v[88:89],v2 offset:20512
v_mfma_f32_16x16x32_bf16 a[28:31],v[84:87],a[104:107],a[28:31]
buffer_load_dwordx4 a[132:135],v6,s[28:31],s87 offen offset:1024 sc1
ds_read_b64_tr_b16 v[84:85],v2 offset:10272
v_mfma_f32_16x16x32_bf16 a[28:31],v[80:83],a[96:99],a[28:31]
ds_read_b64_tr_b16 v[80:81],v2 offset:32
v_mfma_f32_16x16x32_bf16 v[128:131],v[96:99],a[68:71],v[128:131]
ds_read_b64_tr_b16 v[82:83],v2 offset:5152
ds_read_b64_tr_b16 v[86:87],v2 offset:15392
v_mfma_f32_16x16x32_bf16 v[128:131],v[100:103],a[76:79],v[128:131]
ds_read_b64_tr_b16 v[90:91],v2 offset:25632
v_add_u32 v2,0x1e000,v2
v_mfma_f32_16x16x32_bf16 v[128:131],v[104:107],a[84:87],v[128:131]
v_mfma_f32_16x16x32_bf16 v[128:131],v[108:111],a[92:95],v[128:131]
buffer_load_dwordx4 a[136:139],v6,s[28:31],s87 offen offset:2048 sc1
s_nop 0
L_pd18_%=:
s_cbranch_execz L_trtg2F_%=
v_mfma_f32_16x16x32_bf16 v[136:139],v[108:111],a[124:127],v[136:139]
v_mfma_f32_16x16x32_bf16 v[136:139],v[104:107],a[116:119],v[136:139]
v_mfma_f32_16x16x32_bf16 v[136:139],v[100:103],a[108:111],v[136:139]
v_mfma_f32_16x16x32_bf16 v[136:139],v[96:99],a[100:103],v[136:139]
v_mfma_f32_16x16x32_bf16 v[144:147],v[96:99],a[64:67],v[144:147]
buffer_load_dwordx4 a[152:155],v6,s[28:31],s88 offen offset:2048 sc1
v_mfma_f32_16x16x32_bf16 v[144:147],v[100:103],a[72:75],v[144:147]
v_mfma_f32_16x16x32_bf16 v[144:147],v[104:107],a[80:83],v[144:147]
v_mfma_f32_16x16x32_bf16 v[144:147],v[108:111],a[88:91],v[144:147]
v_mfma_f32_16x16x32_bf16 v[152:155],v[108:111],a[120:123],v[152:155]
v_mfma_f32_16x16x32_bf16 v[152:155],v[104:107],a[112:115],v[152:155]
buffer_load_dwordx4 a[168:171],v6,s[28:31],s89 offen offset:2048 sc1
v_mfma_f32_16x16x32_bf16 v[152:155],v[100:103],a[104:107],v[152:155]
v_mfma_f32_16x16x32_bf16 v[152:155],v[96:99],a[96:99],v[152:155]
s_waitcnt vmcnt(13)
s_barrier
s_waitcnt lgkmcnt(0)
v_mfma_f32_16x16x32_bf16 v[160:163],v[112:115],a[68:71],v[160:163]
ds_read_b64_tr_b16 v[96:97],v1
ds_read_b64_tr_b16 v[98:99],v1 offset:5120
)A"
R"A(v_mfma_f32_16x16x32_bf16 v[160:163],v[116:119],a[76:79],v[160:163]
buffer_load_dwordx4 a[184:187],v6,s[28:31],s90 offen offset:2048 sc1
ds_read_b64_tr_b16 v[100:101],v1 offset:10240
v_mfma_f32_16x16x32_bf16 v[160:163],v[120:123],a[84:87],v[160:163]
ds_read_b64_tr_b16 v[102:103],v1 offset:15360
v_mfma_f32_16x16x32_bf16 v[160:163],v[124:127],a[92:95],v[160:163]
ds_read_b64_tr_b16 v[104:105],v1 offset:20480
v_mfma_f32_16x16x32_bf16 v[168:171],v[124:127],a[124:127],v[168:171]
ds_read_b64_tr_b16 v[106:107],v1 offset:25600
ds_read_b64_tr_b16 v[108:109],v1 offset:30720
v_mfma_f32_16x16x32_bf16 v[168:171],v[120:123],a[116:119],v[168:171]
ds_read_b64_tr_b16 v[110:111],v1 offset:35840
v_add_u32 v1,0xfffe2000,v1
v_mfma_f32_16x16x32_bf16 v[168:171],v[116:119],a[108:111],v[168:171]
buffer_load_dwordx4 a[188:191],v28,s[28:31],s90 offen offset:3072 sc1
v_mfma_f32_16x16x32_bf16 v[168:171],v[112:115],a[100:103],v[168:171]
v_mfma_f32_16x16x32_bf16 v[176:179],v[112:115],a[64:67],v[176:179]
v_mfma_f32_16x16x32_bf16 v[176:179],v[116:119],a[72:75],v[176:179]
v_mfma_f32_16x16x32_bf16 v[176:179],v[120:123],a[80:83],v[176:179]
v_mfma_f32_16x16x32_bf16 v[176:179],v[124:127],a[88:91],v[176:179]
buffer_load_dwordx4 a[172:175],v28,s[28:31],s89 offen offset:3072 sc1
v_mfma_f32_16x16x32_bf16 v[184:187],v[124:127],a[120:123],v[184:187]
ds_read_b64_tr_b16 v[124:125],v2 offset:30752
ds_read_b64_tr_b16 v[126:127],v2 offset:35872
v_mfma_f32_16x16x32_bf16 v[184:187],v[120:123],a[112:115],v[184:187]
ds_read_b64_tr_b16 v[120:121],v2 offset:20512
v_mfma_f32_16x16x32_bf16 v[184:187],v[116:119],a[104:107],v[184:187]
ds_read_b64_tr_b16 v[116:117],v2 offset:10272
v_mfma_f32_16x16x32_bf16 v[184:187],v[112:115],a[96:99],v[184:187]
ds_read_b64_tr_b16 v[112:113],v2 offset:32
v_mfma_f32_16x16x32_bf16 a[192:195],v[64:67],v[32:35],a[192:195]
buffer_load_dwordx4 a[156:159],v28,s[28:31],s88 offen offset:3072 sc1
ds_read_b64_tr_b16 v[114:115],v2 offset:5152
v_mfma_f32_16x16x32_bf16 a[192:195],v[68:71],v[36:39],a[192:195]
ds_read_b64_tr_b16 v[118:119],v2 offset:15392
ds_read_b64_tr_b16 v[122:123],v2 offset:25632
v_mfma_f32_16x16x32_bf16 a[192:195],v[72:75],v[40:43],a[192:195]
v_add_u32 v2,0xfffe2000,v2
v_mfma_f32_16x16x32_bf16 a[192:195],v[76:79],v[44:47],a[192:195]
v_mfma_f32_16x16x32_bf16 a[196:199],v[76:79],v[180:183],a[196:199]
v_mfma_f32_16x16x32_bf16 a[196:199],v[72:75],v[164:167],a[196:199]
buffer_load_dwordx4 a[140:143],v28,s[28:31],s87 offen offset:3072 sc1
v_mfma_f32_16x16x32_bf16 a[196:199],v[68:71],v[148:151],a[196:199]
s_mul_i32 s87,s60,81920
s_add_u32 s87,s87,s66
s_add_u32 s83,s83,s87
v_mfma_f32_16x16x32_bf16 a[196:199],v[64:67],v[132:135],a[196:199]
s_lshl_b32 s66,s63,6
s_add_u32 s87,s87,s66
s_add_u32 s88,s87,20480
v_mfma_f32_16x16x32_bf16 a[200:203],v[64:67],v[196:199],a[200:203]
s_add_u32 s89,s87,40960
s_add_u32 s90,s87,61440
s_add_u32 s66,s77,4096
v_mfma_f32_16x16x32_bf16 a[200:203],v[68:71],v[212:215],a[200:203]
v_mfma_f32_16x16x32_bf16 a[200:203],v[72:75],v[228:231],a[200:203]
v_mfma_f32_16x16x32_bf16 a[200:203],v[76:79],v[244:247],a[200:203]
v_mfma_f32_16x16x32_bf16 a[204:207],v[76:79],v[188:191],a[204:207]
ds_read_b64_tr_b16 v[76:77],v1 offset:30784
ds_read_b64_tr_b16 v[78:79],v1 offset:35904
v_mfma_f32_16x16x32_bf16 a[204:207],v[72:75],v[172:175],a[204:207]
s_waitcnt vmcnt(17)
ds_read_b64_tr_b16 v[72:73],v1 offset:20544
v_mfma_f32_16x16x32_bf16 a[204:207],v[68:71],v[156:159],a[204:207]
s_waitcnt vmcnt(16)
ds_read_b64_tr_b16 v[68:69],v1 offset:10304
v_mfma_f32_16x16x32_bf16 a[204:207],v[64:67],v[140:143],a[204:207]
ds_read_b64_tr_b16 v[64:65],v1 offset:64
v_mfma_f32_16x16x32_bf16 a[208:211],v[80:83],v[32:35],a[208:211]
ds_read_b64_tr_b16 v[66:67],v1 offset:5184
ds_read_b64_tr_b16 v[70:71],v1 offset:15424
v_mfma_f32_16x16x32_bf16 a[208:211],v[84:87],v[36:39],a[208:211]
ds_read_b64_tr_b16 v[74:75],v1 offset:25664
v_add_u32 v1,0x1e000,v1
v_mfma_f32_16x16x32_bf16 a[208:211],v[88:91],v[40:43],a[208:211]
v_mfma_f32_16x16x32_bf16 a[208:211],v[92:95],v[44:47],a[208:211]
v_mfma_f32_16x16x32_bf16 a[212:215],v[92:95],v[180:183],a[212:215]
v_mfma_f32_16x16x32_bf16 a[212:215],v[88:91],v[164:167],a[212:215]
v_mfma_f32_16x16x32_bf16 a[212:215],v[84:87],v[148:151],a[212:215]
v_mfma_f32_16x16x32_bf16 a[212:215],v[80:83],v[132:135],a[212:215]
v_mfma_f32_16x16x32_bf16 a[216:219],v[80:83],v[196:199],a[216:219]
v_mfma_f32_16x16x32_bf16 a[216:219],v[84:87],v[212:215],a[216:219]
v_mfma_f32_16x16x32_bf16 a[216:219],v[88:91],v[228:231],a[216:219]
v_mfma_f32_16x16x32_bf16 a[216:219],v[92:95],v[244:247],a[216:219]
v_mfma_f32_16x16x32_bf16 a[220:223],v[92:95],v[188:191],a[220:223]
ds_read_b64_tr_b16 v[92:93],v2 offset:30816
ds_read_b64_tr_b16 v[94:95],v2 offset:35936
v_mfma_f32_16x16x32_bf16 a[220:223],v[88:91],v[172:175],a[220:223]
ds_read_b64_tr_b16 v[88:89],v2 offset:20576
v_mfma_f32_16x16x32_bf16 a[220:223],v[84:87],v[156:159],a[220:223]
ds_read_b64_tr_b16 v[84:85],v2 offset:10336
v_mfma_f32_16x16x32_bf16 a[220:223],v[80:83],v[140:143],a[220:223]
s_waitcnt vmcnt(15) lgkmcnt(4)
ds_read_b64_tr_b16 v[80:81],v2 offset:96
v_mfma_f32_16x16x32_bf16 a[192:195],v[96:99],a[128:131],a[192:195]
s_waitcnt vmcnt(14)
ds_read_b64_tr_b16 v[82:83],v2 offset:5216
ds_read_b64_tr_b16 v[86:87],v2 offset:15456
v_mfma_f32_16x16x32_bf16 a[192:195],v[100:103],a[144:147],a[192:195]
s_waitcnt vmcnt(13)
ds_read_b64_tr_b16 v[90:91],v2 offset:25696
v_add_u32 v2,0x1e000,v2
v_mfma_f32_16x16x32_bf16 a[192:195],v[104:107],a[160:163],a[192:195]
s_waitcnt vmcnt(12)
v_mfma_f32_16x16x32_bf16 a[192:195],v[108:111],a[176:179],a[192:195]
s_waitcnt vmcnt(11)
v_mfma_f32_16x16x32_bf16 a[196:199],v[108:111],a[180:183],a[196:199]
s_waitcnt vmcnt(10)
v_mfma_f32_16x16x32_bf16 a[196:199],v[104:107],a[164:167],a[196:199]
s_waitcnt vmcnt(9)
v_mfma_f32_16x16x32_bf16 a[196:199],v[100:103],a[148:151],a[196:199]
s_waitcnt vmcnt(8)
v_mfma_f32_16x16x32_bf16 a[196:199],v[96:99],a[132:135],a[196:199]
s_waitcnt vmcnt(7)
v_mfma_f32_16x16x32_bf16 a[200:203],v[96:99],a[136:139],a[200:203]
s_waitcnt vmcnt(6)
v_mfma_f32_16x16x32_bf16 a[200:203],v[100:103],a[152:155],a[200:203]
s_waitcnt vmcnt(5)
v_mfma_f32_16x16x32_bf16 a[200:203],v[104:107],a[168:171],a[200:203]
s_waitcnt vmcnt(4)
v_mfma_f32_16x16x32_bf16 a[200:203],v[108:111],a[184:187],a[200:203]
s_waitcnt vmcnt(3)
v_accvgpr_read_b32 v16,a192
v_accvgpr_read_b32 v17,a193
v_mfma_f32_16x16x32_bf16 a[204:207],v[108:111],a[188:191],a[204:207]
s_waitcnt vmcnt(2)
v_accvgpr_read_b32 v18,a194
v_accvgpr_read_b32 v19,a195
v_mfma_f32_16x16x32_bf16 a[204:207],v[104:107],a[172:175],a[204:207]
s_waitcnt vmcnt(1)
v_cvt_pk_bf16_f32 v16,v16,v17
v_cvt_pk_bf16_f32 v17,v18,v19
v_mfma_f32_16x16x32_bf16 a[204:207],v[100:103],a[156:159],a[204:207]
s_waitcnt vmcnt(0)
v_accvgpr_read_b32 v8,a196
v_accvgpr_read_b32 v9,a197
v_mfma_f32_16x16x32_bf16 a[204:207],v[96:99],a[140:143],a[204:207]
v_accvgpr_read_b32 v10,a198
v_accvgpr_read_b32 v11,a199
v_mfma_f32_16x16x32_bf16 a[208:211],v[112:115],a[128:131],a[208:211]
v_cvt_pk_bf16_f32 v8,v8,v9
v_cvt_pk_bf16_f32 v9,v10,v11
v_mfma_f32_16x16x32_bf16 a[208:211],v[116:119],a[144:147],a[208:211]
ds_read_b64_tr_b16 v[96:97],v1 offset:64
ds_read_b64_tr_b16 v[98:99],v1 offset:5184
v_mfma_f32_16x16x32_bf16 a[208:211],v[120:123],a[160:163],a[208:211]
ds_read_b64_tr_b16 v[100:101],v1 offset:10304
v_mfma_f32_16x16x32_bf16 a[208:211],v[124:127],a[176:179],a[208:211]
ds_read_b64_tr_b16 v[102:103],v1 offset:15424
v_mfma_f32_16x16x32_bf16 a[212:215],v[124:127],a[180:183],a[212:215]
ds_read_b64_tr_b16 v[104:105],v1 offset:20544
v_mfma_f32_16x16x32_bf16 a[212:215],v[120:123],a[164:167],a[212:215]
ds_read_b64_tr_b16 v[106:107],v1 offset:25664
ds_read_b64_tr_b16 v[108:109],v1 offset:30784
v_mfma_f32_16x16x32_bf16 a[212:215],v[116:119],a[148:151],a[212:215]
ds_read_b64_tr_b16 v[110:111],v1 offset:35904
v_add_u32 v1,0xfffe2000,v1
v_mfma_f32_16x16x32_bf16 a[212:215],v[112:115],a[132:135],a[212:215]
v_accvgpr_read_b32 v20,a208
v_accvgpr_read_b32 v21,a209
v_mfma_f32_16x16x32_bf16 a[216:219],v[112:115],a[136:139],a[216:219]
v_accvgpr_read_b32 v22,a210
v_accvgpr_read_b32 v23,a211
v_mfma_f32_16x16x32_bf16 a[216:219],v[116:119],a[152:155],a[216:219]
v_cvt_pk_bf16_f32 v18,v20,v21
v_cvt_pk_bf16_f32 v19,v22,v23
v_mfma_f32_16x16x32_bf16 a[216:219],v[120:123],a[168:171],a[216:219]
buffer_store_dwordx4 v[16:19],v6,s[36:39],s74 offen sc1
v_mfma_f32_16x16x32_bf16 a[216:219],v[124:127],a[184:187],a[216:219]
v_accvgpr_read_b32 v12,a212
v_accvgpr_read_b32 v13,a213
v_mfma_f32_16x16x32_bf16 a[220:223],v[124:127],a[188:191],a[220:223]
v_accvgpr_read_b32 v14,a214
v_accvgpr_read_b32 v15,a215
v_mfma_f32_16x16x32_bf16 a[220:223],v[120:123],a[172:175],a[220:223]
v_cvt_pk_bf16_f32 v10,v12,v13
v_cvt_pk_bf16_f32 v11,v14,v15
v_mfma_f32_16x16x32_bf16 a[220:223],v[116:119],a[156:159],a[220:223]
v_accvgpr_read_b32 v16,a200
v_accvgpr_read_b32 v17,a201
v_mfma_f32_16x16x32_bf16 a[220:223],v[112:115],a[140:143],a[220:223]
v_accvgpr_read_b32 v18,a202
v_accvgpr_read_b32 v19,a203
v_mfma_f32_16x16x32_bf16 a[224:227],v[64:67],v[32:35],a[224:227]
v_accvgpr_read_b32 v20,a216
v_accvgpr_read_b32 v21,a217
v_mfma_f32_16x16x32_bf16 a[224:227],v[68:71],v[36:39],a[224:227]
v_accvgpr_read_b32 v22,a218
v_accvgpr_read_b32 v23,a219
v_mfma_f32_16x16x32_bf16 a[224:227],v[72:75],v[40:43],a[224:227]
v_cvt_pk_bf16_f32 v16,v16,v17
v_cvt_pk_bf16_f32 v17,v18,v19
v_mfma_f32_16x16x32_bf16 a[224:227],v[76:79],v[44:47],a[224:227]
v_cvt_pk_bf16_f32 v18,v20,v21
v_cvt_pk_bf16_f32 v19,v22,v23
v_mfma_f32_16x16x32_bf16 a[228:231],v[76:79],v[180:183],a[228:231]
ds_read_b64_tr_b16 v[112:113],v2 offset:96
ds_read_b64_tr_b16 v[114:115],v2 offset:5216
v_mfma_f32_16x16x32_bf16 a[228:231],v[72:75],v[164:167],a[228:231]
buffer_store_dwordx4 v[8:11],v6,s[36:39],s74 offen offset:1024 sc1
v_mfma_f32_16x16x32_bf16 a[228:231],v[68:71],v[148:151],a[228:231]
ds_read_b64_tr_b16 v[116:117],v2 offset:10336
ds_read_b64_tr_b16 v[118:119],v2 offset:15456
v_mfma_f32_16x16x32_bf16 a[228:231],v[64:67],v[132:135],a[228:231]
ds_read_b64_tr_b16 v[120:121],v2 offset:20576
v_accvgpr_read_b32 v8,a204
v_mfma_f32_16x16x32_bf16 a[232:235],v[64:67],v[196:199],a[232:235]
ds_read_b64_tr_b16 v[122:123],v2 offset:25696
v_accvgpr_read_b32 v9,a205
v_mfma_f32_16x16x32_bf16 a[232:235],v[68:71],v[212:215],a[232:235]
ds_read_b64_tr_b16 v[124:125],v2 offset:30816
v_accvgpr_read_b32 v10,a206
v_mfma_f32_16x16x32_bf16 a[232:235],v[72:75],v[228:231],a[232:235]
ds_read_b64_tr_b16 v[126:127],v2 offset:35936
v_accvgpr_read_b32 v11,a207
v_mfma_f32_16x16x32_bf16 a[232:235],v[76:79],v[244:247],a[232:235]
v_accvgpr_read_b32 v12,a220
v_accvgpr_read_b32 v13,a221
v_mfma_f32_16x16x32_bf16 a[236:239],v[76:79],v[188:191],a[236:239]
v_accvgpr_read_b32 v14,a222
v_accvgpr_read_b32 v15,a223
v_mfma_f32_16x16x32_bf16 a[236:239],v[72:75],v[172:175],a[236:239]
v_cvt_pk_bf16_f32 v8,v8,v9
v_cvt_pk_bf16_f32 v9,v10,v11
v_mfma_f32_16x16x32_bf16 a[236:239],v[68:71],v[156:159],a[236:239]
v_cvt_pk_bf16_f32 v10,v12,v13
v_cvt_pk_bf16_f32 v11,v14,v15
v_mfma_f32_16x16x32_bf16 a[236:239],v[64:67],v[140:143],a[236:239]
s_waitcnt lgkmcnt(3)
ds_read_b64_tr_b16 v[64:65],v1 offset:128
ds_read_b64_tr_b16 v[66:67],v1 offset:5248
v_mfma_f32_16x16x32_bf16 a[240:243],v[80:83],v[32:35],a[240:243]
buffer_store_dwordx4 v[16:19],v6,s[36:39],s74 offen offset:2048 sc1
v_mfma_f32_16x16x32_bf16 a[240:243],v[84:87],v[36:39],a[240:243]
ds_read_b64_tr_b16 v[68:69],v1 offset:10368
ds_read_b64_tr_b16 v[70:71],v1 offset:15488
v_mfma_f32_16x16x32_bf16 a[240:243],v[88:91],v[40:43],a[240:243]
ds_read_b64_tr_b16 v[72:73],v1 offset:20608
v_add_u32 v2,0xfffe2000,v2
v_mfma_f32_16x16x32_bf16 a[240:243],v[92:95],v[44:47],a[240:243]
ds_read_b64_tr_b16 v[74:75],v1 offset:25728
v_mfma_f32_16x16x32_bf16 a[244:247],v[92:95],v[180:183],a[244:247]
ds_read_b64_tr_b16 v[76:77],v1 offset:30848
v_mfma_f32_16x16x32_bf16 a[244:247],v[88:91],v[164:167],a[244:247]
ds_read_b64_tr_b16 v[78:79],v1 offset:35968
v_add_u32 v1,0x1e000,v1
v_mfma_f32_16x16x32_bf16 a[244:247],v[84:87],v[148:151],a[244:247]
v_mfma_f32_16x16x32_bf16 a[244:247],v[80:83],v[132:135],a[244:247]
)A"
R"A(v_mfma_f32_16x16x32_bf16 a[248:251],v[80:83],v[196:199],a[248:251]
v_mfma_f32_16x16x32_bf16 a[248:251],v[84:87],v[212:215],a[248:251]
v_mfma_f32_16x16x32_bf16 a[248:251],v[88:91],v[228:231],a[248:251]
v_mfma_f32_16x16x32_bf16 a[248:251],v[92:95],v[244:247],a[248:251]
buffer_store_dwordx4 v[8:11],v28,s[36:39],s74 offen offset:3072 sc1
v_mfma_f32_16x16x32_bf16 a[252:255],v[92:95],v[188:191],a[252:255]
ds_read_b64_tr_b16 v[92:93],v2 offset:30880
ds_read_b64_tr_b16 v[94:95],v2 offset:36000
v_mfma_f32_16x16x32_bf16 a[252:255],v[88:91],v[172:175],a[252:255]
ds_read_b64_tr_b16 v[88:89],v2 offset:20640
v_mfma_f32_16x16x32_bf16 a[252:255],v[84:87],v[156:159],a[252:255]
ds_read_b64_tr_b16 v[84:85],v2 offset:10400
v_mfma_f32_16x16x32_bf16 a[252:255],v[80:83],v[140:143],a[252:255]
ds_read_b64_tr_b16 v[80:81],v2 offset:160
v_mfma_f32_16x16x32_bf16 a[224:227],v[96:99],a[128:131],a[224:227]
ds_read_b64_tr_b16 v[82:83],v2 offset:5280
ds_read_b64_tr_b16 v[86:87],v2 offset:15520
v_mfma_f32_16x16x32_bf16 a[224:227],v[100:103],a[144:147],a[224:227]
ds_read_b64_tr_b16 v[90:91],v2 offset:25760
v_add_u32 v2,0x1e000,v2
v_mfma_f32_16x16x32_bf16 a[224:227],v[104:107],a[160:163],a[224:227]
v_mfma_f32_16x16x32_bf16 a[224:227],v[108:111],a[176:179],a[224:227]
v_mfma_f32_16x16x32_bf16 a[228:231],v[108:111],a[180:183],a[228:231]
v_mfma_f32_16x16x32_bf16 a[228:231],v[104:107],a[164:167],a[228:231]
v_mfma_f32_16x16x32_bf16 a[228:231],v[100:103],a[148:151],a[228:231]
v_mfma_f32_16x16x32_bf16 a[228:231],v[96:99],a[132:135],a[228:231]
v_mfma_f32_16x16x32_bf16 a[232:235],v[96:99],a[136:139],a[232:235]
v_mfma_f32_16x16x32_bf16 a[232:235],v[100:103],a[152:155],a[232:235]
v_mfma_f32_16x16x32_bf16 a[232:235],v[104:107],a[168:171],a[232:235]
v_mfma_f32_16x16x32_bf16 a[232:235],v[108:111],a[184:187],a[232:235]
v_accvgpr_read_b32 v16,a224
v_accvgpr_read_b32 v17,a225
v_mfma_f32_16x16x32_bf16 a[236:239],v[108:111],a[188:191],a[236:239]
v_accvgpr_read_b32 v18,a226
v_accvgpr_read_b32 v19,a227
v_mfma_f32_16x16x32_bf16 a[236:239],v[104:107],a[172:175],a[236:239]
v_cvt_pk_bf16_f32 v16,v16,v17
v_cvt_pk_bf16_f32 v17,v18,v19
v_mfma_f32_16x16x32_bf16 a[236:239],v[100:103],a[156:159],a[236:239]
v_accvgpr_read_b32 v8,a228
v_accvgpr_read_b32 v9,a229
v_mfma_f32_16x16x32_bf16 a[236:239],v[96:99],a[140:143],a[236:239]
v_accvgpr_read_b32 v10,a230
v_accvgpr_read_b32 v11,a231
v_mfma_f32_16x16x32_bf16 a[240:243],v[112:115],a[128:131],a[240:243]
v_cvt_pk_bf16_f32 v8,v8,v9
v_cvt_pk_bf16_f32 v9,v10,v11
v_mfma_f32_16x16x32_bf16 a[240:243],v[116:119],a[144:147],a[240:243]
s_waitcnt lgkmcnt(0)
ds_read_b64_tr_b16 v[96:97],v1 offset:128
ds_read_b64_tr_b16 v[98:99],v1 offset:5248
v_mfma_f32_16x16x32_bf16 a[240:243],v[120:123],a[160:163],a[240:243]
ds_read_b64_tr_b16 v[100:101],v1 offset:10368
v_mfma_f32_16x16x32_bf16 a[240:243],v[124:127],a[176:179],a[240:243]
ds_read_b64_tr_b16 v[102:103],v1 offset:15488
v_mfma_f32_16x16x32_bf16 a[244:247],v[124:127],a[180:183],a[244:247]
ds_read_b64_tr_b16 v[104:105],v1 offset:20608
v_mfma_f32_16x16x32_bf16 a[244:247],v[120:123],a[164:167],a[244:247]
ds_read_b64_tr_b16 v[106:107],v1 offset:25728
ds_read_b64_tr_b16 v[108:109],v1 offset:30848
v_mfma_f32_16x16x32_bf16 a[244:247],v[116:119],a[148:151],a[244:247]
ds_read_b64_tr_b16 v[110:111],v1 offset:35968
v_add_u32 v1,0xfffe2000,v1
v_mfma_f32_16x16x32_bf16 a[244:247],v[112:115],a[132:135],a[244:247]
v_accvgpr_read_b32 v20,a240
v_accvgpr_read_b32 v21,a241
v_mfma_f32_16x16x32_bf16 a[248:251],v[112:115],a[136:139],a[248:251]
v_accvgpr_read_b32 v22,a242
v_accvgpr_read_b32 v23,a243
v_mfma_f32_16x16x32_bf16 a[248:251],v[116:119],a[152:155],a[248:251]
v_cvt_pk_bf16_f32 v18,v20,v21
v_cvt_pk_bf16_f32 v19,v22,v23
v_mfma_f32_16x16x32_bf16 a[248:251],v[120:123],a[168:171],a[248:251]
buffer_store_dwordx4 v[16:19],v6,s[36:39],s75 offen sc1
v_mfma_f32_16x16x32_bf16 a[248:251],v[124:127],a[184:187],a[248:251]
v_accvgpr_read_b32 v12,a244
v_accvgpr_read_b32 v13,a245
v_mfma_f32_16x16x32_bf16 a[252:255],v[124:127],a[188:191],a[252:255]
v_accvgpr_read_b32 v14,a246
v_accvgpr_read_b32 v15,a247
v_mfma_f32_16x16x32_bf16 a[252:255],v[120:123],a[172:175],a[252:255]
v_cvt_pk_bf16_f32 v10,v12,v13
v_cvt_pk_bf16_f32 v11,v14,v15
v_mfma_f32_16x16x32_bf16 a[252:255],v[116:119],a[156:159],a[252:255]
v_accvgpr_read_b32 v16,a232
v_accvgpr_read_b32 v17,a233
v_mfma_f32_16x16x32_bf16 a[252:255],v[112:115],a[140:143],a[252:255]
v_accvgpr_read_b32 v18,a234
v_accvgpr_read_b32 v19,a235
v_mfma_f32_16x16x32_bf16 a[0:3],v[64:67],v[32:35],a[0:3]
v_accvgpr_read_b32 v20,a248
v_accvgpr_read_b32 v21,a249
v_mfma_f32_16x16x32_bf16 a[0:3],v[68:71],v[36:39],a[0:3]
v_accvgpr_read_b32 v22,a250
v_accvgpr_read_b32 v23,a251
v_mfma_f32_16x16x32_bf16 a[0:3],v[72:75],v[40:43],a[0:3]
v_cvt_pk_bf16_f32 v16,v16,v17
v_cvt_pk_bf16_f32 v17,v18,v19
v_mfma_f32_16x16x32_bf16 a[0:3],v[76:79],v[44:47],a[0:3]
v_cvt_pk_bf16_f32 v18,v20,v21
v_cvt_pk_bf16_f32 v19,v22,v23
v_mfma_f32_16x16x32_bf16 a[4:7],v[76:79],v[180:183],a[4:7]
ds_read_b64_tr_b16 v[112:113],v2 offset:160
ds_read_b64_tr_b16 v[114:115],v2 offset:5280
v_mfma_f32_16x16x32_bf16 a[4:7],v[72:75],v[164:167],a[4:7]
buffer_store_dwordx4 v[8:11],v6,s[36:39],s75 offen offset:1024 sc1
v_mfma_f32_16x16x32_bf16 a[4:7],v[68:71],v[148:151],a[4:7]
ds_read_b64_tr_b16 v[116:117],v2 offset:10400
ds_read_b64_tr_b16 v[118:119],v2 offset:15520
v_mfma_f32_16x16x32_bf16 a[4:7],v[64:67],v[132:135],a[4:7]
ds_read_b64_tr_b16 v[120:121],v2 offset:20640
v_accvgpr_read_b32 v8,a236
v_mfma_f32_16x16x32_bf16 a[8:11],v[64:67],v[196:199],a[8:11]
ds_read_b64_tr_b16 v[122:123],v2 offset:25760
v_accvgpr_read_b32 v9,a237
v_mfma_f32_16x16x32_bf16 a[8:11],v[68:71],v[212:215],a[8:11]
ds_read_b64_tr_b16 v[124:125],v2 offset:30880
v_accvgpr_read_b32 v10,a238
v_mfma_f32_16x16x32_bf16 a[8:11],v[72:75],v[228:231],a[8:11]
ds_read_b64_tr_b16 v[126:127],v2 offset:36000
v_accvgpr_read_b32 v11,a239
v_mfma_f32_16x16x32_bf16 a[8:11],v[76:79],v[244:247],a[8:11]
v_accvgpr_read_b32 v12,a252
v_accvgpr_read_b32 v13,a253
v_mfma_f32_16x16x32_bf16 a[12:15],v[76:79],v[188:191],a[12:15]
v_accvgpr_read_b32 v14,a254
v_accvgpr_read_b32 v15,a255
v_mfma_f32_16x16x32_bf16 a[12:15],v[72:75],v[172:175],a[12:15]
v_cvt_pk_bf16_f32 v8,v8,v9
v_cvt_pk_bf16_f32 v9,v10,v11
v_mfma_f32_16x16x32_bf16 a[12:15],v[68:71],v[156:159],a[12:15]
v_cvt_pk_bf16_f32 v10,v12,v13
v_cvt_pk_bf16_f32 v11,v14,v15
v_mfma_f32_16x16x32_bf16 a[12:15],v[64:67],v[140:143],a[12:15]
ds_read_b64_tr_b16 v[64:65],v1 offset:192
ds_read_b64_tr_b16 v[66:67],v1 offset:5312
v_mfma_f32_16x16x32_bf16 a[16:19],v[80:83],v[32:35],a[16:19]
buffer_store_dwordx4 v[16:19],v6,s[36:39],s75 offen offset:2048 sc1
v_mfma_f32_16x16x32_bf16 a[16:19],v[84:87],v[36:39],a[16:19]
ds_read_b64_tr_b16 v[68:69],v1 offset:10432
ds_read_b64_tr_b16 v[70:71],v1 offset:15552
v_mfma_f32_16x16x32_bf16 a[16:19],v[88:91],v[40:43],a[16:19]
ds_read_b64_tr_b16 v[72:73],v1 offset:20672
v_add_u32 v2,0xfffe2000,v2
v_mfma_f32_16x16x32_bf16 a[16:19],v[92:95],v[44:47],a[16:19]
ds_read_b64_tr_b16 v[74:75],v1 offset:25792
v_mfma_f32_16x16x32_bf16 a[20:23],v[92:95],v[180:183],a[20:23]
ds_read_b64_tr_b16 v[76:77],v1 offset:30912
v_mfma_f32_16x16x32_bf16 a[20:23],v[88:91],v[164:167],a[20:23]
ds_read_b64_tr_b16 v[78:79],v1 offset:36032
v_add_u32 v1,0x1e000,v1
v_mfma_f32_16x16x32_bf16 a[20:23],v[84:87],v[148:151],a[20:23]
v_mfma_f32_16x16x32_bf16 a[20:23],v[80:83],v[132:135],a[20:23]
v_mfma_f32_16x16x32_bf16 a[24:27],v[80:83],v[196:199],a[24:27]
v_mfma_f32_16x16x32_bf16 a[24:27],v[84:87],v[212:215],a[24:27]
v_mfma_f32_16x16x32_bf16 a[24:27],v[88:91],v[228:231],a[24:27]
v_mfma_f32_16x16x32_bf16 a[24:27],v[92:95],v[244:247],a[24:27]
buffer_store_dwordx4 v[8:11],v28,s[36:39],s75 offen offset:3072 sc1
v_mfma_f32_16x16x32_bf16 a[28:31],v[92:95],v[188:191],a[28:31]
ds_read_b64_tr_b16 v[92:93],v2 offset:30944
ds_read_b64_tr_b16 v[94:95],v2 offset:36064
v_mfma_f32_16x16x32_bf16 a[28:31],v[88:91],v[172:175],a[28:31]
ds_read_b64_tr_b16 v[88:89],v2 offset:20704
v_mfma_f32_16x16x32_bf16 a[28:31],v[84:87],v[156:159],a[28:31]
ds_read_b64_tr_b16 v[84:85],v2 offset:10464
v_mfma_f32_16x16x32_bf16 a[28:31],v[80:83],v[140:143],a[28:31]
s_waitcnt lgkmcnt(4)
ds_read_b64_tr_b16 v[80:81],v2 offset:224
v_mfma_f32_16x16x32_bf16 a[0:3],v[96:99],a[128:131],a[0:3]
ds_read_b64_tr_b16 v[82:83],v2 offset:5344
ds_read_b64_tr_b16 v[86:87],v2 offset:15584
v_mfma_f32_16x16x32_bf16 a[0:3],v[100:103],a[144:147],a[0:3]
ds_read_b64_tr_b16 v[90:91],v2 offset:25824
v_add_u32 v2,0x1e000,v2
v_mfma_f32_16x16x32_bf16 a[0:3],v[104:107],a[160:163],a[0:3]
v_mfma_f32_16x16x32_bf16 a[0:3],v[108:111],a[176:179],a[0:3]
v_mfma_f32_16x16x32_bf16 a[4:7],v[108:111],a[180:183],a[4:7]
v_mfma_f32_16x16x32_bf16 a[4:7],v[104:107],a[164:167],a[4:7]
v_mfma_f32_16x16x32_bf16 a[4:7],v[100:103],a[148:151],a[4:7]
v_mfma_f32_16x16x32_bf16 a[4:7],v[96:99],a[132:135],a[4:7]
v_mfma_f32_16x16x32_bf16 a[8:11],v[96:99],a[136:139],a[8:11]
v_mfma_f32_16x16x32_bf16 a[8:11],v[100:103],a[152:155],a[8:11]
v_mfma_f32_16x16x32_bf16 a[8:11],v[104:107],a[168:171],a[8:11]
v_mfma_f32_16x16x32_bf16 a[8:11],v[108:111],a[184:187],a[8:11]
v_accvgpr_read_b32 v16,a0
v_accvgpr_read_b32 v17,a1
v_mfma_f32_16x16x32_bf16 a[12:15],v[108:111],a[188:191],a[12:15]
v_accvgpr_read_b32 v18,a2
v_accvgpr_read_b32 v19,a3
v_mfma_f32_16x16x32_bf16 a[12:15],v[104:107],a[172:175],a[12:15]
v_cvt_pk_bf16_f32 v16,v16,v17
v_cvt_pk_bf16_f32 v17,v18,v19
v_mfma_f32_16x16x32_bf16 a[12:15],v[100:103],a[156:159],a[12:15]
v_accvgpr_read_b32 v8,a4
v_accvgpr_read_b32 v9,a5
v_mfma_f32_16x16x32_bf16 a[12:15],v[96:99],a[140:143],a[12:15]
v_accvgpr_read_b32 v10,a6
v_accvgpr_read_b32 v11,a7
v_mfma_f32_16x16x32_bf16 a[16:19],v[112:115],a[128:131],a[16:19]
v_cvt_pk_bf16_f32 v8,v8,v9
v_cvt_pk_bf16_f32 v9,v10,v11
v_mfma_f32_16x16x32_bf16 a[16:19],v[116:119],a[144:147],a[16:19]
ds_read_b64_tr_b16 v[96:97],v1 offset:192
ds_read_b64_tr_b16 v[98:99],v1 offset:5312
v_mfma_f32_16x16x32_bf16 a[16:19],v[120:123],a[160:163],a[16:19]
ds_read_b64_tr_b16 v[100:101],v1 offset:10432
v_mfma_f32_16x16x32_bf16 a[16:19],v[124:127],a[176:179],a[16:19]
ds_read_b64_tr_b16 v[102:103],v1 offset:15552
v_mfma_f32_16x16x32_bf16 a[20:23],v[124:127],a[180:183],a[20:23]
ds_read_b64_tr_b16 v[104:105],v1 offset:20672
v_mfma_f32_16x16x32_bf16 a[20:23],v[120:123],a[164:167],a[20:23]
ds_read_b64_tr_b16 v[106:107],v1 offset:25792
ds_read_b64_tr_b16 v[108:109],v1 offset:30912
v_mfma_f32_16x16x32_bf16 a[20:23],v[116:119],a[148:151],a[20:23]
ds_read_b64_tr_b16 v[110:111],v1 offset:36032
v_add_u32 v1,0xfffec000,v1
v_mfma_f32_16x16x32_bf16 a[20:23],v[112:115],a[132:135],a[20:23]
v_accvgpr_read_b32 v20,a16
v_accvgpr_read_b32 v21,a17
v_mfma_f32_16x16x32_bf16 a[24:27],v[112:115],a[136:139],a[24:27]
v_accvgpr_read_b32 v22,a18
v_accvgpr_read_b32 v23,a19
v_mfma_f32_16x16x32_bf16 a[24:27],v[116:119],a[152:155],a[24:27]
v_cvt_pk_bf16_f32 v18,v20,v21
v_cvt_pk_bf16_f32 v19,v22,v23
v_mfma_f32_16x16x32_bf16 a[24:27],v[120:123],a[168:171],a[24:27]
buffer_store_dwordx4 v[16:19],v6,s[36:39],s76 offen sc1
v_mfma_f32_16x16x32_bf16 a[24:27],v[124:127],a[184:187],a[24:27]
v_accvgpr_read_b32 v12,a20
v_accvgpr_read_b32 v13,a21
v_mfma_f32_16x16x32_bf16 a[28:31],v[124:127],a[188:191],a[28:31]
v_accvgpr_read_b32 v14,a22
v_accvgpr_read_b32 v15,a23
v_mfma_f32_16x16x32_bf16 a[28:31],v[120:123],a[172:175],a[28:31]
v_cvt_pk_bf16_f32 v10,v12,v13
v_cvt_pk_bf16_f32 v11,v14,v15
v_mfma_f32_16x16x32_bf16 a[28:31],v[116:119],a[156:159],a[28:31]
v_accvgpr_read_b32 v16,a8
v_accvgpr_read_b32 v17,a9
v_mfma_f32_16x16x32_bf16 a[28:31],v[112:115],a[140:143],a[28:31]
v_accvgpr_read_b32 v18,a10
v_accvgpr_read_b32 v19,a11
v_mfma_f32_16x16x32_bf16 v[128:131],v[64:67],v[32:35],v[128:131]
v_accvgpr_read_b32 v20,a24
v_accvgpr_read_b32 v21,a25
v_mfma_f32_16x16x32_bf16 v[128:131],v[68:71],v[36:39],v[128:131]
v_accvgpr_read_b32 v22,a26
v_accvgpr_read_b32 v23,a27
v_mfma_f32_16x16x32_bf16 v[128:131],v[72:75],v[40:43],v[128:131]
v_cvt_pk_bf16_f32 v16,v16,v17
v_cvt_pk_bf16_f32 v17,v18,v19
v_mfma_f32_16x16x32_bf16 v[128:131],v[76:79],v[44:47],v[128:131]
v_cvt_pk_bf16_f32 v18,v20,v21
)A"
R"A(v_cvt_pk_bf16_f32 v19,v22,v23
v_mfma_f32_16x16x32_bf16 v[136:139],v[76:79],v[180:183],v[136:139]
ds_read_b64_tr_b16 v[112:113],v2 offset:224
ds_read_b64_tr_b16 v[114:115],v2 offset:5344
s_nop 0
L_pd19_%=:
s_cbranch_execz L_trtg2F_%=
v_mfma_f32_16x16x32_bf16 v[136:139],v[72:75],v[164:167],v[136:139]
buffer_store_dwordx4 v[8:11],v6,s[36:39],s76 offen offset:1024 sc1
v_mfma_f32_16x16x32_bf16 v[136:139],v[68:71],v[148:151],v[136:139]
ds_read_b64_tr_b16 v[116:117],v2 offset:10464
ds_read_b64_tr_b16 v[118:119],v2 offset:15584
v_mfma_f32_16x16x32_bf16 v[136:139],v[64:67],v[132:135],v[136:139]
ds_read_b64_tr_b16 v[120:121],v2 offset:20704
v_accvgpr_read_b32 v8,a12
v_mfma_f32_16x16x32_bf16 v[144:147],v[64:67],v[196:199],v[144:147]
ds_read_b64_tr_b16 v[122:123],v2 offset:25824
v_accvgpr_read_b32 v9,a13
v_mfma_f32_16x16x32_bf16 v[144:147],v[68:71],v[212:215],v[144:147]
ds_read_b64_tr_b16 v[124:125],v2 offset:30944
v_accvgpr_read_b32 v10,a14
v_mfma_f32_16x16x32_bf16 v[144:147],v[72:75],v[228:231],v[144:147]
ds_read_b64_tr_b16 v[126:127],v2 offset:36064
v_accvgpr_read_b32 v11,a15
v_mfma_f32_16x16x32_bf16 v[144:147],v[76:79],v[244:247],v[144:147]
v_accvgpr_read_b32 v12,a28
v_accvgpr_read_b32 v13,a29
v_mfma_f32_16x16x32_bf16 v[152:155],v[76:79],v[188:191],v[152:155]
v_accvgpr_read_b32 v14,a30
v_accvgpr_read_b32 v15,a31
v_mfma_f32_16x16x32_bf16 v[152:155],v[72:75],v[172:175],v[152:155]
v_cvt_pk_bf16_f32 v8,v8,v9
v_cvt_pk_bf16_f32 v9,v10,v11
v_mfma_f32_16x16x32_bf16 v[152:155],v[68:71],v[156:159],v[152:155]
v_cvt_pk_bf16_f32 v10,v12,v13
v_cvt_pk_bf16_f32 v11,v14,v15
v_mfma_f32_16x16x32_bf16 v[152:155],v[64:67],v[140:143],v[152:155]
s_waitcnt lgkmcnt(3)
ds_read_b64_tr_b16 v[64:65],v1 offset:256
ds_read_b64_tr_b16 v[66:67],v1 offset:5376
v_mfma_f32_16x16x32_bf16 v[160:163],v[80:83],v[32:35],v[160:163]
buffer_store_dwordx4 v[16:19],v6,s[36:39],s76 offen offset:2048 sc1
v_mfma_f32_16x16x32_bf16 v[160:163],v[84:87],v[36:39],v[160:163]
ds_read_b64_tr_b16 v[68:69],v1 offset:10496
ds_read_b64_tr_b16 v[70:71],v1 offset:15616
v_mfma_f32_16x16x32_bf16 v[160:163],v[88:91],v[40:43],v[160:163]
ds_read_b64_tr_b16 v[72:73],v1 offset:20736
v_add_u32 v2,0xfffec000,v2
v_mfma_f32_16x16x32_bf16 v[160:163],v[92:95],v[44:47],v[160:163]
ds_read_b64_tr_b16 v[74:75],v1 offset:25856
v_mov_b32 v20,s52
v_mfma_f32_16x16x32_bf16 v[168:171],v[92:95],v[180:183],v[168:171]
ds_read_b64_tr_b16 v[76:77],v1 offset:30976
v_mov_b32 v21,s53
v_mfma_f32_16x16x32_bf16 v[168:171],v[88:91],v[164:167],v[168:171]
ds_read_b64_tr_b16 v[78:79],v1 offset:36096
v_add_u32 v1,0xffff6000,v1
v_mfma_f32_16x16x32_bf16 v[168:171],v[84:87],v[148:151],v[168:171]
v_lshlrev_b32 v22,20,v6
v_mfma_f32_16x16x32_bf16 v[168:171],v[80:83],v[132:135],v[168:171]
v_mfma_f32_16x16x32_bf16 v[176:179],v[80:83],v[196:199],v[176:179]
v_mfma_f32_16x16x32_bf16 v[176:179],v[84:87],v[212:215],v[176:179]
v_mfma_f32_16x16x32_bf16 v[176:179],v[88:91],v[228:231],v[176:179]
v_mfma_f32_16x16x32_bf16 v[176:179],v[92:95],v[244:247],v[176:179]
buffer_store_dwordx4 v[8:11],v28,s[36:39],s76 offen offset:3072 sc1
v_mfma_f32_16x16x32_bf16 v[184:187],v[92:95],v[188:191],v[184:187]
ds_read_b64_tr_b16 v[92:93],v2 offset:31008
ds_read_b64_tr_b16 v[94:95],v2 offset:36128
v_mfma_f32_16x16x32_bf16 v[184:187],v[88:91],v[172:175],v[184:187]
ds_read_b64_tr_b16 v[88:89],v2 offset:20768
v_mfma_f32_16x16x32_bf16 v[184:187],v[84:87],v[156:159],v[184:187]
ds_read_b64_tr_b16 v[84:85],v2 offset:10528
v_mfma_f32_16x16x32_bf16 v[184:187],v[80:83],v[140:143],v[184:187]
ds_read_b64_tr_b16 v[80:81],v2 offset:288
v_mfma_f32_16x16x32_bf16 v[128:131],v[96:99],a[128:131],v[128:131]
ds_read_b64_tr_b16 v[82:83],v2 offset:5408
ds_read_b64_tr_b16 v[86:87],v2 offset:15648
v_mfma_f32_16x16x32_bf16 v[128:131],v[100:103],a[144:147],v[128:131]
ds_read_b64_tr_b16 v[90:91],v2 offset:25888
v_add_u32 v2,0xffff6000,v2
v_mfma_f32_16x16x32_bf16 v[128:131],v[104:107],a[160:163],v[128:131]
v_mfma_f32_16x16x32_bf16 v[128:131],v[108:111],a[176:179],v[128:131]
v_mfma_f32_16x16x32_bf16 v[136:139],v[108:111],a[180:183],v[136:139]
v_mfma_f32_16x16x32_bf16 v[136:139],v[104:107],a[164:167],v[136:139]
v_mfma_f32_16x16x32_bf16 v[136:139],v[100:103],a[148:151],v[136:139]
v_mfma_f32_16x16x32_bf16 v[136:139],v[96:99],a[132:135],v[136:139]
v_mfma_f32_16x16x32_bf16 v[144:147],v[96:99],a[136:139],v[144:147]
v_mfma_f32_16x16x32_bf16 v[144:147],v[100:103],a[152:155],v[144:147]
v_mfma_f32_16x16x32_bf16 v[144:147],v[104:107],a[168:171],v[144:147]
v_mfma_f32_16x16x32_bf16 v[144:147],v[108:111],a[184:187],v[144:147]
v_cvt_pk_bf16_f32 v128,v128,v129
v_cvt_pk_bf16_f32 v129,v130,v131
v_mfma_f32_16x16x32_bf16 v[152:155],v[108:111],a[188:191],v[152:155]
ds_read_b64_tr_b16 v[108:109],v1 offset:30976
ds_read_b64_tr_b16 v[110:111],v1 offset:36096
v_mfma_f32_16x16x32_bf16 v[152:155],v[104:107],a[172:175],v[152:155]
v_cvt_pk_bf16_f32 v136,v136,v137
v_cvt_pk_bf16_f32 v137,v138,v139
v_mfma_f32_16x16x32_bf16 v[152:155],v[100:103],a[156:159],v[152:155]
v_cvt_pk_bf16_f32 v144,v144,v145
v_cvt_pk_bf16_f32 v145,v146,v147
v_mfma_f32_16x16x32_bf16 v[152:155],v[96:99],a[140:143],v[152:155]
ds_read_b64_tr_b16 v[96:97],v1 offset:256
ds_read_b64_tr_b16 v[98:99],v1 offset:5376
v_mfma_f32_16x16x32_bf16 v[160:163],v[112:115],a[128:131],v[160:163]
ds_read_b64_tr_b16 v[100:101],v1 offset:10496
v_mfma_f32_16x16x32_bf16 v[160:163],v[116:119],a[144:147],v[160:163]
s_waitcnt lgkmcnt(5)
ds_read_b64_tr_b16 v[102:103],v1 offset:15616
v_mfma_f32_16x16x32_bf16 v[160:163],v[120:123],a[160:163],v[160:163]
ds_read_b64_tr_b16 v[104:105],v1 offset:20736
v_mfma_f32_16x16x32_bf16 v[160:163],v[124:127],a[176:179],v[160:163]
v_cvt_pk_bf16_f32 v152,v152,v153
v_cvt_pk_bf16_f32 v153,v154,v155
v_mfma_f32_16x16x32_bf16 v[168:171],v[124:127],a[180:183],v[168:171]
ds_read_b64_tr_b16 v[106:107],v1 offset:25856
v_add_u32 v1,0x1e000,v1
v_mfma_f32_16x16x32_bf16 v[168:171],v[120:123],a[164:167],v[168:171]
v_mfma_f32_16x16x32_bf16 v[168:171],v[116:119],a[148:151],v[168:171]
v_mfma_f32_16x16x32_bf16 v[168:171],v[112:115],a[132:135],v[168:171]
v_cvt_pk_bf16_f32 v130,v160,v161
v_cvt_pk_bf16_f32 v131,v162,v163
v_mfma_f32_16x16x32_bf16 v[176:179],v[112:115],a[136:139],v[176:179]
buffer_store_dwordx4 v[128:131],v6,s[36:39],s79 offen sc1
v_mfma_f32_16x16x32_bf16 v[176:179],v[116:119],a[152:155],v[176:179]
v_mfma_f32_16x16x32_bf16 v[176:179],v[120:123],a[168:171],v[176:179]
v_mfma_f32_16x16x32_bf16 v[176:179],v[124:127],a[184:187],v[176:179]
v_mfma_f32_16x16x32_bf16 v[184:187],v[124:127],a[188:191],v[184:187]
v_cvt_pk_bf16_f32 v138,v168,v169
v_cvt_pk_bf16_f32 v139,v170,v171
v_mfma_f32_16x16x32_bf16 v[184:187],v[120:123],a[172:175],v[184:187]
buffer_store_dwordx4 v[136:139],v6,s[36:39],s80 offen sc1
v_mfma_f32_16x16x32_bf16 v[184:187],v[116:119],a[156:159],v[184:187]
ds_read_b64_tr_b16 v[116:117],v2 offset:10528
ds_read_b64_tr_b16 v[118:119],v2 offset:15648
v_mfma_f32_16x16x32_bf16 v[184:187],v[112:115],a[140:143],v[184:187]
v_cvt_pk_bf16_f32 v146,v176,v177
v_cvt_pk_bf16_f32 v147,v178,v179
s_waitcnt vmcnt(6)
s_barrier
v_mfma_f32_16x16x32_bf16 v[192:195],v[64:67],a[68:71],v[192:195]
buffer_store_dwordx4 v[144:147],v6,s[36:39],s81 offen sc1
v_mfma_f32_16x16x32_bf16 v[192:195],v[68:71],a[76:79],v[192:195]
ds_read_b64_tr_b16 v[112:113],v2 offset:288
ds_read_b64_tr_b16 v[114:115],v2 offset:5408
v_mfma_f32_16x16x32_bf16 v[192:195],v[72:75],a[84:87],v[192:195]
buffer_store_dwordx2 v[58:59],v60,s[44:47],s18 offen offset:2312 sc1
v_cvt_pk_bf16_f32 v154,v184,v185
v_mfma_f32_16x16x32_bf16 v[192:195],v[76:79],a[92:95],v[192:195]
ds_read_b64_tr_b16 v[120:121],v2 offset:20768
v_cvt_pk_bf16_f32 v155,v186,v187
v_mfma_f32_16x16x32_bf16 v[200:203],v[76:79],a[124:127],v[200:203]
buffer_store_dwordx4 v[152:155],v28,s[36:39],s82 offen sc1
v_mfma_f32_16x16x32_bf16 v[200:203],v[72:75],a[116:119],v[200:203]
ds_read_b64_tr_b16 v[122:123],v2 offset:25888
ds_read_b64_tr_b16 v[124:125],v2 offset:31008
v_mfma_f32_16x16x32_bf16 v[200:203],v[68:71],a[108:111],v[200:203]
ds_read_b64_tr_b16 v[126:127],v2 offset:36128
v_add_u32 v2,0x1e000,v2
v_mfma_f32_16x16x32_bf16 v[200:203],v[64:67],a[100:103],v[200:203]
v_mfma_f32_16x16x32_bf16 v[208:211],v[64:67],a[64:67],v[208:211]
v_mfma_f32_16x16x32_bf16 v[208:211],v[68:71],a[72:75],v[208:211]
v_mfma_f32_16x16x32_bf16 v[208:211],v[72:75],a[80:83],v[208:211]
v_mfma_f32_16x16x32_bf16 v[208:211],v[76:79],a[88:91],v[208:211]
v_mfma_f32_16x16x32_bf16 v[216:219],v[76:79],a[120:123],v[216:219]
ds_read_b64_tr_b16 v[76:77],v1 offset:30976
ds_read_b64_tr_b16 v[78:79],v1 offset:36096
v_mfma_f32_16x16x32_bf16 v[216:219],v[72:75],a[112:115],v[216:219]
ds_read_b64_tr_b16 v[72:73],v1 offset:20736
v_mfma_f32_16x16x32_bf16 v[216:219],v[68:71],a[104:107],v[216:219]
ds_read_b64_tr_b16 v[68:69],v1 offset:10496
v_mfma_f32_16x16x32_bf16 v[216:219],v[64:67],a[96:99],v[216:219]
ds_read_b64_tr_b16 v[64:65],v1 offset:256
v_mfma_f32_16x16x32_bf16 v[224:227],v[80:83],a[68:71],v[224:227]
ds_read_b64_tr_b16 v[66:67],v1 offset:5376
ds_read_b64_tr_b16 v[70:71],v1 offset:15616
v_mfma_f32_16x16x32_bf16 v[224:227],v[84:87],a[76:79],v[224:227]
ds_read_b64_tr_b16 v[74:75],v1 offset:25856
v_mfma_f32_16x16x32_bf16 v[224:227],v[88:91],a[84:87],v[224:227]
v_mfma_f32_16x16x32_bf16 v[224:227],v[92:95],a[92:95],v[224:227]
v_mfma_f32_16x16x32_bf16 v[232:235],v[92:95],a[124:127],v[232:235]
v_mfma_f32_16x16x32_bf16 v[232:235],v[88:91],a[116:119],v[232:235]
v_mfma_f32_16x16x32_bf16 v[232:235],v[84:87],a[108:111],v[232:235]
v_mfma_f32_16x16x32_bf16 v[232:235],v[80:83],a[100:103],v[232:235]
v_mfma_f32_16x16x32_bf16 v[240:243],v[80:83],a[64:67],v[240:243]
v_mfma_f32_16x16x32_bf16 v[240:243],v[84:87],a[72:75],v[240:243]
v_mfma_f32_16x16x32_bf16 v[240:243],v[88:91],a[80:83],v[240:243]
v_mfma_f32_16x16x32_bf16 v[240:243],v[92:95],a[88:91],v[240:243]
v_mfma_f32_16x16x32_bf16 v[248:251],v[92:95],a[120:123],v[248:251]
ds_read_b64_tr_b16 v[92:93],v2 offset:31008
ds_read_b64_tr_b16 v[94:95],v2 offset:36128
v_mfma_f32_16x16x32_bf16 v[248:251],v[88:91],a[112:115],v[248:251]
ds_read_b64_tr_b16 v[88:89],v2 offset:20768
v_mfma_f32_16x16x32_bf16 v[248:251],v[84:87],a[104:107],v[248:251]
ds_read_b64_tr_b16 v[84:85],v2 offset:10528
v_mfma_f32_16x16x32_bf16 v[248:251],v[80:83],a[96:99],v[248:251]
s_waitcnt lgkmcnt(4)
ds_read_b64_tr_b16 v[80:81],v2 offset:288
v_mfma_f32_16x16x32_bf16 v[192:195],v[96:99],v[32:35],v[192:195]
ds_read_b64_tr_b16 v[82:83],v2 offset:5408
ds_read_b64_tr_b16 v[86:87],v2 offset:15648
v_mfma_f32_16x16x32_bf16 v[192:195],v[100:103],v[36:39],v[192:195]
ds_read_b64_tr_b16 v[90:91],v2 offset:25888
v_mfma_f32_16x16x32_bf16 v[192:195],v[104:107],v[40:43],v[192:195]
v_mfma_f32_16x16x32_bf16 v[192:195],v[108:111],v[44:47],v[192:195]
v_mfma_f32_16x16x32_bf16 v[200:203],v[108:111],v[180:183],v[200:203]
v_mfma_f32_16x16x32_bf16 v[200:203],v[104:107],v[164:167],v[200:203]
v_mfma_f32_16x16x32_bf16 v[200:203],v[100:103],v[148:151],v[200:203]
v_mfma_f32_16x16x32_bf16 v[200:203],v[96:99],v[132:135],v[200:203]
v_mfma_f32_16x16x32_bf16 v[208:211],v[96:99],v[196:199],v[208:211]
v_mfma_f32_16x16x32_bf16 v[208:211],v[100:103],v[212:215],v[208:211]
v_mfma_f32_16x16x32_bf16 v[208:211],v[104:107],v[228:231],v[208:211]
v_mfma_f32_16x16x32_bf16 v[208:211],v[108:111],v[244:247],v[208:211]
v_mfma_f32_16x16x32_bf16 v[216:219],v[108:111],v[188:191],v[216:219]
v_mfma_f32_16x16x32_bf16 v[216:219],v[104:107],v[172:175],v[216:219]
v_mfma_f32_16x16x32_bf16 v[216:219],v[100:103],v[156:159],v[216:219]
v_mfma_f32_16x16x32_bf16 v[216:219],v[96:99],v[140:143],v[216:219]
v_mfma_f32_16x16x32_bf16 v[224:227],v[112:115],v[32:35],v[224:227]
v_mfma_f32_16x16x32_bf16 v[224:227],v[116:119],v[36:39],v[224:227]
v_mfma_f32_16x16x32_bf16 v[224:227],v[120:123],v[40:43],v[224:227]
v_mfma_f32_16x16x32_bf16 v[224:227],v[124:127],v[44:47],v[224:227]
v_mfma_f32_16x16x32_bf16 v[232:235],v[124:127],v[180:183],v[232:235]
v_mfma_f32_16x16x32_bf16 v[232:235],v[120:123],v[164:167],v[232:235]
v_mfma_f32_16x16x32_bf16 v[232:235],v[116:119],v[148:151],v[232:235]
v_mfma_f32_16x16x32_bf16 v[232:235],v[112:115],v[132:135],v[232:235]
)A"
R"A(v_mfma_f32_16x16x32_bf16 v[240:243],v[112:115],v[196:199],v[240:243]
v_mfma_f32_16x16x32_bf16 v[240:243],v[116:119],v[212:215],v[240:243]
v_mfma_f32_16x16x32_bf16 v[240:243],v[120:123],v[228:231],v[240:243]
v_mfma_f32_16x16x32_bf16 v[240:243],v[124:127],v[244:247],v[240:243]
v_mfma_f32_16x16x32_bf16 v[248:251],v[124:127],v[188:191],v[248:251]
v_mfma_f32_16x16x32_bf16 v[248:251],v[120:123],v[172:175],v[248:251]
v_mfma_f32_16x16x32_bf16 v[248:251],v[116:119],v[156:159],v[248:251]
v_mfma_f32_16x16x32_bf16 v[248:251],v[112:115],v[140:143],v[248:251]
s_waitcnt vmcnt(0)
s_barrier
v_mfma_f32_16x16x32_bf16 v[192:195],v[64:67],a[128:131],v[192:195]
buffer_store_dwordx2 v[58:59],v60,s[44:47],s18 offen offset:2304 sc1
v_mfma_f32_16x16x32_bf16 v[192:195],v[68:71],a[144:147],v[192:195]
v_mfma_f32_16x16x32_bf16 v[192:195],v[72:75],a[160:163],v[192:195]
v_mfma_f32_16x16x32_bf16 v[192:195],v[76:79],a[176:179],v[192:195]
v_mfma_f32_16x16x32_bf16 v[200:203],v[76:79],a[180:183],v[200:203]
v_mfma_f32_16x16x32_bf16 v[200:203],v[72:75],a[164:167],v[200:203]
v_mfma_f32_16x16x32_bf16 v[200:203],v[68:71],a[148:151],v[200:203]
v_mfma_f32_16x16x32_bf16 v[200:203],v[64:67],a[132:135],v[200:203]
v_mfma_f32_16x16x32_bf16 v[208:211],v[64:67],a[136:139],v[208:211]
v_mfma_f32_16x16x32_bf16 v[208:211],v[68:71],a[152:155],v[208:211]
v_mfma_f32_16x16x32_bf16 v[208:211],v[72:75],a[168:171],v[208:211]
v_mfma_f32_16x16x32_bf16 v[208:211],v[76:79],a[184:187],v[208:211]
v_mfma_f32_16x16x32_bf16 v[216:219],v[76:79],a[188:191],v[216:219]
v_mfma_f32_16x16x32_bf16 v[216:219],v[72:75],a[172:175],v[216:219]
v_mfma_f32_16x16x32_bf16 v[216:219],v[68:71],a[156:159],v[216:219]
v_mfma_f32_16x16x32_bf16 v[216:219],v[64:67],a[140:143],v[216:219]
s_waitcnt lgkmcnt(0)
v_mfma_f32_16x16x32_bf16 v[224:227],v[80:83],a[128:131],v[224:227]
buffer_load_dwordx2 v[62:63],v61,s[44:47],s91 offen offset:2312 sc1
v_mfma_f32_16x16x32_bf16 v[224:227],v[84:87],a[144:147],v[224:227]
v_mfma_f32_16x16x32_bf16 v[224:227],v[88:91],a[160:163],v[224:227]
v_mfma_f32_16x16x32_bf16 v[224:227],v[92:95],a[176:179],v[224:227]
v_mfma_f32_16x16x32_bf16 v[232:235],v[92:95],a[180:183],v[232:235]
v_mfma_f32_16x16x32_bf16 v[232:235],v[88:91],a[164:167],v[232:235]
v_mfma_f32_16x16x32_bf16 v[232:235],v[84:87],a[148:151],v[232:235]
v_mfma_f32_16x16x32_bf16 v[232:235],v[80:83],a[132:135],v[232:235]
v_mfma_f32_16x16x32_bf16 v[240:243],v[80:83],a[136:139],v[240:243]
v_mfma_f32_16x16x32_bf16 v[240:243],v[84:87],a[152:155],v[240:243]
v_mfma_f32_16x16x32_bf16 v[240:243],v[88:91],a[168:171],v[240:243]
v_mfma_f32_16x16x32_bf16 v[240:243],v[92:95],a[184:187],v[240:243]
v_mfma_f32_16x16x32_bf16 v[248:251],v[92:95],a[188:191],v[248:251]
v_mfma_f32_16x16x32_bf16 v[248:251],v[88:91],a[172:175],v[248:251]
v_mfma_f32_16x16x32_bf16 v[248:251],v[84:87],a[156:159],v[248:251]
v_mfma_f32_16x16x32_bf16 v[248:251],v[80:83],a[140:143],v[248:251]
s_waitcnt vmcnt(0)
v_cmp_ne_u32 s[56:57],s52,v62
v_cmp_ne_u32 s[58:59],s53,v63
s_nop 0
s_or_b64 s[56:57],s[56:57],s[58:59]
s_cbranch_scc1 L_fr4_%=
L_fo4_%=:
buffer_load_dwordx4 v[64:67],v6,s[36:39],s66 offen sc1
buffer_load_dwordx4 v[68:71],v6,s[36:39],s67 offen sc1
buffer_load_dwordx4 v[72:75],v6,s[36:39],s66 offen offset:1024 sc1
buffer_load_dwordx4 v[76:79],v6,s[36:39],s67 offen offset:1024 sc1
buffer_load_dwordx4 v[80:83],v6,s[36:39],s66 offen offset:2048 sc1
buffer_load_dwordx4 v[84:87],v6,s[36:39],s67 offen offset:2048 sc1
buffer_load_dwordx4 v[88:91],v28,s[36:39],s66 offen offset:3072 sc1
buffer_load_dwordx4 v[92:95],v28,s[36:39],s67 offen offset:3072 sc1
buffer_load_dwordx2 v[4:5],v3,s[44:47],s91 offen offset:2304 sc1
s_waitcnt vmcnt(8)
v_mfma_f32_16x16x32_bf16 v[192:195],v[48:51],v[64:67],v[192:195]
s_waitcnt vmcnt(7)
v_mfma_f32_16x16x32_bf16 v[192:195],v[48:51],v[68:71],v[192:195]
v_mfma_f32_16x16x32_bf16 v[224:227],v[52:55],v[64:67],v[224:227]
v_mfma_f32_16x16x32_bf16 v[224:227],v[52:55],v[68:71],v[224:227]
s_waitcnt vmcnt(6)
v_mfma_f32_16x16x32_bf16 v[200:203],v[48:51],v[72:75],v[200:203]
s_waitcnt vmcnt(5)
v_mfma_f32_16x16x32_bf16 v[200:203],v[48:51],v[76:79],v[200:203]
v_mfma_f32_16x16x32_bf16 v[232:235],v[52:55],v[72:75],v[232:235]
v_mfma_f32_16x16x32_bf16 v[232:235],v[52:55],v[76:79],v[232:235]
s_waitcnt vmcnt(4)
v_mfma_f32_16x16x32_bf16 v[208:211],v[48:51],v[80:83],v[208:211]
s_waitcnt vmcnt(3)
v_mfma_f32_16x16x32_bf16 v[208:211],v[48:51],v[84:87],v[208:211]
v_mfma_f32_16x16x32_bf16 v[240:243],v[52:55],v[80:83],v[240:243]
v_mfma_f32_16x16x32_bf16 v[240:243],v[52:55],v[84:87],v[240:243]
s_waitcnt vmcnt(2)
v_mfma_f32_16x16x32_bf16 v[216:219],v[48:51],v[88:91],v[216:219]
s_waitcnt vmcnt(1)
v_mfma_f32_16x16x32_bf16 v[216:219],v[48:51],v[92:95],v[216:219]
v_mfma_f32_16x16x32_bf16 v[248:251],v[52:55],v[88:91],v[248:251]
v_mfma_f32_16x16x32_bf16 v[248:251],v[52:55],v[92:95],v[248:251]
s_waitcnt vmcnt(0)
v_cmp_ne_u32 s[56:57],s52,v4
v_cmp_ne_u32 s[58:59],s53,v5
s_nop 0
s_or_b64 s[56:57],s[56:57],s[58:59]
s_cbranch_scc1 L_fr5_%=
L_fo5_%=:
buffer_load_dwordx4 v[96:99],v6,s[36:39],s68 offen sc1
buffer_load_dwordx4 v[100:103],v6,s[36:39],s68 offen offset:1024 sc1
buffer_load_dwordx4 v[104:107],v6,s[36:39],s68 offen offset:2048 sc1
buffer_load_dwordx4 v[108:111],v6,s[36:39],s68 offen offset:3072 sc1
buffer_load_dwordx4 v[112:115],v6,s[36:39],s65 offen sc1
buffer_load_dwordx4 v[116:119],v6,s[36:39],s65 offen offset:1024 sc1
buffer_load_dwordx4 v[120:123],v6,s[36:39],s65 offen offset:2048 sc1
buffer_load_dwordx4 v[124:127],v28,s[36:39],s65 offen offset:3072 sc1
s_waitcnt vmcnt(7)
v_mfma_f32_16x16x32_bf16 v[128:131],v[48:51],v[96:99],0
s_waitcnt vmcnt(6)
v_mfma_f32_16x16x32_bf16 v[128:131],v[48:51],v[100:103],v[128:131]
s_waitcnt vmcnt(5)
v_mfma_f32_16x16x32_bf16 v[128:131],v[48:51],v[104:107],v[128:131]
s_waitcnt vmcnt(4)
v_mfma_f32_16x16x32_bf16 v[128:131],v[48:51],v[108:111],v[128:131]
v_mfma_f32_16x16x32_bf16 v[160:163],v[52:55],v[96:99],0
v_mfma_f32_16x16x32_bf16 v[160:163],v[52:55],v[100:103],v[160:163]
v_mfma_f32_16x16x32_bf16 v[160:163],v[52:55],v[104:107],v[160:163]
v_mfma_f32_16x16x32_bf16 v[160:163],v[52:55],v[108:111],v[160:163]
s_waitcnt vmcnt(3)
v_mfma_f32_16x16x32_bf16 v[192:195],v[48:51],v[112:115],v[192:195]
v_mfma_f32_16x16x32_bf16 v[224:227],v[52:55],v[112:115],v[224:227]
s_waitcnt vmcnt(2)
v_mfma_f32_16x16x32_bf16 v[200:203],v[48:51],v[116:119],v[200:203]
v_mfma_f32_16x16x32_bf16 v[232:235],v[52:55],v[116:119],v[232:235]
s_waitcnt vmcnt(1)
v_cvt_pk_bf16_f32 v128,v128,v129
v_cvt_pk_bf16_f32 v129,v130,v131
v_mfma_f32_16x16x32_bf16 v[208:211],v[48:51],v[120:123],v[208:211]
v_mfma_f32_16x16x32_bf16 v[240:243],v[52:55],v[120:123],v[240:243]
s_waitcnt vmcnt(0)
v_cvt_pk_bf16_f32 v130,v160,v161
v_cvt_pk_bf16_f32 v131,v162,v163
v_mfma_f32_16x16x32_bf16 v[216:219],v[48:51],v[124:127],v[216:219]
v_cvt_pk_bf16_f32 v192,v192,v193
v_cvt_pk_bf16_f32 v193,v194,v195
v_cvt_pk_bf16_f32 v194,v224,v225
v_cvt_pk_bf16_f32 v195,v226,v227
v_cvt_pk_bf16_f32 v200,v200,v201
v_cvt_pk_bf16_f32 v201,v202,v203
v_mfma_f32_16x16x32_bf16 v[248:251],v[52:55],v[124:127],v[248:251]
v_permlane16_swap_b32 v128,v130
v_permlane16_swap_b32 v129,v131
v_cvt_pk_bf16_f32 v202,v232,v233
v_cvt_pk_bf16_f32 v203,v234,v235
v_cvt_pk_bf16_f32 v208,v208,v209
v_cvt_pk_bf16_f32 v209,v210,v211
v_cvt_pk_bf16_f32 v210,v240,v241
v_cvt_pk_bf16_f32 v211,v242,v243
buffer_store_dwordx4 v[128:131],v56,s[24:27],s83 offen offset:256 sc1 nt
v_permlane16_swap_b32 v192,v194
v_permlane16_swap_b32 v193,v195
buffer_store_dwordx4 v[192:195],v56,s[24:27],s87 offen sc1 nt
v_permlane16_swap_b32 v200,v202
v_permlane16_swap_b32 v201,v203
v_cvt_pk_bf16_f32 v216,v216,v217
v_cvt_pk_bf16_f32 v217,v218,v219
v_cvt_pk_bf16_f32 v218,v248,v249
v_cvt_pk_bf16_f32 v219,v250,v251
buffer_store_dwordx4 v[200:203],v56,s[24:27],s88 offen sc1 nt
v_permlane16_swap_b32 v208,v210
v_permlane16_swap_b32 v209,v211
buffer_store_dwordx4 v[208:211],v56,s[24:27],s89 offen sc1 nt
v_permlane16_swap_b32 v216,v218
v_permlane16_swap_b32 v217,v219
buffer_store_dwordx4 v[216:219],v56,s[24:27],s90 offen sc1 nt
s_lshl_b32 s69,s62,20
s_add_u32 s69,s69,s18
buffer_store_dword v20,v22,s[44:47],s69 offen sc1
L_geF_%=:
s_branch L_end_%=
.p2align 12
L_pd20_%=:
s_cbranch_execz L_trt1_%=
L_GS_%=:
L_gbS_%=:
s_waitcnt lgkmcnt(14)
v_mfma_f32_16x16x32_bf16 a[192:195],v[64:67],a[68:71],0
s_lshl_b32 s91,s63,8
buffer_store_dwordx4 v[32:35],v6,s[40:43],s79 offen sc1
s_waitcnt lgkmcnt(12)
v_mfma_f32_16x16x32_bf16 a[192:195],v[68:71],a[76:79],a[192:195]
s_add_u32 s87,s64,1
v_bfe_u32 v3,v6,4,2
v_min_u32 v3,2,v3
s_waitcnt lgkmcnt(10)
v_mfma_f32_16x16x32_bf16 a[192:195],v[72:75],a[84:87],a[192:195]
s_and_b32 s87,s87,3
v_add3_u32 v3,v3,s64,1
v_and_b32 v3,3,v3
s_waitcnt lgkmcnt(8)
v_mfma_f32_16x16x32_bf16 a[192:195],v[76:79],a[92:95],a[192:195]
s_lshl_b32 s87,s87,1
ds_read_b64_tr_b16 v[96:97],v1 offset:64
v_lshlrev_b32 v3,6,v3
v_mfma_f32_16x16x32_bf16 a[196:199],v[76:79],a[124:127],0
s_lshl_b32 s87,s87,16
ds_read_b64_tr_b16 v[98:99],v1 offset:5184
ds_read_b64_tr_b16 v[100:101],v1 offset:10304
v_mfma_f32_16x16x32_bf16 a[196:199],v[72:75],a[116:119],a[196:199]
s_add_u32 s88,s87,4096
ds_read_b64_tr_b16 v[102:103],v1 offset:15424
v_lshlrev_b32 v29,10,v6
v_mfma_f32_16x16x32_bf16 a[196:199],v[68:71],a[108:111],a[196:199]
s_add_u32 s89,s87,8192
ds_read_b64_tr_b16 v[104:105],v1 offset:20544
v_accvgpr_read_b32 v16,a24
v_mfma_f32_16x16x32_bf16 a[196:199],v[64:67],a[100:103],a[196:199]
s_add_u32 s90,s87,12288
ds_read_b64_tr_b16 v[106:107],v1 offset:25664
v_accvgpr_read_b32 v17,a25
v_mfma_f32_16x16x32_bf16 a[200:203],v[64:67],a[64:67],0
s_add_u32 m0,s86,122880
ds_read_b64_tr_b16 v[108:109],v1 offset:30784
ds_read_b64_tr_b16 v[110:111],v1 offset:35904
v_mfma_f32_16x16x32_bf16 a[200:203],v[68:71],a[72:75],a[200:203]
s_add_u32 s83,s64,3
ds_read_b64_tr_b16 v[112:113],v2 offset:96
v_accvgpr_read_b32 v18,a26
v_mfma_f32_16x16x32_bf16 a[200:203],v[72:75],a[80:83],a[200:203]
s_and_b32 s83,s83,3
ds_read_b64_tr_b16 v[114:115],v2 offset:5216
v_accvgpr_read_b32 v19,a27
v_mfma_f32_16x16x32_bf16 a[200:203],v[76:79],a[88:91],a[200:203]
s_lshl_b32 s83,s83,1
ds_read_b64_tr_b16 v[116:117],v2 offset:10336
v_accvgpr_read_b32 v20,a28
v_mfma_f32_16x16x32_bf16 a[204:207],v[76:79],a[120:123],0
s_mul_i32 s83,s83,163840
ds_read_b64_tr_b16 v[118:119],v2 offset:15456
ds_read_b64_tr_b16 v[120:121],v2 offset:20576
v_mfma_f32_16x16x32_bf16 a[204:207],v[72:75],a[112:115],a[204:207]
s_add_u32 s83,s83,s85
ds_read_b64_tr_b16 v[122:123],v2 offset:25696
v_accvgpr_read_b32 v21,a29
v_mfma_f32_16x16x32_bf16 a[204:207],v[68:71],a[104:107],a[204:207]
s_add_u32 s84,s83,20480
ds_read_b64_tr_b16 v[124:125],v2 offset:30816
v_accvgpr_read_b32 v22,a30
v_mfma_f32_16x16x32_bf16 a[204:207],v[64:67],a[96:99],a[204:207]
s_lshl_b32 s66,s61,2
buffer_store_dwordx4 v[36:39],v6,s[40:43],s80 offen sc1
s_waitcnt lgkmcnt(9)
v_mfma_f32_16x16x32_bf16 a[208:211],v[80:83],a[68:71],0
s_add_u32 s66,s66,s64
ds_read_b64_tr_b16 v[126:127],v2 offset:35936
ds_read_b64_tr_b16 v[64:65],v1 offset:128
v_mfma_f32_16x16x32_bf16 a[208:211],v[84:87],a[76:79],a[208:211]
s_mul_i32 s66,s66,262144
ds_read_b64_tr_b16 v[66:67],v1 offset:5248
v_accvgpr_read_b32 v23,a31
v_mfma_f32_16x16x32_bf16 a[208:211],v[88:91],a[84:87],a[208:211]
s_add_u32 s66,s66,134217728
ds_read_b64_tr_b16 v[68:69],v1 offset:10368
v_mul_f32 v16,v16,v49
v_mfma_f32_16x16x32_bf16 a[208:211],v[92:95],a[92:95],a[208:211]
s_mov_b32 s38,262144
ds_read_b64_tr_b16 v[70:71],v1 offset:15488
v_mul_f32 v17,v17,v49
v_mfma_f32_16x16x32_bf16 a[212:215],v[92:95],a[124:127],0
s_mov_b32 s39,0x20000
s_waitcnt vmcnt(21)
buffer_load_dwordx2 v[4:5],v3,s[44:47],s91 offen offset:256 sc1
ds_read_b64_tr_b16 v[72:73],v1 offset:20608
v_mfma_f32_16x16x32_bf16 a[212:215],v[88:91],a[116:119],a[212:215]
s_add_u32 s74,s63,1
ds_read_b64_tr_b16 v[74:75],v1 offset:25728
ds_read_b64_tr_b16 v[76:77],v1 offset:30848
v_mfma_f32_16x16x32_bf16 a[212:215],v[84:87],a[108:111],a[212:215]
s_and_b32 s74,s74,3
ds_read_b64_tr_b16 v[78:79],v1 offset:35968
v_mul_f32 v18,v18,v49
v_mfma_f32_16x16x32_bf16 a[212:215],v[80:83],a[100:103],a[212:215]
)A"
R"A(s_lshl_b32 s74,s74,2
v_mul_f32 v19,v19,v49
v_mul_f32 v20,v20,v49
v_mfma_f32_16x16x32_bf16 a[216:219],v[80:83],a[64:67],0
s_add_u32 s74,s74,s60
v_mul_f32 v21,v21,v49
v_mul_f32 v22,v22,v49
v_mfma_f32_16x16x32_bf16 a[216:219],v[84:87],a[72:75],a[216:219]
s_mul_i32 s74,s74,16384
v_mul_f32 v23,v23,v49
v_cvt_pk_bf16_f32 v44,v16,v17
v_mfma_f32_16x16x32_bf16 a[216:219],v[88:91],a[80:83],a[216:219]
s_add_u32 s74,s74,8192
v_cvt_pk_bf16_f32 v45,v18,v19
v_cvt_pk_bf16_f32 v46,v20,v21
v_mfma_f32_16x16x32_bf16 a[216:219],v[92:95],a[88:91],a[216:219]
s_add_u32 s75,s63,2
v_cvt_pk_bf16_f32 v47,v22,v23
v_mul_f32 v132,v132,v53
v_mfma_f32_16x16x32_bf16 a[220:223],v[92:95],a[120:123],0
s_and_b32 s75,s75,3
ds_read_b64_tr_b16 v[92:93],v2 offset:30880
ds_read_b64_tr_b16 v[94:95],v2 offset:36000
v_mfma_f32_16x16x32_bf16 a[220:223],v[88:91],a[112:115],a[220:223]
s_lshl_b32 s75,s75,2
ds_read_b64_tr_b16 v[88:89],v2 offset:20640
v_mul_f32 v133,v133,v53
v_mfma_f32_16x16x32_bf16 a[220:223],v[84:87],a[104:107],a[220:223]
s_add_u32 s75,s75,s60
buffer_store_dwordx4 v[40:43],v6,s[40:43],s81 offen sc1
v_mfma_f32_16x16x32_bf16 a[220:223],v[80:83],a[96:99],a[220:223]
s_mul_i32 s75,s75,16384
ds_read_b64_tr_b16 v[80:81],v2 offset:160
ds_read_b64_tr_b16 v[82:83],v2 offset:5280
v_mfma_f32_16x16x32_bf16 a[224:227],v[96:99],a[68:71],0
s_add_u32 s75,s75,4096
ds_read_b64_tr_b16 v[84:85],v2 offset:10400
v_mul_f32 v134,v134,v53
v_mfma_f32_16x16x32_bf16 a[224:227],v[100:103],a[76:79],a[224:227]
s_add_u32 s76,s63,3
ds_read_b64_tr_b16 v[86:87],v2 offset:15520
v_mul_f32 v135,v135,v53
v_mfma_f32_16x16x32_bf16 a[224:227],v[104:107],a[84:87],a[224:227]
s_and_b32 s76,s76,3
ds_read_b64_tr_b16 v[90:91],v2 offset:25760
v_mul_f32 v140,v140,v53
s_waitcnt lgkmcnt(8)
v_mfma_f32_16x16x32_bf16 a[224:227],v[108:111],a[92:95],a[224:227]
s_lshl_b32 s76,s76,2
v_mul_f32 v141,v141,v53
v_mul_f32 v142,v142,v53
v_mfma_f32_16x16x32_bf16 a[228:231],v[108:111],a[124:127],0
s_add_u32 s76,s76,s60
v_mul_f32 v143,v143,v53
v_cvt_pk_bf16_f32 v132,v132,v133
v_mfma_f32_16x16x32_bf16 a[228:231],v[104:107],a[116:119],a[228:231]
s_mul_i32 s76,s76,16384
v_cvt_pk_bf16_f32 v133,v134,v135
v_cvt_pk_bf16_f32 v134,v140,v141
v_mfma_f32_16x16x32_bf16 a[228:231],v[100:103],a[108:111],a[228:231]
s_add_u32 s76,s76,0
v_cvt_pk_bf16_f32 v135,v142,v143
v_mul_f32 v148,v148,v53
v_mfma_f32_16x16x32_bf16 a[228:231],v[96:99],a[100:103],a[228:231]
s_lshl_b32 s67,s63,10
v_mul_f32 v149,v149,v53
v_mul_f32 v150,v150,v53
v_mfma_f32_16x16x32_bf16 a[232:235],v[96:99],a[64:67],0
s_mov_b32 s26,307200
v_mul_f32 v151,v151,v53
v_mul_f32 v156,v156,v53
v_mfma_f32_16x16x32_bf16 a[232:235],v[100:103],a[72:75],a[232:235]
s_mov_b32 s27,0x20000
v_mul_f32 v157,v157,v53
v_mul_f32 v158,v158,v53
v_mfma_f32_16x16x32_bf16 a[232:235],v[104:107],a[80:83],a[232:235]
s_lshl_b32 s77,s63,2
v_mul_f32 v159,v159,v53
v_cvt_pk_bf16_f32 v148,v148,v149
v_mfma_f32_16x16x32_bf16 a[232:235],v[108:111],a[88:91],a[232:235]
s_add_u32 s77,s77,s60
v_cvt_pk_bf16_f32 v149,v150,v151
v_cvt_pk_bf16_f32 v150,v156,v157
v_mfma_f32_16x16x32_bf16 a[236:239],v[108:111],a[120:123],0
s_mul_i32 s77,s77,16384
ds_read_b64_tr_b16 v[108:109],v1 offset:30912
ds_read_b64_tr_b16 v[110:111],v1 offset:36032
v_mfma_f32_16x16x32_bf16 a[236:239],v[104:107],a[112:115],a[236:239]
s_add_u32 s65,s77,0
buffer_store_dwordx4 v[44:47],v6,s[40:43],s82 offen sc1
v_mfma_f32_16x16x32_bf16 a[236:239],v[100:103],a[104:107],a[236:239]
s_add_u32 s68,s77,12288
ds_read_b64_tr_b16 v[100:101],v1 offset:10432
ds_read_b64_tr_b16 v[102:103],v1 offset:15552
v_mfma_f32_16x16x32_bf16 a[236:239],v[96:99],a[96:99],a[236:239]
ds_read_b64_tr_b16 v[96:97],v1 offset:192
v_cvt_pk_bf16_f32 v151,v158,v159
v_mfma_f32_16x16x32_bf16 a[240:243],v[112:115],a[68:71],0
ds_read_b64_tr_b16 v[98:99],v1 offset:5312
v_mul_f32 v164,v164,v53
v_mfma_f32_16x16x32_bf16 a[240:243],v[116:119],a[76:79],a[240:243]
ds_read_b64_tr_b16 v[104:105],v1 offset:20672
v_mul_f32 v165,v165,v53
v_mfma_f32_16x16x32_bf16 a[240:243],v[120:123],a[84:87],a[240:243]
ds_read_b64_tr_b16 v[106:107],v1 offset:25792
v_mul_f32 v166,v166,v53
v_mfma_f32_16x16x32_bf16 a[240:243],v[124:127],a[92:95],a[240:243]
v_mul_f32 v167,v167,v53
v_mul_f32 v172,v172,v53
v_mfma_f32_16x16x32_bf16 a[244:247],v[124:127],a[124:127],0
v_mul_f32 v173,v173,v53
v_mul_f32 v174,v174,v53
v_mfma_f32_16x16x32_bf16 a[244:247],v[120:123],a[116:119],a[244:247]
v_mul_f32 v175,v175,v53
v_cvt_pk_bf16_f32 v164,v164,v165
v_mfma_f32_16x16x32_bf16 a[244:247],v[116:119],a[108:111],a[244:247]
v_cvt_pk_bf16_f32 v165,v166,v167
v_cvt_pk_bf16_f32 v166,v172,v173
v_mfma_f32_16x16x32_bf16 a[244:247],v[112:115],a[100:103],a[244:247]
v_cvt_pk_bf16_f32 v167,v174,v175
v_mul_f32 v180,v180,v53
v_mfma_f32_16x16x32_bf16 a[248:251],v[112:115],a[64:67],0
v_mul_f32 v181,v181,v53
v_mul_f32 v182,v182,v53
v_mfma_f32_16x16x32_bf16 a[248:251],v[116:119],a[72:75],a[248:251]
v_mul_f32 v183,v183,v53
v_mul_f32 v188,v188,v53
v_mfma_f32_16x16x32_bf16 a[248:251],v[120:123],a[80:83],a[248:251]
v_mul_f32 v189,v189,v53
v_mul_f32 v190,v190,v53
v_mfma_f32_16x16x32_bf16 a[248:251],v[124:127],a[88:91],a[248:251]
v_mul_f32 v191,v191,v53
v_cvt_pk_bf16_f32 v180,v180,v181
v_mfma_f32_16x16x32_bf16 a[252:255],v[124:127],a[120:123],0
buffer_load_dword v30,v29,s[8:11],s7 offen
ds_read_b64_tr_b16 v[124:125],v2 offset:30944
v_mfma_f32_16x16x32_bf16 a[252:255],v[120:123],a[112:115],a[252:255]
ds_read_b64_tr_b16 v[120:121],v2 offset:20704
ds_read_b64_tr_b16 v[122:123],v2 offset:25824
v_mfma_f32_16x16x32_bf16 a[252:255],v[116:119],a[104:107],a[252:255]
buffer_store_dwordx4 v[132:135],v6,s[40:43],s79 offen offset:1024 sc1
v_mfma_f32_16x16x32_bf16 a[252:255],v[112:115],a[96:99],a[252:255]
ds_read_b64_tr_b16 v[112:113],v2 offset:224
ds_read_b64_tr_b16 v[114:115],v2 offset:5344
v_mfma_f32_16x16x32_bf16 a[0:3],v[64:67],a[68:71],0
ds_read_b64_tr_b16 v[116:117],v2 offset:10464
v_cvt_pk_bf16_f32 v181,v182,v183
v_mfma_f32_16x16x32_bf16 a[0:3],v[68:71],a[76:79],a[0:3]
ds_read_b64_tr_b16 v[118:119],v2 offset:15584
v_cvt_pk_bf16_f32 v182,v188,v189
v_mfma_f32_16x16x32_bf16 a[0:3],v[72:75],a[84:87],a[0:3]
ds_read_b64_tr_b16 v[126:127],v2 offset:36064
v_cvt_pk_bf16_f32 v183,v190,v191
v_mfma_f32_16x16x32_bf16 a[0:3],v[76:79],a[92:95],a[0:3]
v_mul_f32 v196,v196,v57
v_mul_f32 v197,v197,v57
v_mfma_f32_16x16x32_bf16 a[4:7],v[76:79],a[124:127],0
v_mul_f32 v198,v198,v57
v_mul_f32 v199,v199,v57
v_mfma_f32_16x16x32_bf16 a[4:7],v[72:75],a[116:119],a[4:7]
v_mul_f32 v204,v204,v57
v_mul_f32 v205,v205,v57
v_mfma_f32_16x16x32_bf16 a[4:7],v[68:71],a[108:111],a[4:7]
v_mul_f32 v206,v206,v57
v_mul_f32 v207,v207,v57
v_mfma_f32_16x16x32_bf16 a[4:7],v[64:67],a[100:103],a[4:7]
v_cvt_pk_bf16_f32 v196,v196,v197
v_cvt_pk_bf16_f32 v197,v198,v199
v_mfma_f32_16x16x32_bf16 a[8:11],v[64:67],a[64:67],0
v_cvt_pk_bf16_f32 v198,v204,v205
v_cvt_pk_bf16_f32 v199,v206,v207
v_mfma_f32_16x16x32_bf16 a[8:11],v[68:71],a[72:75],a[8:11]
v_mul_f32 v212,v212,v57
v_mul_f32 v213,v213,v57
v_mfma_f32_16x16x32_bf16 a[8:11],v[72:75],a[80:83],a[8:11]
v_mul_f32 v214,v214,v57
v_mul_f32 v215,v215,v57
v_mfma_f32_16x16x32_bf16 a[8:11],v[76:79],a[88:91],a[8:11]
v_mul_f32 v220,v220,v57
v_mul_f32 v221,v221,v57
v_mfma_f32_16x16x32_bf16 a[12:15],v[76:79],a[120:123],0
ds_read_b64_tr_b16 v[76:77],v1 offset:30976
ds_read_b64_tr_b16 v[78:79],v1 offset:36096
v_mfma_f32_16x16x32_bf16 a[12:15],v[72:75],a[112:115],a[12:15]
buffer_store_dwordx4 v[148:151],v6,s[40:43],s80 offen offset:1024 sc1
v_mfma_f32_16x16x32_bf16 a[12:15],v[68:71],a[104:107],a[12:15]
ds_read_b64_tr_b16 v[68:69],v1 offset:10496
ds_read_b64_tr_b16 v[70:71],v1 offset:15616
v_mfma_f32_16x16x32_bf16 a[12:15],v[64:67],a[96:99],a[12:15]
s_waitcnt lgkmcnt(4)
ds_read_b64_tr_b16 v[64:65],v1 offset:256
v_mul_f32 v222,v222,v57
v_mfma_f32_16x16x32_bf16 a[16:19],v[80:83],a[68:71],0
ds_read_b64_tr_b16 v[66:67],v1 offset:5376
v_mul_f32 v223,v223,v57
v_mfma_f32_16x16x32_bf16 a[16:19],v[84:87],a[76:79],a[16:19]
ds_read_b64_tr_b16 v[72:73],v1 offset:20736
v_cvt_pk_bf16_f32 v212,v212,v213
v_mfma_f32_16x16x32_bf16 a[16:19],v[88:91],a[84:87],a[16:19]
ds_read_b64_tr_b16 v[74:75],v1 offset:25856
v_add_u32 v1,0xffff6000,v1
v_mfma_f32_16x16x32_bf16 a[16:19],v[92:95],a[92:95],a[16:19]
v_cvt_pk_bf16_f32 v213,v214,v215
v_cvt_pk_bf16_f32 v214,v220,v221
v_mfma_f32_16x16x32_bf16 a[20:23],v[92:95],a[124:127],0
v_cvt_pk_bf16_f32 v215,v222,v223
v_mul_f32 v228,v228,v57
v_mfma_f32_16x16x32_bf16 a[20:23],v[88:91],a[116:119],a[20:23]
v_mul_f32 v229,v229,v57
v_mul_f32 v230,v230,v57
v_mfma_f32_16x16x32_bf16 a[20:23],v[84:87],a[108:111],a[20:23]
v_mul_f32 v231,v231,v57
v_mul_f32 v236,v236,v57
v_mfma_f32_16x16x32_bf16 a[20:23],v[80:83],a[100:103],a[20:23]
v_mul_f32 v237,v237,v57
v_mul_f32 v238,v238,v57
v_mfma_f32_16x16x32_bf16 a[24:27],v[80:83],a[64:67],0
v_mul_f32 v239,v239,v57
v_cvt_pk_bf16_f32 v228,v228,v229
v_mfma_f32_16x16x32_bf16 a[24:27],v[84:87],a[72:75],a[24:27]
v_cvt_pk_bf16_f32 v229,v230,v231
v_cvt_pk_bf16_f32 v230,v236,v237
v_mfma_f32_16x16x32_bf16 a[24:27],v[88:91],a[80:83],a[24:27]
s_waitcnt vmcnt(5)
v_cmp_ne_u32 s[56:57],s52,v4
v_cmp_ne_u32 s[58:59],s53,v5
v_mfma_f32_16x16x32_bf16 a[24:27],v[92:95],a[88:91],a[24:27]
s_or_b64 s[56:57],s[56:57],s[58:59]
s_cbranch_scc1 L_fr6_%=
L_fo6_%=:
v_cvt_pk_bf16_f32 v231,v238,v239
v_mfma_f32_16x16x32_bf16 a[28:31],v[92:95],a[120:123],0
s_lshl_b32 s91,s63,8
buffer_store_dwordx4 v[164:167],v6,s[40:43],s81 offen offset:1024 sc1
v_mfma_f32_16x16x32_bf16 a[28:31],v[88:91],a[112:115],a[28:31]
ds_read_b64_tr_b16 v[88:89],v2 offset:20768
ds_read_b64_tr_b16 v[90:91],v2 offset:25888
v_mfma_f32_16x16x32_bf16 a[28:31],v[84:87],a[104:107],a[28:31]
buffer_load_dwordx4 a[128:131],v6,s[28:31],s87 offen sc1
ds_read_b64_tr_b16 v[84:85],v2 offset:10528
v_mfma_f32_16x16x32_bf16 a[28:31],v[80:83],a[96:99],a[28:31]
ds_read_b64_tr_b16 v[80:81],v2 offset:288
v_mul_f32 v244,v244,v57
v_mfma_f32_16x16x32_bf16 v[128:131],v[96:99],a[68:71],0
ds_read_b64_tr_b16 v[82:83],v2 offset:5408
v_mul_f32 v245,v245,v57
v_mfma_f32_16x16x32_bf16 v[128:131],v[100:103],a[76:79],v[128:131]
ds_read_b64_tr_b16 v[86:87],v2 offset:15648
ds_read_b64_tr_b16 v[92:93],v2 offset:31008
v_mfma_f32_16x16x32_bf16 v[128:131],v[104:107],a[84:87],v[128:131]
ds_read_b64_tr_b16 v[94:95],v2 offset:36128
v_add_u32 v2,0xffff6000,v2
v_mfma_f32_16x16x32_bf16 v[128:131],v[108:111],a[92:95],v[128:131]
v_mul_f32 v246,v246,v57
v_mul_f32 v247,v247,v57
v_mfma_f32_16x16x32_bf16 v[136:139],v[108:111],a[124:127],0
v_mul_f32 v252,v252,v57
v_mul_f32 v253,v253,v57
v_mfma_f32_16x16x32_bf16 v[136:139],v[104:107],a[116:119],v[136:139]
v_mul_f32 v254,v254,v57
v_mul_f32 v255,v255,v57
v_mfma_f32_16x16x32_bf16 v[136:139],v[100:103],a[108:111],v[136:139]
v_cvt_pk_bf16_f32 v244,v244,v245
v_cvt_pk_bf16_f32 v245,v246,v247
v_mfma_f32_16x16x32_bf16 v[136:139],v[96:99],a[100:103],v[136:139]
v_cvt_pk_bf16_f32 v246,v252,v253
v_cvt_pk_bf16_f32 v247,v254,v255
v_mfma_f32_16x16x32_bf16 v[144:147],v[96:99],a[64:67],0
v_accvgpr_read_b32 v16,a40
v_accvgpr_read_b32 v17,a41
v_mfma_f32_16x16x32_bf16 v[144:147],v[100:103],a[72:75],v[144:147]
buffer_load_dwordx4 a[144:147],v6,s[28:31],s88 offen sc1
v_accvgpr_read_b32 v18,a42
v_mfma_f32_16x16x32_bf16 v[144:147],v[104:107],a[80:83],v[144:147]
v_accvgpr_read_b32 v19,a43
v_accvgpr_read_b32 v20,a44
v_mfma_f32_16x16x32_bf16 v[144:147],v[108:111],a[88:91],v[144:147]
buffer_store_dwordx4 v[180:183],v6,s[40:43],s82 offen offset:1024 sc1
v_mfma_f32_16x16x32_bf16 v[152:155],v[108:111],a[120:123],0
ds_read_b64_tr_b16 v[108:109],v1 offset:30720
ds_read_b64_tr_b16 v[110:111],v1 offset:35840
v_mfma_f32_16x16x32_bf16 v[152:155],v[104:107],a[112:115],v[152:155]
ds_read_b64_tr_b16 v[104:105],v1 offset:20480
v_accvgpr_read_b32 v21,a45
v_mfma_f32_16x16x32_bf16 v[152:155],v[100:103],a[104:107],v[152:155]
ds_read_b64_tr_b16 v[100:101],v1 offset:10240
v_accvgpr_read_b32 v22,a46
v_mfma_f32_16x16x32_bf16 v[152:155],v[96:99],a[96:99],v[152:155]
ds_read_b64_tr_b16 v[96:97],v1
v_accvgpr_read_b32 v23,a47
v_mfma_f32_16x16x32_bf16 v[160:163],v[112:115],a[68:71],0
ds_read_b64_tr_b16 v[98:99],v1 offset:5120
ds_read_b64_tr_b16 v[102:103],v1 offset:15360
)A"
R"A(v_mfma_f32_16x16x32_bf16 v[160:163],v[116:119],a[76:79],v[160:163]
ds_read_b64_tr_b16 v[106:107],v1 offset:25600
v_mul_f32 v16,v16,v61
v_mfma_f32_16x16x32_bf16 v[160:163],v[120:123],a[84:87],v[160:163]
v_mul_f32 v17,v17,v61
v_mul_f32 v18,v18,v61
v_mfma_f32_16x16x32_bf16 v[160:163],v[124:127],a[92:95],v[160:163]
v_mul_f32 v19,v19,v61
v_mul_f32 v20,v20,v61
v_mfma_f32_16x16x32_bf16 v[168:171],v[124:127],a[124:127],0
v_mul_f32 v21,v21,v61
v_mul_f32 v22,v22,v61
v_mfma_f32_16x16x32_bf16 v[168:171],v[120:123],a[116:119],v[168:171]
v_mul_f32 v23,v23,v61
v_cvt_pk_bf16_f32 v156,v16,v17
v_mfma_f32_16x16x32_bf16 v[168:171],v[116:119],a[108:111],v[168:171]
buffer_load_dwordx4 a[160:163],v6,s[28:31],s89 offen sc1
v_cvt_pk_bf16_f32 v157,v18,v19
v_mfma_f32_16x16x32_bf16 v[168:171],v[112:115],a[100:103],v[168:171]
v_cvt_pk_bf16_f32 v158,v20,v21
v_cvt_pk_bf16_f32 v159,v22,v23
v_mfma_f32_16x16x32_bf16 v[176:179],v[112:115],a[64:67],0
v_accvgpr_read_b32 v16,a56
v_accvgpr_read_b32 v17,a57
v_mfma_f32_16x16x32_bf16 v[176:179],v[116:119],a[72:75],v[176:179]
v_accvgpr_read_b32 v18,a58
v_accvgpr_read_b32 v19,a59
v_mfma_f32_16x16x32_bf16 v[176:179],v[120:123],a[80:83],v[176:179]
buffer_store_dwordx4 v[196:199],v6,s[40:43],s79 offen offset:2048 sc1
v_mfma_f32_16x16x32_bf16 v[176:179],v[124:127],a[88:91],v[176:179]
v_accvgpr_read_b32 v20,a60
v_accvgpr_read_b32 v21,a61
v_mfma_f32_16x16x32_bf16 v[184:187],v[124:127],a[120:123],0
v_accvgpr_read_b32 v22,a62
v_accvgpr_read_b32 v23,a63
v_mfma_f32_16x16x32_bf16 v[184:187],v[120:123],a[112:115],v[184:187]
ds_read_b64_tr_b16 v[120:121],v2 offset:20512
ds_read_b64_tr_b16 v[122:123],v2 offset:25632
v_mfma_f32_16x16x32_bf16 v[184:187],v[116:119],a[104:107],v[184:187]
ds_read_b64_tr_b16 v[116:117],v2 offset:10272
v_mul_f32 v16,v16,v61
v_mfma_f32_16x16x32_bf16 v[184:187],v[112:115],a[96:99],v[184:187]
s_waitcnt lgkmcnt(3)
ds_read_b64_tr_b16 v[112:113],v2 offset:32
v_mul_f32 v17,v17,v61
v_mfma_f32_16x16x32_bf16 v[192:195],v[64:67],a[68:71],0
ds_read_b64_tr_b16 v[114:115],v2 offset:5152
v_mul_f32 v18,v18,v61
v_mfma_f32_16x16x32_bf16 v[192:195],v[68:71],a[76:79],v[192:195]
ds_read_b64_tr_b16 v[118:119],v2 offset:15392
v_mul_f32 v19,v19,v61
v_mfma_f32_16x16x32_bf16 v[192:195],v[72:75],a[84:87],v[192:195]
v_mul_f32 v20,v20,v61
v_mul_f32 v21,v21,v61
v_mfma_f32_16x16x32_bf16 v[192:195],v[76:79],a[92:95],v[192:195]
buffer_load_dwordx4 a[176:179],v6,s[28:31],s90 offen sc1
v_mul_f32 v22,v22,v61
v_mfma_f32_16x16x32_bf16 v[200:203],v[76:79],a[124:127],0
v_mul_f32 v23,v23,v61
v_cvt_pk_bf16_f32 v188,v16,v17
v_mfma_f32_16x16x32_bf16 v[200:203],v[72:75],a[116:119],v[200:203]
v_cvt_pk_bf16_f32 v189,v18,v19
v_cvt_pk_bf16_f32 v190,v20,v21
v_mfma_f32_16x16x32_bf16 v[200:203],v[68:71],a[108:111],v[200:203]
ds_read_b64_tr_b16 v[124:125],v2 offset:30752
v_cvt_pk_bf16_f32 v191,v22,v23
v_mfma_f32_16x16x32_bf16 v[200:203],v[64:67],a[100:103],v[200:203]
ds_read_b64_tr_b16 v[126:127],v2 offset:35872
v_accvgpr_read_b32 v8,a32
v_mfma_f32_16x16x32_bf16 v[208:211],v[64:67],a[64:67],0
v_accvgpr_read_b32 v9,a33
v_accvgpr_read_b32 v10,a34
v_mfma_f32_16x16x32_bf16 v[208:211],v[68:71],a[72:75],v[208:211]
buffer_store_dwordx4 v[212:215],v6,s[40:43],s80 offen offset:2048 sc1
v_mfma_f32_16x16x32_bf16 v[208:211],v[72:75],a[80:83],v[208:211]
v_accvgpr_read_b32 v11,a35
v_accvgpr_read_b32 v12,a36
v_mfma_f32_16x16x32_bf16 v[208:211],v[76:79],a[88:91],v[208:211]
v_accvgpr_read_b32 v13,a37
v_accvgpr_read_b32 v14,a38
v_mfma_f32_16x16x32_bf16 v[216:219],v[76:79],a[120:123],0
v_accvgpr_read_b32 v15,a39
v_mul_f32 v8,v8,v61
v_mfma_f32_16x16x32_bf16 v[216:219],v[72:75],a[112:115],v[216:219]
v_mul_f32 v9,v9,v61
v_mul_f32 v10,v10,v61
v_mfma_f32_16x16x32_bf16 v[216:219],v[68:71],a[104:107],v[216:219]
v_mul_f32 v11,v11,v61
v_mul_f32 v12,v12,v61
v_mfma_f32_16x16x32_bf16 v[216:219],v[64:67],a[96:99],v[216:219]
v_mul_f32 v13,v13,v61
v_mul_f32 v14,v14,v61
v_mfma_f32_16x16x32_bf16 v[224:227],v[80:83],a[68:71],0
buffer_load_dwordx4 a[180:183],v6,s[28:31],s90 offen offset:1024 sc1
v_mul_f32 v15,v15,v61
v_mfma_f32_16x16x32_bf16 v[224:227],v[84:87],a[76:79],v[224:227]
v_cvt_pk_bf16_f32 v140,v8,v9
v_cvt_pk_bf16_f32 v141,v10,v11
v_mfma_f32_16x16x32_bf16 v[224:227],v[88:91],a[84:87],v[224:227]
v_cvt_pk_bf16_f32 v142,v12,v13
v_cvt_pk_bf16_f32 v143,v14,v15
v_mfma_f32_16x16x32_bf16 v[224:227],v[92:95],a[92:95],v[224:227]
v_accvgpr_read_b32 v8,a48
v_accvgpr_read_b32 v9,a49
v_mfma_f32_16x16x32_bf16 v[232:235],v[92:95],a[124:127],0
v_accvgpr_read_b32 v10,a50
v_accvgpr_read_b32 v11,a51
v_mfma_f32_16x16x32_bf16 v[232:235],v[88:91],a[116:119],v[232:235]
v_accvgpr_read_b32 v12,a52
v_accvgpr_read_b32 v13,a53
v_mfma_f32_16x16x32_bf16 v[232:235],v[84:87],a[108:111],v[232:235]
v_accvgpr_read_b32 v14,a54
v_accvgpr_read_b32 v15,a55
v_mfma_f32_16x16x32_bf16 v[232:235],v[80:83],a[100:103],v[232:235]
v_mul_f32 v8,v8,v61
v_mul_f32 v9,v9,v61
v_mfma_f32_16x16x32_bf16 v[240:243],v[80:83],a[64:67],0
buffer_store_dwordx4 v[228:231],v6,s[40:43],s81 offen offset:2048 sc1
v_mfma_f32_16x16x32_bf16 v[240:243],v[84:87],a[72:75],v[240:243]
v_mul_f32 v10,v10,v61
v_mul_f32 v11,v11,v61
v_mfma_f32_16x16x32_bf16 v[240:243],v[88:91],a[80:83],v[240:243]
v_mul_f32 v12,v12,v61
v_mul_f32 v13,v13,v61
v_mfma_f32_16x16x32_bf16 v[240:243],v[92:95],a[88:91],v[240:243]
v_mul_f32 v14,v14,v61
v_mul_f32 v15,v15,v61
v_mfma_f32_16x16x32_bf16 v[248:251],v[92:95],a[120:123],0
v_cvt_pk_bf16_f32 v172,v8,v9
v_cvt_pk_bf16_f32 v173,v10,v11
v_mfma_f32_16x16x32_bf16 v[248:251],v[88:91],a[112:115],v[248:251]
buffer_load_dwordx4 a[164:167],v6,s[28:31],s89 offen offset:1024 sc1
v_cvt_pk_bf16_f32 v174,v12,v13
v_mfma_f32_16x16x32_bf16 v[248:251],v[84:87],a[104:107],v[248:251]
ds_read_b64_tr_b16 v[64:65],v1 offset:64
v_cvt_pk_bf16_f32 v175,v14,v15
v_mfma_f32_16x16x32_bf16 v[248:251],v[80:83],a[96:99],v[248:251]
ds_read_b64_tr_b16 v[66:67],v1 offset:5184
ds_read_b64_tr_b16 v[68:69],v1 offset:10304
v_mfma_f32_16x16x32_bf16 a[192:195],v[96:99],v[32:35],a[192:195]
s_cmp_eq_u32 s60,3
s_cselect_b32 s69,0x40000000,0
ds_read_b64_tr_b16 v[70:71],v1 offset:15424
v_mfma_f32_16x16x32_bf16 a[192:195],v[100:103],v[36:39],a[192:195]
ds_read_b64_tr_b16 v[72:73],v1 offset:20544
v_add_u32 v28,s69,v6
v_mfma_f32_16x16x32_bf16 a[192:195],v[104:107],v[40:43],a[192:195]
s_and_b32 s69,s63,s60
ds_read_b64_tr_b16 v[74:75],v1 offset:25664
v_mov_b32 v8,s52
v_mfma_f32_16x16x32_bf16 a[192:195],v[108:111],v[44:47],a[192:195]
ds_read_b64_tr_b16 v[76:77],v1 offset:30784
ds_read_b64_tr_b16 v[78:79],v1 offset:35904
v_mfma_f32_16x16x32_bf16 a[196:199],v[108:111],v[180:183],a[196:199]
ds_read_b64_tr_b16 v[80:81],v2 offset:96
v_mov_b32 v9,s53
v_mfma_f32_16x16x32_bf16 a[196:199],v[104:107],v[164:167],a[196:199]
ds_read_b64_tr_b16 v[82:83],v2 offset:5216
v_lshlrev_b32 v10,20,v6
v_mfma_f32_16x16x32_bf16 a[196:199],v[100:103],v[148:151],a[196:199]
ds_read_b64_tr_b16 v[84:85],v2 offset:10336
v_bfe_u32 v3,v6,4,2
v_mfma_f32_16x16x32_bf16 a[196:199],v[96:99],v[132:135],a[196:199]
buffer_store_dwordx4 v[244:247],v6,s[40:43],s82 offen offset:2048 sc1
v_mfma_f32_16x16x32_bf16 a[200:203],v[96:99],v[196:199],a[200:203]
ds_read_b64_tr_b16 v[86:87],v2 offset:15456
ds_read_b64_tr_b16 v[88:89],v2 offset:20576
v_mfma_f32_16x16x32_bf16 a[200:203],v[100:103],v[212:215],a[200:203]
ds_read_b64_tr_b16 v[90:91],v2 offset:25696
v_min_u32 v3,2,v3
v_mfma_f32_16x16x32_bf16 a[200:203],v[104:107],v[228:231],a[200:203]
buffer_load_dwordx4 a[148:151],v6,s[28:31],s88 offen offset:1024 sc1
ds_read_b64_tr_b16 v[92:93],v2 offset:30816
v_mfma_f32_16x16x32_bf16 a[200:203],v[108:111],v[244:247],a[200:203]
ds_read_b64_tr_b16 v[94:95],v2 offset:35936
v_add3_u32 v3,v3,s64,1
v_mfma_f32_16x16x32_bf16 a[204:207],v[108:111],v[188:191],a[204:207]
ds_read_b64_tr_b16 v[108:109],v1 offset:30848
ds_read_b64_tr_b16 v[110:111],v1 offset:35968
v_mfma_f32_16x16x32_bf16 a[204:207],v[104:107],v[172:175],a[204:207]
ds_read_b64_tr_b16 v[104:105],v1 offset:20608
v_and_b32 v3,3,v3
v_mfma_f32_16x16x32_bf16 a[204:207],v[100:103],v[156:159],a[204:207]
ds_read_b64_tr_b16 v[100:101],v1 offset:10368
v_lshlrev_b32 v3,6,v3
v_mfma_f32_16x16x32_bf16 a[204:207],v[96:99],v[140:143],a[204:207]
s_waitcnt lgkmcnt(9)
ds_read_b64_tr_b16 v[96:97],v1 offset:128
v_bfe_u32 v56,v6,6,2
v_mfma_f32_16x16x32_bf16 a[208:211],v[112:115],v[32:35],a[208:211]
ds_read_b64_tr_b16 v[98:99],v1 offset:5248
ds_read_b64_tr_b16 v[102:103],v1 offset:15488
v_mfma_f32_16x16x32_bf16 a[208:211],v[116:119],v[36:39],a[208:211]
ds_read_b64_tr_b16 v[106:107],v1 offset:25728
v_lshrrev_b32 v57,8,v6
v_mfma_f32_16x16x32_bf16 a[208:211],v[120:123],v[40:43],a[208:211]
v_cmp_eq_u32 s[56:57],v56,v57
v_bfe_u32 v56,v6,4,1
v_mfma_f32_16x16x32_bf16 a[208:211],v[124:127],v[44:47],a[208:211]
v_lshlrev_b32 v56,4,v56
v_mov_b32 v57,0x3f80
v_mfma_f32_16x16x32_bf16 a[212:215],v[124:127],v[180:183],a[212:215]
v_lshlrev_b32 v56,v56,v57
v_cndmask_b32 v56,0,v56,s[56:57]
L_pd21_%=:
s_cbranch_execz L_trt1_%=
v_mfma_f32_16x16x32_bf16 a[212:215],v[120:123],v[164:167],a[212:215]
v_bfe_u32 v57,v6,5,1
v_cmp_eq_u32 s[56:57],0,v57
v_mfma_f32_16x16x32_bf16 a[212:215],v[116:119],v[148:151],a[212:215]
buffer_store_dwordx4 v[140:143],v28,s[40:43],s79 offen offset:3072 sc1
v_mfma_f32_16x16x32_bf16 a[212:215],v[112:115],v[132:135],a[212:215]
v_cndmask_b32 v48,0,v56,s[56:57]
v_cndmask_b32 v49,v56,0,s[56:57]
v_mfma_f32_16x16x32_bf16 a[216:219],v[112:115],v[196:199],a[216:219]
s_add_u32 s36,s12,s66
s_addc_u32 s37,s13,0
buffer_load_dwordx4 a[132:135],v6,s[28:31],s87 offen offset:1024 sc1
v_mfma_f32_16x16x32_bf16 a[216:219],v[116:119],v[212:215],a[216:219]
s_and_b32 s37,s37,0xffff
v_mov_b32 v58,s52
v_mov_b32 v59,s53
v_mfma_f32_16x16x32_bf16 a[216:219],v[120:123],v[228:231],a[216:219]
s_mul_i32 s66,s61,307200
v_lshlrev_b32 v60,20,v6
v_bfe_u32 v61,v6,4,1
v_mfma_f32_16x16x32_bf16 a[216:219],v[124:127],v[244:247],a[216:219]
s_add_u32 s24,s16,s66
s_addc_u32 s25,s17,0
s_mul_i32 s66,s60,16384
v_mfma_f32_16x16x32_bf16 a[220:223],v[124:127],v[188:191],a[220:223]
s_add_u32 s66,s66,s67
ds_read_b64_tr_b16 v[124:125],v2 offset:30880
ds_read_b64_tr_b16 v[126:127],v2 offset:36000
v_mfma_f32_16x16x32_bf16 a[220:223],v[120:123],v[172:175],a[220:223]
s_add_u32 s79,s66,12288
ds_read_b64_tr_b16 v[120:121],v2 offset:20640
v_add3_u32 v61,v61,s63,2
v_mfma_f32_16x16x32_bf16 a[220:223],v[116:119],v[156:159],a[220:223]
s_and_b32 s25,s25,0xffff
ds_read_b64_tr_b16 v[116:117],v2 offset:10400
v_and_b32 v61,3,v61
v_mfma_f32_16x16x32_bf16 a[220:223],v[112:115],v[140:143],a[220:223]
s_add_u32 s67,s77,8192
ds_read_b64_tr_b16 v[112:113],v2 offset:160
v_lshlrev_b32 v61,8,v61
v_mfma_f32_16x16x32_bf16 a[224:227],v[64:67],v[32:35],a[224:227]
ds_read_b64_tr_b16 v[114:115],v2 offset:5280
ds_read_b64_tr_b16 v[118:119],v2 offset:15520
v_mfma_f32_16x16x32_bf16 a[224:227],v[68:71],v[36:39],a[224:227]
ds_read_b64_tr_b16 v[122:123],v2 offset:25760
v_mov_b32 v50,0
v_mfma_f32_16x16x32_bf16 a[224:227],v[72:75],v[40:43],a[224:227]
v_mov_b32 v51,0
v_mov_b32 v52,0
v_mfma_f32_16x16x32_bf16 a[224:227],v[76:79],v[44:47],a[224:227]
v_mov_b32 v53,0
v_mov_b32 v54,v48
v_mfma_f32_16x16x32_bf16 a[228:231],v[76:79],v[180:183],a[228:231]
buffer_load_dwordx4 a[136:139],v6,s[28:31],s87 offen offset:2048 sc1
v_mov_b32 v55,v49
v_mfma_f32_16x16x32_bf16 a[228:231],v[72:75],v[164:167],a[228:231]
v_bfe_u32 v56,v6,4,4
v_mul_u32_u24 v56,1280,v56
v_mfma_f32_16x16x32_bf16 a[228:231],v[68:71],v[148:151],a[228:231]
buffer_store_dwordx4 v[156:159],v28,s[40:43],s80 offen offset:3072 sc1
v_mfma_f32_16x16x32_bf16 a[228:231],v[64:67],v[132:135],a[228:231]
s_add_u32 s80,s66,77824
v_bfe_u32 v57,v6,8,1
v_lshl_add_u32 v56,v57,5,v56
v_mfma_f32_16x16x32_bf16 a[232:235],v[64:67],v[196:199],a[232:235]
v_lshrrev_b32 v57,9,v6
v_lshl_add_u32 v56,v57,4,v56
v_mfma_f32_16x16x32_bf16 a[232:235],v[68:71],v[212:215],a[232:235]
s_cmp_eq_u32 s69,3
s_cselect_b32 s69,0x40000000,0
s_add_u32 s68,s68,s69
v_mfma_f32_16x16x32_bf16 a[232:235],v[72:75],v[228:231],a[232:235]
v_mfma_f32_16x16x32_bf16 a[232:235],v[76:79],v[244:247],a[232:235]
v_mfma_f32_16x16x32_bf16 a[236:239],v[76:79],v[188:191],a[236:239]
)A"
R"A(ds_read_b64_tr_b16 v[76:77],v1 offset:30912
ds_read_b64_tr_b16 v[78:79],v1 offset:36032
v_mfma_f32_16x16x32_bf16 a[236:239],v[72:75],v[172:175],a[236:239]
ds_read_b64_tr_b16 v[72:73],v1 offset:20672
v_mfma_f32_16x16x32_bf16 a[236:239],v[68:71],v[156:159],a[236:239]
ds_read_b64_tr_b16 v[68:69],v1 offset:10432
v_mfma_f32_16x16x32_bf16 a[236:239],v[64:67],v[140:143],a[236:239]
ds_read_b64_tr_b16 v[64:65],v1 offset:192
v_mfma_f32_16x16x32_bf16 a[240:243],v[80:83],v[32:35],a[240:243]
s_waitcnt lgkmcnt(5)
ds_read_b64_tr_b16 v[66:67],v1 offset:5312
ds_read_b64_tr_b16 v[70:71],v1 offset:15552
v_mfma_f32_16x16x32_bf16 a[240:243],v[84:87],v[36:39],a[240:243]
buffer_load_dwordx4 a[152:155],v6,s[28:31],s88 offen offset:2048 sc1
ds_read_b64_tr_b16 v[74:75],v1 offset:25792
v_mfma_f32_16x16x32_bf16 a[240:243],v[88:91],v[40:43],a[240:243]
v_mfma_f32_16x16x32_bf16 a[240:243],v[92:95],v[44:47],a[240:243]
v_mfma_f32_16x16x32_bf16 a[244:247],v[92:95],v[180:183],a[244:247]
v_mfma_f32_16x16x32_bf16 a[244:247],v[88:91],v[164:167],a[244:247]
buffer_store_dwordx4 v[172:175],v28,s[40:43],s81 offen offset:3072 sc1
v_mfma_f32_16x16x32_bf16 a[244:247],v[84:87],v[148:151],a[244:247]
s_add_u32 s81,s66,143360
v_mfma_f32_16x16x32_bf16 a[244:247],v[80:83],v[132:135],a[244:247]
v_mfma_f32_16x16x32_bf16 a[248:251],v[80:83],v[196:199],a[248:251]
v_mfma_f32_16x16x32_bf16 a[248:251],v[84:87],v[212:215],a[248:251]
v_mfma_f32_16x16x32_bf16 a[248:251],v[88:91],v[228:231],a[248:251]
v_mfma_f32_16x16x32_bf16 a[248:251],v[92:95],v[244:247],a[248:251]
v_mfma_f32_16x16x32_bf16 a[252:255],v[92:95],v[188:191],a[252:255]
ds_read_b64_tr_b16 v[92:93],v2 offset:30944
ds_read_b64_tr_b16 v[94:95],v2 offset:36064
v_mfma_f32_16x16x32_bf16 a[252:255],v[88:91],v[172:175],a[252:255]
ds_read_b64_tr_b16 v[88:89],v2 offset:20704
v_mfma_f32_16x16x32_bf16 a[252:255],v[84:87],v[156:159],a[252:255]
buffer_load_dwordx4 a[168:171],v6,s[28:31],s89 offen offset:2048 sc1
ds_read_b64_tr_b16 v[84:85],v2 offset:10464
v_mfma_f32_16x16x32_bf16 a[252:255],v[80:83],v[140:143],a[252:255]
ds_read_b64_tr_b16 v[80:81],v2 offset:224
v_mfma_f32_16x16x32_bf16 a[0:3],v[96:99],v[32:35],a[0:3]
ds_read_b64_tr_b16 v[82:83],v2 offset:5344
ds_read_b64_tr_b16 v[86:87],v2 offset:15584
v_mfma_f32_16x16x32_bf16 a[0:3],v[100:103],v[36:39],a[0:3]
ds_read_b64_tr_b16 v[90:91],v2 offset:25824
v_mfma_f32_16x16x32_bf16 a[0:3],v[104:107],v[40:43],a[0:3]
v_mfma_f32_16x16x32_bf16 a[0:3],v[108:111],v[44:47],a[0:3]
v_mfma_f32_16x16x32_bf16 a[4:7],v[108:111],v[180:183],a[4:7]
buffer_store_dwordx4 v[188:191],v28,s[40:43],s82 offen offset:3072 sc1
v_mfma_f32_16x16x32_bf16 a[4:7],v[104:107],v[164:167],a[4:7]
s_add_u32 s82,s66,208896
s_mul_i32 s66,s64,320
v_mfma_f32_16x16x32_bf16 a[4:7],v[100:103],v[148:151],a[4:7]
v_mfma_f32_16x16x32_bf16 a[4:7],v[96:99],v[132:135],a[4:7]
v_mfma_f32_16x16x32_bf16 a[8:11],v[96:99],v[196:199],a[8:11]
v_mfma_f32_16x16x32_bf16 a[8:11],v[100:103],v[212:215],a[8:11]
v_mfma_f32_16x16x32_bf16 a[8:11],v[104:107],v[228:231],a[8:11]
v_mfma_f32_16x16x32_bf16 a[8:11],v[108:111],v[244:247],a[8:11]
buffer_load_dwordx4 a[184:187],v6,s[28:31],s90 offen offset:2048 sc1
v_mfma_f32_16x16x32_bf16 a[12:15],v[108:111],v[188:191],a[12:15]
ds_read_b64_tr_b16 v[108:109],v1 offset:30976
ds_read_b64_tr_b16 v[110:111],v1 offset:36096
v_mfma_f32_16x16x32_bf16 a[12:15],v[104:107],v[172:175],a[12:15]
ds_read_b64_tr_b16 v[104:105],v1 offset:20736
v_mfma_f32_16x16x32_bf16 a[12:15],v[100:103],v[156:159],a[12:15]
ds_read_b64_tr_b16 v[100:101],v1 offset:10496
v_mfma_f32_16x16x32_bf16 a[12:15],v[96:99],v[140:143],a[12:15]
ds_read_b64_tr_b16 v[96:97],v1 offset:256
v_mfma_f32_16x16x32_bf16 a[16:19],v[112:115],v[32:35],a[16:19]
ds_read_b64_tr_b16 v[98:99],v1 offset:5376
ds_read_b64_tr_b16 v[102:103],v1 offset:15616
v_mfma_f32_16x16x32_bf16 a[16:19],v[116:119],v[36:39],a[16:19]
ds_read_b64_tr_b16 v[106:107],v1 offset:25856
v_add_u32 v1,0xffff6000,v1
v_mfma_f32_16x16x32_bf16 a[16:19],v[120:123],v[40:43],a[16:19]
v_mfma_f32_16x16x32_bf16 a[16:19],v[124:127],v[44:47],a[16:19]
v_mfma_f32_16x16x32_bf16 a[20:23],v[124:127],v[180:183],a[20:23]
v_mfma_f32_16x16x32_bf16 a[20:23],v[120:123],v[164:167],a[20:23]
v_mfma_f32_16x16x32_bf16 a[20:23],v[116:119],v[148:151],a[20:23]
v_mfma_f32_16x16x32_bf16 a[20:23],v[112:115],v[132:135],a[20:23]
v_mfma_f32_16x16x32_bf16 a[24:27],v[112:115],v[196:199],a[24:27]
buffer_load_dwordx4 a[188:191],v28,s[28:31],s90 offen offset:3072 sc1
v_mfma_f32_16x16x32_bf16 a[24:27],v[116:119],v[212:215],a[24:27]
v_mfma_f32_16x16x32_bf16 a[24:27],v[120:123],v[228:231],a[24:27]
v_mfma_f32_16x16x32_bf16 a[24:27],v[124:127],v[244:247],a[24:27]
v_mfma_f32_16x16x32_bf16 a[28:31],v[124:127],v[188:191],a[28:31]
ds_read_b64_tr_b16 v[124:125],v2 offset:31008
ds_read_b64_tr_b16 v[126:127],v2 offset:36128
v_mfma_f32_16x16x32_bf16 a[28:31],v[120:123],v[172:175],a[28:31]
ds_read_b64_tr_b16 v[120:121],v2 offset:20768
v_mfma_f32_16x16x32_bf16 a[28:31],v[116:119],v[156:159],a[28:31]
ds_read_b64_tr_b16 v[116:117],v2 offset:10528
v_mfma_f32_16x16x32_bf16 a[28:31],v[112:115],v[140:143],a[28:31]
s_waitcnt lgkmcnt(4)
ds_read_b64_tr_b16 v[112:113],v2 offset:288
v_mfma_f32_16x16x32_bf16 v[128:131],v[64:67],v[32:35],v[128:131]
ds_read_b64_tr_b16 v[114:115],v2 offset:5408
ds_read_b64_tr_b16 v[118:119],v2 offset:15648
v_mfma_f32_16x16x32_bf16 v[128:131],v[68:71],v[36:39],v[128:131]
ds_read_b64_tr_b16 v[122:123],v2 offset:25888
v_add_u32 v2,0xffff6000,v2
v_mfma_f32_16x16x32_bf16 v[128:131],v[72:75],v[40:43],v[128:131]
v_mfma_f32_16x16x32_bf16 v[128:131],v[76:79],v[44:47],v[128:131]
v_mfma_f32_16x16x32_bf16 v[136:139],v[76:79],v[180:183],v[136:139]
v_mfma_f32_16x16x32_bf16 v[136:139],v[72:75],v[164:167],v[136:139]
buffer_load_dwordx4 a[172:175],v28,s[28:31],s89 offen offset:3072 sc1
v_mfma_f32_16x16x32_bf16 v[136:139],v[68:71],v[148:151],v[136:139]
v_mfma_f32_16x16x32_bf16 v[136:139],v[64:67],v[132:135],v[136:139]
v_mfma_f32_16x16x32_bf16 v[144:147],v[64:67],v[196:199],v[144:147]
v_mfma_f32_16x16x32_bf16 v[144:147],v[68:71],v[212:215],v[144:147]
v_mfma_f32_16x16x32_bf16 v[144:147],v[72:75],v[228:231],v[144:147]
v_mfma_f32_16x16x32_bf16 v[144:147],v[76:79],v[244:247],v[144:147]
v_mfma_f32_16x16x32_bf16 v[152:155],v[76:79],v[188:191],v[152:155]
v_mfma_f32_16x16x32_bf16 v[152:155],v[72:75],v[172:175],v[152:155]
v_mfma_f32_16x16x32_bf16 v[152:155],v[68:71],v[156:159],v[152:155]
v_mfma_f32_16x16x32_bf16 v[152:155],v[64:67],v[140:143],v[152:155]
s_mov_b64 exec,0
s_cmp_eq_u32 s60,0
s_cbranch_scc1 L_pd22_%=
s_cmp_eq_u32 s60,1
s_cbranch_scc1 L_pd23_%=
s_cmp_eq_u32 s60,2
s_cbranch_scc1 L_pd24_%=
s_branch L_trtg2S_%=
L_trtg2S_%=:
s_mov_b64 exec,-1
s_waitcnt lgkmcnt(0)
s_barrier
v_mfma_f32_16x16x32_bf16 v[160:163],v[80:83],v[32:35],v[160:163]
buffer_load_dwordx4 v24,s[48:51],s83 offen lds
ds_read_b64_tr_b16 v[64:65],v1
v_mfma_f32_16x16x32_bf16 v[160:163],v[84:87],v[36:39],v[160:163]
ds_read_b64_tr_b16 v[66:67],v1 offset:5120
ds_read_b64_tr_b16 v[68:69],v1 offset:10240
v_mfma_f32_16x16x32_bf16 v[160:163],v[88:91],v[40:43],v[160:163]
ds_read_b64_tr_b16 v[70:71],v1 offset:15360
v_mfma_f32_16x16x32_bf16 v[160:163],v[92:95],v[44:47],v[160:163]
ds_read_b64_tr_b16 v[72:73],v1 offset:20480
v_mfma_f32_16x16x32_bf16 v[168:171],v[92:95],v[180:183],v[168:171]
ds_read_b64_tr_b16 v[74:75],v1 offset:25600
v_mfma_f32_16x16x32_bf16 v[168:171],v[88:91],v[164:167],v[168:171]
buffer_load_dwordx4 a[156:159],v28,s[28:31],s88 offen offset:3072 sc1
ds_read_b64_tr_b16 v[76:77],v1 offset:30720
v_mfma_f32_16x16x32_bf16 v[168:171],v[84:87],v[148:151],v[168:171]
ds_read_b64_tr_b16 v[78:79],v1 offset:35840
v_mfma_f32_16x16x32_bf16 v[168:171],v[80:83],v[132:135],v[168:171]
v_mfma_f32_16x16x32_bf16 v[176:179],v[80:83],v[196:199],v[176:179]
v_mfma_f32_16x16x32_bf16 v[176:179],v[84:87],v[212:215],v[176:179]
v_mfma_f32_16x16x32_bf16 v[176:179],v[88:91],v[228:231],v[176:179]
v_mfma_f32_16x16x32_bf16 v[176:179],v[92:95],v[244:247],v[176:179]
buffer_load_dwordx4 v25,s[48:51],s83 offen offset:1024 lds
v_mfma_f32_16x16x32_bf16 v[184:187],v[92:95],v[188:191],v[184:187]
ds_read_b64_tr_b16 v[92:93],v2 offset:30752
ds_read_b64_tr_b16 v[94:95],v2 offset:35872
v_mfma_f32_16x16x32_bf16 v[184:187],v[88:91],v[172:175],v[184:187]
ds_read_b64_tr_b16 v[88:89],v2 offset:20512
v_mfma_f32_16x16x32_bf16 v[184:187],v[84:87],v[156:159],v[184:187]
ds_read_b64_tr_b16 v[84:85],v2 offset:10272
v_mfma_f32_16x16x32_bf16 v[184:187],v[80:83],v[140:143],v[184:187]
ds_read_b64_tr_b16 v[80:81],v2 offset:32
v_mfma_f32_16x16x32_bf16 v[192:195],v[96:99],v[32:35],v[192:195]
buffer_load_dwordx4 a[140:143],v28,s[28:31],s87 offen offset:3072 sc1
ds_read_b64_tr_b16 v[82:83],v2 offset:5152
v_mfma_f32_16x16x32_bf16 v[192:195],v[100:103],v[36:39],v[192:195]
s_add_u32 s87,s64,2
s_and_b32 s87,s87,3
s_lshl_b32 s87,s87,1
v_mfma_f32_16x16x32_bf16 v[192:195],v[104:107],v[40:43],v[192:195]
s_lshl_b32 s87,s87,16
s_add_u32 s88,s87,4096
s_add_u32 s89,s87,8192
v_mfma_f32_16x16x32_bf16 v[192:195],v[108:111],v[44:47],v[192:195]
s_add_u32 s90,s87,12288
ds_read_b64_tr_b16 v[86:87],v2 offset:15392
ds_read_b64_tr_b16 v[90:91],v2 offset:25632
v_mfma_f32_16x16x32_bf16 v[200:203],v[108:111],v[180:183],v[200:203]
v_mfma_f32_16x16x32_bf16 v[200:203],v[104:107],v[164:167],v[200:203]
buffer_load_dwordx4 a[68:71],v6,s[28:31],s87 offen sc1
v_mfma_f32_16x16x32_bf16 v[200:203],v[100:103],v[148:151],v[200:203]
v_mfma_f32_16x16x32_bf16 v[200:203],v[96:99],v[132:135],v[200:203]
v_mfma_f32_16x16x32_bf16 v[208:211],v[96:99],v[196:199],v[208:211]
v_mfma_f32_16x16x32_bf16 v[208:211],v[100:103],v[212:215],v[208:211]
v_mfma_f32_16x16x32_bf16 v[208:211],v[104:107],v[228:231],v[208:211]
buffer_load_dwordx4 a[76:79],v6,s[28:31],s88 offen sc1
v_mfma_f32_16x16x32_bf16 v[208:211],v[108:111],v[244:247],v[208:211]
v_mfma_f32_16x16x32_bf16 v[216:219],v[108:111],v[188:191],v[216:219]
ds_read_b64_tr_b16 v[108:109],v1 offset:30784
ds_read_b64_tr_b16 v[110:111],v1 offset:35904
v_mfma_f32_16x16x32_bf16 v[216:219],v[104:107],v[172:175],v[216:219]
ds_read_b64_tr_b16 v[104:105],v1 offset:20544
v_mfma_f32_16x16x32_bf16 v[216:219],v[100:103],v[156:159],v[216:219]
ds_read_b64_tr_b16 v[100:101],v1 offset:10304
v_mfma_f32_16x16x32_bf16 v[216:219],v[96:99],v[140:143],v[216:219]
buffer_load_dwordx4 a[84:87],v6,s[28:31],s89 offen sc1
ds_read_b64_tr_b16 v[96:97],v1 offset:64
v_mfma_f32_16x16x32_bf16 v[224:227],v[112:115],v[32:35],v[224:227]
ds_read_b64_tr_b16 v[98:99],v1 offset:5184
ds_read_b64_tr_b16 v[102:103],v1 offset:15424
v_mfma_f32_16x16x32_bf16 v[224:227],v[116:119],v[36:39],v[224:227]
ds_read_b64_tr_b16 v[106:107],v1 offset:25664
v_mfma_f32_16x16x32_bf16 v[224:227],v[120:123],v[40:43],v[224:227]
v_mfma_f32_16x16x32_bf16 v[224:227],v[124:127],v[44:47],v[224:227]
v_mfma_f32_16x16x32_bf16 v[232:235],v[124:127],v[180:183],v[232:235]
buffer_load_dwordx4 a[92:95],v6,s[28:31],s90 offen sc1
v_mfma_f32_16x16x32_bf16 v[232:235],v[120:123],v[164:167],v[232:235]
v_mfma_f32_16x16x32_bf16 v[232:235],v[116:119],v[148:151],v[232:235]
v_mfma_f32_16x16x32_bf16 v[232:235],v[112:115],v[132:135],v[232:235]
v_mfma_f32_16x16x32_bf16 v[240:243],v[112:115],v[196:199],v[240:243]
v_mfma_f32_16x16x32_bf16 v[240:243],v[116:119],v[212:215],v[240:243]
buffer_load_dwordx4 a[124:127],v6,s[28:31],s90 offen offset:1024 sc1
v_mfma_f32_16x16x32_bf16 v[240:243],v[120:123],v[228:231],v[240:243]
v_mfma_f32_16x16x32_bf16 v[240:243],v[124:127],v[244:247],v[240:243]
v_mfma_f32_16x16x32_bf16 v[248:251],v[124:127],v[188:191],v[248:251]
ds_read_b64_tr_b16 v[124:125],v2 offset:30816
ds_read_b64_tr_b16 v[126:127],v2 offset:35936
v_mfma_f32_16x16x32_bf16 v[248:251],v[120:123],v[172:175],v[248:251]
ds_read_b64_tr_b16 v[120:121],v2 offset:20576
v_mfma_f32_16x16x32_bf16 v[248:251],v[116:119],v[156:159],v[248:251]
buffer_load_dwordx4 a[116:119],v6,s[28:31],s89 offen offset:1024 sc1
ds_read_b64_tr_b16 v[116:117],v2 offset:10336
v_mfma_f32_16x16x32_bf16 v[248:251],v[112:115],v[140:143],v[248:251]
s_waitcnt vmcnt(31) lgkmcnt(4)
ds_read_b64_tr_b16 v[112:113],v2 offset:96
v_mfma_f32_16x16x32_bf16 a[192:195],v[64:67],a[128:131],a[192:195]
)A"
R"A(ds_read_b64_tr_b16 v[114:115],v2 offset:5216
ds_read_b64_tr_b16 v[118:119],v2 offset:15456
v_mfma_f32_16x16x32_bf16 a[192:195],v[68:71],a[144:147],a[192:195]
s_waitcnt vmcnt(29)
ds_read_b64_tr_b16 v[122:123],v2 offset:25696
v_mfma_f32_16x16x32_bf16 a[192:195],v[72:75],a[160:163],a[192:195]
s_waitcnt vmcnt(27)
v_mfma_f32_16x16x32_bf16 a[192:195],v[76:79],a[176:179],a[192:195]
s_waitcnt vmcnt(25)
buffer_load_dwordx4 a[108:111],v6,s[28:31],s88 offen offset:1024 sc1
v_mfma_f32_16x16x32_bf16 a[196:199],v[76:79],a[180:183],a[196:199]
s_waitcnt vmcnt(24)
v_mfma_f32_16x16x32_bf16 a[196:199],v[72:75],a[164:167],a[196:199]
s_waitcnt vmcnt(22)
v_mfma_f32_16x16x32_bf16 a[196:199],v[68:71],a[148:151],a[196:199]
s_waitcnt vmcnt(19)
v_mfma_f32_16x16x32_bf16 a[196:199],v[64:67],a[132:135],a[196:199]
v_mfma_f32_16x16x32_bf16 a[200:203],v[64:67],a[136:139],a[200:203]
s_waitcnt vmcnt(17)
buffer_load_dwordx4 a[100:103],v6,s[28:31],s87 offen offset:1024 sc1
v_mfma_f32_16x16x32_bf16 a[200:203],v[68:71],a[152:155],a[200:203]
s_waitcnt vmcnt(16)
v_mfma_f32_16x16x32_bf16 a[200:203],v[72:75],a[168:171],a[200:203]
s_waitcnt vmcnt(14)
v_mfma_f32_16x16x32_bf16 a[200:203],v[76:79],a[184:187],a[200:203]
s_waitcnt vmcnt(13)
v_mfma_f32_16x16x32_bf16 a[204:207],v[76:79],a[188:191],a[204:207]
s_waitcnt vmcnt(12)
ds_read_b64_tr_b16 v[76:77],v1 offset:30848
ds_read_b64_tr_b16 v[78:79],v1 offset:35968
v_mfma_f32_16x16x32_bf16 a[204:207],v[72:75],a[172:175],a[204:207]
s_waitcnt vmcnt(10)
buffer_load_dwordx4 a[64:67],v6,s[28:31],s87 offen offset:2048 sc1
ds_read_b64_tr_b16 v[72:73],v1 offset:20608
v_mfma_f32_16x16x32_bf16 a[204:207],v[68:71],a[156:159],a[204:207]
s_waitcnt vmcnt(9)
ds_read_b64_tr_b16 v[68:69],v1 offset:10368
v_mfma_f32_16x16x32_bf16 a[204:207],v[64:67],a[140:143],a[204:207]
ds_read_b64_tr_b16 v[64:65],v1 offset:128
v_mfma_f32_16x16x32_bf16 a[208:211],v[80:83],a[128:131],a[208:211]
ds_read_b64_tr_b16 v[66:67],v1 offset:5248
ds_read_b64_tr_b16 v[70:71],v1 offset:15488
v_mfma_f32_16x16x32_bf16 a[208:211],v[84:87],a[144:147],a[208:211]
ds_read_b64_tr_b16 v[74:75],v1 offset:25728
v_mfma_f32_16x16x32_bf16 a[208:211],v[88:91],a[160:163],a[208:211]
v_mfma_f32_16x16x32_bf16 a[208:211],v[92:95],a[176:179],a[208:211]
buffer_load_dwordx4 v26,s[48:51],s83 offen offset:2048 lds
v_mfma_f32_16x16x32_bf16 a[212:215],v[92:95],a[180:183],a[212:215]
v_mfma_f32_16x16x32_bf16 a[212:215],v[88:91],a[164:167],a[212:215]
v_mfma_f32_16x16x32_bf16 a[212:215],v[84:87],a[148:151],a[212:215]
v_mfma_f32_16x16x32_bf16 a[212:215],v[80:83],a[132:135],a[212:215]
v_mfma_f32_16x16x32_bf16 a[216:219],v[80:83],a[136:139],a[216:219]
buffer_load_dwordx4 a[72:75],v6,s[28:31],s88 offen offset:2048 sc1
v_mfma_f32_16x16x32_bf16 a[216:219],v[84:87],a[152:155],a[216:219]
v_mfma_f32_16x16x32_bf16 a[216:219],v[88:91],a[168:171],a[216:219]
v_mfma_f32_16x16x32_bf16 a[216:219],v[92:95],a[184:187],a[216:219]
v_mfma_f32_16x16x32_bf16 a[220:223],v[92:95],a[188:191],a[220:223]
ds_read_b64_tr_b16 v[92:93],v2 offset:30880
ds_read_b64_tr_b16 v[94:95],v2 offset:36000
v_mfma_f32_16x16x32_bf16 a[220:223],v[88:91],a[172:175],a[220:223]
ds_read_b64_tr_b16 v[88:89],v2 offset:20640
v_mfma_f32_16x16x32_bf16 a[220:223],v[84:87],a[156:159],a[220:223]
buffer_load_dwordx4 v27,s[48:51],s83 offen offset:3072 lds
ds_read_b64_tr_b16 v[84:85],v2 offset:10400
v_mfma_f32_16x16x32_bf16 a[220:223],v[80:83],a[140:143],a[220:223]
s_add_u32 m0,s86,126976
ds_read_b64_tr_b16 v[80:81],v2 offset:160
s_barrier
v_mfma_f32_16x16x32_bf16 a[224:227],v[96:99],a[128:131],a[224:227]
ds_read_b64_tr_b16 v[82:83],v2 offset:5280
ds_read_b64_tr_b16 v[86:87],v2 offset:15520
v_mfma_f32_16x16x32_bf16 a[224:227],v[100:103],a[144:147],a[224:227]
ds_read_b64_tr_b16 v[90:91],v2 offset:25760
v_mfma_f32_16x16x32_bf16 a[224:227],v[104:107],a[160:163],a[224:227]
buffer_store_dwordx2 v[8:9],v10,s[44:47],s18 offen offset:1280 sc1
v_mfma_f32_16x16x32_bf16 a[224:227],v[108:111],a[176:179],a[224:227]
v_mfma_f32_16x16x32_bf16 a[228:231],v[108:111],a[180:183],a[228:231]
v_mfma_f32_16x16x32_bf16 a[228:231],v[104:107],a[164:167],a[228:231]
v_mfma_f32_16x16x32_bf16 a[228:231],v[100:103],a[148:151],a[228:231]
v_mfma_f32_16x16x32_bf16 a[228:231],v[96:99],a[132:135],a[228:231]
buffer_load_dwordx4 a[80:83],v6,s[28:31],s89 offen offset:2048 sc1
v_mfma_f32_16x16x32_bf16 a[232:235],v[96:99],a[136:139],a[232:235]
v_mfma_f32_16x16x32_bf16 a[232:235],v[100:103],a[152:155],a[232:235]
v_mfma_f32_16x16x32_bf16 a[232:235],v[104:107],a[168:171],a[232:235]
v_mfma_f32_16x16x32_bf16 a[232:235],v[108:111],a[184:187],a[232:235]
v_mfma_f32_16x16x32_bf16 a[236:239],v[108:111],a[188:191],a[236:239]
ds_read_b64_tr_b16 v[108:109],v1 offset:30912
ds_read_b64_tr_b16 v[110:111],v1 offset:36032
v_mfma_f32_16x16x32_bf16 a[236:239],v[104:107],a[172:175],a[236:239]
buffer_load_dwordx4 v7,s[48:51],s83 offen lds
ds_read_b64_tr_b16 v[104:105],v1 offset:20672
v_mfma_f32_16x16x32_bf16 a[236:239],v[100:103],a[156:159],a[236:239]
s_add_u32 m0,s86,128000
s_add_u32 s83,s64,1
ds_read_b64_tr_b16 v[100:101],v1 offset:10432
v_mfma_f32_16x16x32_bf16 a[236:239],v[96:99],a[140:143],a[236:239]
s_and_b32 s83,s83,3
s_lshl_b32 s83,s83,1
ds_read_b64_tr_b16 v[96:97],v1 offset:192
s_waitcnt lgkmcnt(5)
v_mfma_f32_16x16x32_bf16 a[240:243],v[112:115],a[128:131],a[240:243]
s_or_b32 s83,s83,1
ds_read_b64_tr_b16 v[98:99],v1 offset:5312
ds_read_b64_tr_b16 v[102:103],v1 offset:15552
v_mfma_f32_16x16x32_bf16 a[240:243],v[116:119],a[144:147],a[240:243]
s_mul_i32 s83,s83,163840
s_add_u32 s83,s83,s85
ds_read_b64_tr_b16 v[106:107],v1 offset:25792
v_mfma_f32_16x16x32_bf16 a[240:243],v[120:123],a[160:163],a[240:243]
buffer_load_dwordx4 a[88:91],v6,s[28:31],s90 offen offset:2048 sc1
v_mfma_f32_16x16x32_bf16 a[240:243],v[124:127],a[176:179],a[240:243]
v_mfma_f32_16x16x32_bf16 a[244:247],v[124:127],a[180:183],a[244:247]
v_mfma_f32_16x16x32_bf16 a[244:247],v[120:123],a[164:167],a[244:247]
v_mfma_f32_16x16x32_bf16 a[244:247],v[116:119],a[148:151],a[244:247]
v_mfma_f32_16x16x32_bf16 a[244:247],v[112:115],a[132:135],a[244:247]
v_mfma_f32_16x16x32_bf16 a[248:251],v[112:115],a[136:139],a[248:251]
buffer_load_dwordx4 v24,s[48:51],s84 offen lds
v_mfma_f32_16x16x32_bf16 a[248:251],v[116:119],a[152:155],a[248:251]
v_mfma_f32_16x16x32_bf16 a[248:251],v[120:123],a[168:171],a[248:251]
v_mfma_f32_16x16x32_bf16 a[248:251],v[124:127],a[184:187],a[248:251]
v_mfma_f32_16x16x32_bf16 a[252:255],v[124:127],a[188:191],a[252:255]
ds_read_b64_tr_b16 v[124:125],v2 offset:30944
ds_read_b64_tr_b16 v[126:127],v2 offset:36064
v_mfma_f32_16x16x32_bf16 a[252:255],v[120:123],a[172:175],a[252:255]
buffer_load_dwordx4 a[120:123],v28,s[28:31],s90 offen offset:3072 sc1
ds_read_b64_tr_b16 v[120:121],v2 offset:20704
v_mfma_f32_16x16x32_bf16 a[252:255],v[116:119],a[156:159],a[252:255]
ds_read_b64_tr_b16 v[116:117],v2 offset:10464
v_mfma_f32_16x16x32_bf16 a[252:255],v[112:115],a[140:143],a[252:255]
ds_read_b64_tr_b16 v[112:113],v2 offset:224
v_mfma_f32_16x16x32_bf16 a[0:3],v[64:67],a[128:131],a[0:3]
ds_read_b64_tr_b16 v[114:115],v2 offset:5344
ds_read_b64_tr_b16 v[118:119],v2 offset:15584
v_mfma_f32_16x16x32_bf16 a[0:3],v[68:71],a[144:147],a[0:3]
ds_read_b64_tr_b16 v[122:123],v2 offset:25824
v_mfma_f32_16x16x32_bf16 a[0:3],v[72:75],a[160:163],a[0:3]
v_mfma_f32_16x16x32_bf16 a[0:3],v[76:79],a[176:179],a[0:3]
buffer_load_dwordx4 v25,s[48:51],s84 offen offset:1024 lds
v_mfma_f32_16x16x32_bf16 a[4:7],v[76:79],a[180:183],a[4:7]
v_mfma_f32_16x16x32_bf16 a[4:7],v[72:75],a[164:167],a[4:7]
v_mfma_f32_16x16x32_bf16 a[4:7],v[68:71],a[148:151],a[4:7]
v_mfma_f32_16x16x32_bf16 a[4:7],v[64:67],a[132:135],a[4:7]
v_mfma_f32_16x16x32_bf16 a[8:11],v[64:67],a[136:139],a[8:11]
buffer_load_dwordx4 a[112:115],v28,s[28:31],s89 offen offset:3072 sc1
v_mfma_f32_16x16x32_bf16 a[8:11],v[68:71],a[152:155],a[8:11]
v_mfma_f32_16x16x32_bf16 a[8:11],v[72:75],a[168:171],a[8:11]
v_mfma_f32_16x16x32_bf16 a[8:11],v[76:79],a[184:187],a[8:11]
v_mfma_f32_16x16x32_bf16 a[12:15],v[76:79],a[188:191],a[12:15]
ds_read_b64_tr_b16 v[76:77],v1 offset:30976
ds_read_b64_tr_b16 v[78:79],v1 offset:36096
v_mfma_f32_16x16x32_bf16 a[12:15],v[72:75],a[172:175],a[12:15]
buffer_load_dwordx4 a[104:107],v28,s[28:31],s88 offen offset:3072 sc1
ds_read_b64_tr_b16 v[72:73],v1 offset:20736
v_mfma_f32_16x16x32_bf16 a[12:15],v[68:71],a[156:159],a[12:15]
ds_read_b64_tr_b16 v[68:69],v1 offset:10496
v_mfma_f32_16x16x32_bf16 a[12:15],v[64:67],a[140:143],a[12:15]
ds_read_b64_tr_b16 v[64:65],v1 offset:256
v_mfma_f32_16x16x32_bf16 a[16:19],v[80:83],a[128:131],a[16:19]
ds_read_b64_tr_b16 v[66:67],v1 offset:5376
ds_read_b64_tr_b16 v[70:71],v1 offset:15616
v_mfma_f32_16x16x32_bf16 a[16:19],v[84:87],a[144:147],a[16:19]
ds_read_b64_tr_b16 v[74:75],v1 offset:25856
v_add_u32 v1,0xffff6000,v1
v_mfma_f32_16x16x32_bf16 a[16:19],v[88:91],a[160:163],a[16:19]
v_mfma_f32_16x16x32_bf16 a[16:19],v[92:95],a[176:179],a[16:19]
buffer_load_dwordx4 v26,s[48:51],s84 offen offset:2048 lds
v_mfma_f32_16x16x32_bf16 a[20:23],v[92:95],a[180:183],a[20:23]
v_mfma_f32_16x16x32_bf16 a[20:23],v[88:91],a[164:167],a[20:23]
v_mfma_f32_16x16x32_bf16 a[20:23],v[84:87],a[148:151],a[20:23]
v_mfma_f32_16x16x32_bf16 a[20:23],v[80:83],a[132:135],a[20:23]
v_mfma_f32_16x16x32_bf16 a[24:27],v[80:83],a[136:139],a[24:27]
buffer_load_dwordx4 a[96:99],v28,s[28:31],s87 offen offset:3072 sc1
v_mfma_f32_16x16x32_bf16 a[24:27],v[84:87],a[152:155],a[24:27]
s_add_u32 s87,s64,3
s_and_b32 s87,s87,3
s_lshl_b32 s87,s87,1
v_mfma_f32_16x16x32_bf16 a[24:27],v[88:91],a[168:171],a[24:27]
s_lshl_b32 s87,s87,16
s_add_u32 s88,s87,4096
s_add_u32 s89,s87,8192
v_mfma_f32_16x16x32_bf16 a[24:27],v[92:95],a[184:187],a[24:27]
s_add_u32 s90,s87,12288
v_mfma_f32_16x16x32_bf16 a[28:31],v[92:95],a[188:191],a[28:31]
ds_read_b64_tr_b16 v[92:93],v2 offset:31008
ds_read_b64_tr_b16 v[94:95],v2 offset:36128
v_mfma_f32_16x16x32_bf16 a[28:31],v[88:91],a[172:175],a[28:31]
ds_read_b64_tr_b16 v[88:89],v2 offset:20768
v_mfma_f32_16x16x32_bf16 a[28:31],v[84:87],a[156:159],a[28:31]
buffer_load_dwordx4 v27,s[48:51],s84 offen offset:3072 lds
ds_read_b64_tr_b16 v[84:85],v2 offset:10528
v_mfma_f32_16x16x32_bf16 a[28:31],v[80:83],a[140:143],a[28:31]
s_add_u32 m0,s86,132096
ds_read_b64_tr_b16 v[80:81],v2 offset:288
s_waitcnt lgkmcnt(5)
v_mfma_f32_16x16x32_bf16 v[128:131],v[96:99],a[128:131],v[128:131]
ds_read_b64_tr_b16 v[82:83],v2 offset:5408
ds_read_b64_tr_b16 v[86:87],v2 offset:15648
v_mfma_f32_16x16x32_bf16 v[128:131],v[100:103],a[144:147],v[128:131]
ds_read_b64_tr_b16 v[90:91],v2 offset:25888
v_add_u32 v2,0xffff6000,v2
v_mfma_f32_16x16x32_bf16 v[128:131],v[104:107],a[160:163],v[128:131]
v_mfma_f32_16x16x32_bf16 v[128:131],v[108:111],a[176:179],v[128:131]
v_mfma_f32_16x16x32_bf16 v[136:139],v[108:111],a[180:183],v[136:139]
buffer_load_dwordx4 v7,s[48:51],s84 offen lds
v_mfma_f32_16x16x32_bf16 v[136:139],v[104:107],a[164:167],v[136:139]
s_add_u32 m0,s86,81920
s_add_u32 s84,s83,20480
v_mfma_f32_16x16x32_bf16 v[136:139],v[100:103],a[148:151],v[136:139]
v_mfma_f32_16x16x32_bf16 v[136:139],v[96:99],a[132:135],v[136:139]
v_mfma_f32_16x16x32_bf16 v[144:147],v[96:99],a[136:139],v[144:147]
v_mfma_f32_16x16x32_bf16 v[144:147],v[100:103],a[152:155],v[144:147]
v_mfma_f32_16x16x32_bf16 v[144:147],v[104:107],a[168:171],v[144:147]
buffer_load_dwordx4 v24,s[48:51],s83 offen lds
v_mfma_f32_16x16x32_bf16 v[144:147],v[108:111],a[184:187],v[144:147]
v_mfma_f32_16x16x32_bf16 v[152:155],v[108:111],a[188:191],v[152:155]
ds_read_b64_tr_b16 v[108:109],v1 offset:30720
ds_read_b64_tr_b16 v[110:111],v1 offset:35840
v_mfma_f32_16x16x32_bf16 v[152:155],v[104:107],a[172:175],v[152:155]
ds_read_b64_tr_b16 v[104:105],v1 offset:20480
v_mfma_f32_16x16x32_bf16 v[152:155],v[100:103],a[156:159],v[152:155]
ds_read_b64_tr_b16 v[100:101],v1 offset:10240
v_mfma_f32_16x16x32_bf16 v[152:155],v[96:99],a[140:143],v[152:155]
ds_read_b64_tr_b16 v[96:97],v1
v_mfma_f32_16x16x32_bf16 v[160:163],v[112:115],a[128:131],v[160:163]
buffer_load_dwordx4 v25,s[48:51],s83 offen offset:1024 lds
ds_read_b64_tr_b16 v[98:99],v1 offset:5120
v_mfma_f32_16x16x32_bf16 v[160:163],v[116:119],a[144:147],v[160:163]
)A"
R"A(ds_read_b64_tr_b16 v[102:103],v1 offset:15360
ds_read_b64_tr_b16 v[106:107],v1 offset:25600
v_mfma_f32_16x16x32_bf16 v[160:163],v[120:123],a[160:163],v[160:163]
v_mfma_f32_16x16x32_bf16 v[160:163],v[124:127],a[176:179],v[160:163]
v_mfma_f32_16x16x32_bf16 v[168:171],v[124:127],a[180:183],v[168:171]
v_mfma_f32_16x16x32_bf16 v[168:171],v[120:123],a[164:167],v[168:171]
v_mfma_f32_16x16x32_bf16 v[168:171],v[116:119],a[148:151],v[168:171]
buffer_load_dwordx4 v26,s[48:51],s83 offen offset:2048 lds
v_mfma_f32_16x16x32_bf16 v[168:171],v[112:115],a[132:135],v[168:171]
v_mfma_f32_16x16x32_bf16 v[176:179],v[112:115],a[136:139],v[176:179]
v_mfma_f32_16x16x32_bf16 v[176:179],v[116:119],a[152:155],v[176:179]
v_mfma_f32_16x16x32_bf16 v[176:179],v[120:123],a[168:171],v[176:179]
v_mfma_f32_16x16x32_bf16 v[176:179],v[124:127],a[184:187],v[176:179]
v_mfma_f32_16x16x32_bf16 v[184:187],v[124:127],a[188:191],v[184:187]
buffer_load_dwordx4 v27,s[48:51],s83 offen offset:3072 lds
ds_read_b64_tr_b16 v[124:125],v2 offset:30752
v_mfma_f32_16x16x32_bf16 v[184:187],v[120:123],a[172:175],v[184:187]
s_add_u32 m0,s86,86016
ds_read_b64_tr_b16 v[120:121],v2 offset:20512
ds_read_b64_tr_b16 v[122:123],v2 offset:25632
v_mfma_f32_16x16x32_bf16 v[184:187],v[116:119],a[156:159],v[184:187]
ds_read_b64_tr_b16 v[116:117],v2 offset:10272
L_pd22_%=:
s_cbranch_execz L_trtg2S_%=
v_mfma_f32_16x16x32_bf16 v[184:187],v[112:115],a[140:143],v[184:187]
ds_read_b64_tr_b16 v[112:113],v2 offset:32
v_mfma_f32_16x16x32_bf16 v[192:195],v[64:67],a[128:131],v[192:195]
ds_read_b64_tr_b16 v[114:115],v2 offset:5152
v_mfma_f32_16x16x32_bf16 v[192:195],v[68:71],a[144:147],v[192:195]
ds_read_b64_tr_b16 v[118:119],v2 offset:15392
ds_read_b64_tr_b16 v[126:127],v2 offset:35872
v_mfma_f32_16x16x32_bf16 v[192:195],v[72:75],a[160:163],v[192:195]
buffer_load_dwordx4 v7,s[48:51],s83 offen lds
v_mfma_f32_16x16x32_bf16 v[192:195],v[76:79],a[176:179],v[192:195]
s_add_u32 m0,s86,87040
s_add_u32 s83,s64,2
s_and_b32 s83,s83,3
v_mfma_f32_16x16x32_bf16 v[200:203],v[76:79],a[180:183],v[200:203]
s_lshl_b32 s83,s83,1
s_or_b32 s83,s83,1
s_mul_i32 s83,s83,163840
v_mfma_f32_16x16x32_bf16 v[200:203],v[72:75],a[164:167],v[200:203]
s_add_u32 s83,s83,s85
v_mfma_f32_16x16x32_bf16 v[200:203],v[68:71],a[148:151],v[200:203]
v_mfma_f32_16x16x32_bf16 v[200:203],v[64:67],a[132:135],v[200:203]
v_mfma_f32_16x16x32_bf16 v[208:211],v[64:67],a[136:139],v[208:211]
buffer_load_dwordx4 v24,s[48:51],s84 offen lds
v_mfma_f32_16x16x32_bf16 v[208:211],v[68:71],a[152:155],v[208:211]
v_mfma_f32_16x16x32_bf16 v[208:211],v[72:75],a[168:171],v[208:211]
v_mfma_f32_16x16x32_bf16 v[208:211],v[76:79],a[184:187],v[208:211]
v_mfma_f32_16x16x32_bf16 v[216:219],v[76:79],a[188:191],v[216:219]
ds_read_b64_tr_b16 v[76:77],v1 offset:30784
ds_read_b64_tr_b16 v[78:79],v1 offset:35904
v_mfma_f32_16x16x32_bf16 v[216:219],v[72:75],a[172:175],v[216:219]
ds_read_b64_tr_b16 v[72:73],v1 offset:20544
v_mfma_f32_16x16x32_bf16 v[216:219],v[68:71],a[156:159],v[216:219]
buffer_load_dwordx2 v[4:5],v3,s[44:47],s91 offen offset:1280 sc1
ds_read_b64_tr_b16 v[68:69],v1 offset:10304
v_mfma_f32_16x16x32_bf16 v[216:219],v[64:67],a[140:143],v[216:219]
s_waitcnt lgkmcnt(4)
ds_read_b64_tr_b16 v[64:65],v1 offset:64
v_mfma_f32_16x16x32_bf16 v[224:227],v[80:83],a[128:131],v[224:227]
ds_read_b64_tr_b16 v[66:67],v1 offset:5184
ds_read_b64_tr_b16 v[70:71],v1 offset:15424
v_mfma_f32_16x16x32_bf16 v[224:227],v[84:87],a[144:147],v[224:227]
ds_read_b64_tr_b16 v[74:75],v1 offset:25664
v_mfma_f32_16x16x32_bf16 v[224:227],v[88:91],a[160:163],v[224:227]
v_mfma_f32_16x16x32_bf16 v[224:227],v[92:95],a[176:179],v[224:227]
v_mfma_f32_16x16x32_bf16 v[232:235],v[92:95],a[180:183],v[232:235]
buffer_load_dwordx4 v25,s[48:51],s84 offen offset:1024 lds
v_mfma_f32_16x16x32_bf16 v[232:235],v[88:91],a[164:167],v[232:235]
v_mfma_f32_16x16x32_bf16 v[232:235],v[84:87],a[148:151],v[232:235]
v_mfma_f32_16x16x32_bf16 v[232:235],v[80:83],a[132:135],v[232:235]
v_mfma_f32_16x16x32_bf16 v[240:243],v[80:83],a[136:139],v[240:243]
v_mfma_f32_16x16x32_bf16 v[240:243],v[84:87],a[152:155],v[240:243]
v_mfma_f32_16x16x32_bf16 v[240:243],v[88:91],a[168:171],v[240:243]
buffer_load_dwordx4 v26,s[48:51],s84 offen offset:2048 lds
v_mfma_f32_16x16x32_bf16 v[240:243],v[92:95],a[184:187],v[240:243]
v_mfma_f32_16x16x32_bf16 v[248:251],v[92:95],a[188:191],v[248:251]
ds_read_b64_tr_b16 v[92:93],v2 offset:30816
ds_read_b64_tr_b16 v[94:95],v2 offset:35936
v_mfma_f32_16x16x32_bf16 v[248:251],v[88:91],a[172:175],v[248:251]
ds_read_b64_tr_b16 v[88:89],v2 offset:20576
v_mfma_f32_16x16x32_bf16 v[248:251],v[84:87],a[156:159],v[248:251]
ds_read_b64_tr_b16 v[84:85],v2 offset:10336
v_mfma_f32_16x16x32_bf16 v[248:251],v[80:83],a[140:143],v[248:251]
s_waitcnt vmcnt(25)
ds_read_b64_tr_b16 v[80:81],v2 offset:96
v_mfma_f32_16x16x32_bf16 a[192:195],v[96:99],a[68:71],a[192:195]
buffer_load_dwordx4 v27,s[48:51],s84 offen offset:3072 lds
ds_read_b64_tr_b16 v[82:83],v2 offset:5216
v_mfma_f32_16x16x32_bf16 a[192:195],v[100:103],a[76:79],a[192:195]
s_add_u32 m0,s86,91136
ds_read_b64_tr_b16 v[86:87],v2 offset:15456
ds_read_b64_tr_b16 v[90:91],v2 offset:25696
v_mfma_f32_16x16x32_bf16 a[192:195],v[104:107],a[84:87],a[192:195]
v_mfma_f32_16x16x32_bf16 a[192:195],v[108:111],a[92:95],a[192:195]
v_mfma_f32_16x16x32_bf16 a[196:199],v[108:111],a[124:127],a[196:199]
v_mfma_f32_16x16x32_bf16 a[196:199],v[104:107],a[116:119],a[196:199]
v_mfma_f32_16x16x32_bf16 a[196:199],v[100:103],a[108:111],a[196:199]
buffer_load_dwordx4 v7,s[48:51],s84 offen lds
v_mfma_f32_16x16x32_bf16 a[196:199],v[96:99],a[100:103],a[196:199]
s_add_u32 m0,s86,40960
s_add_u32 s84,s83,20480
v_mfma_f32_16x16x32_bf16 a[200:203],v[96:99],a[64:67],a[200:203]
s_waitcnt vmcnt(25)
v_mfma_f32_16x16x32_bf16 a[200:203],v[100:103],a[72:75],a[200:203]
s_waitcnt vmcnt(22)
v_mfma_f32_16x16x32_bf16 a[200:203],v[104:107],a[80:83],a[200:203]
s_waitcnt vmcnt(20)
v_mfma_f32_16x16x32_bf16 a[200:203],v[108:111],a[88:91],a[200:203]
s_waitcnt vmcnt(18)
buffer_load_dwordx4 v[32:35],v6,s[28:31],s87 offen sc1
v_mfma_f32_16x16x32_bf16 a[204:207],v[108:111],a[120:123],a[204:207]
s_waitcnt vmcnt(17)
ds_read_b64_tr_b16 v[108:109],v1 offset:30848
ds_read_b64_tr_b16 v[110:111],v1 offset:35968
v_mfma_f32_16x16x32_bf16 a[204:207],v[104:107],a[112:115],a[204:207]
s_waitcnt vmcnt(16)
ds_read_b64_tr_b16 v[104:105],v1 offset:20608
v_mfma_f32_16x16x32_bf16 a[204:207],v[100:103],a[104:107],a[204:207]
s_waitcnt vmcnt(14)
ds_read_b64_tr_b16 v[100:101],v1 offset:10368
v_mfma_f32_16x16x32_bf16 a[204:207],v[96:99],a[96:99],a[204:207]
ds_read_b64_tr_b16 v[96:97],v1 offset:128
v_mfma_f32_16x16x32_bf16 a[208:211],v[112:115],a[68:71],a[208:211]
buffer_load_dwordx4 v[132:135],v6,s[28:31],s87 offen offset:1024 sc1
ds_read_b64_tr_b16 v[98:99],v1 offset:5248
v_mfma_f32_16x16x32_bf16 a[208:211],v[116:119],a[76:79],a[208:211]
ds_read_b64_tr_b16 v[102:103],v1 offset:15488
ds_read_b64_tr_b16 v[106:107],v1 offset:25728
v_mfma_f32_16x16x32_bf16 a[208:211],v[120:123],a[84:87],a[208:211]
v_mfma_f32_16x16x32_bf16 a[208:211],v[124:127],a[92:95],a[208:211]
v_mfma_f32_16x16x32_bf16 a[212:215],v[124:127],a[124:127],a[212:215]
v_mfma_f32_16x16x32_bf16 a[212:215],v[120:123],a[116:119],a[212:215]
buffer_load_dwordx4 v[196:199],v6,s[28:31],s87 offen offset:2048 sc1
v_mfma_f32_16x16x32_bf16 a[212:215],v[116:119],a[108:111],a[212:215]
v_mfma_f32_16x16x32_bf16 a[212:215],v[112:115],a[100:103],a[212:215]
v_mfma_f32_16x16x32_bf16 a[216:219],v[112:115],a[64:67],a[216:219]
v_mfma_f32_16x16x32_bf16 a[216:219],v[116:119],a[72:75],a[216:219]
v_mfma_f32_16x16x32_bf16 a[216:219],v[120:123],a[80:83],a[216:219]
buffer_load_dwordx4 v[36:39],v6,s[28:31],s88 offen sc1
v_mfma_f32_16x16x32_bf16 a[216:219],v[124:127],a[88:91],a[216:219]
v_mfma_f32_16x16x32_bf16 a[220:223],v[124:127],a[120:123],a[220:223]
ds_read_b64_tr_b16 v[124:125],v2 offset:30880
ds_read_b64_tr_b16 v[126:127],v2 offset:36000
v_mfma_f32_16x16x32_bf16 a[220:223],v[120:123],a[112:115],a[220:223]
ds_read_b64_tr_b16 v[120:121],v2 offset:20640
v_mfma_f32_16x16x32_bf16 a[220:223],v[116:119],a[104:107],a[220:223]
ds_read_b64_tr_b16 v[116:117],v2 offset:10400
v_mfma_f32_16x16x32_bf16 a[220:223],v[112:115],a[96:99],a[220:223]
s_waitcnt lgkmcnt(4)
buffer_load_dwordx4 v[140:143],v28,s[28:31],s87 offen offset:3072 sc1
ds_read_b64_tr_b16 v[112:113],v2 offset:160
v_mfma_f32_16x16x32_bf16 a[224:227],v[64:67],a[68:71],a[224:227]
s_add_u32 s87,s64,1
s_and_b32 s87,s87,3
s_lshl_b32 s87,s87,1
v_mfma_f32_16x16x32_bf16 a[224:227],v[68:71],a[76:79],a[224:227]
s_or_b32 s87,s87,1
s_lshl_b32 s87,s87,16
ds_read_b64_tr_b16 v[114:115],v2 offset:5280
v_mfma_f32_16x16x32_bf16 a[224:227],v[72:75],a[84:87],a[224:227]
ds_read_b64_tr_b16 v[118:119],v2 offset:15520
ds_read_b64_tr_b16 v[122:123],v2 offset:25760
v_mfma_f32_16x16x32_bf16 a[224:227],v[76:79],a[92:95],a[224:227]
v_mfma_f32_16x16x32_bf16 a[228:231],v[76:79],a[124:127],a[228:231]
buffer_load_dwordx4 v[212:215],v6,s[28:31],s88 offen offset:2048 sc1
v_mfma_f32_16x16x32_bf16 a[228:231],v[72:75],a[116:119],a[228:231]
v_mfma_f32_16x16x32_bf16 a[228:231],v[68:71],a[108:111],a[228:231]
v_mfma_f32_16x16x32_bf16 a[228:231],v[64:67],a[100:103],a[228:231]
v_mfma_f32_16x16x32_bf16 a[232:235],v[64:67],a[64:67],a[232:235]
v_mfma_f32_16x16x32_bf16 a[232:235],v[68:71],a[72:75],a[232:235]
buffer_load_dwordx4 v[148:151],v6,s[28:31],s88 offen offset:1024 sc1
v_mfma_f32_16x16x32_bf16 a[232:235],v[72:75],a[80:83],a[232:235]
s_waitcnt vmcnt(11)
v_cmp_ne_u32 s[56:57],s52,v4
v_cmp_ne_u32 s[58:59],s53,v5
v_mfma_f32_16x16x32_bf16 a[232:235],v[76:79],a[88:91],a[232:235]
s_or_b64 s[56:57],s[56:57],s[58:59]
s_cbranch_scc1 L_fr7_%=
L_fo7_%=:
s_lshl_b32 s91,s64,6
v_mfma_f32_16x16x32_bf16 a[236:239],v[76:79],a[120:123],a[236:239]
ds_read_b64_tr_b16 v[76:77],v1 offset:30912
ds_read_b64_tr_b16 v[78:79],v1 offset:36032
v_mfma_f32_16x16x32_bf16 a[236:239],v[72:75],a[112:115],a[236:239]
ds_read_b64_tr_b16 v[72:73],v1 offset:20672
v_bfe_u32 v3,v6,4,2
v_mfma_f32_16x16x32_bf16 a[236:239],v[68:71],a[104:107],a[236:239]
buffer_load_dwordx4 v[40:43],v6,s[28:31],s89 offen sc1
ds_read_b64_tr_b16 v[68:69],v1 offset:10432
v_mfma_f32_16x16x32_bf16 a[236:239],v[64:67],a[96:99],a[236:239]
ds_read_b64_tr_b16 v[64:65],v1 offset:192
v_min_u32 v3,2,v3
v_mfma_f32_16x16x32_bf16 a[240:243],v[80:83],a[68:71],a[240:243]
ds_read_b64_tr_b16 v[66:67],v1 offset:5312
ds_read_b64_tr_b16 v[70:71],v1 offset:15552
v_mfma_f32_16x16x32_bf16 a[240:243],v[84:87],a[76:79],a[240:243]
ds_read_b64_tr_b16 v[74:75],v1 offset:25792
v_add3_u32 v3,v3,s63,1
v_mfma_f32_16x16x32_bf16 a[240:243],v[88:91],a[84:87],a[240:243]
v_and_b32 v3,3,v3
v_lshlrev_b32 v3,8,v3
v_mfma_f32_16x16x32_bf16 a[240:243],v[92:95],a[92:95],a[240:243]
buffer_load_dwordx4 a[128:131],v6,s[28:31],s87 offen sc1
v_mfma_f32_16x16x32_bf16 a[244:247],v[92:95],a[124:127],a[244:247]
v_mfma_f32_16x16x32_bf16 a[244:247],v[88:91],a[116:119],a[244:247]
v_mfma_f32_16x16x32_bf16 a[244:247],v[84:87],a[108:111],a[244:247]
v_mfma_f32_16x16x32_bf16 a[244:247],v[80:83],a[100:103],a[244:247]
v_mfma_f32_16x16x32_bf16 a[248:251],v[80:83],a[64:67],a[248:251]
buffer_load_dwordx4 v[156:159],v28,s[28:31],s88 offen offset:3072 sc1
v_mfma_f32_16x16x32_bf16 a[248:251],v[84:87],a[72:75],a[248:251]
s_add_u32 s88,s87,4096
v_mfma_f32_16x16x32_bf16 a[248:251],v[88:91],a[80:83],a[248:251]
v_mfma_f32_16x16x32_bf16 a[248:251],v[92:95],a[88:91],a[248:251]
v_mfma_f32_16x16x32_bf16 a[252:255],v[92:95],a[120:123],a[252:255]
ds_read_b64_tr_b16 v[92:93],v2 offset:30944
ds_read_b64_tr_b16 v[94:95],v2 offset:36064
v_mfma_f32_16x16x32_bf16 a[252:255],v[88:91],a[112:115],a[252:255]
buffer_load_dwordx4 v[228:231],v6,s[28:31],s89 offen offset:2048 sc1
ds_read_b64_tr_b16 v[88:89],v2 offset:20704
v_mfma_f32_16x16x32_bf16 a[252:255],v[84:87],a[104:107],a[252:255]
ds_read_b64_tr_b16 v[84:85],v2 offset:10464
v_mfma_f32_16x16x32_bf16 a[252:255],v[80:83],a[96:99],a[252:255]
ds_read_b64_tr_b16 v[80:81],v2 offset:224
v_mfma_f32_16x16x32_bf16 a[0:3],v[96:99],a[68:71],a[0:3]
ds_read_b64_tr_b16 v[82:83],v2 offset:5344
)A"
R"A(ds_read_b64_tr_b16 v[86:87],v2 offset:15584
v_mfma_f32_16x16x32_bf16 a[0:3],v[100:103],a[76:79],a[0:3]
ds_read_b64_tr_b16 v[90:91],v2 offset:25824
v_mfma_f32_16x16x32_bf16 a[0:3],v[104:107],a[84:87],a[0:3]
buffer_load_dwordx4 v[164:167],v6,s[28:31],s89 offen offset:1024 sc1
v_mfma_f32_16x16x32_bf16 a[0:3],v[108:111],a[92:95],a[0:3]
v_mfma_f32_16x16x32_bf16 a[4:7],v[108:111],a[124:127],a[4:7]
v_mfma_f32_16x16x32_bf16 a[4:7],v[104:107],a[116:119],a[4:7]
v_mfma_f32_16x16x32_bf16 a[4:7],v[100:103],a[108:111],a[4:7]
v_mfma_f32_16x16x32_bf16 a[4:7],v[96:99],a[100:103],a[4:7]
buffer_load_dwordx4 v[44:47],v6,s[28:31],s90 offen sc1
v_mfma_f32_16x16x32_bf16 a[8:11],v[96:99],a[64:67],a[8:11]
v_mfma_f32_16x16x32_bf16 a[8:11],v[100:103],a[72:75],a[8:11]
v_mfma_f32_16x16x32_bf16 a[8:11],v[104:107],a[80:83],a[8:11]
v_mfma_f32_16x16x32_bf16 a[8:11],v[108:111],a[88:91],a[8:11]
v_mfma_f32_16x16x32_bf16 a[12:15],v[108:111],a[120:123],a[12:15]
buffer_load_dwordx4 a[144:147],v6,s[28:31],s88 offen sc1
ds_read_b64_tr_b16 v[108:109],v1 offset:30976
v_mfma_f32_16x16x32_bf16 a[12:15],v[104:107],a[112:115],a[12:15]
ds_read_b64_tr_b16 v[104:105],v1 offset:20736
ds_read_b64_tr_b16 v[106:107],v1 offset:25856
v_mfma_f32_16x16x32_bf16 a[12:15],v[100:103],a[104:107],a[12:15]
ds_read_b64_tr_b16 v[100:101],v1 offset:10496
v_mfma_f32_16x16x32_bf16 a[12:15],v[96:99],a[96:99],a[12:15]
s_waitcnt lgkmcnt(4)
ds_read_b64_tr_b16 v[96:97],v1 offset:256
v_mfma_f32_16x16x32_bf16 a[16:19],v[112:115],a[68:71],a[16:19]
ds_read_b64_tr_b16 v[98:99],v1 offset:5376
v_mfma_f32_16x16x32_bf16 a[16:19],v[116:119],a[76:79],a[16:19]
buffer_load_dwordx4 v[172:175],v28,s[28:31],s89 offen offset:3072 sc1
ds_read_b64_tr_b16 v[102:103],v1 offset:15616
v_mfma_f32_16x16x32_bf16 a[16:19],v[120:123],a[84:87],a[16:19]
s_add_u32 s89,s87,8192
ds_read_b64_tr_b16 v[110:111],v1 offset:36096
v_add_u32 v1,0x1e000,v1
v_mfma_f32_16x16x32_bf16 a[16:19],v[124:127],a[92:95],a[16:19]
v_mfma_f32_16x16x32_bf16 a[20:23],v[124:127],a[124:127],a[20:23]
v_mfma_f32_16x16x32_bf16 a[20:23],v[120:123],a[116:119],a[20:23]
v_mfma_f32_16x16x32_bf16 a[20:23],v[116:119],a[108:111],a[20:23]
buffer_load_dwordx4 v[180:183],v6,s[28:31],s90 offen offset:1024 sc1
v_mfma_f32_16x16x32_bf16 a[20:23],v[112:115],a[100:103],a[20:23]
v_mfma_f32_16x16x32_bf16 a[24:27],v[112:115],a[64:67],a[24:27]
v_mfma_f32_16x16x32_bf16 a[24:27],v[116:119],a[72:75],a[24:27]
v_mfma_f32_16x16x32_bf16 a[24:27],v[120:123],a[80:83],a[24:27]
v_mfma_f32_16x16x32_bf16 a[24:27],v[124:127],a[88:91],a[24:27]
buffer_load_dwordx4 a[160:163],v6,s[28:31],s89 offen sc1
v_mfma_f32_16x16x32_bf16 a[28:31],v[124:127],a[120:123],a[28:31]
ds_read_b64_tr_b16 v[124:125],v2 offset:31008
ds_read_b64_tr_b16 v[126:127],v2 offset:36128
v_mfma_f32_16x16x32_bf16 a[28:31],v[120:123],a[112:115],a[28:31]
ds_read_b64_tr_b16 v[120:121],v2 offset:20768
v_mfma_f32_16x16x32_bf16 a[28:31],v[116:119],a[104:107],a[28:31]
ds_read_b64_tr_b16 v[116:117],v2 offset:10528
v_mfma_f32_16x16x32_bf16 a[28:31],v[112:115],a[96:99],a[28:31]
ds_read_b64_tr_b16 v[112:113],v2 offset:288
v_mfma_f32_16x16x32_bf16 v[128:131],v[64:67],a[68:71],v[128:131]
buffer_load_dwordx4 v[244:247],v6,s[28:31],s90 offen offset:2048 sc1
ds_read_b64_tr_b16 v[114:115],v2 offset:5408
v_mfma_f32_16x16x32_bf16 v[128:131],v[68:71],a[76:79],v[128:131]
ds_read_b64_tr_b16 v[118:119],v2 offset:15648
ds_read_b64_tr_b16 v[122:123],v2 offset:25888
v_mfma_f32_16x16x32_bf16 v[128:131],v[72:75],a[84:87],v[128:131]
v_add_u32 v2,0x1e000,v2
v_mfma_f32_16x16x32_bf16 v[128:131],v[76:79],a[92:95],v[128:131]
v_mfma_f32_16x16x32_bf16 v[136:139],v[76:79],a[124:127],v[136:139]
v_mfma_f32_16x16x32_bf16 v[136:139],v[72:75],a[116:119],v[136:139]
buffer_load_dwordx4 v[188:191],v28,s[28:31],s90 offen offset:3072 sc1
v_mfma_f32_16x16x32_bf16 v[136:139],v[68:71],a[108:111],v[136:139]
s_add_u32 s90,s87,12288
v_mfma_f32_16x16x32_bf16 v[136:139],v[64:67],a[100:103],v[136:139]
v_mfma_f32_16x16x32_bf16 v[144:147],v[64:67],a[64:67],v[144:147]
v_mfma_f32_16x16x32_bf16 v[144:147],v[68:71],a[72:75],v[144:147]
v_mfma_f32_16x16x32_bf16 v[144:147],v[72:75],a[80:83],v[144:147]
buffer_load_dwordx4 a[176:179],v6,s[28:31],s90 offen sc1
v_mfma_f32_16x16x32_bf16 v[144:147],v[76:79],a[88:91],v[144:147]
v_mfma_f32_16x16x32_bf16 v[152:155],v[76:79],a[120:123],v[152:155]
v_mfma_f32_16x16x32_bf16 v[152:155],v[72:75],a[112:115],v[152:155]
v_mfma_f32_16x16x32_bf16 v[152:155],v[68:71],a[104:107],v[152:155]
v_mfma_f32_16x16x32_bf16 v[152:155],v[64:67],a[96:99],v[152:155]
buffer_load_dwordx4 a[180:183],v6,s[28:31],s90 offen offset:1024 sc1
s_waitcnt vmcnt(21)
s_barrier
v_mfma_f32_16x16x32_bf16 v[160:163],v[80:83],a[68:71],v[160:163]
ds_read_b64_tr_b16 v[64:65],v1
ds_read_b64_tr_b16 v[66:67],v1 offset:5120
v_mfma_f32_16x16x32_bf16 v[160:163],v[84:87],a[76:79],v[160:163]
ds_read_b64_tr_b16 v[68:69],v1 offset:10240
v_mfma_f32_16x16x32_bf16 v[160:163],v[88:91],a[84:87],v[160:163]
ds_read_b64_tr_b16 v[70:71],v1 offset:15360
v_mfma_f32_16x16x32_bf16 v[160:163],v[92:95],a[92:95],v[160:163]
buffer_load_dwordx4 a[164:167],v6,s[28:31],s89 offen offset:1024 sc1
ds_read_b64_tr_b16 v[72:73],v1 offset:20480
v_mfma_f32_16x16x32_bf16 v[168:171],v[92:95],a[124:127],v[168:171]
ds_read_b64_tr_b16 v[74:75],v1 offset:25600
ds_read_b64_tr_b16 v[76:77],v1 offset:30720
v_mfma_f32_16x16x32_bf16 v[168:171],v[88:91],a[116:119],v[168:171]
ds_read_b64_tr_b16 v[78:79],v1 offset:35840
v_mfma_f32_16x16x32_bf16 v[168:171],v[84:87],a[108:111],v[168:171]
v_mfma_f32_16x16x32_bf16 v[168:171],v[80:83],a[100:103],v[168:171]
v_mfma_f32_16x16x32_bf16 v[176:179],v[80:83],a[64:67],v[176:179]
buffer_load_dwordx4 a[148:151],v6,s[28:31],s88 offen offset:1024 sc1
v_mfma_f32_16x16x32_bf16 v[176:179],v[84:87],a[72:75],v[176:179]
v_mfma_f32_16x16x32_bf16 v[176:179],v[88:91],a[80:83],v[176:179]
v_mfma_f32_16x16x32_bf16 v[176:179],v[92:95],a[88:91],v[176:179]
v_mfma_f32_16x16x32_bf16 v[184:187],v[92:95],a[120:123],v[184:187]
ds_read_b64_tr_b16 v[92:93],v2 offset:30752
ds_read_b64_tr_b16 v[94:95],v2 offset:35872
v_mfma_f32_16x16x32_bf16 v[184:187],v[88:91],a[112:115],v[184:187]
buffer_load_dwordx4 a[132:135],v6,s[28:31],s87 offen offset:1024 sc1
ds_read_b64_tr_b16 v[88:89],v2 offset:20512
v_mfma_f32_16x16x32_bf16 v[184:187],v[84:87],a[104:107],v[184:187]
ds_read_b64_tr_b16 v[84:85],v2 offset:10272
v_mfma_f32_16x16x32_bf16 v[184:187],v[80:83],a[96:99],v[184:187]
s_waitcnt lgkmcnt(4)
ds_read_b64_tr_b16 v[80:81],v2 offset:32
v_mfma_f32_16x16x32_bf16 v[192:195],v[96:99],a[68:71],v[192:195]
ds_read_b64_tr_b16 v[82:83],v2 offset:5152
ds_read_b64_tr_b16 v[86:87],v2 offset:15392
v_mfma_f32_16x16x32_bf16 v[192:195],v[100:103],a[76:79],v[192:195]
ds_read_b64_tr_b16 v[90:91],v2 offset:25632
v_mfma_f32_16x16x32_bf16 v[192:195],v[104:107],a[84:87],v[192:195]
v_mfma_f32_16x16x32_bf16 v[192:195],v[108:111],a[92:95],v[192:195]
buffer_load_dwordx4 v24,s[48:51],s83 offen lds
v_mfma_f32_16x16x32_bf16 v[200:203],v[108:111],a[124:127],v[200:203]
v_mfma_f32_16x16x32_bf16 v[200:203],v[104:107],a[116:119],v[200:203]
v_mfma_f32_16x16x32_bf16 v[200:203],v[100:103],a[108:111],v[200:203]
v_mfma_f32_16x16x32_bf16 v[200:203],v[96:99],a[100:103],v[200:203]
v_mfma_f32_16x16x32_bf16 v[208:211],v[96:99],a[64:67],v[208:211]
buffer_load_dwordx4 a[136:139],v6,s[28:31],s87 offen offset:2048 sc1
v_mfma_f32_16x16x32_bf16 v[208:211],v[100:103],a[72:75],v[208:211]
v_mfma_f32_16x16x32_bf16 v[208:211],v[104:107],a[80:83],v[208:211]
v_mfma_f32_16x16x32_bf16 v[208:211],v[108:111],a[88:91],v[208:211]
v_mfma_f32_16x16x32_bf16 v[216:219],v[108:111],a[120:123],v[216:219]
ds_read_b64_tr_b16 v[108:109],v1 offset:30784
ds_read_b64_tr_b16 v[110:111],v1 offset:35904
v_mfma_f32_16x16x32_bf16 v[216:219],v[104:107],a[112:115],v[216:219]
ds_read_b64_tr_b16 v[104:105],v1 offset:20544
v_mfma_f32_16x16x32_bf16 v[216:219],v[100:103],a[104:107],v[216:219]
buffer_load_dwordx4 v25,s[48:51],s83 offen offset:1024 lds
ds_read_b64_tr_b16 v[100:101],v1 offset:10304
v_mfma_f32_16x16x32_bf16 v[216:219],v[96:99],a[96:99],v[216:219]
ds_read_b64_tr_b16 v[96:97],v1 offset:64
v_mfma_f32_16x16x32_bf16 v[224:227],v[112:115],a[68:71],v[224:227]
ds_read_b64_tr_b16 v[98:99],v1 offset:5184
ds_read_b64_tr_b16 v[102:103],v1 offset:15424
v_mfma_f32_16x16x32_bf16 v[224:227],v[116:119],a[76:79],v[224:227]
ds_read_b64_tr_b16 v[106:107],v1 offset:25664
v_mfma_f32_16x16x32_bf16 v[224:227],v[120:123],a[84:87],v[224:227]
v_mfma_f32_16x16x32_bf16 v[224:227],v[124:127],a[92:95],v[224:227]
buffer_load_dwordx4 a[152:155],v6,s[28:31],s88 offen offset:2048 sc1
v_mfma_f32_16x16x32_bf16 v[232:235],v[124:127],a[124:127],v[232:235]
v_mfma_f32_16x16x32_bf16 v[232:235],v[120:123],a[116:119],v[232:235]
v_mfma_f32_16x16x32_bf16 v[232:235],v[116:119],a[108:111],v[232:235]
v_mfma_f32_16x16x32_bf16 v[232:235],v[112:115],a[100:103],v[232:235]
v_mfma_f32_16x16x32_bf16 v[240:243],v[112:115],a[64:67],v[240:243]
v_mfma_f32_16x16x32_bf16 v[240:243],v[116:119],a[72:75],v[240:243]
buffer_load_dwordx4 v26,s[48:51],s83 offen offset:2048 lds
v_mfma_f32_16x16x32_bf16 v[240:243],v[120:123],a[80:83],v[240:243]
v_mfma_f32_16x16x32_bf16 v[240:243],v[124:127],a[88:91],v[240:243]
v_mfma_f32_16x16x32_bf16 v[248:251],v[124:127],a[120:123],v[248:251]
ds_read_b64_tr_b16 v[124:125],v2 offset:30816
ds_read_b64_tr_b16 v[126:127],v2 offset:35936
v_mfma_f32_16x16x32_bf16 v[248:251],v[120:123],a[112:115],v[248:251]
ds_read_b64_tr_b16 v[120:121],v2 offset:20576
v_mfma_f32_16x16x32_bf16 v[248:251],v[116:119],a[104:107],v[248:251]
buffer_load_dwordx4 a[168:171],v6,s[28:31],s89 offen offset:2048 sc1
ds_read_b64_tr_b16 v[116:117],v2 offset:10336
v_mfma_f32_16x16x32_bf16 v[248:251],v[112:115],a[96:99],v[248:251]
s_waitcnt vmcnt(22)
ds_read_b64_tr_b16 v[112:113],v2 offset:96
v_mfma_f32_16x16x32_bf16 a[192:195],v[64:67],v[32:35],a[192:195]
ds_read_b64_tr_b16 v[114:115],v2 offset:5216
ds_read_b64_tr_b16 v[118:119],v2 offset:15456
v_mfma_f32_16x16x32_bf16 a[192:195],v[68:71],v[36:39],a[192:195]
ds_read_b64_tr_b16 v[122:123],v2 offset:25696
v_mfma_f32_16x16x32_bf16 a[192:195],v[72:75],v[40:43],a[192:195]
s_waitcnt vmcnt(17)
v_mfma_f32_16x16x32_bf16 a[192:195],v[76:79],v[44:47],a[192:195]
s_waitcnt vmcnt(14)
v_mfma_f32_16x16x32_bf16 a[196:199],v[76:79],v[180:183],a[196:199]
buffer_load_dwordx4 v27,s[48:51],s83 offen offset:3072 lds
v_mfma_f32_16x16x32_bf16 a[196:199],v[72:75],v[164:167],a[196:199]
s_add_u32 m0,s86,45056
v_mfma_f32_16x16x32_bf16 a[196:199],v[68:71],v[148:151],a[196:199]
v_mfma_f32_16x16x32_bf16 a[196:199],v[64:67],v[132:135],a[196:199]
v_mfma_f32_16x16x32_bf16 a[200:203],v[64:67],v[196:199],a[200:203]
v_mfma_f32_16x16x32_bf16 a[200:203],v[68:71],v[212:215],a[200:203]
buffer_load_dwordx4 a[184:187],v6,s[28:31],s90 offen offset:2048 sc1
v_mfma_f32_16x16x32_bf16 a[200:203],v[72:75],v[228:231],a[200:203]
s_waitcnt vmcnt(14)
v_mfma_f32_16x16x32_bf16 a[200:203],v[76:79],v[244:247],a[200:203]
s_waitcnt vmcnt(13)
v_mfma_f32_16x16x32_bf16 a[204:207],v[76:79],v[188:191],a[204:207]
ds_read_b64_tr_b16 v[76:77],v1 offset:30848
ds_read_b64_tr_b16 v[78:79],v1 offset:35968
v_mfma_f32_16x16x32_bf16 a[204:207],v[72:75],v[172:175],a[204:207]
ds_read_b64_tr_b16 v[72:73],v1 offset:20608
v_mfma_f32_16x16x32_bf16 a[204:207],v[68:71],v[156:159],a[204:207]
ds_read_b64_tr_b16 v[68:69],v1 offset:10368
v_mfma_f32_16x16x32_bf16 a[204:207],v[64:67],v[140:143],a[204:207]
s_waitcnt lgkmcnt(4)
buffer_load_dwordx4 v7,s[48:51],s83 offen lds
ds_read_b64_tr_b16 v[64:65],v1 offset:128
v_mfma_f32_16x16x32_bf16 a[208:211],v[80:83],v[32:35],a[208:211]
s_add_u32 m0,s86,46080
ds_read_b64_tr_b16 v[66:67],v1 offset:5248
ds_read_b64_tr_b16 v[70:71],v1 offset:15488
v_mfma_f32_16x16x32_bf16 a[208:211],v[84:87],v[36:39],a[208:211]
s_mul_i32 s83,s63,20480
ds_read_b64_tr_b16 v[74:75],v1 offset:25728
v_mfma_f32_16x16x32_bf16 a[208:211],v[88:91],v[40:43],a[208:211]
v_mfma_f32_16x16x32_bf16 a[208:211],v[92:95],v[44:47],a[208:211]
v_mfma_f32_16x16x32_bf16 a[212:215],v[92:95],v[180:183],a[212:215]
buffer_load_dwordx4 a[188:191],v28,s[28:31],s90 offen offset:3072 sc1
v_mfma_f32_16x16x32_bf16 a[212:215],v[88:91],v[164:167],a[212:215]
)A"
R"A(v_mfma_f32_16x16x32_bf16 a[212:215],v[84:87],v[148:151],a[212:215]
v_mfma_f32_16x16x32_bf16 a[212:215],v[80:83],v[132:135],a[212:215]
v_mfma_f32_16x16x32_bf16 a[216:219],v[80:83],v[196:199],a[216:219]
v_mfma_f32_16x16x32_bf16 a[216:219],v[84:87],v[212:215],a[216:219]
v_mfma_f32_16x16x32_bf16 a[216:219],v[88:91],v[228:231],a[216:219]
buffer_load_dwordx4 v24,s[48:51],s84 offen lds
v_mfma_f32_16x16x32_bf16 a[216:219],v[92:95],v[244:247],a[216:219]
v_mfma_f32_16x16x32_bf16 a[220:223],v[92:95],v[188:191],a[220:223]
ds_read_b64_tr_b16 v[92:93],v2 offset:30880
ds_read_b64_tr_b16 v[94:95],v2 offset:36000
v_mfma_f32_16x16x32_bf16 a[220:223],v[88:91],v[172:175],a[220:223]
ds_read_b64_tr_b16 v[88:89],v2 offset:20640
v_mfma_f32_16x16x32_bf16 a[220:223],v[84:87],v[156:159],a[220:223]
ds_read_b64_tr_b16 v[84:85],v2 offset:10400
v_mfma_f32_16x16x32_bf16 a[220:223],v[80:83],v[140:143],a[220:223]
ds_read_b64_tr_b16 v[80:81],v2 offset:160
v_mfma_f32_16x16x32_bf16 a[224:227],v[96:99],v[32:35],a[224:227]
buffer_load_dwordx4 v25,s[48:51],s84 offen offset:1024 lds
ds_read_b64_tr_b16 v[82:83],v2 offset:5280
v_mfma_f32_16x16x32_bf16 a[224:227],v[100:103],v[36:39],a[224:227]
ds_read_b64_tr_b16 v[86:87],v2 offset:15520
ds_read_b64_tr_b16 v[90:91],v2 offset:25760
v_mfma_f32_16x16x32_bf16 a[224:227],v[104:107],v[40:43],a[224:227]
v_mfma_f32_16x16x32_bf16 a[224:227],v[108:111],v[44:47],a[224:227]
v_mfma_f32_16x16x32_bf16 a[228:231],v[108:111],v[180:183],a[228:231]
v_mfma_f32_16x16x32_bf16 a[228:231],v[104:107],v[164:167],a[228:231]
buffer_load_dwordx4 a[172:175],v28,s[28:31],s89 offen offset:3072 sc1
v_mfma_f32_16x16x32_bf16 a[228:231],v[100:103],v[148:151],a[228:231]
v_mfma_f32_16x16x32_bf16 a[228:231],v[96:99],v[132:135],a[228:231]
v_mfma_f32_16x16x32_bf16 a[232:235],v[96:99],v[196:199],a[232:235]
v_mfma_f32_16x16x32_bf16 a[232:235],v[100:103],v[212:215],a[232:235]
v_mfma_f32_16x16x32_bf16 a[232:235],v[104:107],v[228:231],a[232:235]
v_mfma_f32_16x16x32_bf16 a[232:235],v[108:111],v[244:247],a[232:235]
buffer_load_dwordx4 v26,s[48:51],s84 offen offset:2048 lds
v_mfma_f32_16x16x32_bf16 a[236:239],v[108:111],v[188:191],a[236:239]
ds_read_b64_tr_b16 v[108:109],v1 offset:30912
ds_read_b64_tr_b16 v[110:111],v1 offset:36032
v_mfma_f32_16x16x32_bf16 a[236:239],v[104:107],v[172:175],a[236:239]
ds_read_b64_tr_b16 v[104:105],v1 offset:20672
v_mfma_f32_16x16x32_bf16 a[236:239],v[100:103],v[156:159],a[236:239]
ds_read_b64_tr_b16 v[100:101],v1 offset:10432
v_mfma_f32_16x16x32_bf16 a[236:239],v[96:99],v[140:143],a[236:239]
ds_read_b64_tr_b16 v[96:97],v1 offset:192
v_mfma_f32_16x16x32_bf16 a[240:243],v[112:115],v[32:35],a[240:243]
ds_read_b64_tr_b16 v[98:99],v1 offset:5312
ds_read_b64_tr_b16 v[102:103],v1 offset:15552
v_mfma_f32_16x16x32_bf16 a[240:243],v[116:119],v[36:39],a[240:243]
buffer_load_dwordx4 v27,s[48:51],s84 offen offset:3072 lds
ds_read_b64_tr_b16 v[106:107],v1 offset:25792
v_mfma_f32_16x16x32_bf16 a[240:243],v[120:123],v[40:43],a[240:243]
s_add_u32 m0,s86,50176
v_add_u32 v1,0xffff6000,v1
v_mfma_f32_16x16x32_bf16 a[240:243],v[124:127],v[44:47],a[240:243]
v_mfma_f32_16x16x32_bf16 a[244:247],v[124:127],v[180:183],a[244:247]
v_mfma_f32_16x16x32_bf16 a[244:247],v[120:123],v[164:167],a[244:247]
v_mfma_f32_16x16x32_bf16 a[244:247],v[116:119],v[148:151],a[244:247]
buffer_load_dwordx4 a[140:143],v28,s[28:31],s87 offen offset:3072 sc1
v_mfma_f32_16x16x32_bf16 a[244:247],v[112:115],v[132:135],a[244:247]
s_add_u32 s87,s64,2
s_and_b32 s87,s87,3
s_lshl_b32 s87,s87,1
s_or_b32 s87,s87,1
s_lshl_b32 s87,s87,16
s_add_u32 s89,s87,8192
s_add_u32 s90,s87,12288
v_mfma_f32_16x16x32_bf16 a[248:251],v[112:115],v[196:199],a[248:251]
v_mfma_f32_16x16x32_bf16 a[248:251],v[116:119],v[212:215],a[248:251]
v_mfma_f32_16x16x32_bf16 a[248:251],v[120:123],v[228:231],a[248:251]
v_mfma_f32_16x16x32_bf16 a[248:251],v[124:127],v[244:247],a[248:251]
buffer_load_dwordx4 a[68:71],v6,s[28:31],s87 offen sc1
v_mfma_f32_16x16x32_bf16 a[252:255],v[124:127],v[188:191],a[252:255]
ds_read_b64_tr_b16 v[124:125],v2 offset:30944
ds_read_b64_tr_b16 v[126:127],v2 offset:36064
v_mfma_f32_16x16x32_bf16 a[252:255],v[120:123],v[172:175],a[252:255]
ds_read_b64_tr_b16 v[120:121],v2 offset:20704
v_mfma_f32_16x16x32_bf16 a[252:255],v[116:119],v[156:159],a[252:255]
ds_read_b64_tr_b16 v[116:117],v2 offset:10464
v_mfma_f32_16x16x32_bf16 a[252:255],v[112:115],v[140:143],a[252:255]
s_waitcnt lgkmcnt(4)
ds_read_b64_tr_b16 v[112:113],v2 offset:224
v_mfma_f32_16x16x32_bf16 a[0:3],v[64:67],v[32:35],a[0:3]
buffer_load_dwordx4 a[156:159],v28,s[28:31],s88 offen offset:3072 sc1
ds_read_b64_tr_b16 v[114:115],v2 offset:5344
v_mfma_f32_16x16x32_bf16 a[0:3],v[68:71],v[36:39],a[0:3]
s_add_u32 s88,s87,4096
ds_read_b64_tr_b16 v[118:119],v2 offset:15584
ds_read_b64_tr_b16 v[122:123],v2 offset:25824
v_mfma_f32_16x16x32_bf16 a[0:3],v[72:75],v[40:43],a[0:3]
v_add_u32 v2,0xffff6000,v2
v_mfma_f32_16x16x32_bf16 a[0:3],v[76:79],v[44:47],a[0:3]
v_mfma_f32_16x16x32_bf16 a[4:7],v[76:79],v[180:183],a[4:7]
v_mfma_f32_16x16x32_bf16 a[4:7],v[72:75],v[164:167],a[4:7]
v_mfma_f32_16x16x32_bf16 a[4:7],v[68:71],v[148:151],a[4:7]
buffer_load_dwordx4 v7,s[48:51],s84 offen lds
v_mfma_f32_16x16x32_bf16 a[4:7],v[64:67],v[132:135],a[4:7]
v_mfma_f32_16x16x32_bf16 a[8:11],v[64:67],v[196:199],a[8:11]
v_mfma_f32_16x16x32_bf16 a[8:11],v[68:71],v[212:215],a[8:11]
v_mfma_f32_16x16x32_bf16 a[8:11],v[72:75],v[228:231],a[8:11]
v_mfma_f32_16x16x32_bf16 a[8:11],v[76:79],v[244:247],a[8:11]
buffer_load_dwordx4 a[76:79],v6,s[28:31],s88 offen sc1
v_mfma_f32_16x16x32_bf16 a[12:15],v[76:79],v[188:191],a[12:15]
ds_read_b64_tr_b16 v[76:77],v1 offset:30720
ds_read_b64_tr_b16 v[78:79],v1 offset:35840
v_mfma_f32_16x16x32_bf16 a[12:15],v[72:75],v[172:175],a[12:15]
ds_read_b64_tr_b16 v[72:73],v1 offset:20480
v_mfma_f32_16x16x32_bf16 a[12:15],v[68:71],v[156:159],a[12:15]
ds_read_b64_tr_b16 v[68:69],v1 offset:10240
v_mfma_f32_16x16x32_bf16 a[12:15],v[64:67],v[140:143],a[12:15]
ds_read_b64_tr_b16 v[64:65],v1
v_mfma_f32_16x16x32_bf16 a[16:19],v[80:83],v[32:35],a[16:19]
buffer_load_dwordx4 a[84:87],v6,s[28:31],s89 offen sc1
ds_read_b64_tr_b16 v[66:67],v1 offset:5120
v_mfma_f32_16x16x32_bf16 a[16:19],v[84:87],v[36:39],a[16:19]
ds_read_b64_tr_b16 v[70:71],v1 offset:15360
ds_read_b64_tr_b16 v[74:75],v1 offset:25600
L_pd23_%=:
s_cbranch_execz L_trtg2S_%=
v_mfma_f32_16x16x32_bf16 a[16:19],v[88:91],v[40:43],a[16:19]
v_add_u32 v1,0xffff6000,v1
v_mfma_f32_16x16x32_bf16 a[16:19],v[92:95],v[44:47],a[16:19]
v_mfma_f32_16x16x32_bf16 a[20:23],v[92:95],v[180:183],a[20:23]
v_mfma_f32_16x16x32_bf16 a[20:23],v[88:91],v[164:167],a[20:23]
buffer_load_dwordx4 a[92:95],v6,s[28:31],s90 offen sc1
v_mfma_f32_16x16x32_bf16 a[20:23],v[84:87],v[148:151],a[20:23]
v_mfma_f32_16x16x32_bf16 a[20:23],v[80:83],v[132:135],a[20:23]
v_mfma_f32_16x16x32_bf16 a[24:27],v[80:83],v[196:199],a[24:27]
v_mfma_f32_16x16x32_bf16 a[24:27],v[84:87],v[212:215],a[24:27]
v_mfma_f32_16x16x32_bf16 a[24:27],v[88:91],v[228:231],a[24:27]
buffer_load_dwordx4 a[124:127],v6,s[28:31],s90 offen offset:1024 sc1
v_mfma_f32_16x16x32_bf16 a[24:27],v[92:95],v[244:247],a[24:27]
v_mfma_f32_16x16x32_bf16 a[28:31],v[92:95],v[188:191],a[28:31]
ds_read_b64_tr_b16 v[92:93],v2 offset:30752
ds_read_b64_tr_b16 v[94:95],v2 offset:35872
v_mfma_f32_16x16x32_bf16 a[28:31],v[88:91],v[172:175],a[28:31]
ds_read_b64_tr_b16 v[88:89],v2 offset:20512
v_mfma_f32_16x16x32_bf16 a[28:31],v[84:87],v[156:159],a[28:31]
ds_read_b64_tr_b16 v[84:85],v2 offset:10272
v_mfma_f32_16x16x32_bf16 a[28:31],v[80:83],v[140:143],a[28:31]
buffer_load_dwordx4 a[116:119],v6,s[28:31],s89 offen offset:1024 sc1
ds_read_b64_tr_b16 v[80:81],v2 offset:32
v_mfma_f32_16x16x32_bf16 v[128:131],v[96:99],v[32:35],v[128:131]
ds_read_b64_tr_b16 v[82:83],v2 offset:5152
ds_read_b64_tr_b16 v[86:87],v2 offset:15392
v_mfma_f32_16x16x32_bf16 v[128:131],v[100:103],v[36:39],v[128:131]
ds_read_b64_tr_b16 v[90:91],v2 offset:25632
v_add_u32 v2,0xffff6000,v2
v_mfma_f32_16x16x32_bf16 v[128:131],v[104:107],v[40:43],v[128:131]
v_mfma_f32_16x16x32_bf16 v[128:131],v[108:111],v[44:47],v[128:131]
v_mfma_f32_16x16x32_bf16 v[136:139],v[108:111],v[180:183],v[136:139]
buffer_load_dwordx4 a[108:111],v6,s[28:31],s88 offen offset:1024 sc1
v_mfma_f32_16x16x32_bf16 v[136:139],v[104:107],v[164:167],v[136:139]
v_mfma_f32_16x16x32_bf16 v[136:139],v[100:103],v[148:151],v[136:139]
v_mfma_f32_16x16x32_bf16 v[136:139],v[96:99],v[132:135],v[136:139]
v_mfma_f32_16x16x32_bf16 v[144:147],v[96:99],v[196:199],v[144:147]
v_mfma_f32_16x16x32_bf16 v[144:147],v[100:103],v[212:215],v[144:147]
buffer_load_dwordx4 a[100:103],v6,s[28:31],s87 offen offset:1024 sc1
v_mfma_f32_16x16x32_bf16 v[144:147],v[104:107],v[228:231],v[144:147]
v_mfma_f32_16x16x32_bf16 v[144:147],v[108:111],v[244:247],v[144:147]
v_mfma_f32_16x16x32_bf16 v[152:155],v[108:111],v[188:191],v[152:155]
v_mfma_f32_16x16x32_bf16 v[152:155],v[104:107],v[172:175],v[152:155]
v_mfma_f32_16x16x32_bf16 v[152:155],v[100:103],v[156:159],v[152:155]
buffer_load_dwordx4 a[64:67],v6,s[28:31],s87 offen offset:2048 sc1
v_mfma_f32_16x16x32_bf16 v[152:155],v[96:99],v[140:143],v[152:155]
s_waitcnt vmcnt(8)
s_barrier
s_waitcnt lgkmcnt(0)
v_mfma_f32_16x16x32_bf16 v[160:163],v[112:115],v[32:35],v[160:163]
ds_read_b64_tr_b16 v[96:97],v1
ds_read_b64_tr_b16 v[98:99],v1 offset:5120
v_mfma_f32_16x16x32_bf16 v[160:163],v[116:119],v[36:39],v[160:163]
ds_read_b64_tr_b16 v[100:101],v1 offset:10240
v_mfma_f32_16x16x32_bf16 v[160:163],v[120:123],v[40:43],v[160:163]
buffer_load_dwordx4 a[72:75],v6,s[28:31],s88 offen offset:2048 sc1
ds_read_b64_tr_b16 v[102:103],v1 offset:15360
v_mfma_f32_16x16x32_bf16 v[160:163],v[124:127],v[44:47],v[160:163]
ds_read_b64_tr_b16 v[104:105],v1 offset:20480
v_mfma_f32_16x16x32_bf16 v[168:171],v[124:127],v[180:183],v[168:171]
ds_read_b64_tr_b16 v[106:107],v1 offset:25600
ds_read_b64_tr_b16 v[108:109],v1 offset:30720
v_mfma_f32_16x16x32_bf16 v[168:171],v[120:123],v[164:167],v[168:171]
ds_read_b64_tr_b16 v[110:111],v1 offset:35840
v_add_u32 v1,0xa000,v1
v_mfma_f32_16x16x32_bf16 v[168:171],v[116:119],v[148:151],v[168:171]
v_mfma_f32_16x16x32_bf16 v[168:171],v[112:115],v[132:135],v[168:171]
buffer_load_dwordx4 a[80:83],v6,s[28:31],s89 offen offset:2048 sc1
v_mfma_f32_16x16x32_bf16 v[176:179],v[112:115],v[196:199],v[176:179]
v_mfma_f32_16x16x32_bf16 v[176:179],v[116:119],v[212:215],v[176:179]
v_mfma_f32_16x16x32_bf16 v[176:179],v[120:123],v[228:231],v[176:179]
v_mfma_f32_16x16x32_bf16 v[176:179],v[124:127],v[244:247],v[176:179]
v_mfma_f32_16x16x32_bf16 v[184:187],v[124:127],v[188:191],v[184:187]
buffer_load_dwordx4 a[88:91],v6,s[28:31],s90 offen offset:2048 sc1
ds_read_b64_tr_b16 v[124:125],v2 offset:30752
v_mfma_f32_16x16x32_bf16 v[184:187],v[120:123],v[172:175],v[184:187]
ds_read_b64_tr_b16 v[120:121],v2 offset:20512
ds_read_b64_tr_b16 v[122:123],v2 offset:25632
v_mfma_f32_16x16x32_bf16 v[184:187],v[116:119],v[156:159],v[184:187]
ds_read_b64_tr_b16 v[116:117],v2 offset:10272
v_mfma_f32_16x16x32_bf16 v[184:187],v[112:115],v[140:143],v[184:187]
ds_read_b64_tr_b16 v[112:113],v2 offset:32
v_mfma_f32_16x16x32_bf16 a[192:195],v[64:67],a[128:131],a[192:195]
ds_read_b64_tr_b16 v[114:115],v2 offset:5152
v_mfma_f32_16x16x32_bf16 a[192:195],v[68:71],a[144:147],a[192:195]
buffer_load_dwordx4 a[120:123],v28,s[28:31],s90 offen offset:3072 sc1
ds_read_b64_tr_b16 v[118:119],v2 offset:15392
v_mfma_f32_16x16x32_bf16 a[192:195],v[72:75],a[160:163],a[192:195]
ds_read_b64_tr_b16 v[126:127],v2 offset:35872
v_add_u32 v2,0xa000,v2
v_mfma_f32_16x16x32_bf16 a[192:195],v[76:79],a[176:179],a[192:195]
v_mfma_f32_16x16x32_bf16 a[196:199],v[76:79],a[180:183],a[196:199]
v_mfma_f32_16x16x32_bf16 a[196:199],v[72:75],a[164:167],a[196:199]
v_mfma_f32_16x16x32_bf16 a[196:199],v[68:71],a[148:151],a[196:199]
buffer_load_dwordx4 a[112:115],v28,s[28:31],s89 offen offset:3072 sc1
v_mfma_f32_16x16x32_bf16 a[196:199],v[64:67],a[132:135],a[196:199]
v_mfma_f32_16x16x32_bf16 a[200:203],v[64:67],a[136:139],a[200:203]
v_mfma_f32_16x16x32_bf16 a[200:203],v[68:71],a[152:155],a[200:203]
v_mfma_f32_16x16x32_bf16 a[200:203],v[72:75],a[168:171],a[200:203]
)A"
R"A(v_mfma_f32_16x16x32_bf16 a[200:203],v[76:79],a[184:187],a[200:203]
buffer_load_dwordx4 a[104:107],v28,s[28:31],s88 offen offset:3072 sc1
v_mfma_f32_16x16x32_bf16 a[204:207],v[76:79],a[188:191],a[204:207]
ds_read_b64_tr_b16 v[76:77],v1 offset:30784
ds_read_b64_tr_b16 v[78:79],v1 offset:35904
v_mfma_f32_16x16x32_bf16 a[204:207],v[72:75],a[172:175],a[204:207]
ds_read_b64_tr_b16 v[72:73],v1 offset:20544
v_mfma_f32_16x16x32_bf16 a[204:207],v[68:71],a[156:159],a[204:207]
ds_read_b64_tr_b16 v[68:69],v1 offset:10304
v_mfma_f32_16x16x32_bf16 a[204:207],v[64:67],a[140:143],a[204:207]
ds_read_b64_tr_b16 v[64:65],v1 offset:64
v_mfma_f32_16x16x32_bf16 a[208:211],v[80:83],a[128:131],a[208:211]
buffer_load_dwordx4 a[96:99],v28,s[28:31],s87 offen offset:3072 sc1
ds_read_b64_tr_b16 v[66:67],v1 offset:5184
v_mfma_f32_16x16x32_bf16 a[208:211],v[84:87],a[144:147],a[208:211]
s_mul_i32 s87,s60,81920
ds_read_b64_tr_b16 v[70:71],v1 offset:15424
ds_read_b64_tr_b16 v[74:75],v1 offset:25664
v_mfma_f32_16x16x32_bf16 a[208:211],v[88:91],a[160:163],a[208:211]
s_add_u32 s87,s87,s66
s_add_u32 s83,s83,s87
v_add_u32 v1,0xffff6000,v1
v_mfma_f32_16x16x32_bf16 a[208:211],v[92:95],a[176:179],a[208:211]
s_lshl_b32 s66,s63,6
s_add_u32 s87,s87,s66
s_add_u32 s88,s87,20480
v_mfma_f32_16x16x32_bf16 a[212:215],v[92:95],a[180:183],a[212:215]
s_add_u32 s89,s87,40960
s_add_u32 s90,s87,61440
s_add_u32 s66,s77,4096
v_mfma_f32_16x16x32_bf16 a[212:215],v[88:91],a[164:167],a[212:215]
v_mfma_f32_16x16x32_bf16 a[212:215],v[84:87],a[148:151],a[212:215]
v_mfma_f32_16x16x32_bf16 a[212:215],v[80:83],a[132:135],a[212:215]
v_mfma_f32_16x16x32_bf16 a[216:219],v[80:83],a[136:139],a[216:219]
v_mfma_f32_16x16x32_bf16 a[216:219],v[84:87],a[152:155],a[216:219]
v_mfma_f32_16x16x32_bf16 a[216:219],v[88:91],a[168:171],a[216:219]
v_mfma_f32_16x16x32_bf16 a[216:219],v[92:95],a[184:187],a[216:219]
v_mfma_f32_16x16x32_bf16 a[220:223],v[92:95],a[188:191],a[220:223]
ds_read_b64_tr_b16 v[92:93],v2 offset:30816
ds_read_b64_tr_b16 v[94:95],v2 offset:35936
v_mfma_f32_16x16x32_bf16 a[220:223],v[88:91],a[172:175],a[220:223]
ds_read_b64_tr_b16 v[88:89],v2 offset:20576
v_mfma_f32_16x16x32_bf16 a[220:223],v[84:87],a[156:159],a[220:223]
ds_read_b64_tr_b16 v[84:85],v2 offset:10336
v_mfma_f32_16x16x32_bf16 a[220:223],v[80:83],a[140:143],a[220:223]
s_waitcnt lgkmcnt(4)
ds_read_b64_tr_b16 v[80:81],v2 offset:96
v_mfma_f32_16x16x32_bf16 a[192:195],v[96:99],a[68:71],a[192:195]
s_waitcnt vmcnt(14)
ds_read_b64_tr_b16 v[82:83],v2 offset:5216
ds_read_b64_tr_b16 v[86:87],v2 offset:15456
v_mfma_f32_16x16x32_bf16 a[192:195],v[100:103],a[76:79],a[192:195]
s_waitcnt vmcnt(13)
ds_read_b64_tr_b16 v[90:91],v2 offset:25696
v_add_u32 v2,0xffff6000,v2
v_mfma_f32_16x16x32_bf16 a[192:195],v[104:107],a[84:87],a[192:195]
s_waitcnt vmcnt(12)
v_mfma_f32_16x16x32_bf16 a[192:195],v[108:111],a[92:95],a[192:195]
s_waitcnt vmcnt(11)
v_mfma_f32_16x16x32_bf16 a[196:199],v[108:111],a[124:127],a[196:199]
s_waitcnt vmcnt(10)
v_mfma_f32_16x16x32_bf16 a[196:199],v[104:107],a[116:119],a[196:199]
s_waitcnt vmcnt(9)
v_mfma_f32_16x16x32_bf16 a[196:199],v[100:103],a[108:111],a[196:199]
s_waitcnt vmcnt(8)
v_mfma_f32_16x16x32_bf16 a[196:199],v[96:99],a[100:103],a[196:199]
s_waitcnt vmcnt(7)
v_mfma_f32_16x16x32_bf16 a[200:203],v[96:99],a[64:67],a[200:203]
s_waitcnt vmcnt(6)
v_mfma_f32_16x16x32_bf16 a[200:203],v[100:103],a[72:75],a[200:203]
s_waitcnt vmcnt(5)
v_mfma_f32_16x16x32_bf16 a[200:203],v[104:107],a[80:83],a[200:203]
s_waitcnt vmcnt(4)
v_mfma_f32_16x16x32_bf16 a[200:203],v[108:111],a[88:91],a[200:203]
s_waitcnt vmcnt(3)
v_accvgpr_read_b32 v8,a192
v_accvgpr_read_b32 v9,a193
v_mfma_f32_16x16x32_bf16 a[204:207],v[108:111],a[120:123],a[204:207]
s_waitcnt vmcnt(2)
v_accvgpr_read_b32 v10,a194
v_accvgpr_read_b32 v11,a195
v_mfma_f32_16x16x32_bf16 a[204:207],v[104:107],a[112:115],a[204:207]
s_waitcnt vmcnt(1)
v_cvt_pk_bf16_f32 v8,v8,v9
v_cvt_pk_bf16_f32 v9,v10,v11
v_mfma_f32_16x16x32_bf16 a[204:207],v[100:103],a[104:107],a[204:207]
s_waitcnt vmcnt(0)
v_accvgpr_read_b32 v16,a196
v_accvgpr_read_b32 v17,a197
v_mfma_f32_16x16x32_bf16 a[204:207],v[96:99],a[96:99],a[204:207]
v_accvgpr_read_b32 v18,a198
v_accvgpr_read_b32 v19,a199
v_mfma_f32_16x16x32_bf16 a[208:211],v[112:115],a[68:71],a[208:211]
v_cvt_pk_bf16_f32 v16,v16,v17
v_cvt_pk_bf16_f32 v17,v18,v19
v_mfma_f32_16x16x32_bf16 a[208:211],v[116:119],a[76:79],a[208:211]
ds_read_b64_tr_b16 v[96:97],v1 offset:64
ds_read_b64_tr_b16 v[98:99],v1 offset:5184
v_mfma_f32_16x16x32_bf16 a[208:211],v[120:123],a[84:87],a[208:211]
ds_read_b64_tr_b16 v[100:101],v1 offset:10304
v_mfma_f32_16x16x32_bf16 a[208:211],v[124:127],a[92:95],a[208:211]
ds_read_b64_tr_b16 v[102:103],v1 offset:15424
v_mfma_f32_16x16x32_bf16 a[212:215],v[124:127],a[124:127],a[212:215]
ds_read_b64_tr_b16 v[104:105],v1 offset:20544
v_mfma_f32_16x16x32_bf16 a[212:215],v[120:123],a[116:119],a[212:215]
ds_read_b64_tr_b16 v[106:107],v1 offset:25664
ds_read_b64_tr_b16 v[108:109],v1 offset:30784
v_mfma_f32_16x16x32_bf16 a[212:215],v[116:119],a[108:111],a[212:215]
ds_read_b64_tr_b16 v[110:111],v1 offset:35904
v_add_u32 v1,0xa000,v1
v_mfma_f32_16x16x32_bf16 a[212:215],v[112:115],a[100:103],a[212:215]
v_accvgpr_read_b32 v12,a208
v_accvgpr_read_b32 v13,a209
v_mfma_f32_16x16x32_bf16 a[216:219],v[112:115],a[64:67],a[216:219]
v_accvgpr_read_b32 v14,a210
v_accvgpr_read_b32 v15,a211
v_mfma_f32_16x16x32_bf16 a[216:219],v[116:119],a[72:75],a[216:219]
v_cvt_pk_bf16_f32 v10,v12,v13
v_cvt_pk_bf16_f32 v11,v14,v15
v_mfma_f32_16x16x32_bf16 a[216:219],v[120:123],a[80:83],a[216:219]
buffer_store_dwordx4 v[8:11],v6,s[36:39],s74 offen sc1
v_mfma_f32_16x16x32_bf16 a[216:219],v[124:127],a[88:91],a[216:219]
v_accvgpr_read_b32 v20,a212
v_accvgpr_read_b32 v21,a213
v_mfma_f32_16x16x32_bf16 a[220:223],v[124:127],a[120:123],a[220:223]
v_accvgpr_read_b32 v22,a214
v_accvgpr_read_b32 v23,a215
v_mfma_f32_16x16x32_bf16 a[220:223],v[120:123],a[112:115],a[220:223]
v_cvt_pk_bf16_f32 v18,v20,v21
v_cvt_pk_bf16_f32 v19,v22,v23
v_mfma_f32_16x16x32_bf16 a[220:223],v[116:119],a[104:107],a[220:223]
v_accvgpr_read_b32 v8,a200
v_accvgpr_read_b32 v9,a201
v_mfma_f32_16x16x32_bf16 a[220:223],v[112:115],a[96:99],a[220:223]
v_accvgpr_read_b32 v10,a202
v_accvgpr_read_b32 v11,a203
v_mfma_f32_16x16x32_bf16 a[224:227],v[64:67],a[128:131],a[224:227]
v_accvgpr_read_b32 v12,a216
v_accvgpr_read_b32 v13,a217
v_mfma_f32_16x16x32_bf16 a[224:227],v[68:71],a[144:147],a[224:227]
v_accvgpr_read_b32 v14,a218
v_accvgpr_read_b32 v15,a219
v_mfma_f32_16x16x32_bf16 a[224:227],v[72:75],a[160:163],a[224:227]
v_cvt_pk_bf16_f32 v8,v8,v9
v_cvt_pk_bf16_f32 v9,v10,v11
v_mfma_f32_16x16x32_bf16 a[224:227],v[76:79],a[176:179],a[224:227]
v_cvt_pk_bf16_f32 v10,v12,v13
v_cvt_pk_bf16_f32 v11,v14,v15
v_mfma_f32_16x16x32_bf16 a[228:231],v[76:79],a[180:183],a[228:231]
ds_read_b64_tr_b16 v[112:113],v2 offset:96
ds_read_b64_tr_b16 v[114:115],v2 offset:5216
v_mfma_f32_16x16x32_bf16 a[228:231],v[72:75],a[164:167],a[228:231]
buffer_store_dwordx4 v[16:19],v6,s[36:39],s74 offen offset:1024 sc1
v_mfma_f32_16x16x32_bf16 a[228:231],v[68:71],a[148:151],a[228:231]
ds_read_b64_tr_b16 v[116:117],v2 offset:10336
ds_read_b64_tr_b16 v[118:119],v2 offset:15456
v_mfma_f32_16x16x32_bf16 a[228:231],v[64:67],a[132:135],a[228:231]
ds_read_b64_tr_b16 v[120:121],v2 offset:20576
v_accvgpr_read_b32 v16,a204
v_mfma_f32_16x16x32_bf16 a[232:235],v[64:67],a[136:139],a[232:235]
ds_read_b64_tr_b16 v[122:123],v2 offset:25696
v_accvgpr_read_b32 v17,a205
v_mfma_f32_16x16x32_bf16 a[232:235],v[68:71],a[152:155],a[232:235]
ds_read_b64_tr_b16 v[124:125],v2 offset:30816
v_accvgpr_read_b32 v18,a206
v_mfma_f32_16x16x32_bf16 a[232:235],v[72:75],a[168:171],a[232:235]
ds_read_b64_tr_b16 v[126:127],v2 offset:35936
v_accvgpr_read_b32 v19,a207
v_mfma_f32_16x16x32_bf16 a[232:235],v[76:79],a[184:187],a[232:235]
v_accvgpr_read_b32 v20,a220
v_accvgpr_read_b32 v21,a221
v_mfma_f32_16x16x32_bf16 a[236:239],v[76:79],a[188:191],a[236:239]
v_accvgpr_read_b32 v22,a222
v_accvgpr_read_b32 v23,a223
v_mfma_f32_16x16x32_bf16 a[236:239],v[72:75],a[172:175],a[236:239]
v_cvt_pk_bf16_f32 v16,v16,v17
v_cvt_pk_bf16_f32 v17,v18,v19
v_mfma_f32_16x16x32_bf16 a[236:239],v[68:71],a[156:159],a[236:239]
v_cvt_pk_bf16_f32 v18,v20,v21
v_cvt_pk_bf16_f32 v19,v22,v23
v_mfma_f32_16x16x32_bf16 a[236:239],v[64:67],a[140:143],a[236:239]
s_waitcnt lgkmcnt(3)
ds_read_b64_tr_b16 v[64:65],v1 offset:128
ds_read_b64_tr_b16 v[66:67],v1 offset:5248
v_mfma_f32_16x16x32_bf16 a[240:243],v[80:83],a[128:131],a[240:243]
buffer_store_dwordx4 v[8:11],v6,s[36:39],s74 offen offset:2048 sc1
v_mfma_f32_16x16x32_bf16 a[240:243],v[84:87],a[144:147],a[240:243]
ds_read_b64_tr_b16 v[68:69],v1 offset:10368
ds_read_b64_tr_b16 v[70:71],v1 offset:15488
v_mfma_f32_16x16x32_bf16 a[240:243],v[88:91],a[160:163],a[240:243]
ds_read_b64_tr_b16 v[72:73],v1 offset:20608
v_add_u32 v2,0xa000,v2
v_mfma_f32_16x16x32_bf16 a[240:243],v[92:95],a[176:179],a[240:243]
ds_read_b64_tr_b16 v[74:75],v1 offset:25728
v_mfma_f32_16x16x32_bf16 a[244:247],v[92:95],a[180:183],a[244:247]
ds_read_b64_tr_b16 v[76:77],v1 offset:30848
v_mfma_f32_16x16x32_bf16 a[244:247],v[88:91],a[164:167],a[244:247]
ds_read_b64_tr_b16 v[78:79],v1 offset:35968
v_add_u32 v1,0xffff6000,v1
v_mfma_f32_16x16x32_bf16 a[244:247],v[84:87],a[148:151],a[244:247]
v_mfma_f32_16x16x32_bf16 a[244:247],v[80:83],a[132:135],a[244:247]
v_mfma_f32_16x16x32_bf16 a[248:251],v[80:83],a[136:139],a[248:251]
v_mfma_f32_16x16x32_bf16 a[248:251],v[84:87],a[152:155],a[248:251]
v_mfma_f32_16x16x32_bf16 a[248:251],v[88:91],a[168:171],a[248:251]
v_mfma_f32_16x16x32_bf16 a[248:251],v[92:95],a[184:187],a[248:251]
buffer_store_dwordx4 v[16:19],v28,s[36:39],s74 offen offset:3072 sc1
v_mfma_f32_16x16x32_bf16 a[252:255],v[92:95],a[188:191],a[252:255]
ds_read_b64_tr_b16 v[92:93],v2 offset:30880
ds_read_b64_tr_b16 v[94:95],v2 offset:36000
v_mfma_f32_16x16x32_bf16 a[252:255],v[88:91],a[172:175],a[252:255]
ds_read_b64_tr_b16 v[88:89],v2 offset:20640
v_mfma_f32_16x16x32_bf16 a[252:255],v[84:87],a[156:159],a[252:255]
ds_read_b64_tr_b16 v[84:85],v2 offset:10400
v_mfma_f32_16x16x32_bf16 a[252:255],v[80:83],a[140:143],a[252:255]
ds_read_b64_tr_b16 v[80:81],v2 offset:160
v_mfma_f32_16x16x32_bf16 a[224:227],v[96:99],a[68:71],a[224:227]
ds_read_b64_tr_b16 v[82:83],v2 offset:5280
ds_read_b64_tr_b16 v[86:87],v2 offset:15520
v_mfma_f32_16x16x32_bf16 a[224:227],v[100:103],a[76:79],a[224:227]
ds_read_b64_tr_b16 v[90:91],v2 offset:25760
v_add_u32 v2,0xffff6000,v2
v_mfma_f32_16x16x32_bf16 a[224:227],v[104:107],a[84:87],a[224:227]
v_mfma_f32_16x16x32_bf16 a[224:227],v[108:111],a[92:95],a[224:227]
v_mfma_f32_16x16x32_bf16 a[228:231],v[108:111],a[124:127],a[228:231]
v_mfma_f32_16x16x32_bf16 a[228:231],v[104:107],a[116:119],a[228:231]
v_mfma_f32_16x16x32_bf16 a[228:231],v[100:103],a[108:111],a[228:231]
v_mfma_f32_16x16x32_bf16 a[228:231],v[96:99],a[100:103],a[228:231]
v_mfma_f32_16x16x32_bf16 a[232:235],v[96:99],a[64:67],a[232:235]
v_mfma_f32_16x16x32_bf16 a[232:235],v[100:103],a[72:75],a[232:235]
v_mfma_f32_16x16x32_bf16 a[232:235],v[104:107],a[80:83],a[232:235]
v_mfma_f32_16x16x32_bf16 a[232:235],v[108:111],a[88:91],a[232:235]
v_accvgpr_read_b32 v8,a224
v_accvgpr_read_b32 v9,a225
v_mfma_f32_16x16x32_bf16 a[236:239],v[108:111],a[120:123],a[236:239]
v_accvgpr_read_b32 v10,a226
v_accvgpr_read_b32 v11,a227
v_mfma_f32_16x16x32_bf16 a[236:239],v[104:107],a[112:115],a[236:239]
v_cvt_pk_bf16_f32 v8,v8,v9
v_cvt_pk_bf16_f32 v9,v10,v11
v_mfma_f32_16x16x32_bf16 a[236:239],v[100:103],a[104:107],a[236:239]
v_accvgpr_read_b32 v16,a228
v_accvgpr_read_b32 v17,a229
v_mfma_f32_16x16x32_bf16 a[236:239],v[96:99],a[96:99],a[236:239]
v_accvgpr_read_b32 v18,a230
v_accvgpr_read_b32 v19,a231
v_mfma_f32_16x16x32_bf16 a[240:243],v[112:115],a[68:71],a[240:243]
v_cvt_pk_bf16_f32 v16,v16,v17
v_cvt_pk_bf16_f32 v17,v18,v19
v_mfma_f32_16x16x32_bf16 a[240:243],v[116:119],a[76:79],a[240:243]
s_waitcnt lgkmcnt(0)
ds_read_b64_tr_b16 v[96:97],v1 offset:128
ds_read_b64_tr_b16 v[98:99],v1 offset:5248
v_mfma_f32_16x16x32_bf16 a[240:243],v[120:123],a[84:87],a[240:243]
)A"
R"A(ds_read_b64_tr_b16 v[100:101],v1 offset:10368
v_mfma_f32_16x16x32_bf16 a[240:243],v[124:127],a[92:95],a[240:243]
ds_read_b64_tr_b16 v[102:103],v1 offset:15488
v_mfma_f32_16x16x32_bf16 a[244:247],v[124:127],a[124:127],a[244:247]
ds_read_b64_tr_b16 v[104:105],v1 offset:20608
v_mfma_f32_16x16x32_bf16 a[244:247],v[120:123],a[116:119],a[244:247]
ds_read_b64_tr_b16 v[106:107],v1 offset:25728
ds_read_b64_tr_b16 v[108:109],v1 offset:30848
v_mfma_f32_16x16x32_bf16 a[244:247],v[116:119],a[108:111],a[244:247]
ds_read_b64_tr_b16 v[110:111],v1 offset:35968
v_add_u32 v1,0xa000,v1
v_mfma_f32_16x16x32_bf16 a[244:247],v[112:115],a[100:103],a[244:247]
v_accvgpr_read_b32 v12,a240
v_accvgpr_read_b32 v13,a241
v_mfma_f32_16x16x32_bf16 a[248:251],v[112:115],a[64:67],a[248:251]
v_accvgpr_read_b32 v14,a242
v_accvgpr_read_b32 v15,a243
v_mfma_f32_16x16x32_bf16 a[248:251],v[116:119],a[72:75],a[248:251]
v_cvt_pk_bf16_f32 v10,v12,v13
v_cvt_pk_bf16_f32 v11,v14,v15
v_mfma_f32_16x16x32_bf16 a[248:251],v[120:123],a[80:83],a[248:251]
buffer_store_dwordx4 v[8:11],v6,s[36:39],s75 offen sc1
v_mfma_f32_16x16x32_bf16 a[248:251],v[124:127],a[88:91],a[248:251]
v_accvgpr_read_b32 v20,a244
v_accvgpr_read_b32 v21,a245
v_mfma_f32_16x16x32_bf16 a[252:255],v[124:127],a[120:123],a[252:255]
v_accvgpr_read_b32 v22,a246
v_accvgpr_read_b32 v23,a247
v_mfma_f32_16x16x32_bf16 a[252:255],v[120:123],a[112:115],a[252:255]
v_cvt_pk_bf16_f32 v18,v20,v21
v_cvt_pk_bf16_f32 v19,v22,v23
v_mfma_f32_16x16x32_bf16 a[252:255],v[116:119],a[104:107],a[252:255]
v_accvgpr_read_b32 v8,a232
v_accvgpr_read_b32 v9,a233
v_mfma_f32_16x16x32_bf16 a[252:255],v[112:115],a[96:99],a[252:255]
v_accvgpr_read_b32 v10,a234
v_accvgpr_read_b32 v11,a235
v_mfma_f32_16x16x32_bf16 a[0:3],v[64:67],a[128:131],a[0:3]
v_accvgpr_read_b32 v12,a248
v_accvgpr_read_b32 v13,a249
v_mfma_f32_16x16x32_bf16 a[0:3],v[68:71],a[144:147],a[0:3]
v_accvgpr_read_b32 v14,a250
v_accvgpr_read_b32 v15,a251
v_mfma_f32_16x16x32_bf16 a[0:3],v[72:75],a[160:163],a[0:3]
v_cvt_pk_bf16_f32 v8,v8,v9
v_cvt_pk_bf16_f32 v9,v10,v11
v_mfma_f32_16x16x32_bf16 a[0:3],v[76:79],a[176:179],a[0:3]
v_cvt_pk_bf16_f32 v10,v12,v13
v_cvt_pk_bf16_f32 v11,v14,v15
v_mfma_f32_16x16x32_bf16 a[4:7],v[76:79],a[180:183],a[4:7]
ds_read_b64_tr_b16 v[112:113],v2 offset:160
ds_read_b64_tr_b16 v[114:115],v2 offset:5280
v_mfma_f32_16x16x32_bf16 a[4:7],v[72:75],a[164:167],a[4:7]
buffer_store_dwordx4 v[16:19],v6,s[36:39],s75 offen offset:1024 sc1
v_mfma_f32_16x16x32_bf16 a[4:7],v[68:71],a[148:151],a[4:7]
ds_read_b64_tr_b16 v[116:117],v2 offset:10400
ds_read_b64_tr_b16 v[118:119],v2 offset:15520
v_mfma_f32_16x16x32_bf16 a[4:7],v[64:67],a[132:135],a[4:7]
ds_read_b64_tr_b16 v[120:121],v2 offset:20640
v_accvgpr_read_b32 v16,a236
v_mfma_f32_16x16x32_bf16 a[8:11],v[64:67],a[136:139],a[8:11]
ds_read_b64_tr_b16 v[122:123],v2 offset:25760
v_accvgpr_read_b32 v17,a237
v_mfma_f32_16x16x32_bf16 a[8:11],v[68:71],a[152:155],a[8:11]
ds_read_b64_tr_b16 v[124:125],v2 offset:30880
v_accvgpr_read_b32 v18,a238
v_mfma_f32_16x16x32_bf16 a[8:11],v[72:75],a[168:171],a[8:11]
ds_read_b64_tr_b16 v[126:127],v2 offset:36000
v_accvgpr_read_b32 v19,a239
v_mfma_f32_16x16x32_bf16 a[8:11],v[76:79],a[184:187],a[8:11]
v_accvgpr_read_b32 v20,a252
v_accvgpr_read_b32 v21,a253
v_mfma_f32_16x16x32_bf16 a[12:15],v[76:79],a[188:191],a[12:15]
v_accvgpr_read_b32 v22,a254
v_accvgpr_read_b32 v23,a255
v_mfma_f32_16x16x32_bf16 a[12:15],v[72:75],a[172:175],a[12:15]
v_cvt_pk_bf16_f32 v16,v16,v17
v_cvt_pk_bf16_f32 v17,v18,v19
v_mfma_f32_16x16x32_bf16 a[12:15],v[68:71],a[156:159],a[12:15]
v_cvt_pk_bf16_f32 v18,v20,v21
v_cvt_pk_bf16_f32 v19,v22,v23
v_mfma_f32_16x16x32_bf16 a[12:15],v[64:67],a[140:143],a[12:15]
ds_read_b64_tr_b16 v[64:65],v1 offset:192
ds_read_b64_tr_b16 v[66:67],v1 offset:5312
v_mfma_f32_16x16x32_bf16 a[16:19],v[80:83],a[128:131],a[16:19]
buffer_store_dwordx4 v[8:11],v6,s[36:39],s75 offen offset:2048 sc1
v_mfma_f32_16x16x32_bf16 a[16:19],v[84:87],a[144:147],a[16:19]
ds_read_b64_tr_b16 v[68:69],v1 offset:10432
ds_read_b64_tr_b16 v[70:71],v1 offset:15552
v_mfma_f32_16x16x32_bf16 a[16:19],v[88:91],a[160:163],a[16:19]
ds_read_b64_tr_b16 v[72:73],v1 offset:20672
v_add_u32 v2,0xa000,v2
v_mfma_f32_16x16x32_bf16 a[16:19],v[92:95],a[176:179],a[16:19]
ds_read_b64_tr_b16 v[74:75],v1 offset:25792
v_mfma_f32_16x16x32_bf16 a[20:23],v[92:95],a[180:183],a[20:23]
ds_read_b64_tr_b16 v[76:77],v1 offset:30912
v_mfma_f32_16x16x32_bf16 a[20:23],v[88:91],a[164:167],a[20:23]
ds_read_b64_tr_b16 v[78:79],v1 offset:36032
v_add_u32 v1,0xffff6000,v1
v_mfma_f32_16x16x32_bf16 a[20:23],v[84:87],a[148:151],a[20:23]
v_mfma_f32_16x16x32_bf16 a[20:23],v[80:83],a[132:135],a[20:23]
v_mfma_f32_16x16x32_bf16 a[24:27],v[80:83],a[136:139],a[24:27]
v_mfma_f32_16x16x32_bf16 a[24:27],v[84:87],a[152:155],a[24:27]
v_mfma_f32_16x16x32_bf16 a[24:27],v[88:91],a[168:171],a[24:27]
v_mfma_f32_16x16x32_bf16 a[24:27],v[92:95],a[184:187],a[24:27]
buffer_store_dwordx4 v[16:19],v28,s[36:39],s75 offen offset:3072 sc1
v_mfma_f32_16x16x32_bf16 a[28:31],v[92:95],a[188:191],a[28:31]
ds_read_b64_tr_b16 v[92:93],v2 offset:30944
ds_read_b64_tr_b16 v[94:95],v2 offset:36064
v_mfma_f32_16x16x32_bf16 a[28:31],v[88:91],a[172:175],a[28:31]
ds_read_b64_tr_b16 v[88:89],v2 offset:20704
v_mfma_f32_16x16x32_bf16 a[28:31],v[84:87],a[156:159],a[28:31]
ds_read_b64_tr_b16 v[84:85],v2 offset:10464
v_mfma_f32_16x16x32_bf16 a[28:31],v[80:83],a[140:143],a[28:31]
s_waitcnt lgkmcnt(4)
ds_read_b64_tr_b16 v[80:81],v2 offset:224
v_mfma_f32_16x16x32_bf16 a[0:3],v[96:99],a[68:71],a[0:3]
ds_read_b64_tr_b16 v[82:83],v2 offset:5344
ds_read_b64_tr_b16 v[86:87],v2 offset:15584
v_mfma_f32_16x16x32_bf16 a[0:3],v[100:103],a[76:79],a[0:3]
ds_read_b64_tr_b16 v[90:91],v2 offset:25824
v_add_u32 v2,0xffff6000,v2
v_mfma_f32_16x16x32_bf16 a[0:3],v[104:107],a[84:87],a[0:3]
v_mfma_f32_16x16x32_bf16 a[0:3],v[108:111],a[92:95],a[0:3]
v_mfma_f32_16x16x32_bf16 a[4:7],v[108:111],a[124:127],a[4:7]
v_mfma_f32_16x16x32_bf16 a[4:7],v[104:107],a[116:119],a[4:7]
v_mfma_f32_16x16x32_bf16 a[4:7],v[100:103],a[108:111],a[4:7]
v_mfma_f32_16x16x32_bf16 a[4:7],v[96:99],a[100:103],a[4:7]
v_mfma_f32_16x16x32_bf16 a[8:11],v[96:99],a[64:67],a[8:11]
v_mfma_f32_16x16x32_bf16 a[8:11],v[100:103],a[72:75],a[8:11]
v_mfma_f32_16x16x32_bf16 a[8:11],v[104:107],a[80:83],a[8:11]
v_mfma_f32_16x16x32_bf16 a[8:11],v[108:111],a[88:91],a[8:11]
v_accvgpr_read_b32 v8,a0
v_accvgpr_read_b32 v9,a1
v_mfma_f32_16x16x32_bf16 a[12:15],v[108:111],a[120:123],a[12:15]
v_accvgpr_read_b32 v10,a2
v_accvgpr_read_b32 v11,a3
v_mfma_f32_16x16x32_bf16 a[12:15],v[104:107],a[112:115],a[12:15]
v_cvt_pk_bf16_f32 v8,v8,v9
v_cvt_pk_bf16_f32 v9,v10,v11
v_mfma_f32_16x16x32_bf16 a[12:15],v[100:103],a[104:107],a[12:15]
v_accvgpr_read_b32 v16,a4
v_accvgpr_read_b32 v17,a5
v_mfma_f32_16x16x32_bf16 a[12:15],v[96:99],a[96:99],a[12:15]
v_accvgpr_read_b32 v18,a6
v_accvgpr_read_b32 v19,a7
v_mfma_f32_16x16x32_bf16 a[16:19],v[112:115],a[68:71],a[16:19]
v_cvt_pk_bf16_f32 v16,v16,v17
v_cvt_pk_bf16_f32 v17,v18,v19
v_mfma_f32_16x16x32_bf16 a[16:19],v[116:119],a[76:79],a[16:19]
ds_read_b64_tr_b16 v[96:97],v1 offset:192
ds_read_b64_tr_b16 v[98:99],v1 offset:5312
v_mfma_f32_16x16x32_bf16 a[16:19],v[120:123],a[84:87],a[16:19]
ds_read_b64_tr_b16 v[100:101],v1 offset:10432
v_mfma_f32_16x16x32_bf16 a[16:19],v[124:127],a[92:95],a[16:19]
ds_read_b64_tr_b16 v[102:103],v1 offset:15552
v_mfma_f32_16x16x32_bf16 a[20:23],v[124:127],a[124:127],a[20:23]
ds_read_b64_tr_b16 v[104:105],v1 offset:20672
v_mfma_f32_16x16x32_bf16 a[20:23],v[120:123],a[116:119],a[20:23]
ds_read_b64_tr_b16 v[106:107],v1 offset:25792
ds_read_b64_tr_b16 v[108:109],v1 offset:30912
v_mfma_f32_16x16x32_bf16 a[20:23],v[116:119],a[108:111],a[20:23]
ds_read_b64_tr_b16 v[110:111],v1 offset:36032
v_add_u32 v1,0x14000,v1
v_mfma_f32_16x16x32_bf16 a[20:23],v[112:115],a[100:103],a[20:23]
v_accvgpr_read_b32 v12,a16
v_accvgpr_read_b32 v13,a17
v_mfma_f32_16x16x32_bf16 a[24:27],v[112:115],a[64:67],a[24:27]
v_accvgpr_read_b32 v14,a18
v_accvgpr_read_b32 v15,a19
v_mfma_f32_16x16x32_bf16 a[24:27],v[116:119],a[72:75],a[24:27]
v_cvt_pk_bf16_f32 v10,v12,v13
s_nop 0
L_pd24_%=:
s_cbranch_execz L_trtg2S_%=
v_cvt_pk_bf16_f32 v11,v14,v15
v_mfma_f32_16x16x32_bf16 a[24:27],v[120:123],a[80:83],a[24:27]
buffer_store_dwordx4 v[8:11],v6,s[36:39],s76 offen sc1
v_mfma_f32_16x16x32_bf16 a[24:27],v[124:127],a[88:91],a[24:27]
v_accvgpr_read_b32 v20,a20
v_accvgpr_read_b32 v21,a21
v_mfma_f32_16x16x32_bf16 a[28:31],v[124:127],a[120:123],a[28:31]
v_accvgpr_read_b32 v22,a22
v_accvgpr_read_b32 v23,a23
v_mfma_f32_16x16x32_bf16 a[28:31],v[120:123],a[112:115],a[28:31]
v_cvt_pk_bf16_f32 v18,v20,v21
v_cvt_pk_bf16_f32 v19,v22,v23
v_mfma_f32_16x16x32_bf16 a[28:31],v[116:119],a[104:107],a[28:31]
v_accvgpr_read_b32 v8,a8
v_accvgpr_read_b32 v9,a9
v_mfma_f32_16x16x32_bf16 a[28:31],v[112:115],a[96:99],a[28:31]
v_accvgpr_read_b32 v10,a10
v_accvgpr_read_b32 v11,a11
v_mfma_f32_16x16x32_bf16 v[128:131],v[64:67],a[128:131],v[128:131]
v_accvgpr_read_b32 v12,a24
v_accvgpr_read_b32 v13,a25
v_mfma_f32_16x16x32_bf16 v[128:131],v[68:71],a[144:147],v[128:131]
v_accvgpr_read_b32 v14,a26
v_accvgpr_read_b32 v15,a27
v_mfma_f32_16x16x32_bf16 v[128:131],v[72:75],a[160:163],v[128:131]
v_cvt_pk_bf16_f32 v8,v8,v9
v_cvt_pk_bf16_f32 v9,v10,v11
v_mfma_f32_16x16x32_bf16 v[128:131],v[76:79],a[176:179],v[128:131]
v_cvt_pk_bf16_f32 v10,v12,v13
v_cvt_pk_bf16_f32 v11,v14,v15
v_mfma_f32_16x16x32_bf16 v[136:139],v[76:79],a[180:183],v[136:139]
ds_read_b64_tr_b16 v[112:113],v2 offset:224
ds_read_b64_tr_b16 v[114:115],v2 offset:5344
v_mfma_f32_16x16x32_bf16 v[136:139],v[72:75],a[164:167],v[136:139]
buffer_store_dwordx4 v[16:19],v6,s[36:39],s76 offen offset:1024 sc1
v_mfma_f32_16x16x32_bf16 v[136:139],v[68:71],a[148:151],v[136:139]
ds_read_b64_tr_b16 v[116:117],v2 offset:10464
ds_read_b64_tr_b16 v[118:119],v2 offset:15584
v_mfma_f32_16x16x32_bf16 v[136:139],v[64:67],a[132:135],v[136:139]
ds_read_b64_tr_b16 v[120:121],v2 offset:20704
v_accvgpr_read_b32 v16,a12
v_mfma_f32_16x16x32_bf16 v[144:147],v[64:67],a[136:139],v[144:147]
ds_read_b64_tr_b16 v[122:123],v2 offset:25824
v_accvgpr_read_b32 v17,a13
v_mfma_f32_16x16x32_bf16 v[144:147],v[68:71],a[152:155],v[144:147]
ds_read_b64_tr_b16 v[124:125],v2 offset:30944
v_accvgpr_read_b32 v18,a14
v_mfma_f32_16x16x32_bf16 v[144:147],v[72:75],a[168:171],v[144:147]
ds_read_b64_tr_b16 v[126:127],v2 offset:36064
v_accvgpr_read_b32 v19,a15
v_mfma_f32_16x16x32_bf16 v[144:147],v[76:79],a[184:187],v[144:147]
v_accvgpr_read_b32 v20,a28
v_accvgpr_read_b32 v21,a29
v_mfma_f32_16x16x32_bf16 v[152:155],v[76:79],a[188:191],v[152:155]
v_accvgpr_read_b32 v22,a30
v_accvgpr_read_b32 v23,a31
v_mfma_f32_16x16x32_bf16 v[152:155],v[72:75],a[172:175],v[152:155]
v_cvt_pk_bf16_f32 v16,v16,v17
v_cvt_pk_bf16_f32 v17,v18,v19
v_mfma_f32_16x16x32_bf16 v[152:155],v[68:71],a[156:159],v[152:155]
v_cvt_pk_bf16_f32 v18,v20,v21
v_cvt_pk_bf16_f32 v19,v22,v23
v_mfma_f32_16x16x32_bf16 v[152:155],v[64:67],a[140:143],v[152:155]
s_waitcnt lgkmcnt(3)
ds_read_b64_tr_b16 v[64:65],v1 offset:256
ds_read_b64_tr_b16 v[66:67],v1 offset:5376
v_mfma_f32_16x16x32_bf16 v[160:163],v[80:83],a[128:131],v[160:163]
buffer_store_dwordx4 v[8:11],v6,s[36:39],s76 offen offset:2048 sc1
v_mfma_f32_16x16x32_bf16 v[160:163],v[84:87],a[144:147],v[160:163]
ds_read_b64_tr_b16 v[68:69],v1 offset:10496
ds_read_b64_tr_b16 v[70:71],v1 offset:15616
v_mfma_f32_16x16x32_bf16 v[160:163],v[88:91],a[160:163],v[160:163]
ds_read_b64_tr_b16 v[72:73],v1 offset:20736
v_add_u32 v2,0x14000,v2
v_mfma_f32_16x16x32_bf16 v[160:163],v[92:95],a[176:179],v[160:163]
ds_read_b64_tr_b16 v[74:75],v1 offset:25856
v_mov_b32 v20,s52
v_mfma_f32_16x16x32_bf16 v[168:171],v[92:95],a[180:183],v[168:171]
ds_read_b64_tr_b16 v[76:77],v1 offset:30976
v_mov_b32 v21,s53
v_mfma_f32_16x16x32_bf16 v[168:171],v[88:91],a[164:167],v[168:171]
ds_read_b64_tr_b16 v[78:79],v1 offset:36096
v_add_u32 v1,0xffff6000,v1
v_mfma_f32_16x16x32_bf16 v[168:171],v[84:87],a[148:151],v[168:171]
v_lshlrev_b32 v22,20,v6
v_mfma_f32_16x16x32_bf16 v[168:171],v[80:83],a[132:135],v[168:171]
v_mfma_f32_16x16x32_bf16 v[176:179],v[80:83],a[136:139],v[176:179]
)A"
R"A(v_mfma_f32_16x16x32_bf16 v[176:179],v[84:87],a[152:155],v[176:179]
v_mfma_f32_16x16x32_bf16 v[176:179],v[88:91],a[168:171],v[176:179]
v_mfma_f32_16x16x32_bf16 v[176:179],v[92:95],a[184:187],v[176:179]
buffer_store_dwordx4 v[16:19],v28,s[36:39],s76 offen offset:3072 sc1
v_mfma_f32_16x16x32_bf16 v[184:187],v[92:95],a[188:191],v[184:187]
ds_read_b64_tr_b16 v[92:93],v2 offset:31008
ds_read_b64_tr_b16 v[94:95],v2 offset:36128
v_mfma_f32_16x16x32_bf16 v[184:187],v[88:91],a[172:175],v[184:187]
ds_read_b64_tr_b16 v[88:89],v2 offset:20768
v_mfma_f32_16x16x32_bf16 v[184:187],v[84:87],a[156:159],v[184:187]
ds_read_b64_tr_b16 v[84:85],v2 offset:10528
v_mfma_f32_16x16x32_bf16 v[184:187],v[80:83],a[140:143],v[184:187]
ds_read_b64_tr_b16 v[80:81],v2 offset:288
v_mfma_f32_16x16x32_bf16 v[128:131],v[96:99],a[68:71],v[128:131]
ds_read_b64_tr_b16 v[82:83],v2 offset:5408
ds_read_b64_tr_b16 v[86:87],v2 offset:15648
v_mfma_f32_16x16x32_bf16 v[128:131],v[100:103],a[76:79],v[128:131]
ds_read_b64_tr_b16 v[90:91],v2 offset:25888
v_add_u32 v2,0xffff6000,v2
v_mfma_f32_16x16x32_bf16 v[128:131],v[104:107],a[84:87],v[128:131]
v_mfma_f32_16x16x32_bf16 v[128:131],v[108:111],a[92:95],v[128:131]
v_mfma_f32_16x16x32_bf16 v[136:139],v[108:111],a[124:127],v[136:139]
v_mfma_f32_16x16x32_bf16 v[136:139],v[104:107],a[116:119],v[136:139]
v_mfma_f32_16x16x32_bf16 v[136:139],v[100:103],a[108:111],v[136:139]
v_mfma_f32_16x16x32_bf16 v[136:139],v[96:99],a[100:103],v[136:139]
v_mfma_f32_16x16x32_bf16 v[144:147],v[96:99],a[64:67],v[144:147]
v_mfma_f32_16x16x32_bf16 v[144:147],v[100:103],a[72:75],v[144:147]
v_mfma_f32_16x16x32_bf16 v[144:147],v[104:107],a[80:83],v[144:147]
v_mfma_f32_16x16x32_bf16 v[144:147],v[108:111],a[88:91],v[144:147]
v_cvt_pk_bf16_f32 v128,v128,v129
v_cvt_pk_bf16_f32 v129,v130,v131
v_mfma_f32_16x16x32_bf16 v[152:155],v[108:111],a[120:123],v[152:155]
ds_read_b64_tr_b16 v[108:109],v1 offset:30976
ds_read_b64_tr_b16 v[110:111],v1 offset:36096
v_mfma_f32_16x16x32_bf16 v[152:155],v[104:107],a[112:115],v[152:155]
v_cvt_pk_bf16_f32 v136,v136,v137
v_cvt_pk_bf16_f32 v137,v138,v139
v_mfma_f32_16x16x32_bf16 v[152:155],v[100:103],a[104:107],v[152:155]
v_cvt_pk_bf16_f32 v144,v144,v145
v_cvt_pk_bf16_f32 v145,v146,v147
v_mfma_f32_16x16x32_bf16 v[152:155],v[96:99],a[96:99],v[152:155]
ds_read_b64_tr_b16 v[96:97],v1 offset:256
ds_read_b64_tr_b16 v[98:99],v1 offset:5376
v_mfma_f32_16x16x32_bf16 v[160:163],v[112:115],a[68:71],v[160:163]
ds_read_b64_tr_b16 v[100:101],v1 offset:10496
v_mfma_f32_16x16x32_bf16 v[160:163],v[116:119],a[76:79],v[160:163]
s_waitcnt lgkmcnt(5)
ds_read_b64_tr_b16 v[102:103],v1 offset:15616
v_mfma_f32_16x16x32_bf16 v[160:163],v[120:123],a[84:87],v[160:163]
ds_read_b64_tr_b16 v[104:105],v1 offset:20736
v_mfma_f32_16x16x32_bf16 v[160:163],v[124:127],a[92:95],v[160:163]
v_cvt_pk_bf16_f32 v152,v152,v153
v_cvt_pk_bf16_f32 v153,v154,v155
v_mfma_f32_16x16x32_bf16 v[168:171],v[124:127],a[124:127],v[168:171]
ds_read_b64_tr_b16 v[106:107],v1 offset:25856
v_add_u32 v1,0xffff6000,v1
v_mfma_f32_16x16x32_bf16 v[168:171],v[120:123],a[116:119],v[168:171]
v_mfma_f32_16x16x32_bf16 v[168:171],v[116:119],a[108:111],v[168:171]
v_mfma_f32_16x16x32_bf16 v[168:171],v[112:115],a[100:103],v[168:171]
v_cvt_pk_bf16_f32 v130,v160,v161
v_cvt_pk_bf16_f32 v131,v162,v163
v_mfma_f32_16x16x32_bf16 v[176:179],v[112:115],a[64:67],v[176:179]
buffer_store_dwordx4 v[128:131],v6,s[36:39],s79 offen sc1
v_mfma_f32_16x16x32_bf16 v[176:179],v[116:119],a[72:75],v[176:179]
v_mfma_f32_16x16x32_bf16 v[176:179],v[120:123],a[80:83],v[176:179]
v_mfma_f32_16x16x32_bf16 v[176:179],v[124:127],a[88:91],v[176:179]
v_mfma_f32_16x16x32_bf16 v[184:187],v[124:127],a[120:123],v[184:187]
v_cvt_pk_bf16_f32 v138,v168,v169
v_cvt_pk_bf16_f32 v139,v170,v171
v_mfma_f32_16x16x32_bf16 v[184:187],v[120:123],a[112:115],v[184:187]
buffer_store_dwordx4 v[136:139],v6,s[36:39],s80 offen sc1
v_mfma_f32_16x16x32_bf16 v[184:187],v[116:119],a[104:107],v[184:187]
ds_read_b64_tr_b16 v[116:117],v2 offset:10528
ds_read_b64_tr_b16 v[118:119],v2 offset:15648
v_mfma_f32_16x16x32_bf16 v[184:187],v[112:115],a[96:99],v[184:187]
v_cvt_pk_bf16_f32 v146,v176,v177
v_cvt_pk_bf16_f32 v147,v178,v179
s_waitcnt vmcnt(6)
s_barrier
v_mfma_f32_16x16x32_bf16 v[192:195],v[64:67],v[32:35],v[192:195]
buffer_store_dwordx4 v[144:147],v6,s[36:39],s81 offen sc1
v_mfma_f32_16x16x32_bf16 v[192:195],v[68:71],v[36:39],v[192:195]
ds_read_b64_tr_b16 v[112:113],v2 offset:288
ds_read_b64_tr_b16 v[114:115],v2 offset:5408
v_mfma_f32_16x16x32_bf16 v[192:195],v[72:75],v[40:43],v[192:195]
buffer_store_dwordx2 v[58:59],v60,s[44:47],s18 offen offset:2312 sc1
v_cvt_pk_bf16_f32 v154,v184,v185
v_mfma_f32_16x16x32_bf16 v[192:195],v[76:79],v[44:47],v[192:195]
ds_read_b64_tr_b16 v[120:121],v2 offset:20768
v_cvt_pk_bf16_f32 v155,v186,v187
v_mfma_f32_16x16x32_bf16 v[200:203],v[76:79],v[180:183],v[200:203]
buffer_store_dwordx4 v[152:155],v28,s[36:39],s82 offen sc1
v_mfma_f32_16x16x32_bf16 v[200:203],v[72:75],v[164:167],v[200:203]
ds_read_b64_tr_b16 v[122:123],v2 offset:25888
ds_read_b64_tr_b16 v[124:125],v2 offset:31008
v_mfma_f32_16x16x32_bf16 v[200:203],v[68:71],v[148:151],v[200:203]
ds_read_b64_tr_b16 v[126:127],v2 offset:36128
v_add_u32 v2,0xffff6000,v2
v_mfma_f32_16x16x32_bf16 v[200:203],v[64:67],v[132:135],v[200:203]
v_mfma_f32_16x16x32_bf16 v[208:211],v[64:67],v[196:199],v[208:211]
v_mfma_f32_16x16x32_bf16 v[208:211],v[68:71],v[212:215],v[208:211]
v_mfma_f32_16x16x32_bf16 v[208:211],v[72:75],v[228:231],v[208:211]
v_mfma_f32_16x16x32_bf16 v[208:211],v[76:79],v[244:247],v[208:211]
v_mfma_f32_16x16x32_bf16 v[216:219],v[76:79],v[188:191],v[216:219]
ds_read_b64_tr_b16 v[76:77],v1 offset:30976
ds_read_b64_tr_b16 v[78:79],v1 offset:36096
v_mfma_f32_16x16x32_bf16 v[216:219],v[72:75],v[172:175],v[216:219]
ds_read_b64_tr_b16 v[72:73],v1 offset:20736
v_mfma_f32_16x16x32_bf16 v[216:219],v[68:71],v[156:159],v[216:219]
ds_read_b64_tr_b16 v[68:69],v1 offset:10496
v_mfma_f32_16x16x32_bf16 v[216:219],v[64:67],v[140:143],v[216:219]
ds_read_b64_tr_b16 v[64:65],v1 offset:256
v_mfma_f32_16x16x32_bf16 v[224:227],v[80:83],v[32:35],v[224:227]
ds_read_b64_tr_b16 v[66:67],v1 offset:5376
ds_read_b64_tr_b16 v[70:71],v1 offset:15616
v_mfma_f32_16x16x32_bf16 v[224:227],v[84:87],v[36:39],v[224:227]
ds_read_b64_tr_b16 v[74:75],v1 offset:25856
v_mfma_f32_16x16x32_bf16 v[224:227],v[88:91],v[40:43],v[224:227]
v_mfma_f32_16x16x32_bf16 v[224:227],v[92:95],v[44:47],v[224:227]
v_mfma_f32_16x16x32_bf16 v[232:235],v[92:95],v[180:183],v[232:235]
v_mfma_f32_16x16x32_bf16 v[232:235],v[88:91],v[164:167],v[232:235]
v_mfma_f32_16x16x32_bf16 v[232:235],v[84:87],v[148:151],v[232:235]
v_mfma_f32_16x16x32_bf16 v[232:235],v[80:83],v[132:135],v[232:235]
v_mfma_f32_16x16x32_bf16 v[240:243],v[80:83],v[196:199],v[240:243]
v_mfma_f32_16x16x32_bf16 v[240:243],v[84:87],v[212:215],v[240:243]
v_mfma_f32_16x16x32_bf16 v[240:243],v[88:91],v[228:231],v[240:243]
v_mfma_f32_16x16x32_bf16 v[240:243],v[92:95],v[244:247],v[240:243]
v_mfma_f32_16x16x32_bf16 v[248:251],v[92:95],v[188:191],v[248:251]
ds_read_b64_tr_b16 v[92:93],v2 offset:31008
ds_read_b64_tr_b16 v[94:95],v2 offset:36128
v_mfma_f32_16x16x32_bf16 v[248:251],v[88:91],v[172:175],v[248:251]
ds_read_b64_tr_b16 v[88:89],v2 offset:20768
v_mfma_f32_16x16x32_bf16 v[248:251],v[84:87],v[156:159],v[248:251]
ds_read_b64_tr_b16 v[84:85],v2 offset:10528
v_mfma_f32_16x16x32_bf16 v[248:251],v[80:83],v[140:143],v[248:251]
s_waitcnt lgkmcnt(4)
ds_read_b64_tr_b16 v[80:81],v2 offset:288
v_mfma_f32_16x16x32_bf16 v[192:195],v[96:99],a[128:131],v[192:195]
ds_read_b64_tr_b16 v[82:83],v2 offset:5408
ds_read_b64_tr_b16 v[86:87],v2 offset:15648
v_mfma_f32_16x16x32_bf16 v[192:195],v[100:103],a[144:147],v[192:195]
ds_read_b64_tr_b16 v[90:91],v2 offset:25888
v_mfma_f32_16x16x32_bf16 v[192:195],v[104:107],a[160:163],v[192:195]
v_mfma_f32_16x16x32_bf16 v[192:195],v[108:111],a[176:179],v[192:195]
v_mfma_f32_16x16x32_bf16 v[200:203],v[108:111],a[180:183],v[200:203]
v_mfma_f32_16x16x32_bf16 v[200:203],v[104:107],a[164:167],v[200:203]
v_mfma_f32_16x16x32_bf16 v[200:203],v[100:103],a[148:151],v[200:203]
v_mfma_f32_16x16x32_bf16 v[200:203],v[96:99],a[132:135],v[200:203]
v_mfma_f32_16x16x32_bf16 v[208:211],v[96:99],a[136:139],v[208:211]
v_mfma_f32_16x16x32_bf16 v[208:211],v[100:103],a[152:155],v[208:211]
v_mfma_f32_16x16x32_bf16 v[208:211],v[104:107],a[168:171],v[208:211]
v_mfma_f32_16x16x32_bf16 v[208:211],v[108:111],a[184:187],v[208:211]
v_mfma_f32_16x16x32_bf16 v[216:219],v[108:111],a[188:191],v[216:219]
v_mfma_f32_16x16x32_bf16 v[216:219],v[104:107],a[172:175],v[216:219]
v_mfma_f32_16x16x32_bf16 v[216:219],v[100:103],a[156:159],v[216:219]
v_mfma_f32_16x16x32_bf16 v[216:219],v[96:99],a[140:143],v[216:219]
v_mfma_f32_16x16x32_bf16 v[224:227],v[112:115],a[128:131],v[224:227]
v_mfma_f32_16x16x32_bf16 v[224:227],v[116:119],a[144:147],v[224:227]
v_mfma_f32_16x16x32_bf16 v[224:227],v[120:123],a[160:163],v[224:227]
v_mfma_f32_16x16x32_bf16 v[224:227],v[124:127],a[176:179],v[224:227]
v_mfma_f32_16x16x32_bf16 v[232:235],v[124:127],a[180:183],v[232:235]
v_mfma_f32_16x16x32_bf16 v[232:235],v[120:123],a[164:167],v[232:235]
v_mfma_f32_16x16x32_bf16 v[232:235],v[116:119],a[148:151],v[232:235]
v_mfma_f32_16x16x32_bf16 v[232:235],v[112:115],a[132:135],v[232:235]
v_mfma_f32_16x16x32_bf16 v[240:243],v[112:115],a[136:139],v[240:243]
v_mfma_f32_16x16x32_bf16 v[240:243],v[116:119],a[152:155],v[240:243]
v_mfma_f32_16x16x32_bf16 v[240:243],v[120:123],a[168:171],v[240:243]
v_mfma_f32_16x16x32_bf16 v[240:243],v[124:127],a[184:187],v[240:243]
v_mfma_f32_16x16x32_bf16 v[248:251],v[124:127],a[188:191],v[248:251]
v_mfma_f32_16x16x32_bf16 v[248:251],v[120:123],a[172:175],v[248:251]
v_mfma_f32_16x16x32_bf16 v[248:251],v[116:119],a[156:159],v[248:251]
v_mfma_f32_16x16x32_bf16 v[248:251],v[112:115],a[140:143],v[248:251]
s_waitcnt vmcnt(0)
s_barrier
v_mfma_f32_16x16x32_bf16 v[192:195],v[64:67],a[68:71],v[192:195]
buffer_store_dwordx2 v[58:59],v60,s[44:47],s18 offen offset:2304 sc1
v_mfma_f32_16x16x32_bf16 v[192:195],v[68:71],a[76:79],v[192:195]
v_mfma_f32_16x16x32_bf16 v[192:195],v[72:75],a[84:87],v[192:195]
v_mfma_f32_16x16x32_bf16 v[192:195],v[76:79],a[92:95],v[192:195]
v_mfma_f32_16x16x32_bf16 v[200:203],v[76:79],a[124:127],v[200:203]
v_mfma_f32_16x16x32_bf16 v[200:203],v[72:75],a[116:119],v[200:203]
v_mfma_f32_16x16x32_bf16 v[200:203],v[68:71],a[108:111],v[200:203]
v_mfma_f32_16x16x32_bf16 v[200:203],v[64:67],a[100:103],v[200:203]
v_mfma_f32_16x16x32_bf16 v[208:211],v[64:67],a[64:67],v[208:211]
v_mfma_f32_16x16x32_bf16 v[208:211],v[68:71],a[72:75],v[208:211]
v_mfma_f32_16x16x32_bf16 v[208:211],v[72:75],a[80:83],v[208:211]
v_mfma_f32_16x16x32_bf16 v[208:211],v[76:79],a[88:91],v[208:211]
v_mfma_f32_16x16x32_bf16 v[216:219],v[76:79],a[120:123],v[216:219]
v_mfma_f32_16x16x32_bf16 v[216:219],v[72:75],a[112:115],v[216:219]
v_mfma_f32_16x16x32_bf16 v[216:219],v[68:71],a[104:107],v[216:219]
v_mfma_f32_16x16x32_bf16 v[216:219],v[64:67],a[96:99],v[216:219]
s_waitcnt lgkmcnt(0)
v_mfma_f32_16x16x32_bf16 v[224:227],v[80:83],a[68:71],v[224:227]
buffer_load_dwordx2 v[62:63],v61,s[44:47],s91 offen offset:2312 sc1
v_mfma_f32_16x16x32_bf16 v[224:227],v[84:87],a[76:79],v[224:227]
v_mfma_f32_16x16x32_bf16 v[224:227],v[88:91],a[84:87],v[224:227]
v_mfma_f32_16x16x32_bf16 v[224:227],v[92:95],a[92:95],v[224:227]
v_mfma_f32_16x16x32_bf16 v[232:235],v[92:95],a[124:127],v[232:235]
v_mfma_f32_16x16x32_bf16 v[232:235],v[88:91],a[116:119],v[232:235]
v_mfma_f32_16x16x32_bf16 v[232:235],v[84:87],a[108:111],v[232:235]
v_mfma_f32_16x16x32_bf16 v[232:235],v[80:83],a[100:103],v[232:235]
v_mfma_f32_16x16x32_bf16 v[240:243],v[80:83],a[64:67],v[240:243]
v_mfma_f32_16x16x32_bf16 v[240:243],v[84:87],a[72:75],v[240:243]
v_mfma_f32_16x16x32_bf16 v[240:243],v[88:91],a[80:83],v[240:243]
v_mfma_f32_16x16x32_bf16 v[240:243],v[92:95],a[88:91],v[240:243]
v_mfma_f32_16x16x32_bf16 v[248:251],v[92:95],a[120:123],v[248:251]
v_mfma_f32_16x16x32_bf16 v[248:251],v[88:91],a[112:115],v[248:251]
v_mfma_f32_16x16x32_bf16 v[248:251],v[84:87],a[104:107],v[248:251]
)A"
R"A(v_mfma_f32_16x16x32_bf16 v[248:251],v[80:83],a[96:99],v[248:251]
s_waitcnt vmcnt(0)
v_cmp_ne_u32 s[56:57],s52,v62
v_cmp_ne_u32 s[58:59],s53,v63
s_nop 0
s_or_b64 s[56:57],s[56:57],s[58:59]
s_cbranch_scc1 L_fr8_%=
L_fo8_%=:
buffer_load_dwordx4 v[64:67],v6,s[36:39],s66 offen sc1
buffer_load_dwordx4 v[68:71],v6,s[36:39],s67 offen sc1
buffer_load_dwordx4 v[72:75],v6,s[36:39],s66 offen offset:1024 sc1
buffer_load_dwordx4 v[76:79],v6,s[36:39],s67 offen offset:1024 sc1
buffer_load_dwordx4 v[80:83],v6,s[36:39],s66 offen offset:2048 sc1
buffer_load_dwordx4 v[84:87],v6,s[36:39],s67 offen offset:2048 sc1
buffer_load_dwordx4 v[88:91],v28,s[36:39],s66 offen offset:3072 sc1
buffer_load_dwordx4 v[92:95],v28,s[36:39],s67 offen offset:3072 sc1
buffer_load_dwordx2 v[4:5],v3,s[44:47],s91 offen offset:2304 sc1
s_waitcnt vmcnt(8)
v_mfma_f32_16x16x32_bf16 v[192:195],v[48:51],v[64:67],v[192:195]
s_waitcnt vmcnt(7)
v_mfma_f32_16x16x32_bf16 v[192:195],v[48:51],v[68:71],v[192:195]
v_mfma_f32_16x16x32_bf16 v[224:227],v[52:55],v[64:67],v[224:227]
v_mfma_f32_16x16x32_bf16 v[224:227],v[52:55],v[68:71],v[224:227]
s_waitcnt vmcnt(6)
v_mfma_f32_16x16x32_bf16 v[200:203],v[48:51],v[72:75],v[200:203]
s_waitcnt vmcnt(5)
v_mfma_f32_16x16x32_bf16 v[200:203],v[48:51],v[76:79],v[200:203]
v_mfma_f32_16x16x32_bf16 v[232:235],v[52:55],v[72:75],v[232:235]
v_mfma_f32_16x16x32_bf16 v[232:235],v[52:55],v[76:79],v[232:235]
s_waitcnt vmcnt(4)
v_mfma_f32_16x16x32_bf16 v[208:211],v[48:51],v[80:83],v[208:211]
s_waitcnt vmcnt(3)
v_mfma_f32_16x16x32_bf16 v[208:211],v[48:51],v[84:87],v[208:211]
v_mfma_f32_16x16x32_bf16 v[240:243],v[52:55],v[80:83],v[240:243]
v_mfma_f32_16x16x32_bf16 v[240:243],v[52:55],v[84:87],v[240:243]
s_waitcnt vmcnt(2)
v_mfma_f32_16x16x32_bf16 v[216:219],v[48:51],v[88:91],v[216:219]
s_waitcnt vmcnt(1)
v_mfma_f32_16x16x32_bf16 v[216:219],v[48:51],v[92:95],v[216:219]
v_mfma_f32_16x16x32_bf16 v[248:251],v[52:55],v[88:91],v[248:251]
v_mfma_f32_16x16x32_bf16 v[248:251],v[52:55],v[92:95],v[248:251]
s_waitcnt vmcnt(0)
v_cmp_ne_u32 s[56:57],s52,v4
v_cmp_ne_u32 s[58:59],s53,v5
s_nop 0
s_or_b64 s[56:57],s[56:57],s[58:59]
s_cbranch_scc1 L_fr9_%=
L_fo9_%=:
buffer_load_dwordx4 v[96:99],v6,s[36:39],s68 offen sc1
buffer_load_dwordx4 v[100:103],v6,s[36:39],s68 offen offset:1024 sc1
buffer_load_dwordx4 v[104:107],v6,s[36:39],s68 offen offset:2048 sc1
buffer_load_dwordx4 v[108:111],v6,s[36:39],s68 offen offset:3072 sc1
buffer_load_dwordx4 v[112:115],v6,s[36:39],s65 offen sc1
buffer_load_dwordx4 v[116:119],v6,s[36:39],s65 offen offset:1024 sc1
buffer_load_dwordx4 v[120:123],v6,s[36:39],s65 offen offset:2048 sc1
buffer_load_dwordx4 v[124:127],v28,s[36:39],s65 offen offset:3072 sc1
s_waitcnt vmcnt(7)
v_mfma_f32_16x16x32_bf16 v[128:131],v[48:51],v[96:99],0
s_waitcnt vmcnt(6)
v_mfma_f32_16x16x32_bf16 v[128:131],v[48:51],v[100:103],v[128:131]
s_waitcnt vmcnt(5)
v_mfma_f32_16x16x32_bf16 v[128:131],v[48:51],v[104:107],v[128:131]
s_waitcnt vmcnt(4)
v_mfma_f32_16x16x32_bf16 v[128:131],v[48:51],v[108:111],v[128:131]
v_mfma_f32_16x16x32_bf16 v[160:163],v[52:55],v[96:99],0
v_mfma_f32_16x16x32_bf16 v[160:163],v[52:55],v[100:103],v[160:163]
v_mfma_f32_16x16x32_bf16 v[160:163],v[52:55],v[104:107],v[160:163]
v_mfma_f32_16x16x32_bf16 v[160:163],v[52:55],v[108:111],v[160:163]
s_waitcnt vmcnt(3)
v_mfma_f32_16x16x32_bf16 v[192:195],v[48:51],v[112:115],v[192:195]
v_mfma_f32_16x16x32_bf16 v[224:227],v[52:55],v[112:115],v[224:227]
s_waitcnt vmcnt(2)
v_mfma_f32_16x16x32_bf16 v[200:203],v[48:51],v[116:119],v[200:203]
v_mfma_f32_16x16x32_bf16 v[232:235],v[52:55],v[116:119],v[232:235]
s_waitcnt vmcnt(1)
v_cvt_pk_bf16_f32 v128,v128,v129
v_cvt_pk_bf16_f32 v129,v130,v131
v_mfma_f32_16x16x32_bf16 v[208:211],v[48:51],v[120:123],v[208:211]
v_mfma_f32_16x16x32_bf16 v[240:243],v[52:55],v[120:123],v[240:243]
s_waitcnt vmcnt(0)
v_cvt_pk_bf16_f32 v130,v160,v161
v_cvt_pk_bf16_f32 v131,v162,v163
v_mfma_f32_16x16x32_bf16 v[216:219],v[48:51],v[124:127],v[216:219]
v_cvt_pk_bf16_f32 v192,v192,v193
v_cvt_pk_bf16_f32 v193,v194,v195
v_cvt_pk_bf16_f32 v194,v224,v225
v_cvt_pk_bf16_f32 v195,v226,v227
v_cvt_pk_bf16_f32 v200,v200,v201
v_cvt_pk_bf16_f32 v201,v202,v203
v_mfma_f32_16x16x32_bf16 v[248:251],v[52:55],v[124:127],v[248:251]
v_permlane16_swap_b32 v128,v130
v_permlane16_swap_b32 v129,v131
v_cvt_pk_bf16_f32 v202,v232,v233
v_cvt_pk_bf16_f32 v203,v234,v235
v_cvt_pk_bf16_f32 v208,v208,v209
v_cvt_pk_bf16_f32 v209,v210,v211
v_cvt_pk_bf16_f32 v210,v240,v241
v_cvt_pk_bf16_f32 v211,v242,v243
buffer_store_dwordx4 v[128:131],v56,s[24:27],s83 offen offset:256 sc1 nt
v_permlane16_swap_b32 v192,v194
v_permlane16_swap_b32 v193,v195
buffer_store_dwordx4 v[192:195],v56,s[24:27],s87 offen sc1 nt
v_permlane16_swap_b32 v200,v202
v_permlane16_swap_b32 v201,v203
v_cvt_pk_bf16_f32 v216,v216,v217
v_cvt_pk_bf16_f32 v217,v218,v219
v_cvt_pk_bf16_f32 v218,v248,v249
v_cvt_pk_bf16_f32 v219,v250,v251
buffer_store_dwordx4 v[200:203],v56,s[24:27],s88 offen sc1 nt
v_permlane16_swap_b32 v208,v210
v_permlane16_swap_b32 v209,v211
buffer_store_dwordx4 v[208:211],v56,s[24:27],s89 offen sc1 nt
v_permlane16_swap_b32 v216,v218
v_permlane16_swap_b32 v217,v219
buffer_store_dwordx4 v[216:219],v56,s[24:27],s90 offen sc1 nt
s_lshl_b32 s69,s62,20
s_add_u32 s69,s69,s18
buffer_store_dword v20,v22,s[44:47],s69 offen sc1
L_geS_%=:
s_branch L_end_%=
L_hot_%=:
L_fr1_%=:
s_mov_b32 s19,65536
L_fl1_%=:
buffer_load_dwordx2 v[4:5],v3,s[44:47],s91 offen offset:256 sc1
s_waitcnt vmcnt(0)
s_sub_u32 s19,s19,1
s_cmp_eq_u32 s19,0
s_cbranch_scc1 L_fo1_%=
v_cmp_ne_u32 s[56:57],s52,v4
v_cmp_ne_u32 s[58:59],s53,v5
s_nop 1
s_or_b64 s[56:57],s[56:57],s[58:59]
s_cbranch_scc1 L_fl1_%=
s_branch L_fo1_%=
L_fr2_%=:
s_mov_b32 s19,65536
L_fl2_%=:
buffer_load_dwordx2 v[4:5],v3,s[44:47],s91 offen offset:1280 sc1
s_waitcnt vmcnt(0)
s_sub_u32 s19,s19,1
s_cmp_eq_u32 s19,0
s_cbranch_scc1 L_fo2_%=
v_cmp_ne_u32 s[56:57],s52,v4
v_cmp_ne_u32 s[58:59],s53,v5
s_nop 1
s_or_b64 s[56:57],s[56:57],s[58:59]
s_cbranch_scc1 L_fl2_%=
s_branch L_fo2_%=
L_fr3_%=:
s_mov_b32 s19,65536
L_fl3_%=:
buffer_load_dwordx2 v[4:5],v3,s[44:47],s91 offen offset:1280 sc1
s_waitcnt vmcnt(0)
s_sub_u32 s19,s19,1
s_cmp_eq_u32 s19,0
s_cbranch_scc1 L_fo3_%=
v_cmp_ne_u32 s[56:57],s52,v4
v_cmp_ne_u32 s[58:59],s53,v5
s_nop 1
s_or_b64 s[56:57],s[56:57],s[58:59]
s_cbranch_scc1 L_fl3_%=
s_branch L_fo3_%=
L_fr4_%=:
s_mov_b32 s19,65536
L_fl4_%=:
buffer_load_dwordx2 v[62:63],v61,s[44:47],s91 offen offset:2312 sc1
s_waitcnt vmcnt(0)
s_sub_u32 s19,s19,1
s_cmp_eq_u32 s19,0
s_cbranch_scc1 L_fo4_%=
v_cmp_ne_u32 s[56:57],s52,v62
v_cmp_ne_u32 s[58:59],s53,v63
s_nop 1
s_or_b64 s[56:57],s[56:57],s[58:59]
s_cbranch_scc1 L_fl4_%=
s_branch L_fo4_%=
L_fr5_%=:
s_mov_b32 s19,65536
L_fl5_%=:
buffer_load_dwordx2 v[4:5],v3,s[44:47],s91 offen offset:2304 sc1
s_waitcnt vmcnt(0)
s_sub_u32 s19,s19,1
s_cmp_eq_u32 s19,0
s_cbranch_scc1 L_fo5_%=
v_cmp_ne_u32 s[56:57],s52,v4
v_cmp_ne_u32 s[58:59],s53,v5
s_nop 1
s_or_b64 s[56:57],s[56:57],s[58:59]
s_cbranch_scc1 L_fl5_%=
s_branch L_fo5_%=
L_fr6_%=:
s_mov_b32 s19,65536
L_fl6_%=:
buffer_load_dwordx2 v[4:5],v3,s[44:47],s91 offen offset:256 sc1
s_waitcnt vmcnt(0)
s_sub_u32 s19,s19,1
s_cmp_eq_u32 s19,0
s_cbranch_scc1 L_fo6_%=
v_cmp_ne_u32 s[56:57],s52,v4
v_cmp_ne_u32 s[58:59],s53,v5
s_nop 1
s_or_b64 s[56:57],s[56:57],s[58:59]
s_cbranch_scc1 L_fl6_%=
s_branch L_fo6_%=
L_fr7_%=:
s_mov_b32 s19,65536
L_fl7_%=:
buffer_load_dwordx2 v[4:5],v3,s[44:47],s91 offen offset:1280 sc1
s_waitcnt vmcnt(0)
s_sub_u32 s19,s19,1
s_cmp_eq_u32 s19,0
s_cbranch_scc1 L_fo7_%=
v_cmp_ne_u32 s[56:57],s52,v4
v_cmp_ne_u32 s[58:59],s53,v5
s_nop 1
s_or_b64 s[56:57],s[56:57],s[58:59]
s_cbranch_scc1 L_fl7_%=
s_branch L_fo7_%=
L_fr8_%=:
s_mov_b32 s19,65536
L_fl8_%=:
buffer_load_dwordx2 v[62:63],v61,s[44:47],s91 offen offset:2312 sc1
s_waitcnt vmcnt(0)
s_sub_u32 s19,s19,1
s_cmp_eq_u32 s19,0
s_cbranch_scc1 L_fo8_%=
v_cmp_ne_u32 s[56:57],s52,v62
v_cmp_ne_u32 s[58:59],s53,v63
s_nop 1
s_or_b64 s[56:57],s[56:57],s[58:59]
s_cbranch_scc1 L_fl8_%=
s_branch L_fo8_%=
L_fr9_%=:
s_mov_b32 s19,65536
L_fl9_%=:
buffer_load_dwordx2 v[4:5],v3,s[44:47],s91 offen offset:2304 sc1
s_waitcnt vmcnt(0)
s_sub_u32 s19,s19,1
s_cmp_eq_u32 s19,0
s_cbranch_scc1 L_fo9_%=
v_cmp_ne_u32 s[56:57],s52,v4
v_cmp_ne_u32 s[58:59],s53,v5
s_nop 1
s_or_b64 s[56:57],s[56:57],s[58:59]
s_cbranch_scc1 L_fl9_%=
s_branch L_fo9_%=
L_slow0_%=:
s_waitcnt vmcnt(0) lgkmcnt(0)
s_nop 7
v_accvgpr_write_b32 a160,v32
v_accvgpr_write_b32 a161,v33
v_accvgpr_write_b32 a162,v34
v_accvgpr_write_b32 a163,v35
v_accvgpr_write_b32 a164,v36
v_accvgpr_write_b32 a165,v37
v_accvgpr_write_b32 a166,v38
v_accvgpr_write_b32 a167,v39
v_accvgpr_write_b32 a168,v40
v_accvgpr_write_b32 a169,v41
v_accvgpr_write_b32 a170,v42
v_accvgpr_write_b32 a171,v43
v_accvgpr_write_b32 a172,v44
v_accvgpr_write_b32 a173,v45
v_accvgpr_write_b32 a174,v46
v_accvgpr_write_b32 a175,v47
s_waitcnt vmcnt(0) lgkmcnt(0)
s_nop 7
v_bfe_u32 v11,v6,4,4
v_lshrrev_b32 v12,8,v6
v_lshlrev_b32 v8,8,v11
v_lshl_add_u32 v8,v12,4,v8
v_lshlrev_b32 v9,10,v12
v_lshl_add_u32 v9,v11,1,v9
v_add_u32 v10,4096,v9
v_mov_b32 v48,0xff800000
v_mov_b32 v52,0xff800000
v_mov_b32 v56,0xff800000
v_mov_b32 v60,0xff800000
s_mov_b32 s65,0
s_mov_b32 s66,64
L_s10_%=:
buffer_load_dwordx4 a[128:131],v8,s[20:23],s65 offen
buffer_load_dwordx4 a[132:135],v8,s[20:23],s65 offen offset:64
buffer_load_dwordx4 a[136:139],v8,s[20:23],s65 offen offset:128
buffer_load_dwordx4 a[140:143],v8,s[20:23],s65 offen offset:192
s_waitcnt vmcnt(0)
v_mfma_f32_16x16x32_bf16 v[64:67],a[128:131],v[132:135],0
v_mfma_f32_16x16x32_bf16 v[64:67],a[132:135],v[140:143],v[64:67]
v_mfma_f32_16x16x32_bf16 v[64:67],a[136:139],v[148:151],v[64:67]
v_mfma_f32_16x16x32_bf16 v[64:67],a[140:143],v[156:159],v[64:67]
v_mfma_f32_16x16x32_bf16 v[80:83],a[128:131],v[164:167],0
v_mfma_f32_16x16x32_bf16 v[80:83],a[132:135],v[172:175],v[80:83]
v_mfma_f32_16x16x32_bf16 v[80:83],a[136:139],v[180:183],v[80:83]
v_mfma_f32_16x16x32_bf16 v[80:83],a[140:143],v[188:191],v[80:83]
v_mfma_f32_16x16x32_bf16 v[96:99],a[128:131],v[196:199],0
v_mfma_f32_16x16x32_bf16 v[96:99],a[132:135],v[204:207],v[96:99]
v_mfma_f32_16x16x32_bf16 v[96:99],a[136:139],v[212:215],v[96:99]
v_mfma_f32_16x16x32_bf16 v[96:99],a[140:143],v[220:223],v[96:99]
v_mfma_f32_16x16x32_bf16 v[112:115],a[128:131],v[228:231],0
v_mfma_f32_16x16x32_bf16 v[112:115],a[132:135],v[236:239],v[112:115]
v_mfma_f32_16x16x32_bf16 v[112:115],a[136:139],v[244:247],v[112:115]
v_mfma_f32_16x16x32_bf16 v[112:115],a[140:143],v[252:255],v[112:115]
s_nop 7
v_max3_f32 v48,v48,v64,v65
v_max3_f32 v48,v48,v66,v67
v_max3_f32 v52,v52,v80,v81
v_max3_f32 v52,v52,v82,v83
v_max3_f32 v56,v56,v96,v97
v_max3_f32 v56,v56,v98,v99
v_max3_f32 v60,v60,v112,v113
v_max3_f32 v60,v60,v114,v115
s_add_u32 s65,s65,4096
s_sub_u32 s66,s66,1
s_cmp_lg_u32 s66,0
s_cbranch_scc1 L_s10_%=
v_mov_b32 v11,v48
s_nop 1
v_permlane32_swap_b32 v48,v11
v_max_f32 v48,v48,v11
v_mov_b32 v11,v48
s_nop 1
v_permlane16_swap_b32 v48,v11
v_max_f32 v48,v48,v11
v_sub_f32 v32,0,v48
v_sub_f32 v33,0,v48
v_sub_f32 v34,0,v48
v_sub_f32 v35,0,v48
v_mov_b32 v11,v52
s_nop 1
v_permlane32_swap_b32 v52,v11
v_max_f32 v52,v52,v11
v_mov_b32 v11,v52
s_nop 1
v_permlane16_swap_b32 v52,v11
v_max_f32 v52,v52,v11
v_sub_f32 v36,0,v52
v_sub_f32 v37,0,v52
v_sub_f32 v38,0,v52
v_sub_f32 v39,0,v52
v_mov_b32 v11,v56
s_nop 1
v_permlane32_swap_b32 v56,v11
v_max_f32 v56,v56,v11
v_mov_b32 v11,v56
s_nop 1
v_permlane16_swap_b32 v56,v11
v_max_f32 v56,v56,v11
v_sub_f32 v40,0,v56
v_sub_f32 v41,0,v56
v_sub_f32 v42,0,v56
v_sub_f32 v43,0,v56
v_mov_b32 v11,v60
s_nop 1
v_permlane32_swap_b32 v60,v11
v_max_f32 v60,v60,v11
v_mov_b32 v11,v60
s_nop 1
v_permlane16_swap_b32 v60,v11
v_max_f32 v60,v60,v11
v_max_f32 v60,s72,v60
v_sub_f32 v44,0,v60
v_sub_f32 v45,0,v60
v_sub_f32 v46,0,v60
v_sub_f32 v47,0,v60
v_mov_b32 v48,0
v_mov_b32 v49,0
v_mov_b32 v50,0
v_mov_b32 v51,0
v_mov_b32 v52,0
v_mov_b32 v53,0
v_mov_b32 v54,0
v_mov_b32 v55,0
v_mov_b32 v56,0
v_mov_b32 v57,0
v_mov_b32 v58,0
v_mov_b32 v59,0
v_mov_b32 v60,0
v_mov_b32 v61,0
v_mov_b32 v62,0
v_mov_b32 v63,0
v_accvgpr_write_b32 a0,0
)A"
R"A(v_accvgpr_write_b32 a1,0
v_accvgpr_write_b32 a2,0
v_accvgpr_write_b32 a3,0
v_accvgpr_write_b32 a4,0
v_accvgpr_write_b32 a5,0
v_accvgpr_write_b32 a6,0
v_accvgpr_write_b32 a7,0
v_accvgpr_write_b32 a8,0
v_accvgpr_write_b32 a9,0
v_accvgpr_write_b32 a10,0
v_accvgpr_write_b32 a11,0
v_accvgpr_write_b32 a12,0
v_accvgpr_write_b32 a13,0
v_accvgpr_write_b32 a14,0
v_accvgpr_write_b32 a15,0
v_accvgpr_write_b32 a16,0
v_accvgpr_write_b32 a17,0
v_accvgpr_write_b32 a18,0
v_accvgpr_write_b32 a19,0
v_accvgpr_write_b32 a20,0
v_accvgpr_write_b32 a21,0
v_accvgpr_write_b32 a22,0
v_accvgpr_write_b32 a23,0
v_accvgpr_write_b32 a24,0
v_accvgpr_write_b32 a25,0
v_accvgpr_write_b32 a26,0
v_accvgpr_write_b32 a27,0
v_accvgpr_write_b32 a28,0
v_accvgpr_write_b32 a29,0
v_accvgpr_write_b32 a30,0
v_accvgpr_write_b32 a31,0
v_accvgpr_write_b32 a32,0
v_accvgpr_write_b32 a33,0
v_accvgpr_write_b32 a34,0
v_accvgpr_write_b32 a35,0
v_accvgpr_write_b32 a36,0
v_accvgpr_write_b32 a37,0
v_accvgpr_write_b32 a38,0
v_accvgpr_write_b32 a39,0
v_accvgpr_write_b32 a40,0
v_accvgpr_write_b32 a41,0
v_accvgpr_write_b32 a42,0
v_accvgpr_write_b32 a43,0
v_accvgpr_write_b32 a44,0
v_accvgpr_write_b32 a45,0
v_accvgpr_write_b32 a46,0
v_accvgpr_write_b32 a47,0
v_accvgpr_write_b32 a48,0
v_accvgpr_write_b32 a49,0
v_accvgpr_write_b32 a50,0
v_accvgpr_write_b32 a51,0
v_accvgpr_write_b32 a52,0
v_accvgpr_write_b32 a53,0
v_accvgpr_write_b32 a54,0
v_accvgpr_write_b32 a55,0
v_accvgpr_write_b32 a56,0
v_accvgpr_write_b32 a57,0
v_accvgpr_write_b32 a58,0
v_accvgpr_write_b32 a59,0
v_accvgpr_write_b32 a60,0
v_accvgpr_write_b32 a61,0
v_accvgpr_write_b32 a62,0
v_accvgpr_write_b32 a63,0
v_accvgpr_write_b32 a64,0
v_accvgpr_write_b32 a65,0
v_accvgpr_write_b32 a66,0
v_accvgpr_write_b32 a67,0
v_accvgpr_write_b32 a68,0
v_accvgpr_write_b32 a69,0
v_accvgpr_write_b32 a70,0
v_accvgpr_write_b32 a71,0
v_accvgpr_write_b32 a72,0
v_accvgpr_write_b32 a73,0
v_accvgpr_write_b32 a74,0
v_accvgpr_write_b32 a75,0
v_accvgpr_write_b32 a76,0
v_accvgpr_write_b32 a77,0
v_accvgpr_write_b32 a78,0
v_accvgpr_write_b32 a79,0
v_accvgpr_write_b32 a80,0
v_accvgpr_write_b32 a81,0
v_accvgpr_write_b32 a82,0
v_accvgpr_write_b32 a83,0
v_accvgpr_write_b32 a84,0
v_accvgpr_write_b32 a85,0
v_accvgpr_write_b32 a86,0
v_accvgpr_write_b32 a87,0
v_accvgpr_write_b32 a88,0
v_accvgpr_write_b32 a89,0
v_accvgpr_write_b32 a90,0
v_accvgpr_write_b32 a91,0
v_accvgpr_write_b32 a92,0
v_accvgpr_write_b32 a93,0
v_accvgpr_write_b32 a94,0
v_accvgpr_write_b32 a95,0
v_accvgpr_write_b32 a96,0
v_accvgpr_write_b32 a97,0
v_accvgpr_write_b32 a98,0
v_accvgpr_write_b32 a99,0
v_accvgpr_write_b32 a100,0
v_accvgpr_write_b32 a101,0
v_accvgpr_write_b32 a102,0
v_accvgpr_write_b32 a103,0
v_accvgpr_write_b32 a104,0
v_accvgpr_write_b32 a105,0
v_accvgpr_write_b32 a106,0
v_accvgpr_write_b32 a107,0
v_accvgpr_write_b32 a108,0
v_accvgpr_write_b32 a109,0
v_accvgpr_write_b32 a110,0
v_accvgpr_write_b32 a111,0
v_accvgpr_write_b32 a112,0
v_accvgpr_write_b32 a113,0
v_accvgpr_write_b32 a114,0
v_accvgpr_write_b32 a115,0
v_accvgpr_write_b32 a116,0
v_accvgpr_write_b32 a117,0
v_accvgpr_write_b32 a118,0
v_accvgpr_write_b32 a119,0
v_accvgpr_write_b32 a120,0
v_accvgpr_write_b32 a121,0
v_accvgpr_write_b32 a122,0
v_accvgpr_write_b32 a123,0
v_accvgpr_write_b32 a124,0
v_accvgpr_write_b32 a125,0
v_accvgpr_write_b32 a126,0
v_accvgpr_write_b32 a127,0
s_mov_b32 s65,0
s_mov_b32 s66,32
L_s20_%=:
s_add_u32 s67,s65,4096
buffer_load_dwordx4 a[128:131],v8,s[20:23],s65 offen
buffer_load_dwordx4 a[132:135],v8,s[20:23],s65 offen offset:64
buffer_load_dwordx4 a[136:139],v8,s[20:23],s65 offen offset:128
buffer_load_dwordx4 a[140:143],v8,s[20:23],s65 offen offset:192
buffer_load_dwordx4 a[144:147],v8,s[20:23],s67 offen
buffer_load_dwordx4 a[148:151],v8,s[20:23],s67 offen offset:64
buffer_load_dwordx4 a[152:155],v8,s[20:23],s67 offen offset:128
buffer_load_dwordx4 a[156:159],v8,s[20:23],s67 offen offset:192
s_waitcnt vmcnt(0)
v_mfma_f32_16x16x32_bf16 v[64:67],a[128:131],v[132:135],v[32:35]
v_mfma_f32_16x16x32_bf16 v[64:67],a[132:135],v[140:143],v[64:67]
v_mfma_f32_16x16x32_bf16 v[64:67],a[136:139],v[148:151],v[64:67]
v_mfma_f32_16x16x32_bf16 v[64:67],a[140:143],v[156:159],v[64:67]
v_mfma_f32_16x16x32_bf16 v[68:71],a[144:147],v[132:135],v[32:35]
v_mfma_f32_16x16x32_bf16 v[68:71],a[148:151],v[140:143],v[68:71]
v_mfma_f32_16x16x32_bf16 v[68:71],a[152:155],v[148:151],v[68:71]
v_mfma_f32_16x16x32_bf16 v[68:71],a[156:159],v[156:159],v[68:71]
v_mfma_f32_16x16x32_bf16 v[80:83],a[128:131],v[164:167],v[36:39]
v_mfma_f32_16x16x32_bf16 v[80:83],a[132:135],v[172:175],v[80:83]
v_mfma_f32_16x16x32_bf16 v[80:83],a[136:139],v[180:183],v[80:83]
v_mfma_f32_16x16x32_bf16 v[80:83],a[140:143],v[188:191],v[80:83]
v_mfma_f32_16x16x32_bf16 v[84:87],a[144:147],v[164:167],v[36:39]
v_mfma_f32_16x16x32_bf16 v[84:87],a[148:151],v[172:175],v[84:87]
v_mfma_f32_16x16x32_bf16 v[84:87],a[152:155],v[180:183],v[84:87]
v_mfma_f32_16x16x32_bf16 v[84:87],a[156:159],v[188:191],v[84:87]
v_mfma_f32_16x16x32_bf16 v[96:99],a[128:131],v[196:199],v[40:43]
v_mfma_f32_16x16x32_bf16 v[96:99],a[132:135],v[204:207],v[96:99]
v_mfma_f32_16x16x32_bf16 v[96:99],a[136:139],v[212:215],v[96:99]
v_mfma_f32_16x16x32_bf16 v[96:99],a[140:143],v[220:223],v[96:99]
v_mfma_f32_16x16x32_bf16 v[100:103],a[144:147],v[196:199],v[40:43]
v_mfma_f32_16x16x32_bf16 v[100:103],a[148:151],v[204:207],v[100:103]
v_mfma_f32_16x16x32_bf16 v[100:103],a[152:155],v[212:215],v[100:103]
v_mfma_f32_16x16x32_bf16 v[100:103],a[156:159],v[220:223],v[100:103]
v_mfma_f32_16x16x32_bf16 v[112:115],a[128:131],v[228:231],v[44:47]
v_mfma_f32_16x16x32_bf16 v[112:115],a[132:135],v[236:239],v[112:115]
v_mfma_f32_16x16x32_bf16 v[112:115],a[136:139],v[244:247],v[112:115]
v_mfma_f32_16x16x32_bf16 v[112:115],a[140:143],v[252:255],v[112:115]
v_mfma_f32_16x16x32_bf16 v[116:119],a[144:147],v[228:231],v[44:47]
v_mfma_f32_16x16x32_bf16 v[116:119],a[148:151],v[236:239],v[116:119]
v_mfma_f32_16x16x32_bf16 v[116:119],a[152:155],v[244:247],v[116:119]
v_mfma_f32_16x16x32_bf16 v[116:119],a[156:159],v[252:255],v[116:119]
s_nop 7
v_exp_f32 v64,v64
v_exp_f32 v65,v65
v_exp_f32 v66,v66
v_exp_f32 v67,v67
v_exp_f32 v68,v68
v_exp_f32 v69,v69
v_exp_f32 v70,v70
v_exp_f32 v71,v71
s_nop 0
v_cvt_pk_bf16_f32 v64,v64,v65
v_cvt_pk_bf16_f32 v65,v66,v67
v_cvt_pk_bf16_f32 v66,v68,v69
v_cvt_pk_bf16_f32 v67,v70,v71
v_exp_f32 v80,v80
v_exp_f32 v81,v81
v_exp_f32 v82,v82
v_exp_f32 v83,v83
v_exp_f32 v84,v84
v_exp_f32 v85,v85
v_exp_f32 v86,v86
v_exp_f32 v87,v87
s_nop 0
v_cvt_pk_bf16_f32 v80,v80,v81
v_cvt_pk_bf16_f32 v81,v82,v83
v_cvt_pk_bf16_f32 v82,v84,v85
v_cvt_pk_bf16_f32 v83,v86,v87
v_exp_f32 v96,v96
v_exp_f32 v97,v97
v_exp_f32 v98,v98
v_exp_f32 v99,v99
v_exp_f32 v100,v100
v_exp_f32 v101,v101
v_exp_f32 v102,v102
v_exp_f32 v103,v103
s_nop 0
v_cvt_pk_bf16_f32 v96,v96,v97
v_cvt_pk_bf16_f32 v97,v98,v99
v_cvt_pk_bf16_f32 v98,v100,v101
v_cvt_pk_bf16_f32 v99,v102,v103
v_exp_f32 v112,v112
v_exp_f32 v113,v113
v_exp_f32 v114,v114
v_exp_f32 v115,v115
v_exp_f32 v116,v116
v_exp_f32 v117,v117
v_exp_f32 v118,v118
v_exp_f32 v119,v119
s_nop 0
v_cvt_pk_bf16_f32 v112,v112,v113
v_cvt_pk_bf16_f32 v113,v114,v115
v_cvt_pk_bf16_f32 v114,v116,v117
v_cvt_pk_bf16_f32 v115,v118,v119
buffer_load_ushort v72,v9,s[24:27],s65 offen
buffer_load_ushort v73,v9,s[24:27],s65 offen offset:256
buffer_load_ushort v74,v9,s[24:27],s65 offen offset:512
buffer_load_ushort v75,v9,s[24:27],s65 offen offset:768
buffer_load_ushort v76,v10,s[24:27],s65 offen
buffer_load_ushort v77,v10,s[24:27],s65 offen offset:256
buffer_load_ushort v78,v10,s[24:27],s65 offen offset:512
buffer_load_ushort v79,v10,s[24:27],s65 offen offset:768
s_waitcnt vmcnt(0)
v_lshl_or_b32 v88,v73,16,v72
v_lshl_or_b32 v89,v75,16,v74
v_lshl_or_b32 v90,v77,16,v76
v_lshl_or_b32 v91,v79,16,v78
s_nop 1
v_mfma_f32_16x16x32_bf16 a[0:3],v[88:91],v[64:67],a[0:3]
v_mfma_f32_16x16x32_bf16 a[32:35],v[88:91],v[80:83],a[32:35]
v_mfma_f32_16x16x32_bf16 a[64:67],v[88:91],v[96:99],a[64:67]
v_mfma_f32_16x16x32_bf16 a[96:99],v[88:91],v[112:115],a[96:99]
buffer_load_ushort v72,v9,s[24:27],s65 offen offset:32
buffer_load_ushort v73,v9,s[24:27],s65 offen offset:288
buffer_load_ushort v74,v9,s[24:27],s65 offen offset:544
buffer_load_ushort v75,v9,s[24:27],s65 offen offset:800
buffer_load_ushort v76,v10,s[24:27],s65 offen offset:32
buffer_load_ushort v77,v10,s[24:27],s65 offen offset:288
buffer_load_ushort v78,v10,s[24:27],s65 offen offset:544
buffer_load_ushort v79,v10,s[24:27],s65 offen offset:800
s_waitcnt vmcnt(0)
v_lshl_or_b32 v88,v73,16,v72
v_lshl_or_b32 v89,v75,16,v74
v_lshl_or_b32 v90,v77,16,v76
v_lshl_or_b32 v91,v79,16,v78
s_nop 1
v_mfma_f32_16x16x32_bf16 a[4:7],v[88:91],v[64:67],a[4:7]
v_mfma_f32_16x16x32_bf16 a[36:39],v[88:91],v[80:83],a[36:39]
v_mfma_f32_16x16x32_bf16 a[68:71],v[88:91],v[96:99],a[68:71]
v_mfma_f32_16x16x32_bf16 a[100:103],v[88:91],v[112:115],a[100:103]
buffer_load_ushort v72,v9,s[24:27],s65 offen offset:64
buffer_load_ushort v73,v9,s[24:27],s65 offen offset:320
buffer_load_ushort v74,v9,s[24:27],s65 offen offset:576
buffer_load_ushort v75,v9,s[24:27],s65 offen offset:832
buffer_load_ushort v76,v10,s[24:27],s65 offen offset:64
buffer_load_ushort v77,v10,s[24:27],s65 offen offset:320
buffer_load_ushort v78,v10,s[24:27],s65 offen offset:576
buffer_load_ushort v79,v10,s[24:27],s65 offen offset:832
s_waitcnt vmcnt(0)
v_lshl_or_b32 v88,v73,16,v72
v_lshl_or_b32 v89,v75,16,v74
v_lshl_or_b32 v90,v77,16,v76
v_lshl_or_b32 v91,v79,16,v78
s_nop 1
v_mfma_f32_16x16x32_bf16 a[8:11],v[88:91],v[64:67],a[8:11]
v_mfma_f32_16x16x32_bf16 a[40:43],v[88:91],v[80:83],a[40:43]
v_mfma_f32_16x16x32_bf16 a[72:75],v[88:91],v[96:99],a[72:75]
v_mfma_f32_16x16x32_bf16 a[104:107],v[88:91],v[112:115],a[104:107]
buffer_load_ushort v72,v9,s[24:27],s65 offen offset:96
buffer_load_ushort v73,v9,s[24:27],s65 offen offset:352
buffer_load_ushort v74,v9,s[24:27],s65 offen offset:608
buffer_load_ushort v75,v9,s[24:27],s65 offen offset:864
buffer_load_ushort v76,v10,s[24:27],s65 offen offset:96
buffer_load_ushort v77,v10,s[24:27],s65 offen offset:352
buffer_load_ushort v78,v10,s[24:27],s65 offen offset:608
buffer_load_ushort v79,v10,s[24:27],s65 offen offset:864
s_waitcnt vmcnt(0)
v_lshl_or_b32 v88,v73,16,v72
v_lshl_or_b32 v89,v75,16,v74
v_lshl_or_b32 v90,v77,16,v76
v_lshl_or_b32 v91,v79,16,v78
s_nop 1
v_mfma_f32_16x16x32_bf16 a[12:15],v[88:91],v[64:67],a[12:15]
v_mfma_f32_16x16x32_bf16 a[44:47],v[88:91],v[80:83],a[44:47]
v_mfma_f32_16x16x32_bf16 a[76:79],v[88:91],v[96:99],a[76:79]
v_mfma_f32_16x16x32_bf16 a[108:111],v[88:91],v[112:115],a[108:111]
buffer_load_ushort v72,v9,s[24:27],s65 offen offset:128
buffer_load_ushort v73,v9,s[24:27],s65 offen offset:384
buffer_load_ushort v74,v9,s[24:27],s65 offen offset:640
buffer_load_ushort v75,v9,s[24:27],s65 offen offset:896
buffer_load_ushort v76,v10,s[24:27],s65 offen offset:128
buffer_load_ushort v77,v10,s[24:27],s65 offen offset:384
buffer_load_ushort v78,v10,s[24:27],s65 offen offset:640
buffer_load_ushort v79,v10,s[24:27],s65 offen offset:896
s_waitcnt vmcnt(0)
v_lshl_or_b32 v88,v73,16,v72
v_lshl_or_b32 v89,v75,16,v74
v_lshl_or_b32 v90,v77,16,v76
v_lshl_or_b32 v91,v79,16,v78
s_nop 1
v_mfma_f32_16x16x32_bf16 a[16:19],v[88:91],v[64:67],a[16:19]
v_mfma_f32_16x16x32_bf16 a[48:51],v[88:91],v[80:83],a[48:51]
v_mfma_f32_16x16x32_bf16 a[80:83],v[88:91],v[96:99],a[80:83]
v_mfma_f32_16x16x32_bf16 a[112:115],v[88:91],v[112:115],a[112:115]
buffer_load_ushort v72,v9,s[24:27],s65 offen offset:160
buffer_load_ushort v73,v9,s[24:27],s65 offen offset:416
buffer_load_ushort v74,v9,s[24:27],s65 offen offset:672
buffer_load_ushort v75,v9,s[24:27],s65 offen offset:928
buffer_load_ushort v76,v10,s[24:27],s65 offen offset:160
buffer_load_ushort v77,v10,s[24:27],s65 offen offset:416
buffer_load_ushort v78,v10,s[24:27],s65 offen offset:672
buffer_load_ushort v79,v10,s[24:27],s65 offen offset:928
s_waitcnt vmcnt(0)
v_lshl_or_b32 v88,v73,16,v72
v_lshl_or_b32 v89,v75,16,v74
v_lshl_or_b32 v90,v77,16,v76
v_lshl_or_b32 v91,v79,16,v78
s_nop 1
v_mfma_f32_16x16x32_bf16 a[20:23],v[88:91],v[64:67],a[20:23]
v_mfma_f32_16x16x32_bf16 a[52:55],v[88:91],v[80:83],a[52:55]
)A"
R"A(v_mfma_f32_16x16x32_bf16 a[84:87],v[88:91],v[96:99],a[84:87]
v_mfma_f32_16x16x32_bf16 a[116:119],v[88:91],v[112:115],a[116:119]
buffer_load_ushort v72,v9,s[24:27],s65 offen offset:192
buffer_load_ushort v73,v9,s[24:27],s65 offen offset:448
buffer_load_ushort v74,v9,s[24:27],s65 offen offset:704
buffer_load_ushort v75,v9,s[24:27],s65 offen offset:960
buffer_load_ushort v76,v10,s[24:27],s65 offen offset:192
buffer_load_ushort v77,v10,s[24:27],s65 offen offset:448
buffer_load_ushort v78,v10,s[24:27],s65 offen offset:704
buffer_load_ushort v79,v10,s[24:27],s65 offen offset:960
s_waitcnt vmcnt(0)
v_lshl_or_b32 v88,v73,16,v72
v_lshl_or_b32 v89,v75,16,v74
v_lshl_or_b32 v90,v77,16,v76
v_lshl_or_b32 v91,v79,16,v78
s_nop 1
v_mfma_f32_16x16x32_bf16 a[24:27],v[88:91],v[64:67],a[24:27]
v_mfma_f32_16x16x32_bf16 a[56:59],v[88:91],v[80:83],a[56:59]
v_mfma_f32_16x16x32_bf16 a[88:91],v[88:91],v[96:99],a[88:91]
v_mfma_f32_16x16x32_bf16 a[120:123],v[88:91],v[112:115],a[120:123]
buffer_load_ushort v72,v9,s[24:27],s65 offen offset:224
buffer_load_ushort v73,v9,s[24:27],s65 offen offset:480
buffer_load_ushort v74,v9,s[24:27],s65 offen offset:736
buffer_load_ushort v75,v9,s[24:27],s65 offen offset:992
buffer_load_ushort v76,v10,s[24:27],s65 offen offset:224
buffer_load_ushort v77,v10,s[24:27],s65 offen offset:480
buffer_load_ushort v78,v10,s[24:27],s65 offen offset:736
buffer_load_ushort v79,v10,s[24:27],s65 offen offset:992
s_waitcnt vmcnt(0)
v_lshl_or_b32 v88,v73,16,v72
v_lshl_or_b32 v89,v75,16,v74
v_lshl_or_b32 v90,v77,16,v76
v_lshl_or_b32 v91,v79,16,v78
s_nop 1
v_mfma_f32_16x16x32_bf16 a[28:31],v[88:91],v[64:67],a[28:31]
v_mfma_f32_16x16x32_bf16 a[60:63],v[88:91],v[80:83],a[60:63]
v_mfma_f32_16x16x32_bf16 a[92:95],v[88:91],v[96:99],a[92:95]
v_mfma_f32_16x16x32_bf16 a[124:127],v[88:91],v[112:115],a[124:127]
v_mfma_f32_16x16x32_bf16 v[48:51],v[28:31],v[64:67],v[48:51]
v_mfma_f32_16x16x32_bf16 v[52:55],v[28:31],v[80:83],v[52:55]
v_mfma_f32_16x16x32_bf16 v[56:59],v[28:31],v[96:99],v[56:59]
v_mfma_f32_16x16x32_bf16 v[60:63],v[28:31],v[112:115],v[60:63]
s_add_u32 s65,s65,8192
s_sub_u32 s66,s66,1
s_cmp_lg_u32 s66,0
s_cbranch_scc1 L_s20_%=
s_nop 7
v_max_f32 v24,0x00800000,v48
v_rcp_f32 v24,v24
v_max_f32 v25,0x00800000,v52
v_rcp_f32 v25,v25
v_max_f32 v26,0x00800000,v56
v_rcp_f32 v26,v26
v_max_f32 v27,0x00800000,v60
v_rcp_f32 v27,v27
v_accvgpr_read_b32 v8,a0
v_accvgpr_read_b32 v9,a1
v_accvgpr_read_b32 v10,a2
v_accvgpr_read_b32 v11,a3
v_accvgpr_read_b32 v12,a4
v_accvgpr_read_b32 v13,a5
v_accvgpr_read_b32 v14,a6
v_accvgpr_read_b32 v15,a7
v_mul_f32 v8,v8,v24
v_mul_f32 v9,v9,v24
v_mul_f32 v10,v10,v24
v_mul_f32 v11,v11,v24
v_mul_f32 v12,v12,v24
v_mul_f32 v13,v13,v24
v_mul_f32 v14,v14,v24
v_mul_f32 v15,v15,v24
v_cvt_pk_bf16_f32 v8,v8,v9
v_cvt_pk_bf16_f32 v9,v10,v11
v_cvt_pk_bf16_f32 v10,v12,v13
v_cvt_pk_bf16_f32 v11,v14,v15
ds_write_b128 v7,v[8:11] offset:6144
v_accvgpr_read_b32 v16,a8
v_accvgpr_read_b32 v17,a9
v_accvgpr_read_b32 v18,a10
v_accvgpr_read_b32 v19,a11
v_accvgpr_read_b32 v20,a12
v_accvgpr_read_b32 v21,a13
v_accvgpr_read_b32 v22,a14
v_accvgpr_read_b32 v23,a15
v_mul_f32 v16,v16,v24
v_mul_f32 v17,v17,v24
v_mul_f32 v18,v18,v24
v_mul_f32 v19,v19,v24
v_mul_f32 v20,v20,v24
v_mul_f32 v21,v21,v24
v_mul_f32 v22,v22,v24
v_mul_f32 v23,v23,v24
v_cvt_pk_bf16_f32 v16,v16,v17
v_cvt_pk_bf16_f32 v17,v18,v19
v_cvt_pk_bf16_f32 v18,v20,v21
v_cvt_pk_bf16_f32 v19,v22,v23
ds_write_b128 v7,v[16:19] offset:7168
v_accvgpr_read_b32 v8,a16
v_accvgpr_read_b32 v9,a17
v_accvgpr_read_b32 v10,a18
v_accvgpr_read_b32 v11,a19
v_accvgpr_read_b32 v12,a20
v_accvgpr_read_b32 v13,a21
v_accvgpr_read_b32 v14,a22
v_accvgpr_read_b32 v15,a23
v_mul_f32 v8,v8,v24
v_mul_f32 v9,v9,v24
v_mul_f32 v10,v10,v24
v_mul_f32 v11,v11,v24
v_mul_f32 v12,v12,v24
v_mul_f32 v13,v13,v24
v_mul_f32 v14,v14,v24
v_mul_f32 v15,v15,v24
v_cvt_pk_bf16_f32 v8,v8,v9
v_cvt_pk_bf16_f32 v9,v10,v11
v_cvt_pk_bf16_f32 v10,v12,v13
v_cvt_pk_bf16_f32 v11,v14,v15
ds_write_b128 v7,v[8:11]
v_accvgpr_read_b32 v16,a24
v_accvgpr_read_b32 v17,a25
v_accvgpr_read_b32 v18,a26
v_accvgpr_read_b32 v19,a27
v_accvgpr_read_b32 v20,a28
v_accvgpr_read_b32 v21,a29
v_accvgpr_read_b32 v22,a30
v_accvgpr_read_b32 v23,a31
v_mul_f32 v16,v16,v24
v_mul_f32 v17,v17,v24
v_mul_f32 v18,v18,v24
v_mul_f32 v19,v19,v24
v_mul_f32 v20,v20,v24
v_mul_f32 v21,v21,v24
v_mul_f32 v22,v22,v24
v_mul_f32 v23,v23,v24
v_cvt_pk_bf16_f32 v16,v16,v17
v_cvt_pk_bf16_f32 v17,v18,v19
v_cvt_pk_bf16_f32 v18,v20,v21
v_cvt_pk_bf16_f32 v19,v22,v23
ds_write_b128 v7,v[16:19] offset:1024
v_accvgpr_read_b32 v32,a160
v_accvgpr_read_b32 v33,a161
v_accvgpr_read_b32 v34,a162
v_accvgpr_read_b32 v35,a163
v_accvgpr_read_b32 v36,a164
v_accvgpr_read_b32 v37,a165
v_accvgpr_read_b32 v38,a166
v_accvgpr_read_b32 v39,a167
v_accvgpr_read_b32 v40,a168
v_accvgpr_read_b32 v41,a169
v_accvgpr_read_b32 v42,a170
v_accvgpr_read_b32 v43,a171
v_accvgpr_read_b32 v44,a172
v_accvgpr_read_b32 v45,a173
v_accvgpr_read_b32 v46,a174
v_accvgpr_read_b32 v47,a175
s_waitcnt lgkmcnt(0)
v_subrev_u32 v8,s78,v1
ds_read_b128 a[128:131],v8
ds_read_b128 a[132:135],v8 offset:256
ds_read_b128 a[136:139],v8 offset:512
ds_read_b128 a[140:143],v8 offset:768
ds_read_b128 a[144:147],v8 offset:4096
ds_read_b128 a[148:151],v8 offset:4352
ds_read_b128 a[152:155],v8 offset:4608
ds_read_b128 a[156:159],v8 offset:4864
ds_read_b128 a[160:163],v8 offset:8192
ds_read_b128 a[164:167],v8 offset:8448
ds_read_b128 a[168:171],v8 offset:8704
ds_read_b128 a[172:175],v8 offset:8960
ds_read_b128 a[176:179],v8 offset:12288
ds_read_b128 a[180:183],v8 offset:12544
ds_read_b128 a[184:187],v8 offset:12800
ds_read_b128 a[188:191],v8 offset:13056
s_waitcnt lgkmcnt(15)
v_mfma_f32_16x16x32_bf16 v[64:67],a[128:131],v[128:131],v[32:35]
s_waitcnt lgkmcnt(14)
v_mfma_f32_16x16x32_bf16 v[64:67],a[132:135],v[136:139],v[64:67]
s_waitcnt lgkmcnt(13)
v_mfma_f32_16x16x32_bf16 v[64:67],a[136:139],v[144:147],v[64:67]
s_waitcnt lgkmcnt(12)
v_mfma_f32_16x16x32_bf16 v[64:67],a[140:143],v[152:155],v[64:67]
s_waitcnt lgkmcnt(8)
v_mfma_f32_16x16x32_bf16 v[68:71],a[156:159],v[152:155],v[32:35]
v_mfma_f32_16x16x32_bf16 v[68:71],a[152:155],v[144:147],v[68:71]
v_mfma_f32_16x16x32_bf16 v[68:71],a[148:151],v[136:139],v[68:71]
v_mfma_f32_16x16x32_bf16 v[68:71],a[144:147],v[128:131],v[68:71]
s_waitcnt lgkmcnt(0)
v_mfma_f32_16x16x32_bf16 v[72:75],a[160:163],v[128:131],v[32:35]
v_mfma_f32_16x16x32_bf16 v[72:75],a[164:167],v[136:139],v[72:75]
v_mfma_f32_16x16x32_bf16 v[72:75],a[168:171],v[144:147],v[72:75]
v_mfma_f32_16x16x32_bf16 v[72:75],a[172:175],v[152:155],v[72:75]
v_mfma_f32_16x16x32_bf16 v[76:79],a[188:191],v[152:155],v[32:35]
v_mfma_f32_16x16x32_bf16 v[76:79],a[184:187],v[144:147],v[76:79]
v_mfma_f32_16x16x32_bf16 v[76:79],a[180:183],v[136:139],v[76:79]
v_mfma_f32_16x16x32_bf16 v[76:79],a[176:179],v[128:131],v[76:79]
v_mfma_f32_16x16x32_bf16 v[80:83],a[128:131],v[160:163],v[36:39]
v_mfma_f32_16x16x32_bf16 v[80:83],a[132:135],v[168:171],v[80:83]
v_mfma_f32_16x16x32_bf16 v[80:83],a[136:139],v[176:179],v[80:83]
v_mfma_f32_16x16x32_bf16 v[80:83],a[140:143],v[184:187],v[80:83]
v_mfma_f32_16x16x32_bf16 v[84:87],a[156:159],v[184:187],v[36:39]
v_mfma_f32_16x16x32_bf16 v[84:87],a[152:155],v[176:179],v[84:87]
v_mfma_f32_16x16x32_bf16 v[84:87],a[148:151],v[168:171],v[84:87]
v_mfma_f32_16x16x32_bf16 v[84:87],a[144:147],v[160:163],v[84:87]
v_mfma_f32_16x16x32_bf16 v[88:91],a[160:163],v[160:163],v[36:39]
v_mfma_f32_16x16x32_bf16 v[88:91],a[164:167],v[168:171],v[88:91]
v_mfma_f32_16x16x32_bf16 v[88:91],a[168:171],v[176:179],v[88:91]
v_mfma_f32_16x16x32_bf16 v[88:91],a[172:175],v[184:187],v[88:91]
v_mfma_f32_16x16x32_bf16 v[92:95],a[188:191],v[184:187],v[36:39]
v_mfma_f32_16x16x32_bf16 v[92:95],a[184:187],v[176:179],v[92:95]
v_mfma_f32_16x16x32_bf16 v[92:95],a[180:183],v[168:171],v[92:95]
v_mfma_f32_16x16x32_bf16 v[92:95],a[176:179],v[160:163],v[92:95]
v_mfma_f32_16x16x32_bf16 v[96:99],a[128:131],v[192:195],v[40:43]
v_mfma_f32_16x16x32_bf16 v[96:99],a[132:135],v[200:203],v[96:99]
v_mfma_f32_16x16x32_bf16 v[96:99],a[136:139],v[208:211],v[96:99]
v_mfma_f32_16x16x32_bf16 v[96:99],a[140:143],v[216:219],v[96:99]
v_mfma_f32_16x16x32_bf16 v[100:103],a[156:159],v[216:219],v[40:43]
v_mfma_f32_16x16x32_bf16 v[100:103],a[152:155],v[208:211],v[100:103]
v_mfma_f32_16x16x32_bf16 v[100:103],a[148:151],v[200:203],v[100:103]
v_mfma_f32_16x16x32_bf16 v[100:103],a[144:147],v[192:195],v[100:103]
v_mfma_f32_16x16x32_bf16 v[104:107],a[160:163],v[192:195],v[40:43]
v_mfma_f32_16x16x32_bf16 v[104:107],a[164:167],v[200:203],v[104:107]
v_mfma_f32_16x16x32_bf16 v[104:107],a[168:171],v[208:211],v[104:107]
v_mfma_f32_16x16x32_bf16 v[104:107],a[172:175],v[216:219],v[104:107]
v_mfma_f32_16x16x32_bf16 v[108:111],a[188:191],v[216:219],v[40:43]
v_mfma_f32_16x16x32_bf16 v[108:111],a[184:187],v[208:211],v[108:111]
v_mfma_f32_16x16x32_bf16 v[108:111],a[180:183],v[200:203],v[108:111]
v_mfma_f32_16x16x32_bf16 v[108:111],a[176:179],v[192:195],v[108:111]
v_exp_f32 v64,v64
v_exp_f32 v65,v65
s_nop 0
v_cvt_pk_bf16_f32 v64,v64,v65
v_exp_f32 v66,v66
v_exp_f32 v67,v67
s_nop 0
v_cvt_pk_bf16_f32 v65,v66,v67
v_exp_f32 v68,v68
v_exp_f32 v69,v69
s_nop 0
v_cvt_pk_bf16_f32 v66,v68,v69
v_exp_f32 v70,v70
v_exp_f32 v71,v71
s_nop 0
v_cvt_pk_bf16_f32 v67,v70,v71
v_exp_f32 v72,v72
v_exp_f32 v73,v73
s_nop 0
v_cvt_pk_bf16_f32 v72,v72,v73
v_exp_f32 v74,v74
v_exp_f32 v75,v75
s_nop 0
v_cvt_pk_bf16_f32 v73,v74,v75
v_exp_f32 v76,v76
v_exp_f32 v77,v77
s_nop 0
v_cvt_pk_bf16_f32 v74,v76,v77
v_exp_f32 v78,v78
v_exp_f32 v79,v79
s_nop 0
v_cvt_pk_bf16_f32 v75,v78,v79
v_exp_f32 v80,v80
v_exp_f32 v81,v81
s_nop 0
v_cvt_pk_bf16_f32 v80,v80,v81
v_exp_f32 v82,v82
v_exp_f32 v83,v83
s_nop 0
v_cvt_pk_bf16_f32 v81,v82,v83
v_exp_f32 v84,v84
v_exp_f32 v85,v85
s_nop 0
v_cvt_pk_bf16_f32 v82,v84,v85
v_exp_f32 v86,v86
v_exp_f32 v87,v87
s_nop 0
v_cvt_pk_bf16_f32 v83,v86,v87
v_exp_f32 v88,v88
v_exp_f32 v89,v89
s_nop 0
v_cvt_pk_bf16_f32 v88,v88,v89
v_mfma_f32_16x16x32_bf16 a[0:3],a[192:195],v[64:67],0
v_mfma_f32_16x16x32_bf16 a[0:3],a[196:199],v[72:75],a[0:3]
v_mfma_f32_16x16x32_bf16 a[4:7],a[200:203],v[64:67],0
v_mfma_f32_16x16x32_bf16 a[4:7],a[204:207],v[72:75],a[4:7]
v_mfma_f32_16x16x32_bf16 a[8:11],a[208:211],v[64:67],0
v_mfma_f32_16x16x32_bf16 a[8:11],a[212:215],v[72:75],a[8:11]
v_mfma_f32_16x16x32_bf16 a[12:15],a[216:219],v[64:67],0
v_mfma_f32_16x16x32_bf16 a[12:15],a[220:223],v[72:75],a[12:15]
v_mfma_f32_16x16x32_bf16 a[16:19],a[224:227],v[64:67],0
v_mfma_f32_16x16x32_bf16 a[16:19],a[228:231],v[72:75],a[16:19]
v_mfma_f32_16x16x32_bf16 a[20:23],a[232:235],v[64:67],0
v_mfma_f32_16x16x32_bf16 a[20:23],a[236:239],v[72:75],a[20:23]
v_mfma_f32_16x16x32_bf16 a[24:27],a[240:243],v[64:67],0
v_mfma_f32_16x16x32_bf16 a[24:27],a[244:247],v[72:75],a[24:27]
v_mfma_f32_16x16x32_bf16 a[28:31],a[248:251],v[64:67],0
v_mfma_f32_16x16x32_bf16 a[28:31],a[252:255],v[72:75],a[28:31]
v_mfma_f32_16x16x32_bf16 v[48:51],v[28:31],v[64:67],0
v_mfma_f32_16x16x32_bf16 v[48:51],v[28:31],v[72:75],v[48:51]
s_waitcnt vmcnt(0) lgkmcnt(0)
s_nop 7
s_branch L_ok0_%=
L_slow1_%=:
s_waitcnt vmcnt(0) lgkmcnt(0)
s_nop 7
v_bfe_u32 v11,v6,4,4
v_lshrrev_b32 v12,8,v6
v_lshlrev_b32 v8,8,v11
v_lshl_add_u32 v8,v12,4,v8
v_lshlrev_b32 v9,10,v12
v_lshl_add_u32 v9,v11,1,v9
v_add_u32 v10,4096,v9
v_mov_b32 v48,0xff800000
v_mov_b32 v52,0xff800000
v_mov_b32 v56,0xff800000
v_mov_b32 v60,0xff800000
s_mov_b32 s65,262144
s_mov_b32 s66,64
L_s11_%=:
buffer_load_dwordx4 a[128:131],v8,s[20:23],s65 offen
buffer_load_dwordx4 a[132:135],v8,s[20:23],s65 offen offset:64
buffer_load_dwordx4 a[136:139],v8,s[20:23],s65 offen offset:128
buffer_load_dwordx4 a[140:143],v8,s[20:23],s65 offen offset:192
s_waitcnt vmcnt(0)
v_mfma_f32_16x16x32_bf16 v[64:67],a[128:131],v[128:131],0
v_mfma_f32_16x16x32_bf16 v[64:67],a[132:135],v[136:139],v[64:67]
v_mfma_f32_16x16x32_bf16 v[64:67],a[136:139],v[144:147],v[64:67]
v_mfma_f32_16x16x32_bf16 v[64:67],a[140:143],v[152:155],v[64:67]
v_mfma_f32_16x16x32_bf16 v[80:83],a[128:131],v[160:163],0
v_mfma_f32_16x16x32_bf16 v[80:83],a[132:135],v[168:171],v[80:83]
v_mfma_f32_16x16x32_bf16 v[80:83],a[136:139],v[176:179],v[80:83]
)A"
R"A(v_mfma_f32_16x16x32_bf16 v[80:83],a[140:143],v[184:187],v[80:83]
v_mfma_f32_16x16x32_bf16 v[96:99],a[128:131],v[192:195],0
v_mfma_f32_16x16x32_bf16 v[96:99],a[132:135],v[200:203],v[96:99]
v_mfma_f32_16x16x32_bf16 v[96:99],a[136:139],v[208:211],v[96:99]
v_mfma_f32_16x16x32_bf16 v[96:99],a[140:143],v[216:219],v[96:99]
v_mfma_f32_16x16x32_bf16 v[112:115],a[128:131],v[224:227],0
v_mfma_f32_16x16x32_bf16 v[112:115],a[132:135],v[232:235],v[112:115]
v_mfma_f32_16x16x32_bf16 v[112:115],a[136:139],v[240:243],v[112:115]
v_mfma_f32_16x16x32_bf16 v[112:115],a[140:143],v[248:251],v[112:115]
s_nop 7
v_max3_f32 v48,v48,v64,v65
v_max3_f32 v48,v48,v66,v67
v_max3_f32 v52,v52,v80,v81
v_max3_f32 v52,v52,v82,v83
v_max3_f32 v56,v56,v96,v97
v_max3_f32 v56,v56,v98,v99
v_max3_f32 v60,v60,v112,v113
v_max3_f32 v60,v60,v114,v115
s_add_u32 s65,s65,4096
s_sub_u32 s66,s66,1
s_cmp_lg_u32 s66,0
s_cbranch_scc1 L_s11_%=
v_mov_b32 v11,v48
s_nop 1
v_permlane32_swap_b32 v48,v11
v_max_f32 v48,v48,v11
v_mov_b32 v11,v48
s_nop 1
v_permlane16_swap_b32 v48,v11
v_max_f32 v48,v48,v11
v_sub_f32 v32,0,v48
v_sub_f32 v33,0,v48
v_sub_f32 v34,0,v48
v_sub_f32 v35,0,v48
v_mov_b32 v11,v52
s_nop 1
v_permlane32_swap_b32 v52,v11
v_max_f32 v52,v52,v11
v_mov_b32 v11,v52
s_nop 1
v_permlane16_swap_b32 v52,v11
v_max_f32 v52,v52,v11
v_sub_f32 v36,0,v52
v_sub_f32 v37,0,v52
v_sub_f32 v38,0,v52
v_sub_f32 v39,0,v52
v_mov_b32 v11,v56
s_nop 1
v_permlane32_swap_b32 v56,v11
v_max_f32 v56,v56,v11
v_mov_b32 v11,v56
s_nop 1
v_permlane16_swap_b32 v56,v11
v_max_f32 v56,v56,v11
v_sub_f32 v40,0,v56
v_sub_f32 v41,0,v56
v_sub_f32 v42,0,v56
v_sub_f32 v43,0,v56
v_mov_b32 v11,v60
s_nop 1
v_permlane32_swap_b32 v60,v11
v_max_f32 v60,v60,v11
v_mov_b32 v11,v60
s_nop 1
v_permlane16_swap_b32 v60,v11
v_max_f32 v60,v60,v11
v_max_f32 v60,s72,v60
v_sub_f32 v44,0,v60
v_sub_f32 v45,0,v60
v_sub_f32 v46,0,v60
v_sub_f32 v47,0,v60
v_mov_b32 v48,0
v_mov_b32 v49,0
v_mov_b32 v50,0
v_mov_b32 v51,0
v_mov_b32 v52,0
v_mov_b32 v53,0
v_mov_b32 v54,0
v_mov_b32 v55,0
v_mov_b32 v56,0
v_mov_b32 v57,0
v_mov_b32 v58,0
v_mov_b32 v59,0
v_mov_b32 v60,0
v_mov_b32 v61,0
v_mov_b32 v62,0
v_mov_b32 v63,0
v_accvgpr_write_b32 a0,0
v_accvgpr_write_b32 a1,0
v_accvgpr_write_b32 a2,0
v_accvgpr_write_b32 a3,0
v_accvgpr_write_b32 a4,0
v_accvgpr_write_b32 a5,0
v_accvgpr_write_b32 a6,0
v_accvgpr_write_b32 a7,0
v_accvgpr_write_b32 a8,0
v_accvgpr_write_b32 a9,0
v_accvgpr_write_b32 a10,0
v_accvgpr_write_b32 a11,0
v_accvgpr_write_b32 a12,0
v_accvgpr_write_b32 a13,0
v_accvgpr_write_b32 a14,0
v_accvgpr_write_b32 a15,0
v_accvgpr_write_b32 a16,0
v_accvgpr_write_b32 a17,0
v_accvgpr_write_b32 a18,0
v_accvgpr_write_b32 a19,0
v_accvgpr_write_b32 a20,0
v_accvgpr_write_b32 a21,0
v_accvgpr_write_b32 a22,0
v_accvgpr_write_b32 a23,0
v_accvgpr_write_b32 a24,0
v_accvgpr_write_b32 a25,0
v_accvgpr_write_b32 a26,0
v_accvgpr_write_b32 a27,0
v_accvgpr_write_b32 a28,0
v_accvgpr_write_b32 a29,0
v_accvgpr_write_b32 a30,0
v_accvgpr_write_b32 a31,0
v_mov_b32 v132,0
v_mov_b32 v133,0
v_mov_b32 v134,0
v_mov_b32 v135,0
v_mov_b32 v140,0
v_mov_b32 v141,0
v_mov_b32 v142,0
v_mov_b32 v143,0
v_mov_b32 v148,0
v_mov_b32 v149,0
v_mov_b32 v150,0
v_mov_b32 v151,0
v_mov_b32 v156,0
v_mov_b32 v157,0
v_mov_b32 v158,0
v_mov_b32 v159,0
v_mov_b32 v164,0
v_mov_b32 v165,0
v_mov_b32 v166,0
v_mov_b32 v167,0
v_mov_b32 v172,0
v_mov_b32 v173,0
v_mov_b32 v174,0
v_mov_b32 v175,0
v_mov_b32 v180,0
v_mov_b32 v181,0
v_mov_b32 v182,0
v_mov_b32 v183,0
v_mov_b32 v188,0
v_mov_b32 v189,0
v_mov_b32 v190,0
v_mov_b32 v191,0
v_mov_b32 v196,0
v_mov_b32 v197,0
v_mov_b32 v198,0
v_mov_b32 v199,0
v_mov_b32 v204,0
v_mov_b32 v205,0
v_mov_b32 v206,0
v_mov_b32 v207,0
v_mov_b32 v212,0
v_mov_b32 v213,0
v_mov_b32 v214,0
v_mov_b32 v215,0
v_mov_b32 v220,0
v_mov_b32 v221,0
v_mov_b32 v222,0
v_mov_b32 v223,0
v_mov_b32 v228,0
v_mov_b32 v229,0
v_mov_b32 v230,0
v_mov_b32 v231,0
v_mov_b32 v236,0
v_mov_b32 v237,0
v_mov_b32 v238,0
v_mov_b32 v239,0
v_mov_b32 v244,0
v_mov_b32 v245,0
v_mov_b32 v246,0
v_mov_b32 v247,0
v_mov_b32 v252,0
v_mov_b32 v253,0
v_mov_b32 v254,0
v_mov_b32 v255,0
v_accvgpr_write_b32 a32,0
v_accvgpr_write_b32 a33,0
v_accvgpr_write_b32 a34,0
v_accvgpr_write_b32 a35,0
v_accvgpr_write_b32 a36,0
v_accvgpr_write_b32 a37,0
v_accvgpr_write_b32 a38,0
v_accvgpr_write_b32 a39,0
v_accvgpr_write_b32 a40,0
v_accvgpr_write_b32 a41,0
v_accvgpr_write_b32 a42,0
v_accvgpr_write_b32 a43,0
v_accvgpr_write_b32 a44,0
v_accvgpr_write_b32 a45,0
v_accvgpr_write_b32 a46,0
v_accvgpr_write_b32 a47,0
v_accvgpr_write_b32 a48,0
v_accvgpr_write_b32 a49,0
v_accvgpr_write_b32 a50,0
v_accvgpr_write_b32 a51,0
v_accvgpr_write_b32 a52,0
v_accvgpr_write_b32 a53,0
v_accvgpr_write_b32 a54,0
v_accvgpr_write_b32 a55,0
v_accvgpr_write_b32 a56,0
v_accvgpr_write_b32 a57,0
v_accvgpr_write_b32 a58,0
v_accvgpr_write_b32 a59,0
v_accvgpr_write_b32 a60,0
v_accvgpr_write_b32 a61,0
v_accvgpr_write_b32 a62,0
v_accvgpr_write_b32 a63,0
s_mov_b32 s65,262144
s_mov_b32 s66,32
L_s21_%=:
s_add_u32 s67,s65,4096
buffer_load_dwordx4 a[128:131],v8,s[20:23],s65 offen
buffer_load_dwordx4 a[132:135],v8,s[20:23],s65 offen offset:64
buffer_load_dwordx4 a[136:139],v8,s[20:23],s65 offen offset:128
buffer_load_dwordx4 a[140:143],v8,s[20:23],s65 offen offset:192
buffer_load_dwordx4 a[144:147],v8,s[20:23],s67 offen
buffer_load_dwordx4 a[148:151],v8,s[20:23],s67 offen offset:64
buffer_load_dwordx4 a[152:155],v8,s[20:23],s67 offen offset:128
buffer_load_dwordx4 a[156:159],v8,s[20:23],s67 offen offset:192
s_waitcnt vmcnt(0)
v_mfma_f32_16x16x32_bf16 v[64:67],a[128:131],v[128:131],v[32:35]
v_mfma_f32_16x16x32_bf16 v[64:67],a[132:135],v[136:139],v[64:67]
v_mfma_f32_16x16x32_bf16 v[64:67],a[136:139],v[144:147],v[64:67]
v_mfma_f32_16x16x32_bf16 v[64:67],a[140:143],v[152:155],v[64:67]
v_mfma_f32_16x16x32_bf16 v[68:71],a[144:147],v[128:131],v[32:35]
v_mfma_f32_16x16x32_bf16 v[68:71],a[148:151],v[136:139],v[68:71]
v_mfma_f32_16x16x32_bf16 v[68:71],a[152:155],v[144:147],v[68:71]
v_mfma_f32_16x16x32_bf16 v[68:71],a[156:159],v[152:155],v[68:71]
v_mfma_f32_16x16x32_bf16 v[80:83],a[128:131],v[160:163],v[36:39]
v_mfma_f32_16x16x32_bf16 v[80:83],a[132:135],v[168:171],v[80:83]
v_mfma_f32_16x16x32_bf16 v[80:83],a[136:139],v[176:179],v[80:83]
v_mfma_f32_16x16x32_bf16 v[80:83],a[140:143],v[184:187],v[80:83]
v_mfma_f32_16x16x32_bf16 v[84:87],a[144:147],v[160:163],v[36:39]
v_mfma_f32_16x16x32_bf16 v[84:87],a[148:151],v[168:171],v[84:87]
v_mfma_f32_16x16x32_bf16 v[84:87],a[152:155],v[176:179],v[84:87]
v_mfma_f32_16x16x32_bf16 v[84:87],a[156:159],v[184:187],v[84:87]
v_mfma_f32_16x16x32_bf16 v[96:99],a[128:131],v[192:195],v[40:43]
v_mfma_f32_16x16x32_bf16 v[96:99],a[132:135],v[200:203],v[96:99]
v_mfma_f32_16x16x32_bf16 v[96:99],a[136:139],v[208:211],v[96:99]
v_mfma_f32_16x16x32_bf16 v[96:99],a[140:143],v[216:219],v[96:99]
v_mfma_f32_16x16x32_bf16 v[100:103],a[144:147],v[192:195],v[40:43]
v_mfma_f32_16x16x32_bf16 v[100:103],a[148:151],v[200:203],v[100:103]
v_mfma_f32_16x16x32_bf16 v[100:103],a[152:155],v[208:211],v[100:103]
v_mfma_f32_16x16x32_bf16 v[100:103],a[156:159],v[216:219],v[100:103]
v_mfma_f32_16x16x32_bf16 v[112:115],a[128:131],v[224:227],v[44:47]
v_mfma_f32_16x16x32_bf16 v[112:115],a[132:135],v[232:235],v[112:115]
v_mfma_f32_16x16x32_bf16 v[112:115],a[136:139],v[240:243],v[112:115]
v_mfma_f32_16x16x32_bf16 v[112:115],a[140:143],v[248:251],v[112:115]
v_mfma_f32_16x16x32_bf16 v[116:119],a[144:147],v[224:227],v[44:47]
v_mfma_f32_16x16x32_bf16 v[116:119],a[148:151],v[232:235],v[116:119]
v_mfma_f32_16x16x32_bf16 v[116:119],a[152:155],v[240:243],v[116:119]
v_mfma_f32_16x16x32_bf16 v[116:119],a[156:159],v[248:251],v[116:119]
s_nop 7
v_exp_f32 v64,v64
v_exp_f32 v65,v65
v_exp_f32 v66,v66
v_exp_f32 v67,v67
v_exp_f32 v68,v68
v_exp_f32 v69,v69
v_exp_f32 v70,v70
v_exp_f32 v71,v71
s_nop 0
v_cvt_pk_bf16_f32 v64,v64,v65
v_cvt_pk_bf16_f32 v65,v66,v67
v_cvt_pk_bf16_f32 v66,v68,v69
v_cvt_pk_bf16_f32 v67,v70,v71
v_exp_f32 v80,v80
v_exp_f32 v81,v81
v_exp_f32 v82,v82
v_exp_f32 v83,v83
v_exp_f32 v84,v84
v_exp_f32 v85,v85
v_exp_f32 v86,v86
v_exp_f32 v87,v87
s_nop 0
v_cvt_pk_bf16_f32 v80,v80,v81
v_cvt_pk_bf16_f32 v81,v82,v83
v_cvt_pk_bf16_f32 v82,v84,v85
v_cvt_pk_bf16_f32 v83,v86,v87
v_exp_f32 v96,v96
v_exp_f32 v97,v97
v_exp_f32 v98,v98
v_exp_f32 v99,v99
v_exp_f32 v100,v100
v_exp_f32 v101,v101
v_exp_f32 v102,v102
v_exp_f32 v103,v103
s_nop 0
v_cvt_pk_bf16_f32 v96,v96,v97
v_cvt_pk_bf16_f32 v97,v98,v99
v_cvt_pk_bf16_f32 v98,v100,v101
v_cvt_pk_bf16_f32 v99,v102,v103
v_exp_f32 v112,v112
v_exp_f32 v113,v113
v_exp_f32 v114,v114
v_exp_f32 v115,v115
v_exp_f32 v116,v116
v_exp_f32 v117,v117
v_exp_f32 v118,v118
v_exp_f32 v119,v119
s_nop 0
v_cvt_pk_bf16_f32 v112,v112,v113
v_cvt_pk_bf16_f32 v113,v114,v115
v_cvt_pk_bf16_f32 v114,v116,v117
v_cvt_pk_bf16_f32 v115,v118,v119
buffer_load_ushort v72,v9,s[24:27],s65 offen
buffer_load_ushort v73,v9,s[24:27],s65 offen offset:256
buffer_load_ushort v74,v9,s[24:27],s65 offen offset:512
buffer_load_ushort v75,v9,s[24:27],s65 offen offset:768
buffer_load_ushort v76,v10,s[24:27],s65 offen
buffer_load_ushort v77,v10,s[24:27],s65 offen offset:256
buffer_load_ushort v78,v10,s[24:27],s65 offen offset:512
buffer_load_ushort v79,v10,s[24:27],s65 offen offset:768
s_waitcnt vmcnt(0)
v_lshl_or_b32 v88,v73,16,v72
v_lshl_or_b32 v89,v75,16,v74
v_lshl_or_b32 v90,v77,16,v76
v_lshl_or_b32 v91,v79,16,v78
s_nop 1
v_mfma_f32_16x16x32_bf16 a[0:3],v[88:91],v[64:67],a[0:3]
v_mfma_f32_16x16x32_bf16 v[132:135],v[88:91],v[80:83],v[132:135]
v_mfma_f32_16x16x32_bf16 v[196:199],v[88:91],v[96:99],v[196:199]
v_mfma_f32_16x16x32_bf16 a[32:35],v[88:91],v[112:115],a[32:35]
buffer_load_ushort v72,v9,s[24:27],s65 offen offset:32
buffer_load_ushort v73,v9,s[24:27],s65 offen offset:288
buffer_load_ushort v74,v9,s[24:27],s65 offen offset:544
buffer_load_ushort v75,v9,s[24:27],s65 offen offset:800
buffer_load_ushort v76,v10,s[24:27],s65 offen offset:32
buffer_load_ushort v77,v10,s[24:27],s65 offen offset:288
buffer_load_ushort v78,v10,s[24:27],s65 offen offset:544
buffer_load_ushort v79,v10,s[24:27],s65 offen offset:800
s_waitcnt vmcnt(0)
v_lshl_or_b32 v88,v73,16,v72
v_lshl_or_b32 v89,v75,16,v74
v_lshl_or_b32 v90,v77,16,v76
v_lshl_or_b32 v91,v79,16,v78
s_nop 1
v_mfma_f32_16x16x32_bf16 a[4:7],v[88:91],v[64:67],a[4:7]
v_mfma_f32_16x16x32_bf16 v[140:143],v[88:91],v[80:83],v[140:143]
v_mfma_f32_16x16x32_bf16 v[204:207],v[88:91],v[96:99],v[204:207]
v_mfma_f32_16x16x32_bf16 a[36:39],v[88:91],v[112:115],a[36:39]
buffer_load_ushort v72,v9,s[24:27],s65 offen offset:64
buffer_load_ushort v73,v9,s[24:27],s65 offen offset:320
buffer_load_ushort v74,v9,s[24:27],s65 offen offset:576
buffer_load_ushort v75,v9,s[24:27],s65 offen offset:832
buffer_load_ushort v76,v10,s[24:27],s65 offen offset:64
buffer_load_ushort v77,v10,s[24:27],s65 offen offset:320
buffer_load_ushort v78,v10,s[24:27],s65 offen offset:576
buffer_load_ushort v79,v10,s[24:27],s65 offen offset:832
s_waitcnt vmcnt(0)
v_lshl_or_b32 v88,v73,16,v72
v_lshl_or_b32 v89,v75,16,v74
v_lshl_or_b32 v90,v77,16,v76
v_lshl_or_b32 v91,v79,16,v78
s_nop 1
v_mfma_f32_16x16x32_bf16 a[8:11],v[88:91],v[64:67],a[8:11]
v_mfma_f32_16x16x32_bf16 v[148:151],v[88:91],v[80:83],v[148:151]
v_mfma_f32_16x16x32_bf16 v[212:215],v[88:91],v[96:99],v[212:215]
v_mfma_f32_16x16x32_bf16 a[40:43],v[88:91],v[112:115],a[40:43]
buffer_load_ushort v72,v9,s[24:27],s65 offen offset:96
buffer_load_ushort v73,v9,s[24:27],s65 offen offset:352
buffer_load_ushort v74,v9,s[24:27],s65 offen offset:608
buffer_load_ushort v75,v9,s[24:27],s65 offen offset:864
buffer_load_ushort v76,v10,s[24:27],s65 offen offset:96
buffer_load_ushort v77,v10,s[24:27],s65 offen offset:352
buffer_load_ushort v78,v10,s[24:27],s65 offen offset:608
buffer_load_ushort v79,v10,s[24:27],s65 offen offset:864
s_waitcnt vmcnt(0)
v_lshl_or_b32 v88,v73,16,v72
v_lshl_or_b32 v89,v75,16,v74
v_lshl_or_b32 v90,v77,16,v76
v_lshl_or_b32 v91,v79,16,v78
s_nop 1
v_mfma_f32_16x16x32_bf16 a[12:15],v[88:91],v[64:67],a[12:15]
v_mfma_f32_16x16x32_bf16 v[156:159],v[88:91],v[80:83],v[156:159]
v_mfma_f32_16x16x32_bf16 v[220:223],v[88:91],v[96:99],v[220:223]
v_mfma_f32_16x16x32_bf16 a[44:47],v[88:91],v[112:115],a[44:47]
)A"
R"A(buffer_load_ushort v72,v9,s[24:27],s65 offen offset:128
buffer_load_ushort v73,v9,s[24:27],s65 offen offset:384
buffer_load_ushort v74,v9,s[24:27],s65 offen offset:640
buffer_load_ushort v75,v9,s[24:27],s65 offen offset:896
buffer_load_ushort v76,v10,s[24:27],s65 offen offset:128
buffer_load_ushort v77,v10,s[24:27],s65 offen offset:384
buffer_load_ushort v78,v10,s[24:27],s65 offen offset:640
buffer_load_ushort v79,v10,s[24:27],s65 offen offset:896
s_waitcnt vmcnt(0)
v_lshl_or_b32 v88,v73,16,v72
v_lshl_or_b32 v89,v75,16,v74
v_lshl_or_b32 v90,v77,16,v76
v_lshl_or_b32 v91,v79,16,v78
s_nop 1
v_mfma_f32_16x16x32_bf16 a[16:19],v[88:91],v[64:67],a[16:19]
v_mfma_f32_16x16x32_bf16 v[164:167],v[88:91],v[80:83],v[164:167]
v_mfma_f32_16x16x32_bf16 v[228:231],v[88:91],v[96:99],v[228:231]
v_mfma_f32_16x16x32_bf16 a[48:51],v[88:91],v[112:115],a[48:51]
buffer_load_ushort v72,v9,s[24:27],s65 offen offset:160
buffer_load_ushort v73,v9,s[24:27],s65 offen offset:416
buffer_load_ushort v74,v9,s[24:27],s65 offen offset:672
buffer_load_ushort v75,v9,s[24:27],s65 offen offset:928
buffer_load_ushort v76,v10,s[24:27],s65 offen offset:160
buffer_load_ushort v77,v10,s[24:27],s65 offen offset:416
buffer_load_ushort v78,v10,s[24:27],s65 offen offset:672
buffer_load_ushort v79,v10,s[24:27],s65 offen offset:928
s_waitcnt vmcnt(0)
v_lshl_or_b32 v88,v73,16,v72
v_lshl_or_b32 v89,v75,16,v74
v_lshl_or_b32 v90,v77,16,v76
v_lshl_or_b32 v91,v79,16,v78
s_nop 1
v_mfma_f32_16x16x32_bf16 a[20:23],v[88:91],v[64:67],a[20:23]
v_mfma_f32_16x16x32_bf16 v[172:175],v[88:91],v[80:83],v[172:175]
v_mfma_f32_16x16x32_bf16 v[236:239],v[88:91],v[96:99],v[236:239]
v_mfma_f32_16x16x32_bf16 a[52:55],v[88:91],v[112:115],a[52:55]
buffer_load_ushort v72,v9,s[24:27],s65 offen offset:192
buffer_load_ushort v73,v9,s[24:27],s65 offen offset:448
buffer_load_ushort v74,v9,s[24:27],s65 offen offset:704
buffer_load_ushort v75,v9,s[24:27],s65 offen offset:960
buffer_load_ushort v76,v10,s[24:27],s65 offen offset:192
buffer_load_ushort v77,v10,s[24:27],s65 offen offset:448
buffer_load_ushort v78,v10,s[24:27],s65 offen offset:704
buffer_load_ushort v79,v10,s[24:27],s65 offen offset:960
s_waitcnt vmcnt(0)
v_lshl_or_b32 v88,v73,16,v72
v_lshl_or_b32 v89,v75,16,v74
v_lshl_or_b32 v90,v77,16,v76
v_lshl_or_b32 v91,v79,16,v78
s_nop 1
v_mfma_f32_16x16x32_bf16 a[24:27],v[88:91],v[64:67],a[24:27]
v_mfma_f32_16x16x32_bf16 v[180:183],v[88:91],v[80:83],v[180:183]
v_mfma_f32_16x16x32_bf16 v[244:247],v[88:91],v[96:99],v[244:247]
v_mfma_f32_16x16x32_bf16 a[56:59],v[88:91],v[112:115],a[56:59]
buffer_load_ushort v72,v9,s[24:27],s65 offen offset:224
buffer_load_ushort v73,v9,s[24:27],s65 offen offset:480
buffer_load_ushort v74,v9,s[24:27],s65 offen offset:736
buffer_load_ushort v75,v9,s[24:27],s65 offen offset:992
buffer_load_ushort v76,v10,s[24:27],s65 offen offset:224
buffer_load_ushort v77,v10,s[24:27],s65 offen offset:480
buffer_load_ushort v78,v10,s[24:27],s65 offen offset:736
buffer_load_ushort v79,v10,s[24:27],s65 offen offset:992
s_waitcnt vmcnt(0)
v_lshl_or_b32 v88,v73,16,v72
v_lshl_or_b32 v89,v75,16,v74
v_lshl_or_b32 v90,v77,16,v76
v_lshl_or_b32 v91,v79,16,v78
s_nop 1
v_mfma_f32_16x16x32_bf16 a[28:31],v[88:91],v[64:67],a[28:31]
v_mfma_f32_16x16x32_bf16 v[188:191],v[88:91],v[80:83],v[188:191]
v_mfma_f32_16x16x32_bf16 v[252:255],v[88:91],v[96:99],v[252:255]
v_mfma_f32_16x16x32_bf16 a[60:63],v[88:91],v[112:115],a[60:63]
v_mfma_f32_16x16x32_bf16 v[48:51],v[28:31],v[64:67],v[48:51]
v_mfma_f32_16x16x32_bf16 v[52:55],v[28:31],v[80:83],v[52:55]
v_mfma_f32_16x16x32_bf16 v[56:59],v[28:31],v[96:99],v[56:59]
v_mfma_f32_16x16x32_bf16 v[60:63],v[28:31],v[112:115],v[60:63]
s_add_u32 s65,s65,8192
s_sub_u32 s66,s66,1
s_cmp_lg_u32 s66,0
s_cbranch_scc1 L_s21_%=
s_nop 7
v_max_f32 v49,0x00800000,v48
v_rcp_f32 v49,v49
v_max_f32 v53,0x00800000,v52
v_rcp_f32 v53,v53
v_max_f32 v57,0x00800000,v56
v_rcp_f32 v57,v57
v_max_f32 v61,0x00800000,v60
v_rcp_f32 v61,v61
v_accvgpr_read_b32 v16,a0
v_accvgpr_read_b32 v17,a1
v_accvgpr_read_b32 v18,a2
v_accvgpr_read_b32 v19,a3
v_accvgpr_read_b32 v20,a4
v_accvgpr_read_b32 v21,a5
v_accvgpr_read_b32 v22,a6
v_accvgpr_read_b32 v23,a7
v_mul_f32 v16,v16,v49
v_mul_f32 v17,v17,v49
v_mul_f32 v18,v18,v49
v_mul_f32 v19,v19,v49
v_mul_f32 v20,v20,v49
v_mul_f32 v21,v21,v49
v_mul_f32 v22,v22,v49
v_mul_f32 v23,v23,v49
v_cvt_pk_bf16_f32 v32,v16,v17
v_cvt_pk_bf16_f32 v33,v18,v19
v_cvt_pk_bf16_f32 v34,v20,v21
v_cvt_pk_bf16_f32 v35,v22,v23
v_accvgpr_read_b32 v8,a8
v_accvgpr_read_b32 v9,a9
v_accvgpr_read_b32 v10,a10
v_accvgpr_read_b32 v11,a11
v_accvgpr_read_b32 v12,a12
v_accvgpr_read_b32 v13,a13
v_accvgpr_read_b32 v14,a14
v_accvgpr_read_b32 v15,a15
v_mul_f32 v8,v8,v49
v_mul_f32 v9,v9,v49
v_mul_f32 v10,v10,v49
v_mul_f32 v11,v11,v49
v_mul_f32 v12,v12,v49
v_mul_f32 v13,v13,v49
v_mul_f32 v14,v14,v49
v_mul_f32 v15,v15,v49
v_cvt_pk_bf16_f32 v36,v8,v9
v_cvt_pk_bf16_f32 v37,v10,v11
v_cvt_pk_bf16_f32 v38,v12,v13
v_cvt_pk_bf16_f32 v39,v14,v15
v_accvgpr_read_b32 v16,a16
v_accvgpr_read_b32 v17,a17
v_accvgpr_read_b32 v18,a18
v_accvgpr_read_b32 v19,a19
v_accvgpr_read_b32 v20,a20
v_accvgpr_read_b32 v21,a21
v_accvgpr_read_b32 v22,a22
v_accvgpr_read_b32 v23,a23
v_mul_f32 v16,v16,v49
v_mul_f32 v17,v17,v49
v_mul_f32 v18,v18,v49
v_mul_f32 v19,v19,v49
v_mul_f32 v20,v20,v49
v_mul_f32 v21,v21,v49
v_mul_f32 v22,v22,v49
v_mul_f32 v23,v23,v49
v_cvt_pk_bf16_f32 v40,v16,v17
v_cvt_pk_bf16_f32 v41,v18,v19
v_cvt_pk_bf16_f32 v42,v20,v21
v_cvt_pk_bf16_f32 v43,v22,v23
ds_read_b64_tr_b16 v[64:65],v1
ds_read_b64_tr_b16 v[66:67],v1 offset:5120
ds_read_b64_tr_b16 v[68:69],v1 offset:10240
ds_read_b64_tr_b16 v[70:71],v1 offset:15360
ds_read_b64_tr_b16 v[72:73],v1 offset:20480
ds_read_b64_tr_b16 v[74:75],v1 offset:25600
ds_read_b64_tr_b16 v[76:77],v1 offset:30720
ds_read_b64_tr_b16 v[78:79],v1 offset:35840
ds_read_b64_tr_b16 v[80:81],v2 offset:32
ds_read_b64_tr_b16 v[82:83],v2 offset:5152
ds_read_b64_tr_b16 v[84:85],v2 offset:10272
ds_read_b64_tr_b16 v[86:87],v2 offset:15392
ds_read_b64_tr_b16 v[88:89],v2 offset:20512
ds_read_b64_tr_b16 v[90:91],v2 offset:25632
ds_read_b64_tr_b16 v[92:93],v2 offset:30752
ds_read_b64_tr_b16 v[94:95],v2 offset:35872
s_waitcnt vmcnt(0) lgkmcnt(0)
s_nop 7
s_branch L_ok1_%=
L_end_%=:
)A"
    :: "s"(ka), "s"(blk), "s"(wid)
    : "v0", "v1", "v2", "v3", "v4", "v5", "v6", "v7", "v8", "v9", "v10", "v11", "v12", "v13", "v14", "v15", "v16", "v17", "v18", "v19", "v20", "v21", "v22", "v23", "v24", "v25", "v26", "v27", "v28", "v29", "v30", "v31", "v32", "v33", "v34", "v35", "v36", "v37", "v38", "v39", "v40", "v41", "v42", "v43", "v44", "v45", "v46", "v47", "v48", "v49", "v50", "v51", "v52", "v53", "v54", "v55", "v56", "v57", "v58", "v59", "v60", "v61", "v62", "v63", "v64", "v65", "v66", "v67", "v68", "v69", "v70", "v71", "v72", "v73", "v74", "v75", "v76", "v77", "v78", "v79", "v80", "v81", "v82", "v83", "v84", "v85", "v86", "v87", "v88", "v89", "v90", "v91", "v92", "v93", "v94", "v95", "v96", "v97", "v98", "v99", "v100", "v101", "v102", "v103", "v104", "v105", "v106", "v107", "v108", "v109", "v110", "v111", "v112", "v113", "v114", "v115", "v116", "v117", "v118", "v119", "v120", "v121", "v122", "v123", "v124", "v125", "v126", "v127", "v128", "v129", "v130", "v131", "v132", "v133", "v134", "v135", "v136", "v137", "v138", "v139", "v140", "v141", "v142", "v143", "v144", "v145", "v146", "v147", "v148", "v149", "v150", "v151", "v152", "v153", "v154", "v155", "v156", "v157", "v158", "v159", "v160", "v161", "v162", "v163", "v164", "v165", "v166", "v167", "v168", "v169", "v170", "v171", "v172", "v173", "v174", "v175", "v176", "v177", "v178", "v179", "v180", "v181", "v182", "v183", "v184", "v185", "v186", "v187", "v188", "v189", "v190", "v191", "v192", "v193", "v194", "v195", "v196", "v197", "v198", "v199", "v200", "v201", "v202", "v203", "v204", "v205", "v206", "v207", "v208", "v209", "v210", "v211", "v212", "v213", "v214", "v215", "v216", "v217", "v218", "v219", "v220", "v221", "v222", "v223", "v224", "v225", "v226", "v227", "v228", "v229", "v230", "v231", "v232", "v233", "v234", "v235", "v236", "v237", "v238", "v239", "v240", "v241", "v242", "v243", "v244", "v245", "v246", "v247", "v248", "v249", "v250", "v251", "v252", "v253", "v254", "v255", "a0", "a1", "a2", "a3", "a4", "a5", "a6", "a7", "a8", "a9", "a10", "a11", "a12", "a13", "a14", "a15", "a16", "a17", "a18", "a19", "a20", "a21", "a22", "a23", "a24", "a25", "a26", "a27", "a28", "a29", "a30", "a31", "a32", "a33", "a34", "a35", "a36", "a37", "a38", "a39", "a40", "a41", "a42", "a43", "a44", "a45", "a46", "a47", "a48", "a49", "a50", "a51", "a52", "a53", "a54", "a55", "a56", "a57", "a58", "a59", "a60", "a61", "a62", "a63", "a64", "a65", "a66", "a67", "a68", "a69", "a70", "a71", "a72", "a73", "a74", "a75", "a76", "a77", "a78", "a79", "a80", "a81", "a82", "a83", "a84", "a85", "a86", "a87", "a88", "a89", "a90", "a91", "a92", "a93", "a94", "a95", "a96", "a97", "a98", "a99", "a100", "a101", "a102", "a103", "a104", "a105", "a106", "a107", "a108", "a109", "a110", "a111", "a112", "a113", "a114", "a115", "a116", "a117", "a118", "a119", "a120", "a121", "a122", "a123", "a124", "a125", "a126", "a127", "a128", "a129", "a130", "a131", "a132", "a133", "a134", "a135", "a136", "a137", "a138", "a139", "a140", "a141", "a142", "a143", "a144", "a145", "a146", "a147", "a148", "a149", "a150", "a151", "a152", "a153", "a154", "a155", "a156", "a157", "a158", "a159", "a160", "a161", "a162", "a163", "a164", "a165", "a166", "a167", "a168", "a169", "a170", "a171", "a172", "a173", "a174", "a175", "a176", "a177", "a178", "a179", "a180", "a181", "a182", "a183", "a184", "a185", "a186", "a187", "a188", "a189", "a190", "a191", "a192", "a193", "a194", "a195", "a196", "a197", "a198", "a199", "a200", "a201", "a202", "a203", "a204", "a205", "a206", "a207", "a208", "a209", "a210", "a211", "a212", "a213", "a214", "a215", "a216", "a217", "a218", "a219", "a220", "a221", "a222", "a223", "a224", "a225", "a226", "a227", "a228", "a229", "a230", "a231", "a232", "a233", "a234", "a235", "a236", "a237", "a238", "a239", "a240", "a241", "a242", "a243", "a244", "a245", "a246", "a247", "a248", "a249", "a250", "a251", "a252", "a253", "a254", "a255", "s4", "s5", "s6", "s7", "s8", "s9", "s10", "s11", "s12", "s13", "s14", "s15", "s16", "s17", "s18", "s19", "s20", "s21", "s22", "s23", "s24", "s25", "s26", "s27", "s28", "s29", "s30", "s31", "s36", "s37", "s38", "s39", "s40", "s41", "s42", "s43", "s44", "s45", "s46", "s47", "s48", "s49", "s50", "s51", "s52", "s53", "s54", "s55", "s56", "s57", "s58", "s59", "s60", "s61", "s62", "s63", "s64", "s65", "s66", "s67", "s68", "s69", "s70", "s71", "s72", "s73", "s74", "s75", "s76", "s77", "s78", "s79", "s80", "s81", "s82", "s83", "s84", "s85", "s86", "s87", "s88", "s89", "s90", "s91", "s92", "s93", "s94", "s95", "s96", "s97", "s98", "s99", "vcc", "scc", "memory");
}

extern "C" void kernel_launch(void* const* d_in, const int* in_sizes, int n_in,
                              void* d_out, int out_size, void* d_ws, size_t ws_size,
                              hipStream_t stream) {
  (void)hipFuncSetAttribute((const void*)qattn_kernel, hipFuncAttributeMaxDynamicSharedMemorySize, LDS_ATT);
  qattn_kernel<<<256, 256, LDS_ATT, stream>>>(d_in[0], d_in[1], d_in[2], d_in[3], d_ws, d_in[4], d_out);
}
```

```cpp
#include <hip/hip_runtime.h>
#include <hip/hip_bf16.h>

constexpr int LDS_ATT = 163840;

__global__ __launch_bounds__(256, 1) __attribute__((aligned(4096))) void qattn_kernel(const void* __restrict__ x, const void* __restrict__ kc, const void* __restrict__ vc,
                                                       const void* __restrict__ wq, void* __restrict__ ws, const void* __restrict__ wo,
                                                       void* __restrict__ out) {
  const unsigned blk = blockIdx.x;
  const unsigned wid = __builtin_amdgcn_readfirstlane(threadIdx.x >> 6);
  auto ka = __builtin_amdgcn_kernarg_segment_ptr();
  asm volatile(
R"A(s_load_dwordx8 s[4:11],%0,0x0
s_load_dwordx4 s[12:15],%0,0x20
s_load_dwordx2 s[16:17],%0,0x30
v_mbcnt_lo_u32_b32 v23,-1,0
v_mbcnt_hi_u32_b32 v23,-1,v23
s_mov_b32 s60,%2
s_and_b32 s66,%1,7
s_lshr_b32 s67,%1,3
s_getpc_b64 s[20:21]
L_pc_%=:
s_and_b32 s20,s20,0xffffff80
s_and_b32 s21,s21,0xffff
s_mov_b32 s22,L_hot_%=-L_pc_%=
s_mov_b32 s23,0x20000
s_lshl_b32 s68,s67,2
s_add_u32 s68,s68,s60
s_mul_i32 s68,s68,896
v_lshlrev_b32 v67,7,v23
s_mov_b64 exec,127
buffer_load_dword v8,v67,s[20:23],s68 offen
s_mov_b64 exec,-1
s_lshr_b32 s61,s66,2
s_lshl_b32 s61,s61,3
s_lshr_b32 s68,s67,2
s_add_u32 s61,s61,s68
s_and_b32 s62,s66,1
s_lshl_b32 s62,s62,2
s_and_b32 s68,s66,2
s_add_u32 s62,s62,s68
s_lshl_b32 s62,s62,1
s_and_b32 s68,s67,3
s_add_u32 s62,s62,s68
s_lshl_b32 s83,s62,9
s_mov_b32 s84,0
s_lshl_b32 s89,s60,13
s_add_u32 s85,s89,65536
s_add_u32 s88,s89,131072
v_and_b32 v20,3,v23
v_bfe_u32 v21,v23,2,2
v_lshrrev_b32 v22,4,v23
v_lshrrev_b32 v75,3,v23
s_lshl_b32 s66,s60,6
v_add_u32 v75,s66,v75
v_mul_u32_u24 v75,1280,v75
v_and_b32 v76,7,v23
v_or_b32 v77,0,v22
v_xor_b32 v77,v77,v76
v_lshl_add_u32 v77,v77,4,v75
v_add_u32 v32,0,v77
v_add_u32 v34,18432,v77
v_add_u32 v36,40960,v77
v_add_u32 v38,59392,v77
v_or_b32 v77,4,v22
v_xor_b32 v77,v77,v76
v_lshl_add_u32 v77,v77,4,v75
v_add_u32 v33,9216,v77
v_add_u32 v35,27648,v77
v_add_u32 v37,50176,v77
v_add_u32 v39,68608,v77
s_lshl_b32 s66,s60,17
v_lshlrev_b32 v75,13,v21
v_lshl_add_u32 v75,v22,6,v75
v_lshl_add_u32 v75,v20,4,v75
v_add_u32 v75,s66,v75
v_add_u32 v40,0,v75
v_add_u32 v41,256,v75
v_add_u32 v42,32768,v75
v_add_u32 v43,33024,v75
v_add_u32 v44,65536,v75
v_add_u32 v45,65792,v75
v_add_u32 v46,98304,v75
v_add_u32 v47,98560,v75
s_mul_i32 s66,s61,307200
s_waitcnt lgkmcnt(0)
s_add_u32 s28,s4,s66
s_addc_u32 s29,s5,0
s_and_b32 s29,s29,0xffff
s_mov_b32 s30,307200
s_mov_b32 s31,0x20000
s_mov_b32 s36,s10
s_and_b32 s37,s11,0xffff
s_mov_b32 s38,5242880
s_mov_b32 s39,0x20000
s_add_u32 m0,s85,0
s_nop 0
buffer_load_dwordx4 v32,s[28:31],s84 offen lds
buffer_load_dwordx4 v33,s[28:31],s84 offen offset:1024 lds
buffer_load_dwordx4 v34,s[28:31],s84 offen offset:2048 lds
buffer_load_dwordx4 v35,s[28:31],s84 offen offset:3072 lds
s_add_u32 m0,s85,4096
s_nop 0
buffer_load_dwordx4 v36,s[28:31],s84 offen lds
buffer_load_dwordx4 v37,s[28:31],s84 offen offset:1024 lds
buffer_load_dwordx4 v38,s[28:31],s84 offen offset:2048 lds
buffer_load_dwordx4 v39,s[28:31],s84 offen offset:3072 lds
s_add_u32 s84,s84,128
s_xor_b32 s85,s85,98304
s_add_u32 m0,s88,0
s_nop 0
buffer_load_dwordx4 v40,s[36:39],s83 offen lds
s_add_u32 m0,s88,1024
s_nop 0
buffer_load_dwordx4 v41,s[36:39],s83 offen lds
s_add_u32 m0,s88,2048
s_nop 0
buffer_load_dwordx4 v42,s[36:39],s83 offen lds
s_add_u32 m0,s88,3072
s_nop 0
buffer_load_dwordx4 v43,s[36:39],s83 offen lds
s_add_u32 m0,s88,4096
s_nop 0
buffer_load_dwordx4 v44,s[36:39],s83 offen lds
s_add_u32 m0,s88,5120
s_nop 0
buffer_load_dwordx4 v45,s[36:39],s83 offen lds
s_add_u32 m0,s88,6144
s_nop 0
buffer_load_dwordx4 v46,s[36:39],s83 offen lds
s_add_u32 m0,s88,7168
s_nop 0
buffer_load_dwordx4 v47,s[36:39],s83 offen lds
s_add_u32 s83,s83,524288
s_lshl_b32 s66,s61,12
s_add_u32 s66,s66,104857600
s_add_u32 s44,s12,s66
s_addc_u32 s45,s13,0
s_and_b32 s45,s45,0xffff
s_mov_b32 s46,4096
s_mov_b32 s47,0x20000
v_mov_b32 v10,0
buffer_load_dword v9,v10,s[44:47],0 offen sc1
v_lshlrev_b32 v6,4,v23
v_lshlrev_b32 v72,12,v22
v_lshl_add_u32 v72,v21,6,v72
v_lshl_add_u32 v72,v20,4,v72
v_subrev_u32 v51,s89,v72
v_add_u32 v50,131072,v72
v_and_b32 v74,15,v23
v_lshrrev_b32 v76,1,v74
v_lshlrev_b32 v75,7,v74
s_add_u32 s66,s89,65536
v_add_u32 v75,s66,v75
v_or_b32 v77,0,v22
v_xor_b32 v77,v77,v76
v_lshl_add_u32 v48,v77,4,v75
v_or_b32 v77,4,v22
v_xor_b32 v77,v77,v76
v_lshl_add_u32 v49,v77,4,v75
s_add_u32 s86,s89,131072
s_add_u32 s87,s89,98304
s_mov_b64 exec,0
s_cmp_eq_u32 s60,0
s_cbranch_scc1 L_pd1_%=
s_cmp_eq_u32 s60,1
s_cbranch_scc1 L_pd2_%=
s_cmp_eq_u32 s60,2
s_cbranch_scc1 L_pd3_%=
s_branch L_pd4_%=
L_trt0_%=:
s_mov_b64 exec,-1
s_mov_b32 s88,s87
s_add_u32 m0,s88,0
s_nop 0
buffer_load_dwordx4 v40,s[36:39],s83 offen lds
s_add_u32 m0,s88,1024
s_nop 0
buffer_load_dwordx4 v41,s[36:39],s83 offen lds
s_add_u32 m0,s88,2048
s_nop 0
buffer_load_dwordx4 v42,s[36:39],s83 offen lds
s_add_u32 m0,s88,3072
s_nop 0
buffer_load_dwordx4 v43,s[36:39],s83 offen lds
s_add_u32 m0,s88,4096
s_nop 0
buffer_load_dwordx4 v44,s[36:39],s83 offen lds
s_add_u32 m0,s88,5120
s_nop 0
buffer_load_dwordx4 v45,s[36:39],s83 offen lds
s_add_u32 m0,s88,6144
s_nop 0
buffer_load_dwordx4 v46,s[36:39],s83 offen lds
s_add_u32 m0,s88,7168
s_nop 0
buffer_load_dwordx4 v47,s[36:39],s83 offen lds
s_add_u32 s83,s83,524288
s_add_u32 s88,s89,0
s_mov_b64 exec,0
s_cmp_eq_u32 s60,0
s_cbranch_scc1 L_pd9_%=
s_cmp_eq_u32 s60,1
s_cbranch_scc1 L_pd10_%=
s_cmp_eq_u32 s60,2
s_cbranch_scc1 L_pd11_%=
s_branch L_pd12_%=
L_t1b_%=:
s_cmp_ge_u32 s62,8
s_cbranch_scc1 L_pd19_%=
s_branch L_pd14_%=
L_t1c_%=:
s_cmp_ge_u32 s62,8
s_cbranch_scc1 L_pd20_%=
s_branch L_pd15_%=
s_nop 0
s_nop 0
s_nop 0
s_nop 0
s_nop 0
s_nop 0
L_trt1_%=:
s_mov_b64 exec,-1
s_add_u32 m0,s85,0
s_nop 0
buffer_load_dwordx4 v32,s[28:31],s84 offen lds
buffer_load_dwordx4 v33,s[28:31],s84 offen offset:1024 lds
buffer_load_dwordx4 v34,s[28:31],s84 offen offset:2048 lds
buffer_load_dwordx4 v35,s[28:31],s84 offen offset:3072 lds
s_add_u32 m0,s85,4096
s_nop 0
buffer_load_dwordx4 v36,s[28:31],s84 offen lds
buffer_load_dwordx4 v37,s[28:31],s84 offen offset:1024 lds
buffer_load_dwordx4 v38,s[28:31],s84 offen offset:2048 lds
buffer_load_dwordx4 v39,s[28:31],s84 offen offset:3072 lds
s_add_u32 s84,s84,128
s_xor_b32 s85,s85,98304
s_branch L_pacx_%=
L_pace_%=:
s_sub_u32 s5,s4,1
s_max_i32 s5,s5,s7
L_pacl_%=:
s_sub_u32 s4,s4,1
s_cmp_eq_u32 s4,0
s_cbranch_scc1 L_pace2_%=
s_nop 2
s_branch L_pacl_%=
L_pace2_%=:
s_setpc_b64 s[92:93]
L_pacx_%=:
s_waitcnt vmcnt(25)
ds_read_b128 a[192:195],v48
ds_read_b128 a[196:199],v49
ds_read_b128 a[200:203],v48 offset:2048
ds_read_b128 a[204:207],v49 offset:2048
ds_read_b128 a[208:211],v48 offset:4096
ds_read_b128 a[212:215],v49 offset:4096
ds_read_b128 a[216:219],v48 offset:6144
ds_read_b128 a[220:223],v49 offset:6144
s_waitcnt vmcnt(17)
s_barrier
ds_read_b64_tr_b16 a[64:65],v50
ds_read_b64_tr_b16 a[66:67],v50 offset:2048
ds_read_b64_tr_b16 a[68:69],v50 offset:16384
ds_read_b64_tr_b16 a[70:71],v50 offset:18432
ds_read_b64_tr_b16 a[72:73],v50 offset:8
ds_read_b64_tr_b16 a[74:75],v50 offset:2056
ds_read_b64_tr_b16 a[76:77],v50 offset:16392
ds_read_b64_tr_b16 a[78:79],v50 offset:18440
s_waitcnt lgkmcnt(6)
v_mfma_f32_16x16x32_bf16 a[0:3],a[64:67],a[192:195],0
s_add_u32 m0,s88,0
ds_read_b64_tr_b16 a[80:81],v50 offset:256
v_xor_b32 v48,98304,v48
s_waitcnt lgkmcnt(5)
v_mfma_f32_16x16x32_bf16 a[0:3],a[68:71],a[196:199],a[0:3]
buffer_load_dwordx4 v40,s[36:39],s83 offen lds
v_mfma_f32_16x16x32_bf16 a[16:19],a[68:71],a[204:207],0
s_mov_b32 s69,s86
ds_read_b64_tr_b16 a[82:83],v50 offset:2304
v_xor_b32 v49,98304,v49
v_mfma_f32_16x16x32_bf16 a[16:19],a[64:67],a[200:203],a[16:19]
s_mov_b32 s86,s87
s_mov_b32 s87,s88
ds_read_b64_tr_b16 a[84:85],v50 offset:16640
v_mfma_f32_16x16x32_bf16 a[32:35],a[64:67],a[208:211],0
ds_read_b64_tr_b16 a[86:87],v50 offset:18688
v_mfma_f32_16x16x32_bf16 a[32:35],a[68:71],a[212:215],a[32:35]
ds_read_b64_tr_b16 a[88:89],v50 offset:264
v_mfma_f32_16x16x32_bf16 a[48:51],a[68:71],a[220:223],0
s_add_u32 m0,s88,1024
v_mfma_f32_16x16x32_bf16 a[48:51],a[64:67],a[216:219],a[48:51]
s_waitcnt lgkmcnt(5)
ds_read_b64_tr_b16 a[90:91],v50 offset:2312
v_mfma_f32_16x16x32_bf16 a[4:7],a[72:75],a[192:195],0
buffer_load_dwordx4 v41,s[36:39],s83 offen lds
v_mfma_f32_16x16x32_bf16 a[4:7],a[76:79],a[196:199],a[4:7]
s_lshl_b32 s67,s60,12
ds_read_b64_tr_b16 a[92:93],v50 offset:16648
v_cmp_lt_u32 vcc,6,v23
v_mfma_f32_16x16x32_bf16 a[20:23],a[76:79],a[204:207],0
s_lshl_b32 s65,s61,5
ds_read_b64_tr_b16 a[94:95],v50 offset:18696
v_lshlrev_b32 v19,3,v6
v_mfma_f32_16x16x32_bf16 a[20:23],a[72:75],a[200:203],a[20:23]
s_lshl_b32 s66,s62,1
ds_read_b64_tr_b16 a[96:97],v50 offset:512
v_cndmask_b32_e64 v19,v19,-1,vcc
v_mfma_f32_16x16x32_bf16 a[36:39],a[72:75],a[208:211],0
s_add_u32 s65,s65,s66
ds_read_b64_tr_b16 a[98:99],v50 offset:2560
v_lshrrev_b32 v18,1,v19
v_mfma_f32_16x16x32_bf16 a[36:39],a[76:79],a[212:215],a[36:39]
s_add_u32 m0,s88,2048
v_min_u32 v19,v19,v19
v_cndmask_b32_e64 v19,v19,v18,vcc
v_mfma_f32_16x16x32_bf16 a[52:55],a[76:79],a[220:223],0
s_lshl_b32 s66,s65,18
ds_read_b64_tr_b16 a[100:101],v50 offset:16896
v_lshlrev_b32 v11,8,v21
v_mfma_f32_16x16x32_bf16 a[52:55],a[72:75],a[216:219],a[52:55]
buffer_load_dwordx4 v42,s[36:39],s83 offen lds
s_waitcnt lgkmcnt(5)
v_mfma_f32_16x16x32_bf16 a[8:11],a[80:83],a[192:195],0
s_lshr_b32 s63,s62,2
ds_read_b64_tr_b16 a[102:103],v50 offset:18944
v_lshl_add_u32 v11,v22,6,v11
v_mfma_f32_16x16x32_bf16 a[8:11],a[84:87],a[196:199],a[8:11]
s_mov_b32 s22,524288
ds_read_b64_tr_b16 a[104:105],v50 offset:520
v_add_u32 v11,s67,v11
v_mfma_f32_16x16x32_bf16 a[24:27],a[84:87],a[204:207],0
s_mov_b32 s23,0x20000
ds_read_b64_tr_b16 a[106:107],v50 offset:2568
v_lshl_add_u32 v4,v20,4,v11
v_mfma_f32_16x16x32_bf16 a[24:27],a[80:83],a[200:203],a[24:27]
s_mov_b32 s26,524288
ds_read_b64_tr_b16 a[108:109],v50 offset:16904
v_xor_b32 v12,2,v20
v_mfma_f32_16x16x32_bf16 a[40:43],a[80:83],a[208:211],0
s_add_u32 m0,s88,3072
v_lshl_add_u32 v5,v12,4,v11
v_lshlrev_b32 v11,10,v21
v_mfma_f32_16x16x32_bf16 a[40:43],a[84:87],a[212:215],a[40:43]
s_mov_b32 s27,0x20000
ds_read_b64_tr_b16 a[110:111],v50 offset:18952
v_lshl_add_u32 v11,v20,6,v11
v_mfma_f32_16x16x32_bf16 a[56:59],a[84:87],a[220:223],0
buffer_load_dwordx4 v43,s[36:39],s83 offen lds
v_mfma_f32_16x16x32_bf16 a[56:59],a[80:83],a[216:219],a[56:59]
s_mov_b32 s42,131072
ds_read_b64_tr_b16 a[112:113],v50 offset:768
v_and_b32 v12,2,v21
v_mfma_f32_16x16x32_bf16 a[12:15],a[88:91],a[192:195],0
s_mov_b32 s43,0x20000
ds_read_b64_tr_b16 a[114:115],v50 offset:2816
v_xor_b32 v12,v12,v22
s_waitcnt lgkmcnt(6)
v_mfma_f32_16x16x32_bf16 a[12:15],a[92:95],a[196:199],a[12:15]
s_mov_b32 s48,s14
ds_read_b64_tr_b16 a[116:117],v50 offset:17152
v_lshl_add_u32 v11,v12,4,v11
v_mfma_f32_16x16x32_bf16 a[28:31],a[92:95],a[204:207],0
s_and_b32 s49,s15,0xffff
ds_read_b64_tr_b16 a[118:119],v50 offset:19200
v_add_u32 v1,98304,v11
v_mfma_f32_16x16x32_bf16 a[28:31],a[88:91],a[200:203],a[28:31]
s_add_u32 m0,s88,4096
v_lshlrev_b32 v11,10,v22
v_lshl_add_u32 v11,v21,6,v11
v_mfma_f32_16x16x32_bf16 a[44:47],a[88:91],a[208:211],0
s_mov_b32 s50,5242880
ds_read_b64_tr_b16 a[120:121],v50 offset:776
v_lshl_add_u32 v11,v20,3,v11
v_mfma_f32_16x16x32_bf16 a[44:47],a[92:95],a[212:215],a[44:47]
buffer_load_dwordx4 v44,s[36:39],s83 offen lds
v_mfma_f32_16x16x32_bf16 a[60:63],a[92:95],a[220:223],0
s_mov_b32 s51,0x20000
ds_read_b64_tr_b16 a[122:123],v50 offset:2824
v_add_u32 v11,114688,v11
v_mfma_f32_16x16x32_bf16 a[60:63],a[88:91],a[216:219],a[60:63]
s_add_u32 s76,s67,98304
ds_read_b64_tr_b16 a[124:125],v50 offset:17160
v_and_b32 v12,1,v22
v_mfma_f32_16x16x32_bf16 v[64:67],a[96:99],a[192:195],0
s_mul_i32 s74,s61,5
ds_read_b64_tr_b16 a[126:127],v50 offset:19208
v_lshl_add_u32 v2,v12,5,v11
v_mfma_f32_16x16x32_bf16 v[64:67],a[100:103],a[196:199],v[64:67]
s_add_u32 s74,s74,s62
ds_read_b64_tr_b16 a[64:65],v50 offset:1024
v_xor_b32 v12,1,v12
v_mfma_f32_16x16x32_bf16 v[80:83],a[100:103],a[204:207],0
s_add_u32 m0,s88,5120
v_lshl_add_u32 v3,v12,5,v11
v_add_u32 v11,0,v23
v_mfma_f32_16x16x32_bf16 v[80:83],a[96:99],a[200:203],v[80:83]
s_and_b32 s74,s74,15
ds_read_b64_tr_b16 a[66:67],v50 offset:3072
v_mul_u32_u24 v12,3277,v11
v_mfma_f32_16x16x32_bf16 v[96:99],a[96:99],a[208:211],0
buffer_load_dwordx4 v45,s[36:39],s83 offen lds
v_mfma_f32_16x16x32_bf16 v[96:99],a[100:103],a[212:215],v[96:99]
s_lshl_b32 s74,s74,14
ds_read_b64_tr_b16 a[68:69],v50 offset:17408
v_lshrrev_b32 v12,16,v12
v_mfma_f32_16x16x32_bf16 v[112:115],a[100:103],a[220:223],0
s_mov_b32 s75,0
ds_read_b64_tr_b16 a[70:71],v50 offset:19456
v_mul_u32_u24 v13,20,v12
v_mfma_f32_16x16x32_bf16 v[112:115],a[96:99],a[216:219],v[112:115]
s_mov_b32 s77,98304
)A"
R"A(ds_read_b64_tr_b16 a[72:73],v50 offset:1032
v_sub_u32 v13,v11,v13
s_waitcnt lgkmcnt(6)
v_mfma_f32_16x16x32_bf16 v[68:71],a[104:107],a[192:195],0
s_mov_b32 s71,0x3e0293ee
ds_read_b64_tr_b16 a[74:75],v50 offset:3080
v_bfe_u32 v14,v12,2,1
v_mfma_f32_16x16x32_bf16 v[68:71],a[108:111],a[196:199],v[68:71]
s_add_u32 m0,s88,6144
v_lshlrev_b32 v14,1,v14
v_xor_b32 v13,v13,v14
v_mfma_f32_16x16x32_bf16 v[84:87],a[108:111],a[204:207],0
s_mov_b32 s73,0x53800000
ds_read_b64_tr_b16 a[76:77],v50 offset:17416
v_lshrrev_b32 v14,2,v13
v_mfma_f32_16x16x32_bf16 v[84:87],a[104:107],a[200:203],v[84:87]
buffer_load_dwordx4 v46,s[36:39],s83 offen lds
v_mfma_f32_16x16x32_bf16 v[100:103],a[104:107],a[208:211],0
s_lshl_b32 s79,s60,14
ds_read_b64_tr_b16 a[78:79],v50 offset:19464
v_cmp_eq_u32 vcc,3,v14
v_mfma_f32_16x16x32_bf16 v[100:103],a[108:111],a[212:215],v[100:103]
s_add_u32 s20,s6,s66
s_addc_u32 s21,s7,0
ds_read_b64_tr_b16 a[80:81],v50 offset:1280
v_mfma_f32_16x16x32_bf16 v[116:119],a[108:111],a[220:223],0
s_add_u32 s24,s8,s66
s_addc_u32 s25,s9,0
ds_read_b64_tr_b16 a[82:83],v50 offset:3328
v_mfma_f32_16x16x32_bf16 v[116:119],a[104:107],a[216:219],v[116:119]
s_lshl_b32 s66,s65,16
s_add_u32 s65,s63,1
ds_read_b64_tr_b16 a[84:85],v50 offset:17664
v_mfma_f32_16x16x32_bf16 v[72:75],a[112:115],a[192:195],0
s_add_u32 m0,s88,7168
s_add_u32 s40,s12,s66
s_addc_u32 s41,s13,0
v_mfma_f32_16x16x32_bf16 v[72:75],a[116:119],a[196:199],v[72:75]
s_mul_i32 s66,s60,40960
ds_read_b64_tr_b16 a[86:87],v50 offset:19712
v_min_u32 v15,3,v14
v_mfma_f32_16x16x32_bf16 v[88:91],a[116:119],a[204:207],0
buffer_load_dwordx4 v47,s[36:39],s83 offen lds
v_mfma_f32_16x16x32_bf16 v[88:91],a[112:115],a[200:203],v[88:91]
s_add_u32 s83,s83,524288
ds_read_b64_tr_b16 a[88:89],v50 offset:1288
v_add_u32 v15,s65,v15
v_mfma_f32_16x16x32_bf16 v[104:107],a[112:115],a[208:211],0
s_mov_b32 s88,s69
ds_read_b64_tr_b16 a[90:91],v50 offset:3336
v_and_b32 v15,3,v15
v_mfma_f32_16x16x32_bf16 v[104:107],a[116:119],a[212:215],v[104:107]
s_and_b32 s21,s21,0xffff
ds_read_b64_tr_b16 a[92:93],v50 offset:17672
v_cndmask_b32_e64 v15,v15,4,vcc
v_mfma_f32_16x16x32_bf16 v[120:123],a[116:119],a[220:223],0
s_and_b32 s25,s25,0xffff
ds_read_b64_tr_b16 a[94:95],v50 offset:19720
v_and_b32 v13,3,v13
v_mfma_f32_16x16x32_bf16 v[120:123],a[112:115],a[216:219],v[120:123]
s_add_u32 m0,s85,0
v_lshl_add_u32 v13,v15,2,v13
v_mul_u32_u24 v12,1280,v12
v_mfma_f32_16x16x32_bf16 v[76:79],a[120:123],a[192:195],0
s_and_b32 s41,s41,0xffff
ds_read_b64_tr_b16 a[96:97],v50 offset:1536
v_lshl_add_u32 v12,v13,4,v12
s_waitcnt lgkmcnt(6)
v_mfma_f32_16x16x32_bf16 v[76:79],a[124:127],a[196:199],v[76:79]
buffer_load_dwordx4 v32,s[28:31],s84 offen lds
v_mfma_f32_16x16x32_bf16 v[92:95],a[124:127],a[204:207],0
s_add_u32 s80,s79,4096
ds_read_b64_tr_b16 a[98:99],v50 offset:3584
v_add_u32 v12,s66,v12
v_mfma_f32_16x16x32_bf16 v[92:95],a[120:123],a[200:203],v[92:95]
s_add_u32 s81,s79,8192
ds_read_b64_tr_b16 a[100:101],v50 offset:17920
v_subrev_u32 v24,0,v12
v_mfma_f32_16x16x32_bf16 v[108:111],a[120:123],a[208:211],0
s_add_u32 s82,s79,12288
ds_read_b64_tr_b16 a[102:103],v50 offset:19968
v_add_u32 v11,64,v23
v_mfma_f32_16x16x32_bf16 v[108:111],a[124:127],a[212:215],v[108:111]
s_and_b32 s64,s62,3
ds_read_b64_tr_b16 a[104:105],v50 offset:1544
v_mul_u32_u24 v12,3277,v11
v_mfma_f32_16x16x32_bf16 v[124:127],a[124:127],a[220:223],0
s_mov_b32 s72,0xff800000
v_lshrrev_b32 v12,16,v12
v_mul_u32_u24 v13,20,v12
v_mfma_f32_16x16x32_bf16 v[124:127],a[120:123],a[216:219],v[124:127]
s_lshl_b32 s18,s60,24
ds_read_b64_tr_b16 a[106:107],v50 offset:3592
v_sub_u32 v13,v11,v13
v_mfma_f32_16x16x32_bf16 v[128:131],a[64:67],a[192:195],0
buffer_load_dwordx4 v33,s[28:31],s84 offen offset:1024 lds
v_mfma_f32_16x16x32_bf16 v[128:131],a[68:71],a[196:199],v[128:131]
s_lshl_b32 s68,s62,6
ds_read_b64_tr_b16 a[108:109],v50 offset:17928
v_bfe_u32 v14,v12,2,1
v_mfma_f32_16x16x32_bf16 v[160:163],a[68:71],a[204:207],0
s_add_u32 s18,s18,s68
ds_read_b64_tr_b16 a[110:111],v50 offset:19976
v_lshlrev_b32 v14,1,v14
v_mfma_f32_16x16x32_bf16 v[160:163],a[64:67],a[200:203],v[160:163]
s_lshl_b32 s90,%1,2
ds_read_b64_tr_b16 a[112:113],v50 offset:1792
v_xor_b32 v13,v13,v14
v_mfma_f32_16x16x32_bf16 v[192:195],a[64:67],a[208:211],0
s_add_u32 s90,s90,s60
ds_read_b64_tr_b16 a[114:115],v50 offset:3840
v_lshrrev_b32 v14,2,v13
v_mfma_f32_16x16x32_bf16 v[192:195],a[68:71],a[212:215],v[192:195]
s_mul_i32 s90,s90,5120
v_cmp_eq_u32 vcc,3,v14
v_min_u32 v15,3,v14
v_mfma_f32_16x16x32_bf16 v[224:227],a[68:71],a[220:223],0
ds_read_b64_tr_b16 a[116:117],v50 offset:18176
v_add_u32 v15,s65,v15
v_mfma_f32_16x16x32_bf16 v[224:227],a[64:67],a[216:219],v[224:227]
buffer_load_dwordx4 v34,s[28:31],s84 offen offset:2048 lds
v_mfma_f32_16x16x32_bf16 v[132:135],a[72:75],a[192:195],0
ds_read_b64_tr_b16 a[118:119],v50 offset:20224
v_and_b32 v15,3,v15
v_mfma_f32_16x16x32_bf16 v[132:135],a[76:79],a[196:199],v[132:135]
ds_read_b64_tr_b16 a[120:121],v50 offset:1800
v_cndmask_b32_e64 v15,v15,4,vcc
v_mfma_f32_16x16x32_bf16 v[164:167],a[76:79],a[204:207],0
ds_read_b64_tr_b16 a[122:123],v50 offset:3848
v_and_b32 v13,3,v13
v_mfma_f32_16x16x32_bf16 v[164:167],a[72:75],a[200:203],v[164:167]
ds_read_b64_tr_b16 a[124:125],v50 offset:18184
v_lshl_add_u32 v13,v15,2,v13
v_mfma_f32_16x16x32_bf16 v[196:199],a[72:75],a[208:211],0
v_mul_u32_u24 v12,1280,v12
v_lshl_add_u32 v12,v13,4,v12
v_mfma_f32_16x16x32_bf16 v[196:199],a[76:79],a[212:215],v[196:199]
ds_read_b64_tr_b16 a[126:127],v50 offset:20232
v_add_u32 v50,s86,v51
v_mfma_f32_16x16x32_bf16 v[228:231],a[76:79],a[220:223],0
buffer_load_dwordx4 v35,s[28:31],s84 offen offset:3072 lds
v_mfma_f32_16x16x32_bf16 v[228:231],a[72:75],a[216:219],v[228:231]
v_add_u32 v12,s66,v12
v_subrev_u32 v25,1024,v12
v_mfma_f32_16x16x32_bf16 v[136:139],a[80:83],a[192:195],0
s_waitcnt lgkmcnt(4)
v_add_u32 v11,128,v23
v_mul_u32_u24 v12,3277,v11
v_mfma_f32_16x16x32_bf16 v[136:139],a[84:87],a[196:199],v[136:139]
v_lshrrev_b32 v12,16,v12
v_mul_u32_u24 v13,20,v12
v_mfma_f32_16x16x32_bf16 v[168:171],a[84:87],a[204:207],0
v_sub_u32 v13,v11,v13
v_bfe_u32 v14,v12,2,1
v_mfma_f32_16x16x32_bf16 v[168:171],a[80:83],a[200:203],v[168:171]
s_add_u32 m0,s85,4096
s_xor_b32 s85,s85,98304
v_lshlrev_b32 v14,1,v14
v_mfma_f32_16x16x32_bf16 v[200:203],a[80:83],a[208:211],0
v_xor_b32 v13,v13,v14
v_lshrrev_b32 v14,2,v13
v_mfma_f32_16x16x32_bf16 v[200:203],a[84:87],a[212:215],v[200:203]
buffer_load_dwordx4 v36,s[28:31],s84 offen lds
v_mfma_f32_16x16x32_bf16 v[232:235],a[84:87],a[220:223],0
v_cmp_eq_u32 vcc,3,v14
v_min_u32 v15,3,v14
v_mfma_f32_16x16x32_bf16 v[232:235],a[80:83],a[216:219],v[232:235]
v_add_u32 v15,s65,v15
v_and_b32 v15,3,v15
v_mfma_f32_16x16x32_bf16 v[140:143],a[88:91],a[192:195],0
v_cndmask_b32_e64 v15,v15,4,vcc
v_and_b32 v13,3,v13
v_mfma_f32_16x16x32_bf16 v[140:143],a[92:95],a[196:199],v[140:143]
v_lshl_add_u32 v13,v15,2,v13
v_mul_u32_u24 v12,1280,v12
v_mfma_f32_16x16x32_bf16 v[172:175],a[92:95],a[204:207],0
v_lshl_add_u32 v12,v13,4,v12
v_add_u32 v12,s66,v12
v_mfma_f32_16x16x32_bf16 v[172:175],a[88:91],a[200:203],v[172:175]
v_subrev_u32 v26,2048,v12
v_add_u32 v11,192,v23
v_mfma_f32_16x16x32_bf16 v[204:207],a[88:91],a[208:211],0
buffer_load_dwordx4 v37,s[28:31],s84 offen offset:1024 lds
v_mfma_f32_16x16x32_bf16 v[204:207],a[92:95],a[212:215],v[204:207]
v_mul_u32_u24 v12,3277,v11
v_lshrrev_b32 v12,16,v12
v_mfma_f32_16x16x32_bf16 v[236:239],a[92:95],a[220:223],0
v_mul_u32_u24 v13,20,v12
v_sub_u32 v13,v11,v13
v_mfma_f32_16x16x32_bf16 v[236:239],a[88:91],a[216:219],v[236:239]
v_bfe_u32 v14,v12,2,1
v_lshlrev_b32 v14,1,v14
s_waitcnt vmcnt(22) lgkmcnt(0)
s_barrier
v_mfma_f32_16x16x32_bf16 v[144:147],a[96:99],a[192:195],0
s_waitcnt vmcnt(14)
ds_read_b128 a[224:227],v48
v_xor_b32 v13,v13,v14
v_mfma_f32_16x16x32_bf16 v[144:147],a[100:103],a[196:199],v[144:147]
ds_read_b128 a[228:231],v49
v_lshrrev_b32 v14,2,v13
v_mfma_f32_16x16x32_bf16 v[176:179],a[100:103],a[204:207],0
buffer_load_dwordx4 v38,s[28:31],s84 offen offset:2048 lds
v_mfma_f32_16x16x32_bf16 v[176:179],a[96:99],a[200:203],v[176:179]
ds_read_b128 a[232:235],v48 offset:2048
v_cmp_eq_u32 vcc,3,v14
v_mfma_f32_16x16x32_bf16 v[208:211],a[96:99],a[208:211],0
ds_read_b128 a[236:239],v49 offset:2048
v_min_u32 v15,3,v14
v_mfma_f32_16x16x32_bf16 v[208:211],a[100:103],a[212:215],v[208:211]
ds_read_b128 a[240:243],v48 offset:4096
v_add_u32 v15,s65,v15
v_mfma_f32_16x16x32_bf16 v[240:243],a[100:103],a[220:223],0
ds_read_b128 a[244:247],v49 offset:4096
v_and_b32 v15,3,v15
v_mfma_f32_16x16x32_bf16 v[240:243],a[96:99],a[216:219],v[240:243]
v_cndmask_b32_e64 v15,v15,4,vcc
v_and_b32 v13,3,v13
v_mfma_f32_16x16x32_bf16 v[148:151],a[104:107],a[192:195],0
ds_read_b128 a[248:251],v48 offset:6144
v_lshl_add_u32 v13,v15,2,v13
v_mfma_f32_16x16x32_bf16 v[148:151],a[108:111],a[196:199],v[148:151]
buffer_load_dwordx4 v39,s[28:31],s84 offen offset:3072 lds
v_mfma_f32_16x16x32_bf16 v[180:183],a[108:111],a[204:207],0
s_add_u32 s84,s84,128
ds_read_b128 a[252:255],v49 offset:6144
v_mul_u32_u24 v12,1280,v12
v_mfma_f32_16x16x32_bf16 v[180:183],a[104:107],a[200:203],v[180:183]
ds_read_b64_tr_b16 a[64:65],v50
v_lshl_add_u32 v12,v13,4,v12
v_mfma_f32_16x16x32_bf16 v[212:215],a[104:107],a[208:211],0
ds_read_b64_tr_b16 a[66:67],v50 offset:2048
v_add_u32 v12,s66,v12
v_mfma_f32_16x16x32_bf16 v[212:215],a[108:111],a[212:215],v[212:215]
ds_read_b64_tr_b16 a[68:69],v50 offset:16384
v_subrev_u32 v27,3072,v12
s_nop 0
L_pd1_%=:
s_cbranch_execz L_pd5_%=
v_mfma_f32_16x16x32_bf16 v[244:247],a[108:111],a[220:223],0
v_add_u32 v11,256,v23
v_mul_u32_u24 v12,3277,v11
v_mfma_f32_16x16x32_bf16 v[244:247],a[104:107],a[216:219],v[244:247]
ds_read_b64_tr_b16 a[70:71],v50 offset:18432
v_lshrrev_b32 v12,16,v12
v_mfma_f32_16x16x32_bf16 v[152:155],a[112:115],a[192:195],0
ds_read_b64_tr_b16 a[72:73],v50 offset:8
v_mul_u32_u24 v13,20,v12
v_mfma_f32_16x16x32_bf16 v[152:155],a[116:119],a[196:199],v[152:155]
ds_read_b64_tr_b16 a[74:75],v50 offset:2056
v_sub_u32 v13,v11,v13
v_mfma_f32_16x16x32_bf16 v[184:187],a[116:119],a[204:207],0
v_bfe_u32 v14,v12,2,1
v_lshlrev_b32 v14,1,v14
v_mfma_f32_16x16x32_bf16 v[184:187],a[112:115],a[200:203],v[184:187]
ds_read_b64_tr_b16 a[76:77],v50 offset:16392
v_xor_b32 v13,v13,v14
v_mfma_f32_16x16x32_bf16 v[216:219],a[112:115],a[208:211],0
ds_read_b64_tr_b16 a[78:79],v50 offset:18440
v_lshrrev_b32 v14,2,v13
v_mfma_f32_16x16x32_bf16 v[216:219],a[116:119],a[212:215],v[216:219]
ds_read_b64_tr_b16 a[80:81],v50 offset:256
v_cmp_eq_u32 vcc,3,v14
v_mfma_f32_16x16x32_bf16 v[248:251],a[116:119],a[220:223],0
s_cmp_eq_u32 s60,3
s_cselect_b32 s72,0x7149f2ca,s72
v_readfirstlane_b32 s52,v9
v_mfma_f32_16x16x32_bf16 v[248:251],a[112:115],a[216:219],v[248:251]
s_add_u32 s52,s52,1
s_xor_b32 s53,s52,0x9e3779b9
ds_read_b64_tr_b16 a[82:83],v50 offset:2304
v_mfma_f32_16x16x32_bf16 v[156:159],a[120:123],a[192:195],0
ds_read_b64_tr_b16 a[84:85],v50 offset:16640
v_mov_b32 v28,0x3f803f80
v_mfma_f32_16x16x32_bf16 v[156:159],a[124:127],a[196:199],v[156:159]
ds_read_b64_tr_b16 a[86:87],v50 offset:18688
v_mov_b32 v29,0x3f803f80
v_mfma_f32_16x16x32_bf16 v[188:191],a[124:127],a[204:207],0
v_mov_b32 v30,0x3f803f80
v_mov_b32 v31,0x3f803f80
v_mfma_f32_16x16x32_bf16 v[188:191],a[120:123],a[200:203],v[188:191]
ds_read_b64_tr_b16 a[88:89],v50 offset:264
v_min_u32 v15,3,v14
v_mfma_f32_16x16x32_bf16 v[220:223],a[120:123],a[208:211],0
ds_read_b64_tr_b16 a[90:91],v50 offset:2312
v_add_u32 v15,s65,v15
v_mfma_f32_16x16x32_bf16 v[220:223],a[124:127],a[212:215],v[220:223]
ds_read_b64_tr_b16 a[92:93],v50 offset:16648
v_and_b32 v15,3,v15
v_mfma_f32_16x16x32_bf16 v[252:255],a[124:127],a[220:223],0
v_cndmask_b32_e64 v15,v15,4,vcc
v_and_b32 v13,3,v13
v_mfma_f32_16x16x32_bf16 v[252:255],a[120:123],a[216:219],v[252:255]
ds_read_b64_tr_b16 a[94:95],v50 offset:18696
v_lshl_add_u32 v13,v15,2,v13
v_mul_u32_u24 v12,1280,v12
v_lshl_add_u32 v12,v13,4,v12
v_add_u32 v12,s66,v12
v_subrev_u32 v7,0,v12
s_mov_b32 s70,3
s_memrealtime s[8:9]
L_qp_%=:
s_waitcnt lgkmcnt(6)
v_mfma_f32_16x16x32_bf16 a[0:3],a[64:67],a[224:227],a[0:3]
s_add_u32 m0,s88,0
)A"
R"A(ds_read_b64_tr_b16 a[96:97],v50 offset:512
v_xor_b32 v48,98304,v48
v_mfma_f32_16x16x32_bf16 a[0:3],a[68:71],a[228:231],a[0:3]
buffer_load_dwordx4 v40,s[36:39],s83 offen lds
v_mfma_f32_16x16x32_bf16 a[16:19],a[68:71],a[236:239],a[16:19]
s_mov_b32 s69,s86
ds_read_b64_tr_b16 a[98:99],v50 offset:2560
v_xor_b32 v49,98304,v49
v_mfma_f32_16x16x32_bf16 a[16:19],a[64:67],a[232:235],a[16:19]
s_mov_b32 s86,s87
s_mov_b32 s87,s88
ds_read_b64_tr_b16 a[100:101],v50 offset:16896
v_mfma_f32_16x16x32_bf16 a[32:35],a[64:67],a[240:243],a[32:35]
ds_read_b64_tr_b16 a[102:103],v50 offset:18944
v_mfma_f32_16x16x32_bf16 a[32:35],a[68:71],a[244:247],a[32:35]
ds_read_b64_tr_b16 a[104:105],v50 offset:520
v_mfma_f32_16x16x32_bf16 a[48:51],a[68:71],a[252:255],a[48:51]
s_add_u32 m0,s88,1024
v_mfma_f32_16x16x32_bf16 a[48:51],a[64:67],a[248:251],a[48:51]
ds_read_b64_tr_b16 a[106:107],v50 offset:2568
v_mfma_f32_16x16x32_bf16 a[4:7],a[72:75],a[224:227],a[4:7]
buffer_load_dwordx4 v41,s[36:39],s83 offen lds
v_mfma_f32_16x16x32_bf16 a[4:7],a[76:79],a[228:231],a[4:7]
ds_read_b64_tr_b16 a[108:109],v50 offset:16904
v_mfma_f32_16x16x32_bf16 a[20:23],a[76:79],a[236:239],a[20:23]
ds_read_b64_tr_b16 a[110:111],v50 offset:18952
v_mfma_f32_16x16x32_bf16 a[20:23],a[72:75],a[232:235],a[20:23]
ds_read_b64_tr_b16 a[112:113],v50 offset:768
v_mfma_f32_16x16x32_bf16 a[36:39],a[72:75],a[240:243],a[36:39]
ds_read_b64_tr_b16 a[114:115],v50 offset:2816
v_mfma_f32_16x16x32_bf16 a[36:39],a[76:79],a[244:247],a[36:39]
s_add_u32 m0,s88,2048
v_mfma_f32_16x16x32_bf16 a[52:55],a[76:79],a[252:255],a[52:55]
ds_read_b64_tr_b16 a[116:117],v50 offset:17152
v_mfma_f32_16x16x32_bf16 a[52:55],a[72:75],a[248:251],a[52:55]
buffer_load_dwordx4 v42,s[36:39],s83 offen lds
v_mfma_f32_16x16x32_bf16 a[8:11],a[80:83],a[224:227],a[8:11]
s_waitcnt lgkmcnt(5)
ds_read_b64_tr_b16 a[118:119],v50 offset:19200
v_mfma_f32_16x16x32_bf16 a[8:11],a[84:87],a[228:231],a[8:11]
ds_read_b64_tr_b16 a[120:121],v50 offset:776
v_mfma_f32_16x16x32_bf16 a[24:27],a[84:87],a[236:239],a[24:27]
ds_read_b64_tr_b16 a[122:123],v50 offset:2824
v_mfma_f32_16x16x32_bf16 a[24:27],a[80:83],a[232:235],a[24:27]
ds_read_b64_tr_b16 a[124:125],v50 offset:17160
v_mfma_f32_16x16x32_bf16 a[40:43],a[80:83],a[240:243],a[40:43]
s_add_u32 m0,s88,3072
v_mfma_f32_16x16x32_bf16 a[40:43],a[84:87],a[244:247],a[40:43]
ds_read_b64_tr_b16 a[126:127],v50 offset:19208
v_mfma_f32_16x16x32_bf16 a[56:59],a[84:87],a[252:255],a[56:59]
buffer_load_dwordx4 v43,s[36:39],s83 offen lds
v_mfma_f32_16x16x32_bf16 a[56:59],a[80:83],a[248:251],a[56:59]
ds_read_b64_tr_b16 a[64:65],v50 offset:1024
v_mfma_f32_16x16x32_bf16 a[12:15],a[88:91],a[224:227],a[12:15]
ds_read_b64_tr_b16 a[66:67],v50 offset:3072
v_mfma_f32_16x16x32_bf16 a[12:15],a[92:95],a[228:231],a[12:15]
ds_read_b64_tr_b16 a[68:69],v50 offset:17408
v_mfma_f32_16x16x32_bf16 a[28:31],a[92:95],a[236:239],a[28:31]
ds_read_b64_tr_b16 a[70:71],v50 offset:19456
v_mfma_f32_16x16x32_bf16 a[28:31],a[88:91],a[232:235],a[28:31]
s_add_u32 m0,s88,4096
v_mfma_f32_16x16x32_bf16 a[44:47],a[88:91],a[240:243],a[44:47]
ds_read_b64_tr_b16 a[72:73],v50 offset:1032
v_mfma_f32_16x16x32_bf16 a[44:47],a[92:95],a[244:247],a[44:47]
buffer_load_dwordx4 v44,s[36:39],s83 offen lds
v_mfma_f32_16x16x32_bf16 a[60:63],a[92:95],a[252:255],a[60:63]
ds_read_b64_tr_b16 a[74:75],v50 offset:3080
v_mfma_f32_16x16x32_bf16 a[60:63],a[88:91],a[248:251],a[60:63]
ds_read_b64_tr_b16 a[76:77],v50 offset:17416
v_mfma_f32_16x16x32_bf16 v[64:67],a[96:99],a[224:227],v[64:67]
ds_read_b64_tr_b16 a[78:79],v50 offset:19464
v_mfma_f32_16x16x32_bf16 v[64:67],a[100:103],a[228:231],v[64:67]
ds_read_b64_tr_b16 a[80:81],v50 offset:1280
v_mfma_f32_16x16x32_bf16 v[80:83],a[100:103],a[236:239],v[80:83]
s_add_u32 m0,s88,5120
v_mfma_f32_16x16x32_bf16 v[80:83],a[96:99],a[232:235],v[80:83]
ds_read_b64_tr_b16 a[82:83],v50 offset:3328
v_mfma_f32_16x16x32_bf16 v[96:99],a[96:99],a[240:243],v[96:99]
buffer_load_dwordx4 v45,s[36:39],s83 offen lds
v_mfma_f32_16x16x32_bf16 v[96:99],a[100:103],a[244:247],v[96:99]
ds_read_b64_tr_b16 a[84:85],v50 offset:17664
v_mfma_f32_16x16x32_bf16 v[112:115],a[100:103],a[252:255],v[112:115]
ds_read_b64_tr_b16 a[86:87],v50 offset:19712
v_mfma_f32_16x16x32_bf16 v[112:115],a[96:99],a[248:251],v[112:115]
ds_read_b64_tr_b16 a[88:89],v50 offset:1288
v_mfma_f32_16x16x32_bf16 v[68:71],a[104:107],a[224:227],v[68:71]
s_waitcnt lgkmcnt(5)
ds_read_b64_tr_b16 a[90:91],v50 offset:3336
v_mfma_f32_16x16x32_bf16 v[68:71],a[108:111],a[228:231],v[68:71]
s_add_u32 m0,s88,6144
v_mfma_f32_16x16x32_bf16 v[84:87],a[108:111],a[236:239],v[84:87]
ds_read_b64_tr_b16 a[92:93],v50 offset:17672
v_mfma_f32_16x16x32_bf16 v[84:87],a[104:107],a[232:235],v[84:87]
buffer_load_dwordx4 v46,s[36:39],s83 offen lds
v_mfma_f32_16x16x32_bf16 v[100:103],a[104:107],a[240:243],v[100:103]
ds_read_b64_tr_b16 a[94:95],v50 offset:19720
v_mfma_f32_16x16x32_bf16 v[100:103],a[108:111],a[244:247],v[100:103]
ds_read_b64_tr_b16 a[96:97],v50 offset:1536
v_mfma_f32_16x16x32_bf16 v[116:119],a[108:111],a[252:255],v[116:119]
ds_read_b64_tr_b16 a[98:99],v50 offset:3584
v_mfma_f32_16x16x32_bf16 v[116:119],a[104:107],a[248:251],v[116:119]
ds_read_b64_tr_b16 a[100:101],v50 offset:17920
v_mfma_f32_16x16x32_bf16 v[72:75],a[112:115],a[224:227],v[72:75]
s_add_u32 m0,s88,7168
s_mov_b32 s88,s69
s_mov_b32 s69,s86
v_mfma_f32_16x16x32_bf16 v[72:75],a[116:119],a[228:231],v[72:75]
ds_read_b64_tr_b16 a[102:103],v50 offset:19968
v_mfma_f32_16x16x32_bf16 v[88:91],a[116:119],a[236:239],v[88:91]
buffer_load_dwordx4 v47,s[36:39],s83 offen lds
v_mfma_f32_16x16x32_bf16 v[88:91],a[112:115],a[232:235],v[88:91]
s_add_u32 s83,s83,524288
ds_read_b64_tr_b16 a[104:105],v50 offset:1544
v_mfma_f32_16x16x32_bf16 v[104:107],a[112:115],a[240:243],v[104:107]
ds_read_b64_tr_b16 a[106:107],v50 offset:3592
v_mfma_f32_16x16x32_bf16 v[104:107],a[116:119],a[244:247],v[104:107]
ds_read_b64_tr_b16 a[108:109],v50 offset:17928
v_mfma_f32_16x16x32_bf16 v[120:123],a[116:119],a[252:255],v[120:123]
ds_read_b64_tr_b16 a[110:111],v50 offset:19976
v_mfma_f32_16x16x32_bf16 v[120:123],a[112:115],a[248:251],v[120:123]
s_add_u32 m0,s85,0
v_mfma_f32_16x16x32_bf16 v[76:79],a[120:123],a[224:227],v[76:79]
ds_read_b64_tr_b16 a[112:113],v50 offset:1792
v_mfma_f32_16x16x32_bf16 v[76:79],a[124:127],a[228:231],v[76:79]
buffer_load_dwordx4 v32,s[28:31],s84 offen lds
v_mfma_f32_16x16x32_bf16 v[92:95],a[124:127],a[236:239],v[92:95]
ds_read_b64_tr_b16 a[114:115],v50 offset:3840
v_mfma_f32_16x16x32_bf16 v[92:95],a[120:123],a[232:235],v[92:95]
ds_read_b64_tr_b16 a[116:117],v50 offset:18176
v_mfma_f32_16x16x32_bf16 v[108:111],a[120:123],a[240:243],v[108:111]
ds_read_b64_tr_b16 a[118:119],v50 offset:20224
v_mfma_f32_16x16x32_bf16 v[108:111],a[124:127],a[244:247],v[108:111]
s_cmp_eq_u32 s70,1
s_cselect_b32 s65,256,0
s_cselect_b32 s66,524288,0
v_mfma_f32_16x16x32_bf16 v[124:127],a[124:127],a[252:255],v[124:127]
ds_read_b64_tr_b16 a[124:125],v50 offset:18184
v_add_u32 v40,s65,v40
v_mfma_f32_16x16x32_bf16 v[124:127],a[120:123],a[248:251],v[124:127]
ds_read_b64_tr_b16 a[120:121],v50 offset:1800
v_add_u32 v41,s66,v41
v_mfma_f32_16x16x32_bf16 v[128:131],a[64:67],a[224:227],v[128:131]
buffer_load_dwordx4 v33,s[28:31],s84 offen offset:1024 lds
v_mfma_f32_16x16x32_bf16 v[128:131],a[68:71],a[228:231],v[128:131]
ds_read_b64_tr_b16 a[122:123],v50 offset:3848
v_add_u32 v42,s65,v42
v_mfma_f32_16x16x32_bf16 v[160:163],a[68:71],a[236:239],v[160:163]
ds_read_b64_tr_b16 a[126:127],v50 offset:20232
v_add_u32 v50,s86,v51
v_mfma_f32_16x16x32_bf16 v[160:163],a[64:67],a[232:235],v[160:163]
s_mov_b32 s86,s87
v_add_u32 v43,s66,v43
v_add_u32 v44,s65,v44
v_mfma_f32_16x16x32_bf16 v[192:195],a[64:67],a[240:243],v[192:195]
s_mov_b32 s87,s88
v_add_u32 v45,s66,v45
v_add_u32 v46,s65,v46
v_mfma_f32_16x16x32_bf16 v[192:195],a[68:71],a[244:247],v[192:195]
v_add_u32 v47,s66,v47
v_mfma_f32_16x16x32_bf16 v[224:227],a[68:71],a[252:255],v[224:227]
v_mfma_f32_16x16x32_bf16 v[224:227],a[64:67],a[248:251],v[224:227]
buffer_load_dwordx4 v34,s[28:31],s84 offen offset:2048 lds
v_mfma_f32_16x16x32_bf16 v[132:135],a[72:75],a[224:227],v[132:135]
v_mfma_f32_16x16x32_bf16 v[132:135],a[76:79],a[228:231],v[132:135]
v_mfma_f32_16x16x32_bf16 v[164:167],a[76:79],a[236:239],v[164:167]
v_mfma_f32_16x16x32_bf16 v[164:167],a[72:75],a[232:235],v[164:167]
v_mfma_f32_16x16x32_bf16 v[196:199],a[72:75],a[240:243],v[196:199]
v_mfma_f32_16x16x32_bf16 v[196:199],a[76:79],a[244:247],v[196:199]
v_mfma_f32_16x16x32_bf16 v[228:231],a[76:79],a[252:255],v[228:231]
buffer_load_dwordx4 v35,s[28:31],s84 offen offset:3072 lds
v_mfma_f32_16x16x32_bf16 v[228:231],a[72:75],a[248:251],v[228:231]
s_waitcnt lgkmcnt(0)
v_mfma_f32_16x16x32_bf16 v[136:139],a[80:83],a[224:227],v[136:139]
v_mfma_f32_16x16x32_bf16 v[136:139],a[84:87],a[228:231],v[136:139]
v_mfma_f32_16x16x32_bf16 v[168:171],a[84:87],a[236:239],v[168:171]
v_mfma_f32_16x16x32_bf16 v[168:171],a[80:83],a[232:235],v[168:171]
s_add_u32 m0,s85,4096
s_xor_b32 s85,s85,98304
v_mfma_f32_16x16x32_bf16 v[200:203],a[80:83],a[240:243],v[200:203]
v_mfma_f32_16x16x32_bf16 v[200:203],a[84:87],a[244:247],v[200:203]
buffer_load_dwordx4 v36,s[28:31],s84 offen lds
v_mfma_f32_16x16x32_bf16 v[232:235],a[84:87],a[252:255],v[232:235]
v_mfma_f32_16x16x32_bf16 v[232:235],a[80:83],a[248:251],v[232:235]
v_mfma_f32_16x16x32_bf16 v[140:143],a[88:91],a[224:227],v[140:143]
v_mfma_f32_16x16x32_bf16 v[140:143],a[92:95],a[228:231],v[140:143]
v_mfma_f32_16x16x32_bf16 v[172:175],a[92:95],a[236:239],v[172:175]
v_mfma_f32_16x16x32_bf16 v[172:175],a[88:91],a[232:235],v[172:175]
v_mfma_f32_16x16x32_bf16 v[204:207],a[88:91],a[240:243],v[204:207]
buffer_load_dwordx4 v37,s[28:31],s84 offen offset:1024 lds
v_mfma_f32_16x16x32_bf16 v[204:207],a[92:95],a[244:247],v[204:207]
v_mfma_f32_16x16x32_bf16 v[236:239],a[92:95],a[252:255],v[236:239]
v_mfma_f32_16x16x32_bf16 v[236:239],a[88:91],a[248:251],v[236:239]
s_waitcnt vmcnt(22)
s_barrier
v_mfma_f32_16x16x32_bf16 v[144:147],a[96:99],a[224:227],v[144:147]
s_waitcnt vmcnt(14)
ds_read_b128 a[192:195],v48
v_mfma_f32_16x16x32_bf16 v[144:147],a[100:103],a[228:231],v[144:147]
ds_read_b128 a[196:199],v49
v_mfma_f32_16x16x32_bf16 v[176:179],a[100:103],a[236:239],v[176:179]
buffer_load_dwordx4 v38,s[28:31],s84 offen offset:2048 lds
v_mfma_f32_16x16x32_bf16 v[176:179],a[96:99],a[232:235],v[176:179]
ds_read_b128 a[200:203],v48 offset:2048
v_mfma_f32_16x16x32_bf16 v[208:211],a[96:99],a[240:243],v[208:211]
ds_read_b128 a[204:207],v49 offset:2048
v_mfma_f32_16x16x32_bf16 v[208:211],a[100:103],a[244:247],v[208:211]
ds_read_b128 a[208:211],v48 offset:4096
v_mfma_f32_16x16x32_bf16 v[240:243],a[100:103],a[252:255],v[240:243]
ds_read_b128 a[212:215],v49 offset:4096
v_mfma_f32_16x16x32_bf16 v[240:243],a[96:99],a[248:251],v[240:243]
v_mfma_f32_16x16x32_bf16 v[148:151],a[104:107],a[224:227],v[148:151]
ds_read_b128 a[216:219],v48 offset:6144
v_xor_b32 v48,98304,v48
v_mfma_f32_16x16x32_bf16 v[148:151],a[108:111],a[228:231],v[148:151]
buffer_load_dwordx4 v39,s[28:31],s84 offen offset:3072 lds
v_mfma_f32_16x16x32_bf16 v[180:183],a[108:111],a[236:239],v[180:183]
s_add_u32 s84,s84,128
ds_read_b128 a[220:223],v49 offset:6144
v_xor_b32 v49,98304,v49
v_mfma_f32_16x16x32_bf16 v[180:183],a[104:107],a[232:235],v[180:183]
ds_read_b64_tr_b16 a[64:65],v50
v_mfma_f32_16x16x32_bf16 v[212:215],a[104:107],a[240:243],v[212:215]
ds_read_b64_tr_b16 a[66:67],v50 offset:2048
v_mfma_f32_16x16x32_bf16 v[212:215],a[108:111],a[244:247],v[212:215]
ds_read_b64_tr_b16 a[68:69],v50 offset:16384
v_mfma_f32_16x16x32_bf16 v[244:247],a[108:111],a[252:255],v[244:247]
v_mfma_f32_16x16x32_bf16 v[244:247],a[104:107],a[248:251],v[244:247]
ds_read_b64_tr_b16 a[70:71],v50 offset:18432
v_mfma_f32_16x16x32_bf16 v[152:155],a[112:115],a[224:227],v[152:155]
buffer_load_dword v9,v19,s[48:51],s90 offen
v_mfma_f32_16x16x32_bf16 v[152:155],a[116:119],a[228:231],v[152:155]
s_add_u32 s90,s90,896
ds_read_b64_tr_b16 a[72:73],v50 offset:8
v_mfma_f32_16x16x32_bf16 v[184:187],a[116:119],a[236:239],v[184:187]
ds_read_b64_tr_b16 a[74:75],v50 offset:2056
)A"
R"A(v_mfma_f32_16x16x32_bf16 v[184:187],a[112:115],a[232:235],v[184:187]
ds_read_b64_tr_b16 a[76:77],v50 offset:16392
v_mfma_f32_16x16x32_bf16 v[216:219],a[112:115],a[240:243],v[216:219]
ds_read_b64_tr_b16 a[78:79],v50 offset:18440
v_mfma_f32_16x16x32_bf16 v[216:219],a[116:119],a[244:247],v[216:219]
v_mfma_f32_16x16x32_bf16 v[248:251],a[116:119],a[252:255],v[248:251]
ds_read_b64_tr_b16 a[80:81],v50 offset:256
v_mfma_f32_16x16x32_bf16 v[248:251],a[112:115],a[248:251],v[248:251]
ds_read_b64_tr_b16 a[82:83],v50 offset:2304
v_mfma_f32_16x16x32_bf16 v[156:159],a[120:123],a[224:227],v[156:159]
ds_read_b64_tr_b16 a[84:85],v50 offset:16640
v_mfma_f32_16x16x32_bf16 v[156:159],a[124:127],a[228:231],v[156:159]
v_mfma_f32_16x16x32_bf16 v[188:191],a[124:127],a[236:239],v[188:191]
ds_read_b64_tr_b16 a[86:87],v50 offset:18688
v_mfma_f32_16x16x32_bf16 v[188:191],a[120:123],a[232:235],v[188:191]
ds_read_b64_tr_b16 a[88:89],v50 offset:264
v_mfma_f32_16x16x32_bf16 v[220:223],a[120:123],a[240:243],v[220:223]
ds_read_b64_tr_b16 a[90:91],v50 offset:2312
v_mfma_f32_16x16x32_bf16 v[220:223],a[124:127],a[244:247],v[220:223]
v_mfma_f32_16x16x32_bf16 v[252:255],a[124:127],a[252:255],v[252:255]
s_waitcnt lgkmcnt(4)
ds_read_b64_tr_b16 a[92:93],v50 offset:16648
v_mfma_f32_16x16x32_bf16 v[252:255],a[120:123],a[248:251],v[252:255]
s_add_u32 m0,s88,0
ds_read_b64_tr_b16 a[94:95],v50 offset:18696
v_mfma_f32_16x16x32_bf16 a[0:3],a[64:67],a[192:195],a[0:3]
ds_read_b64_tr_b16 a[96:97],v50 offset:512
v_mfma_f32_16x16x32_bf16 a[0:3],a[68:71],a[196:199],a[0:3]
buffer_load_dwordx4 v40,s[36:39],s83 offen lds
v_mfma_f32_16x16x32_bf16 a[16:19],a[68:71],a[204:207],a[16:19]
ds_read_b64_tr_b16 a[98:99],v50 offset:2560
v_mfma_f32_16x16x32_bf16 a[16:19],a[64:67],a[200:203],a[16:19]
ds_read_b64_tr_b16 a[100:101],v50 offset:16896
v_mfma_f32_16x16x32_bf16 a[32:35],a[64:67],a[208:211],a[32:35]
ds_read_b64_tr_b16 a[102:103],v50 offset:18944
v_mfma_f32_16x16x32_bf16 a[32:35],a[68:71],a[212:215],a[32:35]
v_mfma_f32_16x16x32_bf16 a[48:51],a[68:71],a[220:223],a[48:51]
s_add_u32 m0,s88,1024
ds_read_b64_tr_b16 a[104:105],v50 offset:520
v_mfma_f32_16x16x32_bf16 a[48:51],a[64:67],a[216:219],a[48:51]
ds_read_b64_tr_b16 a[106:107],v50 offset:2568
v_mfma_f32_16x16x32_bf16 a[4:7],a[72:75],a[192:195],a[4:7]
buffer_load_dwordx4 v41,s[36:39],s83 offen lds
v_mfma_f32_16x16x32_bf16 a[4:7],a[76:79],a[196:199],a[4:7]
ds_read_b64_tr_b16 a[108:109],v50 offset:16904
v_mfma_f32_16x16x32_bf16 a[20:23],a[76:79],a[204:207],a[20:23]
ds_read_b64_tr_b16 a[110:111],v50 offset:18952
v_mfma_f32_16x16x32_bf16 a[20:23],a[72:75],a[200:203],a[20:23]
ds_read_b64_tr_b16 a[112:113],v50 offset:768
v_mfma_f32_16x16x32_bf16 a[36:39],a[72:75],a[208:211],a[36:39]
ds_read_b64_tr_b16 a[114:115],v50 offset:2816
v_mfma_f32_16x16x32_bf16 a[36:39],a[76:79],a[212:215],a[36:39]
s_add_u32 m0,s88,2048
v_mfma_f32_16x16x32_bf16 a[52:55],a[76:79],a[220:223],a[52:55]
ds_read_b64_tr_b16 a[116:117],v50 offset:17152
v_mfma_f32_16x16x32_bf16 a[52:55],a[72:75],a[216:219],a[52:55]
buffer_load_dwordx4 v42,s[36:39],s83 offen lds
v_mfma_f32_16x16x32_bf16 a[8:11],a[80:83],a[192:195],a[8:11]
s_waitcnt lgkmcnt(5)
ds_read_b64_tr_b16 a[118:119],v50 offset:19200
v_mfma_f32_16x16x32_bf16 a[8:11],a[84:87],a[196:199],a[8:11]
ds_read_b64_tr_b16 a[120:121],v50 offset:776
v_mfma_f32_16x16x32_bf16 a[24:27],a[84:87],a[204:207],a[24:27]
ds_read_b64_tr_b16 a[122:123],v50 offset:2824
v_mfma_f32_16x16x32_bf16 a[24:27],a[80:83],a[200:203],a[24:27]
ds_read_b64_tr_b16 a[124:125],v50 offset:17160
v_mfma_f32_16x16x32_bf16 a[40:43],a[80:83],a[208:211],a[40:43]
s_add_u32 m0,s88,3072
v_mfma_f32_16x16x32_bf16 a[40:43],a[84:87],a[212:215],a[40:43]
ds_read_b64_tr_b16 a[126:127],v50 offset:19208
v_mfma_f32_16x16x32_bf16 a[56:59],a[84:87],a[220:223],a[56:59]
buffer_load_dwordx4 v43,s[36:39],s83 offen lds
v_mfma_f32_16x16x32_bf16 a[56:59],a[80:83],a[216:219],a[56:59]
ds_read_b64_tr_b16 a[64:65],v50 offset:1024
v_mfma_f32_16x16x32_bf16 a[12:15],a[88:91],a[192:195],a[12:15]
ds_read_b64_tr_b16 a[66:67],v50 offset:3072
v_mfma_f32_16x16x32_bf16 a[12:15],a[92:95],a[196:199],a[12:15]
ds_read_b64_tr_b16 a[68:69],v50 offset:17408
v_mfma_f32_16x16x32_bf16 a[28:31],a[92:95],a[204:207],a[28:31]
ds_read_b64_tr_b16 a[70:71],v50 offset:19456
v_mfma_f32_16x16x32_bf16 a[28:31],a[88:91],a[200:203],a[28:31]
s_add_u32 m0,s88,4096
v_mfma_f32_16x16x32_bf16 a[44:47],a[88:91],a[208:211],a[44:47]
ds_read_b64_tr_b16 a[72:73],v50 offset:1032
v_mfma_f32_16x16x32_bf16 a[44:47],a[92:95],a[212:215],a[44:47]
buffer_load_dwordx4 v44,s[36:39],s83 offen lds
v_mfma_f32_16x16x32_bf16 a[60:63],a[92:95],a[220:223],a[60:63]
ds_read_b64_tr_b16 a[74:75],v50 offset:3080
v_mfma_f32_16x16x32_bf16 a[60:63],a[88:91],a[216:219],a[60:63]
ds_read_b64_tr_b16 a[76:77],v50 offset:17416
v_mfma_f32_16x16x32_bf16 v[64:67],a[96:99],a[192:195],v[64:67]
ds_read_b64_tr_b16 a[78:79],v50 offset:19464
v_mfma_f32_16x16x32_bf16 v[64:67],a[100:103],a[196:199],v[64:67]
ds_read_b64_tr_b16 a[80:81],v50 offset:1280
v_mfma_f32_16x16x32_bf16 v[80:83],a[100:103],a[204:207],v[80:83]
s_add_u32 m0,s88,5120
v_mfma_f32_16x16x32_bf16 v[80:83],a[96:99],a[200:203],v[80:83]
ds_read_b64_tr_b16 a[82:83],v50 offset:3328
v_mfma_f32_16x16x32_bf16 v[96:99],a[96:99],a[208:211],v[96:99]
buffer_load_dwordx4 v45,s[36:39],s83 offen lds
v_mfma_f32_16x16x32_bf16 v[96:99],a[100:103],a[212:215],v[96:99]
ds_read_b64_tr_b16 a[84:85],v50 offset:17664
v_mfma_f32_16x16x32_bf16 v[112:115],a[100:103],a[220:223],v[112:115]
ds_read_b64_tr_b16 a[86:87],v50 offset:19712
v_mfma_f32_16x16x32_bf16 v[112:115],a[96:99],a[216:219],v[112:115]
ds_read_b64_tr_b16 a[88:89],v50 offset:1288
v_mfma_f32_16x16x32_bf16 v[68:71],a[104:107],a[192:195],v[68:71]
s_waitcnt lgkmcnt(5)
ds_read_b64_tr_b16 a[90:91],v50 offset:3336
v_mfma_f32_16x16x32_bf16 v[68:71],a[108:111],a[196:199],v[68:71]
s_add_u32 m0,s88,6144
v_mfma_f32_16x16x32_bf16 v[84:87],a[108:111],a[204:207],v[84:87]
ds_read_b64_tr_b16 a[92:93],v50 offset:17672
v_mfma_f32_16x16x32_bf16 v[84:87],a[104:107],a[200:203],v[84:87]
buffer_load_dwordx4 v46,s[36:39],s83 offen lds
v_mfma_f32_16x16x32_bf16 v[100:103],a[104:107],a[208:211],v[100:103]
ds_read_b64_tr_b16 a[94:95],v50 offset:19720
v_mfma_f32_16x16x32_bf16 v[100:103],a[108:111],a[212:215],v[100:103]
ds_read_b64_tr_b16 a[96:97],v50 offset:1536
v_mfma_f32_16x16x32_bf16 v[116:119],a[108:111],a[220:223],v[116:119]
ds_read_b64_tr_b16 a[98:99],v50 offset:3584
v_mfma_f32_16x16x32_bf16 v[116:119],a[104:107],a[216:219],v[116:119]
ds_read_b64_tr_b16 a[100:101],v50 offset:17920
v_mfma_f32_16x16x32_bf16 v[72:75],a[112:115],a[192:195],v[72:75]
s_add_u32 m0,s88,7168
s_mov_b32 s88,s69
v_mfma_f32_16x16x32_bf16 v[72:75],a[116:119],a[196:199],v[72:75]
ds_read_b64_tr_b16 a[102:103],v50 offset:19968
v_mfma_f32_16x16x32_bf16 v[88:91],a[116:119],a[204:207],v[88:91]
buffer_load_dwordx4 v47,s[36:39],s83 offen lds
v_mfma_f32_16x16x32_bf16 v[88:91],a[112:115],a[200:203],v[88:91]
s_add_u32 s83,s83,524288
ds_read_b64_tr_b16 a[104:105],v50 offset:1544
v_mfma_f32_16x16x32_bf16 v[104:107],a[112:115],a[208:211],v[104:107]
ds_read_b64_tr_b16 a[106:107],v50 offset:3592
v_mfma_f32_16x16x32_bf16 v[104:107],a[116:119],a[212:215],v[104:107]
ds_read_b64_tr_b16 a[108:109],v50 offset:17928
v_mfma_f32_16x16x32_bf16 v[120:123],a[116:119],a[220:223],v[120:123]
ds_read_b64_tr_b16 a[110:111],v50 offset:19976
v_mfma_f32_16x16x32_bf16 v[120:123],a[112:115],a[216:219],v[120:123]
s_add_u32 m0,s85,0
v_mfma_f32_16x16x32_bf16 v[76:79],a[120:123],a[192:195],v[76:79]
ds_read_b64_tr_b16 a[112:113],v50 offset:1792
v_mfma_f32_16x16x32_bf16 v[76:79],a[124:127],a[196:199],v[76:79]
buffer_load_dwordx4 v32,s[28:31],s84 offen lds
v_mfma_f32_16x16x32_bf16 v[92:95],a[124:127],a[204:207],v[92:95]
ds_read_b64_tr_b16 a[114:115],v50 offset:3840
v_mfma_f32_16x16x32_bf16 v[92:95],a[120:123],a[200:203],v[92:95]
ds_read_b64_tr_b16 a[116:117],v50 offset:18176
v_mfma_f32_16x16x32_bf16 v[108:111],a[120:123],a[208:211],v[108:111]
ds_read_b64_tr_b16 a[118:119],v50 offset:20224
v_mfma_f32_16x16x32_bf16 v[108:111],a[124:127],a[212:215],v[108:111]
v_mfma_f32_16x16x32_bf16 v[124:127],a[124:127],a[220:223],v[124:127]
ds_read_b64_tr_b16 a[124:125],v50 offset:18184
v_mfma_f32_16x16x32_bf16 v[124:127],a[120:123],a[216:219],v[124:127]
ds_read_b64_tr_b16 a[120:121],v50 offset:1800
v_mfma_f32_16x16x32_bf16 v[128:131],a[64:67],a[192:195],v[128:131]
buffer_load_dwordx4 v33,s[28:31],s84 offen offset:1024 lds
v_mfma_f32_16x16x32_bf16 v[128:131],a[68:71],a[196:199],v[128:131]
ds_read_b64_tr_b16 a[122:123],v50 offset:3848
v_mfma_f32_16x16x32_bf16 v[160:163],a[68:71],a[204:207],v[160:163]
ds_read_b64_tr_b16 a[126:127],v50 offset:20232
v_add_u32 v50,s86,v51
v_mfma_f32_16x16x32_bf16 v[160:163],a[64:67],a[200:203],v[160:163]
v_mfma_f32_16x16x32_bf16 v[192:195],a[64:67],a[208:211],v[192:195]
v_mfma_f32_16x16x32_bf16 v[192:195],a[68:71],a[212:215],v[192:195]
v_mfma_f32_16x16x32_bf16 v[224:227],a[68:71],a[220:223],v[224:227]
v_mfma_f32_16x16x32_bf16 v[224:227],a[64:67],a[216:219],v[224:227]
buffer_load_dwordx4 v34,s[28:31],s84 offen offset:2048 lds
v_mfma_f32_16x16x32_bf16 v[132:135],a[72:75],a[192:195],v[132:135]
v_mfma_f32_16x16x32_bf16 v[132:135],a[76:79],a[196:199],v[132:135]
v_mfma_f32_16x16x32_bf16 v[164:167],a[76:79],a[204:207],v[164:167]
v_mfma_f32_16x16x32_bf16 v[164:167],a[72:75],a[200:203],v[164:167]
v_mfma_f32_16x16x32_bf16 v[196:199],a[72:75],a[208:211],v[196:199]
v_mfma_f32_16x16x32_bf16 v[196:199],a[76:79],a[212:215],v[196:199]
v_mfma_f32_16x16x32_bf16 v[228:231],a[76:79],a[220:223],v[228:231]
buffer_load_dwordx4 v35,s[28:31],s84 offen offset:3072 lds
v_mfma_f32_16x16x32_bf16 v[228:231],a[72:75],a[216:219],v[228:231]
s_waitcnt lgkmcnt(0)
v_mfma_f32_16x16x32_bf16 v[136:139],a[80:83],a[192:195],v[136:139]
v_mfma_f32_16x16x32_bf16 v[136:139],a[84:87],a[196:199],v[136:139]
v_mfma_f32_16x16x32_bf16 v[168:171],a[84:87],a[204:207],v[168:171]
v_mfma_f32_16x16x32_bf16 v[168:171],a[80:83],a[200:203],v[168:171]
s_add_u32 m0,s85,4096
s_xor_b32 s85,s85,98304
v_mfma_f32_16x16x32_bf16 v[200:203],a[80:83],a[208:211],v[200:203]
v_mfma_f32_16x16x32_bf16 v[200:203],a[84:87],a[212:215],v[200:203]
buffer_load_dwordx4 v36,s[28:31],s84 offen lds
v_mfma_f32_16x16x32_bf16 v[232:235],a[84:87],a[220:223],v[232:235]
v_mfma_f32_16x16x32_bf16 v[232:235],a[80:83],a[216:219],v[232:235]
v_mfma_f32_16x16x32_bf16 v[140:143],a[88:91],a[192:195],v[140:143]
v_mfma_f32_16x16x32_bf16 v[140:143],a[92:95],a[196:199],v[140:143]
v_mfma_f32_16x16x32_bf16 v[172:175],a[92:95],a[204:207],v[172:175]
v_mfma_f32_16x16x32_bf16 v[172:175],a[88:91],a[200:203],v[172:175]
v_mfma_f32_16x16x32_bf16 v[204:207],a[88:91],a[208:211],v[204:207]
buffer_load_dwordx4 v37,s[28:31],s84 offen offset:1024 lds
v_mfma_f32_16x16x32_bf16 v[204:207],a[92:95],a[212:215],v[204:207]
v_mfma_f32_16x16x32_bf16 v[236:239],a[92:95],a[220:223],v[236:239]
v_mfma_f32_16x16x32_bf16 v[236:239],a[88:91],a[216:219],v[236:239]
s_waitcnt vmcnt(23)
s_barrier
v_mfma_f32_16x16x32_bf16 v[144:147],a[96:99],a[192:195],v[144:147]
s_waitcnt vmcnt(15)
ds_read_b128 a[224:227],v48
v_mfma_f32_16x16x32_bf16 v[144:147],a[100:103],a[196:199],v[144:147]
ds_read_b128 a[228:231],v49
v_mfma_f32_16x16x32_bf16 v[176:179],a[100:103],a[204:207],v[176:179]
buffer_load_dwordx4 v38,s[28:31],s84 offen offset:2048 lds
v_mfma_f32_16x16x32_bf16 v[176:179],a[96:99],a[200:203],v[176:179]
ds_read_b128 a[232:235],v48 offset:2048
v_mfma_f32_16x16x32_bf16 v[208:211],a[96:99],a[208:211],v[208:211]
ds_read_b128 a[236:239],v49 offset:2048
v_mfma_f32_16x16x32_bf16 v[208:211],a[100:103],a[212:215],v[208:211]
ds_read_b128 a[240:243],v48 offset:4096
v_mfma_f32_16x16x32_bf16 v[240:243],a[100:103],a[220:223],v[240:243]
ds_read_b128 a[244:247],v49 offset:4096
v_mfma_f32_16x16x32_bf16 v[240:243],a[96:99],a[216:219],v[240:243]
v_mfma_f32_16x16x32_bf16 v[148:151],a[104:107],a[192:195],v[148:151]
ds_read_b128 a[248:251],v48 offset:6144
v_mfma_f32_16x16x32_bf16 v[148:151],a[108:111],a[196:199],v[148:151]
buffer_load_dwordx4 v39,s[28:31],s84 offen offset:3072 lds
v_mfma_f32_16x16x32_bf16 v[180:183],a[108:111],a[204:207],v[180:183]
)A"
R"A(s_add_u32 s84,s84,128
ds_read_b128 a[252:255],v49 offset:6144
v_mfma_f32_16x16x32_bf16 v[180:183],a[104:107],a[200:203],v[180:183]
ds_read_b64_tr_b16 a[64:65],v50
v_mfma_f32_16x16x32_bf16 v[212:215],a[104:107],a[208:211],v[212:215]
ds_read_b64_tr_b16 a[66:67],v50 offset:2048
v_mfma_f32_16x16x32_bf16 v[212:215],a[108:111],a[212:215],v[212:215]
ds_read_b64_tr_b16 a[68:69],v50 offset:16384
v_mfma_f32_16x16x32_bf16 v[244:247],a[108:111],a[220:223],v[244:247]
v_mfma_f32_16x16x32_bf16 v[244:247],a[104:107],a[216:219],v[244:247]
ds_read_b64_tr_b16 a[70:71],v50 offset:18432
v_mfma_f32_16x16x32_bf16 v[152:155],a[112:115],a[192:195],v[152:155]
buffer_load_dword v8,v19,s[48:51],s90 offen
v_mfma_f32_16x16x32_bf16 v[152:155],a[116:119],a[196:199],v[152:155]
s_add_u32 s90,s90,896
ds_read_b64_tr_b16 a[72:73],v50 offset:8
s_nop 0
L_pd2_%=:
s_cbranch_execz L_pd6_%=
v_mfma_f32_16x16x32_bf16 v[184:187],a[116:119],a[204:207],v[184:187]
ds_read_b64_tr_b16 a[74:75],v50 offset:2056
v_mfma_f32_16x16x32_bf16 v[184:187],a[112:115],a[200:203],v[184:187]
ds_read_b64_tr_b16 a[76:77],v50 offset:16392
v_mfma_f32_16x16x32_bf16 v[216:219],a[112:115],a[208:211],v[216:219]
ds_read_b64_tr_b16 a[78:79],v50 offset:18440
v_mfma_f32_16x16x32_bf16 v[216:219],a[116:119],a[212:215],v[216:219]
v_mfma_f32_16x16x32_bf16 v[248:251],a[116:119],a[220:223],v[248:251]
ds_read_b64_tr_b16 a[80:81],v50 offset:256
v_mfma_f32_16x16x32_bf16 v[248:251],a[112:115],a[216:219],v[248:251]
ds_read_b64_tr_b16 a[82:83],v50 offset:2304
v_mfma_f32_16x16x32_bf16 v[156:159],a[120:123],a[192:195],v[156:159]
ds_read_b64_tr_b16 a[84:85],v50 offset:16640
v_mfma_f32_16x16x32_bf16 v[156:159],a[124:127],a[196:199],v[156:159]
v_mfma_f32_16x16x32_bf16 v[188:191],a[124:127],a[204:207],v[188:191]
ds_read_b64_tr_b16 a[86:87],v50 offset:18688
v_mfma_f32_16x16x32_bf16 v[188:191],a[120:123],a[200:203],v[188:191]
ds_read_b64_tr_b16 a[88:89],v50 offset:264
v_mfma_f32_16x16x32_bf16 v[220:223],a[120:123],a[208:211],v[220:223]
ds_read_b64_tr_b16 a[90:91],v50 offset:2312
v_mfma_f32_16x16x32_bf16 v[220:223],a[124:127],a[212:215],v[220:223]
v_mfma_f32_16x16x32_bf16 v[252:255],a[124:127],a[220:223],v[252:255]
ds_read_b64_tr_b16 a[92:93],v50 offset:16648
v_mfma_f32_16x16x32_bf16 v[252:255],a[120:123],a[216:219],v[252:255]
ds_read_b64_tr_b16 a[94:95],v50 offset:18696
s_sub_u32 s70,s70,1
s_cmp_lg_u32 s70,0
s_cbranch_scc1 L_qp_%=
s_waitcnt lgkmcnt(6)
v_mfma_f32_16x16x32_bf16 a[0:3],a[64:67],a[224:227],a[0:3]
s_memrealtime s[92:93]
s_add_u32 m0,s85,0
ds_read_b64_tr_b16 a[96:97],v50 offset:512
v_mfma_f32_16x16x32_bf16 a[0:3],a[68:71],a[228:231],a[0:3]
buffer_load_dwordx4 v32,s[28:31],s84 offen lds
v_mfma_f32_16x16x32_bf16 a[16:19],a[68:71],a[236:239],a[16:19]
s_sub_u32 s83,s83,524288
ds_read_b64_tr_b16 a[98:99],v50 offset:2560
v_subrev_u32 v40,256,v40
v_mfma_f32_16x16x32_bf16 a[16:19],a[64:67],a[232:235],a[16:19]
s_mov_b32 s69,s86
ds_read_b64_tr_b16 a[100:101],v50 offset:16896
v_subrev_u32 v41,256,v41
v_mfma_f32_16x16x32_bf16 a[32:35],a[64:67],a[240:243],a[32:35]
s_mov_b32 s86,s87
ds_read_b64_tr_b16 a[102:103],v50 offset:18944
v_subrev_u32 v42,256,v42
v_mfma_f32_16x16x32_bf16 a[32:35],a[68:71],a[244:247],a[32:35]
s_mov_b32 s87,s88
ds_read_b64_tr_b16 a[104:105],v50 offset:520
v_subrev_u32 v43,256,v43
v_mfma_f32_16x16x32_bf16 a[48:51],a[68:71],a[252:255],a[48:51]
s_add_u32 s75,s75,16384
v_subrev_u32 v44,256,v44
v_subrev_u32 v45,256,v45
v_mfma_f32_16x16x32_bf16 a[48:51],a[64:67],a[248:251],a[48:51]
buffer_load_dwordx4 v33,s[28:31],s84 offen offset:1024 lds
v_mfma_f32_16x16x32_bf16 a[4:7],a[72:75],a[224:227],a[4:7]
s_sub_u32 s78,s77,32768
ds_read_b64_tr_b16 a[106:107],v50 offset:2568
v_subrev_u32 v46,256,v46
v_mfma_f32_16x16x32_bf16 a[4:7],a[76:79],a[228:231],a[4:7]
s_and_b32 s78,s78,0x1ffff
ds_read_b64_tr_b16 a[108:109],v50 offset:16904
v_subrev_u32 v47,256,v47
v_mfma_f32_16x16x32_bf16 a[20:23],a[76:79],a[236:239],a[20:23]
s_sub_u32 s78,s78,s77
ds_read_b64_tr_b16 a[110:111],v50 offset:18952
v_xor_b32 v48,98304,v48
v_mfma_f32_16x16x32_bf16 a[20:23],a[72:75],a[232:235],a[20:23]
s_mov_b32 s65,262144
ds_read_b64_tr_b16 a[112:113],v50 offset:768
v_xor_b32 v49,98304,v49
v_mfma_f32_16x16x32_bf16 a[36:39],a[72:75],a[240:243],a[36:39]
s_add_u32 s77,s77,s78
s_mov_b32 s6,0
v_add_u32 v52,s87,v51
v_mfma_f32_16x16x32_bf16 a[36:39],a[76:79],a[244:247],a[36:39]
buffer_load_dwordx4 v34,s[28:31],s84 offen offset:2048 lds
v_mfma_f32_16x16x32_bf16 a[52:55],a[76:79],a[252:255],a[52:55]
ds_read_b64_tr_b16 a[114:115],v50 offset:2816
v_mfma_f32_16x16x32_bf16 a[52:55],a[72:75],a[248:251],a[52:55]
ds_read_b64_tr_b16 a[116:117],v50 offset:17152
v_mfma_f32_16x16x32_bf16 a[8:11],a[80:83],a[224:227],a[8:11]
s_waitcnt lgkmcnt(5)
ds_read_b64_tr_b16 a[118:119],v50 offset:19200
v_mfma_f32_16x16x32_bf16 a[8:11],a[84:87],a[228:231],a[8:11]
ds_read_b64_tr_b16 a[120:121],v50 offset:776
v_mfma_f32_16x16x32_bf16 a[24:27],a[84:87],a[236:239],a[24:27]
v_mfma_f32_16x16x32_bf16 a[24:27],a[80:83],a[232:235],a[24:27]
buffer_load_dwordx4 v35,s[28:31],s84 offen offset:3072 lds
v_mfma_f32_16x16x32_bf16 a[40:43],a[80:83],a[240:243],a[40:43]
ds_read_b64_tr_b16 a[122:123],v50 offset:2824
v_mfma_f32_16x16x32_bf16 a[40:43],a[84:87],a[244:247],a[40:43]
ds_read_b64_tr_b16 a[124:125],v50 offset:17160
v_mfma_f32_16x16x32_bf16 a[56:59],a[84:87],a[252:255],a[56:59]
ds_read_b64_tr_b16 a[126:127],v50 offset:19208
v_mfma_f32_16x16x32_bf16 a[56:59],a[80:83],a[248:251],a[56:59]
s_add_u32 m0,s85,4096
s_xor_b32 s85,s85,98304
ds_read_b64_tr_b16 a[64:65],v50 offset:1024
v_mfma_f32_16x16x32_bf16 a[12:15],a[88:91],a[224:227],a[12:15]
v_mfma_f32_16x16x32_bf16 a[12:15],a[92:95],a[228:231],a[12:15]
buffer_load_dwordx4 v36,s[28:31],s84 offen lds
v_mfma_f32_16x16x32_bf16 a[28:31],a[92:95],a[236:239],a[28:31]
ds_read_b64_tr_b16 a[66:67],v50 offset:3072
v_mfma_f32_16x16x32_bf16 a[28:31],a[88:91],a[232:235],a[28:31]
ds_read_b64_tr_b16 a[68:69],v50 offset:17408
v_mfma_f32_16x16x32_bf16 a[44:47],a[88:91],a[240:243],a[44:47]
ds_read_b64_tr_b16 a[70:71],v50 offset:19456
v_mfma_f32_16x16x32_bf16 a[44:47],a[92:95],a[244:247],a[44:47]
ds_read_b64_tr_b16 a[72:73],v50 offset:1032
v_mfma_f32_16x16x32_bf16 a[60:63],a[92:95],a[252:255],a[60:63]
v_mfma_f32_16x16x32_bf16 a[60:63],a[88:91],a[248:251],a[60:63]
buffer_load_dwordx4 v37,s[28:31],s84 offen offset:1024 lds
v_mfma_f32_16x16x32_bf16 v[64:67],a[96:99],a[224:227],v[64:67]
ds_read_b64_tr_b16 a[74:75],v50 offset:3080
v_mfma_f32_16x16x32_bf16 v[64:67],a[100:103],a[228:231],v[64:67]
ds_read_b64_tr_b16 a[76:77],v50 offset:17416
v_mfma_f32_16x16x32_bf16 v[80:83],a[100:103],a[236:239],v[80:83]
ds_read_b64_tr_b16 a[78:79],v50 offset:19464
v_mfma_f32_16x16x32_bf16 v[80:83],a[96:99],a[232:235],v[80:83]
ds_read_b64_tr_b16 a[80:81],v50 offset:1280
v_mfma_f32_16x16x32_bf16 v[96:99],a[96:99],a[240:243],v[96:99]
v_mfma_f32_16x16x32_bf16 v[96:99],a[100:103],a[244:247],v[96:99]
buffer_load_dwordx4 v38,s[28:31],s84 offen offset:2048 lds
v_mfma_f32_16x16x32_bf16 v[112:115],a[100:103],a[252:255],v[112:115]
ds_read_b64_tr_b16 a[82:83],v50 offset:3328
v_mfma_f32_16x16x32_bf16 v[112:115],a[96:99],a[248:251],v[112:115]
ds_read_b64_tr_b16 a[84:85],v50 offset:17664
v_mfma_f32_16x16x32_bf16 v[68:71],a[104:107],a[224:227],v[68:71]
s_waitcnt lgkmcnt(5)
ds_read_b64_tr_b16 a[86:87],v50 offset:19712
v_mfma_f32_16x16x32_bf16 v[68:71],a[108:111],a[228:231],v[68:71]
ds_read_b64_tr_b16 a[88:89],v50 offset:1288
v_mfma_f32_16x16x32_bf16 v[84:87],a[108:111],a[236:239],v[84:87]
v_mfma_f32_16x16x32_bf16 v[84:87],a[104:107],a[232:235],v[84:87]
buffer_load_dwordx4 v39,s[28:31],s84 offen offset:3072 lds
v_mfma_f32_16x16x32_bf16 v[100:103],a[104:107],a[240:243],v[100:103]
s_add_u32 s84,s84,128
ds_read_b64_tr_b16 a[90:91],v50 offset:3336
v_mfma_f32_16x16x32_bf16 v[100:103],a[108:111],a[244:247],v[100:103]
ds_read_b64_tr_b16 a[92:93],v50 offset:17672
v_mfma_f32_16x16x32_bf16 v[116:119],a[108:111],a[252:255],v[116:119]
ds_read_b64_tr_b16 a[94:95],v50 offset:19720
v_mfma_f32_16x16x32_bf16 v[116:119],a[104:107],a[248:251],v[116:119]
s_add_u32 m0,s88,0
ds_read_b64_tr_b16 a[96:97],v50 offset:1536
v_mfma_f32_16x16x32_bf16 v[72:75],a[112:115],a[224:227],v[72:75]
v_mfma_f32_16x16x32_bf16 v[72:75],a[116:119],a[228:231],v[72:75]
buffer_load_dwordx4 v40,s[36:39],s83 offen lds
v_mfma_f32_16x16x32_bf16 v[88:91],a[116:119],a[236:239],v[88:91]
ds_read_b64_tr_b16 a[98:99],v50 offset:3584
v_mfma_f32_16x16x32_bf16 v[88:91],a[112:115],a[232:235],v[88:91]
ds_read_b64_tr_b16 a[100:101],v50 offset:17920
v_mfma_f32_16x16x32_bf16 v[104:107],a[112:115],a[240:243],v[104:107]
ds_read_b64_tr_b16 a[102:103],v50 offset:19968
v_mfma_f32_16x16x32_bf16 v[104:107],a[116:119],a[244:247],v[104:107]
s_add_u32 m0,s88,1024
ds_read_b64_tr_b16 a[104:105],v50 offset:1544
v_mfma_f32_16x16x32_bf16 v[120:123],a[116:119],a[252:255],v[120:123]
v_mfma_f32_16x16x32_bf16 v[120:123],a[112:115],a[248:251],v[120:123]
buffer_load_dwordx4 v41,s[36:39],s83 offen lds
v_mfma_f32_16x16x32_bf16 v[76:79],a[120:123],a[224:227],v[76:79]
ds_read_b64_tr_b16 a[106:107],v50 offset:3592
v_mfma_f32_16x16x32_bf16 v[76:79],a[124:127],a[228:231],v[76:79]
ds_read_b64_tr_b16 a[108:109],v50 offset:17928
v_mfma_f32_16x16x32_bf16 v[92:95],a[124:127],a[236:239],v[92:95]
ds_read_b64_tr_b16 a[110:111],v50 offset:19976
v_mfma_f32_16x16x32_bf16 v[92:95],a[120:123],a[232:235],v[92:95]
s_add_u32 m0,s88,2048
ds_read_b64_tr_b16 a[112:113],v50 offset:1792
v_mfma_f32_16x16x32_bf16 v[108:111],a[120:123],a[240:243],v[108:111]
v_mfma_f32_16x16x32_bf16 v[108:111],a[124:127],a[244:247],v[108:111]
buffer_load_dwordx4 v42,s[36:39],s83 offen lds
v_mfma_f32_16x16x32_bf16 v[124:127],a[124:127],a[252:255],v[124:127]
ds_read_b64_tr_b16 a[114:115],v50 offset:3840
v_mfma_f32_16x16x32_bf16 v[124:127],a[120:123],a[248:251],v[124:127]
ds_read_b64_tr_b16 a[116:117],v50 offset:18176
v_mfma_f32_16x16x32_bf16 v[128:131],a[64:67],a[224:227],v[128:131]
ds_read_b64_tr_b16 a[118:119],v50 offset:20224
v_mfma_f32_16x16x32_bf16 v[128:131],a[68:71],a[228:231],v[128:131]
s_add_u32 m0,s88,3072
ds_read_b64_tr_b16 a[120:121],v50 offset:1800
v_mfma_f32_16x16x32_bf16 v[160:163],a[68:71],a[236:239],v[160:163]
v_mfma_f32_16x16x32_bf16 v[160:163],a[64:67],a[232:235],v[160:163]
buffer_load_dwordx4 v43,s[36:39],s83 offen lds
v_mfma_f32_16x16x32_bf16 v[192:195],a[64:67],a[240:243],v[192:195]
ds_read_b64_tr_b16 a[122:123],v50 offset:3848
v_mfma_f32_16x16x32_bf16 v[192:195],a[68:71],a[244:247],v[192:195]
ds_read_b64_tr_b16 a[124:125],v50 offset:18184
v_mfma_f32_16x16x32_bf16 v[224:227],a[68:71],a[252:255],v[224:227]
ds_read_b64_tr_b16 a[126:127],v50 offset:20232
v_add_u32 v50,s86,v51
v_mfma_f32_16x16x32_bf16 v[224:227],a[64:67],a[248:251],v[224:227]
s_add_u32 m0,s88,4096
v_mfma_f32_16x16x32_bf16 v[132:135],a[72:75],a[224:227],v[132:135]
s_waitcnt lgkmcnt(4)
v_mfma_f32_16x16x32_bf16 v[132:135],a[76:79],a[228:231],v[132:135]
buffer_load_dwordx4 v44,s[36:39],s83 offen lds
v_mfma_f32_16x16x32_bf16 v[164:167],a[76:79],a[236:239],v[164:167]
v_mfma_f32_16x16x32_bf16 v[164:167],a[72:75],a[232:235],v[164:167]
v_mfma_f32_16x16x32_bf16 v[196:199],a[72:75],a[240:243],v[196:199]
v_mfma_f32_16x16x32_bf16 v[196:199],a[76:79],a[244:247],v[196:199]
s_add_u32 m0,s88,5120
v_mfma_f32_16x16x32_bf16 v[228:231],a[76:79],a[252:255],v[228:231]
v_mfma_f32_16x16x32_bf16 v[228:231],a[72:75],a[248:251],v[228:231]
buffer_load_dwordx4 v45,s[36:39],s83 offen lds
v_mfma_f32_16x16x32_bf16 v[136:139],a[80:83],a[224:227],v[136:139]
v_mfma_f32_16x16x32_bf16 v[136:139],a[84:87],a[228:231],v[136:139]
v_mfma_f32_16x16x32_bf16 v[168:171],a[84:87],a[236:239],v[168:171]
v_mfma_f32_16x16x32_bf16 v[168:171],a[80:83],a[232:235],v[168:171]
s_add_u32 m0,s88,6144
v_mfma_f32_16x16x32_bf16 v[200:203],a[80:83],a[240:243],v[200:203]
v_mfma_f32_16x16x32_bf16 v[200:203],a[84:87],a[244:247],v[200:203]
buffer_load_dwordx4 v46,s[36:39],s83 offen lds
v_mfma_f32_16x16x32_bf16 v[232:235],a[84:87],a[252:255],v[232:235]
v_mfma_f32_16x16x32_bf16 v[232:235],a[80:83],a[248:251],v[232:235]
v_mfma_f32_16x16x32_bf16 v[140:143],a[88:91],a[224:227],v[140:143]
)A"
R"A(v_mfma_f32_16x16x32_bf16 v[140:143],a[92:95],a[228:231],v[140:143]
s_add_u32 m0,s88,7168
s_mov_b32 s88,s69
s_and_b32 s69,s75,0xfffc0000
v_mfma_f32_16x16x32_bf16 v[172:175],a[92:95],a[236:239],v[172:175]
s_add_u32 s75,s75,16384
v_mfma_f32_16x16x32_bf16 v[172:175],a[88:91],a[232:235],v[172:175]
buffer_load_dwordx4 v47,s[36:39],s83 offen lds
v_mfma_f32_16x16x32_bf16 v[204:207],a[88:91],a[240:243],v[204:207]
s_waitcnt lgkmcnt(0)
s_add_u32 s83,s83,524288
s_sub_u32 s92,s92,s8
v_mfma_f32_16x16x32_bf16 v[204:207],a[92:95],a[244:247],v[204:207]
s_mov_b32 s93,782
s_cmp_ge_u32 s63,2
s_cselect_b32 s93,823,s93
v_mfma_f32_16x16x32_bf16 v[236:239],a[92:95],a[252:255],v[236:239]
v_mfma_f32_16x16x32_bf16 v[236:239],a[88:91],a[248:251],v[236:239]
s_add_u32 s10,s20,s74
s_addc_u32 s11,s21,0
s_waitcnt vmcnt(25)
s_barrier
v_mfma_f32_16x16x32_bf16 v[144:147],a[96:99],a[224:227],v[144:147]
s_add_u32 m0,s76,0
s_waitcnt vmcnt(17)
ds_read_b128 a[192:195],v48
v_mfma_f32_16x16x32_bf16 v[144:147],a[100:103],a[228:231],v[144:147]
ds_read_b128 a[196:199],v49
v_mfma_f32_16x16x32_bf16 v[176:179],a[100:103],a[236:239],v[176:179]
global_load_lds_dwordx4 v4,s[10:11] nt
ds_read_b128 a[200:203],v48 offset:2048
v_mfma_f32_16x16x32_bf16 v[176:179],a[96:99],a[232:235],v[176:179]
ds_read_b128 a[204:207],v49 offset:2048
v_mfma_f32_16x16x32_bf16 v[208:211],a[96:99],a[240:243],v[208:211]
v_mfma_f32_16x16x32_bf16 v[208:211],a[100:103],a[244:247],v[208:211]
ds_read_b128 a[208:211],v48 offset:4096
v_mfma_f32_16x16x32_bf16 v[240:243],a[100:103],a[252:255],v[240:243]
ds_read_b128 a[212:215],v49 offset:4096
v_mfma_f32_16x16x32_bf16 v[240:243],a[96:99],a[248:251],v[240:243]
ds_read_b128 a[216:219],v48 offset:6144
v_xor_b32 v48,98304,v48
v_mfma_f32_16x16x32_bf16 v[148:151],a[104:107],a[224:227],v[148:151]
v_mfma_f32_16x16x32_bf16 v[148:151],a[108:111],a[228:231],v[148:151]
ds_read_b128 a[220:223],v49 offset:6144
v_xor_b32 v49,98304,v49
v_mfma_f32_16x16x32_bf16 v[180:183],a[108:111],a[236:239],v[180:183]
ds_read_b64_tr_b16 a[64:65],v50
v_mfma_f32_16x16x32_bf16 v[180:183],a[104:107],a[232:235],v[180:183]
ds_read_b64_tr_b16 a[66:67],v50 offset:2048
v_mfma_f32_16x16x32_bf16 v[212:215],a[104:107],a[240:243],v[212:215]
v_mfma_f32_16x16x32_bf16 v[212:215],a[108:111],a[244:247],v[212:215]
ds_read_b64_tr_b16 a[68:69],v50 offset:16384
v_mfma_f32_16x16x32_bf16 v[244:247],a[108:111],a[252:255],v[244:247]
ds_read_b64_tr_b16 a[70:71],v50 offset:18432
v_mfma_f32_16x16x32_bf16 v[244:247],a[104:107],a[248:251],v[244:247]
ds_read_b64_tr_b16 a[72:73],v50 offset:8
v_mfma_f32_16x16x32_bf16 v[152:155],a[112:115],a[224:227],v[152:155]
v_mfma_f32_16x16x32_bf16 v[152:155],a[116:119],a[228:231],v[152:155]
ds_read_b64_tr_b16 a[74:75],v50 offset:2056
v_mfma_f32_16x16x32_bf16 v[184:187],a[116:119],a[236:239],v[184:187]
global_load_lds_dwordx4 v4,s[10:11] offset:1024 nt
ds_read_b64_tr_b16 a[76:77],v50 offset:16392
v_mfma_f32_16x16x32_bf16 v[184:187],a[112:115],a[232:235],v[184:187]
ds_read_b64_tr_b16 a[78:79],v50 offset:18440
v_mfma_f32_16x16x32_bf16 v[216:219],a[112:115],a[240:243],v[216:219]
v_mfma_f32_16x16x32_bf16 v[216:219],a[116:119],a[244:247],v[216:219]
ds_read_b64_tr_b16 a[80:81],v50 offset:1024
v_mfma_f32_16x16x32_bf16 v[248:251],a[116:119],a[252:255],v[248:251]
ds_read_b64_tr_b16 a[82:83],v50 offset:3072
v_mfma_f32_16x16x32_bf16 v[248:251],a[112:115],a[248:251],v[248:251]
ds_read_b64_tr_b16 a[84:85],v50 offset:17408
v_mfma_f32_16x16x32_bf16 v[156:159],a[120:123],a[224:227],v[156:159]
v_mfma_f32_16x16x32_bf16 v[156:159],a[124:127],a[228:231],v[156:159]
ds_read_b64_tr_b16 a[86:87],v50 offset:19456
v_mfma_f32_16x16x32_bf16 v[188:191],a[124:127],a[236:239],v[188:191]
ds_read_b64_tr_b16 a[88:89],v50 offset:1032
v_mfma_f32_16x16x32_bf16 v[188:191],a[120:123],a[232:235],v[188:191]
s_cmp_lt_u32 s92,s93
s_cselect_b32 s5,5,0
s_cselect_b32 s7,2,0
s_cselect_b32 s9,8,10
ds_read_b64_tr_b16 a[90:91],v50 offset:3080
v_mfma_f32_16x16x32_bf16 v[220:223],a[120:123],a[240:243],v[220:223]
s_mov_b32 s4,s5
v_mfma_f32_16x16x32_bf16 v[220:223],a[124:127],a[244:247],v[220:223]
ds_read_b64_tr_b16 a[92:93],v50 offset:17416
v_mfma_f32_16x16x32_bf16 v[252:255],a[124:127],a[252:255],v[252:255]
ds_read_b64_tr_b16 a[94:95],v50 offset:19464
v_mfma_f32_16x16x32_bf16 v[252:255],a[120:123],a[248:251],v[252:255]
s_waitcnt vmcnt(10) lgkmcnt(5)
ds_read_b128 a[224:227],v48
v_mfma_f32_16x16x32_bf16 v[128:131],a[64:67],a[192:195],v[128:131]
v_mfma_f32_16x16x32_bf16 v[128:131],a[68:71],a[196:199],v[128:131]
ds_read_b128 a[228:231],v49
v_mfma_f32_16x16x32_bf16 v[160:163],a[68:71],a[204:207],v[160:163]
global_load_lds_dwordx4 v5,s[10:11] offset:2048 nt
ds_read_b128 a[236:239],v49 offset:2048
v_mfma_f32_16x16x32_bf16 v[160:163],a[64:67],a[200:203],v[160:163]
ds_read_b128 a[232:235],v48 offset:2048
v_mfma_f32_16x16x32_bf16 v[192:195],a[64:67],a[208:211],v[192:195]
v_mfma_f32_16x16x32_bf16 v[192:195],a[68:71],a[212:215],v[192:195]
ds_read_b128 a[240:243],v48 offset:4096
v_mfma_f32_16x16x32_bf16 v[224:227],a[68:71],a[220:223],v[224:227]
ds_read_b128 a[244:247],v49 offset:4096
v_mfma_f32_16x16x32_bf16 v[224:227],a[64:67],a[216:219],v[224:227]
ds_read_b128 a[252:255],v49 offset:6144
v_mfma_f32_16x16x32_bf16 v[132:135],a[72:75],a[192:195],v[132:135]
v_mfma_f32_16x16x32_bf16 v[132:135],a[76:79],a[196:199],v[132:135]
ds_read_b128 a[248:251],v48 offset:6144
v_mfma_f32_16x16x32_bf16 v[164:167],a[76:79],a[204:207],v[164:167]
ds_read_b64_tr_b16 a[96:97],v50 offset:256
v_mfma_f32_16x16x32_bf16 v[164:167],a[72:75],a[200:203],v[164:167]
ds_read_b64_tr_b16 a[98:99],v50 offset:2304
v_mfma_f32_16x16x32_bf16 v[196:199],a[72:75],a[208:211],v[196:199]
v_mfma_f32_16x16x32_bf16 v[196:199],a[76:79],a[212:215],v[196:199]
ds_read_b64_tr_b16 a[100:101],v50 offset:16640
v_mfma_f32_16x16x32_bf16 v[228:231],a[76:79],a[220:223],v[228:231]
ds_read_b64_tr_b16 a[102:103],v50 offset:18688
v_mfma_f32_16x16x32_bf16 v[228:231],a[72:75],a[216:219],v[228:231]
s_waitcnt lgkmcnt(5)
ds_read_b64_tr_b16 a[104:105],v50 offset:1280
v_mfma_f32_16x16x32_bf16 v[128:131],a[80:83],a[224:227],v[128:131]
v_mfma_f32_16x16x32_bf16 v[128:131],a[84:87],a[228:231],v[128:131]
ds_read_b64_tr_b16 a[106:107],v50 offset:3328
v_mfma_f32_16x16x32_bf16 v[160:163],a[84:87],a[236:239],v[160:163]
global_load_lds_dwordx4 v5,s[10:11] offset:3072
ds_read_b64_tr_b16 a[108:109],v50 offset:17664
v_mfma_f32_16x16x32_bf16 v[160:163],a[80:83],a[232:235],v[160:163]
ds_read_b64_tr_b16 a[110:111],v50 offset:19712
v_mfma_f32_16x16x32_bf16 v[192:195],a[80:83],a[240:243],v[192:195]
v_mfma_f32_16x16x32_bf16 v[192:195],a[84:87],a[244:247],v[192:195]
ds_read_b64_tr_b16 a[112:113],v50 offset:264
v_mul_f32 v128,s71,v128
v_mfma_f32_16x16x32_bf16 v[224:227],a[84:87],a[252:255],v[224:227]
s_waitcnt lgkmcnt(5)
ds_read_b64_tr_b16 a[114:115],v50 offset:2312
v_mul_f32 v129,s71,v129
v_mfma_f32_16x16x32_bf16 v[224:227],a[80:83],a[248:251],v[224:227]
ds_read_b64_tr_b16 a[116:117],v50 offset:16648
v_mul_f32 v130,s71,v130
v_mfma_f32_16x16x32_bf16 v[132:135],a[88:91],a[224:227],v[132:135]
v_mul_f32 v131,s71,v131
v_cvt_pk_bf16_f32 v128,v128,v129
v_mfma_f32_16x16x32_bf16 v[132:135],a[92:95],a[228:231],v[132:135]
ds_read_b64_tr_b16 a[118:119],v50 offset:18696
v_cvt_pk_bf16_f32 v129,v130,v131
v_mfma_f32_16x16x32_bf16 v[164:167],a[92:95],a[236:239],v[164:167]
ds_read_b64_tr_b16 a[120:121],v50 offset:1288
v_mul_f32 v192,s71,v192
v_mfma_f32_16x16x32_bf16 v[164:167],a[88:91],a[232:235],v[164:167]
ds_read_b64_tr_b16 a[122:123],v50 offset:3336
v_mul_f32 v193,s71,v193
v_mfma_f32_16x16x32_bf16 v[196:199],a[88:91],a[240:243],v[196:199]
v_mul_f32 v132,s71,v132
v_mul_f32 v133,s71,v133
v_mfma_f32_16x16x32_bf16 v[196:199],a[92:95],a[244:247],v[196:199]
ds_read_b64_tr_b16 a[124:125],v50 offset:17672
v_mul_f32 v134,s71,v134
v_mfma_f32_16x16x32_bf16 v[228:231],a[92:95],a[252:255],v[228:231]
s_add_u32 s10,s24,s74
s_addc_u32 s11,s25,0
ds_read_b64_tr_b16 a[126:127],v50 offset:19720
v_mfma_f32_16x16x32_bf16 v[228:231],a[88:91],a[248:251],v[228:231]
s_add_u32 s74,s74,16384
s_and_b32 s74,s74,0x3ffff
ds_read_b64_tr_b16 a[128:129],v50 offset:512
v_mfma_f32_16x16x32_bf16 v[136:139],a[96:99],a[192:195],v[136:139]
s_add_u32 m0,s76,16384
s_or_b32 s74,s74,s69
s_sub_u32 s76,s76,32768
v_mfma_f32_16x16x32_bf16 v[136:139],a[100:103],a[196:199],v[136:139]
s_and_b32 s76,s76,0x1ffff
s_and_b32 s69,s75,0xfffc0000
ds_read_b64_tr_b16 a[130:131],v50 offset:2560
s_waitcnt lgkmcnt(6)
v_mfma_f32_16x16x32_bf16 v[136:139],a[104:107],a[224:227],v[136:139]
global_load_lds_dwordx4 v4,s[10:11]
ds_read_b64_tr_b16 a[132:133],v50 offset:16896
v_mfma_f32_16x16x32_bf16 v[136:139],a[108:111],a[228:231],v[136:139]
s_add_u32 s75,s75,16384
ds_read_b64_tr_b16 a[134:135],v50 offset:18944
v_mul_f32 v135,s71,v135
v_mfma_f32_16x16x32_bf16 v[168:171],a[108:111],a[236:239],v[168:171]
v_cvt_pk_bf16_f32 v130,v132,v133
v_cvt_pk_bf16_f32 v131,v134,v135
v_mfma_f32_16x16x32_bf16 v[168:171],a[104:107],a[232:235],v[168:171]
ds_read_b64_tr_b16 a[136:137],v50 offset:1536
v_mul_f32 v194,s71,v194
v_mfma_f32_16x16x32_bf16 v[168:171],a[100:103],a[204:207],v[168:171]
ds_read_b64_tr_b16 a[138:139],v50 offset:3584
v_mul_f32 v195,s71,v195
v_mfma_f32_16x16x32_bf16 v[168:171],a[96:99],a[200:203],v[168:171]
ds_read_b64_tr_b16 a[140:141],v50 offset:17920
v_mul_f32 v196,s71,v196
v_mfma_f32_16x16x32_bf16 v[200:203],a[96:99],a[208:211],v[200:203]
v_mul_f32 v197,s71,v197
v_mul_f32 v198,s71,v198
v_mfma_f32_16x16x32_bf16 v[200:203],a[100:103],a[212:215],v[200:203]
ds_read_b64_tr_b16 a[142:143],v50 offset:19968
v_mul_f32 v199,s71,v199
v_mfma_f32_16x16x32_bf16 v[200:203],a[104:107],a[240:243],v[200:203]
ds_read_b64_tr_b16 a[144:145],v50 offset:520
v_cvt_pk_bf16_f32 v192,v192,v193
v_mfma_f32_16x16x32_bf16 v[200:203],a[108:111],a[244:247],v[200:203]
ds_read_b64_tr_b16 a[146:147],v50 offset:2568
v_cvt_pk_bf16_f32 v193,v194,v195
v_mfma_f32_16x16x32_bf16 v[232:235],a[108:111],a[252:255],v[232:235]
v_cvt_pk_bf16_f32 v194,v196,v197
v_cvt_pk_bf16_f32 v195,v198,v199
v_mfma_f32_16x16x32_bf16 v[232:235],a[104:107],a[248:251],v[232:235]
ds_read_b64_tr_b16 a[148:149],v50 offset:16904
v_mul_f32 v136,s71,v136
v_mfma_f32_16x16x32_bf16 v[232:235],a[100:103],a[220:223],v[232:235]
ds_read_b64_tr_b16 a[150:151],v50 offset:18952
v_mul_f32 v137,s71,v137
v_mfma_f32_16x16x32_bf16 v[232:235],a[96:99],a[216:219],v[232:235]
ds_read_b64_tr_b16 a[152:153],v50 offset:1544
v_mul_f32 v138,s71,v138
v_mfma_f32_16x16x32_bf16 v[140:143],a[112:115],a[192:195],v[140:143]
v_mul_f32 v139,s71,v139
v_cvt_pk_bf16_f32 v136,v136,v137
v_mfma_f32_16x16x32_bf16 v[140:143],a[116:119],a[196:199],v[140:143]
s_waitcnt lgkmcnt(5)
ds_read_b64_tr_b16 a[154:155],v50 offset:3592
v_cvt_pk_bf16_f32 v137,v138,v139
v_mfma_f32_16x16x32_bf16 v[140:143],a[120:123],a[224:227],v[140:143]
global_load_lds_dwordx4 v5,s[10:11] offset:1024
ds_read_b64_tr_b16 a[156:157],v50 offset:17928
v_mfma_f32_16x16x32_bf16 v[140:143],a[124:127],a[228:231],v[140:143]
ds_read_b64_tr_b16 a[158:159],v50 offset:19976
v_mul_f32 v160,s71,v160
v_mfma_f32_16x16x32_bf16 v[172:175],a[124:127],a[236:239],v[172:175]
v_mul_f32 v161,s71,v161
v_mul_f32 v162,s71,v162
v_mfma_f32_16x16x32_bf16 v[172:175],a[120:123],a[232:235],v[172:175]
ds_read_b64_tr_b16 a[160:161],v50 offset:768
v_mul_f32 v163,s71,v163
v_mfma_f32_16x16x32_bf16 v[172:175],a[116:119],a[204:207],v[172:175]
ds_read_b64_tr_b16 a[162:163],v50 offset:2816
v_mul_f32 v140,s71,v140
v_mfma_f32_16x16x32_bf16 v[172:175],a[112:115],a[200:203],v[172:175]
ds_read_b64_tr_b16 a[164:165],v50 offset:17152
v_mul_f32 v141,s71,v141
v_mfma_f32_16x16x32_bf16 v[204:207],a[112:115],a[208:211],v[204:207]
v_mul_f32 v142,s71,v142
v_mul_f32 v143,s71,v143
v_mfma_f32_16x16x32_bf16 v[204:207],a[116:119],a[212:215],v[204:207]
ds_read_b64_tr_b16 a[166:167],v50 offset:19200
v_cvt_pk_bf16_f32 v138,v140,v141
v_mfma_f32_16x16x32_bf16 v[204:207],a[120:123],a[240:243],v[204:207]
ds_read_b64_tr_b16 a[168:169],v50 offset:1792
v_cvt_pk_bf16_f32 v139,v142,v143
v_mfma_f32_16x16x32_bf16 v[204:207],a[124:127],a[244:247],v[204:207]
ds_read_b64_tr_b16 a[170:171],v50 offset:3840
)A"
R"A(v_mul_f32 v164,s71,v164
v_mfma_f32_16x16x32_bf16 v[236:239],a[124:127],a[252:255],v[236:239]
v_mul_f32 v165,s71,v165
v_mul_f32 v166,s71,v166
v_mfma_f32_16x16x32_bf16 v[236:239],a[120:123],a[248:251],v[236:239]
ds_read_b64_tr_b16 a[172:173],v50 offset:18176
v_mul_f32 v167,s71,v167
v_mfma_f32_16x16x32_bf16 v[236:239],a[116:119],a[220:223],v[236:239]
ds_read_b64_tr_b16 a[174:175],v50 offset:20224
v_cvt_pk_bf16_f32 v160,v160,v161
v_mfma_f32_16x16x32_bf16 v[236:239],a[112:115],a[216:219],v[236:239]
ds_read_b64_tr_b16 a[176:177],v50 offset:776
v_cvt_pk_bf16_f32 v161,v162,v163
v_mfma_f32_16x16x32_bf16 v[144:147],a[128:131],a[192:195],v[144:147]
v_cvt_pk_bf16_f32 v162,v164,v165
v_cvt_pk_bf16_f32 v163,v166,v167
v_mfma_f32_16x16x32_bf16 v[144:147],a[132:135],a[196:199],v[144:147]
ds_read_b64_tr_b16 a[178:179],v50 offset:2824
v_mul_f32 v224,s71,v224
v_mfma_f32_16x16x32_bf16 v[144:147],a[136:139],a[224:227],v[144:147]
global_load_lds_dwordx4 v4,s[10:11] offset:2048
ds_read_b64_tr_b16 a[180:181],v50 offset:17160
v_mfma_f32_16x16x32_bf16 v[144:147],a[140:143],a[228:231],v[144:147]
ds_read_b64_tr_b16 a[182:183],v50 offset:19208
v_mul_f32 v225,s71,v225
v_mfma_f32_16x16x32_bf16 v[176:179],a[140:143],a[236:239],v[176:179]
v_mul_f32 v226,s71,v226
v_mul_f32 v227,s71,v227
v_mfma_f32_16x16x32_bf16 v[176:179],a[136:139],a[232:235],v[176:179]
ds_read_b64_tr_b16 a[184:185],v50 offset:1800
v_mul_f32 v228,s71,v228
v_mfma_f32_16x16x32_bf16 v[176:179],a[132:135],a[204:207],v[176:179]
ds_read_b64_tr_b16 a[186:187],v50 offset:3848
v_mul_f32 v144,s71,v144
v_mfma_f32_16x16x32_bf16 v[176:179],a[128:131],a[200:203],v[176:179]
ds_read_b64_tr_b16 a[188:189],v50 offset:18184
v_mul_f32 v145,s71,v145
v_mfma_f32_16x16x32_bf16 v[208:211],a[128:131],a[208:211],v[208:211]
v_mul_f32 v146,s71,v146
v_mul_f32 v147,s71,v147
v_mfma_f32_16x16x32_bf16 v[208:211],a[132:135],a[212:215],v[208:211]
ds_read_b64_tr_b16 a[190:191],v50 offset:20232
v_cvt_pk_bf16_f32 v144,v144,v145
v_mfma_f32_16x16x32_bf16 v[208:211],a[136:139],a[240:243],v[208:211]
v_cvt_pk_bf16_f32 v145,v146,v147
v_mul_f32 v229,s71,v229
v_mfma_f32_16x16x32_bf16 v[208:211],a[140:143],a[244:247],v[208:211]
v_mul_f32 v230,s71,v230
v_mul_f32 v231,s71,v231
v_mfma_f32_16x16x32_bf16 v[240:243],a[140:143],a[252:255],v[240:243]
v_cvt_pk_bf16_f32 v224,v224,v225
v_cvt_pk_bf16_f32 v225,v226,v227
v_mfma_f32_16x16x32_bf16 v[240:243],a[136:139],a[248:251],v[240:243]
v_cvt_pk_bf16_f32 v226,v228,v229
v_cvt_pk_bf16_f32 v227,v230,v231
v_mfma_f32_16x16x32_bf16 v[240:243],a[132:135],a[220:223],v[240:243]
v_mul_f32 v168,s71,v168
v_mul_f32 v169,s71,v169
v_mfma_f32_16x16x32_bf16 v[240:243],a[128:131],a[216:219],v[240:243]
v_mul_f32 v170,s71,v170
v_mul_f32 v171,s71,v171
s_waitcnt vmcnt(7)
s_barrier
s_waitcnt lgkmcnt(1)
v_mfma_f32_16x16x32_bf16 v[148:151],a[144:147],a[192:195],v[148:151]
ds_read_b64_tr_b16 a[64:65],v52
v_mul_f32 v172,s71,v172
v_mfma_f32_16x16x32_bf16 v[148:151],a[148:151],a[196:199],v[148:151]
global_load_lds_dwordx4 v5,s[10:11] offset:3072
ds_read_b64_tr_b16 a[66:67],v52 offset:2048
v_mfma_f32_16x16x32_bf16 v[148:151],a[152:155],a[224:227],v[148:151]
ds_read_b64_tr_b16 a[68:69],v52 offset:16384
v_mul_f32 v173,s71,v173
v_mfma_f32_16x16x32_bf16 v[148:151],a[156:159],a[228:231],v[148:151]
ds_read_b64_tr_b16 a[70:71],v52 offset:18432
v_mul_f32 v174,s71,v174
v_mfma_f32_16x16x32_bf16 v[180:183],a[156:159],a[236:239],v[180:183]
v_mul_f32 v175,s71,v175
v_cvt_pk_bf16_f32 v168,v168,v169
v_mfma_f32_16x16x32_bf16 v[180:183],a[152:155],a[232:235],v[180:183]
ds_read_b64_tr_b16 a[72:73],v52 offset:1024
v_cvt_pk_bf16_f32 v169,v170,v171
v_mfma_f32_16x16x32_bf16 v[180:183],a[148:151],a[204:207],v[180:183]
ds_read_b64_tr_b16 a[74:75],v52 offset:3072
v_mul_f32 v148,s71,v148
v_mfma_f32_16x16x32_bf16 v[180:183],a[144:147],a[200:203],v[180:183]
s_nop 0
L_pd3_%=:
s_cbranch_execz L_pd7_%=
ds_read_b64_tr_b16 a[76:77],v52 offset:17408
v_mul_f32 v149,s71,v149
v_mfma_f32_16x16x32_bf16 v[212:215],a[144:147],a[208:211],v[212:215]
v_mul_f32 v150,s71,v150
v_mul_f32 v151,s71,v151
v_mfma_f32_16x16x32_bf16 v[212:215],a[148:151],a[212:215],v[212:215]
ds_read_b64_tr_b16 a[78:79],v52 offset:19456
v_cvt_pk_bf16_f32 v146,v148,v149
v_mfma_f32_16x16x32_bf16 v[212:215],a[152:155],a[240:243],v[212:215]
ds_read_b64_tr_b16 a[80:81],v52 offset:8
v_cvt_pk_bf16_f32 v147,v150,v151
v_mfma_f32_16x16x32_bf16 v[212:215],a[156:159],a[244:247],v[212:215]
ds_read_b64_tr_b16 a[82:83],v52 offset:2056
v_cvt_pk_bf16_f32 v170,v172,v173
v_mfma_f32_16x16x32_bf16 v[244:247],a[156:159],a[252:255],v[244:247]
v_cvt_pk_bf16_f32 v171,v174,v175
v_mul_f32 v176,s71,v176
v_mfma_f32_16x16x32_bf16 v[244:247],a[152:155],a[248:251],v[244:247]
ds_read_b64_tr_b16 a[84:85],v52 offset:16392
v_mul_f32 v177,s71,v177
v_mfma_f32_16x16x32_bf16 v[244:247],a[148:151],a[220:223],v[244:247]
s_add_u32 s10,s20,s74
s_addc_u32 s11,s21,0
ds_read_b64_tr_b16 a[86:87],v52 offset:18440
v_mfma_f32_16x16x32_bf16 v[244:247],a[144:147],a[216:219],v[244:247]
ds_read_b64_tr_b16 a[88:89],v52 offset:1032
v_mul_f32 v178,s71,v178
v_mfma_f32_16x16x32_bf16 v[152:155],a[160:163],a[192:195],v[152:155]
s_add_u32 m0,s76,0
v_mul_f32 v179,s71,v179
v_mul_f32 v180,s71,v180
v_mfma_f32_16x16x32_bf16 v[152:155],a[164:167],a[196:199],v[152:155]
ds_read_b64_tr_b16 a[90:91],v52 offset:3080
v_mul_f32 v181,s71,v181
v_mfma_f32_16x16x32_bf16 v[152:155],a[168:171],a[224:227],v[152:155]
global_load_lds_dwordx4 v4,s[10:11] nt
ds_read_b64_tr_b16 a[92:93],v52 offset:17416
v_mfma_f32_16x16x32_bf16 v[152:155],a[172:175],a[228:231],v[152:155]
ds_read_b64_tr_b16 a[94:95],v52 offset:19464
v_mul_f32 v182,s71,v182
v_mfma_f32_16x16x32_bf16 v[184:187],a[172:175],a[236:239],v[184:187]
v_mul_f32 v183,s71,v183
v_cvt_pk_bf16_f32 v176,v176,v177
v_mfma_f32_16x16x32_bf16 v[184:187],a[168:171],a[232:235],v[184:187]
ds_read_b64_tr_b16 a[96:97],v52 offset:256
v_cvt_pk_bf16_f32 v177,v178,v179
v_mfma_f32_16x16x32_bf16 v[184:187],a[164:167],a[204:207],v[184:187]
ds_read_b64_tr_b16 a[98:99],v52 offset:2304
v_mul_f32 v152,s71,v152
v_mfma_f32_16x16x32_bf16 v[184:187],a[160:163],a[200:203],v[184:187]
ds_read_b64_tr_b16 a[100:101],v52 offset:16640
v_mul_f32 v153,s71,v153
v_mfma_f32_16x16x32_bf16 v[216:219],a[160:163],a[208:211],v[216:219]
v_mul_f32 v154,s71,v154
v_mul_f32 v155,s71,v155
v_mfma_f32_16x16x32_bf16 v[216:219],a[164:167],a[212:215],v[216:219]
ds_read_b64_tr_b16 a[102:103],v52 offset:18688
v_cvt_pk_bf16_f32 v152,v152,v153
v_mfma_f32_16x16x32_bf16 v[216:219],a[168:171],a[240:243],v[216:219]
ds_read_b64_tr_b16 a[104:105],v52 offset:1280
v_cvt_pk_bf16_f32 v153,v154,v155
v_mfma_f32_16x16x32_bf16 v[216:219],a[172:175],a[244:247],v[216:219]
ds_read_b64_tr_b16 a[106:107],v52 offset:3328
v_cvt_pk_bf16_f32 v178,v180,v181
v_mfma_f32_16x16x32_bf16 v[248:251],a[172:175],a[252:255],v[248:251]
v_cvt_pk_bf16_f32 v179,v182,v183
v_mul_f32 v184,s71,v184
v_mfma_f32_16x16x32_bf16 v[248:251],a[168:171],a[248:251],v[248:251]
ds_read_b64_tr_b16 a[108:109],v52 offset:17664
v_mul_f32 v185,s71,v185
v_mfma_f32_16x16x32_bf16 v[248:251],a[164:167],a[220:223],v[248:251]
ds_read_b64_tr_b16 a[110:111],v52 offset:19712
v_mul_f32 v186,s71,v186
v_mfma_f32_16x16x32_bf16 v[248:251],a[160:163],a[216:219],v[248:251]
ds_read_b64_tr_b16 a[112:113],v52 offset:264
v_mul_f32 v187,s71,v187
v_mfma_f32_16x16x32_bf16 v[156:159],a[176:179],a[192:195],v[156:159]
v_cvt_pk_bf16_f32 v184,v184,v185
v_cvt_pk_bf16_f32 v185,v186,v187
v_mfma_f32_16x16x32_bf16 v[156:159],a[180:183],a[196:199],v[156:159]
global_load_lds_dwordx4 v4,s[10:11] offset:1024 nt
ds_read_b64_tr_b16 a[114:115],v52 offset:2312
v_mfma_f32_16x16x32_bf16 v[156:159],a[184:187],a[224:227],v[156:159]
s_waitcnt lgkmcnt(5)
ds_read_b64_tr_b16 a[116:117],v52 offset:16648
v_mul_f32 v200,s71,v200
v_mfma_f32_16x16x32_bf16 v[156:159],a[188:191],a[228:231],v[156:159]
ds_read_b64_tr_b16 a[118:119],v52 offset:18696
v_mul_f32 v201,s71,v201
v_mfma_f32_16x16x32_bf16 v[188:191],a[188:191],a[236:239],v[188:191]
v_mul_f32 v202,s71,v202
v_mul_f32 v203,s71,v203
v_mfma_f32_16x16x32_bf16 v[188:191],a[184:187],a[232:235],v[188:191]
ds_read_b64_tr_b16 a[120:121],v52 offset:1288
v_mul_f32 v204,s71,v204
v_mfma_f32_16x16x32_bf16 v[188:191],a[180:183],a[204:207],v[188:191]
ds_read_b64_tr_b16 a[122:123],v52 offset:3336
v_mul_f32 v156,s71,v156
v_mfma_f32_16x16x32_bf16 v[188:191],a[176:179],a[200:203],v[188:191]
ds_read_b64_tr_b16 a[124:125],v52 offset:17672
v_mul_f32 v157,s71,v157
v_mfma_f32_16x16x32_bf16 v[220:223],a[176:179],a[208:211],v[220:223]
v_mul_f32 v158,s71,v158
v_mul_f32 v159,s71,v159
v_mfma_f32_16x16x32_bf16 v[220:223],a[180:183],a[212:215],v[220:223]
ds_read_b64_tr_b16 a[126:127],v52 offset:19720
v_cvt_pk_bf16_f32 v154,v156,v157
v_mfma_f32_16x16x32_bf16 v[220:223],a[184:187],a[240:243],v[220:223]
v_cvt_pk_bf16_f32 v155,v158,v159
v_mul_f32 v188,s71,v188
v_mfma_f32_16x16x32_bf16 v[220:223],a[188:191],a[244:247],v[220:223]
v_mul_f32 v189,s71,v189
v_mul_f32 v190,s71,v190
v_mfma_f32_16x16x32_bf16 v[252:255],a[188:191],a[252:255],v[252:255]
v_mul_f32 v191,s71,v191
v_cvt_pk_bf16_f32 v186,v188,v189
v_mfma_f32_16x16x32_bf16 v[252:255],a[184:187],a[248:251],v[252:255]
v_cvt_pk_bf16_f32 v187,v190,v191
v_mul_f32 v205,s71,v205
v_mfma_f32_16x16x32_bf16 v[252:255],a[180:183],a[220:223],v[252:255]
v_mul_f32 v206,s71,v206
v_mul_f32 v207,s71,v207
v_mfma_f32_16x16x32_bf16 v[252:255],a[176:179],a[216:219],v[252:255]
v_cvt_pk_bf16_f32 v200,v200,v201
v_cvt_pk_bf16_f32 v201,v202,v203
v_mfma_f32_16x16x32_bf16 a[0:3],a[64:67],a[192:195],a[0:3]
global_load_lds_dwordx4 v5,s[10:11] offset:2048 nt
v_cvt_pk_bf16_f32 v202,v204,v205
v_mfma_f32_16x16x32_bf16 a[0:3],a[68:71],a[196:199],a[0:3]
v_cvt_pk_bf16_f32 v203,v206,v207
v_mul_f32 v208,s71,v208
v_mfma_f32_16x16x32_bf16 a[0:3],a[72:75],a[224:227],a[0:3]
v_mul_f32 v209,s71,v209
v_mul_f32 v210,s71,v210
v_mfma_f32_16x16x32_bf16 a[0:3],a[76:79],a[228:231],a[0:3]
v_mul_f32 v211,s71,v211
v_mul_f32 v212,s71,v212
v_mfma_f32_16x16x32_bf16 a[16:19],a[76:79],a[236:239],a[16:19]
v_mul_f32 v213,s71,v213
v_mul_f32 v214,s71,v214
v_mfma_f32_16x16x32_bf16 a[16:19],a[72:75],a[232:235],a[16:19]
v_mul_f32 v215,s71,v215
v_cvt_pk_bf16_f32 v208,v208,v209
v_mfma_f32_16x16x32_bf16 a[16:19],a[68:71],a[204:207],a[16:19]
v_accvgpr_read_b32 v8,a0
v_accvgpr_read_b32 v9,a1
v_mfma_f32_16x16x32_bf16 a[16:19],a[64:67],a[200:203],a[16:19]
v_accvgpr_read_b32 v10,a2
v_accvgpr_read_b32 v11,a3
v_mfma_f32_16x16x32_bf16 a[32:35],a[64:67],a[208:211],a[32:35]
v_mul_f32 v8,s71,v8
v_mul_f32 v9,s71,v9
v_mfma_f32_16x16x32_bf16 a[32:35],a[68:71],a[212:215],a[32:35]
v_mul_f32 v10,s71,v10
v_mul_f32 v11,s71,v11
v_mfma_f32_16x16x32_bf16 a[32:35],a[72:75],a[240:243],a[32:35]
v_cvt_pk_bf16_f32 v132,v8,v9
v_cvt_pk_bf16_f32 v133,v10,v11
v_mfma_f32_16x16x32_bf16 a[32:35],a[76:79],a[244:247],a[32:35]
v_accvgpr_read_b32 v16,a16
v_accvgpr_read_b32 v17,a17
v_mfma_f32_16x16x32_bf16 a[48:51],a[76:79],a[252:255],a[48:51]
ds_read_b64_tr_b16 a[76:77],v52 offset:17920
v_accvgpr_read_b32 v18,a18
v_mfma_f32_16x16x32_bf16 a[48:51],a[72:75],a[248:251],a[48:51]
ds_read_b64_tr_b16 a[72:73],v52 offset:1536
v_accvgpr_read_b32 v19,a19
v_mfma_f32_16x16x32_bf16 a[48:51],a[68:71],a[220:223],a[48:51]
ds_read_b64_tr_b16 a[68:69],v52 offset:16896
v_accvgpr_read_b32 v8,a32
v_mfma_f32_16x16x32_bf16 a[48:51],a[64:67],a[216:219],a[48:51]
global_load_lds_dwordx4 v5,s[10:11] offset:3072
ds_read_b64_tr_b16 a[64:65],v52 offset:512
v_mfma_f32_16x16x32_bf16 a[4:7],a[80:83],a[192:195],a[4:7]
v_accvgpr_read_b32 v9,a33
v_accvgpr_read_b32 v10,a34
v_mfma_f32_16x16x32_bf16 a[4:7],a[84:87],a[196:199],a[4:7]
ds_read_b64_tr_b16 a[66:67],v52 offset:2560
v_accvgpr_read_b32 v11,a35
v_mfma_f32_16x16x32_bf16 a[4:7],a[88:91],a[224:227],a[4:7]
ds_read_b64_tr_b16 a[70:71],v52 offset:18944
v_mul_f32 v8,s71,v8
v_mfma_f32_16x16x32_bf16 a[4:7],a[92:95],a[228:231],a[4:7]
ds_read_b64_tr_b16 a[74:75],v52 offset:3584
v_mul_f32 v9,s71,v9
v_mfma_f32_16x16x32_bf16 a[20:23],a[92:95],a[236:239],a[20:23]
v_mul_f32 v10,s71,v10
v_mul_f32 v11,s71,v11
v_mfma_f32_16x16x32_bf16 a[20:23],a[88:91],a[232:235],a[20:23]
ds_read_b64_tr_b16 a[78:79],v52 offset:19968
)A"
R"A(v_cvt_pk_bf16_f32 v196,v8,v9
v_mfma_f32_16x16x32_bf16 a[20:23],a[84:87],a[204:207],a[20:23]
v_accvgpr_read_b32 v12,a4
v_accvgpr_read_b32 v13,a5
v_mfma_f32_16x16x32_bf16 a[20:23],a[80:83],a[200:203],a[20:23]
v_accvgpr_read_b32 v14,a6
v_accvgpr_read_b32 v15,a7
v_mfma_f32_16x16x32_bf16 a[36:39],a[80:83],a[208:211],a[36:39]
v_mul_f32 v12,s71,v12
v_mul_f32 v13,s71,v13
v_mfma_f32_16x16x32_bf16 a[36:39],a[84:87],a[212:215],a[36:39]
v_mul_f32 v14,s71,v14
v_mul_f32 v15,s71,v15
v_mfma_f32_16x16x32_bf16 a[36:39],a[88:91],a[240:243],a[36:39]
s_add_u32 s10,s24,s74
s_addc_u32 s11,s25,0
s_add_u32 s74,s74,16384
v_mfma_f32_16x16x32_bf16 a[36:39],a[92:95],a[244:247],a[36:39]
s_and_b32 s74,s74,0x3ffff
s_or_b32 s74,s74,s69
v_cvt_pk_bf16_f32 v134,v12,v13
v_mfma_f32_16x16x32_bf16 a[52:55],a[92:95],a[252:255],a[52:55]
s_add_u32 m0,s76,16384
s_sub_u32 s76,s76,32768
ds_read_b64_tr_b16 a[92:93],v52 offset:17928
v_mfma_f32_16x16x32_bf16 a[52:55],a[88:91],a[248:251],a[52:55]
s_and_b32 s76,s76,0x1ffff
ds_read_b64_tr_b16 a[88:89],v52 offset:1544
v_cvt_pk_bf16_f32 v135,v14,v15
v_mfma_f32_16x16x32_bf16 a[52:55],a[84:87],a[220:223],a[52:55]
global_load_lds_dwordx4 v4,s[10:11]
ds_read_b64_tr_b16 a[84:85],v52 offset:16904
v_mfma_f32_16x16x32_bf16 a[52:55],a[80:83],a[216:219],a[52:55]
s_and_b32 s69,s75,0xfffc0000
ds_read_b64_tr_b16 a[80:81],v52 offset:520
v_accvgpr_read_b32 v12,a36
v_mfma_f32_16x16x32_bf16 a[8:11],a[96:99],a[192:195],a[8:11]
s_add_u32 s75,s75,16384
v_accvgpr_read_b32 v13,a37
v_accvgpr_read_b32 v14,a38
v_mfma_f32_16x16x32_bf16 a[8:11],a[100:103],a[196:199],a[8:11]
s_waitcnt lgkmcnt(4)
ds_read_b64_tr_b16 a[82:83],v52 offset:2568
v_accvgpr_read_b32 v15,a39
v_mfma_f32_16x16x32_bf16 a[8:11],a[104:107],a[224:227],a[8:11]
ds_read_b64_tr_b16 a[86:87],v52 offset:18952
v_mul_f32 v12,s71,v12
v_mfma_f32_16x16x32_bf16 a[8:11],a[108:111],a[228:231],a[8:11]
ds_read_b64_tr_b16 a[90:91],v52 offset:3592
v_mul_f32 v13,s71,v13
v_mfma_f32_16x16x32_bf16 a[24:27],a[108:111],a[236:239],a[24:27]
v_mul_f32 v14,s71,v14
v_mul_f32 v15,s71,v15
v_mfma_f32_16x16x32_bf16 a[24:27],a[104:107],a[232:235],a[24:27]
ds_read_b64_tr_b16 a[94:95],v52 offset:19976
v_cvt_pk_bf16_f32 v197,v10,v11
v_mfma_f32_16x16x32_bf16 a[24:27],a[100:103],a[204:207],a[24:27]
v_cvt_pk_bf16_f32 v198,v12,v13
v_cvt_pk_bf16_f32 v199,v14,v15
v_mfma_f32_16x16x32_bf16 a[24:27],a[96:99],a[200:203],a[24:27]
v_accvgpr_read_b32 v8,a8
v_accvgpr_read_b32 v9,a9
v_mfma_f32_16x16x32_bf16 a[40:43],a[96:99],a[208:211],a[40:43]
v_accvgpr_read_b32 v10,a10
v_accvgpr_read_b32 v11,a11
v_mfma_f32_16x16x32_bf16 a[40:43],a[100:103],a[212:215],a[40:43]
v_mul_f32 v8,s71,v8
v_mul_f32 v9,s71,v9
v_mfma_f32_16x16x32_bf16 a[40:43],a[104:107],a[240:243],a[40:43]
v_mul_f32 v10,s71,v10
v_mul_f32 v11,s71,v11
v_mfma_f32_16x16x32_bf16 a[40:43],a[108:111],a[244:247],a[40:43]
v_cvt_pk_bf16_f32 v140,v8,v9
v_cvt_pk_bf16_f32 v141,v10,v11
v_mfma_f32_16x16x32_bf16 a[56:59],a[108:111],a[252:255],a[56:59]
ds_read_b64_tr_b16 a[108:109],v52 offset:18176
v_accvgpr_read_b32 v20,a20
v_mfma_f32_16x16x32_bf16 a[56:59],a[104:107],a[248:251],a[56:59]
global_load_lds_dwordx4 v5,s[10:11] offset:1024
ds_read_b64_tr_b16 a[104:105],v52 offset:1792
v_mfma_f32_16x16x32_bf16 a[56:59],a[100:103],a[220:223],a[56:59]
ds_read_b64_tr_b16 a[100:101],v52 offset:17152
v_accvgpr_read_b32 v21,a21
v_mfma_f32_16x16x32_bf16 a[56:59],a[96:99],a[216:219],a[56:59]
ds_read_b64_tr_b16 a[96:97],v52 offset:768
v_accvgpr_read_b32 v22,a22
v_mfma_f32_16x16x32_bf16 a[12:15],a[112:115],a[192:195],a[12:15]
v_accvgpr_read_b32 v23,a23
v_mul_f32 v16,s71,v16
v_mfma_f32_16x16x32_bf16 a[12:15],a[116:119],a[196:199],a[12:15]
ds_read_b64_tr_b16 a[98:99],v52 offset:2816
v_mul_f32 v17,s71,v17
v_mfma_f32_16x16x32_bf16 a[12:15],a[120:123],a[224:227],a[12:15]
ds_read_b64_tr_b16 a[102:103],v52 offset:19200
v_mul_f32 v18,s71,v18
v_mfma_f32_16x16x32_bf16 a[12:15],a[124:127],a[228:231],a[12:15]
ds_read_b64_tr_b16 a[106:107],v52 offset:3840
v_mul_f32 v19,s71,v19
v_mfma_f32_16x16x32_bf16 a[28:31],a[124:127],a[236:239],a[28:31]
v_mul_f32 v20,s71,v20
v_mul_f32 v21,s71,v21
v_mfma_f32_16x16x32_bf16 a[28:31],a[120:123],a[232:235],a[28:31]
ds_read_b64_tr_b16 a[110:111],v52 offset:20224
v_mul_f32 v22,s71,v22
v_mfma_f32_16x16x32_bf16 a[28:31],a[116:119],a[204:207],a[28:31]
v_accvgpr_read_b32 v12,a12
v_accvgpr_read_b32 v13,a13
v_mfma_f32_16x16x32_bf16 a[28:31],a[112:115],a[200:203],a[28:31]
v_accvgpr_read_b32 v14,a14
v_accvgpr_read_b32 v15,a15
v_mfma_f32_16x16x32_bf16 a[44:47],a[112:115],a[208:211],a[44:47]
v_mul_f32 v12,s71,v12
v_mul_f32 v13,s71,v13
v_mfma_f32_16x16x32_bf16 a[44:47],a[116:119],a[212:215],a[44:47]
v_mul_f32 v14,s71,v14
v_mul_f32 v15,s71,v15
v_mfma_f32_16x16x32_bf16 a[44:47],a[120:123],a[240:243],a[44:47]
v_cvt_pk_bf16_f32 v142,v12,v13
v_cvt_pk_bf16_f32 v143,v14,v15
v_mfma_f32_16x16x32_bf16 a[44:47],a[124:127],a[244:247],a[44:47]
v_mul_f32 v23,s71,v23
v_cvt_pk_bf16_f32 v164,v16,v17
v_mfma_f32_16x16x32_bf16 a[60:63],a[124:127],a[252:255],a[60:63]
global_load_lds_dwordx4 v4,s[10:11] offset:2048
ds_read_b64_tr_b16 a[124:125],v52 offset:18184
v_mfma_f32_16x16x32_bf16 a[60:63],a[120:123],a[248:251],a[60:63]
ds_read_b64_tr_b16 a[120:121],v52 offset:1800
v_cvt_pk_bf16_f32 v165,v18,v19
v_mfma_f32_16x16x32_bf16 a[60:63],a[116:119],a[220:223],a[60:63]
ds_read_b64_tr_b16 a[116:117],v52 offset:17160
v_cvt_pk_bf16_f32 v166,v20,v21
v_mfma_f32_16x16x32_bf16 a[60:63],a[112:115],a[216:219],a[60:63]
ds_read_b64_tr_b16 a[112:113],v52 offset:776
v_cvt_pk_bf16_f32 v167,v22,v23
v_mfma_f32_16x16x32_bf16 v[64:67],a[64:67],a[192:195],v[64:67]
v_accvgpr_read_b32 v16,a48
v_accvgpr_read_b32 v17,a49
v_mfma_f32_16x16x32_bf16 v[64:67],a[68:71],a[196:199],v[64:67]
ds_read_b64_tr_b16 a[114:115],v52 offset:2824
v_accvgpr_read_b32 v18,a50
v_mfma_f32_16x16x32_bf16 v[64:67],a[72:75],a[224:227],v[64:67]
ds_read_b64_tr_b16 a[118:119],v52 offset:19208
v_accvgpr_read_b32 v19,a51
v_mfma_f32_16x16x32_bf16 v[64:67],a[76:79],a[228:231],v[64:67]
ds_read_b64_tr_b16 a[122:123],v52 offset:3848
v_accvgpr_read_b32 v20,a52
v_mfma_f32_16x16x32_bf16 v[80:83],a[76:79],a[236:239],v[80:83]
v_accvgpr_read_b32 v21,a53
v_accvgpr_read_b32 v22,a54
v_mfma_f32_16x16x32_bf16 v[80:83],a[72:75],a[232:235],v[80:83]
ds_read_b64_tr_b16 a[126:127],v52 offset:20232
v_accvgpr_read_b32 v23,a55
v_mfma_f32_16x16x32_bf16 v[80:83],a[68:71],a[204:207],v[80:83]
v_mul_f32 v64,s71,v64
v_mul_f32 v65,s71,v65
v_mfma_f32_16x16x32_bf16 v[80:83],a[64:67],a[200:203],v[80:83]
v_mul_f32 v66,s71,v66
v_mul_f32 v67,s71,v67
v_mfma_f32_16x16x32_bf16 v[96:99],a[64:67],a[208:211],v[96:99]
v_cvt_pk_bf16_f32 v148,v64,v65
v_cvt_pk_bf16_f32 v149,v66,v67
v_mfma_f32_16x16x32_bf16 v[96:99],a[68:71],a[212:215],v[96:99]
v_mul_f32 v16,s71,v16
v_mul_f32 v17,s71,v17
v_mfma_f32_16x16x32_bf16 v[96:99],a[72:75],a[240:243],v[96:99]
v_mul_f32 v18,s71,v18
v_mul_f32 v19,s71,v19
v_mfma_f32_16x16x32_bf16 v[96:99],a[76:79],a[244:247],v[96:99]
global_load_lds_dwordx4 v5,s[10:11] offset:3072
v_mul_f32 v20,s71,v20
v_mfma_f32_16x16x32_bf16 v[112:115],a[76:79],a[252:255],v[112:115]
v_mul_f32 v21,s71,v21
v_mul_f32 v22,s71,v22
v_mfma_f32_16x16x32_bf16 v[112:115],a[72:75],a[248:251],v[112:115]
v_mul_f32 v23,s71,v23
v_cvt_pk_bf16_f32 v228,v16,v17
v_mfma_f32_16x16x32_bf16 v[112:115],a[68:71],a[220:223],v[112:115]
v_cvt_pk_bf16_f32 v229,v18,v19
v_cvt_pk_bf16_f32 v230,v20,v21
v_mfma_f32_16x16x32_bf16 v[112:115],a[64:67],a[216:219],v[112:115]
v_cvt_pk_bf16_f32 v231,v22,v23
v_accvgpr_read_b32 v16,a24
s_waitcnt vmcnt(8) lgkmcnt(0)
s_barrier
v_mfma_f32_16x16x32_bf16 v[68:71],a[80:83],a[192:195],v[68:71]
ds_read_b128 a[128:131],v1
v_accvgpr_read_b32 v17,a25
v_mfma_f32_16x16x32_bf16 v[68:71],a[84:87],a[196:199],v[68:71]
ds_read_b128 a[132:135],v1 offset:256
v_accvgpr_read_b32 v18,a26
v_mfma_f32_16x16x32_bf16 v[68:71],a[88:91],a[224:227],v[68:71]
ds_read_b128 a[136:139],v1 offset:512
v_accvgpr_read_b32 v19,a27
v_mfma_f32_16x16x32_bf16 v[68:71],a[92:95],a[228:231],v[68:71]
ds_read_b128 a[140:143],v1 offset:768
v_accvgpr_read_b32 v20,a28
v_mfma_f32_16x16x32_bf16 v[84:87],a[92:95],a[236:239],v[84:87]
v_accvgpr_read_b32 v21,a29
v_accvgpr_read_b32 v22,a30
v_mfma_f32_16x16x32_bf16 v[84:87],a[88:91],a[232:235],v[84:87]
v_accvgpr_read_b32 v23,a31
v_mul_f32 v16,s71,v16
v_mfma_f32_16x16x32_bf16 v[84:87],a[84:87],a[204:207],v[84:87]
s_add_u32 s10,s20,s74
s_addc_u32 s11,s21,0
v_mul_f32 v68,s71,v68
v_mfma_f32_16x16x32_bf16 v[84:87],a[80:83],a[200:203],v[84:87]
v_mul_f32 v69,s71,v69
v_mul_f32 v70,s71,v70
v_mfma_f32_16x16x32_bf16 v[100:103],a[80:83],a[208:211],v[100:103]
s_add_u32 m0,s76,0
v_mul_f32 v71,s71,v71
v_cvt_pk_bf16_f32 v150,v68,v69
v_mfma_f32_16x16x32_bf16 v[100:103],a[84:87],a[212:215],v[100:103]
v_cvt_pk_bf16_f32 v151,v70,v71
v_mul_f32 v17,s71,v17
v_mfma_f32_16x16x32_bf16 v[100:103],a[88:91],a[240:243],v[100:103]
global_load_lds_dwordx4 v4,s[10:11] nt
v_mul_f32 v18,s71,v18
v_mfma_f32_16x16x32_bf16 v[100:103],a[92:95],a[244:247],v[100:103]
v_mul_f32 v19,s71,v19
v_mul_f32 v20,s71,v20
v_mfma_f32_16x16x32_bf16 v[116:119],a[92:95],a[252:255],v[116:119]
v_mul_f32 v21,s71,v21
v_mul_f32 v22,s71,v22
v_mfma_f32_16x16x32_bf16 v[116:119],a[88:91],a[248:251],v[116:119]
v_mul_f32 v23,s71,v23
v_cvt_pk_bf16_f32 v172,v16,v17
v_mfma_f32_16x16x32_bf16 v[116:119],a[84:87],a[220:223],v[116:119]
v_cvt_pk_bf16_f32 v173,v18,v19
v_cvt_pk_bf16_f32 v174,v20,v21
v_mfma_f32_16x16x32_bf16 v[116:119],a[80:83],a[216:219],v[116:119]
v_cvt_pk_bf16_f32 v175,v22,v23
v_mul_f32 v80,s71,v80
v_mfma_f32_16x16x32_bf16 v[72:75],a[96:99],a[192:195],v[72:75]
v_mul_f32 v81,s71,v81
v_mul_f32 v82,s71,v82
v_mfma_f32_16x16x32_bf16 v[72:75],a[100:103],a[196:199],v[72:75]
v_mul_f32 v83,s71,v83
v_mul_f32 v84,s71,v84
v_mfma_f32_16x16x32_bf16 v[72:75],a[104:107],a[224:227],v[72:75]
v_mul_f32 v85,s71,v85
v_mul_f32 v86,s71,v86
v_mfma_f32_16x16x32_bf16 v[72:75],a[108:111],a[228:231],v[72:75]
v_mul_f32 v87,s71,v87
v_cvt_pk_bf16_f32 v180,v80,v81
v_mfma_f32_16x16x32_bf16 v[88:91],a[108:111],a[236:239],v[88:91]
v_cvt_pk_bf16_f32 v181,v82,v83
v_cvt_pk_bf16_f32 v182,v84,v85
v_mfma_f32_16x16x32_bf16 v[88:91],a[104:107],a[232:235],v[88:91]
v_cvt_pk_bf16_f32 v183,v86,v87
v_accvgpr_read_b32 v8,a40
v_mfma_f32_16x16x32_bf16 v[88:91],a[100:103],a[204:207],v[88:91]
v_mul_f32 v72,s71,v72
v_mul_f32 v73,s71,v73
v_mfma_f32_16x16x32_bf16 v[88:91],a[96:99],a[200:203],v[88:91]
v_mul_f32 v74,s71,v74
v_mul_f32 v75,s71,v75
v_mfma_f32_16x16x32_bf16 v[104:107],a[96:99],a[208:211],v[104:107]
global_load_lds_dwordx4 v4,s[10:11] offset:1024 nt
v_cvt_pk_bf16_f32 v156,v72,v73
v_mfma_f32_16x16x32_bf16 v[104:107],a[100:103],a[212:215],v[104:107]
v_cvt_pk_bf16_f32 v157,v74,v75
v_accvgpr_read_b32 v9,a41
v_mfma_f32_16x16x32_bf16 v[104:107],a[104:107],a[240:243],v[104:107]
v_mul_f32 v88,s71,v88
v_mul_f32 v89,s71,v89
v_mfma_f32_16x16x32_bf16 v[104:107],a[108:111],a[244:247],v[104:107]
v_mul_f32 v90,s71,v90
v_mul_f32 v91,s71,v91
v_mfma_f32_16x16x32_bf16 v[120:123],a[108:111],a[252:255],v[120:123]
v_cvt_pk_bf16_f32 v188,v88,v89
v_cvt_pk_bf16_f32 v189,v90,v91
v_mfma_f32_16x16x32_bf16 v[120:123],a[104:107],a[248:251],v[120:123]
v_accvgpr_read_b32 v10,a42
v_accvgpr_read_b32 v11,a43
v_mfma_f32_16x16x32_bf16 v[120:123],a[100:103],a[220:223],v[120:123]
v_accvgpr_read_b32 v12,a44
v_accvgpr_read_b32 v13,a45
v_mfma_f32_16x16x32_bf16 v[120:123],a[96:99],a[216:219],v[120:123]
v_accvgpr_read_b32 v14,a46
v_accvgpr_read_b32 v15,a47
v_mfma_f32_16x16x32_bf16 v[76:79],a[112:115],a[192:195],v[76:79]
v_mul_f32 v8,s71,v8
v_mul_f32 v9,s71,v9
v_mfma_f32_16x16x32_bf16 v[76:79],a[116:119],a[196:199],v[76:79]
v_mul_f32 v10,s71,v10
v_mul_f32 v11,s71,v11
v_mfma_f32_16x16x32_bf16 v[76:79],a[120:123],a[224:227],v[76:79]
v_mul_f32 v12,s71,v12
v_mul_f32 v13,s71,v13
v_mfma_f32_16x16x32_bf16 v[76:79],a[124:127],a[228:231],v[76:79]
v_mul_f32 v14,s71,v14
v_mul_f32 v15,s71,v15
v_mfma_f32_16x16x32_bf16 v[92:95],a[124:127],a[236:239],v[92:95]
v_cvt_pk_bf16_f32 v204,v8,v9
v_cvt_pk_bf16_f32 v205,v10,v11
v_mfma_f32_16x16x32_bf16 v[92:95],a[120:123],a[232:235],v[92:95]
v_cvt_pk_bf16_f32 v206,v12,v13
v_cvt_pk_bf16_f32 v207,v14,v15
)A"
R"A(v_mfma_f32_16x16x32_bf16 v[92:95],a[116:119],a[204:207],v[92:95]
global_load_lds_dwordx4 v5,s[10:11] offset:2048 nt
v_mul_f32 v76,s71,v76
v_mfma_f32_16x16x32_bf16 v[92:95],a[112:115],a[200:203],v[92:95]
v_mul_f32 v77,s71,v77
v_mul_f32 v78,s71,v78
v_mfma_f32_16x16x32_bf16 v[108:111],a[112:115],a[208:211],v[108:111]
v_mul_f32 v79,s71,v79
v_cvt_pk_bf16_f32 v158,v76,v77
v_mfma_f32_16x16x32_bf16 v[108:111],a[116:119],a[212:215],v[108:111]
ds_read_b128 a[156:159],v1 offset:4864
v_cvt_pk_bf16_f32 v159,v78,v79
v_mfma_f32_16x16x32_bf16 v[108:111],a[120:123],a[240:243],v[108:111]
v_mul_f32 v92,s71,v92
v_mul_f32 v93,s71,v93
v_mfma_f32_16x16x32_bf16 v[108:111],a[124:127],a[244:247],v[108:111]
v_mul_f32 v94,s71,v94
v_mul_f32 v95,s71,v95
v_mfma_f32_16x16x32_bf16 v[124:127],a[124:127],a[252:255],v[124:127]
v_cvt_pk_bf16_f32 v190,v92,v93
v_cvt_pk_bf16_f32 v191,v94,v95
v_mfma_f32_16x16x32_bf16 v[124:127],a[120:123],a[248:251],v[124:127]
v_cvt_pk_bf16_f32 v209,v210,v211
v_cvt_pk_bf16_f32 v210,v212,v213
v_mfma_f32_16x16x32_bf16 v[124:127],a[116:119],a[220:223],v[124:127]
ds_read_b128 a[152:155],v1 offset:4608
v_cvt_pk_bf16_f32 v211,v214,v215
v_mfma_f32_16x16x32_bf16 v[124:127],a[112:115],a[216:219],v[124:127]
v_mul_f32 v96,s71,v96
v_mul_f32 v97,s71,v97
s_waitcnt lgkmcnt(2)
v_mfma_f32_16x16x32_bf16 v[64:67],a[128:131],v[132:135],0
v_mul_f32 v98,s71,v98
v_mul_f32 v99,s71,v99
v_mfma_f32_16x16x32_bf16 v[64:67],a[132:135],v[140:143],v[64:67]
v_mul_f32 v100,s71,v100
v_mul_f32 v101,s71,v101
v_mfma_f32_16x16x32_bf16 v[64:67],a[136:139],v[148:151],v[64:67]
v_mul_f32 v102,s71,v102
v_mul_f32 v103,s71,v103
v_mfma_f32_16x16x32_bf16 v[64:67],a[140:143],v[156:159],v[64:67]
global_load_lds_dwordx4 v5,s[10:11] offset:3072
v_cvt_pk_bf16_f32 v212,v96,v97
v_mfma_f32_16x16x32_bf16 v[80:83],a[128:131],v[164:167],0
v_cvt_pk_bf16_f32 v213,v98,v99
v_cvt_pk_bf16_f32 v214,v100,v101
v_mfma_f32_16x16x32_bf16 v[80:83],a[132:135],v[172:175],v[80:83]
v_cvt_pk_bf16_f32 v215,v102,v103
v_mul_f32 v216,s71,v216
v_mfma_f32_16x16x32_bf16 v[80:83],a[136:139],v[180:183],v[80:83]
v_max3_f32 v64,v64,v65,v66
v_max_f32 v64,v64,v67
v_mov_b32 v65,v64
v_mfma_f32_16x16x32_bf16 v[80:83],a[140:143],v[188:191],v[80:83]
v_mul_f32 v217,s71,v217
v_mul_f32 v218,s71,v218
v_mfma_f32_16x16x32_bf16 v[96:99],a[128:131],v[196:199],0
v_permlane32_swap_b32 v64,v65
v_max_f32 v64,v64,v65
v_mov_b32 v65,v64
v_mul_f32 v219,s71,v219
v_mul_f32 v220,s71,v220
v_mul_f32 v221,s71,v221
v_mul_f32 v222,s71,v222
v_mul_f32 v223,s71,v223
v_cvt_pk_bf16_f32 v216,v216,v217
v_cvt_pk_bf16_f32 v217,v218,v219
v_cvt_pk_bf16_f32 v218,v220,v221
v_cvt_pk_bf16_f32 v219,v222,v223
v_mul_f32 v104,s71,v104
v_mul_f32 v105,s71,v105
v_mul_f32 v106,s71,v106
v_mul_f32 v107,s71,v107
v_mul_f32 v108,s71,v108
v_mul_f32 v109,s71,v109
v_mul_f32 v110,s71,v110
v_mul_f32 v111,s71,v111
v_cvt_pk_bf16_f32 v220,v104,v105
v_cvt_pk_bf16_f32 v221,v106,v107
v_cvt_pk_bf16_f32 v222,v108,v109
v_cvt_pk_bf16_f32 v223,v110,v111
v_mfma_f32_16x16x32_bf16 v[96:99],a[132:135],v[204:207],v[96:99]
ds_read_b128 a[148:151],v1 offset:4352
v_max3_f32 v80,v80,v81,v82
v_mfma_f32_16x16x32_bf16 v[96:99],a[136:139],v[212:215],v[96:99]
ds_read_b128 a[144:147],v1 offset:4096
v_permlane16_swap_b32 v64,v65
v_max_f32 v64,v64,v65
v_sub_f32 v35,0,v64
v_sub_f32 v34,0,v64
v_sub_f32 v33,0,v64
v_sub_f32 v32,0,v64
v_mfma_f32_16x16x32_bf16 v[96:99],a[140:143],v[220:223],v[96:99]
ds_read_b128 a[160:163],v1 offset:8192
v_max_f32 v80,v80,v83
v_mfma_f32_16x16x32_bf16 v[64:67],a[128:131],v[132:135],v[32:35]
s_waitcnt lgkmcnt(3)
ds_read_b128 a[164:167],v1 offset:8448
v_mov_b32 v81,v80
v_mfma_f32_16x16x32_bf16 v[64:67],a[132:135],v[140:143],v[64:67]
s_add_u32 s10,s24,s74
s_addc_u32 s11,s25,0
v_mul_f32 v232,s71,v232
v_mfma_f32_16x16x32_bf16 v[64:67],a[136:139],v[148:151],v[64:67]
s_add_u32 s74,s74,16384
ds_read_b128 a[168:171],v1 offset:8704
v_max3_f32 v96,v96,v97,v98
v_mfma_f32_16x16x32_bf16 v[64:67],a[140:143],v[156:159],v[64:67]
s_add_u32 m0,s76,16384
ds_read_b128 a[172:175],v1 offset:8960
v_max_f32 v96,v96,v99
v_mfma_f32_16x16x32_bf16 v[68:71],a[156:159],v[156:159],v[32:35]
s_and_b32 s74,s74,0x3ffff
ds_read_b128 a[188:191],v1 offset:13056
v_mov_b32 v97,v96
s_nop 0
L_pd4_%=:
s_cbranch_execz L_pd8_%=
v_mfma_f32_16x16x32_bf16 v[68:71],a[152:155],v[148:151],v[68:71]
global_load_lds_dwordx4 v4,s[10:11]
v_mul_f32 v233,s71,v233
s_waitcnt lgkmcnt(6)
v_mfma_f32_16x16x32_bf16 v[68:71],a[148:151],v[140:143],v[68:71]
s_or_b32 s74,s74,s69
ds_read_b128 a[184:187],v1 offset:12800
v_mul_f32 v234,s71,v234
s_waitcnt lgkmcnt(6)
v_mfma_f32_16x16x32_bf16 v[68:71],a[144:147],v[132:135],v[68:71]
s_sub_u32 s76,s76,32768
ds_read_b128 a[180:183],v1 offset:12544
v_mul_f32 v235,s71,v235
s_waitcnt lgkmcnt(6)
v_mfma_f32_16x16x32_bf16 v[72:75],a[160:163],v[132:135],v[32:35]
s_and_b32 s76,s76,0x1ffff
ds_read_b128 a[176:179],v1 offset:12288
v_mul_f32 v236,s71,v236
s_waitcnt lgkmcnt(6)
v_mfma_f32_16x16x32_bf16 v[72:75],a[164:167],v[140:143],v[72:75]
s_and_b32 s69,s75,0xfffc0000
v_permlane32_swap_b32 v80,v81
s_waitcnt lgkmcnt(5)
v_mfma_f32_16x16x32_bf16 v[72:75],a[168:171],v[148:151],v[72:75]
s_add_u32 s75,s75,16384
v_max_f32 v80,v80,v81
v_mov_b32 v81,v80
s_waitcnt lgkmcnt(4)
v_mfma_f32_16x16x32_bf16 v[72:75],a[172:175],v[156:159],v[72:75]
s_waitcnt lgkmcnt(3)
v_mul_f32 v237,s71,v237
v_mul_f32 v238,s71,v238
v_mfma_f32_16x16x32_bf16 v[76:79],a[188:191],v[156:159],v[32:35]
s_waitcnt lgkmcnt(2)
v_permlane16_swap_b32 v80,v81
v_mfma_f32_16x16x32_bf16 v[76:79],a[184:187],v[148:151],v[76:79]
s_waitcnt lgkmcnt(1)
v_max_f32 v80,v80,v81
v_sub_f32 v39,0,v80
v_sub_f32 v38,0,v80
v_sub_f32 v37,0,v80
v_sub_f32 v36,0,v80
v_mfma_f32_16x16x32_bf16 v[76:79],a[180:183],v[140:143],v[76:79]
s_waitcnt lgkmcnt(0)
v_mul_f32 v239,s71,v239
v_cvt_pk_bf16_f32 v232,v232,v233
v_mfma_f32_16x16x32_bf16 v[76:79],a[176:179],v[132:135],v[76:79]
v_cvt_pk_bf16_f32 v233,v234,v235
v_cvt_pk_bf16_f32 v234,v236,v237
v_mfma_f32_16x16x32_bf16 v[80:83],a[128:131],v[164:167],v[36:39]
v_cvt_pk_bf16_f32 v235,v238,v239
v_accvgpr_read_b32 v16,a56
v_mfma_f32_16x16x32_bf16 v[80:83],a[132:135],v[172:175],v[80:83]
v_accvgpr_read_b32 v17,a57
v_accvgpr_read_b32 v18,a58
v_mfma_f32_16x16x32_bf16 v[80:83],a[136:139],v[180:183],v[80:83]
v_accvgpr_read_b32 v19,a59
v_accvgpr_read_b32 v20,a60
v_mfma_f32_16x16x32_bf16 v[80:83],a[140:143],v[188:191],v[80:83]
global_load_lds_dwordx4 v5,s[10:11] offset:1024
v_accvgpr_read_b32 v21,a61
v_mfma_f32_16x16x32_bf16 v[84:87],a[156:159],v[188:191],v[36:39]
v_accvgpr_read_b32 v22,a62
v_accvgpr_read_b32 v23,a63
v_mfma_f32_16x16x32_bf16 v[84:87],a[152:155],v[180:183],v[84:87]
v_mul_f32 v16,s71,v16
v_mul_f32 v17,s71,v17
v_mfma_f32_16x16x32_bf16 v[84:87],a[148:151],v[172:175],v[84:87]
v_mul_f32 v18,s71,v18
v_mul_f32 v19,s71,v19
v_mfma_f32_16x16x32_bf16 v[84:87],a[144:147],v[164:167],v[84:87]
v_mul_f32 v20,s71,v20
v_mul_f32 v21,s71,v21
v_mfma_f32_16x16x32_bf16 v[88:91],a[160:163],v[164:167],v[36:39]
v_mul_f32 v22,s71,v22
v_mul_f32 v23,s71,v23
v_mfma_f32_16x16x32_bf16 v[88:91],a[164:167],v[172:175],v[88:91]
v_cvt_pk_bf16_f32 v236,v16,v17
v_cvt_pk_bf16_f32 v237,v18,v19
v_mfma_f32_16x16x32_bf16 v[88:91],a[168:171],v[180:183],v[88:91]
v_cvt_pk_bf16_f32 v238,v20,v21
v_cvt_pk_bf16_f32 v239,v22,v23
v_mfma_f32_16x16x32_bf16 v[88:91],a[172:175],v[188:191],v[88:91]
v_mul_f32 v240,s71,v240
v_mul_f32 v241,s71,v241
v_mfma_f32_16x16x32_bf16 v[92:95],a[188:191],v[188:191],v[36:39]
v_mul_f32 v242,s71,v242
v_mul_f32 v243,s71,v243
v_mfma_f32_16x16x32_bf16 v[92:95],a[184:187],v[180:183],v[92:95]
v_mul_f32 v244,s71,v244
v_mul_f32 v245,s71,v245
v_mfma_f32_16x16x32_bf16 v[92:95],a[180:183],v[172:175],v[92:95]
v_mul_f32 v246,s71,v246
v_mul_f32 v247,s71,v247
v_mfma_f32_16x16x32_bf16 v[92:95],a[176:179],v[164:167],v[92:95]
v_cvt_pk_bf16_f32 v240,v240,v241
v_cvt_pk_bf16_f32 v241,v242,v243
v_cvt_pk_bf16_f32 v242,v244,v245
v_cvt_pk_bf16_f32 v243,v246,v247
v_mul_f32 v112,s71,v112
v_mul_f32 v113,s71,v113
v_mul_f32 v114,s71,v114
v_mul_f32 v115,s71,v115
v_mul_f32 v116,s71,v116
v_mul_f32 v117,s71,v117
v_mul_f32 v118,s71,v118
v_mul_f32 v119,s71,v119
v_cvt_pk_bf16_f32 v244,v112,v113
v_cvt_pk_bf16_f32 v245,v114,v115
v_cvt_pk_bf16_f32 v246,v116,v117
v_cvt_pk_bf16_f32 v247,v118,v119
v_permlane32_swap_b32 v96,v97
v_max_f32 v96,v96,v97
v_mov_b32 v97,v96
v_mfma_f32_16x16x32_bf16 v[112:115],a[128:131],v[228:231],0
v_mul_f32 v248,s71,v248
v_mul_f32 v249,s71,v249
v_mul_f32 v250,s71,v250
v_mul_f32 v251,s71,v251
v_mul_f32 v252,s71,v252
v_mul_f32 v253,s71,v253
v_mul_f32 v254,s71,v254
v_mul_f32 v255,s71,v255
v_cvt_pk_bf16_f32 v248,v248,v249
v_cvt_pk_bf16_f32 v249,v250,v251
v_cvt_pk_bf16_f32 v250,v252,v253
v_cvt_pk_bf16_f32 v251,v254,v255
v_mul_f32 v120,s71,v120
v_mul_f32 v121,s71,v121
v_mul_f32 v122,s71,v122
v_mul_f32 v123,s71,v123
v_mul_f32 v124,s71,v124
v_mul_f32 v125,s71,v125
v_mul_f32 v126,s71,v126
v_mul_f32 v127,s71,v127
v_cvt_pk_bf16_f32 v252,v120,v121
v_cvt_pk_bf16_f32 v253,v122,v123
v_cvt_pk_bf16_f32 v254,v124,v125
v_cvt_pk_bf16_f32 v255,v126,v127
v_mfma_f32_16x16x32_bf16 v[112:115],a[132:135],v[236:239],v[112:115]
global_load_lds_dwordx4 v4,s[10:11] offset:2048
v_permlane16_swap_b32 v96,v97
v_max_f32 v96,v96,v97
v_sub_f32 v43,0,v96
v_sub_f32 v42,0,v96
v_sub_f32 v41,0,v96
v_sub_f32 v40,0,v96
v_mfma_f32_16x16x32_bf16 v[112:115],a[136:139],v[244:247],v[112:115]
ds_read_b64_tr_b16 a[192:193],v2
ds_read_b64_tr_b16 a[194:195],v2 offset:4096
v_mfma_f32_16x16x32_bf16 v[112:115],a[140:143],v[252:255],v[112:115]
ds_read_b64_tr_b16 a[196:197],v2 offset:8192
ds_read_b64_tr_b16 a[198:199],v2 offset:12288
v_mfma_f32_16x16x32_bf16 v[96:99],a[128:131],v[196:199],v[40:43]
ds_read_b64_tr_b16 a[200:201],v3
ds_read_b64_tr_b16 a[202:203],v3 offset:4096
v_mfma_f32_16x16x32_bf16 v[96:99],a[132:135],v[204:207],v[96:99]
ds_read_b64_tr_b16 a[204:205],v3 offset:8192
ds_read_b64_tr_b16 a[206:207],v3 offset:12288
v_mfma_f32_16x16x32_bf16 v[96:99],a[136:139],v[212:215],v[96:99]
ds_read_b64_tr_b16 a[208:209],v2 offset:256
v_max3_f32 v112,v112,v113,v114
v_mfma_f32_16x16x32_bf16 v[96:99],a[140:143],v[220:223],v[96:99]
ds_read_b64_tr_b16 a[210:211],v2 offset:4352
ds_read_b64_tr_b16 a[212:213],v2 offset:8448
v_mfma_f32_16x16x32_bf16 v[100:103],a[156:159],v[220:223],v[40:43]
ds_read_b64_tr_b16 a[214:215],v2 offset:12544
v_max_f32 v112,v112,v115
v_mfma_f32_16x16x32_bf16 v[100:103],a[152:155],v[212:215],v[100:103]
ds_read_b64_tr_b16 a[216:217],v3 offset:256
ds_read_b64_tr_b16 a[218:219],v3 offset:4352
v_mfma_f32_16x16x32_bf16 v[100:103],a[148:151],v[204:207],v[100:103]
v_exp_f32 v64,v64
v_mfma_f32_16x16x32_bf16 v[100:103],a[144:147],v[196:199],v[100:103]
v_exp_f32 v65,v65
v_mfma_f32_16x16x32_bf16 v[104:107],a[160:163],v[196:199],v[40:43]
v_exp_f32 v66,v66
v_mfma_f32_16x16x32_bf16 v[104:107],a[164:167],v[204:207],v[104:107]
v_exp_f32 v67,v67
v_mfma_f32_16x16x32_bf16 v[104:107],a[168:171],v[212:215],v[104:107]
v_exp_f32 v68,v68
v_mfma_f32_16x16x32_bf16 v[104:107],a[172:175],v[220:223],v[104:107]
global_load_lds_dwordx4 v5,s[10:11] offset:3072
ds_read_b64_tr_b16 a[220:221],v3 offset:8448
v_mfma_f32_16x16x32_bf16 v[108:111],a[188:191],v[220:223],v[40:43]
v_exp_f32 v69,v69
v_exp_f32 v70,v70
v_exp_f32 v71,v71
v_mfma_f32_16x16x32_bf16 v[108:111],a[184:187],v[212:215],v[108:111]
ds_read_b64_tr_b16 a[222:223],v3 offset:12544
v_cvt_pk_bf16_f32 v64,v64,v65
v_cvt_pk_bf16_f32 v65,v66,v67
v_cvt_pk_bf16_f32 v66,v68,v69
v_cvt_pk_bf16_f32 v67,v70,v71
v_exp_f32 v72,v72
v_exp_f32 v73,v73
v_exp_f32 v74,v74
v_exp_f32 v75,v75
v_exp_f32 v76,v76
v_exp_f32 v77,v77
v_exp_f32 v78,v78
v_exp_f32 v79,v79
v_mfma_f32_16x16x32_bf16 v[108:111],a[180:183],v[204:207],v[108:111]
ds_read_b64_tr_b16 a[224:225],v2 offset:512
ds_read_b64_tr_b16 a[226:227],v2 offset:4608
v_cvt_pk_bf16_f32 v72,v72,v73
v_cvt_pk_bf16_f32 v73,v74,v75
v_cvt_pk_bf16_f32 v74,v76,v77
v_cvt_pk_bf16_f32 v75,v78,v79
v_mfma_f32_16x16x32_bf16 v[108:111],a[176:179],v[196:199],v[108:111]
s_waitcnt lgkmcnt(4)
ds_read_b64_tr_b16 a[228:229],v2 offset:8704
ds_read_b64_tr_b16 a[230:231],v2 offset:12800
v_mfma_f32_16x16x32_bf16 a[0:3],a[192:195],v[64:67],0
ds_read_b64_tr_b16 a[232:233],v3 offset:512
)A"
R"A(ds_read_b64_tr_b16 a[234:235],v3 offset:4608
v_mfma_f32_16x16x32_bf16 a[0:3],a[196:199],v[72:75],a[0:3]
ds_read_b64_tr_b16 a[236:237],v3 offset:8704
ds_read_b64_tr_b16 a[238:239],v3 offset:12800
v_mfma_f32_16x16x32_bf16 a[4:7],a[200:203],v[64:67],0
ds_read_b64_tr_b16 a[240:241],v2 offset:768
v_mov_b32 v113,v112
v_mfma_f32_16x16x32_bf16 a[4:7],a[204:207],v[72:75],a[4:7]
ds_read_b64_tr_b16 a[242:243],v2 offset:4864
ds_read_b64_tr_b16 a[244:245],v2 offset:8960
v_mfma_f32_16x16x32_bf16 a[8:11],a[208:211],v[64:67],0
ds_read_b64_tr_b16 a[246:247],v2 offset:13056
v_add_u32 v1,s78,v1
v_mfma_f32_16x16x32_bf16 a[8:11],a[212:215],v[72:75],a[8:11]
ds_read_b64_tr_b16 a[248:249],v3 offset:768
ds_read_b64_tr_b16 a[250:251],v3 offset:4864
v_mfma_f32_16x16x32_bf16 a[12:15],a[216:219],v[64:67],0
s_waitcnt lgkmcnt(12)
ds_read_b64_tr_b16 a[252:253],v3 offset:8960
v_add_u32 v2,s78,v2
v_mfma_f32_16x16x32_bf16 a[12:15],a[220:223],v[72:75],a[12:15]
s_waitcnt lgkmcnt(6)
ds_read_b64_tr_b16 a[254:255],v3 offset:13056
v_add_u32 v3,s78,v3
v_mfma_f32_16x16x32_bf16 a[16:19],a[224:227],v[64:67],0
s_sub_u32 s78,s77,32768
v_permlane32_swap_b32 v112,v113
v_mfma_f32_16x16x32_bf16 a[16:19],a[228:231],v[72:75],a[16:19]
s_and_b32 s78,s78,0x1ffff
v_max_f32 v112,v112,v113
v_mov_b32 v113,v112
v_mfma_f32_16x16x32_bf16 a[20:23],a[232:235],v[64:67],0
s_sub_u32 s78,s78,s77
v_exp_f32 v80,v80
v_mfma_f32_16x16x32_bf16 a[20:23],a[236:239],v[72:75],a[20:23]
s_add_u32 s77,s77,s78
v_permlane16_swap_b32 v112,v113
s_waitcnt lgkmcnt(5)
v_mfma_f32_16x16x32_bf16 a[24:27],a[240:243],v[64:67],0
s_waitcnt lgkmcnt(4)
v_max_f32 v112,v112,v113
v_max_f32 v112,s72,v112
v_mfma_f32_16x16x32_bf16 a[24:27],a[244:247],v[72:75],a[24:27]
s_waitcnt lgkmcnt(2)
v_sub_f32 v47,0,v112
v_sub_f32 v46,0,v112
v_mfma_f32_16x16x32_bf16 a[28:31],a[248:251],v[64:67],0
s_waitcnt lgkmcnt(0)
v_sub_f32 v45,0,v112
v_sub_f32 v44,0,v112
v_mfma_f32_16x16x32_bf16 a[28:31],a[252:255],v[72:75],a[28:31]
v_exp_f32 v81,v81
v_mfma_f32_16x16x32_bf16 v[48:51],v[28:31],v[64:67],0
v_exp_f32 v82,v82
v_mfma_f32_16x16x32_bf16 v[48:51],v[28:31],v[72:75],v[48:51]
v_exp_f32 v83,v83
s_waitcnt vmcnt(8)
s_barrier
v_mfma_f32_16x16x32_bf16 v[112:115],a[128:131],v[228:231],v[44:47]
s_add_u32 s10,s20,s74
s_addc_u32 s11,s21,0
s_add_u32 m0,s76,0
v_mfma_f32_16x16x32_bf16 v[112:115],a[132:135],v[236:239],v[112:115]
v_exp_f32 v84,v84
v_mfma_f32_16x16x32_bf16 v[112:115],a[136:139],v[244:247],v[112:115]
global_load_lds_dwordx4 v4,s[10:11] nt
v_cvt_pk_bf16_f32 v80,v80,v81
v_mfma_f32_16x16x32_bf16 v[112:115],a[140:143],v[252:255],v[112:115]
v_exp_f32 v85,v85
v_mfma_f32_16x16x32_bf16 v[116:119],a[156:159],v[252:255],v[44:47]
v_exp_f32 v86,v86
v_mfma_f32_16x16x32_bf16 v[116:119],a[152:155],v[244:247],v[116:119]
v_exp_f32 v87,v87
v_mfma_f32_16x16x32_bf16 v[116:119],a[148:151],v[236:239],v[116:119]
v_cvt_pk_bf16_f32 v81,v82,v83
v_cvt_pk_bf16_f32 v82,v84,v85
v_mfma_f32_16x16x32_bf16 v[116:119],a[144:147],v[228:231],v[116:119]
ds_read_b128 a[128:131],v1
v_cvt_pk_bf16_f32 v83,v86,v87
v_mfma_f32_16x16x32_bf16 v[120:123],a[160:163],v[228:231],v[44:47]
v_exp_f32 v88,v88
v_mfma_f32_16x16x32_bf16 v[120:123],a[164:167],v[236:239],v[120:123]
v_exp_f32 v89,v89
v_mfma_f32_16x16x32_bf16 v[120:123],a[168:171],v[244:247],v[120:123]
v_exp_f32 v90,v90
v_mfma_f32_16x16x32_bf16 v[120:123],a[172:175],v[252:255],v[120:123]
v_exp_f32 v91,v91
v_mfma_f32_16x16x32_bf16 v[124:127],a[188:191],v[252:255],v[44:47]
v_exp_f32 v92,v92
v_mfma_f32_16x16x32_bf16 v[124:127],a[184:187],v[244:247],v[124:127]
v_exp_f32 v93,v93
v_exp_f32 v94,v94
v_exp_f32 v95,v95
v_mfma_f32_16x16x32_bf16 v[124:127],a[180:183],v[236:239],v[124:127]
v_cvt_pk_bf16_f32 v88,v88,v89
v_cvt_pk_bf16_f32 v89,v90,v91
v_cvt_pk_bf16_f32 v90,v92,v93
v_cvt_pk_bf16_f32 v91,v94,v95
v_mfma_f32_16x16x32_bf16 v[124:127],a[176:179],v[228:231],v[124:127]
v_exp_f32 v96,v96
v_mfma_f32_16x16x32_bf16 a[32:35],a[192:195],v[80:83],0
global_load_lds_dwordx4 v4,s[10:11] offset:1024 nt
ds_read_b128 a[132:135],v1 offset:256
v_mfma_f32_16x16x32_bf16 a[32:35],a[196:199],v[88:91],a[32:35]
v_exp_f32 v97,v97
v_mfma_f32_16x16x32_bf16 a[36:39],a[200:203],v[80:83],0
ds_read_b128 a[136:139],v1 offset:512
v_cvt_pk_bf16_f32 v96,v96,v97
v_mfma_f32_16x16x32_bf16 a[36:39],a[204:207],v[88:91],a[36:39]
v_exp_f32 v98,v98
v_mfma_f32_16x16x32_bf16 a[40:43],a[208:211],v[80:83],0
v_exp_f32 v99,v99
v_mfma_f32_16x16x32_bf16 a[40:43],a[212:215],v[88:91],a[40:43]
ds_read_b128 a[140:143],v1 offset:768
v_cvt_pk_bf16_f32 v97,v98,v99
v_mfma_f32_16x16x32_bf16 a[44:47],a[216:219],v[80:83],0
v_exp_f32 v100,v100
v_mfma_f32_16x16x32_bf16 a[44:47],a[220:223],v[88:91],a[44:47]
v_exp_f32 v101,v101
v_mfma_f32_16x16x32_bf16 a[48:51],a[224:227],v[80:83],0
ds_read_b128 a[156:159],v1 offset:4864
v_cvt_pk_bf16_f32 v98,v100,v101
v_mfma_f32_16x16x32_bf16 a[48:51],a[228:231],v[88:91],a[48:51]
v_exp_f32 v102,v102
v_mfma_f32_16x16x32_bf16 a[52:55],a[232:235],v[80:83],0
v_exp_f32 v103,v103
v_mfma_f32_16x16x32_bf16 a[52:55],a[236:239],v[88:91],a[52:55]
ds_read_b128 a[152:155],v1 offset:4608
v_cvt_pk_bf16_f32 v99,v102,v103
v_mfma_f32_16x16x32_bf16 a[56:59],a[240:243],v[80:83],0
ds_read_b128 a[148:151],v1 offset:4352
v_mfma_f32_16x16x32_bf16 a[56:59],a[244:247],v[88:91],a[56:59]
ds_read_b128 a[144:147],v1 offset:4096
v_mfma_f32_16x16x32_bf16 a[60:63],a[248:251],v[80:83],0
global_load_lds_dwordx4 v5,s[10:11] offset:2048 nt
ds_read_b128 a[160:163],v1 offset:8192
v_mfma_f32_16x16x32_bf16 a[60:63],a[252:255],v[88:91],a[60:63]
v_exp_f32 v104,v104
v_mfma_f32_16x16x32_bf16 v[52:55],v[28:31],v[80:83],0
ds_read_b128 a[164:167],v1 offset:8448
v_mfma_f32_16x16x32_bf16 v[52:55],v[28:31],v[88:91],v[52:55]
s_waitcnt lgkmcnt(5)
ds_read_b128 a[168:171],v1 offset:8704
v_mfma_f32_16x16x32_bf16 v[64:67],a[128:131],v[132:135],v[32:35]
ds_read_b128 a[172:175],v1 offset:8960
v_mfma_f32_16x16x32_bf16 v[64:67],a[132:135],v[140:143],v[64:67]
v_exp_f32 v105,v105
v_mfma_f32_16x16x32_bf16 v[64:67],a[136:139],v[148:151],v[64:67]
ds_read_b128 a[188:191],v1 offset:13056
v_cvt_pk_bf16_f32 v104,v104,v105
v_mfma_f32_16x16x32_bf16 v[64:67],a[140:143],v[156:159],v[64:67]
ds_read_b128 a[184:187],v1 offset:12800
v_mfma_f32_16x16x32_bf16 v[68:71],a[156:159],v[156:159],v[32:35]
s_waitcnt lgkmcnt(5)
ds_read_b128 a[180:183],v1 offset:12544
v_mfma_f32_16x16x32_bf16 v[68:71],a[152:155],v[148:151],v[68:71]
v_exp_f32 v106,v106
v_mfma_f32_16x16x32_bf16 v[68:71],a[148:151],v[140:143],v[68:71]
ds_read_b128 a[176:179],v1 offset:12288
v_add_u32 v1,s78,v1
v_mfma_f32_16x16x32_bf16 v[68:71],a[144:147],v[132:135],v[68:71]
v_exp_f32 v107,v107
v_mfma_f32_16x16x32_bf16 v[72:75],a[160:163],v[132:135],v[32:35]
s_waitcnt lgkmcnt(4)
v_exp_f32 v108,v108
v_mfma_f32_16x16x32_bf16 v[72:75],a[164:167],v[140:143],v[72:75]
v_exp_f32 v109,v109
v_mfma_f32_16x16x32_bf16 v[72:75],a[168:171],v[148:151],v[72:75]
global_load_lds_dwordx4 v5,s[10:11] offset:3072
v_cvt_pk_bf16_f32 v105,v106,v107
v_mfma_f32_16x16x32_bf16 v[72:75],a[172:175],v[156:159],v[72:75]
s_waitcnt lgkmcnt(2)
v_exp_f32 v110,v110
v_mfma_f32_16x16x32_bf16 v[76:79],a[188:191],v[156:159],v[32:35]
v_exp_f32 v111,v111
v_mfma_f32_16x16x32_bf16 v[76:79],a[184:187],v[148:151],v[76:79]
s_waitcnt lgkmcnt(1)
v_cvt_pk_bf16_f32 v106,v108,v109
v_cvt_pk_bf16_f32 v107,v110,v111
v_mfma_f32_16x16x32_bf16 v[76:79],a[180:183],v[140:143],v[76:79]
s_waitcnt lgkmcnt(0)
v_exp_f32 v112,v112
v_mfma_f32_16x16x32_bf16 v[76:79],a[176:179],v[132:135],v[76:79]
v_exp_f32 v113,v113
v_mfma_f32_16x16x32_bf16 a[64:67],a[192:195],v[96:99],0
v_exp_f32 v114,v114
v_mfma_f32_16x16x32_bf16 a[64:67],a[196:199],v[104:107],a[64:67]
v_exp_f32 v115,v115
v_mfma_f32_16x16x32_bf16 a[68:71],a[200:203],v[96:99],0
v_exp_f32 v116,v116
v_mfma_f32_16x16x32_bf16 a[68:71],a[204:207],v[104:107],a[68:71]
v_exp_f32 v117,v117
v_mfma_f32_16x16x32_bf16 a[72:75],a[208:211],v[96:99],0
s_add_u32 s10,s24,s74
s_addc_u32 s11,s25,0
s_add_u32 s74,s74,16384
s_and_b32 s74,s74,0x3ffff
s_or_b32 s74,s74,s69
s_lshl_b32 s69,s60,13
s_add_u32 s69,s69,131072
v_add_u32 v8,s69,v6
v_mfma_f32_16x16x32_bf16 a[72:75],a[212:215],v[104:107],a[72:75]
s_lshl_b32 s69,s60,13
ds_write_b128 v8,v[24:27] offset:4096
v_mfma_f32_16x16x32_bf16 a[76:79],a[216:219],v[96:99],0
s_add_u32 m0,s76,16384
ds_write_b32 v8,v7 offset:5120
v_cvt_pk_bf16_f32 v112,v112,v113
v_mfma_f32_16x16x32_bf16 a[76:79],a[220:223],v[104:107],a[76:79]
s_sub_u32 s76,s76,32768
v_bfe_u32 v24,v6,4,4
v_cvt_pk_bf16_f32 v113,v114,v115
v_mfma_f32_16x16x32_bf16 a[80:83],a[224:227],v[96:99],0
global_load_lds_dwordx4 v4,s[10:11]
v_lshrrev_b32 v7,8,v6
v_lshlrev_b32 v7,4,v7
v_lshl_add_u32 v7,v24,8,v7
v_mfma_f32_16x16x32_bf16 a[80:83],a[228:231],v[104:107],a[80:83]
s_and_b32 s76,s76,0x1ffff
v_exp_f32 v118,v118
v_mfma_f32_16x16x32_bf16 a[84:87],a[232:235],v[96:99],0
s_add_u32 s69,s69,131072
v_exp_f32 v119,v119
v_mfma_f32_16x16x32_bf16 a[84:87],a[236:239],v[104:107],a[84:87]
v_cvt_pk_bf16_f32 v114,v116,v117
v_cvt_pk_bf16_f32 v115,v118,v119
v_mfma_f32_16x16x32_bf16 a[88:91],a[240:243],v[96:99],0
v_exp_f32 v120,v120
v_mfma_f32_16x16x32_bf16 a[88:91],a[244:247],v[104:107],a[88:91]
buffer_load_dwordx4 v[8:11],v7,s[20:23],s65 offen nt
v_mfma_f32_16x16x32_bf16 a[92:95],a[248:251],v[96:99],0
v_exp_f32 v121,v121
v_mfma_f32_16x16x32_bf16 a[92:95],a[252:255],v[104:107],a[92:95]
v_exp_f32 v122,v122
v_mfma_f32_16x16x32_bf16 v[56:59],v[28:31],v[96:99],0
v_exp_f32 v123,v123
v_mfma_f32_16x16x32_bf16 v[56:59],v[28:31],v[104:107],v[56:59]
v_exp_f32 v124,v124
v_mfma_f32_16x16x32_bf16 v[80:83],a[128:131],v[164:167],v[36:39]
buffer_load_dwordx4 v[12:15],v7,s[20:23],s65 offen offset:64 nt
v_mfma_f32_16x16x32_bf16 v[80:83],a[132:135],v[172:175],v[80:83]
v_exp_f32 v125,v125
v_mfma_f32_16x16x32_bf16 v[80:83],a[136:139],v[180:183],v[80:83]
v_exp_f32 v126,v126
v_mfma_f32_16x16x32_bf16 v[80:83],a[140:143],v[188:191],v[80:83]
v_exp_f32 v127,v127
v_mfma_f32_16x16x32_bf16 v[84:87],a[156:159],v[188:191],v[36:39]
v_cvt_pk_bf16_f32 v120,v120,v121
v_cvt_pk_bf16_f32 v121,v122,v123
v_mfma_f32_16x16x32_bf16 v[84:87],a[152:155],v[180:183],v[84:87]
buffer_load_dwordx4 v[16:19],v7,s[20:23],s65 offen offset:128 nt
v_mfma_f32_16x16x32_bf16 v[84:87],a[148:151],v[172:175],v[84:87]
v_cvt_pk_bf16_f32 v122,v124,v125
v_cvt_pk_bf16_f32 v123,v126,v127
v_mfma_f32_16x16x32_bf16 v[84:87],a[144:147],v[164:167],v[84:87]
v_exp_f32 v64,v64
v_mfma_f32_16x16x32_bf16 v[88:91],a[160:163],v[164:167],v[36:39]
v_exp_f32 v65,v65
v_mfma_f32_16x16x32_bf16 v[88:91],a[164:167],v[172:175],v[88:91]
v_exp_f32 v66,v66
v_mfma_f32_16x16x32_bf16 v[88:91],a[168:171],v[180:183],v[88:91]
v_exp_f32 v67,v67
v_mfma_f32_16x16x32_bf16 v[88:91],a[172:175],v[188:191],v[88:91]
global_load_lds_dwordx4 v5,s[10:11] offset:1024
v_cvt_pk_bf16_f32 v64,v64,v65
v_mfma_f32_16x16x32_bf16 v[92:95],a[188:191],v[188:191],v[36:39]
v_exp_f32 v68,v68
v_mfma_f32_16x16x32_bf16 v[92:95],a[184:187],v[180:183],v[92:95]
v_exp_f32 v69,v69
v_mfma_f32_16x16x32_bf16 v[92:95],a[180:183],v[172:175],v[92:95]
v_exp_f32 v70,v70
v_mfma_f32_16x16x32_bf16 v[92:95],a[176:179],v[164:167],v[92:95]
v_exp_f32 v71,v71
v_mfma_f32_16x16x32_bf16 a[96:99],a[192:195],v[112:115],0
buffer_load_dwordx4 v[20:23],v7,s[20:23],s65 offen offset:192 nt
v_mfma_f32_16x16x32_bf16 a[96:99],a[196:199],v[120:123],a[96:99]
ds_read_b64_tr_b16 a[192:193],v2
ds_read_b64_tr_b16 a[194:195],v2 offset:4096
v_mfma_f32_16x16x32_bf16 a[100:103],a[200:203],v[112:115],0
ds_read_b64_tr_b16 a[196:197],v2 offset:8192
ds_read_b64_tr_b16 a[198:199],v2 offset:12288
v_mfma_f32_16x16x32_bf16 a[100:103],a[204:207],v[120:123],a[100:103]
ds_read_b64_tr_b16 a[200:201],v3
ds_read_b64_tr_b16 a[202:203],v3 offset:4096
v_mfma_f32_16x16x32_bf16 a[104:107],a[208:211],v[112:115],0
ds_read_b64_tr_b16 a[204:205],v3 offset:8192
ds_read_b64_tr_b16 a[206:207],v3 offset:12288
v_mfma_f32_16x16x32_bf16 a[104:107],a[212:215],v[120:123],a[104:107]
ds_read_b64_tr_b16 a[208:209],v2 offset:256
v_cvt_pk_bf16_f32 v65,v66,v67
v_mfma_f32_16x16x32_bf16 a[108:111],a[216:219],v[112:115],0
global_load_lds_dwordx4 v4,s[10:11] offset:2048
ds_read_b64_tr_b16 a[210:211],v2 offset:4352
v_mfma_f32_16x16x32_bf16 a[108:111],a[220:223],v[120:123],a[108:111]
)A"
R"A(ds_read_b64_tr_b16 a[212:213],v2 offset:8448
ds_read_b64_tr_b16 a[214:215],v2 offset:12544
v_mfma_f32_16x16x32_bf16 a[112:115],a[224:227],v[112:115],0
ds_read_b64_tr_b16 a[216:217],v3 offset:256
ds_read_b64_tr_b16 a[218:219],v3 offset:4352
v_mfma_f32_16x16x32_bf16 a[112:115],a[228:231],v[120:123],a[112:115]
v_cvt_pk_bf16_f32 v66,v68,v69
v_cvt_pk_bf16_f32 v67,v70,v71
v_mfma_f32_16x16x32_bf16 a[116:119],a[232:235],v[112:115],0
v_exp_f32 v72,v72
v_mfma_f32_16x16x32_bf16 a[116:119],a[236:239],v[120:123],a[116:119]
v_exp_f32 v73,v73
v_mfma_f32_16x16x32_bf16 a[120:123],a[240:243],v[112:115],0
v_exp_f32 v74,v74
v_mfma_f32_16x16x32_bf16 a[120:123],a[244:247],v[120:123],a[120:123]
v_exp_f32 v75,v75
v_mfma_f32_16x16x32_bf16 a[124:127],a[248:251],v[112:115],0
v_exp_f32 v76,v76
v_mfma_f32_16x16x32_bf16 a[124:127],a[252:255],v[120:123],a[124:127]
v_exp_f32 v77,v77
v_mfma_f32_16x16x32_bf16 v[60:63],v[28:31],v[112:115],0
v_exp_f32 v78,v78
v_mfma_f32_16x16x32_bf16 v[60:63],v[28:31],v[120:123],v[60:63]
global_load_lds_dwordx4 v5,s[10:11] offset:3072
ds_read_b64_tr_b16 a[220:221],v3 offset:8448
v_mfma_f32_16x16x32_bf16 v[96:99],a[128:131],v[196:199],v[40:43]
v_exp_f32 v79,v79
v_mfma_f32_16x16x32_bf16 v[96:99],a[132:135],v[204:207],v[96:99]
ds_read_b64_tr_b16 a[222:223],v3 offset:12544
v_cvt_pk_bf16_f32 v72,v72,v73
v_mfma_f32_16x16x32_bf16 v[96:99],a[136:139],v[212:215],v[96:99]
v_cvt_pk_bf16_f32 v73,v74,v75
v_cvt_pk_bf16_f32 v74,v76,v77
v_mfma_f32_16x16x32_bf16 v[96:99],a[140:143],v[220:223],v[96:99]
ds_read_b64_tr_b16 a[224:225],v2 offset:512
v_cvt_pk_bf16_f32 v75,v78,v79
v_mfma_f32_16x16x32_bf16 v[100:103],a[156:159],v[220:223],v[40:43]
ds_read_b64_tr_b16 a[226:227],v2 offset:4608
ds_read_b64_tr_b16 a[228:229],v2 offset:8704
v_mfma_f32_16x16x32_bf16 v[100:103],a[152:155],v[212:215],v[100:103]
ds_read_b64_tr_b16 a[230:231],v2 offset:12800
ds_read_b64_tr_b16 a[232:233],v3 offset:512
v_mfma_f32_16x16x32_bf16 v[100:103],a[148:151],v[204:207],v[100:103]
ds_read_b64_tr_b16 a[234:235],v3 offset:4608
ds_read_b64_tr_b16 a[236:237],v3 offset:8704
v_mfma_f32_16x16x32_bf16 v[100:103],a[144:147],v[196:199],v[100:103]
ds_read_b64_tr_b16 a[238:239],v3 offset:12800
ds_read_b64_tr_b16 a[240:241],v2 offset:768
v_mfma_f32_16x16x32_bf16 v[104:107],a[160:163],v[196:199],v[40:43]
ds_read_b64_tr_b16 a[242:243],v2 offset:4864
v_add_u32 v7,s69,v6
v_mfma_f32_16x16x32_bf16 v[104:107],a[164:167],v[204:207],v[104:107]
s_add_u32 s10,s20,s74
s_addc_u32 s11,s21,0
ds_read_b64_tr_b16 a[244:245],v2 offset:8960
v_mfma_f32_16x16x32_bf16 v[104:107],a[168:171],v[212:215],v[104:107]
s_and_b32 s69,s75,0xfffc0000
ds_read_b64_tr_b16 a[246:247],v2 offset:13056
ds_read_b64_tr_b16 a[248:249],v3 offset:768
v_mfma_f32_16x16x32_bf16 v[104:107],a[172:175],v[220:223],v[104:107]
s_add_u32 m0,s76,0
ds_read_b64_tr_b16 a[250:251],v3 offset:4864
ds_read_b64_tr_b16 a[252:253],v3 offset:8960
v_mfma_f32_16x16x32_bf16 v[108:111],a[188:191],v[220:223],v[40:43]
s_add_u32 s75,s75,16384
ds_read_b64_tr_b16 a[254:255],v3 offset:13056
v_add_u32 v2,s78,v2
v_mfma_f32_16x16x32_bf16 v[108:111],a[184:187],v[212:215],v[108:111]
global_load_lds_dwordx4 v4,s[10:11] nt
v_add_u32 v3,s78,v3
v_mfma_f32_16x16x32_bf16 v[108:111],a[180:183],v[204:207],v[108:111]
s_sub_u32 s78,s77,32768
v_exp_f32 v80,v80
v_mfma_f32_16x16x32_bf16 v[108:111],a[176:179],v[196:199],v[108:111]
s_and_b32 s78,s78,0x1ffff
v_exp_f32 v81,v81
s_waitcnt lgkmcnt(7)
v_mfma_f32_16x16x32_bf16 a[0:3],a[192:195],v[64:67],a[0:3]
s_sub_u32 s78,s78,s77
v_exp_f32 v82,v82
v_mfma_f32_16x16x32_bf16 a[0:3],a[196:199],v[72:75],a[0:3]
s_add_u32 s77,s77,s78
v_exp_f32 v83,v83
v_mfma_f32_16x16x32_bf16 a[4:7],a[200:203],v[64:67],a[4:7]
v_exp_f32 v84,v84
v_mfma_f32_16x16x32_bf16 a[4:7],a[204:207],v[72:75],a[4:7]
v_exp_f32 v85,v85
v_mfma_f32_16x16x32_bf16 a[8:11],a[208:211],v[64:67],a[8:11]
v_exp_f32 v86,v86
v_mfma_f32_16x16x32_bf16 a[8:11],a[212:215],v[72:75],a[8:11]
v_exp_f32 v87,v87
v_mfma_f32_16x16x32_bf16 a[12:15],a[216:219],v[64:67],a[12:15]
v_cvt_pk_bf16_f32 v80,v80,v81
v_cvt_pk_bf16_f32 v81,v82,v83
v_mfma_f32_16x16x32_bf16 a[12:15],a[220:223],v[72:75],a[12:15]
v_cvt_pk_bf16_f32 v82,v84,v85
v_cvt_pk_bf16_f32 v83,v86,v87
v_mfma_f32_16x16x32_bf16 a[16:19],a[224:227],v[64:67],a[16:19]
v_exp_f32 v88,v88
v_mfma_f32_16x16x32_bf16 a[16:19],a[228:231],v[72:75],a[16:19]
v_exp_f32 v89,v89
v_mfma_f32_16x16x32_bf16 a[20:23],a[232:235],v[64:67],a[20:23]
v_exp_f32 v90,v90
v_mfma_f32_16x16x32_bf16 a[20:23],a[236:239],v[72:75],a[20:23]
s_waitcnt lgkmcnt(0)
v_exp_f32 v91,v91
v_mfma_f32_16x16x32_bf16 a[24:27],a[240:243],v[64:67],a[24:27]
v_exp_f32 v92,v92
v_mfma_f32_16x16x32_bf16 a[24:27],a[244:247],v[72:75],a[24:27]
v_exp_f32 v93,v93
v_mfma_f32_16x16x32_bf16 a[28:31],a[248:251],v[64:67],a[28:31]
v_exp_f32 v94,v94
v_mfma_f32_16x16x32_bf16 a[28:31],a[252:255],v[72:75],a[28:31]
s_getpc_b64 s[92:93]
s_cmp_lg_u32 s4,0
s_cbranch_scc1 L_pace_%=
s_nop 0
L_pd5_%=:
s_cbranch_execz L_trt0_%=
v_mfma_f32_16x16x32_bf16 v[48:51],v[28:31],v[64:67],v[48:51]
v_exp_f32 v95,v95
v_mfma_f32_16x16x32_bf16 v[48:51],v[28:31],v[72:75],v[48:51]
s_add_u32 s10,s20,s74
s_addc_u32 s11,s21,0
v_cvt_pk_bf16_f32 v88,v88,v89
s_waitcnt vmcnt(13)
s_barrier
v_mfma_f32_16x16x32_bf16 v[112:115],a[128:131],v[228:231],v[44:47]
s_add_u32 m0,s76,0
v_cvt_pk_bf16_f32 v89,v90,v91
v_cvt_pk_bf16_f32 v90,v92,v93
v_mfma_f32_16x16x32_bf16 v[112:115],a[132:135],v[236:239],v[112:115]
ds_read_b128 a[128:131],v1
v_cvt_pk_bf16_f32 v91,v94,v95
v_mfma_f32_16x16x32_bf16 v[112:115],a[136:139],v[244:247],v[112:115]
global_load_lds_dwordx4 v4,s[10:11] offset:1024 nt
ds_read_b128 a[132:135],v1 offset:256
v_mfma_f32_16x16x32_bf16 v[112:115],a[140:143],v[252:255],v[112:115]
v_exp_f32 v96,v96
v_mfma_f32_16x16x32_bf16 v[116:119],a[156:159],v[252:255],v[44:47]
v_exp_f32 v97,v97
v_mfma_f32_16x16x32_bf16 v[116:119],a[152:155],v[244:247],v[116:119]
ds_read_b128 a[136:139],v1 offset:512
v_cvt_pk_bf16_f32 v96,v96,v97
v_mfma_f32_16x16x32_bf16 v[116:119],a[148:151],v[236:239],v[116:119]
v_exp_f32 v98,v98
v_mfma_f32_16x16x32_bf16 v[116:119],a[144:147],v[228:231],v[116:119]
v_exp_f32 v99,v99
v_mfma_f32_16x16x32_bf16 v[120:123],a[160:163],v[228:231],v[44:47]
ds_read_b128 a[140:143],v1 offset:768
v_cvt_pk_bf16_f32 v97,v98,v99
v_mfma_f32_16x16x32_bf16 v[120:123],a[164:167],v[236:239],v[120:123]
v_exp_f32 v100,v100
v_mfma_f32_16x16x32_bf16 v[120:123],a[168:171],v[244:247],v[120:123]
v_exp_f32 v101,v101
v_mfma_f32_16x16x32_bf16 v[120:123],a[172:175],v[252:255],v[120:123]
ds_read_b128 a[156:159],v1 offset:4864
v_cvt_pk_bf16_f32 v98,v100,v101
v_mfma_f32_16x16x32_bf16 v[124:127],a[188:191],v[252:255],v[44:47]
v_exp_f32 v102,v102
v_mfma_f32_16x16x32_bf16 v[124:127],a[184:187],v[244:247],v[124:127]
v_exp_f32 v103,v103
v_mfma_f32_16x16x32_bf16 v[124:127],a[180:183],v[236:239],v[124:127]
ds_read_b128 a[152:155],v1 offset:4608
v_cvt_pk_bf16_f32 v99,v102,v103
v_mfma_f32_16x16x32_bf16 v[124:127],a[176:179],v[228:231],v[124:127]
v_exp_f32 v104,v104
v_mfma_f32_16x16x32_bf16 a[32:35],a[192:195],v[80:83],a[32:35]
global_load_lds_dwordx4 v5,s[10:11] offset:2048 nt
ds_read_b128 a[148:151],v1 offset:4352
v_mfma_f32_16x16x32_bf16 a[32:35],a[196:199],v[88:91],a[32:35]
v_exp_f32 v105,v105
v_mfma_f32_16x16x32_bf16 a[36:39],a[200:203],v[80:83],a[36:39]
ds_read_b128 a[144:147],v1 offset:4096
v_cvt_pk_bf16_f32 v104,v104,v105
v_mfma_f32_16x16x32_bf16 a[36:39],a[204:207],v[88:91],a[36:39]
v_exp_f32 v106,v106
v_mfma_f32_16x16x32_bf16 a[40:43],a[208:211],v[80:83],a[40:43]
v_exp_f32 v107,v107
v_mfma_f32_16x16x32_bf16 a[40:43],a[212:215],v[88:91],a[40:43]
ds_read_b128 a[160:163],v1 offset:8192
v_cvt_pk_bf16_f32 v105,v106,v107
v_mfma_f32_16x16x32_bf16 a[44:47],a[216:219],v[80:83],a[44:47]
v_exp_f32 v108,v108
v_mfma_f32_16x16x32_bf16 a[44:47],a[220:223],v[88:91],a[44:47]
v_exp_f32 v109,v109
v_mfma_f32_16x16x32_bf16 a[48:51],a[224:227],v[80:83],a[48:51]
ds_read_b128 a[164:167],v1 offset:8448
v_cvt_pk_bf16_f32 v106,v108,v109
v_mfma_f32_16x16x32_bf16 a[48:51],a[228:231],v[88:91],a[48:51]
v_exp_f32 v110,v110
v_mfma_f32_16x16x32_bf16 a[52:55],a[232:235],v[80:83],a[52:55]
v_exp_f32 v111,v111
v_mfma_f32_16x16x32_bf16 a[52:55],a[236:239],v[88:91],a[52:55]
ds_read_b128 a[168:171],v1 offset:8704
v_cvt_pk_bf16_f32 v107,v110,v111
v_mfma_f32_16x16x32_bf16 a[56:59],a[240:243],v[80:83],a[56:59]
v_exp_f32 v112,v112
v_mfma_f32_16x16x32_bf16 a[56:59],a[244:247],v[88:91],a[56:59]
v_exp_f32 v113,v113
v_mfma_f32_16x16x32_bf16 a[60:63],a[248:251],v[80:83],a[60:63]
global_load_lds_dwordx4 v5,s[10:11] offset:3072
ds_read_b128 a[172:175],v1 offset:8960
v_mfma_f32_16x16x32_bf16 a[60:63],a[252:255],v[88:91],a[60:63]
ds_read_b128 a[188:191],v1 offset:13056
v_cvt_pk_bf16_f32 v112,v112,v113
v_mfma_f32_16x16x32_bf16 v[52:55],v[28:31],v[80:83],v[52:55]
v_exp_f32 v114,v114
v_mfma_f32_16x16x32_bf16 v[52:55],v[28:31],v[88:91],v[52:55]
s_waitcnt lgkmcnt(3)
v_exp_f32 v115,v115
v_mfma_f32_16x16x32_bf16 v[64:67],a[128:131],v[132:135],v[32:35]
ds_read_b128 a[184:187],v1 offset:12800
v_cvt_pk_bf16_f32 v113,v114,v115
v_mfma_f32_16x16x32_bf16 v[64:67],a[132:135],v[140:143],v[64:67]
v_exp_f32 v116,v116
v_mfma_f32_16x16x32_bf16 v[64:67],a[136:139],v[148:151],v[64:67]
ds_read_b128 a[180:183],v1 offset:12544
v_mfma_f32_16x16x32_bf16 v[64:67],a[140:143],v[156:159],v[64:67]
ds_read_b128 a[176:179],v1 offset:12288
v_add_u32 v1,s78,v1
v_mfma_f32_16x16x32_bf16 v[68:71],a[156:159],v[156:159],v[32:35]
v_exp_f32 v117,v117
v_mfma_f32_16x16x32_bf16 v[68:71],a[152:155],v[148:151],v[68:71]
v_exp_f32 v118,v118
v_mfma_f32_16x16x32_bf16 v[68:71],a[148:151],v[140:143],v[68:71]
s_add_u32 s10,s24,s74
s_addc_u32 s11,s25,0
v_cvt_pk_bf16_f32 v114,v116,v117
v_mfma_f32_16x16x32_bf16 v[68:71],a[144:147],v[132:135],v[68:71]
s_add_u32 s74,s74,16384
v_exp_f32 v119,v119
v_mfma_f32_16x16x32_bf16 v[72:75],a[160:163],v[132:135],v[32:35]
s_add_u32 m0,s76,16384
v_exp_f32 v120,v120
v_mfma_f32_16x16x32_bf16 v[72:75],a[164:167],v[140:143],v[72:75]
s_and_b32 s74,s74,0x3ffff
v_exp_f32 v121,v121
s_waitcnt lgkmcnt(2)
v_mfma_f32_16x16x32_bf16 v[72:75],a[168:171],v[148:151],v[72:75]
global_load_lds_dwordx4 v4,s[10:11]
v_cvt_pk_bf16_f32 v115,v118,v119
v_mfma_f32_16x16x32_bf16 v[72:75],a[172:175],v[156:159],v[72:75]
s_or_b32 s74,s74,s69
v_exp_f32 v122,v122
v_mfma_f32_16x16x32_bf16 v[76:79],a[188:191],v[156:159],v[32:35]
s_sub_u32 s76,s76,32768
v_exp_f32 v123,v123
v_mfma_f32_16x16x32_bf16 v[76:79],a[184:187],v[148:151],v[76:79]
s_and_b32 s76,s76,0x1ffff
v_exp_f32 v124,v124
s_waitcnt lgkmcnt(0)
v_mfma_f32_16x16x32_bf16 v[76:79],a[180:183],v[140:143],v[76:79]
s_and_b32 s69,s75,0xfffc0000
v_exp_f32 v125,v125
v_mfma_f32_16x16x32_bf16 v[76:79],a[176:179],v[132:135],v[76:79]
v_exp_f32 v126,v126
v_mfma_f32_16x16x32_bf16 a[64:67],a[192:195],v[96:99],a[64:67]
v_exp_f32 v127,v127
v_mfma_f32_16x16x32_bf16 a[64:67],a[196:199],v[104:107],a[64:67]
v_cvt_pk_bf16_f32 v120,v120,v121
v_cvt_pk_bf16_f32 v121,v122,v123
v_mfma_f32_16x16x32_bf16 a[68:71],a[200:203],v[96:99],a[68:71]
v_cvt_pk_bf16_f32 v122,v124,v125
v_cvt_pk_bf16_f32 v123,v126,v127
v_mfma_f32_16x16x32_bf16 a[68:71],a[204:207],v[104:107],a[68:71]
v_exp_f32 v64,v64
v_mfma_f32_16x16x32_bf16 a[72:75],a[208:211],v[96:99],a[72:75]
v_exp_f32 v65,v65
v_mfma_f32_16x16x32_bf16 a[72:75],a[212:215],v[104:107],a[72:75]
v_exp_f32 v66,v66
v_mfma_f32_16x16x32_bf16 a[76:79],a[216:219],v[96:99],a[76:79]
v_exp_f32 v67,v67
v_mfma_f32_16x16x32_bf16 a[76:79],a[220:223],v[104:107],a[76:79]
v_exp_f32 v68,v68
v_mfma_f32_16x16x32_bf16 a[80:83],a[224:227],v[96:99],a[80:83]
global_load_lds_dwordx4 v5,s[10:11] offset:1024
v_cvt_pk_bf16_f32 v64,v64,v65
v_mfma_f32_16x16x32_bf16 a[80:83],a[228:231],v[104:107],a[80:83]
v_exp_f32 v69,v69
v_mfma_f32_16x16x32_bf16 a[84:87],a[232:235],v[96:99],a[84:87]
v_exp_f32 v70,v70
v_mfma_f32_16x16x32_bf16 a[84:87],a[236:239],v[104:107],a[84:87]
v_exp_f32 v71,v71
v_mfma_f32_16x16x32_bf16 a[88:91],a[240:243],v[96:99],a[88:91]
v_cvt_pk_bf16_f32 v65,v66,v67
v_cvt_pk_bf16_f32 v66,v68,v69
v_mfma_f32_16x16x32_bf16 a[88:91],a[244:247],v[104:107],a[88:91]
v_exp_f32 v72,v72
v_mfma_f32_16x16x32_bf16 a[92:95],a[248:251],v[96:99],a[92:95]
)A"
R"A(v_exp_f32 v73,v73
v_mfma_f32_16x16x32_bf16 a[92:95],a[252:255],v[104:107],a[92:95]
v_cvt_pk_bf16_f32 v67,v70,v71
v_cvt_pk_bf16_f32 v72,v72,v73
v_mfma_f32_16x16x32_bf16 v[56:59],v[28:31],v[96:99],v[56:59]
v_exp_f32 v74,v74
v_mfma_f32_16x16x32_bf16 v[56:59],v[28:31],v[104:107],v[56:59]
v_exp_f32 v75,v75
v_mfma_f32_16x16x32_bf16 v[80:83],a[128:131],v[164:167],v[36:39]
v_exp_f32 v76,v76
v_mfma_f32_16x16x32_bf16 v[80:83],a[132:135],v[172:175],v[80:83]
v_exp_f32 v77,v77
v_mfma_f32_16x16x32_bf16 v[80:83],a[136:139],v[180:183],v[80:83]
v_exp_f32 v78,v78
v_mfma_f32_16x16x32_bf16 v[80:83],a[140:143],v[188:191],v[80:83]
v_exp_f32 v79,v79
v_mfma_f32_16x16x32_bf16 v[84:87],a[156:159],v[188:191],v[36:39]
v_cvt_pk_bf16_f32 v73,v74,v75
v_cvt_pk_bf16_f32 v74,v76,v77
v_mfma_f32_16x16x32_bf16 v[84:87],a[152:155],v[180:183],v[84:87]
global_load_lds_dwordx4 v4,s[10:11] offset:2048
v_cvt_pk_bf16_f32 v75,v78,v79
v_mfma_f32_16x16x32_bf16 v[84:87],a[148:151],v[172:175],v[84:87]
v_exp_f32 v80,v80
v_mfma_f32_16x16x32_bf16 v[84:87],a[144:147],v[164:167],v[84:87]
v_exp_f32 v81,v81
v_mfma_f32_16x16x32_bf16 v[88:91],a[160:163],v[164:167],v[36:39]
v_exp_f32 v82,v82
v_mfma_f32_16x16x32_bf16 v[88:91],a[164:167],v[172:175],v[88:91]
v_exp_f32 v83,v83
v_mfma_f32_16x16x32_bf16 v[88:91],a[168:171],v[180:183],v[88:91]
v_cvt_pk_bf16_f32 v80,v80,v81
v_cvt_pk_bf16_f32 v81,v82,v83
v_mfma_f32_16x16x32_bf16 v[88:91],a[172:175],v[188:191],v[88:91]
v_exp_f32 v84,v84
v_mfma_f32_16x16x32_bf16 v[92:95],a[188:191],v[188:191],v[36:39]
v_exp_f32 v85,v85
v_mfma_f32_16x16x32_bf16 v[92:95],a[184:187],v[180:183],v[92:95]
v_exp_f32 v86,v86
v_mfma_f32_16x16x32_bf16 v[92:95],a[180:183],v[172:175],v[92:95]
v_exp_f32 v87,v87
v_mfma_f32_16x16x32_bf16 v[92:95],a[176:179],v[164:167],v[92:95]
v_cvt_pk_bf16_f32 v82,v84,v85
v_cvt_pk_bf16_f32 v83,v86,v87
v_mfma_f32_16x16x32_bf16 a[96:99],a[192:195],v[112:115],a[96:99]
ds_read_b64_tr_b16 a[192:193],v2
ds_read_b64_tr_b16 a[194:195],v2 offset:4096
v_mfma_f32_16x16x32_bf16 a[96:99],a[196:199],v[120:123],a[96:99]
ds_read_b64_tr_b16 a[196:197],v2 offset:8192
ds_read_b64_tr_b16 a[198:199],v2 offset:12288
v_mfma_f32_16x16x32_bf16 a[100:103],a[200:203],v[112:115],a[100:103]
ds_read_b64_tr_b16 a[200:201],v3
ds_read_b64_tr_b16 a[202:203],v3 offset:4096
v_mfma_f32_16x16x32_bf16 a[100:103],a[204:207],v[120:123],a[100:103]
global_load_lds_dwordx4 v5,s[10:11] offset:3072
ds_read_b64_tr_b16 a[204:205],v3 offset:8192
v_mfma_f32_16x16x32_bf16 a[104:107],a[208:211],v[112:115],a[104:107]
ds_read_b64_tr_b16 a[206:207],v3 offset:12288
ds_read_b64_tr_b16 a[208:209],v2 offset:256
v_mfma_f32_16x16x32_bf16 a[104:107],a[212:215],v[120:123],a[104:107]
ds_read_b64_tr_b16 a[210:211],v2 offset:4352
ds_read_b64_tr_b16 a[212:213],v2 offset:8448
v_mfma_f32_16x16x32_bf16 a[108:111],a[216:219],v[112:115],a[108:111]
v_exp_f32 v88,v88
v_mfma_f32_16x16x32_bf16 a[108:111],a[220:223],v[120:123],a[108:111]
ds_read_b64_tr_b16 a[214:215],v2 offset:12544
ds_read_b64_tr_b16 a[216:217],v3 offset:256
v_mfma_f32_16x16x32_bf16 a[112:115],a[224:227],v[112:115],a[112:115]
ds_read_b64_tr_b16 a[218:219],v3 offset:4352
ds_read_b64_tr_b16 a[220:221],v3 offset:8448
v_mfma_f32_16x16x32_bf16 a[112:115],a[228:231],v[120:123],a[112:115]
ds_read_b64_tr_b16 a[222:223],v3 offset:12544
ds_read_b64_tr_b16 a[224:225],v2 offset:512
v_mfma_f32_16x16x32_bf16 a[116:119],a[232:235],v[112:115],a[116:119]
v_exp_f32 v89,v89
v_mfma_f32_16x16x32_bf16 a[116:119],a[236:239],v[120:123],a[116:119]
ds_read_b64_tr_b16 a[226:227],v2 offset:4608
ds_read_b64_tr_b16 a[228:229],v2 offset:8704
v_mfma_f32_16x16x32_bf16 a[120:123],a[240:243],v[112:115],a[120:123]
ds_read_b64_tr_b16 a[230:231],v2 offset:12800
ds_read_b64_tr_b16 a[232:233],v3 offset:512
v_mfma_f32_16x16x32_bf16 a[120:123],a[244:247],v[120:123],a[120:123]
s_add_u32 s10,s20,s74
s_addc_u32 s11,s21,0
ds_read_b64_tr_b16 a[234:235],v3 offset:4608
v_mfma_f32_16x16x32_bf16 a[124:127],a[248:251],v[112:115],a[124:127]
ds_read_b64_tr_b16 a[236:237],v3 offset:8704
ds_read_b64_tr_b16 a[238:239],v3 offset:12800
v_mfma_f32_16x16x32_bf16 a[124:127],a[252:255],v[120:123],a[124:127]
s_add_u32 m0,s76,0
ds_read_b64_tr_b16 a[240:241],v2 offset:768
ds_read_b64_tr_b16 a[242:243],v2 offset:4864
v_mfma_f32_16x16x32_bf16 v[60:63],v[28:31],v[112:115],v[60:63]
ds_read_b64_tr_b16 a[244:245],v2 offset:8960
v_cvt_pk_bf16_f32 v88,v88,v89
v_mfma_f32_16x16x32_bf16 v[60:63],v[28:31],v[120:123],v[60:63]
global_load_lds_dwordx4 v4,s[10:11] nt
ds_read_b64_tr_b16 a[246:247],v2 offset:13056
v_mfma_f32_16x16x32_bf16 v[96:99],a[128:131],v[196:199],v[40:43]
ds_read_b64_tr_b16 a[248:249],v3 offset:768
ds_read_b64_tr_b16 a[250:251],v3 offset:4864
v_mfma_f32_16x16x32_bf16 v[96:99],a[132:135],v[204:207],v[96:99]
ds_read_b64_tr_b16 a[252:253],v3 offset:8960
ds_read_b64_tr_b16 a[254:255],v3 offset:13056
v_mfma_f32_16x16x32_bf16 v[96:99],a[136:139],v[212:215],v[96:99]
v_exp_f32 v90,v90
v_mfma_f32_16x16x32_bf16 v[96:99],a[140:143],v[220:223],v[96:99]
v_exp_f32 v91,v91
v_mfma_f32_16x16x32_bf16 v[100:103],a[156:159],v[220:223],v[40:43]
v_exp_f32 v92,v92
v_mfma_f32_16x16x32_bf16 v[100:103],a[152:155],v[212:215],v[100:103]
v_exp_f32 v93,v93
v_mfma_f32_16x16x32_bf16 v[100:103],a[148:151],v[204:207],v[100:103]
v_exp_f32 v94,v94
v_mfma_f32_16x16x32_bf16 v[100:103],a[144:147],v[196:199],v[100:103]
v_exp_f32 v95,v95
v_mfma_f32_16x16x32_bf16 v[104:107],a[160:163],v[196:199],v[40:43]
v_cvt_pk_bf16_f32 v89,v90,v91
v_cvt_pk_bf16_f32 v90,v92,v93
v_mfma_f32_16x16x32_bf16 v[104:107],a[164:167],v[204:207],v[104:107]
v_cvt_pk_bf16_f32 v91,v94,v95
v_add_u32 v2,s78,v2
v_mfma_f32_16x16x32_bf16 v[104:107],a[168:171],v[212:215],v[104:107]
v_exp_f32 v96,v96
v_mfma_f32_16x16x32_bf16 v[104:107],a[172:175],v[220:223],v[104:107]
v_exp_f32 v97,v97
v_mfma_f32_16x16x32_bf16 v[108:111],a[188:191],v[220:223],v[40:43]
v_exp_f32 v98,v98
v_mfma_f32_16x16x32_bf16 v[108:111],a[184:187],v[212:215],v[108:111]
global_load_lds_dwordx4 v4,s[10:11] offset:1024 nt
v_cvt_pk_bf16_f32 v96,v96,v97
v_mfma_f32_16x16x32_bf16 v[108:111],a[180:183],v[204:207],v[108:111]
v_exp_f32 v99,v99
v_mfma_f32_16x16x32_bf16 v[108:111],a[176:179],v[196:199],v[108:111]
s_waitcnt lgkmcnt(0)
v_exp_f32 v100,v100
v_mfma_f32_16x16x32_bf16 a[0:3],a[192:195],v[64:67],a[0:3]
v_exp_f32 v101,v101
v_mfma_f32_16x16x32_bf16 a[0:3],a[196:199],v[72:75],a[0:3]
v_exp_f32 v102,v102
v_mfma_f32_16x16x32_bf16 a[4:7],a[200:203],v[64:67],a[4:7]
v_exp_f32 v103,v103
v_mfma_f32_16x16x32_bf16 a[4:7],a[204:207],v[72:75],a[4:7]
v_cvt_pk_bf16_f32 v97,v98,v99
v_cvt_pk_bf16_f32 v98,v100,v101
v_mfma_f32_16x16x32_bf16 a[8:11],a[208:211],v[64:67],a[8:11]
v_cvt_pk_bf16_f32 v99,v102,v103
v_add_u32 v3,s78,v3
v_mfma_f32_16x16x32_bf16 a[8:11],a[212:215],v[72:75],a[8:11]
s_sub_u32 s78,s77,32768
v_exp_f32 v104,v104
v_mfma_f32_16x16x32_bf16 a[12:15],a[216:219],v[64:67],a[12:15]
s_and_b32 s78,s78,0x1ffff
v_exp_f32 v105,v105
v_mfma_f32_16x16x32_bf16 a[12:15],a[220:223],v[72:75],a[12:15]
s_sub_u32 s78,s78,s77
v_exp_f32 v106,v106
v_mfma_f32_16x16x32_bf16 a[16:19],a[224:227],v[64:67],a[16:19]
s_add_u32 s77,s77,s78
v_exp_f32 v107,v107
v_mfma_f32_16x16x32_bf16 a[16:19],a[228:231],v[72:75],a[16:19]
v_exp_f32 v108,v108
v_mfma_f32_16x16x32_bf16 a[20:23],a[232:235],v[64:67],a[20:23]
s_getreg_b32 s92,hwreg(HW_REG_IB_STS,0,4)
s_min_u32 s4,s92,s6
s_mov_b32 s6,s92
v_mfma_f32_16x16x32_bf16 a[20:23],a[236:239],v[72:75],a[20:23]
s_cmp_gt_u32 s4,s9
s_cselect_b32 s4,15,s5
v_cvt_pk_bf16_f32 v104,v104,v105
v_mfma_f32_16x16x32_bf16 a[24:27],a[240:243],v[64:67],a[24:27]
v_exp_f32 v109,v109
v_mfma_f32_16x16x32_bf16 a[24:27],a[244:247],v[72:75],a[24:27]
v_exp_f32 v110,v110
v_mfma_f32_16x16x32_bf16 a[28:31],a[248:251],v[64:67],a[28:31]
v_exp_f32 v111,v111
v_mfma_f32_16x16x32_bf16 a[28:31],a[252:255],v[72:75],a[28:31]
s_getpc_b64 s[92:93]
s_cmp_lg_u32 s4,0
s_cbranch_scc1 L_pace_%=
v_mfma_f32_16x16x32_bf16 v[48:51],v[28:31],v[64:67],v[48:51]
v_cvt_pk_bf16_f32 v105,v106,v107
v_cvt_pk_bf16_f32 v106,v108,v109
v_mfma_f32_16x16x32_bf16 v[48:51],v[28:31],v[72:75],v[48:51]
s_add_u32 s10,s20,s74
s_addc_u32 s11,s21,0
v_cvt_pk_bf16_f32 v107,v110,v111
s_waitcnt vmcnt(10)
s_barrier
v_mfma_f32_16x16x32_bf16 v[24:27],v[8:11],v[128:131],0
s_add_u32 m0,s76,0
v_mfma_f32_16x16x32_bf16 v[24:27],v[12:15],v[136:139],v[24:27]
v_mfma_f32_16x16x32_bf16 v[24:27],v[16:19],v[144:147],v[24:27]
global_load_lds_dwordx4 v5,s[10:11] offset:2048 nt
v_mfma_f32_16x16x32_bf16 v[24:27],v[20:23],v[152:155],v[24:27]
v_mfma_f32_16x16x32_bf16 v[112:115],a[128:131],v[228:231],v[44:47]
ds_read_b128 a[128:131],v1
v_mfma_f32_16x16x32_bf16 v[112:115],a[132:135],v[236:239],v[112:115]
ds_read_b128 a[132:135],v1 offset:256
v_mfma_f32_16x16x32_bf16 v[112:115],a[136:139],v[244:247],v[112:115]
ds_read_b128 a[136:139],v1 offset:512
v_mfma_f32_16x16x32_bf16 v[112:115],a[140:143],v[252:255],v[112:115]
ds_read_b128 a[140:143],v1 offset:768
v_mfma_f32_16x16x32_bf16 v[116:119],a[156:159],v[252:255],v[44:47]
v_max3_f32 v24,v24,v25,v26
v_max_f32 v24,v24,v27
v_mfma_f32_16x16x32_bf16 v[116:119],a[152:155],v[244:247],v[116:119]
ds_read_b128 a[156:159],v1 offset:4864
v_mov_b32 v25,v24
v_mfma_f32_16x16x32_bf16 v[116:119],a[148:151],v[236:239],v[116:119]
v_exp_f32 v112,v112
v_mfma_f32_16x16x32_bf16 v[116:119],a[144:147],v[228:231],v[116:119]
v_permlane32_swap_b32 v24,v25
v_mfma_f32_16x16x32_bf16 v[120:123],a[160:163],v[228:231],v[44:47]
v_max_f32 v24,v24,v25
v_mov_b32 v25,v24
v_mfma_f32_16x16x32_bf16 v[120:123],a[164:167],v[236:239],v[120:123]
v_exp_f32 v113,v113
v_mfma_f32_16x16x32_bf16 v[120:123],a[168:171],v[244:247],v[120:123]
v_permlane16_swap_b32 v24,v25
v_mfma_f32_16x16x32_bf16 v[120:123],a[172:175],v[252:255],v[120:123]
v_max_f32 v24,v24,v25
v_sub_f32 v27,0,v24
v_mfma_f32_16x16x32_bf16 v[124:127],a[188:191],v[252:255],v[44:47]
global_load_lds_dwordx4 v5,s[10:11] offset:3072
v_sub_f32 v26,0,v24
v_sub_f32 v25,0,v24
v_sub_f32 v24,0,v24
v_mfma_f32_16x16x32_bf16 v[124:127],a[184:187],v[244:247],v[124:127]
ds_write_b128 v7,v[24:27]
v_mfma_f32_16x16x32_bf16 v[124:127],a[180:183],v[236:239],v[124:127]
ds_read_b128 a[152:155],v1 offset:4608
v_cvt_pk_bf16_f32 v112,v112,v113
v_mfma_f32_16x16x32_bf16 v[124:127],a[176:179],v[228:231],v[124:127]
v_exp_f32 v114,v114
v_mfma_f32_16x16x32_bf16 v[24:27],v[8:11],v[160:163],0
v_exp_f32 v115,v115
v_mfma_f32_16x16x32_bf16 v[24:27],v[12:15],v[168:171],v[24:27]
ds_read_b128 a[148:151],v1 offset:4352
v_cvt_pk_bf16_f32 v113,v114,v115
v_mfma_f32_16x16x32_bf16 v[24:27],v[16:19],v[176:179],v[24:27]
v_exp_f32 v116,v116
v_mfma_f32_16x16x32_bf16 v[24:27],v[20:23],v[184:187],v[24:27]
v_exp_f32 v117,v117
v_mfma_f32_16x16x32_bf16 a[32:35],a[192:195],v[80:83],a[32:35]
ds_read_b128 a[144:147],v1 offset:4096
v_cvt_pk_bf16_f32 v114,v116,v117
v_mfma_f32_16x16x32_bf16 a[32:35],a[196:199],v[88:91],a[32:35]
v_exp_f32 v118,v118
v_mfma_f32_16x16x32_bf16 a[36:39],a[200:203],v[80:83],a[36:39]
s_add_u32 s10,s24,s74
s_addc_u32 s11,s25,0
ds_read_b128 a[160:163],v1 offset:8192
v_mfma_f32_16x16x32_bf16 a[36:39],a[204:207],v[88:91],a[36:39]
s_add_u32 s74,s74,16384
v_max3_f32 v24,v24,v25,v26
v_max_f32 v24,v24,v27
v_mfma_f32_16x16x32_bf16 a[40:43],a[208:211],v[80:83],a[40:43]
s_add_u32 m0,s76,16384
ds_read_b128 a[164:167],v1 offset:8448
v_mov_b32 v25,v24
v_mfma_f32_16x16x32_bf16 a[40:43],a[212:215],v[88:91],a[40:43]
s_and_b32 s74,s74,0x3ffff
v_exp_f32 v119,v119
v_mfma_f32_16x16x32_bf16 a[44:47],a[216:219],v[80:83],a[44:47]
global_load_lds_dwordx4 v4,s[10:11]
ds_read_b128 a[168:171],v1 offset:8704
v_mfma_f32_16x16x32_bf16 a[44:47],a[220:223],v[88:91],a[44:47]
s_or_b32 s74,s74,s69
v_permlane32_swap_b32 v24,v25
v_mfma_f32_16x16x32_bf16 a[48:51],a[224:227],v[80:83],a[48:51]
s_sub_u32 s76,s76,32768
v_max_f32 v24,v24,v25
v_mov_b32 v25,v24
v_mfma_f32_16x16x32_bf16 a[48:51],a[228:231],v[88:91],a[48:51]
s_and_b32 s76,s76,0x1ffff
ds_read_b128 a[172:175],v1 offset:8960
v_cvt_pk_bf16_f32 v115,v118,v119
v_mfma_f32_16x16x32_bf16 a[52:55],a[232:235],v[80:83],a[52:55]
v_permlane16_swap_b32 v24,v25
v_mfma_f32_16x16x32_bf16 a[52:55],a[236:239],v[88:91],a[52:55]
v_max_f32 v24,v24,v25
)A"
R"A(v_sub_f32 v27,0,v24
v_mfma_f32_16x16x32_bf16 a[56:59],a[240:243],v[80:83],a[56:59]
v_sub_f32 v26,0,v24
v_sub_f32 v25,0,v24
v_mfma_f32_16x16x32_bf16 a[56:59],a[244:247],v[88:91],a[56:59]
ds_read_b128 a[188:191],v1 offset:13056
v_sub_f32 v24,0,v24
v_mfma_f32_16x16x32_bf16 a[60:63],a[248:251],v[80:83],a[60:63]
ds_write_b128 v7,v[24:27] offset:1024
v_mfma_f32_16x16x32_bf16 a[60:63],a[252:255],v[88:91],a[60:63]
v_exp_f32 v120,v120
v_mfma_f32_16x16x32_bf16 v[52:55],v[28:31],v[80:83],v[52:55]
v_exp_f32 v121,v121
v_mfma_f32_16x16x32_bf16 v[52:55],v[28:31],v[88:91],v[52:55]
ds_read_b128 a[184:187],v1 offset:12800
v_cvt_pk_bf16_f32 v120,v120,v121
v_mfma_f32_16x16x32_bf16 v[24:27],v[8:11],v[192:195],0
v_exp_f32 v122,v122
v_mfma_f32_16x16x32_bf16 v[24:27],v[12:15],v[200:203],v[24:27]
v_exp_f32 v123,v123
v_mfma_f32_16x16x32_bf16 v[24:27],v[16:19],v[208:211],v[24:27]
s_waitcnt lgkmcnt(2)
ds_read_b128 a[180:183],v1 offset:12544
v_cvt_pk_bf16_f32 v121,v122,v123
v_mfma_f32_16x16x32_bf16 v[24:27],v[20:23],v[216:219],v[24:27]
global_load_lds_dwordx4 v5,s[10:11] offset:1024
ds_read_b128 a[176:179],v1 offset:12288
v_mfma_f32_16x16x32_bf16 v[64:67],a[128:131],v[132:135],v[32:35]
v_exp_f32 v124,v124
v_mfma_f32_16x16x32_bf16 v[64:67],a[132:135],v[140:143],v[64:67]
v_exp_f32 v125,v125
v_mfma_f32_16x16x32_bf16 v[64:67],a[136:139],v[148:151],v[64:67]
v_exp_f32 v126,v126
v_mfma_f32_16x16x32_bf16 v[64:67],a[140:143],v[156:159],v[64:67]
v_max3_f32 v24,v24,v25,v26
v_max_f32 v24,v24,v27
v_mfma_f32_16x16x32_bf16 v[68:71],a[156:159],v[156:159],v[32:35]
v_mov_b32 v25,v24
v_cvt_pk_bf16_f32 v122,v124,v125
v_mfma_f32_16x16x32_bf16 v[68:71],a[152:155],v[148:151],v[68:71]
v_exp_f32 v127,v127
v_mfma_f32_16x16x32_bf16 v[68:71],a[148:151],v[140:143],v[68:71]
v_permlane32_swap_b32 v24,v25
v_mfma_f32_16x16x32_bf16 v[68:71],a[144:147],v[132:135],v[68:71]
v_max_f32 v24,v24,v25
v_mov_b32 v25,v24
v_mfma_f32_16x16x32_bf16 v[72:75],a[160:163],v[132:135],v[32:35]
v_cvt_pk_bf16_f32 v123,v126,v127
v_add_u32 v1,s78,v1
v_mfma_f32_16x16x32_bf16 v[72:75],a[164:167],v[140:143],v[72:75]
v_permlane16_swap_b32 v24,v25
v_mfma_f32_16x16x32_bf16 v[72:75],a[168:171],v[148:151],v[72:75]
v_max_f32 v24,v24,v25
v_sub_f32 v27,0,v24
v_mfma_f32_16x16x32_bf16 v[72:75],a[172:175],v[156:159],v[72:75]
v_sub_f32 v26,0,v24
v_sub_f32 v25,0,v24
v_mfma_f32_16x16x32_bf16 v[76:79],a[188:191],v[156:159],v[32:35]
s_waitcnt lgkmcnt(0)
v_exp_f32 v64,v64
v_mfma_f32_16x16x32_bf16 v[76:79],a[184:187],v[148:151],v[76:79]
global_load_lds_dwordx4 v4,s[10:11] offset:2048
v_sub_f32 v24,0,v24
v_mfma_f32_16x16x32_bf16 v[76:79],a[180:183],v[140:143],v[76:79]
ds_write_b128 v7,v[24:27] offset:2048
v_mfma_f32_16x16x32_bf16 v[76:79],a[176:179],v[132:135],v[76:79]
v_exp_f32 v65,v65
v_mfma_f32_16x16x32_bf16 a[64:67],a[192:195],v[96:99],a[64:67]
v_exp_f32 v66,v66
v_mfma_f32_16x16x32_bf16 a[64:67],a[196:199],v[104:107],a[64:67]
v_exp_f32 v67,v67
v_mfma_f32_16x16x32_bf16 a[68:71],a[200:203],v[96:99],a[68:71]
v_exp_f32 v68,v68
v_mfma_f32_16x16x32_bf16 a[68:71],a[204:207],v[104:107],a[68:71]
v_exp_f32 v69,v69
v_mfma_f32_16x16x32_bf16 a[72:75],a[208:211],v[96:99],a[72:75]
v_exp_f32 v70,v70
v_mfma_f32_16x16x32_bf16 a[72:75],a[212:215],v[104:107],a[72:75]
v_exp_f32 v71,v71
v_mfma_f32_16x16x32_bf16 a[76:79],a[216:219],v[96:99],a[76:79]
v_cvt_pk_bf16_f32 v64,v64,v65
v_cvt_pk_bf16_f32 v65,v66,v67
v_mfma_f32_16x16x32_bf16 a[76:79],a[220:223],v[104:107],a[76:79]
v_cvt_pk_bf16_f32 v66,v68,v69
v_cvt_pk_bf16_f32 v67,v70,v71
v_mfma_f32_16x16x32_bf16 a[80:83],a[224:227],v[96:99],a[80:83]
v_exp_f32 v72,v72
v_mfma_f32_16x16x32_bf16 a[80:83],a[228:231],v[104:107],a[80:83]
v_exp_f32 v73,v73
v_mfma_f32_16x16x32_bf16 a[84:87],a[232:235],v[96:99],a[84:87]
v_exp_f32 v74,v74
v_mfma_f32_16x16x32_bf16 a[84:87],a[236:239],v[104:107],a[84:87]
v_exp_f32 v75,v75
v_mfma_f32_16x16x32_bf16 a[88:91],a[240:243],v[96:99],a[88:91]
global_load_lds_dwordx4 v5,s[10:11] offset:3072
v_cvt_pk_bf16_f32 v72,v72,v73
v_mfma_f32_16x16x32_bf16 a[88:91],a[244:247],v[104:107],a[88:91]
v_exp_f32 v76,v76
v_mfma_f32_16x16x32_bf16 a[92:95],a[248:251],v[96:99],a[92:95]
v_exp_f32 v77,v77
v_mfma_f32_16x16x32_bf16 a[92:95],a[252:255],v[104:107],a[92:95]
v_exp_f32 v78,v78
v_mfma_f32_16x16x32_bf16 v[56:59],v[28:31],v[96:99],v[56:59]
v_exp_f32 v79,v79
v_mfma_f32_16x16x32_bf16 v[56:59],v[28:31],v[104:107],v[56:59]
v_cvt_pk_bf16_f32 v73,v74,v75
v_cvt_pk_bf16_f32 v74,v76,v77
v_mfma_f32_16x16x32_bf16 v[24:27],v[8:11],v[224:227],0
v_cvt_pk_bf16_f32 v75,v78,v79
v_mfma_f32_16x16x32_bf16 v[24:27],v[12:15],v[232:235],v[24:27]
v_mfma_f32_16x16x32_bf16 v[24:27],v[16:19],v[240:243],v[24:27]
v_mfma_f32_16x16x32_bf16 v[24:27],v[20:23],v[248:251],v[24:27]
v_mfma_f32_16x16x32_bf16 v[80:83],a[128:131],v[164:167],v[36:39]
s_add_u32 s10,s20,s74
s_addc_u32 s11,s21,0
v_mfma_f32_16x16x32_bf16 v[80:83],a[132:135],v[172:175],v[80:83]
v_mfma_f32_16x16x32_bf16 v[80:83],a[136:139],v[180:183],v[80:83]
s_add_u32 m0,s76,0
v_mfma_f32_16x16x32_bf16 v[80:83],a[140:143],v[188:191],v[80:83]
v_mfma_f32_16x16x32_bf16 v[84:87],a[156:159],v[188:191],v[36:39]
global_load_lds_dwordx4 v4,s[10:11] nt
v_max3_f32 v24,v24,v25,v26
v_mfma_f32_16x16x32_bf16 v[84:87],a[152:155],v[180:183],v[84:87]
v_max_f32 v24,v24,v27
v_mov_b32 v25,v24
v_mfma_f32_16x16x32_bf16 v[84:87],a[148:151],v[172:175],v[84:87]
v_mfma_f32_16x16x32_bf16 v[84:87],a[144:147],v[164:167],v[84:87]
v_permlane32_swap_b32 v24,v25
v_mfma_f32_16x16x32_bf16 v[88:91],a[160:163],v[164:167],v[36:39]
v_max_f32 v24,v24,v25
v_mov_b32 v25,v24
v_mfma_f32_16x16x32_bf16 v[88:91],a[164:167],v[172:175],v[88:91]
v_exp_f32 v80,v80
v_mfma_f32_16x16x32_bf16 v[88:91],a[168:171],v[180:183],v[88:91]
v_permlane16_swap_b32 v24,v25
v_mfma_f32_16x16x32_bf16 v[88:91],a[172:175],v[188:191],v[88:91]
v_max_f32 v24,v24,v25
v_max_f32 v24,s72,v24
v_mfma_f32_16x16x32_bf16 v[92:95],a[188:191],v[188:191],v[36:39]
v_sub_f32 v27,0,v24
v_sub_f32 v26,0,v24
v_mfma_f32_16x16x32_bf16 v[92:95],a[184:187],v[180:183],v[92:95]
v_sub_f32 v25,0,v24
v_sub_f32 v24,0,v24
v_mfma_f32_16x16x32_bf16 v[92:95],a[180:183],v[172:175],v[92:95]
v_exp_f32 v81,v81
v_mfma_f32_16x16x32_bf16 v[92:95],a[176:179],v[164:167],v[92:95]
v_exp_f32 v82,v82
v_mfma_f32_16x16x32_bf16 a[96:99],a[192:195],v[112:115],a[96:99]
ds_read_b64_tr_b16 a[192:193],v2
ds_read_b64_tr_b16 a[194:195],v2 offset:4096
v_mfma_f32_16x16x32_bf16 a[96:99],a[196:199],v[120:123],a[96:99]
ds_read_b64_tr_b16 a[196:197],v2 offset:8192
ds_read_b64_tr_b16 a[198:199],v2 offset:12288
v_mfma_f32_16x16x32_bf16 a[100:103],a[200:203],v[112:115],a[100:103]
ds_read_b64_tr_b16 a[200:201],v3
ds_read_b64_tr_b16 a[202:203],v3 offset:4096
v_mfma_f32_16x16x32_bf16 a[100:103],a[204:207],v[120:123],a[100:103]
global_load_lds_dwordx4 v4,s[10:11] offset:1024 nt
ds_read_b64_tr_b16 a[204:205],v3 offset:8192
L_pd6_%=:
s_cbranch_execz L_trt0_%=
v_mfma_f32_16x16x32_bf16 a[104:107],a[208:211],v[112:115],a[104:107]
ds_read_b64_tr_b16 a[206:207],v3 offset:12288
ds_read_b64_tr_b16 a[208:209],v2 offset:256
v_mfma_f32_16x16x32_bf16 a[104:107],a[212:215],v[120:123],a[104:107]
ds_read_b64_tr_b16 a[210:211],v2 offset:4352
ds_read_b64_tr_b16 a[212:213],v2 offset:8448
v_mfma_f32_16x16x32_bf16 a[108:111],a[216:219],v[112:115],a[108:111]
ds_read_b64_tr_b16 a[214:215],v2 offset:12544
v_cvt_pk_bf16_f32 v80,v80,v81
v_mfma_f32_16x16x32_bf16 a[108:111],a[220:223],v[120:123],a[108:111]
ds_read_b64_tr_b16 a[216:217],v3 offset:256
ds_read_b64_tr_b16 a[218:219],v3 offset:4352
v_mfma_f32_16x16x32_bf16 a[112:115],a[224:227],v[112:115],a[112:115]
v_exp_f32 v83,v83
v_mfma_f32_16x16x32_bf16 a[112:115],a[228:231],v[120:123],a[112:115]
ds_read_b64_tr_b16 a[220:221],v3 offset:8448
ds_read_b64_tr_b16 a[222:223],v3 offset:12544
v_mfma_f32_16x16x32_bf16 a[116:119],a[232:235],v[112:115],a[116:119]
ds_read_b64_tr_b16 a[224:225],v2 offset:512
ds_read_b64_tr_b16 a[226:227],v2 offset:4608
v_mfma_f32_16x16x32_bf16 a[116:119],a[236:239],v[120:123],a[116:119]
ds_read_b64_tr_b16 a[228:229],v2 offset:8704
ds_read_b64_tr_b16 a[230:231],v2 offset:12800
v_mfma_f32_16x16x32_bf16 a[120:123],a[240:243],v[112:115],a[120:123]
ds_read_b64_tr_b16 a[232:233],v3 offset:512
v_cvt_pk_bf16_f32 v81,v82,v83
v_mfma_f32_16x16x32_bf16 a[120:123],a[244:247],v[120:123],a[120:123]
ds_read_b64_tr_b16 a[234:235],v3 offset:4608
ds_read_b64_tr_b16 a[236:237],v3 offset:8704
v_mfma_f32_16x16x32_bf16 a[124:127],a[248:251],v[112:115],a[124:127]
v_exp_f32 v84,v84
v_mfma_f32_16x16x32_bf16 a[124:127],a[252:255],v[120:123],a[124:127]
ds_read_b64_tr_b16 a[238:239],v3 offset:12800
ds_read_b64_tr_b16 a[240:241],v2 offset:768
v_mfma_f32_16x16x32_bf16 v[60:63],v[28:31],v[112:115],v[60:63]
ds_read_b64_tr_b16 a[242:243],v2 offset:4864
ds_read_b64_tr_b16 a[244:245],v2 offset:8960
v_mfma_f32_16x16x32_bf16 v[60:63],v[28:31],v[120:123],v[60:63]
global_load_lds_dwordx4 v5,s[10:11] offset:2048 nt
ds_read_b64_tr_b16 a[246:247],v2 offset:13056
v_mfma_f32_16x16x32_bf16 v[96:99],a[128:131],v[196:199],v[40:43]
ds_read_b64_tr_b16 a[248:249],v3 offset:768
ds_read_b64_tr_b16 a[250:251],v3 offset:4864
v_mfma_f32_16x16x32_bf16 v[96:99],a[132:135],v[204:207],v[96:99]
ds_read_b64_tr_b16 a[252:253],v3 offset:8960
ds_read_b64_tr_b16 a[254:255],v3 offset:13056
v_mfma_f32_16x16x32_bf16 v[96:99],a[136:139],v[212:215],v[96:99]
v_exp_f32 v85,v85
v_mfma_f32_16x16x32_bf16 v[96:99],a[140:143],v[220:223],v[96:99]
v_exp_f32 v86,v86
v_mfma_f32_16x16x32_bf16 v[100:103],a[156:159],v[220:223],v[40:43]
v_exp_f32 v87,v87
v_mfma_f32_16x16x32_bf16 v[100:103],a[152:155],v[212:215],v[100:103]
v_exp_f32 v88,v88
v_mfma_f32_16x16x32_bf16 v[100:103],a[148:151],v[204:207],v[100:103]
v_exp_f32 v89,v89
v_mfma_f32_16x16x32_bf16 v[100:103],a[144:147],v[196:199],v[100:103]
v_exp_f32 v90,v90
v_mfma_f32_16x16x32_bf16 v[104:107],a[160:163],v[196:199],v[40:43]
v_exp_f32 v91,v91
v_mfma_f32_16x16x32_bf16 v[104:107],a[164:167],v[204:207],v[104:107]
v_exp_f32 v92,v92
v_mfma_f32_16x16x32_bf16 v[104:107],a[168:171],v[212:215],v[104:107]
v_exp_f32 v93,v93
v_mfma_f32_16x16x32_bf16 v[104:107],a[172:175],v[220:223],v[104:107]
v_exp_f32 v94,v94
v_mfma_f32_16x16x32_bf16 v[108:111],a[188:191],v[220:223],v[40:43]
v_exp_f32 v95,v95
v_mfma_f32_16x16x32_bf16 v[108:111],a[184:187],v[212:215],v[108:111]
v_exp_f32 v96,v96
v_mfma_f32_16x16x32_bf16 v[108:111],a[180:183],v[204:207],v[108:111]
global_load_lds_dwordx4 v5,s[10:11] offset:3072
v_cvt_pk_bf16_f32 v82,v84,v85
v_mfma_f32_16x16x32_bf16 v[108:111],a[176:179],v[196:199],v[108:111]
s_waitcnt lgkmcnt(0)
v_exp_f32 v97,v97
v_mfma_f32_16x16x32_bf16 a[0:3],a[192:195],v[64:67],a[0:3]
v_exp_f32 v98,v98
v_mfma_f32_16x16x32_bf16 a[0:3],a[196:199],v[72:75],a[0:3]
v_exp_f32 v99,v99
v_mfma_f32_16x16x32_bf16 a[4:7],a[200:203],v[64:67],a[4:7]
v_exp_f32 v100,v100
v_mfma_f32_16x16x32_bf16 a[4:7],a[204:207],v[72:75],a[4:7]
v_exp_f32 v101,v101
v_mfma_f32_16x16x32_bf16 a[8:11],a[208:211],v[64:67],a[8:11]
v_exp_f32 v102,v102
v_mfma_f32_16x16x32_bf16 a[8:11],a[212:215],v[72:75],a[8:11]
v_exp_f32 v103,v103
v_mfma_f32_16x16x32_bf16 a[12:15],a[216:219],v[64:67],a[12:15]
v_exp_f32 v104,v104
v_mfma_f32_16x16x32_bf16 a[12:15],a[220:223],v[72:75],a[12:15]
v_exp_f32 v105,v105
v_mfma_f32_16x16x32_bf16 a[16:19],a[224:227],v[64:67],a[16:19]
v_exp_f32 v106,v106
v_mfma_f32_16x16x32_bf16 a[16:19],a[228:231],v[72:75],a[16:19]
v_exp_f32 v107,v107
v_mfma_f32_16x16x32_bf16 a[20:23],a[232:235],v[64:67],a[20:23]
s_getreg_b32 s92,hwreg(HW_REG_IB_STS,0,4)
s_min_u32 s4,s92,s6
s_mov_b32 s6,s92
v_mfma_f32_16x16x32_bf16 a[20:23],a[236:239],v[72:75],a[20:23]
s_cmp_gt_u32 s4,s9
s_cselect_b32 s4,15,s5
v_cvt_pk_bf16_f32 v83,v86,v87
v_mfma_f32_16x16x32_bf16 a[24:27],a[240:243],v[64:67],a[24:27]
ds_write_b128 v7,v[24:27] offset:3072
v_mfma_f32_16x16x32_bf16 a[24:27],a[244:247],v[72:75],a[24:27]
v_cvt_pk_bf16_f32 v88,v88,v89
v_cvt_pk_bf16_f32 v89,v90,v91
v_mfma_f32_16x16x32_bf16 a[28:31],a[248:251],v[64:67],a[28:31]
v_cvt_pk_bf16_f32 v90,v92,v93
v_cvt_pk_bf16_f32 v91,v94,v95
v_mfma_f32_16x16x32_bf16 a[28:31],a[252:255],v[72:75],a[28:31]
s_getpc_b64 s[92:93]
s_cmp_lg_u32 s4,0
s_cbranch_scc1 L_pace_%=
v_mfma_f32_16x16x32_bf16 v[48:51],v[28:31],v[64:67],v[48:51]
)A"
R"A(v_cvt_pk_bf16_f32 v96,v96,v97
v_cvt_pk_bf16_f32 v97,v98,v99
v_mfma_f32_16x16x32_bf16 v[48:51],v[28:31],v[72:75],v[48:51]
v_cvt_pk_bf16_f32 v98,v100,v101
v_cvt_pk_bf16_f32 v99,v102,v103
v_cvt_pk_bf16_f32 v104,v104,v105
v_cvt_pk_bf16_f32 v105,v106,v107
v_exp_f32 v108,v108
v_exp_f32 v109,v109
s_mov_b32 s70,12
L_a0_%=:
s_waitcnt vmcnt(12)
s_barrier
v_mfma_f32_16x16x32_bf16 v[112:115],a[128:131],v[228:231],v[44:47]
s_add_u32 s10,s24,s74
s_addc_u32 s11,s25,0
s_add_u32 m0,s76,16384
v_mfma_f32_16x16x32_bf16 v[112:115],a[132:135],v[236:239],v[112:115]
s_add_u32 s75,s75,16384
ds_read_b128 a[128:131],v1
v_cvt_pk_bf16_f32 v106,v108,v109
v_mfma_f32_16x16x32_bf16 v[112:115],a[136:139],v[244:247],v[112:115]
s_add_u32 s74,s74,16384
global_load_lds_dwordx4 v4,s[10:11]
v_mfma_f32_16x16x32_bf16 v[112:115],a[140:143],v[252:255],v[112:115]
s_and_b32 s74,s74,0x3ffff
s_and_b32 s69,s75,0xfffc0000
ds_read_b128 a[132:135],v1 offset:256
v_mfma_f32_16x16x32_bf16 v[116:119],a[156:159],v[252:255],v[44:47]
s_or_b32 s74,s74,s69
v_exp_f32 v110,v110
v_mfma_f32_16x16x32_bf16 v[116:119],a[152:155],v[244:247],v[116:119]
s_sub_u32 s76,s76,32768
s_and_b32 s76,s76,0x1ffff
ds_read_b128 a[136:139],v1 offset:512
v_mfma_f32_16x16x32_bf16 v[116:119],a[148:151],v[236:239],v[116:119]
v_exp_f32 v111,v111
v_mfma_f32_16x16x32_bf16 v[116:119],a[144:147],v[228:231],v[116:119]
ds_read_b128 a[140:143],v1 offset:768
v_cvt_pk_bf16_f32 v107,v110,v111
v_mfma_f32_16x16x32_bf16 v[120:123],a[160:163],v[228:231],v[44:47]
v_exp_f32 v112,v112
v_mfma_f32_16x16x32_bf16 v[120:123],a[164:167],v[236:239],v[120:123]
ds_read_b128 a[156:159],v1 offset:4864
v_add_u32 v2,s78,v2
v_mfma_f32_16x16x32_bf16 v[120:123],a[168:171],v[244:247],v[120:123]
v_exp_f32 v113,v113
v_mfma_f32_16x16x32_bf16 v[120:123],a[172:175],v[252:255],v[120:123]
ds_read_b128 a[152:155],v1 offset:4608
v_cvt_pk_bf16_f32 v112,v112,v113
v_mfma_f32_16x16x32_bf16 v[124:127],a[188:191],v[252:255],v[44:47]
v_exp_f32 v114,v114
v_mfma_f32_16x16x32_bf16 v[124:127],a[184:187],v[244:247],v[124:127]
ds_read_b128 a[148:151],v1 offset:4352
v_add_u32 v3,s78,v3
v_mfma_f32_16x16x32_bf16 v[124:127],a[180:183],v[236:239],v[124:127]
s_sub_u32 s78,s77,32768
v_exp_f32 v115,v115
v_mfma_f32_16x16x32_bf16 v[124:127],a[176:179],v[228:231],v[124:127]
s_and_b32 s78,s78,0x1ffff
ds_read_b128 a[144:147],v1 offset:4096
v_cvt_pk_bf16_f32 v113,v114,v115
v_mfma_f32_16x16x32_bf16 a[32:35],a[192:195],v[80:83],a[32:35]
s_sub_u32 s78,s78,s77
v_exp_f32 v116,v116
v_mfma_f32_16x16x32_bf16 a[32:35],a[196:199],v[88:91],a[32:35]
s_add_u32 s77,s77,s78
v_exp_f32 v117,v117
v_mfma_f32_16x16x32_bf16 a[36:39],a[200:203],v[80:83],a[36:39]
global_load_lds_dwordx4 v5,s[10:11] offset:1024
ds_read_b128 a[160:163],v1 offset:8192
v_mfma_f32_16x16x32_bf16 a[36:39],a[204:207],v[88:91],a[36:39]
v_exp_f32 v118,v118
v_mfma_f32_16x16x32_bf16 a[40:43],a[208:211],v[80:83],a[40:43]
ds_read_b128 a[164:167],v1 offset:8448
v_cvt_pk_bf16_f32 v114,v116,v117
v_mfma_f32_16x16x32_bf16 a[40:43],a[212:215],v[88:91],a[40:43]
v_exp_f32 v119,v119
v_mfma_f32_16x16x32_bf16 a[44:47],a[216:219],v[80:83],a[44:47]
ds_read_b128 a[168:171],v1 offset:8704
v_cvt_pk_bf16_f32 v115,v118,v119
v_mfma_f32_16x16x32_bf16 a[44:47],a[220:223],v[88:91],a[44:47]
v_exp_f32 v120,v120
v_mfma_f32_16x16x32_bf16 a[48:51],a[224:227],v[80:83],a[48:51]
v_exp_f32 v121,v121
v_mfma_f32_16x16x32_bf16 a[48:51],a[228:231],v[88:91],a[48:51]
ds_read_b128 a[172:175],v1 offset:8960
v_cvt_pk_bf16_f32 v120,v120,v121
v_mfma_f32_16x16x32_bf16 a[52:55],a[232:235],v[80:83],a[52:55]
v_exp_f32 v122,v122
v_mfma_f32_16x16x32_bf16 a[52:55],a[236:239],v[88:91],a[52:55]
v_exp_f32 v123,v123
v_mfma_f32_16x16x32_bf16 a[56:59],a[240:243],v[80:83],a[56:59]
ds_read_b128 a[188:191],v1 offset:13056
v_cvt_pk_bf16_f32 v121,v122,v123
v_mfma_f32_16x16x32_bf16 a[56:59],a[244:247],v[88:91],a[56:59]
v_exp_f32 v124,v124
v_mfma_f32_16x16x32_bf16 a[60:63],a[248:251],v[80:83],a[60:63]
v_exp_f32 v125,v125
v_mfma_f32_16x16x32_bf16 a[60:63],a[252:255],v[88:91],a[60:63]
ds_read_b128 a[184:187],v1 offset:12800
v_cvt_pk_bf16_f32 v122,v124,v125
v_mfma_f32_16x16x32_bf16 v[52:55],v[28:31],v[80:83],v[52:55]
v_exp_f32 v126,v126
v_mfma_f32_16x16x32_bf16 v[52:55],v[28:31],v[88:91],v[52:55]
s_waitcnt lgkmcnt(2)
v_exp_f32 v127,v127
v_mfma_f32_16x16x32_bf16 v[64:67],a[128:131],v[132:135],v[32:35]
global_load_lds_dwordx4 v4,s[10:11] offset:2048
ds_read_b128 a[180:183],v1 offset:12544
v_mfma_f32_16x16x32_bf16 v[64:67],a[132:135],v[140:143],v[64:67]
v_cvt_pk_bf16_f32 v123,v126,v127
v_mfma_f32_16x16x32_bf16 v[64:67],a[136:139],v[148:151],v[64:67]
ds_read_b128 a[176:179],v1 offset:12288
v_add_u32 v1,s78,v1
v_mfma_f32_16x16x32_bf16 v[64:67],a[140:143],v[156:159],v[64:67]
v_mfma_f32_16x16x32_bf16 v[68:71],a[156:159],v[156:159],v[32:35]
v_mfma_f32_16x16x32_bf16 v[68:71],a[152:155],v[148:151],v[68:71]
v_mfma_f32_16x16x32_bf16 v[68:71],a[148:151],v[140:143],v[68:71]
v_mfma_f32_16x16x32_bf16 v[68:71],a[144:147],v[132:135],v[68:71]
v_mfma_f32_16x16x32_bf16 v[72:75],a[160:163],v[132:135],v[32:35]
v_mfma_f32_16x16x32_bf16 v[72:75],a[164:167],v[140:143],v[72:75]
v_mfma_f32_16x16x32_bf16 v[72:75],a[168:171],v[148:151],v[72:75]
v_mfma_f32_16x16x32_bf16 v[72:75],a[172:175],v[156:159],v[72:75]
s_waitcnt lgkmcnt(0)
v_exp_f32 v64,v64
v_mfma_f32_16x16x32_bf16 v[76:79],a[188:191],v[156:159],v[32:35]
v_exp_f32 v65,v65
v_mfma_f32_16x16x32_bf16 v[76:79],a[184:187],v[148:151],v[76:79]
v_exp_f32 v66,v66
v_mfma_f32_16x16x32_bf16 v[76:79],a[180:183],v[140:143],v[76:79]
v_exp_f32 v67,v67
v_mfma_f32_16x16x32_bf16 v[76:79],a[176:179],v[132:135],v[76:79]
v_exp_f32 v68,v68
v_mfma_f32_16x16x32_bf16 a[64:67],a[192:195],v[96:99],a[64:67]
global_load_lds_dwordx4 v5,s[10:11] offset:3072
v_cvt_pk_bf16_f32 v64,v64,v65
v_mfma_f32_16x16x32_bf16 a[64:67],a[196:199],v[104:107],a[64:67]
v_exp_f32 v69,v69
v_mfma_f32_16x16x32_bf16 a[68:71],a[200:203],v[96:99],a[68:71]
v_exp_f32 v70,v70
v_mfma_f32_16x16x32_bf16 a[68:71],a[204:207],v[104:107],a[68:71]
v_exp_f32 v71,v71
v_mfma_f32_16x16x32_bf16 a[72:75],a[208:211],v[96:99],a[72:75]
v_cvt_pk_bf16_f32 v65,v66,v67
v_cvt_pk_bf16_f32 v66,v68,v69
v_mfma_f32_16x16x32_bf16 a[72:75],a[212:215],v[104:107],a[72:75]
v_exp_f32 v72,v72
v_mfma_f32_16x16x32_bf16 a[76:79],a[216:219],v[96:99],a[76:79]
v_exp_f32 v73,v73
v_mfma_f32_16x16x32_bf16 a[76:79],a[220:223],v[104:107],a[76:79]
v_cvt_pk_bf16_f32 v67,v70,v71
v_cvt_pk_bf16_f32 v72,v72,v73
v_mfma_f32_16x16x32_bf16 a[80:83],a[224:227],v[96:99],a[80:83]
v_exp_f32 v74,v74
v_mfma_f32_16x16x32_bf16 a[80:83],a[228:231],v[104:107],a[80:83]
v_exp_f32 v75,v75
v_mfma_f32_16x16x32_bf16 a[84:87],a[232:235],v[96:99],a[84:87]
v_exp_f32 v76,v76
v_mfma_f32_16x16x32_bf16 a[84:87],a[236:239],v[104:107],a[84:87]
v_exp_f32 v77,v77
v_mfma_f32_16x16x32_bf16 a[88:91],a[240:243],v[96:99],a[88:91]
s_add_u32 s10,s20,s74
s_addc_u32 s11,s21,0
v_cvt_pk_bf16_f32 v73,v74,v75
v_mfma_f32_16x16x32_bf16 a[88:91],a[244:247],v[104:107],a[88:91]
v_exp_f32 v78,v78
v_mfma_f32_16x16x32_bf16 a[92:95],a[248:251],v[96:99],a[92:95]
s_add_u32 m0,s76,0
v_exp_f32 v79,v79
v_mfma_f32_16x16x32_bf16 a[92:95],a[252:255],v[104:107],a[92:95]
v_cvt_pk_bf16_f32 v74,v76,v77
v_cvt_pk_bf16_f32 v75,v78,v79
v_mfma_f32_16x16x32_bf16 v[56:59],v[28:31],v[96:99],v[56:59]
global_load_lds_dwordx4 v4,s[10:11] nt
v_mfma_f32_16x16x32_bf16 v[56:59],v[28:31],v[104:107],v[56:59]
v_mfma_f32_16x16x32_bf16 v[80:83],a[128:131],v[164:167],v[36:39]
v_mfma_f32_16x16x32_bf16 v[80:83],a[132:135],v[172:175],v[80:83]
v_mfma_f32_16x16x32_bf16 v[80:83],a[136:139],v[180:183],v[80:83]
v_mfma_f32_16x16x32_bf16 v[80:83],a[140:143],v[188:191],v[80:83]
v_mfma_f32_16x16x32_bf16 v[84:87],a[156:159],v[188:191],v[36:39]
v_mfma_f32_16x16x32_bf16 v[84:87],a[152:155],v[180:183],v[84:87]
v_mfma_f32_16x16x32_bf16 v[84:87],a[148:151],v[172:175],v[84:87]
v_mfma_f32_16x16x32_bf16 v[84:87],a[144:147],v[164:167],v[84:87]
v_mfma_f32_16x16x32_bf16 v[88:91],a[160:163],v[164:167],v[36:39]
v_mfma_f32_16x16x32_bf16 v[88:91],a[164:167],v[172:175],v[88:91]
v_mfma_f32_16x16x32_bf16 v[88:91],a[168:171],v[180:183],v[88:91]
v_mfma_f32_16x16x32_bf16 v[88:91],a[172:175],v[188:191],v[88:91]
v_exp_f32 v80,v80
v_mfma_f32_16x16x32_bf16 v[92:95],a[188:191],v[188:191],v[36:39]
v_exp_f32 v81,v81
v_mfma_f32_16x16x32_bf16 v[92:95],a[184:187],v[180:183],v[92:95]
v_exp_f32 v82,v82
v_mfma_f32_16x16x32_bf16 v[92:95],a[180:183],v[172:175],v[92:95]
global_load_lds_dwordx4 v4,s[10:11] offset:1024 nt
v_cvt_pk_bf16_f32 v80,v80,v81
v_mfma_f32_16x16x32_bf16 v[92:95],a[176:179],v[164:167],v[92:95]
v_exp_f32 v83,v83
v_mfma_f32_16x16x32_bf16 a[96:99],a[192:195],v[112:115],a[96:99]
ds_read_b64_tr_b16 a[192:193],v2
ds_read_b64_tr_b16 a[194:195],v2 offset:4096
v_mfma_f32_16x16x32_bf16 a[96:99],a[196:199],v[120:123],a[96:99]
ds_read_b64_tr_b16 a[196:197],v2 offset:8192
v_cvt_pk_bf16_f32 v81,v82,v83
v_mfma_f32_16x16x32_bf16 a[100:103],a[200:203],v[112:115],a[100:103]
v_exp_f32 v84,v84
v_mfma_f32_16x16x32_bf16 a[100:103],a[204:207],v[120:123],a[100:103]
ds_read_b64_tr_b16 a[198:199],v2 offset:12288
ds_read_b64_tr_b16 a[200:201],v3
v_mfma_f32_16x16x32_bf16 a[104:107],a[208:211],v[112:115],a[104:107]
v_exp_f32 v85,v85
v_mfma_f32_16x16x32_bf16 a[104:107],a[212:215],v[120:123],a[104:107]
ds_read_b64_tr_b16 a[202:203],v3 offset:4096
ds_read_b64_tr_b16 a[204:205],v3 offset:8192
v_mfma_f32_16x16x32_bf16 a[108:111],a[216:219],v[112:115],a[108:111]
ds_read_b64_tr_b16 a[206:207],v3 offset:12288
v_cvt_pk_bf16_f32 v82,v84,v85
v_mfma_f32_16x16x32_bf16 a[108:111],a[220:223],v[120:123],a[108:111]
v_exp_f32 v86,v86
v_mfma_f32_16x16x32_bf16 a[112:115],a[224:227],v[112:115],a[112:115]
ds_read_b64_tr_b16 a[208:209],v2 offset:256
ds_read_b64_tr_b16 a[210:211],v2 offset:4352
v_mfma_f32_16x16x32_bf16 a[112:115],a[228:231],v[120:123],a[112:115]
v_exp_f32 v87,v87
v_mfma_f32_16x16x32_bf16 a[116:119],a[232:235],v[112:115],a[116:119]
ds_read_b64_tr_b16 a[212:213],v2 offset:8448
ds_read_b64_tr_b16 a[214:215],v2 offset:12544
v_mfma_f32_16x16x32_bf16 a[116:119],a[236:239],v[120:123],a[116:119]
ds_read_b64_tr_b16 a[216:217],v3 offset:256
v_cvt_pk_bf16_f32 v83,v86,v87
v_mfma_f32_16x16x32_bf16 a[120:123],a[240:243],v[112:115],a[120:123]
v_exp_f32 v88,v88
v_mfma_f32_16x16x32_bf16 a[120:123],a[244:247],v[120:123],a[120:123]
ds_read_b64_tr_b16 a[218:219],v3 offset:4352
ds_read_b64_tr_b16 a[220:221],v3 offset:8448
v_mfma_f32_16x16x32_bf16 a[124:127],a[248:251],v[112:115],a[124:127]
global_load_lds_dwordx4 v5,s[10:11] offset:2048 nt
ds_read_b64_tr_b16 a[222:223],v3 offset:12544
v_mfma_f32_16x16x32_bf16 a[124:127],a[252:255],v[120:123],a[124:127]
v_exp_f32 v89,v89
v_mfma_f32_16x16x32_bf16 v[60:63],v[28:31],v[112:115],v[60:63]
ds_read_b64_tr_b16 a[224:225],v2 offset:512
ds_read_b64_tr_b16 a[226:227],v2 offset:4608
v_mfma_f32_16x16x32_bf16 v[60:63],v[28:31],v[120:123],v[60:63]
ds_read_b64_tr_b16 a[228:229],v2 offset:8704
v_cvt_pk_bf16_f32 v88,v88,v89
v_mfma_f32_16x16x32_bf16 v[96:99],a[128:131],v[196:199],v[40:43]
v_exp_f32 v90,v90
v_mfma_f32_16x16x32_bf16 v[96:99],a[132:135],v[204:207],v[96:99]
ds_read_b64_tr_b16 a[230:231],v2 offset:12800
ds_read_b64_tr_b16 a[232:233],v3 offset:512
v_mfma_f32_16x16x32_bf16 v[96:99],a[136:139],v[212:215],v[96:99]
v_exp_f32 v91,v91
v_mfma_f32_16x16x32_bf16 v[96:99],a[140:143],v[220:223],v[96:99]
ds_read_b64_tr_b16 a[234:235],v3 offset:4608
ds_read_b64_tr_b16 a[236:237],v3 offset:8704
v_mfma_f32_16x16x32_bf16 v[100:103],a[156:159],v[220:223],v[40:43]
ds_read_b64_tr_b16 a[238:239],v3 offset:12800
v_cvt_pk_bf16_f32 v89,v90,v91
v_mfma_f32_16x16x32_bf16 v[100:103],a[152:155],v[212:215],v[100:103]
v_exp_f32 v92,v92
v_mfma_f32_16x16x32_bf16 v[100:103],a[148:151],v[204:207],v[100:103]
ds_read_b64_tr_b16 a[240:241],v2 offset:768
ds_read_b64_tr_b16 a[242:243],v2 offset:4864
v_mfma_f32_16x16x32_bf16 v[100:103],a[144:147],v[196:199],v[100:103]
v_exp_f32 v93,v93
v_mfma_f32_16x16x32_bf16 v[104:107],a[160:163],v[196:199],v[40:43]
ds_read_b64_tr_b16 a[244:245],v2 offset:8960
ds_read_b64_tr_b16 a[246:247],v2 offset:13056
v_mfma_f32_16x16x32_bf16 v[104:107],a[164:167],v[204:207],v[104:107]
)A"
R"A(ds_read_b64_tr_b16 a[248:249],v3 offset:768
v_cvt_pk_bf16_f32 v90,v92,v93
v_mfma_f32_16x16x32_bf16 v[104:107],a[168:171],v[212:215],v[104:107]
v_exp_f32 v94,v94
v_mfma_f32_16x16x32_bf16 v[104:107],a[172:175],v[220:223],v[104:107]
ds_read_b64_tr_b16 a[250:251],v3 offset:4864
ds_read_b64_tr_b16 a[252:253],v3 offset:8960
v_mfma_f32_16x16x32_bf16 v[108:111],a[188:191],v[220:223],v[40:43]
global_load_lds_dwordx4 v5,s[10:11] offset:3072
ds_read_b64_tr_b16 a[254:255],v3 offset:13056
v_mfma_f32_16x16x32_bf16 v[108:111],a[184:187],v[212:215],v[108:111]
v_exp_f32 v95,v95
v_mfma_f32_16x16x32_bf16 v[108:111],a[180:183],v[204:207],v[108:111]
v_exp_f32 v96,v96
v_mfma_f32_16x16x32_bf16 v[108:111],a[176:179],v[196:199],v[108:111]
s_waitcnt lgkmcnt(6)
v_exp_f32 v97,v97
v_mfma_f32_16x16x32_bf16 a[0:3],a[192:195],v[64:67],a[0:3]
v_exp_f32 v98,v98
v_mfma_f32_16x16x32_bf16 a[0:3],a[196:199],v[72:75],a[0:3]
v_exp_f32 v99,v99
v_mfma_f32_16x16x32_bf16 a[4:7],a[200:203],v[64:67],a[4:7]
v_exp_f32 v100,v100
v_mfma_f32_16x16x32_bf16 a[4:7],a[204:207],v[72:75],a[4:7]
v_exp_f32 v101,v101
v_mfma_f32_16x16x32_bf16 a[8:11],a[208:211],v[64:67],a[8:11]
v_exp_f32 v102,v102
v_mfma_f32_16x16x32_bf16 a[8:11],a[212:215],v[72:75],a[8:11]
v_exp_f32 v103,v103
v_mfma_f32_16x16x32_bf16 a[12:15],a[216:219],v[64:67],a[12:15]
v_exp_f32 v104,v104
v_mfma_f32_16x16x32_bf16 a[12:15],a[220:223],v[72:75],a[12:15]
v_exp_f32 v105,v105
v_mfma_f32_16x16x32_bf16 a[16:19],a[224:227],v[64:67],a[16:19]
v_exp_f32 v106,v106
v_mfma_f32_16x16x32_bf16 a[16:19],a[228:231],v[72:75],a[16:19]
v_exp_f32 v107,v107
v_mfma_f32_16x16x32_bf16 a[20:23],a[232:235],v[64:67],a[20:23]
s_getreg_b32 s92,hwreg(HW_REG_IB_STS,0,4)
s_min_u32 s4,s92,s6
s_mov_b32 s6,s92
v_mfma_f32_16x16x32_bf16 a[20:23],a[236:239],v[72:75],a[20:23]
s_cmp_ge_u32 s4,s9
s_cselect_b32 s4,15,s5
v_cvt_pk_bf16_f32 v91,v94,v95
v_mfma_f32_16x16x32_bf16 a[24:27],a[240:243],v[64:67],a[24:27]
s_waitcnt lgkmcnt(0)
v_cvt_pk_bf16_f32 v96,v96,v97
v_cvt_pk_bf16_f32 v97,v98,v99
v_mfma_f32_16x16x32_bf16 a[24:27],a[244:247],v[72:75],a[24:27]
v_cvt_pk_bf16_f32 v98,v100,v101
v_cvt_pk_bf16_f32 v99,v102,v103
v_mfma_f32_16x16x32_bf16 a[28:31],a[248:251],v[64:67],a[28:31]
v_cvt_pk_bf16_f32 v104,v104,v105
v_cvt_pk_bf16_f32 v105,v106,v107
v_mfma_f32_16x16x32_bf16 a[28:31],a[252:255],v[72:75],a[28:31]
s_getpc_b64 s[92:93]
s_cmp_lg_u32 s4,0
s_cbranch_scc1 L_pace_%=
v_mfma_f32_16x16x32_bf16 v[48:51],v[28:31],v[64:67],v[48:51]
v_exp_f32 v108,v108
v_mfma_f32_16x16x32_bf16 v[48:51],v[28:31],v[72:75],v[48:51]
v_exp_f32 v109,v109
s_sub_u32 s70,s70,1
s_cmp_lg_u32 s70,0
s_cbranch_scc1 L_a0_%=
s_waitcnt vmcnt(12)
s_barrier
v_mfma_f32_16x16x32_bf16 v[112:115],a[128:131],v[228:231],v[44:47]
s_add_u32 s10,s24,s74
s_addc_u32 s11,s25,0
s_add_u32 m0,s76,16384
v_mfma_f32_16x16x32_bf16 v[112:115],a[132:135],v[236:239],v[112:115]
s_add_u32 s75,s75,16384
ds_read_b128 v[32:35],v7
v_cvt_pk_bf16_f32 v106,v108,v109
v_mfma_f32_16x16x32_bf16 v[112:115],a[136:139],v[244:247],v[112:115]
s_add_u32 s74,s74,16384
global_load_lds_dwordx4 v4,s[10:11]
v_mfma_f32_16x16x32_bf16 v[112:115],a[140:143],v[252:255],v[112:115]
s_and_b32 s74,s74,0x3ffff
s_and_b32 s69,s75,0xfffc0000
ds_read_b128 a[128:131],v1
v_mfma_f32_16x16x32_bf16 v[116:119],a[156:159],v[252:255],v[44:47]
s_or_b32 s74,s74,s69
v_exp_f32 v110,v110
v_mfma_f32_16x16x32_bf16 v[116:119],a[152:155],v[244:247],v[116:119]
s_sub_u32 s76,s76,32768
s_and_b32 s76,s76,0x1ffff
ds_read_b128 a[132:135],v1 offset:256
v_mfma_f32_16x16x32_bf16 v[116:119],a[148:151],v[236:239],v[116:119]
s_min_u32 s7,s7,0
v_exp_f32 v111,v111
v_mfma_f32_16x16x32_bf16 v[116:119],a[144:147],v[228:231],v[116:119]
s_min_u32 s4,s5,0
ds_read_b128 a[136:139],v1 offset:512
v_cvt_pk_bf16_f32 v107,v110,v111
v_mfma_f32_16x16x32_bf16 v[120:123],a[160:163],v[228:231],v[44:47]
v_exp_f32 v112,v112
v_mfma_f32_16x16x32_bf16 v[120:123],a[164:167],v[236:239],v[120:123]
ds_read_b128 a[140:143],v1 offset:768
v_add_u32 v2,s78,v2
v_mfma_f32_16x16x32_bf16 v[120:123],a[168:171],v[244:247],v[120:123]
v_exp_f32 v113,v113
v_mfma_f32_16x16x32_bf16 v[120:123],a[172:175],v[252:255],v[120:123]
ds_read_b128 a[156:159],v1 offset:4864
v_cvt_pk_bf16_f32 v112,v112,v113
v_mfma_f32_16x16x32_bf16 v[124:127],a[188:191],v[252:255],v[44:47]
v_exp_f32 v114,v114
v_mfma_f32_16x16x32_bf16 v[124:127],a[184:187],v[244:247],v[124:127]
ds_read_b128 a[152:155],v1 offset:4608
v_add_u32 v3,s78,v3
v_mfma_f32_16x16x32_bf16 v[124:127],a[180:183],v[236:239],v[124:127]
s_sub_u32 s78,s77,32768
v_exp_f32 v115,v115
v_mfma_f32_16x16x32_bf16 v[124:127],a[176:179],v[228:231],v[124:127]
s_and_b32 s78,s78,0x1ffff
ds_read_b128 a[148:151],v1 offset:4352
v_cvt_pk_bf16_f32 v113,v114,v115
v_mfma_f32_16x16x32_bf16 v[52:55],v[28:31],v[80:83],v[52:55]
global_load_lds_dwordx4 v5,s[10:11] offset:1024
v_accvgpr_read_b32 v8,a0
v_mfma_f32_16x16x32_bf16 v[52:55],v[28:31],v[88:91],v[52:55]
s_sub_u32 s78,s78,s77
ds_read_b128 a[144:147],v1 offset:4096
v_accvgpr_read_b32 v9,a1
v_mfma_f32_16x16x32_bf16 a[32:35],a[192:195],v[80:83],a[32:35]
s_add_u32 s77,s77,s78
v_exp_f32 v116,v116
v_mfma_f32_16x16x32_bf16 a[32:35],a[196:199],v[88:91],a[32:35]
ds_read_b128 a[160:163],v1 offset:8192
v_accvgpr_read_b32 v10,a2
v_mfma_f32_16x16x32_bf16 a[36:39],a[200:203],v[80:83],a[36:39]
v_exp_f32 v117,v117
v_mfma_f32_16x16x32_bf16 a[36:39],a[204:207],v[88:91],a[36:39]
ds_read_b128 a[164:167],v1 offset:8448
v_cvt_pk_bf16_f32 v114,v116,v117
v_mfma_f32_16x16x32_bf16 a[40:43],a[208:211],v[80:83],a[40:43]
v_exp_f32 v118,v118
v_mfma_f32_16x16x32_bf16 a[40:43],a[212:215],v[88:91],a[40:43]
ds_read_b128 a[168:171],v1 offset:8704
v_accvgpr_read_b32 v11,a3
v_mfma_f32_16x16x32_bf16 a[44:47],a[216:219],v[80:83],a[44:47]
v_exp_f32 v119,v119
v_mfma_f32_16x16x32_bf16 a[44:47],a[220:223],v[88:91],a[44:47]
ds_read_b128 a[172:175],v1 offset:8960
v_cvt_pk_bf16_f32 v115,v118,v119
v_mfma_f32_16x16x32_bf16 a[48:51],a[224:227],v[80:83],a[48:51]
v_exp_f32 v120,v120
v_mfma_f32_16x16x32_bf16 a[48:51],a[228:231],v[88:91],a[48:51]
ds_read_b128 a[188:191],v1 offset:13056
v_accvgpr_read_b32 v12,a4
v_mfma_f32_16x16x32_bf16 a[52:55],a[232:235],v[80:83],a[52:55]
v_exp_f32 v121,v121
v_mfma_f32_16x16x32_bf16 a[52:55],a[236:239],v[88:91],a[52:55]
ds_read_b128 a[184:187],v1 offset:12800
v_cvt_pk_bf16_f32 v120,v120,v121
v_mfma_f32_16x16x32_bf16 a[56:59],a[240:243],v[80:83],a[56:59]
global_load_lds_dwordx4 v4,s[10:11] offset:2048
v_accvgpr_read_b32 v13,a5
v_mfma_f32_16x16x32_bf16 a[56:59],a[244:247],v[88:91],a[56:59]
ds_read_b128 a[180:183],v1 offset:12544
v_accvgpr_read_b32 v14,a6
v_mfma_f32_16x16x32_bf16 a[60:63],a[248:251],v[80:83],a[60:63]
v_exp_f32 v122,v122
v_mfma_f32_16x16x32_bf16 a[60:63],a[252:255],v[88:91],a[60:63]
s_waitcnt lgkmcnt(3)
ds_read_b128 a[176:179],v1 offset:12288
v_accvgpr_read_b32 v15,a7
v_mfma_f32_16x16x32_bf16 v[64:67],a[128:131],v[128:131],v[32:35]
v_exp_f32 v123,v123
v_mfma_f32_16x16x32_bf16 v[64:67],a[132:135],v[136:139],v[64:67]
ds_read_b128 v[36:39],v7 offset:1024
v_cvt_pk_bf16_f32 v121,v122,v123
v_mfma_f32_16x16x32_bf16 v[64:67],a[136:139],v[144:147],v[64:67]
v_exp_f32 v124,v124
v_mfma_f32_16x16x32_bf16 v[64:67],a[140:143],v[152:155],v[64:67]
v_exp_f32 v125,v125
v_mfma_f32_16x16x32_bf16 v[68:71],a[156:159],v[152:155],v[32:35]
v_exp_f32 v126,v126
v_mfma_f32_16x16x32_bf16 v[68:71],a[152:155],v[144:147],v[68:71]
v_exp_f32 v127,v127
v_mfma_f32_16x16x32_bf16 v[68:71],a[148:151],v[136:139],v[68:71]
v_cvt_pk_bf16_f32 v122,v124,v125
v_cvt_pk_bf16_f32 v123,v126,v127
v_mfma_f32_16x16x32_bf16 v[68:71],a[144:147],v[128:131],v[68:71]
ds_read_b128 v[40:43],v7 offset:2048
v_max_f32 v24,0x00800000,v48
v_mfma_f32_16x16x32_bf16 v[72:75],a[160:163],v[128:131],v[32:35]
v_exp_f32 v64,v64
v_mfma_f32_16x16x32_bf16 v[72:75],a[164:167],v[136:139],v[72:75]
v_exp_f32 v65,v65
v_mfma_f32_16x16x32_bf16 v[72:75],a[168:171],v[144:147],v[72:75]
s_waitcnt lgkmcnt(1)
v_exp_f32 v66,v66
v_mfma_f32_16x16x32_bf16 v[72:75],a[172:175],v[152:155],v[72:75]
global_load_lds_dwordx4 v5,s[10:11] offset:3072
v_cvt_pk_bf16_f32 v64,v64,v65
v_mfma_f32_16x16x32_bf16 v[76:79],a[188:191],v[152:155],v[32:35]
v_exp_f32 v67,v67
v_mfma_f32_16x16x32_bf16 v[76:79],a[184:187],v[144:147],v[76:79]
v_exp_f32 v68,v68
v_mfma_f32_16x16x32_bf16 v[76:79],a[180:183],v[136:139],v[76:79]
v_exp_f32 v69,v69
v_mfma_f32_16x16x32_bf16 v[76:79],a[176:179],v[128:131],v[76:79]
v_exp_f32 v70,v70
v_mfma_f32_16x16x32_bf16 v[56:59],v[28:31],v[96:99],v[56:59]
v_exp_f32 v71,v71
v_mfma_f32_16x16x32_bf16 v[56:59],v[28:31],v[104:107],v[56:59]
s_nop 0
L_pd7_%=:
s_cbranch_execz L_trt0_%=
v_cvt_pk_bf16_f32 v65,v66,v67
v_cvt_pk_bf16_f32 v66,v68,v69
v_mfma_f32_16x16x32_bf16 a[64:67],a[192:195],v[96:99],a[64:67]
v_cvt_pk_bf16_f32 v67,v70,v71
v_accvgpr_read_b32 v16,a8
v_mfma_f32_16x16x32_bf16 a[64:67],a[196:199],v[104:107],a[64:67]
v_exp_f32 v72,v72
v_mfma_f32_16x16x32_bf16 a[68:71],a[200:203],v[96:99],a[68:71]
v_exp_f32 v73,v73
v_mfma_f32_16x16x32_bf16 a[68:71],a[204:207],v[104:107],a[68:71]
s_add_u32 s10,s20,s74
s_addc_u32 s11,s21,0
v_cvt_pk_bf16_f32 v72,v72,v73
v_mfma_f32_16x16x32_bf16 a[72:75],a[208:211],v[96:99],a[72:75]
v_exp_f32 v74,v74
v_mfma_f32_16x16x32_bf16 a[72:75],a[212:215],v[104:107],a[72:75]
s_add_u32 m0,s76,0
v_exp_f32 v75,v75
v_mfma_f32_16x16x32_bf16 a[76:79],a[216:219],v[96:99],a[76:79]
v_exp_f32 v76,v76
v_mfma_f32_16x16x32_bf16 a[76:79],a[220:223],v[104:107],a[76:79]
global_load_lds_dwordx4 v4,s[10:11] nt
v_cvt_pk_bf16_f32 v73,v74,v75
v_mfma_f32_16x16x32_bf16 a[80:83],a[224:227],v[96:99],a[80:83]
v_exp_f32 v77,v77
v_mfma_f32_16x16x32_bf16 a[80:83],a[228:231],v[104:107],a[80:83]
v_exp_f32 v78,v78
v_mfma_f32_16x16x32_bf16 a[84:87],a[232:235],v[96:99],a[84:87]
v_exp_f32 v79,v79
v_mfma_f32_16x16x32_bf16 a[84:87],a[236:239],v[104:107],a[84:87]
v_cvt_pk_bf16_f32 v74,v76,v77
v_cvt_pk_bf16_f32 v75,v78,v79
v_mfma_f32_16x16x32_bf16 a[88:91],a[240:243],v[96:99],a[88:91]
v_rcp_f32 v24,v24
v_mfma_f32_16x16x32_bf16 a[88:91],a[244:247],v[104:107],a[88:91]
v_accvgpr_read_b32 v17,a9
v_accvgpr_read_b32 v18,a10
v_mfma_f32_16x16x32_bf16 a[92:95],a[248:251],v[96:99],a[92:95]
v_accvgpr_read_b32 v19,a11
v_mul_f32 v8,v8,v24
v_mfma_f32_16x16x32_bf16 a[92:95],a[252:255],v[104:107],a[92:95]
v_mul_f32 v9,v9,v24
v_mul_f32 v10,v10,v24
v_mfma_f32_16x16x32_bf16 v[80:83],a[128:131],v[160:163],v[36:39]
v_mul_f32 v11,v11,v24
v_mul_f32 v12,v12,v24
v_mfma_f32_16x16x32_bf16 v[80:83],a[132:135],v[168:171],v[80:83]
v_mul_f32 v13,v13,v24
v_mul_f32 v14,v14,v24
v_mfma_f32_16x16x32_bf16 v[80:83],a[136:139],v[176:179],v[80:83]
v_mul_f32 v15,v15,v24
v_cvt_pk_bf16_f32 v8,v8,v9
v_mfma_f32_16x16x32_bf16 v[80:83],a[140:143],v[184:187],v[80:83]
v_cvt_pk_bf16_f32 v9,v10,v11
v_cvt_pk_bf16_f32 v10,v12,v13
v_mfma_f32_16x16x32_bf16 v[84:87],a[156:159],v[184:187],v[36:39]
v_cvt_pk_bf16_f32 v11,v14,v15
v_accvgpr_read_b32 v20,a12
v_mfma_f32_16x16x32_bf16 v[84:87],a[152:155],v[176:179],v[84:87]
ds_write_b128 v7,v[8:11] offset:6144
v_mfma_f32_16x16x32_bf16 v[84:87],a[148:151],v[168:171],v[84:87]
global_load_lds_dwordx4 v4,s[10:11] offset:1024 nt
v_accvgpr_read_b32 v21,a13
v_mfma_f32_16x16x32_bf16 v[84:87],a[144:147],v[160:163],v[84:87]
v_accvgpr_read_b32 v22,a14
v_accvgpr_read_b32 v23,a15
v_mfma_f32_16x16x32_bf16 v[88:91],a[160:163],v[160:163],v[36:39]
v_accvgpr_read_b32 v8,a16
v_accvgpr_read_b32 v9,a17
v_mfma_f32_16x16x32_bf16 v[88:91],a[164:167],v[168:171],v[88:91]
v_accvgpr_read_b32 v10,a18
v_accvgpr_read_b32 v11,a19
v_mfma_f32_16x16x32_bf16 v[88:91],a[168:171],v[176:179],v[88:91]
v_mul_f32 v16,v16,v24
v_mul_f32 v17,v17,v24
v_mfma_f32_16x16x32_bf16 v[88:91],a[172:175],v[184:187],v[88:91]
v_mul_f32 v18,v18,v24
v_mul_f32 v19,v19,v24
v_mfma_f32_16x16x32_bf16 v[92:95],a[188:191],v[184:187],v[36:39]
v_mul_f32 v20,v20,v24
v_mul_f32 v21,v21,v24
v_mfma_f32_16x16x32_bf16 v[92:95],a[184:187],v[176:179],v[92:95]
v_mul_f32 v22,v22,v24
v_mul_f32 v23,v23,v24
v_mfma_f32_16x16x32_bf16 v[92:95],a[180:183],v[168:171],v[92:95]
v_cvt_pk_bf16_f32 v16,v16,v17
v_cvt_pk_bf16_f32 v17,v18,v19
v_mfma_f32_16x16x32_bf16 v[92:95],a[176:179],v[160:163],v[92:95]
v_cvt_pk_bf16_f32 v18,v20,v21
)A"
R"A(v_cvt_pk_bf16_f32 v19,v22,v23
v_mfma_f32_16x16x32_bf16 v[60:63],v[28:31],v[112:115],v[60:63]
ds_write_b128 v7,v[16:19] offset:7168
v_mfma_f32_16x16x32_bf16 v[60:63],v[28:31],v[120:123],v[60:63]
v_accvgpr_read_b32 v12,a20
v_accvgpr_read_b32 v13,a21
v_mfma_f32_16x16x32_bf16 a[96:99],a[192:195],v[112:115],a[96:99]
ds_read_b64_tr_b16 a[192:193],v2
ds_read_b64_tr_b16 a[194:195],v2 offset:4096
v_mfma_f32_16x16x32_bf16 a[96:99],a[196:199],v[120:123],a[96:99]
ds_read_b64_tr_b16 a[196:197],v2 offset:8192
v_accvgpr_read_b32 v14,a22
v_mfma_f32_16x16x32_bf16 a[100:103],a[200:203],v[112:115],a[100:103]
global_load_lds_dwordx4 v5,s[10:11] offset:2048 nt
ds_read_b64_tr_b16 a[198:199],v2 offset:12288
v_mfma_f32_16x16x32_bf16 a[100:103],a[204:207],v[120:123],a[100:103]
ds_read_b64_tr_b16 a[200:201],v3
v_accvgpr_read_b32 v15,a23
v_mfma_f32_16x16x32_bf16 a[104:107],a[208:211],v[112:115],a[104:107]
ds_read_b64_tr_b16 a[202:203],v3 offset:4096
ds_read_b64_tr_b16 a[204:205],v3 offset:8192
v_mfma_f32_16x16x32_bf16 a[104:107],a[212:215],v[120:123],a[104:107]
s_cmp_le_u32 s5,2
s_cselect_b32 s5,s4,s5
ds_read_b64_tr_b16 a[206:207],v3 offset:12288
v_mfma_f32_16x16x32_bf16 a[108:111],a[216:219],v[112:115],a[108:111]
ds_read_b64_tr_b16 a[208:209],v2 offset:256
v_accvgpr_read_b32 v16,a24
v_mfma_f32_16x16x32_bf16 a[108:111],a[220:223],v[120:123],a[108:111]
ds_read_b64_tr_b16 a[210:211],v2 offset:4352
v_accvgpr_read_b32 v17,a25
v_mfma_f32_16x16x32_bf16 a[112:115],a[224:227],v[112:115],a[112:115]
ds_read_b64_tr_b16 a[212:213],v2 offset:8448
ds_read_b64_tr_b16 a[214:215],v2 offset:12544
v_mfma_f32_16x16x32_bf16 a[112:115],a[228:231],v[120:123],a[112:115]
ds_read_b64_tr_b16 a[216:217],v3 offset:256
v_accvgpr_read_b32 v18,a26
v_mfma_f32_16x16x32_bf16 a[116:119],a[232:235],v[112:115],a[116:119]
ds_read_b64_tr_b16 a[218:219],v3 offset:4352
v_accvgpr_read_b32 v19,a27
v_mfma_f32_16x16x32_bf16 a[116:119],a[236:239],v[120:123],a[116:119]
ds_read_b64_tr_b16 a[220:221],v3 offset:8448
v_accvgpr_read_b32 v20,a28
v_mfma_f32_16x16x32_bf16 a[120:123],a[240:243],v[112:115],a[120:123]
ds_read_b64_tr_b16 a[222:223],v3 offset:12544
ds_read_b64_tr_b16 a[224:225],v2 offset:512
v_mfma_f32_16x16x32_bf16 a[120:123],a[244:247],v[120:123],a[120:123]
ds_read_b64_tr_b16 a[226:227],v2 offset:4608
v_accvgpr_read_b32 v21,a29
v_mfma_f32_16x16x32_bf16 a[124:127],a[248:251],v[112:115],a[124:127]
ds_read_b64_tr_b16 a[228:229],v2 offset:8704
v_accvgpr_read_b32 v22,a30
v_mfma_f32_16x16x32_bf16 a[124:127],a[252:255],v[120:123],a[124:127]
s_waitcnt lgkmcnt(9)
ds_read_b64_tr_b16 a[230:231],v2 offset:12800
v_accvgpr_read_b32 v23,a31
v_mfma_f32_16x16x32_bf16 v[96:99],a[128:131],v[192:195],v[40:43]
ds_read_b64_tr_b16 a[232:233],v3 offset:512
ds_read_b64_tr_b16 a[234:235],v3 offset:4608
v_mfma_f32_16x16x32_bf16 v[96:99],a[132:135],v[200:203],v[96:99]
global_load_lds_dwordx4 v5,s[10:11] offset:3072
ds_read_b64_tr_b16 a[236:237],v3 offset:8704
v_mfma_f32_16x16x32_bf16 v[96:99],a[136:139],v[208:211],v[96:99]
ds_read_b64_tr_b16 a[238:239],v3 offset:12800
v_cmp_nlt_f32 s[54:55],v48,s73
v_mfma_f32_16x16x32_bf16 v[96:99],a[140:143],v[216:219],v[96:99]
ds_read_b64_tr_b16 a[240:241],v2 offset:768
v_cmp_nlt_f32 s[56:57],v52,s73
v_mfma_f32_16x16x32_bf16 v[100:103],a[156:159],v[216:219],v[40:43]
s_or_b64 s[54:55],s[54:55],s[56:57]
ds_read_b64_tr_b16 a[242:243],v2 offset:4864
ds_read_b64_tr_b16 a[244:245],v2 offset:8960
v_mfma_f32_16x16x32_bf16 v[100:103],a[152:155],v[208:211],v[100:103]
ds_read_b64_tr_b16 a[246:247],v2 offset:13056
v_cmp_nlt_f32 s[56:57],v56,s73
v_mfma_f32_16x16x32_bf16 v[100:103],a[148:151],v[200:203],v[100:103]
s_or_b64 s[54:55],s[54:55],s[56:57]
ds_read_b64_tr_b16 a[248:249],v3 offset:768
v_mul_f32 v8,v8,v24
v_mfma_f32_16x16x32_bf16 v[100:103],a[144:147],v[192:195],v[100:103]
ds_read_b64_tr_b16 a[250:251],v3 offset:4864
v_mul_f32 v9,v9,v24
v_mfma_f32_16x16x32_bf16 v[104:107],a[160:163],v[192:195],v[40:43]
ds_read_b64_tr_b16 a[252:253],v3 offset:8960
ds_read_b64_tr_b16 a[254:255],v3 offset:13056
v_mfma_f32_16x16x32_bf16 v[104:107],a[164:167],v[200:203],v[104:107]
v_mul_f32 v10,v10,v24
v_mul_f32 v11,v11,v24
v_mfma_f32_16x16x32_bf16 v[104:107],a[168:171],v[208:211],v[104:107]
v_mul_f32 v12,v12,v24
v_mul_f32 v13,v13,v24
v_mfma_f32_16x16x32_bf16 v[104:107],a[172:175],v[216:219],v[104:107]
s_add_u32 s10,s24,s74
s_addc_u32 s11,s25,0
v_mul_f32 v14,v14,v24
v_mfma_f32_16x16x32_bf16 v[108:111],a[188:191],v[216:219],v[40:43]
v_mul_f32 v15,v15,v24
v_cvt_pk_bf16_f32 v8,v8,v9
v_mfma_f32_16x16x32_bf16 v[108:111],a[184:187],v[208:211],v[108:111]
s_add_u32 m0,s76,16384
v_cvt_pk_bf16_f32 v9,v10,v11
v_cvt_pk_bf16_f32 v10,v12,v13
v_mfma_f32_16x16x32_bf16 v[108:111],a[180:183],v[200:203],v[108:111]
v_cvt_pk_bf16_f32 v11,v14,v15
v_mul_f32 v16,v16,v24
v_mfma_f32_16x16x32_bf16 v[108:111],a[176:179],v[192:195],v[108:111]
global_load_lds_dwordx4 v4,s[10:11]
v_mul_f32 v17,v17,v24
v_mfma_f32_16x16x32_bf16 a[0:3],a[192:195],v[64:67],0
v_mul_f32 v18,v18,v24
v_mul_f32 v19,v19,v24
v_mfma_f32_16x16x32_bf16 a[0:3],a[196:199],v[72:75],a[0:3]
v_mul_f32 v20,v20,v24
v_mul_f32 v21,v21,v24
v_mfma_f32_16x16x32_bf16 a[4:7],a[200:203],v[64:67],0
v_mul_f32 v22,v22,v24
v_mul_f32 v23,v23,v24
v_mfma_f32_16x16x32_bf16 a[4:7],a[204:207],v[72:75],a[4:7]
v_cvt_pk_bf16_f32 v16,v16,v17
v_cvt_pk_bf16_f32 v17,v18,v19
v_mfma_f32_16x16x32_bf16 a[8:11],a[208:211],v[64:67],0
s_waitcnt lgkmcnt(0)
v_cvt_pk_bf16_f32 v18,v20,v21
v_cvt_pk_bf16_f32 v19,v22,v23
v_mfma_f32_16x16x32_bf16 a[8:11],a[212:215],v[72:75],a[8:11]
v_exp_f32 v80,v80
v_mfma_f32_16x16x32_bf16 a[12:15],a[216:219],v[64:67],0
v_exp_f32 v81,v81
v_mfma_f32_16x16x32_bf16 a[12:15],a[220:223],v[72:75],a[12:15]
v_exp_f32 v82,v82
v_mfma_f32_16x16x32_bf16 a[16:19],a[224:227],v[64:67],0
v_exp_f32 v83,v83
v_mfma_f32_16x16x32_bf16 a[16:19],a[228:231],v[72:75],a[16:19]
v_exp_f32 v84,v84
v_mfma_f32_16x16x32_bf16 a[20:23],a[232:235],v[64:67],0
s_getreg_b32 s92,hwreg(HW_REG_IB_STS,0,4)
s_min_u32 s4,s92,s6
s_mov_b32 s6,s92
v_mfma_f32_16x16x32_bf16 a[20:23],a[236:239],v[72:75],a[20:23]
s_cmp_gt_u32 s4,s9
s_cselect_b32 s4,15,s5
v_cmp_nlt_f32 s[56:57],v60,s73
v_mfma_f32_16x16x32_bf16 a[24:27],a[240:243],v[64:67],0
s_or_b64 s[54:55],s[54:55],s[56:57]
v_exp_f32 v85,v85
v_mfma_f32_16x16x32_bf16 a[24:27],a[244:247],v[72:75],a[24:27]
v_exp_f32 v86,v86
v_mfma_f32_16x16x32_bf16 a[28:31],a[248:251],v[64:67],0
global_load_lds_dwordx4 v5,s[10:11] offset:1024
v_add_u32 v1,s78,v1
v_mfma_f32_16x16x32_bf16 a[28:31],a[252:255],v[72:75],a[28:31]
v_exp_f32 v87,v87
v_mfma_f32_16x16x32_bf16 v[48:51],v[28:31],v[64:67],0
s_getpc_b64 s[92:93]
s_cmp_lg_u32 s4,0
s_cbranch_scc1 L_pace_%=
v_exp_f32 v88,v88
v_exp_f32 v89,v89
v_mfma_f32_16x16x32_bf16 v[48:51],v[28:31],v[72:75],v[48:51]
ds_write_b128 v7,v[8:11]
v_max_f32 v25,0x00800000,v52
v_rcp_f32 v25,v25
v_max_f32 v26,0x00800000,v56
v_rcp_f32 v26,v26
v_cvt_pk_bf16_f32 v80,v80,v81
v_cvt_pk_bf16_f32 v81,v82,v83
v_cvt_pk_bf16_f32 v82,v84,v85
v_cvt_pk_bf16_f32 v83,v86,v87
v_cvt_pk_bf16_f32 v88,v88,v89
v_max_f32 v27,0x00800000,v60
v_rcp_f32 v27,v27
ds_write_b128 v7,v[16:19] offset:1024
ds_read_b128 v[44:47],v7 offset:3072
s_cmp_lg_u64 s[54:55],0
s_cbranch_scc1 L_slow0_%=
L_ok0_%=:
s_waitcnt vmcnt(14)
s_barrier
s_waitcnt lgkmcnt(0)
v_mfma_f32_16x16x32_bf16 v[112:115],a[128:131],v[224:227],v[44:47]
s_add_u32 s10,s24,s74
s_addc_u32 s11,s25,0
s_add_u32 m0,s76,16384
v_mfma_f32_16x16x32_bf16 v[112:115],a[132:135],v[232:235],v[112:115]
s_add_u32 s75,s75,16384
v_exp_f32 v90,v90
v_mfma_f32_16x16x32_bf16 v[112:115],a[136:139],v[240:243],v[112:115]
global_load_lds_dwordx4 v4,s[10:11] offset:2048
ds_read_b128 a[128:131],v1
v_mfma_f32_16x16x32_bf16 v[112:115],a[140:143],v[248:251],v[112:115]
s_add_u32 s74,s74,16384
v_exp_f32 v91,v91
v_mfma_f32_16x16x32_bf16 v[116:119],a[156:159],v[248:251],v[44:47]
s_and_b32 s74,s74,0x3ffff
v_exp_f32 v92,v92
v_mfma_f32_16x16x32_bf16 v[116:119],a[152:155],v[240:243],v[116:119]
s_and_b32 s69,s75,0xfffc0000
v_exp_f32 v93,v93
v_mfma_f32_16x16x32_bf16 v[116:119],a[148:151],v[232:235],v[116:119]
s_or_b32 s74,s74,s69
v_exp_f32 v94,v94
v_mfma_f32_16x16x32_bf16 v[116:119],a[144:147],v[224:227],v[116:119]
s_sub_u32 s76,s76,32768
v_exp_f32 v95,v95
v_mfma_f32_16x16x32_bf16 v[120:123],a[160:163],v[224:227],v[44:47]
s_and_b32 s76,s76,0x1ffff
v_cvt_pk_bf16_f32 v89,v90,v91
v_cvt_pk_bf16_f32 v90,v92,v93
v_mfma_f32_16x16x32_bf16 v[120:123],a[164:167],v[232:235],v[120:123]
s_add_u32 s75,s75,16384
ds_read_b128 a[132:135],v1 offset:256
v_cvt_pk_bf16_f32 v91,v94,v95
v_mfma_f32_16x16x32_bf16 v[120:123],a[168:171],v[240:243],v[120:123]
s_and_b32 s69,s75,0xfffc0000
v_exp_f32 v96,v96
v_mfma_f32_16x16x32_bf16 v[120:123],a[172:175],v[248:251],v[120:123]
s_add_u32 s75,s75,16384
ds_read_b128 a[136:139],v1 offset:512
v_accvgpr_read_b32 v16,a32
v_mfma_f32_16x16x32_bf16 v[124:127],a[188:191],v[248:251],v[44:47]
v_exp_f32 v97,v97
v_mfma_f32_16x16x32_bf16 v[124:127],a[184:187],v[240:243],v[124:127]
ds_read_b128 a[140:143],v1 offset:768
v_cvt_pk_bf16_f32 v96,v96,v97
v_mfma_f32_16x16x32_bf16 v[124:127],a[180:183],v[232:235],v[124:127]
v_exp_f32 v98,v98
v_mfma_f32_16x16x32_bf16 v[124:127],a[176:179],v[224:227],v[124:127]
ds_read_b128 a[156:159],v1 offset:4864
v_accvgpr_read_b32 v17,a33
v_mfma_f32_16x16x32_bf16 v[132:135],a[192:195],v[80:83],0
global_load_lds_dwordx4 v5,s[10:11] offset:3072
v_accvgpr_read_b32 v18,a34
v_mfma_f32_16x16x32_bf16 v[132:135],a[196:199],v[88:91],v[132:135]
ds_read_b128 a[152:155],v1 offset:4608
v_accvgpr_read_b32 v19,a35
v_mfma_f32_16x16x32_bf16 v[140:143],a[200:203],v[80:83],0
v_exp_f32 v99,v99
v_mfma_f32_16x16x32_bf16 v[140:143],a[204:207],v[88:91],v[140:143]
ds_read_b128 a[148:151],v1 offset:4352
v_cvt_pk_bf16_f32 v97,v98,v99
v_mfma_f32_16x16x32_bf16 v[148:151],a[208:211],v[80:83],0
v_exp_f32 v100,v100
v_mfma_f32_16x16x32_bf16 v[148:151],a[212:215],v[88:91],v[148:151]
ds_read_b128 a[144:147],v1 offset:4096
v_accvgpr_read_b32 v20,a36
v_mfma_f32_16x16x32_bf16 v[156:159],a[216:219],v[80:83],0
v_exp_f32 v101,v101
v_mfma_f32_16x16x32_bf16 v[156:159],a[220:223],v[88:91],v[156:159]
ds_read_b128 a[160:163],v1 offset:8192
v_cvt_pk_bf16_f32 v98,v100,v101
v_mfma_f32_16x16x32_bf16 v[164:167],a[224:227],v[80:83],0
v_exp_f32 v102,v102
v_mfma_f32_16x16x32_bf16 v[164:167],a[228:231],v[88:91],v[164:167]
ds_read_b128 a[164:167],v1 offset:8448
v_accvgpr_read_b32 v21,a37
v_mfma_f32_16x16x32_bf16 v[172:175],a[232:235],v[80:83],0
s_add_u32 s10,s20,s74
s_addc_u32 s11,s21,0
v_accvgpr_read_b32 v22,a38
v_mfma_f32_16x16x32_bf16 v[172:175],a[236:239],v[88:91],v[172:175]
ds_read_b128 a[168:171],v1 offset:8704
v_accvgpr_read_b32 v23,a39
v_mfma_f32_16x16x32_bf16 v[180:183],a[240:243],v[80:83],0
s_add_u32 m0,s76,0
v_exp_f32 v103,v103
v_mfma_f32_16x16x32_bf16 v[180:183],a[244:247],v[88:91],v[180:183]
ds_read_b128 a[172:175],v1 offset:8960
v_cvt_pk_bf16_f32 v99,v102,v103
v_mfma_f32_16x16x32_bf16 v[188:191],a[248:251],v[80:83],0
global_load_lds_dwordx4 v4,s[10:11] nt
v_mul_f32 v16,v16,v25
v_mfma_f32_16x16x32_bf16 v[188:191],a[252:255],v[88:91],v[188:191]
ds_read_b128 a[188:191],v1 offset:13056
v_mul_f32 v17,v17,v25
v_mfma_f32_16x16x32_bf16 v[52:55],v[28:31],v[80:83],0
v_exp_f32 v104,v104
v_mfma_f32_16x16x32_bf16 v[52:55],v[28:31],v[88:91],v[52:55]
s_waitcnt lgkmcnt(3)
ds_read_b128 a[184:187],v1 offset:12800
v_mul_f32 v18,v18,v25
v_mfma_f32_16x16x32_bf16 v[64:67],a[128:131],v[128:131],v[32:35]
v_exp_f32 v105,v105
v_mfma_f32_16x16x32_bf16 v[64:67],a[132:135],v[136:139],v[64:67]
ds_read_b128 a[180:183],v1 offset:12544
v_cvt_pk_bf16_f32 v104,v104,v105
v_mfma_f32_16x16x32_bf16 v[64:67],a[136:139],v[144:147],v[64:67]
v_exp_f32 v106,v106
v_mfma_f32_16x16x32_bf16 v[64:67],a[140:143],v[152:155],v[64:67]
ds_read_b128 a[176:179],v1 offset:12288
v_mul_f32 v19,v19,v25
v_mfma_f32_16x16x32_bf16 v[68:71],a[156:159],v[152:155],v[32:35]
v_exp_f32 v107,v107
v_mfma_f32_16x16x32_bf16 v[68:71],a[152:155],v[144:147],v[68:71]
v_exp_f32 v108,v108
v_mfma_f32_16x16x32_bf16 v[68:71],a[148:151],v[136:139],v[68:71]
v_exp_f32 v109,v109
v_mfma_f32_16x16x32_bf16 v[68:71],a[144:147],v[128:131],v[68:71]
)A"
R"A(v_exp_f32 v110,v110
v_mfma_f32_16x16x32_bf16 v[72:75],a[160:163],v[128:131],v[32:35]
v_exp_f32 v111,v111
v_mfma_f32_16x16x32_bf16 v[72:75],a[164:167],v[136:139],v[72:75]
s_waitcnt lgkmcnt(1)
v_cvt_pk_bf16_f32 v105,v106,v107
v_cvt_pk_bf16_f32 v106,v108,v109
v_mfma_f32_16x16x32_bf16 v[72:75],a[168:171],v[144:147],v[72:75]
global_load_lds_dwordx4 v4,s[10:11] offset:1024 nt
v_cvt_pk_bf16_f32 v107,v110,v111
v_mfma_f32_16x16x32_bf16 v[72:75],a[172:175],v[152:155],v[72:75]
v_exp_f32 v112,v112
v_mfma_f32_16x16x32_bf16 v[76:79],a[188:191],v[152:155],v[32:35]
v_exp_f32 v113,v113
v_mfma_f32_16x16x32_bf16 v[76:79],a[184:187],v[144:147],v[76:79]
v_exp_f32 v114,v114
v_mfma_f32_16x16x32_bf16 v[76:79],a[180:183],v[136:139],v[76:79]
s_waitcnt lgkmcnt(0)
v_exp_f32 v115,v115
v_mfma_f32_16x16x32_bf16 v[76:79],a[176:179],v[128:131],v[76:79]
v_exp_f32 v116,v116
v_mfma_f32_16x16x32_bf16 v[196:199],a[192:195],v[96:99],0
v_exp_f32 v117,v117
v_mfma_f32_16x16x32_bf16 v[196:199],a[196:199],v[104:107],v[196:199]
v_exp_f32 v118,v118
v_mfma_f32_16x16x32_bf16 v[204:207],a[200:203],v[96:99],0
v_exp_f32 v119,v119
v_mfma_f32_16x16x32_bf16 v[204:207],a[204:207],v[104:107],v[204:207]
v_cvt_pk_bf16_f32 v112,v112,v113
v_cvt_pk_bf16_f32 v113,v114,v115
v_mfma_f32_16x16x32_bf16 v[212:215],a[208:211],v[96:99],0
v_cvt_pk_bf16_f32 v114,v116,v117
v_cvt_pk_bf16_f32 v115,v118,v119
v_mfma_f32_16x16x32_bf16 v[212:215],a[212:215],v[104:107],v[212:215]
v_exp_f32 v120,v120
v_mfma_f32_16x16x32_bf16 v[220:223],a[216:219],v[96:99],0
v_exp_f32 v121,v121
v_mfma_f32_16x16x32_bf16 v[220:223],a[220:223],v[104:107],v[220:223]
v_exp_f32 v122,v122
v_mfma_f32_16x16x32_bf16 v[228:231],a[224:227],v[96:99],0
global_load_lds_dwordx4 v5,s[10:11] offset:2048 nt
v_cvt_pk_bf16_f32 v120,v120,v121
v_mfma_f32_16x16x32_bf16 v[228:231],a[228:231],v[104:107],v[228:231]
v_exp_f32 v123,v123
v_mfma_f32_16x16x32_bf16 v[236:239],a[232:235],v[96:99],0
v_exp_f32 v124,v124
v_mfma_f32_16x16x32_bf16 v[236:239],a[236:239],v[104:107],v[236:239]
v_exp_f32 v125,v125
v_mfma_f32_16x16x32_bf16 v[244:247],a[240:243],v[96:99],0
v_exp_f32 v126,v126
v_mfma_f32_16x16x32_bf16 v[244:247],a[244:247],v[104:107],v[244:247]
v_exp_f32 v127,v127
v_mfma_f32_16x16x32_bf16 v[252:255],a[248:251],v[96:99],0
v_cvt_pk_bf16_f32 v121,v122,v123
v_cvt_pk_bf16_f32 v122,v124,v125
v_mfma_f32_16x16x32_bf16 v[252:255],a[252:255],v[104:107],v[252:255]
v_cvt_pk_bf16_f32 v123,v126,v127
v_mul_f32 v20,v20,v25
v_mfma_f32_16x16x32_bf16 v[56:59],v[28:31],v[96:99],0
v_mul_f32 v21,v21,v25
v_mul_f32 v22,v22,v25
v_mfma_f32_16x16x32_bf16 v[56:59],v[28:31],v[104:107],v[56:59]
v_mul_f32 v23,v23,v25
v_cvt_pk_bf16_f32 v16,v16,v17
v_mfma_f32_16x16x32_bf16 v[80:83],a[128:131],v[160:163],v[36:39]
v_cvt_pk_bf16_f32 v17,v18,v19
v_cvt_pk_bf16_f32 v18,v20,v21
v_mfma_f32_16x16x32_bf16 v[80:83],a[132:135],v[168:171],v[80:83]
v_cvt_pk_bf16_f32 v19,v22,v23
v_accvgpr_read_b32 v20,a44
v_mfma_f32_16x16x32_bf16 v[80:83],a[136:139],v[176:179],v[80:83]
ds_write_b128 v7,v[16:19] offset:2048
v_mfma_f32_16x16x32_bf16 v[80:83],a[140:143],v[184:187],v[80:83]
v_accvgpr_read_b32 v21,a45
v_accvgpr_read_b32 v22,a46
v_mfma_f32_16x16x32_bf16 v[84:87],a[156:159],v[184:187],v[36:39]
v_accvgpr_read_b32 v16,a40
v_accvgpr_read_b32 v17,a41
v_mfma_f32_16x16x32_bf16 v[84:87],a[152:155],v[176:179],v[84:87]
global_load_lds_dwordx4 v5,s[10:11] offset:3072
v_accvgpr_read_b32 v18,a42
v_mfma_f32_16x16x32_bf16 v[84:87],a[148:151],v[168:171],v[84:87]
v_accvgpr_read_b32 v19,a43
v_accvgpr_read_b32 v23,a47
v_mfma_f32_16x16x32_bf16 v[84:87],a[144:147],v[160:163],v[84:87]
v_accvgpr_read_b32 v8,a48
v_accvgpr_read_b32 v9,a49
v_mfma_f32_16x16x32_bf16 v[88:91],a[160:163],v[160:163],v[36:39]
v_accvgpr_read_b32 v10,a50
v_accvgpr_read_b32 v11,a51
v_mfma_f32_16x16x32_bf16 v[88:91],a[164:167],v[168:171],v[88:91]
v_mul_f32 v16,v16,v25
v_mul_f32 v17,v17,v25
v_mfma_f32_16x16x32_bf16 v[88:91],a[168:171],v[176:179],v[88:91]
v_mul_f32 v18,v18,v25
v_mul_f32 v19,v19,v25
v_mfma_f32_16x16x32_bf16 v[88:91],a[172:175],v[184:187],v[88:91]
v_mul_f32 v20,v20,v25
v_mul_f32 v21,v21,v25
v_mfma_f32_16x16x32_bf16 v[92:95],a[188:191],v[184:187],v[36:39]
v_mul_f32 v22,v22,v25
v_mul_f32 v23,v23,v25
v_mfma_f32_16x16x32_bf16 v[92:95],a[184:187],v[176:179],v[92:95]
v_cvt_pk_bf16_f32 v16,v16,v17
v_cvt_pk_bf16_f32 v17,v18,v19
v_mfma_f32_16x16x32_bf16 v[92:95],a[180:183],v[168:171],v[92:95]
v_cvt_pk_bf16_f32 v18,v20,v21
v_cvt_pk_bf16_f32 v19,v22,v23
v_mfma_f32_16x16x32_bf16 v[92:95],a[176:179],v[160:163],v[92:95]
s_add_u32 s10,s24,s74
s_addc_u32 s11,s25,0
v_accvgpr_read_b32 v12,a52
v_mfma_f32_16x16x32_bf16 a[32:35],a[192:195],v[112:115],0
ds_write_b128 v7,v[16:19] offset:3072
v_mfma_f32_16x16x32_bf16 a[32:35],a[196:199],v[120:123],a[32:35]
s_add_u32 m0,s76,16384
v_accvgpr_read_b32 v13,a53
v_accvgpr_read_b32 v14,a54
v_mfma_f32_16x16x32_bf16 a[36:39],a[200:203],v[112:115],0
v_accvgpr_read_b32 v15,a55
v_accvgpr_read_b32 v16,a56
v_mfma_f32_16x16x32_bf16 a[36:39],a[204:207],v[120:123],a[36:39]
global_load_lds_dwordx4 v4,s[10:11]
v_accvgpr_read_b32 v17,a57
v_mfma_f32_16x16x32_bf16 a[40:43],a[208:211],v[112:115],0
v_accvgpr_read_b32 v18,a58
v_accvgpr_read_b32 v19,a59
v_mfma_f32_16x16x32_bf16 a[40:43],a[212:215],v[120:123],a[40:43]
v_accvgpr_read_b32 v20,a60
v_accvgpr_read_b32 v21,a61
v_mfma_f32_16x16x32_bf16 a[44:47],a[216:219],v[112:115],0
v_accvgpr_read_b32 v22,a62
v_accvgpr_read_b32 v23,a63
v_mfma_f32_16x16x32_bf16 a[44:47],a[220:223],v[120:123],a[44:47]
v_add_u32 v2,s78,v2
v_add_u32 v3,s78,v3
v_mfma_f32_16x16x32_bf16 a[48:51],a[224:227],v[112:115],0
s_sub_u32 s78,s77,32768
ds_read_b64_tr_b16 a[192:193],v2
ds_read_b64_tr_b16 a[194:195],v2 offset:4096
v_mfma_f32_16x16x32_bf16 a[48:51],a[228:231],v[120:123],a[48:51]
s_and_b32 s78,s78,0x1ffff
ds_read_b64_tr_b16 a[196:197],v2 offset:8192
v_mul_f32 v8,v8,v25
v_mfma_f32_16x16x32_bf16 a[52:55],a[232:235],v[112:115],0
s_sub_u32 s78,s78,s77
ds_read_b64_tr_b16 a[198:199],v2 offset:12288
v_mul_f32 v9,v9,v25
v_mfma_f32_16x16x32_bf16 a[52:55],a[236:239],v[120:123],a[52:55]
s_add_u32 s77,s77,s78
ds_read_b64_tr_b16 a[200:201],v3
v_mul_f32 v10,v10,v25
v_mfma_f32_16x16x32_bf16 a[56:59],a[240:243],v[112:115],0
ds_read_b64_tr_b16 a[202:203],v3 offset:4096
ds_read_b64_tr_b16 a[204:205],v3 offset:8192
v_mfma_f32_16x16x32_bf16 a[56:59],a[244:247],v[120:123],a[56:59]
ds_read_b64_tr_b16 a[206:207],v3 offset:12288
v_mul_f32 v11,v11,v25
v_mfma_f32_16x16x32_bf16 a[60:63],a[248:251],v[112:115],0
ds_read_b64_tr_b16 a[208:209],v2 offset:256
v_mul_f32 v12,v12,v25
v_mfma_f32_16x16x32_bf16 a[60:63],a[252:255],v[120:123],a[60:63]
ds_read_b64_tr_b16 a[210:211],v2 offset:4352
v_mul_f32 v13,v13,v25
v_mfma_f32_16x16x32_bf16 v[60:63],v[28:31],v[112:115],0
ds_read_b64_tr_b16 a[212:213],v2 offset:8448
ds_read_b64_tr_b16 a[214:215],v2 offset:12544
v_mfma_f32_16x16x32_bf16 v[60:63],v[28:31],v[120:123],v[60:63]
global_load_lds_dwordx4 v5,s[10:11] offset:1024
ds_read_b64_tr_b16 a[216:217],v3 offset:256
v_mfma_f32_16x16x32_bf16 v[96:99],a[128:131],v[192:195],v[40:43]
v_exp_f32 v64,v64
v_mfma_f32_16x16x32_bf16 v[96:99],a[132:135],v[200:203],v[96:99]
v_exp_f32 v65,v65
v_mfma_f32_16x16x32_bf16 v[96:99],a[136:139],v[208:211],v[96:99]
v_exp_f32 v66,v66
v_mfma_f32_16x16x32_bf16 v[96:99],a[140:143],v[216:219],v[96:99]
v_exp_f32 v67,v67
v_mfma_f32_16x16x32_bf16 v[100:103],a[156:159],v[216:219],v[40:43]
v_exp_f32 v68,v68
v_mfma_f32_16x16x32_bf16 v[100:103],a[152:155],v[208:211],v[100:103]
v_exp_f32 v69,v69
v_mfma_f32_16x16x32_bf16 v[100:103],a[148:151],v[200:203],v[100:103]
v_exp_f32 v70,v70
v_mfma_f32_16x16x32_bf16 v[100:103],a[144:147],v[192:195],v[100:103]
v_exp_f32 v71,v71
v_mfma_f32_16x16x32_bf16 v[104:107],a[160:163],v[192:195],v[40:43]
ds_read_b64_tr_b16 a[218:219],v3 offset:4352
v_cvt_pk_bf16_f32 v64,v64,v65
v_mfma_f32_16x16x32_bf16 v[104:107],a[164:167],v[200:203],v[104:107]
v_cvt_pk_bf16_f32 v65,v66,v67
v_cvt_pk_bf16_f32 v66,v68,v69
v_mfma_f32_16x16x32_bf16 v[104:107],a[168:171],v[208:211],v[104:107]
ds_read_b64_tr_b16 a[220:221],v3 offset:8448
v_cvt_pk_bf16_f32 v67,v70,v71
v_mfma_f32_16x16x32_bf16 v[104:107],a[172:175],v[216:219],v[104:107]
v_exp_f32 v72,v72
v_mfma_f32_16x16x32_bf16 v[108:111],a[188:191],v[216:219],v[40:43]
v_exp_f32 v73,v73
v_mfma_f32_16x16x32_bf16 v[108:111],a[184:187],v[208:211],v[108:111]
global_load_lds_dwordx4 v4,s[10:11] offset:2048
v_exp_f32 v74,v74
v_exp_f32 v75,v75
v_exp_f32 v76,v76
v_exp_f32 v77,v77
v_exp_f32 v78,v78
v_exp_f32 v79,v79
v_mfma_f32_16x16x32_bf16 v[108:111],a[180:183],v[200:203],v[108:111]
ds_read_b64_tr_b16 a[222:223],v3 offset:12544
v_cvt_pk_bf16_f32 v72,v72,v73
v_cvt_pk_bf16_f32 v73,v74,v75
v_cvt_pk_bf16_f32 v74,v76,v77
L_pd8_%=:
s_cbranch_execz L_trt0_%=
v_cvt_pk_bf16_f32 v75,v78,v79
v_mfma_f32_16x16x32_bf16 v[108:111],a[176:179],v[192:195],v[108:111]
s_waitcnt lgkmcnt(3)
ds_read_b64_tr_b16 a[224:225],v2 offset:512
ds_read_b64_tr_b16 a[226:227],v2 offset:4608
v_mfma_f32_16x16x32_bf16 a[0:3],a[192:195],v[64:67],a[0:3]
ds_read_b64_tr_b16 a[228:229],v2 offset:8704
v_mul_f32 v14,v14,v25
v_mfma_f32_16x16x32_bf16 a[0:3],a[196:199],v[72:75],a[0:3]
ds_read_b64_tr_b16 a[230:231],v2 offset:12800
v_mul_f32 v15,v15,v25
v_mfma_f32_16x16x32_bf16 a[4:7],a[200:203],v[64:67],a[4:7]
ds_read_b64_tr_b16 a[232:233],v3 offset:512
v_cvt_pk_bf16_f32 v8,v8,v9
v_mfma_f32_16x16x32_bf16 a[4:7],a[204:207],v[72:75],a[4:7]
ds_read_b64_tr_b16 a[234:235],v3 offset:4608
ds_read_b64_tr_b16 a[236:237],v3 offset:8704
v_mfma_f32_16x16x32_bf16 a[8:11],a[208:211],v[64:67],a[8:11]
ds_read_b64_tr_b16 a[238:239],v3 offset:12800
v_cvt_pk_bf16_f32 v9,v10,v11
v_mfma_f32_16x16x32_bf16 a[8:11],a[212:215],v[72:75],a[8:11]
s_waitcnt lgkmcnt(9)
ds_read_b64_tr_b16 a[240:241],v2 offset:768
v_cvt_pk_bf16_f32 v10,v12,v13
v_mfma_f32_16x16x32_bf16 a[12:15],a[216:219],v[64:67],a[12:15]
s_waitcnt lgkmcnt(9)
ds_read_b64_tr_b16 a[242:243],v2 offset:4864
v_cvt_pk_bf16_f32 v11,v14,v15
v_mfma_f32_16x16x32_bf16 a[12:15],a[220:223],v[72:75],a[12:15]
s_waitcnt lgkmcnt(8)
ds_read_b64_tr_b16 a[244:245],v2 offset:8960
ds_read_b64_tr_b16 a[246:247],v2 offset:13056
v_mfma_f32_16x16x32_bf16 a[16:19],a[224:227],v[64:67],a[16:19]
s_waitcnt lgkmcnt(8)
ds_read_b64_tr_b16 a[248:249],v3 offset:768
v_mul_f32 v16,v16,v25
v_mfma_f32_16x16x32_bf16 a[16:19],a[228:231],v[72:75],a[16:19]
s_waitcnt lgkmcnt(1)
ds_read_b64_tr_b16 a[250:251],v3 offset:4864
v_mul_f32 v17,v17,v25
v_mfma_f32_16x16x32_bf16 a[20:23],a[232:235],v[64:67],a[20:23]
s_getreg_b32 s92,hwreg(HW_REG_IB_STS,0,4)
s_min_u32 s4,s92,s6
ds_read_b64_tr_b16 a[252:253],v3 offset:8960
v_mfma_f32_16x16x32_bf16 a[20:23],a[236:239],v[72:75],a[20:23]
s_mov_b32 s6,s92
ds_read_b64_tr_b16 a[254:255],v3 offset:13056
v_mul_f32 v18,v18,v25
v_mfma_f32_16x16x32_bf16 a[24:27],a[240:243],v[64:67],a[24:27]
s_cmp_gt_u32 s4,s9
s_cselect_b32 s4,15,s5
v_mul_f32 v19,v19,v25
v_mfma_f32_16x16x32_bf16 a[24:27],a[244:247],v[72:75],a[24:27]
s_waitcnt lgkmcnt(2)
v_mul_f32 v20,v20,v25
v_mul_f32 v21,v21,v25
v_mfma_f32_16x16x32_bf16 a[28:31],a[248:251],v[64:67],a[28:31]
s_waitcnt lgkmcnt(0)
v_mul_f32 v22,v22,v25
v_mul_f32 v23,v23,v25
v_mfma_f32_16x16x32_bf16 a[28:31],a[252:255],v[72:75],a[28:31]
s_getpc_b64 s[92:93]
s_cmp_lg_u32 s4,0
s_cbranch_scc1 L_pace_%=
v_mfma_f32_16x16x32_bf16 v[48:51],v[28:31],v[64:67],v[48:51]
v_cvt_pk_bf16_f32 v12,v16,v17
v_cvt_pk_bf16_f32 v13,v18,v19
v_mfma_f32_16x16x32_bf16 v[48:51],v[28:31],v[72:75],v[48:51]
s_add_u32 s10,s24,s74
s_addc_u32 s11,s25,0
v_cvt_pk_bf16_f32 v14,v20,v21
s_waitcnt vmcnt(15)
s_barrier
v_mfma_f32_16x16x32_bf16 v[112:115],a[128:131],v[224:227],v[44:47]
s_add_u32 m0,s76,16384
v_cvt_pk_bf16_f32 v15,v22,v23
v_accvgpr_read_b32 v16,a112
v_mfma_f32_16x16x32_bf16 v[112:115],a[132:135],v[232:235],v[112:115]
s_add_u32 s74,s74,16384
v_accvgpr_read_b32 v17,a113
v_accvgpr_read_b32 v18,a114
v_mfma_f32_16x16x32_bf16 v[112:115],a[136:139],v[240:243],v[112:115]
global_load_lds_dwordx4 v5,s[10:11] offset:3072
v_accvgpr_read_b32 v19,a115
v_mfma_f32_16x16x32_bf16 v[112:115],a[140:143],v[248:251],v[112:115]
s_and_b32 s74,s74,0x3ffff
v_accvgpr_read_b32 v20,a116
v_accvgpr_read_b32 v21,a117
)A"
R"A(v_mfma_f32_16x16x32_bf16 v[116:119],a[156:159],v[248:251],v[44:47]
s_or_b32 s74,s74,s69
v_accvgpr_read_b32 v22,a118
v_accvgpr_read_b32 v23,a119
v_mfma_f32_16x16x32_bf16 v[116:119],a[152:155],v[240:243],v[116:119]
s_sub_u32 s76,s76,32768
v_mul_f32 v16,v16,v27
v_mul_f32 v17,v17,v27
v_mfma_f32_16x16x32_bf16 v[116:119],a[148:151],v[232:235],v[116:119]
s_and_b32 s76,s76,0x1ffff
v_mul_f32 v18,v18,v27
v_mul_f32 v19,v19,v27
v_mfma_f32_16x16x32_bf16 v[116:119],a[144:147],v[224:227],v[116:119]
s_and_b32 s69,s75,0xfffc0000
v_mul_f32 v20,v20,v27
v_mul_f32 v21,v21,v27
v_mfma_f32_16x16x32_bf16 v[120:123],a[160:163],v[224:227],v[44:47]
s_add_u32 s75,s75,16384
v_mul_f32 v22,v22,v27
v_mul_f32 v23,v23,v27
v_mfma_f32_16x16x32_bf16 v[120:123],a[164:167],v[232:235],v[120:123]
v_cvt_pk_bf16_f32 v16,v16,v17
v_cvt_pk_bf16_f32 v17,v18,v19
v_mfma_f32_16x16x32_bf16 v[120:123],a[168:171],v[240:243],v[120:123]
v_cvt_pk_bf16_f32 v18,v20,v21
v_cvt_pk_bf16_f32 v19,v22,v23
v_mfma_f32_16x16x32_bf16 v[120:123],a[172:175],v[248:251],v[120:123]
v_accvgpr_write_b32 a112,v16
v_accvgpr_write_b32 a113,v17
v_mfma_f32_16x16x32_bf16 v[124:127],a[188:191],v[248:251],v[44:47]
v_accvgpr_write_b32 a114,v18
v_accvgpr_write_b32 a115,v19
v_accvgpr_write_b32 a116,v8
v_accvgpr_write_b32 a117,v9
v_accvgpr_write_b32 a118,v10
v_accvgpr_write_b32 a119,v11
v_exp_f32 v80,v80
v_exp_f32 v81,v81
v_exp_f32 v82,v82
v_exp_f32 v83,v83
v_exp_f32 v84,v84
v_exp_f32 v85,v85
v_exp_f32 v86,v86
v_exp_f32 v87,v87
v_mfma_f32_16x16x32_bf16 v[124:127],a[184:187],v[240:243],v[124:127]
v_cvt_pk_bf16_f32 v80,v80,v81
v_cvt_pk_bf16_f32 v81,v82,v83
v_cvt_pk_bf16_f32 v82,v84,v85
v_cvt_pk_bf16_f32 v83,v86,v87
v_exp_f32 v88,v88
v_exp_f32 v89,v89
v_exp_f32 v90,v90
v_exp_f32 v91,v91
v_exp_f32 v92,v92
v_exp_f32 v93,v93
v_exp_f32 v94,v94
v_exp_f32 v95,v95
v_mfma_f32_16x16x32_bf16 v[124:127],a[180:183],v[232:235],v[124:127]
v_cvt_pk_bf16_f32 v88,v88,v89
v_cvt_pk_bf16_f32 v89,v90,v91
v_cvt_pk_bf16_f32 v90,v92,v93
v_cvt_pk_bf16_f32 v91,v94,v95
v_mfma_f32_16x16x32_bf16 v[124:127],a[176:179],v[224:227],v[124:127]
s_add_u32 s10,s20,s74
s_addc_u32 s11,s21,0
s_add_u32 m0,s76,0
v_mfma_f32_16x16x32_bf16 v[132:135],a[192:195],v[80:83],v[132:135]
global_load_lds_dwordx4 v4,s[10:11] nt
v_add_u32 v1,s78,v1
v_mfma_f32_16x16x32_bf16 v[132:135],a[196:199],v[88:91],v[132:135]
ds_read_b128 a[128:131],v1
v_accvgpr_read_b32 v16,a120
v_mfma_f32_16x16x32_bf16 v[140:143],a[200:203],v[80:83],v[140:143]
v_exp_f32 v96,v96
v_mfma_f32_16x16x32_bf16 v[140:143],a[204:207],v[88:91],v[140:143]
ds_read_b128 a[132:135],v1 offset:256
v_accvgpr_read_b32 v17,a121
v_mfma_f32_16x16x32_bf16 v[148:151],a[208:211],v[80:83],v[148:151]
v_exp_f32 v97,v97
v_mfma_f32_16x16x32_bf16 v[148:151],a[212:215],v[88:91],v[148:151]
ds_read_b128 a[136:139],v1 offset:512
v_cvt_pk_bf16_f32 v96,v96,v97
v_mfma_f32_16x16x32_bf16 v[156:159],a[216:219],v[80:83],v[156:159]
v_exp_f32 v98,v98
v_mfma_f32_16x16x32_bf16 v[156:159],a[220:223],v[88:91],v[156:159]
ds_read_b128 a[140:143],v1 offset:768
v_accvgpr_read_b32 v18,a122
v_mfma_f32_16x16x32_bf16 v[164:167],a[224:227],v[80:83],v[164:167]
v_exp_f32 v99,v99
v_mfma_f32_16x16x32_bf16 v[164:167],a[228:231],v[88:91],v[164:167]
ds_read_b128 a[156:159],v1 offset:4864
v_cvt_pk_bf16_f32 v97,v98,v99
v_mfma_f32_16x16x32_bf16 v[172:175],a[232:235],v[80:83],v[172:175]
v_exp_f32 v100,v100
v_mfma_f32_16x16x32_bf16 v[172:175],a[236:239],v[88:91],v[172:175]
ds_read_b128 a[152:155],v1 offset:4608
v_accvgpr_read_b32 v19,a123
v_mfma_f32_16x16x32_bf16 v[180:183],a[240:243],v[80:83],v[180:183]
v_exp_f32 v101,v101
v_mfma_f32_16x16x32_bf16 v[180:183],a[244:247],v[88:91],v[180:183]
ds_read_b128 a[148:151],v1 offset:4352
v_cvt_pk_bf16_f32 v98,v100,v101
v_mfma_f32_16x16x32_bf16 v[188:191],a[248:251],v[80:83],v[188:191]
global_load_lds_dwordx4 v4,s[10:11] offset:1024 nt
v_accvgpr_read_b32 v20,a124
v_mfma_f32_16x16x32_bf16 v[188:191],a[252:255],v[88:91],v[188:191]
ds_read_b128 a[144:147],v1 offset:4096
v_accvgpr_read_b32 v21,a125
v_mfma_f32_16x16x32_bf16 v[52:55],v[28:31],v[80:83],v[52:55]
v_exp_f32 v102,v102
v_mfma_f32_16x16x32_bf16 v[52:55],v[28:31],v[88:91],v[52:55]
s_waitcnt lgkmcnt(3)
ds_read_b128 a[160:163],v1 offset:8192
v_accvgpr_read_b32 v22,a126
v_mfma_f32_16x16x32_bf16 v[64:67],a[128:131],v[128:131],v[32:35]
v_exp_f32 v103,v103
v_mfma_f32_16x16x32_bf16 v[64:67],a[132:135],v[136:139],v[64:67]
ds_read_b128 a[164:167],v1 offset:8448
v_cvt_pk_bf16_f32 v99,v102,v103
v_mfma_f32_16x16x32_bf16 v[64:67],a[136:139],v[144:147],v[64:67]
v_exp_f32 v104,v104
v_mfma_f32_16x16x32_bf16 v[64:67],a[140:143],v[152:155],v[64:67]
ds_read_b128 a[168:171],v1 offset:8704
v_accvgpr_read_b32 v23,a127
v_mfma_f32_16x16x32_bf16 v[68:71],a[156:159],v[152:155],v[32:35]
s_waitcnt lgkmcnt(4)
v_exp_f32 v105,v105
v_mfma_f32_16x16x32_bf16 v[68:71],a[152:155],v[144:147],v[68:71]
ds_read_b128 a[172:175],v1 offset:8960
v_cvt_pk_bf16_f32 v104,v104,v105
v_mfma_f32_16x16x32_bf16 v[68:71],a[148:151],v[136:139],v[68:71]
s_waitcnt lgkmcnt(4)
v_exp_f32 v106,v106
v_mfma_f32_16x16x32_bf16 v[68:71],a[144:147],v[128:131],v[68:71]
s_waitcnt lgkmcnt(3)
ds_read_b128 a[188:191],v1 offset:13056
v_mul_f32 v16,v16,v27
v_mfma_f32_16x16x32_bf16 v[72:75],a[160:163],v[128:131],v[32:35]
s_waitcnt lgkmcnt(3)
v_exp_f32 v107,v107
v_mfma_f32_16x16x32_bf16 v[72:75],a[164:167],v[136:139],v[72:75]
s_waitcnt lgkmcnt(1)
ds_read_b128 a[184:187],v1 offset:12800
v_cvt_pk_bf16_f32 v105,v106,v107
v_mfma_f32_16x16x32_bf16 v[72:75],a[168:171],v[144:147],v[72:75]
global_load_lds_dwordx4 v5,s[10:11] offset:2048 nt
ds_read_b128 a[180:183],v1 offset:12544
v_mfma_f32_16x16x32_bf16 v[72:75],a[172:175],v[152:155],v[72:75]
s_waitcnt lgkmcnt(2)
ds_read_b128 a[176:179],v1 offset:12288
v_mul_f32 v17,v17,v27
v_mfma_f32_16x16x32_bf16 v[76:79],a[188:191],v[152:155],v[32:35]
s_waitcnt lgkmcnt(2)
v_exp_f32 v108,v108
v_mfma_f32_16x16x32_bf16 v[76:79],a[184:187],v[144:147],v[76:79]
s_waitcnt lgkmcnt(1)
v_exp_f32 v109,v109
v_exp_f32 v110,v110
v_exp_f32 v111,v111
v_mfma_f32_16x16x32_bf16 v[76:79],a[180:183],v[136:139],v[76:79]
s_waitcnt lgkmcnt(0)
v_cvt_pk_bf16_f32 v106,v108,v109
v_cvt_pk_bf16_f32 v107,v110,v111
v_mfma_f32_16x16x32_bf16 v[76:79],a[176:179],v[128:131],v[76:79]
v_mul_f32 v18,v18,v27
v_mul_f32 v19,v19,v27
v_mfma_f32_16x16x32_bf16 v[196:199],a[192:195],v[96:99],v[196:199]
v_mul_f32 v20,v20,v27
v_mul_f32 v21,v21,v27
v_mfma_f32_16x16x32_bf16 v[196:199],a[196:199],v[104:107],v[196:199]
v_mul_f32 v22,v22,v27
v_mul_f32 v23,v23,v27
v_mfma_f32_16x16x32_bf16 v[204:207],a[200:203],v[96:99],v[204:207]
v_cvt_pk_bf16_f32 v16,v16,v17
v_cvt_pk_bf16_f32 v17,v18,v19
v_mfma_f32_16x16x32_bf16 v[204:207],a[204:207],v[104:107],v[204:207]
v_cvt_pk_bf16_f32 v18,v20,v21
v_cvt_pk_bf16_f32 v19,v22,v23
v_mfma_f32_16x16x32_bf16 v[212:215],a[208:211],v[96:99],v[212:215]
v_accvgpr_write_b32 a120,v16
v_accvgpr_write_b32 a121,v17
v_mfma_f32_16x16x32_bf16 v[212:215],a[212:215],v[104:107],v[212:215]
v_accvgpr_write_b32 a122,v18
v_accvgpr_write_b32 a123,v19
v_mfma_f32_16x16x32_bf16 v[220:223],a[216:219],v[96:99],v[220:223]
v_accvgpr_write_b32 a124,v12
v_accvgpr_write_b32 a125,v13
v_mfma_f32_16x16x32_bf16 v[220:223],a[220:223],v[104:107],v[220:223]
v_accvgpr_write_b32 a126,v14
v_accvgpr_write_b32 a127,v15
v_mfma_f32_16x16x32_bf16 v[228:231],a[224:227],v[96:99],v[228:231]
global_load_lds_dwordx4 v5,s[10:11] offset:3072
v_add_u32 v2,s78,v2
v_mfma_f32_16x16x32_bf16 v[228:231],a[228:231],v[104:107],v[228:231]
v_exp_f32 v112,v112
v_mfma_f32_16x16x32_bf16 v[236:239],a[232:235],v[96:99],v[236:239]
v_exp_f32 v113,v113
v_mfma_f32_16x16x32_bf16 v[236:239],a[236:239],v[104:107],v[236:239]
v_exp_f32 v114,v114
v_mfma_f32_16x16x32_bf16 v[244:247],a[240:243],v[96:99],v[244:247]
v_exp_f32 v115,v115
v_mfma_f32_16x16x32_bf16 v[244:247],a[244:247],v[104:107],v[244:247]
v_exp_f32 v116,v116
v_mfma_f32_16x16x32_bf16 v[252:255],a[248:251],v[96:99],v[252:255]
v_exp_f32 v117,v117
v_mfma_f32_16x16x32_bf16 v[252:255],a[252:255],v[104:107],v[252:255]
v_exp_f32 v118,v118
v_mfma_f32_16x16x32_bf16 v[56:59],v[28:31],v[96:99],v[56:59]
v_exp_f32 v119,v119
v_mfma_f32_16x16x32_bf16 v[56:59],v[28:31],v[104:107],v[56:59]
v_cvt_pk_bf16_f32 v112,v112,v113
v_cvt_pk_bf16_f32 v113,v114,v115
v_mfma_f32_16x16x32_bf16 v[80:83],a[128:131],v[160:163],v[36:39]
v_cvt_pk_bf16_f32 v114,v116,v117
v_cvt_pk_bf16_f32 v115,v118,v119
v_mfma_f32_16x16x32_bf16 v[80:83],a[132:135],v[168:171],v[80:83]
s_add_u32 s10,s24,s74
s_addc_u32 s11,s25,0
v_add_u32 v3,s78,v3
v_mfma_f32_16x16x32_bf16 v[80:83],a[136:139],v[176:179],v[80:83]
s_add_u32 s74,s74,16384
v_exp_f32 v120,v120
v_mfma_f32_16x16x32_bf16 v[80:83],a[140:143],v[184:187],v[80:83]
s_add_u32 m0,s76,16384
v_exp_f32 v121,v121
v_mfma_f32_16x16x32_bf16 v[84:87],a[156:159],v[184:187],v[36:39]
s_and_b32 s74,s74,0x3ffff
v_exp_f32 v122,v122
v_mfma_f32_16x16x32_bf16 v[84:87],a[152:155],v[176:179],v[84:87]
global_load_lds_dwordx4 v4,s[10:11]
v_cvt_pk_bf16_f32 v120,v120,v121
v_mfma_f32_16x16x32_bf16 v[84:87],a[148:151],v[168:171],v[84:87]
s_or_b32 s74,s74,s69
v_exp_f32 v123,v123
v_mfma_f32_16x16x32_bf16 v[84:87],a[144:147],v[160:163],v[84:87]
s_sub_u32 s76,s76,32768
v_exp_f32 v124,v124
v_mfma_f32_16x16x32_bf16 v[88:91],a[160:163],v[160:163],v[36:39]
s_and_b32 s76,s76,0x1ffff
v_exp_f32 v125,v125
v_mfma_f32_16x16x32_bf16 v[88:91],a[164:167],v[168:171],v[88:91]
s_sub_u32 s78,s77,32768
v_exp_f32 v126,v126
v_mfma_f32_16x16x32_bf16 v[88:91],a[168:171],v[176:179],v[88:91]
s_and_b32 s78,s78,0x1ffff
v_exp_f32 v127,v127
v_mfma_f32_16x16x32_bf16 v[88:91],a[172:175],v[184:187],v[88:91]
s_sub_u32 s78,s78,s77
v_cvt_pk_bf16_f32 v121,v122,v123
v_cvt_pk_bf16_f32 v122,v124,v125
v_mfma_f32_16x16x32_bf16 v[92:95],a[188:191],v[184:187],v[36:39]
s_and_b32 s69,s75,0xfffc0000
v_cvt_pk_bf16_f32 v123,v126,v127
v_accvgpr_read_b32 v12,a100
v_mfma_f32_16x16x32_bf16 v[92:95],a[184:187],v[176:179],v[92:95]
s_add_u32 s77,s77,s78
v_exp_f32 v64,v64
v_mfma_f32_16x16x32_bf16 v[92:95],a[180:183],v[168:171],v[92:95]
s_add_u32 s75,s75,16384
v_exp_f32 v65,v65
v_mfma_f32_16x16x32_bf16 v[92:95],a[176:179],v[160:163],v[92:95]
v_exp_f32 v66,v66
v_mfma_f32_16x16x32_bf16 a[32:35],a[192:195],v[112:115],a[32:35]
ds_read_b64_tr_b16 a[192:193],v2
ds_read_b64_tr_b16 a[194:195],v2 offset:4096
v_mfma_f32_16x16x32_bf16 a[32:35],a[196:199],v[120:123],a[32:35]
ds_read_b64_tr_b16 a[196:197],v2 offset:8192
v_cvt_pk_bf16_f32 v64,v64,v65
v_mfma_f32_16x16x32_bf16 a[36:39],a[200:203],v[112:115],a[36:39]
ds_read_b64_tr_b16 a[198:199],v2 offset:12288
v_accvgpr_read_b32 v13,a101
v_mfma_f32_16x16x32_bf16 a[36:39],a[204:207],v[120:123],a[36:39]
global_load_lds_dwordx4 v5,s[10:11] offset:1024
ds_read_b64_tr_b16 a[200:201],v3
v_mfma_f32_16x16x32_bf16 a[40:43],a[208:211],v[112:115],a[40:43]
ds_read_b64_tr_b16 a[202:203],v3 offset:4096
ds_read_b64_tr_b16 a[204:205],v3 offset:8192
v_mfma_f32_16x16x32_bf16 a[40:43],a[212:215],v[120:123],a[40:43]
ds_read_b64_tr_b16 a[206:207],v3 offset:12288
v_accvgpr_read_b32 v14,a102
v_mfma_f32_16x16x32_bf16 a[44:47],a[216:219],v[112:115],a[44:47]
ds_read_b64_tr_b16 a[208:209],v2 offset:256
v_accvgpr_read_b32 v15,a103
v_mfma_f32_16x16x32_bf16 a[44:47],a[220:223],v[120:123],a[44:47]
ds_read_b64_tr_b16 a[210:211],v2 offset:4352
v_accvgpr_read_b32 v8,a96
v_mfma_f32_16x16x32_bf16 a[48:51],a[224:227],v[112:115],a[48:51]
ds_read_b64_tr_b16 a[212:213],v2 offset:8448
ds_read_b64_tr_b16 a[214:215],v2 offset:12544
v_mfma_f32_16x16x32_bf16 a[48:51],a[228:231],v[120:123],a[48:51]
v_exp_f32 v67,v67
v_mfma_f32_16x16x32_bf16 a[52:55],a[232:235],v[112:115],a[52:55]
v_exp_f32 v68,v68
v_mfma_f32_16x16x32_bf16 a[52:55],a[236:239],v[120:123],a[52:55]
v_exp_f32 v69,v69
v_mfma_f32_16x16x32_bf16 a[56:59],a[240:243],v[112:115],a[56:59]
v_exp_f32 v70,v70
v_mfma_f32_16x16x32_bf16 a[56:59],a[244:247],v[120:123],a[56:59]
v_exp_f32 v71,v71
v_mfma_f32_16x16x32_bf16 a[60:63],a[248:251],v[112:115],a[60:63]
ds_read_b64_tr_b16 a[216:217],v3 offset:256
ds_read_b64_tr_b16 a[218:219],v3 offset:4352
v_mfma_f32_16x16x32_bf16 a[60:63],a[252:255],v[120:123],a[60:63]
v_cvt_pk_bf16_f32 v65,v66,v67
v_cvt_pk_bf16_f32 v66,v68,v69
)A"
R"A(v_mfma_f32_16x16x32_bf16 v[60:63],v[28:31],v[112:115],v[60:63]
ds_read_b64_tr_b16 a[220:221],v3 offset:8448
v_cvt_pk_bf16_f32 v67,v70,v71
v_mfma_f32_16x16x32_bf16 v[60:63],v[28:31],v[120:123],v[60:63]
global_load_lds_dwordx4 v4,s[10:11] offset:2048
ds_read_b64_tr_b16 a[222:223],v3 offset:12544
v_mfma_f32_16x16x32_bf16 v[96:99],a[128:131],v[192:195],v[40:43]
v_exp_f32 v72,v72
v_mfma_f32_16x16x32_bf16 v[96:99],a[132:135],v[200:203],v[96:99]
v_exp_f32 v73,v73
v_mfma_f32_16x16x32_bf16 v[96:99],a[136:139],v[208:211],v[96:99]
v_exp_f32 v74,v74
v_mfma_f32_16x16x32_bf16 v[96:99],a[140:143],v[216:219],v[96:99]
v_exp_f32 v75,v75
v_mfma_f32_16x16x32_bf16 v[100:103],a[156:159],v[216:219],v[40:43]
v_exp_f32 v76,v76
v_mfma_f32_16x16x32_bf16 v[100:103],a[152:155],v[208:211],v[100:103]
v_exp_f32 v77,v77
v_mfma_f32_16x16x32_bf16 v[100:103],a[148:151],v[200:203],v[100:103]
v_exp_f32 v78,v78
v_mfma_f32_16x16x32_bf16 v[100:103],a[144:147],v[192:195],v[100:103]
v_exp_f32 v79,v79
v_mfma_f32_16x16x32_bf16 v[104:107],a[160:163],v[192:195],v[40:43]
v_cvt_pk_bf16_f32 v72,v72,v73
v_cvt_pk_bf16_f32 v73,v74,v75
v_mfma_f32_16x16x32_bf16 v[104:107],a[164:167],v[200:203],v[104:107]
v_cvt_pk_bf16_f32 v74,v76,v77
v_cvt_pk_bf16_f32 v75,v78,v79
v_mfma_f32_16x16x32_bf16 v[104:107],a[168:171],v[208:211],v[104:107]
ds_read_b64_tr_b16 a[224:225],v2 offset:512
ds_read_b64_tr_b16 a[226:227],v2 offset:4608
v_mfma_f32_16x16x32_bf16 v[104:107],a[172:175],v[216:219],v[104:107]
v_accvgpr_read_b32 v9,a97
v_accvgpr_read_b32 v10,a98
v_mfma_f32_16x16x32_bf16 v[108:111],a[188:191],v[216:219],v[40:43]
v_accvgpr_read_b32 v11,a99
v_mul_f32 v8,v8,v27
v_mfma_f32_16x16x32_bf16 v[108:111],a[184:187],v[208:211],v[108:111]
global_load_lds_dwordx4 v5,s[10:11] offset:3072
v_mul_f32 v9,v9,v27
v_mfma_f32_16x16x32_bf16 v[108:111],a[180:183],v[200:203],v[108:111]
v_mul_f32 v10,v10,v27
v_mul_f32 v11,v11,v27
v_mfma_f32_16x16x32_bf16 v[108:111],a[176:179],v[192:195],v[108:111]
s_waitcnt lgkmcnt(2)
v_mul_f32 v12,v12,v27
v_mul_f32 v13,v13,v27
v_mul_f32 v14,v14,v27
v_mul_f32 v15,v15,v27
v_cvt_pk_bf16_f32 v8,v8,v9
v_cvt_pk_bf16_f32 v9,v10,v11
v_cvt_pk_bf16_f32 v10,v12,v13
v_cvt_pk_bf16_f32 v11,v14,v15
v_accvgpr_write_b32 a96,v8
v_accvgpr_write_b32 a97,v9
v_accvgpr_write_b32 a98,v10
v_accvgpr_write_b32 a99,v11
v_mfma_f32_16x16x32_bf16 a[0:3],a[192:195],v[64:67],a[0:3]
ds_read_b128 a[100:103],v7 offset:2048
v_add_u32 v1,s78,v1
v_mfma_f32_16x16x32_bf16 a[0:3],a[196:199],v[72:75],a[0:3]
ds_read_b64_tr_b16 a[228:229],v2 offset:8704
v_accvgpr_read_b32 v20,a108
v_mfma_f32_16x16x32_bf16 a[4:7],a[200:203],v[64:67],a[4:7]
ds_read_b64_tr_b16 a[230:231],v2 offset:12800
v_accvgpr_read_b32 v21,a109
v_mfma_f32_16x16x32_bf16 a[4:7],a[204:207],v[72:75],a[4:7]
ds_read_b64_tr_b16 a[232:233],v3 offset:512
v_accvgpr_read_b32 v22,a110
v_mfma_f32_16x16x32_bf16 a[8:11],a[208:211],v[64:67],a[8:11]
ds_read_b64_tr_b16 a[234:235],v3 offset:4608
ds_read_b64_tr_b16 a[236:237],v3 offset:8704
v_mfma_f32_16x16x32_bf16 a[8:11],a[212:215],v[72:75],a[8:11]
ds_read_b64_tr_b16 a[238:239],v3 offset:12800
v_accvgpr_read_b32 v23,a111
v_mfma_f32_16x16x32_bf16 a[12:15],a[216:219],v[64:67],a[12:15]
ds_read_b64_tr_b16 a[240:241],v2 offset:768
v_accvgpr_read_b32 v16,a104
v_mfma_f32_16x16x32_bf16 a[12:15],a[220:223],v[72:75],a[12:15]
s_waitcnt lgkmcnt(8)
ds_read_b64_tr_b16 a[242:243],v2 offset:4864
v_accvgpr_read_b32 v17,a105
v_mfma_f32_16x16x32_bf16 a[16:19],a[224:227],v[64:67],a[16:19]
s_waitcnt lgkmcnt(6)
ds_read_b64_tr_b16 a[244:245],v2 offset:8960
ds_read_b64_tr_b16 a[246:247],v2 offset:13056
v_mfma_f32_16x16x32_bf16 a[16:19],a[228:231],v[72:75],a[16:19]
s_waitcnt lgkmcnt(0)
ds_read_b64_tr_b16 a[248:249],v3 offset:768
v_accvgpr_read_b32 v18,a106
v_mfma_f32_16x16x32_bf16 a[20:23],a[232:235],v[64:67],a[20:23]
s_getreg_b32 s92,hwreg(HW_REG_IB_STS,0,4)
s_min_u32 s4,s92,s6
ds_read_b64_tr_b16 a[250:251],v3 offset:4864
v_mfma_f32_16x16x32_bf16 a[20:23],a[236:239],v[72:75],a[20:23]
s_mov_b32 s6,s92
ds_read_b64_tr_b16 a[252:253],v3 offset:8960
ds_read_b64_tr_b16 a[254:255],v3 offset:13056
v_mfma_f32_16x16x32_bf16 a[24:27],a[240:243],v[64:67],a[24:27]
s_cmp_gt_u32 s4,s9
s_cselect_b32 s4,15,s5
v_accvgpr_read_b32 v19,a107
v_mfma_f32_16x16x32_bf16 a[24:27],a[244:247],v[72:75],a[24:27]
s_waitcnt lgkmcnt(2)
v_exp_f32 v80,v80
v_mfma_f32_16x16x32_bf16 a[28:31],a[248:251],v[64:67],a[28:31]
s_waitcnt lgkmcnt(0)
v_exp_f32 v81,v81
v_mfma_f32_16x16x32_bf16 a[28:31],a[252:255],v[72:75],a[28:31]
s_getpc_b64 s[92:93]
s_cmp_lg_u32 s4,0
s_cbranch_scc1 L_pace_%=
v_mfma_f32_16x16x32_bf16 v[48:51],v[28:31],v[64:67],v[48:51]
v_exp_f32 v82,v82
v_mfma_f32_16x16x32_bf16 v[48:51],v[28:31],v[72:75],v[48:51]
s_add_u32 s10,s20,s74
s_addc_u32 s11,s21,0
v_cvt_pk_bf16_f32 v80,v80,v81
s_waitcnt vmcnt(16)
s_barrier
v_mfma_f32_16x16x32_bf16 v[112:115],a[128:131],v[224:227],v[44:47]
s_add_u32 m0,s76,0
v_exp_f32 v83,v83
v_mfma_f32_16x16x32_bf16 v[112:115],a[132:135],v[232:235],v[112:115]
v_exp_f32 v84,v84
v_mfma_f32_16x16x32_bf16 v[112:115],a[136:139],v[240:243],v[112:115]
global_load_lds_dwordx4 v4,s[10:11] nt
v_cvt_pk_bf16_f32 v81,v82,v83
v_mfma_f32_16x16x32_bf16 v[112:115],a[140:143],v[248:251],v[112:115]
v_exp_f32 v85,v85
v_mfma_f32_16x16x32_bf16 v[116:119],a[156:159],v[248:251],v[44:47]
v_exp_f32 v86,v86
v_mfma_f32_16x16x32_bf16 v[116:119],a[152:155],v[240:243],v[116:119]
v_exp_f32 v87,v87
v_mfma_f32_16x16x32_bf16 v[116:119],a[148:151],v[232:235],v[116:119]
v_cvt_pk_bf16_f32 v82,v84,v85
v_cvt_pk_bf16_f32 v83,v86,v87
v_mfma_f32_16x16x32_bf16 v[116:119],a[144:147],v[224:227],v[116:119]
v_exp_f32 v88,v88
v_mfma_f32_16x16x32_bf16 v[120:123],a[160:163],v[224:227],v[44:47]
v_exp_f32 v89,v89
v_mfma_f32_16x16x32_bf16 v[120:123],a[164:167],v[232:235],v[120:123]
v_exp_f32 v90,v90
v_mfma_f32_16x16x32_bf16 v[120:123],a[168:171],v[240:243],v[120:123]
v_exp_f32 v91,v91
v_mfma_f32_16x16x32_bf16 v[120:123],a[172:175],v[248:251],v[120:123]
v_exp_f32 v92,v92
v_mfma_f32_16x16x32_bf16 v[124:127],a[188:191],v[248:251],v[44:47]
v_exp_f32 v93,v93
v_mfma_f32_16x16x32_bf16 v[124:127],a[184:187],v[240:243],v[124:127]
v_exp_f32 v94,v94
v_exp_f32 v95,v95
v_mfma_f32_16x16x32_bf16 v[124:127],a[180:183],v[232:235],v[124:127]
v_cvt_pk_bf16_f32 v88,v88,v89
v_cvt_pk_bf16_f32 v89,v90,v91
v_cvt_pk_bf16_f32 v90,v92,v93
v_cvt_pk_bf16_f32 v91,v94,v95
v_mfma_f32_16x16x32_bf16 v[124:127],a[176:179],v[224:227],v[124:127]
ds_read_b128 a[128:131],v1
v_mul_f32 v16,v16,v27
v_mfma_f32_16x16x32_bf16 v[132:135],a[192:195],v[80:83],v[132:135]
v_mul_f32 v17,v17,v27
v_mul_f32 v18,v18,v27
v_mfma_f32_16x16x32_bf16 v[132:135],a[196:199],v[88:91],v[132:135]
ds_read_b128 a[132:135],v1 offset:256
v_mul_f32 v19,v19,v27
v_mfma_f32_16x16x32_bf16 v[140:143],a[200:203],v[80:83],v[140:143]
global_load_lds_dwordx4 v4,s[10:11] offset:1024 nt
v_mul_f32 v20,v20,v27
v_mfma_f32_16x16x32_bf16 v[140:143],a[204:207],v[88:91],v[140:143]
ds_read_b128 a[136:139],v1 offset:512
v_mul_f32 v21,v21,v27
v_mfma_f32_16x16x32_bf16 v[148:151],a[208:211],v[80:83],v[148:151]
v_mul_f32 v22,v22,v27
v_mul_f32 v23,v23,v27
v_mfma_f32_16x16x32_bf16 v[148:151],a[212:215],v[88:91],v[148:151]
ds_read_b128 a[140:143],v1 offset:768
v_cvt_pk_bf16_f32 v16,v16,v17
v_mfma_f32_16x16x32_bf16 v[156:159],a[216:219],v[80:83],v[156:159]
v_cvt_pk_bf16_f32 v17,v18,v19
v_cvt_pk_bf16_f32 v18,v20,v21
v_mfma_f32_16x16x32_bf16 v[156:159],a[220:223],v[88:91],v[156:159]
ds_read_b128 a[156:159],v1 offset:4864
v_cvt_pk_bf16_f32 v19,v22,v23
v_mfma_f32_16x16x32_bf16 v[164:167],a[224:227],v[80:83],v[164:167]
v_accvgpr_write_b32 a104,v16
v_accvgpr_write_b32 a105,v17
v_mfma_f32_16x16x32_bf16 v[164:167],a[228:231],v[88:91],v[164:167]
ds_read_b128 a[152:155],v1 offset:4608
v_accvgpr_write_b32 a106,v18
v_mfma_f32_16x16x32_bf16 v[172:175],a[232:235],v[80:83],v[172:175]
v_accvgpr_write_b32 a107,v19
v_accvgpr_read_b32 v12,a68
v_mfma_f32_16x16x32_bf16 v[172:175],a[236:239],v[88:91],v[172:175]
ds_read_b128 a[148:151],v1 offset:4352
v_accvgpr_read_b32 v13,a69
v_mfma_f32_16x16x32_bf16 v[180:183],a[240:243],v[80:83],v[180:183]
v_exp_f32 v96,v96
v_mfma_f32_16x16x32_bf16 v[180:183],a[244:247],v[88:91],v[180:183]
ds_read_b128 a[144:147],v1 offset:4096
v_accvgpr_read_b32 v14,a70
v_mfma_f32_16x16x32_bf16 v[188:191],a[248:251],v[80:83],v[188:191]
v_exp_f32 v97,v97
v_mfma_f32_16x16x32_bf16 v[188:191],a[252:255],v[88:91],v[188:191]
ds_read_b128 a[160:163],v1 offset:8192
v_cvt_pk_bf16_f32 v96,v96,v97
v_mfma_f32_16x16x32_bf16 v[52:55],v[28:31],v[80:83],v[52:55]
v_exp_f32 v98,v98
v_mfma_f32_16x16x32_bf16 v[52:55],v[28:31],v[88:91],v[52:55]
s_waitcnt lgkmcnt(3)
ds_read_b128 a[164:167],v1 offset:8448
v_accvgpr_read_b32 v15,a71
s_nop 0
L_pd9_%=:
s_cbranch_execz L_pd13_%=
v_mfma_f32_16x16x32_bf16 v[64:67],a[128:131],v[128:131],v[32:35]
global_load_lds_dwordx4 v5,s[10:11] offset:2048 nt
v_accvgpr_read_b32 v8,a64
v_mfma_f32_16x16x32_bf16 v[64:67],a[132:135],v[136:139],v[64:67]
ds_read_b128 a[168:171],v1 offset:8704
v_accvgpr_read_b32 v9,a65
v_mfma_f32_16x16x32_bf16 v[64:67],a[136:139],v[144:147],v[64:67]
v_exp_f32 v99,v99
v_mfma_f32_16x16x32_bf16 v[64:67],a[140:143],v[152:155],v[64:67]
ds_read_b128 a[108:111],v7 offset:3072
v_cvt_pk_bf16_f32 v97,v98,v99
v_mfma_f32_16x16x32_bf16 v[68:71],a[156:159],v[152:155],v[32:35]
v_exp_f32 v100,v100
v_mfma_f32_16x16x32_bf16 v[68:71],a[152:155],v[144:147],v[68:71]
s_waitcnt lgkmcnt(3)
ds_read_b128 a[172:175],v1 offset:8960
v_accvgpr_read_b32 v10,a66
v_mfma_f32_16x16x32_bf16 v[68:71],a[148:151],v[136:139],v[68:71]
v_exp_f32 v101,v101
v_mfma_f32_16x16x32_bf16 v[68:71],a[144:147],v[128:131],v[68:71]
ds_read_b128 a[188:191],v1 offset:13056
v_cvt_pk_bf16_f32 v98,v100,v101
v_mfma_f32_16x16x32_bf16 v[72:75],a[160:163],v[128:131],v[32:35]
s_waitcnt lgkmcnt(4)
v_exp_f32 v102,v102
v_mfma_f32_16x16x32_bf16 v[72:75],a[164:167],v[136:139],v[72:75]
s_waitcnt lgkmcnt(3)
ds_read_b128 a[184:187],v1 offset:12800
v_accvgpr_read_b32 v11,a67
v_mfma_f32_16x16x32_bf16 v[72:75],a[168:171],v[144:147],v[72:75]
s_waitcnt lgkmcnt(2)
ds_read_b128 a[180:183],v1 offset:12544
v_mul_f32 v8,v8,v26
v_mfma_f32_16x16x32_bf16 v[72:75],a[172:175],v[152:155],v[72:75]
s_waitcnt lgkmcnt(2)
ds_read_b128 a[176:179],v1 offset:12288
v_mul_f32 v9,v9,v26
v_mfma_f32_16x16x32_bf16 v[76:79],a[188:191],v[152:155],v[32:35]
s_waitcnt lgkmcnt(2)
v_exp_f32 v103,v103
v_mfma_f32_16x16x32_bf16 v[76:79],a[184:187],v[144:147],v[76:79]
s_waitcnt lgkmcnt(1)
v_cvt_pk_bf16_f32 v99,v102,v103
v_exp_f32 v104,v104
v_exp_f32 v105,v105
v_exp_f32 v106,v106
v_exp_f32 v107,v107
v_exp_f32 v108,v108
v_exp_f32 v109,v109
v_exp_f32 v110,v110
v_exp_f32 v111,v111
v_mfma_f32_16x16x32_bf16 v[76:79],a[180:183],v[136:139],v[76:79]
s_waitcnt lgkmcnt(0)
v_cvt_pk_bf16_f32 v104,v104,v105
v_cvt_pk_bf16_f32 v105,v106,v107
v_cvt_pk_bf16_f32 v106,v108,v109
v_cvt_pk_bf16_f32 v107,v110,v111
v_mfma_f32_16x16x32_bf16 v[76:79],a[176:179],v[128:131],v[76:79]
v_exp_f32 v112,v112
v_mfma_f32_16x16x32_bf16 v[196:199],a[192:195],v[96:99],v[196:199]
global_load_lds_dwordx4 v5,s[10:11] offset:3072
v_mul_f32 v10,v10,v26
v_mfma_f32_16x16x32_bf16 v[196:199],a[196:199],v[104:107],v[196:199]
v_exp_f32 v113,v113
v_mfma_f32_16x16x32_bf16 v[204:207],a[200:203],v[96:99],v[204:207]
v_exp_f32 v114,v114
v_mfma_f32_16x16x32_bf16 v[204:207],a[204:207],v[104:107],v[204:207]
v_exp_f32 v115,v115
v_mfma_f32_16x16x32_bf16 v[212:215],a[208:211],v[96:99],v[212:215]
v_exp_f32 v116,v116
v_mfma_f32_16x16x32_bf16 v[212:215],a[212:215],v[104:107],v[212:215]
v_exp_f32 v117,v117
v_mfma_f32_16x16x32_bf16 v[220:223],a[216:219],v[96:99],v[220:223]
v_exp_f32 v118,v118
v_mfma_f32_16x16x32_bf16 v[220:223],a[220:223],v[104:107],v[220:223]
v_exp_f32 v119,v119
v_mfma_f32_16x16x32_bf16 v[228:231],a[224:227],v[96:99],v[228:231]
v_cvt_pk_bf16_f32 v112,v112,v113
v_cvt_pk_bf16_f32 v113,v114,v115
v_mfma_f32_16x16x32_bf16 v[228:231],a[228:231],v[104:107],v[228:231]
v_cvt_pk_bf16_f32 v114,v116,v117
v_cvt_pk_bf16_f32 v115,v118,v119
v_mfma_f32_16x16x32_bf16 v[236:239],a[232:235],v[96:99],v[236:239]
v_exp_f32 v120,v120
v_mfma_f32_16x16x32_bf16 v[236:239],a[236:239],v[104:107],v[236:239]
v_exp_f32 v121,v121
)A"
R"A(v_mfma_f32_16x16x32_bf16 v[244:247],a[240:243],v[96:99],v[244:247]
s_add_u32 s10,s24,s74
s_addc_u32 s11,s25,0
v_cvt_pk_bf16_f32 v120,v120,v121
v_mfma_f32_16x16x32_bf16 v[244:247],a[244:247],v[104:107],v[244:247]
s_add_u32 s74,s74,16384
v_exp_f32 v122,v122
v_mfma_f32_16x16x32_bf16 v[252:255],a[248:251],v[96:99],v[252:255]
s_add_u32 m0,s76,16384
v_exp_f32 v123,v123
v_mfma_f32_16x16x32_bf16 v[252:255],a[252:255],v[104:107],v[252:255]
s_and_b32 s74,s74,0x3ffff
v_exp_f32 v124,v124
v_mfma_f32_16x16x32_bf16 v[56:59],v[28:31],v[96:99],v[56:59]
global_load_lds_dwordx4 v4,s[10:11]
v_cvt_pk_bf16_f32 v121,v122,v123
v_mfma_f32_16x16x32_bf16 v[56:59],v[28:31],v[104:107],v[56:59]
s_or_b32 s74,s74,s69
v_exp_f32 v125,v125
v_mfma_f32_16x16x32_bf16 v[80:83],a[128:131],v[160:163],v[36:39]
s_sub_u32 s76,s76,32768
v_exp_f32 v126,v126
v_mfma_f32_16x16x32_bf16 v[80:83],a[132:135],v[168:171],v[80:83]
s_and_b32 s76,s76,0x1ffff
v_exp_f32 v127,v127
v_mfma_f32_16x16x32_bf16 v[80:83],a[136:139],v[176:179],v[80:83]
s_and_b32 s69,s75,0xfffc0000
v_cvt_pk_bf16_f32 v122,v124,v125
v_cvt_pk_bf16_f32 v123,v126,v127
v_mfma_f32_16x16x32_bf16 v[80:83],a[140:143],v[184:187],v[80:83]
v_mul_f32 v11,v11,v26
v_mul_f32 v12,v12,v26
v_mfma_f32_16x16x32_bf16 v[84:87],a[156:159],v[184:187],v[36:39]
v_mul_f32 v13,v13,v26
v_mul_f32 v14,v14,v26
v_mfma_f32_16x16x32_bf16 v[84:87],a[152:155],v[176:179],v[84:87]
v_mul_f32 v15,v15,v26
v_cvt_pk_bf16_f32 v8,v8,v9
v_mfma_f32_16x16x32_bf16 v[84:87],a[148:151],v[168:171],v[84:87]
v_cvt_pk_bf16_f32 v9,v10,v11
v_cvt_pk_bf16_f32 v10,v12,v13
v_mfma_f32_16x16x32_bf16 v[84:87],a[144:147],v[160:163],v[84:87]
v_cvt_pk_bf16_f32 v11,v14,v15
v_accvgpr_write_b32 a64,v8
v_mfma_f32_16x16x32_bf16 v[88:91],a[160:163],v[160:163],v[36:39]
v_accvgpr_write_b32 a65,v9
v_accvgpr_write_b32 a66,v10
v_mfma_f32_16x16x32_bf16 v[88:91],a[164:167],v[168:171],v[88:91]
ds_read_b128 a[68:71],v7 offset:6144
v_accvgpr_write_b32 a67,v11
v_mfma_f32_16x16x32_bf16 v[88:91],a[168:171],v[176:179],v[88:91]
v_add_u32 v2,s78,v2
v_add_u32 v3,s78,v3
v_mfma_f32_16x16x32_bf16 v[88:91],a[172:175],v[184:187],v[88:91]
s_sub_u32 s78,s77,32768
v_exp_f32 v64,v64
v_mfma_f32_16x16x32_bf16 v[92:95],a[188:191],v[184:187],v[36:39]
s_and_b32 s78,s78,0x1ffff
v_exp_f32 v65,v65
v_mfma_f32_16x16x32_bf16 v[92:95],a[184:187],v[176:179],v[92:95]
s_sub_u32 s78,s78,s77
v_exp_f32 v66,v66
v_mfma_f32_16x16x32_bf16 v[92:95],a[180:183],v[168:171],v[92:95]
global_load_lds_dwordx4 v5,s[10:11] offset:1024
v_cvt_pk_bf16_f32 v64,v64,v65
v_mfma_f32_16x16x32_bf16 v[92:95],a[176:179],v[160:163],v[92:95]
s_add_u32 s77,s77,s78
v_exp_f32 v67,v67
v_mfma_f32_16x16x32_bf16 a[32:35],a[192:195],v[112:115],a[32:35]
ds_read_b64_tr_b16 a[192:193],v2
ds_read_b64_tr_b16 a[194:195],v2 offset:4096
v_mfma_f32_16x16x32_bf16 a[32:35],a[196:199],v[120:123],a[32:35]
ds_read_b64_tr_b16 a[196:197],v2 offset:8192
v_cvt_pk_bf16_f32 v65,v66,v67
v_mfma_f32_16x16x32_bf16 a[36:39],a[200:203],v[112:115],a[36:39]
ds_read_b64_tr_b16 a[198:199],v2 offset:12288
v_accvgpr_read_b32 v20,a76
v_mfma_f32_16x16x32_bf16 a[36:39],a[204:207],v[120:123],a[36:39]
ds_read_b64_tr_b16 a[200:201],v3
v_accvgpr_read_b32 v21,a77
v_mfma_f32_16x16x32_bf16 a[40:43],a[208:211],v[112:115],a[40:43]
ds_read_b64_tr_b16 a[202:203],v3 offset:4096
ds_read_b64_tr_b16 a[204:205],v3 offset:8192
v_mfma_f32_16x16x32_bf16 a[40:43],a[212:215],v[120:123],a[40:43]
ds_read_b64_tr_b16 a[206:207],v3 offset:12288
v_accvgpr_read_b32 v22,a78
v_mfma_f32_16x16x32_bf16 a[44:47],a[216:219],v[112:115],a[44:47]
ds_read_b64_tr_b16 a[208:209],v2 offset:256
v_accvgpr_read_b32 v23,a79
v_mfma_f32_16x16x32_bf16 a[44:47],a[220:223],v[120:123],a[44:47]
ds_read_b64_tr_b16 a[210:211],v2 offset:4352
v_accvgpr_read_b32 v16,a72
v_mfma_f32_16x16x32_bf16 a[48:51],a[224:227],v[112:115],a[48:51]
ds_read_b64_tr_b16 a[212:213],v2 offset:8448
ds_read_b64_tr_b16 a[214:215],v2 offset:12544
v_mfma_f32_16x16x32_bf16 a[48:51],a[228:231],v[120:123],a[48:51]
v_exp_f32 v68,v68
v_mfma_f32_16x16x32_bf16 a[52:55],a[232:235],v[112:115],a[52:55]
v_exp_f32 v69,v69
v_mfma_f32_16x16x32_bf16 a[52:55],a[236:239],v[120:123],a[52:55]
v_exp_f32 v70,v70
v_mfma_f32_16x16x32_bf16 a[56:59],a[240:243],v[112:115],a[56:59]
v_exp_f32 v71,v71
v_mfma_f32_16x16x32_bf16 a[56:59],a[244:247],v[120:123],a[56:59]
ds_read_b64_tr_b16 a[216:217],v3 offset:256
ds_read_b64_tr_b16 a[218:219],v3 offset:4352
v_mfma_f32_16x16x32_bf16 a[60:63],a[248:251],v[112:115],a[60:63]
global_load_lds_dwordx4 v4,s[10:11] offset:2048
v_cvt_pk_bf16_f32 v66,v68,v69
v_mfma_f32_16x16x32_bf16 a[60:63],a[252:255],v[120:123],a[60:63]
ds_read_b64_tr_b16 a[220:221],v3 offset:8448
v_cvt_pk_bf16_f32 v67,v70,v71
v_mfma_f32_16x16x32_bf16 v[60:63],v[28:31],v[112:115],v[60:63]
v_exp_f32 v72,v72
v_mfma_f32_16x16x32_bf16 v[60:63],v[28:31],v[120:123],v[60:63]
v_exp_f32 v73,v73
v_mfma_f32_16x16x32_bf16 v[96:99],a[128:131],v[192:195],v[40:43]
v_exp_f32 v74,v74
v_mfma_f32_16x16x32_bf16 v[96:99],a[132:135],v[200:203],v[96:99]
v_exp_f32 v75,v75
v_mfma_f32_16x16x32_bf16 v[96:99],a[136:139],v[208:211],v[96:99]
v_exp_f32 v76,v76
v_mfma_f32_16x16x32_bf16 v[96:99],a[140:143],v[216:219],v[96:99]
v_exp_f32 v77,v77
v_mfma_f32_16x16x32_bf16 v[100:103],a[156:159],v[216:219],v[40:43]
v_exp_f32 v78,v78
v_mfma_f32_16x16x32_bf16 v[100:103],a[152:155],v[208:211],v[100:103]
v_exp_f32 v79,v79
v_mfma_f32_16x16x32_bf16 v[100:103],a[148:151],v[200:203],v[100:103]
ds_read_b64_tr_b16 a[222:223],v3 offset:12544
v_cvt_pk_bf16_f32 v72,v72,v73
v_mfma_f32_16x16x32_bf16 v[100:103],a[144:147],v[192:195],v[100:103]
v_cvt_pk_bf16_f32 v73,v74,v75
v_cvt_pk_bf16_f32 v74,v76,v77
v_mfma_f32_16x16x32_bf16 v[104:107],a[160:163],v[192:195],v[40:43]
ds_read_b64_tr_b16 a[224:225],v2 offset:512
v_cvt_pk_bf16_f32 v75,v78,v79
v_mfma_f32_16x16x32_bf16 v[104:107],a[164:167],v[200:203],v[104:107]
ds_read_b64_tr_b16 a[226:227],v2 offset:4608
ds_read_b64_tr_b16 a[228:229],v2 offset:8704
v_mfma_f32_16x16x32_bf16 v[104:107],a[168:171],v[208:211],v[104:107]
ds_read_b64_tr_b16 a[230:231],v2 offset:12800
v_accvgpr_read_b32 v17,a73
v_mfma_f32_16x16x32_bf16 v[104:107],a[172:175],v[216:219],v[104:107]
ds_read_b64_tr_b16 a[232:233],v3 offset:512
v_accvgpr_read_b32 v18,a74
v_mfma_f32_16x16x32_bf16 v[108:111],a[188:191],v[216:219],v[40:43]
global_load_lds_dwordx4 v5,s[10:11] offset:3072
ds_read_b64_tr_b16 a[234:235],v3 offset:4608
v_mfma_f32_16x16x32_bf16 v[108:111],a[184:187],v[208:211],v[108:111]
ds_read_b64_tr_b16 a[236:237],v3 offset:8704
ds_read_b64_tr_b16 a[238:239],v3 offset:12800
v_mfma_f32_16x16x32_bf16 v[108:111],a[180:183],v[200:203],v[108:111]
ds_read_b64_tr_b16 a[240:241],v2 offset:768
v_accvgpr_read_b32 v19,a75
v_mfma_f32_16x16x32_bf16 v[108:111],a[176:179],v[192:195],v[108:111]
s_waitcnt lgkmcnt(9)
ds_read_b64_tr_b16 a[242:243],v2 offset:4864
v_mul_f32 v16,v16,v26
v_mfma_f32_16x16x32_bf16 a[0:3],a[192:195],v[64:67],a[0:3]
ds_read_b64_tr_b16 a[244:245],v2 offset:8960
v_mul_f32 v17,v17,v26
v_mfma_f32_16x16x32_bf16 a[0:3],a[196:199],v[72:75],a[0:3]
ds_read_b64_tr_b16 a[246:247],v2 offset:13056
ds_read_b64_tr_b16 a[248:249],v3 offset:768
v_mfma_f32_16x16x32_bf16 a[4:7],a[200:203],v[64:67],a[4:7]
ds_read_b64_tr_b16 a[250:251],v3 offset:4864
v_mul_f32 v18,v18,v26
v_mfma_f32_16x16x32_bf16 a[4:7],a[204:207],v[72:75],a[4:7]
ds_read_b64_tr_b16 a[252:253],v3 offset:8960
v_mul_f32 v19,v19,v26
v_mfma_f32_16x16x32_bf16 a[8:11],a[208:211],v[64:67],a[8:11]
ds_read_b64_tr_b16 a[254:255],v3 offset:13056
v_mul_f32 v20,v20,v26
v_mfma_f32_16x16x32_bf16 a[8:11],a[212:215],v[72:75],a[8:11]
v_exp_f32 v80,v80
v_mfma_f32_16x16x32_bf16 a[12:15],a[216:219],v[64:67],a[12:15]
v_exp_f32 v81,v81
v_mfma_f32_16x16x32_bf16 a[12:15],a[220:223],v[72:75],a[12:15]
s_waitcnt lgkmcnt(6)
v_exp_f32 v82,v82
v_mfma_f32_16x16x32_bf16 a[16:19],a[224:227],v[64:67],a[16:19]
v_exp_f32 v83,v83
v_mfma_f32_16x16x32_bf16 a[16:19],a[228:231],v[72:75],a[16:19]
v_exp_f32 v84,v84
v_mfma_f32_16x16x32_bf16 a[20:23],a[232:235],v[64:67],a[20:23]
s_getreg_b32 s92,hwreg(HW_REG_IB_STS,0,4)
s_min_u32 s4,s92,s6
s_mov_b32 s6,s92
v_mfma_f32_16x16x32_bf16 a[20:23],a[236:239],v[72:75],a[20:23]
s_cmp_ge_u32 s4,s9
s_cselect_b32 s4,15,s5
v_cvt_pk_bf16_f32 v80,v80,v81
v_mfma_f32_16x16x32_bf16 a[24:27],a[240:243],v[64:67],a[24:27]
s_waitcnt lgkmcnt(0)
v_exp_f32 v85,v85
v_mfma_f32_16x16x32_bf16 a[24:27],a[244:247],v[72:75],a[24:27]
v_exp_f32 v86,v86
v_mfma_f32_16x16x32_bf16 a[28:31],a[248:251],v[64:67],a[28:31]
v_exp_f32 v87,v87
v_mfma_f32_16x16x32_bf16 a[28:31],a[252:255],v[72:75],a[28:31]
s_getpc_b64 s[92:93]
s_cmp_lg_u32 s4,0
s_cbranch_scc1 L_pace_%=
v_mfma_f32_16x16x32_bf16 v[48:51],v[28:31],v[64:67],v[48:51]
v_cvt_pk_bf16_f32 v81,v82,v83
v_cvt_pk_bf16_f32 v82,v84,v85
v_mfma_f32_16x16x32_bf16 v[48:51],v[28:31],v[72:75],v[48:51]
s_add_u32 s10,s20,s74
s_addc_u32 s11,s21,0
v_cvt_pk_bf16_f32 v83,v86,v87
s_waitcnt vmcnt(16)
s_barrier
v_mfma_f32_16x16x32_bf16 v[112:115],a[128:131],v[224:227],v[44:47]
s_add_u32 m0,s76,0
v_exp_f32 v88,v88
v_mfma_f32_16x16x32_bf16 v[112:115],a[132:135],v[232:235],v[112:115]
v_exp_f32 v89,v89
v_mfma_f32_16x16x32_bf16 v[112:115],a[136:139],v[240:243],v[112:115]
global_load_lds_dwordx4 v4,s[10:11] nt
v_cvt_pk_bf16_f32 v88,v88,v89
v_mfma_f32_16x16x32_bf16 v[112:115],a[140:143],v[248:251],v[112:115]
v_exp_f32 v90,v90
v_mfma_f32_16x16x32_bf16 v[116:119],a[156:159],v[248:251],v[44:47]
v_exp_f32 v91,v91
v_mfma_f32_16x16x32_bf16 v[116:119],a[152:155],v[240:243],v[116:119]
v_exp_f32 v92,v92
v_mfma_f32_16x16x32_bf16 v[116:119],a[148:151],v[232:235],v[116:119]
v_exp_f32 v93,v93
v_mfma_f32_16x16x32_bf16 v[116:119],a[144:147],v[224:227],v[116:119]
v_exp_f32 v94,v94
v_mfma_f32_16x16x32_bf16 v[120:123],a[160:163],v[224:227],v[44:47]
v_exp_f32 v95,v95
v_mfma_f32_16x16x32_bf16 v[120:123],a[164:167],v[232:235],v[120:123]
v_cvt_pk_bf16_f32 v89,v90,v91
v_cvt_pk_bf16_f32 v90,v92,v93
v_mfma_f32_16x16x32_bf16 v[120:123],a[168:171],v[240:243],v[120:123]
v_cvt_pk_bf16_f32 v91,v94,v95
v_mul_f32 v21,v21,v26
v_mfma_f32_16x16x32_bf16 v[120:123],a[172:175],v[248:251],v[120:123]
v_mul_f32 v22,v22,v26
v_mul_f32 v23,v23,v26
v_mfma_f32_16x16x32_bf16 v[124:127],a[188:191],v[248:251],v[44:47]
v_cvt_pk_bf16_f32 v16,v16,v17
v_cvt_pk_bf16_f32 v17,v18,v19
v_mfma_f32_16x16x32_bf16 v[124:127],a[184:187],v[240:243],v[124:127]
v_cvt_pk_bf16_f32 v18,v20,v21
v_cvt_pk_bf16_f32 v19,v22,v23
v_mfma_f32_16x16x32_bf16 v[124:127],a[180:183],v[232:235],v[124:127]
v_accvgpr_write_b32 a72,v16
v_accvgpr_write_b32 a73,v17
v_mfma_f32_16x16x32_bf16 v[124:127],a[176:179],v[224:227],v[124:127]
v_accvgpr_write_b32 a74,v18
v_accvgpr_write_b32 a75,v19
v_mfma_f32_16x16x32_bf16 v[132:135],a[192:195],v[80:83],v[132:135]
ds_read_b128 a[76:79],v7 offset:7168
v_add_u32 v1,s78,v1
v_mfma_f32_16x16x32_bf16 v[132:135],a[196:199],v[88:91],v[132:135]
v_exp_f32 v96,v96
v_mfma_f32_16x16x32_bf16 v[140:143],a[200:203],v[80:83],v[140:143]
global_load_lds_dwordx4 v4,s[10:11] offset:1024 nt
ds_read_b128 a[128:131],v1
v_mfma_f32_16x16x32_bf16 v[140:143],a[204:207],v[88:91],v[140:143]
v_exp_f32 v97,v97
v_mfma_f32_16x16x32_bf16 v[148:151],a[208:211],v[80:83],v[148:151]
ds_read_b128 a[132:135],v1 offset:256
v_cvt_pk_bf16_f32 v96,v96,v97
v_mfma_f32_16x16x32_bf16 v[148:151],a[212:215],v[88:91],v[148:151]
v_exp_f32 v98,v98
v_mfma_f32_16x16x32_bf16 v[156:159],a[216:219],v[80:83],v[156:159]
ds_read_b128 a[136:139],v1 offset:512
v_accvgpr_read_b32 v12,a84
v_mfma_f32_16x16x32_bf16 v[156:159],a[220:223],v[88:91],v[156:159]
v_exp_f32 v99,v99
v_mfma_f32_16x16x32_bf16 v[164:167],a[224:227],v[80:83],v[164:167]
ds_read_b128 a[140:143],v1 offset:768
v_cvt_pk_bf16_f32 v97,v98,v99
v_mfma_f32_16x16x32_bf16 v[164:167],a[228:231],v[88:91],v[164:167]
v_exp_f32 v100,v100
v_mfma_f32_16x16x32_bf16 v[172:175],a[232:235],v[80:83],v[172:175]
ds_read_b128 a[156:159],v1 offset:4864
v_accvgpr_read_b32 v13,a85
v_mfma_f32_16x16x32_bf16 v[172:175],a[236:239],v[88:91],v[172:175]
v_exp_f32 v101,v101
v_mfma_f32_16x16x32_bf16 v[180:183],a[240:243],v[80:83],v[180:183]
ds_read_b128 a[152:155],v1 offset:4608
v_cvt_pk_bf16_f32 v98,v100,v101
v_mfma_f32_16x16x32_bf16 v[180:183],a[244:247],v[88:91],v[180:183]
)A"
R"A(v_exp_f32 v102,v102
v_mfma_f32_16x16x32_bf16 v[188:191],a[248:251],v[80:83],v[188:191]
ds_read_b128 a[148:151],v1 offset:4352
v_accvgpr_read_b32 v14,a86
v_mfma_f32_16x16x32_bf16 v[188:191],a[252:255],v[88:91],v[188:191]
v_exp_f32 v103,v103
v_mfma_f32_16x16x32_bf16 v[52:55],v[28:31],v[80:83],v[52:55]
ds_read_b128 a[144:147],v1 offset:4096
v_cvt_pk_bf16_f32 v99,v102,v103
v_mfma_f32_16x16x32_bf16 v[52:55],v[28:31],v[88:91],v[52:55]
s_waitcnt lgkmcnt(4)
v_exp_f32 v104,v104
v_mfma_f32_16x16x32_bf16 v[64:67],a[128:131],v[128:131],v[32:35]
global_load_lds_dwordx4 v5,s[10:11] offset:2048 nt
ds_read_b128 a[160:163],v1 offset:8192
v_mfma_f32_16x16x32_bf16 v[64:67],a[132:135],v[136:139],v[64:67]
v_exp_f32 v105,v105
v_mfma_f32_16x16x32_bf16 v[64:67],a[136:139],v[144:147],v[64:67]
ds_read_b128 a[164:167],v1 offset:8448
v_cvt_pk_bf16_f32 v104,v104,v105
v_mfma_f32_16x16x32_bf16 v[64:67],a[140:143],v[152:155],v[64:67]
s_waitcnt lgkmcnt(4)
v_exp_f32 v106,v106
v_mfma_f32_16x16x32_bf16 v[68:71],a[156:159],v[152:155],v[32:35]
ds_read_b128 a[168:171],v1 offset:8704
v_accvgpr_read_b32 v15,a87
v_mfma_f32_16x16x32_bf16 v[68:71],a[152:155],v[144:147],v[68:71]
s_waitcnt lgkmcnt(4)
v_exp_f32 v107,v107
v_mfma_f32_16x16x32_bf16 v[68:71],a[148:151],v[136:139],v[68:71]
s_waitcnt lgkmcnt(3)
ds_read_b128 a[172:175],v1 offset:8960
v_cvt_pk_bf16_f32 v105,v106,v107
v_mfma_f32_16x16x32_bf16 v[68:71],a[144:147],v[128:131],v[68:71]
s_waitcnt lgkmcnt(3)
v_exp_f32 v108,v108
v_mfma_f32_16x16x32_bf16 v[72:75],a[160:163],v[128:131],v[32:35]
s_waitcnt lgkmcnt(2)
ds_read_b128 a[188:191],v1 offset:13056
v_accvgpr_read_b32 v8,a80
v_mfma_f32_16x16x32_bf16 v[72:75],a[164:167],v[136:139],v[72:75]
s_waitcnt lgkmcnt(2)
ds_read_b128 a[184:187],v1 offset:12800
v_accvgpr_read_b32 v9,a81
v_mfma_f32_16x16x32_bf16 v[72:75],a[168:171],v[144:147],v[72:75]
s_waitcnt lgkmcnt(2)
ds_read_b128 a[180:183],v1 offset:12544
v_accvgpr_read_b32 v10,a82
v_mfma_f32_16x16x32_bf16 v[72:75],a[172:175],v[152:155],v[72:75]
s_waitcnt lgkmcnt(2)
ds_read_b128 a[176:179],v1 offset:12288
v_accvgpr_read_b32 v11,a83
v_mfma_f32_16x16x32_bf16 v[76:79],a[188:191],v[152:155],v[32:35]
s_waitcnt lgkmcnt(2)
v_exp_f32 v109,v109
v_mfma_f32_16x16x32_bf16 v[76:79],a[184:187],v[144:147],v[76:79]
s_waitcnt lgkmcnt(1)
v_exp_f32 v110,v110
v_exp_f32 v111,v111
v_mfma_f32_16x16x32_bf16 v[76:79],a[180:183],v[136:139],v[76:79]
s_waitcnt lgkmcnt(0)
v_cvt_pk_bf16_f32 v106,v108,v109
v_cvt_pk_bf16_f32 v107,v110,v111
v_mfma_f32_16x16x32_bf16 v[76:79],a[176:179],v[128:131],v[76:79]
v_mul_f32 v8,v8,v26
v_mul_f32 v9,v9,v26
v_mfma_f32_16x16x32_bf16 v[196:199],a[192:195],v[96:99],v[196:199]
global_load_lds_dwordx4 v5,s[10:11] offset:3072
v_mul_f32 v10,v10,v26
v_mfma_f32_16x16x32_bf16 v[196:199],a[196:199],v[104:107],v[196:199]
v_mul_f32 v11,v11,v26
v_mul_f32 v12,v12,v26
v_mfma_f32_16x16x32_bf16 v[204:207],a[200:203],v[96:99],v[204:207]
v_mul_f32 v13,v13,v26
v_mul_f32 v14,v14,v26
v_mfma_f32_16x16x32_bf16 v[204:207],a[204:207],v[104:107],v[204:207]
v_mul_f32 v15,v15,v26
v_cvt_pk_bf16_f32 v8,v8,v9
v_mfma_f32_16x16x32_bf16 v[212:215],a[208:211],v[96:99],v[212:215]
v_cvt_pk_bf16_f32 v9,v10,v11
v_cvt_pk_bf16_f32 v10,v12,v13
v_mfma_f32_16x16x32_bf16 v[212:215],a[212:215],v[104:107],v[212:215]
v_cvt_pk_bf16_f32 v11,v14,v15
v_accvgpr_write_b32 a80,v8
v_mfma_f32_16x16x32_bf16 v[220:223],a[216:219],v[96:99],v[220:223]
v_accvgpr_write_b32 a81,v9
v_accvgpr_write_b32 a82,v10
v_mfma_f32_16x16x32_bf16 v[220:223],a[220:223],v[104:107],v[220:223]
ds_read_b128 a[84:87],v7
v_accvgpr_write_b32 a83,v11
v_mfma_f32_16x16x32_bf16 v[228:231],a[224:227],v[96:99],v[228:231]
v_exp_f32 v112,v112
v_mfma_f32_16x16x32_bf16 v[228:231],a[228:231],v[104:107],v[228:231]
v_exp_f32 v113,v113
v_mfma_f32_16x16x32_bf16 v[236:239],a[232:235],v[96:99],v[236:239]
v_exp_f32 v114,v114
v_mfma_f32_16x16x32_bf16 v[236:239],a[236:239],v[104:107],v[236:239]
v_exp_f32 v115,v115
v_mfma_f32_16x16x32_bf16 v[244:247],a[240:243],v[96:99],v[244:247]
s_add_u32 s10,s24,s74
s_addc_u32 s11,s25,0
v_cvt_pk_bf16_f32 v112,v112,v113
v_mfma_f32_16x16x32_bf16 v[244:247],a[244:247],v[104:107],v[244:247]
s_add_u32 s74,s74,16384
v_exp_f32 v116,v116
v_mfma_f32_16x16x32_bf16 v[252:255],a[248:251],v[96:99],v[252:255]
s_add_u32 m0,s76,16384
v_exp_f32 v117,v117
v_mfma_f32_16x16x32_bf16 v[252:255],a[252:255],v[104:107],v[252:255]
s_and_b32 s74,s74,0x3ffff
v_exp_f32 v118,v118
v_mfma_f32_16x16x32_bf16 v[56:59],v[28:31],v[96:99],v[56:59]
global_load_lds_dwordx4 v4,s[10:11]
v_cvt_pk_bf16_f32 v113,v114,v115
v_mfma_f32_16x16x32_bf16 v[56:59],v[28:31],v[104:107],v[56:59]
s_or_b32 s74,s74,s69
v_exp_f32 v119,v119
v_mfma_f32_16x16x32_bf16 v[80:83],a[128:131],v[160:163],v[36:39]
s_sub_u32 s76,s76,32768
v_cvt_pk_bf16_f32 v114,v116,v117
v_cvt_pk_bf16_f32 v115,v118,v119
v_mfma_f32_16x16x32_bf16 v[80:83],a[132:135],v[168:171],v[80:83]
s_and_b32 s76,s76,0x1ffff
v_exp_f32 v120,v120
v_mfma_f32_16x16x32_bf16 v[80:83],a[136:139],v[176:179],v[80:83]
v_exp_f32 v121,v121
v_mfma_f32_16x16x32_bf16 v[80:83],a[140:143],v[184:187],v[80:83]
v_exp_f32 v122,v122
v_mfma_f32_16x16x32_bf16 v[84:87],a[156:159],v[184:187],v[36:39]
v_exp_f32 v123,v123
v_mfma_f32_16x16x32_bf16 v[84:87],a[152:155],v[176:179],v[84:87]
v_exp_f32 v124,v124
v_mfma_f32_16x16x32_bf16 v[84:87],a[148:151],v[168:171],v[84:87]
v_exp_f32 v125,v125
v_mfma_f32_16x16x32_bf16 v[84:87],a[144:147],v[160:163],v[84:87]
v_exp_f32 v126,v126
v_mfma_f32_16x16x32_bf16 v[88:91],a[160:163],v[160:163],v[36:39]
v_exp_f32 v127,v127
v_mfma_f32_16x16x32_bf16 v[88:91],a[164:167],v[168:171],v[88:91]
v_cvt_pk_bf16_f32 v120,v120,v121
v_cvt_pk_bf16_f32 v121,v122,v123
v_mfma_f32_16x16x32_bf16 v[88:91],a[168:171],v[176:179],v[88:91]
v_cvt_pk_bf16_f32 v122,v124,v125
v_cvt_pk_bf16_f32 v123,v126,v127
v_mfma_f32_16x16x32_bf16 v[88:91],a[172:175],v[184:187],v[88:91]
v_add_u32 v2,s78,v2
v_add_u32 v3,s78,v3
v_mfma_f32_16x16x32_bf16 v[92:95],a[188:191],v[184:187],v[36:39]
s_sub_u32 s78,s77,32768
v_exp_f32 v64,v64
v_mfma_f32_16x16x32_bf16 v[92:95],a[184:187],v[176:179],v[92:95]
s_and_b32 s78,s78,0x1ffff
v_exp_f32 v65,v65
v_mfma_f32_16x16x32_bf16 v[92:95],a[180:183],v[168:171],v[92:95]
global_load_lds_dwordx4 v5,s[10:11] offset:1024
v_cvt_pk_bf16_f32 v64,v64,v65
v_mfma_f32_16x16x32_bf16 v[92:95],a[176:179],v[160:163],v[92:95]
s_sub_u32 s78,s78,s77
v_exp_f32 v66,v66
v_mfma_f32_16x16x32_bf16 a[32:35],a[192:195],v[112:115],a[32:35]
s_add_u32 s77,s77,s78
ds_read_b64_tr_b16 a[192:193],v2
ds_read_b64_tr_b16 a[194:195],v2 offset:4096
v_mfma_f32_16x16x32_bf16 a[32:35],a[196:199],v[120:123],a[32:35]
ds_read_b64_tr_b16 a[196:197],v2 offset:8192
v_accvgpr_read_b32 v20,a92
v_mfma_f32_16x16x32_bf16 a[36:39],a[200:203],v[112:115],a[36:39]
ds_read_b64_tr_b16 a[198:199],v2 offset:12288
v_accvgpr_read_b32 v21,a93
v_mfma_f32_16x16x32_bf16 a[36:39],a[204:207],v[120:123],a[36:39]
ds_read_b64_tr_b16 a[200:201],v3
v_accvgpr_read_b32 v22,a94
v_mfma_f32_16x16x32_bf16 a[40:43],a[208:211],v[112:115],a[40:43]
ds_read_b64_tr_b16 a[202:203],v3 offset:4096
ds_read_b64_tr_b16 a[204:205],v3 offset:8192
v_mfma_f32_16x16x32_bf16 a[40:43],a[212:215],v[120:123],a[40:43]
ds_read_b64_tr_b16 a[206:207],v3 offset:12288
v_accvgpr_read_b32 v23,a95
v_mfma_f32_16x16x32_bf16 a[44:47],a[216:219],v[112:115],a[44:47]
ds_read_b64_tr_b16 a[208:209],v2 offset:256
v_accvgpr_read_b32 v16,a88
v_mfma_f32_16x16x32_bf16 a[44:47],a[220:223],v[120:123],a[44:47]
ds_read_b64_tr_b16 a[210:211],v2 offset:4352
v_accvgpr_read_b32 v17,a89
v_mfma_f32_16x16x32_bf16 a[48:51],a[224:227],v[112:115],a[48:51]
ds_read_b64_tr_b16 a[212:213],v2 offset:8448
ds_read_b64_tr_b16 a[214:215],v2 offset:12544
v_mfma_f32_16x16x32_bf16 a[48:51],a[228:231],v[120:123],a[48:51]
v_exp_f32 v67,v67
v_mfma_f32_16x16x32_bf16 a[52:55],a[232:235],v[112:115],a[52:55]
v_exp_f32 v68,v68
v_mfma_f32_16x16x32_bf16 a[52:55],a[236:239],v[120:123],a[52:55]
v_exp_f32 v69,v69
v_mfma_f32_16x16x32_bf16 a[56:59],a[240:243],v[112:115],a[56:59]
v_exp_f32 v70,v70
v_mfma_f32_16x16x32_bf16 a[56:59],a[244:247],v[120:123],a[56:59]
v_exp_f32 v71,v71
v_mfma_f32_16x16x32_bf16 a[60:63],a[248:251],v[112:115],a[60:63]
global_load_lds_dwordx4 v4,s[10:11] offset:2048
ds_read_b64_tr_b16 a[216:217],v3 offset:256
v_mfma_f32_16x16x32_bf16 a[60:63],a[252:255],v[120:123],a[60:63]
ds_read_b64_tr_b16 a[218:219],v3 offset:4352
v_cvt_pk_bf16_f32 v65,v66,v67
v_mfma_f32_16x16x32_bf16 v[60:63],v[28:31],v[112:115],v[60:63]
v_cvt_pk_bf16_f32 v66,v68,v69
v_cvt_pk_bf16_f32 v67,v70,v71
v_mfma_f32_16x16x32_bf16 v[60:63],v[28:31],v[120:123],v[60:63]
v_exp_f32 v72,v72
v_mfma_f32_16x16x32_bf16 v[96:99],a[128:131],v[192:195],v[40:43]
v_exp_f32 v73,v73
v_mfma_f32_16x16x32_bf16 v[96:99],a[132:135],v[200:203],v[96:99]
v_exp_f32 v74,v74
v_mfma_f32_16x16x32_bf16 v[96:99],a[136:139],v[208:211],v[96:99]
v_exp_f32 v75,v75
v_mfma_f32_16x16x32_bf16 v[96:99],a[140:143],v[216:219],v[96:99]
v_exp_f32 v76,v76
v_mfma_f32_16x16x32_bf16 v[100:103],a[156:159],v[216:219],v[40:43]
v_exp_f32 v77,v77
v_mfma_f32_16x16x32_bf16 v[100:103],a[152:155],v[208:211],v[100:103]
v_exp_f32 v78,v78
v_mfma_f32_16x16x32_bf16 v[100:103],a[148:151],v[200:203],v[100:103]
v_exp_f32 v79,v79
v_mfma_f32_16x16x32_bf16 v[100:103],a[144:147],v[192:195],v[100:103]
ds_read_b64_tr_b16 a[220:221],v3 offset:8448
ds_read_b64_tr_b16 a[222:223],v3 offset:12544
v_mfma_f32_16x16x32_bf16 v[104:107],a[160:163],v[192:195],v[40:43]
v_cvt_pk_bf16_f32 v72,v72,v73
v_cvt_pk_bf16_f32 v73,v74,v75
v_mfma_f32_16x16x32_bf16 v[104:107],a[164:167],v[200:203],v[104:107]
s_nop 0
L_pd10_%=:
s_cbranch_execz L_t1b_%=
v_cvt_pk_bf16_f32 v74,v76,v77
v_cvt_pk_bf16_f32 v75,v78,v79
v_mfma_f32_16x16x32_bf16 v[104:107],a[168:171],v[208:211],v[104:107]
ds_read_b64_tr_b16 a[224:225],v2 offset:512
ds_read_b64_tr_b16 a[226:227],v2 offset:4608
v_mfma_f32_16x16x32_bf16 v[104:107],a[172:175],v[216:219],v[104:107]
ds_read_b64_tr_b16 a[228:229],v2 offset:8704
v_accvgpr_read_b32 v18,a90
v_mfma_f32_16x16x32_bf16 v[108:111],a[188:191],v[216:219],v[40:43]
global_load_lds_dwordx4 v5,s[10:11] offset:3072
ds_read_b64_tr_b16 a[230:231],v2 offset:12800
v_mfma_f32_16x16x32_bf16 v[108:111],a[184:187],v[208:211],v[108:111]
ds_read_b64_tr_b16 a[232:233],v3 offset:512
v_accvgpr_read_b32 v19,a91
v_mfma_f32_16x16x32_bf16 v[108:111],a[180:183],v[200:203],v[108:111]
ds_read_b64_tr_b16 a[234:235],v3 offset:4608
ds_read_b64_tr_b16 a[236:237],v3 offset:8704
v_mfma_f32_16x16x32_bf16 v[108:111],a[176:179],v[192:195],v[108:111]
s_waitcnt lgkmcnt(7)
ds_read_b64_tr_b16 a[238:239],v3 offset:12800
v_mul_f32 v16,v16,v26
v_mfma_f32_16x16x32_bf16 a[0:3],a[192:195],v[64:67],a[0:3]
ds_read_b64_tr_b16 a[240:241],v2 offset:768
v_mul_f32 v17,v17,v26
v_mfma_f32_16x16x32_bf16 a[0:3],a[196:199],v[72:75],a[0:3]
ds_read_b64_tr_b16 a[242:243],v2 offset:4864
v_mul_f32 v18,v18,v26
v_mfma_f32_16x16x32_bf16 a[4:7],a[200:203],v[64:67],a[4:7]
ds_read_b64_tr_b16 a[244:245],v2 offset:8960
ds_read_b64_tr_b16 a[246:247],v2 offset:13056
v_mfma_f32_16x16x32_bf16 a[4:7],a[204:207],v[72:75],a[4:7]
ds_read_b64_tr_b16 a[248:249],v3 offset:768
v_mul_f32 v19,v19,v26
v_mfma_f32_16x16x32_bf16 a[8:11],a[208:211],v[64:67],a[8:11]
ds_read_b64_tr_b16 a[250:251],v3 offset:4864
v_mul_f32 v20,v20,v26
v_mfma_f32_16x16x32_bf16 a[8:11],a[212:215],v[72:75],a[8:11]
v_mul_f32 v21,v21,v26
v_mul_f32 v22,v22,v26
v_mul_f32 v23,v23,v26
v_cvt_pk_bf16_f32 v16,v16,v17
v_cvt_pk_bf16_f32 v17,v18,v19
v_cvt_pk_bf16_f32 v18,v20,v21
v_cvt_pk_bf16_f32 v19,v22,v23
v_accvgpr_write_b32 a88,v16
v_accvgpr_write_b32 a89,v17
v_accvgpr_write_b32 a90,v18
v_accvgpr_write_b32 a91,v19
v_mfma_f32_16x16x32_bf16 a[12:15],a[216:219],v[64:67],a[12:15]
ds_read_b128 a[92:95],v7 offset:1024
v_mov_b32 v8,v7
v_mfma_f32_16x16x32_bf16 a[12:15],a[220:223],v[72:75],a[12:15]
s_waitcnt lgkmcnt(7)
ds_read_b64_tr_b16 a[252:253],v3 offset:8960
v_add_u32 v1,s78,v1
v_mfma_f32_16x16x32_bf16 a[16:19],a[224:227],v[64:67],a[16:19]
ds_read_b64_tr_b16 a[254:255],v3 offset:13056
v_mfma_f32_16x16x32_bf16 a[16:19],a[228:231],v[72:75],a[16:19]
s_waitcnt lgkmcnt(4)
v_exp_f32 v80,v80
v_mfma_f32_16x16x32_bf16 a[20:23],a[232:235],v[64:67],a[20:23]
)A"
R"A(s_getreg_b32 s92,hwreg(HW_REG_IB_STS,0,4)
s_min_u32 s4,s92,s6
s_mov_b32 s6,s92
v_mfma_f32_16x16x32_bf16 a[20:23],a[236:239],v[72:75],a[20:23]
s_cmp_ge_u32 s4,s9
s_cselect_b32 s4,15,s5
ds_read_b128 v[24:27],v8 offset:4096
v_mfma_f32_16x16x32_bf16 a[24:27],a[240:243],v[64:67],a[24:27]
v_exp_f32 v81,v81
v_mfma_f32_16x16x32_bf16 a[24:27],a[244:247],v[72:75],a[24:27]
s_waitcnt lgkmcnt(4)
v_exp_f32 v82,v82
v_mfma_f32_16x16x32_bf16 a[28:31],a[248:251],v[64:67],a[28:31]
s_waitcnt lgkmcnt(1)
v_exp_f32 v83,v83
v_mfma_f32_16x16x32_bf16 a[28:31],a[252:255],v[72:75],a[28:31]
v_exp_f32 v84,v84
v_mfma_f32_16x16x32_bf16 v[48:51],v[28:31],v[64:67],v[48:51]
s_getpc_b64 s[92:93]
s_cmp_lg_u32 s4,0
s_cbranch_scc1 L_pace_%=
v_exp_f32 v85,v85
v_exp_f32 v86,v86
v_exp_f32 v87,v87
v_exp_f32 v88,v88
v_exp_f32 v89,v89
v_mfma_f32_16x16x32_bf16 v[48:51],v[28:31],v[72:75],v[48:51]
ds_read_b32 v7,v8 offset:5120
v_cvt_pk_bf16_f32 v80,v80,v81
v_cvt_pk_bf16_f32 v81,v82,v83
v_cvt_pk_bf16_f32 v82,v84,v85
v_cvt_pk_bf16_f32 v83,v86,v87
v_cvt_pk_bf16_f32 v88,v88,v89
s_mov_b32 s70,8
L_a1_%=:
s_waitcnt vmcnt(16)
s_barrier
v_mfma_f32_16x16x32_bf16 v[112:115],a[128:131],v[224:227],v[44:47]
s_add_u32 s10,s20,s74
s_addc_u32 s11,s21,0
s_add_u32 m0,s76,0
v_mfma_f32_16x16x32_bf16 v[112:115],a[132:135],v[232:235],v[112:115]
s_add_u32 s75,s75,16384
v_exp_f32 v90,v90
v_mfma_f32_16x16x32_bf16 v[112:115],a[136:139],v[240:243],v[112:115]
global_load_lds_dwordx4 v4,s[10:11] nt
ds_read_b128 a[128:131],v1
v_mfma_f32_16x16x32_bf16 v[112:115],a[140:143],v[248:251],v[112:115]
s_and_b32 s69,s75,0xfffc0000
v_exp_f32 v91,v91
v_mfma_f32_16x16x32_bf16 v[116:119],a[156:159],v[248:251],v[44:47]
v_exp_f32 v92,v92
v_mfma_f32_16x16x32_bf16 v[116:119],a[152:155],v[240:243],v[116:119]
v_exp_f32 v93,v93
v_mfma_f32_16x16x32_bf16 v[116:119],a[148:151],v[232:235],v[116:119]
v_exp_f32 v94,v94
v_mfma_f32_16x16x32_bf16 v[116:119],a[144:147],v[224:227],v[116:119]
v_exp_f32 v95,v95
v_mfma_f32_16x16x32_bf16 v[120:123],a[160:163],v[224:227],v[44:47]
v_cvt_pk_bf16_f32 v89,v90,v91
v_cvt_pk_bf16_f32 v90,v92,v93
v_mfma_f32_16x16x32_bf16 v[120:123],a[164:167],v[232:235],v[120:123]
ds_read_b128 a[132:135],v1 offset:256
v_cvt_pk_bf16_f32 v91,v94,v95
v_mfma_f32_16x16x32_bf16 v[120:123],a[168:171],v[240:243],v[120:123]
v_exp_f32 v96,v96
v_mfma_f32_16x16x32_bf16 v[120:123],a[172:175],v[248:251],v[120:123]
ds_read_b128 a[136:139],v1 offset:512
v_add_u32 v2,s78,v2
v_mfma_f32_16x16x32_bf16 v[124:127],a[188:191],v[248:251],v[44:47]
v_exp_f32 v97,v97
v_mfma_f32_16x16x32_bf16 v[124:127],a[184:187],v[240:243],v[124:127]
ds_read_b128 a[140:143],v1 offset:768
v_cvt_pk_bf16_f32 v96,v96,v97
v_mfma_f32_16x16x32_bf16 v[124:127],a[180:183],v[232:235],v[124:127]
v_exp_f32 v98,v98
v_mfma_f32_16x16x32_bf16 v[124:127],a[176:179],v[224:227],v[124:127]
ds_read_b128 a[156:159],v1 offset:4864
v_add_u32 v3,s78,v3
v_mfma_f32_16x16x32_bf16 v[132:135],a[192:195],v[80:83],v[132:135]
s_sub_u32 s78,s77,32768
v_exp_f32 v99,v99
v_mfma_f32_16x16x32_bf16 v[132:135],a[196:199],v[88:91],v[132:135]
s_and_b32 s78,s78,0x1ffff
ds_read_b128 a[152:155],v1 offset:4608
v_cvt_pk_bf16_f32 v97,v98,v99
v_mfma_f32_16x16x32_bf16 v[140:143],a[200:203],v[80:83],v[140:143]
s_sub_u32 s78,s78,s77
global_load_lds_dwordx4 v4,s[10:11] offset:1024 nt
v_mfma_f32_16x16x32_bf16 v[140:143],a[204:207],v[88:91],v[140:143]
s_add_u32 s77,s77,s78
v_exp_f32 v100,v100
v_mfma_f32_16x16x32_bf16 v[148:151],a[208:211],v[80:83],v[148:151]
v_exp_f32 v101,v101
v_mfma_f32_16x16x32_bf16 v[148:151],a[212:215],v[88:91],v[148:151]
ds_read_b128 a[148:151],v1 offset:4352
v_cvt_pk_bf16_f32 v98,v100,v101
v_mfma_f32_16x16x32_bf16 v[156:159],a[216:219],v[80:83],v[156:159]
v_exp_f32 v102,v102
v_mfma_f32_16x16x32_bf16 v[156:159],a[220:223],v[88:91],v[156:159]
v_exp_f32 v103,v103
v_mfma_f32_16x16x32_bf16 v[164:167],a[224:227],v[80:83],v[164:167]
ds_read_b128 a[144:147],v1 offset:4096
v_cvt_pk_bf16_f32 v99,v102,v103
v_mfma_f32_16x16x32_bf16 v[164:167],a[228:231],v[88:91],v[164:167]
v_exp_f32 v104,v104
v_mfma_f32_16x16x32_bf16 v[172:175],a[232:235],v[80:83],v[172:175]
v_exp_f32 v105,v105
v_mfma_f32_16x16x32_bf16 v[172:175],a[236:239],v[88:91],v[172:175]
ds_read_b128 a[160:163],v1 offset:8192
v_cvt_pk_bf16_f32 v104,v104,v105
v_mfma_f32_16x16x32_bf16 v[180:183],a[240:243],v[80:83],v[180:183]
v_exp_f32 v106,v106
v_mfma_f32_16x16x32_bf16 v[180:183],a[244:247],v[88:91],v[180:183]
v_exp_f32 v107,v107
v_mfma_f32_16x16x32_bf16 v[188:191],a[248:251],v[80:83],v[188:191]
ds_read_b128 a[164:167],v1 offset:8448
v_cvt_pk_bf16_f32 v105,v106,v107
v_mfma_f32_16x16x32_bf16 v[188:191],a[252:255],v[88:91],v[188:191]
v_exp_f32 v108,v108
v_mfma_f32_16x16x32_bf16 v[52:55],v[28:31],v[80:83],v[52:55]
ds_read_b128 a[168:171],v1 offset:8704
v_mfma_f32_16x16x32_bf16 v[52:55],v[28:31],v[88:91],v[52:55]
s_waitcnt lgkmcnt(3)
v_exp_f32 v109,v109
v_mfma_f32_16x16x32_bf16 v[64:67],a[128:131],v[128:131],v[32:35]
global_load_lds_dwordx4 v5,s[10:11] offset:2048 nt
ds_read_b128 a[172:175],v1 offset:8960
v_mfma_f32_16x16x32_bf16 v[64:67],a[132:135],v[136:139],v[64:67]
v_exp_f32 v110,v110
v_mfma_f32_16x16x32_bf16 v[64:67],a[136:139],v[144:147],v[64:67]
ds_read_b128 a[188:191],v1 offset:13056
v_cvt_pk_bf16_f32 v106,v108,v109
v_mfma_f32_16x16x32_bf16 v[64:67],a[140:143],v[152:155],v[64:67]
v_exp_f32 v111,v111
v_mfma_f32_16x16x32_bf16 v[68:71],a[156:159],v[152:155],v[32:35]
ds_read_b128 a[184:187],v1 offset:12800
v_cvt_pk_bf16_f32 v107,v110,v111
v_mfma_f32_16x16x32_bf16 v[68:71],a[152:155],v[144:147],v[68:71]
v_exp_f32 v112,v112
v_mfma_f32_16x16x32_bf16 v[68:71],a[148:151],v[136:139],v[68:71]
ds_read_b128 a[180:183],v1 offset:12544
v_mfma_f32_16x16x32_bf16 v[68:71],a[144:147],v[128:131],v[68:71]
s_waitcnt lgkmcnt(4)
v_exp_f32 v113,v113
v_mfma_f32_16x16x32_bf16 v[72:75],a[160:163],v[128:131],v[32:35]
ds_read_b128 a[176:179],v1 offset:12288
v_cvt_pk_bf16_f32 v112,v112,v113
v_mfma_f32_16x16x32_bf16 v[72:75],a[164:167],v[136:139],v[72:75]
v_exp_f32 v114,v114
v_mfma_f32_16x16x32_bf16 v[72:75],a[168:171],v[144:147],v[72:75]
s_waitcnt lgkmcnt(3)
v_exp_f32 v115,v115
v_mfma_f32_16x16x32_bf16 v[72:75],a[172:175],v[152:155],v[72:75]
v_exp_f32 v116,v116
v_mfma_f32_16x16x32_bf16 v[76:79],a[188:191],v[152:155],v[32:35]
s_waitcnt lgkmcnt(2)
v_exp_f32 v117,v117
v_mfma_f32_16x16x32_bf16 v[76:79],a[184:187],v[144:147],v[76:79]
s_waitcnt lgkmcnt(1)
v_exp_f32 v118,v118
v_mfma_f32_16x16x32_bf16 v[76:79],a[180:183],v[136:139],v[76:79]
s_waitcnt lgkmcnt(0)
v_exp_f32 v119,v119
v_mfma_f32_16x16x32_bf16 v[76:79],a[176:179],v[128:131],v[76:79]
v_cvt_pk_bf16_f32 v113,v114,v115
v_cvt_pk_bf16_f32 v114,v116,v117
v_mfma_f32_16x16x32_bf16 v[196:199],a[192:195],v[96:99],v[196:199]
global_load_lds_dwordx4 v5,s[10:11] offset:3072
v_cvt_pk_bf16_f32 v115,v118,v119
v_mfma_f32_16x16x32_bf16 v[196:199],a[196:199],v[104:107],v[196:199]
v_exp_f32 v120,v120
v_mfma_f32_16x16x32_bf16 v[204:207],a[200:203],v[96:99],v[204:207]
v_exp_f32 v121,v121
v_mfma_f32_16x16x32_bf16 v[204:207],a[204:207],v[104:107],v[204:207]
v_exp_f32 v122,v122
v_mfma_f32_16x16x32_bf16 v[212:215],a[208:211],v[96:99],v[212:215]
v_exp_f32 v123,v123
v_mfma_f32_16x16x32_bf16 v[212:215],a[212:215],v[104:107],v[212:215]
v_exp_f32 v124,v124
v_mfma_f32_16x16x32_bf16 v[220:223],a[216:219],v[96:99],v[220:223]
v_exp_f32 v125,v125
v_mfma_f32_16x16x32_bf16 v[220:223],a[220:223],v[104:107],v[220:223]
v_exp_f32 v126,v126
v_mfma_f32_16x16x32_bf16 v[228:231],a[224:227],v[96:99],v[228:231]
v_exp_f32 v127,v127
v_mfma_f32_16x16x32_bf16 v[228:231],a[228:231],v[104:107],v[228:231]
v_cvt_pk_bf16_f32 v120,v120,v121
v_cvt_pk_bf16_f32 v121,v122,v123
v_mfma_f32_16x16x32_bf16 v[236:239],a[232:235],v[96:99],v[236:239]
v_cvt_pk_bf16_f32 v122,v124,v125
v_cvt_pk_bf16_f32 v123,v126,v127
v_mfma_f32_16x16x32_bf16 v[236:239],a[236:239],v[104:107],v[236:239]
v_exp_f32 v64,v64
v_mfma_f32_16x16x32_bf16 v[244:247],a[240:243],v[96:99],v[244:247]
s_add_u32 s10,s24,s74
s_addc_u32 s11,s25,0
s_add_u32 s74,s74,16384
v_mfma_f32_16x16x32_bf16 v[244:247],a[244:247],v[104:107],v[244:247]
s_and_b32 s74,s74,0x3ffff
v_exp_f32 v65,v65
v_mfma_f32_16x16x32_bf16 v[252:255],a[248:251],v[96:99],v[252:255]
s_add_u32 m0,s76,16384
v_exp_f32 v66,v66
v_mfma_f32_16x16x32_bf16 v[252:255],a[252:255],v[104:107],v[252:255]
s_or_b32 s74,s74,s69
v_exp_f32 v67,v67
v_mfma_f32_16x16x32_bf16 v[56:59],v[28:31],v[96:99],v[56:59]
global_load_lds_dwordx4 v4,s[10:11]
v_cvt_pk_bf16_f32 v64,v64,v65
v_mfma_f32_16x16x32_bf16 v[56:59],v[28:31],v[104:107],v[56:59]
s_sub_u32 s76,s76,32768
v_exp_f32 v68,v68
v_mfma_f32_16x16x32_bf16 v[80:83],a[128:131],v[160:163],v[36:39]
s_and_b32 s76,s76,0x1ffff
v_exp_f32 v69,v69
v_mfma_f32_16x16x32_bf16 v[80:83],a[132:135],v[168:171],v[80:83]
v_exp_f32 v70,v70
v_mfma_f32_16x16x32_bf16 v[80:83],a[136:139],v[176:179],v[80:83]
v_exp_f32 v71,v71
v_mfma_f32_16x16x32_bf16 v[80:83],a[140:143],v[184:187],v[80:83]
v_cvt_pk_bf16_f32 v65,v66,v67
v_cvt_pk_bf16_f32 v66,v68,v69
v_mfma_f32_16x16x32_bf16 v[84:87],a[156:159],v[184:187],v[36:39]
v_cvt_pk_bf16_f32 v67,v70,v71
v_add_u32 v1,s78,v1
v_mfma_f32_16x16x32_bf16 v[84:87],a[152:155],v[176:179],v[84:87]
v_exp_f32 v72,v72
v_mfma_f32_16x16x32_bf16 v[84:87],a[148:151],v[168:171],v[84:87]
v_exp_f32 v73,v73
v_mfma_f32_16x16x32_bf16 v[84:87],a[144:147],v[160:163],v[84:87]
v_exp_f32 v74,v74
v_mfma_f32_16x16x32_bf16 v[88:91],a[160:163],v[160:163],v[36:39]
v_exp_f32 v75,v75
v_mfma_f32_16x16x32_bf16 v[88:91],a[164:167],v[168:171],v[88:91]
v_exp_f32 v76,v76
v_mfma_f32_16x16x32_bf16 v[88:91],a[168:171],v[176:179],v[88:91]
v_exp_f32 v77,v77
v_mfma_f32_16x16x32_bf16 v[88:91],a[172:175],v[184:187],v[88:91]
v_exp_f32 v78,v78
v_mfma_f32_16x16x32_bf16 v[92:95],a[188:191],v[184:187],v[36:39]
v_exp_f32 v79,v79
v_mfma_f32_16x16x32_bf16 v[92:95],a[184:187],v[176:179],v[92:95]
v_cvt_pk_bf16_f32 v72,v72,v73
v_cvt_pk_bf16_f32 v73,v74,v75
v_mfma_f32_16x16x32_bf16 v[92:95],a[180:183],v[168:171],v[92:95]
global_load_lds_dwordx4 v5,s[10:11] offset:1024
v_cvt_pk_bf16_f32 v74,v76,v77
v_mfma_f32_16x16x32_bf16 v[92:95],a[176:179],v[160:163],v[92:95]
v_exp_f32 v80,v80
v_mfma_f32_16x16x32_bf16 a[32:35],a[192:195],v[112:115],a[32:35]
ds_read_b64_tr_b16 a[192:193],v2
ds_read_b64_tr_b16 a[194:195],v2 offset:4096
v_mfma_f32_16x16x32_bf16 a[32:35],a[196:199],v[120:123],a[32:35]
ds_read_b64_tr_b16 a[196:197],v2 offset:8192
v_cvt_pk_bf16_f32 v75,v78,v79
v_mfma_f32_16x16x32_bf16 a[36:39],a[200:203],v[112:115],a[36:39]
v_exp_f32 v81,v81
v_mfma_f32_16x16x32_bf16 a[36:39],a[204:207],v[120:123],a[36:39]
ds_read_b64_tr_b16 a[198:199],v2 offset:12288
ds_read_b64_tr_b16 a[200:201],v3
v_mfma_f32_16x16x32_bf16 a[40:43],a[208:211],v[112:115],a[40:43]
ds_read_b64_tr_b16 a[202:203],v3 offset:4096
v_cvt_pk_bf16_f32 v80,v80,v81
v_mfma_f32_16x16x32_bf16 a[40:43],a[212:215],v[120:123],a[40:43]
v_exp_f32 v82,v82
v_mfma_f32_16x16x32_bf16 a[44:47],a[216:219],v[112:115],a[44:47]
ds_read_b64_tr_b16 a[204:205],v3 offset:8192
ds_read_b64_tr_b16 a[206:207],v3 offset:12288
v_mfma_f32_16x16x32_bf16 a[44:47],a[220:223],v[120:123],a[44:47]
v_exp_f32 v83,v83
v_mfma_f32_16x16x32_bf16 a[48:51],a[224:227],v[112:115],a[48:51]
ds_read_b64_tr_b16 a[208:209],v2 offset:256
ds_read_b64_tr_b16 a[210:211],v2 offset:4352
v_mfma_f32_16x16x32_bf16 a[48:51],a[228:231],v[120:123],a[48:51]
ds_read_b64_tr_b16 a[212:213],v2 offset:8448
v_cvt_pk_bf16_f32 v81,v82,v83
v_mfma_f32_16x16x32_bf16 a[52:55],a[232:235],v[112:115],a[52:55]
v_exp_f32 v84,v84
v_mfma_f32_16x16x32_bf16 a[52:55],a[236:239],v[120:123],a[52:55]
ds_read_b64_tr_b16 a[214:215],v2 offset:12544
ds_read_b64_tr_b16 a[216:217],v3 offset:256
v_mfma_f32_16x16x32_bf16 a[56:59],a[240:243],v[112:115],a[56:59]
v_exp_f32 v85,v85
v_mfma_f32_16x16x32_bf16 a[56:59],a[244:247],v[120:123],a[56:59]
ds_read_b64_tr_b16 a[218:219],v3 offset:4352
ds_read_b64_tr_b16 a[220:221],v3 offset:8448
v_mfma_f32_16x16x32_bf16 a[60:63],a[248:251],v[112:115],a[60:63]
global_load_lds_dwordx4 v4,s[10:11] offset:2048
ds_read_b64_tr_b16 a[222:223],v3 offset:12544
v_mfma_f32_16x16x32_bf16 a[60:63],a[252:255],v[120:123],a[60:63]
ds_read_b64_tr_b16 a[224:225],v2 offset:512
)A"
R"A(v_cvt_pk_bf16_f32 v82,v84,v85
v_mfma_f32_16x16x32_bf16 v[60:63],v[28:31],v[112:115],v[60:63]
v_exp_f32 v86,v86
v_mfma_f32_16x16x32_bf16 v[60:63],v[28:31],v[120:123],v[60:63]
ds_read_b64_tr_b16 a[226:227],v2 offset:4608
ds_read_b64_tr_b16 a[228:229],v2 offset:8704
v_mfma_f32_16x16x32_bf16 v[96:99],a[128:131],v[192:195],v[40:43]
v_exp_f32 v87,v87
v_mfma_f32_16x16x32_bf16 v[96:99],a[132:135],v[200:203],v[96:99]
ds_read_b64_tr_b16 a[230:231],v2 offset:12800
ds_read_b64_tr_b16 a[232:233],v3 offset:512
v_mfma_f32_16x16x32_bf16 v[96:99],a[136:139],v[208:211],v[96:99]
ds_read_b64_tr_b16 a[234:235],v3 offset:4608
v_cvt_pk_bf16_f32 v83,v86,v87
v_mfma_f32_16x16x32_bf16 v[96:99],a[140:143],v[216:219],v[96:99]
v_exp_f32 v88,v88
v_mfma_f32_16x16x32_bf16 v[100:103],a[156:159],v[216:219],v[40:43]
ds_read_b64_tr_b16 a[236:237],v3 offset:8704
ds_read_b64_tr_b16 a[238:239],v3 offset:12800
v_mfma_f32_16x16x32_bf16 v[100:103],a[152:155],v[208:211],v[100:103]
v_exp_f32 v89,v89
v_mfma_f32_16x16x32_bf16 v[100:103],a[148:151],v[200:203],v[100:103]
ds_read_b64_tr_b16 a[240:241],v2 offset:768
ds_read_b64_tr_b16 a[242:243],v2 offset:4864
v_mfma_f32_16x16x32_bf16 v[100:103],a[144:147],v[192:195],v[100:103]
ds_read_b64_tr_b16 a[244:245],v2 offset:8960
v_cvt_pk_bf16_f32 v88,v88,v89
v_mfma_f32_16x16x32_bf16 v[104:107],a[160:163],v[192:195],v[40:43]
ds_read_b64_tr_b16 a[246:247],v2 offset:13056
v_mfma_f32_16x16x32_bf16 v[104:107],a[164:167],v[200:203],v[104:107]
ds_read_b64_tr_b16 a[248:249],v3 offset:768
v_mfma_f32_16x16x32_bf16 v[104:107],a[168:171],v[208:211],v[104:107]
ds_read_b64_tr_b16 a[250:251],v3 offset:4864
ds_read_b64_tr_b16 a[252:253],v3 offset:8960
v_mfma_f32_16x16x32_bf16 v[104:107],a[172:175],v[216:219],v[104:107]
ds_read_b64_tr_b16 a[254:255],v3 offset:13056
v_mfma_f32_16x16x32_bf16 v[108:111],a[188:191],v[216:219],v[40:43]
global_load_lds_dwordx4 v5,s[10:11] offset:3072
v_mfma_f32_16x16x32_bf16 v[108:111],a[184:187],v[208:211],v[108:111]
v_mfma_f32_16x16x32_bf16 v[108:111],a[180:183],v[200:203],v[108:111]
v_mfma_f32_16x16x32_bf16 v[108:111],a[176:179],v[192:195],v[108:111]
s_waitcnt lgkmcnt(5)
v_mfma_f32_16x16x32_bf16 a[0:3],a[192:195],v[64:67],a[0:3]
v_mfma_f32_16x16x32_bf16 a[0:3],a[196:199],v[72:75],a[0:3]
v_mfma_f32_16x16x32_bf16 a[4:7],a[200:203],v[64:67],a[4:7]
v_mfma_f32_16x16x32_bf16 a[4:7],a[204:207],v[72:75],a[4:7]
v_mfma_f32_16x16x32_bf16 a[8:11],a[208:211],v[64:67],a[8:11]
v_mfma_f32_16x16x32_bf16 a[8:11],a[212:215],v[72:75],a[8:11]
v_mfma_f32_16x16x32_bf16 a[12:15],a[216:219],v[64:67],a[12:15]
v_mfma_f32_16x16x32_bf16 a[12:15],a[220:223],v[72:75],a[12:15]
v_mfma_f32_16x16x32_bf16 a[16:19],a[224:227],v[64:67],a[16:19]
v_mfma_f32_16x16x32_bf16 a[16:19],a[228:231],v[72:75],a[16:19]
v_mfma_f32_16x16x32_bf16 a[20:23],a[232:235],v[64:67],a[20:23]
s_getreg_b32 s92,hwreg(HW_REG_IB_STS,0,4)
s_min_u32 s4,s92,s6
s_mov_b32 s6,s92
v_mfma_f32_16x16x32_bf16 a[20:23],a[236:239],v[72:75],a[20:23]
s_cmp_ge_u32 s4,s9
s_cselect_b32 s4,15,s5
v_mfma_f32_16x16x32_bf16 a[24:27],a[240:243],v[64:67],a[24:27]
s_waitcnt lgkmcnt(0)
v_mfma_f32_16x16x32_bf16 a[24:27],a[244:247],v[72:75],a[24:27]
v_mfma_f32_16x16x32_bf16 a[28:31],a[248:251],v[64:67],a[28:31]
v_mfma_f32_16x16x32_bf16 a[28:31],a[252:255],v[72:75],a[28:31]
s_getpc_b64 s[92:93]
s_cmp_lg_u32 s4,0
s_cbranch_scc1 L_pace_%=
v_mfma_f32_16x16x32_bf16 v[48:51],v[28:31],v[64:67],v[48:51]
v_mfma_f32_16x16x32_bf16 v[48:51],v[28:31],v[72:75],v[48:51]
s_sub_u32 s70,s70,1
s_cmp_lg_u32 s70,0
s_cbranch_scc1 L_a1_%=
s_waitcnt vmcnt(16)
s_barrier
v_mfma_f32_16x16x32_bf16 v[112:115],a[128:131],v[224:227],v[44:47]
s_mul_i32 s86,s60,10240
s_mul_i32 s85,s63,1310720
s_mul_i32 s66,s64,320
v_mfma_f32_16x16x32_bf16 v[112:115],a[132:135],v[232:235],v[112:115]
s_add_u32 s85,s85,s66
s_add_u32 s83,s64,0
s_and_b32 s83,s83,3
v_mfma_f32_16x16x32_bf16 v[112:115],a[136:139],v[240:243],v[112:115]
s_lshl_b32 s83,s83,1
s_mul_i32 s83,s83,163840
s_add_u32 s83,s83,s85
v_mfma_f32_16x16x32_bf16 v[112:115],a[140:143],v[248:251],v[112:115]
s_cmp_eq_u32 s60,3
s_cselect_b32 s69,0x40000000,0
v_add_u32 v14,s69,v6
v_mfma_f32_16x16x32_bf16 v[116:119],a[156:159],v[248:251],v[44:47]
s_add_u32 s84,s83,20480
v_exp_f32 v90,v90
v_mfma_f32_16x16x32_bf16 v[116:119],a[152:155],v[240:243],v[116:119]
s_add_u32 m0,s86,122880
v_exp_f32 v91,v91
v_mfma_f32_16x16x32_bf16 v[116:119],a[148:151],v[232:235],v[116:119]
s_getpc_b64 s[8:9]
L_pcg_%=:
v_exp_f32 v92,v92
v_mfma_f32_16x16x32_bf16 v[116:119],a[144:147],v[224:227],v[116:119]
buffer_load_dwordx4 v24,s[48:51],s83 offen lds
v_mfma_f32_16x16x32_bf16 v[120:123],a[160:163],v[224:227],v[44:47]
s_mov_b32 s6,L_gbF_%=-L_pcg_%=
v_exp_f32 v93,v93
v_mfma_f32_16x16x32_bf16 v[120:123],a[164:167],v[232:235],v[120:123]
buffer_store_dwordx4 a[112:115],v14,s[40:43],s81 offen offset:3072 sc1
v_mfma_f32_16x16x32_bf16 v[120:123],a[168:171],v[240:243],v[120:123]
s_mov_b32 s10,L_geF_%=-L_gbF_%=
v_exp_f32 v94,v94
v_mfma_f32_16x16x32_bf16 v[120:123],a[172:175],v[248:251],v[120:123]
s_mov_b32 s11,0x20000
v_exp_f32 v95,v95
v_mfma_f32_16x16x32_bf16 v[124:127],a[188:191],v[248:251],v[44:47]
s_lshr_b32 s7,%1,3
v_cvt_pk_bf16_f32 v89,v90,v91
v_cvt_pk_bf16_f32 v90,v92,v93
v_mfma_f32_16x16x32_bf16 v[124:127],a[184:187],v[240:243],v[124:127]
s_lshl_b32 s7,s7,2
ds_read_b128 a[128:131],v1
v_cvt_pk_bf16_f32 v91,v94,v95
v_mfma_f32_16x16x32_bf16 v[124:127],a[180:183],v[232:235],v[124:127]
s_add_u32 s7,s7,s60
v_exp_f32 v96,v96
v_mfma_f32_16x16x32_bf16 v[124:127],a[176:179],v[224:227],v[124:127]
s_lshl_b32 s7,s7,7
ds_read_b128 a[132:135],v1 offset:256
v_add_u32 v2,s78,v2
v_mfma_f32_16x16x32_bf16 v[132:135],a[192:195],v[80:83],v[132:135]
s_lshl_b32 s66,s61,5
v_exp_f32 v97,v97
v_mfma_f32_16x16x32_bf16 v[132:135],a[196:199],v[88:91],v[132:135]
buffer_load_dwordx4 v25,s[48:51],s83 offen offset:1024 lds
v_mfma_f32_16x16x32_bf16 v[140:143],a[200:203],v[80:83],v[140:143]
s_lshl_b32 s67,s63,3
ds_read_b128 a[136:139],v1 offset:512
v_cvt_pk_bf16_f32 v96,v96,v97
v_mfma_f32_16x16x32_bf16 v[140:143],a[204:207],v[88:91],v[140:143]
s_add_u32 s66,s66,s67
v_exp_f32 v98,v98
v_mfma_f32_16x16x32_bf16 v[148:151],a[208:211],v[80:83],v[148:151]
s_lshl_b32 s66,s66,16
ds_read_b128 a[140:143],v1 offset:768
v_add_u32 v3,s78,v3
v_mfma_f32_16x16x32_bf16 v[148:151],a[212:215],v[88:91],v[148:151]
s_sub_u32 s78,s77,32768
v_exp_f32 v99,v99
v_mfma_f32_16x16x32_bf16 v[156:159],a[216:219],v[80:83],v[156:159]
s_and_b32 s78,s78,0x1ffff
ds_read_b128 a[156:159],v1 offset:4864
v_cvt_pk_bf16_f32 v97,v98,v99
v_mfma_f32_16x16x32_bf16 v[156:159],a[220:223],v[88:91],v[156:159]
s_sub_u32 s78,s78,s77
v_exp_f32 v100,v100
v_mfma_f32_16x16x32_bf16 v[164:167],a[224:227],v[80:83],v[164:167]
s_add_u32 s77,s77,s78
ds_read_b128 a[152:155],v1 offset:4608
v_lshlrev_b32 v4,10,v6
v_mfma_f32_16x16x32_bf16 v[164:167],a[228:231],v[88:91],v[164:167]
s_lshl_b32 s67,s60,14
v_exp_f32 v101,v101
v_mfma_f32_16x16x32_bf16 v[172:175],a[232:235],v[80:83],v[172:175]
buffer_store_dwordx4 a[116:119],v6,s[40:43],s81 offen offset:1024 sc1
v_mfma_f32_16x16x32_bf16 v[172:175],a[236:239],v[88:91],v[172:175]
s_add_u32 s66,s66,s67
ds_read_b128 a[148:151],v1 offset:4352
v_cvt_pk_bf16_f32 v98,v100,v101
v_mfma_f32_16x16x32_bf16 v[180:183],a[240:243],v[80:83],v[180:183]
buffer_load_dwordx4 v26,s[48:51],s83 offen offset:2048 lds
v_mfma_f32_16x16x32_bf16 v[180:183],a[244:247],v[88:91],v[180:183]
s_mov_b32 s30,1523712
ds_read_b128 a[144:147],v1 offset:4096
v_mov_b32 v8,s52
v_mfma_f32_16x16x32_bf16 v[188:191],a[248:251],v[80:83],v[188:191]
s_mov_b32 s31,0x20000
v_exp_f32 v102,v102
v_mfma_f32_16x16x32_bf16 v[188:191],a[252:255],v[88:91],v[188:191]
ds_read_b128 a[160:163],v1 offset:8192
v_mov_b32 v9,s53
v_mfma_f32_16x16x32_bf16 v[52:55],v[28:31],v[80:83],v[52:55]
v_exp_f32 v103,v103
v_mfma_f32_16x16x32_bf16 v[52:55],v[28:31],v[88:91],v[52:55]
s_waitcnt lgkmcnt(3)
ds_read_b128 a[164:167],v1 offset:8448
v_cvt_pk_bf16_f32 v99,v102,v103
v_mfma_f32_16x16x32_bf16 v[64:67],a[128:131],v[128:131],v[32:35]
v_exp_f32 v104,v104
v_mfma_f32_16x16x32_bf16 v[64:67],a[132:135],v[136:139],v[64:67]
ds_read_b128 a[168:171],v1 offset:8704
v_lshlrev_b32 v10,20,v6
v_mfma_f32_16x16x32_bf16 v[64:67],a[136:139],v[144:147],v[64:67]
v_exp_f32 v105,v105
v_mfma_f32_16x16x32_bf16 v[64:67],a[140:143],v[152:155],v[64:67]
buffer_load_dwordx4 v27,s[48:51],s83 offen offset:3072 lds
v_mfma_f32_16x16x32_bf16 v[68:71],a[156:159],v[152:155],v[32:35]
ds_read_b128 a[172:175],v1 offset:8960
v_cvt_pk_bf16_f32 v104,v104,v105
v_mfma_f32_16x16x32_bf16 v[68:71],a[152:155],v[144:147],v[68:71]
s_waitcnt lgkmcnt(3)
v_exp_f32 v106,v106
v_mfma_f32_16x16x32_bf16 v[68:71],a[148:151],v[136:139],v[68:71]
s_add_u32 s28,s12,s66
s_addc_u32 s29,s13,0
ds_read_b128 a[188:191],v1 offset:13056
v_mfma_f32_16x16x32_bf16 v[68:71],a[144:147],v[128:131],v[68:71]
s_and_b32 s29,s29,0xffff
v_exp_f32 v107,v107
v_mfma_f32_16x16x32_bf16 v[72:75],a[160:163],v[128:131],v[32:35]
s_waitcnt lgkmcnt(2)
ds_read_b128 a[184:187],v1 offset:12800
v_cvt_pk_bf16_f32 v105,v106,v107
v_mfma_f32_16x16x32_bf16 v[72:75],a[164:167],v[136:139],v[72:75]
buffer_store_dwordx4 a[120:123],v14,s[40:43],s82 offen offset:3072 sc1
v_mfma_f32_16x16x32_bf16 v[72:75],a[168:171],v[144:147],v[72:75]
s_waitcnt lgkmcnt(1)
ds_read_b128 a[180:183],v1 offset:12544
v_mfma_f32_16x16x32_bf16 v[72:75],a[172:175],v[152:155],v[72:75]
s_add_u32 m0,s86,126976
ds_read_b128 a[176:179],v1 offset:12288
v_add_u32 v1,s78,v1
v_mfma_f32_16x16x32_bf16 v[76:79],a[188:191],v[152:155],v[32:35]
s_waitcnt lgkmcnt(0)
v_exp_f32 v108,v108
v_mfma_f32_16x16x32_bf16 v[76:79],a[184:187],v[144:147],v[76:79]
buffer_load_dwordx4 v7,s[48:51],s83 offen lds
v_exp_f32 v109,v109
v_exp_f32 v110,v110
v_exp_f32 v111,v111
v_mfma_f32_16x16x32_bf16 v[76:79],a[180:183],v[136:139],v[76:79]
s_add_u32 s83,s64,0
v_cvt_pk_bf16_f32 v106,v108,v109
v_cvt_pk_bf16_f32 v107,v110,v111
v_mfma_f32_16x16x32_bf16 v[76:79],a[176:179],v[128:131],v[76:79]
s_and_b32 s83,s83,3
v_exp_f32 v112,v112
v_mfma_f32_16x16x32_bf16 v[196:199],a[192:195],v[96:99],v[196:199]
s_lshl_b32 s83,s83,1
v_exp_f32 v113,v113
v_mfma_f32_16x16x32_bf16 v[196:199],a[196:199],v[104:107],v[196:199]
s_or_b32 s83,s83,1
v_exp_f32 v114,v114
v_mfma_f32_16x16x32_bf16 v[204:207],a[200:203],v[96:99],v[204:207]
s_mul_i32 s83,s83,163840
v_exp_f32 v115,v115
v_mfma_f32_16x16x32_bf16 v[204:207],a[204:207],v[104:107],v[204:207]
s_add_u32 s83,s83,s85
v_exp_f32 v116,v116
v_mfma_f32_16x16x32_bf16 v[212:215],a[208:211],v[96:99],v[212:215]
v_exp_f32 v117,v117
v_mfma_f32_16x16x32_bf16 v[212:215],a[212:215],v[104:107],v[212:215]
s_add_u32 m0,s86,128000
v_exp_f32 v118,v118
v_mfma_f32_16x16x32_bf16 v[220:223],a[216:219],v[96:99],v[220:223]
v_exp_f32 v119,v119
v_mfma_f32_16x16x32_bf16 v[220:223],a[220:223],v[104:107],v[220:223]
buffer_load_dwordx4 v24,s[48:51],s84 offen lds
v_mfma_f32_16x16x32_bf16 v[228:231],a[224:227],v[96:99],v[228:231]
v_cvt_pk_bf16_f32 v112,v112,v113
v_cvt_pk_bf16_f32 v113,v114,v115
v_mfma_f32_16x16x32_bf16 v[228:231],a[228:231],v[104:107],v[228:231]
v_cvt_pk_bf16_f32 v114,v116,v117
s_nop 0
L_pd11_%=:
s_cbranch_execz L_t1c_%=
v_cvt_pk_bf16_f32 v115,v118,v119
v_mfma_f32_16x16x32_bf16 v[236:239],a[232:235],v[96:99],v[236:239]
buffer_store_dwordx4 a[124:127],v6,s[40:43],s82 offen offset:1024 sc1
v_mfma_f32_16x16x32_bf16 v[236:239],a[236:239],v[104:107],v[236:239]
v_exp_f32 v120,v120
v_mfma_f32_16x16x32_bf16 v[244:247],a[240:243],v[96:99],v[244:247]
v_exp_f32 v121,v121
v_mfma_f32_16x16x32_bf16 v[244:247],a[244:247],v[104:107],v[244:247]
v_exp_f32 v122,v122
v_mfma_f32_16x16x32_bf16 v[252:255],a[248:251],v[96:99],v[252:255]
v_exp_f32 v123,v123
v_mfma_f32_16x16x32_bf16 v[252:255],a[252:255],v[104:107],v[252:255]
v_exp_f32 v124,v124
v_mfma_f32_16x16x32_bf16 v[56:59],v[28:31],v[96:99],v[56:59]
v_exp_f32 v125,v125
v_mfma_f32_16x16x32_bf16 v[56:59],v[28:31],v[104:107],v[56:59]
buffer_load_dwordx4 v25,s[48:51],s84 offen offset:1024 lds
v_mfma_f32_16x16x32_bf16 v[80:83],a[128:131],v[160:163],v[36:39]
v_exp_f32 v126,v126
v_mfma_f32_16x16x32_bf16 v[80:83],a[132:135],v[168:171],v[80:83]
v_exp_f32 v127,v127
v_mfma_f32_16x16x32_bf16 v[80:83],a[136:139],v[176:179],v[80:83]
)A"
R"A(v_cvt_pk_bf16_f32 v120,v120,v121
v_cvt_pk_bf16_f32 v121,v122,v123
v_mfma_f32_16x16x32_bf16 v[80:83],a[140:143],v[184:187],v[80:83]
v_cvt_pk_bf16_f32 v122,v124,v125
v_cvt_pk_bf16_f32 v123,v126,v127
v_mfma_f32_16x16x32_bf16 v[84:87],a[156:159],v[184:187],v[36:39]
v_exp_f32 v64,v64
v_mfma_f32_16x16x32_bf16 v[84:87],a[152:155],v[176:179],v[84:87]
v_exp_f32 v65,v65
v_mfma_f32_16x16x32_bf16 v[84:87],a[148:151],v[168:171],v[84:87]
v_exp_f32 v66,v66
v_mfma_f32_16x16x32_bf16 v[84:87],a[144:147],v[160:163],v[84:87]
v_exp_f32 v67,v67
v_mfma_f32_16x16x32_bf16 v[88:91],a[160:163],v[160:163],v[36:39]
v_exp_f32 v68,v68
v_mfma_f32_16x16x32_bf16 v[88:91],a[164:167],v[168:171],v[88:91]
buffer_store_dwordx4 a[96:99],v14,s[40:43],s79 offen offset:3072 sc1
v_mfma_f32_16x16x32_bf16 v[88:91],a[168:171],v[176:179],v[88:91]
v_exp_f32 v69,v69
v_mfma_f32_16x16x32_bf16 v[88:91],a[172:175],v[184:187],v[88:91]
buffer_load_dwordx4 v26,s[48:51],s84 offen offset:2048 lds
v_mfma_f32_16x16x32_bf16 v[92:95],a[188:191],v[184:187],v[36:39]
v_exp_f32 v70,v70
v_mfma_f32_16x16x32_bf16 v[92:95],a[184:187],v[176:179],v[92:95]
v_exp_f32 v71,v71
v_mfma_f32_16x16x32_bf16 v[92:95],a[180:183],v[168:171],v[92:95]
v_cvt_pk_bf16_f32 v64,v64,v65
v_cvt_pk_bf16_f32 v65,v66,v67
v_mfma_f32_16x16x32_bf16 v[92:95],a[176:179],v[160:163],v[92:95]
v_cvt_pk_bf16_f32 v66,v68,v69
v_cvt_pk_bf16_f32 v67,v70,v71
v_mfma_f32_16x16x32_bf16 a[32:35],a[192:195],v[112:115],a[32:35]
ds_read_b64_tr_b16 a[192:193],v2
ds_read_b64_tr_b16 a[194:195],v2 offset:4096
v_mfma_f32_16x16x32_bf16 a[32:35],a[196:199],v[120:123],a[32:35]
v_exp_f32 v72,v72
v_mfma_f32_16x16x32_bf16 a[36:39],a[200:203],v[112:115],a[36:39]
ds_read_b64_tr_b16 a[196:197],v2 offset:8192
ds_read_b64_tr_b16 a[198:199],v2 offset:12288
v_mfma_f32_16x16x32_bf16 a[36:39],a[204:207],v[120:123],a[36:39]
buffer_load_dwordx4 v27,s[48:51],s84 offen offset:3072 lds
v_mfma_f32_16x16x32_bf16 a[40:43],a[208:211],v[112:115],a[40:43]
ds_read_b64_tr_b16 a[200:201],v3
ds_read_b64_tr_b16 a[202:203],v3 offset:4096
v_mfma_f32_16x16x32_bf16 a[40:43],a[212:215],v[120:123],a[40:43]
v_exp_f32 v73,v73
v_mfma_f32_16x16x32_bf16 a[44:47],a[216:219],v[112:115],a[44:47]
ds_read_b64_tr_b16 a[204:205],v3 offset:8192
ds_read_b64_tr_b16 a[206:207],v3 offset:12288
v_mfma_f32_16x16x32_bf16 a[44:47],a[220:223],v[120:123],a[44:47]
ds_read_b64_tr_b16 a[208:209],v2 offset:256
v_cvt_pk_bf16_f32 v72,v72,v73
v_mfma_f32_16x16x32_bf16 a[48:51],a[224:227],v[112:115],a[48:51]
v_exp_f32 v74,v74
v_mfma_f32_16x16x32_bf16 a[48:51],a[228:231],v[120:123],a[48:51]
ds_read_b64_tr_b16 a[210:211],v2 offset:4352
ds_read_b64_tr_b16 a[212:213],v2 offset:8448
v_mfma_f32_16x16x32_bf16 a[52:55],a[232:235],v[112:115],a[52:55]
buffer_store_dwordx4 a[100:103],v6,s[40:43],s79 offen offset:1024 sc1
v_mfma_f32_16x16x32_bf16 a[52:55],a[236:239],v[120:123],a[52:55]
s_add_u32 m0,s86,132096
ds_read_b64_tr_b16 a[214:215],v2 offset:12544
ds_read_b64_tr_b16 a[216:217],v3 offset:256
v_mfma_f32_16x16x32_bf16 a[56:59],a[240:243],v[112:115],a[56:59]
v_exp_f32 v75,v75
v_mfma_f32_16x16x32_bf16 a[56:59],a[244:247],v[120:123],a[56:59]
buffer_load_dwordx4 v7,s[48:51],s84 offen lds
v_mfma_f32_16x16x32_bf16 a[60:63],a[248:251],v[112:115],a[60:63]
s_add_u32 s84,s83,20480
ds_read_b64_tr_b16 a[218:219],v3 offset:4352
ds_read_b64_tr_b16 a[220:221],v3 offset:8448
v_mfma_f32_16x16x32_bf16 a[60:63],a[252:255],v[120:123],a[60:63]
v_exp_f32 v76,v76
v_mfma_f32_16x16x32_bf16 v[60:63],v[28:31],v[112:115],v[60:63]
v_exp_f32 v77,v77
v_mfma_f32_16x16x32_bf16 v[60:63],v[28:31],v[120:123],v[60:63]
v_exp_f32 v78,v78
v_mfma_f32_16x16x32_bf16 v[96:99],a[128:131],v[192:195],v[40:43]
v_exp_f32 v79,v79
v_mfma_f32_16x16x32_bf16 v[96:99],a[132:135],v[200:203],v[96:99]
ds_read_b64_tr_b16 a[222:223],v3 offset:12544
v_cvt_pk_bf16_f32 v73,v74,v75
v_mfma_f32_16x16x32_bf16 v[96:99],a[136:139],v[208:211],v[96:99]
v_cvt_pk_bf16_f32 v74,v76,v77
v_cvt_pk_bf16_f32 v75,v78,v79
v_mfma_f32_16x16x32_bf16 v[96:99],a[140:143],v[216:219],v[96:99]
ds_read_b64_tr_b16 a[224:225],v2 offset:512
ds_read_b64_tr_b16 a[226:227],v2 offset:4608
v_mfma_f32_16x16x32_bf16 v[100:103],a[156:159],v[216:219],v[40:43]
v_exp_f32 v80,v80
v_mfma_f32_16x16x32_bf16 v[100:103],a[152:155],v[208:211],v[100:103]
ds_read_b64_tr_b16 a[228:229],v2 offset:8704
ds_read_b64_tr_b16 a[230:231],v2 offset:12800
v_mfma_f32_16x16x32_bf16 v[100:103],a[148:151],v[200:203],v[100:103]
v_exp_f32 v81,v81
v_mfma_f32_16x16x32_bf16 v[100:103],a[144:147],v[192:195],v[100:103]
ds_read_b64_tr_b16 a[232:233],v3 offset:512
ds_read_b64_tr_b16 a[234:235],v3 offset:4608
v_mfma_f32_16x16x32_bf16 v[104:107],a[160:163],v[192:195],v[40:43]
ds_read_b64_tr_b16 a[236:237],v3 offset:8704
v_cvt_pk_bf16_f32 v80,v80,v81
v_mfma_f32_16x16x32_bf16 v[104:107],a[164:167],v[200:203],v[104:107]
buffer_store_dwordx4 a[104:107],v14,s[40:43],s80 offen offset:3072 sc1
v_mfma_f32_16x16x32_bf16 v[104:107],a[168:171],v[208:211],v[104:107]
ds_read_b64_tr_b16 a[238:239],v3 offset:12800
ds_read_b64_tr_b16 a[240:241],v2 offset:768
v_mfma_f32_16x16x32_bf16 v[104:107],a[172:175],v[216:219],v[104:107]
v_exp_f32 v82,v82
v_mfma_f32_16x16x32_bf16 v[108:111],a[188:191],v[216:219],v[40:43]
ds_read_b64_tr_b16 a[242:243],v2 offset:4864
ds_read_b64_tr_b16 a[244:245],v2 offset:8960
v_mfma_f32_16x16x32_bf16 v[108:111],a[184:187],v[208:211],v[108:111]
ds_read_b64_tr_b16 a[246:247],v2 offset:13056
v_add_u32 v2,s78,v2
v_mfma_f32_16x16x32_bf16 v[108:111],a[180:183],v[200:203],v[108:111]
v_exp_f32 v83,v83
v_mfma_f32_16x16x32_bf16 v[108:111],a[176:179],v[192:195],v[108:111]
s_waitcnt lgkmcnt(6)
ds_read_b64_tr_b16 a[248:249],v3 offset:768
ds_read_b64_tr_b16 a[250:251],v3 offset:4864
v_mfma_f32_16x16x32_bf16 a[0:3],a[192:195],v[64:67],a[0:3]
ds_read_b64_tr_b16 a[252:253],v3 offset:8960
v_cvt_pk_bf16_f32 v81,v82,v83
v_mfma_f32_16x16x32_bf16 a[0:3],a[196:199],v[72:75],a[0:3]
ds_read_b64_tr_b16 a[254:255],v3 offset:13056
v_add_u32 v3,s78,v3
v_mfma_f32_16x16x32_bf16 a[4:7],a[200:203],v[64:67],a[4:7]
s_sub_u32 s78,s77,32768
v_exp_f32 v84,v84
v_mfma_f32_16x16x32_bf16 a[4:7],a[204:207],v[72:75],a[4:7]
s_and_b32 s78,s78,0x1ffff
v_exp_f32 v85,v85
v_mfma_f32_16x16x32_bf16 a[8:11],a[208:211],v[64:67],a[8:11]
s_sub_u32 s78,s78,s77
v_exp_f32 v86,v86
v_mfma_f32_16x16x32_bf16 a[8:11],a[212:215],v[72:75],a[8:11]
s_add_u32 s77,s77,s78
v_exp_f32 v87,v87
v_mfma_f32_16x16x32_bf16 a[12:15],a[216:219],v[64:67],a[12:15]
v_cvt_pk_bf16_f32 v82,v84,v85
v_cvt_pk_bf16_f32 v83,v86,v87
v_mfma_f32_16x16x32_bf16 a[12:15],a[220:223],v[72:75],a[12:15]
v_exp_f32 v88,v88
v_mfma_f32_16x16x32_bf16 a[16:19],a[224:227],v[64:67],a[16:19]
v_exp_f32 v89,v89
v_mfma_f32_16x16x32_bf16 a[16:19],a[228:231],v[72:75],a[16:19]
s_waitcnt lgkmcnt(0)
v_exp_f32 v90,v90
v_mfma_f32_16x16x32_bf16 a[20:23],a[232:235],v[64:67],a[20:23]
buffer_store_dwordx4 a[108:111],v6,s[40:43],s80 offen offset:1024 sc1
v_mfma_f32_16x16x32_bf16 a[20:23],a[236:239],v[72:75],a[20:23]
v_exp_f32 v91,v91
v_mfma_f32_16x16x32_bf16 a[24:27],a[240:243],v[64:67],a[24:27]
v_exp_f32 v92,v92
v_mfma_f32_16x16x32_bf16 a[24:27],a[244:247],v[72:75],a[24:27]
v_exp_f32 v93,v93
v_mfma_f32_16x16x32_bf16 a[28:31],a[248:251],v[64:67],a[28:31]
v_exp_f32 v94,v94
v_mfma_f32_16x16x32_bf16 a[28:31],a[252:255],v[72:75],a[28:31]
v_exp_f32 v95,v95
v_mfma_f32_16x16x32_bf16 v[48:51],v[28:31],v[64:67],v[48:51]
v_cvt_pk_bf16_f32 v88,v88,v89
v_cvt_pk_bf16_f32 v89,v90,v91
v_mfma_f32_16x16x32_bf16 v[48:51],v[28:31],v[72:75],v[48:51]
v_cvt_pk_bf16_f32 v90,v92,v93
v_cvt_pk_bf16_f32 v91,v94,v95
s_waitcnt vmcnt(26)
s_barrier
v_mfma_f32_16x16x32_bf16 v[112:115],a[128:131],v[224:227],v[44:47]
v_exp_f32 v96,v96
v_mfma_f32_16x16x32_bf16 v[112:115],a[132:135],v[232:235],v[112:115]
v_exp_f32 v97,v97
v_mfma_f32_16x16x32_bf16 v[112:115],a[136:139],v[240:243],v[112:115]
ds_read_b128 a[128:131],v1
v_cvt_pk_bf16_f32 v96,v96,v97
v_mfma_f32_16x16x32_bf16 v[112:115],a[140:143],v[248:251],v[112:115]
v_exp_f32 v98,v98
v_mfma_f32_16x16x32_bf16 v[116:119],a[156:159],v[248:251],v[44:47]
v_exp_f32 v99,v99
v_mfma_f32_16x16x32_bf16 v[116:119],a[152:155],v[240:243],v[116:119]
s_add_u32 m0,s86,81920
ds_read_b128 a[132:135],v1 offset:256
v_cvt_pk_bf16_f32 v97,v98,v99
v_mfma_f32_16x16x32_bf16 v[116:119],a[148:151],v[232:235],v[116:119]
v_exp_f32 v100,v100
v_mfma_f32_16x16x32_bf16 v[116:119],a[144:147],v[224:227],v[116:119]
buffer_load_dwordx4 v24,s[48:51],s83 offen lds
v_mfma_f32_16x16x32_bf16 v[120:123],a[160:163],v[224:227],v[44:47]
v_exp_f32 v101,v101
v_mfma_f32_16x16x32_bf16 v[120:123],a[164:167],v[232:235],v[120:123]
buffer_store_dwordx4 a[64:67],v6,s[40:43],s79 offen offset:2048 sc1
v_mfma_f32_16x16x32_bf16 v[120:123],a[168:171],v[240:243],v[120:123]
ds_read_b128 a[136:139],v1 offset:512
v_cvt_pk_bf16_f32 v98,v100,v101
v_mfma_f32_16x16x32_bf16 v[120:123],a[172:175],v[248:251],v[120:123]
v_exp_f32 v102,v102
v_mfma_f32_16x16x32_bf16 v[124:127],a[188:191],v[248:251],v[44:47]
v_exp_f32 v103,v103
v_mfma_f32_16x16x32_bf16 v[124:127],a[184:187],v[240:243],v[124:127]
ds_read_b128 a[140:143],v1 offset:768
v_cvt_pk_bf16_f32 v99,v102,v103
v_mfma_f32_16x16x32_bf16 v[124:127],a[180:183],v[232:235],v[124:127]
v_exp_f32 v104,v104
v_mfma_f32_16x16x32_bf16 v[124:127],a[176:179],v[224:227],v[124:127]
v_exp_f32 v105,v105
v_mfma_f32_16x16x32_bf16 v[132:135],a[192:195],v[80:83],v[132:135]
ds_read_b128 a[156:159],v1 offset:4864
v_cvt_pk_bf16_f32 v104,v104,v105
v_mfma_f32_16x16x32_bf16 v[132:135],a[196:199],v[88:91],v[132:135]
buffer_load_dwordx4 v25,s[48:51],s83 offen offset:1024 lds
v_mfma_f32_16x16x32_bf16 v[140:143],a[200:203],v[80:83],v[140:143]
v_exp_f32 v106,v106
v_mfma_f32_16x16x32_bf16 v[140:143],a[204:207],v[88:91],v[140:143]
v_exp_f32 v107,v107
v_mfma_f32_16x16x32_bf16 v[148:151],a[208:211],v[80:83],v[148:151]
ds_read_b128 a[152:155],v1 offset:4608
v_cvt_pk_bf16_f32 v105,v106,v107
v_mfma_f32_16x16x32_bf16 v[148:151],a[212:215],v[88:91],v[148:151]
v_exp_f32 v108,v108
v_mfma_f32_16x16x32_bf16 v[156:159],a[216:219],v[80:83],v[156:159]
v_exp_f32 v109,v109
v_mfma_f32_16x16x32_bf16 v[156:159],a[220:223],v[88:91],v[156:159]
ds_read_b128 a[148:151],v1 offset:4352
v_cvt_pk_bf16_f32 v106,v108,v109
v_mfma_f32_16x16x32_bf16 v[164:167],a[224:227],v[80:83],v[164:167]
v_exp_f32 v110,v110
v_mfma_f32_16x16x32_bf16 v[164:167],a[228:231],v[88:91],v[164:167]
buffer_store_dwordx4 a[68:71],v6,s[40:43],s79 offen sc1
v_mfma_f32_16x16x32_bf16 v[172:175],a[232:235],v[80:83],v[172:175]
s_add_u32 s79,s79,65536
v_exp_f32 v111,v111
v_mfma_f32_16x16x32_bf16 v[172:175],a[236:239],v[88:91],v[172:175]
buffer_load_dwordx4 v26,s[48:51],s83 offen offset:2048 lds
v_mfma_f32_16x16x32_bf16 v[180:183],a[240:243],v[80:83],v[180:183]
ds_read_b128 a[144:147],v1 offset:4096
v_cvt_pk_bf16_f32 v107,v110,v111
v_mfma_f32_16x16x32_bf16 v[180:183],a[244:247],v[88:91],v[180:183]
v_exp_f32 v112,v112
v_mfma_f32_16x16x32_bf16 v[188:191],a[248:251],v[80:83],v[188:191]
ds_read_b128 a[160:163],v1 offset:8192
v_mfma_f32_16x16x32_bf16 v[188:191],a[252:255],v[88:91],v[188:191]
v_exp_f32 v113,v113
v_mfma_f32_16x16x32_bf16 v[52:55],v[28:31],v[80:83],v[52:55]
ds_read_b128 a[164:167],v1 offset:8448
v_cvt_pk_bf16_f32 v112,v112,v113
v_mfma_f32_16x16x32_bf16 v[52:55],v[28:31],v[88:91],v[52:55]
s_waitcnt lgkmcnt(3)
v_exp_f32 v114,v114
v_mfma_f32_16x16x32_bf16 v[64:67],a[128:131],v[128:131],v[32:35]
ds_read_b128 a[168:171],v1 offset:8704
v_mfma_f32_16x16x32_bf16 v[64:67],a[132:135],v[136:139],v[64:67]
v_exp_f32 v115,v115
v_mfma_f32_16x16x32_bf16 v[64:67],a[136:139],v[144:147],v[64:67]
ds_read_b128 a[172:175],v1 offset:8960
v_cvt_pk_bf16_f32 v113,v114,v115
v_mfma_f32_16x16x32_bf16 v[64:67],a[140:143],v[152:155],v[64:67]
buffer_load_dwordx4 v27,s[48:51],s83 offen offset:3072 lds
v_mfma_f32_16x16x32_bf16 v[68:71],a[156:159],v[152:155],v[32:35]
ds_read_b128 a[188:191],v1 offset:13056
v_mfma_f32_16x16x32_bf16 v[68:71],a[152:155],v[144:147],v[68:71]
v_exp_f32 v116,v116
v_mfma_f32_16x16x32_bf16 v[68:71],a[148:151],v[136:139],v[68:71]
s_waitcnt lgkmcnt(3)
ds_read_b128 a[184:187],v1 offset:12800
v_mfma_f32_16x16x32_bf16 v[68:71],a[144:147],v[128:131],v[68:71]
)A"
R"A(v_exp_f32 v117,v117
v_mfma_f32_16x16x32_bf16 v[72:75],a[160:163],v[128:131],v[32:35]
buffer_store_dwordx4 a[72:75],v6,s[40:43],s80 offen offset:2048 sc1
v_mfma_f32_16x16x32_bf16 v[72:75],a[164:167],v[136:139],v[72:75]
s_waitcnt lgkmcnt(3)
ds_read_b128 a[180:183],v1 offset:12544
v_cvt_pk_bf16_f32 v114,v116,v117
v_mfma_f32_16x16x32_bf16 v[72:75],a[168:171],v[144:147],v[72:75]
s_waitcnt lgkmcnt(2)
v_exp_f32 v118,v118
v_mfma_f32_16x16x32_bf16 v[72:75],a[172:175],v[152:155],v[72:75]
s_add_u32 m0,s86,86016
ds_read_b128 a[176:179],v1 offset:12288
v_add_u32 v1,s78,v1
v_mfma_f32_16x16x32_bf16 v[76:79],a[188:191],v[152:155],v[32:35]
s_waitcnt lgkmcnt(0)
v_exp_f32 v119,v119
v_mfma_f32_16x16x32_bf16 v[76:79],a[184:187],v[144:147],v[76:79]
buffer_load_dwordx4 v7,s[48:51],s83 offen lds
v_mfma_f32_16x16x32_bf16 v[76:79],a[180:183],v[136:139],v[76:79]
s_add_u32 s83,s64,1
v_exp_f32 v120,v120
v_mfma_f32_16x16x32_bf16 v[76:79],a[176:179],v[128:131],v[76:79]
s_and_b32 s83,s83,3
v_exp_f32 v121,v121
v_mfma_f32_16x16x32_bf16 v[196:199],a[192:195],v[96:99],v[196:199]
s_lshl_b32 s83,s83,1
v_cvt_pk_bf16_f32 v115,v118,v119
v_cvt_pk_bf16_f32 v120,v120,v121
v_mfma_f32_16x16x32_bf16 v[196:199],a[196:199],v[104:107],v[196:199]
s_mul_i32 s83,s83,163840
v_exp_f32 v122,v122
v_mfma_f32_16x16x32_bf16 v[204:207],a[200:203],v[96:99],v[204:207]
s_add_u32 s83,s83,s85
v_exp_f32 v123,v123
v_mfma_f32_16x16x32_bf16 v[204:207],a[204:207],v[104:107],v[204:207]
v_exp_f32 v124,v124
v_mfma_f32_16x16x32_bf16 v[212:215],a[208:211],v[96:99],v[212:215]
v_exp_f32 v125,v125
v_mfma_f32_16x16x32_bf16 v[212:215],a[212:215],v[104:107],v[212:215]
s_add_u32 m0,s86,87040
v_exp_f32 v126,v126
v_mfma_f32_16x16x32_bf16 v[220:223],a[216:219],v[96:99],v[220:223]
v_exp_f32 v127,v127
v_mfma_f32_16x16x32_bf16 v[220:223],a[220:223],v[104:107],v[220:223]
buffer_load_dwordx4 v24,s[48:51],s84 offen lds
v_mfma_f32_16x16x32_bf16 v[228:231],a[224:227],v[96:99],v[228:231]
v_cvt_pk_bf16_f32 v121,v122,v123
v_cvt_pk_bf16_f32 v122,v124,v125
v_mfma_f32_16x16x32_bf16 v[228:231],a[228:231],v[104:107],v[228:231]
buffer_store_dwordx4 a[76:79],v6,s[40:43],s80 offen sc1
v_mfma_f32_16x16x32_bf16 v[236:239],a[232:235],v[96:99],v[236:239]
s_add_u32 s80,s80,65536
v_exp_f32 v64,v64
v_mfma_f32_16x16x32_bf16 v[236:239],a[236:239],v[104:107],v[236:239]
v_exp_f32 v65,v65
v_mfma_f32_16x16x32_bf16 v[244:247],a[240:243],v[96:99],v[244:247]
v_cvt_pk_bf16_f32 v123,v126,v127
v_cvt_pk_bf16_f32 v64,v64,v65
v_mfma_f32_16x16x32_bf16 v[244:247],a[244:247],v[104:107],v[244:247]
v_exp_f32 v66,v66
v_mfma_f32_16x16x32_bf16 v[252:255],a[248:251],v[96:99],v[252:255]
v_exp_f32 v67,v67
v_mfma_f32_16x16x32_bf16 v[252:255],a[252:255],v[104:107],v[252:255]
v_exp_f32 v68,v68
v_mfma_f32_16x16x32_bf16 v[56:59],v[28:31],v[96:99],v[56:59]
v_exp_f32 v69,v69
v_mfma_f32_16x16x32_bf16 v[56:59],v[28:31],v[104:107],v[56:59]
buffer_load_dwordx4 v25,s[48:51],s84 offen offset:1024 lds
v_mfma_f32_16x16x32_bf16 v[80:83],a[128:131],v[160:163],v[36:39]
v_exp_f32 v70,v70
v_mfma_f32_16x16x32_bf16 v[80:83],a[132:135],v[168:171],v[80:83]
v_exp_f32 v71,v71
v_mfma_f32_16x16x32_bf16 v[80:83],a[136:139],v[176:179],v[80:83]
v_cvt_pk_bf16_f32 v65,v66,v67
v_cvt_pk_bf16_f32 v66,v68,v69
v_mfma_f32_16x16x32_bf16 v[80:83],a[140:143],v[184:187],v[80:83]
v_exp_f32 v72,v72
v_mfma_f32_16x16x32_bf16 v[84:87],a[156:159],v[184:187],v[36:39]
v_exp_f32 v73,v73
v_mfma_f32_16x16x32_bf16 v[84:87],a[152:155],v[176:179],v[84:87]
v_cvt_pk_bf16_f32 v67,v70,v71
v_cvt_pk_bf16_f32 v72,v72,v73
v_mfma_f32_16x16x32_bf16 v[84:87],a[148:151],v[168:171],v[84:87]
v_exp_f32 v74,v74
v_mfma_f32_16x16x32_bf16 v[84:87],a[144:147],v[160:163],v[84:87]
v_exp_f32 v75,v75
v_mfma_f32_16x16x32_bf16 v[88:91],a[160:163],v[160:163],v[36:39]
buffer_store_dwordx4 a[80:83],v6,s[40:43],s81 offen offset:2048 sc1
v_mfma_f32_16x16x32_bf16 v[88:91],a[164:167],v[168:171],v[88:91]
v_exp_f32 v76,v76
v_mfma_f32_16x16x32_bf16 v[88:91],a[168:171],v[176:179],v[88:91]
buffer_load_dwordx4 v26,s[48:51],s84 offen offset:2048 lds
v_mfma_f32_16x16x32_bf16 v[88:91],a[172:175],v[184:187],v[88:91]
v_exp_f32 v77,v77
v_mfma_f32_16x16x32_bf16 v[92:95],a[188:191],v[184:187],v[36:39]
v_exp_f32 v78,v78
v_mfma_f32_16x16x32_bf16 v[92:95],a[184:187],v[176:179],v[92:95]
v_exp_f32 v79,v79
v_mfma_f32_16x16x32_bf16 v[92:95],a[180:183],v[168:171],v[92:95]
v_cvt_pk_bf16_f32 v73,v74,v75
v_cvt_pk_bf16_f32 v74,v76,v77
v_mfma_f32_16x16x32_bf16 v[92:95],a[176:179],v[160:163],v[92:95]
v_exp_f32 v80,v80
v_mfma_f32_16x16x32_bf16 a[32:35],a[192:195],v[112:115],a[32:35]
ds_read_b64_tr_b16 a[192:193],v2
ds_read_b64_tr_b16 a[194:195],v2 offset:4096
v_mfma_f32_16x16x32_bf16 a[32:35],a[196:199],v[120:123],a[32:35]
ds_read_b64_tr_b16 a[196:197],v2 offset:8192
v_cvt_pk_bf16_f32 v75,v78,v79
v_mfma_f32_16x16x32_bf16 a[36:39],a[200:203],v[112:115],a[36:39]
v_exp_f32 v81,v81
v_mfma_f32_16x16x32_bf16 a[36:39],a[204:207],v[120:123],a[36:39]
buffer_load_dwordx4 v27,s[48:51],s84 offen offset:3072 lds
v_mfma_f32_16x16x32_bf16 a[40:43],a[208:211],v[112:115],a[40:43]
ds_read_b64_tr_b16 a[198:199],v2 offset:12288
ds_read_b64_tr_b16 a[200:201],v3
v_mfma_f32_16x16x32_bf16 a[40:43],a[212:215],v[120:123],a[40:43]
ds_read_b64_tr_b16 a[202:203],v3 offset:4096
v_cvt_pk_bf16_f32 v80,v80,v81
v_mfma_f32_16x16x32_bf16 a[44:47],a[216:219],v[112:115],a[44:47]
v_exp_f32 v82,v82
v_mfma_f32_16x16x32_bf16 a[44:47],a[220:223],v[120:123],a[44:47]
ds_read_b64_tr_b16 a[204:205],v3 offset:8192
ds_read_b64_tr_b16 a[206:207],v3 offset:12288
v_mfma_f32_16x16x32_bf16 a[48:51],a[224:227],v[112:115],a[48:51]
v_exp_f32 v83,v83
v_mfma_f32_16x16x32_bf16 a[48:51],a[228:231],v[120:123],a[48:51]
buffer_store_dwordx4 a[84:87],v6,s[40:43],s81 offen sc1
v_mfma_f32_16x16x32_bf16 a[52:55],a[232:235],v[112:115],a[52:55]
s_add_u32 s81,s81,65536
ds_read_b64_tr_b16 a[208:209],v2 offset:256
ds_read_b64_tr_b16 a[210:211],v2 offset:4352
v_mfma_f32_16x16x32_bf16 a[52:55],a[236:239],v[120:123],a[52:55]
s_add_u32 m0,s86,91136
ds_read_b64_tr_b16 a[212:213],v2 offset:8448
v_cvt_pk_bf16_f32 v81,v82,v83
v_mfma_f32_16x16x32_bf16 a[56:59],a[240:243],v[112:115],a[56:59]
v_exp_f32 v84,v84
v_mfma_f32_16x16x32_bf16 a[56:59],a[244:247],v[120:123],a[56:59]
buffer_load_dwordx4 v7,s[48:51],s84 offen lds
v_mfma_f32_16x16x32_bf16 a[60:63],a[248:251],v[112:115],a[60:63]
s_add_u32 s84,s83,20480
ds_read_b64_tr_b16 a[214:215],v2 offset:12544
ds_read_b64_tr_b16 a[216:217],v3 offset:256
v_mfma_f32_16x16x32_bf16 a[60:63],a[252:255],v[120:123],a[60:63]
v_exp_f32 v85,v85
v_mfma_f32_16x16x32_bf16 v[60:63],v[28:31],v[112:115],v[60:63]
ds_read_b64_tr_b16 a[218:219],v3 offset:4352
ds_read_b64_tr_b16 a[220:221],v3 offset:8448
v_mfma_f32_16x16x32_bf16 v[60:63],v[28:31],v[120:123],v[60:63]
ds_read_b64_tr_b16 a[222:223],v3 offset:12544
v_cvt_pk_bf16_f32 v82,v84,v85
v_mfma_f32_16x16x32_bf16 v[96:99],a[128:131],v[192:195],v[40:43]
v_exp_f32 v86,v86
v_mfma_f32_16x16x32_bf16 v[96:99],a[132:135],v[200:203],v[96:99]
ds_read_b64_tr_b16 a[224:225],v2 offset:512
ds_read_b64_tr_b16 a[226:227],v2 offset:4608
v_mfma_f32_16x16x32_bf16 v[96:99],a[136:139],v[208:211],v[96:99]
v_exp_f32 v87,v87
v_mfma_f32_16x16x32_bf16 v[96:99],a[140:143],v[216:219],v[96:99]
ds_read_b64_tr_b16 a[228:229],v2 offset:8704
ds_read_b64_tr_b16 a[230:231],v2 offset:12800
v_mfma_f32_16x16x32_bf16 v[100:103],a[156:159],v[216:219],v[40:43]
ds_read_b64_tr_b16 a[232:233],v3 offset:512
v_cvt_pk_bf16_f32 v83,v86,v87
v_mfma_f32_16x16x32_bf16 v[100:103],a[152:155],v[208:211],v[100:103]
v_exp_f32 v88,v88
v_mfma_f32_16x16x32_bf16 v[100:103],a[148:151],v[200:203],v[100:103]
ds_read_b64_tr_b16 a[234:235],v3 offset:4608
ds_read_b64_tr_b16 a[236:237],v3 offset:8704
v_mfma_f32_16x16x32_bf16 v[100:103],a[144:147],v[192:195],v[100:103]
v_exp_f32 v89,v89
v_mfma_f32_16x16x32_bf16 v[104:107],a[160:163],v[192:195],v[40:43]
buffer_store_dwordx4 a[88:91],v6,s[40:43],s82 offen offset:2048 sc1
v_mfma_f32_16x16x32_bf16 v[104:107],a[164:167],v[200:203],v[104:107]
ds_read_b64_tr_b16 a[238:239],v3 offset:12800
ds_read_b64_tr_b16 a[240:241],v2 offset:768
v_mfma_f32_16x16x32_bf16 v[104:107],a[168:171],v[208:211],v[104:107]
ds_read_b64_tr_b16 a[242:243],v2 offset:4864
v_cvt_pk_bf16_f32 v88,v88,v89
v_mfma_f32_16x16x32_bf16 v[104:107],a[172:175],v[216:219],v[104:107]
v_exp_f32 v90,v90
v_mfma_f32_16x16x32_bf16 v[108:111],a[188:191],v[216:219],v[40:43]
ds_read_b64_tr_b16 a[244:245],v2 offset:8960
ds_read_b64_tr_b16 a[246:247],v2 offset:13056
v_mfma_f32_16x16x32_bf16 v[108:111],a[184:187],v[208:211],v[108:111]
ds_read_b64_tr_b16 a[248:249],v3 offset:768
v_add_u32 v2,s78,v2
v_mfma_f32_16x16x32_bf16 v[108:111],a[180:183],v[200:203],v[108:111]
v_exp_f32 v91,v91
v_mfma_f32_16x16x32_bf16 v[108:111],a[176:179],v[192:195],v[108:111]
s_waitcnt lgkmcnt(6)
ds_read_b64_tr_b16 a[250:251],v3 offset:4864
ds_read_b64_tr_b16 a[252:253],v3 offset:8960
v_mfma_f32_16x16x32_bf16 a[0:3],a[192:195],v[64:67],a[0:3]
ds_read_b64_tr_b16 a[254:255],v3 offset:13056
v_cvt_pk_bf16_f32 v89,v90,v91
v_mfma_f32_16x16x32_bf16 a[0:3],a[196:199],v[72:75],a[0:3]
v_exp_f32 v92,v92
v_mfma_f32_16x16x32_bf16 a[4:7],a[200:203],v[64:67],a[4:7]
v_exp_f32 v93,v93
v_mfma_f32_16x16x32_bf16 a[4:7],a[204:207],v[72:75],a[4:7]
v_exp_f32 v94,v94
v_mfma_f32_16x16x32_bf16 a[8:11],a[208:211],v[64:67],a[8:11]
v_exp_f32 v95,v95
v_mfma_f32_16x16x32_bf16 a[8:11],a[212:215],v[72:75],a[8:11]
v_cvt_pk_bf16_f32 v90,v92,v93
v_cvt_pk_bf16_f32 v91,v94,v95
v_mfma_f32_16x16x32_bf16 a[12:15],a[216:219],v[64:67],a[12:15]
s_cmp_ge_u32 s63,2
s_cselect_b32 s6,L_gbS_%=-L_pcg_%=,s6
s_cselect_b32 s10,L_geS_%=-L_gbS_%=,s10
v_mfma_f32_16x16x32_bf16 a[12:15],a[220:223],v[72:75],a[12:15]
s_add_u32 s8,s8,s6
s_addc_u32 s9,s9,0
s_and_b32 s8,s8,0xffffff80
v_mfma_f32_16x16x32_bf16 a[16:19],a[224:227],v[64:67],a[16:19]
s_and_b32 s9,s9,0xffff
v_exp_f32 v96,v96
v_mfma_f32_16x16x32_bf16 a[16:19],a[228:231],v[72:75],a[16:19]
buffer_store_dwordx4 a[92:95],v6,s[40:43],s82 offen sc1
v_mfma_f32_16x16x32_bf16 a[20:23],a[232:235],v[64:67],a[20:23]
s_add_u32 s82,s82,65536
v_exp_f32 v97,v97
s_waitcnt lgkmcnt(0)
v_mfma_f32_16x16x32_bf16 a[20:23],a[236:239],v[72:75],a[20:23]
v_exp_f32 v98,v98
v_mfma_f32_16x16x32_bf16 a[24:27],a[240:243],v[64:67],a[24:27]
v_exp_f32 v99,v99
v_mfma_f32_16x16x32_bf16 a[24:27],a[244:247],v[72:75],a[24:27]
v_exp_f32 v100,v100
v_mfma_f32_16x16x32_bf16 a[28:31],a[248:251],v[64:67],a[28:31]
v_exp_f32 v101,v101
v_mfma_f32_16x16x32_bf16 a[28:31],a[252:255],v[72:75],a[28:31]
v_exp_f32 v102,v102
v_mfma_f32_16x16x32_bf16 v[48:51],v[28:31],v[64:67],v[48:51]
v_exp_f32 v103,v103
v_mfma_f32_16x16x32_bf16 v[48:51],v[28:31],v[72:75],v[48:51]
v_cvt_pk_bf16_f32 v96,v96,v97
v_cvt_pk_bf16_f32 v97,v98,v99
s_waitcnt vmcnt(36)
s_barrier
v_mfma_f32_16x16x32_bf16 v[112:115],a[128:131],v[224:227],v[44:47]
ds_read_b128 a[128:131],v1
v_cvt_pk_bf16_f32 v98,v100,v101
v_mfma_f32_16x16x32_bf16 v[112:115],a[132:135],v[232:235],v[112:115]
v_cvt_pk_bf16_f32 v99,v102,v103
v_add_u32 v3,s78,v3
v_mfma_f32_16x16x32_bf16 v[112:115],a[136:139],v[240:243],v[112:115]
s_sub_u32 s78,s77,32768
v_exp_f32 v104,v104
v_mfma_f32_16x16x32_bf16 v[112:115],a[140:143],v[248:251],v[112:115]
s_and_b32 s78,s78,0x1ffff
v_exp_f32 v105,v105
v_mfma_f32_16x16x32_bf16 v[116:119],a[156:159],v[248:251],v[44:47]
s_sub_u32 s78,s78,s77
ds_read_b128 a[132:135],v1 offset:256
v_cvt_pk_bf16_f32 v104,v104,v105
v_mfma_f32_16x16x32_bf16 v[116:119],a[152:155],v[240:243],v[116:119]
s_add_u32 m0,s86,40960
v_exp_f32 v106,v106
v_mfma_f32_16x16x32_bf16 v[116:119],a[148:151],v[232:235],v[116:119]
s_add_u32 s77,s77,s78
v_exp_f32 v107,v107
v_mfma_f32_16x16x32_bf16 v[116:119],a[144:147],v[224:227],v[116:119]
buffer_load_dwordx4 v24,s[48:51],s83 offen lds
v_mfma_f32_16x16x32_bf16 v[120:123],a[160:163],v[224:227],v[44:47]
ds_read_b128 a[136:139],v1 offset:512
v_cvt_pk_bf16_f32 v105,v106,v107
v_mfma_f32_16x16x32_bf16 v[120:123],a[164:167],v[232:235],v[120:123]
v_exp_f32 v108,v108
v_mfma_f32_16x16x32_bf16 v[120:123],a[168:171],v[240:243],v[120:123]
v_exp_f32 v109,v109
v_mfma_f32_16x16x32_bf16 v[120:123],a[172:175],v[248:251],v[120:123]
ds_read_b128 a[140:143],v1 offset:768
)A"
R"A(v_cvt_pk_bf16_f32 v106,v108,v109
v_mfma_f32_16x16x32_bf16 v[124:127],a[188:191],v[248:251],v[44:47]
v_exp_f32 v110,v110
v_mfma_f32_16x16x32_bf16 v[124:127],a[184:187],v[240:243],v[124:127]
v_exp_f32 v111,v111
v_mfma_f32_16x16x32_bf16 v[124:127],a[180:183],v[232:235],v[124:127]
ds_read_b128 a[156:159],v1 offset:4864
v_cvt_pk_bf16_f32 v107,v110,v111
v_mfma_f32_16x16x32_bf16 v[124:127],a[176:179],v[224:227],v[124:127]
v_exp_f32 v112,v112
v_mfma_f32_16x16x32_bf16 v[132:135],a[192:195],v[80:83],v[132:135]
v_exp_f32 v113,v113
v_mfma_f32_16x16x32_bf16 v[132:135],a[196:199],v[88:91],v[132:135]
buffer_load_dwordx4 v25,s[48:51],s83 offen offset:1024 lds
v_mfma_f32_16x16x32_bf16 v[140:143],a[200:203],v[80:83],v[140:143]
ds_read_b128 a[152:155],v1 offset:4608
v_cvt_pk_bf16_f32 v112,v112,v113
v_mfma_f32_16x16x32_bf16 v[140:143],a[204:207],v[88:91],v[140:143]
v_exp_f32 v114,v114
v_mfma_f32_16x16x32_bf16 v[148:151],a[208:211],v[80:83],v[148:151]
v_exp_f32 v115,v115
v_mfma_f32_16x16x32_bf16 v[148:151],a[212:215],v[88:91],v[148:151]
ds_read_b128 a[148:151],v1 offset:4352
v_cvt_pk_bf16_f32 v113,v114,v115
v_mfma_f32_16x16x32_bf16 v[156:159],a[216:219],v[80:83],v[156:159]
L_pd12_%=:
s_cbranch_execz L_trt1_%=
v_exp_f32 v116,v116
v_mfma_f32_16x16x32_bf16 v[156:159],a[220:223],v[88:91],v[156:159]
v_exp_f32 v117,v117
v_mfma_f32_16x16x32_bf16 v[164:167],a[224:227],v[80:83],v[164:167]
buffer_load_dword v5,v4,s[8:11],s7 offen
v_mfma_f32_16x16x32_bf16 v[164:167],a[228:231],v[88:91],v[164:167]
ds_read_b128 a[144:147],v1 offset:4096
v_cvt_pk_bf16_f32 v114,v116,v117
v_mfma_f32_16x16x32_bf16 v[172:175],a[232:235],v[80:83],v[172:175]
v_exp_f32 v118,v118
v_mfma_f32_16x16x32_bf16 v[172:175],a[236:239],v[88:91],v[172:175]
v_exp_f32 v119,v119
v_mfma_f32_16x16x32_bf16 v[180:183],a[240:243],v[80:83],v[180:183]
ds_read_b128 a[160:163],v1 offset:8192
v_cvt_pk_bf16_f32 v115,v118,v119
v_mfma_f32_16x16x32_bf16 v[180:183],a[244:247],v[88:91],v[180:183]
v_exp_f32 v120,v120
v_mfma_f32_16x16x32_bf16 v[188:191],a[248:251],v[80:83],v[188:191]
buffer_load_dwordx4 v26,s[48:51],s83 offen offset:2048 lds
v_mfma_f32_16x16x32_bf16 v[188:191],a[252:255],v[88:91],v[188:191]
ds_read_b128 a[164:167],v1 offset:8448
v_mfma_f32_16x16x32_bf16 v[52:55],v[28:31],v[80:83],v[52:55]
v_exp_f32 v121,v121
v_mfma_f32_16x16x32_bf16 v[52:55],v[28:31],v[88:91],v[52:55]
s_waitcnt lgkmcnt(2)
ds_read_b128 a[168:171],v1 offset:8704
v_cvt_pk_bf16_f32 v120,v120,v121
v_mfma_f32_16x16x32_bf16 v[64:67],a[128:131],v[128:131],v[32:35]
v_exp_f32 v122,v122
v_mfma_f32_16x16x32_bf16 v[64:67],a[132:135],v[136:139],v[64:67]
ds_read_b128 a[172:175],v1 offset:8960
v_mfma_f32_16x16x32_bf16 v[64:67],a[136:139],v[144:147],v[64:67]
v_exp_f32 v123,v123
v_mfma_f32_16x16x32_bf16 v[64:67],a[140:143],v[152:155],v[64:67]
buffer_load_dwordx4 v27,s[48:51],s83 offen offset:3072 lds
v_mfma_f32_16x16x32_bf16 v[68:71],a[156:159],v[152:155],v[32:35]
ds_read_b128 a[188:191],v1 offset:13056
v_cvt_pk_bf16_f32 v121,v122,v123
v_mfma_f32_16x16x32_bf16 v[68:71],a[152:155],v[144:147],v[68:71]
v_exp_f32 v124,v124
v_mfma_f32_16x16x32_bf16 v[68:71],a[148:151],v[136:139],v[68:71]
ds_read_b128 a[184:187],v1 offset:12800
v_mfma_f32_16x16x32_bf16 v[68:71],a[144:147],v[128:131],v[68:71]
s_waitcnt lgkmcnt(3)
v_exp_f32 v125,v125
v_mfma_f32_16x16x32_bf16 v[72:75],a[160:163],v[128:131],v[32:35]
ds_read_b128 a[180:183],v1 offset:12544
v_cvt_pk_bf16_f32 v122,v124,v125
v_mfma_f32_16x16x32_bf16 v[72:75],a[164:167],v[136:139],v[72:75]
v_exp_f32 v126,v126
v_mfma_f32_16x16x32_bf16 v[72:75],a[168:171],v[144:147],v[72:75]
s_waitcnt lgkmcnt(2)
ds_read_b128 a[176:179],v1 offset:12288
v_add_u32 v1,s78,v1
v_mfma_f32_16x16x32_bf16 v[72:75],a[172:175],v[152:155],v[72:75]
s_add_u32 m0,s86,45056
v_exp_f32 v127,v127
v_mfma_f32_16x16x32_bf16 v[76:79],a[188:191],v[152:155],v[32:35]
s_waitcnt lgkmcnt(0)
v_cvt_pk_bf16_f32 v123,v126,v127
v_lshrrev_b32 v1,6,v6
v_mfma_f32_16x16x32_bf16 v[76:79],a[184:187],v[144:147],v[76:79]
buffer_load_dwordx4 v7,s[48:51],s83 offen lds
v_mfma_f32_16x16x32_bf16 v[76:79],a[180:183],v[136:139],v[76:79]
s_add_u32 s83,s64,2
v_exp_f32 v64,v64
v_mfma_f32_16x16x32_bf16 v[76:79],a[176:179],v[128:131],v[76:79]
s_and_b32 s83,s83,3
v_exp_f32 v65,v65
v_mfma_f32_16x16x32_bf16 v[196:199],a[192:195],v[96:99],v[196:199]
s_lshl_b32 s83,s83,1
v_exp_f32 v66,v66
v_mfma_f32_16x16x32_bf16 v[196:199],a[196:199],v[104:107],v[196:199]
s_mul_i32 s83,s83,163840
v_exp_f32 v67,v67
v_mfma_f32_16x16x32_bf16 v[204:207],a[200:203],v[96:99],v[204:207]
s_add_u32 s83,s83,s85
v_exp_f32 v68,v68
v_mfma_f32_16x16x32_bf16 v[204:207],a[204:207],v[104:107],v[204:207]
v_exp_f32 v69,v69
v_mfma_f32_16x16x32_bf16 v[212:215],a[208:211],v[96:99],v[212:215]
v_exp_f32 v70,v70
v_mfma_f32_16x16x32_bf16 v[212:215],a[212:215],v[104:107],v[212:215]
s_add_u32 m0,s86,46080
v_exp_f32 v71,v71
v_mfma_f32_16x16x32_bf16 v[220:223],a[216:219],v[96:99],v[220:223]
v_cvt_pk_bf16_f32 v64,v64,v65
v_cvt_pk_bf16_f32 v65,v66,v67
v_mfma_f32_16x16x32_bf16 v[220:223],a[220:223],v[104:107],v[220:223]
buffer_load_dwordx4 v24,s[48:51],s84 offen lds
v_mfma_f32_16x16x32_bf16 v[228:231],a[224:227],v[96:99],v[228:231]
v_cvt_pk_bf16_f32 v66,v68,v69
v_cvt_pk_bf16_f32 v67,v70,v71
v_mfma_f32_16x16x32_bf16 v[228:231],a[228:231],v[104:107],v[228:231]
v_exp_f32 v72,v72
v_mfma_f32_16x16x32_bf16 v[236:239],a[232:235],v[96:99],v[236:239]
v_exp_f32 v73,v73
v_mfma_f32_16x16x32_bf16 v[236:239],a[236:239],v[104:107],v[236:239]
v_exp_f32 v74,v74
v_mfma_f32_16x16x32_bf16 v[244:247],a[240:243],v[96:99],v[244:247]
v_exp_f32 v75,v75
v_mfma_f32_16x16x32_bf16 v[244:247],a[244:247],v[104:107],v[244:247]
v_exp_f32 v76,v76
v_mfma_f32_16x16x32_bf16 v[252:255],a[248:251],v[96:99],v[252:255]
v_exp_f32 v77,v77
v_mfma_f32_16x16x32_bf16 v[252:255],a[252:255],v[104:107],v[252:255]
v_exp_f32 v78,v78
v_mfma_f32_16x16x32_bf16 v[56:59],v[28:31],v[96:99],v[56:59]
v_exp_f32 v79,v79
v_mfma_f32_16x16x32_bf16 v[56:59],v[28:31],v[104:107],v[56:59]
buffer_load_dwordx4 v25,s[48:51],s84 offen offset:1024 lds
v_mfma_f32_16x16x32_bf16 v[80:83],a[128:131],v[160:163],v[36:39]
v_cvt_pk_bf16_f32 v72,v72,v73
v_cvt_pk_bf16_f32 v73,v74,v75
v_mfma_f32_16x16x32_bf16 v[80:83],a[132:135],v[168:171],v[80:83]
v_cvt_pk_bf16_f32 v74,v76,v77
v_cvt_pk_bf16_f32 v75,v78,v79
v_mfma_f32_16x16x32_bf16 v[80:83],a[136:139],v[176:179],v[80:83]
v_mul_u32_u24 v1,320,v1
v_mfma_f32_16x16x32_bf16 v[80:83],a[140:143],v[184:187],v[80:83]
v_mfma_f32_16x16x32_bf16 v[84:87],a[156:159],v[184:187],v[36:39]
v_mfma_f32_16x16x32_bf16 v[84:87],a[152:155],v[176:179],v[84:87]
v_mfma_f32_16x16x32_bf16 v[84:87],a[148:151],v[168:171],v[84:87]
v_mfma_f32_16x16x32_bf16 v[84:87],a[144:147],v[160:163],v[84:87]
v_mfma_f32_16x16x32_bf16 v[88:91],a[160:163],v[160:163],v[36:39]
v_mfma_f32_16x16x32_bf16 v[88:91],a[164:167],v[168:171],v[88:91]
buffer_load_dwordx4 v26,s[48:51],s84 offen offset:2048 lds
v_mfma_f32_16x16x32_bf16 v[88:91],a[168:171],v[176:179],v[88:91]
v_exp_f32 v80,v80
v_mfma_f32_16x16x32_bf16 v[88:91],a[172:175],v[184:187],v[88:91]
v_exp_f32 v81,v81
v_mfma_f32_16x16x32_bf16 v[92:95],a[188:191],v[184:187],v[36:39]
v_exp_f32 v82,v82
v_mfma_f32_16x16x32_bf16 v[92:95],a[184:187],v[176:179],v[92:95]
v_exp_f32 v83,v83
v_mfma_f32_16x16x32_bf16 v[92:95],a[180:183],v[168:171],v[92:95]
v_exp_f32 v84,v84
v_mfma_f32_16x16x32_bf16 v[92:95],a[176:179],v[160:163],v[92:95]
v_exp_f32 v85,v85
v_mfma_f32_16x16x32_bf16 a[32:35],a[192:195],v[112:115],a[32:35]
ds_read_b64_tr_b16 a[192:193],v2
ds_read_b64_tr_b16 a[194:195],v2 offset:4096
v_mfma_f32_16x16x32_bf16 a[32:35],a[196:199],v[120:123],a[32:35]
ds_read_b64_tr_b16 a[196:197],v2 offset:8192
v_cvt_pk_bf16_f32 v80,v80,v81
v_mfma_f32_16x16x32_bf16 a[36:39],a[200:203],v[112:115],a[36:39]
ds_read_b64_tr_b16 a[198:199],v2 offset:12288
v_cvt_pk_bf16_f32 v81,v82,v83
v_mfma_f32_16x16x32_bf16 a[36:39],a[204:207],v[120:123],a[36:39]
buffer_load_dwordx4 v27,s[48:51],s84 offen offset:3072 lds
v_mfma_f32_16x16x32_bf16 a[40:43],a[208:211],v[112:115],a[40:43]
ds_read_b64_tr_b16 a[200:201],v3
ds_read_b64_tr_b16 a[202:203],v3 offset:4096
v_mfma_f32_16x16x32_bf16 a[40:43],a[212:215],v[120:123],a[40:43]
ds_read_b64_tr_b16 a[204:205],v3 offset:8192
v_cvt_pk_bf16_f32 v82,v84,v85
v_mfma_f32_16x16x32_bf16 a[44:47],a[216:219],v[112:115],a[44:47]
v_exp_f32 v86,v86
v_mfma_f32_16x16x32_bf16 a[44:47],a[220:223],v[120:123],a[44:47]
ds_read_b64_tr_b16 a[206:207],v3 offset:12288
ds_read_b64_tr_b16 a[208:209],v2 offset:256
v_mfma_f32_16x16x32_bf16 a[48:51],a[224:227],v[112:115],a[48:51]
v_exp_f32 v87,v87
v_mfma_f32_16x16x32_bf16 a[48:51],a[228:231],v[120:123],a[48:51]
ds_read_b64_tr_b16 a[210:211],v2 offset:4352
ds_read_b64_tr_b16 a[212:213],v2 offset:8448
v_mfma_f32_16x16x32_bf16 a[52:55],a[232:235],v[112:115],a[52:55]
ds_read_b64_tr_b16 a[214:215],v2 offset:12544
v_cvt_pk_bf16_f32 v83,v86,v87
v_mfma_f32_16x16x32_bf16 a[52:55],a[236:239],v[120:123],a[52:55]
s_add_u32 m0,s86,50176
v_exp_f32 v88,v88
v_mfma_f32_16x16x32_bf16 a[56:59],a[240:243],v[112:115],a[56:59]
ds_read_b64_tr_b16 a[216:217],v3 offset:256
ds_read_b64_tr_b16 a[218:219],v3 offset:4352
v_mfma_f32_16x16x32_bf16 a[56:59],a[244:247],v[120:123],a[56:59]
buffer_load_dwordx4 v7,s[48:51],s84 offen lds
v_mfma_f32_16x16x32_bf16 a[60:63],a[248:251],v[112:115],a[60:63]
s_add_u32 s84,s83,20480
ds_read_b64_tr_b16 a[220:221],v3 offset:8448
ds_read_b64_tr_b16 a[222:223],v3 offset:12544
v_mfma_f32_16x16x32_bf16 a[60:63],a[252:255],v[120:123],a[60:63]
v_exp_f32 v89,v89
v_mfma_f32_16x16x32_bf16 v[60:63],v[28:31],v[112:115],v[60:63]
ds_read_b64_tr_b16 a[224:225],v2 offset:512
ds_read_b64_tr_b16 a[226:227],v2 offset:4608
v_mfma_f32_16x16x32_bf16 v[60:63],v[28:31],v[120:123],v[60:63]
ds_read_b64_tr_b16 a[228:229],v2 offset:8704
v_cvt_pk_bf16_f32 v88,v88,v89
v_mfma_f32_16x16x32_bf16 v[96:99],a[128:131],v[192:195],v[40:43]
v_exp_f32 v90,v90
v_mfma_f32_16x16x32_bf16 v[96:99],a[132:135],v[200:203],v[96:99]
ds_read_b64_tr_b16 a[230:231],v2 offset:12800
ds_read_b64_tr_b16 a[232:233],v3 offset:512
v_mfma_f32_16x16x32_bf16 v[96:99],a[136:139],v[208:211],v[96:99]
v_exp_f32 v91,v91
v_mfma_f32_16x16x32_bf16 v[96:99],a[140:143],v[216:219],v[96:99]
ds_read_b64_tr_b16 a[234:235],v3 offset:4608
ds_read_b64_tr_b16 a[236:237],v3 offset:8704
v_mfma_f32_16x16x32_bf16 v[100:103],a[156:159],v[216:219],v[40:43]
ds_read_b64_tr_b16 a[238:239],v3 offset:12800
v_cvt_pk_bf16_f32 v89,v90,v91
v_mfma_f32_16x16x32_bf16 v[100:103],a[152:155],v[208:211],v[100:103]
v_exp_f32 v92,v92
v_mfma_f32_16x16x32_bf16 v[100:103],a[148:151],v[200:203],v[100:103]
ds_read_b64_tr_b16 a[240:241],v2 offset:768
ds_read_b64_tr_b16 a[242:243],v2 offset:4864
v_mfma_f32_16x16x32_bf16 v[100:103],a[144:147],v[192:195],v[100:103]
v_exp_f32 v93,v93
v_mfma_f32_16x16x32_bf16 v[104:107],a[160:163],v[192:195],v[40:43]
ds_read_b64_tr_b16 a[244:245],v2 offset:8960
ds_read_b64_tr_b16 a[246:247],v2 offset:13056
v_mfma_f32_16x16x32_bf16 v[104:107],a[164:167],v[200:203],v[104:107]
ds_read_b64_tr_b16 a[248:249],v3 offset:768
v_cvt_pk_bf16_f32 v90,v92,v93
v_mfma_f32_16x16x32_bf16 v[104:107],a[168:171],v[208:211],v[104:107]
ds_read_b64_tr_b16 a[250:251],v3 offset:4864
v_bfe_u32 v2,v6,4,2
v_mfma_f32_16x16x32_bf16 v[104:107],a[172:175],v[216:219],v[104:107]
ds_read_b64_tr_b16 a[252:253],v3 offset:8960
v_lshl_add_u32 v1,v2,3,v1
v_mfma_f32_16x16x32_bf16 v[108:111],a[188:191],v[216:219],v[40:43]
ds_read_b64_tr_b16 a[254:255],v3 offset:13056
v_add_u32 v1,122880,v1
v_mfma_f32_16x16x32_bf16 v[108:111],a[184:187],v[208:211],v[108:111]
v_exp_f32 v94,v94
v_mfma_f32_16x16x32_bf16 v[108:111],a[180:183],v[200:203],v[108:111]
v_exp_f32 v95,v95
v_mfma_f32_16x16x32_bf16 v[108:111],a[176:179],v[192:195],v[108:111]
s_waitcnt lgkmcnt(6)
v_cvt_pk_bf16_f32 v91,v94,v95
v_bfe_u32 v2,v6,8,1
v_mfma_f32_16x16x32_bf16 a[0:3],a[192:195],v[64:67],a[0:3]
v_exp_f32 v96,v96
v_mfma_f32_16x16x32_bf16 a[0:3],a[196:199],v[72:75],a[0:3]
v_exp_f32 v97,v97
v_mfma_f32_16x16x32_bf16 a[4:7],a[200:203],v[64:67],a[4:7]
v_exp_f32 v98,v98
v_mfma_f32_16x16x32_bf16 a[4:7],a[204:207],v[72:75],a[4:7]
v_exp_f32 v99,v99
v_mfma_f32_16x16x32_bf16 a[8:11],a[208:211],v[64:67],a[8:11]
)A"
R"A(v_exp_f32 v100,v100
v_mfma_f32_16x16x32_bf16 a[8:11],a[212:215],v[72:75],a[8:11]
v_exp_f32 v101,v101
v_mfma_f32_16x16x32_bf16 a[12:15],a[216:219],v[64:67],a[12:15]
v_exp_f32 v102,v102
v_mfma_f32_16x16x32_bf16 a[12:15],a[220:223],v[72:75],a[12:15]
v_exp_f32 v103,v103
v_mfma_f32_16x16x32_bf16 a[16:19],a[224:227],v[64:67],a[16:19]
v_cvt_pk_bf16_f32 v96,v96,v97
v_cvt_pk_bf16_f32 v97,v98,v99
v_mfma_f32_16x16x32_bf16 a[16:19],a[228:231],v[72:75],a[16:19]
v_cvt_pk_bf16_f32 v98,v100,v101
v_cvt_pk_bf16_f32 v99,v102,v103
v_mfma_f32_16x16x32_bf16 a[20:23],a[232:235],v[64:67],a[20:23]
v_exp_f32 v104,v104
v_mfma_f32_16x16x32_bf16 a[20:23],a[236:239],v[72:75],a[20:23]
v_exp_f32 v105,v105
v_mfma_f32_16x16x32_bf16 a[24:27],a[240:243],v[64:67],a[24:27]
s_waitcnt lgkmcnt(0)
v_exp_f32 v106,v106
v_mfma_f32_16x16x32_bf16 a[24:27],a[244:247],v[72:75],a[24:27]
v_exp_f32 v107,v107
v_mfma_f32_16x16x32_bf16 a[28:31],a[248:251],v[64:67],a[28:31]
v_exp_f32 v108,v108
v_mfma_f32_16x16x32_bf16 a[28:31],a[252:255],v[72:75],a[28:31]
v_exp_f32 v109,v109
v_mfma_f32_16x16x32_bf16 v[48:51],v[28:31],v[64:67],v[48:51]
v_exp_f32 v110,v110
v_mfma_f32_16x16x32_bf16 v[48:51],v[28:31],v[72:75],v[48:51]
v_exp_f32 v111,v111
s_waitcnt vmcnt(11)
s_barrier
v_mfma_f32_16x16x32_bf16 v[112:115],a[128:131],v[224:227],v[44:47]
buffer_store_dwordx2 v[8:9],v10,s[44:47],s18 offen offset:256 sc1
v_mfma_f32_16x16x32_bf16 v[112:115],a[132:135],v[232:235],v[112:115]
v_cvt_pk_bf16_f32 v104,v104,v105
v_cvt_pk_bf16_f32 v105,v106,v107
v_mfma_f32_16x16x32_bf16 v[112:115],a[136:139],v[240:243],v[112:115]
v_cvt_pk_bf16_f32 v106,v108,v109
v_cvt_pk_bf16_f32 v107,v110,v111
v_mfma_f32_16x16x32_bf16 v[112:115],a[140:143],v[248:251],v[112:115]
s_add_u32 m0,s86,0
v_cmp_nlt_f32 s[54:55],v48,s73
v_max_f32 v49,0x00800000,v48
v_mfma_f32_16x16x32_bf16 v[116:119],a[156:159],v[248:251],v[44:47]
v_rcp_f32 v49,v49
v_mfma_f32_16x16x32_bf16 v[116:119],a[152:155],v[240:243],v[116:119]
v_lshl_add_u32 v1,v2,5,v1
v_lshlrev_b32 v2,6,v2
v_mfma_f32_16x16x32_bf16 v[116:119],a[148:151],v[232:235],v[116:119]
buffer_load_dwordx4 v24,s[48:51],s83 offen lds
v_mfma_f32_16x16x32_bf16 v[116:119],a[144:147],v[224:227],v[116:119]
v_exp_f32 v112,v112
v_mfma_f32_16x16x32_bf16 v[120:123],a[160:163],v[224:227],v[44:47]
v_exp_f32 v113,v113
v_mfma_f32_16x16x32_bf16 v[120:123],a[164:167],v[232:235],v[120:123]
v_exp_f32 v114,v114
v_mfma_f32_16x16x32_bf16 v[120:123],a[168:171],v[240:243],v[120:123]
v_exp_f32 v115,v115
v_mfma_f32_16x16x32_bf16 v[120:123],a[172:175],v[248:251],v[120:123]
v_exp_f32 v116,v116
v_mfma_f32_16x16x32_bf16 v[124:127],a[188:191],v[248:251],v[44:47]
buffer_load_dwordx4 v25,s[48:51],s83 offen offset:1024 lds
v_mfma_f32_16x16x32_bf16 v[124:127],a[184:187],v[240:243],v[124:127]
v_exp_f32 v117,v117
v_mfma_f32_16x16x32_bf16 v[124:127],a[180:183],v[232:235],v[124:127]
v_exp_f32 v118,v118
v_mfma_f32_16x16x32_bf16 v[124:127],a[176:179],v[224:227],v[124:127]
v_exp_f32 v119,v119
v_mfma_f32_16x16x32_bf16 v[52:55],v[28:31],v[80:83],v[52:55]
v_cvt_pk_bf16_f32 v112,v112,v113
v_cvt_pk_bf16_f32 v113,v114,v115
v_mfma_f32_16x16x32_bf16 v[52:55],v[28:31],v[88:91],v[52:55]
v_cvt_pk_bf16_f32 v114,v116,v117
v_cvt_pk_bf16_f32 v115,v118,v119
v_mfma_f32_16x16x32_bf16 v[132:135],a[192:195],v[80:83],v[132:135]
buffer_load_dwordx4 v26,s[48:51],s83 offen offset:2048 lds
v_mfma_f32_16x16x32_bf16 v[132:135],a[196:199],v[88:91],v[132:135]
v_exp_f32 v120,v120
v_mfma_f32_16x16x32_bf16 v[140:143],a[200:203],v[80:83],v[140:143]
v_exp_f32 v121,v121
v_mfma_f32_16x16x32_bf16 v[140:143],a[204:207],v[88:91],v[140:143]
v_exp_f32 v122,v122
v_mfma_f32_16x16x32_bf16 v[148:151],a[208:211],v[80:83],v[148:151]
v_exp_f32 v123,v123
v_mfma_f32_16x16x32_bf16 v[148:151],a[212:215],v[88:91],v[148:151]
v_exp_f32 v124,v124
v_mfma_f32_16x16x32_bf16 v[156:159],a[216:219],v[80:83],v[156:159]
buffer_load_dwordx4 v27,s[48:51],s83 offen offset:3072 lds
v_mfma_f32_16x16x32_bf16 v[156:159],a[220:223],v[88:91],v[156:159]
v_exp_f32 v125,v125
v_mfma_f32_16x16x32_bf16 v[164:167],a[224:227],v[80:83],v[164:167]
v_exp_f32 v126,v126
v_mfma_f32_16x16x32_bf16 v[164:167],a[228:231],v[88:91],v[164:167]
s_add_u32 m0,s86,4096
v_exp_f32 v127,v127
v_mfma_f32_16x16x32_bf16 v[172:175],a[232:235],v[80:83],v[172:175]
v_cvt_pk_bf16_f32 v120,v120,v121
v_cvt_pk_bf16_f32 v121,v122,v123
v_mfma_f32_16x16x32_bf16 v[172:175],a[236:239],v[88:91],v[172:175]
v_cvt_pk_bf16_f32 v122,v124,v125
v_cvt_pk_bf16_f32 v123,v126,v127
v_mfma_f32_16x16x32_bf16 v[180:183],a[240:243],v[80:83],v[180:183]
buffer_load_dwordx4 v7,s[48:51],s83 offen lds
v_mfma_f32_16x16x32_bf16 v[180:183],a[244:247],v[88:91],v[180:183]
v_cmp_nlt_f32 s[56:57],v52,s73
v_sub_u32 v2,v1,v2
v_mfma_f32_16x16x32_bf16 v[188:191],a[248:251],v[80:83],v[188:191]
s_or_b64 s[54:55],s[54:55],s[56:57]
v_accvgpr_read_b32 v8,a0
v_accvgpr_read_b32 v9,a1
v_mfma_f32_16x16x32_bf16 v[188:191],a[252:255],v[88:91],v[188:191]
s_add_u32 m0,s86,5120
v_accvgpr_read_b32 v10,a2
v_accvgpr_read_b32 v11,a3
v_mfma_f32_16x16x32_bf16 v[56:59],v[28:31],v[96:99],v[56:59]
v_accvgpr_read_b32 v12,a4
v_accvgpr_read_b32 v13,a5
v_mfma_f32_16x16x32_bf16 v[56:59],v[28:31],v[104:107],v[56:59]
v_accvgpr_read_b32 v14,a6
v_accvgpr_read_b32 v15,a7
v_mfma_f32_16x16x32_bf16 v[196:199],a[192:195],v[96:99],v[196:199]
buffer_load_dwordx4 v24,s[48:51],s84 offen lds
v_mfma_f32_16x16x32_bf16 v[196:199],a[196:199],v[104:107],v[196:199]
v_mul_f32 v8,v8,v49
v_mul_f32 v9,v9,v49
v_mfma_f32_16x16x32_bf16 v[204:207],a[200:203],v[96:99],v[204:207]
v_cmp_nlt_f32 s[56:57],v56,s73
v_mul_f32 v10,v10,v49
v_mfma_f32_16x16x32_bf16 v[204:207],a[204:207],v[104:107],v[204:207]
s_or_b64 s[54:55],s[54:55],s[56:57]
v_mul_f32 v11,v11,v49
v_mul_f32 v12,v12,v49
v_mfma_f32_16x16x32_bf16 v[212:215],a[208:211],v[96:99],v[212:215]
v_mul_f32 v13,v13,v49
v_mul_f32 v14,v14,v49
v_mfma_f32_16x16x32_bf16 v[212:215],a[212:215],v[104:107],v[212:215]
v_mul_f32 v15,v15,v49
v_cvt_pk_bf16_f32 v32,v8,v9
v_mfma_f32_16x16x32_bf16 v[220:223],a[216:219],v[96:99],v[220:223]
buffer_load_dwordx4 v25,s[48:51],s84 offen offset:1024 lds
v_mfma_f32_16x16x32_bf16 v[220:223],a[220:223],v[104:107],v[220:223]
v_cvt_pk_bf16_f32 v33,v10,v11
v_cvt_pk_bf16_f32 v34,v12,v13
v_mfma_f32_16x16x32_bf16 v[228:231],a[224:227],v[96:99],v[228:231]
v_cvt_pk_bf16_f32 v35,v14,v15
v_accvgpr_read_b32 v16,a8
v_mfma_f32_16x16x32_bf16 v[228:231],a[228:231],v[104:107],v[228:231]
v_accvgpr_read_b32 v17,a9
v_accvgpr_read_b32 v18,a10
v_mfma_f32_16x16x32_bf16 v[236:239],a[232:235],v[96:99],v[236:239]
v_accvgpr_read_b32 v19,a11
v_accvgpr_read_b32 v20,a12
v_mfma_f32_16x16x32_bf16 v[236:239],a[236:239],v[104:107],v[236:239]
v_accvgpr_read_b32 v21,a13
v_accvgpr_read_b32 v22,a14
v_mfma_f32_16x16x32_bf16 v[244:247],a[240:243],v[96:99],v[244:247]
buffer_load_dwordx4 v26,s[48:51],s84 offen offset:2048 lds
v_mfma_f32_16x16x32_bf16 v[244:247],a[244:247],v[104:107],v[244:247]
v_accvgpr_read_b32 v23,a15
v_mul_f32 v16,v16,v49
v_mfma_f32_16x16x32_bf16 v[252:255],a[248:251],v[96:99],v[252:255]
v_mul_f32 v17,v17,v49
v_mul_f32 v18,v18,v49
v_mfma_f32_16x16x32_bf16 v[252:255],a[252:255],v[104:107],v[252:255]
v_mul_f32 v19,v19,v49
v_mul_f32 v20,v20,v49
v_mfma_f32_16x16x32_bf16 v[60:63],v[28:31],v[112:115],v[60:63]
v_mul_f32 v21,v21,v49
v_mul_f32 v22,v22,v49
v_mfma_f32_16x16x32_bf16 v[60:63],v[28:31],v[120:123],v[60:63]
v_mul_f32 v23,v23,v49
v_cvt_pk_bf16_f32 v36,v16,v17
v_mfma_f32_16x16x32_bf16 a[32:35],a[192:195],v[112:115],a[32:35]
buffer_load_dwordx4 v27,s[48:51],s84 offen offset:3072 lds
v_mfma_f32_16x16x32_bf16 a[32:35],a[196:199],v[120:123],a[32:35]
v_cvt_pk_bf16_f32 v37,v18,v19
v_cvt_pk_bf16_f32 v38,v20,v21
v_mfma_f32_16x16x32_bf16 a[36:39],a[200:203],v[112:115],a[36:39]
v_cmp_nlt_f32 s[56:57],v60,s73
v_cvt_pk_bf16_f32 v39,v22,v23
v_mfma_f32_16x16x32_bf16 a[36:39],a[204:207],v[120:123],a[36:39]
s_add_u32 m0,s86,9216
v_accvgpr_read_b32 v8,a16
v_accvgpr_read_b32 v9,a17
v_mfma_f32_16x16x32_bf16 a[40:43],a[208:211],v[112:115],a[40:43]
s_or_b64 s[54:55],s[54:55],s[56:57]
v_accvgpr_read_b32 v10,a18
v_accvgpr_read_b32 v11,a19
v_mfma_f32_16x16x32_bf16 a[40:43],a[212:215],v[120:123],a[40:43]
v_accvgpr_read_b32 v12,a20
v_accvgpr_read_b32 v13,a21
v_mfma_f32_16x16x32_bf16 a[44:47],a[216:219],v[112:115],a[44:47]
buffer_load_dwordx4 v7,s[48:51],s84 offen lds
v_mfma_f32_16x16x32_bf16 a[44:47],a[220:223],v[120:123],a[44:47]
v_accvgpr_read_b32 v14,a22
v_accvgpr_read_b32 v15,a23
v_mfma_f32_16x16x32_bf16 a[48:51],a[224:227],v[112:115],a[48:51]
v_mul_f32 v8,v8,v49
v_mul_f32 v9,v9,v49
v_mfma_f32_16x16x32_bf16 a[48:51],a[228:231],v[120:123],a[48:51]
v_mul_f32 v10,v10,v49
v_mul_f32 v11,v11,v49
v_mfma_f32_16x16x32_bf16 a[52:55],a[232:235],v[112:115],a[52:55]
v_mul_f32 v12,v12,v49
v_mul_f32 v13,v13,v49
v_mfma_f32_16x16x32_bf16 a[52:55],a[236:239],v[120:123],a[52:55]
v_mul_f32 v14,v14,v49
v_mul_f32 v15,v15,v49
v_mfma_f32_16x16x32_bf16 a[56:59],a[240:243],v[112:115],a[56:59]
v_cvt_pk_bf16_f32 v40,v8,v9
v_cvt_pk_bf16_f32 v41,v10,v11
v_mfma_f32_16x16x32_bf16 a[56:59],a[244:247],v[120:123],a[56:59]
v_cvt_pk_bf16_f32 v42,v12,v13
v_cvt_pk_bf16_f32 v43,v14,v15
v_mfma_f32_16x16x32_bf16 a[60:63],a[248:251],v[112:115],a[60:63]
ds_read_b64_tr_b16 v[64:65],v1
ds_read_b64_tr_b16 v[66:67],v1 offset:5120
v_mfma_f32_16x16x32_bf16 a[60:63],a[252:255],v[120:123],a[60:63]
ds_read_b64_tr_b16 v[68:69],v1 offset:10240
v_max_f32 v53,0x00800000,v52
v_rcp_f32 v53,v53
v_max_f32 v57,0x00800000,v56
v_rcp_f32 v57,v57
v_max_f32 v61,0x00800000,v60
v_rcp_f32 v61,v61
ds_read_b64_tr_b16 v[70:71],v1 offset:15360
ds_read_b64_tr_b16 v[72:73],v1 offset:20480
ds_read_b64_tr_b16 v[74:75],v1 offset:25600
ds_read_b64_tr_b16 v[76:77],v1 offset:30720
ds_read_b64_tr_b16 v[78:79],v1 offset:35840
ds_read_b64_tr_b16 v[80:81],v2 offset:32
ds_read_b64_tr_b16 v[82:83],v2 offset:5152
ds_read_b64_tr_b16 v[84:85],v2 offset:10272
ds_read_b64_tr_b16 v[86:87],v2 offset:15392
ds_read_b64_tr_b16 v[88:89],v2 offset:20512
ds_read_b64_tr_b16 v[90:91],v2 offset:25632
ds_read_b64_tr_b16 v[92:93],v2 offset:30752
ds_read_b64_tr_b16 v[94:95],v2 offset:35872
s_cmp_lg_u64 s[54:55],0
s_cbranch_scc1 L_slow1_%=
L_ok1_%=:
s_cmp_ge_u32 s63,2
s_cbranch_scc1 L_GS_%=
L_gbF_%=:
s_waitcnt lgkmcnt(14)
v_mfma_f32_16x16x32_bf16 a[192:195],v[64:67],a[68:71],0
buffer_store_dwordx4 v[32:35],v6,s[40:43],s79 offen sc1
s_waitcnt lgkmcnt(12)
v_mfma_f32_16x16x32_bf16 a[192:195],v[68:71],a[76:79],a[192:195]
s_lshl_b32 s91,s63,8
v_bfe_u32 v3,v6,4,2
v_min_u32 v3,2,v3
s_waitcnt lgkmcnt(10)
v_mfma_f32_16x16x32_bf16 a[192:195],v[72:75],a[84:87],a[192:195]
s_add_u32 s87,s64,1
v_add3_u32 v3,v3,s64,1
v_and_b32 v3,3,v3
s_waitcnt lgkmcnt(8)
v_mfma_f32_16x16x32_bf16 a[192:195],v[76:79],a[92:95],a[192:195]
s_and_b32 s87,s87,3
ds_read_b64_tr_b16 v[96:97],v1 offset:64
v_lshlrev_b32 v3,6,v3
v_mfma_f32_16x16x32_bf16 a[196:199],v[76:79],a[124:127],0
s_lshl_b32 s87,s87,1
ds_read_b64_tr_b16 v[98:99],v1 offset:5184
ds_read_b64_tr_b16 v[100:101],v1 offset:10304
v_mfma_f32_16x16x32_bf16 a[196:199],v[72:75],a[116:119],a[196:199]
s_lshl_b32 s87,s87,16
ds_read_b64_tr_b16 v[102:103],v1 offset:15424
v_lshlrev_b32 v29,10,v6
v_mfma_f32_16x16x32_bf16 a[196:199],v[68:71],a[108:111],a[196:199]
s_add_u32 s88,s87,4096
ds_read_b64_tr_b16 v[104:105],v1 offset:20544
v_accvgpr_read_b32 v8,a24
v_mfma_f32_16x16x32_bf16 a[196:199],v[64:67],a[100:103],a[196:199]
s_add_u32 s89,s87,8192
ds_read_b64_tr_b16 v[106:107],v1 offset:25664
v_accvgpr_read_b32 v9,a25
v_mfma_f32_16x16x32_bf16 a[200:203],v[64:67],a[64:67],0
s_add_u32 s90,s87,12288
ds_read_b64_tr_b16 v[108:109],v1 offset:30784
ds_read_b64_tr_b16 v[110:111],v1 offset:35904
v_mfma_f32_16x16x32_bf16 a[200:203],v[68:71],a[72:75],a[200:203]
s_add_u32 m0,s86,122880
ds_read_b64_tr_b16 v[112:113],v2 offset:96
v_accvgpr_read_b32 v10,a26
v_mfma_f32_16x16x32_bf16 a[200:203],v[72:75],a[80:83],a[200:203]
s_add_u32 s83,s64,3
ds_read_b64_tr_b16 v[114:115],v2 offset:5216
v_accvgpr_read_b32 v11,a27
v_mfma_f32_16x16x32_bf16 a[200:203],v[76:79],a[88:91],a[200:203]
s_and_b32 s83,s83,3
ds_read_b64_tr_b16 v[116:117],v2 offset:10336
v_accvgpr_read_b32 v12,a28
v_mfma_f32_16x16x32_bf16 a[204:207],v[76:79],a[120:123],0
)A"
R"A(s_lshl_b32 s83,s83,1
ds_read_b64_tr_b16 v[118:119],v2 offset:15456
ds_read_b64_tr_b16 v[120:121],v2 offset:20576
v_mfma_f32_16x16x32_bf16 a[204:207],v[72:75],a[112:115],a[204:207]
s_mul_i32 s83,s83,163840
ds_read_b64_tr_b16 v[122:123],v2 offset:25696
v_accvgpr_read_b32 v13,a29
v_mfma_f32_16x16x32_bf16 a[204:207],v[68:71],a[104:107],a[204:207]
s_add_u32 s83,s83,s85
ds_read_b64_tr_b16 v[124:125],v2 offset:30816
v_accvgpr_read_b32 v14,a30
v_mfma_f32_16x16x32_bf16 a[204:207],v[64:67],a[96:99],a[204:207]
buffer_store_dwordx4 v[36:39],v6,s[40:43],s80 offen sc1
s_waitcnt lgkmcnt(9)
v_mfma_f32_16x16x32_bf16 a[208:211],v[80:83],a[68:71],0
s_add_u32 s84,s83,20480
ds_read_b64_tr_b16 v[126:127],v2 offset:35936
ds_read_b64_tr_b16 v[64:65],v1 offset:128
v_mfma_f32_16x16x32_bf16 a[208:211],v[84:87],a[76:79],a[208:211]
s_lshl_b32 s66,s61,2
ds_read_b64_tr_b16 v[66:67],v1 offset:5248
v_accvgpr_read_b32 v15,a31
v_mfma_f32_16x16x32_bf16 a[208:211],v[88:91],a[84:87],a[208:211]
s_add_u32 s66,s66,s64
ds_read_b64_tr_b16 v[68:69],v1 offset:10368
v_mul_f32 v8,v8,v49
v_mfma_f32_16x16x32_bf16 a[208:211],v[92:95],a[92:95],a[208:211]
s_mul_i32 s66,s66,262144
ds_read_b64_tr_b16 v[70:71],v1 offset:15488
v_mul_f32 v9,v9,v49
v_mfma_f32_16x16x32_bf16 a[212:215],v[92:95],a[124:127],0
s_waitcnt vmcnt(21)
buffer_load_dwordx2 v[4:5],v3,s[44:47],s91 offen offset:256 sc1
v_mfma_f32_16x16x32_bf16 a[212:215],v[88:91],a[116:119],a[212:215]
s_add_u32 s66,s66,134217728
ds_read_b64_tr_b16 v[72:73],v1 offset:20608
ds_read_b64_tr_b16 v[74:75],v1 offset:25728
v_mfma_f32_16x16x32_bf16 a[212:215],v[84:87],a[108:111],a[212:215]
s_mov_b32 s38,262144
ds_read_b64_tr_b16 v[76:77],v1 offset:30848
v_mul_f32 v10,v10,v49
v_mfma_f32_16x16x32_bf16 a[212:215],v[80:83],a[100:103],a[212:215]
s_mov_b32 s39,0x20000
ds_read_b64_tr_b16 v[78:79],v1 offset:35968
v_mul_f32 v11,v11,v49
v_mfma_f32_16x16x32_bf16 a[216:219],v[80:83],a[64:67],0
s_add_u32 s74,s63,1
v_mul_f32 v12,v12,v49
v_mul_f32 v13,v13,v49
v_mfma_f32_16x16x32_bf16 a[216:219],v[84:87],a[72:75],a[216:219]
s_and_b32 s74,s74,3
v_mul_f32 v14,v14,v49
v_mul_f32 v15,v15,v49
v_mfma_f32_16x16x32_bf16 a[216:219],v[88:91],a[80:83],a[216:219]
s_lshl_b32 s74,s74,2
v_cvt_pk_bf16_f32 v44,v8,v9
s_nop 0
L_pd13_%=:
s_cbranch_execz L_trt1_%=
v_cvt_pk_bf16_f32 v45,v10,v11
v_mfma_f32_16x16x32_bf16 a[216:219],v[92:95],a[88:91],a[216:219]
s_add_u32 s74,s74,s60
v_cvt_pk_bf16_f32 v46,v12,v13
v_cvt_pk_bf16_f32 v47,v14,v15
v_mfma_f32_16x16x32_bf16 a[220:223],v[92:95],a[120:123],0
s_mul_i32 s74,s74,16384
ds_read_b64_tr_b16 v[92:93],v2 offset:30880
ds_read_b64_tr_b16 v[94:95],v2 offset:36000
v_mfma_f32_16x16x32_bf16 a[220:223],v[88:91],a[112:115],a[220:223]
s_add_u32 s74,s74,8192
ds_read_b64_tr_b16 v[88:89],v2 offset:20640
v_mul_f32 v132,v132,v53
v_mfma_f32_16x16x32_bf16 a[220:223],v[84:87],a[104:107],a[220:223]
buffer_store_dwordx4 v[40:43],v6,s[40:43],s81 offen sc1
v_mfma_f32_16x16x32_bf16 a[220:223],v[80:83],a[96:99],a[220:223]
s_add_u32 s75,s63,2
ds_read_b64_tr_b16 v[80:81],v2 offset:160
ds_read_b64_tr_b16 v[82:83],v2 offset:5280
v_mfma_f32_16x16x32_bf16 a[224:227],v[96:99],a[68:71],0
s_and_b32 s75,s75,3
ds_read_b64_tr_b16 v[84:85],v2 offset:10400
v_mul_f32 v133,v133,v53
v_mfma_f32_16x16x32_bf16 a[224:227],v[100:103],a[76:79],a[224:227]
s_lshl_b32 s75,s75,2
ds_read_b64_tr_b16 v[86:87],v2 offset:15520
v_mul_f32 v134,v134,v53
v_mfma_f32_16x16x32_bf16 a[224:227],v[104:107],a[84:87],a[224:227]
s_add_u32 s75,s75,s60
ds_read_b64_tr_b16 v[90:91],v2 offset:25760
v_mul_f32 v135,v135,v53
s_waitcnt lgkmcnt(8)
v_mfma_f32_16x16x32_bf16 a[224:227],v[108:111],a[92:95],a[224:227]
s_mul_i32 s75,s75,16384
v_mul_f32 v140,v140,v53
v_mul_f32 v141,v141,v53
v_mfma_f32_16x16x32_bf16 a[228:231],v[108:111],a[124:127],0
s_add_u32 s75,s75,4096
v_mul_f32 v142,v142,v53
v_mul_f32 v143,v143,v53
v_mfma_f32_16x16x32_bf16 a[228:231],v[104:107],a[116:119],a[228:231]
s_add_u32 s76,s63,3
v_cvt_pk_bf16_f32 v132,v132,v133
v_cvt_pk_bf16_f32 v133,v134,v135
v_mfma_f32_16x16x32_bf16 a[228:231],v[100:103],a[108:111],a[228:231]
s_and_b32 s76,s76,3
v_cvt_pk_bf16_f32 v134,v140,v141
v_cvt_pk_bf16_f32 v135,v142,v143
v_mfma_f32_16x16x32_bf16 a[228:231],v[96:99],a[100:103],a[228:231]
s_lshl_b32 s76,s76,2
v_mul_f32 v148,v148,v53
v_mul_f32 v149,v149,v53
v_mfma_f32_16x16x32_bf16 a[232:235],v[96:99],a[64:67],0
s_add_u32 s76,s76,s60
v_mul_f32 v150,v150,v53
v_mul_f32 v151,v151,v53
v_mfma_f32_16x16x32_bf16 a[232:235],v[100:103],a[72:75],a[232:235]
s_mul_i32 s76,s76,16384
v_mul_f32 v156,v156,v53
v_mul_f32 v157,v157,v53
v_mfma_f32_16x16x32_bf16 a[232:235],v[104:107],a[80:83],a[232:235]
s_add_u32 s76,s76,0
v_mul_f32 v158,v158,v53
v_mul_f32 v159,v159,v53
v_mfma_f32_16x16x32_bf16 a[232:235],v[108:111],a[88:91],a[232:235]
s_lshl_b32 s67,s63,10
v_cvt_pk_bf16_f32 v148,v148,v149
v_cvt_pk_bf16_f32 v149,v150,v151
v_mfma_f32_16x16x32_bf16 a[236:239],v[108:111],a[120:123],0
s_mov_b32 s26,307200
ds_read_b64_tr_b16 v[108:109],v1 offset:30912
ds_read_b64_tr_b16 v[110:111],v1 offset:36032
v_mfma_f32_16x16x32_bf16 a[236:239],v[104:107],a[112:115],a[236:239]
buffer_store_dwordx4 v[44:47],v6,s[40:43],s82 offen sc1
v_mfma_f32_16x16x32_bf16 a[236:239],v[100:103],a[104:107],a[236:239]
s_mov_b32 s27,0x20000
ds_read_b64_tr_b16 v[100:101],v1 offset:10432
ds_read_b64_tr_b16 v[102:103],v1 offset:15552
v_mfma_f32_16x16x32_bf16 a[236:239],v[96:99],a[96:99],a[236:239]
s_lshl_b32 s77,s63,2
ds_read_b64_tr_b16 v[96:97],v1 offset:192
v_cvt_pk_bf16_f32 v150,v156,v157
v_mfma_f32_16x16x32_bf16 a[240:243],v[112:115],a[68:71],0
s_add_u32 s77,s77,s60
ds_read_b64_tr_b16 v[98:99],v1 offset:5312
v_cvt_pk_bf16_f32 v151,v158,v159
v_mfma_f32_16x16x32_bf16 a[240:243],v[116:119],a[76:79],a[240:243]
s_mul_i32 s77,s77,16384
ds_read_b64_tr_b16 v[104:105],v1 offset:20672
v_mul_f32 v164,v164,v53
v_mfma_f32_16x16x32_bf16 a[240:243],v[120:123],a[84:87],a[240:243]
s_add_u32 s65,s77,0
ds_read_b64_tr_b16 v[106:107],v1 offset:25792
v_mul_f32 v165,v165,v53
v_mfma_f32_16x16x32_bf16 a[240:243],v[124:127],a[92:95],a[240:243]
s_add_u32 s68,s77,12288
v_mul_f32 v166,v166,v53
v_mul_f32 v167,v167,v53
v_mfma_f32_16x16x32_bf16 a[244:247],v[124:127],a[124:127],0
v_mul_f32 v172,v172,v53
v_mul_f32 v173,v173,v53
v_mfma_f32_16x16x32_bf16 a[244:247],v[120:123],a[116:119],a[244:247]
v_mul_f32 v174,v174,v53
v_mul_f32 v175,v175,v53
v_mfma_f32_16x16x32_bf16 a[244:247],v[116:119],a[108:111],a[244:247]
v_cvt_pk_bf16_f32 v164,v164,v165
v_cvt_pk_bf16_f32 v165,v166,v167
v_mfma_f32_16x16x32_bf16 a[244:247],v[112:115],a[100:103],a[244:247]
v_cvt_pk_bf16_f32 v166,v172,v173
v_cvt_pk_bf16_f32 v167,v174,v175
v_mfma_f32_16x16x32_bf16 a[248:251],v[112:115],a[64:67],0
v_mul_f32 v180,v180,v53
v_mul_f32 v181,v181,v53
v_mfma_f32_16x16x32_bf16 a[248:251],v[116:119],a[72:75],a[248:251]
v_mul_f32 v182,v182,v53
v_mul_f32 v183,v183,v53
v_mfma_f32_16x16x32_bf16 a[248:251],v[120:123],a[80:83],a[248:251]
v_mul_f32 v188,v188,v53
v_mul_f32 v189,v189,v53
v_mfma_f32_16x16x32_bf16 a[248:251],v[124:127],a[88:91],a[248:251]
v_mul_f32 v190,v190,v53
v_mul_f32 v191,v191,v53
v_mfma_f32_16x16x32_bf16 a[252:255],v[124:127],a[120:123],0
buffer_load_dword v30,v29,s[8:11],s7 offen
v_mfma_f32_16x16x32_bf16 a[252:255],v[120:123],a[112:115],a[252:255]
ds_read_b64_tr_b16 v[120:121],v2 offset:20704
ds_read_b64_tr_b16 v[122:123],v2 offset:25824
v_mfma_f32_16x16x32_bf16 a[252:255],v[116:119],a[104:107],a[252:255]
buffer_store_dwordx4 v[132:135],v6,s[40:43],s79 offen offset:1024 sc1
v_mfma_f32_16x16x32_bf16 a[252:255],v[112:115],a[96:99],a[252:255]
ds_read_b64_tr_b16 v[112:113],v2 offset:224
ds_read_b64_tr_b16 v[114:115],v2 offset:5344
v_mfma_f32_16x16x32_bf16 a[0:3],v[64:67],a[68:71],0
ds_read_b64_tr_b16 v[116:117],v2 offset:10464
v_cvt_pk_bf16_f32 v180,v180,v181
v_mfma_f32_16x16x32_bf16 a[0:3],v[68:71],a[76:79],a[0:3]
ds_read_b64_tr_b16 v[118:119],v2 offset:15584
v_cvt_pk_bf16_f32 v181,v182,v183
v_mfma_f32_16x16x32_bf16 a[0:3],v[72:75],a[84:87],a[0:3]
ds_read_b64_tr_b16 v[124:125],v2 offset:30944
v_cvt_pk_bf16_f32 v182,v188,v189
v_mfma_f32_16x16x32_bf16 a[0:3],v[76:79],a[92:95],a[0:3]
ds_read_b64_tr_b16 v[126:127],v2 offset:36064
v_cvt_pk_bf16_f32 v183,v190,v191
v_mfma_f32_16x16x32_bf16 a[4:7],v[76:79],a[124:127],0
v_mul_f32 v196,v196,v57
v_mul_f32 v197,v197,v57
v_mfma_f32_16x16x32_bf16 a[4:7],v[72:75],a[116:119],a[4:7]
v_mul_f32 v198,v198,v57
v_mul_f32 v199,v199,v57
v_mfma_f32_16x16x32_bf16 a[4:7],v[68:71],a[108:111],a[4:7]
v_mul_f32 v204,v204,v57
v_mul_f32 v205,v205,v57
v_mfma_f32_16x16x32_bf16 a[4:7],v[64:67],a[100:103],a[4:7]
v_mul_f32 v206,v206,v57
v_mul_f32 v207,v207,v57
v_mfma_f32_16x16x32_bf16 a[8:11],v[64:67],a[64:67],0
v_cvt_pk_bf16_f32 v196,v196,v197
v_cvt_pk_bf16_f32 v197,v198,v199
v_mfma_f32_16x16x32_bf16 a[8:11],v[68:71],a[72:75],a[8:11]
v_cvt_pk_bf16_f32 v198,v204,v205
v_cvt_pk_bf16_f32 v199,v206,v207
v_mfma_f32_16x16x32_bf16 a[8:11],v[72:75],a[80:83],a[8:11]
v_mul_f32 v212,v212,v57
v_mul_f32 v213,v213,v57
v_mfma_f32_16x16x32_bf16 a[8:11],v[76:79],a[88:91],a[8:11]
v_mul_f32 v214,v214,v57
v_mul_f32 v215,v215,v57
v_mfma_f32_16x16x32_bf16 a[12:15],v[76:79],a[120:123],0
ds_read_b64_tr_b16 v[76:77],v1 offset:30976
ds_read_b64_tr_b16 v[78:79],v1 offset:36096
v_mfma_f32_16x16x32_bf16 a[12:15],v[72:75],a[112:115],a[12:15]
buffer_store_dwordx4 v[148:151],v6,s[40:43],s80 offen offset:1024 sc1
v_mfma_f32_16x16x32_bf16 a[12:15],v[68:71],a[104:107],a[12:15]
ds_read_b64_tr_b16 v[68:69],v1 offset:10496
ds_read_b64_tr_b16 v[70:71],v1 offset:15616
v_mfma_f32_16x16x32_bf16 a[12:15],v[64:67],a[96:99],a[12:15]
s_waitcnt lgkmcnt(4)
ds_read_b64_tr_b16 v[64:65],v1 offset:256
v_mul_f32 v220,v220,v57
v_mfma_f32_16x16x32_bf16 a[16:19],v[80:83],a[68:71],0
ds_read_b64_tr_b16 v[66:67],v1 offset:5376
v_mul_f32 v221,v221,v57
v_mfma_f32_16x16x32_bf16 a[16:19],v[84:87],a[76:79],a[16:19]
ds_read_b64_tr_b16 v[72:73],v1 offset:20736
v_mul_f32 v222,v222,v57
v_mfma_f32_16x16x32_bf16 a[16:19],v[88:91],a[84:87],a[16:19]
ds_read_b64_tr_b16 v[74:75],v1 offset:25856
v_add_u32 v1,0xffff6000,v1
v_mfma_f32_16x16x32_bf16 a[16:19],v[92:95],a[92:95],a[16:19]
v_mul_f32 v223,v223,v57
v_cvt_pk_bf16_f32 v212,v212,v213
v_mfma_f32_16x16x32_bf16 a[20:23],v[92:95],a[124:127],0
v_cvt_pk_bf16_f32 v213,v214,v215
v_cvt_pk_bf16_f32 v214,v220,v221
v_mfma_f32_16x16x32_bf16 a[20:23],v[88:91],a[116:119],a[20:23]
v_cvt_pk_bf16_f32 v215,v222,v223
v_mul_f32 v228,v228,v57
v_mfma_f32_16x16x32_bf16 a[20:23],v[84:87],a[108:111],a[20:23]
v_mul_f32 v229,v229,v57
v_mul_f32 v230,v230,v57
v_mfma_f32_16x16x32_bf16 a[20:23],v[80:83],a[100:103],a[20:23]
v_mul_f32 v231,v231,v57
v_mul_f32 v236,v236,v57
v_mfma_f32_16x16x32_bf16 a[24:27],v[80:83],a[64:67],0
v_mul_f32 v237,v237,v57
v_mul_f32 v238,v238,v57
v_mfma_f32_16x16x32_bf16 a[24:27],v[84:87],a[72:75],a[24:27]
v_mul_f32 v239,v239,v57
v_cvt_pk_bf16_f32 v228,v228,v229
v_mfma_f32_16x16x32_bf16 a[24:27],v[88:91],a[80:83],a[24:27]
s_waitcnt vmcnt(5)
v_cmp_ne_u32 s[56:57],s52,v4
v_cmp_ne_u32 s[58:59],s53,v5
v_mfma_f32_16x16x32_bf16 a[24:27],v[92:95],a[88:91],a[24:27]
s_or_b64 s[56:57],s[56:57],s[58:59]
s_cbranch_scc1 L_fr1_%=
L_fo1_%=:
v_cvt_pk_bf16_f32 v229,v230,v231
v_mfma_f32_16x16x32_bf16 a[28:31],v[92:95],a[120:123],0
buffer_store_dwordx4 v[164:167],v6,s[40:43],s81 offen offset:1024 sc1
v_mfma_f32_16x16x32_bf16 a[28:31],v[88:91],a[112:115],a[28:31]
s_lshl_b32 s91,s63,8
ds_read_b64_tr_b16 v[88:89],v2 offset:20768
ds_read_b64_tr_b16 v[90:91],v2 offset:25888
v_mfma_f32_16x16x32_bf16 a[28:31],v[84:87],a[104:107],a[28:31]
buffer_load_dwordx4 a[128:131],v6,s[28:31],s87 offen sc1
v_mfma_f32_16x16x32_bf16 a[28:31],v[80:83],a[96:99],a[28:31]
ds_read_b64_tr_b16 v[80:81],v2 offset:288
ds_read_b64_tr_b16 v[82:83],v2 offset:5408
v_mfma_f32_16x16x32_bf16 v[128:131],v[96:99],a[68:71],0
ds_read_b64_tr_b16 v[84:85],v2 offset:10528
v_cvt_pk_bf16_f32 v230,v236,v237
v_mfma_f32_16x16x32_bf16 v[128:131],v[100:103],a[76:79],v[128:131]
ds_read_b64_tr_b16 v[86:87],v2 offset:15648
v_cvt_pk_bf16_f32 v231,v238,v239
v_mfma_f32_16x16x32_bf16 v[128:131],v[104:107],a[84:87],v[128:131]
ds_read_b64_tr_b16 v[92:93],v2 offset:31008
v_mul_f32 v244,v244,v57
)A"
R"A(v_mfma_f32_16x16x32_bf16 v[128:131],v[108:111],a[92:95],v[128:131]
ds_read_b64_tr_b16 v[94:95],v2 offset:36128
v_add_u32 v2,0xffff6000,v2
v_mfma_f32_16x16x32_bf16 v[136:139],v[108:111],a[124:127],0
v_mul_f32 v245,v245,v57
v_mul_f32 v246,v246,v57
v_mfma_f32_16x16x32_bf16 v[136:139],v[104:107],a[116:119],v[136:139]
v_mul_f32 v247,v247,v57
v_mul_f32 v252,v252,v57
v_mfma_f32_16x16x32_bf16 v[136:139],v[100:103],a[108:111],v[136:139]
v_mul_f32 v253,v253,v57
v_mul_f32 v254,v254,v57
v_mfma_f32_16x16x32_bf16 v[136:139],v[96:99],a[100:103],v[136:139]
v_mul_f32 v255,v255,v57
v_cvt_pk_bf16_f32 v244,v244,v245
v_mfma_f32_16x16x32_bf16 v[144:147],v[96:99],a[64:67],0
v_cvt_pk_bf16_f32 v245,v246,v247
v_cvt_pk_bf16_f32 v246,v252,v253
v_mfma_f32_16x16x32_bf16 v[144:147],v[100:103],a[72:75],v[144:147]
buffer_load_dwordx4 a[144:147],v6,s[28:31],s88 offen sc1
v_mfma_f32_16x16x32_bf16 v[144:147],v[104:107],a[80:83],v[144:147]
v_cvt_pk_bf16_f32 v247,v254,v255
v_accvgpr_read_b32 v8,a40
v_mfma_f32_16x16x32_bf16 v[144:147],v[108:111],a[88:91],v[144:147]
buffer_store_dwordx4 v[180:183],v6,s[40:43],s82 offen offset:1024 sc1
v_mfma_f32_16x16x32_bf16 v[152:155],v[108:111],a[120:123],0
ds_read_b64_tr_b16 v[108:109],v1 offset:30720
ds_read_b64_tr_b16 v[110:111],v1 offset:35840
v_mfma_f32_16x16x32_bf16 v[152:155],v[104:107],a[112:115],v[152:155]
ds_read_b64_tr_b16 v[104:105],v1 offset:20480
v_accvgpr_read_b32 v9,a41
v_mfma_f32_16x16x32_bf16 v[152:155],v[100:103],a[104:107],v[152:155]
ds_read_b64_tr_b16 v[100:101],v1 offset:10240
v_accvgpr_read_b32 v10,a42
v_mfma_f32_16x16x32_bf16 v[152:155],v[96:99],a[96:99],v[152:155]
ds_read_b64_tr_b16 v[96:97],v1
v_accvgpr_read_b32 v11,a43
v_mfma_f32_16x16x32_bf16 v[160:163],v[112:115],a[68:71],0
ds_read_b64_tr_b16 v[98:99],v1 offset:5120
ds_read_b64_tr_b16 v[102:103],v1 offset:15360
v_mfma_f32_16x16x32_bf16 v[160:163],v[116:119],a[76:79],v[160:163]
ds_read_b64_tr_b16 v[106:107],v1 offset:25600
v_accvgpr_read_b32 v12,a44
v_mfma_f32_16x16x32_bf16 v[160:163],v[120:123],a[84:87],v[160:163]
v_accvgpr_read_b32 v13,a45
v_accvgpr_read_b32 v14,a46
v_mfma_f32_16x16x32_bf16 v[160:163],v[124:127],a[92:95],v[160:163]
v_accvgpr_read_b32 v15,a47
v_mul_f32 v8,v8,v61
v_mfma_f32_16x16x32_bf16 v[168:171],v[124:127],a[124:127],0
v_mul_f32 v9,v9,v61
v_mul_f32 v10,v10,v61
v_mfma_f32_16x16x32_bf16 v[168:171],v[120:123],a[116:119],v[168:171]
v_mul_f32 v11,v11,v61
v_mul_f32 v12,v12,v61
v_mfma_f32_16x16x32_bf16 v[168:171],v[116:119],a[108:111],v[168:171]
buffer_load_dwordx4 a[160:163],v6,s[28:31],s89 offen sc1
v_mfma_f32_16x16x32_bf16 v[168:171],v[112:115],a[100:103],v[168:171]
v_mul_f32 v13,v13,v61
v_mul_f32 v14,v14,v61
v_mfma_f32_16x16x32_bf16 v[176:179],v[112:115],a[64:67],0
v_mul_f32 v15,v15,v61
v_cvt_pk_bf16_f32 v156,v8,v9
v_mfma_f32_16x16x32_bf16 v[176:179],v[116:119],a[72:75],v[176:179]
v_cvt_pk_bf16_f32 v157,v10,v11
v_cvt_pk_bf16_f32 v158,v12,v13
v_mfma_f32_16x16x32_bf16 v[176:179],v[120:123],a[80:83],v[176:179]
buffer_store_dwordx4 v[196:199],v6,s[40:43],s79 offen offset:2048 sc1
v_mfma_f32_16x16x32_bf16 v[176:179],v[124:127],a[88:91],v[176:179]
v_cvt_pk_bf16_f32 v159,v14,v15
v_accvgpr_read_b32 v8,a56
v_mfma_f32_16x16x32_bf16 v[184:187],v[124:127],a[120:123],0
v_accvgpr_read_b32 v9,a57
v_accvgpr_read_b32 v10,a58
v_mfma_f32_16x16x32_bf16 v[184:187],v[120:123],a[112:115],v[184:187]
ds_read_b64_tr_b16 v[120:121],v2 offset:20512
ds_read_b64_tr_b16 v[122:123],v2 offset:25632
v_mfma_f32_16x16x32_bf16 v[184:187],v[116:119],a[104:107],v[184:187]
ds_read_b64_tr_b16 v[116:117],v2 offset:10272
v_accvgpr_read_b32 v11,a59
v_mfma_f32_16x16x32_bf16 v[184:187],v[112:115],a[96:99],v[184:187]
s_waitcnt lgkmcnt(3)
ds_read_b64_tr_b16 v[112:113],v2 offset:32
v_accvgpr_read_b32 v12,a60
v_mfma_f32_16x16x32_bf16 v[192:195],v[64:67],a[68:71],0
ds_read_b64_tr_b16 v[114:115],v2 offset:5152
v_accvgpr_read_b32 v13,a61
v_mfma_f32_16x16x32_bf16 v[192:195],v[68:71],a[76:79],v[192:195]
ds_read_b64_tr_b16 v[118:119],v2 offset:15392
v_accvgpr_read_b32 v14,a62
v_mfma_f32_16x16x32_bf16 v[192:195],v[72:75],a[84:87],v[192:195]
v_accvgpr_read_b32 v15,a63
v_mul_f32 v8,v8,v61
v_mfma_f32_16x16x32_bf16 v[192:195],v[76:79],a[92:95],v[192:195]
buffer_load_dwordx4 a[176:179],v6,s[28:31],s90 offen sc1
v_mfma_f32_16x16x32_bf16 v[200:203],v[76:79],a[124:127],0
v_mul_f32 v9,v9,v61
v_mul_f32 v10,v10,v61
v_mfma_f32_16x16x32_bf16 v[200:203],v[72:75],a[116:119],v[200:203]
v_mul_f32 v11,v11,v61
v_mul_f32 v12,v12,v61
v_mfma_f32_16x16x32_bf16 v[200:203],v[68:71],a[108:111],v[200:203]
v_mul_f32 v13,v13,v61
v_mul_f32 v14,v14,v61
v_mfma_f32_16x16x32_bf16 v[200:203],v[64:67],a[100:103],v[200:203]
v_mul_f32 v15,v15,v61
v_cvt_pk_bf16_f32 v188,v8,v9
v_mfma_f32_16x16x32_bf16 v[208:211],v[64:67],a[64:67],0
v_cvt_pk_bf16_f32 v189,v10,v11
v_cvt_pk_bf16_f32 v190,v12,v13
v_mfma_f32_16x16x32_bf16 v[208:211],v[68:71],a[72:75],v[208:211]
buffer_store_dwordx4 v[212:215],v6,s[40:43],s80 offen offset:2048 sc1
v_mfma_f32_16x16x32_bf16 v[208:211],v[72:75],a[80:83],v[208:211]
ds_read_b64_tr_b16 v[124:125],v2 offset:30752
v_cvt_pk_bf16_f32 v191,v14,v15
v_mfma_f32_16x16x32_bf16 v[208:211],v[76:79],a[88:91],v[208:211]
ds_read_b64_tr_b16 v[126:127],v2 offset:35872
v_accvgpr_read_b32 v16,a32
v_mfma_f32_16x16x32_bf16 v[216:219],v[76:79],a[120:123],0
v_accvgpr_read_b32 v17,a33
v_accvgpr_read_b32 v18,a34
v_mfma_f32_16x16x32_bf16 v[216:219],v[72:75],a[112:115],v[216:219]
v_accvgpr_read_b32 v19,a35
v_accvgpr_read_b32 v20,a36
v_mfma_f32_16x16x32_bf16 v[216:219],v[68:71],a[104:107],v[216:219]
v_accvgpr_read_b32 v21,a37
v_accvgpr_read_b32 v22,a38
v_mfma_f32_16x16x32_bf16 v[216:219],v[64:67],a[96:99],v[216:219]
v_accvgpr_read_b32 v23,a39
v_mul_f32 v16,v16,v61
v_mfma_f32_16x16x32_bf16 v[224:227],v[80:83],a[68:71],0
buffer_load_dwordx4 a[180:183],v6,s[28:31],s90 offen offset:1024 sc1
v_mfma_f32_16x16x32_bf16 v[224:227],v[84:87],a[76:79],v[224:227]
v_mul_f32 v17,v17,v61
v_mul_f32 v18,v18,v61
v_mfma_f32_16x16x32_bf16 v[224:227],v[88:91],a[84:87],v[224:227]
v_mul_f32 v19,v19,v61
v_mul_f32 v20,v20,v61
v_mfma_f32_16x16x32_bf16 v[224:227],v[92:95],a[92:95],v[224:227]
v_mul_f32 v21,v21,v61
v_mul_f32 v22,v22,v61
v_mfma_f32_16x16x32_bf16 v[232:235],v[92:95],a[124:127],0
v_mul_f32 v23,v23,v61
v_cvt_pk_bf16_f32 v140,v16,v17
v_mfma_f32_16x16x32_bf16 v[232:235],v[88:91],a[116:119],v[232:235]
v_cvt_pk_bf16_f32 v141,v18,v19
v_cvt_pk_bf16_f32 v142,v20,v21
v_mfma_f32_16x16x32_bf16 v[232:235],v[84:87],a[108:111],v[232:235]
v_cvt_pk_bf16_f32 v143,v22,v23
v_accvgpr_read_b32 v16,a48
v_mfma_f32_16x16x32_bf16 v[232:235],v[80:83],a[100:103],v[232:235]
v_accvgpr_read_b32 v17,a49
v_accvgpr_read_b32 v18,a50
v_mfma_f32_16x16x32_bf16 v[240:243],v[80:83],a[64:67],0
buffer_store_dwordx4 v[228:231],v6,s[40:43],s81 offen offset:2048 sc1
v_mfma_f32_16x16x32_bf16 v[240:243],v[84:87],a[72:75],v[240:243]
v_accvgpr_read_b32 v19,a51
v_accvgpr_read_b32 v20,a52
v_mfma_f32_16x16x32_bf16 v[240:243],v[88:91],a[80:83],v[240:243]
v_accvgpr_read_b32 v21,a53
v_accvgpr_read_b32 v22,a54
v_mfma_f32_16x16x32_bf16 v[240:243],v[92:95],a[88:91],v[240:243]
v_accvgpr_read_b32 v23,a55
v_mul_f32 v16,v16,v61
v_mfma_f32_16x16x32_bf16 v[248:251],v[92:95],a[120:123],0
v_mul_f32 v17,v17,v61
v_mul_f32 v18,v18,v61
v_mfma_f32_16x16x32_bf16 v[248:251],v[88:91],a[112:115],v[248:251]
buffer_load_dwordx4 a[164:167],v6,s[28:31],s89 offen offset:1024 sc1
v_mfma_f32_16x16x32_bf16 v[248:251],v[84:87],a[104:107],v[248:251]
v_mul_f32 v19,v19,v61
v_mul_f32 v20,v20,v61
v_mfma_f32_16x16x32_bf16 v[248:251],v[80:83],a[96:99],v[248:251]
v_mul_f32 v21,v21,v61
v_mul_f32 v22,v22,v61
v_mfma_f32_16x16x32_bf16 a[192:195],v[96:99],v[32:35],a[192:195]
v_mul_f32 v23,v23,v61
v_cvt_pk_bf16_f32 v172,v16,v17
v_mfma_f32_16x16x32_bf16 a[192:195],v[100:103],v[36:39],a[192:195]
v_cvt_pk_bf16_f32 v173,v18,v19
v_cvt_pk_bf16_f32 v174,v20,v21
v_mfma_f32_16x16x32_bf16 a[192:195],v[104:107],v[40:43],a[192:195]
ds_read_b64_tr_b16 v[64:65],v1 offset:64
v_cvt_pk_bf16_f32 v175,v22,v23
v_mfma_f32_16x16x32_bf16 a[192:195],v[108:111],v[44:47],a[192:195]
ds_read_b64_tr_b16 v[66:67],v1 offset:5184
ds_read_b64_tr_b16 v[68:69],v1 offset:10304
v_mfma_f32_16x16x32_bf16 a[196:199],v[108:111],v[180:183],a[196:199]
s_cmp_eq_u32 s60,3
s_cselect_b32 s69,0x40000000,0
ds_read_b64_tr_b16 v[70:71],v1 offset:15424
v_mfma_f32_16x16x32_bf16 a[196:199],v[104:107],v[164:167],a[196:199]
ds_read_b64_tr_b16 v[72:73],v1 offset:20544
v_add_u32 v28,s69,v6
v_mfma_f32_16x16x32_bf16 a[196:199],v[100:103],v[148:151],a[196:199]
s_lshl_b32 s69,s63,2
ds_read_b64_tr_b16 v[74:75],v1 offset:25664
v_mov_b32 v8,s52
v_mfma_f32_16x16x32_bf16 a[196:199],v[96:99],v[132:135],a[196:199]
buffer_store_dwordx4 v[244:247],v6,s[40:43],s82 offen offset:2048 sc1
v_mfma_f32_16x16x32_bf16 a[200:203],v[96:99],v[196:199],a[200:203]
ds_read_b64_tr_b16 v[76:77],v1 offset:30784
ds_read_b64_tr_b16 v[78:79],v1 offset:35904
v_mfma_f32_16x16x32_bf16 a[200:203],v[100:103],v[212:215],a[200:203]
ds_read_b64_tr_b16 v[80:81],v2 offset:96
v_mov_b32 v9,s53
v_mfma_f32_16x16x32_bf16 a[200:203],v[104:107],v[228:231],a[200:203]
buffer_load_dwordx4 a[148:151],v6,s[28:31],s88 offen offset:1024 sc1
v_mfma_f32_16x16x32_bf16 a[200:203],v[108:111],v[244:247],a[200:203]
ds_read_b64_tr_b16 v[82:83],v2 offset:5216
ds_read_b64_tr_b16 v[84:85],v2 offset:10336
v_mfma_f32_16x16x32_bf16 a[204:207],v[108:111],v[188:191],a[204:207]
ds_read_b64_tr_b16 v[86:87],v2 offset:15456
v_lshlrev_b32 v10,20,v6
v_mfma_f32_16x16x32_bf16 a[204:207],v[104:107],v[172:175],a[204:207]
ds_read_b64_tr_b16 v[88:89],v2 offset:20576
v_bfe_u32 v3,v6,4,2
v_mfma_f32_16x16x32_bf16 a[204:207],v[100:103],v[156:159],a[204:207]
ds_read_b64_tr_b16 v[90:91],v2 offset:25696
v_min_u32 v3,2,v3
v_mfma_f32_16x16x32_bf16 a[204:207],v[96:99],v[140:143],a[204:207]
s_waitcnt lgkmcnt(8)
ds_read_b64_tr_b16 v[92:93],v2 offset:30816
ds_read_b64_tr_b16 v[94:95],v2 offset:35936
v_mfma_f32_16x16x32_bf16 a[208:211],v[112:115],v[32:35],a[208:211]
ds_read_b64_tr_b16 v[96:97],v1 offset:128
v_add3_u32 v3,v3,s64,1
v_mfma_f32_16x16x32_bf16 a[208:211],v[116:119],v[36:39],a[208:211]
ds_read_b64_tr_b16 v[98:99],v1 offset:5248
v_and_b32 v3,3,v3
v_mfma_f32_16x16x32_bf16 a[208:211],v[120:123],v[40:43],a[208:211]
ds_read_b64_tr_b16 v[100:101],v1 offset:10368
v_lshlrev_b32 v3,6,v3
v_mfma_f32_16x16x32_bf16 a[208:211],v[124:127],v[44:47],a[208:211]
ds_read_b64_tr_b16 v[102:103],v1 offset:15488
ds_read_b64_tr_b16 v[104:105],v1 offset:20608
v_mfma_f32_16x16x32_bf16 a[212:215],v[124:127],v[180:183],a[212:215]
ds_read_b64_tr_b16 v[106:107],v1 offset:25728
v_bfe_u32 v56,v6,6,2
v_mfma_f32_16x16x32_bf16 a[212:215],v[120:123],v[164:167],a[212:215]
ds_read_b64_tr_b16 v[108:109],v1 offset:30848
v_lshrrev_b32 v57,8,v6
v_mfma_f32_16x16x32_bf16 a[212:215],v[116:119],v[148:151],a[212:215]
buffer_store_dwordx4 v[140:143],v28,s[40:43],s79 offen offset:3072 sc1
v_mfma_f32_16x16x32_bf16 a[212:215],v[112:115],v[132:135],a[212:215]
ds_read_b64_tr_b16 v[110:111],v1 offset:35968
v_cmp_eq_u32 s[56:57],v56,v57
v_mfma_f32_16x16x32_bf16 a[216:219],v[112:115],v[196:199],a[216:219]
buffer_load_dwordx4 a[132:135],v6,s[28:31],s87 offen offset:1024 sc1
v_mfma_f32_16x16x32_bf16 a[216:219],v[116:119],v[212:215],a[216:219]
v_bfe_u32 v56,v6,4,1
v_lshlrev_b32 v56,4,v56
v_mfma_f32_16x16x32_bf16 a[216:219],v[120:123],v[228:231],a[216:219]
v_mov_b32 v57,0x3f80
v_lshlrev_b32 v56,v56,v57
v_mfma_f32_16x16x32_bf16 a[216:219],v[124:127],v[244:247],a[216:219]
v_cndmask_b32 v56,0,v56,s[56:57]
v_bfe_u32 v57,v6,5,1
v_mfma_f32_16x16x32_bf16 a[220:223],v[124:127],v[188:191],a[220:223]
ds_read_b64_tr_b16 v[124:125],v2 offset:30880
ds_read_b64_tr_b16 v[126:127],v2 offset:36000
v_mfma_f32_16x16x32_bf16 a[220:223],v[120:123],v[172:175],a[220:223]
ds_read_b64_tr_b16 v[120:121],v2 offset:20640
v_cmp_eq_u32 s[56:57],0,v57
v_mfma_f32_16x16x32_bf16 a[220:223],v[116:119],v[156:159],a[220:223]
s_add_u32 s36,s12,s66
s_addc_u32 s37,s13,0
ds_read_b64_tr_b16 v[116:117],v2 offset:10400
v_mfma_f32_16x16x32_bf16 a[220:223],v[112:115],v[140:143],a[220:223]
s_and_b32 s37,s37,0xffff
ds_read_b64_tr_b16 v[112:113],v2 offset:160
)A"
R"A(v_cndmask_b32 v48,0,v56,s[56:57]
v_mfma_f32_16x16x32_bf16 a[224:227],v[64:67],v[32:35],a[224:227]
s_mul_i32 s66,s61,307200
ds_read_b64_tr_b16 v[114:115],v2 offset:5280
ds_read_b64_tr_b16 v[118:119],v2 offset:15520
v_mfma_f32_16x16x32_bf16 a[224:227],v[68:71],v[36:39],a[224:227]
ds_read_b64_tr_b16 v[122:123],v2 offset:25760
v_cndmask_b32 v49,v56,0,s[56:57]
v_mfma_f32_16x16x32_bf16 a[224:227],v[72:75],v[40:43],a[224:227]
s_waitcnt lgkmcnt(8)
v_mov_b32 v58,s52
v_mov_b32 v59,s53
v_mfma_f32_16x16x32_bf16 a[224:227],v[76:79],v[44:47],a[224:227]
s_add_u32 s24,s16,s66
s_addc_u32 s25,s17,0
v_lshlrev_b32 v60,20,v6
v_mfma_f32_16x16x32_bf16 a[228:231],v[76:79],v[180:183],a[228:231]
buffer_load_dwordx4 a[136:139],v6,s[28:31],s87 offen offset:2048 sc1
v_mfma_f32_16x16x32_bf16 a[228:231],v[72:75],v[164:167],a[228:231]
s_mul_i32 s66,s60,16384
s_add_u32 s66,s66,s67
s_add_u32 s79,s66,12288
v_mfma_f32_16x16x32_bf16 a[228:231],v[68:71],v[148:151],a[228:231]
buffer_store_dwordx4 v[156:159],v28,s[40:43],s80 offen offset:3072 sc1
v_mfma_f32_16x16x32_bf16 a[228:231],v[64:67],v[132:135],a[228:231]
s_add_u32 s80,s66,77824
v_bfe_u32 v61,v6,4,1
v_add3_u32 v61,v61,s63,2
v_mfma_f32_16x16x32_bf16 a[232:235],v[64:67],v[196:199],a[232:235]
s_and_b32 s25,s25,0xffff
v_and_b32 v61,3,v61
v_lshlrev_b32 v61,8,v61
v_mfma_f32_16x16x32_bf16 a[232:235],v[68:71],v[212:215],a[232:235]
s_add_u32 s67,s77,8192
v_mov_b32 v50,0
v_mov_b32 v51,0
v_mfma_f32_16x16x32_bf16 a[232:235],v[72:75],v[228:231],a[232:235]
v_mov_b32 v52,0
v_mov_b32 v53,0
v_mfma_f32_16x16x32_bf16 a[232:235],v[76:79],v[244:247],a[232:235]
v_mov_b32 v54,v48
v_mov_b32 v55,v49
v_mfma_f32_16x16x32_bf16 a[236:239],v[76:79],v[188:191],a[236:239]
ds_read_b64_tr_b16 v[76:77],v1 offset:30912
ds_read_b64_tr_b16 v[78:79],v1 offset:36032
v_mfma_f32_16x16x32_bf16 a[236:239],v[72:75],v[172:175],a[236:239]
ds_read_b64_tr_b16 v[72:73],v1 offset:20672
v_bfe_u32 v56,v6,4,4
v_mfma_f32_16x16x32_bf16 a[236:239],v[68:71],v[156:159],a[236:239]
ds_read_b64_tr_b16 v[68:69],v1 offset:10432
v_mul_u32_u24 v56,1280,v56
v_mfma_f32_16x16x32_bf16 a[236:239],v[64:67],v[140:143],a[236:239]
ds_read_b64_tr_b16 v[64:65],v1 offset:192
v_bfe_u32 v57,v6,8,1
v_mfma_f32_16x16x32_bf16 a[240:243],v[80:83],v[32:35],a[240:243]
ds_read_b64_tr_b16 v[66:67],v1 offset:5312
ds_read_b64_tr_b16 v[70:71],v1 offset:15552
v_mfma_f32_16x16x32_bf16 a[240:243],v[84:87],v[36:39],a[240:243]
buffer_load_dwordx4 a[152:155],v6,s[28:31],s88 offen offset:2048 sc1
v_mfma_f32_16x16x32_bf16 a[240:243],v[88:91],v[40:43],a[240:243]
ds_read_b64_tr_b16 v[74:75],v1 offset:25792
v_lshl_add_u32 v56,v57,5,v56
v_mfma_f32_16x16x32_bf16 a[240:243],v[92:95],v[44:47],a[240:243]
v_lshrrev_b32 v57,9,v6
v_lshl_add_u32 v56,v57,4,v56
v_mfma_f32_16x16x32_bf16 a[244:247],v[92:95],v[180:183],a[244:247]
v_mfma_f32_16x16x32_bf16 a[244:247],v[88:91],v[164:167],a[244:247]
buffer_store_dwordx4 v[172:175],v28,s[40:43],s81 offen offset:3072 sc1
v_mfma_f32_16x16x32_bf16 a[244:247],v[84:87],v[148:151],a[244:247]
s_add_u32 s81,s66,143360
v_mfma_f32_16x16x32_bf16 a[244:247],v[80:83],v[132:135],a[244:247]
v_mfma_f32_16x16x32_bf16 a[248:251],v[80:83],v[196:199],a[248:251]
v_mfma_f32_16x16x32_bf16 a[248:251],v[84:87],v[212:215],a[248:251]
v_mfma_f32_16x16x32_bf16 a[248:251],v[88:91],v[228:231],a[248:251]
v_mfma_f32_16x16x32_bf16 a[248:251],v[92:95],v[244:247],a[248:251]
v_mfma_f32_16x16x32_bf16 a[252:255],v[92:95],v[188:191],a[252:255]
ds_read_b64_tr_b16 v[92:93],v2 offset:30944
L_pd14_%=:
s_cbranch_execz L_trt1_%=
ds_read_b64_tr_b16 v[94:95],v2 offset:36064
v_mfma_f32_16x16x32_bf16 a[252:255],v[88:91],v[172:175],a[252:255]
ds_read_b64_tr_b16 v[88:89],v2 offset:20704
v_mfma_f32_16x16x32_bf16 a[252:255],v[84:87],v[156:159],a[252:255]
buffer_load_dwordx4 a[168:171],v6,s[28:31],s89 offen offset:2048 sc1
v_mfma_f32_16x16x32_bf16 a[252:255],v[80:83],v[140:143],a[252:255]
ds_read_b64_tr_b16 v[80:81],v2 offset:224
ds_read_b64_tr_b16 v[82:83],v2 offset:5344
v_mfma_f32_16x16x32_bf16 a[0:3],v[96:99],v[32:35],a[0:3]
ds_read_b64_tr_b16 v[84:85],v2 offset:10464
v_mfma_f32_16x16x32_bf16 a[0:3],v[100:103],v[36:39],a[0:3]
ds_read_b64_tr_b16 v[86:87],v2 offset:15584
v_mfma_f32_16x16x32_bf16 a[0:3],v[104:107],v[40:43],a[0:3]
ds_read_b64_tr_b16 v[90:91],v2 offset:25824
v_mfma_f32_16x16x32_bf16 a[0:3],v[108:111],v[44:47],a[0:3]
v_mfma_f32_16x16x32_bf16 a[4:7],v[108:111],v[180:183],a[4:7]
buffer_store_dwordx4 v[188:191],v28,s[40:43],s82 offen offset:3072 sc1
v_mfma_f32_16x16x32_bf16 a[4:7],v[104:107],v[164:167],a[4:7]
s_add_u32 s82,s66,208896
s_mul_i32 s66,s64,320
v_mfma_f32_16x16x32_bf16 a[4:7],v[100:103],v[148:151],a[4:7]
v_mfma_f32_16x16x32_bf16 a[4:7],v[96:99],v[132:135],a[4:7]
v_mfma_f32_16x16x32_bf16 a[8:11],v[96:99],v[196:199],a[8:11]
v_mfma_f32_16x16x32_bf16 a[8:11],v[100:103],v[212:215],a[8:11]
v_mfma_f32_16x16x32_bf16 a[8:11],v[104:107],v[228:231],a[8:11]
v_mfma_f32_16x16x32_bf16 a[8:11],v[108:111],v[244:247],a[8:11]
buffer_load_dwordx4 a[184:187],v6,s[28:31],s90 offen offset:2048 sc1
v_mfma_f32_16x16x32_bf16 a[12:15],v[108:111],v[188:191],a[12:15]
ds_read_b64_tr_b16 v[108:109],v1 offset:30976
ds_read_b64_tr_b16 v[110:111],v1 offset:36096
v_mfma_f32_16x16x32_bf16 a[12:15],v[104:107],v[172:175],a[12:15]
ds_read_b64_tr_b16 v[104:105],v1 offset:20736
v_mfma_f32_16x16x32_bf16 a[12:15],v[100:103],v[156:159],a[12:15]
ds_read_b64_tr_b16 v[100:101],v1 offset:10496
v_mfma_f32_16x16x32_bf16 a[12:15],v[96:99],v[140:143],a[12:15]
s_waitcnt lgkmcnt(4)
ds_read_b64_tr_b16 v[96:97],v1 offset:256
v_mfma_f32_16x16x32_bf16 a[16:19],v[112:115],v[32:35],a[16:19]
ds_read_b64_tr_b16 v[98:99],v1 offset:5376
ds_read_b64_tr_b16 v[102:103],v1 offset:15616
v_mfma_f32_16x16x32_bf16 a[16:19],v[116:119],v[36:39],a[16:19]
ds_read_b64_tr_b16 v[106:107],v1 offset:25856
v_add_u32 v1,0xffff6000,v1
v_mfma_f32_16x16x32_bf16 a[16:19],v[120:123],v[40:43],a[16:19]
v_mfma_f32_16x16x32_bf16 a[16:19],v[124:127],v[44:47],a[16:19]
v_mfma_f32_16x16x32_bf16 a[20:23],v[124:127],v[180:183],a[20:23]
v_mfma_f32_16x16x32_bf16 a[20:23],v[120:123],v[164:167],a[20:23]
v_mfma_f32_16x16x32_bf16 a[20:23],v[116:119],v[148:151],a[20:23]
v_mfma_f32_16x16x32_bf16 a[20:23],v[112:115],v[132:135],a[20:23]
v_mfma_f32_16x16x32_bf16 a[24:27],v[112:115],v[196:199],a[24:27]
buffer_load_dwordx4 a[188:191],v28,s[28:31],s90 offen offset:3072 sc1
v_mfma_f32_16x16x32_bf16 a[24:27],v[116:119],v[212:215],a[24:27]
v_mfma_f32_16x16x32_bf16 a[24:27],v[120:123],v[228:231],a[24:27]
v_mfma_f32_16x16x32_bf16 a[24:27],v[124:127],v[244:247],a[24:27]
v_mfma_f32_16x16x32_bf16 a[28:31],v[124:127],v[188:191],a[28:31]
ds_read_b64_tr_b16 v[124:125],v2 offset:31008
ds_read_b64_tr_b16 v[126:127],v2 offset:36128
v_mfma_f32_16x16x32_bf16 a[28:31],v[120:123],v[172:175],a[28:31]
ds_read_b64_tr_b16 v[120:121],v2 offset:20768
v_mfma_f32_16x16x32_bf16 a[28:31],v[116:119],v[156:159],a[28:31]
ds_read_b64_tr_b16 v[116:117],v2 offset:10528
v_mfma_f32_16x16x32_bf16 a[28:31],v[112:115],v[140:143],a[28:31]
ds_read_b64_tr_b16 v[112:113],v2 offset:288
v_mfma_f32_16x16x32_bf16 v[128:131],v[64:67],v[32:35],v[128:131]
ds_read_b64_tr_b16 v[114:115],v2 offset:5408
ds_read_b64_tr_b16 v[118:119],v2 offset:15648
v_mfma_f32_16x16x32_bf16 v[128:131],v[68:71],v[36:39],v[128:131]
ds_read_b64_tr_b16 v[122:123],v2 offset:25888
v_add_u32 v2,0xffff6000,v2
v_mfma_f32_16x16x32_bf16 v[128:131],v[72:75],v[40:43],v[128:131]
v_mfma_f32_16x16x32_bf16 v[128:131],v[76:79],v[44:47],v[128:131]
v_mfma_f32_16x16x32_bf16 v[136:139],v[76:79],v[180:183],v[136:139]
v_mfma_f32_16x16x32_bf16 v[136:139],v[72:75],v[164:167],v[136:139]
buffer_load_dwordx4 a[172:175],v28,s[28:31],s89 offen offset:3072 sc1
v_mfma_f32_16x16x32_bf16 v[136:139],v[68:71],v[148:151],v[136:139]
v_mfma_f32_16x16x32_bf16 v[136:139],v[64:67],v[132:135],v[136:139]
v_mfma_f32_16x16x32_bf16 v[144:147],v[64:67],v[196:199],v[144:147]
v_mfma_f32_16x16x32_bf16 v[144:147],v[68:71],v[212:215],v[144:147]
v_mfma_f32_16x16x32_bf16 v[144:147],v[72:75],v[228:231],v[144:147]
v_mfma_f32_16x16x32_bf16 v[144:147],v[76:79],v[244:247],v[144:147]
v_mfma_f32_16x16x32_bf16 v[152:155],v[76:79],v[188:191],v[152:155]
v_mfma_f32_16x16x32_bf16 v[152:155],v[72:75],v[172:175],v[152:155]
v_mfma_f32_16x16x32_bf16 v[152:155],v[68:71],v[156:159],v[152:155]
v_mfma_f32_16x16x32_bf16 v[152:155],v[64:67],v[140:143],v[152:155]
s_mov_b64 exec,0
s_cmp_eq_u32 s60,0
s_cbranch_scc1 L_pd16_%=
s_cmp_eq_u32 s60,1
s_cbranch_scc1 L_pd17_%=
s_cmp_eq_u32 s60,2
s_cbranch_scc1 L_pd18_%=
s_branch L_trtg2F_%=
L_trtg2F_%=:
s_mov_b64 exec,-1
s_waitcnt lgkmcnt(0)
s_barrier
v_mfma_f32_16x16x32_bf16 v[160:163],v[80:83],v[32:35],v[160:163]
buffer_load_dwordx4 v24,s[48:51],s83 offen lds
v_mfma_f32_16x16x32_bf16 v[160:163],v[84:87],v[36:39],v[160:163]
ds_read_b64_tr_b16 v[64:65],v1
ds_read_b64_tr_b16 v[66:67],v1 offset:5120
v_mfma_f32_16x16x32_bf16 v[160:163],v[88:91],v[40:43],v[160:163]
ds_read_b64_tr_b16 v[68:69],v1 offset:10240
v_mfma_f32_16x16x32_bf16 v[160:163],v[92:95],v[44:47],v[160:163]
ds_read_b64_tr_b16 v[70:71],v1 offset:15360
v_mfma_f32_16x16x32_bf16 v[168:171],v[92:95],v[180:183],v[168:171]
ds_read_b64_tr_b16 v[72:73],v1 offset:20480
v_mfma_f32_16x16x32_bf16 v[168:171],v[88:91],v[164:167],v[168:171]
buffer_load_dwordx4 a[156:159],v28,s[28:31],s88 offen offset:3072 sc1
v_mfma_f32_16x16x32_bf16 v[168:171],v[84:87],v[148:151],v[168:171]
ds_read_b64_tr_b16 v[74:75],v1 offset:25600
ds_read_b64_tr_b16 v[76:77],v1 offset:30720
v_mfma_f32_16x16x32_bf16 v[168:171],v[80:83],v[132:135],v[168:171]
ds_read_b64_tr_b16 v[78:79],v1 offset:35840
v_mfma_f32_16x16x32_bf16 v[176:179],v[80:83],v[196:199],v[176:179]
v_mfma_f32_16x16x32_bf16 v[176:179],v[84:87],v[212:215],v[176:179]
v_mfma_f32_16x16x32_bf16 v[176:179],v[88:91],v[228:231],v[176:179]
v_mfma_f32_16x16x32_bf16 v[176:179],v[92:95],v[244:247],v[176:179]
buffer_load_dwordx4 v25,s[48:51],s83 offen offset:1024 lds
v_mfma_f32_16x16x32_bf16 v[184:187],v[92:95],v[188:191],v[184:187]
ds_read_b64_tr_b16 v[92:93],v2 offset:30752
ds_read_b64_tr_b16 v[94:95],v2 offset:35872
v_mfma_f32_16x16x32_bf16 v[184:187],v[88:91],v[172:175],v[184:187]
ds_read_b64_tr_b16 v[88:89],v2 offset:20512
v_mfma_f32_16x16x32_bf16 v[184:187],v[84:87],v[156:159],v[184:187]
ds_read_b64_tr_b16 v[84:85],v2 offset:10272
v_mfma_f32_16x16x32_bf16 v[184:187],v[80:83],v[140:143],v[184:187]
ds_read_b64_tr_b16 v[80:81],v2 offset:32
v_mfma_f32_16x16x32_bf16 v[192:195],v[96:99],v[32:35],v[192:195]
buffer_load_dwordx4 a[140:143],v28,s[28:31],s87 offen offset:3072 sc1
v_mfma_f32_16x16x32_bf16 v[192:195],v[100:103],v[36:39],v[192:195]
s_add_u32 s87,s64,2
s_and_b32 s87,s87,3
s_lshl_b32 s87,s87,1
v_mfma_f32_16x16x32_bf16 v[192:195],v[104:107],v[40:43],v[192:195]
s_lshl_b32 s87,s87,16
s_add_u32 s88,s87,4096
s_add_u32 s89,s87,8192
v_mfma_f32_16x16x32_bf16 v[192:195],v[108:111],v[44:47],v[192:195]
s_add_u32 s90,s87,12288
ds_read_b64_tr_b16 v[82:83],v2 offset:5152
ds_read_b64_tr_b16 v[86:87],v2 offset:15392
v_mfma_f32_16x16x32_bf16 v[200:203],v[108:111],v[180:183],v[200:203]
ds_read_b64_tr_b16 v[90:91],v2 offset:25632
v_mfma_f32_16x16x32_bf16 v[200:203],v[104:107],v[164:167],v[200:203]
buffer_load_dwordx4 a[68:71],v6,s[28:31],s87 offen sc1
v_mfma_f32_16x16x32_bf16 v[200:203],v[100:103],v[148:151],v[200:203]
v_mfma_f32_16x16x32_bf16 v[200:203],v[96:99],v[132:135],v[200:203]
v_mfma_f32_16x16x32_bf16 v[208:211],v[96:99],v[196:199],v[208:211]
v_mfma_f32_16x16x32_bf16 v[208:211],v[100:103],v[212:215],v[208:211]
v_mfma_f32_16x16x32_bf16 v[208:211],v[104:107],v[228:231],v[208:211]
buffer_load_dwordx4 a[76:79],v6,s[28:31],s88 offen sc1
v_mfma_f32_16x16x32_bf16 v[208:211],v[108:111],v[244:247],v[208:211]
v_mfma_f32_16x16x32_bf16 v[216:219],v[108:111],v[188:191],v[216:219]
ds_read_b64_tr_b16 v[108:109],v1 offset:30784
ds_read_b64_tr_b16 v[110:111],v1 offset:35904
v_mfma_f32_16x16x32_bf16 v[216:219],v[104:107],v[172:175],v[216:219]
ds_read_b64_tr_b16 v[104:105],v1 offset:20544
v_mfma_f32_16x16x32_bf16 v[216:219],v[100:103],v[156:159],v[216:219]
ds_read_b64_tr_b16 v[100:101],v1 offset:10304
v_mfma_f32_16x16x32_bf16 v[216:219],v[96:99],v[140:143],v[216:219]
)A"
R"A(buffer_load_dwordx4 a[84:87],v6,s[28:31],s89 offen sc1
v_mfma_f32_16x16x32_bf16 v[224:227],v[112:115],v[32:35],v[224:227]
ds_read_b64_tr_b16 v[96:97],v1 offset:64
ds_read_b64_tr_b16 v[98:99],v1 offset:5184
v_mfma_f32_16x16x32_bf16 v[224:227],v[116:119],v[36:39],v[224:227]
ds_read_b64_tr_b16 v[102:103],v1 offset:15424
v_mfma_f32_16x16x32_bf16 v[224:227],v[120:123],v[40:43],v[224:227]
ds_read_b64_tr_b16 v[106:107],v1 offset:25664
v_mfma_f32_16x16x32_bf16 v[224:227],v[124:127],v[44:47],v[224:227]
v_mfma_f32_16x16x32_bf16 v[232:235],v[124:127],v[180:183],v[232:235]
buffer_load_dwordx4 a[92:95],v6,s[28:31],s90 offen sc1
v_mfma_f32_16x16x32_bf16 v[232:235],v[120:123],v[164:167],v[232:235]
v_mfma_f32_16x16x32_bf16 v[232:235],v[116:119],v[148:151],v[232:235]
v_mfma_f32_16x16x32_bf16 v[232:235],v[112:115],v[132:135],v[232:235]
v_mfma_f32_16x16x32_bf16 v[240:243],v[112:115],v[196:199],v[240:243]
v_mfma_f32_16x16x32_bf16 v[240:243],v[116:119],v[212:215],v[240:243]
buffer_load_dwordx4 a[124:127],v6,s[28:31],s90 offen offset:1024 sc1
v_mfma_f32_16x16x32_bf16 v[240:243],v[120:123],v[228:231],v[240:243]
v_mfma_f32_16x16x32_bf16 v[240:243],v[124:127],v[244:247],v[240:243]
v_mfma_f32_16x16x32_bf16 v[248:251],v[124:127],v[188:191],v[248:251]
ds_read_b64_tr_b16 v[124:125],v2 offset:30816
ds_read_b64_tr_b16 v[126:127],v2 offset:35936
v_mfma_f32_16x16x32_bf16 v[248:251],v[120:123],v[172:175],v[248:251]
ds_read_b64_tr_b16 v[120:121],v2 offset:20576
v_mfma_f32_16x16x32_bf16 v[248:251],v[116:119],v[156:159],v[248:251]
buffer_load_dwordx4 a[116:119],v6,s[28:31],s89 offen offset:1024 sc1
v_mfma_f32_16x16x32_bf16 v[248:251],v[112:115],v[140:143],v[248:251]
s_waitcnt vmcnt(31) lgkmcnt(3)
ds_read_b64_tr_b16 v[112:113],v2 offset:96
ds_read_b64_tr_b16 v[114:115],v2 offset:5216
v_mfma_f32_16x16x32_bf16 a[192:195],v[64:67],a[128:131],a[192:195]
ds_read_b64_tr_b16 v[116:117],v2 offset:10336
v_mfma_f32_16x16x32_bf16 a[192:195],v[68:71],a[144:147],a[192:195]
s_waitcnt vmcnt(29)
ds_read_b64_tr_b16 v[118:119],v2 offset:15456
v_mfma_f32_16x16x32_bf16 a[192:195],v[72:75],a[160:163],a[192:195]
s_waitcnt vmcnt(25)
ds_read_b64_tr_b16 v[122:123],v2 offset:25696
v_mfma_f32_16x16x32_bf16 a[192:195],v[76:79],a[176:179],a[192:195]
buffer_load_dwordx4 a[108:111],v6,s[28:31],s88 offen offset:1024 sc1
v_mfma_f32_16x16x32_bf16 a[196:199],v[76:79],a[180:183],a[196:199]
s_waitcnt vmcnt(24)
v_mfma_f32_16x16x32_bf16 a[196:199],v[72:75],a[164:167],a[196:199]
s_waitcnt vmcnt(22)
v_mfma_f32_16x16x32_bf16 a[196:199],v[68:71],a[148:151],a[196:199]
s_waitcnt vmcnt(19)
v_mfma_f32_16x16x32_bf16 a[196:199],v[64:67],a[132:135],a[196:199]
s_waitcnt vmcnt(17)
v_mfma_f32_16x16x32_bf16 a[200:203],v[64:67],a[136:139],a[200:203]
buffer_load_dwordx4 a[100:103],v6,s[28:31],s87 offen offset:1024 sc1
v_mfma_f32_16x16x32_bf16 a[200:203],v[68:71],a[152:155],a[200:203]
s_waitcnt vmcnt(16)
v_mfma_f32_16x16x32_bf16 a[200:203],v[72:75],a[168:171],a[200:203]
s_waitcnt vmcnt(14)
v_mfma_f32_16x16x32_bf16 a[200:203],v[76:79],a[184:187],a[200:203]
s_waitcnt vmcnt(13)
v_mfma_f32_16x16x32_bf16 a[204:207],v[76:79],a[188:191],a[204:207]
s_waitcnt vmcnt(10)
ds_read_b64_tr_b16 v[76:77],v1 offset:30848
ds_read_b64_tr_b16 v[78:79],v1 offset:35968
v_mfma_f32_16x16x32_bf16 a[204:207],v[72:75],a[172:175],a[204:207]
buffer_load_dwordx4 a[64:67],v6,s[28:31],s87 offen offset:2048 sc1
v_mfma_f32_16x16x32_bf16 a[204:207],v[68:71],a[156:159],a[204:207]
s_waitcnt vmcnt(9)
ds_read_b64_tr_b16 v[68:69],v1 offset:10368
ds_read_b64_tr_b16 v[70:71],v1 offset:15488
v_mfma_f32_16x16x32_bf16 a[204:207],v[64:67],a[140:143],a[204:207]
ds_read_b64_tr_b16 v[64:65],v1 offset:128
v_mfma_f32_16x16x32_bf16 a[208:211],v[80:83],a[128:131],a[208:211]
ds_read_b64_tr_b16 v[66:67],v1 offset:5248
v_mfma_f32_16x16x32_bf16 a[208:211],v[84:87],a[144:147],a[208:211]
ds_read_b64_tr_b16 v[72:73],v1 offset:20608
v_mfma_f32_16x16x32_bf16 a[208:211],v[88:91],a[160:163],a[208:211]
ds_read_b64_tr_b16 v[74:75],v1 offset:25728
v_mfma_f32_16x16x32_bf16 a[208:211],v[92:95],a[176:179],a[208:211]
buffer_load_dwordx4 v26,s[48:51],s83 offen offset:2048 lds
v_mfma_f32_16x16x32_bf16 a[212:215],v[92:95],a[180:183],a[212:215]
v_mfma_f32_16x16x32_bf16 a[212:215],v[88:91],a[164:167],a[212:215]
v_mfma_f32_16x16x32_bf16 a[212:215],v[84:87],a[148:151],a[212:215]
v_mfma_f32_16x16x32_bf16 a[212:215],v[80:83],a[132:135],a[212:215]
v_mfma_f32_16x16x32_bf16 a[216:219],v[80:83],a[136:139],a[216:219]
buffer_load_dwordx4 a[72:75],v6,s[28:31],s88 offen offset:2048 sc1
v_mfma_f32_16x16x32_bf16 a[216:219],v[84:87],a[152:155],a[216:219]
v_mfma_f32_16x16x32_bf16 a[216:219],v[88:91],a[168:171],a[216:219]
v_mfma_f32_16x16x32_bf16 a[216:219],v[92:95],a[184:187],a[216:219]
v_mfma_f32_16x16x32_bf16 a[220:223],v[92:95],a[188:191],a[220:223]
ds_read_b64_tr_b16 v[92:93],v2 offset:30880
ds_read_b64_tr_b16 v[94:95],v2 offset:36000
v_mfma_f32_16x16x32_bf16 a[220:223],v[88:91],a[172:175],a[220:223]
ds_read_b64_tr_b16 v[88:89],v2 offset:20640
v_mfma_f32_16x16x32_bf16 a[220:223],v[84:87],a[156:159],a[220:223]
buffer_load_dwordx4 v27,s[48:51],s83 offen offset:3072 lds
v_mfma_f32_16x16x32_bf16 a[220:223],v[80:83],a[140:143],a[220:223]
s_add_u32 m0,s86,126976
ds_read_b64_tr_b16 v[80:81],v2 offset:160
ds_read_b64_tr_b16 v[82:83],v2 offset:5280
s_barrier
v_mfma_f32_16x16x32_bf16 a[224:227],v[96:99],a[128:131],a[224:227]
ds_read_b64_tr_b16 v[84:85],v2 offset:10400
v_mfma_f32_16x16x32_bf16 a[224:227],v[100:103],a[144:147],a[224:227]
ds_read_b64_tr_b16 v[86:87],v2 offset:15520
v_mfma_f32_16x16x32_bf16 a[224:227],v[104:107],a[160:163],a[224:227]
buffer_store_dwordx2 v[8:9],v10,s[44:47],s18 offen offset:1280 sc1
v_mfma_f32_16x16x32_bf16 a[224:227],v[108:111],a[176:179],a[224:227]
ds_read_b64_tr_b16 v[90:91],v2 offset:25760
v_mfma_f32_16x16x32_bf16 a[228:231],v[108:111],a[180:183],a[228:231]
v_mfma_f32_16x16x32_bf16 a[228:231],v[104:107],a[164:167],a[228:231]
v_mfma_f32_16x16x32_bf16 a[228:231],v[100:103],a[148:151],a[228:231]
v_mfma_f32_16x16x32_bf16 a[228:231],v[96:99],a[132:135],a[228:231]
buffer_load_dwordx4 a[80:83],v6,s[28:31],s89 offen offset:2048 sc1
v_mfma_f32_16x16x32_bf16 a[232:235],v[96:99],a[136:139],a[232:235]
v_mfma_f32_16x16x32_bf16 a[232:235],v[100:103],a[152:155],a[232:235]
v_mfma_f32_16x16x32_bf16 a[232:235],v[104:107],a[168:171],a[232:235]
v_mfma_f32_16x16x32_bf16 a[232:235],v[108:111],a[184:187],a[232:235]
v_mfma_f32_16x16x32_bf16 a[236:239],v[108:111],a[188:191],a[236:239]
s_waitcnt lgkmcnt(0)
ds_read_b64_tr_b16 v[108:109],v1 offset:30912
ds_read_b64_tr_b16 v[110:111],v1 offset:36032
v_mfma_f32_16x16x32_bf16 a[236:239],v[104:107],a[172:175],a[236:239]
buffer_load_dwordx4 v7,s[48:51],s83 offen lds
v_mfma_f32_16x16x32_bf16 a[236:239],v[100:103],a[156:159],a[236:239]
s_add_u32 m0,s86,128000
ds_read_b64_tr_b16 v[100:101],v1 offset:10432
ds_read_b64_tr_b16 v[102:103],v1 offset:15552
v_mfma_f32_16x16x32_bf16 a[236:239],v[96:99],a[140:143],a[236:239]
s_add_u32 s83,s64,1
s_and_b32 s83,s83,3
ds_read_b64_tr_b16 v[96:97],v1 offset:192
v_mfma_f32_16x16x32_bf16 a[240:243],v[112:115],a[128:131],a[240:243]
s_lshl_b32 s83,s83,1
s_or_b32 s83,s83,1
ds_read_b64_tr_b16 v[98:99],v1 offset:5312
v_mfma_f32_16x16x32_bf16 a[240:243],v[116:119],a[144:147],a[240:243]
s_mul_i32 s83,s83,163840
s_add_u32 s83,s83,s85
ds_read_b64_tr_b16 v[104:105],v1 offset:20672
v_mfma_f32_16x16x32_bf16 a[240:243],v[120:123],a[160:163],a[240:243]
buffer_load_dwordx4 a[88:91],v6,s[28:31],s90 offen offset:2048 sc1
v_mfma_f32_16x16x32_bf16 a[240:243],v[124:127],a[176:179],a[240:243]
ds_read_b64_tr_b16 v[106:107],v1 offset:25792
v_mfma_f32_16x16x32_bf16 a[244:247],v[124:127],a[180:183],a[244:247]
v_mfma_f32_16x16x32_bf16 a[244:247],v[120:123],a[164:167],a[244:247]
v_mfma_f32_16x16x32_bf16 a[244:247],v[116:119],a[148:151],a[244:247]
v_mfma_f32_16x16x32_bf16 a[244:247],v[112:115],a[132:135],a[244:247]
v_mfma_f32_16x16x32_bf16 a[248:251],v[112:115],a[136:139],a[248:251]
buffer_load_dwordx4 v24,s[48:51],s84 offen lds
v_mfma_f32_16x16x32_bf16 a[248:251],v[116:119],a[152:155],a[248:251]
v_mfma_f32_16x16x32_bf16 a[248:251],v[120:123],a[168:171],a[248:251]
v_mfma_f32_16x16x32_bf16 a[248:251],v[124:127],a[184:187],a[248:251]
v_mfma_f32_16x16x32_bf16 a[252:255],v[124:127],a[188:191],a[252:255]
ds_read_b64_tr_b16 v[124:125],v2 offset:30944
ds_read_b64_tr_b16 v[126:127],v2 offset:36064
v_mfma_f32_16x16x32_bf16 a[252:255],v[120:123],a[172:175],a[252:255]
buffer_load_dwordx4 a[120:123],v28,s[28:31],s90 offen offset:3072 sc1
v_mfma_f32_16x16x32_bf16 a[252:255],v[116:119],a[156:159],a[252:255]
ds_read_b64_tr_b16 v[116:117],v2 offset:10464
ds_read_b64_tr_b16 v[118:119],v2 offset:15584
v_mfma_f32_16x16x32_bf16 a[252:255],v[112:115],a[140:143],a[252:255]
ds_read_b64_tr_b16 v[112:113],v2 offset:224
v_mfma_f32_16x16x32_bf16 a[0:3],v[64:67],a[128:131],a[0:3]
ds_read_b64_tr_b16 v[114:115],v2 offset:5344
v_mfma_f32_16x16x32_bf16 a[0:3],v[68:71],a[144:147],a[0:3]
ds_read_b64_tr_b16 v[120:121],v2 offset:20704
v_mfma_f32_16x16x32_bf16 a[0:3],v[72:75],a[160:163],a[0:3]
ds_read_b64_tr_b16 v[122:123],v2 offset:25824
v_mfma_f32_16x16x32_bf16 a[0:3],v[76:79],a[176:179],a[0:3]
buffer_load_dwordx4 v25,s[48:51],s84 offen offset:1024 lds
v_mfma_f32_16x16x32_bf16 a[4:7],v[76:79],a[180:183],a[4:7]
v_mfma_f32_16x16x32_bf16 a[4:7],v[72:75],a[164:167],a[4:7]
v_mfma_f32_16x16x32_bf16 a[4:7],v[68:71],a[148:151],a[4:7]
v_mfma_f32_16x16x32_bf16 a[4:7],v[64:67],a[132:135],a[4:7]
v_mfma_f32_16x16x32_bf16 a[8:11],v[64:67],a[136:139],a[8:11]
buffer_load_dwordx4 a[112:115],v28,s[28:31],s89 offen offset:3072 sc1
v_mfma_f32_16x16x32_bf16 a[8:11],v[68:71],a[152:155],a[8:11]
v_mfma_f32_16x16x32_bf16 a[8:11],v[72:75],a[168:171],a[8:11]
v_mfma_f32_16x16x32_bf16 a[8:11],v[76:79],a[184:187],a[8:11]
v_mfma_f32_16x16x32_bf16 a[12:15],v[76:79],a[188:191],a[12:15]
ds_read_b64_tr_b16 v[76:77],v1 offset:30976
ds_read_b64_tr_b16 v[78:79],v1 offset:36096
v_mfma_f32_16x16x32_bf16 a[12:15],v[72:75],a[172:175],a[12:15]
buffer_load_dwordx4 a[104:107],v28,s[28:31],s88 offen offset:3072 sc1
v_mfma_f32_16x16x32_bf16 a[12:15],v[68:71],a[156:159],a[12:15]
ds_read_b64_tr_b16 v[68:69],v1 offset:10496
ds_read_b64_tr_b16 v[70:71],v1 offset:15616
v_mfma_f32_16x16x32_bf16 a[12:15],v[64:67],a[140:143],a[12:15]
ds_read_b64_tr_b16 v[64:65],v1 offset:256
v_mfma_f32_16x16x32_bf16 a[16:19],v[80:83],a[128:131],a[16:19]
ds_read_b64_tr_b16 v[66:67],v1 offset:5376
v_mfma_f32_16x16x32_bf16 a[16:19],v[84:87],a[144:147],a[16:19]
ds_read_b64_tr_b16 v[72:73],v1 offset:20736
v_mfma_f32_16x16x32_bf16 a[16:19],v[88:91],a[160:163],a[16:19]
ds_read_b64_tr_b16 v[74:75],v1 offset:25856
v_add_u32 v1,0xffff6000,v1
v_mfma_f32_16x16x32_bf16 a[16:19],v[92:95],a[176:179],a[16:19]
buffer_load_dwordx4 v26,s[48:51],s84 offen offset:2048 lds
v_mfma_f32_16x16x32_bf16 a[20:23],v[92:95],a[180:183],a[20:23]
v_mfma_f32_16x16x32_bf16 a[20:23],v[88:91],a[164:167],a[20:23]
v_mfma_f32_16x16x32_bf16 a[20:23],v[84:87],a[148:151],a[20:23]
v_mfma_f32_16x16x32_bf16 a[20:23],v[80:83],a[132:135],a[20:23]
v_mfma_f32_16x16x32_bf16 a[24:27],v[80:83],a[136:139],a[24:27]
buffer_load_dwordx4 a[96:99],v28,s[28:31],s87 offen offset:3072 sc1
v_mfma_f32_16x16x32_bf16 a[24:27],v[84:87],a[152:155],a[24:27]
s_add_u32 s87,s64,3
s_and_b32 s87,s87,3
s_lshl_b32 s87,s87,1
v_mfma_f32_16x16x32_bf16 a[24:27],v[88:91],a[168:171],a[24:27]
s_lshl_b32 s87,s87,16
s_add_u32 s88,s87,4096
s_add_u32 s89,s87,8192
v_mfma_f32_16x16x32_bf16 a[24:27],v[92:95],a[184:187],a[24:27]
s_add_u32 s90,s87,12288
v_mfma_f32_16x16x32_bf16 a[28:31],v[92:95],a[188:191],a[28:31]
ds_read_b64_tr_b16 v[92:93],v2 offset:31008
ds_read_b64_tr_b16 v[94:95],v2 offset:36128
v_mfma_f32_16x16x32_bf16 a[28:31],v[88:91],a[172:175],a[28:31]
s_waitcnt lgkmcnt(2)
ds_read_b64_tr_b16 v[88:89],v2 offset:20768
v_mfma_f32_16x16x32_bf16 a[28:31],v[84:87],a[156:159],a[28:31]
buffer_load_dwordx4 v27,s[48:51],s84 offen offset:3072 lds
v_mfma_f32_16x16x32_bf16 a[28:31],v[80:83],a[140:143],a[28:31]
s_add_u32 m0,s86,132096
ds_read_b64_tr_b16 v[80:81],v2 offset:288
ds_read_b64_tr_b16 v[82:83],v2 offset:5408
)A"
R"A(v_mfma_f32_16x16x32_bf16 v[128:131],v[96:99],a[128:131],v[128:131]
ds_read_b64_tr_b16 v[84:85],v2 offset:10528
v_mfma_f32_16x16x32_bf16 v[128:131],v[100:103],a[144:147],v[128:131]
ds_read_b64_tr_b16 v[86:87],v2 offset:15648
v_mfma_f32_16x16x32_bf16 v[128:131],v[104:107],a[160:163],v[128:131]
ds_read_b64_tr_b16 v[90:91],v2 offset:25888
v_add_u32 v2,0xffff6000,v2
v_mfma_f32_16x16x32_bf16 v[128:131],v[108:111],a[176:179],v[128:131]
v_mfma_f32_16x16x32_bf16 v[136:139],v[108:111],a[180:183],v[136:139]
buffer_load_dwordx4 v7,s[48:51],s84 offen lds
v_mfma_f32_16x16x32_bf16 v[136:139],v[104:107],a[164:167],v[136:139]
s_add_u32 m0,s86,81920
s_add_u32 s84,s83,20480
v_mfma_f32_16x16x32_bf16 v[136:139],v[100:103],a[148:151],v[136:139]
v_mfma_f32_16x16x32_bf16 v[136:139],v[96:99],a[132:135],v[136:139]
v_mfma_f32_16x16x32_bf16 v[144:147],v[96:99],a[136:139],v[144:147]
v_mfma_f32_16x16x32_bf16 v[144:147],v[100:103],a[152:155],v[144:147]
v_mfma_f32_16x16x32_bf16 v[144:147],v[104:107],a[168:171],v[144:147]
buffer_load_dwordx4 v24,s[48:51],s83 offen lds
v_mfma_f32_16x16x32_bf16 v[144:147],v[108:111],a[184:187],v[144:147]
v_mfma_f32_16x16x32_bf16 v[152:155],v[108:111],a[188:191],v[152:155]
ds_read_b64_tr_b16 v[108:109],v1 offset:30720
ds_read_b64_tr_b16 v[110:111],v1 offset:35840
v_mfma_f32_16x16x32_bf16 v[152:155],v[104:107],a[172:175],v[152:155]
ds_read_b64_tr_b16 v[104:105],v1 offset:20480
v_mfma_f32_16x16x32_bf16 v[152:155],v[100:103],a[156:159],v[152:155]
ds_read_b64_tr_b16 v[100:101],v1 offset:10240
v_mfma_f32_16x16x32_bf16 v[152:155],v[96:99],a[140:143],v[152:155]
ds_read_b64_tr_b16 v[96:97],v1
v_mfma_f32_16x16x32_bf16 v[160:163],v[112:115],a[128:131],v[160:163]
buffer_load_dwordx4 v25,s[48:51],s83 offen offset:1024 lds
v_mfma_f32_16x16x32_bf16 v[160:163],v[116:119],a[144:147],v[160:163]
ds_read_b64_tr_b16 v[98:99],v1 offset:5120
ds_read_b64_tr_b16 v[102:103],v1 offset:15360
v_mfma_f32_16x16x32_bf16 v[160:163],v[120:123],a[160:163],v[160:163]
ds_read_b64_tr_b16 v[106:107],v1 offset:25600
v_mfma_f32_16x16x32_bf16 v[160:163],v[124:127],a[176:179],v[160:163]
v_mfma_f32_16x16x32_bf16 v[168:171],v[124:127],a[180:183],v[168:171]
v_mfma_f32_16x16x32_bf16 v[168:171],v[120:123],a[164:167],v[168:171]
v_mfma_f32_16x16x32_bf16 v[168:171],v[116:119],a[148:151],v[168:171]
buffer_load_dwordx4 v26,s[48:51],s83 offen offset:2048 lds
v_mfma_f32_16x16x32_bf16 v[168:171],v[112:115],a[132:135],v[168:171]
v_mfma_f32_16x16x32_bf16 v[176:179],v[112:115],a[136:139],v[176:179]
v_mfma_f32_16x16x32_bf16 v[176:179],v[116:119],a[152:155],v[176:179]
v_mfma_f32_16x16x32_bf16 v[176:179],v[120:123],a[168:171],v[176:179]
v_mfma_f32_16x16x32_bf16 v[176:179],v[124:127],a[184:187],v[176:179]
v_mfma_f32_16x16x32_bf16 v[184:187],v[124:127],a[188:191],v[184:187]
buffer_load_dwordx4 v27,s[48:51],s83 offen offset:3072 lds
v_mfma_f32_16x16x32_bf16 v[184:187],v[120:123],a[172:175],v[184:187]
s_add_u32 m0,s86,86016
ds_read_b64_tr_b16 v[120:121],v2 offset:20512
ds_read_b64_tr_b16 v[122:123],v2 offset:25632
v_mfma_f32_16x16x32_bf16 v[184:187],v[116:119],a[156:159],v[184:187]
ds_read_b64_tr_b16 v[116:117],v2 offset:10272
v_mfma_f32_16x16x32_bf16 v[184:187],v[112:115],a[140:143],v[184:187]
ds_read_b64_tr_b16 v[112:113],v2 offset:32
v_mfma_f32_16x16x32_bf16 v[192:195],v[64:67],a[128:131],v[192:195]
ds_read_b64_tr_b16 v[114:115],v2 offset:5152
v_mfma_f32_16x16x32_bf16 v[192:195],v[68:71],a[144:147],v[192:195]
ds_read_b64_tr_b16 v[118:119],v2 offset:15392
ds_read_b64_tr_b16 v[124:125],v2 offset:30752
v_mfma_f32_16x16x32_bf16 v[192:195],v[72:75],a[160:163],v[192:195]
buffer_load_dwordx4 v7,s[48:51],s83 offen lds
v_mfma_f32_16x16x32_bf16 v[192:195],v[76:79],a[176:179],v[192:195]
s_add_u32 m0,s86,87040
s_add_u32 s83,s64,2
ds_read_b64_tr_b16 v[126:127],v2 offset:35872
v_mfma_f32_16x16x32_bf16 v[200:203],v[76:79],a[180:183],v[200:203]
s_and_b32 s83,s83,3
s_lshl_b32 s83,s83,1
s_or_b32 s83,s83,1
v_mfma_f32_16x16x32_bf16 v[200:203],v[72:75],a[164:167],v[200:203]
s_mul_i32 s83,s83,163840
s_add_u32 s83,s83,s85
v_mfma_f32_16x16x32_bf16 v[200:203],v[68:71],a[148:151],v[200:203]
v_mfma_f32_16x16x32_bf16 v[200:203],v[64:67],a[132:135],v[200:203]
v_mfma_f32_16x16x32_bf16 v[208:211],v[64:67],a[136:139],v[208:211]
buffer_load_dwordx4 v24,s[48:51],s84 offen lds
v_mfma_f32_16x16x32_bf16 v[208:211],v[68:71],a[152:155],v[208:211]
v_mfma_f32_16x16x32_bf16 v[208:211],v[72:75],a[168:171],v[208:211]
v_mfma_f32_16x16x32_bf16 v[208:211],v[76:79],a[184:187],v[208:211]
v_mfma_f32_16x16x32_bf16 v[216:219],v[76:79],a[188:191],v[216:219]
ds_read_b64_tr_b16 v[76:77],v1 offset:30784
ds_read_b64_tr_b16 v[78:79],v1 offset:35904
v_mfma_f32_16x16x32_bf16 v[216:219],v[72:75],a[172:175],v[216:219]
ds_read_b64_tr_b16 v[72:73],v1 offset:20544
v_mfma_f32_16x16x32_bf16 v[216:219],v[68:71],a[156:159],v[216:219]
buffer_load_dwordx2 v[4:5],v3,s[44:47],s91 offen offset:1280 sc1
v_mfma_f32_16x16x32_bf16 v[216:219],v[64:67],a[140:143],v[216:219]
s_waitcnt lgkmcnt(3)
ds_read_b64_tr_b16 v[64:65],v1 offset:64
ds_read_b64_tr_b16 v[66:67],v1 offset:5184
v_mfma_f32_16x16x32_bf16 v[224:227],v[80:83],a[128:131],v[224:227]
ds_read_b64_tr_b16 v[68:69],v1 offset:10304
v_mfma_f32_16x16x32_bf16 v[224:227],v[84:87],a[144:147],v[224:227]
ds_read_b64_tr_b16 v[70:71],v1 offset:15424
v_mfma_f32_16x16x32_bf16 v[224:227],v[88:91],a[160:163],v[224:227]
ds_read_b64_tr_b16 v[74:75],v1 offset:25664
v_mfma_f32_16x16x32_bf16 v[224:227],v[92:95],a[176:179],v[224:227]
v_mfma_f32_16x16x32_bf16 v[232:235],v[92:95],a[180:183],v[232:235]
buffer_load_dwordx4 v25,s[48:51],s84 offen offset:1024 lds
v_mfma_f32_16x16x32_bf16 v[232:235],v[88:91],a[164:167],v[232:235]
v_mfma_f32_16x16x32_bf16 v[232:235],v[84:87],a[148:151],v[232:235]
v_mfma_f32_16x16x32_bf16 v[232:235],v[80:83],a[132:135],v[232:235]
v_mfma_f32_16x16x32_bf16 v[240:243],v[80:83],a[136:139],v[240:243]
v_mfma_f32_16x16x32_bf16 v[240:243],v[84:87],a[152:155],v[240:243]
v_mfma_f32_16x16x32_bf16 v[240:243],v[88:91],a[168:171],v[240:243]
buffer_load_dwordx4 v26,s[48:51],s84 offen offset:2048 lds
v_mfma_f32_16x16x32_bf16 v[240:243],v[92:95],a[184:187],v[240:243]
v_mfma_f32_16x16x32_bf16 v[248:251],v[92:95],a[188:191],v[248:251]
ds_read_b64_tr_b16 v[92:93],v2 offset:30816
ds_read_b64_tr_b16 v[94:95],v2 offset:35936
v_mfma_f32_16x16x32_bf16 v[248:251],v[88:91],a[172:175],v[248:251]
ds_read_b64_tr_b16 v[88:89],v2 offset:20576
v_mfma_f32_16x16x32_bf16 v[248:251],v[84:87],a[156:159],v[248:251]
ds_read_b64_tr_b16 v[84:85],v2 offset:10336
v_mfma_f32_16x16x32_bf16 v[248:251],v[80:83],a[140:143],v[248:251]
s_waitcnt vmcnt(25)
ds_read_b64_tr_b16 v[80:81],v2 offset:96
v_mfma_f32_16x16x32_bf16 a[192:195],v[96:99],a[68:71],a[192:195]
buffer_load_dwordx4 v27,s[48:51],s84 offen offset:3072 lds
v_mfma_f32_16x16x32_bf16 a[192:195],v[100:103],a[76:79],a[192:195]
s_add_u32 m0,s86,91136
ds_read_b64_tr_b16 v[82:83],v2 offset:5216
ds_read_b64_tr_b16 v[86:87],v2 offset:15456
v_mfma_f32_16x16x32_bf16 a[192:195],v[104:107],a[84:87],a[192:195]
ds_read_b64_tr_b16 v[90:91],v2 offset:25696
v_mfma_f32_16x16x32_bf16 a[192:195],v[108:111],a[92:95],a[192:195]
v_mfma_f32_16x16x32_bf16 a[196:199],v[108:111],a[124:127],a[196:199]
v_mfma_f32_16x16x32_bf16 a[196:199],v[104:107],a[116:119],a[196:199]
v_mfma_f32_16x16x32_bf16 a[196:199],v[100:103],a[108:111],a[196:199]
buffer_load_dwordx4 v7,s[48:51],s84 offen lds
v_mfma_f32_16x16x32_bf16 a[196:199],v[96:99],a[100:103],a[196:199]
s_add_u32 m0,s86,40960
s_add_u32 s84,s83,20480
v_mfma_f32_16x16x32_bf16 a[200:203],v[96:99],a[64:67],a[200:203]
s_waitcnt vmcnt(25)
v_mfma_f32_16x16x32_bf16 a[200:203],v[100:103],a[72:75],a[200:203]
s_waitcnt vmcnt(22)
v_mfma_f32_16x16x32_bf16 a[200:203],v[104:107],a[80:83],a[200:203]
s_waitcnt vmcnt(18)
v_mfma_f32_16x16x32_bf16 a[200:203],v[108:111],a[88:91],a[200:203]
buffer_load_dwordx4 v[32:35],v6,s[28:31],s87 offen sc1
v_mfma_f32_16x16x32_bf16 a[204:207],v[108:111],a[120:123],a[204:207]
s_waitcnt vmcnt(17)
ds_read_b64_tr_b16 v[108:109],v1 offset:30848
ds_read_b64_tr_b16 v[110:111],v1 offset:35968
v_mfma_f32_16x16x32_bf16 a[204:207],v[104:107],a[112:115],a[204:207]
s_waitcnt vmcnt(16)
ds_read_b64_tr_b16 v[104:105],v1 offset:20608
v_mfma_f32_16x16x32_bf16 a[204:207],v[100:103],a[104:107],a[204:207]
s_waitcnt vmcnt(14)
ds_read_b64_tr_b16 v[100:101],v1 offset:10368
v_mfma_f32_16x16x32_bf16 a[204:207],v[96:99],a[96:99],a[204:207]
ds_read_b64_tr_b16 v[96:97],v1 offset:128
L_pd15_%=:
s_cbranch_execz L_trt1_%=
v_mfma_f32_16x16x32_bf16 a[208:211],v[112:115],a[68:71],a[208:211]
buffer_load_dwordx4 v[132:135],v6,s[28:31],s87 offen offset:1024 sc1
v_mfma_f32_16x16x32_bf16 a[208:211],v[116:119],a[76:79],a[208:211]
ds_read_b64_tr_b16 v[98:99],v1 offset:5248
ds_read_b64_tr_b16 v[102:103],v1 offset:15488
v_mfma_f32_16x16x32_bf16 a[208:211],v[120:123],a[84:87],a[208:211]
ds_read_b64_tr_b16 v[106:107],v1 offset:25728
v_mfma_f32_16x16x32_bf16 a[208:211],v[124:127],a[92:95],a[208:211]
v_mfma_f32_16x16x32_bf16 a[212:215],v[124:127],a[124:127],a[212:215]
v_mfma_f32_16x16x32_bf16 a[212:215],v[120:123],a[116:119],a[212:215]
buffer_load_dwordx4 v[196:199],v6,s[28:31],s87 offen offset:2048 sc1
v_mfma_f32_16x16x32_bf16 a[212:215],v[116:119],a[108:111],a[212:215]
v_mfma_f32_16x16x32_bf16 a[212:215],v[112:115],a[100:103],a[212:215]
v_mfma_f32_16x16x32_bf16 a[216:219],v[112:115],a[64:67],a[216:219]
v_mfma_f32_16x16x32_bf16 a[216:219],v[116:119],a[72:75],a[216:219]
v_mfma_f32_16x16x32_bf16 a[216:219],v[120:123],a[80:83],a[216:219]
buffer_load_dwordx4 v[36:39],v6,s[28:31],s88 offen sc1
v_mfma_f32_16x16x32_bf16 a[216:219],v[124:127],a[88:91],a[216:219]
v_mfma_f32_16x16x32_bf16 a[220:223],v[124:127],a[120:123],a[220:223]
ds_read_b64_tr_b16 v[124:125],v2 offset:30880
ds_read_b64_tr_b16 v[126:127],v2 offset:36000
v_mfma_f32_16x16x32_bf16 a[220:223],v[120:123],a[112:115],a[220:223]
ds_read_b64_tr_b16 v[120:121],v2 offset:20640
v_mfma_f32_16x16x32_bf16 a[220:223],v[116:119],a[104:107],a[220:223]
s_waitcnt lgkmcnt(3)
ds_read_b64_tr_b16 v[116:117],v2 offset:10400
v_mfma_f32_16x16x32_bf16 a[220:223],v[112:115],a[96:99],a[220:223]
buffer_load_dwordx4 v[140:143],v28,s[28:31],s87 offen offset:3072 sc1
v_mfma_f32_16x16x32_bf16 a[224:227],v[64:67],a[68:71],a[224:227]
s_add_u32 s87,s64,1
s_and_b32 s87,s87,3
s_lshl_b32 s87,s87,1
v_mfma_f32_16x16x32_bf16 a[224:227],v[68:71],a[76:79],a[224:227]
s_or_b32 s87,s87,1
s_lshl_b32 s87,s87,16
ds_read_b64_tr_b16 v[112:113],v2 offset:160
v_mfma_f32_16x16x32_bf16 a[224:227],v[72:75],a[84:87],a[224:227]
ds_read_b64_tr_b16 v[114:115],v2 offset:5280
ds_read_b64_tr_b16 v[118:119],v2 offset:15520
v_mfma_f32_16x16x32_bf16 a[224:227],v[76:79],a[92:95],a[224:227]
ds_read_b64_tr_b16 v[122:123],v2 offset:25760
v_mfma_f32_16x16x32_bf16 a[228:231],v[76:79],a[124:127],a[228:231]
buffer_load_dwordx4 v[212:215],v6,s[28:31],s88 offen offset:2048 sc1
v_mfma_f32_16x16x32_bf16 a[228:231],v[72:75],a[116:119],a[228:231]
v_mfma_f32_16x16x32_bf16 a[228:231],v[68:71],a[108:111],a[228:231]
v_mfma_f32_16x16x32_bf16 a[228:231],v[64:67],a[100:103],a[228:231]
v_mfma_f32_16x16x32_bf16 a[232:235],v[64:67],a[64:67],a[232:235]
v_mfma_f32_16x16x32_bf16 a[232:235],v[68:71],a[72:75],a[232:235]
buffer_load_dwordx4 v[148:151],v6,s[28:31],s88 offen offset:1024 sc1
v_mfma_f32_16x16x32_bf16 a[232:235],v[72:75],a[80:83],a[232:235]
s_waitcnt vmcnt(11)
v_cmp_ne_u32 s[56:57],s52,v4
v_cmp_ne_u32 s[58:59],s53,v5
v_mfma_f32_16x16x32_bf16 a[232:235],v[76:79],a[88:91],a[232:235]
s_or_b64 s[56:57],s[56:57],s[58:59]
s_cbranch_scc1 L_fr2_%=
L_fo2_%=:
v_mov_b32 v3,0
v_mfma_f32_16x16x32_bf16 a[236:239],v[76:79],a[120:123],a[236:239]
s_add_u32 s91,s64,3
ds_read_b64_tr_b16 v[76:77],v1 offset:30912
ds_read_b64_tr_b16 v[78:79],v1 offset:36032
v_mfma_f32_16x16x32_bf16 a[236:239],v[72:75],a[112:115],a[236:239]
s_and_b32 s91,s91,3
s_add_u32 s91,s91,s69
ds_read_b64_tr_b16 v[72:73],v1 offset:20672
v_mfma_f32_16x16x32_bf16 a[236:239],v[68:71],a[104:107],a[236:239]
buffer_load_dwordx4 v[40:43],v6,s[28:31],s89 offen sc1
v_mfma_f32_16x16x32_bf16 a[236:239],v[64:67],a[96:99],a[236:239]
s_add_u32 s91,s91,8
)A"
R"A(ds_read_b64_tr_b16 v[64:65],v1 offset:192
ds_read_b64_tr_b16 v[66:67],v1 offset:5312
v_mfma_f32_16x16x32_bf16 a[240:243],v[80:83],a[68:71],a[240:243]
s_lshl_b32 s91,s91,6
s_and_b32 s69,s63,s60
ds_read_b64_tr_b16 v[68:69],v1 offset:10432
v_mfma_f32_16x16x32_bf16 a[240:243],v[84:87],a[76:79],a[240:243]
s_cmp_eq_u32 s69,3
s_cselect_b32 s69,0x40000000,0
ds_read_b64_tr_b16 v[70:71],v1 offset:15552
v_mfma_f32_16x16x32_bf16 a[240:243],v[88:91],a[84:87],a[240:243]
s_add_u32 s68,s68,s69
ds_read_b64_tr_b16 v[74:75],v1 offset:25792
v_mfma_f32_16x16x32_bf16 a[240:243],v[92:95],a[92:95],a[240:243]
buffer_load_dwordx4 a[128:131],v6,s[28:31],s87 offen sc1
v_mfma_f32_16x16x32_bf16 a[244:247],v[92:95],a[124:127],a[244:247]
v_mfma_f32_16x16x32_bf16 a[244:247],v[88:91],a[116:119],a[244:247]
v_mfma_f32_16x16x32_bf16 a[244:247],v[84:87],a[108:111],a[244:247]
v_mfma_f32_16x16x32_bf16 a[244:247],v[80:83],a[100:103],a[244:247]
v_mfma_f32_16x16x32_bf16 a[248:251],v[80:83],a[64:67],a[248:251]
buffer_load_dwordx4 v[156:159],v28,s[28:31],s88 offen offset:3072 sc1
v_mfma_f32_16x16x32_bf16 a[248:251],v[84:87],a[72:75],a[248:251]
s_add_u32 s88,s87,4096
v_mfma_f32_16x16x32_bf16 a[248:251],v[88:91],a[80:83],a[248:251]
v_mfma_f32_16x16x32_bf16 a[248:251],v[92:95],a[88:91],a[248:251]
v_mfma_f32_16x16x32_bf16 a[252:255],v[92:95],a[120:123],a[252:255]
ds_read_b64_tr_b16 v[92:93],v2 offset:30944
ds_read_b64_tr_b16 v[94:95],v2 offset:36064
v_mfma_f32_16x16x32_bf16 a[252:255],v[88:91],a[112:115],a[252:255]
buffer_load_dwordx4 v[228:231],v6,s[28:31],s89 offen offset:2048 sc1
v_mfma_f32_16x16x32_bf16 a[252:255],v[84:87],a[104:107],a[252:255]
ds_read_b64_tr_b16 v[84:85],v2 offset:10464
ds_read_b64_tr_b16 v[86:87],v2 offset:15584
v_mfma_f32_16x16x32_bf16 a[252:255],v[80:83],a[96:99],a[252:255]
ds_read_b64_tr_b16 v[80:81],v2 offset:224
v_mfma_f32_16x16x32_bf16 a[0:3],v[96:99],a[68:71],a[0:3]
ds_read_b64_tr_b16 v[82:83],v2 offset:5344
v_mfma_f32_16x16x32_bf16 a[0:3],v[100:103],a[76:79],a[0:3]
ds_read_b64_tr_b16 v[88:89],v2 offset:20704
v_mfma_f32_16x16x32_bf16 a[0:3],v[104:107],a[84:87],a[0:3]
buffer_load_dwordx4 v[164:167],v6,s[28:31],s89 offen offset:1024 sc1
v_mfma_f32_16x16x32_bf16 a[0:3],v[108:111],a[92:95],a[0:3]
ds_read_b64_tr_b16 v[90:91],v2 offset:25824
v_mfma_f32_16x16x32_bf16 a[4:7],v[108:111],a[124:127],a[4:7]
v_mfma_f32_16x16x32_bf16 a[4:7],v[104:107],a[116:119],a[4:7]
v_mfma_f32_16x16x32_bf16 a[4:7],v[100:103],a[108:111],a[4:7]
v_mfma_f32_16x16x32_bf16 a[4:7],v[96:99],a[100:103],a[4:7]
buffer_load_dwordx4 v[44:47],v6,s[28:31],s90 offen sc1
v_mfma_f32_16x16x32_bf16 a[8:11],v[96:99],a[64:67],a[8:11]
v_mfma_f32_16x16x32_bf16 a[8:11],v[100:103],a[72:75],a[8:11]
v_mfma_f32_16x16x32_bf16 a[8:11],v[104:107],a[80:83],a[8:11]
v_mfma_f32_16x16x32_bf16 a[8:11],v[108:111],a[88:91],a[8:11]
v_mfma_f32_16x16x32_bf16 a[12:15],v[108:111],a[120:123],a[12:15]
buffer_load_dwordx4 a[144:147],v6,s[28:31],s88 offen sc1
v_mfma_f32_16x16x32_bf16 a[12:15],v[104:107],a[112:115],a[12:15]
ds_read_b64_tr_b16 v[104:105],v1 offset:20736
ds_read_b64_tr_b16 v[106:107],v1 offset:25856
v_mfma_f32_16x16x32_bf16 a[12:15],v[100:103],a[104:107],a[12:15]
ds_read_b64_tr_b16 v[100:101],v1 offset:10496
v_mfma_f32_16x16x32_bf16 a[12:15],v[96:99],a[96:99],a[12:15]
s_waitcnt lgkmcnt(3)
ds_read_b64_tr_b16 v[96:97],v1 offset:256
v_mfma_f32_16x16x32_bf16 a[16:19],v[112:115],a[68:71],a[16:19]
ds_read_b64_tr_b16 v[98:99],v1 offset:5376
v_mfma_f32_16x16x32_bf16 a[16:19],v[116:119],a[76:79],a[16:19]
buffer_load_dwordx4 v[172:175],v28,s[28:31],s89 offen offset:3072 sc1
v_mfma_f32_16x16x32_bf16 a[16:19],v[120:123],a[84:87],a[16:19]
s_add_u32 s89,s87,8192
ds_read_b64_tr_b16 v[102:103],v1 offset:15616
ds_read_b64_tr_b16 v[108:109],v1 offset:30976
v_mfma_f32_16x16x32_bf16 a[16:19],v[124:127],a[92:95],a[16:19]
ds_read_b64_tr_b16 v[110:111],v1 offset:36096
v_add_u32 v1,0x1e000,v1
v_mfma_f32_16x16x32_bf16 a[20:23],v[124:127],a[124:127],a[20:23]
v_mfma_f32_16x16x32_bf16 a[20:23],v[120:123],a[116:119],a[20:23]
v_mfma_f32_16x16x32_bf16 a[20:23],v[116:119],a[108:111],a[20:23]
buffer_load_dwordx4 v[180:183],v6,s[28:31],s90 offen offset:1024 sc1
v_mfma_f32_16x16x32_bf16 a[20:23],v[112:115],a[100:103],a[20:23]
v_mfma_f32_16x16x32_bf16 a[24:27],v[112:115],a[64:67],a[24:27]
v_mfma_f32_16x16x32_bf16 a[24:27],v[116:119],a[72:75],a[24:27]
v_mfma_f32_16x16x32_bf16 a[24:27],v[120:123],a[80:83],a[24:27]
v_mfma_f32_16x16x32_bf16 a[24:27],v[124:127],a[88:91],a[24:27]
buffer_load_dwordx4 a[160:163],v6,s[28:31],s89 offen sc1
v_mfma_f32_16x16x32_bf16 a[28:31],v[124:127],a[120:123],a[28:31]
ds_read_b64_tr_b16 v[124:125],v2 offset:31008
ds_read_b64_tr_b16 v[126:127],v2 offset:36128
v_mfma_f32_16x16x32_bf16 a[28:31],v[120:123],a[112:115],a[28:31]
ds_read_b64_tr_b16 v[120:121],v2 offset:20768
v_mfma_f32_16x16x32_bf16 a[28:31],v[116:119],a[104:107],a[28:31]
ds_read_b64_tr_b16 v[116:117],v2 offset:10528
v_mfma_f32_16x16x32_bf16 a[28:31],v[112:115],a[96:99],a[28:31]
ds_read_b64_tr_b16 v[112:113],v2 offset:288
v_mfma_f32_16x16x32_bf16 v[128:131],v[64:67],a[68:71],v[128:131]
buffer_load_dwordx4 v[244:247],v6,s[28:31],s90 offen offset:2048 sc1
v_mfma_f32_16x16x32_bf16 v[128:131],v[68:71],a[76:79],v[128:131]
ds_read_b64_tr_b16 v[114:115],v2 offset:5408
ds_read_b64_tr_b16 v[118:119],v2 offset:15648
v_mfma_f32_16x16x32_bf16 v[128:131],v[72:75],a[84:87],v[128:131]
ds_read_b64_tr_b16 v[122:123],v2 offset:25888
v_add_u32 v2,0x1e000,v2
v_mfma_f32_16x16x32_bf16 v[128:131],v[76:79],a[92:95],v[128:131]
v_mfma_f32_16x16x32_bf16 v[136:139],v[76:79],a[124:127],v[136:139]
v_mfma_f32_16x16x32_bf16 v[136:139],v[72:75],a[116:119],v[136:139]
buffer_load_dwordx4 v[188:191],v28,s[28:31],s90 offen offset:3072 sc1
v_mfma_f32_16x16x32_bf16 v[136:139],v[68:71],a[108:111],v[136:139]
s_add_u32 s90,s87,12288
v_mfma_f32_16x16x32_bf16 v[136:139],v[64:67],a[100:103],v[136:139]
v_mfma_f32_16x16x32_bf16 v[144:147],v[64:67],a[64:67],v[144:147]
v_mfma_f32_16x16x32_bf16 v[144:147],v[68:71],a[72:75],v[144:147]
v_mfma_f32_16x16x32_bf16 v[144:147],v[72:75],a[80:83],v[144:147]
buffer_load_dwordx4 a[176:179],v6,s[28:31],s90 offen sc1
v_mfma_f32_16x16x32_bf16 v[144:147],v[76:79],a[88:91],v[144:147]
v_mfma_f32_16x16x32_bf16 v[152:155],v[76:79],a[120:123],v[152:155]
v_mfma_f32_16x16x32_bf16 v[152:155],v[72:75],a[112:115],v[152:155]
v_mfma_f32_16x16x32_bf16 v[152:155],v[68:71],a[104:107],v[152:155]
v_mfma_f32_16x16x32_bf16 v[152:155],v[64:67],a[96:99],v[152:155]
buffer_load_dwordx4 a[180:183],v6,s[28:31],s90 offen offset:1024 sc1
s_waitcnt vmcnt(21) lgkmcnt(0)
s_barrier
v_mfma_f32_16x16x32_bf16 v[160:163],v[80:83],a[68:71],v[160:163]
ds_read_b64_tr_b16 v[64:65],v1
ds_read_b64_tr_b16 v[66:67],v1 offset:5120
v_mfma_f32_16x16x32_bf16 v[160:163],v[84:87],a[76:79],v[160:163]
ds_read_b64_tr_b16 v[68:69],v1 offset:10240
v_mfma_f32_16x16x32_bf16 v[160:163],v[88:91],a[84:87],v[160:163]
ds_read_b64_tr_b16 v[70:71],v1 offset:15360
v_mfma_f32_16x16x32_bf16 v[160:163],v[92:95],a[92:95],v[160:163]
buffer_load_dwordx4 a[164:167],v6,s[28:31],s89 offen offset:1024 sc1
v_mfma_f32_16x16x32_bf16 v[168:171],v[92:95],a[124:127],v[168:171]
ds_read_b64_tr_b16 v[72:73],v1 offset:20480
ds_read_b64_tr_b16 v[74:75],v1 offset:25600
v_mfma_f32_16x16x32_bf16 v[168:171],v[88:91],a[116:119],v[168:171]
ds_read_b64_tr_b16 v[76:77],v1 offset:30720
v_mfma_f32_16x16x32_bf16 v[168:171],v[84:87],a[108:111],v[168:171]
ds_read_b64_tr_b16 v[78:79],v1 offset:35840
v_mfma_f32_16x16x32_bf16 v[168:171],v[80:83],a[100:103],v[168:171]
v_mfma_f32_16x16x32_bf16 v[176:179],v[80:83],a[64:67],v[176:179]
v_mfma_f32_16x16x32_bf16 v[176:179],v[84:87],a[72:75],v[176:179]
buffer_load_dwordx4 v24,s[48:51],s83 offen lds
v_mfma_f32_16x16x32_bf16 v[176:179],v[88:91],a[80:83],v[176:179]
v_mfma_f32_16x16x32_bf16 v[176:179],v[92:95],a[88:91],v[176:179]
v_mfma_f32_16x16x32_bf16 v[184:187],v[92:95],a[120:123],v[184:187]
ds_read_b64_tr_b16 v[92:93],v2 offset:30752
ds_read_b64_tr_b16 v[94:95],v2 offset:35872
v_mfma_f32_16x16x32_bf16 v[184:187],v[88:91],a[112:115],v[184:187]
ds_read_b64_tr_b16 v[88:89],v2 offset:20512
v_mfma_f32_16x16x32_bf16 v[184:187],v[84:87],a[104:107],v[184:187]
buffer_load_dwordx4 a[148:151],v6,s[28:31],s88 offen offset:1024 sc1
v_mfma_f32_16x16x32_bf16 v[184:187],v[80:83],a[96:99],v[184:187]
ds_read_b64_tr_b16 v[80:81],v2 offset:32
ds_read_b64_tr_b16 v[82:83],v2 offset:5152
v_mfma_f32_16x16x32_bf16 v[192:195],v[96:99],a[68:71],v[192:195]
ds_read_b64_tr_b16 v[84:85],v2 offset:10272
v_mfma_f32_16x16x32_bf16 v[192:195],v[100:103],a[76:79],v[192:195]
ds_read_b64_tr_b16 v[86:87],v2 offset:15392
v_mfma_f32_16x16x32_bf16 v[192:195],v[104:107],a[84:87],v[192:195]
ds_read_b64_tr_b16 v[90:91],v2 offset:25632
v_mfma_f32_16x16x32_bf16 v[192:195],v[108:111],a[92:95],v[192:195]
v_mfma_f32_16x16x32_bf16 v[200:203],v[108:111],a[124:127],v[200:203]
buffer_load_dwordx4 v25,s[48:51],s83 offen offset:1024 lds
v_mfma_f32_16x16x32_bf16 v[200:203],v[104:107],a[116:119],v[200:203]
v_mfma_f32_16x16x32_bf16 v[200:203],v[100:103],a[108:111],v[200:203]
v_mfma_f32_16x16x32_bf16 v[200:203],v[96:99],a[100:103],v[200:203]
v_mfma_f32_16x16x32_bf16 v[208:211],v[96:99],a[64:67],v[208:211]
v_mfma_f32_16x16x32_bf16 v[208:211],v[100:103],a[72:75],v[208:211]
buffer_load_dwordx4 a[132:135],v6,s[28:31],s87 offen offset:1024 sc1
v_mfma_f32_16x16x32_bf16 v[208:211],v[104:107],a[80:83],v[208:211]
v_mfma_f32_16x16x32_bf16 v[208:211],v[108:111],a[88:91],v[208:211]
v_mfma_f32_16x16x32_bf16 v[216:219],v[108:111],a[120:123],v[216:219]
ds_read_b64_tr_b16 v[108:109],v1 offset:30784
ds_read_b64_tr_b16 v[110:111],v1 offset:35904
v_mfma_f32_16x16x32_bf16 v[216:219],v[104:107],a[112:115],v[216:219]
ds_read_b64_tr_b16 v[104:105],v1 offset:20544
v_mfma_f32_16x16x32_bf16 v[216:219],v[100:103],a[104:107],v[216:219]
ds_read_b64_tr_b16 v[100:101],v1 offset:10304
v_mfma_f32_16x16x32_bf16 v[216:219],v[96:99],a[96:99],v[216:219]
buffer_load_dwordx4 v26,s[48:51],s83 offen offset:2048 lds
v_mfma_f32_16x16x32_bf16 v[224:227],v[112:115],a[68:71],v[224:227]
ds_read_b64_tr_b16 v[96:97],v1 offset:64
ds_read_b64_tr_b16 v[98:99],v1 offset:5184
v_mfma_f32_16x16x32_bf16 v[224:227],v[116:119],a[76:79],v[224:227]
ds_read_b64_tr_b16 v[102:103],v1 offset:15424
v_mfma_f32_16x16x32_bf16 v[224:227],v[120:123],a[84:87],v[224:227]
ds_read_b64_tr_b16 v[106:107],v1 offset:25664
v_mfma_f32_16x16x32_bf16 v[224:227],v[124:127],a[92:95],v[224:227]
v_mfma_f32_16x16x32_bf16 v[232:235],v[124:127],a[124:127],v[232:235]
v_mfma_f32_16x16x32_bf16 v[232:235],v[120:123],a[116:119],v[232:235]
buffer_load_dwordx4 a[136:139],v6,s[28:31],s87 offen offset:2048 sc1
v_mfma_f32_16x16x32_bf16 v[232:235],v[116:119],a[108:111],v[232:235]
v_mfma_f32_16x16x32_bf16 v[232:235],v[112:115],a[100:103],v[232:235]
v_mfma_f32_16x16x32_bf16 v[240:243],v[112:115],a[64:67],v[240:243]
v_mfma_f32_16x16x32_bf16 v[240:243],v[116:119],a[72:75],v[240:243]
v_mfma_f32_16x16x32_bf16 v[240:243],v[120:123],a[80:83],v[240:243]
v_mfma_f32_16x16x32_bf16 v[240:243],v[124:127],a[88:91],v[240:243]
buffer_load_dwordx4 v27,s[48:51],s83 offen offset:3072 lds
v_mfma_f32_16x16x32_bf16 v[248:251],v[124:127],a[120:123],v[248:251]
s_add_u32 m0,s86,45056
ds_read_b64_tr_b16 v[124:125],v2 offset:30816
ds_read_b64_tr_b16 v[126:127],v2 offset:35936
v_mfma_f32_16x16x32_bf16 v[248:251],v[120:123],a[112:115],v[248:251]
ds_read_b64_tr_b16 v[120:121],v2 offset:20576
v_mfma_f32_16x16x32_bf16 v[248:251],v[116:119],a[104:107],v[248:251]
ds_read_b64_tr_b16 v[116:117],v2 offset:10336
v_mfma_f32_16x16x32_bf16 v[248:251],v[112:115],a[96:99],v[248:251]
s_waitcnt vmcnt(21) lgkmcnt(4)
ds_read_b64_tr_b16 v[112:113],v2 offset:96
v_mfma_f32_16x16x32_bf16 a[192:195],v[64:67],v[32:35],a[192:195]
s_waitcnt vmcnt(13)
ds_read_b64_tr_b16 v[114:115],v2 offset:5216
ds_read_b64_tr_b16 v[118:119],v2 offset:15456
v_mfma_f32_16x16x32_bf16 a[192:195],v[68:71],v[36:39],a[192:195]
buffer_load_dwordx4 v7,s[48:51],s83 offen lds
v_mfma_f32_16x16x32_bf16 a[192:195],v[72:75],v[40:43],a[192:195]
s_add_u32 m0,s86,46080
s_add_u32 s83,s64,3
ds_read_b64_tr_b16 v[122:123],v2 offset:25696
)A"
R"A(v_mfma_f32_16x16x32_bf16 a[192:195],v[76:79],v[44:47],a[192:195]
s_and_b32 s83,s83,3
s_lshl_b32 s83,s83,1
s_or_b32 s83,s83,17
v_mfma_f32_16x16x32_bf16 a[196:199],v[76:79],v[180:183],a[196:199]
s_mul_i32 s83,s83,163840
s_add_u32 s83,s83,s85
v_mfma_f32_16x16x32_bf16 a[196:199],v[72:75],v[164:167],a[196:199]
v_mfma_f32_16x16x32_bf16 a[196:199],v[68:71],v[148:151],a[196:199]
buffer_load_dwordx4 a[152:155],v6,s[28:31],s88 offen offset:2048 sc1
v_mfma_f32_16x16x32_bf16 a[196:199],v[64:67],v[132:135],a[196:199]
v_mfma_f32_16x16x32_bf16 a[200:203],v[64:67],v[196:199],a[200:203]
v_mfma_f32_16x16x32_bf16 a[200:203],v[68:71],v[212:215],a[200:203]
v_mfma_f32_16x16x32_bf16 a[200:203],v[72:75],v[228:231],a[200:203]
s_waitcnt vmcnt(13)
v_mfma_f32_16x16x32_bf16 a[200:203],v[76:79],v[244:247],a[200:203]
s_waitcnt vmcnt(12)
v_mfma_f32_16x16x32_bf16 a[204:207],v[76:79],v[188:191],a[204:207]
buffer_load_dwordx4 v24,s[48:51],s84 offen lds
v_mfma_f32_16x16x32_bf16 a[204:207],v[72:75],v[172:175],a[204:207]
ds_read_b64_tr_b16 v[72:73],v1 offset:20608
ds_read_b64_tr_b16 v[74:75],v1 offset:25728
v_mfma_f32_16x16x32_bf16 a[204:207],v[68:71],v[156:159],a[204:207]
ds_read_b64_tr_b16 v[68:69],v1 offset:10368
v_mfma_f32_16x16x32_bf16 a[204:207],v[64:67],v[140:143],a[204:207]
ds_read_b64_tr_b16 v[64:65],v1 offset:128
v_mfma_f32_16x16x32_bf16 a[208:211],v[80:83],v[32:35],a[208:211]
ds_read_b64_tr_b16 v[66:67],v1 offset:5248
v_mfma_f32_16x16x32_bf16 a[208:211],v[84:87],v[36:39],a[208:211]
buffer_load_dwordx4 a[168:171],v6,s[28:31],s89 offen offset:2048 sc1
v_mfma_f32_16x16x32_bf16 a[208:211],v[88:91],v[40:43],a[208:211]
ds_read_b64_tr_b16 v[70:71],v1 offset:15488
ds_read_b64_tr_b16 v[76:77],v1 offset:30848
v_mfma_f32_16x16x32_bf16 a[208:211],v[92:95],v[44:47],a[208:211]
ds_read_b64_tr_b16 v[78:79],v1 offset:35968
v_mfma_f32_16x16x32_bf16 a[212:215],v[92:95],v[180:183],a[212:215]
v_mfma_f32_16x16x32_bf16 a[212:215],v[88:91],v[164:167],a[212:215]
v_mfma_f32_16x16x32_bf16 a[212:215],v[84:87],v[148:151],a[212:215]
v_mfma_f32_16x16x32_bf16 a[212:215],v[80:83],v[132:135],a[212:215]
buffer_load_dwordx4 v25,s[48:51],s84 offen offset:1024 lds
v_mfma_f32_16x16x32_bf16 a[216:219],v[80:83],v[196:199],a[216:219]
v_mfma_f32_16x16x32_bf16 a[216:219],v[84:87],v[212:215],a[216:219]
v_mfma_f32_16x16x32_bf16 a[216:219],v[88:91],v[228:231],a[216:219]
v_mfma_f32_16x16x32_bf16 a[216:219],v[92:95],v[244:247],a[216:219]
v_mfma_f32_16x16x32_bf16 a[220:223],v[92:95],v[188:191],a[220:223]
buffer_load_dwordx4 a[184:187],v6,s[28:31],s90 offen offset:2048 sc1
v_mfma_f32_16x16x32_bf16 a[220:223],v[88:91],v[172:175],a[220:223]
ds_read_b64_tr_b16 v[88:89],v2 offset:20640
ds_read_b64_tr_b16 v[90:91],v2 offset:25760
v_mfma_f32_16x16x32_bf16 a[220:223],v[84:87],v[156:159],a[220:223]
ds_read_b64_tr_b16 v[84:85],v2 offset:10400
v_mfma_f32_16x16x32_bf16 a[220:223],v[80:83],v[140:143],a[220:223]
ds_read_b64_tr_b16 v[80:81],v2 offset:160
v_mfma_f32_16x16x32_bf16 a[224:227],v[96:99],v[32:35],a[224:227]
ds_read_b64_tr_b16 v[82:83],v2 offset:5280
v_mfma_f32_16x16x32_bf16 a[224:227],v[100:103],v[36:39],a[224:227]
ds_read_b64_tr_b16 v[86:87],v2 offset:15520
ds_read_b64_tr_b16 v[92:93],v2 offset:30880
v_mfma_f32_16x16x32_bf16 a[224:227],v[104:107],v[40:43],a[224:227]
buffer_load_dwordx4 v26,s[48:51],s84 offen offset:2048 lds
v_mfma_f32_16x16x32_bf16 a[224:227],v[108:111],v[44:47],a[224:227]
ds_read_b64_tr_b16 v[94:95],v2 offset:36000
v_mfma_f32_16x16x32_bf16 a[228:231],v[108:111],v[180:183],a[228:231]
v_mfma_f32_16x16x32_bf16 a[228:231],v[104:107],v[164:167],a[228:231]
v_mfma_f32_16x16x32_bf16 a[228:231],v[100:103],v[148:151],a[228:231]
v_mfma_f32_16x16x32_bf16 a[228:231],v[96:99],v[132:135],a[228:231]
v_mfma_f32_16x16x32_bf16 a[232:235],v[96:99],v[196:199],a[232:235]
buffer_load_dwordx4 v27,s[48:51],s84 offen offset:3072 lds
v_mfma_f32_16x16x32_bf16 a[232:235],v[100:103],v[212:215],a[232:235]
s_add_u32 m0,s86,50176
v_mfma_f32_16x16x32_bf16 a[232:235],v[104:107],v[228:231],a[232:235]
v_mfma_f32_16x16x32_bf16 a[232:235],v[108:111],v[244:247],a[232:235]
v_mfma_f32_16x16x32_bf16 a[236:239],v[108:111],v[188:191],a[236:239]
ds_read_b64_tr_b16 v[108:109],v1 offset:30912
ds_read_b64_tr_b16 v[110:111],v1 offset:36032
v_mfma_f32_16x16x32_bf16 a[236:239],v[104:107],v[172:175],a[236:239]
buffer_load_dwordx4 a[188:191],v28,s[28:31],s90 offen offset:3072 sc1
v_mfma_f32_16x16x32_bf16 a[236:239],v[100:103],v[156:159],a[236:239]
ds_read_b64_tr_b16 v[100:101],v1 offset:10432
ds_read_b64_tr_b16 v[102:103],v1 offset:15552
v_mfma_f32_16x16x32_bf16 a[236:239],v[96:99],v[140:143],a[236:239]
s_waitcnt lgkmcnt(4)
ds_read_b64_tr_b16 v[96:97],v1 offset:192
v_mfma_f32_16x16x32_bf16 a[240:243],v[112:115],v[32:35],a[240:243]
ds_read_b64_tr_b16 v[98:99],v1 offset:5312
v_mfma_f32_16x16x32_bf16 a[240:243],v[116:119],v[36:39],a[240:243]
ds_read_b64_tr_b16 v[104:105],v1 offset:20672
v_mfma_f32_16x16x32_bf16 a[240:243],v[120:123],v[40:43],a[240:243]
ds_read_b64_tr_b16 v[106:107],v1 offset:25792
v_mfma_f32_16x16x32_bf16 a[240:243],v[124:127],v[44:47],a[240:243]
buffer_load_dwordx4 v7,s[48:51],s84 offen lds
v_mfma_f32_16x16x32_bf16 a[244:247],v[124:127],v[180:183],a[244:247]
s_add_u32 m0,s86,0
s_add_u32 s84,s83,20480
v_mfma_f32_16x16x32_bf16 a[244:247],v[120:123],v[164:167],a[244:247]
v_mfma_f32_16x16x32_bf16 a[244:247],v[116:119],v[148:151],a[244:247]
v_mfma_f32_16x16x32_bf16 a[244:247],v[112:115],v[132:135],a[244:247]
v_mfma_f32_16x16x32_bf16 a[248:251],v[112:115],v[196:199],a[248:251]
buffer_load_dwordx4 a[172:175],v28,s[28:31],s89 offen offset:3072 sc1
v_mfma_f32_16x16x32_bf16 a[248:251],v[116:119],v[212:215],a[248:251]
v_mfma_f32_16x16x32_bf16 a[248:251],v[120:123],v[228:231],a[248:251]
v_mfma_f32_16x16x32_bf16 a[248:251],v[124:127],v[244:247],a[248:251]
v_mfma_f32_16x16x32_bf16 a[252:255],v[124:127],v[188:191],a[252:255]
ds_read_b64_tr_b16 v[124:125],v2 offset:30944
ds_read_b64_tr_b16 v[126:127],v2 offset:36064
v_mfma_f32_16x16x32_bf16 a[252:255],v[120:123],v[172:175],a[252:255]
ds_read_b64_tr_b16 v[120:121],v2 offset:20704
v_mfma_f32_16x16x32_bf16 a[252:255],v[116:119],v[156:159],a[252:255]
buffer_load_dwordx4 v24,s[48:51],s83 offen lds
v_mfma_f32_16x16x32_bf16 a[252:255],v[112:115],v[140:143],a[252:255]
ds_read_b64_tr_b16 v[112:113],v2 offset:224
ds_read_b64_tr_b16 v[114:115],v2 offset:5344
v_mfma_f32_16x16x32_bf16 a[0:3],v[64:67],v[32:35],a[0:3]
ds_read_b64_tr_b16 v[116:117],v2 offset:10464
v_mfma_f32_16x16x32_bf16 a[0:3],v[68:71],v[36:39],a[0:3]
ds_read_b64_tr_b16 v[118:119],v2 offset:15584
v_mfma_f32_16x16x32_bf16 a[0:3],v[72:75],v[40:43],a[0:3]
ds_read_b64_tr_b16 v[122:123],v2 offset:25824
v_mfma_f32_16x16x32_bf16 a[0:3],v[76:79],v[44:47],a[0:3]
buffer_load_dwordx4 a[156:159],v28,s[28:31],s88 offen offset:3072 sc1
v_mfma_f32_16x16x32_bf16 a[4:7],v[76:79],v[180:183],a[4:7]
v_mfma_f32_16x16x32_bf16 a[4:7],v[72:75],v[164:167],a[4:7]
v_mfma_f32_16x16x32_bf16 a[4:7],v[68:71],v[148:151],a[4:7]
v_mfma_f32_16x16x32_bf16 a[4:7],v[64:67],v[132:135],a[4:7]
v_mfma_f32_16x16x32_bf16 a[8:11],v[64:67],v[196:199],a[8:11]
v_mfma_f32_16x16x32_bf16 a[8:11],v[68:71],v[212:215],a[8:11]
buffer_load_dwordx4 v25,s[48:51],s83 offen offset:1024 lds
v_mfma_f32_16x16x32_bf16 a[8:11],v[72:75],v[228:231],a[8:11]
v_mfma_f32_16x16x32_bf16 a[8:11],v[76:79],v[244:247],a[8:11]
v_mfma_f32_16x16x32_bf16 a[12:15],v[76:79],v[188:191],a[12:15]
ds_read_b64_tr_b16 v[76:77],v1 offset:30976
ds_read_b64_tr_b16 v[78:79],v1 offset:36096
v_mfma_f32_16x16x32_bf16 a[12:15],v[72:75],v[172:175],a[12:15]
ds_read_b64_tr_b16 v[72:73],v1 offset:20736
v_mfma_f32_16x16x32_bf16 a[12:15],v[68:71],v[156:159],a[12:15]
ds_read_b64_tr_b16 v[68:69],v1 offset:10496
v_mfma_f32_16x16x32_bf16 a[12:15],v[64:67],v[140:143],a[12:15]
buffer_load_dwordx4 v26,s[48:51],s83 offen offset:2048 lds
v_mfma_f32_16x16x32_bf16 a[16:19],v[80:83],v[32:35],a[16:19]
ds_read_b64_tr_b16 v[64:65],v1 offset:256
ds_read_b64_tr_b16 v[66:67],v1 offset:5376
v_mfma_f32_16x16x32_bf16 a[16:19],v[84:87],v[36:39],a[16:19]
ds_read_b64_tr_b16 v[70:71],v1 offset:15616
v_mfma_f32_16x16x32_bf16 a[16:19],v[88:91],v[40:43],a[16:19]
ds_read_b64_tr_b16 v[74:75],v1 offset:25856
v_add_u32 v1,0xffff6000,v1
v_mfma_f32_16x16x32_bf16 a[16:19],v[92:95],v[44:47],a[16:19]
v_mfma_f32_16x16x32_bf16 a[20:23],v[92:95],v[180:183],a[20:23]
buffer_load_dwordx4 a[140:143],v28,s[28:31],s87 offen offset:3072 sc1
v_mfma_f32_16x16x32_bf16 a[20:23],v[88:91],v[164:167],a[20:23]
s_add_u32 s87,s64,2
s_and_b32 s87,s87,3
s_lshl_b32 s87,s87,1
v_mfma_f32_16x16x32_bf16 a[20:23],v[84:87],v[148:151],a[20:23]
s_or_b32 s87,s87,1
s_lshl_b32 s87,s87,16
s_add_u32 s88,s87,4096
v_mfma_f32_16x16x32_bf16 a[20:23],v[80:83],v[132:135],a[20:23]
s_add_u32 s89,s87,8192
s_add_u32 s90,s87,12288
v_mfma_f32_16x16x32_bf16 a[24:27],v[80:83],v[196:199],a[24:27]
v_mfma_f32_16x16x32_bf16 a[24:27],v[84:87],v[212:215],a[24:27]
v_mfma_f32_16x16x32_bf16 a[24:27],v[88:91],v[228:231],a[24:27]
buffer_load_dwordx4 v27,s[48:51],s83 offen offset:3072 lds
v_mfma_f32_16x16x32_bf16 a[24:27],v[92:95],v[244:247],a[24:27]
s_add_u32 m0,s86,4096
v_mfma_f32_16x16x32_bf16 a[28:31],v[92:95],v[188:191],a[28:31]
ds_read_b64_tr_b16 v[92:93],v2 offset:31008
ds_read_b64_tr_b16 v[94:95],v2 offset:36128
v_mfma_f32_16x16x32_bf16 a[28:31],v[88:91],v[172:175],a[28:31]
ds_read_b64_tr_b16 v[88:89],v2 offset:20768
v_mfma_f32_16x16x32_bf16 a[28:31],v[84:87],v[156:159],a[28:31]
ds_read_b64_tr_b16 v[84:85],v2 offset:10528
v_mfma_f32_16x16x32_bf16 a[28:31],v[80:83],v[140:143],a[28:31]
s_waitcnt lgkmcnt(4)
ds_read_b64_tr_b16 v[80:81],v2 offset:288
v_mfma_f32_16x16x32_bf16 v[128:131],v[96:99],v[32:35],v[128:131]
buffer_load_dwordx4 v7,s[48:51],s83 offen lds
v_mfma_f32_16x16x32_bf16 v[128:131],v[100:103],v[36:39],v[128:131]
s_add_u32 m0,s86,5120
ds_read_b64_tr_b16 v[82:83],v2 offset:5408
ds_read_b64_tr_b16 v[86:87],v2 offset:15648
v_mfma_f32_16x16x32_bf16 v[128:131],v[104:107],v[40:43],v[128:131]
s_add_u32 s83,s64,3
ds_read_b64_tr_b16 v[90:91],v2 offset:25888
v_add_u32 v2,0xffff6000,v2
v_mfma_f32_16x16x32_bf16 v[128:131],v[108:111],v[44:47],v[128:131]
s_and_b32 s83,s83,3
s_lshl_b32 s83,s83,1
s_or_b32 s83,s83,1
v_mfma_f32_16x16x32_bf16 v[136:139],v[108:111],v[180:183],v[136:139]
s_mul_i32 s83,s83,163840
s_add_u32 s83,s83,s85
v_mfma_f32_16x16x32_bf16 v[136:139],v[104:107],v[164:167],v[136:139]
v_mfma_f32_16x16x32_bf16 v[136:139],v[100:103],v[148:151],v[136:139]
buffer_load_dwordx4 v24,s[48:51],s84 offen lds
v_mfma_f32_16x16x32_bf16 v[136:139],v[96:99],v[132:135],v[136:139]
v_mfma_f32_16x16x32_bf16 v[144:147],v[96:99],v[196:199],v[144:147]
v_mfma_f32_16x16x32_bf16 v[144:147],v[100:103],v[212:215],v[144:147]
v_mfma_f32_16x16x32_bf16 v[144:147],v[104:107],v[228:231],v[144:147]
v_mfma_f32_16x16x32_bf16 v[144:147],v[108:111],v[244:247],v[144:147]
v_mfma_f32_16x16x32_bf16 v[152:155],v[108:111],v[188:191],v[152:155]
buffer_load_dwordx4 v25,s[48:51],s84 offen offset:1024 lds
v_mfma_f32_16x16x32_bf16 v[152:155],v[104:107],v[172:175],v[152:155]
ds_read_b64_tr_b16 v[104:105],v1 offset:20480
ds_read_b64_tr_b16 v[106:107],v1 offset:25600
v_mfma_f32_16x16x32_bf16 v[152:155],v[100:103],v[156:159],v[152:155]
ds_read_b64_tr_b16 v[100:101],v1 offset:10240
v_mfma_f32_16x16x32_bf16 v[152:155],v[96:99],v[140:143],v[152:155]
ds_read_b64_tr_b16 v[96:97],v1
v_mfma_f32_16x16x32_bf16 v[160:163],v[112:115],v[32:35],v[160:163]
ds_read_b64_tr_b16 v[98:99],v1 offset:5120
v_mfma_f32_16x16x32_bf16 v[160:163],v[116:119],v[36:39],v[160:163]
ds_read_b64_tr_b16 v[102:103],v1 offset:15360
ds_read_b64_tr_b16 v[108:109],v1 offset:30720
v_mfma_f32_16x16x32_bf16 v[160:163],v[120:123],v[40:43],v[160:163]
buffer_load_dwordx4 v26,s[48:51],s84 offen offset:2048 lds
v_mfma_f32_16x16x32_bf16 v[160:163],v[124:127],v[44:47],v[160:163]
ds_read_b64_tr_b16 v[110:111],v1 offset:35840
v_mfma_f32_16x16x32_bf16 v[168:171],v[124:127],v[180:183],v[168:171]
v_mfma_f32_16x16x32_bf16 v[168:171],v[120:123],v[164:167],v[168:171]
v_mfma_f32_16x16x32_bf16 v[168:171],v[116:119],v[148:151],v[168:171]
v_mfma_f32_16x16x32_bf16 v[168:171],v[112:115],v[132:135],v[168:171]
)A"
R"A(v_mfma_f32_16x16x32_bf16 v[176:179],v[112:115],v[196:199],v[176:179]
buffer_load_dwordx4 v27,s[48:51],s84 offen offset:3072 lds
v_mfma_f32_16x16x32_bf16 v[176:179],v[116:119],v[212:215],v[176:179]
s_add_u32 m0,s86,9216
v_mfma_f32_16x16x32_bf16 v[176:179],v[120:123],v[228:231],v[176:179]
v_mfma_f32_16x16x32_bf16 v[176:179],v[124:127],v[244:247],v[176:179]
v_mfma_f32_16x16x32_bf16 v[184:187],v[124:127],v[188:191],v[184:187]
ds_read_b64_tr_b16 v[124:125],v2 offset:30752
ds_read_b64_tr_b16 v[126:127],v2 offset:35872
v_mfma_f32_16x16x32_bf16 v[184:187],v[120:123],v[172:175],v[184:187]
ds_read_b64_tr_b16 v[120:121],v2 offset:20512
v_mfma_f32_16x16x32_bf16 v[184:187],v[116:119],v[156:159],v[184:187]
buffer_load_dwordx4 v7,s[48:51],s84 offen lds
v_mfma_f32_16x16x32_bf16 v[184:187],v[112:115],v[140:143],v[184:187]
s_add_u32 m0,s86,122880
ds_read_b64_tr_b16 v[112:113],v2 offset:32
ds_read_b64_tr_b16 v[114:115],v2 offset:5152
v_mfma_f32_16x16x32_bf16 v[192:195],v[64:67],v[32:35],v[192:195]
s_add_u32 s84,s83,20480
ds_read_b64_tr_b16 v[116:117],v2 offset:10272
v_mfma_f32_16x16x32_bf16 v[192:195],v[68:71],v[36:39],v[192:195]
ds_read_b64_tr_b16 v[118:119],v2 offset:15392
v_mfma_f32_16x16x32_bf16 v[192:195],v[72:75],v[40:43],v[192:195]
ds_read_b64_tr_b16 v[122:123],v2 offset:25632
v_mfma_f32_16x16x32_bf16 v[192:195],v[76:79],v[44:47],v[192:195]
s_nop 0
L_pd16_%=:
s_cbranch_execz L_trtg2F_%=
buffer_load_dwordx4 a[68:71],v6,s[28:31],s87 offen sc1
v_mfma_f32_16x16x32_bf16 v[200:203],v[76:79],v[180:183],v[200:203]
v_mfma_f32_16x16x32_bf16 v[200:203],v[72:75],v[164:167],v[200:203]
v_mfma_f32_16x16x32_bf16 v[200:203],v[68:71],v[148:151],v[200:203]
v_mfma_f32_16x16x32_bf16 v[200:203],v[64:67],v[132:135],v[200:203]
v_mfma_f32_16x16x32_bf16 v[208:211],v[64:67],v[196:199],v[208:211]
buffer_load_dwordx4 a[100:103],v6,s[28:31],s87 offen offset:1024 sc1
v_mfma_f32_16x16x32_bf16 v[208:211],v[68:71],v[212:215],v[208:211]
v_mfma_f32_16x16x32_bf16 v[208:211],v[72:75],v[228:231],v[208:211]
v_mfma_f32_16x16x32_bf16 v[208:211],v[76:79],v[244:247],v[208:211]
v_mfma_f32_16x16x32_bf16 v[216:219],v[76:79],v[188:191],v[216:219]
ds_read_b64_tr_b16 v[76:77],v1 offset:30784
ds_read_b64_tr_b16 v[78:79],v1 offset:35904
v_mfma_f32_16x16x32_bf16 v[216:219],v[72:75],v[172:175],v[216:219]
buffer_load_dwordx2 v[4:5],v3,s[44:47],s91 offen offset:1280 sc1
v_mfma_f32_16x16x32_bf16 v[216:219],v[68:71],v[156:159],v[216:219]
ds_read_b64_tr_b16 v[68:69],v1 offset:10304
ds_read_b64_tr_b16 v[70:71],v1 offset:15424
v_mfma_f32_16x16x32_bf16 v[216:219],v[64:67],v[140:143],v[216:219]
s_waitcnt lgkmcnt(4)
ds_read_b64_tr_b16 v[64:65],v1 offset:64
v_mfma_f32_16x16x32_bf16 v[224:227],v[80:83],v[32:35],v[224:227]
ds_read_b64_tr_b16 v[66:67],v1 offset:5184
v_mfma_f32_16x16x32_bf16 v[224:227],v[84:87],v[36:39],v[224:227]
ds_read_b64_tr_b16 v[72:73],v1 offset:20544
v_mfma_f32_16x16x32_bf16 v[224:227],v[88:91],v[40:43],v[224:227]
buffer_load_dwordx4 a[64:67],v6,s[28:31],s87 offen offset:2048 sc1
v_mfma_f32_16x16x32_bf16 v[224:227],v[92:95],v[44:47],v[224:227]
ds_read_b64_tr_b16 v[74:75],v1 offset:25664
v_mfma_f32_16x16x32_bf16 v[232:235],v[92:95],v[180:183],v[232:235]
v_mfma_f32_16x16x32_bf16 v[232:235],v[88:91],v[164:167],v[232:235]
v_mfma_f32_16x16x32_bf16 v[232:235],v[84:87],v[148:151],v[232:235]
v_mfma_f32_16x16x32_bf16 v[232:235],v[80:83],v[132:135],v[232:235]
buffer_load_dwordx4 a[76:79],v6,s[28:31],s88 offen sc1
v_mfma_f32_16x16x32_bf16 v[240:243],v[80:83],v[196:199],v[240:243]
v_mfma_f32_16x16x32_bf16 v[240:243],v[84:87],v[212:215],v[240:243]
v_mfma_f32_16x16x32_bf16 v[240:243],v[88:91],v[228:231],v[240:243]
v_mfma_f32_16x16x32_bf16 v[240:243],v[92:95],v[244:247],v[240:243]
v_mfma_f32_16x16x32_bf16 v[248:251],v[92:95],v[188:191],v[248:251]
buffer_load_dwordx4 a[96:99],v28,s[28:31],s87 offen offset:3072 sc1
v_mfma_f32_16x16x32_bf16 v[248:251],v[88:91],v[172:175],v[248:251]
s_add_u32 s87,s64,3
ds_read_b64_tr_b16 v[88:89],v2 offset:20576
ds_read_b64_tr_b16 v[90:91],v2 offset:25696
v_mfma_f32_16x16x32_bf16 v[248:251],v[84:87],v[156:159],v[248:251]
s_and_b32 s87,s87,3
s_lshl_b32 s87,s87,1
ds_read_b64_tr_b16 v[84:85],v2 offset:10336
v_mfma_f32_16x16x32_bf16 v[248:251],v[80:83],v[140:143],v[248:251]
s_or_b32 s87,s87,17
s_lshl_b32 s87,s87,16
ds_read_b64_tr_b16 v[80:81],v2 offset:96
v_mfma_f32_16x16x32_bf16 a[192:195],v[96:99],a[128:131],a[192:195]
ds_read_b64_tr_b16 v[82:83],v2 offset:5216
v_mfma_f32_16x16x32_bf16 a[192:195],v[100:103],a[144:147],a[192:195]
buffer_load_dwordx4 a[72:75],v6,s[28:31],s88 offen offset:2048 sc1
v_mfma_f32_16x16x32_bf16 a[192:195],v[104:107],a[160:163],a[192:195]
s_waitcnt vmcnt(37)
ds_read_b64_tr_b16 v[86:87],v2 offset:15456
ds_read_b64_tr_b16 v[92:93],v2 offset:30816
v_mfma_f32_16x16x32_bf16 a[192:195],v[108:111],a[176:179],a[192:195]
ds_read_b64_tr_b16 v[94:95],v2 offset:35936
v_mfma_f32_16x16x32_bf16 a[196:199],v[108:111],a[180:183],a[196:199]
v_mfma_f32_16x16x32_bf16 a[196:199],v[104:107],a[164:167],a[196:199]
s_waitcnt vmcnt(33)
v_mfma_f32_16x16x32_bf16 a[196:199],v[100:103],a[148:151],a[196:199]
buffer_load_dwordx4 a[108:111],v6,s[28:31],s88 offen offset:1024 sc1
v_mfma_f32_16x16x32_bf16 a[196:199],v[96:99],a[132:135],a[196:199]
s_waitcnt vmcnt(32)
v_mfma_f32_16x16x32_bf16 a[200:203],v[96:99],a[136:139],a[200:203]
s_waitcnt vmcnt(29)
v_mfma_f32_16x16x32_bf16 a[200:203],v[100:103],a[152:155],a[200:203]
s_waitcnt vmcnt(27)
v_mfma_f32_16x16x32_bf16 a[200:203],v[104:107],a[168:171],a[200:203]
s_waitcnt vmcnt(22)
v_mfma_f32_16x16x32_bf16 a[200:203],v[108:111],a[184:187],a[200:203]
buffer_load_dwordx4 a[84:87],v6,s[28:31],s89 offen sc1
v_mfma_f32_16x16x32_bf16 a[204:207],v[108:111],a[188:191],a[204:207]
s_waitcnt vmcnt(21)
ds_read_b64_tr_b16 v[108:109],v1 offset:30848
ds_read_b64_tr_b16 v[110:111],v1 offset:35968
v_mfma_f32_16x16x32_bf16 a[204:207],v[104:107],a[172:175],a[204:207]
s_waitcnt vmcnt(19)
ds_read_b64_tr_b16 v[104:105],v1 offset:20608
v_mfma_f32_16x16x32_bf16 a[204:207],v[100:103],a[156:159],a[204:207]
s_waitcnt vmcnt(16)
ds_read_b64_tr_b16 v[100:101],v1 offset:10368
v_mfma_f32_16x16x32_bf16 a[204:207],v[96:99],a[140:143],a[204:207]
ds_read_b64_tr_b16 v[96:97],v1 offset:128
v_mfma_f32_16x16x32_bf16 a[208:211],v[112:115],a[128:131],a[208:211]
buffer_load_dwordx4 a[104:107],v28,s[28:31],s88 offen offset:3072 sc1
v_mfma_f32_16x16x32_bf16 a[208:211],v[116:119],a[144:147],a[208:211]
s_add_u32 s88,s87,4096
ds_read_b64_tr_b16 v[98:99],v1 offset:5248
ds_read_b64_tr_b16 v[102:103],v1 offset:15488
v_mfma_f32_16x16x32_bf16 a[208:211],v[120:123],a[160:163],a[208:211]
ds_read_b64_tr_b16 v[106:107],v1 offset:25728
v_mfma_f32_16x16x32_bf16 a[208:211],v[124:127],a[176:179],a[208:211]
v_mfma_f32_16x16x32_bf16 a[212:215],v[124:127],a[180:183],a[212:215]
v_mfma_f32_16x16x32_bf16 a[212:215],v[120:123],a[164:167],a[212:215]
buffer_load_dwordx4 a[80:83],v6,s[28:31],s89 offen offset:2048 sc1
v_mfma_f32_16x16x32_bf16 a[212:215],v[116:119],a[148:151],a[212:215]
v_mfma_f32_16x16x32_bf16 a[212:215],v[112:115],a[132:135],a[212:215]
v_mfma_f32_16x16x32_bf16 a[216:219],v[112:115],a[136:139],a[216:219]
v_mfma_f32_16x16x32_bf16 a[216:219],v[116:119],a[152:155],a[216:219]
v_mfma_f32_16x16x32_bf16 a[216:219],v[120:123],a[168:171],a[216:219]
buffer_load_dwordx4 a[116:119],v6,s[28:31],s89 offen offset:1024 sc1
v_mfma_f32_16x16x32_bf16 a[216:219],v[124:127],a[184:187],a[216:219]
v_mfma_f32_16x16x32_bf16 a[220:223],v[124:127],a[188:191],a[220:223]
ds_read_b64_tr_b16 v[124:125],v2 offset:30880
ds_read_b64_tr_b16 v[126:127],v2 offset:36000
v_mfma_f32_16x16x32_bf16 a[220:223],v[120:123],a[172:175],a[220:223]
ds_read_b64_tr_b16 v[120:121],v2 offset:20640
v_mfma_f32_16x16x32_bf16 a[220:223],v[116:119],a[156:159],a[220:223]
s_waitcnt lgkmcnt(3)
ds_read_b64_tr_b16 v[116:117],v2 offset:10400
v_mfma_f32_16x16x32_bf16 a[220:223],v[112:115],a[140:143],a[220:223]
buffer_load_dwordx4 a[92:95],v6,s[28:31],s90 offen sc1
v_mfma_f32_16x16x32_bf16 a[224:227],v[64:67],a[128:131],a[224:227]
ds_read_b64_tr_b16 v[112:113],v2 offset:160
ds_read_b64_tr_b16 v[114:115],v2 offset:5280
v_mfma_f32_16x16x32_bf16 a[224:227],v[68:71],a[144:147],a[224:227]
ds_read_b64_tr_b16 v[118:119],v2 offset:15520
v_mfma_f32_16x16x32_bf16 a[224:227],v[72:75],a[160:163],a[224:227]
ds_read_b64_tr_b16 v[122:123],v2 offset:25760
v_mfma_f32_16x16x32_bf16 a[224:227],v[76:79],a[176:179],a[224:227]
v_mfma_f32_16x16x32_bf16 a[228:231],v[76:79],a[180:183],a[228:231]
buffer_load_dwordx4 a[112:115],v28,s[28:31],s89 offen offset:3072 sc1
v_mfma_f32_16x16x32_bf16 a[228:231],v[72:75],a[164:167],a[228:231]
s_add_u32 s89,s87,8192
v_mfma_f32_16x16x32_bf16 a[228:231],v[68:71],a[148:151],a[228:231]
v_mfma_f32_16x16x32_bf16 a[228:231],v[64:67],a[132:135],a[228:231]
v_mfma_f32_16x16x32_bf16 a[232:235],v[64:67],a[136:139],a[232:235]
v_mfma_f32_16x16x32_bf16 a[232:235],v[68:71],a[152:155],a[232:235]
buffer_load_dwordx4 a[124:127],v6,s[28:31],s90 offen offset:1024 sc1
v_mfma_f32_16x16x32_bf16 a[232:235],v[72:75],a[168:171],a[232:235]
s_waitcnt vmcnt(12)
v_cmp_ne_u32 s[56:57],s52,v4
v_cmp_ne_u32 s[58:59],s53,v5
v_mfma_f32_16x16x32_bf16 a[232:235],v[76:79],a[184:187],a[232:235]
s_or_b64 s[56:57],s[56:57],s[58:59]
s_cbranch_scc1 L_fr3_%=
L_fo3_%=:
s_lshl_b32 s91,s64,6
v_mfma_f32_16x16x32_bf16 a[236:239],v[76:79],a[188:191],a[236:239]
ds_read_b64_tr_b16 v[76:77],v1 offset:30912
ds_read_b64_tr_b16 v[78:79],v1 offset:36032
v_mfma_f32_16x16x32_bf16 a[236:239],v[72:75],a[172:175],a[236:239]
ds_read_b64_tr_b16 v[72:73],v1 offset:20672
v_bfe_u32 v3,v6,4,2
v_mfma_f32_16x16x32_bf16 a[236:239],v[68:71],a[156:159],a[236:239]
buffer_load_dwordx4 v[32:35],v6,s[28:31],s87 offen sc1
v_mfma_f32_16x16x32_bf16 a[236:239],v[64:67],a[140:143],a[236:239]
ds_read_b64_tr_b16 v[64:65],v1 offset:192
ds_read_b64_tr_b16 v[66:67],v1 offset:5312
v_mfma_f32_16x16x32_bf16 a[240:243],v[80:83],a[128:131],a[240:243]
ds_read_b64_tr_b16 v[68:69],v1 offset:10432
v_min_u32 v3,2,v3
v_mfma_f32_16x16x32_bf16 a[240:243],v[84:87],a[144:147],a[240:243]
ds_read_b64_tr_b16 v[70:71],v1 offset:15552
v_add3_u32 v3,v3,s63,1
v_mfma_f32_16x16x32_bf16 a[240:243],v[88:91],a[160:163],a[240:243]
ds_read_b64_tr_b16 v[74:75],v1 offset:25792
v_and_b32 v3,3,v3
v_mfma_f32_16x16x32_bf16 a[240:243],v[92:95],a[176:179],a[240:243]
buffer_load_dwordx4 a[88:91],v6,s[28:31],s90 offen offset:2048 sc1
v_mfma_f32_16x16x32_bf16 a[244:247],v[92:95],a[180:183],a[244:247]
v_lshlrev_b32 v3,8,v3
v_mfma_f32_16x16x32_bf16 a[244:247],v[88:91],a[164:167],a[244:247]
v_mfma_f32_16x16x32_bf16 a[244:247],v[84:87],a[148:151],a[244:247]
v_mfma_f32_16x16x32_bf16 a[244:247],v[80:83],a[132:135],a[244:247]
v_mfma_f32_16x16x32_bf16 a[248:251],v[80:83],a[136:139],a[248:251]
buffer_load_dwordx4 v[36:39],v6,s[28:31],s88 offen sc1
v_mfma_f32_16x16x32_bf16 a[248:251],v[84:87],a[152:155],a[248:251]
v_mfma_f32_16x16x32_bf16 a[248:251],v[88:91],a[168:171],a[248:251]
v_mfma_f32_16x16x32_bf16 a[248:251],v[92:95],a[184:187],a[248:251]
v_mfma_f32_16x16x32_bf16 a[252:255],v[92:95],a[188:191],a[252:255]
ds_read_b64_tr_b16 v[92:93],v2 offset:30944
ds_read_b64_tr_b16 v[94:95],v2 offset:36064
v_mfma_f32_16x16x32_bf16 a[252:255],v[88:91],a[172:175],a[252:255]
buffer_load_dwordx4 a[120:123],v28,s[28:31],s90 offen offset:3072 sc1
v_mfma_f32_16x16x32_bf16 a[252:255],v[84:87],a[156:159],a[252:255]
s_add_u32 s90,s87,12288
ds_read_b64_tr_b16 v[84:85],v2 offset:10464
ds_read_b64_tr_b16 v[86:87],v2 offset:15584
v_mfma_f32_16x16x32_bf16 a[252:255],v[80:83],a[140:143],a[252:255]
ds_read_b64_tr_b16 v[80:81],v2 offset:224
v_mfma_f32_16x16x32_bf16 a[0:3],v[96:99],a[128:131],a[0:3]
ds_read_b64_tr_b16 v[82:83],v2 offset:5344
v_mfma_f32_16x16x32_bf16 a[0:3],v[100:103],a[144:147],a[0:3]
ds_read_b64_tr_b16 v[88:89],v2 offset:20704
v_mfma_f32_16x16x32_bf16 a[0:3],v[104:107],a[160:163],a[0:3]
buffer_load_dwordx4 v[40:43],v6,s[28:31],s89 offen sc1
v_mfma_f32_16x16x32_bf16 a[0:3],v[108:111],a[176:179],a[0:3]
ds_read_b64_tr_b16 v[90:91],v2 offset:25824
v_mfma_f32_16x16x32_bf16 a[4:7],v[108:111],a[180:183],a[4:7]
v_mfma_f32_16x16x32_bf16 a[4:7],v[104:107],a[164:167],a[4:7]
)A"
R"A(v_mfma_f32_16x16x32_bf16 a[4:7],v[100:103],a[148:151],a[4:7]
v_mfma_f32_16x16x32_bf16 a[4:7],v[96:99],a[132:135],a[4:7]
v_mfma_f32_16x16x32_bf16 a[8:11],v[96:99],a[136:139],a[8:11]
v_mfma_f32_16x16x32_bf16 a[8:11],v[100:103],a[152:155],a[8:11]
v_mfma_f32_16x16x32_bf16 a[8:11],v[104:107],a[168:171],a[8:11]
v_mfma_f32_16x16x32_bf16 a[8:11],v[108:111],a[184:187],a[8:11]
v_mfma_f32_16x16x32_bf16 a[12:15],v[108:111],a[188:191],a[12:15]
ds_read_b64_tr_b16 v[108:109],v1 offset:30976
ds_read_b64_tr_b16 v[110:111],v1 offset:36096
v_mfma_f32_16x16x32_bf16 a[12:15],v[104:107],a[172:175],a[12:15]
buffer_load_dwordx4 v[44:47],v6,s[28:31],s90 offen sc1
v_mfma_f32_16x16x32_bf16 a[12:15],v[100:103],a[156:159],a[12:15]
ds_read_b64_tr_b16 v[100:101],v1 offset:10496
ds_read_b64_tr_b16 v[102:103],v1 offset:15616
v_mfma_f32_16x16x32_bf16 a[12:15],v[96:99],a[140:143],a[12:15]
s_waitcnt lgkmcnt(4)
ds_read_b64_tr_b16 v[96:97],v1 offset:256
v_mfma_f32_16x16x32_bf16 a[16:19],v[112:115],a[128:131],a[16:19]
ds_read_b64_tr_b16 v[98:99],v1 offset:5376
v_mfma_f32_16x16x32_bf16 a[16:19],v[116:119],a[144:147],a[16:19]
ds_read_b64_tr_b16 v[104:105],v1 offset:20736
v_mfma_f32_16x16x32_bf16 a[16:19],v[120:123],a[160:163],a[16:19]
ds_read_b64_tr_b16 v[106:107],v1 offset:25856
v_add_u32 v1,0xffff6000,v1
v_mfma_f32_16x16x32_bf16 a[16:19],v[124:127],a[176:179],a[16:19]
v_mfma_f32_16x16x32_bf16 a[20:23],v[124:127],a[180:183],a[20:23]
v_mfma_f32_16x16x32_bf16 a[20:23],v[120:123],a[164:167],a[20:23]
v_mfma_f32_16x16x32_bf16 a[20:23],v[116:119],a[148:151],a[20:23]
v_mfma_f32_16x16x32_bf16 a[20:23],v[112:115],a[132:135],a[20:23]
v_mfma_f32_16x16x32_bf16 a[24:27],v[112:115],a[136:139],a[24:27]
buffer_load_dwordx4 v[180:183],v6,s[28:31],s90 offen offset:1024 sc1
v_mfma_f32_16x16x32_bf16 a[24:27],v[116:119],a[152:155],a[24:27]
v_mfma_f32_16x16x32_bf16 a[24:27],v[120:123],a[168:171],a[24:27]
v_mfma_f32_16x16x32_bf16 a[24:27],v[124:127],a[184:187],a[24:27]
v_mfma_f32_16x16x32_bf16 a[28:31],v[124:127],a[188:191],a[28:31]
ds_read_b64_tr_b16 v[124:125],v2 offset:31008
ds_read_b64_tr_b16 v[126:127],v2 offset:36128
v_mfma_f32_16x16x32_bf16 a[28:31],v[120:123],a[172:175],a[28:31]
ds_read_b64_tr_b16 v[120:121],v2 offset:20768
v_mfma_f32_16x16x32_bf16 a[28:31],v[116:119],a[156:159],a[28:31]
ds_read_b64_tr_b16 v[116:117],v2 offset:10528
v_mfma_f32_16x16x32_bf16 a[28:31],v[112:115],a[140:143],a[28:31]
ds_read_b64_tr_b16 v[112:113],v2 offset:288
v_mfma_f32_16x16x32_bf16 v[128:131],v[64:67],a[128:131],v[128:131]
ds_read_b64_tr_b16 v[114:115],v2 offset:5408
ds_read_b64_tr_b16 v[118:119],v2 offset:15648
v_mfma_f32_16x16x32_bf16 v[128:131],v[68:71],a[144:147],v[128:131]
ds_read_b64_tr_b16 v[122:123],v2 offset:25888
v_add_u32 v2,0xffff6000,v2
v_mfma_f32_16x16x32_bf16 v[128:131],v[72:75],a[160:163],v[128:131]
v_mfma_f32_16x16x32_bf16 v[128:131],v[76:79],a[176:179],v[128:131]
buffer_load_dwordx4 v[164:167],v6,s[28:31],s89 offen offset:1024 sc1
v_mfma_f32_16x16x32_bf16 v[136:139],v[76:79],a[180:183],v[136:139]
v_mfma_f32_16x16x32_bf16 v[136:139],v[72:75],a[164:167],v[136:139]
v_mfma_f32_16x16x32_bf16 v[136:139],v[68:71],a[148:151],v[136:139]
v_mfma_f32_16x16x32_bf16 v[136:139],v[64:67],a[132:135],v[136:139]
v_mfma_f32_16x16x32_bf16 v[144:147],v[64:67],a[136:139],v[144:147]
v_mfma_f32_16x16x32_bf16 v[144:147],v[68:71],a[152:155],v[144:147]
v_mfma_f32_16x16x32_bf16 v[144:147],v[72:75],a[168:171],v[144:147]
v_mfma_f32_16x16x32_bf16 v[144:147],v[76:79],a[184:187],v[144:147]
v_mfma_f32_16x16x32_bf16 v[152:155],v[76:79],a[188:191],v[152:155]
v_mfma_f32_16x16x32_bf16 v[152:155],v[72:75],a[172:175],v[152:155]
v_mfma_f32_16x16x32_bf16 v[152:155],v[68:71],a[156:159],v[152:155]
buffer_load_dwordx4 v[148:151],v6,s[28:31],s88 offen offset:1024 sc1
v_mfma_f32_16x16x32_bf16 v[152:155],v[64:67],a[140:143],v[152:155]
s_barrier
v_mfma_f32_16x16x32_bf16 v[160:163],v[80:83],a[128:131],v[160:163]
ds_read_b64_tr_b16 v[64:65],v1
ds_read_b64_tr_b16 v[66:67],v1 offset:5120
v_mfma_f32_16x16x32_bf16 v[160:163],v[84:87],a[144:147],v[160:163]
ds_read_b64_tr_b16 v[68:69],v1 offset:10240
v_mfma_f32_16x16x32_bf16 v[160:163],v[88:91],a[160:163],v[160:163]
ds_read_b64_tr_b16 v[70:71],v1 offset:15360
v_mfma_f32_16x16x32_bf16 v[160:163],v[92:95],a[176:179],v[160:163]
buffer_load_dwordx4 v24,s[48:51],s83 offen lds
v_mfma_f32_16x16x32_bf16 v[168:171],v[92:95],a[180:183],v[168:171]
ds_read_b64_tr_b16 v[72:73],v1 offset:20480
ds_read_b64_tr_b16 v[74:75],v1 offset:25600
v_mfma_f32_16x16x32_bf16 v[168:171],v[88:91],a[164:167],v[168:171]
ds_read_b64_tr_b16 v[76:77],v1 offset:30720
v_mfma_f32_16x16x32_bf16 v[168:171],v[84:87],a[148:151],v[168:171]
ds_read_b64_tr_b16 v[78:79],v1 offset:35840
v_mfma_f32_16x16x32_bf16 v[168:171],v[80:83],a[132:135],v[168:171]
v_mfma_f32_16x16x32_bf16 v[176:179],v[80:83],a[136:139],v[176:179]
buffer_load_dwordx4 v[132:135],v6,s[28:31],s87 offen offset:1024 sc1
v_mfma_f32_16x16x32_bf16 v[176:179],v[84:87],a[152:155],v[176:179]
v_mfma_f32_16x16x32_bf16 v[176:179],v[88:91],a[168:171],v[176:179]
v_mfma_f32_16x16x32_bf16 v[176:179],v[92:95],a[184:187],v[176:179]
v_mfma_f32_16x16x32_bf16 v[184:187],v[92:95],a[188:191],v[184:187]
ds_read_b64_tr_b16 v[92:93],v2 offset:30752
ds_read_b64_tr_b16 v[94:95],v2 offset:35872
v_mfma_f32_16x16x32_bf16 v[184:187],v[88:91],a[172:175],v[184:187]
ds_read_b64_tr_b16 v[88:89],v2 offset:20512
v_mfma_f32_16x16x32_bf16 v[184:187],v[84:87],a[156:159],v[184:187]
buffer_load_dwordx4 v25,s[48:51],s83 offen offset:1024 lds
v_mfma_f32_16x16x32_bf16 v[184:187],v[80:83],a[140:143],v[184:187]
s_waitcnt lgkmcnt(3)
ds_read_b64_tr_b16 v[80:81],v2 offset:32
ds_read_b64_tr_b16 v[82:83],v2 offset:5152
v_mfma_f32_16x16x32_bf16 v[192:195],v[96:99],a[128:131],v[192:195]
ds_read_b64_tr_b16 v[84:85],v2 offset:10272
v_mfma_f32_16x16x32_bf16 v[192:195],v[100:103],a[144:147],v[192:195]
ds_read_b64_tr_b16 v[86:87],v2 offset:15392
v_mfma_f32_16x16x32_bf16 v[192:195],v[104:107],a[160:163],v[192:195]
ds_read_b64_tr_b16 v[90:91],v2 offset:25632
v_mfma_f32_16x16x32_bf16 v[192:195],v[108:111],a[176:179],v[192:195]
v_mfma_f32_16x16x32_bf16 v[200:203],v[108:111],a[180:183],v[200:203]
buffer_load_dwordx4 v26,s[48:51],s83 offen offset:2048 lds
v_mfma_f32_16x16x32_bf16 v[200:203],v[104:107],a[164:167],v[200:203]
v_mfma_f32_16x16x32_bf16 v[200:203],v[100:103],a[148:151],v[200:203]
v_mfma_f32_16x16x32_bf16 v[200:203],v[96:99],a[132:135],v[200:203]
v_mfma_f32_16x16x32_bf16 v[208:211],v[96:99],a[136:139],v[208:211]
v_mfma_f32_16x16x32_bf16 v[208:211],v[100:103],a[152:155],v[208:211]
buffer_load_dwordx4 v[196:199],v6,s[28:31],s87 offen offset:2048 sc1
v_mfma_f32_16x16x32_bf16 v[208:211],v[104:107],a[168:171],v[208:211]
v_mfma_f32_16x16x32_bf16 v[208:211],v[108:111],a[184:187],v[208:211]
v_mfma_f32_16x16x32_bf16 v[216:219],v[108:111],a[188:191],v[216:219]
ds_read_b64_tr_b16 v[108:109],v1 offset:30784
ds_read_b64_tr_b16 v[110:111],v1 offset:35904
v_mfma_f32_16x16x32_bf16 v[216:219],v[104:107],a[172:175],v[216:219]
ds_read_b64_tr_b16 v[104:105],v1 offset:20544
v_mfma_f32_16x16x32_bf16 v[216:219],v[100:103],a[156:159],v[216:219]
ds_read_b64_tr_b16 v[100:101],v1 offset:10304
v_mfma_f32_16x16x32_bf16 v[216:219],v[96:99],a[140:143],v[216:219]
buffer_load_dwordx4 v27,s[48:51],s83 offen offset:3072 lds
v_mfma_f32_16x16x32_bf16 v[224:227],v[112:115],a[128:131],v[224:227]
s_add_u32 m0,s86,126976
ds_read_b64_tr_b16 v[96:97],v1 offset:64
ds_read_b64_tr_b16 v[98:99],v1 offset:5184
v_mfma_f32_16x16x32_bf16 v[224:227],v[116:119],a[144:147],v[224:227]
ds_read_b64_tr_b16 v[102:103],v1 offset:15424
v_mfma_f32_16x16x32_bf16 v[224:227],v[120:123],a[160:163],v[224:227]
ds_read_b64_tr_b16 v[106:107],v1 offset:25664
v_mfma_f32_16x16x32_bf16 v[224:227],v[124:127],a[176:179],v[224:227]
v_mfma_f32_16x16x32_bf16 v[232:235],v[124:127],a[180:183],v[232:235]
buffer_load_dwordx4 v[212:215],v6,s[28:31],s88 offen offset:2048 sc1
v_mfma_f32_16x16x32_bf16 v[232:235],v[120:123],a[164:167],v[232:235]
v_mfma_f32_16x16x32_bf16 v[232:235],v[116:119],a[148:151],v[232:235]
v_mfma_f32_16x16x32_bf16 v[232:235],v[112:115],a[132:135],v[232:235]
v_mfma_f32_16x16x32_bf16 v[240:243],v[112:115],a[136:139],v[240:243]
v_mfma_f32_16x16x32_bf16 v[240:243],v[116:119],a[152:155],v[240:243]
v_mfma_f32_16x16x32_bf16 v[240:243],v[120:123],a[168:171],v[240:243]
buffer_load_dwordx4 v7,s[48:51],s83 offen lds
v_mfma_f32_16x16x32_bf16 v[240:243],v[124:127],a[184:187],v[240:243]
s_add_u32 m0,s86,128000
s_mul_i32 s83,s63,20480
v_mfma_f32_16x16x32_bf16 v[248:251],v[124:127],a[188:191],v[248:251]
ds_read_b64_tr_b16 v[124:125],v2 offset:30816
ds_read_b64_tr_b16 v[126:127],v2 offset:35936
v_mfma_f32_16x16x32_bf16 v[248:251],v[120:123],a[172:175],v[248:251]
ds_read_b64_tr_b16 v[120:121],v2 offset:20576
v_mfma_f32_16x16x32_bf16 v[248:251],v[116:119],a[156:159],v[248:251]
ds_read_b64_tr_b16 v[116:117],v2 offset:10336
v_mfma_f32_16x16x32_bf16 v[248:251],v[112:115],a[140:143],v[248:251]
buffer_load_dwordx4 v[228:231],v6,s[28:31],s89 offen offset:2048 sc1
v_mfma_f32_16x16x32_bf16 a[192:195],v[64:67],a[68:71],a[192:195]
s_waitcnt vmcnt(17)
ds_read_b64_tr_b16 v[112:113],v2 offset:96
ds_read_b64_tr_b16 v[114:115],v2 offset:5216
v_mfma_f32_16x16x32_bf16 a[192:195],v[68:71],a[76:79],a[192:195]
ds_read_b64_tr_b16 v[118:119],v2 offset:15456
v_mfma_f32_16x16x32_bf16 a[192:195],v[72:75],a[84:87],a[192:195]
ds_read_b64_tr_b16 v[122:123],v2 offset:25696
v_mfma_f32_16x16x32_bf16 a[192:195],v[76:79],a[92:95],a[192:195]
v_mfma_f32_16x16x32_bf16 a[196:199],v[76:79],a[124:127],a[196:199]
v_mfma_f32_16x16x32_bf16 a[196:199],v[72:75],a[116:119],a[196:199]
buffer_load_dwordx4 v24,s[48:51],s84 offen lds
v_mfma_f32_16x16x32_bf16 a[196:199],v[68:71],a[108:111],a[196:199]
v_mfma_f32_16x16x32_bf16 a[196:199],v[64:67],a[100:103],a[196:199]
v_mfma_f32_16x16x32_bf16 a[200:203],v[64:67],a[64:67],a[200:203]
v_mfma_f32_16x16x32_bf16 a[200:203],v[68:71],a[72:75],a[200:203]
s_waitcnt vmcnt(16)
v_mfma_f32_16x16x32_bf16 a[200:203],v[72:75],a[80:83],a[200:203]
buffer_load_dwordx4 v[244:247],v6,s[28:31],s90 offen offset:2048 sc1
v_mfma_f32_16x16x32_bf16 a[200:203],v[76:79],a[88:91],a[200:203]
s_waitcnt vmcnt(16)
v_mfma_f32_16x16x32_bf16 a[204:207],v[76:79],a[120:123],a[204:207]
ds_read_b64_tr_b16 v[76:77],v1 offset:30848
ds_read_b64_tr_b16 v[78:79],v1 offset:35968
v_mfma_f32_16x16x32_bf16 a[204:207],v[72:75],a[112:115],a[204:207]
ds_read_b64_tr_b16 v[72:73],v1 offset:20608
v_mfma_f32_16x16x32_bf16 a[204:207],v[68:71],a[104:107],a[204:207]
ds_read_b64_tr_b16 v[68:69],v1 offset:10368
v_mfma_f32_16x16x32_bf16 a[204:207],v[64:67],a[96:99],a[204:207]
s_waitcnt lgkmcnt(4)
ds_read_b64_tr_b16 v[64:65],v1 offset:128
v_mfma_f32_16x16x32_bf16 a[208:211],v[80:83],a[68:71],a[208:211]
buffer_load_dwordx4 v25,s[48:51],s84 offen offset:1024 lds
v_mfma_f32_16x16x32_bf16 a[208:211],v[84:87],a[76:79],a[208:211]
ds_read_b64_tr_b16 v[66:67],v1 offset:5248
ds_read_b64_tr_b16 v[70:71],v1 offset:15488
v_mfma_f32_16x16x32_bf16 a[208:211],v[88:91],a[84:87],a[208:211]
ds_read_b64_tr_b16 v[74:75],v1 offset:25728
v_mfma_f32_16x16x32_bf16 a[208:211],v[92:95],a[92:95],a[208:211]
v_mfma_f32_16x16x32_bf16 a[212:215],v[92:95],a[124:127],a[212:215]
v_mfma_f32_16x16x32_bf16 a[212:215],v[88:91],a[116:119],a[212:215]
buffer_load_dwordx4 v[188:191],v28,s[28:31],s90 offen offset:3072 sc1
v_mfma_f32_16x16x32_bf16 a[212:215],v[84:87],a[108:111],a[212:215]
v_mfma_f32_16x16x32_bf16 a[212:215],v[80:83],a[100:103],a[212:215]
v_mfma_f32_16x16x32_bf16 a[216:219],v[80:83],a[64:67],a[216:219]
v_mfma_f32_16x16x32_bf16 a[216:219],v[84:87],a[72:75],a[216:219]
v_mfma_f32_16x16x32_bf16 a[216:219],v[88:91],a[80:83],a[216:219]
v_mfma_f32_16x16x32_bf16 a[216:219],v[92:95],a[88:91],a[216:219]
buffer_load_dwordx4 v26,s[48:51],s84 offen offset:2048 lds
v_mfma_f32_16x16x32_bf16 a[220:223],v[92:95],a[120:123],a[220:223]
ds_read_b64_tr_b16 v[92:93],v2 offset:30880
ds_read_b64_tr_b16 v[94:95],v2 offset:36000
v_mfma_f32_16x16x32_bf16 a[220:223],v[88:91],a[112:115],a[220:223]
ds_read_b64_tr_b16 v[88:89],v2 offset:20640
v_mfma_f32_16x16x32_bf16 a[220:223],v[84:87],a[104:107],a[220:223]
)A"
R"A(ds_read_b64_tr_b16 v[84:85],v2 offset:10400
v_mfma_f32_16x16x32_bf16 a[220:223],v[80:83],a[96:99],a[220:223]
ds_read_b64_tr_b16 v[80:81],v2 offset:160
v_mfma_f32_16x16x32_bf16 a[224:227],v[96:99],a[68:71],a[224:227]
ds_read_b64_tr_b16 v[82:83],v2 offset:5280
ds_read_b64_tr_b16 v[86:87],v2 offset:15520
v_mfma_f32_16x16x32_bf16 a[224:227],v[100:103],a[76:79],a[224:227]
buffer_load_dwordx4 v27,s[48:51],s84 offen offset:3072 lds
v_mfma_f32_16x16x32_bf16 a[224:227],v[104:107],a[84:87],a[224:227]
s_add_u32 m0,s86,132096
ds_read_b64_tr_b16 v[90:91],v2 offset:25760
v_mfma_f32_16x16x32_bf16 a[224:227],v[108:111],a[92:95],a[224:227]
v_mfma_f32_16x16x32_bf16 a[228:231],v[108:111],a[124:127],a[228:231]
v_mfma_f32_16x16x32_bf16 a[228:231],v[104:107],a[116:119],a[228:231]
v_mfma_f32_16x16x32_bf16 a[228:231],v[100:103],a[108:111],a[228:231]
buffer_load_dwordx4 v[172:175],v28,s[28:31],s89 offen offset:3072 sc1
v_mfma_f32_16x16x32_bf16 a[228:231],v[96:99],a[100:103],a[228:231]
v_mfma_f32_16x16x32_bf16 a[232:235],v[96:99],a[64:67],a[232:235]
v_mfma_f32_16x16x32_bf16 a[232:235],v[100:103],a[72:75],a[232:235]
v_mfma_f32_16x16x32_bf16 a[232:235],v[104:107],a[80:83],a[232:235]
v_mfma_f32_16x16x32_bf16 a[232:235],v[108:111],a[88:91],a[232:235]
v_mfma_f32_16x16x32_bf16 a[236:239],v[108:111],a[120:123],a[236:239]
buffer_load_dwordx4 v7,s[48:51],s84 offen lds
v_mfma_f32_16x16x32_bf16 a[236:239],v[104:107],a[112:115],a[236:239]
ds_read_b64_tr_b16 v[104:105],v1 offset:20672
ds_read_b64_tr_b16 v[106:107],v1 offset:25792
v_mfma_f32_16x16x32_bf16 a[236:239],v[100:103],a[104:107],a[236:239]
ds_read_b64_tr_b16 v[100:101],v1 offset:10432
v_mfma_f32_16x16x32_bf16 a[236:239],v[96:99],a[96:99],a[236:239]
ds_read_b64_tr_b16 v[96:97],v1 offset:192
v_mfma_f32_16x16x32_bf16 a[240:243],v[112:115],a[68:71],a[240:243]
ds_read_b64_tr_b16 v[98:99],v1 offset:5312
v_mfma_f32_16x16x32_bf16 a[240:243],v[116:119],a[76:79],a[240:243]
buffer_load_dwordx4 v[156:159],v28,s[28:31],s88 offen offset:3072 sc1
v_mfma_f32_16x16x32_bf16 a[240:243],v[120:123],a[84:87],a[240:243]
ds_read_b64_tr_b16 v[102:103],v1 offset:15552
ds_read_b64_tr_b16 v[108:109],v1 offset:30912
v_mfma_f32_16x16x32_bf16 a[240:243],v[124:127],a[92:95],a[240:243]
ds_read_b64_tr_b16 v[110:111],v1 offset:36032
v_add_u32 v1,0xffff6000,v1
v_mfma_f32_16x16x32_bf16 a[244:247],v[124:127],a[124:127],a[244:247]
v_mfma_f32_16x16x32_bf16 a[244:247],v[120:123],a[116:119],a[244:247]
v_mfma_f32_16x16x32_bf16 a[244:247],v[116:119],a[108:111],a[244:247]
buffer_load_dwordx4 v[140:143],v28,s[28:31],s87 offen offset:3072 sc1
v_mfma_f32_16x16x32_bf16 a[244:247],v[112:115],a[100:103],a[244:247]
s_add_u32 s87,s64,3
s_and_b32 s87,s87,3
s_lshl_b32 s87,s87,1
s_or_b32 s87,s87,1
s_lshl_b32 s87,s87,16
s_add_u32 s88,s87,4096
s_add_u32 s89,s87,8192
s_add_u32 s90,s87,12288
v_mfma_f32_16x16x32_bf16 a[248:251],v[112:115],a[64:67],a[248:251]
v_mfma_f32_16x16x32_bf16 a[248:251],v[116:119],a[72:75],a[248:251]
v_mfma_f32_16x16x32_bf16 a[248:251],v[120:123],a[80:83],a[248:251]
v_mfma_f32_16x16x32_bf16 a[248:251],v[124:127],a[88:91],a[248:251]
buffer_load_dwordx4 a[128:131],v6,s[28:31],s87 offen sc1
v_mfma_f32_16x16x32_bf16 a[252:255],v[124:127],a[120:123],a[252:255]
ds_read_b64_tr_b16 v[124:125],v2 offset:30944
ds_read_b64_tr_b16 v[126:127],v2 offset:36064
v_mfma_f32_16x16x32_bf16 a[252:255],v[120:123],a[112:115],a[252:255]
ds_read_b64_tr_b16 v[120:121],v2 offset:20704
v_mfma_f32_16x16x32_bf16 a[252:255],v[116:119],a[104:107],a[252:255]
ds_read_b64_tr_b16 v[116:117],v2 offset:10464
v_mfma_f32_16x16x32_bf16 a[252:255],v[112:115],a[96:99],a[252:255]
s_waitcnt lgkmcnt(4)
ds_read_b64_tr_b16 v[112:113],v2 offset:224
v_mfma_f32_16x16x32_bf16 a[0:3],v[64:67],a[68:71],a[0:3]
buffer_load_dwordx4 a[144:147],v6,s[28:31],s88 offen sc1
v_mfma_f32_16x16x32_bf16 a[0:3],v[68:71],a[76:79],a[0:3]
ds_read_b64_tr_b16 v[114:115],v2 offset:5344
ds_read_b64_tr_b16 v[118:119],v2 offset:15584
v_mfma_f32_16x16x32_bf16 a[0:3],v[72:75],a[84:87],a[0:3]
ds_read_b64_tr_b16 v[122:123],v2 offset:25824
v_add_u32 v2,0xffff6000,v2
v_mfma_f32_16x16x32_bf16 a[0:3],v[76:79],a[92:95],a[0:3]
v_mfma_f32_16x16x32_bf16 a[4:7],v[76:79],a[124:127],a[4:7]
v_mfma_f32_16x16x32_bf16 a[4:7],v[72:75],a[116:119],a[4:7]
buffer_load_dwordx4 a[160:163],v6,s[28:31],s89 offen sc1
v_mfma_f32_16x16x32_bf16 a[4:7],v[68:71],a[108:111],a[4:7]
v_mfma_f32_16x16x32_bf16 a[4:7],v[64:67],a[100:103],a[4:7]
v_mfma_f32_16x16x32_bf16 a[8:11],v[64:67],a[64:67],a[8:11]
v_mfma_f32_16x16x32_bf16 a[8:11],v[68:71],a[72:75],a[8:11]
v_mfma_f32_16x16x32_bf16 a[8:11],v[72:75],a[80:83],a[8:11]
buffer_load_dwordx4 a[176:179],v6,s[28:31],s90 offen sc1
v_mfma_f32_16x16x32_bf16 a[8:11],v[76:79],a[88:91],a[8:11]
v_mfma_f32_16x16x32_bf16 a[12:15],v[76:79],a[120:123],a[12:15]
ds_read_b64_tr_b16 v[76:77],v1 offset:30720
ds_read_b64_tr_b16 v[78:79],v1 offset:35840
v_mfma_f32_16x16x32_bf16 a[12:15],v[72:75],a[112:115],a[12:15]
ds_read_b64_tr_b16 v[72:73],v1 offset:20480
v_mfma_f32_16x16x32_bf16 a[12:15],v[68:71],a[104:107],a[12:15]
ds_read_b64_tr_b16 v[68:69],v1 offset:10240
v_mfma_f32_16x16x32_bf16 a[12:15],v[64:67],a[96:99],a[12:15]
buffer_load_dwordx4 a[180:183],v6,s[28:31],s90 offen offset:1024 sc1
v_mfma_f32_16x16x32_bf16 a[16:19],v[80:83],a[68:71],a[16:19]
ds_read_b64_tr_b16 v[64:65],v1
ds_read_b64_tr_b16 v[66:67],v1 offset:5120
v_mfma_f32_16x16x32_bf16 a[16:19],v[84:87],a[76:79],a[16:19]
ds_read_b64_tr_b16 v[70:71],v1 offset:15360
v_mfma_f32_16x16x32_bf16 a[16:19],v[88:91],a[84:87],a[16:19]
ds_read_b64_tr_b16 v[74:75],v1 offset:25600
v_add_u32 v1,0x1e000,v1
v_mfma_f32_16x16x32_bf16 a[16:19],v[92:95],a[92:95],a[16:19]
v_mfma_f32_16x16x32_bf16 a[20:23],v[92:95],a[124:127],a[20:23]
buffer_load_dwordx4 a[164:167],v6,s[28:31],s89 offen offset:1024 sc1
v_mfma_f32_16x16x32_bf16 a[20:23],v[88:91],a[116:119],a[20:23]
v_mfma_f32_16x16x32_bf16 a[20:23],v[84:87],a[108:111],a[20:23]
v_mfma_f32_16x16x32_bf16 a[20:23],v[80:83],a[100:103],a[20:23]
v_mfma_f32_16x16x32_bf16 a[24:27],v[80:83],a[64:67],a[24:27]
v_mfma_f32_16x16x32_bf16 a[24:27],v[84:87],a[72:75],a[24:27]
buffer_load_dwordx4 a[148:151],v6,s[28:31],s88 offen offset:1024 sc1
v_mfma_f32_16x16x32_bf16 a[24:27],v[88:91],a[80:83],a[24:27]
v_mfma_f32_16x16x32_bf16 a[24:27],v[92:95],a[88:91],a[24:27]
v_mfma_f32_16x16x32_bf16 a[28:31],v[92:95],a[120:123],a[28:31]
ds_read_b64_tr_b16 v[92:93],v2 offset:30752
ds_read_b64_tr_b16 v[94:95],v2 offset:35872
v_mfma_f32_16x16x32_bf16 a[28:31],v[88:91],a[112:115],a[28:31]
ds_read_b64_tr_b16 v[88:89],v2 offset:20512
v_mfma_f32_16x16x32_bf16 a[28:31],v[84:87],a[104:107],a[28:31]
buffer_load_dwordx4 a[132:135],v6,s[28:31],s87 offen offset:1024 sc1
v_mfma_f32_16x16x32_bf16 a[28:31],v[80:83],a[96:99],a[28:31]
s_nop 0
L_pd17_%=:
s_cbranch_execz L_trtg2F_%=
ds_read_b64_tr_b16 v[80:81],v2 offset:32
ds_read_b64_tr_b16 v[82:83],v2 offset:5152
v_mfma_f32_16x16x32_bf16 v[128:131],v[96:99],a[68:71],v[128:131]
ds_read_b64_tr_b16 v[84:85],v2 offset:10272
v_mfma_f32_16x16x32_bf16 v[128:131],v[100:103],a[76:79],v[128:131]
ds_read_b64_tr_b16 v[86:87],v2 offset:15392
v_mfma_f32_16x16x32_bf16 v[128:131],v[104:107],a[84:87],v[128:131]
ds_read_b64_tr_b16 v[90:91],v2 offset:25632
v_add_u32 v2,0x1e000,v2
v_mfma_f32_16x16x32_bf16 v[128:131],v[108:111],a[92:95],v[128:131]
buffer_load_dwordx4 a[136:139],v6,s[28:31],s87 offen offset:2048 sc1
v_mfma_f32_16x16x32_bf16 v[136:139],v[108:111],a[124:127],v[136:139]
v_mfma_f32_16x16x32_bf16 v[136:139],v[104:107],a[116:119],v[136:139]
v_mfma_f32_16x16x32_bf16 v[136:139],v[100:103],a[108:111],v[136:139]
v_mfma_f32_16x16x32_bf16 v[136:139],v[96:99],a[100:103],v[136:139]
v_mfma_f32_16x16x32_bf16 v[144:147],v[96:99],a[64:67],v[144:147]
buffer_load_dwordx4 a[152:155],v6,s[28:31],s88 offen offset:2048 sc1
v_mfma_f32_16x16x32_bf16 v[144:147],v[100:103],a[72:75],v[144:147]
v_mfma_f32_16x16x32_bf16 v[144:147],v[104:107],a[80:83],v[144:147]
v_mfma_f32_16x16x32_bf16 v[144:147],v[108:111],a[88:91],v[144:147]
v_mfma_f32_16x16x32_bf16 v[152:155],v[108:111],a[120:123],v[152:155]
v_mfma_f32_16x16x32_bf16 v[152:155],v[104:107],a[112:115],v[152:155]
buffer_load_dwordx4 a[168:171],v6,s[28:31],s89 offen offset:2048 sc1
v_mfma_f32_16x16x32_bf16 v[152:155],v[100:103],a[104:107],v[152:155]
v_mfma_f32_16x16x32_bf16 v[152:155],v[96:99],a[96:99],v[152:155]
s_waitcnt vmcnt(13)
s_barrier
s_waitcnt lgkmcnt(0)
v_mfma_f32_16x16x32_bf16 v[160:163],v[112:115],a[68:71],v[160:163]
ds_read_b64_tr_b16 v[96:97],v1
ds_read_b64_tr_b16 v[98:99],v1 offset:5120
v_mfma_f32_16x16x32_bf16 v[160:163],v[116:119],a[76:79],v[160:163]
buffer_load_dwordx4 a[184:187],v6,s[28:31],s90 offen offset:2048 sc1
v_mfma_f32_16x16x32_bf16 v[160:163],v[120:123],a[84:87],v[160:163]
ds_read_b64_tr_b16 v[100:101],v1 offset:10240
ds_read_b64_tr_b16 v[102:103],v1 offset:15360
v_mfma_f32_16x16x32_bf16 v[160:163],v[124:127],a[92:95],v[160:163]
ds_read_b64_tr_b16 v[104:105],v1 offset:20480
v_mfma_f32_16x16x32_bf16 v[168:171],v[124:127],a[124:127],v[168:171]
ds_read_b64_tr_b16 v[106:107],v1 offset:25600
v_mfma_f32_16x16x32_bf16 v[168:171],v[120:123],a[116:119],v[168:171]
ds_read_b64_tr_b16 v[108:109],v1 offset:30720
v_mfma_f32_16x16x32_bf16 v[168:171],v[116:119],a[108:111],v[168:171]
buffer_load_dwordx4 a[188:191],v28,s[28:31],s90 offen offset:3072 sc1
v_mfma_f32_16x16x32_bf16 v[168:171],v[112:115],a[100:103],v[168:171]
ds_read_b64_tr_b16 v[110:111],v1 offset:35840
v_add_u32 v1,0xfffe2000,v1
v_mfma_f32_16x16x32_bf16 v[176:179],v[112:115],a[64:67],v[176:179]
v_mfma_f32_16x16x32_bf16 v[176:179],v[116:119],a[72:75],v[176:179]
v_mfma_f32_16x16x32_bf16 v[176:179],v[120:123],a[80:83],v[176:179]
v_mfma_f32_16x16x32_bf16 v[176:179],v[124:127],a[88:91],v[176:179]
buffer_load_dwordx4 a[172:175],v28,s[28:31],s89 offen offset:3072 sc1
v_mfma_f32_16x16x32_bf16 v[184:187],v[124:127],a[120:123],v[184:187]
ds_read_b64_tr_b16 v[124:125],v2 offset:30752
ds_read_b64_tr_b16 v[126:127],v2 offset:35872
v_mfma_f32_16x16x32_bf16 v[184:187],v[120:123],a[112:115],v[184:187]
ds_read_b64_tr_b16 v[120:121],v2 offset:20512
v_mfma_f32_16x16x32_bf16 v[184:187],v[116:119],a[104:107],v[184:187]
ds_read_b64_tr_b16 v[116:117],v2 offset:10272
v_mfma_f32_16x16x32_bf16 v[184:187],v[112:115],a[96:99],v[184:187]
ds_read_b64_tr_b16 v[112:113],v2 offset:32
v_mfma_f32_16x16x32_bf16 a[192:195],v[64:67],v[32:35],a[192:195]
buffer_load_dwordx4 a[156:159],v28,s[28:31],s88 offen offset:3072 sc1
v_mfma_f32_16x16x32_bf16 a[192:195],v[68:71],v[36:39],a[192:195]
ds_read_b64_tr_b16 v[114:115],v2 offset:5152
ds_read_b64_tr_b16 v[118:119],v2 offset:15392
v_mfma_f32_16x16x32_bf16 a[192:195],v[72:75],v[40:43],a[192:195]
ds_read_b64_tr_b16 v[122:123],v2 offset:25632
v_add_u32 v2,0xfffe2000,v2
v_mfma_f32_16x16x32_bf16 a[192:195],v[76:79],v[44:47],a[192:195]
v_mfma_f32_16x16x32_bf16 a[196:199],v[76:79],v[180:183],a[196:199]
v_mfma_f32_16x16x32_bf16 a[196:199],v[72:75],v[164:167],a[196:199]
buffer_load_dwordx4 a[140:143],v28,s[28:31],s87 offen offset:3072 sc1
v_mfma_f32_16x16x32_bf16 a[196:199],v[68:71],v[148:151],a[196:199]
s_mul_i32 s87,s60,81920
s_add_u32 s87,s87,s66
s_add_u32 s83,s83,s87
v_mfma_f32_16x16x32_bf16 a[196:199],v[64:67],v[132:135],a[196:199]
s_lshl_b32 s66,s63,6
s_add_u32 s87,s87,s66
s_add_u32 s88,s87,20480
v_mfma_f32_16x16x32_bf16 a[200:203],v[64:67],v[196:199],a[200:203]
s_add_u32 s89,s87,40960
s_add_u32 s90,s87,61440
s_add_u32 s66,s77,4096
v_mfma_f32_16x16x32_bf16 a[200:203],v[68:71],v[212:215],a[200:203]
v_mfma_f32_16x16x32_bf16 a[200:203],v[72:75],v[228:231],a[200:203]
v_mfma_f32_16x16x32_bf16 a[200:203],v[76:79],v[244:247],a[200:203]
v_mfma_f32_16x16x32_bf16 a[204:207],v[76:79],v[188:191],a[204:207]
ds_read_b64_tr_b16 v[76:77],v1 offset:30784
ds_read_b64_tr_b16 v[78:79],v1 offset:35904
v_mfma_f32_16x16x32_bf16 a[204:207],v[72:75],v[172:175],a[204:207]
s_waitcnt vmcnt(17)
ds_read_b64_tr_b16 v[72:73],v1 offset:20544
v_mfma_f32_16x16x32_bf16 a[204:207],v[68:71],v[156:159],a[204:207]
s_waitcnt vmcnt(16)
ds_read_b64_tr_b16 v[68:69],v1 offset:10304
v_mfma_f32_16x16x32_bf16 a[204:207],v[64:67],v[140:143],a[204:207]
)A"
R"A(ds_read_b64_tr_b16 v[64:65],v1 offset:64
v_mfma_f32_16x16x32_bf16 a[208:211],v[80:83],v[32:35],a[208:211]
ds_read_b64_tr_b16 v[66:67],v1 offset:5184
ds_read_b64_tr_b16 v[70:71],v1 offset:15424
v_mfma_f32_16x16x32_bf16 a[208:211],v[84:87],v[36:39],a[208:211]
ds_read_b64_tr_b16 v[74:75],v1 offset:25664
v_add_u32 v1,0x1e000,v1
v_mfma_f32_16x16x32_bf16 a[208:211],v[88:91],v[40:43],a[208:211]
v_mfma_f32_16x16x32_bf16 a[208:211],v[92:95],v[44:47],a[208:211]
v_mfma_f32_16x16x32_bf16 a[212:215],v[92:95],v[180:183],a[212:215]
v_mfma_f32_16x16x32_bf16 a[212:215],v[88:91],v[164:167],a[212:215]
v_mfma_f32_16x16x32_bf16 a[212:215],v[84:87],v[148:151],a[212:215]
v_mfma_f32_16x16x32_bf16 a[212:215],v[80:83],v[132:135],a[212:215]
v_mfma_f32_16x16x32_bf16 a[216:219],v[80:83],v[196:199],a[216:219]
v_mfma_f32_16x16x32_bf16 a[216:219],v[84:87],v[212:215],a[216:219]
v_mfma_f32_16x16x32_bf16 a[216:219],v[88:91],v[228:231],a[216:219]
v_mfma_f32_16x16x32_bf16 a[216:219],v[92:95],v[244:247],a[216:219]
v_mfma_f32_16x16x32_bf16 a[220:223],v[92:95],v[188:191],a[220:223]
ds_read_b64_tr_b16 v[92:93],v2 offset:30816
ds_read_b64_tr_b16 v[94:95],v2 offset:35936
v_mfma_f32_16x16x32_bf16 a[220:223],v[88:91],v[172:175],a[220:223]
ds_read_b64_tr_b16 v[88:89],v2 offset:20576
v_mfma_f32_16x16x32_bf16 a[220:223],v[84:87],v[156:159],a[220:223]
ds_read_b64_tr_b16 v[84:85],v2 offset:10336
v_mfma_f32_16x16x32_bf16 a[220:223],v[80:83],v[140:143],a[220:223]
s_waitcnt vmcnt(15) lgkmcnt(4)
ds_read_b64_tr_b16 v[80:81],v2 offset:96
v_mfma_f32_16x16x32_bf16 a[192:195],v[96:99],a[128:131],a[192:195]
s_waitcnt vmcnt(14)
ds_read_b64_tr_b16 v[82:83],v2 offset:5216
ds_read_b64_tr_b16 v[86:87],v2 offset:15456
v_mfma_f32_16x16x32_bf16 a[192:195],v[100:103],a[144:147],a[192:195]
s_waitcnt vmcnt(13)
ds_read_b64_tr_b16 v[90:91],v2 offset:25696
v_add_u32 v2,0x1e000,v2
v_mfma_f32_16x16x32_bf16 a[192:195],v[104:107],a[160:163],a[192:195]
s_waitcnt vmcnt(12)
v_mfma_f32_16x16x32_bf16 a[192:195],v[108:111],a[176:179],a[192:195]
s_waitcnt vmcnt(11)
v_mfma_f32_16x16x32_bf16 a[196:199],v[108:111],a[180:183],a[196:199]
s_waitcnt vmcnt(10)
v_mfma_f32_16x16x32_bf16 a[196:199],v[104:107],a[164:167],a[196:199]
s_waitcnt vmcnt(9)
v_mfma_f32_16x16x32_bf16 a[196:199],v[100:103],a[148:151],a[196:199]
s_waitcnt vmcnt(8)
v_mfma_f32_16x16x32_bf16 a[196:199],v[96:99],a[132:135],a[196:199]
s_waitcnt vmcnt(7)
v_mfma_f32_16x16x32_bf16 a[200:203],v[96:99],a[136:139],a[200:203]
s_waitcnt vmcnt(6)
v_mfma_f32_16x16x32_bf16 a[200:203],v[100:103],a[152:155],a[200:203]
s_waitcnt vmcnt(5)
v_mfma_f32_16x16x32_bf16 a[200:203],v[104:107],a[168:171],a[200:203]
s_waitcnt vmcnt(4)
v_mfma_f32_16x16x32_bf16 a[200:203],v[108:111],a[184:187],a[200:203]
s_waitcnt vmcnt(3)
v_accvgpr_read_b32 v16,a192
v_accvgpr_read_b32 v17,a193
v_mfma_f32_16x16x32_bf16 a[204:207],v[108:111],a[188:191],a[204:207]
s_waitcnt vmcnt(2)
v_accvgpr_read_b32 v18,a194
v_accvgpr_read_b32 v19,a195
v_mfma_f32_16x16x32_bf16 a[204:207],v[104:107],a[172:175],a[204:207]
s_waitcnt vmcnt(1)
v_cvt_pk_bf16_f32 v16,v16,v17
v_cvt_pk_bf16_f32 v17,v18,v19
v_mfma_f32_16x16x32_bf16 a[204:207],v[100:103],a[156:159],a[204:207]
s_waitcnt vmcnt(0)
v_accvgpr_read_b32 v8,a196
v_accvgpr_read_b32 v9,a197
v_mfma_f32_16x16x32_bf16 a[204:207],v[96:99],a[140:143],a[204:207]
v_accvgpr_read_b32 v10,a198
v_accvgpr_read_b32 v11,a199
v_mfma_f32_16x16x32_bf16 a[208:211],v[112:115],a[128:131],a[208:211]
v_cvt_pk_bf16_f32 v8,v8,v9
v_cvt_pk_bf16_f32 v9,v10,v11
v_mfma_f32_16x16x32_bf16 a[208:211],v[116:119],a[144:147],a[208:211]
ds_read_b64_tr_b16 v[96:97],v1 offset:64
ds_read_b64_tr_b16 v[98:99],v1 offset:5184
v_mfma_f32_16x16x32_bf16 a[208:211],v[120:123],a[160:163],a[208:211]
ds_read_b64_tr_b16 v[100:101],v1 offset:10304
v_mfma_f32_16x16x32_bf16 a[208:211],v[124:127],a[176:179],a[208:211]
ds_read_b64_tr_b16 v[102:103],v1 offset:15424
v_mfma_f32_16x16x32_bf16 a[212:215],v[124:127],a[180:183],a[212:215]
ds_read_b64_tr_b16 v[104:105],v1 offset:20544
v_mfma_f32_16x16x32_bf16 a[212:215],v[120:123],a[164:167],a[212:215]
ds_read_b64_tr_b16 v[106:107],v1 offset:25664
ds_read_b64_tr_b16 v[108:109],v1 offset:30784
v_mfma_f32_16x16x32_bf16 a[212:215],v[116:119],a[148:151],a[212:215]
ds_read_b64_tr_b16 v[110:111],v1 offset:35904
v_add_u32 v1,0xfffe2000,v1
v_mfma_f32_16x16x32_bf16 a[212:215],v[112:115],a[132:135],a[212:215]
v_accvgpr_read_b32 v20,a208
v_accvgpr_read_b32 v21,a209
v_mfma_f32_16x16x32_bf16 a[216:219],v[112:115],a[136:139],a[216:219]
v_accvgpr_read_b32 v22,a210
v_accvgpr_read_b32 v23,a211
v_mfma_f32_16x16x32_bf16 a[216:219],v[116:119],a[152:155],a[216:219]
v_cvt_pk_bf16_f32 v18,v20,v21
v_cvt_pk_bf16_f32 v19,v22,v23
v_mfma_f32_16x16x32_bf16 a[216:219],v[120:123],a[168:171],a[216:219]
buffer_store_dwordx4 v[16:19],v6,s[36:39],s74 offen sc1
v_mfma_f32_16x16x32_bf16 a[216:219],v[124:127],a[184:187],a[216:219]
v_accvgpr_read_b32 v12,a212
v_accvgpr_read_b32 v13,a213
v_mfma_f32_16x16x32_bf16 a[220:223],v[124:127],a[188:191],a[220:223]
v_accvgpr_read_b32 v14,a214
v_accvgpr_read_b32 v15,a215
v_mfma_f32_16x16x32_bf16 a[220:223],v[120:123],a[172:175],a[220:223]
v_cvt_pk_bf16_f32 v10,v12,v13
v_cvt_pk_bf16_f32 v11,v14,v15
v_mfma_f32_16x16x32_bf16 a[220:223],v[116:119],a[156:159],a[220:223]
v_accvgpr_read_b32 v16,a200
v_accvgpr_read_b32 v17,a201
v_mfma_f32_16x16x32_bf16 a[220:223],v[112:115],a[140:143],a[220:223]
v_accvgpr_read_b32 v18,a202
v_accvgpr_read_b32 v19,a203
v_mfma_f32_16x16x32_bf16 a[224:227],v[64:67],v[32:35],a[224:227]
v_accvgpr_read_b32 v20,a216
v_accvgpr_read_b32 v21,a217
v_mfma_f32_16x16x32_bf16 a[224:227],v[68:71],v[36:39],a[224:227]
v_accvgpr_read_b32 v22,a218
v_accvgpr_read_b32 v23,a219
v_mfma_f32_16x16x32_bf16 a[224:227],v[72:75],v[40:43],a[224:227]
v_cvt_pk_bf16_f32 v16,v16,v17
v_cvt_pk_bf16_f32 v17,v18,v19
v_mfma_f32_16x16x32_bf16 a[224:227],v[76:79],v[44:47],a[224:227]
v_cvt_pk_bf16_f32 v18,v20,v21
v_cvt_pk_bf16_f32 v19,v22,v23
v_mfma_f32_16x16x32_bf16 a[228:231],v[76:79],v[180:183],a[228:231]
ds_read_b64_tr_b16 v[112:113],v2 offset:96
ds_read_b64_tr_b16 v[114:115],v2 offset:5216
v_mfma_f32_16x16x32_bf16 a[228:231],v[72:75],v[164:167],a[228:231]
buffer_store_dwordx4 v[8:11],v6,s[36:39],s74 offen offset:1024 sc1
v_mfma_f32_16x16x32_bf16 a[228:231],v[68:71],v[148:151],a[228:231]
ds_read_b64_tr_b16 v[116:117],v2 offset:10336
ds_read_b64_tr_b16 v[118:119],v2 offset:15456
v_mfma_f32_16x16x32_bf16 a[228:231],v[64:67],v[132:135],a[228:231]
ds_read_b64_tr_b16 v[120:121],v2 offset:20576
v_accvgpr_read_b32 v8,a204
v_mfma_f32_16x16x32_bf16 a[232:235],v[64:67],v[196:199],a[232:235]
ds_read_b64_tr_b16 v[122:123],v2 offset:25696
v_accvgpr_read_b32 v9,a205
v_mfma_f32_16x16x32_bf16 a[232:235],v[68:71],v[212:215],a[232:235]
ds_read_b64_tr_b16 v[124:125],v2 offset:30816
v_accvgpr_read_b32 v10,a206
v_mfma_f32_16x16x32_bf16 a[232:235],v[72:75],v[228:231],a[232:235]
ds_read_b64_tr_b16 v[126:127],v2 offset:35936
v_accvgpr_read_b32 v11,a207
v_mfma_f32_16x16x32_bf16 a[232:235],v[76:79],v[244:247],a[232:235]
v_accvgpr_read_b32 v12,a220
v_accvgpr_read_b32 v13,a221
v_mfma_f32_16x16x32_bf16 a[236:239],v[76:79],v[188:191],a[236:239]
v_accvgpr_read_b32 v14,a222
v_accvgpr_read_b32 v15,a223
v_mfma_f32_16x16x32_bf16 a[236:239],v[72:75],v[172:175],a[236:239]
v_cvt_pk_bf16_f32 v8,v8,v9
v_cvt_pk_bf16_f32 v9,v10,v11
v_mfma_f32_16x16x32_bf16 a[236:239],v[68:71],v[156:159],a[236:239]
v_cvt_pk_bf16_f32 v10,v12,v13
v_cvt_pk_bf16_f32 v11,v14,v15
v_mfma_f32_16x16x32_bf16 a[236:239],v[64:67],v[140:143],a[236:239]
s_waitcnt lgkmcnt(3)
ds_read_b64_tr_b16 v[64:65],v1 offset:128
ds_read_b64_tr_b16 v[66:67],v1 offset:5248
v_mfma_f32_16x16x32_bf16 a[240:243],v[80:83],v[32:35],a[240:243]
buffer_store_dwordx4 v[16:19],v6,s[36:39],s74 offen offset:2048 sc1
v_mfma_f32_16x16x32_bf16 a[240:243],v[84:87],v[36:39],a[240:243]
ds_read_b64_tr_b16 v[68:69],v1 offset:10368
ds_read_b64_tr_b16 v[70:71],v1 offset:15488
v_mfma_f32_16x16x32_bf16 a[240:243],v[88:91],v[40:43],a[240:243]
ds_read_b64_tr_b16 v[72:73],v1 offset:20608
v_add_u32 v2,0xfffe2000,v2
v_mfma_f32_16x16x32_bf16 a[240:243],v[92:95],v[44:47],a[240:243]
ds_read_b64_tr_b16 v[74:75],v1 offset:25728
v_mfma_f32_16x16x32_bf16 a[244:247],v[92:95],v[180:183],a[244:247]
ds_read_b64_tr_b16 v[76:77],v1 offset:30848
v_mfma_f32_16x16x32_bf16 a[244:247],v[88:91],v[164:167],a[244:247]
ds_read_b64_tr_b16 v[78:79],v1 offset:35968
v_add_u32 v1,0x1e000,v1
v_mfma_f32_16x16x32_bf16 a[244:247],v[84:87],v[148:151],a[244:247]
v_mfma_f32_16x16x32_bf16 a[244:247],v[80:83],v[132:135],a[244:247]
v_mfma_f32_16x16x32_bf16 a[248:251],v[80:83],v[196:199],a[248:251]
v_mfma_f32_16x16x32_bf16 a[248:251],v[84:87],v[212:215],a[248:251]
v_mfma_f32_16x16x32_bf16 a[248:251],v[88:91],v[228:231],a[248:251]
v_mfma_f32_16x16x32_bf16 a[248:251],v[92:95],v[244:247],a[248:251]
buffer_store_dwordx4 v[8:11],v28,s[36:39],s74 offen offset:3072 sc1
v_mfma_f32_16x16x32_bf16 a[252:255],v[92:95],v[188:191],a[252:255]
ds_read_b64_tr_b16 v[92:93],v2 offset:30880
ds_read_b64_tr_b16 v[94:95],v2 offset:36000
v_mfma_f32_16x16x32_bf16 a[252:255],v[88:91],v[172:175],a[252:255]
ds_read_b64_tr_b16 v[88:89],v2 offset:20640
v_mfma_f32_16x16x32_bf16 a[252:255],v[84:87],v[156:159],a[252:255]
ds_read_b64_tr_b16 v[84:85],v2 offset:10400
v_mfma_f32_16x16x32_bf16 a[252:255],v[80:83],v[140:143],a[252:255]
ds_read_b64_tr_b16 v[80:81],v2 offset:160
v_mfma_f32_16x16x32_bf16 a[224:227],v[96:99],a[128:131],a[224:227]
ds_read_b64_tr_b16 v[82:83],v2 offset:5280
ds_read_b64_tr_b16 v[86:87],v2 offset:15520
v_mfma_f32_16x16x32_bf16 a[224:227],v[100:103],a[144:147],a[224:227]
ds_read_b64_tr_b16 v[90:91],v2 offset:25760
v_add_u32 v2,0x1e000,v2
v_mfma_f32_16x16x32_bf16 a[224:227],v[104:107],a[160:163],a[224:227]
v_mfma_f32_16x16x32_bf16 a[224:227],v[108:111],a[176:179],a[224:227]
v_mfma_f32_16x16x32_bf16 a[228:231],v[108:111],a[180:183],a[228:231]
v_mfma_f32_16x16x32_bf16 a[228:231],v[104:107],a[164:167],a[228:231]
v_mfma_f32_16x16x32_bf16 a[228:231],v[100:103],a[148:151],a[228:231]
v_mfma_f32_16x16x32_bf16 a[228:231],v[96:99],a[132:135],a[228:231]
v_mfma_f32_16x16x32_bf16 a[232:235],v[96:99],a[136:139],a[232:235]
v_mfma_f32_16x16x32_bf16 a[232:235],v[100:103],a[152:155],a[232:235]
v_mfma_f32_16x16x32_bf16 a[232:235],v[104:107],a[168:171],a[232:235]
v_mfma_f32_16x16x32_bf16 a[232:235],v[108:111],a[184:187],a[232:235]
v_accvgpr_read_b32 v16,a224
v_accvgpr_read_b32 v17,a225
v_mfma_f32_16x16x32_bf16 a[236:239],v[108:111],a[188:191],a[236:239]
v_accvgpr_read_b32 v18,a226
v_accvgpr_read_b32 v19,a227
v_mfma_f32_16x16x32_bf16 a[236:239],v[104:107],a[172:175],a[236:239]
v_cvt_pk_bf16_f32 v16,v16,v17
v_cvt_pk_bf16_f32 v17,v18,v19
v_mfma_f32_16x16x32_bf16 a[236:239],v[100:103],a[156:159],a[236:239]
v_accvgpr_read_b32 v8,a228
v_accvgpr_read_b32 v9,a229
v_mfma_f32_16x16x32_bf16 a[236:239],v[96:99],a[140:143],a[236:239]
v_accvgpr_read_b32 v10,a230
v_accvgpr_read_b32 v11,a231
v_mfma_f32_16x16x32_bf16 a[240:243],v[112:115],a[128:131],a[240:243]
v_cvt_pk_bf16_f32 v8,v8,v9
v_cvt_pk_bf16_f32 v9,v10,v11
v_mfma_f32_16x16x32_bf16 a[240:243],v[116:119],a[144:147],a[240:243]
s_waitcnt lgkmcnt(0)
ds_read_b64_tr_b16 v[96:97],v1 offset:128
ds_read_b64_tr_b16 v[98:99],v1 offset:5248
v_mfma_f32_16x16x32_bf16 a[240:243],v[120:123],a[160:163],a[240:243]
ds_read_b64_tr_b16 v[100:101],v1 offset:10368
v_mfma_f32_16x16x32_bf16 a[240:243],v[124:127],a[176:179],a[240:243]
ds_read_b64_tr_b16 v[102:103],v1 offset:15488
v_mfma_f32_16x16x32_bf16 a[244:247],v[124:127],a[180:183],a[244:247]
ds_read_b64_tr_b16 v[104:105],v1 offset:20608
v_mfma_f32_16x16x32_bf16 a[244:247],v[120:123],a[164:167],a[244:247]
ds_read_b64_tr_b16 v[106:107],v1 offset:25728
ds_read_b64_tr_b16 v[108:109],v1 offset:30848
v_mfma_f32_16x16x32_bf16 a[244:247],v[116:119],a[148:151],a[244:247]
ds_read_b64_tr_b16 v[110:111],v1 offset:35968
v_add_u32 v1,0xfffe2000,v1
v_mfma_f32_16x16x32_bf16 a[244:247],v[112:115],a[132:135],a[244:247]
v_accvgpr_read_b32 v20,a240
v_accvgpr_read_b32 v21,a241
v_mfma_f32_16x16x32_bf16 a[248:251],v[112:115],a[136:139],a[248:251]
v_accvgpr_read_b32 v22,a242
v_accvgpr_read_b32 v23,a243
)A"
R"A(v_mfma_f32_16x16x32_bf16 a[248:251],v[116:119],a[152:155],a[248:251]
v_cvt_pk_bf16_f32 v18,v20,v21
v_cvt_pk_bf16_f32 v19,v22,v23
v_mfma_f32_16x16x32_bf16 a[248:251],v[120:123],a[168:171],a[248:251]
buffer_store_dwordx4 v[16:19],v6,s[36:39],s75 offen sc1
v_mfma_f32_16x16x32_bf16 a[248:251],v[124:127],a[184:187],a[248:251]
v_accvgpr_read_b32 v12,a244
v_accvgpr_read_b32 v13,a245
v_mfma_f32_16x16x32_bf16 a[252:255],v[124:127],a[188:191],a[252:255]
v_accvgpr_read_b32 v14,a246
v_accvgpr_read_b32 v15,a247
v_mfma_f32_16x16x32_bf16 a[252:255],v[120:123],a[172:175],a[252:255]
v_cvt_pk_bf16_f32 v10,v12,v13
v_cvt_pk_bf16_f32 v11,v14,v15
v_mfma_f32_16x16x32_bf16 a[252:255],v[116:119],a[156:159],a[252:255]
v_accvgpr_read_b32 v16,a232
v_accvgpr_read_b32 v17,a233
v_mfma_f32_16x16x32_bf16 a[252:255],v[112:115],a[140:143],a[252:255]
v_accvgpr_read_b32 v18,a234
v_accvgpr_read_b32 v19,a235
v_mfma_f32_16x16x32_bf16 a[0:3],v[64:67],v[32:35],a[0:3]
v_accvgpr_read_b32 v20,a248
v_accvgpr_read_b32 v21,a249
v_mfma_f32_16x16x32_bf16 a[0:3],v[68:71],v[36:39],a[0:3]
v_accvgpr_read_b32 v22,a250
v_accvgpr_read_b32 v23,a251
v_mfma_f32_16x16x32_bf16 a[0:3],v[72:75],v[40:43],a[0:3]
v_cvt_pk_bf16_f32 v16,v16,v17
v_cvt_pk_bf16_f32 v17,v18,v19
v_mfma_f32_16x16x32_bf16 a[0:3],v[76:79],v[44:47],a[0:3]
v_cvt_pk_bf16_f32 v18,v20,v21
v_cvt_pk_bf16_f32 v19,v22,v23
v_mfma_f32_16x16x32_bf16 a[4:7],v[76:79],v[180:183],a[4:7]
ds_read_b64_tr_b16 v[112:113],v2 offset:160
ds_read_b64_tr_b16 v[114:115],v2 offset:5280
v_mfma_f32_16x16x32_bf16 a[4:7],v[72:75],v[164:167],a[4:7]
buffer_store_dwordx4 v[8:11],v6,s[36:39],s75 offen offset:1024 sc1
v_mfma_f32_16x16x32_bf16 a[4:7],v[68:71],v[148:151],a[4:7]
ds_read_b64_tr_b16 v[116:117],v2 offset:10400
ds_read_b64_tr_b16 v[118:119],v2 offset:15520
v_mfma_f32_16x16x32_bf16 a[4:7],v[64:67],v[132:135],a[4:7]
ds_read_b64_tr_b16 v[120:121],v2 offset:20640
v_accvgpr_read_b32 v8,a236
v_mfma_f32_16x16x32_bf16 a[8:11],v[64:67],v[196:199],a[8:11]
ds_read_b64_tr_b16 v[122:123],v2 offset:25760
v_accvgpr_read_b32 v9,a237
v_mfma_f32_16x16x32_bf16 a[8:11],v[68:71],v[212:215],a[8:11]
ds_read_b64_tr_b16 v[124:125],v2 offset:30880
v_accvgpr_read_b32 v10,a238
v_mfma_f32_16x16x32_bf16 a[8:11],v[72:75],v[228:231],a[8:11]
ds_read_b64_tr_b16 v[126:127],v2 offset:36000
v_accvgpr_read_b32 v11,a239
v_mfma_f32_16x16x32_bf16 a[8:11],v[76:79],v[244:247],a[8:11]
v_accvgpr_read_b32 v12,a252
v_accvgpr_read_b32 v13,a253
v_mfma_f32_16x16x32_bf16 a[12:15],v[76:79],v[188:191],a[12:15]
v_accvgpr_read_b32 v14,a254
v_accvgpr_read_b32 v15,a255
v_mfma_f32_16x16x32_bf16 a[12:15],v[72:75],v[172:175],a[12:15]
v_cvt_pk_bf16_f32 v8,v8,v9
v_cvt_pk_bf16_f32 v9,v10,v11
v_mfma_f32_16x16x32_bf16 a[12:15],v[68:71],v[156:159],a[12:15]
v_cvt_pk_bf16_f32 v10,v12,v13
v_cvt_pk_bf16_f32 v11,v14,v15
v_mfma_f32_16x16x32_bf16 a[12:15],v[64:67],v[140:143],a[12:15]
ds_read_b64_tr_b16 v[64:65],v1 offset:192
ds_read_b64_tr_b16 v[66:67],v1 offset:5312
v_mfma_f32_16x16x32_bf16 a[16:19],v[80:83],v[32:35],a[16:19]
buffer_store_dwordx4 v[16:19],v6,s[36:39],s75 offen offset:2048 sc1
v_mfma_f32_16x16x32_bf16 a[16:19],v[84:87],v[36:39],a[16:19]
ds_read_b64_tr_b16 v[68:69],v1 offset:10432
ds_read_b64_tr_b16 v[70:71],v1 offset:15552
v_mfma_f32_16x16x32_bf16 a[16:19],v[88:91],v[40:43],a[16:19]
ds_read_b64_tr_b16 v[72:73],v1 offset:20672
v_add_u32 v2,0xfffe2000,v2
v_mfma_f32_16x16x32_bf16 a[16:19],v[92:95],v[44:47],a[16:19]
ds_read_b64_tr_b16 v[74:75],v1 offset:25792
v_mfma_f32_16x16x32_bf16 a[20:23],v[92:95],v[180:183],a[20:23]
ds_read_b64_tr_b16 v[76:77],v1 offset:30912
v_mfma_f32_16x16x32_bf16 a[20:23],v[88:91],v[164:167],a[20:23]
ds_read_b64_tr_b16 v[78:79],v1 offset:36032
v_add_u32 v1,0x1e000,v1
v_mfma_f32_16x16x32_bf16 a[20:23],v[84:87],v[148:151],a[20:23]
v_mfma_f32_16x16x32_bf16 a[20:23],v[80:83],v[132:135],a[20:23]
v_mfma_f32_16x16x32_bf16 a[24:27],v[80:83],v[196:199],a[24:27]
v_mfma_f32_16x16x32_bf16 a[24:27],v[84:87],v[212:215],a[24:27]
v_mfma_f32_16x16x32_bf16 a[24:27],v[88:91],v[228:231],a[24:27]
v_mfma_f32_16x16x32_bf16 a[24:27],v[92:95],v[244:247],a[24:27]
buffer_store_dwordx4 v[8:11],v28,s[36:39],s75 offen offset:3072 sc1
v_mfma_f32_16x16x32_bf16 a[28:31],v[92:95],v[188:191],a[28:31]
ds_read_b64_tr_b16 v[92:93],v2 offset:30944
ds_read_b64_tr_b16 v[94:95],v2 offset:36064
v_mfma_f32_16x16x32_bf16 a[28:31],v[88:91],v[172:175],a[28:31]
ds_read_b64_tr_b16 v[88:89],v2 offset:20704
v_mfma_f32_16x16x32_bf16 a[28:31],v[84:87],v[156:159],a[28:31]
ds_read_b64_tr_b16 v[84:85],v2 offset:10464
v_mfma_f32_16x16x32_bf16 a[28:31],v[80:83],v[140:143],a[28:31]
s_waitcnt lgkmcnt(4)
ds_read_b64_tr_b16 v[80:81],v2 offset:224
v_mfma_f32_16x16x32_bf16 a[0:3],v[96:99],a[128:131],a[0:3]
ds_read_b64_tr_b16 v[82:83],v2 offset:5344
ds_read_b64_tr_b16 v[86:87],v2 offset:15584
v_mfma_f32_16x16x32_bf16 a[0:3],v[100:103],a[144:147],a[0:3]
ds_read_b64_tr_b16 v[90:91],v2 offset:25824
v_add_u32 v2,0x1e000,v2
v_mfma_f32_16x16x32_bf16 a[0:3],v[104:107],a[160:163],a[0:3]
v_mfma_f32_16x16x32_bf16 a[0:3],v[108:111],a[176:179],a[0:3]
v_mfma_f32_16x16x32_bf16 a[4:7],v[108:111],a[180:183],a[4:7]
v_mfma_f32_16x16x32_bf16 a[4:7],v[104:107],a[164:167],a[4:7]
v_mfma_f32_16x16x32_bf16 a[4:7],v[100:103],a[148:151],a[4:7]
v_mfma_f32_16x16x32_bf16 a[4:7],v[96:99],a[132:135],a[4:7]
v_mfma_f32_16x16x32_bf16 a[8:11],v[96:99],a[136:139],a[8:11]
v_mfma_f32_16x16x32_bf16 a[8:11],v[100:103],a[152:155],a[8:11]
v_mfma_f32_16x16x32_bf16 a[8:11],v[104:107],a[168:171],a[8:11]
v_mfma_f32_16x16x32_bf16 a[8:11],v[108:111],a[184:187],a[8:11]
v_accvgpr_read_b32 v16,a0
v_accvgpr_read_b32 v17,a1
v_mfma_f32_16x16x32_bf16 a[12:15],v[108:111],a[188:191],a[12:15]
v_accvgpr_read_b32 v18,a2
v_accvgpr_read_b32 v19,a3
v_mfma_f32_16x16x32_bf16 a[12:15],v[104:107],a[172:175],a[12:15]
v_cvt_pk_bf16_f32 v16,v16,v17
v_cvt_pk_bf16_f32 v17,v18,v19
v_mfma_f32_16x16x32_bf16 a[12:15],v[100:103],a[156:159],a[12:15]
v_accvgpr_read_b32 v8,a4
v_accvgpr_read_b32 v9,a5
v_mfma_f32_16x16x32_bf16 a[12:15],v[96:99],a[140:143],a[12:15]
v_accvgpr_read_b32 v10,a6
v_accvgpr_read_b32 v11,a7
v_mfma_f32_16x16x32_bf16 a[16:19],v[112:115],a[128:131],a[16:19]
v_cvt_pk_bf16_f32 v8,v8,v9
v_cvt_pk_bf16_f32 v9,v10,v11
v_mfma_f32_16x16x32_bf16 a[16:19],v[116:119],a[144:147],a[16:19]
ds_read_b64_tr_b16 v[96:97],v1 offset:192
ds_read_b64_tr_b16 v[98:99],v1 offset:5312
v_mfma_f32_16x16x32_bf16 a[16:19],v[120:123],a[160:163],a[16:19]
ds_read_b64_tr_b16 v[100:101],v1 offset:10432
v_mfma_f32_16x16x32_bf16 a[16:19],v[124:127],a[176:179],a[16:19]
ds_read_b64_tr_b16 v[102:103],v1 offset:15552
v_mfma_f32_16x16x32_bf16 a[20:23],v[124:127],a[180:183],a[20:23]
ds_read_b64_tr_b16 v[104:105],v1 offset:20672
v_mfma_f32_16x16x32_bf16 a[20:23],v[120:123],a[164:167],a[20:23]
ds_read_b64_tr_b16 v[106:107],v1 offset:25792
ds_read_b64_tr_b16 v[108:109],v1 offset:30912
v_mfma_f32_16x16x32_bf16 a[20:23],v[116:119],a[148:151],a[20:23]
ds_read_b64_tr_b16 v[110:111],v1 offset:36032
v_add_u32 v1,0xfffec000,v1
v_mfma_f32_16x16x32_bf16 a[20:23],v[112:115],a[132:135],a[20:23]
v_accvgpr_read_b32 v20,a16
v_accvgpr_read_b32 v21,a17
v_mfma_f32_16x16x32_bf16 a[24:27],v[112:115],a[136:139],a[24:27]
v_accvgpr_read_b32 v22,a18
v_accvgpr_read_b32 v23,a19
v_mfma_f32_16x16x32_bf16 a[24:27],v[116:119],a[152:155],a[24:27]
v_cvt_pk_bf16_f32 v18,v20,v21
v_cvt_pk_bf16_f32 v19,v22,v23
v_mfma_f32_16x16x32_bf16 a[24:27],v[120:123],a[168:171],a[24:27]
buffer_store_dwordx4 v[16:19],v6,s[36:39],s76 offen sc1
v_mfma_f32_16x16x32_bf16 a[24:27],v[124:127],a[184:187],a[24:27]
v_accvgpr_read_b32 v12,a20
v_accvgpr_read_b32 v13,a21
v_mfma_f32_16x16x32_bf16 a[28:31],v[124:127],a[188:191],a[28:31]
v_accvgpr_read_b32 v14,a22
v_accvgpr_read_b32 v15,a23
v_mfma_f32_16x16x32_bf16 a[28:31],v[120:123],a[172:175],a[28:31]
v_cvt_pk_bf16_f32 v10,v12,v13
v_cvt_pk_bf16_f32 v11,v14,v15
v_mfma_f32_16x16x32_bf16 a[28:31],v[116:119],a[156:159],a[28:31]
v_accvgpr_read_b32 v16,a8
v_accvgpr_read_b32 v17,a9
v_mfma_f32_16x16x32_bf16 a[28:31],v[112:115],a[140:143],a[28:31]
v_accvgpr_read_b32 v18,a10
v_accvgpr_read_b32 v19,a11
v_mfma_f32_16x16x32_bf16 v[128:131],v[64:67],v[32:35],v[128:131]
v_accvgpr_read_b32 v20,a24
v_accvgpr_read_b32 v21,a25
v_mfma_f32_16x16x32_bf16 v[128:131],v[68:71],v[36:39],v[128:131]
s_nop 0
L_pd18_%=:
s_cbranch_execz L_trtg2F_%=
v_accvgpr_read_b32 v22,a26
v_accvgpr_read_b32 v23,a27
v_mfma_f32_16x16x32_bf16 v[128:131],v[72:75],v[40:43],v[128:131]
v_cvt_pk_bf16_f32 v16,v16,v17
v_cvt_pk_bf16_f32 v17,v18,v19
v_mfma_f32_16x16x32_bf16 v[128:131],v[76:79],v[44:47],v[128:131]
v_cvt_pk_bf16_f32 v18,v20,v21
v_cvt_pk_bf16_f32 v19,v22,v23
v_mfma_f32_16x16x32_bf16 v[136:139],v[76:79],v[180:183],v[136:139]
ds_read_b64_tr_b16 v[112:113],v2 offset:224
ds_read_b64_tr_b16 v[114:115],v2 offset:5344
v_mfma_f32_16x16x32_bf16 v[136:139],v[72:75],v[164:167],v[136:139]
buffer_store_dwordx4 v[8:11],v6,s[36:39],s76 offen offset:1024 sc1
v_mfma_f32_16x16x32_bf16 v[136:139],v[68:71],v[148:151],v[136:139]
ds_read_b64_tr_b16 v[116:117],v2 offset:10464
ds_read_b64_tr_b16 v[118:119],v2 offset:15584
v_mfma_f32_16x16x32_bf16 v[136:139],v[64:67],v[132:135],v[136:139]
ds_read_b64_tr_b16 v[120:121],v2 offset:20704
v_accvgpr_read_b32 v8,a12
v_mfma_f32_16x16x32_bf16 v[144:147],v[64:67],v[196:199],v[144:147]
ds_read_b64_tr_b16 v[122:123],v2 offset:25824
v_accvgpr_read_b32 v9,a13
v_mfma_f32_16x16x32_bf16 v[144:147],v[68:71],v[212:215],v[144:147]
ds_read_b64_tr_b16 v[124:125],v2 offset:30944
v_accvgpr_read_b32 v10,a14
v_mfma_f32_16x16x32_bf16 v[144:147],v[72:75],v[228:231],v[144:147]
ds_read_b64_tr_b16 v[126:127],v2 offset:36064
v_accvgpr_read_b32 v11,a15
v_mfma_f32_16x16x32_bf16 v[144:147],v[76:79],v[244:247],v[144:147]
v_accvgpr_read_b32 v12,a28
v_accvgpr_read_b32 v13,a29
v_mfma_f32_16x16x32_bf16 v[152:155],v[76:79],v[188:191],v[152:155]
v_accvgpr_read_b32 v14,a30
v_accvgpr_read_b32 v15,a31
v_mfma_f32_16x16x32_bf16 v[152:155],v[72:75],v[172:175],v[152:155]
v_cvt_pk_bf16_f32 v8,v8,v9
v_cvt_pk_bf16_f32 v9,v10,v11
v_mfma_f32_16x16x32_bf16 v[152:155],v[68:71],v[156:159],v[152:155]
v_cvt_pk_bf16_f32 v10,v12,v13
v_cvt_pk_bf16_f32 v11,v14,v15
v_mfma_f32_16x16x32_bf16 v[152:155],v[64:67],v[140:143],v[152:155]
s_waitcnt lgkmcnt(3)
ds_read_b64_tr_b16 v[64:65],v1 offset:256
ds_read_b64_tr_b16 v[66:67],v1 offset:5376
v_mfma_f32_16x16x32_bf16 v[160:163],v[80:83],v[32:35],v[160:163]
buffer_store_dwordx4 v[16:19],v6,s[36:39],s76 offen offset:2048 sc1
v_mfma_f32_16x16x32_bf16 v[160:163],v[84:87],v[36:39],v[160:163]
ds_read_b64_tr_b16 v[68:69],v1 offset:10496
ds_read_b64_tr_b16 v[70:71],v1 offset:15616
v_mfma_f32_16x16x32_bf16 v[160:163],v[88:91],v[40:43],v[160:163]
ds_read_b64_tr_b16 v[72:73],v1 offset:20736
v_add_u32 v2,0xfffec000,v2
v_mfma_f32_16x16x32_bf16 v[160:163],v[92:95],v[44:47],v[160:163]
ds_read_b64_tr_b16 v[74:75],v1 offset:25856
v_mov_b32 v20,s52
v_mfma_f32_16x16x32_bf16 v[168:171],v[92:95],v[180:183],v[168:171]
ds_read_b64_tr_b16 v[76:77],v1 offset:30976
v_mov_b32 v21,s53
v_mfma_f32_16x16x32_bf16 v[168:171],v[88:91],v[164:167],v[168:171]
ds_read_b64_tr_b16 v[78:79],v1 offset:36096
v_add_u32 v1,0xffff6000,v1
v_mfma_f32_16x16x32_bf16 v[168:171],v[84:87],v[148:151],v[168:171]
v_lshlrev_b32 v22,20,v6
v_mfma_f32_16x16x32_bf16 v[168:171],v[80:83],v[132:135],v[168:171]
v_mfma_f32_16x16x32_bf16 v[176:179],v[80:83],v[196:199],v[176:179]
v_mfma_f32_16x16x32_bf16 v[176:179],v[84:87],v[212:215],v[176:179]
v_mfma_f32_16x16x32_bf16 v[176:179],v[88:91],v[228:231],v[176:179]
v_mfma_f32_16x16x32_bf16 v[176:179],v[92:95],v[244:247],v[176:179]
buffer_store_dwordx4 v[8:11],v28,s[36:39],s76 offen offset:3072 sc1
v_mfma_f32_16x16x32_bf16 v[184:187],v[92:95],v[188:191],v[184:187]
ds_read_b64_tr_b16 v[92:93],v2 offset:31008
ds_read_b64_tr_b16 v[94:95],v2 offset:36128
v_mfma_f32_16x16x32_bf16 v[184:187],v[88:91],v[172:175],v[184:187]
ds_read_b64_tr_b16 v[88:89],v2 offset:20768
v_mfma_f32_16x16x32_bf16 v[184:187],v[84:87],v[156:159],v[184:187]
ds_read_b64_tr_b16 v[84:85],v2 offset:10528
v_mfma_f32_16x16x32_bf16 v[184:187],v[80:83],v[140:143],v[184:187]
ds_read_b64_tr_b16 v[80:81],v2 offset:288
)A"
R"A(v_mfma_f32_16x16x32_bf16 v[128:131],v[96:99],a[128:131],v[128:131]
ds_read_b64_tr_b16 v[82:83],v2 offset:5408
ds_read_b64_tr_b16 v[86:87],v2 offset:15648
v_mfma_f32_16x16x32_bf16 v[128:131],v[100:103],a[144:147],v[128:131]
ds_read_b64_tr_b16 v[90:91],v2 offset:25888
v_add_u32 v2,0xffff6000,v2
v_mfma_f32_16x16x32_bf16 v[128:131],v[104:107],a[160:163],v[128:131]
v_mfma_f32_16x16x32_bf16 v[128:131],v[108:111],a[176:179],v[128:131]
v_mfma_f32_16x16x32_bf16 v[136:139],v[108:111],a[180:183],v[136:139]
v_mfma_f32_16x16x32_bf16 v[136:139],v[104:107],a[164:167],v[136:139]
v_mfma_f32_16x16x32_bf16 v[136:139],v[100:103],a[148:151],v[136:139]
v_mfma_f32_16x16x32_bf16 v[136:139],v[96:99],a[132:135],v[136:139]
v_mfma_f32_16x16x32_bf16 v[144:147],v[96:99],a[136:139],v[144:147]
v_mfma_f32_16x16x32_bf16 v[144:147],v[100:103],a[152:155],v[144:147]
v_mfma_f32_16x16x32_bf16 v[144:147],v[104:107],a[168:171],v[144:147]
v_mfma_f32_16x16x32_bf16 v[144:147],v[108:111],a[184:187],v[144:147]
v_cvt_pk_bf16_f32 v128,v128,v129
v_cvt_pk_bf16_f32 v129,v130,v131
v_mfma_f32_16x16x32_bf16 v[152:155],v[108:111],a[188:191],v[152:155]
ds_read_b64_tr_b16 v[108:109],v1 offset:30976
ds_read_b64_tr_b16 v[110:111],v1 offset:36096
v_mfma_f32_16x16x32_bf16 v[152:155],v[104:107],a[172:175],v[152:155]
v_cvt_pk_bf16_f32 v136,v136,v137
v_cvt_pk_bf16_f32 v137,v138,v139
v_mfma_f32_16x16x32_bf16 v[152:155],v[100:103],a[156:159],v[152:155]
v_cvt_pk_bf16_f32 v144,v144,v145
v_cvt_pk_bf16_f32 v145,v146,v147
v_mfma_f32_16x16x32_bf16 v[152:155],v[96:99],a[140:143],v[152:155]
ds_read_b64_tr_b16 v[96:97],v1 offset:256
ds_read_b64_tr_b16 v[98:99],v1 offset:5376
v_mfma_f32_16x16x32_bf16 v[160:163],v[112:115],a[128:131],v[160:163]
ds_read_b64_tr_b16 v[100:101],v1 offset:10496
v_mfma_f32_16x16x32_bf16 v[160:163],v[116:119],a[144:147],v[160:163]
s_waitcnt lgkmcnt(5)
ds_read_b64_tr_b16 v[102:103],v1 offset:15616
v_mfma_f32_16x16x32_bf16 v[160:163],v[120:123],a[160:163],v[160:163]
ds_read_b64_tr_b16 v[104:105],v1 offset:20736
v_mfma_f32_16x16x32_bf16 v[160:163],v[124:127],a[176:179],v[160:163]
v_cvt_pk_bf16_f32 v152,v152,v153
v_cvt_pk_bf16_f32 v153,v154,v155
v_mfma_f32_16x16x32_bf16 v[168:171],v[124:127],a[180:183],v[168:171]
ds_read_b64_tr_b16 v[106:107],v1 offset:25856
v_add_u32 v1,0x1e000,v1
v_mfma_f32_16x16x32_bf16 v[168:171],v[120:123],a[164:167],v[168:171]
v_mfma_f32_16x16x32_bf16 v[168:171],v[116:119],a[148:151],v[168:171]
v_mfma_f32_16x16x32_bf16 v[168:171],v[112:115],a[132:135],v[168:171]
v_cvt_pk_bf16_f32 v130,v160,v161
v_cvt_pk_bf16_f32 v131,v162,v163
v_mfma_f32_16x16x32_bf16 v[176:179],v[112:115],a[136:139],v[176:179]
buffer_store_dwordx4 v[128:131],v6,s[36:39],s79 offen sc1
v_mfma_f32_16x16x32_bf16 v[176:179],v[116:119],a[152:155],v[176:179]
v_mfma_f32_16x16x32_bf16 v[176:179],v[120:123],a[168:171],v[176:179]
v_mfma_f32_16x16x32_bf16 v[176:179],v[124:127],a[184:187],v[176:179]
v_mfma_f32_16x16x32_bf16 v[184:187],v[124:127],a[188:191],v[184:187]
v_cvt_pk_bf16_f32 v138,v168,v169
v_cvt_pk_bf16_f32 v139,v170,v171
v_mfma_f32_16x16x32_bf16 v[184:187],v[120:123],a[172:175],v[184:187]
buffer_store_dwordx4 v[136:139],v6,s[36:39],s80 offen sc1
v_mfma_f32_16x16x32_bf16 v[184:187],v[116:119],a[156:159],v[184:187]
ds_read_b64_tr_b16 v[116:117],v2 offset:10528
ds_read_b64_tr_b16 v[118:119],v2 offset:15648
v_mfma_f32_16x16x32_bf16 v[184:187],v[112:115],a[140:143],v[184:187]
v_cvt_pk_bf16_f32 v146,v176,v177
v_cvt_pk_bf16_f32 v147,v178,v179
s_waitcnt vmcnt(6)
s_barrier
v_mfma_f32_16x16x32_bf16 v[192:195],v[64:67],a[68:71],v[192:195]
buffer_store_dwordx4 v[144:147],v6,s[36:39],s81 offen sc1
v_mfma_f32_16x16x32_bf16 v[192:195],v[68:71],a[76:79],v[192:195]
ds_read_b64_tr_b16 v[112:113],v2 offset:288
ds_read_b64_tr_b16 v[114:115],v2 offset:5408
v_mfma_f32_16x16x32_bf16 v[192:195],v[72:75],a[84:87],v[192:195]
buffer_store_dwordx2 v[58:59],v60,s[44:47],s18 offen offset:2312 sc1
v_mfma_f32_16x16x32_bf16 v[192:195],v[76:79],a[92:95],v[192:195]
v_cvt_pk_bf16_f32 v154,v184,v185
v_cvt_pk_bf16_f32 v155,v186,v187
v_mfma_f32_16x16x32_bf16 v[200:203],v[76:79],a[124:127],v[200:203]
buffer_store_dwordx4 v[152:155],v28,s[36:39],s82 offen sc1
v_mfma_f32_16x16x32_bf16 v[200:203],v[72:75],a[116:119],v[200:203]
ds_read_b64_tr_b16 v[120:121],v2 offset:20768
ds_read_b64_tr_b16 v[122:123],v2 offset:25888
v_mfma_f32_16x16x32_bf16 v[200:203],v[68:71],a[108:111],v[200:203]
ds_read_b64_tr_b16 v[124:125],v2 offset:31008
v_mfma_f32_16x16x32_bf16 v[200:203],v[64:67],a[100:103],v[200:203]
ds_read_b64_tr_b16 v[126:127],v2 offset:36128
v_add_u32 v2,0x1e000,v2
v_mfma_f32_16x16x32_bf16 v[208:211],v[64:67],a[64:67],v[208:211]
v_mfma_f32_16x16x32_bf16 v[208:211],v[68:71],a[72:75],v[208:211]
v_mfma_f32_16x16x32_bf16 v[208:211],v[72:75],a[80:83],v[208:211]
v_mfma_f32_16x16x32_bf16 v[208:211],v[76:79],a[88:91],v[208:211]
v_mfma_f32_16x16x32_bf16 v[216:219],v[76:79],a[120:123],v[216:219]
ds_read_b64_tr_b16 v[76:77],v1 offset:30976
ds_read_b64_tr_b16 v[78:79],v1 offset:36096
v_mfma_f32_16x16x32_bf16 v[216:219],v[72:75],a[112:115],v[216:219]
ds_read_b64_tr_b16 v[72:73],v1 offset:20736
v_mfma_f32_16x16x32_bf16 v[216:219],v[68:71],a[104:107],v[216:219]
ds_read_b64_tr_b16 v[68:69],v1 offset:10496
v_mfma_f32_16x16x32_bf16 v[216:219],v[64:67],a[96:99],v[216:219]
ds_read_b64_tr_b16 v[64:65],v1 offset:256
v_mfma_f32_16x16x32_bf16 v[224:227],v[80:83],a[68:71],v[224:227]
ds_read_b64_tr_b16 v[66:67],v1 offset:5376
ds_read_b64_tr_b16 v[70:71],v1 offset:15616
v_mfma_f32_16x16x32_bf16 v[224:227],v[84:87],a[76:79],v[224:227]
ds_read_b64_tr_b16 v[74:75],v1 offset:25856
v_mfma_f32_16x16x32_bf16 v[224:227],v[88:91],a[84:87],v[224:227]
v_mfma_f32_16x16x32_bf16 v[224:227],v[92:95],a[92:95],v[224:227]
v_mfma_f32_16x16x32_bf16 v[232:235],v[92:95],a[124:127],v[232:235]
v_mfma_f32_16x16x32_bf16 v[232:235],v[88:91],a[116:119],v[232:235]
v_mfma_f32_16x16x32_bf16 v[232:235],v[84:87],a[108:111],v[232:235]
v_mfma_f32_16x16x32_bf16 v[232:235],v[80:83],a[100:103],v[232:235]
v_mfma_f32_16x16x32_bf16 v[240:243],v[80:83],a[64:67],v[240:243]
v_mfma_f32_16x16x32_bf16 v[240:243],v[84:87],a[72:75],v[240:243]
v_mfma_f32_16x16x32_bf16 v[240:243],v[88:91],a[80:83],v[240:243]
v_mfma_f32_16x16x32_bf16 v[240:243],v[92:95],a[88:91],v[240:243]
v_mfma_f32_16x16x32_bf16 v[248:251],v[92:95],a[120:123],v[248:251]
ds_read_b64_tr_b16 v[92:93],v2 offset:31008
ds_read_b64_tr_b16 v[94:95],v2 offset:36128
v_mfma_f32_16x16x32_bf16 v[248:251],v[88:91],a[112:115],v[248:251]
ds_read_b64_tr_b16 v[88:89],v2 offset:20768
v_mfma_f32_16x16x32_bf16 v[248:251],v[84:87],a[104:107],v[248:251]
ds_read_b64_tr_b16 v[84:85],v2 offset:10528
v_mfma_f32_16x16x32_bf16 v[248:251],v[80:83],a[96:99],v[248:251]
s_waitcnt lgkmcnt(4)
ds_read_b64_tr_b16 v[80:81],v2 offset:288
v_mfma_f32_16x16x32_bf16 v[192:195],v[96:99],v[32:35],v[192:195]
ds_read_b64_tr_b16 v[82:83],v2 offset:5408
ds_read_b64_tr_b16 v[86:87],v2 offset:15648
v_mfma_f32_16x16x32_bf16 v[192:195],v[100:103],v[36:39],v[192:195]
ds_read_b64_tr_b16 v[90:91],v2 offset:25888
v_mfma_f32_16x16x32_bf16 v[192:195],v[104:107],v[40:43],v[192:195]
v_mfma_f32_16x16x32_bf16 v[192:195],v[108:111],v[44:47],v[192:195]
v_mfma_f32_16x16x32_bf16 v[200:203],v[108:111],v[180:183],v[200:203]
v_mfma_f32_16x16x32_bf16 v[200:203],v[104:107],v[164:167],v[200:203]
v_mfma_f32_16x16x32_bf16 v[200:203],v[100:103],v[148:151],v[200:203]
v_mfma_f32_16x16x32_bf16 v[200:203],v[96:99],v[132:135],v[200:203]
v_mfma_f32_16x16x32_bf16 v[208:211],v[96:99],v[196:199],v[208:211]
v_mfma_f32_16x16x32_bf16 v[208:211],v[100:103],v[212:215],v[208:211]
v_mfma_f32_16x16x32_bf16 v[208:211],v[104:107],v[228:231],v[208:211]
v_mfma_f32_16x16x32_bf16 v[208:211],v[108:111],v[244:247],v[208:211]
v_mfma_f32_16x16x32_bf16 v[216:219],v[108:111],v[188:191],v[216:219]
v_mfma_f32_16x16x32_bf16 v[216:219],v[104:107],v[172:175],v[216:219]
v_mfma_f32_16x16x32_bf16 v[216:219],v[100:103],v[156:159],v[216:219]
v_mfma_f32_16x16x32_bf16 v[216:219],v[96:99],v[140:143],v[216:219]
v_mfma_f32_16x16x32_bf16 v[224:227],v[112:115],v[32:35],v[224:227]
v_mfma_f32_16x16x32_bf16 v[224:227],v[116:119],v[36:39],v[224:227]
v_mfma_f32_16x16x32_bf16 v[224:227],v[120:123],v[40:43],v[224:227]
v_mfma_f32_16x16x32_bf16 v[224:227],v[124:127],v[44:47],v[224:227]
v_mfma_f32_16x16x32_bf16 v[232:235],v[124:127],v[180:183],v[232:235]
v_mfma_f32_16x16x32_bf16 v[232:235],v[120:123],v[164:167],v[232:235]
v_mfma_f32_16x16x32_bf16 v[232:235],v[116:119],v[148:151],v[232:235]
v_mfma_f32_16x16x32_bf16 v[232:235],v[112:115],v[132:135],v[232:235]
v_mfma_f32_16x16x32_bf16 v[240:243],v[112:115],v[196:199],v[240:243]
v_mfma_f32_16x16x32_bf16 v[240:243],v[116:119],v[212:215],v[240:243]
v_mfma_f32_16x16x32_bf16 v[240:243],v[120:123],v[228:231],v[240:243]
v_mfma_f32_16x16x32_bf16 v[240:243],v[124:127],v[244:247],v[240:243]
v_mfma_f32_16x16x32_bf16 v[248:251],v[124:127],v[188:191],v[248:251]
v_mfma_f32_16x16x32_bf16 v[248:251],v[120:123],v[172:175],v[248:251]
v_mfma_f32_16x16x32_bf16 v[248:251],v[116:119],v[156:159],v[248:251]
v_mfma_f32_16x16x32_bf16 v[248:251],v[112:115],v[140:143],v[248:251]
s_waitcnt vmcnt(0)
s_barrier
v_mfma_f32_16x16x32_bf16 v[192:195],v[64:67],a[128:131],v[192:195]
buffer_store_dwordx2 v[58:59],v60,s[44:47],s18 offen offset:2304 sc1
v_mfma_f32_16x16x32_bf16 v[192:195],v[68:71],a[144:147],v[192:195]
v_mfma_f32_16x16x32_bf16 v[192:195],v[72:75],a[160:163],v[192:195]
v_mfma_f32_16x16x32_bf16 v[192:195],v[76:79],a[176:179],v[192:195]
v_mfma_f32_16x16x32_bf16 v[200:203],v[76:79],a[180:183],v[200:203]
v_mfma_f32_16x16x32_bf16 v[200:203],v[72:75],a[164:167],v[200:203]
v_mfma_f32_16x16x32_bf16 v[200:203],v[68:71],a[148:151],v[200:203]
v_mfma_f32_16x16x32_bf16 v[200:203],v[64:67],a[132:135],v[200:203]
v_mfma_f32_16x16x32_bf16 v[208:211],v[64:67],a[136:139],v[208:211]
v_mfma_f32_16x16x32_bf16 v[208:211],v[68:71],a[152:155],v[208:211]
v_mfma_f32_16x16x32_bf16 v[208:211],v[72:75],a[168:171],v[208:211]
v_mfma_f32_16x16x32_bf16 v[208:211],v[76:79],a[184:187],v[208:211]
v_mfma_f32_16x16x32_bf16 v[216:219],v[76:79],a[188:191],v[216:219]
v_mfma_f32_16x16x32_bf16 v[216:219],v[72:75],a[172:175],v[216:219]
v_mfma_f32_16x16x32_bf16 v[216:219],v[68:71],a[156:159],v[216:219]
v_mfma_f32_16x16x32_bf16 v[216:219],v[64:67],a[140:143],v[216:219]
s_waitcnt lgkmcnt(0)
v_mfma_f32_16x16x32_bf16 v[224:227],v[80:83],a[128:131],v[224:227]
buffer_load_dwordx2 v[62:63],v61,s[44:47],s91 offen offset:2312 sc1
v_mfma_f32_16x16x32_bf16 v[224:227],v[84:87],a[144:147],v[224:227]
v_mfma_f32_16x16x32_bf16 v[224:227],v[88:91],a[160:163],v[224:227]
v_mfma_f32_16x16x32_bf16 v[224:227],v[92:95],a[176:179],v[224:227]
v_mfma_f32_16x16x32_bf16 v[232:235],v[92:95],a[180:183],v[232:235]
v_mfma_f32_16x16x32_bf16 v[232:235],v[88:91],a[164:167],v[232:235]
v_mfma_f32_16x16x32_bf16 v[232:235],v[84:87],a[148:151],v[232:235]
v_mfma_f32_16x16x32_bf16 v[232:235],v[80:83],a[132:135],v[232:235]
v_mfma_f32_16x16x32_bf16 v[240:243],v[80:83],a[136:139],v[240:243]
v_mfma_f32_16x16x32_bf16 v[240:243],v[84:87],a[152:155],v[240:243]
v_mfma_f32_16x16x32_bf16 v[240:243],v[88:91],a[168:171],v[240:243]
v_mfma_f32_16x16x32_bf16 v[240:243],v[92:95],a[184:187],v[240:243]
v_mfma_f32_16x16x32_bf16 v[248:251],v[92:95],a[188:191],v[248:251]
v_mfma_f32_16x16x32_bf16 v[248:251],v[88:91],a[172:175],v[248:251]
v_mfma_f32_16x16x32_bf16 v[248:251],v[84:87],a[156:159],v[248:251]
v_mfma_f32_16x16x32_bf16 v[248:251],v[80:83],a[140:143],v[248:251]
s_waitcnt vmcnt(0)
v_cmp_ne_u32 s[56:57],s52,v62
v_cmp_ne_u32 s[58:59],s53,v63
s_nop 0
s_or_b64 s[56:57],s[56:57],s[58:59]
s_cbranch_scc1 L_fr4_%=
L_fo4_%=:
buffer_load_dwordx4 v[64:67],v6,s[36:39],s66 offen sc1
buffer_load_dwordx4 v[68:71],v6,s[36:39],s67 offen sc1
buffer_load_dwordx4 v[72:75],v6,s[36:39],s66 offen offset:1024 sc1
buffer_load_dwordx4 v[76:79],v6,s[36:39],s67 offen offset:1024 sc1
buffer_load_dwordx4 v[80:83],v6,s[36:39],s66 offen offset:2048 sc1
buffer_load_dwordx4 v[84:87],v6,s[36:39],s67 offen offset:2048 sc1
buffer_load_dwordx4 v[88:91],v28,s[36:39],s66 offen offset:3072 sc1
buffer_load_dwordx4 v[92:95],v28,s[36:39],s67 offen offset:3072 sc1
)A"
R"A(buffer_load_dwordx2 v[4:5],v3,s[44:47],s91 offen offset:2304 sc1
s_waitcnt vmcnt(8)
v_mfma_f32_16x16x32_bf16 v[192:195],v[48:51],v[64:67],v[192:195]
s_waitcnt vmcnt(7)
v_mfma_f32_16x16x32_bf16 v[192:195],v[48:51],v[68:71],v[192:195]
v_mfma_f32_16x16x32_bf16 v[224:227],v[52:55],v[64:67],v[224:227]
v_mfma_f32_16x16x32_bf16 v[224:227],v[52:55],v[68:71],v[224:227]
s_waitcnt vmcnt(6)
v_mfma_f32_16x16x32_bf16 v[200:203],v[48:51],v[72:75],v[200:203]
s_waitcnt vmcnt(5)
v_mfma_f32_16x16x32_bf16 v[200:203],v[48:51],v[76:79],v[200:203]
v_mfma_f32_16x16x32_bf16 v[232:235],v[52:55],v[72:75],v[232:235]
v_mfma_f32_16x16x32_bf16 v[232:235],v[52:55],v[76:79],v[232:235]
s_waitcnt vmcnt(4)
v_mfma_f32_16x16x32_bf16 v[208:211],v[48:51],v[80:83],v[208:211]
s_waitcnt vmcnt(3)
v_mfma_f32_16x16x32_bf16 v[208:211],v[48:51],v[84:87],v[208:211]
v_mfma_f32_16x16x32_bf16 v[240:243],v[52:55],v[80:83],v[240:243]
v_mfma_f32_16x16x32_bf16 v[240:243],v[52:55],v[84:87],v[240:243]
s_waitcnt vmcnt(2)
v_mfma_f32_16x16x32_bf16 v[216:219],v[48:51],v[88:91],v[216:219]
s_waitcnt vmcnt(1)
v_mfma_f32_16x16x32_bf16 v[216:219],v[48:51],v[92:95],v[216:219]
v_mfma_f32_16x16x32_bf16 v[248:251],v[52:55],v[88:91],v[248:251]
v_mfma_f32_16x16x32_bf16 v[248:251],v[52:55],v[92:95],v[248:251]
s_waitcnt vmcnt(0)
v_cmp_ne_u32 s[56:57],s52,v4
v_cmp_ne_u32 s[58:59],s53,v5
s_nop 0
s_or_b64 s[56:57],s[56:57],s[58:59]
s_cbranch_scc1 L_fr5_%=
L_fo5_%=:
buffer_load_dwordx4 v[96:99],v6,s[36:39],s68 offen sc1
buffer_load_dwordx4 v[100:103],v6,s[36:39],s68 offen offset:1024 sc1
buffer_load_dwordx4 v[104:107],v6,s[36:39],s68 offen offset:2048 sc1
buffer_load_dwordx4 v[108:111],v6,s[36:39],s68 offen offset:3072 sc1
buffer_load_dwordx4 v[112:115],v6,s[36:39],s65 offen sc1
buffer_load_dwordx4 v[116:119],v6,s[36:39],s65 offen offset:1024 sc1
buffer_load_dwordx4 v[120:123],v6,s[36:39],s65 offen offset:2048 sc1
buffer_load_dwordx4 v[124:127],v28,s[36:39],s65 offen offset:3072 sc1
s_waitcnt vmcnt(7)
v_mfma_f32_16x16x32_bf16 v[128:131],v[48:51],v[96:99],0
s_waitcnt vmcnt(6)
v_mfma_f32_16x16x32_bf16 v[128:131],v[48:51],v[100:103],v[128:131]
s_waitcnt vmcnt(5)
v_mfma_f32_16x16x32_bf16 v[128:131],v[48:51],v[104:107],v[128:131]
s_waitcnt vmcnt(4)
v_mfma_f32_16x16x32_bf16 v[128:131],v[48:51],v[108:111],v[128:131]
v_mfma_f32_16x16x32_bf16 v[160:163],v[52:55],v[96:99],0
v_mfma_f32_16x16x32_bf16 v[160:163],v[52:55],v[100:103],v[160:163]
v_mfma_f32_16x16x32_bf16 v[160:163],v[52:55],v[104:107],v[160:163]
v_mfma_f32_16x16x32_bf16 v[160:163],v[52:55],v[108:111],v[160:163]
s_waitcnt vmcnt(3)
v_mfma_f32_16x16x32_bf16 v[192:195],v[48:51],v[112:115],v[192:195]
v_mfma_f32_16x16x32_bf16 v[224:227],v[52:55],v[112:115],v[224:227]
s_waitcnt vmcnt(2)
v_mfma_f32_16x16x32_bf16 v[200:203],v[48:51],v[116:119],v[200:203]
v_mfma_f32_16x16x32_bf16 v[232:235],v[52:55],v[116:119],v[232:235]
s_waitcnt vmcnt(1)
v_cvt_pk_bf16_f32 v128,v128,v129
v_cvt_pk_bf16_f32 v129,v130,v131
v_mfma_f32_16x16x32_bf16 v[208:211],v[48:51],v[120:123],v[208:211]
v_mfma_f32_16x16x32_bf16 v[240:243],v[52:55],v[120:123],v[240:243]
s_waitcnt vmcnt(0)
v_cvt_pk_bf16_f32 v130,v160,v161
v_cvt_pk_bf16_f32 v131,v162,v163
v_mfma_f32_16x16x32_bf16 v[216:219],v[48:51],v[124:127],v[216:219]
v_cvt_pk_bf16_f32 v192,v192,v193
v_cvt_pk_bf16_f32 v193,v194,v195
v_cvt_pk_bf16_f32 v194,v224,v225
v_cvt_pk_bf16_f32 v195,v226,v227
v_cvt_pk_bf16_f32 v200,v200,v201
v_cvt_pk_bf16_f32 v201,v202,v203
v_mfma_f32_16x16x32_bf16 v[248:251],v[52:55],v[124:127],v[248:251]
v_permlane16_swap_b32 v128,v130
v_permlane16_swap_b32 v129,v131
v_cvt_pk_bf16_f32 v202,v232,v233
v_cvt_pk_bf16_f32 v203,v234,v235
v_cvt_pk_bf16_f32 v208,v208,v209
v_cvt_pk_bf16_f32 v209,v210,v211
v_cvt_pk_bf16_f32 v210,v240,v241
v_cvt_pk_bf16_f32 v211,v242,v243
buffer_store_dwordx4 v[128:131],v56,s[24:27],s83 offen offset:256 sc1 nt
v_permlane16_swap_b32 v192,v194
v_permlane16_swap_b32 v193,v195
buffer_store_dwordx4 v[192:195],v56,s[24:27],s87 offen sc1 nt
v_permlane16_swap_b32 v200,v202
v_permlane16_swap_b32 v201,v203
v_cvt_pk_bf16_f32 v216,v216,v217
v_cvt_pk_bf16_f32 v217,v218,v219
v_cvt_pk_bf16_f32 v218,v248,v249
v_cvt_pk_bf16_f32 v219,v250,v251
buffer_store_dwordx4 v[200:203],v56,s[24:27],s88 offen sc1 nt
v_permlane16_swap_b32 v208,v210
v_permlane16_swap_b32 v209,v211
buffer_store_dwordx4 v[208:211],v56,s[24:27],s89 offen sc1 nt
v_permlane16_swap_b32 v216,v218
v_permlane16_swap_b32 v217,v219
buffer_store_dwordx4 v[216:219],v56,s[24:27],s90 offen sc1 nt
s_lshl_b32 s69,s62,20
s_add_u32 s69,s69,s18
buffer_store_dword v20,v22,s[44:47],s69 offen sc1
L_geF_%=:
s_branch L_end_%=
.p2align 12
L_pd19_%=:
s_cbranch_execz L_trt1_%=
L_GS_%=:
L_gbS_%=:
s_waitcnt lgkmcnt(14)
v_mfma_f32_16x16x32_bf16 a[192:195],v[64:67],a[68:71],0
buffer_store_dwordx4 v[32:35],v6,s[40:43],s79 offen sc1
s_waitcnt lgkmcnt(12)
v_mfma_f32_16x16x32_bf16 a[192:195],v[68:71],a[76:79],a[192:195]
s_lshl_b32 s91,s63,8
v_bfe_u32 v3,v6,4,2
v_min_u32 v3,2,v3
s_waitcnt lgkmcnt(10)
v_mfma_f32_16x16x32_bf16 a[192:195],v[72:75],a[84:87],a[192:195]
s_add_u32 s87,s64,1
v_add3_u32 v3,v3,s64,1
v_and_b32 v3,3,v3
s_waitcnt lgkmcnt(8)
v_mfma_f32_16x16x32_bf16 a[192:195],v[76:79],a[92:95],a[192:195]
s_and_b32 s87,s87,3
ds_read_b64_tr_b16 v[96:97],v1 offset:64
v_lshlrev_b32 v3,6,v3
v_mfma_f32_16x16x32_bf16 a[196:199],v[76:79],a[124:127],0
s_lshl_b32 s87,s87,1
ds_read_b64_tr_b16 v[98:99],v1 offset:5184
ds_read_b64_tr_b16 v[100:101],v1 offset:10304
v_mfma_f32_16x16x32_bf16 a[196:199],v[72:75],a[116:119],a[196:199]
s_lshl_b32 s87,s87,16
ds_read_b64_tr_b16 v[102:103],v1 offset:15424
v_lshlrev_b32 v29,10,v6
v_mfma_f32_16x16x32_bf16 a[196:199],v[68:71],a[108:111],a[196:199]
s_add_u32 s88,s87,4096
ds_read_b64_tr_b16 v[104:105],v1 offset:20544
v_accvgpr_read_b32 v16,a24
v_mfma_f32_16x16x32_bf16 a[196:199],v[64:67],a[100:103],a[196:199]
s_add_u32 s89,s87,8192
ds_read_b64_tr_b16 v[106:107],v1 offset:25664
v_accvgpr_read_b32 v17,a25
v_mfma_f32_16x16x32_bf16 a[200:203],v[64:67],a[64:67],0
s_add_u32 s90,s87,12288
ds_read_b64_tr_b16 v[108:109],v1 offset:30784
ds_read_b64_tr_b16 v[110:111],v1 offset:35904
v_mfma_f32_16x16x32_bf16 a[200:203],v[68:71],a[72:75],a[200:203]
s_add_u32 m0,s86,122880
ds_read_b64_tr_b16 v[112:113],v2 offset:96
v_accvgpr_read_b32 v18,a26
v_mfma_f32_16x16x32_bf16 a[200:203],v[72:75],a[80:83],a[200:203]
s_add_u32 s83,s64,3
ds_read_b64_tr_b16 v[114:115],v2 offset:5216
v_accvgpr_read_b32 v19,a27
v_mfma_f32_16x16x32_bf16 a[200:203],v[76:79],a[88:91],a[200:203]
s_and_b32 s83,s83,3
ds_read_b64_tr_b16 v[116:117],v2 offset:10336
v_accvgpr_read_b32 v20,a28
v_mfma_f32_16x16x32_bf16 a[204:207],v[76:79],a[120:123],0
s_lshl_b32 s83,s83,1
ds_read_b64_tr_b16 v[118:119],v2 offset:15456
ds_read_b64_tr_b16 v[120:121],v2 offset:20576
v_mfma_f32_16x16x32_bf16 a[204:207],v[72:75],a[112:115],a[204:207]
s_mul_i32 s83,s83,163840
ds_read_b64_tr_b16 v[122:123],v2 offset:25696
v_accvgpr_read_b32 v21,a29
v_mfma_f32_16x16x32_bf16 a[204:207],v[68:71],a[104:107],a[204:207]
s_add_u32 s83,s83,s85
ds_read_b64_tr_b16 v[124:125],v2 offset:30816
v_accvgpr_read_b32 v22,a30
v_mfma_f32_16x16x32_bf16 a[204:207],v[64:67],a[96:99],a[204:207]
buffer_store_dwordx4 v[36:39],v6,s[40:43],s80 offen sc1
s_waitcnt lgkmcnt(9)
v_mfma_f32_16x16x32_bf16 a[208:211],v[80:83],a[68:71],0
s_add_u32 s84,s83,20480
ds_read_b64_tr_b16 v[126:127],v2 offset:35936
ds_read_b64_tr_b16 v[64:65],v1 offset:128
v_mfma_f32_16x16x32_bf16 a[208:211],v[84:87],a[76:79],a[208:211]
s_lshl_b32 s66,s61,2
ds_read_b64_tr_b16 v[66:67],v1 offset:5248
v_accvgpr_read_b32 v23,a31
v_mfma_f32_16x16x32_bf16 a[208:211],v[88:91],a[84:87],a[208:211]
s_add_u32 s66,s66,s64
ds_read_b64_tr_b16 v[68:69],v1 offset:10368
v_mul_f32 v16,v16,v49
v_mfma_f32_16x16x32_bf16 a[208:211],v[92:95],a[92:95],a[208:211]
s_mul_i32 s66,s66,262144
ds_read_b64_tr_b16 v[70:71],v1 offset:15488
v_mul_f32 v17,v17,v49
v_mfma_f32_16x16x32_bf16 a[212:215],v[92:95],a[124:127],0
s_waitcnt vmcnt(21)
buffer_load_dwordx2 v[4:5],v3,s[44:47],s91 offen offset:256 sc1
v_mfma_f32_16x16x32_bf16 a[212:215],v[88:91],a[116:119],a[212:215]
s_add_u32 s66,s66,134217728
ds_read_b64_tr_b16 v[72:73],v1 offset:20608
ds_read_b64_tr_b16 v[74:75],v1 offset:25728
v_mfma_f32_16x16x32_bf16 a[212:215],v[84:87],a[108:111],a[212:215]
s_mov_b32 s38,262144
ds_read_b64_tr_b16 v[76:77],v1 offset:30848
v_mul_f32 v18,v18,v49
v_mfma_f32_16x16x32_bf16 a[212:215],v[80:83],a[100:103],a[212:215]
s_mov_b32 s39,0x20000
ds_read_b64_tr_b16 v[78:79],v1 offset:35968
v_mul_f32 v19,v19,v49
v_mfma_f32_16x16x32_bf16 a[216:219],v[80:83],a[64:67],0
s_add_u32 s74,s63,1
v_mul_f32 v20,v20,v49
v_mul_f32 v21,v21,v49
v_mfma_f32_16x16x32_bf16 a[216:219],v[84:87],a[72:75],a[216:219]
s_and_b32 s74,s74,3
v_mul_f32 v22,v22,v49
v_mul_f32 v23,v23,v49
v_mfma_f32_16x16x32_bf16 a[216:219],v[88:91],a[80:83],a[216:219]
s_lshl_b32 s74,s74,2
v_cvt_pk_bf16_f32 v44,v16,v17
v_cvt_pk_bf16_f32 v45,v18,v19
v_mfma_f32_16x16x32_bf16 a[216:219],v[92:95],a[88:91],a[216:219]
s_add_u32 s74,s74,s60
v_cvt_pk_bf16_f32 v46,v20,v21
v_cvt_pk_bf16_f32 v47,v22,v23
v_mfma_f32_16x16x32_bf16 a[220:223],v[92:95],a[120:123],0
s_mul_i32 s74,s74,16384
ds_read_b64_tr_b16 v[92:93],v2 offset:30880
ds_read_b64_tr_b16 v[94:95],v2 offset:36000
v_mfma_f32_16x16x32_bf16 a[220:223],v[88:91],a[112:115],a[220:223]
s_add_u32 s74,s74,8192
ds_read_b64_tr_b16 v[88:89],v2 offset:20640
v_mul_f32 v132,v132,v53
v_mfma_f32_16x16x32_bf16 a[220:223],v[84:87],a[104:107],a[220:223]
buffer_store_dwordx4 v[40:43],v6,s[40:43],s81 offen sc1
v_mfma_f32_16x16x32_bf16 a[220:223],v[80:83],a[96:99],a[220:223]
s_add_u32 s75,s63,2
ds_read_b64_tr_b16 v[80:81],v2 offset:160
ds_read_b64_tr_b16 v[82:83],v2 offset:5280
v_mfma_f32_16x16x32_bf16 a[224:227],v[96:99],a[68:71],0
s_and_b32 s75,s75,3
ds_read_b64_tr_b16 v[84:85],v2 offset:10400
v_mul_f32 v133,v133,v53
v_mfma_f32_16x16x32_bf16 a[224:227],v[100:103],a[76:79],a[224:227]
s_lshl_b32 s75,s75,2
ds_read_b64_tr_b16 v[86:87],v2 offset:15520
v_mul_f32 v134,v134,v53
v_mfma_f32_16x16x32_bf16 a[224:227],v[104:107],a[84:87],a[224:227]
s_add_u32 s75,s75,s60
ds_read_b64_tr_b16 v[90:91],v2 offset:25760
v_mul_f32 v135,v135,v53
s_waitcnt lgkmcnt(8)
v_mfma_f32_16x16x32_bf16 a[224:227],v[108:111],a[92:95],a[224:227]
s_mul_i32 s75,s75,16384
v_mul_f32 v140,v140,v53
v_mul_f32 v141,v141,v53
v_mfma_f32_16x16x32_bf16 a[228:231],v[108:111],a[124:127],0
s_add_u32 s75,s75,4096
v_mul_f32 v142,v142,v53
v_mul_f32 v143,v143,v53
v_mfma_f32_16x16x32_bf16 a[228:231],v[104:107],a[116:119],a[228:231]
s_add_u32 s76,s63,3
v_cvt_pk_bf16_f32 v132,v132,v133
v_cvt_pk_bf16_f32 v133,v134,v135
v_mfma_f32_16x16x32_bf16 a[228:231],v[100:103],a[108:111],a[228:231]
s_and_b32 s76,s76,3
v_cvt_pk_bf16_f32 v134,v140,v141
v_cvt_pk_bf16_f32 v135,v142,v143
v_mfma_f32_16x16x32_bf16 a[228:231],v[96:99],a[100:103],a[228:231]
s_lshl_b32 s76,s76,2
v_mul_f32 v148,v148,v53
v_mul_f32 v149,v149,v53
v_mfma_f32_16x16x32_bf16 a[232:235],v[96:99],a[64:67],0
s_add_u32 s76,s76,s60
v_mul_f32 v150,v150,v53
v_mul_f32 v151,v151,v53
v_mfma_f32_16x16x32_bf16 a[232:235],v[100:103],a[72:75],a[232:235]
s_mul_i32 s76,s76,16384
v_mul_f32 v156,v156,v53
v_mul_f32 v157,v157,v53
v_mfma_f32_16x16x32_bf16 a[232:235],v[104:107],a[80:83],a[232:235]
s_add_u32 s76,s76,0
v_mul_f32 v158,v158,v53
v_mul_f32 v159,v159,v53
v_mfma_f32_16x16x32_bf16 a[232:235],v[108:111],a[88:91],a[232:235]
s_lshl_b32 s67,s63,10
v_cvt_pk_bf16_f32 v148,v148,v149
v_cvt_pk_bf16_f32 v149,v150,v151
v_mfma_f32_16x16x32_bf16 a[236:239],v[108:111],a[120:123],0
s_mov_b32 s26,307200
ds_read_b64_tr_b16 v[108:109],v1 offset:30912
ds_read_b64_tr_b16 v[110:111],v1 offset:36032
v_mfma_f32_16x16x32_bf16 a[236:239],v[104:107],a[112:115],a[236:239]
buffer_store_dwordx4 v[44:47],v6,s[40:43],s82 offen sc1
v_mfma_f32_16x16x32_bf16 a[236:239],v[100:103],a[104:107],a[236:239]
s_mov_b32 s27,0x20000
ds_read_b64_tr_b16 v[100:101],v1 offset:10432
ds_read_b64_tr_b16 v[102:103],v1 offset:15552
v_mfma_f32_16x16x32_bf16 a[236:239],v[96:99],a[96:99],a[236:239]
s_lshl_b32 s77,s63,2
ds_read_b64_tr_b16 v[96:97],v1 offset:192
v_cvt_pk_bf16_f32 v150,v156,v157
v_mfma_f32_16x16x32_bf16 a[240:243],v[112:115],a[68:71],0
)A"
R"A(s_add_u32 s77,s77,s60
ds_read_b64_tr_b16 v[98:99],v1 offset:5312
v_cvt_pk_bf16_f32 v151,v158,v159
v_mfma_f32_16x16x32_bf16 a[240:243],v[116:119],a[76:79],a[240:243]
s_mul_i32 s77,s77,16384
ds_read_b64_tr_b16 v[104:105],v1 offset:20672
v_mul_f32 v164,v164,v53
v_mfma_f32_16x16x32_bf16 a[240:243],v[120:123],a[84:87],a[240:243]
s_add_u32 s65,s77,0
ds_read_b64_tr_b16 v[106:107],v1 offset:25792
v_mul_f32 v165,v165,v53
v_mfma_f32_16x16x32_bf16 a[240:243],v[124:127],a[92:95],a[240:243]
s_add_u32 s68,s77,12288
v_mul_f32 v166,v166,v53
v_mul_f32 v167,v167,v53
v_mfma_f32_16x16x32_bf16 a[244:247],v[124:127],a[124:127],0
v_mul_f32 v172,v172,v53
v_mul_f32 v173,v173,v53
v_mfma_f32_16x16x32_bf16 a[244:247],v[120:123],a[116:119],a[244:247]
v_mul_f32 v174,v174,v53
v_mul_f32 v175,v175,v53
v_mfma_f32_16x16x32_bf16 a[244:247],v[116:119],a[108:111],a[244:247]
v_cvt_pk_bf16_f32 v164,v164,v165
v_cvt_pk_bf16_f32 v165,v166,v167
v_mfma_f32_16x16x32_bf16 a[244:247],v[112:115],a[100:103],a[244:247]
v_cvt_pk_bf16_f32 v166,v172,v173
v_cvt_pk_bf16_f32 v167,v174,v175
v_mfma_f32_16x16x32_bf16 a[248:251],v[112:115],a[64:67],0
v_mul_f32 v180,v180,v53
v_mul_f32 v181,v181,v53
v_mfma_f32_16x16x32_bf16 a[248:251],v[116:119],a[72:75],a[248:251]
v_mul_f32 v182,v182,v53
v_mul_f32 v183,v183,v53
v_mfma_f32_16x16x32_bf16 a[248:251],v[120:123],a[80:83],a[248:251]
v_mul_f32 v188,v188,v53
v_mul_f32 v189,v189,v53
v_mfma_f32_16x16x32_bf16 a[248:251],v[124:127],a[88:91],a[248:251]
v_mul_f32 v190,v190,v53
v_mul_f32 v191,v191,v53
v_mfma_f32_16x16x32_bf16 a[252:255],v[124:127],a[120:123],0
buffer_load_dword v30,v29,s[8:11],s7 offen
v_mfma_f32_16x16x32_bf16 a[252:255],v[120:123],a[112:115],a[252:255]
ds_read_b64_tr_b16 v[120:121],v2 offset:20704
ds_read_b64_tr_b16 v[122:123],v2 offset:25824
v_mfma_f32_16x16x32_bf16 a[252:255],v[116:119],a[104:107],a[252:255]
buffer_store_dwordx4 v[132:135],v6,s[40:43],s79 offen offset:1024 sc1
v_mfma_f32_16x16x32_bf16 a[252:255],v[112:115],a[96:99],a[252:255]
ds_read_b64_tr_b16 v[112:113],v2 offset:224
ds_read_b64_tr_b16 v[114:115],v2 offset:5344
v_mfma_f32_16x16x32_bf16 a[0:3],v[64:67],a[68:71],0
ds_read_b64_tr_b16 v[116:117],v2 offset:10464
v_cvt_pk_bf16_f32 v180,v180,v181
v_mfma_f32_16x16x32_bf16 a[0:3],v[68:71],a[76:79],a[0:3]
ds_read_b64_tr_b16 v[118:119],v2 offset:15584
v_cvt_pk_bf16_f32 v181,v182,v183
v_mfma_f32_16x16x32_bf16 a[0:3],v[72:75],a[84:87],a[0:3]
ds_read_b64_tr_b16 v[124:125],v2 offset:30944
v_cvt_pk_bf16_f32 v182,v188,v189
v_mfma_f32_16x16x32_bf16 a[0:3],v[76:79],a[92:95],a[0:3]
ds_read_b64_tr_b16 v[126:127],v2 offset:36064
v_cvt_pk_bf16_f32 v183,v190,v191
v_mfma_f32_16x16x32_bf16 a[4:7],v[76:79],a[124:127],0
v_mul_f32 v196,v196,v57
v_mul_f32 v197,v197,v57
v_mfma_f32_16x16x32_bf16 a[4:7],v[72:75],a[116:119],a[4:7]
v_mul_f32 v198,v198,v57
v_mul_f32 v199,v199,v57
v_mfma_f32_16x16x32_bf16 a[4:7],v[68:71],a[108:111],a[4:7]
v_mul_f32 v204,v204,v57
v_mul_f32 v205,v205,v57
v_mfma_f32_16x16x32_bf16 a[4:7],v[64:67],a[100:103],a[4:7]
v_mul_f32 v206,v206,v57
v_mul_f32 v207,v207,v57
v_mfma_f32_16x16x32_bf16 a[8:11],v[64:67],a[64:67],0
v_cvt_pk_bf16_f32 v196,v196,v197
v_cvt_pk_bf16_f32 v197,v198,v199
v_mfma_f32_16x16x32_bf16 a[8:11],v[68:71],a[72:75],a[8:11]
v_cvt_pk_bf16_f32 v198,v204,v205
v_cvt_pk_bf16_f32 v199,v206,v207
v_mfma_f32_16x16x32_bf16 a[8:11],v[72:75],a[80:83],a[8:11]
v_mul_f32 v212,v212,v57
v_mul_f32 v213,v213,v57
v_mfma_f32_16x16x32_bf16 a[8:11],v[76:79],a[88:91],a[8:11]
v_mul_f32 v214,v214,v57
v_mul_f32 v215,v215,v57
v_mfma_f32_16x16x32_bf16 a[12:15],v[76:79],a[120:123],0
ds_read_b64_tr_b16 v[76:77],v1 offset:30976
ds_read_b64_tr_b16 v[78:79],v1 offset:36096
v_mfma_f32_16x16x32_bf16 a[12:15],v[72:75],a[112:115],a[12:15]
buffer_store_dwordx4 v[148:151],v6,s[40:43],s80 offen offset:1024 sc1
v_mfma_f32_16x16x32_bf16 a[12:15],v[68:71],a[104:107],a[12:15]
ds_read_b64_tr_b16 v[68:69],v1 offset:10496
ds_read_b64_tr_b16 v[70:71],v1 offset:15616
v_mfma_f32_16x16x32_bf16 a[12:15],v[64:67],a[96:99],a[12:15]
s_waitcnt lgkmcnt(4)
ds_read_b64_tr_b16 v[64:65],v1 offset:256
v_mul_f32 v220,v220,v57
v_mfma_f32_16x16x32_bf16 a[16:19],v[80:83],a[68:71],0
ds_read_b64_tr_b16 v[66:67],v1 offset:5376
v_mul_f32 v221,v221,v57
v_mfma_f32_16x16x32_bf16 a[16:19],v[84:87],a[76:79],a[16:19]
ds_read_b64_tr_b16 v[72:73],v1 offset:20736
v_mul_f32 v222,v222,v57
v_mfma_f32_16x16x32_bf16 a[16:19],v[88:91],a[84:87],a[16:19]
ds_read_b64_tr_b16 v[74:75],v1 offset:25856
v_add_u32 v1,0xffff6000,v1
v_mfma_f32_16x16x32_bf16 a[16:19],v[92:95],a[92:95],a[16:19]
v_mul_f32 v223,v223,v57
v_cvt_pk_bf16_f32 v212,v212,v213
v_mfma_f32_16x16x32_bf16 a[20:23],v[92:95],a[124:127],0
v_cvt_pk_bf16_f32 v213,v214,v215
v_cvt_pk_bf16_f32 v214,v220,v221
v_mfma_f32_16x16x32_bf16 a[20:23],v[88:91],a[116:119],a[20:23]
v_cvt_pk_bf16_f32 v215,v222,v223
v_mul_f32 v228,v228,v57
v_mfma_f32_16x16x32_bf16 a[20:23],v[84:87],a[108:111],a[20:23]
v_mul_f32 v229,v229,v57
v_mul_f32 v230,v230,v57
v_mfma_f32_16x16x32_bf16 a[20:23],v[80:83],a[100:103],a[20:23]
v_mul_f32 v231,v231,v57
v_mul_f32 v236,v236,v57
v_mfma_f32_16x16x32_bf16 a[24:27],v[80:83],a[64:67],0
v_mul_f32 v237,v237,v57
v_mul_f32 v238,v238,v57
v_mfma_f32_16x16x32_bf16 a[24:27],v[84:87],a[72:75],a[24:27]
v_mul_f32 v239,v239,v57
v_cvt_pk_bf16_f32 v228,v228,v229
v_mfma_f32_16x16x32_bf16 a[24:27],v[88:91],a[80:83],a[24:27]
s_waitcnt vmcnt(5)
v_cmp_ne_u32 s[56:57],s52,v4
v_cmp_ne_u32 s[58:59],s53,v5
v_mfma_f32_16x16x32_bf16 a[24:27],v[92:95],a[88:91],a[24:27]
s_or_b64 s[56:57],s[56:57],s[58:59]
s_cbranch_scc1 L_fr6_%=
L_fo6_%=:
v_cvt_pk_bf16_f32 v229,v230,v231
v_mfma_f32_16x16x32_bf16 a[28:31],v[92:95],a[120:123],0
buffer_store_dwordx4 v[164:167],v6,s[40:43],s81 offen offset:1024 sc1
v_mfma_f32_16x16x32_bf16 a[28:31],v[88:91],a[112:115],a[28:31]
s_lshl_b32 s91,s63,8
ds_read_b64_tr_b16 v[88:89],v2 offset:20768
ds_read_b64_tr_b16 v[90:91],v2 offset:25888
v_mfma_f32_16x16x32_bf16 a[28:31],v[84:87],a[104:107],a[28:31]
buffer_load_dwordx4 a[128:131],v6,s[28:31],s87 offen sc1
v_mfma_f32_16x16x32_bf16 a[28:31],v[80:83],a[96:99],a[28:31]
ds_read_b64_tr_b16 v[80:81],v2 offset:288
ds_read_b64_tr_b16 v[82:83],v2 offset:5408
v_mfma_f32_16x16x32_bf16 v[128:131],v[96:99],a[68:71],0
ds_read_b64_tr_b16 v[84:85],v2 offset:10528
v_cvt_pk_bf16_f32 v230,v236,v237
v_mfma_f32_16x16x32_bf16 v[128:131],v[100:103],a[76:79],v[128:131]
ds_read_b64_tr_b16 v[86:87],v2 offset:15648
v_cvt_pk_bf16_f32 v231,v238,v239
v_mfma_f32_16x16x32_bf16 v[128:131],v[104:107],a[84:87],v[128:131]
ds_read_b64_tr_b16 v[92:93],v2 offset:31008
v_mul_f32 v244,v244,v57
v_mfma_f32_16x16x32_bf16 v[128:131],v[108:111],a[92:95],v[128:131]
ds_read_b64_tr_b16 v[94:95],v2 offset:36128
v_add_u32 v2,0xffff6000,v2
v_mfma_f32_16x16x32_bf16 v[136:139],v[108:111],a[124:127],0
v_mul_f32 v245,v245,v57
v_mul_f32 v246,v246,v57
v_mfma_f32_16x16x32_bf16 v[136:139],v[104:107],a[116:119],v[136:139]
v_mul_f32 v247,v247,v57
v_mul_f32 v252,v252,v57
v_mfma_f32_16x16x32_bf16 v[136:139],v[100:103],a[108:111],v[136:139]
v_mul_f32 v253,v253,v57
v_mul_f32 v254,v254,v57
v_mfma_f32_16x16x32_bf16 v[136:139],v[96:99],a[100:103],v[136:139]
v_mul_f32 v255,v255,v57
v_cvt_pk_bf16_f32 v244,v244,v245
v_mfma_f32_16x16x32_bf16 v[144:147],v[96:99],a[64:67],0
v_cvt_pk_bf16_f32 v245,v246,v247
v_cvt_pk_bf16_f32 v246,v252,v253
v_mfma_f32_16x16x32_bf16 v[144:147],v[100:103],a[72:75],v[144:147]
buffer_load_dwordx4 a[144:147],v6,s[28:31],s88 offen sc1
v_mfma_f32_16x16x32_bf16 v[144:147],v[104:107],a[80:83],v[144:147]
v_cvt_pk_bf16_f32 v247,v254,v255
v_accvgpr_read_b32 v16,a40
v_mfma_f32_16x16x32_bf16 v[144:147],v[108:111],a[88:91],v[144:147]
buffer_store_dwordx4 v[180:183],v6,s[40:43],s82 offen offset:1024 sc1
v_mfma_f32_16x16x32_bf16 v[152:155],v[108:111],a[120:123],0
ds_read_b64_tr_b16 v[108:109],v1 offset:30720
ds_read_b64_tr_b16 v[110:111],v1 offset:35840
v_mfma_f32_16x16x32_bf16 v[152:155],v[104:107],a[112:115],v[152:155]
ds_read_b64_tr_b16 v[104:105],v1 offset:20480
v_accvgpr_read_b32 v17,a41
v_mfma_f32_16x16x32_bf16 v[152:155],v[100:103],a[104:107],v[152:155]
ds_read_b64_tr_b16 v[100:101],v1 offset:10240
v_accvgpr_read_b32 v18,a42
v_mfma_f32_16x16x32_bf16 v[152:155],v[96:99],a[96:99],v[152:155]
ds_read_b64_tr_b16 v[96:97],v1
v_accvgpr_read_b32 v19,a43
v_mfma_f32_16x16x32_bf16 v[160:163],v[112:115],a[68:71],0
ds_read_b64_tr_b16 v[98:99],v1 offset:5120
ds_read_b64_tr_b16 v[102:103],v1 offset:15360
v_mfma_f32_16x16x32_bf16 v[160:163],v[116:119],a[76:79],v[160:163]
ds_read_b64_tr_b16 v[106:107],v1 offset:25600
v_accvgpr_read_b32 v20,a44
v_mfma_f32_16x16x32_bf16 v[160:163],v[120:123],a[84:87],v[160:163]
v_accvgpr_read_b32 v21,a45
v_accvgpr_read_b32 v22,a46
v_mfma_f32_16x16x32_bf16 v[160:163],v[124:127],a[92:95],v[160:163]
v_accvgpr_read_b32 v23,a47
v_mul_f32 v16,v16,v61
v_mfma_f32_16x16x32_bf16 v[168:171],v[124:127],a[124:127],0
v_mul_f32 v17,v17,v61
v_mul_f32 v18,v18,v61
v_mfma_f32_16x16x32_bf16 v[168:171],v[120:123],a[116:119],v[168:171]
v_mul_f32 v19,v19,v61
v_mul_f32 v20,v20,v61
v_mfma_f32_16x16x32_bf16 v[168:171],v[116:119],a[108:111],v[168:171]
buffer_load_dwordx4 a[160:163],v6,s[28:31],s89 offen sc1
v_mfma_f32_16x16x32_bf16 v[168:171],v[112:115],a[100:103],v[168:171]
v_mul_f32 v21,v21,v61
v_mul_f32 v22,v22,v61
v_mfma_f32_16x16x32_bf16 v[176:179],v[112:115],a[64:67],0
v_mul_f32 v23,v23,v61
v_cvt_pk_bf16_f32 v156,v16,v17
v_mfma_f32_16x16x32_bf16 v[176:179],v[116:119],a[72:75],v[176:179]
v_cvt_pk_bf16_f32 v157,v18,v19
v_cvt_pk_bf16_f32 v158,v20,v21
v_mfma_f32_16x16x32_bf16 v[176:179],v[120:123],a[80:83],v[176:179]
buffer_store_dwordx4 v[196:199],v6,s[40:43],s79 offen offset:2048 sc1
v_mfma_f32_16x16x32_bf16 v[176:179],v[124:127],a[88:91],v[176:179]
v_cvt_pk_bf16_f32 v159,v22,v23
v_accvgpr_read_b32 v16,a56
v_mfma_f32_16x16x32_bf16 v[184:187],v[124:127],a[120:123],0
v_accvgpr_read_b32 v17,a57
v_accvgpr_read_b32 v18,a58
v_mfma_f32_16x16x32_bf16 v[184:187],v[120:123],a[112:115],v[184:187]
ds_read_b64_tr_b16 v[120:121],v2 offset:20512
ds_read_b64_tr_b16 v[122:123],v2 offset:25632
v_mfma_f32_16x16x32_bf16 v[184:187],v[116:119],a[104:107],v[184:187]
ds_read_b64_tr_b16 v[116:117],v2 offset:10272
v_accvgpr_read_b32 v19,a59
v_mfma_f32_16x16x32_bf16 v[184:187],v[112:115],a[96:99],v[184:187]
s_waitcnt lgkmcnt(3)
ds_read_b64_tr_b16 v[112:113],v2 offset:32
v_accvgpr_read_b32 v20,a60
v_mfma_f32_16x16x32_bf16 v[192:195],v[64:67],a[68:71],0
ds_read_b64_tr_b16 v[114:115],v2 offset:5152
v_accvgpr_read_b32 v21,a61
v_mfma_f32_16x16x32_bf16 v[192:195],v[68:71],a[76:79],v[192:195]
ds_read_b64_tr_b16 v[118:119],v2 offset:15392
v_accvgpr_read_b32 v22,a62
v_mfma_f32_16x16x32_bf16 v[192:195],v[72:75],a[84:87],v[192:195]
v_accvgpr_read_b32 v23,a63
v_mul_f32 v16,v16,v61
v_mfma_f32_16x16x32_bf16 v[192:195],v[76:79],a[92:95],v[192:195]
buffer_load_dwordx4 a[176:179],v6,s[28:31],s90 offen sc1
v_mfma_f32_16x16x32_bf16 v[200:203],v[76:79],a[124:127],0
v_mul_f32 v17,v17,v61
v_mul_f32 v18,v18,v61
v_mfma_f32_16x16x32_bf16 v[200:203],v[72:75],a[116:119],v[200:203]
v_mul_f32 v19,v19,v61
v_mul_f32 v20,v20,v61
v_mfma_f32_16x16x32_bf16 v[200:203],v[68:71],a[108:111],v[200:203]
v_mul_f32 v21,v21,v61
v_mul_f32 v22,v22,v61
v_mfma_f32_16x16x32_bf16 v[200:203],v[64:67],a[100:103],v[200:203]
v_mul_f32 v23,v23,v61
v_cvt_pk_bf16_f32 v188,v16,v17
v_mfma_f32_16x16x32_bf16 v[208:211],v[64:67],a[64:67],0
v_cvt_pk_bf16_f32 v189,v18,v19
v_cvt_pk_bf16_f32 v190,v20,v21
v_mfma_f32_16x16x32_bf16 v[208:211],v[68:71],a[72:75],v[208:211]
buffer_store_dwordx4 v[212:215],v6,s[40:43],s80 offen offset:2048 sc1
v_mfma_f32_16x16x32_bf16 v[208:211],v[72:75],a[80:83],v[208:211]
ds_read_b64_tr_b16 v[124:125],v2 offset:30752
v_cvt_pk_bf16_f32 v191,v22,v23
v_mfma_f32_16x16x32_bf16 v[208:211],v[76:79],a[88:91],v[208:211]
ds_read_b64_tr_b16 v[126:127],v2 offset:35872
v_accvgpr_read_b32 v8,a32
v_mfma_f32_16x16x32_bf16 v[216:219],v[76:79],a[120:123],0
v_accvgpr_read_b32 v9,a33
v_accvgpr_read_b32 v10,a34
v_mfma_f32_16x16x32_bf16 v[216:219],v[72:75],a[112:115],v[216:219]
v_accvgpr_read_b32 v11,a35
v_accvgpr_read_b32 v12,a36
v_mfma_f32_16x16x32_bf16 v[216:219],v[68:71],a[104:107],v[216:219]
)A"
R"A(v_accvgpr_read_b32 v13,a37
v_accvgpr_read_b32 v14,a38
v_mfma_f32_16x16x32_bf16 v[216:219],v[64:67],a[96:99],v[216:219]
v_accvgpr_read_b32 v15,a39
v_mul_f32 v8,v8,v61
v_mfma_f32_16x16x32_bf16 v[224:227],v[80:83],a[68:71],0
buffer_load_dwordx4 a[180:183],v6,s[28:31],s90 offen offset:1024 sc1
v_mfma_f32_16x16x32_bf16 v[224:227],v[84:87],a[76:79],v[224:227]
v_mul_f32 v9,v9,v61
v_mul_f32 v10,v10,v61
v_mfma_f32_16x16x32_bf16 v[224:227],v[88:91],a[84:87],v[224:227]
v_mul_f32 v11,v11,v61
v_mul_f32 v12,v12,v61
v_mfma_f32_16x16x32_bf16 v[224:227],v[92:95],a[92:95],v[224:227]
v_mul_f32 v13,v13,v61
v_mul_f32 v14,v14,v61
v_mfma_f32_16x16x32_bf16 v[232:235],v[92:95],a[124:127],0
v_mul_f32 v15,v15,v61
v_cvt_pk_bf16_f32 v140,v8,v9
v_mfma_f32_16x16x32_bf16 v[232:235],v[88:91],a[116:119],v[232:235]
v_cvt_pk_bf16_f32 v141,v10,v11
v_cvt_pk_bf16_f32 v142,v12,v13
v_mfma_f32_16x16x32_bf16 v[232:235],v[84:87],a[108:111],v[232:235]
v_cvt_pk_bf16_f32 v143,v14,v15
v_accvgpr_read_b32 v8,a48
v_mfma_f32_16x16x32_bf16 v[232:235],v[80:83],a[100:103],v[232:235]
v_accvgpr_read_b32 v9,a49
v_accvgpr_read_b32 v10,a50
v_mfma_f32_16x16x32_bf16 v[240:243],v[80:83],a[64:67],0
buffer_store_dwordx4 v[228:231],v6,s[40:43],s81 offen offset:2048 sc1
v_mfma_f32_16x16x32_bf16 v[240:243],v[84:87],a[72:75],v[240:243]
v_accvgpr_read_b32 v11,a51
v_accvgpr_read_b32 v12,a52
v_mfma_f32_16x16x32_bf16 v[240:243],v[88:91],a[80:83],v[240:243]
v_accvgpr_read_b32 v13,a53
v_accvgpr_read_b32 v14,a54
v_mfma_f32_16x16x32_bf16 v[240:243],v[92:95],a[88:91],v[240:243]
v_accvgpr_read_b32 v15,a55
v_mul_f32 v8,v8,v61
v_mfma_f32_16x16x32_bf16 v[248:251],v[92:95],a[120:123],0
v_mul_f32 v9,v9,v61
v_mul_f32 v10,v10,v61
v_mfma_f32_16x16x32_bf16 v[248:251],v[88:91],a[112:115],v[248:251]
buffer_load_dwordx4 a[164:167],v6,s[28:31],s89 offen offset:1024 sc1
v_mfma_f32_16x16x32_bf16 v[248:251],v[84:87],a[104:107],v[248:251]
v_mul_f32 v11,v11,v61
v_mul_f32 v12,v12,v61
v_mfma_f32_16x16x32_bf16 v[248:251],v[80:83],a[96:99],v[248:251]
v_mul_f32 v13,v13,v61
v_mul_f32 v14,v14,v61
v_mfma_f32_16x16x32_bf16 a[192:195],v[96:99],v[32:35],a[192:195]
v_mul_f32 v15,v15,v61
v_cvt_pk_bf16_f32 v172,v8,v9
v_mfma_f32_16x16x32_bf16 a[192:195],v[100:103],v[36:39],a[192:195]
v_cvt_pk_bf16_f32 v173,v10,v11
v_cvt_pk_bf16_f32 v174,v12,v13
v_mfma_f32_16x16x32_bf16 a[192:195],v[104:107],v[40:43],a[192:195]
ds_read_b64_tr_b16 v[64:65],v1 offset:64
v_cvt_pk_bf16_f32 v175,v14,v15
v_mfma_f32_16x16x32_bf16 a[192:195],v[108:111],v[44:47],a[192:195]
ds_read_b64_tr_b16 v[66:67],v1 offset:5184
ds_read_b64_tr_b16 v[68:69],v1 offset:10304
v_mfma_f32_16x16x32_bf16 a[196:199],v[108:111],v[180:183],a[196:199]
s_cmp_eq_u32 s60,3
s_cselect_b32 s69,0x40000000,0
ds_read_b64_tr_b16 v[70:71],v1 offset:15424
v_mfma_f32_16x16x32_bf16 a[196:199],v[104:107],v[164:167],a[196:199]
ds_read_b64_tr_b16 v[72:73],v1 offset:20544
v_add_u32 v28,s69,v6
v_mfma_f32_16x16x32_bf16 a[196:199],v[100:103],v[148:151],a[196:199]
s_and_b32 s69,s63,s60
ds_read_b64_tr_b16 v[74:75],v1 offset:25664
v_mov_b32 v8,s52
v_mfma_f32_16x16x32_bf16 a[196:199],v[96:99],v[132:135],a[196:199]
buffer_store_dwordx4 v[244:247],v6,s[40:43],s82 offen offset:2048 sc1
v_mfma_f32_16x16x32_bf16 a[200:203],v[96:99],v[196:199],a[200:203]
ds_read_b64_tr_b16 v[76:77],v1 offset:30784
ds_read_b64_tr_b16 v[78:79],v1 offset:35904
v_mfma_f32_16x16x32_bf16 a[200:203],v[100:103],v[212:215],a[200:203]
ds_read_b64_tr_b16 v[80:81],v2 offset:96
v_mov_b32 v9,s53
v_mfma_f32_16x16x32_bf16 a[200:203],v[104:107],v[228:231],a[200:203]
buffer_load_dwordx4 a[148:151],v6,s[28:31],s88 offen offset:1024 sc1
v_mfma_f32_16x16x32_bf16 a[200:203],v[108:111],v[244:247],a[200:203]
ds_read_b64_tr_b16 v[82:83],v2 offset:5216
ds_read_b64_tr_b16 v[84:85],v2 offset:10336
v_mfma_f32_16x16x32_bf16 a[204:207],v[108:111],v[188:191],a[204:207]
ds_read_b64_tr_b16 v[86:87],v2 offset:15456
v_lshlrev_b32 v10,20,v6
v_mfma_f32_16x16x32_bf16 a[204:207],v[104:107],v[172:175],a[204:207]
ds_read_b64_tr_b16 v[88:89],v2 offset:20576
v_bfe_u32 v3,v6,4,2
v_mfma_f32_16x16x32_bf16 a[204:207],v[100:103],v[156:159],a[204:207]
ds_read_b64_tr_b16 v[90:91],v2 offset:25696
v_min_u32 v3,2,v3
v_mfma_f32_16x16x32_bf16 a[204:207],v[96:99],v[140:143],a[204:207]
s_waitcnt lgkmcnt(8)
ds_read_b64_tr_b16 v[92:93],v2 offset:30816
ds_read_b64_tr_b16 v[94:95],v2 offset:35936
v_mfma_f32_16x16x32_bf16 a[208:211],v[112:115],v[32:35],a[208:211]
ds_read_b64_tr_b16 v[96:97],v1 offset:128
v_add3_u32 v3,v3,s64,1
v_mfma_f32_16x16x32_bf16 a[208:211],v[116:119],v[36:39],a[208:211]
ds_read_b64_tr_b16 v[98:99],v1 offset:5248
v_and_b32 v3,3,v3
v_mfma_f32_16x16x32_bf16 a[208:211],v[120:123],v[40:43],a[208:211]
ds_read_b64_tr_b16 v[100:101],v1 offset:10368
v_lshlrev_b32 v3,6,v3
v_mfma_f32_16x16x32_bf16 a[208:211],v[124:127],v[44:47],a[208:211]
ds_read_b64_tr_b16 v[102:103],v1 offset:15488
ds_read_b64_tr_b16 v[104:105],v1 offset:20608
v_mfma_f32_16x16x32_bf16 a[212:215],v[124:127],v[180:183],a[212:215]
ds_read_b64_tr_b16 v[106:107],v1 offset:25728
v_bfe_u32 v56,v6,6,2
v_mfma_f32_16x16x32_bf16 a[212:215],v[120:123],v[164:167],a[212:215]
ds_read_b64_tr_b16 v[108:109],v1 offset:30848
v_lshrrev_b32 v57,8,v6
v_mfma_f32_16x16x32_bf16 a[212:215],v[116:119],v[148:151],a[212:215]
buffer_store_dwordx4 v[140:143],v28,s[40:43],s79 offen offset:3072 sc1
v_mfma_f32_16x16x32_bf16 a[212:215],v[112:115],v[132:135],a[212:215]
ds_read_b64_tr_b16 v[110:111],v1 offset:35968
v_cmp_eq_u32 s[56:57],v56,v57
L_pd20_%=:
s_cbranch_execz L_trt1_%=
v_mfma_f32_16x16x32_bf16 a[216:219],v[112:115],v[196:199],a[216:219]
buffer_load_dwordx4 a[132:135],v6,s[28:31],s87 offen offset:1024 sc1
v_mfma_f32_16x16x32_bf16 a[216:219],v[116:119],v[212:215],a[216:219]
v_bfe_u32 v56,v6,4,1
v_lshlrev_b32 v56,4,v56
v_mfma_f32_16x16x32_bf16 a[216:219],v[120:123],v[228:231],a[216:219]
v_mov_b32 v57,0x3f80
v_lshlrev_b32 v56,v56,v57
v_mfma_f32_16x16x32_bf16 a[216:219],v[124:127],v[244:247],a[216:219]
v_cndmask_b32 v56,0,v56,s[56:57]
v_bfe_u32 v57,v6,5,1
v_mfma_f32_16x16x32_bf16 a[220:223],v[124:127],v[188:191],a[220:223]
ds_read_b64_tr_b16 v[124:125],v2 offset:30880
ds_read_b64_tr_b16 v[126:127],v2 offset:36000
v_mfma_f32_16x16x32_bf16 a[220:223],v[120:123],v[172:175],a[220:223]
ds_read_b64_tr_b16 v[120:121],v2 offset:20640
v_cmp_eq_u32 s[56:57],0,v57
v_mfma_f32_16x16x32_bf16 a[220:223],v[116:119],v[156:159],a[220:223]
s_add_u32 s36,s12,s66
s_addc_u32 s37,s13,0
ds_read_b64_tr_b16 v[116:117],v2 offset:10400
v_mfma_f32_16x16x32_bf16 a[220:223],v[112:115],v[140:143],a[220:223]
s_and_b32 s37,s37,0xffff
ds_read_b64_tr_b16 v[112:113],v2 offset:160
v_cndmask_b32 v48,0,v56,s[56:57]
v_mfma_f32_16x16x32_bf16 a[224:227],v[64:67],v[32:35],a[224:227]
s_mul_i32 s66,s61,307200
ds_read_b64_tr_b16 v[114:115],v2 offset:5280
ds_read_b64_tr_b16 v[118:119],v2 offset:15520
v_mfma_f32_16x16x32_bf16 a[224:227],v[68:71],v[36:39],a[224:227]
ds_read_b64_tr_b16 v[122:123],v2 offset:25760
v_cndmask_b32 v49,v56,0,s[56:57]
v_mfma_f32_16x16x32_bf16 a[224:227],v[72:75],v[40:43],a[224:227]
s_waitcnt lgkmcnt(8)
v_mov_b32 v58,s52
v_mov_b32 v59,s53
v_mfma_f32_16x16x32_bf16 a[224:227],v[76:79],v[44:47],a[224:227]
s_add_u32 s24,s16,s66
s_addc_u32 s25,s17,0
v_lshlrev_b32 v60,20,v6
v_mfma_f32_16x16x32_bf16 a[228:231],v[76:79],v[180:183],a[228:231]
buffer_load_dwordx4 a[136:139],v6,s[28:31],s87 offen offset:2048 sc1
v_mfma_f32_16x16x32_bf16 a[228:231],v[72:75],v[164:167],a[228:231]
s_mul_i32 s66,s60,16384
s_add_u32 s66,s66,s67
s_add_u32 s79,s66,12288
v_mfma_f32_16x16x32_bf16 a[228:231],v[68:71],v[148:151],a[228:231]
buffer_store_dwordx4 v[156:159],v28,s[40:43],s80 offen offset:3072 sc1
v_mfma_f32_16x16x32_bf16 a[228:231],v[64:67],v[132:135],a[228:231]
s_add_u32 s80,s66,77824
v_bfe_u32 v61,v6,4,1
v_add3_u32 v61,v61,s63,2
v_mfma_f32_16x16x32_bf16 a[232:235],v[64:67],v[196:199],a[232:235]
s_and_b32 s25,s25,0xffff
v_and_b32 v61,3,v61
v_lshlrev_b32 v61,8,v61
v_mfma_f32_16x16x32_bf16 a[232:235],v[68:71],v[212:215],a[232:235]
s_add_u32 s67,s77,8192
v_mov_b32 v50,0
v_mov_b32 v51,0
v_mfma_f32_16x16x32_bf16 a[232:235],v[72:75],v[228:231],a[232:235]
v_mov_b32 v52,0
v_mov_b32 v53,0
v_mfma_f32_16x16x32_bf16 a[232:235],v[76:79],v[244:247],a[232:235]
v_mov_b32 v54,v48
v_mov_b32 v55,v49
v_mfma_f32_16x16x32_bf16 a[236:239],v[76:79],v[188:191],a[236:239]
ds_read_b64_tr_b16 v[76:77],v1 offset:30912
ds_read_b64_tr_b16 v[78:79],v1 offset:36032
v_mfma_f32_16x16x32_bf16 a[236:239],v[72:75],v[172:175],a[236:239]
ds_read_b64_tr_b16 v[72:73],v1 offset:20672
v_bfe_u32 v56,v6,4,4
v_mfma_f32_16x16x32_bf16 a[236:239],v[68:71],v[156:159],a[236:239]
ds_read_b64_tr_b16 v[68:69],v1 offset:10432
v_mul_u32_u24 v56,1280,v56
v_mfma_f32_16x16x32_bf16 a[236:239],v[64:67],v[140:143],a[236:239]
ds_read_b64_tr_b16 v[64:65],v1 offset:192
v_bfe_u32 v57,v6,8,1
v_mfma_f32_16x16x32_bf16 a[240:243],v[80:83],v[32:35],a[240:243]
ds_read_b64_tr_b16 v[66:67],v1 offset:5312
ds_read_b64_tr_b16 v[70:71],v1 offset:15552
v_mfma_f32_16x16x32_bf16 a[240:243],v[84:87],v[36:39],a[240:243]
buffer_load_dwordx4 a[152:155],v6,s[28:31],s88 offen offset:2048 sc1
v_mfma_f32_16x16x32_bf16 a[240:243],v[88:91],v[40:43],a[240:243]
ds_read_b64_tr_b16 v[74:75],v1 offset:25792
v_lshl_add_u32 v56,v57,5,v56
v_mfma_f32_16x16x32_bf16 a[240:243],v[92:95],v[44:47],a[240:243]
v_lshrrev_b32 v57,9,v6
v_lshl_add_u32 v56,v57,4,v56
v_mfma_f32_16x16x32_bf16 a[244:247],v[92:95],v[180:183],a[244:247]
s_cmp_eq_u32 s69,3
s_cselect_b32 s69,0x40000000,0
s_add_u32 s68,s68,s69
v_mfma_f32_16x16x32_bf16 a[244:247],v[88:91],v[164:167],a[244:247]
buffer_store_dwordx4 v[172:175],v28,s[40:43],s81 offen offset:3072 sc1
v_mfma_f32_16x16x32_bf16 a[244:247],v[84:87],v[148:151],a[244:247]
s_add_u32 s81,s66,143360
v_mfma_f32_16x16x32_bf16 a[244:247],v[80:83],v[132:135],a[244:247]
v_mfma_f32_16x16x32_bf16 a[248:251],v[80:83],v[196:199],a[248:251]
v_mfma_f32_16x16x32_bf16 a[248:251],v[84:87],v[212:215],a[248:251]
v_mfma_f32_16x16x32_bf16 a[248:251],v[88:91],v[228:231],a[248:251]
v_mfma_f32_16x16x32_bf16 a[248:251],v[92:95],v[244:247],a[248:251]
v_mfma_f32_16x16x32_bf16 a[252:255],v[92:95],v[188:191],a[252:255]
ds_read_b64_tr_b16 v[92:93],v2 offset:30944
ds_read_b64_tr_b16 v[94:95],v2 offset:36064
v_mfma_f32_16x16x32_bf16 a[252:255],v[88:91],v[172:175],a[252:255]
ds_read_b64_tr_b16 v[88:89],v2 offset:20704
v_mfma_f32_16x16x32_bf16 a[252:255],v[84:87],v[156:159],a[252:255]
buffer_load_dwordx4 a[168:171],v6,s[28:31],s89 offen offset:2048 sc1
v_mfma_f32_16x16x32_bf16 a[252:255],v[80:83],v[140:143],a[252:255]
ds_read_b64_tr_b16 v[80:81],v2 offset:224
ds_read_b64_tr_b16 v[82:83],v2 offset:5344
v_mfma_f32_16x16x32_bf16 a[0:3],v[96:99],v[32:35],a[0:3]
ds_read_b64_tr_b16 v[84:85],v2 offset:10464
v_mfma_f32_16x16x32_bf16 a[0:3],v[100:103],v[36:39],a[0:3]
ds_read_b64_tr_b16 v[86:87],v2 offset:15584
v_mfma_f32_16x16x32_bf16 a[0:3],v[104:107],v[40:43],a[0:3]
ds_read_b64_tr_b16 v[90:91],v2 offset:25824
v_mfma_f32_16x16x32_bf16 a[0:3],v[108:111],v[44:47],a[0:3]
v_mfma_f32_16x16x32_bf16 a[4:7],v[108:111],v[180:183],a[4:7]
buffer_store_dwordx4 v[188:191],v28,s[40:43],s82 offen offset:3072 sc1
v_mfma_f32_16x16x32_bf16 a[4:7],v[104:107],v[164:167],a[4:7]
s_add_u32 s82,s66,208896
s_mul_i32 s66,s64,320
v_mfma_f32_16x16x32_bf16 a[4:7],v[100:103],v[148:151],a[4:7]
v_mfma_f32_16x16x32_bf16 a[4:7],v[96:99],v[132:135],a[4:7]
v_mfma_f32_16x16x32_bf16 a[8:11],v[96:99],v[196:199],a[8:11]
v_mfma_f32_16x16x32_bf16 a[8:11],v[100:103],v[212:215],a[8:11]
v_mfma_f32_16x16x32_bf16 a[8:11],v[104:107],v[228:231],a[8:11]
v_mfma_f32_16x16x32_bf16 a[8:11],v[108:111],v[244:247],a[8:11]
buffer_load_dwordx4 a[184:187],v6,s[28:31],s90 offen offset:2048 sc1
v_mfma_f32_16x16x32_bf16 a[12:15],v[108:111],v[188:191],a[12:15]
ds_read_b64_tr_b16 v[108:109],v1 offset:30976
ds_read_b64_tr_b16 v[110:111],v1 offset:36096
v_mfma_f32_16x16x32_bf16 a[12:15],v[104:107],v[172:175],a[12:15]
ds_read_b64_tr_b16 v[104:105],v1 offset:20736
v_mfma_f32_16x16x32_bf16 a[12:15],v[100:103],v[156:159],a[12:15]
ds_read_b64_tr_b16 v[100:101],v1 offset:10496
v_mfma_f32_16x16x32_bf16 a[12:15],v[96:99],v[140:143],a[12:15]
)A"
R"A(s_waitcnt lgkmcnt(4)
ds_read_b64_tr_b16 v[96:97],v1 offset:256
v_mfma_f32_16x16x32_bf16 a[16:19],v[112:115],v[32:35],a[16:19]
ds_read_b64_tr_b16 v[98:99],v1 offset:5376
ds_read_b64_tr_b16 v[102:103],v1 offset:15616
v_mfma_f32_16x16x32_bf16 a[16:19],v[116:119],v[36:39],a[16:19]
ds_read_b64_tr_b16 v[106:107],v1 offset:25856
v_add_u32 v1,0xffff6000,v1
v_mfma_f32_16x16x32_bf16 a[16:19],v[120:123],v[40:43],a[16:19]
v_mfma_f32_16x16x32_bf16 a[16:19],v[124:127],v[44:47],a[16:19]
v_mfma_f32_16x16x32_bf16 a[20:23],v[124:127],v[180:183],a[20:23]
v_mfma_f32_16x16x32_bf16 a[20:23],v[120:123],v[164:167],a[20:23]
v_mfma_f32_16x16x32_bf16 a[20:23],v[116:119],v[148:151],a[20:23]
v_mfma_f32_16x16x32_bf16 a[20:23],v[112:115],v[132:135],a[20:23]
v_mfma_f32_16x16x32_bf16 a[24:27],v[112:115],v[196:199],a[24:27]
buffer_load_dwordx4 a[188:191],v28,s[28:31],s90 offen offset:3072 sc1
v_mfma_f32_16x16x32_bf16 a[24:27],v[116:119],v[212:215],a[24:27]
v_mfma_f32_16x16x32_bf16 a[24:27],v[120:123],v[228:231],a[24:27]
v_mfma_f32_16x16x32_bf16 a[24:27],v[124:127],v[244:247],a[24:27]
v_mfma_f32_16x16x32_bf16 a[28:31],v[124:127],v[188:191],a[28:31]
ds_read_b64_tr_b16 v[124:125],v2 offset:31008
ds_read_b64_tr_b16 v[126:127],v2 offset:36128
v_mfma_f32_16x16x32_bf16 a[28:31],v[120:123],v[172:175],a[28:31]
ds_read_b64_tr_b16 v[120:121],v2 offset:20768
v_mfma_f32_16x16x32_bf16 a[28:31],v[116:119],v[156:159],a[28:31]
ds_read_b64_tr_b16 v[116:117],v2 offset:10528
v_mfma_f32_16x16x32_bf16 a[28:31],v[112:115],v[140:143],a[28:31]
ds_read_b64_tr_b16 v[112:113],v2 offset:288
v_mfma_f32_16x16x32_bf16 v[128:131],v[64:67],v[32:35],v[128:131]
ds_read_b64_tr_b16 v[114:115],v2 offset:5408
ds_read_b64_tr_b16 v[118:119],v2 offset:15648
v_mfma_f32_16x16x32_bf16 v[128:131],v[68:71],v[36:39],v[128:131]
ds_read_b64_tr_b16 v[122:123],v2 offset:25888
v_add_u32 v2,0xffff6000,v2
v_mfma_f32_16x16x32_bf16 v[128:131],v[72:75],v[40:43],v[128:131]
v_mfma_f32_16x16x32_bf16 v[128:131],v[76:79],v[44:47],v[128:131]
v_mfma_f32_16x16x32_bf16 v[136:139],v[76:79],v[180:183],v[136:139]
v_mfma_f32_16x16x32_bf16 v[136:139],v[72:75],v[164:167],v[136:139]
buffer_load_dwordx4 a[172:175],v28,s[28:31],s89 offen offset:3072 sc1
v_mfma_f32_16x16x32_bf16 v[136:139],v[68:71],v[148:151],v[136:139]
v_mfma_f32_16x16x32_bf16 v[136:139],v[64:67],v[132:135],v[136:139]
v_mfma_f32_16x16x32_bf16 v[144:147],v[64:67],v[196:199],v[144:147]
v_mfma_f32_16x16x32_bf16 v[144:147],v[68:71],v[212:215],v[144:147]
v_mfma_f32_16x16x32_bf16 v[144:147],v[72:75],v[228:231],v[144:147]
v_mfma_f32_16x16x32_bf16 v[144:147],v[76:79],v[244:247],v[144:147]
v_mfma_f32_16x16x32_bf16 v[152:155],v[76:79],v[188:191],v[152:155]
v_mfma_f32_16x16x32_bf16 v[152:155],v[72:75],v[172:175],v[152:155]
v_mfma_f32_16x16x32_bf16 v[152:155],v[68:71],v[156:159],v[152:155]
v_mfma_f32_16x16x32_bf16 v[152:155],v[64:67],v[140:143],v[152:155]
s_mov_b64 exec,0
s_cmp_eq_u32 s60,0
s_cbranch_scc1 L_pd21_%=
s_cmp_eq_u32 s60,1
s_cbranch_scc1 L_pd22_%=
s_cmp_eq_u32 s60,2
s_cbranch_scc1 L_pd23_%=
s_branch L_trtg2S_%=
L_trtg2S_%=:
s_mov_b64 exec,-1
s_waitcnt lgkmcnt(0)
s_barrier
v_mfma_f32_16x16x32_bf16 v[160:163],v[80:83],v[32:35],v[160:163]
buffer_load_dwordx4 v24,s[48:51],s83 offen lds
v_mfma_f32_16x16x32_bf16 v[160:163],v[84:87],v[36:39],v[160:163]
ds_read_b64_tr_b16 v[64:65],v1
ds_read_b64_tr_b16 v[66:67],v1 offset:5120
v_mfma_f32_16x16x32_bf16 v[160:163],v[88:91],v[40:43],v[160:163]
ds_read_b64_tr_b16 v[68:69],v1 offset:10240
v_mfma_f32_16x16x32_bf16 v[160:163],v[92:95],v[44:47],v[160:163]
ds_read_b64_tr_b16 v[70:71],v1 offset:15360
v_mfma_f32_16x16x32_bf16 v[168:171],v[92:95],v[180:183],v[168:171]
ds_read_b64_tr_b16 v[72:73],v1 offset:20480
v_mfma_f32_16x16x32_bf16 v[168:171],v[88:91],v[164:167],v[168:171]
buffer_load_dwordx4 a[156:159],v28,s[28:31],s88 offen offset:3072 sc1
v_mfma_f32_16x16x32_bf16 v[168:171],v[84:87],v[148:151],v[168:171]
ds_read_b64_tr_b16 v[74:75],v1 offset:25600
ds_read_b64_tr_b16 v[76:77],v1 offset:30720
v_mfma_f32_16x16x32_bf16 v[168:171],v[80:83],v[132:135],v[168:171]
ds_read_b64_tr_b16 v[78:79],v1 offset:35840
v_mfma_f32_16x16x32_bf16 v[176:179],v[80:83],v[196:199],v[176:179]
v_mfma_f32_16x16x32_bf16 v[176:179],v[84:87],v[212:215],v[176:179]
v_mfma_f32_16x16x32_bf16 v[176:179],v[88:91],v[228:231],v[176:179]
v_mfma_f32_16x16x32_bf16 v[176:179],v[92:95],v[244:247],v[176:179]
buffer_load_dwordx4 v25,s[48:51],s83 offen offset:1024 lds
v_mfma_f32_16x16x32_bf16 v[184:187],v[92:95],v[188:191],v[184:187]
ds_read_b64_tr_b16 v[92:93],v2 offset:30752
ds_read_b64_tr_b16 v[94:95],v2 offset:35872
v_mfma_f32_16x16x32_bf16 v[184:187],v[88:91],v[172:175],v[184:187]
ds_read_b64_tr_b16 v[88:89],v2 offset:20512
v_mfma_f32_16x16x32_bf16 v[184:187],v[84:87],v[156:159],v[184:187]
ds_read_b64_tr_b16 v[84:85],v2 offset:10272
v_mfma_f32_16x16x32_bf16 v[184:187],v[80:83],v[140:143],v[184:187]
ds_read_b64_tr_b16 v[80:81],v2 offset:32
v_mfma_f32_16x16x32_bf16 v[192:195],v[96:99],v[32:35],v[192:195]
buffer_load_dwordx4 a[140:143],v28,s[28:31],s87 offen offset:3072 sc1
v_mfma_f32_16x16x32_bf16 v[192:195],v[100:103],v[36:39],v[192:195]
s_add_u32 s87,s64,2
s_and_b32 s87,s87,3
s_lshl_b32 s87,s87,1
v_mfma_f32_16x16x32_bf16 v[192:195],v[104:107],v[40:43],v[192:195]
s_lshl_b32 s87,s87,16
s_add_u32 s88,s87,4096
s_add_u32 s89,s87,8192
v_mfma_f32_16x16x32_bf16 v[192:195],v[108:111],v[44:47],v[192:195]
s_add_u32 s90,s87,12288
ds_read_b64_tr_b16 v[82:83],v2 offset:5152
ds_read_b64_tr_b16 v[86:87],v2 offset:15392
v_mfma_f32_16x16x32_bf16 v[200:203],v[108:111],v[180:183],v[200:203]
ds_read_b64_tr_b16 v[90:91],v2 offset:25632
v_mfma_f32_16x16x32_bf16 v[200:203],v[104:107],v[164:167],v[200:203]
buffer_load_dwordx4 a[68:71],v6,s[28:31],s87 offen sc1
v_mfma_f32_16x16x32_bf16 v[200:203],v[100:103],v[148:151],v[200:203]
v_mfma_f32_16x16x32_bf16 v[200:203],v[96:99],v[132:135],v[200:203]
v_mfma_f32_16x16x32_bf16 v[208:211],v[96:99],v[196:199],v[208:211]
v_mfma_f32_16x16x32_bf16 v[208:211],v[100:103],v[212:215],v[208:211]
v_mfma_f32_16x16x32_bf16 v[208:211],v[104:107],v[228:231],v[208:211]
buffer_load_dwordx4 a[76:79],v6,s[28:31],s88 offen sc1
v_mfma_f32_16x16x32_bf16 v[208:211],v[108:111],v[244:247],v[208:211]
v_mfma_f32_16x16x32_bf16 v[216:219],v[108:111],v[188:191],v[216:219]
ds_read_b64_tr_b16 v[108:109],v1 offset:30784
ds_read_b64_tr_b16 v[110:111],v1 offset:35904
v_mfma_f32_16x16x32_bf16 v[216:219],v[104:107],v[172:175],v[216:219]
ds_read_b64_tr_b16 v[104:105],v1 offset:20544
v_mfma_f32_16x16x32_bf16 v[216:219],v[100:103],v[156:159],v[216:219]
ds_read_b64_tr_b16 v[100:101],v1 offset:10304
v_mfma_f32_16x16x32_bf16 v[216:219],v[96:99],v[140:143],v[216:219]
buffer_load_dwordx4 a[84:87],v6,s[28:31],s89 offen sc1
v_mfma_f32_16x16x32_bf16 v[224:227],v[112:115],v[32:35],v[224:227]
ds_read_b64_tr_b16 v[96:97],v1 offset:64
ds_read_b64_tr_b16 v[98:99],v1 offset:5184
v_mfma_f32_16x16x32_bf16 v[224:227],v[116:119],v[36:39],v[224:227]
ds_read_b64_tr_b16 v[102:103],v1 offset:15424
v_mfma_f32_16x16x32_bf16 v[224:227],v[120:123],v[40:43],v[224:227]
ds_read_b64_tr_b16 v[106:107],v1 offset:25664
v_mfma_f32_16x16x32_bf16 v[224:227],v[124:127],v[44:47],v[224:227]
v_mfma_f32_16x16x32_bf16 v[232:235],v[124:127],v[180:183],v[232:235]
buffer_load_dwordx4 a[92:95],v6,s[28:31],s90 offen sc1
v_mfma_f32_16x16x32_bf16 v[232:235],v[120:123],v[164:167],v[232:235]
v_mfma_f32_16x16x32_bf16 v[232:235],v[116:119],v[148:151],v[232:235]
v_mfma_f32_16x16x32_bf16 v[232:235],v[112:115],v[132:135],v[232:235]
v_mfma_f32_16x16x32_bf16 v[240:243],v[112:115],v[196:199],v[240:243]
v_mfma_f32_16x16x32_bf16 v[240:243],v[116:119],v[212:215],v[240:243]
buffer_load_dwordx4 a[124:127],v6,s[28:31],s90 offen offset:1024 sc1
v_mfma_f32_16x16x32_bf16 v[240:243],v[120:123],v[228:231],v[240:243]
v_mfma_f32_16x16x32_bf16 v[240:243],v[124:127],v[244:247],v[240:243]
v_mfma_f32_16x16x32_bf16 v[248:251],v[124:127],v[188:191],v[248:251]
ds_read_b64_tr_b16 v[124:125],v2 offset:30816
ds_read_b64_tr_b16 v[126:127],v2 offset:35936
v_mfma_f32_16x16x32_bf16 v[248:251],v[120:123],v[172:175],v[248:251]
ds_read_b64_tr_b16 v[120:121],v2 offset:20576
v_mfma_f32_16x16x32_bf16 v[248:251],v[116:119],v[156:159],v[248:251]
buffer_load_dwordx4 a[116:119],v6,s[28:31],s89 offen offset:1024 sc1
v_mfma_f32_16x16x32_bf16 v[248:251],v[112:115],v[140:143],v[248:251]
s_waitcnt vmcnt(31) lgkmcnt(3)
ds_read_b64_tr_b16 v[112:113],v2 offset:96
ds_read_b64_tr_b16 v[114:115],v2 offset:5216
v_mfma_f32_16x16x32_bf16 a[192:195],v[64:67],a[128:131],a[192:195]
ds_read_b64_tr_b16 v[116:117],v2 offset:10336
v_mfma_f32_16x16x32_bf16 a[192:195],v[68:71],a[144:147],a[192:195]
s_waitcnt vmcnt(29)
ds_read_b64_tr_b16 v[118:119],v2 offset:15456
v_mfma_f32_16x16x32_bf16 a[192:195],v[72:75],a[160:163],a[192:195]
s_waitcnt vmcnt(25)
ds_read_b64_tr_b16 v[122:123],v2 offset:25696
v_mfma_f32_16x16x32_bf16 a[192:195],v[76:79],a[176:179],a[192:195]
buffer_load_dwordx4 a[108:111],v6,s[28:31],s88 offen offset:1024 sc1
v_mfma_f32_16x16x32_bf16 a[196:199],v[76:79],a[180:183],a[196:199]
s_waitcnt vmcnt(24)
v_mfma_f32_16x16x32_bf16 a[196:199],v[72:75],a[164:167],a[196:199]
s_waitcnt vmcnt(22)
v_mfma_f32_16x16x32_bf16 a[196:199],v[68:71],a[148:151],a[196:199]
s_waitcnt vmcnt(19)
v_mfma_f32_16x16x32_bf16 a[196:199],v[64:67],a[132:135],a[196:199]
s_waitcnt vmcnt(17)
v_mfma_f32_16x16x32_bf16 a[200:203],v[64:67],a[136:139],a[200:203]
buffer_load_dwordx4 a[100:103],v6,s[28:31],s87 offen offset:1024 sc1
v_mfma_f32_16x16x32_bf16 a[200:203],v[68:71],a[152:155],a[200:203]
s_waitcnt vmcnt(16)
v_mfma_f32_16x16x32_bf16 a[200:203],v[72:75],a[168:171],a[200:203]
s_waitcnt vmcnt(14)
v_mfma_f32_16x16x32_bf16 a[200:203],v[76:79],a[184:187],a[200:203]
s_waitcnt vmcnt(13)
v_mfma_f32_16x16x32_bf16 a[204:207],v[76:79],a[188:191],a[204:207]
s_waitcnt vmcnt(10)
ds_read_b64_tr_b16 v[76:77],v1 offset:30848
ds_read_b64_tr_b16 v[78:79],v1 offset:35968
v_mfma_f32_16x16x32_bf16 a[204:207],v[72:75],a[172:175],a[204:207]
buffer_load_dwordx4 a[64:67],v6,s[28:31],s87 offen offset:2048 sc1
v_mfma_f32_16x16x32_bf16 a[204:207],v[68:71],a[156:159],a[204:207]
s_waitcnt vmcnt(9)
ds_read_b64_tr_b16 v[68:69],v1 offset:10368
ds_read_b64_tr_b16 v[70:71],v1 offset:15488
v_mfma_f32_16x16x32_bf16 a[204:207],v[64:67],a[140:143],a[204:207]
ds_read_b64_tr_b16 v[64:65],v1 offset:128
v_mfma_f32_16x16x32_bf16 a[208:211],v[80:83],a[128:131],a[208:211]
ds_read_b64_tr_b16 v[66:67],v1 offset:5248
v_mfma_f32_16x16x32_bf16 a[208:211],v[84:87],a[144:147],a[208:211]
ds_read_b64_tr_b16 v[72:73],v1 offset:20608
v_mfma_f32_16x16x32_bf16 a[208:211],v[88:91],a[160:163],a[208:211]
ds_read_b64_tr_b16 v[74:75],v1 offset:25728
v_mfma_f32_16x16x32_bf16 a[208:211],v[92:95],a[176:179],a[208:211]
buffer_load_dwordx4 v26,s[48:51],s83 offen offset:2048 lds
v_mfma_f32_16x16x32_bf16 a[212:215],v[92:95],a[180:183],a[212:215]
v_mfma_f32_16x16x32_bf16 a[212:215],v[88:91],a[164:167],a[212:215]
v_mfma_f32_16x16x32_bf16 a[212:215],v[84:87],a[148:151],a[212:215]
v_mfma_f32_16x16x32_bf16 a[212:215],v[80:83],a[132:135],a[212:215]
v_mfma_f32_16x16x32_bf16 a[216:219],v[80:83],a[136:139],a[216:219]
buffer_load_dwordx4 a[72:75],v6,s[28:31],s88 offen offset:2048 sc1
v_mfma_f32_16x16x32_bf16 a[216:219],v[84:87],a[152:155],a[216:219]
v_mfma_f32_16x16x32_bf16 a[216:219],v[88:91],a[168:171],a[216:219]
v_mfma_f32_16x16x32_bf16 a[216:219],v[92:95],a[184:187],a[216:219]
v_mfma_f32_16x16x32_bf16 a[220:223],v[92:95],a[188:191],a[220:223]
ds_read_b64_tr_b16 v[92:93],v2 offset:30880
ds_read_b64_tr_b16 v[94:95],v2 offset:36000
v_mfma_f32_16x16x32_bf16 a[220:223],v[88:91],a[172:175],a[220:223]
ds_read_b64_tr_b16 v[88:89],v2 offset:20640
v_mfma_f32_16x16x32_bf16 a[220:223],v[84:87],a[156:159],a[220:223]
buffer_load_dwordx4 v27,s[48:51],s83 offen offset:3072 lds
v_mfma_f32_16x16x32_bf16 a[220:223],v[80:83],a[140:143],a[220:223]
s_add_u32 m0,s86,126976
ds_read_b64_tr_b16 v[80:81],v2 offset:160
ds_read_b64_tr_b16 v[82:83],v2 offset:5280
s_barrier
v_mfma_f32_16x16x32_bf16 a[224:227],v[96:99],a[128:131],a[224:227]
ds_read_b64_tr_b16 v[84:85],v2 offset:10400
v_mfma_f32_16x16x32_bf16 a[224:227],v[100:103],a[144:147],a[224:227]
)A"
R"A(ds_read_b64_tr_b16 v[86:87],v2 offset:15520
v_mfma_f32_16x16x32_bf16 a[224:227],v[104:107],a[160:163],a[224:227]
buffer_store_dwordx2 v[8:9],v10,s[44:47],s18 offen offset:1280 sc1
v_mfma_f32_16x16x32_bf16 a[224:227],v[108:111],a[176:179],a[224:227]
ds_read_b64_tr_b16 v[90:91],v2 offset:25760
v_mfma_f32_16x16x32_bf16 a[228:231],v[108:111],a[180:183],a[228:231]
v_mfma_f32_16x16x32_bf16 a[228:231],v[104:107],a[164:167],a[228:231]
v_mfma_f32_16x16x32_bf16 a[228:231],v[100:103],a[148:151],a[228:231]
v_mfma_f32_16x16x32_bf16 a[228:231],v[96:99],a[132:135],a[228:231]
buffer_load_dwordx4 a[80:83],v6,s[28:31],s89 offen offset:2048 sc1
v_mfma_f32_16x16x32_bf16 a[232:235],v[96:99],a[136:139],a[232:235]
v_mfma_f32_16x16x32_bf16 a[232:235],v[100:103],a[152:155],a[232:235]
v_mfma_f32_16x16x32_bf16 a[232:235],v[104:107],a[168:171],a[232:235]
v_mfma_f32_16x16x32_bf16 a[232:235],v[108:111],a[184:187],a[232:235]
v_mfma_f32_16x16x32_bf16 a[236:239],v[108:111],a[188:191],a[236:239]
s_waitcnt lgkmcnt(0)
ds_read_b64_tr_b16 v[108:109],v1 offset:30912
ds_read_b64_tr_b16 v[110:111],v1 offset:36032
v_mfma_f32_16x16x32_bf16 a[236:239],v[104:107],a[172:175],a[236:239]
buffer_load_dwordx4 v7,s[48:51],s83 offen lds
v_mfma_f32_16x16x32_bf16 a[236:239],v[100:103],a[156:159],a[236:239]
s_add_u32 m0,s86,128000
ds_read_b64_tr_b16 v[100:101],v1 offset:10432
ds_read_b64_tr_b16 v[102:103],v1 offset:15552
v_mfma_f32_16x16x32_bf16 a[236:239],v[96:99],a[140:143],a[236:239]
s_add_u32 s83,s64,1
s_and_b32 s83,s83,3
ds_read_b64_tr_b16 v[96:97],v1 offset:192
v_mfma_f32_16x16x32_bf16 a[240:243],v[112:115],a[128:131],a[240:243]
s_lshl_b32 s83,s83,1
s_or_b32 s83,s83,1
ds_read_b64_tr_b16 v[98:99],v1 offset:5312
v_mfma_f32_16x16x32_bf16 a[240:243],v[116:119],a[144:147],a[240:243]
s_mul_i32 s83,s83,163840
s_add_u32 s83,s83,s85
ds_read_b64_tr_b16 v[104:105],v1 offset:20672
v_mfma_f32_16x16x32_bf16 a[240:243],v[120:123],a[160:163],a[240:243]
buffer_load_dwordx4 a[88:91],v6,s[28:31],s90 offen offset:2048 sc1
v_mfma_f32_16x16x32_bf16 a[240:243],v[124:127],a[176:179],a[240:243]
ds_read_b64_tr_b16 v[106:107],v1 offset:25792
v_mfma_f32_16x16x32_bf16 a[244:247],v[124:127],a[180:183],a[244:247]
v_mfma_f32_16x16x32_bf16 a[244:247],v[120:123],a[164:167],a[244:247]
v_mfma_f32_16x16x32_bf16 a[244:247],v[116:119],a[148:151],a[244:247]
v_mfma_f32_16x16x32_bf16 a[244:247],v[112:115],a[132:135],a[244:247]
v_mfma_f32_16x16x32_bf16 a[248:251],v[112:115],a[136:139],a[248:251]
buffer_load_dwordx4 v24,s[48:51],s84 offen lds
v_mfma_f32_16x16x32_bf16 a[248:251],v[116:119],a[152:155],a[248:251]
v_mfma_f32_16x16x32_bf16 a[248:251],v[120:123],a[168:171],a[248:251]
v_mfma_f32_16x16x32_bf16 a[248:251],v[124:127],a[184:187],a[248:251]
v_mfma_f32_16x16x32_bf16 a[252:255],v[124:127],a[188:191],a[252:255]
ds_read_b64_tr_b16 v[124:125],v2 offset:30944
ds_read_b64_tr_b16 v[126:127],v2 offset:36064
v_mfma_f32_16x16x32_bf16 a[252:255],v[120:123],a[172:175],a[252:255]
buffer_load_dwordx4 a[120:123],v28,s[28:31],s90 offen offset:3072 sc1
v_mfma_f32_16x16x32_bf16 a[252:255],v[116:119],a[156:159],a[252:255]
ds_read_b64_tr_b16 v[116:117],v2 offset:10464
ds_read_b64_tr_b16 v[118:119],v2 offset:15584
v_mfma_f32_16x16x32_bf16 a[252:255],v[112:115],a[140:143],a[252:255]
ds_read_b64_tr_b16 v[112:113],v2 offset:224
v_mfma_f32_16x16x32_bf16 a[0:3],v[64:67],a[128:131],a[0:3]
ds_read_b64_tr_b16 v[114:115],v2 offset:5344
v_mfma_f32_16x16x32_bf16 a[0:3],v[68:71],a[144:147],a[0:3]
ds_read_b64_tr_b16 v[120:121],v2 offset:20704
v_mfma_f32_16x16x32_bf16 a[0:3],v[72:75],a[160:163],a[0:3]
ds_read_b64_tr_b16 v[122:123],v2 offset:25824
v_mfma_f32_16x16x32_bf16 a[0:3],v[76:79],a[176:179],a[0:3]
buffer_load_dwordx4 v25,s[48:51],s84 offen offset:1024 lds
v_mfma_f32_16x16x32_bf16 a[4:7],v[76:79],a[180:183],a[4:7]
v_mfma_f32_16x16x32_bf16 a[4:7],v[72:75],a[164:167],a[4:7]
v_mfma_f32_16x16x32_bf16 a[4:7],v[68:71],a[148:151],a[4:7]
v_mfma_f32_16x16x32_bf16 a[4:7],v[64:67],a[132:135],a[4:7]
v_mfma_f32_16x16x32_bf16 a[8:11],v[64:67],a[136:139],a[8:11]
buffer_load_dwordx4 a[112:115],v28,s[28:31],s89 offen offset:3072 sc1
v_mfma_f32_16x16x32_bf16 a[8:11],v[68:71],a[152:155],a[8:11]
v_mfma_f32_16x16x32_bf16 a[8:11],v[72:75],a[168:171],a[8:11]
v_mfma_f32_16x16x32_bf16 a[8:11],v[76:79],a[184:187],a[8:11]
v_mfma_f32_16x16x32_bf16 a[12:15],v[76:79],a[188:191],a[12:15]
ds_read_b64_tr_b16 v[76:77],v1 offset:30976
ds_read_b64_tr_b16 v[78:79],v1 offset:36096
v_mfma_f32_16x16x32_bf16 a[12:15],v[72:75],a[172:175],a[12:15]
buffer_load_dwordx4 a[104:107],v28,s[28:31],s88 offen offset:3072 sc1
v_mfma_f32_16x16x32_bf16 a[12:15],v[68:71],a[156:159],a[12:15]
ds_read_b64_tr_b16 v[68:69],v1 offset:10496
ds_read_b64_tr_b16 v[70:71],v1 offset:15616
v_mfma_f32_16x16x32_bf16 a[12:15],v[64:67],a[140:143],a[12:15]
ds_read_b64_tr_b16 v[64:65],v1 offset:256
v_mfma_f32_16x16x32_bf16 a[16:19],v[80:83],a[128:131],a[16:19]
ds_read_b64_tr_b16 v[66:67],v1 offset:5376
v_mfma_f32_16x16x32_bf16 a[16:19],v[84:87],a[144:147],a[16:19]
ds_read_b64_tr_b16 v[72:73],v1 offset:20736
v_mfma_f32_16x16x32_bf16 a[16:19],v[88:91],a[160:163],a[16:19]
ds_read_b64_tr_b16 v[74:75],v1 offset:25856
v_add_u32 v1,0xffff6000,v1
v_mfma_f32_16x16x32_bf16 a[16:19],v[92:95],a[176:179],a[16:19]
buffer_load_dwordx4 v26,s[48:51],s84 offen offset:2048 lds
v_mfma_f32_16x16x32_bf16 a[20:23],v[92:95],a[180:183],a[20:23]
v_mfma_f32_16x16x32_bf16 a[20:23],v[88:91],a[164:167],a[20:23]
v_mfma_f32_16x16x32_bf16 a[20:23],v[84:87],a[148:151],a[20:23]
v_mfma_f32_16x16x32_bf16 a[20:23],v[80:83],a[132:135],a[20:23]
v_mfma_f32_16x16x32_bf16 a[24:27],v[80:83],a[136:139],a[24:27]
buffer_load_dwordx4 a[96:99],v28,s[28:31],s87 offen offset:3072 sc1
v_mfma_f32_16x16x32_bf16 a[24:27],v[84:87],a[152:155],a[24:27]
s_add_u32 s87,s64,3
s_and_b32 s87,s87,3
s_lshl_b32 s87,s87,1
v_mfma_f32_16x16x32_bf16 a[24:27],v[88:91],a[168:171],a[24:27]
s_lshl_b32 s87,s87,16
s_add_u32 s88,s87,4096
s_add_u32 s89,s87,8192
v_mfma_f32_16x16x32_bf16 a[24:27],v[92:95],a[184:187],a[24:27]
s_add_u32 s90,s87,12288
v_mfma_f32_16x16x32_bf16 a[28:31],v[92:95],a[188:191],a[28:31]
ds_read_b64_tr_b16 v[92:93],v2 offset:31008
ds_read_b64_tr_b16 v[94:95],v2 offset:36128
v_mfma_f32_16x16x32_bf16 a[28:31],v[88:91],a[172:175],a[28:31]
s_waitcnt lgkmcnt(2)
ds_read_b64_tr_b16 v[88:89],v2 offset:20768
v_mfma_f32_16x16x32_bf16 a[28:31],v[84:87],a[156:159],a[28:31]
buffer_load_dwordx4 v27,s[48:51],s84 offen offset:3072 lds
v_mfma_f32_16x16x32_bf16 a[28:31],v[80:83],a[140:143],a[28:31]
s_add_u32 m0,s86,132096
ds_read_b64_tr_b16 v[80:81],v2 offset:288
ds_read_b64_tr_b16 v[82:83],v2 offset:5408
v_mfma_f32_16x16x32_bf16 v[128:131],v[96:99],a[128:131],v[128:131]
ds_read_b64_tr_b16 v[84:85],v2 offset:10528
v_mfma_f32_16x16x32_bf16 v[128:131],v[100:103],a[144:147],v[128:131]
ds_read_b64_tr_b16 v[86:87],v2 offset:15648
v_mfma_f32_16x16x32_bf16 v[128:131],v[104:107],a[160:163],v[128:131]
ds_read_b64_tr_b16 v[90:91],v2 offset:25888
v_add_u32 v2,0xffff6000,v2
v_mfma_f32_16x16x32_bf16 v[128:131],v[108:111],a[176:179],v[128:131]
v_mfma_f32_16x16x32_bf16 v[136:139],v[108:111],a[180:183],v[136:139]
buffer_load_dwordx4 v7,s[48:51],s84 offen lds
v_mfma_f32_16x16x32_bf16 v[136:139],v[104:107],a[164:167],v[136:139]
s_add_u32 m0,s86,81920
s_add_u32 s84,s83,20480
v_mfma_f32_16x16x32_bf16 v[136:139],v[100:103],a[148:151],v[136:139]
v_mfma_f32_16x16x32_bf16 v[136:139],v[96:99],a[132:135],v[136:139]
v_mfma_f32_16x16x32_bf16 v[144:147],v[96:99],a[136:139],v[144:147]
v_mfma_f32_16x16x32_bf16 v[144:147],v[100:103],a[152:155],v[144:147]
v_mfma_f32_16x16x32_bf16 v[144:147],v[104:107],a[168:171],v[144:147]
buffer_load_dwordx4 v24,s[48:51],s83 offen lds
v_mfma_f32_16x16x32_bf16 v[144:147],v[108:111],a[184:187],v[144:147]
v_mfma_f32_16x16x32_bf16 v[152:155],v[108:111],a[188:191],v[152:155]
ds_read_b64_tr_b16 v[108:109],v1 offset:30720
ds_read_b64_tr_b16 v[110:111],v1 offset:35840
v_mfma_f32_16x16x32_bf16 v[152:155],v[104:107],a[172:175],v[152:155]
ds_read_b64_tr_b16 v[104:105],v1 offset:20480
v_mfma_f32_16x16x32_bf16 v[152:155],v[100:103],a[156:159],v[152:155]
ds_read_b64_tr_b16 v[100:101],v1 offset:10240
v_mfma_f32_16x16x32_bf16 v[152:155],v[96:99],a[140:143],v[152:155]
ds_read_b64_tr_b16 v[96:97],v1
v_mfma_f32_16x16x32_bf16 v[160:163],v[112:115],a[128:131],v[160:163]
buffer_load_dwordx4 v25,s[48:51],s83 offen offset:1024 lds
v_mfma_f32_16x16x32_bf16 v[160:163],v[116:119],a[144:147],v[160:163]
ds_read_b64_tr_b16 v[98:99],v1 offset:5120
ds_read_b64_tr_b16 v[102:103],v1 offset:15360
v_mfma_f32_16x16x32_bf16 v[160:163],v[120:123],a[160:163],v[160:163]
ds_read_b64_tr_b16 v[106:107],v1 offset:25600
v_mfma_f32_16x16x32_bf16 v[160:163],v[124:127],a[176:179],v[160:163]
v_mfma_f32_16x16x32_bf16 v[168:171],v[124:127],a[180:183],v[168:171]
v_mfma_f32_16x16x32_bf16 v[168:171],v[120:123],a[164:167],v[168:171]
v_mfma_f32_16x16x32_bf16 v[168:171],v[116:119],a[148:151],v[168:171]
buffer_load_dwordx4 v26,s[48:51],s83 offen offset:2048 lds
v_mfma_f32_16x16x32_bf16 v[168:171],v[112:115],a[132:135],v[168:171]
v_mfma_f32_16x16x32_bf16 v[176:179],v[112:115],a[136:139],v[176:179]
v_mfma_f32_16x16x32_bf16 v[176:179],v[116:119],a[152:155],v[176:179]
v_mfma_f32_16x16x32_bf16 v[176:179],v[120:123],a[168:171],v[176:179]
v_mfma_f32_16x16x32_bf16 v[176:179],v[124:127],a[184:187],v[176:179]
v_mfma_f32_16x16x32_bf16 v[184:187],v[124:127],a[188:191],v[184:187]
buffer_load_dwordx4 v27,s[48:51],s83 offen offset:3072 lds
v_mfma_f32_16x16x32_bf16 v[184:187],v[120:123],a[172:175],v[184:187]
s_add_u32 m0,s86,86016
ds_read_b64_tr_b16 v[120:121],v2 offset:20512
ds_read_b64_tr_b16 v[122:123],v2 offset:25632
v_mfma_f32_16x16x32_bf16 v[184:187],v[116:119],a[156:159],v[184:187]
ds_read_b64_tr_b16 v[116:117],v2 offset:10272
v_mfma_f32_16x16x32_bf16 v[184:187],v[112:115],a[140:143],v[184:187]
ds_read_b64_tr_b16 v[112:113],v2 offset:32
L_pd21_%=:
s_cbranch_execz L_trtg2S_%=
v_mfma_f32_16x16x32_bf16 v[192:195],v[64:67],a[128:131],v[192:195]
ds_read_b64_tr_b16 v[114:115],v2 offset:5152
v_mfma_f32_16x16x32_bf16 v[192:195],v[68:71],a[144:147],v[192:195]
ds_read_b64_tr_b16 v[118:119],v2 offset:15392
ds_read_b64_tr_b16 v[124:125],v2 offset:30752
v_mfma_f32_16x16x32_bf16 v[192:195],v[72:75],a[160:163],v[192:195]
buffer_load_dwordx4 v7,s[48:51],s83 offen lds
v_mfma_f32_16x16x32_bf16 v[192:195],v[76:79],a[176:179],v[192:195]
s_add_u32 m0,s86,87040
s_add_u32 s83,s64,2
ds_read_b64_tr_b16 v[126:127],v2 offset:35872
v_mfma_f32_16x16x32_bf16 v[200:203],v[76:79],a[180:183],v[200:203]
s_and_b32 s83,s83,3
s_lshl_b32 s83,s83,1
s_or_b32 s83,s83,1
v_mfma_f32_16x16x32_bf16 v[200:203],v[72:75],a[164:167],v[200:203]
s_mul_i32 s83,s83,163840
s_add_u32 s83,s83,s85
v_mfma_f32_16x16x32_bf16 v[200:203],v[68:71],a[148:151],v[200:203]
v_mfma_f32_16x16x32_bf16 v[200:203],v[64:67],a[132:135],v[200:203]
v_mfma_f32_16x16x32_bf16 v[208:211],v[64:67],a[136:139],v[208:211]
buffer_load_dwordx4 v24,s[48:51],s84 offen lds
v_mfma_f32_16x16x32_bf16 v[208:211],v[68:71],a[152:155],v[208:211]
v_mfma_f32_16x16x32_bf16 v[208:211],v[72:75],a[168:171],v[208:211]
v_mfma_f32_16x16x32_bf16 v[208:211],v[76:79],a[184:187],v[208:211]
v_mfma_f32_16x16x32_bf16 v[216:219],v[76:79],a[188:191],v[216:219]
ds_read_b64_tr_b16 v[76:77],v1 offset:30784
ds_read_b64_tr_b16 v[78:79],v1 offset:35904
v_mfma_f32_16x16x32_bf16 v[216:219],v[72:75],a[172:175],v[216:219]
ds_read_b64_tr_b16 v[72:73],v1 offset:20544
v_mfma_f32_16x16x32_bf16 v[216:219],v[68:71],a[156:159],v[216:219]
buffer_load_dwordx2 v[4:5],v3,s[44:47],s91 offen offset:1280 sc1
v_mfma_f32_16x16x32_bf16 v[216:219],v[64:67],a[140:143],v[216:219]
s_waitcnt lgkmcnt(3)
ds_read_b64_tr_b16 v[64:65],v1 offset:64
ds_read_b64_tr_b16 v[66:67],v1 offset:5184
v_mfma_f32_16x16x32_bf16 v[224:227],v[80:83],a[128:131],v[224:227]
ds_read_b64_tr_b16 v[68:69],v1 offset:10304
v_mfma_f32_16x16x32_bf16 v[224:227],v[84:87],a[144:147],v[224:227]
ds_read_b64_tr_b16 v[70:71],v1 offset:15424
v_mfma_f32_16x16x32_bf16 v[224:227],v[88:91],a[160:163],v[224:227]
ds_read_b64_tr_b16 v[74:75],v1 offset:25664
v_mfma_f32_16x16x32_bf16 v[224:227],v[92:95],a[176:179],v[224:227]
)A"
R"A(v_mfma_f32_16x16x32_bf16 v[232:235],v[92:95],a[180:183],v[232:235]
buffer_load_dwordx4 v25,s[48:51],s84 offen offset:1024 lds
v_mfma_f32_16x16x32_bf16 v[232:235],v[88:91],a[164:167],v[232:235]
v_mfma_f32_16x16x32_bf16 v[232:235],v[84:87],a[148:151],v[232:235]
v_mfma_f32_16x16x32_bf16 v[232:235],v[80:83],a[132:135],v[232:235]
v_mfma_f32_16x16x32_bf16 v[240:243],v[80:83],a[136:139],v[240:243]
v_mfma_f32_16x16x32_bf16 v[240:243],v[84:87],a[152:155],v[240:243]
v_mfma_f32_16x16x32_bf16 v[240:243],v[88:91],a[168:171],v[240:243]
buffer_load_dwordx4 v26,s[48:51],s84 offen offset:2048 lds
v_mfma_f32_16x16x32_bf16 v[240:243],v[92:95],a[184:187],v[240:243]
v_mfma_f32_16x16x32_bf16 v[248:251],v[92:95],a[188:191],v[248:251]
ds_read_b64_tr_b16 v[92:93],v2 offset:30816
ds_read_b64_tr_b16 v[94:95],v2 offset:35936
v_mfma_f32_16x16x32_bf16 v[248:251],v[88:91],a[172:175],v[248:251]
ds_read_b64_tr_b16 v[88:89],v2 offset:20576
v_mfma_f32_16x16x32_bf16 v[248:251],v[84:87],a[156:159],v[248:251]
ds_read_b64_tr_b16 v[84:85],v2 offset:10336
v_mfma_f32_16x16x32_bf16 v[248:251],v[80:83],a[140:143],v[248:251]
s_waitcnt vmcnt(25)
ds_read_b64_tr_b16 v[80:81],v2 offset:96
v_mfma_f32_16x16x32_bf16 a[192:195],v[96:99],a[68:71],a[192:195]
buffer_load_dwordx4 v27,s[48:51],s84 offen offset:3072 lds
v_mfma_f32_16x16x32_bf16 a[192:195],v[100:103],a[76:79],a[192:195]
s_add_u32 m0,s86,91136
ds_read_b64_tr_b16 v[82:83],v2 offset:5216
ds_read_b64_tr_b16 v[86:87],v2 offset:15456
v_mfma_f32_16x16x32_bf16 a[192:195],v[104:107],a[84:87],a[192:195]
ds_read_b64_tr_b16 v[90:91],v2 offset:25696
v_mfma_f32_16x16x32_bf16 a[192:195],v[108:111],a[92:95],a[192:195]
v_mfma_f32_16x16x32_bf16 a[196:199],v[108:111],a[124:127],a[196:199]
v_mfma_f32_16x16x32_bf16 a[196:199],v[104:107],a[116:119],a[196:199]
v_mfma_f32_16x16x32_bf16 a[196:199],v[100:103],a[108:111],a[196:199]
buffer_load_dwordx4 v7,s[48:51],s84 offen lds
v_mfma_f32_16x16x32_bf16 a[196:199],v[96:99],a[100:103],a[196:199]
s_add_u32 m0,s86,40960
s_add_u32 s84,s83,20480
v_mfma_f32_16x16x32_bf16 a[200:203],v[96:99],a[64:67],a[200:203]
s_waitcnt vmcnt(25)
v_mfma_f32_16x16x32_bf16 a[200:203],v[100:103],a[72:75],a[200:203]
s_waitcnt vmcnt(22)
v_mfma_f32_16x16x32_bf16 a[200:203],v[104:107],a[80:83],a[200:203]
s_waitcnt vmcnt(18)
v_mfma_f32_16x16x32_bf16 a[200:203],v[108:111],a[88:91],a[200:203]
buffer_load_dwordx4 v[32:35],v6,s[28:31],s87 offen sc1
v_mfma_f32_16x16x32_bf16 a[204:207],v[108:111],a[120:123],a[204:207]
s_waitcnt vmcnt(17)
ds_read_b64_tr_b16 v[108:109],v1 offset:30848
ds_read_b64_tr_b16 v[110:111],v1 offset:35968
v_mfma_f32_16x16x32_bf16 a[204:207],v[104:107],a[112:115],a[204:207]
s_waitcnt vmcnt(16)
ds_read_b64_tr_b16 v[104:105],v1 offset:20608
v_mfma_f32_16x16x32_bf16 a[204:207],v[100:103],a[104:107],a[204:207]
s_waitcnt vmcnt(14)
ds_read_b64_tr_b16 v[100:101],v1 offset:10368
v_mfma_f32_16x16x32_bf16 a[204:207],v[96:99],a[96:99],a[204:207]
ds_read_b64_tr_b16 v[96:97],v1 offset:128
v_mfma_f32_16x16x32_bf16 a[208:211],v[112:115],a[68:71],a[208:211]
buffer_load_dwordx4 v[132:135],v6,s[28:31],s87 offen offset:1024 sc1
v_mfma_f32_16x16x32_bf16 a[208:211],v[116:119],a[76:79],a[208:211]
ds_read_b64_tr_b16 v[98:99],v1 offset:5248
ds_read_b64_tr_b16 v[102:103],v1 offset:15488
v_mfma_f32_16x16x32_bf16 a[208:211],v[120:123],a[84:87],a[208:211]
ds_read_b64_tr_b16 v[106:107],v1 offset:25728
v_mfma_f32_16x16x32_bf16 a[208:211],v[124:127],a[92:95],a[208:211]
v_mfma_f32_16x16x32_bf16 a[212:215],v[124:127],a[124:127],a[212:215]
v_mfma_f32_16x16x32_bf16 a[212:215],v[120:123],a[116:119],a[212:215]
buffer_load_dwordx4 v[196:199],v6,s[28:31],s87 offen offset:2048 sc1
v_mfma_f32_16x16x32_bf16 a[212:215],v[116:119],a[108:111],a[212:215]
v_mfma_f32_16x16x32_bf16 a[212:215],v[112:115],a[100:103],a[212:215]
v_mfma_f32_16x16x32_bf16 a[216:219],v[112:115],a[64:67],a[216:219]
v_mfma_f32_16x16x32_bf16 a[216:219],v[116:119],a[72:75],a[216:219]
v_mfma_f32_16x16x32_bf16 a[216:219],v[120:123],a[80:83],a[216:219]
buffer_load_dwordx4 v[36:39],v6,s[28:31],s88 offen sc1
v_mfma_f32_16x16x32_bf16 a[216:219],v[124:127],a[88:91],a[216:219]
v_mfma_f32_16x16x32_bf16 a[220:223],v[124:127],a[120:123],a[220:223]
ds_read_b64_tr_b16 v[124:125],v2 offset:30880
ds_read_b64_tr_b16 v[126:127],v2 offset:36000
v_mfma_f32_16x16x32_bf16 a[220:223],v[120:123],a[112:115],a[220:223]
ds_read_b64_tr_b16 v[120:121],v2 offset:20640
v_mfma_f32_16x16x32_bf16 a[220:223],v[116:119],a[104:107],a[220:223]
s_waitcnt lgkmcnt(3)
ds_read_b64_tr_b16 v[116:117],v2 offset:10400
v_mfma_f32_16x16x32_bf16 a[220:223],v[112:115],a[96:99],a[220:223]
buffer_load_dwordx4 v[140:143],v28,s[28:31],s87 offen offset:3072 sc1
v_mfma_f32_16x16x32_bf16 a[224:227],v[64:67],a[68:71],a[224:227]
s_add_u32 s87,s64,1
s_and_b32 s87,s87,3
s_lshl_b32 s87,s87,1
v_mfma_f32_16x16x32_bf16 a[224:227],v[68:71],a[76:79],a[224:227]
s_or_b32 s87,s87,1
s_lshl_b32 s87,s87,16
ds_read_b64_tr_b16 v[112:113],v2 offset:160
v_mfma_f32_16x16x32_bf16 a[224:227],v[72:75],a[84:87],a[224:227]
ds_read_b64_tr_b16 v[114:115],v2 offset:5280
ds_read_b64_tr_b16 v[118:119],v2 offset:15520
v_mfma_f32_16x16x32_bf16 a[224:227],v[76:79],a[92:95],a[224:227]
ds_read_b64_tr_b16 v[122:123],v2 offset:25760
v_mfma_f32_16x16x32_bf16 a[228:231],v[76:79],a[124:127],a[228:231]
buffer_load_dwordx4 v[212:215],v6,s[28:31],s88 offen offset:2048 sc1
v_mfma_f32_16x16x32_bf16 a[228:231],v[72:75],a[116:119],a[228:231]
v_mfma_f32_16x16x32_bf16 a[228:231],v[68:71],a[108:111],a[228:231]
v_mfma_f32_16x16x32_bf16 a[228:231],v[64:67],a[100:103],a[228:231]
v_mfma_f32_16x16x32_bf16 a[232:235],v[64:67],a[64:67],a[232:235]
v_mfma_f32_16x16x32_bf16 a[232:235],v[68:71],a[72:75],a[232:235]
buffer_load_dwordx4 v[148:151],v6,s[28:31],s88 offen offset:1024 sc1
v_mfma_f32_16x16x32_bf16 a[232:235],v[72:75],a[80:83],a[232:235]
s_waitcnt vmcnt(11)
v_cmp_ne_u32 s[56:57],s52,v4
v_cmp_ne_u32 s[58:59],s53,v5
v_mfma_f32_16x16x32_bf16 a[232:235],v[76:79],a[88:91],a[232:235]
s_or_b64 s[56:57],s[56:57],s[58:59]
s_cbranch_scc1 L_fr7_%=
L_fo7_%=:
s_lshl_b32 s91,s64,6
v_mfma_f32_16x16x32_bf16 a[236:239],v[76:79],a[120:123],a[236:239]
ds_read_b64_tr_b16 v[76:77],v1 offset:30912
ds_read_b64_tr_b16 v[78:79],v1 offset:36032
v_mfma_f32_16x16x32_bf16 a[236:239],v[72:75],a[112:115],a[236:239]
ds_read_b64_tr_b16 v[72:73],v1 offset:20672
v_bfe_u32 v3,v6,4,2
v_mfma_f32_16x16x32_bf16 a[236:239],v[68:71],a[104:107],a[236:239]
buffer_load_dwordx4 v[40:43],v6,s[28:31],s89 offen sc1
v_mfma_f32_16x16x32_bf16 a[236:239],v[64:67],a[96:99],a[236:239]
ds_read_b64_tr_b16 v[64:65],v1 offset:192
ds_read_b64_tr_b16 v[66:67],v1 offset:5312
v_mfma_f32_16x16x32_bf16 a[240:243],v[80:83],a[68:71],a[240:243]
ds_read_b64_tr_b16 v[68:69],v1 offset:10432
v_min_u32 v3,2,v3
v_mfma_f32_16x16x32_bf16 a[240:243],v[84:87],a[76:79],a[240:243]
ds_read_b64_tr_b16 v[70:71],v1 offset:15552
v_add3_u32 v3,v3,s63,1
v_mfma_f32_16x16x32_bf16 a[240:243],v[88:91],a[84:87],a[240:243]
ds_read_b64_tr_b16 v[74:75],v1 offset:25792
v_and_b32 v3,3,v3
v_mfma_f32_16x16x32_bf16 a[240:243],v[92:95],a[92:95],a[240:243]
buffer_load_dwordx4 a[128:131],v6,s[28:31],s87 offen sc1
v_mfma_f32_16x16x32_bf16 a[244:247],v[92:95],a[124:127],a[244:247]
v_lshlrev_b32 v3,8,v3
v_mfma_f32_16x16x32_bf16 a[244:247],v[88:91],a[116:119],a[244:247]
v_mfma_f32_16x16x32_bf16 a[244:247],v[84:87],a[108:111],a[244:247]
v_mfma_f32_16x16x32_bf16 a[244:247],v[80:83],a[100:103],a[244:247]
v_mfma_f32_16x16x32_bf16 a[248:251],v[80:83],a[64:67],a[248:251]
buffer_load_dwordx4 v[156:159],v28,s[28:31],s88 offen offset:3072 sc1
v_mfma_f32_16x16x32_bf16 a[248:251],v[84:87],a[72:75],a[248:251]
s_add_u32 s88,s87,4096
v_mfma_f32_16x16x32_bf16 a[248:251],v[88:91],a[80:83],a[248:251]
v_mfma_f32_16x16x32_bf16 a[248:251],v[92:95],a[88:91],a[248:251]
v_mfma_f32_16x16x32_bf16 a[252:255],v[92:95],a[120:123],a[252:255]
ds_read_b64_tr_b16 v[92:93],v2 offset:30944
ds_read_b64_tr_b16 v[94:95],v2 offset:36064
v_mfma_f32_16x16x32_bf16 a[252:255],v[88:91],a[112:115],a[252:255]
buffer_load_dwordx4 v[228:231],v6,s[28:31],s89 offen offset:2048 sc1
v_mfma_f32_16x16x32_bf16 a[252:255],v[84:87],a[104:107],a[252:255]
ds_read_b64_tr_b16 v[84:85],v2 offset:10464
ds_read_b64_tr_b16 v[86:87],v2 offset:15584
v_mfma_f32_16x16x32_bf16 a[252:255],v[80:83],a[96:99],a[252:255]
ds_read_b64_tr_b16 v[80:81],v2 offset:224
v_mfma_f32_16x16x32_bf16 a[0:3],v[96:99],a[68:71],a[0:3]
ds_read_b64_tr_b16 v[82:83],v2 offset:5344
v_mfma_f32_16x16x32_bf16 a[0:3],v[100:103],a[76:79],a[0:3]
ds_read_b64_tr_b16 v[88:89],v2 offset:20704
v_mfma_f32_16x16x32_bf16 a[0:3],v[104:107],a[84:87],a[0:3]
buffer_load_dwordx4 v[164:167],v6,s[28:31],s89 offen offset:1024 sc1
v_mfma_f32_16x16x32_bf16 a[0:3],v[108:111],a[92:95],a[0:3]
ds_read_b64_tr_b16 v[90:91],v2 offset:25824
v_mfma_f32_16x16x32_bf16 a[4:7],v[108:111],a[124:127],a[4:7]
v_mfma_f32_16x16x32_bf16 a[4:7],v[104:107],a[116:119],a[4:7]
v_mfma_f32_16x16x32_bf16 a[4:7],v[100:103],a[108:111],a[4:7]
v_mfma_f32_16x16x32_bf16 a[4:7],v[96:99],a[100:103],a[4:7]
buffer_load_dwordx4 v[44:47],v6,s[28:31],s90 offen sc1
v_mfma_f32_16x16x32_bf16 a[8:11],v[96:99],a[64:67],a[8:11]
v_mfma_f32_16x16x32_bf16 a[8:11],v[100:103],a[72:75],a[8:11]
v_mfma_f32_16x16x32_bf16 a[8:11],v[104:107],a[80:83],a[8:11]
v_mfma_f32_16x16x32_bf16 a[8:11],v[108:111],a[88:91],a[8:11]
v_mfma_f32_16x16x32_bf16 a[12:15],v[108:111],a[120:123],a[12:15]
buffer_load_dwordx4 a[144:147],v6,s[28:31],s88 offen sc1
v_mfma_f32_16x16x32_bf16 a[12:15],v[104:107],a[112:115],a[12:15]
ds_read_b64_tr_b16 v[104:105],v1 offset:20736
ds_read_b64_tr_b16 v[106:107],v1 offset:25856
v_mfma_f32_16x16x32_bf16 a[12:15],v[100:103],a[104:107],a[12:15]
ds_read_b64_tr_b16 v[100:101],v1 offset:10496
v_mfma_f32_16x16x32_bf16 a[12:15],v[96:99],a[96:99],a[12:15]
s_waitcnt lgkmcnt(3)
ds_read_b64_tr_b16 v[96:97],v1 offset:256
v_mfma_f32_16x16x32_bf16 a[16:19],v[112:115],a[68:71],a[16:19]
ds_read_b64_tr_b16 v[98:99],v1 offset:5376
v_mfma_f32_16x16x32_bf16 a[16:19],v[116:119],a[76:79],a[16:19]
buffer_load_dwordx4 v[172:175],v28,s[28:31],s89 offen offset:3072 sc1
v_mfma_f32_16x16x32_bf16 a[16:19],v[120:123],a[84:87],a[16:19]
s_add_u32 s89,s87,8192
ds_read_b64_tr_b16 v[102:103],v1 offset:15616
ds_read_b64_tr_b16 v[108:109],v1 offset:30976
v_mfma_f32_16x16x32_bf16 a[16:19],v[124:127],a[92:95],a[16:19]
ds_read_b64_tr_b16 v[110:111],v1 offset:36096
v_add_u32 v1,0x1e000,v1
v_mfma_f32_16x16x32_bf16 a[20:23],v[124:127],a[124:127],a[20:23]
v_mfma_f32_16x16x32_bf16 a[20:23],v[120:123],a[116:119],a[20:23]
v_mfma_f32_16x16x32_bf16 a[20:23],v[116:119],a[108:111],a[20:23]
buffer_load_dwordx4 v[180:183],v6,s[28:31],s90 offen offset:1024 sc1
v_mfma_f32_16x16x32_bf16 a[20:23],v[112:115],a[100:103],a[20:23]
v_mfma_f32_16x16x32_bf16 a[24:27],v[112:115],a[64:67],a[24:27]
v_mfma_f32_16x16x32_bf16 a[24:27],v[116:119],a[72:75],a[24:27]
v_mfma_f32_16x16x32_bf16 a[24:27],v[120:123],a[80:83],a[24:27]
v_mfma_f32_16x16x32_bf16 a[24:27],v[124:127],a[88:91],a[24:27]
buffer_load_dwordx4 a[160:163],v6,s[28:31],s89 offen sc1
v_mfma_f32_16x16x32_bf16 a[28:31],v[124:127],a[120:123],a[28:31]
ds_read_b64_tr_b16 v[124:125],v2 offset:31008
ds_read_b64_tr_b16 v[126:127],v2 offset:36128
v_mfma_f32_16x16x32_bf16 a[28:31],v[120:123],a[112:115],a[28:31]
ds_read_b64_tr_b16 v[120:121],v2 offset:20768
v_mfma_f32_16x16x32_bf16 a[28:31],v[116:119],a[104:107],a[28:31]
ds_read_b64_tr_b16 v[116:117],v2 offset:10528
v_mfma_f32_16x16x32_bf16 a[28:31],v[112:115],a[96:99],a[28:31]
ds_read_b64_tr_b16 v[112:113],v2 offset:288
v_mfma_f32_16x16x32_bf16 v[128:131],v[64:67],a[68:71],v[128:131]
buffer_load_dwordx4 v[244:247],v6,s[28:31],s90 offen offset:2048 sc1
v_mfma_f32_16x16x32_bf16 v[128:131],v[68:71],a[76:79],v[128:131]
ds_read_b64_tr_b16 v[114:115],v2 offset:5408
ds_read_b64_tr_b16 v[118:119],v2 offset:15648
v_mfma_f32_16x16x32_bf16 v[128:131],v[72:75],a[84:87],v[128:131]
ds_read_b64_tr_b16 v[122:123],v2 offset:25888
v_add_u32 v2,0x1e000,v2
v_mfma_f32_16x16x32_bf16 v[128:131],v[76:79],a[92:95],v[128:131]
v_mfma_f32_16x16x32_bf16 v[136:139],v[76:79],a[124:127],v[136:139]
v_mfma_f32_16x16x32_bf16 v[136:139],v[72:75],a[116:119],v[136:139]
)A"
R"A(buffer_load_dwordx4 v[188:191],v28,s[28:31],s90 offen offset:3072 sc1
v_mfma_f32_16x16x32_bf16 v[136:139],v[68:71],a[108:111],v[136:139]
s_add_u32 s90,s87,12288
v_mfma_f32_16x16x32_bf16 v[136:139],v[64:67],a[100:103],v[136:139]
v_mfma_f32_16x16x32_bf16 v[144:147],v[64:67],a[64:67],v[144:147]
v_mfma_f32_16x16x32_bf16 v[144:147],v[68:71],a[72:75],v[144:147]
v_mfma_f32_16x16x32_bf16 v[144:147],v[72:75],a[80:83],v[144:147]
buffer_load_dwordx4 a[176:179],v6,s[28:31],s90 offen sc1
v_mfma_f32_16x16x32_bf16 v[144:147],v[76:79],a[88:91],v[144:147]
v_mfma_f32_16x16x32_bf16 v[152:155],v[76:79],a[120:123],v[152:155]
v_mfma_f32_16x16x32_bf16 v[152:155],v[72:75],a[112:115],v[152:155]
v_mfma_f32_16x16x32_bf16 v[152:155],v[68:71],a[104:107],v[152:155]
v_mfma_f32_16x16x32_bf16 v[152:155],v[64:67],a[96:99],v[152:155]
buffer_load_dwordx4 a[180:183],v6,s[28:31],s90 offen offset:1024 sc1
s_waitcnt vmcnt(21)
s_barrier
v_mfma_f32_16x16x32_bf16 v[160:163],v[80:83],a[68:71],v[160:163]
ds_read_b64_tr_b16 v[64:65],v1
ds_read_b64_tr_b16 v[66:67],v1 offset:5120
v_mfma_f32_16x16x32_bf16 v[160:163],v[84:87],a[76:79],v[160:163]
ds_read_b64_tr_b16 v[68:69],v1 offset:10240
v_mfma_f32_16x16x32_bf16 v[160:163],v[88:91],a[84:87],v[160:163]
ds_read_b64_tr_b16 v[70:71],v1 offset:15360
v_mfma_f32_16x16x32_bf16 v[160:163],v[92:95],a[92:95],v[160:163]
buffer_load_dwordx4 a[164:167],v6,s[28:31],s89 offen offset:1024 sc1
v_mfma_f32_16x16x32_bf16 v[168:171],v[92:95],a[124:127],v[168:171]
ds_read_b64_tr_b16 v[72:73],v1 offset:20480
ds_read_b64_tr_b16 v[74:75],v1 offset:25600
v_mfma_f32_16x16x32_bf16 v[168:171],v[88:91],a[116:119],v[168:171]
ds_read_b64_tr_b16 v[76:77],v1 offset:30720
v_mfma_f32_16x16x32_bf16 v[168:171],v[84:87],a[108:111],v[168:171]
ds_read_b64_tr_b16 v[78:79],v1 offset:35840
v_mfma_f32_16x16x32_bf16 v[168:171],v[80:83],a[100:103],v[168:171]
v_mfma_f32_16x16x32_bf16 v[176:179],v[80:83],a[64:67],v[176:179]
buffer_load_dwordx4 a[148:151],v6,s[28:31],s88 offen offset:1024 sc1
v_mfma_f32_16x16x32_bf16 v[176:179],v[84:87],a[72:75],v[176:179]
v_mfma_f32_16x16x32_bf16 v[176:179],v[88:91],a[80:83],v[176:179]
v_mfma_f32_16x16x32_bf16 v[176:179],v[92:95],a[88:91],v[176:179]
v_mfma_f32_16x16x32_bf16 v[184:187],v[92:95],a[120:123],v[184:187]
ds_read_b64_tr_b16 v[92:93],v2 offset:30752
ds_read_b64_tr_b16 v[94:95],v2 offset:35872
v_mfma_f32_16x16x32_bf16 v[184:187],v[88:91],a[112:115],v[184:187]
buffer_load_dwordx4 a[132:135],v6,s[28:31],s87 offen offset:1024 sc1
v_mfma_f32_16x16x32_bf16 v[184:187],v[84:87],a[104:107],v[184:187]
ds_read_b64_tr_b16 v[84:85],v2 offset:10272
ds_read_b64_tr_b16 v[86:87],v2 offset:15392
v_mfma_f32_16x16x32_bf16 v[184:187],v[80:83],a[96:99],v[184:187]
s_waitcnt lgkmcnt(4)
ds_read_b64_tr_b16 v[80:81],v2 offset:32
v_mfma_f32_16x16x32_bf16 v[192:195],v[96:99],a[68:71],v[192:195]
ds_read_b64_tr_b16 v[82:83],v2 offset:5152
v_mfma_f32_16x16x32_bf16 v[192:195],v[100:103],a[76:79],v[192:195]
ds_read_b64_tr_b16 v[88:89],v2 offset:20512
v_mfma_f32_16x16x32_bf16 v[192:195],v[104:107],a[84:87],v[192:195]
ds_read_b64_tr_b16 v[90:91],v2 offset:25632
v_mfma_f32_16x16x32_bf16 v[192:195],v[108:111],a[92:95],v[192:195]
buffer_load_dwordx4 v24,s[48:51],s83 offen lds
v_mfma_f32_16x16x32_bf16 v[200:203],v[108:111],a[124:127],v[200:203]
v_mfma_f32_16x16x32_bf16 v[200:203],v[104:107],a[116:119],v[200:203]
v_mfma_f32_16x16x32_bf16 v[200:203],v[100:103],a[108:111],v[200:203]
v_mfma_f32_16x16x32_bf16 v[200:203],v[96:99],a[100:103],v[200:203]
v_mfma_f32_16x16x32_bf16 v[208:211],v[96:99],a[64:67],v[208:211]
buffer_load_dwordx4 a[136:139],v6,s[28:31],s87 offen offset:2048 sc1
v_mfma_f32_16x16x32_bf16 v[208:211],v[100:103],a[72:75],v[208:211]
v_mfma_f32_16x16x32_bf16 v[208:211],v[104:107],a[80:83],v[208:211]
v_mfma_f32_16x16x32_bf16 v[208:211],v[108:111],a[88:91],v[208:211]
v_mfma_f32_16x16x32_bf16 v[216:219],v[108:111],a[120:123],v[216:219]
ds_read_b64_tr_b16 v[108:109],v1 offset:30784
ds_read_b64_tr_b16 v[110:111],v1 offset:35904
v_mfma_f32_16x16x32_bf16 v[216:219],v[104:107],a[112:115],v[216:219]
ds_read_b64_tr_b16 v[104:105],v1 offset:20544
v_mfma_f32_16x16x32_bf16 v[216:219],v[100:103],a[104:107],v[216:219]
buffer_load_dwordx4 v25,s[48:51],s83 offen offset:1024 lds
v_mfma_f32_16x16x32_bf16 v[216:219],v[96:99],a[96:99],v[216:219]
ds_read_b64_tr_b16 v[96:97],v1 offset:64
ds_read_b64_tr_b16 v[98:99],v1 offset:5184
v_mfma_f32_16x16x32_bf16 v[224:227],v[112:115],a[68:71],v[224:227]
ds_read_b64_tr_b16 v[100:101],v1 offset:10304
v_mfma_f32_16x16x32_bf16 v[224:227],v[116:119],a[76:79],v[224:227]
ds_read_b64_tr_b16 v[102:103],v1 offset:15424
v_mfma_f32_16x16x32_bf16 v[224:227],v[120:123],a[84:87],v[224:227]
ds_read_b64_tr_b16 v[106:107],v1 offset:25664
v_mfma_f32_16x16x32_bf16 v[224:227],v[124:127],a[92:95],v[224:227]
buffer_load_dwordx4 a[152:155],v6,s[28:31],s88 offen offset:2048 sc1
v_mfma_f32_16x16x32_bf16 v[232:235],v[124:127],a[124:127],v[232:235]
v_mfma_f32_16x16x32_bf16 v[232:235],v[120:123],a[116:119],v[232:235]
v_mfma_f32_16x16x32_bf16 v[232:235],v[116:119],a[108:111],v[232:235]
v_mfma_f32_16x16x32_bf16 v[232:235],v[112:115],a[100:103],v[232:235]
v_mfma_f32_16x16x32_bf16 v[240:243],v[112:115],a[64:67],v[240:243]
v_mfma_f32_16x16x32_bf16 v[240:243],v[116:119],a[72:75],v[240:243]
buffer_load_dwordx4 v26,s[48:51],s83 offen offset:2048 lds
v_mfma_f32_16x16x32_bf16 v[240:243],v[120:123],a[80:83],v[240:243]
v_mfma_f32_16x16x32_bf16 v[240:243],v[124:127],a[88:91],v[240:243]
v_mfma_f32_16x16x32_bf16 v[248:251],v[124:127],a[120:123],v[248:251]
ds_read_b64_tr_b16 v[124:125],v2 offset:30816
ds_read_b64_tr_b16 v[126:127],v2 offset:35936
v_mfma_f32_16x16x32_bf16 v[248:251],v[120:123],a[112:115],v[248:251]
ds_read_b64_tr_b16 v[120:121],v2 offset:20576
v_mfma_f32_16x16x32_bf16 v[248:251],v[116:119],a[104:107],v[248:251]
buffer_load_dwordx4 a[168:171],v6,s[28:31],s89 offen offset:2048 sc1
v_mfma_f32_16x16x32_bf16 v[248:251],v[112:115],a[96:99],v[248:251]
s_waitcnt vmcnt(22)
ds_read_b64_tr_b16 v[112:113],v2 offset:96
ds_read_b64_tr_b16 v[114:115],v2 offset:5216
v_mfma_f32_16x16x32_bf16 a[192:195],v[64:67],v[32:35],a[192:195]
ds_read_b64_tr_b16 v[116:117],v2 offset:10336
v_mfma_f32_16x16x32_bf16 a[192:195],v[68:71],v[36:39],a[192:195]
ds_read_b64_tr_b16 v[118:119],v2 offset:15456
v_mfma_f32_16x16x32_bf16 a[192:195],v[72:75],v[40:43],a[192:195]
s_waitcnt vmcnt(17)
ds_read_b64_tr_b16 v[122:123],v2 offset:25696
v_mfma_f32_16x16x32_bf16 a[192:195],v[76:79],v[44:47],a[192:195]
s_waitcnt vmcnt(14)
v_mfma_f32_16x16x32_bf16 a[196:199],v[76:79],v[180:183],a[196:199]
buffer_load_dwordx4 v27,s[48:51],s83 offen offset:3072 lds
v_mfma_f32_16x16x32_bf16 a[196:199],v[72:75],v[164:167],a[196:199]
s_add_u32 m0,s86,45056
v_mfma_f32_16x16x32_bf16 a[196:199],v[68:71],v[148:151],a[196:199]
v_mfma_f32_16x16x32_bf16 a[196:199],v[64:67],v[132:135],a[196:199]
v_mfma_f32_16x16x32_bf16 a[200:203],v[64:67],v[196:199],a[200:203]
v_mfma_f32_16x16x32_bf16 a[200:203],v[68:71],v[212:215],a[200:203]
buffer_load_dwordx4 a[184:187],v6,s[28:31],s90 offen offset:2048 sc1
v_mfma_f32_16x16x32_bf16 a[200:203],v[72:75],v[228:231],a[200:203]
s_waitcnt vmcnt(14)
v_mfma_f32_16x16x32_bf16 a[200:203],v[76:79],v[244:247],a[200:203]
s_waitcnt vmcnt(13)
v_mfma_f32_16x16x32_bf16 a[204:207],v[76:79],v[188:191],a[204:207]
ds_read_b64_tr_b16 v[76:77],v1 offset:30848
ds_read_b64_tr_b16 v[78:79],v1 offset:35968
v_mfma_f32_16x16x32_bf16 a[204:207],v[72:75],v[172:175],a[204:207]
ds_read_b64_tr_b16 v[72:73],v1 offset:20608
v_mfma_f32_16x16x32_bf16 a[204:207],v[68:71],v[156:159],a[204:207]
s_waitcnt lgkmcnt(3)
ds_read_b64_tr_b16 v[68:69],v1 offset:10368
v_mfma_f32_16x16x32_bf16 a[204:207],v[64:67],v[140:143],a[204:207]
buffer_load_dwordx4 v7,s[48:51],s83 offen lds
v_mfma_f32_16x16x32_bf16 a[208:211],v[80:83],v[32:35],a[208:211]
s_add_u32 m0,s86,46080
ds_read_b64_tr_b16 v[64:65],v1 offset:128
ds_read_b64_tr_b16 v[66:67],v1 offset:5248
v_mfma_f32_16x16x32_bf16 a[208:211],v[84:87],v[36:39],a[208:211]
s_mul_i32 s83,s63,20480
ds_read_b64_tr_b16 v[70:71],v1 offset:15488
v_mfma_f32_16x16x32_bf16 a[208:211],v[88:91],v[40:43],a[208:211]
ds_read_b64_tr_b16 v[74:75],v1 offset:25728
v_mfma_f32_16x16x32_bf16 a[208:211],v[92:95],v[44:47],a[208:211]
v_mfma_f32_16x16x32_bf16 a[212:215],v[92:95],v[180:183],a[212:215]
buffer_load_dwordx4 a[188:191],v28,s[28:31],s90 offen offset:3072 sc1
v_mfma_f32_16x16x32_bf16 a[212:215],v[88:91],v[164:167],a[212:215]
v_mfma_f32_16x16x32_bf16 a[212:215],v[84:87],v[148:151],a[212:215]
v_mfma_f32_16x16x32_bf16 a[212:215],v[80:83],v[132:135],a[212:215]
v_mfma_f32_16x16x32_bf16 a[216:219],v[80:83],v[196:199],a[216:219]
v_mfma_f32_16x16x32_bf16 a[216:219],v[84:87],v[212:215],a[216:219]
v_mfma_f32_16x16x32_bf16 a[216:219],v[88:91],v[228:231],a[216:219]
buffer_load_dwordx4 v24,s[48:51],s84 offen lds
v_mfma_f32_16x16x32_bf16 a[216:219],v[92:95],v[244:247],a[216:219]
v_mfma_f32_16x16x32_bf16 a[220:223],v[92:95],v[188:191],a[220:223]
ds_read_b64_tr_b16 v[92:93],v2 offset:30880
ds_read_b64_tr_b16 v[94:95],v2 offset:36000
v_mfma_f32_16x16x32_bf16 a[220:223],v[88:91],v[172:175],a[220:223]
ds_read_b64_tr_b16 v[88:89],v2 offset:20640
v_mfma_f32_16x16x32_bf16 a[220:223],v[84:87],v[156:159],a[220:223]
ds_read_b64_tr_b16 v[84:85],v2 offset:10400
v_mfma_f32_16x16x32_bf16 a[220:223],v[80:83],v[140:143],a[220:223]
ds_read_b64_tr_b16 v[80:81],v2 offset:160
v_mfma_f32_16x16x32_bf16 a[224:227],v[96:99],v[32:35],a[224:227]
buffer_load_dwordx4 v25,s[48:51],s84 offen offset:1024 lds
v_mfma_f32_16x16x32_bf16 a[224:227],v[100:103],v[36:39],a[224:227]
ds_read_b64_tr_b16 v[82:83],v2 offset:5280
ds_read_b64_tr_b16 v[86:87],v2 offset:15520
v_mfma_f32_16x16x32_bf16 a[224:227],v[104:107],v[40:43],a[224:227]
ds_read_b64_tr_b16 v[90:91],v2 offset:25760
v_mfma_f32_16x16x32_bf16 a[224:227],v[108:111],v[44:47],a[224:227]
v_mfma_f32_16x16x32_bf16 a[228:231],v[108:111],v[180:183],a[228:231]
v_mfma_f32_16x16x32_bf16 a[228:231],v[104:107],v[164:167],a[228:231]
buffer_load_dwordx4 a[172:175],v28,s[28:31],s89 offen offset:3072 sc1
v_mfma_f32_16x16x32_bf16 a[228:231],v[100:103],v[148:151],a[228:231]
v_mfma_f32_16x16x32_bf16 a[228:231],v[96:99],v[132:135],a[228:231]
v_mfma_f32_16x16x32_bf16 a[232:235],v[96:99],v[196:199],a[232:235]
v_mfma_f32_16x16x32_bf16 a[232:235],v[100:103],v[212:215],a[232:235]
v_mfma_f32_16x16x32_bf16 a[232:235],v[104:107],v[228:231],a[232:235]
v_mfma_f32_16x16x32_bf16 a[232:235],v[108:111],v[244:247],a[232:235]
buffer_load_dwordx4 v26,s[48:51],s84 offen offset:2048 lds
v_mfma_f32_16x16x32_bf16 a[236:239],v[108:111],v[188:191],a[236:239]
ds_read_b64_tr_b16 v[108:109],v1 offset:30912
ds_read_b64_tr_b16 v[110:111],v1 offset:36032
v_mfma_f32_16x16x32_bf16 a[236:239],v[104:107],v[172:175],a[236:239]
ds_read_b64_tr_b16 v[104:105],v1 offset:20672
v_mfma_f32_16x16x32_bf16 a[236:239],v[100:103],v[156:159],a[236:239]
ds_read_b64_tr_b16 v[100:101],v1 offset:10432
v_mfma_f32_16x16x32_bf16 a[236:239],v[96:99],v[140:143],a[236:239]
ds_read_b64_tr_b16 v[96:97],v1 offset:192
v_mfma_f32_16x16x32_bf16 a[240:243],v[112:115],v[32:35],a[240:243]
ds_read_b64_tr_b16 v[98:99],v1 offset:5312
ds_read_b64_tr_b16 v[102:103],v1 offset:15552
v_mfma_f32_16x16x32_bf16 a[240:243],v[116:119],v[36:39],a[240:243]
buffer_load_dwordx4 v27,s[48:51],s84 offen offset:3072 lds
v_mfma_f32_16x16x32_bf16 a[240:243],v[120:123],v[40:43],a[240:243]
s_add_u32 m0,s86,50176
ds_read_b64_tr_b16 v[106:107],v1 offset:25792
v_add_u32 v1,0xffff6000,v1
v_mfma_f32_16x16x32_bf16 a[240:243],v[124:127],v[44:47],a[240:243]
v_mfma_f32_16x16x32_bf16 a[244:247],v[124:127],v[180:183],a[244:247]
v_mfma_f32_16x16x32_bf16 a[244:247],v[120:123],v[164:167],a[244:247]
v_mfma_f32_16x16x32_bf16 a[244:247],v[116:119],v[148:151],a[244:247]
buffer_load_dwordx4 a[140:143],v28,s[28:31],s87 offen offset:3072 sc1
v_mfma_f32_16x16x32_bf16 a[244:247],v[112:115],v[132:135],a[244:247]
s_add_u32 s87,s64,2
s_and_b32 s87,s87,3
s_lshl_b32 s87,s87,1
s_or_b32 s87,s87,1
s_lshl_b32 s87,s87,16
s_add_u32 s89,s87,8192
s_add_u32 s90,s87,12288
v_mfma_f32_16x16x32_bf16 a[248:251],v[112:115],v[196:199],a[248:251]
)A"
R"A(v_mfma_f32_16x16x32_bf16 a[248:251],v[116:119],v[212:215],a[248:251]
v_mfma_f32_16x16x32_bf16 a[248:251],v[120:123],v[228:231],a[248:251]
v_mfma_f32_16x16x32_bf16 a[248:251],v[124:127],v[244:247],a[248:251]
buffer_load_dwordx4 a[68:71],v6,s[28:31],s87 offen sc1
v_mfma_f32_16x16x32_bf16 a[252:255],v[124:127],v[188:191],a[252:255]
ds_read_b64_tr_b16 v[124:125],v2 offset:30944
ds_read_b64_tr_b16 v[126:127],v2 offset:36064
v_mfma_f32_16x16x32_bf16 a[252:255],v[120:123],v[172:175],a[252:255]
ds_read_b64_tr_b16 v[120:121],v2 offset:20704
v_mfma_f32_16x16x32_bf16 a[252:255],v[116:119],v[156:159],a[252:255]
ds_read_b64_tr_b16 v[116:117],v2 offset:10464
v_mfma_f32_16x16x32_bf16 a[252:255],v[112:115],v[140:143],a[252:255]
s_waitcnt lgkmcnt(4)
ds_read_b64_tr_b16 v[112:113],v2 offset:224
v_mfma_f32_16x16x32_bf16 a[0:3],v[64:67],v[32:35],a[0:3]
buffer_load_dwordx4 a[156:159],v28,s[28:31],s88 offen offset:3072 sc1
v_mfma_f32_16x16x32_bf16 a[0:3],v[68:71],v[36:39],a[0:3]
s_add_u32 s88,s87,4096
ds_read_b64_tr_b16 v[114:115],v2 offset:5344
ds_read_b64_tr_b16 v[118:119],v2 offset:15584
v_mfma_f32_16x16x32_bf16 a[0:3],v[72:75],v[40:43],a[0:3]
ds_read_b64_tr_b16 v[122:123],v2 offset:25824
v_add_u32 v2,0xffff6000,v2
v_mfma_f32_16x16x32_bf16 a[0:3],v[76:79],v[44:47],a[0:3]
v_mfma_f32_16x16x32_bf16 a[4:7],v[76:79],v[180:183],a[4:7]
v_mfma_f32_16x16x32_bf16 a[4:7],v[72:75],v[164:167],a[4:7]
v_mfma_f32_16x16x32_bf16 a[4:7],v[68:71],v[148:151],a[4:7]
buffer_load_dwordx4 v7,s[48:51],s84 offen lds
v_mfma_f32_16x16x32_bf16 a[4:7],v[64:67],v[132:135],a[4:7]
v_mfma_f32_16x16x32_bf16 a[8:11],v[64:67],v[196:199],a[8:11]
v_mfma_f32_16x16x32_bf16 a[8:11],v[68:71],v[212:215],a[8:11]
v_mfma_f32_16x16x32_bf16 a[8:11],v[72:75],v[228:231],a[8:11]
v_mfma_f32_16x16x32_bf16 a[8:11],v[76:79],v[244:247],a[8:11]
buffer_load_dwordx4 a[76:79],v6,s[28:31],s88 offen sc1
v_mfma_f32_16x16x32_bf16 a[12:15],v[76:79],v[188:191],a[12:15]
ds_read_b64_tr_b16 v[76:77],v1 offset:30720
ds_read_b64_tr_b16 v[78:79],v1 offset:35840
v_mfma_f32_16x16x32_bf16 a[12:15],v[72:75],v[172:175],a[12:15]
ds_read_b64_tr_b16 v[72:73],v1 offset:20480
v_mfma_f32_16x16x32_bf16 a[12:15],v[68:71],v[156:159],a[12:15]
ds_read_b64_tr_b16 v[68:69],v1 offset:10240
v_mfma_f32_16x16x32_bf16 a[12:15],v[64:67],v[140:143],a[12:15]
ds_read_b64_tr_b16 v[64:65],v1
v_mfma_f32_16x16x32_bf16 a[16:19],v[80:83],v[32:35],a[16:19]
buffer_load_dwordx4 a[84:87],v6,s[28:31],s89 offen sc1
v_mfma_f32_16x16x32_bf16 a[16:19],v[84:87],v[36:39],a[16:19]
ds_read_b64_tr_b16 v[66:67],v1 offset:5120
ds_read_b64_tr_b16 v[70:71],v1 offset:15360
v_mfma_f32_16x16x32_bf16 a[16:19],v[88:91],v[40:43],a[16:19]
ds_read_b64_tr_b16 v[74:75],v1 offset:25600
s_nop 0
L_pd22_%=:
s_cbranch_execz L_trtg2S_%=
v_add_u32 v1,0xffff6000,v1
v_mfma_f32_16x16x32_bf16 a[16:19],v[92:95],v[44:47],a[16:19]
v_mfma_f32_16x16x32_bf16 a[20:23],v[92:95],v[180:183],a[20:23]
v_mfma_f32_16x16x32_bf16 a[20:23],v[88:91],v[164:167],a[20:23]
buffer_load_dwordx4 a[92:95],v6,s[28:31],s90 offen sc1
v_mfma_f32_16x16x32_bf16 a[20:23],v[84:87],v[148:151],a[20:23]
v_mfma_f32_16x16x32_bf16 a[20:23],v[80:83],v[132:135],a[20:23]
v_mfma_f32_16x16x32_bf16 a[24:27],v[80:83],v[196:199],a[24:27]
v_mfma_f32_16x16x32_bf16 a[24:27],v[84:87],v[212:215],a[24:27]
v_mfma_f32_16x16x32_bf16 a[24:27],v[88:91],v[228:231],a[24:27]
buffer_load_dwordx4 a[124:127],v6,s[28:31],s90 offen offset:1024 sc1
v_mfma_f32_16x16x32_bf16 a[24:27],v[92:95],v[244:247],a[24:27]
v_mfma_f32_16x16x32_bf16 a[28:31],v[92:95],v[188:191],a[28:31]
ds_read_b64_tr_b16 v[92:93],v2 offset:30752
ds_read_b64_tr_b16 v[94:95],v2 offset:35872
v_mfma_f32_16x16x32_bf16 a[28:31],v[88:91],v[172:175],a[28:31]
ds_read_b64_tr_b16 v[88:89],v2 offset:20512
v_mfma_f32_16x16x32_bf16 a[28:31],v[84:87],v[156:159],a[28:31]
ds_read_b64_tr_b16 v[84:85],v2 offset:10272
v_mfma_f32_16x16x32_bf16 a[28:31],v[80:83],v[140:143],a[28:31]
buffer_load_dwordx4 a[116:119],v6,s[28:31],s89 offen offset:1024 sc1
v_mfma_f32_16x16x32_bf16 v[128:131],v[96:99],v[32:35],v[128:131]
ds_read_b64_tr_b16 v[80:81],v2 offset:32
ds_read_b64_tr_b16 v[82:83],v2 offset:5152
v_mfma_f32_16x16x32_bf16 v[128:131],v[100:103],v[36:39],v[128:131]
ds_read_b64_tr_b16 v[86:87],v2 offset:15392
v_mfma_f32_16x16x32_bf16 v[128:131],v[104:107],v[40:43],v[128:131]
ds_read_b64_tr_b16 v[90:91],v2 offset:25632
v_add_u32 v2,0xffff6000,v2
v_mfma_f32_16x16x32_bf16 v[128:131],v[108:111],v[44:47],v[128:131]
v_mfma_f32_16x16x32_bf16 v[136:139],v[108:111],v[180:183],v[136:139]
buffer_load_dwordx4 a[108:111],v6,s[28:31],s88 offen offset:1024 sc1
v_mfma_f32_16x16x32_bf16 v[136:139],v[104:107],v[164:167],v[136:139]
v_mfma_f32_16x16x32_bf16 v[136:139],v[100:103],v[148:151],v[136:139]
v_mfma_f32_16x16x32_bf16 v[136:139],v[96:99],v[132:135],v[136:139]
v_mfma_f32_16x16x32_bf16 v[144:147],v[96:99],v[196:199],v[144:147]
v_mfma_f32_16x16x32_bf16 v[144:147],v[100:103],v[212:215],v[144:147]
buffer_load_dwordx4 a[100:103],v6,s[28:31],s87 offen offset:1024 sc1
v_mfma_f32_16x16x32_bf16 v[144:147],v[104:107],v[228:231],v[144:147]
v_mfma_f32_16x16x32_bf16 v[144:147],v[108:111],v[244:247],v[144:147]
v_mfma_f32_16x16x32_bf16 v[152:155],v[108:111],v[188:191],v[152:155]
v_mfma_f32_16x16x32_bf16 v[152:155],v[104:107],v[172:175],v[152:155]
v_mfma_f32_16x16x32_bf16 v[152:155],v[100:103],v[156:159],v[152:155]
buffer_load_dwordx4 a[64:67],v6,s[28:31],s87 offen offset:2048 sc1
v_mfma_f32_16x16x32_bf16 v[152:155],v[96:99],v[140:143],v[152:155]
s_waitcnt vmcnt(8)
s_barrier
s_waitcnt lgkmcnt(0)
v_mfma_f32_16x16x32_bf16 v[160:163],v[112:115],v[32:35],v[160:163]
ds_read_b64_tr_b16 v[96:97],v1
ds_read_b64_tr_b16 v[98:99],v1 offset:5120
v_mfma_f32_16x16x32_bf16 v[160:163],v[116:119],v[36:39],v[160:163]
ds_read_b64_tr_b16 v[100:101],v1 offset:10240
v_mfma_f32_16x16x32_bf16 v[160:163],v[120:123],v[40:43],v[160:163]
buffer_load_dwordx4 a[72:75],v6,s[28:31],s88 offen offset:2048 sc1
v_mfma_f32_16x16x32_bf16 v[160:163],v[124:127],v[44:47],v[160:163]
ds_read_b64_tr_b16 v[102:103],v1 offset:15360
ds_read_b64_tr_b16 v[104:105],v1 offset:20480
v_mfma_f32_16x16x32_bf16 v[168:171],v[124:127],v[180:183],v[168:171]
ds_read_b64_tr_b16 v[106:107],v1 offset:25600
v_mfma_f32_16x16x32_bf16 v[168:171],v[120:123],v[164:167],v[168:171]
ds_read_b64_tr_b16 v[108:109],v1 offset:30720
v_mfma_f32_16x16x32_bf16 v[168:171],v[116:119],v[148:151],v[168:171]
ds_read_b64_tr_b16 v[110:111],v1 offset:35840
v_add_u32 v1,0xa000,v1
v_mfma_f32_16x16x32_bf16 v[168:171],v[112:115],v[132:135],v[168:171]
buffer_load_dwordx4 a[80:83],v6,s[28:31],s89 offen offset:2048 sc1
v_mfma_f32_16x16x32_bf16 v[176:179],v[112:115],v[196:199],v[176:179]
v_mfma_f32_16x16x32_bf16 v[176:179],v[116:119],v[212:215],v[176:179]
v_mfma_f32_16x16x32_bf16 v[176:179],v[120:123],v[228:231],v[176:179]
v_mfma_f32_16x16x32_bf16 v[176:179],v[124:127],v[244:247],v[176:179]
v_mfma_f32_16x16x32_bf16 v[184:187],v[124:127],v[188:191],v[184:187]
buffer_load_dwordx4 a[88:91],v6,s[28:31],s90 offen offset:2048 sc1
v_mfma_f32_16x16x32_bf16 v[184:187],v[120:123],v[172:175],v[184:187]
ds_read_b64_tr_b16 v[120:121],v2 offset:20512
ds_read_b64_tr_b16 v[122:123],v2 offset:25632
v_mfma_f32_16x16x32_bf16 v[184:187],v[116:119],v[156:159],v[184:187]
ds_read_b64_tr_b16 v[116:117],v2 offset:10272
v_mfma_f32_16x16x32_bf16 v[184:187],v[112:115],v[140:143],v[184:187]
ds_read_b64_tr_b16 v[112:113],v2 offset:32
v_mfma_f32_16x16x32_bf16 a[192:195],v[64:67],a[128:131],a[192:195]
ds_read_b64_tr_b16 v[114:115],v2 offset:5152
v_mfma_f32_16x16x32_bf16 a[192:195],v[68:71],a[144:147],a[192:195]
buffer_load_dwordx4 a[120:123],v28,s[28:31],s90 offen offset:3072 sc1
v_mfma_f32_16x16x32_bf16 a[192:195],v[72:75],a[160:163],a[192:195]
ds_read_b64_tr_b16 v[118:119],v2 offset:15392
ds_read_b64_tr_b16 v[124:125],v2 offset:30752
v_mfma_f32_16x16x32_bf16 a[192:195],v[76:79],a[176:179],a[192:195]
ds_read_b64_tr_b16 v[126:127],v2 offset:35872
v_add_u32 v2,0xa000,v2
v_mfma_f32_16x16x32_bf16 a[196:199],v[76:79],a[180:183],a[196:199]
v_mfma_f32_16x16x32_bf16 a[196:199],v[72:75],a[164:167],a[196:199]
v_mfma_f32_16x16x32_bf16 a[196:199],v[68:71],a[148:151],a[196:199]
buffer_load_dwordx4 a[112:115],v28,s[28:31],s89 offen offset:3072 sc1
v_mfma_f32_16x16x32_bf16 a[196:199],v[64:67],a[132:135],a[196:199]
v_mfma_f32_16x16x32_bf16 a[200:203],v[64:67],a[136:139],a[200:203]
v_mfma_f32_16x16x32_bf16 a[200:203],v[68:71],a[152:155],a[200:203]
v_mfma_f32_16x16x32_bf16 a[200:203],v[72:75],a[168:171],a[200:203]
v_mfma_f32_16x16x32_bf16 a[200:203],v[76:79],a[184:187],a[200:203]
buffer_load_dwordx4 a[104:107],v28,s[28:31],s88 offen offset:3072 sc1
v_mfma_f32_16x16x32_bf16 a[204:207],v[76:79],a[188:191],a[204:207]
ds_read_b64_tr_b16 v[76:77],v1 offset:30784
ds_read_b64_tr_b16 v[78:79],v1 offset:35904
v_mfma_f32_16x16x32_bf16 a[204:207],v[72:75],a[172:175],a[204:207]
ds_read_b64_tr_b16 v[72:73],v1 offset:20544
v_mfma_f32_16x16x32_bf16 a[204:207],v[68:71],a[156:159],a[204:207]
ds_read_b64_tr_b16 v[68:69],v1 offset:10304
v_mfma_f32_16x16x32_bf16 a[204:207],v[64:67],a[140:143],a[204:207]
ds_read_b64_tr_b16 v[64:65],v1 offset:64
v_mfma_f32_16x16x32_bf16 a[208:211],v[80:83],a[128:131],a[208:211]
buffer_load_dwordx4 a[96:99],v28,s[28:31],s87 offen offset:3072 sc1
v_mfma_f32_16x16x32_bf16 a[208:211],v[84:87],a[144:147],a[208:211]
s_mul_i32 s87,s60,81920
ds_read_b64_tr_b16 v[66:67],v1 offset:5184
ds_read_b64_tr_b16 v[70:71],v1 offset:15424
v_mfma_f32_16x16x32_bf16 a[208:211],v[88:91],a[160:163],a[208:211]
s_add_u32 s87,s87,s66
ds_read_b64_tr_b16 v[74:75],v1 offset:25664
v_add_u32 v1,0xffff6000,v1
v_mfma_f32_16x16x32_bf16 a[208:211],v[92:95],a[176:179],a[208:211]
s_add_u32 s83,s83,s87
s_lshl_b32 s66,s63,6
s_add_u32 s87,s87,s66
v_mfma_f32_16x16x32_bf16 a[212:215],v[92:95],a[180:183],a[212:215]
s_add_u32 s88,s87,20480
s_add_u32 s89,s87,40960
s_add_u32 s90,s87,61440
v_mfma_f32_16x16x32_bf16 a[212:215],v[88:91],a[164:167],a[212:215]
s_add_u32 s66,s77,4096
v_mfma_f32_16x16x32_bf16 a[212:215],v[84:87],a[148:151],a[212:215]
v_mfma_f32_16x16x32_bf16 a[212:215],v[80:83],a[132:135],a[212:215]
v_mfma_f32_16x16x32_bf16 a[216:219],v[80:83],a[136:139],a[216:219]
v_mfma_f32_16x16x32_bf16 a[216:219],v[84:87],a[152:155],a[216:219]
v_mfma_f32_16x16x32_bf16 a[216:219],v[88:91],a[168:171],a[216:219]
v_mfma_f32_16x16x32_bf16 a[216:219],v[92:95],a[184:187],a[216:219]
v_mfma_f32_16x16x32_bf16 a[220:223],v[92:95],a[188:191],a[220:223]
ds_read_b64_tr_b16 v[92:93],v2 offset:30816
ds_read_b64_tr_b16 v[94:95],v2 offset:35936
v_mfma_f32_16x16x32_bf16 a[220:223],v[88:91],a[172:175],a[220:223]
ds_read_b64_tr_b16 v[88:89],v2 offset:20576
v_mfma_f32_16x16x32_bf16 a[220:223],v[84:87],a[156:159],a[220:223]
ds_read_b64_tr_b16 v[84:85],v2 offset:10336
v_mfma_f32_16x16x32_bf16 a[220:223],v[80:83],a[140:143],a[220:223]
s_waitcnt lgkmcnt(4)
ds_read_b64_tr_b16 v[80:81],v2 offset:96
v_mfma_f32_16x16x32_bf16 a[192:195],v[96:99],a[68:71],a[192:195]
s_waitcnt vmcnt(14)
ds_read_b64_tr_b16 v[82:83],v2 offset:5216
ds_read_b64_tr_b16 v[86:87],v2 offset:15456
v_mfma_f32_16x16x32_bf16 a[192:195],v[100:103],a[76:79],a[192:195]
s_waitcnt vmcnt(13)
ds_read_b64_tr_b16 v[90:91],v2 offset:25696
v_add_u32 v2,0xffff6000,v2
v_mfma_f32_16x16x32_bf16 a[192:195],v[104:107],a[84:87],a[192:195]
s_waitcnt vmcnt(12)
v_mfma_f32_16x16x32_bf16 a[192:195],v[108:111],a[92:95],a[192:195]
s_waitcnt vmcnt(11)
v_mfma_f32_16x16x32_bf16 a[196:199],v[108:111],a[124:127],a[196:199]
s_waitcnt vmcnt(10)
v_mfma_f32_16x16x32_bf16 a[196:199],v[104:107],a[116:119],a[196:199]
s_waitcnt vmcnt(9)
v_mfma_f32_16x16x32_bf16 a[196:199],v[100:103],a[108:111],a[196:199]
s_waitcnt vmcnt(8)
v_mfma_f32_16x16x32_bf16 a[196:199],v[96:99],a[100:103],a[196:199]
s_waitcnt vmcnt(7)
v_mfma_f32_16x16x32_bf16 a[200:203],v[96:99],a[64:67],a[200:203]
s_waitcnt vmcnt(6)
v_mfma_f32_16x16x32_bf16 a[200:203],v[100:103],a[72:75],a[200:203]
s_waitcnt vmcnt(5)
v_mfma_f32_16x16x32_bf16 a[200:203],v[104:107],a[80:83],a[200:203]
s_waitcnt vmcnt(4)
v_mfma_f32_16x16x32_bf16 a[200:203],v[108:111],a[88:91],a[200:203]
s_waitcnt vmcnt(3)
v_accvgpr_read_b32 v8,a192
)A"
R"A(v_accvgpr_read_b32 v9,a193
v_mfma_f32_16x16x32_bf16 a[204:207],v[108:111],a[120:123],a[204:207]
s_waitcnt vmcnt(2)
v_accvgpr_read_b32 v10,a194
v_accvgpr_read_b32 v11,a195
v_mfma_f32_16x16x32_bf16 a[204:207],v[104:107],a[112:115],a[204:207]
s_waitcnt vmcnt(1)
v_cvt_pk_bf16_f32 v8,v8,v9
v_cvt_pk_bf16_f32 v9,v10,v11
v_mfma_f32_16x16x32_bf16 a[204:207],v[100:103],a[104:107],a[204:207]
s_waitcnt vmcnt(0)
v_accvgpr_read_b32 v16,a196
v_accvgpr_read_b32 v17,a197
v_mfma_f32_16x16x32_bf16 a[204:207],v[96:99],a[96:99],a[204:207]
v_accvgpr_read_b32 v18,a198
v_accvgpr_read_b32 v19,a199
v_mfma_f32_16x16x32_bf16 a[208:211],v[112:115],a[68:71],a[208:211]
v_cvt_pk_bf16_f32 v16,v16,v17
v_cvt_pk_bf16_f32 v17,v18,v19
v_mfma_f32_16x16x32_bf16 a[208:211],v[116:119],a[76:79],a[208:211]
ds_read_b64_tr_b16 v[96:97],v1 offset:64
ds_read_b64_tr_b16 v[98:99],v1 offset:5184
v_mfma_f32_16x16x32_bf16 a[208:211],v[120:123],a[84:87],a[208:211]
ds_read_b64_tr_b16 v[100:101],v1 offset:10304
v_mfma_f32_16x16x32_bf16 a[208:211],v[124:127],a[92:95],a[208:211]
ds_read_b64_tr_b16 v[102:103],v1 offset:15424
v_mfma_f32_16x16x32_bf16 a[212:215],v[124:127],a[124:127],a[212:215]
ds_read_b64_tr_b16 v[104:105],v1 offset:20544
v_mfma_f32_16x16x32_bf16 a[212:215],v[120:123],a[116:119],a[212:215]
ds_read_b64_tr_b16 v[106:107],v1 offset:25664
ds_read_b64_tr_b16 v[108:109],v1 offset:30784
v_mfma_f32_16x16x32_bf16 a[212:215],v[116:119],a[108:111],a[212:215]
ds_read_b64_tr_b16 v[110:111],v1 offset:35904
v_add_u32 v1,0xa000,v1
v_mfma_f32_16x16x32_bf16 a[212:215],v[112:115],a[100:103],a[212:215]
v_accvgpr_read_b32 v12,a208
v_accvgpr_read_b32 v13,a209
v_mfma_f32_16x16x32_bf16 a[216:219],v[112:115],a[64:67],a[216:219]
v_accvgpr_read_b32 v14,a210
v_accvgpr_read_b32 v15,a211
v_mfma_f32_16x16x32_bf16 a[216:219],v[116:119],a[72:75],a[216:219]
v_cvt_pk_bf16_f32 v10,v12,v13
v_cvt_pk_bf16_f32 v11,v14,v15
v_mfma_f32_16x16x32_bf16 a[216:219],v[120:123],a[80:83],a[216:219]
buffer_store_dwordx4 v[8:11],v6,s[36:39],s74 offen sc1
v_mfma_f32_16x16x32_bf16 a[216:219],v[124:127],a[88:91],a[216:219]
v_accvgpr_read_b32 v20,a212
v_accvgpr_read_b32 v21,a213
v_mfma_f32_16x16x32_bf16 a[220:223],v[124:127],a[120:123],a[220:223]
v_accvgpr_read_b32 v22,a214
v_accvgpr_read_b32 v23,a215
v_mfma_f32_16x16x32_bf16 a[220:223],v[120:123],a[112:115],a[220:223]
v_cvt_pk_bf16_f32 v18,v20,v21
v_cvt_pk_bf16_f32 v19,v22,v23
v_mfma_f32_16x16x32_bf16 a[220:223],v[116:119],a[104:107],a[220:223]
v_accvgpr_read_b32 v8,a200
v_accvgpr_read_b32 v9,a201
v_mfma_f32_16x16x32_bf16 a[220:223],v[112:115],a[96:99],a[220:223]
v_accvgpr_read_b32 v10,a202
v_accvgpr_read_b32 v11,a203
v_mfma_f32_16x16x32_bf16 a[224:227],v[64:67],a[128:131],a[224:227]
v_accvgpr_read_b32 v12,a216
v_accvgpr_read_b32 v13,a217
v_mfma_f32_16x16x32_bf16 a[224:227],v[68:71],a[144:147],a[224:227]
v_accvgpr_read_b32 v14,a218
v_accvgpr_read_b32 v15,a219
v_mfma_f32_16x16x32_bf16 a[224:227],v[72:75],a[160:163],a[224:227]
v_cvt_pk_bf16_f32 v8,v8,v9
v_cvt_pk_bf16_f32 v9,v10,v11
v_mfma_f32_16x16x32_bf16 a[224:227],v[76:79],a[176:179],a[224:227]
v_cvt_pk_bf16_f32 v10,v12,v13
v_cvt_pk_bf16_f32 v11,v14,v15
v_mfma_f32_16x16x32_bf16 a[228:231],v[76:79],a[180:183],a[228:231]
ds_read_b64_tr_b16 v[112:113],v2 offset:96
ds_read_b64_tr_b16 v[114:115],v2 offset:5216
v_mfma_f32_16x16x32_bf16 a[228:231],v[72:75],a[164:167],a[228:231]
buffer_store_dwordx4 v[16:19],v6,s[36:39],s74 offen offset:1024 sc1
v_mfma_f32_16x16x32_bf16 a[228:231],v[68:71],a[148:151],a[228:231]
ds_read_b64_tr_b16 v[116:117],v2 offset:10336
ds_read_b64_tr_b16 v[118:119],v2 offset:15456
v_mfma_f32_16x16x32_bf16 a[228:231],v[64:67],a[132:135],a[228:231]
ds_read_b64_tr_b16 v[120:121],v2 offset:20576
v_accvgpr_read_b32 v16,a204
v_mfma_f32_16x16x32_bf16 a[232:235],v[64:67],a[136:139],a[232:235]
ds_read_b64_tr_b16 v[122:123],v2 offset:25696
v_accvgpr_read_b32 v17,a205
v_mfma_f32_16x16x32_bf16 a[232:235],v[68:71],a[152:155],a[232:235]
ds_read_b64_tr_b16 v[124:125],v2 offset:30816
v_accvgpr_read_b32 v18,a206
v_mfma_f32_16x16x32_bf16 a[232:235],v[72:75],a[168:171],a[232:235]
ds_read_b64_tr_b16 v[126:127],v2 offset:35936
v_accvgpr_read_b32 v19,a207
v_mfma_f32_16x16x32_bf16 a[232:235],v[76:79],a[184:187],a[232:235]
v_accvgpr_read_b32 v20,a220
v_accvgpr_read_b32 v21,a221
v_mfma_f32_16x16x32_bf16 a[236:239],v[76:79],a[188:191],a[236:239]
v_accvgpr_read_b32 v22,a222
v_accvgpr_read_b32 v23,a223
v_mfma_f32_16x16x32_bf16 a[236:239],v[72:75],a[172:175],a[236:239]
v_cvt_pk_bf16_f32 v16,v16,v17
v_cvt_pk_bf16_f32 v17,v18,v19
v_mfma_f32_16x16x32_bf16 a[236:239],v[68:71],a[156:159],a[236:239]
v_cvt_pk_bf16_f32 v18,v20,v21
v_cvt_pk_bf16_f32 v19,v22,v23
v_mfma_f32_16x16x32_bf16 a[236:239],v[64:67],a[140:143],a[236:239]
s_waitcnt lgkmcnt(3)
ds_read_b64_tr_b16 v[64:65],v1 offset:128
ds_read_b64_tr_b16 v[66:67],v1 offset:5248
v_mfma_f32_16x16x32_bf16 a[240:243],v[80:83],a[128:131],a[240:243]
buffer_store_dwordx4 v[8:11],v6,s[36:39],s74 offen offset:2048 sc1
v_mfma_f32_16x16x32_bf16 a[240:243],v[84:87],a[144:147],a[240:243]
ds_read_b64_tr_b16 v[68:69],v1 offset:10368
ds_read_b64_tr_b16 v[70:71],v1 offset:15488
v_mfma_f32_16x16x32_bf16 a[240:243],v[88:91],a[160:163],a[240:243]
ds_read_b64_tr_b16 v[72:73],v1 offset:20608
v_add_u32 v2,0xa000,v2
v_mfma_f32_16x16x32_bf16 a[240:243],v[92:95],a[176:179],a[240:243]
ds_read_b64_tr_b16 v[74:75],v1 offset:25728
v_mfma_f32_16x16x32_bf16 a[244:247],v[92:95],a[180:183],a[244:247]
ds_read_b64_tr_b16 v[76:77],v1 offset:30848
v_mfma_f32_16x16x32_bf16 a[244:247],v[88:91],a[164:167],a[244:247]
ds_read_b64_tr_b16 v[78:79],v1 offset:35968
v_add_u32 v1,0xffff6000,v1
v_mfma_f32_16x16x32_bf16 a[244:247],v[84:87],a[148:151],a[244:247]
v_mfma_f32_16x16x32_bf16 a[244:247],v[80:83],a[132:135],a[244:247]
v_mfma_f32_16x16x32_bf16 a[248:251],v[80:83],a[136:139],a[248:251]
v_mfma_f32_16x16x32_bf16 a[248:251],v[84:87],a[152:155],a[248:251]
v_mfma_f32_16x16x32_bf16 a[248:251],v[88:91],a[168:171],a[248:251]
v_mfma_f32_16x16x32_bf16 a[248:251],v[92:95],a[184:187],a[248:251]
buffer_store_dwordx4 v[16:19],v28,s[36:39],s74 offen offset:3072 sc1
v_mfma_f32_16x16x32_bf16 a[252:255],v[92:95],a[188:191],a[252:255]
ds_read_b64_tr_b16 v[92:93],v2 offset:30880
ds_read_b64_tr_b16 v[94:95],v2 offset:36000
v_mfma_f32_16x16x32_bf16 a[252:255],v[88:91],a[172:175],a[252:255]
ds_read_b64_tr_b16 v[88:89],v2 offset:20640
v_mfma_f32_16x16x32_bf16 a[252:255],v[84:87],a[156:159],a[252:255]
ds_read_b64_tr_b16 v[84:85],v2 offset:10400
v_mfma_f32_16x16x32_bf16 a[252:255],v[80:83],a[140:143],a[252:255]
ds_read_b64_tr_b16 v[80:81],v2 offset:160
v_mfma_f32_16x16x32_bf16 a[224:227],v[96:99],a[68:71],a[224:227]
ds_read_b64_tr_b16 v[82:83],v2 offset:5280
ds_read_b64_tr_b16 v[86:87],v2 offset:15520
v_mfma_f32_16x16x32_bf16 a[224:227],v[100:103],a[76:79],a[224:227]
ds_read_b64_tr_b16 v[90:91],v2 offset:25760
v_add_u32 v2,0xffff6000,v2
v_mfma_f32_16x16x32_bf16 a[224:227],v[104:107],a[84:87],a[224:227]
v_mfma_f32_16x16x32_bf16 a[224:227],v[108:111],a[92:95],a[224:227]
v_mfma_f32_16x16x32_bf16 a[228:231],v[108:111],a[124:127],a[228:231]
v_mfma_f32_16x16x32_bf16 a[228:231],v[104:107],a[116:119],a[228:231]
v_mfma_f32_16x16x32_bf16 a[228:231],v[100:103],a[108:111],a[228:231]
v_mfma_f32_16x16x32_bf16 a[228:231],v[96:99],a[100:103],a[228:231]
v_mfma_f32_16x16x32_bf16 a[232:235],v[96:99],a[64:67],a[232:235]
v_mfma_f32_16x16x32_bf16 a[232:235],v[100:103],a[72:75],a[232:235]
v_mfma_f32_16x16x32_bf16 a[232:235],v[104:107],a[80:83],a[232:235]
v_mfma_f32_16x16x32_bf16 a[232:235],v[108:111],a[88:91],a[232:235]
v_accvgpr_read_b32 v8,a224
v_accvgpr_read_b32 v9,a225
v_mfma_f32_16x16x32_bf16 a[236:239],v[108:111],a[120:123],a[236:239]
v_accvgpr_read_b32 v10,a226
v_accvgpr_read_b32 v11,a227
v_mfma_f32_16x16x32_bf16 a[236:239],v[104:107],a[112:115],a[236:239]
v_cvt_pk_bf16_f32 v8,v8,v9
v_cvt_pk_bf16_f32 v9,v10,v11
v_mfma_f32_16x16x32_bf16 a[236:239],v[100:103],a[104:107],a[236:239]
v_accvgpr_read_b32 v16,a228
v_accvgpr_read_b32 v17,a229
v_mfma_f32_16x16x32_bf16 a[236:239],v[96:99],a[96:99],a[236:239]
v_accvgpr_read_b32 v18,a230
v_accvgpr_read_b32 v19,a231
v_mfma_f32_16x16x32_bf16 a[240:243],v[112:115],a[68:71],a[240:243]
v_cvt_pk_bf16_f32 v16,v16,v17
v_cvt_pk_bf16_f32 v17,v18,v19
v_mfma_f32_16x16x32_bf16 a[240:243],v[116:119],a[76:79],a[240:243]
s_waitcnt lgkmcnt(0)
ds_read_b64_tr_b16 v[96:97],v1 offset:128
ds_read_b64_tr_b16 v[98:99],v1 offset:5248
v_mfma_f32_16x16x32_bf16 a[240:243],v[120:123],a[84:87],a[240:243]
ds_read_b64_tr_b16 v[100:101],v1 offset:10368
v_mfma_f32_16x16x32_bf16 a[240:243],v[124:127],a[92:95],a[240:243]
ds_read_b64_tr_b16 v[102:103],v1 offset:15488
v_mfma_f32_16x16x32_bf16 a[244:247],v[124:127],a[124:127],a[244:247]
ds_read_b64_tr_b16 v[104:105],v1 offset:20608
v_mfma_f32_16x16x32_bf16 a[244:247],v[120:123],a[116:119],a[244:247]
ds_read_b64_tr_b16 v[106:107],v1 offset:25728
ds_read_b64_tr_b16 v[108:109],v1 offset:30848
v_mfma_f32_16x16x32_bf16 a[244:247],v[116:119],a[108:111],a[244:247]
ds_read_b64_tr_b16 v[110:111],v1 offset:35968
v_add_u32 v1,0xa000,v1
v_mfma_f32_16x16x32_bf16 a[244:247],v[112:115],a[100:103],a[244:247]
v_accvgpr_read_b32 v12,a240
v_accvgpr_read_b32 v13,a241
v_mfma_f32_16x16x32_bf16 a[248:251],v[112:115],a[64:67],a[248:251]
v_accvgpr_read_b32 v14,a242
v_accvgpr_read_b32 v15,a243
v_mfma_f32_16x16x32_bf16 a[248:251],v[116:119],a[72:75],a[248:251]
v_cvt_pk_bf16_f32 v10,v12,v13
v_cvt_pk_bf16_f32 v11,v14,v15
v_mfma_f32_16x16x32_bf16 a[248:251],v[120:123],a[80:83],a[248:251]
buffer_store_dwordx4 v[8:11],v6,s[36:39],s75 offen sc1
v_mfma_f32_16x16x32_bf16 a[248:251],v[124:127],a[88:91],a[248:251]
v_accvgpr_read_b32 v20,a244
v_accvgpr_read_b32 v21,a245
v_mfma_f32_16x16x32_bf16 a[252:255],v[124:127],a[120:123],a[252:255]
v_accvgpr_read_b32 v22,a246
v_accvgpr_read_b32 v23,a247
v_mfma_f32_16x16x32_bf16 a[252:255],v[120:123],a[112:115],a[252:255]
v_cvt_pk_bf16_f32 v18,v20,v21
v_cvt_pk_bf16_f32 v19,v22,v23
v_mfma_f32_16x16x32_bf16 a[252:255],v[116:119],a[104:107],a[252:255]
v_accvgpr_read_b32 v8,a232
v_accvgpr_read_b32 v9,a233
v_mfma_f32_16x16x32_bf16 a[252:255],v[112:115],a[96:99],a[252:255]
v_accvgpr_read_b32 v10,a234
v_accvgpr_read_b32 v11,a235
v_mfma_f32_16x16x32_bf16 a[0:3],v[64:67],a[128:131],a[0:3]
v_accvgpr_read_b32 v12,a248
v_accvgpr_read_b32 v13,a249
v_mfma_f32_16x16x32_bf16 a[0:3],v[68:71],a[144:147],a[0:3]
v_accvgpr_read_b32 v14,a250
v_accvgpr_read_b32 v15,a251
v_mfma_f32_16x16x32_bf16 a[0:3],v[72:75],a[160:163],a[0:3]
v_cvt_pk_bf16_f32 v8,v8,v9
v_cvt_pk_bf16_f32 v9,v10,v11
v_mfma_f32_16x16x32_bf16 a[0:3],v[76:79],a[176:179],a[0:3]
v_cvt_pk_bf16_f32 v10,v12,v13
v_cvt_pk_bf16_f32 v11,v14,v15
v_mfma_f32_16x16x32_bf16 a[4:7],v[76:79],a[180:183],a[4:7]
ds_read_b64_tr_b16 v[112:113],v2 offset:160
ds_read_b64_tr_b16 v[114:115],v2 offset:5280
v_mfma_f32_16x16x32_bf16 a[4:7],v[72:75],a[164:167],a[4:7]
buffer_store_dwordx4 v[16:19],v6,s[36:39],s75 offen offset:1024 sc1
v_mfma_f32_16x16x32_bf16 a[4:7],v[68:71],a[148:151],a[4:7]
ds_read_b64_tr_b16 v[116:117],v2 offset:10400
ds_read_b64_tr_b16 v[118:119],v2 offset:15520
v_mfma_f32_16x16x32_bf16 a[4:7],v[64:67],a[132:135],a[4:7]
ds_read_b64_tr_b16 v[120:121],v2 offset:20640
v_accvgpr_read_b32 v16,a236
v_mfma_f32_16x16x32_bf16 a[8:11],v[64:67],a[136:139],a[8:11]
ds_read_b64_tr_b16 v[122:123],v2 offset:25760
v_accvgpr_read_b32 v17,a237
v_mfma_f32_16x16x32_bf16 a[8:11],v[68:71],a[152:155],a[8:11]
ds_read_b64_tr_b16 v[124:125],v2 offset:30880
v_accvgpr_read_b32 v18,a238
v_mfma_f32_16x16x32_bf16 a[8:11],v[72:75],a[168:171],a[8:11]
ds_read_b64_tr_b16 v[126:127],v2 offset:36000
v_accvgpr_read_b32 v19,a239
v_mfma_f32_16x16x32_bf16 a[8:11],v[76:79],a[184:187],a[8:11]
v_accvgpr_read_b32 v20,a252
v_accvgpr_read_b32 v21,a253
v_mfma_f32_16x16x32_bf16 a[12:15],v[76:79],a[188:191],a[12:15]
v_accvgpr_read_b32 v22,a254
v_accvgpr_read_b32 v23,a255
v_mfma_f32_16x16x32_bf16 a[12:15],v[72:75],a[172:175],a[12:15]
v_cvt_pk_bf16_f32 v16,v16,v17
v_cvt_pk_bf16_f32 v17,v18,v19
v_mfma_f32_16x16x32_bf16 a[12:15],v[68:71],a[156:159],a[12:15]
v_cvt_pk_bf16_f32 v18,v20,v21
v_cvt_pk_bf16_f32 v19,v22,v23
)A"
R"A(v_mfma_f32_16x16x32_bf16 a[12:15],v[64:67],a[140:143],a[12:15]
ds_read_b64_tr_b16 v[64:65],v1 offset:192
ds_read_b64_tr_b16 v[66:67],v1 offset:5312
v_mfma_f32_16x16x32_bf16 a[16:19],v[80:83],a[128:131],a[16:19]
buffer_store_dwordx4 v[8:11],v6,s[36:39],s75 offen offset:2048 sc1
v_mfma_f32_16x16x32_bf16 a[16:19],v[84:87],a[144:147],a[16:19]
ds_read_b64_tr_b16 v[68:69],v1 offset:10432
ds_read_b64_tr_b16 v[70:71],v1 offset:15552
v_mfma_f32_16x16x32_bf16 a[16:19],v[88:91],a[160:163],a[16:19]
ds_read_b64_tr_b16 v[72:73],v1 offset:20672
v_add_u32 v2,0xa000,v2
v_mfma_f32_16x16x32_bf16 a[16:19],v[92:95],a[176:179],a[16:19]
ds_read_b64_tr_b16 v[74:75],v1 offset:25792
v_mfma_f32_16x16x32_bf16 a[20:23],v[92:95],a[180:183],a[20:23]
ds_read_b64_tr_b16 v[76:77],v1 offset:30912
v_mfma_f32_16x16x32_bf16 a[20:23],v[88:91],a[164:167],a[20:23]
ds_read_b64_tr_b16 v[78:79],v1 offset:36032
v_add_u32 v1,0xffff6000,v1
v_mfma_f32_16x16x32_bf16 a[20:23],v[84:87],a[148:151],a[20:23]
v_mfma_f32_16x16x32_bf16 a[20:23],v[80:83],a[132:135],a[20:23]
v_mfma_f32_16x16x32_bf16 a[24:27],v[80:83],a[136:139],a[24:27]
v_mfma_f32_16x16x32_bf16 a[24:27],v[84:87],a[152:155],a[24:27]
v_mfma_f32_16x16x32_bf16 a[24:27],v[88:91],a[168:171],a[24:27]
v_mfma_f32_16x16x32_bf16 a[24:27],v[92:95],a[184:187],a[24:27]
buffer_store_dwordx4 v[16:19],v28,s[36:39],s75 offen offset:3072 sc1
v_mfma_f32_16x16x32_bf16 a[28:31],v[92:95],a[188:191],a[28:31]
ds_read_b64_tr_b16 v[92:93],v2 offset:30944
ds_read_b64_tr_b16 v[94:95],v2 offset:36064
v_mfma_f32_16x16x32_bf16 a[28:31],v[88:91],a[172:175],a[28:31]
ds_read_b64_tr_b16 v[88:89],v2 offset:20704
v_mfma_f32_16x16x32_bf16 a[28:31],v[84:87],a[156:159],a[28:31]
ds_read_b64_tr_b16 v[84:85],v2 offset:10464
v_mfma_f32_16x16x32_bf16 a[28:31],v[80:83],a[140:143],a[28:31]
s_waitcnt lgkmcnt(4)
ds_read_b64_tr_b16 v[80:81],v2 offset:224
v_mfma_f32_16x16x32_bf16 a[0:3],v[96:99],a[68:71],a[0:3]
ds_read_b64_tr_b16 v[82:83],v2 offset:5344
ds_read_b64_tr_b16 v[86:87],v2 offset:15584
v_mfma_f32_16x16x32_bf16 a[0:3],v[100:103],a[76:79],a[0:3]
ds_read_b64_tr_b16 v[90:91],v2 offset:25824
v_add_u32 v2,0xffff6000,v2
v_mfma_f32_16x16x32_bf16 a[0:3],v[104:107],a[84:87],a[0:3]
v_mfma_f32_16x16x32_bf16 a[0:3],v[108:111],a[92:95],a[0:3]
v_mfma_f32_16x16x32_bf16 a[4:7],v[108:111],a[124:127],a[4:7]
v_mfma_f32_16x16x32_bf16 a[4:7],v[104:107],a[116:119],a[4:7]
v_mfma_f32_16x16x32_bf16 a[4:7],v[100:103],a[108:111],a[4:7]
v_mfma_f32_16x16x32_bf16 a[4:7],v[96:99],a[100:103],a[4:7]
v_mfma_f32_16x16x32_bf16 a[8:11],v[96:99],a[64:67],a[8:11]
v_mfma_f32_16x16x32_bf16 a[8:11],v[100:103],a[72:75],a[8:11]
v_mfma_f32_16x16x32_bf16 a[8:11],v[104:107],a[80:83],a[8:11]
v_mfma_f32_16x16x32_bf16 a[8:11],v[108:111],a[88:91],a[8:11]
v_accvgpr_read_b32 v8,a0
v_accvgpr_read_b32 v9,a1
v_mfma_f32_16x16x32_bf16 a[12:15],v[108:111],a[120:123],a[12:15]
v_accvgpr_read_b32 v10,a2
v_accvgpr_read_b32 v11,a3
v_mfma_f32_16x16x32_bf16 a[12:15],v[104:107],a[112:115],a[12:15]
v_cvt_pk_bf16_f32 v8,v8,v9
v_cvt_pk_bf16_f32 v9,v10,v11
v_mfma_f32_16x16x32_bf16 a[12:15],v[100:103],a[104:107],a[12:15]
v_accvgpr_read_b32 v16,a4
v_accvgpr_read_b32 v17,a5
v_mfma_f32_16x16x32_bf16 a[12:15],v[96:99],a[96:99],a[12:15]
v_accvgpr_read_b32 v18,a6
v_accvgpr_read_b32 v19,a7
v_mfma_f32_16x16x32_bf16 a[16:19],v[112:115],a[68:71],a[16:19]
v_cvt_pk_bf16_f32 v16,v16,v17
v_cvt_pk_bf16_f32 v17,v18,v19
v_mfma_f32_16x16x32_bf16 a[16:19],v[116:119],a[76:79],a[16:19]
ds_read_b64_tr_b16 v[96:97],v1 offset:192
ds_read_b64_tr_b16 v[98:99],v1 offset:5312
v_mfma_f32_16x16x32_bf16 a[16:19],v[120:123],a[84:87],a[16:19]
ds_read_b64_tr_b16 v[100:101],v1 offset:10432
v_mfma_f32_16x16x32_bf16 a[16:19],v[124:127],a[92:95],a[16:19]
ds_read_b64_tr_b16 v[102:103],v1 offset:15552
v_mfma_f32_16x16x32_bf16 a[20:23],v[124:127],a[124:127],a[20:23]
ds_read_b64_tr_b16 v[104:105],v1 offset:20672
v_mfma_f32_16x16x32_bf16 a[20:23],v[120:123],a[116:119],a[20:23]
ds_read_b64_tr_b16 v[106:107],v1 offset:25792
ds_read_b64_tr_b16 v[108:109],v1 offset:30912
v_mfma_f32_16x16x32_bf16 a[20:23],v[116:119],a[108:111],a[20:23]
ds_read_b64_tr_b16 v[110:111],v1 offset:36032
v_add_u32 v1,0x14000,v1
v_mfma_f32_16x16x32_bf16 a[20:23],v[112:115],a[100:103],a[20:23]
v_accvgpr_read_b32 v12,a16
v_accvgpr_read_b32 v13,a17
v_mfma_f32_16x16x32_bf16 a[24:27],v[112:115],a[64:67],a[24:27]
v_accvgpr_read_b32 v14,a18
v_accvgpr_read_b32 v15,a19
v_mfma_f32_16x16x32_bf16 a[24:27],v[116:119],a[72:75],a[24:27]
v_cvt_pk_bf16_f32 v10,v12,v13
v_cvt_pk_bf16_f32 v11,v14,v15
s_nop 0
L_pd23_%=:
s_cbranch_execz L_trtg2S_%=
v_mfma_f32_16x16x32_bf16 a[24:27],v[120:123],a[80:83],a[24:27]
buffer_store_dwordx4 v[8:11],v6,s[36:39],s76 offen sc1
v_mfma_f32_16x16x32_bf16 a[24:27],v[124:127],a[88:91],a[24:27]
v_accvgpr_read_b32 v20,a20
v_accvgpr_read_b32 v21,a21
v_mfma_f32_16x16x32_bf16 a[28:31],v[124:127],a[120:123],a[28:31]
v_accvgpr_read_b32 v22,a22
v_accvgpr_read_b32 v23,a23
v_mfma_f32_16x16x32_bf16 a[28:31],v[120:123],a[112:115],a[28:31]
v_cvt_pk_bf16_f32 v18,v20,v21
v_cvt_pk_bf16_f32 v19,v22,v23
v_mfma_f32_16x16x32_bf16 a[28:31],v[116:119],a[104:107],a[28:31]
v_accvgpr_read_b32 v8,a8
v_accvgpr_read_b32 v9,a9
v_mfma_f32_16x16x32_bf16 a[28:31],v[112:115],a[96:99],a[28:31]
v_accvgpr_read_b32 v10,a10
v_accvgpr_read_b32 v11,a11
v_mfma_f32_16x16x32_bf16 v[128:131],v[64:67],a[128:131],v[128:131]
v_accvgpr_read_b32 v12,a24
v_accvgpr_read_b32 v13,a25
v_mfma_f32_16x16x32_bf16 v[128:131],v[68:71],a[144:147],v[128:131]
v_accvgpr_read_b32 v14,a26
v_accvgpr_read_b32 v15,a27
v_mfma_f32_16x16x32_bf16 v[128:131],v[72:75],a[160:163],v[128:131]
v_cvt_pk_bf16_f32 v8,v8,v9
v_cvt_pk_bf16_f32 v9,v10,v11
v_mfma_f32_16x16x32_bf16 v[128:131],v[76:79],a[176:179],v[128:131]
v_cvt_pk_bf16_f32 v10,v12,v13
v_cvt_pk_bf16_f32 v11,v14,v15
v_mfma_f32_16x16x32_bf16 v[136:139],v[76:79],a[180:183],v[136:139]
ds_read_b64_tr_b16 v[112:113],v2 offset:224
ds_read_b64_tr_b16 v[114:115],v2 offset:5344
v_mfma_f32_16x16x32_bf16 v[136:139],v[72:75],a[164:167],v[136:139]
buffer_store_dwordx4 v[16:19],v6,s[36:39],s76 offen offset:1024 sc1
v_mfma_f32_16x16x32_bf16 v[136:139],v[68:71],a[148:151],v[136:139]
ds_read_b64_tr_b16 v[116:117],v2 offset:10464
ds_read_b64_tr_b16 v[118:119],v2 offset:15584
v_mfma_f32_16x16x32_bf16 v[136:139],v[64:67],a[132:135],v[136:139]
ds_read_b64_tr_b16 v[120:121],v2 offset:20704
v_accvgpr_read_b32 v16,a12
v_mfma_f32_16x16x32_bf16 v[144:147],v[64:67],a[136:139],v[144:147]
ds_read_b64_tr_b16 v[122:123],v2 offset:25824
v_accvgpr_read_b32 v17,a13
v_mfma_f32_16x16x32_bf16 v[144:147],v[68:71],a[152:155],v[144:147]
ds_read_b64_tr_b16 v[124:125],v2 offset:30944
v_accvgpr_read_b32 v18,a14
v_mfma_f32_16x16x32_bf16 v[144:147],v[72:75],a[168:171],v[144:147]
ds_read_b64_tr_b16 v[126:127],v2 offset:36064
v_accvgpr_read_b32 v19,a15
v_mfma_f32_16x16x32_bf16 v[144:147],v[76:79],a[184:187],v[144:147]
v_accvgpr_read_b32 v20,a28
v_accvgpr_read_b32 v21,a29
v_mfma_f32_16x16x32_bf16 v[152:155],v[76:79],a[188:191],v[152:155]
v_accvgpr_read_b32 v22,a30
v_accvgpr_read_b32 v23,a31
v_mfma_f32_16x16x32_bf16 v[152:155],v[72:75],a[172:175],v[152:155]
v_cvt_pk_bf16_f32 v16,v16,v17
v_cvt_pk_bf16_f32 v17,v18,v19
v_mfma_f32_16x16x32_bf16 v[152:155],v[68:71],a[156:159],v[152:155]
v_cvt_pk_bf16_f32 v18,v20,v21
v_cvt_pk_bf16_f32 v19,v22,v23
v_mfma_f32_16x16x32_bf16 v[152:155],v[64:67],a[140:143],v[152:155]
s_waitcnt lgkmcnt(3)
ds_read_b64_tr_b16 v[64:65],v1 offset:256
ds_read_b64_tr_b16 v[66:67],v1 offset:5376
v_mfma_f32_16x16x32_bf16 v[160:163],v[80:83],a[128:131],v[160:163]
buffer_store_dwordx4 v[8:11],v6,s[36:39],s76 offen offset:2048 sc1
v_mfma_f32_16x16x32_bf16 v[160:163],v[84:87],a[144:147],v[160:163]
ds_read_b64_tr_b16 v[68:69],v1 offset:10496
ds_read_b64_tr_b16 v[70:71],v1 offset:15616
v_mfma_f32_16x16x32_bf16 v[160:163],v[88:91],a[160:163],v[160:163]
ds_read_b64_tr_b16 v[72:73],v1 offset:20736
v_add_u32 v2,0x14000,v2
v_mfma_f32_16x16x32_bf16 v[160:163],v[92:95],a[176:179],v[160:163]
ds_read_b64_tr_b16 v[74:75],v1 offset:25856
v_mov_b32 v20,s52
v_mfma_f32_16x16x32_bf16 v[168:171],v[92:95],a[180:183],v[168:171]
ds_read_b64_tr_b16 v[76:77],v1 offset:30976
v_mov_b32 v21,s53
v_mfma_f32_16x16x32_bf16 v[168:171],v[88:91],a[164:167],v[168:171]
ds_read_b64_tr_b16 v[78:79],v1 offset:36096
v_add_u32 v1,0xffff6000,v1
v_mfma_f32_16x16x32_bf16 v[168:171],v[84:87],a[148:151],v[168:171]
v_lshlrev_b32 v22,20,v6
v_mfma_f32_16x16x32_bf16 v[168:171],v[80:83],a[132:135],v[168:171]
v_mfma_f32_16x16x32_bf16 v[176:179],v[80:83],a[136:139],v[176:179]
v_mfma_f32_16x16x32_bf16 v[176:179],v[84:87],a[152:155],v[176:179]
v_mfma_f32_16x16x32_bf16 v[176:179],v[88:91],a[168:171],v[176:179]
v_mfma_f32_16x16x32_bf16 v[176:179],v[92:95],a[184:187],v[176:179]
buffer_store_dwordx4 v[16:19],v28,s[36:39],s76 offen offset:3072 sc1
v_mfma_f32_16x16x32_bf16 v[184:187],v[92:95],a[188:191],v[184:187]
ds_read_b64_tr_b16 v[92:93],v2 offset:31008
ds_read_b64_tr_b16 v[94:95],v2 offset:36128
v_mfma_f32_16x16x32_bf16 v[184:187],v[88:91],a[172:175],v[184:187]
ds_read_b64_tr_b16 v[88:89],v2 offset:20768
v_mfma_f32_16x16x32_bf16 v[184:187],v[84:87],a[156:159],v[184:187]
ds_read_b64_tr_b16 v[84:85],v2 offset:10528
v_mfma_f32_16x16x32_bf16 v[184:187],v[80:83],a[140:143],v[184:187]
ds_read_b64_tr_b16 v[80:81],v2 offset:288
v_mfma_f32_16x16x32_bf16 v[128:131],v[96:99],a[68:71],v[128:131]
ds_read_b64_tr_b16 v[82:83],v2 offset:5408
ds_read_b64_tr_b16 v[86:87],v2 offset:15648
v_mfma_f32_16x16x32_bf16 v[128:131],v[100:103],a[76:79],v[128:131]
ds_read_b64_tr_b16 v[90:91],v2 offset:25888
v_add_u32 v2,0xffff6000,v2
v_mfma_f32_16x16x32_bf16 v[128:131],v[104:107],a[84:87],v[128:131]
v_mfma_f32_16x16x32_bf16 v[128:131],v[108:111],a[92:95],v[128:131]
v_mfma_f32_16x16x32_bf16 v[136:139],v[108:111],a[124:127],v[136:139]
v_mfma_f32_16x16x32_bf16 v[136:139],v[104:107],a[116:119],v[136:139]
v_mfma_f32_16x16x32_bf16 v[136:139],v[100:103],a[108:111],v[136:139]
v_mfma_f32_16x16x32_bf16 v[136:139],v[96:99],a[100:103],v[136:139]
v_mfma_f32_16x16x32_bf16 v[144:147],v[96:99],a[64:67],v[144:147]
v_mfma_f32_16x16x32_bf16 v[144:147],v[100:103],a[72:75],v[144:147]
v_mfma_f32_16x16x32_bf16 v[144:147],v[104:107],a[80:83],v[144:147]
v_mfma_f32_16x16x32_bf16 v[144:147],v[108:111],a[88:91],v[144:147]
v_cvt_pk_bf16_f32 v128,v128,v129
v_cvt_pk_bf16_f32 v129,v130,v131
v_mfma_f32_16x16x32_bf16 v[152:155],v[108:111],a[120:123],v[152:155]
ds_read_b64_tr_b16 v[108:109],v1 offset:30976
ds_read_b64_tr_b16 v[110:111],v1 offset:36096
v_mfma_f32_16x16x32_bf16 v[152:155],v[104:107],a[112:115],v[152:155]
v_cvt_pk_bf16_f32 v136,v136,v137
v_cvt_pk_bf16_f32 v137,v138,v139
v_mfma_f32_16x16x32_bf16 v[152:155],v[100:103],a[104:107],v[152:155]
v_cvt_pk_bf16_f32 v144,v144,v145
v_cvt_pk_bf16_f32 v145,v146,v147
v_mfma_f32_16x16x32_bf16 v[152:155],v[96:99],a[96:99],v[152:155]
ds_read_b64_tr_b16 v[96:97],v1 offset:256
ds_read_b64_tr_b16 v[98:99],v1 offset:5376
v_mfma_f32_16x16x32_bf16 v[160:163],v[112:115],a[68:71],v[160:163]
ds_read_b64_tr_b16 v[100:101],v1 offset:10496
v_mfma_f32_16x16x32_bf16 v[160:163],v[116:119],a[76:79],v[160:163]
s_waitcnt lgkmcnt(5)
ds_read_b64_tr_b16 v[102:103],v1 offset:15616
v_mfma_f32_16x16x32_bf16 v[160:163],v[120:123],a[84:87],v[160:163]
ds_read_b64_tr_b16 v[104:105],v1 offset:20736
v_mfma_f32_16x16x32_bf16 v[160:163],v[124:127],a[92:95],v[160:163]
v_cvt_pk_bf16_f32 v152,v152,v153
v_cvt_pk_bf16_f32 v153,v154,v155
v_mfma_f32_16x16x32_bf16 v[168:171],v[124:127],a[124:127],v[168:171]
ds_read_b64_tr_b16 v[106:107],v1 offset:25856
v_add_u32 v1,0xffff6000,v1
v_mfma_f32_16x16x32_bf16 v[168:171],v[120:123],a[116:119],v[168:171]
v_mfma_f32_16x16x32_bf16 v[168:171],v[116:119],a[108:111],v[168:171]
v_mfma_f32_16x16x32_bf16 v[168:171],v[112:115],a[100:103],v[168:171]
v_cvt_pk_bf16_f32 v130,v160,v161
v_cvt_pk_bf16_f32 v131,v162,v163
v_mfma_f32_16x16x32_bf16 v[176:179],v[112:115],a[64:67],v[176:179]
buffer_store_dwordx4 v[128:131],v6,s[36:39],s79 offen sc1
v_mfma_f32_16x16x32_bf16 v[176:179],v[116:119],a[72:75],v[176:179]
v_mfma_f32_16x16x32_bf16 v[176:179],v[120:123],a[80:83],v[176:179]
v_mfma_f32_16x16x32_bf16 v[176:179],v[124:127],a[88:91],v[176:179]
)A"
R"A(v_mfma_f32_16x16x32_bf16 v[184:187],v[124:127],a[120:123],v[184:187]
v_cvt_pk_bf16_f32 v138,v168,v169
v_cvt_pk_bf16_f32 v139,v170,v171
v_mfma_f32_16x16x32_bf16 v[184:187],v[120:123],a[112:115],v[184:187]
buffer_store_dwordx4 v[136:139],v6,s[36:39],s80 offen sc1
v_mfma_f32_16x16x32_bf16 v[184:187],v[116:119],a[104:107],v[184:187]
ds_read_b64_tr_b16 v[116:117],v2 offset:10528
ds_read_b64_tr_b16 v[118:119],v2 offset:15648
v_mfma_f32_16x16x32_bf16 v[184:187],v[112:115],a[96:99],v[184:187]
v_cvt_pk_bf16_f32 v146,v176,v177
v_cvt_pk_bf16_f32 v147,v178,v179
s_waitcnt vmcnt(6)
s_barrier
v_mfma_f32_16x16x32_bf16 v[192:195],v[64:67],v[32:35],v[192:195]
buffer_store_dwordx4 v[144:147],v6,s[36:39],s81 offen sc1
v_mfma_f32_16x16x32_bf16 v[192:195],v[68:71],v[36:39],v[192:195]
ds_read_b64_tr_b16 v[112:113],v2 offset:288
ds_read_b64_tr_b16 v[114:115],v2 offset:5408
v_mfma_f32_16x16x32_bf16 v[192:195],v[72:75],v[40:43],v[192:195]
buffer_store_dwordx2 v[58:59],v60,s[44:47],s18 offen offset:2312 sc1
v_mfma_f32_16x16x32_bf16 v[192:195],v[76:79],v[44:47],v[192:195]
v_cvt_pk_bf16_f32 v154,v184,v185
v_cvt_pk_bf16_f32 v155,v186,v187
v_mfma_f32_16x16x32_bf16 v[200:203],v[76:79],v[180:183],v[200:203]
buffer_store_dwordx4 v[152:155],v28,s[36:39],s82 offen sc1
v_mfma_f32_16x16x32_bf16 v[200:203],v[72:75],v[164:167],v[200:203]
ds_read_b64_tr_b16 v[120:121],v2 offset:20768
ds_read_b64_tr_b16 v[122:123],v2 offset:25888
v_mfma_f32_16x16x32_bf16 v[200:203],v[68:71],v[148:151],v[200:203]
ds_read_b64_tr_b16 v[124:125],v2 offset:31008
v_mfma_f32_16x16x32_bf16 v[200:203],v[64:67],v[132:135],v[200:203]
ds_read_b64_tr_b16 v[126:127],v2 offset:36128
v_add_u32 v2,0xffff6000,v2
v_mfma_f32_16x16x32_bf16 v[208:211],v[64:67],v[196:199],v[208:211]
v_mfma_f32_16x16x32_bf16 v[208:211],v[68:71],v[212:215],v[208:211]
v_mfma_f32_16x16x32_bf16 v[208:211],v[72:75],v[228:231],v[208:211]
v_mfma_f32_16x16x32_bf16 v[208:211],v[76:79],v[244:247],v[208:211]
v_mfma_f32_16x16x32_bf16 v[216:219],v[76:79],v[188:191],v[216:219]
ds_read_b64_tr_b16 v[76:77],v1 offset:30976
ds_read_b64_tr_b16 v[78:79],v1 offset:36096
v_mfma_f32_16x16x32_bf16 v[216:219],v[72:75],v[172:175],v[216:219]
ds_read_b64_tr_b16 v[72:73],v1 offset:20736
v_mfma_f32_16x16x32_bf16 v[216:219],v[68:71],v[156:159],v[216:219]
ds_read_b64_tr_b16 v[68:69],v1 offset:10496
v_mfma_f32_16x16x32_bf16 v[216:219],v[64:67],v[140:143],v[216:219]
ds_read_b64_tr_b16 v[64:65],v1 offset:256
v_mfma_f32_16x16x32_bf16 v[224:227],v[80:83],v[32:35],v[224:227]
ds_read_b64_tr_b16 v[66:67],v1 offset:5376
ds_read_b64_tr_b16 v[70:71],v1 offset:15616
v_mfma_f32_16x16x32_bf16 v[224:227],v[84:87],v[36:39],v[224:227]
ds_read_b64_tr_b16 v[74:75],v1 offset:25856
v_mfma_f32_16x16x32_bf16 v[224:227],v[88:91],v[40:43],v[224:227]
v_mfma_f32_16x16x32_bf16 v[224:227],v[92:95],v[44:47],v[224:227]
v_mfma_f32_16x16x32_bf16 v[232:235],v[92:95],v[180:183],v[232:235]
v_mfma_f32_16x16x32_bf16 v[232:235],v[88:91],v[164:167],v[232:235]
v_mfma_f32_16x16x32_bf16 v[232:235],v[84:87],v[148:151],v[232:235]
v_mfma_f32_16x16x32_bf16 v[232:235],v[80:83],v[132:135],v[232:235]
v_mfma_f32_16x16x32_bf16 v[240:243],v[80:83],v[196:199],v[240:243]
v_mfma_f32_16x16x32_bf16 v[240:243],v[84:87],v[212:215],v[240:243]
v_mfma_f32_16x16x32_bf16 v[240:243],v[88:91],v[228:231],v[240:243]
v_mfma_f32_16x16x32_bf16 v[240:243],v[92:95],v[244:247],v[240:243]
v_mfma_f32_16x16x32_bf16 v[248:251],v[92:95],v[188:191],v[248:251]
ds_read_b64_tr_b16 v[92:93],v2 offset:31008
ds_read_b64_tr_b16 v[94:95],v2 offset:36128
v_mfma_f32_16x16x32_bf16 v[248:251],v[88:91],v[172:175],v[248:251]
ds_read_b64_tr_b16 v[88:89],v2 offset:20768
v_mfma_f32_16x16x32_bf16 v[248:251],v[84:87],v[156:159],v[248:251]
ds_read_b64_tr_b16 v[84:85],v2 offset:10528
v_mfma_f32_16x16x32_bf16 v[248:251],v[80:83],v[140:143],v[248:251]
s_waitcnt lgkmcnt(4)
ds_read_b64_tr_b16 v[80:81],v2 offset:288
v_mfma_f32_16x16x32_bf16 v[192:195],v[96:99],a[128:131],v[192:195]
ds_read_b64_tr_b16 v[82:83],v2 offset:5408
ds_read_b64_tr_b16 v[86:87],v2 offset:15648
v_mfma_f32_16x16x32_bf16 v[192:195],v[100:103],a[144:147],v[192:195]
ds_read_b64_tr_b16 v[90:91],v2 offset:25888
v_mfma_f32_16x16x32_bf16 v[192:195],v[104:107],a[160:163],v[192:195]
v_mfma_f32_16x16x32_bf16 v[192:195],v[108:111],a[176:179],v[192:195]
v_mfma_f32_16x16x32_bf16 v[200:203],v[108:111],a[180:183],v[200:203]
v_mfma_f32_16x16x32_bf16 v[200:203],v[104:107],a[164:167],v[200:203]
v_mfma_f32_16x16x32_bf16 v[200:203],v[100:103],a[148:151],v[200:203]
v_mfma_f32_16x16x32_bf16 v[200:203],v[96:99],a[132:135],v[200:203]
v_mfma_f32_16x16x32_bf16 v[208:211],v[96:99],a[136:139],v[208:211]
v_mfma_f32_16x16x32_bf16 v[208:211],v[100:103],a[152:155],v[208:211]
v_mfma_f32_16x16x32_bf16 v[208:211],v[104:107],a[168:171],v[208:211]
v_mfma_f32_16x16x32_bf16 v[208:211],v[108:111],a[184:187],v[208:211]
v_mfma_f32_16x16x32_bf16 v[216:219],v[108:111],a[188:191],v[216:219]
v_mfma_f32_16x16x32_bf16 v[216:219],v[104:107],a[172:175],v[216:219]
v_mfma_f32_16x16x32_bf16 v[216:219],v[100:103],a[156:159],v[216:219]
v_mfma_f32_16x16x32_bf16 v[216:219],v[96:99],a[140:143],v[216:219]
v_mfma_f32_16x16x32_bf16 v[224:227],v[112:115],a[128:131],v[224:227]
v_mfma_f32_16x16x32_bf16 v[224:227],v[116:119],a[144:147],v[224:227]
v_mfma_f32_16x16x32_bf16 v[224:227],v[120:123],a[160:163],v[224:227]
v_mfma_f32_16x16x32_bf16 v[224:227],v[124:127],a[176:179],v[224:227]
v_mfma_f32_16x16x32_bf16 v[232:235],v[124:127],a[180:183],v[232:235]
v_mfma_f32_16x16x32_bf16 v[232:235],v[120:123],a[164:167],v[232:235]
v_mfma_f32_16x16x32_bf16 v[232:235],v[116:119],a[148:151],v[232:235]
v_mfma_f32_16x16x32_bf16 v[232:235],v[112:115],a[132:135],v[232:235]
v_mfma_f32_16x16x32_bf16 v[240:243],v[112:115],a[136:139],v[240:243]
v_mfma_f32_16x16x32_bf16 v[240:243],v[116:119],a[152:155],v[240:243]
v_mfma_f32_16x16x32_bf16 v[240:243],v[120:123],a[168:171],v[240:243]
v_mfma_f32_16x16x32_bf16 v[240:243],v[124:127],a[184:187],v[240:243]
v_mfma_f32_16x16x32_bf16 v[248:251],v[124:127],a[188:191],v[248:251]
v_mfma_f32_16x16x32_bf16 v[248:251],v[120:123],a[172:175],v[248:251]
v_mfma_f32_16x16x32_bf16 v[248:251],v[116:119],a[156:159],v[248:251]
v_mfma_f32_16x16x32_bf16 v[248:251],v[112:115],a[140:143],v[248:251]
s_waitcnt vmcnt(0)
s_barrier
v_mfma_f32_16x16x32_bf16 v[192:195],v[64:67],a[68:71],v[192:195]
buffer_store_dwordx2 v[58:59],v60,s[44:47],s18 offen offset:2304 sc1
v_mfma_f32_16x16x32_bf16 v[192:195],v[68:71],a[76:79],v[192:195]
v_mfma_f32_16x16x32_bf16 v[192:195],v[72:75],a[84:87],v[192:195]
v_mfma_f32_16x16x32_bf16 v[192:195],v[76:79],a[92:95],v[192:195]
v_mfma_f32_16x16x32_bf16 v[200:203],v[76:79],a[124:127],v[200:203]
v_mfma_f32_16x16x32_bf16 v[200:203],v[72:75],a[116:119],v[200:203]
v_mfma_f32_16x16x32_bf16 v[200:203],v[68:71],a[108:111],v[200:203]
v_mfma_f32_16x16x32_bf16 v[200:203],v[64:67],a[100:103],v[200:203]
v_mfma_f32_16x16x32_bf16 v[208:211],v[64:67],a[64:67],v[208:211]
v_mfma_f32_16x16x32_bf16 v[208:211],v[68:71],a[72:75],v[208:211]
v_mfma_f32_16x16x32_bf16 v[208:211],v[72:75],a[80:83],v[208:211]
v_mfma_f32_16x16x32_bf16 v[208:211],v[76:79],a[88:91],v[208:211]
v_mfma_f32_16x16x32_bf16 v[216:219],v[76:79],a[120:123],v[216:219]
v_mfma_f32_16x16x32_bf16 v[216:219],v[72:75],a[112:115],v[216:219]
v_mfma_f32_16x16x32_bf16 v[216:219],v[68:71],a[104:107],v[216:219]
v_mfma_f32_16x16x32_bf16 v[216:219],v[64:67],a[96:99],v[216:219]
s_waitcnt lgkmcnt(0)
v_mfma_f32_16x16x32_bf16 v[224:227],v[80:83],a[68:71],v[224:227]
buffer_load_dwordx2 v[62:63],v61,s[44:47],s91 offen offset:2312 sc1
v_mfma_f32_16x16x32_bf16 v[224:227],v[84:87],a[76:79],v[224:227]
v_mfma_f32_16x16x32_bf16 v[224:227],v[88:91],a[84:87],v[224:227]
v_mfma_f32_16x16x32_bf16 v[224:227],v[92:95],a[92:95],v[224:227]
v_mfma_f32_16x16x32_bf16 v[232:235],v[92:95],a[124:127],v[232:235]
v_mfma_f32_16x16x32_bf16 v[232:235],v[88:91],a[116:119],v[232:235]
v_mfma_f32_16x16x32_bf16 v[232:235],v[84:87],a[108:111],v[232:235]
v_mfma_f32_16x16x32_bf16 v[232:235],v[80:83],a[100:103],v[232:235]
v_mfma_f32_16x16x32_bf16 v[240:243],v[80:83],a[64:67],v[240:243]
v_mfma_f32_16x16x32_bf16 v[240:243],v[84:87],a[72:75],v[240:243]
v_mfma_f32_16x16x32_bf16 v[240:243],v[88:91],a[80:83],v[240:243]
v_mfma_f32_16x16x32_bf16 v[240:243],v[92:95],a[88:91],v[240:243]
v_mfma_f32_16x16x32_bf16 v[248:251],v[92:95],a[120:123],v[248:251]
v_mfma_f32_16x16x32_bf16 v[248:251],v[88:91],a[112:115],v[248:251]
v_mfma_f32_16x16x32_bf16 v[248:251],v[84:87],a[104:107],v[248:251]
v_mfma_f32_16x16x32_bf16 v[248:251],v[80:83],a[96:99],v[248:251]
s_waitcnt vmcnt(0)
v_cmp_ne_u32 s[56:57],s52,v62
v_cmp_ne_u32 s[58:59],s53,v63
s_nop 0
s_or_b64 s[56:57],s[56:57],s[58:59]
s_cbranch_scc1 L_fr8_%=
L_fo8_%=:
buffer_load_dwordx4 v[64:67],v6,s[36:39],s66 offen sc1
buffer_load_dwordx4 v[68:71],v6,s[36:39],s67 offen sc1
buffer_load_dwordx4 v[72:75],v6,s[36:39],s66 offen offset:1024 sc1
buffer_load_dwordx4 v[76:79],v6,s[36:39],s67 offen offset:1024 sc1
buffer_load_dwordx4 v[80:83],v6,s[36:39],s66 offen offset:2048 sc1
buffer_load_dwordx4 v[84:87],v6,s[36:39],s67 offen offset:2048 sc1
buffer_load_dwordx4 v[88:91],v28,s[36:39],s66 offen offset:3072 sc1
buffer_load_dwordx4 v[92:95],v28,s[36:39],s67 offen offset:3072 sc1
buffer_load_dwordx2 v[4:5],v3,s[44:47],s91 offen offset:2304 sc1
s_waitcnt vmcnt(8)
v_mfma_f32_16x16x32_bf16 v[192:195],v[48:51],v[64:67],v[192:195]
s_waitcnt vmcnt(7)
v_mfma_f32_16x16x32_bf16 v[192:195],v[48:51],v[68:71],v[192:195]
v_mfma_f32_16x16x32_bf16 v[224:227],v[52:55],v[64:67],v[224:227]
v_mfma_f32_16x16x32_bf16 v[224:227],v[52:55],v[68:71],v[224:227]
s_waitcnt vmcnt(6)
v_mfma_f32_16x16x32_bf16 v[200:203],v[48:51],v[72:75],v[200:203]
s_waitcnt vmcnt(5)
v_mfma_f32_16x16x32_bf16 v[200:203],v[48:51],v[76:79],v[200:203]
v_mfma_f32_16x16x32_bf16 v[232:235],v[52:55],v[72:75],v[232:235]
v_mfma_f32_16x16x32_bf16 v[232:235],v[52:55],v[76:79],v[232:235]
s_waitcnt vmcnt(4)
v_mfma_f32_16x16x32_bf16 v[208:211],v[48:51],v[80:83],v[208:211]
s_waitcnt vmcnt(3)
v_mfma_f32_16x16x32_bf16 v[208:211],v[48:51],v[84:87],v[208:211]
v_mfma_f32_16x16x32_bf16 v[240:243],v[52:55],v[80:83],v[240:243]
v_mfma_f32_16x16x32_bf16 v[240:243],v[52:55],v[84:87],v[240:243]
s_waitcnt vmcnt(2)
v_mfma_f32_16x16x32_bf16 v[216:219],v[48:51],v[88:91],v[216:219]
s_waitcnt vmcnt(1)
v_mfma_f32_16x16x32_bf16 v[216:219],v[48:51],v[92:95],v[216:219]
v_mfma_f32_16x16x32_bf16 v[248:251],v[52:55],v[88:91],v[248:251]
v_mfma_f32_16x16x32_bf16 v[248:251],v[52:55],v[92:95],v[248:251]
s_waitcnt vmcnt(0)
v_cmp_ne_u32 s[56:57],s52,v4
v_cmp_ne_u32 s[58:59],s53,v5
s_nop 0
s_or_b64 s[56:57],s[56:57],s[58:59]
s_cbranch_scc1 L_fr9_%=
L_fo9_%=:
buffer_load_dwordx4 v[96:99],v6,s[36:39],s68 offen sc1
buffer_load_dwordx4 v[100:103],v6,s[36:39],s68 offen offset:1024 sc1
buffer_load_dwordx4 v[104:107],v6,s[36:39],s68 offen offset:2048 sc1
buffer_load_dwordx4 v[108:111],v6,s[36:39],s68 offen offset:3072 sc1
buffer_load_dwordx4 v[112:115],v6,s[36:39],s65 offen sc1
buffer_load_dwordx4 v[116:119],v6,s[36:39],s65 offen offset:1024 sc1
buffer_load_dwordx4 v[120:123],v6,s[36:39],s65 offen offset:2048 sc1
buffer_load_dwordx4 v[124:127],v28,s[36:39],s65 offen offset:3072 sc1
s_waitcnt vmcnt(7)
v_mfma_f32_16x16x32_bf16 v[128:131],v[48:51],v[96:99],0
s_waitcnt vmcnt(6)
v_mfma_f32_16x16x32_bf16 v[128:131],v[48:51],v[100:103],v[128:131]
s_waitcnt vmcnt(5)
v_mfma_f32_16x16x32_bf16 v[128:131],v[48:51],v[104:107],v[128:131]
s_waitcnt vmcnt(4)
v_mfma_f32_16x16x32_bf16 v[128:131],v[48:51],v[108:111],v[128:131]
v_mfma_f32_16x16x32_bf16 v[160:163],v[52:55],v[96:99],0
v_mfma_f32_16x16x32_bf16 v[160:163],v[52:55],v[100:103],v[160:163]
v_mfma_f32_16x16x32_bf16 v[160:163],v[52:55],v[104:107],v[160:163]
v_mfma_f32_16x16x32_bf16 v[160:163],v[52:55],v[108:111],v[160:163]
s_waitcnt vmcnt(3)
v_mfma_f32_16x16x32_bf16 v[192:195],v[48:51],v[112:115],v[192:195]
v_mfma_f32_16x16x32_bf16 v[224:227],v[52:55],v[112:115],v[224:227]
s_waitcnt vmcnt(2)
v_mfma_f32_16x16x32_bf16 v[200:203],v[48:51],v[116:119],v[200:203]
v_mfma_f32_16x16x32_bf16 v[232:235],v[52:55],v[116:119],v[232:235]
s_waitcnt vmcnt(1)
)A"
R"A(v_cvt_pk_bf16_f32 v128,v128,v129
v_cvt_pk_bf16_f32 v129,v130,v131
v_mfma_f32_16x16x32_bf16 v[208:211],v[48:51],v[120:123],v[208:211]
v_mfma_f32_16x16x32_bf16 v[240:243],v[52:55],v[120:123],v[240:243]
s_waitcnt vmcnt(0)
v_cvt_pk_bf16_f32 v130,v160,v161
v_cvt_pk_bf16_f32 v131,v162,v163
v_mfma_f32_16x16x32_bf16 v[216:219],v[48:51],v[124:127],v[216:219]
v_cvt_pk_bf16_f32 v192,v192,v193
v_cvt_pk_bf16_f32 v193,v194,v195
v_cvt_pk_bf16_f32 v194,v224,v225
v_cvt_pk_bf16_f32 v195,v226,v227
v_cvt_pk_bf16_f32 v200,v200,v201
v_cvt_pk_bf16_f32 v201,v202,v203
v_mfma_f32_16x16x32_bf16 v[248:251],v[52:55],v[124:127],v[248:251]
v_permlane16_swap_b32 v128,v130
v_permlane16_swap_b32 v129,v131
v_cvt_pk_bf16_f32 v202,v232,v233
v_cvt_pk_bf16_f32 v203,v234,v235
v_cvt_pk_bf16_f32 v208,v208,v209
v_cvt_pk_bf16_f32 v209,v210,v211
v_cvt_pk_bf16_f32 v210,v240,v241
v_cvt_pk_bf16_f32 v211,v242,v243
buffer_store_dwordx4 v[128:131],v56,s[24:27],s83 offen offset:256 sc1 nt
v_permlane16_swap_b32 v192,v194
v_permlane16_swap_b32 v193,v195
buffer_store_dwordx4 v[192:195],v56,s[24:27],s87 offen sc1 nt
v_permlane16_swap_b32 v200,v202
v_permlane16_swap_b32 v201,v203
v_cvt_pk_bf16_f32 v216,v216,v217
v_cvt_pk_bf16_f32 v217,v218,v219
v_cvt_pk_bf16_f32 v218,v248,v249
v_cvt_pk_bf16_f32 v219,v250,v251
buffer_store_dwordx4 v[200:203],v56,s[24:27],s88 offen sc1 nt
v_permlane16_swap_b32 v208,v210
v_permlane16_swap_b32 v209,v211
buffer_store_dwordx4 v[208:211],v56,s[24:27],s89 offen sc1 nt
v_permlane16_swap_b32 v216,v218
v_permlane16_swap_b32 v217,v219
buffer_store_dwordx4 v[216:219],v56,s[24:27],s90 offen sc1 nt
s_lshl_b32 s69,s62,20
s_add_u32 s69,s69,s18
buffer_store_dword v20,v22,s[44:47],s69 offen sc1
L_geS_%=:
s_branch L_end_%=
L_hot_%=:
L_fr1_%=:
s_mov_b32 s19,65536
L_fl1_%=:
buffer_load_dwordx2 v[4:5],v3,s[44:47],s91 offen offset:256 sc1
s_waitcnt vmcnt(0)
s_sub_u32 s19,s19,1
s_cmp_eq_u32 s19,0
s_cbranch_scc1 L_fo1_%=
v_cmp_ne_u32 s[56:57],s52,v4
v_cmp_ne_u32 s[58:59],s53,v5
s_nop 1
s_or_b64 s[56:57],s[56:57],s[58:59]
s_cbranch_scc1 L_fl1_%=
s_branch L_fo1_%=
L_fr2_%=:
s_mov_b32 s19,65536
L_fl2_%=:
buffer_load_dwordx2 v[4:5],v3,s[44:47],s91 offen offset:1280 sc1
s_waitcnt vmcnt(0)
s_sub_u32 s19,s19,1
s_cmp_eq_u32 s19,0
s_cbranch_scc1 L_fo2_%=
v_cmp_ne_u32 s[56:57],s52,v4
v_cmp_ne_u32 s[58:59],s53,v5
s_nop 1
s_or_b64 s[56:57],s[56:57],s[58:59]
s_cbranch_scc1 L_fl2_%=
s_branch L_fo2_%=
L_fr3_%=:
s_mov_b32 s19,65536
L_fl3_%=:
buffer_load_dwordx2 v[4:5],v3,s[44:47],s91 offen offset:1280 sc1
s_waitcnt vmcnt(0)
s_sub_u32 s19,s19,1
s_cmp_eq_u32 s19,0
s_cbranch_scc1 L_fo3_%=
v_cmp_ne_u32 s[56:57],s52,v4
v_cmp_ne_u32 s[58:59],s53,v5
s_nop 1
s_or_b64 s[56:57],s[56:57],s[58:59]
s_cbranch_scc1 L_fl3_%=
s_branch L_fo3_%=
L_fr4_%=:
s_mov_b32 s19,65536
L_fl4_%=:
buffer_load_dwordx2 v[62:63],v61,s[44:47],s91 offen offset:2312 sc1
s_waitcnt vmcnt(0)
s_sub_u32 s19,s19,1
s_cmp_eq_u32 s19,0
s_cbranch_scc1 L_fo4_%=
v_cmp_ne_u32 s[56:57],s52,v62
v_cmp_ne_u32 s[58:59],s53,v63
s_nop 1
s_or_b64 s[56:57],s[56:57],s[58:59]
s_cbranch_scc1 L_fl4_%=
s_branch L_fo4_%=
L_fr5_%=:
s_mov_b32 s19,65536
L_fl5_%=:
buffer_load_dwordx2 v[4:5],v3,s[44:47],s91 offen offset:2304 sc1
s_waitcnt vmcnt(0)
s_sub_u32 s19,s19,1
s_cmp_eq_u32 s19,0
s_cbranch_scc1 L_fo5_%=
v_cmp_ne_u32 s[56:57],s52,v4
v_cmp_ne_u32 s[58:59],s53,v5
s_nop 1
s_or_b64 s[56:57],s[56:57],s[58:59]
s_cbranch_scc1 L_fl5_%=
s_branch L_fo5_%=
L_fr6_%=:
s_mov_b32 s19,65536
L_fl6_%=:
buffer_load_dwordx2 v[4:5],v3,s[44:47],s91 offen offset:256 sc1
s_waitcnt vmcnt(0)
s_sub_u32 s19,s19,1
s_cmp_eq_u32 s19,0
s_cbranch_scc1 L_fo6_%=
v_cmp_ne_u32 s[56:57],s52,v4
v_cmp_ne_u32 s[58:59],s53,v5
s_nop 1
s_or_b64 s[56:57],s[56:57],s[58:59]
s_cbranch_scc1 L_fl6_%=
s_branch L_fo6_%=
L_fr7_%=:
s_mov_b32 s19,65536
L_fl7_%=:
buffer_load_dwordx2 v[4:5],v3,s[44:47],s91 offen offset:1280 sc1
s_waitcnt vmcnt(0)
s_sub_u32 s19,s19,1
s_cmp_eq_u32 s19,0
s_cbranch_scc1 L_fo7_%=
v_cmp_ne_u32 s[56:57],s52,v4
v_cmp_ne_u32 s[58:59],s53,v5
s_nop 1
s_or_b64 s[56:57],s[56:57],s[58:59]
s_cbranch_scc1 L_fl7_%=
s_branch L_fo7_%=
L_fr8_%=:
s_mov_b32 s19,65536
L_fl8_%=:
buffer_load_dwordx2 v[62:63],v61,s[44:47],s91 offen offset:2312 sc1
s_waitcnt vmcnt(0)
s_sub_u32 s19,s19,1
s_cmp_eq_u32 s19,0
s_cbranch_scc1 L_fo8_%=
v_cmp_ne_u32 s[56:57],s52,v62
v_cmp_ne_u32 s[58:59],s53,v63
s_nop 1
s_or_b64 s[56:57],s[56:57],s[58:59]
s_cbranch_scc1 L_fl8_%=
s_branch L_fo8_%=
L_fr9_%=:
s_mov_b32 s19,65536
L_fl9_%=:
buffer_load_dwordx2 v[4:5],v3,s[44:47],s91 offen offset:2304 sc1
s_waitcnt vmcnt(0)
s_sub_u32 s19,s19,1
s_cmp_eq_u32 s19,0
s_cbranch_scc1 L_fo9_%=
v_cmp_ne_u32 s[56:57],s52,v4
v_cmp_ne_u32 s[58:59],s53,v5
s_nop 1
s_or_b64 s[56:57],s[56:57],s[58:59]
s_cbranch_scc1 L_fl9_%=
s_branch L_fo9_%=
L_slow0_%=:
s_waitcnt vmcnt(0) lgkmcnt(0)
s_nop 7
v_accvgpr_write_b32 a160,v32
v_accvgpr_write_b32 a161,v33
v_accvgpr_write_b32 a162,v34
v_accvgpr_write_b32 a163,v35
v_accvgpr_write_b32 a164,v36
v_accvgpr_write_b32 a165,v37
v_accvgpr_write_b32 a166,v38
v_accvgpr_write_b32 a167,v39
v_accvgpr_write_b32 a168,v40
v_accvgpr_write_b32 a169,v41
v_accvgpr_write_b32 a170,v42
v_accvgpr_write_b32 a171,v43
v_accvgpr_write_b32 a172,v44
v_accvgpr_write_b32 a173,v45
v_accvgpr_write_b32 a174,v46
v_accvgpr_write_b32 a175,v47
s_waitcnt vmcnt(0) lgkmcnt(0)
s_nop 7
v_bfe_u32 v11,v6,4,4
v_lshrrev_b32 v12,8,v6
v_lshlrev_b32 v8,8,v11
v_lshl_add_u32 v8,v12,4,v8
v_lshlrev_b32 v9,10,v12
v_lshl_add_u32 v9,v11,1,v9
v_add_u32 v10,4096,v9
v_mov_b32 v48,0xff800000
v_mov_b32 v52,0xff800000
v_mov_b32 v56,0xff800000
v_mov_b32 v60,0xff800000
s_mov_b32 s65,0
s_mov_b32 s66,64
L_s10_%=:
buffer_load_dwordx4 a[128:131],v8,s[20:23],s65 offen
buffer_load_dwordx4 a[132:135],v8,s[20:23],s65 offen offset:64
buffer_load_dwordx4 a[136:139],v8,s[20:23],s65 offen offset:128
buffer_load_dwordx4 a[140:143],v8,s[20:23],s65 offen offset:192
s_waitcnt vmcnt(0)
v_mfma_f32_16x16x32_bf16 v[64:67],a[128:131],v[132:135],0
v_mfma_f32_16x16x32_bf16 v[64:67],a[132:135],v[140:143],v[64:67]
v_mfma_f32_16x16x32_bf16 v[64:67],a[136:139],v[148:151],v[64:67]
v_mfma_f32_16x16x32_bf16 v[64:67],a[140:143],v[156:159],v[64:67]
v_mfma_f32_16x16x32_bf16 v[80:83],a[128:131],v[164:167],0
v_mfma_f32_16x16x32_bf16 v[80:83],a[132:135],v[172:175],v[80:83]
v_mfma_f32_16x16x32_bf16 v[80:83],a[136:139],v[180:183],v[80:83]
v_mfma_f32_16x16x32_bf16 v[80:83],a[140:143],v[188:191],v[80:83]
v_mfma_f32_16x16x32_bf16 v[96:99],a[128:131],v[196:199],0
v_mfma_f32_16x16x32_bf16 v[96:99],a[132:135],v[204:207],v[96:99]
v_mfma_f32_16x16x32_bf16 v[96:99],a[136:139],v[212:215],v[96:99]
v_mfma_f32_16x16x32_bf16 v[96:99],a[140:143],v[220:223],v[96:99]
v_mfma_f32_16x16x32_bf16 v[112:115],a[128:131],v[228:231],0
v_mfma_f32_16x16x32_bf16 v[112:115],a[132:135],v[236:239],v[112:115]
v_mfma_f32_16x16x32_bf16 v[112:115],a[136:139],v[244:247],v[112:115]
v_mfma_f32_16x16x32_bf16 v[112:115],a[140:143],v[252:255],v[112:115]
s_nop 7
v_max3_f32 v48,v48,v64,v65
v_max3_f32 v48,v48,v66,v67
v_max3_f32 v52,v52,v80,v81
v_max3_f32 v52,v52,v82,v83
v_max3_f32 v56,v56,v96,v97
v_max3_f32 v56,v56,v98,v99
v_max3_f32 v60,v60,v112,v113
v_max3_f32 v60,v60,v114,v115
s_add_u32 s65,s65,4096
s_sub_u32 s66,s66,1
s_cmp_lg_u32 s66,0
s_cbranch_scc1 L_s10_%=
v_mov_b32 v11,v48
s_nop 1
v_permlane32_swap_b32 v48,v11
v_max_f32 v48,v48,v11
v_mov_b32 v11,v48
s_nop 1
v_permlane16_swap_b32 v48,v11
v_max_f32 v48,v48,v11
v_sub_f32 v32,0,v48
v_sub_f32 v33,0,v48
v_sub_f32 v34,0,v48
v_sub_f32 v35,0,v48
v_mov_b32 v11,v52
s_nop 1
v_permlane32_swap_b32 v52,v11
v_max_f32 v52,v52,v11
v_mov_b32 v11,v52
s_nop 1
v_permlane16_swap_b32 v52,v11
v_max_f32 v52,v52,v11
v_sub_f32 v36,0,v52
v_sub_f32 v37,0,v52
v_sub_f32 v38,0,v52
v_sub_f32 v39,0,v52
v_mov_b32 v11,v56
s_nop 1
v_permlane32_swap_b32 v56,v11
v_max_f32 v56,v56,v11
v_mov_b32 v11,v56
s_nop 1
v_permlane16_swap_b32 v56,v11
v_max_f32 v56,v56,v11
v_sub_f32 v40,0,v56
v_sub_f32 v41,0,v56
v_sub_f32 v42,0,v56
v_sub_f32 v43,0,v56
v_mov_b32 v11,v60
s_nop 1
v_permlane32_swap_b32 v60,v11
v_max_f32 v60,v60,v11
v_mov_b32 v11,v60
s_nop 1
v_permlane16_swap_b32 v60,v11
v_max_f32 v60,v60,v11
v_max_f32 v60,s72,v60
v_sub_f32 v44,0,v60
v_sub_f32 v45,0,v60
v_sub_f32 v46,0,v60
v_sub_f32 v47,0,v60
v_mov_b32 v48,0
v_mov_b32 v49,0
v_mov_b32 v50,0
v_mov_b32 v51,0
v_mov_b32 v52,0
v_mov_b32 v53,0
v_mov_b32 v54,0
v_mov_b32 v55,0
v_mov_b32 v56,0
v_mov_b32 v57,0
v_mov_b32 v58,0
v_mov_b32 v59,0
v_mov_b32 v60,0
v_mov_b32 v61,0
v_mov_b32 v62,0
v_mov_b32 v63,0
v_accvgpr_write_b32 a0,0
v_accvgpr_write_b32 a1,0
v_accvgpr_write_b32 a2,0
v_accvgpr_write_b32 a3,0
v_accvgpr_write_b32 a4,0
v_accvgpr_write_b32 a5,0
v_accvgpr_write_b32 a6,0
v_accvgpr_write_b32 a7,0
v_accvgpr_write_b32 a8,0
v_accvgpr_write_b32 a9,0
v_accvgpr_write_b32 a10,0
v_accvgpr_write_b32 a11,0
v_accvgpr_write_b32 a12,0
v_accvgpr_write_b32 a13,0
v_accvgpr_write_b32 a14,0
v_accvgpr_write_b32 a15,0
v_accvgpr_write_b32 a16,0
v_accvgpr_write_b32 a17,0
v_accvgpr_write_b32 a18,0
v_accvgpr_write_b32 a19,0
v_accvgpr_write_b32 a20,0
v_accvgpr_write_b32 a21,0
v_accvgpr_write_b32 a22,0
v_accvgpr_write_b32 a23,0
v_accvgpr_write_b32 a24,0
v_accvgpr_write_b32 a25,0
v_accvgpr_write_b32 a26,0
v_accvgpr_write_b32 a27,0
v_accvgpr_write_b32 a28,0
v_accvgpr_write_b32 a29,0
v_accvgpr_write_b32 a30,0
v_accvgpr_write_b32 a31,0
v_accvgpr_write_b32 a32,0
v_accvgpr_write_b32 a33,0
v_accvgpr_write_b32 a34,0
v_accvgpr_write_b32 a35,0
v_accvgpr_write_b32 a36,0
v_accvgpr_write_b32 a37,0
v_accvgpr_write_b32 a38,0
v_accvgpr_write_b32 a39,0
v_accvgpr_write_b32 a40,0
v_accvgpr_write_b32 a41,0
v_accvgpr_write_b32 a42,0
v_accvgpr_write_b32 a43,0
v_accvgpr_write_b32 a44,0
v_accvgpr_write_b32 a45,0
v_accvgpr_write_b32 a46,0
v_accvgpr_write_b32 a47,0
v_accvgpr_write_b32 a48,0
v_accvgpr_write_b32 a49,0
v_accvgpr_write_b32 a50,0
v_accvgpr_write_b32 a51,0
v_accvgpr_write_b32 a52,0
v_accvgpr_write_b32 a53,0
v_accvgpr_write_b32 a54,0
v_accvgpr_write_b32 a55,0
v_accvgpr_write_b32 a56,0
v_accvgpr_write_b32 a57,0
v_accvgpr_write_b32 a58,0
v_accvgpr_write_b32 a59,0
v_accvgpr_write_b32 a60,0
v_accvgpr_write_b32 a61,0
v_accvgpr_write_b32 a62,0
v_accvgpr_write_b32 a63,0
v_accvgpr_write_b32 a64,0
v_accvgpr_write_b32 a65,0
v_accvgpr_write_b32 a66,0
v_accvgpr_write_b32 a67,0
v_accvgpr_write_b32 a68,0
v_accvgpr_write_b32 a69,0
v_accvgpr_write_b32 a70,0
v_accvgpr_write_b32 a71,0
v_accvgpr_write_b32 a72,0
v_accvgpr_write_b32 a73,0
v_accvgpr_write_b32 a74,0
v_accvgpr_write_b32 a75,0
v_accvgpr_write_b32 a76,0
v_accvgpr_write_b32 a77,0
v_accvgpr_write_b32 a78,0
v_accvgpr_write_b32 a79,0
v_accvgpr_write_b32 a80,0
v_accvgpr_write_b32 a81,0
v_accvgpr_write_b32 a82,0
v_accvgpr_write_b32 a83,0
v_accvgpr_write_b32 a84,0
v_accvgpr_write_b32 a85,0
v_accvgpr_write_b32 a86,0
v_accvgpr_write_b32 a87,0
v_accvgpr_write_b32 a88,0
v_accvgpr_write_b32 a89,0
v_accvgpr_write_b32 a90,0
v_accvgpr_write_b32 a91,0
v_accvgpr_write_b32 a92,0
v_accvgpr_write_b32 a93,0
v_accvgpr_write_b32 a94,0
v_accvgpr_write_b32 a95,0
v_accvgpr_write_b32 a96,0
v_accvgpr_write_b32 a97,0
v_accvgpr_write_b32 a98,0
v_accvgpr_write_b32 a99,0
v_accvgpr_write_b32 a100,0
v_accvgpr_write_b32 a101,0
v_accvgpr_write_b32 a102,0
v_accvgpr_write_b32 a103,0
v_accvgpr_write_b32 a104,0
v_accvgpr_write_b32 a105,0
v_accvgpr_write_b32 a106,0
v_accvgpr_write_b32 a107,0
v_accvgpr_write_b32 a108,0
v_accvgpr_write_b32 a109,0
v_accvgpr_write_b32 a110,0
v_accvgpr_write_b32 a111,0
v_accvgpr_write_b32 a112,0
v_accvgpr_write_b32 a113,0
v_accvgpr_write_b32 a114,0
v_accvgpr_write_b32 a115,0
v_accvgpr_write_b32 a116,0
v_accvgpr_write_b32 a117,0
v_accvgpr_write_b32 a118,0
v_accvgpr_write_b32 a119,0
v_accvgpr_write_b32 a120,0
v_accvgpr_write_b32 a121,0
v_accvgpr_write_b32 a122,0
v_accvgpr_write_b32 a123,0
v_accvgpr_write_b32 a124,0
v_accvgpr_write_b32 a125,0
v_accvgpr_write_b32 a126,0
v_accvgpr_write_b32 a127,0
s_mov_b32 s65,0
s_mov_b32 s66,32
L_s20_%=:
s_add_u32 s67,s65,4096
buffer_load_dwordx4 a[128:131],v8,s[20:23],s65 offen
buffer_load_dwordx4 a[132:135],v8,s[20:23],s65 offen offset:64
buffer_load_dwordx4 a[136:139],v8,s[20:23],s65 offen offset:128
buffer_load_dwordx4 a[140:143],v8,s[20:23],s65 offen offset:192
)A"
R"A(buffer_load_dwordx4 a[144:147],v8,s[20:23],s67 offen
buffer_load_dwordx4 a[148:151],v8,s[20:23],s67 offen offset:64
buffer_load_dwordx4 a[152:155],v8,s[20:23],s67 offen offset:128
buffer_load_dwordx4 a[156:159],v8,s[20:23],s67 offen offset:192
s_waitcnt vmcnt(0)
v_mfma_f32_16x16x32_bf16 v[64:67],a[128:131],v[132:135],v[32:35]
v_mfma_f32_16x16x32_bf16 v[64:67],a[132:135],v[140:143],v[64:67]
v_mfma_f32_16x16x32_bf16 v[64:67],a[136:139],v[148:151],v[64:67]
v_mfma_f32_16x16x32_bf16 v[64:67],a[140:143],v[156:159],v[64:67]
v_mfma_f32_16x16x32_bf16 v[68:71],a[144:147],v[132:135],v[32:35]
v_mfma_f32_16x16x32_bf16 v[68:71],a[148:151],v[140:143],v[68:71]
v_mfma_f32_16x16x32_bf16 v[68:71],a[152:155],v[148:151],v[68:71]
v_mfma_f32_16x16x32_bf16 v[68:71],a[156:159],v[156:159],v[68:71]
v_mfma_f32_16x16x32_bf16 v[80:83],a[128:131],v[164:167],v[36:39]
v_mfma_f32_16x16x32_bf16 v[80:83],a[132:135],v[172:175],v[80:83]
v_mfma_f32_16x16x32_bf16 v[80:83],a[136:139],v[180:183],v[80:83]
v_mfma_f32_16x16x32_bf16 v[80:83],a[140:143],v[188:191],v[80:83]
v_mfma_f32_16x16x32_bf16 v[84:87],a[144:147],v[164:167],v[36:39]
v_mfma_f32_16x16x32_bf16 v[84:87],a[148:151],v[172:175],v[84:87]
v_mfma_f32_16x16x32_bf16 v[84:87],a[152:155],v[180:183],v[84:87]
v_mfma_f32_16x16x32_bf16 v[84:87],a[156:159],v[188:191],v[84:87]
v_mfma_f32_16x16x32_bf16 v[96:99],a[128:131],v[196:199],v[40:43]
v_mfma_f32_16x16x32_bf16 v[96:99],a[132:135],v[204:207],v[96:99]
v_mfma_f32_16x16x32_bf16 v[96:99],a[136:139],v[212:215],v[96:99]
v_mfma_f32_16x16x32_bf16 v[96:99],a[140:143],v[220:223],v[96:99]
v_mfma_f32_16x16x32_bf16 v[100:103],a[144:147],v[196:199],v[40:43]
v_mfma_f32_16x16x32_bf16 v[100:103],a[148:151],v[204:207],v[100:103]
v_mfma_f32_16x16x32_bf16 v[100:103],a[152:155],v[212:215],v[100:103]
v_mfma_f32_16x16x32_bf16 v[100:103],a[156:159],v[220:223],v[100:103]
v_mfma_f32_16x16x32_bf16 v[112:115],a[128:131],v[228:231],v[44:47]
v_mfma_f32_16x16x32_bf16 v[112:115],a[132:135],v[236:239],v[112:115]
v_mfma_f32_16x16x32_bf16 v[112:115],a[136:139],v[244:247],v[112:115]
v_mfma_f32_16x16x32_bf16 v[112:115],a[140:143],v[252:255],v[112:115]
v_mfma_f32_16x16x32_bf16 v[116:119],a[144:147],v[228:231],v[44:47]
v_mfma_f32_16x16x32_bf16 v[116:119],a[148:151],v[236:239],v[116:119]
v_mfma_f32_16x16x32_bf16 v[116:119],a[152:155],v[244:247],v[116:119]
v_mfma_f32_16x16x32_bf16 v[116:119],a[156:159],v[252:255],v[116:119]
s_nop 7
v_exp_f32 v64,v64
v_exp_f32 v65,v65
v_exp_f32 v66,v66
v_exp_f32 v67,v67
v_exp_f32 v68,v68
v_exp_f32 v69,v69
v_exp_f32 v70,v70
v_exp_f32 v71,v71
s_nop 0
v_cvt_pk_bf16_f32 v64,v64,v65
v_cvt_pk_bf16_f32 v65,v66,v67
v_cvt_pk_bf16_f32 v66,v68,v69
v_cvt_pk_bf16_f32 v67,v70,v71
v_exp_f32 v80,v80
v_exp_f32 v81,v81
v_exp_f32 v82,v82
v_exp_f32 v83,v83
v_exp_f32 v84,v84
v_exp_f32 v85,v85
v_exp_f32 v86,v86
v_exp_f32 v87,v87
s_nop 0
v_cvt_pk_bf16_f32 v80,v80,v81
v_cvt_pk_bf16_f32 v81,v82,v83
v_cvt_pk_bf16_f32 v82,v84,v85
v_cvt_pk_bf16_f32 v83,v86,v87
v_exp_f32 v96,v96
v_exp_f32 v97,v97
v_exp_f32 v98,v98
v_exp_f32 v99,v99
v_exp_f32 v100,v100
v_exp_f32 v101,v101
v_exp_f32 v102,v102
v_exp_f32 v103,v103
s_nop 0
v_cvt_pk_bf16_f32 v96,v96,v97
v_cvt_pk_bf16_f32 v97,v98,v99
v_cvt_pk_bf16_f32 v98,v100,v101
v_cvt_pk_bf16_f32 v99,v102,v103
v_exp_f32 v112,v112
v_exp_f32 v113,v113
v_exp_f32 v114,v114
v_exp_f32 v115,v115
v_exp_f32 v116,v116
v_exp_f32 v117,v117
v_exp_f32 v118,v118
v_exp_f32 v119,v119
s_nop 0
v_cvt_pk_bf16_f32 v112,v112,v113
v_cvt_pk_bf16_f32 v113,v114,v115
v_cvt_pk_bf16_f32 v114,v116,v117
v_cvt_pk_bf16_f32 v115,v118,v119
buffer_load_ushort v72,v9,s[24:27],s65 offen
buffer_load_ushort v73,v9,s[24:27],s65 offen offset:256
buffer_load_ushort v74,v9,s[24:27],s65 offen offset:512
buffer_load_ushort v75,v9,s[24:27],s65 offen offset:768
buffer_load_ushort v76,v10,s[24:27],s65 offen
buffer_load_ushort v77,v10,s[24:27],s65 offen offset:256
buffer_load_ushort v78,v10,s[24:27],s65 offen offset:512
buffer_load_ushort v79,v10,s[24:27],s65 offen offset:768
s_waitcnt vmcnt(0)
v_lshl_or_b32 v88,v73,16,v72
v_lshl_or_b32 v89,v75,16,v74
v_lshl_or_b32 v90,v77,16,v76
v_lshl_or_b32 v91,v79,16,v78
s_nop 1
v_mfma_f32_16x16x32_bf16 a[0:3],v[88:91],v[64:67],a[0:3]
v_mfma_f32_16x16x32_bf16 a[32:35],v[88:91],v[80:83],a[32:35]
v_mfma_f32_16x16x32_bf16 a[64:67],v[88:91],v[96:99],a[64:67]
v_mfma_f32_16x16x32_bf16 a[96:99],v[88:91],v[112:115],a[96:99]
buffer_load_ushort v72,v9,s[24:27],s65 offen offset:32
buffer_load_ushort v73,v9,s[24:27],s65 offen offset:288
buffer_load_ushort v74,v9,s[24:27],s65 offen offset:544
buffer_load_ushort v75,v9,s[24:27],s65 offen offset:800
buffer_load_ushort v76,v10,s[24:27],s65 offen offset:32
buffer_load_ushort v77,v10,s[24:27],s65 offen offset:288
buffer_load_ushort v78,v10,s[24:27],s65 offen offset:544
buffer_load_ushort v79,v10,s[24:27],s65 offen offset:800
s_waitcnt vmcnt(0)
v_lshl_or_b32 v88,v73,16,v72
v_lshl_or_b32 v89,v75,16,v74
v_lshl_or_b32 v90,v77,16,v76
v_lshl_or_b32 v91,v79,16,v78
s_nop 1
v_mfma_f32_16x16x32_bf16 a[4:7],v[88:91],v[64:67],a[4:7]
v_mfma_f32_16x16x32_bf16 a[36:39],v[88:91],v[80:83],a[36:39]
v_mfma_f32_16x16x32_bf16 a[68:71],v[88:91],v[96:99],a[68:71]
v_mfma_f32_16x16x32_bf16 a[100:103],v[88:91],v[112:115],a[100:103]
buffer_load_ushort v72,v9,s[24:27],s65 offen offset:64
buffer_load_ushort v73,v9,s[24:27],s65 offen offset:320
buffer_load_ushort v74,v9,s[24:27],s65 offen offset:576
buffer_load_ushort v75,v9,s[24:27],s65 offen offset:832
buffer_load_ushort v76,v10,s[24:27],s65 offen offset:64
buffer_load_ushort v77,v10,s[24:27],s65 offen offset:320
buffer_load_ushort v78,v10,s[24:27],s65 offen offset:576
buffer_load_ushort v79,v10,s[24:27],s65 offen offset:832
s_waitcnt vmcnt(0)
v_lshl_or_b32 v88,v73,16,v72
v_lshl_or_b32 v89,v75,16,v74
v_lshl_or_b32 v90,v77,16,v76
v_lshl_or_b32 v91,v79,16,v78
s_nop 1
v_mfma_f32_16x16x32_bf16 a[8:11],v[88:91],v[64:67],a[8:11]
v_mfma_f32_16x16x32_bf16 a[40:43],v[88:91],v[80:83],a[40:43]
v_mfma_f32_16x16x32_bf16 a[72:75],v[88:91],v[96:99],a[72:75]
v_mfma_f32_16x16x32_bf16 a[104:107],v[88:91],v[112:115],a[104:107]
buffer_load_ushort v72,v9,s[24:27],s65 offen offset:96
buffer_load_ushort v73,v9,s[24:27],s65 offen offset:352
buffer_load_ushort v74,v9,s[24:27],s65 offen offset:608
buffer_load_ushort v75,v9,s[24:27],s65 offen offset:864
buffer_load_ushort v76,v10,s[24:27],s65 offen offset:96
buffer_load_ushort v77,v10,s[24:27],s65 offen offset:352
buffer_load_ushort v78,v10,s[24:27],s65 offen offset:608
buffer_load_ushort v79,v10,s[24:27],s65 offen offset:864
s_waitcnt vmcnt(0)
v_lshl_or_b32 v88,v73,16,v72
v_lshl_or_b32 v89,v75,16,v74
v_lshl_or_b32 v90,v77,16,v76
v_lshl_or_b32 v91,v79,16,v78
s_nop 1
v_mfma_f32_16x16x32_bf16 a[12:15],v[88:91],v[64:67],a[12:15]
v_mfma_f32_16x16x32_bf16 a[44:47],v[88:91],v[80:83],a[44:47]
v_mfma_f32_16x16x32_bf16 a[76:79],v[88:91],v[96:99],a[76:79]
v_mfma_f32_16x16x32_bf16 a[108:111],v[88:91],v[112:115],a[108:111]
buffer_load_ushort v72,v9,s[24:27],s65 offen offset:128
buffer_load_ushort v73,v9,s[24:27],s65 offen offset:384
buffer_load_ushort v74,v9,s[24:27],s65 offen offset:640
buffer_load_ushort v75,v9,s[24:27],s65 offen offset:896
buffer_load_ushort v76,v10,s[24:27],s65 offen offset:128
buffer_load_ushort v77,v10,s[24:27],s65 offen offset:384
buffer_load_ushort v78,v10,s[24:27],s65 offen offset:640
buffer_load_ushort v79,v10,s[24:27],s65 offen offset:896
s_waitcnt vmcnt(0)
v_lshl_or_b32 v88,v73,16,v72
v_lshl_or_b32 v89,v75,16,v74
v_lshl_or_b32 v90,v77,16,v76
v_lshl_or_b32 v91,v79,16,v78
s_nop 1
v_mfma_f32_16x16x32_bf16 a[16:19],v[88:91],v[64:67],a[16:19]
v_mfma_f32_16x16x32_bf16 a[48:51],v[88:91],v[80:83],a[48:51]
v_mfma_f32_16x16x32_bf16 a[80:83],v[88:91],v[96:99],a[80:83]
v_mfma_f32_16x16x32_bf16 a[112:115],v[88:91],v[112:115],a[112:115]
buffer_load_ushort v72,v9,s[24:27],s65 offen offset:160
buffer_load_ushort v73,v9,s[24:27],s65 offen offset:416
buffer_load_ushort v74,v9,s[24:27],s65 offen offset:672
buffer_load_ushort v75,v9,s[24:27],s65 offen offset:928
buffer_load_ushort v76,v10,s[24:27],s65 offen offset:160
buffer_load_ushort v77,v10,s[24:27],s65 offen offset:416
buffer_load_ushort v78,v10,s[24:27],s65 offen offset:672
buffer_load_ushort v79,v10,s[24:27],s65 offen offset:928
s_waitcnt vmcnt(0)
v_lshl_or_b32 v88,v73,16,v72
v_lshl_or_b32 v89,v75,16,v74
v_lshl_or_b32 v90,v77,16,v76
v_lshl_or_b32 v91,v79,16,v78
s_nop 1
v_mfma_f32_16x16x32_bf16 a[20:23],v[88:91],v[64:67],a[20:23]
v_mfma_f32_16x16x32_bf16 a[52:55],v[88:91],v[80:83],a[52:55]
v_mfma_f32_16x16x32_bf16 a[84:87],v[88:91],v[96:99],a[84:87]
v_mfma_f32_16x16x32_bf16 a[116:119],v[88:91],v[112:115],a[116:119]
buffer_load_ushort v72,v9,s[24:27],s65 offen offset:192
buffer_load_ushort v73,v9,s[24:27],s65 offen offset:448
buffer_load_ushort v74,v9,s[24:27],s65 offen offset:704
buffer_load_ushort v75,v9,s[24:27],s65 offen offset:960
buffer_load_ushort v76,v10,s[24:27],s65 offen offset:192
buffer_load_ushort v77,v10,s[24:27],s65 offen offset:448
buffer_load_ushort v78,v10,s[24:27],s65 offen offset:704
buffer_load_ushort v79,v10,s[24:27],s65 offen offset:960
s_waitcnt vmcnt(0)
v_lshl_or_b32 v88,v73,16,v72
v_lshl_or_b32 v89,v75,16,v74
v_lshl_or_b32 v90,v77,16,v76
v_lshl_or_b32 v91,v79,16,v78
s_nop 1
v_mfma_f32_16x16x32_bf16 a[24:27],v[88:91],v[64:67],a[24:27]
v_mfma_f32_16x16x32_bf16 a[56:59],v[88:91],v[80:83],a[56:59]
v_mfma_f32_16x16x32_bf16 a[88:91],v[88:91],v[96:99],a[88:91]
v_mfma_f32_16x16x32_bf16 a[120:123],v[88:91],v[112:115],a[120:123]
buffer_load_ushort v72,v9,s[24:27],s65 offen offset:224
buffer_load_ushort v73,v9,s[24:27],s65 offen offset:480
buffer_load_ushort v74,v9,s[24:27],s65 offen offset:736
buffer_load_ushort v75,v9,s[24:27],s65 offen offset:992
buffer_load_ushort v76,v10,s[24:27],s65 offen offset:224
buffer_load_ushort v77,v10,s[24:27],s65 offen offset:480
buffer_load_ushort v78,v10,s[24:27],s65 offen offset:736
buffer_load_ushort v79,v10,s[24:27],s65 offen offset:992
s_waitcnt vmcnt(0)
v_lshl_or_b32 v88,v73,16,v72
v_lshl_or_b32 v89,v75,16,v74
v_lshl_or_b32 v90,v77,16,v76
v_lshl_or_b32 v91,v79,16,v78
s_nop 1
v_mfma_f32_16x16x32_bf16 a[28:31],v[88:91],v[64:67],a[28:31]
v_mfma_f32_16x16x32_bf16 a[60:63],v[88:91],v[80:83],a[60:63]
v_mfma_f32_16x16x32_bf16 a[92:95],v[88:91],v[96:99],a[92:95]
v_mfma_f32_16x16x32_bf16 a[124:127],v[88:91],v[112:115],a[124:127]
v_mfma_f32_16x16x32_bf16 v[48:51],v[28:31],v[64:67],v[48:51]
v_mfma_f32_16x16x32_bf16 v[52:55],v[28:31],v[80:83],v[52:55]
v_mfma_f32_16x16x32_bf16 v[56:59],v[28:31],v[96:99],v[56:59]
v_mfma_f32_16x16x32_bf16 v[60:63],v[28:31],v[112:115],v[60:63]
s_add_u32 s65,s65,8192
s_sub_u32 s66,s66,1
s_cmp_lg_u32 s66,0
s_cbranch_scc1 L_s20_%=
s_nop 7
v_max_f32 v24,0x00800000,v48
v_rcp_f32 v24,v24
v_max_f32 v25,0x00800000,v52
v_rcp_f32 v25,v25
v_max_f32 v26,0x00800000,v56
v_rcp_f32 v26,v26
v_max_f32 v27,0x00800000,v60
v_rcp_f32 v27,v27
v_accvgpr_read_b32 v8,a0
v_accvgpr_read_b32 v9,a1
v_accvgpr_read_b32 v10,a2
v_accvgpr_read_b32 v11,a3
v_accvgpr_read_b32 v12,a4
v_accvgpr_read_b32 v13,a5
v_accvgpr_read_b32 v14,a6
v_accvgpr_read_b32 v15,a7
v_mul_f32 v8,v8,v24
v_mul_f32 v9,v9,v24
v_mul_f32 v10,v10,v24
v_mul_f32 v11,v11,v24
v_mul_f32 v12,v12,v24
v_mul_f32 v13,v13,v24
v_mul_f32 v14,v14,v24
v_mul_f32 v15,v15,v24
v_cvt_pk_bf16_f32 v8,v8,v9
v_cvt_pk_bf16_f32 v9,v10,v11
v_cvt_pk_bf16_f32 v10,v12,v13
v_cvt_pk_bf16_f32 v11,v14,v15
ds_write_b128 v7,v[8:11] offset:6144
v_accvgpr_read_b32 v16,a8
v_accvgpr_read_b32 v17,a9
v_accvgpr_read_b32 v18,a10
v_accvgpr_read_b32 v19,a11
v_accvgpr_read_b32 v20,a12
v_accvgpr_read_b32 v21,a13
v_accvgpr_read_b32 v22,a14
v_accvgpr_read_b32 v23,a15
v_mul_f32 v16,v16,v24
v_mul_f32 v17,v17,v24
v_mul_f32 v18,v18,v24
v_mul_f32 v19,v19,v24
v_mul_f32 v20,v20,v24
v_mul_f32 v21,v21,v24
v_mul_f32 v22,v22,v24
v_mul_f32 v23,v23,v24
v_cvt_pk_bf16_f32 v16,v16,v17
v_cvt_pk_bf16_f32 v17,v18,v19
v_cvt_pk_bf16_f32 v18,v20,v21
v_cvt_pk_bf16_f32 v19,v22,v23
ds_write_b128 v7,v[16:19] offset:7168
v_accvgpr_read_b32 v8,a16
v_accvgpr_read_b32 v9,a17
v_accvgpr_read_b32 v10,a18
v_accvgpr_read_b32 v11,a19
v_accvgpr_read_b32 v12,a20
v_accvgpr_read_b32 v13,a21
)A"
R"A(v_accvgpr_read_b32 v14,a22
v_accvgpr_read_b32 v15,a23
v_mul_f32 v8,v8,v24
v_mul_f32 v9,v9,v24
v_mul_f32 v10,v10,v24
v_mul_f32 v11,v11,v24
v_mul_f32 v12,v12,v24
v_mul_f32 v13,v13,v24
v_mul_f32 v14,v14,v24
v_mul_f32 v15,v15,v24
v_cvt_pk_bf16_f32 v8,v8,v9
v_cvt_pk_bf16_f32 v9,v10,v11
v_cvt_pk_bf16_f32 v10,v12,v13
v_cvt_pk_bf16_f32 v11,v14,v15
ds_write_b128 v7,v[8:11]
v_accvgpr_read_b32 v16,a24
v_accvgpr_read_b32 v17,a25
v_accvgpr_read_b32 v18,a26
v_accvgpr_read_b32 v19,a27
v_accvgpr_read_b32 v20,a28
v_accvgpr_read_b32 v21,a29
v_accvgpr_read_b32 v22,a30
v_accvgpr_read_b32 v23,a31
v_mul_f32 v16,v16,v24
v_mul_f32 v17,v17,v24
v_mul_f32 v18,v18,v24
v_mul_f32 v19,v19,v24
v_mul_f32 v20,v20,v24
v_mul_f32 v21,v21,v24
v_mul_f32 v22,v22,v24
v_mul_f32 v23,v23,v24
v_cvt_pk_bf16_f32 v16,v16,v17
v_cvt_pk_bf16_f32 v17,v18,v19
v_cvt_pk_bf16_f32 v18,v20,v21
v_cvt_pk_bf16_f32 v19,v22,v23
ds_write_b128 v7,v[16:19] offset:1024
v_accvgpr_read_b32 v32,a160
v_accvgpr_read_b32 v33,a161
v_accvgpr_read_b32 v34,a162
v_accvgpr_read_b32 v35,a163
v_accvgpr_read_b32 v36,a164
v_accvgpr_read_b32 v37,a165
v_accvgpr_read_b32 v38,a166
v_accvgpr_read_b32 v39,a167
v_accvgpr_read_b32 v40,a168
v_accvgpr_read_b32 v41,a169
v_accvgpr_read_b32 v42,a170
v_accvgpr_read_b32 v43,a171
v_accvgpr_read_b32 v44,a172
v_accvgpr_read_b32 v45,a173
v_accvgpr_read_b32 v46,a174
v_accvgpr_read_b32 v47,a175
s_waitcnt lgkmcnt(0)
v_subrev_u32 v8,s78,v1
ds_read_b128 a[128:131],v8
ds_read_b128 a[132:135],v8 offset:256
ds_read_b128 a[136:139],v8 offset:512
ds_read_b128 a[140:143],v8 offset:768
ds_read_b128 a[144:147],v8 offset:4096
ds_read_b128 a[148:151],v8 offset:4352
ds_read_b128 a[152:155],v8 offset:4608
ds_read_b128 a[156:159],v8 offset:4864
ds_read_b128 a[160:163],v8 offset:8192
ds_read_b128 a[164:167],v8 offset:8448
ds_read_b128 a[168:171],v8 offset:8704
ds_read_b128 a[172:175],v8 offset:8960
ds_read_b128 a[176:179],v8 offset:12288
ds_read_b128 a[180:183],v8 offset:12544
ds_read_b128 a[184:187],v8 offset:12800
ds_read_b128 a[188:191],v8 offset:13056
s_waitcnt lgkmcnt(15)
v_mfma_f32_16x16x32_bf16 v[64:67],a[128:131],v[128:131],v[32:35]
s_waitcnt lgkmcnt(14)
v_mfma_f32_16x16x32_bf16 v[64:67],a[132:135],v[136:139],v[64:67]
s_waitcnt lgkmcnt(13)
v_mfma_f32_16x16x32_bf16 v[64:67],a[136:139],v[144:147],v[64:67]
s_waitcnt lgkmcnt(12)
v_mfma_f32_16x16x32_bf16 v[64:67],a[140:143],v[152:155],v[64:67]
s_waitcnt lgkmcnt(8)
v_mfma_f32_16x16x32_bf16 v[68:71],a[156:159],v[152:155],v[32:35]
v_mfma_f32_16x16x32_bf16 v[68:71],a[152:155],v[144:147],v[68:71]
v_mfma_f32_16x16x32_bf16 v[68:71],a[148:151],v[136:139],v[68:71]
v_mfma_f32_16x16x32_bf16 v[68:71],a[144:147],v[128:131],v[68:71]
s_waitcnt lgkmcnt(0)
v_mfma_f32_16x16x32_bf16 v[72:75],a[160:163],v[128:131],v[32:35]
v_mfma_f32_16x16x32_bf16 v[72:75],a[164:167],v[136:139],v[72:75]
v_mfma_f32_16x16x32_bf16 v[72:75],a[168:171],v[144:147],v[72:75]
v_mfma_f32_16x16x32_bf16 v[72:75],a[172:175],v[152:155],v[72:75]
v_mfma_f32_16x16x32_bf16 v[76:79],a[188:191],v[152:155],v[32:35]
v_mfma_f32_16x16x32_bf16 v[76:79],a[184:187],v[144:147],v[76:79]
v_mfma_f32_16x16x32_bf16 v[76:79],a[180:183],v[136:139],v[76:79]
v_mfma_f32_16x16x32_bf16 v[76:79],a[176:179],v[128:131],v[76:79]
v_mfma_f32_16x16x32_bf16 v[80:83],a[128:131],v[160:163],v[36:39]
v_mfma_f32_16x16x32_bf16 v[80:83],a[132:135],v[168:171],v[80:83]
v_mfma_f32_16x16x32_bf16 v[80:83],a[136:139],v[176:179],v[80:83]
v_mfma_f32_16x16x32_bf16 v[80:83],a[140:143],v[184:187],v[80:83]
v_mfma_f32_16x16x32_bf16 v[84:87],a[156:159],v[184:187],v[36:39]
v_mfma_f32_16x16x32_bf16 v[84:87],a[152:155],v[176:179],v[84:87]
v_mfma_f32_16x16x32_bf16 v[84:87],a[148:151],v[168:171],v[84:87]
v_mfma_f32_16x16x32_bf16 v[84:87],a[144:147],v[160:163],v[84:87]
v_mfma_f32_16x16x32_bf16 v[88:91],a[160:163],v[160:163],v[36:39]
v_mfma_f32_16x16x32_bf16 v[88:91],a[164:167],v[168:171],v[88:91]
v_mfma_f32_16x16x32_bf16 v[88:91],a[168:171],v[176:179],v[88:91]
v_mfma_f32_16x16x32_bf16 v[88:91],a[172:175],v[184:187],v[88:91]
v_mfma_f32_16x16x32_bf16 v[92:95],a[188:191],v[184:187],v[36:39]
v_mfma_f32_16x16x32_bf16 v[92:95],a[184:187],v[176:179],v[92:95]
v_mfma_f32_16x16x32_bf16 v[92:95],a[180:183],v[168:171],v[92:95]
v_mfma_f32_16x16x32_bf16 v[92:95],a[176:179],v[160:163],v[92:95]
v_mfma_f32_16x16x32_bf16 v[96:99],a[128:131],v[192:195],v[40:43]
v_mfma_f32_16x16x32_bf16 v[96:99],a[132:135],v[200:203],v[96:99]
v_mfma_f32_16x16x32_bf16 v[96:99],a[136:139],v[208:211],v[96:99]
v_mfma_f32_16x16x32_bf16 v[96:99],a[140:143],v[216:219],v[96:99]
v_mfma_f32_16x16x32_bf16 v[100:103],a[156:159],v[216:219],v[40:43]
v_mfma_f32_16x16x32_bf16 v[100:103],a[152:155],v[208:211],v[100:103]
v_mfma_f32_16x16x32_bf16 v[100:103],a[148:151],v[200:203],v[100:103]
v_mfma_f32_16x16x32_bf16 v[100:103],a[144:147],v[192:195],v[100:103]
v_mfma_f32_16x16x32_bf16 v[104:107],a[160:163],v[192:195],v[40:43]
v_mfma_f32_16x16x32_bf16 v[104:107],a[164:167],v[200:203],v[104:107]
v_mfma_f32_16x16x32_bf16 v[104:107],a[168:171],v[208:211],v[104:107]
v_mfma_f32_16x16x32_bf16 v[104:107],a[172:175],v[216:219],v[104:107]
v_mfma_f32_16x16x32_bf16 v[108:111],a[188:191],v[216:219],v[40:43]
v_mfma_f32_16x16x32_bf16 v[108:111],a[184:187],v[208:211],v[108:111]
v_mfma_f32_16x16x32_bf16 v[108:111],a[180:183],v[200:203],v[108:111]
v_mfma_f32_16x16x32_bf16 v[108:111],a[176:179],v[192:195],v[108:111]
v_exp_f32 v64,v64
v_exp_f32 v65,v65
s_nop 0
v_cvt_pk_bf16_f32 v64,v64,v65
v_exp_f32 v66,v66
v_exp_f32 v67,v67
s_nop 0
v_cvt_pk_bf16_f32 v65,v66,v67
v_exp_f32 v68,v68
v_exp_f32 v69,v69
s_nop 0
v_cvt_pk_bf16_f32 v66,v68,v69
v_exp_f32 v70,v70
v_exp_f32 v71,v71
s_nop 0
v_cvt_pk_bf16_f32 v67,v70,v71
v_exp_f32 v72,v72
v_exp_f32 v73,v73
s_nop 0
v_cvt_pk_bf16_f32 v72,v72,v73
v_exp_f32 v74,v74
v_exp_f32 v75,v75
s_nop 0
v_cvt_pk_bf16_f32 v73,v74,v75
v_exp_f32 v76,v76
v_exp_f32 v77,v77
s_nop 0
v_cvt_pk_bf16_f32 v74,v76,v77
v_exp_f32 v78,v78
v_exp_f32 v79,v79
s_nop 0
v_cvt_pk_bf16_f32 v75,v78,v79
v_exp_f32 v80,v80
v_exp_f32 v81,v81
s_nop 0
v_cvt_pk_bf16_f32 v80,v80,v81
v_exp_f32 v82,v82
v_exp_f32 v83,v83
s_nop 0
v_cvt_pk_bf16_f32 v81,v82,v83
v_exp_f32 v84,v84
v_exp_f32 v85,v85
s_nop 0
v_cvt_pk_bf16_f32 v82,v84,v85
v_exp_f32 v86,v86
v_exp_f32 v87,v87
s_nop 0
v_cvt_pk_bf16_f32 v83,v86,v87
v_exp_f32 v88,v88
v_exp_f32 v89,v89
s_nop 0
v_cvt_pk_bf16_f32 v88,v88,v89
v_mfma_f32_16x16x32_bf16 a[0:3],a[192:195],v[64:67],0
v_mfma_f32_16x16x32_bf16 a[0:3],a[196:199],v[72:75],a[0:3]
v_mfma_f32_16x16x32_bf16 a[4:7],a[200:203],v[64:67],0
v_mfma_f32_16x16x32_bf16 a[4:7],a[204:207],v[72:75],a[4:7]
v_mfma_f32_16x16x32_bf16 a[8:11],a[208:211],v[64:67],0
v_mfma_f32_16x16x32_bf16 a[8:11],a[212:215],v[72:75],a[8:11]
v_mfma_f32_16x16x32_bf16 a[12:15],a[216:219],v[64:67],0
v_mfma_f32_16x16x32_bf16 a[12:15],a[220:223],v[72:75],a[12:15]
v_mfma_f32_16x16x32_bf16 a[16:19],a[224:227],v[64:67],0
v_mfma_f32_16x16x32_bf16 a[16:19],a[228:231],v[72:75],a[16:19]
v_mfma_f32_16x16x32_bf16 a[20:23],a[232:235],v[64:67],0
v_mfma_f32_16x16x32_bf16 a[20:23],a[236:239],v[72:75],a[20:23]
v_mfma_f32_16x16x32_bf16 a[24:27],a[240:243],v[64:67],0
v_mfma_f32_16x16x32_bf16 a[24:27],a[244:247],v[72:75],a[24:27]
v_mfma_f32_16x16x32_bf16 a[28:31],a[248:251],v[64:67],0
v_mfma_f32_16x16x32_bf16 a[28:31],a[252:255],v[72:75],a[28:31]
v_mfma_f32_16x16x32_bf16 v[48:51],v[28:31],v[64:67],0
v_mfma_f32_16x16x32_bf16 v[48:51],v[28:31],v[72:75],v[48:51]
s_waitcnt vmcnt(0) lgkmcnt(0)
s_nop 7
s_branch L_ok0_%=
L_slow1_%=:
s_waitcnt vmcnt(0) lgkmcnt(0)
s_nop 7
v_bfe_u32 v11,v6,4,4
v_lshrrev_b32 v12,8,v6
v_lshlrev_b32 v8,8,v11
v_lshl_add_u32 v8,v12,4,v8
v_lshlrev_b32 v9,10,v12
v_lshl_add_u32 v9,v11,1,v9
v_add_u32 v10,4096,v9
v_mov_b32 v48,0xff800000
v_mov_b32 v52,0xff800000
v_mov_b32 v56,0xff800000
v_mov_b32 v60,0xff800000
s_mov_b32 s65,262144
s_mov_b32 s66,64
L_s11_%=:
buffer_load_dwordx4 a[128:131],v8,s[20:23],s65 offen
buffer_load_dwordx4 a[132:135],v8,s[20:23],s65 offen offset:64
buffer_load_dwordx4 a[136:139],v8,s[20:23],s65 offen offset:128
buffer_load_dwordx4 a[140:143],v8,s[20:23],s65 offen offset:192
s_waitcnt vmcnt(0)
v_mfma_f32_16x16x32_bf16 v[64:67],a[128:131],v[128:131],0
v_mfma_f32_16x16x32_bf16 v[64:67],a[132:135],v[136:139],v[64:67]
v_mfma_f32_16x16x32_bf16 v[64:67],a[136:139],v[144:147],v[64:67]
v_mfma_f32_16x16x32_bf16 v[64:67],a[140:143],v[152:155],v[64:67]
v_mfma_f32_16x16x32_bf16 v[80:83],a[128:131],v[160:163],0
v_mfma_f32_16x16x32_bf16 v[80:83],a[132:135],v[168:171],v[80:83]
v_mfma_f32_16x16x32_bf16 v[80:83],a[136:139],v[176:179],v[80:83]
v_mfma_f32_16x16x32_bf16 v[80:83],a[140:143],v[184:187],v[80:83]
v_mfma_f32_16x16x32_bf16 v[96:99],a[128:131],v[192:195],0
v_mfma_f32_16x16x32_bf16 v[96:99],a[132:135],v[200:203],v[96:99]
v_mfma_f32_16x16x32_bf16 v[96:99],a[136:139],v[208:211],v[96:99]
v_mfma_f32_16x16x32_bf16 v[96:99],a[140:143],v[216:219],v[96:99]
v_mfma_f32_16x16x32_bf16 v[112:115],a[128:131],v[224:227],0
v_mfma_f32_16x16x32_bf16 v[112:115],a[132:135],v[232:235],v[112:115]
v_mfma_f32_16x16x32_bf16 v[112:115],a[136:139],v[240:243],v[112:115]
v_mfma_f32_16x16x32_bf16 v[112:115],a[140:143],v[248:251],v[112:115]
s_nop 7
v_max3_f32 v48,v48,v64,v65
v_max3_f32 v48,v48,v66,v67
v_max3_f32 v52,v52,v80,v81
v_max3_f32 v52,v52,v82,v83
v_max3_f32 v56,v56,v96,v97
v_max3_f32 v56,v56,v98,v99
v_max3_f32 v60,v60,v112,v113
v_max3_f32 v60,v60,v114,v115
s_add_u32 s65,s65,4096
s_sub_u32 s66,s66,1
s_cmp_lg_u32 s66,0
s_cbranch_scc1 L_s11_%=
v_mov_b32 v11,v48
s_nop 1
v_permlane32_swap_b32 v48,v11
v_max_f32 v48,v48,v11
v_mov_b32 v11,v48
s_nop 1
v_permlane16_swap_b32 v48,v11
v_max_f32 v48,v48,v11
v_sub_f32 v32,0,v48
v_sub_f32 v33,0,v48
v_sub_f32 v34,0,v48
v_sub_f32 v35,0,v48
v_mov_b32 v11,v52
s_nop 1
v_permlane32_swap_b32 v52,v11
v_max_f32 v52,v52,v11
v_mov_b32 v11,v52
s_nop 1
v_permlane16_swap_b32 v52,v11
v_max_f32 v52,v52,v11
v_sub_f32 v36,0,v52
v_sub_f32 v37,0,v52
v_sub_f32 v38,0,v52
v_sub_f32 v39,0,v52
v_mov_b32 v11,v56
s_nop 1
v_permlane32_swap_b32 v56,v11
v_max_f32 v56,v56,v11
v_mov_b32 v11,v56
s_nop 1
v_permlane16_swap_b32 v56,v11
v_max_f32 v56,v56,v11
v_sub_f32 v40,0,v56
v_sub_f32 v41,0,v56
v_sub_f32 v42,0,v56
v_sub_f32 v43,0,v56
v_mov_b32 v11,v60
s_nop 1
v_permlane32_swap_b32 v60,v11
v_max_f32 v60,v60,v11
v_mov_b32 v11,v60
s_nop 1
v_permlane16_swap_b32 v60,v11
v_max_f32 v60,v60,v11
v_max_f32 v60,s72,v60
v_sub_f32 v44,0,v60
v_sub_f32 v45,0,v60
v_sub_f32 v46,0,v60
v_sub_f32 v47,0,v60
v_mov_b32 v48,0
v_mov_b32 v49,0
v_mov_b32 v50,0
v_mov_b32 v51,0
v_mov_b32 v52,0
v_mov_b32 v53,0
v_mov_b32 v54,0
v_mov_b32 v55,0
v_mov_b32 v56,0
v_mov_b32 v57,0
v_mov_b32 v58,0
v_mov_b32 v59,0
v_mov_b32 v60,0
v_mov_b32 v61,0
v_mov_b32 v62,0
v_mov_b32 v63,0
v_accvgpr_write_b32 a0,0
v_accvgpr_write_b32 a1,0
v_accvgpr_write_b32 a2,0
v_accvgpr_write_b32 a3,0
v_accvgpr_write_b32 a4,0
v_accvgpr_write_b32 a5,0
v_accvgpr_write_b32 a6,0
v_accvgpr_write_b32 a7,0
v_accvgpr_write_b32 a8,0
v_accvgpr_write_b32 a9,0
v_accvgpr_write_b32 a10,0
v_accvgpr_write_b32 a11,0
v_accvgpr_write_b32 a12,0
v_accvgpr_write_b32 a13,0
v_accvgpr_write_b32 a14,0
v_accvgpr_write_b32 a15,0
v_accvgpr_write_b32 a16,0
v_accvgpr_write_b32 a17,0
v_accvgpr_write_b32 a18,0
v_accvgpr_write_b32 a19,0
v_accvgpr_write_b32 a20,0
v_accvgpr_write_b32 a21,0
v_accvgpr_write_b32 a22,0
v_accvgpr_write_b32 a23,0
v_accvgpr_write_b32 a24,0
v_accvgpr_write_b32 a25,0
v_accvgpr_write_b32 a26,0
v_accvgpr_write_b32 a27,0
v_accvgpr_write_b32 a28,0
v_accvgpr_write_b32 a29,0
v_accvgpr_write_b32 a30,0
v_accvgpr_write_b32 a31,0
v_mov_b32 v132,0
v_mov_b32 v133,0
v_mov_b32 v134,0
v_mov_b32 v135,0
v_mov_b32 v140,0
v_mov_b32 v141,0
v_mov_b32 v142,0
v_mov_b32 v143,0
v_mov_b32 v148,0
v_mov_b32 v149,0
v_mov_b32 v150,0
v_mov_b32 v151,0
v_mov_b32 v156,0
v_mov_b32 v157,0
v_mov_b32 v158,0
v_mov_b32 v159,0
v_mov_b32 v164,0
v_mov_b32 v165,0
v_mov_b32 v166,0
v_mov_b32 v167,0
v_mov_b32 v172,0
v_mov_b32 v173,0
v_mov_b32 v174,0
v_mov_b32 v175,0
v_mov_b32 v180,0
v_mov_b32 v181,0
v_mov_b32 v182,0
v_mov_b32 v183,0
v_mov_b32 v188,0
v_mov_b32 v189,0
v_mov_b32 v190,0
v_mov_b32 v191,0
v_mov_b32 v196,0
v_mov_b32 v197,0
v_mov_b32 v198,0
v_mov_b32 v199,0
)A"
R"A(v_mov_b32 v204,0
v_mov_b32 v205,0
v_mov_b32 v206,0
v_mov_b32 v207,0
v_mov_b32 v212,0
v_mov_b32 v213,0
v_mov_b32 v214,0
v_mov_b32 v215,0
v_mov_b32 v220,0
v_mov_b32 v221,0
v_mov_b32 v222,0
v_mov_b32 v223,0
v_mov_b32 v228,0
v_mov_b32 v229,0
v_mov_b32 v230,0
v_mov_b32 v231,0
v_mov_b32 v236,0
v_mov_b32 v237,0
v_mov_b32 v238,0
v_mov_b32 v239,0
v_mov_b32 v244,0
v_mov_b32 v245,0
v_mov_b32 v246,0
v_mov_b32 v247,0
v_mov_b32 v252,0
v_mov_b32 v253,0
v_mov_b32 v254,0
v_mov_b32 v255,0
v_accvgpr_write_b32 a32,0
v_accvgpr_write_b32 a33,0
v_accvgpr_write_b32 a34,0
v_accvgpr_write_b32 a35,0
v_accvgpr_write_b32 a36,0
v_accvgpr_write_b32 a37,0
v_accvgpr_write_b32 a38,0
v_accvgpr_write_b32 a39,0
v_accvgpr_write_b32 a40,0
v_accvgpr_write_b32 a41,0
v_accvgpr_write_b32 a42,0
v_accvgpr_write_b32 a43,0
v_accvgpr_write_b32 a44,0
v_accvgpr_write_b32 a45,0
v_accvgpr_write_b32 a46,0
v_accvgpr_write_b32 a47,0
v_accvgpr_write_b32 a48,0
v_accvgpr_write_b32 a49,0
v_accvgpr_write_b32 a50,0
v_accvgpr_write_b32 a51,0
v_accvgpr_write_b32 a52,0
v_accvgpr_write_b32 a53,0
v_accvgpr_write_b32 a54,0
v_accvgpr_write_b32 a55,0
v_accvgpr_write_b32 a56,0
v_accvgpr_write_b32 a57,0
v_accvgpr_write_b32 a58,0
v_accvgpr_write_b32 a59,0
v_accvgpr_write_b32 a60,0
v_accvgpr_write_b32 a61,0
v_accvgpr_write_b32 a62,0
v_accvgpr_write_b32 a63,0
s_mov_b32 s65,262144
s_mov_b32 s66,32
L_s21_%=:
s_add_u32 s67,s65,4096
buffer_load_dwordx4 a[128:131],v8,s[20:23],s65 offen
buffer_load_dwordx4 a[132:135],v8,s[20:23],s65 offen offset:64
buffer_load_dwordx4 a[136:139],v8,s[20:23],s65 offen offset:128
buffer_load_dwordx4 a[140:143],v8,s[20:23],s65 offen offset:192
buffer_load_dwordx4 a[144:147],v8,s[20:23],s67 offen
buffer_load_dwordx4 a[148:151],v8,s[20:23],s67 offen offset:64
buffer_load_dwordx4 a[152:155],v8,s[20:23],s67 offen offset:128
buffer_load_dwordx4 a[156:159],v8,s[20:23],s67 offen offset:192
s_waitcnt vmcnt(0)
v_mfma_f32_16x16x32_bf16 v[64:67],a[128:131],v[128:131],v[32:35]
v_mfma_f32_16x16x32_bf16 v[64:67],a[132:135],v[136:139],v[64:67]
v_mfma_f32_16x16x32_bf16 v[64:67],a[136:139],v[144:147],v[64:67]
v_mfma_f32_16x16x32_bf16 v[64:67],a[140:143],v[152:155],v[64:67]
v_mfma_f32_16x16x32_bf16 v[68:71],a[144:147],v[128:131],v[32:35]
v_mfma_f32_16x16x32_bf16 v[68:71],a[148:151],v[136:139],v[68:71]
v_mfma_f32_16x16x32_bf16 v[68:71],a[152:155],v[144:147],v[68:71]
v_mfma_f32_16x16x32_bf16 v[68:71],a[156:159],v[152:155],v[68:71]
v_mfma_f32_16x16x32_bf16 v[80:83],a[128:131],v[160:163],v[36:39]
v_mfma_f32_16x16x32_bf16 v[80:83],a[132:135],v[168:171],v[80:83]
v_mfma_f32_16x16x32_bf16 v[80:83],a[136:139],v[176:179],v[80:83]
v_mfma_f32_16x16x32_bf16 v[80:83],a[140:143],v[184:187],v[80:83]
v_mfma_f32_16x16x32_bf16 v[84:87],a[144:147],v[160:163],v[36:39]
v_mfma_f32_16x16x32_bf16 v[84:87],a[148:151],v[168:171],v[84:87]
v_mfma_f32_16x16x32_bf16 v[84:87],a[152:155],v[176:179],v[84:87]
v_mfma_f32_16x16x32_bf16 v[84:87],a[156:159],v[184:187],v[84:87]
v_mfma_f32_16x16x32_bf16 v[96:99],a[128:131],v[192:195],v[40:43]
v_mfma_f32_16x16x32_bf16 v[96:99],a[132:135],v[200:203],v[96:99]
v_mfma_f32_16x16x32_bf16 v[96:99],a[136:139],v[208:211],v[96:99]
v_mfma_f32_16x16x32_bf16 v[96:99],a[140:143],v[216:219],v[96:99]
v_mfma_f32_16x16x32_bf16 v[100:103],a[144:147],v[192:195],v[40:43]
v_mfma_f32_16x16x32_bf16 v[100:103],a[148:151],v[200:203],v[100:103]
v_mfma_f32_16x16x32_bf16 v[100:103],a[152:155],v[208:211],v[100:103]
v_mfma_f32_16x16x32_bf16 v[100:103],a[156:159],v[216:219],v[100:103]
v_mfma_f32_16x16x32_bf16 v[112:115],a[128:131],v[224:227],v[44:47]
v_mfma_f32_16x16x32_bf16 v[112:115],a[132:135],v[232:235],v[112:115]
v_mfma_f32_16x16x32_bf16 v[112:115],a[136:139],v[240:243],v[112:115]
v_mfma_f32_16x16x32_bf16 v[112:115],a[140:143],v[248:251],v[112:115]
v_mfma_f32_16x16x32_bf16 v[116:119],a[144:147],v[224:227],v[44:47]
v_mfma_f32_16x16x32_bf16 v[116:119],a[148:151],v[232:235],v[116:119]
v_mfma_f32_16x16x32_bf16 v[116:119],a[152:155],v[240:243],v[116:119]
v_mfma_f32_16x16x32_bf16 v[116:119],a[156:159],v[248:251],v[116:119]
s_nop 7
v_exp_f32 v64,v64
v_exp_f32 v65,v65
v_exp_f32 v66,v66
v_exp_f32 v67,v67
v_exp_f32 v68,v68
v_exp_f32 v69,v69
v_exp_f32 v70,v70
v_exp_f32 v71,v71
s_nop 0
v_cvt_pk_bf16_f32 v64,v64,v65
v_cvt_pk_bf16_f32 v65,v66,v67
v_cvt_pk_bf16_f32 v66,v68,v69
v_cvt_pk_bf16_f32 v67,v70,v71
v_exp_f32 v80,v80
v_exp_f32 v81,v81
v_exp_f32 v82,v82
v_exp_f32 v83,v83
v_exp_f32 v84,v84
v_exp_f32 v85,v85
v_exp_f32 v86,v86
v_exp_f32 v87,v87
s_nop 0
v_cvt_pk_bf16_f32 v80,v80,v81
v_cvt_pk_bf16_f32 v81,v82,v83
v_cvt_pk_bf16_f32 v82,v84,v85
v_cvt_pk_bf16_f32 v83,v86,v87
v_exp_f32 v96,v96
v_exp_f32 v97,v97
v_exp_f32 v98,v98
v_exp_f32 v99,v99
v_exp_f32 v100,v100
v_exp_f32 v101,v101
v_exp_f32 v102,v102
v_exp_f32 v103,v103
s_nop 0
v_cvt_pk_bf16_f32 v96,v96,v97
v_cvt_pk_bf16_f32 v97,v98,v99
v_cvt_pk_bf16_f32 v98,v100,v101
v_cvt_pk_bf16_f32 v99,v102,v103
v_exp_f32 v112,v112
v_exp_f32 v113,v113
v_exp_f32 v114,v114
v_exp_f32 v115,v115
v_exp_f32 v116,v116
v_exp_f32 v117,v117
v_exp_f32 v118,v118
v_exp_f32 v119,v119
s_nop 0
v_cvt_pk_bf16_f32 v112,v112,v113
v_cvt_pk_bf16_f32 v113,v114,v115
v_cvt_pk_bf16_f32 v114,v116,v117
v_cvt_pk_bf16_f32 v115,v118,v119
buffer_load_ushort v72,v9,s[24:27],s65 offen
buffer_load_ushort v73,v9,s[24:27],s65 offen offset:256
buffer_load_ushort v74,v9,s[24:27],s65 offen offset:512
buffer_load_ushort v75,v9,s[24:27],s65 offen offset:768
buffer_load_ushort v76,v10,s[24:27],s65 offen
buffer_load_ushort v77,v10,s[24:27],s65 offen offset:256
buffer_load_ushort v78,v10,s[24:27],s65 offen offset:512
buffer_load_ushort v79,v10,s[24:27],s65 offen offset:768
s_waitcnt vmcnt(0)
v_lshl_or_b32 v88,v73,16,v72
v_lshl_or_b32 v89,v75,16,v74
v_lshl_or_b32 v90,v77,16,v76
v_lshl_or_b32 v91,v79,16,v78
s_nop 1
v_mfma_f32_16x16x32_bf16 a[0:3],v[88:91],v[64:67],a[0:3]
v_mfma_f32_16x16x32_bf16 v[132:135],v[88:91],v[80:83],v[132:135]
v_mfma_f32_16x16x32_bf16 v[196:199],v[88:91],v[96:99],v[196:199]
v_mfma_f32_16x16x32_bf16 a[32:35],v[88:91],v[112:115],a[32:35]
buffer_load_ushort v72,v9,s[24:27],s65 offen offset:32
buffer_load_ushort v73,v9,s[24:27],s65 offen offset:288
buffer_load_ushort v74,v9,s[24:27],s65 offen offset:544
buffer_load_ushort v75,v9,s[24:27],s65 offen offset:800
buffer_load_ushort v76,v10,s[24:27],s65 offen offset:32
buffer_load_ushort v77,v10,s[24:27],s65 offen offset:288
buffer_load_ushort v78,v10,s[24:27],s65 offen offset:544
buffer_load_ushort v79,v10,s[24:27],s65 offen offset:800
s_waitcnt vmcnt(0)
v_lshl_or_b32 v88,v73,16,v72
v_lshl_or_b32 v89,v75,16,v74
v_lshl_or_b32 v90,v77,16,v76
v_lshl_or_b32 v91,v79,16,v78
s_nop 1
v_mfma_f32_16x16x32_bf16 a[4:7],v[88:91],v[64:67],a[4:7]
v_mfma_f32_16x16x32_bf16 v[140:143],v[88:91],v[80:83],v[140:143]
v_mfma_f32_16x16x32_bf16 v[204:207],v[88:91],v[96:99],v[204:207]
v_mfma_f32_16x16x32_bf16 a[36:39],v[88:91],v[112:115],a[36:39]
buffer_load_ushort v72,v9,s[24:27],s65 offen offset:64
buffer_load_ushort v73,v9,s[24:27],s65 offen offset:320
buffer_load_ushort v74,v9,s[24:27],s65 offen offset:576
buffer_load_ushort v75,v9,s[24:27],s65 offen offset:832
buffer_load_ushort v76,v10,s[24:27],s65 offen offset:64
buffer_load_ushort v77,v10,s[24:27],s65 offen offset:320
buffer_load_ushort v78,v10,s[24:27],s65 offen offset:576
buffer_load_ushort v79,v10,s[24:27],s65 offen offset:832
s_waitcnt vmcnt(0)
v_lshl_or_b32 v88,v73,16,v72
v_lshl_or_b32 v89,v75,16,v74
v_lshl_or_b32 v90,v77,16,v76
v_lshl_or_b32 v91,v79,16,v78
s_nop 1
v_mfma_f32_16x16x32_bf16 a[8:11],v[88:91],v[64:67],a[8:11]
v_mfma_f32_16x16x32_bf16 v[148:151],v[88:91],v[80:83],v[148:151]
v_mfma_f32_16x16x32_bf16 v[212:215],v[88:91],v[96:99],v[212:215]
v_mfma_f32_16x16x32_bf16 a[40:43],v[88:91],v[112:115],a[40:43]
buffer_load_ushort v72,v9,s[24:27],s65 offen offset:96
buffer_load_ushort v73,v9,s[24:27],s65 offen offset:352
buffer_load_ushort v74,v9,s[24:27],s65 offen offset:608
buffer_load_ushort v75,v9,s[24:27],s65 offen offset:864
buffer_load_ushort v76,v10,s[24:27],s65 offen offset:96
buffer_load_ushort v77,v10,s[24:27],s65 offen offset:352
buffer_load_ushort v78,v10,s[24:27],s65 offen offset:608
buffer_load_ushort v79,v10,s[24:27],s65 offen offset:864
s_waitcnt vmcnt(0)
v_lshl_or_b32 v88,v73,16,v72
v_lshl_or_b32 v89,v75,16,v74
v_lshl_or_b32 v90,v77,16,v76
v_lshl_or_b32 v91,v79,16,v78
s_nop 1
v_mfma_f32_16x16x32_bf16 a[12:15],v[88:91],v[64:67],a[12:15]
v_mfma_f32_16x16x32_bf16 v[156:159],v[88:91],v[80:83],v[156:159]
v_mfma_f32_16x16x32_bf16 v[220:223],v[88:91],v[96:99],v[220:223]
v_mfma_f32_16x16x32_bf16 a[44:47],v[88:91],v[112:115],a[44:47]
buffer_load_ushort v72,v9,s[24:27],s65 offen offset:128
buffer_load_ushort v73,v9,s[24:27],s65 offen offset:384
buffer_load_ushort v74,v9,s[24:27],s65 offen offset:640
buffer_load_ushort v75,v9,s[24:27],s65 offen offset:896
buffer_load_ushort v76,v10,s[24:27],s65 offen offset:128
buffer_load_ushort v77,v10,s[24:27],s65 offen offset:384
buffer_load_ushort v78,v10,s[24:27],s65 offen offset:640
buffer_load_ushort v79,v10,s[24:27],s65 offen offset:896
s_waitcnt vmcnt(0)
v_lshl_or_b32 v88,v73,16,v72
v_lshl_or_b32 v89,v75,16,v74
v_lshl_or_b32 v90,v77,16,v76
v_lshl_or_b32 v91,v79,16,v78
s_nop 1
v_mfma_f32_16x16x32_bf16 a[16:19],v[88:91],v[64:67],a[16:19]
v_mfma_f32_16x16x32_bf16 v[164:167],v[88:91],v[80:83],v[164:167]
v_mfma_f32_16x16x32_bf16 v[228:231],v[88:91],v[96:99],v[228:231]
v_mfma_f32_16x16x32_bf16 a[48:51],v[88:91],v[112:115],a[48:51]
buffer_load_ushort v72,v9,s[24:27],s65 offen offset:160
buffer_load_ushort v73,v9,s[24:27],s65 offen offset:416
buffer_load_ushort v74,v9,s[24:27],s65 offen offset:672
buffer_load_ushort v75,v9,s[24:27],s65 offen offset:928
buffer_load_ushort v76,v10,s[24:27],s65 offen offset:160
buffer_load_ushort v77,v10,s[24:27],s65 offen offset:416
buffer_load_ushort v78,v10,s[24:27],s65 offen offset:672
buffer_load_ushort v79,v10,s[24:27],s65 offen offset:928
s_waitcnt vmcnt(0)
v_lshl_or_b32 v88,v73,16,v72
v_lshl_or_b32 v89,v75,16,v74
v_lshl_or_b32 v90,v77,16,v76
v_lshl_or_b32 v91,v79,16,v78
s_nop 1
v_mfma_f32_16x16x32_bf16 a[20:23],v[88:91],v[64:67],a[20:23]
v_mfma_f32_16x16x32_bf16 v[172:175],v[88:91],v[80:83],v[172:175]
v_mfma_f32_16x16x32_bf16 v[236:239],v[88:91],v[96:99],v[236:239]
v_mfma_f32_16x16x32_bf16 a[52:55],v[88:91],v[112:115],a[52:55]
buffer_load_ushort v72,v9,s[24:27],s65 offen offset:192
buffer_load_ushort v73,v9,s[24:27],s65 offen offset:448
buffer_load_ushort v74,v9,s[24:27],s65 offen offset:704
buffer_load_ushort v75,v9,s[24:27],s65 offen offset:960
buffer_load_ushort v76,v10,s[24:27],s65 offen offset:192
buffer_load_ushort v77,v10,s[24:27],s65 offen offset:448
buffer_load_ushort v78,v10,s[24:27],s65 offen offset:704
buffer_load_ushort v79,v10,s[24:27],s65 offen offset:960
s_waitcnt vmcnt(0)
v_lshl_or_b32 v88,v73,16,v72
v_lshl_or_b32 v89,v75,16,v74
v_lshl_or_b32 v90,v77,16,v76
v_lshl_or_b32 v91,v79,16,v78
s_nop 1
v_mfma_f32_16x16x32_bf16 a[24:27],v[88:91],v[64:67],a[24:27]
v_mfma_f32_16x16x32_bf16 v[180:183],v[88:91],v[80:83],v[180:183]
v_mfma_f32_16x16x32_bf16 v[244:247],v[88:91],v[96:99],v[244:247]
v_mfma_f32_16x16x32_bf16 a[56:59],v[88:91],v[112:115],a[56:59]
buffer_load_ushort v72,v9,s[24:27],s65 offen offset:224
buffer_load_ushort v73,v9,s[24:27],s65 offen offset:480
buffer_load_ushort v74,v9,s[24:27],s65 offen offset:736
buffer_load_ushort v75,v9,s[24:27],s65 offen offset:992
buffer_load_ushort v76,v10,s[24:27],s65 offen offset:224
buffer_load_ushort v77,v10,s[24:27],s65 offen offset:480
buffer_load_ushort v78,v10,s[24:27],s65 offen offset:736
buffer_load_ushort v79,v10,s[24:27],s65 offen offset:992
s_waitcnt vmcnt(0)
v_lshl_or_b32 v88,v73,16,v72
v_lshl_or_b32 v89,v75,16,v74
v_lshl_or_b32 v90,v77,16,v76
v_lshl_or_b32 v91,v79,16,v78
s_nop 1
v_mfma_f32_16x16x32_bf16 a[28:31],v[88:91],v[64:67],a[28:31]
v_mfma_f32_16x16x32_bf16 v[188:191],v[88:91],v[80:83],v[188:191]
v_mfma_f32_16x16x32_bf16 v[252:255],v[88:91],v[96:99],v[252:255]
v_mfma_f32_16x16x32_bf16 a[60:63],v[88:91],v[112:115],a[60:63]
v_mfma_f32_16x16x32_bf16 v[48:51],v[28:31],v[64:67],v[48:51]
v_mfma_f32_16x16x32_bf16 v[52:55],v[28:31],v[80:83],v[52:55]
)A"
R"A(v_mfma_f32_16x16x32_bf16 v[56:59],v[28:31],v[96:99],v[56:59]
v_mfma_f32_16x16x32_bf16 v[60:63],v[28:31],v[112:115],v[60:63]
s_add_u32 s65,s65,8192
s_sub_u32 s66,s66,1
s_cmp_lg_u32 s66,0
s_cbranch_scc1 L_s21_%=
s_nop 7
v_max_f32 v49,0x00800000,v48
v_rcp_f32 v49,v49
v_max_f32 v53,0x00800000,v52
v_rcp_f32 v53,v53
v_max_f32 v57,0x00800000,v56
v_rcp_f32 v57,v57
v_max_f32 v61,0x00800000,v60
v_rcp_f32 v61,v61
v_accvgpr_read_b32 v16,a0
v_accvgpr_read_b32 v17,a1
v_accvgpr_read_b32 v18,a2
v_accvgpr_read_b32 v19,a3
v_accvgpr_read_b32 v20,a4
v_accvgpr_read_b32 v21,a5
v_accvgpr_read_b32 v22,a6
v_accvgpr_read_b32 v23,a7
v_mul_f32 v16,v16,v49
v_mul_f32 v17,v17,v49
v_mul_f32 v18,v18,v49
v_mul_f32 v19,v19,v49
v_mul_f32 v20,v20,v49
v_mul_f32 v21,v21,v49
v_mul_f32 v22,v22,v49
v_mul_f32 v23,v23,v49
v_cvt_pk_bf16_f32 v32,v16,v17
v_cvt_pk_bf16_f32 v33,v18,v19
v_cvt_pk_bf16_f32 v34,v20,v21
v_cvt_pk_bf16_f32 v35,v22,v23
v_accvgpr_read_b32 v8,a8
v_accvgpr_read_b32 v9,a9
v_accvgpr_read_b32 v10,a10
v_accvgpr_read_b32 v11,a11
v_accvgpr_read_b32 v12,a12
v_accvgpr_read_b32 v13,a13
v_accvgpr_read_b32 v14,a14
v_accvgpr_read_b32 v15,a15
v_mul_f32 v8,v8,v49
v_mul_f32 v9,v9,v49
v_mul_f32 v10,v10,v49
v_mul_f32 v11,v11,v49
v_mul_f32 v12,v12,v49
v_mul_f32 v13,v13,v49
v_mul_f32 v14,v14,v49
v_mul_f32 v15,v15,v49
v_cvt_pk_bf16_f32 v36,v8,v9
v_cvt_pk_bf16_f32 v37,v10,v11
v_cvt_pk_bf16_f32 v38,v12,v13
v_cvt_pk_bf16_f32 v39,v14,v15
v_accvgpr_read_b32 v16,a16
v_accvgpr_read_b32 v17,a17
v_accvgpr_read_b32 v18,a18
v_accvgpr_read_b32 v19,a19
v_accvgpr_read_b32 v20,a20
v_accvgpr_read_b32 v21,a21
v_accvgpr_read_b32 v22,a22
v_accvgpr_read_b32 v23,a23
v_mul_f32 v16,v16,v49
v_mul_f32 v17,v17,v49
v_mul_f32 v18,v18,v49
v_mul_f32 v19,v19,v49
v_mul_f32 v20,v20,v49
v_mul_f32 v21,v21,v49
v_mul_f32 v22,v22,v49
v_mul_f32 v23,v23,v49
v_cvt_pk_bf16_f32 v40,v16,v17
v_cvt_pk_bf16_f32 v41,v18,v19
v_cvt_pk_bf16_f32 v42,v20,v21
v_cvt_pk_bf16_f32 v43,v22,v23
ds_read_b64_tr_b16 v[64:65],v1
ds_read_b64_tr_b16 v[66:67],v1 offset:5120
ds_read_b64_tr_b16 v[68:69],v1 offset:10240
ds_read_b64_tr_b16 v[70:71],v1 offset:15360
ds_read_b64_tr_b16 v[72:73],v1 offset:20480
ds_read_b64_tr_b16 v[74:75],v1 offset:25600
ds_read_b64_tr_b16 v[76:77],v1 offset:30720
ds_read_b64_tr_b16 v[78:79],v1 offset:35840
ds_read_b64_tr_b16 v[80:81],v2 offset:32
ds_read_b64_tr_b16 v[82:83],v2 offset:5152
ds_read_b64_tr_b16 v[84:85],v2 offset:10272
ds_read_b64_tr_b16 v[86:87],v2 offset:15392
ds_read_b64_tr_b16 v[88:89],v2 offset:20512
ds_read_b64_tr_b16 v[90:91],v2 offset:25632
ds_read_b64_tr_b16 v[92:93],v2 offset:30752
ds_read_b64_tr_b16 v[94:95],v2 offset:35872
s_waitcnt vmcnt(0) lgkmcnt(0)
s_nop 7
s_branch L_ok1_%=
L_end_%=:
)A"
    :: "s"(ka), "s"(blk), "s"(wid)
    : "v0", "v1", "v2", "v3", "v4", "v5", "v6", "v7", "v8", "v9", "v10", "v11", "v12", "v13", "v14", "v15", "v16", "v17", "v18", "v19", "v20", "v21", "v22", "v23", "v24", "v25", "v26", "v27", "v28", "v29", "v30", "v31", "v32", "v33", "v34", "v35", "v36", "v37", "v38", "v39", "v40", "v41", "v42", "v43", "v44", "v45", "v46", "v47", "v48", "v49", "v50", "v51", "v52", "v53", "v54", "v55", "v56", "v57", "v58", "v59", "v60", "v61", "v62", "v63", "v64", "v65", "v66", "v67", "v68", "v69", "v70", "v71", "v72", "v73", "v74", "v75", "v76", "v77", "v78", "v79", "v80", "v81", "v82", "v83", "v84", "v85", "v86", "v87", "v88", "v89", "v90", "v91", "v92", "v93", "v94", "v95", "v96", "v97", "v98", "v99", "v100", "v101", "v102", "v103", "v104", "v105", "v106", "v107", "v108", "v109", "v110", "v111", "v112", "v113", "v114", "v115", "v116", "v117", "v118", "v119", "v120", "v121", "v122", "v123", "v124", "v125", "v126", "v127", "v128", "v129", "v130", "v131", "v132", "v133", "v134", "v135", "v136", "v137", "v138", "v139", "v140", "v141", "v142", "v143", "v144", "v145", "v146", "v147", "v148", "v149", "v150", "v151", "v152", "v153", "v154", "v155", "v156", "v157", "v158", "v159", "v160", "v161", "v162", "v163", "v164", "v165", "v166", "v167", "v168", "v169", "v170", "v171", "v172", "v173", "v174", "v175", "v176", "v177", "v178", "v179", "v180", "v181", "v182", "v183", "v184", "v185", "v186", "v187", "v188", "v189", "v190", "v191", "v192", "v193", "v194", "v195", "v196", "v197", "v198", "v199", "v200", "v201", "v202", "v203", "v204", "v205", "v206", "v207", "v208", "v209", "v210", "v211", "v212", "v213", "v214", "v215", "v216", "v217", "v218", "v219", "v220", "v221", "v222", "v223", "v224", "v225", "v226", "v227", "v228", "v229", "v230", "v231", "v232", "v233", "v234", "v235", "v236", "v237", "v238", "v239", "v240", "v241", "v242", "v243", "v244", "v245", "v246", "v247", "v248", "v249", "v250", "v251", "v252", "v253", "v254", "v255", "a0", "a1", "a2", "a3", "a4", "a5", "a6", "a7", "a8", "a9", "a10", "a11", "a12", "a13", "a14", "a15", "a16", "a17", "a18", "a19", "a20", "a21", "a22", "a23", "a24", "a25", "a26", "a27", "a28", "a29", "a30", "a31", "a32", "a33", "a34", "a35", "a36", "a37", "a38", "a39", "a40", "a41", "a42", "a43", "a44", "a45", "a46", "a47", "a48", "a49", "a50", "a51", "a52", "a53", "a54", "a55", "a56", "a57", "a58", "a59", "a60", "a61", "a62", "a63", "a64", "a65", "a66", "a67", "a68", "a69", "a70", "a71", "a72", "a73", "a74", "a75", "a76", "a77", "a78", "a79", "a80", "a81", "a82", "a83", "a84", "a85", "a86", "a87", "a88", "a89", "a90", "a91", "a92", "a93", "a94", "a95", "a96", "a97", "a98", "a99", "a100", "a101", "a102", "a103", "a104", "a105", "a106", "a107", "a108", "a109", "a110", "a111", "a112", "a113", "a114", "a115", "a116", "a117", "a118", "a119", "a120", "a121", "a122", "a123", "a124", "a125", "a126", "a127", "a128", "a129", "a130", "a131", "a132", "a133", "a134", "a135", "a136", "a137", "a138", "a139", "a140", "a141", "a142", "a143", "a144", "a145", "a146", "a147", "a148", "a149", "a150", "a151", "a152", "a153", "a154", "a155", "a156", "a157", "a158", "a159", "a160", "a161", "a162", "a163", "a164", "a165", "a166", "a167", "a168", "a169", "a170", "a171", "a172", "a173", "a174", "a175", "a176", "a177", "a178", "a179", "a180", "a181", "a182", "a183", "a184", "a185", "a186", "a187", "a188", "a189", "a190", "a191", "a192", "a193", "a194", "a195", "a196", "a197", "a198", "a199", "a200", "a201", "a202", "a203", "a204", "a205", "a206", "a207", "a208", "a209", "a210", "a211", "a212", "a213", "a214", "a215", "a216", "a217", "a218", "a219", "a220", "a221", "a222", "a223", "a224", "a225", "a226", "a227", "a228", "a229", "a230", "a231", "a232", "a233", "a234", "a235", "a236", "a237", "a238", "a239", "a240", "a241", "a242", "a243", "a244", "a245", "a246", "a247", "a248", "a249", "a250", "a251", "a252", "a253", "a254", "a255", "s4", "s5", "s6", "s7", "s8", "s9", "s10", "s11", "s12", "s13", "s14", "s15", "s16", "s17", "s18", "s19", "s20", "s21", "s22", "s23", "s24", "s25", "s26", "s27", "s28", "s29", "s30", "s31", "s36", "s37", "s38", "s39", "s40", "s41", "s42", "s43", "s44", "s45", "s46", "s47", "s48", "s49", "s50", "s51", "s52", "s53", "s54", "s55", "s56", "s57", "s58", "s59", "s60", "s61", "s62", "s63", "s64", "s65", "s66", "s67", "s68", "s69", "s70", "s71", "s72", "s73", "s74", "s75", "s76", "s77", "s78", "s79", "s80", "s81", "s82", "s83", "s84", "s85", "s86", "s87", "s88", "s89", "s90", "s91", "s92", "s93", "s94", "s95", "s96", "s97", "s98", "s99", "vcc", "scc", "memory");
}

extern "C" void kernel_launch(void* const* d_in, const int* in_sizes, int n_in,
                              void* d_out, int out_size, void* d_ws, size_t ws_size,
                              hipStream_t stream) {
  (void)hipFuncSetAttribute((const void*)qattn_kernel, hipFuncAttributeMaxDynamicSharedMemorySize, LDS_ATT);
  qattn_kernel<<<256, 256, LDS_ATT, stream>>>(d_in[0], d_in[1], d_in[2], d_in[3], d_ws, d_in[4], d_out);
}
```

```cpp
#include <hip/hip_runtime.h>
#include <hip/hip_bf16.h>

constexpr int LDS_ATT = 163840;

__global__ __launch_bounds__(256, 1) __attribute__((aligned(4096))) void qattn_kernel(const void* __restrict__ x, const void* __restrict__ kc, const void* __restrict__ vc,
                                                       const void* __restrict__ wq, void* __restrict__ ws, const void* __restrict__ wo,
                                                       void* __restrict__ out) {
  const unsigned blk = blockIdx.x;
  const unsigned wid = __builtin_amdgcn_readfirstlane(threadIdx.x >> 6);
  auto ka = __builtin_amdgcn_kernarg_segment_ptr();
  asm volatile(
R"A(s_load_dwordx8 s[4:11],%0,0x0
s_load_dwordx4 s[12:15],%0,0x20
s_load_dwordx2 s[16:17],%0,0x30
v_mbcnt_lo_u32_b32 v23,-1,0
v_mbcnt_hi_u32_b32 v23,-1,v23
s_mov_b32 s60,%2
s_and_b32 s66,%1,7
s_lshr_b32 s67,%1,3
s_getpc_b64 s[20:21]
L_pc_%=:
s_and_b32 s20,s20,0xffffff80
s_and_b32 s21,s21,0xffff
s_mov_b32 s22,L_hot_%=-L_pc_%=
s_mov_b32 s23,0x20000
s_lshl_b32 s68,s67,2
s_add_u32 s68,s68,s60
s_mul_i32 s68,s68,896
v_lshlrev_b32 v67,7,v23
s_mov_b64 exec,127
buffer_load_dword v8,v67,s[20:23],s68 offen
s_mov_b64 exec,-1
s_lshr_b32 s61,s66,2
s_lshl_b32 s61,s61,3
s_lshr_b32 s68,s67,2
s_add_u32 s61,s61,s68
s_and_b32 s62,s66,1
s_lshl_b32 s62,s62,2
s_and_b32 s68,s66,2
s_add_u32 s62,s62,s68
s_lshl_b32 s62,s62,1
s_and_b32 s68,s67,3
s_add_u32 s62,s62,s68
s_lshl_b32 s83,s62,9
s_mov_b32 s84,0
s_lshl_b32 s89,s60,13
s_add_u32 s85,s89,65536
s_add_u32 s88,s89,131072
v_and_b32 v20,3,v23
v_bfe_u32 v21,v23,2,2
v_lshrrev_b32 v22,4,v23
v_lshrrev_b32 v75,3,v23
s_lshl_b32 s66,s60,6
v_add_u32 v75,s66,v75
v_mul_u32_u24 v75,1280,v75
v_and_b32 v76,7,v23
v_or_b32 v77,0,v22
v_xor_b32 v77,v77,v76
v_lshl_add_u32 v77,v77,4,v75
v_add_u32 v32,0,v77
v_add_u32 v34,18432,v77
v_add_u32 v36,40960,v77
v_add_u32 v38,59392,v77
v_or_b32 v77,4,v22
v_xor_b32 v77,v77,v76
v_lshl_add_u32 v77,v77,4,v75
v_add_u32 v33,9216,v77
v_add_u32 v35,27648,v77
v_add_u32 v37,50176,v77
v_add_u32 v39,68608,v77
s_lshl_b32 s66,s60,17
v_lshlrev_b32 v75,13,v21
v_lshl_add_u32 v75,v22,6,v75
v_lshl_add_u32 v75,v20,4,v75
v_add_u32 v75,s66,v75
v_add_u32 v40,0,v75
v_add_u32 v41,256,v75
v_add_u32 v42,32768,v75
v_add_u32 v43,33024,v75
v_add_u32 v44,65536,v75
v_add_u32 v45,65792,v75
v_add_u32 v46,98304,v75
v_add_u32 v47,98560,v75
s_mul_i32 s66,s61,307200
s_waitcnt lgkmcnt(0)
s_add_u32 s28,s4,s66
s_addc_u32 s29,s5,0
s_and_b32 s29,s29,0xffff
s_mov_b32 s30,307200
s_mov_b32 s31,0x20000
s_mov_b32 s36,s10
s_and_b32 s37,s11,0xffff
s_mov_b32 s38,5242880
s_mov_b32 s39,0x20000
s_add_u32 m0,s85,0
s_nop 0
buffer_load_dwordx4 v32,s[28:31],s84 offen lds
buffer_load_dwordx4 v33,s[28:31],s84 offen offset:1024 lds
buffer_load_dwordx4 v34,s[28:31],s84 offen offset:2048 lds
buffer_load_dwordx4 v35,s[28:31],s84 offen offset:3072 lds
s_add_u32 m0,s85,4096
s_nop 0
buffer_load_dwordx4 v36,s[28:31],s84 offen lds
buffer_load_dwordx4 v37,s[28:31],s84 offen offset:1024 lds
buffer_load_dwordx4 v38,s[28:31],s84 offen offset:2048 lds
buffer_load_dwordx4 v39,s[28:31],s84 offen offset:3072 lds
s_add_u32 s84,s84,128
s_xor_b32 s85,s85,98304
s_add_u32 m0,s88,0
s_nop 0
buffer_load_dwordx4 v40,s[36:39],s83 offen lds
s_add_u32 m0,s88,1024
s_nop 0
buffer_load_dwordx4 v41,s[36:39],s83 offen lds
s_add_u32 m0,s88,2048
s_nop 0
buffer_load_dwordx4 v42,s[36:39],s83 offen lds
s_add_u32 m0,s88,3072
s_nop 0
buffer_load_dwordx4 v43,s[36:39],s83 offen lds
s_add_u32 m0,s88,4096
s_nop 0
buffer_load_dwordx4 v44,s[36:39],s83 offen lds
s_add_u32 m0,s88,5120
s_nop 0
buffer_load_dwordx4 v45,s[36:39],s83 offen lds
s_add_u32 m0,s88,6144
s_nop 0
buffer_load_dwordx4 v46,s[36:39],s83 offen lds
s_add_u32 m0,s88,7168
s_nop 0
buffer_load_dwordx4 v47,s[36:39],s83 offen lds
s_add_u32 s83,s83,524288
s_lshl_b32 s66,s61,12
s_add_u32 s66,s66,104857600
s_add_u32 s44,s12,s66
s_addc_u32 s45,s13,0
s_and_b32 s45,s45,0xffff
s_mov_b32 s46,4096
s_mov_b32 s47,0x20000
v_mov_b32 v10,0
buffer_load_dword v9,v10,s[44:47],0 offen sc1
v_lshlrev_b32 v6,4,v23
v_lshlrev_b32 v72,12,v22
v_lshl_add_u32 v72,v21,6,v72
v_lshl_add_u32 v72,v20,4,v72
v_subrev_u32 v51,s89,v72
v_add_u32 v50,131072,v72
v_and_b32 v74,15,v23
v_lshrrev_b32 v76,1,v74
v_lshlrev_b32 v75,7,v74
s_add_u32 s66,s89,65536
v_add_u32 v75,s66,v75
v_or_b32 v77,0,v22
v_xor_b32 v77,v77,v76
v_lshl_add_u32 v48,v77,4,v75
v_or_b32 v77,4,v22
v_xor_b32 v77,v77,v76
v_lshl_add_u32 v49,v77,4,v75
s_add_u32 s86,s89,131072
s_add_u32 s87,s89,98304
s_mov_b64 exec,0
s_cmp_eq_u32 s60,0
s_cbranch_scc1 L_pd1_%=
s_cmp_eq_u32 s60,1
s_cbranch_scc1 L_pd2_%=
s_cmp_eq_u32 s60,2
s_cbranch_scc1 L_pd3_%=
s_branch L_pd4_%=
L_trt0_%=:
s_mov_b64 exec,-1
s_mov_b32 s88,s87
s_add_u32 m0,s88,0
s_nop 0
buffer_load_dwordx4 v40,s[36:39],s83 offen lds
s_add_u32 m0,s88,1024
s_nop 0
buffer_load_dwordx4 v41,s[36:39],s83 offen lds
s_add_u32 m0,s88,2048
s_nop 0
buffer_load_dwordx4 v42,s[36:39],s83 offen lds
s_add_u32 m0,s88,3072
s_nop 0
buffer_load_dwordx4 v43,s[36:39],s83 offen lds
s_add_u32 m0,s88,4096
s_nop 0
buffer_load_dwordx4 v44,s[36:39],s83 offen lds
s_add_u32 m0,s88,5120
s_nop 0
buffer_load_dwordx4 v45,s[36:39],s83 offen lds
s_add_u32 m0,s88,6144
s_nop 0
buffer_load_dwordx4 v46,s[36:39],s83 offen lds
s_add_u32 m0,s88,7168
s_nop 0
buffer_load_dwordx4 v47,s[36:39],s83 offen lds
s_add_u32 s83,s83,524288
s_add_u32 s88,s89,0
s_mov_b64 exec,0
s_cmp_eq_u32 s60,0
s_cbranch_scc1 L_pd9_%=
s_cmp_eq_u32 s60,1
s_cbranch_scc1 L_pd10_%=
s_cmp_eq_u32 s60,2
s_cbranch_scc1 L_pd11_%=
s_branch L_pd12_%=
L_t1b_%=:
s_cmp_ge_u32 s62,8
s_cbranch_scc1 L_pd19_%=
s_branch L_pd14_%=
L_t1c_%=:
s_cmp_ge_u32 s62,8
s_cbranch_scc1 L_pd20_%=
s_branch L_pd15_%=
s_nop 0
s_nop 0
s_nop 0
s_nop 0
s_nop 0
s_nop 0
L_trt1_%=:
s_mov_b64 exec,-1
s_add_u32 m0,s85,0
s_nop 0
buffer_load_dwordx4 v32,s[28:31],s84 offen lds
buffer_load_dwordx4 v33,s[28:31],s84 offen offset:1024 lds
buffer_load_dwordx4 v34,s[28:31],s84 offen offset:2048 lds
buffer_load_dwordx4 v35,s[28:31],s84 offen offset:3072 lds
s_add_u32 m0,s85,4096
s_nop 0
buffer_load_dwordx4 v36,s[28:31],s84 offen lds
buffer_load_dwordx4 v37,s[28:31],s84 offen offset:1024 lds
buffer_load_dwordx4 v38,s[28:31],s84 offen offset:2048 lds
buffer_load_dwordx4 v39,s[28:31],s84 offen offset:3072 lds
s_add_u32 s84,s84,128
s_xor_b32 s85,s85,98304
s_branch L_pacx_%=
L_pace_%=:
s_sub_u32 s5,s4,1
s_max_i32 s5,s5,s7
L_pacl_%=:
s_sub_u32 s4,s4,1
s_cmp_eq_u32 s4,0
s_cbranch_scc1 L_pace2_%=
s_nop 2
s_branch L_pacl_%=
L_pace2_%=:
s_setpc_b64 s[92:93]
L_pacx_%=:
s_waitcnt vmcnt(25)
ds_read_b128 a[192:195],v48
ds_read_b128 a[196:199],v49
ds_read_b128 a[200:203],v48 offset:2048
ds_read_b128 a[204:207],v49 offset:2048
ds_read_b128 a[208:211],v48 offset:4096
ds_read_b128 a[212:215],v49 offset:4096
ds_read_b128 a[216:219],v48 offset:6144
ds_read_b128 a[220:223],v49 offset:6144
s_waitcnt vmcnt(17)
s_barrier
ds_read_b64_tr_b16 a[64:65],v50
ds_read_b64_tr_b16 a[66:67],v50 offset:2048
ds_read_b64_tr_b16 a[68:69],v50 offset:16384
ds_read_b64_tr_b16 a[70:71],v50 offset:18432
ds_read_b64_tr_b16 a[72:73],v50 offset:8
ds_read_b64_tr_b16 a[74:75],v50 offset:2056
ds_read_b64_tr_b16 a[76:77],v50 offset:16392
ds_read_b64_tr_b16 a[78:79],v50 offset:18440
s_waitcnt lgkmcnt(6)
v_mfma_f32_16x16x32_bf16 a[0:3],a[64:67],a[192:195],0
s_add_u32 m0,s88,0
ds_read_b64_tr_b16 a[80:81],v50 offset:256
v_xor_b32 v48,98304,v48
s_waitcnt lgkmcnt(5)
v_mfma_f32_16x16x32_bf16 a[0:3],a[68:71],a[196:199],a[0:3]
buffer_load_dwordx4 v40,s[36:39],s83 offen lds
v_mfma_f32_16x16x32_bf16 a[16:19],a[68:71],a[204:207],0
s_mov_b32 s69,s86
ds_read_b64_tr_b16 a[82:83],v50 offset:2304
v_xor_b32 v49,98304,v49
v_mfma_f32_16x16x32_bf16 a[16:19],a[64:67],a[200:203],a[16:19]
s_mov_b32 s86,s87
s_mov_b32 s87,s88
ds_read_b64_tr_b16 a[84:85],v50 offset:16640
v_mfma_f32_16x16x32_bf16 a[32:35],a[64:67],a[208:211],0
ds_read_b64_tr_b16 a[86:87],v50 offset:18688
v_mfma_f32_16x16x32_bf16 a[32:35],a[68:71],a[212:215],a[32:35]
ds_read_b64_tr_b16 a[88:89],v50 offset:264
v_mfma_f32_16x16x32_bf16 a[48:51],a[68:71],a[220:223],0
s_add_u32 m0,s88,1024
v_mfma_f32_16x16x32_bf16 a[48:51],a[64:67],a[216:219],a[48:51]
s_waitcnt lgkmcnt(5)
ds_read_b64_tr_b16 a[90:91],v50 offset:2312
v_mfma_f32_16x16x32_bf16 a[4:7],a[72:75],a[192:195],0
buffer_load_dwordx4 v41,s[36:39],s83 offen lds
v_mfma_f32_16x16x32_bf16 a[4:7],a[76:79],a[196:199],a[4:7]
s_lshl_b32 s67,s60,12
ds_read_b64_tr_b16 a[92:93],v50 offset:16648
v_cmp_lt_u32 vcc,6,v23
v_mfma_f32_16x16x32_bf16 a[20:23],a[76:79],a[204:207],0
s_lshl_b32 s65,s61,5
ds_read_b64_tr_b16 a[94:95],v50 offset:18696
v_lshlrev_b32 v19,3,v6
v_mfma_f32_16x16x32_bf16 a[20:23],a[72:75],a[200:203],a[20:23]
s_lshl_b32 s66,s62,1
ds_read_b64_tr_b16 a[96:97],v50 offset:512
v_cndmask_b32_e64 v19,v19,-1,vcc
v_mfma_f32_16x16x32_bf16 a[36:39],a[72:75],a[208:211],0
s_add_u32 s65,s65,s66
ds_read_b64_tr_b16 a[98:99],v50 offset:2560
v_lshrrev_b32 v18,1,v19
v_mfma_f32_16x16x32_bf16 a[36:39],a[76:79],a[212:215],a[36:39]
s_add_u32 m0,s88,2048
v_min_u32 v19,v19,v19
v_cndmask_b32_e64 v19,v19,v18,vcc
v_mfma_f32_16x16x32_bf16 a[52:55],a[76:79],a[220:223],0
s_lshl_b32 s66,s65,18
ds_read_b64_tr_b16 a[100:101],v50 offset:16896
v_lshlrev_b32 v11,8,v21
v_mfma_f32_16x16x32_bf16 a[52:55],a[72:75],a[216:219],a[52:55]
buffer_load_dwordx4 v42,s[36:39],s83 offen lds
s_waitcnt lgkmcnt(5)
v_mfma_f32_16x16x32_bf16 a[8:11],a[80:83],a[192:195],0
s_lshr_b32 s63,s62,2
ds_read_b64_tr_b16 a[102:103],v50 offset:18944
v_lshl_add_u32 v11,v22,6,v11
v_mfma_f32_16x16x32_bf16 a[8:11],a[84:87],a[196:199],a[8:11]
s_mov_b32 s22,524288
ds_read_b64_tr_b16 a[104:105],v50 offset:520
v_add_u32 v11,s67,v11
v_mfma_f32_16x16x32_bf16 a[24:27],a[84:87],a[204:207],0
s_mov_b32 s23,0x20000
ds_read_b64_tr_b16 a[106:107],v50 offset:2568
v_lshl_add_u32 v4,v20,4,v11
v_mfma_f32_16x16x32_bf16 a[24:27],a[80:83],a[200:203],a[24:27]
s_mov_b32 s26,524288
ds_read_b64_tr_b16 a[108:109],v50 offset:16904
v_xor_b32 v12,2,v20
v_mfma_f32_16x16x32_bf16 a[40:43],a[80:83],a[208:211],0
s_add_u32 m0,s88,3072
v_lshl_add_u32 v5,v12,4,v11
v_lshlrev_b32 v11,10,v21
v_mfma_f32_16x16x32_bf16 a[40:43],a[84:87],a[212:215],a[40:43]
s_mov_b32 s27,0x20000
ds_read_b64_tr_b16 a[110:111],v50 offset:18952
v_lshl_add_u32 v11,v20,6,v11
v_mfma_f32_16x16x32_bf16 a[56:59],a[84:87],a[220:223],0
buffer_load_dwordx4 v43,s[36:39],s83 offen lds
v_mfma_f32_16x16x32_bf16 a[56:59],a[80:83],a[216:219],a[56:59]
s_mov_b32 s42,131072
ds_read_b64_tr_b16 a[112:113],v50 offset:768
v_and_b32 v12,2,v21
v_mfma_f32_16x16x32_bf16 a[12:15],a[88:91],a[192:195],0
s_mov_b32 s43,0x20000
ds_read_b64_tr_b16 a[114:115],v50 offset:2816
v_xor_b32 v12,v12,v22
s_waitcnt lgkmcnt(6)
v_mfma_f32_16x16x32_bf16 a[12:15],a[92:95],a[196:199],a[12:15]
s_mov_b32 s48,s14
ds_read_b64_tr_b16 a[116:117],v50 offset:17152
v_lshl_add_u32 v11,v12,4,v11
v_mfma_f32_16x16x32_bf16 a[28:31],a[92:95],a[204:207],0
s_and_b32 s49,s15,0xffff
ds_read_b64_tr_b16 a[118:119],v50 offset:19200
v_add_u32 v1,98304,v11
v_mfma_f32_16x16x32_bf16 a[28:31],a[88:91],a[200:203],a[28:31]
s_add_u32 m0,s88,4096
v_lshlrev_b32 v11,10,v22
v_lshl_add_u32 v11,v21,6,v11
v_mfma_f32_16x16x32_bf16 a[44:47],a[88:91],a[208:211],0
s_mov_b32 s50,5242880
ds_read_b64_tr_b16 a[120:121],v50 offset:776
v_lshl_add_u32 v11,v20,3,v11
v_mfma_f32_16x16x32_bf16 a[44:47],a[92:95],a[212:215],a[44:47]
buffer_load_dwordx4 v44,s[36:39],s83 offen lds
v_mfma_f32_16x16x32_bf16 a[60:63],a[92:95],a[220:223],0
s_mov_b32 s51,0x20000
ds_read_b64_tr_b16 a[122:123],v50 offset:2824
v_add_u32 v11,114688,v11
v_mfma_f32_16x16x32_bf16 a[60:63],a[88:91],a[216:219],a[60:63]
s_add_u32 s76,s67,98304
ds_read_b64_tr_b16 a[124:125],v50 offset:17160
v_and_b32 v12,1,v22
v_mfma_f32_16x16x32_bf16 v[64:67],a[96:99],a[192:195],0
s_mul_i32 s74,s61,5
ds_read_b64_tr_b16 a[126:127],v50 offset:19208
v_lshl_add_u32 v2,v12,5,v11
v_mfma_f32_16x16x32_bf16 v[64:67],a[100:103],a[196:199],v[64:67]
s_add_u32 s74,s74,s62
ds_read_b64_tr_b16 a[64:65],v50 offset:1024
v_xor_b32 v12,1,v12
v_mfma_f32_16x16x32_bf16 v[80:83],a[100:103],a[204:207],0
s_add_u32 m0,s88,5120
v_lshl_add_u32 v3,v12,5,v11
v_add_u32 v11,0,v23
v_mfma_f32_16x16x32_bf16 v[80:83],a[96:99],a[200:203],v[80:83]
s_and_b32 s74,s74,15
ds_read_b64_tr_b16 a[66:67],v50 offset:3072
v_mul_u32_u24 v12,3277,v11
v_mfma_f32_16x16x32_bf16 v[96:99],a[96:99],a[208:211],0
buffer_load_dwordx4 v45,s[36:39],s83 offen lds
v_mfma_f32_16x16x32_bf16 v[96:99],a[100:103],a[212:215],v[96:99]
s_lshl_b32 s74,s74,14
ds_read_b64_tr_b16 a[68:69],v50 offset:17408
v_lshrrev_b32 v12,16,v12
v_mfma_f32_16x16x32_bf16 v[112:115],a[100:103],a[220:223],0
s_mov_b32 s75,0
ds_read_b64_tr_b16 a[70:71],v50 offset:19456
v_mul_u32_u24 v13,20,v12
v_mfma_f32_16x16x32_bf16 v[112:115],a[96:99],a[216:219],v[112:115]
s_mov_b32 s77,98304
)A"
R"A(ds_read_b64_tr_b16 a[72:73],v50 offset:1032
v_sub_u32 v13,v11,v13
s_waitcnt lgkmcnt(6)
v_mfma_f32_16x16x32_bf16 v[68:71],a[104:107],a[192:195],0
s_mov_b32 s71,0x3e0293ee
ds_read_b64_tr_b16 a[74:75],v50 offset:3080
v_bfe_u32 v14,v12,2,1
v_mfma_f32_16x16x32_bf16 v[68:71],a[108:111],a[196:199],v[68:71]
s_add_u32 m0,s88,6144
v_lshlrev_b32 v14,1,v14
v_xor_b32 v13,v13,v14
v_mfma_f32_16x16x32_bf16 v[84:87],a[108:111],a[204:207],0
s_mov_b32 s73,0x53800000
ds_read_b64_tr_b16 a[76:77],v50 offset:17416
v_lshrrev_b32 v14,2,v13
v_mfma_f32_16x16x32_bf16 v[84:87],a[104:107],a[200:203],v[84:87]
buffer_load_dwordx4 v46,s[36:39],s83 offen lds
v_mfma_f32_16x16x32_bf16 v[100:103],a[104:107],a[208:211],0
s_lshl_b32 s79,s60,14
ds_read_b64_tr_b16 a[78:79],v50 offset:19464
v_cmp_eq_u32 vcc,3,v14
v_mfma_f32_16x16x32_bf16 v[100:103],a[108:111],a[212:215],v[100:103]
s_add_u32 s20,s6,s66
s_addc_u32 s21,s7,0
ds_read_b64_tr_b16 a[80:81],v50 offset:1280
v_mfma_f32_16x16x32_bf16 v[116:119],a[108:111],a[220:223],0
s_add_u32 s24,s8,s66
s_addc_u32 s25,s9,0
ds_read_b64_tr_b16 a[82:83],v50 offset:3328
v_mfma_f32_16x16x32_bf16 v[116:119],a[104:107],a[216:219],v[116:119]
s_lshl_b32 s66,s65,16
s_add_u32 s65,s63,1
ds_read_b64_tr_b16 a[84:85],v50 offset:17664
v_mfma_f32_16x16x32_bf16 v[72:75],a[112:115],a[192:195],0
s_add_u32 m0,s88,7168
s_add_u32 s40,s12,s66
s_addc_u32 s41,s13,0
v_mfma_f32_16x16x32_bf16 v[72:75],a[116:119],a[196:199],v[72:75]
s_mul_i32 s66,s60,40960
ds_read_b64_tr_b16 a[86:87],v50 offset:19712
v_min_u32 v15,3,v14
v_mfma_f32_16x16x32_bf16 v[88:91],a[116:119],a[204:207],0
buffer_load_dwordx4 v47,s[36:39],s83 offen lds
v_mfma_f32_16x16x32_bf16 v[88:91],a[112:115],a[200:203],v[88:91]
s_add_u32 s83,s83,524288
ds_read_b64_tr_b16 a[88:89],v50 offset:1288
v_add_u32 v15,s65,v15
v_mfma_f32_16x16x32_bf16 v[104:107],a[112:115],a[208:211],0
s_mov_b32 s88,s69
ds_read_b64_tr_b16 a[90:91],v50 offset:3336
v_and_b32 v15,3,v15
v_mfma_f32_16x16x32_bf16 v[104:107],a[116:119],a[212:215],v[104:107]
s_and_b32 s21,s21,0xffff
ds_read_b64_tr_b16 a[92:93],v50 offset:17672
v_cndmask_b32_e64 v15,v15,4,vcc
v_mfma_f32_16x16x32_bf16 v[120:123],a[116:119],a[220:223],0
s_and_b32 s25,s25,0xffff
ds_read_b64_tr_b16 a[94:95],v50 offset:19720
v_and_b32 v13,3,v13
v_mfma_f32_16x16x32_bf16 v[120:123],a[112:115],a[216:219],v[120:123]
s_add_u32 m0,s85,0
v_lshl_add_u32 v13,v15,2,v13
v_mul_u32_u24 v12,1280,v12
v_mfma_f32_16x16x32_bf16 v[76:79],a[120:123],a[192:195],0
s_and_b32 s41,s41,0xffff
ds_read_b64_tr_b16 a[96:97],v50 offset:1536
v_lshl_add_u32 v12,v13,4,v12
s_waitcnt lgkmcnt(6)
v_mfma_f32_16x16x32_bf16 v[76:79],a[124:127],a[196:199],v[76:79]
buffer_load_dwordx4 v32,s[28:31],s84 offen lds
v_mfma_f32_16x16x32_bf16 v[92:95],a[124:127],a[204:207],0
s_add_u32 s80,s79,4096
ds_read_b64_tr_b16 a[98:99],v50 offset:3584
v_add_u32 v12,s66,v12
v_mfma_f32_16x16x32_bf16 v[92:95],a[120:123],a[200:203],v[92:95]
s_add_u32 s81,s79,8192
ds_read_b64_tr_b16 a[100:101],v50 offset:17920
v_subrev_u32 v24,0,v12
v_mfma_f32_16x16x32_bf16 v[108:111],a[120:123],a[208:211],0
s_add_u32 s82,s79,12288
ds_read_b64_tr_b16 a[102:103],v50 offset:19968
v_add_u32 v11,64,v23
v_mfma_f32_16x16x32_bf16 v[108:111],a[124:127],a[212:215],v[108:111]
s_and_b32 s64,s62,3
ds_read_b64_tr_b16 a[104:105],v50 offset:1544
v_mul_u32_u24 v12,3277,v11
v_mfma_f32_16x16x32_bf16 v[124:127],a[124:127],a[220:223],0
s_mov_b32 s72,0xff800000
v_lshrrev_b32 v12,16,v12
v_mul_u32_u24 v13,20,v12
v_mfma_f32_16x16x32_bf16 v[124:127],a[120:123],a[216:219],v[124:127]
s_lshl_b32 s18,s60,24
ds_read_b64_tr_b16 a[106:107],v50 offset:3592
v_sub_u32 v13,v11,v13
v_mfma_f32_16x16x32_bf16 v[128:131],a[64:67],a[192:195],0
buffer_load_dwordx4 v33,s[28:31],s84 offen offset:1024 lds
v_mfma_f32_16x16x32_bf16 v[128:131],a[68:71],a[196:199],v[128:131]
s_lshl_b32 s68,s62,6
ds_read_b64_tr_b16 a[108:109],v50 offset:17928
v_bfe_u32 v14,v12,2,1
v_mfma_f32_16x16x32_bf16 v[160:163],a[68:71],a[204:207],0
s_add_u32 s18,s18,s68
ds_read_b64_tr_b16 a[110:111],v50 offset:19976
v_lshlrev_b32 v14,1,v14
v_mfma_f32_16x16x32_bf16 v[160:163],a[64:67],a[200:203],v[160:163]
s_lshl_b32 s90,%1,2
ds_read_b64_tr_b16 a[112:113],v50 offset:1792
v_xor_b32 v13,v13,v14
v_mfma_f32_16x16x32_bf16 v[192:195],a[64:67],a[208:211],0
s_add_u32 s90,s90,s60
ds_read_b64_tr_b16 a[114:115],v50 offset:3840
v_lshrrev_b32 v14,2,v13
v_mfma_f32_16x16x32_bf16 v[192:195],a[68:71],a[212:215],v[192:195]
s_mul_i32 s90,s90,5120
v_cmp_eq_u32 vcc,3,v14
v_min_u32 v15,3,v14
v_mfma_f32_16x16x32_bf16 v[224:227],a[68:71],a[220:223],0
ds_read_b64_tr_b16 a[116:117],v50 offset:18176
v_add_u32 v15,s65,v15
v_mfma_f32_16x16x32_bf16 v[224:227],a[64:67],a[216:219],v[224:227]
buffer_load_dwordx4 v34,s[28:31],s84 offen offset:2048 lds
v_mfma_f32_16x16x32_bf16 v[132:135],a[72:75],a[192:195],0
ds_read_b64_tr_b16 a[118:119],v50 offset:20224
v_and_b32 v15,3,v15
v_mfma_f32_16x16x32_bf16 v[132:135],a[76:79],a[196:199],v[132:135]
ds_read_b64_tr_b16 a[120:121],v50 offset:1800
v_cndmask_b32_e64 v15,v15,4,vcc
v_mfma_f32_16x16x32_bf16 v[164:167],a[76:79],a[204:207],0
ds_read_b64_tr_b16 a[122:123],v50 offset:3848
v_and_b32 v13,3,v13
v_mfma_f32_16x16x32_bf16 v[164:167],a[72:75],a[200:203],v[164:167]
ds_read_b64_tr_b16 a[124:125],v50 offset:18184
v_lshl_add_u32 v13,v15,2,v13
v_mfma_f32_16x16x32_bf16 v[196:199],a[72:75],a[208:211],0
v_mul_u32_u24 v12,1280,v12
v_lshl_add_u32 v12,v13,4,v12
v_mfma_f32_16x16x32_bf16 v[196:199],a[76:79],a[212:215],v[196:199]
ds_read_b64_tr_b16 a[126:127],v50 offset:20232
v_add_u32 v50,s86,v51
v_mfma_f32_16x16x32_bf16 v[228:231],a[76:79],a[220:223],0
buffer_load_dwordx4 v35,s[28:31],s84 offen offset:3072 lds
v_mfma_f32_16x16x32_bf16 v[228:231],a[72:75],a[216:219],v[228:231]
v_add_u32 v12,s66,v12
v_subrev_u32 v25,1024,v12
v_mfma_f32_16x16x32_bf16 v[136:139],a[80:83],a[192:195],0
s_waitcnt lgkmcnt(4)
v_add_u32 v11,128,v23
v_mul_u32_u24 v12,3277,v11
v_mfma_f32_16x16x32_bf16 v[136:139],a[84:87],a[196:199],v[136:139]
v_lshrrev_b32 v12,16,v12
v_mul_u32_u24 v13,20,v12
v_mfma_f32_16x16x32_bf16 v[168:171],a[84:87],a[204:207],0
v_sub_u32 v13,v11,v13
v_bfe_u32 v14,v12,2,1
v_mfma_f32_16x16x32_bf16 v[168:171],a[80:83],a[200:203],v[168:171]
s_add_u32 m0,s85,4096
s_xor_b32 s85,s85,98304
v_lshlrev_b32 v14,1,v14
v_mfma_f32_16x16x32_bf16 v[200:203],a[80:83],a[208:211],0
v_xor_b32 v13,v13,v14
v_lshrrev_b32 v14,2,v13
v_mfma_f32_16x16x32_bf16 v[200:203],a[84:87],a[212:215],v[200:203]
buffer_load_dwordx4 v36,s[28:31],s84 offen lds
v_mfma_f32_16x16x32_bf16 v[232:235],a[84:87],a[220:223],0
v_cmp_eq_u32 vcc,3,v14
v_min_u32 v15,3,v14
v_mfma_f32_16x16x32_bf16 v[232:235],a[80:83],a[216:219],v[232:235]
v_add_u32 v15,s65,v15
v_and_b32 v15,3,v15
v_mfma_f32_16x16x32_bf16 v[140:143],a[88:91],a[192:195],0
v_cndmask_b32_e64 v15,v15,4,vcc
v_and_b32 v13,3,v13
v_mfma_f32_16x16x32_bf16 v[140:143],a[92:95],a[196:199],v[140:143]
v_lshl_add_u32 v13,v15,2,v13
v_mul_u32_u24 v12,1280,v12
v_mfma_f32_16x16x32_bf16 v[172:175],a[92:95],a[204:207],0
v_lshl_add_u32 v12,v13,4,v12
v_add_u32 v12,s66,v12
v_mfma_f32_16x16x32_bf16 v[172:175],a[88:91],a[200:203],v[172:175]
v_subrev_u32 v26,2048,v12
v_add_u32 v11,192,v23
v_mfma_f32_16x16x32_bf16 v[204:207],a[88:91],a[208:211],0
buffer_load_dwordx4 v37,s[28:31],s84 offen offset:1024 lds
v_mfma_f32_16x16x32_bf16 v[204:207],a[92:95],a[212:215],v[204:207]
v_mul_u32_u24 v12,3277,v11
v_lshrrev_b32 v12,16,v12
v_mfma_f32_16x16x32_bf16 v[236:239],a[92:95],a[220:223],0
v_mul_u32_u24 v13,20,v12
v_sub_u32 v13,v11,v13
v_mfma_f32_16x16x32_bf16 v[236:239],a[88:91],a[216:219],v[236:239]
v_bfe_u32 v14,v12,2,1
v_lshlrev_b32 v14,1,v14
s_waitcnt vmcnt(22) lgkmcnt(0)
s_barrier
v_mfma_f32_16x16x32_bf16 v[144:147],a[96:99],a[192:195],0
s_waitcnt vmcnt(14)
ds_read_b128 a[224:227],v48
v_xor_b32 v13,v13,v14
v_mfma_f32_16x16x32_bf16 v[144:147],a[100:103],a[196:199],v[144:147]
ds_read_b128 a[228:231],v49
v_lshrrev_b32 v14,2,v13
v_mfma_f32_16x16x32_bf16 v[176:179],a[100:103],a[204:207],0
buffer_load_dwordx4 v38,s[28:31],s84 offen offset:2048 lds
v_mfma_f32_16x16x32_bf16 v[176:179],a[96:99],a[200:203],v[176:179]
ds_read_b128 a[232:235],v48 offset:2048
v_cmp_eq_u32 vcc,3,v14
v_mfma_f32_16x16x32_bf16 v[208:211],a[96:99],a[208:211],0
ds_read_b128 a[236:239],v49 offset:2048
v_min_u32 v15,3,v14
v_mfma_f32_16x16x32_bf16 v[208:211],a[100:103],a[212:215],v[208:211]
ds_read_b128 a[240:243],v48 offset:4096
v_add_u32 v15,s65,v15
v_mfma_f32_16x16x32_bf16 v[240:243],a[100:103],a[220:223],0
ds_read_b128 a[244:247],v49 offset:4096
v_and_b32 v15,3,v15
v_mfma_f32_16x16x32_bf16 v[240:243],a[96:99],a[216:219],v[240:243]
v_cndmask_b32_e64 v15,v15,4,vcc
v_and_b32 v13,3,v13
v_mfma_f32_16x16x32_bf16 v[148:151],a[104:107],a[192:195],0
ds_read_b128 a[248:251],v48 offset:6144
v_lshl_add_u32 v13,v15,2,v13
v_mfma_f32_16x16x32_bf16 v[148:151],a[108:111],a[196:199],v[148:151]
buffer_load_dwordx4 v39,s[28:31],s84 offen offset:3072 lds
v_mfma_f32_16x16x32_bf16 v[180:183],a[108:111],a[204:207],0
s_add_u32 s84,s84,128
ds_read_b128 a[252:255],v49 offset:6144
v_mul_u32_u24 v12,1280,v12
v_mfma_f32_16x16x32_bf16 v[180:183],a[104:107],a[200:203],v[180:183]
ds_read_b64_tr_b16 a[64:65],v50
v_lshl_add_u32 v12,v13,4,v12
v_mfma_f32_16x16x32_bf16 v[212:215],a[104:107],a[208:211],0
ds_read_b64_tr_b16 a[66:67],v50 offset:2048
v_add_u32 v12,s66,v12
v_mfma_f32_16x16x32_bf16 v[212:215],a[108:111],a[212:215],v[212:215]
ds_read_b64_tr_b16 a[68:69],v50 offset:16384
v_subrev_u32 v27,3072,v12
s_nop 0
L_pd1_%=:
s_cbranch_execz L_pd5_%=
v_mfma_f32_16x16x32_bf16 v[244:247],a[108:111],a[220:223],0
v_add_u32 v11,256,v23
v_mul_u32_u24 v12,3277,v11
v_mfma_f32_16x16x32_bf16 v[244:247],a[104:107],a[216:219],v[244:247]
ds_read_b64_tr_b16 a[70:71],v50 offset:18432
v_lshrrev_b32 v12,16,v12
v_mfma_f32_16x16x32_bf16 v[152:155],a[112:115],a[192:195],0
ds_read_b64_tr_b16 a[72:73],v50 offset:8
v_mul_u32_u24 v13,20,v12
v_mfma_f32_16x16x32_bf16 v[152:155],a[116:119],a[196:199],v[152:155]
ds_read_b64_tr_b16 a[74:75],v50 offset:2056
v_sub_u32 v13,v11,v13
v_mfma_f32_16x16x32_bf16 v[184:187],a[116:119],a[204:207],0
v_bfe_u32 v14,v12,2,1
v_lshlrev_b32 v14,1,v14
v_mfma_f32_16x16x32_bf16 v[184:187],a[112:115],a[200:203],v[184:187]
ds_read_b64_tr_b16 a[76:77],v50 offset:16392
v_xor_b32 v13,v13,v14
v_mfma_f32_16x16x32_bf16 v[216:219],a[112:115],a[208:211],0
ds_read_b64_tr_b16 a[78:79],v50 offset:18440
v_lshrrev_b32 v14,2,v13
v_mfma_f32_16x16x32_bf16 v[216:219],a[116:119],a[212:215],v[216:219]
ds_read_b64_tr_b16 a[80:81],v50 offset:256
v_cmp_eq_u32 vcc,3,v14
v_mfma_f32_16x16x32_bf16 v[248:251],a[116:119],a[220:223],0
s_cmp_eq_u32 s60,3
s_cselect_b32 s72,0x7149f2ca,s72
v_readfirstlane_b32 s52,v9
v_mfma_f32_16x16x32_bf16 v[248:251],a[112:115],a[216:219],v[248:251]
s_add_u32 s52,s52,1
s_xor_b32 s53,s52,0x9e3779b9
ds_read_b64_tr_b16 a[82:83],v50 offset:2304
v_mfma_f32_16x16x32_bf16 v[156:159],a[120:123],a[192:195],0
ds_read_b64_tr_b16 a[84:85],v50 offset:16640
v_mov_b32 v28,0x3f803f80
v_mfma_f32_16x16x32_bf16 v[156:159],a[124:127],a[196:199],v[156:159]
ds_read_b64_tr_b16 a[86:87],v50 offset:18688
v_mov_b32 v29,0x3f803f80
v_mfma_f32_16x16x32_bf16 v[188:191],a[124:127],a[204:207],0
v_mov_b32 v30,0x3f803f80
v_mov_b32 v31,0x3f803f80
v_mfma_f32_16x16x32_bf16 v[188:191],a[120:123],a[200:203],v[188:191]
ds_read_b64_tr_b16 a[88:89],v50 offset:264
v_min_u32 v15,3,v14
v_mfma_f32_16x16x32_bf16 v[220:223],a[120:123],a[208:211],0
ds_read_b64_tr_b16 a[90:91],v50 offset:2312
v_add_u32 v15,s65,v15
v_mfma_f32_16x16x32_bf16 v[220:223],a[124:127],a[212:215],v[220:223]
ds_read_b64_tr_b16 a[92:93],v50 offset:16648
v_and_b32 v15,3,v15
v_mfma_f32_16x16x32_bf16 v[252:255],a[124:127],a[220:223],0
v_cndmask_b32_e64 v15,v15,4,vcc
v_and_b32 v13,3,v13
v_mfma_f32_16x16x32_bf16 v[252:255],a[120:123],a[216:219],v[252:255]
ds_read_b64_tr_b16 a[94:95],v50 offset:18696
v_lshl_add_u32 v13,v15,2,v13
v_mul_u32_u24 v12,1280,v12
v_lshl_add_u32 v12,v13,4,v12
v_add_u32 v12,s66,v12
v_subrev_u32 v7,0,v12
s_mov_b32 s70,3
s_memrealtime s[8:9]
s_memtime s[4:5]
L_qp_%=:
s_waitcnt lgkmcnt(6)
v_mfma_f32_16x16x32_bf16 a[0:3],a[64:67],a[224:227],a[0:3]
s_add_u32 m0,s88,0
)A"
R"A(ds_read_b64_tr_b16 a[96:97],v50 offset:512
v_xor_b32 v48,98304,v48
v_mfma_f32_16x16x32_bf16 a[0:3],a[68:71],a[228:231],a[0:3]
buffer_load_dwordx4 v40,s[36:39],s83 offen lds
v_mfma_f32_16x16x32_bf16 a[16:19],a[68:71],a[236:239],a[16:19]
s_mov_b32 s69,s86
ds_read_b64_tr_b16 a[98:99],v50 offset:2560
v_xor_b32 v49,98304,v49
v_mfma_f32_16x16x32_bf16 a[16:19],a[64:67],a[232:235],a[16:19]
s_mov_b32 s86,s87
s_mov_b32 s87,s88
ds_read_b64_tr_b16 a[100:101],v50 offset:16896
v_mfma_f32_16x16x32_bf16 a[32:35],a[64:67],a[240:243],a[32:35]
ds_read_b64_tr_b16 a[102:103],v50 offset:18944
v_mfma_f32_16x16x32_bf16 a[32:35],a[68:71],a[244:247],a[32:35]
ds_read_b64_tr_b16 a[104:105],v50 offset:520
v_mfma_f32_16x16x32_bf16 a[48:51],a[68:71],a[252:255],a[48:51]
s_add_u32 m0,s88,1024
v_mfma_f32_16x16x32_bf16 a[48:51],a[64:67],a[248:251],a[48:51]
ds_read_b64_tr_b16 a[106:107],v50 offset:2568
v_mfma_f32_16x16x32_bf16 a[4:7],a[72:75],a[224:227],a[4:7]
buffer_load_dwordx4 v41,s[36:39],s83 offen lds
v_mfma_f32_16x16x32_bf16 a[4:7],a[76:79],a[228:231],a[4:7]
ds_read_b64_tr_b16 a[108:109],v50 offset:16904
v_mfma_f32_16x16x32_bf16 a[20:23],a[76:79],a[236:239],a[20:23]
ds_read_b64_tr_b16 a[110:111],v50 offset:18952
v_mfma_f32_16x16x32_bf16 a[20:23],a[72:75],a[232:235],a[20:23]
ds_read_b64_tr_b16 a[112:113],v50 offset:768
v_mfma_f32_16x16x32_bf16 a[36:39],a[72:75],a[240:243],a[36:39]
ds_read_b64_tr_b16 a[114:115],v50 offset:2816
v_mfma_f32_16x16x32_bf16 a[36:39],a[76:79],a[244:247],a[36:39]
s_add_u32 m0,s88,2048
v_mfma_f32_16x16x32_bf16 a[52:55],a[76:79],a[252:255],a[52:55]
ds_read_b64_tr_b16 a[116:117],v50 offset:17152
v_mfma_f32_16x16x32_bf16 a[52:55],a[72:75],a[248:251],a[52:55]
buffer_load_dwordx4 v42,s[36:39],s83 offen lds
v_mfma_f32_16x16x32_bf16 a[8:11],a[80:83],a[224:227],a[8:11]
s_waitcnt lgkmcnt(5)
ds_read_b64_tr_b16 a[118:119],v50 offset:19200
v_mfma_f32_16x16x32_bf16 a[8:11],a[84:87],a[228:231],a[8:11]
ds_read_b64_tr_b16 a[120:121],v50 offset:776
v_mfma_f32_16x16x32_bf16 a[24:27],a[84:87],a[236:239],a[24:27]
ds_read_b64_tr_b16 a[122:123],v50 offset:2824
v_mfma_f32_16x16x32_bf16 a[24:27],a[80:83],a[232:235],a[24:27]
ds_read_b64_tr_b16 a[124:125],v50 offset:17160
v_mfma_f32_16x16x32_bf16 a[40:43],a[80:83],a[240:243],a[40:43]
s_add_u32 m0,s88,3072
v_mfma_f32_16x16x32_bf16 a[40:43],a[84:87],a[244:247],a[40:43]
ds_read_b64_tr_b16 a[126:127],v50 offset:19208
v_mfma_f32_16x16x32_bf16 a[56:59],a[84:87],a[252:255],a[56:59]
buffer_load_dwordx4 v43,s[36:39],s83 offen lds
v_mfma_f32_16x16x32_bf16 a[56:59],a[80:83],a[248:251],a[56:59]
ds_read_b64_tr_b16 a[64:65],v50 offset:1024
v_mfma_f32_16x16x32_bf16 a[12:15],a[88:91],a[224:227],a[12:15]
ds_read_b64_tr_b16 a[66:67],v50 offset:3072
v_mfma_f32_16x16x32_bf16 a[12:15],a[92:95],a[228:231],a[12:15]
ds_read_b64_tr_b16 a[68:69],v50 offset:17408
v_mfma_f32_16x16x32_bf16 a[28:31],a[92:95],a[236:239],a[28:31]
ds_read_b64_tr_b16 a[70:71],v50 offset:19456
v_mfma_f32_16x16x32_bf16 a[28:31],a[88:91],a[232:235],a[28:31]
s_add_u32 m0,s88,4096
v_mfma_f32_16x16x32_bf16 a[44:47],a[88:91],a[240:243],a[44:47]
ds_read_b64_tr_b16 a[72:73],v50 offset:1032
v_mfma_f32_16x16x32_bf16 a[44:47],a[92:95],a[244:247],a[44:47]
buffer_load_dwordx4 v44,s[36:39],s83 offen lds
v_mfma_f32_16x16x32_bf16 a[60:63],a[92:95],a[252:255],a[60:63]
ds_read_b64_tr_b16 a[74:75],v50 offset:3080
v_mfma_f32_16x16x32_bf16 a[60:63],a[88:91],a[248:251],a[60:63]
ds_read_b64_tr_b16 a[76:77],v50 offset:17416
v_mfma_f32_16x16x32_bf16 v[64:67],a[96:99],a[224:227],v[64:67]
ds_read_b64_tr_b16 a[78:79],v50 offset:19464
v_mfma_f32_16x16x32_bf16 v[64:67],a[100:103],a[228:231],v[64:67]
ds_read_b64_tr_b16 a[80:81],v50 offset:1280
v_mfma_f32_16x16x32_bf16 v[80:83],a[100:103],a[236:239],v[80:83]
s_add_u32 m0,s88,5120
v_mfma_f32_16x16x32_bf16 v[80:83],a[96:99],a[232:235],v[80:83]
ds_read_b64_tr_b16 a[82:83],v50 offset:3328
v_mfma_f32_16x16x32_bf16 v[96:99],a[96:99],a[240:243],v[96:99]
buffer_load_dwordx4 v45,s[36:39],s83 offen lds
v_mfma_f32_16x16x32_bf16 v[96:99],a[100:103],a[244:247],v[96:99]
ds_read_b64_tr_b16 a[84:85],v50 offset:17664
v_mfma_f32_16x16x32_bf16 v[112:115],a[100:103],a[252:255],v[112:115]
ds_read_b64_tr_b16 a[86:87],v50 offset:19712
v_mfma_f32_16x16x32_bf16 v[112:115],a[96:99],a[248:251],v[112:115]
ds_read_b64_tr_b16 a[88:89],v50 offset:1288
v_mfma_f32_16x16x32_bf16 v[68:71],a[104:107],a[224:227],v[68:71]
s_waitcnt lgkmcnt(5)
ds_read_b64_tr_b16 a[90:91],v50 offset:3336
v_mfma_f32_16x16x32_bf16 v[68:71],a[108:111],a[228:231],v[68:71]
s_add_u32 m0,s88,6144
v_mfma_f32_16x16x32_bf16 v[84:87],a[108:111],a[236:239],v[84:87]
ds_read_b64_tr_b16 a[92:93],v50 offset:17672
v_mfma_f32_16x16x32_bf16 v[84:87],a[104:107],a[232:235],v[84:87]
buffer_load_dwordx4 v46,s[36:39],s83 offen lds
v_mfma_f32_16x16x32_bf16 v[100:103],a[104:107],a[240:243],v[100:103]
ds_read_b64_tr_b16 a[94:95],v50 offset:19720
v_mfma_f32_16x16x32_bf16 v[100:103],a[108:111],a[244:247],v[100:103]
ds_read_b64_tr_b16 a[96:97],v50 offset:1536
v_mfma_f32_16x16x32_bf16 v[116:119],a[108:111],a[252:255],v[116:119]
ds_read_b64_tr_b16 a[98:99],v50 offset:3584
v_mfma_f32_16x16x32_bf16 v[116:119],a[104:107],a[248:251],v[116:119]
ds_read_b64_tr_b16 a[100:101],v50 offset:17920
v_mfma_f32_16x16x32_bf16 v[72:75],a[112:115],a[224:227],v[72:75]
s_add_u32 m0,s88,7168
s_mov_b32 s88,s69
s_mov_b32 s69,s86
v_mfma_f32_16x16x32_bf16 v[72:75],a[116:119],a[228:231],v[72:75]
ds_read_b64_tr_b16 a[102:103],v50 offset:19968
v_mfma_f32_16x16x32_bf16 v[88:91],a[116:119],a[236:239],v[88:91]
buffer_load_dwordx4 v47,s[36:39],s83 offen lds
v_mfma_f32_16x16x32_bf16 v[88:91],a[112:115],a[232:235],v[88:91]
s_add_u32 s83,s83,524288
ds_read_b64_tr_b16 a[104:105],v50 offset:1544
v_mfma_f32_16x16x32_bf16 v[104:107],a[112:115],a[240:243],v[104:107]
ds_read_b64_tr_b16 a[106:107],v50 offset:3592
v_mfma_f32_16x16x32_bf16 v[104:107],a[116:119],a[244:247],v[104:107]
ds_read_b64_tr_b16 a[108:109],v50 offset:17928
v_mfma_f32_16x16x32_bf16 v[120:123],a[116:119],a[252:255],v[120:123]
ds_read_b64_tr_b16 a[110:111],v50 offset:19976
v_mfma_f32_16x16x32_bf16 v[120:123],a[112:115],a[248:251],v[120:123]
s_add_u32 m0,s85,0
v_mfma_f32_16x16x32_bf16 v[76:79],a[120:123],a[224:227],v[76:79]
ds_read_b64_tr_b16 a[112:113],v50 offset:1792
v_mfma_f32_16x16x32_bf16 v[76:79],a[124:127],a[228:231],v[76:79]
buffer_load_dwordx4 v32,s[28:31],s84 offen lds
v_mfma_f32_16x16x32_bf16 v[92:95],a[124:127],a[236:239],v[92:95]
ds_read_b64_tr_b16 a[114:115],v50 offset:3840
v_mfma_f32_16x16x32_bf16 v[92:95],a[120:123],a[232:235],v[92:95]
ds_read_b64_tr_b16 a[116:117],v50 offset:18176
v_mfma_f32_16x16x32_bf16 v[108:111],a[120:123],a[240:243],v[108:111]
ds_read_b64_tr_b16 a[118:119],v50 offset:20224
v_mfma_f32_16x16x32_bf16 v[108:111],a[124:127],a[244:247],v[108:111]
s_cmp_eq_u32 s70,1
s_cselect_b32 s65,256,0
s_cselect_b32 s66,524288,0
v_mfma_f32_16x16x32_bf16 v[124:127],a[124:127],a[252:255],v[124:127]
ds_read_b64_tr_b16 a[124:125],v50 offset:18184
v_add_u32 v40,s65,v40
v_mfma_f32_16x16x32_bf16 v[124:127],a[120:123],a[248:251],v[124:127]
ds_read_b64_tr_b16 a[120:121],v50 offset:1800
v_add_u32 v41,s66,v41
v_mfma_f32_16x16x32_bf16 v[128:131],a[64:67],a[224:227],v[128:131]
buffer_load_dwordx4 v33,s[28:31],s84 offen offset:1024 lds
v_mfma_f32_16x16x32_bf16 v[128:131],a[68:71],a[228:231],v[128:131]
ds_read_b64_tr_b16 a[122:123],v50 offset:3848
v_add_u32 v42,s65,v42
v_mfma_f32_16x16x32_bf16 v[160:163],a[68:71],a[236:239],v[160:163]
ds_read_b64_tr_b16 a[126:127],v50 offset:20232
v_add_u32 v50,s86,v51
v_mfma_f32_16x16x32_bf16 v[160:163],a[64:67],a[232:235],v[160:163]
s_mov_b32 s86,s87
v_add_u32 v43,s66,v43
v_add_u32 v44,s65,v44
v_mfma_f32_16x16x32_bf16 v[192:195],a[64:67],a[240:243],v[192:195]
s_mov_b32 s87,s88
v_add_u32 v45,s66,v45
v_add_u32 v46,s65,v46
v_mfma_f32_16x16x32_bf16 v[192:195],a[68:71],a[244:247],v[192:195]
v_add_u32 v47,s66,v47
v_mfma_f32_16x16x32_bf16 v[224:227],a[68:71],a[252:255],v[224:227]
v_mfma_f32_16x16x32_bf16 v[224:227],a[64:67],a[248:251],v[224:227]
buffer_load_dwordx4 v34,s[28:31],s84 offen offset:2048 lds
v_mfma_f32_16x16x32_bf16 v[132:135],a[72:75],a[224:227],v[132:135]
v_mfma_f32_16x16x32_bf16 v[132:135],a[76:79],a[228:231],v[132:135]
v_mfma_f32_16x16x32_bf16 v[164:167],a[76:79],a[236:239],v[164:167]
v_mfma_f32_16x16x32_bf16 v[164:167],a[72:75],a[232:235],v[164:167]
v_mfma_f32_16x16x32_bf16 v[196:199],a[72:75],a[240:243],v[196:199]
v_mfma_f32_16x16x32_bf16 v[196:199],a[76:79],a[244:247],v[196:199]
v_mfma_f32_16x16x32_bf16 v[228:231],a[76:79],a[252:255],v[228:231]
buffer_load_dwordx4 v35,s[28:31],s84 offen offset:3072 lds
v_mfma_f32_16x16x32_bf16 v[228:231],a[72:75],a[248:251],v[228:231]
s_waitcnt lgkmcnt(0)
v_mfma_f32_16x16x32_bf16 v[136:139],a[80:83],a[224:227],v[136:139]
v_mfma_f32_16x16x32_bf16 v[136:139],a[84:87],a[228:231],v[136:139]
v_mfma_f32_16x16x32_bf16 v[168:171],a[84:87],a[236:239],v[168:171]
v_mfma_f32_16x16x32_bf16 v[168:171],a[80:83],a[232:235],v[168:171]
s_add_u32 m0,s85,4096
s_xor_b32 s85,s85,98304
v_mfma_f32_16x16x32_bf16 v[200:203],a[80:83],a[240:243],v[200:203]
v_mfma_f32_16x16x32_bf16 v[200:203],a[84:87],a[244:247],v[200:203]
buffer_load_dwordx4 v36,s[28:31],s84 offen lds
v_mfma_f32_16x16x32_bf16 v[232:235],a[84:87],a[252:255],v[232:235]
v_mfma_f32_16x16x32_bf16 v[232:235],a[80:83],a[248:251],v[232:235]
v_mfma_f32_16x16x32_bf16 v[140:143],a[88:91],a[224:227],v[140:143]
v_mfma_f32_16x16x32_bf16 v[140:143],a[92:95],a[228:231],v[140:143]
v_mfma_f32_16x16x32_bf16 v[172:175],a[92:95],a[236:239],v[172:175]
v_mfma_f32_16x16x32_bf16 v[172:175],a[88:91],a[232:235],v[172:175]
v_mfma_f32_16x16x32_bf16 v[204:207],a[88:91],a[240:243],v[204:207]
buffer_load_dwordx4 v37,s[28:31],s84 offen offset:1024 lds
v_mfma_f32_16x16x32_bf16 v[204:207],a[92:95],a[244:247],v[204:207]
v_mfma_f32_16x16x32_bf16 v[236:239],a[92:95],a[252:255],v[236:239]
v_mfma_f32_16x16x32_bf16 v[236:239],a[88:91],a[248:251],v[236:239]
s_waitcnt vmcnt(22)
s_barrier
v_mfma_f32_16x16x32_bf16 v[144:147],a[96:99],a[224:227],v[144:147]
s_waitcnt vmcnt(14)
ds_read_b128 a[192:195],v48
v_mfma_f32_16x16x32_bf16 v[144:147],a[100:103],a[228:231],v[144:147]
ds_read_b128 a[196:199],v49
v_mfma_f32_16x16x32_bf16 v[176:179],a[100:103],a[236:239],v[176:179]
buffer_load_dwordx4 v38,s[28:31],s84 offen offset:2048 lds
v_mfma_f32_16x16x32_bf16 v[176:179],a[96:99],a[232:235],v[176:179]
ds_read_b128 a[200:203],v48 offset:2048
v_mfma_f32_16x16x32_bf16 v[208:211],a[96:99],a[240:243],v[208:211]
ds_read_b128 a[204:207],v49 offset:2048
v_mfma_f32_16x16x32_bf16 v[208:211],a[100:103],a[244:247],v[208:211]
ds_read_b128 a[208:211],v48 offset:4096
v_mfma_f32_16x16x32_bf16 v[240:243],a[100:103],a[252:255],v[240:243]
ds_read_b128 a[212:215],v49 offset:4096
v_mfma_f32_16x16x32_bf16 v[240:243],a[96:99],a[248:251],v[240:243]
v_mfma_f32_16x16x32_bf16 v[148:151],a[104:107],a[224:227],v[148:151]
ds_read_b128 a[216:219],v48 offset:6144
v_xor_b32 v48,98304,v48
v_mfma_f32_16x16x32_bf16 v[148:151],a[108:111],a[228:231],v[148:151]
buffer_load_dwordx4 v39,s[28:31],s84 offen offset:3072 lds
v_mfma_f32_16x16x32_bf16 v[180:183],a[108:111],a[236:239],v[180:183]
s_add_u32 s84,s84,128
ds_read_b128 a[220:223],v49 offset:6144
v_xor_b32 v49,98304,v49
v_mfma_f32_16x16x32_bf16 v[180:183],a[104:107],a[232:235],v[180:183]
ds_read_b64_tr_b16 a[64:65],v50
v_mfma_f32_16x16x32_bf16 v[212:215],a[104:107],a[240:243],v[212:215]
ds_read_b64_tr_b16 a[66:67],v50 offset:2048
v_mfma_f32_16x16x32_bf16 v[212:215],a[108:111],a[244:247],v[212:215]
ds_read_b64_tr_b16 a[68:69],v50 offset:16384
v_mfma_f32_16x16x32_bf16 v[244:247],a[108:111],a[252:255],v[244:247]
v_mfma_f32_16x16x32_bf16 v[244:247],a[104:107],a[248:251],v[244:247]
ds_read_b64_tr_b16 a[70:71],v50 offset:18432
v_mfma_f32_16x16x32_bf16 v[152:155],a[112:115],a[224:227],v[152:155]
buffer_load_dword v9,v19,s[48:51],s90 offen
v_mfma_f32_16x16x32_bf16 v[152:155],a[116:119],a[228:231],v[152:155]
s_add_u32 s90,s90,896
ds_read_b64_tr_b16 a[72:73],v50 offset:8
v_mfma_f32_16x16x32_bf16 v[184:187],a[116:119],a[236:239],v[184:187]
ds_read_b64_tr_b16 a[74:75],v50 offset:2056
)A"
R"A(v_mfma_f32_16x16x32_bf16 v[184:187],a[112:115],a[232:235],v[184:187]
ds_read_b64_tr_b16 a[76:77],v50 offset:16392
v_mfma_f32_16x16x32_bf16 v[216:219],a[112:115],a[240:243],v[216:219]
ds_read_b64_tr_b16 a[78:79],v50 offset:18440
v_mfma_f32_16x16x32_bf16 v[216:219],a[116:119],a[244:247],v[216:219]
v_mfma_f32_16x16x32_bf16 v[248:251],a[116:119],a[252:255],v[248:251]
ds_read_b64_tr_b16 a[80:81],v50 offset:256
v_mfma_f32_16x16x32_bf16 v[248:251],a[112:115],a[248:251],v[248:251]
ds_read_b64_tr_b16 a[82:83],v50 offset:2304
v_mfma_f32_16x16x32_bf16 v[156:159],a[120:123],a[224:227],v[156:159]
ds_read_b64_tr_b16 a[84:85],v50 offset:16640
v_mfma_f32_16x16x32_bf16 v[156:159],a[124:127],a[228:231],v[156:159]
v_mfma_f32_16x16x32_bf16 v[188:191],a[124:127],a[236:239],v[188:191]
ds_read_b64_tr_b16 a[86:87],v50 offset:18688
v_mfma_f32_16x16x32_bf16 v[188:191],a[120:123],a[232:235],v[188:191]
ds_read_b64_tr_b16 a[88:89],v50 offset:264
v_mfma_f32_16x16x32_bf16 v[220:223],a[120:123],a[240:243],v[220:223]
ds_read_b64_tr_b16 a[90:91],v50 offset:2312
v_mfma_f32_16x16x32_bf16 v[220:223],a[124:127],a[244:247],v[220:223]
v_mfma_f32_16x16x32_bf16 v[252:255],a[124:127],a[252:255],v[252:255]
s_waitcnt lgkmcnt(4)
ds_read_b64_tr_b16 a[92:93],v50 offset:16648
v_mfma_f32_16x16x32_bf16 v[252:255],a[120:123],a[248:251],v[252:255]
s_add_u32 m0,s88,0
ds_read_b64_tr_b16 a[94:95],v50 offset:18696
v_mfma_f32_16x16x32_bf16 a[0:3],a[64:67],a[192:195],a[0:3]
ds_read_b64_tr_b16 a[96:97],v50 offset:512
v_mfma_f32_16x16x32_bf16 a[0:3],a[68:71],a[196:199],a[0:3]
buffer_load_dwordx4 v40,s[36:39],s83 offen lds
v_mfma_f32_16x16x32_bf16 a[16:19],a[68:71],a[204:207],a[16:19]
ds_read_b64_tr_b16 a[98:99],v50 offset:2560
v_mfma_f32_16x16x32_bf16 a[16:19],a[64:67],a[200:203],a[16:19]
ds_read_b64_tr_b16 a[100:101],v50 offset:16896
v_mfma_f32_16x16x32_bf16 a[32:35],a[64:67],a[208:211],a[32:35]
ds_read_b64_tr_b16 a[102:103],v50 offset:18944
v_mfma_f32_16x16x32_bf16 a[32:35],a[68:71],a[212:215],a[32:35]
v_mfma_f32_16x16x32_bf16 a[48:51],a[68:71],a[220:223],a[48:51]
s_add_u32 m0,s88,1024
ds_read_b64_tr_b16 a[104:105],v50 offset:520
v_mfma_f32_16x16x32_bf16 a[48:51],a[64:67],a[216:219],a[48:51]
ds_read_b64_tr_b16 a[106:107],v50 offset:2568
v_mfma_f32_16x16x32_bf16 a[4:7],a[72:75],a[192:195],a[4:7]
buffer_load_dwordx4 v41,s[36:39],s83 offen lds
v_mfma_f32_16x16x32_bf16 a[4:7],a[76:79],a[196:199],a[4:7]
ds_read_b64_tr_b16 a[108:109],v50 offset:16904
v_mfma_f32_16x16x32_bf16 a[20:23],a[76:79],a[204:207],a[20:23]
ds_read_b64_tr_b16 a[110:111],v50 offset:18952
v_mfma_f32_16x16x32_bf16 a[20:23],a[72:75],a[200:203],a[20:23]
ds_read_b64_tr_b16 a[112:113],v50 offset:768
v_mfma_f32_16x16x32_bf16 a[36:39],a[72:75],a[208:211],a[36:39]
ds_read_b64_tr_b16 a[114:115],v50 offset:2816
v_mfma_f32_16x16x32_bf16 a[36:39],a[76:79],a[212:215],a[36:39]
s_add_u32 m0,s88,2048
v_mfma_f32_16x16x32_bf16 a[52:55],a[76:79],a[220:223],a[52:55]
ds_read_b64_tr_b16 a[116:117],v50 offset:17152
v_mfma_f32_16x16x32_bf16 a[52:55],a[72:75],a[216:219],a[52:55]
buffer_load_dwordx4 v42,s[36:39],s83 offen lds
v_mfma_f32_16x16x32_bf16 a[8:11],a[80:83],a[192:195],a[8:11]
s_waitcnt lgkmcnt(5)
ds_read_b64_tr_b16 a[118:119],v50 offset:19200
v_mfma_f32_16x16x32_bf16 a[8:11],a[84:87],a[196:199],a[8:11]
ds_read_b64_tr_b16 a[120:121],v50 offset:776
v_mfma_f32_16x16x32_bf16 a[24:27],a[84:87],a[204:207],a[24:27]
ds_read_b64_tr_b16 a[122:123],v50 offset:2824
v_mfma_f32_16x16x32_bf16 a[24:27],a[80:83],a[200:203],a[24:27]
ds_read_b64_tr_b16 a[124:125],v50 offset:17160
v_mfma_f32_16x16x32_bf16 a[40:43],a[80:83],a[208:211],a[40:43]
s_add_u32 m0,s88,3072
v_mfma_f32_16x16x32_bf16 a[40:43],a[84:87],a[212:215],a[40:43]
ds_read_b64_tr_b16 a[126:127],v50 offset:19208
v_mfma_f32_16x16x32_bf16 a[56:59],a[84:87],a[220:223],a[56:59]
buffer_load_dwordx4 v43,s[36:39],s83 offen lds
v_mfma_f32_16x16x32_bf16 a[56:59],a[80:83],a[216:219],a[56:59]
ds_read_b64_tr_b16 a[64:65],v50 offset:1024
v_mfma_f32_16x16x32_bf16 a[12:15],a[88:91],a[192:195],a[12:15]
ds_read_b64_tr_b16 a[66:67],v50 offset:3072
v_mfma_f32_16x16x32_bf16 a[12:15],a[92:95],a[196:199],a[12:15]
ds_read_b64_tr_b16 a[68:69],v50 offset:17408
v_mfma_f32_16x16x32_bf16 a[28:31],a[92:95],a[204:207],a[28:31]
ds_read_b64_tr_b16 a[70:71],v50 offset:19456
v_mfma_f32_16x16x32_bf16 a[28:31],a[88:91],a[200:203],a[28:31]
s_add_u32 m0,s88,4096
v_mfma_f32_16x16x32_bf16 a[44:47],a[88:91],a[208:211],a[44:47]
ds_read_b64_tr_b16 a[72:73],v50 offset:1032
v_mfma_f32_16x16x32_bf16 a[44:47],a[92:95],a[212:215],a[44:47]
buffer_load_dwordx4 v44,s[36:39],s83 offen lds
v_mfma_f32_16x16x32_bf16 a[60:63],a[92:95],a[220:223],a[60:63]
ds_read_b64_tr_b16 a[74:75],v50 offset:3080
v_mfma_f32_16x16x32_bf16 a[60:63],a[88:91],a[216:219],a[60:63]
ds_read_b64_tr_b16 a[76:77],v50 offset:17416
v_mfma_f32_16x16x32_bf16 v[64:67],a[96:99],a[192:195],v[64:67]
ds_read_b64_tr_b16 a[78:79],v50 offset:19464
v_mfma_f32_16x16x32_bf16 v[64:67],a[100:103],a[196:199],v[64:67]
ds_read_b64_tr_b16 a[80:81],v50 offset:1280
v_mfma_f32_16x16x32_bf16 v[80:83],a[100:103],a[204:207],v[80:83]
s_add_u32 m0,s88,5120
v_mfma_f32_16x16x32_bf16 v[80:83],a[96:99],a[200:203],v[80:83]
ds_read_b64_tr_b16 a[82:83],v50 offset:3328
v_mfma_f32_16x16x32_bf16 v[96:99],a[96:99],a[208:211],v[96:99]
buffer_load_dwordx4 v45,s[36:39],s83 offen lds
v_mfma_f32_16x16x32_bf16 v[96:99],a[100:103],a[212:215],v[96:99]
ds_read_b64_tr_b16 a[84:85],v50 offset:17664
v_mfma_f32_16x16x32_bf16 v[112:115],a[100:103],a[220:223],v[112:115]
ds_read_b64_tr_b16 a[86:87],v50 offset:19712
v_mfma_f32_16x16x32_bf16 v[112:115],a[96:99],a[216:219],v[112:115]
ds_read_b64_tr_b16 a[88:89],v50 offset:1288
v_mfma_f32_16x16x32_bf16 v[68:71],a[104:107],a[192:195],v[68:71]
s_waitcnt lgkmcnt(5)
ds_read_b64_tr_b16 a[90:91],v50 offset:3336
v_mfma_f32_16x16x32_bf16 v[68:71],a[108:111],a[196:199],v[68:71]
s_add_u32 m0,s88,6144
v_mfma_f32_16x16x32_bf16 v[84:87],a[108:111],a[204:207],v[84:87]
ds_read_b64_tr_b16 a[92:93],v50 offset:17672
v_mfma_f32_16x16x32_bf16 v[84:87],a[104:107],a[200:203],v[84:87]
buffer_load_dwordx4 v46,s[36:39],s83 offen lds
v_mfma_f32_16x16x32_bf16 v[100:103],a[104:107],a[208:211],v[100:103]
ds_read_b64_tr_b16 a[94:95],v50 offset:19720
v_mfma_f32_16x16x32_bf16 v[100:103],a[108:111],a[212:215],v[100:103]
ds_read_b64_tr_b16 a[96:97],v50 offset:1536
v_mfma_f32_16x16x32_bf16 v[116:119],a[108:111],a[220:223],v[116:119]
ds_read_b64_tr_b16 a[98:99],v50 offset:3584
v_mfma_f32_16x16x32_bf16 v[116:119],a[104:107],a[216:219],v[116:119]
ds_read_b64_tr_b16 a[100:101],v50 offset:17920
v_mfma_f32_16x16x32_bf16 v[72:75],a[112:115],a[192:195],v[72:75]
s_add_u32 m0,s88,7168
s_mov_b32 s88,s69
v_mfma_f32_16x16x32_bf16 v[72:75],a[116:119],a[196:199],v[72:75]
ds_read_b64_tr_b16 a[102:103],v50 offset:19968
v_mfma_f32_16x16x32_bf16 v[88:91],a[116:119],a[204:207],v[88:91]
buffer_load_dwordx4 v47,s[36:39],s83 offen lds
v_mfma_f32_16x16x32_bf16 v[88:91],a[112:115],a[200:203],v[88:91]
s_add_u32 s83,s83,524288
ds_read_b64_tr_b16 a[104:105],v50 offset:1544
v_mfma_f32_16x16x32_bf16 v[104:107],a[112:115],a[208:211],v[104:107]
ds_read_b64_tr_b16 a[106:107],v50 offset:3592
v_mfma_f32_16x16x32_bf16 v[104:107],a[116:119],a[212:215],v[104:107]
ds_read_b64_tr_b16 a[108:109],v50 offset:17928
v_mfma_f32_16x16x32_bf16 v[120:123],a[116:119],a[220:223],v[120:123]
ds_read_b64_tr_b16 a[110:111],v50 offset:19976
v_mfma_f32_16x16x32_bf16 v[120:123],a[112:115],a[216:219],v[120:123]
s_add_u32 m0,s85,0
v_mfma_f32_16x16x32_bf16 v[76:79],a[120:123],a[192:195],v[76:79]
ds_read_b64_tr_b16 a[112:113],v50 offset:1792
v_mfma_f32_16x16x32_bf16 v[76:79],a[124:127],a[196:199],v[76:79]
buffer_load_dwordx4 v32,s[28:31],s84 offen lds
v_mfma_f32_16x16x32_bf16 v[92:95],a[124:127],a[204:207],v[92:95]
ds_read_b64_tr_b16 a[114:115],v50 offset:3840
v_mfma_f32_16x16x32_bf16 v[92:95],a[120:123],a[200:203],v[92:95]
ds_read_b64_tr_b16 a[116:117],v50 offset:18176
v_mfma_f32_16x16x32_bf16 v[108:111],a[120:123],a[208:211],v[108:111]
ds_read_b64_tr_b16 a[118:119],v50 offset:20224
v_mfma_f32_16x16x32_bf16 v[108:111],a[124:127],a[212:215],v[108:111]
v_mfma_f32_16x16x32_bf16 v[124:127],a[124:127],a[220:223],v[124:127]
ds_read_b64_tr_b16 a[124:125],v50 offset:18184
v_mfma_f32_16x16x32_bf16 v[124:127],a[120:123],a[216:219],v[124:127]
ds_read_b64_tr_b16 a[120:121],v50 offset:1800
v_mfma_f32_16x16x32_bf16 v[128:131],a[64:67],a[192:195],v[128:131]
buffer_load_dwordx4 v33,s[28:31],s84 offen offset:1024 lds
v_mfma_f32_16x16x32_bf16 v[128:131],a[68:71],a[196:199],v[128:131]
ds_read_b64_tr_b16 a[122:123],v50 offset:3848
v_mfma_f32_16x16x32_bf16 v[160:163],a[68:71],a[204:207],v[160:163]
ds_read_b64_tr_b16 a[126:127],v50 offset:20232
v_add_u32 v50,s86,v51
v_mfma_f32_16x16x32_bf16 v[160:163],a[64:67],a[200:203],v[160:163]
v_mfma_f32_16x16x32_bf16 v[192:195],a[64:67],a[208:211],v[192:195]
v_mfma_f32_16x16x32_bf16 v[192:195],a[68:71],a[212:215],v[192:195]
v_mfma_f32_16x16x32_bf16 v[224:227],a[68:71],a[220:223],v[224:227]
v_mfma_f32_16x16x32_bf16 v[224:227],a[64:67],a[216:219],v[224:227]
buffer_load_dwordx4 v34,s[28:31],s84 offen offset:2048 lds
v_mfma_f32_16x16x32_bf16 v[132:135],a[72:75],a[192:195],v[132:135]
v_mfma_f32_16x16x32_bf16 v[132:135],a[76:79],a[196:199],v[132:135]
v_mfma_f32_16x16x32_bf16 v[164:167],a[76:79],a[204:207],v[164:167]
v_mfma_f32_16x16x32_bf16 v[164:167],a[72:75],a[200:203],v[164:167]
v_mfma_f32_16x16x32_bf16 v[196:199],a[72:75],a[208:211],v[196:199]
v_mfma_f32_16x16x32_bf16 v[196:199],a[76:79],a[212:215],v[196:199]
v_mfma_f32_16x16x32_bf16 v[228:231],a[76:79],a[220:223],v[228:231]
buffer_load_dwordx4 v35,s[28:31],s84 offen offset:3072 lds
v_mfma_f32_16x16x32_bf16 v[228:231],a[72:75],a[216:219],v[228:231]
s_waitcnt lgkmcnt(0)
v_mfma_f32_16x16x32_bf16 v[136:139],a[80:83],a[192:195],v[136:139]
v_mfma_f32_16x16x32_bf16 v[136:139],a[84:87],a[196:199],v[136:139]
v_mfma_f32_16x16x32_bf16 v[168:171],a[84:87],a[204:207],v[168:171]
v_mfma_f32_16x16x32_bf16 v[168:171],a[80:83],a[200:203],v[168:171]
s_add_u32 m0,s85,4096
s_xor_b32 s85,s85,98304
v_mfma_f32_16x16x32_bf16 v[200:203],a[80:83],a[208:211],v[200:203]
v_mfma_f32_16x16x32_bf16 v[200:203],a[84:87],a[212:215],v[200:203]
buffer_load_dwordx4 v36,s[28:31],s84 offen lds
v_mfma_f32_16x16x32_bf16 v[232:235],a[84:87],a[220:223],v[232:235]
v_mfma_f32_16x16x32_bf16 v[232:235],a[80:83],a[216:219],v[232:235]
v_mfma_f32_16x16x32_bf16 v[140:143],a[88:91],a[192:195],v[140:143]
v_mfma_f32_16x16x32_bf16 v[140:143],a[92:95],a[196:199],v[140:143]
v_mfma_f32_16x16x32_bf16 v[172:175],a[92:95],a[204:207],v[172:175]
v_mfma_f32_16x16x32_bf16 v[172:175],a[88:91],a[200:203],v[172:175]
v_mfma_f32_16x16x32_bf16 v[204:207],a[88:91],a[208:211],v[204:207]
buffer_load_dwordx4 v37,s[28:31],s84 offen offset:1024 lds
v_mfma_f32_16x16x32_bf16 v[204:207],a[92:95],a[212:215],v[204:207]
v_mfma_f32_16x16x32_bf16 v[236:239],a[92:95],a[220:223],v[236:239]
v_mfma_f32_16x16x32_bf16 v[236:239],a[88:91],a[216:219],v[236:239]
s_waitcnt vmcnt(23)
s_barrier
v_mfma_f32_16x16x32_bf16 v[144:147],a[96:99],a[192:195],v[144:147]
s_waitcnt vmcnt(15)
ds_read_b128 a[224:227],v48
v_mfma_f32_16x16x32_bf16 v[144:147],a[100:103],a[196:199],v[144:147]
ds_read_b128 a[228:231],v49
v_mfma_f32_16x16x32_bf16 v[176:179],a[100:103],a[204:207],v[176:179]
buffer_load_dwordx4 v38,s[28:31],s84 offen offset:2048 lds
v_mfma_f32_16x16x32_bf16 v[176:179],a[96:99],a[200:203],v[176:179]
ds_read_b128 a[232:235],v48 offset:2048
v_mfma_f32_16x16x32_bf16 v[208:211],a[96:99],a[208:211],v[208:211]
ds_read_b128 a[236:239],v49 offset:2048
v_mfma_f32_16x16x32_bf16 v[208:211],a[100:103],a[212:215],v[208:211]
ds_read_b128 a[240:243],v48 offset:4096
v_mfma_f32_16x16x32_bf16 v[240:243],a[100:103],a[220:223],v[240:243]
ds_read_b128 a[244:247],v49 offset:4096
v_mfma_f32_16x16x32_bf16 v[240:243],a[96:99],a[216:219],v[240:243]
v_mfma_f32_16x16x32_bf16 v[148:151],a[104:107],a[192:195],v[148:151]
ds_read_b128 a[248:251],v48 offset:6144
v_mfma_f32_16x16x32_bf16 v[148:151],a[108:111],a[196:199],v[148:151]
buffer_load_dwordx4 v39,s[28:31],s84 offen offset:3072 lds
v_mfma_f32_16x16x32_bf16 v[180:183],a[108:111],a[204:207],v[180:183]
)A"
R"A(s_add_u32 s84,s84,128
ds_read_b128 a[252:255],v49 offset:6144
v_mfma_f32_16x16x32_bf16 v[180:183],a[104:107],a[200:203],v[180:183]
ds_read_b64_tr_b16 a[64:65],v50
v_mfma_f32_16x16x32_bf16 v[212:215],a[104:107],a[208:211],v[212:215]
ds_read_b64_tr_b16 a[66:67],v50 offset:2048
v_mfma_f32_16x16x32_bf16 v[212:215],a[108:111],a[212:215],v[212:215]
ds_read_b64_tr_b16 a[68:69],v50 offset:16384
v_mfma_f32_16x16x32_bf16 v[244:247],a[108:111],a[220:223],v[244:247]
v_mfma_f32_16x16x32_bf16 v[244:247],a[104:107],a[216:219],v[244:247]
ds_read_b64_tr_b16 a[70:71],v50 offset:18432
v_mfma_f32_16x16x32_bf16 v[152:155],a[112:115],a[192:195],v[152:155]
buffer_load_dword v8,v19,s[48:51],s90 offen
v_mfma_f32_16x16x32_bf16 v[152:155],a[116:119],a[196:199],v[152:155]
s_add_u32 s90,s90,896
s_nop 0
L_pd2_%=:
s_cbranch_execz L_pd6_%=
ds_read_b64_tr_b16 a[72:73],v50 offset:8
v_mfma_f32_16x16x32_bf16 v[184:187],a[116:119],a[204:207],v[184:187]
ds_read_b64_tr_b16 a[74:75],v50 offset:2056
v_mfma_f32_16x16x32_bf16 v[184:187],a[112:115],a[200:203],v[184:187]
ds_read_b64_tr_b16 a[76:77],v50 offset:16392
v_mfma_f32_16x16x32_bf16 v[216:219],a[112:115],a[208:211],v[216:219]
ds_read_b64_tr_b16 a[78:79],v50 offset:18440
v_mfma_f32_16x16x32_bf16 v[216:219],a[116:119],a[212:215],v[216:219]
v_mfma_f32_16x16x32_bf16 v[248:251],a[116:119],a[220:223],v[248:251]
ds_read_b64_tr_b16 a[80:81],v50 offset:256
v_mfma_f32_16x16x32_bf16 v[248:251],a[112:115],a[216:219],v[248:251]
ds_read_b64_tr_b16 a[82:83],v50 offset:2304
v_mfma_f32_16x16x32_bf16 v[156:159],a[120:123],a[192:195],v[156:159]
ds_read_b64_tr_b16 a[84:85],v50 offset:16640
v_mfma_f32_16x16x32_bf16 v[156:159],a[124:127],a[196:199],v[156:159]
v_mfma_f32_16x16x32_bf16 v[188:191],a[124:127],a[204:207],v[188:191]
ds_read_b64_tr_b16 a[86:87],v50 offset:18688
v_mfma_f32_16x16x32_bf16 v[188:191],a[120:123],a[200:203],v[188:191]
ds_read_b64_tr_b16 a[88:89],v50 offset:264
v_mfma_f32_16x16x32_bf16 v[220:223],a[120:123],a[208:211],v[220:223]
ds_read_b64_tr_b16 a[90:91],v50 offset:2312
v_mfma_f32_16x16x32_bf16 v[220:223],a[124:127],a[212:215],v[220:223]
v_mfma_f32_16x16x32_bf16 v[252:255],a[124:127],a[220:223],v[252:255]
ds_read_b64_tr_b16 a[92:93],v50 offset:16648
v_mfma_f32_16x16x32_bf16 v[252:255],a[120:123],a[216:219],v[252:255]
ds_read_b64_tr_b16 a[94:95],v50 offset:18696
s_sub_u32 s70,s70,1
s_cmp_lg_u32 s70,0
s_cbranch_scc1 L_qp_%=
s_waitcnt lgkmcnt(6)
v_mfma_f32_16x16x32_bf16 a[0:3],a[64:67],a[224:227],a[0:3]
s_memrealtime s[92:93]
s_memtime s[56:57]
s_add_u32 m0,s85,0
v_mfma_f32_16x16x32_bf16 a[0:3],a[68:71],a[228:231],a[0:3]
buffer_load_dwordx4 v32,s[28:31],s84 offen lds
v_mfma_f32_16x16x32_bf16 a[16:19],a[68:71],a[236:239],a[16:19]
s_sub_u32 s83,s83,524288
ds_read_b64_tr_b16 a[96:97],v50 offset:512
v_subrev_u32 v40,256,v40
v_mfma_f32_16x16x32_bf16 a[16:19],a[64:67],a[232:235],a[16:19]
s_mov_b32 s69,s86
ds_read_b64_tr_b16 a[98:99],v50 offset:2560
v_subrev_u32 v41,256,v41
v_mfma_f32_16x16x32_bf16 a[32:35],a[64:67],a[240:243],a[32:35]
s_mov_b32 s86,s87
ds_read_b64_tr_b16 a[100:101],v50 offset:16896
v_subrev_u32 v42,256,v42
v_mfma_f32_16x16x32_bf16 a[32:35],a[68:71],a[244:247],a[32:35]
s_mov_b32 s87,s88
ds_read_b64_tr_b16 a[102:103],v50 offset:18944
v_subrev_u32 v43,256,v43
v_mfma_f32_16x16x32_bf16 a[48:51],a[68:71],a[252:255],a[48:51]
s_add_u32 s75,s75,16384
v_subrev_u32 v44,256,v44
v_subrev_u32 v45,256,v45
v_mfma_f32_16x16x32_bf16 a[48:51],a[64:67],a[248:251],a[48:51]
buffer_load_dwordx4 v33,s[28:31],s84 offen offset:1024 lds
v_mfma_f32_16x16x32_bf16 a[4:7],a[72:75],a[224:227],a[4:7]
s_sub_u32 s78,s77,32768
ds_read_b64_tr_b16 a[104:105],v50 offset:520
v_subrev_u32 v46,256,v46
v_mfma_f32_16x16x32_bf16 a[4:7],a[76:79],a[228:231],a[4:7]
s_and_b32 s78,s78,0x1ffff
ds_read_b64_tr_b16 a[106:107],v50 offset:2568
v_subrev_u32 v47,256,v47
v_mfma_f32_16x16x32_bf16 a[20:23],a[76:79],a[236:239],a[20:23]
s_sub_u32 s78,s78,s77
ds_read_b64_tr_b16 a[108:109],v50 offset:16904
v_xor_b32 v48,98304,v48
v_mfma_f32_16x16x32_bf16 a[20:23],a[72:75],a[232:235],a[20:23]
s_mov_b32 s65,262144
ds_read_b64_tr_b16 a[110:111],v50 offset:18952
v_xor_b32 v49,98304,v49
v_mfma_f32_16x16x32_bf16 a[36:39],a[72:75],a[240:243],a[36:39]
s_add_u32 s77,s77,s78
s_mov_b32 s6,0
v_add_u32 v52,s87,v51
v_mfma_f32_16x16x32_bf16 a[36:39],a[76:79],a[244:247],a[36:39]
buffer_load_dwordx4 v34,s[28:31],s84 offen offset:2048 lds
v_mfma_f32_16x16x32_bf16 a[52:55],a[76:79],a[252:255],a[52:55]
ds_read_b64_tr_b16 a[112:113],v50 offset:768
v_mfma_f32_16x16x32_bf16 a[52:55],a[72:75],a[248:251],a[52:55]
ds_read_b64_tr_b16 a[114:115],v50 offset:2816
v_mfma_f32_16x16x32_bf16 a[8:11],a[80:83],a[224:227],a[8:11]
s_waitcnt lgkmcnt(5)
ds_read_b64_tr_b16 a[116:117],v50 offset:17152
v_mfma_f32_16x16x32_bf16 a[8:11],a[84:87],a[228:231],a[8:11]
ds_read_b64_tr_b16 a[118:119],v50 offset:19200
v_mfma_f32_16x16x32_bf16 a[24:27],a[84:87],a[236:239],a[24:27]
v_mfma_f32_16x16x32_bf16 a[24:27],a[80:83],a[232:235],a[24:27]
buffer_load_dwordx4 v35,s[28:31],s84 offen offset:3072 lds
v_mfma_f32_16x16x32_bf16 a[40:43],a[80:83],a[240:243],a[40:43]
ds_read_b64_tr_b16 a[120:121],v50 offset:776
v_mfma_f32_16x16x32_bf16 a[40:43],a[84:87],a[244:247],a[40:43]
ds_read_b64_tr_b16 a[122:123],v50 offset:2824
v_mfma_f32_16x16x32_bf16 a[56:59],a[84:87],a[252:255],a[56:59]
ds_read_b64_tr_b16 a[124:125],v50 offset:17160
v_mfma_f32_16x16x32_bf16 a[56:59],a[80:83],a[248:251],a[56:59]
s_add_u32 m0,s85,4096
s_xor_b32 s85,s85,98304
ds_read_b64_tr_b16 a[126:127],v50 offset:19208
v_mfma_f32_16x16x32_bf16 a[12:15],a[88:91],a[224:227],a[12:15]
v_mfma_f32_16x16x32_bf16 a[12:15],a[92:95],a[228:231],a[12:15]
buffer_load_dwordx4 v36,s[28:31],s84 offen lds
v_mfma_f32_16x16x32_bf16 a[28:31],a[92:95],a[236:239],a[28:31]
ds_read_b64_tr_b16 a[64:65],v50 offset:1024
v_mfma_f32_16x16x32_bf16 a[28:31],a[88:91],a[232:235],a[28:31]
ds_read_b64_tr_b16 a[66:67],v50 offset:3072
v_mfma_f32_16x16x32_bf16 a[44:47],a[88:91],a[240:243],a[44:47]
ds_read_b64_tr_b16 a[68:69],v50 offset:17408
v_mfma_f32_16x16x32_bf16 a[44:47],a[92:95],a[244:247],a[44:47]
ds_read_b64_tr_b16 a[70:71],v50 offset:19456
v_mfma_f32_16x16x32_bf16 a[60:63],a[92:95],a[252:255],a[60:63]
v_mfma_f32_16x16x32_bf16 a[60:63],a[88:91],a[248:251],a[60:63]
buffer_load_dwordx4 v37,s[28:31],s84 offen offset:1024 lds
v_mfma_f32_16x16x32_bf16 v[64:67],a[96:99],a[224:227],v[64:67]
ds_read_b64_tr_b16 a[72:73],v50 offset:1032
v_mfma_f32_16x16x32_bf16 v[64:67],a[100:103],a[228:231],v[64:67]
ds_read_b64_tr_b16 a[74:75],v50 offset:3080
v_mfma_f32_16x16x32_bf16 v[80:83],a[100:103],a[236:239],v[80:83]
ds_read_b64_tr_b16 a[76:77],v50 offset:17416
v_mfma_f32_16x16x32_bf16 v[80:83],a[96:99],a[232:235],v[80:83]
ds_read_b64_tr_b16 a[78:79],v50 offset:19464
v_mfma_f32_16x16x32_bf16 v[96:99],a[96:99],a[240:243],v[96:99]
v_mfma_f32_16x16x32_bf16 v[96:99],a[100:103],a[244:247],v[96:99]
buffer_load_dwordx4 v38,s[28:31],s84 offen offset:2048 lds
v_mfma_f32_16x16x32_bf16 v[112:115],a[100:103],a[252:255],v[112:115]
ds_read_b64_tr_b16 a[80:81],v50 offset:1280
v_mfma_f32_16x16x32_bf16 v[112:115],a[96:99],a[248:251],v[112:115]
s_waitcnt lgkmcnt(5)
ds_read_b64_tr_b16 a[82:83],v50 offset:3328
v_mfma_f32_16x16x32_bf16 v[68:71],a[104:107],a[224:227],v[68:71]
ds_read_b64_tr_b16 a[84:85],v50 offset:17664
v_mfma_f32_16x16x32_bf16 v[68:71],a[108:111],a[228:231],v[68:71]
ds_read_b64_tr_b16 a[86:87],v50 offset:19712
v_mfma_f32_16x16x32_bf16 v[84:87],a[108:111],a[236:239],v[84:87]
v_mfma_f32_16x16x32_bf16 v[84:87],a[104:107],a[232:235],v[84:87]
buffer_load_dwordx4 v39,s[28:31],s84 offen offset:3072 lds
v_mfma_f32_16x16x32_bf16 v[100:103],a[104:107],a[240:243],v[100:103]
s_add_u32 s84,s84,128
ds_read_b64_tr_b16 a[88:89],v50 offset:1288
v_mfma_f32_16x16x32_bf16 v[100:103],a[108:111],a[244:247],v[100:103]
ds_read_b64_tr_b16 a[90:91],v50 offset:3336
v_mfma_f32_16x16x32_bf16 v[116:119],a[108:111],a[252:255],v[116:119]
ds_read_b64_tr_b16 a[92:93],v50 offset:17672
v_mfma_f32_16x16x32_bf16 v[116:119],a[104:107],a[248:251],v[116:119]
s_add_u32 m0,s88,0
ds_read_b64_tr_b16 a[94:95],v50 offset:19720
v_mfma_f32_16x16x32_bf16 v[72:75],a[112:115],a[224:227],v[72:75]
v_mfma_f32_16x16x32_bf16 v[72:75],a[116:119],a[228:231],v[72:75]
buffer_load_dwordx4 v40,s[36:39],s83 offen lds
v_mfma_f32_16x16x32_bf16 v[88:91],a[116:119],a[236:239],v[88:91]
ds_read_b64_tr_b16 a[96:97],v50 offset:1536
v_mfma_f32_16x16x32_bf16 v[88:91],a[112:115],a[232:235],v[88:91]
ds_read_b64_tr_b16 a[98:99],v50 offset:3584
v_mfma_f32_16x16x32_bf16 v[104:107],a[112:115],a[240:243],v[104:107]
ds_read_b64_tr_b16 a[100:101],v50 offset:17920
v_mfma_f32_16x16x32_bf16 v[104:107],a[116:119],a[244:247],v[104:107]
s_add_u32 m0,s88,1024
ds_read_b64_tr_b16 a[102:103],v50 offset:19968
v_mfma_f32_16x16x32_bf16 v[120:123],a[116:119],a[252:255],v[120:123]
v_mfma_f32_16x16x32_bf16 v[120:123],a[112:115],a[248:251],v[120:123]
buffer_load_dwordx4 v41,s[36:39],s83 offen lds
v_mfma_f32_16x16x32_bf16 v[76:79],a[120:123],a[224:227],v[76:79]
ds_read_b64_tr_b16 a[104:105],v50 offset:1544
v_mfma_f32_16x16x32_bf16 v[76:79],a[124:127],a[228:231],v[76:79]
ds_read_b64_tr_b16 a[106:107],v50 offset:3592
v_mfma_f32_16x16x32_bf16 v[92:95],a[124:127],a[236:239],v[92:95]
ds_read_b64_tr_b16 a[108:109],v50 offset:17928
v_mfma_f32_16x16x32_bf16 v[92:95],a[120:123],a[232:235],v[92:95]
s_add_u32 m0,s88,2048
ds_read_b64_tr_b16 a[110:111],v50 offset:19976
v_mfma_f32_16x16x32_bf16 v[108:111],a[120:123],a[240:243],v[108:111]
v_mfma_f32_16x16x32_bf16 v[108:111],a[124:127],a[244:247],v[108:111]
buffer_load_dwordx4 v42,s[36:39],s83 offen lds
v_mfma_f32_16x16x32_bf16 v[124:127],a[124:127],a[252:255],v[124:127]
ds_read_b64_tr_b16 a[112:113],v50 offset:1792
v_mfma_f32_16x16x32_bf16 v[124:127],a[120:123],a[248:251],v[124:127]
ds_read_b64_tr_b16 a[114:115],v50 offset:3840
v_mfma_f32_16x16x32_bf16 v[128:131],a[64:67],a[224:227],v[128:131]
ds_read_b64_tr_b16 a[116:117],v50 offset:18176
v_mfma_f32_16x16x32_bf16 v[128:131],a[68:71],a[228:231],v[128:131]
s_add_u32 m0,s88,3072
ds_read_b64_tr_b16 a[118:119],v50 offset:20224
v_mfma_f32_16x16x32_bf16 v[160:163],a[68:71],a[236:239],v[160:163]
v_mfma_f32_16x16x32_bf16 v[160:163],a[64:67],a[232:235],v[160:163]
buffer_load_dwordx4 v43,s[36:39],s83 offen lds
v_mfma_f32_16x16x32_bf16 v[192:195],a[64:67],a[240:243],v[192:195]
ds_read_b64_tr_b16 a[120:121],v50 offset:1800
v_mfma_f32_16x16x32_bf16 v[192:195],a[68:71],a[244:247],v[192:195]
ds_read_b64_tr_b16 a[122:123],v50 offset:3848
v_mfma_f32_16x16x32_bf16 v[224:227],a[68:71],a[252:255],v[224:227]
s_waitcnt lgkmcnt(4)
ds_read_b64_tr_b16 a[124:125],v50 offset:18184
v_mfma_f32_16x16x32_bf16 v[224:227],a[64:67],a[248:251],v[224:227]
s_add_u32 m0,s88,4096
ds_read_b64_tr_b16 a[126:127],v50 offset:20232
v_add_u32 v50,s86,v51
v_mfma_f32_16x16x32_bf16 v[132:135],a[72:75],a[224:227],v[132:135]
v_mfma_f32_16x16x32_bf16 v[132:135],a[76:79],a[228:231],v[132:135]
buffer_load_dwordx4 v44,s[36:39],s83 offen lds
v_mfma_f32_16x16x32_bf16 v[164:167],a[76:79],a[236:239],v[164:167]
v_mfma_f32_16x16x32_bf16 v[164:167],a[72:75],a[232:235],v[164:167]
v_mfma_f32_16x16x32_bf16 v[196:199],a[72:75],a[240:243],v[196:199]
v_mfma_f32_16x16x32_bf16 v[196:199],a[76:79],a[244:247],v[196:199]
s_add_u32 m0,s88,5120
v_mfma_f32_16x16x32_bf16 v[228:231],a[76:79],a[252:255],v[228:231]
v_mfma_f32_16x16x32_bf16 v[228:231],a[72:75],a[248:251],v[228:231]
buffer_load_dwordx4 v45,s[36:39],s83 offen lds
v_mfma_f32_16x16x32_bf16 v[136:139],a[80:83],a[224:227],v[136:139]
v_mfma_f32_16x16x32_bf16 v[136:139],a[84:87],a[228:231],v[136:139]
v_mfma_f32_16x16x32_bf16 v[168:171],a[84:87],a[236:239],v[168:171]
v_mfma_f32_16x16x32_bf16 v[168:171],a[80:83],a[232:235],v[168:171]
s_add_u32 m0,s88,6144
v_mfma_f32_16x16x32_bf16 v[200:203],a[80:83],a[240:243],v[200:203]
v_mfma_f32_16x16x32_bf16 v[200:203],a[84:87],a[244:247],v[200:203]
buffer_load_dwordx4 v46,s[36:39],s83 offen lds
v_mfma_f32_16x16x32_bf16 v[232:235],a[84:87],a[252:255],v[232:235]
v_mfma_f32_16x16x32_bf16 v[232:235],a[80:83],a[248:251],v[232:235]
v_mfma_f32_16x16x32_bf16 v[140:143],a[88:91],a[224:227],v[140:143]
)A"
R"A(v_mfma_f32_16x16x32_bf16 v[140:143],a[92:95],a[228:231],v[140:143]
s_add_u32 m0,s88,7168
s_mov_b32 s88,s69
s_and_b32 s69,s75,0xfffc0000
v_mfma_f32_16x16x32_bf16 v[172:175],a[92:95],a[236:239],v[172:175]
s_add_u32 s75,s75,16384
v_mfma_f32_16x16x32_bf16 v[172:175],a[88:91],a[232:235],v[172:175]
buffer_load_dwordx4 v47,s[36:39],s83 offen lds
v_mfma_f32_16x16x32_bf16 v[204:207],a[88:91],a[240:243],v[204:207]
s_waitcnt lgkmcnt(0)
s_add_u32 s83,s83,524288
s_sub_u32 s92,s92,s8
v_mfma_f32_16x16x32_bf16 v[204:207],a[92:95],a[244:247],v[204:207]
s_sub_u32 s56,s56,s4
s_mov_b32 s93,782
s_mul_i32 s92,s92,12600
v_mfma_f32_16x16x32_bf16 v[236:239],a[92:95],a[252:255],v[236:239]
s_cmp_ge_u32 s63,2
s_cselect_b32 s93,823,s93
s_mul_i32 s93,s93,s56
v_mfma_f32_16x16x32_bf16 v[236:239],a[88:91],a[248:251],v[236:239]
s_add_u32 s10,s20,s74
s_addc_u32 s11,s21,0
s_waitcnt vmcnt(25)
s_barrier
v_mfma_f32_16x16x32_bf16 v[144:147],a[96:99],a[224:227],v[144:147]
s_add_u32 m0,s76,0
s_waitcnt vmcnt(17)
ds_read_b128 a[192:195],v48
v_mfma_f32_16x16x32_bf16 v[144:147],a[100:103],a[228:231],v[144:147]
ds_read_b128 a[196:199],v49
v_mfma_f32_16x16x32_bf16 v[176:179],a[100:103],a[236:239],v[176:179]
global_load_lds_dwordx4 v4,s[10:11] nt
ds_read_b128 a[200:203],v48 offset:2048
v_mfma_f32_16x16x32_bf16 v[176:179],a[96:99],a[232:235],v[176:179]
ds_read_b128 a[204:207],v49 offset:2048
v_mfma_f32_16x16x32_bf16 v[208:211],a[96:99],a[240:243],v[208:211]
v_mfma_f32_16x16x32_bf16 v[208:211],a[100:103],a[244:247],v[208:211]
ds_read_b128 a[208:211],v48 offset:4096
v_mfma_f32_16x16x32_bf16 v[240:243],a[100:103],a[252:255],v[240:243]
ds_read_b128 a[212:215],v49 offset:4096
v_mfma_f32_16x16x32_bf16 v[240:243],a[96:99],a[248:251],v[240:243]
ds_read_b128 a[216:219],v48 offset:6144
v_xor_b32 v48,98304,v48
v_mfma_f32_16x16x32_bf16 v[148:151],a[104:107],a[224:227],v[148:151]
v_mfma_f32_16x16x32_bf16 v[148:151],a[108:111],a[228:231],v[148:151]
ds_read_b128 a[220:223],v49 offset:6144
v_xor_b32 v49,98304,v49
v_mfma_f32_16x16x32_bf16 v[180:183],a[108:111],a[236:239],v[180:183]
ds_read_b64_tr_b16 a[64:65],v50
v_mfma_f32_16x16x32_bf16 v[180:183],a[104:107],a[232:235],v[180:183]
ds_read_b64_tr_b16 a[66:67],v50 offset:2048
v_mfma_f32_16x16x32_bf16 v[212:215],a[104:107],a[240:243],v[212:215]
v_mfma_f32_16x16x32_bf16 v[212:215],a[108:111],a[244:247],v[212:215]
ds_read_b64_tr_b16 a[68:69],v50 offset:16384
v_mfma_f32_16x16x32_bf16 v[244:247],a[108:111],a[252:255],v[244:247]
ds_read_b64_tr_b16 a[70:71],v50 offset:18432
v_mfma_f32_16x16x32_bf16 v[244:247],a[104:107],a[248:251],v[244:247]
ds_read_b64_tr_b16 a[72:73],v50 offset:8
v_mfma_f32_16x16x32_bf16 v[152:155],a[112:115],a[224:227],v[152:155]
v_mfma_f32_16x16x32_bf16 v[152:155],a[116:119],a[228:231],v[152:155]
ds_read_b64_tr_b16 a[74:75],v50 offset:2056
v_mfma_f32_16x16x32_bf16 v[184:187],a[116:119],a[236:239],v[184:187]
global_load_lds_dwordx4 v4,s[10:11] offset:1024 nt
ds_read_b64_tr_b16 a[76:77],v50 offset:16392
v_mfma_f32_16x16x32_bf16 v[184:187],a[112:115],a[232:235],v[184:187]
ds_read_b64_tr_b16 a[78:79],v50 offset:18440
v_mfma_f32_16x16x32_bf16 v[216:219],a[112:115],a[240:243],v[216:219]
v_mfma_f32_16x16x32_bf16 v[216:219],a[116:119],a[244:247],v[216:219]
ds_read_b64_tr_b16 a[80:81],v50 offset:1024
v_mfma_f32_16x16x32_bf16 v[248:251],a[116:119],a[252:255],v[248:251]
ds_read_b64_tr_b16 a[82:83],v50 offset:3072
v_mfma_f32_16x16x32_bf16 v[248:251],a[112:115],a[248:251],v[248:251]
ds_read_b64_tr_b16 a[84:85],v50 offset:17408
v_mfma_f32_16x16x32_bf16 v[156:159],a[120:123],a[224:227],v[156:159]
v_mfma_f32_16x16x32_bf16 v[156:159],a[124:127],a[228:231],v[156:159]
ds_read_b64_tr_b16 a[86:87],v50 offset:19456
v_mfma_f32_16x16x32_bf16 v[188:191],a[124:127],a[236:239],v[188:191]
ds_read_b64_tr_b16 a[88:89],v50 offset:1032
v_mfma_f32_16x16x32_bf16 v[188:191],a[120:123],a[232:235],v[188:191]
s_cmp_lt_u32 s92,s93
s_cselect_b32 s5,5,0
s_cselect_b32 s7,2,0
s_cselect_b32 s9,8,10
ds_read_b64_tr_b16 a[90:91],v50 offset:3080
v_mfma_f32_16x16x32_bf16 v[220:223],a[120:123],a[240:243],v[220:223]
s_mov_b32 s4,s5
v_mfma_f32_16x16x32_bf16 v[220:223],a[124:127],a[244:247],v[220:223]
ds_read_b64_tr_b16 a[92:93],v50 offset:17416
v_mfma_f32_16x16x32_bf16 v[252:255],a[124:127],a[252:255],v[252:255]
ds_read_b64_tr_b16 a[94:95],v50 offset:19464
v_mfma_f32_16x16x32_bf16 v[252:255],a[120:123],a[248:251],v[252:255]
s_waitcnt vmcnt(10) lgkmcnt(5)
ds_read_b128 a[224:227],v48
v_mfma_f32_16x16x32_bf16 v[128:131],a[64:67],a[192:195],v[128:131]
v_mfma_f32_16x16x32_bf16 v[128:131],a[68:71],a[196:199],v[128:131]
ds_read_b128 a[228:231],v49
v_mfma_f32_16x16x32_bf16 v[160:163],a[68:71],a[204:207],v[160:163]
global_load_lds_dwordx4 v5,s[10:11] offset:2048 nt
ds_read_b128 a[236:239],v49 offset:2048
v_mfma_f32_16x16x32_bf16 v[160:163],a[64:67],a[200:203],v[160:163]
ds_read_b128 a[232:235],v48 offset:2048
v_mfma_f32_16x16x32_bf16 v[192:195],a[64:67],a[208:211],v[192:195]
v_mfma_f32_16x16x32_bf16 v[192:195],a[68:71],a[212:215],v[192:195]
ds_read_b128 a[240:243],v48 offset:4096
v_mfma_f32_16x16x32_bf16 v[224:227],a[68:71],a[220:223],v[224:227]
ds_read_b128 a[244:247],v49 offset:4096
v_mfma_f32_16x16x32_bf16 v[224:227],a[64:67],a[216:219],v[224:227]
ds_read_b128 a[252:255],v49 offset:6144
v_mfma_f32_16x16x32_bf16 v[132:135],a[72:75],a[192:195],v[132:135]
v_mfma_f32_16x16x32_bf16 v[132:135],a[76:79],a[196:199],v[132:135]
ds_read_b128 a[248:251],v48 offset:6144
v_mfma_f32_16x16x32_bf16 v[164:167],a[76:79],a[204:207],v[164:167]
ds_read_b64_tr_b16 a[96:97],v50 offset:256
v_mfma_f32_16x16x32_bf16 v[164:167],a[72:75],a[200:203],v[164:167]
ds_read_b64_tr_b16 a[98:99],v50 offset:2304
v_mfma_f32_16x16x32_bf16 v[196:199],a[72:75],a[208:211],v[196:199]
v_mfma_f32_16x16x32_bf16 v[196:199],a[76:79],a[212:215],v[196:199]
ds_read_b64_tr_b16 a[100:101],v50 offset:16640
v_mfma_f32_16x16x32_bf16 v[228:231],a[76:79],a[220:223],v[228:231]
ds_read_b64_tr_b16 a[102:103],v50 offset:18688
v_mfma_f32_16x16x32_bf16 v[228:231],a[72:75],a[216:219],v[228:231]
s_waitcnt lgkmcnt(5)
ds_read_b64_tr_b16 a[104:105],v50 offset:1280
v_mfma_f32_16x16x32_bf16 v[128:131],a[80:83],a[224:227],v[128:131]
v_mfma_f32_16x16x32_bf16 v[128:131],a[84:87],a[228:231],v[128:131]
ds_read_b64_tr_b16 a[106:107],v50 offset:3328
v_mfma_f32_16x16x32_bf16 v[160:163],a[84:87],a[236:239],v[160:163]
global_load_lds_dwordx4 v5,s[10:11] offset:3072
ds_read_b64_tr_b16 a[108:109],v50 offset:17664
v_mfma_f32_16x16x32_bf16 v[160:163],a[80:83],a[232:235],v[160:163]
ds_read_b64_tr_b16 a[110:111],v50 offset:19712
v_mfma_f32_16x16x32_bf16 v[192:195],a[80:83],a[240:243],v[192:195]
v_mfma_f32_16x16x32_bf16 v[192:195],a[84:87],a[244:247],v[192:195]
ds_read_b64_tr_b16 a[112:113],v50 offset:264
v_mul_f32 v128,s71,v128
v_mfma_f32_16x16x32_bf16 v[224:227],a[84:87],a[252:255],v[224:227]
s_waitcnt lgkmcnt(5)
ds_read_b64_tr_b16 a[114:115],v50 offset:2312
v_mul_f32 v129,s71,v129
v_mfma_f32_16x16x32_bf16 v[224:227],a[80:83],a[248:251],v[224:227]
ds_read_b64_tr_b16 a[116:117],v50 offset:16648
v_mul_f32 v130,s71,v130
v_mfma_f32_16x16x32_bf16 v[132:135],a[88:91],a[224:227],v[132:135]
v_mul_f32 v131,s71,v131
v_cvt_pk_bf16_f32 v128,v128,v129
v_mfma_f32_16x16x32_bf16 v[132:135],a[92:95],a[228:231],v[132:135]
ds_read_b64_tr_b16 a[118:119],v50 offset:18696
v_cvt_pk_bf16_f32 v129,v130,v131
v_mfma_f32_16x16x32_bf16 v[164:167],a[92:95],a[236:239],v[164:167]
ds_read_b64_tr_b16 a[120:121],v50 offset:1288
v_mul_f32 v192,s71,v192
v_mfma_f32_16x16x32_bf16 v[164:167],a[88:91],a[232:235],v[164:167]
ds_read_b64_tr_b16 a[122:123],v50 offset:3336
v_mul_f32 v193,s71,v193
v_mfma_f32_16x16x32_bf16 v[196:199],a[88:91],a[240:243],v[196:199]
v_mul_f32 v132,s71,v132
v_mul_f32 v133,s71,v133
v_mfma_f32_16x16x32_bf16 v[196:199],a[92:95],a[244:247],v[196:199]
ds_read_b64_tr_b16 a[124:125],v50 offset:17672
v_mul_f32 v134,s71,v134
v_mfma_f32_16x16x32_bf16 v[228:231],a[92:95],a[252:255],v[228:231]
s_add_u32 s10,s24,s74
s_addc_u32 s11,s25,0
ds_read_b64_tr_b16 a[126:127],v50 offset:19720
v_mfma_f32_16x16x32_bf16 v[228:231],a[88:91],a[248:251],v[228:231]
s_add_u32 s74,s74,16384
s_and_b32 s74,s74,0x3ffff
ds_read_b64_tr_b16 a[128:129],v50 offset:512
v_mfma_f32_16x16x32_bf16 v[136:139],a[96:99],a[192:195],v[136:139]
s_add_u32 m0,s76,16384
s_or_b32 s74,s74,s69
s_sub_u32 s76,s76,32768
v_mfma_f32_16x16x32_bf16 v[136:139],a[100:103],a[196:199],v[136:139]
s_and_b32 s76,s76,0x1ffff
s_and_b32 s69,s75,0xfffc0000
ds_read_b64_tr_b16 a[130:131],v50 offset:2560
s_waitcnt lgkmcnt(6)
v_mfma_f32_16x16x32_bf16 v[136:139],a[104:107],a[224:227],v[136:139]
global_load_lds_dwordx4 v4,s[10:11]
ds_read_b64_tr_b16 a[132:133],v50 offset:16896
v_mfma_f32_16x16x32_bf16 v[136:139],a[108:111],a[228:231],v[136:139]
s_add_u32 s75,s75,16384
ds_read_b64_tr_b16 a[134:135],v50 offset:18944
v_mul_f32 v135,s71,v135
v_mfma_f32_16x16x32_bf16 v[168:171],a[108:111],a[236:239],v[168:171]
v_cvt_pk_bf16_f32 v130,v132,v133
v_cvt_pk_bf16_f32 v131,v134,v135
v_mfma_f32_16x16x32_bf16 v[168:171],a[104:107],a[232:235],v[168:171]
ds_read_b64_tr_b16 a[136:137],v50 offset:1536
v_mul_f32 v194,s71,v194
v_mfma_f32_16x16x32_bf16 v[168:171],a[100:103],a[204:207],v[168:171]
ds_read_b64_tr_b16 a[138:139],v50 offset:3584
v_mul_f32 v195,s71,v195
v_mfma_f32_16x16x32_bf16 v[168:171],a[96:99],a[200:203],v[168:171]
ds_read_b64_tr_b16 a[140:141],v50 offset:17920
v_mul_f32 v196,s71,v196
v_mfma_f32_16x16x32_bf16 v[200:203],a[96:99],a[208:211],v[200:203]
v_mul_f32 v197,s71,v197
v_mul_f32 v198,s71,v198
v_mfma_f32_16x16x32_bf16 v[200:203],a[100:103],a[212:215],v[200:203]
ds_read_b64_tr_b16 a[142:143],v50 offset:19968
v_mul_f32 v199,s71,v199
v_mfma_f32_16x16x32_bf16 v[200:203],a[104:107],a[240:243],v[200:203]
ds_read_b64_tr_b16 a[144:145],v50 offset:520
v_cvt_pk_bf16_f32 v192,v192,v193
v_mfma_f32_16x16x32_bf16 v[200:203],a[108:111],a[244:247],v[200:203]
ds_read_b64_tr_b16 a[146:147],v50 offset:2568
v_cvt_pk_bf16_f32 v193,v194,v195
v_mfma_f32_16x16x32_bf16 v[232:235],a[108:111],a[252:255],v[232:235]
v_cvt_pk_bf16_f32 v194,v196,v197
v_cvt_pk_bf16_f32 v195,v198,v199
v_mfma_f32_16x16x32_bf16 v[232:235],a[104:107],a[248:251],v[232:235]
ds_read_b64_tr_b16 a[148:149],v50 offset:16904
v_mul_f32 v136,s71,v136
v_mfma_f32_16x16x32_bf16 v[232:235],a[100:103],a[220:223],v[232:235]
ds_read_b64_tr_b16 a[150:151],v50 offset:18952
v_mul_f32 v137,s71,v137
v_mfma_f32_16x16x32_bf16 v[232:235],a[96:99],a[216:219],v[232:235]
ds_read_b64_tr_b16 a[152:153],v50 offset:1544
v_mul_f32 v138,s71,v138
v_mfma_f32_16x16x32_bf16 v[140:143],a[112:115],a[192:195],v[140:143]
v_mul_f32 v139,s71,v139
v_cvt_pk_bf16_f32 v136,v136,v137
v_mfma_f32_16x16x32_bf16 v[140:143],a[116:119],a[196:199],v[140:143]
s_waitcnt lgkmcnt(5)
ds_read_b64_tr_b16 a[154:155],v50 offset:3592
v_cvt_pk_bf16_f32 v137,v138,v139
v_mfma_f32_16x16x32_bf16 v[140:143],a[120:123],a[224:227],v[140:143]
global_load_lds_dwordx4 v5,s[10:11] offset:1024
ds_read_b64_tr_b16 a[156:157],v50 offset:17928
v_mfma_f32_16x16x32_bf16 v[140:143],a[124:127],a[228:231],v[140:143]
ds_read_b64_tr_b16 a[158:159],v50 offset:19976
v_mul_f32 v160,s71,v160
v_mfma_f32_16x16x32_bf16 v[172:175],a[124:127],a[236:239],v[172:175]
v_mul_f32 v161,s71,v161
v_mul_f32 v162,s71,v162
v_mfma_f32_16x16x32_bf16 v[172:175],a[120:123],a[232:235],v[172:175]
ds_read_b64_tr_b16 a[160:161],v50 offset:768
v_mul_f32 v163,s71,v163
v_mfma_f32_16x16x32_bf16 v[172:175],a[116:119],a[204:207],v[172:175]
ds_read_b64_tr_b16 a[162:163],v50 offset:2816
v_mul_f32 v140,s71,v140
v_mfma_f32_16x16x32_bf16 v[172:175],a[112:115],a[200:203],v[172:175]
ds_read_b64_tr_b16 a[164:165],v50 offset:17152
v_mul_f32 v141,s71,v141
v_mfma_f32_16x16x32_bf16 v[204:207],a[112:115],a[208:211],v[204:207]
v_mul_f32 v142,s71,v142
v_mul_f32 v143,s71,v143
v_mfma_f32_16x16x32_bf16 v[204:207],a[116:119],a[212:215],v[204:207]
ds_read_b64_tr_b16 a[166:167],v50 offset:19200
v_cvt_pk_bf16_f32 v138,v140,v141
v_mfma_f32_16x16x32_bf16 v[204:207],a[120:123],a[240:243],v[204:207]
ds_read_b64_tr_b16 a[168:169],v50 offset:1792
v_cvt_pk_bf16_f32 v139,v142,v143
v_mfma_f32_16x16x32_bf16 v[204:207],a[124:127],a[244:247],v[204:207]
)A"
R"A(ds_read_b64_tr_b16 a[170:171],v50 offset:3840
v_mul_f32 v164,s71,v164
v_mfma_f32_16x16x32_bf16 v[236:239],a[124:127],a[252:255],v[236:239]
v_mul_f32 v165,s71,v165
v_mul_f32 v166,s71,v166
v_mfma_f32_16x16x32_bf16 v[236:239],a[120:123],a[248:251],v[236:239]
ds_read_b64_tr_b16 a[172:173],v50 offset:18176
v_mul_f32 v167,s71,v167
v_mfma_f32_16x16x32_bf16 v[236:239],a[116:119],a[220:223],v[236:239]
ds_read_b64_tr_b16 a[174:175],v50 offset:20224
v_cvt_pk_bf16_f32 v160,v160,v161
v_mfma_f32_16x16x32_bf16 v[236:239],a[112:115],a[216:219],v[236:239]
ds_read_b64_tr_b16 a[176:177],v50 offset:776
v_cvt_pk_bf16_f32 v161,v162,v163
v_mfma_f32_16x16x32_bf16 v[144:147],a[128:131],a[192:195],v[144:147]
v_cvt_pk_bf16_f32 v162,v164,v165
v_cvt_pk_bf16_f32 v163,v166,v167
v_mfma_f32_16x16x32_bf16 v[144:147],a[132:135],a[196:199],v[144:147]
ds_read_b64_tr_b16 a[178:179],v50 offset:2824
v_mul_f32 v224,s71,v224
v_mfma_f32_16x16x32_bf16 v[144:147],a[136:139],a[224:227],v[144:147]
global_load_lds_dwordx4 v4,s[10:11] offset:2048
ds_read_b64_tr_b16 a[180:181],v50 offset:17160
v_mfma_f32_16x16x32_bf16 v[144:147],a[140:143],a[228:231],v[144:147]
ds_read_b64_tr_b16 a[182:183],v50 offset:19208
v_mul_f32 v225,s71,v225
v_mfma_f32_16x16x32_bf16 v[176:179],a[140:143],a[236:239],v[176:179]
v_mul_f32 v226,s71,v226
v_mul_f32 v227,s71,v227
v_mfma_f32_16x16x32_bf16 v[176:179],a[136:139],a[232:235],v[176:179]
ds_read_b64_tr_b16 a[184:185],v50 offset:1800
v_mul_f32 v228,s71,v228
v_mfma_f32_16x16x32_bf16 v[176:179],a[132:135],a[204:207],v[176:179]
ds_read_b64_tr_b16 a[186:187],v50 offset:3848
v_mul_f32 v144,s71,v144
v_mfma_f32_16x16x32_bf16 v[176:179],a[128:131],a[200:203],v[176:179]
ds_read_b64_tr_b16 a[188:189],v50 offset:18184
v_mul_f32 v145,s71,v145
v_mfma_f32_16x16x32_bf16 v[208:211],a[128:131],a[208:211],v[208:211]
v_mul_f32 v146,s71,v146
v_mul_f32 v147,s71,v147
v_mfma_f32_16x16x32_bf16 v[208:211],a[132:135],a[212:215],v[208:211]
ds_read_b64_tr_b16 a[190:191],v50 offset:20232
v_cvt_pk_bf16_f32 v144,v144,v145
v_mfma_f32_16x16x32_bf16 v[208:211],a[136:139],a[240:243],v[208:211]
v_cvt_pk_bf16_f32 v145,v146,v147
v_mul_f32 v229,s71,v229
v_mfma_f32_16x16x32_bf16 v[208:211],a[140:143],a[244:247],v[208:211]
v_mul_f32 v230,s71,v230
v_mul_f32 v231,s71,v231
v_mfma_f32_16x16x32_bf16 v[240:243],a[140:143],a[252:255],v[240:243]
v_cvt_pk_bf16_f32 v224,v224,v225
v_cvt_pk_bf16_f32 v225,v226,v227
v_mfma_f32_16x16x32_bf16 v[240:243],a[136:139],a[248:251],v[240:243]
v_cvt_pk_bf16_f32 v226,v228,v229
v_cvt_pk_bf16_f32 v227,v230,v231
v_mfma_f32_16x16x32_bf16 v[240:243],a[132:135],a[220:223],v[240:243]
v_mul_f32 v168,s71,v168
v_mul_f32 v169,s71,v169
v_mfma_f32_16x16x32_bf16 v[240:243],a[128:131],a[216:219],v[240:243]
v_mul_f32 v170,s71,v170
v_mul_f32 v171,s71,v171
s_waitcnt vmcnt(7)
s_barrier
s_waitcnt lgkmcnt(1)
v_mfma_f32_16x16x32_bf16 v[148:151],a[144:147],a[192:195],v[148:151]
ds_read_b64_tr_b16 a[64:65],v52
v_mul_f32 v172,s71,v172
v_mfma_f32_16x16x32_bf16 v[148:151],a[148:151],a[196:199],v[148:151]
global_load_lds_dwordx4 v5,s[10:11] offset:3072
ds_read_b64_tr_b16 a[66:67],v52 offset:2048
v_mfma_f32_16x16x32_bf16 v[148:151],a[152:155],a[224:227],v[148:151]
ds_read_b64_tr_b16 a[68:69],v52 offset:16384
v_mul_f32 v173,s71,v173
v_mfma_f32_16x16x32_bf16 v[148:151],a[156:159],a[228:231],v[148:151]
ds_read_b64_tr_b16 a[70:71],v52 offset:18432
v_mul_f32 v174,s71,v174
v_mfma_f32_16x16x32_bf16 v[180:183],a[156:159],a[236:239],v[180:183]
v_mul_f32 v175,s71,v175
v_cvt_pk_bf16_f32 v168,v168,v169
v_mfma_f32_16x16x32_bf16 v[180:183],a[152:155],a[232:235],v[180:183]
ds_read_b64_tr_b16 a[72:73],v52 offset:1024
v_cvt_pk_bf16_f32 v169,v170,v171
L_pd3_%=:
s_cbranch_execz L_pd7_%=
v_mfma_f32_16x16x32_bf16 v[180:183],a[148:151],a[204:207],v[180:183]
ds_read_b64_tr_b16 a[74:75],v52 offset:3072
v_mul_f32 v148,s71,v148
v_mfma_f32_16x16x32_bf16 v[180:183],a[144:147],a[200:203],v[180:183]
ds_read_b64_tr_b16 a[76:77],v52 offset:17408
v_mul_f32 v149,s71,v149
v_mfma_f32_16x16x32_bf16 v[212:215],a[144:147],a[208:211],v[212:215]
v_mul_f32 v150,s71,v150
v_mul_f32 v151,s71,v151
v_mfma_f32_16x16x32_bf16 v[212:215],a[148:151],a[212:215],v[212:215]
ds_read_b64_tr_b16 a[78:79],v52 offset:19456
v_cvt_pk_bf16_f32 v146,v148,v149
v_mfma_f32_16x16x32_bf16 v[212:215],a[152:155],a[240:243],v[212:215]
ds_read_b64_tr_b16 a[80:81],v52 offset:8
v_cvt_pk_bf16_f32 v147,v150,v151
v_mfma_f32_16x16x32_bf16 v[212:215],a[156:159],a[244:247],v[212:215]
ds_read_b64_tr_b16 a[82:83],v52 offset:2056
v_cvt_pk_bf16_f32 v170,v172,v173
v_mfma_f32_16x16x32_bf16 v[244:247],a[156:159],a[252:255],v[244:247]
v_cvt_pk_bf16_f32 v171,v174,v175
v_mul_f32 v176,s71,v176
v_mfma_f32_16x16x32_bf16 v[244:247],a[152:155],a[248:251],v[244:247]
ds_read_b64_tr_b16 a[84:85],v52 offset:16392
v_mul_f32 v177,s71,v177
v_mfma_f32_16x16x32_bf16 v[244:247],a[148:151],a[220:223],v[244:247]
s_add_u32 s10,s20,s74
s_addc_u32 s11,s21,0
ds_read_b64_tr_b16 a[86:87],v52 offset:18440
v_mfma_f32_16x16x32_bf16 v[244:247],a[144:147],a[216:219],v[244:247]
ds_read_b64_tr_b16 a[88:89],v52 offset:1032
v_mul_f32 v178,s71,v178
v_mfma_f32_16x16x32_bf16 v[152:155],a[160:163],a[192:195],v[152:155]
s_add_u32 m0,s76,0
v_mul_f32 v179,s71,v179
v_mul_f32 v180,s71,v180
v_mfma_f32_16x16x32_bf16 v[152:155],a[164:167],a[196:199],v[152:155]
ds_read_b64_tr_b16 a[90:91],v52 offset:3080
v_mul_f32 v181,s71,v181
v_mfma_f32_16x16x32_bf16 v[152:155],a[168:171],a[224:227],v[152:155]
global_load_lds_dwordx4 v4,s[10:11] nt
ds_read_b64_tr_b16 a[92:93],v52 offset:17416
v_mfma_f32_16x16x32_bf16 v[152:155],a[172:175],a[228:231],v[152:155]
ds_read_b64_tr_b16 a[94:95],v52 offset:19464
v_mul_f32 v182,s71,v182
v_mfma_f32_16x16x32_bf16 v[184:187],a[172:175],a[236:239],v[184:187]
v_mul_f32 v183,s71,v183
v_cvt_pk_bf16_f32 v176,v176,v177
v_mfma_f32_16x16x32_bf16 v[184:187],a[168:171],a[232:235],v[184:187]
ds_read_b64_tr_b16 a[96:97],v52 offset:256
v_cvt_pk_bf16_f32 v177,v178,v179
v_mfma_f32_16x16x32_bf16 v[184:187],a[164:167],a[204:207],v[184:187]
ds_read_b64_tr_b16 a[98:99],v52 offset:2304
v_mul_f32 v152,s71,v152
v_mfma_f32_16x16x32_bf16 v[184:187],a[160:163],a[200:203],v[184:187]
ds_read_b64_tr_b16 a[100:101],v52 offset:16640
v_mul_f32 v153,s71,v153
v_mfma_f32_16x16x32_bf16 v[216:219],a[160:163],a[208:211],v[216:219]
v_mul_f32 v154,s71,v154
v_mul_f32 v155,s71,v155
v_mfma_f32_16x16x32_bf16 v[216:219],a[164:167],a[212:215],v[216:219]
ds_read_b64_tr_b16 a[102:103],v52 offset:18688
v_cvt_pk_bf16_f32 v152,v152,v153
v_mfma_f32_16x16x32_bf16 v[216:219],a[168:171],a[240:243],v[216:219]
ds_read_b64_tr_b16 a[104:105],v52 offset:1280
v_cvt_pk_bf16_f32 v153,v154,v155
v_mfma_f32_16x16x32_bf16 v[216:219],a[172:175],a[244:247],v[216:219]
ds_read_b64_tr_b16 a[106:107],v52 offset:3328
v_cvt_pk_bf16_f32 v178,v180,v181
v_mfma_f32_16x16x32_bf16 v[248:251],a[172:175],a[252:255],v[248:251]
v_cvt_pk_bf16_f32 v179,v182,v183
v_mul_f32 v184,s71,v184
v_mfma_f32_16x16x32_bf16 v[248:251],a[168:171],a[248:251],v[248:251]
ds_read_b64_tr_b16 a[108:109],v52 offset:17664
v_mul_f32 v185,s71,v185
v_mfma_f32_16x16x32_bf16 v[248:251],a[164:167],a[220:223],v[248:251]
ds_read_b64_tr_b16 a[110:111],v52 offset:19712
v_mul_f32 v186,s71,v186
v_mfma_f32_16x16x32_bf16 v[248:251],a[160:163],a[216:219],v[248:251]
ds_read_b64_tr_b16 a[112:113],v52 offset:264
v_mul_f32 v187,s71,v187
v_mfma_f32_16x16x32_bf16 v[156:159],a[176:179],a[192:195],v[156:159]
v_cvt_pk_bf16_f32 v184,v184,v185
v_cvt_pk_bf16_f32 v185,v186,v187
v_mfma_f32_16x16x32_bf16 v[156:159],a[180:183],a[196:199],v[156:159]
global_load_lds_dwordx4 v4,s[10:11] offset:1024 nt
ds_read_b64_tr_b16 a[114:115],v52 offset:2312
v_mfma_f32_16x16x32_bf16 v[156:159],a[184:187],a[224:227],v[156:159]
s_waitcnt lgkmcnt(5)
ds_read_b64_tr_b16 a[116:117],v52 offset:16648
v_mul_f32 v200,s71,v200
v_mfma_f32_16x16x32_bf16 v[156:159],a[188:191],a[228:231],v[156:159]
ds_read_b64_tr_b16 a[118:119],v52 offset:18696
v_mul_f32 v201,s71,v201
v_mfma_f32_16x16x32_bf16 v[188:191],a[188:191],a[236:239],v[188:191]
v_mul_f32 v202,s71,v202
v_mul_f32 v203,s71,v203
v_mfma_f32_16x16x32_bf16 v[188:191],a[184:187],a[232:235],v[188:191]
ds_read_b64_tr_b16 a[120:121],v52 offset:1288
v_mul_f32 v204,s71,v204
v_mfma_f32_16x16x32_bf16 v[188:191],a[180:183],a[204:207],v[188:191]
ds_read_b64_tr_b16 a[122:123],v52 offset:3336
v_mul_f32 v156,s71,v156
v_mfma_f32_16x16x32_bf16 v[188:191],a[176:179],a[200:203],v[188:191]
ds_read_b64_tr_b16 a[124:125],v52 offset:17672
v_mul_f32 v157,s71,v157
v_mfma_f32_16x16x32_bf16 v[220:223],a[176:179],a[208:211],v[220:223]
v_mul_f32 v158,s71,v158
v_mul_f32 v159,s71,v159
v_mfma_f32_16x16x32_bf16 v[220:223],a[180:183],a[212:215],v[220:223]
ds_read_b64_tr_b16 a[126:127],v52 offset:19720
v_cvt_pk_bf16_f32 v154,v156,v157
v_mfma_f32_16x16x32_bf16 v[220:223],a[184:187],a[240:243],v[220:223]
v_cvt_pk_bf16_f32 v155,v158,v159
v_mul_f32 v188,s71,v188
v_mfma_f32_16x16x32_bf16 v[220:223],a[188:191],a[244:247],v[220:223]
v_mul_f32 v189,s71,v189
v_mul_f32 v190,s71,v190
v_mfma_f32_16x16x32_bf16 v[252:255],a[188:191],a[252:255],v[252:255]
v_mul_f32 v191,s71,v191
v_cvt_pk_bf16_f32 v186,v188,v189
v_mfma_f32_16x16x32_bf16 v[252:255],a[184:187],a[248:251],v[252:255]
v_cvt_pk_bf16_f32 v187,v190,v191
v_mul_f32 v205,s71,v205
v_mfma_f32_16x16x32_bf16 v[252:255],a[180:183],a[220:223],v[252:255]
v_mul_f32 v206,s71,v206
v_mul_f32 v207,s71,v207
v_mfma_f32_16x16x32_bf16 v[252:255],a[176:179],a[216:219],v[252:255]
v_cvt_pk_bf16_f32 v200,v200,v201
v_cvt_pk_bf16_f32 v201,v202,v203
v_mfma_f32_16x16x32_bf16 a[0:3],a[64:67],a[192:195],a[0:3]
global_load_lds_dwordx4 v5,s[10:11] offset:2048 nt
v_cvt_pk_bf16_f32 v202,v204,v205
v_mfma_f32_16x16x32_bf16 a[0:3],a[68:71],a[196:199],a[0:3]
v_cvt_pk_bf16_f32 v203,v206,v207
v_mul_f32 v208,s71,v208
v_mfma_f32_16x16x32_bf16 a[0:3],a[72:75],a[224:227],a[0:3]
v_mul_f32 v209,s71,v209
v_mul_f32 v210,s71,v210
v_mfma_f32_16x16x32_bf16 a[0:3],a[76:79],a[228:231],a[0:3]
v_mul_f32 v211,s71,v211
v_mul_f32 v212,s71,v212
v_mfma_f32_16x16x32_bf16 a[16:19],a[76:79],a[236:239],a[16:19]
v_mul_f32 v213,s71,v213
v_mul_f32 v214,s71,v214
v_mfma_f32_16x16x32_bf16 a[16:19],a[72:75],a[232:235],a[16:19]
v_mul_f32 v215,s71,v215
v_cvt_pk_bf16_f32 v208,v208,v209
v_mfma_f32_16x16x32_bf16 a[16:19],a[68:71],a[204:207],a[16:19]
v_accvgpr_read_b32 v8,a0
v_accvgpr_read_b32 v9,a1
v_mfma_f32_16x16x32_bf16 a[16:19],a[64:67],a[200:203],a[16:19]
v_accvgpr_read_b32 v10,a2
v_accvgpr_read_b32 v11,a3
v_mfma_f32_16x16x32_bf16 a[32:35],a[64:67],a[208:211],a[32:35]
v_mul_f32 v8,s71,v8
v_mul_f32 v9,s71,v9
v_mfma_f32_16x16x32_bf16 a[32:35],a[68:71],a[212:215],a[32:35]
v_mul_f32 v10,s71,v10
v_mul_f32 v11,s71,v11
v_mfma_f32_16x16x32_bf16 a[32:35],a[72:75],a[240:243],a[32:35]
v_cvt_pk_bf16_f32 v132,v8,v9
v_cvt_pk_bf16_f32 v133,v10,v11
v_mfma_f32_16x16x32_bf16 a[32:35],a[76:79],a[244:247],a[32:35]
v_accvgpr_read_b32 v16,a16
v_accvgpr_read_b32 v17,a17
v_mfma_f32_16x16x32_bf16 a[48:51],a[76:79],a[252:255],a[48:51]
ds_read_b64_tr_b16 a[76:77],v52 offset:17920
v_accvgpr_read_b32 v18,a18
v_mfma_f32_16x16x32_bf16 a[48:51],a[72:75],a[248:251],a[48:51]
ds_read_b64_tr_b16 a[72:73],v52 offset:1536
v_accvgpr_read_b32 v19,a19
v_mfma_f32_16x16x32_bf16 a[48:51],a[68:71],a[220:223],a[48:51]
ds_read_b64_tr_b16 a[68:69],v52 offset:16896
v_accvgpr_read_b32 v8,a32
v_mfma_f32_16x16x32_bf16 a[48:51],a[64:67],a[216:219],a[48:51]
global_load_lds_dwordx4 v5,s[10:11] offset:3072
ds_read_b64_tr_b16 a[64:65],v52 offset:512
v_mfma_f32_16x16x32_bf16 a[4:7],a[80:83],a[192:195],a[4:7]
v_accvgpr_read_b32 v9,a33
v_accvgpr_read_b32 v10,a34
v_mfma_f32_16x16x32_bf16 a[4:7],a[84:87],a[196:199],a[4:7]
ds_read_b64_tr_b16 a[66:67],v52 offset:2560
v_accvgpr_read_b32 v11,a35
v_mfma_f32_16x16x32_bf16 a[4:7],a[88:91],a[224:227],a[4:7]
ds_read_b64_tr_b16 a[70:71],v52 offset:18944
v_mul_f32 v8,s71,v8
v_mfma_f32_16x16x32_bf16 a[4:7],a[92:95],a[228:231],a[4:7]
ds_read_b64_tr_b16 a[74:75],v52 offset:3584
v_mul_f32 v9,s71,v9
v_mfma_f32_16x16x32_bf16 a[20:23],a[92:95],a[236:239],a[20:23]
v_mul_f32 v10,s71,v10
v_mul_f32 v11,s71,v11
v_mfma_f32_16x16x32_bf16 a[20:23],a[88:91],a[232:235],a[20:23]
)A"
R"A(ds_read_b64_tr_b16 a[78:79],v52 offset:19968
v_cvt_pk_bf16_f32 v196,v8,v9
v_mfma_f32_16x16x32_bf16 a[20:23],a[84:87],a[204:207],a[20:23]
v_accvgpr_read_b32 v12,a4
v_accvgpr_read_b32 v13,a5
v_mfma_f32_16x16x32_bf16 a[20:23],a[80:83],a[200:203],a[20:23]
v_accvgpr_read_b32 v14,a6
v_accvgpr_read_b32 v15,a7
v_mfma_f32_16x16x32_bf16 a[36:39],a[80:83],a[208:211],a[36:39]
v_mul_f32 v12,s71,v12
v_mul_f32 v13,s71,v13
v_mfma_f32_16x16x32_bf16 a[36:39],a[84:87],a[212:215],a[36:39]
v_mul_f32 v14,s71,v14
v_mul_f32 v15,s71,v15
v_mfma_f32_16x16x32_bf16 a[36:39],a[88:91],a[240:243],a[36:39]
s_add_u32 s10,s24,s74
s_addc_u32 s11,s25,0
s_add_u32 s74,s74,16384
v_mfma_f32_16x16x32_bf16 a[36:39],a[92:95],a[244:247],a[36:39]
s_and_b32 s74,s74,0x3ffff
s_or_b32 s74,s74,s69
v_cvt_pk_bf16_f32 v134,v12,v13
v_mfma_f32_16x16x32_bf16 a[52:55],a[92:95],a[252:255],a[52:55]
s_add_u32 m0,s76,16384
s_sub_u32 s76,s76,32768
ds_read_b64_tr_b16 a[92:93],v52 offset:17928
v_mfma_f32_16x16x32_bf16 a[52:55],a[88:91],a[248:251],a[52:55]
s_and_b32 s76,s76,0x1ffff
ds_read_b64_tr_b16 a[88:89],v52 offset:1544
v_cvt_pk_bf16_f32 v135,v14,v15
v_mfma_f32_16x16x32_bf16 a[52:55],a[84:87],a[220:223],a[52:55]
global_load_lds_dwordx4 v4,s[10:11]
ds_read_b64_tr_b16 a[84:85],v52 offset:16904
v_mfma_f32_16x16x32_bf16 a[52:55],a[80:83],a[216:219],a[52:55]
s_and_b32 s69,s75,0xfffc0000
ds_read_b64_tr_b16 a[80:81],v52 offset:520
v_accvgpr_read_b32 v12,a36
v_mfma_f32_16x16x32_bf16 a[8:11],a[96:99],a[192:195],a[8:11]
s_add_u32 s75,s75,16384
v_accvgpr_read_b32 v13,a37
v_accvgpr_read_b32 v14,a38
v_mfma_f32_16x16x32_bf16 a[8:11],a[100:103],a[196:199],a[8:11]
s_waitcnt lgkmcnt(4)
ds_read_b64_tr_b16 a[82:83],v52 offset:2568
v_accvgpr_read_b32 v15,a39
v_mfma_f32_16x16x32_bf16 a[8:11],a[104:107],a[224:227],a[8:11]
ds_read_b64_tr_b16 a[86:87],v52 offset:18952
v_mul_f32 v12,s71,v12
v_mfma_f32_16x16x32_bf16 a[8:11],a[108:111],a[228:231],a[8:11]
ds_read_b64_tr_b16 a[90:91],v52 offset:3592
v_mul_f32 v13,s71,v13
v_mfma_f32_16x16x32_bf16 a[24:27],a[108:111],a[236:239],a[24:27]
v_mul_f32 v14,s71,v14
v_mul_f32 v15,s71,v15
v_mfma_f32_16x16x32_bf16 a[24:27],a[104:107],a[232:235],a[24:27]
ds_read_b64_tr_b16 a[94:95],v52 offset:19976
v_cvt_pk_bf16_f32 v197,v10,v11
v_mfma_f32_16x16x32_bf16 a[24:27],a[100:103],a[204:207],a[24:27]
v_cvt_pk_bf16_f32 v198,v12,v13
v_cvt_pk_bf16_f32 v199,v14,v15
v_mfma_f32_16x16x32_bf16 a[24:27],a[96:99],a[200:203],a[24:27]
v_accvgpr_read_b32 v8,a8
v_accvgpr_read_b32 v9,a9
v_mfma_f32_16x16x32_bf16 a[40:43],a[96:99],a[208:211],a[40:43]
v_accvgpr_read_b32 v10,a10
v_accvgpr_read_b32 v11,a11
v_mfma_f32_16x16x32_bf16 a[40:43],a[100:103],a[212:215],a[40:43]
v_mul_f32 v8,s71,v8
v_mul_f32 v9,s71,v9
v_mfma_f32_16x16x32_bf16 a[40:43],a[104:107],a[240:243],a[40:43]
v_mul_f32 v10,s71,v10
v_mul_f32 v11,s71,v11
v_mfma_f32_16x16x32_bf16 a[40:43],a[108:111],a[244:247],a[40:43]
v_cvt_pk_bf16_f32 v140,v8,v9
v_cvt_pk_bf16_f32 v141,v10,v11
v_mfma_f32_16x16x32_bf16 a[56:59],a[108:111],a[252:255],a[56:59]
ds_read_b64_tr_b16 a[108:109],v52 offset:18176
v_accvgpr_read_b32 v20,a20
v_mfma_f32_16x16x32_bf16 a[56:59],a[104:107],a[248:251],a[56:59]
global_load_lds_dwordx4 v5,s[10:11] offset:1024
ds_read_b64_tr_b16 a[104:105],v52 offset:1792
v_mfma_f32_16x16x32_bf16 a[56:59],a[100:103],a[220:223],a[56:59]
ds_read_b64_tr_b16 a[100:101],v52 offset:17152
v_accvgpr_read_b32 v21,a21
v_mfma_f32_16x16x32_bf16 a[56:59],a[96:99],a[216:219],a[56:59]
ds_read_b64_tr_b16 a[96:97],v52 offset:768
v_accvgpr_read_b32 v22,a22
v_mfma_f32_16x16x32_bf16 a[12:15],a[112:115],a[192:195],a[12:15]
v_accvgpr_read_b32 v23,a23
v_mul_f32 v16,s71,v16
v_mfma_f32_16x16x32_bf16 a[12:15],a[116:119],a[196:199],a[12:15]
ds_read_b64_tr_b16 a[98:99],v52 offset:2816
v_mul_f32 v17,s71,v17
v_mfma_f32_16x16x32_bf16 a[12:15],a[120:123],a[224:227],a[12:15]
ds_read_b64_tr_b16 a[102:103],v52 offset:19200
v_mul_f32 v18,s71,v18
v_mfma_f32_16x16x32_bf16 a[12:15],a[124:127],a[228:231],a[12:15]
ds_read_b64_tr_b16 a[106:107],v52 offset:3840
v_mul_f32 v19,s71,v19
v_mfma_f32_16x16x32_bf16 a[28:31],a[124:127],a[236:239],a[28:31]
v_mul_f32 v20,s71,v20
v_mul_f32 v21,s71,v21
v_mfma_f32_16x16x32_bf16 a[28:31],a[120:123],a[232:235],a[28:31]
ds_read_b64_tr_b16 a[110:111],v52 offset:20224
v_mul_f32 v22,s71,v22
v_mfma_f32_16x16x32_bf16 a[28:31],a[116:119],a[204:207],a[28:31]
v_accvgpr_read_b32 v12,a12
v_accvgpr_read_b32 v13,a13
v_mfma_f32_16x16x32_bf16 a[28:31],a[112:115],a[200:203],a[28:31]
v_accvgpr_read_b32 v14,a14
v_accvgpr_read_b32 v15,a15
v_mfma_f32_16x16x32_bf16 a[44:47],a[112:115],a[208:211],a[44:47]
v_mul_f32 v12,s71,v12
v_mul_f32 v13,s71,v13
v_mfma_f32_16x16x32_bf16 a[44:47],a[116:119],a[212:215],a[44:47]
v_mul_f32 v14,s71,v14
v_mul_f32 v15,s71,v15
v_mfma_f32_16x16x32_bf16 a[44:47],a[120:123],a[240:243],a[44:47]
v_cvt_pk_bf16_f32 v142,v12,v13
v_cvt_pk_bf16_f32 v143,v14,v15
v_mfma_f32_16x16x32_bf16 a[44:47],a[124:127],a[244:247],a[44:47]
v_mul_f32 v23,s71,v23
v_cvt_pk_bf16_f32 v164,v16,v17
v_mfma_f32_16x16x32_bf16 a[60:63],a[124:127],a[252:255],a[60:63]
global_load_lds_dwordx4 v4,s[10:11] offset:2048
ds_read_b64_tr_b16 a[124:125],v52 offset:18184
v_mfma_f32_16x16x32_bf16 a[60:63],a[120:123],a[248:251],a[60:63]
ds_read_b64_tr_b16 a[120:121],v52 offset:1800
v_cvt_pk_bf16_f32 v165,v18,v19
v_mfma_f32_16x16x32_bf16 a[60:63],a[116:119],a[220:223],a[60:63]
ds_read_b64_tr_b16 a[116:117],v52 offset:17160
v_cvt_pk_bf16_f32 v166,v20,v21
v_mfma_f32_16x16x32_bf16 a[60:63],a[112:115],a[216:219],a[60:63]
ds_read_b64_tr_b16 a[112:113],v52 offset:776
v_cvt_pk_bf16_f32 v167,v22,v23
v_mfma_f32_16x16x32_bf16 v[64:67],a[64:67],a[192:195],v[64:67]
v_accvgpr_read_b32 v16,a48
v_accvgpr_read_b32 v17,a49
v_mfma_f32_16x16x32_bf16 v[64:67],a[68:71],a[196:199],v[64:67]
ds_read_b64_tr_b16 a[114:115],v52 offset:2824
v_accvgpr_read_b32 v18,a50
v_mfma_f32_16x16x32_bf16 v[64:67],a[72:75],a[224:227],v[64:67]
ds_read_b64_tr_b16 a[118:119],v52 offset:19208
v_accvgpr_read_b32 v19,a51
v_mfma_f32_16x16x32_bf16 v[64:67],a[76:79],a[228:231],v[64:67]
ds_read_b64_tr_b16 a[122:123],v52 offset:3848
v_accvgpr_read_b32 v20,a52
v_mfma_f32_16x16x32_bf16 v[80:83],a[76:79],a[236:239],v[80:83]
v_accvgpr_read_b32 v21,a53
v_accvgpr_read_b32 v22,a54
v_mfma_f32_16x16x32_bf16 v[80:83],a[72:75],a[232:235],v[80:83]
ds_read_b64_tr_b16 a[126:127],v52 offset:20232
v_accvgpr_read_b32 v23,a55
v_mfma_f32_16x16x32_bf16 v[80:83],a[68:71],a[204:207],v[80:83]
v_mul_f32 v64,s71,v64
v_mul_f32 v65,s71,v65
v_mfma_f32_16x16x32_bf16 v[80:83],a[64:67],a[200:203],v[80:83]
v_mul_f32 v66,s71,v66
v_mul_f32 v67,s71,v67
v_mfma_f32_16x16x32_bf16 v[96:99],a[64:67],a[208:211],v[96:99]
v_cvt_pk_bf16_f32 v148,v64,v65
v_cvt_pk_bf16_f32 v149,v66,v67
v_mfma_f32_16x16x32_bf16 v[96:99],a[68:71],a[212:215],v[96:99]
v_mul_f32 v16,s71,v16
v_mul_f32 v17,s71,v17
v_mfma_f32_16x16x32_bf16 v[96:99],a[72:75],a[240:243],v[96:99]
v_mul_f32 v18,s71,v18
v_mul_f32 v19,s71,v19
v_mfma_f32_16x16x32_bf16 v[96:99],a[76:79],a[244:247],v[96:99]
global_load_lds_dwordx4 v5,s[10:11] offset:3072
v_mul_f32 v20,s71,v20
v_mfma_f32_16x16x32_bf16 v[112:115],a[76:79],a[252:255],v[112:115]
v_mul_f32 v21,s71,v21
v_mul_f32 v22,s71,v22
v_mfma_f32_16x16x32_bf16 v[112:115],a[72:75],a[248:251],v[112:115]
v_mul_f32 v23,s71,v23
v_cvt_pk_bf16_f32 v228,v16,v17
v_mfma_f32_16x16x32_bf16 v[112:115],a[68:71],a[220:223],v[112:115]
v_cvt_pk_bf16_f32 v229,v18,v19
v_cvt_pk_bf16_f32 v230,v20,v21
v_mfma_f32_16x16x32_bf16 v[112:115],a[64:67],a[216:219],v[112:115]
v_cvt_pk_bf16_f32 v231,v22,v23
v_accvgpr_read_b32 v16,a24
s_waitcnt vmcnt(8) lgkmcnt(0)
s_barrier
v_mfma_f32_16x16x32_bf16 v[68:71],a[80:83],a[192:195],v[68:71]
ds_read_b128 a[128:131],v1
v_accvgpr_read_b32 v17,a25
v_mfma_f32_16x16x32_bf16 v[68:71],a[84:87],a[196:199],v[68:71]
ds_read_b128 a[132:135],v1 offset:256
v_accvgpr_read_b32 v18,a26
v_mfma_f32_16x16x32_bf16 v[68:71],a[88:91],a[224:227],v[68:71]
ds_read_b128 a[136:139],v1 offset:512
v_accvgpr_read_b32 v19,a27
v_mfma_f32_16x16x32_bf16 v[68:71],a[92:95],a[228:231],v[68:71]
ds_read_b128 a[140:143],v1 offset:768
v_accvgpr_read_b32 v20,a28
v_mfma_f32_16x16x32_bf16 v[84:87],a[92:95],a[236:239],v[84:87]
v_accvgpr_read_b32 v21,a29
v_accvgpr_read_b32 v22,a30
v_mfma_f32_16x16x32_bf16 v[84:87],a[88:91],a[232:235],v[84:87]
v_accvgpr_read_b32 v23,a31
v_mul_f32 v16,s71,v16
v_mfma_f32_16x16x32_bf16 v[84:87],a[84:87],a[204:207],v[84:87]
s_add_u32 s10,s20,s74
s_addc_u32 s11,s21,0
v_mul_f32 v68,s71,v68
v_mfma_f32_16x16x32_bf16 v[84:87],a[80:83],a[200:203],v[84:87]
v_mul_f32 v69,s71,v69
v_mul_f32 v70,s71,v70
v_mfma_f32_16x16x32_bf16 v[100:103],a[80:83],a[208:211],v[100:103]
s_add_u32 m0,s76,0
v_mul_f32 v71,s71,v71
v_cvt_pk_bf16_f32 v150,v68,v69
v_mfma_f32_16x16x32_bf16 v[100:103],a[84:87],a[212:215],v[100:103]
v_cvt_pk_bf16_f32 v151,v70,v71
v_mul_f32 v17,s71,v17
v_mfma_f32_16x16x32_bf16 v[100:103],a[88:91],a[240:243],v[100:103]
global_load_lds_dwordx4 v4,s[10:11] nt
v_mul_f32 v18,s71,v18
v_mfma_f32_16x16x32_bf16 v[100:103],a[92:95],a[244:247],v[100:103]
v_mul_f32 v19,s71,v19
v_mul_f32 v20,s71,v20
v_mfma_f32_16x16x32_bf16 v[116:119],a[92:95],a[252:255],v[116:119]
v_mul_f32 v21,s71,v21
v_mul_f32 v22,s71,v22
v_mfma_f32_16x16x32_bf16 v[116:119],a[88:91],a[248:251],v[116:119]
v_mul_f32 v23,s71,v23
v_cvt_pk_bf16_f32 v172,v16,v17
v_mfma_f32_16x16x32_bf16 v[116:119],a[84:87],a[220:223],v[116:119]
v_cvt_pk_bf16_f32 v173,v18,v19
v_cvt_pk_bf16_f32 v174,v20,v21
v_mfma_f32_16x16x32_bf16 v[116:119],a[80:83],a[216:219],v[116:119]
v_cvt_pk_bf16_f32 v175,v22,v23
v_mul_f32 v80,s71,v80
v_mfma_f32_16x16x32_bf16 v[72:75],a[96:99],a[192:195],v[72:75]
v_mul_f32 v81,s71,v81
v_mul_f32 v82,s71,v82
v_mfma_f32_16x16x32_bf16 v[72:75],a[100:103],a[196:199],v[72:75]
v_mul_f32 v83,s71,v83
v_mul_f32 v84,s71,v84
v_mfma_f32_16x16x32_bf16 v[72:75],a[104:107],a[224:227],v[72:75]
v_mul_f32 v85,s71,v85
v_mul_f32 v86,s71,v86
v_mfma_f32_16x16x32_bf16 v[72:75],a[108:111],a[228:231],v[72:75]
v_mul_f32 v87,s71,v87
v_cvt_pk_bf16_f32 v180,v80,v81
v_mfma_f32_16x16x32_bf16 v[88:91],a[108:111],a[236:239],v[88:91]
v_cvt_pk_bf16_f32 v181,v82,v83
v_cvt_pk_bf16_f32 v182,v84,v85
v_mfma_f32_16x16x32_bf16 v[88:91],a[104:107],a[232:235],v[88:91]
v_cvt_pk_bf16_f32 v183,v86,v87
v_accvgpr_read_b32 v8,a40
v_mfma_f32_16x16x32_bf16 v[88:91],a[100:103],a[204:207],v[88:91]
v_mul_f32 v72,s71,v72
v_mul_f32 v73,s71,v73
v_mfma_f32_16x16x32_bf16 v[88:91],a[96:99],a[200:203],v[88:91]
v_mul_f32 v74,s71,v74
v_mul_f32 v75,s71,v75
v_mfma_f32_16x16x32_bf16 v[104:107],a[96:99],a[208:211],v[104:107]
global_load_lds_dwordx4 v4,s[10:11] offset:1024 nt
v_cvt_pk_bf16_f32 v156,v72,v73
v_mfma_f32_16x16x32_bf16 v[104:107],a[100:103],a[212:215],v[104:107]
v_cvt_pk_bf16_f32 v157,v74,v75
v_accvgpr_read_b32 v9,a41
v_mfma_f32_16x16x32_bf16 v[104:107],a[104:107],a[240:243],v[104:107]
v_mul_f32 v88,s71,v88
v_mul_f32 v89,s71,v89
v_mfma_f32_16x16x32_bf16 v[104:107],a[108:111],a[244:247],v[104:107]
v_mul_f32 v90,s71,v90
v_mul_f32 v91,s71,v91
v_mfma_f32_16x16x32_bf16 v[120:123],a[108:111],a[252:255],v[120:123]
v_cvt_pk_bf16_f32 v188,v88,v89
v_cvt_pk_bf16_f32 v189,v90,v91
v_mfma_f32_16x16x32_bf16 v[120:123],a[104:107],a[248:251],v[120:123]
v_accvgpr_read_b32 v10,a42
v_accvgpr_read_b32 v11,a43
v_mfma_f32_16x16x32_bf16 v[120:123],a[100:103],a[220:223],v[120:123]
v_accvgpr_read_b32 v12,a44
v_accvgpr_read_b32 v13,a45
v_mfma_f32_16x16x32_bf16 v[120:123],a[96:99],a[216:219],v[120:123]
v_accvgpr_read_b32 v14,a46
v_accvgpr_read_b32 v15,a47
v_mfma_f32_16x16x32_bf16 v[76:79],a[112:115],a[192:195],v[76:79]
v_mul_f32 v8,s71,v8
v_mul_f32 v9,s71,v9
v_mfma_f32_16x16x32_bf16 v[76:79],a[116:119],a[196:199],v[76:79]
v_mul_f32 v10,s71,v10
v_mul_f32 v11,s71,v11
v_mfma_f32_16x16x32_bf16 v[76:79],a[120:123],a[224:227],v[76:79]
v_mul_f32 v12,s71,v12
v_mul_f32 v13,s71,v13
v_mfma_f32_16x16x32_bf16 v[76:79],a[124:127],a[228:231],v[76:79]
v_mul_f32 v14,s71,v14
v_mul_f32 v15,s71,v15
v_mfma_f32_16x16x32_bf16 v[92:95],a[124:127],a[236:239],v[92:95]
v_cvt_pk_bf16_f32 v204,v8,v9
v_cvt_pk_bf16_f32 v205,v10,v11
v_mfma_f32_16x16x32_bf16 v[92:95],a[120:123],a[232:235],v[92:95]
v_cvt_pk_bf16_f32 v206,v12,v13
)A"
R"A(v_cvt_pk_bf16_f32 v207,v14,v15
v_mfma_f32_16x16x32_bf16 v[92:95],a[116:119],a[204:207],v[92:95]
global_load_lds_dwordx4 v5,s[10:11] offset:2048 nt
v_mul_f32 v76,s71,v76
v_mfma_f32_16x16x32_bf16 v[92:95],a[112:115],a[200:203],v[92:95]
v_mul_f32 v77,s71,v77
v_mul_f32 v78,s71,v78
v_mfma_f32_16x16x32_bf16 v[108:111],a[112:115],a[208:211],v[108:111]
v_mul_f32 v79,s71,v79
v_cvt_pk_bf16_f32 v158,v76,v77
v_mfma_f32_16x16x32_bf16 v[108:111],a[116:119],a[212:215],v[108:111]
ds_read_b128 a[156:159],v1 offset:4864
v_cvt_pk_bf16_f32 v159,v78,v79
v_mfma_f32_16x16x32_bf16 v[108:111],a[120:123],a[240:243],v[108:111]
v_mul_f32 v92,s71,v92
v_mul_f32 v93,s71,v93
v_mfma_f32_16x16x32_bf16 v[108:111],a[124:127],a[244:247],v[108:111]
v_mul_f32 v94,s71,v94
v_mul_f32 v95,s71,v95
v_mfma_f32_16x16x32_bf16 v[124:127],a[124:127],a[252:255],v[124:127]
v_cvt_pk_bf16_f32 v190,v92,v93
v_cvt_pk_bf16_f32 v191,v94,v95
v_mfma_f32_16x16x32_bf16 v[124:127],a[120:123],a[248:251],v[124:127]
v_cvt_pk_bf16_f32 v209,v210,v211
v_cvt_pk_bf16_f32 v210,v212,v213
v_mfma_f32_16x16x32_bf16 v[124:127],a[116:119],a[220:223],v[124:127]
ds_read_b128 a[152:155],v1 offset:4608
v_cvt_pk_bf16_f32 v211,v214,v215
v_mfma_f32_16x16x32_bf16 v[124:127],a[112:115],a[216:219],v[124:127]
v_mul_f32 v96,s71,v96
v_mul_f32 v97,s71,v97
s_waitcnt lgkmcnt(2)
v_mfma_f32_16x16x32_bf16 v[64:67],a[128:131],v[132:135],0
v_mul_f32 v98,s71,v98
v_mul_f32 v99,s71,v99
v_mfma_f32_16x16x32_bf16 v[64:67],a[132:135],v[140:143],v[64:67]
v_mul_f32 v100,s71,v100
v_mul_f32 v101,s71,v101
v_mfma_f32_16x16x32_bf16 v[64:67],a[136:139],v[148:151],v[64:67]
v_mul_f32 v102,s71,v102
v_mul_f32 v103,s71,v103
v_mfma_f32_16x16x32_bf16 v[64:67],a[140:143],v[156:159],v[64:67]
global_load_lds_dwordx4 v5,s[10:11] offset:3072
v_cvt_pk_bf16_f32 v212,v96,v97
v_mfma_f32_16x16x32_bf16 v[80:83],a[128:131],v[164:167],0
v_cvt_pk_bf16_f32 v213,v98,v99
v_cvt_pk_bf16_f32 v214,v100,v101
v_mfma_f32_16x16x32_bf16 v[80:83],a[132:135],v[172:175],v[80:83]
v_cvt_pk_bf16_f32 v215,v102,v103
v_mul_f32 v216,s71,v216
v_mfma_f32_16x16x32_bf16 v[80:83],a[136:139],v[180:183],v[80:83]
v_max3_f32 v64,v64,v65,v66
v_max_f32 v64,v64,v67
v_mov_b32 v65,v64
v_mfma_f32_16x16x32_bf16 v[80:83],a[140:143],v[188:191],v[80:83]
v_mul_f32 v217,s71,v217
v_mul_f32 v218,s71,v218
v_mfma_f32_16x16x32_bf16 v[96:99],a[128:131],v[196:199],0
v_permlane32_swap_b32 v64,v65
v_max_f32 v64,v64,v65
v_mov_b32 v65,v64
v_mul_f32 v219,s71,v219
v_mul_f32 v220,s71,v220
v_mul_f32 v221,s71,v221
v_mul_f32 v222,s71,v222
v_mul_f32 v223,s71,v223
v_cvt_pk_bf16_f32 v216,v216,v217
v_cvt_pk_bf16_f32 v217,v218,v219
v_cvt_pk_bf16_f32 v218,v220,v221
v_cvt_pk_bf16_f32 v219,v222,v223
v_mul_f32 v104,s71,v104
v_mul_f32 v105,s71,v105
v_mul_f32 v106,s71,v106
v_mul_f32 v107,s71,v107
v_mul_f32 v108,s71,v108
v_mul_f32 v109,s71,v109
v_mul_f32 v110,s71,v110
v_mul_f32 v111,s71,v111
v_cvt_pk_bf16_f32 v220,v104,v105
v_cvt_pk_bf16_f32 v221,v106,v107
v_cvt_pk_bf16_f32 v222,v108,v109
v_cvt_pk_bf16_f32 v223,v110,v111
v_mfma_f32_16x16x32_bf16 v[96:99],a[132:135],v[204:207],v[96:99]
ds_read_b128 a[148:151],v1 offset:4352
v_max3_f32 v80,v80,v81,v82
v_mfma_f32_16x16x32_bf16 v[96:99],a[136:139],v[212:215],v[96:99]
ds_read_b128 a[144:147],v1 offset:4096
v_permlane16_swap_b32 v64,v65
v_max_f32 v64,v64,v65
v_sub_f32 v35,0,v64
v_sub_f32 v34,0,v64
v_sub_f32 v33,0,v64
v_sub_f32 v32,0,v64
v_mfma_f32_16x16x32_bf16 v[96:99],a[140:143],v[220:223],v[96:99]
ds_read_b128 a[160:163],v1 offset:8192
v_max_f32 v80,v80,v83
v_mfma_f32_16x16x32_bf16 v[64:67],a[128:131],v[132:135],v[32:35]
s_waitcnt lgkmcnt(3)
ds_read_b128 a[164:167],v1 offset:8448
v_mov_b32 v81,v80
v_mfma_f32_16x16x32_bf16 v[64:67],a[132:135],v[140:143],v[64:67]
s_add_u32 s10,s24,s74
s_addc_u32 s11,s25,0
v_mul_f32 v232,s71,v232
v_mfma_f32_16x16x32_bf16 v[64:67],a[136:139],v[148:151],v[64:67]
s_add_u32 s74,s74,16384
ds_read_b128 a[168:171],v1 offset:8704
v_max3_f32 v96,v96,v97,v98
v_mfma_f32_16x16x32_bf16 v[64:67],a[140:143],v[156:159],v[64:67]
s_add_u32 m0,s76,16384
ds_read_b128 a[172:175],v1 offset:8960
v_max_f32 v96,v96,v99
s_nop 0
L_pd4_%=:
s_cbranch_execz L_pd8_%=
v_mfma_f32_16x16x32_bf16 v[68:71],a[156:159],v[156:159],v[32:35]
s_and_b32 s74,s74,0x3ffff
ds_read_b128 a[188:191],v1 offset:13056
v_mov_b32 v97,v96
v_mfma_f32_16x16x32_bf16 v[68:71],a[152:155],v[148:151],v[68:71]
global_load_lds_dwordx4 v4,s[10:11]
v_mul_f32 v233,s71,v233
s_waitcnt lgkmcnt(6)
v_mfma_f32_16x16x32_bf16 v[68:71],a[148:151],v[140:143],v[68:71]
s_or_b32 s74,s74,s69
ds_read_b128 a[184:187],v1 offset:12800
v_mul_f32 v234,s71,v234
s_waitcnt lgkmcnt(6)
v_mfma_f32_16x16x32_bf16 v[68:71],a[144:147],v[132:135],v[68:71]
s_sub_u32 s76,s76,32768
ds_read_b128 a[180:183],v1 offset:12544
v_mul_f32 v235,s71,v235
s_waitcnt lgkmcnt(6)
v_mfma_f32_16x16x32_bf16 v[72:75],a[160:163],v[132:135],v[32:35]
s_and_b32 s76,s76,0x1ffff
ds_read_b128 a[176:179],v1 offset:12288
v_mul_f32 v236,s71,v236
s_waitcnt lgkmcnt(6)
v_mfma_f32_16x16x32_bf16 v[72:75],a[164:167],v[140:143],v[72:75]
s_and_b32 s69,s75,0xfffc0000
v_permlane32_swap_b32 v80,v81
s_waitcnt lgkmcnt(5)
v_mfma_f32_16x16x32_bf16 v[72:75],a[168:171],v[148:151],v[72:75]
s_add_u32 s75,s75,16384
v_max_f32 v80,v80,v81
v_mov_b32 v81,v80
s_waitcnt lgkmcnt(4)
v_mfma_f32_16x16x32_bf16 v[72:75],a[172:175],v[156:159],v[72:75]
s_waitcnt lgkmcnt(3)
v_mul_f32 v237,s71,v237
v_mul_f32 v238,s71,v238
v_mfma_f32_16x16x32_bf16 v[76:79],a[188:191],v[156:159],v[32:35]
s_waitcnt lgkmcnt(2)
v_permlane16_swap_b32 v80,v81
v_mfma_f32_16x16x32_bf16 v[76:79],a[184:187],v[148:151],v[76:79]
s_waitcnt lgkmcnt(1)
v_max_f32 v80,v80,v81
v_sub_f32 v39,0,v80
v_sub_f32 v38,0,v80
v_sub_f32 v37,0,v80
v_sub_f32 v36,0,v80
v_mfma_f32_16x16x32_bf16 v[76:79],a[180:183],v[140:143],v[76:79]
s_waitcnt lgkmcnt(0)
v_mul_f32 v239,s71,v239
v_cvt_pk_bf16_f32 v232,v232,v233
v_mfma_f32_16x16x32_bf16 v[76:79],a[176:179],v[132:135],v[76:79]
v_cvt_pk_bf16_f32 v233,v234,v235
v_cvt_pk_bf16_f32 v234,v236,v237
v_mfma_f32_16x16x32_bf16 v[80:83],a[128:131],v[164:167],v[36:39]
v_cvt_pk_bf16_f32 v235,v238,v239
v_accvgpr_read_b32 v16,a56
v_mfma_f32_16x16x32_bf16 v[80:83],a[132:135],v[172:175],v[80:83]
v_accvgpr_read_b32 v17,a57
v_accvgpr_read_b32 v18,a58
v_mfma_f32_16x16x32_bf16 v[80:83],a[136:139],v[180:183],v[80:83]
v_accvgpr_read_b32 v19,a59
v_accvgpr_read_b32 v20,a60
v_mfma_f32_16x16x32_bf16 v[80:83],a[140:143],v[188:191],v[80:83]
global_load_lds_dwordx4 v5,s[10:11] offset:1024
v_accvgpr_read_b32 v21,a61
v_mfma_f32_16x16x32_bf16 v[84:87],a[156:159],v[188:191],v[36:39]
v_accvgpr_read_b32 v22,a62
v_accvgpr_read_b32 v23,a63
v_mfma_f32_16x16x32_bf16 v[84:87],a[152:155],v[180:183],v[84:87]
v_mul_f32 v16,s71,v16
v_mul_f32 v17,s71,v17
v_mfma_f32_16x16x32_bf16 v[84:87],a[148:151],v[172:175],v[84:87]
v_mul_f32 v18,s71,v18
v_mul_f32 v19,s71,v19
v_mfma_f32_16x16x32_bf16 v[84:87],a[144:147],v[164:167],v[84:87]
v_mul_f32 v20,s71,v20
v_mul_f32 v21,s71,v21
v_mfma_f32_16x16x32_bf16 v[88:91],a[160:163],v[164:167],v[36:39]
v_mul_f32 v22,s71,v22
v_mul_f32 v23,s71,v23
v_mfma_f32_16x16x32_bf16 v[88:91],a[164:167],v[172:175],v[88:91]
v_cvt_pk_bf16_f32 v236,v16,v17
v_cvt_pk_bf16_f32 v237,v18,v19
v_mfma_f32_16x16x32_bf16 v[88:91],a[168:171],v[180:183],v[88:91]
v_cvt_pk_bf16_f32 v238,v20,v21
v_cvt_pk_bf16_f32 v239,v22,v23
v_mfma_f32_16x16x32_bf16 v[88:91],a[172:175],v[188:191],v[88:91]
v_mul_f32 v240,s71,v240
v_mul_f32 v241,s71,v241
v_mfma_f32_16x16x32_bf16 v[92:95],a[188:191],v[188:191],v[36:39]
v_mul_f32 v242,s71,v242
v_mul_f32 v243,s71,v243
v_mfma_f32_16x16x32_bf16 v[92:95],a[184:187],v[180:183],v[92:95]
v_mul_f32 v244,s71,v244
v_mul_f32 v245,s71,v245
v_mfma_f32_16x16x32_bf16 v[92:95],a[180:183],v[172:175],v[92:95]
v_mul_f32 v246,s71,v246
v_mul_f32 v247,s71,v247
v_mfma_f32_16x16x32_bf16 v[92:95],a[176:179],v[164:167],v[92:95]
v_cvt_pk_bf16_f32 v240,v240,v241
v_cvt_pk_bf16_f32 v241,v242,v243
v_cvt_pk_bf16_f32 v242,v244,v245
v_cvt_pk_bf16_f32 v243,v246,v247
v_mul_f32 v112,s71,v112
v_mul_f32 v113,s71,v113
v_mul_f32 v114,s71,v114
v_mul_f32 v115,s71,v115
v_mul_f32 v116,s71,v116
v_mul_f32 v117,s71,v117
v_mul_f32 v118,s71,v118
v_mul_f32 v119,s71,v119
v_cvt_pk_bf16_f32 v244,v112,v113
v_cvt_pk_bf16_f32 v245,v114,v115
v_cvt_pk_bf16_f32 v246,v116,v117
v_cvt_pk_bf16_f32 v247,v118,v119
v_permlane32_swap_b32 v96,v97
v_max_f32 v96,v96,v97
v_mov_b32 v97,v96
v_mfma_f32_16x16x32_bf16 v[112:115],a[128:131],v[228:231],0
v_mul_f32 v248,s71,v248
v_mul_f32 v249,s71,v249
v_mul_f32 v250,s71,v250
v_mul_f32 v251,s71,v251
v_mul_f32 v252,s71,v252
v_mul_f32 v253,s71,v253
v_mul_f32 v254,s71,v254
v_mul_f32 v255,s71,v255
v_cvt_pk_bf16_f32 v248,v248,v249
v_cvt_pk_bf16_f32 v249,v250,v251
v_cvt_pk_bf16_f32 v250,v252,v253
v_cvt_pk_bf16_f32 v251,v254,v255
v_mul_f32 v120,s71,v120
v_mul_f32 v121,s71,v121
v_mul_f32 v122,s71,v122
v_mul_f32 v123,s71,v123
v_mul_f32 v124,s71,v124
v_mul_f32 v125,s71,v125
v_mul_f32 v126,s71,v126
v_mul_f32 v127,s71,v127
v_cvt_pk_bf16_f32 v252,v120,v121
v_cvt_pk_bf16_f32 v253,v122,v123
v_cvt_pk_bf16_f32 v254,v124,v125
v_cvt_pk_bf16_f32 v255,v126,v127
v_mfma_f32_16x16x32_bf16 v[112:115],a[132:135],v[236:239],v[112:115]
global_load_lds_dwordx4 v4,s[10:11] offset:2048
v_permlane16_swap_b32 v96,v97
v_max_f32 v96,v96,v97
v_sub_f32 v43,0,v96
v_sub_f32 v42,0,v96
v_sub_f32 v41,0,v96
v_sub_f32 v40,0,v96
v_mfma_f32_16x16x32_bf16 v[112:115],a[136:139],v[244:247],v[112:115]
ds_read_b64_tr_b16 a[192:193],v2
ds_read_b64_tr_b16 a[194:195],v2 offset:4096
v_mfma_f32_16x16x32_bf16 v[112:115],a[140:143],v[252:255],v[112:115]
ds_read_b64_tr_b16 a[196:197],v2 offset:8192
ds_read_b64_tr_b16 a[198:199],v2 offset:12288
v_mfma_f32_16x16x32_bf16 v[96:99],a[128:131],v[196:199],v[40:43]
ds_read_b64_tr_b16 a[200:201],v3
ds_read_b64_tr_b16 a[202:203],v3 offset:4096
v_mfma_f32_16x16x32_bf16 v[96:99],a[132:135],v[204:207],v[96:99]
ds_read_b64_tr_b16 a[204:205],v3 offset:8192
ds_read_b64_tr_b16 a[206:207],v3 offset:12288
v_mfma_f32_16x16x32_bf16 v[96:99],a[136:139],v[212:215],v[96:99]
ds_read_b64_tr_b16 a[208:209],v2 offset:256
v_max3_f32 v112,v112,v113,v114
v_mfma_f32_16x16x32_bf16 v[96:99],a[140:143],v[220:223],v[96:99]
ds_read_b64_tr_b16 a[210:211],v2 offset:4352
ds_read_b64_tr_b16 a[212:213],v2 offset:8448
v_mfma_f32_16x16x32_bf16 v[100:103],a[156:159],v[220:223],v[40:43]
ds_read_b64_tr_b16 a[214:215],v2 offset:12544
v_max_f32 v112,v112,v115
v_mfma_f32_16x16x32_bf16 v[100:103],a[152:155],v[212:215],v[100:103]
ds_read_b64_tr_b16 a[216:217],v3 offset:256
ds_read_b64_tr_b16 a[218:219],v3 offset:4352
v_mfma_f32_16x16x32_bf16 v[100:103],a[148:151],v[204:207],v[100:103]
v_exp_f32 v64,v64
v_mfma_f32_16x16x32_bf16 v[100:103],a[144:147],v[196:199],v[100:103]
v_exp_f32 v65,v65
v_mfma_f32_16x16x32_bf16 v[104:107],a[160:163],v[196:199],v[40:43]
v_exp_f32 v66,v66
v_mfma_f32_16x16x32_bf16 v[104:107],a[164:167],v[204:207],v[104:107]
v_exp_f32 v67,v67
v_mfma_f32_16x16x32_bf16 v[104:107],a[168:171],v[212:215],v[104:107]
v_exp_f32 v68,v68
v_mfma_f32_16x16x32_bf16 v[104:107],a[172:175],v[220:223],v[104:107]
global_load_lds_dwordx4 v5,s[10:11] offset:3072
ds_read_b64_tr_b16 a[220:221],v3 offset:8448
v_mfma_f32_16x16x32_bf16 v[108:111],a[188:191],v[220:223],v[40:43]
v_exp_f32 v69,v69
v_exp_f32 v70,v70
v_exp_f32 v71,v71
v_mfma_f32_16x16x32_bf16 v[108:111],a[184:187],v[212:215],v[108:111]
ds_read_b64_tr_b16 a[222:223],v3 offset:12544
v_cvt_pk_bf16_f32 v64,v64,v65
v_cvt_pk_bf16_f32 v65,v66,v67
v_cvt_pk_bf16_f32 v66,v68,v69
v_cvt_pk_bf16_f32 v67,v70,v71
v_exp_f32 v72,v72
v_exp_f32 v73,v73
v_exp_f32 v74,v74
v_exp_f32 v75,v75
v_exp_f32 v76,v76
v_exp_f32 v77,v77
v_exp_f32 v78,v78
v_exp_f32 v79,v79
v_mfma_f32_16x16x32_bf16 v[108:111],a[180:183],v[204:207],v[108:111]
ds_read_b64_tr_b16 a[224:225],v2 offset:512
ds_read_b64_tr_b16 a[226:227],v2 offset:4608
v_cvt_pk_bf16_f32 v72,v72,v73
v_cvt_pk_bf16_f32 v73,v74,v75
v_cvt_pk_bf16_f32 v74,v76,v77
v_cvt_pk_bf16_f32 v75,v78,v79
v_mfma_f32_16x16x32_bf16 v[108:111],a[176:179],v[196:199],v[108:111]
s_waitcnt lgkmcnt(4)
ds_read_b64_tr_b16 a[228:229],v2 offset:8704
ds_read_b64_tr_b16 a[230:231],v2 offset:12800
v_mfma_f32_16x16x32_bf16 a[0:3],a[192:195],v[64:67],0
)A"
R"A(ds_read_b64_tr_b16 a[232:233],v3 offset:512
ds_read_b64_tr_b16 a[234:235],v3 offset:4608
v_mfma_f32_16x16x32_bf16 a[0:3],a[196:199],v[72:75],a[0:3]
ds_read_b64_tr_b16 a[236:237],v3 offset:8704
ds_read_b64_tr_b16 a[238:239],v3 offset:12800
v_mfma_f32_16x16x32_bf16 a[4:7],a[200:203],v[64:67],0
ds_read_b64_tr_b16 a[240:241],v2 offset:768
v_mov_b32 v113,v112
v_mfma_f32_16x16x32_bf16 a[4:7],a[204:207],v[72:75],a[4:7]
ds_read_b64_tr_b16 a[242:243],v2 offset:4864
ds_read_b64_tr_b16 a[244:245],v2 offset:8960
v_mfma_f32_16x16x32_bf16 a[8:11],a[208:211],v[64:67],0
ds_read_b64_tr_b16 a[246:247],v2 offset:13056
v_add_u32 v1,s78,v1
v_mfma_f32_16x16x32_bf16 a[8:11],a[212:215],v[72:75],a[8:11]
ds_read_b64_tr_b16 a[248:249],v3 offset:768
ds_read_b64_tr_b16 a[250:251],v3 offset:4864
v_mfma_f32_16x16x32_bf16 a[12:15],a[216:219],v[64:67],0
s_waitcnt lgkmcnt(12)
ds_read_b64_tr_b16 a[252:253],v3 offset:8960
v_add_u32 v2,s78,v2
v_mfma_f32_16x16x32_bf16 a[12:15],a[220:223],v[72:75],a[12:15]
s_waitcnt lgkmcnt(6)
ds_read_b64_tr_b16 a[254:255],v3 offset:13056
v_add_u32 v3,s78,v3
v_mfma_f32_16x16x32_bf16 a[16:19],a[224:227],v[64:67],0
s_sub_u32 s78,s77,32768
v_permlane32_swap_b32 v112,v113
v_mfma_f32_16x16x32_bf16 a[16:19],a[228:231],v[72:75],a[16:19]
s_and_b32 s78,s78,0x1ffff
v_max_f32 v112,v112,v113
v_mov_b32 v113,v112
v_mfma_f32_16x16x32_bf16 a[20:23],a[232:235],v[64:67],0
s_sub_u32 s78,s78,s77
v_exp_f32 v80,v80
v_mfma_f32_16x16x32_bf16 a[20:23],a[236:239],v[72:75],a[20:23]
s_add_u32 s77,s77,s78
v_permlane16_swap_b32 v112,v113
s_waitcnt lgkmcnt(5)
v_mfma_f32_16x16x32_bf16 a[24:27],a[240:243],v[64:67],0
s_waitcnt lgkmcnt(4)
v_max_f32 v112,v112,v113
v_max_f32 v112,s72,v112
v_mfma_f32_16x16x32_bf16 a[24:27],a[244:247],v[72:75],a[24:27]
s_waitcnt lgkmcnt(2)
v_sub_f32 v47,0,v112
v_sub_f32 v46,0,v112
v_mfma_f32_16x16x32_bf16 a[28:31],a[248:251],v[64:67],0
s_waitcnt lgkmcnt(0)
v_sub_f32 v45,0,v112
v_sub_f32 v44,0,v112
v_mfma_f32_16x16x32_bf16 a[28:31],a[252:255],v[72:75],a[28:31]
v_exp_f32 v81,v81
v_mfma_f32_16x16x32_bf16 v[48:51],v[28:31],v[64:67],0
v_exp_f32 v82,v82
v_mfma_f32_16x16x32_bf16 v[48:51],v[28:31],v[72:75],v[48:51]
v_exp_f32 v83,v83
s_waitcnt vmcnt(8)
s_barrier
v_mfma_f32_16x16x32_bf16 v[112:115],a[128:131],v[228:231],v[44:47]
s_add_u32 s10,s20,s74
s_addc_u32 s11,s21,0
s_add_u32 m0,s76,0
v_mfma_f32_16x16x32_bf16 v[112:115],a[132:135],v[236:239],v[112:115]
v_exp_f32 v84,v84
v_mfma_f32_16x16x32_bf16 v[112:115],a[136:139],v[244:247],v[112:115]
global_load_lds_dwordx4 v4,s[10:11] nt
v_cvt_pk_bf16_f32 v80,v80,v81
v_mfma_f32_16x16x32_bf16 v[112:115],a[140:143],v[252:255],v[112:115]
v_exp_f32 v85,v85
v_mfma_f32_16x16x32_bf16 v[116:119],a[156:159],v[252:255],v[44:47]
v_exp_f32 v86,v86
v_mfma_f32_16x16x32_bf16 v[116:119],a[152:155],v[244:247],v[116:119]
v_exp_f32 v87,v87
v_mfma_f32_16x16x32_bf16 v[116:119],a[148:151],v[236:239],v[116:119]
v_cvt_pk_bf16_f32 v81,v82,v83
v_cvt_pk_bf16_f32 v82,v84,v85
v_mfma_f32_16x16x32_bf16 v[116:119],a[144:147],v[228:231],v[116:119]
ds_read_b128 a[128:131],v1
v_cvt_pk_bf16_f32 v83,v86,v87
v_mfma_f32_16x16x32_bf16 v[120:123],a[160:163],v[228:231],v[44:47]
v_exp_f32 v88,v88
v_mfma_f32_16x16x32_bf16 v[120:123],a[164:167],v[236:239],v[120:123]
v_exp_f32 v89,v89
v_mfma_f32_16x16x32_bf16 v[120:123],a[168:171],v[244:247],v[120:123]
v_exp_f32 v90,v90
v_mfma_f32_16x16x32_bf16 v[120:123],a[172:175],v[252:255],v[120:123]
v_exp_f32 v91,v91
v_mfma_f32_16x16x32_bf16 v[124:127],a[188:191],v[252:255],v[44:47]
v_exp_f32 v92,v92
v_mfma_f32_16x16x32_bf16 v[124:127],a[184:187],v[244:247],v[124:127]
v_exp_f32 v93,v93
v_exp_f32 v94,v94
v_exp_f32 v95,v95
v_mfma_f32_16x16x32_bf16 v[124:127],a[180:183],v[236:239],v[124:127]
v_cvt_pk_bf16_f32 v88,v88,v89
v_cvt_pk_bf16_f32 v89,v90,v91
v_cvt_pk_bf16_f32 v90,v92,v93
v_cvt_pk_bf16_f32 v91,v94,v95
v_mfma_f32_16x16x32_bf16 v[124:127],a[176:179],v[228:231],v[124:127]
v_exp_f32 v96,v96
v_mfma_f32_16x16x32_bf16 a[32:35],a[192:195],v[80:83],0
global_load_lds_dwordx4 v4,s[10:11] offset:1024 nt
ds_read_b128 a[132:135],v1 offset:256
v_mfma_f32_16x16x32_bf16 a[32:35],a[196:199],v[88:91],a[32:35]
v_exp_f32 v97,v97
v_mfma_f32_16x16x32_bf16 a[36:39],a[200:203],v[80:83],0
ds_read_b128 a[136:139],v1 offset:512
v_cvt_pk_bf16_f32 v96,v96,v97
v_mfma_f32_16x16x32_bf16 a[36:39],a[204:207],v[88:91],a[36:39]
v_exp_f32 v98,v98
v_mfma_f32_16x16x32_bf16 a[40:43],a[208:211],v[80:83],0
v_exp_f32 v99,v99
v_mfma_f32_16x16x32_bf16 a[40:43],a[212:215],v[88:91],a[40:43]
ds_read_b128 a[140:143],v1 offset:768
v_cvt_pk_bf16_f32 v97,v98,v99
v_mfma_f32_16x16x32_bf16 a[44:47],a[216:219],v[80:83],0
v_exp_f32 v100,v100
v_mfma_f32_16x16x32_bf16 a[44:47],a[220:223],v[88:91],a[44:47]
v_exp_f32 v101,v101
v_mfma_f32_16x16x32_bf16 a[48:51],a[224:227],v[80:83],0
ds_read_b128 a[156:159],v1 offset:4864
v_cvt_pk_bf16_f32 v98,v100,v101
v_mfma_f32_16x16x32_bf16 a[48:51],a[228:231],v[88:91],a[48:51]
v_exp_f32 v102,v102
v_mfma_f32_16x16x32_bf16 a[52:55],a[232:235],v[80:83],0
v_exp_f32 v103,v103
v_mfma_f32_16x16x32_bf16 a[52:55],a[236:239],v[88:91],a[52:55]
ds_read_b128 a[152:155],v1 offset:4608
v_cvt_pk_bf16_f32 v99,v102,v103
v_mfma_f32_16x16x32_bf16 a[56:59],a[240:243],v[80:83],0
ds_read_b128 a[148:151],v1 offset:4352
v_mfma_f32_16x16x32_bf16 a[56:59],a[244:247],v[88:91],a[56:59]
ds_read_b128 a[144:147],v1 offset:4096
v_mfma_f32_16x16x32_bf16 a[60:63],a[248:251],v[80:83],0
global_load_lds_dwordx4 v5,s[10:11] offset:2048 nt
ds_read_b128 a[160:163],v1 offset:8192
v_mfma_f32_16x16x32_bf16 a[60:63],a[252:255],v[88:91],a[60:63]
v_exp_f32 v104,v104
v_mfma_f32_16x16x32_bf16 v[52:55],v[28:31],v[80:83],0
ds_read_b128 a[164:167],v1 offset:8448
v_mfma_f32_16x16x32_bf16 v[52:55],v[28:31],v[88:91],v[52:55]
s_waitcnt lgkmcnt(5)
ds_read_b128 a[168:171],v1 offset:8704
v_mfma_f32_16x16x32_bf16 v[64:67],a[128:131],v[132:135],v[32:35]
ds_read_b128 a[172:175],v1 offset:8960
v_mfma_f32_16x16x32_bf16 v[64:67],a[132:135],v[140:143],v[64:67]
v_exp_f32 v105,v105
v_mfma_f32_16x16x32_bf16 v[64:67],a[136:139],v[148:151],v[64:67]
ds_read_b128 a[188:191],v1 offset:13056
v_cvt_pk_bf16_f32 v104,v104,v105
v_mfma_f32_16x16x32_bf16 v[64:67],a[140:143],v[156:159],v[64:67]
ds_read_b128 a[184:187],v1 offset:12800
v_mfma_f32_16x16x32_bf16 v[68:71],a[156:159],v[156:159],v[32:35]
s_waitcnt lgkmcnt(5)
ds_read_b128 a[180:183],v1 offset:12544
v_mfma_f32_16x16x32_bf16 v[68:71],a[152:155],v[148:151],v[68:71]
v_exp_f32 v106,v106
v_mfma_f32_16x16x32_bf16 v[68:71],a[148:151],v[140:143],v[68:71]
ds_read_b128 a[176:179],v1 offset:12288
v_add_u32 v1,s78,v1
v_mfma_f32_16x16x32_bf16 v[68:71],a[144:147],v[132:135],v[68:71]
v_exp_f32 v107,v107
v_mfma_f32_16x16x32_bf16 v[72:75],a[160:163],v[132:135],v[32:35]
s_waitcnt lgkmcnt(4)
v_exp_f32 v108,v108
v_mfma_f32_16x16x32_bf16 v[72:75],a[164:167],v[140:143],v[72:75]
v_exp_f32 v109,v109
v_mfma_f32_16x16x32_bf16 v[72:75],a[168:171],v[148:151],v[72:75]
global_load_lds_dwordx4 v5,s[10:11] offset:3072
v_cvt_pk_bf16_f32 v105,v106,v107
v_mfma_f32_16x16x32_bf16 v[72:75],a[172:175],v[156:159],v[72:75]
s_waitcnt lgkmcnt(2)
v_exp_f32 v110,v110
v_mfma_f32_16x16x32_bf16 v[76:79],a[188:191],v[156:159],v[32:35]
v_exp_f32 v111,v111
v_mfma_f32_16x16x32_bf16 v[76:79],a[184:187],v[148:151],v[76:79]
s_waitcnt lgkmcnt(1)
v_cvt_pk_bf16_f32 v106,v108,v109
v_cvt_pk_bf16_f32 v107,v110,v111
v_mfma_f32_16x16x32_bf16 v[76:79],a[180:183],v[140:143],v[76:79]
s_waitcnt lgkmcnt(0)
v_exp_f32 v112,v112
v_mfma_f32_16x16x32_bf16 v[76:79],a[176:179],v[132:135],v[76:79]
v_exp_f32 v113,v113
v_mfma_f32_16x16x32_bf16 a[64:67],a[192:195],v[96:99],0
v_exp_f32 v114,v114
v_mfma_f32_16x16x32_bf16 a[64:67],a[196:199],v[104:107],a[64:67]
v_exp_f32 v115,v115
v_mfma_f32_16x16x32_bf16 a[68:71],a[200:203],v[96:99],0
v_exp_f32 v116,v116
v_mfma_f32_16x16x32_bf16 a[68:71],a[204:207],v[104:107],a[68:71]
v_exp_f32 v117,v117
v_mfma_f32_16x16x32_bf16 a[72:75],a[208:211],v[96:99],0
s_add_u32 s10,s24,s74
s_addc_u32 s11,s25,0
s_add_u32 s74,s74,16384
s_and_b32 s74,s74,0x3ffff
s_or_b32 s74,s74,s69
s_lshl_b32 s69,s60,13
s_add_u32 s69,s69,131072
v_add_u32 v8,s69,v6
v_mfma_f32_16x16x32_bf16 a[72:75],a[212:215],v[104:107],a[72:75]
s_lshl_b32 s69,s60,13
ds_write_b128 v8,v[24:27] offset:4096
v_mfma_f32_16x16x32_bf16 a[76:79],a[216:219],v[96:99],0
s_add_u32 m0,s76,16384
ds_write_b32 v8,v7 offset:5120
v_cvt_pk_bf16_f32 v112,v112,v113
v_mfma_f32_16x16x32_bf16 a[76:79],a[220:223],v[104:107],a[76:79]
s_sub_u32 s76,s76,32768
v_bfe_u32 v24,v6,4,4
v_cvt_pk_bf16_f32 v113,v114,v115
v_mfma_f32_16x16x32_bf16 a[80:83],a[224:227],v[96:99],0
global_load_lds_dwordx4 v4,s[10:11]
v_lshrrev_b32 v7,8,v6
v_lshlrev_b32 v7,4,v7
v_lshl_add_u32 v7,v24,8,v7
v_mfma_f32_16x16x32_bf16 a[80:83],a[228:231],v[104:107],a[80:83]
s_and_b32 s76,s76,0x1ffff
v_exp_f32 v118,v118
v_mfma_f32_16x16x32_bf16 a[84:87],a[232:235],v[96:99],0
s_add_u32 s69,s69,131072
v_exp_f32 v119,v119
v_mfma_f32_16x16x32_bf16 a[84:87],a[236:239],v[104:107],a[84:87]
v_cvt_pk_bf16_f32 v114,v116,v117
v_cvt_pk_bf16_f32 v115,v118,v119
v_mfma_f32_16x16x32_bf16 a[88:91],a[240:243],v[96:99],0
v_exp_f32 v120,v120
v_mfma_f32_16x16x32_bf16 a[88:91],a[244:247],v[104:107],a[88:91]
buffer_load_dwordx4 v[8:11],v7,s[20:23],s65 offen nt
v_mfma_f32_16x16x32_bf16 a[92:95],a[248:251],v[96:99],0
v_exp_f32 v121,v121
v_mfma_f32_16x16x32_bf16 a[92:95],a[252:255],v[104:107],a[92:95]
v_exp_f32 v122,v122
v_mfma_f32_16x16x32_bf16 v[56:59],v[28:31],v[96:99],0
v_exp_f32 v123,v123
v_mfma_f32_16x16x32_bf16 v[56:59],v[28:31],v[104:107],v[56:59]
v_exp_f32 v124,v124
v_mfma_f32_16x16x32_bf16 v[80:83],a[128:131],v[164:167],v[36:39]
buffer_load_dwordx4 v[12:15],v7,s[20:23],s65 offen offset:64 nt
v_mfma_f32_16x16x32_bf16 v[80:83],a[132:135],v[172:175],v[80:83]
v_exp_f32 v125,v125
v_mfma_f32_16x16x32_bf16 v[80:83],a[136:139],v[180:183],v[80:83]
v_exp_f32 v126,v126
v_mfma_f32_16x16x32_bf16 v[80:83],a[140:143],v[188:191],v[80:83]
v_exp_f32 v127,v127
v_mfma_f32_16x16x32_bf16 v[84:87],a[156:159],v[188:191],v[36:39]
v_cvt_pk_bf16_f32 v120,v120,v121
v_cvt_pk_bf16_f32 v121,v122,v123
v_mfma_f32_16x16x32_bf16 v[84:87],a[152:155],v[180:183],v[84:87]
buffer_load_dwordx4 v[16:19],v7,s[20:23],s65 offen offset:128 nt
v_mfma_f32_16x16x32_bf16 v[84:87],a[148:151],v[172:175],v[84:87]
v_cvt_pk_bf16_f32 v122,v124,v125
v_cvt_pk_bf16_f32 v123,v126,v127
v_mfma_f32_16x16x32_bf16 v[84:87],a[144:147],v[164:167],v[84:87]
v_exp_f32 v64,v64
v_mfma_f32_16x16x32_bf16 v[88:91],a[160:163],v[164:167],v[36:39]
v_exp_f32 v65,v65
v_mfma_f32_16x16x32_bf16 v[88:91],a[164:167],v[172:175],v[88:91]
v_exp_f32 v66,v66
v_mfma_f32_16x16x32_bf16 v[88:91],a[168:171],v[180:183],v[88:91]
v_exp_f32 v67,v67
v_mfma_f32_16x16x32_bf16 v[88:91],a[172:175],v[188:191],v[88:91]
global_load_lds_dwordx4 v5,s[10:11] offset:1024
v_cvt_pk_bf16_f32 v64,v64,v65
v_mfma_f32_16x16x32_bf16 v[92:95],a[188:191],v[188:191],v[36:39]
v_exp_f32 v68,v68
v_mfma_f32_16x16x32_bf16 v[92:95],a[184:187],v[180:183],v[92:95]
v_exp_f32 v69,v69
v_mfma_f32_16x16x32_bf16 v[92:95],a[180:183],v[172:175],v[92:95]
v_exp_f32 v70,v70
v_mfma_f32_16x16x32_bf16 v[92:95],a[176:179],v[164:167],v[92:95]
v_exp_f32 v71,v71
v_mfma_f32_16x16x32_bf16 a[96:99],a[192:195],v[112:115],0
buffer_load_dwordx4 v[20:23],v7,s[20:23],s65 offen offset:192 nt
v_mfma_f32_16x16x32_bf16 a[96:99],a[196:199],v[120:123],a[96:99]
ds_read_b64_tr_b16 a[192:193],v2
ds_read_b64_tr_b16 a[194:195],v2 offset:4096
v_mfma_f32_16x16x32_bf16 a[100:103],a[200:203],v[112:115],0
ds_read_b64_tr_b16 a[196:197],v2 offset:8192
ds_read_b64_tr_b16 a[198:199],v2 offset:12288
v_mfma_f32_16x16x32_bf16 a[100:103],a[204:207],v[120:123],a[100:103]
ds_read_b64_tr_b16 a[200:201],v3
ds_read_b64_tr_b16 a[202:203],v3 offset:4096
v_mfma_f32_16x16x32_bf16 a[104:107],a[208:211],v[112:115],0
ds_read_b64_tr_b16 a[204:205],v3 offset:8192
ds_read_b64_tr_b16 a[206:207],v3 offset:12288
v_mfma_f32_16x16x32_bf16 a[104:107],a[212:215],v[120:123],a[104:107]
ds_read_b64_tr_b16 a[208:209],v2 offset:256
v_cvt_pk_bf16_f32 v65,v66,v67
v_mfma_f32_16x16x32_bf16 a[108:111],a[216:219],v[112:115],0
global_load_lds_dwordx4 v4,s[10:11] offset:2048
ds_read_b64_tr_b16 a[210:211],v2 offset:4352
)A"
R"A(v_mfma_f32_16x16x32_bf16 a[108:111],a[220:223],v[120:123],a[108:111]
ds_read_b64_tr_b16 a[212:213],v2 offset:8448
ds_read_b64_tr_b16 a[214:215],v2 offset:12544
v_mfma_f32_16x16x32_bf16 a[112:115],a[224:227],v[112:115],0
ds_read_b64_tr_b16 a[216:217],v3 offset:256
ds_read_b64_tr_b16 a[218:219],v3 offset:4352
v_mfma_f32_16x16x32_bf16 a[112:115],a[228:231],v[120:123],a[112:115]
v_cvt_pk_bf16_f32 v66,v68,v69
v_cvt_pk_bf16_f32 v67,v70,v71
v_mfma_f32_16x16x32_bf16 a[116:119],a[232:235],v[112:115],0
v_exp_f32 v72,v72
v_mfma_f32_16x16x32_bf16 a[116:119],a[236:239],v[120:123],a[116:119]
v_exp_f32 v73,v73
v_mfma_f32_16x16x32_bf16 a[120:123],a[240:243],v[112:115],0
v_exp_f32 v74,v74
v_mfma_f32_16x16x32_bf16 a[120:123],a[244:247],v[120:123],a[120:123]
v_exp_f32 v75,v75
v_mfma_f32_16x16x32_bf16 a[124:127],a[248:251],v[112:115],0
v_exp_f32 v76,v76
v_mfma_f32_16x16x32_bf16 a[124:127],a[252:255],v[120:123],a[124:127]
v_exp_f32 v77,v77
v_mfma_f32_16x16x32_bf16 v[60:63],v[28:31],v[112:115],0
v_exp_f32 v78,v78
v_mfma_f32_16x16x32_bf16 v[60:63],v[28:31],v[120:123],v[60:63]
global_load_lds_dwordx4 v5,s[10:11] offset:3072
ds_read_b64_tr_b16 a[220:221],v3 offset:8448
v_mfma_f32_16x16x32_bf16 v[96:99],a[128:131],v[196:199],v[40:43]
v_exp_f32 v79,v79
v_mfma_f32_16x16x32_bf16 v[96:99],a[132:135],v[204:207],v[96:99]
ds_read_b64_tr_b16 a[222:223],v3 offset:12544
v_cvt_pk_bf16_f32 v72,v72,v73
v_mfma_f32_16x16x32_bf16 v[96:99],a[136:139],v[212:215],v[96:99]
v_cvt_pk_bf16_f32 v73,v74,v75
v_cvt_pk_bf16_f32 v74,v76,v77
v_mfma_f32_16x16x32_bf16 v[96:99],a[140:143],v[220:223],v[96:99]
ds_read_b64_tr_b16 a[224:225],v2 offset:512
v_cvt_pk_bf16_f32 v75,v78,v79
v_mfma_f32_16x16x32_bf16 v[100:103],a[156:159],v[220:223],v[40:43]
ds_read_b64_tr_b16 a[226:227],v2 offset:4608
ds_read_b64_tr_b16 a[228:229],v2 offset:8704
v_mfma_f32_16x16x32_bf16 v[100:103],a[152:155],v[212:215],v[100:103]
ds_read_b64_tr_b16 a[230:231],v2 offset:12800
ds_read_b64_tr_b16 a[232:233],v3 offset:512
v_mfma_f32_16x16x32_bf16 v[100:103],a[148:151],v[204:207],v[100:103]
ds_read_b64_tr_b16 a[234:235],v3 offset:4608
ds_read_b64_tr_b16 a[236:237],v3 offset:8704
v_mfma_f32_16x16x32_bf16 v[100:103],a[144:147],v[196:199],v[100:103]
ds_read_b64_tr_b16 a[238:239],v3 offset:12800
ds_read_b64_tr_b16 a[240:241],v2 offset:768
v_mfma_f32_16x16x32_bf16 v[104:107],a[160:163],v[196:199],v[40:43]
ds_read_b64_tr_b16 a[242:243],v2 offset:4864
v_add_u32 v7,s69,v6
v_mfma_f32_16x16x32_bf16 v[104:107],a[164:167],v[204:207],v[104:107]
s_add_u32 s10,s20,s74
s_addc_u32 s11,s21,0
ds_read_b64_tr_b16 a[244:245],v2 offset:8960
v_mfma_f32_16x16x32_bf16 v[104:107],a[168:171],v[212:215],v[104:107]
s_and_b32 s69,s75,0xfffc0000
ds_read_b64_tr_b16 a[246:247],v2 offset:13056
ds_read_b64_tr_b16 a[248:249],v3 offset:768
v_mfma_f32_16x16x32_bf16 v[104:107],a[172:175],v[220:223],v[104:107]
s_add_u32 m0,s76,0
ds_read_b64_tr_b16 a[250:251],v3 offset:4864
ds_read_b64_tr_b16 a[252:253],v3 offset:8960
v_mfma_f32_16x16x32_bf16 v[108:111],a[188:191],v[220:223],v[40:43]
s_add_u32 s75,s75,16384
ds_read_b64_tr_b16 a[254:255],v3 offset:13056
v_add_u32 v2,s78,v2
v_mfma_f32_16x16x32_bf16 v[108:111],a[184:187],v[212:215],v[108:111]
global_load_lds_dwordx4 v4,s[10:11] nt
v_add_u32 v3,s78,v3
v_mfma_f32_16x16x32_bf16 v[108:111],a[180:183],v[204:207],v[108:111]
s_sub_u32 s78,s77,32768
v_exp_f32 v80,v80
v_mfma_f32_16x16x32_bf16 v[108:111],a[176:179],v[196:199],v[108:111]
s_and_b32 s78,s78,0x1ffff
v_exp_f32 v81,v81
s_waitcnt lgkmcnt(7)
v_mfma_f32_16x16x32_bf16 a[0:3],a[192:195],v[64:67],a[0:3]
s_sub_u32 s78,s78,s77
v_exp_f32 v82,v82
v_mfma_f32_16x16x32_bf16 a[0:3],a[196:199],v[72:75],a[0:3]
s_add_u32 s77,s77,s78
v_exp_f32 v83,v83
v_mfma_f32_16x16x32_bf16 a[4:7],a[200:203],v[64:67],a[4:7]
v_exp_f32 v84,v84
v_mfma_f32_16x16x32_bf16 a[4:7],a[204:207],v[72:75],a[4:7]
v_exp_f32 v85,v85
v_mfma_f32_16x16x32_bf16 a[8:11],a[208:211],v[64:67],a[8:11]
v_exp_f32 v86,v86
v_mfma_f32_16x16x32_bf16 a[8:11],a[212:215],v[72:75],a[8:11]
v_exp_f32 v87,v87
v_mfma_f32_16x16x32_bf16 a[12:15],a[216:219],v[64:67],a[12:15]
v_cvt_pk_bf16_f32 v80,v80,v81
v_cvt_pk_bf16_f32 v81,v82,v83
v_mfma_f32_16x16x32_bf16 a[12:15],a[220:223],v[72:75],a[12:15]
v_cvt_pk_bf16_f32 v82,v84,v85
v_cvt_pk_bf16_f32 v83,v86,v87
v_mfma_f32_16x16x32_bf16 a[16:19],a[224:227],v[64:67],a[16:19]
v_exp_f32 v88,v88
v_mfma_f32_16x16x32_bf16 a[16:19],a[228:231],v[72:75],a[16:19]
v_exp_f32 v89,v89
v_mfma_f32_16x16x32_bf16 a[20:23],a[232:235],v[64:67],a[20:23]
v_exp_f32 v90,v90
v_mfma_f32_16x16x32_bf16 a[20:23],a[236:239],v[72:75],a[20:23]
s_waitcnt lgkmcnt(0)
v_exp_f32 v91,v91
v_mfma_f32_16x16x32_bf16 a[24:27],a[240:243],v[64:67],a[24:27]
v_exp_f32 v92,v92
v_mfma_f32_16x16x32_bf16 a[24:27],a[244:247],v[72:75],a[24:27]
v_exp_f32 v93,v93
v_mfma_f32_16x16x32_bf16 a[28:31],a[248:251],v[64:67],a[28:31]
L_pd5_%=:
s_cbranch_execz L_trt0_%=
v_exp_f32 v94,v94
v_mfma_f32_16x16x32_bf16 a[28:31],a[252:255],v[72:75],a[28:31]
s_getpc_b64 s[92:93]
s_cmp_lg_u32 s4,0
s_cbranch_scc1 L_pace_%=
v_mfma_f32_16x16x32_bf16 v[48:51],v[28:31],v[64:67],v[48:51]
v_exp_f32 v95,v95
v_mfma_f32_16x16x32_bf16 v[48:51],v[28:31],v[72:75],v[48:51]
s_add_u32 s10,s20,s74
s_addc_u32 s11,s21,0
v_cvt_pk_bf16_f32 v88,v88,v89
s_waitcnt vmcnt(13)
s_barrier
v_mfma_f32_16x16x32_bf16 v[112:115],a[128:131],v[228:231],v[44:47]
s_add_u32 m0,s76,0
v_cvt_pk_bf16_f32 v89,v90,v91
v_cvt_pk_bf16_f32 v90,v92,v93
v_mfma_f32_16x16x32_bf16 v[112:115],a[132:135],v[236:239],v[112:115]
ds_read_b128 a[128:131],v1
v_cvt_pk_bf16_f32 v91,v94,v95
v_mfma_f32_16x16x32_bf16 v[112:115],a[136:139],v[244:247],v[112:115]
global_load_lds_dwordx4 v4,s[10:11] offset:1024 nt
ds_read_b128 a[132:135],v1 offset:256
v_mfma_f32_16x16x32_bf16 v[112:115],a[140:143],v[252:255],v[112:115]
v_exp_f32 v96,v96
v_mfma_f32_16x16x32_bf16 v[116:119],a[156:159],v[252:255],v[44:47]
v_exp_f32 v97,v97
v_mfma_f32_16x16x32_bf16 v[116:119],a[152:155],v[244:247],v[116:119]
ds_read_b128 a[136:139],v1 offset:512
v_cvt_pk_bf16_f32 v96,v96,v97
v_mfma_f32_16x16x32_bf16 v[116:119],a[148:151],v[236:239],v[116:119]
v_exp_f32 v98,v98
v_mfma_f32_16x16x32_bf16 v[116:119],a[144:147],v[228:231],v[116:119]
v_exp_f32 v99,v99
v_mfma_f32_16x16x32_bf16 v[120:123],a[160:163],v[228:231],v[44:47]
ds_read_b128 a[140:143],v1 offset:768
v_cvt_pk_bf16_f32 v97,v98,v99
v_mfma_f32_16x16x32_bf16 v[120:123],a[164:167],v[236:239],v[120:123]
v_exp_f32 v100,v100
v_mfma_f32_16x16x32_bf16 v[120:123],a[168:171],v[244:247],v[120:123]
v_exp_f32 v101,v101
v_mfma_f32_16x16x32_bf16 v[120:123],a[172:175],v[252:255],v[120:123]
ds_read_b128 a[156:159],v1 offset:4864
v_cvt_pk_bf16_f32 v98,v100,v101
v_mfma_f32_16x16x32_bf16 v[124:127],a[188:191],v[252:255],v[44:47]
v_exp_f32 v102,v102
v_mfma_f32_16x16x32_bf16 v[124:127],a[184:187],v[244:247],v[124:127]
v_exp_f32 v103,v103
v_mfma_f32_16x16x32_bf16 v[124:127],a[180:183],v[236:239],v[124:127]
ds_read_b128 a[152:155],v1 offset:4608
v_cvt_pk_bf16_f32 v99,v102,v103
v_mfma_f32_16x16x32_bf16 v[124:127],a[176:179],v[228:231],v[124:127]
v_exp_f32 v104,v104
v_mfma_f32_16x16x32_bf16 a[32:35],a[192:195],v[80:83],a[32:35]
global_load_lds_dwordx4 v5,s[10:11] offset:2048 nt
ds_read_b128 a[148:151],v1 offset:4352
v_mfma_f32_16x16x32_bf16 a[32:35],a[196:199],v[88:91],a[32:35]
v_exp_f32 v105,v105
v_mfma_f32_16x16x32_bf16 a[36:39],a[200:203],v[80:83],a[36:39]
ds_read_b128 a[144:147],v1 offset:4096
v_cvt_pk_bf16_f32 v104,v104,v105
v_mfma_f32_16x16x32_bf16 a[36:39],a[204:207],v[88:91],a[36:39]
v_exp_f32 v106,v106
v_mfma_f32_16x16x32_bf16 a[40:43],a[208:211],v[80:83],a[40:43]
v_exp_f32 v107,v107
v_mfma_f32_16x16x32_bf16 a[40:43],a[212:215],v[88:91],a[40:43]
ds_read_b128 a[160:163],v1 offset:8192
v_cvt_pk_bf16_f32 v105,v106,v107
v_mfma_f32_16x16x32_bf16 a[44:47],a[216:219],v[80:83],a[44:47]
v_exp_f32 v108,v108
v_mfma_f32_16x16x32_bf16 a[44:47],a[220:223],v[88:91],a[44:47]
v_exp_f32 v109,v109
v_mfma_f32_16x16x32_bf16 a[48:51],a[224:227],v[80:83],a[48:51]
ds_read_b128 a[164:167],v1 offset:8448
v_cvt_pk_bf16_f32 v106,v108,v109
v_mfma_f32_16x16x32_bf16 a[48:51],a[228:231],v[88:91],a[48:51]
v_exp_f32 v110,v110
v_mfma_f32_16x16x32_bf16 a[52:55],a[232:235],v[80:83],a[52:55]
v_exp_f32 v111,v111
v_mfma_f32_16x16x32_bf16 a[52:55],a[236:239],v[88:91],a[52:55]
ds_read_b128 a[168:171],v1 offset:8704
v_cvt_pk_bf16_f32 v107,v110,v111
v_mfma_f32_16x16x32_bf16 a[56:59],a[240:243],v[80:83],a[56:59]
v_exp_f32 v112,v112
v_mfma_f32_16x16x32_bf16 a[56:59],a[244:247],v[88:91],a[56:59]
v_exp_f32 v113,v113
v_mfma_f32_16x16x32_bf16 a[60:63],a[248:251],v[80:83],a[60:63]
global_load_lds_dwordx4 v5,s[10:11] offset:3072
ds_read_b128 a[172:175],v1 offset:8960
v_mfma_f32_16x16x32_bf16 a[60:63],a[252:255],v[88:91],a[60:63]
ds_read_b128 a[188:191],v1 offset:13056
v_cvt_pk_bf16_f32 v112,v112,v113
v_mfma_f32_16x16x32_bf16 v[52:55],v[28:31],v[80:83],v[52:55]
v_exp_f32 v114,v114
v_mfma_f32_16x16x32_bf16 v[52:55],v[28:31],v[88:91],v[52:55]
s_waitcnt lgkmcnt(3)
v_exp_f32 v115,v115
v_mfma_f32_16x16x32_bf16 v[64:67],a[128:131],v[132:135],v[32:35]
ds_read_b128 a[184:187],v1 offset:12800
v_cvt_pk_bf16_f32 v113,v114,v115
v_mfma_f32_16x16x32_bf16 v[64:67],a[132:135],v[140:143],v[64:67]
v_exp_f32 v116,v116
v_mfma_f32_16x16x32_bf16 v[64:67],a[136:139],v[148:151],v[64:67]
ds_read_b128 a[180:183],v1 offset:12544
v_mfma_f32_16x16x32_bf16 v[64:67],a[140:143],v[156:159],v[64:67]
ds_read_b128 a[176:179],v1 offset:12288
v_add_u32 v1,s78,v1
v_mfma_f32_16x16x32_bf16 v[68:71],a[156:159],v[156:159],v[32:35]
v_exp_f32 v117,v117
v_mfma_f32_16x16x32_bf16 v[68:71],a[152:155],v[148:151],v[68:71]
v_exp_f32 v118,v118
v_mfma_f32_16x16x32_bf16 v[68:71],a[148:151],v[140:143],v[68:71]
s_add_u32 s10,s24,s74
s_addc_u32 s11,s25,0
v_cvt_pk_bf16_f32 v114,v116,v117
v_mfma_f32_16x16x32_bf16 v[68:71],a[144:147],v[132:135],v[68:71]
s_add_u32 s74,s74,16384
v_exp_f32 v119,v119
v_mfma_f32_16x16x32_bf16 v[72:75],a[160:163],v[132:135],v[32:35]
s_add_u32 m0,s76,16384
v_exp_f32 v120,v120
v_mfma_f32_16x16x32_bf16 v[72:75],a[164:167],v[140:143],v[72:75]
s_and_b32 s74,s74,0x3ffff
v_exp_f32 v121,v121
s_waitcnt lgkmcnt(2)
v_mfma_f32_16x16x32_bf16 v[72:75],a[168:171],v[148:151],v[72:75]
global_load_lds_dwordx4 v4,s[10:11]
v_cvt_pk_bf16_f32 v115,v118,v119
v_mfma_f32_16x16x32_bf16 v[72:75],a[172:175],v[156:159],v[72:75]
s_or_b32 s74,s74,s69
v_exp_f32 v122,v122
v_mfma_f32_16x16x32_bf16 v[76:79],a[188:191],v[156:159],v[32:35]
s_sub_u32 s76,s76,32768
v_exp_f32 v123,v123
v_mfma_f32_16x16x32_bf16 v[76:79],a[184:187],v[148:151],v[76:79]
s_and_b32 s76,s76,0x1ffff
v_exp_f32 v124,v124
s_waitcnt lgkmcnt(0)
v_mfma_f32_16x16x32_bf16 v[76:79],a[180:183],v[140:143],v[76:79]
s_and_b32 s69,s75,0xfffc0000
v_exp_f32 v125,v125
v_mfma_f32_16x16x32_bf16 v[76:79],a[176:179],v[132:135],v[76:79]
v_exp_f32 v126,v126
v_mfma_f32_16x16x32_bf16 a[64:67],a[192:195],v[96:99],a[64:67]
v_exp_f32 v127,v127
v_mfma_f32_16x16x32_bf16 a[64:67],a[196:199],v[104:107],a[64:67]
v_cvt_pk_bf16_f32 v120,v120,v121
v_cvt_pk_bf16_f32 v121,v122,v123
v_mfma_f32_16x16x32_bf16 a[68:71],a[200:203],v[96:99],a[68:71]
v_cvt_pk_bf16_f32 v122,v124,v125
v_cvt_pk_bf16_f32 v123,v126,v127
v_mfma_f32_16x16x32_bf16 a[68:71],a[204:207],v[104:107],a[68:71]
v_exp_f32 v64,v64
v_mfma_f32_16x16x32_bf16 a[72:75],a[208:211],v[96:99],a[72:75]
v_exp_f32 v65,v65
v_mfma_f32_16x16x32_bf16 a[72:75],a[212:215],v[104:107],a[72:75]
v_exp_f32 v66,v66
v_mfma_f32_16x16x32_bf16 a[76:79],a[216:219],v[96:99],a[76:79]
v_exp_f32 v67,v67
v_mfma_f32_16x16x32_bf16 a[76:79],a[220:223],v[104:107],a[76:79]
v_exp_f32 v68,v68
v_mfma_f32_16x16x32_bf16 a[80:83],a[224:227],v[96:99],a[80:83]
global_load_lds_dwordx4 v5,s[10:11] offset:1024
v_cvt_pk_bf16_f32 v64,v64,v65
v_mfma_f32_16x16x32_bf16 a[80:83],a[228:231],v[104:107],a[80:83]
v_exp_f32 v69,v69
v_mfma_f32_16x16x32_bf16 a[84:87],a[232:235],v[96:99],a[84:87]
v_exp_f32 v70,v70
v_mfma_f32_16x16x32_bf16 a[84:87],a[236:239],v[104:107],a[84:87]
v_exp_f32 v71,v71
v_mfma_f32_16x16x32_bf16 a[88:91],a[240:243],v[96:99],a[88:91]
v_cvt_pk_bf16_f32 v65,v66,v67
v_cvt_pk_bf16_f32 v66,v68,v69
v_mfma_f32_16x16x32_bf16 a[88:91],a[244:247],v[104:107],a[88:91]
)A"
R"A(v_exp_f32 v72,v72
v_mfma_f32_16x16x32_bf16 a[92:95],a[248:251],v[96:99],a[92:95]
v_exp_f32 v73,v73
v_mfma_f32_16x16x32_bf16 a[92:95],a[252:255],v[104:107],a[92:95]
v_cvt_pk_bf16_f32 v67,v70,v71
v_cvt_pk_bf16_f32 v72,v72,v73
v_mfma_f32_16x16x32_bf16 v[56:59],v[28:31],v[96:99],v[56:59]
v_exp_f32 v74,v74
v_mfma_f32_16x16x32_bf16 v[56:59],v[28:31],v[104:107],v[56:59]
v_exp_f32 v75,v75
v_mfma_f32_16x16x32_bf16 v[80:83],a[128:131],v[164:167],v[36:39]
v_exp_f32 v76,v76
v_mfma_f32_16x16x32_bf16 v[80:83],a[132:135],v[172:175],v[80:83]
v_exp_f32 v77,v77
v_mfma_f32_16x16x32_bf16 v[80:83],a[136:139],v[180:183],v[80:83]
v_exp_f32 v78,v78
v_mfma_f32_16x16x32_bf16 v[80:83],a[140:143],v[188:191],v[80:83]
v_exp_f32 v79,v79
v_mfma_f32_16x16x32_bf16 v[84:87],a[156:159],v[188:191],v[36:39]
v_cvt_pk_bf16_f32 v73,v74,v75
v_cvt_pk_bf16_f32 v74,v76,v77
v_mfma_f32_16x16x32_bf16 v[84:87],a[152:155],v[180:183],v[84:87]
global_load_lds_dwordx4 v4,s[10:11] offset:2048
v_cvt_pk_bf16_f32 v75,v78,v79
v_mfma_f32_16x16x32_bf16 v[84:87],a[148:151],v[172:175],v[84:87]
v_exp_f32 v80,v80
v_mfma_f32_16x16x32_bf16 v[84:87],a[144:147],v[164:167],v[84:87]
v_exp_f32 v81,v81
v_mfma_f32_16x16x32_bf16 v[88:91],a[160:163],v[164:167],v[36:39]
v_exp_f32 v82,v82
v_mfma_f32_16x16x32_bf16 v[88:91],a[164:167],v[172:175],v[88:91]
v_exp_f32 v83,v83
v_mfma_f32_16x16x32_bf16 v[88:91],a[168:171],v[180:183],v[88:91]
v_cvt_pk_bf16_f32 v80,v80,v81
v_cvt_pk_bf16_f32 v81,v82,v83
v_mfma_f32_16x16x32_bf16 v[88:91],a[172:175],v[188:191],v[88:91]
v_exp_f32 v84,v84
v_mfma_f32_16x16x32_bf16 v[92:95],a[188:191],v[188:191],v[36:39]
v_exp_f32 v85,v85
v_mfma_f32_16x16x32_bf16 v[92:95],a[184:187],v[180:183],v[92:95]
v_exp_f32 v86,v86
v_mfma_f32_16x16x32_bf16 v[92:95],a[180:183],v[172:175],v[92:95]
v_exp_f32 v87,v87
v_mfma_f32_16x16x32_bf16 v[92:95],a[176:179],v[164:167],v[92:95]
v_cvt_pk_bf16_f32 v82,v84,v85
v_cvt_pk_bf16_f32 v83,v86,v87
v_mfma_f32_16x16x32_bf16 a[96:99],a[192:195],v[112:115],a[96:99]
ds_read_b64_tr_b16 a[192:193],v2
ds_read_b64_tr_b16 a[194:195],v2 offset:4096
v_mfma_f32_16x16x32_bf16 a[96:99],a[196:199],v[120:123],a[96:99]
ds_read_b64_tr_b16 a[196:197],v2 offset:8192
ds_read_b64_tr_b16 a[198:199],v2 offset:12288
v_mfma_f32_16x16x32_bf16 a[100:103],a[200:203],v[112:115],a[100:103]
ds_read_b64_tr_b16 a[200:201],v3
ds_read_b64_tr_b16 a[202:203],v3 offset:4096
v_mfma_f32_16x16x32_bf16 a[100:103],a[204:207],v[120:123],a[100:103]
global_load_lds_dwordx4 v5,s[10:11] offset:3072
ds_read_b64_tr_b16 a[204:205],v3 offset:8192
v_mfma_f32_16x16x32_bf16 a[104:107],a[208:211],v[112:115],a[104:107]
ds_read_b64_tr_b16 a[206:207],v3 offset:12288
ds_read_b64_tr_b16 a[208:209],v2 offset:256
v_mfma_f32_16x16x32_bf16 a[104:107],a[212:215],v[120:123],a[104:107]
ds_read_b64_tr_b16 a[210:211],v2 offset:4352
ds_read_b64_tr_b16 a[212:213],v2 offset:8448
v_mfma_f32_16x16x32_bf16 a[108:111],a[216:219],v[112:115],a[108:111]
v_exp_f32 v88,v88
v_mfma_f32_16x16x32_bf16 a[108:111],a[220:223],v[120:123],a[108:111]
ds_read_b64_tr_b16 a[214:215],v2 offset:12544
ds_read_b64_tr_b16 a[216:217],v3 offset:256
v_mfma_f32_16x16x32_bf16 a[112:115],a[224:227],v[112:115],a[112:115]
ds_read_b64_tr_b16 a[218:219],v3 offset:4352
ds_read_b64_tr_b16 a[220:221],v3 offset:8448
v_mfma_f32_16x16x32_bf16 a[112:115],a[228:231],v[120:123],a[112:115]
ds_read_b64_tr_b16 a[222:223],v3 offset:12544
ds_read_b64_tr_b16 a[224:225],v2 offset:512
v_mfma_f32_16x16x32_bf16 a[116:119],a[232:235],v[112:115],a[116:119]
v_exp_f32 v89,v89
v_mfma_f32_16x16x32_bf16 a[116:119],a[236:239],v[120:123],a[116:119]
ds_read_b64_tr_b16 a[226:227],v2 offset:4608
ds_read_b64_tr_b16 a[228:229],v2 offset:8704
v_mfma_f32_16x16x32_bf16 a[120:123],a[240:243],v[112:115],a[120:123]
ds_read_b64_tr_b16 a[230:231],v2 offset:12800
ds_read_b64_tr_b16 a[232:233],v3 offset:512
v_mfma_f32_16x16x32_bf16 a[120:123],a[244:247],v[120:123],a[120:123]
s_add_u32 s10,s20,s74
s_addc_u32 s11,s21,0
ds_read_b64_tr_b16 a[234:235],v3 offset:4608
v_mfma_f32_16x16x32_bf16 a[124:127],a[248:251],v[112:115],a[124:127]
ds_read_b64_tr_b16 a[236:237],v3 offset:8704
ds_read_b64_tr_b16 a[238:239],v3 offset:12800
v_mfma_f32_16x16x32_bf16 a[124:127],a[252:255],v[120:123],a[124:127]
s_add_u32 m0,s76,0
ds_read_b64_tr_b16 a[240:241],v2 offset:768
ds_read_b64_tr_b16 a[242:243],v2 offset:4864
v_mfma_f32_16x16x32_bf16 v[60:63],v[28:31],v[112:115],v[60:63]
ds_read_b64_tr_b16 a[244:245],v2 offset:8960
v_cvt_pk_bf16_f32 v88,v88,v89
v_mfma_f32_16x16x32_bf16 v[60:63],v[28:31],v[120:123],v[60:63]
global_load_lds_dwordx4 v4,s[10:11] nt
ds_read_b64_tr_b16 a[246:247],v2 offset:13056
v_mfma_f32_16x16x32_bf16 v[96:99],a[128:131],v[196:199],v[40:43]
ds_read_b64_tr_b16 a[248:249],v3 offset:768
ds_read_b64_tr_b16 a[250:251],v3 offset:4864
v_mfma_f32_16x16x32_bf16 v[96:99],a[132:135],v[204:207],v[96:99]
ds_read_b64_tr_b16 a[252:253],v3 offset:8960
ds_read_b64_tr_b16 a[254:255],v3 offset:13056
v_mfma_f32_16x16x32_bf16 v[96:99],a[136:139],v[212:215],v[96:99]
v_exp_f32 v90,v90
v_mfma_f32_16x16x32_bf16 v[96:99],a[140:143],v[220:223],v[96:99]
v_exp_f32 v91,v91
v_mfma_f32_16x16x32_bf16 v[100:103],a[156:159],v[220:223],v[40:43]
v_exp_f32 v92,v92
v_mfma_f32_16x16x32_bf16 v[100:103],a[152:155],v[212:215],v[100:103]
v_exp_f32 v93,v93
v_mfma_f32_16x16x32_bf16 v[100:103],a[148:151],v[204:207],v[100:103]
v_exp_f32 v94,v94
v_mfma_f32_16x16x32_bf16 v[100:103],a[144:147],v[196:199],v[100:103]
v_exp_f32 v95,v95
v_mfma_f32_16x16x32_bf16 v[104:107],a[160:163],v[196:199],v[40:43]
v_cvt_pk_bf16_f32 v89,v90,v91
v_cvt_pk_bf16_f32 v90,v92,v93
v_mfma_f32_16x16x32_bf16 v[104:107],a[164:167],v[204:207],v[104:107]
v_cvt_pk_bf16_f32 v91,v94,v95
v_add_u32 v2,s78,v2
v_mfma_f32_16x16x32_bf16 v[104:107],a[168:171],v[212:215],v[104:107]
v_exp_f32 v96,v96
v_mfma_f32_16x16x32_bf16 v[104:107],a[172:175],v[220:223],v[104:107]
v_exp_f32 v97,v97
v_mfma_f32_16x16x32_bf16 v[108:111],a[188:191],v[220:223],v[40:43]
v_exp_f32 v98,v98
v_mfma_f32_16x16x32_bf16 v[108:111],a[184:187],v[212:215],v[108:111]
global_load_lds_dwordx4 v4,s[10:11] offset:1024 nt
v_cvt_pk_bf16_f32 v96,v96,v97
v_mfma_f32_16x16x32_bf16 v[108:111],a[180:183],v[204:207],v[108:111]
v_exp_f32 v99,v99
v_mfma_f32_16x16x32_bf16 v[108:111],a[176:179],v[196:199],v[108:111]
s_waitcnt lgkmcnt(0)
v_exp_f32 v100,v100
v_mfma_f32_16x16x32_bf16 a[0:3],a[192:195],v[64:67],a[0:3]
v_exp_f32 v101,v101
v_mfma_f32_16x16x32_bf16 a[0:3],a[196:199],v[72:75],a[0:3]
v_exp_f32 v102,v102
v_mfma_f32_16x16x32_bf16 a[4:7],a[200:203],v[64:67],a[4:7]
v_exp_f32 v103,v103
v_mfma_f32_16x16x32_bf16 a[4:7],a[204:207],v[72:75],a[4:7]
v_cvt_pk_bf16_f32 v97,v98,v99
v_cvt_pk_bf16_f32 v98,v100,v101
v_mfma_f32_16x16x32_bf16 a[8:11],a[208:211],v[64:67],a[8:11]
v_cvt_pk_bf16_f32 v99,v102,v103
v_add_u32 v3,s78,v3
v_mfma_f32_16x16x32_bf16 a[8:11],a[212:215],v[72:75],a[8:11]
s_sub_u32 s78,s77,32768
v_exp_f32 v104,v104
v_mfma_f32_16x16x32_bf16 a[12:15],a[216:219],v[64:67],a[12:15]
s_and_b32 s78,s78,0x1ffff
v_exp_f32 v105,v105
v_mfma_f32_16x16x32_bf16 a[12:15],a[220:223],v[72:75],a[12:15]
s_sub_u32 s78,s78,s77
v_exp_f32 v106,v106
v_mfma_f32_16x16x32_bf16 a[16:19],a[224:227],v[64:67],a[16:19]
s_add_u32 s77,s77,s78
v_exp_f32 v107,v107
v_mfma_f32_16x16x32_bf16 a[16:19],a[228:231],v[72:75],a[16:19]
v_exp_f32 v108,v108
v_mfma_f32_16x16x32_bf16 a[20:23],a[232:235],v[64:67],a[20:23]
s_getreg_b32 s92,hwreg(HW_REG_IB_STS,0,4)
s_min_u32 s4,s92,s6
s_mov_b32 s6,s92
v_mfma_f32_16x16x32_bf16 a[20:23],a[236:239],v[72:75],a[20:23]
s_cmp_gt_u32 s4,s9
s_cselect_b32 s4,15,s5
v_cvt_pk_bf16_f32 v104,v104,v105
v_mfma_f32_16x16x32_bf16 a[24:27],a[240:243],v[64:67],a[24:27]
v_exp_f32 v109,v109
v_mfma_f32_16x16x32_bf16 a[24:27],a[244:247],v[72:75],a[24:27]
v_exp_f32 v110,v110
v_mfma_f32_16x16x32_bf16 a[28:31],a[248:251],v[64:67],a[28:31]
v_exp_f32 v111,v111
v_mfma_f32_16x16x32_bf16 a[28:31],a[252:255],v[72:75],a[28:31]
s_getpc_b64 s[92:93]
s_cmp_lg_u32 s4,0
s_cbranch_scc1 L_pace_%=
v_mfma_f32_16x16x32_bf16 v[48:51],v[28:31],v[64:67],v[48:51]
v_cvt_pk_bf16_f32 v105,v106,v107
v_cvt_pk_bf16_f32 v106,v108,v109
v_mfma_f32_16x16x32_bf16 v[48:51],v[28:31],v[72:75],v[48:51]
s_add_u32 s10,s20,s74
s_addc_u32 s11,s21,0
v_cvt_pk_bf16_f32 v107,v110,v111
s_waitcnt vmcnt(10)
s_barrier
v_mfma_f32_16x16x32_bf16 v[24:27],v[8:11],v[128:131],0
s_add_u32 m0,s76,0
v_mfma_f32_16x16x32_bf16 v[24:27],v[12:15],v[136:139],v[24:27]
v_mfma_f32_16x16x32_bf16 v[24:27],v[16:19],v[144:147],v[24:27]
global_load_lds_dwordx4 v5,s[10:11] offset:2048 nt
v_mfma_f32_16x16x32_bf16 v[24:27],v[20:23],v[152:155],v[24:27]
v_mfma_f32_16x16x32_bf16 v[112:115],a[128:131],v[228:231],v[44:47]
ds_read_b128 a[128:131],v1
v_mfma_f32_16x16x32_bf16 v[112:115],a[132:135],v[236:239],v[112:115]
ds_read_b128 a[132:135],v1 offset:256
v_mfma_f32_16x16x32_bf16 v[112:115],a[136:139],v[244:247],v[112:115]
ds_read_b128 a[136:139],v1 offset:512
v_mfma_f32_16x16x32_bf16 v[112:115],a[140:143],v[252:255],v[112:115]
ds_read_b128 a[140:143],v1 offset:768
v_mfma_f32_16x16x32_bf16 v[116:119],a[156:159],v[252:255],v[44:47]
v_max3_f32 v24,v24,v25,v26
v_max_f32 v24,v24,v27
v_mfma_f32_16x16x32_bf16 v[116:119],a[152:155],v[244:247],v[116:119]
ds_read_b128 a[156:159],v1 offset:4864
v_mov_b32 v25,v24
v_mfma_f32_16x16x32_bf16 v[116:119],a[148:151],v[236:239],v[116:119]
v_exp_f32 v112,v112
v_mfma_f32_16x16x32_bf16 v[116:119],a[144:147],v[228:231],v[116:119]
v_permlane32_swap_b32 v24,v25
v_mfma_f32_16x16x32_bf16 v[120:123],a[160:163],v[228:231],v[44:47]
v_max_f32 v24,v24,v25
v_mov_b32 v25,v24
v_mfma_f32_16x16x32_bf16 v[120:123],a[164:167],v[236:239],v[120:123]
v_exp_f32 v113,v113
v_mfma_f32_16x16x32_bf16 v[120:123],a[168:171],v[244:247],v[120:123]
v_permlane16_swap_b32 v24,v25
v_mfma_f32_16x16x32_bf16 v[120:123],a[172:175],v[252:255],v[120:123]
v_max_f32 v24,v24,v25
v_sub_f32 v27,0,v24
v_mfma_f32_16x16x32_bf16 v[124:127],a[188:191],v[252:255],v[44:47]
global_load_lds_dwordx4 v5,s[10:11] offset:3072
v_sub_f32 v26,0,v24
v_sub_f32 v25,0,v24
v_sub_f32 v24,0,v24
v_mfma_f32_16x16x32_bf16 v[124:127],a[184:187],v[244:247],v[124:127]
ds_write_b128 v7,v[24:27]
v_mfma_f32_16x16x32_bf16 v[124:127],a[180:183],v[236:239],v[124:127]
ds_read_b128 a[152:155],v1 offset:4608
v_cvt_pk_bf16_f32 v112,v112,v113
v_mfma_f32_16x16x32_bf16 v[124:127],a[176:179],v[228:231],v[124:127]
v_exp_f32 v114,v114
v_mfma_f32_16x16x32_bf16 v[24:27],v[8:11],v[160:163],0
v_exp_f32 v115,v115
v_mfma_f32_16x16x32_bf16 v[24:27],v[12:15],v[168:171],v[24:27]
ds_read_b128 a[148:151],v1 offset:4352
v_cvt_pk_bf16_f32 v113,v114,v115
v_mfma_f32_16x16x32_bf16 v[24:27],v[16:19],v[176:179],v[24:27]
v_exp_f32 v116,v116
v_mfma_f32_16x16x32_bf16 v[24:27],v[20:23],v[184:187],v[24:27]
v_exp_f32 v117,v117
v_mfma_f32_16x16x32_bf16 a[32:35],a[192:195],v[80:83],a[32:35]
ds_read_b128 a[144:147],v1 offset:4096
v_cvt_pk_bf16_f32 v114,v116,v117
v_mfma_f32_16x16x32_bf16 a[32:35],a[196:199],v[88:91],a[32:35]
v_exp_f32 v118,v118
v_mfma_f32_16x16x32_bf16 a[36:39],a[200:203],v[80:83],a[36:39]
s_add_u32 s10,s24,s74
s_addc_u32 s11,s25,0
ds_read_b128 a[160:163],v1 offset:8192
v_mfma_f32_16x16x32_bf16 a[36:39],a[204:207],v[88:91],a[36:39]
s_add_u32 s74,s74,16384
v_max3_f32 v24,v24,v25,v26
v_max_f32 v24,v24,v27
v_mfma_f32_16x16x32_bf16 a[40:43],a[208:211],v[80:83],a[40:43]
s_add_u32 m0,s76,16384
ds_read_b128 a[164:167],v1 offset:8448
v_mov_b32 v25,v24
v_mfma_f32_16x16x32_bf16 a[40:43],a[212:215],v[88:91],a[40:43]
s_and_b32 s74,s74,0x3ffff
v_exp_f32 v119,v119
v_mfma_f32_16x16x32_bf16 a[44:47],a[216:219],v[80:83],a[44:47]
global_load_lds_dwordx4 v4,s[10:11]
ds_read_b128 a[168:171],v1 offset:8704
v_mfma_f32_16x16x32_bf16 a[44:47],a[220:223],v[88:91],a[44:47]
s_or_b32 s74,s74,s69
v_permlane32_swap_b32 v24,v25
v_mfma_f32_16x16x32_bf16 a[48:51],a[224:227],v[80:83],a[48:51]
s_sub_u32 s76,s76,32768
v_max_f32 v24,v24,v25
v_mov_b32 v25,v24
v_mfma_f32_16x16x32_bf16 a[48:51],a[228:231],v[88:91],a[48:51]
s_and_b32 s76,s76,0x1ffff
ds_read_b128 a[172:175],v1 offset:8960
v_cvt_pk_bf16_f32 v115,v118,v119
v_mfma_f32_16x16x32_bf16 a[52:55],a[232:235],v[80:83],a[52:55]
v_permlane16_swap_b32 v24,v25
v_mfma_f32_16x16x32_bf16 a[52:55],a[236:239],v[88:91],a[52:55]
)A"
R"A(v_max_f32 v24,v24,v25
v_sub_f32 v27,0,v24
v_mfma_f32_16x16x32_bf16 a[56:59],a[240:243],v[80:83],a[56:59]
v_sub_f32 v26,0,v24
v_sub_f32 v25,0,v24
v_mfma_f32_16x16x32_bf16 a[56:59],a[244:247],v[88:91],a[56:59]
ds_read_b128 a[188:191],v1 offset:13056
v_sub_f32 v24,0,v24
v_mfma_f32_16x16x32_bf16 a[60:63],a[248:251],v[80:83],a[60:63]
ds_write_b128 v7,v[24:27] offset:1024
v_mfma_f32_16x16x32_bf16 a[60:63],a[252:255],v[88:91],a[60:63]
v_exp_f32 v120,v120
v_mfma_f32_16x16x32_bf16 v[52:55],v[28:31],v[80:83],v[52:55]
v_exp_f32 v121,v121
v_mfma_f32_16x16x32_bf16 v[52:55],v[28:31],v[88:91],v[52:55]
ds_read_b128 a[184:187],v1 offset:12800
v_cvt_pk_bf16_f32 v120,v120,v121
v_mfma_f32_16x16x32_bf16 v[24:27],v[8:11],v[192:195],0
v_exp_f32 v122,v122
v_mfma_f32_16x16x32_bf16 v[24:27],v[12:15],v[200:203],v[24:27]
v_exp_f32 v123,v123
v_mfma_f32_16x16x32_bf16 v[24:27],v[16:19],v[208:211],v[24:27]
s_waitcnt lgkmcnt(2)
ds_read_b128 a[180:183],v1 offset:12544
v_cvt_pk_bf16_f32 v121,v122,v123
v_mfma_f32_16x16x32_bf16 v[24:27],v[20:23],v[216:219],v[24:27]
global_load_lds_dwordx4 v5,s[10:11] offset:1024
ds_read_b128 a[176:179],v1 offset:12288
v_mfma_f32_16x16x32_bf16 v[64:67],a[128:131],v[132:135],v[32:35]
v_exp_f32 v124,v124
v_mfma_f32_16x16x32_bf16 v[64:67],a[132:135],v[140:143],v[64:67]
v_exp_f32 v125,v125
v_mfma_f32_16x16x32_bf16 v[64:67],a[136:139],v[148:151],v[64:67]
v_exp_f32 v126,v126
v_mfma_f32_16x16x32_bf16 v[64:67],a[140:143],v[156:159],v[64:67]
v_max3_f32 v24,v24,v25,v26
v_max_f32 v24,v24,v27
v_mfma_f32_16x16x32_bf16 v[68:71],a[156:159],v[156:159],v[32:35]
v_mov_b32 v25,v24
v_cvt_pk_bf16_f32 v122,v124,v125
v_mfma_f32_16x16x32_bf16 v[68:71],a[152:155],v[148:151],v[68:71]
v_exp_f32 v127,v127
v_mfma_f32_16x16x32_bf16 v[68:71],a[148:151],v[140:143],v[68:71]
v_permlane32_swap_b32 v24,v25
v_mfma_f32_16x16x32_bf16 v[68:71],a[144:147],v[132:135],v[68:71]
v_max_f32 v24,v24,v25
v_mov_b32 v25,v24
v_mfma_f32_16x16x32_bf16 v[72:75],a[160:163],v[132:135],v[32:35]
v_cvt_pk_bf16_f32 v123,v126,v127
v_add_u32 v1,s78,v1
v_mfma_f32_16x16x32_bf16 v[72:75],a[164:167],v[140:143],v[72:75]
v_permlane16_swap_b32 v24,v25
v_mfma_f32_16x16x32_bf16 v[72:75],a[168:171],v[148:151],v[72:75]
v_max_f32 v24,v24,v25
v_sub_f32 v27,0,v24
v_mfma_f32_16x16x32_bf16 v[72:75],a[172:175],v[156:159],v[72:75]
v_sub_f32 v26,0,v24
v_sub_f32 v25,0,v24
v_mfma_f32_16x16x32_bf16 v[76:79],a[188:191],v[156:159],v[32:35]
s_waitcnt lgkmcnt(0)
v_exp_f32 v64,v64
v_mfma_f32_16x16x32_bf16 v[76:79],a[184:187],v[148:151],v[76:79]
global_load_lds_dwordx4 v4,s[10:11] offset:2048
v_sub_f32 v24,0,v24
v_mfma_f32_16x16x32_bf16 v[76:79],a[180:183],v[140:143],v[76:79]
ds_write_b128 v7,v[24:27] offset:2048
v_mfma_f32_16x16x32_bf16 v[76:79],a[176:179],v[132:135],v[76:79]
v_exp_f32 v65,v65
v_mfma_f32_16x16x32_bf16 a[64:67],a[192:195],v[96:99],a[64:67]
v_exp_f32 v66,v66
v_mfma_f32_16x16x32_bf16 a[64:67],a[196:199],v[104:107],a[64:67]
v_exp_f32 v67,v67
v_mfma_f32_16x16x32_bf16 a[68:71],a[200:203],v[96:99],a[68:71]
v_exp_f32 v68,v68
v_mfma_f32_16x16x32_bf16 a[68:71],a[204:207],v[104:107],a[68:71]
v_exp_f32 v69,v69
v_mfma_f32_16x16x32_bf16 a[72:75],a[208:211],v[96:99],a[72:75]
v_exp_f32 v70,v70
v_mfma_f32_16x16x32_bf16 a[72:75],a[212:215],v[104:107],a[72:75]
v_exp_f32 v71,v71
v_mfma_f32_16x16x32_bf16 a[76:79],a[216:219],v[96:99],a[76:79]
v_cvt_pk_bf16_f32 v64,v64,v65
v_cvt_pk_bf16_f32 v65,v66,v67
v_mfma_f32_16x16x32_bf16 a[76:79],a[220:223],v[104:107],a[76:79]
v_cvt_pk_bf16_f32 v66,v68,v69
v_cvt_pk_bf16_f32 v67,v70,v71
v_mfma_f32_16x16x32_bf16 a[80:83],a[224:227],v[96:99],a[80:83]
v_exp_f32 v72,v72
v_mfma_f32_16x16x32_bf16 a[80:83],a[228:231],v[104:107],a[80:83]
v_exp_f32 v73,v73
v_mfma_f32_16x16x32_bf16 a[84:87],a[232:235],v[96:99],a[84:87]
v_exp_f32 v74,v74
v_mfma_f32_16x16x32_bf16 a[84:87],a[236:239],v[104:107],a[84:87]
v_exp_f32 v75,v75
v_mfma_f32_16x16x32_bf16 a[88:91],a[240:243],v[96:99],a[88:91]
global_load_lds_dwordx4 v5,s[10:11] offset:3072
v_cvt_pk_bf16_f32 v72,v72,v73
v_mfma_f32_16x16x32_bf16 a[88:91],a[244:247],v[104:107],a[88:91]
v_exp_f32 v76,v76
v_mfma_f32_16x16x32_bf16 a[92:95],a[248:251],v[96:99],a[92:95]
v_exp_f32 v77,v77
v_mfma_f32_16x16x32_bf16 a[92:95],a[252:255],v[104:107],a[92:95]
v_exp_f32 v78,v78
v_mfma_f32_16x16x32_bf16 v[56:59],v[28:31],v[96:99],v[56:59]
v_exp_f32 v79,v79
v_mfma_f32_16x16x32_bf16 v[56:59],v[28:31],v[104:107],v[56:59]
v_cvt_pk_bf16_f32 v73,v74,v75
v_cvt_pk_bf16_f32 v74,v76,v77
v_mfma_f32_16x16x32_bf16 v[24:27],v[8:11],v[224:227],0
v_cvt_pk_bf16_f32 v75,v78,v79
v_mfma_f32_16x16x32_bf16 v[24:27],v[12:15],v[232:235],v[24:27]
v_mfma_f32_16x16x32_bf16 v[24:27],v[16:19],v[240:243],v[24:27]
v_mfma_f32_16x16x32_bf16 v[24:27],v[20:23],v[248:251],v[24:27]
v_mfma_f32_16x16x32_bf16 v[80:83],a[128:131],v[164:167],v[36:39]
s_add_u32 s10,s20,s74
s_addc_u32 s11,s21,0
v_mfma_f32_16x16x32_bf16 v[80:83],a[132:135],v[172:175],v[80:83]
v_mfma_f32_16x16x32_bf16 v[80:83],a[136:139],v[180:183],v[80:83]
s_add_u32 m0,s76,0
v_mfma_f32_16x16x32_bf16 v[80:83],a[140:143],v[188:191],v[80:83]
v_mfma_f32_16x16x32_bf16 v[84:87],a[156:159],v[188:191],v[36:39]
global_load_lds_dwordx4 v4,s[10:11] nt
v_max3_f32 v24,v24,v25,v26
v_mfma_f32_16x16x32_bf16 v[84:87],a[152:155],v[180:183],v[84:87]
v_max_f32 v24,v24,v27
v_mov_b32 v25,v24
v_mfma_f32_16x16x32_bf16 v[84:87],a[148:151],v[172:175],v[84:87]
v_mfma_f32_16x16x32_bf16 v[84:87],a[144:147],v[164:167],v[84:87]
v_permlane32_swap_b32 v24,v25
v_mfma_f32_16x16x32_bf16 v[88:91],a[160:163],v[164:167],v[36:39]
v_max_f32 v24,v24,v25
v_mov_b32 v25,v24
v_mfma_f32_16x16x32_bf16 v[88:91],a[164:167],v[172:175],v[88:91]
v_exp_f32 v80,v80
v_mfma_f32_16x16x32_bf16 v[88:91],a[168:171],v[180:183],v[88:91]
v_permlane16_swap_b32 v24,v25
v_mfma_f32_16x16x32_bf16 v[88:91],a[172:175],v[188:191],v[88:91]
v_max_f32 v24,v24,v25
v_max_f32 v24,s72,v24
v_mfma_f32_16x16x32_bf16 v[92:95],a[188:191],v[188:191],v[36:39]
v_sub_f32 v27,0,v24
v_sub_f32 v26,0,v24
v_mfma_f32_16x16x32_bf16 v[92:95],a[184:187],v[180:183],v[92:95]
v_sub_f32 v25,0,v24
v_sub_f32 v24,0,v24
v_mfma_f32_16x16x32_bf16 v[92:95],a[180:183],v[172:175],v[92:95]
v_exp_f32 v81,v81
v_mfma_f32_16x16x32_bf16 v[92:95],a[176:179],v[164:167],v[92:95]
v_exp_f32 v82,v82
v_mfma_f32_16x16x32_bf16 a[96:99],a[192:195],v[112:115],a[96:99]
ds_read_b64_tr_b16 a[192:193],v2
ds_read_b64_tr_b16 a[194:195],v2 offset:4096
v_mfma_f32_16x16x32_bf16 a[96:99],a[196:199],v[120:123],a[96:99]
ds_read_b64_tr_b16 a[196:197],v2 offset:8192
ds_read_b64_tr_b16 a[198:199],v2 offset:12288
v_mfma_f32_16x16x32_bf16 a[100:103],a[200:203],v[112:115],a[100:103]
ds_read_b64_tr_b16 a[200:201],v3
ds_read_b64_tr_b16 a[202:203],v3 offset:4096
L_pd6_%=:
s_cbranch_execz L_trt0_%=
v_mfma_f32_16x16x32_bf16 a[100:103],a[204:207],v[120:123],a[100:103]
global_load_lds_dwordx4 v4,s[10:11] offset:1024 nt
ds_read_b64_tr_b16 a[204:205],v3 offset:8192
v_mfma_f32_16x16x32_bf16 a[104:107],a[208:211],v[112:115],a[104:107]
ds_read_b64_tr_b16 a[206:207],v3 offset:12288
ds_read_b64_tr_b16 a[208:209],v2 offset:256
v_mfma_f32_16x16x32_bf16 a[104:107],a[212:215],v[120:123],a[104:107]
ds_read_b64_tr_b16 a[210:211],v2 offset:4352
ds_read_b64_tr_b16 a[212:213],v2 offset:8448
v_mfma_f32_16x16x32_bf16 a[108:111],a[216:219],v[112:115],a[108:111]
ds_read_b64_tr_b16 a[214:215],v2 offset:12544
v_cvt_pk_bf16_f32 v80,v80,v81
v_mfma_f32_16x16x32_bf16 a[108:111],a[220:223],v[120:123],a[108:111]
ds_read_b64_tr_b16 a[216:217],v3 offset:256
ds_read_b64_tr_b16 a[218:219],v3 offset:4352
v_mfma_f32_16x16x32_bf16 a[112:115],a[224:227],v[112:115],a[112:115]
v_exp_f32 v83,v83
v_mfma_f32_16x16x32_bf16 a[112:115],a[228:231],v[120:123],a[112:115]
ds_read_b64_tr_b16 a[220:221],v3 offset:8448
ds_read_b64_tr_b16 a[222:223],v3 offset:12544
v_mfma_f32_16x16x32_bf16 a[116:119],a[232:235],v[112:115],a[116:119]
ds_read_b64_tr_b16 a[224:225],v2 offset:512
ds_read_b64_tr_b16 a[226:227],v2 offset:4608
v_mfma_f32_16x16x32_bf16 a[116:119],a[236:239],v[120:123],a[116:119]
ds_read_b64_tr_b16 a[228:229],v2 offset:8704
ds_read_b64_tr_b16 a[230:231],v2 offset:12800
v_mfma_f32_16x16x32_bf16 a[120:123],a[240:243],v[112:115],a[120:123]
ds_read_b64_tr_b16 a[232:233],v3 offset:512
v_cvt_pk_bf16_f32 v81,v82,v83
v_mfma_f32_16x16x32_bf16 a[120:123],a[244:247],v[120:123],a[120:123]
ds_read_b64_tr_b16 a[234:235],v3 offset:4608
ds_read_b64_tr_b16 a[236:237],v3 offset:8704
v_mfma_f32_16x16x32_bf16 a[124:127],a[248:251],v[112:115],a[124:127]
v_exp_f32 v84,v84
v_mfma_f32_16x16x32_bf16 a[124:127],a[252:255],v[120:123],a[124:127]
ds_read_b64_tr_b16 a[238:239],v3 offset:12800
ds_read_b64_tr_b16 a[240:241],v2 offset:768
v_mfma_f32_16x16x32_bf16 v[60:63],v[28:31],v[112:115],v[60:63]
ds_read_b64_tr_b16 a[242:243],v2 offset:4864
ds_read_b64_tr_b16 a[244:245],v2 offset:8960
v_mfma_f32_16x16x32_bf16 v[60:63],v[28:31],v[120:123],v[60:63]
global_load_lds_dwordx4 v5,s[10:11] offset:2048 nt
ds_read_b64_tr_b16 a[246:247],v2 offset:13056
v_mfma_f32_16x16x32_bf16 v[96:99],a[128:131],v[196:199],v[40:43]
ds_read_b64_tr_b16 a[248:249],v3 offset:768
ds_read_b64_tr_b16 a[250:251],v3 offset:4864
v_mfma_f32_16x16x32_bf16 v[96:99],a[132:135],v[204:207],v[96:99]
ds_read_b64_tr_b16 a[252:253],v3 offset:8960
ds_read_b64_tr_b16 a[254:255],v3 offset:13056
v_mfma_f32_16x16x32_bf16 v[96:99],a[136:139],v[212:215],v[96:99]
v_exp_f32 v85,v85
v_mfma_f32_16x16x32_bf16 v[96:99],a[140:143],v[220:223],v[96:99]
v_exp_f32 v86,v86
v_mfma_f32_16x16x32_bf16 v[100:103],a[156:159],v[220:223],v[40:43]
v_exp_f32 v87,v87
v_mfma_f32_16x16x32_bf16 v[100:103],a[152:155],v[212:215],v[100:103]
v_exp_f32 v88,v88
v_mfma_f32_16x16x32_bf16 v[100:103],a[148:151],v[204:207],v[100:103]
v_exp_f32 v89,v89
v_mfma_f32_16x16x32_bf16 v[100:103],a[144:147],v[196:199],v[100:103]
v_exp_f32 v90,v90
v_mfma_f32_16x16x32_bf16 v[104:107],a[160:163],v[196:199],v[40:43]
v_exp_f32 v91,v91
v_mfma_f32_16x16x32_bf16 v[104:107],a[164:167],v[204:207],v[104:107]
v_exp_f32 v92,v92
v_mfma_f32_16x16x32_bf16 v[104:107],a[168:171],v[212:215],v[104:107]
v_exp_f32 v93,v93
v_mfma_f32_16x16x32_bf16 v[104:107],a[172:175],v[220:223],v[104:107]
v_exp_f32 v94,v94
v_mfma_f32_16x16x32_bf16 v[108:111],a[188:191],v[220:223],v[40:43]
v_exp_f32 v95,v95
v_mfma_f32_16x16x32_bf16 v[108:111],a[184:187],v[212:215],v[108:111]
v_exp_f32 v96,v96
v_mfma_f32_16x16x32_bf16 v[108:111],a[180:183],v[204:207],v[108:111]
global_load_lds_dwordx4 v5,s[10:11] offset:3072
v_cvt_pk_bf16_f32 v82,v84,v85
v_mfma_f32_16x16x32_bf16 v[108:111],a[176:179],v[196:199],v[108:111]
s_waitcnt lgkmcnt(0)
v_exp_f32 v97,v97
v_mfma_f32_16x16x32_bf16 a[0:3],a[192:195],v[64:67],a[0:3]
v_exp_f32 v98,v98
v_mfma_f32_16x16x32_bf16 a[0:3],a[196:199],v[72:75],a[0:3]
v_exp_f32 v99,v99
v_mfma_f32_16x16x32_bf16 a[4:7],a[200:203],v[64:67],a[4:7]
v_exp_f32 v100,v100
v_mfma_f32_16x16x32_bf16 a[4:7],a[204:207],v[72:75],a[4:7]
v_exp_f32 v101,v101
v_mfma_f32_16x16x32_bf16 a[8:11],a[208:211],v[64:67],a[8:11]
v_exp_f32 v102,v102
v_mfma_f32_16x16x32_bf16 a[8:11],a[212:215],v[72:75],a[8:11]
v_exp_f32 v103,v103
v_mfma_f32_16x16x32_bf16 a[12:15],a[216:219],v[64:67],a[12:15]
v_exp_f32 v104,v104
v_mfma_f32_16x16x32_bf16 a[12:15],a[220:223],v[72:75],a[12:15]
v_exp_f32 v105,v105
v_mfma_f32_16x16x32_bf16 a[16:19],a[224:227],v[64:67],a[16:19]
v_exp_f32 v106,v106
v_mfma_f32_16x16x32_bf16 a[16:19],a[228:231],v[72:75],a[16:19]
v_exp_f32 v107,v107
v_mfma_f32_16x16x32_bf16 a[20:23],a[232:235],v[64:67],a[20:23]
s_getreg_b32 s92,hwreg(HW_REG_IB_STS,0,4)
s_min_u32 s4,s92,s6
s_mov_b32 s6,s92
v_mfma_f32_16x16x32_bf16 a[20:23],a[236:239],v[72:75],a[20:23]
s_cmp_gt_u32 s4,s9
s_cselect_b32 s4,15,s5
v_cvt_pk_bf16_f32 v83,v86,v87
v_mfma_f32_16x16x32_bf16 a[24:27],a[240:243],v[64:67],a[24:27]
ds_write_b128 v7,v[24:27] offset:3072
v_mfma_f32_16x16x32_bf16 a[24:27],a[244:247],v[72:75],a[24:27]
v_cvt_pk_bf16_f32 v88,v88,v89
v_cvt_pk_bf16_f32 v89,v90,v91
v_mfma_f32_16x16x32_bf16 a[28:31],a[248:251],v[64:67],a[28:31]
v_cvt_pk_bf16_f32 v90,v92,v93
v_cvt_pk_bf16_f32 v91,v94,v95
v_mfma_f32_16x16x32_bf16 a[28:31],a[252:255],v[72:75],a[28:31]
s_getpc_b64 s[92:93]
s_cmp_lg_u32 s4,0
s_cbranch_scc1 L_pace_%=
)A"
R"A(v_mfma_f32_16x16x32_bf16 v[48:51],v[28:31],v[64:67],v[48:51]
v_cvt_pk_bf16_f32 v96,v96,v97
v_cvt_pk_bf16_f32 v97,v98,v99
v_mfma_f32_16x16x32_bf16 v[48:51],v[28:31],v[72:75],v[48:51]
v_cvt_pk_bf16_f32 v98,v100,v101
v_cvt_pk_bf16_f32 v99,v102,v103
v_cvt_pk_bf16_f32 v104,v104,v105
v_cvt_pk_bf16_f32 v105,v106,v107
v_exp_f32 v108,v108
v_exp_f32 v109,v109
s_mov_b32 s70,12
L_a0_%=:
s_waitcnt vmcnt(12)
s_barrier
v_mfma_f32_16x16x32_bf16 v[112:115],a[128:131],v[228:231],v[44:47]
s_add_u32 s10,s24,s74
s_addc_u32 s11,s25,0
s_add_u32 m0,s76,16384
v_mfma_f32_16x16x32_bf16 v[112:115],a[132:135],v[236:239],v[112:115]
s_add_u32 s75,s75,16384
ds_read_b128 a[128:131],v1
v_cvt_pk_bf16_f32 v106,v108,v109
v_mfma_f32_16x16x32_bf16 v[112:115],a[136:139],v[244:247],v[112:115]
s_add_u32 s74,s74,16384
global_load_lds_dwordx4 v4,s[10:11]
v_mfma_f32_16x16x32_bf16 v[112:115],a[140:143],v[252:255],v[112:115]
s_and_b32 s74,s74,0x3ffff
s_and_b32 s69,s75,0xfffc0000
ds_read_b128 a[132:135],v1 offset:256
v_mfma_f32_16x16x32_bf16 v[116:119],a[156:159],v[252:255],v[44:47]
s_or_b32 s74,s74,s69
v_exp_f32 v110,v110
v_mfma_f32_16x16x32_bf16 v[116:119],a[152:155],v[244:247],v[116:119]
s_sub_u32 s76,s76,32768
s_and_b32 s76,s76,0x1ffff
ds_read_b128 a[136:139],v1 offset:512
v_mfma_f32_16x16x32_bf16 v[116:119],a[148:151],v[236:239],v[116:119]
v_exp_f32 v111,v111
v_mfma_f32_16x16x32_bf16 v[116:119],a[144:147],v[228:231],v[116:119]
ds_read_b128 a[140:143],v1 offset:768
v_cvt_pk_bf16_f32 v107,v110,v111
v_mfma_f32_16x16x32_bf16 v[120:123],a[160:163],v[228:231],v[44:47]
v_exp_f32 v112,v112
v_mfma_f32_16x16x32_bf16 v[120:123],a[164:167],v[236:239],v[120:123]
ds_read_b128 a[156:159],v1 offset:4864
v_add_u32 v2,s78,v2
v_mfma_f32_16x16x32_bf16 v[120:123],a[168:171],v[244:247],v[120:123]
v_exp_f32 v113,v113
v_mfma_f32_16x16x32_bf16 v[120:123],a[172:175],v[252:255],v[120:123]
ds_read_b128 a[152:155],v1 offset:4608
v_cvt_pk_bf16_f32 v112,v112,v113
v_mfma_f32_16x16x32_bf16 v[124:127],a[188:191],v[252:255],v[44:47]
v_exp_f32 v114,v114
v_mfma_f32_16x16x32_bf16 v[124:127],a[184:187],v[244:247],v[124:127]
ds_read_b128 a[148:151],v1 offset:4352
v_add_u32 v3,s78,v3
v_mfma_f32_16x16x32_bf16 v[124:127],a[180:183],v[236:239],v[124:127]
s_sub_u32 s78,s77,32768
v_exp_f32 v115,v115
v_mfma_f32_16x16x32_bf16 v[124:127],a[176:179],v[228:231],v[124:127]
s_and_b32 s78,s78,0x1ffff
ds_read_b128 a[144:147],v1 offset:4096
v_cvt_pk_bf16_f32 v113,v114,v115
v_mfma_f32_16x16x32_bf16 a[32:35],a[192:195],v[80:83],a[32:35]
s_sub_u32 s78,s78,s77
v_exp_f32 v116,v116
v_mfma_f32_16x16x32_bf16 a[32:35],a[196:199],v[88:91],a[32:35]
s_add_u32 s77,s77,s78
v_exp_f32 v117,v117
v_mfma_f32_16x16x32_bf16 a[36:39],a[200:203],v[80:83],a[36:39]
global_load_lds_dwordx4 v5,s[10:11] offset:1024
ds_read_b128 a[160:163],v1 offset:8192
v_mfma_f32_16x16x32_bf16 a[36:39],a[204:207],v[88:91],a[36:39]
v_exp_f32 v118,v118
v_mfma_f32_16x16x32_bf16 a[40:43],a[208:211],v[80:83],a[40:43]
ds_read_b128 a[164:167],v1 offset:8448
v_cvt_pk_bf16_f32 v114,v116,v117
v_mfma_f32_16x16x32_bf16 a[40:43],a[212:215],v[88:91],a[40:43]
v_exp_f32 v119,v119
v_mfma_f32_16x16x32_bf16 a[44:47],a[216:219],v[80:83],a[44:47]
ds_read_b128 a[168:171],v1 offset:8704
v_cvt_pk_bf16_f32 v115,v118,v119
v_mfma_f32_16x16x32_bf16 a[44:47],a[220:223],v[88:91],a[44:47]
v_exp_f32 v120,v120
v_mfma_f32_16x16x32_bf16 a[48:51],a[224:227],v[80:83],a[48:51]
v_exp_f32 v121,v121
v_mfma_f32_16x16x32_bf16 a[48:51],a[228:231],v[88:91],a[48:51]
ds_read_b128 a[172:175],v1 offset:8960
v_cvt_pk_bf16_f32 v120,v120,v121
v_mfma_f32_16x16x32_bf16 a[52:55],a[232:235],v[80:83],a[52:55]
v_exp_f32 v122,v122
v_mfma_f32_16x16x32_bf16 a[52:55],a[236:239],v[88:91],a[52:55]
v_exp_f32 v123,v123
v_mfma_f32_16x16x32_bf16 a[56:59],a[240:243],v[80:83],a[56:59]
ds_read_b128 a[188:191],v1 offset:13056
v_cvt_pk_bf16_f32 v121,v122,v123
v_mfma_f32_16x16x32_bf16 a[56:59],a[244:247],v[88:91],a[56:59]
v_exp_f32 v124,v124
v_mfma_f32_16x16x32_bf16 a[60:63],a[248:251],v[80:83],a[60:63]
v_exp_f32 v125,v125
v_mfma_f32_16x16x32_bf16 a[60:63],a[252:255],v[88:91],a[60:63]
ds_read_b128 a[184:187],v1 offset:12800
v_cvt_pk_bf16_f32 v122,v124,v125
v_mfma_f32_16x16x32_bf16 v[52:55],v[28:31],v[80:83],v[52:55]
v_exp_f32 v126,v126
v_mfma_f32_16x16x32_bf16 v[52:55],v[28:31],v[88:91],v[52:55]
s_waitcnt lgkmcnt(2)
v_exp_f32 v127,v127
v_mfma_f32_16x16x32_bf16 v[64:67],a[128:131],v[132:135],v[32:35]
global_load_lds_dwordx4 v4,s[10:11] offset:2048
ds_read_b128 a[180:183],v1 offset:12544
v_mfma_f32_16x16x32_bf16 v[64:67],a[132:135],v[140:143],v[64:67]
v_cvt_pk_bf16_f32 v123,v126,v127
v_mfma_f32_16x16x32_bf16 v[64:67],a[136:139],v[148:151],v[64:67]
ds_read_b128 a[176:179],v1 offset:12288
v_add_u32 v1,s78,v1
v_mfma_f32_16x16x32_bf16 v[64:67],a[140:143],v[156:159],v[64:67]
v_mfma_f32_16x16x32_bf16 v[68:71],a[156:159],v[156:159],v[32:35]
v_mfma_f32_16x16x32_bf16 v[68:71],a[152:155],v[148:151],v[68:71]
v_mfma_f32_16x16x32_bf16 v[68:71],a[148:151],v[140:143],v[68:71]
v_mfma_f32_16x16x32_bf16 v[68:71],a[144:147],v[132:135],v[68:71]
v_mfma_f32_16x16x32_bf16 v[72:75],a[160:163],v[132:135],v[32:35]
v_mfma_f32_16x16x32_bf16 v[72:75],a[164:167],v[140:143],v[72:75]
v_mfma_f32_16x16x32_bf16 v[72:75],a[168:171],v[148:151],v[72:75]
v_mfma_f32_16x16x32_bf16 v[72:75],a[172:175],v[156:159],v[72:75]
s_waitcnt lgkmcnt(0)
v_exp_f32 v64,v64
v_mfma_f32_16x16x32_bf16 v[76:79],a[188:191],v[156:159],v[32:35]
v_exp_f32 v65,v65
v_mfma_f32_16x16x32_bf16 v[76:79],a[184:187],v[148:151],v[76:79]
v_exp_f32 v66,v66
v_mfma_f32_16x16x32_bf16 v[76:79],a[180:183],v[140:143],v[76:79]
v_exp_f32 v67,v67
v_mfma_f32_16x16x32_bf16 v[76:79],a[176:179],v[132:135],v[76:79]
v_exp_f32 v68,v68
v_mfma_f32_16x16x32_bf16 a[64:67],a[192:195],v[96:99],a[64:67]
global_load_lds_dwordx4 v5,s[10:11] offset:3072
v_cvt_pk_bf16_f32 v64,v64,v65
v_mfma_f32_16x16x32_bf16 a[64:67],a[196:199],v[104:107],a[64:67]
v_exp_f32 v69,v69
v_mfma_f32_16x16x32_bf16 a[68:71],a[200:203],v[96:99],a[68:71]
v_exp_f32 v70,v70
v_mfma_f32_16x16x32_bf16 a[68:71],a[204:207],v[104:107],a[68:71]
v_exp_f32 v71,v71
v_mfma_f32_16x16x32_bf16 a[72:75],a[208:211],v[96:99],a[72:75]
v_cvt_pk_bf16_f32 v65,v66,v67
v_cvt_pk_bf16_f32 v66,v68,v69
v_mfma_f32_16x16x32_bf16 a[72:75],a[212:215],v[104:107],a[72:75]
v_exp_f32 v72,v72
v_mfma_f32_16x16x32_bf16 a[76:79],a[216:219],v[96:99],a[76:79]
v_exp_f32 v73,v73
v_mfma_f32_16x16x32_bf16 a[76:79],a[220:223],v[104:107],a[76:79]
v_cvt_pk_bf16_f32 v67,v70,v71
v_cvt_pk_bf16_f32 v72,v72,v73
v_mfma_f32_16x16x32_bf16 a[80:83],a[224:227],v[96:99],a[80:83]
v_exp_f32 v74,v74
v_mfma_f32_16x16x32_bf16 a[80:83],a[228:231],v[104:107],a[80:83]
v_exp_f32 v75,v75
v_mfma_f32_16x16x32_bf16 a[84:87],a[232:235],v[96:99],a[84:87]
v_exp_f32 v76,v76
v_mfma_f32_16x16x32_bf16 a[84:87],a[236:239],v[104:107],a[84:87]
v_exp_f32 v77,v77
v_mfma_f32_16x16x32_bf16 a[88:91],a[240:243],v[96:99],a[88:91]
s_add_u32 s10,s20,s74
s_addc_u32 s11,s21,0
v_cvt_pk_bf16_f32 v73,v74,v75
v_mfma_f32_16x16x32_bf16 a[88:91],a[244:247],v[104:107],a[88:91]
v_exp_f32 v78,v78
v_mfma_f32_16x16x32_bf16 a[92:95],a[248:251],v[96:99],a[92:95]
s_add_u32 m0,s76,0
v_exp_f32 v79,v79
v_mfma_f32_16x16x32_bf16 a[92:95],a[252:255],v[104:107],a[92:95]
v_cvt_pk_bf16_f32 v74,v76,v77
v_cvt_pk_bf16_f32 v75,v78,v79
v_mfma_f32_16x16x32_bf16 v[56:59],v[28:31],v[96:99],v[56:59]
global_load_lds_dwordx4 v4,s[10:11] nt
v_mfma_f32_16x16x32_bf16 v[56:59],v[28:31],v[104:107],v[56:59]
v_mfma_f32_16x16x32_bf16 v[80:83],a[128:131],v[164:167],v[36:39]
v_mfma_f32_16x16x32_bf16 v[80:83],a[132:135],v[172:175],v[80:83]
v_mfma_f32_16x16x32_bf16 v[80:83],a[136:139],v[180:183],v[80:83]
v_mfma_f32_16x16x32_bf16 v[80:83],a[140:143],v[188:191],v[80:83]
v_mfma_f32_16x16x32_bf16 v[84:87],a[156:159],v[188:191],v[36:39]
v_mfma_f32_16x16x32_bf16 v[84:87],a[152:155],v[180:183],v[84:87]
v_mfma_f32_16x16x32_bf16 v[84:87],a[148:151],v[172:175],v[84:87]
v_mfma_f32_16x16x32_bf16 v[84:87],a[144:147],v[164:167],v[84:87]
v_mfma_f32_16x16x32_bf16 v[88:91],a[160:163],v[164:167],v[36:39]
v_mfma_f32_16x16x32_bf16 v[88:91],a[164:167],v[172:175],v[88:91]
v_mfma_f32_16x16x32_bf16 v[88:91],a[168:171],v[180:183],v[88:91]
v_mfma_f32_16x16x32_bf16 v[88:91],a[172:175],v[188:191],v[88:91]
v_exp_f32 v80,v80
v_mfma_f32_16x16x32_bf16 v[92:95],a[188:191],v[188:191],v[36:39]
v_exp_f32 v81,v81
v_mfma_f32_16x16x32_bf16 v[92:95],a[184:187],v[180:183],v[92:95]
v_exp_f32 v82,v82
v_mfma_f32_16x16x32_bf16 v[92:95],a[180:183],v[172:175],v[92:95]
global_load_lds_dwordx4 v4,s[10:11] offset:1024 nt
v_cvt_pk_bf16_f32 v80,v80,v81
v_mfma_f32_16x16x32_bf16 v[92:95],a[176:179],v[164:167],v[92:95]
v_exp_f32 v83,v83
v_mfma_f32_16x16x32_bf16 a[96:99],a[192:195],v[112:115],a[96:99]
ds_read_b64_tr_b16 a[192:193],v2
ds_read_b64_tr_b16 a[194:195],v2 offset:4096
v_mfma_f32_16x16x32_bf16 a[96:99],a[196:199],v[120:123],a[96:99]
ds_read_b64_tr_b16 a[196:197],v2 offset:8192
v_cvt_pk_bf16_f32 v81,v82,v83
v_mfma_f32_16x16x32_bf16 a[100:103],a[200:203],v[112:115],a[100:103]
v_exp_f32 v84,v84
v_mfma_f32_16x16x32_bf16 a[100:103],a[204:207],v[120:123],a[100:103]
ds_read_b64_tr_b16 a[198:199],v2 offset:12288
ds_read_b64_tr_b16 a[200:201],v3
v_mfma_f32_16x16x32_bf16 a[104:107],a[208:211],v[112:115],a[104:107]
v_exp_f32 v85,v85
v_mfma_f32_16x16x32_bf16 a[104:107],a[212:215],v[120:123],a[104:107]
ds_read_b64_tr_b16 a[202:203],v3 offset:4096
ds_read_b64_tr_b16 a[204:205],v3 offset:8192
v_mfma_f32_16x16x32_bf16 a[108:111],a[216:219],v[112:115],a[108:111]
ds_read_b64_tr_b16 a[206:207],v3 offset:12288
v_cvt_pk_bf16_f32 v82,v84,v85
v_mfma_f32_16x16x32_bf16 a[108:111],a[220:223],v[120:123],a[108:111]
v_exp_f32 v86,v86
v_mfma_f32_16x16x32_bf16 a[112:115],a[224:227],v[112:115],a[112:115]
ds_read_b64_tr_b16 a[208:209],v2 offset:256
ds_read_b64_tr_b16 a[210:211],v2 offset:4352
v_mfma_f32_16x16x32_bf16 a[112:115],a[228:231],v[120:123],a[112:115]
v_exp_f32 v87,v87
v_mfma_f32_16x16x32_bf16 a[116:119],a[232:235],v[112:115],a[116:119]
ds_read_b64_tr_b16 a[212:213],v2 offset:8448
ds_read_b64_tr_b16 a[214:215],v2 offset:12544
v_mfma_f32_16x16x32_bf16 a[116:119],a[236:239],v[120:123],a[116:119]
ds_read_b64_tr_b16 a[216:217],v3 offset:256
v_cvt_pk_bf16_f32 v83,v86,v87
v_mfma_f32_16x16x32_bf16 a[120:123],a[240:243],v[112:115],a[120:123]
v_exp_f32 v88,v88
v_mfma_f32_16x16x32_bf16 a[120:123],a[244:247],v[120:123],a[120:123]
ds_read_b64_tr_b16 a[218:219],v3 offset:4352
ds_read_b64_tr_b16 a[220:221],v3 offset:8448
v_mfma_f32_16x16x32_bf16 a[124:127],a[248:251],v[112:115],a[124:127]
global_load_lds_dwordx4 v5,s[10:11] offset:2048 nt
ds_read_b64_tr_b16 a[222:223],v3 offset:12544
v_mfma_f32_16x16x32_bf16 a[124:127],a[252:255],v[120:123],a[124:127]
v_exp_f32 v89,v89
v_mfma_f32_16x16x32_bf16 v[60:63],v[28:31],v[112:115],v[60:63]
ds_read_b64_tr_b16 a[224:225],v2 offset:512
ds_read_b64_tr_b16 a[226:227],v2 offset:4608
v_mfma_f32_16x16x32_bf16 v[60:63],v[28:31],v[120:123],v[60:63]
ds_read_b64_tr_b16 a[228:229],v2 offset:8704
v_cvt_pk_bf16_f32 v88,v88,v89
v_mfma_f32_16x16x32_bf16 v[96:99],a[128:131],v[196:199],v[40:43]
v_exp_f32 v90,v90
v_mfma_f32_16x16x32_bf16 v[96:99],a[132:135],v[204:207],v[96:99]
ds_read_b64_tr_b16 a[230:231],v2 offset:12800
ds_read_b64_tr_b16 a[232:233],v3 offset:512
v_mfma_f32_16x16x32_bf16 v[96:99],a[136:139],v[212:215],v[96:99]
v_exp_f32 v91,v91
v_mfma_f32_16x16x32_bf16 v[96:99],a[140:143],v[220:223],v[96:99]
ds_read_b64_tr_b16 a[234:235],v3 offset:4608
ds_read_b64_tr_b16 a[236:237],v3 offset:8704
v_mfma_f32_16x16x32_bf16 v[100:103],a[156:159],v[220:223],v[40:43]
ds_read_b64_tr_b16 a[238:239],v3 offset:12800
v_cvt_pk_bf16_f32 v89,v90,v91
v_mfma_f32_16x16x32_bf16 v[100:103],a[152:155],v[212:215],v[100:103]
v_exp_f32 v92,v92
v_mfma_f32_16x16x32_bf16 v[100:103],a[148:151],v[204:207],v[100:103]
ds_read_b64_tr_b16 a[240:241],v2 offset:768
ds_read_b64_tr_b16 a[242:243],v2 offset:4864
v_mfma_f32_16x16x32_bf16 v[100:103],a[144:147],v[196:199],v[100:103]
v_exp_f32 v93,v93
v_mfma_f32_16x16x32_bf16 v[104:107],a[160:163],v[196:199],v[40:43]
ds_read_b64_tr_b16 a[244:245],v2 offset:8960
ds_read_b64_tr_b16 a[246:247],v2 offset:13056
)A"
R"A(v_mfma_f32_16x16x32_bf16 v[104:107],a[164:167],v[204:207],v[104:107]
ds_read_b64_tr_b16 a[248:249],v3 offset:768
v_cvt_pk_bf16_f32 v90,v92,v93
v_mfma_f32_16x16x32_bf16 v[104:107],a[168:171],v[212:215],v[104:107]
v_exp_f32 v94,v94
v_mfma_f32_16x16x32_bf16 v[104:107],a[172:175],v[220:223],v[104:107]
ds_read_b64_tr_b16 a[250:251],v3 offset:4864
ds_read_b64_tr_b16 a[252:253],v3 offset:8960
v_mfma_f32_16x16x32_bf16 v[108:111],a[188:191],v[220:223],v[40:43]
global_load_lds_dwordx4 v5,s[10:11] offset:3072
ds_read_b64_tr_b16 a[254:255],v3 offset:13056
v_mfma_f32_16x16x32_bf16 v[108:111],a[184:187],v[212:215],v[108:111]
v_exp_f32 v95,v95
v_mfma_f32_16x16x32_bf16 v[108:111],a[180:183],v[204:207],v[108:111]
v_exp_f32 v96,v96
v_mfma_f32_16x16x32_bf16 v[108:111],a[176:179],v[196:199],v[108:111]
s_waitcnt lgkmcnt(6)
v_exp_f32 v97,v97
v_mfma_f32_16x16x32_bf16 a[0:3],a[192:195],v[64:67],a[0:3]
v_exp_f32 v98,v98
v_mfma_f32_16x16x32_bf16 a[0:3],a[196:199],v[72:75],a[0:3]
v_exp_f32 v99,v99
v_mfma_f32_16x16x32_bf16 a[4:7],a[200:203],v[64:67],a[4:7]
v_exp_f32 v100,v100
v_mfma_f32_16x16x32_bf16 a[4:7],a[204:207],v[72:75],a[4:7]
v_exp_f32 v101,v101
v_mfma_f32_16x16x32_bf16 a[8:11],a[208:211],v[64:67],a[8:11]
v_exp_f32 v102,v102
v_mfma_f32_16x16x32_bf16 a[8:11],a[212:215],v[72:75],a[8:11]
v_exp_f32 v103,v103
v_mfma_f32_16x16x32_bf16 a[12:15],a[216:219],v[64:67],a[12:15]
v_exp_f32 v104,v104
v_mfma_f32_16x16x32_bf16 a[12:15],a[220:223],v[72:75],a[12:15]
v_exp_f32 v105,v105
v_mfma_f32_16x16x32_bf16 a[16:19],a[224:227],v[64:67],a[16:19]
v_exp_f32 v106,v106
v_mfma_f32_16x16x32_bf16 a[16:19],a[228:231],v[72:75],a[16:19]
v_exp_f32 v107,v107
v_mfma_f32_16x16x32_bf16 a[20:23],a[232:235],v[64:67],a[20:23]
s_getreg_b32 s92,hwreg(HW_REG_IB_STS,0,4)
s_min_u32 s4,s92,s6
s_mov_b32 s6,s92
v_mfma_f32_16x16x32_bf16 a[20:23],a[236:239],v[72:75],a[20:23]
s_cmp_ge_u32 s4,s9
s_cselect_b32 s4,15,s5
v_cvt_pk_bf16_f32 v91,v94,v95
v_mfma_f32_16x16x32_bf16 a[24:27],a[240:243],v[64:67],a[24:27]
s_waitcnt lgkmcnt(0)
v_cvt_pk_bf16_f32 v96,v96,v97
v_cvt_pk_bf16_f32 v97,v98,v99
v_mfma_f32_16x16x32_bf16 a[24:27],a[244:247],v[72:75],a[24:27]
v_cvt_pk_bf16_f32 v98,v100,v101
v_cvt_pk_bf16_f32 v99,v102,v103
v_mfma_f32_16x16x32_bf16 a[28:31],a[248:251],v[64:67],a[28:31]
v_cvt_pk_bf16_f32 v104,v104,v105
v_cvt_pk_bf16_f32 v105,v106,v107
v_mfma_f32_16x16x32_bf16 a[28:31],a[252:255],v[72:75],a[28:31]
s_getpc_b64 s[92:93]
s_cmp_lg_u32 s4,0
s_cbranch_scc1 L_pace_%=
v_mfma_f32_16x16x32_bf16 v[48:51],v[28:31],v[64:67],v[48:51]
v_exp_f32 v108,v108
v_mfma_f32_16x16x32_bf16 v[48:51],v[28:31],v[72:75],v[48:51]
v_exp_f32 v109,v109
s_sub_u32 s70,s70,1
s_cmp_lg_u32 s70,0
s_cbranch_scc1 L_a0_%=
s_waitcnt vmcnt(12)
s_barrier
v_mfma_f32_16x16x32_bf16 v[112:115],a[128:131],v[228:231],v[44:47]
s_add_u32 s10,s24,s74
s_addc_u32 s11,s25,0
s_add_u32 m0,s76,16384
v_mfma_f32_16x16x32_bf16 v[112:115],a[132:135],v[236:239],v[112:115]
s_add_u32 s75,s75,16384
ds_read_b128 v[32:35],v7
v_cvt_pk_bf16_f32 v106,v108,v109
v_mfma_f32_16x16x32_bf16 v[112:115],a[136:139],v[244:247],v[112:115]
s_add_u32 s74,s74,16384
global_load_lds_dwordx4 v4,s[10:11]
v_mfma_f32_16x16x32_bf16 v[112:115],a[140:143],v[252:255],v[112:115]
s_and_b32 s74,s74,0x3ffff
s_and_b32 s69,s75,0xfffc0000
ds_read_b128 a[128:131],v1
v_mfma_f32_16x16x32_bf16 v[116:119],a[156:159],v[252:255],v[44:47]
s_or_b32 s74,s74,s69
v_exp_f32 v110,v110
v_mfma_f32_16x16x32_bf16 v[116:119],a[152:155],v[244:247],v[116:119]
s_sub_u32 s76,s76,32768
s_and_b32 s76,s76,0x1ffff
ds_read_b128 a[132:135],v1 offset:256
v_mfma_f32_16x16x32_bf16 v[116:119],a[148:151],v[236:239],v[116:119]
s_min_u32 s7,s7,0
v_exp_f32 v111,v111
v_mfma_f32_16x16x32_bf16 v[116:119],a[144:147],v[228:231],v[116:119]
s_min_u32 s4,s5,0
ds_read_b128 a[136:139],v1 offset:512
v_cvt_pk_bf16_f32 v107,v110,v111
v_mfma_f32_16x16x32_bf16 v[120:123],a[160:163],v[228:231],v[44:47]
v_exp_f32 v112,v112
v_mfma_f32_16x16x32_bf16 v[120:123],a[164:167],v[236:239],v[120:123]
ds_read_b128 a[140:143],v1 offset:768
v_add_u32 v2,s78,v2
v_mfma_f32_16x16x32_bf16 v[120:123],a[168:171],v[244:247],v[120:123]
v_exp_f32 v113,v113
v_mfma_f32_16x16x32_bf16 v[120:123],a[172:175],v[252:255],v[120:123]
ds_read_b128 a[156:159],v1 offset:4864
v_cvt_pk_bf16_f32 v112,v112,v113
v_mfma_f32_16x16x32_bf16 v[124:127],a[188:191],v[252:255],v[44:47]
v_exp_f32 v114,v114
v_mfma_f32_16x16x32_bf16 v[124:127],a[184:187],v[244:247],v[124:127]
ds_read_b128 a[152:155],v1 offset:4608
v_add_u32 v3,s78,v3
v_mfma_f32_16x16x32_bf16 v[124:127],a[180:183],v[236:239],v[124:127]
s_sub_u32 s78,s77,32768
v_exp_f32 v115,v115
v_mfma_f32_16x16x32_bf16 v[124:127],a[176:179],v[228:231],v[124:127]
s_and_b32 s78,s78,0x1ffff
ds_read_b128 a[148:151],v1 offset:4352
v_cvt_pk_bf16_f32 v113,v114,v115
v_mfma_f32_16x16x32_bf16 v[52:55],v[28:31],v[80:83],v[52:55]
global_load_lds_dwordx4 v5,s[10:11] offset:1024
v_accvgpr_read_b32 v8,a0
v_mfma_f32_16x16x32_bf16 v[52:55],v[28:31],v[88:91],v[52:55]
s_sub_u32 s78,s78,s77
ds_read_b128 a[144:147],v1 offset:4096
v_accvgpr_read_b32 v9,a1
v_mfma_f32_16x16x32_bf16 a[32:35],a[192:195],v[80:83],a[32:35]
s_add_u32 s77,s77,s78
v_exp_f32 v116,v116
v_mfma_f32_16x16x32_bf16 a[32:35],a[196:199],v[88:91],a[32:35]
ds_read_b128 a[160:163],v1 offset:8192
v_accvgpr_read_b32 v10,a2
v_mfma_f32_16x16x32_bf16 a[36:39],a[200:203],v[80:83],a[36:39]
v_exp_f32 v117,v117
v_mfma_f32_16x16x32_bf16 a[36:39],a[204:207],v[88:91],a[36:39]
ds_read_b128 a[164:167],v1 offset:8448
v_cvt_pk_bf16_f32 v114,v116,v117
v_mfma_f32_16x16x32_bf16 a[40:43],a[208:211],v[80:83],a[40:43]
v_exp_f32 v118,v118
v_mfma_f32_16x16x32_bf16 a[40:43],a[212:215],v[88:91],a[40:43]
ds_read_b128 a[168:171],v1 offset:8704
v_accvgpr_read_b32 v11,a3
v_mfma_f32_16x16x32_bf16 a[44:47],a[216:219],v[80:83],a[44:47]
v_exp_f32 v119,v119
v_mfma_f32_16x16x32_bf16 a[44:47],a[220:223],v[88:91],a[44:47]
ds_read_b128 a[172:175],v1 offset:8960
v_cvt_pk_bf16_f32 v115,v118,v119
v_mfma_f32_16x16x32_bf16 a[48:51],a[224:227],v[80:83],a[48:51]
v_exp_f32 v120,v120
v_mfma_f32_16x16x32_bf16 a[48:51],a[228:231],v[88:91],a[48:51]
ds_read_b128 a[188:191],v1 offset:13056
v_accvgpr_read_b32 v12,a4
v_mfma_f32_16x16x32_bf16 a[52:55],a[232:235],v[80:83],a[52:55]
v_exp_f32 v121,v121
v_mfma_f32_16x16x32_bf16 a[52:55],a[236:239],v[88:91],a[52:55]
ds_read_b128 a[184:187],v1 offset:12800
v_cvt_pk_bf16_f32 v120,v120,v121
v_mfma_f32_16x16x32_bf16 a[56:59],a[240:243],v[80:83],a[56:59]
global_load_lds_dwordx4 v4,s[10:11] offset:2048
v_accvgpr_read_b32 v13,a5
v_mfma_f32_16x16x32_bf16 a[56:59],a[244:247],v[88:91],a[56:59]
ds_read_b128 a[180:183],v1 offset:12544
v_accvgpr_read_b32 v14,a6
v_mfma_f32_16x16x32_bf16 a[60:63],a[248:251],v[80:83],a[60:63]
v_exp_f32 v122,v122
v_mfma_f32_16x16x32_bf16 a[60:63],a[252:255],v[88:91],a[60:63]
s_waitcnt lgkmcnt(3)
ds_read_b128 a[176:179],v1 offset:12288
v_accvgpr_read_b32 v15,a7
v_mfma_f32_16x16x32_bf16 v[64:67],a[128:131],v[128:131],v[32:35]
v_exp_f32 v123,v123
v_mfma_f32_16x16x32_bf16 v[64:67],a[132:135],v[136:139],v[64:67]
ds_read_b128 v[36:39],v7 offset:1024
v_cvt_pk_bf16_f32 v121,v122,v123
v_mfma_f32_16x16x32_bf16 v[64:67],a[136:139],v[144:147],v[64:67]
v_exp_f32 v124,v124
v_mfma_f32_16x16x32_bf16 v[64:67],a[140:143],v[152:155],v[64:67]
v_exp_f32 v125,v125
v_mfma_f32_16x16x32_bf16 v[68:71],a[156:159],v[152:155],v[32:35]
v_exp_f32 v126,v126
v_mfma_f32_16x16x32_bf16 v[68:71],a[152:155],v[144:147],v[68:71]
v_exp_f32 v127,v127
v_mfma_f32_16x16x32_bf16 v[68:71],a[148:151],v[136:139],v[68:71]
v_cvt_pk_bf16_f32 v122,v124,v125
v_cvt_pk_bf16_f32 v123,v126,v127
v_mfma_f32_16x16x32_bf16 v[68:71],a[144:147],v[128:131],v[68:71]
ds_read_b128 v[40:43],v7 offset:2048
v_max_f32 v24,0x00800000,v48
v_mfma_f32_16x16x32_bf16 v[72:75],a[160:163],v[128:131],v[32:35]
v_exp_f32 v64,v64
v_mfma_f32_16x16x32_bf16 v[72:75],a[164:167],v[136:139],v[72:75]
v_exp_f32 v65,v65
v_mfma_f32_16x16x32_bf16 v[72:75],a[168:171],v[144:147],v[72:75]
s_waitcnt lgkmcnt(1)
v_exp_f32 v66,v66
v_mfma_f32_16x16x32_bf16 v[72:75],a[172:175],v[152:155],v[72:75]
global_load_lds_dwordx4 v5,s[10:11] offset:3072
v_cvt_pk_bf16_f32 v64,v64,v65
v_mfma_f32_16x16x32_bf16 v[76:79],a[188:191],v[152:155],v[32:35]
v_exp_f32 v67,v67
v_mfma_f32_16x16x32_bf16 v[76:79],a[184:187],v[144:147],v[76:79]
v_exp_f32 v68,v68
v_mfma_f32_16x16x32_bf16 v[76:79],a[180:183],v[136:139],v[76:79]
v_exp_f32 v69,v69
v_mfma_f32_16x16x32_bf16 v[76:79],a[176:179],v[128:131],v[76:79]
v_exp_f32 v70,v70
L_pd7_%=:
s_cbranch_execz L_trt0_%=
v_mfma_f32_16x16x32_bf16 v[56:59],v[28:31],v[96:99],v[56:59]
v_exp_f32 v71,v71
v_mfma_f32_16x16x32_bf16 v[56:59],v[28:31],v[104:107],v[56:59]
v_cvt_pk_bf16_f32 v65,v66,v67
v_cvt_pk_bf16_f32 v66,v68,v69
v_mfma_f32_16x16x32_bf16 a[64:67],a[192:195],v[96:99],a[64:67]
v_cvt_pk_bf16_f32 v67,v70,v71
v_accvgpr_read_b32 v16,a8
v_mfma_f32_16x16x32_bf16 a[64:67],a[196:199],v[104:107],a[64:67]
v_exp_f32 v72,v72
v_mfma_f32_16x16x32_bf16 a[68:71],a[200:203],v[96:99],a[68:71]
v_exp_f32 v73,v73
v_mfma_f32_16x16x32_bf16 a[68:71],a[204:207],v[104:107],a[68:71]
s_add_u32 s10,s20,s74
s_addc_u32 s11,s21,0
v_cvt_pk_bf16_f32 v72,v72,v73
v_mfma_f32_16x16x32_bf16 a[72:75],a[208:211],v[96:99],a[72:75]
v_exp_f32 v74,v74
v_mfma_f32_16x16x32_bf16 a[72:75],a[212:215],v[104:107],a[72:75]
s_add_u32 m0,s76,0
v_exp_f32 v75,v75
v_mfma_f32_16x16x32_bf16 a[76:79],a[216:219],v[96:99],a[76:79]
v_exp_f32 v76,v76
v_mfma_f32_16x16x32_bf16 a[76:79],a[220:223],v[104:107],a[76:79]
global_load_lds_dwordx4 v4,s[10:11] nt
v_cvt_pk_bf16_f32 v73,v74,v75
v_mfma_f32_16x16x32_bf16 a[80:83],a[224:227],v[96:99],a[80:83]
v_exp_f32 v77,v77
v_mfma_f32_16x16x32_bf16 a[80:83],a[228:231],v[104:107],a[80:83]
v_exp_f32 v78,v78
v_mfma_f32_16x16x32_bf16 a[84:87],a[232:235],v[96:99],a[84:87]
v_exp_f32 v79,v79
v_mfma_f32_16x16x32_bf16 a[84:87],a[236:239],v[104:107],a[84:87]
v_cvt_pk_bf16_f32 v74,v76,v77
v_cvt_pk_bf16_f32 v75,v78,v79
v_mfma_f32_16x16x32_bf16 a[88:91],a[240:243],v[96:99],a[88:91]
v_rcp_f32 v24,v24
v_mfma_f32_16x16x32_bf16 a[88:91],a[244:247],v[104:107],a[88:91]
v_accvgpr_read_b32 v17,a9
v_accvgpr_read_b32 v18,a10
v_mfma_f32_16x16x32_bf16 a[92:95],a[248:251],v[96:99],a[92:95]
v_accvgpr_read_b32 v19,a11
v_mul_f32 v8,v8,v24
v_mfma_f32_16x16x32_bf16 a[92:95],a[252:255],v[104:107],a[92:95]
v_mul_f32 v9,v9,v24
v_mul_f32 v10,v10,v24
v_mfma_f32_16x16x32_bf16 v[80:83],a[128:131],v[160:163],v[36:39]
v_mul_f32 v11,v11,v24
v_mul_f32 v12,v12,v24
v_mfma_f32_16x16x32_bf16 v[80:83],a[132:135],v[168:171],v[80:83]
v_mul_f32 v13,v13,v24
v_mul_f32 v14,v14,v24
v_mfma_f32_16x16x32_bf16 v[80:83],a[136:139],v[176:179],v[80:83]
v_mul_f32 v15,v15,v24
v_cvt_pk_bf16_f32 v8,v8,v9
v_mfma_f32_16x16x32_bf16 v[80:83],a[140:143],v[184:187],v[80:83]
v_cvt_pk_bf16_f32 v9,v10,v11
v_cvt_pk_bf16_f32 v10,v12,v13
v_mfma_f32_16x16x32_bf16 v[84:87],a[156:159],v[184:187],v[36:39]
v_cvt_pk_bf16_f32 v11,v14,v15
v_accvgpr_read_b32 v20,a12
v_mfma_f32_16x16x32_bf16 v[84:87],a[152:155],v[176:179],v[84:87]
ds_write_b128 v7,v[8:11] offset:6144
v_mfma_f32_16x16x32_bf16 v[84:87],a[148:151],v[168:171],v[84:87]
global_load_lds_dwordx4 v4,s[10:11] offset:1024 nt
v_accvgpr_read_b32 v21,a13
v_mfma_f32_16x16x32_bf16 v[84:87],a[144:147],v[160:163],v[84:87]
v_accvgpr_read_b32 v22,a14
v_accvgpr_read_b32 v23,a15
v_mfma_f32_16x16x32_bf16 v[88:91],a[160:163],v[160:163],v[36:39]
v_accvgpr_read_b32 v8,a16
v_accvgpr_read_b32 v9,a17
v_mfma_f32_16x16x32_bf16 v[88:91],a[164:167],v[168:171],v[88:91]
v_accvgpr_read_b32 v10,a18
v_accvgpr_read_b32 v11,a19
v_mfma_f32_16x16x32_bf16 v[88:91],a[168:171],v[176:179],v[88:91]
v_mul_f32 v16,v16,v24
v_mul_f32 v17,v17,v24
v_mfma_f32_16x16x32_bf16 v[88:91],a[172:175],v[184:187],v[88:91]
v_mul_f32 v18,v18,v24
v_mul_f32 v19,v19,v24
v_mfma_f32_16x16x32_bf16 v[92:95],a[188:191],v[184:187],v[36:39]
v_mul_f32 v20,v20,v24
v_mul_f32 v21,v21,v24
v_mfma_f32_16x16x32_bf16 v[92:95],a[184:187],v[176:179],v[92:95]
v_mul_f32 v22,v22,v24
v_mul_f32 v23,v23,v24
v_mfma_f32_16x16x32_bf16 v[92:95],a[180:183],v[168:171],v[92:95]
v_cvt_pk_bf16_f32 v16,v16,v17
v_cvt_pk_bf16_f32 v17,v18,v19
v_mfma_f32_16x16x32_bf16 v[92:95],a[176:179],v[160:163],v[92:95]
)A"
R"A(v_cvt_pk_bf16_f32 v18,v20,v21
v_cvt_pk_bf16_f32 v19,v22,v23
v_mfma_f32_16x16x32_bf16 v[60:63],v[28:31],v[112:115],v[60:63]
ds_write_b128 v7,v[16:19] offset:7168
v_mfma_f32_16x16x32_bf16 v[60:63],v[28:31],v[120:123],v[60:63]
v_accvgpr_read_b32 v12,a20
v_accvgpr_read_b32 v13,a21
v_mfma_f32_16x16x32_bf16 a[96:99],a[192:195],v[112:115],a[96:99]
ds_read_b64_tr_b16 a[192:193],v2
ds_read_b64_tr_b16 a[194:195],v2 offset:4096
v_mfma_f32_16x16x32_bf16 a[96:99],a[196:199],v[120:123],a[96:99]
ds_read_b64_tr_b16 a[196:197],v2 offset:8192
v_accvgpr_read_b32 v14,a22
v_mfma_f32_16x16x32_bf16 a[100:103],a[200:203],v[112:115],a[100:103]
global_load_lds_dwordx4 v5,s[10:11] offset:2048 nt
ds_read_b64_tr_b16 a[198:199],v2 offset:12288
v_mfma_f32_16x16x32_bf16 a[100:103],a[204:207],v[120:123],a[100:103]
ds_read_b64_tr_b16 a[200:201],v3
v_accvgpr_read_b32 v15,a23
v_mfma_f32_16x16x32_bf16 a[104:107],a[208:211],v[112:115],a[104:107]
ds_read_b64_tr_b16 a[202:203],v3 offset:4096
ds_read_b64_tr_b16 a[204:205],v3 offset:8192
v_mfma_f32_16x16x32_bf16 a[104:107],a[212:215],v[120:123],a[104:107]
s_cmp_le_u32 s5,2
s_cselect_b32 s5,s4,s5
ds_read_b64_tr_b16 a[206:207],v3 offset:12288
v_mfma_f32_16x16x32_bf16 a[108:111],a[216:219],v[112:115],a[108:111]
ds_read_b64_tr_b16 a[208:209],v2 offset:256
v_accvgpr_read_b32 v16,a24
v_mfma_f32_16x16x32_bf16 a[108:111],a[220:223],v[120:123],a[108:111]
ds_read_b64_tr_b16 a[210:211],v2 offset:4352
v_accvgpr_read_b32 v17,a25
v_mfma_f32_16x16x32_bf16 a[112:115],a[224:227],v[112:115],a[112:115]
ds_read_b64_tr_b16 a[212:213],v2 offset:8448
ds_read_b64_tr_b16 a[214:215],v2 offset:12544
v_mfma_f32_16x16x32_bf16 a[112:115],a[228:231],v[120:123],a[112:115]
ds_read_b64_tr_b16 a[216:217],v3 offset:256
v_accvgpr_read_b32 v18,a26
v_mfma_f32_16x16x32_bf16 a[116:119],a[232:235],v[112:115],a[116:119]
ds_read_b64_tr_b16 a[218:219],v3 offset:4352
v_accvgpr_read_b32 v19,a27
v_mfma_f32_16x16x32_bf16 a[116:119],a[236:239],v[120:123],a[116:119]
ds_read_b64_tr_b16 a[220:221],v3 offset:8448
v_accvgpr_read_b32 v20,a28
v_mfma_f32_16x16x32_bf16 a[120:123],a[240:243],v[112:115],a[120:123]
ds_read_b64_tr_b16 a[222:223],v3 offset:12544
ds_read_b64_tr_b16 a[224:225],v2 offset:512
v_mfma_f32_16x16x32_bf16 a[120:123],a[244:247],v[120:123],a[120:123]
ds_read_b64_tr_b16 a[226:227],v2 offset:4608
v_accvgpr_read_b32 v21,a29
v_mfma_f32_16x16x32_bf16 a[124:127],a[248:251],v[112:115],a[124:127]
ds_read_b64_tr_b16 a[228:229],v2 offset:8704
v_accvgpr_read_b32 v22,a30
v_mfma_f32_16x16x32_bf16 a[124:127],a[252:255],v[120:123],a[124:127]
s_waitcnt lgkmcnt(9)
ds_read_b64_tr_b16 a[230:231],v2 offset:12800
v_accvgpr_read_b32 v23,a31
v_mfma_f32_16x16x32_bf16 v[96:99],a[128:131],v[192:195],v[40:43]
ds_read_b64_tr_b16 a[232:233],v3 offset:512
ds_read_b64_tr_b16 a[234:235],v3 offset:4608
v_mfma_f32_16x16x32_bf16 v[96:99],a[132:135],v[200:203],v[96:99]
global_load_lds_dwordx4 v5,s[10:11] offset:3072
ds_read_b64_tr_b16 a[236:237],v3 offset:8704
v_mfma_f32_16x16x32_bf16 v[96:99],a[136:139],v[208:211],v[96:99]
ds_read_b64_tr_b16 a[238:239],v3 offset:12800
v_cmp_nlt_f32 s[54:55],v48,s73
v_mfma_f32_16x16x32_bf16 v[96:99],a[140:143],v[216:219],v[96:99]
ds_read_b64_tr_b16 a[240:241],v2 offset:768
v_cmp_nlt_f32 s[56:57],v52,s73
v_mfma_f32_16x16x32_bf16 v[100:103],a[156:159],v[216:219],v[40:43]
s_or_b64 s[54:55],s[54:55],s[56:57]
ds_read_b64_tr_b16 a[242:243],v2 offset:4864
ds_read_b64_tr_b16 a[244:245],v2 offset:8960
v_mfma_f32_16x16x32_bf16 v[100:103],a[152:155],v[208:211],v[100:103]
ds_read_b64_tr_b16 a[246:247],v2 offset:13056
v_cmp_nlt_f32 s[56:57],v56,s73
v_mfma_f32_16x16x32_bf16 v[100:103],a[148:151],v[200:203],v[100:103]
s_or_b64 s[54:55],s[54:55],s[56:57]
ds_read_b64_tr_b16 a[248:249],v3 offset:768
v_mul_f32 v8,v8,v24
v_mfma_f32_16x16x32_bf16 v[100:103],a[144:147],v[192:195],v[100:103]
ds_read_b64_tr_b16 a[250:251],v3 offset:4864
v_mul_f32 v9,v9,v24
v_mfma_f32_16x16x32_bf16 v[104:107],a[160:163],v[192:195],v[40:43]
ds_read_b64_tr_b16 a[252:253],v3 offset:8960
ds_read_b64_tr_b16 a[254:255],v3 offset:13056
v_mfma_f32_16x16x32_bf16 v[104:107],a[164:167],v[200:203],v[104:107]
v_mul_f32 v10,v10,v24
v_mul_f32 v11,v11,v24
v_mfma_f32_16x16x32_bf16 v[104:107],a[168:171],v[208:211],v[104:107]
v_mul_f32 v12,v12,v24
v_mul_f32 v13,v13,v24
v_mfma_f32_16x16x32_bf16 v[104:107],a[172:175],v[216:219],v[104:107]
s_add_u32 s10,s24,s74
s_addc_u32 s11,s25,0
v_mul_f32 v14,v14,v24
v_mfma_f32_16x16x32_bf16 v[108:111],a[188:191],v[216:219],v[40:43]
v_mul_f32 v15,v15,v24
v_cvt_pk_bf16_f32 v8,v8,v9
v_mfma_f32_16x16x32_bf16 v[108:111],a[184:187],v[208:211],v[108:111]
s_add_u32 m0,s76,16384
v_cvt_pk_bf16_f32 v9,v10,v11
v_cvt_pk_bf16_f32 v10,v12,v13
v_mfma_f32_16x16x32_bf16 v[108:111],a[180:183],v[200:203],v[108:111]
v_cvt_pk_bf16_f32 v11,v14,v15
v_mul_f32 v16,v16,v24
v_mfma_f32_16x16x32_bf16 v[108:111],a[176:179],v[192:195],v[108:111]
global_load_lds_dwordx4 v4,s[10:11]
v_mul_f32 v17,v17,v24
v_mfma_f32_16x16x32_bf16 a[0:3],a[192:195],v[64:67],0
v_mul_f32 v18,v18,v24
v_mul_f32 v19,v19,v24
v_mfma_f32_16x16x32_bf16 a[0:3],a[196:199],v[72:75],a[0:3]
v_mul_f32 v20,v20,v24
v_mul_f32 v21,v21,v24
v_mfma_f32_16x16x32_bf16 a[4:7],a[200:203],v[64:67],0
v_mul_f32 v22,v22,v24
v_mul_f32 v23,v23,v24
v_mfma_f32_16x16x32_bf16 a[4:7],a[204:207],v[72:75],a[4:7]
v_cvt_pk_bf16_f32 v16,v16,v17
v_cvt_pk_bf16_f32 v17,v18,v19
v_mfma_f32_16x16x32_bf16 a[8:11],a[208:211],v[64:67],0
s_waitcnt lgkmcnt(0)
v_cvt_pk_bf16_f32 v18,v20,v21
v_cvt_pk_bf16_f32 v19,v22,v23
v_mfma_f32_16x16x32_bf16 a[8:11],a[212:215],v[72:75],a[8:11]
v_exp_f32 v80,v80
v_mfma_f32_16x16x32_bf16 a[12:15],a[216:219],v[64:67],0
v_exp_f32 v81,v81
v_mfma_f32_16x16x32_bf16 a[12:15],a[220:223],v[72:75],a[12:15]
v_exp_f32 v82,v82
v_mfma_f32_16x16x32_bf16 a[16:19],a[224:227],v[64:67],0
v_exp_f32 v83,v83
v_mfma_f32_16x16x32_bf16 a[16:19],a[228:231],v[72:75],a[16:19]
v_exp_f32 v84,v84
v_mfma_f32_16x16x32_bf16 a[20:23],a[232:235],v[64:67],0
s_getreg_b32 s92,hwreg(HW_REG_IB_STS,0,4)
s_min_u32 s4,s92,s6
s_mov_b32 s6,s92
v_mfma_f32_16x16x32_bf16 a[20:23],a[236:239],v[72:75],a[20:23]
s_cmp_gt_u32 s4,s9
s_cselect_b32 s4,15,s5
v_cmp_nlt_f32 s[56:57],v60,s73
v_mfma_f32_16x16x32_bf16 a[24:27],a[240:243],v[64:67],0
s_or_b64 s[54:55],s[54:55],s[56:57]
v_exp_f32 v85,v85
v_mfma_f32_16x16x32_bf16 a[24:27],a[244:247],v[72:75],a[24:27]
v_exp_f32 v86,v86
v_mfma_f32_16x16x32_bf16 a[28:31],a[248:251],v[64:67],0
global_load_lds_dwordx4 v5,s[10:11] offset:1024
v_add_u32 v1,s78,v1
v_mfma_f32_16x16x32_bf16 a[28:31],a[252:255],v[72:75],a[28:31]
v_exp_f32 v87,v87
v_mfma_f32_16x16x32_bf16 v[48:51],v[28:31],v[64:67],0
s_getpc_b64 s[92:93]
s_cmp_lg_u32 s4,0
s_cbranch_scc1 L_pace_%=
v_exp_f32 v88,v88
v_exp_f32 v89,v89
v_mfma_f32_16x16x32_bf16 v[48:51],v[28:31],v[72:75],v[48:51]
ds_write_b128 v7,v[8:11]
v_max_f32 v25,0x00800000,v52
v_rcp_f32 v25,v25
v_max_f32 v26,0x00800000,v56
v_rcp_f32 v26,v26
v_cvt_pk_bf16_f32 v80,v80,v81
v_cvt_pk_bf16_f32 v81,v82,v83
v_cvt_pk_bf16_f32 v82,v84,v85
v_cvt_pk_bf16_f32 v83,v86,v87
v_cvt_pk_bf16_f32 v88,v88,v89
v_max_f32 v27,0x00800000,v60
v_rcp_f32 v27,v27
ds_write_b128 v7,v[16:19] offset:1024
ds_read_b128 v[44:47],v7 offset:3072
s_cmp_lg_u64 s[54:55],0
s_cbranch_scc1 L_slow0_%=
L_ok0_%=:
s_waitcnt vmcnt(14)
s_barrier
s_waitcnt lgkmcnt(0)
v_mfma_f32_16x16x32_bf16 v[112:115],a[128:131],v[224:227],v[44:47]
s_add_u32 s10,s24,s74
s_addc_u32 s11,s25,0
s_add_u32 m0,s76,16384
v_mfma_f32_16x16x32_bf16 v[112:115],a[132:135],v[232:235],v[112:115]
s_add_u32 s75,s75,16384
v_exp_f32 v90,v90
v_mfma_f32_16x16x32_bf16 v[112:115],a[136:139],v[240:243],v[112:115]
global_load_lds_dwordx4 v4,s[10:11] offset:2048
ds_read_b128 a[128:131],v1
v_mfma_f32_16x16x32_bf16 v[112:115],a[140:143],v[248:251],v[112:115]
s_add_u32 s74,s74,16384
v_exp_f32 v91,v91
v_mfma_f32_16x16x32_bf16 v[116:119],a[156:159],v[248:251],v[44:47]
s_and_b32 s74,s74,0x3ffff
v_exp_f32 v92,v92
v_mfma_f32_16x16x32_bf16 v[116:119],a[152:155],v[240:243],v[116:119]
s_and_b32 s69,s75,0xfffc0000
v_exp_f32 v93,v93
v_mfma_f32_16x16x32_bf16 v[116:119],a[148:151],v[232:235],v[116:119]
s_or_b32 s74,s74,s69
v_exp_f32 v94,v94
v_mfma_f32_16x16x32_bf16 v[116:119],a[144:147],v[224:227],v[116:119]
s_sub_u32 s76,s76,32768
v_exp_f32 v95,v95
v_mfma_f32_16x16x32_bf16 v[120:123],a[160:163],v[224:227],v[44:47]
s_and_b32 s76,s76,0x1ffff
v_cvt_pk_bf16_f32 v89,v90,v91
v_cvt_pk_bf16_f32 v90,v92,v93
v_mfma_f32_16x16x32_bf16 v[120:123],a[164:167],v[232:235],v[120:123]
s_add_u32 s75,s75,16384
ds_read_b128 a[132:135],v1 offset:256
v_cvt_pk_bf16_f32 v91,v94,v95
v_mfma_f32_16x16x32_bf16 v[120:123],a[168:171],v[240:243],v[120:123]
s_and_b32 s69,s75,0xfffc0000
v_exp_f32 v96,v96
v_mfma_f32_16x16x32_bf16 v[120:123],a[172:175],v[248:251],v[120:123]
s_add_u32 s75,s75,16384
ds_read_b128 a[136:139],v1 offset:512
v_accvgpr_read_b32 v16,a32
v_mfma_f32_16x16x32_bf16 v[124:127],a[188:191],v[248:251],v[44:47]
v_exp_f32 v97,v97
v_mfma_f32_16x16x32_bf16 v[124:127],a[184:187],v[240:243],v[124:127]
ds_read_b128 a[140:143],v1 offset:768
v_cvt_pk_bf16_f32 v96,v96,v97
v_mfma_f32_16x16x32_bf16 v[124:127],a[180:183],v[232:235],v[124:127]
v_exp_f32 v98,v98
v_mfma_f32_16x16x32_bf16 v[124:127],a[176:179],v[224:227],v[124:127]
ds_read_b128 a[156:159],v1 offset:4864
v_accvgpr_read_b32 v17,a33
v_mfma_f32_16x16x32_bf16 v[132:135],a[192:195],v[80:83],0
global_load_lds_dwordx4 v5,s[10:11] offset:3072
v_accvgpr_read_b32 v18,a34
v_mfma_f32_16x16x32_bf16 v[132:135],a[196:199],v[88:91],v[132:135]
ds_read_b128 a[152:155],v1 offset:4608
v_accvgpr_read_b32 v19,a35
v_mfma_f32_16x16x32_bf16 v[140:143],a[200:203],v[80:83],0
v_exp_f32 v99,v99
v_mfma_f32_16x16x32_bf16 v[140:143],a[204:207],v[88:91],v[140:143]
ds_read_b128 a[148:151],v1 offset:4352
v_cvt_pk_bf16_f32 v97,v98,v99
v_mfma_f32_16x16x32_bf16 v[148:151],a[208:211],v[80:83],0
v_exp_f32 v100,v100
v_mfma_f32_16x16x32_bf16 v[148:151],a[212:215],v[88:91],v[148:151]
ds_read_b128 a[144:147],v1 offset:4096
v_accvgpr_read_b32 v20,a36
v_mfma_f32_16x16x32_bf16 v[156:159],a[216:219],v[80:83],0
v_exp_f32 v101,v101
v_mfma_f32_16x16x32_bf16 v[156:159],a[220:223],v[88:91],v[156:159]
ds_read_b128 a[160:163],v1 offset:8192
v_cvt_pk_bf16_f32 v98,v100,v101
v_mfma_f32_16x16x32_bf16 v[164:167],a[224:227],v[80:83],0
v_exp_f32 v102,v102
v_mfma_f32_16x16x32_bf16 v[164:167],a[228:231],v[88:91],v[164:167]
ds_read_b128 a[164:167],v1 offset:8448
v_accvgpr_read_b32 v21,a37
v_mfma_f32_16x16x32_bf16 v[172:175],a[232:235],v[80:83],0
s_add_u32 s10,s20,s74
s_addc_u32 s11,s21,0
v_accvgpr_read_b32 v22,a38
v_mfma_f32_16x16x32_bf16 v[172:175],a[236:239],v[88:91],v[172:175]
ds_read_b128 a[168:171],v1 offset:8704
v_accvgpr_read_b32 v23,a39
v_mfma_f32_16x16x32_bf16 v[180:183],a[240:243],v[80:83],0
s_add_u32 m0,s76,0
v_exp_f32 v103,v103
v_mfma_f32_16x16x32_bf16 v[180:183],a[244:247],v[88:91],v[180:183]
ds_read_b128 a[172:175],v1 offset:8960
v_cvt_pk_bf16_f32 v99,v102,v103
v_mfma_f32_16x16x32_bf16 v[188:191],a[248:251],v[80:83],0
global_load_lds_dwordx4 v4,s[10:11] nt
v_mul_f32 v16,v16,v25
v_mfma_f32_16x16x32_bf16 v[188:191],a[252:255],v[88:91],v[188:191]
ds_read_b128 a[188:191],v1 offset:13056
v_mul_f32 v17,v17,v25
v_mfma_f32_16x16x32_bf16 v[52:55],v[28:31],v[80:83],0
v_exp_f32 v104,v104
v_mfma_f32_16x16x32_bf16 v[52:55],v[28:31],v[88:91],v[52:55]
s_waitcnt lgkmcnt(3)
ds_read_b128 a[184:187],v1 offset:12800
v_mul_f32 v18,v18,v25
v_mfma_f32_16x16x32_bf16 v[64:67],a[128:131],v[128:131],v[32:35]
v_exp_f32 v105,v105
v_mfma_f32_16x16x32_bf16 v[64:67],a[132:135],v[136:139],v[64:67]
ds_read_b128 a[180:183],v1 offset:12544
v_cvt_pk_bf16_f32 v104,v104,v105
v_mfma_f32_16x16x32_bf16 v[64:67],a[136:139],v[144:147],v[64:67]
v_exp_f32 v106,v106
v_mfma_f32_16x16x32_bf16 v[64:67],a[140:143],v[152:155],v[64:67]
ds_read_b128 a[176:179],v1 offset:12288
v_mul_f32 v19,v19,v25
v_mfma_f32_16x16x32_bf16 v[68:71],a[156:159],v[152:155],v[32:35]
v_exp_f32 v107,v107
v_mfma_f32_16x16x32_bf16 v[68:71],a[152:155],v[144:147],v[68:71]
v_exp_f32 v108,v108
v_mfma_f32_16x16x32_bf16 v[68:71],a[148:151],v[136:139],v[68:71]
v_exp_f32 v109,v109
v_mfma_f32_16x16x32_bf16 v[68:71],a[144:147],v[128:131],v[68:71]
)A"
R"A(v_exp_f32 v110,v110
v_mfma_f32_16x16x32_bf16 v[72:75],a[160:163],v[128:131],v[32:35]
v_exp_f32 v111,v111
v_mfma_f32_16x16x32_bf16 v[72:75],a[164:167],v[136:139],v[72:75]
s_waitcnt lgkmcnt(1)
v_cvt_pk_bf16_f32 v105,v106,v107
v_cvt_pk_bf16_f32 v106,v108,v109
v_mfma_f32_16x16x32_bf16 v[72:75],a[168:171],v[144:147],v[72:75]
global_load_lds_dwordx4 v4,s[10:11] offset:1024 nt
v_cvt_pk_bf16_f32 v107,v110,v111
v_mfma_f32_16x16x32_bf16 v[72:75],a[172:175],v[152:155],v[72:75]
v_exp_f32 v112,v112
v_mfma_f32_16x16x32_bf16 v[76:79],a[188:191],v[152:155],v[32:35]
v_exp_f32 v113,v113
v_mfma_f32_16x16x32_bf16 v[76:79],a[184:187],v[144:147],v[76:79]
v_exp_f32 v114,v114
v_mfma_f32_16x16x32_bf16 v[76:79],a[180:183],v[136:139],v[76:79]
s_waitcnt lgkmcnt(0)
v_exp_f32 v115,v115
v_mfma_f32_16x16x32_bf16 v[76:79],a[176:179],v[128:131],v[76:79]
v_exp_f32 v116,v116
v_mfma_f32_16x16x32_bf16 v[196:199],a[192:195],v[96:99],0
v_exp_f32 v117,v117
v_mfma_f32_16x16x32_bf16 v[196:199],a[196:199],v[104:107],v[196:199]
v_exp_f32 v118,v118
v_mfma_f32_16x16x32_bf16 v[204:207],a[200:203],v[96:99],0
v_exp_f32 v119,v119
v_mfma_f32_16x16x32_bf16 v[204:207],a[204:207],v[104:107],v[204:207]
v_cvt_pk_bf16_f32 v112,v112,v113
v_cvt_pk_bf16_f32 v113,v114,v115
v_mfma_f32_16x16x32_bf16 v[212:215],a[208:211],v[96:99],0
v_cvt_pk_bf16_f32 v114,v116,v117
v_cvt_pk_bf16_f32 v115,v118,v119
v_mfma_f32_16x16x32_bf16 v[212:215],a[212:215],v[104:107],v[212:215]
v_exp_f32 v120,v120
v_mfma_f32_16x16x32_bf16 v[220:223],a[216:219],v[96:99],0
v_exp_f32 v121,v121
v_mfma_f32_16x16x32_bf16 v[220:223],a[220:223],v[104:107],v[220:223]
v_exp_f32 v122,v122
v_mfma_f32_16x16x32_bf16 v[228:231],a[224:227],v[96:99],0
global_load_lds_dwordx4 v5,s[10:11] offset:2048 nt
v_cvt_pk_bf16_f32 v120,v120,v121
v_mfma_f32_16x16x32_bf16 v[228:231],a[228:231],v[104:107],v[228:231]
v_exp_f32 v123,v123
v_mfma_f32_16x16x32_bf16 v[236:239],a[232:235],v[96:99],0
v_exp_f32 v124,v124
v_mfma_f32_16x16x32_bf16 v[236:239],a[236:239],v[104:107],v[236:239]
v_exp_f32 v125,v125
v_mfma_f32_16x16x32_bf16 v[244:247],a[240:243],v[96:99],0
v_exp_f32 v126,v126
v_mfma_f32_16x16x32_bf16 v[244:247],a[244:247],v[104:107],v[244:247]
v_exp_f32 v127,v127
v_mfma_f32_16x16x32_bf16 v[252:255],a[248:251],v[96:99],0
v_cvt_pk_bf16_f32 v121,v122,v123
v_cvt_pk_bf16_f32 v122,v124,v125
v_mfma_f32_16x16x32_bf16 v[252:255],a[252:255],v[104:107],v[252:255]
v_cvt_pk_bf16_f32 v123,v126,v127
v_mul_f32 v20,v20,v25
v_mfma_f32_16x16x32_bf16 v[56:59],v[28:31],v[96:99],0
v_mul_f32 v21,v21,v25
v_mul_f32 v22,v22,v25
v_mfma_f32_16x16x32_bf16 v[56:59],v[28:31],v[104:107],v[56:59]
v_mul_f32 v23,v23,v25
v_cvt_pk_bf16_f32 v16,v16,v17
v_mfma_f32_16x16x32_bf16 v[80:83],a[128:131],v[160:163],v[36:39]
v_cvt_pk_bf16_f32 v17,v18,v19
v_cvt_pk_bf16_f32 v18,v20,v21
v_mfma_f32_16x16x32_bf16 v[80:83],a[132:135],v[168:171],v[80:83]
v_cvt_pk_bf16_f32 v19,v22,v23
v_accvgpr_read_b32 v20,a44
v_mfma_f32_16x16x32_bf16 v[80:83],a[136:139],v[176:179],v[80:83]
ds_write_b128 v7,v[16:19] offset:2048
v_mfma_f32_16x16x32_bf16 v[80:83],a[140:143],v[184:187],v[80:83]
v_accvgpr_read_b32 v21,a45
v_accvgpr_read_b32 v22,a46
v_mfma_f32_16x16x32_bf16 v[84:87],a[156:159],v[184:187],v[36:39]
v_accvgpr_read_b32 v16,a40
v_accvgpr_read_b32 v17,a41
v_mfma_f32_16x16x32_bf16 v[84:87],a[152:155],v[176:179],v[84:87]
global_load_lds_dwordx4 v5,s[10:11] offset:3072
v_accvgpr_read_b32 v18,a42
v_mfma_f32_16x16x32_bf16 v[84:87],a[148:151],v[168:171],v[84:87]
v_accvgpr_read_b32 v19,a43
v_accvgpr_read_b32 v23,a47
v_mfma_f32_16x16x32_bf16 v[84:87],a[144:147],v[160:163],v[84:87]
v_accvgpr_read_b32 v8,a48
v_accvgpr_read_b32 v9,a49
v_mfma_f32_16x16x32_bf16 v[88:91],a[160:163],v[160:163],v[36:39]
v_accvgpr_read_b32 v10,a50
v_accvgpr_read_b32 v11,a51
v_mfma_f32_16x16x32_bf16 v[88:91],a[164:167],v[168:171],v[88:91]
v_mul_f32 v16,v16,v25
v_mul_f32 v17,v17,v25
v_mfma_f32_16x16x32_bf16 v[88:91],a[168:171],v[176:179],v[88:91]
v_mul_f32 v18,v18,v25
v_mul_f32 v19,v19,v25
v_mfma_f32_16x16x32_bf16 v[88:91],a[172:175],v[184:187],v[88:91]
v_mul_f32 v20,v20,v25
v_mul_f32 v21,v21,v25
v_mfma_f32_16x16x32_bf16 v[92:95],a[188:191],v[184:187],v[36:39]
v_mul_f32 v22,v22,v25
v_mul_f32 v23,v23,v25
v_mfma_f32_16x16x32_bf16 v[92:95],a[184:187],v[176:179],v[92:95]
v_cvt_pk_bf16_f32 v16,v16,v17
v_cvt_pk_bf16_f32 v17,v18,v19
v_mfma_f32_16x16x32_bf16 v[92:95],a[180:183],v[168:171],v[92:95]
v_cvt_pk_bf16_f32 v18,v20,v21
v_cvt_pk_bf16_f32 v19,v22,v23
v_mfma_f32_16x16x32_bf16 v[92:95],a[176:179],v[160:163],v[92:95]
s_add_u32 s10,s24,s74
s_addc_u32 s11,s25,0
v_accvgpr_read_b32 v12,a52
v_mfma_f32_16x16x32_bf16 a[32:35],a[192:195],v[112:115],0
ds_write_b128 v7,v[16:19] offset:3072
v_mfma_f32_16x16x32_bf16 a[32:35],a[196:199],v[120:123],a[32:35]
s_add_u32 m0,s76,16384
v_accvgpr_read_b32 v13,a53
v_accvgpr_read_b32 v14,a54
v_mfma_f32_16x16x32_bf16 a[36:39],a[200:203],v[112:115],0
v_accvgpr_read_b32 v15,a55
v_accvgpr_read_b32 v16,a56
v_mfma_f32_16x16x32_bf16 a[36:39],a[204:207],v[120:123],a[36:39]
global_load_lds_dwordx4 v4,s[10:11]
v_accvgpr_read_b32 v17,a57
v_mfma_f32_16x16x32_bf16 a[40:43],a[208:211],v[112:115],0
v_accvgpr_read_b32 v18,a58
v_accvgpr_read_b32 v19,a59
v_mfma_f32_16x16x32_bf16 a[40:43],a[212:215],v[120:123],a[40:43]
v_accvgpr_read_b32 v20,a60
v_accvgpr_read_b32 v21,a61
v_mfma_f32_16x16x32_bf16 a[44:47],a[216:219],v[112:115],0
v_accvgpr_read_b32 v22,a62
v_accvgpr_read_b32 v23,a63
v_mfma_f32_16x16x32_bf16 a[44:47],a[220:223],v[120:123],a[44:47]
v_add_u32 v2,s78,v2
v_add_u32 v3,s78,v3
v_mfma_f32_16x16x32_bf16 a[48:51],a[224:227],v[112:115],0
s_sub_u32 s78,s77,32768
ds_read_b64_tr_b16 a[192:193],v2
ds_read_b64_tr_b16 a[194:195],v2 offset:4096
v_mfma_f32_16x16x32_bf16 a[48:51],a[228:231],v[120:123],a[48:51]
s_and_b32 s78,s78,0x1ffff
ds_read_b64_tr_b16 a[196:197],v2 offset:8192
v_mul_f32 v8,v8,v25
v_mfma_f32_16x16x32_bf16 a[52:55],a[232:235],v[112:115],0
s_sub_u32 s78,s78,s77
ds_read_b64_tr_b16 a[198:199],v2 offset:12288
v_mul_f32 v9,v9,v25
v_mfma_f32_16x16x32_bf16 a[52:55],a[236:239],v[120:123],a[52:55]
s_add_u32 s77,s77,s78
ds_read_b64_tr_b16 a[200:201],v3
v_mul_f32 v10,v10,v25
v_mfma_f32_16x16x32_bf16 a[56:59],a[240:243],v[112:115],0
ds_read_b64_tr_b16 a[202:203],v3 offset:4096
ds_read_b64_tr_b16 a[204:205],v3 offset:8192
v_mfma_f32_16x16x32_bf16 a[56:59],a[244:247],v[120:123],a[56:59]
ds_read_b64_tr_b16 a[206:207],v3 offset:12288
v_mul_f32 v11,v11,v25
v_mfma_f32_16x16x32_bf16 a[60:63],a[248:251],v[112:115],0
ds_read_b64_tr_b16 a[208:209],v2 offset:256
v_mul_f32 v12,v12,v25
v_mfma_f32_16x16x32_bf16 a[60:63],a[252:255],v[120:123],a[60:63]
ds_read_b64_tr_b16 a[210:211],v2 offset:4352
v_mul_f32 v13,v13,v25
v_mfma_f32_16x16x32_bf16 v[60:63],v[28:31],v[112:115],0
ds_read_b64_tr_b16 a[212:213],v2 offset:8448
ds_read_b64_tr_b16 a[214:215],v2 offset:12544
v_mfma_f32_16x16x32_bf16 v[60:63],v[28:31],v[120:123],v[60:63]
global_load_lds_dwordx4 v5,s[10:11] offset:1024
ds_read_b64_tr_b16 a[216:217],v3 offset:256
v_mfma_f32_16x16x32_bf16 v[96:99],a[128:131],v[192:195],v[40:43]
v_exp_f32 v64,v64
v_mfma_f32_16x16x32_bf16 v[96:99],a[132:135],v[200:203],v[96:99]
v_exp_f32 v65,v65
v_mfma_f32_16x16x32_bf16 v[96:99],a[136:139],v[208:211],v[96:99]
v_exp_f32 v66,v66
v_mfma_f32_16x16x32_bf16 v[96:99],a[140:143],v[216:219],v[96:99]
v_exp_f32 v67,v67
v_mfma_f32_16x16x32_bf16 v[100:103],a[156:159],v[216:219],v[40:43]
v_exp_f32 v68,v68
v_mfma_f32_16x16x32_bf16 v[100:103],a[152:155],v[208:211],v[100:103]
v_exp_f32 v69,v69
v_mfma_f32_16x16x32_bf16 v[100:103],a[148:151],v[200:203],v[100:103]
v_exp_f32 v70,v70
v_mfma_f32_16x16x32_bf16 v[100:103],a[144:147],v[192:195],v[100:103]
v_exp_f32 v71,v71
v_mfma_f32_16x16x32_bf16 v[104:107],a[160:163],v[192:195],v[40:43]
ds_read_b64_tr_b16 a[218:219],v3 offset:4352
v_cvt_pk_bf16_f32 v64,v64,v65
v_mfma_f32_16x16x32_bf16 v[104:107],a[164:167],v[200:203],v[104:107]
v_cvt_pk_bf16_f32 v65,v66,v67
v_cvt_pk_bf16_f32 v66,v68,v69
v_mfma_f32_16x16x32_bf16 v[104:107],a[168:171],v[208:211],v[104:107]
ds_read_b64_tr_b16 a[220:221],v3 offset:8448
v_cvt_pk_bf16_f32 v67,v70,v71
v_mfma_f32_16x16x32_bf16 v[104:107],a[172:175],v[216:219],v[104:107]
v_exp_f32 v72,v72
v_mfma_f32_16x16x32_bf16 v[108:111],a[188:191],v[216:219],v[40:43]
v_exp_f32 v73,v73
v_mfma_f32_16x16x32_bf16 v[108:111],a[184:187],v[208:211],v[108:111]
global_load_lds_dwordx4 v4,s[10:11] offset:2048
v_exp_f32 v74,v74
v_exp_f32 v75,v75
v_exp_f32 v76,v76
v_exp_f32 v77,v77
v_exp_f32 v78,v78
v_exp_f32 v79,v79
v_mfma_f32_16x16x32_bf16 v[108:111],a[180:183],v[200:203],v[108:111]
ds_read_b64_tr_b16 a[222:223],v3 offset:12544
s_nop 0
L_pd8_%=:
s_cbranch_execz L_trt0_%=
v_cvt_pk_bf16_f32 v72,v72,v73
v_cvt_pk_bf16_f32 v73,v74,v75
v_cvt_pk_bf16_f32 v74,v76,v77
v_cvt_pk_bf16_f32 v75,v78,v79
v_mfma_f32_16x16x32_bf16 v[108:111],a[176:179],v[192:195],v[108:111]
s_waitcnt lgkmcnt(3)
ds_read_b64_tr_b16 a[224:225],v2 offset:512
ds_read_b64_tr_b16 a[226:227],v2 offset:4608
v_mfma_f32_16x16x32_bf16 a[0:3],a[192:195],v[64:67],a[0:3]
ds_read_b64_tr_b16 a[228:229],v2 offset:8704
v_mul_f32 v14,v14,v25
v_mfma_f32_16x16x32_bf16 a[0:3],a[196:199],v[72:75],a[0:3]
ds_read_b64_tr_b16 a[230:231],v2 offset:12800
v_mul_f32 v15,v15,v25
v_mfma_f32_16x16x32_bf16 a[4:7],a[200:203],v[64:67],a[4:7]
ds_read_b64_tr_b16 a[232:233],v3 offset:512
v_cvt_pk_bf16_f32 v8,v8,v9
v_mfma_f32_16x16x32_bf16 a[4:7],a[204:207],v[72:75],a[4:7]
ds_read_b64_tr_b16 a[234:235],v3 offset:4608
ds_read_b64_tr_b16 a[236:237],v3 offset:8704
v_mfma_f32_16x16x32_bf16 a[8:11],a[208:211],v[64:67],a[8:11]
ds_read_b64_tr_b16 a[238:239],v3 offset:12800
v_cvt_pk_bf16_f32 v9,v10,v11
v_mfma_f32_16x16x32_bf16 a[8:11],a[212:215],v[72:75],a[8:11]
s_waitcnt lgkmcnt(9)
ds_read_b64_tr_b16 a[240:241],v2 offset:768
v_cvt_pk_bf16_f32 v10,v12,v13
v_mfma_f32_16x16x32_bf16 a[12:15],a[216:219],v[64:67],a[12:15]
s_waitcnt lgkmcnt(9)
ds_read_b64_tr_b16 a[242:243],v2 offset:4864
v_cvt_pk_bf16_f32 v11,v14,v15
v_mfma_f32_16x16x32_bf16 a[12:15],a[220:223],v[72:75],a[12:15]
s_waitcnt lgkmcnt(8)
ds_read_b64_tr_b16 a[244:245],v2 offset:8960
ds_read_b64_tr_b16 a[246:247],v2 offset:13056
v_mfma_f32_16x16x32_bf16 a[16:19],a[224:227],v[64:67],a[16:19]
s_waitcnt lgkmcnt(8)
ds_read_b64_tr_b16 a[248:249],v3 offset:768
v_mul_f32 v16,v16,v25
v_mfma_f32_16x16x32_bf16 a[16:19],a[228:231],v[72:75],a[16:19]
s_waitcnt lgkmcnt(1)
ds_read_b64_tr_b16 a[250:251],v3 offset:4864
v_mul_f32 v17,v17,v25
v_mfma_f32_16x16x32_bf16 a[20:23],a[232:235],v[64:67],a[20:23]
s_getreg_b32 s92,hwreg(HW_REG_IB_STS,0,4)
s_min_u32 s4,s92,s6
ds_read_b64_tr_b16 a[252:253],v3 offset:8960
v_mfma_f32_16x16x32_bf16 a[20:23],a[236:239],v[72:75],a[20:23]
s_mov_b32 s6,s92
ds_read_b64_tr_b16 a[254:255],v3 offset:13056
v_mul_f32 v18,v18,v25
v_mfma_f32_16x16x32_bf16 a[24:27],a[240:243],v[64:67],a[24:27]
s_cmp_gt_u32 s4,s9
s_cselect_b32 s4,15,s5
v_mul_f32 v19,v19,v25
v_mfma_f32_16x16x32_bf16 a[24:27],a[244:247],v[72:75],a[24:27]
s_waitcnt lgkmcnt(2)
v_mul_f32 v20,v20,v25
v_mul_f32 v21,v21,v25
v_mfma_f32_16x16x32_bf16 a[28:31],a[248:251],v[64:67],a[28:31]
s_waitcnt lgkmcnt(0)
v_mul_f32 v22,v22,v25
v_mul_f32 v23,v23,v25
v_mfma_f32_16x16x32_bf16 a[28:31],a[252:255],v[72:75],a[28:31]
s_getpc_b64 s[92:93]
s_cmp_lg_u32 s4,0
s_cbranch_scc1 L_pace_%=
v_mfma_f32_16x16x32_bf16 v[48:51],v[28:31],v[64:67],v[48:51]
v_cvt_pk_bf16_f32 v12,v16,v17
v_cvt_pk_bf16_f32 v13,v18,v19
v_mfma_f32_16x16x32_bf16 v[48:51],v[28:31],v[72:75],v[48:51]
s_add_u32 s10,s24,s74
s_addc_u32 s11,s25,0
v_cvt_pk_bf16_f32 v14,v20,v21
s_waitcnt vmcnt(15)
s_barrier
v_mfma_f32_16x16x32_bf16 v[112:115],a[128:131],v[224:227],v[44:47]
s_add_u32 m0,s76,16384
v_cvt_pk_bf16_f32 v15,v22,v23
v_accvgpr_read_b32 v16,a112
v_mfma_f32_16x16x32_bf16 v[112:115],a[132:135],v[232:235],v[112:115]
s_add_u32 s74,s74,16384
v_accvgpr_read_b32 v17,a113
v_accvgpr_read_b32 v18,a114
v_mfma_f32_16x16x32_bf16 v[112:115],a[136:139],v[240:243],v[112:115]
global_load_lds_dwordx4 v5,s[10:11] offset:3072
v_accvgpr_read_b32 v19,a115
v_mfma_f32_16x16x32_bf16 v[112:115],a[140:143],v[248:251],v[112:115]
s_and_b32 s74,s74,0x3ffff
v_accvgpr_read_b32 v20,a116
v_accvgpr_read_b32 v21,a117
)A"
R"A(v_mfma_f32_16x16x32_bf16 v[116:119],a[156:159],v[248:251],v[44:47]
s_or_b32 s74,s74,s69
v_accvgpr_read_b32 v22,a118
v_accvgpr_read_b32 v23,a119
v_mfma_f32_16x16x32_bf16 v[116:119],a[152:155],v[240:243],v[116:119]
s_sub_u32 s76,s76,32768
v_mul_f32 v16,v16,v27
v_mul_f32 v17,v17,v27
v_mfma_f32_16x16x32_bf16 v[116:119],a[148:151],v[232:235],v[116:119]
s_and_b32 s76,s76,0x1ffff
v_mul_f32 v18,v18,v27
v_mul_f32 v19,v19,v27
v_mfma_f32_16x16x32_bf16 v[116:119],a[144:147],v[224:227],v[116:119]
s_and_b32 s69,s75,0xfffc0000
v_mul_f32 v20,v20,v27
v_mul_f32 v21,v21,v27
v_mfma_f32_16x16x32_bf16 v[120:123],a[160:163],v[224:227],v[44:47]
s_add_u32 s75,s75,16384
v_mul_f32 v22,v22,v27
v_mul_f32 v23,v23,v27
v_mfma_f32_16x16x32_bf16 v[120:123],a[164:167],v[232:235],v[120:123]
v_cvt_pk_bf16_f32 v16,v16,v17
v_cvt_pk_bf16_f32 v17,v18,v19
v_mfma_f32_16x16x32_bf16 v[120:123],a[168:171],v[240:243],v[120:123]
v_cvt_pk_bf16_f32 v18,v20,v21
v_cvt_pk_bf16_f32 v19,v22,v23
v_mfma_f32_16x16x32_bf16 v[120:123],a[172:175],v[248:251],v[120:123]
v_accvgpr_write_b32 a112,v16
v_accvgpr_write_b32 a113,v17
v_mfma_f32_16x16x32_bf16 v[124:127],a[188:191],v[248:251],v[44:47]
v_accvgpr_write_b32 a114,v18
v_accvgpr_write_b32 a115,v19
v_accvgpr_write_b32 a116,v8
v_accvgpr_write_b32 a117,v9
v_accvgpr_write_b32 a118,v10
v_accvgpr_write_b32 a119,v11
v_exp_f32 v80,v80
v_exp_f32 v81,v81
v_exp_f32 v82,v82
v_exp_f32 v83,v83
v_exp_f32 v84,v84
v_exp_f32 v85,v85
v_exp_f32 v86,v86
v_exp_f32 v87,v87
v_mfma_f32_16x16x32_bf16 v[124:127],a[184:187],v[240:243],v[124:127]
v_cvt_pk_bf16_f32 v80,v80,v81
v_cvt_pk_bf16_f32 v81,v82,v83
v_cvt_pk_bf16_f32 v82,v84,v85
v_cvt_pk_bf16_f32 v83,v86,v87
v_exp_f32 v88,v88
v_exp_f32 v89,v89
v_exp_f32 v90,v90
v_exp_f32 v91,v91
v_exp_f32 v92,v92
v_exp_f32 v93,v93
v_exp_f32 v94,v94
v_exp_f32 v95,v95
v_mfma_f32_16x16x32_bf16 v[124:127],a[180:183],v[232:235],v[124:127]
v_cvt_pk_bf16_f32 v88,v88,v89
v_cvt_pk_bf16_f32 v89,v90,v91
v_cvt_pk_bf16_f32 v90,v92,v93
v_cvt_pk_bf16_f32 v91,v94,v95
v_mfma_f32_16x16x32_bf16 v[124:127],a[176:179],v[224:227],v[124:127]
s_add_u32 s10,s20,s74
s_addc_u32 s11,s21,0
s_add_u32 m0,s76,0
v_mfma_f32_16x16x32_bf16 v[132:135],a[192:195],v[80:83],v[132:135]
global_load_lds_dwordx4 v4,s[10:11] nt
v_add_u32 v1,s78,v1
v_mfma_f32_16x16x32_bf16 v[132:135],a[196:199],v[88:91],v[132:135]
ds_read_b128 a[128:131],v1
v_accvgpr_read_b32 v16,a120
v_mfma_f32_16x16x32_bf16 v[140:143],a[200:203],v[80:83],v[140:143]
v_exp_f32 v96,v96
v_mfma_f32_16x16x32_bf16 v[140:143],a[204:207],v[88:91],v[140:143]
ds_read_b128 a[132:135],v1 offset:256
v_accvgpr_read_b32 v17,a121
v_mfma_f32_16x16x32_bf16 v[148:151],a[208:211],v[80:83],v[148:151]
v_exp_f32 v97,v97
v_mfma_f32_16x16x32_bf16 v[148:151],a[212:215],v[88:91],v[148:151]
ds_read_b128 a[136:139],v1 offset:512
v_cvt_pk_bf16_f32 v96,v96,v97
v_mfma_f32_16x16x32_bf16 v[156:159],a[216:219],v[80:83],v[156:159]
v_exp_f32 v98,v98
v_mfma_f32_16x16x32_bf16 v[156:159],a[220:223],v[88:91],v[156:159]
ds_read_b128 a[140:143],v1 offset:768
v_accvgpr_read_b32 v18,a122
v_mfma_f32_16x16x32_bf16 v[164:167],a[224:227],v[80:83],v[164:167]
v_exp_f32 v99,v99
v_mfma_f32_16x16x32_bf16 v[164:167],a[228:231],v[88:91],v[164:167]
ds_read_b128 a[156:159],v1 offset:4864
v_cvt_pk_bf16_f32 v97,v98,v99
v_mfma_f32_16x16x32_bf16 v[172:175],a[232:235],v[80:83],v[172:175]
v_exp_f32 v100,v100
v_mfma_f32_16x16x32_bf16 v[172:175],a[236:239],v[88:91],v[172:175]
ds_read_b128 a[152:155],v1 offset:4608
v_accvgpr_read_b32 v19,a123
v_mfma_f32_16x16x32_bf16 v[180:183],a[240:243],v[80:83],v[180:183]
v_exp_f32 v101,v101
v_mfma_f32_16x16x32_bf16 v[180:183],a[244:247],v[88:91],v[180:183]
ds_read_b128 a[148:151],v1 offset:4352
v_cvt_pk_bf16_f32 v98,v100,v101
v_mfma_f32_16x16x32_bf16 v[188:191],a[248:251],v[80:83],v[188:191]
global_load_lds_dwordx4 v4,s[10:11] offset:1024 nt
v_accvgpr_read_b32 v20,a124
v_mfma_f32_16x16x32_bf16 v[188:191],a[252:255],v[88:91],v[188:191]
ds_read_b128 a[144:147],v1 offset:4096
v_accvgpr_read_b32 v21,a125
v_mfma_f32_16x16x32_bf16 v[52:55],v[28:31],v[80:83],v[52:55]
v_exp_f32 v102,v102
v_mfma_f32_16x16x32_bf16 v[52:55],v[28:31],v[88:91],v[52:55]
s_waitcnt lgkmcnt(3)
ds_read_b128 a[160:163],v1 offset:8192
v_accvgpr_read_b32 v22,a126
v_mfma_f32_16x16x32_bf16 v[64:67],a[128:131],v[128:131],v[32:35]
v_exp_f32 v103,v103
v_mfma_f32_16x16x32_bf16 v[64:67],a[132:135],v[136:139],v[64:67]
ds_read_b128 a[164:167],v1 offset:8448
v_cvt_pk_bf16_f32 v99,v102,v103
v_mfma_f32_16x16x32_bf16 v[64:67],a[136:139],v[144:147],v[64:67]
v_exp_f32 v104,v104
v_mfma_f32_16x16x32_bf16 v[64:67],a[140:143],v[152:155],v[64:67]
ds_read_b128 a[168:171],v1 offset:8704
v_accvgpr_read_b32 v23,a127
v_mfma_f32_16x16x32_bf16 v[68:71],a[156:159],v[152:155],v[32:35]
s_waitcnt lgkmcnt(4)
v_exp_f32 v105,v105
v_mfma_f32_16x16x32_bf16 v[68:71],a[152:155],v[144:147],v[68:71]
ds_read_b128 a[172:175],v1 offset:8960
v_cvt_pk_bf16_f32 v104,v104,v105
v_mfma_f32_16x16x32_bf16 v[68:71],a[148:151],v[136:139],v[68:71]
s_waitcnt lgkmcnt(4)
v_exp_f32 v106,v106
v_mfma_f32_16x16x32_bf16 v[68:71],a[144:147],v[128:131],v[68:71]
s_waitcnt lgkmcnt(3)
ds_read_b128 a[188:191],v1 offset:13056
v_mul_f32 v16,v16,v27
v_mfma_f32_16x16x32_bf16 v[72:75],a[160:163],v[128:131],v[32:35]
s_waitcnt lgkmcnt(3)
v_exp_f32 v107,v107
v_mfma_f32_16x16x32_bf16 v[72:75],a[164:167],v[136:139],v[72:75]
s_waitcnt lgkmcnt(1)
ds_read_b128 a[184:187],v1 offset:12800
v_cvt_pk_bf16_f32 v105,v106,v107
v_mfma_f32_16x16x32_bf16 v[72:75],a[168:171],v[144:147],v[72:75]
global_load_lds_dwordx4 v5,s[10:11] offset:2048 nt
ds_read_b128 a[180:183],v1 offset:12544
v_mfma_f32_16x16x32_bf16 v[72:75],a[172:175],v[152:155],v[72:75]
s_waitcnt lgkmcnt(2)
ds_read_b128 a[176:179],v1 offset:12288
v_mul_f32 v17,v17,v27
v_mfma_f32_16x16x32_bf16 v[76:79],a[188:191],v[152:155],v[32:35]
s_waitcnt lgkmcnt(2)
v_exp_f32 v108,v108
v_mfma_f32_16x16x32_bf16 v[76:79],a[184:187],v[144:147],v[76:79]
s_waitcnt lgkmcnt(1)
v_exp_f32 v109,v109
v_exp_f32 v110,v110
v_exp_f32 v111,v111
v_mfma_f32_16x16x32_bf16 v[76:79],a[180:183],v[136:139],v[76:79]
s_waitcnt lgkmcnt(0)
v_cvt_pk_bf16_f32 v106,v108,v109
v_cvt_pk_bf16_f32 v107,v110,v111
v_mfma_f32_16x16x32_bf16 v[76:79],a[176:179],v[128:131],v[76:79]
v_mul_f32 v18,v18,v27
v_mul_f32 v19,v19,v27
v_mfma_f32_16x16x32_bf16 v[196:199],a[192:195],v[96:99],v[196:199]
v_mul_f32 v20,v20,v27
v_mul_f32 v21,v21,v27
v_mfma_f32_16x16x32_bf16 v[196:199],a[196:199],v[104:107],v[196:199]
v_mul_f32 v22,v22,v27
v_mul_f32 v23,v23,v27
v_mfma_f32_16x16x32_bf16 v[204:207],a[200:203],v[96:99],v[204:207]
v_cvt_pk_bf16_f32 v16,v16,v17
v_cvt_pk_bf16_f32 v17,v18,v19
v_mfma_f32_16x16x32_bf16 v[204:207],a[204:207],v[104:107],v[204:207]
v_cvt_pk_bf16_f32 v18,v20,v21
v_cvt_pk_bf16_f32 v19,v22,v23
v_mfma_f32_16x16x32_bf16 v[212:215],a[208:211],v[96:99],v[212:215]
v_accvgpr_write_b32 a120,v16
v_accvgpr_write_b32 a121,v17
v_mfma_f32_16x16x32_bf16 v[212:215],a[212:215],v[104:107],v[212:215]
v_accvgpr_write_b32 a122,v18
v_accvgpr_write_b32 a123,v19
v_mfma_f32_16x16x32_bf16 v[220:223],a[216:219],v[96:99],v[220:223]
v_accvgpr_write_b32 a124,v12
v_accvgpr_write_b32 a125,v13
v_mfma_f32_16x16x32_bf16 v[220:223],a[220:223],v[104:107],v[220:223]
v_accvgpr_write_b32 a126,v14
v_accvgpr_write_b32 a127,v15
v_mfma_f32_16x16x32_bf16 v[228:231],a[224:227],v[96:99],v[228:231]
global_load_lds_dwordx4 v5,s[10:11] offset:3072
v_add_u32 v2,s78,v2
v_mfma_f32_16x16x32_bf16 v[228:231],a[228:231],v[104:107],v[228:231]
v_exp_f32 v112,v112
v_mfma_f32_16x16x32_bf16 v[236:239],a[232:235],v[96:99],v[236:239]
v_exp_f32 v113,v113
v_mfma_f32_16x16x32_bf16 v[236:239],a[236:239],v[104:107],v[236:239]
v_exp_f32 v114,v114
v_mfma_f32_16x16x32_bf16 v[244:247],a[240:243],v[96:99],v[244:247]
v_exp_f32 v115,v115
v_mfma_f32_16x16x32_bf16 v[244:247],a[244:247],v[104:107],v[244:247]
v_exp_f32 v116,v116
v_mfma_f32_16x16x32_bf16 v[252:255],a[248:251],v[96:99],v[252:255]
v_exp_f32 v117,v117
v_mfma_f32_16x16x32_bf16 v[252:255],a[252:255],v[104:107],v[252:255]
v_exp_f32 v118,v118
v_mfma_f32_16x16x32_bf16 v[56:59],v[28:31],v[96:99],v[56:59]
v_exp_f32 v119,v119
v_mfma_f32_16x16x32_bf16 v[56:59],v[28:31],v[104:107],v[56:59]
v_cvt_pk_bf16_f32 v112,v112,v113
v_cvt_pk_bf16_f32 v113,v114,v115
v_mfma_f32_16x16x32_bf16 v[80:83],a[128:131],v[160:163],v[36:39]
v_cvt_pk_bf16_f32 v114,v116,v117
v_cvt_pk_bf16_f32 v115,v118,v119
v_mfma_f32_16x16x32_bf16 v[80:83],a[132:135],v[168:171],v[80:83]
s_add_u32 s10,s24,s74
s_addc_u32 s11,s25,0
v_add_u32 v3,s78,v3
v_mfma_f32_16x16x32_bf16 v[80:83],a[136:139],v[176:179],v[80:83]
s_add_u32 s74,s74,16384
v_exp_f32 v120,v120
v_mfma_f32_16x16x32_bf16 v[80:83],a[140:143],v[184:187],v[80:83]
s_add_u32 m0,s76,16384
v_exp_f32 v121,v121
v_mfma_f32_16x16x32_bf16 v[84:87],a[156:159],v[184:187],v[36:39]
s_and_b32 s74,s74,0x3ffff
v_exp_f32 v122,v122
v_mfma_f32_16x16x32_bf16 v[84:87],a[152:155],v[176:179],v[84:87]
global_load_lds_dwordx4 v4,s[10:11]
v_cvt_pk_bf16_f32 v120,v120,v121
v_mfma_f32_16x16x32_bf16 v[84:87],a[148:151],v[168:171],v[84:87]
s_or_b32 s74,s74,s69
v_exp_f32 v123,v123
v_mfma_f32_16x16x32_bf16 v[84:87],a[144:147],v[160:163],v[84:87]
s_sub_u32 s76,s76,32768
v_exp_f32 v124,v124
v_mfma_f32_16x16x32_bf16 v[88:91],a[160:163],v[160:163],v[36:39]
s_and_b32 s76,s76,0x1ffff
v_exp_f32 v125,v125
v_mfma_f32_16x16x32_bf16 v[88:91],a[164:167],v[168:171],v[88:91]
s_sub_u32 s78,s77,32768
v_exp_f32 v126,v126
v_mfma_f32_16x16x32_bf16 v[88:91],a[168:171],v[176:179],v[88:91]
s_and_b32 s78,s78,0x1ffff
v_exp_f32 v127,v127
v_mfma_f32_16x16x32_bf16 v[88:91],a[172:175],v[184:187],v[88:91]
s_sub_u32 s78,s78,s77
v_cvt_pk_bf16_f32 v121,v122,v123
v_cvt_pk_bf16_f32 v122,v124,v125
v_mfma_f32_16x16x32_bf16 v[92:95],a[188:191],v[184:187],v[36:39]
s_and_b32 s69,s75,0xfffc0000
v_cvt_pk_bf16_f32 v123,v126,v127
v_accvgpr_read_b32 v12,a100
v_mfma_f32_16x16x32_bf16 v[92:95],a[184:187],v[176:179],v[92:95]
s_add_u32 s77,s77,s78
v_exp_f32 v64,v64
v_mfma_f32_16x16x32_bf16 v[92:95],a[180:183],v[168:171],v[92:95]
s_add_u32 s75,s75,16384
v_exp_f32 v65,v65
v_mfma_f32_16x16x32_bf16 v[92:95],a[176:179],v[160:163],v[92:95]
v_exp_f32 v66,v66
v_mfma_f32_16x16x32_bf16 a[32:35],a[192:195],v[112:115],a[32:35]
ds_read_b64_tr_b16 a[192:193],v2
ds_read_b64_tr_b16 a[194:195],v2 offset:4096
v_mfma_f32_16x16x32_bf16 a[32:35],a[196:199],v[120:123],a[32:35]
ds_read_b64_tr_b16 a[196:197],v2 offset:8192
v_cvt_pk_bf16_f32 v64,v64,v65
v_mfma_f32_16x16x32_bf16 a[36:39],a[200:203],v[112:115],a[36:39]
ds_read_b64_tr_b16 a[198:199],v2 offset:12288
v_accvgpr_read_b32 v13,a101
v_mfma_f32_16x16x32_bf16 a[36:39],a[204:207],v[120:123],a[36:39]
global_load_lds_dwordx4 v5,s[10:11] offset:1024
ds_read_b64_tr_b16 a[200:201],v3
v_mfma_f32_16x16x32_bf16 a[40:43],a[208:211],v[112:115],a[40:43]
ds_read_b64_tr_b16 a[202:203],v3 offset:4096
ds_read_b64_tr_b16 a[204:205],v3 offset:8192
v_mfma_f32_16x16x32_bf16 a[40:43],a[212:215],v[120:123],a[40:43]
ds_read_b64_tr_b16 a[206:207],v3 offset:12288
v_accvgpr_read_b32 v14,a102
v_mfma_f32_16x16x32_bf16 a[44:47],a[216:219],v[112:115],a[44:47]
ds_read_b64_tr_b16 a[208:209],v2 offset:256
v_accvgpr_read_b32 v15,a103
v_mfma_f32_16x16x32_bf16 a[44:47],a[220:223],v[120:123],a[44:47]
ds_read_b64_tr_b16 a[210:211],v2 offset:4352
v_accvgpr_read_b32 v8,a96
v_mfma_f32_16x16x32_bf16 a[48:51],a[224:227],v[112:115],a[48:51]
ds_read_b64_tr_b16 a[212:213],v2 offset:8448
ds_read_b64_tr_b16 a[214:215],v2 offset:12544
v_mfma_f32_16x16x32_bf16 a[48:51],a[228:231],v[120:123],a[48:51]
v_exp_f32 v67,v67
v_mfma_f32_16x16x32_bf16 a[52:55],a[232:235],v[112:115],a[52:55]
v_exp_f32 v68,v68
v_mfma_f32_16x16x32_bf16 a[52:55],a[236:239],v[120:123],a[52:55]
v_exp_f32 v69,v69
v_mfma_f32_16x16x32_bf16 a[56:59],a[240:243],v[112:115],a[56:59]
v_exp_f32 v70,v70
v_mfma_f32_16x16x32_bf16 a[56:59],a[244:247],v[120:123],a[56:59]
v_exp_f32 v71,v71
v_mfma_f32_16x16x32_bf16 a[60:63],a[248:251],v[112:115],a[60:63]
ds_read_b64_tr_b16 a[216:217],v3 offset:256
ds_read_b64_tr_b16 a[218:219],v3 offset:4352
v_mfma_f32_16x16x32_bf16 a[60:63],a[252:255],v[120:123],a[60:63]
v_cvt_pk_bf16_f32 v65,v66,v67
v_cvt_pk_bf16_f32 v66,v68,v69
)A"
R"A(v_mfma_f32_16x16x32_bf16 v[60:63],v[28:31],v[112:115],v[60:63]
ds_read_b64_tr_b16 a[220:221],v3 offset:8448
v_cvt_pk_bf16_f32 v67,v70,v71
v_mfma_f32_16x16x32_bf16 v[60:63],v[28:31],v[120:123],v[60:63]
global_load_lds_dwordx4 v4,s[10:11] offset:2048
ds_read_b64_tr_b16 a[222:223],v3 offset:12544
v_mfma_f32_16x16x32_bf16 v[96:99],a[128:131],v[192:195],v[40:43]
v_exp_f32 v72,v72
v_mfma_f32_16x16x32_bf16 v[96:99],a[132:135],v[200:203],v[96:99]
v_exp_f32 v73,v73
v_mfma_f32_16x16x32_bf16 v[96:99],a[136:139],v[208:211],v[96:99]
v_exp_f32 v74,v74
v_mfma_f32_16x16x32_bf16 v[96:99],a[140:143],v[216:219],v[96:99]
v_exp_f32 v75,v75
v_mfma_f32_16x16x32_bf16 v[100:103],a[156:159],v[216:219],v[40:43]
v_exp_f32 v76,v76
v_mfma_f32_16x16x32_bf16 v[100:103],a[152:155],v[208:211],v[100:103]
v_exp_f32 v77,v77
v_mfma_f32_16x16x32_bf16 v[100:103],a[148:151],v[200:203],v[100:103]
v_exp_f32 v78,v78
v_mfma_f32_16x16x32_bf16 v[100:103],a[144:147],v[192:195],v[100:103]
v_exp_f32 v79,v79
v_mfma_f32_16x16x32_bf16 v[104:107],a[160:163],v[192:195],v[40:43]
v_cvt_pk_bf16_f32 v72,v72,v73
v_cvt_pk_bf16_f32 v73,v74,v75
v_mfma_f32_16x16x32_bf16 v[104:107],a[164:167],v[200:203],v[104:107]
v_cvt_pk_bf16_f32 v74,v76,v77
v_cvt_pk_bf16_f32 v75,v78,v79
v_mfma_f32_16x16x32_bf16 v[104:107],a[168:171],v[208:211],v[104:107]
ds_read_b64_tr_b16 a[224:225],v2 offset:512
ds_read_b64_tr_b16 a[226:227],v2 offset:4608
v_mfma_f32_16x16x32_bf16 v[104:107],a[172:175],v[216:219],v[104:107]
v_accvgpr_read_b32 v9,a97
v_accvgpr_read_b32 v10,a98
v_mfma_f32_16x16x32_bf16 v[108:111],a[188:191],v[216:219],v[40:43]
v_accvgpr_read_b32 v11,a99
v_mul_f32 v8,v8,v27
v_mfma_f32_16x16x32_bf16 v[108:111],a[184:187],v[208:211],v[108:111]
global_load_lds_dwordx4 v5,s[10:11] offset:3072
v_mul_f32 v9,v9,v27
v_mfma_f32_16x16x32_bf16 v[108:111],a[180:183],v[200:203],v[108:111]
v_mul_f32 v10,v10,v27
v_mul_f32 v11,v11,v27
v_mfma_f32_16x16x32_bf16 v[108:111],a[176:179],v[192:195],v[108:111]
s_waitcnt lgkmcnt(2)
v_mul_f32 v12,v12,v27
v_mul_f32 v13,v13,v27
v_mul_f32 v14,v14,v27
v_mul_f32 v15,v15,v27
v_cvt_pk_bf16_f32 v8,v8,v9
v_cvt_pk_bf16_f32 v9,v10,v11
v_cvt_pk_bf16_f32 v10,v12,v13
v_cvt_pk_bf16_f32 v11,v14,v15
v_accvgpr_write_b32 a96,v8
v_accvgpr_write_b32 a97,v9
v_accvgpr_write_b32 a98,v10
v_accvgpr_write_b32 a99,v11
v_mfma_f32_16x16x32_bf16 a[0:3],a[192:195],v[64:67],a[0:3]
ds_read_b128 a[100:103],v7 offset:2048
v_add_u32 v1,s78,v1
v_mfma_f32_16x16x32_bf16 a[0:3],a[196:199],v[72:75],a[0:3]
ds_read_b64_tr_b16 a[228:229],v2 offset:8704
v_accvgpr_read_b32 v20,a108
v_mfma_f32_16x16x32_bf16 a[4:7],a[200:203],v[64:67],a[4:7]
ds_read_b64_tr_b16 a[230:231],v2 offset:12800
v_accvgpr_read_b32 v21,a109
v_mfma_f32_16x16x32_bf16 a[4:7],a[204:207],v[72:75],a[4:7]
ds_read_b64_tr_b16 a[232:233],v3 offset:512
v_accvgpr_read_b32 v22,a110
v_mfma_f32_16x16x32_bf16 a[8:11],a[208:211],v[64:67],a[8:11]
ds_read_b64_tr_b16 a[234:235],v3 offset:4608
ds_read_b64_tr_b16 a[236:237],v3 offset:8704
v_mfma_f32_16x16x32_bf16 a[8:11],a[212:215],v[72:75],a[8:11]
ds_read_b64_tr_b16 a[238:239],v3 offset:12800
v_accvgpr_read_b32 v23,a111
v_mfma_f32_16x16x32_bf16 a[12:15],a[216:219],v[64:67],a[12:15]
ds_read_b64_tr_b16 a[240:241],v2 offset:768
v_accvgpr_read_b32 v16,a104
v_mfma_f32_16x16x32_bf16 a[12:15],a[220:223],v[72:75],a[12:15]
s_waitcnt lgkmcnt(8)
ds_read_b64_tr_b16 a[242:243],v2 offset:4864
v_accvgpr_read_b32 v17,a105
v_mfma_f32_16x16x32_bf16 a[16:19],a[224:227],v[64:67],a[16:19]
s_waitcnt lgkmcnt(6)
ds_read_b64_tr_b16 a[244:245],v2 offset:8960
ds_read_b64_tr_b16 a[246:247],v2 offset:13056
v_mfma_f32_16x16x32_bf16 a[16:19],a[228:231],v[72:75],a[16:19]
s_waitcnt lgkmcnt(0)
ds_read_b64_tr_b16 a[248:249],v3 offset:768
v_accvgpr_read_b32 v18,a106
v_mfma_f32_16x16x32_bf16 a[20:23],a[232:235],v[64:67],a[20:23]
s_getreg_b32 s92,hwreg(HW_REG_IB_STS,0,4)
s_min_u32 s4,s92,s6
ds_read_b64_tr_b16 a[250:251],v3 offset:4864
v_mfma_f32_16x16x32_bf16 a[20:23],a[236:239],v[72:75],a[20:23]
s_mov_b32 s6,s92
ds_read_b64_tr_b16 a[252:253],v3 offset:8960
ds_read_b64_tr_b16 a[254:255],v3 offset:13056
v_mfma_f32_16x16x32_bf16 a[24:27],a[240:243],v[64:67],a[24:27]
s_cmp_gt_u32 s4,s9
s_cselect_b32 s4,15,s5
v_accvgpr_read_b32 v19,a107
v_mfma_f32_16x16x32_bf16 a[24:27],a[244:247],v[72:75],a[24:27]
s_waitcnt lgkmcnt(2)
v_exp_f32 v80,v80
v_mfma_f32_16x16x32_bf16 a[28:31],a[248:251],v[64:67],a[28:31]
s_waitcnt lgkmcnt(0)
v_exp_f32 v81,v81
v_mfma_f32_16x16x32_bf16 a[28:31],a[252:255],v[72:75],a[28:31]
s_getpc_b64 s[92:93]
s_cmp_lg_u32 s4,0
s_cbranch_scc1 L_pace_%=
v_mfma_f32_16x16x32_bf16 v[48:51],v[28:31],v[64:67],v[48:51]
v_exp_f32 v82,v82
v_mfma_f32_16x16x32_bf16 v[48:51],v[28:31],v[72:75],v[48:51]
s_add_u32 s10,s20,s74
s_addc_u32 s11,s21,0
v_cvt_pk_bf16_f32 v80,v80,v81
s_waitcnt vmcnt(16)
s_barrier
v_mfma_f32_16x16x32_bf16 v[112:115],a[128:131],v[224:227],v[44:47]
s_add_u32 m0,s76,0
v_exp_f32 v83,v83
v_mfma_f32_16x16x32_bf16 v[112:115],a[132:135],v[232:235],v[112:115]
v_exp_f32 v84,v84
v_mfma_f32_16x16x32_bf16 v[112:115],a[136:139],v[240:243],v[112:115]
global_load_lds_dwordx4 v4,s[10:11] nt
v_cvt_pk_bf16_f32 v81,v82,v83
v_mfma_f32_16x16x32_bf16 v[112:115],a[140:143],v[248:251],v[112:115]
v_exp_f32 v85,v85
v_mfma_f32_16x16x32_bf16 v[116:119],a[156:159],v[248:251],v[44:47]
v_exp_f32 v86,v86
v_mfma_f32_16x16x32_bf16 v[116:119],a[152:155],v[240:243],v[116:119]
v_exp_f32 v87,v87
v_mfma_f32_16x16x32_bf16 v[116:119],a[148:151],v[232:235],v[116:119]
v_cvt_pk_bf16_f32 v82,v84,v85
v_cvt_pk_bf16_f32 v83,v86,v87
v_mfma_f32_16x16x32_bf16 v[116:119],a[144:147],v[224:227],v[116:119]
v_exp_f32 v88,v88
v_mfma_f32_16x16x32_bf16 v[120:123],a[160:163],v[224:227],v[44:47]
v_exp_f32 v89,v89
v_mfma_f32_16x16x32_bf16 v[120:123],a[164:167],v[232:235],v[120:123]
v_exp_f32 v90,v90
v_mfma_f32_16x16x32_bf16 v[120:123],a[168:171],v[240:243],v[120:123]
v_exp_f32 v91,v91
v_mfma_f32_16x16x32_bf16 v[120:123],a[172:175],v[248:251],v[120:123]
v_exp_f32 v92,v92
v_mfma_f32_16x16x32_bf16 v[124:127],a[188:191],v[248:251],v[44:47]
v_exp_f32 v93,v93
v_mfma_f32_16x16x32_bf16 v[124:127],a[184:187],v[240:243],v[124:127]
v_exp_f32 v94,v94
v_exp_f32 v95,v95
v_mfma_f32_16x16x32_bf16 v[124:127],a[180:183],v[232:235],v[124:127]
v_cvt_pk_bf16_f32 v88,v88,v89
v_cvt_pk_bf16_f32 v89,v90,v91
v_cvt_pk_bf16_f32 v90,v92,v93
v_cvt_pk_bf16_f32 v91,v94,v95
v_mfma_f32_16x16x32_bf16 v[124:127],a[176:179],v[224:227],v[124:127]
ds_read_b128 a[128:131],v1
v_mul_f32 v16,v16,v27
v_mfma_f32_16x16x32_bf16 v[132:135],a[192:195],v[80:83],v[132:135]
v_mul_f32 v17,v17,v27
v_mul_f32 v18,v18,v27
v_mfma_f32_16x16x32_bf16 v[132:135],a[196:199],v[88:91],v[132:135]
ds_read_b128 a[132:135],v1 offset:256
v_mul_f32 v19,v19,v27
v_mfma_f32_16x16x32_bf16 v[140:143],a[200:203],v[80:83],v[140:143]
global_load_lds_dwordx4 v4,s[10:11] offset:1024 nt
v_mul_f32 v20,v20,v27
v_mfma_f32_16x16x32_bf16 v[140:143],a[204:207],v[88:91],v[140:143]
ds_read_b128 a[136:139],v1 offset:512
v_mul_f32 v21,v21,v27
v_mfma_f32_16x16x32_bf16 v[148:151],a[208:211],v[80:83],v[148:151]
v_mul_f32 v22,v22,v27
v_mul_f32 v23,v23,v27
v_mfma_f32_16x16x32_bf16 v[148:151],a[212:215],v[88:91],v[148:151]
ds_read_b128 a[140:143],v1 offset:768
v_cvt_pk_bf16_f32 v16,v16,v17
v_mfma_f32_16x16x32_bf16 v[156:159],a[216:219],v[80:83],v[156:159]
v_cvt_pk_bf16_f32 v17,v18,v19
v_cvt_pk_bf16_f32 v18,v20,v21
v_mfma_f32_16x16x32_bf16 v[156:159],a[220:223],v[88:91],v[156:159]
ds_read_b128 a[156:159],v1 offset:4864
v_cvt_pk_bf16_f32 v19,v22,v23
v_mfma_f32_16x16x32_bf16 v[164:167],a[224:227],v[80:83],v[164:167]
v_accvgpr_write_b32 a104,v16
v_accvgpr_write_b32 a105,v17
v_mfma_f32_16x16x32_bf16 v[164:167],a[228:231],v[88:91],v[164:167]
ds_read_b128 a[152:155],v1 offset:4608
v_accvgpr_write_b32 a106,v18
v_mfma_f32_16x16x32_bf16 v[172:175],a[232:235],v[80:83],v[172:175]
v_accvgpr_write_b32 a107,v19
v_accvgpr_read_b32 v12,a68
v_mfma_f32_16x16x32_bf16 v[172:175],a[236:239],v[88:91],v[172:175]
ds_read_b128 a[148:151],v1 offset:4352
v_accvgpr_read_b32 v13,a69
v_mfma_f32_16x16x32_bf16 v[180:183],a[240:243],v[80:83],v[180:183]
v_exp_f32 v96,v96
v_mfma_f32_16x16x32_bf16 v[180:183],a[244:247],v[88:91],v[180:183]
ds_read_b128 a[144:147],v1 offset:4096
v_accvgpr_read_b32 v14,a70
v_mfma_f32_16x16x32_bf16 v[188:191],a[248:251],v[80:83],v[188:191]
v_exp_f32 v97,v97
v_mfma_f32_16x16x32_bf16 v[188:191],a[252:255],v[88:91],v[188:191]
ds_read_b128 a[160:163],v1 offset:8192
v_cvt_pk_bf16_f32 v96,v96,v97
v_mfma_f32_16x16x32_bf16 v[52:55],v[28:31],v[80:83],v[52:55]
v_exp_f32 v98,v98
v_mfma_f32_16x16x32_bf16 v[52:55],v[28:31],v[88:91],v[52:55]
L_pd9_%=:
s_cbranch_execz L_pd13_%=
s_waitcnt lgkmcnt(3)
ds_read_b128 a[164:167],v1 offset:8448
v_accvgpr_read_b32 v15,a71
v_mfma_f32_16x16x32_bf16 v[64:67],a[128:131],v[128:131],v[32:35]
global_load_lds_dwordx4 v5,s[10:11] offset:2048 nt
v_accvgpr_read_b32 v8,a64
v_mfma_f32_16x16x32_bf16 v[64:67],a[132:135],v[136:139],v[64:67]
ds_read_b128 a[168:171],v1 offset:8704
v_accvgpr_read_b32 v9,a65
v_mfma_f32_16x16x32_bf16 v[64:67],a[136:139],v[144:147],v[64:67]
v_exp_f32 v99,v99
v_mfma_f32_16x16x32_bf16 v[64:67],a[140:143],v[152:155],v[64:67]
ds_read_b128 a[108:111],v7 offset:3072
v_cvt_pk_bf16_f32 v97,v98,v99
v_mfma_f32_16x16x32_bf16 v[68:71],a[156:159],v[152:155],v[32:35]
v_exp_f32 v100,v100
v_mfma_f32_16x16x32_bf16 v[68:71],a[152:155],v[144:147],v[68:71]
s_waitcnt lgkmcnt(3)
ds_read_b128 a[172:175],v1 offset:8960
v_accvgpr_read_b32 v10,a66
v_mfma_f32_16x16x32_bf16 v[68:71],a[148:151],v[136:139],v[68:71]
v_exp_f32 v101,v101
v_mfma_f32_16x16x32_bf16 v[68:71],a[144:147],v[128:131],v[68:71]
ds_read_b128 a[188:191],v1 offset:13056
v_cvt_pk_bf16_f32 v98,v100,v101
v_mfma_f32_16x16x32_bf16 v[72:75],a[160:163],v[128:131],v[32:35]
s_waitcnt lgkmcnt(4)
v_exp_f32 v102,v102
v_mfma_f32_16x16x32_bf16 v[72:75],a[164:167],v[136:139],v[72:75]
s_waitcnt lgkmcnt(3)
ds_read_b128 a[184:187],v1 offset:12800
v_accvgpr_read_b32 v11,a67
v_mfma_f32_16x16x32_bf16 v[72:75],a[168:171],v[144:147],v[72:75]
s_waitcnt lgkmcnt(2)
ds_read_b128 a[180:183],v1 offset:12544
v_mul_f32 v8,v8,v26
v_mfma_f32_16x16x32_bf16 v[72:75],a[172:175],v[152:155],v[72:75]
s_waitcnt lgkmcnt(2)
ds_read_b128 a[176:179],v1 offset:12288
v_mul_f32 v9,v9,v26
v_mfma_f32_16x16x32_bf16 v[76:79],a[188:191],v[152:155],v[32:35]
s_waitcnt lgkmcnt(2)
v_exp_f32 v103,v103
v_mfma_f32_16x16x32_bf16 v[76:79],a[184:187],v[144:147],v[76:79]
s_waitcnt lgkmcnt(1)
v_cvt_pk_bf16_f32 v99,v102,v103
v_exp_f32 v104,v104
v_exp_f32 v105,v105
v_exp_f32 v106,v106
v_exp_f32 v107,v107
v_exp_f32 v108,v108
v_exp_f32 v109,v109
v_exp_f32 v110,v110
v_exp_f32 v111,v111
v_mfma_f32_16x16x32_bf16 v[76:79],a[180:183],v[136:139],v[76:79]
s_waitcnt lgkmcnt(0)
v_cvt_pk_bf16_f32 v104,v104,v105
v_cvt_pk_bf16_f32 v105,v106,v107
v_cvt_pk_bf16_f32 v106,v108,v109
v_cvt_pk_bf16_f32 v107,v110,v111
v_mfma_f32_16x16x32_bf16 v[76:79],a[176:179],v[128:131],v[76:79]
v_exp_f32 v112,v112
v_mfma_f32_16x16x32_bf16 v[196:199],a[192:195],v[96:99],v[196:199]
global_load_lds_dwordx4 v5,s[10:11] offset:3072
v_mul_f32 v10,v10,v26
v_mfma_f32_16x16x32_bf16 v[196:199],a[196:199],v[104:107],v[196:199]
v_exp_f32 v113,v113
v_mfma_f32_16x16x32_bf16 v[204:207],a[200:203],v[96:99],v[204:207]
v_exp_f32 v114,v114
v_mfma_f32_16x16x32_bf16 v[204:207],a[204:207],v[104:107],v[204:207]
v_exp_f32 v115,v115
v_mfma_f32_16x16x32_bf16 v[212:215],a[208:211],v[96:99],v[212:215]
v_exp_f32 v116,v116
v_mfma_f32_16x16x32_bf16 v[212:215],a[212:215],v[104:107],v[212:215]
v_exp_f32 v117,v117
v_mfma_f32_16x16x32_bf16 v[220:223],a[216:219],v[96:99],v[220:223]
v_exp_f32 v118,v118
v_mfma_f32_16x16x32_bf16 v[220:223],a[220:223],v[104:107],v[220:223]
v_exp_f32 v119,v119
v_mfma_f32_16x16x32_bf16 v[228:231],a[224:227],v[96:99],v[228:231]
v_cvt_pk_bf16_f32 v112,v112,v113
v_cvt_pk_bf16_f32 v113,v114,v115
v_mfma_f32_16x16x32_bf16 v[228:231],a[228:231],v[104:107],v[228:231]
v_cvt_pk_bf16_f32 v114,v116,v117
v_cvt_pk_bf16_f32 v115,v118,v119
v_mfma_f32_16x16x32_bf16 v[236:239],a[232:235],v[96:99],v[236:239]
v_exp_f32 v120,v120
v_mfma_f32_16x16x32_bf16 v[236:239],a[236:239],v[104:107],v[236:239]
v_exp_f32 v121,v121
)A"
R"A(v_mfma_f32_16x16x32_bf16 v[244:247],a[240:243],v[96:99],v[244:247]
s_add_u32 s10,s24,s74
s_addc_u32 s11,s25,0
v_cvt_pk_bf16_f32 v120,v120,v121
v_mfma_f32_16x16x32_bf16 v[244:247],a[244:247],v[104:107],v[244:247]
s_add_u32 s74,s74,16384
v_exp_f32 v122,v122
v_mfma_f32_16x16x32_bf16 v[252:255],a[248:251],v[96:99],v[252:255]
s_add_u32 m0,s76,16384
v_exp_f32 v123,v123
v_mfma_f32_16x16x32_bf16 v[252:255],a[252:255],v[104:107],v[252:255]
s_and_b32 s74,s74,0x3ffff
v_exp_f32 v124,v124
v_mfma_f32_16x16x32_bf16 v[56:59],v[28:31],v[96:99],v[56:59]
global_load_lds_dwordx4 v4,s[10:11]
v_cvt_pk_bf16_f32 v121,v122,v123
v_mfma_f32_16x16x32_bf16 v[56:59],v[28:31],v[104:107],v[56:59]
s_or_b32 s74,s74,s69
v_exp_f32 v125,v125
v_mfma_f32_16x16x32_bf16 v[80:83],a[128:131],v[160:163],v[36:39]
s_sub_u32 s76,s76,32768
v_exp_f32 v126,v126
v_mfma_f32_16x16x32_bf16 v[80:83],a[132:135],v[168:171],v[80:83]
s_and_b32 s76,s76,0x1ffff
v_exp_f32 v127,v127
v_mfma_f32_16x16x32_bf16 v[80:83],a[136:139],v[176:179],v[80:83]
s_and_b32 s69,s75,0xfffc0000
v_cvt_pk_bf16_f32 v122,v124,v125
v_cvt_pk_bf16_f32 v123,v126,v127
v_mfma_f32_16x16x32_bf16 v[80:83],a[140:143],v[184:187],v[80:83]
v_mul_f32 v11,v11,v26
v_mul_f32 v12,v12,v26
v_mfma_f32_16x16x32_bf16 v[84:87],a[156:159],v[184:187],v[36:39]
v_mul_f32 v13,v13,v26
v_mul_f32 v14,v14,v26
v_mfma_f32_16x16x32_bf16 v[84:87],a[152:155],v[176:179],v[84:87]
v_mul_f32 v15,v15,v26
v_cvt_pk_bf16_f32 v8,v8,v9
v_mfma_f32_16x16x32_bf16 v[84:87],a[148:151],v[168:171],v[84:87]
v_cvt_pk_bf16_f32 v9,v10,v11
v_cvt_pk_bf16_f32 v10,v12,v13
v_mfma_f32_16x16x32_bf16 v[84:87],a[144:147],v[160:163],v[84:87]
v_cvt_pk_bf16_f32 v11,v14,v15
v_accvgpr_write_b32 a64,v8
v_mfma_f32_16x16x32_bf16 v[88:91],a[160:163],v[160:163],v[36:39]
v_accvgpr_write_b32 a65,v9
v_accvgpr_write_b32 a66,v10
v_mfma_f32_16x16x32_bf16 v[88:91],a[164:167],v[168:171],v[88:91]
ds_read_b128 a[68:71],v7 offset:6144
v_accvgpr_write_b32 a67,v11
v_mfma_f32_16x16x32_bf16 v[88:91],a[168:171],v[176:179],v[88:91]
v_add_u32 v2,s78,v2
v_add_u32 v3,s78,v3
v_mfma_f32_16x16x32_bf16 v[88:91],a[172:175],v[184:187],v[88:91]
s_sub_u32 s78,s77,32768
v_exp_f32 v64,v64
v_mfma_f32_16x16x32_bf16 v[92:95],a[188:191],v[184:187],v[36:39]
s_and_b32 s78,s78,0x1ffff
v_exp_f32 v65,v65
v_mfma_f32_16x16x32_bf16 v[92:95],a[184:187],v[176:179],v[92:95]
s_sub_u32 s78,s78,s77
v_exp_f32 v66,v66
v_mfma_f32_16x16x32_bf16 v[92:95],a[180:183],v[168:171],v[92:95]
global_load_lds_dwordx4 v5,s[10:11] offset:1024
v_cvt_pk_bf16_f32 v64,v64,v65
v_mfma_f32_16x16x32_bf16 v[92:95],a[176:179],v[160:163],v[92:95]
s_add_u32 s77,s77,s78
v_exp_f32 v67,v67
v_mfma_f32_16x16x32_bf16 a[32:35],a[192:195],v[112:115],a[32:35]
ds_read_b64_tr_b16 a[192:193],v2
ds_read_b64_tr_b16 a[194:195],v2 offset:4096
v_mfma_f32_16x16x32_bf16 a[32:35],a[196:199],v[120:123],a[32:35]
ds_read_b64_tr_b16 a[196:197],v2 offset:8192
v_cvt_pk_bf16_f32 v65,v66,v67
v_mfma_f32_16x16x32_bf16 a[36:39],a[200:203],v[112:115],a[36:39]
ds_read_b64_tr_b16 a[198:199],v2 offset:12288
v_accvgpr_read_b32 v20,a76
v_mfma_f32_16x16x32_bf16 a[36:39],a[204:207],v[120:123],a[36:39]
ds_read_b64_tr_b16 a[200:201],v3
v_accvgpr_read_b32 v21,a77
v_mfma_f32_16x16x32_bf16 a[40:43],a[208:211],v[112:115],a[40:43]
ds_read_b64_tr_b16 a[202:203],v3 offset:4096
ds_read_b64_tr_b16 a[204:205],v3 offset:8192
v_mfma_f32_16x16x32_bf16 a[40:43],a[212:215],v[120:123],a[40:43]
ds_read_b64_tr_b16 a[206:207],v3 offset:12288
v_accvgpr_read_b32 v22,a78
v_mfma_f32_16x16x32_bf16 a[44:47],a[216:219],v[112:115],a[44:47]
ds_read_b64_tr_b16 a[208:209],v2 offset:256
v_accvgpr_read_b32 v23,a79
v_mfma_f32_16x16x32_bf16 a[44:47],a[220:223],v[120:123],a[44:47]
ds_read_b64_tr_b16 a[210:211],v2 offset:4352
v_accvgpr_read_b32 v16,a72
v_mfma_f32_16x16x32_bf16 a[48:51],a[224:227],v[112:115],a[48:51]
ds_read_b64_tr_b16 a[212:213],v2 offset:8448
ds_read_b64_tr_b16 a[214:215],v2 offset:12544
v_mfma_f32_16x16x32_bf16 a[48:51],a[228:231],v[120:123],a[48:51]
v_exp_f32 v68,v68
v_mfma_f32_16x16x32_bf16 a[52:55],a[232:235],v[112:115],a[52:55]
v_exp_f32 v69,v69
v_mfma_f32_16x16x32_bf16 a[52:55],a[236:239],v[120:123],a[52:55]
v_exp_f32 v70,v70
v_mfma_f32_16x16x32_bf16 a[56:59],a[240:243],v[112:115],a[56:59]
v_exp_f32 v71,v71
v_mfma_f32_16x16x32_bf16 a[56:59],a[244:247],v[120:123],a[56:59]
ds_read_b64_tr_b16 a[216:217],v3 offset:256
ds_read_b64_tr_b16 a[218:219],v3 offset:4352
v_mfma_f32_16x16x32_bf16 a[60:63],a[248:251],v[112:115],a[60:63]
global_load_lds_dwordx4 v4,s[10:11] offset:2048
v_cvt_pk_bf16_f32 v66,v68,v69
v_mfma_f32_16x16x32_bf16 a[60:63],a[252:255],v[120:123],a[60:63]
ds_read_b64_tr_b16 a[220:221],v3 offset:8448
v_cvt_pk_bf16_f32 v67,v70,v71
v_mfma_f32_16x16x32_bf16 v[60:63],v[28:31],v[112:115],v[60:63]
v_exp_f32 v72,v72
v_mfma_f32_16x16x32_bf16 v[60:63],v[28:31],v[120:123],v[60:63]
v_exp_f32 v73,v73
v_mfma_f32_16x16x32_bf16 v[96:99],a[128:131],v[192:195],v[40:43]
v_exp_f32 v74,v74
v_mfma_f32_16x16x32_bf16 v[96:99],a[132:135],v[200:203],v[96:99]
v_exp_f32 v75,v75
v_mfma_f32_16x16x32_bf16 v[96:99],a[136:139],v[208:211],v[96:99]
v_exp_f32 v76,v76
v_mfma_f32_16x16x32_bf16 v[96:99],a[140:143],v[216:219],v[96:99]
v_exp_f32 v77,v77
v_mfma_f32_16x16x32_bf16 v[100:103],a[156:159],v[216:219],v[40:43]
v_exp_f32 v78,v78
v_mfma_f32_16x16x32_bf16 v[100:103],a[152:155],v[208:211],v[100:103]
v_exp_f32 v79,v79
v_mfma_f32_16x16x32_bf16 v[100:103],a[148:151],v[200:203],v[100:103]
ds_read_b64_tr_b16 a[222:223],v3 offset:12544
v_cvt_pk_bf16_f32 v72,v72,v73
v_mfma_f32_16x16x32_bf16 v[100:103],a[144:147],v[192:195],v[100:103]
v_cvt_pk_bf16_f32 v73,v74,v75
v_cvt_pk_bf16_f32 v74,v76,v77
v_mfma_f32_16x16x32_bf16 v[104:107],a[160:163],v[192:195],v[40:43]
ds_read_b64_tr_b16 a[224:225],v2 offset:512
v_cvt_pk_bf16_f32 v75,v78,v79
v_mfma_f32_16x16x32_bf16 v[104:107],a[164:167],v[200:203],v[104:107]
ds_read_b64_tr_b16 a[226:227],v2 offset:4608
ds_read_b64_tr_b16 a[228:229],v2 offset:8704
v_mfma_f32_16x16x32_bf16 v[104:107],a[168:171],v[208:211],v[104:107]
ds_read_b64_tr_b16 a[230:231],v2 offset:12800
v_accvgpr_read_b32 v17,a73
v_mfma_f32_16x16x32_bf16 v[104:107],a[172:175],v[216:219],v[104:107]
ds_read_b64_tr_b16 a[232:233],v3 offset:512
v_accvgpr_read_b32 v18,a74
v_mfma_f32_16x16x32_bf16 v[108:111],a[188:191],v[216:219],v[40:43]
global_load_lds_dwordx4 v5,s[10:11] offset:3072
ds_read_b64_tr_b16 a[234:235],v3 offset:4608
v_mfma_f32_16x16x32_bf16 v[108:111],a[184:187],v[208:211],v[108:111]
ds_read_b64_tr_b16 a[236:237],v3 offset:8704
ds_read_b64_tr_b16 a[238:239],v3 offset:12800
v_mfma_f32_16x16x32_bf16 v[108:111],a[180:183],v[200:203],v[108:111]
ds_read_b64_tr_b16 a[240:241],v2 offset:768
v_accvgpr_read_b32 v19,a75
v_mfma_f32_16x16x32_bf16 v[108:111],a[176:179],v[192:195],v[108:111]
s_waitcnt lgkmcnt(9)
ds_read_b64_tr_b16 a[242:243],v2 offset:4864
v_mul_f32 v16,v16,v26
v_mfma_f32_16x16x32_bf16 a[0:3],a[192:195],v[64:67],a[0:3]
ds_read_b64_tr_b16 a[244:245],v2 offset:8960
v_mul_f32 v17,v17,v26
v_mfma_f32_16x16x32_bf16 a[0:3],a[196:199],v[72:75],a[0:3]
ds_read_b64_tr_b16 a[246:247],v2 offset:13056
ds_read_b64_tr_b16 a[248:249],v3 offset:768
v_mfma_f32_16x16x32_bf16 a[4:7],a[200:203],v[64:67],a[4:7]
ds_read_b64_tr_b16 a[250:251],v3 offset:4864
v_mul_f32 v18,v18,v26
v_mfma_f32_16x16x32_bf16 a[4:7],a[204:207],v[72:75],a[4:7]
ds_read_b64_tr_b16 a[252:253],v3 offset:8960
v_mul_f32 v19,v19,v26
v_mfma_f32_16x16x32_bf16 a[8:11],a[208:211],v[64:67],a[8:11]
ds_read_b64_tr_b16 a[254:255],v3 offset:13056
v_mul_f32 v20,v20,v26
v_mfma_f32_16x16x32_bf16 a[8:11],a[212:215],v[72:75],a[8:11]
v_exp_f32 v80,v80
v_mfma_f32_16x16x32_bf16 a[12:15],a[216:219],v[64:67],a[12:15]
v_exp_f32 v81,v81
v_mfma_f32_16x16x32_bf16 a[12:15],a[220:223],v[72:75],a[12:15]
s_waitcnt lgkmcnt(6)
v_exp_f32 v82,v82
v_mfma_f32_16x16x32_bf16 a[16:19],a[224:227],v[64:67],a[16:19]
v_exp_f32 v83,v83
v_mfma_f32_16x16x32_bf16 a[16:19],a[228:231],v[72:75],a[16:19]
v_exp_f32 v84,v84
v_mfma_f32_16x16x32_bf16 a[20:23],a[232:235],v[64:67],a[20:23]
s_getreg_b32 s92,hwreg(HW_REG_IB_STS,0,4)
s_min_u32 s4,s92,s6
s_mov_b32 s6,s92
v_mfma_f32_16x16x32_bf16 a[20:23],a[236:239],v[72:75],a[20:23]
s_cmp_ge_u32 s4,s9
s_cselect_b32 s4,15,s5
v_cvt_pk_bf16_f32 v80,v80,v81
v_mfma_f32_16x16x32_bf16 a[24:27],a[240:243],v[64:67],a[24:27]
s_waitcnt lgkmcnt(0)
v_exp_f32 v85,v85
v_mfma_f32_16x16x32_bf16 a[24:27],a[244:247],v[72:75],a[24:27]
v_exp_f32 v86,v86
v_mfma_f32_16x16x32_bf16 a[28:31],a[248:251],v[64:67],a[28:31]
v_exp_f32 v87,v87
v_mfma_f32_16x16x32_bf16 a[28:31],a[252:255],v[72:75],a[28:31]
s_getpc_b64 s[92:93]
s_cmp_lg_u32 s4,0
s_cbranch_scc1 L_pace_%=
v_mfma_f32_16x16x32_bf16 v[48:51],v[28:31],v[64:67],v[48:51]
v_cvt_pk_bf16_f32 v81,v82,v83
v_cvt_pk_bf16_f32 v82,v84,v85
v_mfma_f32_16x16x32_bf16 v[48:51],v[28:31],v[72:75],v[48:51]
s_add_u32 s10,s20,s74
s_addc_u32 s11,s21,0
v_cvt_pk_bf16_f32 v83,v86,v87
s_waitcnt vmcnt(16)
s_barrier
v_mfma_f32_16x16x32_bf16 v[112:115],a[128:131],v[224:227],v[44:47]
s_add_u32 m0,s76,0
v_exp_f32 v88,v88
v_mfma_f32_16x16x32_bf16 v[112:115],a[132:135],v[232:235],v[112:115]
v_exp_f32 v89,v89
v_mfma_f32_16x16x32_bf16 v[112:115],a[136:139],v[240:243],v[112:115]
global_load_lds_dwordx4 v4,s[10:11] nt
v_cvt_pk_bf16_f32 v88,v88,v89
v_mfma_f32_16x16x32_bf16 v[112:115],a[140:143],v[248:251],v[112:115]
v_exp_f32 v90,v90
v_mfma_f32_16x16x32_bf16 v[116:119],a[156:159],v[248:251],v[44:47]
v_exp_f32 v91,v91
v_mfma_f32_16x16x32_bf16 v[116:119],a[152:155],v[240:243],v[116:119]
v_exp_f32 v92,v92
v_mfma_f32_16x16x32_bf16 v[116:119],a[148:151],v[232:235],v[116:119]
v_exp_f32 v93,v93
v_mfma_f32_16x16x32_bf16 v[116:119],a[144:147],v[224:227],v[116:119]
v_exp_f32 v94,v94
v_mfma_f32_16x16x32_bf16 v[120:123],a[160:163],v[224:227],v[44:47]
v_exp_f32 v95,v95
v_mfma_f32_16x16x32_bf16 v[120:123],a[164:167],v[232:235],v[120:123]
v_cvt_pk_bf16_f32 v89,v90,v91
v_cvt_pk_bf16_f32 v90,v92,v93
v_mfma_f32_16x16x32_bf16 v[120:123],a[168:171],v[240:243],v[120:123]
v_cvt_pk_bf16_f32 v91,v94,v95
v_mul_f32 v21,v21,v26
v_mfma_f32_16x16x32_bf16 v[120:123],a[172:175],v[248:251],v[120:123]
v_mul_f32 v22,v22,v26
v_mul_f32 v23,v23,v26
v_mfma_f32_16x16x32_bf16 v[124:127],a[188:191],v[248:251],v[44:47]
v_cvt_pk_bf16_f32 v16,v16,v17
v_cvt_pk_bf16_f32 v17,v18,v19
v_mfma_f32_16x16x32_bf16 v[124:127],a[184:187],v[240:243],v[124:127]
v_cvt_pk_bf16_f32 v18,v20,v21
v_cvt_pk_bf16_f32 v19,v22,v23
v_mfma_f32_16x16x32_bf16 v[124:127],a[180:183],v[232:235],v[124:127]
v_accvgpr_write_b32 a72,v16
v_accvgpr_write_b32 a73,v17
v_mfma_f32_16x16x32_bf16 v[124:127],a[176:179],v[224:227],v[124:127]
v_accvgpr_write_b32 a74,v18
v_accvgpr_write_b32 a75,v19
v_mfma_f32_16x16x32_bf16 v[132:135],a[192:195],v[80:83],v[132:135]
ds_read_b128 a[76:79],v7 offset:7168
v_add_u32 v1,s78,v1
v_mfma_f32_16x16x32_bf16 v[132:135],a[196:199],v[88:91],v[132:135]
v_exp_f32 v96,v96
v_mfma_f32_16x16x32_bf16 v[140:143],a[200:203],v[80:83],v[140:143]
global_load_lds_dwordx4 v4,s[10:11] offset:1024 nt
ds_read_b128 a[128:131],v1
v_mfma_f32_16x16x32_bf16 v[140:143],a[204:207],v[88:91],v[140:143]
v_exp_f32 v97,v97
v_mfma_f32_16x16x32_bf16 v[148:151],a[208:211],v[80:83],v[148:151]
ds_read_b128 a[132:135],v1 offset:256
v_cvt_pk_bf16_f32 v96,v96,v97
v_mfma_f32_16x16x32_bf16 v[148:151],a[212:215],v[88:91],v[148:151]
v_exp_f32 v98,v98
v_mfma_f32_16x16x32_bf16 v[156:159],a[216:219],v[80:83],v[156:159]
ds_read_b128 a[136:139],v1 offset:512
v_accvgpr_read_b32 v12,a84
v_mfma_f32_16x16x32_bf16 v[156:159],a[220:223],v[88:91],v[156:159]
v_exp_f32 v99,v99
v_mfma_f32_16x16x32_bf16 v[164:167],a[224:227],v[80:83],v[164:167]
ds_read_b128 a[140:143],v1 offset:768
v_cvt_pk_bf16_f32 v97,v98,v99
v_mfma_f32_16x16x32_bf16 v[164:167],a[228:231],v[88:91],v[164:167]
v_exp_f32 v100,v100
v_mfma_f32_16x16x32_bf16 v[172:175],a[232:235],v[80:83],v[172:175]
ds_read_b128 a[156:159],v1 offset:4864
v_accvgpr_read_b32 v13,a85
v_mfma_f32_16x16x32_bf16 v[172:175],a[236:239],v[88:91],v[172:175]
v_exp_f32 v101,v101
v_mfma_f32_16x16x32_bf16 v[180:183],a[240:243],v[80:83],v[180:183]
ds_read_b128 a[152:155],v1 offset:4608
v_cvt_pk_bf16_f32 v98,v100,v101
v_mfma_f32_16x16x32_bf16 v[180:183],a[244:247],v[88:91],v[180:183]
)A"
R"A(v_exp_f32 v102,v102
v_mfma_f32_16x16x32_bf16 v[188:191],a[248:251],v[80:83],v[188:191]
ds_read_b128 a[148:151],v1 offset:4352
v_accvgpr_read_b32 v14,a86
v_mfma_f32_16x16x32_bf16 v[188:191],a[252:255],v[88:91],v[188:191]
v_exp_f32 v103,v103
v_mfma_f32_16x16x32_bf16 v[52:55],v[28:31],v[80:83],v[52:55]
ds_read_b128 a[144:147],v1 offset:4096
v_cvt_pk_bf16_f32 v99,v102,v103
v_mfma_f32_16x16x32_bf16 v[52:55],v[28:31],v[88:91],v[52:55]
s_waitcnt lgkmcnt(4)
v_exp_f32 v104,v104
v_mfma_f32_16x16x32_bf16 v[64:67],a[128:131],v[128:131],v[32:35]
global_load_lds_dwordx4 v5,s[10:11] offset:2048 nt
ds_read_b128 a[160:163],v1 offset:8192
v_mfma_f32_16x16x32_bf16 v[64:67],a[132:135],v[136:139],v[64:67]
v_exp_f32 v105,v105
v_mfma_f32_16x16x32_bf16 v[64:67],a[136:139],v[144:147],v[64:67]
ds_read_b128 a[164:167],v1 offset:8448
v_cvt_pk_bf16_f32 v104,v104,v105
v_mfma_f32_16x16x32_bf16 v[64:67],a[140:143],v[152:155],v[64:67]
s_waitcnt lgkmcnt(4)
v_exp_f32 v106,v106
v_mfma_f32_16x16x32_bf16 v[68:71],a[156:159],v[152:155],v[32:35]
ds_read_b128 a[168:171],v1 offset:8704
v_accvgpr_read_b32 v15,a87
v_mfma_f32_16x16x32_bf16 v[68:71],a[152:155],v[144:147],v[68:71]
s_waitcnt lgkmcnt(4)
v_exp_f32 v107,v107
v_mfma_f32_16x16x32_bf16 v[68:71],a[148:151],v[136:139],v[68:71]
s_waitcnt lgkmcnt(3)
ds_read_b128 a[172:175],v1 offset:8960
v_cvt_pk_bf16_f32 v105,v106,v107
v_mfma_f32_16x16x32_bf16 v[68:71],a[144:147],v[128:131],v[68:71]
s_waitcnt lgkmcnt(3)
v_exp_f32 v108,v108
v_mfma_f32_16x16x32_bf16 v[72:75],a[160:163],v[128:131],v[32:35]
s_waitcnt lgkmcnt(2)
ds_read_b128 a[188:191],v1 offset:13056
v_accvgpr_read_b32 v8,a80
v_mfma_f32_16x16x32_bf16 v[72:75],a[164:167],v[136:139],v[72:75]
s_waitcnt lgkmcnt(2)
ds_read_b128 a[184:187],v1 offset:12800
v_accvgpr_read_b32 v9,a81
v_mfma_f32_16x16x32_bf16 v[72:75],a[168:171],v[144:147],v[72:75]
s_waitcnt lgkmcnt(2)
ds_read_b128 a[180:183],v1 offset:12544
v_accvgpr_read_b32 v10,a82
v_mfma_f32_16x16x32_bf16 v[72:75],a[172:175],v[152:155],v[72:75]
s_waitcnt lgkmcnt(2)
ds_read_b128 a[176:179],v1 offset:12288
v_accvgpr_read_b32 v11,a83
v_mfma_f32_16x16x32_bf16 v[76:79],a[188:191],v[152:155],v[32:35]
s_waitcnt lgkmcnt(2)
v_exp_f32 v109,v109
v_mfma_f32_16x16x32_bf16 v[76:79],a[184:187],v[144:147],v[76:79]
s_waitcnt lgkmcnt(1)
v_exp_f32 v110,v110
v_exp_f32 v111,v111
v_mfma_f32_16x16x32_bf16 v[76:79],a[180:183],v[136:139],v[76:79]
s_waitcnt lgkmcnt(0)
v_cvt_pk_bf16_f32 v106,v108,v109
v_cvt_pk_bf16_f32 v107,v110,v111
v_mfma_f32_16x16x32_bf16 v[76:79],a[176:179],v[128:131],v[76:79]
v_mul_f32 v8,v8,v26
v_mul_f32 v9,v9,v26
v_mfma_f32_16x16x32_bf16 v[196:199],a[192:195],v[96:99],v[196:199]
global_load_lds_dwordx4 v5,s[10:11] offset:3072
v_mul_f32 v10,v10,v26
v_mfma_f32_16x16x32_bf16 v[196:199],a[196:199],v[104:107],v[196:199]
v_mul_f32 v11,v11,v26
v_mul_f32 v12,v12,v26
v_mfma_f32_16x16x32_bf16 v[204:207],a[200:203],v[96:99],v[204:207]
v_mul_f32 v13,v13,v26
v_mul_f32 v14,v14,v26
v_mfma_f32_16x16x32_bf16 v[204:207],a[204:207],v[104:107],v[204:207]
v_mul_f32 v15,v15,v26
v_cvt_pk_bf16_f32 v8,v8,v9
v_mfma_f32_16x16x32_bf16 v[212:215],a[208:211],v[96:99],v[212:215]
v_cvt_pk_bf16_f32 v9,v10,v11
v_cvt_pk_bf16_f32 v10,v12,v13
v_mfma_f32_16x16x32_bf16 v[212:215],a[212:215],v[104:107],v[212:215]
v_cvt_pk_bf16_f32 v11,v14,v15
v_accvgpr_write_b32 a80,v8
v_mfma_f32_16x16x32_bf16 v[220:223],a[216:219],v[96:99],v[220:223]
v_accvgpr_write_b32 a81,v9
v_accvgpr_write_b32 a82,v10
v_mfma_f32_16x16x32_bf16 v[220:223],a[220:223],v[104:107],v[220:223]
ds_read_b128 a[84:87],v7
v_accvgpr_write_b32 a83,v11
v_mfma_f32_16x16x32_bf16 v[228:231],a[224:227],v[96:99],v[228:231]
v_exp_f32 v112,v112
v_mfma_f32_16x16x32_bf16 v[228:231],a[228:231],v[104:107],v[228:231]
v_exp_f32 v113,v113
v_mfma_f32_16x16x32_bf16 v[236:239],a[232:235],v[96:99],v[236:239]
v_exp_f32 v114,v114
v_mfma_f32_16x16x32_bf16 v[236:239],a[236:239],v[104:107],v[236:239]
v_exp_f32 v115,v115
v_mfma_f32_16x16x32_bf16 v[244:247],a[240:243],v[96:99],v[244:247]
s_add_u32 s10,s24,s74
s_addc_u32 s11,s25,0
v_cvt_pk_bf16_f32 v112,v112,v113
v_mfma_f32_16x16x32_bf16 v[244:247],a[244:247],v[104:107],v[244:247]
s_add_u32 s74,s74,16384
v_exp_f32 v116,v116
v_mfma_f32_16x16x32_bf16 v[252:255],a[248:251],v[96:99],v[252:255]
s_add_u32 m0,s76,16384
v_exp_f32 v117,v117
v_mfma_f32_16x16x32_bf16 v[252:255],a[252:255],v[104:107],v[252:255]
s_and_b32 s74,s74,0x3ffff
v_exp_f32 v118,v118
v_mfma_f32_16x16x32_bf16 v[56:59],v[28:31],v[96:99],v[56:59]
global_load_lds_dwordx4 v4,s[10:11]
v_cvt_pk_bf16_f32 v113,v114,v115
v_mfma_f32_16x16x32_bf16 v[56:59],v[28:31],v[104:107],v[56:59]
s_or_b32 s74,s74,s69
v_exp_f32 v119,v119
v_mfma_f32_16x16x32_bf16 v[80:83],a[128:131],v[160:163],v[36:39]
s_sub_u32 s76,s76,32768
v_cvt_pk_bf16_f32 v114,v116,v117
v_cvt_pk_bf16_f32 v115,v118,v119
v_mfma_f32_16x16x32_bf16 v[80:83],a[132:135],v[168:171],v[80:83]
s_and_b32 s76,s76,0x1ffff
v_exp_f32 v120,v120
v_mfma_f32_16x16x32_bf16 v[80:83],a[136:139],v[176:179],v[80:83]
v_exp_f32 v121,v121
v_mfma_f32_16x16x32_bf16 v[80:83],a[140:143],v[184:187],v[80:83]
v_exp_f32 v122,v122
v_mfma_f32_16x16x32_bf16 v[84:87],a[156:159],v[184:187],v[36:39]
v_exp_f32 v123,v123
v_mfma_f32_16x16x32_bf16 v[84:87],a[152:155],v[176:179],v[84:87]
v_exp_f32 v124,v124
v_mfma_f32_16x16x32_bf16 v[84:87],a[148:151],v[168:171],v[84:87]
v_exp_f32 v125,v125
v_mfma_f32_16x16x32_bf16 v[84:87],a[144:147],v[160:163],v[84:87]
v_exp_f32 v126,v126
v_mfma_f32_16x16x32_bf16 v[88:91],a[160:163],v[160:163],v[36:39]
v_exp_f32 v127,v127
v_mfma_f32_16x16x32_bf16 v[88:91],a[164:167],v[168:171],v[88:91]
v_cvt_pk_bf16_f32 v120,v120,v121
v_cvt_pk_bf16_f32 v121,v122,v123
v_mfma_f32_16x16x32_bf16 v[88:91],a[168:171],v[176:179],v[88:91]
v_cvt_pk_bf16_f32 v122,v124,v125
v_cvt_pk_bf16_f32 v123,v126,v127
v_mfma_f32_16x16x32_bf16 v[88:91],a[172:175],v[184:187],v[88:91]
v_add_u32 v2,s78,v2
v_add_u32 v3,s78,v3
v_mfma_f32_16x16x32_bf16 v[92:95],a[188:191],v[184:187],v[36:39]
s_sub_u32 s78,s77,32768
v_exp_f32 v64,v64
v_mfma_f32_16x16x32_bf16 v[92:95],a[184:187],v[176:179],v[92:95]
s_and_b32 s78,s78,0x1ffff
v_exp_f32 v65,v65
v_mfma_f32_16x16x32_bf16 v[92:95],a[180:183],v[168:171],v[92:95]
global_load_lds_dwordx4 v5,s[10:11] offset:1024
v_cvt_pk_bf16_f32 v64,v64,v65
v_mfma_f32_16x16x32_bf16 v[92:95],a[176:179],v[160:163],v[92:95]
s_sub_u32 s78,s78,s77
v_exp_f32 v66,v66
v_mfma_f32_16x16x32_bf16 a[32:35],a[192:195],v[112:115],a[32:35]
s_add_u32 s77,s77,s78
ds_read_b64_tr_b16 a[192:193],v2
ds_read_b64_tr_b16 a[194:195],v2 offset:4096
v_mfma_f32_16x16x32_bf16 a[32:35],a[196:199],v[120:123],a[32:35]
ds_read_b64_tr_b16 a[196:197],v2 offset:8192
v_accvgpr_read_b32 v20,a92
v_mfma_f32_16x16x32_bf16 a[36:39],a[200:203],v[112:115],a[36:39]
ds_read_b64_tr_b16 a[198:199],v2 offset:12288
v_accvgpr_read_b32 v21,a93
v_mfma_f32_16x16x32_bf16 a[36:39],a[204:207],v[120:123],a[36:39]
ds_read_b64_tr_b16 a[200:201],v3
v_accvgpr_read_b32 v22,a94
v_mfma_f32_16x16x32_bf16 a[40:43],a[208:211],v[112:115],a[40:43]
ds_read_b64_tr_b16 a[202:203],v3 offset:4096
ds_read_b64_tr_b16 a[204:205],v3 offset:8192
v_mfma_f32_16x16x32_bf16 a[40:43],a[212:215],v[120:123],a[40:43]
ds_read_b64_tr_b16 a[206:207],v3 offset:12288
v_accvgpr_read_b32 v23,a95
v_mfma_f32_16x16x32_bf16 a[44:47],a[216:219],v[112:115],a[44:47]
ds_read_b64_tr_b16 a[208:209],v2 offset:256
v_accvgpr_read_b32 v16,a88
v_mfma_f32_16x16x32_bf16 a[44:47],a[220:223],v[120:123],a[44:47]
ds_read_b64_tr_b16 a[210:211],v2 offset:4352
v_accvgpr_read_b32 v17,a89
v_mfma_f32_16x16x32_bf16 a[48:51],a[224:227],v[112:115],a[48:51]
ds_read_b64_tr_b16 a[212:213],v2 offset:8448
ds_read_b64_tr_b16 a[214:215],v2 offset:12544
v_mfma_f32_16x16x32_bf16 a[48:51],a[228:231],v[120:123],a[48:51]
v_exp_f32 v67,v67
v_mfma_f32_16x16x32_bf16 a[52:55],a[232:235],v[112:115],a[52:55]
v_exp_f32 v68,v68
v_mfma_f32_16x16x32_bf16 a[52:55],a[236:239],v[120:123],a[52:55]
v_exp_f32 v69,v69
v_mfma_f32_16x16x32_bf16 a[56:59],a[240:243],v[112:115],a[56:59]
v_exp_f32 v70,v70
v_mfma_f32_16x16x32_bf16 a[56:59],a[244:247],v[120:123],a[56:59]
v_exp_f32 v71,v71
v_mfma_f32_16x16x32_bf16 a[60:63],a[248:251],v[112:115],a[60:63]
global_load_lds_dwordx4 v4,s[10:11] offset:2048
ds_read_b64_tr_b16 a[216:217],v3 offset:256
v_mfma_f32_16x16x32_bf16 a[60:63],a[252:255],v[120:123],a[60:63]
ds_read_b64_tr_b16 a[218:219],v3 offset:4352
v_cvt_pk_bf16_f32 v65,v66,v67
v_mfma_f32_16x16x32_bf16 v[60:63],v[28:31],v[112:115],v[60:63]
v_cvt_pk_bf16_f32 v66,v68,v69
v_cvt_pk_bf16_f32 v67,v70,v71
v_mfma_f32_16x16x32_bf16 v[60:63],v[28:31],v[120:123],v[60:63]
v_exp_f32 v72,v72
v_mfma_f32_16x16x32_bf16 v[96:99],a[128:131],v[192:195],v[40:43]
v_exp_f32 v73,v73
v_mfma_f32_16x16x32_bf16 v[96:99],a[132:135],v[200:203],v[96:99]
v_exp_f32 v74,v74
v_mfma_f32_16x16x32_bf16 v[96:99],a[136:139],v[208:211],v[96:99]
v_exp_f32 v75,v75
v_mfma_f32_16x16x32_bf16 v[96:99],a[140:143],v[216:219],v[96:99]
v_exp_f32 v76,v76
v_mfma_f32_16x16x32_bf16 v[100:103],a[156:159],v[216:219],v[40:43]
v_exp_f32 v77,v77
v_mfma_f32_16x16x32_bf16 v[100:103],a[152:155],v[208:211],v[100:103]
v_exp_f32 v78,v78
v_mfma_f32_16x16x32_bf16 v[100:103],a[148:151],v[200:203],v[100:103]
v_exp_f32 v79,v79
v_mfma_f32_16x16x32_bf16 v[100:103],a[144:147],v[192:195],v[100:103]
ds_read_b64_tr_b16 a[220:221],v3 offset:8448
ds_read_b64_tr_b16 a[222:223],v3 offset:12544
v_mfma_f32_16x16x32_bf16 v[104:107],a[160:163],v[192:195],v[40:43]
v_cvt_pk_bf16_f32 v72,v72,v73
L_pd10_%=:
s_cbranch_execz L_t1b_%=
v_cvt_pk_bf16_f32 v73,v74,v75
v_mfma_f32_16x16x32_bf16 v[104:107],a[164:167],v[200:203],v[104:107]
v_cvt_pk_bf16_f32 v74,v76,v77
v_cvt_pk_bf16_f32 v75,v78,v79
v_mfma_f32_16x16x32_bf16 v[104:107],a[168:171],v[208:211],v[104:107]
ds_read_b64_tr_b16 a[224:225],v2 offset:512
ds_read_b64_tr_b16 a[226:227],v2 offset:4608
v_mfma_f32_16x16x32_bf16 v[104:107],a[172:175],v[216:219],v[104:107]
ds_read_b64_tr_b16 a[228:229],v2 offset:8704
v_accvgpr_read_b32 v18,a90
v_mfma_f32_16x16x32_bf16 v[108:111],a[188:191],v[216:219],v[40:43]
global_load_lds_dwordx4 v5,s[10:11] offset:3072
ds_read_b64_tr_b16 a[230:231],v2 offset:12800
v_mfma_f32_16x16x32_bf16 v[108:111],a[184:187],v[208:211],v[108:111]
ds_read_b64_tr_b16 a[232:233],v3 offset:512
v_accvgpr_read_b32 v19,a91
v_mfma_f32_16x16x32_bf16 v[108:111],a[180:183],v[200:203],v[108:111]
ds_read_b64_tr_b16 a[234:235],v3 offset:4608
ds_read_b64_tr_b16 a[236:237],v3 offset:8704
v_mfma_f32_16x16x32_bf16 v[108:111],a[176:179],v[192:195],v[108:111]
s_waitcnt lgkmcnt(7)
ds_read_b64_tr_b16 a[238:239],v3 offset:12800
v_mul_f32 v16,v16,v26
v_mfma_f32_16x16x32_bf16 a[0:3],a[192:195],v[64:67],a[0:3]
ds_read_b64_tr_b16 a[240:241],v2 offset:768
v_mul_f32 v17,v17,v26
v_mfma_f32_16x16x32_bf16 a[0:3],a[196:199],v[72:75],a[0:3]
ds_read_b64_tr_b16 a[242:243],v2 offset:4864
v_mul_f32 v18,v18,v26
v_mfma_f32_16x16x32_bf16 a[4:7],a[200:203],v[64:67],a[4:7]
ds_read_b64_tr_b16 a[244:245],v2 offset:8960
ds_read_b64_tr_b16 a[246:247],v2 offset:13056
v_mfma_f32_16x16x32_bf16 a[4:7],a[204:207],v[72:75],a[4:7]
ds_read_b64_tr_b16 a[248:249],v3 offset:768
v_mul_f32 v19,v19,v26
v_mfma_f32_16x16x32_bf16 a[8:11],a[208:211],v[64:67],a[8:11]
ds_read_b64_tr_b16 a[250:251],v3 offset:4864
v_mul_f32 v20,v20,v26
v_mfma_f32_16x16x32_bf16 a[8:11],a[212:215],v[72:75],a[8:11]
v_mul_f32 v21,v21,v26
v_mul_f32 v22,v22,v26
v_mul_f32 v23,v23,v26
v_cvt_pk_bf16_f32 v16,v16,v17
v_cvt_pk_bf16_f32 v17,v18,v19
v_cvt_pk_bf16_f32 v18,v20,v21
v_cvt_pk_bf16_f32 v19,v22,v23
v_accvgpr_write_b32 a88,v16
v_accvgpr_write_b32 a89,v17
v_accvgpr_write_b32 a90,v18
v_accvgpr_write_b32 a91,v19
v_mfma_f32_16x16x32_bf16 a[12:15],a[216:219],v[64:67],a[12:15]
ds_read_b128 a[92:95],v7 offset:1024
v_mov_b32 v8,v7
v_mfma_f32_16x16x32_bf16 a[12:15],a[220:223],v[72:75],a[12:15]
s_waitcnt lgkmcnt(7)
ds_read_b64_tr_b16 a[252:253],v3 offset:8960
v_add_u32 v1,s78,v1
v_mfma_f32_16x16x32_bf16 a[16:19],a[224:227],v[64:67],a[16:19]
ds_read_b64_tr_b16 a[254:255],v3 offset:13056
v_mfma_f32_16x16x32_bf16 a[16:19],a[228:231],v[72:75],a[16:19]
s_waitcnt lgkmcnt(4)
v_exp_f32 v80,v80
v_mfma_f32_16x16x32_bf16 a[20:23],a[232:235],v[64:67],a[20:23]
)A"
R"A(s_getreg_b32 s92,hwreg(HW_REG_IB_STS,0,4)
s_min_u32 s4,s92,s6
s_mov_b32 s6,s92
v_mfma_f32_16x16x32_bf16 a[20:23],a[236:239],v[72:75],a[20:23]
s_cmp_ge_u32 s4,s9
s_cselect_b32 s4,15,s5
ds_read_b128 v[24:27],v8 offset:4096
v_mfma_f32_16x16x32_bf16 a[24:27],a[240:243],v[64:67],a[24:27]
v_exp_f32 v81,v81
v_mfma_f32_16x16x32_bf16 a[24:27],a[244:247],v[72:75],a[24:27]
s_waitcnt lgkmcnt(4)
v_exp_f32 v82,v82
v_mfma_f32_16x16x32_bf16 a[28:31],a[248:251],v[64:67],a[28:31]
s_waitcnt lgkmcnt(1)
v_exp_f32 v83,v83
v_mfma_f32_16x16x32_bf16 a[28:31],a[252:255],v[72:75],a[28:31]
v_exp_f32 v84,v84
v_mfma_f32_16x16x32_bf16 v[48:51],v[28:31],v[64:67],v[48:51]
s_getpc_b64 s[92:93]
s_cmp_lg_u32 s4,0
s_cbranch_scc1 L_pace_%=
v_exp_f32 v85,v85
v_exp_f32 v86,v86
v_exp_f32 v87,v87
v_exp_f32 v88,v88
v_exp_f32 v89,v89
v_mfma_f32_16x16x32_bf16 v[48:51],v[28:31],v[72:75],v[48:51]
ds_read_b32 v7,v8 offset:5120
v_cvt_pk_bf16_f32 v80,v80,v81
v_cvt_pk_bf16_f32 v81,v82,v83
v_cvt_pk_bf16_f32 v82,v84,v85
v_cvt_pk_bf16_f32 v83,v86,v87
v_cvt_pk_bf16_f32 v88,v88,v89
s_mov_b32 s70,8
L_a1_%=:
s_waitcnt vmcnt(16)
s_barrier
v_mfma_f32_16x16x32_bf16 v[112:115],a[128:131],v[224:227],v[44:47]
s_add_u32 s10,s20,s74
s_addc_u32 s11,s21,0
s_add_u32 m0,s76,0
v_mfma_f32_16x16x32_bf16 v[112:115],a[132:135],v[232:235],v[112:115]
s_add_u32 s75,s75,16384
v_exp_f32 v90,v90
v_mfma_f32_16x16x32_bf16 v[112:115],a[136:139],v[240:243],v[112:115]
global_load_lds_dwordx4 v4,s[10:11] nt
ds_read_b128 a[128:131],v1
v_mfma_f32_16x16x32_bf16 v[112:115],a[140:143],v[248:251],v[112:115]
s_and_b32 s69,s75,0xfffc0000
v_exp_f32 v91,v91
v_mfma_f32_16x16x32_bf16 v[116:119],a[156:159],v[248:251],v[44:47]
v_exp_f32 v92,v92
v_mfma_f32_16x16x32_bf16 v[116:119],a[152:155],v[240:243],v[116:119]
v_exp_f32 v93,v93
v_mfma_f32_16x16x32_bf16 v[116:119],a[148:151],v[232:235],v[116:119]
v_exp_f32 v94,v94
v_mfma_f32_16x16x32_bf16 v[116:119],a[144:147],v[224:227],v[116:119]
v_exp_f32 v95,v95
v_mfma_f32_16x16x32_bf16 v[120:123],a[160:163],v[224:227],v[44:47]
v_cvt_pk_bf16_f32 v89,v90,v91
v_cvt_pk_bf16_f32 v90,v92,v93
v_mfma_f32_16x16x32_bf16 v[120:123],a[164:167],v[232:235],v[120:123]
ds_read_b128 a[132:135],v1 offset:256
v_cvt_pk_bf16_f32 v91,v94,v95
v_mfma_f32_16x16x32_bf16 v[120:123],a[168:171],v[240:243],v[120:123]
v_exp_f32 v96,v96
v_mfma_f32_16x16x32_bf16 v[120:123],a[172:175],v[248:251],v[120:123]
ds_read_b128 a[136:139],v1 offset:512
v_add_u32 v2,s78,v2
v_mfma_f32_16x16x32_bf16 v[124:127],a[188:191],v[248:251],v[44:47]
v_exp_f32 v97,v97
v_mfma_f32_16x16x32_bf16 v[124:127],a[184:187],v[240:243],v[124:127]
ds_read_b128 a[140:143],v1 offset:768
v_cvt_pk_bf16_f32 v96,v96,v97
v_mfma_f32_16x16x32_bf16 v[124:127],a[180:183],v[232:235],v[124:127]
v_exp_f32 v98,v98
v_mfma_f32_16x16x32_bf16 v[124:127],a[176:179],v[224:227],v[124:127]
ds_read_b128 a[156:159],v1 offset:4864
v_add_u32 v3,s78,v3
v_mfma_f32_16x16x32_bf16 v[132:135],a[192:195],v[80:83],v[132:135]
s_sub_u32 s78,s77,32768
v_exp_f32 v99,v99
v_mfma_f32_16x16x32_bf16 v[132:135],a[196:199],v[88:91],v[132:135]
s_and_b32 s78,s78,0x1ffff
ds_read_b128 a[152:155],v1 offset:4608
v_cvt_pk_bf16_f32 v97,v98,v99
v_mfma_f32_16x16x32_bf16 v[140:143],a[200:203],v[80:83],v[140:143]
s_sub_u32 s78,s78,s77
global_load_lds_dwordx4 v4,s[10:11] offset:1024 nt
v_mfma_f32_16x16x32_bf16 v[140:143],a[204:207],v[88:91],v[140:143]
s_add_u32 s77,s77,s78
v_exp_f32 v100,v100
v_mfma_f32_16x16x32_bf16 v[148:151],a[208:211],v[80:83],v[148:151]
v_exp_f32 v101,v101
v_mfma_f32_16x16x32_bf16 v[148:151],a[212:215],v[88:91],v[148:151]
ds_read_b128 a[148:151],v1 offset:4352
v_cvt_pk_bf16_f32 v98,v100,v101
v_mfma_f32_16x16x32_bf16 v[156:159],a[216:219],v[80:83],v[156:159]
v_exp_f32 v102,v102
v_mfma_f32_16x16x32_bf16 v[156:159],a[220:223],v[88:91],v[156:159]
v_exp_f32 v103,v103
v_mfma_f32_16x16x32_bf16 v[164:167],a[224:227],v[80:83],v[164:167]
ds_read_b128 a[144:147],v1 offset:4096
v_cvt_pk_bf16_f32 v99,v102,v103
v_mfma_f32_16x16x32_bf16 v[164:167],a[228:231],v[88:91],v[164:167]
v_exp_f32 v104,v104
v_mfma_f32_16x16x32_bf16 v[172:175],a[232:235],v[80:83],v[172:175]
v_exp_f32 v105,v105
v_mfma_f32_16x16x32_bf16 v[172:175],a[236:239],v[88:91],v[172:175]
ds_read_b128 a[160:163],v1 offset:8192
v_cvt_pk_bf16_f32 v104,v104,v105
v_mfma_f32_16x16x32_bf16 v[180:183],a[240:243],v[80:83],v[180:183]
v_exp_f32 v106,v106
v_mfma_f32_16x16x32_bf16 v[180:183],a[244:247],v[88:91],v[180:183]
v_exp_f32 v107,v107
v_mfma_f32_16x16x32_bf16 v[188:191],a[248:251],v[80:83],v[188:191]
ds_read_b128 a[164:167],v1 offset:8448
v_cvt_pk_bf16_f32 v105,v106,v107
v_mfma_f32_16x16x32_bf16 v[188:191],a[252:255],v[88:91],v[188:191]
v_exp_f32 v108,v108
v_mfma_f32_16x16x32_bf16 v[52:55],v[28:31],v[80:83],v[52:55]
ds_read_b128 a[168:171],v1 offset:8704
v_mfma_f32_16x16x32_bf16 v[52:55],v[28:31],v[88:91],v[52:55]
s_waitcnt lgkmcnt(3)
v_exp_f32 v109,v109
v_mfma_f32_16x16x32_bf16 v[64:67],a[128:131],v[128:131],v[32:35]
global_load_lds_dwordx4 v5,s[10:11] offset:2048 nt
ds_read_b128 a[172:175],v1 offset:8960
v_mfma_f32_16x16x32_bf16 v[64:67],a[132:135],v[136:139],v[64:67]
v_exp_f32 v110,v110
v_mfma_f32_16x16x32_bf16 v[64:67],a[136:139],v[144:147],v[64:67]
ds_read_b128 a[188:191],v1 offset:13056
v_cvt_pk_bf16_f32 v106,v108,v109
v_mfma_f32_16x16x32_bf16 v[64:67],a[140:143],v[152:155],v[64:67]
v_exp_f32 v111,v111
v_mfma_f32_16x16x32_bf16 v[68:71],a[156:159],v[152:155],v[32:35]
ds_read_b128 a[184:187],v1 offset:12800
v_cvt_pk_bf16_f32 v107,v110,v111
v_mfma_f32_16x16x32_bf16 v[68:71],a[152:155],v[144:147],v[68:71]
v_exp_f32 v112,v112
v_mfma_f32_16x16x32_bf16 v[68:71],a[148:151],v[136:139],v[68:71]
ds_read_b128 a[180:183],v1 offset:12544
v_mfma_f32_16x16x32_bf16 v[68:71],a[144:147],v[128:131],v[68:71]
s_waitcnt lgkmcnt(4)
v_exp_f32 v113,v113
v_mfma_f32_16x16x32_bf16 v[72:75],a[160:163],v[128:131],v[32:35]
ds_read_b128 a[176:179],v1 offset:12288
v_cvt_pk_bf16_f32 v112,v112,v113
v_mfma_f32_16x16x32_bf16 v[72:75],a[164:167],v[136:139],v[72:75]
v_exp_f32 v114,v114
v_mfma_f32_16x16x32_bf16 v[72:75],a[168:171],v[144:147],v[72:75]
s_waitcnt lgkmcnt(3)
v_exp_f32 v115,v115
v_mfma_f32_16x16x32_bf16 v[72:75],a[172:175],v[152:155],v[72:75]
v_exp_f32 v116,v116
v_mfma_f32_16x16x32_bf16 v[76:79],a[188:191],v[152:155],v[32:35]
s_waitcnt lgkmcnt(2)
v_exp_f32 v117,v117
v_mfma_f32_16x16x32_bf16 v[76:79],a[184:187],v[144:147],v[76:79]
s_waitcnt lgkmcnt(1)
v_exp_f32 v118,v118
v_mfma_f32_16x16x32_bf16 v[76:79],a[180:183],v[136:139],v[76:79]
s_waitcnt lgkmcnt(0)
v_exp_f32 v119,v119
v_mfma_f32_16x16x32_bf16 v[76:79],a[176:179],v[128:131],v[76:79]
v_cvt_pk_bf16_f32 v113,v114,v115
v_cvt_pk_bf16_f32 v114,v116,v117
v_mfma_f32_16x16x32_bf16 v[196:199],a[192:195],v[96:99],v[196:199]
global_load_lds_dwordx4 v5,s[10:11] offset:3072
v_cvt_pk_bf16_f32 v115,v118,v119
v_mfma_f32_16x16x32_bf16 v[196:199],a[196:199],v[104:107],v[196:199]
v_exp_f32 v120,v120
v_mfma_f32_16x16x32_bf16 v[204:207],a[200:203],v[96:99],v[204:207]
v_exp_f32 v121,v121
v_mfma_f32_16x16x32_bf16 v[204:207],a[204:207],v[104:107],v[204:207]
v_exp_f32 v122,v122
v_mfma_f32_16x16x32_bf16 v[212:215],a[208:211],v[96:99],v[212:215]
v_exp_f32 v123,v123
v_mfma_f32_16x16x32_bf16 v[212:215],a[212:215],v[104:107],v[212:215]
v_exp_f32 v124,v124
v_mfma_f32_16x16x32_bf16 v[220:223],a[216:219],v[96:99],v[220:223]
v_exp_f32 v125,v125
v_mfma_f32_16x16x32_bf16 v[220:223],a[220:223],v[104:107],v[220:223]
v_exp_f32 v126,v126
v_mfma_f32_16x16x32_bf16 v[228:231],a[224:227],v[96:99],v[228:231]
v_exp_f32 v127,v127
v_mfma_f32_16x16x32_bf16 v[228:231],a[228:231],v[104:107],v[228:231]
v_cvt_pk_bf16_f32 v120,v120,v121
v_cvt_pk_bf16_f32 v121,v122,v123
v_mfma_f32_16x16x32_bf16 v[236:239],a[232:235],v[96:99],v[236:239]
v_cvt_pk_bf16_f32 v122,v124,v125
v_cvt_pk_bf16_f32 v123,v126,v127
v_mfma_f32_16x16x32_bf16 v[236:239],a[236:239],v[104:107],v[236:239]
v_exp_f32 v64,v64
v_mfma_f32_16x16x32_bf16 v[244:247],a[240:243],v[96:99],v[244:247]
s_add_u32 s10,s24,s74
s_addc_u32 s11,s25,0
s_add_u32 s74,s74,16384
v_mfma_f32_16x16x32_bf16 v[244:247],a[244:247],v[104:107],v[244:247]
s_and_b32 s74,s74,0x3ffff
v_exp_f32 v65,v65
v_mfma_f32_16x16x32_bf16 v[252:255],a[248:251],v[96:99],v[252:255]
s_add_u32 m0,s76,16384
v_exp_f32 v66,v66
v_mfma_f32_16x16x32_bf16 v[252:255],a[252:255],v[104:107],v[252:255]
s_or_b32 s74,s74,s69
v_exp_f32 v67,v67
v_mfma_f32_16x16x32_bf16 v[56:59],v[28:31],v[96:99],v[56:59]
global_load_lds_dwordx4 v4,s[10:11]
v_cvt_pk_bf16_f32 v64,v64,v65
v_mfma_f32_16x16x32_bf16 v[56:59],v[28:31],v[104:107],v[56:59]
s_sub_u32 s76,s76,32768
v_exp_f32 v68,v68
v_mfma_f32_16x16x32_bf16 v[80:83],a[128:131],v[160:163],v[36:39]
s_and_b32 s76,s76,0x1ffff
v_exp_f32 v69,v69
v_mfma_f32_16x16x32_bf16 v[80:83],a[132:135],v[168:171],v[80:83]
v_exp_f32 v70,v70
v_mfma_f32_16x16x32_bf16 v[80:83],a[136:139],v[176:179],v[80:83]
v_exp_f32 v71,v71
v_mfma_f32_16x16x32_bf16 v[80:83],a[140:143],v[184:187],v[80:83]
v_cvt_pk_bf16_f32 v65,v66,v67
v_cvt_pk_bf16_f32 v66,v68,v69
v_mfma_f32_16x16x32_bf16 v[84:87],a[156:159],v[184:187],v[36:39]
v_cvt_pk_bf16_f32 v67,v70,v71
v_add_u32 v1,s78,v1
v_mfma_f32_16x16x32_bf16 v[84:87],a[152:155],v[176:179],v[84:87]
v_exp_f32 v72,v72
v_mfma_f32_16x16x32_bf16 v[84:87],a[148:151],v[168:171],v[84:87]
v_exp_f32 v73,v73
v_mfma_f32_16x16x32_bf16 v[84:87],a[144:147],v[160:163],v[84:87]
v_exp_f32 v74,v74
v_mfma_f32_16x16x32_bf16 v[88:91],a[160:163],v[160:163],v[36:39]
v_exp_f32 v75,v75
v_mfma_f32_16x16x32_bf16 v[88:91],a[164:167],v[168:171],v[88:91]
v_exp_f32 v76,v76
v_mfma_f32_16x16x32_bf16 v[88:91],a[168:171],v[176:179],v[88:91]
v_exp_f32 v77,v77
v_mfma_f32_16x16x32_bf16 v[88:91],a[172:175],v[184:187],v[88:91]
v_exp_f32 v78,v78
v_mfma_f32_16x16x32_bf16 v[92:95],a[188:191],v[184:187],v[36:39]
v_exp_f32 v79,v79
v_mfma_f32_16x16x32_bf16 v[92:95],a[184:187],v[176:179],v[92:95]
v_cvt_pk_bf16_f32 v72,v72,v73
v_cvt_pk_bf16_f32 v73,v74,v75
v_mfma_f32_16x16x32_bf16 v[92:95],a[180:183],v[168:171],v[92:95]
global_load_lds_dwordx4 v5,s[10:11] offset:1024
v_cvt_pk_bf16_f32 v74,v76,v77
v_mfma_f32_16x16x32_bf16 v[92:95],a[176:179],v[160:163],v[92:95]
v_exp_f32 v80,v80
v_mfma_f32_16x16x32_bf16 a[32:35],a[192:195],v[112:115],a[32:35]
ds_read_b64_tr_b16 a[192:193],v2
ds_read_b64_tr_b16 a[194:195],v2 offset:4096
v_mfma_f32_16x16x32_bf16 a[32:35],a[196:199],v[120:123],a[32:35]
ds_read_b64_tr_b16 a[196:197],v2 offset:8192
v_cvt_pk_bf16_f32 v75,v78,v79
v_mfma_f32_16x16x32_bf16 a[36:39],a[200:203],v[112:115],a[36:39]
v_exp_f32 v81,v81
v_mfma_f32_16x16x32_bf16 a[36:39],a[204:207],v[120:123],a[36:39]
ds_read_b64_tr_b16 a[198:199],v2 offset:12288
ds_read_b64_tr_b16 a[200:201],v3
v_mfma_f32_16x16x32_bf16 a[40:43],a[208:211],v[112:115],a[40:43]
ds_read_b64_tr_b16 a[202:203],v3 offset:4096
v_cvt_pk_bf16_f32 v80,v80,v81
v_mfma_f32_16x16x32_bf16 a[40:43],a[212:215],v[120:123],a[40:43]
v_exp_f32 v82,v82
v_mfma_f32_16x16x32_bf16 a[44:47],a[216:219],v[112:115],a[44:47]
ds_read_b64_tr_b16 a[204:205],v3 offset:8192
ds_read_b64_tr_b16 a[206:207],v3 offset:12288
v_mfma_f32_16x16x32_bf16 a[44:47],a[220:223],v[120:123],a[44:47]
v_exp_f32 v83,v83
v_mfma_f32_16x16x32_bf16 a[48:51],a[224:227],v[112:115],a[48:51]
ds_read_b64_tr_b16 a[208:209],v2 offset:256
ds_read_b64_tr_b16 a[210:211],v2 offset:4352
v_mfma_f32_16x16x32_bf16 a[48:51],a[228:231],v[120:123],a[48:51]
ds_read_b64_tr_b16 a[212:213],v2 offset:8448
v_cvt_pk_bf16_f32 v81,v82,v83
v_mfma_f32_16x16x32_bf16 a[52:55],a[232:235],v[112:115],a[52:55]
v_exp_f32 v84,v84
v_mfma_f32_16x16x32_bf16 a[52:55],a[236:239],v[120:123],a[52:55]
ds_read_b64_tr_b16 a[214:215],v2 offset:12544
ds_read_b64_tr_b16 a[216:217],v3 offset:256
v_mfma_f32_16x16x32_bf16 a[56:59],a[240:243],v[112:115],a[56:59]
v_exp_f32 v85,v85
v_mfma_f32_16x16x32_bf16 a[56:59],a[244:247],v[120:123],a[56:59]
ds_read_b64_tr_b16 a[218:219],v3 offset:4352
ds_read_b64_tr_b16 a[220:221],v3 offset:8448
v_mfma_f32_16x16x32_bf16 a[60:63],a[248:251],v[112:115],a[60:63]
global_load_lds_dwordx4 v4,s[10:11] offset:2048
ds_read_b64_tr_b16 a[222:223],v3 offset:12544
v_mfma_f32_16x16x32_bf16 a[60:63],a[252:255],v[120:123],a[60:63]
ds_read_b64_tr_b16 a[224:225],v2 offset:512
)A"
R"A(v_cvt_pk_bf16_f32 v82,v84,v85
v_mfma_f32_16x16x32_bf16 v[60:63],v[28:31],v[112:115],v[60:63]
v_exp_f32 v86,v86
v_mfma_f32_16x16x32_bf16 v[60:63],v[28:31],v[120:123],v[60:63]
ds_read_b64_tr_b16 a[226:227],v2 offset:4608
ds_read_b64_tr_b16 a[228:229],v2 offset:8704
v_mfma_f32_16x16x32_bf16 v[96:99],a[128:131],v[192:195],v[40:43]
v_exp_f32 v87,v87
v_mfma_f32_16x16x32_bf16 v[96:99],a[132:135],v[200:203],v[96:99]
ds_read_b64_tr_b16 a[230:231],v2 offset:12800
ds_read_b64_tr_b16 a[232:233],v3 offset:512
v_mfma_f32_16x16x32_bf16 v[96:99],a[136:139],v[208:211],v[96:99]
ds_read_b64_tr_b16 a[234:235],v3 offset:4608
v_cvt_pk_bf16_f32 v83,v86,v87
v_mfma_f32_16x16x32_bf16 v[96:99],a[140:143],v[216:219],v[96:99]
v_exp_f32 v88,v88
v_mfma_f32_16x16x32_bf16 v[100:103],a[156:159],v[216:219],v[40:43]
ds_read_b64_tr_b16 a[236:237],v3 offset:8704
ds_read_b64_tr_b16 a[238:239],v3 offset:12800
v_mfma_f32_16x16x32_bf16 v[100:103],a[152:155],v[208:211],v[100:103]
v_exp_f32 v89,v89
v_mfma_f32_16x16x32_bf16 v[100:103],a[148:151],v[200:203],v[100:103]
ds_read_b64_tr_b16 a[240:241],v2 offset:768
ds_read_b64_tr_b16 a[242:243],v2 offset:4864
v_mfma_f32_16x16x32_bf16 v[100:103],a[144:147],v[192:195],v[100:103]
ds_read_b64_tr_b16 a[244:245],v2 offset:8960
v_cvt_pk_bf16_f32 v88,v88,v89
v_mfma_f32_16x16x32_bf16 v[104:107],a[160:163],v[192:195],v[40:43]
ds_read_b64_tr_b16 a[246:247],v2 offset:13056
v_mfma_f32_16x16x32_bf16 v[104:107],a[164:167],v[200:203],v[104:107]
ds_read_b64_tr_b16 a[248:249],v3 offset:768
v_mfma_f32_16x16x32_bf16 v[104:107],a[168:171],v[208:211],v[104:107]
ds_read_b64_tr_b16 a[250:251],v3 offset:4864
ds_read_b64_tr_b16 a[252:253],v3 offset:8960
v_mfma_f32_16x16x32_bf16 v[104:107],a[172:175],v[216:219],v[104:107]
ds_read_b64_tr_b16 a[254:255],v3 offset:13056
v_mfma_f32_16x16x32_bf16 v[108:111],a[188:191],v[216:219],v[40:43]
global_load_lds_dwordx4 v5,s[10:11] offset:3072
v_mfma_f32_16x16x32_bf16 v[108:111],a[184:187],v[208:211],v[108:111]
v_mfma_f32_16x16x32_bf16 v[108:111],a[180:183],v[200:203],v[108:111]
v_mfma_f32_16x16x32_bf16 v[108:111],a[176:179],v[192:195],v[108:111]
s_waitcnt lgkmcnt(5)
v_mfma_f32_16x16x32_bf16 a[0:3],a[192:195],v[64:67],a[0:3]
v_mfma_f32_16x16x32_bf16 a[0:3],a[196:199],v[72:75],a[0:3]
v_mfma_f32_16x16x32_bf16 a[4:7],a[200:203],v[64:67],a[4:7]
v_mfma_f32_16x16x32_bf16 a[4:7],a[204:207],v[72:75],a[4:7]
v_mfma_f32_16x16x32_bf16 a[8:11],a[208:211],v[64:67],a[8:11]
v_mfma_f32_16x16x32_bf16 a[8:11],a[212:215],v[72:75],a[8:11]
v_mfma_f32_16x16x32_bf16 a[12:15],a[216:219],v[64:67],a[12:15]
v_mfma_f32_16x16x32_bf16 a[12:15],a[220:223],v[72:75],a[12:15]
v_mfma_f32_16x16x32_bf16 a[16:19],a[224:227],v[64:67],a[16:19]
v_mfma_f32_16x16x32_bf16 a[16:19],a[228:231],v[72:75],a[16:19]
v_mfma_f32_16x16x32_bf16 a[20:23],a[232:235],v[64:67],a[20:23]
s_getreg_b32 s92,hwreg(HW_REG_IB_STS,0,4)
s_min_u32 s4,s92,s6
s_mov_b32 s6,s92
v_mfma_f32_16x16x32_bf16 a[20:23],a[236:239],v[72:75],a[20:23]
s_cmp_ge_u32 s4,s9
s_cselect_b32 s4,15,s5
v_mfma_f32_16x16x32_bf16 a[24:27],a[240:243],v[64:67],a[24:27]
s_waitcnt lgkmcnt(0)
v_mfma_f32_16x16x32_bf16 a[24:27],a[244:247],v[72:75],a[24:27]
v_mfma_f32_16x16x32_bf16 a[28:31],a[248:251],v[64:67],a[28:31]
v_mfma_f32_16x16x32_bf16 a[28:31],a[252:255],v[72:75],a[28:31]
s_getpc_b64 s[92:93]
s_cmp_lg_u32 s4,0
s_cbranch_scc1 L_pace_%=
v_mfma_f32_16x16x32_bf16 v[48:51],v[28:31],v[64:67],v[48:51]
v_mfma_f32_16x16x32_bf16 v[48:51],v[28:31],v[72:75],v[48:51]
s_sub_u32 s70,s70,1
s_cmp_lg_u32 s70,0
s_cbranch_scc1 L_a1_%=
s_waitcnt vmcnt(16)
s_barrier
v_mfma_f32_16x16x32_bf16 v[112:115],a[128:131],v[224:227],v[44:47]
s_mul_i32 s86,s60,10240
s_mul_i32 s85,s63,1310720
s_mul_i32 s66,s64,320
v_mfma_f32_16x16x32_bf16 v[112:115],a[132:135],v[232:235],v[112:115]
s_add_u32 s85,s85,s66
s_add_u32 s83,s64,0
s_and_b32 s83,s83,3
v_mfma_f32_16x16x32_bf16 v[112:115],a[136:139],v[240:243],v[112:115]
s_lshl_b32 s83,s83,1
s_mul_i32 s83,s83,163840
s_add_u32 s83,s83,s85
v_mfma_f32_16x16x32_bf16 v[112:115],a[140:143],v[248:251],v[112:115]
s_cmp_eq_u32 s60,3
s_cselect_b32 s69,0x40000000,0
v_add_u32 v14,s69,v6
v_mfma_f32_16x16x32_bf16 v[116:119],a[156:159],v[248:251],v[44:47]
s_add_u32 s84,s83,20480
v_exp_f32 v90,v90
v_mfma_f32_16x16x32_bf16 v[116:119],a[152:155],v[240:243],v[116:119]
s_add_u32 m0,s86,122880
v_exp_f32 v91,v91
v_mfma_f32_16x16x32_bf16 v[116:119],a[148:151],v[232:235],v[116:119]
s_getpc_b64 s[8:9]
L_pcg_%=:
v_exp_f32 v92,v92
v_mfma_f32_16x16x32_bf16 v[116:119],a[144:147],v[224:227],v[116:119]
buffer_load_dwordx4 v24,s[48:51],s83 offen lds
v_mfma_f32_16x16x32_bf16 v[120:123],a[160:163],v[224:227],v[44:47]
s_mov_b32 s6,L_gbF_%=-L_pcg_%=
v_exp_f32 v93,v93
v_mfma_f32_16x16x32_bf16 v[120:123],a[164:167],v[232:235],v[120:123]
buffer_store_dwordx4 a[112:115],v14,s[40:43],s81 offen offset:3072 sc1
v_mfma_f32_16x16x32_bf16 v[120:123],a[168:171],v[240:243],v[120:123]
s_mov_b32 s10,L_geF_%=-L_gbF_%=
v_exp_f32 v94,v94
v_mfma_f32_16x16x32_bf16 v[120:123],a[172:175],v[248:251],v[120:123]
s_mov_b32 s11,0x20000
v_exp_f32 v95,v95
v_mfma_f32_16x16x32_bf16 v[124:127],a[188:191],v[248:251],v[44:47]
s_lshr_b32 s7,%1,3
v_cvt_pk_bf16_f32 v89,v90,v91
v_cvt_pk_bf16_f32 v90,v92,v93
v_mfma_f32_16x16x32_bf16 v[124:127],a[184:187],v[240:243],v[124:127]
s_lshl_b32 s7,s7,2
ds_read_b128 a[128:131],v1
v_cvt_pk_bf16_f32 v91,v94,v95
v_mfma_f32_16x16x32_bf16 v[124:127],a[180:183],v[232:235],v[124:127]
s_add_u32 s7,s7,s60
v_exp_f32 v96,v96
v_mfma_f32_16x16x32_bf16 v[124:127],a[176:179],v[224:227],v[124:127]
s_lshl_b32 s7,s7,7
ds_read_b128 a[132:135],v1 offset:256
v_add_u32 v2,s78,v2
v_mfma_f32_16x16x32_bf16 v[132:135],a[192:195],v[80:83],v[132:135]
s_lshl_b32 s66,s61,5
v_exp_f32 v97,v97
v_mfma_f32_16x16x32_bf16 v[132:135],a[196:199],v[88:91],v[132:135]
buffer_load_dwordx4 v25,s[48:51],s83 offen offset:1024 lds
v_mfma_f32_16x16x32_bf16 v[140:143],a[200:203],v[80:83],v[140:143]
s_lshl_b32 s67,s63,3
ds_read_b128 a[136:139],v1 offset:512
v_cvt_pk_bf16_f32 v96,v96,v97
v_mfma_f32_16x16x32_bf16 v[140:143],a[204:207],v[88:91],v[140:143]
s_add_u32 s66,s66,s67
v_exp_f32 v98,v98
v_mfma_f32_16x16x32_bf16 v[148:151],a[208:211],v[80:83],v[148:151]
s_lshl_b32 s66,s66,16
ds_read_b128 a[140:143],v1 offset:768
v_add_u32 v3,s78,v3
v_mfma_f32_16x16x32_bf16 v[148:151],a[212:215],v[88:91],v[148:151]
s_sub_u32 s78,s77,32768
v_exp_f32 v99,v99
v_mfma_f32_16x16x32_bf16 v[156:159],a[216:219],v[80:83],v[156:159]
s_and_b32 s78,s78,0x1ffff
ds_read_b128 a[156:159],v1 offset:4864
v_cvt_pk_bf16_f32 v97,v98,v99
v_mfma_f32_16x16x32_bf16 v[156:159],a[220:223],v[88:91],v[156:159]
s_sub_u32 s78,s78,s77
v_exp_f32 v100,v100
v_mfma_f32_16x16x32_bf16 v[164:167],a[224:227],v[80:83],v[164:167]
s_add_u32 s77,s77,s78
ds_read_b128 a[152:155],v1 offset:4608
v_lshlrev_b32 v4,10,v6
v_mfma_f32_16x16x32_bf16 v[164:167],a[228:231],v[88:91],v[164:167]
s_lshl_b32 s67,s60,14
v_exp_f32 v101,v101
v_mfma_f32_16x16x32_bf16 v[172:175],a[232:235],v[80:83],v[172:175]
buffer_store_dwordx4 a[116:119],v6,s[40:43],s81 offen offset:1024 sc1
v_mfma_f32_16x16x32_bf16 v[172:175],a[236:239],v[88:91],v[172:175]
s_add_u32 s66,s66,s67
ds_read_b128 a[148:151],v1 offset:4352
v_cvt_pk_bf16_f32 v98,v100,v101
v_mfma_f32_16x16x32_bf16 v[180:183],a[240:243],v[80:83],v[180:183]
buffer_load_dwordx4 v26,s[48:51],s83 offen offset:2048 lds
v_mfma_f32_16x16x32_bf16 v[180:183],a[244:247],v[88:91],v[180:183]
s_mov_b32 s30,1523712
ds_read_b128 a[144:147],v1 offset:4096
v_mov_b32 v8,s52
v_mfma_f32_16x16x32_bf16 v[188:191],a[248:251],v[80:83],v[188:191]
s_mov_b32 s31,0x20000
v_exp_f32 v102,v102
v_mfma_f32_16x16x32_bf16 v[188:191],a[252:255],v[88:91],v[188:191]
ds_read_b128 a[160:163],v1 offset:8192
v_mov_b32 v9,s53
v_mfma_f32_16x16x32_bf16 v[52:55],v[28:31],v[80:83],v[52:55]
v_exp_f32 v103,v103
v_mfma_f32_16x16x32_bf16 v[52:55],v[28:31],v[88:91],v[52:55]
s_waitcnt lgkmcnt(3)
ds_read_b128 a[164:167],v1 offset:8448
v_cvt_pk_bf16_f32 v99,v102,v103
v_mfma_f32_16x16x32_bf16 v[64:67],a[128:131],v[128:131],v[32:35]
v_exp_f32 v104,v104
v_mfma_f32_16x16x32_bf16 v[64:67],a[132:135],v[136:139],v[64:67]
ds_read_b128 a[168:171],v1 offset:8704
v_lshlrev_b32 v10,20,v6
v_mfma_f32_16x16x32_bf16 v[64:67],a[136:139],v[144:147],v[64:67]
v_exp_f32 v105,v105
v_mfma_f32_16x16x32_bf16 v[64:67],a[140:143],v[152:155],v[64:67]
buffer_load_dwordx4 v27,s[48:51],s83 offen offset:3072 lds
v_mfma_f32_16x16x32_bf16 v[68:71],a[156:159],v[152:155],v[32:35]
ds_read_b128 a[172:175],v1 offset:8960
v_cvt_pk_bf16_f32 v104,v104,v105
v_mfma_f32_16x16x32_bf16 v[68:71],a[152:155],v[144:147],v[68:71]
s_waitcnt lgkmcnt(3)
v_exp_f32 v106,v106
v_mfma_f32_16x16x32_bf16 v[68:71],a[148:151],v[136:139],v[68:71]
s_add_u32 s28,s12,s66
s_addc_u32 s29,s13,0
ds_read_b128 a[188:191],v1 offset:13056
v_mfma_f32_16x16x32_bf16 v[68:71],a[144:147],v[128:131],v[68:71]
s_and_b32 s29,s29,0xffff
v_exp_f32 v107,v107
v_mfma_f32_16x16x32_bf16 v[72:75],a[160:163],v[128:131],v[32:35]
s_waitcnt lgkmcnt(2)
ds_read_b128 a[184:187],v1 offset:12800
v_cvt_pk_bf16_f32 v105,v106,v107
v_mfma_f32_16x16x32_bf16 v[72:75],a[164:167],v[136:139],v[72:75]
buffer_store_dwordx4 a[120:123],v14,s[40:43],s82 offen offset:3072 sc1
v_mfma_f32_16x16x32_bf16 v[72:75],a[168:171],v[144:147],v[72:75]
s_waitcnt lgkmcnt(1)
ds_read_b128 a[180:183],v1 offset:12544
v_mfma_f32_16x16x32_bf16 v[72:75],a[172:175],v[152:155],v[72:75]
s_add_u32 m0,s86,126976
ds_read_b128 a[176:179],v1 offset:12288
v_add_u32 v1,s78,v1
v_mfma_f32_16x16x32_bf16 v[76:79],a[188:191],v[152:155],v[32:35]
s_waitcnt lgkmcnt(0)
v_exp_f32 v108,v108
v_mfma_f32_16x16x32_bf16 v[76:79],a[184:187],v[144:147],v[76:79]
buffer_load_dwordx4 v7,s[48:51],s83 offen lds
v_exp_f32 v109,v109
v_exp_f32 v110,v110
v_exp_f32 v111,v111
v_mfma_f32_16x16x32_bf16 v[76:79],a[180:183],v[136:139],v[76:79]
s_add_u32 s83,s64,0
v_cvt_pk_bf16_f32 v106,v108,v109
v_cvt_pk_bf16_f32 v107,v110,v111
v_mfma_f32_16x16x32_bf16 v[76:79],a[176:179],v[128:131],v[76:79]
s_and_b32 s83,s83,3
v_exp_f32 v112,v112
v_mfma_f32_16x16x32_bf16 v[196:199],a[192:195],v[96:99],v[196:199]
s_lshl_b32 s83,s83,1
v_exp_f32 v113,v113
v_mfma_f32_16x16x32_bf16 v[196:199],a[196:199],v[104:107],v[196:199]
s_or_b32 s83,s83,1
v_exp_f32 v114,v114
v_mfma_f32_16x16x32_bf16 v[204:207],a[200:203],v[96:99],v[204:207]
s_mul_i32 s83,s83,163840
v_exp_f32 v115,v115
v_mfma_f32_16x16x32_bf16 v[204:207],a[204:207],v[104:107],v[204:207]
s_add_u32 s83,s83,s85
v_exp_f32 v116,v116
v_mfma_f32_16x16x32_bf16 v[212:215],a[208:211],v[96:99],v[212:215]
v_exp_f32 v117,v117
v_mfma_f32_16x16x32_bf16 v[212:215],a[212:215],v[104:107],v[212:215]
s_add_u32 m0,s86,128000
v_exp_f32 v118,v118
v_mfma_f32_16x16x32_bf16 v[220:223],a[216:219],v[96:99],v[220:223]
v_exp_f32 v119,v119
v_mfma_f32_16x16x32_bf16 v[220:223],a[220:223],v[104:107],v[220:223]
buffer_load_dwordx4 v24,s[48:51],s84 offen lds
v_mfma_f32_16x16x32_bf16 v[228:231],a[224:227],v[96:99],v[228:231]
v_cvt_pk_bf16_f32 v112,v112,v113
v_cvt_pk_bf16_f32 v113,v114,v115
s_nop 0
L_pd11_%=:
s_cbranch_execz L_t1c_%=
v_mfma_f32_16x16x32_bf16 v[228:231],a[228:231],v[104:107],v[228:231]
v_cvt_pk_bf16_f32 v114,v116,v117
v_cvt_pk_bf16_f32 v115,v118,v119
v_mfma_f32_16x16x32_bf16 v[236:239],a[232:235],v[96:99],v[236:239]
buffer_store_dwordx4 a[124:127],v6,s[40:43],s82 offen offset:1024 sc1
v_mfma_f32_16x16x32_bf16 v[236:239],a[236:239],v[104:107],v[236:239]
v_exp_f32 v120,v120
v_mfma_f32_16x16x32_bf16 v[244:247],a[240:243],v[96:99],v[244:247]
v_exp_f32 v121,v121
v_mfma_f32_16x16x32_bf16 v[244:247],a[244:247],v[104:107],v[244:247]
v_exp_f32 v122,v122
v_mfma_f32_16x16x32_bf16 v[252:255],a[248:251],v[96:99],v[252:255]
v_exp_f32 v123,v123
v_mfma_f32_16x16x32_bf16 v[252:255],a[252:255],v[104:107],v[252:255]
v_exp_f32 v124,v124
v_mfma_f32_16x16x32_bf16 v[56:59],v[28:31],v[96:99],v[56:59]
v_exp_f32 v125,v125
v_mfma_f32_16x16x32_bf16 v[56:59],v[28:31],v[104:107],v[56:59]
buffer_load_dwordx4 v25,s[48:51],s84 offen offset:1024 lds
v_mfma_f32_16x16x32_bf16 v[80:83],a[128:131],v[160:163],v[36:39]
v_exp_f32 v126,v126
v_mfma_f32_16x16x32_bf16 v[80:83],a[132:135],v[168:171],v[80:83]
v_exp_f32 v127,v127
v_mfma_f32_16x16x32_bf16 v[80:83],a[136:139],v[176:179],v[80:83]
)A"
R"A(v_cvt_pk_bf16_f32 v120,v120,v121
v_cvt_pk_bf16_f32 v121,v122,v123
v_mfma_f32_16x16x32_bf16 v[80:83],a[140:143],v[184:187],v[80:83]
v_cvt_pk_bf16_f32 v122,v124,v125
v_cvt_pk_bf16_f32 v123,v126,v127
v_mfma_f32_16x16x32_bf16 v[84:87],a[156:159],v[184:187],v[36:39]
v_exp_f32 v64,v64
v_mfma_f32_16x16x32_bf16 v[84:87],a[152:155],v[176:179],v[84:87]
v_exp_f32 v65,v65
v_mfma_f32_16x16x32_bf16 v[84:87],a[148:151],v[168:171],v[84:87]
v_exp_f32 v66,v66
v_mfma_f32_16x16x32_bf16 v[84:87],a[144:147],v[160:163],v[84:87]
v_exp_f32 v67,v67
v_mfma_f32_16x16x32_bf16 v[88:91],a[160:163],v[160:163],v[36:39]
v_exp_f32 v68,v68
v_mfma_f32_16x16x32_bf16 v[88:91],a[164:167],v[168:171],v[88:91]
buffer_store_dwordx4 a[96:99],v14,s[40:43],s79 offen offset:3072 sc1
v_mfma_f32_16x16x32_bf16 v[88:91],a[168:171],v[176:179],v[88:91]
v_exp_f32 v69,v69
v_mfma_f32_16x16x32_bf16 v[88:91],a[172:175],v[184:187],v[88:91]
buffer_load_dwordx4 v26,s[48:51],s84 offen offset:2048 lds
v_mfma_f32_16x16x32_bf16 v[92:95],a[188:191],v[184:187],v[36:39]
v_exp_f32 v70,v70
v_mfma_f32_16x16x32_bf16 v[92:95],a[184:187],v[176:179],v[92:95]
v_exp_f32 v71,v71
v_mfma_f32_16x16x32_bf16 v[92:95],a[180:183],v[168:171],v[92:95]
v_cvt_pk_bf16_f32 v64,v64,v65
v_cvt_pk_bf16_f32 v65,v66,v67
v_mfma_f32_16x16x32_bf16 v[92:95],a[176:179],v[160:163],v[92:95]
v_cvt_pk_bf16_f32 v66,v68,v69
v_cvt_pk_bf16_f32 v67,v70,v71
v_mfma_f32_16x16x32_bf16 a[32:35],a[192:195],v[112:115],a[32:35]
ds_read_b64_tr_b16 a[192:193],v2
ds_read_b64_tr_b16 a[194:195],v2 offset:4096
v_mfma_f32_16x16x32_bf16 a[32:35],a[196:199],v[120:123],a[32:35]
v_exp_f32 v72,v72
v_mfma_f32_16x16x32_bf16 a[36:39],a[200:203],v[112:115],a[36:39]
ds_read_b64_tr_b16 a[196:197],v2 offset:8192
ds_read_b64_tr_b16 a[198:199],v2 offset:12288
v_mfma_f32_16x16x32_bf16 a[36:39],a[204:207],v[120:123],a[36:39]
buffer_load_dwordx4 v27,s[48:51],s84 offen offset:3072 lds
v_mfma_f32_16x16x32_bf16 a[40:43],a[208:211],v[112:115],a[40:43]
ds_read_b64_tr_b16 a[200:201],v3
ds_read_b64_tr_b16 a[202:203],v3 offset:4096
v_mfma_f32_16x16x32_bf16 a[40:43],a[212:215],v[120:123],a[40:43]
v_exp_f32 v73,v73
v_mfma_f32_16x16x32_bf16 a[44:47],a[216:219],v[112:115],a[44:47]
ds_read_b64_tr_b16 a[204:205],v3 offset:8192
ds_read_b64_tr_b16 a[206:207],v3 offset:12288
v_mfma_f32_16x16x32_bf16 a[44:47],a[220:223],v[120:123],a[44:47]
ds_read_b64_tr_b16 a[208:209],v2 offset:256
v_cvt_pk_bf16_f32 v72,v72,v73
v_mfma_f32_16x16x32_bf16 a[48:51],a[224:227],v[112:115],a[48:51]
v_exp_f32 v74,v74
v_mfma_f32_16x16x32_bf16 a[48:51],a[228:231],v[120:123],a[48:51]
ds_read_b64_tr_b16 a[210:211],v2 offset:4352
ds_read_b64_tr_b16 a[212:213],v2 offset:8448
v_mfma_f32_16x16x32_bf16 a[52:55],a[232:235],v[112:115],a[52:55]
buffer_store_dwordx4 a[100:103],v6,s[40:43],s79 offen offset:1024 sc1
v_mfma_f32_16x16x32_bf16 a[52:55],a[236:239],v[120:123],a[52:55]
s_add_u32 m0,s86,132096
ds_read_b64_tr_b16 a[214:215],v2 offset:12544
ds_read_b64_tr_b16 a[216:217],v3 offset:256
v_mfma_f32_16x16x32_bf16 a[56:59],a[240:243],v[112:115],a[56:59]
v_exp_f32 v75,v75
v_mfma_f32_16x16x32_bf16 a[56:59],a[244:247],v[120:123],a[56:59]
buffer_load_dwordx4 v7,s[48:51],s84 offen lds
v_mfma_f32_16x16x32_bf16 a[60:63],a[248:251],v[112:115],a[60:63]
s_add_u32 s84,s83,20480
ds_read_b64_tr_b16 a[218:219],v3 offset:4352
ds_read_b64_tr_b16 a[220:221],v3 offset:8448
v_mfma_f32_16x16x32_bf16 a[60:63],a[252:255],v[120:123],a[60:63]
v_exp_f32 v76,v76
v_mfma_f32_16x16x32_bf16 v[60:63],v[28:31],v[112:115],v[60:63]
v_exp_f32 v77,v77
v_mfma_f32_16x16x32_bf16 v[60:63],v[28:31],v[120:123],v[60:63]
v_exp_f32 v78,v78
v_mfma_f32_16x16x32_bf16 v[96:99],a[128:131],v[192:195],v[40:43]
v_exp_f32 v79,v79
v_mfma_f32_16x16x32_bf16 v[96:99],a[132:135],v[200:203],v[96:99]
ds_read_b64_tr_b16 a[222:223],v3 offset:12544
v_cvt_pk_bf16_f32 v73,v74,v75
v_mfma_f32_16x16x32_bf16 v[96:99],a[136:139],v[208:211],v[96:99]
v_cvt_pk_bf16_f32 v74,v76,v77
v_cvt_pk_bf16_f32 v75,v78,v79
v_mfma_f32_16x16x32_bf16 v[96:99],a[140:143],v[216:219],v[96:99]
ds_read_b64_tr_b16 a[224:225],v2 offset:512
ds_read_b64_tr_b16 a[226:227],v2 offset:4608
v_mfma_f32_16x16x32_bf16 v[100:103],a[156:159],v[216:219],v[40:43]
v_exp_f32 v80,v80
v_mfma_f32_16x16x32_bf16 v[100:103],a[152:155],v[208:211],v[100:103]
ds_read_b64_tr_b16 a[228:229],v2 offset:8704
ds_read_b64_tr_b16 a[230:231],v2 offset:12800
v_mfma_f32_16x16x32_bf16 v[100:103],a[148:151],v[200:203],v[100:103]
v_exp_f32 v81,v81
v_mfma_f32_16x16x32_bf16 v[100:103],a[144:147],v[192:195],v[100:103]
ds_read_b64_tr_b16 a[232:233],v3 offset:512
ds_read_b64_tr_b16 a[234:235],v3 offset:4608
v_mfma_f32_16x16x32_bf16 v[104:107],a[160:163],v[192:195],v[40:43]
ds_read_b64_tr_b16 a[236:237],v3 offset:8704
v_cvt_pk_bf16_f32 v80,v80,v81
v_mfma_f32_16x16x32_bf16 v[104:107],a[164:167],v[200:203],v[104:107]
buffer_store_dwordx4 a[104:107],v14,s[40:43],s80 offen offset:3072 sc1
v_mfma_f32_16x16x32_bf16 v[104:107],a[168:171],v[208:211],v[104:107]
ds_read_b64_tr_b16 a[238:239],v3 offset:12800
ds_read_b64_tr_b16 a[240:241],v2 offset:768
v_mfma_f32_16x16x32_bf16 v[104:107],a[172:175],v[216:219],v[104:107]
v_exp_f32 v82,v82
v_mfma_f32_16x16x32_bf16 v[108:111],a[188:191],v[216:219],v[40:43]
ds_read_b64_tr_b16 a[242:243],v2 offset:4864
ds_read_b64_tr_b16 a[244:245],v2 offset:8960
v_mfma_f32_16x16x32_bf16 v[108:111],a[184:187],v[208:211],v[108:111]
ds_read_b64_tr_b16 a[246:247],v2 offset:13056
v_add_u32 v2,s78,v2
v_mfma_f32_16x16x32_bf16 v[108:111],a[180:183],v[200:203],v[108:111]
v_exp_f32 v83,v83
v_mfma_f32_16x16x32_bf16 v[108:111],a[176:179],v[192:195],v[108:111]
s_waitcnt lgkmcnt(6)
ds_read_b64_tr_b16 a[248:249],v3 offset:768
ds_read_b64_tr_b16 a[250:251],v3 offset:4864
v_mfma_f32_16x16x32_bf16 a[0:3],a[192:195],v[64:67],a[0:3]
ds_read_b64_tr_b16 a[252:253],v3 offset:8960
v_cvt_pk_bf16_f32 v81,v82,v83
v_mfma_f32_16x16x32_bf16 a[0:3],a[196:199],v[72:75],a[0:3]
ds_read_b64_tr_b16 a[254:255],v3 offset:13056
v_add_u32 v3,s78,v3
v_mfma_f32_16x16x32_bf16 a[4:7],a[200:203],v[64:67],a[4:7]
s_sub_u32 s78,s77,32768
v_exp_f32 v84,v84
v_mfma_f32_16x16x32_bf16 a[4:7],a[204:207],v[72:75],a[4:7]
s_and_b32 s78,s78,0x1ffff
v_exp_f32 v85,v85
v_mfma_f32_16x16x32_bf16 a[8:11],a[208:211],v[64:67],a[8:11]
s_sub_u32 s78,s78,s77
v_exp_f32 v86,v86
v_mfma_f32_16x16x32_bf16 a[8:11],a[212:215],v[72:75],a[8:11]
s_add_u32 s77,s77,s78
v_exp_f32 v87,v87
v_mfma_f32_16x16x32_bf16 a[12:15],a[216:219],v[64:67],a[12:15]
v_cvt_pk_bf16_f32 v82,v84,v85
v_cvt_pk_bf16_f32 v83,v86,v87
v_mfma_f32_16x16x32_bf16 a[12:15],a[220:223],v[72:75],a[12:15]
v_exp_f32 v88,v88
v_mfma_f32_16x16x32_bf16 a[16:19],a[224:227],v[64:67],a[16:19]
v_exp_f32 v89,v89
v_mfma_f32_16x16x32_bf16 a[16:19],a[228:231],v[72:75],a[16:19]
s_waitcnt lgkmcnt(0)
v_exp_f32 v90,v90
v_mfma_f32_16x16x32_bf16 a[20:23],a[232:235],v[64:67],a[20:23]
buffer_store_dwordx4 a[108:111],v6,s[40:43],s80 offen offset:1024 sc1
v_mfma_f32_16x16x32_bf16 a[20:23],a[236:239],v[72:75],a[20:23]
v_exp_f32 v91,v91
v_mfma_f32_16x16x32_bf16 a[24:27],a[240:243],v[64:67],a[24:27]
v_exp_f32 v92,v92
v_mfma_f32_16x16x32_bf16 a[24:27],a[244:247],v[72:75],a[24:27]
v_exp_f32 v93,v93
v_mfma_f32_16x16x32_bf16 a[28:31],a[248:251],v[64:67],a[28:31]
v_exp_f32 v94,v94
v_mfma_f32_16x16x32_bf16 a[28:31],a[252:255],v[72:75],a[28:31]
v_exp_f32 v95,v95
v_mfma_f32_16x16x32_bf16 v[48:51],v[28:31],v[64:67],v[48:51]
v_cvt_pk_bf16_f32 v88,v88,v89
v_cvt_pk_bf16_f32 v89,v90,v91
v_mfma_f32_16x16x32_bf16 v[48:51],v[28:31],v[72:75],v[48:51]
v_cvt_pk_bf16_f32 v90,v92,v93
v_cvt_pk_bf16_f32 v91,v94,v95
s_waitcnt vmcnt(26)
s_barrier
v_mfma_f32_16x16x32_bf16 v[112:115],a[128:131],v[224:227],v[44:47]
v_exp_f32 v96,v96
v_mfma_f32_16x16x32_bf16 v[112:115],a[132:135],v[232:235],v[112:115]
v_exp_f32 v97,v97
v_mfma_f32_16x16x32_bf16 v[112:115],a[136:139],v[240:243],v[112:115]
ds_read_b128 a[128:131],v1
v_cvt_pk_bf16_f32 v96,v96,v97
v_mfma_f32_16x16x32_bf16 v[112:115],a[140:143],v[248:251],v[112:115]
v_exp_f32 v98,v98
v_mfma_f32_16x16x32_bf16 v[116:119],a[156:159],v[248:251],v[44:47]
v_exp_f32 v99,v99
v_mfma_f32_16x16x32_bf16 v[116:119],a[152:155],v[240:243],v[116:119]
s_add_u32 m0,s86,81920
ds_read_b128 a[132:135],v1 offset:256
v_cvt_pk_bf16_f32 v97,v98,v99
v_mfma_f32_16x16x32_bf16 v[116:119],a[148:151],v[232:235],v[116:119]
v_exp_f32 v100,v100
v_mfma_f32_16x16x32_bf16 v[116:119],a[144:147],v[224:227],v[116:119]
buffer_load_dwordx4 v24,s[48:51],s83 offen lds
v_mfma_f32_16x16x32_bf16 v[120:123],a[160:163],v[224:227],v[44:47]
v_exp_f32 v101,v101
v_mfma_f32_16x16x32_bf16 v[120:123],a[164:167],v[232:235],v[120:123]
buffer_store_dwordx4 a[64:67],v6,s[40:43],s79 offen offset:2048 sc1
v_mfma_f32_16x16x32_bf16 v[120:123],a[168:171],v[240:243],v[120:123]
ds_read_b128 a[136:139],v1 offset:512
v_cvt_pk_bf16_f32 v98,v100,v101
v_mfma_f32_16x16x32_bf16 v[120:123],a[172:175],v[248:251],v[120:123]
v_exp_f32 v102,v102
v_mfma_f32_16x16x32_bf16 v[124:127],a[188:191],v[248:251],v[44:47]
v_exp_f32 v103,v103
v_mfma_f32_16x16x32_bf16 v[124:127],a[184:187],v[240:243],v[124:127]
ds_read_b128 a[140:143],v1 offset:768
v_cvt_pk_bf16_f32 v99,v102,v103
v_mfma_f32_16x16x32_bf16 v[124:127],a[180:183],v[232:235],v[124:127]
v_exp_f32 v104,v104
v_mfma_f32_16x16x32_bf16 v[124:127],a[176:179],v[224:227],v[124:127]
v_exp_f32 v105,v105
v_mfma_f32_16x16x32_bf16 v[132:135],a[192:195],v[80:83],v[132:135]
ds_read_b128 a[156:159],v1 offset:4864
v_cvt_pk_bf16_f32 v104,v104,v105
v_mfma_f32_16x16x32_bf16 v[132:135],a[196:199],v[88:91],v[132:135]
buffer_load_dwordx4 v25,s[48:51],s83 offen offset:1024 lds
v_mfma_f32_16x16x32_bf16 v[140:143],a[200:203],v[80:83],v[140:143]
v_exp_f32 v106,v106
v_mfma_f32_16x16x32_bf16 v[140:143],a[204:207],v[88:91],v[140:143]
v_exp_f32 v107,v107
v_mfma_f32_16x16x32_bf16 v[148:151],a[208:211],v[80:83],v[148:151]
ds_read_b128 a[152:155],v1 offset:4608
v_cvt_pk_bf16_f32 v105,v106,v107
v_mfma_f32_16x16x32_bf16 v[148:151],a[212:215],v[88:91],v[148:151]
v_exp_f32 v108,v108
v_mfma_f32_16x16x32_bf16 v[156:159],a[216:219],v[80:83],v[156:159]
v_exp_f32 v109,v109
v_mfma_f32_16x16x32_bf16 v[156:159],a[220:223],v[88:91],v[156:159]
ds_read_b128 a[148:151],v1 offset:4352
v_cvt_pk_bf16_f32 v106,v108,v109
v_mfma_f32_16x16x32_bf16 v[164:167],a[224:227],v[80:83],v[164:167]
v_exp_f32 v110,v110
v_mfma_f32_16x16x32_bf16 v[164:167],a[228:231],v[88:91],v[164:167]
buffer_store_dwordx4 a[68:71],v6,s[40:43],s79 offen sc1
v_mfma_f32_16x16x32_bf16 v[172:175],a[232:235],v[80:83],v[172:175]
s_add_u32 s79,s79,65536
v_exp_f32 v111,v111
v_mfma_f32_16x16x32_bf16 v[172:175],a[236:239],v[88:91],v[172:175]
buffer_load_dwordx4 v26,s[48:51],s83 offen offset:2048 lds
v_mfma_f32_16x16x32_bf16 v[180:183],a[240:243],v[80:83],v[180:183]
ds_read_b128 a[144:147],v1 offset:4096
v_cvt_pk_bf16_f32 v107,v110,v111
v_mfma_f32_16x16x32_bf16 v[180:183],a[244:247],v[88:91],v[180:183]
v_exp_f32 v112,v112
v_mfma_f32_16x16x32_bf16 v[188:191],a[248:251],v[80:83],v[188:191]
ds_read_b128 a[160:163],v1 offset:8192
v_mfma_f32_16x16x32_bf16 v[188:191],a[252:255],v[88:91],v[188:191]
v_exp_f32 v113,v113
v_mfma_f32_16x16x32_bf16 v[52:55],v[28:31],v[80:83],v[52:55]
ds_read_b128 a[164:167],v1 offset:8448
v_cvt_pk_bf16_f32 v112,v112,v113
v_mfma_f32_16x16x32_bf16 v[52:55],v[28:31],v[88:91],v[52:55]
s_waitcnt lgkmcnt(3)
v_exp_f32 v114,v114
v_mfma_f32_16x16x32_bf16 v[64:67],a[128:131],v[128:131],v[32:35]
ds_read_b128 a[168:171],v1 offset:8704
v_mfma_f32_16x16x32_bf16 v[64:67],a[132:135],v[136:139],v[64:67]
v_exp_f32 v115,v115
v_mfma_f32_16x16x32_bf16 v[64:67],a[136:139],v[144:147],v[64:67]
ds_read_b128 a[172:175],v1 offset:8960
v_cvt_pk_bf16_f32 v113,v114,v115
v_mfma_f32_16x16x32_bf16 v[64:67],a[140:143],v[152:155],v[64:67]
buffer_load_dwordx4 v27,s[48:51],s83 offen offset:3072 lds
v_mfma_f32_16x16x32_bf16 v[68:71],a[156:159],v[152:155],v[32:35]
ds_read_b128 a[188:191],v1 offset:13056
v_mfma_f32_16x16x32_bf16 v[68:71],a[152:155],v[144:147],v[68:71]
v_exp_f32 v116,v116
v_mfma_f32_16x16x32_bf16 v[68:71],a[148:151],v[136:139],v[68:71]
s_waitcnt lgkmcnt(3)
ds_read_b128 a[184:187],v1 offset:12800
v_mfma_f32_16x16x32_bf16 v[68:71],a[144:147],v[128:131],v[68:71]
)A"
R"A(v_exp_f32 v117,v117
v_mfma_f32_16x16x32_bf16 v[72:75],a[160:163],v[128:131],v[32:35]
buffer_store_dwordx4 a[72:75],v6,s[40:43],s80 offen offset:2048 sc1
v_mfma_f32_16x16x32_bf16 v[72:75],a[164:167],v[136:139],v[72:75]
s_waitcnt lgkmcnt(3)
ds_read_b128 a[180:183],v1 offset:12544
v_cvt_pk_bf16_f32 v114,v116,v117
v_mfma_f32_16x16x32_bf16 v[72:75],a[168:171],v[144:147],v[72:75]
s_waitcnt lgkmcnt(2)
v_exp_f32 v118,v118
v_mfma_f32_16x16x32_bf16 v[72:75],a[172:175],v[152:155],v[72:75]
s_add_u32 m0,s86,86016
ds_read_b128 a[176:179],v1 offset:12288
v_add_u32 v1,s78,v1
v_mfma_f32_16x16x32_bf16 v[76:79],a[188:191],v[152:155],v[32:35]
s_waitcnt lgkmcnt(0)
v_exp_f32 v119,v119
v_mfma_f32_16x16x32_bf16 v[76:79],a[184:187],v[144:147],v[76:79]
buffer_load_dwordx4 v7,s[48:51],s83 offen lds
v_mfma_f32_16x16x32_bf16 v[76:79],a[180:183],v[136:139],v[76:79]
s_add_u32 s83,s64,1
v_exp_f32 v120,v120
v_mfma_f32_16x16x32_bf16 v[76:79],a[176:179],v[128:131],v[76:79]
s_and_b32 s83,s83,3
v_exp_f32 v121,v121
v_mfma_f32_16x16x32_bf16 v[196:199],a[192:195],v[96:99],v[196:199]
s_lshl_b32 s83,s83,1
v_cvt_pk_bf16_f32 v115,v118,v119
v_cvt_pk_bf16_f32 v120,v120,v121
v_mfma_f32_16x16x32_bf16 v[196:199],a[196:199],v[104:107],v[196:199]
s_mul_i32 s83,s83,163840
v_exp_f32 v122,v122
v_mfma_f32_16x16x32_bf16 v[204:207],a[200:203],v[96:99],v[204:207]
s_add_u32 s83,s83,s85
v_exp_f32 v123,v123
v_mfma_f32_16x16x32_bf16 v[204:207],a[204:207],v[104:107],v[204:207]
v_exp_f32 v124,v124
v_mfma_f32_16x16x32_bf16 v[212:215],a[208:211],v[96:99],v[212:215]
v_exp_f32 v125,v125
v_mfma_f32_16x16x32_bf16 v[212:215],a[212:215],v[104:107],v[212:215]
s_add_u32 m0,s86,87040
v_exp_f32 v126,v126
v_mfma_f32_16x16x32_bf16 v[220:223],a[216:219],v[96:99],v[220:223]
v_exp_f32 v127,v127
v_mfma_f32_16x16x32_bf16 v[220:223],a[220:223],v[104:107],v[220:223]
buffer_load_dwordx4 v24,s[48:51],s84 offen lds
v_mfma_f32_16x16x32_bf16 v[228:231],a[224:227],v[96:99],v[228:231]
v_cvt_pk_bf16_f32 v121,v122,v123
v_cvt_pk_bf16_f32 v122,v124,v125
v_mfma_f32_16x16x32_bf16 v[228:231],a[228:231],v[104:107],v[228:231]
buffer_store_dwordx4 a[76:79],v6,s[40:43],s80 offen sc1
v_mfma_f32_16x16x32_bf16 v[236:239],a[232:235],v[96:99],v[236:239]
s_add_u32 s80,s80,65536
v_exp_f32 v64,v64
v_mfma_f32_16x16x32_bf16 v[236:239],a[236:239],v[104:107],v[236:239]
v_exp_f32 v65,v65
v_mfma_f32_16x16x32_bf16 v[244:247],a[240:243],v[96:99],v[244:247]
v_cvt_pk_bf16_f32 v123,v126,v127
v_cvt_pk_bf16_f32 v64,v64,v65
v_mfma_f32_16x16x32_bf16 v[244:247],a[244:247],v[104:107],v[244:247]
v_exp_f32 v66,v66
v_mfma_f32_16x16x32_bf16 v[252:255],a[248:251],v[96:99],v[252:255]
v_exp_f32 v67,v67
v_mfma_f32_16x16x32_bf16 v[252:255],a[252:255],v[104:107],v[252:255]
v_exp_f32 v68,v68
v_mfma_f32_16x16x32_bf16 v[56:59],v[28:31],v[96:99],v[56:59]
v_exp_f32 v69,v69
v_mfma_f32_16x16x32_bf16 v[56:59],v[28:31],v[104:107],v[56:59]
buffer_load_dwordx4 v25,s[48:51],s84 offen offset:1024 lds
v_mfma_f32_16x16x32_bf16 v[80:83],a[128:131],v[160:163],v[36:39]
v_exp_f32 v70,v70
v_mfma_f32_16x16x32_bf16 v[80:83],a[132:135],v[168:171],v[80:83]
v_exp_f32 v71,v71
v_mfma_f32_16x16x32_bf16 v[80:83],a[136:139],v[176:179],v[80:83]
v_cvt_pk_bf16_f32 v65,v66,v67
v_cvt_pk_bf16_f32 v66,v68,v69
v_mfma_f32_16x16x32_bf16 v[80:83],a[140:143],v[184:187],v[80:83]
v_exp_f32 v72,v72
v_mfma_f32_16x16x32_bf16 v[84:87],a[156:159],v[184:187],v[36:39]
v_exp_f32 v73,v73
v_mfma_f32_16x16x32_bf16 v[84:87],a[152:155],v[176:179],v[84:87]
v_cvt_pk_bf16_f32 v67,v70,v71
v_cvt_pk_bf16_f32 v72,v72,v73
v_mfma_f32_16x16x32_bf16 v[84:87],a[148:151],v[168:171],v[84:87]
v_exp_f32 v74,v74
v_mfma_f32_16x16x32_bf16 v[84:87],a[144:147],v[160:163],v[84:87]
v_exp_f32 v75,v75
v_mfma_f32_16x16x32_bf16 v[88:91],a[160:163],v[160:163],v[36:39]
buffer_store_dwordx4 a[80:83],v6,s[40:43],s81 offen offset:2048 sc1
v_mfma_f32_16x16x32_bf16 v[88:91],a[164:167],v[168:171],v[88:91]
v_exp_f32 v76,v76
v_mfma_f32_16x16x32_bf16 v[88:91],a[168:171],v[176:179],v[88:91]
buffer_load_dwordx4 v26,s[48:51],s84 offen offset:2048 lds
v_mfma_f32_16x16x32_bf16 v[88:91],a[172:175],v[184:187],v[88:91]
v_exp_f32 v77,v77
v_mfma_f32_16x16x32_bf16 v[92:95],a[188:191],v[184:187],v[36:39]
v_exp_f32 v78,v78
v_mfma_f32_16x16x32_bf16 v[92:95],a[184:187],v[176:179],v[92:95]
v_exp_f32 v79,v79
v_mfma_f32_16x16x32_bf16 v[92:95],a[180:183],v[168:171],v[92:95]
v_cvt_pk_bf16_f32 v73,v74,v75
v_cvt_pk_bf16_f32 v74,v76,v77
v_mfma_f32_16x16x32_bf16 v[92:95],a[176:179],v[160:163],v[92:95]
v_exp_f32 v80,v80
v_mfma_f32_16x16x32_bf16 a[32:35],a[192:195],v[112:115],a[32:35]
ds_read_b64_tr_b16 a[192:193],v2
ds_read_b64_tr_b16 a[194:195],v2 offset:4096
v_mfma_f32_16x16x32_bf16 a[32:35],a[196:199],v[120:123],a[32:35]
ds_read_b64_tr_b16 a[196:197],v2 offset:8192
v_cvt_pk_bf16_f32 v75,v78,v79
v_mfma_f32_16x16x32_bf16 a[36:39],a[200:203],v[112:115],a[36:39]
v_exp_f32 v81,v81
v_mfma_f32_16x16x32_bf16 a[36:39],a[204:207],v[120:123],a[36:39]
buffer_load_dwordx4 v27,s[48:51],s84 offen offset:3072 lds
v_mfma_f32_16x16x32_bf16 a[40:43],a[208:211],v[112:115],a[40:43]
ds_read_b64_tr_b16 a[198:199],v2 offset:12288
ds_read_b64_tr_b16 a[200:201],v3
v_mfma_f32_16x16x32_bf16 a[40:43],a[212:215],v[120:123],a[40:43]
ds_read_b64_tr_b16 a[202:203],v3 offset:4096
v_cvt_pk_bf16_f32 v80,v80,v81
v_mfma_f32_16x16x32_bf16 a[44:47],a[216:219],v[112:115],a[44:47]
v_exp_f32 v82,v82
v_mfma_f32_16x16x32_bf16 a[44:47],a[220:223],v[120:123],a[44:47]
ds_read_b64_tr_b16 a[204:205],v3 offset:8192
ds_read_b64_tr_b16 a[206:207],v3 offset:12288
v_mfma_f32_16x16x32_bf16 a[48:51],a[224:227],v[112:115],a[48:51]
v_exp_f32 v83,v83
v_mfma_f32_16x16x32_bf16 a[48:51],a[228:231],v[120:123],a[48:51]
buffer_store_dwordx4 a[84:87],v6,s[40:43],s81 offen sc1
v_mfma_f32_16x16x32_bf16 a[52:55],a[232:235],v[112:115],a[52:55]
s_add_u32 s81,s81,65536
ds_read_b64_tr_b16 a[208:209],v2 offset:256
ds_read_b64_tr_b16 a[210:211],v2 offset:4352
v_mfma_f32_16x16x32_bf16 a[52:55],a[236:239],v[120:123],a[52:55]
s_add_u32 m0,s86,91136
ds_read_b64_tr_b16 a[212:213],v2 offset:8448
v_cvt_pk_bf16_f32 v81,v82,v83
v_mfma_f32_16x16x32_bf16 a[56:59],a[240:243],v[112:115],a[56:59]
v_exp_f32 v84,v84
v_mfma_f32_16x16x32_bf16 a[56:59],a[244:247],v[120:123],a[56:59]
buffer_load_dwordx4 v7,s[48:51],s84 offen lds
v_mfma_f32_16x16x32_bf16 a[60:63],a[248:251],v[112:115],a[60:63]
s_add_u32 s84,s83,20480
ds_read_b64_tr_b16 a[214:215],v2 offset:12544
ds_read_b64_tr_b16 a[216:217],v3 offset:256
v_mfma_f32_16x16x32_bf16 a[60:63],a[252:255],v[120:123],a[60:63]
v_exp_f32 v85,v85
v_mfma_f32_16x16x32_bf16 v[60:63],v[28:31],v[112:115],v[60:63]
ds_read_b64_tr_b16 a[218:219],v3 offset:4352
ds_read_b64_tr_b16 a[220:221],v3 offset:8448
v_mfma_f32_16x16x32_bf16 v[60:63],v[28:31],v[120:123],v[60:63]
ds_read_b64_tr_b16 a[222:223],v3 offset:12544
v_cvt_pk_bf16_f32 v82,v84,v85
v_mfma_f32_16x16x32_bf16 v[96:99],a[128:131],v[192:195],v[40:43]
v_exp_f32 v86,v86
v_mfma_f32_16x16x32_bf16 v[96:99],a[132:135],v[200:203],v[96:99]
ds_read_b64_tr_b16 a[224:225],v2 offset:512
ds_read_b64_tr_b16 a[226:227],v2 offset:4608
v_mfma_f32_16x16x32_bf16 v[96:99],a[136:139],v[208:211],v[96:99]
v_exp_f32 v87,v87
v_mfma_f32_16x16x32_bf16 v[96:99],a[140:143],v[216:219],v[96:99]
ds_read_b64_tr_b16 a[228:229],v2 offset:8704
ds_read_b64_tr_b16 a[230:231],v2 offset:12800
v_mfma_f32_16x16x32_bf16 v[100:103],a[156:159],v[216:219],v[40:43]
ds_read_b64_tr_b16 a[232:233],v3 offset:512
v_cvt_pk_bf16_f32 v83,v86,v87
v_mfma_f32_16x16x32_bf16 v[100:103],a[152:155],v[208:211],v[100:103]
v_exp_f32 v88,v88
v_mfma_f32_16x16x32_bf16 v[100:103],a[148:151],v[200:203],v[100:103]
ds_read_b64_tr_b16 a[234:235],v3 offset:4608
ds_read_b64_tr_b16 a[236:237],v3 offset:8704
v_mfma_f32_16x16x32_bf16 v[100:103],a[144:147],v[192:195],v[100:103]
v_exp_f32 v89,v89
v_mfma_f32_16x16x32_bf16 v[104:107],a[160:163],v[192:195],v[40:43]
buffer_store_dwordx4 a[88:91],v6,s[40:43],s82 offen offset:2048 sc1
v_mfma_f32_16x16x32_bf16 v[104:107],a[164:167],v[200:203],v[104:107]
ds_read_b64_tr_b16 a[238:239],v3 offset:12800
ds_read_b64_tr_b16 a[240:241],v2 offset:768
v_mfma_f32_16x16x32_bf16 v[104:107],a[168:171],v[208:211],v[104:107]
ds_read_b64_tr_b16 a[242:243],v2 offset:4864
v_cvt_pk_bf16_f32 v88,v88,v89
v_mfma_f32_16x16x32_bf16 v[104:107],a[172:175],v[216:219],v[104:107]
v_exp_f32 v90,v90
v_mfma_f32_16x16x32_bf16 v[108:111],a[188:191],v[216:219],v[40:43]
ds_read_b64_tr_b16 a[244:245],v2 offset:8960
ds_read_b64_tr_b16 a[246:247],v2 offset:13056
v_mfma_f32_16x16x32_bf16 v[108:111],a[184:187],v[208:211],v[108:111]
ds_read_b64_tr_b16 a[248:249],v3 offset:768
v_add_u32 v2,s78,v2
v_mfma_f32_16x16x32_bf16 v[108:111],a[180:183],v[200:203],v[108:111]
v_exp_f32 v91,v91
v_mfma_f32_16x16x32_bf16 v[108:111],a[176:179],v[192:195],v[108:111]
s_waitcnt lgkmcnt(6)
ds_read_b64_tr_b16 a[250:251],v3 offset:4864
ds_read_b64_tr_b16 a[252:253],v3 offset:8960
v_mfma_f32_16x16x32_bf16 a[0:3],a[192:195],v[64:67],a[0:3]
ds_read_b64_tr_b16 a[254:255],v3 offset:13056
v_cvt_pk_bf16_f32 v89,v90,v91
v_mfma_f32_16x16x32_bf16 a[0:3],a[196:199],v[72:75],a[0:3]
v_exp_f32 v92,v92
v_mfma_f32_16x16x32_bf16 a[4:7],a[200:203],v[64:67],a[4:7]
v_exp_f32 v93,v93
v_mfma_f32_16x16x32_bf16 a[4:7],a[204:207],v[72:75],a[4:7]
v_exp_f32 v94,v94
v_mfma_f32_16x16x32_bf16 a[8:11],a[208:211],v[64:67],a[8:11]
v_exp_f32 v95,v95
v_mfma_f32_16x16x32_bf16 a[8:11],a[212:215],v[72:75],a[8:11]
v_cvt_pk_bf16_f32 v90,v92,v93
v_cvt_pk_bf16_f32 v91,v94,v95
v_mfma_f32_16x16x32_bf16 a[12:15],a[216:219],v[64:67],a[12:15]
s_cmp_ge_u32 s63,2
s_cselect_b32 s6,L_gbS_%=-L_pcg_%=,s6
s_cselect_b32 s10,L_geS_%=-L_gbS_%=,s10
v_mfma_f32_16x16x32_bf16 a[12:15],a[220:223],v[72:75],a[12:15]
s_add_u32 s8,s8,s6
s_addc_u32 s9,s9,0
s_and_b32 s8,s8,0xffffff80
v_mfma_f32_16x16x32_bf16 a[16:19],a[224:227],v[64:67],a[16:19]
s_and_b32 s9,s9,0xffff
v_exp_f32 v96,v96
v_mfma_f32_16x16x32_bf16 a[16:19],a[228:231],v[72:75],a[16:19]
buffer_store_dwordx4 a[92:95],v6,s[40:43],s82 offen sc1
v_mfma_f32_16x16x32_bf16 a[20:23],a[232:235],v[64:67],a[20:23]
s_add_u32 s82,s82,65536
v_exp_f32 v97,v97
s_waitcnt lgkmcnt(0)
v_mfma_f32_16x16x32_bf16 a[20:23],a[236:239],v[72:75],a[20:23]
v_exp_f32 v98,v98
v_mfma_f32_16x16x32_bf16 a[24:27],a[240:243],v[64:67],a[24:27]
v_exp_f32 v99,v99
v_mfma_f32_16x16x32_bf16 a[24:27],a[244:247],v[72:75],a[24:27]
v_exp_f32 v100,v100
v_mfma_f32_16x16x32_bf16 a[28:31],a[248:251],v[64:67],a[28:31]
v_exp_f32 v101,v101
v_mfma_f32_16x16x32_bf16 a[28:31],a[252:255],v[72:75],a[28:31]
v_exp_f32 v102,v102
v_mfma_f32_16x16x32_bf16 v[48:51],v[28:31],v[64:67],v[48:51]
v_exp_f32 v103,v103
v_mfma_f32_16x16x32_bf16 v[48:51],v[28:31],v[72:75],v[48:51]
v_cvt_pk_bf16_f32 v96,v96,v97
v_cvt_pk_bf16_f32 v97,v98,v99
s_waitcnt vmcnt(36)
s_barrier
v_mfma_f32_16x16x32_bf16 v[112:115],a[128:131],v[224:227],v[44:47]
ds_read_b128 a[128:131],v1
v_cvt_pk_bf16_f32 v98,v100,v101
v_mfma_f32_16x16x32_bf16 v[112:115],a[132:135],v[232:235],v[112:115]
v_cvt_pk_bf16_f32 v99,v102,v103
v_add_u32 v3,s78,v3
v_mfma_f32_16x16x32_bf16 v[112:115],a[136:139],v[240:243],v[112:115]
s_sub_u32 s78,s77,32768
v_exp_f32 v104,v104
v_mfma_f32_16x16x32_bf16 v[112:115],a[140:143],v[248:251],v[112:115]
s_and_b32 s78,s78,0x1ffff
v_exp_f32 v105,v105
v_mfma_f32_16x16x32_bf16 v[116:119],a[156:159],v[248:251],v[44:47]
s_sub_u32 s78,s78,s77
ds_read_b128 a[132:135],v1 offset:256
v_cvt_pk_bf16_f32 v104,v104,v105
v_mfma_f32_16x16x32_bf16 v[116:119],a[152:155],v[240:243],v[116:119]
s_add_u32 m0,s86,40960
v_exp_f32 v106,v106
v_mfma_f32_16x16x32_bf16 v[116:119],a[148:151],v[232:235],v[116:119]
s_add_u32 s77,s77,s78
v_exp_f32 v107,v107
v_mfma_f32_16x16x32_bf16 v[116:119],a[144:147],v[224:227],v[116:119]
buffer_load_dwordx4 v24,s[48:51],s83 offen lds
v_mfma_f32_16x16x32_bf16 v[120:123],a[160:163],v[224:227],v[44:47]
ds_read_b128 a[136:139],v1 offset:512
v_cvt_pk_bf16_f32 v105,v106,v107
v_mfma_f32_16x16x32_bf16 v[120:123],a[164:167],v[232:235],v[120:123]
v_exp_f32 v108,v108
v_mfma_f32_16x16x32_bf16 v[120:123],a[168:171],v[240:243],v[120:123]
v_exp_f32 v109,v109
v_mfma_f32_16x16x32_bf16 v[120:123],a[172:175],v[248:251],v[120:123]
ds_read_b128 a[140:143],v1 offset:768
)A"
R"A(v_cvt_pk_bf16_f32 v106,v108,v109
v_mfma_f32_16x16x32_bf16 v[124:127],a[188:191],v[248:251],v[44:47]
v_exp_f32 v110,v110
v_mfma_f32_16x16x32_bf16 v[124:127],a[184:187],v[240:243],v[124:127]
v_exp_f32 v111,v111
v_mfma_f32_16x16x32_bf16 v[124:127],a[180:183],v[232:235],v[124:127]
ds_read_b128 a[156:159],v1 offset:4864
v_cvt_pk_bf16_f32 v107,v110,v111
v_mfma_f32_16x16x32_bf16 v[124:127],a[176:179],v[224:227],v[124:127]
v_exp_f32 v112,v112
v_mfma_f32_16x16x32_bf16 v[132:135],a[192:195],v[80:83],v[132:135]
v_exp_f32 v113,v113
v_mfma_f32_16x16x32_bf16 v[132:135],a[196:199],v[88:91],v[132:135]
buffer_load_dwordx4 v25,s[48:51],s83 offen offset:1024 lds
v_mfma_f32_16x16x32_bf16 v[140:143],a[200:203],v[80:83],v[140:143]
ds_read_b128 a[152:155],v1 offset:4608
v_cvt_pk_bf16_f32 v112,v112,v113
v_mfma_f32_16x16x32_bf16 v[140:143],a[204:207],v[88:91],v[140:143]
v_exp_f32 v114,v114
v_mfma_f32_16x16x32_bf16 v[148:151],a[208:211],v[80:83],v[148:151]
v_exp_f32 v115,v115
v_mfma_f32_16x16x32_bf16 v[148:151],a[212:215],v[88:91],v[148:151]
ds_read_b128 a[148:151],v1 offset:4352
L_pd12_%=:
s_cbranch_execz L_trt1_%=
v_cvt_pk_bf16_f32 v113,v114,v115
v_mfma_f32_16x16x32_bf16 v[156:159],a[216:219],v[80:83],v[156:159]
v_exp_f32 v116,v116
v_mfma_f32_16x16x32_bf16 v[156:159],a[220:223],v[88:91],v[156:159]
v_exp_f32 v117,v117
v_mfma_f32_16x16x32_bf16 v[164:167],a[224:227],v[80:83],v[164:167]
buffer_load_dword v5,v4,s[8:11],s7 offen
v_mfma_f32_16x16x32_bf16 v[164:167],a[228:231],v[88:91],v[164:167]
ds_read_b128 a[144:147],v1 offset:4096
v_cvt_pk_bf16_f32 v114,v116,v117
v_mfma_f32_16x16x32_bf16 v[172:175],a[232:235],v[80:83],v[172:175]
v_exp_f32 v118,v118
v_mfma_f32_16x16x32_bf16 v[172:175],a[236:239],v[88:91],v[172:175]
v_exp_f32 v119,v119
v_mfma_f32_16x16x32_bf16 v[180:183],a[240:243],v[80:83],v[180:183]
ds_read_b128 a[160:163],v1 offset:8192
v_cvt_pk_bf16_f32 v115,v118,v119
v_mfma_f32_16x16x32_bf16 v[180:183],a[244:247],v[88:91],v[180:183]
v_exp_f32 v120,v120
v_mfma_f32_16x16x32_bf16 v[188:191],a[248:251],v[80:83],v[188:191]
buffer_load_dwordx4 v26,s[48:51],s83 offen offset:2048 lds
v_mfma_f32_16x16x32_bf16 v[188:191],a[252:255],v[88:91],v[188:191]
ds_read_b128 a[164:167],v1 offset:8448
v_mfma_f32_16x16x32_bf16 v[52:55],v[28:31],v[80:83],v[52:55]
v_exp_f32 v121,v121
v_mfma_f32_16x16x32_bf16 v[52:55],v[28:31],v[88:91],v[52:55]
s_waitcnt lgkmcnt(2)
ds_read_b128 a[168:171],v1 offset:8704
v_cvt_pk_bf16_f32 v120,v120,v121
v_mfma_f32_16x16x32_bf16 v[64:67],a[128:131],v[128:131],v[32:35]
v_exp_f32 v122,v122
v_mfma_f32_16x16x32_bf16 v[64:67],a[132:135],v[136:139],v[64:67]
ds_read_b128 a[172:175],v1 offset:8960
v_mfma_f32_16x16x32_bf16 v[64:67],a[136:139],v[144:147],v[64:67]
v_exp_f32 v123,v123
v_mfma_f32_16x16x32_bf16 v[64:67],a[140:143],v[152:155],v[64:67]
buffer_load_dwordx4 v27,s[48:51],s83 offen offset:3072 lds
v_mfma_f32_16x16x32_bf16 v[68:71],a[156:159],v[152:155],v[32:35]
ds_read_b128 a[188:191],v1 offset:13056
v_cvt_pk_bf16_f32 v121,v122,v123
v_mfma_f32_16x16x32_bf16 v[68:71],a[152:155],v[144:147],v[68:71]
v_exp_f32 v124,v124
v_mfma_f32_16x16x32_bf16 v[68:71],a[148:151],v[136:139],v[68:71]
ds_read_b128 a[184:187],v1 offset:12800
v_mfma_f32_16x16x32_bf16 v[68:71],a[144:147],v[128:131],v[68:71]
s_waitcnt lgkmcnt(3)
v_exp_f32 v125,v125
v_mfma_f32_16x16x32_bf16 v[72:75],a[160:163],v[128:131],v[32:35]
ds_read_b128 a[180:183],v1 offset:12544
v_cvt_pk_bf16_f32 v122,v124,v125
v_mfma_f32_16x16x32_bf16 v[72:75],a[164:167],v[136:139],v[72:75]
v_exp_f32 v126,v126
v_mfma_f32_16x16x32_bf16 v[72:75],a[168:171],v[144:147],v[72:75]
s_waitcnt lgkmcnt(2)
ds_read_b128 a[176:179],v1 offset:12288
v_add_u32 v1,s78,v1
v_mfma_f32_16x16x32_bf16 v[72:75],a[172:175],v[152:155],v[72:75]
s_add_u32 m0,s86,45056
v_exp_f32 v127,v127
v_mfma_f32_16x16x32_bf16 v[76:79],a[188:191],v[152:155],v[32:35]
s_waitcnt lgkmcnt(0)
v_cvt_pk_bf16_f32 v123,v126,v127
v_lshrrev_b32 v1,6,v6
v_mfma_f32_16x16x32_bf16 v[76:79],a[184:187],v[144:147],v[76:79]
buffer_load_dwordx4 v7,s[48:51],s83 offen lds
v_mfma_f32_16x16x32_bf16 v[76:79],a[180:183],v[136:139],v[76:79]
s_add_u32 s83,s64,2
v_exp_f32 v64,v64
v_mfma_f32_16x16x32_bf16 v[76:79],a[176:179],v[128:131],v[76:79]
s_and_b32 s83,s83,3
v_exp_f32 v65,v65
v_mfma_f32_16x16x32_bf16 v[196:199],a[192:195],v[96:99],v[196:199]
s_lshl_b32 s83,s83,1
v_exp_f32 v66,v66
v_mfma_f32_16x16x32_bf16 v[196:199],a[196:199],v[104:107],v[196:199]
s_mul_i32 s83,s83,163840
v_exp_f32 v67,v67
v_mfma_f32_16x16x32_bf16 v[204:207],a[200:203],v[96:99],v[204:207]
s_add_u32 s83,s83,s85
v_exp_f32 v68,v68
v_mfma_f32_16x16x32_bf16 v[204:207],a[204:207],v[104:107],v[204:207]
v_exp_f32 v69,v69
v_mfma_f32_16x16x32_bf16 v[212:215],a[208:211],v[96:99],v[212:215]
v_exp_f32 v70,v70
v_mfma_f32_16x16x32_bf16 v[212:215],a[212:215],v[104:107],v[212:215]
s_add_u32 m0,s86,46080
v_exp_f32 v71,v71
v_mfma_f32_16x16x32_bf16 v[220:223],a[216:219],v[96:99],v[220:223]
v_cvt_pk_bf16_f32 v64,v64,v65
v_cvt_pk_bf16_f32 v65,v66,v67
v_mfma_f32_16x16x32_bf16 v[220:223],a[220:223],v[104:107],v[220:223]
buffer_load_dwordx4 v24,s[48:51],s84 offen lds
v_mfma_f32_16x16x32_bf16 v[228:231],a[224:227],v[96:99],v[228:231]
v_cvt_pk_bf16_f32 v66,v68,v69
v_cvt_pk_bf16_f32 v67,v70,v71
v_mfma_f32_16x16x32_bf16 v[228:231],a[228:231],v[104:107],v[228:231]
v_exp_f32 v72,v72
v_mfma_f32_16x16x32_bf16 v[236:239],a[232:235],v[96:99],v[236:239]
v_exp_f32 v73,v73
v_mfma_f32_16x16x32_bf16 v[236:239],a[236:239],v[104:107],v[236:239]
v_exp_f32 v74,v74
v_mfma_f32_16x16x32_bf16 v[244:247],a[240:243],v[96:99],v[244:247]
v_exp_f32 v75,v75
v_mfma_f32_16x16x32_bf16 v[244:247],a[244:247],v[104:107],v[244:247]
v_exp_f32 v76,v76
v_mfma_f32_16x16x32_bf16 v[252:255],a[248:251],v[96:99],v[252:255]
v_exp_f32 v77,v77
v_mfma_f32_16x16x32_bf16 v[252:255],a[252:255],v[104:107],v[252:255]
v_exp_f32 v78,v78
v_mfma_f32_16x16x32_bf16 v[56:59],v[28:31],v[96:99],v[56:59]
v_exp_f32 v79,v79
v_mfma_f32_16x16x32_bf16 v[56:59],v[28:31],v[104:107],v[56:59]
buffer_load_dwordx4 v25,s[48:51],s84 offen offset:1024 lds
v_mfma_f32_16x16x32_bf16 v[80:83],a[128:131],v[160:163],v[36:39]
v_cvt_pk_bf16_f32 v72,v72,v73
v_cvt_pk_bf16_f32 v73,v74,v75
v_mfma_f32_16x16x32_bf16 v[80:83],a[132:135],v[168:171],v[80:83]
v_cvt_pk_bf16_f32 v74,v76,v77
v_cvt_pk_bf16_f32 v75,v78,v79
v_mfma_f32_16x16x32_bf16 v[80:83],a[136:139],v[176:179],v[80:83]
v_mul_u32_u24 v1,320,v1
v_mfma_f32_16x16x32_bf16 v[80:83],a[140:143],v[184:187],v[80:83]
v_mfma_f32_16x16x32_bf16 v[84:87],a[156:159],v[184:187],v[36:39]
v_mfma_f32_16x16x32_bf16 v[84:87],a[152:155],v[176:179],v[84:87]
v_mfma_f32_16x16x32_bf16 v[84:87],a[148:151],v[168:171],v[84:87]
v_mfma_f32_16x16x32_bf16 v[84:87],a[144:147],v[160:163],v[84:87]
v_mfma_f32_16x16x32_bf16 v[88:91],a[160:163],v[160:163],v[36:39]
v_mfma_f32_16x16x32_bf16 v[88:91],a[164:167],v[168:171],v[88:91]
buffer_load_dwordx4 v26,s[48:51],s84 offen offset:2048 lds
v_mfma_f32_16x16x32_bf16 v[88:91],a[168:171],v[176:179],v[88:91]
v_exp_f32 v80,v80
v_mfma_f32_16x16x32_bf16 v[88:91],a[172:175],v[184:187],v[88:91]
v_exp_f32 v81,v81
v_mfma_f32_16x16x32_bf16 v[92:95],a[188:191],v[184:187],v[36:39]
v_exp_f32 v82,v82
v_mfma_f32_16x16x32_bf16 v[92:95],a[184:187],v[176:179],v[92:95]
v_exp_f32 v83,v83
v_mfma_f32_16x16x32_bf16 v[92:95],a[180:183],v[168:171],v[92:95]
v_exp_f32 v84,v84
v_mfma_f32_16x16x32_bf16 v[92:95],a[176:179],v[160:163],v[92:95]
v_exp_f32 v85,v85
v_mfma_f32_16x16x32_bf16 a[32:35],a[192:195],v[112:115],a[32:35]
ds_read_b64_tr_b16 a[192:193],v2
ds_read_b64_tr_b16 a[194:195],v2 offset:4096
v_mfma_f32_16x16x32_bf16 a[32:35],a[196:199],v[120:123],a[32:35]
ds_read_b64_tr_b16 a[196:197],v2 offset:8192
v_cvt_pk_bf16_f32 v80,v80,v81
v_mfma_f32_16x16x32_bf16 a[36:39],a[200:203],v[112:115],a[36:39]
ds_read_b64_tr_b16 a[198:199],v2 offset:12288
v_cvt_pk_bf16_f32 v81,v82,v83
v_mfma_f32_16x16x32_bf16 a[36:39],a[204:207],v[120:123],a[36:39]
buffer_load_dwordx4 v27,s[48:51],s84 offen offset:3072 lds
v_mfma_f32_16x16x32_bf16 a[40:43],a[208:211],v[112:115],a[40:43]
ds_read_b64_tr_b16 a[200:201],v3
ds_read_b64_tr_b16 a[202:203],v3 offset:4096
v_mfma_f32_16x16x32_bf16 a[40:43],a[212:215],v[120:123],a[40:43]
ds_read_b64_tr_b16 a[204:205],v3 offset:8192
v_cvt_pk_bf16_f32 v82,v84,v85
v_mfma_f32_16x16x32_bf16 a[44:47],a[216:219],v[112:115],a[44:47]
v_exp_f32 v86,v86
v_mfma_f32_16x16x32_bf16 a[44:47],a[220:223],v[120:123],a[44:47]
ds_read_b64_tr_b16 a[206:207],v3 offset:12288
ds_read_b64_tr_b16 a[208:209],v2 offset:256
v_mfma_f32_16x16x32_bf16 a[48:51],a[224:227],v[112:115],a[48:51]
v_exp_f32 v87,v87
v_mfma_f32_16x16x32_bf16 a[48:51],a[228:231],v[120:123],a[48:51]
ds_read_b64_tr_b16 a[210:211],v2 offset:4352
ds_read_b64_tr_b16 a[212:213],v2 offset:8448
v_mfma_f32_16x16x32_bf16 a[52:55],a[232:235],v[112:115],a[52:55]
ds_read_b64_tr_b16 a[214:215],v2 offset:12544
v_cvt_pk_bf16_f32 v83,v86,v87
v_mfma_f32_16x16x32_bf16 a[52:55],a[236:239],v[120:123],a[52:55]
s_add_u32 m0,s86,50176
v_exp_f32 v88,v88
v_mfma_f32_16x16x32_bf16 a[56:59],a[240:243],v[112:115],a[56:59]
ds_read_b64_tr_b16 a[216:217],v3 offset:256
ds_read_b64_tr_b16 a[218:219],v3 offset:4352
v_mfma_f32_16x16x32_bf16 a[56:59],a[244:247],v[120:123],a[56:59]
buffer_load_dwordx4 v7,s[48:51],s84 offen lds
v_mfma_f32_16x16x32_bf16 a[60:63],a[248:251],v[112:115],a[60:63]
s_add_u32 s84,s83,20480
ds_read_b64_tr_b16 a[220:221],v3 offset:8448
ds_read_b64_tr_b16 a[222:223],v3 offset:12544
v_mfma_f32_16x16x32_bf16 a[60:63],a[252:255],v[120:123],a[60:63]
v_exp_f32 v89,v89
v_mfma_f32_16x16x32_bf16 v[60:63],v[28:31],v[112:115],v[60:63]
ds_read_b64_tr_b16 a[224:225],v2 offset:512
ds_read_b64_tr_b16 a[226:227],v2 offset:4608
v_mfma_f32_16x16x32_bf16 v[60:63],v[28:31],v[120:123],v[60:63]
ds_read_b64_tr_b16 a[228:229],v2 offset:8704
v_cvt_pk_bf16_f32 v88,v88,v89
v_mfma_f32_16x16x32_bf16 v[96:99],a[128:131],v[192:195],v[40:43]
v_exp_f32 v90,v90
v_mfma_f32_16x16x32_bf16 v[96:99],a[132:135],v[200:203],v[96:99]
ds_read_b64_tr_b16 a[230:231],v2 offset:12800
ds_read_b64_tr_b16 a[232:233],v3 offset:512
v_mfma_f32_16x16x32_bf16 v[96:99],a[136:139],v[208:211],v[96:99]
v_exp_f32 v91,v91
v_mfma_f32_16x16x32_bf16 v[96:99],a[140:143],v[216:219],v[96:99]
ds_read_b64_tr_b16 a[234:235],v3 offset:4608
ds_read_b64_tr_b16 a[236:237],v3 offset:8704
v_mfma_f32_16x16x32_bf16 v[100:103],a[156:159],v[216:219],v[40:43]
ds_read_b64_tr_b16 a[238:239],v3 offset:12800
v_cvt_pk_bf16_f32 v89,v90,v91
v_mfma_f32_16x16x32_bf16 v[100:103],a[152:155],v[208:211],v[100:103]
v_exp_f32 v92,v92
v_mfma_f32_16x16x32_bf16 v[100:103],a[148:151],v[200:203],v[100:103]
ds_read_b64_tr_b16 a[240:241],v2 offset:768
ds_read_b64_tr_b16 a[242:243],v2 offset:4864
v_mfma_f32_16x16x32_bf16 v[100:103],a[144:147],v[192:195],v[100:103]
v_exp_f32 v93,v93
v_mfma_f32_16x16x32_bf16 v[104:107],a[160:163],v[192:195],v[40:43]
ds_read_b64_tr_b16 a[244:245],v2 offset:8960
ds_read_b64_tr_b16 a[246:247],v2 offset:13056
v_mfma_f32_16x16x32_bf16 v[104:107],a[164:167],v[200:203],v[104:107]
ds_read_b64_tr_b16 a[248:249],v3 offset:768
v_cvt_pk_bf16_f32 v90,v92,v93
v_mfma_f32_16x16x32_bf16 v[104:107],a[168:171],v[208:211],v[104:107]
ds_read_b64_tr_b16 a[250:251],v3 offset:4864
v_bfe_u32 v2,v6,4,2
v_mfma_f32_16x16x32_bf16 v[104:107],a[172:175],v[216:219],v[104:107]
ds_read_b64_tr_b16 a[252:253],v3 offset:8960
v_lshl_add_u32 v1,v2,3,v1
v_mfma_f32_16x16x32_bf16 v[108:111],a[188:191],v[216:219],v[40:43]
ds_read_b64_tr_b16 a[254:255],v3 offset:13056
v_add_u32 v1,122880,v1
v_mfma_f32_16x16x32_bf16 v[108:111],a[184:187],v[208:211],v[108:111]
v_exp_f32 v94,v94
v_mfma_f32_16x16x32_bf16 v[108:111],a[180:183],v[200:203],v[108:111]
v_exp_f32 v95,v95
v_mfma_f32_16x16x32_bf16 v[108:111],a[176:179],v[192:195],v[108:111]
s_waitcnt lgkmcnt(6)
v_cvt_pk_bf16_f32 v91,v94,v95
v_bfe_u32 v2,v6,8,1
v_mfma_f32_16x16x32_bf16 a[0:3],a[192:195],v[64:67],a[0:3]
v_exp_f32 v96,v96
v_mfma_f32_16x16x32_bf16 a[0:3],a[196:199],v[72:75],a[0:3]
v_exp_f32 v97,v97
v_mfma_f32_16x16x32_bf16 a[4:7],a[200:203],v[64:67],a[4:7]
v_exp_f32 v98,v98
v_mfma_f32_16x16x32_bf16 a[4:7],a[204:207],v[72:75],a[4:7]
v_exp_f32 v99,v99
v_mfma_f32_16x16x32_bf16 a[8:11],a[208:211],v[64:67],a[8:11]
)A"
R"A(v_exp_f32 v100,v100
v_mfma_f32_16x16x32_bf16 a[8:11],a[212:215],v[72:75],a[8:11]
v_exp_f32 v101,v101
v_mfma_f32_16x16x32_bf16 a[12:15],a[216:219],v[64:67],a[12:15]
v_exp_f32 v102,v102
v_mfma_f32_16x16x32_bf16 a[12:15],a[220:223],v[72:75],a[12:15]
v_exp_f32 v103,v103
v_mfma_f32_16x16x32_bf16 a[16:19],a[224:227],v[64:67],a[16:19]
v_cvt_pk_bf16_f32 v96,v96,v97
v_cvt_pk_bf16_f32 v97,v98,v99
v_mfma_f32_16x16x32_bf16 a[16:19],a[228:231],v[72:75],a[16:19]
v_cvt_pk_bf16_f32 v98,v100,v101
v_cvt_pk_bf16_f32 v99,v102,v103
v_mfma_f32_16x16x32_bf16 a[20:23],a[232:235],v[64:67],a[20:23]
v_exp_f32 v104,v104
v_mfma_f32_16x16x32_bf16 a[20:23],a[236:239],v[72:75],a[20:23]
v_exp_f32 v105,v105
v_mfma_f32_16x16x32_bf16 a[24:27],a[240:243],v[64:67],a[24:27]
s_waitcnt lgkmcnt(0)
v_exp_f32 v106,v106
v_mfma_f32_16x16x32_bf16 a[24:27],a[244:247],v[72:75],a[24:27]
v_exp_f32 v107,v107
v_mfma_f32_16x16x32_bf16 a[28:31],a[248:251],v[64:67],a[28:31]
v_exp_f32 v108,v108
v_mfma_f32_16x16x32_bf16 a[28:31],a[252:255],v[72:75],a[28:31]
v_exp_f32 v109,v109
v_mfma_f32_16x16x32_bf16 v[48:51],v[28:31],v[64:67],v[48:51]
v_exp_f32 v110,v110
v_mfma_f32_16x16x32_bf16 v[48:51],v[28:31],v[72:75],v[48:51]
v_exp_f32 v111,v111
s_waitcnt vmcnt(11)
s_barrier
v_mfma_f32_16x16x32_bf16 v[112:115],a[128:131],v[224:227],v[44:47]
buffer_store_dwordx2 v[8:9],v10,s[44:47],s18 offen offset:256 sc1
v_mfma_f32_16x16x32_bf16 v[112:115],a[132:135],v[232:235],v[112:115]
v_cvt_pk_bf16_f32 v104,v104,v105
v_cvt_pk_bf16_f32 v105,v106,v107
v_mfma_f32_16x16x32_bf16 v[112:115],a[136:139],v[240:243],v[112:115]
v_cvt_pk_bf16_f32 v106,v108,v109
v_cvt_pk_bf16_f32 v107,v110,v111
v_mfma_f32_16x16x32_bf16 v[112:115],a[140:143],v[248:251],v[112:115]
s_add_u32 m0,s86,0
v_cmp_nlt_f32 s[54:55],v48,s73
v_max_f32 v49,0x00800000,v48
v_mfma_f32_16x16x32_bf16 v[116:119],a[156:159],v[248:251],v[44:47]
v_rcp_f32 v49,v49
v_mfma_f32_16x16x32_bf16 v[116:119],a[152:155],v[240:243],v[116:119]
v_lshl_add_u32 v1,v2,5,v1
v_lshlrev_b32 v2,6,v2
v_mfma_f32_16x16x32_bf16 v[116:119],a[148:151],v[232:235],v[116:119]
buffer_load_dwordx4 v24,s[48:51],s83 offen lds
v_mfma_f32_16x16x32_bf16 v[116:119],a[144:147],v[224:227],v[116:119]
v_exp_f32 v112,v112
v_mfma_f32_16x16x32_bf16 v[120:123],a[160:163],v[224:227],v[44:47]
v_exp_f32 v113,v113
v_mfma_f32_16x16x32_bf16 v[120:123],a[164:167],v[232:235],v[120:123]
v_exp_f32 v114,v114
v_mfma_f32_16x16x32_bf16 v[120:123],a[168:171],v[240:243],v[120:123]
v_exp_f32 v115,v115
v_mfma_f32_16x16x32_bf16 v[120:123],a[172:175],v[248:251],v[120:123]
v_exp_f32 v116,v116
v_mfma_f32_16x16x32_bf16 v[124:127],a[188:191],v[248:251],v[44:47]
buffer_load_dwordx4 v25,s[48:51],s83 offen offset:1024 lds
v_mfma_f32_16x16x32_bf16 v[124:127],a[184:187],v[240:243],v[124:127]
v_exp_f32 v117,v117
v_mfma_f32_16x16x32_bf16 v[124:127],a[180:183],v[232:235],v[124:127]
v_exp_f32 v118,v118
v_mfma_f32_16x16x32_bf16 v[124:127],a[176:179],v[224:227],v[124:127]
v_exp_f32 v119,v119
v_mfma_f32_16x16x32_bf16 v[52:55],v[28:31],v[80:83],v[52:55]
v_cvt_pk_bf16_f32 v112,v112,v113
v_cvt_pk_bf16_f32 v113,v114,v115
v_mfma_f32_16x16x32_bf16 v[52:55],v[28:31],v[88:91],v[52:55]
v_cvt_pk_bf16_f32 v114,v116,v117
v_cvt_pk_bf16_f32 v115,v118,v119
v_mfma_f32_16x16x32_bf16 v[132:135],a[192:195],v[80:83],v[132:135]
buffer_load_dwordx4 v26,s[48:51],s83 offen offset:2048 lds
v_mfma_f32_16x16x32_bf16 v[132:135],a[196:199],v[88:91],v[132:135]
v_exp_f32 v120,v120
v_mfma_f32_16x16x32_bf16 v[140:143],a[200:203],v[80:83],v[140:143]
v_exp_f32 v121,v121
v_mfma_f32_16x16x32_bf16 v[140:143],a[204:207],v[88:91],v[140:143]
v_exp_f32 v122,v122
v_mfma_f32_16x16x32_bf16 v[148:151],a[208:211],v[80:83],v[148:151]
v_exp_f32 v123,v123
v_mfma_f32_16x16x32_bf16 v[148:151],a[212:215],v[88:91],v[148:151]
v_exp_f32 v124,v124
v_mfma_f32_16x16x32_bf16 v[156:159],a[216:219],v[80:83],v[156:159]
buffer_load_dwordx4 v27,s[48:51],s83 offen offset:3072 lds
v_mfma_f32_16x16x32_bf16 v[156:159],a[220:223],v[88:91],v[156:159]
v_exp_f32 v125,v125
v_mfma_f32_16x16x32_bf16 v[164:167],a[224:227],v[80:83],v[164:167]
v_exp_f32 v126,v126
v_mfma_f32_16x16x32_bf16 v[164:167],a[228:231],v[88:91],v[164:167]
s_add_u32 m0,s86,4096
v_exp_f32 v127,v127
v_mfma_f32_16x16x32_bf16 v[172:175],a[232:235],v[80:83],v[172:175]
v_cvt_pk_bf16_f32 v120,v120,v121
v_cvt_pk_bf16_f32 v121,v122,v123
v_mfma_f32_16x16x32_bf16 v[172:175],a[236:239],v[88:91],v[172:175]
v_cvt_pk_bf16_f32 v122,v124,v125
v_cvt_pk_bf16_f32 v123,v126,v127
v_mfma_f32_16x16x32_bf16 v[180:183],a[240:243],v[80:83],v[180:183]
buffer_load_dwordx4 v7,s[48:51],s83 offen lds
v_mfma_f32_16x16x32_bf16 v[180:183],a[244:247],v[88:91],v[180:183]
v_cmp_nlt_f32 s[56:57],v52,s73
v_sub_u32 v2,v1,v2
v_mfma_f32_16x16x32_bf16 v[188:191],a[248:251],v[80:83],v[188:191]
s_or_b64 s[54:55],s[54:55],s[56:57]
v_accvgpr_read_b32 v8,a0
v_accvgpr_read_b32 v9,a1
v_mfma_f32_16x16x32_bf16 v[188:191],a[252:255],v[88:91],v[188:191]
s_add_u32 m0,s86,5120
v_accvgpr_read_b32 v10,a2
v_accvgpr_read_b32 v11,a3
v_mfma_f32_16x16x32_bf16 v[56:59],v[28:31],v[96:99],v[56:59]
v_accvgpr_read_b32 v12,a4
v_accvgpr_read_b32 v13,a5
v_mfma_f32_16x16x32_bf16 v[56:59],v[28:31],v[104:107],v[56:59]
v_accvgpr_read_b32 v14,a6
v_accvgpr_read_b32 v15,a7
v_mfma_f32_16x16x32_bf16 v[196:199],a[192:195],v[96:99],v[196:199]
buffer_load_dwordx4 v24,s[48:51],s84 offen lds
v_mfma_f32_16x16x32_bf16 v[196:199],a[196:199],v[104:107],v[196:199]
v_mul_f32 v8,v8,v49
v_mul_f32 v9,v9,v49
v_mfma_f32_16x16x32_bf16 v[204:207],a[200:203],v[96:99],v[204:207]
v_cmp_nlt_f32 s[56:57],v56,s73
v_mul_f32 v10,v10,v49
v_mfma_f32_16x16x32_bf16 v[204:207],a[204:207],v[104:107],v[204:207]
s_or_b64 s[54:55],s[54:55],s[56:57]
v_mul_f32 v11,v11,v49
v_mul_f32 v12,v12,v49
v_mfma_f32_16x16x32_bf16 v[212:215],a[208:211],v[96:99],v[212:215]
v_mul_f32 v13,v13,v49
v_mul_f32 v14,v14,v49
v_mfma_f32_16x16x32_bf16 v[212:215],a[212:215],v[104:107],v[212:215]
v_mul_f32 v15,v15,v49
v_cvt_pk_bf16_f32 v32,v8,v9
v_mfma_f32_16x16x32_bf16 v[220:223],a[216:219],v[96:99],v[220:223]
buffer_load_dwordx4 v25,s[48:51],s84 offen offset:1024 lds
v_mfma_f32_16x16x32_bf16 v[220:223],a[220:223],v[104:107],v[220:223]
v_cvt_pk_bf16_f32 v33,v10,v11
v_cvt_pk_bf16_f32 v34,v12,v13
v_mfma_f32_16x16x32_bf16 v[228:231],a[224:227],v[96:99],v[228:231]
v_cvt_pk_bf16_f32 v35,v14,v15
v_accvgpr_read_b32 v16,a8
v_mfma_f32_16x16x32_bf16 v[228:231],a[228:231],v[104:107],v[228:231]
v_accvgpr_read_b32 v17,a9
v_accvgpr_read_b32 v18,a10
v_mfma_f32_16x16x32_bf16 v[236:239],a[232:235],v[96:99],v[236:239]
v_accvgpr_read_b32 v19,a11
v_accvgpr_read_b32 v20,a12
v_mfma_f32_16x16x32_bf16 v[236:239],a[236:239],v[104:107],v[236:239]
v_accvgpr_read_b32 v21,a13
v_accvgpr_read_b32 v22,a14
v_mfma_f32_16x16x32_bf16 v[244:247],a[240:243],v[96:99],v[244:247]
buffer_load_dwordx4 v26,s[48:51],s84 offen offset:2048 lds
v_mfma_f32_16x16x32_bf16 v[244:247],a[244:247],v[104:107],v[244:247]
v_accvgpr_read_b32 v23,a15
v_mul_f32 v16,v16,v49
v_mfma_f32_16x16x32_bf16 v[252:255],a[248:251],v[96:99],v[252:255]
v_mul_f32 v17,v17,v49
v_mul_f32 v18,v18,v49
v_mfma_f32_16x16x32_bf16 v[252:255],a[252:255],v[104:107],v[252:255]
v_mul_f32 v19,v19,v49
v_mul_f32 v20,v20,v49
v_mfma_f32_16x16x32_bf16 v[60:63],v[28:31],v[112:115],v[60:63]
v_mul_f32 v21,v21,v49
v_mul_f32 v22,v22,v49
v_mfma_f32_16x16x32_bf16 v[60:63],v[28:31],v[120:123],v[60:63]
v_mul_f32 v23,v23,v49
v_cvt_pk_bf16_f32 v36,v16,v17
v_mfma_f32_16x16x32_bf16 a[32:35],a[192:195],v[112:115],a[32:35]
buffer_load_dwordx4 v27,s[48:51],s84 offen offset:3072 lds
v_mfma_f32_16x16x32_bf16 a[32:35],a[196:199],v[120:123],a[32:35]
v_cvt_pk_bf16_f32 v37,v18,v19
v_cvt_pk_bf16_f32 v38,v20,v21
v_mfma_f32_16x16x32_bf16 a[36:39],a[200:203],v[112:115],a[36:39]
v_cmp_nlt_f32 s[56:57],v60,s73
v_cvt_pk_bf16_f32 v39,v22,v23
v_mfma_f32_16x16x32_bf16 a[36:39],a[204:207],v[120:123],a[36:39]
s_add_u32 m0,s86,9216
v_accvgpr_read_b32 v8,a16
v_accvgpr_read_b32 v9,a17
v_mfma_f32_16x16x32_bf16 a[40:43],a[208:211],v[112:115],a[40:43]
s_or_b64 s[54:55],s[54:55],s[56:57]
v_accvgpr_read_b32 v10,a18
v_accvgpr_read_b32 v11,a19
v_mfma_f32_16x16x32_bf16 a[40:43],a[212:215],v[120:123],a[40:43]
v_accvgpr_read_b32 v12,a20
v_accvgpr_read_b32 v13,a21
v_mfma_f32_16x16x32_bf16 a[44:47],a[216:219],v[112:115],a[44:47]
buffer_load_dwordx4 v7,s[48:51],s84 offen lds
v_mfma_f32_16x16x32_bf16 a[44:47],a[220:223],v[120:123],a[44:47]
v_accvgpr_read_b32 v14,a22
v_accvgpr_read_b32 v15,a23
v_mfma_f32_16x16x32_bf16 a[48:51],a[224:227],v[112:115],a[48:51]
v_mul_f32 v8,v8,v49
v_mul_f32 v9,v9,v49
v_mfma_f32_16x16x32_bf16 a[48:51],a[228:231],v[120:123],a[48:51]
v_mul_f32 v10,v10,v49
v_mul_f32 v11,v11,v49
v_mfma_f32_16x16x32_bf16 a[52:55],a[232:235],v[112:115],a[52:55]
v_mul_f32 v12,v12,v49
v_mul_f32 v13,v13,v49
v_mfma_f32_16x16x32_bf16 a[52:55],a[236:239],v[120:123],a[52:55]
v_mul_f32 v14,v14,v49
v_mul_f32 v15,v15,v49
v_mfma_f32_16x16x32_bf16 a[56:59],a[240:243],v[112:115],a[56:59]
v_cvt_pk_bf16_f32 v40,v8,v9
v_cvt_pk_bf16_f32 v41,v10,v11
v_mfma_f32_16x16x32_bf16 a[56:59],a[244:247],v[120:123],a[56:59]
v_cvt_pk_bf16_f32 v42,v12,v13
v_cvt_pk_bf16_f32 v43,v14,v15
v_mfma_f32_16x16x32_bf16 a[60:63],a[248:251],v[112:115],a[60:63]
ds_read_b64_tr_b16 v[64:65],v1
ds_read_b64_tr_b16 v[66:67],v1 offset:5120
v_mfma_f32_16x16x32_bf16 a[60:63],a[252:255],v[120:123],a[60:63]
ds_read_b64_tr_b16 v[68:69],v1 offset:10240
v_max_f32 v53,0x00800000,v52
v_rcp_f32 v53,v53
v_max_f32 v57,0x00800000,v56
v_rcp_f32 v57,v57
v_max_f32 v61,0x00800000,v60
v_rcp_f32 v61,v61
ds_read_b64_tr_b16 v[70:71],v1 offset:15360
ds_read_b64_tr_b16 v[72:73],v1 offset:20480
ds_read_b64_tr_b16 v[74:75],v1 offset:25600
ds_read_b64_tr_b16 v[76:77],v1 offset:30720
ds_read_b64_tr_b16 v[78:79],v1 offset:35840
ds_read_b64_tr_b16 v[80:81],v2 offset:32
ds_read_b64_tr_b16 v[82:83],v2 offset:5152
ds_read_b64_tr_b16 v[84:85],v2 offset:10272
ds_read_b64_tr_b16 v[86:87],v2 offset:15392
ds_read_b64_tr_b16 v[88:89],v2 offset:20512
ds_read_b64_tr_b16 v[90:91],v2 offset:25632
ds_read_b64_tr_b16 v[92:93],v2 offset:30752
ds_read_b64_tr_b16 v[94:95],v2 offset:35872
s_cmp_lg_u64 s[54:55],0
s_cbranch_scc1 L_slow1_%=
L_ok1_%=:
s_cmp_ge_u32 s63,2
s_cbranch_scc1 L_GS_%=
L_gbF_%=:
s_waitcnt lgkmcnt(14)
v_mfma_f32_16x16x32_bf16 a[192:195],v[64:67],a[68:71],0
buffer_store_dwordx4 v[32:35],v6,s[40:43],s79 offen sc1
s_waitcnt lgkmcnt(12)
v_mfma_f32_16x16x32_bf16 a[192:195],v[68:71],a[76:79],a[192:195]
s_lshl_b32 s91,s63,8
v_bfe_u32 v3,v6,4,2
v_min_u32 v3,2,v3
s_waitcnt lgkmcnt(10)
v_mfma_f32_16x16x32_bf16 a[192:195],v[72:75],a[84:87],a[192:195]
s_add_u32 s87,s64,1
v_add3_u32 v3,v3,s64,1
v_and_b32 v3,3,v3
s_waitcnt lgkmcnt(8)
v_mfma_f32_16x16x32_bf16 a[192:195],v[76:79],a[92:95],a[192:195]
s_and_b32 s87,s87,3
ds_read_b64_tr_b16 v[96:97],v1 offset:64
v_lshlrev_b32 v3,6,v3
v_mfma_f32_16x16x32_bf16 a[196:199],v[76:79],a[124:127],0
s_lshl_b32 s87,s87,1
ds_read_b64_tr_b16 v[98:99],v1 offset:5184
ds_read_b64_tr_b16 v[100:101],v1 offset:10304
v_mfma_f32_16x16x32_bf16 a[196:199],v[72:75],a[116:119],a[196:199]
s_lshl_b32 s87,s87,16
ds_read_b64_tr_b16 v[102:103],v1 offset:15424
v_lshlrev_b32 v29,10,v6
v_mfma_f32_16x16x32_bf16 a[196:199],v[68:71],a[108:111],a[196:199]
s_add_u32 s88,s87,4096
ds_read_b64_tr_b16 v[104:105],v1 offset:20544
v_accvgpr_read_b32 v8,a24
v_mfma_f32_16x16x32_bf16 a[196:199],v[64:67],a[100:103],a[196:199]
s_add_u32 s89,s87,8192
ds_read_b64_tr_b16 v[106:107],v1 offset:25664
v_accvgpr_read_b32 v9,a25
v_mfma_f32_16x16x32_bf16 a[200:203],v[64:67],a[64:67],0
s_add_u32 s90,s87,12288
ds_read_b64_tr_b16 v[108:109],v1 offset:30784
ds_read_b64_tr_b16 v[110:111],v1 offset:35904
v_mfma_f32_16x16x32_bf16 a[200:203],v[68:71],a[72:75],a[200:203]
s_add_u32 m0,s86,122880
ds_read_b64_tr_b16 v[112:113],v2 offset:96
v_accvgpr_read_b32 v10,a26
v_mfma_f32_16x16x32_bf16 a[200:203],v[72:75],a[80:83],a[200:203]
s_add_u32 s83,s64,3
ds_read_b64_tr_b16 v[114:115],v2 offset:5216
v_accvgpr_read_b32 v11,a27
v_mfma_f32_16x16x32_bf16 a[200:203],v[76:79],a[88:91],a[200:203]
s_and_b32 s83,s83,3
ds_read_b64_tr_b16 v[116:117],v2 offset:10336
v_accvgpr_read_b32 v12,a28
v_mfma_f32_16x16x32_bf16 a[204:207],v[76:79],a[120:123],0
)A"
R"A(s_lshl_b32 s83,s83,1
ds_read_b64_tr_b16 v[118:119],v2 offset:15456
ds_read_b64_tr_b16 v[120:121],v2 offset:20576
v_mfma_f32_16x16x32_bf16 a[204:207],v[72:75],a[112:115],a[204:207]
s_mul_i32 s83,s83,163840
ds_read_b64_tr_b16 v[122:123],v2 offset:25696
v_accvgpr_read_b32 v13,a29
v_mfma_f32_16x16x32_bf16 a[204:207],v[68:71],a[104:107],a[204:207]
s_add_u32 s83,s83,s85
ds_read_b64_tr_b16 v[124:125],v2 offset:30816
v_accvgpr_read_b32 v14,a30
v_mfma_f32_16x16x32_bf16 a[204:207],v[64:67],a[96:99],a[204:207]
buffer_store_dwordx4 v[36:39],v6,s[40:43],s80 offen sc1
s_waitcnt lgkmcnt(9)
v_mfma_f32_16x16x32_bf16 a[208:211],v[80:83],a[68:71],0
s_add_u32 s84,s83,20480
ds_read_b64_tr_b16 v[126:127],v2 offset:35936
ds_read_b64_tr_b16 v[64:65],v1 offset:128
v_mfma_f32_16x16x32_bf16 a[208:211],v[84:87],a[76:79],a[208:211]
s_lshl_b32 s66,s61,2
ds_read_b64_tr_b16 v[66:67],v1 offset:5248
v_accvgpr_read_b32 v15,a31
v_mfma_f32_16x16x32_bf16 a[208:211],v[88:91],a[84:87],a[208:211]
s_add_u32 s66,s66,s64
ds_read_b64_tr_b16 v[68:69],v1 offset:10368
v_mul_f32 v8,v8,v49
v_mfma_f32_16x16x32_bf16 a[208:211],v[92:95],a[92:95],a[208:211]
s_mul_i32 s66,s66,262144
ds_read_b64_tr_b16 v[70:71],v1 offset:15488
v_mul_f32 v9,v9,v49
v_mfma_f32_16x16x32_bf16 a[212:215],v[92:95],a[124:127],0
s_waitcnt vmcnt(21)
buffer_load_dwordx2 v[4:5],v3,s[44:47],s91 offen offset:256 sc1
v_mfma_f32_16x16x32_bf16 a[212:215],v[88:91],a[116:119],a[212:215]
s_add_u32 s66,s66,134217728
ds_read_b64_tr_b16 v[72:73],v1 offset:20608
ds_read_b64_tr_b16 v[74:75],v1 offset:25728
v_mfma_f32_16x16x32_bf16 a[212:215],v[84:87],a[108:111],a[212:215]
s_mov_b32 s38,262144
ds_read_b64_tr_b16 v[76:77],v1 offset:30848
v_mul_f32 v10,v10,v49
v_mfma_f32_16x16x32_bf16 a[212:215],v[80:83],a[100:103],a[212:215]
s_mov_b32 s39,0x20000
ds_read_b64_tr_b16 v[78:79],v1 offset:35968
v_mul_f32 v11,v11,v49
v_mfma_f32_16x16x32_bf16 a[216:219],v[80:83],a[64:67],0
s_add_u32 s74,s63,1
v_mul_f32 v12,v12,v49
v_mul_f32 v13,v13,v49
v_mfma_f32_16x16x32_bf16 a[216:219],v[84:87],a[72:75],a[216:219]
s_and_b32 s74,s74,3
v_mul_f32 v14,v14,v49
v_mul_f32 v15,v15,v49
v_mfma_f32_16x16x32_bf16 a[216:219],v[88:91],a[80:83],a[216:219]
L_pd13_%=:
s_cbranch_execz L_trt1_%=
s_lshl_b32 s74,s74,2
v_cvt_pk_bf16_f32 v44,v8,v9
v_cvt_pk_bf16_f32 v45,v10,v11
v_mfma_f32_16x16x32_bf16 a[216:219],v[92:95],a[88:91],a[216:219]
s_add_u32 s74,s74,s60
v_cvt_pk_bf16_f32 v46,v12,v13
v_cvt_pk_bf16_f32 v47,v14,v15
v_mfma_f32_16x16x32_bf16 a[220:223],v[92:95],a[120:123],0
s_mul_i32 s74,s74,16384
ds_read_b64_tr_b16 v[92:93],v2 offset:30880
ds_read_b64_tr_b16 v[94:95],v2 offset:36000
v_mfma_f32_16x16x32_bf16 a[220:223],v[88:91],a[112:115],a[220:223]
s_add_u32 s74,s74,8192
ds_read_b64_tr_b16 v[88:89],v2 offset:20640
v_mul_f32 v132,v132,v53
v_mfma_f32_16x16x32_bf16 a[220:223],v[84:87],a[104:107],a[220:223]
buffer_store_dwordx4 v[40:43],v6,s[40:43],s81 offen sc1
v_mfma_f32_16x16x32_bf16 a[220:223],v[80:83],a[96:99],a[220:223]
s_add_u32 s75,s63,2
ds_read_b64_tr_b16 v[80:81],v2 offset:160
ds_read_b64_tr_b16 v[82:83],v2 offset:5280
v_mfma_f32_16x16x32_bf16 a[224:227],v[96:99],a[68:71],0
s_and_b32 s75,s75,3
ds_read_b64_tr_b16 v[84:85],v2 offset:10400
v_mul_f32 v133,v133,v53
v_mfma_f32_16x16x32_bf16 a[224:227],v[100:103],a[76:79],a[224:227]
s_lshl_b32 s75,s75,2
ds_read_b64_tr_b16 v[86:87],v2 offset:15520
v_mul_f32 v134,v134,v53
v_mfma_f32_16x16x32_bf16 a[224:227],v[104:107],a[84:87],a[224:227]
s_add_u32 s75,s75,s60
ds_read_b64_tr_b16 v[90:91],v2 offset:25760
v_mul_f32 v135,v135,v53
s_waitcnt lgkmcnt(8)
v_mfma_f32_16x16x32_bf16 a[224:227],v[108:111],a[92:95],a[224:227]
s_mul_i32 s75,s75,16384
v_mul_f32 v140,v140,v53
v_mul_f32 v141,v141,v53
v_mfma_f32_16x16x32_bf16 a[228:231],v[108:111],a[124:127],0
s_add_u32 s75,s75,4096
v_mul_f32 v142,v142,v53
v_mul_f32 v143,v143,v53
v_mfma_f32_16x16x32_bf16 a[228:231],v[104:107],a[116:119],a[228:231]
s_add_u32 s76,s63,3
v_cvt_pk_bf16_f32 v132,v132,v133
v_cvt_pk_bf16_f32 v133,v134,v135
v_mfma_f32_16x16x32_bf16 a[228:231],v[100:103],a[108:111],a[228:231]
s_and_b32 s76,s76,3
v_cvt_pk_bf16_f32 v134,v140,v141
v_cvt_pk_bf16_f32 v135,v142,v143
v_mfma_f32_16x16x32_bf16 a[228:231],v[96:99],a[100:103],a[228:231]
s_lshl_b32 s76,s76,2
v_mul_f32 v148,v148,v53
v_mul_f32 v149,v149,v53
v_mfma_f32_16x16x32_bf16 a[232:235],v[96:99],a[64:67],0
s_add_u32 s76,s76,s60
v_mul_f32 v150,v150,v53
v_mul_f32 v151,v151,v53
v_mfma_f32_16x16x32_bf16 a[232:235],v[100:103],a[72:75],a[232:235]
s_mul_i32 s76,s76,16384
v_mul_f32 v156,v156,v53
v_mul_f32 v157,v157,v53
v_mfma_f32_16x16x32_bf16 a[232:235],v[104:107],a[80:83],a[232:235]
s_add_u32 s76,s76,0
v_mul_f32 v158,v158,v53
v_mul_f32 v159,v159,v53
v_mfma_f32_16x16x32_bf16 a[232:235],v[108:111],a[88:91],a[232:235]
s_lshl_b32 s67,s63,10
v_cvt_pk_bf16_f32 v148,v148,v149
v_cvt_pk_bf16_f32 v149,v150,v151
v_mfma_f32_16x16x32_bf16 a[236:239],v[108:111],a[120:123],0
s_mov_b32 s26,307200
ds_read_b64_tr_b16 v[108:109],v1 offset:30912
ds_read_b64_tr_b16 v[110:111],v1 offset:36032
v_mfma_f32_16x16x32_bf16 a[236:239],v[104:107],a[112:115],a[236:239]
buffer_store_dwordx4 v[44:47],v6,s[40:43],s82 offen sc1
v_mfma_f32_16x16x32_bf16 a[236:239],v[100:103],a[104:107],a[236:239]
s_mov_b32 s27,0x20000
ds_read_b64_tr_b16 v[100:101],v1 offset:10432
ds_read_b64_tr_b16 v[102:103],v1 offset:15552
v_mfma_f32_16x16x32_bf16 a[236:239],v[96:99],a[96:99],a[236:239]
s_lshl_b32 s77,s63,2
ds_read_b64_tr_b16 v[96:97],v1 offset:192
v_cvt_pk_bf16_f32 v150,v156,v157
v_mfma_f32_16x16x32_bf16 a[240:243],v[112:115],a[68:71],0
s_add_u32 s77,s77,s60
ds_read_b64_tr_b16 v[98:99],v1 offset:5312
v_cvt_pk_bf16_f32 v151,v158,v159
v_mfma_f32_16x16x32_bf16 a[240:243],v[116:119],a[76:79],a[240:243]
s_mul_i32 s77,s77,16384
ds_read_b64_tr_b16 v[104:105],v1 offset:20672
v_mul_f32 v164,v164,v53
v_mfma_f32_16x16x32_bf16 a[240:243],v[120:123],a[84:87],a[240:243]
s_add_u32 s65,s77,0
ds_read_b64_tr_b16 v[106:107],v1 offset:25792
v_mul_f32 v165,v165,v53
v_mfma_f32_16x16x32_bf16 a[240:243],v[124:127],a[92:95],a[240:243]
s_add_u32 s68,s77,12288
v_mul_f32 v166,v166,v53
v_mul_f32 v167,v167,v53
v_mfma_f32_16x16x32_bf16 a[244:247],v[124:127],a[124:127],0
v_mul_f32 v172,v172,v53
v_mul_f32 v173,v173,v53
v_mfma_f32_16x16x32_bf16 a[244:247],v[120:123],a[116:119],a[244:247]
v_mul_f32 v174,v174,v53
v_mul_f32 v175,v175,v53
v_mfma_f32_16x16x32_bf16 a[244:247],v[116:119],a[108:111],a[244:247]
v_cvt_pk_bf16_f32 v164,v164,v165
v_cvt_pk_bf16_f32 v165,v166,v167
v_mfma_f32_16x16x32_bf16 a[244:247],v[112:115],a[100:103],a[244:247]
v_cvt_pk_bf16_f32 v166,v172,v173
v_cvt_pk_bf16_f32 v167,v174,v175
v_mfma_f32_16x16x32_bf16 a[248:251],v[112:115],a[64:67],0
v_mul_f32 v180,v180,v53
v_mul_f32 v181,v181,v53
v_mfma_f32_16x16x32_bf16 a[248:251],v[116:119],a[72:75],a[248:251]
v_mul_f32 v182,v182,v53
v_mul_f32 v183,v183,v53
v_mfma_f32_16x16x32_bf16 a[248:251],v[120:123],a[80:83],a[248:251]
v_mul_f32 v188,v188,v53
v_mul_f32 v189,v189,v53
v_mfma_f32_16x16x32_bf16 a[248:251],v[124:127],a[88:91],a[248:251]
v_mul_f32 v190,v190,v53
v_mul_f32 v191,v191,v53
v_mfma_f32_16x16x32_bf16 a[252:255],v[124:127],a[120:123],0
buffer_load_dword v30,v29,s[8:11],s7 offen
v_mfma_f32_16x16x32_bf16 a[252:255],v[120:123],a[112:115],a[252:255]
ds_read_b64_tr_b16 v[120:121],v2 offset:20704
ds_read_b64_tr_b16 v[122:123],v2 offset:25824
v_mfma_f32_16x16x32_bf16 a[252:255],v[116:119],a[104:107],a[252:255]
buffer_store_dwordx4 v[132:135],v6,s[40:43],s79 offen offset:1024 sc1
v_mfma_f32_16x16x32_bf16 a[252:255],v[112:115],a[96:99],a[252:255]
ds_read_b64_tr_b16 v[112:113],v2 offset:224
ds_read_b64_tr_b16 v[114:115],v2 offset:5344
v_mfma_f32_16x16x32_bf16 a[0:3],v[64:67],a[68:71],0
ds_read_b64_tr_b16 v[116:117],v2 offset:10464
v_cvt_pk_bf16_f32 v180,v180,v181
v_mfma_f32_16x16x32_bf16 a[0:3],v[68:71],a[76:79],a[0:3]
ds_read_b64_tr_b16 v[118:119],v2 offset:15584
v_cvt_pk_bf16_f32 v181,v182,v183
v_mfma_f32_16x16x32_bf16 a[0:3],v[72:75],a[84:87],a[0:3]
ds_read_b64_tr_b16 v[124:125],v2 offset:30944
v_cvt_pk_bf16_f32 v182,v188,v189
v_mfma_f32_16x16x32_bf16 a[0:3],v[76:79],a[92:95],a[0:3]
ds_read_b64_tr_b16 v[126:127],v2 offset:36064
v_cvt_pk_bf16_f32 v183,v190,v191
v_mfma_f32_16x16x32_bf16 a[4:7],v[76:79],a[124:127],0
v_mul_f32 v196,v196,v57
v_mul_f32 v197,v197,v57
v_mfma_f32_16x16x32_bf16 a[4:7],v[72:75],a[116:119],a[4:7]
v_mul_f32 v198,v198,v57
v_mul_f32 v199,v199,v57
v_mfma_f32_16x16x32_bf16 a[4:7],v[68:71],a[108:111],a[4:7]
v_mul_f32 v204,v204,v57
v_mul_f32 v205,v205,v57
v_mfma_f32_16x16x32_bf16 a[4:7],v[64:67],a[100:103],a[4:7]
v_mul_f32 v206,v206,v57
v_mul_f32 v207,v207,v57
v_mfma_f32_16x16x32_bf16 a[8:11],v[64:67],a[64:67],0
v_cvt_pk_bf16_f32 v196,v196,v197
v_cvt_pk_bf16_f32 v197,v198,v199
v_mfma_f32_16x16x32_bf16 a[8:11],v[68:71],a[72:75],a[8:11]
v_cvt_pk_bf16_f32 v198,v204,v205
v_cvt_pk_bf16_f32 v199,v206,v207
v_mfma_f32_16x16x32_bf16 a[8:11],v[72:75],a[80:83],a[8:11]
v_mul_f32 v212,v212,v57
v_mul_f32 v213,v213,v57
v_mfma_f32_16x16x32_bf16 a[8:11],v[76:79],a[88:91],a[8:11]
v_mul_f32 v214,v214,v57
v_mul_f32 v215,v215,v57
v_mfma_f32_16x16x32_bf16 a[12:15],v[76:79],a[120:123],0
ds_read_b64_tr_b16 v[76:77],v1 offset:30976
ds_read_b64_tr_b16 v[78:79],v1 offset:36096
v_mfma_f32_16x16x32_bf16 a[12:15],v[72:75],a[112:115],a[12:15]
buffer_store_dwordx4 v[148:151],v6,s[40:43],s80 offen offset:1024 sc1
v_mfma_f32_16x16x32_bf16 a[12:15],v[68:71],a[104:107],a[12:15]
ds_read_b64_tr_b16 v[68:69],v1 offset:10496
ds_read_b64_tr_b16 v[70:71],v1 offset:15616
v_mfma_f32_16x16x32_bf16 a[12:15],v[64:67],a[96:99],a[12:15]
s_waitcnt lgkmcnt(4)
ds_read_b64_tr_b16 v[64:65],v1 offset:256
v_mul_f32 v220,v220,v57
v_mfma_f32_16x16x32_bf16 a[16:19],v[80:83],a[68:71],0
ds_read_b64_tr_b16 v[66:67],v1 offset:5376
v_mul_f32 v221,v221,v57
v_mfma_f32_16x16x32_bf16 a[16:19],v[84:87],a[76:79],a[16:19]
ds_read_b64_tr_b16 v[72:73],v1 offset:20736
v_mul_f32 v222,v222,v57
v_mfma_f32_16x16x32_bf16 a[16:19],v[88:91],a[84:87],a[16:19]
ds_read_b64_tr_b16 v[74:75],v1 offset:25856
v_add_u32 v1,0xffff6000,v1
v_mfma_f32_16x16x32_bf16 a[16:19],v[92:95],a[92:95],a[16:19]
v_mul_f32 v223,v223,v57
v_cvt_pk_bf16_f32 v212,v212,v213
v_mfma_f32_16x16x32_bf16 a[20:23],v[92:95],a[124:127],0
v_cvt_pk_bf16_f32 v213,v214,v215
v_cvt_pk_bf16_f32 v214,v220,v221
v_mfma_f32_16x16x32_bf16 a[20:23],v[88:91],a[116:119],a[20:23]
v_cvt_pk_bf16_f32 v215,v222,v223
v_mul_f32 v228,v228,v57
v_mfma_f32_16x16x32_bf16 a[20:23],v[84:87],a[108:111],a[20:23]
v_mul_f32 v229,v229,v57
v_mul_f32 v230,v230,v57
v_mfma_f32_16x16x32_bf16 a[20:23],v[80:83],a[100:103],a[20:23]
v_mul_f32 v231,v231,v57
v_mul_f32 v236,v236,v57
v_mfma_f32_16x16x32_bf16 a[24:27],v[80:83],a[64:67],0
v_mul_f32 v237,v237,v57
v_mul_f32 v238,v238,v57
v_mfma_f32_16x16x32_bf16 a[24:27],v[84:87],a[72:75],a[24:27]
v_mul_f32 v239,v239,v57
v_cvt_pk_bf16_f32 v228,v228,v229
v_mfma_f32_16x16x32_bf16 a[24:27],v[88:91],a[80:83],a[24:27]
s_waitcnt vmcnt(5)
v_cmp_ne_u32 s[56:57],s52,v4
v_cmp_ne_u32 s[58:59],s53,v5
v_mfma_f32_16x16x32_bf16 a[24:27],v[92:95],a[88:91],a[24:27]
s_or_b64 s[56:57],s[56:57],s[58:59]
s_cbranch_scc1 L_fr1_%=
L_fo1_%=:
v_cvt_pk_bf16_f32 v229,v230,v231
v_mfma_f32_16x16x32_bf16 a[28:31],v[92:95],a[120:123],0
buffer_store_dwordx4 v[164:167],v6,s[40:43],s81 offen offset:1024 sc1
v_mfma_f32_16x16x32_bf16 a[28:31],v[88:91],a[112:115],a[28:31]
s_lshl_b32 s91,s63,8
ds_read_b64_tr_b16 v[88:89],v2 offset:20768
ds_read_b64_tr_b16 v[90:91],v2 offset:25888
v_mfma_f32_16x16x32_bf16 a[28:31],v[84:87],a[104:107],a[28:31]
buffer_load_dwordx4 a[128:131],v6,s[28:31],s87 offen sc1
v_mfma_f32_16x16x32_bf16 a[28:31],v[80:83],a[96:99],a[28:31]
ds_read_b64_tr_b16 v[80:81],v2 offset:288
ds_read_b64_tr_b16 v[82:83],v2 offset:5408
v_mfma_f32_16x16x32_bf16 v[128:131],v[96:99],a[68:71],0
ds_read_b64_tr_b16 v[84:85],v2 offset:10528
v_cvt_pk_bf16_f32 v230,v236,v237
v_mfma_f32_16x16x32_bf16 v[128:131],v[100:103],a[76:79],v[128:131]
ds_read_b64_tr_b16 v[86:87],v2 offset:15648
v_cvt_pk_bf16_f32 v231,v238,v239
v_mfma_f32_16x16x32_bf16 v[128:131],v[104:107],a[84:87],v[128:131]
ds_read_b64_tr_b16 v[92:93],v2 offset:31008
v_mul_f32 v244,v244,v57
)A"
R"A(v_mfma_f32_16x16x32_bf16 v[128:131],v[108:111],a[92:95],v[128:131]
ds_read_b64_tr_b16 v[94:95],v2 offset:36128
v_add_u32 v2,0xffff6000,v2
v_mfma_f32_16x16x32_bf16 v[136:139],v[108:111],a[124:127],0
v_mul_f32 v245,v245,v57
v_mul_f32 v246,v246,v57
v_mfma_f32_16x16x32_bf16 v[136:139],v[104:107],a[116:119],v[136:139]
v_mul_f32 v247,v247,v57
v_mul_f32 v252,v252,v57
v_mfma_f32_16x16x32_bf16 v[136:139],v[100:103],a[108:111],v[136:139]
v_mul_f32 v253,v253,v57
v_mul_f32 v254,v254,v57
v_mfma_f32_16x16x32_bf16 v[136:139],v[96:99],a[100:103],v[136:139]
v_mul_f32 v255,v255,v57
v_cvt_pk_bf16_f32 v244,v244,v245
v_mfma_f32_16x16x32_bf16 v[144:147],v[96:99],a[64:67],0
v_cvt_pk_bf16_f32 v245,v246,v247
v_cvt_pk_bf16_f32 v246,v252,v253
v_mfma_f32_16x16x32_bf16 v[144:147],v[100:103],a[72:75],v[144:147]
buffer_load_dwordx4 a[144:147],v6,s[28:31],s88 offen sc1
v_mfma_f32_16x16x32_bf16 v[144:147],v[104:107],a[80:83],v[144:147]
v_cvt_pk_bf16_f32 v247,v254,v255
v_accvgpr_read_b32 v8,a40
v_mfma_f32_16x16x32_bf16 v[144:147],v[108:111],a[88:91],v[144:147]
buffer_store_dwordx4 v[180:183],v6,s[40:43],s82 offen offset:1024 sc1
v_mfma_f32_16x16x32_bf16 v[152:155],v[108:111],a[120:123],0
ds_read_b64_tr_b16 v[108:109],v1 offset:30720
ds_read_b64_tr_b16 v[110:111],v1 offset:35840
v_mfma_f32_16x16x32_bf16 v[152:155],v[104:107],a[112:115],v[152:155]
ds_read_b64_tr_b16 v[104:105],v1 offset:20480
v_accvgpr_read_b32 v9,a41
v_mfma_f32_16x16x32_bf16 v[152:155],v[100:103],a[104:107],v[152:155]
ds_read_b64_tr_b16 v[100:101],v1 offset:10240
v_accvgpr_read_b32 v10,a42
v_mfma_f32_16x16x32_bf16 v[152:155],v[96:99],a[96:99],v[152:155]
ds_read_b64_tr_b16 v[96:97],v1
v_accvgpr_read_b32 v11,a43
v_mfma_f32_16x16x32_bf16 v[160:163],v[112:115],a[68:71],0
ds_read_b64_tr_b16 v[98:99],v1 offset:5120
ds_read_b64_tr_b16 v[102:103],v1 offset:15360
v_mfma_f32_16x16x32_bf16 v[160:163],v[116:119],a[76:79],v[160:163]
ds_read_b64_tr_b16 v[106:107],v1 offset:25600
v_accvgpr_read_b32 v12,a44
v_mfma_f32_16x16x32_bf16 v[160:163],v[120:123],a[84:87],v[160:163]
v_accvgpr_read_b32 v13,a45
v_accvgpr_read_b32 v14,a46
v_mfma_f32_16x16x32_bf16 v[160:163],v[124:127],a[92:95],v[160:163]
v_accvgpr_read_b32 v15,a47
v_mul_f32 v8,v8,v61
v_mfma_f32_16x16x32_bf16 v[168:171],v[124:127],a[124:127],0
v_mul_f32 v9,v9,v61
v_mul_f32 v10,v10,v61
v_mfma_f32_16x16x32_bf16 v[168:171],v[120:123],a[116:119],v[168:171]
v_mul_f32 v11,v11,v61
v_mul_f32 v12,v12,v61
v_mfma_f32_16x16x32_bf16 v[168:171],v[116:119],a[108:111],v[168:171]
buffer_load_dwordx4 a[160:163],v6,s[28:31],s89 offen sc1
v_mfma_f32_16x16x32_bf16 v[168:171],v[112:115],a[100:103],v[168:171]
v_mul_f32 v13,v13,v61
v_mul_f32 v14,v14,v61
v_mfma_f32_16x16x32_bf16 v[176:179],v[112:115],a[64:67],0
v_mul_f32 v15,v15,v61
v_cvt_pk_bf16_f32 v156,v8,v9
v_mfma_f32_16x16x32_bf16 v[176:179],v[116:119],a[72:75],v[176:179]
v_cvt_pk_bf16_f32 v157,v10,v11
v_cvt_pk_bf16_f32 v158,v12,v13
v_mfma_f32_16x16x32_bf16 v[176:179],v[120:123],a[80:83],v[176:179]
buffer_store_dwordx4 v[196:199],v6,s[40:43],s79 offen offset:2048 sc1
v_mfma_f32_16x16x32_bf16 v[176:179],v[124:127],a[88:91],v[176:179]
v_cvt_pk_bf16_f32 v159,v14,v15
v_accvgpr_read_b32 v8,a56
v_mfma_f32_16x16x32_bf16 v[184:187],v[124:127],a[120:123],0
v_accvgpr_read_b32 v9,a57
v_accvgpr_read_b32 v10,a58
v_mfma_f32_16x16x32_bf16 v[184:187],v[120:123],a[112:115],v[184:187]
ds_read_b64_tr_b16 v[120:121],v2 offset:20512
ds_read_b64_tr_b16 v[122:123],v2 offset:25632
v_mfma_f32_16x16x32_bf16 v[184:187],v[116:119],a[104:107],v[184:187]
ds_read_b64_tr_b16 v[116:117],v2 offset:10272
v_accvgpr_read_b32 v11,a59
v_mfma_f32_16x16x32_bf16 v[184:187],v[112:115],a[96:99],v[184:187]
s_waitcnt lgkmcnt(3)
ds_read_b64_tr_b16 v[112:113],v2 offset:32
v_accvgpr_read_b32 v12,a60
v_mfma_f32_16x16x32_bf16 v[192:195],v[64:67],a[68:71],0
ds_read_b64_tr_b16 v[114:115],v2 offset:5152
v_accvgpr_read_b32 v13,a61
v_mfma_f32_16x16x32_bf16 v[192:195],v[68:71],a[76:79],v[192:195]
ds_read_b64_tr_b16 v[118:119],v2 offset:15392
v_accvgpr_read_b32 v14,a62
v_mfma_f32_16x16x32_bf16 v[192:195],v[72:75],a[84:87],v[192:195]
v_accvgpr_read_b32 v15,a63
v_mul_f32 v8,v8,v61
v_mfma_f32_16x16x32_bf16 v[192:195],v[76:79],a[92:95],v[192:195]
buffer_load_dwordx4 a[176:179],v6,s[28:31],s90 offen sc1
v_mfma_f32_16x16x32_bf16 v[200:203],v[76:79],a[124:127],0
v_mul_f32 v9,v9,v61
v_mul_f32 v10,v10,v61
v_mfma_f32_16x16x32_bf16 v[200:203],v[72:75],a[116:119],v[200:203]
v_mul_f32 v11,v11,v61
v_mul_f32 v12,v12,v61
v_mfma_f32_16x16x32_bf16 v[200:203],v[68:71],a[108:111],v[200:203]
v_mul_f32 v13,v13,v61
v_mul_f32 v14,v14,v61
v_mfma_f32_16x16x32_bf16 v[200:203],v[64:67],a[100:103],v[200:203]
v_mul_f32 v15,v15,v61
v_cvt_pk_bf16_f32 v188,v8,v9
v_mfma_f32_16x16x32_bf16 v[208:211],v[64:67],a[64:67],0
v_cvt_pk_bf16_f32 v189,v10,v11
v_cvt_pk_bf16_f32 v190,v12,v13
v_mfma_f32_16x16x32_bf16 v[208:211],v[68:71],a[72:75],v[208:211]
buffer_store_dwordx4 v[212:215],v6,s[40:43],s80 offen offset:2048 sc1
v_mfma_f32_16x16x32_bf16 v[208:211],v[72:75],a[80:83],v[208:211]
ds_read_b64_tr_b16 v[124:125],v2 offset:30752
v_cvt_pk_bf16_f32 v191,v14,v15
v_mfma_f32_16x16x32_bf16 v[208:211],v[76:79],a[88:91],v[208:211]
ds_read_b64_tr_b16 v[126:127],v2 offset:35872
v_accvgpr_read_b32 v16,a32
v_mfma_f32_16x16x32_bf16 v[216:219],v[76:79],a[120:123],0
v_accvgpr_read_b32 v17,a33
v_accvgpr_read_b32 v18,a34
v_mfma_f32_16x16x32_bf16 v[216:219],v[72:75],a[112:115],v[216:219]
v_accvgpr_read_b32 v19,a35
v_accvgpr_read_b32 v20,a36
v_mfma_f32_16x16x32_bf16 v[216:219],v[68:71],a[104:107],v[216:219]
v_accvgpr_read_b32 v21,a37
v_accvgpr_read_b32 v22,a38
v_mfma_f32_16x16x32_bf16 v[216:219],v[64:67],a[96:99],v[216:219]
v_accvgpr_read_b32 v23,a39
v_mul_f32 v16,v16,v61
v_mfma_f32_16x16x32_bf16 v[224:227],v[80:83],a[68:71],0
buffer_load_dwordx4 a[180:183],v6,s[28:31],s90 offen offset:1024 sc1
v_mfma_f32_16x16x32_bf16 v[224:227],v[84:87],a[76:79],v[224:227]
v_mul_f32 v17,v17,v61
v_mul_f32 v18,v18,v61
v_mfma_f32_16x16x32_bf16 v[224:227],v[88:91],a[84:87],v[224:227]
v_mul_f32 v19,v19,v61
v_mul_f32 v20,v20,v61
v_mfma_f32_16x16x32_bf16 v[224:227],v[92:95],a[92:95],v[224:227]
v_mul_f32 v21,v21,v61
v_mul_f32 v22,v22,v61
v_mfma_f32_16x16x32_bf16 v[232:235],v[92:95],a[124:127],0
v_mul_f32 v23,v23,v61
v_cvt_pk_bf16_f32 v140,v16,v17
v_mfma_f32_16x16x32_bf16 v[232:235],v[88:91],a[116:119],v[232:235]
v_cvt_pk_bf16_f32 v141,v18,v19
v_cvt_pk_bf16_f32 v142,v20,v21
v_mfma_f32_16x16x32_bf16 v[232:235],v[84:87],a[108:111],v[232:235]
v_cvt_pk_bf16_f32 v143,v22,v23
v_accvgpr_read_b32 v16,a48
v_mfma_f32_16x16x32_bf16 v[232:235],v[80:83],a[100:103],v[232:235]
v_accvgpr_read_b32 v17,a49
v_accvgpr_read_b32 v18,a50
v_mfma_f32_16x16x32_bf16 v[240:243],v[80:83],a[64:67],0
buffer_store_dwordx4 v[228:231],v6,s[40:43],s81 offen offset:2048 sc1
v_mfma_f32_16x16x32_bf16 v[240:243],v[84:87],a[72:75],v[240:243]
v_accvgpr_read_b32 v19,a51
v_accvgpr_read_b32 v20,a52
v_mfma_f32_16x16x32_bf16 v[240:243],v[88:91],a[80:83],v[240:243]
v_accvgpr_read_b32 v21,a53
v_accvgpr_read_b32 v22,a54
v_mfma_f32_16x16x32_bf16 v[240:243],v[92:95],a[88:91],v[240:243]
v_accvgpr_read_b32 v23,a55
v_mul_f32 v16,v16,v61
v_mfma_f32_16x16x32_bf16 v[248:251],v[92:95],a[120:123],0
v_mul_f32 v17,v17,v61
v_mul_f32 v18,v18,v61
v_mfma_f32_16x16x32_bf16 v[248:251],v[88:91],a[112:115],v[248:251]
buffer_load_dwordx4 a[164:167],v6,s[28:31],s89 offen offset:1024 sc1
v_mfma_f32_16x16x32_bf16 v[248:251],v[84:87],a[104:107],v[248:251]
v_mul_f32 v19,v19,v61
v_mul_f32 v20,v20,v61
v_mfma_f32_16x16x32_bf16 v[248:251],v[80:83],a[96:99],v[248:251]
v_mul_f32 v21,v21,v61
v_mul_f32 v22,v22,v61
v_mfma_f32_16x16x32_bf16 a[192:195],v[96:99],v[32:35],a[192:195]
v_mul_f32 v23,v23,v61
v_cvt_pk_bf16_f32 v172,v16,v17
v_mfma_f32_16x16x32_bf16 a[192:195],v[100:103],v[36:39],a[192:195]
v_cvt_pk_bf16_f32 v173,v18,v19
v_cvt_pk_bf16_f32 v174,v20,v21
v_mfma_f32_16x16x32_bf16 a[192:195],v[104:107],v[40:43],a[192:195]
ds_read_b64_tr_b16 v[64:65],v1 offset:64
v_cvt_pk_bf16_f32 v175,v22,v23
v_mfma_f32_16x16x32_bf16 a[192:195],v[108:111],v[44:47],a[192:195]
ds_read_b64_tr_b16 v[66:67],v1 offset:5184
ds_read_b64_tr_b16 v[68:69],v1 offset:10304
v_mfma_f32_16x16x32_bf16 a[196:199],v[108:111],v[180:183],a[196:199]
s_cmp_eq_u32 s60,3
s_cselect_b32 s69,0x40000000,0
ds_read_b64_tr_b16 v[70:71],v1 offset:15424
v_mfma_f32_16x16x32_bf16 a[196:199],v[104:107],v[164:167],a[196:199]
ds_read_b64_tr_b16 v[72:73],v1 offset:20544
v_add_u32 v28,s69,v6
v_mfma_f32_16x16x32_bf16 a[196:199],v[100:103],v[148:151],a[196:199]
s_lshl_b32 s69,s63,2
ds_read_b64_tr_b16 v[74:75],v1 offset:25664
v_mov_b32 v8,s52
v_mfma_f32_16x16x32_bf16 a[196:199],v[96:99],v[132:135],a[196:199]
buffer_store_dwordx4 v[244:247],v6,s[40:43],s82 offen offset:2048 sc1
v_mfma_f32_16x16x32_bf16 a[200:203],v[96:99],v[196:199],a[200:203]
ds_read_b64_tr_b16 v[76:77],v1 offset:30784
ds_read_b64_tr_b16 v[78:79],v1 offset:35904
v_mfma_f32_16x16x32_bf16 a[200:203],v[100:103],v[212:215],a[200:203]
ds_read_b64_tr_b16 v[80:81],v2 offset:96
v_mov_b32 v9,s53
v_mfma_f32_16x16x32_bf16 a[200:203],v[104:107],v[228:231],a[200:203]
buffer_load_dwordx4 a[148:151],v6,s[28:31],s88 offen offset:1024 sc1
v_mfma_f32_16x16x32_bf16 a[200:203],v[108:111],v[244:247],a[200:203]
ds_read_b64_tr_b16 v[82:83],v2 offset:5216
ds_read_b64_tr_b16 v[84:85],v2 offset:10336
v_mfma_f32_16x16x32_bf16 a[204:207],v[108:111],v[188:191],a[204:207]
ds_read_b64_tr_b16 v[86:87],v2 offset:15456
v_lshlrev_b32 v10,20,v6
v_mfma_f32_16x16x32_bf16 a[204:207],v[104:107],v[172:175],a[204:207]
ds_read_b64_tr_b16 v[88:89],v2 offset:20576
v_bfe_u32 v3,v6,4,2
v_mfma_f32_16x16x32_bf16 a[204:207],v[100:103],v[156:159],a[204:207]
ds_read_b64_tr_b16 v[90:91],v2 offset:25696
v_min_u32 v3,2,v3
v_mfma_f32_16x16x32_bf16 a[204:207],v[96:99],v[140:143],a[204:207]
s_waitcnt lgkmcnt(8)
ds_read_b64_tr_b16 v[92:93],v2 offset:30816
ds_read_b64_tr_b16 v[94:95],v2 offset:35936
v_mfma_f32_16x16x32_bf16 a[208:211],v[112:115],v[32:35],a[208:211]
ds_read_b64_tr_b16 v[96:97],v1 offset:128
v_add3_u32 v3,v3,s64,1
v_mfma_f32_16x16x32_bf16 a[208:211],v[116:119],v[36:39],a[208:211]
ds_read_b64_tr_b16 v[98:99],v1 offset:5248
v_and_b32 v3,3,v3
v_mfma_f32_16x16x32_bf16 a[208:211],v[120:123],v[40:43],a[208:211]
ds_read_b64_tr_b16 v[100:101],v1 offset:10368
v_lshlrev_b32 v3,6,v3
v_mfma_f32_16x16x32_bf16 a[208:211],v[124:127],v[44:47],a[208:211]
ds_read_b64_tr_b16 v[102:103],v1 offset:15488
ds_read_b64_tr_b16 v[104:105],v1 offset:20608
v_mfma_f32_16x16x32_bf16 a[212:215],v[124:127],v[180:183],a[212:215]
ds_read_b64_tr_b16 v[106:107],v1 offset:25728
v_bfe_u32 v56,v6,6,2
v_mfma_f32_16x16x32_bf16 a[212:215],v[120:123],v[164:167],a[212:215]
ds_read_b64_tr_b16 v[108:109],v1 offset:30848
v_lshrrev_b32 v57,8,v6
v_mfma_f32_16x16x32_bf16 a[212:215],v[116:119],v[148:151],a[212:215]
buffer_store_dwordx4 v[140:143],v28,s[40:43],s79 offen offset:3072 sc1
v_mfma_f32_16x16x32_bf16 a[212:215],v[112:115],v[132:135],a[212:215]
ds_read_b64_tr_b16 v[110:111],v1 offset:35968
v_cmp_eq_u32 s[56:57],v56,v57
v_mfma_f32_16x16x32_bf16 a[216:219],v[112:115],v[196:199],a[216:219]
buffer_load_dwordx4 a[132:135],v6,s[28:31],s87 offen offset:1024 sc1
v_mfma_f32_16x16x32_bf16 a[216:219],v[116:119],v[212:215],a[216:219]
v_bfe_u32 v56,v6,4,1
v_lshlrev_b32 v56,4,v56
v_mfma_f32_16x16x32_bf16 a[216:219],v[120:123],v[228:231],a[216:219]
v_mov_b32 v57,0x3f80
v_lshlrev_b32 v56,v56,v57
v_mfma_f32_16x16x32_bf16 a[216:219],v[124:127],v[244:247],a[216:219]
v_cndmask_b32 v56,0,v56,s[56:57]
v_bfe_u32 v57,v6,5,1
v_mfma_f32_16x16x32_bf16 a[220:223],v[124:127],v[188:191],a[220:223]
ds_read_b64_tr_b16 v[124:125],v2 offset:30880
ds_read_b64_tr_b16 v[126:127],v2 offset:36000
v_mfma_f32_16x16x32_bf16 a[220:223],v[120:123],v[172:175],a[220:223]
ds_read_b64_tr_b16 v[120:121],v2 offset:20640
v_cmp_eq_u32 s[56:57],0,v57
v_mfma_f32_16x16x32_bf16 a[220:223],v[116:119],v[156:159],a[220:223]
s_add_u32 s36,s12,s66
s_addc_u32 s37,s13,0
ds_read_b64_tr_b16 v[116:117],v2 offset:10400
v_mfma_f32_16x16x32_bf16 a[220:223],v[112:115],v[140:143],a[220:223]
s_and_b32 s37,s37,0xffff
ds_read_b64_tr_b16 v[112:113],v2 offset:160
)A"
R"A(v_cndmask_b32 v48,0,v56,s[56:57]
v_mfma_f32_16x16x32_bf16 a[224:227],v[64:67],v[32:35],a[224:227]
s_mul_i32 s66,s61,307200
ds_read_b64_tr_b16 v[114:115],v2 offset:5280
ds_read_b64_tr_b16 v[118:119],v2 offset:15520
v_mfma_f32_16x16x32_bf16 a[224:227],v[68:71],v[36:39],a[224:227]
ds_read_b64_tr_b16 v[122:123],v2 offset:25760
v_cndmask_b32 v49,v56,0,s[56:57]
v_mfma_f32_16x16x32_bf16 a[224:227],v[72:75],v[40:43],a[224:227]
s_waitcnt lgkmcnt(8)
v_mov_b32 v58,s52
v_mov_b32 v59,s53
v_mfma_f32_16x16x32_bf16 a[224:227],v[76:79],v[44:47],a[224:227]
s_add_u32 s24,s16,s66
s_addc_u32 s25,s17,0
v_lshlrev_b32 v60,20,v6
v_mfma_f32_16x16x32_bf16 a[228:231],v[76:79],v[180:183],a[228:231]
buffer_load_dwordx4 a[136:139],v6,s[28:31],s87 offen offset:2048 sc1
v_mfma_f32_16x16x32_bf16 a[228:231],v[72:75],v[164:167],a[228:231]
s_mul_i32 s66,s60,16384
s_add_u32 s66,s66,s67
s_add_u32 s79,s66,12288
v_mfma_f32_16x16x32_bf16 a[228:231],v[68:71],v[148:151],a[228:231]
buffer_store_dwordx4 v[156:159],v28,s[40:43],s80 offen offset:3072 sc1
v_mfma_f32_16x16x32_bf16 a[228:231],v[64:67],v[132:135],a[228:231]
s_add_u32 s80,s66,77824
v_bfe_u32 v61,v6,4,1
v_add3_u32 v61,v61,s63,2
v_mfma_f32_16x16x32_bf16 a[232:235],v[64:67],v[196:199],a[232:235]
s_and_b32 s25,s25,0xffff
v_and_b32 v61,3,v61
v_lshlrev_b32 v61,8,v61
v_mfma_f32_16x16x32_bf16 a[232:235],v[68:71],v[212:215],a[232:235]
s_add_u32 s67,s77,8192
v_mov_b32 v50,0
v_mov_b32 v51,0
v_mfma_f32_16x16x32_bf16 a[232:235],v[72:75],v[228:231],a[232:235]
v_mov_b32 v52,0
v_mov_b32 v53,0
v_mfma_f32_16x16x32_bf16 a[232:235],v[76:79],v[244:247],a[232:235]
v_mov_b32 v54,v48
v_mov_b32 v55,v49
v_mfma_f32_16x16x32_bf16 a[236:239],v[76:79],v[188:191],a[236:239]
ds_read_b64_tr_b16 v[76:77],v1 offset:30912
ds_read_b64_tr_b16 v[78:79],v1 offset:36032
v_mfma_f32_16x16x32_bf16 a[236:239],v[72:75],v[172:175],a[236:239]
ds_read_b64_tr_b16 v[72:73],v1 offset:20672
v_bfe_u32 v56,v6,4,4
v_mfma_f32_16x16x32_bf16 a[236:239],v[68:71],v[156:159],a[236:239]
ds_read_b64_tr_b16 v[68:69],v1 offset:10432
v_mul_u32_u24 v56,1280,v56
v_mfma_f32_16x16x32_bf16 a[236:239],v[64:67],v[140:143],a[236:239]
ds_read_b64_tr_b16 v[64:65],v1 offset:192
v_bfe_u32 v57,v6,8,1
v_mfma_f32_16x16x32_bf16 a[240:243],v[80:83],v[32:35],a[240:243]
ds_read_b64_tr_b16 v[66:67],v1 offset:5312
ds_read_b64_tr_b16 v[70:71],v1 offset:15552
v_mfma_f32_16x16x32_bf16 a[240:243],v[84:87],v[36:39],a[240:243]
buffer_load_dwordx4 a[152:155],v6,s[28:31],s88 offen offset:2048 sc1
v_mfma_f32_16x16x32_bf16 a[240:243],v[88:91],v[40:43],a[240:243]
ds_read_b64_tr_b16 v[74:75],v1 offset:25792
v_lshl_add_u32 v56,v57,5,v56
v_mfma_f32_16x16x32_bf16 a[240:243],v[92:95],v[44:47],a[240:243]
v_lshrrev_b32 v57,9,v6
v_lshl_add_u32 v56,v57,4,v56
v_mfma_f32_16x16x32_bf16 a[244:247],v[92:95],v[180:183],a[244:247]
v_mfma_f32_16x16x32_bf16 a[244:247],v[88:91],v[164:167],a[244:247]
buffer_store_dwordx4 v[172:175],v28,s[40:43],s81 offen offset:3072 sc1
v_mfma_f32_16x16x32_bf16 a[244:247],v[84:87],v[148:151],a[244:247]
s_add_u32 s81,s66,143360
v_mfma_f32_16x16x32_bf16 a[244:247],v[80:83],v[132:135],a[244:247]
v_mfma_f32_16x16x32_bf16 a[248:251],v[80:83],v[196:199],a[248:251]
v_mfma_f32_16x16x32_bf16 a[248:251],v[84:87],v[212:215],a[248:251]
v_mfma_f32_16x16x32_bf16 a[248:251],v[88:91],v[228:231],a[248:251]
v_mfma_f32_16x16x32_bf16 a[248:251],v[92:95],v[244:247],a[248:251]
s_nop 0
L_pd14_%=:
s_cbranch_execz L_trt1_%=
v_mfma_f32_16x16x32_bf16 a[252:255],v[92:95],v[188:191],a[252:255]
ds_read_b64_tr_b16 v[92:93],v2 offset:30944
ds_read_b64_tr_b16 v[94:95],v2 offset:36064
v_mfma_f32_16x16x32_bf16 a[252:255],v[88:91],v[172:175],a[252:255]
ds_read_b64_tr_b16 v[88:89],v2 offset:20704
v_mfma_f32_16x16x32_bf16 a[252:255],v[84:87],v[156:159],a[252:255]
buffer_load_dwordx4 a[168:171],v6,s[28:31],s89 offen offset:2048 sc1
v_mfma_f32_16x16x32_bf16 a[252:255],v[80:83],v[140:143],a[252:255]
ds_read_b64_tr_b16 v[80:81],v2 offset:224
ds_read_b64_tr_b16 v[82:83],v2 offset:5344
v_mfma_f32_16x16x32_bf16 a[0:3],v[96:99],v[32:35],a[0:3]
ds_read_b64_tr_b16 v[84:85],v2 offset:10464
v_mfma_f32_16x16x32_bf16 a[0:3],v[100:103],v[36:39],a[0:3]
ds_read_b64_tr_b16 v[86:87],v2 offset:15584
v_mfma_f32_16x16x32_bf16 a[0:3],v[104:107],v[40:43],a[0:3]
ds_read_b64_tr_b16 v[90:91],v2 offset:25824
v_mfma_f32_16x16x32_bf16 a[0:3],v[108:111],v[44:47],a[0:3]
v_mfma_f32_16x16x32_bf16 a[4:7],v[108:111],v[180:183],a[4:7]
buffer_store_dwordx4 v[188:191],v28,s[40:43],s82 offen offset:3072 sc1
v_mfma_f32_16x16x32_bf16 a[4:7],v[104:107],v[164:167],a[4:7]
s_add_u32 s82,s66,208896
s_mul_i32 s66,s64,320
v_mfma_f32_16x16x32_bf16 a[4:7],v[100:103],v[148:151],a[4:7]
v_mfma_f32_16x16x32_bf16 a[4:7],v[96:99],v[132:135],a[4:7]
v_mfma_f32_16x16x32_bf16 a[8:11],v[96:99],v[196:199],a[8:11]
v_mfma_f32_16x16x32_bf16 a[8:11],v[100:103],v[212:215],a[8:11]
v_mfma_f32_16x16x32_bf16 a[8:11],v[104:107],v[228:231],a[8:11]
v_mfma_f32_16x16x32_bf16 a[8:11],v[108:111],v[244:247],a[8:11]
buffer_load_dwordx4 a[184:187],v6,s[28:31],s90 offen offset:2048 sc1
v_mfma_f32_16x16x32_bf16 a[12:15],v[108:111],v[188:191],a[12:15]
ds_read_b64_tr_b16 v[108:109],v1 offset:30976
ds_read_b64_tr_b16 v[110:111],v1 offset:36096
v_mfma_f32_16x16x32_bf16 a[12:15],v[104:107],v[172:175],a[12:15]
ds_read_b64_tr_b16 v[104:105],v1 offset:20736
v_mfma_f32_16x16x32_bf16 a[12:15],v[100:103],v[156:159],a[12:15]
ds_read_b64_tr_b16 v[100:101],v1 offset:10496
v_mfma_f32_16x16x32_bf16 a[12:15],v[96:99],v[140:143],a[12:15]
s_waitcnt lgkmcnt(4)
ds_read_b64_tr_b16 v[96:97],v1 offset:256
v_mfma_f32_16x16x32_bf16 a[16:19],v[112:115],v[32:35],a[16:19]
ds_read_b64_tr_b16 v[98:99],v1 offset:5376
ds_read_b64_tr_b16 v[102:103],v1 offset:15616
v_mfma_f32_16x16x32_bf16 a[16:19],v[116:119],v[36:39],a[16:19]
ds_read_b64_tr_b16 v[106:107],v1 offset:25856
v_add_u32 v1,0xffff6000,v1
v_mfma_f32_16x16x32_bf16 a[16:19],v[120:123],v[40:43],a[16:19]
v_mfma_f32_16x16x32_bf16 a[16:19],v[124:127],v[44:47],a[16:19]
v_mfma_f32_16x16x32_bf16 a[20:23],v[124:127],v[180:183],a[20:23]
v_mfma_f32_16x16x32_bf16 a[20:23],v[120:123],v[164:167],a[20:23]
v_mfma_f32_16x16x32_bf16 a[20:23],v[116:119],v[148:151],a[20:23]
v_mfma_f32_16x16x32_bf16 a[20:23],v[112:115],v[132:135],a[20:23]
v_mfma_f32_16x16x32_bf16 a[24:27],v[112:115],v[196:199],a[24:27]
buffer_load_dwordx4 a[188:191],v28,s[28:31],s90 offen offset:3072 sc1
v_mfma_f32_16x16x32_bf16 a[24:27],v[116:119],v[212:215],a[24:27]
v_mfma_f32_16x16x32_bf16 a[24:27],v[120:123],v[228:231],a[24:27]
v_mfma_f32_16x16x32_bf16 a[24:27],v[124:127],v[244:247],a[24:27]
v_mfma_f32_16x16x32_bf16 a[28:31],v[124:127],v[188:191],a[28:31]
ds_read_b64_tr_b16 v[124:125],v2 offset:31008
ds_read_b64_tr_b16 v[126:127],v2 offset:36128
v_mfma_f32_16x16x32_bf16 a[28:31],v[120:123],v[172:175],a[28:31]
ds_read_b64_tr_b16 v[120:121],v2 offset:20768
v_mfma_f32_16x16x32_bf16 a[28:31],v[116:119],v[156:159],a[28:31]
ds_read_b64_tr_b16 v[116:117],v2 offset:10528
v_mfma_f32_16x16x32_bf16 a[28:31],v[112:115],v[140:143],a[28:31]
ds_read_b64_tr_b16 v[112:113],v2 offset:288
v_mfma_f32_16x16x32_bf16 v[128:131],v[64:67],v[32:35],v[128:131]
ds_read_b64_tr_b16 v[114:115],v2 offset:5408
ds_read_b64_tr_b16 v[118:119],v2 offset:15648
v_mfma_f32_16x16x32_bf16 v[128:131],v[68:71],v[36:39],v[128:131]
ds_read_b64_tr_b16 v[122:123],v2 offset:25888
v_add_u32 v2,0xffff6000,v2
v_mfma_f32_16x16x32_bf16 v[128:131],v[72:75],v[40:43],v[128:131]
v_mfma_f32_16x16x32_bf16 v[128:131],v[76:79],v[44:47],v[128:131]
v_mfma_f32_16x16x32_bf16 v[136:139],v[76:79],v[180:183],v[136:139]
v_mfma_f32_16x16x32_bf16 v[136:139],v[72:75],v[164:167],v[136:139]
buffer_load_dwordx4 a[172:175],v28,s[28:31],s89 offen offset:3072 sc1
v_mfma_f32_16x16x32_bf16 v[136:139],v[68:71],v[148:151],v[136:139]
v_mfma_f32_16x16x32_bf16 v[136:139],v[64:67],v[132:135],v[136:139]
v_mfma_f32_16x16x32_bf16 v[144:147],v[64:67],v[196:199],v[144:147]
v_mfma_f32_16x16x32_bf16 v[144:147],v[68:71],v[212:215],v[144:147]
v_mfma_f32_16x16x32_bf16 v[144:147],v[72:75],v[228:231],v[144:147]
v_mfma_f32_16x16x32_bf16 v[144:147],v[76:79],v[244:247],v[144:147]
v_mfma_f32_16x16x32_bf16 v[152:155],v[76:79],v[188:191],v[152:155]
v_mfma_f32_16x16x32_bf16 v[152:155],v[72:75],v[172:175],v[152:155]
v_mfma_f32_16x16x32_bf16 v[152:155],v[68:71],v[156:159],v[152:155]
v_mfma_f32_16x16x32_bf16 v[152:155],v[64:67],v[140:143],v[152:155]
s_mov_b64 exec,0
s_cmp_eq_u32 s60,0
s_cbranch_scc1 L_pd16_%=
s_cmp_eq_u32 s60,1
s_cbranch_scc1 L_pd17_%=
s_cmp_eq_u32 s60,2
s_cbranch_scc1 L_pd18_%=
s_branch L_trtg2F_%=
L_trtg2F_%=:
s_mov_b64 exec,-1
s_waitcnt lgkmcnt(0)
s_barrier
v_mfma_f32_16x16x32_bf16 v[160:163],v[80:83],v[32:35],v[160:163]
buffer_load_dwordx4 v24,s[48:51],s83 offen lds
v_mfma_f32_16x16x32_bf16 v[160:163],v[84:87],v[36:39],v[160:163]
ds_read_b64_tr_b16 v[64:65],v1
ds_read_b64_tr_b16 v[66:67],v1 offset:5120
v_mfma_f32_16x16x32_bf16 v[160:163],v[88:91],v[40:43],v[160:163]
ds_read_b64_tr_b16 v[68:69],v1 offset:10240
v_mfma_f32_16x16x32_bf16 v[160:163],v[92:95],v[44:47],v[160:163]
ds_read_b64_tr_b16 v[70:71],v1 offset:15360
v_mfma_f32_16x16x32_bf16 v[168:171],v[92:95],v[180:183],v[168:171]
ds_read_b64_tr_b16 v[72:73],v1 offset:20480
v_mfma_f32_16x16x32_bf16 v[168:171],v[88:91],v[164:167],v[168:171]
buffer_load_dwordx4 a[156:159],v28,s[28:31],s88 offen offset:3072 sc1
v_mfma_f32_16x16x32_bf16 v[168:171],v[84:87],v[148:151],v[168:171]
ds_read_b64_tr_b16 v[74:75],v1 offset:25600
ds_read_b64_tr_b16 v[76:77],v1 offset:30720
v_mfma_f32_16x16x32_bf16 v[168:171],v[80:83],v[132:135],v[168:171]
ds_read_b64_tr_b16 v[78:79],v1 offset:35840
v_mfma_f32_16x16x32_bf16 v[176:179],v[80:83],v[196:199],v[176:179]
v_mfma_f32_16x16x32_bf16 v[176:179],v[84:87],v[212:215],v[176:179]
v_mfma_f32_16x16x32_bf16 v[176:179],v[88:91],v[228:231],v[176:179]
v_mfma_f32_16x16x32_bf16 v[176:179],v[92:95],v[244:247],v[176:179]
buffer_load_dwordx4 v25,s[48:51],s83 offen offset:1024 lds
v_mfma_f32_16x16x32_bf16 v[184:187],v[92:95],v[188:191],v[184:187]
ds_read_b64_tr_b16 v[92:93],v2 offset:30752
ds_read_b64_tr_b16 v[94:95],v2 offset:35872
v_mfma_f32_16x16x32_bf16 v[184:187],v[88:91],v[172:175],v[184:187]
ds_read_b64_tr_b16 v[88:89],v2 offset:20512
v_mfma_f32_16x16x32_bf16 v[184:187],v[84:87],v[156:159],v[184:187]
ds_read_b64_tr_b16 v[84:85],v2 offset:10272
v_mfma_f32_16x16x32_bf16 v[184:187],v[80:83],v[140:143],v[184:187]
ds_read_b64_tr_b16 v[80:81],v2 offset:32
v_mfma_f32_16x16x32_bf16 v[192:195],v[96:99],v[32:35],v[192:195]
buffer_load_dwordx4 a[140:143],v28,s[28:31],s87 offen offset:3072 sc1
v_mfma_f32_16x16x32_bf16 v[192:195],v[100:103],v[36:39],v[192:195]
s_add_u32 s87,s64,2
s_and_b32 s87,s87,3
s_lshl_b32 s87,s87,1
v_mfma_f32_16x16x32_bf16 v[192:195],v[104:107],v[40:43],v[192:195]
s_lshl_b32 s87,s87,16
s_add_u32 s88,s87,4096
s_add_u32 s89,s87,8192
v_mfma_f32_16x16x32_bf16 v[192:195],v[108:111],v[44:47],v[192:195]
s_add_u32 s90,s87,12288
ds_read_b64_tr_b16 v[82:83],v2 offset:5152
ds_read_b64_tr_b16 v[86:87],v2 offset:15392
v_mfma_f32_16x16x32_bf16 v[200:203],v[108:111],v[180:183],v[200:203]
ds_read_b64_tr_b16 v[90:91],v2 offset:25632
v_mfma_f32_16x16x32_bf16 v[200:203],v[104:107],v[164:167],v[200:203]
buffer_load_dwordx4 a[68:71],v6,s[28:31],s87 offen sc1
v_mfma_f32_16x16x32_bf16 v[200:203],v[100:103],v[148:151],v[200:203]
v_mfma_f32_16x16x32_bf16 v[200:203],v[96:99],v[132:135],v[200:203]
v_mfma_f32_16x16x32_bf16 v[208:211],v[96:99],v[196:199],v[208:211]
v_mfma_f32_16x16x32_bf16 v[208:211],v[100:103],v[212:215],v[208:211]
v_mfma_f32_16x16x32_bf16 v[208:211],v[104:107],v[228:231],v[208:211]
buffer_load_dwordx4 a[76:79],v6,s[28:31],s88 offen sc1
v_mfma_f32_16x16x32_bf16 v[208:211],v[108:111],v[244:247],v[208:211]
v_mfma_f32_16x16x32_bf16 v[216:219],v[108:111],v[188:191],v[216:219]
ds_read_b64_tr_b16 v[108:109],v1 offset:30784
ds_read_b64_tr_b16 v[110:111],v1 offset:35904
v_mfma_f32_16x16x32_bf16 v[216:219],v[104:107],v[172:175],v[216:219]
ds_read_b64_tr_b16 v[104:105],v1 offset:20544
v_mfma_f32_16x16x32_bf16 v[216:219],v[100:103],v[156:159],v[216:219]
ds_read_b64_tr_b16 v[100:101],v1 offset:10304
v_mfma_f32_16x16x32_bf16 v[216:219],v[96:99],v[140:143],v[216:219]
)A"
R"A(buffer_load_dwordx4 a[84:87],v6,s[28:31],s89 offen sc1
v_mfma_f32_16x16x32_bf16 v[224:227],v[112:115],v[32:35],v[224:227]
ds_read_b64_tr_b16 v[96:97],v1 offset:64
ds_read_b64_tr_b16 v[98:99],v1 offset:5184
v_mfma_f32_16x16x32_bf16 v[224:227],v[116:119],v[36:39],v[224:227]
ds_read_b64_tr_b16 v[102:103],v1 offset:15424
v_mfma_f32_16x16x32_bf16 v[224:227],v[120:123],v[40:43],v[224:227]
ds_read_b64_tr_b16 v[106:107],v1 offset:25664
v_mfma_f32_16x16x32_bf16 v[224:227],v[124:127],v[44:47],v[224:227]
v_mfma_f32_16x16x32_bf16 v[232:235],v[124:127],v[180:183],v[232:235]
buffer_load_dwordx4 a[92:95],v6,s[28:31],s90 offen sc1
v_mfma_f32_16x16x32_bf16 v[232:235],v[120:123],v[164:167],v[232:235]
v_mfma_f32_16x16x32_bf16 v[232:235],v[116:119],v[148:151],v[232:235]
v_mfma_f32_16x16x32_bf16 v[232:235],v[112:115],v[132:135],v[232:235]
v_mfma_f32_16x16x32_bf16 v[240:243],v[112:115],v[196:199],v[240:243]
v_mfma_f32_16x16x32_bf16 v[240:243],v[116:119],v[212:215],v[240:243]
buffer_load_dwordx4 a[124:127],v6,s[28:31],s90 offen offset:1024 sc1
v_mfma_f32_16x16x32_bf16 v[240:243],v[120:123],v[228:231],v[240:243]
v_mfma_f32_16x16x32_bf16 v[240:243],v[124:127],v[244:247],v[240:243]
v_mfma_f32_16x16x32_bf16 v[248:251],v[124:127],v[188:191],v[248:251]
ds_read_b64_tr_b16 v[124:125],v2 offset:30816
ds_read_b64_tr_b16 v[126:127],v2 offset:35936
v_mfma_f32_16x16x32_bf16 v[248:251],v[120:123],v[172:175],v[248:251]
ds_read_b64_tr_b16 v[120:121],v2 offset:20576
v_mfma_f32_16x16x32_bf16 v[248:251],v[116:119],v[156:159],v[248:251]
buffer_load_dwordx4 a[116:119],v6,s[28:31],s89 offen offset:1024 sc1
v_mfma_f32_16x16x32_bf16 v[248:251],v[112:115],v[140:143],v[248:251]
s_waitcnt vmcnt(31) lgkmcnt(3)
ds_read_b64_tr_b16 v[112:113],v2 offset:96
ds_read_b64_tr_b16 v[114:115],v2 offset:5216
v_mfma_f32_16x16x32_bf16 a[192:195],v[64:67],a[128:131],a[192:195]
ds_read_b64_tr_b16 v[116:117],v2 offset:10336
v_mfma_f32_16x16x32_bf16 a[192:195],v[68:71],a[144:147],a[192:195]
s_waitcnt vmcnt(29)
ds_read_b64_tr_b16 v[118:119],v2 offset:15456
v_mfma_f32_16x16x32_bf16 a[192:195],v[72:75],a[160:163],a[192:195]
s_waitcnt vmcnt(25)
ds_read_b64_tr_b16 v[122:123],v2 offset:25696
v_mfma_f32_16x16x32_bf16 a[192:195],v[76:79],a[176:179],a[192:195]
buffer_load_dwordx4 a[108:111],v6,s[28:31],s88 offen offset:1024 sc1
v_mfma_f32_16x16x32_bf16 a[196:199],v[76:79],a[180:183],a[196:199]
s_waitcnt vmcnt(24)
v_mfma_f32_16x16x32_bf16 a[196:199],v[72:75],a[164:167],a[196:199]
s_waitcnt vmcnt(22)
v_mfma_f32_16x16x32_bf16 a[196:199],v[68:71],a[148:151],a[196:199]
s_waitcnt vmcnt(19)
v_mfma_f32_16x16x32_bf16 a[196:199],v[64:67],a[132:135],a[196:199]
s_waitcnt vmcnt(17)
v_mfma_f32_16x16x32_bf16 a[200:203],v[64:67],a[136:139],a[200:203]
buffer_load_dwordx4 a[100:103],v6,s[28:31],s87 offen offset:1024 sc1
v_mfma_f32_16x16x32_bf16 a[200:203],v[68:71],a[152:155],a[200:203]
s_waitcnt vmcnt(16)
v_mfma_f32_16x16x32_bf16 a[200:203],v[72:75],a[168:171],a[200:203]
s_waitcnt vmcnt(14)
v_mfma_f32_16x16x32_bf16 a[200:203],v[76:79],a[184:187],a[200:203]
s_waitcnt vmcnt(13)
v_mfma_f32_16x16x32_bf16 a[204:207],v[76:79],a[188:191],a[204:207]
s_waitcnt vmcnt(10)
ds_read_b64_tr_b16 v[76:77],v1 offset:30848
ds_read_b64_tr_b16 v[78:79],v1 offset:35968
v_mfma_f32_16x16x32_bf16 a[204:207],v[72:75],a[172:175],a[204:207]
buffer_load_dwordx4 a[64:67],v6,s[28:31],s87 offen offset:2048 sc1
v_mfma_f32_16x16x32_bf16 a[204:207],v[68:71],a[156:159],a[204:207]
s_waitcnt vmcnt(9)
ds_read_b64_tr_b16 v[68:69],v1 offset:10368
ds_read_b64_tr_b16 v[70:71],v1 offset:15488
v_mfma_f32_16x16x32_bf16 a[204:207],v[64:67],a[140:143],a[204:207]
ds_read_b64_tr_b16 v[64:65],v1 offset:128
v_mfma_f32_16x16x32_bf16 a[208:211],v[80:83],a[128:131],a[208:211]
ds_read_b64_tr_b16 v[66:67],v1 offset:5248
v_mfma_f32_16x16x32_bf16 a[208:211],v[84:87],a[144:147],a[208:211]
ds_read_b64_tr_b16 v[72:73],v1 offset:20608
v_mfma_f32_16x16x32_bf16 a[208:211],v[88:91],a[160:163],a[208:211]
ds_read_b64_tr_b16 v[74:75],v1 offset:25728
v_mfma_f32_16x16x32_bf16 a[208:211],v[92:95],a[176:179],a[208:211]
buffer_load_dwordx4 v26,s[48:51],s83 offen offset:2048 lds
v_mfma_f32_16x16x32_bf16 a[212:215],v[92:95],a[180:183],a[212:215]
v_mfma_f32_16x16x32_bf16 a[212:215],v[88:91],a[164:167],a[212:215]
v_mfma_f32_16x16x32_bf16 a[212:215],v[84:87],a[148:151],a[212:215]
v_mfma_f32_16x16x32_bf16 a[212:215],v[80:83],a[132:135],a[212:215]
v_mfma_f32_16x16x32_bf16 a[216:219],v[80:83],a[136:139],a[216:219]
buffer_load_dwordx4 a[72:75],v6,s[28:31],s88 offen offset:2048 sc1
v_mfma_f32_16x16x32_bf16 a[216:219],v[84:87],a[152:155],a[216:219]
v_mfma_f32_16x16x32_bf16 a[216:219],v[88:91],a[168:171],a[216:219]
v_mfma_f32_16x16x32_bf16 a[216:219],v[92:95],a[184:187],a[216:219]
v_mfma_f32_16x16x32_bf16 a[220:223],v[92:95],a[188:191],a[220:223]
ds_read_b64_tr_b16 v[92:93],v2 offset:30880
ds_read_b64_tr_b16 v[94:95],v2 offset:36000
v_mfma_f32_16x16x32_bf16 a[220:223],v[88:91],a[172:175],a[220:223]
ds_read_b64_tr_b16 v[88:89],v2 offset:20640
v_mfma_f32_16x16x32_bf16 a[220:223],v[84:87],a[156:159],a[220:223]
buffer_load_dwordx4 v27,s[48:51],s83 offen offset:3072 lds
v_mfma_f32_16x16x32_bf16 a[220:223],v[80:83],a[140:143],a[220:223]
s_add_u32 m0,s86,126976
ds_read_b64_tr_b16 v[80:81],v2 offset:160
ds_read_b64_tr_b16 v[82:83],v2 offset:5280
s_barrier
v_mfma_f32_16x16x32_bf16 a[224:227],v[96:99],a[128:131],a[224:227]
ds_read_b64_tr_b16 v[84:85],v2 offset:10400
v_mfma_f32_16x16x32_bf16 a[224:227],v[100:103],a[144:147],a[224:227]
ds_read_b64_tr_b16 v[86:87],v2 offset:15520
v_mfma_f32_16x16x32_bf16 a[224:227],v[104:107],a[160:163],a[224:227]
buffer_store_dwordx2 v[8:9],v10,s[44:47],s18 offen offset:1280 sc1
v_mfma_f32_16x16x32_bf16 a[224:227],v[108:111],a[176:179],a[224:227]
ds_read_b64_tr_b16 v[90:91],v2 offset:25760
v_mfma_f32_16x16x32_bf16 a[228:231],v[108:111],a[180:183],a[228:231]
v_mfma_f32_16x16x32_bf16 a[228:231],v[104:107],a[164:167],a[228:231]
v_mfma_f32_16x16x32_bf16 a[228:231],v[100:103],a[148:151],a[228:231]
v_mfma_f32_16x16x32_bf16 a[228:231],v[96:99],a[132:135],a[228:231]
buffer_load_dwordx4 a[80:83],v6,s[28:31],s89 offen offset:2048 sc1
v_mfma_f32_16x16x32_bf16 a[232:235],v[96:99],a[136:139],a[232:235]
v_mfma_f32_16x16x32_bf16 a[232:235],v[100:103],a[152:155],a[232:235]
v_mfma_f32_16x16x32_bf16 a[232:235],v[104:107],a[168:171],a[232:235]
v_mfma_f32_16x16x32_bf16 a[232:235],v[108:111],a[184:187],a[232:235]
v_mfma_f32_16x16x32_bf16 a[236:239],v[108:111],a[188:191],a[236:239]
s_waitcnt lgkmcnt(0)
ds_read_b64_tr_b16 v[108:109],v1 offset:30912
ds_read_b64_tr_b16 v[110:111],v1 offset:36032
v_mfma_f32_16x16x32_bf16 a[236:239],v[104:107],a[172:175],a[236:239]
buffer_load_dwordx4 v7,s[48:51],s83 offen lds
v_mfma_f32_16x16x32_bf16 a[236:239],v[100:103],a[156:159],a[236:239]
s_add_u32 m0,s86,128000
ds_read_b64_tr_b16 v[100:101],v1 offset:10432
ds_read_b64_tr_b16 v[102:103],v1 offset:15552
v_mfma_f32_16x16x32_bf16 a[236:239],v[96:99],a[140:143],a[236:239]
s_add_u32 s83,s64,1
s_and_b32 s83,s83,3
ds_read_b64_tr_b16 v[96:97],v1 offset:192
v_mfma_f32_16x16x32_bf16 a[240:243],v[112:115],a[128:131],a[240:243]
s_lshl_b32 s83,s83,1
s_or_b32 s83,s83,1
ds_read_b64_tr_b16 v[98:99],v1 offset:5312
v_mfma_f32_16x16x32_bf16 a[240:243],v[116:119],a[144:147],a[240:243]
s_mul_i32 s83,s83,163840
s_add_u32 s83,s83,s85
ds_read_b64_tr_b16 v[104:105],v1 offset:20672
v_mfma_f32_16x16x32_bf16 a[240:243],v[120:123],a[160:163],a[240:243]
buffer_load_dwordx4 a[88:91],v6,s[28:31],s90 offen offset:2048 sc1
v_mfma_f32_16x16x32_bf16 a[240:243],v[124:127],a[176:179],a[240:243]
ds_read_b64_tr_b16 v[106:107],v1 offset:25792
v_mfma_f32_16x16x32_bf16 a[244:247],v[124:127],a[180:183],a[244:247]
v_mfma_f32_16x16x32_bf16 a[244:247],v[120:123],a[164:167],a[244:247]
v_mfma_f32_16x16x32_bf16 a[244:247],v[116:119],a[148:151],a[244:247]
v_mfma_f32_16x16x32_bf16 a[244:247],v[112:115],a[132:135],a[244:247]
v_mfma_f32_16x16x32_bf16 a[248:251],v[112:115],a[136:139],a[248:251]
buffer_load_dwordx4 v24,s[48:51],s84 offen lds
v_mfma_f32_16x16x32_bf16 a[248:251],v[116:119],a[152:155],a[248:251]
v_mfma_f32_16x16x32_bf16 a[248:251],v[120:123],a[168:171],a[248:251]
v_mfma_f32_16x16x32_bf16 a[248:251],v[124:127],a[184:187],a[248:251]
v_mfma_f32_16x16x32_bf16 a[252:255],v[124:127],a[188:191],a[252:255]
ds_read_b64_tr_b16 v[124:125],v2 offset:30944
ds_read_b64_tr_b16 v[126:127],v2 offset:36064
v_mfma_f32_16x16x32_bf16 a[252:255],v[120:123],a[172:175],a[252:255]
buffer_load_dwordx4 a[120:123],v28,s[28:31],s90 offen offset:3072 sc1
v_mfma_f32_16x16x32_bf16 a[252:255],v[116:119],a[156:159],a[252:255]
ds_read_b64_tr_b16 v[116:117],v2 offset:10464
ds_read_b64_tr_b16 v[118:119],v2 offset:15584
v_mfma_f32_16x16x32_bf16 a[252:255],v[112:115],a[140:143],a[252:255]
ds_read_b64_tr_b16 v[112:113],v2 offset:224
v_mfma_f32_16x16x32_bf16 a[0:3],v[64:67],a[128:131],a[0:3]
ds_read_b64_tr_b16 v[114:115],v2 offset:5344
v_mfma_f32_16x16x32_bf16 a[0:3],v[68:71],a[144:147],a[0:3]
ds_read_b64_tr_b16 v[120:121],v2 offset:20704
v_mfma_f32_16x16x32_bf16 a[0:3],v[72:75],a[160:163],a[0:3]
ds_read_b64_tr_b16 v[122:123],v2 offset:25824
v_mfma_f32_16x16x32_bf16 a[0:3],v[76:79],a[176:179],a[0:3]
buffer_load_dwordx4 v25,s[48:51],s84 offen offset:1024 lds
v_mfma_f32_16x16x32_bf16 a[4:7],v[76:79],a[180:183],a[4:7]
v_mfma_f32_16x16x32_bf16 a[4:7],v[72:75],a[164:167],a[4:7]
v_mfma_f32_16x16x32_bf16 a[4:7],v[68:71],a[148:151],a[4:7]
v_mfma_f32_16x16x32_bf16 a[4:7],v[64:67],a[132:135],a[4:7]
v_mfma_f32_16x16x32_bf16 a[8:11],v[64:67],a[136:139],a[8:11]
buffer_load_dwordx4 a[112:115],v28,s[28:31],s89 offen offset:3072 sc1
v_mfma_f32_16x16x32_bf16 a[8:11],v[68:71],a[152:155],a[8:11]
v_mfma_f32_16x16x32_bf16 a[8:11],v[72:75],a[168:171],a[8:11]
v_mfma_f32_16x16x32_bf16 a[8:11],v[76:79],a[184:187],a[8:11]
v_mfma_f32_16x16x32_bf16 a[12:15],v[76:79],a[188:191],a[12:15]
ds_read_b64_tr_b16 v[76:77],v1 offset:30976
ds_read_b64_tr_b16 v[78:79],v1 offset:36096
v_mfma_f32_16x16x32_bf16 a[12:15],v[72:75],a[172:175],a[12:15]
buffer_load_dwordx4 a[104:107],v28,s[28:31],s88 offen offset:3072 sc1
v_mfma_f32_16x16x32_bf16 a[12:15],v[68:71],a[156:159],a[12:15]
ds_read_b64_tr_b16 v[68:69],v1 offset:10496
ds_read_b64_tr_b16 v[70:71],v1 offset:15616
v_mfma_f32_16x16x32_bf16 a[12:15],v[64:67],a[140:143],a[12:15]
ds_read_b64_tr_b16 v[64:65],v1 offset:256
v_mfma_f32_16x16x32_bf16 a[16:19],v[80:83],a[128:131],a[16:19]
ds_read_b64_tr_b16 v[66:67],v1 offset:5376
v_mfma_f32_16x16x32_bf16 a[16:19],v[84:87],a[144:147],a[16:19]
ds_read_b64_tr_b16 v[72:73],v1 offset:20736
v_mfma_f32_16x16x32_bf16 a[16:19],v[88:91],a[160:163],a[16:19]
ds_read_b64_tr_b16 v[74:75],v1 offset:25856
v_add_u32 v1,0xffff6000,v1
v_mfma_f32_16x16x32_bf16 a[16:19],v[92:95],a[176:179],a[16:19]
buffer_load_dwordx4 v26,s[48:51],s84 offen offset:2048 lds
v_mfma_f32_16x16x32_bf16 a[20:23],v[92:95],a[180:183],a[20:23]
v_mfma_f32_16x16x32_bf16 a[20:23],v[88:91],a[164:167],a[20:23]
v_mfma_f32_16x16x32_bf16 a[20:23],v[84:87],a[148:151],a[20:23]
v_mfma_f32_16x16x32_bf16 a[20:23],v[80:83],a[132:135],a[20:23]
v_mfma_f32_16x16x32_bf16 a[24:27],v[80:83],a[136:139],a[24:27]
buffer_load_dwordx4 a[96:99],v28,s[28:31],s87 offen offset:3072 sc1
v_mfma_f32_16x16x32_bf16 a[24:27],v[84:87],a[152:155],a[24:27]
s_add_u32 s87,s64,3
s_and_b32 s87,s87,3
s_lshl_b32 s87,s87,1
v_mfma_f32_16x16x32_bf16 a[24:27],v[88:91],a[168:171],a[24:27]
s_lshl_b32 s87,s87,16
s_add_u32 s88,s87,4096
s_add_u32 s89,s87,8192
v_mfma_f32_16x16x32_bf16 a[24:27],v[92:95],a[184:187],a[24:27]
s_add_u32 s90,s87,12288
v_mfma_f32_16x16x32_bf16 a[28:31],v[92:95],a[188:191],a[28:31]
ds_read_b64_tr_b16 v[92:93],v2 offset:31008
ds_read_b64_tr_b16 v[94:95],v2 offset:36128
v_mfma_f32_16x16x32_bf16 a[28:31],v[88:91],a[172:175],a[28:31]
s_waitcnt lgkmcnt(2)
ds_read_b64_tr_b16 v[88:89],v2 offset:20768
v_mfma_f32_16x16x32_bf16 a[28:31],v[84:87],a[156:159],a[28:31]
buffer_load_dwordx4 v27,s[48:51],s84 offen offset:3072 lds
v_mfma_f32_16x16x32_bf16 a[28:31],v[80:83],a[140:143],a[28:31]
s_add_u32 m0,s86,132096
ds_read_b64_tr_b16 v[80:81],v2 offset:288
ds_read_b64_tr_b16 v[82:83],v2 offset:5408
)A"
R"A(v_mfma_f32_16x16x32_bf16 v[128:131],v[96:99],a[128:131],v[128:131]
ds_read_b64_tr_b16 v[84:85],v2 offset:10528
v_mfma_f32_16x16x32_bf16 v[128:131],v[100:103],a[144:147],v[128:131]
ds_read_b64_tr_b16 v[86:87],v2 offset:15648
v_mfma_f32_16x16x32_bf16 v[128:131],v[104:107],a[160:163],v[128:131]
ds_read_b64_tr_b16 v[90:91],v2 offset:25888
v_add_u32 v2,0xffff6000,v2
v_mfma_f32_16x16x32_bf16 v[128:131],v[108:111],a[176:179],v[128:131]
v_mfma_f32_16x16x32_bf16 v[136:139],v[108:111],a[180:183],v[136:139]
buffer_load_dwordx4 v7,s[48:51],s84 offen lds
v_mfma_f32_16x16x32_bf16 v[136:139],v[104:107],a[164:167],v[136:139]
s_add_u32 m0,s86,81920
s_add_u32 s84,s83,20480
v_mfma_f32_16x16x32_bf16 v[136:139],v[100:103],a[148:151],v[136:139]
v_mfma_f32_16x16x32_bf16 v[136:139],v[96:99],a[132:135],v[136:139]
v_mfma_f32_16x16x32_bf16 v[144:147],v[96:99],a[136:139],v[144:147]
v_mfma_f32_16x16x32_bf16 v[144:147],v[100:103],a[152:155],v[144:147]
v_mfma_f32_16x16x32_bf16 v[144:147],v[104:107],a[168:171],v[144:147]
buffer_load_dwordx4 v24,s[48:51],s83 offen lds
v_mfma_f32_16x16x32_bf16 v[144:147],v[108:111],a[184:187],v[144:147]
v_mfma_f32_16x16x32_bf16 v[152:155],v[108:111],a[188:191],v[152:155]
ds_read_b64_tr_b16 v[108:109],v1 offset:30720
ds_read_b64_tr_b16 v[110:111],v1 offset:35840
v_mfma_f32_16x16x32_bf16 v[152:155],v[104:107],a[172:175],v[152:155]
ds_read_b64_tr_b16 v[104:105],v1 offset:20480
v_mfma_f32_16x16x32_bf16 v[152:155],v[100:103],a[156:159],v[152:155]
ds_read_b64_tr_b16 v[100:101],v1 offset:10240
v_mfma_f32_16x16x32_bf16 v[152:155],v[96:99],a[140:143],v[152:155]
ds_read_b64_tr_b16 v[96:97],v1
v_mfma_f32_16x16x32_bf16 v[160:163],v[112:115],a[128:131],v[160:163]
buffer_load_dwordx4 v25,s[48:51],s83 offen offset:1024 lds
v_mfma_f32_16x16x32_bf16 v[160:163],v[116:119],a[144:147],v[160:163]
ds_read_b64_tr_b16 v[98:99],v1 offset:5120
ds_read_b64_tr_b16 v[102:103],v1 offset:15360
v_mfma_f32_16x16x32_bf16 v[160:163],v[120:123],a[160:163],v[160:163]
ds_read_b64_tr_b16 v[106:107],v1 offset:25600
v_mfma_f32_16x16x32_bf16 v[160:163],v[124:127],a[176:179],v[160:163]
v_mfma_f32_16x16x32_bf16 v[168:171],v[124:127],a[180:183],v[168:171]
v_mfma_f32_16x16x32_bf16 v[168:171],v[120:123],a[164:167],v[168:171]
v_mfma_f32_16x16x32_bf16 v[168:171],v[116:119],a[148:151],v[168:171]
buffer_load_dwordx4 v26,s[48:51],s83 offen offset:2048 lds
v_mfma_f32_16x16x32_bf16 v[168:171],v[112:115],a[132:135],v[168:171]
v_mfma_f32_16x16x32_bf16 v[176:179],v[112:115],a[136:139],v[176:179]
v_mfma_f32_16x16x32_bf16 v[176:179],v[116:119],a[152:155],v[176:179]
v_mfma_f32_16x16x32_bf16 v[176:179],v[120:123],a[168:171],v[176:179]
v_mfma_f32_16x16x32_bf16 v[176:179],v[124:127],a[184:187],v[176:179]
v_mfma_f32_16x16x32_bf16 v[184:187],v[124:127],a[188:191],v[184:187]
buffer_load_dwordx4 v27,s[48:51],s83 offen offset:3072 lds
v_mfma_f32_16x16x32_bf16 v[184:187],v[120:123],a[172:175],v[184:187]
s_add_u32 m0,s86,86016
ds_read_b64_tr_b16 v[120:121],v2 offset:20512
ds_read_b64_tr_b16 v[122:123],v2 offset:25632
v_mfma_f32_16x16x32_bf16 v[184:187],v[116:119],a[156:159],v[184:187]
ds_read_b64_tr_b16 v[116:117],v2 offset:10272
v_mfma_f32_16x16x32_bf16 v[184:187],v[112:115],a[140:143],v[184:187]
ds_read_b64_tr_b16 v[112:113],v2 offset:32
v_mfma_f32_16x16x32_bf16 v[192:195],v[64:67],a[128:131],v[192:195]
ds_read_b64_tr_b16 v[114:115],v2 offset:5152
v_mfma_f32_16x16x32_bf16 v[192:195],v[68:71],a[144:147],v[192:195]
ds_read_b64_tr_b16 v[118:119],v2 offset:15392
ds_read_b64_tr_b16 v[124:125],v2 offset:30752
v_mfma_f32_16x16x32_bf16 v[192:195],v[72:75],a[160:163],v[192:195]
buffer_load_dwordx4 v7,s[48:51],s83 offen lds
v_mfma_f32_16x16x32_bf16 v[192:195],v[76:79],a[176:179],v[192:195]
s_add_u32 m0,s86,87040
s_add_u32 s83,s64,2
ds_read_b64_tr_b16 v[126:127],v2 offset:35872
v_mfma_f32_16x16x32_bf16 v[200:203],v[76:79],a[180:183],v[200:203]
s_and_b32 s83,s83,3
s_lshl_b32 s83,s83,1
s_or_b32 s83,s83,1
v_mfma_f32_16x16x32_bf16 v[200:203],v[72:75],a[164:167],v[200:203]
s_mul_i32 s83,s83,163840
s_add_u32 s83,s83,s85
v_mfma_f32_16x16x32_bf16 v[200:203],v[68:71],a[148:151],v[200:203]
v_mfma_f32_16x16x32_bf16 v[200:203],v[64:67],a[132:135],v[200:203]
v_mfma_f32_16x16x32_bf16 v[208:211],v[64:67],a[136:139],v[208:211]
buffer_load_dwordx4 v24,s[48:51],s84 offen lds
v_mfma_f32_16x16x32_bf16 v[208:211],v[68:71],a[152:155],v[208:211]
v_mfma_f32_16x16x32_bf16 v[208:211],v[72:75],a[168:171],v[208:211]
v_mfma_f32_16x16x32_bf16 v[208:211],v[76:79],a[184:187],v[208:211]
v_mfma_f32_16x16x32_bf16 v[216:219],v[76:79],a[188:191],v[216:219]
ds_read_b64_tr_b16 v[76:77],v1 offset:30784
ds_read_b64_tr_b16 v[78:79],v1 offset:35904
v_mfma_f32_16x16x32_bf16 v[216:219],v[72:75],a[172:175],v[216:219]
ds_read_b64_tr_b16 v[72:73],v1 offset:20544
v_mfma_f32_16x16x32_bf16 v[216:219],v[68:71],a[156:159],v[216:219]
buffer_load_dwordx2 v[4:5],v3,s[44:47],s91 offen offset:1280 sc1
v_mfma_f32_16x16x32_bf16 v[216:219],v[64:67],a[140:143],v[216:219]
s_waitcnt lgkmcnt(3)
ds_read_b64_tr_b16 v[64:65],v1 offset:64
ds_read_b64_tr_b16 v[66:67],v1 offset:5184
v_mfma_f32_16x16x32_bf16 v[224:227],v[80:83],a[128:131],v[224:227]
ds_read_b64_tr_b16 v[68:69],v1 offset:10304
v_mfma_f32_16x16x32_bf16 v[224:227],v[84:87],a[144:147],v[224:227]
ds_read_b64_tr_b16 v[70:71],v1 offset:15424
v_mfma_f32_16x16x32_bf16 v[224:227],v[88:91],a[160:163],v[224:227]
ds_read_b64_tr_b16 v[74:75],v1 offset:25664
v_mfma_f32_16x16x32_bf16 v[224:227],v[92:95],a[176:179],v[224:227]
v_mfma_f32_16x16x32_bf16 v[232:235],v[92:95],a[180:183],v[232:235]
buffer_load_dwordx4 v25,s[48:51],s84 offen offset:1024 lds
v_mfma_f32_16x16x32_bf16 v[232:235],v[88:91],a[164:167],v[232:235]
v_mfma_f32_16x16x32_bf16 v[232:235],v[84:87],a[148:151],v[232:235]
v_mfma_f32_16x16x32_bf16 v[232:235],v[80:83],a[132:135],v[232:235]
v_mfma_f32_16x16x32_bf16 v[240:243],v[80:83],a[136:139],v[240:243]
v_mfma_f32_16x16x32_bf16 v[240:243],v[84:87],a[152:155],v[240:243]
v_mfma_f32_16x16x32_bf16 v[240:243],v[88:91],a[168:171],v[240:243]
buffer_load_dwordx4 v26,s[48:51],s84 offen offset:2048 lds
v_mfma_f32_16x16x32_bf16 v[240:243],v[92:95],a[184:187],v[240:243]
v_mfma_f32_16x16x32_bf16 v[248:251],v[92:95],a[188:191],v[248:251]
ds_read_b64_tr_b16 v[92:93],v2 offset:30816
ds_read_b64_tr_b16 v[94:95],v2 offset:35936
v_mfma_f32_16x16x32_bf16 v[248:251],v[88:91],a[172:175],v[248:251]
ds_read_b64_tr_b16 v[88:89],v2 offset:20576
v_mfma_f32_16x16x32_bf16 v[248:251],v[84:87],a[156:159],v[248:251]
ds_read_b64_tr_b16 v[84:85],v2 offset:10336
v_mfma_f32_16x16x32_bf16 v[248:251],v[80:83],a[140:143],v[248:251]
s_waitcnt vmcnt(25)
ds_read_b64_tr_b16 v[80:81],v2 offset:96
v_mfma_f32_16x16x32_bf16 a[192:195],v[96:99],a[68:71],a[192:195]
buffer_load_dwordx4 v27,s[48:51],s84 offen offset:3072 lds
v_mfma_f32_16x16x32_bf16 a[192:195],v[100:103],a[76:79],a[192:195]
s_add_u32 m0,s86,91136
ds_read_b64_tr_b16 v[82:83],v2 offset:5216
ds_read_b64_tr_b16 v[86:87],v2 offset:15456
v_mfma_f32_16x16x32_bf16 a[192:195],v[104:107],a[84:87],a[192:195]
ds_read_b64_tr_b16 v[90:91],v2 offset:25696
v_mfma_f32_16x16x32_bf16 a[192:195],v[108:111],a[92:95],a[192:195]
v_mfma_f32_16x16x32_bf16 a[196:199],v[108:111],a[124:127],a[196:199]
v_mfma_f32_16x16x32_bf16 a[196:199],v[104:107],a[116:119],a[196:199]
v_mfma_f32_16x16x32_bf16 a[196:199],v[100:103],a[108:111],a[196:199]
buffer_load_dwordx4 v7,s[48:51],s84 offen lds
v_mfma_f32_16x16x32_bf16 a[196:199],v[96:99],a[100:103],a[196:199]
s_add_u32 m0,s86,40960
s_add_u32 s84,s83,20480
v_mfma_f32_16x16x32_bf16 a[200:203],v[96:99],a[64:67],a[200:203]
s_waitcnt vmcnt(25)
v_mfma_f32_16x16x32_bf16 a[200:203],v[100:103],a[72:75],a[200:203]
s_waitcnt vmcnt(22)
v_mfma_f32_16x16x32_bf16 a[200:203],v[104:107],a[80:83],a[200:203]
s_waitcnt vmcnt(18)
v_mfma_f32_16x16x32_bf16 a[200:203],v[108:111],a[88:91],a[200:203]
buffer_load_dwordx4 v[32:35],v6,s[28:31],s87 offen sc1
v_mfma_f32_16x16x32_bf16 a[204:207],v[108:111],a[120:123],a[204:207]
s_waitcnt vmcnt(17)
ds_read_b64_tr_b16 v[108:109],v1 offset:30848
ds_read_b64_tr_b16 v[110:111],v1 offset:35968
v_mfma_f32_16x16x32_bf16 a[204:207],v[104:107],a[112:115],a[204:207]
s_waitcnt vmcnt(16)
ds_read_b64_tr_b16 v[104:105],v1 offset:20608
v_mfma_f32_16x16x32_bf16 a[204:207],v[100:103],a[104:107],a[204:207]
s_waitcnt vmcnt(14)
ds_read_b64_tr_b16 v[100:101],v1 offset:10368
L_pd15_%=:
s_cbranch_execz L_trt1_%=
v_mfma_f32_16x16x32_bf16 a[204:207],v[96:99],a[96:99],a[204:207]
ds_read_b64_tr_b16 v[96:97],v1 offset:128
v_mfma_f32_16x16x32_bf16 a[208:211],v[112:115],a[68:71],a[208:211]
buffer_load_dwordx4 v[132:135],v6,s[28:31],s87 offen offset:1024 sc1
v_mfma_f32_16x16x32_bf16 a[208:211],v[116:119],a[76:79],a[208:211]
ds_read_b64_tr_b16 v[98:99],v1 offset:5248
ds_read_b64_tr_b16 v[102:103],v1 offset:15488
v_mfma_f32_16x16x32_bf16 a[208:211],v[120:123],a[84:87],a[208:211]
ds_read_b64_tr_b16 v[106:107],v1 offset:25728
v_mfma_f32_16x16x32_bf16 a[208:211],v[124:127],a[92:95],a[208:211]
v_mfma_f32_16x16x32_bf16 a[212:215],v[124:127],a[124:127],a[212:215]
v_mfma_f32_16x16x32_bf16 a[212:215],v[120:123],a[116:119],a[212:215]
buffer_load_dwordx4 v[196:199],v6,s[28:31],s87 offen offset:2048 sc1
v_mfma_f32_16x16x32_bf16 a[212:215],v[116:119],a[108:111],a[212:215]
v_mfma_f32_16x16x32_bf16 a[212:215],v[112:115],a[100:103],a[212:215]
v_mfma_f32_16x16x32_bf16 a[216:219],v[112:115],a[64:67],a[216:219]
v_mfma_f32_16x16x32_bf16 a[216:219],v[116:119],a[72:75],a[216:219]
v_mfma_f32_16x16x32_bf16 a[216:219],v[120:123],a[80:83],a[216:219]
buffer_load_dwordx4 v[36:39],v6,s[28:31],s88 offen sc1
v_mfma_f32_16x16x32_bf16 a[216:219],v[124:127],a[88:91],a[216:219]
v_mfma_f32_16x16x32_bf16 a[220:223],v[124:127],a[120:123],a[220:223]
ds_read_b64_tr_b16 v[124:125],v2 offset:30880
ds_read_b64_tr_b16 v[126:127],v2 offset:36000
v_mfma_f32_16x16x32_bf16 a[220:223],v[120:123],a[112:115],a[220:223]
ds_read_b64_tr_b16 v[120:121],v2 offset:20640
v_mfma_f32_16x16x32_bf16 a[220:223],v[116:119],a[104:107],a[220:223]
s_waitcnt lgkmcnt(3)
ds_read_b64_tr_b16 v[116:117],v2 offset:10400
v_mfma_f32_16x16x32_bf16 a[220:223],v[112:115],a[96:99],a[220:223]
buffer_load_dwordx4 v[140:143],v28,s[28:31],s87 offen offset:3072 sc1
v_mfma_f32_16x16x32_bf16 a[224:227],v[64:67],a[68:71],a[224:227]
s_add_u32 s87,s64,1
s_and_b32 s87,s87,3
s_lshl_b32 s87,s87,1
v_mfma_f32_16x16x32_bf16 a[224:227],v[68:71],a[76:79],a[224:227]
s_or_b32 s87,s87,1
s_lshl_b32 s87,s87,16
ds_read_b64_tr_b16 v[112:113],v2 offset:160
v_mfma_f32_16x16x32_bf16 a[224:227],v[72:75],a[84:87],a[224:227]
ds_read_b64_tr_b16 v[114:115],v2 offset:5280
ds_read_b64_tr_b16 v[118:119],v2 offset:15520
v_mfma_f32_16x16x32_bf16 a[224:227],v[76:79],a[92:95],a[224:227]
ds_read_b64_tr_b16 v[122:123],v2 offset:25760
v_mfma_f32_16x16x32_bf16 a[228:231],v[76:79],a[124:127],a[228:231]
buffer_load_dwordx4 v[212:215],v6,s[28:31],s88 offen offset:2048 sc1
v_mfma_f32_16x16x32_bf16 a[228:231],v[72:75],a[116:119],a[228:231]
v_mfma_f32_16x16x32_bf16 a[228:231],v[68:71],a[108:111],a[228:231]
v_mfma_f32_16x16x32_bf16 a[228:231],v[64:67],a[100:103],a[228:231]
v_mfma_f32_16x16x32_bf16 a[232:235],v[64:67],a[64:67],a[232:235]
v_mfma_f32_16x16x32_bf16 a[232:235],v[68:71],a[72:75],a[232:235]
buffer_load_dwordx4 v[148:151],v6,s[28:31],s88 offen offset:1024 sc1
v_mfma_f32_16x16x32_bf16 a[232:235],v[72:75],a[80:83],a[232:235]
s_waitcnt vmcnt(11)
v_cmp_ne_u32 s[56:57],s52,v4
v_cmp_ne_u32 s[58:59],s53,v5
v_mfma_f32_16x16x32_bf16 a[232:235],v[76:79],a[88:91],a[232:235]
s_or_b64 s[56:57],s[56:57],s[58:59]
s_cbranch_scc1 L_fr2_%=
L_fo2_%=:
v_mov_b32 v3,0
v_mfma_f32_16x16x32_bf16 a[236:239],v[76:79],a[120:123],a[236:239]
s_add_u32 s91,s64,3
ds_read_b64_tr_b16 v[76:77],v1 offset:30912
ds_read_b64_tr_b16 v[78:79],v1 offset:36032
v_mfma_f32_16x16x32_bf16 a[236:239],v[72:75],a[112:115],a[236:239]
s_and_b32 s91,s91,3
s_add_u32 s91,s91,s69
ds_read_b64_tr_b16 v[72:73],v1 offset:20672
v_mfma_f32_16x16x32_bf16 a[236:239],v[68:71],a[104:107],a[236:239]
buffer_load_dwordx4 v[40:43],v6,s[28:31],s89 offen sc1
v_mfma_f32_16x16x32_bf16 a[236:239],v[64:67],a[96:99],a[236:239]
s_add_u32 s91,s91,8
)A"
R"A(ds_read_b64_tr_b16 v[64:65],v1 offset:192
ds_read_b64_tr_b16 v[66:67],v1 offset:5312
v_mfma_f32_16x16x32_bf16 a[240:243],v[80:83],a[68:71],a[240:243]
s_lshl_b32 s91,s91,6
s_and_b32 s69,s63,s60
ds_read_b64_tr_b16 v[68:69],v1 offset:10432
v_mfma_f32_16x16x32_bf16 a[240:243],v[84:87],a[76:79],a[240:243]
s_cmp_eq_u32 s69,3
s_cselect_b32 s69,0x40000000,0
ds_read_b64_tr_b16 v[70:71],v1 offset:15552
v_mfma_f32_16x16x32_bf16 a[240:243],v[88:91],a[84:87],a[240:243]
s_add_u32 s68,s68,s69
ds_read_b64_tr_b16 v[74:75],v1 offset:25792
v_mfma_f32_16x16x32_bf16 a[240:243],v[92:95],a[92:95],a[240:243]
buffer_load_dwordx4 a[128:131],v6,s[28:31],s87 offen sc1
v_mfma_f32_16x16x32_bf16 a[244:247],v[92:95],a[124:127],a[244:247]
v_mfma_f32_16x16x32_bf16 a[244:247],v[88:91],a[116:119],a[244:247]
v_mfma_f32_16x16x32_bf16 a[244:247],v[84:87],a[108:111],a[244:247]
v_mfma_f32_16x16x32_bf16 a[244:247],v[80:83],a[100:103],a[244:247]
v_mfma_f32_16x16x32_bf16 a[248:251],v[80:83],a[64:67],a[248:251]
buffer_load_dwordx4 v[156:159],v28,s[28:31],s88 offen offset:3072 sc1
v_mfma_f32_16x16x32_bf16 a[248:251],v[84:87],a[72:75],a[248:251]
s_add_u32 s88,s87,4096
v_mfma_f32_16x16x32_bf16 a[248:251],v[88:91],a[80:83],a[248:251]
v_mfma_f32_16x16x32_bf16 a[248:251],v[92:95],a[88:91],a[248:251]
v_mfma_f32_16x16x32_bf16 a[252:255],v[92:95],a[120:123],a[252:255]
ds_read_b64_tr_b16 v[92:93],v2 offset:30944
ds_read_b64_tr_b16 v[94:95],v2 offset:36064
v_mfma_f32_16x16x32_bf16 a[252:255],v[88:91],a[112:115],a[252:255]
buffer_load_dwordx4 v[228:231],v6,s[28:31],s89 offen offset:2048 sc1
v_mfma_f32_16x16x32_bf16 a[252:255],v[84:87],a[104:107],a[252:255]
ds_read_b64_tr_b16 v[84:85],v2 offset:10464
ds_read_b64_tr_b16 v[86:87],v2 offset:15584
v_mfma_f32_16x16x32_bf16 a[252:255],v[80:83],a[96:99],a[252:255]
ds_read_b64_tr_b16 v[80:81],v2 offset:224
v_mfma_f32_16x16x32_bf16 a[0:3],v[96:99],a[68:71],a[0:3]
ds_read_b64_tr_b16 v[82:83],v2 offset:5344
v_mfma_f32_16x16x32_bf16 a[0:3],v[100:103],a[76:79],a[0:3]
ds_read_b64_tr_b16 v[88:89],v2 offset:20704
v_mfma_f32_16x16x32_bf16 a[0:3],v[104:107],a[84:87],a[0:3]
buffer_load_dwordx4 v[164:167],v6,s[28:31],s89 offen offset:1024 sc1
v_mfma_f32_16x16x32_bf16 a[0:3],v[108:111],a[92:95],a[0:3]
ds_read_b64_tr_b16 v[90:91],v2 offset:25824
v_mfma_f32_16x16x32_bf16 a[4:7],v[108:111],a[124:127],a[4:7]
v_mfma_f32_16x16x32_bf16 a[4:7],v[104:107],a[116:119],a[4:7]
v_mfma_f32_16x16x32_bf16 a[4:7],v[100:103],a[108:111],a[4:7]
v_mfma_f32_16x16x32_bf16 a[4:7],v[96:99],a[100:103],a[4:7]
buffer_load_dwordx4 v[44:47],v6,s[28:31],s90 offen sc1
v_mfma_f32_16x16x32_bf16 a[8:11],v[96:99],a[64:67],a[8:11]
v_mfma_f32_16x16x32_bf16 a[8:11],v[100:103],a[72:75],a[8:11]
v_mfma_f32_16x16x32_bf16 a[8:11],v[104:107],a[80:83],a[8:11]
v_mfma_f32_16x16x32_bf16 a[8:11],v[108:111],a[88:91],a[8:11]
v_mfma_f32_16x16x32_bf16 a[12:15],v[108:111],a[120:123],a[12:15]
buffer_load_dwordx4 a[144:147],v6,s[28:31],s88 offen sc1
v_mfma_f32_16x16x32_bf16 a[12:15],v[104:107],a[112:115],a[12:15]
ds_read_b64_tr_b16 v[104:105],v1 offset:20736
ds_read_b64_tr_b16 v[106:107],v1 offset:25856
v_mfma_f32_16x16x32_bf16 a[12:15],v[100:103],a[104:107],a[12:15]
ds_read_b64_tr_b16 v[100:101],v1 offset:10496
v_mfma_f32_16x16x32_bf16 a[12:15],v[96:99],a[96:99],a[12:15]
s_waitcnt lgkmcnt(3)
ds_read_b64_tr_b16 v[96:97],v1 offset:256
v_mfma_f32_16x16x32_bf16 a[16:19],v[112:115],a[68:71],a[16:19]
ds_read_b64_tr_b16 v[98:99],v1 offset:5376
v_mfma_f32_16x16x32_bf16 a[16:19],v[116:119],a[76:79],a[16:19]
buffer_load_dwordx4 v[172:175],v28,s[28:31],s89 offen offset:3072 sc1
v_mfma_f32_16x16x32_bf16 a[16:19],v[120:123],a[84:87],a[16:19]
s_add_u32 s89,s87,8192
ds_read_b64_tr_b16 v[102:103],v1 offset:15616
ds_read_b64_tr_b16 v[108:109],v1 offset:30976
v_mfma_f32_16x16x32_bf16 a[16:19],v[124:127],a[92:95],a[16:19]
ds_read_b64_tr_b16 v[110:111],v1 offset:36096
v_add_u32 v1,0x1e000,v1
v_mfma_f32_16x16x32_bf16 a[20:23],v[124:127],a[124:127],a[20:23]
v_mfma_f32_16x16x32_bf16 a[20:23],v[120:123],a[116:119],a[20:23]
v_mfma_f32_16x16x32_bf16 a[20:23],v[116:119],a[108:111],a[20:23]
buffer_load_dwordx4 v[180:183],v6,s[28:31],s90 offen offset:1024 sc1
v_mfma_f32_16x16x32_bf16 a[20:23],v[112:115],a[100:103],a[20:23]
v_mfma_f32_16x16x32_bf16 a[24:27],v[112:115],a[64:67],a[24:27]
v_mfma_f32_16x16x32_bf16 a[24:27],v[116:119],a[72:75],a[24:27]
v_mfma_f32_16x16x32_bf16 a[24:27],v[120:123],a[80:83],a[24:27]
v_mfma_f32_16x16x32_bf16 a[24:27],v[124:127],a[88:91],a[24:27]
buffer_load_dwordx4 a[160:163],v6,s[28:31],s89 offen sc1
v_mfma_f32_16x16x32_bf16 a[28:31],v[124:127],a[120:123],a[28:31]
ds_read_b64_tr_b16 v[124:125],v2 offset:31008
ds_read_b64_tr_b16 v[126:127],v2 offset:36128
v_mfma_f32_16x16x32_bf16 a[28:31],v[120:123],a[112:115],a[28:31]
ds_read_b64_tr_b16 v[120:121],v2 offset:20768
v_mfma_f32_16x16x32_bf16 a[28:31],v[116:119],a[104:107],a[28:31]
ds_read_b64_tr_b16 v[116:117],v2 offset:10528
v_mfma_f32_16x16x32_bf16 a[28:31],v[112:115],a[96:99],a[28:31]
ds_read_b64_tr_b16 v[112:113],v2 offset:288
v_mfma_f32_16x16x32_bf16 v[128:131],v[64:67],a[68:71],v[128:131]
buffer_load_dwordx4 v[244:247],v6,s[28:31],s90 offen offset:2048 sc1
v_mfma_f32_16x16x32_bf16 v[128:131],v[68:71],a[76:79],v[128:131]
ds_read_b64_tr_b16 v[114:115],v2 offset:5408
ds_read_b64_tr_b16 v[118:119],v2 offset:15648
v_mfma_f32_16x16x32_bf16 v[128:131],v[72:75],a[84:87],v[128:131]
ds_read_b64_tr_b16 v[122:123],v2 offset:25888
v_add_u32 v2,0x1e000,v2
v_mfma_f32_16x16x32_bf16 v[128:131],v[76:79],a[92:95],v[128:131]
v_mfma_f32_16x16x32_bf16 v[136:139],v[76:79],a[124:127],v[136:139]
v_mfma_f32_16x16x32_bf16 v[136:139],v[72:75],a[116:119],v[136:139]
buffer_load_dwordx4 v[188:191],v28,s[28:31],s90 offen offset:3072 sc1
v_mfma_f32_16x16x32_bf16 v[136:139],v[68:71],a[108:111],v[136:139]
s_add_u32 s90,s87,12288
v_mfma_f32_16x16x32_bf16 v[136:139],v[64:67],a[100:103],v[136:139]
v_mfma_f32_16x16x32_bf16 v[144:147],v[64:67],a[64:67],v[144:147]
v_mfma_f32_16x16x32_bf16 v[144:147],v[68:71],a[72:75],v[144:147]
v_mfma_f32_16x16x32_bf16 v[144:147],v[72:75],a[80:83],v[144:147]
buffer_load_dwordx4 a[176:179],v6,s[28:31],s90 offen sc1
v_mfma_f32_16x16x32_bf16 v[144:147],v[76:79],a[88:91],v[144:147]
v_mfma_f32_16x16x32_bf16 v[152:155],v[76:79],a[120:123],v[152:155]
v_mfma_f32_16x16x32_bf16 v[152:155],v[72:75],a[112:115],v[152:155]
v_mfma_f32_16x16x32_bf16 v[152:155],v[68:71],a[104:107],v[152:155]
v_mfma_f32_16x16x32_bf16 v[152:155],v[64:67],a[96:99],v[152:155]
buffer_load_dwordx4 a[180:183],v6,s[28:31],s90 offen offset:1024 sc1
s_waitcnt vmcnt(21) lgkmcnt(0)
s_barrier
v_mfma_f32_16x16x32_bf16 v[160:163],v[80:83],a[68:71],v[160:163]
ds_read_b64_tr_b16 v[64:65],v1
ds_read_b64_tr_b16 v[66:67],v1 offset:5120
v_mfma_f32_16x16x32_bf16 v[160:163],v[84:87],a[76:79],v[160:163]
ds_read_b64_tr_b16 v[68:69],v1 offset:10240
v_mfma_f32_16x16x32_bf16 v[160:163],v[88:91],a[84:87],v[160:163]
ds_read_b64_tr_b16 v[70:71],v1 offset:15360
v_mfma_f32_16x16x32_bf16 v[160:163],v[92:95],a[92:95],v[160:163]
buffer_load_dwordx4 a[164:167],v6,s[28:31],s89 offen offset:1024 sc1
v_mfma_f32_16x16x32_bf16 v[168:171],v[92:95],a[124:127],v[168:171]
ds_read_b64_tr_b16 v[72:73],v1 offset:20480
ds_read_b64_tr_b16 v[74:75],v1 offset:25600
v_mfma_f32_16x16x32_bf16 v[168:171],v[88:91],a[116:119],v[168:171]
ds_read_b64_tr_b16 v[76:77],v1 offset:30720
v_mfma_f32_16x16x32_bf16 v[168:171],v[84:87],a[108:111],v[168:171]
ds_read_b64_tr_b16 v[78:79],v1 offset:35840
v_mfma_f32_16x16x32_bf16 v[168:171],v[80:83],a[100:103],v[168:171]
v_mfma_f32_16x16x32_bf16 v[176:179],v[80:83],a[64:67],v[176:179]
v_mfma_f32_16x16x32_bf16 v[176:179],v[84:87],a[72:75],v[176:179]
buffer_load_dwordx4 v24,s[48:51],s83 offen lds
v_mfma_f32_16x16x32_bf16 v[176:179],v[88:91],a[80:83],v[176:179]
v_mfma_f32_16x16x32_bf16 v[176:179],v[92:95],a[88:91],v[176:179]
v_mfma_f32_16x16x32_bf16 v[184:187],v[92:95],a[120:123],v[184:187]
ds_read_b64_tr_b16 v[92:93],v2 offset:30752
ds_read_b64_tr_b16 v[94:95],v2 offset:35872
v_mfma_f32_16x16x32_bf16 v[184:187],v[88:91],a[112:115],v[184:187]
ds_read_b64_tr_b16 v[88:89],v2 offset:20512
v_mfma_f32_16x16x32_bf16 v[184:187],v[84:87],a[104:107],v[184:187]
buffer_load_dwordx4 a[148:151],v6,s[28:31],s88 offen offset:1024 sc1
v_mfma_f32_16x16x32_bf16 v[184:187],v[80:83],a[96:99],v[184:187]
ds_read_b64_tr_b16 v[80:81],v2 offset:32
ds_read_b64_tr_b16 v[82:83],v2 offset:5152
v_mfma_f32_16x16x32_bf16 v[192:195],v[96:99],a[68:71],v[192:195]
ds_read_b64_tr_b16 v[84:85],v2 offset:10272
v_mfma_f32_16x16x32_bf16 v[192:195],v[100:103],a[76:79],v[192:195]
ds_read_b64_tr_b16 v[86:87],v2 offset:15392
v_mfma_f32_16x16x32_bf16 v[192:195],v[104:107],a[84:87],v[192:195]
ds_read_b64_tr_b16 v[90:91],v2 offset:25632
v_mfma_f32_16x16x32_bf16 v[192:195],v[108:111],a[92:95],v[192:195]
v_mfma_f32_16x16x32_bf16 v[200:203],v[108:111],a[124:127],v[200:203]
buffer_load_dwordx4 v25,s[48:51],s83 offen offset:1024 lds
v_mfma_f32_16x16x32_bf16 v[200:203],v[104:107],a[116:119],v[200:203]
v_mfma_f32_16x16x32_bf16 v[200:203],v[100:103],a[108:111],v[200:203]
v_mfma_f32_16x16x32_bf16 v[200:203],v[96:99],a[100:103],v[200:203]
v_mfma_f32_16x16x32_bf16 v[208:211],v[96:99],a[64:67],v[208:211]
v_mfma_f32_16x16x32_bf16 v[208:211],v[100:103],a[72:75],v[208:211]
buffer_load_dwordx4 a[132:135],v6,s[28:31],s87 offen offset:1024 sc1
v_mfma_f32_16x16x32_bf16 v[208:211],v[104:107],a[80:83],v[208:211]
v_mfma_f32_16x16x32_bf16 v[208:211],v[108:111],a[88:91],v[208:211]
v_mfma_f32_16x16x32_bf16 v[216:219],v[108:111],a[120:123],v[216:219]
ds_read_b64_tr_b16 v[108:109],v1 offset:30784
ds_read_b64_tr_b16 v[110:111],v1 offset:35904
v_mfma_f32_16x16x32_bf16 v[216:219],v[104:107],a[112:115],v[216:219]
ds_read_b64_tr_b16 v[104:105],v1 offset:20544
v_mfma_f32_16x16x32_bf16 v[216:219],v[100:103],a[104:107],v[216:219]
ds_read_b64_tr_b16 v[100:101],v1 offset:10304
v_mfma_f32_16x16x32_bf16 v[216:219],v[96:99],a[96:99],v[216:219]
buffer_load_dwordx4 v26,s[48:51],s83 offen offset:2048 lds
v_mfma_f32_16x16x32_bf16 v[224:227],v[112:115],a[68:71],v[224:227]
ds_read_b64_tr_b16 v[96:97],v1 offset:64
ds_read_b64_tr_b16 v[98:99],v1 offset:5184
v_mfma_f32_16x16x32_bf16 v[224:227],v[116:119],a[76:79],v[224:227]
ds_read_b64_tr_b16 v[102:103],v1 offset:15424
v_mfma_f32_16x16x32_bf16 v[224:227],v[120:123],a[84:87],v[224:227]
ds_read_b64_tr_b16 v[106:107],v1 offset:25664
v_mfma_f32_16x16x32_bf16 v[224:227],v[124:127],a[92:95],v[224:227]
v_mfma_f32_16x16x32_bf16 v[232:235],v[124:127],a[124:127],v[232:235]
v_mfma_f32_16x16x32_bf16 v[232:235],v[120:123],a[116:119],v[232:235]
buffer_load_dwordx4 a[136:139],v6,s[28:31],s87 offen offset:2048 sc1
v_mfma_f32_16x16x32_bf16 v[232:235],v[116:119],a[108:111],v[232:235]
v_mfma_f32_16x16x32_bf16 v[232:235],v[112:115],a[100:103],v[232:235]
v_mfma_f32_16x16x32_bf16 v[240:243],v[112:115],a[64:67],v[240:243]
v_mfma_f32_16x16x32_bf16 v[240:243],v[116:119],a[72:75],v[240:243]
v_mfma_f32_16x16x32_bf16 v[240:243],v[120:123],a[80:83],v[240:243]
v_mfma_f32_16x16x32_bf16 v[240:243],v[124:127],a[88:91],v[240:243]
buffer_load_dwordx4 v27,s[48:51],s83 offen offset:3072 lds
v_mfma_f32_16x16x32_bf16 v[248:251],v[124:127],a[120:123],v[248:251]
s_add_u32 m0,s86,45056
ds_read_b64_tr_b16 v[124:125],v2 offset:30816
ds_read_b64_tr_b16 v[126:127],v2 offset:35936
v_mfma_f32_16x16x32_bf16 v[248:251],v[120:123],a[112:115],v[248:251]
ds_read_b64_tr_b16 v[120:121],v2 offset:20576
v_mfma_f32_16x16x32_bf16 v[248:251],v[116:119],a[104:107],v[248:251]
ds_read_b64_tr_b16 v[116:117],v2 offset:10336
v_mfma_f32_16x16x32_bf16 v[248:251],v[112:115],a[96:99],v[248:251]
s_waitcnt vmcnt(21) lgkmcnt(4)
ds_read_b64_tr_b16 v[112:113],v2 offset:96
v_mfma_f32_16x16x32_bf16 a[192:195],v[64:67],v[32:35],a[192:195]
s_waitcnt vmcnt(13)
ds_read_b64_tr_b16 v[114:115],v2 offset:5216
ds_read_b64_tr_b16 v[118:119],v2 offset:15456
v_mfma_f32_16x16x32_bf16 a[192:195],v[68:71],v[36:39],a[192:195]
buffer_load_dwordx4 v7,s[48:51],s83 offen lds
v_mfma_f32_16x16x32_bf16 a[192:195],v[72:75],v[40:43],a[192:195]
s_add_u32 m0,s86,46080
s_add_u32 s83,s64,3
ds_read_b64_tr_b16 v[122:123],v2 offset:25696
)A"
R"A(v_mfma_f32_16x16x32_bf16 a[192:195],v[76:79],v[44:47],a[192:195]
s_and_b32 s83,s83,3
s_lshl_b32 s83,s83,1
s_or_b32 s83,s83,17
v_mfma_f32_16x16x32_bf16 a[196:199],v[76:79],v[180:183],a[196:199]
s_mul_i32 s83,s83,163840
s_add_u32 s83,s83,s85
v_mfma_f32_16x16x32_bf16 a[196:199],v[72:75],v[164:167],a[196:199]
v_mfma_f32_16x16x32_bf16 a[196:199],v[68:71],v[148:151],a[196:199]
buffer_load_dwordx4 a[152:155],v6,s[28:31],s88 offen offset:2048 sc1
v_mfma_f32_16x16x32_bf16 a[196:199],v[64:67],v[132:135],a[196:199]
v_mfma_f32_16x16x32_bf16 a[200:203],v[64:67],v[196:199],a[200:203]
v_mfma_f32_16x16x32_bf16 a[200:203],v[68:71],v[212:215],a[200:203]
v_mfma_f32_16x16x32_bf16 a[200:203],v[72:75],v[228:231],a[200:203]
s_waitcnt vmcnt(13)
v_mfma_f32_16x16x32_bf16 a[200:203],v[76:79],v[244:247],a[200:203]
s_waitcnt vmcnt(12)
v_mfma_f32_16x16x32_bf16 a[204:207],v[76:79],v[188:191],a[204:207]
buffer_load_dwordx4 v24,s[48:51],s84 offen lds
v_mfma_f32_16x16x32_bf16 a[204:207],v[72:75],v[172:175],a[204:207]
ds_read_b64_tr_b16 v[72:73],v1 offset:20608
ds_read_b64_tr_b16 v[74:75],v1 offset:25728
v_mfma_f32_16x16x32_bf16 a[204:207],v[68:71],v[156:159],a[204:207]
ds_read_b64_tr_b16 v[68:69],v1 offset:10368
v_mfma_f32_16x16x32_bf16 a[204:207],v[64:67],v[140:143],a[204:207]
ds_read_b64_tr_b16 v[64:65],v1 offset:128
v_mfma_f32_16x16x32_bf16 a[208:211],v[80:83],v[32:35],a[208:211]
ds_read_b64_tr_b16 v[66:67],v1 offset:5248
v_mfma_f32_16x16x32_bf16 a[208:211],v[84:87],v[36:39],a[208:211]
buffer_load_dwordx4 a[168:171],v6,s[28:31],s89 offen offset:2048 sc1
v_mfma_f32_16x16x32_bf16 a[208:211],v[88:91],v[40:43],a[208:211]
ds_read_b64_tr_b16 v[70:71],v1 offset:15488
ds_read_b64_tr_b16 v[76:77],v1 offset:30848
v_mfma_f32_16x16x32_bf16 a[208:211],v[92:95],v[44:47],a[208:211]
ds_read_b64_tr_b16 v[78:79],v1 offset:35968
v_mfma_f32_16x16x32_bf16 a[212:215],v[92:95],v[180:183],a[212:215]
v_mfma_f32_16x16x32_bf16 a[212:215],v[88:91],v[164:167],a[212:215]
v_mfma_f32_16x16x32_bf16 a[212:215],v[84:87],v[148:151],a[212:215]
v_mfma_f32_16x16x32_bf16 a[212:215],v[80:83],v[132:135],a[212:215]
buffer_load_dwordx4 v25,s[48:51],s84 offen offset:1024 lds
v_mfma_f32_16x16x32_bf16 a[216:219],v[80:83],v[196:199],a[216:219]
v_mfma_f32_16x16x32_bf16 a[216:219],v[84:87],v[212:215],a[216:219]
v_mfma_f32_16x16x32_bf16 a[216:219],v[88:91],v[228:231],a[216:219]
v_mfma_f32_16x16x32_bf16 a[216:219],v[92:95],v[244:247],a[216:219]
v_mfma_f32_16x16x32_bf16 a[220:223],v[92:95],v[188:191],a[220:223]
buffer_load_dwordx4 a[184:187],v6,s[28:31],s90 offen offset:2048 sc1
v_mfma_f32_16x16x32_bf16 a[220:223],v[88:91],v[172:175],a[220:223]
ds_read_b64_tr_b16 v[88:89],v2 offset:20640
ds_read_b64_tr_b16 v[90:91],v2 offset:25760
v_mfma_f32_16x16x32_bf16 a[220:223],v[84:87],v[156:159],a[220:223]
ds_read_b64_tr_b16 v[84:85],v2 offset:10400
v_mfma_f32_16x16x32_bf16 a[220:223],v[80:83],v[140:143],a[220:223]
ds_read_b64_tr_b16 v[80:81],v2 offset:160
v_mfma_f32_16x16x32_bf16 a[224:227],v[96:99],v[32:35],a[224:227]
ds_read_b64_tr_b16 v[82:83],v2 offset:5280
v_mfma_f32_16x16x32_bf16 a[224:227],v[100:103],v[36:39],a[224:227]
ds_read_b64_tr_b16 v[86:87],v2 offset:15520
ds_read_b64_tr_b16 v[92:93],v2 offset:30880
v_mfma_f32_16x16x32_bf16 a[224:227],v[104:107],v[40:43],a[224:227]
buffer_load_dwordx4 v26,s[48:51],s84 offen offset:2048 lds
v_mfma_f32_16x16x32_bf16 a[224:227],v[108:111],v[44:47],a[224:227]
ds_read_b64_tr_b16 v[94:95],v2 offset:36000
v_mfma_f32_16x16x32_bf16 a[228:231],v[108:111],v[180:183],a[228:231]
v_mfma_f32_16x16x32_bf16 a[228:231],v[104:107],v[164:167],a[228:231]
v_mfma_f32_16x16x32_bf16 a[228:231],v[100:103],v[148:151],a[228:231]
v_mfma_f32_16x16x32_bf16 a[228:231],v[96:99],v[132:135],a[228:231]
v_mfma_f32_16x16x32_bf16 a[232:235],v[96:99],v[196:199],a[232:235]
buffer_load_dwordx4 v27,s[48:51],s84 offen offset:3072 lds
v_mfma_f32_16x16x32_bf16 a[232:235],v[100:103],v[212:215],a[232:235]
s_add_u32 m0,s86,50176
v_mfma_f32_16x16x32_bf16 a[232:235],v[104:107],v[228:231],a[232:235]
v_mfma_f32_16x16x32_bf16 a[232:235],v[108:111],v[244:247],a[232:235]
v_mfma_f32_16x16x32_bf16 a[236:239],v[108:111],v[188:191],a[236:239]
ds_read_b64_tr_b16 v[108:109],v1 offset:30912
ds_read_b64_tr_b16 v[110:111],v1 offset:36032
v_mfma_f32_16x16x32_bf16 a[236:239],v[104:107],v[172:175],a[236:239]
buffer_load_dwordx4 a[188:191],v28,s[28:31],s90 offen offset:3072 sc1
v_mfma_f32_16x16x32_bf16 a[236:239],v[100:103],v[156:159],a[236:239]
ds_read_b64_tr_b16 v[100:101],v1 offset:10432
ds_read_b64_tr_b16 v[102:103],v1 offset:15552
v_mfma_f32_16x16x32_bf16 a[236:239],v[96:99],v[140:143],a[236:239]
s_waitcnt lgkmcnt(4)
ds_read_b64_tr_b16 v[96:97],v1 offset:192
v_mfma_f32_16x16x32_bf16 a[240:243],v[112:115],v[32:35],a[240:243]
ds_read_b64_tr_b16 v[98:99],v1 offset:5312
v_mfma_f32_16x16x32_bf16 a[240:243],v[116:119],v[36:39],a[240:243]
ds_read_b64_tr_b16 v[104:105],v1 offset:20672
v_mfma_f32_16x16x32_bf16 a[240:243],v[120:123],v[40:43],a[240:243]
ds_read_b64_tr_b16 v[106:107],v1 offset:25792
v_mfma_f32_16x16x32_bf16 a[240:243],v[124:127],v[44:47],a[240:243]
buffer_load_dwordx4 v7,s[48:51],s84 offen lds
v_mfma_f32_16x16x32_bf16 a[244:247],v[124:127],v[180:183],a[244:247]
s_add_u32 m0,s86,0
s_add_u32 s84,s83,20480
v_mfma_f32_16x16x32_bf16 a[244:247],v[120:123],v[164:167],a[244:247]
v_mfma_f32_16x16x32_bf16 a[244:247],v[116:119],v[148:151],a[244:247]
v_mfma_f32_16x16x32_bf16 a[244:247],v[112:115],v[132:135],a[244:247]
v_mfma_f32_16x16x32_bf16 a[248:251],v[112:115],v[196:199],a[248:251]
buffer_load_dwordx4 a[172:175],v28,s[28:31],s89 offen offset:3072 sc1
v_mfma_f32_16x16x32_bf16 a[248:251],v[116:119],v[212:215],a[248:251]
v_mfma_f32_16x16x32_bf16 a[248:251],v[120:123],v[228:231],a[248:251]
v_mfma_f32_16x16x32_bf16 a[248:251],v[124:127],v[244:247],a[248:251]
v_mfma_f32_16x16x32_bf16 a[252:255],v[124:127],v[188:191],a[252:255]
ds_read_b64_tr_b16 v[124:125],v2 offset:30944
ds_read_b64_tr_b16 v[126:127],v2 offset:36064
v_mfma_f32_16x16x32_bf16 a[252:255],v[120:123],v[172:175],a[252:255]
ds_read_b64_tr_b16 v[120:121],v2 offset:20704
v_mfma_f32_16x16x32_bf16 a[252:255],v[116:119],v[156:159],a[252:255]
buffer_load_dwordx4 v24,s[48:51],s83 offen lds
v_mfma_f32_16x16x32_bf16 a[252:255],v[112:115],v[140:143],a[252:255]
ds_read_b64_tr_b16 v[112:113],v2 offset:224
ds_read_b64_tr_b16 v[114:115],v2 offset:5344
v_mfma_f32_16x16x32_bf16 a[0:3],v[64:67],v[32:35],a[0:3]
ds_read_b64_tr_b16 v[116:117],v2 offset:10464
v_mfma_f32_16x16x32_bf16 a[0:3],v[68:71],v[36:39],a[0:3]
ds_read_b64_tr_b16 v[118:119],v2 offset:15584
v_mfma_f32_16x16x32_bf16 a[0:3],v[72:75],v[40:43],a[0:3]
ds_read_b64_tr_b16 v[122:123],v2 offset:25824
v_mfma_f32_16x16x32_bf16 a[0:3],v[76:79],v[44:47],a[0:3]
buffer_load_dwordx4 a[156:159],v28,s[28:31],s88 offen offset:3072 sc1
v_mfma_f32_16x16x32_bf16 a[4:7],v[76:79],v[180:183],a[4:7]
v_mfma_f32_16x16x32_bf16 a[4:7],v[72:75],v[164:167],a[4:7]
v_mfma_f32_16x16x32_bf16 a[4:7],v[68:71],v[148:151],a[4:7]
v_mfma_f32_16x16x32_bf16 a[4:7],v[64:67],v[132:135],a[4:7]
v_mfma_f32_16x16x32_bf16 a[8:11],v[64:67],v[196:199],a[8:11]
v_mfma_f32_16x16x32_bf16 a[8:11],v[68:71],v[212:215],a[8:11]
buffer_load_dwordx4 v25,s[48:51],s83 offen offset:1024 lds
v_mfma_f32_16x16x32_bf16 a[8:11],v[72:75],v[228:231],a[8:11]
v_mfma_f32_16x16x32_bf16 a[8:11],v[76:79],v[244:247],a[8:11]
v_mfma_f32_16x16x32_bf16 a[12:15],v[76:79],v[188:191],a[12:15]
ds_read_b64_tr_b16 v[76:77],v1 offset:30976
ds_read_b64_tr_b16 v[78:79],v1 offset:36096
v_mfma_f32_16x16x32_bf16 a[12:15],v[72:75],v[172:175],a[12:15]
ds_read_b64_tr_b16 v[72:73],v1 offset:20736
v_mfma_f32_16x16x32_bf16 a[12:15],v[68:71],v[156:159],a[12:15]
ds_read_b64_tr_b16 v[68:69],v1 offset:10496
v_mfma_f32_16x16x32_bf16 a[12:15],v[64:67],v[140:143],a[12:15]
buffer_load_dwordx4 v26,s[48:51],s83 offen offset:2048 lds
v_mfma_f32_16x16x32_bf16 a[16:19],v[80:83],v[32:35],a[16:19]
ds_read_b64_tr_b16 v[64:65],v1 offset:256
ds_read_b64_tr_b16 v[66:67],v1 offset:5376
v_mfma_f32_16x16x32_bf16 a[16:19],v[84:87],v[36:39],a[16:19]
ds_read_b64_tr_b16 v[70:71],v1 offset:15616
v_mfma_f32_16x16x32_bf16 a[16:19],v[88:91],v[40:43],a[16:19]
ds_read_b64_tr_b16 v[74:75],v1 offset:25856
v_add_u32 v1,0xffff6000,v1
v_mfma_f32_16x16x32_bf16 a[16:19],v[92:95],v[44:47],a[16:19]
v_mfma_f32_16x16x32_bf16 a[20:23],v[92:95],v[180:183],a[20:23]
buffer_load_dwordx4 a[140:143],v28,s[28:31],s87 offen offset:3072 sc1
v_mfma_f32_16x16x32_bf16 a[20:23],v[88:91],v[164:167],a[20:23]
s_add_u32 s87,s64,2
s_and_b32 s87,s87,3
s_lshl_b32 s87,s87,1
v_mfma_f32_16x16x32_bf16 a[20:23],v[84:87],v[148:151],a[20:23]
s_or_b32 s87,s87,1
s_lshl_b32 s87,s87,16
s_add_u32 s88,s87,4096
v_mfma_f32_16x16x32_bf16 a[20:23],v[80:83],v[132:135],a[20:23]
s_add_u32 s89,s87,8192
s_add_u32 s90,s87,12288
v_mfma_f32_16x16x32_bf16 a[24:27],v[80:83],v[196:199],a[24:27]
v_mfma_f32_16x16x32_bf16 a[24:27],v[84:87],v[212:215],a[24:27]
v_mfma_f32_16x16x32_bf16 a[24:27],v[88:91],v[228:231],a[24:27]
buffer_load_dwordx4 v27,s[48:51],s83 offen offset:3072 lds
v_mfma_f32_16x16x32_bf16 a[24:27],v[92:95],v[244:247],a[24:27]
s_add_u32 m0,s86,4096
v_mfma_f32_16x16x32_bf16 a[28:31],v[92:95],v[188:191],a[28:31]
ds_read_b64_tr_b16 v[92:93],v2 offset:31008
ds_read_b64_tr_b16 v[94:95],v2 offset:36128
v_mfma_f32_16x16x32_bf16 a[28:31],v[88:91],v[172:175],a[28:31]
ds_read_b64_tr_b16 v[88:89],v2 offset:20768
v_mfma_f32_16x16x32_bf16 a[28:31],v[84:87],v[156:159],a[28:31]
ds_read_b64_tr_b16 v[84:85],v2 offset:10528
v_mfma_f32_16x16x32_bf16 a[28:31],v[80:83],v[140:143],a[28:31]
s_waitcnt lgkmcnt(4)
ds_read_b64_tr_b16 v[80:81],v2 offset:288
v_mfma_f32_16x16x32_bf16 v[128:131],v[96:99],v[32:35],v[128:131]
buffer_load_dwordx4 v7,s[48:51],s83 offen lds
v_mfma_f32_16x16x32_bf16 v[128:131],v[100:103],v[36:39],v[128:131]
s_add_u32 m0,s86,5120
ds_read_b64_tr_b16 v[82:83],v2 offset:5408
ds_read_b64_tr_b16 v[86:87],v2 offset:15648
v_mfma_f32_16x16x32_bf16 v[128:131],v[104:107],v[40:43],v[128:131]
s_add_u32 s83,s64,3
ds_read_b64_tr_b16 v[90:91],v2 offset:25888
v_add_u32 v2,0xffff6000,v2
v_mfma_f32_16x16x32_bf16 v[128:131],v[108:111],v[44:47],v[128:131]
s_and_b32 s83,s83,3
s_lshl_b32 s83,s83,1
s_or_b32 s83,s83,1
v_mfma_f32_16x16x32_bf16 v[136:139],v[108:111],v[180:183],v[136:139]
s_mul_i32 s83,s83,163840
s_add_u32 s83,s83,s85
v_mfma_f32_16x16x32_bf16 v[136:139],v[104:107],v[164:167],v[136:139]
v_mfma_f32_16x16x32_bf16 v[136:139],v[100:103],v[148:151],v[136:139]
buffer_load_dwordx4 v24,s[48:51],s84 offen lds
v_mfma_f32_16x16x32_bf16 v[136:139],v[96:99],v[132:135],v[136:139]
v_mfma_f32_16x16x32_bf16 v[144:147],v[96:99],v[196:199],v[144:147]
v_mfma_f32_16x16x32_bf16 v[144:147],v[100:103],v[212:215],v[144:147]
v_mfma_f32_16x16x32_bf16 v[144:147],v[104:107],v[228:231],v[144:147]
v_mfma_f32_16x16x32_bf16 v[144:147],v[108:111],v[244:247],v[144:147]
v_mfma_f32_16x16x32_bf16 v[152:155],v[108:111],v[188:191],v[152:155]
buffer_load_dwordx4 v25,s[48:51],s84 offen offset:1024 lds
v_mfma_f32_16x16x32_bf16 v[152:155],v[104:107],v[172:175],v[152:155]
ds_read_b64_tr_b16 v[104:105],v1 offset:20480
ds_read_b64_tr_b16 v[106:107],v1 offset:25600
v_mfma_f32_16x16x32_bf16 v[152:155],v[100:103],v[156:159],v[152:155]
ds_read_b64_tr_b16 v[100:101],v1 offset:10240
v_mfma_f32_16x16x32_bf16 v[152:155],v[96:99],v[140:143],v[152:155]
ds_read_b64_tr_b16 v[96:97],v1
v_mfma_f32_16x16x32_bf16 v[160:163],v[112:115],v[32:35],v[160:163]
ds_read_b64_tr_b16 v[98:99],v1 offset:5120
v_mfma_f32_16x16x32_bf16 v[160:163],v[116:119],v[36:39],v[160:163]
ds_read_b64_tr_b16 v[102:103],v1 offset:15360
ds_read_b64_tr_b16 v[108:109],v1 offset:30720
v_mfma_f32_16x16x32_bf16 v[160:163],v[120:123],v[40:43],v[160:163]
buffer_load_dwordx4 v26,s[48:51],s84 offen offset:2048 lds
v_mfma_f32_16x16x32_bf16 v[160:163],v[124:127],v[44:47],v[160:163]
ds_read_b64_tr_b16 v[110:111],v1 offset:35840
v_mfma_f32_16x16x32_bf16 v[168:171],v[124:127],v[180:183],v[168:171]
v_mfma_f32_16x16x32_bf16 v[168:171],v[120:123],v[164:167],v[168:171]
v_mfma_f32_16x16x32_bf16 v[168:171],v[116:119],v[148:151],v[168:171]
v_mfma_f32_16x16x32_bf16 v[168:171],v[112:115],v[132:135],v[168:171]
)A"
R"A(v_mfma_f32_16x16x32_bf16 v[176:179],v[112:115],v[196:199],v[176:179]
buffer_load_dwordx4 v27,s[48:51],s84 offen offset:3072 lds
v_mfma_f32_16x16x32_bf16 v[176:179],v[116:119],v[212:215],v[176:179]
s_add_u32 m0,s86,9216
v_mfma_f32_16x16x32_bf16 v[176:179],v[120:123],v[228:231],v[176:179]
v_mfma_f32_16x16x32_bf16 v[176:179],v[124:127],v[244:247],v[176:179]
v_mfma_f32_16x16x32_bf16 v[184:187],v[124:127],v[188:191],v[184:187]
ds_read_b64_tr_b16 v[124:125],v2 offset:30752
ds_read_b64_tr_b16 v[126:127],v2 offset:35872
v_mfma_f32_16x16x32_bf16 v[184:187],v[120:123],v[172:175],v[184:187]
ds_read_b64_tr_b16 v[120:121],v2 offset:20512
v_mfma_f32_16x16x32_bf16 v[184:187],v[116:119],v[156:159],v[184:187]
buffer_load_dwordx4 v7,s[48:51],s84 offen lds
v_mfma_f32_16x16x32_bf16 v[184:187],v[112:115],v[140:143],v[184:187]
s_add_u32 m0,s86,122880
ds_read_b64_tr_b16 v[112:113],v2 offset:32
ds_read_b64_tr_b16 v[114:115],v2 offset:5152
v_mfma_f32_16x16x32_bf16 v[192:195],v[64:67],v[32:35],v[192:195]
s_add_u32 s84,s83,20480
ds_read_b64_tr_b16 v[116:117],v2 offset:10272
v_mfma_f32_16x16x32_bf16 v[192:195],v[68:71],v[36:39],v[192:195]
ds_read_b64_tr_b16 v[118:119],v2 offset:15392
v_mfma_f32_16x16x32_bf16 v[192:195],v[72:75],v[40:43],v[192:195]
s_nop 0
L_pd16_%=:
s_cbranch_execz L_trtg2F_%=
ds_read_b64_tr_b16 v[122:123],v2 offset:25632
v_mfma_f32_16x16x32_bf16 v[192:195],v[76:79],v[44:47],v[192:195]
buffer_load_dwordx4 a[68:71],v6,s[28:31],s87 offen sc1
v_mfma_f32_16x16x32_bf16 v[200:203],v[76:79],v[180:183],v[200:203]
v_mfma_f32_16x16x32_bf16 v[200:203],v[72:75],v[164:167],v[200:203]
v_mfma_f32_16x16x32_bf16 v[200:203],v[68:71],v[148:151],v[200:203]
v_mfma_f32_16x16x32_bf16 v[200:203],v[64:67],v[132:135],v[200:203]
v_mfma_f32_16x16x32_bf16 v[208:211],v[64:67],v[196:199],v[208:211]
buffer_load_dwordx4 a[100:103],v6,s[28:31],s87 offen offset:1024 sc1
v_mfma_f32_16x16x32_bf16 v[208:211],v[68:71],v[212:215],v[208:211]
v_mfma_f32_16x16x32_bf16 v[208:211],v[72:75],v[228:231],v[208:211]
v_mfma_f32_16x16x32_bf16 v[208:211],v[76:79],v[244:247],v[208:211]
v_mfma_f32_16x16x32_bf16 v[216:219],v[76:79],v[188:191],v[216:219]
ds_read_b64_tr_b16 v[76:77],v1 offset:30784
ds_read_b64_tr_b16 v[78:79],v1 offset:35904
v_mfma_f32_16x16x32_bf16 v[216:219],v[72:75],v[172:175],v[216:219]
buffer_load_dwordx2 v[4:5],v3,s[44:47],s91 offen offset:1280 sc1
v_mfma_f32_16x16x32_bf16 v[216:219],v[68:71],v[156:159],v[216:219]
ds_read_b64_tr_b16 v[68:69],v1 offset:10304
ds_read_b64_tr_b16 v[70:71],v1 offset:15424
v_mfma_f32_16x16x32_bf16 v[216:219],v[64:67],v[140:143],v[216:219]
s_waitcnt lgkmcnt(4)
ds_read_b64_tr_b16 v[64:65],v1 offset:64
v_mfma_f32_16x16x32_bf16 v[224:227],v[80:83],v[32:35],v[224:227]
ds_read_b64_tr_b16 v[66:67],v1 offset:5184
v_mfma_f32_16x16x32_bf16 v[224:227],v[84:87],v[36:39],v[224:227]
ds_read_b64_tr_b16 v[72:73],v1 offset:20544
v_mfma_f32_16x16x32_bf16 v[224:227],v[88:91],v[40:43],v[224:227]
buffer_load_dwordx4 a[64:67],v6,s[28:31],s87 offen offset:2048 sc1
v_mfma_f32_16x16x32_bf16 v[224:227],v[92:95],v[44:47],v[224:227]
ds_read_b64_tr_b16 v[74:75],v1 offset:25664
v_mfma_f32_16x16x32_bf16 v[232:235],v[92:95],v[180:183],v[232:235]
v_mfma_f32_16x16x32_bf16 v[232:235],v[88:91],v[164:167],v[232:235]
v_mfma_f32_16x16x32_bf16 v[232:235],v[84:87],v[148:151],v[232:235]
v_mfma_f32_16x16x32_bf16 v[232:235],v[80:83],v[132:135],v[232:235]
buffer_load_dwordx4 a[76:79],v6,s[28:31],s88 offen sc1
v_mfma_f32_16x16x32_bf16 v[240:243],v[80:83],v[196:199],v[240:243]
v_mfma_f32_16x16x32_bf16 v[240:243],v[84:87],v[212:215],v[240:243]
v_mfma_f32_16x16x32_bf16 v[240:243],v[88:91],v[228:231],v[240:243]
v_mfma_f32_16x16x32_bf16 v[240:243],v[92:95],v[244:247],v[240:243]
v_mfma_f32_16x16x32_bf16 v[248:251],v[92:95],v[188:191],v[248:251]
buffer_load_dwordx4 a[96:99],v28,s[28:31],s87 offen offset:3072 sc1
v_mfma_f32_16x16x32_bf16 v[248:251],v[88:91],v[172:175],v[248:251]
s_add_u32 s87,s64,3
ds_read_b64_tr_b16 v[88:89],v2 offset:20576
ds_read_b64_tr_b16 v[90:91],v2 offset:25696
v_mfma_f32_16x16x32_bf16 v[248:251],v[84:87],v[156:159],v[248:251]
s_and_b32 s87,s87,3
s_lshl_b32 s87,s87,1
ds_read_b64_tr_b16 v[84:85],v2 offset:10336
v_mfma_f32_16x16x32_bf16 v[248:251],v[80:83],v[140:143],v[248:251]
s_or_b32 s87,s87,17
s_lshl_b32 s87,s87,16
ds_read_b64_tr_b16 v[80:81],v2 offset:96
v_mfma_f32_16x16x32_bf16 a[192:195],v[96:99],a[128:131],a[192:195]
ds_read_b64_tr_b16 v[82:83],v2 offset:5216
v_mfma_f32_16x16x32_bf16 a[192:195],v[100:103],a[144:147],a[192:195]
buffer_load_dwordx4 a[72:75],v6,s[28:31],s88 offen offset:2048 sc1
v_mfma_f32_16x16x32_bf16 a[192:195],v[104:107],a[160:163],a[192:195]
s_waitcnt vmcnt(37)
ds_read_b64_tr_b16 v[86:87],v2 offset:15456
ds_read_b64_tr_b16 v[92:93],v2 offset:30816
v_mfma_f32_16x16x32_bf16 a[192:195],v[108:111],a[176:179],a[192:195]
ds_read_b64_tr_b16 v[94:95],v2 offset:35936
v_mfma_f32_16x16x32_bf16 a[196:199],v[108:111],a[180:183],a[196:199]
v_mfma_f32_16x16x32_bf16 a[196:199],v[104:107],a[164:167],a[196:199]
s_waitcnt vmcnt(33)
v_mfma_f32_16x16x32_bf16 a[196:199],v[100:103],a[148:151],a[196:199]
buffer_load_dwordx4 a[108:111],v6,s[28:31],s88 offen offset:1024 sc1
v_mfma_f32_16x16x32_bf16 a[196:199],v[96:99],a[132:135],a[196:199]
s_waitcnt vmcnt(32)
v_mfma_f32_16x16x32_bf16 a[200:203],v[96:99],a[136:139],a[200:203]
s_waitcnt vmcnt(29)
v_mfma_f32_16x16x32_bf16 a[200:203],v[100:103],a[152:155],a[200:203]
s_waitcnt vmcnt(27)
v_mfma_f32_16x16x32_bf16 a[200:203],v[104:107],a[168:171],a[200:203]
s_waitcnt vmcnt(22)
v_mfma_f32_16x16x32_bf16 a[200:203],v[108:111],a[184:187],a[200:203]
buffer_load_dwordx4 a[84:87],v6,s[28:31],s89 offen sc1
v_mfma_f32_16x16x32_bf16 a[204:207],v[108:111],a[188:191],a[204:207]
s_waitcnt vmcnt(21)
ds_read_b64_tr_b16 v[108:109],v1 offset:30848
ds_read_b64_tr_b16 v[110:111],v1 offset:35968
v_mfma_f32_16x16x32_bf16 a[204:207],v[104:107],a[172:175],a[204:207]
s_waitcnt vmcnt(19)
ds_read_b64_tr_b16 v[104:105],v1 offset:20608
v_mfma_f32_16x16x32_bf16 a[204:207],v[100:103],a[156:159],a[204:207]
s_waitcnt vmcnt(16)
ds_read_b64_tr_b16 v[100:101],v1 offset:10368
v_mfma_f32_16x16x32_bf16 a[204:207],v[96:99],a[140:143],a[204:207]
ds_read_b64_tr_b16 v[96:97],v1 offset:128
v_mfma_f32_16x16x32_bf16 a[208:211],v[112:115],a[128:131],a[208:211]
buffer_load_dwordx4 a[104:107],v28,s[28:31],s88 offen offset:3072 sc1
v_mfma_f32_16x16x32_bf16 a[208:211],v[116:119],a[144:147],a[208:211]
s_add_u32 s88,s87,4096
ds_read_b64_tr_b16 v[98:99],v1 offset:5248
ds_read_b64_tr_b16 v[102:103],v1 offset:15488
v_mfma_f32_16x16x32_bf16 a[208:211],v[120:123],a[160:163],a[208:211]
ds_read_b64_tr_b16 v[106:107],v1 offset:25728
v_mfma_f32_16x16x32_bf16 a[208:211],v[124:127],a[176:179],a[208:211]
v_mfma_f32_16x16x32_bf16 a[212:215],v[124:127],a[180:183],a[212:215]
v_mfma_f32_16x16x32_bf16 a[212:215],v[120:123],a[164:167],a[212:215]
buffer_load_dwordx4 a[80:83],v6,s[28:31],s89 offen offset:2048 sc1
v_mfma_f32_16x16x32_bf16 a[212:215],v[116:119],a[148:151],a[212:215]
v_mfma_f32_16x16x32_bf16 a[212:215],v[112:115],a[132:135],a[212:215]
v_mfma_f32_16x16x32_bf16 a[216:219],v[112:115],a[136:139],a[216:219]
v_mfma_f32_16x16x32_bf16 a[216:219],v[116:119],a[152:155],a[216:219]
v_mfma_f32_16x16x32_bf16 a[216:219],v[120:123],a[168:171],a[216:219]
buffer_load_dwordx4 a[116:119],v6,s[28:31],s89 offen offset:1024 sc1
v_mfma_f32_16x16x32_bf16 a[216:219],v[124:127],a[184:187],a[216:219]
v_mfma_f32_16x16x32_bf16 a[220:223],v[124:127],a[188:191],a[220:223]
ds_read_b64_tr_b16 v[124:125],v2 offset:30880
ds_read_b64_tr_b16 v[126:127],v2 offset:36000
v_mfma_f32_16x16x32_bf16 a[220:223],v[120:123],a[172:175],a[220:223]
ds_read_b64_tr_b16 v[120:121],v2 offset:20640
v_mfma_f32_16x16x32_bf16 a[220:223],v[116:119],a[156:159],a[220:223]
s_waitcnt lgkmcnt(3)
ds_read_b64_tr_b16 v[116:117],v2 offset:10400
v_mfma_f32_16x16x32_bf16 a[220:223],v[112:115],a[140:143],a[220:223]
buffer_load_dwordx4 a[92:95],v6,s[28:31],s90 offen sc1
v_mfma_f32_16x16x32_bf16 a[224:227],v[64:67],a[128:131],a[224:227]
ds_read_b64_tr_b16 v[112:113],v2 offset:160
ds_read_b64_tr_b16 v[114:115],v2 offset:5280
v_mfma_f32_16x16x32_bf16 a[224:227],v[68:71],a[144:147],a[224:227]
ds_read_b64_tr_b16 v[118:119],v2 offset:15520
v_mfma_f32_16x16x32_bf16 a[224:227],v[72:75],a[160:163],a[224:227]
ds_read_b64_tr_b16 v[122:123],v2 offset:25760
v_mfma_f32_16x16x32_bf16 a[224:227],v[76:79],a[176:179],a[224:227]
v_mfma_f32_16x16x32_bf16 a[228:231],v[76:79],a[180:183],a[228:231]
buffer_load_dwordx4 a[112:115],v28,s[28:31],s89 offen offset:3072 sc1
v_mfma_f32_16x16x32_bf16 a[228:231],v[72:75],a[164:167],a[228:231]
s_add_u32 s89,s87,8192
v_mfma_f32_16x16x32_bf16 a[228:231],v[68:71],a[148:151],a[228:231]
v_mfma_f32_16x16x32_bf16 a[228:231],v[64:67],a[132:135],a[228:231]
v_mfma_f32_16x16x32_bf16 a[232:235],v[64:67],a[136:139],a[232:235]
v_mfma_f32_16x16x32_bf16 a[232:235],v[68:71],a[152:155],a[232:235]
buffer_load_dwordx4 a[124:127],v6,s[28:31],s90 offen offset:1024 sc1
v_mfma_f32_16x16x32_bf16 a[232:235],v[72:75],a[168:171],a[232:235]
s_waitcnt vmcnt(12)
v_cmp_ne_u32 s[56:57],s52,v4
v_cmp_ne_u32 s[58:59],s53,v5
v_mfma_f32_16x16x32_bf16 a[232:235],v[76:79],a[184:187],a[232:235]
s_or_b64 s[56:57],s[56:57],s[58:59]
s_cbranch_scc1 L_fr3_%=
L_fo3_%=:
s_lshl_b32 s91,s64,6
v_mfma_f32_16x16x32_bf16 a[236:239],v[76:79],a[188:191],a[236:239]
ds_read_b64_tr_b16 v[76:77],v1 offset:30912
ds_read_b64_tr_b16 v[78:79],v1 offset:36032
v_mfma_f32_16x16x32_bf16 a[236:239],v[72:75],a[172:175],a[236:239]
ds_read_b64_tr_b16 v[72:73],v1 offset:20672
v_bfe_u32 v3,v6,4,2
v_mfma_f32_16x16x32_bf16 a[236:239],v[68:71],a[156:159],a[236:239]
buffer_load_dwordx4 v[32:35],v6,s[28:31],s87 offen sc1
v_mfma_f32_16x16x32_bf16 a[236:239],v[64:67],a[140:143],a[236:239]
ds_read_b64_tr_b16 v[64:65],v1 offset:192
ds_read_b64_tr_b16 v[66:67],v1 offset:5312
v_mfma_f32_16x16x32_bf16 a[240:243],v[80:83],a[128:131],a[240:243]
ds_read_b64_tr_b16 v[68:69],v1 offset:10432
v_min_u32 v3,2,v3
v_mfma_f32_16x16x32_bf16 a[240:243],v[84:87],a[144:147],a[240:243]
ds_read_b64_tr_b16 v[70:71],v1 offset:15552
v_add3_u32 v3,v3,s63,1
v_mfma_f32_16x16x32_bf16 a[240:243],v[88:91],a[160:163],a[240:243]
ds_read_b64_tr_b16 v[74:75],v1 offset:25792
v_and_b32 v3,3,v3
v_mfma_f32_16x16x32_bf16 a[240:243],v[92:95],a[176:179],a[240:243]
buffer_load_dwordx4 a[88:91],v6,s[28:31],s90 offen offset:2048 sc1
v_mfma_f32_16x16x32_bf16 a[244:247],v[92:95],a[180:183],a[244:247]
v_lshlrev_b32 v3,8,v3
v_mfma_f32_16x16x32_bf16 a[244:247],v[88:91],a[164:167],a[244:247]
v_mfma_f32_16x16x32_bf16 a[244:247],v[84:87],a[148:151],a[244:247]
v_mfma_f32_16x16x32_bf16 a[244:247],v[80:83],a[132:135],a[244:247]
v_mfma_f32_16x16x32_bf16 a[248:251],v[80:83],a[136:139],a[248:251]
buffer_load_dwordx4 v[36:39],v6,s[28:31],s88 offen sc1
v_mfma_f32_16x16x32_bf16 a[248:251],v[84:87],a[152:155],a[248:251]
v_mfma_f32_16x16x32_bf16 a[248:251],v[88:91],a[168:171],a[248:251]
v_mfma_f32_16x16x32_bf16 a[248:251],v[92:95],a[184:187],a[248:251]
v_mfma_f32_16x16x32_bf16 a[252:255],v[92:95],a[188:191],a[252:255]
ds_read_b64_tr_b16 v[92:93],v2 offset:30944
ds_read_b64_tr_b16 v[94:95],v2 offset:36064
v_mfma_f32_16x16x32_bf16 a[252:255],v[88:91],a[172:175],a[252:255]
buffer_load_dwordx4 a[120:123],v28,s[28:31],s90 offen offset:3072 sc1
v_mfma_f32_16x16x32_bf16 a[252:255],v[84:87],a[156:159],a[252:255]
s_add_u32 s90,s87,12288
ds_read_b64_tr_b16 v[84:85],v2 offset:10464
ds_read_b64_tr_b16 v[86:87],v2 offset:15584
v_mfma_f32_16x16x32_bf16 a[252:255],v[80:83],a[140:143],a[252:255]
ds_read_b64_tr_b16 v[80:81],v2 offset:224
v_mfma_f32_16x16x32_bf16 a[0:3],v[96:99],a[128:131],a[0:3]
ds_read_b64_tr_b16 v[82:83],v2 offset:5344
v_mfma_f32_16x16x32_bf16 a[0:3],v[100:103],a[144:147],a[0:3]
ds_read_b64_tr_b16 v[88:89],v2 offset:20704
v_mfma_f32_16x16x32_bf16 a[0:3],v[104:107],a[160:163],a[0:3]
buffer_load_dwordx4 v[40:43],v6,s[28:31],s89 offen sc1
v_mfma_f32_16x16x32_bf16 a[0:3],v[108:111],a[176:179],a[0:3]
ds_read_b64_tr_b16 v[90:91],v2 offset:25824
v_mfma_f32_16x16x32_bf16 a[4:7],v[108:111],a[180:183],a[4:7]
v_mfma_f32_16x16x32_bf16 a[4:7],v[104:107],a[164:167],a[4:7]
)A"
R"A(v_mfma_f32_16x16x32_bf16 a[4:7],v[100:103],a[148:151],a[4:7]
v_mfma_f32_16x16x32_bf16 a[4:7],v[96:99],a[132:135],a[4:7]
v_mfma_f32_16x16x32_bf16 a[8:11],v[96:99],a[136:139],a[8:11]
v_mfma_f32_16x16x32_bf16 a[8:11],v[100:103],a[152:155],a[8:11]
v_mfma_f32_16x16x32_bf16 a[8:11],v[104:107],a[168:171],a[8:11]
v_mfma_f32_16x16x32_bf16 a[8:11],v[108:111],a[184:187],a[8:11]
v_mfma_f32_16x16x32_bf16 a[12:15],v[108:111],a[188:191],a[12:15]
ds_read_b64_tr_b16 v[108:109],v1 offset:30976
ds_read_b64_tr_b16 v[110:111],v1 offset:36096
v_mfma_f32_16x16x32_bf16 a[12:15],v[104:107],a[172:175],a[12:15]
buffer_load_dwordx4 v[44:47],v6,s[28:31],s90 offen sc1
v_mfma_f32_16x16x32_bf16 a[12:15],v[100:103],a[156:159],a[12:15]
ds_read_b64_tr_b16 v[100:101],v1 offset:10496
ds_read_b64_tr_b16 v[102:103],v1 offset:15616
v_mfma_f32_16x16x32_bf16 a[12:15],v[96:99],a[140:143],a[12:15]
s_waitcnt lgkmcnt(4)
ds_read_b64_tr_b16 v[96:97],v1 offset:256
v_mfma_f32_16x16x32_bf16 a[16:19],v[112:115],a[128:131],a[16:19]
ds_read_b64_tr_b16 v[98:99],v1 offset:5376
v_mfma_f32_16x16x32_bf16 a[16:19],v[116:119],a[144:147],a[16:19]
ds_read_b64_tr_b16 v[104:105],v1 offset:20736
v_mfma_f32_16x16x32_bf16 a[16:19],v[120:123],a[160:163],a[16:19]
ds_read_b64_tr_b16 v[106:107],v1 offset:25856
v_add_u32 v1,0xffff6000,v1
v_mfma_f32_16x16x32_bf16 a[16:19],v[124:127],a[176:179],a[16:19]
v_mfma_f32_16x16x32_bf16 a[20:23],v[124:127],a[180:183],a[20:23]
v_mfma_f32_16x16x32_bf16 a[20:23],v[120:123],a[164:167],a[20:23]
v_mfma_f32_16x16x32_bf16 a[20:23],v[116:119],a[148:151],a[20:23]
v_mfma_f32_16x16x32_bf16 a[20:23],v[112:115],a[132:135],a[20:23]
v_mfma_f32_16x16x32_bf16 a[24:27],v[112:115],a[136:139],a[24:27]
buffer_load_dwordx4 v[180:183],v6,s[28:31],s90 offen offset:1024 sc1
v_mfma_f32_16x16x32_bf16 a[24:27],v[116:119],a[152:155],a[24:27]
v_mfma_f32_16x16x32_bf16 a[24:27],v[120:123],a[168:171],a[24:27]
v_mfma_f32_16x16x32_bf16 a[24:27],v[124:127],a[184:187],a[24:27]
v_mfma_f32_16x16x32_bf16 a[28:31],v[124:127],a[188:191],a[28:31]
ds_read_b64_tr_b16 v[124:125],v2 offset:31008
ds_read_b64_tr_b16 v[126:127],v2 offset:36128
v_mfma_f32_16x16x32_bf16 a[28:31],v[120:123],a[172:175],a[28:31]
ds_read_b64_tr_b16 v[120:121],v2 offset:20768
v_mfma_f32_16x16x32_bf16 a[28:31],v[116:119],a[156:159],a[28:31]
ds_read_b64_tr_b16 v[116:117],v2 offset:10528
v_mfma_f32_16x16x32_bf16 a[28:31],v[112:115],a[140:143],a[28:31]
ds_read_b64_tr_b16 v[112:113],v2 offset:288
v_mfma_f32_16x16x32_bf16 v[128:131],v[64:67],a[128:131],v[128:131]
ds_read_b64_tr_b16 v[114:115],v2 offset:5408
ds_read_b64_tr_b16 v[118:119],v2 offset:15648
v_mfma_f32_16x16x32_bf16 v[128:131],v[68:71],a[144:147],v[128:131]
ds_read_b64_tr_b16 v[122:123],v2 offset:25888
v_add_u32 v2,0xffff6000,v2
v_mfma_f32_16x16x32_bf16 v[128:131],v[72:75],a[160:163],v[128:131]
v_mfma_f32_16x16x32_bf16 v[128:131],v[76:79],a[176:179],v[128:131]
buffer_load_dwordx4 v[164:167],v6,s[28:31],s89 offen offset:1024 sc1
v_mfma_f32_16x16x32_bf16 v[136:139],v[76:79],a[180:183],v[136:139]
v_mfma_f32_16x16x32_bf16 v[136:139],v[72:75],a[164:167],v[136:139]
v_mfma_f32_16x16x32_bf16 v[136:139],v[68:71],a[148:151],v[136:139]
v_mfma_f32_16x16x32_bf16 v[136:139],v[64:67],a[132:135],v[136:139]
v_mfma_f32_16x16x32_bf16 v[144:147],v[64:67],a[136:139],v[144:147]
v_mfma_f32_16x16x32_bf16 v[144:147],v[68:71],a[152:155],v[144:147]
v_mfma_f32_16x16x32_bf16 v[144:147],v[72:75],a[168:171],v[144:147]
v_mfma_f32_16x16x32_bf16 v[144:147],v[76:79],a[184:187],v[144:147]
v_mfma_f32_16x16x32_bf16 v[152:155],v[76:79],a[188:191],v[152:155]
v_mfma_f32_16x16x32_bf16 v[152:155],v[72:75],a[172:175],v[152:155]
v_mfma_f32_16x16x32_bf16 v[152:155],v[68:71],a[156:159],v[152:155]
buffer_load_dwordx4 v[148:151],v6,s[28:31],s88 offen offset:1024 sc1
v_mfma_f32_16x16x32_bf16 v[152:155],v[64:67],a[140:143],v[152:155]
s_barrier
v_mfma_f32_16x16x32_bf16 v[160:163],v[80:83],a[128:131],v[160:163]
ds_read_b64_tr_b16 v[64:65],v1
ds_read_b64_tr_b16 v[66:67],v1 offset:5120
v_mfma_f32_16x16x32_bf16 v[160:163],v[84:87],a[144:147],v[160:163]
ds_read_b64_tr_b16 v[68:69],v1 offset:10240
v_mfma_f32_16x16x32_bf16 v[160:163],v[88:91],a[160:163],v[160:163]
ds_read_b64_tr_b16 v[70:71],v1 offset:15360
v_mfma_f32_16x16x32_bf16 v[160:163],v[92:95],a[176:179],v[160:163]
buffer_load_dwordx4 v24,s[48:51],s83 offen lds
v_mfma_f32_16x16x32_bf16 v[168:171],v[92:95],a[180:183],v[168:171]
ds_read_b64_tr_b16 v[72:73],v1 offset:20480
ds_read_b64_tr_b16 v[74:75],v1 offset:25600
v_mfma_f32_16x16x32_bf16 v[168:171],v[88:91],a[164:167],v[168:171]
ds_read_b64_tr_b16 v[76:77],v1 offset:30720
v_mfma_f32_16x16x32_bf16 v[168:171],v[84:87],a[148:151],v[168:171]
ds_read_b64_tr_b16 v[78:79],v1 offset:35840
v_mfma_f32_16x16x32_bf16 v[168:171],v[80:83],a[132:135],v[168:171]
v_mfma_f32_16x16x32_bf16 v[176:179],v[80:83],a[136:139],v[176:179]
buffer_load_dwordx4 v[132:135],v6,s[28:31],s87 offen offset:1024 sc1
v_mfma_f32_16x16x32_bf16 v[176:179],v[84:87],a[152:155],v[176:179]
v_mfma_f32_16x16x32_bf16 v[176:179],v[88:91],a[168:171],v[176:179]
v_mfma_f32_16x16x32_bf16 v[176:179],v[92:95],a[184:187],v[176:179]
v_mfma_f32_16x16x32_bf16 v[184:187],v[92:95],a[188:191],v[184:187]
ds_read_b64_tr_b16 v[92:93],v2 offset:30752
ds_read_b64_tr_b16 v[94:95],v2 offset:35872
v_mfma_f32_16x16x32_bf16 v[184:187],v[88:91],a[172:175],v[184:187]
ds_read_b64_tr_b16 v[88:89],v2 offset:20512
v_mfma_f32_16x16x32_bf16 v[184:187],v[84:87],a[156:159],v[184:187]
buffer_load_dwordx4 v25,s[48:51],s83 offen offset:1024 lds
v_mfma_f32_16x16x32_bf16 v[184:187],v[80:83],a[140:143],v[184:187]
s_waitcnt lgkmcnt(3)
ds_read_b64_tr_b16 v[80:81],v2 offset:32
ds_read_b64_tr_b16 v[82:83],v2 offset:5152
v_mfma_f32_16x16x32_bf16 v[192:195],v[96:99],a[128:131],v[192:195]
ds_read_b64_tr_b16 v[84:85],v2 offset:10272
v_mfma_f32_16x16x32_bf16 v[192:195],v[100:103],a[144:147],v[192:195]
ds_read_b64_tr_b16 v[86:87],v2 offset:15392
v_mfma_f32_16x16x32_bf16 v[192:195],v[104:107],a[160:163],v[192:195]
ds_read_b64_tr_b16 v[90:91],v2 offset:25632
v_mfma_f32_16x16x32_bf16 v[192:195],v[108:111],a[176:179],v[192:195]
v_mfma_f32_16x16x32_bf16 v[200:203],v[108:111],a[180:183],v[200:203]
buffer_load_dwordx4 v26,s[48:51],s83 offen offset:2048 lds
v_mfma_f32_16x16x32_bf16 v[200:203],v[104:107],a[164:167],v[200:203]
v_mfma_f32_16x16x32_bf16 v[200:203],v[100:103],a[148:151],v[200:203]
v_mfma_f32_16x16x32_bf16 v[200:203],v[96:99],a[132:135],v[200:203]
v_mfma_f32_16x16x32_bf16 v[208:211],v[96:99],a[136:139],v[208:211]
v_mfma_f32_16x16x32_bf16 v[208:211],v[100:103],a[152:155],v[208:211]
buffer_load_dwordx4 v[196:199],v6,s[28:31],s87 offen offset:2048 sc1
v_mfma_f32_16x16x32_bf16 v[208:211],v[104:107],a[168:171],v[208:211]
v_mfma_f32_16x16x32_bf16 v[208:211],v[108:111],a[184:187],v[208:211]
v_mfma_f32_16x16x32_bf16 v[216:219],v[108:111],a[188:191],v[216:219]
ds_read_b64_tr_b16 v[108:109],v1 offset:30784
ds_read_b64_tr_b16 v[110:111],v1 offset:35904
v_mfma_f32_16x16x32_bf16 v[216:219],v[104:107],a[172:175],v[216:219]
ds_read_b64_tr_b16 v[104:105],v1 offset:20544
v_mfma_f32_16x16x32_bf16 v[216:219],v[100:103],a[156:159],v[216:219]
ds_read_b64_tr_b16 v[100:101],v1 offset:10304
v_mfma_f32_16x16x32_bf16 v[216:219],v[96:99],a[140:143],v[216:219]
buffer_load_dwordx4 v27,s[48:51],s83 offen offset:3072 lds
v_mfma_f32_16x16x32_bf16 v[224:227],v[112:115],a[128:131],v[224:227]
s_add_u32 m0,s86,126976
ds_read_b64_tr_b16 v[96:97],v1 offset:64
ds_read_b64_tr_b16 v[98:99],v1 offset:5184
v_mfma_f32_16x16x32_bf16 v[224:227],v[116:119],a[144:147],v[224:227]
ds_read_b64_tr_b16 v[102:103],v1 offset:15424
v_mfma_f32_16x16x32_bf16 v[224:227],v[120:123],a[160:163],v[224:227]
ds_read_b64_tr_b16 v[106:107],v1 offset:25664
v_mfma_f32_16x16x32_bf16 v[224:227],v[124:127],a[176:179],v[224:227]
v_mfma_f32_16x16x32_bf16 v[232:235],v[124:127],a[180:183],v[232:235]
buffer_load_dwordx4 v[212:215],v6,s[28:31],s88 offen offset:2048 sc1
v_mfma_f32_16x16x32_bf16 v[232:235],v[120:123],a[164:167],v[232:235]
v_mfma_f32_16x16x32_bf16 v[232:235],v[116:119],a[148:151],v[232:235]
v_mfma_f32_16x16x32_bf16 v[232:235],v[112:115],a[132:135],v[232:235]
v_mfma_f32_16x16x32_bf16 v[240:243],v[112:115],a[136:139],v[240:243]
v_mfma_f32_16x16x32_bf16 v[240:243],v[116:119],a[152:155],v[240:243]
v_mfma_f32_16x16x32_bf16 v[240:243],v[120:123],a[168:171],v[240:243]
buffer_load_dwordx4 v7,s[48:51],s83 offen lds
v_mfma_f32_16x16x32_bf16 v[240:243],v[124:127],a[184:187],v[240:243]
s_add_u32 m0,s86,128000
s_mul_i32 s83,s63,20480
v_mfma_f32_16x16x32_bf16 v[248:251],v[124:127],a[188:191],v[248:251]
ds_read_b64_tr_b16 v[124:125],v2 offset:30816
ds_read_b64_tr_b16 v[126:127],v2 offset:35936
v_mfma_f32_16x16x32_bf16 v[248:251],v[120:123],a[172:175],v[248:251]
ds_read_b64_tr_b16 v[120:121],v2 offset:20576
v_mfma_f32_16x16x32_bf16 v[248:251],v[116:119],a[156:159],v[248:251]
ds_read_b64_tr_b16 v[116:117],v2 offset:10336
v_mfma_f32_16x16x32_bf16 v[248:251],v[112:115],a[140:143],v[248:251]
buffer_load_dwordx4 v[228:231],v6,s[28:31],s89 offen offset:2048 sc1
v_mfma_f32_16x16x32_bf16 a[192:195],v[64:67],a[68:71],a[192:195]
s_waitcnt vmcnt(17)
ds_read_b64_tr_b16 v[112:113],v2 offset:96
ds_read_b64_tr_b16 v[114:115],v2 offset:5216
v_mfma_f32_16x16x32_bf16 a[192:195],v[68:71],a[76:79],a[192:195]
ds_read_b64_tr_b16 v[118:119],v2 offset:15456
v_mfma_f32_16x16x32_bf16 a[192:195],v[72:75],a[84:87],a[192:195]
ds_read_b64_tr_b16 v[122:123],v2 offset:25696
v_mfma_f32_16x16x32_bf16 a[192:195],v[76:79],a[92:95],a[192:195]
v_mfma_f32_16x16x32_bf16 a[196:199],v[76:79],a[124:127],a[196:199]
v_mfma_f32_16x16x32_bf16 a[196:199],v[72:75],a[116:119],a[196:199]
buffer_load_dwordx4 v24,s[48:51],s84 offen lds
v_mfma_f32_16x16x32_bf16 a[196:199],v[68:71],a[108:111],a[196:199]
v_mfma_f32_16x16x32_bf16 a[196:199],v[64:67],a[100:103],a[196:199]
v_mfma_f32_16x16x32_bf16 a[200:203],v[64:67],a[64:67],a[200:203]
v_mfma_f32_16x16x32_bf16 a[200:203],v[68:71],a[72:75],a[200:203]
s_waitcnt vmcnt(16)
v_mfma_f32_16x16x32_bf16 a[200:203],v[72:75],a[80:83],a[200:203]
buffer_load_dwordx4 v[244:247],v6,s[28:31],s90 offen offset:2048 sc1
v_mfma_f32_16x16x32_bf16 a[200:203],v[76:79],a[88:91],a[200:203]
s_waitcnt vmcnt(16)
v_mfma_f32_16x16x32_bf16 a[204:207],v[76:79],a[120:123],a[204:207]
ds_read_b64_tr_b16 v[76:77],v1 offset:30848
ds_read_b64_tr_b16 v[78:79],v1 offset:35968
v_mfma_f32_16x16x32_bf16 a[204:207],v[72:75],a[112:115],a[204:207]
ds_read_b64_tr_b16 v[72:73],v1 offset:20608
v_mfma_f32_16x16x32_bf16 a[204:207],v[68:71],a[104:107],a[204:207]
ds_read_b64_tr_b16 v[68:69],v1 offset:10368
v_mfma_f32_16x16x32_bf16 a[204:207],v[64:67],a[96:99],a[204:207]
s_waitcnt lgkmcnt(4)
ds_read_b64_tr_b16 v[64:65],v1 offset:128
v_mfma_f32_16x16x32_bf16 a[208:211],v[80:83],a[68:71],a[208:211]
buffer_load_dwordx4 v25,s[48:51],s84 offen offset:1024 lds
v_mfma_f32_16x16x32_bf16 a[208:211],v[84:87],a[76:79],a[208:211]
ds_read_b64_tr_b16 v[66:67],v1 offset:5248
ds_read_b64_tr_b16 v[70:71],v1 offset:15488
v_mfma_f32_16x16x32_bf16 a[208:211],v[88:91],a[84:87],a[208:211]
ds_read_b64_tr_b16 v[74:75],v1 offset:25728
v_mfma_f32_16x16x32_bf16 a[208:211],v[92:95],a[92:95],a[208:211]
v_mfma_f32_16x16x32_bf16 a[212:215],v[92:95],a[124:127],a[212:215]
v_mfma_f32_16x16x32_bf16 a[212:215],v[88:91],a[116:119],a[212:215]
buffer_load_dwordx4 v[188:191],v28,s[28:31],s90 offen offset:3072 sc1
v_mfma_f32_16x16x32_bf16 a[212:215],v[84:87],a[108:111],a[212:215]
v_mfma_f32_16x16x32_bf16 a[212:215],v[80:83],a[100:103],a[212:215]
v_mfma_f32_16x16x32_bf16 a[216:219],v[80:83],a[64:67],a[216:219]
v_mfma_f32_16x16x32_bf16 a[216:219],v[84:87],a[72:75],a[216:219]
v_mfma_f32_16x16x32_bf16 a[216:219],v[88:91],a[80:83],a[216:219]
v_mfma_f32_16x16x32_bf16 a[216:219],v[92:95],a[88:91],a[216:219]
buffer_load_dwordx4 v26,s[48:51],s84 offen offset:2048 lds
v_mfma_f32_16x16x32_bf16 a[220:223],v[92:95],a[120:123],a[220:223]
ds_read_b64_tr_b16 v[92:93],v2 offset:30880
ds_read_b64_tr_b16 v[94:95],v2 offset:36000
v_mfma_f32_16x16x32_bf16 a[220:223],v[88:91],a[112:115],a[220:223]
ds_read_b64_tr_b16 v[88:89],v2 offset:20640
v_mfma_f32_16x16x32_bf16 a[220:223],v[84:87],a[104:107],a[220:223]
)A"
R"A(ds_read_b64_tr_b16 v[84:85],v2 offset:10400
v_mfma_f32_16x16x32_bf16 a[220:223],v[80:83],a[96:99],a[220:223]
ds_read_b64_tr_b16 v[80:81],v2 offset:160
v_mfma_f32_16x16x32_bf16 a[224:227],v[96:99],a[68:71],a[224:227]
ds_read_b64_tr_b16 v[82:83],v2 offset:5280
ds_read_b64_tr_b16 v[86:87],v2 offset:15520
v_mfma_f32_16x16x32_bf16 a[224:227],v[100:103],a[76:79],a[224:227]
buffer_load_dwordx4 v27,s[48:51],s84 offen offset:3072 lds
v_mfma_f32_16x16x32_bf16 a[224:227],v[104:107],a[84:87],a[224:227]
s_add_u32 m0,s86,132096
ds_read_b64_tr_b16 v[90:91],v2 offset:25760
v_mfma_f32_16x16x32_bf16 a[224:227],v[108:111],a[92:95],a[224:227]
v_mfma_f32_16x16x32_bf16 a[228:231],v[108:111],a[124:127],a[228:231]
v_mfma_f32_16x16x32_bf16 a[228:231],v[104:107],a[116:119],a[228:231]
v_mfma_f32_16x16x32_bf16 a[228:231],v[100:103],a[108:111],a[228:231]
buffer_load_dwordx4 v[172:175],v28,s[28:31],s89 offen offset:3072 sc1
v_mfma_f32_16x16x32_bf16 a[228:231],v[96:99],a[100:103],a[228:231]
v_mfma_f32_16x16x32_bf16 a[232:235],v[96:99],a[64:67],a[232:235]
v_mfma_f32_16x16x32_bf16 a[232:235],v[100:103],a[72:75],a[232:235]
v_mfma_f32_16x16x32_bf16 a[232:235],v[104:107],a[80:83],a[232:235]
v_mfma_f32_16x16x32_bf16 a[232:235],v[108:111],a[88:91],a[232:235]
v_mfma_f32_16x16x32_bf16 a[236:239],v[108:111],a[120:123],a[236:239]
buffer_load_dwordx4 v7,s[48:51],s84 offen lds
v_mfma_f32_16x16x32_bf16 a[236:239],v[104:107],a[112:115],a[236:239]
ds_read_b64_tr_b16 v[104:105],v1 offset:20672
ds_read_b64_tr_b16 v[106:107],v1 offset:25792
v_mfma_f32_16x16x32_bf16 a[236:239],v[100:103],a[104:107],a[236:239]
ds_read_b64_tr_b16 v[100:101],v1 offset:10432
v_mfma_f32_16x16x32_bf16 a[236:239],v[96:99],a[96:99],a[236:239]
ds_read_b64_tr_b16 v[96:97],v1 offset:192
v_mfma_f32_16x16x32_bf16 a[240:243],v[112:115],a[68:71],a[240:243]
ds_read_b64_tr_b16 v[98:99],v1 offset:5312
v_mfma_f32_16x16x32_bf16 a[240:243],v[116:119],a[76:79],a[240:243]
buffer_load_dwordx4 v[156:159],v28,s[28:31],s88 offen offset:3072 sc1
v_mfma_f32_16x16x32_bf16 a[240:243],v[120:123],a[84:87],a[240:243]
ds_read_b64_tr_b16 v[102:103],v1 offset:15552
ds_read_b64_tr_b16 v[108:109],v1 offset:30912
v_mfma_f32_16x16x32_bf16 a[240:243],v[124:127],a[92:95],a[240:243]
ds_read_b64_tr_b16 v[110:111],v1 offset:36032
v_add_u32 v1,0xffff6000,v1
v_mfma_f32_16x16x32_bf16 a[244:247],v[124:127],a[124:127],a[244:247]
v_mfma_f32_16x16x32_bf16 a[244:247],v[120:123],a[116:119],a[244:247]
v_mfma_f32_16x16x32_bf16 a[244:247],v[116:119],a[108:111],a[244:247]
buffer_load_dwordx4 v[140:143],v28,s[28:31],s87 offen offset:3072 sc1
v_mfma_f32_16x16x32_bf16 a[244:247],v[112:115],a[100:103],a[244:247]
s_add_u32 s87,s64,3
s_and_b32 s87,s87,3
s_lshl_b32 s87,s87,1
s_or_b32 s87,s87,1
s_lshl_b32 s87,s87,16
s_add_u32 s88,s87,4096
s_add_u32 s89,s87,8192
s_add_u32 s90,s87,12288
v_mfma_f32_16x16x32_bf16 a[248:251],v[112:115],a[64:67],a[248:251]
v_mfma_f32_16x16x32_bf16 a[248:251],v[116:119],a[72:75],a[248:251]
v_mfma_f32_16x16x32_bf16 a[248:251],v[120:123],a[80:83],a[248:251]
v_mfma_f32_16x16x32_bf16 a[248:251],v[124:127],a[88:91],a[248:251]
buffer_load_dwordx4 a[128:131],v6,s[28:31],s87 offen sc1
v_mfma_f32_16x16x32_bf16 a[252:255],v[124:127],a[120:123],a[252:255]
ds_read_b64_tr_b16 v[124:125],v2 offset:30944
ds_read_b64_tr_b16 v[126:127],v2 offset:36064
v_mfma_f32_16x16x32_bf16 a[252:255],v[120:123],a[112:115],a[252:255]
ds_read_b64_tr_b16 v[120:121],v2 offset:20704
v_mfma_f32_16x16x32_bf16 a[252:255],v[116:119],a[104:107],a[252:255]
ds_read_b64_tr_b16 v[116:117],v2 offset:10464
v_mfma_f32_16x16x32_bf16 a[252:255],v[112:115],a[96:99],a[252:255]
s_waitcnt lgkmcnt(4)
ds_read_b64_tr_b16 v[112:113],v2 offset:224
v_mfma_f32_16x16x32_bf16 a[0:3],v[64:67],a[68:71],a[0:3]
buffer_load_dwordx4 a[144:147],v6,s[28:31],s88 offen sc1
v_mfma_f32_16x16x32_bf16 a[0:3],v[68:71],a[76:79],a[0:3]
ds_read_b64_tr_b16 v[114:115],v2 offset:5344
ds_read_b64_tr_b16 v[118:119],v2 offset:15584
v_mfma_f32_16x16x32_bf16 a[0:3],v[72:75],a[84:87],a[0:3]
ds_read_b64_tr_b16 v[122:123],v2 offset:25824
v_add_u32 v2,0xffff6000,v2
v_mfma_f32_16x16x32_bf16 a[0:3],v[76:79],a[92:95],a[0:3]
v_mfma_f32_16x16x32_bf16 a[4:7],v[76:79],a[124:127],a[4:7]
v_mfma_f32_16x16x32_bf16 a[4:7],v[72:75],a[116:119],a[4:7]
buffer_load_dwordx4 a[160:163],v6,s[28:31],s89 offen sc1
v_mfma_f32_16x16x32_bf16 a[4:7],v[68:71],a[108:111],a[4:7]
v_mfma_f32_16x16x32_bf16 a[4:7],v[64:67],a[100:103],a[4:7]
v_mfma_f32_16x16x32_bf16 a[8:11],v[64:67],a[64:67],a[8:11]
v_mfma_f32_16x16x32_bf16 a[8:11],v[68:71],a[72:75],a[8:11]
v_mfma_f32_16x16x32_bf16 a[8:11],v[72:75],a[80:83],a[8:11]
buffer_load_dwordx4 a[176:179],v6,s[28:31],s90 offen sc1
v_mfma_f32_16x16x32_bf16 a[8:11],v[76:79],a[88:91],a[8:11]
v_mfma_f32_16x16x32_bf16 a[12:15],v[76:79],a[120:123],a[12:15]
ds_read_b64_tr_b16 v[76:77],v1 offset:30720
ds_read_b64_tr_b16 v[78:79],v1 offset:35840
v_mfma_f32_16x16x32_bf16 a[12:15],v[72:75],a[112:115],a[12:15]
ds_read_b64_tr_b16 v[72:73],v1 offset:20480
v_mfma_f32_16x16x32_bf16 a[12:15],v[68:71],a[104:107],a[12:15]
ds_read_b64_tr_b16 v[68:69],v1 offset:10240
v_mfma_f32_16x16x32_bf16 a[12:15],v[64:67],a[96:99],a[12:15]
buffer_load_dwordx4 a[180:183],v6,s[28:31],s90 offen offset:1024 sc1
v_mfma_f32_16x16x32_bf16 a[16:19],v[80:83],a[68:71],a[16:19]
ds_read_b64_tr_b16 v[64:65],v1
ds_read_b64_tr_b16 v[66:67],v1 offset:5120
v_mfma_f32_16x16x32_bf16 a[16:19],v[84:87],a[76:79],a[16:19]
ds_read_b64_tr_b16 v[70:71],v1 offset:15360
v_mfma_f32_16x16x32_bf16 a[16:19],v[88:91],a[84:87],a[16:19]
ds_read_b64_tr_b16 v[74:75],v1 offset:25600
v_add_u32 v1,0x1e000,v1
v_mfma_f32_16x16x32_bf16 a[16:19],v[92:95],a[92:95],a[16:19]
v_mfma_f32_16x16x32_bf16 a[20:23],v[92:95],a[124:127],a[20:23]
buffer_load_dwordx4 a[164:167],v6,s[28:31],s89 offen offset:1024 sc1
v_mfma_f32_16x16x32_bf16 a[20:23],v[88:91],a[116:119],a[20:23]
v_mfma_f32_16x16x32_bf16 a[20:23],v[84:87],a[108:111],a[20:23]
v_mfma_f32_16x16x32_bf16 a[20:23],v[80:83],a[100:103],a[20:23]
v_mfma_f32_16x16x32_bf16 a[24:27],v[80:83],a[64:67],a[24:27]
v_mfma_f32_16x16x32_bf16 a[24:27],v[84:87],a[72:75],a[24:27]
buffer_load_dwordx4 a[148:151],v6,s[28:31],s88 offen offset:1024 sc1
v_mfma_f32_16x16x32_bf16 a[24:27],v[88:91],a[80:83],a[24:27]
v_mfma_f32_16x16x32_bf16 a[24:27],v[92:95],a[88:91],a[24:27]
v_mfma_f32_16x16x32_bf16 a[28:31],v[92:95],a[120:123],a[28:31]
ds_read_b64_tr_b16 v[92:93],v2 offset:30752
ds_read_b64_tr_b16 v[94:95],v2 offset:35872
v_mfma_f32_16x16x32_bf16 a[28:31],v[88:91],a[112:115],a[28:31]
ds_read_b64_tr_b16 v[88:89],v2 offset:20512
v_mfma_f32_16x16x32_bf16 a[28:31],v[84:87],a[104:107],a[28:31]
s_nop 0
L_pd17_%=:
s_cbranch_execz L_trtg2F_%=
buffer_load_dwordx4 a[132:135],v6,s[28:31],s87 offen offset:1024 sc1
v_mfma_f32_16x16x32_bf16 a[28:31],v[80:83],a[96:99],a[28:31]
ds_read_b64_tr_b16 v[80:81],v2 offset:32
ds_read_b64_tr_b16 v[82:83],v2 offset:5152
v_mfma_f32_16x16x32_bf16 v[128:131],v[96:99],a[68:71],v[128:131]
ds_read_b64_tr_b16 v[84:85],v2 offset:10272
v_mfma_f32_16x16x32_bf16 v[128:131],v[100:103],a[76:79],v[128:131]
ds_read_b64_tr_b16 v[86:87],v2 offset:15392
v_mfma_f32_16x16x32_bf16 v[128:131],v[104:107],a[84:87],v[128:131]
ds_read_b64_tr_b16 v[90:91],v2 offset:25632
v_add_u32 v2,0x1e000,v2
v_mfma_f32_16x16x32_bf16 v[128:131],v[108:111],a[92:95],v[128:131]
buffer_load_dwordx4 a[136:139],v6,s[28:31],s87 offen offset:2048 sc1
v_mfma_f32_16x16x32_bf16 v[136:139],v[108:111],a[124:127],v[136:139]
v_mfma_f32_16x16x32_bf16 v[136:139],v[104:107],a[116:119],v[136:139]
v_mfma_f32_16x16x32_bf16 v[136:139],v[100:103],a[108:111],v[136:139]
v_mfma_f32_16x16x32_bf16 v[136:139],v[96:99],a[100:103],v[136:139]
v_mfma_f32_16x16x32_bf16 v[144:147],v[96:99],a[64:67],v[144:147]
buffer_load_dwordx4 a[152:155],v6,s[28:31],s88 offen offset:2048 sc1
v_mfma_f32_16x16x32_bf16 v[144:147],v[100:103],a[72:75],v[144:147]
v_mfma_f32_16x16x32_bf16 v[144:147],v[104:107],a[80:83],v[144:147]
v_mfma_f32_16x16x32_bf16 v[144:147],v[108:111],a[88:91],v[144:147]
v_mfma_f32_16x16x32_bf16 v[152:155],v[108:111],a[120:123],v[152:155]
v_mfma_f32_16x16x32_bf16 v[152:155],v[104:107],a[112:115],v[152:155]
buffer_load_dwordx4 a[168:171],v6,s[28:31],s89 offen offset:2048 sc1
v_mfma_f32_16x16x32_bf16 v[152:155],v[100:103],a[104:107],v[152:155]
v_mfma_f32_16x16x32_bf16 v[152:155],v[96:99],a[96:99],v[152:155]
s_waitcnt vmcnt(13)
s_barrier
s_waitcnt lgkmcnt(0)
v_mfma_f32_16x16x32_bf16 v[160:163],v[112:115],a[68:71],v[160:163]
ds_read_b64_tr_b16 v[96:97],v1
ds_read_b64_tr_b16 v[98:99],v1 offset:5120
v_mfma_f32_16x16x32_bf16 v[160:163],v[116:119],a[76:79],v[160:163]
buffer_load_dwordx4 a[184:187],v6,s[28:31],s90 offen offset:2048 sc1
v_mfma_f32_16x16x32_bf16 v[160:163],v[120:123],a[84:87],v[160:163]
ds_read_b64_tr_b16 v[100:101],v1 offset:10240
ds_read_b64_tr_b16 v[102:103],v1 offset:15360
v_mfma_f32_16x16x32_bf16 v[160:163],v[124:127],a[92:95],v[160:163]
ds_read_b64_tr_b16 v[104:105],v1 offset:20480
v_mfma_f32_16x16x32_bf16 v[168:171],v[124:127],a[124:127],v[168:171]
ds_read_b64_tr_b16 v[106:107],v1 offset:25600
v_mfma_f32_16x16x32_bf16 v[168:171],v[120:123],a[116:119],v[168:171]
ds_read_b64_tr_b16 v[108:109],v1 offset:30720
v_mfma_f32_16x16x32_bf16 v[168:171],v[116:119],a[108:111],v[168:171]
buffer_load_dwordx4 a[188:191],v28,s[28:31],s90 offen offset:3072 sc1
v_mfma_f32_16x16x32_bf16 v[168:171],v[112:115],a[100:103],v[168:171]
ds_read_b64_tr_b16 v[110:111],v1 offset:35840
v_add_u32 v1,0xfffe2000,v1
v_mfma_f32_16x16x32_bf16 v[176:179],v[112:115],a[64:67],v[176:179]
v_mfma_f32_16x16x32_bf16 v[176:179],v[116:119],a[72:75],v[176:179]
v_mfma_f32_16x16x32_bf16 v[176:179],v[120:123],a[80:83],v[176:179]
v_mfma_f32_16x16x32_bf16 v[176:179],v[124:127],a[88:91],v[176:179]
buffer_load_dwordx4 a[172:175],v28,s[28:31],s89 offen offset:3072 sc1
v_mfma_f32_16x16x32_bf16 v[184:187],v[124:127],a[120:123],v[184:187]
ds_read_b64_tr_b16 v[124:125],v2 offset:30752
ds_read_b64_tr_b16 v[126:127],v2 offset:35872
v_mfma_f32_16x16x32_bf16 v[184:187],v[120:123],a[112:115],v[184:187]
ds_read_b64_tr_b16 v[120:121],v2 offset:20512
v_mfma_f32_16x16x32_bf16 v[184:187],v[116:119],a[104:107],v[184:187]
ds_read_b64_tr_b16 v[116:117],v2 offset:10272
v_mfma_f32_16x16x32_bf16 v[184:187],v[112:115],a[96:99],v[184:187]
ds_read_b64_tr_b16 v[112:113],v2 offset:32
v_mfma_f32_16x16x32_bf16 a[192:195],v[64:67],v[32:35],a[192:195]
buffer_load_dwordx4 a[156:159],v28,s[28:31],s88 offen offset:3072 sc1
v_mfma_f32_16x16x32_bf16 a[192:195],v[68:71],v[36:39],a[192:195]
ds_read_b64_tr_b16 v[114:115],v2 offset:5152
ds_read_b64_tr_b16 v[118:119],v2 offset:15392
v_mfma_f32_16x16x32_bf16 a[192:195],v[72:75],v[40:43],a[192:195]
ds_read_b64_tr_b16 v[122:123],v2 offset:25632
v_add_u32 v2,0xfffe2000,v2
v_mfma_f32_16x16x32_bf16 a[192:195],v[76:79],v[44:47],a[192:195]
v_mfma_f32_16x16x32_bf16 a[196:199],v[76:79],v[180:183],a[196:199]
v_mfma_f32_16x16x32_bf16 a[196:199],v[72:75],v[164:167],a[196:199]
buffer_load_dwordx4 a[140:143],v28,s[28:31],s87 offen offset:3072 sc1
v_mfma_f32_16x16x32_bf16 a[196:199],v[68:71],v[148:151],a[196:199]
s_mul_i32 s87,s60,81920
s_add_u32 s87,s87,s66
s_add_u32 s83,s83,s87
v_mfma_f32_16x16x32_bf16 a[196:199],v[64:67],v[132:135],a[196:199]
s_lshl_b32 s66,s63,6
s_add_u32 s87,s87,s66
s_add_u32 s88,s87,20480
v_mfma_f32_16x16x32_bf16 a[200:203],v[64:67],v[196:199],a[200:203]
s_add_u32 s89,s87,40960
s_add_u32 s90,s87,61440
s_add_u32 s66,s77,4096
v_mfma_f32_16x16x32_bf16 a[200:203],v[68:71],v[212:215],a[200:203]
v_mfma_f32_16x16x32_bf16 a[200:203],v[72:75],v[228:231],a[200:203]
v_mfma_f32_16x16x32_bf16 a[200:203],v[76:79],v[244:247],a[200:203]
v_mfma_f32_16x16x32_bf16 a[204:207],v[76:79],v[188:191],a[204:207]
ds_read_b64_tr_b16 v[76:77],v1 offset:30784
ds_read_b64_tr_b16 v[78:79],v1 offset:35904
v_mfma_f32_16x16x32_bf16 a[204:207],v[72:75],v[172:175],a[204:207]
s_waitcnt vmcnt(17)
ds_read_b64_tr_b16 v[72:73],v1 offset:20544
v_mfma_f32_16x16x32_bf16 a[204:207],v[68:71],v[156:159],a[204:207]
s_waitcnt vmcnt(16)
ds_read_b64_tr_b16 v[68:69],v1 offset:10304
v_mfma_f32_16x16x32_bf16 a[204:207],v[64:67],v[140:143],a[204:207]
)A"
R"A(ds_read_b64_tr_b16 v[64:65],v1 offset:64
v_mfma_f32_16x16x32_bf16 a[208:211],v[80:83],v[32:35],a[208:211]
ds_read_b64_tr_b16 v[66:67],v1 offset:5184
ds_read_b64_tr_b16 v[70:71],v1 offset:15424
v_mfma_f32_16x16x32_bf16 a[208:211],v[84:87],v[36:39],a[208:211]
ds_read_b64_tr_b16 v[74:75],v1 offset:25664
v_add_u32 v1,0x1e000,v1
v_mfma_f32_16x16x32_bf16 a[208:211],v[88:91],v[40:43],a[208:211]
v_mfma_f32_16x16x32_bf16 a[208:211],v[92:95],v[44:47],a[208:211]
v_mfma_f32_16x16x32_bf16 a[212:215],v[92:95],v[180:183],a[212:215]
v_mfma_f32_16x16x32_bf16 a[212:215],v[88:91],v[164:167],a[212:215]
v_mfma_f32_16x16x32_bf16 a[212:215],v[84:87],v[148:151],a[212:215]
v_mfma_f32_16x16x32_bf16 a[212:215],v[80:83],v[132:135],a[212:215]
v_mfma_f32_16x16x32_bf16 a[216:219],v[80:83],v[196:199],a[216:219]
v_mfma_f32_16x16x32_bf16 a[216:219],v[84:87],v[212:215],a[216:219]
v_mfma_f32_16x16x32_bf16 a[216:219],v[88:91],v[228:231],a[216:219]
v_mfma_f32_16x16x32_bf16 a[216:219],v[92:95],v[244:247],a[216:219]
v_mfma_f32_16x16x32_bf16 a[220:223],v[92:95],v[188:191],a[220:223]
ds_read_b64_tr_b16 v[92:93],v2 offset:30816
ds_read_b64_tr_b16 v[94:95],v2 offset:35936
v_mfma_f32_16x16x32_bf16 a[220:223],v[88:91],v[172:175],a[220:223]
ds_read_b64_tr_b16 v[88:89],v2 offset:20576
v_mfma_f32_16x16x32_bf16 a[220:223],v[84:87],v[156:159],a[220:223]
ds_read_b64_tr_b16 v[84:85],v2 offset:10336
v_mfma_f32_16x16x32_bf16 a[220:223],v[80:83],v[140:143],a[220:223]
s_waitcnt vmcnt(15) lgkmcnt(4)
ds_read_b64_tr_b16 v[80:81],v2 offset:96
v_mfma_f32_16x16x32_bf16 a[192:195],v[96:99],a[128:131],a[192:195]
s_waitcnt vmcnt(14)
ds_read_b64_tr_b16 v[82:83],v2 offset:5216
ds_read_b64_tr_b16 v[86:87],v2 offset:15456
v_mfma_f32_16x16x32_bf16 a[192:195],v[100:103],a[144:147],a[192:195]
s_waitcnt vmcnt(13)
ds_read_b64_tr_b16 v[90:91],v2 offset:25696
v_add_u32 v2,0x1e000,v2
v_mfma_f32_16x16x32_bf16 a[192:195],v[104:107],a[160:163],a[192:195]
s_waitcnt vmcnt(12)
v_mfma_f32_16x16x32_bf16 a[192:195],v[108:111],a[176:179],a[192:195]
s_waitcnt vmcnt(11)
v_mfma_f32_16x16x32_bf16 a[196:199],v[108:111],a[180:183],a[196:199]
s_waitcnt vmcnt(10)
v_mfma_f32_16x16x32_bf16 a[196:199],v[104:107],a[164:167],a[196:199]
s_waitcnt vmcnt(9)
v_mfma_f32_16x16x32_bf16 a[196:199],v[100:103],a[148:151],a[196:199]
s_waitcnt vmcnt(8)
v_mfma_f32_16x16x32_bf16 a[196:199],v[96:99],a[132:135],a[196:199]
s_waitcnt vmcnt(7)
v_mfma_f32_16x16x32_bf16 a[200:203],v[96:99],a[136:139],a[200:203]
s_waitcnt vmcnt(6)
v_mfma_f32_16x16x32_bf16 a[200:203],v[100:103],a[152:155],a[200:203]
s_waitcnt vmcnt(5)
v_mfma_f32_16x16x32_bf16 a[200:203],v[104:107],a[168:171],a[200:203]
s_waitcnt vmcnt(4)
v_mfma_f32_16x16x32_bf16 a[200:203],v[108:111],a[184:187],a[200:203]
s_waitcnt vmcnt(3)
v_accvgpr_read_b32 v16,a192
v_accvgpr_read_b32 v17,a193
v_mfma_f32_16x16x32_bf16 a[204:207],v[108:111],a[188:191],a[204:207]
s_waitcnt vmcnt(2)
v_accvgpr_read_b32 v18,a194
v_accvgpr_read_b32 v19,a195
v_mfma_f32_16x16x32_bf16 a[204:207],v[104:107],a[172:175],a[204:207]
s_waitcnt vmcnt(1)
v_cvt_pk_bf16_f32 v16,v16,v17
v_cvt_pk_bf16_f32 v17,v18,v19
v_mfma_f32_16x16x32_bf16 a[204:207],v[100:103],a[156:159],a[204:207]
s_waitcnt vmcnt(0)
v_accvgpr_read_b32 v8,a196
v_accvgpr_read_b32 v9,a197
v_mfma_f32_16x16x32_bf16 a[204:207],v[96:99],a[140:143],a[204:207]
v_accvgpr_read_b32 v10,a198
v_accvgpr_read_b32 v11,a199
v_mfma_f32_16x16x32_bf16 a[208:211],v[112:115],a[128:131],a[208:211]
v_cvt_pk_bf16_f32 v8,v8,v9
v_cvt_pk_bf16_f32 v9,v10,v11
v_mfma_f32_16x16x32_bf16 a[208:211],v[116:119],a[144:147],a[208:211]
ds_read_b64_tr_b16 v[96:97],v1 offset:64
ds_read_b64_tr_b16 v[98:99],v1 offset:5184
v_mfma_f32_16x16x32_bf16 a[208:211],v[120:123],a[160:163],a[208:211]
ds_read_b64_tr_b16 v[100:101],v1 offset:10304
v_mfma_f32_16x16x32_bf16 a[208:211],v[124:127],a[176:179],a[208:211]
ds_read_b64_tr_b16 v[102:103],v1 offset:15424
v_mfma_f32_16x16x32_bf16 a[212:215],v[124:127],a[180:183],a[212:215]
ds_read_b64_tr_b16 v[104:105],v1 offset:20544
v_mfma_f32_16x16x32_bf16 a[212:215],v[120:123],a[164:167],a[212:215]
ds_read_b64_tr_b16 v[106:107],v1 offset:25664
ds_read_b64_tr_b16 v[108:109],v1 offset:30784
v_mfma_f32_16x16x32_bf16 a[212:215],v[116:119],a[148:151],a[212:215]
ds_read_b64_tr_b16 v[110:111],v1 offset:35904
v_add_u32 v1,0xfffe2000,v1
v_mfma_f32_16x16x32_bf16 a[212:215],v[112:115],a[132:135],a[212:215]
v_accvgpr_read_b32 v20,a208
v_accvgpr_read_b32 v21,a209
v_mfma_f32_16x16x32_bf16 a[216:219],v[112:115],a[136:139],a[216:219]
v_accvgpr_read_b32 v22,a210
v_accvgpr_read_b32 v23,a211
v_mfma_f32_16x16x32_bf16 a[216:219],v[116:119],a[152:155],a[216:219]
v_cvt_pk_bf16_f32 v18,v20,v21
v_cvt_pk_bf16_f32 v19,v22,v23
v_mfma_f32_16x16x32_bf16 a[216:219],v[120:123],a[168:171],a[216:219]
buffer_store_dwordx4 v[16:19],v6,s[36:39],s74 offen sc1
v_mfma_f32_16x16x32_bf16 a[216:219],v[124:127],a[184:187],a[216:219]
v_accvgpr_read_b32 v12,a212
v_accvgpr_read_b32 v13,a213
v_mfma_f32_16x16x32_bf16 a[220:223],v[124:127],a[188:191],a[220:223]
v_accvgpr_read_b32 v14,a214
v_accvgpr_read_b32 v15,a215
v_mfma_f32_16x16x32_bf16 a[220:223],v[120:123],a[172:175],a[220:223]
v_cvt_pk_bf16_f32 v10,v12,v13
v_cvt_pk_bf16_f32 v11,v14,v15
v_mfma_f32_16x16x32_bf16 a[220:223],v[116:119],a[156:159],a[220:223]
v_accvgpr_read_b32 v16,a200
v_accvgpr_read_b32 v17,a201
v_mfma_f32_16x16x32_bf16 a[220:223],v[112:115],a[140:143],a[220:223]
v_accvgpr_read_b32 v18,a202
v_accvgpr_read_b32 v19,a203
v_mfma_f32_16x16x32_bf16 a[224:227],v[64:67],v[32:35],a[224:227]
v_accvgpr_read_b32 v20,a216
v_accvgpr_read_b32 v21,a217
v_mfma_f32_16x16x32_bf16 a[224:227],v[68:71],v[36:39],a[224:227]
v_accvgpr_read_b32 v22,a218
v_accvgpr_read_b32 v23,a219
v_mfma_f32_16x16x32_bf16 a[224:227],v[72:75],v[40:43],a[224:227]
v_cvt_pk_bf16_f32 v16,v16,v17
v_cvt_pk_bf16_f32 v17,v18,v19
v_mfma_f32_16x16x32_bf16 a[224:227],v[76:79],v[44:47],a[224:227]
v_cvt_pk_bf16_f32 v18,v20,v21
v_cvt_pk_bf16_f32 v19,v22,v23
v_mfma_f32_16x16x32_bf16 a[228:231],v[76:79],v[180:183],a[228:231]
ds_read_b64_tr_b16 v[112:113],v2 offset:96
ds_read_b64_tr_b16 v[114:115],v2 offset:5216
v_mfma_f32_16x16x32_bf16 a[228:231],v[72:75],v[164:167],a[228:231]
buffer_store_dwordx4 v[8:11],v6,s[36:39],s74 offen offset:1024 sc1
v_mfma_f32_16x16x32_bf16 a[228:231],v[68:71],v[148:151],a[228:231]
ds_read_b64_tr_b16 v[116:117],v2 offset:10336
ds_read_b64_tr_b16 v[118:119],v2 offset:15456
v_mfma_f32_16x16x32_bf16 a[228:231],v[64:67],v[132:135],a[228:231]
ds_read_b64_tr_b16 v[120:121],v2 offset:20576
v_accvgpr_read_b32 v8,a204
v_mfma_f32_16x16x32_bf16 a[232:235],v[64:67],v[196:199],a[232:235]
ds_read_b64_tr_b16 v[122:123],v2 offset:25696
v_accvgpr_read_b32 v9,a205
v_mfma_f32_16x16x32_bf16 a[232:235],v[68:71],v[212:215],a[232:235]
ds_read_b64_tr_b16 v[124:125],v2 offset:30816
v_accvgpr_read_b32 v10,a206
v_mfma_f32_16x16x32_bf16 a[232:235],v[72:75],v[228:231],a[232:235]
ds_read_b64_tr_b16 v[126:127],v2 offset:35936
v_accvgpr_read_b32 v11,a207
v_mfma_f32_16x16x32_bf16 a[232:235],v[76:79],v[244:247],a[232:235]
v_accvgpr_read_b32 v12,a220
v_accvgpr_read_b32 v13,a221
v_mfma_f32_16x16x32_bf16 a[236:239],v[76:79],v[188:191],a[236:239]
v_accvgpr_read_b32 v14,a222
v_accvgpr_read_b32 v15,a223
v_mfma_f32_16x16x32_bf16 a[236:239],v[72:75],v[172:175],a[236:239]
v_cvt_pk_bf16_f32 v8,v8,v9
v_cvt_pk_bf16_f32 v9,v10,v11
v_mfma_f32_16x16x32_bf16 a[236:239],v[68:71],v[156:159],a[236:239]
v_cvt_pk_bf16_f32 v10,v12,v13
v_cvt_pk_bf16_f32 v11,v14,v15
v_mfma_f32_16x16x32_bf16 a[236:239],v[64:67],v[140:143],a[236:239]
s_waitcnt lgkmcnt(3)
ds_read_b64_tr_b16 v[64:65],v1 offset:128
ds_read_b64_tr_b16 v[66:67],v1 offset:5248
v_mfma_f32_16x16x32_bf16 a[240:243],v[80:83],v[32:35],a[240:243]
buffer_store_dwordx4 v[16:19],v6,s[36:39],s74 offen offset:2048 sc1
v_mfma_f32_16x16x32_bf16 a[240:243],v[84:87],v[36:39],a[240:243]
ds_read_b64_tr_b16 v[68:69],v1 offset:10368
ds_read_b64_tr_b16 v[70:71],v1 offset:15488
v_mfma_f32_16x16x32_bf16 a[240:243],v[88:91],v[40:43],a[240:243]
ds_read_b64_tr_b16 v[72:73],v1 offset:20608
v_add_u32 v2,0xfffe2000,v2
v_mfma_f32_16x16x32_bf16 a[240:243],v[92:95],v[44:47],a[240:243]
ds_read_b64_tr_b16 v[74:75],v1 offset:25728
v_mfma_f32_16x16x32_bf16 a[244:247],v[92:95],v[180:183],a[244:247]
ds_read_b64_tr_b16 v[76:77],v1 offset:30848
v_mfma_f32_16x16x32_bf16 a[244:247],v[88:91],v[164:167],a[244:247]
ds_read_b64_tr_b16 v[78:79],v1 offset:35968
v_add_u32 v1,0x1e000,v1
v_mfma_f32_16x16x32_bf16 a[244:247],v[84:87],v[148:151],a[244:247]
v_mfma_f32_16x16x32_bf16 a[244:247],v[80:83],v[132:135],a[244:247]
v_mfma_f32_16x16x32_bf16 a[248:251],v[80:83],v[196:199],a[248:251]
v_mfma_f32_16x16x32_bf16 a[248:251],v[84:87],v[212:215],a[248:251]
v_mfma_f32_16x16x32_bf16 a[248:251],v[88:91],v[228:231],a[248:251]
v_mfma_f32_16x16x32_bf16 a[248:251],v[92:95],v[244:247],a[248:251]
buffer_store_dwordx4 v[8:11],v28,s[36:39],s74 offen offset:3072 sc1
v_mfma_f32_16x16x32_bf16 a[252:255],v[92:95],v[188:191],a[252:255]
ds_read_b64_tr_b16 v[92:93],v2 offset:30880
ds_read_b64_tr_b16 v[94:95],v2 offset:36000
v_mfma_f32_16x16x32_bf16 a[252:255],v[88:91],v[172:175],a[252:255]
ds_read_b64_tr_b16 v[88:89],v2 offset:20640
v_mfma_f32_16x16x32_bf16 a[252:255],v[84:87],v[156:159],a[252:255]
ds_read_b64_tr_b16 v[84:85],v2 offset:10400
v_mfma_f32_16x16x32_bf16 a[252:255],v[80:83],v[140:143],a[252:255]
ds_read_b64_tr_b16 v[80:81],v2 offset:160
v_mfma_f32_16x16x32_bf16 a[224:227],v[96:99],a[128:131],a[224:227]
ds_read_b64_tr_b16 v[82:83],v2 offset:5280
ds_read_b64_tr_b16 v[86:87],v2 offset:15520
v_mfma_f32_16x16x32_bf16 a[224:227],v[100:103],a[144:147],a[224:227]
ds_read_b64_tr_b16 v[90:91],v2 offset:25760
v_add_u32 v2,0x1e000,v2
v_mfma_f32_16x16x32_bf16 a[224:227],v[104:107],a[160:163],a[224:227]
v_mfma_f32_16x16x32_bf16 a[224:227],v[108:111],a[176:179],a[224:227]
v_mfma_f32_16x16x32_bf16 a[228:231],v[108:111],a[180:183],a[228:231]
v_mfma_f32_16x16x32_bf16 a[228:231],v[104:107],a[164:167],a[228:231]
v_mfma_f32_16x16x32_bf16 a[228:231],v[100:103],a[148:151],a[228:231]
v_mfma_f32_16x16x32_bf16 a[228:231],v[96:99],a[132:135],a[228:231]
v_mfma_f32_16x16x32_bf16 a[232:235],v[96:99],a[136:139],a[232:235]
v_mfma_f32_16x16x32_bf16 a[232:235],v[100:103],a[152:155],a[232:235]
v_mfma_f32_16x16x32_bf16 a[232:235],v[104:107],a[168:171],a[232:235]
v_mfma_f32_16x16x32_bf16 a[232:235],v[108:111],a[184:187],a[232:235]
v_accvgpr_read_b32 v16,a224
v_accvgpr_read_b32 v17,a225
v_mfma_f32_16x16x32_bf16 a[236:239],v[108:111],a[188:191],a[236:239]
v_accvgpr_read_b32 v18,a226
v_accvgpr_read_b32 v19,a227
v_mfma_f32_16x16x32_bf16 a[236:239],v[104:107],a[172:175],a[236:239]
v_cvt_pk_bf16_f32 v16,v16,v17
v_cvt_pk_bf16_f32 v17,v18,v19
v_mfma_f32_16x16x32_bf16 a[236:239],v[100:103],a[156:159],a[236:239]
v_accvgpr_read_b32 v8,a228
v_accvgpr_read_b32 v9,a229
v_mfma_f32_16x16x32_bf16 a[236:239],v[96:99],a[140:143],a[236:239]
v_accvgpr_read_b32 v10,a230
v_accvgpr_read_b32 v11,a231
v_mfma_f32_16x16x32_bf16 a[240:243],v[112:115],a[128:131],a[240:243]
v_cvt_pk_bf16_f32 v8,v8,v9
v_cvt_pk_bf16_f32 v9,v10,v11
v_mfma_f32_16x16x32_bf16 a[240:243],v[116:119],a[144:147],a[240:243]
s_waitcnt lgkmcnt(0)
ds_read_b64_tr_b16 v[96:97],v1 offset:128
ds_read_b64_tr_b16 v[98:99],v1 offset:5248
v_mfma_f32_16x16x32_bf16 a[240:243],v[120:123],a[160:163],a[240:243]
ds_read_b64_tr_b16 v[100:101],v1 offset:10368
v_mfma_f32_16x16x32_bf16 a[240:243],v[124:127],a[176:179],a[240:243]
ds_read_b64_tr_b16 v[102:103],v1 offset:15488
v_mfma_f32_16x16x32_bf16 a[244:247],v[124:127],a[180:183],a[244:247]
ds_read_b64_tr_b16 v[104:105],v1 offset:20608
v_mfma_f32_16x16x32_bf16 a[244:247],v[120:123],a[164:167],a[244:247]
ds_read_b64_tr_b16 v[106:107],v1 offset:25728
ds_read_b64_tr_b16 v[108:109],v1 offset:30848
v_mfma_f32_16x16x32_bf16 a[244:247],v[116:119],a[148:151],a[244:247]
ds_read_b64_tr_b16 v[110:111],v1 offset:35968
v_add_u32 v1,0xfffe2000,v1
v_mfma_f32_16x16x32_bf16 a[244:247],v[112:115],a[132:135],a[244:247]
v_accvgpr_read_b32 v20,a240
v_accvgpr_read_b32 v21,a241
v_mfma_f32_16x16x32_bf16 a[248:251],v[112:115],a[136:139],a[248:251]
v_accvgpr_read_b32 v22,a242
v_accvgpr_read_b32 v23,a243
)A"
R"A(v_mfma_f32_16x16x32_bf16 a[248:251],v[116:119],a[152:155],a[248:251]
v_cvt_pk_bf16_f32 v18,v20,v21
v_cvt_pk_bf16_f32 v19,v22,v23
v_mfma_f32_16x16x32_bf16 a[248:251],v[120:123],a[168:171],a[248:251]
buffer_store_dwordx4 v[16:19],v6,s[36:39],s75 offen sc1
v_mfma_f32_16x16x32_bf16 a[248:251],v[124:127],a[184:187],a[248:251]
v_accvgpr_read_b32 v12,a244
v_accvgpr_read_b32 v13,a245
v_mfma_f32_16x16x32_bf16 a[252:255],v[124:127],a[188:191],a[252:255]
v_accvgpr_read_b32 v14,a246
v_accvgpr_read_b32 v15,a247
v_mfma_f32_16x16x32_bf16 a[252:255],v[120:123],a[172:175],a[252:255]
v_cvt_pk_bf16_f32 v10,v12,v13
v_cvt_pk_bf16_f32 v11,v14,v15
v_mfma_f32_16x16x32_bf16 a[252:255],v[116:119],a[156:159],a[252:255]
v_accvgpr_read_b32 v16,a232
v_accvgpr_read_b32 v17,a233
v_mfma_f32_16x16x32_bf16 a[252:255],v[112:115],a[140:143],a[252:255]
v_accvgpr_read_b32 v18,a234
v_accvgpr_read_b32 v19,a235
v_mfma_f32_16x16x32_bf16 a[0:3],v[64:67],v[32:35],a[0:3]
v_accvgpr_read_b32 v20,a248
v_accvgpr_read_b32 v21,a249
v_mfma_f32_16x16x32_bf16 a[0:3],v[68:71],v[36:39],a[0:3]
v_accvgpr_read_b32 v22,a250
v_accvgpr_read_b32 v23,a251
v_mfma_f32_16x16x32_bf16 a[0:3],v[72:75],v[40:43],a[0:3]
v_cvt_pk_bf16_f32 v16,v16,v17
v_cvt_pk_bf16_f32 v17,v18,v19
v_mfma_f32_16x16x32_bf16 a[0:3],v[76:79],v[44:47],a[0:3]
v_cvt_pk_bf16_f32 v18,v20,v21
v_cvt_pk_bf16_f32 v19,v22,v23
v_mfma_f32_16x16x32_bf16 a[4:7],v[76:79],v[180:183],a[4:7]
ds_read_b64_tr_b16 v[112:113],v2 offset:160
ds_read_b64_tr_b16 v[114:115],v2 offset:5280
v_mfma_f32_16x16x32_bf16 a[4:7],v[72:75],v[164:167],a[4:7]
buffer_store_dwordx4 v[8:11],v6,s[36:39],s75 offen offset:1024 sc1
v_mfma_f32_16x16x32_bf16 a[4:7],v[68:71],v[148:151],a[4:7]
ds_read_b64_tr_b16 v[116:117],v2 offset:10400
ds_read_b64_tr_b16 v[118:119],v2 offset:15520
v_mfma_f32_16x16x32_bf16 a[4:7],v[64:67],v[132:135],a[4:7]
ds_read_b64_tr_b16 v[120:121],v2 offset:20640
v_accvgpr_read_b32 v8,a236
v_mfma_f32_16x16x32_bf16 a[8:11],v[64:67],v[196:199],a[8:11]
ds_read_b64_tr_b16 v[122:123],v2 offset:25760
v_accvgpr_read_b32 v9,a237
v_mfma_f32_16x16x32_bf16 a[8:11],v[68:71],v[212:215],a[8:11]
ds_read_b64_tr_b16 v[124:125],v2 offset:30880
v_accvgpr_read_b32 v10,a238
v_mfma_f32_16x16x32_bf16 a[8:11],v[72:75],v[228:231],a[8:11]
ds_read_b64_tr_b16 v[126:127],v2 offset:36000
v_accvgpr_read_b32 v11,a239
v_mfma_f32_16x16x32_bf16 a[8:11],v[76:79],v[244:247],a[8:11]
v_accvgpr_read_b32 v12,a252
v_accvgpr_read_b32 v13,a253
v_mfma_f32_16x16x32_bf16 a[12:15],v[76:79],v[188:191],a[12:15]
v_accvgpr_read_b32 v14,a254
v_accvgpr_read_b32 v15,a255
v_mfma_f32_16x16x32_bf16 a[12:15],v[72:75],v[172:175],a[12:15]
v_cvt_pk_bf16_f32 v8,v8,v9
v_cvt_pk_bf16_f32 v9,v10,v11
v_mfma_f32_16x16x32_bf16 a[12:15],v[68:71],v[156:159],a[12:15]
v_cvt_pk_bf16_f32 v10,v12,v13
v_cvt_pk_bf16_f32 v11,v14,v15
v_mfma_f32_16x16x32_bf16 a[12:15],v[64:67],v[140:143],a[12:15]
ds_read_b64_tr_b16 v[64:65],v1 offset:192
ds_read_b64_tr_b16 v[66:67],v1 offset:5312
v_mfma_f32_16x16x32_bf16 a[16:19],v[80:83],v[32:35],a[16:19]
buffer_store_dwordx4 v[16:19],v6,s[36:39],s75 offen offset:2048 sc1
v_mfma_f32_16x16x32_bf16 a[16:19],v[84:87],v[36:39],a[16:19]
ds_read_b64_tr_b16 v[68:69],v1 offset:10432
ds_read_b64_tr_b16 v[70:71],v1 offset:15552
v_mfma_f32_16x16x32_bf16 a[16:19],v[88:91],v[40:43],a[16:19]
ds_read_b64_tr_b16 v[72:73],v1 offset:20672
v_add_u32 v2,0xfffe2000,v2
v_mfma_f32_16x16x32_bf16 a[16:19],v[92:95],v[44:47],a[16:19]
ds_read_b64_tr_b16 v[74:75],v1 offset:25792
v_mfma_f32_16x16x32_bf16 a[20:23],v[92:95],v[180:183],a[20:23]
ds_read_b64_tr_b16 v[76:77],v1 offset:30912
v_mfma_f32_16x16x32_bf16 a[20:23],v[88:91],v[164:167],a[20:23]
ds_read_b64_tr_b16 v[78:79],v1 offset:36032
v_add_u32 v1,0x1e000,v1
v_mfma_f32_16x16x32_bf16 a[20:23],v[84:87],v[148:151],a[20:23]
v_mfma_f32_16x16x32_bf16 a[20:23],v[80:83],v[132:135],a[20:23]
v_mfma_f32_16x16x32_bf16 a[24:27],v[80:83],v[196:199],a[24:27]
v_mfma_f32_16x16x32_bf16 a[24:27],v[84:87],v[212:215],a[24:27]
v_mfma_f32_16x16x32_bf16 a[24:27],v[88:91],v[228:231],a[24:27]
v_mfma_f32_16x16x32_bf16 a[24:27],v[92:95],v[244:247],a[24:27]
buffer_store_dwordx4 v[8:11],v28,s[36:39],s75 offen offset:3072 sc1
v_mfma_f32_16x16x32_bf16 a[28:31],v[92:95],v[188:191],a[28:31]
ds_read_b64_tr_b16 v[92:93],v2 offset:30944
ds_read_b64_tr_b16 v[94:95],v2 offset:36064
v_mfma_f32_16x16x32_bf16 a[28:31],v[88:91],v[172:175],a[28:31]
ds_read_b64_tr_b16 v[88:89],v2 offset:20704
v_mfma_f32_16x16x32_bf16 a[28:31],v[84:87],v[156:159],a[28:31]
ds_read_b64_tr_b16 v[84:85],v2 offset:10464
v_mfma_f32_16x16x32_bf16 a[28:31],v[80:83],v[140:143],a[28:31]
s_waitcnt lgkmcnt(4)
ds_read_b64_tr_b16 v[80:81],v2 offset:224
v_mfma_f32_16x16x32_bf16 a[0:3],v[96:99],a[128:131],a[0:3]
ds_read_b64_tr_b16 v[82:83],v2 offset:5344
ds_read_b64_tr_b16 v[86:87],v2 offset:15584
v_mfma_f32_16x16x32_bf16 a[0:3],v[100:103],a[144:147],a[0:3]
ds_read_b64_tr_b16 v[90:91],v2 offset:25824
v_add_u32 v2,0x1e000,v2
v_mfma_f32_16x16x32_bf16 a[0:3],v[104:107],a[160:163],a[0:3]
v_mfma_f32_16x16x32_bf16 a[0:3],v[108:111],a[176:179],a[0:3]
v_mfma_f32_16x16x32_bf16 a[4:7],v[108:111],a[180:183],a[4:7]
v_mfma_f32_16x16x32_bf16 a[4:7],v[104:107],a[164:167],a[4:7]
v_mfma_f32_16x16x32_bf16 a[4:7],v[100:103],a[148:151],a[4:7]
v_mfma_f32_16x16x32_bf16 a[4:7],v[96:99],a[132:135],a[4:7]
v_mfma_f32_16x16x32_bf16 a[8:11],v[96:99],a[136:139],a[8:11]
v_mfma_f32_16x16x32_bf16 a[8:11],v[100:103],a[152:155],a[8:11]
v_mfma_f32_16x16x32_bf16 a[8:11],v[104:107],a[168:171],a[8:11]
v_mfma_f32_16x16x32_bf16 a[8:11],v[108:111],a[184:187],a[8:11]
v_accvgpr_read_b32 v16,a0
v_accvgpr_read_b32 v17,a1
v_mfma_f32_16x16x32_bf16 a[12:15],v[108:111],a[188:191],a[12:15]
v_accvgpr_read_b32 v18,a2
v_accvgpr_read_b32 v19,a3
v_mfma_f32_16x16x32_bf16 a[12:15],v[104:107],a[172:175],a[12:15]
v_cvt_pk_bf16_f32 v16,v16,v17
v_cvt_pk_bf16_f32 v17,v18,v19
v_mfma_f32_16x16x32_bf16 a[12:15],v[100:103],a[156:159],a[12:15]
v_accvgpr_read_b32 v8,a4
v_accvgpr_read_b32 v9,a5
v_mfma_f32_16x16x32_bf16 a[12:15],v[96:99],a[140:143],a[12:15]
v_accvgpr_read_b32 v10,a6
v_accvgpr_read_b32 v11,a7
v_mfma_f32_16x16x32_bf16 a[16:19],v[112:115],a[128:131],a[16:19]
v_cvt_pk_bf16_f32 v8,v8,v9
v_cvt_pk_bf16_f32 v9,v10,v11
v_mfma_f32_16x16x32_bf16 a[16:19],v[116:119],a[144:147],a[16:19]
ds_read_b64_tr_b16 v[96:97],v1 offset:192
ds_read_b64_tr_b16 v[98:99],v1 offset:5312
v_mfma_f32_16x16x32_bf16 a[16:19],v[120:123],a[160:163],a[16:19]
ds_read_b64_tr_b16 v[100:101],v1 offset:10432
v_mfma_f32_16x16x32_bf16 a[16:19],v[124:127],a[176:179],a[16:19]
ds_read_b64_tr_b16 v[102:103],v1 offset:15552
v_mfma_f32_16x16x32_bf16 a[20:23],v[124:127],a[180:183],a[20:23]
ds_read_b64_tr_b16 v[104:105],v1 offset:20672
v_mfma_f32_16x16x32_bf16 a[20:23],v[120:123],a[164:167],a[20:23]
ds_read_b64_tr_b16 v[106:107],v1 offset:25792
ds_read_b64_tr_b16 v[108:109],v1 offset:30912
v_mfma_f32_16x16x32_bf16 a[20:23],v[116:119],a[148:151],a[20:23]
ds_read_b64_tr_b16 v[110:111],v1 offset:36032
v_add_u32 v1,0xfffec000,v1
v_mfma_f32_16x16x32_bf16 a[20:23],v[112:115],a[132:135],a[20:23]
v_accvgpr_read_b32 v20,a16
v_accvgpr_read_b32 v21,a17
v_mfma_f32_16x16x32_bf16 a[24:27],v[112:115],a[136:139],a[24:27]
v_accvgpr_read_b32 v22,a18
v_accvgpr_read_b32 v23,a19
v_mfma_f32_16x16x32_bf16 a[24:27],v[116:119],a[152:155],a[24:27]
v_cvt_pk_bf16_f32 v18,v20,v21
v_cvt_pk_bf16_f32 v19,v22,v23
v_mfma_f32_16x16x32_bf16 a[24:27],v[120:123],a[168:171],a[24:27]
buffer_store_dwordx4 v[16:19],v6,s[36:39],s76 offen sc1
v_mfma_f32_16x16x32_bf16 a[24:27],v[124:127],a[184:187],a[24:27]
v_accvgpr_read_b32 v12,a20
v_accvgpr_read_b32 v13,a21
v_mfma_f32_16x16x32_bf16 a[28:31],v[124:127],a[188:191],a[28:31]
v_accvgpr_read_b32 v14,a22
v_accvgpr_read_b32 v15,a23
v_mfma_f32_16x16x32_bf16 a[28:31],v[120:123],a[172:175],a[28:31]
v_cvt_pk_bf16_f32 v10,v12,v13
v_cvt_pk_bf16_f32 v11,v14,v15
v_mfma_f32_16x16x32_bf16 a[28:31],v[116:119],a[156:159],a[28:31]
v_accvgpr_read_b32 v16,a8
v_accvgpr_read_b32 v17,a9
v_mfma_f32_16x16x32_bf16 a[28:31],v[112:115],a[140:143],a[28:31]
v_accvgpr_read_b32 v18,a10
v_accvgpr_read_b32 v19,a11
v_mfma_f32_16x16x32_bf16 v[128:131],v[64:67],v[32:35],v[128:131]
v_accvgpr_read_b32 v20,a24
s_nop 0
L_pd18_%=:
s_cbranch_execz L_trtg2F_%=
v_accvgpr_read_b32 v21,a25
v_mfma_f32_16x16x32_bf16 v[128:131],v[68:71],v[36:39],v[128:131]
v_accvgpr_read_b32 v22,a26
v_accvgpr_read_b32 v23,a27
v_mfma_f32_16x16x32_bf16 v[128:131],v[72:75],v[40:43],v[128:131]
v_cvt_pk_bf16_f32 v16,v16,v17
v_cvt_pk_bf16_f32 v17,v18,v19
v_mfma_f32_16x16x32_bf16 v[128:131],v[76:79],v[44:47],v[128:131]
v_cvt_pk_bf16_f32 v18,v20,v21
v_cvt_pk_bf16_f32 v19,v22,v23
v_mfma_f32_16x16x32_bf16 v[136:139],v[76:79],v[180:183],v[136:139]
ds_read_b64_tr_b16 v[112:113],v2 offset:224
ds_read_b64_tr_b16 v[114:115],v2 offset:5344
v_mfma_f32_16x16x32_bf16 v[136:139],v[72:75],v[164:167],v[136:139]
buffer_store_dwordx4 v[8:11],v6,s[36:39],s76 offen offset:1024 sc1
v_mfma_f32_16x16x32_bf16 v[136:139],v[68:71],v[148:151],v[136:139]
ds_read_b64_tr_b16 v[116:117],v2 offset:10464
ds_read_b64_tr_b16 v[118:119],v2 offset:15584
v_mfma_f32_16x16x32_bf16 v[136:139],v[64:67],v[132:135],v[136:139]
ds_read_b64_tr_b16 v[120:121],v2 offset:20704
v_accvgpr_read_b32 v8,a12
v_mfma_f32_16x16x32_bf16 v[144:147],v[64:67],v[196:199],v[144:147]
ds_read_b64_tr_b16 v[122:123],v2 offset:25824
v_accvgpr_read_b32 v9,a13
v_mfma_f32_16x16x32_bf16 v[144:147],v[68:71],v[212:215],v[144:147]
ds_read_b64_tr_b16 v[124:125],v2 offset:30944
v_accvgpr_read_b32 v10,a14
v_mfma_f32_16x16x32_bf16 v[144:147],v[72:75],v[228:231],v[144:147]
ds_read_b64_tr_b16 v[126:127],v2 offset:36064
v_accvgpr_read_b32 v11,a15
v_mfma_f32_16x16x32_bf16 v[144:147],v[76:79],v[244:247],v[144:147]
v_accvgpr_read_b32 v12,a28
v_accvgpr_read_b32 v13,a29
v_mfma_f32_16x16x32_bf16 v[152:155],v[76:79],v[188:191],v[152:155]
v_accvgpr_read_b32 v14,a30
v_accvgpr_read_b32 v15,a31
v_mfma_f32_16x16x32_bf16 v[152:155],v[72:75],v[172:175],v[152:155]
v_cvt_pk_bf16_f32 v8,v8,v9
v_cvt_pk_bf16_f32 v9,v10,v11
v_mfma_f32_16x16x32_bf16 v[152:155],v[68:71],v[156:159],v[152:155]
v_cvt_pk_bf16_f32 v10,v12,v13
v_cvt_pk_bf16_f32 v11,v14,v15
v_mfma_f32_16x16x32_bf16 v[152:155],v[64:67],v[140:143],v[152:155]
s_waitcnt lgkmcnt(3)
ds_read_b64_tr_b16 v[64:65],v1 offset:256
ds_read_b64_tr_b16 v[66:67],v1 offset:5376
v_mfma_f32_16x16x32_bf16 v[160:163],v[80:83],v[32:35],v[160:163]
buffer_store_dwordx4 v[16:19],v6,s[36:39],s76 offen offset:2048 sc1
v_mfma_f32_16x16x32_bf16 v[160:163],v[84:87],v[36:39],v[160:163]
ds_read_b64_tr_b16 v[68:69],v1 offset:10496
ds_read_b64_tr_b16 v[70:71],v1 offset:15616
v_mfma_f32_16x16x32_bf16 v[160:163],v[88:91],v[40:43],v[160:163]
ds_read_b64_tr_b16 v[72:73],v1 offset:20736
v_add_u32 v2,0xfffec000,v2
v_mfma_f32_16x16x32_bf16 v[160:163],v[92:95],v[44:47],v[160:163]
ds_read_b64_tr_b16 v[74:75],v1 offset:25856
v_mov_b32 v20,s52
v_mfma_f32_16x16x32_bf16 v[168:171],v[92:95],v[180:183],v[168:171]
ds_read_b64_tr_b16 v[76:77],v1 offset:30976
v_mov_b32 v21,s53
v_mfma_f32_16x16x32_bf16 v[168:171],v[88:91],v[164:167],v[168:171]
ds_read_b64_tr_b16 v[78:79],v1 offset:36096
v_add_u32 v1,0xffff6000,v1
v_mfma_f32_16x16x32_bf16 v[168:171],v[84:87],v[148:151],v[168:171]
v_lshlrev_b32 v22,20,v6
v_mfma_f32_16x16x32_bf16 v[168:171],v[80:83],v[132:135],v[168:171]
v_mfma_f32_16x16x32_bf16 v[176:179],v[80:83],v[196:199],v[176:179]
v_mfma_f32_16x16x32_bf16 v[176:179],v[84:87],v[212:215],v[176:179]
v_mfma_f32_16x16x32_bf16 v[176:179],v[88:91],v[228:231],v[176:179]
v_mfma_f32_16x16x32_bf16 v[176:179],v[92:95],v[244:247],v[176:179]
buffer_store_dwordx4 v[8:11],v28,s[36:39],s76 offen offset:3072 sc1
v_mfma_f32_16x16x32_bf16 v[184:187],v[92:95],v[188:191],v[184:187]
ds_read_b64_tr_b16 v[92:93],v2 offset:31008
ds_read_b64_tr_b16 v[94:95],v2 offset:36128
v_mfma_f32_16x16x32_bf16 v[184:187],v[88:91],v[172:175],v[184:187]
ds_read_b64_tr_b16 v[88:89],v2 offset:20768
v_mfma_f32_16x16x32_bf16 v[184:187],v[84:87],v[156:159],v[184:187]
ds_read_b64_tr_b16 v[84:85],v2 offset:10528
v_mfma_f32_16x16x32_bf16 v[184:187],v[80:83],v[140:143],v[184:187]
ds_read_b64_tr_b16 v[80:81],v2 offset:288
)A"
R"A(v_mfma_f32_16x16x32_bf16 v[128:131],v[96:99],a[128:131],v[128:131]
ds_read_b64_tr_b16 v[82:83],v2 offset:5408
ds_read_b64_tr_b16 v[86:87],v2 offset:15648
v_mfma_f32_16x16x32_bf16 v[128:131],v[100:103],a[144:147],v[128:131]
ds_read_b64_tr_b16 v[90:91],v2 offset:25888
v_add_u32 v2,0xffff6000,v2
v_mfma_f32_16x16x32_bf16 v[128:131],v[104:107],a[160:163],v[128:131]
v_mfma_f32_16x16x32_bf16 v[128:131],v[108:111],a[176:179],v[128:131]
v_mfma_f32_16x16x32_bf16 v[136:139],v[108:111],a[180:183],v[136:139]
v_mfma_f32_16x16x32_bf16 v[136:139],v[104:107],a[164:167],v[136:139]
v_mfma_f32_16x16x32_bf16 v[136:139],v[100:103],a[148:151],v[136:139]
v_mfma_f32_16x16x32_bf16 v[136:139],v[96:99],a[132:135],v[136:139]
v_mfma_f32_16x16x32_bf16 v[144:147],v[96:99],a[136:139],v[144:147]
v_mfma_f32_16x16x32_bf16 v[144:147],v[100:103],a[152:155],v[144:147]
v_mfma_f32_16x16x32_bf16 v[144:147],v[104:107],a[168:171],v[144:147]
v_mfma_f32_16x16x32_bf16 v[144:147],v[108:111],a[184:187],v[144:147]
v_cvt_pk_bf16_f32 v128,v128,v129
v_cvt_pk_bf16_f32 v129,v130,v131
v_mfma_f32_16x16x32_bf16 v[152:155],v[108:111],a[188:191],v[152:155]
ds_read_b64_tr_b16 v[108:109],v1 offset:30976
ds_read_b64_tr_b16 v[110:111],v1 offset:36096
v_mfma_f32_16x16x32_bf16 v[152:155],v[104:107],a[172:175],v[152:155]
v_cvt_pk_bf16_f32 v136,v136,v137
v_cvt_pk_bf16_f32 v137,v138,v139
v_mfma_f32_16x16x32_bf16 v[152:155],v[100:103],a[156:159],v[152:155]
v_cvt_pk_bf16_f32 v144,v144,v145
v_cvt_pk_bf16_f32 v145,v146,v147
v_mfma_f32_16x16x32_bf16 v[152:155],v[96:99],a[140:143],v[152:155]
ds_read_b64_tr_b16 v[96:97],v1 offset:256
ds_read_b64_tr_b16 v[98:99],v1 offset:5376
v_mfma_f32_16x16x32_bf16 v[160:163],v[112:115],a[128:131],v[160:163]
ds_read_b64_tr_b16 v[100:101],v1 offset:10496
v_mfma_f32_16x16x32_bf16 v[160:163],v[116:119],a[144:147],v[160:163]
s_waitcnt lgkmcnt(5)
ds_read_b64_tr_b16 v[102:103],v1 offset:15616
v_mfma_f32_16x16x32_bf16 v[160:163],v[120:123],a[160:163],v[160:163]
ds_read_b64_tr_b16 v[104:105],v1 offset:20736
v_mfma_f32_16x16x32_bf16 v[160:163],v[124:127],a[176:179],v[160:163]
v_cvt_pk_bf16_f32 v152,v152,v153
v_cvt_pk_bf16_f32 v153,v154,v155
v_mfma_f32_16x16x32_bf16 v[168:171],v[124:127],a[180:183],v[168:171]
ds_read_b64_tr_b16 v[106:107],v1 offset:25856
v_add_u32 v1,0x1e000,v1
v_mfma_f32_16x16x32_bf16 v[168:171],v[120:123],a[164:167],v[168:171]
v_mfma_f32_16x16x32_bf16 v[168:171],v[116:119],a[148:151],v[168:171]
v_mfma_f32_16x16x32_bf16 v[168:171],v[112:115],a[132:135],v[168:171]
v_cvt_pk_bf16_f32 v130,v160,v161
v_cvt_pk_bf16_f32 v131,v162,v163
v_mfma_f32_16x16x32_bf16 v[176:179],v[112:115],a[136:139],v[176:179]
buffer_store_dwordx4 v[128:131],v6,s[36:39],s79 offen sc1
v_mfma_f32_16x16x32_bf16 v[176:179],v[116:119],a[152:155],v[176:179]
v_mfma_f32_16x16x32_bf16 v[176:179],v[120:123],a[168:171],v[176:179]
v_mfma_f32_16x16x32_bf16 v[176:179],v[124:127],a[184:187],v[176:179]
v_mfma_f32_16x16x32_bf16 v[184:187],v[124:127],a[188:191],v[184:187]
v_cvt_pk_bf16_f32 v138,v168,v169
v_cvt_pk_bf16_f32 v139,v170,v171
v_mfma_f32_16x16x32_bf16 v[184:187],v[120:123],a[172:175],v[184:187]
buffer_store_dwordx4 v[136:139],v6,s[36:39],s80 offen sc1
v_mfma_f32_16x16x32_bf16 v[184:187],v[116:119],a[156:159],v[184:187]
ds_read_b64_tr_b16 v[116:117],v2 offset:10528
ds_read_b64_tr_b16 v[118:119],v2 offset:15648
v_mfma_f32_16x16x32_bf16 v[184:187],v[112:115],a[140:143],v[184:187]
v_cvt_pk_bf16_f32 v146,v176,v177
v_cvt_pk_bf16_f32 v147,v178,v179
s_waitcnt vmcnt(6)
s_barrier
v_mfma_f32_16x16x32_bf16 v[192:195],v[64:67],a[68:71],v[192:195]
buffer_store_dwordx4 v[144:147],v6,s[36:39],s81 offen sc1
v_mfma_f32_16x16x32_bf16 v[192:195],v[68:71],a[76:79],v[192:195]
ds_read_b64_tr_b16 v[112:113],v2 offset:288
ds_read_b64_tr_b16 v[114:115],v2 offset:5408
v_mfma_f32_16x16x32_bf16 v[192:195],v[72:75],a[84:87],v[192:195]
buffer_store_dwordx2 v[58:59],v60,s[44:47],s18 offen offset:2312 sc1
v_mfma_f32_16x16x32_bf16 v[192:195],v[76:79],a[92:95],v[192:195]
v_cvt_pk_bf16_f32 v154,v184,v185
v_cvt_pk_bf16_f32 v155,v186,v187
v_mfma_f32_16x16x32_bf16 v[200:203],v[76:79],a[124:127],v[200:203]
buffer_store_dwordx4 v[152:155],v28,s[36:39],s82 offen sc1
v_mfma_f32_16x16x32_bf16 v[200:203],v[72:75],a[116:119],v[200:203]
ds_read_b64_tr_b16 v[120:121],v2 offset:20768
ds_read_b64_tr_b16 v[122:123],v2 offset:25888
v_mfma_f32_16x16x32_bf16 v[200:203],v[68:71],a[108:111],v[200:203]
ds_read_b64_tr_b16 v[124:125],v2 offset:31008
v_mfma_f32_16x16x32_bf16 v[200:203],v[64:67],a[100:103],v[200:203]
ds_read_b64_tr_b16 v[126:127],v2 offset:36128
v_add_u32 v2,0x1e000,v2
v_mfma_f32_16x16x32_bf16 v[208:211],v[64:67],a[64:67],v[208:211]
v_mfma_f32_16x16x32_bf16 v[208:211],v[68:71],a[72:75],v[208:211]
v_mfma_f32_16x16x32_bf16 v[208:211],v[72:75],a[80:83],v[208:211]
v_mfma_f32_16x16x32_bf16 v[208:211],v[76:79],a[88:91],v[208:211]
v_mfma_f32_16x16x32_bf16 v[216:219],v[76:79],a[120:123],v[216:219]
ds_read_b64_tr_b16 v[76:77],v1 offset:30976
ds_read_b64_tr_b16 v[78:79],v1 offset:36096
v_mfma_f32_16x16x32_bf16 v[216:219],v[72:75],a[112:115],v[216:219]
ds_read_b64_tr_b16 v[72:73],v1 offset:20736
v_mfma_f32_16x16x32_bf16 v[216:219],v[68:71],a[104:107],v[216:219]
ds_read_b64_tr_b16 v[68:69],v1 offset:10496
v_mfma_f32_16x16x32_bf16 v[216:219],v[64:67],a[96:99],v[216:219]
ds_read_b64_tr_b16 v[64:65],v1 offset:256
v_mfma_f32_16x16x32_bf16 v[224:227],v[80:83],a[68:71],v[224:227]
ds_read_b64_tr_b16 v[66:67],v1 offset:5376
ds_read_b64_tr_b16 v[70:71],v1 offset:15616
v_mfma_f32_16x16x32_bf16 v[224:227],v[84:87],a[76:79],v[224:227]
ds_read_b64_tr_b16 v[74:75],v1 offset:25856
v_mfma_f32_16x16x32_bf16 v[224:227],v[88:91],a[84:87],v[224:227]
v_mfma_f32_16x16x32_bf16 v[224:227],v[92:95],a[92:95],v[224:227]
v_mfma_f32_16x16x32_bf16 v[232:235],v[92:95],a[124:127],v[232:235]
v_mfma_f32_16x16x32_bf16 v[232:235],v[88:91],a[116:119],v[232:235]
v_mfma_f32_16x16x32_bf16 v[232:235],v[84:87],a[108:111],v[232:235]
v_mfma_f32_16x16x32_bf16 v[232:235],v[80:83],a[100:103],v[232:235]
v_mfma_f32_16x16x32_bf16 v[240:243],v[80:83],a[64:67],v[240:243]
v_mfma_f32_16x16x32_bf16 v[240:243],v[84:87],a[72:75],v[240:243]
v_mfma_f32_16x16x32_bf16 v[240:243],v[88:91],a[80:83],v[240:243]
v_mfma_f32_16x16x32_bf16 v[240:243],v[92:95],a[88:91],v[240:243]
v_mfma_f32_16x16x32_bf16 v[248:251],v[92:95],a[120:123],v[248:251]
ds_read_b64_tr_b16 v[92:93],v2 offset:31008
ds_read_b64_tr_b16 v[94:95],v2 offset:36128
v_mfma_f32_16x16x32_bf16 v[248:251],v[88:91],a[112:115],v[248:251]
ds_read_b64_tr_b16 v[88:89],v2 offset:20768
v_mfma_f32_16x16x32_bf16 v[248:251],v[84:87],a[104:107],v[248:251]
ds_read_b64_tr_b16 v[84:85],v2 offset:10528
v_mfma_f32_16x16x32_bf16 v[248:251],v[80:83],a[96:99],v[248:251]
s_waitcnt lgkmcnt(4)
ds_read_b64_tr_b16 v[80:81],v2 offset:288
v_mfma_f32_16x16x32_bf16 v[192:195],v[96:99],v[32:35],v[192:195]
ds_read_b64_tr_b16 v[82:83],v2 offset:5408
ds_read_b64_tr_b16 v[86:87],v2 offset:15648
v_mfma_f32_16x16x32_bf16 v[192:195],v[100:103],v[36:39],v[192:195]
ds_read_b64_tr_b16 v[90:91],v2 offset:25888
v_mfma_f32_16x16x32_bf16 v[192:195],v[104:107],v[40:43],v[192:195]
v_mfma_f32_16x16x32_bf16 v[192:195],v[108:111],v[44:47],v[192:195]
v_mfma_f32_16x16x32_bf16 v[200:203],v[108:111],v[180:183],v[200:203]
v_mfma_f32_16x16x32_bf16 v[200:203],v[104:107],v[164:167],v[200:203]
v_mfma_f32_16x16x32_bf16 v[200:203],v[100:103],v[148:151],v[200:203]
v_mfma_f32_16x16x32_bf16 v[200:203],v[96:99],v[132:135],v[200:203]
v_mfma_f32_16x16x32_bf16 v[208:211],v[96:99],v[196:199],v[208:211]
v_mfma_f32_16x16x32_bf16 v[208:211],v[100:103],v[212:215],v[208:211]
v_mfma_f32_16x16x32_bf16 v[208:211],v[104:107],v[228:231],v[208:211]
v_mfma_f32_16x16x32_bf16 v[208:211],v[108:111],v[244:247],v[208:211]
v_mfma_f32_16x16x32_bf16 v[216:219],v[108:111],v[188:191],v[216:219]
v_mfma_f32_16x16x32_bf16 v[216:219],v[104:107],v[172:175],v[216:219]
v_mfma_f32_16x16x32_bf16 v[216:219],v[100:103],v[156:159],v[216:219]
v_mfma_f32_16x16x32_bf16 v[216:219],v[96:99],v[140:143],v[216:219]
v_mfma_f32_16x16x32_bf16 v[224:227],v[112:115],v[32:35],v[224:227]
v_mfma_f32_16x16x32_bf16 v[224:227],v[116:119],v[36:39],v[224:227]
v_mfma_f32_16x16x32_bf16 v[224:227],v[120:123],v[40:43],v[224:227]
v_mfma_f32_16x16x32_bf16 v[224:227],v[124:127],v[44:47],v[224:227]
v_mfma_f32_16x16x32_bf16 v[232:235],v[124:127],v[180:183],v[232:235]
v_mfma_f32_16x16x32_bf16 v[232:235],v[120:123],v[164:167],v[232:235]
v_mfma_f32_16x16x32_bf16 v[232:235],v[116:119],v[148:151],v[232:235]
v_mfma_f32_16x16x32_bf16 v[232:235],v[112:115],v[132:135],v[232:235]
v_mfma_f32_16x16x32_bf16 v[240:243],v[112:115],v[196:199],v[240:243]
v_mfma_f32_16x16x32_bf16 v[240:243],v[116:119],v[212:215],v[240:243]
v_mfma_f32_16x16x32_bf16 v[240:243],v[120:123],v[228:231],v[240:243]
v_mfma_f32_16x16x32_bf16 v[240:243],v[124:127],v[244:247],v[240:243]
v_mfma_f32_16x16x32_bf16 v[248:251],v[124:127],v[188:191],v[248:251]
v_mfma_f32_16x16x32_bf16 v[248:251],v[120:123],v[172:175],v[248:251]
v_mfma_f32_16x16x32_bf16 v[248:251],v[116:119],v[156:159],v[248:251]
v_mfma_f32_16x16x32_bf16 v[248:251],v[112:115],v[140:143],v[248:251]
s_waitcnt vmcnt(0)
s_barrier
v_mfma_f32_16x16x32_bf16 v[192:195],v[64:67],a[128:131],v[192:195]
buffer_store_dwordx2 v[58:59],v60,s[44:47],s18 offen offset:2304 sc1
v_mfma_f32_16x16x32_bf16 v[192:195],v[68:71],a[144:147],v[192:195]
v_mfma_f32_16x16x32_bf16 v[192:195],v[72:75],a[160:163],v[192:195]
v_mfma_f32_16x16x32_bf16 v[192:195],v[76:79],a[176:179],v[192:195]
v_mfma_f32_16x16x32_bf16 v[200:203],v[76:79],a[180:183],v[200:203]
v_mfma_f32_16x16x32_bf16 v[200:203],v[72:75],a[164:167],v[200:203]
v_mfma_f32_16x16x32_bf16 v[200:203],v[68:71],a[148:151],v[200:203]
v_mfma_f32_16x16x32_bf16 v[200:203],v[64:67],a[132:135],v[200:203]
v_mfma_f32_16x16x32_bf16 v[208:211],v[64:67],a[136:139],v[208:211]
v_mfma_f32_16x16x32_bf16 v[208:211],v[68:71],a[152:155],v[208:211]
v_mfma_f32_16x16x32_bf16 v[208:211],v[72:75],a[168:171],v[208:211]
v_mfma_f32_16x16x32_bf16 v[208:211],v[76:79],a[184:187],v[208:211]
v_mfma_f32_16x16x32_bf16 v[216:219],v[76:79],a[188:191],v[216:219]
v_mfma_f32_16x16x32_bf16 v[216:219],v[72:75],a[172:175],v[216:219]
v_mfma_f32_16x16x32_bf16 v[216:219],v[68:71],a[156:159],v[216:219]
v_mfma_f32_16x16x32_bf16 v[216:219],v[64:67],a[140:143],v[216:219]
s_waitcnt lgkmcnt(0)
v_mfma_f32_16x16x32_bf16 v[224:227],v[80:83],a[128:131],v[224:227]
buffer_load_dwordx2 v[62:63],v61,s[44:47],s91 offen offset:2312 sc1
v_mfma_f32_16x16x32_bf16 v[224:227],v[84:87],a[144:147],v[224:227]
v_mfma_f32_16x16x32_bf16 v[224:227],v[88:91],a[160:163],v[224:227]
v_mfma_f32_16x16x32_bf16 v[224:227],v[92:95],a[176:179],v[224:227]
v_mfma_f32_16x16x32_bf16 v[232:235],v[92:95],a[180:183],v[232:235]
v_mfma_f32_16x16x32_bf16 v[232:235],v[88:91],a[164:167],v[232:235]
v_mfma_f32_16x16x32_bf16 v[232:235],v[84:87],a[148:151],v[232:235]
v_mfma_f32_16x16x32_bf16 v[232:235],v[80:83],a[132:135],v[232:235]
v_mfma_f32_16x16x32_bf16 v[240:243],v[80:83],a[136:139],v[240:243]
v_mfma_f32_16x16x32_bf16 v[240:243],v[84:87],a[152:155],v[240:243]
v_mfma_f32_16x16x32_bf16 v[240:243],v[88:91],a[168:171],v[240:243]
v_mfma_f32_16x16x32_bf16 v[240:243],v[92:95],a[184:187],v[240:243]
v_mfma_f32_16x16x32_bf16 v[248:251],v[92:95],a[188:191],v[248:251]
v_mfma_f32_16x16x32_bf16 v[248:251],v[88:91],a[172:175],v[248:251]
v_mfma_f32_16x16x32_bf16 v[248:251],v[84:87],a[156:159],v[248:251]
v_mfma_f32_16x16x32_bf16 v[248:251],v[80:83],a[140:143],v[248:251]
s_waitcnt vmcnt(0)
v_cmp_ne_u32 s[56:57],s52,v62
v_cmp_ne_u32 s[58:59],s53,v63
s_nop 0
s_or_b64 s[56:57],s[56:57],s[58:59]
s_cbranch_scc1 L_fr4_%=
L_fo4_%=:
buffer_load_dwordx4 v[64:67],v6,s[36:39],s66 offen sc1
buffer_load_dwordx4 v[68:71],v6,s[36:39],s67 offen sc1
buffer_load_dwordx4 v[72:75],v6,s[36:39],s66 offen offset:1024 sc1
buffer_load_dwordx4 v[76:79],v6,s[36:39],s67 offen offset:1024 sc1
buffer_load_dwordx4 v[80:83],v6,s[36:39],s66 offen offset:2048 sc1
buffer_load_dwordx4 v[84:87],v6,s[36:39],s67 offen offset:2048 sc1
buffer_load_dwordx4 v[88:91],v28,s[36:39],s66 offen offset:3072 sc1
buffer_load_dwordx4 v[92:95],v28,s[36:39],s67 offen offset:3072 sc1
)A"
R"A(buffer_load_dwordx2 v[4:5],v3,s[44:47],s91 offen offset:2304 sc1
s_waitcnt vmcnt(8)
v_mfma_f32_16x16x32_bf16 v[192:195],v[48:51],v[64:67],v[192:195]
s_waitcnt vmcnt(7)
v_mfma_f32_16x16x32_bf16 v[192:195],v[48:51],v[68:71],v[192:195]
v_mfma_f32_16x16x32_bf16 v[224:227],v[52:55],v[64:67],v[224:227]
v_mfma_f32_16x16x32_bf16 v[224:227],v[52:55],v[68:71],v[224:227]
s_waitcnt vmcnt(6)
v_mfma_f32_16x16x32_bf16 v[200:203],v[48:51],v[72:75],v[200:203]
s_waitcnt vmcnt(5)
v_mfma_f32_16x16x32_bf16 v[200:203],v[48:51],v[76:79],v[200:203]
v_mfma_f32_16x16x32_bf16 v[232:235],v[52:55],v[72:75],v[232:235]
v_mfma_f32_16x16x32_bf16 v[232:235],v[52:55],v[76:79],v[232:235]
s_waitcnt vmcnt(4)
v_mfma_f32_16x16x32_bf16 v[208:211],v[48:51],v[80:83],v[208:211]
s_waitcnt vmcnt(3)
v_mfma_f32_16x16x32_bf16 v[208:211],v[48:51],v[84:87],v[208:211]
v_mfma_f32_16x16x32_bf16 v[240:243],v[52:55],v[80:83],v[240:243]
v_mfma_f32_16x16x32_bf16 v[240:243],v[52:55],v[84:87],v[240:243]
s_waitcnt vmcnt(2)
v_mfma_f32_16x16x32_bf16 v[216:219],v[48:51],v[88:91],v[216:219]
s_waitcnt vmcnt(1)
v_mfma_f32_16x16x32_bf16 v[216:219],v[48:51],v[92:95],v[216:219]
v_mfma_f32_16x16x32_bf16 v[248:251],v[52:55],v[88:91],v[248:251]
v_mfma_f32_16x16x32_bf16 v[248:251],v[52:55],v[92:95],v[248:251]
s_waitcnt vmcnt(0)
v_cmp_ne_u32 s[56:57],s52,v4
v_cmp_ne_u32 s[58:59],s53,v5
s_nop 0
s_or_b64 s[56:57],s[56:57],s[58:59]
s_cbranch_scc1 L_fr5_%=
L_fo5_%=:
buffer_load_dwordx4 v[96:99],v6,s[36:39],s68 offen sc1
buffer_load_dwordx4 v[100:103],v6,s[36:39],s68 offen offset:1024 sc1
buffer_load_dwordx4 v[104:107],v6,s[36:39],s68 offen offset:2048 sc1
buffer_load_dwordx4 v[108:111],v6,s[36:39],s68 offen offset:3072 sc1
buffer_load_dwordx4 v[112:115],v6,s[36:39],s65 offen sc1
buffer_load_dwordx4 v[116:119],v6,s[36:39],s65 offen offset:1024 sc1
buffer_load_dwordx4 v[120:123],v6,s[36:39],s65 offen offset:2048 sc1
buffer_load_dwordx4 v[124:127],v28,s[36:39],s65 offen offset:3072 sc1
s_waitcnt vmcnt(7)
v_mfma_f32_16x16x32_bf16 v[128:131],v[48:51],v[96:99],0
s_waitcnt vmcnt(6)
v_mfma_f32_16x16x32_bf16 v[128:131],v[48:51],v[100:103],v[128:131]
s_waitcnt vmcnt(5)
v_mfma_f32_16x16x32_bf16 v[128:131],v[48:51],v[104:107],v[128:131]
s_waitcnt vmcnt(4)
v_mfma_f32_16x16x32_bf16 v[128:131],v[48:51],v[108:111],v[128:131]
v_mfma_f32_16x16x32_bf16 v[160:163],v[52:55],v[96:99],0
v_mfma_f32_16x16x32_bf16 v[160:163],v[52:55],v[100:103],v[160:163]
v_mfma_f32_16x16x32_bf16 v[160:163],v[52:55],v[104:107],v[160:163]
v_mfma_f32_16x16x32_bf16 v[160:163],v[52:55],v[108:111],v[160:163]
s_waitcnt vmcnt(3)
v_mfma_f32_16x16x32_bf16 v[192:195],v[48:51],v[112:115],v[192:195]
v_mfma_f32_16x16x32_bf16 v[224:227],v[52:55],v[112:115],v[224:227]
s_waitcnt vmcnt(2)
v_mfma_f32_16x16x32_bf16 v[200:203],v[48:51],v[116:119],v[200:203]
v_mfma_f32_16x16x32_bf16 v[232:235],v[52:55],v[116:119],v[232:235]
s_waitcnt vmcnt(1)
v_cvt_pk_bf16_f32 v128,v128,v129
v_cvt_pk_bf16_f32 v129,v130,v131
v_mfma_f32_16x16x32_bf16 v[208:211],v[48:51],v[120:123],v[208:211]
v_mfma_f32_16x16x32_bf16 v[240:243],v[52:55],v[120:123],v[240:243]
s_waitcnt vmcnt(0)
v_cvt_pk_bf16_f32 v130,v160,v161
v_cvt_pk_bf16_f32 v131,v162,v163
v_mfma_f32_16x16x32_bf16 v[216:219],v[48:51],v[124:127],v[216:219]
v_cvt_pk_bf16_f32 v192,v192,v193
v_cvt_pk_bf16_f32 v193,v194,v195
v_cvt_pk_bf16_f32 v194,v224,v225
v_cvt_pk_bf16_f32 v195,v226,v227
v_cvt_pk_bf16_f32 v200,v200,v201
v_cvt_pk_bf16_f32 v201,v202,v203
v_mfma_f32_16x16x32_bf16 v[248:251],v[52:55],v[124:127],v[248:251]
v_permlane16_swap_b32 v128,v130
v_permlane16_swap_b32 v129,v131
v_cvt_pk_bf16_f32 v202,v232,v233
v_cvt_pk_bf16_f32 v203,v234,v235
v_cvt_pk_bf16_f32 v208,v208,v209
v_cvt_pk_bf16_f32 v209,v210,v211
v_cvt_pk_bf16_f32 v210,v240,v241
v_cvt_pk_bf16_f32 v211,v242,v243
buffer_store_dwordx4 v[128:131],v56,s[24:27],s83 offen offset:256 sc1 nt
v_permlane16_swap_b32 v192,v194
v_permlane16_swap_b32 v193,v195
buffer_store_dwordx4 v[192:195],v56,s[24:27],s87 offen sc1 nt
v_permlane16_swap_b32 v200,v202
v_permlane16_swap_b32 v201,v203
v_cvt_pk_bf16_f32 v216,v216,v217
v_cvt_pk_bf16_f32 v217,v218,v219
v_cvt_pk_bf16_f32 v218,v248,v249
v_cvt_pk_bf16_f32 v219,v250,v251
buffer_store_dwordx4 v[200:203],v56,s[24:27],s88 offen sc1 nt
v_permlane16_swap_b32 v208,v210
v_permlane16_swap_b32 v209,v211
buffer_store_dwordx4 v[208:211],v56,s[24:27],s89 offen sc1 nt
v_permlane16_swap_b32 v216,v218
v_permlane16_swap_b32 v217,v219
buffer_store_dwordx4 v[216:219],v56,s[24:27],s90 offen sc1 nt
s_lshl_b32 s69,s62,20
s_add_u32 s69,s69,s18
buffer_store_dword v20,v22,s[44:47],s69 offen sc1
L_geF_%=:
s_branch L_end_%=
.p2align 12
L_pd19_%=:
s_cbranch_execz L_trt1_%=
L_GS_%=:
L_gbS_%=:
s_waitcnt lgkmcnt(14)
v_mfma_f32_16x16x32_bf16 a[192:195],v[64:67],a[68:71],0
buffer_store_dwordx4 v[32:35],v6,s[40:43],s79 offen sc1
s_waitcnt lgkmcnt(12)
v_mfma_f32_16x16x32_bf16 a[192:195],v[68:71],a[76:79],a[192:195]
s_lshl_b32 s91,s63,8
v_bfe_u32 v3,v6,4,2
v_min_u32 v3,2,v3
s_waitcnt lgkmcnt(10)
v_mfma_f32_16x16x32_bf16 a[192:195],v[72:75],a[84:87],a[192:195]
s_add_u32 s87,s64,1
v_add3_u32 v3,v3,s64,1
v_and_b32 v3,3,v3
s_waitcnt lgkmcnt(8)
v_mfma_f32_16x16x32_bf16 a[192:195],v[76:79],a[92:95],a[192:195]
s_and_b32 s87,s87,3
ds_read_b64_tr_b16 v[96:97],v1 offset:64
v_lshlrev_b32 v3,6,v3
v_mfma_f32_16x16x32_bf16 a[196:199],v[76:79],a[124:127],0
s_lshl_b32 s87,s87,1
ds_read_b64_tr_b16 v[98:99],v1 offset:5184
ds_read_b64_tr_b16 v[100:101],v1 offset:10304
v_mfma_f32_16x16x32_bf16 a[196:199],v[72:75],a[116:119],a[196:199]
s_lshl_b32 s87,s87,16
ds_read_b64_tr_b16 v[102:103],v1 offset:15424
v_lshlrev_b32 v29,10,v6
v_mfma_f32_16x16x32_bf16 a[196:199],v[68:71],a[108:111],a[196:199]
s_add_u32 s88,s87,4096
ds_read_b64_tr_b16 v[104:105],v1 offset:20544
v_accvgpr_read_b32 v16,a24
v_mfma_f32_16x16x32_bf16 a[196:199],v[64:67],a[100:103],a[196:199]
s_add_u32 s89,s87,8192
ds_read_b64_tr_b16 v[106:107],v1 offset:25664
v_accvgpr_read_b32 v17,a25
v_mfma_f32_16x16x32_bf16 a[200:203],v[64:67],a[64:67],0
s_add_u32 s90,s87,12288
ds_read_b64_tr_b16 v[108:109],v1 offset:30784
ds_read_b64_tr_b16 v[110:111],v1 offset:35904
v_mfma_f32_16x16x32_bf16 a[200:203],v[68:71],a[72:75],a[200:203]
s_add_u32 m0,s86,122880
ds_read_b64_tr_b16 v[112:113],v2 offset:96
v_accvgpr_read_b32 v18,a26
v_mfma_f32_16x16x32_bf16 a[200:203],v[72:75],a[80:83],a[200:203]
s_add_u32 s83,s64,3
ds_read_b64_tr_b16 v[114:115],v2 offset:5216
v_accvgpr_read_b32 v19,a27
v_mfma_f32_16x16x32_bf16 a[200:203],v[76:79],a[88:91],a[200:203]
s_and_b32 s83,s83,3
ds_read_b64_tr_b16 v[116:117],v2 offset:10336
v_accvgpr_read_b32 v20,a28
v_mfma_f32_16x16x32_bf16 a[204:207],v[76:79],a[120:123],0
s_lshl_b32 s83,s83,1
ds_read_b64_tr_b16 v[118:119],v2 offset:15456
ds_read_b64_tr_b16 v[120:121],v2 offset:20576
v_mfma_f32_16x16x32_bf16 a[204:207],v[72:75],a[112:115],a[204:207]
s_mul_i32 s83,s83,163840
ds_read_b64_tr_b16 v[122:123],v2 offset:25696
v_accvgpr_read_b32 v21,a29
v_mfma_f32_16x16x32_bf16 a[204:207],v[68:71],a[104:107],a[204:207]
s_add_u32 s83,s83,s85
ds_read_b64_tr_b16 v[124:125],v2 offset:30816
v_accvgpr_read_b32 v22,a30
v_mfma_f32_16x16x32_bf16 a[204:207],v[64:67],a[96:99],a[204:207]
buffer_store_dwordx4 v[36:39],v6,s[40:43],s80 offen sc1
s_waitcnt lgkmcnt(9)
v_mfma_f32_16x16x32_bf16 a[208:211],v[80:83],a[68:71],0
s_add_u32 s84,s83,20480
ds_read_b64_tr_b16 v[126:127],v2 offset:35936
ds_read_b64_tr_b16 v[64:65],v1 offset:128
v_mfma_f32_16x16x32_bf16 a[208:211],v[84:87],a[76:79],a[208:211]
s_lshl_b32 s66,s61,2
ds_read_b64_tr_b16 v[66:67],v1 offset:5248
v_accvgpr_read_b32 v23,a31
v_mfma_f32_16x16x32_bf16 a[208:211],v[88:91],a[84:87],a[208:211]
s_add_u32 s66,s66,s64
ds_read_b64_tr_b16 v[68:69],v1 offset:10368
v_mul_f32 v16,v16,v49
v_mfma_f32_16x16x32_bf16 a[208:211],v[92:95],a[92:95],a[208:211]
s_mul_i32 s66,s66,262144
ds_read_b64_tr_b16 v[70:71],v1 offset:15488
v_mul_f32 v17,v17,v49
v_mfma_f32_16x16x32_bf16 a[212:215],v[92:95],a[124:127],0
s_waitcnt vmcnt(21)
buffer_load_dwordx2 v[4:5],v3,s[44:47],s91 offen offset:256 sc1
v_mfma_f32_16x16x32_bf16 a[212:215],v[88:91],a[116:119],a[212:215]
s_add_u32 s66,s66,134217728
ds_read_b64_tr_b16 v[72:73],v1 offset:20608
ds_read_b64_tr_b16 v[74:75],v1 offset:25728
v_mfma_f32_16x16x32_bf16 a[212:215],v[84:87],a[108:111],a[212:215]
s_mov_b32 s38,262144
ds_read_b64_tr_b16 v[76:77],v1 offset:30848
v_mul_f32 v18,v18,v49
v_mfma_f32_16x16x32_bf16 a[212:215],v[80:83],a[100:103],a[212:215]
s_mov_b32 s39,0x20000
ds_read_b64_tr_b16 v[78:79],v1 offset:35968
v_mul_f32 v19,v19,v49
v_mfma_f32_16x16x32_bf16 a[216:219],v[80:83],a[64:67],0
s_add_u32 s74,s63,1
v_mul_f32 v20,v20,v49
v_mul_f32 v21,v21,v49
v_mfma_f32_16x16x32_bf16 a[216:219],v[84:87],a[72:75],a[216:219]
s_and_b32 s74,s74,3
v_mul_f32 v22,v22,v49
v_mul_f32 v23,v23,v49
v_mfma_f32_16x16x32_bf16 a[216:219],v[88:91],a[80:83],a[216:219]
s_lshl_b32 s74,s74,2
v_cvt_pk_bf16_f32 v44,v16,v17
v_cvt_pk_bf16_f32 v45,v18,v19
v_mfma_f32_16x16x32_bf16 a[216:219],v[92:95],a[88:91],a[216:219]
s_add_u32 s74,s74,s60
v_cvt_pk_bf16_f32 v46,v20,v21
v_cvt_pk_bf16_f32 v47,v22,v23
v_mfma_f32_16x16x32_bf16 a[220:223],v[92:95],a[120:123],0
s_mul_i32 s74,s74,16384
ds_read_b64_tr_b16 v[92:93],v2 offset:30880
ds_read_b64_tr_b16 v[94:95],v2 offset:36000
v_mfma_f32_16x16x32_bf16 a[220:223],v[88:91],a[112:115],a[220:223]
s_add_u32 s74,s74,8192
ds_read_b64_tr_b16 v[88:89],v2 offset:20640
v_mul_f32 v132,v132,v53
v_mfma_f32_16x16x32_bf16 a[220:223],v[84:87],a[104:107],a[220:223]
buffer_store_dwordx4 v[40:43],v6,s[40:43],s81 offen sc1
v_mfma_f32_16x16x32_bf16 a[220:223],v[80:83],a[96:99],a[220:223]
s_add_u32 s75,s63,2
ds_read_b64_tr_b16 v[80:81],v2 offset:160
ds_read_b64_tr_b16 v[82:83],v2 offset:5280
v_mfma_f32_16x16x32_bf16 a[224:227],v[96:99],a[68:71],0
s_and_b32 s75,s75,3
ds_read_b64_tr_b16 v[84:85],v2 offset:10400
v_mul_f32 v133,v133,v53
v_mfma_f32_16x16x32_bf16 a[224:227],v[100:103],a[76:79],a[224:227]
s_lshl_b32 s75,s75,2
ds_read_b64_tr_b16 v[86:87],v2 offset:15520
v_mul_f32 v134,v134,v53
v_mfma_f32_16x16x32_bf16 a[224:227],v[104:107],a[84:87],a[224:227]
s_add_u32 s75,s75,s60
ds_read_b64_tr_b16 v[90:91],v2 offset:25760
v_mul_f32 v135,v135,v53
s_waitcnt lgkmcnt(8)
v_mfma_f32_16x16x32_bf16 a[224:227],v[108:111],a[92:95],a[224:227]
s_mul_i32 s75,s75,16384
v_mul_f32 v140,v140,v53
v_mul_f32 v141,v141,v53
v_mfma_f32_16x16x32_bf16 a[228:231],v[108:111],a[124:127],0
s_add_u32 s75,s75,4096
v_mul_f32 v142,v142,v53
v_mul_f32 v143,v143,v53
v_mfma_f32_16x16x32_bf16 a[228:231],v[104:107],a[116:119],a[228:231]
s_add_u32 s76,s63,3
v_cvt_pk_bf16_f32 v132,v132,v133
v_cvt_pk_bf16_f32 v133,v134,v135
v_mfma_f32_16x16x32_bf16 a[228:231],v[100:103],a[108:111],a[228:231]
s_and_b32 s76,s76,3
v_cvt_pk_bf16_f32 v134,v140,v141
v_cvt_pk_bf16_f32 v135,v142,v143
v_mfma_f32_16x16x32_bf16 a[228:231],v[96:99],a[100:103],a[228:231]
s_lshl_b32 s76,s76,2
v_mul_f32 v148,v148,v53
v_mul_f32 v149,v149,v53
v_mfma_f32_16x16x32_bf16 a[232:235],v[96:99],a[64:67],0
s_add_u32 s76,s76,s60
v_mul_f32 v150,v150,v53
v_mul_f32 v151,v151,v53
v_mfma_f32_16x16x32_bf16 a[232:235],v[100:103],a[72:75],a[232:235]
s_mul_i32 s76,s76,16384
v_mul_f32 v156,v156,v53
v_mul_f32 v157,v157,v53
v_mfma_f32_16x16x32_bf16 a[232:235],v[104:107],a[80:83],a[232:235]
s_add_u32 s76,s76,0
v_mul_f32 v158,v158,v53
v_mul_f32 v159,v159,v53
v_mfma_f32_16x16x32_bf16 a[232:235],v[108:111],a[88:91],a[232:235]
s_lshl_b32 s67,s63,10
v_cvt_pk_bf16_f32 v148,v148,v149
v_cvt_pk_bf16_f32 v149,v150,v151
v_mfma_f32_16x16x32_bf16 a[236:239],v[108:111],a[120:123],0
s_mov_b32 s26,307200
ds_read_b64_tr_b16 v[108:109],v1 offset:30912
ds_read_b64_tr_b16 v[110:111],v1 offset:36032
v_mfma_f32_16x16x32_bf16 a[236:239],v[104:107],a[112:115],a[236:239]
buffer_store_dwordx4 v[44:47],v6,s[40:43],s82 offen sc1
v_mfma_f32_16x16x32_bf16 a[236:239],v[100:103],a[104:107],a[236:239]
s_mov_b32 s27,0x20000
ds_read_b64_tr_b16 v[100:101],v1 offset:10432
ds_read_b64_tr_b16 v[102:103],v1 offset:15552
v_mfma_f32_16x16x32_bf16 a[236:239],v[96:99],a[96:99],a[236:239]
s_lshl_b32 s77,s63,2
ds_read_b64_tr_b16 v[96:97],v1 offset:192
v_cvt_pk_bf16_f32 v150,v156,v157
v_mfma_f32_16x16x32_bf16 a[240:243],v[112:115],a[68:71],0
)A"
R"A(s_add_u32 s77,s77,s60
ds_read_b64_tr_b16 v[98:99],v1 offset:5312
v_cvt_pk_bf16_f32 v151,v158,v159
v_mfma_f32_16x16x32_bf16 a[240:243],v[116:119],a[76:79],a[240:243]
s_mul_i32 s77,s77,16384
ds_read_b64_tr_b16 v[104:105],v1 offset:20672
v_mul_f32 v164,v164,v53
v_mfma_f32_16x16x32_bf16 a[240:243],v[120:123],a[84:87],a[240:243]
s_add_u32 s65,s77,0
ds_read_b64_tr_b16 v[106:107],v1 offset:25792
v_mul_f32 v165,v165,v53
v_mfma_f32_16x16x32_bf16 a[240:243],v[124:127],a[92:95],a[240:243]
s_add_u32 s68,s77,12288
v_mul_f32 v166,v166,v53
v_mul_f32 v167,v167,v53
v_mfma_f32_16x16x32_bf16 a[244:247],v[124:127],a[124:127],0
v_mul_f32 v172,v172,v53
v_mul_f32 v173,v173,v53
v_mfma_f32_16x16x32_bf16 a[244:247],v[120:123],a[116:119],a[244:247]
v_mul_f32 v174,v174,v53
v_mul_f32 v175,v175,v53
v_mfma_f32_16x16x32_bf16 a[244:247],v[116:119],a[108:111],a[244:247]
v_cvt_pk_bf16_f32 v164,v164,v165
v_cvt_pk_bf16_f32 v165,v166,v167
v_mfma_f32_16x16x32_bf16 a[244:247],v[112:115],a[100:103],a[244:247]
v_cvt_pk_bf16_f32 v166,v172,v173
v_cvt_pk_bf16_f32 v167,v174,v175
v_mfma_f32_16x16x32_bf16 a[248:251],v[112:115],a[64:67],0
v_mul_f32 v180,v180,v53
v_mul_f32 v181,v181,v53
v_mfma_f32_16x16x32_bf16 a[248:251],v[116:119],a[72:75],a[248:251]
v_mul_f32 v182,v182,v53
v_mul_f32 v183,v183,v53
v_mfma_f32_16x16x32_bf16 a[248:251],v[120:123],a[80:83],a[248:251]
v_mul_f32 v188,v188,v53
v_mul_f32 v189,v189,v53
v_mfma_f32_16x16x32_bf16 a[248:251],v[124:127],a[88:91],a[248:251]
v_mul_f32 v190,v190,v53
v_mul_f32 v191,v191,v53
v_mfma_f32_16x16x32_bf16 a[252:255],v[124:127],a[120:123],0
buffer_load_dword v30,v29,s[8:11],s7 offen
v_mfma_f32_16x16x32_bf16 a[252:255],v[120:123],a[112:115],a[252:255]
ds_read_b64_tr_b16 v[120:121],v2 offset:20704
ds_read_b64_tr_b16 v[122:123],v2 offset:25824
v_mfma_f32_16x16x32_bf16 a[252:255],v[116:119],a[104:107],a[252:255]
buffer_store_dwordx4 v[132:135],v6,s[40:43],s79 offen offset:1024 sc1
v_mfma_f32_16x16x32_bf16 a[252:255],v[112:115],a[96:99],a[252:255]
ds_read_b64_tr_b16 v[112:113],v2 offset:224
ds_read_b64_tr_b16 v[114:115],v2 offset:5344
v_mfma_f32_16x16x32_bf16 a[0:3],v[64:67],a[68:71],0
ds_read_b64_tr_b16 v[116:117],v2 offset:10464
v_cvt_pk_bf16_f32 v180,v180,v181
v_mfma_f32_16x16x32_bf16 a[0:3],v[68:71],a[76:79],a[0:3]
ds_read_b64_tr_b16 v[118:119],v2 offset:15584
v_cvt_pk_bf16_f32 v181,v182,v183
v_mfma_f32_16x16x32_bf16 a[0:3],v[72:75],a[84:87],a[0:3]
ds_read_b64_tr_b16 v[124:125],v2 offset:30944
v_cvt_pk_bf16_f32 v182,v188,v189
v_mfma_f32_16x16x32_bf16 a[0:3],v[76:79],a[92:95],a[0:3]
ds_read_b64_tr_b16 v[126:127],v2 offset:36064
v_cvt_pk_bf16_f32 v183,v190,v191
v_mfma_f32_16x16x32_bf16 a[4:7],v[76:79],a[124:127],0
v_mul_f32 v196,v196,v57
v_mul_f32 v197,v197,v57
v_mfma_f32_16x16x32_bf16 a[4:7],v[72:75],a[116:119],a[4:7]
v_mul_f32 v198,v198,v57
v_mul_f32 v199,v199,v57
v_mfma_f32_16x16x32_bf16 a[4:7],v[68:71],a[108:111],a[4:7]
v_mul_f32 v204,v204,v57
v_mul_f32 v205,v205,v57
v_mfma_f32_16x16x32_bf16 a[4:7],v[64:67],a[100:103],a[4:7]
v_mul_f32 v206,v206,v57
v_mul_f32 v207,v207,v57
v_mfma_f32_16x16x32_bf16 a[8:11],v[64:67],a[64:67],0
v_cvt_pk_bf16_f32 v196,v196,v197
v_cvt_pk_bf16_f32 v197,v198,v199
v_mfma_f32_16x16x32_bf16 a[8:11],v[68:71],a[72:75],a[8:11]
v_cvt_pk_bf16_f32 v198,v204,v205
v_cvt_pk_bf16_f32 v199,v206,v207
v_mfma_f32_16x16x32_bf16 a[8:11],v[72:75],a[80:83],a[8:11]
v_mul_f32 v212,v212,v57
v_mul_f32 v213,v213,v57
v_mfma_f32_16x16x32_bf16 a[8:11],v[76:79],a[88:91],a[8:11]
v_mul_f32 v214,v214,v57
v_mul_f32 v215,v215,v57
v_mfma_f32_16x16x32_bf16 a[12:15],v[76:79],a[120:123],0
ds_read_b64_tr_b16 v[76:77],v1 offset:30976
ds_read_b64_tr_b16 v[78:79],v1 offset:36096
v_mfma_f32_16x16x32_bf16 a[12:15],v[72:75],a[112:115],a[12:15]
buffer_store_dwordx4 v[148:151],v6,s[40:43],s80 offen offset:1024 sc1
v_mfma_f32_16x16x32_bf16 a[12:15],v[68:71],a[104:107],a[12:15]
ds_read_b64_tr_b16 v[68:69],v1 offset:10496
ds_read_b64_tr_b16 v[70:71],v1 offset:15616
v_mfma_f32_16x16x32_bf16 a[12:15],v[64:67],a[96:99],a[12:15]
s_waitcnt lgkmcnt(4)
ds_read_b64_tr_b16 v[64:65],v1 offset:256
v_mul_f32 v220,v220,v57
v_mfma_f32_16x16x32_bf16 a[16:19],v[80:83],a[68:71],0
ds_read_b64_tr_b16 v[66:67],v1 offset:5376
v_mul_f32 v221,v221,v57
v_mfma_f32_16x16x32_bf16 a[16:19],v[84:87],a[76:79],a[16:19]
ds_read_b64_tr_b16 v[72:73],v1 offset:20736
v_mul_f32 v222,v222,v57
v_mfma_f32_16x16x32_bf16 a[16:19],v[88:91],a[84:87],a[16:19]
ds_read_b64_tr_b16 v[74:75],v1 offset:25856
v_add_u32 v1,0xffff6000,v1
v_mfma_f32_16x16x32_bf16 a[16:19],v[92:95],a[92:95],a[16:19]
v_mul_f32 v223,v223,v57
v_cvt_pk_bf16_f32 v212,v212,v213
v_mfma_f32_16x16x32_bf16 a[20:23],v[92:95],a[124:127],0
v_cvt_pk_bf16_f32 v213,v214,v215
v_cvt_pk_bf16_f32 v214,v220,v221
v_mfma_f32_16x16x32_bf16 a[20:23],v[88:91],a[116:119],a[20:23]
v_cvt_pk_bf16_f32 v215,v222,v223
v_mul_f32 v228,v228,v57
v_mfma_f32_16x16x32_bf16 a[20:23],v[84:87],a[108:111],a[20:23]
v_mul_f32 v229,v229,v57
v_mul_f32 v230,v230,v57
v_mfma_f32_16x16x32_bf16 a[20:23],v[80:83],a[100:103],a[20:23]
v_mul_f32 v231,v231,v57
v_mul_f32 v236,v236,v57
v_mfma_f32_16x16x32_bf16 a[24:27],v[80:83],a[64:67],0
v_mul_f32 v237,v237,v57
v_mul_f32 v238,v238,v57
v_mfma_f32_16x16x32_bf16 a[24:27],v[84:87],a[72:75],a[24:27]
v_mul_f32 v239,v239,v57
v_cvt_pk_bf16_f32 v228,v228,v229
v_mfma_f32_16x16x32_bf16 a[24:27],v[88:91],a[80:83],a[24:27]
s_waitcnt vmcnt(5)
v_cmp_ne_u32 s[56:57],s52,v4
v_cmp_ne_u32 s[58:59],s53,v5
v_mfma_f32_16x16x32_bf16 a[24:27],v[92:95],a[88:91],a[24:27]
s_or_b64 s[56:57],s[56:57],s[58:59]
s_cbranch_scc1 L_fr6_%=
L_fo6_%=:
v_cvt_pk_bf16_f32 v229,v230,v231
v_mfma_f32_16x16x32_bf16 a[28:31],v[92:95],a[120:123],0
buffer_store_dwordx4 v[164:167],v6,s[40:43],s81 offen offset:1024 sc1
v_mfma_f32_16x16x32_bf16 a[28:31],v[88:91],a[112:115],a[28:31]
s_lshl_b32 s91,s63,8
ds_read_b64_tr_b16 v[88:89],v2 offset:20768
ds_read_b64_tr_b16 v[90:91],v2 offset:25888
v_mfma_f32_16x16x32_bf16 a[28:31],v[84:87],a[104:107],a[28:31]
buffer_load_dwordx4 a[128:131],v6,s[28:31],s87 offen sc1
v_mfma_f32_16x16x32_bf16 a[28:31],v[80:83],a[96:99],a[28:31]
ds_read_b64_tr_b16 v[80:81],v2 offset:288
ds_read_b64_tr_b16 v[82:83],v2 offset:5408
v_mfma_f32_16x16x32_bf16 v[128:131],v[96:99],a[68:71],0
ds_read_b64_tr_b16 v[84:85],v2 offset:10528
v_cvt_pk_bf16_f32 v230,v236,v237
v_mfma_f32_16x16x32_bf16 v[128:131],v[100:103],a[76:79],v[128:131]
ds_read_b64_tr_b16 v[86:87],v2 offset:15648
v_cvt_pk_bf16_f32 v231,v238,v239
v_mfma_f32_16x16x32_bf16 v[128:131],v[104:107],a[84:87],v[128:131]
ds_read_b64_tr_b16 v[92:93],v2 offset:31008
v_mul_f32 v244,v244,v57
v_mfma_f32_16x16x32_bf16 v[128:131],v[108:111],a[92:95],v[128:131]
ds_read_b64_tr_b16 v[94:95],v2 offset:36128
v_add_u32 v2,0xffff6000,v2
v_mfma_f32_16x16x32_bf16 v[136:139],v[108:111],a[124:127],0
v_mul_f32 v245,v245,v57
v_mul_f32 v246,v246,v57
v_mfma_f32_16x16x32_bf16 v[136:139],v[104:107],a[116:119],v[136:139]
v_mul_f32 v247,v247,v57
v_mul_f32 v252,v252,v57
v_mfma_f32_16x16x32_bf16 v[136:139],v[100:103],a[108:111],v[136:139]
v_mul_f32 v253,v253,v57
v_mul_f32 v254,v254,v57
v_mfma_f32_16x16x32_bf16 v[136:139],v[96:99],a[100:103],v[136:139]
v_mul_f32 v255,v255,v57
v_cvt_pk_bf16_f32 v244,v244,v245
v_mfma_f32_16x16x32_bf16 v[144:147],v[96:99],a[64:67],0
v_cvt_pk_bf16_f32 v245,v246,v247
v_cvt_pk_bf16_f32 v246,v252,v253
v_mfma_f32_16x16x32_bf16 v[144:147],v[100:103],a[72:75],v[144:147]
buffer_load_dwordx4 a[144:147],v6,s[28:31],s88 offen sc1
v_mfma_f32_16x16x32_bf16 v[144:147],v[104:107],a[80:83],v[144:147]
v_cvt_pk_bf16_f32 v247,v254,v255
v_accvgpr_read_b32 v16,a40
v_mfma_f32_16x16x32_bf16 v[144:147],v[108:111],a[88:91],v[144:147]
buffer_store_dwordx4 v[180:183],v6,s[40:43],s82 offen offset:1024 sc1
v_mfma_f32_16x16x32_bf16 v[152:155],v[108:111],a[120:123],0
ds_read_b64_tr_b16 v[108:109],v1 offset:30720
ds_read_b64_tr_b16 v[110:111],v1 offset:35840
v_mfma_f32_16x16x32_bf16 v[152:155],v[104:107],a[112:115],v[152:155]
ds_read_b64_tr_b16 v[104:105],v1 offset:20480
v_accvgpr_read_b32 v17,a41
v_mfma_f32_16x16x32_bf16 v[152:155],v[100:103],a[104:107],v[152:155]
ds_read_b64_tr_b16 v[100:101],v1 offset:10240
v_accvgpr_read_b32 v18,a42
v_mfma_f32_16x16x32_bf16 v[152:155],v[96:99],a[96:99],v[152:155]
ds_read_b64_tr_b16 v[96:97],v1
v_accvgpr_read_b32 v19,a43
v_mfma_f32_16x16x32_bf16 v[160:163],v[112:115],a[68:71],0
ds_read_b64_tr_b16 v[98:99],v1 offset:5120
ds_read_b64_tr_b16 v[102:103],v1 offset:15360
v_mfma_f32_16x16x32_bf16 v[160:163],v[116:119],a[76:79],v[160:163]
ds_read_b64_tr_b16 v[106:107],v1 offset:25600
v_accvgpr_read_b32 v20,a44
v_mfma_f32_16x16x32_bf16 v[160:163],v[120:123],a[84:87],v[160:163]
v_accvgpr_read_b32 v21,a45
v_accvgpr_read_b32 v22,a46
v_mfma_f32_16x16x32_bf16 v[160:163],v[124:127],a[92:95],v[160:163]
v_accvgpr_read_b32 v23,a47
v_mul_f32 v16,v16,v61
v_mfma_f32_16x16x32_bf16 v[168:171],v[124:127],a[124:127],0
v_mul_f32 v17,v17,v61
v_mul_f32 v18,v18,v61
v_mfma_f32_16x16x32_bf16 v[168:171],v[120:123],a[116:119],v[168:171]
v_mul_f32 v19,v19,v61
v_mul_f32 v20,v20,v61
v_mfma_f32_16x16x32_bf16 v[168:171],v[116:119],a[108:111],v[168:171]
buffer_load_dwordx4 a[160:163],v6,s[28:31],s89 offen sc1
v_mfma_f32_16x16x32_bf16 v[168:171],v[112:115],a[100:103],v[168:171]
v_mul_f32 v21,v21,v61
v_mul_f32 v22,v22,v61
v_mfma_f32_16x16x32_bf16 v[176:179],v[112:115],a[64:67],0
v_mul_f32 v23,v23,v61
v_cvt_pk_bf16_f32 v156,v16,v17
v_mfma_f32_16x16x32_bf16 v[176:179],v[116:119],a[72:75],v[176:179]
v_cvt_pk_bf16_f32 v157,v18,v19
v_cvt_pk_bf16_f32 v158,v20,v21
v_mfma_f32_16x16x32_bf16 v[176:179],v[120:123],a[80:83],v[176:179]
buffer_store_dwordx4 v[196:199],v6,s[40:43],s79 offen offset:2048 sc1
v_mfma_f32_16x16x32_bf16 v[176:179],v[124:127],a[88:91],v[176:179]
v_cvt_pk_bf16_f32 v159,v22,v23
v_accvgpr_read_b32 v16,a56
v_mfma_f32_16x16x32_bf16 v[184:187],v[124:127],a[120:123],0
v_accvgpr_read_b32 v17,a57
v_accvgpr_read_b32 v18,a58
v_mfma_f32_16x16x32_bf16 v[184:187],v[120:123],a[112:115],v[184:187]
ds_read_b64_tr_b16 v[120:121],v2 offset:20512
ds_read_b64_tr_b16 v[122:123],v2 offset:25632
v_mfma_f32_16x16x32_bf16 v[184:187],v[116:119],a[104:107],v[184:187]
ds_read_b64_tr_b16 v[116:117],v2 offset:10272
v_accvgpr_read_b32 v19,a59
v_mfma_f32_16x16x32_bf16 v[184:187],v[112:115],a[96:99],v[184:187]
s_waitcnt lgkmcnt(3)
ds_read_b64_tr_b16 v[112:113],v2 offset:32
v_accvgpr_read_b32 v20,a60
v_mfma_f32_16x16x32_bf16 v[192:195],v[64:67],a[68:71],0
ds_read_b64_tr_b16 v[114:115],v2 offset:5152
v_accvgpr_read_b32 v21,a61
v_mfma_f32_16x16x32_bf16 v[192:195],v[68:71],a[76:79],v[192:195]
ds_read_b64_tr_b16 v[118:119],v2 offset:15392
v_accvgpr_read_b32 v22,a62
v_mfma_f32_16x16x32_bf16 v[192:195],v[72:75],a[84:87],v[192:195]
v_accvgpr_read_b32 v23,a63
v_mul_f32 v16,v16,v61
v_mfma_f32_16x16x32_bf16 v[192:195],v[76:79],a[92:95],v[192:195]
buffer_load_dwordx4 a[176:179],v6,s[28:31],s90 offen sc1
v_mfma_f32_16x16x32_bf16 v[200:203],v[76:79],a[124:127],0
v_mul_f32 v17,v17,v61
v_mul_f32 v18,v18,v61
v_mfma_f32_16x16x32_bf16 v[200:203],v[72:75],a[116:119],v[200:203]
v_mul_f32 v19,v19,v61
v_mul_f32 v20,v20,v61
v_mfma_f32_16x16x32_bf16 v[200:203],v[68:71],a[108:111],v[200:203]
v_mul_f32 v21,v21,v61
v_mul_f32 v22,v22,v61
v_mfma_f32_16x16x32_bf16 v[200:203],v[64:67],a[100:103],v[200:203]
v_mul_f32 v23,v23,v61
v_cvt_pk_bf16_f32 v188,v16,v17
v_mfma_f32_16x16x32_bf16 v[208:211],v[64:67],a[64:67],0
v_cvt_pk_bf16_f32 v189,v18,v19
v_cvt_pk_bf16_f32 v190,v20,v21
v_mfma_f32_16x16x32_bf16 v[208:211],v[68:71],a[72:75],v[208:211]
buffer_store_dwordx4 v[212:215],v6,s[40:43],s80 offen offset:2048 sc1
v_mfma_f32_16x16x32_bf16 v[208:211],v[72:75],a[80:83],v[208:211]
ds_read_b64_tr_b16 v[124:125],v2 offset:30752
v_cvt_pk_bf16_f32 v191,v22,v23
v_mfma_f32_16x16x32_bf16 v[208:211],v[76:79],a[88:91],v[208:211]
ds_read_b64_tr_b16 v[126:127],v2 offset:35872
v_accvgpr_read_b32 v8,a32
v_mfma_f32_16x16x32_bf16 v[216:219],v[76:79],a[120:123],0
v_accvgpr_read_b32 v9,a33
v_accvgpr_read_b32 v10,a34
v_mfma_f32_16x16x32_bf16 v[216:219],v[72:75],a[112:115],v[216:219]
v_accvgpr_read_b32 v11,a35
v_accvgpr_read_b32 v12,a36
v_mfma_f32_16x16x32_bf16 v[216:219],v[68:71],a[104:107],v[216:219]
)A"
R"A(v_accvgpr_read_b32 v13,a37
v_accvgpr_read_b32 v14,a38
v_mfma_f32_16x16x32_bf16 v[216:219],v[64:67],a[96:99],v[216:219]
v_accvgpr_read_b32 v15,a39
v_mul_f32 v8,v8,v61
v_mfma_f32_16x16x32_bf16 v[224:227],v[80:83],a[68:71],0
buffer_load_dwordx4 a[180:183],v6,s[28:31],s90 offen offset:1024 sc1
v_mfma_f32_16x16x32_bf16 v[224:227],v[84:87],a[76:79],v[224:227]
v_mul_f32 v9,v9,v61
v_mul_f32 v10,v10,v61
v_mfma_f32_16x16x32_bf16 v[224:227],v[88:91],a[84:87],v[224:227]
v_mul_f32 v11,v11,v61
v_mul_f32 v12,v12,v61
v_mfma_f32_16x16x32_bf16 v[224:227],v[92:95],a[92:95],v[224:227]
v_mul_f32 v13,v13,v61
v_mul_f32 v14,v14,v61
v_mfma_f32_16x16x32_bf16 v[232:235],v[92:95],a[124:127],0
v_mul_f32 v15,v15,v61
v_cvt_pk_bf16_f32 v140,v8,v9
v_mfma_f32_16x16x32_bf16 v[232:235],v[88:91],a[116:119],v[232:235]
v_cvt_pk_bf16_f32 v141,v10,v11
v_cvt_pk_bf16_f32 v142,v12,v13
v_mfma_f32_16x16x32_bf16 v[232:235],v[84:87],a[108:111],v[232:235]
v_cvt_pk_bf16_f32 v143,v14,v15
v_accvgpr_read_b32 v8,a48
v_mfma_f32_16x16x32_bf16 v[232:235],v[80:83],a[100:103],v[232:235]
v_accvgpr_read_b32 v9,a49
v_accvgpr_read_b32 v10,a50
v_mfma_f32_16x16x32_bf16 v[240:243],v[80:83],a[64:67],0
buffer_store_dwordx4 v[228:231],v6,s[40:43],s81 offen offset:2048 sc1
v_mfma_f32_16x16x32_bf16 v[240:243],v[84:87],a[72:75],v[240:243]
v_accvgpr_read_b32 v11,a51
v_accvgpr_read_b32 v12,a52
v_mfma_f32_16x16x32_bf16 v[240:243],v[88:91],a[80:83],v[240:243]
v_accvgpr_read_b32 v13,a53
v_accvgpr_read_b32 v14,a54
v_mfma_f32_16x16x32_bf16 v[240:243],v[92:95],a[88:91],v[240:243]
v_accvgpr_read_b32 v15,a55
v_mul_f32 v8,v8,v61
v_mfma_f32_16x16x32_bf16 v[248:251],v[92:95],a[120:123],0
v_mul_f32 v9,v9,v61
v_mul_f32 v10,v10,v61
v_mfma_f32_16x16x32_bf16 v[248:251],v[88:91],a[112:115],v[248:251]
buffer_load_dwordx4 a[164:167],v6,s[28:31],s89 offen offset:1024 sc1
v_mfma_f32_16x16x32_bf16 v[248:251],v[84:87],a[104:107],v[248:251]
v_mul_f32 v11,v11,v61
v_mul_f32 v12,v12,v61
v_mfma_f32_16x16x32_bf16 v[248:251],v[80:83],a[96:99],v[248:251]
v_mul_f32 v13,v13,v61
v_mul_f32 v14,v14,v61
v_mfma_f32_16x16x32_bf16 a[192:195],v[96:99],v[32:35],a[192:195]
v_mul_f32 v15,v15,v61
v_cvt_pk_bf16_f32 v172,v8,v9
v_mfma_f32_16x16x32_bf16 a[192:195],v[100:103],v[36:39],a[192:195]
v_cvt_pk_bf16_f32 v173,v10,v11
v_cvt_pk_bf16_f32 v174,v12,v13
v_mfma_f32_16x16x32_bf16 a[192:195],v[104:107],v[40:43],a[192:195]
ds_read_b64_tr_b16 v[64:65],v1 offset:64
v_cvt_pk_bf16_f32 v175,v14,v15
v_mfma_f32_16x16x32_bf16 a[192:195],v[108:111],v[44:47],a[192:195]
ds_read_b64_tr_b16 v[66:67],v1 offset:5184
ds_read_b64_tr_b16 v[68:69],v1 offset:10304
v_mfma_f32_16x16x32_bf16 a[196:199],v[108:111],v[180:183],a[196:199]
s_cmp_eq_u32 s60,3
s_cselect_b32 s69,0x40000000,0
ds_read_b64_tr_b16 v[70:71],v1 offset:15424
v_mfma_f32_16x16x32_bf16 a[196:199],v[104:107],v[164:167],a[196:199]
ds_read_b64_tr_b16 v[72:73],v1 offset:20544
v_add_u32 v28,s69,v6
v_mfma_f32_16x16x32_bf16 a[196:199],v[100:103],v[148:151],a[196:199]
s_and_b32 s69,s63,s60
ds_read_b64_tr_b16 v[74:75],v1 offset:25664
v_mov_b32 v8,s52
v_mfma_f32_16x16x32_bf16 a[196:199],v[96:99],v[132:135],a[196:199]
buffer_store_dwordx4 v[244:247],v6,s[40:43],s82 offen offset:2048 sc1
v_mfma_f32_16x16x32_bf16 a[200:203],v[96:99],v[196:199],a[200:203]
ds_read_b64_tr_b16 v[76:77],v1 offset:30784
ds_read_b64_tr_b16 v[78:79],v1 offset:35904
v_mfma_f32_16x16x32_bf16 a[200:203],v[100:103],v[212:215],a[200:203]
ds_read_b64_tr_b16 v[80:81],v2 offset:96
v_mov_b32 v9,s53
v_mfma_f32_16x16x32_bf16 a[200:203],v[104:107],v[228:231],a[200:203]
buffer_load_dwordx4 a[148:151],v6,s[28:31],s88 offen offset:1024 sc1
v_mfma_f32_16x16x32_bf16 a[200:203],v[108:111],v[244:247],a[200:203]
ds_read_b64_tr_b16 v[82:83],v2 offset:5216
ds_read_b64_tr_b16 v[84:85],v2 offset:10336
v_mfma_f32_16x16x32_bf16 a[204:207],v[108:111],v[188:191],a[204:207]
ds_read_b64_tr_b16 v[86:87],v2 offset:15456
v_lshlrev_b32 v10,20,v6
v_mfma_f32_16x16x32_bf16 a[204:207],v[104:107],v[172:175],a[204:207]
ds_read_b64_tr_b16 v[88:89],v2 offset:20576
v_bfe_u32 v3,v6,4,2
v_mfma_f32_16x16x32_bf16 a[204:207],v[100:103],v[156:159],a[204:207]
ds_read_b64_tr_b16 v[90:91],v2 offset:25696
v_min_u32 v3,2,v3
v_mfma_f32_16x16x32_bf16 a[204:207],v[96:99],v[140:143],a[204:207]
s_waitcnt lgkmcnt(8)
ds_read_b64_tr_b16 v[92:93],v2 offset:30816
ds_read_b64_tr_b16 v[94:95],v2 offset:35936
v_mfma_f32_16x16x32_bf16 a[208:211],v[112:115],v[32:35],a[208:211]
ds_read_b64_tr_b16 v[96:97],v1 offset:128
v_add3_u32 v3,v3,s64,1
v_mfma_f32_16x16x32_bf16 a[208:211],v[116:119],v[36:39],a[208:211]
ds_read_b64_tr_b16 v[98:99],v1 offset:5248
v_and_b32 v3,3,v3
v_mfma_f32_16x16x32_bf16 a[208:211],v[120:123],v[40:43],a[208:211]
ds_read_b64_tr_b16 v[100:101],v1 offset:10368
v_lshlrev_b32 v3,6,v3
v_mfma_f32_16x16x32_bf16 a[208:211],v[124:127],v[44:47],a[208:211]
ds_read_b64_tr_b16 v[102:103],v1 offset:15488
ds_read_b64_tr_b16 v[104:105],v1 offset:20608
v_mfma_f32_16x16x32_bf16 a[212:215],v[124:127],v[180:183],a[212:215]
ds_read_b64_tr_b16 v[106:107],v1 offset:25728
v_bfe_u32 v56,v6,6,2
v_mfma_f32_16x16x32_bf16 a[212:215],v[120:123],v[164:167],a[212:215]
ds_read_b64_tr_b16 v[108:109],v1 offset:30848
v_lshrrev_b32 v57,8,v6
v_mfma_f32_16x16x32_bf16 a[212:215],v[116:119],v[148:151],a[212:215]
buffer_store_dwordx4 v[140:143],v28,s[40:43],s79 offen offset:3072 sc1
v_mfma_f32_16x16x32_bf16 a[212:215],v[112:115],v[132:135],a[212:215]
ds_read_b64_tr_b16 v[110:111],v1 offset:35968
v_cmp_eq_u32 s[56:57],v56,v57
L_pd20_%=:
s_cbranch_execz L_trt1_%=
v_mfma_f32_16x16x32_bf16 a[216:219],v[112:115],v[196:199],a[216:219]
buffer_load_dwordx4 a[132:135],v6,s[28:31],s87 offen offset:1024 sc1
v_mfma_f32_16x16x32_bf16 a[216:219],v[116:119],v[212:215],a[216:219]
v_bfe_u32 v56,v6,4,1
v_lshlrev_b32 v56,4,v56
v_mfma_f32_16x16x32_bf16 a[216:219],v[120:123],v[228:231],a[216:219]
v_mov_b32 v57,0x3f80
v_lshlrev_b32 v56,v56,v57
v_mfma_f32_16x16x32_bf16 a[216:219],v[124:127],v[244:247],a[216:219]
v_cndmask_b32 v56,0,v56,s[56:57]
v_bfe_u32 v57,v6,5,1
v_mfma_f32_16x16x32_bf16 a[220:223],v[124:127],v[188:191],a[220:223]
ds_read_b64_tr_b16 v[124:125],v2 offset:30880
ds_read_b64_tr_b16 v[126:127],v2 offset:36000
v_mfma_f32_16x16x32_bf16 a[220:223],v[120:123],v[172:175],a[220:223]
ds_read_b64_tr_b16 v[120:121],v2 offset:20640
v_cmp_eq_u32 s[56:57],0,v57
v_mfma_f32_16x16x32_bf16 a[220:223],v[116:119],v[156:159],a[220:223]
s_add_u32 s36,s12,s66
s_addc_u32 s37,s13,0
ds_read_b64_tr_b16 v[116:117],v2 offset:10400
v_mfma_f32_16x16x32_bf16 a[220:223],v[112:115],v[140:143],a[220:223]
s_and_b32 s37,s37,0xffff
ds_read_b64_tr_b16 v[112:113],v2 offset:160
v_cndmask_b32 v48,0,v56,s[56:57]
v_mfma_f32_16x16x32_bf16 a[224:227],v[64:67],v[32:35],a[224:227]
s_mul_i32 s66,s61,307200
ds_read_b64_tr_b16 v[114:115],v2 offset:5280
ds_read_b64_tr_b16 v[118:119],v2 offset:15520
v_mfma_f32_16x16x32_bf16 a[224:227],v[68:71],v[36:39],a[224:227]
ds_read_b64_tr_b16 v[122:123],v2 offset:25760
v_cndmask_b32 v49,v56,0,s[56:57]
v_mfma_f32_16x16x32_bf16 a[224:227],v[72:75],v[40:43],a[224:227]
s_waitcnt lgkmcnt(8)
v_mov_b32 v58,s52
v_mov_b32 v59,s53
v_mfma_f32_16x16x32_bf16 a[224:227],v[76:79],v[44:47],a[224:227]
s_add_u32 s24,s16,s66
s_addc_u32 s25,s17,0
v_lshlrev_b32 v60,20,v6
v_mfma_f32_16x16x32_bf16 a[228:231],v[76:79],v[180:183],a[228:231]
buffer_load_dwordx4 a[136:139],v6,s[28:31],s87 offen offset:2048 sc1
v_mfma_f32_16x16x32_bf16 a[228:231],v[72:75],v[164:167],a[228:231]
s_mul_i32 s66,s60,16384
s_add_u32 s66,s66,s67
s_add_u32 s79,s66,12288
v_mfma_f32_16x16x32_bf16 a[228:231],v[68:71],v[148:151],a[228:231]
buffer_store_dwordx4 v[156:159],v28,s[40:43],s80 offen offset:3072 sc1
v_mfma_f32_16x16x32_bf16 a[228:231],v[64:67],v[132:135],a[228:231]
s_add_u32 s80,s66,77824
v_bfe_u32 v61,v6,4,1
v_add3_u32 v61,v61,s63,2
v_mfma_f32_16x16x32_bf16 a[232:235],v[64:67],v[196:199],a[232:235]
s_and_b32 s25,s25,0xffff
v_and_b32 v61,3,v61
v_lshlrev_b32 v61,8,v61
v_mfma_f32_16x16x32_bf16 a[232:235],v[68:71],v[212:215],a[232:235]
s_add_u32 s67,s77,8192
v_mov_b32 v50,0
v_mov_b32 v51,0
v_mfma_f32_16x16x32_bf16 a[232:235],v[72:75],v[228:231],a[232:235]
v_mov_b32 v52,0
v_mov_b32 v53,0
v_mfma_f32_16x16x32_bf16 a[232:235],v[76:79],v[244:247],a[232:235]
v_mov_b32 v54,v48
v_mov_b32 v55,v49
v_mfma_f32_16x16x32_bf16 a[236:239],v[76:79],v[188:191],a[236:239]
ds_read_b64_tr_b16 v[76:77],v1 offset:30912
ds_read_b64_tr_b16 v[78:79],v1 offset:36032
v_mfma_f32_16x16x32_bf16 a[236:239],v[72:75],v[172:175],a[236:239]
ds_read_b64_tr_b16 v[72:73],v1 offset:20672
v_bfe_u32 v56,v6,4,4
v_mfma_f32_16x16x32_bf16 a[236:239],v[68:71],v[156:159],a[236:239]
ds_read_b64_tr_b16 v[68:69],v1 offset:10432
v_mul_u32_u24 v56,1280,v56
v_mfma_f32_16x16x32_bf16 a[236:239],v[64:67],v[140:143],a[236:239]
ds_read_b64_tr_b16 v[64:65],v1 offset:192
v_bfe_u32 v57,v6,8,1
v_mfma_f32_16x16x32_bf16 a[240:243],v[80:83],v[32:35],a[240:243]
ds_read_b64_tr_b16 v[66:67],v1 offset:5312
ds_read_b64_tr_b16 v[70:71],v1 offset:15552
v_mfma_f32_16x16x32_bf16 a[240:243],v[84:87],v[36:39],a[240:243]
buffer_load_dwordx4 a[152:155],v6,s[28:31],s88 offen offset:2048 sc1
v_mfma_f32_16x16x32_bf16 a[240:243],v[88:91],v[40:43],a[240:243]
ds_read_b64_tr_b16 v[74:75],v1 offset:25792
v_lshl_add_u32 v56,v57,5,v56
v_mfma_f32_16x16x32_bf16 a[240:243],v[92:95],v[44:47],a[240:243]
v_lshrrev_b32 v57,9,v6
v_lshl_add_u32 v56,v57,4,v56
v_mfma_f32_16x16x32_bf16 a[244:247],v[92:95],v[180:183],a[244:247]
s_cmp_eq_u32 s69,3
s_cselect_b32 s69,0x40000000,0
s_add_u32 s68,s68,s69
v_mfma_f32_16x16x32_bf16 a[244:247],v[88:91],v[164:167],a[244:247]
buffer_store_dwordx4 v[172:175],v28,s[40:43],s81 offen offset:3072 sc1
v_mfma_f32_16x16x32_bf16 a[244:247],v[84:87],v[148:151],a[244:247]
s_add_u32 s81,s66,143360
v_mfma_f32_16x16x32_bf16 a[244:247],v[80:83],v[132:135],a[244:247]
v_mfma_f32_16x16x32_bf16 a[248:251],v[80:83],v[196:199],a[248:251]
v_mfma_f32_16x16x32_bf16 a[248:251],v[84:87],v[212:215],a[248:251]
v_mfma_f32_16x16x32_bf16 a[248:251],v[88:91],v[228:231],a[248:251]
v_mfma_f32_16x16x32_bf16 a[248:251],v[92:95],v[244:247],a[248:251]
v_mfma_f32_16x16x32_bf16 a[252:255],v[92:95],v[188:191],a[252:255]
ds_read_b64_tr_b16 v[92:93],v2 offset:30944
ds_read_b64_tr_b16 v[94:95],v2 offset:36064
v_mfma_f32_16x16x32_bf16 a[252:255],v[88:91],v[172:175],a[252:255]
ds_read_b64_tr_b16 v[88:89],v2 offset:20704
v_mfma_f32_16x16x32_bf16 a[252:255],v[84:87],v[156:159],a[252:255]
buffer_load_dwordx4 a[168:171],v6,s[28:31],s89 offen offset:2048 sc1
v_mfma_f32_16x16x32_bf16 a[252:255],v[80:83],v[140:143],a[252:255]
ds_read_b64_tr_b16 v[80:81],v2 offset:224
ds_read_b64_tr_b16 v[82:83],v2 offset:5344
v_mfma_f32_16x16x32_bf16 a[0:3],v[96:99],v[32:35],a[0:3]
ds_read_b64_tr_b16 v[84:85],v2 offset:10464
v_mfma_f32_16x16x32_bf16 a[0:3],v[100:103],v[36:39],a[0:3]
ds_read_b64_tr_b16 v[86:87],v2 offset:15584
v_mfma_f32_16x16x32_bf16 a[0:3],v[104:107],v[40:43],a[0:3]
ds_read_b64_tr_b16 v[90:91],v2 offset:25824
v_mfma_f32_16x16x32_bf16 a[0:3],v[108:111],v[44:47],a[0:3]
v_mfma_f32_16x16x32_bf16 a[4:7],v[108:111],v[180:183],a[4:7]
buffer_store_dwordx4 v[188:191],v28,s[40:43],s82 offen offset:3072 sc1
v_mfma_f32_16x16x32_bf16 a[4:7],v[104:107],v[164:167],a[4:7]
s_add_u32 s82,s66,208896
s_mul_i32 s66,s64,320
v_mfma_f32_16x16x32_bf16 a[4:7],v[100:103],v[148:151],a[4:7]
v_mfma_f32_16x16x32_bf16 a[4:7],v[96:99],v[132:135],a[4:7]
v_mfma_f32_16x16x32_bf16 a[8:11],v[96:99],v[196:199],a[8:11]
v_mfma_f32_16x16x32_bf16 a[8:11],v[100:103],v[212:215],a[8:11]
v_mfma_f32_16x16x32_bf16 a[8:11],v[104:107],v[228:231],a[8:11]
v_mfma_f32_16x16x32_bf16 a[8:11],v[108:111],v[244:247],a[8:11]
buffer_load_dwordx4 a[184:187],v6,s[28:31],s90 offen offset:2048 sc1
v_mfma_f32_16x16x32_bf16 a[12:15],v[108:111],v[188:191],a[12:15]
ds_read_b64_tr_b16 v[108:109],v1 offset:30976
ds_read_b64_tr_b16 v[110:111],v1 offset:36096
v_mfma_f32_16x16x32_bf16 a[12:15],v[104:107],v[172:175],a[12:15]
ds_read_b64_tr_b16 v[104:105],v1 offset:20736
v_mfma_f32_16x16x32_bf16 a[12:15],v[100:103],v[156:159],a[12:15]
ds_read_b64_tr_b16 v[100:101],v1 offset:10496
v_mfma_f32_16x16x32_bf16 a[12:15],v[96:99],v[140:143],a[12:15]
)A"
R"A(s_waitcnt lgkmcnt(4)
ds_read_b64_tr_b16 v[96:97],v1 offset:256
v_mfma_f32_16x16x32_bf16 a[16:19],v[112:115],v[32:35],a[16:19]
ds_read_b64_tr_b16 v[98:99],v1 offset:5376
ds_read_b64_tr_b16 v[102:103],v1 offset:15616
v_mfma_f32_16x16x32_bf16 a[16:19],v[116:119],v[36:39],a[16:19]
ds_read_b64_tr_b16 v[106:107],v1 offset:25856
v_add_u32 v1,0xffff6000,v1
v_mfma_f32_16x16x32_bf16 a[16:19],v[120:123],v[40:43],a[16:19]
v_mfma_f32_16x16x32_bf16 a[16:19],v[124:127],v[44:47],a[16:19]
v_mfma_f32_16x16x32_bf16 a[20:23],v[124:127],v[180:183],a[20:23]
v_mfma_f32_16x16x32_bf16 a[20:23],v[120:123],v[164:167],a[20:23]
v_mfma_f32_16x16x32_bf16 a[20:23],v[116:119],v[148:151],a[20:23]
v_mfma_f32_16x16x32_bf16 a[20:23],v[112:115],v[132:135],a[20:23]
v_mfma_f32_16x16x32_bf16 a[24:27],v[112:115],v[196:199],a[24:27]
buffer_load_dwordx4 a[188:191],v28,s[28:31],s90 offen offset:3072 sc1
v_mfma_f32_16x16x32_bf16 a[24:27],v[116:119],v[212:215],a[24:27]
v_mfma_f32_16x16x32_bf16 a[24:27],v[120:123],v[228:231],a[24:27]
v_mfma_f32_16x16x32_bf16 a[24:27],v[124:127],v[244:247],a[24:27]
v_mfma_f32_16x16x32_bf16 a[28:31],v[124:127],v[188:191],a[28:31]
ds_read_b64_tr_b16 v[124:125],v2 offset:31008
ds_read_b64_tr_b16 v[126:127],v2 offset:36128
v_mfma_f32_16x16x32_bf16 a[28:31],v[120:123],v[172:175],a[28:31]
ds_read_b64_tr_b16 v[120:121],v2 offset:20768
v_mfma_f32_16x16x32_bf16 a[28:31],v[116:119],v[156:159],a[28:31]
ds_read_b64_tr_b16 v[116:117],v2 offset:10528
v_mfma_f32_16x16x32_bf16 a[28:31],v[112:115],v[140:143],a[28:31]
ds_read_b64_tr_b16 v[112:113],v2 offset:288
v_mfma_f32_16x16x32_bf16 v[128:131],v[64:67],v[32:35],v[128:131]
ds_read_b64_tr_b16 v[114:115],v2 offset:5408
ds_read_b64_tr_b16 v[118:119],v2 offset:15648
v_mfma_f32_16x16x32_bf16 v[128:131],v[68:71],v[36:39],v[128:131]
ds_read_b64_tr_b16 v[122:123],v2 offset:25888
v_add_u32 v2,0xffff6000,v2
v_mfma_f32_16x16x32_bf16 v[128:131],v[72:75],v[40:43],v[128:131]
v_mfma_f32_16x16x32_bf16 v[128:131],v[76:79],v[44:47],v[128:131]
v_mfma_f32_16x16x32_bf16 v[136:139],v[76:79],v[180:183],v[136:139]
v_mfma_f32_16x16x32_bf16 v[136:139],v[72:75],v[164:167],v[136:139]
buffer_load_dwordx4 a[172:175],v28,s[28:31],s89 offen offset:3072 sc1
v_mfma_f32_16x16x32_bf16 v[136:139],v[68:71],v[148:151],v[136:139]
v_mfma_f32_16x16x32_bf16 v[136:139],v[64:67],v[132:135],v[136:139]
v_mfma_f32_16x16x32_bf16 v[144:147],v[64:67],v[196:199],v[144:147]
v_mfma_f32_16x16x32_bf16 v[144:147],v[68:71],v[212:215],v[144:147]
v_mfma_f32_16x16x32_bf16 v[144:147],v[72:75],v[228:231],v[144:147]
v_mfma_f32_16x16x32_bf16 v[144:147],v[76:79],v[244:247],v[144:147]
v_mfma_f32_16x16x32_bf16 v[152:155],v[76:79],v[188:191],v[152:155]
v_mfma_f32_16x16x32_bf16 v[152:155],v[72:75],v[172:175],v[152:155]
v_mfma_f32_16x16x32_bf16 v[152:155],v[68:71],v[156:159],v[152:155]
v_mfma_f32_16x16x32_bf16 v[152:155],v[64:67],v[140:143],v[152:155]
s_mov_b64 exec,0
s_cmp_eq_u32 s60,0
s_cbranch_scc1 L_pd21_%=
s_cmp_eq_u32 s60,1
s_cbranch_scc1 L_pd22_%=
s_cmp_eq_u32 s60,2
s_cbranch_scc1 L_pd23_%=
s_branch L_trtg2S_%=
L_trtg2S_%=:
s_mov_b64 exec,-1
s_waitcnt lgkmcnt(0)
s_barrier
v_mfma_f32_16x16x32_bf16 v[160:163],v[80:83],v[32:35],v[160:163]
buffer_load_dwordx4 v24,s[48:51],s83 offen lds
v_mfma_f32_16x16x32_bf16 v[160:163],v[84:87],v[36:39],v[160:163]
ds_read_b64_tr_b16 v[64:65],v1
ds_read_b64_tr_b16 v[66:67],v1 offset:5120
v_mfma_f32_16x16x32_bf16 v[160:163],v[88:91],v[40:43],v[160:163]
ds_read_b64_tr_b16 v[68:69],v1 offset:10240
v_mfma_f32_16x16x32_bf16 v[160:163],v[92:95],v[44:47],v[160:163]
ds_read_b64_tr_b16 v[70:71],v1 offset:15360
v_mfma_f32_16x16x32_bf16 v[168:171],v[92:95],v[180:183],v[168:171]
ds_read_b64_tr_b16 v[72:73],v1 offset:20480
v_mfma_f32_16x16x32_bf16 v[168:171],v[88:91],v[164:167],v[168:171]
buffer_load_dwordx4 a[156:159],v28,s[28:31],s88 offen offset:3072 sc1
v_mfma_f32_16x16x32_bf16 v[168:171],v[84:87],v[148:151],v[168:171]
ds_read_b64_tr_b16 v[74:75],v1 offset:25600
ds_read_b64_tr_b16 v[76:77],v1 offset:30720
v_mfma_f32_16x16x32_bf16 v[168:171],v[80:83],v[132:135],v[168:171]
ds_read_b64_tr_b16 v[78:79],v1 offset:35840
v_mfma_f32_16x16x32_bf16 v[176:179],v[80:83],v[196:199],v[176:179]
v_mfma_f32_16x16x32_bf16 v[176:179],v[84:87],v[212:215],v[176:179]
v_mfma_f32_16x16x32_bf16 v[176:179],v[88:91],v[228:231],v[176:179]
v_mfma_f32_16x16x32_bf16 v[176:179],v[92:95],v[244:247],v[176:179]
buffer_load_dwordx4 v25,s[48:51],s83 offen offset:1024 lds
v_mfma_f32_16x16x32_bf16 v[184:187],v[92:95],v[188:191],v[184:187]
ds_read_b64_tr_b16 v[92:93],v2 offset:30752
ds_read_b64_tr_b16 v[94:95],v2 offset:35872
v_mfma_f32_16x16x32_bf16 v[184:187],v[88:91],v[172:175],v[184:187]
ds_read_b64_tr_b16 v[88:89],v2 offset:20512
v_mfma_f32_16x16x32_bf16 v[184:187],v[84:87],v[156:159],v[184:187]
ds_read_b64_tr_b16 v[84:85],v2 offset:10272
v_mfma_f32_16x16x32_bf16 v[184:187],v[80:83],v[140:143],v[184:187]
ds_read_b64_tr_b16 v[80:81],v2 offset:32
v_mfma_f32_16x16x32_bf16 v[192:195],v[96:99],v[32:35],v[192:195]
buffer_load_dwordx4 a[140:143],v28,s[28:31],s87 offen offset:3072 sc1
v_mfma_f32_16x16x32_bf16 v[192:195],v[100:103],v[36:39],v[192:195]
s_add_u32 s87,s64,2
s_and_b32 s87,s87,3
s_lshl_b32 s87,s87,1
v_mfma_f32_16x16x32_bf16 v[192:195],v[104:107],v[40:43],v[192:195]
s_lshl_b32 s87,s87,16
s_add_u32 s88,s87,4096
s_add_u32 s89,s87,8192
v_mfma_f32_16x16x32_bf16 v[192:195],v[108:111],v[44:47],v[192:195]
s_add_u32 s90,s87,12288
ds_read_b64_tr_b16 v[82:83],v2 offset:5152
ds_read_b64_tr_b16 v[86:87],v2 offset:15392
v_mfma_f32_16x16x32_bf16 v[200:203],v[108:111],v[180:183],v[200:203]
ds_read_b64_tr_b16 v[90:91],v2 offset:25632
v_mfma_f32_16x16x32_bf16 v[200:203],v[104:107],v[164:167],v[200:203]
buffer_load_dwordx4 a[68:71],v6,s[28:31],s87 offen sc1
v_mfma_f32_16x16x32_bf16 v[200:203],v[100:103],v[148:151],v[200:203]
v_mfma_f32_16x16x32_bf16 v[200:203],v[96:99],v[132:135],v[200:203]
v_mfma_f32_16x16x32_bf16 v[208:211],v[96:99],v[196:199],v[208:211]
v_mfma_f32_16x16x32_bf16 v[208:211],v[100:103],v[212:215],v[208:211]
v_mfma_f32_16x16x32_bf16 v[208:211],v[104:107],v[228:231],v[208:211]
buffer_load_dwordx4 a[76:79],v6,s[28:31],s88 offen sc1
v_mfma_f32_16x16x32_bf16 v[208:211],v[108:111],v[244:247],v[208:211]
v_mfma_f32_16x16x32_bf16 v[216:219],v[108:111],v[188:191],v[216:219]
ds_read_b64_tr_b16 v[108:109],v1 offset:30784
ds_read_b64_tr_b16 v[110:111],v1 offset:35904
v_mfma_f32_16x16x32_bf16 v[216:219],v[104:107],v[172:175],v[216:219]
ds_read_b64_tr_b16 v[104:105],v1 offset:20544
v_mfma_f32_16x16x32_bf16 v[216:219],v[100:103],v[156:159],v[216:219]
ds_read_b64_tr_b16 v[100:101],v1 offset:10304
v_mfma_f32_16x16x32_bf16 v[216:219],v[96:99],v[140:143],v[216:219]
buffer_load_dwordx4 a[84:87],v6,s[28:31],s89 offen sc1
v_mfma_f32_16x16x32_bf16 v[224:227],v[112:115],v[32:35],v[224:227]
ds_read_b64_tr_b16 v[96:97],v1 offset:64
ds_read_b64_tr_b16 v[98:99],v1 offset:5184
v_mfma_f32_16x16x32_bf16 v[224:227],v[116:119],v[36:39],v[224:227]
ds_read_b64_tr_b16 v[102:103],v1 offset:15424
v_mfma_f32_16x16x32_bf16 v[224:227],v[120:123],v[40:43],v[224:227]
ds_read_b64_tr_b16 v[106:107],v1 offset:25664
v_mfma_f32_16x16x32_bf16 v[224:227],v[124:127],v[44:47],v[224:227]
v_mfma_f32_16x16x32_bf16 v[232:235],v[124:127],v[180:183],v[232:235]
buffer_load_dwordx4 a[92:95],v6,s[28:31],s90 offen sc1
v_mfma_f32_16x16x32_bf16 v[232:235],v[120:123],v[164:167],v[232:235]
v_mfma_f32_16x16x32_bf16 v[232:235],v[116:119],v[148:151],v[232:235]
v_mfma_f32_16x16x32_bf16 v[232:235],v[112:115],v[132:135],v[232:235]
v_mfma_f32_16x16x32_bf16 v[240:243],v[112:115],v[196:199],v[240:243]
v_mfma_f32_16x16x32_bf16 v[240:243],v[116:119],v[212:215],v[240:243]
buffer_load_dwordx4 a[124:127],v6,s[28:31],s90 offen offset:1024 sc1
v_mfma_f32_16x16x32_bf16 v[240:243],v[120:123],v[228:231],v[240:243]
v_mfma_f32_16x16x32_bf16 v[240:243],v[124:127],v[244:247],v[240:243]
v_mfma_f32_16x16x32_bf16 v[248:251],v[124:127],v[188:191],v[248:251]
ds_read_b64_tr_b16 v[124:125],v2 offset:30816
ds_read_b64_tr_b16 v[126:127],v2 offset:35936
v_mfma_f32_16x16x32_bf16 v[248:251],v[120:123],v[172:175],v[248:251]
ds_read_b64_tr_b16 v[120:121],v2 offset:20576
v_mfma_f32_16x16x32_bf16 v[248:251],v[116:119],v[156:159],v[248:251]
buffer_load_dwordx4 a[116:119],v6,s[28:31],s89 offen offset:1024 sc1
v_mfma_f32_16x16x32_bf16 v[248:251],v[112:115],v[140:143],v[248:251]
s_waitcnt vmcnt(31) lgkmcnt(3)
ds_read_b64_tr_b16 v[112:113],v2 offset:96
ds_read_b64_tr_b16 v[114:115],v2 offset:5216
v_mfma_f32_16x16x32_bf16 a[192:195],v[64:67],a[128:131],a[192:195]
ds_read_b64_tr_b16 v[116:117],v2 offset:10336
v_mfma_f32_16x16x32_bf16 a[192:195],v[68:71],a[144:147],a[192:195]
s_waitcnt vmcnt(29)
ds_read_b64_tr_b16 v[118:119],v2 offset:15456
v_mfma_f32_16x16x32_bf16 a[192:195],v[72:75],a[160:163],a[192:195]
s_waitcnt vmcnt(25)
ds_read_b64_tr_b16 v[122:123],v2 offset:25696
v_mfma_f32_16x16x32_bf16 a[192:195],v[76:79],a[176:179],a[192:195]
buffer_load_dwordx4 a[108:111],v6,s[28:31],s88 offen offset:1024 sc1
v_mfma_f32_16x16x32_bf16 a[196:199],v[76:79],a[180:183],a[196:199]
s_waitcnt vmcnt(24)
v_mfma_f32_16x16x32_bf16 a[196:199],v[72:75],a[164:167],a[196:199]
s_waitcnt vmcnt(22)
v_mfma_f32_16x16x32_bf16 a[196:199],v[68:71],a[148:151],a[196:199]
s_waitcnt vmcnt(19)
v_mfma_f32_16x16x32_bf16 a[196:199],v[64:67],a[132:135],a[196:199]
s_waitcnt vmcnt(17)
v_mfma_f32_16x16x32_bf16 a[200:203],v[64:67],a[136:139],a[200:203]
buffer_load_dwordx4 a[100:103],v6,s[28:31],s87 offen offset:1024 sc1
v_mfma_f32_16x16x32_bf16 a[200:203],v[68:71],a[152:155],a[200:203]
s_waitcnt vmcnt(16)
v_mfma_f32_16x16x32_bf16 a[200:203],v[72:75],a[168:171],a[200:203]
s_waitcnt vmcnt(14)
v_mfma_f32_16x16x32_bf16 a[200:203],v[76:79],a[184:187],a[200:203]
s_waitcnt vmcnt(13)
v_mfma_f32_16x16x32_bf16 a[204:207],v[76:79],a[188:191],a[204:207]
s_waitcnt vmcnt(10)
ds_read_b64_tr_b16 v[76:77],v1 offset:30848
ds_read_b64_tr_b16 v[78:79],v1 offset:35968
v_mfma_f32_16x16x32_bf16 a[204:207],v[72:75],a[172:175],a[204:207]
buffer_load_dwordx4 a[64:67],v6,s[28:31],s87 offen offset:2048 sc1
v_mfma_f32_16x16x32_bf16 a[204:207],v[68:71],a[156:159],a[204:207]
s_waitcnt vmcnt(9)
ds_read_b64_tr_b16 v[68:69],v1 offset:10368
ds_read_b64_tr_b16 v[70:71],v1 offset:15488
v_mfma_f32_16x16x32_bf16 a[204:207],v[64:67],a[140:143],a[204:207]
ds_read_b64_tr_b16 v[64:65],v1 offset:128
v_mfma_f32_16x16x32_bf16 a[208:211],v[80:83],a[128:131],a[208:211]
ds_read_b64_tr_b16 v[66:67],v1 offset:5248
v_mfma_f32_16x16x32_bf16 a[208:211],v[84:87],a[144:147],a[208:211]
ds_read_b64_tr_b16 v[72:73],v1 offset:20608
v_mfma_f32_16x16x32_bf16 a[208:211],v[88:91],a[160:163],a[208:211]
ds_read_b64_tr_b16 v[74:75],v1 offset:25728
v_mfma_f32_16x16x32_bf16 a[208:211],v[92:95],a[176:179],a[208:211]
buffer_load_dwordx4 v26,s[48:51],s83 offen offset:2048 lds
v_mfma_f32_16x16x32_bf16 a[212:215],v[92:95],a[180:183],a[212:215]
v_mfma_f32_16x16x32_bf16 a[212:215],v[88:91],a[164:167],a[212:215]
v_mfma_f32_16x16x32_bf16 a[212:215],v[84:87],a[148:151],a[212:215]
v_mfma_f32_16x16x32_bf16 a[212:215],v[80:83],a[132:135],a[212:215]
v_mfma_f32_16x16x32_bf16 a[216:219],v[80:83],a[136:139],a[216:219]
buffer_load_dwordx4 a[72:75],v6,s[28:31],s88 offen offset:2048 sc1
v_mfma_f32_16x16x32_bf16 a[216:219],v[84:87],a[152:155],a[216:219]
v_mfma_f32_16x16x32_bf16 a[216:219],v[88:91],a[168:171],a[216:219]
v_mfma_f32_16x16x32_bf16 a[216:219],v[92:95],a[184:187],a[216:219]
v_mfma_f32_16x16x32_bf16 a[220:223],v[92:95],a[188:191],a[220:223]
ds_read_b64_tr_b16 v[92:93],v2 offset:30880
ds_read_b64_tr_b16 v[94:95],v2 offset:36000
v_mfma_f32_16x16x32_bf16 a[220:223],v[88:91],a[172:175],a[220:223]
ds_read_b64_tr_b16 v[88:89],v2 offset:20640
v_mfma_f32_16x16x32_bf16 a[220:223],v[84:87],a[156:159],a[220:223]
buffer_load_dwordx4 v27,s[48:51],s83 offen offset:3072 lds
v_mfma_f32_16x16x32_bf16 a[220:223],v[80:83],a[140:143],a[220:223]
s_add_u32 m0,s86,126976
ds_read_b64_tr_b16 v[80:81],v2 offset:160
ds_read_b64_tr_b16 v[82:83],v2 offset:5280
s_barrier
v_mfma_f32_16x16x32_bf16 a[224:227],v[96:99],a[128:131],a[224:227]
ds_read_b64_tr_b16 v[84:85],v2 offset:10400
v_mfma_f32_16x16x32_bf16 a[224:227],v[100:103],a[144:147],a[224:227]
)A"
R"A(ds_read_b64_tr_b16 v[86:87],v2 offset:15520
v_mfma_f32_16x16x32_bf16 a[224:227],v[104:107],a[160:163],a[224:227]
buffer_store_dwordx2 v[8:9],v10,s[44:47],s18 offen offset:1280 sc1
v_mfma_f32_16x16x32_bf16 a[224:227],v[108:111],a[176:179],a[224:227]
ds_read_b64_tr_b16 v[90:91],v2 offset:25760
v_mfma_f32_16x16x32_bf16 a[228:231],v[108:111],a[180:183],a[228:231]
v_mfma_f32_16x16x32_bf16 a[228:231],v[104:107],a[164:167],a[228:231]
v_mfma_f32_16x16x32_bf16 a[228:231],v[100:103],a[148:151],a[228:231]
v_mfma_f32_16x16x32_bf16 a[228:231],v[96:99],a[132:135],a[228:231]
buffer_load_dwordx4 a[80:83],v6,s[28:31],s89 offen offset:2048 sc1
v_mfma_f32_16x16x32_bf16 a[232:235],v[96:99],a[136:139],a[232:235]
v_mfma_f32_16x16x32_bf16 a[232:235],v[100:103],a[152:155],a[232:235]
v_mfma_f32_16x16x32_bf16 a[232:235],v[104:107],a[168:171],a[232:235]
v_mfma_f32_16x16x32_bf16 a[232:235],v[108:111],a[184:187],a[232:235]
v_mfma_f32_16x16x32_bf16 a[236:239],v[108:111],a[188:191],a[236:239]
s_waitcnt lgkmcnt(0)
ds_read_b64_tr_b16 v[108:109],v1 offset:30912
ds_read_b64_tr_b16 v[110:111],v1 offset:36032
v_mfma_f32_16x16x32_bf16 a[236:239],v[104:107],a[172:175],a[236:239]
buffer_load_dwordx4 v7,s[48:51],s83 offen lds
v_mfma_f32_16x16x32_bf16 a[236:239],v[100:103],a[156:159],a[236:239]
s_add_u32 m0,s86,128000
ds_read_b64_tr_b16 v[100:101],v1 offset:10432
ds_read_b64_tr_b16 v[102:103],v1 offset:15552
v_mfma_f32_16x16x32_bf16 a[236:239],v[96:99],a[140:143],a[236:239]
s_add_u32 s83,s64,1
s_and_b32 s83,s83,3
ds_read_b64_tr_b16 v[96:97],v1 offset:192
v_mfma_f32_16x16x32_bf16 a[240:243],v[112:115],a[128:131],a[240:243]
s_lshl_b32 s83,s83,1
s_or_b32 s83,s83,1
ds_read_b64_tr_b16 v[98:99],v1 offset:5312
v_mfma_f32_16x16x32_bf16 a[240:243],v[116:119],a[144:147],a[240:243]
s_mul_i32 s83,s83,163840
s_add_u32 s83,s83,s85
ds_read_b64_tr_b16 v[104:105],v1 offset:20672
v_mfma_f32_16x16x32_bf16 a[240:243],v[120:123],a[160:163],a[240:243]
buffer_load_dwordx4 a[88:91],v6,s[28:31],s90 offen offset:2048 sc1
v_mfma_f32_16x16x32_bf16 a[240:243],v[124:127],a[176:179],a[240:243]
ds_read_b64_tr_b16 v[106:107],v1 offset:25792
v_mfma_f32_16x16x32_bf16 a[244:247],v[124:127],a[180:183],a[244:247]
v_mfma_f32_16x16x32_bf16 a[244:247],v[120:123],a[164:167],a[244:247]
v_mfma_f32_16x16x32_bf16 a[244:247],v[116:119],a[148:151],a[244:247]
v_mfma_f32_16x16x32_bf16 a[244:247],v[112:115],a[132:135],a[244:247]
v_mfma_f32_16x16x32_bf16 a[248:251],v[112:115],a[136:139],a[248:251]
buffer_load_dwordx4 v24,s[48:51],s84 offen lds
v_mfma_f32_16x16x32_bf16 a[248:251],v[116:119],a[152:155],a[248:251]
v_mfma_f32_16x16x32_bf16 a[248:251],v[120:123],a[168:171],a[248:251]
v_mfma_f32_16x16x32_bf16 a[248:251],v[124:127],a[184:187],a[248:251]
v_mfma_f32_16x16x32_bf16 a[252:255],v[124:127],a[188:191],a[252:255]
ds_read_b64_tr_b16 v[124:125],v2 offset:30944
ds_read_b64_tr_b16 v[126:127],v2 offset:36064
v_mfma_f32_16x16x32_bf16 a[252:255],v[120:123],a[172:175],a[252:255]
buffer_load_dwordx4 a[120:123],v28,s[28:31],s90 offen offset:3072 sc1
v_mfma_f32_16x16x32_bf16 a[252:255],v[116:119],a[156:159],a[252:255]
ds_read_b64_tr_b16 v[116:117],v2 offset:10464
ds_read_b64_tr_b16 v[118:119],v2 offset:15584
v_mfma_f32_16x16x32_bf16 a[252:255],v[112:115],a[140:143],a[252:255]
ds_read_b64_tr_b16 v[112:113],v2 offset:224
v_mfma_f32_16x16x32_bf16 a[0:3],v[64:67],a[128:131],a[0:3]
ds_read_b64_tr_b16 v[114:115],v2 offset:5344
v_mfma_f32_16x16x32_bf16 a[0:3],v[68:71],a[144:147],a[0:3]
ds_read_b64_tr_b16 v[120:121],v2 offset:20704
v_mfma_f32_16x16x32_bf16 a[0:3],v[72:75],a[160:163],a[0:3]
ds_read_b64_tr_b16 v[122:123],v2 offset:25824
v_mfma_f32_16x16x32_bf16 a[0:3],v[76:79],a[176:179],a[0:3]
buffer_load_dwordx4 v25,s[48:51],s84 offen offset:1024 lds
v_mfma_f32_16x16x32_bf16 a[4:7],v[76:79],a[180:183],a[4:7]
v_mfma_f32_16x16x32_bf16 a[4:7],v[72:75],a[164:167],a[4:7]
v_mfma_f32_16x16x32_bf16 a[4:7],v[68:71],a[148:151],a[4:7]
v_mfma_f32_16x16x32_bf16 a[4:7],v[64:67],a[132:135],a[4:7]
v_mfma_f32_16x16x32_bf16 a[8:11],v[64:67],a[136:139],a[8:11]
buffer_load_dwordx4 a[112:115],v28,s[28:31],s89 offen offset:3072 sc1
v_mfma_f32_16x16x32_bf16 a[8:11],v[68:71],a[152:155],a[8:11]
v_mfma_f32_16x16x32_bf16 a[8:11],v[72:75],a[168:171],a[8:11]
v_mfma_f32_16x16x32_bf16 a[8:11],v[76:79],a[184:187],a[8:11]
v_mfma_f32_16x16x32_bf16 a[12:15],v[76:79],a[188:191],a[12:15]
ds_read_b64_tr_b16 v[76:77],v1 offset:30976
ds_read_b64_tr_b16 v[78:79],v1 offset:36096
v_mfma_f32_16x16x32_bf16 a[12:15],v[72:75],a[172:175],a[12:15]
buffer_load_dwordx4 a[104:107],v28,s[28:31],s88 offen offset:3072 sc1
v_mfma_f32_16x16x32_bf16 a[12:15],v[68:71],a[156:159],a[12:15]
ds_read_b64_tr_b16 v[68:69],v1 offset:10496
ds_read_b64_tr_b16 v[70:71],v1 offset:15616
v_mfma_f32_16x16x32_bf16 a[12:15],v[64:67],a[140:143],a[12:15]
ds_read_b64_tr_b16 v[64:65],v1 offset:256
v_mfma_f32_16x16x32_bf16 a[16:19],v[80:83],a[128:131],a[16:19]
ds_read_b64_tr_b16 v[66:67],v1 offset:5376
v_mfma_f32_16x16x32_bf16 a[16:19],v[84:87],a[144:147],a[16:19]
ds_read_b64_tr_b16 v[72:73],v1 offset:20736
v_mfma_f32_16x16x32_bf16 a[16:19],v[88:91],a[160:163],a[16:19]
ds_read_b64_tr_b16 v[74:75],v1 offset:25856
v_add_u32 v1,0xffff6000,v1
v_mfma_f32_16x16x32_bf16 a[16:19],v[92:95],a[176:179],a[16:19]
buffer_load_dwordx4 v26,s[48:51],s84 offen offset:2048 lds
v_mfma_f32_16x16x32_bf16 a[20:23],v[92:95],a[180:183],a[20:23]
v_mfma_f32_16x16x32_bf16 a[20:23],v[88:91],a[164:167],a[20:23]
v_mfma_f32_16x16x32_bf16 a[20:23],v[84:87],a[148:151],a[20:23]
v_mfma_f32_16x16x32_bf16 a[20:23],v[80:83],a[132:135],a[20:23]
v_mfma_f32_16x16x32_bf16 a[24:27],v[80:83],a[136:139],a[24:27]
buffer_load_dwordx4 a[96:99],v28,s[28:31],s87 offen offset:3072 sc1
v_mfma_f32_16x16x32_bf16 a[24:27],v[84:87],a[152:155],a[24:27]
s_add_u32 s87,s64,3
s_and_b32 s87,s87,3
s_lshl_b32 s87,s87,1
v_mfma_f32_16x16x32_bf16 a[24:27],v[88:91],a[168:171],a[24:27]
s_lshl_b32 s87,s87,16
s_add_u32 s88,s87,4096
s_add_u32 s89,s87,8192
v_mfma_f32_16x16x32_bf16 a[24:27],v[92:95],a[184:187],a[24:27]
s_add_u32 s90,s87,12288
v_mfma_f32_16x16x32_bf16 a[28:31],v[92:95],a[188:191],a[28:31]
ds_read_b64_tr_b16 v[92:93],v2 offset:31008
ds_read_b64_tr_b16 v[94:95],v2 offset:36128
v_mfma_f32_16x16x32_bf16 a[28:31],v[88:91],a[172:175],a[28:31]
s_waitcnt lgkmcnt(2)
ds_read_b64_tr_b16 v[88:89],v2 offset:20768
v_mfma_f32_16x16x32_bf16 a[28:31],v[84:87],a[156:159],a[28:31]
buffer_load_dwordx4 v27,s[48:51],s84 offen offset:3072 lds
v_mfma_f32_16x16x32_bf16 a[28:31],v[80:83],a[140:143],a[28:31]
s_add_u32 m0,s86,132096
ds_read_b64_tr_b16 v[80:81],v2 offset:288
ds_read_b64_tr_b16 v[82:83],v2 offset:5408
v_mfma_f32_16x16x32_bf16 v[128:131],v[96:99],a[128:131],v[128:131]
ds_read_b64_tr_b16 v[84:85],v2 offset:10528
v_mfma_f32_16x16x32_bf16 v[128:131],v[100:103],a[144:147],v[128:131]
ds_read_b64_tr_b16 v[86:87],v2 offset:15648
v_mfma_f32_16x16x32_bf16 v[128:131],v[104:107],a[160:163],v[128:131]
ds_read_b64_tr_b16 v[90:91],v2 offset:25888
v_add_u32 v2,0xffff6000,v2
v_mfma_f32_16x16x32_bf16 v[128:131],v[108:111],a[176:179],v[128:131]
v_mfma_f32_16x16x32_bf16 v[136:139],v[108:111],a[180:183],v[136:139]
buffer_load_dwordx4 v7,s[48:51],s84 offen lds
v_mfma_f32_16x16x32_bf16 v[136:139],v[104:107],a[164:167],v[136:139]
s_add_u32 m0,s86,81920
s_add_u32 s84,s83,20480
v_mfma_f32_16x16x32_bf16 v[136:139],v[100:103],a[148:151],v[136:139]
v_mfma_f32_16x16x32_bf16 v[136:139],v[96:99],a[132:135],v[136:139]
v_mfma_f32_16x16x32_bf16 v[144:147],v[96:99],a[136:139],v[144:147]
v_mfma_f32_16x16x32_bf16 v[144:147],v[100:103],a[152:155],v[144:147]
v_mfma_f32_16x16x32_bf16 v[144:147],v[104:107],a[168:171],v[144:147]
buffer_load_dwordx4 v24,s[48:51],s83 offen lds
v_mfma_f32_16x16x32_bf16 v[144:147],v[108:111],a[184:187],v[144:147]
v_mfma_f32_16x16x32_bf16 v[152:155],v[108:111],a[188:191],v[152:155]
ds_read_b64_tr_b16 v[108:109],v1 offset:30720
ds_read_b64_tr_b16 v[110:111],v1 offset:35840
v_mfma_f32_16x16x32_bf16 v[152:155],v[104:107],a[172:175],v[152:155]
ds_read_b64_tr_b16 v[104:105],v1 offset:20480
v_mfma_f32_16x16x32_bf16 v[152:155],v[100:103],a[156:159],v[152:155]
ds_read_b64_tr_b16 v[100:101],v1 offset:10240
v_mfma_f32_16x16x32_bf16 v[152:155],v[96:99],a[140:143],v[152:155]
ds_read_b64_tr_b16 v[96:97],v1
v_mfma_f32_16x16x32_bf16 v[160:163],v[112:115],a[128:131],v[160:163]
buffer_load_dwordx4 v25,s[48:51],s83 offen offset:1024 lds
v_mfma_f32_16x16x32_bf16 v[160:163],v[116:119],a[144:147],v[160:163]
ds_read_b64_tr_b16 v[98:99],v1 offset:5120
ds_read_b64_tr_b16 v[102:103],v1 offset:15360
v_mfma_f32_16x16x32_bf16 v[160:163],v[120:123],a[160:163],v[160:163]
ds_read_b64_tr_b16 v[106:107],v1 offset:25600
v_mfma_f32_16x16x32_bf16 v[160:163],v[124:127],a[176:179],v[160:163]
v_mfma_f32_16x16x32_bf16 v[168:171],v[124:127],a[180:183],v[168:171]
v_mfma_f32_16x16x32_bf16 v[168:171],v[120:123],a[164:167],v[168:171]
v_mfma_f32_16x16x32_bf16 v[168:171],v[116:119],a[148:151],v[168:171]
buffer_load_dwordx4 v26,s[48:51],s83 offen offset:2048 lds
v_mfma_f32_16x16x32_bf16 v[168:171],v[112:115],a[132:135],v[168:171]
v_mfma_f32_16x16x32_bf16 v[176:179],v[112:115],a[136:139],v[176:179]
v_mfma_f32_16x16x32_bf16 v[176:179],v[116:119],a[152:155],v[176:179]
v_mfma_f32_16x16x32_bf16 v[176:179],v[120:123],a[168:171],v[176:179]
v_mfma_f32_16x16x32_bf16 v[176:179],v[124:127],a[184:187],v[176:179]
v_mfma_f32_16x16x32_bf16 v[184:187],v[124:127],a[188:191],v[184:187]
buffer_load_dwordx4 v27,s[48:51],s83 offen offset:3072 lds
v_mfma_f32_16x16x32_bf16 v[184:187],v[120:123],a[172:175],v[184:187]
s_add_u32 m0,s86,86016
ds_read_b64_tr_b16 v[120:121],v2 offset:20512
ds_read_b64_tr_b16 v[122:123],v2 offset:25632
v_mfma_f32_16x16x32_bf16 v[184:187],v[116:119],a[156:159],v[184:187]
ds_read_b64_tr_b16 v[116:117],v2 offset:10272
v_mfma_f32_16x16x32_bf16 v[184:187],v[112:115],a[140:143],v[184:187]
ds_read_b64_tr_b16 v[112:113],v2 offset:32
L_pd21_%=:
s_cbranch_execz L_trtg2S_%=
v_mfma_f32_16x16x32_bf16 v[192:195],v[64:67],a[128:131],v[192:195]
ds_read_b64_tr_b16 v[114:115],v2 offset:5152
v_mfma_f32_16x16x32_bf16 v[192:195],v[68:71],a[144:147],v[192:195]
ds_read_b64_tr_b16 v[118:119],v2 offset:15392
ds_read_b64_tr_b16 v[124:125],v2 offset:30752
v_mfma_f32_16x16x32_bf16 v[192:195],v[72:75],a[160:163],v[192:195]
buffer_load_dwordx4 v7,s[48:51],s83 offen lds
v_mfma_f32_16x16x32_bf16 v[192:195],v[76:79],a[176:179],v[192:195]
s_add_u32 m0,s86,87040
s_add_u32 s83,s64,2
ds_read_b64_tr_b16 v[126:127],v2 offset:35872
v_mfma_f32_16x16x32_bf16 v[200:203],v[76:79],a[180:183],v[200:203]
s_and_b32 s83,s83,3
s_lshl_b32 s83,s83,1
s_or_b32 s83,s83,1
v_mfma_f32_16x16x32_bf16 v[200:203],v[72:75],a[164:167],v[200:203]
s_mul_i32 s83,s83,163840
s_add_u32 s83,s83,s85
v_mfma_f32_16x16x32_bf16 v[200:203],v[68:71],a[148:151],v[200:203]
v_mfma_f32_16x16x32_bf16 v[200:203],v[64:67],a[132:135],v[200:203]
v_mfma_f32_16x16x32_bf16 v[208:211],v[64:67],a[136:139],v[208:211]
buffer_load_dwordx4 v24,s[48:51],s84 offen lds
v_mfma_f32_16x16x32_bf16 v[208:211],v[68:71],a[152:155],v[208:211]
v_mfma_f32_16x16x32_bf16 v[208:211],v[72:75],a[168:171],v[208:211]
v_mfma_f32_16x16x32_bf16 v[208:211],v[76:79],a[184:187],v[208:211]
v_mfma_f32_16x16x32_bf16 v[216:219],v[76:79],a[188:191],v[216:219]
ds_read_b64_tr_b16 v[76:77],v1 offset:30784
ds_read_b64_tr_b16 v[78:79],v1 offset:35904
v_mfma_f32_16x16x32_bf16 v[216:219],v[72:75],a[172:175],v[216:219]
ds_read_b64_tr_b16 v[72:73],v1 offset:20544
v_mfma_f32_16x16x32_bf16 v[216:219],v[68:71],a[156:159],v[216:219]
buffer_load_dwordx2 v[4:5],v3,s[44:47],s91 offen offset:1280 sc1
v_mfma_f32_16x16x32_bf16 v[216:219],v[64:67],a[140:143],v[216:219]
s_waitcnt lgkmcnt(3)
ds_read_b64_tr_b16 v[64:65],v1 offset:64
ds_read_b64_tr_b16 v[66:67],v1 offset:5184
v_mfma_f32_16x16x32_bf16 v[224:227],v[80:83],a[128:131],v[224:227]
ds_read_b64_tr_b16 v[68:69],v1 offset:10304
v_mfma_f32_16x16x32_bf16 v[224:227],v[84:87],a[144:147],v[224:227]
ds_read_b64_tr_b16 v[70:71],v1 offset:15424
v_mfma_f32_16x16x32_bf16 v[224:227],v[88:91],a[160:163],v[224:227]
ds_read_b64_tr_b16 v[74:75],v1 offset:25664
v_mfma_f32_16x16x32_bf16 v[224:227],v[92:95],a[176:179],v[224:227]
)A"
R"A(v_mfma_f32_16x16x32_bf16 v[232:235],v[92:95],a[180:183],v[232:235]
buffer_load_dwordx4 v25,s[48:51],s84 offen offset:1024 lds
v_mfma_f32_16x16x32_bf16 v[232:235],v[88:91],a[164:167],v[232:235]
v_mfma_f32_16x16x32_bf16 v[232:235],v[84:87],a[148:151],v[232:235]
v_mfma_f32_16x16x32_bf16 v[232:235],v[80:83],a[132:135],v[232:235]
v_mfma_f32_16x16x32_bf16 v[240:243],v[80:83],a[136:139],v[240:243]
v_mfma_f32_16x16x32_bf16 v[240:243],v[84:87],a[152:155],v[240:243]
v_mfma_f32_16x16x32_bf16 v[240:243],v[88:91],a[168:171],v[240:243]
buffer_load_dwordx4 v26,s[48:51],s84 offen offset:2048 lds
v_mfma_f32_16x16x32_bf16 v[240:243],v[92:95],a[184:187],v[240:243]
v_mfma_f32_16x16x32_bf16 v[248:251],v[92:95],a[188:191],v[248:251]
ds_read_b64_tr_b16 v[92:93],v2 offset:30816
ds_read_b64_tr_b16 v[94:95],v2 offset:35936
v_mfma_f32_16x16x32_bf16 v[248:251],v[88:91],a[172:175],v[248:251]
ds_read_b64_tr_b16 v[88:89],v2 offset:20576
v_mfma_f32_16x16x32_bf16 v[248:251],v[84:87],a[156:159],v[248:251]
ds_read_b64_tr_b16 v[84:85],v2 offset:10336
v_mfma_f32_16x16x32_bf16 v[248:251],v[80:83],a[140:143],v[248:251]
s_waitcnt vmcnt(25)
ds_read_b64_tr_b16 v[80:81],v2 offset:96
v_mfma_f32_16x16x32_bf16 a[192:195],v[96:99],a[68:71],a[192:195]
buffer_load_dwordx4 v27,s[48:51],s84 offen offset:3072 lds
v_mfma_f32_16x16x32_bf16 a[192:195],v[100:103],a[76:79],a[192:195]
s_add_u32 m0,s86,91136
ds_read_b64_tr_b16 v[82:83],v2 offset:5216
ds_read_b64_tr_b16 v[86:87],v2 offset:15456
v_mfma_f32_16x16x32_bf16 a[192:195],v[104:107],a[84:87],a[192:195]
ds_read_b64_tr_b16 v[90:91],v2 offset:25696
v_mfma_f32_16x16x32_bf16 a[192:195],v[108:111],a[92:95],a[192:195]
v_mfma_f32_16x16x32_bf16 a[196:199],v[108:111],a[124:127],a[196:199]
v_mfma_f32_16x16x32_bf16 a[196:199],v[104:107],a[116:119],a[196:199]
v_mfma_f32_16x16x32_bf16 a[196:199],v[100:103],a[108:111],a[196:199]
buffer_load_dwordx4 v7,s[48:51],s84 offen lds
v_mfma_f32_16x16x32_bf16 a[196:199],v[96:99],a[100:103],a[196:199]
s_add_u32 m0,s86,40960
s_add_u32 s84,s83,20480
v_mfma_f32_16x16x32_bf16 a[200:203],v[96:99],a[64:67],a[200:203]
s_waitcnt vmcnt(25)
v_mfma_f32_16x16x32_bf16 a[200:203],v[100:103],a[72:75],a[200:203]
s_waitcnt vmcnt(22)
v_mfma_f32_16x16x32_bf16 a[200:203],v[104:107],a[80:83],a[200:203]
s_waitcnt vmcnt(18)
v_mfma_f32_16x16x32_bf16 a[200:203],v[108:111],a[88:91],a[200:203]
buffer_load_dwordx4 v[32:35],v6,s[28:31],s87 offen sc1
v_mfma_f32_16x16x32_bf16 a[204:207],v[108:111],a[120:123],a[204:207]
s_waitcnt vmcnt(17)
ds_read_b64_tr_b16 v[108:109],v1 offset:30848
ds_read_b64_tr_b16 v[110:111],v1 offset:35968
v_mfma_f32_16x16x32_bf16 a[204:207],v[104:107],a[112:115],a[204:207]
s_waitcnt vmcnt(16)
ds_read_b64_tr_b16 v[104:105],v1 offset:20608
v_mfma_f32_16x16x32_bf16 a[204:207],v[100:103],a[104:107],a[204:207]
s_waitcnt vmcnt(14)
ds_read_b64_tr_b16 v[100:101],v1 offset:10368
v_mfma_f32_16x16x32_bf16 a[204:207],v[96:99],a[96:99],a[204:207]
ds_read_b64_tr_b16 v[96:97],v1 offset:128
v_mfma_f32_16x16x32_bf16 a[208:211],v[112:115],a[68:71],a[208:211]
buffer_load_dwordx4 v[132:135],v6,s[28:31],s87 offen offset:1024 sc1
v_mfma_f32_16x16x32_bf16 a[208:211],v[116:119],a[76:79],a[208:211]
ds_read_b64_tr_b16 v[98:99],v1 offset:5248
ds_read_b64_tr_b16 v[102:103],v1 offset:15488
v_mfma_f32_16x16x32_bf16 a[208:211],v[120:123],a[84:87],a[208:211]
ds_read_b64_tr_b16 v[106:107],v1 offset:25728
v_mfma_f32_16x16x32_bf16 a[208:211],v[124:127],a[92:95],a[208:211]
v_mfma_f32_16x16x32_bf16 a[212:215],v[124:127],a[124:127],a[212:215]
v_mfma_f32_16x16x32_bf16 a[212:215],v[120:123],a[116:119],a[212:215]
buffer_load_dwordx4 v[196:199],v6,s[28:31],s87 offen offset:2048 sc1
v_mfma_f32_16x16x32_bf16 a[212:215],v[116:119],a[108:111],a[212:215]
v_mfma_f32_16x16x32_bf16 a[212:215],v[112:115],a[100:103],a[212:215]
v_mfma_f32_16x16x32_bf16 a[216:219],v[112:115],a[64:67],a[216:219]
v_mfma_f32_16x16x32_bf16 a[216:219],v[116:119],a[72:75],a[216:219]
v_mfma_f32_16x16x32_bf16 a[216:219],v[120:123],a[80:83],a[216:219]
buffer_load_dwordx4 v[36:39],v6,s[28:31],s88 offen sc1
v_mfma_f32_16x16x32_bf16 a[216:219],v[124:127],a[88:91],a[216:219]
v_mfma_f32_16x16x32_bf16 a[220:223],v[124:127],a[120:123],a[220:223]
ds_read_b64_tr_b16 v[124:125],v2 offset:30880
ds_read_b64_tr_b16 v[126:127],v2 offset:36000
v_mfma_f32_16x16x32_bf16 a[220:223],v[120:123],a[112:115],a[220:223]
ds_read_b64_tr_b16 v[120:121],v2 offset:20640
v_mfma_f32_16x16x32_bf16 a[220:223],v[116:119],a[104:107],a[220:223]
s_waitcnt lgkmcnt(3)
ds_read_b64_tr_b16 v[116:117],v2 offset:10400
v_mfma_f32_16x16x32_bf16 a[220:223],v[112:115],a[96:99],a[220:223]
buffer_load_dwordx4 v[140:143],v28,s[28:31],s87 offen offset:3072 sc1
v_mfma_f32_16x16x32_bf16 a[224:227],v[64:67],a[68:71],a[224:227]
s_add_u32 s87,s64,1
s_and_b32 s87,s87,3
s_lshl_b32 s87,s87,1
v_mfma_f32_16x16x32_bf16 a[224:227],v[68:71],a[76:79],a[224:227]
s_or_b32 s87,s87,1
s_lshl_b32 s87,s87,16
ds_read_b64_tr_b16 v[112:113],v2 offset:160
v_mfma_f32_16x16x32_bf16 a[224:227],v[72:75],a[84:87],a[224:227]
ds_read_b64_tr_b16 v[114:115],v2 offset:5280
ds_read_b64_tr_b16 v[118:119],v2 offset:15520
v_mfma_f32_16x16x32_bf16 a[224:227],v[76:79],a[92:95],a[224:227]
ds_read_b64_tr_b16 v[122:123],v2 offset:25760
v_mfma_f32_16x16x32_bf16 a[228:231],v[76:79],a[124:127],a[228:231]
buffer_load_dwordx4 v[212:215],v6,s[28:31],s88 offen offset:2048 sc1
v_mfma_f32_16x16x32_bf16 a[228:231],v[72:75],a[116:119],a[228:231]
v_mfma_f32_16x16x32_bf16 a[228:231],v[68:71],a[108:111],a[228:231]
v_mfma_f32_16x16x32_bf16 a[228:231],v[64:67],a[100:103],a[228:231]
v_mfma_f32_16x16x32_bf16 a[232:235],v[64:67],a[64:67],a[232:235]
v_mfma_f32_16x16x32_bf16 a[232:235],v[68:71],a[72:75],a[232:235]
buffer_load_dwordx4 v[148:151],v6,s[28:31],s88 offen offset:1024 sc1
v_mfma_f32_16x16x32_bf16 a[232:235],v[72:75],a[80:83],a[232:235]
s_waitcnt vmcnt(11)
v_cmp_ne_u32 s[56:57],s52,v4
v_cmp_ne_u32 s[58:59],s53,v5
v_mfma_f32_16x16x32_bf16 a[232:235],v[76:79],a[88:91],a[232:235]
s_or_b64 s[56:57],s[56:57],s[58:59]
s_cbranch_scc1 L_fr7_%=
L_fo7_%=:
s_lshl_b32 s91,s64,6
v_mfma_f32_16x16x32_bf16 a[236:239],v[76:79],a[120:123],a[236:239]
ds_read_b64_tr_b16 v[76:77],v1 offset:30912
ds_read_b64_tr_b16 v[78:79],v1 offset:36032
v_mfma_f32_16x16x32_bf16 a[236:239],v[72:75],a[112:115],a[236:239]
ds_read_b64_tr_b16 v[72:73],v1 offset:20672
v_bfe_u32 v3,v6,4,2
v_mfma_f32_16x16x32_bf16 a[236:239],v[68:71],a[104:107],a[236:239]
buffer_load_dwordx4 v[40:43],v6,s[28:31],s89 offen sc1
v_mfma_f32_16x16x32_bf16 a[236:239],v[64:67],a[96:99],a[236:239]
ds_read_b64_tr_b16 v[64:65],v1 offset:192
ds_read_b64_tr_b16 v[66:67],v1 offset:5312
v_mfma_f32_16x16x32_bf16 a[240:243],v[80:83],a[68:71],a[240:243]
ds_read_b64_tr_b16 v[68:69],v1 offset:10432
v_min_u32 v3,2,v3
v_mfma_f32_16x16x32_bf16 a[240:243],v[84:87],a[76:79],a[240:243]
ds_read_b64_tr_b16 v[70:71],v1 offset:15552
v_add3_u32 v3,v3,s63,1
v_mfma_f32_16x16x32_bf16 a[240:243],v[88:91],a[84:87],a[240:243]
ds_read_b64_tr_b16 v[74:75],v1 offset:25792
v_and_b32 v3,3,v3
v_mfma_f32_16x16x32_bf16 a[240:243],v[92:95],a[92:95],a[240:243]
buffer_load_dwordx4 a[128:131],v6,s[28:31],s87 offen sc1
v_mfma_f32_16x16x32_bf16 a[244:247],v[92:95],a[124:127],a[244:247]
v_lshlrev_b32 v3,8,v3
v_mfma_f32_16x16x32_bf16 a[244:247],v[88:91],a[116:119],a[244:247]
v_mfma_f32_16x16x32_bf16 a[244:247],v[84:87],a[108:111],a[244:247]
v_mfma_f32_16x16x32_bf16 a[244:247],v[80:83],a[100:103],a[244:247]
v_mfma_f32_16x16x32_bf16 a[248:251],v[80:83],a[64:67],a[248:251]
buffer_load_dwordx4 v[156:159],v28,s[28:31],s88 offen offset:3072 sc1
v_mfma_f32_16x16x32_bf16 a[248:251],v[84:87],a[72:75],a[248:251]
s_add_u32 s88,s87,4096
v_mfma_f32_16x16x32_bf16 a[248:251],v[88:91],a[80:83],a[248:251]
v_mfma_f32_16x16x32_bf16 a[248:251],v[92:95],a[88:91],a[248:251]
v_mfma_f32_16x16x32_bf16 a[252:255],v[92:95],a[120:123],a[252:255]
ds_read_b64_tr_b16 v[92:93],v2 offset:30944
ds_read_b64_tr_b16 v[94:95],v2 offset:36064
v_mfma_f32_16x16x32_bf16 a[252:255],v[88:91],a[112:115],a[252:255]
buffer_load_dwordx4 v[228:231],v6,s[28:31],s89 offen offset:2048 sc1
v_mfma_f32_16x16x32_bf16 a[252:255],v[84:87],a[104:107],a[252:255]
ds_read_b64_tr_b16 v[84:85],v2 offset:10464
ds_read_b64_tr_b16 v[86:87],v2 offset:15584
v_mfma_f32_16x16x32_bf16 a[252:255],v[80:83],a[96:99],a[252:255]
ds_read_b64_tr_b16 v[80:81],v2 offset:224
v_mfma_f32_16x16x32_bf16 a[0:3],v[96:99],a[68:71],a[0:3]
ds_read_b64_tr_b16 v[82:83],v2 offset:5344
v_mfma_f32_16x16x32_bf16 a[0:3],v[100:103],a[76:79],a[0:3]
ds_read_b64_tr_b16 v[88:89],v2 offset:20704
v_mfma_f32_16x16x32_bf16 a[0:3],v[104:107],a[84:87],a[0:3]
buffer_load_dwordx4 v[164:167],v6,s[28:31],s89 offen offset:1024 sc1
v_mfma_f32_16x16x32_bf16 a[0:3],v[108:111],a[92:95],a[0:3]
ds_read_b64_tr_b16 v[90:91],v2 offset:25824
v_mfma_f32_16x16x32_bf16 a[4:7],v[108:111],a[124:127],a[4:7]
v_mfma_f32_16x16x32_bf16 a[4:7],v[104:107],a[116:119],a[4:7]
v_mfma_f32_16x16x32_bf16 a[4:7],v[100:103],a[108:111],a[4:7]
v_mfma_f32_16x16x32_bf16 a[4:7],v[96:99],a[100:103],a[4:7]
buffer_load_dwordx4 v[44:47],v6,s[28:31],s90 offen sc1
v_mfma_f32_16x16x32_bf16 a[8:11],v[96:99],a[64:67],a[8:11]
v_mfma_f32_16x16x32_bf16 a[8:11],v[100:103],a[72:75],a[8:11]
v_mfma_f32_16x16x32_bf16 a[8:11],v[104:107],a[80:83],a[8:11]
v_mfma_f32_16x16x32_bf16 a[8:11],v[108:111],a[88:91],a[8:11]
v_mfma_f32_16x16x32_bf16 a[12:15],v[108:111],a[120:123],a[12:15]
buffer_load_dwordx4 a[144:147],v6,s[28:31],s88 offen sc1
v_mfma_f32_16x16x32_bf16 a[12:15],v[104:107],a[112:115],a[12:15]
ds_read_b64_tr_b16 v[104:105],v1 offset:20736
ds_read_b64_tr_b16 v[106:107],v1 offset:25856
v_mfma_f32_16x16x32_bf16 a[12:15],v[100:103],a[104:107],a[12:15]
ds_read_b64_tr_b16 v[100:101],v1 offset:10496
v_mfma_f32_16x16x32_bf16 a[12:15],v[96:99],a[96:99],a[12:15]
s_waitcnt lgkmcnt(3)
ds_read_b64_tr_b16 v[96:97],v1 offset:256
v_mfma_f32_16x16x32_bf16 a[16:19],v[112:115],a[68:71],a[16:19]
ds_read_b64_tr_b16 v[98:99],v1 offset:5376
v_mfma_f32_16x16x32_bf16 a[16:19],v[116:119],a[76:79],a[16:19]
buffer_load_dwordx4 v[172:175],v28,s[28:31],s89 offen offset:3072 sc1
v_mfma_f32_16x16x32_bf16 a[16:19],v[120:123],a[84:87],a[16:19]
s_add_u32 s89,s87,8192
ds_read_b64_tr_b16 v[102:103],v1 offset:15616
ds_read_b64_tr_b16 v[108:109],v1 offset:30976
v_mfma_f32_16x16x32_bf16 a[16:19],v[124:127],a[92:95],a[16:19]
ds_read_b64_tr_b16 v[110:111],v1 offset:36096
v_add_u32 v1,0x1e000,v1
v_mfma_f32_16x16x32_bf16 a[20:23],v[124:127],a[124:127],a[20:23]
v_mfma_f32_16x16x32_bf16 a[20:23],v[120:123],a[116:119],a[20:23]
v_mfma_f32_16x16x32_bf16 a[20:23],v[116:119],a[108:111],a[20:23]
buffer_load_dwordx4 v[180:183],v6,s[28:31],s90 offen offset:1024 sc1
v_mfma_f32_16x16x32_bf16 a[20:23],v[112:115],a[100:103],a[20:23]
v_mfma_f32_16x16x32_bf16 a[24:27],v[112:115],a[64:67],a[24:27]
v_mfma_f32_16x16x32_bf16 a[24:27],v[116:119],a[72:75],a[24:27]
v_mfma_f32_16x16x32_bf16 a[24:27],v[120:123],a[80:83],a[24:27]
v_mfma_f32_16x16x32_bf16 a[24:27],v[124:127],a[88:91],a[24:27]
buffer_load_dwordx4 a[160:163],v6,s[28:31],s89 offen sc1
v_mfma_f32_16x16x32_bf16 a[28:31],v[124:127],a[120:123],a[28:31]
ds_read_b64_tr_b16 v[124:125],v2 offset:31008
ds_read_b64_tr_b16 v[126:127],v2 offset:36128
v_mfma_f32_16x16x32_bf16 a[28:31],v[120:123],a[112:115],a[28:31]
ds_read_b64_tr_b16 v[120:121],v2 offset:20768
v_mfma_f32_16x16x32_bf16 a[28:31],v[116:119],a[104:107],a[28:31]
ds_read_b64_tr_b16 v[116:117],v2 offset:10528
v_mfma_f32_16x16x32_bf16 a[28:31],v[112:115],a[96:99],a[28:31]
ds_read_b64_tr_b16 v[112:113],v2 offset:288
v_mfma_f32_16x16x32_bf16 v[128:131],v[64:67],a[68:71],v[128:131]
buffer_load_dwordx4 v[244:247],v6,s[28:31],s90 offen offset:2048 sc1
v_mfma_f32_16x16x32_bf16 v[128:131],v[68:71],a[76:79],v[128:131]
ds_read_b64_tr_b16 v[114:115],v2 offset:5408
ds_read_b64_tr_b16 v[118:119],v2 offset:15648
v_mfma_f32_16x16x32_bf16 v[128:131],v[72:75],a[84:87],v[128:131]
ds_read_b64_tr_b16 v[122:123],v2 offset:25888
v_add_u32 v2,0x1e000,v2
v_mfma_f32_16x16x32_bf16 v[128:131],v[76:79],a[92:95],v[128:131]
v_mfma_f32_16x16x32_bf16 v[136:139],v[76:79],a[124:127],v[136:139]
v_mfma_f32_16x16x32_bf16 v[136:139],v[72:75],a[116:119],v[136:139]
)A"
R"A(buffer_load_dwordx4 v[188:191],v28,s[28:31],s90 offen offset:3072 sc1
v_mfma_f32_16x16x32_bf16 v[136:139],v[68:71],a[108:111],v[136:139]
s_add_u32 s90,s87,12288
v_mfma_f32_16x16x32_bf16 v[136:139],v[64:67],a[100:103],v[136:139]
v_mfma_f32_16x16x32_bf16 v[144:147],v[64:67],a[64:67],v[144:147]
v_mfma_f32_16x16x32_bf16 v[144:147],v[68:71],a[72:75],v[144:147]
v_mfma_f32_16x16x32_bf16 v[144:147],v[72:75],a[80:83],v[144:147]
buffer_load_dwordx4 a[176:179],v6,s[28:31],s90 offen sc1
v_mfma_f32_16x16x32_bf16 v[144:147],v[76:79],a[88:91],v[144:147]
v_mfma_f32_16x16x32_bf16 v[152:155],v[76:79],a[120:123],v[152:155]
v_mfma_f32_16x16x32_bf16 v[152:155],v[72:75],a[112:115],v[152:155]
v_mfma_f32_16x16x32_bf16 v[152:155],v[68:71],a[104:107],v[152:155]
v_mfma_f32_16x16x32_bf16 v[152:155],v[64:67],a[96:99],v[152:155]
buffer_load_dwordx4 a[180:183],v6,s[28:31],s90 offen offset:1024 sc1
s_waitcnt vmcnt(21)
s_barrier
v_mfma_f32_16x16x32_bf16 v[160:163],v[80:83],a[68:71],v[160:163]
ds_read_b64_tr_b16 v[64:65],v1
ds_read_b64_tr_b16 v[66:67],v1 offset:5120
v_mfma_f32_16x16x32_bf16 v[160:163],v[84:87],a[76:79],v[160:163]
ds_read_b64_tr_b16 v[68:69],v1 offset:10240
v_mfma_f32_16x16x32_bf16 v[160:163],v[88:91],a[84:87],v[160:163]
ds_read_b64_tr_b16 v[70:71],v1 offset:15360
v_mfma_f32_16x16x32_bf16 v[160:163],v[92:95],a[92:95],v[160:163]
buffer_load_dwordx4 a[164:167],v6,s[28:31],s89 offen offset:1024 sc1
v_mfma_f32_16x16x32_bf16 v[168:171],v[92:95],a[124:127],v[168:171]
ds_read_b64_tr_b16 v[72:73],v1 offset:20480
ds_read_b64_tr_b16 v[74:75],v1 offset:25600
v_mfma_f32_16x16x32_bf16 v[168:171],v[88:91],a[116:119],v[168:171]
ds_read_b64_tr_b16 v[76:77],v1 offset:30720
v_mfma_f32_16x16x32_bf16 v[168:171],v[84:87],a[108:111],v[168:171]
ds_read_b64_tr_b16 v[78:79],v1 offset:35840
v_mfma_f32_16x16x32_bf16 v[168:171],v[80:83],a[100:103],v[168:171]
v_mfma_f32_16x16x32_bf16 v[176:179],v[80:83],a[64:67],v[176:179]
buffer_load_dwordx4 a[148:151],v6,s[28:31],s88 offen offset:1024 sc1
v_mfma_f32_16x16x32_bf16 v[176:179],v[84:87],a[72:75],v[176:179]
v_mfma_f32_16x16x32_bf16 v[176:179],v[88:91],a[80:83],v[176:179]
v_mfma_f32_16x16x32_bf16 v[176:179],v[92:95],a[88:91],v[176:179]
v_mfma_f32_16x16x32_bf16 v[184:187],v[92:95],a[120:123],v[184:187]
ds_read_b64_tr_b16 v[92:93],v2 offset:30752
ds_read_b64_tr_b16 v[94:95],v2 offset:35872
v_mfma_f32_16x16x32_bf16 v[184:187],v[88:91],a[112:115],v[184:187]
buffer_load_dwordx4 a[132:135],v6,s[28:31],s87 offen offset:1024 sc1
v_mfma_f32_16x16x32_bf16 v[184:187],v[84:87],a[104:107],v[184:187]
ds_read_b64_tr_b16 v[84:85],v2 offset:10272
ds_read_b64_tr_b16 v[86:87],v2 offset:15392
v_mfma_f32_16x16x32_bf16 v[184:187],v[80:83],a[96:99],v[184:187]
s_waitcnt lgkmcnt(4)
ds_read_b64_tr_b16 v[80:81],v2 offset:32
v_mfma_f32_16x16x32_bf16 v[192:195],v[96:99],a[68:71],v[192:195]
ds_read_b64_tr_b16 v[82:83],v2 offset:5152
v_mfma_f32_16x16x32_bf16 v[192:195],v[100:103],a[76:79],v[192:195]
ds_read_b64_tr_b16 v[88:89],v2 offset:20512
v_mfma_f32_16x16x32_bf16 v[192:195],v[104:107],a[84:87],v[192:195]
ds_read_b64_tr_b16 v[90:91],v2 offset:25632
v_mfma_f32_16x16x32_bf16 v[192:195],v[108:111],a[92:95],v[192:195]
buffer_load_dwordx4 v24,s[48:51],s83 offen lds
v_mfma_f32_16x16x32_bf16 v[200:203],v[108:111],a[124:127],v[200:203]
v_mfma_f32_16x16x32_bf16 v[200:203],v[104:107],a[116:119],v[200:203]
v_mfma_f32_16x16x32_bf16 v[200:203],v[100:103],a[108:111],v[200:203]
v_mfma_f32_16x16x32_bf16 v[200:203],v[96:99],a[100:103],v[200:203]
v_mfma_f32_16x16x32_bf16 v[208:211],v[96:99],a[64:67],v[208:211]
buffer_load_dwordx4 a[136:139],v6,s[28:31],s87 offen offset:2048 sc1
v_mfma_f32_16x16x32_bf16 v[208:211],v[100:103],a[72:75],v[208:211]
v_mfma_f32_16x16x32_bf16 v[208:211],v[104:107],a[80:83],v[208:211]
v_mfma_f32_16x16x32_bf16 v[208:211],v[108:111],a[88:91],v[208:211]
v_mfma_f32_16x16x32_bf16 v[216:219],v[108:111],a[120:123],v[216:219]
ds_read_b64_tr_b16 v[108:109],v1 offset:30784
ds_read_b64_tr_b16 v[110:111],v1 offset:35904
v_mfma_f32_16x16x32_bf16 v[216:219],v[104:107],a[112:115],v[216:219]
ds_read_b64_tr_b16 v[104:105],v1 offset:20544
v_mfma_f32_16x16x32_bf16 v[216:219],v[100:103],a[104:107],v[216:219]
buffer_load_dwordx4 v25,s[48:51],s83 offen offset:1024 lds
v_mfma_f32_16x16x32_bf16 v[216:219],v[96:99],a[96:99],v[216:219]
ds_read_b64_tr_b16 v[96:97],v1 offset:64
ds_read_b64_tr_b16 v[98:99],v1 offset:5184
v_mfma_f32_16x16x32_bf16 v[224:227],v[112:115],a[68:71],v[224:227]
ds_read_b64_tr_b16 v[100:101],v1 offset:10304
v_mfma_f32_16x16x32_bf16 v[224:227],v[116:119],a[76:79],v[224:227]
ds_read_b64_tr_b16 v[102:103],v1 offset:15424
v_mfma_f32_16x16x32_bf16 v[224:227],v[120:123],a[84:87],v[224:227]
ds_read_b64_tr_b16 v[106:107],v1 offset:25664
v_mfma_f32_16x16x32_bf16 v[224:227],v[124:127],a[92:95],v[224:227]
buffer_load_dwordx4 a[152:155],v6,s[28:31],s88 offen offset:2048 sc1
v_mfma_f32_16x16x32_bf16 v[232:235],v[124:127],a[124:127],v[232:235]
v_mfma_f32_16x16x32_bf16 v[232:235],v[120:123],a[116:119],v[232:235]
v_mfma_f32_16x16x32_bf16 v[232:235],v[116:119],a[108:111],v[232:235]
v_mfma_f32_16x16x32_bf16 v[232:235],v[112:115],a[100:103],v[232:235]
v_mfma_f32_16x16x32_bf16 v[240:243],v[112:115],a[64:67],v[240:243]
v_mfma_f32_16x16x32_bf16 v[240:243],v[116:119],a[72:75],v[240:243]
buffer_load_dwordx4 v26,s[48:51],s83 offen offset:2048 lds
v_mfma_f32_16x16x32_bf16 v[240:243],v[120:123],a[80:83],v[240:243]
v_mfma_f32_16x16x32_bf16 v[240:243],v[124:127],a[88:91],v[240:243]
v_mfma_f32_16x16x32_bf16 v[248:251],v[124:127],a[120:123],v[248:251]
ds_read_b64_tr_b16 v[124:125],v2 offset:30816
ds_read_b64_tr_b16 v[126:127],v2 offset:35936
v_mfma_f32_16x16x32_bf16 v[248:251],v[120:123],a[112:115],v[248:251]
ds_read_b64_tr_b16 v[120:121],v2 offset:20576
v_mfma_f32_16x16x32_bf16 v[248:251],v[116:119],a[104:107],v[248:251]
buffer_load_dwordx4 a[168:171],v6,s[28:31],s89 offen offset:2048 sc1
v_mfma_f32_16x16x32_bf16 v[248:251],v[112:115],a[96:99],v[248:251]
s_waitcnt vmcnt(22)
ds_read_b64_tr_b16 v[112:113],v2 offset:96
ds_read_b64_tr_b16 v[114:115],v2 offset:5216
v_mfma_f32_16x16x32_bf16 a[192:195],v[64:67],v[32:35],a[192:195]
ds_read_b64_tr_b16 v[116:117],v2 offset:10336
v_mfma_f32_16x16x32_bf16 a[192:195],v[68:71],v[36:39],a[192:195]
ds_read_b64_tr_b16 v[118:119],v2 offset:15456
v_mfma_f32_16x16x32_bf16 a[192:195],v[72:75],v[40:43],a[192:195]
s_waitcnt vmcnt(17)
ds_read_b64_tr_b16 v[122:123],v2 offset:25696
v_mfma_f32_16x16x32_bf16 a[192:195],v[76:79],v[44:47],a[192:195]
s_waitcnt vmcnt(14)
v_mfma_f32_16x16x32_bf16 a[196:199],v[76:79],v[180:183],a[196:199]
buffer_load_dwordx4 v27,s[48:51],s83 offen offset:3072 lds
v_mfma_f32_16x16x32_bf16 a[196:199],v[72:75],v[164:167],a[196:199]
s_add_u32 m0,s86,45056
v_mfma_f32_16x16x32_bf16 a[196:199],v[68:71],v[148:151],a[196:199]
v_mfma_f32_16x16x32_bf16 a[196:199],v[64:67],v[132:135],a[196:199]
v_mfma_f32_16x16x32_bf16 a[200:203],v[64:67],v[196:199],a[200:203]
v_mfma_f32_16x16x32_bf16 a[200:203],v[68:71],v[212:215],a[200:203]
buffer_load_dwordx4 a[184:187],v6,s[28:31],s90 offen offset:2048 sc1
v_mfma_f32_16x16x32_bf16 a[200:203],v[72:75],v[228:231],a[200:203]
s_waitcnt vmcnt(14)
v_mfma_f32_16x16x32_bf16 a[200:203],v[76:79],v[244:247],a[200:203]
s_waitcnt vmcnt(13)
v_mfma_f32_16x16x32_bf16 a[204:207],v[76:79],v[188:191],a[204:207]
ds_read_b64_tr_b16 v[76:77],v1 offset:30848
ds_read_b64_tr_b16 v[78:79],v1 offset:35968
v_mfma_f32_16x16x32_bf16 a[204:207],v[72:75],v[172:175],a[204:207]
ds_read_b64_tr_b16 v[72:73],v1 offset:20608
v_mfma_f32_16x16x32_bf16 a[204:207],v[68:71],v[156:159],a[204:207]
s_waitcnt lgkmcnt(3)
ds_read_b64_tr_b16 v[68:69],v1 offset:10368
v_mfma_f32_16x16x32_bf16 a[204:207],v[64:67],v[140:143],a[204:207]
buffer_load_dwordx4 v7,s[48:51],s83 offen lds
v_mfma_f32_16x16x32_bf16 a[208:211],v[80:83],v[32:35],a[208:211]
s_add_u32 m0,s86,46080
ds_read_b64_tr_b16 v[64:65],v1 offset:128
ds_read_b64_tr_b16 v[66:67],v1 offset:5248
v_mfma_f32_16x16x32_bf16 a[208:211],v[84:87],v[36:39],a[208:211]
s_mul_i32 s83,s63,20480
ds_read_b64_tr_b16 v[70:71],v1 offset:15488
v_mfma_f32_16x16x32_bf16 a[208:211],v[88:91],v[40:43],a[208:211]
ds_read_b64_tr_b16 v[74:75],v1 offset:25728
v_mfma_f32_16x16x32_bf16 a[208:211],v[92:95],v[44:47],a[208:211]
v_mfma_f32_16x16x32_bf16 a[212:215],v[92:95],v[180:183],a[212:215]
buffer_load_dwordx4 a[188:191],v28,s[28:31],s90 offen offset:3072 sc1
v_mfma_f32_16x16x32_bf16 a[212:215],v[88:91],v[164:167],a[212:215]
v_mfma_f32_16x16x32_bf16 a[212:215],v[84:87],v[148:151],a[212:215]
v_mfma_f32_16x16x32_bf16 a[212:215],v[80:83],v[132:135],a[212:215]
v_mfma_f32_16x16x32_bf16 a[216:219],v[80:83],v[196:199],a[216:219]
v_mfma_f32_16x16x32_bf16 a[216:219],v[84:87],v[212:215],a[216:219]
v_mfma_f32_16x16x32_bf16 a[216:219],v[88:91],v[228:231],a[216:219]
buffer_load_dwordx4 v24,s[48:51],s84 offen lds
v_mfma_f32_16x16x32_bf16 a[216:219],v[92:95],v[244:247],a[216:219]
v_mfma_f32_16x16x32_bf16 a[220:223],v[92:95],v[188:191],a[220:223]
ds_read_b64_tr_b16 v[92:93],v2 offset:30880
ds_read_b64_tr_b16 v[94:95],v2 offset:36000
v_mfma_f32_16x16x32_bf16 a[220:223],v[88:91],v[172:175],a[220:223]
ds_read_b64_tr_b16 v[88:89],v2 offset:20640
v_mfma_f32_16x16x32_bf16 a[220:223],v[84:87],v[156:159],a[220:223]
ds_read_b64_tr_b16 v[84:85],v2 offset:10400
v_mfma_f32_16x16x32_bf16 a[220:223],v[80:83],v[140:143],a[220:223]
ds_read_b64_tr_b16 v[80:81],v2 offset:160
v_mfma_f32_16x16x32_bf16 a[224:227],v[96:99],v[32:35],a[224:227]
buffer_load_dwordx4 v25,s[48:51],s84 offen offset:1024 lds
v_mfma_f32_16x16x32_bf16 a[224:227],v[100:103],v[36:39],a[224:227]
ds_read_b64_tr_b16 v[82:83],v2 offset:5280
ds_read_b64_tr_b16 v[86:87],v2 offset:15520
v_mfma_f32_16x16x32_bf16 a[224:227],v[104:107],v[40:43],a[224:227]
ds_read_b64_tr_b16 v[90:91],v2 offset:25760
v_mfma_f32_16x16x32_bf16 a[224:227],v[108:111],v[44:47],a[224:227]
v_mfma_f32_16x16x32_bf16 a[228:231],v[108:111],v[180:183],a[228:231]
v_mfma_f32_16x16x32_bf16 a[228:231],v[104:107],v[164:167],a[228:231]
buffer_load_dwordx4 a[172:175],v28,s[28:31],s89 offen offset:3072 sc1
v_mfma_f32_16x16x32_bf16 a[228:231],v[100:103],v[148:151],a[228:231]
v_mfma_f32_16x16x32_bf16 a[228:231],v[96:99],v[132:135],a[228:231]
v_mfma_f32_16x16x32_bf16 a[232:235],v[96:99],v[196:199],a[232:235]
v_mfma_f32_16x16x32_bf16 a[232:235],v[100:103],v[212:215],a[232:235]
v_mfma_f32_16x16x32_bf16 a[232:235],v[104:107],v[228:231],a[232:235]
v_mfma_f32_16x16x32_bf16 a[232:235],v[108:111],v[244:247],a[232:235]
buffer_load_dwordx4 v26,s[48:51],s84 offen offset:2048 lds
v_mfma_f32_16x16x32_bf16 a[236:239],v[108:111],v[188:191],a[236:239]
ds_read_b64_tr_b16 v[108:109],v1 offset:30912
ds_read_b64_tr_b16 v[110:111],v1 offset:36032
v_mfma_f32_16x16x32_bf16 a[236:239],v[104:107],v[172:175],a[236:239]
ds_read_b64_tr_b16 v[104:105],v1 offset:20672
v_mfma_f32_16x16x32_bf16 a[236:239],v[100:103],v[156:159],a[236:239]
ds_read_b64_tr_b16 v[100:101],v1 offset:10432
v_mfma_f32_16x16x32_bf16 a[236:239],v[96:99],v[140:143],a[236:239]
ds_read_b64_tr_b16 v[96:97],v1 offset:192
v_mfma_f32_16x16x32_bf16 a[240:243],v[112:115],v[32:35],a[240:243]
ds_read_b64_tr_b16 v[98:99],v1 offset:5312
ds_read_b64_tr_b16 v[102:103],v1 offset:15552
v_mfma_f32_16x16x32_bf16 a[240:243],v[116:119],v[36:39],a[240:243]
buffer_load_dwordx4 v27,s[48:51],s84 offen offset:3072 lds
v_mfma_f32_16x16x32_bf16 a[240:243],v[120:123],v[40:43],a[240:243]
s_add_u32 m0,s86,50176
ds_read_b64_tr_b16 v[106:107],v1 offset:25792
v_add_u32 v1,0xffff6000,v1
v_mfma_f32_16x16x32_bf16 a[240:243],v[124:127],v[44:47],a[240:243]
v_mfma_f32_16x16x32_bf16 a[244:247],v[124:127],v[180:183],a[244:247]
v_mfma_f32_16x16x32_bf16 a[244:247],v[120:123],v[164:167],a[244:247]
v_mfma_f32_16x16x32_bf16 a[244:247],v[116:119],v[148:151],a[244:247]
buffer_load_dwordx4 a[140:143],v28,s[28:31],s87 offen offset:3072 sc1
v_mfma_f32_16x16x32_bf16 a[244:247],v[112:115],v[132:135],a[244:247]
s_add_u32 s87,s64,2
s_and_b32 s87,s87,3
s_lshl_b32 s87,s87,1
s_or_b32 s87,s87,1
s_lshl_b32 s87,s87,16
s_add_u32 s89,s87,8192
s_add_u32 s90,s87,12288
v_mfma_f32_16x16x32_bf16 a[248:251],v[112:115],v[196:199],a[248:251]
)A"
R"A(v_mfma_f32_16x16x32_bf16 a[248:251],v[116:119],v[212:215],a[248:251]
v_mfma_f32_16x16x32_bf16 a[248:251],v[120:123],v[228:231],a[248:251]
v_mfma_f32_16x16x32_bf16 a[248:251],v[124:127],v[244:247],a[248:251]
buffer_load_dwordx4 a[68:71],v6,s[28:31],s87 offen sc1
v_mfma_f32_16x16x32_bf16 a[252:255],v[124:127],v[188:191],a[252:255]
ds_read_b64_tr_b16 v[124:125],v2 offset:30944
ds_read_b64_tr_b16 v[126:127],v2 offset:36064
v_mfma_f32_16x16x32_bf16 a[252:255],v[120:123],v[172:175],a[252:255]
ds_read_b64_tr_b16 v[120:121],v2 offset:20704
v_mfma_f32_16x16x32_bf16 a[252:255],v[116:119],v[156:159],a[252:255]
ds_read_b64_tr_b16 v[116:117],v2 offset:10464
v_mfma_f32_16x16x32_bf16 a[252:255],v[112:115],v[140:143],a[252:255]
s_waitcnt lgkmcnt(4)
ds_read_b64_tr_b16 v[112:113],v2 offset:224
v_mfma_f32_16x16x32_bf16 a[0:3],v[64:67],v[32:35],a[0:3]
buffer_load_dwordx4 a[156:159],v28,s[28:31],s88 offen offset:3072 sc1
v_mfma_f32_16x16x32_bf16 a[0:3],v[68:71],v[36:39],a[0:3]
s_add_u32 s88,s87,4096
ds_read_b64_tr_b16 v[114:115],v2 offset:5344
ds_read_b64_tr_b16 v[118:119],v2 offset:15584
v_mfma_f32_16x16x32_bf16 a[0:3],v[72:75],v[40:43],a[0:3]
ds_read_b64_tr_b16 v[122:123],v2 offset:25824
v_add_u32 v2,0xffff6000,v2
v_mfma_f32_16x16x32_bf16 a[0:3],v[76:79],v[44:47],a[0:3]
v_mfma_f32_16x16x32_bf16 a[4:7],v[76:79],v[180:183],a[4:7]
v_mfma_f32_16x16x32_bf16 a[4:7],v[72:75],v[164:167],a[4:7]
v_mfma_f32_16x16x32_bf16 a[4:7],v[68:71],v[148:151],a[4:7]
buffer_load_dwordx4 v7,s[48:51],s84 offen lds
v_mfma_f32_16x16x32_bf16 a[4:7],v[64:67],v[132:135],a[4:7]
v_mfma_f32_16x16x32_bf16 a[8:11],v[64:67],v[196:199],a[8:11]
v_mfma_f32_16x16x32_bf16 a[8:11],v[68:71],v[212:215],a[8:11]
v_mfma_f32_16x16x32_bf16 a[8:11],v[72:75],v[228:231],a[8:11]
v_mfma_f32_16x16x32_bf16 a[8:11],v[76:79],v[244:247],a[8:11]
buffer_load_dwordx4 a[76:79],v6,s[28:31],s88 offen sc1
v_mfma_f32_16x16x32_bf16 a[12:15],v[76:79],v[188:191],a[12:15]
ds_read_b64_tr_b16 v[76:77],v1 offset:30720
ds_read_b64_tr_b16 v[78:79],v1 offset:35840
v_mfma_f32_16x16x32_bf16 a[12:15],v[72:75],v[172:175],a[12:15]
ds_read_b64_tr_b16 v[72:73],v1 offset:20480
v_mfma_f32_16x16x32_bf16 a[12:15],v[68:71],v[156:159],a[12:15]
ds_read_b64_tr_b16 v[68:69],v1 offset:10240
v_mfma_f32_16x16x32_bf16 a[12:15],v[64:67],v[140:143],a[12:15]
ds_read_b64_tr_b16 v[64:65],v1
v_mfma_f32_16x16x32_bf16 a[16:19],v[80:83],v[32:35],a[16:19]
buffer_load_dwordx4 a[84:87],v6,s[28:31],s89 offen sc1
v_mfma_f32_16x16x32_bf16 a[16:19],v[84:87],v[36:39],a[16:19]
ds_read_b64_tr_b16 v[66:67],v1 offset:5120
ds_read_b64_tr_b16 v[70:71],v1 offset:15360
v_mfma_f32_16x16x32_bf16 a[16:19],v[88:91],v[40:43],a[16:19]
ds_read_b64_tr_b16 v[74:75],v1 offset:25600
s_nop 0
L_pd22_%=:
s_cbranch_execz L_trtg2S_%=
v_add_u32 v1,0xffff6000,v1
v_mfma_f32_16x16x32_bf16 a[16:19],v[92:95],v[44:47],a[16:19]
v_mfma_f32_16x16x32_bf16 a[20:23],v[92:95],v[180:183],a[20:23]
v_mfma_f32_16x16x32_bf16 a[20:23],v[88:91],v[164:167],a[20:23]
buffer_load_dwordx4 a[92:95],v6,s[28:31],s90 offen sc1
v_mfma_f32_16x16x32_bf16 a[20:23],v[84:87],v[148:151],a[20:23]
v_mfma_f32_16x16x32_bf16 a[20:23],v[80:83],v[132:135],a[20:23]
v_mfma_f32_16x16x32_bf16 a[24:27],v[80:83],v[196:199],a[24:27]
v_mfma_f32_16x16x32_bf16 a[24:27],v[84:87],v[212:215],a[24:27]
v_mfma_f32_16x16x32_bf16 a[24:27],v[88:91],v[228:231],a[24:27]
buffer_load_dwordx4 a[124:127],v6,s[28:31],s90 offen offset:1024 sc1
v_mfma_f32_16x16x32_bf16 a[24:27],v[92:95],v[244:247],a[24:27]
v_mfma_f32_16x16x32_bf16 a[28:31],v[92:95],v[188:191],a[28:31]
ds_read_b64_tr_b16 v[92:93],v2 offset:30752
ds_read_b64_tr_b16 v[94:95],v2 offset:35872
v_mfma_f32_16x16x32_bf16 a[28:31],v[88:91],v[172:175],a[28:31]
ds_read_b64_tr_b16 v[88:89],v2 offset:20512
v_mfma_f32_16x16x32_bf16 a[28:31],v[84:87],v[156:159],a[28:31]
ds_read_b64_tr_b16 v[84:85],v2 offset:10272
v_mfma_f32_16x16x32_bf16 a[28:31],v[80:83],v[140:143],a[28:31]
buffer_load_dwordx4 a[116:119],v6,s[28:31],s89 offen offset:1024 sc1
v_mfma_f32_16x16x32_bf16 v[128:131],v[96:99],v[32:35],v[128:131]
ds_read_b64_tr_b16 v[80:81],v2 offset:32
ds_read_b64_tr_b16 v[82:83],v2 offset:5152
v_mfma_f32_16x16x32_bf16 v[128:131],v[100:103],v[36:39],v[128:131]
ds_read_b64_tr_b16 v[86:87],v2 offset:15392
v_mfma_f32_16x16x32_bf16 v[128:131],v[104:107],v[40:43],v[128:131]
ds_read_b64_tr_b16 v[90:91],v2 offset:25632
v_add_u32 v2,0xffff6000,v2
v_mfma_f32_16x16x32_bf16 v[128:131],v[108:111],v[44:47],v[128:131]
v_mfma_f32_16x16x32_bf16 v[136:139],v[108:111],v[180:183],v[136:139]
buffer_load_dwordx4 a[108:111],v6,s[28:31],s88 offen offset:1024 sc1
v_mfma_f32_16x16x32_bf16 v[136:139],v[104:107],v[164:167],v[136:139]
v_mfma_f32_16x16x32_bf16 v[136:139],v[100:103],v[148:151],v[136:139]
v_mfma_f32_16x16x32_bf16 v[136:139],v[96:99],v[132:135],v[136:139]
v_mfma_f32_16x16x32_bf16 v[144:147],v[96:99],v[196:199],v[144:147]
v_mfma_f32_16x16x32_bf16 v[144:147],v[100:103],v[212:215],v[144:147]
buffer_load_dwordx4 a[100:103],v6,s[28:31],s87 offen offset:1024 sc1
v_mfma_f32_16x16x32_bf16 v[144:147],v[104:107],v[228:231],v[144:147]
v_mfma_f32_16x16x32_bf16 v[144:147],v[108:111],v[244:247],v[144:147]
v_mfma_f32_16x16x32_bf16 v[152:155],v[108:111],v[188:191],v[152:155]
v_mfma_f32_16x16x32_bf16 v[152:155],v[104:107],v[172:175],v[152:155]
v_mfma_f32_16x16x32_bf16 v[152:155],v[100:103],v[156:159],v[152:155]
buffer_load_dwordx4 a[64:67],v6,s[28:31],s87 offen offset:2048 sc1
v_mfma_f32_16x16x32_bf16 v[152:155],v[96:99],v[140:143],v[152:155]
s_waitcnt vmcnt(8)
s_barrier
s_waitcnt lgkmcnt(0)
v_mfma_f32_16x16x32_bf16 v[160:163],v[112:115],v[32:35],v[160:163]
ds_read_b64_tr_b16 v[96:97],v1
ds_read_b64_tr_b16 v[98:99],v1 offset:5120
v_mfma_f32_16x16x32_bf16 v[160:163],v[116:119],v[36:39],v[160:163]
ds_read_b64_tr_b16 v[100:101],v1 offset:10240
v_mfma_f32_16x16x32_bf16 v[160:163],v[120:123],v[40:43],v[160:163]
buffer_load_dwordx4 a[72:75],v6,s[28:31],s88 offen offset:2048 sc1
v_mfma_f32_16x16x32_bf16 v[160:163],v[124:127],v[44:47],v[160:163]
ds_read_b64_tr_b16 v[102:103],v1 offset:15360
ds_read_b64_tr_b16 v[104:105],v1 offset:20480
v_mfma_f32_16x16x32_bf16 v[168:171],v[124:127],v[180:183],v[168:171]
ds_read_b64_tr_b16 v[106:107],v1 offset:25600
v_mfma_f32_16x16x32_bf16 v[168:171],v[120:123],v[164:167],v[168:171]
ds_read_b64_tr_b16 v[108:109],v1 offset:30720
v_mfma_f32_16x16x32_bf16 v[168:171],v[116:119],v[148:151],v[168:171]
ds_read_b64_tr_b16 v[110:111],v1 offset:35840
v_add_u32 v1,0xa000,v1
v_mfma_f32_16x16x32_bf16 v[168:171],v[112:115],v[132:135],v[168:171]
buffer_load_dwordx4 a[80:83],v6,s[28:31],s89 offen offset:2048 sc1
v_mfma_f32_16x16x32_bf16 v[176:179],v[112:115],v[196:199],v[176:179]
v_mfma_f32_16x16x32_bf16 v[176:179],v[116:119],v[212:215],v[176:179]
v_mfma_f32_16x16x32_bf16 v[176:179],v[120:123],v[228:231],v[176:179]
v_mfma_f32_16x16x32_bf16 v[176:179],v[124:127],v[244:247],v[176:179]
v_mfma_f32_16x16x32_bf16 v[184:187],v[124:127],v[188:191],v[184:187]
buffer_load_dwordx4 a[88:91],v6,s[28:31],s90 offen offset:2048 sc1
v_mfma_f32_16x16x32_bf16 v[184:187],v[120:123],v[172:175],v[184:187]
ds_read_b64_tr_b16 v[120:121],v2 offset:20512
ds_read_b64_tr_b16 v[122:123],v2 offset:25632
v_mfma_f32_16x16x32_bf16 v[184:187],v[116:119],v[156:159],v[184:187]
ds_read_b64_tr_b16 v[116:117],v2 offset:10272
v_mfma_f32_16x16x32_bf16 v[184:187],v[112:115],v[140:143],v[184:187]
ds_read_b64_tr_b16 v[112:113],v2 offset:32
v_mfma_f32_16x16x32_bf16 a[192:195],v[64:67],a[128:131],a[192:195]
ds_read_b64_tr_b16 v[114:115],v2 offset:5152
v_mfma_f32_16x16x32_bf16 a[192:195],v[68:71],a[144:147],a[192:195]
buffer_load_dwordx4 a[120:123],v28,s[28:31],s90 offen offset:3072 sc1
v_mfma_f32_16x16x32_bf16 a[192:195],v[72:75],a[160:163],a[192:195]
ds_read_b64_tr_b16 v[118:119],v2 offset:15392
ds_read_b64_tr_b16 v[124:125],v2 offset:30752
v_mfma_f32_16x16x32_bf16 a[192:195],v[76:79],a[176:179],a[192:195]
ds_read_b64_tr_b16 v[126:127],v2 offset:35872
v_add_u32 v2,0xa000,v2
v_mfma_f32_16x16x32_bf16 a[196:199],v[76:79],a[180:183],a[196:199]
v_mfma_f32_16x16x32_bf16 a[196:199],v[72:75],a[164:167],a[196:199]
v_mfma_f32_16x16x32_bf16 a[196:199],v[68:71],a[148:151],a[196:199]
buffer_load_dwordx4 a[112:115],v28,s[28:31],s89 offen offset:3072 sc1
v_mfma_f32_16x16x32_bf16 a[196:199],v[64:67],a[132:135],a[196:199]
v_mfma_f32_16x16x32_bf16 a[200:203],v[64:67],a[136:139],a[200:203]
v_mfma_f32_16x16x32_bf16 a[200:203],v[68:71],a[152:155],a[200:203]
v_mfma_f32_16x16x32_bf16 a[200:203],v[72:75],a[168:171],a[200:203]
v_mfma_f32_16x16x32_bf16 a[200:203],v[76:79],a[184:187],a[200:203]
buffer_load_dwordx4 a[104:107],v28,s[28:31],s88 offen offset:3072 sc1
v_mfma_f32_16x16x32_bf16 a[204:207],v[76:79],a[188:191],a[204:207]
ds_read_b64_tr_b16 v[76:77],v1 offset:30784
ds_read_b64_tr_b16 v[78:79],v1 offset:35904
v_mfma_f32_16x16x32_bf16 a[204:207],v[72:75],a[172:175],a[204:207]
ds_read_b64_tr_b16 v[72:73],v1 offset:20544
v_mfma_f32_16x16x32_bf16 a[204:207],v[68:71],a[156:159],a[204:207]
ds_read_b64_tr_b16 v[68:69],v1 offset:10304
v_mfma_f32_16x16x32_bf16 a[204:207],v[64:67],a[140:143],a[204:207]
ds_read_b64_tr_b16 v[64:65],v1 offset:64
v_mfma_f32_16x16x32_bf16 a[208:211],v[80:83],a[128:131],a[208:211]
buffer_load_dwordx4 a[96:99],v28,s[28:31],s87 offen offset:3072 sc1
v_mfma_f32_16x16x32_bf16 a[208:211],v[84:87],a[144:147],a[208:211]
s_mul_i32 s87,s60,81920
ds_read_b64_tr_b16 v[66:67],v1 offset:5184
ds_read_b64_tr_b16 v[70:71],v1 offset:15424
v_mfma_f32_16x16x32_bf16 a[208:211],v[88:91],a[160:163],a[208:211]
s_add_u32 s87,s87,s66
ds_read_b64_tr_b16 v[74:75],v1 offset:25664
v_add_u32 v1,0xffff6000,v1
v_mfma_f32_16x16x32_bf16 a[208:211],v[92:95],a[176:179],a[208:211]
s_add_u32 s83,s83,s87
s_lshl_b32 s66,s63,6
s_add_u32 s87,s87,s66
v_mfma_f32_16x16x32_bf16 a[212:215],v[92:95],a[180:183],a[212:215]
s_add_u32 s88,s87,20480
s_add_u32 s89,s87,40960
s_add_u32 s90,s87,61440
v_mfma_f32_16x16x32_bf16 a[212:215],v[88:91],a[164:167],a[212:215]
s_add_u32 s66,s77,4096
v_mfma_f32_16x16x32_bf16 a[212:215],v[84:87],a[148:151],a[212:215]
v_mfma_f32_16x16x32_bf16 a[212:215],v[80:83],a[132:135],a[212:215]
v_mfma_f32_16x16x32_bf16 a[216:219],v[80:83],a[136:139],a[216:219]
v_mfma_f32_16x16x32_bf16 a[216:219],v[84:87],a[152:155],a[216:219]
v_mfma_f32_16x16x32_bf16 a[216:219],v[88:91],a[168:171],a[216:219]
v_mfma_f32_16x16x32_bf16 a[216:219],v[92:95],a[184:187],a[216:219]
v_mfma_f32_16x16x32_bf16 a[220:223],v[92:95],a[188:191],a[220:223]
ds_read_b64_tr_b16 v[92:93],v2 offset:30816
ds_read_b64_tr_b16 v[94:95],v2 offset:35936
v_mfma_f32_16x16x32_bf16 a[220:223],v[88:91],a[172:175],a[220:223]
ds_read_b64_tr_b16 v[88:89],v2 offset:20576
v_mfma_f32_16x16x32_bf16 a[220:223],v[84:87],a[156:159],a[220:223]
ds_read_b64_tr_b16 v[84:85],v2 offset:10336
v_mfma_f32_16x16x32_bf16 a[220:223],v[80:83],a[140:143],a[220:223]
s_waitcnt lgkmcnt(4)
ds_read_b64_tr_b16 v[80:81],v2 offset:96
v_mfma_f32_16x16x32_bf16 a[192:195],v[96:99],a[68:71],a[192:195]
s_waitcnt vmcnt(14)
ds_read_b64_tr_b16 v[82:83],v2 offset:5216
ds_read_b64_tr_b16 v[86:87],v2 offset:15456
v_mfma_f32_16x16x32_bf16 a[192:195],v[100:103],a[76:79],a[192:195]
s_waitcnt vmcnt(13)
ds_read_b64_tr_b16 v[90:91],v2 offset:25696
v_add_u32 v2,0xffff6000,v2
v_mfma_f32_16x16x32_bf16 a[192:195],v[104:107],a[84:87],a[192:195]
s_waitcnt vmcnt(12)
v_mfma_f32_16x16x32_bf16 a[192:195],v[108:111],a[92:95],a[192:195]
s_waitcnt vmcnt(11)
v_mfma_f32_16x16x32_bf16 a[196:199],v[108:111],a[124:127],a[196:199]
s_waitcnt vmcnt(10)
v_mfma_f32_16x16x32_bf16 a[196:199],v[104:107],a[116:119],a[196:199]
s_waitcnt vmcnt(9)
v_mfma_f32_16x16x32_bf16 a[196:199],v[100:103],a[108:111],a[196:199]
s_waitcnt vmcnt(8)
v_mfma_f32_16x16x32_bf16 a[196:199],v[96:99],a[100:103],a[196:199]
s_waitcnt vmcnt(7)
v_mfma_f32_16x16x32_bf16 a[200:203],v[96:99],a[64:67],a[200:203]
s_waitcnt vmcnt(6)
v_mfma_f32_16x16x32_bf16 a[200:203],v[100:103],a[72:75],a[200:203]
s_waitcnt vmcnt(5)
v_mfma_f32_16x16x32_bf16 a[200:203],v[104:107],a[80:83],a[200:203]
s_waitcnt vmcnt(4)
v_mfma_f32_16x16x32_bf16 a[200:203],v[108:111],a[88:91],a[200:203]
s_waitcnt vmcnt(3)
v_accvgpr_read_b32 v8,a192
)A"
R"A(v_accvgpr_read_b32 v9,a193
v_mfma_f32_16x16x32_bf16 a[204:207],v[108:111],a[120:123],a[204:207]
s_waitcnt vmcnt(2)
v_accvgpr_read_b32 v10,a194
v_accvgpr_read_b32 v11,a195
v_mfma_f32_16x16x32_bf16 a[204:207],v[104:107],a[112:115],a[204:207]
s_waitcnt vmcnt(1)
v_cvt_pk_bf16_f32 v8,v8,v9
v_cvt_pk_bf16_f32 v9,v10,v11
v_mfma_f32_16x16x32_bf16 a[204:207],v[100:103],a[104:107],a[204:207]
s_waitcnt vmcnt(0)
v_accvgpr_read_b32 v16,a196
v_accvgpr_read_b32 v17,a197
v_mfma_f32_16x16x32_bf16 a[204:207],v[96:99],a[96:99],a[204:207]
v_accvgpr_read_b32 v18,a198
v_accvgpr_read_b32 v19,a199
v_mfma_f32_16x16x32_bf16 a[208:211],v[112:115],a[68:71],a[208:211]
v_cvt_pk_bf16_f32 v16,v16,v17
v_cvt_pk_bf16_f32 v17,v18,v19
v_mfma_f32_16x16x32_bf16 a[208:211],v[116:119],a[76:79],a[208:211]
ds_read_b64_tr_b16 v[96:97],v1 offset:64
ds_read_b64_tr_b16 v[98:99],v1 offset:5184
v_mfma_f32_16x16x32_bf16 a[208:211],v[120:123],a[84:87],a[208:211]
ds_read_b64_tr_b16 v[100:101],v1 offset:10304
v_mfma_f32_16x16x32_bf16 a[208:211],v[124:127],a[92:95],a[208:211]
ds_read_b64_tr_b16 v[102:103],v1 offset:15424
v_mfma_f32_16x16x32_bf16 a[212:215],v[124:127],a[124:127],a[212:215]
ds_read_b64_tr_b16 v[104:105],v1 offset:20544
v_mfma_f32_16x16x32_bf16 a[212:215],v[120:123],a[116:119],a[212:215]
ds_read_b64_tr_b16 v[106:107],v1 offset:25664
ds_read_b64_tr_b16 v[108:109],v1 offset:30784
v_mfma_f32_16x16x32_bf16 a[212:215],v[116:119],a[108:111],a[212:215]
ds_read_b64_tr_b16 v[110:111],v1 offset:35904
v_add_u32 v1,0xa000,v1
v_mfma_f32_16x16x32_bf16 a[212:215],v[112:115],a[100:103],a[212:215]
v_accvgpr_read_b32 v12,a208
v_accvgpr_read_b32 v13,a209
v_mfma_f32_16x16x32_bf16 a[216:219],v[112:115],a[64:67],a[216:219]
v_accvgpr_read_b32 v14,a210
v_accvgpr_read_b32 v15,a211
v_mfma_f32_16x16x32_bf16 a[216:219],v[116:119],a[72:75],a[216:219]
v_cvt_pk_bf16_f32 v10,v12,v13
v_cvt_pk_bf16_f32 v11,v14,v15
v_mfma_f32_16x16x32_bf16 a[216:219],v[120:123],a[80:83],a[216:219]
buffer_store_dwordx4 v[8:11],v6,s[36:39],s74 offen sc1
v_mfma_f32_16x16x32_bf16 a[216:219],v[124:127],a[88:91],a[216:219]
v_accvgpr_read_b32 v20,a212
v_accvgpr_read_b32 v21,a213
v_mfma_f32_16x16x32_bf16 a[220:223],v[124:127],a[120:123],a[220:223]
v_accvgpr_read_b32 v22,a214
v_accvgpr_read_b32 v23,a215
v_mfma_f32_16x16x32_bf16 a[220:223],v[120:123],a[112:115],a[220:223]
v_cvt_pk_bf16_f32 v18,v20,v21
v_cvt_pk_bf16_f32 v19,v22,v23
v_mfma_f32_16x16x32_bf16 a[220:223],v[116:119],a[104:107],a[220:223]
v_accvgpr_read_b32 v8,a200
v_accvgpr_read_b32 v9,a201
v_mfma_f32_16x16x32_bf16 a[220:223],v[112:115],a[96:99],a[220:223]
v_accvgpr_read_b32 v10,a202
v_accvgpr_read_b32 v11,a203
v_mfma_f32_16x16x32_bf16 a[224:227],v[64:67],a[128:131],a[224:227]
v_accvgpr_read_b32 v12,a216
v_accvgpr_read_b32 v13,a217
v_mfma_f32_16x16x32_bf16 a[224:227],v[68:71],a[144:147],a[224:227]
v_accvgpr_read_b32 v14,a218
v_accvgpr_read_b32 v15,a219
v_mfma_f32_16x16x32_bf16 a[224:227],v[72:75],a[160:163],a[224:227]
v_cvt_pk_bf16_f32 v8,v8,v9
v_cvt_pk_bf16_f32 v9,v10,v11
v_mfma_f32_16x16x32_bf16 a[224:227],v[76:79],a[176:179],a[224:227]
v_cvt_pk_bf16_f32 v10,v12,v13
v_cvt_pk_bf16_f32 v11,v14,v15
v_mfma_f32_16x16x32_bf16 a[228:231],v[76:79],a[180:183],a[228:231]
ds_read_b64_tr_b16 v[112:113],v2 offset:96
ds_read_b64_tr_b16 v[114:115],v2 offset:5216
v_mfma_f32_16x16x32_bf16 a[228:231],v[72:75],a[164:167],a[228:231]
buffer_store_dwordx4 v[16:19],v6,s[36:39],s74 offen offset:1024 sc1
v_mfma_f32_16x16x32_bf16 a[228:231],v[68:71],a[148:151],a[228:231]
ds_read_b64_tr_b16 v[116:117],v2 offset:10336
ds_read_b64_tr_b16 v[118:119],v2 offset:15456
v_mfma_f32_16x16x32_bf16 a[228:231],v[64:67],a[132:135],a[228:231]
ds_read_b64_tr_b16 v[120:121],v2 offset:20576
v_accvgpr_read_b32 v16,a204
v_mfma_f32_16x16x32_bf16 a[232:235],v[64:67],a[136:139],a[232:235]
ds_read_b64_tr_b16 v[122:123],v2 offset:25696
v_accvgpr_read_b32 v17,a205
v_mfma_f32_16x16x32_bf16 a[232:235],v[68:71],a[152:155],a[232:235]
ds_read_b64_tr_b16 v[124:125],v2 offset:30816
v_accvgpr_read_b32 v18,a206
v_mfma_f32_16x16x32_bf16 a[232:235],v[72:75],a[168:171],a[232:235]
ds_read_b64_tr_b16 v[126:127],v2 offset:35936
v_accvgpr_read_b32 v19,a207
v_mfma_f32_16x16x32_bf16 a[232:235],v[76:79],a[184:187],a[232:235]
v_accvgpr_read_b32 v20,a220
v_accvgpr_read_b32 v21,a221
v_mfma_f32_16x16x32_bf16 a[236:239],v[76:79],a[188:191],a[236:239]
v_accvgpr_read_b32 v22,a222
v_accvgpr_read_b32 v23,a223
v_mfma_f32_16x16x32_bf16 a[236:239],v[72:75],a[172:175],a[236:239]
v_cvt_pk_bf16_f32 v16,v16,v17
v_cvt_pk_bf16_f32 v17,v18,v19
v_mfma_f32_16x16x32_bf16 a[236:239],v[68:71],a[156:159],a[236:239]
v_cvt_pk_bf16_f32 v18,v20,v21
v_cvt_pk_bf16_f32 v19,v22,v23
v_mfma_f32_16x16x32_bf16 a[236:239],v[64:67],a[140:143],a[236:239]
s_waitcnt lgkmcnt(3)
ds_read_b64_tr_b16 v[64:65],v1 offset:128
ds_read_b64_tr_b16 v[66:67],v1 offset:5248
v_mfma_f32_16x16x32_bf16 a[240:243],v[80:83],a[128:131],a[240:243]
buffer_store_dwordx4 v[8:11],v6,s[36:39],s74 offen offset:2048 sc1
v_mfma_f32_16x16x32_bf16 a[240:243],v[84:87],a[144:147],a[240:243]
ds_read_b64_tr_b16 v[68:69],v1 offset:10368
ds_read_b64_tr_b16 v[70:71],v1 offset:15488
v_mfma_f32_16x16x32_bf16 a[240:243],v[88:91],a[160:163],a[240:243]
ds_read_b64_tr_b16 v[72:73],v1 offset:20608
v_add_u32 v2,0xa000,v2
v_mfma_f32_16x16x32_bf16 a[240:243],v[92:95],a[176:179],a[240:243]
ds_read_b64_tr_b16 v[74:75],v1 offset:25728
v_mfma_f32_16x16x32_bf16 a[244:247],v[92:95],a[180:183],a[244:247]
ds_read_b64_tr_b16 v[76:77],v1 offset:30848
v_mfma_f32_16x16x32_bf16 a[244:247],v[88:91],a[164:167],a[244:247]
ds_read_b64_tr_b16 v[78:79],v1 offset:35968
v_add_u32 v1,0xffff6000,v1
v_mfma_f32_16x16x32_bf16 a[244:247],v[84:87],a[148:151],a[244:247]
v_mfma_f32_16x16x32_bf16 a[244:247],v[80:83],a[132:135],a[244:247]
v_mfma_f32_16x16x32_bf16 a[248:251],v[80:83],a[136:139],a[248:251]
v_mfma_f32_16x16x32_bf16 a[248:251],v[84:87],a[152:155],a[248:251]
v_mfma_f32_16x16x32_bf16 a[248:251],v[88:91],a[168:171],a[248:251]
v_mfma_f32_16x16x32_bf16 a[248:251],v[92:95],a[184:187],a[248:251]
buffer_store_dwordx4 v[16:19],v28,s[36:39],s74 offen offset:3072 sc1
v_mfma_f32_16x16x32_bf16 a[252:255],v[92:95],a[188:191],a[252:255]
ds_read_b64_tr_b16 v[92:93],v2 offset:30880
ds_read_b64_tr_b16 v[94:95],v2 offset:36000
v_mfma_f32_16x16x32_bf16 a[252:255],v[88:91],a[172:175],a[252:255]
ds_read_b64_tr_b16 v[88:89],v2 offset:20640
v_mfma_f32_16x16x32_bf16 a[252:255],v[84:87],a[156:159],a[252:255]
ds_read_b64_tr_b16 v[84:85],v2 offset:10400
v_mfma_f32_16x16x32_bf16 a[252:255],v[80:83],a[140:143],a[252:255]
ds_read_b64_tr_b16 v[80:81],v2 offset:160
v_mfma_f32_16x16x32_bf16 a[224:227],v[96:99],a[68:71],a[224:227]
ds_read_b64_tr_b16 v[82:83],v2 offset:5280
ds_read_b64_tr_b16 v[86:87],v2 offset:15520
v_mfma_f32_16x16x32_bf16 a[224:227],v[100:103],a[76:79],a[224:227]
ds_read_b64_tr_b16 v[90:91],v2 offset:25760
v_add_u32 v2,0xffff6000,v2
v_mfma_f32_16x16x32_bf16 a[224:227],v[104:107],a[84:87],a[224:227]
v_mfma_f32_16x16x32_bf16 a[224:227],v[108:111],a[92:95],a[224:227]
v_mfma_f32_16x16x32_bf16 a[228:231],v[108:111],a[124:127],a[228:231]
v_mfma_f32_16x16x32_bf16 a[228:231],v[104:107],a[116:119],a[228:231]
v_mfma_f32_16x16x32_bf16 a[228:231],v[100:103],a[108:111],a[228:231]
v_mfma_f32_16x16x32_bf16 a[228:231],v[96:99],a[100:103],a[228:231]
v_mfma_f32_16x16x32_bf16 a[232:235],v[96:99],a[64:67],a[232:235]
v_mfma_f32_16x16x32_bf16 a[232:235],v[100:103],a[72:75],a[232:235]
v_mfma_f32_16x16x32_bf16 a[232:235],v[104:107],a[80:83],a[232:235]
v_mfma_f32_16x16x32_bf16 a[232:235],v[108:111],a[88:91],a[232:235]
v_accvgpr_read_b32 v8,a224
v_accvgpr_read_b32 v9,a225
v_mfma_f32_16x16x32_bf16 a[236:239],v[108:111],a[120:123],a[236:239]
v_accvgpr_read_b32 v10,a226
v_accvgpr_read_b32 v11,a227
v_mfma_f32_16x16x32_bf16 a[236:239],v[104:107],a[112:115],a[236:239]
v_cvt_pk_bf16_f32 v8,v8,v9
v_cvt_pk_bf16_f32 v9,v10,v11
v_mfma_f32_16x16x32_bf16 a[236:239],v[100:103],a[104:107],a[236:239]
v_accvgpr_read_b32 v16,a228
v_accvgpr_read_b32 v17,a229
v_mfma_f32_16x16x32_bf16 a[236:239],v[96:99],a[96:99],a[236:239]
v_accvgpr_read_b32 v18,a230
v_accvgpr_read_b32 v19,a231
v_mfma_f32_16x16x32_bf16 a[240:243],v[112:115],a[68:71],a[240:243]
v_cvt_pk_bf16_f32 v16,v16,v17
v_cvt_pk_bf16_f32 v17,v18,v19
v_mfma_f32_16x16x32_bf16 a[240:243],v[116:119],a[76:79],a[240:243]
s_waitcnt lgkmcnt(0)
ds_read_b64_tr_b16 v[96:97],v1 offset:128
ds_read_b64_tr_b16 v[98:99],v1 offset:5248
v_mfma_f32_16x16x32_bf16 a[240:243],v[120:123],a[84:87],a[240:243]
ds_read_b64_tr_b16 v[100:101],v1 offset:10368
v_mfma_f32_16x16x32_bf16 a[240:243],v[124:127],a[92:95],a[240:243]
ds_read_b64_tr_b16 v[102:103],v1 offset:15488
v_mfma_f32_16x16x32_bf16 a[244:247],v[124:127],a[124:127],a[244:247]
ds_read_b64_tr_b16 v[104:105],v1 offset:20608
v_mfma_f32_16x16x32_bf16 a[244:247],v[120:123],a[116:119],a[244:247]
ds_read_b64_tr_b16 v[106:107],v1 offset:25728
ds_read_b64_tr_b16 v[108:109],v1 offset:30848
v_mfma_f32_16x16x32_bf16 a[244:247],v[116:119],a[108:111],a[244:247]
ds_read_b64_tr_b16 v[110:111],v1 offset:35968
v_add_u32 v1,0xa000,v1
v_mfma_f32_16x16x32_bf16 a[244:247],v[112:115],a[100:103],a[244:247]
v_accvgpr_read_b32 v12,a240
v_accvgpr_read_b32 v13,a241
v_mfma_f32_16x16x32_bf16 a[248:251],v[112:115],a[64:67],a[248:251]
v_accvgpr_read_b32 v14,a242
v_accvgpr_read_b32 v15,a243
v_mfma_f32_16x16x32_bf16 a[248:251],v[116:119],a[72:75],a[248:251]
v_cvt_pk_bf16_f32 v10,v12,v13
v_cvt_pk_bf16_f32 v11,v14,v15
v_mfma_f32_16x16x32_bf16 a[248:251],v[120:123],a[80:83],a[248:251]
buffer_store_dwordx4 v[8:11],v6,s[36:39],s75 offen sc1
v_mfma_f32_16x16x32_bf16 a[248:251],v[124:127],a[88:91],a[248:251]
v_accvgpr_read_b32 v20,a244
v_accvgpr_read_b32 v21,a245
v_mfma_f32_16x16x32_bf16 a[252:255],v[124:127],a[120:123],a[252:255]
v_accvgpr_read_b32 v22,a246
v_accvgpr_read_b32 v23,a247
v_mfma_f32_16x16x32_bf16 a[252:255],v[120:123],a[112:115],a[252:255]
v_cvt_pk_bf16_f32 v18,v20,v21
v_cvt_pk_bf16_f32 v19,v22,v23
v_mfma_f32_16x16x32_bf16 a[252:255],v[116:119],a[104:107],a[252:255]
v_accvgpr_read_b32 v8,a232
v_accvgpr_read_b32 v9,a233
v_mfma_f32_16x16x32_bf16 a[252:255],v[112:115],a[96:99],a[252:255]
v_accvgpr_read_b32 v10,a234
v_accvgpr_read_b32 v11,a235
v_mfma_f32_16x16x32_bf16 a[0:3],v[64:67],a[128:131],a[0:3]
v_accvgpr_read_b32 v12,a248
v_accvgpr_read_b32 v13,a249
v_mfma_f32_16x16x32_bf16 a[0:3],v[68:71],a[144:147],a[0:3]
v_accvgpr_read_b32 v14,a250
v_accvgpr_read_b32 v15,a251
v_mfma_f32_16x16x32_bf16 a[0:3],v[72:75],a[160:163],a[0:3]
v_cvt_pk_bf16_f32 v8,v8,v9
v_cvt_pk_bf16_f32 v9,v10,v11
v_mfma_f32_16x16x32_bf16 a[0:3],v[76:79],a[176:179],a[0:3]
v_cvt_pk_bf16_f32 v10,v12,v13
v_cvt_pk_bf16_f32 v11,v14,v15
v_mfma_f32_16x16x32_bf16 a[4:7],v[76:79],a[180:183],a[4:7]
ds_read_b64_tr_b16 v[112:113],v2 offset:160
ds_read_b64_tr_b16 v[114:115],v2 offset:5280
v_mfma_f32_16x16x32_bf16 a[4:7],v[72:75],a[164:167],a[4:7]
buffer_store_dwordx4 v[16:19],v6,s[36:39],s75 offen offset:1024 sc1
v_mfma_f32_16x16x32_bf16 a[4:7],v[68:71],a[148:151],a[4:7]
ds_read_b64_tr_b16 v[116:117],v2 offset:10400
ds_read_b64_tr_b16 v[118:119],v2 offset:15520
v_mfma_f32_16x16x32_bf16 a[4:7],v[64:67],a[132:135],a[4:7]
ds_read_b64_tr_b16 v[120:121],v2 offset:20640
v_accvgpr_read_b32 v16,a236
v_mfma_f32_16x16x32_bf16 a[8:11],v[64:67],a[136:139],a[8:11]
ds_read_b64_tr_b16 v[122:123],v2 offset:25760
v_accvgpr_read_b32 v17,a237
v_mfma_f32_16x16x32_bf16 a[8:11],v[68:71],a[152:155],a[8:11]
ds_read_b64_tr_b16 v[124:125],v2 offset:30880
v_accvgpr_read_b32 v18,a238
v_mfma_f32_16x16x32_bf16 a[8:11],v[72:75],a[168:171],a[8:11]
ds_read_b64_tr_b16 v[126:127],v2 offset:36000
v_accvgpr_read_b32 v19,a239
v_mfma_f32_16x16x32_bf16 a[8:11],v[76:79],a[184:187],a[8:11]
v_accvgpr_read_b32 v20,a252
v_accvgpr_read_b32 v21,a253
v_mfma_f32_16x16x32_bf16 a[12:15],v[76:79],a[188:191],a[12:15]
v_accvgpr_read_b32 v22,a254
v_accvgpr_read_b32 v23,a255
v_mfma_f32_16x16x32_bf16 a[12:15],v[72:75],a[172:175],a[12:15]
v_cvt_pk_bf16_f32 v16,v16,v17
v_cvt_pk_bf16_f32 v17,v18,v19
v_mfma_f32_16x16x32_bf16 a[12:15],v[68:71],a[156:159],a[12:15]
v_cvt_pk_bf16_f32 v18,v20,v21
v_cvt_pk_bf16_f32 v19,v22,v23
)A"
R"A(v_mfma_f32_16x16x32_bf16 a[12:15],v[64:67],a[140:143],a[12:15]
ds_read_b64_tr_b16 v[64:65],v1 offset:192
ds_read_b64_tr_b16 v[66:67],v1 offset:5312
v_mfma_f32_16x16x32_bf16 a[16:19],v[80:83],a[128:131],a[16:19]
buffer_store_dwordx4 v[8:11],v6,s[36:39],s75 offen offset:2048 sc1
v_mfma_f32_16x16x32_bf16 a[16:19],v[84:87],a[144:147],a[16:19]
ds_read_b64_tr_b16 v[68:69],v1 offset:10432
ds_read_b64_tr_b16 v[70:71],v1 offset:15552
v_mfma_f32_16x16x32_bf16 a[16:19],v[88:91],a[160:163],a[16:19]
ds_read_b64_tr_b16 v[72:73],v1 offset:20672
v_add_u32 v2,0xa000,v2
v_mfma_f32_16x16x32_bf16 a[16:19],v[92:95],a[176:179],a[16:19]
ds_read_b64_tr_b16 v[74:75],v1 offset:25792
v_mfma_f32_16x16x32_bf16 a[20:23],v[92:95],a[180:183],a[20:23]
ds_read_b64_tr_b16 v[76:77],v1 offset:30912
v_mfma_f32_16x16x32_bf16 a[20:23],v[88:91],a[164:167],a[20:23]
ds_read_b64_tr_b16 v[78:79],v1 offset:36032
v_add_u32 v1,0xffff6000,v1
v_mfma_f32_16x16x32_bf16 a[20:23],v[84:87],a[148:151],a[20:23]
v_mfma_f32_16x16x32_bf16 a[20:23],v[80:83],a[132:135],a[20:23]
v_mfma_f32_16x16x32_bf16 a[24:27],v[80:83],a[136:139],a[24:27]
v_mfma_f32_16x16x32_bf16 a[24:27],v[84:87],a[152:155],a[24:27]
v_mfma_f32_16x16x32_bf16 a[24:27],v[88:91],a[168:171],a[24:27]
v_mfma_f32_16x16x32_bf16 a[24:27],v[92:95],a[184:187],a[24:27]
buffer_store_dwordx4 v[16:19],v28,s[36:39],s75 offen offset:3072 sc1
v_mfma_f32_16x16x32_bf16 a[28:31],v[92:95],a[188:191],a[28:31]
ds_read_b64_tr_b16 v[92:93],v2 offset:30944
ds_read_b64_tr_b16 v[94:95],v2 offset:36064
v_mfma_f32_16x16x32_bf16 a[28:31],v[88:91],a[172:175],a[28:31]
ds_read_b64_tr_b16 v[88:89],v2 offset:20704
v_mfma_f32_16x16x32_bf16 a[28:31],v[84:87],a[156:159],a[28:31]
ds_read_b64_tr_b16 v[84:85],v2 offset:10464
v_mfma_f32_16x16x32_bf16 a[28:31],v[80:83],a[140:143],a[28:31]
s_waitcnt lgkmcnt(4)
ds_read_b64_tr_b16 v[80:81],v2 offset:224
v_mfma_f32_16x16x32_bf16 a[0:3],v[96:99],a[68:71],a[0:3]
ds_read_b64_tr_b16 v[82:83],v2 offset:5344
ds_read_b64_tr_b16 v[86:87],v2 offset:15584
v_mfma_f32_16x16x32_bf16 a[0:3],v[100:103],a[76:79],a[0:3]
ds_read_b64_tr_b16 v[90:91],v2 offset:25824
v_add_u32 v2,0xffff6000,v2
v_mfma_f32_16x16x32_bf16 a[0:3],v[104:107],a[84:87],a[0:3]
v_mfma_f32_16x16x32_bf16 a[0:3],v[108:111],a[92:95],a[0:3]
v_mfma_f32_16x16x32_bf16 a[4:7],v[108:111],a[124:127],a[4:7]
v_mfma_f32_16x16x32_bf16 a[4:7],v[104:107],a[116:119],a[4:7]
v_mfma_f32_16x16x32_bf16 a[4:7],v[100:103],a[108:111],a[4:7]
v_mfma_f32_16x16x32_bf16 a[4:7],v[96:99],a[100:103],a[4:7]
v_mfma_f32_16x16x32_bf16 a[8:11],v[96:99],a[64:67],a[8:11]
v_mfma_f32_16x16x32_bf16 a[8:11],v[100:103],a[72:75],a[8:11]
v_mfma_f32_16x16x32_bf16 a[8:11],v[104:107],a[80:83],a[8:11]
v_mfma_f32_16x16x32_bf16 a[8:11],v[108:111],a[88:91],a[8:11]
v_accvgpr_read_b32 v8,a0
v_accvgpr_read_b32 v9,a1
v_mfma_f32_16x16x32_bf16 a[12:15],v[108:111],a[120:123],a[12:15]
v_accvgpr_read_b32 v10,a2
v_accvgpr_read_b32 v11,a3
v_mfma_f32_16x16x32_bf16 a[12:15],v[104:107],a[112:115],a[12:15]
v_cvt_pk_bf16_f32 v8,v8,v9
v_cvt_pk_bf16_f32 v9,v10,v11
v_mfma_f32_16x16x32_bf16 a[12:15],v[100:103],a[104:107],a[12:15]
v_accvgpr_read_b32 v16,a4
v_accvgpr_read_b32 v17,a5
v_mfma_f32_16x16x32_bf16 a[12:15],v[96:99],a[96:99],a[12:15]
v_accvgpr_read_b32 v18,a6
v_accvgpr_read_b32 v19,a7
v_mfma_f32_16x16x32_bf16 a[16:19],v[112:115],a[68:71],a[16:19]
v_cvt_pk_bf16_f32 v16,v16,v17
v_cvt_pk_bf16_f32 v17,v18,v19
v_mfma_f32_16x16x32_bf16 a[16:19],v[116:119],a[76:79],a[16:19]
ds_read_b64_tr_b16 v[96:97],v1 offset:192
ds_read_b64_tr_b16 v[98:99],v1 offset:5312
v_mfma_f32_16x16x32_bf16 a[16:19],v[120:123],a[84:87],a[16:19]
ds_read_b64_tr_b16 v[100:101],v1 offset:10432
v_mfma_f32_16x16x32_bf16 a[16:19],v[124:127],a[92:95],a[16:19]
ds_read_b64_tr_b16 v[102:103],v1 offset:15552
v_mfma_f32_16x16x32_bf16 a[20:23],v[124:127],a[124:127],a[20:23]
ds_read_b64_tr_b16 v[104:105],v1 offset:20672
v_mfma_f32_16x16x32_bf16 a[20:23],v[120:123],a[116:119],a[20:23]
ds_read_b64_tr_b16 v[106:107],v1 offset:25792
ds_read_b64_tr_b16 v[108:109],v1 offset:30912
v_mfma_f32_16x16x32_bf16 a[20:23],v[116:119],a[108:111],a[20:23]
ds_read_b64_tr_b16 v[110:111],v1 offset:36032
v_add_u32 v1,0x14000,v1
v_mfma_f32_16x16x32_bf16 a[20:23],v[112:115],a[100:103],a[20:23]
v_accvgpr_read_b32 v12,a16
v_accvgpr_read_b32 v13,a17
v_mfma_f32_16x16x32_bf16 a[24:27],v[112:115],a[64:67],a[24:27]
v_accvgpr_read_b32 v14,a18
v_accvgpr_read_b32 v15,a19
v_mfma_f32_16x16x32_bf16 a[24:27],v[116:119],a[72:75],a[24:27]
v_cvt_pk_bf16_f32 v10,v12,v13
v_cvt_pk_bf16_f32 v11,v14,v15
s_nop 0
L_pd23_%=:
s_cbranch_execz L_trtg2S_%=
v_mfma_f32_16x16x32_bf16 a[24:27],v[120:123],a[80:83],a[24:27]
buffer_store_dwordx4 v[8:11],v6,s[36:39],s76 offen sc1
v_mfma_f32_16x16x32_bf16 a[24:27],v[124:127],a[88:91],a[24:27]
v_accvgpr_read_b32 v20,a20
v_accvgpr_read_b32 v21,a21
v_mfma_f32_16x16x32_bf16 a[28:31],v[124:127],a[120:123],a[28:31]
v_accvgpr_read_b32 v22,a22
v_accvgpr_read_b32 v23,a23
v_mfma_f32_16x16x32_bf16 a[28:31],v[120:123],a[112:115],a[28:31]
v_cvt_pk_bf16_f32 v18,v20,v21
v_cvt_pk_bf16_f32 v19,v22,v23
v_mfma_f32_16x16x32_bf16 a[28:31],v[116:119],a[104:107],a[28:31]
v_accvgpr_read_b32 v8,a8
v_accvgpr_read_b32 v9,a9
v_mfma_f32_16x16x32_bf16 a[28:31],v[112:115],a[96:99],a[28:31]
v_accvgpr_read_b32 v10,a10
v_accvgpr_read_b32 v11,a11
v_mfma_f32_16x16x32_bf16 v[128:131],v[64:67],a[128:131],v[128:131]
v_accvgpr_read_b32 v12,a24
v_accvgpr_read_b32 v13,a25
v_mfma_f32_16x16x32_bf16 v[128:131],v[68:71],a[144:147],v[128:131]
v_accvgpr_read_b32 v14,a26
v_accvgpr_read_b32 v15,a27
v_mfma_f32_16x16x32_bf16 v[128:131],v[72:75],a[160:163],v[128:131]
v_cvt_pk_bf16_f32 v8,v8,v9
v_cvt_pk_bf16_f32 v9,v10,v11
v_mfma_f32_16x16x32_bf16 v[128:131],v[76:79],a[176:179],v[128:131]
v_cvt_pk_bf16_f32 v10,v12,v13
v_cvt_pk_bf16_f32 v11,v14,v15
v_mfma_f32_16x16x32_bf16 v[136:139],v[76:79],a[180:183],v[136:139]
ds_read_b64_tr_b16 v[112:113],v2 offset:224
ds_read_b64_tr_b16 v[114:115],v2 offset:5344
v_mfma_f32_16x16x32_bf16 v[136:139],v[72:75],a[164:167],v[136:139]
buffer_store_dwordx4 v[16:19],v6,s[36:39],s76 offen offset:1024 sc1
v_mfma_f32_16x16x32_bf16 v[136:139],v[68:71],a[148:151],v[136:139]
ds_read_b64_tr_b16 v[116:117],v2 offset:10464
ds_read_b64_tr_b16 v[118:119],v2 offset:15584
v_mfma_f32_16x16x32_bf16 v[136:139],v[64:67],a[132:135],v[136:139]
ds_read_b64_tr_b16 v[120:121],v2 offset:20704
v_accvgpr_read_b32 v16,a12
v_mfma_f32_16x16x32_bf16 v[144:147],v[64:67],a[136:139],v[144:147]
ds_read_b64_tr_b16 v[122:123],v2 offset:25824
v_accvgpr_read_b32 v17,a13
v_mfma_f32_16x16x32_bf16 v[144:147],v[68:71],a[152:155],v[144:147]
ds_read_b64_tr_b16 v[124:125],v2 offset:30944
v_accvgpr_read_b32 v18,a14
v_mfma_f32_16x16x32_bf16 v[144:147],v[72:75],a[168:171],v[144:147]
ds_read_b64_tr_b16 v[126:127],v2 offset:36064
v_accvgpr_read_b32 v19,a15
v_mfma_f32_16x16x32_bf16 v[144:147],v[76:79],a[184:187],v[144:147]
v_accvgpr_read_b32 v20,a28
v_accvgpr_read_b32 v21,a29
v_mfma_f32_16x16x32_bf16 v[152:155],v[76:79],a[188:191],v[152:155]
v_accvgpr_read_b32 v22,a30
v_accvgpr_read_b32 v23,a31
v_mfma_f32_16x16x32_bf16 v[152:155],v[72:75],a[172:175],v[152:155]
v_cvt_pk_bf16_f32 v16,v16,v17
v_cvt_pk_bf16_f32 v17,v18,v19
v_mfma_f32_16x16x32_bf16 v[152:155],v[68:71],a[156:159],v[152:155]
v_cvt_pk_bf16_f32 v18,v20,v21
v_cvt_pk_bf16_f32 v19,v22,v23
v_mfma_f32_16x16x32_bf16 v[152:155],v[64:67],a[140:143],v[152:155]
s_waitcnt lgkmcnt(3)
ds_read_b64_tr_b16 v[64:65],v1 offset:256
ds_read_b64_tr_b16 v[66:67],v1 offset:5376
v_mfma_f32_16x16x32_bf16 v[160:163],v[80:83],a[128:131],v[160:163]
buffer_store_dwordx4 v[8:11],v6,s[36:39],s76 offen offset:2048 sc1
v_mfma_f32_16x16x32_bf16 v[160:163],v[84:87],a[144:147],v[160:163]
ds_read_b64_tr_b16 v[68:69],v1 offset:10496
ds_read_b64_tr_b16 v[70:71],v1 offset:15616
v_mfma_f32_16x16x32_bf16 v[160:163],v[88:91],a[160:163],v[160:163]
ds_read_b64_tr_b16 v[72:73],v1 offset:20736
v_add_u32 v2,0x14000,v2
v_mfma_f32_16x16x32_bf16 v[160:163],v[92:95],a[176:179],v[160:163]
ds_read_b64_tr_b16 v[74:75],v1 offset:25856
v_mov_b32 v20,s52
v_mfma_f32_16x16x32_bf16 v[168:171],v[92:95],a[180:183],v[168:171]
ds_read_b64_tr_b16 v[76:77],v1 offset:30976
v_mov_b32 v21,s53
v_mfma_f32_16x16x32_bf16 v[168:171],v[88:91],a[164:167],v[168:171]
ds_read_b64_tr_b16 v[78:79],v1 offset:36096
v_add_u32 v1,0xffff6000,v1
v_mfma_f32_16x16x32_bf16 v[168:171],v[84:87],a[148:151],v[168:171]
v_lshlrev_b32 v22,20,v6
v_mfma_f32_16x16x32_bf16 v[168:171],v[80:83],a[132:135],v[168:171]
v_mfma_f32_16x16x32_bf16 v[176:179],v[80:83],a[136:139],v[176:179]
v_mfma_f32_16x16x32_bf16 v[176:179],v[84:87],a[152:155],v[176:179]
v_mfma_f32_16x16x32_bf16 v[176:179],v[88:91],a[168:171],v[176:179]
v_mfma_f32_16x16x32_bf16 v[176:179],v[92:95],a[184:187],v[176:179]
buffer_store_dwordx4 v[16:19],v28,s[36:39],s76 offen offset:3072 sc1
v_mfma_f32_16x16x32_bf16 v[184:187],v[92:95],a[188:191],v[184:187]
ds_read_b64_tr_b16 v[92:93],v2 offset:31008
ds_read_b64_tr_b16 v[94:95],v2 offset:36128
v_mfma_f32_16x16x32_bf16 v[184:187],v[88:91],a[172:175],v[184:187]
ds_read_b64_tr_b16 v[88:89],v2 offset:20768
v_mfma_f32_16x16x32_bf16 v[184:187],v[84:87],a[156:159],v[184:187]
ds_read_b64_tr_b16 v[84:85],v2 offset:10528
v_mfma_f32_16x16x32_bf16 v[184:187],v[80:83],a[140:143],v[184:187]
ds_read_b64_tr_b16 v[80:81],v2 offset:288
v_mfma_f32_16x16x32_bf16 v[128:131],v[96:99],a[68:71],v[128:131]
ds_read_b64_tr_b16 v[82:83],v2 offset:5408
ds_read_b64_tr_b16 v[86:87],v2 offset:15648
v_mfma_f32_16x16x32_bf16 v[128:131],v[100:103],a[76:79],v[128:131]
ds_read_b64_tr_b16 v[90:91],v2 offset:25888
v_add_u32 v2,0xffff6000,v2
v_mfma_f32_16x16x32_bf16 v[128:131],v[104:107],a[84:87],v[128:131]
v_mfma_f32_16x16x32_bf16 v[128:131],v[108:111],a[92:95],v[128:131]
v_mfma_f32_16x16x32_bf16 v[136:139],v[108:111],a[124:127],v[136:139]
v_mfma_f32_16x16x32_bf16 v[136:139],v[104:107],a[116:119],v[136:139]
v_mfma_f32_16x16x32_bf16 v[136:139],v[100:103],a[108:111],v[136:139]
v_mfma_f32_16x16x32_bf16 v[136:139],v[96:99],a[100:103],v[136:139]
v_mfma_f32_16x16x32_bf16 v[144:147],v[96:99],a[64:67],v[144:147]
v_mfma_f32_16x16x32_bf16 v[144:147],v[100:103],a[72:75],v[144:147]
v_mfma_f32_16x16x32_bf16 v[144:147],v[104:107],a[80:83],v[144:147]
v_mfma_f32_16x16x32_bf16 v[144:147],v[108:111],a[88:91],v[144:147]
v_cvt_pk_bf16_f32 v128,v128,v129
v_cvt_pk_bf16_f32 v129,v130,v131
v_mfma_f32_16x16x32_bf16 v[152:155],v[108:111],a[120:123],v[152:155]
ds_read_b64_tr_b16 v[108:109],v1 offset:30976
ds_read_b64_tr_b16 v[110:111],v1 offset:36096
v_mfma_f32_16x16x32_bf16 v[152:155],v[104:107],a[112:115],v[152:155]
v_cvt_pk_bf16_f32 v136,v136,v137
v_cvt_pk_bf16_f32 v137,v138,v139
v_mfma_f32_16x16x32_bf16 v[152:155],v[100:103],a[104:107],v[152:155]
v_cvt_pk_bf16_f32 v144,v144,v145
v_cvt_pk_bf16_f32 v145,v146,v147
v_mfma_f32_16x16x32_bf16 v[152:155],v[96:99],a[96:99],v[152:155]
ds_read_b64_tr_b16 v[96:97],v1 offset:256
ds_read_b64_tr_b16 v[98:99],v1 offset:5376
v_mfma_f32_16x16x32_bf16 v[160:163],v[112:115],a[68:71],v[160:163]
ds_read_b64_tr_b16 v[100:101],v1 offset:10496
v_mfma_f32_16x16x32_bf16 v[160:163],v[116:119],a[76:79],v[160:163]
s_waitcnt lgkmcnt(5)
ds_read_b64_tr_b16 v[102:103],v1 offset:15616
v_mfma_f32_16x16x32_bf16 v[160:163],v[120:123],a[84:87],v[160:163]
ds_read_b64_tr_b16 v[104:105],v1 offset:20736
v_mfma_f32_16x16x32_bf16 v[160:163],v[124:127],a[92:95],v[160:163]
v_cvt_pk_bf16_f32 v152,v152,v153
v_cvt_pk_bf16_f32 v153,v154,v155
v_mfma_f32_16x16x32_bf16 v[168:171],v[124:127],a[124:127],v[168:171]
ds_read_b64_tr_b16 v[106:107],v1 offset:25856
v_add_u32 v1,0xffff6000,v1
v_mfma_f32_16x16x32_bf16 v[168:171],v[120:123],a[116:119],v[168:171]
v_mfma_f32_16x16x32_bf16 v[168:171],v[116:119],a[108:111],v[168:171]
v_mfma_f32_16x16x32_bf16 v[168:171],v[112:115],a[100:103],v[168:171]
v_cvt_pk_bf16_f32 v130,v160,v161
v_cvt_pk_bf16_f32 v131,v162,v163
v_mfma_f32_16x16x32_bf16 v[176:179],v[112:115],a[64:67],v[176:179]
buffer_store_dwordx4 v[128:131],v6,s[36:39],s79 offen sc1
v_mfma_f32_16x16x32_bf16 v[176:179],v[116:119],a[72:75],v[176:179]
v_mfma_f32_16x16x32_bf16 v[176:179],v[120:123],a[80:83],v[176:179]
v_mfma_f32_16x16x32_bf16 v[176:179],v[124:127],a[88:91],v[176:179]
)A"
R"A(v_mfma_f32_16x16x32_bf16 v[184:187],v[124:127],a[120:123],v[184:187]
v_cvt_pk_bf16_f32 v138,v168,v169
v_cvt_pk_bf16_f32 v139,v170,v171
v_mfma_f32_16x16x32_bf16 v[184:187],v[120:123],a[112:115],v[184:187]
buffer_store_dwordx4 v[136:139],v6,s[36:39],s80 offen sc1
v_mfma_f32_16x16x32_bf16 v[184:187],v[116:119],a[104:107],v[184:187]
ds_read_b64_tr_b16 v[116:117],v2 offset:10528
ds_read_b64_tr_b16 v[118:119],v2 offset:15648
v_mfma_f32_16x16x32_bf16 v[184:187],v[112:115],a[96:99],v[184:187]
v_cvt_pk_bf16_f32 v146,v176,v177
v_cvt_pk_bf16_f32 v147,v178,v179
s_waitcnt vmcnt(6)
s_barrier
v_mfma_f32_16x16x32_bf16 v[192:195],v[64:67],v[32:35],v[192:195]
buffer_store_dwordx4 v[144:147],v6,s[36:39],s81 offen sc1
v_mfma_f32_16x16x32_bf16 v[192:195],v[68:71],v[36:39],v[192:195]
ds_read_b64_tr_b16 v[112:113],v2 offset:288
ds_read_b64_tr_b16 v[114:115],v2 offset:5408
v_mfma_f32_16x16x32_bf16 v[192:195],v[72:75],v[40:43],v[192:195]
buffer_store_dwordx2 v[58:59],v60,s[44:47],s18 offen offset:2312 sc1
v_mfma_f32_16x16x32_bf16 v[192:195],v[76:79],v[44:47],v[192:195]
v_cvt_pk_bf16_f32 v154,v184,v185
v_cvt_pk_bf16_f32 v155,v186,v187
v_mfma_f32_16x16x32_bf16 v[200:203],v[76:79],v[180:183],v[200:203]
buffer_store_dwordx4 v[152:155],v28,s[36:39],s82 offen sc1
v_mfma_f32_16x16x32_bf16 v[200:203],v[72:75],v[164:167],v[200:203]
ds_read_b64_tr_b16 v[120:121],v2 offset:20768
ds_read_b64_tr_b16 v[122:123],v2 offset:25888
v_mfma_f32_16x16x32_bf16 v[200:203],v[68:71],v[148:151],v[200:203]
ds_read_b64_tr_b16 v[124:125],v2 offset:31008
v_mfma_f32_16x16x32_bf16 v[200:203],v[64:67],v[132:135],v[200:203]
ds_read_b64_tr_b16 v[126:127],v2 offset:36128
v_add_u32 v2,0xffff6000,v2
v_mfma_f32_16x16x32_bf16 v[208:211],v[64:67],v[196:199],v[208:211]
v_mfma_f32_16x16x32_bf16 v[208:211],v[68:71],v[212:215],v[208:211]
v_mfma_f32_16x16x32_bf16 v[208:211],v[72:75],v[228:231],v[208:211]
v_mfma_f32_16x16x32_bf16 v[208:211],v[76:79],v[244:247],v[208:211]
v_mfma_f32_16x16x32_bf16 v[216:219],v[76:79],v[188:191],v[216:219]
ds_read_b64_tr_b16 v[76:77],v1 offset:30976
ds_read_b64_tr_b16 v[78:79],v1 offset:36096
v_mfma_f32_16x16x32_bf16 v[216:219],v[72:75],v[172:175],v[216:219]
ds_read_b64_tr_b16 v[72:73],v1 offset:20736
v_mfma_f32_16x16x32_bf16 v[216:219],v[68:71],v[156:159],v[216:219]
ds_read_b64_tr_b16 v[68:69],v1 offset:10496
v_mfma_f32_16x16x32_bf16 v[216:219],v[64:67],v[140:143],v[216:219]
ds_read_b64_tr_b16 v[64:65],v1 offset:256
v_mfma_f32_16x16x32_bf16 v[224:227],v[80:83],v[32:35],v[224:227]
ds_read_b64_tr_b16 v[66:67],v1 offset:5376
ds_read_b64_tr_b16 v[70:71],v1 offset:15616
v_mfma_f32_16x16x32_bf16 v[224:227],v[84:87],v[36:39],v[224:227]
ds_read_b64_tr_b16 v[74:75],v1 offset:25856
v_mfma_f32_16x16x32_bf16 v[224:227],v[88:91],v[40:43],v[224:227]
v_mfma_f32_16x16x32_bf16 v[224:227],v[92:95],v[44:47],v[224:227]
v_mfma_f32_16x16x32_bf16 v[232:235],v[92:95],v[180:183],v[232:235]
v_mfma_f32_16x16x32_bf16 v[232:235],v[88:91],v[164:167],v[232:235]
v_mfma_f32_16x16x32_bf16 v[232:235],v[84:87],v[148:151],v[232:235]
v_mfma_f32_16x16x32_bf16 v[232:235],v[80:83],v[132:135],v[232:235]
v_mfma_f32_16x16x32_bf16 v[240:243],v[80:83],v[196:199],v[240:243]
v_mfma_f32_16x16x32_bf16 v[240:243],v[84:87],v[212:215],v[240:243]
v_mfma_f32_16x16x32_bf16 v[240:243],v[88:91],v[228:231],v[240:243]
v_mfma_f32_16x16x32_bf16 v[240:243],v[92:95],v[244:247],v[240:243]
v_mfma_f32_16x16x32_bf16 v[248:251],v[92:95],v[188:191],v[248:251]
ds_read_b64_tr_b16 v[92:93],v2 offset:31008
ds_read_b64_tr_b16 v[94:95],v2 offset:36128
v_mfma_f32_16x16x32_bf16 v[248:251],v[88:91],v[172:175],v[248:251]
ds_read_b64_tr_b16 v[88:89],v2 offset:20768
v_mfma_f32_16x16x32_bf16 v[248:251],v[84:87],v[156:159],v[248:251]
ds_read_b64_tr_b16 v[84:85],v2 offset:10528
v_mfma_f32_16x16x32_bf16 v[248:251],v[80:83],v[140:143],v[248:251]
s_waitcnt lgkmcnt(4)
ds_read_b64_tr_b16 v[80:81],v2 offset:288
v_mfma_f32_16x16x32_bf16 v[192:195],v[96:99],a[128:131],v[192:195]
ds_read_b64_tr_b16 v[82:83],v2 offset:5408
ds_read_b64_tr_b16 v[86:87],v2 offset:15648
v_mfma_f32_16x16x32_bf16 v[192:195],v[100:103],a[144:147],v[192:195]
ds_read_b64_tr_b16 v[90:91],v2 offset:25888
v_mfma_f32_16x16x32_bf16 v[192:195],v[104:107],a[160:163],v[192:195]
v_mfma_f32_16x16x32_bf16 v[192:195],v[108:111],a[176:179],v[192:195]
v_mfma_f32_16x16x32_bf16 v[200:203],v[108:111],a[180:183],v[200:203]
v_mfma_f32_16x16x32_bf16 v[200:203],v[104:107],a[164:167],v[200:203]
v_mfma_f32_16x16x32_bf16 v[200:203],v[100:103],a[148:151],v[200:203]
v_mfma_f32_16x16x32_bf16 v[200:203],v[96:99],a[132:135],v[200:203]
v_mfma_f32_16x16x32_bf16 v[208:211],v[96:99],a[136:139],v[208:211]
v_mfma_f32_16x16x32_bf16 v[208:211],v[100:103],a[152:155],v[208:211]
v_mfma_f32_16x16x32_bf16 v[208:211],v[104:107],a[168:171],v[208:211]
v_mfma_f32_16x16x32_bf16 v[208:211],v[108:111],a[184:187],v[208:211]
v_mfma_f32_16x16x32_bf16 v[216:219],v[108:111],a[188:191],v[216:219]
v_mfma_f32_16x16x32_bf16 v[216:219],v[104:107],a[172:175],v[216:219]
v_mfma_f32_16x16x32_bf16 v[216:219],v[100:103],a[156:159],v[216:219]
v_mfma_f32_16x16x32_bf16 v[216:219],v[96:99],a[140:143],v[216:219]
v_mfma_f32_16x16x32_bf16 v[224:227],v[112:115],a[128:131],v[224:227]
v_mfma_f32_16x16x32_bf16 v[224:227],v[116:119],a[144:147],v[224:227]
v_mfma_f32_16x16x32_bf16 v[224:227],v[120:123],a[160:163],v[224:227]
v_mfma_f32_16x16x32_bf16 v[224:227],v[124:127],a[176:179],v[224:227]
v_mfma_f32_16x16x32_bf16 v[232:235],v[124:127],a[180:183],v[232:235]
v_mfma_f32_16x16x32_bf16 v[232:235],v[120:123],a[164:167],v[232:235]
v_mfma_f32_16x16x32_bf16 v[232:235],v[116:119],a[148:151],v[232:235]
v_mfma_f32_16x16x32_bf16 v[232:235],v[112:115],a[132:135],v[232:235]
v_mfma_f32_16x16x32_bf16 v[240:243],v[112:115],a[136:139],v[240:243]
v_mfma_f32_16x16x32_bf16 v[240:243],v[116:119],a[152:155],v[240:243]
v_mfma_f32_16x16x32_bf16 v[240:243],v[120:123],a[168:171],v[240:243]
v_mfma_f32_16x16x32_bf16 v[240:243],v[124:127],a[184:187],v[240:243]
v_mfma_f32_16x16x32_bf16 v[248:251],v[124:127],a[188:191],v[248:251]
v_mfma_f32_16x16x32_bf16 v[248:251],v[120:123],a[172:175],v[248:251]
v_mfma_f32_16x16x32_bf16 v[248:251],v[116:119],a[156:159],v[248:251]
v_mfma_f32_16x16x32_bf16 v[248:251],v[112:115],a[140:143],v[248:251]
s_waitcnt vmcnt(0)
s_barrier
v_mfma_f32_16x16x32_bf16 v[192:195],v[64:67],a[68:71],v[192:195]
buffer_store_dwordx2 v[58:59],v60,s[44:47],s18 offen offset:2304 sc1
v_mfma_f32_16x16x32_bf16 v[192:195],v[68:71],a[76:79],v[192:195]
v_mfma_f32_16x16x32_bf16 v[192:195],v[72:75],a[84:87],v[192:195]
v_mfma_f32_16x16x32_bf16 v[192:195],v[76:79],a[92:95],v[192:195]
v_mfma_f32_16x16x32_bf16 v[200:203],v[76:79],a[124:127],v[200:203]
v_mfma_f32_16x16x32_bf16 v[200:203],v[72:75],a[116:119],v[200:203]
v_mfma_f32_16x16x32_bf16 v[200:203],v[68:71],a[108:111],v[200:203]
v_mfma_f32_16x16x32_bf16 v[200:203],v[64:67],a[100:103],v[200:203]
v_mfma_f32_16x16x32_bf16 v[208:211],v[64:67],a[64:67],v[208:211]
v_mfma_f32_16x16x32_bf16 v[208:211],v[68:71],a[72:75],v[208:211]
v_mfma_f32_16x16x32_bf16 v[208:211],v[72:75],a[80:83],v[208:211]
v_mfma_f32_16x16x32_bf16 v[208:211],v[76:79],a[88:91],v[208:211]
v_mfma_f32_16x16x32_bf16 v[216:219],v[76:79],a[120:123],v[216:219]
v_mfma_f32_16x16x32_bf16 v[216:219],v[72:75],a[112:115],v[216:219]
v_mfma_f32_16x16x32_bf16 v[216:219],v[68:71],a[104:107],v[216:219]
v_mfma_f32_16x16x32_bf16 v[216:219],v[64:67],a[96:99],v[216:219]
s_waitcnt lgkmcnt(0)
v_mfma_f32_16x16x32_bf16 v[224:227],v[80:83],a[68:71],v[224:227]
buffer_load_dwordx2 v[62:63],v61,s[44:47],s91 offen offset:2312 sc1
v_mfma_f32_16x16x32_bf16 v[224:227],v[84:87],a[76:79],v[224:227]
v_mfma_f32_16x16x32_bf16 v[224:227],v[88:91],a[84:87],v[224:227]
v_mfma_f32_16x16x32_bf16 v[224:227],v[92:95],a[92:95],v[224:227]
v_mfma_f32_16x16x32_bf16 v[232:235],v[92:95],a[124:127],v[232:235]
v_mfma_f32_16x16x32_bf16 v[232:235],v[88:91],a[116:119],v[232:235]
v_mfma_f32_16x16x32_bf16 v[232:235],v[84:87],a[108:111],v[232:235]
v_mfma_f32_16x16x32_bf16 v[232:235],v[80:83],a[100:103],v[232:235]
v_mfma_f32_16x16x32_bf16 v[240:243],v[80:83],a[64:67],v[240:243]
v_mfma_f32_16x16x32_bf16 v[240:243],v[84:87],a[72:75],v[240:243]
v_mfma_f32_16x16x32_bf16 v[240:243],v[88:91],a[80:83],v[240:243]
v_mfma_f32_16x16x32_bf16 v[240:243],v[92:95],a[88:91],v[240:243]
v_mfma_f32_16x16x32_bf16 v[248:251],v[92:95],a[120:123],v[248:251]
v_mfma_f32_16x16x32_bf16 v[248:251],v[88:91],a[112:115],v[248:251]
v_mfma_f32_16x16x32_bf16 v[248:251],v[84:87],a[104:107],v[248:251]
v_mfma_f32_16x16x32_bf16 v[248:251],v[80:83],a[96:99],v[248:251]
s_waitcnt vmcnt(0)
v_cmp_ne_u32 s[56:57],s52,v62
v_cmp_ne_u32 s[58:59],s53,v63
s_nop 0
s_or_b64 s[56:57],s[56:57],s[58:59]
s_cbranch_scc1 L_fr8_%=
L_fo8_%=:
buffer_load_dwordx4 v[64:67],v6,s[36:39],s66 offen sc1
buffer_load_dwordx4 v[68:71],v6,s[36:39],s67 offen sc1
buffer_load_dwordx4 v[72:75],v6,s[36:39],s66 offen offset:1024 sc1
buffer_load_dwordx4 v[76:79],v6,s[36:39],s67 offen offset:1024 sc1
buffer_load_dwordx4 v[80:83],v6,s[36:39],s66 offen offset:2048 sc1
buffer_load_dwordx4 v[84:87],v6,s[36:39],s67 offen offset:2048 sc1
buffer_load_dwordx4 v[88:91],v28,s[36:39],s66 offen offset:3072 sc1
buffer_load_dwordx4 v[92:95],v28,s[36:39],s67 offen offset:3072 sc1
buffer_load_dwordx2 v[4:5],v3,s[44:47],s91 offen offset:2304 sc1
s_waitcnt vmcnt(8)
v_mfma_f32_16x16x32_bf16 v[192:195],v[48:51],v[64:67],v[192:195]
s_waitcnt vmcnt(7)
v_mfma_f32_16x16x32_bf16 v[192:195],v[48:51],v[68:71],v[192:195]
v_mfma_f32_16x16x32_bf16 v[224:227],v[52:55],v[64:67],v[224:227]
v_mfma_f32_16x16x32_bf16 v[224:227],v[52:55],v[68:71],v[224:227]
s_waitcnt vmcnt(6)
v_mfma_f32_16x16x32_bf16 v[200:203],v[48:51],v[72:75],v[200:203]
s_waitcnt vmcnt(5)
v_mfma_f32_16x16x32_bf16 v[200:203],v[48:51],v[76:79],v[200:203]
v_mfma_f32_16x16x32_bf16 v[232:235],v[52:55],v[72:75],v[232:235]
v_mfma_f32_16x16x32_bf16 v[232:235],v[52:55],v[76:79],v[232:235]
s_waitcnt vmcnt(4)
v_mfma_f32_16x16x32_bf16 v[208:211],v[48:51],v[80:83],v[208:211]
s_waitcnt vmcnt(3)
v_mfma_f32_16x16x32_bf16 v[208:211],v[48:51],v[84:87],v[208:211]
v_mfma_f32_16x16x32_bf16 v[240:243],v[52:55],v[80:83],v[240:243]
v_mfma_f32_16x16x32_bf16 v[240:243],v[52:55],v[84:87],v[240:243]
s_waitcnt vmcnt(2)
v_mfma_f32_16x16x32_bf16 v[216:219],v[48:51],v[88:91],v[216:219]
s_waitcnt vmcnt(1)
v_mfma_f32_16x16x32_bf16 v[216:219],v[48:51],v[92:95],v[216:219]
v_mfma_f32_16x16x32_bf16 v[248:251],v[52:55],v[88:91],v[248:251]
v_mfma_f32_16x16x32_bf16 v[248:251],v[52:55],v[92:95],v[248:251]
s_waitcnt vmcnt(0)
v_cmp_ne_u32 s[56:57],s52,v4
v_cmp_ne_u32 s[58:59],s53,v5
s_nop 0
s_or_b64 s[56:57],s[56:57],s[58:59]
s_cbranch_scc1 L_fr9_%=
L_fo9_%=:
buffer_load_dwordx4 v[96:99],v6,s[36:39],s68 offen sc1
buffer_load_dwordx4 v[100:103],v6,s[36:39],s68 offen offset:1024 sc1
buffer_load_dwordx4 v[104:107],v6,s[36:39],s68 offen offset:2048 sc1
buffer_load_dwordx4 v[108:111],v6,s[36:39],s68 offen offset:3072 sc1
buffer_load_dwordx4 v[112:115],v6,s[36:39],s65 offen sc1
buffer_load_dwordx4 v[116:119],v6,s[36:39],s65 offen offset:1024 sc1
buffer_load_dwordx4 v[120:123],v6,s[36:39],s65 offen offset:2048 sc1
buffer_load_dwordx4 v[124:127],v28,s[36:39],s65 offen offset:3072 sc1
s_waitcnt vmcnt(7)
v_mfma_f32_16x16x32_bf16 v[128:131],v[48:51],v[96:99],0
s_waitcnt vmcnt(6)
v_mfma_f32_16x16x32_bf16 v[128:131],v[48:51],v[100:103],v[128:131]
s_waitcnt vmcnt(5)
v_mfma_f32_16x16x32_bf16 v[128:131],v[48:51],v[104:107],v[128:131]
s_waitcnt vmcnt(4)
v_mfma_f32_16x16x32_bf16 v[128:131],v[48:51],v[108:111],v[128:131]
v_mfma_f32_16x16x32_bf16 v[160:163],v[52:55],v[96:99],0
v_mfma_f32_16x16x32_bf16 v[160:163],v[52:55],v[100:103],v[160:163]
v_mfma_f32_16x16x32_bf16 v[160:163],v[52:55],v[104:107],v[160:163]
v_mfma_f32_16x16x32_bf16 v[160:163],v[52:55],v[108:111],v[160:163]
s_waitcnt vmcnt(3)
v_mfma_f32_16x16x32_bf16 v[192:195],v[48:51],v[112:115],v[192:195]
v_mfma_f32_16x16x32_bf16 v[224:227],v[52:55],v[112:115],v[224:227]
s_waitcnt vmcnt(2)
v_mfma_f32_16x16x32_bf16 v[200:203],v[48:51],v[116:119],v[200:203]
v_mfma_f32_16x16x32_bf16 v[232:235],v[52:55],v[116:119],v[232:235]
s_waitcnt vmcnt(1)
)A"
R"A(v_cvt_pk_bf16_f32 v128,v128,v129
v_cvt_pk_bf16_f32 v129,v130,v131
v_mfma_f32_16x16x32_bf16 v[208:211],v[48:51],v[120:123],v[208:211]
v_mfma_f32_16x16x32_bf16 v[240:243],v[52:55],v[120:123],v[240:243]
s_waitcnt vmcnt(0)
v_cvt_pk_bf16_f32 v130,v160,v161
v_cvt_pk_bf16_f32 v131,v162,v163
v_mfma_f32_16x16x32_bf16 v[216:219],v[48:51],v[124:127],v[216:219]
v_cvt_pk_bf16_f32 v192,v192,v193
v_cvt_pk_bf16_f32 v193,v194,v195
v_cvt_pk_bf16_f32 v194,v224,v225
v_cvt_pk_bf16_f32 v195,v226,v227
v_cvt_pk_bf16_f32 v200,v200,v201
v_cvt_pk_bf16_f32 v201,v202,v203
v_mfma_f32_16x16x32_bf16 v[248:251],v[52:55],v[124:127],v[248:251]
v_permlane16_swap_b32 v128,v130
v_permlane16_swap_b32 v129,v131
v_cvt_pk_bf16_f32 v202,v232,v233
v_cvt_pk_bf16_f32 v203,v234,v235
v_cvt_pk_bf16_f32 v208,v208,v209
v_cvt_pk_bf16_f32 v209,v210,v211
v_cvt_pk_bf16_f32 v210,v240,v241
v_cvt_pk_bf16_f32 v211,v242,v243
buffer_store_dwordx4 v[128:131],v56,s[24:27],s83 offen offset:256 sc1 nt
v_permlane16_swap_b32 v192,v194
v_permlane16_swap_b32 v193,v195
buffer_store_dwordx4 v[192:195],v56,s[24:27],s87 offen sc1 nt
v_permlane16_swap_b32 v200,v202
v_permlane16_swap_b32 v201,v203
v_cvt_pk_bf16_f32 v216,v216,v217
v_cvt_pk_bf16_f32 v217,v218,v219
v_cvt_pk_bf16_f32 v218,v248,v249
v_cvt_pk_bf16_f32 v219,v250,v251
buffer_store_dwordx4 v[200:203],v56,s[24:27],s88 offen sc1 nt
v_permlane16_swap_b32 v208,v210
v_permlane16_swap_b32 v209,v211
buffer_store_dwordx4 v[208:211],v56,s[24:27],s89 offen sc1 nt
v_permlane16_swap_b32 v216,v218
v_permlane16_swap_b32 v217,v219
buffer_store_dwordx4 v[216:219],v56,s[24:27],s90 offen sc1 nt
s_lshl_b32 s69,s62,20
s_add_u32 s69,s69,s18
buffer_store_dword v20,v22,s[44:47],s69 offen sc1
L_geS_%=:
s_branch L_end_%=
L_hot_%=:
L_fr1_%=:
s_mov_b32 s19,65536
L_fl1_%=:
buffer_load_dwordx2 v[4:5],v3,s[44:47],s91 offen offset:256 sc1
s_waitcnt vmcnt(0)
s_sub_u32 s19,s19,1
s_cmp_eq_u32 s19,0
s_cbranch_scc1 L_fo1_%=
v_cmp_ne_u32 s[56:57],s52,v4
v_cmp_ne_u32 s[58:59],s53,v5
s_nop 1
s_or_b64 s[56:57],s[56:57],s[58:59]
s_cbranch_scc1 L_fl1_%=
s_branch L_fo1_%=
L_fr2_%=:
s_mov_b32 s19,65536
L_fl2_%=:
buffer_load_dwordx2 v[4:5],v3,s[44:47],s91 offen offset:1280 sc1
s_waitcnt vmcnt(0)
s_sub_u32 s19,s19,1
s_cmp_eq_u32 s19,0
s_cbranch_scc1 L_fo2_%=
v_cmp_ne_u32 s[56:57],s52,v4
v_cmp_ne_u32 s[58:59],s53,v5
s_nop 1
s_or_b64 s[56:57],s[56:57],s[58:59]
s_cbranch_scc1 L_fl2_%=
s_branch L_fo2_%=
L_fr3_%=:
s_mov_b32 s19,65536
L_fl3_%=:
buffer_load_dwordx2 v[4:5],v3,s[44:47],s91 offen offset:1280 sc1
s_waitcnt vmcnt(0)
s_sub_u32 s19,s19,1
s_cmp_eq_u32 s19,0
s_cbranch_scc1 L_fo3_%=
v_cmp_ne_u32 s[56:57],s52,v4
v_cmp_ne_u32 s[58:59],s53,v5
s_nop 1
s_or_b64 s[56:57],s[56:57],s[58:59]
s_cbranch_scc1 L_fl3_%=
s_branch L_fo3_%=
L_fr4_%=:
s_mov_b32 s19,65536
L_fl4_%=:
buffer_load_dwordx2 v[62:63],v61,s[44:47],s91 offen offset:2312 sc1
s_waitcnt vmcnt(0)
s_sub_u32 s19,s19,1
s_cmp_eq_u32 s19,0
s_cbranch_scc1 L_fo4_%=
v_cmp_ne_u32 s[56:57],s52,v62
v_cmp_ne_u32 s[58:59],s53,v63
s_nop 1
s_or_b64 s[56:57],s[56:57],s[58:59]
s_cbranch_scc1 L_fl4_%=
s_branch L_fo4_%=
L_fr5_%=:
s_mov_b32 s19,65536
L_fl5_%=:
buffer_load_dwordx2 v[4:5],v3,s[44:47],s91 offen offset:2304 sc1
s_waitcnt vmcnt(0)
s_sub_u32 s19,s19,1
s_cmp_eq_u32 s19,0
s_cbranch_scc1 L_fo5_%=
v_cmp_ne_u32 s[56:57],s52,v4
v_cmp_ne_u32 s[58:59],s53,v5
s_nop 1
s_or_b64 s[56:57],s[56:57],s[58:59]
s_cbranch_scc1 L_fl5_%=
s_branch L_fo5_%=
L_fr6_%=:
s_mov_b32 s19,65536
L_fl6_%=:
buffer_load_dwordx2 v[4:5],v3,s[44:47],s91 offen offset:256 sc1
s_waitcnt vmcnt(0)
s_sub_u32 s19,s19,1
s_cmp_eq_u32 s19,0
s_cbranch_scc1 L_fo6_%=
v_cmp_ne_u32 s[56:57],s52,v4
v_cmp_ne_u32 s[58:59],s53,v5
s_nop 1
s_or_b64 s[56:57],s[56:57],s[58:59]
s_cbranch_scc1 L_fl6_%=
s_branch L_fo6_%=
L_fr7_%=:
s_mov_b32 s19,65536
L_fl7_%=:
buffer_load_dwordx2 v[4:5],v3,s[44:47],s91 offen offset:1280 sc1
s_waitcnt vmcnt(0)
s_sub_u32 s19,s19,1
s_cmp_eq_u32 s19,0
s_cbranch_scc1 L_fo7_%=
v_cmp_ne_u32 s[56:57],s52,v4
v_cmp_ne_u32 s[58:59],s53,v5
s_nop 1
s_or_b64 s[56:57],s[56:57],s[58:59]
s_cbranch_scc1 L_fl7_%=
s_branch L_fo7_%=
L_fr8_%=:
s_mov_b32 s19,65536
L_fl8_%=:
buffer_load_dwordx2 v[62:63],v61,s[44:47],s91 offen offset:2312 sc1
s_waitcnt vmcnt(0)
s_sub_u32 s19,s19,1
s_cmp_eq_u32 s19,0
s_cbranch_scc1 L_fo8_%=
v_cmp_ne_u32 s[56:57],s52,v62
v_cmp_ne_u32 s[58:59],s53,v63
s_nop 1
s_or_b64 s[56:57],s[56:57],s[58:59]
s_cbranch_scc1 L_fl8_%=
s_branch L_fo8_%=
L_fr9_%=:
s_mov_b32 s19,65536
L_fl9_%=:
buffer_load_dwordx2 v[4:5],v3,s[44:47],s91 offen offset:2304 sc1
s_waitcnt vmcnt(0)
s_sub_u32 s19,s19,1
s_cmp_eq_u32 s19,0
s_cbranch_scc1 L_fo9_%=
v_cmp_ne_u32 s[56:57],s52,v4
v_cmp_ne_u32 s[58:59],s53,v5
s_nop 1
s_or_b64 s[56:57],s[56:57],s[58:59]
s_cbranch_scc1 L_fl9_%=
s_branch L_fo9_%=
L_slow0_%=:
s_waitcnt vmcnt(0) lgkmcnt(0)
s_nop 7
v_accvgpr_write_b32 a160,v32
v_accvgpr_write_b32 a161,v33
v_accvgpr_write_b32 a162,v34
v_accvgpr_write_b32 a163,v35
v_accvgpr_write_b32 a164,v36
v_accvgpr_write_b32 a165,v37
v_accvgpr_write_b32 a166,v38
v_accvgpr_write_b32 a167,v39
v_accvgpr_write_b32 a168,v40
v_accvgpr_write_b32 a169,v41
v_accvgpr_write_b32 a170,v42
v_accvgpr_write_b32 a171,v43
v_accvgpr_write_b32 a172,v44
v_accvgpr_write_b32 a173,v45
v_accvgpr_write_b32 a174,v46
v_accvgpr_write_b32 a175,v47
s_waitcnt vmcnt(0) lgkmcnt(0)
s_nop 7
v_bfe_u32 v11,v6,4,4
v_lshrrev_b32 v12,8,v6
v_lshlrev_b32 v8,8,v11
v_lshl_add_u32 v8,v12,4,v8
v_lshlrev_b32 v9,10,v12
v_lshl_add_u32 v9,v11,1,v9
v_add_u32 v10,4096,v9
v_mov_b32 v48,0xff800000
v_mov_b32 v52,0xff800000
v_mov_b32 v56,0xff800000
v_mov_b32 v60,0xff800000
s_mov_b32 s65,0
s_mov_b32 s66,64
L_s10_%=:
buffer_load_dwordx4 a[128:131],v8,s[20:23],s65 offen
buffer_load_dwordx4 a[132:135],v8,s[20:23],s65 offen offset:64
buffer_load_dwordx4 a[136:139],v8,s[20:23],s65 offen offset:128
buffer_load_dwordx4 a[140:143],v8,s[20:23],s65 offen offset:192
s_waitcnt vmcnt(0)
v_mfma_f32_16x16x32_bf16 v[64:67],a[128:131],v[132:135],0
v_mfma_f32_16x16x32_bf16 v[64:67],a[132:135],v[140:143],v[64:67]
v_mfma_f32_16x16x32_bf16 v[64:67],a[136:139],v[148:151],v[64:67]
v_mfma_f32_16x16x32_bf16 v[64:67],a[140:143],v[156:159],v[64:67]
v_mfma_f32_16x16x32_bf16 v[80:83],a[128:131],v[164:167],0
v_mfma_f32_16x16x32_bf16 v[80:83],a[132:135],v[172:175],v[80:83]
v_mfma_f32_16x16x32_bf16 v[80:83],a[136:139],v[180:183],v[80:83]
v_mfma_f32_16x16x32_bf16 v[80:83],a[140:143],v[188:191],v[80:83]
v_mfma_f32_16x16x32_bf16 v[96:99],a[128:131],v[196:199],0
v_mfma_f32_16x16x32_bf16 v[96:99],a[132:135],v[204:207],v[96:99]
v_mfma_f32_16x16x32_bf16 v[96:99],a[136:139],v[212:215],v[96:99]
v_mfma_f32_16x16x32_bf16 v[96:99],a[140:143],v[220:223],v[96:99]
v_mfma_f32_16x16x32_bf16 v[112:115],a[128:131],v[228:231],0
v_mfma_f32_16x16x32_bf16 v[112:115],a[132:135],v[236:239],v[112:115]
v_mfma_f32_16x16x32_bf16 v[112:115],a[136:139],v[244:247],v[112:115]
v_mfma_f32_16x16x32_bf16 v[112:115],a[140:143],v[252:255],v[112:115]
s_nop 7
v_max3_f32 v48,v48,v64,v65
v_max3_f32 v48,v48,v66,v67
v_max3_f32 v52,v52,v80,v81
v_max3_f32 v52,v52,v82,v83
v_max3_f32 v56,v56,v96,v97
v_max3_f32 v56,v56,v98,v99
v_max3_f32 v60,v60,v112,v113
v_max3_f32 v60,v60,v114,v115
s_add_u32 s65,s65,4096
s_sub_u32 s66,s66,1
s_cmp_lg_u32 s66,0
s_cbranch_scc1 L_s10_%=
v_mov_b32 v11,v48
s_nop 1
v_permlane32_swap_b32 v48,v11
v_max_f32 v48,v48,v11
v_mov_b32 v11,v48
s_nop 1
v_permlane16_swap_b32 v48,v11
v_max_f32 v48,v48,v11
v_sub_f32 v32,0,v48
v_sub_f32 v33,0,v48
v_sub_f32 v34,0,v48
v_sub_f32 v35,0,v48
v_mov_b32 v11,v52
s_nop 1
v_permlane32_swap_b32 v52,v11
v_max_f32 v52,v52,v11
v_mov_b32 v11,v52
s_nop 1
v_permlane16_swap_b32 v52,v11
v_max_f32 v52,v52,v11
v_sub_f32 v36,0,v52
v_sub_f32 v37,0,v52
v_sub_f32 v38,0,v52
v_sub_f32 v39,0,v52
v_mov_b32 v11,v56
s_nop 1
v_permlane32_swap_b32 v56,v11
v_max_f32 v56,v56,v11
v_mov_b32 v11,v56
s_nop 1
v_permlane16_swap_b32 v56,v11
v_max_f32 v56,v56,v11
v_sub_f32 v40,0,v56
v_sub_f32 v41,0,v56
v_sub_f32 v42,0,v56
v_sub_f32 v43,0,v56
v_mov_b32 v11,v60
s_nop 1
v_permlane32_swap_b32 v60,v11
v_max_f32 v60,v60,v11
v_mov_b32 v11,v60
s_nop 1
v_permlane16_swap_b32 v60,v11
v_max_f32 v60,v60,v11
v_max_f32 v60,s72,v60
v_sub_f32 v44,0,v60
v_sub_f32 v45,0,v60
v_sub_f32 v46,0,v60
v_sub_f32 v47,0,v60
v_mov_b32 v48,0
v_mov_b32 v49,0
v_mov_b32 v50,0
v_mov_b32 v51,0
v_mov_b32 v52,0
v_mov_b32 v53,0
v_mov_b32 v54,0
v_mov_b32 v55,0
v_mov_b32 v56,0
v_mov_b32 v57,0
v_mov_b32 v58,0
v_mov_b32 v59,0
v_mov_b32 v60,0
v_mov_b32 v61,0
v_mov_b32 v62,0
v_mov_b32 v63,0
v_accvgpr_write_b32 a0,0
v_accvgpr_write_b32 a1,0
v_accvgpr_write_b32 a2,0
v_accvgpr_write_b32 a3,0
v_accvgpr_write_b32 a4,0
v_accvgpr_write_b32 a5,0
v_accvgpr_write_b32 a6,0
v_accvgpr_write_b32 a7,0
v_accvgpr_write_b32 a8,0
v_accvgpr_write_b32 a9,0
v_accvgpr_write_b32 a10,0
v_accvgpr_write_b32 a11,0
v_accvgpr_write_b32 a12,0
v_accvgpr_write_b32 a13,0
v_accvgpr_write_b32 a14,0
v_accvgpr_write_b32 a15,0
v_accvgpr_write_b32 a16,0
v_accvgpr_write_b32 a17,0
v_accvgpr_write_b32 a18,0
v_accvgpr_write_b32 a19,0
v_accvgpr_write_b32 a20,0
v_accvgpr_write_b32 a21,0
v_accvgpr_write_b32 a22,0
v_accvgpr_write_b32 a23,0
v_accvgpr_write_b32 a24,0
v_accvgpr_write_b32 a25,0
v_accvgpr_write_b32 a26,0
v_accvgpr_write_b32 a27,0
v_accvgpr_write_b32 a28,0
v_accvgpr_write_b32 a29,0
v_accvgpr_write_b32 a30,0
v_accvgpr_write_b32 a31,0
v_accvgpr_write_b32 a32,0
v_accvgpr_write_b32 a33,0
v_accvgpr_write_b32 a34,0
v_accvgpr_write_b32 a35,0
v_accvgpr_write_b32 a36,0
v_accvgpr_write_b32 a37,0
v_accvgpr_write_b32 a38,0
v_accvgpr_write_b32 a39,0
v_accvgpr_write_b32 a40,0
v_accvgpr_write_b32 a41,0
v_accvgpr_write_b32 a42,0
v_accvgpr_write_b32 a43,0
v_accvgpr_write_b32 a44,0
v_accvgpr_write_b32 a45,0
v_accvgpr_write_b32 a46,0
v_accvgpr_write_b32 a47,0
v_accvgpr_write_b32 a48,0
v_accvgpr_write_b32 a49,0
v_accvgpr_write_b32 a50,0
v_accvgpr_write_b32 a51,0
v_accvgpr_write_b32 a52,0
v_accvgpr_write_b32 a53,0
v_accvgpr_write_b32 a54,0
v_accvgpr_write_b32 a55,0
v_accvgpr_write_b32 a56,0
v_accvgpr_write_b32 a57,0
v_accvgpr_write_b32 a58,0
v_accvgpr_write_b32 a59,0
v_accvgpr_write_b32 a60,0
v_accvgpr_write_b32 a61,0
v_accvgpr_write_b32 a62,0
v_accvgpr_write_b32 a63,0
v_accvgpr_write_b32 a64,0
v_accvgpr_write_b32 a65,0
v_accvgpr_write_b32 a66,0
v_accvgpr_write_b32 a67,0
v_accvgpr_write_b32 a68,0
v_accvgpr_write_b32 a69,0
v_accvgpr_write_b32 a70,0
v_accvgpr_write_b32 a71,0
v_accvgpr_write_b32 a72,0
v_accvgpr_write_b32 a73,0
v_accvgpr_write_b32 a74,0
v_accvgpr_write_b32 a75,0
v_accvgpr_write_b32 a76,0
v_accvgpr_write_b32 a77,0
v_accvgpr_write_b32 a78,0
v_accvgpr_write_b32 a79,0
v_accvgpr_write_b32 a80,0
v_accvgpr_write_b32 a81,0
v_accvgpr_write_b32 a82,0
v_accvgpr_write_b32 a83,0
v_accvgpr_write_b32 a84,0
v_accvgpr_write_b32 a85,0
v_accvgpr_write_b32 a86,0
v_accvgpr_write_b32 a87,0
v_accvgpr_write_b32 a88,0
v_accvgpr_write_b32 a89,0
v_accvgpr_write_b32 a90,0
v_accvgpr_write_b32 a91,0
v_accvgpr_write_b32 a92,0
v_accvgpr_write_b32 a93,0
v_accvgpr_write_b32 a94,0
v_accvgpr_write_b32 a95,0
v_accvgpr_write_b32 a96,0
v_accvgpr_write_b32 a97,0
v_accvgpr_write_b32 a98,0
v_accvgpr_write_b32 a99,0
v_accvgpr_write_b32 a100,0
v_accvgpr_write_b32 a101,0
v_accvgpr_write_b32 a102,0
v_accvgpr_write_b32 a103,0
v_accvgpr_write_b32 a104,0
v_accvgpr_write_b32 a105,0
v_accvgpr_write_b32 a106,0
v_accvgpr_write_b32 a107,0
v_accvgpr_write_b32 a108,0
v_accvgpr_write_b32 a109,0
v_accvgpr_write_b32 a110,0
v_accvgpr_write_b32 a111,0
v_accvgpr_write_b32 a112,0
v_accvgpr_write_b32 a113,0
v_accvgpr_write_b32 a114,0
v_accvgpr_write_b32 a115,0
v_accvgpr_write_b32 a116,0
v_accvgpr_write_b32 a117,0
v_accvgpr_write_b32 a118,0
v_accvgpr_write_b32 a119,0
v_accvgpr_write_b32 a120,0
v_accvgpr_write_b32 a121,0
v_accvgpr_write_b32 a122,0
v_accvgpr_write_b32 a123,0
v_accvgpr_write_b32 a124,0
v_accvgpr_write_b32 a125,0
v_accvgpr_write_b32 a126,0
v_accvgpr_write_b32 a127,0
s_mov_b32 s65,0
s_mov_b32 s66,32
L_s20_%=:
s_add_u32 s67,s65,4096
buffer_load_dwordx4 a[128:131],v8,s[20:23],s65 offen
buffer_load_dwordx4 a[132:135],v8,s[20:23],s65 offen offset:64
buffer_load_dwordx4 a[136:139],v8,s[20:23],s65 offen offset:128
buffer_load_dwordx4 a[140:143],v8,s[20:23],s65 offen offset:192
)A"
R"A(buffer_load_dwordx4 a[144:147],v8,s[20:23],s67 offen
buffer_load_dwordx4 a[148:151],v8,s[20:23],s67 offen offset:64
buffer_load_dwordx4 a[152:155],v8,s[20:23],s67 offen offset:128
buffer_load_dwordx4 a[156:159],v8,s[20:23],s67 offen offset:192
s_waitcnt vmcnt(0)
v_mfma_f32_16x16x32_bf16 v[64:67],a[128:131],v[132:135],v[32:35]
v_mfma_f32_16x16x32_bf16 v[64:67],a[132:135],v[140:143],v[64:67]
v_mfma_f32_16x16x32_bf16 v[64:67],a[136:139],v[148:151],v[64:67]
v_mfma_f32_16x16x32_bf16 v[64:67],a[140:143],v[156:159],v[64:67]
v_mfma_f32_16x16x32_bf16 v[68:71],a[144:147],v[132:135],v[32:35]
v_mfma_f32_16x16x32_bf16 v[68:71],a[148:151],v[140:143],v[68:71]
v_mfma_f32_16x16x32_bf16 v[68:71],a[152:155],v[148:151],v[68:71]
v_mfma_f32_16x16x32_bf16 v[68:71],a[156:159],v[156:159],v[68:71]
v_mfma_f32_16x16x32_bf16 v[80:83],a[128:131],v[164:167],v[36:39]
v_mfma_f32_16x16x32_bf16 v[80:83],a[132:135],v[172:175],v[80:83]
v_mfma_f32_16x16x32_bf16 v[80:83],a[136:139],v[180:183],v[80:83]
v_mfma_f32_16x16x32_bf16 v[80:83],a[140:143],v[188:191],v[80:83]
v_mfma_f32_16x16x32_bf16 v[84:87],a[144:147],v[164:167],v[36:39]
v_mfma_f32_16x16x32_bf16 v[84:87],a[148:151],v[172:175],v[84:87]
v_mfma_f32_16x16x32_bf16 v[84:87],a[152:155],v[180:183],v[84:87]
v_mfma_f32_16x16x32_bf16 v[84:87],a[156:159],v[188:191],v[84:87]
v_mfma_f32_16x16x32_bf16 v[96:99],a[128:131],v[196:199],v[40:43]
v_mfma_f32_16x16x32_bf16 v[96:99],a[132:135],v[204:207],v[96:99]
v_mfma_f32_16x16x32_bf16 v[96:99],a[136:139],v[212:215],v[96:99]
v_mfma_f32_16x16x32_bf16 v[96:99],a[140:143],v[220:223],v[96:99]
v_mfma_f32_16x16x32_bf16 v[100:103],a[144:147],v[196:199],v[40:43]
v_mfma_f32_16x16x32_bf16 v[100:103],a[148:151],v[204:207],v[100:103]
v_mfma_f32_16x16x32_bf16 v[100:103],a[152:155],v[212:215],v[100:103]
v_mfma_f32_16x16x32_bf16 v[100:103],a[156:159],v[220:223],v[100:103]
v_mfma_f32_16x16x32_bf16 v[112:115],a[128:131],v[228:231],v[44:47]
v_mfma_f32_16x16x32_bf16 v[112:115],a[132:135],v[236:239],v[112:115]
v_mfma_f32_16x16x32_bf16 v[112:115],a[136:139],v[244:247],v[112:115]
v_mfma_f32_16x16x32_bf16 v[112:115],a[140:143],v[252:255],v[112:115]
v_mfma_f32_16x16x32_bf16 v[116:119],a[144:147],v[228:231],v[44:47]
v_mfma_f32_16x16x32_bf16 v[116:119],a[148:151],v[236:239],v[116:119]
v_mfma_f32_16x16x32_bf16 v[116:119],a[152:155],v[244:247],v[116:119]
v_mfma_f32_16x16x32_bf16 v[116:119],a[156:159],v[252:255],v[116:119]
s_nop 7
v_exp_f32 v64,v64
v_exp_f32 v65,v65
v_exp_f32 v66,v66
v_exp_f32 v67,v67
v_exp_f32 v68,v68
v_exp_f32 v69,v69
v_exp_f32 v70,v70
v_exp_f32 v71,v71
s_nop 0
v_cvt_pk_bf16_f32 v64,v64,v65
v_cvt_pk_bf16_f32 v65,v66,v67
v_cvt_pk_bf16_f32 v66,v68,v69
v_cvt_pk_bf16_f32 v67,v70,v71
v_exp_f32 v80,v80
v_exp_f32 v81,v81
v_exp_f32 v82,v82
v_exp_f32 v83,v83
v_exp_f32 v84,v84
v_exp_f32 v85,v85
v_exp_f32 v86,v86
v_exp_f32 v87,v87
s_nop 0
v_cvt_pk_bf16_f32 v80,v80,v81
v_cvt_pk_bf16_f32 v81,v82,v83
v_cvt_pk_bf16_f32 v82,v84,v85
v_cvt_pk_bf16_f32 v83,v86,v87
v_exp_f32 v96,v96
v_exp_f32 v97,v97
v_exp_f32 v98,v98
v_exp_f32 v99,v99
v_exp_f32 v100,v100
v_exp_f32 v101,v101
v_exp_f32 v102,v102
v_exp_f32 v103,v103
s_nop 0
v_cvt_pk_bf16_f32 v96,v96,v97
v_cvt_pk_bf16_f32 v97,v98,v99
v_cvt_pk_bf16_f32 v98,v100,v101
v_cvt_pk_bf16_f32 v99,v102,v103
v_exp_f32 v112,v112
v_exp_f32 v113,v113
v_exp_f32 v114,v114
v_exp_f32 v115,v115
v_exp_f32 v116,v116
v_exp_f32 v117,v117
v_exp_f32 v118,v118
v_exp_f32 v119,v119
s_nop 0
v_cvt_pk_bf16_f32 v112,v112,v113
v_cvt_pk_bf16_f32 v113,v114,v115
v_cvt_pk_bf16_f32 v114,v116,v117
v_cvt_pk_bf16_f32 v115,v118,v119
buffer_load_ushort v72,v9,s[24:27],s65 offen
buffer_load_ushort v73,v9,s[24:27],s65 offen offset:256
buffer_load_ushort v74,v9,s[24:27],s65 offen offset:512
buffer_load_ushort v75,v9,s[24:27],s65 offen offset:768
buffer_load_ushort v76,v10,s[24:27],s65 offen
buffer_load_ushort v77,v10,s[24:27],s65 offen offset:256
buffer_load_ushort v78,v10,s[24:27],s65 offen offset:512
buffer_load_ushort v79,v10,s[24:27],s65 offen offset:768
s_waitcnt vmcnt(0)
v_lshl_or_b32 v88,v73,16,v72
v_lshl_or_b32 v89,v75,16,v74
v_lshl_or_b32 v90,v77,16,v76
v_lshl_or_b32 v91,v79,16,v78
s_nop 1
v_mfma_f32_16x16x32_bf16 a[0:3],v[88:91],v[64:67],a[0:3]
v_mfma_f32_16x16x32_bf16 a[32:35],v[88:91],v[80:83],a[32:35]
v_mfma_f32_16x16x32_bf16 a[64:67],v[88:91],v[96:99],a[64:67]
v_mfma_f32_16x16x32_bf16 a[96:99],v[88:91],v[112:115],a[96:99]
buffer_load_ushort v72,v9,s[24:27],s65 offen offset:32
buffer_load_ushort v73,v9,s[24:27],s65 offen offset:288
buffer_load_ushort v74,v9,s[24:27],s65 offen offset:544
buffer_load_ushort v75,v9,s[24:27],s65 offen offset:800
buffer_load_ushort v76,v10,s[24:27],s65 offen offset:32
buffer_load_ushort v77,v10,s[24:27],s65 offen offset:288
buffer_load_ushort v78,v10,s[24:27],s65 offen offset:544
buffer_load_ushort v79,v10,s[24:27],s65 offen offset:800
s_waitcnt vmcnt(0)
v_lshl_or_b32 v88,v73,16,v72
v_lshl_or_b32 v89,v75,16,v74
v_lshl_or_b32 v90,v77,16,v76
v_lshl_or_b32 v91,v79,16,v78
s_nop 1
v_mfma_f32_16x16x32_bf16 a[4:7],v[88:91],v[64:67],a[4:7]
v_mfma_f32_16x16x32_bf16 a[36:39],v[88:91],v[80:83],a[36:39]
v_mfma_f32_16x16x32_bf16 a[68:71],v[88:91],v[96:99],a[68:71]
v_mfma_f32_16x16x32_bf16 a[100:103],v[88:91],v[112:115],a[100:103]
buffer_load_ushort v72,v9,s[24:27],s65 offen offset:64
buffer_load_ushort v73,v9,s[24:27],s65 offen offset:320
buffer_load_ushort v74,v9,s[24:27],s65 offen offset:576
buffer_load_ushort v75,v9,s[24:27],s65 offen offset:832
buffer_load_ushort v76,v10,s[24:27],s65 offen offset:64
buffer_load_ushort v77,v10,s[24:27],s65 offen offset:320
buffer_load_ushort v78,v10,s[24:27],s65 offen offset:576
buffer_load_ushort v79,v10,s[24:27],s65 offen offset:832
s_waitcnt vmcnt(0)
v_lshl_or_b32 v88,v73,16,v72
v_lshl_or_b32 v89,v75,16,v74
v_lshl_or_b32 v90,v77,16,v76
v_lshl_or_b32 v91,v79,16,v78
s_nop 1
v_mfma_f32_16x16x32_bf16 a[8:11],v[88:91],v[64:67],a[8:11]
v_mfma_f32_16x16x32_bf16 a[40:43],v[88:91],v[80:83],a[40:43]
v_mfma_f32_16x16x32_bf16 a[72:75],v[88:91],v[96:99],a[72:75]
v_mfma_f32_16x16x32_bf16 a[104:107],v[88:91],v[112:115],a[104:107]
buffer_load_ushort v72,v9,s[24:27],s65 offen offset:96
buffer_load_ushort v73,v9,s[24:27],s65 offen offset:352
buffer_load_ushort v74,v9,s[24:27],s65 offen offset:608
buffer_load_ushort v75,v9,s[24:27],s65 offen offset:864
buffer_load_ushort v76,v10,s[24:27],s65 offen offset:96
buffer_load_ushort v77,v10,s[24:27],s65 offen offset:352
buffer_load_ushort v78,v10,s[24:27],s65 offen offset:608
buffer_load_ushort v79,v10,s[24:27],s65 offen offset:864
s_waitcnt vmcnt(0)
v_lshl_or_b32 v88,v73,16,v72
v_lshl_or_b32 v89,v75,16,v74
v_lshl_or_b32 v90,v77,16,v76
v_lshl_or_b32 v91,v79,16,v78
s_nop 1
v_mfma_f32_16x16x32_bf16 a[12:15],v[88:91],v[64:67],a[12:15]
v_mfma_f32_16x16x32_bf16 a[44:47],v[88:91],v[80:83],a[44:47]
v_mfma_f32_16x16x32_bf16 a[76:79],v[88:91],v[96:99],a[76:79]
v_mfma_f32_16x16x32_bf16 a[108:111],v[88:91],v[112:115],a[108:111]
buffer_load_ushort v72,v9,s[24:27],s65 offen offset:128
buffer_load_ushort v73,v9,s[24:27],s65 offen offset:384
buffer_load_ushort v74,v9,s[24:27],s65 offen offset:640
buffer_load_ushort v75,v9,s[24:27],s65 offen offset:896
buffer_load_ushort v76,v10,s[24:27],s65 offen offset:128
buffer_load_ushort v77,v10,s[24:27],s65 offen offset:384
buffer_load_ushort v78,v10,s[24:27],s65 offen offset:640
buffer_load_ushort v79,v10,s[24:27],s65 offen offset:896
s_waitcnt vmcnt(0)
v_lshl_or_b32 v88,v73,16,v72
v_lshl_or_b32 v89,v75,16,v74
v_lshl_or_b32 v90,v77,16,v76
v_lshl_or_b32 v91,v79,16,v78
s_nop 1
v_mfma_f32_16x16x32_bf16 a[16:19],v[88:91],v[64:67],a[16:19]
v_mfma_f32_16x16x32_bf16 a[48:51],v[88:91],v[80:83],a[48:51]
v_mfma_f32_16x16x32_bf16 a[80:83],v[88:91],v[96:99],a[80:83]
v_mfma_f32_16x16x32_bf16 a[112:115],v[88:91],v[112:115],a[112:115]
buffer_load_ushort v72,v9,s[24:27],s65 offen offset:160
buffer_load_ushort v73,v9,s[24:27],s65 offen offset:416
buffer_load_ushort v74,v9,s[24:27],s65 offen offset:672
buffer_load_ushort v75,v9,s[24:27],s65 offen offset:928
buffer_load_ushort v76,v10,s[24:27],s65 offen offset:160
buffer_load_ushort v77,v10,s[24:27],s65 offen offset:416
buffer_load_ushort v78,v10,s[24:27],s65 offen offset:672
buffer_load_ushort v79,v10,s[24:27],s65 offen offset:928
s_waitcnt vmcnt(0)
v_lshl_or_b32 v88,v73,16,v72
v_lshl_or_b32 v89,v75,16,v74
v_lshl_or_b32 v90,v77,16,v76
v_lshl_or_b32 v91,v79,16,v78
s_nop 1
v_mfma_f32_16x16x32_bf16 a[20:23],v[88:91],v[64:67],a[20:23]
v_mfma_f32_16x16x32_bf16 a[52:55],v[88:91],v[80:83],a[52:55]
v_mfma_f32_16x16x32_bf16 a[84:87],v[88:91],v[96:99],a[84:87]
v_mfma_f32_16x16x32_bf16 a[116:119],v[88:91],v[112:115],a[116:119]
buffer_load_ushort v72,v9,s[24:27],s65 offen offset:192
buffer_load_ushort v73,v9,s[24:27],s65 offen offset:448
buffer_load_ushort v74,v9,s[24:27],s65 offen offset:704
buffer_load_ushort v75,v9,s[24:27],s65 offen offset:960
buffer_load_ushort v76,v10,s[24:27],s65 offen offset:192
buffer_load_ushort v77,v10,s[24:27],s65 offen offset:448
buffer_load_ushort v78,v10,s[24:27],s65 offen offset:704
buffer_load_ushort v79,v10,s[24:27],s65 offen offset:960
s_waitcnt vmcnt(0)
v_lshl_or_b32 v88,v73,16,v72
v_lshl_or_b32 v89,v75,16,v74
v_lshl_or_b32 v90,v77,16,v76
v_lshl_or_b32 v91,v79,16,v78
s_nop 1
v_mfma_f32_16x16x32_bf16 a[24:27],v[88:91],v[64:67],a[24:27]
v_mfma_f32_16x16x32_bf16 a[56:59],v[88:91],v[80:83],a[56:59]
v_mfma_f32_16x16x32_bf16 a[88:91],v[88:91],v[96:99],a[88:91]
v_mfma_f32_16x16x32_bf16 a[120:123],v[88:91],v[112:115],a[120:123]
buffer_load_ushort v72,v9,s[24:27],s65 offen offset:224
buffer_load_ushort v73,v9,s[24:27],s65 offen offset:480
buffer_load_ushort v74,v9,s[24:27],s65 offen offset:736
buffer_load_ushort v75,v9,s[24:27],s65 offen offset:992
buffer_load_ushort v76,v10,s[24:27],s65 offen offset:224
buffer_load_ushort v77,v10,s[24:27],s65 offen offset:480
buffer_load_ushort v78,v10,s[24:27],s65 offen offset:736
buffer_load_ushort v79,v10,s[24:27],s65 offen offset:992
s_waitcnt vmcnt(0)
v_lshl_or_b32 v88,v73,16,v72
v_lshl_or_b32 v89,v75,16,v74
v_lshl_or_b32 v90,v77,16,v76
v_lshl_or_b32 v91,v79,16,v78
s_nop 1
v_mfma_f32_16x16x32_bf16 a[28:31],v[88:91],v[64:67],a[28:31]
v_mfma_f32_16x16x32_bf16 a[60:63],v[88:91],v[80:83],a[60:63]
v_mfma_f32_16x16x32_bf16 a[92:95],v[88:91],v[96:99],a[92:95]
v_mfma_f32_16x16x32_bf16 a[124:127],v[88:91],v[112:115],a[124:127]
v_mfma_f32_16x16x32_bf16 v[48:51],v[28:31],v[64:67],v[48:51]
v_mfma_f32_16x16x32_bf16 v[52:55],v[28:31],v[80:83],v[52:55]
v_mfma_f32_16x16x32_bf16 v[56:59],v[28:31],v[96:99],v[56:59]
v_mfma_f32_16x16x32_bf16 v[60:63],v[28:31],v[112:115],v[60:63]
s_add_u32 s65,s65,8192
s_sub_u32 s66,s66,1
s_cmp_lg_u32 s66,0
s_cbranch_scc1 L_s20_%=
s_nop 7
v_max_f32 v24,0x00800000,v48
v_rcp_f32 v24,v24
v_max_f32 v25,0x00800000,v52
v_rcp_f32 v25,v25
v_max_f32 v26,0x00800000,v56
v_rcp_f32 v26,v26
v_max_f32 v27,0x00800000,v60
v_rcp_f32 v27,v27
v_accvgpr_read_b32 v8,a0
v_accvgpr_read_b32 v9,a1
v_accvgpr_read_b32 v10,a2
v_accvgpr_read_b32 v11,a3
v_accvgpr_read_b32 v12,a4
v_accvgpr_read_b32 v13,a5
v_accvgpr_read_b32 v14,a6
v_accvgpr_read_b32 v15,a7
v_mul_f32 v8,v8,v24
v_mul_f32 v9,v9,v24
v_mul_f32 v10,v10,v24
v_mul_f32 v11,v11,v24
v_mul_f32 v12,v12,v24
v_mul_f32 v13,v13,v24
v_mul_f32 v14,v14,v24
v_mul_f32 v15,v15,v24
v_cvt_pk_bf16_f32 v8,v8,v9
v_cvt_pk_bf16_f32 v9,v10,v11
v_cvt_pk_bf16_f32 v10,v12,v13
v_cvt_pk_bf16_f32 v11,v14,v15
ds_write_b128 v7,v[8:11] offset:6144
v_accvgpr_read_b32 v16,a8
v_accvgpr_read_b32 v17,a9
v_accvgpr_read_b32 v18,a10
v_accvgpr_read_b32 v19,a11
v_accvgpr_read_b32 v20,a12
v_accvgpr_read_b32 v21,a13
v_accvgpr_read_b32 v22,a14
v_accvgpr_read_b32 v23,a15
v_mul_f32 v16,v16,v24
v_mul_f32 v17,v17,v24
v_mul_f32 v18,v18,v24
v_mul_f32 v19,v19,v24
v_mul_f32 v20,v20,v24
v_mul_f32 v21,v21,v24
v_mul_f32 v22,v22,v24
v_mul_f32 v23,v23,v24
v_cvt_pk_bf16_f32 v16,v16,v17
v_cvt_pk_bf16_f32 v17,v18,v19
v_cvt_pk_bf16_f32 v18,v20,v21
v_cvt_pk_bf16_f32 v19,v22,v23
ds_write_b128 v7,v[16:19] offset:7168
v_accvgpr_read_b32 v8,a16
v_accvgpr_read_b32 v9,a17
v_accvgpr_read_b32 v10,a18
v_accvgpr_read_b32 v11,a19
v_accvgpr_read_b32 v12,a20
v_accvgpr_read_b32 v13,a21
)A"
R"A(v_accvgpr_read_b32 v14,a22
v_accvgpr_read_b32 v15,a23
v_mul_f32 v8,v8,v24
v_mul_f32 v9,v9,v24
v_mul_f32 v10,v10,v24
v_mul_f32 v11,v11,v24
v_mul_f32 v12,v12,v24
v_mul_f32 v13,v13,v24
v_mul_f32 v14,v14,v24
v_mul_f32 v15,v15,v24
v_cvt_pk_bf16_f32 v8,v8,v9
v_cvt_pk_bf16_f32 v9,v10,v11
v_cvt_pk_bf16_f32 v10,v12,v13
v_cvt_pk_bf16_f32 v11,v14,v15
ds_write_b128 v7,v[8:11]
v_accvgpr_read_b32 v16,a24
v_accvgpr_read_b32 v17,a25
v_accvgpr_read_b32 v18,a26
v_accvgpr_read_b32 v19,a27
v_accvgpr_read_b32 v20,a28
v_accvgpr_read_b32 v21,a29
v_accvgpr_read_b32 v22,a30
v_accvgpr_read_b32 v23,a31
v_mul_f32 v16,v16,v24
v_mul_f32 v17,v17,v24
v_mul_f32 v18,v18,v24
v_mul_f32 v19,v19,v24
v_mul_f32 v20,v20,v24
v_mul_f32 v21,v21,v24
v_mul_f32 v22,v22,v24
v_mul_f32 v23,v23,v24
v_cvt_pk_bf16_f32 v16,v16,v17
v_cvt_pk_bf16_f32 v17,v18,v19
v_cvt_pk_bf16_f32 v18,v20,v21
v_cvt_pk_bf16_f32 v19,v22,v23
ds_write_b128 v7,v[16:19] offset:1024
v_accvgpr_read_b32 v32,a160
v_accvgpr_read_b32 v33,a161
v_accvgpr_read_b32 v34,a162
v_accvgpr_read_b32 v35,a163
v_accvgpr_read_b32 v36,a164
v_accvgpr_read_b32 v37,a165
v_accvgpr_read_b32 v38,a166
v_accvgpr_read_b32 v39,a167
v_accvgpr_read_b32 v40,a168
v_accvgpr_read_b32 v41,a169
v_accvgpr_read_b32 v42,a170
v_accvgpr_read_b32 v43,a171
v_accvgpr_read_b32 v44,a172
v_accvgpr_read_b32 v45,a173
v_accvgpr_read_b32 v46,a174
v_accvgpr_read_b32 v47,a175
s_waitcnt lgkmcnt(0)
v_subrev_u32 v8,s78,v1
ds_read_b128 a[128:131],v8
ds_read_b128 a[132:135],v8 offset:256
ds_read_b128 a[136:139],v8 offset:512
ds_read_b128 a[140:143],v8 offset:768
ds_read_b128 a[144:147],v8 offset:4096
ds_read_b128 a[148:151],v8 offset:4352
ds_read_b128 a[152:155],v8 offset:4608
ds_read_b128 a[156:159],v8 offset:4864
ds_read_b128 a[160:163],v8 offset:8192
ds_read_b128 a[164:167],v8 offset:8448
ds_read_b128 a[168:171],v8 offset:8704
ds_read_b128 a[172:175],v8 offset:8960
ds_read_b128 a[176:179],v8 offset:12288
ds_read_b128 a[180:183],v8 offset:12544
ds_read_b128 a[184:187],v8 offset:12800
ds_read_b128 a[188:191],v8 offset:13056
s_waitcnt lgkmcnt(15)
v_mfma_f32_16x16x32_bf16 v[64:67],a[128:131],v[128:131],v[32:35]
s_waitcnt lgkmcnt(14)
v_mfma_f32_16x16x32_bf16 v[64:67],a[132:135],v[136:139],v[64:67]
s_waitcnt lgkmcnt(13)
v_mfma_f32_16x16x32_bf16 v[64:67],a[136:139],v[144:147],v[64:67]
s_waitcnt lgkmcnt(12)
v_mfma_f32_16x16x32_bf16 v[64:67],a[140:143],v[152:155],v[64:67]
s_waitcnt lgkmcnt(8)
v_mfma_f32_16x16x32_bf16 v[68:71],a[156:159],v[152:155],v[32:35]
v_mfma_f32_16x16x32_bf16 v[68:71],a[152:155],v[144:147],v[68:71]
v_mfma_f32_16x16x32_bf16 v[68:71],a[148:151],v[136:139],v[68:71]
v_mfma_f32_16x16x32_bf16 v[68:71],a[144:147],v[128:131],v[68:71]
s_waitcnt lgkmcnt(0)
v_mfma_f32_16x16x32_bf16 v[72:75],a[160:163],v[128:131],v[32:35]
v_mfma_f32_16x16x32_bf16 v[72:75],a[164:167],v[136:139],v[72:75]
v_mfma_f32_16x16x32_bf16 v[72:75],a[168:171],v[144:147],v[72:75]
v_mfma_f32_16x16x32_bf16 v[72:75],a[172:175],v[152:155],v[72:75]
v_mfma_f32_16x16x32_bf16 v[76:79],a[188:191],v[152:155],v[32:35]
v_mfma_f32_16x16x32_bf16 v[76:79],a[184:187],v[144:147],v[76:79]
v_mfma_f32_16x16x32_bf16 v[76:79],a[180:183],v[136:139],v[76:79]
v_mfma_f32_16x16x32_bf16 v[76:79],a[176:179],v[128:131],v[76:79]
v_mfma_f32_16x16x32_bf16 v[80:83],a[128:131],v[160:163],v[36:39]
v_mfma_f32_16x16x32_bf16 v[80:83],a[132:135],v[168:171],v[80:83]
v_mfma_f32_16x16x32_bf16 v[80:83],a[136:139],v[176:179],v[80:83]
v_mfma_f32_16x16x32_bf16 v[80:83],a[140:143],v[184:187],v[80:83]
v_mfma_f32_16x16x32_bf16 v[84:87],a[156:159],v[184:187],v[36:39]
v_mfma_f32_16x16x32_bf16 v[84:87],a[152:155],v[176:179],v[84:87]
v_mfma_f32_16x16x32_bf16 v[84:87],a[148:151],v[168:171],v[84:87]
v_mfma_f32_16x16x32_bf16 v[84:87],a[144:147],v[160:163],v[84:87]
v_mfma_f32_16x16x32_bf16 v[88:91],a[160:163],v[160:163],v[36:39]
v_mfma_f32_16x16x32_bf16 v[88:91],a[164:167],v[168:171],v[88:91]
v_mfma_f32_16x16x32_bf16 v[88:91],a[168:171],v[176:179],v[88:91]
v_mfma_f32_16x16x32_bf16 v[88:91],a[172:175],v[184:187],v[88:91]
v_mfma_f32_16x16x32_bf16 v[92:95],a[188:191],v[184:187],v[36:39]
v_mfma_f32_16x16x32_bf16 v[92:95],a[184:187],v[176:179],v[92:95]
v_mfma_f32_16x16x32_bf16 v[92:95],a[180:183],v[168:171],v[92:95]
v_mfma_f32_16x16x32_bf16 v[92:95],a[176:179],v[160:163],v[92:95]
v_mfma_f32_16x16x32_bf16 v[96:99],a[128:131],v[192:195],v[40:43]
v_mfma_f32_16x16x32_bf16 v[96:99],a[132:135],v[200:203],v[96:99]
v_mfma_f32_16x16x32_bf16 v[96:99],a[136:139],v[208:211],v[96:99]
v_mfma_f32_16x16x32_bf16 v[96:99],a[140:143],v[216:219],v[96:99]
v_mfma_f32_16x16x32_bf16 v[100:103],a[156:159],v[216:219],v[40:43]
v_mfma_f32_16x16x32_bf16 v[100:103],a[152:155],v[208:211],v[100:103]
v_mfma_f32_16x16x32_bf16 v[100:103],a[148:151],v[200:203],v[100:103]
v_mfma_f32_16x16x32_bf16 v[100:103],a[144:147],v[192:195],v[100:103]
v_mfma_f32_16x16x32_bf16 v[104:107],a[160:163],v[192:195],v[40:43]
v_mfma_f32_16x16x32_bf16 v[104:107],a[164:167],v[200:203],v[104:107]
v_mfma_f32_16x16x32_bf16 v[104:107],a[168:171],v[208:211],v[104:107]
v_mfma_f32_16x16x32_bf16 v[104:107],a[172:175],v[216:219],v[104:107]
v_mfma_f32_16x16x32_bf16 v[108:111],a[188:191],v[216:219],v[40:43]
v_mfma_f32_16x16x32_bf16 v[108:111],a[184:187],v[208:211],v[108:111]
v_mfma_f32_16x16x32_bf16 v[108:111],a[180:183],v[200:203],v[108:111]
v_mfma_f32_16x16x32_bf16 v[108:111],a[176:179],v[192:195],v[108:111]
v_exp_f32 v64,v64
v_exp_f32 v65,v65
s_nop 0
v_cvt_pk_bf16_f32 v64,v64,v65
v_exp_f32 v66,v66
v_exp_f32 v67,v67
s_nop 0
v_cvt_pk_bf16_f32 v65,v66,v67
v_exp_f32 v68,v68
v_exp_f32 v69,v69
s_nop 0
v_cvt_pk_bf16_f32 v66,v68,v69
v_exp_f32 v70,v70
v_exp_f32 v71,v71
s_nop 0
v_cvt_pk_bf16_f32 v67,v70,v71
v_exp_f32 v72,v72
v_exp_f32 v73,v73
s_nop 0
v_cvt_pk_bf16_f32 v72,v72,v73
v_exp_f32 v74,v74
v_exp_f32 v75,v75
s_nop 0
v_cvt_pk_bf16_f32 v73,v74,v75
v_exp_f32 v76,v76
v_exp_f32 v77,v77
s_nop 0
v_cvt_pk_bf16_f32 v74,v76,v77
v_exp_f32 v78,v78
v_exp_f32 v79,v79
s_nop 0
v_cvt_pk_bf16_f32 v75,v78,v79
v_exp_f32 v80,v80
v_exp_f32 v81,v81
s_nop 0
v_cvt_pk_bf16_f32 v80,v80,v81
v_exp_f32 v82,v82
v_exp_f32 v83,v83
s_nop 0
v_cvt_pk_bf16_f32 v81,v82,v83
v_exp_f32 v84,v84
v_exp_f32 v85,v85
s_nop 0
v_cvt_pk_bf16_f32 v82,v84,v85
v_exp_f32 v86,v86
v_exp_f32 v87,v87
s_nop 0
v_cvt_pk_bf16_f32 v83,v86,v87
v_exp_f32 v88,v88
v_exp_f32 v89,v89
s_nop 0
v_cvt_pk_bf16_f32 v88,v88,v89
v_mfma_f32_16x16x32_bf16 a[0:3],a[192:195],v[64:67],0
v_mfma_f32_16x16x32_bf16 a[0:3],a[196:199],v[72:75],a[0:3]
v_mfma_f32_16x16x32_bf16 a[4:7],a[200:203],v[64:67],0
v_mfma_f32_16x16x32_bf16 a[4:7],a[204:207],v[72:75],a[4:7]
v_mfma_f32_16x16x32_bf16 a[8:11],a[208:211],v[64:67],0
v_mfma_f32_16x16x32_bf16 a[8:11],a[212:215],v[72:75],a[8:11]
v_mfma_f32_16x16x32_bf16 a[12:15],a[216:219],v[64:67],0
v_mfma_f32_16x16x32_bf16 a[12:15],a[220:223],v[72:75],a[12:15]
v_mfma_f32_16x16x32_bf16 a[16:19],a[224:227],v[64:67],0
v_mfma_f32_16x16x32_bf16 a[16:19],a[228:231],v[72:75],a[16:19]
v_mfma_f32_16x16x32_bf16 a[20:23],a[232:235],v[64:67],0
v_mfma_f32_16x16x32_bf16 a[20:23],a[236:239],v[72:75],a[20:23]
v_mfma_f32_16x16x32_bf16 a[24:27],a[240:243],v[64:67],0
v_mfma_f32_16x16x32_bf16 a[24:27],a[244:247],v[72:75],a[24:27]
v_mfma_f32_16x16x32_bf16 a[28:31],a[248:251],v[64:67],0
v_mfma_f32_16x16x32_bf16 a[28:31],a[252:255],v[72:75],a[28:31]
v_mfma_f32_16x16x32_bf16 v[48:51],v[28:31],v[64:67],0
v_mfma_f32_16x16x32_bf16 v[48:51],v[28:31],v[72:75],v[48:51]
s_waitcnt vmcnt(0) lgkmcnt(0)
s_nop 7
s_branch L_ok0_%=
L_slow1_%=:
s_waitcnt vmcnt(0) lgkmcnt(0)
s_nop 7
v_bfe_u32 v11,v6,4,4
v_lshrrev_b32 v12,8,v6
v_lshlrev_b32 v8,8,v11
v_lshl_add_u32 v8,v12,4,v8
v_lshlrev_b32 v9,10,v12
v_lshl_add_u32 v9,v11,1,v9
v_add_u32 v10,4096,v9
v_mov_b32 v48,0xff800000
v_mov_b32 v52,0xff800000
v_mov_b32 v56,0xff800000
v_mov_b32 v60,0xff800000
s_mov_b32 s65,262144
s_mov_b32 s66,64
L_s11_%=:
buffer_load_dwordx4 a[128:131],v8,s[20:23],s65 offen
buffer_load_dwordx4 a[132:135],v8,s[20:23],s65 offen offset:64
buffer_load_dwordx4 a[136:139],v8,s[20:23],s65 offen offset:128
buffer_load_dwordx4 a[140:143],v8,s[20:23],s65 offen offset:192
s_waitcnt vmcnt(0)
v_mfma_f32_16x16x32_bf16 v[64:67],a[128:131],v[128:131],0
v_mfma_f32_16x16x32_bf16 v[64:67],a[132:135],v[136:139],v[64:67]
v_mfma_f32_16x16x32_bf16 v[64:67],a[136:139],v[144:147],v[64:67]
v_mfma_f32_16x16x32_bf16 v[64:67],a[140:143],v[152:155],v[64:67]
v_mfma_f32_16x16x32_bf16 v[80:83],a[128:131],v[160:163],0
v_mfma_f32_16x16x32_bf16 v[80:83],a[132:135],v[168:171],v[80:83]
v_mfma_f32_16x16x32_bf16 v[80:83],a[136:139],v[176:179],v[80:83]
v_mfma_f32_16x16x32_bf16 v[80:83],a[140:143],v[184:187],v[80:83]
v_mfma_f32_16x16x32_bf16 v[96:99],a[128:131],v[192:195],0
v_mfma_f32_16x16x32_bf16 v[96:99],a[132:135],v[200:203],v[96:99]
v_mfma_f32_16x16x32_bf16 v[96:99],a[136:139],v[208:211],v[96:99]
v_mfma_f32_16x16x32_bf16 v[96:99],a[140:143],v[216:219],v[96:99]
v_mfma_f32_16x16x32_bf16 v[112:115],a[128:131],v[224:227],0
v_mfma_f32_16x16x32_bf16 v[112:115],a[132:135],v[232:235],v[112:115]
v_mfma_f32_16x16x32_bf16 v[112:115],a[136:139],v[240:243],v[112:115]
v_mfma_f32_16x16x32_bf16 v[112:115],a[140:143],v[248:251],v[112:115]
s_nop 7
v_max3_f32 v48,v48,v64,v65
v_max3_f32 v48,v48,v66,v67
v_max3_f32 v52,v52,v80,v81
v_max3_f32 v52,v52,v82,v83
v_max3_f32 v56,v56,v96,v97
v_max3_f32 v56,v56,v98,v99
v_max3_f32 v60,v60,v112,v113
v_max3_f32 v60,v60,v114,v115
s_add_u32 s65,s65,4096
s_sub_u32 s66,s66,1
s_cmp_lg_u32 s66,0
s_cbranch_scc1 L_s11_%=
v_mov_b32 v11,v48
s_nop 1
v_permlane32_swap_b32 v48,v11
v_max_f32 v48,v48,v11
v_mov_b32 v11,v48
s_nop 1
v_permlane16_swap_b32 v48,v11
v_max_f32 v48,v48,v11
v_sub_f32 v32,0,v48
v_sub_f32 v33,0,v48
v_sub_f32 v34,0,v48
v_sub_f32 v35,0,v48
v_mov_b32 v11,v52
s_nop 1
v_permlane32_swap_b32 v52,v11
v_max_f32 v52,v52,v11
v_mov_b32 v11,v52
s_nop 1
v_permlane16_swap_b32 v52,v11
v_max_f32 v52,v52,v11
v_sub_f32 v36,0,v52
v_sub_f32 v37,0,v52
v_sub_f32 v38,0,v52
v_sub_f32 v39,0,v52
v_mov_b32 v11,v56
s_nop 1
v_permlane32_swap_b32 v56,v11
v_max_f32 v56,v56,v11
v_mov_b32 v11,v56
s_nop 1
v_permlane16_swap_b32 v56,v11
v_max_f32 v56,v56,v11
v_sub_f32 v40,0,v56
v_sub_f32 v41,0,v56
v_sub_f32 v42,0,v56
v_sub_f32 v43,0,v56
v_mov_b32 v11,v60
s_nop 1
v_permlane32_swap_b32 v60,v11
v_max_f32 v60,v60,v11
v_mov_b32 v11,v60
s_nop 1
v_permlane16_swap_b32 v60,v11
v_max_f32 v60,v60,v11
v_max_f32 v60,s72,v60
v_sub_f32 v44,0,v60
v_sub_f32 v45,0,v60
v_sub_f32 v46,0,v60
v_sub_f32 v47,0,v60
v_mov_b32 v48,0
v_mov_b32 v49,0
v_mov_b32 v50,0
v_mov_b32 v51,0
v_mov_b32 v52,0
v_mov_b32 v53,0
v_mov_b32 v54,0
v_mov_b32 v55,0
v_mov_b32 v56,0
v_mov_b32 v57,0
v_mov_b32 v58,0
v_mov_b32 v59,0
v_mov_b32 v60,0
v_mov_b32 v61,0
v_mov_b32 v62,0
v_mov_b32 v63,0
v_accvgpr_write_b32 a0,0
v_accvgpr_write_b32 a1,0
v_accvgpr_write_b32 a2,0
v_accvgpr_write_b32 a3,0
v_accvgpr_write_b32 a4,0
v_accvgpr_write_b32 a5,0
v_accvgpr_write_b32 a6,0
v_accvgpr_write_b32 a7,0
v_accvgpr_write_b32 a8,0
v_accvgpr_write_b32 a9,0
v_accvgpr_write_b32 a10,0
v_accvgpr_write_b32 a11,0
v_accvgpr_write_b32 a12,0
v_accvgpr_write_b32 a13,0
v_accvgpr_write_b32 a14,0
v_accvgpr_write_b32 a15,0
v_accvgpr_write_b32 a16,0
v_accvgpr_write_b32 a17,0
v_accvgpr_write_b32 a18,0
v_accvgpr_write_b32 a19,0
v_accvgpr_write_b32 a20,0
v_accvgpr_write_b32 a21,0
v_accvgpr_write_b32 a22,0
v_accvgpr_write_b32 a23,0
v_accvgpr_write_b32 a24,0
v_accvgpr_write_b32 a25,0
v_accvgpr_write_b32 a26,0
v_accvgpr_write_b32 a27,0
v_accvgpr_write_b32 a28,0
v_accvgpr_write_b32 a29,0
v_accvgpr_write_b32 a30,0
v_accvgpr_write_b32 a31,0
v_mov_b32 v132,0
v_mov_b32 v133,0
v_mov_b32 v134,0
v_mov_b32 v135,0
v_mov_b32 v140,0
v_mov_b32 v141,0
v_mov_b32 v142,0
v_mov_b32 v143,0
v_mov_b32 v148,0
v_mov_b32 v149,0
v_mov_b32 v150,0
v_mov_b32 v151,0
v_mov_b32 v156,0
v_mov_b32 v157,0
v_mov_b32 v158,0
v_mov_b32 v159,0
v_mov_b32 v164,0
v_mov_b32 v165,0
v_mov_b32 v166,0
v_mov_b32 v167,0
v_mov_b32 v172,0
v_mov_b32 v173,0
v_mov_b32 v174,0
v_mov_b32 v175,0
v_mov_b32 v180,0
v_mov_b32 v181,0
v_mov_b32 v182,0
v_mov_b32 v183,0
v_mov_b32 v188,0
v_mov_b32 v189,0
v_mov_b32 v190,0
v_mov_b32 v191,0
v_mov_b32 v196,0
v_mov_b32 v197,0
v_mov_b32 v198,0
v_mov_b32 v199,0
)A"
R"A(v_mov_b32 v204,0
v_mov_b32 v205,0
v_mov_b32 v206,0
v_mov_b32 v207,0
v_mov_b32 v212,0
v_mov_b32 v213,0
v_mov_b32 v214,0
v_mov_b32 v215,0
v_mov_b32 v220,0
v_mov_b32 v221,0
v_mov_b32 v222,0
v_mov_b32 v223,0
v_mov_b32 v228,0
v_mov_b32 v229,0
v_mov_b32 v230,0
v_mov_b32 v231,0
v_mov_b32 v236,0
v_mov_b32 v237,0
v_mov_b32 v238,0
v_mov_b32 v239,0
v_mov_b32 v244,0
v_mov_b32 v245,0
v_mov_b32 v246,0
v_mov_b32 v247,0
v_mov_b32 v252,0
v_mov_b32 v253,0
v_mov_b32 v254,0
v_mov_b32 v255,0
v_accvgpr_write_b32 a32,0
v_accvgpr_write_b32 a33,0
v_accvgpr_write_b32 a34,0
v_accvgpr_write_b32 a35,0
v_accvgpr_write_b32 a36,0
v_accvgpr_write_b32 a37,0
v_accvgpr_write_b32 a38,0
v_accvgpr_write_b32 a39,0
v_accvgpr_write_b32 a40,0
v_accvgpr_write_b32 a41,0
v_accvgpr_write_b32 a42,0
v_accvgpr_write_b32 a43,0
v_accvgpr_write_b32 a44,0
v_accvgpr_write_b32 a45,0
v_accvgpr_write_b32 a46,0
v_accvgpr_write_b32 a47,0
v_accvgpr_write_b32 a48,0
v_accvgpr_write_b32 a49,0
v_accvgpr_write_b32 a50,0
v_accvgpr_write_b32 a51,0
v_accvgpr_write_b32 a52,0
v_accvgpr_write_b32 a53,0
v_accvgpr_write_b32 a54,0
v_accvgpr_write_b32 a55,0
v_accvgpr_write_b32 a56,0
v_accvgpr_write_b32 a57,0
v_accvgpr_write_b32 a58,0
v_accvgpr_write_b32 a59,0
v_accvgpr_write_b32 a60,0
v_accvgpr_write_b32 a61,0
v_accvgpr_write_b32 a62,0
v_accvgpr_write_b32 a63,0
s_mov_b32 s65,262144
s_mov_b32 s66,32
L_s21_%=:
s_add_u32 s67,s65,4096
buffer_load_dwordx4 a[128:131],v8,s[20:23],s65 offen
buffer_load_dwordx4 a[132:135],v8,s[20:23],s65 offen offset:64
buffer_load_dwordx4 a[136:139],v8,s[20:23],s65 offen offset:128
buffer_load_dwordx4 a[140:143],v8,s[20:23],s65 offen offset:192
buffer_load_dwordx4 a[144:147],v8,s[20:23],s67 offen
buffer_load_dwordx4 a[148:151],v8,s[20:23],s67 offen offset:64
buffer_load_dwordx4 a[152:155],v8,s[20:23],s67 offen offset:128
buffer_load_dwordx4 a[156:159],v8,s[20:23],s67 offen offset:192
s_waitcnt vmcnt(0)
v_mfma_f32_16x16x32_bf16 v[64:67],a[128:131],v[128:131],v[32:35]
v_mfma_f32_16x16x32_bf16 v[64:67],a[132:135],v[136:139],v[64:67]
v_mfma_f32_16x16x32_bf16 v[64:67],a[136:139],v[144:147],v[64:67]
v_mfma_f32_16x16x32_bf16 v[64:67],a[140:143],v[152:155],v[64:67]
v_mfma_f32_16x16x32_bf16 v[68:71],a[144:147],v[128:131],v[32:35]
v_mfma_f32_16x16x32_bf16 v[68:71],a[148:151],v[136:139],v[68:71]
v_mfma_f32_16x16x32_bf16 v[68:71],a[152:155],v[144:147],v[68:71]
v_mfma_f32_16x16x32_bf16 v[68:71],a[156:159],v[152:155],v[68:71]
v_mfma_f32_16x16x32_bf16 v[80:83],a[128:131],v[160:163],v[36:39]
v_mfma_f32_16x16x32_bf16 v[80:83],a[132:135],v[168:171],v[80:83]
v_mfma_f32_16x16x32_bf16 v[80:83],a[136:139],v[176:179],v[80:83]
v_mfma_f32_16x16x32_bf16 v[80:83],a[140:143],v[184:187],v[80:83]
v_mfma_f32_16x16x32_bf16 v[84:87],a[144:147],v[160:163],v[36:39]
v_mfma_f32_16x16x32_bf16 v[84:87],a[148:151],v[168:171],v[84:87]
v_mfma_f32_16x16x32_bf16 v[84:87],a[152:155],v[176:179],v[84:87]
v_mfma_f32_16x16x32_bf16 v[84:87],a[156:159],v[184:187],v[84:87]
v_mfma_f32_16x16x32_bf16 v[96:99],a[128:131],v[192:195],v[40:43]
v_mfma_f32_16x16x32_bf16 v[96:99],a[132:135],v[200:203],v[96:99]
v_mfma_f32_16x16x32_bf16 v[96:99],a[136:139],v[208:211],v[96:99]
v_mfma_f32_16x16x32_bf16 v[96:99],a[140:143],v[216:219],v[96:99]
v_mfma_f32_16x16x32_bf16 v[100:103],a[144:147],v[192:195],v[40:43]
v_mfma_f32_16x16x32_bf16 v[100:103],a[148:151],v[200:203],v[100:103]
v_mfma_f32_16x16x32_bf16 v[100:103],a[152:155],v[208:211],v[100:103]
v_mfma_f32_16x16x32_bf16 v[100:103],a[156:159],v[216:219],v[100:103]
v_mfma_f32_16x16x32_bf16 v[112:115],a[128:131],v[224:227],v[44:47]
v_mfma_f32_16x16x32_bf16 v[112:115],a[132:135],v[232:235],v[112:115]
v_mfma_f32_16x16x32_bf16 v[112:115],a[136:139],v[240:243],v[112:115]
v_mfma_f32_16x16x32_bf16 v[112:115],a[140:143],v[248:251],v[112:115]
v_mfma_f32_16x16x32_bf16 v[116:119],a[144:147],v[224:227],v[44:47]
v_mfma_f32_16x16x32_bf16 v[116:119],a[148:151],v[232:235],v[116:119]
v_mfma_f32_16x16x32_bf16 v[116:119],a[152:155],v[240:243],v[116:119]
v_mfma_f32_16x16x32_bf16 v[116:119],a[156:159],v[248:251],v[116:119]
s_nop 7
v_exp_f32 v64,v64
v_exp_f32 v65,v65
v_exp_f32 v66,v66
v_exp_f32 v67,v67
v_exp_f32 v68,v68
v_exp_f32 v69,v69
v_exp_f32 v70,v70
v_exp_f32 v71,v71
s_nop 0
v_cvt_pk_bf16_f32 v64,v64,v65
v_cvt_pk_bf16_f32 v65,v66,v67
v_cvt_pk_bf16_f32 v66,v68,v69
v_cvt_pk_bf16_f32 v67,v70,v71
v_exp_f32 v80,v80
v_exp_f32 v81,v81
v_exp_f32 v82,v82
v_exp_f32 v83,v83
v_exp_f32 v84,v84
v_exp_f32 v85,v85
v_exp_f32 v86,v86
v_exp_f32 v87,v87
s_nop 0
v_cvt_pk_bf16_f32 v80,v80,v81
v_cvt_pk_bf16_f32 v81,v82,v83
v_cvt_pk_bf16_f32 v82,v84,v85
v_cvt_pk_bf16_f32 v83,v86,v87
v_exp_f32 v96,v96
v_exp_f32 v97,v97
v_exp_f32 v98,v98
v_exp_f32 v99,v99
v_exp_f32 v100,v100
v_exp_f32 v101,v101
v_exp_f32 v102,v102
v_exp_f32 v103,v103
s_nop 0
v_cvt_pk_bf16_f32 v96,v96,v97
v_cvt_pk_bf16_f32 v97,v98,v99
v_cvt_pk_bf16_f32 v98,v100,v101
v_cvt_pk_bf16_f32 v99,v102,v103
v_exp_f32 v112,v112
v_exp_f32 v113,v113
v_exp_f32 v114,v114
v_exp_f32 v115,v115
v_exp_f32 v116,v116
v_exp_f32 v117,v117
v_exp_f32 v118,v118
v_exp_f32 v119,v119
s_nop 0
v_cvt_pk_bf16_f32 v112,v112,v113
v_cvt_pk_bf16_f32 v113,v114,v115
v_cvt_pk_bf16_f32 v114,v116,v117
v_cvt_pk_bf16_f32 v115,v118,v119
buffer_load_ushort v72,v9,s[24:27],s65 offen
buffer_load_ushort v73,v9,s[24:27],s65 offen offset:256
buffer_load_ushort v74,v9,s[24:27],s65 offen offset:512
buffer_load_ushort v75,v9,s[24:27],s65 offen offset:768
buffer_load_ushort v76,v10,s[24:27],s65 offen
buffer_load_ushort v77,v10,s[24:27],s65 offen offset:256
buffer_load_ushort v78,v10,s[24:27],s65 offen offset:512
buffer_load_ushort v79,v10,s[24:27],s65 offen offset:768
s_waitcnt vmcnt(0)
v_lshl_or_b32 v88,v73,16,v72
v_lshl_or_b32 v89,v75,16,v74
v_lshl_or_b32 v90,v77,16,v76
v_lshl_or_b32 v91,v79,16,v78
s_nop 1
v_mfma_f32_16x16x32_bf16 a[0:3],v[88:91],v[64:67],a[0:3]
v_mfma_f32_16x16x32_bf16 v[132:135],v[88:91],v[80:83],v[132:135]
v_mfma_f32_16x16x32_bf16 v[196:199],v[88:91],v[96:99],v[196:199]
v_mfma_f32_16x16x32_bf16 a[32:35],v[88:91],v[112:115],a[32:35]
buffer_load_ushort v72,v9,s[24:27],s65 offen offset:32
buffer_load_ushort v73,v9,s[24:27],s65 offen offset:288
buffer_load_ushort v74,v9,s[24:27],s65 offen offset:544
buffer_load_ushort v75,v9,s[24:27],s65 offen offset:800
buffer_load_ushort v76,v10,s[24:27],s65 offen offset:32
buffer_load_ushort v77,v10,s[24:27],s65 offen offset:288
buffer_load_ushort v78,v10,s[24:27],s65 offen offset:544
buffer_load_ushort v79,v10,s[24:27],s65 offen offset:800
s_waitcnt vmcnt(0)
v_lshl_or_b32 v88,v73,16,v72
v_lshl_or_b32 v89,v75,16,v74
v_lshl_or_b32 v90,v77,16,v76
v_lshl_or_b32 v91,v79,16,v78
s_nop 1
v_mfma_f32_16x16x32_bf16 a[4:7],v[88:91],v[64:67],a[4:7]
v_mfma_f32_16x16x32_bf16 v[140:143],v[88:91],v[80:83],v[140:143]
v_mfma_f32_16x16x32_bf16 v[204:207],v[88:91],v[96:99],v[204:207]
v_mfma_f32_16x16x32_bf16 a[36:39],v[88:91],v[112:115],a[36:39]
buffer_load_ushort v72,v9,s[24:27],s65 offen offset:64
buffer_load_ushort v73,v9,s[24:27],s65 offen offset:320
buffer_load_ushort v74,v9,s[24:27],s65 offen offset:576
buffer_load_ushort v75,v9,s[24:27],s65 offen offset:832
buffer_load_ushort v76,v10,s[24:27],s65 offen offset:64
buffer_load_ushort v77,v10,s[24:27],s65 offen offset:320
buffer_load_ushort v78,v10,s[24:27],s65 offen offset:576
buffer_load_ushort v79,v10,s[24:27],s65 offen offset:832
s_waitcnt vmcnt(0)
v_lshl_or_b32 v88,v73,16,v72
v_lshl_or_b32 v89,v75,16,v74
v_lshl_or_b32 v90,v77,16,v76
v_lshl_or_b32 v91,v79,16,v78
s_nop 1
v_mfma_f32_16x16x32_bf16 a[8:11],v[88:91],v[64:67],a[8:11]
v_mfma_f32_16x16x32_bf16 v[148:151],v[88:91],v[80:83],v[148:151]
v_mfma_f32_16x16x32_bf16 v[212:215],v[88:91],v[96:99],v[212:215]
v_mfma_f32_16x16x32_bf16 a[40:43],v[88:91],v[112:115],a[40:43]
buffer_load_ushort v72,v9,s[24:27],s65 offen offset:96
buffer_load_ushort v73,v9,s[24:27],s65 offen offset:352
buffer_load_ushort v74,v9,s[24:27],s65 offen offset:608
buffer_load_ushort v75,v9,s[24:27],s65 offen offset:864
buffer_load_ushort v76,v10,s[24:27],s65 offen offset:96
buffer_load_ushort v77,v10,s[24:27],s65 offen offset:352
buffer_load_ushort v78,v10,s[24:27],s65 offen offset:608
buffer_load_ushort v79,v10,s[24:27],s65 offen offset:864
s_waitcnt vmcnt(0)
v_lshl_or_b32 v88,v73,16,v72
v_lshl_or_b32 v89,v75,16,v74
v_lshl_or_b32 v90,v77,16,v76
v_lshl_or_b32 v91,v79,16,v78
s_nop 1
v_mfma_f32_16x16x32_bf16 a[12:15],v[88:91],v[64:67],a[12:15]
v_mfma_f32_16x16x32_bf16 v[156:159],v[88:91],v[80:83],v[156:159]
v_mfma_f32_16x16x32_bf16 v[220:223],v[88:91],v[96:99],v[220:223]
v_mfma_f32_16x16x32_bf16 a[44:47],v[88:91],v[112:115],a[44:47]
buffer_load_ushort v72,v9,s[24:27],s65 offen offset:128
buffer_load_ushort v73,v9,s[24:27],s65 offen offset:384
buffer_load_ushort v74,v9,s[24:27],s65 offen offset:640
buffer_load_ushort v75,v9,s[24:27],s65 offen offset:896
buffer_load_ushort v76,v10,s[24:27],s65 offen offset:128
buffer_load_ushort v77,v10,s[24:27],s65 offen offset:384
buffer_load_ushort v78,v10,s[24:27],s65 offen offset:640
buffer_load_ushort v79,v10,s[24:27],s65 offen offset:896
s_waitcnt vmcnt(0)
v_lshl_or_b32 v88,v73,16,v72
v_lshl_or_b32 v89,v75,16,v74
v_lshl_or_b32 v90,v77,16,v76
v_lshl_or_b32 v91,v79,16,v78
s_nop 1
v_mfma_f32_16x16x32_bf16 a[16:19],v[88:91],v[64:67],a[16:19]
v_mfma_f32_16x16x32_bf16 v[164:167],v[88:91],v[80:83],v[164:167]
v_mfma_f32_16x16x32_bf16 v[228:231],v[88:91],v[96:99],v[228:231]
v_mfma_f32_16x16x32_bf16 a[48:51],v[88:91],v[112:115],a[48:51]
buffer_load_ushort v72,v9,s[24:27],s65 offen offset:160
buffer_load_ushort v73,v9,s[24:27],s65 offen offset:416
buffer_load_ushort v74,v9,s[24:27],s65 offen offset:672
buffer_load_ushort v75,v9,s[24:27],s65 offen offset:928
buffer_load_ushort v76,v10,s[24:27],s65 offen offset:160
buffer_load_ushort v77,v10,s[24:27],s65 offen offset:416
buffer_load_ushort v78,v10,s[24:27],s65 offen offset:672
buffer_load_ushort v79,v10,s[24:27],s65 offen offset:928
s_waitcnt vmcnt(0)
v_lshl_or_b32 v88,v73,16,v72
v_lshl_or_b32 v89,v75,16,v74
v_lshl_or_b32 v90,v77,16,v76
v_lshl_or_b32 v91,v79,16,v78
s_nop 1
v_mfma_f32_16x16x32_bf16 a[20:23],v[88:91],v[64:67],a[20:23]
v_mfma_f32_16x16x32_bf16 v[172:175],v[88:91],v[80:83],v[172:175]
v_mfma_f32_16x16x32_bf16 v[236:239],v[88:91],v[96:99],v[236:239]
v_mfma_f32_16x16x32_bf16 a[52:55],v[88:91],v[112:115],a[52:55]
buffer_load_ushort v72,v9,s[24:27],s65 offen offset:192
buffer_load_ushort v73,v9,s[24:27],s65 offen offset:448
buffer_load_ushort v74,v9,s[24:27],s65 offen offset:704
buffer_load_ushort v75,v9,s[24:27],s65 offen offset:960
buffer_load_ushort v76,v10,s[24:27],s65 offen offset:192
buffer_load_ushort v77,v10,s[24:27],s65 offen offset:448
buffer_load_ushort v78,v10,s[24:27],s65 offen offset:704
buffer_load_ushort v79,v10,s[24:27],s65 offen offset:960
s_waitcnt vmcnt(0)
v_lshl_or_b32 v88,v73,16,v72
v_lshl_or_b32 v89,v75,16,v74
v_lshl_or_b32 v90,v77,16,v76
v_lshl_or_b32 v91,v79,16,v78
s_nop 1
v_mfma_f32_16x16x32_bf16 a[24:27],v[88:91],v[64:67],a[24:27]
v_mfma_f32_16x16x32_bf16 v[180:183],v[88:91],v[80:83],v[180:183]
v_mfma_f32_16x16x32_bf16 v[244:247],v[88:91],v[96:99],v[244:247]
v_mfma_f32_16x16x32_bf16 a[56:59],v[88:91],v[112:115],a[56:59]
buffer_load_ushort v72,v9,s[24:27],s65 offen offset:224
buffer_load_ushort v73,v9,s[24:27],s65 offen offset:480
buffer_load_ushort v74,v9,s[24:27],s65 offen offset:736
buffer_load_ushort v75,v9,s[24:27],s65 offen offset:992
buffer_load_ushort v76,v10,s[24:27],s65 offen offset:224
buffer_load_ushort v77,v10,s[24:27],s65 offen offset:480
buffer_load_ushort v78,v10,s[24:27],s65 offen offset:736
buffer_load_ushort v79,v10,s[24:27],s65 offen offset:992
s_waitcnt vmcnt(0)
v_lshl_or_b32 v88,v73,16,v72
v_lshl_or_b32 v89,v75,16,v74
v_lshl_or_b32 v90,v77,16,v76
v_lshl_or_b32 v91,v79,16,v78
s_nop 1
v_mfma_f32_16x16x32_bf16 a[28:31],v[88:91],v[64:67],a[28:31]
v_mfma_f32_16x16x32_bf16 v[188:191],v[88:91],v[80:83],v[188:191]
v_mfma_f32_16x16x32_bf16 v[252:255],v[88:91],v[96:99],v[252:255]
v_mfma_f32_16x16x32_bf16 a[60:63],v[88:91],v[112:115],a[60:63]
v_mfma_f32_16x16x32_bf16 v[48:51],v[28:31],v[64:67],v[48:51]
v_mfma_f32_16x16x32_bf16 v[52:55],v[28:31],v[80:83],v[52:55]
)A"
R"A(v_mfma_f32_16x16x32_bf16 v[56:59],v[28:31],v[96:99],v[56:59]
v_mfma_f32_16x16x32_bf16 v[60:63],v[28:31],v[112:115],v[60:63]
s_add_u32 s65,s65,8192
s_sub_u32 s66,s66,1
s_cmp_lg_u32 s66,0
s_cbranch_scc1 L_s21_%=
s_nop 7
v_max_f32 v49,0x00800000,v48
v_rcp_f32 v49,v49
v_max_f32 v53,0x00800000,v52
v_rcp_f32 v53,v53
v_max_f32 v57,0x00800000,v56
v_rcp_f32 v57,v57
v_max_f32 v61,0x00800000,v60
v_rcp_f32 v61,v61
v_accvgpr_read_b32 v16,a0
v_accvgpr_read_b32 v17,a1
v_accvgpr_read_b32 v18,a2
v_accvgpr_read_b32 v19,a3
v_accvgpr_read_b32 v20,a4
v_accvgpr_read_b32 v21,a5
v_accvgpr_read_b32 v22,a6
v_accvgpr_read_b32 v23,a7
v_mul_f32 v16,v16,v49
v_mul_f32 v17,v17,v49
v_mul_f32 v18,v18,v49
v_mul_f32 v19,v19,v49
v_mul_f32 v20,v20,v49
v_mul_f32 v21,v21,v49
v_mul_f32 v22,v22,v49
v_mul_f32 v23,v23,v49
v_cvt_pk_bf16_f32 v32,v16,v17
v_cvt_pk_bf16_f32 v33,v18,v19
v_cvt_pk_bf16_f32 v34,v20,v21
v_cvt_pk_bf16_f32 v35,v22,v23
v_accvgpr_read_b32 v8,a8
v_accvgpr_read_b32 v9,a9
v_accvgpr_read_b32 v10,a10
v_accvgpr_read_b32 v11,a11
v_accvgpr_read_b32 v12,a12
v_accvgpr_read_b32 v13,a13
v_accvgpr_read_b32 v14,a14
v_accvgpr_read_b32 v15,a15
v_mul_f32 v8,v8,v49
v_mul_f32 v9,v9,v49
v_mul_f32 v10,v10,v49
v_mul_f32 v11,v11,v49
v_mul_f32 v12,v12,v49
v_mul_f32 v13,v13,v49
v_mul_f32 v14,v14,v49
v_mul_f32 v15,v15,v49
v_cvt_pk_bf16_f32 v36,v8,v9
v_cvt_pk_bf16_f32 v37,v10,v11
v_cvt_pk_bf16_f32 v38,v12,v13
v_cvt_pk_bf16_f32 v39,v14,v15
v_accvgpr_read_b32 v16,a16
v_accvgpr_read_b32 v17,a17
v_accvgpr_read_b32 v18,a18
v_accvgpr_read_b32 v19,a19
v_accvgpr_read_b32 v20,a20
v_accvgpr_read_b32 v21,a21
v_accvgpr_read_b32 v22,a22
v_accvgpr_read_b32 v23,a23
v_mul_f32 v16,v16,v49
v_mul_f32 v17,v17,v49
v_mul_f32 v18,v18,v49
v_mul_f32 v19,v19,v49
v_mul_f32 v20,v20,v49
v_mul_f32 v21,v21,v49
v_mul_f32 v22,v22,v49
v_mul_f32 v23,v23,v49
v_cvt_pk_bf16_f32 v40,v16,v17
v_cvt_pk_bf16_f32 v41,v18,v19
v_cvt_pk_bf16_f32 v42,v20,v21
v_cvt_pk_bf16_f32 v43,v22,v23
ds_read_b64_tr_b16 v[64:65],v1
ds_read_b64_tr_b16 v[66:67],v1 offset:5120
ds_read_b64_tr_b16 v[68:69],v1 offset:10240
ds_read_b64_tr_b16 v[70:71],v1 offset:15360
ds_read_b64_tr_b16 v[72:73],v1 offset:20480
ds_read_b64_tr_b16 v[74:75],v1 offset:25600
ds_read_b64_tr_b16 v[76:77],v1 offset:30720
ds_read_b64_tr_b16 v[78:79],v1 offset:35840
ds_read_b64_tr_b16 v[80:81],v2 offset:32
ds_read_b64_tr_b16 v[82:83],v2 offset:5152
ds_read_b64_tr_b16 v[84:85],v2 offset:10272
ds_read_b64_tr_b16 v[86:87],v2 offset:15392
ds_read_b64_tr_b16 v[88:89],v2 offset:20512
ds_read_b64_tr_b16 v[90:91],v2 offset:25632
ds_read_b64_tr_b16 v[92:93],v2 offset:30752
ds_read_b64_tr_b16 v[94:95],v2 offset:35872
s_waitcnt vmcnt(0) lgkmcnt(0)
s_nop 7
s_branch L_ok1_%=
L_end_%=:
)A"
    :: "s"(ka), "s"(blk), "s"(wid)
    : "v0", "v1", "v2", "v3", "v4", "v5", "v6", "v7", "v8", "v9", "v10", "v11", "v12", "v13", "v14", "v15", "v16", "v17", "v18", "v19", "v20", "v21", "v22", "v23", "v24", "v25", "v26", "v27", "v28", "v29", "v30", "v31", "v32", "v33", "v34", "v35", "v36", "v37", "v38", "v39", "v40", "v41", "v42", "v43", "v44", "v45", "v46", "v47", "v48", "v49", "v50", "v51", "v52", "v53", "v54", "v55", "v56", "v57", "v58", "v59", "v60", "v61", "v62", "v63", "v64", "v65", "v66", "v67", "v68", "v69", "v70", "v71", "v72", "v73", "v74", "v75", "v76", "v77", "v78", "v79", "v80", "v81", "v82", "v83", "v84", "v85", "v86", "v87", "v88", "v89", "v90", "v91", "v92", "v93", "v94", "v95", "v96", "v97", "v98", "v99", "v100", "v101", "v102", "v103", "v104", "v105", "v106", "v107", "v108", "v109", "v110", "v111", "v112", "v113", "v114", "v115", "v116", "v117", "v118", "v119", "v120", "v121", "v122", "v123", "v124", "v125", "v126", "v127", "v128", "v129", "v130", "v131", "v132", "v133", "v134", "v135", "v136", "v137", "v138", "v139", "v140", "v141", "v142", "v143", "v144", "v145", "v146", "v147", "v148", "v149", "v150", "v151", "v152", "v153", "v154", "v155", "v156", "v157", "v158", "v159", "v160", "v161", "v162", "v163", "v164", "v165", "v166", "v167", "v168", "v169", "v170", "v171", "v172", "v173", "v174", "v175", "v176", "v177", "v178", "v179", "v180", "v181", "v182", "v183", "v184", "v185", "v186", "v187", "v188", "v189", "v190", "v191", "v192", "v193", "v194", "v195", "v196", "v197", "v198", "v199", "v200", "v201", "v202", "v203", "v204", "v205", "v206", "v207", "v208", "v209", "v210", "v211", "v212", "v213", "v214", "v215", "v216", "v217", "v218", "v219", "v220", "v221", "v222", "v223", "v224", "v225", "v226", "v227", "v228", "v229", "v230", "v231", "v232", "v233", "v234", "v235", "v236", "v237", "v238", "v239", "v240", "v241", "v242", "v243", "v244", "v245", "v246", "v247", "v248", "v249", "v250", "v251", "v252", "v253", "v254", "v255", "a0", "a1", "a2", "a3", "a4", "a5", "a6", "a7", "a8", "a9", "a10", "a11", "a12", "a13", "a14", "a15", "a16", "a17", "a18", "a19", "a20", "a21", "a22", "a23", "a24", "a25", "a26", "a27", "a28", "a29", "a30", "a31", "a32", "a33", "a34", "a35", "a36", "a37", "a38", "a39", "a40", "a41", "a42", "a43", "a44", "a45", "a46", "a47", "a48", "a49", "a50", "a51", "a52", "a53", "a54", "a55", "a56", "a57", "a58", "a59", "a60", "a61", "a62", "a63", "a64", "a65", "a66", "a67", "a68", "a69", "a70", "a71", "a72", "a73", "a74", "a75", "a76", "a77", "a78", "a79", "a80", "a81", "a82", "a83", "a84", "a85", "a86", "a87", "a88", "a89", "a90", "a91", "a92", "a93", "a94", "a95", "a96", "a97", "a98", "a99", "a100", "a101", "a102", "a103", "a104", "a105", "a106", "a107", "a108", "a109", "a110", "a111", "a112", "a113", "a114", "a115", "a116", "a117", "a118", "a119", "a120", "a121", "a122", "a123", "a124", "a125", "a126", "a127", "a128", "a129", "a130", "a131", "a132", "a133", "a134", "a135", "a136", "a137", "a138", "a139", "a140", "a141", "a142", "a143", "a144", "a145", "a146", "a147", "a148", "a149", "a150", "a151", "a152", "a153", "a154", "a155", "a156", "a157", "a158", "a159", "a160", "a161", "a162", "a163", "a164", "a165", "a166", "a167", "a168", "a169", "a170", "a171", "a172", "a173", "a174", "a175", "a176", "a177", "a178", "a179", "a180", "a181", "a182", "a183", "a184", "a185", "a186", "a187", "a188", "a189", "a190", "a191", "a192", "a193", "a194", "a195", "a196", "a197", "a198", "a199", "a200", "a201", "a202", "a203", "a204", "a205", "a206", "a207", "a208", "a209", "a210", "a211", "a212", "a213", "a214", "a215", "a216", "a217", "a218", "a219", "a220", "a221", "a222", "a223", "a224", "a225", "a226", "a227", "a228", "a229", "a230", "a231", "a232", "a233", "a234", "a235", "a236", "a237", "a238", "a239", "a240", "a241", "a242", "a243", "a244", "a245", "a246", "a247", "a248", "a249", "a250", "a251", "a252", "a253", "a254", "a255", "s4", "s5", "s6", "s7", "s8", "s9", "s10", "s11", "s12", "s13", "s14", "s15", "s16", "s17", "s18", "s19", "s20", "s21", "s22", "s23", "s24", "s25", "s26", "s27", "s28", "s29", "s30", "s31", "s36", "s37", "s38", "s39", "s40", "s41", "s42", "s43", "s44", "s45", "s46", "s47", "s48", "s49", "s50", "s51", "s52", "s53", "s54", "s55", "s56", "s57", "s58", "s59", "s60", "s61", "s62", "s63", "s64", "s65", "s66", "s67", "s68", "s69", "s70", "s71", "s72", "s73", "s74", "s75", "s76", "s77", "s78", "s79", "s80", "s81", "s82", "s83", "s84", "s85", "s86", "s87", "s88", "s89", "s90", "s91", "s92", "s93", "s94", "s95", "s96", "s97", "s98", "s99", "vcc", "scc", "memory");
}

extern "C" void kernel_launch(void* const* d_in, const int* in_sizes, int n_in,
                              void* d_out, int out_size, void* d_ws, size_t ws_size,
                              hipStream_t stream) {
  (void)hipFuncSetAttribute((const void*)qattn_kernel, hipFuncAttributeMaxDynamicSharedMemorySize, LDS_ATT);
  qattn_kernel<<<256, 256, LDS_ATT, stream>>>(d_in[0], d_in[1], d_in[2], d_in[3], d_ws, d_in[4], d_out);
}
```

```cpp
#include <hip/hip_runtime.h>
#include <hip/hip_bf16.h>

constexpr int LDS_ATT = 163840;

__global__ __launch_bounds__(256, 1) __attribute__((aligned(4096))) void qattn_kernel(const void* __restrict__ x, const void* __restrict__ kc, const void* __restrict__ vc,
                                                       const void* __restrict__ wq, void* __restrict__ ws, const void* __restrict__ wo,
                                                       void* __restrict__ out) {
  const unsigned blk = blockIdx.x;
  const unsigned wid = __builtin_amdgcn_readfirstlane(threadIdx.x >> 6);
  auto ka = __builtin_amdgcn_kernarg_segment_ptr();
  asm volatile(
R"A(s_load_dwordx8 s[4:11],%0,0x0
s_load_dwordx4 s[12:15],%0,0x20
s_load_dwordx2 s[16:17],%0,0x30
v_mbcnt_lo_u32_b32 v23,-1,0
v_mbcnt_hi_u32_b32 v23,-1,v23
s_mov_b32 s60,%2
s_and_b32 s66,%1,7
s_lshr_b32 s67,%1,3
s_getpc_b64 s[20:21]
L_pc_%=:
s_and_b32 s20,s20,0xffffff80
s_and_b32 s21,s21,0xffff
s_mov_b32 s22,L_hot_%=-L_pc_%=
s_mov_b32 s23,0x20000
s_lshl_b32 s68,s67,2
s_add_u32 s68,s68,s60
s_mul_i32 s68,s68,896
v_lshlrev_b32 v67,7,v23
s_mov_b64 exec,127
buffer_load_dword v8,v67,s[20:23],s68 offen
s_mov_b64 exec,-1
s_lshr_b32 s61,s66,2
s_lshl_b32 s61,s61,3
s_lshr_b32 s68,s67,2
s_add_u32 s61,s61,s68
s_and_b32 s62,s66,1
s_lshl_b32 s62,s62,2
s_and_b32 s68,s66,2
s_add_u32 s62,s62,s68
s_lshl_b32 s62,s62,1
s_and_b32 s68,s67,3
s_add_u32 s62,s62,s68
s_lshl_b32 s83,s62,9
s_mov_b32 s84,0
s_lshl_b32 s89,s60,13
s_add_u32 s85,s89,65536
s_add_u32 s88,s89,131072
v_and_b32 v20,3,v23
v_bfe_u32 v21,v23,2,2
v_lshrrev_b32 v22,4,v23
v_lshrrev_b32 v75,3,v23
s_lshl_b32 s66,s60,6
v_add_u32 v75,s66,v75
v_mul_u32_u24 v75,1280,v75
v_and_b32 v76,7,v23
v_or_b32 v77,0,v22
v_xor_b32 v77,v77,v76
v_lshl_add_u32 v77,v77,4,v75
v_add_u32 v32,0,v77
v_add_u32 v34,18432,v77
v_add_u32 v36,40960,v77
v_add_u32 v38,59392,v77
v_or_b32 v77,4,v22
v_xor_b32 v77,v77,v76
v_lshl_add_u32 v77,v77,4,v75
v_add_u32 v33,9216,v77
v_add_u32 v35,27648,v77
v_add_u32 v37,50176,v77
v_add_u32 v39,68608,v77
s_lshl_b32 s66,s60,17
v_lshlrev_b32 v75,13,v21
v_lshl_add_u32 v75,v22,6,v75
v_lshl_add_u32 v75,v20,4,v75
v_add_u32 v75,s66,v75
v_add_u32 v40,0,v75
v_add_u32 v41,256,v75
v_add_u32 v42,32768,v75
v_add_u32 v43,33024,v75
v_add_u32 v44,65536,v75
v_add_u32 v45,65792,v75
v_add_u32 v46,98304,v75
v_add_u32 v47,98560,v75
s_mul_i32 s66,s61,307200
s_waitcnt lgkmcnt(0)
s_add_u32 s28,s4,s66
s_addc_u32 s29,s5,0
s_and_b32 s29,s29,0xffff
s_mov_b32 s30,307200
s_mov_b32 s31,0x20000
s_mov_b32 s36,s10
s_and_b32 s37,s11,0xffff
s_mov_b32 s38,5242880
s_mov_b32 s39,0x20000
s_add_u32 m0,s85,0
s_nop 0
buffer_load_dwordx4 v32,s[28:31],s84 offen lds
buffer_load_dwordx4 v33,s[28:31],s84 offen offset:1024 lds
buffer_load_dwordx4 v34,s[28:31],s84 offen offset:2048 lds
buffer_load_dwordx4 v35,s[28:31],s84 offen offset:3072 lds
s_add_u32 m0,s85,4096
s_nop 0
buffer_load_dwordx4 v36,s[28:31],s84 offen lds
buffer_load_dwordx4 v37,s[28:31],s84 offen offset:1024 lds
buffer_load_dwordx4 v38,s[28:31],s84 offen offset:2048 lds
buffer_load_dwordx4 v39,s[28:31],s84 offen offset:3072 lds
s_add_u32 s84,s84,128
s_xor_b32 s85,s85,98304
s_add_u32 m0,s88,0
s_nop 0
buffer_load_dwordx4 v40,s[36:39],s83 offen lds
s_add_u32 m0,s88,1024
s_nop 0
buffer_load_dwordx4 v41,s[36:39],s83 offen lds
s_add_u32 m0,s88,2048
s_nop 0
buffer_load_dwordx4 v42,s[36:39],s83 offen lds
s_add_u32 m0,s88,3072
s_nop 0
buffer_load_dwordx4 v43,s[36:39],s83 offen lds
s_add_u32 m0,s88,4096
s_nop 0
buffer_load_dwordx4 v44,s[36:39],s83 offen lds
s_add_u32 m0,s88,5120
s_nop 0
buffer_load_dwordx4 v45,s[36:39],s83 offen lds
s_add_u32 m0,s88,6144
s_nop 0
buffer_load_dwordx4 v46,s[36:39],s83 offen lds
s_add_u32 m0,s88,7168
s_nop 0
buffer_load_dwordx4 v47,s[36:39],s83 offen lds
s_add_u32 s83,s83,524288
s_lshl_b32 s67,s61,21
s_add_u32 s68,s67,101711872
s_add_u32 s44,s12,s68
s_addc_u32 s45,s13,0
s_and_b32 s45,s45,0xffff
s_mov_b32 s46,4096
s_mov_b32 s47,0x20000
v_mov_b32 v10,0
buffer_load_dword v9,v10,s[44:47],0 offen sc1
s_cmp_lg_u32 s60,0
s_cbranch_scc1 L_nt_%=
s_add_u32 s67,s67,52224
s_add_u32 s20,s12,s67
s_addc_u32 s21,s13,0
global_load_dword a184,v10,s[20:21] nt
s_mul_i32 s68,s61,307200
s_add_u32 s20,s16,s68
s_addc_u32 s21,s17,0
global_load_dword a185,v10,s[20:21] nt
s_lshl_b32 s68,s61,4
s_add_u32 s68,s68,s62
s_lshl_b32 s68,s68,19
s_add_u32 s20,s6,s68
s_addc_u32 s21,s7,0
global_load_dword a186,v10,s[20:21] nt
s_add_u32 s20,s8,s68
s_addc_u32 s21,s9,0
global_load_dword a187,v10,s[20:21] nt
L_nt_%=:
v_lshlrev_b32 v6,4,v23
v_lshlrev_b32 v72,12,v22
v_lshl_add_u32 v72,v21,6,v72
v_lshl_add_u32 v72,v20,4,v72
v_subrev_u32 v51,s89,v72
v_add_u32 v50,131072,v72
v_and_b32 v74,15,v23
v_lshrrev_b32 v76,1,v74
v_lshlrev_b32 v75,7,v74
s_add_u32 s66,s89,65536
v_add_u32 v75,s66,v75
v_or_b32 v77,0,v22
v_xor_b32 v77,v77,v76
v_lshl_add_u32 v48,v77,4,v75
v_or_b32 v77,4,v22
v_xor_b32 v77,v77,v76
v_lshl_add_u32 v49,v77,4,v75
s_add_u32 s86,s89,131072
s_add_u32 s87,s89,98304
s_mov_b64 exec,0
s_cmp_eq_u32 s60,0
s_cbranch_scc1 L_pd1_%=
s_cmp_eq_u32 s60,1
s_cbranch_scc1 L_pd2_%=
s_cmp_eq_u32 s60,2
s_cbranch_scc1 L_pd3_%=
s_branch L_pd4_%=
L_trt0_%=:
s_mov_b64 exec,-1
s_mov_b32 s88,s87
s_add_u32 m0,s88,0
s_nop 0
buffer_load_dwordx4 v40,s[36:39],s83 offen lds
s_add_u32 m0,s88,1024
s_nop 0
buffer_load_dwordx4 v41,s[36:39],s83 offen lds
s_add_u32 m0,s88,2048
s_nop 0
buffer_load_dwordx4 v42,s[36:39],s83 offen lds
s_add_u32 m0,s88,3072
s_nop 0
buffer_load_dwordx4 v43,s[36:39],s83 offen lds
s_add_u32 m0,s88,4096
s_nop 0
buffer_load_dwordx4 v44,s[36:39],s83 offen lds
s_add_u32 m0,s88,5120
s_nop 0
buffer_load_dwordx4 v45,s[36:39],s83 offen lds
s_add_u32 m0,s88,6144
s_nop 0
buffer_load_dwordx4 v46,s[36:39],s83 offen lds
s_add_u32 m0,s88,7168
s_nop 0
buffer_load_dwordx4 v47,s[36:39],s83 offen lds
s_add_u32 s83,s83,524288
s_add_u32 s88,s89,0
s_mov_b64 exec,0
s_cmp_eq_u32 s60,0
s_cbranch_scc1 L_pd9_%=
s_cmp_eq_u32 s60,1
s_cbranch_scc1 L_pd10_%=
s_cmp_eq_u32 s60,2
s_cbranch_scc1 L_pd11_%=
s_branch L_pd12_%=
L_t1b_%=:
s_cmp_ge_u32 s62,8
s_cbranch_scc1 L_pd19_%=
s_branch L_pd14_%=
L_t1c_%=:
s_cmp_ge_u32 s62,8
s_cbranch_scc1 L_pd20_%=
s_branch L_pd15_%=
s_nop 0
s_nop 0
s_nop 0
s_nop 0
s_nop 0
s_nop 0
L_trt1_%=:
s_mov_b64 exec,-1
s_add_u32 m0,s85,0
s_nop 0
buffer_load_dwordx4 v32,s[28:31],s84 offen lds
buffer_load_dwordx4 v33,s[28:31],s84 offen offset:1024 lds
buffer_load_dwordx4 v34,s[28:31],s84 offen offset:2048 lds
buffer_load_dwordx4 v35,s[28:31],s84 offen offset:3072 lds
s_add_u32 m0,s85,4096
s_nop 0
buffer_load_dwordx4 v36,s[28:31],s84 offen lds
buffer_load_dwordx4 v37,s[28:31],s84 offen offset:1024 lds
buffer_load_dwordx4 v38,s[28:31],s84 offen offset:2048 lds
buffer_load_dwordx4 v39,s[28:31],s84 offen offset:3072 lds
s_add_u32 s84,s84,128
s_xor_b32 s85,s85,98304
s_branch L_pacx_%=
L_pace_%=:
s_sub_u32 s5,s4,1
s_max_i32 s5,s5,s7
L_pacl_%=:
s_sub_u32 s4,s4,1
s_cmp_eq_u32 s4,0
s_cbranch_scc1 L_pace2_%=
s_nop 2
s_branch L_pacl_%=
L_pace2_%=:
s_setpc_b64 s[92:93]
L_pacx_%=:
s_waitcnt vmcnt(25)
ds_read_b128 a[192:195],v48
ds_read_b128 a[196:199],v49
ds_read_b128 a[200:203],v48 offset:2048
ds_read_b128 a[204:207],v49 offset:2048
ds_read_b128 a[208:211],v48 offset:4096
ds_read_b128 a[212:215],v49 offset:4096
ds_read_b128 a[216:219],v48 offset:6144
ds_read_b128 a[220:223],v49 offset:6144
s_waitcnt vmcnt(17)
s_barrier
ds_read_b64_tr_b16 a[64:65],v50
ds_read_b64_tr_b16 a[66:67],v50 offset:2048
ds_read_b64_tr_b16 a[68:69],v50 offset:16384
ds_read_b64_tr_b16 a[70:71],v50 offset:18432
ds_read_b64_tr_b16 a[72:73],v50 offset:8
ds_read_b64_tr_b16 a[74:75],v50 offset:2056
ds_read_b64_tr_b16 a[76:77],v50 offset:16392
ds_read_b64_tr_b16 a[78:79],v50 offset:18440
s_waitcnt lgkmcnt(6)
v_mfma_f32_16x16x32_bf16 a[0:3],a[64:67],a[192:195],0
s_add_u32 m0,s88,0
ds_read_b64_tr_b16 a[80:81],v50 offset:256
v_xor_b32 v48,98304,v48
s_waitcnt lgkmcnt(5)
v_mfma_f32_16x16x32_bf16 a[0:3],a[68:71],a[196:199],a[0:3]
buffer_load_dwordx4 v40,s[36:39],s83 offen lds
v_mfma_f32_16x16x32_bf16 a[16:19],a[68:71],a[204:207],0
s_mov_b32 s69,s86
ds_read_b64_tr_b16 a[82:83],v50 offset:2304
v_xor_b32 v49,98304,v49
v_mfma_f32_16x16x32_bf16 a[16:19],a[64:67],a[200:203],a[16:19]
s_mov_b32 s86,s87
s_mov_b32 s87,s88
ds_read_b64_tr_b16 a[84:85],v50 offset:16640
v_mfma_f32_16x16x32_bf16 a[32:35],a[64:67],a[208:211],0
ds_read_b64_tr_b16 a[86:87],v50 offset:18688
v_mfma_f32_16x16x32_bf16 a[32:35],a[68:71],a[212:215],a[32:35]
ds_read_b64_tr_b16 a[88:89],v50 offset:264
v_mfma_f32_16x16x32_bf16 a[48:51],a[68:71],a[220:223],0
s_add_u32 m0,s88,1024
v_mfma_f32_16x16x32_bf16 a[48:51],a[64:67],a[216:219],a[48:51]
s_waitcnt lgkmcnt(5)
ds_read_b64_tr_b16 a[90:91],v50 offset:2312
v_mfma_f32_16x16x32_bf16 a[4:7],a[72:75],a[192:195],0
buffer_load_dwordx4 v41,s[36:39],s83 offen lds
v_mfma_f32_16x16x32_bf16 a[4:7],a[76:79],a[196:199],a[4:7]
s_lshl_b32 s67,s60,12
ds_read_b64_tr_b16 a[92:93],v50 offset:16648
v_cmp_lt_u32 vcc,6,v23
v_mfma_f32_16x16x32_bf16 a[20:23],a[76:79],a[204:207],0
s_lshl_b32 s65,s61,5
ds_read_b64_tr_b16 a[94:95],v50 offset:18696
v_lshlrev_b32 v19,3,v6
v_mfma_f32_16x16x32_bf16 a[20:23],a[72:75],a[200:203],a[20:23]
s_lshl_b32 s66,s62,1
ds_read_b64_tr_b16 a[96:97],v50 offset:512
v_cndmask_b32_e64 v19,v19,-1,vcc
v_mfma_f32_16x16x32_bf16 a[36:39],a[72:75],a[208:211],0
s_add_u32 s65,s65,s66
ds_read_b64_tr_b16 a[98:99],v50 offset:2560
v_lshrrev_b32 v18,1,v19
v_mfma_f32_16x16x32_bf16 a[36:39],a[76:79],a[212:215],a[36:39]
s_add_u32 m0,s88,2048
v_min_u32 v19,v19,v19
v_cndmask_b32_e64 v19,v19,v18,vcc
v_mfma_f32_16x16x32_bf16 a[52:55],a[76:79],a[220:223],0
s_lshl_b32 s66,s65,18
ds_read_b64_tr_b16 a[100:101],v50 offset:16896
v_lshlrev_b32 v11,8,v21
v_mfma_f32_16x16x32_bf16 a[52:55],a[72:75],a[216:219],a[52:55]
buffer_load_dwordx4 v42,s[36:39],s83 offen lds
s_waitcnt lgkmcnt(5)
v_mfma_f32_16x16x32_bf16 a[8:11],a[80:83],a[192:195],0
s_lshr_b32 s63,s62,2
ds_read_b64_tr_b16 a[102:103],v50 offset:18944
v_lshl_add_u32 v11,v22,6,v11
v_mfma_f32_16x16x32_bf16 a[8:11],a[84:87],a[196:199],a[8:11]
s_mov_b32 s22,524288
ds_read_b64_tr_b16 a[104:105],v50 offset:520
v_add_u32 v11,s67,v11
v_mfma_f32_16x16x32_bf16 a[24:27],a[84:87],a[204:207],0
s_mov_b32 s23,0x20000
ds_read_b64_tr_b16 a[106:107],v50 offset:2568
v_lshl_add_u32 v4,v20,4,v11
v_mfma_f32_16x16x32_bf16 a[24:27],a[80:83],a[200:203],a[24:27]
s_mov_b32 s26,524288
ds_read_b64_tr_b16 a[108:109],v50 offset:16904
v_xor_b32 v12,2,v20
v_mfma_f32_16x16x32_bf16 a[40:43],a[80:83],a[208:211],0
s_add_u32 m0,s88,3072
v_lshl_add_u32 v5,v12,4,v11
v_lshlrev_b32 v11,10,v21
v_mfma_f32_16x16x32_bf16 a[40:43],a[84:87],a[212:215],a[40:43]
s_mov_b32 s27,0x20000
ds_read_b64_tr_b16 a[110:111],v50 offset:18952
v_lshl_add_u32 v11,v20,6,v11
v_mfma_f32_16x16x32_bf16 a[56:59],a[84:87],a[220:223],0
buffer_load_dwordx4 v43,s[36:39],s83 offen lds
v_mfma_f32_16x16x32_bf16 a[56:59],a[80:83],a[216:219],a[56:59]
s_mov_b32 s42,131072
ds_read_b64_tr_b16 a[112:113],v50 offset:768
v_and_b32 v12,2,v21
v_mfma_f32_16x16x32_bf16 a[12:15],a[88:91],a[192:195],0
s_mov_b32 s43,0x20000
ds_read_b64_tr_b16 a[114:115],v50 offset:2816
v_xor_b32 v12,v12,v22
s_waitcnt lgkmcnt(6)
v_mfma_f32_16x16x32_bf16 a[12:15],a[92:95],a[196:199],a[12:15]
s_mov_b32 s48,s14
ds_read_b64_tr_b16 a[116:117],v50 offset:17152
v_lshl_add_u32 v11,v12,4,v11
v_mfma_f32_16x16x32_bf16 a[28:31],a[92:95],a[204:207],0
s_and_b32 s49,s15,0xffff
ds_read_b64_tr_b16 a[118:119],v50 offset:19200
v_add_u32 v1,98304,v11
v_mfma_f32_16x16x32_bf16 a[28:31],a[88:91],a[200:203],a[28:31]
s_add_u32 m0,s88,4096
v_lshlrev_b32 v11,10,v22
v_lshl_add_u32 v11,v21,6,v11
v_mfma_f32_16x16x32_bf16 a[44:47],a[88:91],a[208:211],0
s_mov_b32 s50,5242880
ds_read_b64_tr_b16 a[120:121],v50 offset:776
v_lshl_add_u32 v11,v20,3,v11
v_mfma_f32_16x16x32_bf16 a[44:47],a[92:95],a[212:215],a[44:47]
buffer_load_dwordx4 v44,s[36:39],s83 offen lds
v_mfma_f32_16x16x32_bf16 a[60:63],a[92:95],a[220:223],0
s_mov_b32 s51,0x20000
ds_read_b64_tr_b16 a[122:123],v50 offset:2824
v_add_u32 v11,114688,v11
v_mfma_f32_16x16x32_bf16 a[60:63],a[88:91],a[216:219],a[60:63]
s_add_u32 s76,s67,98304
ds_read_b64_tr_b16 a[124:125],v50 offset:17160
v_and_b32 v12,1,v22
v_mfma_f32_16x16x32_bf16 v[64:67],a[96:99],a[192:195],0
s_mul_i32 s74,s61,5
ds_read_b64_tr_b16 a[126:127],v50 offset:19208
v_lshl_add_u32 v2,v12,5,v11
v_mfma_f32_16x16x32_bf16 v[64:67],a[100:103],a[196:199],v[64:67]
s_add_u32 s74,s74,s62
ds_read_b64_tr_b16 a[64:65],v50 offset:1024
v_xor_b32 v12,1,v12
v_mfma_f32_16x16x32_bf16 v[80:83],a[100:103],a[204:207],0
s_add_u32 m0,s88,5120
v_lshl_add_u32 v3,v12,5,v11
v_add_u32 v11,0,v23
v_mfma_f32_16x16x32_bf16 v[80:83],a[96:99],a[200:203],v[80:83]
s_and_b32 s74,s74,15
ds_read_b64_tr_b16 a[66:67],v50 offset:3072
v_mul_u32_u24 v12,3277,v11
)A"
R"A(v_mfma_f32_16x16x32_bf16 v[96:99],a[96:99],a[208:211],0
buffer_load_dwordx4 v45,s[36:39],s83 offen lds
v_mfma_f32_16x16x32_bf16 v[96:99],a[100:103],a[212:215],v[96:99]
s_lshl_b32 s74,s74,14
ds_read_b64_tr_b16 a[68:69],v50 offset:17408
v_lshrrev_b32 v12,16,v12
v_mfma_f32_16x16x32_bf16 v[112:115],a[100:103],a[220:223],0
s_mov_b32 s75,0
ds_read_b64_tr_b16 a[70:71],v50 offset:19456
v_mul_u32_u24 v13,20,v12
v_mfma_f32_16x16x32_bf16 v[112:115],a[96:99],a[216:219],v[112:115]
s_mov_b32 s77,98304
ds_read_b64_tr_b16 a[72:73],v50 offset:1032
v_sub_u32 v13,v11,v13
s_waitcnt lgkmcnt(6)
v_mfma_f32_16x16x32_bf16 v[68:71],a[104:107],a[192:195],0
s_mov_b32 s71,0x3e0293ee
ds_read_b64_tr_b16 a[74:75],v50 offset:3080
v_bfe_u32 v14,v12,2,1
v_mfma_f32_16x16x32_bf16 v[68:71],a[108:111],a[196:199],v[68:71]
s_add_u32 m0,s88,6144
v_lshlrev_b32 v14,1,v14
v_xor_b32 v13,v13,v14
v_mfma_f32_16x16x32_bf16 v[84:87],a[108:111],a[204:207],0
s_mov_b32 s73,0x53800000
ds_read_b64_tr_b16 a[76:77],v50 offset:17416
v_lshrrev_b32 v14,2,v13
v_mfma_f32_16x16x32_bf16 v[84:87],a[104:107],a[200:203],v[84:87]
buffer_load_dwordx4 v46,s[36:39],s83 offen lds
v_mfma_f32_16x16x32_bf16 v[100:103],a[104:107],a[208:211],0
s_lshl_b32 s79,s60,14
ds_read_b64_tr_b16 a[78:79],v50 offset:19464
v_cmp_eq_u32 vcc,3,v14
v_mfma_f32_16x16x32_bf16 v[100:103],a[108:111],a[212:215],v[100:103]
s_add_u32 s20,s6,s66
s_addc_u32 s21,s7,0
ds_read_b64_tr_b16 a[80:81],v50 offset:1280
v_mfma_f32_16x16x32_bf16 v[116:119],a[108:111],a[220:223],0
s_add_u32 s24,s8,s66
s_addc_u32 s25,s9,0
ds_read_b64_tr_b16 a[82:83],v50 offset:3328
v_mfma_f32_16x16x32_bf16 v[116:119],a[104:107],a[216:219],v[116:119]
s_lshl_b32 s66,s65,16
s_add_u32 s65,s63,1
ds_read_b64_tr_b16 a[84:85],v50 offset:17664
v_mfma_f32_16x16x32_bf16 v[72:75],a[112:115],a[192:195],0
s_add_u32 m0,s88,7168
s_add_u32 s40,s12,s66
s_addc_u32 s41,s13,0
v_mfma_f32_16x16x32_bf16 v[72:75],a[116:119],a[196:199],v[72:75]
s_mul_i32 s66,s60,40960
ds_read_b64_tr_b16 a[86:87],v50 offset:19712
v_min_u32 v15,3,v14
v_mfma_f32_16x16x32_bf16 v[88:91],a[116:119],a[204:207],0
buffer_load_dwordx4 v47,s[36:39],s83 offen lds
v_mfma_f32_16x16x32_bf16 v[88:91],a[112:115],a[200:203],v[88:91]
s_add_u32 s83,s83,524288
ds_read_b64_tr_b16 a[88:89],v50 offset:1288
v_add_u32 v15,s65,v15
v_mfma_f32_16x16x32_bf16 v[104:107],a[112:115],a[208:211],0
s_mov_b32 s88,s69
ds_read_b64_tr_b16 a[90:91],v50 offset:3336
v_and_b32 v15,3,v15
v_mfma_f32_16x16x32_bf16 v[104:107],a[116:119],a[212:215],v[104:107]
s_and_b32 s21,s21,0xffff
ds_read_b64_tr_b16 a[92:93],v50 offset:17672
v_cndmask_b32_e64 v15,v15,4,vcc
v_mfma_f32_16x16x32_bf16 v[120:123],a[116:119],a[220:223],0
s_and_b32 s25,s25,0xffff
ds_read_b64_tr_b16 a[94:95],v50 offset:19720
v_and_b32 v13,3,v13
v_mfma_f32_16x16x32_bf16 v[120:123],a[112:115],a[216:219],v[120:123]
s_add_u32 m0,s85,0
v_lshl_add_u32 v13,v15,2,v13
v_mul_u32_u24 v12,1280,v12
v_mfma_f32_16x16x32_bf16 v[76:79],a[120:123],a[192:195],0
s_and_b32 s41,s41,0xffff
ds_read_b64_tr_b16 a[96:97],v50 offset:1536
v_lshl_add_u32 v12,v13,4,v12
s_waitcnt lgkmcnt(6)
v_mfma_f32_16x16x32_bf16 v[76:79],a[124:127],a[196:199],v[76:79]
buffer_load_dwordx4 v32,s[28:31],s84 offen lds
v_mfma_f32_16x16x32_bf16 v[92:95],a[124:127],a[204:207],0
s_add_u32 s80,s79,4096
ds_read_b64_tr_b16 a[98:99],v50 offset:3584
v_add_u32 v12,s66,v12
v_mfma_f32_16x16x32_bf16 v[92:95],a[120:123],a[200:203],v[92:95]
s_add_u32 s81,s79,8192
ds_read_b64_tr_b16 a[100:101],v50 offset:17920
v_subrev_u32 v24,0,v12
v_mfma_f32_16x16x32_bf16 v[108:111],a[120:123],a[208:211],0
s_add_u32 s82,s79,12288
ds_read_b64_tr_b16 a[102:103],v50 offset:19968
v_add_u32 v11,64,v23
v_mfma_f32_16x16x32_bf16 v[108:111],a[124:127],a[212:215],v[108:111]
s_and_b32 s64,s62,3
ds_read_b64_tr_b16 a[104:105],v50 offset:1544
v_mul_u32_u24 v12,3277,v11
v_mfma_f32_16x16x32_bf16 v[124:127],a[124:127],a[220:223],0
s_mov_b32 s72,0xff800000
v_lshrrev_b32 v12,16,v12
v_mul_u32_u24 v13,20,v12
v_mfma_f32_16x16x32_bf16 v[124:127],a[120:123],a[216:219],v[124:127]
s_lshl_b32 s18,s60,24
ds_read_b64_tr_b16 a[106:107],v50 offset:3592
v_sub_u32 v13,v11,v13
v_mfma_f32_16x16x32_bf16 v[128:131],a[64:67],a[192:195],0
buffer_load_dwordx4 v33,s[28:31],s84 offen offset:1024 lds
v_mfma_f32_16x16x32_bf16 v[128:131],a[68:71],a[196:199],v[128:131]
s_lshl_b32 s68,s62,6
ds_read_b64_tr_b16 a[108:109],v50 offset:17928
v_bfe_u32 v14,v12,2,1
v_mfma_f32_16x16x32_bf16 v[160:163],a[68:71],a[204:207],0
s_add_u32 s18,s18,s68
ds_read_b64_tr_b16 a[110:111],v50 offset:19976
v_lshlrev_b32 v14,1,v14
v_mfma_f32_16x16x32_bf16 v[160:163],a[64:67],a[200:203],v[160:163]
s_lshl_b32 s90,%1,2
ds_read_b64_tr_b16 a[112:113],v50 offset:1792
v_xor_b32 v13,v13,v14
v_mfma_f32_16x16x32_bf16 v[192:195],a[64:67],a[208:211],0
s_add_u32 s90,s90,s60
ds_read_b64_tr_b16 a[114:115],v50 offset:3840
v_lshrrev_b32 v14,2,v13
v_mfma_f32_16x16x32_bf16 v[192:195],a[68:71],a[212:215],v[192:195]
s_mul_i32 s90,s90,5120
v_cmp_eq_u32 vcc,3,v14
v_min_u32 v15,3,v14
v_mfma_f32_16x16x32_bf16 v[224:227],a[68:71],a[220:223],0
ds_read_b64_tr_b16 a[116:117],v50 offset:18176
v_add_u32 v15,s65,v15
v_mfma_f32_16x16x32_bf16 v[224:227],a[64:67],a[216:219],v[224:227]
buffer_load_dwordx4 v34,s[28:31],s84 offen offset:2048 lds
v_mfma_f32_16x16x32_bf16 v[132:135],a[72:75],a[192:195],0
ds_read_b64_tr_b16 a[118:119],v50 offset:20224
v_and_b32 v15,3,v15
v_mfma_f32_16x16x32_bf16 v[132:135],a[76:79],a[196:199],v[132:135]
ds_read_b64_tr_b16 a[120:121],v50 offset:1800
v_cndmask_b32_e64 v15,v15,4,vcc
v_mfma_f32_16x16x32_bf16 v[164:167],a[76:79],a[204:207],0
ds_read_b64_tr_b16 a[122:123],v50 offset:3848
v_and_b32 v13,3,v13
v_mfma_f32_16x16x32_bf16 v[164:167],a[72:75],a[200:203],v[164:167]
ds_read_b64_tr_b16 a[124:125],v50 offset:18184
v_lshl_add_u32 v13,v15,2,v13
v_mfma_f32_16x16x32_bf16 v[196:199],a[72:75],a[208:211],0
v_mul_u32_u24 v12,1280,v12
v_lshl_add_u32 v12,v13,4,v12
v_mfma_f32_16x16x32_bf16 v[196:199],a[76:79],a[212:215],v[196:199]
ds_read_b64_tr_b16 a[126:127],v50 offset:20232
v_add_u32 v50,s86,v51
v_mfma_f32_16x16x32_bf16 v[228:231],a[76:79],a[220:223],0
buffer_load_dwordx4 v35,s[28:31],s84 offen offset:3072 lds
v_mfma_f32_16x16x32_bf16 v[228:231],a[72:75],a[216:219],v[228:231]
v_add_u32 v12,s66,v12
v_subrev_u32 v25,1024,v12
v_mfma_f32_16x16x32_bf16 v[136:139],a[80:83],a[192:195],0
s_waitcnt lgkmcnt(4)
v_add_u32 v11,128,v23
v_mul_u32_u24 v12,3277,v11
v_mfma_f32_16x16x32_bf16 v[136:139],a[84:87],a[196:199],v[136:139]
v_lshrrev_b32 v12,16,v12
v_mul_u32_u24 v13,20,v12
v_mfma_f32_16x16x32_bf16 v[168:171],a[84:87],a[204:207],0
v_sub_u32 v13,v11,v13
v_bfe_u32 v14,v12,2,1
v_mfma_f32_16x16x32_bf16 v[168:171],a[80:83],a[200:203],v[168:171]
s_add_u32 m0,s85,4096
s_xor_b32 s85,s85,98304
v_lshlrev_b32 v14,1,v14
v_mfma_f32_16x16x32_bf16 v[200:203],a[80:83],a[208:211],0
v_xor_b32 v13,v13,v14
v_lshrrev_b32 v14,2,v13
v_mfma_f32_16x16x32_bf16 v[200:203],a[84:87],a[212:215],v[200:203]
buffer_load_dwordx4 v36,s[28:31],s84 offen lds
v_mfma_f32_16x16x32_bf16 v[232:235],a[84:87],a[220:223],0
v_cmp_eq_u32 vcc,3,v14
v_min_u32 v15,3,v14
v_mfma_f32_16x16x32_bf16 v[232:235],a[80:83],a[216:219],v[232:235]
v_add_u32 v15,s65,v15
v_and_b32 v15,3,v15
v_mfma_f32_16x16x32_bf16 v[140:143],a[88:91],a[192:195],0
v_cndmask_b32_e64 v15,v15,4,vcc
v_and_b32 v13,3,v13
v_mfma_f32_16x16x32_bf16 v[140:143],a[92:95],a[196:199],v[140:143]
v_lshl_add_u32 v13,v15,2,v13
v_mul_u32_u24 v12,1280,v12
v_mfma_f32_16x16x32_bf16 v[172:175],a[92:95],a[204:207],0
v_lshl_add_u32 v12,v13,4,v12
v_add_u32 v12,s66,v12
v_mfma_f32_16x16x32_bf16 v[172:175],a[88:91],a[200:203],v[172:175]
v_subrev_u32 v26,2048,v12
v_add_u32 v11,192,v23
v_mfma_f32_16x16x32_bf16 v[204:207],a[88:91],a[208:211],0
buffer_load_dwordx4 v37,s[28:31],s84 offen offset:1024 lds
v_mfma_f32_16x16x32_bf16 v[204:207],a[92:95],a[212:215],v[204:207]
v_mul_u32_u24 v12,3277,v11
v_lshrrev_b32 v12,16,v12
v_mfma_f32_16x16x32_bf16 v[236:239],a[92:95],a[220:223],0
v_mul_u32_u24 v13,20,v12
v_sub_u32 v13,v11,v13
v_mfma_f32_16x16x32_bf16 v[236:239],a[88:91],a[216:219],v[236:239]
v_bfe_u32 v14,v12,2,1
v_lshlrev_b32 v14,1,v14
s_waitcnt vmcnt(22) lgkmcnt(0)
s_barrier
v_mfma_f32_16x16x32_bf16 v[144:147],a[96:99],a[192:195],0
s_waitcnt vmcnt(14)
ds_read_b128 a[224:227],v48
v_xor_b32 v13,v13,v14
v_mfma_f32_16x16x32_bf16 v[144:147],a[100:103],a[196:199],v[144:147]
ds_read_b128 a[228:231],v49
v_lshrrev_b32 v14,2,v13
v_mfma_f32_16x16x32_bf16 v[176:179],a[100:103],a[204:207],0
buffer_load_dwordx4 v38,s[28:31],s84 offen offset:2048 lds
v_mfma_f32_16x16x32_bf16 v[176:179],a[96:99],a[200:203],v[176:179]
ds_read_b128 a[232:235],v48 offset:2048
v_cmp_eq_u32 vcc,3,v14
v_mfma_f32_16x16x32_bf16 v[208:211],a[96:99],a[208:211],0
ds_read_b128 a[236:239],v49 offset:2048
v_min_u32 v15,3,v14
v_mfma_f32_16x16x32_bf16 v[208:211],a[100:103],a[212:215],v[208:211]
ds_read_b128 a[240:243],v48 offset:4096
v_add_u32 v15,s65,v15
v_mfma_f32_16x16x32_bf16 v[240:243],a[100:103],a[220:223],0
ds_read_b128 a[244:247],v49 offset:4096
v_and_b32 v15,3,v15
v_mfma_f32_16x16x32_bf16 v[240:243],a[96:99],a[216:219],v[240:243]
v_cndmask_b32_e64 v15,v15,4,vcc
v_and_b32 v13,3,v13
v_mfma_f32_16x16x32_bf16 v[148:151],a[104:107],a[192:195],0
ds_read_b128 a[248:251],v48 offset:6144
v_lshl_add_u32 v13,v15,2,v13
v_mfma_f32_16x16x32_bf16 v[148:151],a[108:111],a[196:199],v[148:151]
buffer_load_dwordx4 v39,s[28:31],s84 offen offset:3072 lds
s_nop 0
L_pd1_%=:
s_cbranch_execz L_pd5_%=
v_mfma_f32_16x16x32_bf16 v[180:183],a[108:111],a[204:207],0
s_add_u32 s84,s84,128
ds_read_b128 a[252:255],v49 offset:6144
v_mul_u32_u24 v12,1280,v12
v_mfma_f32_16x16x32_bf16 v[180:183],a[104:107],a[200:203],v[180:183]
ds_read_b64_tr_b16 a[64:65],v50
v_lshl_add_u32 v12,v13,4,v12
v_mfma_f32_16x16x32_bf16 v[212:215],a[104:107],a[208:211],0
ds_read_b64_tr_b16 a[66:67],v50 offset:2048
v_add_u32 v12,s66,v12
v_mfma_f32_16x16x32_bf16 v[212:215],a[108:111],a[212:215],v[212:215]
ds_read_b64_tr_b16 a[68:69],v50 offset:16384
v_subrev_u32 v27,3072,v12
v_mfma_f32_16x16x32_bf16 v[244:247],a[108:111],a[220:223],0
v_add_u32 v11,256,v23
v_mul_u32_u24 v12,3277,v11
v_mfma_f32_16x16x32_bf16 v[244:247],a[104:107],a[216:219],v[244:247]
ds_read_b64_tr_b16 a[70:71],v50 offset:18432
v_lshrrev_b32 v12,16,v12
v_mfma_f32_16x16x32_bf16 v[152:155],a[112:115],a[192:195],0
ds_read_b64_tr_b16 a[72:73],v50 offset:8
v_mul_u32_u24 v13,20,v12
v_mfma_f32_16x16x32_bf16 v[152:155],a[116:119],a[196:199],v[152:155]
ds_read_b64_tr_b16 a[74:75],v50 offset:2056
v_sub_u32 v13,v11,v13
v_mfma_f32_16x16x32_bf16 v[184:187],a[116:119],a[204:207],0
v_bfe_u32 v14,v12,2,1
v_lshlrev_b32 v14,1,v14
v_mfma_f32_16x16x32_bf16 v[184:187],a[112:115],a[200:203],v[184:187]
ds_read_b64_tr_b16 a[76:77],v50 offset:16392
v_xor_b32 v13,v13,v14
v_mfma_f32_16x16x32_bf16 v[216:219],a[112:115],a[208:211],0
ds_read_b64_tr_b16 a[78:79],v50 offset:18440
v_lshrrev_b32 v14,2,v13
v_mfma_f32_16x16x32_bf16 v[216:219],a[116:119],a[212:215],v[216:219]
ds_read_b64_tr_b16 a[80:81],v50 offset:256
v_cmp_eq_u32 vcc,3,v14
v_mfma_f32_16x16x32_bf16 v[248:251],a[116:119],a[220:223],0
s_cmp_eq_u32 s60,3
s_cselect_b32 s72,0x7149f2ca,s72
v_readfirstlane_b32 s52,v9
v_mfma_f32_16x16x32_bf16 v[248:251],a[112:115],a[216:219],v[248:251]
s_add_u32 s52,s52,1
s_xor_b32 s53,s52,0x9e3779b9
ds_read_b64_tr_b16 a[82:83],v50 offset:2304
v_mfma_f32_16x16x32_bf16 v[156:159],a[120:123],a[192:195],0
ds_read_b64_tr_b16 a[84:85],v50 offset:16640
v_mov_b32 v28,0x3f803f80
v_mfma_f32_16x16x32_bf16 v[156:159],a[124:127],a[196:199],v[156:159]
ds_read_b64_tr_b16 a[86:87],v50 offset:18688
v_mov_b32 v29,0x3f803f80
v_mfma_f32_16x16x32_bf16 v[188:191],a[124:127],a[204:207],0
v_mov_b32 v30,0x3f803f80
v_mov_b32 v31,0x3f803f80
v_mfma_f32_16x16x32_bf16 v[188:191],a[120:123],a[200:203],v[188:191]
ds_read_b64_tr_b16 a[88:89],v50 offset:264
v_min_u32 v15,3,v14
v_mfma_f32_16x16x32_bf16 v[220:223],a[120:123],a[208:211],0
ds_read_b64_tr_b16 a[90:91],v50 offset:2312
v_add_u32 v15,s65,v15
v_mfma_f32_16x16x32_bf16 v[220:223],a[124:127],a[212:215],v[220:223]
ds_read_b64_tr_b16 a[92:93],v50 offset:16648
v_and_b32 v15,3,v15
v_mfma_f32_16x16x32_bf16 v[252:255],a[124:127],a[220:223],0
)A"
R"A(v_cndmask_b32_e64 v15,v15,4,vcc
v_and_b32 v13,3,v13
v_mfma_f32_16x16x32_bf16 v[252:255],a[120:123],a[216:219],v[252:255]
ds_read_b64_tr_b16 a[94:95],v50 offset:18696
v_lshl_add_u32 v13,v15,2,v13
v_mul_u32_u24 v12,1280,v12
v_lshl_add_u32 v12,v13,4,v12
v_add_u32 v12,s66,v12
v_subrev_u32 v7,0,v12
s_mov_b32 s70,3
s_memrealtime s[8:9]
s_memtime s[4:5]
L_qp_%=:
s_waitcnt lgkmcnt(6)
v_mfma_f32_16x16x32_bf16 a[0:3],a[64:67],a[224:227],a[0:3]
s_add_u32 m0,s88,0
ds_read_b64_tr_b16 a[96:97],v50 offset:512
v_xor_b32 v48,98304,v48
v_mfma_f32_16x16x32_bf16 a[0:3],a[68:71],a[228:231],a[0:3]
buffer_load_dwordx4 v40,s[36:39],s83 offen lds
v_mfma_f32_16x16x32_bf16 a[16:19],a[68:71],a[236:239],a[16:19]
s_mov_b32 s69,s86
ds_read_b64_tr_b16 a[98:99],v50 offset:2560
v_xor_b32 v49,98304,v49
v_mfma_f32_16x16x32_bf16 a[16:19],a[64:67],a[232:235],a[16:19]
s_mov_b32 s86,s87
s_mov_b32 s87,s88
ds_read_b64_tr_b16 a[100:101],v50 offset:16896
v_mfma_f32_16x16x32_bf16 a[32:35],a[64:67],a[240:243],a[32:35]
ds_read_b64_tr_b16 a[102:103],v50 offset:18944
v_mfma_f32_16x16x32_bf16 a[32:35],a[68:71],a[244:247],a[32:35]
ds_read_b64_tr_b16 a[104:105],v50 offset:520
v_mfma_f32_16x16x32_bf16 a[48:51],a[68:71],a[252:255],a[48:51]
s_add_u32 m0,s88,1024
v_mfma_f32_16x16x32_bf16 a[48:51],a[64:67],a[248:251],a[48:51]
ds_read_b64_tr_b16 a[106:107],v50 offset:2568
v_mfma_f32_16x16x32_bf16 a[4:7],a[72:75],a[224:227],a[4:7]
buffer_load_dwordx4 v41,s[36:39],s83 offen lds
v_mfma_f32_16x16x32_bf16 a[4:7],a[76:79],a[228:231],a[4:7]
ds_read_b64_tr_b16 a[108:109],v50 offset:16904
v_mfma_f32_16x16x32_bf16 a[20:23],a[76:79],a[236:239],a[20:23]
ds_read_b64_tr_b16 a[110:111],v50 offset:18952
v_mfma_f32_16x16x32_bf16 a[20:23],a[72:75],a[232:235],a[20:23]
ds_read_b64_tr_b16 a[112:113],v50 offset:768
v_mfma_f32_16x16x32_bf16 a[36:39],a[72:75],a[240:243],a[36:39]
ds_read_b64_tr_b16 a[114:115],v50 offset:2816
v_mfma_f32_16x16x32_bf16 a[36:39],a[76:79],a[244:247],a[36:39]
s_add_u32 m0,s88,2048
v_mfma_f32_16x16x32_bf16 a[52:55],a[76:79],a[252:255],a[52:55]
ds_read_b64_tr_b16 a[116:117],v50 offset:17152
v_mfma_f32_16x16x32_bf16 a[52:55],a[72:75],a[248:251],a[52:55]
buffer_load_dwordx4 v42,s[36:39],s83 offen lds
v_mfma_f32_16x16x32_bf16 a[8:11],a[80:83],a[224:227],a[8:11]
s_waitcnt lgkmcnt(5)
ds_read_b64_tr_b16 a[118:119],v50 offset:19200
v_mfma_f32_16x16x32_bf16 a[8:11],a[84:87],a[228:231],a[8:11]
ds_read_b64_tr_b16 a[120:121],v50 offset:776
v_mfma_f32_16x16x32_bf16 a[24:27],a[84:87],a[236:239],a[24:27]
ds_read_b64_tr_b16 a[122:123],v50 offset:2824
v_mfma_f32_16x16x32_bf16 a[24:27],a[80:83],a[232:235],a[24:27]
ds_read_b64_tr_b16 a[124:125],v50 offset:17160
v_mfma_f32_16x16x32_bf16 a[40:43],a[80:83],a[240:243],a[40:43]
s_add_u32 m0,s88,3072
v_mfma_f32_16x16x32_bf16 a[40:43],a[84:87],a[244:247],a[40:43]
ds_read_b64_tr_b16 a[126:127],v50 offset:19208
v_mfma_f32_16x16x32_bf16 a[56:59],a[84:87],a[252:255],a[56:59]
buffer_load_dwordx4 v43,s[36:39],s83 offen lds
v_mfma_f32_16x16x32_bf16 a[56:59],a[80:83],a[248:251],a[56:59]
ds_read_b64_tr_b16 a[64:65],v50 offset:1024
v_mfma_f32_16x16x32_bf16 a[12:15],a[88:91],a[224:227],a[12:15]
ds_read_b64_tr_b16 a[66:67],v50 offset:3072
v_mfma_f32_16x16x32_bf16 a[12:15],a[92:95],a[228:231],a[12:15]
ds_read_b64_tr_b16 a[68:69],v50 offset:17408
v_mfma_f32_16x16x32_bf16 a[28:31],a[92:95],a[236:239],a[28:31]
ds_read_b64_tr_b16 a[70:71],v50 offset:19456
v_mfma_f32_16x16x32_bf16 a[28:31],a[88:91],a[232:235],a[28:31]
s_add_u32 m0,s88,4096
v_mfma_f32_16x16x32_bf16 a[44:47],a[88:91],a[240:243],a[44:47]
ds_read_b64_tr_b16 a[72:73],v50 offset:1032
v_mfma_f32_16x16x32_bf16 a[44:47],a[92:95],a[244:247],a[44:47]
buffer_load_dwordx4 v44,s[36:39],s83 offen lds
v_mfma_f32_16x16x32_bf16 a[60:63],a[92:95],a[252:255],a[60:63]
ds_read_b64_tr_b16 a[74:75],v50 offset:3080
v_mfma_f32_16x16x32_bf16 a[60:63],a[88:91],a[248:251],a[60:63]
ds_read_b64_tr_b16 a[76:77],v50 offset:17416
v_mfma_f32_16x16x32_bf16 v[64:67],a[96:99],a[224:227],v[64:67]
ds_read_b64_tr_b16 a[78:79],v50 offset:19464
v_mfma_f32_16x16x32_bf16 v[64:67],a[100:103],a[228:231],v[64:67]
ds_read_b64_tr_b16 a[80:81],v50 offset:1280
v_mfma_f32_16x16x32_bf16 v[80:83],a[100:103],a[236:239],v[80:83]
s_add_u32 m0,s88,5120
v_mfma_f32_16x16x32_bf16 v[80:83],a[96:99],a[232:235],v[80:83]
ds_read_b64_tr_b16 a[82:83],v50 offset:3328
v_mfma_f32_16x16x32_bf16 v[96:99],a[96:99],a[240:243],v[96:99]
buffer_load_dwordx4 v45,s[36:39],s83 offen lds
v_mfma_f32_16x16x32_bf16 v[96:99],a[100:103],a[244:247],v[96:99]
ds_read_b64_tr_b16 a[84:85],v50 offset:17664
v_mfma_f32_16x16x32_bf16 v[112:115],a[100:103],a[252:255],v[112:115]
ds_read_b64_tr_b16 a[86:87],v50 offset:19712
v_mfma_f32_16x16x32_bf16 v[112:115],a[96:99],a[248:251],v[112:115]
ds_read_b64_tr_b16 a[88:89],v50 offset:1288
v_mfma_f32_16x16x32_bf16 v[68:71],a[104:107],a[224:227],v[68:71]
s_waitcnt lgkmcnt(5)
ds_read_b64_tr_b16 a[90:91],v50 offset:3336
v_mfma_f32_16x16x32_bf16 v[68:71],a[108:111],a[228:231],v[68:71]
s_add_u32 m0,s88,6144
v_mfma_f32_16x16x32_bf16 v[84:87],a[108:111],a[236:239],v[84:87]
ds_read_b64_tr_b16 a[92:93],v50 offset:17672
v_mfma_f32_16x16x32_bf16 v[84:87],a[104:107],a[232:235],v[84:87]
buffer_load_dwordx4 v46,s[36:39],s83 offen lds
v_mfma_f32_16x16x32_bf16 v[100:103],a[104:107],a[240:243],v[100:103]
ds_read_b64_tr_b16 a[94:95],v50 offset:19720
v_mfma_f32_16x16x32_bf16 v[100:103],a[108:111],a[244:247],v[100:103]
ds_read_b64_tr_b16 a[96:97],v50 offset:1536
v_mfma_f32_16x16x32_bf16 v[116:119],a[108:111],a[252:255],v[116:119]
ds_read_b64_tr_b16 a[98:99],v50 offset:3584
v_mfma_f32_16x16x32_bf16 v[116:119],a[104:107],a[248:251],v[116:119]
ds_read_b64_tr_b16 a[100:101],v50 offset:17920
v_mfma_f32_16x16x32_bf16 v[72:75],a[112:115],a[224:227],v[72:75]
s_add_u32 m0,s88,7168
s_mov_b32 s88,s69
s_mov_b32 s69,s86
v_mfma_f32_16x16x32_bf16 v[72:75],a[116:119],a[228:231],v[72:75]
ds_read_b64_tr_b16 a[102:103],v50 offset:19968
v_mfma_f32_16x16x32_bf16 v[88:91],a[116:119],a[236:239],v[88:91]
buffer_load_dwordx4 v47,s[36:39],s83 offen lds
v_mfma_f32_16x16x32_bf16 v[88:91],a[112:115],a[232:235],v[88:91]
s_add_u32 s83,s83,524288
ds_read_b64_tr_b16 a[104:105],v50 offset:1544
v_mfma_f32_16x16x32_bf16 v[104:107],a[112:115],a[240:243],v[104:107]
ds_read_b64_tr_b16 a[106:107],v50 offset:3592
v_mfma_f32_16x16x32_bf16 v[104:107],a[116:119],a[244:247],v[104:107]
ds_read_b64_tr_b16 a[108:109],v50 offset:17928
v_mfma_f32_16x16x32_bf16 v[120:123],a[116:119],a[252:255],v[120:123]
ds_read_b64_tr_b16 a[110:111],v50 offset:19976
v_mfma_f32_16x16x32_bf16 v[120:123],a[112:115],a[248:251],v[120:123]
s_add_u32 m0,s85,0
v_mfma_f32_16x16x32_bf16 v[76:79],a[120:123],a[224:227],v[76:79]
ds_read_b64_tr_b16 a[112:113],v50 offset:1792
v_mfma_f32_16x16x32_bf16 v[76:79],a[124:127],a[228:231],v[76:79]
buffer_load_dwordx4 v32,s[28:31],s84 offen lds
v_mfma_f32_16x16x32_bf16 v[92:95],a[124:127],a[236:239],v[92:95]
ds_read_b64_tr_b16 a[114:115],v50 offset:3840
v_mfma_f32_16x16x32_bf16 v[92:95],a[120:123],a[232:235],v[92:95]
ds_read_b64_tr_b16 a[116:117],v50 offset:18176
v_mfma_f32_16x16x32_bf16 v[108:111],a[120:123],a[240:243],v[108:111]
ds_read_b64_tr_b16 a[118:119],v50 offset:20224
v_mfma_f32_16x16x32_bf16 v[108:111],a[124:127],a[244:247],v[108:111]
s_cmp_eq_u32 s70,1
s_cselect_b32 s65,256,0
s_cselect_b32 s66,524288,0
v_mfma_f32_16x16x32_bf16 v[124:127],a[124:127],a[252:255],v[124:127]
ds_read_b64_tr_b16 a[124:125],v50 offset:18184
v_add_u32 v40,s65,v40
v_mfma_f32_16x16x32_bf16 v[124:127],a[120:123],a[248:251],v[124:127]
ds_read_b64_tr_b16 a[120:121],v50 offset:1800
v_add_u32 v41,s66,v41
v_mfma_f32_16x16x32_bf16 v[128:131],a[64:67],a[224:227],v[128:131]
buffer_load_dwordx4 v33,s[28:31],s84 offen offset:1024 lds
v_mfma_f32_16x16x32_bf16 v[128:131],a[68:71],a[228:231],v[128:131]
ds_read_b64_tr_b16 a[122:123],v50 offset:3848
v_add_u32 v42,s65,v42
v_mfma_f32_16x16x32_bf16 v[160:163],a[68:71],a[236:239],v[160:163]
ds_read_b64_tr_b16 a[126:127],v50 offset:20232
v_add_u32 v50,s86,v51
v_mfma_f32_16x16x32_bf16 v[160:163],a[64:67],a[232:235],v[160:163]
s_mov_b32 s86,s87
v_add_u32 v43,s66,v43
v_add_u32 v44,s65,v44
v_mfma_f32_16x16x32_bf16 v[192:195],a[64:67],a[240:243],v[192:195]
s_mov_b32 s87,s88
v_add_u32 v45,s66,v45
v_add_u32 v46,s65,v46
v_mfma_f32_16x16x32_bf16 v[192:195],a[68:71],a[244:247],v[192:195]
v_add_u32 v47,s66,v47
v_mfma_f32_16x16x32_bf16 v[224:227],a[68:71],a[252:255],v[224:227]
v_mfma_f32_16x16x32_bf16 v[224:227],a[64:67],a[248:251],v[224:227]
buffer_load_dwordx4 v34,s[28:31],s84 offen offset:2048 lds
v_mfma_f32_16x16x32_bf16 v[132:135],a[72:75],a[224:227],v[132:135]
v_mfma_f32_16x16x32_bf16 v[132:135],a[76:79],a[228:231],v[132:135]
v_mfma_f32_16x16x32_bf16 v[164:167],a[76:79],a[236:239],v[164:167]
v_mfma_f32_16x16x32_bf16 v[164:167],a[72:75],a[232:235],v[164:167]
v_mfma_f32_16x16x32_bf16 v[196:199],a[72:75],a[240:243],v[196:199]
v_mfma_f32_16x16x32_bf16 v[196:199],a[76:79],a[244:247],v[196:199]
v_mfma_f32_16x16x32_bf16 v[228:231],a[76:79],a[252:255],v[228:231]
buffer_load_dwordx4 v35,s[28:31],s84 offen offset:3072 lds
v_mfma_f32_16x16x32_bf16 v[228:231],a[72:75],a[248:251],v[228:231]
s_waitcnt lgkmcnt(0)
v_mfma_f32_16x16x32_bf16 v[136:139],a[80:83],a[224:227],v[136:139]
v_mfma_f32_16x16x32_bf16 v[136:139],a[84:87],a[228:231],v[136:139]
v_mfma_f32_16x16x32_bf16 v[168:171],a[84:87],a[236:239],v[168:171]
v_mfma_f32_16x16x32_bf16 v[168:171],a[80:83],a[232:235],v[168:171]
s_add_u32 m0,s85,4096
s_xor_b32 s85,s85,98304
v_mfma_f32_16x16x32_bf16 v[200:203],a[80:83],a[240:243],v[200:203]
v_mfma_f32_16x16x32_bf16 v[200:203],a[84:87],a[244:247],v[200:203]
buffer_load_dwordx4 v36,s[28:31],s84 offen lds
v_mfma_f32_16x16x32_bf16 v[232:235],a[84:87],a[252:255],v[232:235]
v_mfma_f32_16x16x32_bf16 v[232:235],a[80:83],a[248:251],v[232:235]
v_mfma_f32_16x16x32_bf16 v[140:143],a[88:91],a[224:227],v[140:143]
v_mfma_f32_16x16x32_bf16 v[140:143],a[92:95],a[228:231],v[140:143]
v_mfma_f32_16x16x32_bf16 v[172:175],a[92:95],a[236:239],v[172:175]
v_mfma_f32_16x16x32_bf16 v[172:175],a[88:91],a[232:235],v[172:175]
v_mfma_f32_16x16x32_bf16 v[204:207],a[88:91],a[240:243],v[204:207]
buffer_load_dwordx4 v37,s[28:31],s84 offen offset:1024 lds
v_mfma_f32_16x16x32_bf16 v[204:207],a[92:95],a[244:247],v[204:207]
v_mfma_f32_16x16x32_bf16 v[236:239],a[92:95],a[252:255],v[236:239]
v_mfma_f32_16x16x32_bf16 v[236:239],a[88:91],a[248:251],v[236:239]
s_waitcnt vmcnt(22)
s_barrier
v_mfma_f32_16x16x32_bf16 v[144:147],a[96:99],a[224:227],v[144:147]
s_waitcnt vmcnt(14)
ds_read_b128 a[192:195],v48
v_mfma_f32_16x16x32_bf16 v[144:147],a[100:103],a[228:231],v[144:147]
ds_read_b128 a[196:199],v49
v_mfma_f32_16x16x32_bf16 v[176:179],a[100:103],a[236:239],v[176:179]
buffer_load_dwordx4 v38,s[28:31],s84 offen offset:2048 lds
v_mfma_f32_16x16x32_bf16 v[176:179],a[96:99],a[232:235],v[176:179]
ds_read_b128 a[200:203],v48 offset:2048
v_mfma_f32_16x16x32_bf16 v[208:211],a[96:99],a[240:243],v[208:211]
ds_read_b128 a[204:207],v49 offset:2048
v_mfma_f32_16x16x32_bf16 v[208:211],a[100:103],a[244:247],v[208:211]
ds_read_b128 a[208:211],v48 offset:4096
v_mfma_f32_16x16x32_bf16 v[240:243],a[100:103],a[252:255],v[240:243]
ds_read_b128 a[212:215],v49 offset:4096
v_mfma_f32_16x16x32_bf16 v[240:243],a[96:99],a[248:251],v[240:243]
v_mfma_f32_16x16x32_bf16 v[148:151],a[104:107],a[224:227],v[148:151]
ds_read_b128 a[216:219],v48 offset:6144
v_xor_b32 v48,98304,v48
v_mfma_f32_16x16x32_bf16 v[148:151],a[108:111],a[228:231],v[148:151]
buffer_load_dwordx4 v39,s[28:31],s84 offen offset:3072 lds
v_mfma_f32_16x16x32_bf16 v[180:183],a[108:111],a[236:239],v[180:183]
s_add_u32 s84,s84,128
ds_read_b128 a[220:223],v49 offset:6144
v_xor_b32 v49,98304,v49
v_mfma_f32_16x16x32_bf16 v[180:183],a[104:107],a[232:235],v[180:183]
ds_read_b64_tr_b16 a[64:65],v50
v_mfma_f32_16x16x32_bf16 v[212:215],a[104:107],a[240:243],v[212:215]
ds_read_b64_tr_b16 a[66:67],v50 offset:2048
v_mfma_f32_16x16x32_bf16 v[212:215],a[108:111],a[244:247],v[212:215]
ds_read_b64_tr_b16 a[68:69],v50 offset:16384
v_mfma_f32_16x16x32_bf16 v[244:247],a[108:111],a[252:255],v[244:247]
v_mfma_f32_16x16x32_bf16 v[244:247],a[104:107],a[248:251],v[244:247]
)A"
R"A(ds_read_b64_tr_b16 a[70:71],v50 offset:18432
v_mfma_f32_16x16x32_bf16 v[152:155],a[112:115],a[224:227],v[152:155]
buffer_load_dword v9,v19,s[48:51],s90 offen
v_mfma_f32_16x16x32_bf16 v[152:155],a[116:119],a[228:231],v[152:155]
s_add_u32 s90,s90,896
ds_read_b64_tr_b16 a[72:73],v50 offset:8
v_mfma_f32_16x16x32_bf16 v[184:187],a[116:119],a[236:239],v[184:187]
ds_read_b64_tr_b16 a[74:75],v50 offset:2056
v_mfma_f32_16x16x32_bf16 v[184:187],a[112:115],a[232:235],v[184:187]
ds_read_b64_tr_b16 a[76:77],v50 offset:16392
v_mfma_f32_16x16x32_bf16 v[216:219],a[112:115],a[240:243],v[216:219]
ds_read_b64_tr_b16 a[78:79],v50 offset:18440
v_mfma_f32_16x16x32_bf16 v[216:219],a[116:119],a[244:247],v[216:219]
v_mfma_f32_16x16x32_bf16 v[248:251],a[116:119],a[252:255],v[248:251]
ds_read_b64_tr_b16 a[80:81],v50 offset:256
v_mfma_f32_16x16x32_bf16 v[248:251],a[112:115],a[248:251],v[248:251]
ds_read_b64_tr_b16 a[82:83],v50 offset:2304
v_mfma_f32_16x16x32_bf16 v[156:159],a[120:123],a[224:227],v[156:159]
ds_read_b64_tr_b16 a[84:85],v50 offset:16640
v_mfma_f32_16x16x32_bf16 v[156:159],a[124:127],a[228:231],v[156:159]
v_mfma_f32_16x16x32_bf16 v[188:191],a[124:127],a[236:239],v[188:191]
ds_read_b64_tr_b16 a[86:87],v50 offset:18688
v_mfma_f32_16x16x32_bf16 v[188:191],a[120:123],a[232:235],v[188:191]
ds_read_b64_tr_b16 a[88:89],v50 offset:264
v_mfma_f32_16x16x32_bf16 v[220:223],a[120:123],a[240:243],v[220:223]
ds_read_b64_tr_b16 a[90:91],v50 offset:2312
v_mfma_f32_16x16x32_bf16 v[220:223],a[124:127],a[244:247],v[220:223]
v_mfma_f32_16x16x32_bf16 v[252:255],a[124:127],a[252:255],v[252:255]
s_waitcnt lgkmcnt(4)
ds_read_b64_tr_b16 a[92:93],v50 offset:16648
v_mfma_f32_16x16x32_bf16 v[252:255],a[120:123],a[248:251],v[252:255]
s_add_u32 m0,s88,0
ds_read_b64_tr_b16 a[94:95],v50 offset:18696
v_mfma_f32_16x16x32_bf16 a[0:3],a[64:67],a[192:195],a[0:3]
ds_read_b64_tr_b16 a[96:97],v50 offset:512
v_mfma_f32_16x16x32_bf16 a[0:3],a[68:71],a[196:199],a[0:3]
buffer_load_dwordx4 v40,s[36:39],s83 offen lds
v_mfma_f32_16x16x32_bf16 a[16:19],a[68:71],a[204:207],a[16:19]
ds_read_b64_tr_b16 a[98:99],v50 offset:2560
v_mfma_f32_16x16x32_bf16 a[16:19],a[64:67],a[200:203],a[16:19]
ds_read_b64_tr_b16 a[100:101],v50 offset:16896
v_mfma_f32_16x16x32_bf16 a[32:35],a[64:67],a[208:211],a[32:35]
ds_read_b64_tr_b16 a[102:103],v50 offset:18944
v_mfma_f32_16x16x32_bf16 a[32:35],a[68:71],a[212:215],a[32:35]
v_mfma_f32_16x16x32_bf16 a[48:51],a[68:71],a[220:223],a[48:51]
s_add_u32 m0,s88,1024
ds_read_b64_tr_b16 a[104:105],v50 offset:520
v_mfma_f32_16x16x32_bf16 a[48:51],a[64:67],a[216:219],a[48:51]
ds_read_b64_tr_b16 a[106:107],v50 offset:2568
v_mfma_f32_16x16x32_bf16 a[4:7],a[72:75],a[192:195],a[4:7]
buffer_load_dwordx4 v41,s[36:39],s83 offen lds
v_mfma_f32_16x16x32_bf16 a[4:7],a[76:79],a[196:199],a[4:7]
ds_read_b64_tr_b16 a[108:109],v50 offset:16904
v_mfma_f32_16x16x32_bf16 a[20:23],a[76:79],a[204:207],a[20:23]
ds_read_b64_tr_b16 a[110:111],v50 offset:18952
v_mfma_f32_16x16x32_bf16 a[20:23],a[72:75],a[200:203],a[20:23]
ds_read_b64_tr_b16 a[112:113],v50 offset:768
v_mfma_f32_16x16x32_bf16 a[36:39],a[72:75],a[208:211],a[36:39]
ds_read_b64_tr_b16 a[114:115],v50 offset:2816
v_mfma_f32_16x16x32_bf16 a[36:39],a[76:79],a[212:215],a[36:39]
s_add_u32 m0,s88,2048
v_mfma_f32_16x16x32_bf16 a[52:55],a[76:79],a[220:223],a[52:55]
ds_read_b64_tr_b16 a[116:117],v50 offset:17152
v_mfma_f32_16x16x32_bf16 a[52:55],a[72:75],a[216:219],a[52:55]
buffer_load_dwordx4 v42,s[36:39],s83 offen lds
v_mfma_f32_16x16x32_bf16 a[8:11],a[80:83],a[192:195],a[8:11]
s_waitcnt lgkmcnt(5)
ds_read_b64_tr_b16 a[118:119],v50 offset:19200
v_mfma_f32_16x16x32_bf16 a[8:11],a[84:87],a[196:199],a[8:11]
ds_read_b64_tr_b16 a[120:121],v50 offset:776
v_mfma_f32_16x16x32_bf16 a[24:27],a[84:87],a[204:207],a[24:27]
ds_read_b64_tr_b16 a[122:123],v50 offset:2824
v_mfma_f32_16x16x32_bf16 a[24:27],a[80:83],a[200:203],a[24:27]
ds_read_b64_tr_b16 a[124:125],v50 offset:17160
v_mfma_f32_16x16x32_bf16 a[40:43],a[80:83],a[208:211],a[40:43]
s_add_u32 m0,s88,3072
v_mfma_f32_16x16x32_bf16 a[40:43],a[84:87],a[212:215],a[40:43]
ds_read_b64_tr_b16 a[126:127],v50 offset:19208
v_mfma_f32_16x16x32_bf16 a[56:59],a[84:87],a[220:223],a[56:59]
buffer_load_dwordx4 v43,s[36:39],s83 offen lds
v_mfma_f32_16x16x32_bf16 a[56:59],a[80:83],a[216:219],a[56:59]
ds_read_b64_tr_b16 a[64:65],v50 offset:1024
v_mfma_f32_16x16x32_bf16 a[12:15],a[88:91],a[192:195],a[12:15]
ds_read_b64_tr_b16 a[66:67],v50 offset:3072
v_mfma_f32_16x16x32_bf16 a[12:15],a[92:95],a[196:199],a[12:15]
ds_read_b64_tr_b16 a[68:69],v50 offset:17408
v_mfma_f32_16x16x32_bf16 a[28:31],a[92:95],a[204:207],a[28:31]
ds_read_b64_tr_b16 a[70:71],v50 offset:19456
v_mfma_f32_16x16x32_bf16 a[28:31],a[88:91],a[200:203],a[28:31]
s_add_u32 m0,s88,4096
v_mfma_f32_16x16x32_bf16 a[44:47],a[88:91],a[208:211],a[44:47]
ds_read_b64_tr_b16 a[72:73],v50 offset:1032
v_mfma_f32_16x16x32_bf16 a[44:47],a[92:95],a[212:215],a[44:47]
buffer_load_dwordx4 v44,s[36:39],s83 offen lds
v_mfma_f32_16x16x32_bf16 a[60:63],a[92:95],a[220:223],a[60:63]
ds_read_b64_tr_b16 a[74:75],v50 offset:3080
v_mfma_f32_16x16x32_bf16 a[60:63],a[88:91],a[216:219],a[60:63]
ds_read_b64_tr_b16 a[76:77],v50 offset:17416
v_mfma_f32_16x16x32_bf16 v[64:67],a[96:99],a[192:195],v[64:67]
ds_read_b64_tr_b16 a[78:79],v50 offset:19464
v_mfma_f32_16x16x32_bf16 v[64:67],a[100:103],a[196:199],v[64:67]
ds_read_b64_tr_b16 a[80:81],v50 offset:1280
v_mfma_f32_16x16x32_bf16 v[80:83],a[100:103],a[204:207],v[80:83]
s_add_u32 m0,s88,5120
v_mfma_f32_16x16x32_bf16 v[80:83],a[96:99],a[200:203],v[80:83]
ds_read_b64_tr_b16 a[82:83],v50 offset:3328
v_mfma_f32_16x16x32_bf16 v[96:99],a[96:99],a[208:211],v[96:99]
buffer_load_dwordx4 v45,s[36:39],s83 offen lds
v_mfma_f32_16x16x32_bf16 v[96:99],a[100:103],a[212:215],v[96:99]
ds_read_b64_tr_b16 a[84:85],v50 offset:17664
v_mfma_f32_16x16x32_bf16 v[112:115],a[100:103],a[220:223],v[112:115]
ds_read_b64_tr_b16 a[86:87],v50 offset:19712
v_mfma_f32_16x16x32_bf16 v[112:115],a[96:99],a[216:219],v[112:115]
ds_read_b64_tr_b16 a[88:89],v50 offset:1288
v_mfma_f32_16x16x32_bf16 v[68:71],a[104:107],a[192:195],v[68:71]
s_waitcnt lgkmcnt(5)
ds_read_b64_tr_b16 a[90:91],v50 offset:3336
v_mfma_f32_16x16x32_bf16 v[68:71],a[108:111],a[196:199],v[68:71]
s_add_u32 m0,s88,6144
v_mfma_f32_16x16x32_bf16 v[84:87],a[108:111],a[204:207],v[84:87]
ds_read_b64_tr_b16 a[92:93],v50 offset:17672
v_mfma_f32_16x16x32_bf16 v[84:87],a[104:107],a[200:203],v[84:87]
buffer_load_dwordx4 v46,s[36:39],s83 offen lds
v_mfma_f32_16x16x32_bf16 v[100:103],a[104:107],a[208:211],v[100:103]
ds_read_b64_tr_b16 a[94:95],v50 offset:19720
v_mfma_f32_16x16x32_bf16 v[100:103],a[108:111],a[212:215],v[100:103]
ds_read_b64_tr_b16 a[96:97],v50 offset:1536
v_mfma_f32_16x16x32_bf16 v[116:119],a[108:111],a[220:223],v[116:119]
ds_read_b64_tr_b16 a[98:99],v50 offset:3584
v_mfma_f32_16x16x32_bf16 v[116:119],a[104:107],a[216:219],v[116:119]
ds_read_b64_tr_b16 a[100:101],v50 offset:17920
v_mfma_f32_16x16x32_bf16 v[72:75],a[112:115],a[192:195],v[72:75]
s_add_u32 m0,s88,7168
s_mov_b32 s88,s69
v_mfma_f32_16x16x32_bf16 v[72:75],a[116:119],a[196:199],v[72:75]
ds_read_b64_tr_b16 a[102:103],v50 offset:19968
v_mfma_f32_16x16x32_bf16 v[88:91],a[116:119],a[204:207],v[88:91]
buffer_load_dwordx4 v47,s[36:39],s83 offen lds
v_mfma_f32_16x16x32_bf16 v[88:91],a[112:115],a[200:203],v[88:91]
s_add_u32 s83,s83,524288
ds_read_b64_tr_b16 a[104:105],v50 offset:1544
v_mfma_f32_16x16x32_bf16 v[104:107],a[112:115],a[208:211],v[104:107]
ds_read_b64_tr_b16 a[106:107],v50 offset:3592
v_mfma_f32_16x16x32_bf16 v[104:107],a[116:119],a[212:215],v[104:107]
ds_read_b64_tr_b16 a[108:109],v50 offset:17928
v_mfma_f32_16x16x32_bf16 v[120:123],a[116:119],a[220:223],v[120:123]
ds_read_b64_tr_b16 a[110:111],v50 offset:19976
v_mfma_f32_16x16x32_bf16 v[120:123],a[112:115],a[216:219],v[120:123]
s_add_u32 m0,s85,0
v_mfma_f32_16x16x32_bf16 v[76:79],a[120:123],a[192:195],v[76:79]
ds_read_b64_tr_b16 a[112:113],v50 offset:1792
v_mfma_f32_16x16x32_bf16 v[76:79],a[124:127],a[196:199],v[76:79]
buffer_load_dwordx4 v32,s[28:31],s84 offen lds
v_mfma_f32_16x16x32_bf16 v[92:95],a[124:127],a[204:207],v[92:95]
ds_read_b64_tr_b16 a[114:115],v50 offset:3840
v_mfma_f32_16x16x32_bf16 v[92:95],a[120:123],a[200:203],v[92:95]
ds_read_b64_tr_b16 a[116:117],v50 offset:18176
v_mfma_f32_16x16x32_bf16 v[108:111],a[120:123],a[208:211],v[108:111]
ds_read_b64_tr_b16 a[118:119],v50 offset:20224
v_mfma_f32_16x16x32_bf16 v[108:111],a[124:127],a[212:215],v[108:111]
v_mfma_f32_16x16x32_bf16 v[124:127],a[124:127],a[220:223],v[124:127]
ds_read_b64_tr_b16 a[124:125],v50 offset:18184
v_mfma_f32_16x16x32_bf16 v[124:127],a[120:123],a[216:219],v[124:127]
ds_read_b64_tr_b16 a[120:121],v50 offset:1800
v_mfma_f32_16x16x32_bf16 v[128:131],a[64:67],a[192:195],v[128:131]
buffer_load_dwordx4 v33,s[28:31],s84 offen offset:1024 lds
v_mfma_f32_16x16x32_bf16 v[128:131],a[68:71],a[196:199],v[128:131]
ds_read_b64_tr_b16 a[122:123],v50 offset:3848
v_mfma_f32_16x16x32_bf16 v[160:163],a[68:71],a[204:207],v[160:163]
ds_read_b64_tr_b16 a[126:127],v50 offset:20232
v_add_u32 v50,s86,v51
v_mfma_f32_16x16x32_bf16 v[160:163],a[64:67],a[200:203],v[160:163]
v_mfma_f32_16x16x32_bf16 v[192:195],a[64:67],a[208:211],v[192:195]
v_mfma_f32_16x16x32_bf16 v[192:195],a[68:71],a[212:215],v[192:195]
v_mfma_f32_16x16x32_bf16 v[224:227],a[68:71],a[220:223],v[224:227]
v_mfma_f32_16x16x32_bf16 v[224:227],a[64:67],a[216:219],v[224:227]
buffer_load_dwordx4 v34,s[28:31],s84 offen offset:2048 lds
v_mfma_f32_16x16x32_bf16 v[132:135],a[72:75],a[192:195],v[132:135]
v_mfma_f32_16x16x32_bf16 v[132:135],a[76:79],a[196:199],v[132:135]
v_mfma_f32_16x16x32_bf16 v[164:167],a[76:79],a[204:207],v[164:167]
v_mfma_f32_16x16x32_bf16 v[164:167],a[72:75],a[200:203],v[164:167]
v_mfma_f32_16x16x32_bf16 v[196:199],a[72:75],a[208:211],v[196:199]
v_mfma_f32_16x16x32_bf16 v[196:199],a[76:79],a[212:215],v[196:199]
v_mfma_f32_16x16x32_bf16 v[228:231],a[76:79],a[220:223],v[228:231]
buffer_load_dwordx4 v35,s[28:31],s84 offen offset:3072 lds
v_mfma_f32_16x16x32_bf16 v[228:231],a[72:75],a[216:219],v[228:231]
s_waitcnt lgkmcnt(0)
v_mfma_f32_16x16x32_bf16 v[136:139],a[80:83],a[192:195],v[136:139]
v_mfma_f32_16x16x32_bf16 v[136:139],a[84:87],a[196:199],v[136:139]
v_mfma_f32_16x16x32_bf16 v[168:171],a[84:87],a[204:207],v[168:171]
v_mfma_f32_16x16x32_bf16 v[168:171],a[80:83],a[200:203],v[168:171]
s_add_u32 m0,s85,4096
s_xor_b32 s85,s85,98304
v_mfma_f32_16x16x32_bf16 v[200:203],a[80:83],a[208:211],v[200:203]
v_mfma_f32_16x16x32_bf16 v[200:203],a[84:87],a[212:215],v[200:203]
buffer_load_dwordx4 v36,s[28:31],s84 offen lds
v_mfma_f32_16x16x32_bf16 v[232:235],a[84:87],a[220:223],v[232:235]
v_mfma_f32_16x16x32_bf16 v[232:235],a[80:83],a[216:219],v[232:235]
v_mfma_f32_16x16x32_bf16 v[140:143],a[88:91],a[192:195],v[140:143]
v_mfma_f32_16x16x32_bf16 v[140:143],a[92:95],a[196:199],v[140:143]
v_mfma_f32_16x16x32_bf16 v[172:175],a[92:95],a[204:207],v[172:175]
v_mfma_f32_16x16x32_bf16 v[172:175],a[88:91],a[200:203],v[172:175]
v_mfma_f32_16x16x32_bf16 v[204:207],a[88:91],a[208:211],v[204:207]
buffer_load_dwordx4 v37,s[28:31],s84 offen offset:1024 lds
v_mfma_f32_16x16x32_bf16 v[204:207],a[92:95],a[212:215],v[204:207]
v_mfma_f32_16x16x32_bf16 v[236:239],a[92:95],a[220:223],v[236:239]
v_mfma_f32_16x16x32_bf16 v[236:239],a[88:91],a[216:219],v[236:239]
s_waitcnt vmcnt(23)
s_barrier
v_mfma_f32_16x16x32_bf16 v[144:147],a[96:99],a[192:195],v[144:147]
s_waitcnt vmcnt(15)
ds_read_b128 a[224:227],v48
v_mfma_f32_16x16x32_bf16 v[144:147],a[100:103],a[196:199],v[144:147]
ds_read_b128 a[228:231],v49
v_mfma_f32_16x16x32_bf16 v[176:179],a[100:103],a[204:207],v[176:179]
buffer_load_dwordx4 v38,s[28:31],s84 offen offset:2048 lds
v_mfma_f32_16x16x32_bf16 v[176:179],a[96:99],a[200:203],v[176:179]
ds_read_b128 a[232:235],v48 offset:2048
v_mfma_f32_16x16x32_bf16 v[208:211],a[96:99],a[208:211],v[208:211]
ds_read_b128 a[236:239],v49 offset:2048
v_mfma_f32_16x16x32_bf16 v[208:211],a[100:103],a[212:215],v[208:211]
ds_read_b128 a[240:243],v48 offset:4096
v_mfma_f32_16x16x32_bf16 v[240:243],a[100:103],a[220:223],v[240:243]
)A"
R"A(ds_read_b128 a[244:247],v49 offset:4096
v_mfma_f32_16x16x32_bf16 v[240:243],a[96:99],a[216:219],v[240:243]
v_mfma_f32_16x16x32_bf16 v[148:151],a[104:107],a[192:195],v[148:151]
ds_read_b128 a[248:251],v48 offset:6144
v_mfma_f32_16x16x32_bf16 v[148:151],a[108:111],a[196:199],v[148:151]
buffer_load_dwordx4 v39,s[28:31],s84 offen offset:3072 lds
v_mfma_f32_16x16x32_bf16 v[180:183],a[108:111],a[204:207],v[180:183]
s_add_u32 s84,s84,128
ds_read_b128 a[252:255],v49 offset:6144
v_mfma_f32_16x16x32_bf16 v[180:183],a[104:107],a[200:203],v[180:183]
L_pd2_%=:
s_cbranch_execz L_pd6_%=
ds_read_b64_tr_b16 a[64:65],v50
v_mfma_f32_16x16x32_bf16 v[212:215],a[104:107],a[208:211],v[212:215]
ds_read_b64_tr_b16 a[66:67],v50 offset:2048
v_mfma_f32_16x16x32_bf16 v[212:215],a[108:111],a[212:215],v[212:215]
ds_read_b64_tr_b16 a[68:69],v50 offset:16384
v_mfma_f32_16x16x32_bf16 v[244:247],a[108:111],a[220:223],v[244:247]
v_mfma_f32_16x16x32_bf16 v[244:247],a[104:107],a[216:219],v[244:247]
ds_read_b64_tr_b16 a[70:71],v50 offset:18432
v_mfma_f32_16x16x32_bf16 v[152:155],a[112:115],a[192:195],v[152:155]
buffer_load_dword v8,v19,s[48:51],s90 offen
v_mfma_f32_16x16x32_bf16 v[152:155],a[116:119],a[196:199],v[152:155]
s_add_u32 s90,s90,896
ds_read_b64_tr_b16 a[72:73],v50 offset:8
v_mfma_f32_16x16x32_bf16 v[184:187],a[116:119],a[204:207],v[184:187]
ds_read_b64_tr_b16 a[74:75],v50 offset:2056
v_mfma_f32_16x16x32_bf16 v[184:187],a[112:115],a[200:203],v[184:187]
ds_read_b64_tr_b16 a[76:77],v50 offset:16392
v_mfma_f32_16x16x32_bf16 v[216:219],a[112:115],a[208:211],v[216:219]
ds_read_b64_tr_b16 a[78:79],v50 offset:18440
v_mfma_f32_16x16x32_bf16 v[216:219],a[116:119],a[212:215],v[216:219]
v_mfma_f32_16x16x32_bf16 v[248:251],a[116:119],a[220:223],v[248:251]
ds_read_b64_tr_b16 a[80:81],v50 offset:256
v_mfma_f32_16x16x32_bf16 v[248:251],a[112:115],a[216:219],v[248:251]
ds_read_b64_tr_b16 a[82:83],v50 offset:2304
v_mfma_f32_16x16x32_bf16 v[156:159],a[120:123],a[192:195],v[156:159]
ds_read_b64_tr_b16 a[84:85],v50 offset:16640
v_mfma_f32_16x16x32_bf16 v[156:159],a[124:127],a[196:199],v[156:159]
v_mfma_f32_16x16x32_bf16 v[188:191],a[124:127],a[204:207],v[188:191]
ds_read_b64_tr_b16 a[86:87],v50 offset:18688
v_mfma_f32_16x16x32_bf16 v[188:191],a[120:123],a[200:203],v[188:191]
ds_read_b64_tr_b16 a[88:89],v50 offset:264
v_mfma_f32_16x16x32_bf16 v[220:223],a[120:123],a[208:211],v[220:223]
ds_read_b64_tr_b16 a[90:91],v50 offset:2312
v_mfma_f32_16x16x32_bf16 v[220:223],a[124:127],a[212:215],v[220:223]
v_mfma_f32_16x16x32_bf16 v[252:255],a[124:127],a[220:223],v[252:255]
ds_read_b64_tr_b16 a[92:93],v50 offset:16648
v_mfma_f32_16x16x32_bf16 v[252:255],a[120:123],a[216:219],v[252:255]
ds_read_b64_tr_b16 a[94:95],v50 offset:18696
s_sub_u32 s70,s70,1
s_cmp_lg_u32 s70,0
s_cbranch_scc1 L_qp_%=
s_waitcnt lgkmcnt(6)
v_mfma_f32_16x16x32_bf16 a[0:3],a[64:67],a[224:227],a[0:3]
s_memrealtime s[92:93]
s_memtime s[56:57]
s_add_u32 m0,s85,0
v_mfma_f32_16x16x32_bf16 a[0:3],a[68:71],a[228:231],a[0:3]
buffer_load_dwordx4 v32,s[28:31],s84 offen lds
v_mfma_f32_16x16x32_bf16 a[16:19],a[68:71],a[236:239],a[16:19]
s_sub_u32 s83,s83,524288
ds_read_b64_tr_b16 a[96:97],v50 offset:512
v_subrev_u32 v40,256,v40
v_mfma_f32_16x16x32_bf16 a[16:19],a[64:67],a[232:235],a[16:19]
s_mov_b32 s69,s86
ds_read_b64_tr_b16 a[98:99],v50 offset:2560
v_subrev_u32 v41,256,v41
v_mfma_f32_16x16x32_bf16 a[32:35],a[64:67],a[240:243],a[32:35]
s_mov_b32 s86,s87
ds_read_b64_tr_b16 a[100:101],v50 offset:16896
v_subrev_u32 v42,256,v42
v_mfma_f32_16x16x32_bf16 a[32:35],a[68:71],a[244:247],a[32:35]
s_mov_b32 s87,s88
ds_read_b64_tr_b16 a[102:103],v50 offset:18944
v_subrev_u32 v43,256,v43
v_mfma_f32_16x16x32_bf16 a[48:51],a[68:71],a[252:255],a[48:51]
s_add_u32 s75,s75,16384
v_subrev_u32 v44,256,v44
v_subrev_u32 v45,256,v45
v_mfma_f32_16x16x32_bf16 a[48:51],a[64:67],a[248:251],a[48:51]
buffer_load_dwordx4 v33,s[28:31],s84 offen offset:1024 lds
v_mfma_f32_16x16x32_bf16 a[4:7],a[72:75],a[224:227],a[4:7]
s_sub_u32 s78,s77,32768
ds_read_b64_tr_b16 a[104:105],v50 offset:520
v_subrev_u32 v46,256,v46
v_mfma_f32_16x16x32_bf16 a[4:7],a[76:79],a[228:231],a[4:7]
s_and_b32 s78,s78,0x1ffff
ds_read_b64_tr_b16 a[106:107],v50 offset:2568
v_subrev_u32 v47,256,v47
v_mfma_f32_16x16x32_bf16 a[20:23],a[76:79],a[236:239],a[20:23]
s_sub_u32 s78,s78,s77
ds_read_b64_tr_b16 a[108:109],v50 offset:16904
v_xor_b32 v48,98304,v48
v_mfma_f32_16x16x32_bf16 a[20:23],a[72:75],a[232:235],a[20:23]
s_mov_b32 s65,262144
ds_read_b64_tr_b16 a[110:111],v50 offset:18952
v_xor_b32 v49,98304,v49
v_mfma_f32_16x16x32_bf16 a[36:39],a[72:75],a[240:243],a[36:39]
s_add_u32 s77,s77,s78
s_mov_b32 s6,0
v_add_u32 v52,s87,v51
v_mfma_f32_16x16x32_bf16 a[36:39],a[76:79],a[244:247],a[36:39]
buffer_load_dwordx4 v34,s[28:31],s84 offen offset:2048 lds
v_mfma_f32_16x16x32_bf16 a[52:55],a[76:79],a[252:255],a[52:55]
ds_read_b64_tr_b16 a[112:113],v50 offset:768
v_mfma_f32_16x16x32_bf16 a[52:55],a[72:75],a[248:251],a[52:55]
ds_read_b64_tr_b16 a[114:115],v50 offset:2816
v_mfma_f32_16x16x32_bf16 a[8:11],a[80:83],a[224:227],a[8:11]
s_waitcnt lgkmcnt(5)
ds_read_b64_tr_b16 a[116:117],v50 offset:17152
v_mfma_f32_16x16x32_bf16 a[8:11],a[84:87],a[228:231],a[8:11]
ds_read_b64_tr_b16 a[118:119],v50 offset:19200
v_mfma_f32_16x16x32_bf16 a[24:27],a[84:87],a[236:239],a[24:27]
v_mfma_f32_16x16x32_bf16 a[24:27],a[80:83],a[232:235],a[24:27]
buffer_load_dwordx4 v35,s[28:31],s84 offen offset:3072 lds
v_mfma_f32_16x16x32_bf16 a[40:43],a[80:83],a[240:243],a[40:43]
ds_read_b64_tr_b16 a[120:121],v50 offset:776
v_mfma_f32_16x16x32_bf16 a[40:43],a[84:87],a[244:247],a[40:43]
ds_read_b64_tr_b16 a[122:123],v50 offset:2824
v_mfma_f32_16x16x32_bf16 a[56:59],a[84:87],a[252:255],a[56:59]
ds_read_b64_tr_b16 a[124:125],v50 offset:17160
v_mfma_f32_16x16x32_bf16 a[56:59],a[80:83],a[248:251],a[56:59]
s_add_u32 m0,s85,4096
s_xor_b32 s85,s85,98304
ds_read_b64_tr_b16 a[126:127],v50 offset:19208
v_mfma_f32_16x16x32_bf16 a[12:15],a[88:91],a[224:227],a[12:15]
v_mfma_f32_16x16x32_bf16 a[12:15],a[92:95],a[228:231],a[12:15]
buffer_load_dwordx4 v36,s[28:31],s84 offen lds
v_mfma_f32_16x16x32_bf16 a[28:31],a[92:95],a[236:239],a[28:31]
ds_read_b64_tr_b16 a[64:65],v50 offset:1024
v_mfma_f32_16x16x32_bf16 a[28:31],a[88:91],a[232:235],a[28:31]
ds_read_b64_tr_b16 a[66:67],v50 offset:3072
v_mfma_f32_16x16x32_bf16 a[44:47],a[88:91],a[240:243],a[44:47]
ds_read_b64_tr_b16 a[68:69],v50 offset:17408
v_mfma_f32_16x16x32_bf16 a[44:47],a[92:95],a[244:247],a[44:47]
ds_read_b64_tr_b16 a[70:71],v50 offset:19456
v_mfma_f32_16x16x32_bf16 a[60:63],a[92:95],a[252:255],a[60:63]
v_mfma_f32_16x16x32_bf16 a[60:63],a[88:91],a[248:251],a[60:63]
buffer_load_dwordx4 v37,s[28:31],s84 offen offset:1024 lds
v_mfma_f32_16x16x32_bf16 v[64:67],a[96:99],a[224:227],v[64:67]
ds_read_b64_tr_b16 a[72:73],v50 offset:1032
v_mfma_f32_16x16x32_bf16 v[64:67],a[100:103],a[228:231],v[64:67]
ds_read_b64_tr_b16 a[74:75],v50 offset:3080
v_mfma_f32_16x16x32_bf16 v[80:83],a[100:103],a[236:239],v[80:83]
ds_read_b64_tr_b16 a[76:77],v50 offset:17416
v_mfma_f32_16x16x32_bf16 v[80:83],a[96:99],a[232:235],v[80:83]
ds_read_b64_tr_b16 a[78:79],v50 offset:19464
v_mfma_f32_16x16x32_bf16 v[96:99],a[96:99],a[240:243],v[96:99]
v_mfma_f32_16x16x32_bf16 v[96:99],a[100:103],a[244:247],v[96:99]
buffer_load_dwordx4 v38,s[28:31],s84 offen offset:2048 lds
v_mfma_f32_16x16x32_bf16 v[112:115],a[100:103],a[252:255],v[112:115]
ds_read_b64_tr_b16 a[80:81],v50 offset:1280
v_mfma_f32_16x16x32_bf16 v[112:115],a[96:99],a[248:251],v[112:115]
s_waitcnt lgkmcnt(5)
ds_read_b64_tr_b16 a[82:83],v50 offset:3328
v_mfma_f32_16x16x32_bf16 v[68:71],a[104:107],a[224:227],v[68:71]
ds_read_b64_tr_b16 a[84:85],v50 offset:17664
v_mfma_f32_16x16x32_bf16 v[68:71],a[108:111],a[228:231],v[68:71]
ds_read_b64_tr_b16 a[86:87],v50 offset:19712
v_mfma_f32_16x16x32_bf16 v[84:87],a[108:111],a[236:239],v[84:87]
v_mfma_f32_16x16x32_bf16 v[84:87],a[104:107],a[232:235],v[84:87]
buffer_load_dwordx4 v39,s[28:31],s84 offen offset:3072 lds
v_mfma_f32_16x16x32_bf16 v[100:103],a[104:107],a[240:243],v[100:103]
s_add_u32 s84,s84,128
ds_read_b64_tr_b16 a[88:89],v50 offset:1288
v_mfma_f32_16x16x32_bf16 v[100:103],a[108:111],a[244:247],v[100:103]
ds_read_b64_tr_b16 a[90:91],v50 offset:3336
v_mfma_f32_16x16x32_bf16 v[116:119],a[108:111],a[252:255],v[116:119]
ds_read_b64_tr_b16 a[92:93],v50 offset:17672
v_mfma_f32_16x16x32_bf16 v[116:119],a[104:107],a[248:251],v[116:119]
s_add_u32 m0,s88,0
ds_read_b64_tr_b16 a[94:95],v50 offset:19720
v_mfma_f32_16x16x32_bf16 v[72:75],a[112:115],a[224:227],v[72:75]
v_mfma_f32_16x16x32_bf16 v[72:75],a[116:119],a[228:231],v[72:75]
buffer_load_dwordx4 v40,s[36:39],s83 offen lds
v_mfma_f32_16x16x32_bf16 v[88:91],a[116:119],a[236:239],v[88:91]
ds_read_b64_tr_b16 a[96:97],v50 offset:1536
v_mfma_f32_16x16x32_bf16 v[88:91],a[112:115],a[232:235],v[88:91]
ds_read_b64_tr_b16 a[98:99],v50 offset:3584
v_mfma_f32_16x16x32_bf16 v[104:107],a[112:115],a[240:243],v[104:107]
ds_read_b64_tr_b16 a[100:101],v50 offset:17920
v_mfma_f32_16x16x32_bf16 v[104:107],a[116:119],a[244:247],v[104:107]
s_add_u32 m0,s88,1024
ds_read_b64_tr_b16 a[102:103],v50 offset:19968
v_mfma_f32_16x16x32_bf16 v[120:123],a[116:119],a[252:255],v[120:123]
v_mfma_f32_16x16x32_bf16 v[120:123],a[112:115],a[248:251],v[120:123]
buffer_load_dwordx4 v41,s[36:39],s83 offen lds
v_mfma_f32_16x16x32_bf16 v[76:79],a[120:123],a[224:227],v[76:79]
ds_read_b64_tr_b16 a[104:105],v50 offset:1544
v_mfma_f32_16x16x32_bf16 v[76:79],a[124:127],a[228:231],v[76:79]
ds_read_b64_tr_b16 a[106:107],v50 offset:3592
v_mfma_f32_16x16x32_bf16 v[92:95],a[124:127],a[236:239],v[92:95]
ds_read_b64_tr_b16 a[108:109],v50 offset:17928
v_mfma_f32_16x16x32_bf16 v[92:95],a[120:123],a[232:235],v[92:95]
s_add_u32 m0,s88,2048
ds_read_b64_tr_b16 a[110:111],v50 offset:19976
v_mfma_f32_16x16x32_bf16 v[108:111],a[120:123],a[240:243],v[108:111]
v_mfma_f32_16x16x32_bf16 v[108:111],a[124:127],a[244:247],v[108:111]
buffer_load_dwordx4 v42,s[36:39],s83 offen lds
v_mfma_f32_16x16x32_bf16 v[124:127],a[124:127],a[252:255],v[124:127]
ds_read_b64_tr_b16 a[112:113],v50 offset:1792
v_mfma_f32_16x16x32_bf16 v[124:127],a[120:123],a[248:251],v[124:127]
ds_read_b64_tr_b16 a[114:115],v50 offset:3840
v_mfma_f32_16x16x32_bf16 v[128:131],a[64:67],a[224:227],v[128:131]
ds_read_b64_tr_b16 a[116:117],v50 offset:18176
v_mfma_f32_16x16x32_bf16 v[128:131],a[68:71],a[228:231],v[128:131]
s_add_u32 m0,s88,3072
ds_read_b64_tr_b16 a[118:119],v50 offset:20224
v_mfma_f32_16x16x32_bf16 v[160:163],a[68:71],a[236:239],v[160:163]
v_mfma_f32_16x16x32_bf16 v[160:163],a[64:67],a[232:235],v[160:163]
buffer_load_dwordx4 v43,s[36:39],s83 offen lds
v_mfma_f32_16x16x32_bf16 v[192:195],a[64:67],a[240:243],v[192:195]
ds_read_b64_tr_b16 a[120:121],v50 offset:1800
v_mfma_f32_16x16x32_bf16 v[192:195],a[68:71],a[244:247],v[192:195]
ds_read_b64_tr_b16 a[122:123],v50 offset:3848
v_mfma_f32_16x16x32_bf16 v[224:227],a[68:71],a[252:255],v[224:227]
s_waitcnt lgkmcnt(4)
ds_read_b64_tr_b16 a[124:125],v50 offset:18184
v_mfma_f32_16x16x32_bf16 v[224:227],a[64:67],a[248:251],v[224:227]
s_add_u32 m0,s88,4096
ds_read_b64_tr_b16 a[126:127],v50 offset:20232
v_add_u32 v50,s86,v51
v_mfma_f32_16x16x32_bf16 v[132:135],a[72:75],a[224:227],v[132:135]
v_mfma_f32_16x16x32_bf16 v[132:135],a[76:79],a[228:231],v[132:135]
buffer_load_dwordx4 v44,s[36:39],s83 offen lds
v_mfma_f32_16x16x32_bf16 v[164:167],a[76:79],a[236:239],v[164:167]
v_mfma_f32_16x16x32_bf16 v[164:167],a[72:75],a[232:235],v[164:167]
v_mfma_f32_16x16x32_bf16 v[196:199],a[72:75],a[240:243],v[196:199]
v_mfma_f32_16x16x32_bf16 v[196:199],a[76:79],a[244:247],v[196:199]
s_add_u32 m0,s88,5120
v_mfma_f32_16x16x32_bf16 v[228:231],a[76:79],a[252:255],v[228:231]
v_mfma_f32_16x16x32_bf16 v[228:231],a[72:75],a[248:251],v[228:231]
buffer_load_dwordx4 v45,s[36:39],s83 offen lds
v_mfma_f32_16x16x32_bf16 v[136:139],a[80:83],a[224:227],v[136:139]
v_mfma_f32_16x16x32_bf16 v[136:139],a[84:87],a[228:231],v[136:139]
v_mfma_f32_16x16x32_bf16 v[168:171],a[84:87],a[236:239],v[168:171]
v_mfma_f32_16x16x32_bf16 v[168:171],a[80:83],a[232:235],v[168:171]
)A"
R"A(s_add_u32 m0,s88,6144
v_mfma_f32_16x16x32_bf16 v[200:203],a[80:83],a[240:243],v[200:203]
v_mfma_f32_16x16x32_bf16 v[200:203],a[84:87],a[244:247],v[200:203]
buffer_load_dwordx4 v46,s[36:39],s83 offen lds
v_mfma_f32_16x16x32_bf16 v[232:235],a[84:87],a[252:255],v[232:235]
v_mfma_f32_16x16x32_bf16 v[232:235],a[80:83],a[248:251],v[232:235]
v_mfma_f32_16x16x32_bf16 v[140:143],a[88:91],a[224:227],v[140:143]
v_mfma_f32_16x16x32_bf16 v[140:143],a[92:95],a[228:231],v[140:143]
s_add_u32 m0,s88,7168
s_mov_b32 s88,s69
s_and_b32 s69,s75,0xfffc0000
v_mfma_f32_16x16x32_bf16 v[172:175],a[92:95],a[236:239],v[172:175]
s_add_u32 s75,s75,16384
v_mfma_f32_16x16x32_bf16 v[172:175],a[88:91],a[232:235],v[172:175]
buffer_load_dwordx4 v47,s[36:39],s83 offen lds
v_mfma_f32_16x16x32_bf16 v[204:207],a[88:91],a[240:243],v[204:207]
s_waitcnt lgkmcnt(0)
s_add_u32 s83,s83,524288
s_sub_u32 s92,s92,s8
v_mfma_f32_16x16x32_bf16 v[204:207],a[92:95],a[244:247],v[204:207]
s_sub_u32 s56,s56,s4
s_mov_b32 s93,782
s_mul_i32 s92,s92,12600
v_mfma_f32_16x16x32_bf16 v[236:239],a[92:95],a[252:255],v[236:239]
s_cmp_ge_u32 s63,2
s_cselect_b32 s93,823,s93
s_mul_i32 s93,s93,s56
v_mfma_f32_16x16x32_bf16 v[236:239],a[88:91],a[248:251],v[236:239]
s_add_u32 s10,s20,s74
s_addc_u32 s11,s21,0
s_waitcnt vmcnt(25)
s_barrier
v_mfma_f32_16x16x32_bf16 v[144:147],a[96:99],a[224:227],v[144:147]
s_add_u32 m0,s76,0
s_waitcnt vmcnt(17)
ds_read_b128 a[192:195],v48
v_mfma_f32_16x16x32_bf16 v[144:147],a[100:103],a[228:231],v[144:147]
ds_read_b128 a[196:199],v49
v_mfma_f32_16x16x32_bf16 v[176:179],a[100:103],a[236:239],v[176:179]
global_load_lds_dwordx4 v4,s[10:11] nt
ds_read_b128 a[200:203],v48 offset:2048
v_mfma_f32_16x16x32_bf16 v[176:179],a[96:99],a[232:235],v[176:179]
ds_read_b128 a[204:207],v49 offset:2048
v_mfma_f32_16x16x32_bf16 v[208:211],a[96:99],a[240:243],v[208:211]
v_mfma_f32_16x16x32_bf16 v[208:211],a[100:103],a[244:247],v[208:211]
ds_read_b128 a[208:211],v48 offset:4096
v_mfma_f32_16x16x32_bf16 v[240:243],a[100:103],a[252:255],v[240:243]
ds_read_b128 a[212:215],v49 offset:4096
v_mfma_f32_16x16x32_bf16 v[240:243],a[96:99],a[248:251],v[240:243]
ds_read_b128 a[216:219],v48 offset:6144
v_xor_b32 v48,98304,v48
v_mfma_f32_16x16x32_bf16 v[148:151],a[104:107],a[224:227],v[148:151]
v_mfma_f32_16x16x32_bf16 v[148:151],a[108:111],a[228:231],v[148:151]
ds_read_b128 a[220:223],v49 offset:6144
v_xor_b32 v49,98304,v49
v_mfma_f32_16x16x32_bf16 v[180:183],a[108:111],a[236:239],v[180:183]
ds_read_b64_tr_b16 a[64:65],v50
v_mfma_f32_16x16x32_bf16 v[180:183],a[104:107],a[232:235],v[180:183]
ds_read_b64_tr_b16 a[66:67],v50 offset:2048
v_mfma_f32_16x16x32_bf16 v[212:215],a[104:107],a[240:243],v[212:215]
v_mfma_f32_16x16x32_bf16 v[212:215],a[108:111],a[244:247],v[212:215]
ds_read_b64_tr_b16 a[68:69],v50 offset:16384
v_mfma_f32_16x16x32_bf16 v[244:247],a[108:111],a[252:255],v[244:247]
ds_read_b64_tr_b16 a[70:71],v50 offset:18432
v_mfma_f32_16x16x32_bf16 v[244:247],a[104:107],a[248:251],v[244:247]
ds_read_b64_tr_b16 a[72:73],v50 offset:8
v_mfma_f32_16x16x32_bf16 v[152:155],a[112:115],a[224:227],v[152:155]
v_mfma_f32_16x16x32_bf16 v[152:155],a[116:119],a[228:231],v[152:155]
ds_read_b64_tr_b16 a[74:75],v50 offset:2056
v_mfma_f32_16x16x32_bf16 v[184:187],a[116:119],a[236:239],v[184:187]
global_load_lds_dwordx4 v4,s[10:11] offset:1024 nt
ds_read_b64_tr_b16 a[76:77],v50 offset:16392
v_mfma_f32_16x16x32_bf16 v[184:187],a[112:115],a[232:235],v[184:187]
ds_read_b64_tr_b16 a[78:79],v50 offset:18440
v_mfma_f32_16x16x32_bf16 v[216:219],a[112:115],a[240:243],v[216:219]
v_mfma_f32_16x16x32_bf16 v[216:219],a[116:119],a[244:247],v[216:219]
ds_read_b64_tr_b16 a[80:81],v50 offset:1024
v_mfma_f32_16x16x32_bf16 v[248:251],a[116:119],a[252:255],v[248:251]
ds_read_b64_tr_b16 a[82:83],v50 offset:3072
v_mfma_f32_16x16x32_bf16 v[248:251],a[112:115],a[248:251],v[248:251]
ds_read_b64_tr_b16 a[84:85],v50 offset:17408
v_mfma_f32_16x16x32_bf16 v[156:159],a[120:123],a[224:227],v[156:159]
v_mfma_f32_16x16x32_bf16 v[156:159],a[124:127],a[228:231],v[156:159]
ds_read_b64_tr_b16 a[86:87],v50 offset:19456
v_mfma_f32_16x16x32_bf16 v[188:191],a[124:127],a[236:239],v[188:191]
ds_read_b64_tr_b16 a[88:89],v50 offset:1032
v_mfma_f32_16x16x32_bf16 v[188:191],a[120:123],a[232:235],v[188:191]
s_cmp_lt_u32 s92,s93
s_cselect_b32 s5,5,0
s_cselect_b32 s7,2,0
s_cselect_b32 s9,8,10
ds_read_b64_tr_b16 a[90:91],v50 offset:3080
v_mfma_f32_16x16x32_bf16 v[220:223],a[120:123],a[240:243],v[220:223]
s_mov_b32 s4,s5
v_mfma_f32_16x16x32_bf16 v[220:223],a[124:127],a[244:247],v[220:223]
ds_read_b64_tr_b16 a[92:93],v50 offset:17416
v_mfma_f32_16x16x32_bf16 v[252:255],a[124:127],a[252:255],v[252:255]
ds_read_b64_tr_b16 a[94:95],v50 offset:19464
v_mfma_f32_16x16x32_bf16 v[252:255],a[120:123],a[248:251],v[252:255]
s_waitcnt vmcnt(10) lgkmcnt(5)
ds_read_b128 a[224:227],v48
v_mfma_f32_16x16x32_bf16 v[128:131],a[64:67],a[192:195],v[128:131]
v_mfma_f32_16x16x32_bf16 v[128:131],a[68:71],a[196:199],v[128:131]
ds_read_b128 a[228:231],v49
v_mfma_f32_16x16x32_bf16 v[160:163],a[68:71],a[204:207],v[160:163]
global_load_lds_dwordx4 v5,s[10:11] offset:2048 nt
ds_read_b128 a[236:239],v49 offset:2048
v_mfma_f32_16x16x32_bf16 v[160:163],a[64:67],a[200:203],v[160:163]
ds_read_b128 a[232:235],v48 offset:2048
v_mfma_f32_16x16x32_bf16 v[192:195],a[64:67],a[208:211],v[192:195]
v_mfma_f32_16x16x32_bf16 v[192:195],a[68:71],a[212:215],v[192:195]
ds_read_b128 a[240:243],v48 offset:4096
v_mfma_f32_16x16x32_bf16 v[224:227],a[68:71],a[220:223],v[224:227]
ds_read_b128 a[244:247],v49 offset:4096
v_mfma_f32_16x16x32_bf16 v[224:227],a[64:67],a[216:219],v[224:227]
ds_read_b128 a[252:255],v49 offset:6144
v_mfma_f32_16x16x32_bf16 v[132:135],a[72:75],a[192:195],v[132:135]
v_mfma_f32_16x16x32_bf16 v[132:135],a[76:79],a[196:199],v[132:135]
ds_read_b128 a[248:251],v48 offset:6144
v_mfma_f32_16x16x32_bf16 v[164:167],a[76:79],a[204:207],v[164:167]
ds_read_b64_tr_b16 a[96:97],v50 offset:256
v_mfma_f32_16x16x32_bf16 v[164:167],a[72:75],a[200:203],v[164:167]
ds_read_b64_tr_b16 a[98:99],v50 offset:2304
v_mfma_f32_16x16x32_bf16 v[196:199],a[72:75],a[208:211],v[196:199]
v_mfma_f32_16x16x32_bf16 v[196:199],a[76:79],a[212:215],v[196:199]
ds_read_b64_tr_b16 a[100:101],v50 offset:16640
v_mfma_f32_16x16x32_bf16 v[228:231],a[76:79],a[220:223],v[228:231]
ds_read_b64_tr_b16 a[102:103],v50 offset:18688
v_mfma_f32_16x16x32_bf16 v[228:231],a[72:75],a[216:219],v[228:231]
s_waitcnt lgkmcnt(5)
ds_read_b64_tr_b16 a[104:105],v50 offset:1280
v_mfma_f32_16x16x32_bf16 v[128:131],a[80:83],a[224:227],v[128:131]
v_mfma_f32_16x16x32_bf16 v[128:131],a[84:87],a[228:231],v[128:131]
ds_read_b64_tr_b16 a[106:107],v50 offset:3328
v_mfma_f32_16x16x32_bf16 v[160:163],a[84:87],a[236:239],v[160:163]
global_load_lds_dwordx4 v5,s[10:11] offset:3072
ds_read_b64_tr_b16 a[108:109],v50 offset:17664
v_mfma_f32_16x16x32_bf16 v[160:163],a[80:83],a[232:235],v[160:163]
ds_read_b64_tr_b16 a[110:111],v50 offset:19712
v_mfma_f32_16x16x32_bf16 v[192:195],a[80:83],a[240:243],v[192:195]
v_mfma_f32_16x16x32_bf16 v[192:195],a[84:87],a[244:247],v[192:195]
ds_read_b64_tr_b16 a[112:113],v50 offset:264
v_mul_f32 v128,s71,v128
v_mfma_f32_16x16x32_bf16 v[224:227],a[84:87],a[252:255],v[224:227]
s_waitcnt lgkmcnt(5)
ds_read_b64_tr_b16 a[114:115],v50 offset:2312
v_mul_f32 v129,s71,v129
v_mfma_f32_16x16x32_bf16 v[224:227],a[80:83],a[248:251],v[224:227]
ds_read_b64_tr_b16 a[116:117],v50 offset:16648
v_mul_f32 v130,s71,v130
v_mfma_f32_16x16x32_bf16 v[132:135],a[88:91],a[224:227],v[132:135]
v_mul_f32 v131,s71,v131
v_cvt_pk_bf16_f32 v128,v128,v129
v_mfma_f32_16x16x32_bf16 v[132:135],a[92:95],a[228:231],v[132:135]
ds_read_b64_tr_b16 a[118:119],v50 offset:18696
v_cvt_pk_bf16_f32 v129,v130,v131
v_mfma_f32_16x16x32_bf16 v[164:167],a[92:95],a[236:239],v[164:167]
ds_read_b64_tr_b16 a[120:121],v50 offset:1288
v_mul_f32 v192,s71,v192
v_mfma_f32_16x16x32_bf16 v[164:167],a[88:91],a[232:235],v[164:167]
ds_read_b64_tr_b16 a[122:123],v50 offset:3336
v_mul_f32 v193,s71,v193
v_mfma_f32_16x16x32_bf16 v[196:199],a[88:91],a[240:243],v[196:199]
v_mul_f32 v132,s71,v132
v_mul_f32 v133,s71,v133
v_mfma_f32_16x16x32_bf16 v[196:199],a[92:95],a[244:247],v[196:199]
ds_read_b64_tr_b16 a[124:125],v50 offset:17672
v_mul_f32 v134,s71,v134
v_mfma_f32_16x16x32_bf16 v[228:231],a[92:95],a[252:255],v[228:231]
s_add_u32 s10,s24,s74
s_addc_u32 s11,s25,0
ds_read_b64_tr_b16 a[126:127],v50 offset:19720
v_mfma_f32_16x16x32_bf16 v[228:231],a[88:91],a[248:251],v[228:231]
s_add_u32 s74,s74,16384
s_and_b32 s74,s74,0x3ffff
ds_read_b64_tr_b16 a[128:129],v50 offset:512
v_mfma_f32_16x16x32_bf16 v[136:139],a[96:99],a[192:195],v[136:139]
s_add_u32 m0,s76,16384
s_or_b32 s74,s74,s69
s_sub_u32 s76,s76,32768
v_mfma_f32_16x16x32_bf16 v[136:139],a[100:103],a[196:199],v[136:139]
s_and_b32 s76,s76,0x1ffff
s_and_b32 s69,s75,0xfffc0000
ds_read_b64_tr_b16 a[130:131],v50 offset:2560
s_waitcnt lgkmcnt(6)
v_mfma_f32_16x16x32_bf16 v[136:139],a[104:107],a[224:227],v[136:139]
global_load_lds_dwordx4 v4,s[10:11]
ds_read_b64_tr_b16 a[132:133],v50 offset:16896
v_mfma_f32_16x16x32_bf16 v[136:139],a[108:111],a[228:231],v[136:139]
s_add_u32 s75,s75,16384
ds_read_b64_tr_b16 a[134:135],v50 offset:18944
v_mul_f32 v135,s71,v135
v_mfma_f32_16x16x32_bf16 v[168:171],a[108:111],a[236:239],v[168:171]
v_cvt_pk_bf16_f32 v130,v132,v133
v_cvt_pk_bf16_f32 v131,v134,v135
v_mfma_f32_16x16x32_bf16 v[168:171],a[104:107],a[232:235],v[168:171]
ds_read_b64_tr_b16 a[136:137],v50 offset:1536
v_mul_f32 v194,s71,v194
v_mfma_f32_16x16x32_bf16 v[168:171],a[100:103],a[204:207],v[168:171]
ds_read_b64_tr_b16 a[138:139],v50 offset:3584
v_mul_f32 v195,s71,v195
v_mfma_f32_16x16x32_bf16 v[168:171],a[96:99],a[200:203],v[168:171]
ds_read_b64_tr_b16 a[140:141],v50 offset:17920
v_mul_f32 v196,s71,v196
v_mfma_f32_16x16x32_bf16 v[200:203],a[96:99],a[208:211],v[200:203]
v_mul_f32 v197,s71,v197
v_mul_f32 v198,s71,v198
v_mfma_f32_16x16x32_bf16 v[200:203],a[100:103],a[212:215],v[200:203]
ds_read_b64_tr_b16 a[142:143],v50 offset:19968
v_mul_f32 v199,s71,v199
v_mfma_f32_16x16x32_bf16 v[200:203],a[104:107],a[240:243],v[200:203]
ds_read_b64_tr_b16 a[144:145],v50 offset:520
v_cvt_pk_bf16_f32 v192,v192,v193
v_mfma_f32_16x16x32_bf16 v[200:203],a[108:111],a[244:247],v[200:203]
ds_read_b64_tr_b16 a[146:147],v50 offset:2568
v_cvt_pk_bf16_f32 v193,v194,v195
v_mfma_f32_16x16x32_bf16 v[232:235],a[108:111],a[252:255],v[232:235]
v_cvt_pk_bf16_f32 v194,v196,v197
v_cvt_pk_bf16_f32 v195,v198,v199
v_mfma_f32_16x16x32_bf16 v[232:235],a[104:107],a[248:251],v[232:235]
ds_read_b64_tr_b16 a[148:149],v50 offset:16904
v_mul_f32 v136,s71,v136
v_mfma_f32_16x16x32_bf16 v[232:235],a[100:103],a[220:223],v[232:235]
ds_read_b64_tr_b16 a[150:151],v50 offset:18952
v_mul_f32 v137,s71,v137
v_mfma_f32_16x16x32_bf16 v[232:235],a[96:99],a[216:219],v[232:235]
ds_read_b64_tr_b16 a[152:153],v50 offset:1544
v_mul_f32 v138,s71,v138
v_mfma_f32_16x16x32_bf16 v[140:143],a[112:115],a[192:195],v[140:143]
v_mul_f32 v139,s71,v139
v_cvt_pk_bf16_f32 v136,v136,v137
v_mfma_f32_16x16x32_bf16 v[140:143],a[116:119],a[196:199],v[140:143]
s_waitcnt lgkmcnt(5)
ds_read_b64_tr_b16 a[154:155],v50 offset:3592
v_cvt_pk_bf16_f32 v137,v138,v139
v_mfma_f32_16x16x32_bf16 v[140:143],a[120:123],a[224:227],v[140:143]
global_load_lds_dwordx4 v5,s[10:11] offset:1024
ds_read_b64_tr_b16 a[156:157],v50 offset:17928
v_mfma_f32_16x16x32_bf16 v[140:143],a[124:127],a[228:231],v[140:143]
ds_read_b64_tr_b16 a[158:159],v50 offset:19976
v_mul_f32 v160,s71,v160
v_mfma_f32_16x16x32_bf16 v[172:175],a[124:127],a[236:239],v[172:175]
v_mul_f32 v161,s71,v161
v_mul_f32 v162,s71,v162
v_mfma_f32_16x16x32_bf16 v[172:175],a[120:123],a[232:235],v[172:175]
ds_read_b64_tr_b16 a[160:161],v50 offset:768
v_mul_f32 v163,s71,v163
v_mfma_f32_16x16x32_bf16 v[172:175],a[116:119],a[204:207],v[172:175]
ds_read_b64_tr_b16 a[162:163],v50 offset:2816
v_mul_f32 v140,s71,v140
v_mfma_f32_16x16x32_bf16 v[172:175],a[112:115],a[200:203],v[172:175]
ds_read_b64_tr_b16 a[164:165],v50 offset:17152
v_mul_f32 v141,s71,v141
v_mfma_f32_16x16x32_bf16 v[204:207],a[112:115],a[208:211],v[204:207]
)A"
R"A(v_mul_f32 v142,s71,v142
v_mul_f32 v143,s71,v143
v_mfma_f32_16x16x32_bf16 v[204:207],a[116:119],a[212:215],v[204:207]
ds_read_b64_tr_b16 a[166:167],v50 offset:19200
v_cvt_pk_bf16_f32 v138,v140,v141
v_mfma_f32_16x16x32_bf16 v[204:207],a[120:123],a[240:243],v[204:207]
ds_read_b64_tr_b16 a[168:169],v50 offset:1792
v_cvt_pk_bf16_f32 v139,v142,v143
v_mfma_f32_16x16x32_bf16 v[204:207],a[124:127],a[244:247],v[204:207]
ds_read_b64_tr_b16 a[170:171],v50 offset:3840
v_mul_f32 v164,s71,v164
v_mfma_f32_16x16x32_bf16 v[236:239],a[124:127],a[252:255],v[236:239]
v_mul_f32 v165,s71,v165
v_mul_f32 v166,s71,v166
v_mfma_f32_16x16x32_bf16 v[236:239],a[120:123],a[248:251],v[236:239]
ds_read_b64_tr_b16 a[172:173],v50 offset:18176
v_mul_f32 v167,s71,v167
v_mfma_f32_16x16x32_bf16 v[236:239],a[116:119],a[220:223],v[236:239]
ds_read_b64_tr_b16 a[174:175],v50 offset:20224
v_cvt_pk_bf16_f32 v160,v160,v161
v_mfma_f32_16x16x32_bf16 v[236:239],a[112:115],a[216:219],v[236:239]
ds_read_b64_tr_b16 a[176:177],v50 offset:776
v_cvt_pk_bf16_f32 v161,v162,v163
v_mfma_f32_16x16x32_bf16 v[144:147],a[128:131],a[192:195],v[144:147]
v_cvt_pk_bf16_f32 v162,v164,v165
v_cvt_pk_bf16_f32 v163,v166,v167
v_mfma_f32_16x16x32_bf16 v[144:147],a[132:135],a[196:199],v[144:147]
ds_read_b64_tr_b16 a[178:179],v50 offset:2824
v_mul_f32 v224,s71,v224
v_mfma_f32_16x16x32_bf16 v[144:147],a[136:139],a[224:227],v[144:147]
global_load_lds_dwordx4 v4,s[10:11] offset:2048
ds_read_b64_tr_b16 a[180:181],v50 offset:17160
v_mfma_f32_16x16x32_bf16 v[144:147],a[140:143],a[228:231],v[144:147]
ds_read_b64_tr_b16 a[182:183],v50 offset:19208
v_mul_f32 v225,s71,v225
v_mfma_f32_16x16x32_bf16 v[176:179],a[140:143],a[236:239],v[176:179]
v_mul_f32 v226,s71,v226
v_mul_f32 v227,s71,v227
v_mfma_f32_16x16x32_bf16 v[176:179],a[136:139],a[232:235],v[176:179]
ds_read_b64_tr_b16 a[184:185],v50 offset:1800
v_mul_f32 v228,s71,v228
v_mfma_f32_16x16x32_bf16 v[176:179],a[132:135],a[204:207],v[176:179]
ds_read_b64_tr_b16 a[186:187],v50 offset:3848
v_mul_f32 v144,s71,v144
v_mfma_f32_16x16x32_bf16 v[176:179],a[128:131],a[200:203],v[176:179]
ds_read_b64_tr_b16 a[188:189],v50 offset:18184
v_mul_f32 v145,s71,v145
v_mfma_f32_16x16x32_bf16 v[208:211],a[128:131],a[208:211],v[208:211]
v_mul_f32 v146,s71,v146
v_mul_f32 v147,s71,v147
v_mfma_f32_16x16x32_bf16 v[208:211],a[132:135],a[212:215],v[208:211]
ds_read_b64_tr_b16 a[190:191],v50 offset:20232
v_cvt_pk_bf16_f32 v144,v144,v145
v_mfma_f32_16x16x32_bf16 v[208:211],a[136:139],a[240:243],v[208:211]
v_cvt_pk_bf16_f32 v145,v146,v147
v_mul_f32 v229,s71,v229
v_mfma_f32_16x16x32_bf16 v[208:211],a[140:143],a[244:247],v[208:211]
v_mul_f32 v230,s71,v230
v_mul_f32 v231,s71,v231
v_mfma_f32_16x16x32_bf16 v[240:243],a[140:143],a[252:255],v[240:243]
v_cvt_pk_bf16_f32 v224,v224,v225
v_cvt_pk_bf16_f32 v225,v226,v227
v_mfma_f32_16x16x32_bf16 v[240:243],a[136:139],a[248:251],v[240:243]
v_cvt_pk_bf16_f32 v226,v228,v229
v_cvt_pk_bf16_f32 v227,v230,v231
v_mfma_f32_16x16x32_bf16 v[240:243],a[132:135],a[220:223],v[240:243]
v_mul_f32 v168,s71,v168
v_mul_f32 v169,s71,v169
v_mfma_f32_16x16x32_bf16 v[240:243],a[128:131],a[216:219],v[240:243]
v_mul_f32 v170,s71,v170
v_mul_f32 v171,s71,v171
s_waitcnt vmcnt(7)
s_barrier
s_waitcnt lgkmcnt(1)
v_mfma_f32_16x16x32_bf16 v[148:151],a[144:147],a[192:195],v[148:151]
ds_read_b64_tr_b16 a[64:65],v52
v_mul_f32 v172,s71,v172
v_mfma_f32_16x16x32_bf16 v[148:151],a[148:151],a[196:199],v[148:151]
s_nop 0
L_pd3_%=:
s_cbranch_execz L_pd7_%=
global_load_lds_dwordx4 v5,s[10:11] offset:3072
ds_read_b64_tr_b16 a[66:67],v52 offset:2048
v_mfma_f32_16x16x32_bf16 v[148:151],a[152:155],a[224:227],v[148:151]
ds_read_b64_tr_b16 a[68:69],v52 offset:16384
v_mul_f32 v173,s71,v173
v_mfma_f32_16x16x32_bf16 v[148:151],a[156:159],a[228:231],v[148:151]
ds_read_b64_tr_b16 a[70:71],v52 offset:18432
v_mul_f32 v174,s71,v174
v_mfma_f32_16x16x32_bf16 v[180:183],a[156:159],a[236:239],v[180:183]
v_mul_f32 v175,s71,v175
v_cvt_pk_bf16_f32 v168,v168,v169
v_mfma_f32_16x16x32_bf16 v[180:183],a[152:155],a[232:235],v[180:183]
ds_read_b64_tr_b16 a[72:73],v52 offset:1024
v_cvt_pk_bf16_f32 v169,v170,v171
v_mfma_f32_16x16x32_bf16 v[180:183],a[148:151],a[204:207],v[180:183]
ds_read_b64_tr_b16 a[74:75],v52 offset:3072
v_mul_f32 v148,s71,v148
v_mfma_f32_16x16x32_bf16 v[180:183],a[144:147],a[200:203],v[180:183]
ds_read_b64_tr_b16 a[76:77],v52 offset:17408
v_mul_f32 v149,s71,v149
v_mfma_f32_16x16x32_bf16 v[212:215],a[144:147],a[208:211],v[212:215]
v_mul_f32 v150,s71,v150
v_mul_f32 v151,s71,v151
v_mfma_f32_16x16x32_bf16 v[212:215],a[148:151],a[212:215],v[212:215]
ds_read_b64_tr_b16 a[78:79],v52 offset:19456
v_cvt_pk_bf16_f32 v146,v148,v149
v_mfma_f32_16x16x32_bf16 v[212:215],a[152:155],a[240:243],v[212:215]
ds_read_b64_tr_b16 a[80:81],v52 offset:8
v_cvt_pk_bf16_f32 v147,v150,v151
v_mfma_f32_16x16x32_bf16 v[212:215],a[156:159],a[244:247],v[212:215]
ds_read_b64_tr_b16 a[82:83],v52 offset:2056
v_cvt_pk_bf16_f32 v170,v172,v173
v_mfma_f32_16x16x32_bf16 v[244:247],a[156:159],a[252:255],v[244:247]
v_cvt_pk_bf16_f32 v171,v174,v175
v_mul_f32 v176,s71,v176
v_mfma_f32_16x16x32_bf16 v[244:247],a[152:155],a[248:251],v[244:247]
ds_read_b64_tr_b16 a[84:85],v52 offset:16392
v_mul_f32 v177,s71,v177
v_mfma_f32_16x16x32_bf16 v[244:247],a[148:151],a[220:223],v[244:247]
s_add_u32 s10,s20,s74
s_addc_u32 s11,s21,0
ds_read_b64_tr_b16 a[86:87],v52 offset:18440
v_mfma_f32_16x16x32_bf16 v[244:247],a[144:147],a[216:219],v[244:247]
ds_read_b64_tr_b16 a[88:89],v52 offset:1032
v_mul_f32 v178,s71,v178
v_mfma_f32_16x16x32_bf16 v[152:155],a[160:163],a[192:195],v[152:155]
s_add_u32 m0,s76,0
v_mul_f32 v179,s71,v179
v_mul_f32 v180,s71,v180
v_mfma_f32_16x16x32_bf16 v[152:155],a[164:167],a[196:199],v[152:155]
ds_read_b64_tr_b16 a[90:91],v52 offset:3080
v_mul_f32 v181,s71,v181
v_mfma_f32_16x16x32_bf16 v[152:155],a[168:171],a[224:227],v[152:155]
global_load_lds_dwordx4 v4,s[10:11] nt
ds_read_b64_tr_b16 a[92:93],v52 offset:17416
v_mfma_f32_16x16x32_bf16 v[152:155],a[172:175],a[228:231],v[152:155]
ds_read_b64_tr_b16 a[94:95],v52 offset:19464
v_mul_f32 v182,s71,v182
v_mfma_f32_16x16x32_bf16 v[184:187],a[172:175],a[236:239],v[184:187]
v_mul_f32 v183,s71,v183
v_cvt_pk_bf16_f32 v176,v176,v177
v_mfma_f32_16x16x32_bf16 v[184:187],a[168:171],a[232:235],v[184:187]
ds_read_b64_tr_b16 a[96:97],v52 offset:256
v_cvt_pk_bf16_f32 v177,v178,v179
v_mfma_f32_16x16x32_bf16 v[184:187],a[164:167],a[204:207],v[184:187]
ds_read_b64_tr_b16 a[98:99],v52 offset:2304
v_mul_f32 v152,s71,v152
v_mfma_f32_16x16x32_bf16 v[184:187],a[160:163],a[200:203],v[184:187]
ds_read_b64_tr_b16 a[100:101],v52 offset:16640
v_mul_f32 v153,s71,v153
v_mfma_f32_16x16x32_bf16 v[216:219],a[160:163],a[208:211],v[216:219]
v_mul_f32 v154,s71,v154
v_mul_f32 v155,s71,v155
v_mfma_f32_16x16x32_bf16 v[216:219],a[164:167],a[212:215],v[216:219]
ds_read_b64_tr_b16 a[102:103],v52 offset:18688
v_cvt_pk_bf16_f32 v152,v152,v153
v_mfma_f32_16x16x32_bf16 v[216:219],a[168:171],a[240:243],v[216:219]
ds_read_b64_tr_b16 a[104:105],v52 offset:1280
v_cvt_pk_bf16_f32 v153,v154,v155
v_mfma_f32_16x16x32_bf16 v[216:219],a[172:175],a[244:247],v[216:219]
ds_read_b64_tr_b16 a[106:107],v52 offset:3328
v_cvt_pk_bf16_f32 v178,v180,v181
v_mfma_f32_16x16x32_bf16 v[248:251],a[172:175],a[252:255],v[248:251]
v_cvt_pk_bf16_f32 v179,v182,v183
v_mul_f32 v184,s71,v184
v_mfma_f32_16x16x32_bf16 v[248:251],a[168:171],a[248:251],v[248:251]
ds_read_b64_tr_b16 a[108:109],v52 offset:17664
v_mul_f32 v185,s71,v185
v_mfma_f32_16x16x32_bf16 v[248:251],a[164:167],a[220:223],v[248:251]
ds_read_b64_tr_b16 a[110:111],v52 offset:19712
v_mul_f32 v186,s71,v186
v_mfma_f32_16x16x32_bf16 v[248:251],a[160:163],a[216:219],v[248:251]
ds_read_b64_tr_b16 a[112:113],v52 offset:264
v_mul_f32 v187,s71,v187
v_mfma_f32_16x16x32_bf16 v[156:159],a[176:179],a[192:195],v[156:159]
v_cvt_pk_bf16_f32 v184,v184,v185
v_cvt_pk_bf16_f32 v185,v186,v187
v_mfma_f32_16x16x32_bf16 v[156:159],a[180:183],a[196:199],v[156:159]
global_load_lds_dwordx4 v4,s[10:11] offset:1024 nt
ds_read_b64_tr_b16 a[114:115],v52 offset:2312
v_mfma_f32_16x16x32_bf16 v[156:159],a[184:187],a[224:227],v[156:159]
s_waitcnt lgkmcnt(5)
ds_read_b64_tr_b16 a[116:117],v52 offset:16648
v_mul_f32 v200,s71,v200
v_mfma_f32_16x16x32_bf16 v[156:159],a[188:191],a[228:231],v[156:159]
ds_read_b64_tr_b16 a[118:119],v52 offset:18696
v_mul_f32 v201,s71,v201
v_mfma_f32_16x16x32_bf16 v[188:191],a[188:191],a[236:239],v[188:191]
v_mul_f32 v202,s71,v202
v_mul_f32 v203,s71,v203
v_mfma_f32_16x16x32_bf16 v[188:191],a[184:187],a[232:235],v[188:191]
ds_read_b64_tr_b16 a[120:121],v52 offset:1288
v_mul_f32 v204,s71,v204
v_mfma_f32_16x16x32_bf16 v[188:191],a[180:183],a[204:207],v[188:191]
ds_read_b64_tr_b16 a[122:123],v52 offset:3336
v_mul_f32 v156,s71,v156
v_mfma_f32_16x16x32_bf16 v[188:191],a[176:179],a[200:203],v[188:191]
ds_read_b64_tr_b16 a[124:125],v52 offset:17672
v_mul_f32 v157,s71,v157
v_mfma_f32_16x16x32_bf16 v[220:223],a[176:179],a[208:211],v[220:223]
v_mul_f32 v158,s71,v158
v_mul_f32 v159,s71,v159
v_mfma_f32_16x16x32_bf16 v[220:223],a[180:183],a[212:215],v[220:223]
ds_read_b64_tr_b16 a[126:127],v52 offset:19720
v_cvt_pk_bf16_f32 v154,v156,v157
v_mfma_f32_16x16x32_bf16 v[220:223],a[184:187],a[240:243],v[220:223]
v_cvt_pk_bf16_f32 v155,v158,v159
v_mul_f32 v188,s71,v188
v_mfma_f32_16x16x32_bf16 v[220:223],a[188:191],a[244:247],v[220:223]
v_mul_f32 v189,s71,v189
v_mul_f32 v190,s71,v190
v_mfma_f32_16x16x32_bf16 v[252:255],a[188:191],a[252:255],v[252:255]
v_mul_f32 v191,s71,v191
v_cvt_pk_bf16_f32 v186,v188,v189
v_mfma_f32_16x16x32_bf16 v[252:255],a[184:187],a[248:251],v[252:255]
v_cvt_pk_bf16_f32 v187,v190,v191
v_mul_f32 v205,s71,v205
v_mfma_f32_16x16x32_bf16 v[252:255],a[180:183],a[220:223],v[252:255]
v_mul_f32 v206,s71,v206
v_mul_f32 v207,s71,v207
v_mfma_f32_16x16x32_bf16 v[252:255],a[176:179],a[216:219],v[252:255]
v_cvt_pk_bf16_f32 v200,v200,v201
v_cvt_pk_bf16_f32 v201,v202,v203
v_mfma_f32_16x16x32_bf16 a[0:3],a[64:67],a[192:195],a[0:3]
global_load_lds_dwordx4 v5,s[10:11] offset:2048 nt
v_cvt_pk_bf16_f32 v202,v204,v205
v_mfma_f32_16x16x32_bf16 a[0:3],a[68:71],a[196:199],a[0:3]
v_cvt_pk_bf16_f32 v203,v206,v207
v_mul_f32 v208,s71,v208
v_mfma_f32_16x16x32_bf16 a[0:3],a[72:75],a[224:227],a[0:3]
v_mul_f32 v209,s71,v209
v_mul_f32 v210,s71,v210
v_mfma_f32_16x16x32_bf16 a[0:3],a[76:79],a[228:231],a[0:3]
v_mul_f32 v211,s71,v211
v_mul_f32 v212,s71,v212
v_mfma_f32_16x16x32_bf16 a[16:19],a[76:79],a[236:239],a[16:19]
v_mul_f32 v213,s71,v213
v_mul_f32 v214,s71,v214
v_mfma_f32_16x16x32_bf16 a[16:19],a[72:75],a[232:235],a[16:19]
v_mul_f32 v215,s71,v215
v_cvt_pk_bf16_f32 v208,v208,v209
v_mfma_f32_16x16x32_bf16 a[16:19],a[68:71],a[204:207],a[16:19]
v_accvgpr_read_b32 v8,a0
v_accvgpr_read_b32 v9,a1
v_mfma_f32_16x16x32_bf16 a[16:19],a[64:67],a[200:203],a[16:19]
v_accvgpr_read_b32 v10,a2
v_accvgpr_read_b32 v11,a3
v_mfma_f32_16x16x32_bf16 a[32:35],a[64:67],a[208:211],a[32:35]
v_mul_f32 v8,s71,v8
v_mul_f32 v9,s71,v9
v_mfma_f32_16x16x32_bf16 a[32:35],a[68:71],a[212:215],a[32:35]
v_mul_f32 v10,s71,v10
v_mul_f32 v11,s71,v11
v_mfma_f32_16x16x32_bf16 a[32:35],a[72:75],a[240:243],a[32:35]
v_cvt_pk_bf16_f32 v132,v8,v9
v_cvt_pk_bf16_f32 v133,v10,v11
v_mfma_f32_16x16x32_bf16 a[32:35],a[76:79],a[244:247],a[32:35]
v_accvgpr_read_b32 v16,a16
v_accvgpr_read_b32 v17,a17
v_mfma_f32_16x16x32_bf16 a[48:51],a[76:79],a[252:255],a[48:51]
ds_read_b64_tr_b16 a[76:77],v52 offset:17920
v_accvgpr_read_b32 v18,a18
v_mfma_f32_16x16x32_bf16 a[48:51],a[72:75],a[248:251],a[48:51]
ds_read_b64_tr_b16 a[72:73],v52 offset:1536
v_accvgpr_read_b32 v19,a19
v_mfma_f32_16x16x32_bf16 a[48:51],a[68:71],a[220:223],a[48:51]
ds_read_b64_tr_b16 a[68:69],v52 offset:16896
v_accvgpr_read_b32 v8,a32
v_mfma_f32_16x16x32_bf16 a[48:51],a[64:67],a[216:219],a[48:51]
global_load_lds_dwordx4 v5,s[10:11] offset:3072
ds_read_b64_tr_b16 a[64:65],v52 offset:512
v_mfma_f32_16x16x32_bf16 a[4:7],a[80:83],a[192:195],a[4:7]
v_accvgpr_read_b32 v9,a33
v_accvgpr_read_b32 v10,a34
v_mfma_f32_16x16x32_bf16 a[4:7],a[84:87],a[196:199],a[4:7]
ds_read_b64_tr_b16 a[66:67],v52 offset:2560
v_accvgpr_read_b32 v11,a35
)A"
R"A(v_mfma_f32_16x16x32_bf16 a[4:7],a[88:91],a[224:227],a[4:7]
ds_read_b64_tr_b16 a[70:71],v52 offset:18944
v_mul_f32 v8,s71,v8
v_mfma_f32_16x16x32_bf16 a[4:7],a[92:95],a[228:231],a[4:7]
ds_read_b64_tr_b16 a[74:75],v52 offset:3584
v_mul_f32 v9,s71,v9
v_mfma_f32_16x16x32_bf16 a[20:23],a[92:95],a[236:239],a[20:23]
v_mul_f32 v10,s71,v10
v_mul_f32 v11,s71,v11
v_mfma_f32_16x16x32_bf16 a[20:23],a[88:91],a[232:235],a[20:23]
ds_read_b64_tr_b16 a[78:79],v52 offset:19968
v_cvt_pk_bf16_f32 v196,v8,v9
v_mfma_f32_16x16x32_bf16 a[20:23],a[84:87],a[204:207],a[20:23]
v_accvgpr_read_b32 v12,a4
v_accvgpr_read_b32 v13,a5
v_mfma_f32_16x16x32_bf16 a[20:23],a[80:83],a[200:203],a[20:23]
v_accvgpr_read_b32 v14,a6
v_accvgpr_read_b32 v15,a7
v_mfma_f32_16x16x32_bf16 a[36:39],a[80:83],a[208:211],a[36:39]
v_mul_f32 v12,s71,v12
v_mul_f32 v13,s71,v13
v_mfma_f32_16x16x32_bf16 a[36:39],a[84:87],a[212:215],a[36:39]
v_mul_f32 v14,s71,v14
v_mul_f32 v15,s71,v15
v_mfma_f32_16x16x32_bf16 a[36:39],a[88:91],a[240:243],a[36:39]
s_add_u32 s10,s24,s74
s_addc_u32 s11,s25,0
s_add_u32 s74,s74,16384
v_mfma_f32_16x16x32_bf16 a[36:39],a[92:95],a[244:247],a[36:39]
s_and_b32 s74,s74,0x3ffff
s_or_b32 s74,s74,s69
v_cvt_pk_bf16_f32 v134,v12,v13
v_mfma_f32_16x16x32_bf16 a[52:55],a[92:95],a[252:255],a[52:55]
s_add_u32 m0,s76,16384
s_sub_u32 s76,s76,32768
ds_read_b64_tr_b16 a[92:93],v52 offset:17928
v_mfma_f32_16x16x32_bf16 a[52:55],a[88:91],a[248:251],a[52:55]
s_and_b32 s76,s76,0x1ffff
ds_read_b64_tr_b16 a[88:89],v52 offset:1544
v_cvt_pk_bf16_f32 v135,v14,v15
v_mfma_f32_16x16x32_bf16 a[52:55],a[84:87],a[220:223],a[52:55]
global_load_lds_dwordx4 v4,s[10:11]
ds_read_b64_tr_b16 a[84:85],v52 offset:16904
v_mfma_f32_16x16x32_bf16 a[52:55],a[80:83],a[216:219],a[52:55]
s_and_b32 s69,s75,0xfffc0000
ds_read_b64_tr_b16 a[80:81],v52 offset:520
v_accvgpr_read_b32 v12,a36
v_mfma_f32_16x16x32_bf16 a[8:11],a[96:99],a[192:195],a[8:11]
s_add_u32 s75,s75,16384
v_accvgpr_read_b32 v13,a37
v_accvgpr_read_b32 v14,a38
v_mfma_f32_16x16x32_bf16 a[8:11],a[100:103],a[196:199],a[8:11]
s_waitcnt lgkmcnt(4)
ds_read_b64_tr_b16 a[82:83],v52 offset:2568
v_accvgpr_read_b32 v15,a39
v_mfma_f32_16x16x32_bf16 a[8:11],a[104:107],a[224:227],a[8:11]
ds_read_b64_tr_b16 a[86:87],v52 offset:18952
v_mul_f32 v12,s71,v12
v_mfma_f32_16x16x32_bf16 a[8:11],a[108:111],a[228:231],a[8:11]
ds_read_b64_tr_b16 a[90:91],v52 offset:3592
v_mul_f32 v13,s71,v13
v_mfma_f32_16x16x32_bf16 a[24:27],a[108:111],a[236:239],a[24:27]
v_mul_f32 v14,s71,v14
v_mul_f32 v15,s71,v15
v_mfma_f32_16x16x32_bf16 a[24:27],a[104:107],a[232:235],a[24:27]
ds_read_b64_tr_b16 a[94:95],v52 offset:19976
v_cvt_pk_bf16_f32 v197,v10,v11
v_mfma_f32_16x16x32_bf16 a[24:27],a[100:103],a[204:207],a[24:27]
v_cvt_pk_bf16_f32 v198,v12,v13
v_cvt_pk_bf16_f32 v199,v14,v15
v_mfma_f32_16x16x32_bf16 a[24:27],a[96:99],a[200:203],a[24:27]
v_accvgpr_read_b32 v8,a8
v_accvgpr_read_b32 v9,a9
v_mfma_f32_16x16x32_bf16 a[40:43],a[96:99],a[208:211],a[40:43]
v_accvgpr_read_b32 v10,a10
v_accvgpr_read_b32 v11,a11
v_mfma_f32_16x16x32_bf16 a[40:43],a[100:103],a[212:215],a[40:43]
v_mul_f32 v8,s71,v8
v_mul_f32 v9,s71,v9
v_mfma_f32_16x16x32_bf16 a[40:43],a[104:107],a[240:243],a[40:43]
v_mul_f32 v10,s71,v10
v_mul_f32 v11,s71,v11
v_mfma_f32_16x16x32_bf16 a[40:43],a[108:111],a[244:247],a[40:43]
v_cvt_pk_bf16_f32 v140,v8,v9
v_cvt_pk_bf16_f32 v141,v10,v11
v_mfma_f32_16x16x32_bf16 a[56:59],a[108:111],a[252:255],a[56:59]
ds_read_b64_tr_b16 a[108:109],v52 offset:18176
v_accvgpr_read_b32 v20,a20
v_mfma_f32_16x16x32_bf16 a[56:59],a[104:107],a[248:251],a[56:59]
global_load_lds_dwordx4 v5,s[10:11] offset:1024
ds_read_b64_tr_b16 a[104:105],v52 offset:1792
v_mfma_f32_16x16x32_bf16 a[56:59],a[100:103],a[220:223],a[56:59]
ds_read_b64_tr_b16 a[100:101],v52 offset:17152
v_accvgpr_read_b32 v21,a21
v_mfma_f32_16x16x32_bf16 a[56:59],a[96:99],a[216:219],a[56:59]
ds_read_b64_tr_b16 a[96:97],v52 offset:768
v_accvgpr_read_b32 v22,a22
v_mfma_f32_16x16x32_bf16 a[12:15],a[112:115],a[192:195],a[12:15]
v_accvgpr_read_b32 v23,a23
v_mul_f32 v16,s71,v16
v_mfma_f32_16x16x32_bf16 a[12:15],a[116:119],a[196:199],a[12:15]
ds_read_b64_tr_b16 a[98:99],v52 offset:2816
v_mul_f32 v17,s71,v17
v_mfma_f32_16x16x32_bf16 a[12:15],a[120:123],a[224:227],a[12:15]
ds_read_b64_tr_b16 a[102:103],v52 offset:19200
v_mul_f32 v18,s71,v18
v_mfma_f32_16x16x32_bf16 a[12:15],a[124:127],a[228:231],a[12:15]
ds_read_b64_tr_b16 a[106:107],v52 offset:3840
v_mul_f32 v19,s71,v19
v_mfma_f32_16x16x32_bf16 a[28:31],a[124:127],a[236:239],a[28:31]
v_mul_f32 v20,s71,v20
v_mul_f32 v21,s71,v21
v_mfma_f32_16x16x32_bf16 a[28:31],a[120:123],a[232:235],a[28:31]
ds_read_b64_tr_b16 a[110:111],v52 offset:20224
v_mul_f32 v22,s71,v22
v_mfma_f32_16x16x32_bf16 a[28:31],a[116:119],a[204:207],a[28:31]
v_accvgpr_read_b32 v12,a12
v_accvgpr_read_b32 v13,a13
v_mfma_f32_16x16x32_bf16 a[28:31],a[112:115],a[200:203],a[28:31]
v_accvgpr_read_b32 v14,a14
v_accvgpr_read_b32 v15,a15
v_mfma_f32_16x16x32_bf16 a[44:47],a[112:115],a[208:211],a[44:47]
v_mul_f32 v12,s71,v12
v_mul_f32 v13,s71,v13
v_mfma_f32_16x16x32_bf16 a[44:47],a[116:119],a[212:215],a[44:47]
v_mul_f32 v14,s71,v14
v_mul_f32 v15,s71,v15
v_mfma_f32_16x16x32_bf16 a[44:47],a[120:123],a[240:243],a[44:47]
v_cvt_pk_bf16_f32 v142,v12,v13
v_cvt_pk_bf16_f32 v143,v14,v15
v_mfma_f32_16x16x32_bf16 a[44:47],a[124:127],a[244:247],a[44:47]
v_mul_f32 v23,s71,v23
v_cvt_pk_bf16_f32 v164,v16,v17
v_mfma_f32_16x16x32_bf16 a[60:63],a[124:127],a[252:255],a[60:63]
global_load_lds_dwordx4 v4,s[10:11] offset:2048
ds_read_b64_tr_b16 a[124:125],v52 offset:18184
v_mfma_f32_16x16x32_bf16 a[60:63],a[120:123],a[248:251],a[60:63]
ds_read_b64_tr_b16 a[120:121],v52 offset:1800
v_cvt_pk_bf16_f32 v165,v18,v19
v_mfma_f32_16x16x32_bf16 a[60:63],a[116:119],a[220:223],a[60:63]
ds_read_b64_tr_b16 a[116:117],v52 offset:17160
v_cvt_pk_bf16_f32 v166,v20,v21
v_mfma_f32_16x16x32_bf16 a[60:63],a[112:115],a[216:219],a[60:63]
ds_read_b64_tr_b16 a[112:113],v52 offset:776
v_cvt_pk_bf16_f32 v167,v22,v23
v_mfma_f32_16x16x32_bf16 v[64:67],a[64:67],a[192:195],v[64:67]
v_accvgpr_read_b32 v16,a48
v_accvgpr_read_b32 v17,a49
v_mfma_f32_16x16x32_bf16 v[64:67],a[68:71],a[196:199],v[64:67]
ds_read_b64_tr_b16 a[114:115],v52 offset:2824
v_accvgpr_read_b32 v18,a50
v_mfma_f32_16x16x32_bf16 v[64:67],a[72:75],a[224:227],v[64:67]
ds_read_b64_tr_b16 a[118:119],v52 offset:19208
v_accvgpr_read_b32 v19,a51
v_mfma_f32_16x16x32_bf16 v[64:67],a[76:79],a[228:231],v[64:67]
ds_read_b64_tr_b16 a[122:123],v52 offset:3848
v_accvgpr_read_b32 v20,a52
v_mfma_f32_16x16x32_bf16 v[80:83],a[76:79],a[236:239],v[80:83]
v_accvgpr_read_b32 v21,a53
v_accvgpr_read_b32 v22,a54
v_mfma_f32_16x16x32_bf16 v[80:83],a[72:75],a[232:235],v[80:83]
ds_read_b64_tr_b16 a[126:127],v52 offset:20232
v_accvgpr_read_b32 v23,a55
v_mfma_f32_16x16x32_bf16 v[80:83],a[68:71],a[204:207],v[80:83]
v_mul_f32 v64,s71,v64
v_mul_f32 v65,s71,v65
v_mfma_f32_16x16x32_bf16 v[80:83],a[64:67],a[200:203],v[80:83]
v_mul_f32 v66,s71,v66
v_mul_f32 v67,s71,v67
v_mfma_f32_16x16x32_bf16 v[96:99],a[64:67],a[208:211],v[96:99]
v_cvt_pk_bf16_f32 v148,v64,v65
v_cvt_pk_bf16_f32 v149,v66,v67
v_mfma_f32_16x16x32_bf16 v[96:99],a[68:71],a[212:215],v[96:99]
v_mul_f32 v16,s71,v16
v_mul_f32 v17,s71,v17
v_mfma_f32_16x16x32_bf16 v[96:99],a[72:75],a[240:243],v[96:99]
v_mul_f32 v18,s71,v18
v_mul_f32 v19,s71,v19
v_mfma_f32_16x16x32_bf16 v[96:99],a[76:79],a[244:247],v[96:99]
global_load_lds_dwordx4 v5,s[10:11] offset:3072
v_mul_f32 v20,s71,v20
v_mfma_f32_16x16x32_bf16 v[112:115],a[76:79],a[252:255],v[112:115]
v_mul_f32 v21,s71,v21
v_mul_f32 v22,s71,v22
v_mfma_f32_16x16x32_bf16 v[112:115],a[72:75],a[248:251],v[112:115]
v_mul_f32 v23,s71,v23
v_cvt_pk_bf16_f32 v228,v16,v17
v_mfma_f32_16x16x32_bf16 v[112:115],a[68:71],a[220:223],v[112:115]
v_cvt_pk_bf16_f32 v229,v18,v19
v_cvt_pk_bf16_f32 v230,v20,v21
v_mfma_f32_16x16x32_bf16 v[112:115],a[64:67],a[216:219],v[112:115]
v_cvt_pk_bf16_f32 v231,v22,v23
v_accvgpr_read_b32 v16,a24
s_waitcnt vmcnt(8) lgkmcnt(0)
s_barrier
v_mfma_f32_16x16x32_bf16 v[68:71],a[80:83],a[192:195],v[68:71]
ds_read_b128 a[128:131],v1
v_accvgpr_read_b32 v17,a25
v_mfma_f32_16x16x32_bf16 v[68:71],a[84:87],a[196:199],v[68:71]
ds_read_b128 a[132:135],v1 offset:256
v_accvgpr_read_b32 v18,a26
v_mfma_f32_16x16x32_bf16 v[68:71],a[88:91],a[224:227],v[68:71]
ds_read_b128 a[136:139],v1 offset:512
v_accvgpr_read_b32 v19,a27
v_mfma_f32_16x16x32_bf16 v[68:71],a[92:95],a[228:231],v[68:71]
ds_read_b128 a[140:143],v1 offset:768
v_accvgpr_read_b32 v20,a28
v_mfma_f32_16x16x32_bf16 v[84:87],a[92:95],a[236:239],v[84:87]
v_accvgpr_read_b32 v21,a29
v_accvgpr_read_b32 v22,a30
v_mfma_f32_16x16x32_bf16 v[84:87],a[88:91],a[232:235],v[84:87]
v_accvgpr_read_b32 v23,a31
v_mul_f32 v16,s71,v16
v_mfma_f32_16x16x32_bf16 v[84:87],a[84:87],a[204:207],v[84:87]
s_add_u32 s10,s20,s74
s_addc_u32 s11,s21,0
v_mul_f32 v68,s71,v68
v_mfma_f32_16x16x32_bf16 v[84:87],a[80:83],a[200:203],v[84:87]
v_mul_f32 v69,s71,v69
v_mul_f32 v70,s71,v70
v_mfma_f32_16x16x32_bf16 v[100:103],a[80:83],a[208:211],v[100:103]
s_add_u32 m0,s76,0
v_mul_f32 v71,s71,v71
v_cvt_pk_bf16_f32 v150,v68,v69
v_mfma_f32_16x16x32_bf16 v[100:103],a[84:87],a[212:215],v[100:103]
v_cvt_pk_bf16_f32 v151,v70,v71
v_mul_f32 v17,s71,v17
v_mfma_f32_16x16x32_bf16 v[100:103],a[88:91],a[240:243],v[100:103]
global_load_lds_dwordx4 v4,s[10:11] nt
v_mul_f32 v18,s71,v18
v_mfma_f32_16x16x32_bf16 v[100:103],a[92:95],a[244:247],v[100:103]
v_mul_f32 v19,s71,v19
v_mul_f32 v20,s71,v20
v_mfma_f32_16x16x32_bf16 v[116:119],a[92:95],a[252:255],v[116:119]
v_mul_f32 v21,s71,v21
v_mul_f32 v22,s71,v22
v_mfma_f32_16x16x32_bf16 v[116:119],a[88:91],a[248:251],v[116:119]
v_mul_f32 v23,s71,v23
v_cvt_pk_bf16_f32 v172,v16,v17
v_mfma_f32_16x16x32_bf16 v[116:119],a[84:87],a[220:223],v[116:119]
v_cvt_pk_bf16_f32 v173,v18,v19
v_cvt_pk_bf16_f32 v174,v20,v21
v_mfma_f32_16x16x32_bf16 v[116:119],a[80:83],a[216:219],v[116:119]
v_cvt_pk_bf16_f32 v175,v22,v23
v_mul_f32 v80,s71,v80
v_mfma_f32_16x16x32_bf16 v[72:75],a[96:99],a[192:195],v[72:75]
v_mul_f32 v81,s71,v81
v_mul_f32 v82,s71,v82
v_mfma_f32_16x16x32_bf16 v[72:75],a[100:103],a[196:199],v[72:75]
v_mul_f32 v83,s71,v83
v_mul_f32 v84,s71,v84
v_mfma_f32_16x16x32_bf16 v[72:75],a[104:107],a[224:227],v[72:75]
v_mul_f32 v85,s71,v85
v_mul_f32 v86,s71,v86
v_mfma_f32_16x16x32_bf16 v[72:75],a[108:111],a[228:231],v[72:75]
v_mul_f32 v87,s71,v87
v_cvt_pk_bf16_f32 v180,v80,v81
v_mfma_f32_16x16x32_bf16 v[88:91],a[108:111],a[236:239],v[88:91]
v_cvt_pk_bf16_f32 v181,v82,v83
v_cvt_pk_bf16_f32 v182,v84,v85
v_mfma_f32_16x16x32_bf16 v[88:91],a[104:107],a[232:235],v[88:91]
v_cvt_pk_bf16_f32 v183,v86,v87
v_accvgpr_read_b32 v8,a40
v_mfma_f32_16x16x32_bf16 v[88:91],a[100:103],a[204:207],v[88:91]
v_mul_f32 v72,s71,v72
v_mul_f32 v73,s71,v73
v_mfma_f32_16x16x32_bf16 v[88:91],a[96:99],a[200:203],v[88:91]
v_mul_f32 v74,s71,v74
v_mul_f32 v75,s71,v75
v_mfma_f32_16x16x32_bf16 v[104:107],a[96:99],a[208:211],v[104:107]
global_load_lds_dwordx4 v4,s[10:11] offset:1024 nt
v_cvt_pk_bf16_f32 v156,v72,v73
v_mfma_f32_16x16x32_bf16 v[104:107],a[100:103],a[212:215],v[104:107]
v_cvt_pk_bf16_f32 v157,v74,v75
v_accvgpr_read_b32 v9,a41
v_mfma_f32_16x16x32_bf16 v[104:107],a[104:107],a[240:243],v[104:107]
v_mul_f32 v88,s71,v88
v_mul_f32 v89,s71,v89
v_mfma_f32_16x16x32_bf16 v[104:107],a[108:111],a[244:247],v[104:107]
v_mul_f32 v90,s71,v90
v_mul_f32 v91,s71,v91
v_mfma_f32_16x16x32_bf16 v[120:123],a[108:111],a[252:255],v[120:123]
v_cvt_pk_bf16_f32 v188,v88,v89
v_cvt_pk_bf16_f32 v189,v90,v91
v_mfma_f32_16x16x32_bf16 v[120:123],a[104:107],a[248:251],v[120:123]
v_accvgpr_read_b32 v10,a42
v_accvgpr_read_b32 v11,a43
v_mfma_f32_16x16x32_bf16 v[120:123],a[100:103],a[220:223],v[120:123]
v_accvgpr_read_b32 v12,a44
v_accvgpr_read_b32 v13,a45
v_mfma_f32_16x16x32_bf16 v[120:123],a[96:99],a[216:219],v[120:123]
v_accvgpr_read_b32 v14,a46
v_accvgpr_read_b32 v15,a47
v_mfma_f32_16x16x32_bf16 v[76:79],a[112:115],a[192:195],v[76:79]
v_mul_f32 v8,s71,v8
v_mul_f32 v9,s71,v9
v_mfma_f32_16x16x32_bf16 v[76:79],a[116:119],a[196:199],v[76:79]
v_mul_f32 v10,s71,v10
v_mul_f32 v11,s71,v11
v_mfma_f32_16x16x32_bf16 v[76:79],a[120:123],a[224:227],v[76:79]
)A"
R"A(v_mul_f32 v12,s71,v12
v_mul_f32 v13,s71,v13
v_mfma_f32_16x16x32_bf16 v[76:79],a[124:127],a[228:231],v[76:79]
v_mul_f32 v14,s71,v14
v_mul_f32 v15,s71,v15
v_mfma_f32_16x16x32_bf16 v[92:95],a[124:127],a[236:239],v[92:95]
v_cvt_pk_bf16_f32 v204,v8,v9
v_cvt_pk_bf16_f32 v205,v10,v11
v_mfma_f32_16x16x32_bf16 v[92:95],a[120:123],a[232:235],v[92:95]
v_cvt_pk_bf16_f32 v206,v12,v13
v_cvt_pk_bf16_f32 v207,v14,v15
v_mfma_f32_16x16x32_bf16 v[92:95],a[116:119],a[204:207],v[92:95]
global_load_lds_dwordx4 v5,s[10:11] offset:2048 nt
v_mul_f32 v76,s71,v76
v_mfma_f32_16x16x32_bf16 v[92:95],a[112:115],a[200:203],v[92:95]
v_mul_f32 v77,s71,v77
v_mul_f32 v78,s71,v78
v_mfma_f32_16x16x32_bf16 v[108:111],a[112:115],a[208:211],v[108:111]
v_mul_f32 v79,s71,v79
v_cvt_pk_bf16_f32 v158,v76,v77
v_mfma_f32_16x16x32_bf16 v[108:111],a[116:119],a[212:215],v[108:111]
ds_read_b128 a[156:159],v1 offset:4864
v_cvt_pk_bf16_f32 v159,v78,v79
v_mfma_f32_16x16x32_bf16 v[108:111],a[120:123],a[240:243],v[108:111]
v_mul_f32 v92,s71,v92
v_mul_f32 v93,s71,v93
v_mfma_f32_16x16x32_bf16 v[108:111],a[124:127],a[244:247],v[108:111]
v_mul_f32 v94,s71,v94
v_mul_f32 v95,s71,v95
v_mfma_f32_16x16x32_bf16 v[124:127],a[124:127],a[252:255],v[124:127]
v_cvt_pk_bf16_f32 v190,v92,v93
v_cvt_pk_bf16_f32 v191,v94,v95
v_mfma_f32_16x16x32_bf16 v[124:127],a[120:123],a[248:251],v[124:127]
v_cvt_pk_bf16_f32 v209,v210,v211
v_cvt_pk_bf16_f32 v210,v212,v213
v_mfma_f32_16x16x32_bf16 v[124:127],a[116:119],a[220:223],v[124:127]
ds_read_b128 a[152:155],v1 offset:4608
v_cvt_pk_bf16_f32 v211,v214,v215
v_mfma_f32_16x16x32_bf16 v[124:127],a[112:115],a[216:219],v[124:127]
v_mul_f32 v96,s71,v96
v_mul_f32 v97,s71,v97
s_waitcnt lgkmcnt(2)
v_mfma_f32_16x16x32_bf16 v[64:67],a[128:131],v[132:135],0
v_mul_f32 v98,s71,v98
v_mul_f32 v99,s71,v99
v_mfma_f32_16x16x32_bf16 v[64:67],a[132:135],v[140:143],v[64:67]
v_mul_f32 v100,s71,v100
v_mul_f32 v101,s71,v101
v_mfma_f32_16x16x32_bf16 v[64:67],a[136:139],v[148:151],v[64:67]
v_mul_f32 v102,s71,v102
v_mul_f32 v103,s71,v103
v_mfma_f32_16x16x32_bf16 v[64:67],a[140:143],v[156:159],v[64:67]
global_load_lds_dwordx4 v5,s[10:11] offset:3072
v_cvt_pk_bf16_f32 v212,v96,v97
v_mfma_f32_16x16x32_bf16 v[80:83],a[128:131],v[164:167],0
v_cvt_pk_bf16_f32 v213,v98,v99
v_cvt_pk_bf16_f32 v214,v100,v101
v_mfma_f32_16x16x32_bf16 v[80:83],a[132:135],v[172:175],v[80:83]
v_cvt_pk_bf16_f32 v215,v102,v103
v_mul_f32 v216,s71,v216
v_mfma_f32_16x16x32_bf16 v[80:83],a[136:139],v[180:183],v[80:83]
v_max3_f32 v64,v64,v65,v66
v_max_f32 v64,v64,v67
v_mov_b32 v65,v64
v_mfma_f32_16x16x32_bf16 v[80:83],a[140:143],v[188:191],v[80:83]
v_mul_f32 v217,s71,v217
v_mul_f32 v218,s71,v218
v_mfma_f32_16x16x32_bf16 v[96:99],a[128:131],v[196:199],0
v_permlane32_swap_b32 v64,v65
v_max_f32 v64,v64,v65
v_mov_b32 v65,v64
v_mul_f32 v219,s71,v219
v_mul_f32 v220,s71,v220
v_mul_f32 v221,s71,v221
v_mul_f32 v222,s71,v222
v_mul_f32 v223,s71,v223
v_cvt_pk_bf16_f32 v216,v216,v217
v_cvt_pk_bf16_f32 v217,v218,v219
v_cvt_pk_bf16_f32 v218,v220,v221
v_cvt_pk_bf16_f32 v219,v222,v223
v_mul_f32 v104,s71,v104
v_mul_f32 v105,s71,v105
v_mul_f32 v106,s71,v106
v_mul_f32 v107,s71,v107
v_mul_f32 v108,s71,v108
v_mul_f32 v109,s71,v109
v_mul_f32 v110,s71,v110
v_mul_f32 v111,s71,v111
v_cvt_pk_bf16_f32 v220,v104,v105
v_cvt_pk_bf16_f32 v221,v106,v107
v_cvt_pk_bf16_f32 v222,v108,v109
v_cvt_pk_bf16_f32 v223,v110,v111
v_mfma_f32_16x16x32_bf16 v[96:99],a[132:135],v[204:207],v[96:99]
ds_read_b128 a[148:151],v1 offset:4352
v_max3_f32 v80,v80,v81,v82
v_mfma_f32_16x16x32_bf16 v[96:99],a[136:139],v[212:215],v[96:99]
ds_read_b128 a[144:147],v1 offset:4096
v_permlane16_swap_b32 v64,v65
v_max_f32 v64,v64,v65
v_sub_f32 v35,0,v64
v_sub_f32 v34,0,v64
v_sub_f32 v33,0,v64
v_sub_f32 v32,0,v64
v_mfma_f32_16x16x32_bf16 v[96:99],a[140:143],v[220:223],v[96:99]
ds_read_b128 a[160:163],v1 offset:8192
v_max_f32 v80,v80,v83
v_mfma_f32_16x16x32_bf16 v[64:67],a[128:131],v[132:135],v[32:35]
L_pd4_%=:
s_cbranch_execz L_pd8_%=
s_waitcnt lgkmcnt(3)
ds_read_b128 a[164:167],v1 offset:8448
v_mov_b32 v81,v80
v_mfma_f32_16x16x32_bf16 v[64:67],a[132:135],v[140:143],v[64:67]
s_add_u32 s10,s24,s74
s_addc_u32 s11,s25,0
v_mul_f32 v232,s71,v232
v_mfma_f32_16x16x32_bf16 v[64:67],a[136:139],v[148:151],v[64:67]
s_add_u32 s74,s74,16384
ds_read_b128 a[168:171],v1 offset:8704
v_max3_f32 v96,v96,v97,v98
v_mfma_f32_16x16x32_bf16 v[64:67],a[140:143],v[156:159],v[64:67]
s_add_u32 m0,s76,16384
ds_read_b128 a[172:175],v1 offset:8960
v_max_f32 v96,v96,v99
v_mfma_f32_16x16x32_bf16 v[68:71],a[156:159],v[156:159],v[32:35]
s_and_b32 s74,s74,0x3ffff
ds_read_b128 a[188:191],v1 offset:13056
v_mov_b32 v97,v96
v_mfma_f32_16x16x32_bf16 v[68:71],a[152:155],v[148:151],v[68:71]
global_load_lds_dwordx4 v4,s[10:11]
v_mul_f32 v233,s71,v233
s_waitcnt lgkmcnt(6)
v_mfma_f32_16x16x32_bf16 v[68:71],a[148:151],v[140:143],v[68:71]
s_or_b32 s74,s74,s69
ds_read_b128 a[184:187],v1 offset:12800
v_mul_f32 v234,s71,v234
s_waitcnt lgkmcnt(6)
v_mfma_f32_16x16x32_bf16 v[68:71],a[144:147],v[132:135],v[68:71]
s_sub_u32 s76,s76,32768
ds_read_b128 a[180:183],v1 offset:12544
v_mul_f32 v235,s71,v235
s_waitcnt lgkmcnt(6)
v_mfma_f32_16x16x32_bf16 v[72:75],a[160:163],v[132:135],v[32:35]
s_and_b32 s76,s76,0x1ffff
ds_read_b128 a[176:179],v1 offset:12288
v_mul_f32 v236,s71,v236
s_waitcnt lgkmcnt(6)
v_mfma_f32_16x16x32_bf16 v[72:75],a[164:167],v[140:143],v[72:75]
s_and_b32 s69,s75,0xfffc0000
v_permlane32_swap_b32 v80,v81
s_waitcnt lgkmcnt(5)
v_mfma_f32_16x16x32_bf16 v[72:75],a[168:171],v[148:151],v[72:75]
s_add_u32 s75,s75,16384
v_max_f32 v80,v80,v81
v_mov_b32 v81,v80
s_waitcnt lgkmcnt(4)
v_mfma_f32_16x16x32_bf16 v[72:75],a[172:175],v[156:159],v[72:75]
s_waitcnt lgkmcnt(3)
v_mul_f32 v237,s71,v237
v_mul_f32 v238,s71,v238
v_mfma_f32_16x16x32_bf16 v[76:79],a[188:191],v[156:159],v[32:35]
s_waitcnt lgkmcnt(2)
v_permlane16_swap_b32 v80,v81
v_mfma_f32_16x16x32_bf16 v[76:79],a[184:187],v[148:151],v[76:79]
s_waitcnt lgkmcnt(1)
v_max_f32 v80,v80,v81
v_sub_f32 v39,0,v80
v_sub_f32 v38,0,v80
v_sub_f32 v37,0,v80
v_sub_f32 v36,0,v80
v_mfma_f32_16x16x32_bf16 v[76:79],a[180:183],v[140:143],v[76:79]
s_waitcnt lgkmcnt(0)
v_mul_f32 v239,s71,v239
v_cvt_pk_bf16_f32 v232,v232,v233
v_mfma_f32_16x16x32_bf16 v[76:79],a[176:179],v[132:135],v[76:79]
v_cvt_pk_bf16_f32 v233,v234,v235
v_cvt_pk_bf16_f32 v234,v236,v237
v_mfma_f32_16x16x32_bf16 v[80:83],a[128:131],v[164:167],v[36:39]
v_cvt_pk_bf16_f32 v235,v238,v239
v_accvgpr_read_b32 v16,a56
v_mfma_f32_16x16x32_bf16 v[80:83],a[132:135],v[172:175],v[80:83]
v_accvgpr_read_b32 v17,a57
v_accvgpr_read_b32 v18,a58
v_mfma_f32_16x16x32_bf16 v[80:83],a[136:139],v[180:183],v[80:83]
v_accvgpr_read_b32 v19,a59
v_accvgpr_read_b32 v20,a60
v_mfma_f32_16x16x32_bf16 v[80:83],a[140:143],v[188:191],v[80:83]
global_load_lds_dwordx4 v5,s[10:11] offset:1024
v_accvgpr_read_b32 v21,a61
v_mfma_f32_16x16x32_bf16 v[84:87],a[156:159],v[188:191],v[36:39]
v_accvgpr_read_b32 v22,a62
v_accvgpr_read_b32 v23,a63
v_mfma_f32_16x16x32_bf16 v[84:87],a[152:155],v[180:183],v[84:87]
v_mul_f32 v16,s71,v16
v_mul_f32 v17,s71,v17
v_mfma_f32_16x16x32_bf16 v[84:87],a[148:151],v[172:175],v[84:87]
v_mul_f32 v18,s71,v18
v_mul_f32 v19,s71,v19
v_mfma_f32_16x16x32_bf16 v[84:87],a[144:147],v[164:167],v[84:87]
v_mul_f32 v20,s71,v20
v_mul_f32 v21,s71,v21
v_mfma_f32_16x16x32_bf16 v[88:91],a[160:163],v[164:167],v[36:39]
v_mul_f32 v22,s71,v22
v_mul_f32 v23,s71,v23
v_mfma_f32_16x16x32_bf16 v[88:91],a[164:167],v[172:175],v[88:91]
v_cvt_pk_bf16_f32 v236,v16,v17
v_cvt_pk_bf16_f32 v237,v18,v19
v_mfma_f32_16x16x32_bf16 v[88:91],a[168:171],v[180:183],v[88:91]
v_cvt_pk_bf16_f32 v238,v20,v21
v_cvt_pk_bf16_f32 v239,v22,v23
v_mfma_f32_16x16x32_bf16 v[88:91],a[172:175],v[188:191],v[88:91]
v_mul_f32 v240,s71,v240
v_mul_f32 v241,s71,v241
v_mfma_f32_16x16x32_bf16 v[92:95],a[188:191],v[188:191],v[36:39]
v_mul_f32 v242,s71,v242
v_mul_f32 v243,s71,v243
v_mfma_f32_16x16x32_bf16 v[92:95],a[184:187],v[180:183],v[92:95]
v_mul_f32 v244,s71,v244
v_mul_f32 v245,s71,v245
v_mfma_f32_16x16x32_bf16 v[92:95],a[180:183],v[172:175],v[92:95]
v_mul_f32 v246,s71,v246
v_mul_f32 v247,s71,v247
v_mfma_f32_16x16x32_bf16 v[92:95],a[176:179],v[164:167],v[92:95]
v_cvt_pk_bf16_f32 v240,v240,v241
v_cvt_pk_bf16_f32 v241,v242,v243
v_cvt_pk_bf16_f32 v242,v244,v245
v_cvt_pk_bf16_f32 v243,v246,v247
v_mul_f32 v112,s71,v112
v_mul_f32 v113,s71,v113
v_mul_f32 v114,s71,v114
v_mul_f32 v115,s71,v115
v_mul_f32 v116,s71,v116
v_mul_f32 v117,s71,v117
v_mul_f32 v118,s71,v118
v_mul_f32 v119,s71,v119
v_cvt_pk_bf16_f32 v244,v112,v113
v_cvt_pk_bf16_f32 v245,v114,v115
v_cvt_pk_bf16_f32 v246,v116,v117
v_cvt_pk_bf16_f32 v247,v118,v119
v_permlane32_swap_b32 v96,v97
v_max_f32 v96,v96,v97
v_mov_b32 v97,v96
v_mfma_f32_16x16x32_bf16 v[112:115],a[128:131],v[228:231],0
v_mul_f32 v248,s71,v248
v_mul_f32 v249,s71,v249
v_mul_f32 v250,s71,v250
v_mul_f32 v251,s71,v251
v_mul_f32 v252,s71,v252
v_mul_f32 v253,s71,v253
v_mul_f32 v254,s71,v254
v_mul_f32 v255,s71,v255
v_cvt_pk_bf16_f32 v248,v248,v249
v_cvt_pk_bf16_f32 v249,v250,v251
v_cvt_pk_bf16_f32 v250,v252,v253
v_cvt_pk_bf16_f32 v251,v254,v255
v_mul_f32 v120,s71,v120
v_mul_f32 v121,s71,v121
v_mul_f32 v122,s71,v122
v_mul_f32 v123,s71,v123
v_mul_f32 v124,s71,v124
v_mul_f32 v125,s71,v125
v_mul_f32 v126,s71,v126
v_mul_f32 v127,s71,v127
v_cvt_pk_bf16_f32 v252,v120,v121
v_cvt_pk_bf16_f32 v253,v122,v123
v_cvt_pk_bf16_f32 v254,v124,v125
v_cvt_pk_bf16_f32 v255,v126,v127
v_mfma_f32_16x16x32_bf16 v[112:115],a[132:135],v[236:239],v[112:115]
global_load_lds_dwordx4 v4,s[10:11] offset:2048
v_permlane16_swap_b32 v96,v97
v_max_f32 v96,v96,v97
v_sub_f32 v43,0,v96
v_sub_f32 v42,0,v96
v_sub_f32 v41,0,v96
v_sub_f32 v40,0,v96
v_mfma_f32_16x16x32_bf16 v[112:115],a[136:139],v[244:247],v[112:115]
ds_read_b64_tr_b16 a[192:193],v2
ds_read_b64_tr_b16 a[194:195],v2 offset:4096
v_mfma_f32_16x16x32_bf16 v[112:115],a[140:143],v[252:255],v[112:115]
ds_read_b64_tr_b16 a[196:197],v2 offset:8192
ds_read_b64_tr_b16 a[198:199],v2 offset:12288
v_mfma_f32_16x16x32_bf16 v[96:99],a[128:131],v[196:199],v[40:43]
ds_read_b64_tr_b16 a[200:201],v3
ds_read_b64_tr_b16 a[202:203],v3 offset:4096
v_mfma_f32_16x16x32_bf16 v[96:99],a[132:135],v[204:207],v[96:99]
ds_read_b64_tr_b16 a[204:205],v3 offset:8192
ds_read_b64_tr_b16 a[206:207],v3 offset:12288
v_mfma_f32_16x16x32_bf16 v[96:99],a[136:139],v[212:215],v[96:99]
ds_read_b64_tr_b16 a[208:209],v2 offset:256
v_max3_f32 v112,v112,v113,v114
v_mfma_f32_16x16x32_bf16 v[96:99],a[140:143],v[220:223],v[96:99]
ds_read_b64_tr_b16 a[210:211],v2 offset:4352
ds_read_b64_tr_b16 a[212:213],v2 offset:8448
v_mfma_f32_16x16x32_bf16 v[100:103],a[156:159],v[220:223],v[40:43]
ds_read_b64_tr_b16 a[214:215],v2 offset:12544
v_max_f32 v112,v112,v115
v_mfma_f32_16x16x32_bf16 v[100:103],a[152:155],v[212:215],v[100:103]
ds_read_b64_tr_b16 a[216:217],v3 offset:256
ds_read_b64_tr_b16 a[218:219],v3 offset:4352
v_mfma_f32_16x16x32_bf16 v[100:103],a[148:151],v[204:207],v[100:103]
v_exp_f32 v64,v64
v_mfma_f32_16x16x32_bf16 v[100:103],a[144:147],v[196:199],v[100:103]
v_exp_f32 v65,v65
v_mfma_f32_16x16x32_bf16 v[104:107],a[160:163],v[196:199],v[40:43]
v_exp_f32 v66,v66
v_mfma_f32_16x16x32_bf16 v[104:107],a[164:167],v[204:207],v[104:107]
v_exp_f32 v67,v67
v_mfma_f32_16x16x32_bf16 v[104:107],a[168:171],v[212:215],v[104:107]
v_exp_f32 v68,v68
v_mfma_f32_16x16x32_bf16 v[104:107],a[172:175],v[220:223],v[104:107]
global_load_lds_dwordx4 v5,s[10:11] offset:3072
ds_read_b64_tr_b16 a[220:221],v3 offset:8448
v_mfma_f32_16x16x32_bf16 v[108:111],a[188:191],v[220:223],v[40:43]
v_exp_f32 v69,v69
v_exp_f32 v70,v70
v_exp_f32 v71,v71
v_mfma_f32_16x16x32_bf16 v[108:111],a[184:187],v[212:215],v[108:111]
ds_read_b64_tr_b16 a[222:223],v3 offset:12544
v_cvt_pk_bf16_f32 v64,v64,v65
v_cvt_pk_bf16_f32 v65,v66,v67
v_cvt_pk_bf16_f32 v66,v68,v69
v_cvt_pk_bf16_f32 v67,v70,v71
v_exp_f32 v72,v72
v_exp_f32 v73,v73
v_exp_f32 v74,v74
v_exp_f32 v75,v75
v_exp_f32 v76,v76
v_exp_f32 v77,v77
v_exp_f32 v78,v78
v_exp_f32 v79,v79
v_mfma_f32_16x16x32_bf16 v[108:111],a[180:183],v[204:207],v[108:111]
ds_read_b64_tr_b16 a[224:225],v2 offset:512
ds_read_b64_tr_b16 a[226:227],v2 offset:4608
)A"
R"A(v_cvt_pk_bf16_f32 v72,v72,v73
v_cvt_pk_bf16_f32 v73,v74,v75
v_cvt_pk_bf16_f32 v74,v76,v77
v_cvt_pk_bf16_f32 v75,v78,v79
v_mfma_f32_16x16x32_bf16 v[108:111],a[176:179],v[196:199],v[108:111]
s_waitcnt lgkmcnt(4)
ds_read_b64_tr_b16 a[228:229],v2 offset:8704
ds_read_b64_tr_b16 a[230:231],v2 offset:12800
v_mfma_f32_16x16x32_bf16 a[0:3],a[192:195],v[64:67],0
ds_read_b64_tr_b16 a[232:233],v3 offset:512
ds_read_b64_tr_b16 a[234:235],v3 offset:4608
v_mfma_f32_16x16x32_bf16 a[0:3],a[196:199],v[72:75],a[0:3]
ds_read_b64_tr_b16 a[236:237],v3 offset:8704
ds_read_b64_tr_b16 a[238:239],v3 offset:12800
v_mfma_f32_16x16x32_bf16 a[4:7],a[200:203],v[64:67],0
ds_read_b64_tr_b16 a[240:241],v2 offset:768
v_mov_b32 v113,v112
v_mfma_f32_16x16x32_bf16 a[4:7],a[204:207],v[72:75],a[4:7]
ds_read_b64_tr_b16 a[242:243],v2 offset:4864
ds_read_b64_tr_b16 a[244:245],v2 offset:8960
v_mfma_f32_16x16x32_bf16 a[8:11],a[208:211],v[64:67],0
ds_read_b64_tr_b16 a[246:247],v2 offset:13056
v_add_u32 v1,s78,v1
v_mfma_f32_16x16x32_bf16 a[8:11],a[212:215],v[72:75],a[8:11]
ds_read_b64_tr_b16 a[248:249],v3 offset:768
ds_read_b64_tr_b16 a[250:251],v3 offset:4864
v_mfma_f32_16x16x32_bf16 a[12:15],a[216:219],v[64:67],0
s_waitcnt lgkmcnt(12)
ds_read_b64_tr_b16 a[252:253],v3 offset:8960
v_add_u32 v2,s78,v2
v_mfma_f32_16x16x32_bf16 a[12:15],a[220:223],v[72:75],a[12:15]
s_waitcnt lgkmcnt(6)
ds_read_b64_tr_b16 a[254:255],v3 offset:13056
v_add_u32 v3,s78,v3
v_mfma_f32_16x16x32_bf16 a[16:19],a[224:227],v[64:67],0
s_sub_u32 s78,s77,32768
v_permlane32_swap_b32 v112,v113
v_mfma_f32_16x16x32_bf16 a[16:19],a[228:231],v[72:75],a[16:19]
s_and_b32 s78,s78,0x1ffff
v_max_f32 v112,v112,v113
v_mov_b32 v113,v112
v_mfma_f32_16x16x32_bf16 a[20:23],a[232:235],v[64:67],0
s_sub_u32 s78,s78,s77
v_exp_f32 v80,v80
v_mfma_f32_16x16x32_bf16 a[20:23],a[236:239],v[72:75],a[20:23]
s_add_u32 s77,s77,s78
v_permlane16_swap_b32 v112,v113
s_waitcnt lgkmcnt(5)
v_mfma_f32_16x16x32_bf16 a[24:27],a[240:243],v[64:67],0
s_waitcnt lgkmcnt(4)
v_max_f32 v112,v112,v113
v_max_f32 v112,s72,v112
v_mfma_f32_16x16x32_bf16 a[24:27],a[244:247],v[72:75],a[24:27]
s_waitcnt lgkmcnt(2)
v_sub_f32 v47,0,v112
v_sub_f32 v46,0,v112
v_mfma_f32_16x16x32_bf16 a[28:31],a[248:251],v[64:67],0
s_waitcnt lgkmcnt(0)
v_sub_f32 v45,0,v112
v_sub_f32 v44,0,v112
v_mfma_f32_16x16x32_bf16 a[28:31],a[252:255],v[72:75],a[28:31]
v_exp_f32 v81,v81
v_mfma_f32_16x16x32_bf16 v[48:51],v[28:31],v[64:67],0
v_exp_f32 v82,v82
v_mfma_f32_16x16x32_bf16 v[48:51],v[28:31],v[72:75],v[48:51]
v_exp_f32 v83,v83
s_waitcnt vmcnt(8)
s_barrier
v_mfma_f32_16x16x32_bf16 v[112:115],a[128:131],v[228:231],v[44:47]
s_add_u32 s10,s20,s74
s_addc_u32 s11,s21,0
s_add_u32 m0,s76,0
v_mfma_f32_16x16x32_bf16 v[112:115],a[132:135],v[236:239],v[112:115]
v_exp_f32 v84,v84
v_mfma_f32_16x16x32_bf16 v[112:115],a[136:139],v[244:247],v[112:115]
global_load_lds_dwordx4 v4,s[10:11] nt
v_cvt_pk_bf16_f32 v80,v80,v81
v_mfma_f32_16x16x32_bf16 v[112:115],a[140:143],v[252:255],v[112:115]
v_exp_f32 v85,v85
v_mfma_f32_16x16x32_bf16 v[116:119],a[156:159],v[252:255],v[44:47]
v_exp_f32 v86,v86
v_mfma_f32_16x16x32_bf16 v[116:119],a[152:155],v[244:247],v[116:119]
v_exp_f32 v87,v87
v_mfma_f32_16x16x32_bf16 v[116:119],a[148:151],v[236:239],v[116:119]
v_cvt_pk_bf16_f32 v81,v82,v83
v_cvt_pk_bf16_f32 v82,v84,v85
v_mfma_f32_16x16x32_bf16 v[116:119],a[144:147],v[228:231],v[116:119]
ds_read_b128 a[128:131],v1
v_cvt_pk_bf16_f32 v83,v86,v87
v_mfma_f32_16x16x32_bf16 v[120:123],a[160:163],v[228:231],v[44:47]
v_exp_f32 v88,v88
v_mfma_f32_16x16x32_bf16 v[120:123],a[164:167],v[236:239],v[120:123]
v_exp_f32 v89,v89
v_mfma_f32_16x16x32_bf16 v[120:123],a[168:171],v[244:247],v[120:123]
v_exp_f32 v90,v90
v_mfma_f32_16x16x32_bf16 v[120:123],a[172:175],v[252:255],v[120:123]
v_exp_f32 v91,v91
v_mfma_f32_16x16x32_bf16 v[124:127],a[188:191],v[252:255],v[44:47]
v_exp_f32 v92,v92
v_mfma_f32_16x16x32_bf16 v[124:127],a[184:187],v[244:247],v[124:127]
v_exp_f32 v93,v93
v_exp_f32 v94,v94
v_exp_f32 v95,v95
v_mfma_f32_16x16x32_bf16 v[124:127],a[180:183],v[236:239],v[124:127]
v_cvt_pk_bf16_f32 v88,v88,v89
v_cvt_pk_bf16_f32 v89,v90,v91
v_cvt_pk_bf16_f32 v90,v92,v93
v_cvt_pk_bf16_f32 v91,v94,v95
v_mfma_f32_16x16x32_bf16 v[124:127],a[176:179],v[228:231],v[124:127]
v_exp_f32 v96,v96
v_mfma_f32_16x16x32_bf16 a[32:35],a[192:195],v[80:83],0
global_load_lds_dwordx4 v4,s[10:11] offset:1024 nt
ds_read_b128 a[132:135],v1 offset:256
v_mfma_f32_16x16x32_bf16 a[32:35],a[196:199],v[88:91],a[32:35]
v_exp_f32 v97,v97
v_mfma_f32_16x16x32_bf16 a[36:39],a[200:203],v[80:83],0
ds_read_b128 a[136:139],v1 offset:512
v_cvt_pk_bf16_f32 v96,v96,v97
v_mfma_f32_16x16x32_bf16 a[36:39],a[204:207],v[88:91],a[36:39]
v_exp_f32 v98,v98
v_mfma_f32_16x16x32_bf16 a[40:43],a[208:211],v[80:83],0
v_exp_f32 v99,v99
v_mfma_f32_16x16x32_bf16 a[40:43],a[212:215],v[88:91],a[40:43]
ds_read_b128 a[140:143],v1 offset:768
v_cvt_pk_bf16_f32 v97,v98,v99
v_mfma_f32_16x16x32_bf16 a[44:47],a[216:219],v[80:83],0
v_exp_f32 v100,v100
v_mfma_f32_16x16x32_bf16 a[44:47],a[220:223],v[88:91],a[44:47]
v_exp_f32 v101,v101
v_mfma_f32_16x16x32_bf16 a[48:51],a[224:227],v[80:83],0
ds_read_b128 a[156:159],v1 offset:4864
v_cvt_pk_bf16_f32 v98,v100,v101
v_mfma_f32_16x16x32_bf16 a[48:51],a[228:231],v[88:91],a[48:51]
v_exp_f32 v102,v102
v_mfma_f32_16x16x32_bf16 a[52:55],a[232:235],v[80:83],0
v_exp_f32 v103,v103
v_mfma_f32_16x16x32_bf16 a[52:55],a[236:239],v[88:91],a[52:55]
ds_read_b128 a[152:155],v1 offset:4608
v_cvt_pk_bf16_f32 v99,v102,v103
v_mfma_f32_16x16x32_bf16 a[56:59],a[240:243],v[80:83],0
ds_read_b128 a[148:151],v1 offset:4352
v_mfma_f32_16x16x32_bf16 a[56:59],a[244:247],v[88:91],a[56:59]
ds_read_b128 a[144:147],v1 offset:4096
v_mfma_f32_16x16x32_bf16 a[60:63],a[248:251],v[80:83],0
global_load_lds_dwordx4 v5,s[10:11] offset:2048 nt
ds_read_b128 a[160:163],v1 offset:8192
v_mfma_f32_16x16x32_bf16 a[60:63],a[252:255],v[88:91],a[60:63]
v_exp_f32 v104,v104
v_mfma_f32_16x16x32_bf16 v[52:55],v[28:31],v[80:83],0
ds_read_b128 a[164:167],v1 offset:8448
v_mfma_f32_16x16x32_bf16 v[52:55],v[28:31],v[88:91],v[52:55]
s_waitcnt lgkmcnt(5)
ds_read_b128 a[168:171],v1 offset:8704
v_mfma_f32_16x16x32_bf16 v[64:67],a[128:131],v[132:135],v[32:35]
ds_read_b128 a[172:175],v1 offset:8960
v_mfma_f32_16x16x32_bf16 v[64:67],a[132:135],v[140:143],v[64:67]
v_exp_f32 v105,v105
v_mfma_f32_16x16x32_bf16 v[64:67],a[136:139],v[148:151],v[64:67]
ds_read_b128 a[188:191],v1 offset:13056
v_cvt_pk_bf16_f32 v104,v104,v105
v_mfma_f32_16x16x32_bf16 v[64:67],a[140:143],v[156:159],v[64:67]
ds_read_b128 a[184:187],v1 offset:12800
v_mfma_f32_16x16x32_bf16 v[68:71],a[156:159],v[156:159],v[32:35]
s_waitcnt lgkmcnt(5)
ds_read_b128 a[180:183],v1 offset:12544
v_mfma_f32_16x16x32_bf16 v[68:71],a[152:155],v[148:151],v[68:71]
v_exp_f32 v106,v106
v_mfma_f32_16x16x32_bf16 v[68:71],a[148:151],v[140:143],v[68:71]
ds_read_b128 a[176:179],v1 offset:12288
v_add_u32 v1,s78,v1
v_mfma_f32_16x16x32_bf16 v[68:71],a[144:147],v[132:135],v[68:71]
v_exp_f32 v107,v107
v_mfma_f32_16x16x32_bf16 v[72:75],a[160:163],v[132:135],v[32:35]
s_waitcnt lgkmcnt(4)
v_exp_f32 v108,v108
v_mfma_f32_16x16x32_bf16 v[72:75],a[164:167],v[140:143],v[72:75]
v_exp_f32 v109,v109
v_mfma_f32_16x16x32_bf16 v[72:75],a[168:171],v[148:151],v[72:75]
global_load_lds_dwordx4 v5,s[10:11] offset:3072
v_cvt_pk_bf16_f32 v105,v106,v107
v_mfma_f32_16x16x32_bf16 v[72:75],a[172:175],v[156:159],v[72:75]
s_waitcnt lgkmcnt(2)
v_exp_f32 v110,v110
v_mfma_f32_16x16x32_bf16 v[76:79],a[188:191],v[156:159],v[32:35]
v_exp_f32 v111,v111
v_mfma_f32_16x16x32_bf16 v[76:79],a[184:187],v[148:151],v[76:79]
s_waitcnt lgkmcnt(1)
v_cvt_pk_bf16_f32 v106,v108,v109
v_cvt_pk_bf16_f32 v107,v110,v111
v_mfma_f32_16x16x32_bf16 v[76:79],a[180:183],v[140:143],v[76:79]
s_waitcnt lgkmcnt(0)
v_exp_f32 v112,v112
v_mfma_f32_16x16x32_bf16 v[76:79],a[176:179],v[132:135],v[76:79]
v_exp_f32 v113,v113
v_mfma_f32_16x16x32_bf16 a[64:67],a[192:195],v[96:99],0
v_exp_f32 v114,v114
v_mfma_f32_16x16x32_bf16 a[64:67],a[196:199],v[104:107],a[64:67]
v_exp_f32 v115,v115
v_mfma_f32_16x16x32_bf16 a[68:71],a[200:203],v[96:99],0
v_exp_f32 v116,v116
v_mfma_f32_16x16x32_bf16 a[68:71],a[204:207],v[104:107],a[68:71]
v_exp_f32 v117,v117
v_mfma_f32_16x16x32_bf16 a[72:75],a[208:211],v[96:99],0
s_add_u32 s10,s24,s74
s_addc_u32 s11,s25,0
s_add_u32 s74,s74,16384
s_and_b32 s74,s74,0x3ffff
s_or_b32 s74,s74,s69
s_lshl_b32 s69,s60,13
s_add_u32 s69,s69,131072
v_add_u32 v8,s69,v6
v_mfma_f32_16x16x32_bf16 a[72:75],a[212:215],v[104:107],a[72:75]
s_lshl_b32 s69,s60,13
ds_write_b128 v8,v[24:27] offset:4096
v_mfma_f32_16x16x32_bf16 a[76:79],a[216:219],v[96:99],0
s_add_u32 m0,s76,16384
ds_write_b32 v8,v7 offset:5120
v_cvt_pk_bf16_f32 v112,v112,v113
v_mfma_f32_16x16x32_bf16 a[76:79],a[220:223],v[104:107],a[76:79]
s_sub_u32 s76,s76,32768
v_bfe_u32 v24,v6,4,4
v_cvt_pk_bf16_f32 v113,v114,v115
v_mfma_f32_16x16x32_bf16 a[80:83],a[224:227],v[96:99],0
global_load_lds_dwordx4 v4,s[10:11]
v_lshrrev_b32 v7,8,v6
v_lshlrev_b32 v7,4,v7
v_lshl_add_u32 v7,v24,8,v7
v_mfma_f32_16x16x32_bf16 a[80:83],a[228:231],v[104:107],a[80:83]
s_and_b32 s76,s76,0x1ffff
v_exp_f32 v118,v118
v_mfma_f32_16x16x32_bf16 a[84:87],a[232:235],v[96:99],0
s_add_u32 s69,s69,131072
v_exp_f32 v119,v119
v_mfma_f32_16x16x32_bf16 a[84:87],a[236:239],v[104:107],a[84:87]
v_cvt_pk_bf16_f32 v114,v116,v117
v_cvt_pk_bf16_f32 v115,v118,v119
v_mfma_f32_16x16x32_bf16 a[88:91],a[240:243],v[96:99],0
v_exp_f32 v120,v120
v_mfma_f32_16x16x32_bf16 a[88:91],a[244:247],v[104:107],a[88:91]
buffer_load_dwordx4 v[8:11],v7,s[20:23],s65 offen nt
v_mfma_f32_16x16x32_bf16 a[92:95],a[248:251],v[96:99],0
v_exp_f32 v121,v121
v_mfma_f32_16x16x32_bf16 a[92:95],a[252:255],v[104:107],a[92:95]
v_exp_f32 v122,v122
v_mfma_f32_16x16x32_bf16 v[56:59],v[28:31],v[96:99],0
v_exp_f32 v123,v123
v_mfma_f32_16x16x32_bf16 v[56:59],v[28:31],v[104:107],v[56:59]
v_exp_f32 v124,v124
v_mfma_f32_16x16x32_bf16 v[80:83],a[128:131],v[164:167],v[36:39]
buffer_load_dwordx4 v[12:15],v7,s[20:23],s65 offen offset:64 nt
v_mfma_f32_16x16x32_bf16 v[80:83],a[132:135],v[172:175],v[80:83]
v_exp_f32 v125,v125
v_mfma_f32_16x16x32_bf16 v[80:83],a[136:139],v[180:183],v[80:83]
v_exp_f32 v126,v126
v_mfma_f32_16x16x32_bf16 v[80:83],a[140:143],v[188:191],v[80:83]
v_exp_f32 v127,v127
v_mfma_f32_16x16x32_bf16 v[84:87],a[156:159],v[188:191],v[36:39]
v_cvt_pk_bf16_f32 v120,v120,v121
v_cvt_pk_bf16_f32 v121,v122,v123
v_mfma_f32_16x16x32_bf16 v[84:87],a[152:155],v[180:183],v[84:87]
buffer_load_dwordx4 v[16:19],v7,s[20:23],s65 offen offset:128 nt
v_mfma_f32_16x16x32_bf16 v[84:87],a[148:151],v[172:175],v[84:87]
v_cvt_pk_bf16_f32 v122,v124,v125
v_cvt_pk_bf16_f32 v123,v126,v127
v_mfma_f32_16x16x32_bf16 v[84:87],a[144:147],v[164:167],v[84:87]
v_exp_f32 v64,v64
v_mfma_f32_16x16x32_bf16 v[88:91],a[160:163],v[164:167],v[36:39]
v_exp_f32 v65,v65
v_mfma_f32_16x16x32_bf16 v[88:91],a[164:167],v[172:175],v[88:91]
v_exp_f32 v66,v66
v_mfma_f32_16x16x32_bf16 v[88:91],a[168:171],v[180:183],v[88:91]
v_exp_f32 v67,v67
v_mfma_f32_16x16x32_bf16 v[88:91],a[172:175],v[188:191],v[88:91]
global_load_lds_dwordx4 v5,s[10:11] offset:1024
v_cvt_pk_bf16_f32 v64,v64,v65
v_mfma_f32_16x16x32_bf16 v[92:95],a[188:191],v[188:191],v[36:39]
v_exp_f32 v68,v68
v_mfma_f32_16x16x32_bf16 v[92:95],a[184:187],v[180:183],v[92:95]
v_exp_f32 v69,v69
v_mfma_f32_16x16x32_bf16 v[92:95],a[180:183],v[172:175],v[92:95]
v_exp_f32 v70,v70
v_mfma_f32_16x16x32_bf16 v[92:95],a[176:179],v[164:167],v[92:95]
v_exp_f32 v71,v71
v_mfma_f32_16x16x32_bf16 a[96:99],a[192:195],v[112:115],0
buffer_load_dwordx4 v[20:23],v7,s[20:23],s65 offen offset:192 nt
v_mfma_f32_16x16x32_bf16 a[96:99],a[196:199],v[120:123],a[96:99]
ds_read_b64_tr_b16 a[192:193],v2
ds_read_b64_tr_b16 a[194:195],v2 offset:4096
v_mfma_f32_16x16x32_bf16 a[100:103],a[200:203],v[112:115],0
ds_read_b64_tr_b16 a[196:197],v2 offset:8192
ds_read_b64_tr_b16 a[198:199],v2 offset:12288
v_mfma_f32_16x16x32_bf16 a[100:103],a[204:207],v[120:123],a[100:103]
ds_read_b64_tr_b16 a[200:201],v3
ds_read_b64_tr_b16 a[202:203],v3 offset:4096
v_mfma_f32_16x16x32_bf16 a[104:107],a[208:211],v[112:115],0
)A"
R"A(ds_read_b64_tr_b16 a[204:205],v3 offset:8192
ds_read_b64_tr_b16 a[206:207],v3 offset:12288
v_mfma_f32_16x16x32_bf16 a[104:107],a[212:215],v[120:123],a[104:107]
ds_read_b64_tr_b16 a[208:209],v2 offset:256
v_cvt_pk_bf16_f32 v65,v66,v67
v_mfma_f32_16x16x32_bf16 a[108:111],a[216:219],v[112:115],0
global_load_lds_dwordx4 v4,s[10:11] offset:2048
ds_read_b64_tr_b16 a[210:211],v2 offset:4352
v_mfma_f32_16x16x32_bf16 a[108:111],a[220:223],v[120:123],a[108:111]
ds_read_b64_tr_b16 a[212:213],v2 offset:8448
ds_read_b64_tr_b16 a[214:215],v2 offset:12544
v_mfma_f32_16x16x32_bf16 a[112:115],a[224:227],v[112:115],0
ds_read_b64_tr_b16 a[216:217],v3 offset:256
ds_read_b64_tr_b16 a[218:219],v3 offset:4352
v_mfma_f32_16x16x32_bf16 a[112:115],a[228:231],v[120:123],a[112:115]
v_cvt_pk_bf16_f32 v66,v68,v69
v_cvt_pk_bf16_f32 v67,v70,v71
v_mfma_f32_16x16x32_bf16 a[116:119],a[232:235],v[112:115],0
v_exp_f32 v72,v72
v_mfma_f32_16x16x32_bf16 a[116:119],a[236:239],v[120:123],a[116:119]
v_exp_f32 v73,v73
v_mfma_f32_16x16x32_bf16 a[120:123],a[240:243],v[112:115],0
v_exp_f32 v74,v74
v_mfma_f32_16x16x32_bf16 a[120:123],a[244:247],v[120:123],a[120:123]
v_exp_f32 v75,v75
v_mfma_f32_16x16x32_bf16 a[124:127],a[248:251],v[112:115],0
v_exp_f32 v76,v76
v_mfma_f32_16x16x32_bf16 a[124:127],a[252:255],v[120:123],a[124:127]
v_exp_f32 v77,v77
v_mfma_f32_16x16x32_bf16 v[60:63],v[28:31],v[112:115],0
v_exp_f32 v78,v78
v_mfma_f32_16x16x32_bf16 v[60:63],v[28:31],v[120:123],v[60:63]
global_load_lds_dwordx4 v5,s[10:11] offset:3072
ds_read_b64_tr_b16 a[220:221],v3 offset:8448
v_mfma_f32_16x16x32_bf16 v[96:99],a[128:131],v[196:199],v[40:43]
v_exp_f32 v79,v79
v_mfma_f32_16x16x32_bf16 v[96:99],a[132:135],v[204:207],v[96:99]
ds_read_b64_tr_b16 a[222:223],v3 offset:12544
v_cvt_pk_bf16_f32 v72,v72,v73
v_mfma_f32_16x16x32_bf16 v[96:99],a[136:139],v[212:215],v[96:99]
v_cvt_pk_bf16_f32 v73,v74,v75
v_cvt_pk_bf16_f32 v74,v76,v77
v_mfma_f32_16x16x32_bf16 v[96:99],a[140:143],v[220:223],v[96:99]
ds_read_b64_tr_b16 a[224:225],v2 offset:512
v_cvt_pk_bf16_f32 v75,v78,v79
v_mfma_f32_16x16x32_bf16 v[100:103],a[156:159],v[220:223],v[40:43]
ds_read_b64_tr_b16 a[226:227],v2 offset:4608
ds_read_b64_tr_b16 a[228:229],v2 offset:8704
v_mfma_f32_16x16x32_bf16 v[100:103],a[152:155],v[212:215],v[100:103]
ds_read_b64_tr_b16 a[230:231],v2 offset:12800
ds_read_b64_tr_b16 a[232:233],v3 offset:512
v_mfma_f32_16x16x32_bf16 v[100:103],a[148:151],v[204:207],v[100:103]
ds_read_b64_tr_b16 a[234:235],v3 offset:4608
ds_read_b64_tr_b16 a[236:237],v3 offset:8704
v_mfma_f32_16x16x32_bf16 v[100:103],a[144:147],v[196:199],v[100:103]
ds_read_b64_tr_b16 a[238:239],v3 offset:12800
ds_read_b64_tr_b16 a[240:241],v2 offset:768
v_mfma_f32_16x16x32_bf16 v[104:107],a[160:163],v[196:199],v[40:43]
ds_read_b64_tr_b16 a[242:243],v2 offset:4864
v_add_u32 v7,s69,v6
v_mfma_f32_16x16x32_bf16 v[104:107],a[164:167],v[204:207],v[104:107]
s_add_u32 s10,s20,s74
s_addc_u32 s11,s21,0
ds_read_b64_tr_b16 a[244:245],v2 offset:8960
v_mfma_f32_16x16x32_bf16 v[104:107],a[168:171],v[212:215],v[104:107]
s_and_b32 s69,s75,0xfffc0000
ds_read_b64_tr_b16 a[246:247],v2 offset:13056
ds_read_b64_tr_b16 a[248:249],v3 offset:768
v_mfma_f32_16x16x32_bf16 v[104:107],a[172:175],v[220:223],v[104:107]
s_add_u32 m0,s76,0
ds_read_b64_tr_b16 a[250:251],v3 offset:4864
ds_read_b64_tr_b16 a[252:253],v3 offset:8960
v_mfma_f32_16x16x32_bf16 v[108:111],a[188:191],v[220:223],v[40:43]
s_add_u32 s75,s75,16384
ds_read_b64_tr_b16 a[254:255],v3 offset:13056
v_add_u32 v2,s78,v2
v_mfma_f32_16x16x32_bf16 v[108:111],a[184:187],v[212:215],v[108:111]
global_load_lds_dwordx4 v4,s[10:11] nt
v_add_u32 v3,s78,v3
v_mfma_f32_16x16x32_bf16 v[108:111],a[180:183],v[204:207],v[108:111]
s_sub_u32 s78,s77,32768
v_exp_f32 v80,v80
v_mfma_f32_16x16x32_bf16 v[108:111],a[176:179],v[196:199],v[108:111]
s_and_b32 s78,s78,0x1ffff
v_exp_f32 v81,v81
s_waitcnt lgkmcnt(7)
v_mfma_f32_16x16x32_bf16 a[0:3],a[192:195],v[64:67],a[0:3]
s_sub_u32 s78,s78,s77
v_exp_f32 v82,v82
v_mfma_f32_16x16x32_bf16 a[0:3],a[196:199],v[72:75],a[0:3]
s_add_u32 s77,s77,s78
v_exp_f32 v83,v83
v_mfma_f32_16x16x32_bf16 a[4:7],a[200:203],v[64:67],a[4:7]
v_exp_f32 v84,v84
v_mfma_f32_16x16x32_bf16 a[4:7],a[204:207],v[72:75],a[4:7]
v_exp_f32 v85,v85
v_mfma_f32_16x16x32_bf16 a[8:11],a[208:211],v[64:67],a[8:11]
v_exp_f32 v86,v86
v_mfma_f32_16x16x32_bf16 a[8:11],a[212:215],v[72:75],a[8:11]
v_exp_f32 v87,v87
v_mfma_f32_16x16x32_bf16 a[12:15],a[216:219],v[64:67],a[12:15]
v_cvt_pk_bf16_f32 v80,v80,v81
v_cvt_pk_bf16_f32 v81,v82,v83
v_mfma_f32_16x16x32_bf16 a[12:15],a[220:223],v[72:75],a[12:15]
s_nop 0
L_pd5_%=:
s_cbranch_execz L_trt0_%=
v_cvt_pk_bf16_f32 v82,v84,v85
v_cvt_pk_bf16_f32 v83,v86,v87
v_mfma_f32_16x16x32_bf16 a[16:19],a[224:227],v[64:67],a[16:19]
v_exp_f32 v88,v88
v_mfma_f32_16x16x32_bf16 a[16:19],a[228:231],v[72:75],a[16:19]
v_exp_f32 v89,v89
v_mfma_f32_16x16x32_bf16 a[20:23],a[232:235],v[64:67],a[20:23]
v_exp_f32 v90,v90
v_mfma_f32_16x16x32_bf16 a[20:23],a[236:239],v[72:75],a[20:23]
s_waitcnt lgkmcnt(0)
v_exp_f32 v91,v91
v_mfma_f32_16x16x32_bf16 a[24:27],a[240:243],v[64:67],a[24:27]
v_exp_f32 v92,v92
v_mfma_f32_16x16x32_bf16 a[24:27],a[244:247],v[72:75],a[24:27]
v_exp_f32 v93,v93
v_mfma_f32_16x16x32_bf16 a[28:31],a[248:251],v[64:67],a[28:31]
v_exp_f32 v94,v94
v_mfma_f32_16x16x32_bf16 a[28:31],a[252:255],v[72:75],a[28:31]
s_getpc_b64 s[92:93]
s_cmp_lg_u32 s4,0
s_cbranch_scc1 L_pace_%=
v_mfma_f32_16x16x32_bf16 v[48:51],v[28:31],v[64:67],v[48:51]
v_exp_f32 v95,v95
v_mfma_f32_16x16x32_bf16 v[48:51],v[28:31],v[72:75],v[48:51]
s_add_u32 s10,s20,s74
s_addc_u32 s11,s21,0
v_cvt_pk_bf16_f32 v88,v88,v89
s_waitcnt vmcnt(13)
s_barrier
v_mfma_f32_16x16x32_bf16 v[112:115],a[128:131],v[228:231],v[44:47]
s_add_u32 m0,s76,0
v_cvt_pk_bf16_f32 v89,v90,v91
v_cvt_pk_bf16_f32 v90,v92,v93
v_mfma_f32_16x16x32_bf16 v[112:115],a[132:135],v[236:239],v[112:115]
ds_read_b128 a[128:131],v1
v_cvt_pk_bf16_f32 v91,v94,v95
v_mfma_f32_16x16x32_bf16 v[112:115],a[136:139],v[244:247],v[112:115]
global_load_lds_dwordx4 v4,s[10:11] offset:1024 nt
ds_read_b128 a[132:135],v1 offset:256
v_mfma_f32_16x16x32_bf16 v[112:115],a[140:143],v[252:255],v[112:115]
v_exp_f32 v96,v96
v_mfma_f32_16x16x32_bf16 v[116:119],a[156:159],v[252:255],v[44:47]
v_exp_f32 v97,v97
v_mfma_f32_16x16x32_bf16 v[116:119],a[152:155],v[244:247],v[116:119]
ds_read_b128 a[136:139],v1 offset:512
v_cvt_pk_bf16_f32 v96,v96,v97
v_mfma_f32_16x16x32_bf16 v[116:119],a[148:151],v[236:239],v[116:119]
v_exp_f32 v98,v98
v_mfma_f32_16x16x32_bf16 v[116:119],a[144:147],v[228:231],v[116:119]
v_exp_f32 v99,v99
v_mfma_f32_16x16x32_bf16 v[120:123],a[160:163],v[228:231],v[44:47]
ds_read_b128 a[140:143],v1 offset:768
v_cvt_pk_bf16_f32 v97,v98,v99
v_mfma_f32_16x16x32_bf16 v[120:123],a[164:167],v[236:239],v[120:123]
v_exp_f32 v100,v100
v_mfma_f32_16x16x32_bf16 v[120:123],a[168:171],v[244:247],v[120:123]
v_exp_f32 v101,v101
v_mfma_f32_16x16x32_bf16 v[120:123],a[172:175],v[252:255],v[120:123]
ds_read_b128 a[156:159],v1 offset:4864
v_cvt_pk_bf16_f32 v98,v100,v101
v_mfma_f32_16x16x32_bf16 v[124:127],a[188:191],v[252:255],v[44:47]
v_exp_f32 v102,v102
v_mfma_f32_16x16x32_bf16 v[124:127],a[184:187],v[244:247],v[124:127]
v_exp_f32 v103,v103
v_mfma_f32_16x16x32_bf16 v[124:127],a[180:183],v[236:239],v[124:127]
ds_read_b128 a[152:155],v1 offset:4608
v_cvt_pk_bf16_f32 v99,v102,v103
v_mfma_f32_16x16x32_bf16 v[124:127],a[176:179],v[228:231],v[124:127]
v_exp_f32 v104,v104
v_mfma_f32_16x16x32_bf16 a[32:35],a[192:195],v[80:83],a[32:35]
global_load_lds_dwordx4 v5,s[10:11] offset:2048 nt
ds_read_b128 a[148:151],v1 offset:4352
v_mfma_f32_16x16x32_bf16 a[32:35],a[196:199],v[88:91],a[32:35]
v_exp_f32 v105,v105
v_mfma_f32_16x16x32_bf16 a[36:39],a[200:203],v[80:83],a[36:39]
ds_read_b128 a[144:147],v1 offset:4096
v_cvt_pk_bf16_f32 v104,v104,v105
v_mfma_f32_16x16x32_bf16 a[36:39],a[204:207],v[88:91],a[36:39]
v_exp_f32 v106,v106
v_mfma_f32_16x16x32_bf16 a[40:43],a[208:211],v[80:83],a[40:43]
v_exp_f32 v107,v107
v_mfma_f32_16x16x32_bf16 a[40:43],a[212:215],v[88:91],a[40:43]
ds_read_b128 a[160:163],v1 offset:8192
v_cvt_pk_bf16_f32 v105,v106,v107
v_mfma_f32_16x16x32_bf16 a[44:47],a[216:219],v[80:83],a[44:47]
v_exp_f32 v108,v108
v_mfma_f32_16x16x32_bf16 a[44:47],a[220:223],v[88:91],a[44:47]
v_exp_f32 v109,v109
v_mfma_f32_16x16x32_bf16 a[48:51],a[224:227],v[80:83],a[48:51]
ds_read_b128 a[164:167],v1 offset:8448
v_cvt_pk_bf16_f32 v106,v108,v109
v_mfma_f32_16x16x32_bf16 a[48:51],a[228:231],v[88:91],a[48:51]
v_exp_f32 v110,v110
v_mfma_f32_16x16x32_bf16 a[52:55],a[232:235],v[80:83],a[52:55]
v_exp_f32 v111,v111
v_mfma_f32_16x16x32_bf16 a[52:55],a[236:239],v[88:91],a[52:55]
ds_read_b128 a[168:171],v1 offset:8704
v_cvt_pk_bf16_f32 v107,v110,v111
v_mfma_f32_16x16x32_bf16 a[56:59],a[240:243],v[80:83],a[56:59]
v_exp_f32 v112,v112
v_mfma_f32_16x16x32_bf16 a[56:59],a[244:247],v[88:91],a[56:59]
v_exp_f32 v113,v113
v_mfma_f32_16x16x32_bf16 a[60:63],a[248:251],v[80:83],a[60:63]
global_load_lds_dwordx4 v5,s[10:11] offset:3072
ds_read_b128 a[172:175],v1 offset:8960
v_mfma_f32_16x16x32_bf16 a[60:63],a[252:255],v[88:91],a[60:63]
ds_read_b128 a[188:191],v1 offset:13056
v_cvt_pk_bf16_f32 v112,v112,v113
v_mfma_f32_16x16x32_bf16 v[52:55],v[28:31],v[80:83],v[52:55]
v_exp_f32 v114,v114
v_mfma_f32_16x16x32_bf16 v[52:55],v[28:31],v[88:91],v[52:55]
s_waitcnt lgkmcnt(3)
v_exp_f32 v115,v115
v_mfma_f32_16x16x32_bf16 v[64:67],a[128:131],v[132:135],v[32:35]
ds_read_b128 a[184:187],v1 offset:12800
v_cvt_pk_bf16_f32 v113,v114,v115
v_mfma_f32_16x16x32_bf16 v[64:67],a[132:135],v[140:143],v[64:67]
v_exp_f32 v116,v116
v_mfma_f32_16x16x32_bf16 v[64:67],a[136:139],v[148:151],v[64:67]
ds_read_b128 a[180:183],v1 offset:12544
v_mfma_f32_16x16x32_bf16 v[64:67],a[140:143],v[156:159],v[64:67]
ds_read_b128 a[176:179],v1 offset:12288
v_add_u32 v1,s78,v1
v_mfma_f32_16x16x32_bf16 v[68:71],a[156:159],v[156:159],v[32:35]
v_exp_f32 v117,v117
v_mfma_f32_16x16x32_bf16 v[68:71],a[152:155],v[148:151],v[68:71]
v_exp_f32 v118,v118
v_mfma_f32_16x16x32_bf16 v[68:71],a[148:151],v[140:143],v[68:71]
s_add_u32 s10,s24,s74
s_addc_u32 s11,s25,0
v_cvt_pk_bf16_f32 v114,v116,v117
v_mfma_f32_16x16x32_bf16 v[68:71],a[144:147],v[132:135],v[68:71]
s_add_u32 s74,s74,16384
v_exp_f32 v119,v119
v_mfma_f32_16x16x32_bf16 v[72:75],a[160:163],v[132:135],v[32:35]
s_add_u32 m0,s76,16384
v_exp_f32 v120,v120
v_mfma_f32_16x16x32_bf16 v[72:75],a[164:167],v[140:143],v[72:75]
s_and_b32 s74,s74,0x3ffff
v_exp_f32 v121,v121
s_waitcnt lgkmcnt(2)
v_mfma_f32_16x16x32_bf16 v[72:75],a[168:171],v[148:151],v[72:75]
global_load_lds_dwordx4 v4,s[10:11]
v_cvt_pk_bf16_f32 v115,v118,v119
v_mfma_f32_16x16x32_bf16 v[72:75],a[172:175],v[156:159],v[72:75]
s_or_b32 s74,s74,s69
v_exp_f32 v122,v122
v_mfma_f32_16x16x32_bf16 v[76:79],a[188:191],v[156:159],v[32:35]
s_sub_u32 s76,s76,32768
v_exp_f32 v123,v123
v_mfma_f32_16x16x32_bf16 v[76:79],a[184:187],v[148:151],v[76:79]
s_and_b32 s76,s76,0x1ffff
v_exp_f32 v124,v124
s_waitcnt lgkmcnt(0)
v_mfma_f32_16x16x32_bf16 v[76:79],a[180:183],v[140:143],v[76:79]
s_and_b32 s69,s75,0xfffc0000
v_exp_f32 v125,v125
v_mfma_f32_16x16x32_bf16 v[76:79],a[176:179],v[132:135],v[76:79]
v_exp_f32 v126,v126
v_mfma_f32_16x16x32_bf16 a[64:67],a[192:195],v[96:99],a[64:67]
v_exp_f32 v127,v127
v_mfma_f32_16x16x32_bf16 a[64:67],a[196:199],v[104:107],a[64:67]
v_cvt_pk_bf16_f32 v120,v120,v121
v_cvt_pk_bf16_f32 v121,v122,v123
v_mfma_f32_16x16x32_bf16 a[68:71],a[200:203],v[96:99],a[68:71]
v_cvt_pk_bf16_f32 v122,v124,v125
v_cvt_pk_bf16_f32 v123,v126,v127
v_mfma_f32_16x16x32_bf16 a[68:71],a[204:207],v[104:107],a[68:71]
v_exp_f32 v64,v64
v_mfma_f32_16x16x32_bf16 a[72:75],a[208:211],v[96:99],a[72:75]
v_exp_f32 v65,v65
v_mfma_f32_16x16x32_bf16 a[72:75],a[212:215],v[104:107],a[72:75]
v_exp_f32 v66,v66
v_mfma_f32_16x16x32_bf16 a[76:79],a[216:219],v[96:99],a[76:79]
v_exp_f32 v67,v67
v_mfma_f32_16x16x32_bf16 a[76:79],a[220:223],v[104:107],a[76:79]
v_exp_f32 v68,v68
v_mfma_f32_16x16x32_bf16 a[80:83],a[224:227],v[96:99],a[80:83]
global_load_lds_dwordx4 v5,s[10:11] offset:1024
v_cvt_pk_bf16_f32 v64,v64,v65
v_mfma_f32_16x16x32_bf16 a[80:83],a[228:231],v[104:107],a[80:83]
)A"
R"A(v_exp_f32 v69,v69
v_mfma_f32_16x16x32_bf16 a[84:87],a[232:235],v[96:99],a[84:87]
v_exp_f32 v70,v70
v_mfma_f32_16x16x32_bf16 a[84:87],a[236:239],v[104:107],a[84:87]
v_exp_f32 v71,v71
v_mfma_f32_16x16x32_bf16 a[88:91],a[240:243],v[96:99],a[88:91]
v_cvt_pk_bf16_f32 v65,v66,v67
v_cvt_pk_bf16_f32 v66,v68,v69
v_mfma_f32_16x16x32_bf16 a[88:91],a[244:247],v[104:107],a[88:91]
v_exp_f32 v72,v72
v_mfma_f32_16x16x32_bf16 a[92:95],a[248:251],v[96:99],a[92:95]
v_exp_f32 v73,v73
v_mfma_f32_16x16x32_bf16 a[92:95],a[252:255],v[104:107],a[92:95]
v_cvt_pk_bf16_f32 v67,v70,v71
v_cvt_pk_bf16_f32 v72,v72,v73
v_mfma_f32_16x16x32_bf16 v[56:59],v[28:31],v[96:99],v[56:59]
v_exp_f32 v74,v74
v_mfma_f32_16x16x32_bf16 v[56:59],v[28:31],v[104:107],v[56:59]
v_exp_f32 v75,v75
v_mfma_f32_16x16x32_bf16 v[80:83],a[128:131],v[164:167],v[36:39]
v_exp_f32 v76,v76
v_mfma_f32_16x16x32_bf16 v[80:83],a[132:135],v[172:175],v[80:83]
v_exp_f32 v77,v77
v_mfma_f32_16x16x32_bf16 v[80:83],a[136:139],v[180:183],v[80:83]
v_exp_f32 v78,v78
v_mfma_f32_16x16x32_bf16 v[80:83],a[140:143],v[188:191],v[80:83]
v_exp_f32 v79,v79
v_mfma_f32_16x16x32_bf16 v[84:87],a[156:159],v[188:191],v[36:39]
v_cvt_pk_bf16_f32 v73,v74,v75
v_cvt_pk_bf16_f32 v74,v76,v77
v_mfma_f32_16x16x32_bf16 v[84:87],a[152:155],v[180:183],v[84:87]
global_load_lds_dwordx4 v4,s[10:11] offset:2048
v_cvt_pk_bf16_f32 v75,v78,v79
v_mfma_f32_16x16x32_bf16 v[84:87],a[148:151],v[172:175],v[84:87]
v_exp_f32 v80,v80
v_mfma_f32_16x16x32_bf16 v[84:87],a[144:147],v[164:167],v[84:87]
v_exp_f32 v81,v81
v_mfma_f32_16x16x32_bf16 v[88:91],a[160:163],v[164:167],v[36:39]
v_exp_f32 v82,v82
v_mfma_f32_16x16x32_bf16 v[88:91],a[164:167],v[172:175],v[88:91]
v_exp_f32 v83,v83
v_mfma_f32_16x16x32_bf16 v[88:91],a[168:171],v[180:183],v[88:91]
v_cvt_pk_bf16_f32 v80,v80,v81
v_cvt_pk_bf16_f32 v81,v82,v83
v_mfma_f32_16x16x32_bf16 v[88:91],a[172:175],v[188:191],v[88:91]
v_exp_f32 v84,v84
v_mfma_f32_16x16x32_bf16 v[92:95],a[188:191],v[188:191],v[36:39]
v_exp_f32 v85,v85
v_mfma_f32_16x16x32_bf16 v[92:95],a[184:187],v[180:183],v[92:95]
v_exp_f32 v86,v86
v_mfma_f32_16x16x32_bf16 v[92:95],a[180:183],v[172:175],v[92:95]
v_exp_f32 v87,v87
v_mfma_f32_16x16x32_bf16 v[92:95],a[176:179],v[164:167],v[92:95]
v_cvt_pk_bf16_f32 v82,v84,v85
v_cvt_pk_bf16_f32 v83,v86,v87
v_mfma_f32_16x16x32_bf16 a[96:99],a[192:195],v[112:115],a[96:99]
ds_read_b64_tr_b16 a[192:193],v2
ds_read_b64_tr_b16 a[194:195],v2 offset:4096
v_mfma_f32_16x16x32_bf16 a[96:99],a[196:199],v[120:123],a[96:99]
ds_read_b64_tr_b16 a[196:197],v2 offset:8192
ds_read_b64_tr_b16 a[198:199],v2 offset:12288
v_mfma_f32_16x16x32_bf16 a[100:103],a[200:203],v[112:115],a[100:103]
ds_read_b64_tr_b16 a[200:201],v3
ds_read_b64_tr_b16 a[202:203],v3 offset:4096
v_mfma_f32_16x16x32_bf16 a[100:103],a[204:207],v[120:123],a[100:103]
global_load_lds_dwordx4 v5,s[10:11] offset:3072
ds_read_b64_tr_b16 a[204:205],v3 offset:8192
v_mfma_f32_16x16x32_bf16 a[104:107],a[208:211],v[112:115],a[104:107]
ds_read_b64_tr_b16 a[206:207],v3 offset:12288
ds_read_b64_tr_b16 a[208:209],v2 offset:256
v_mfma_f32_16x16x32_bf16 a[104:107],a[212:215],v[120:123],a[104:107]
ds_read_b64_tr_b16 a[210:211],v2 offset:4352
ds_read_b64_tr_b16 a[212:213],v2 offset:8448
v_mfma_f32_16x16x32_bf16 a[108:111],a[216:219],v[112:115],a[108:111]
v_exp_f32 v88,v88
v_mfma_f32_16x16x32_bf16 a[108:111],a[220:223],v[120:123],a[108:111]
ds_read_b64_tr_b16 a[214:215],v2 offset:12544
ds_read_b64_tr_b16 a[216:217],v3 offset:256
v_mfma_f32_16x16x32_bf16 a[112:115],a[224:227],v[112:115],a[112:115]
ds_read_b64_tr_b16 a[218:219],v3 offset:4352
ds_read_b64_tr_b16 a[220:221],v3 offset:8448
v_mfma_f32_16x16x32_bf16 a[112:115],a[228:231],v[120:123],a[112:115]
ds_read_b64_tr_b16 a[222:223],v3 offset:12544
ds_read_b64_tr_b16 a[224:225],v2 offset:512
v_mfma_f32_16x16x32_bf16 a[116:119],a[232:235],v[112:115],a[116:119]
v_exp_f32 v89,v89
v_mfma_f32_16x16x32_bf16 a[116:119],a[236:239],v[120:123],a[116:119]
ds_read_b64_tr_b16 a[226:227],v2 offset:4608
ds_read_b64_tr_b16 a[228:229],v2 offset:8704
v_mfma_f32_16x16x32_bf16 a[120:123],a[240:243],v[112:115],a[120:123]
ds_read_b64_tr_b16 a[230:231],v2 offset:12800
ds_read_b64_tr_b16 a[232:233],v3 offset:512
v_mfma_f32_16x16x32_bf16 a[120:123],a[244:247],v[120:123],a[120:123]
s_add_u32 s10,s20,s74
s_addc_u32 s11,s21,0
ds_read_b64_tr_b16 a[234:235],v3 offset:4608
v_mfma_f32_16x16x32_bf16 a[124:127],a[248:251],v[112:115],a[124:127]
ds_read_b64_tr_b16 a[236:237],v3 offset:8704
ds_read_b64_tr_b16 a[238:239],v3 offset:12800
v_mfma_f32_16x16x32_bf16 a[124:127],a[252:255],v[120:123],a[124:127]
s_add_u32 m0,s76,0
ds_read_b64_tr_b16 a[240:241],v2 offset:768
ds_read_b64_tr_b16 a[242:243],v2 offset:4864
v_mfma_f32_16x16x32_bf16 v[60:63],v[28:31],v[112:115],v[60:63]
ds_read_b64_tr_b16 a[244:245],v2 offset:8960
v_cvt_pk_bf16_f32 v88,v88,v89
v_mfma_f32_16x16x32_bf16 v[60:63],v[28:31],v[120:123],v[60:63]
global_load_lds_dwordx4 v4,s[10:11] nt
ds_read_b64_tr_b16 a[246:247],v2 offset:13056
v_mfma_f32_16x16x32_bf16 v[96:99],a[128:131],v[196:199],v[40:43]
ds_read_b64_tr_b16 a[248:249],v3 offset:768
ds_read_b64_tr_b16 a[250:251],v3 offset:4864
v_mfma_f32_16x16x32_bf16 v[96:99],a[132:135],v[204:207],v[96:99]
ds_read_b64_tr_b16 a[252:253],v3 offset:8960
ds_read_b64_tr_b16 a[254:255],v3 offset:13056
v_mfma_f32_16x16x32_bf16 v[96:99],a[136:139],v[212:215],v[96:99]
v_exp_f32 v90,v90
v_mfma_f32_16x16x32_bf16 v[96:99],a[140:143],v[220:223],v[96:99]
v_exp_f32 v91,v91
v_mfma_f32_16x16x32_bf16 v[100:103],a[156:159],v[220:223],v[40:43]
v_exp_f32 v92,v92
v_mfma_f32_16x16x32_bf16 v[100:103],a[152:155],v[212:215],v[100:103]
v_exp_f32 v93,v93
v_mfma_f32_16x16x32_bf16 v[100:103],a[148:151],v[204:207],v[100:103]
v_exp_f32 v94,v94
v_mfma_f32_16x16x32_bf16 v[100:103],a[144:147],v[196:199],v[100:103]
v_exp_f32 v95,v95
v_mfma_f32_16x16x32_bf16 v[104:107],a[160:163],v[196:199],v[40:43]
v_cvt_pk_bf16_f32 v89,v90,v91
v_cvt_pk_bf16_f32 v90,v92,v93
v_mfma_f32_16x16x32_bf16 v[104:107],a[164:167],v[204:207],v[104:107]
v_cvt_pk_bf16_f32 v91,v94,v95
v_add_u32 v2,s78,v2
v_mfma_f32_16x16x32_bf16 v[104:107],a[168:171],v[212:215],v[104:107]
v_exp_f32 v96,v96
v_mfma_f32_16x16x32_bf16 v[104:107],a[172:175],v[220:223],v[104:107]
v_exp_f32 v97,v97
v_mfma_f32_16x16x32_bf16 v[108:111],a[188:191],v[220:223],v[40:43]
v_exp_f32 v98,v98
v_mfma_f32_16x16x32_bf16 v[108:111],a[184:187],v[212:215],v[108:111]
global_load_lds_dwordx4 v4,s[10:11] offset:1024 nt
v_cvt_pk_bf16_f32 v96,v96,v97
v_mfma_f32_16x16x32_bf16 v[108:111],a[180:183],v[204:207],v[108:111]
v_exp_f32 v99,v99
v_mfma_f32_16x16x32_bf16 v[108:111],a[176:179],v[196:199],v[108:111]
s_waitcnt lgkmcnt(0)
v_exp_f32 v100,v100
v_mfma_f32_16x16x32_bf16 a[0:3],a[192:195],v[64:67],a[0:3]
v_exp_f32 v101,v101
v_mfma_f32_16x16x32_bf16 a[0:3],a[196:199],v[72:75],a[0:3]
v_exp_f32 v102,v102
v_mfma_f32_16x16x32_bf16 a[4:7],a[200:203],v[64:67],a[4:7]
v_exp_f32 v103,v103
v_mfma_f32_16x16x32_bf16 a[4:7],a[204:207],v[72:75],a[4:7]
v_cvt_pk_bf16_f32 v97,v98,v99
v_cvt_pk_bf16_f32 v98,v100,v101
v_mfma_f32_16x16x32_bf16 a[8:11],a[208:211],v[64:67],a[8:11]
v_cvt_pk_bf16_f32 v99,v102,v103
v_add_u32 v3,s78,v3
v_mfma_f32_16x16x32_bf16 a[8:11],a[212:215],v[72:75],a[8:11]
s_sub_u32 s78,s77,32768
v_exp_f32 v104,v104
v_mfma_f32_16x16x32_bf16 a[12:15],a[216:219],v[64:67],a[12:15]
s_and_b32 s78,s78,0x1ffff
v_exp_f32 v105,v105
v_mfma_f32_16x16x32_bf16 a[12:15],a[220:223],v[72:75],a[12:15]
s_sub_u32 s78,s78,s77
v_exp_f32 v106,v106
v_mfma_f32_16x16x32_bf16 a[16:19],a[224:227],v[64:67],a[16:19]
s_add_u32 s77,s77,s78
v_exp_f32 v107,v107
v_mfma_f32_16x16x32_bf16 a[16:19],a[228:231],v[72:75],a[16:19]
v_exp_f32 v108,v108
v_mfma_f32_16x16x32_bf16 a[20:23],a[232:235],v[64:67],a[20:23]
s_getreg_b32 s92,hwreg(HW_REG_IB_STS,0,4)
s_min_u32 s4,s92,s6
s_mov_b32 s6,s92
v_mfma_f32_16x16x32_bf16 a[20:23],a[236:239],v[72:75],a[20:23]
s_cmp_gt_u32 s4,s9
s_cselect_b32 s4,15,s5
v_cvt_pk_bf16_f32 v104,v104,v105
v_mfma_f32_16x16x32_bf16 a[24:27],a[240:243],v[64:67],a[24:27]
v_exp_f32 v109,v109
v_mfma_f32_16x16x32_bf16 a[24:27],a[244:247],v[72:75],a[24:27]
v_exp_f32 v110,v110
v_mfma_f32_16x16x32_bf16 a[28:31],a[248:251],v[64:67],a[28:31]
v_exp_f32 v111,v111
v_mfma_f32_16x16x32_bf16 a[28:31],a[252:255],v[72:75],a[28:31]
s_getpc_b64 s[92:93]
s_cmp_lg_u32 s4,0
s_cbranch_scc1 L_pace_%=
v_mfma_f32_16x16x32_bf16 v[48:51],v[28:31],v[64:67],v[48:51]
v_cvt_pk_bf16_f32 v105,v106,v107
v_cvt_pk_bf16_f32 v106,v108,v109
v_mfma_f32_16x16x32_bf16 v[48:51],v[28:31],v[72:75],v[48:51]
s_add_u32 s10,s20,s74
s_addc_u32 s11,s21,0
v_cvt_pk_bf16_f32 v107,v110,v111
s_waitcnt vmcnt(10)
s_barrier
v_mfma_f32_16x16x32_bf16 v[24:27],v[8:11],v[128:131],0
s_add_u32 m0,s76,0
v_mfma_f32_16x16x32_bf16 v[24:27],v[12:15],v[136:139],v[24:27]
v_mfma_f32_16x16x32_bf16 v[24:27],v[16:19],v[144:147],v[24:27]
global_load_lds_dwordx4 v5,s[10:11] offset:2048 nt
v_mfma_f32_16x16x32_bf16 v[24:27],v[20:23],v[152:155],v[24:27]
v_mfma_f32_16x16x32_bf16 v[112:115],a[128:131],v[228:231],v[44:47]
ds_read_b128 a[128:131],v1
v_mfma_f32_16x16x32_bf16 v[112:115],a[132:135],v[236:239],v[112:115]
ds_read_b128 a[132:135],v1 offset:256
v_mfma_f32_16x16x32_bf16 v[112:115],a[136:139],v[244:247],v[112:115]
ds_read_b128 a[136:139],v1 offset:512
v_mfma_f32_16x16x32_bf16 v[112:115],a[140:143],v[252:255],v[112:115]
ds_read_b128 a[140:143],v1 offset:768
v_mfma_f32_16x16x32_bf16 v[116:119],a[156:159],v[252:255],v[44:47]
v_max3_f32 v24,v24,v25,v26
v_max_f32 v24,v24,v27
v_mfma_f32_16x16x32_bf16 v[116:119],a[152:155],v[244:247],v[116:119]
ds_read_b128 a[156:159],v1 offset:4864
v_mov_b32 v25,v24
v_mfma_f32_16x16x32_bf16 v[116:119],a[148:151],v[236:239],v[116:119]
v_exp_f32 v112,v112
v_mfma_f32_16x16x32_bf16 v[116:119],a[144:147],v[228:231],v[116:119]
v_permlane32_swap_b32 v24,v25
v_mfma_f32_16x16x32_bf16 v[120:123],a[160:163],v[228:231],v[44:47]
v_max_f32 v24,v24,v25
v_mov_b32 v25,v24
v_mfma_f32_16x16x32_bf16 v[120:123],a[164:167],v[236:239],v[120:123]
v_exp_f32 v113,v113
v_mfma_f32_16x16x32_bf16 v[120:123],a[168:171],v[244:247],v[120:123]
v_permlane16_swap_b32 v24,v25
v_mfma_f32_16x16x32_bf16 v[120:123],a[172:175],v[252:255],v[120:123]
v_max_f32 v24,v24,v25
v_sub_f32 v27,0,v24
v_mfma_f32_16x16x32_bf16 v[124:127],a[188:191],v[252:255],v[44:47]
global_load_lds_dwordx4 v5,s[10:11] offset:3072
v_sub_f32 v26,0,v24
v_sub_f32 v25,0,v24
v_sub_f32 v24,0,v24
v_mfma_f32_16x16x32_bf16 v[124:127],a[184:187],v[244:247],v[124:127]
ds_write_b128 v7,v[24:27]
v_mfma_f32_16x16x32_bf16 v[124:127],a[180:183],v[236:239],v[124:127]
ds_read_b128 a[152:155],v1 offset:4608
v_cvt_pk_bf16_f32 v112,v112,v113
v_mfma_f32_16x16x32_bf16 v[124:127],a[176:179],v[228:231],v[124:127]
v_exp_f32 v114,v114
v_mfma_f32_16x16x32_bf16 v[24:27],v[8:11],v[160:163],0
v_exp_f32 v115,v115
v_mfma_f32_16x16x32_bf16 v[24:27],v[12:15],v[168:171],v[24:27]
ds_read_b128 a[148:151],v1 offset:4352
v_cvt_pk_bf16_f32 v113,v114,v115
v_mfma_f32_16x16x32_bf16 v[24:27],v[16:19],v[176:179],v[24:27]
v_exp_f32 v116,v116
v_mfma_f32_16x16x32_bf16 v[24:27],v[20:23],v[184:187],v[24:27]
v_exp_f32 v117,v117
v_mfma_f32_16x16x32_bf16 a[32:35],a[192:195],v[80:83],a[32:35]
ds_read_b128 a[144:147],v1 offset:4096
v_cvt_pk_bf16_f32 v114,v116,v117
v_mfma_f32_16x16x32_bf16 a[32:35],a[196:199],v[88:91],a[32:35]
v_exp_f32 v118,v118
v_mfma_f32_16x16x32_bf16 a[36:39],a[200:203],v[80:83],a[36:39]
s_add_u32 s10,s24,s74
s_addc_u32 s11,s25,0
ds_read_b128 a[160:163],v1 offset:8192
v_mfma_f32_16x16x32_bf16 a[36:39],a[204:207],v[88:91],a[36:39]
s_add_u32 s74,s74,16384
v_max3_f32 v24,v24,v25,v26
v_max_f32 v24,v24,v27
v_mfma_f32_16x16x32_bf16 a[40:43],a[208:211],v[80:83],a[40:43]
s_add_u32 m0,s76,16384
ds_read_b128 a[164:167],v1 offset:8448
v_mov_b32 v25,v24
v_mfma_f32_16x16x32_bf16 a[40:43],a[212:215],v[88:91],a[40:43]
s_and_b32 s74,s74,0x3ffff
v_exp_f32 v119,v119
v_mfma_f32_16x16x32_bf16 a[44:47],a[216:219],v[80:83],a[44:47]
global_load_lds_dwordx4 v4,s[10:11]
ds_read_b128 a[168:171],v1 offset:8704
v_mfma_f32_16x16x32_bf16 a[44:47],a[220:223],v[88:91],a[44:47]
s_or_b32 s74,s74,s69
v_permlane32_swap_b32 v24,v25
v_mfma_f32_16x16x32_bf16 a[48:51],a[224:227],v[80:83],a[48:51]
)A"
R"A(s_sub_u32 s76,s76,32768
v_max_f32 v24,v24,v25
v_mov_b32 v25,v24
v_mfma_f32_16x16x32_bf16 a[48:51],a[228:231],v[88:91],a[48:51]
s_and_b32 s76,s76,0x1ffff
ds_read_b128 a[172:175],v1 offset:8960
v_cvt_pk_bf16_f32 v115,v118,v119
v_mfma_f32_16x16x32_bf16 a[52:55],a[232:235],v[80:83],a[52:55]
v_permlane16_swap_b32 v24,v25
v_mfma_f32_16x16x32_bf16 a[52:55],a[236:239],v[88:91],a[52:55]
v_max_f32 v24,v24,v25
v_sub_f32 v27,0,v24
v_mfma_f32_16x16x32_bf16 a[56:59],a[240:243],v[80:83],a[56:59]
v_sub_f32 v26,0,v24
v_sub_f32 v25,0,v24
v_mfma_f32_16x16x32_bf16 a[56:59],a[244:247],v[88:91],a[56:59]
ds_read_b128 a[188:191],v1 offset:13056
v_sub_f32 v24,0,v24
v_mfma_f32_16x16x32_bf16 a[60:63],a[248:251],v[80:83],a[60:63]
ds_write_b128 v7,v[24:27] offset:1024
v_mfma_f32_16x16x32_bf16 a[60:63],a[252:255],v[88:91],a[60:63]
v_exp_f32 v120,v120
v_mfma_f32_16x16x32_bf16 v[52:55],v[28:31],v[80:83],v[52:55]
v_exp_f32 v121,v121
v_mfma_f32_16x16x32_bf16 v[52:55],v[28:31],v[88:91],v[52:55]
ds_read_b128 a[184:187],v1 offset:12800
v_cvt_pk_bf16_f32 v120,v120,v121
v_mfma_f32_16x16x32_bf16 v[24:27],v[8:11],v[192:195],0
v_exp_f32 v122,v122
v_mfma_f32_16x16x32_bf16 v[24:27],v[12:15],v[200:203],v[24:27]
v_exp_f32 v123,v123
v_mfma_f32_16x16x32_bf16 v[24:27],v[16:19],v[208:211],v[24:27]
s_waitcnt lgkmcnt(2)
ds_read_b128 a[180:183],v1 offset:12544
v_cvt_pk_bf16_f32 v121,v122,v123
v_mfma_f32_16x16x32_bf16 v[24:27],v[20:23],v[216:219],v[24:27]
global_load_lds_dwordx4 v5,s[10:11] offset:1024
ds_read_b128 a[176:179],v1 offset:12288
v_mfma_f32_16x16x32_bf16 v[64:67],a[128:131],v[132:135],v[32:35]
v_exp_f32 v124,v124
v_mfma_f32_16x16x32_bf16 v[64:67],a[132:135],v[140:143],v[64:67]
v_exp_f32 v125,v125
v_mfma_f32_16x16x32_bf16 v[64:67],a[136:139],v[148:151],v[64:67]
v_exp_f32 v126,v126
v_mfma_f32_16x16x32_bf16 v[64:67],a[140:143],v[156:159],v[64:67]
v_max3_f32 v24,v24,v25,v26
v_max_f32 v24,v24,v27
v_mfma_f32_16x16x32_bf16 v[68:71],a[156:159],v[156:159],v[32:35]
v_mov_b32 v25,v24
v_cvt_pk_bf16_f32 v122,v124,v125
v_mfma_f32_16x16x32_bf16 v[68:71],a[152:155],v[148:151],v[68:71]
v_exp_f32 v127,v127
v_mfma_f32_16x16x32_bf16 v[68:71],a[148:151],v[140:143],v[68:71]
v_permlane32_swap_b32 v24,v25
v_mfma_f32_16x16x32_bf16 v[68:71],a[144:147],v[132:135],v[68:71]
v_max_f32 v24,v24,v25
v_mov_b32 v25,v24
v_mfma_f32_16x16x32_bf16 v[72:75],a[160:163],v[132:135],v[32:35]
v_cvt_pk_bf16_f32 v123,v126,v127
v_add_u32 v1,s78,v1
v_mfma_f32_16x16x32_bf16 v[72:75],a[164:167],v[140:143],v[72:75]
v_permlane16_swap_b32 v24,v25
v_mfma_f32_16x16x32_bf16 v[72:75],a[168:171],v[148:151],v[72:75]
v_max_f32 v24,v24,v25
v_sub_f32 v27,0,v24
v_mfma_f32_16x16x32_bf16 v[72:75],a[172:175],v[156:159],v[72:75]
v_sub_f32 v26,0,v24
v_sub_f32 v25,0,v24
v_mfma_f32_16x16x32_bf16 v[76:79],a[188:191],v[156:159],v[32:35]
s_waitcnt lgkmcnt(0)
v_exp_f32 v64,v64
v_mfma_f32_16x16x32_bf16 v[76:79],a[184:187],v[148:151],v[76:79]
global_load_lds_dwordx4 v4,s[10:11] offset:2048
v_sub_f32 v24,0,v24
v_mfma_f32_16x16x32_bf16 v[76:79],a[180:183],v[140:143],v[76:79]
ds_write_b128 v7,v[24:27] offset:2048
v_mfma_f32_16x16x32_bf16 v[76:79],a[176:179],v[132:135],v[76:79]
v_exp_f32 v65,v65
v_mfma_f32_16x16x32_bf16 a[64:67],a[192:195],v[96:99],a[64:67]
v_exp_f32 v66,v66
v_mfma_f32_16x16x32_bf16 a[64:67],a[196:199],v[104:107],a[64:67]
v_exp_f32 v67,v67
v_mfma_f32_16x16x32_bf16 a[68:71],a[200:203],v[96:99],a[68:71]
v_exp_f32 v68,v68
v_mfma_f32_16x16x32_bf16 a[68:71],a[204:207],v[104:107],a[68:71]
v_exp_f32 v69,v69
v_mfma_f32_16x16x32_bf16 a[72:75],a[208:211],v[96:99],a[72:75]
v_exp_f32 v70,v70
v_mfma_f32_16x16x32_bf16 a[72:75],a[212:215],v[104:107],a[72:75]
v_exp_f32 v71,v71
v_mfma_f32_16x16x32_bf16 a[76:79],a[216:219],v[96:99],a[76:79]
v_cvt_pk_bf16_f32 v64,v64,v65
v_cvt_pk_bf16_f32 v65,v66,v67
v_mfma_f32_16x16x32_bf16 a[76:79],a[220:223],v[104:107],a[76:79]
v_cvt_pk_bf16_f32 v66,v68,v69
v_cvt_pk_bf16_f32 v67,v70,v71
v_mfma_f32_16x16x32_bf16 a[80:83],a[224:227],v[96:99],a[80:83]
v_exp_f32 v72,v72
v_mfma_f32_16x16x32_bf16 a[80:83],a[228:231],v[104:107],a[80:83]
v_exp_f32 v73,v73
v_mfma_f32_16x16x32_bf16 a[84:87],a[232:235],v[96:99],a[84:87]
v_exp_f32 v74,v74
v_mfma_f32_16x16x32_bf16 a[84:87],a[236:239],v[104:107],a[84:87]
v_exp_f32 v75,v75
v_mfma_f32_16x16x32_bf16 a[88:91],a[240:243],v[96:99],a[88:91]
global_load_lds_dwordx4 v5,s[10:11] offset:3072
v_cvt_pk_bf16_f32 v72,v72,v73
v_mfma_f32_16x16x32_bf16 a[88:91],a[244:247],v[104:107],a[88:91]
v_exp_f32 v76,v76
v_mfma_f32_16x16x32_bf16 a[92:95],a[248:251],v[96:99],a[92:95]
v_exp_f32 v77,v77
v_mfma_f32_16x16x32_bf16 a[92:95],a[252:255],v[104:107],a[92:95]
v_exp_f32 v78,v78
v_mfma_f32_16x16x32_bf16 v[56:59],v[28:31],v[96:99],v[56:59]
v_exp_f32 v79,v79
v_mfma_f32_16x16x32_bf16 v[56:59],v[28:31],v[104:107],v[56:59]
v_cvt_pk_bf16_f32 v73,v74,v75
v_cvt_pk_bf16_f32 v74,v76,v77
v_mfma_f32_16x16x32_bf16 v[24:27],v[8:11],v[224:227],0
v_cvt_pk_bf16_f32 v75,v78,v79
v_mfma_f32_16x16x32_bf16 v[24:27],v[12:15],v[232:235],v[24:27]
v_mfma_f32_16x16x32_bf16 v[24:27],v[16:19],v[240:243],v[24:27]
v_mfma_f32_16x16x32_bf16 v[24:27],v[20:23],v[248:251],v[24:27]
v_mfma_f32_16x16x32_bf16 v[80:83],a[128:131],v[164:167],v[36:39]
s_add_u32 s10,s20,s74
s_addc_u32 s11,s21,0
v_mfma_f32_16x16x32_bf16 v[80:83],a[132:135],v[172:175],v[80:83]
v_mfma_f32_16x16x32_bf16 v[80:83],a[136:139],v[180:183],v[80:83]
s_add_u32 m0,s76,0
v_mfma_f32_16x16x32_bf16 v[80:83],a[140:143],v[188:191],v[80:83]
v_mfma_f32_16x16x32_bf16 v[84:87],a[156:159],v[188:191],v[36:39]
global_load_lds_dwordx4 v4,s[10:11] nt
v_max3_f32 v24,v24,v25,v26
v_mfma_f32_16x16x32_bf16 v[84:87],a[152:155],v[180:183],v[84:87]
v_max_f32 v24,v24,v27
v_mov_b32 v25,v24
v_mfma_f32_16x16x32_bf16 v[84:87],a[148:151],v[172:175],v[84:87]
v_mfma_f32_16x16x32_bf16 v[84:87],a[144:147],v[164:167],v[84:87]
v_permlane32_swap_b32 v24,v25
v_mfma_f32_16x16x32_bf16 v[88:91],a[160:163],v[164:167],v[36:39]
v_max_f32 v24,v24,v25
v_mov_b32 v25,v24
v_mfma_f32_16x16x32_bf16 v[88:91],a[164:167],v[172:175],v[88:91]
v_exp_f32 v80,v80
v_mfma_f32_16x16x32_bf16 v[88:91],a[168:171],v[180:183],v[88:91]
v_permlane16_swap_b32 v24,v25
v_mfma_f32_16x16x32_bf16 v[88:91],a[172:175],v[188:191],v[88:91]
v_max_f32 v24,v24,v25
v_max_f32 v24,s72,v24
v_mfma_f32_16x16x32_bf16 v[92:95],a[188:191],v[188:191],v[36:39]
v_sub_f32 v27,0,v24
v_sub_f32 v26,0,v24
v_mfma_f32_16x16x32_bf16 v[92:95],a[184:187],v[180:183],v[92:95]
v_sub_f32 v25,0,v24
L_pd6_%=:
s_cbranch_execz L_trt0_%=
v_sub_f32 v24,0,v24
v_mfma_f32_16x16x32_bf16 v[92:95],a[180:183],v[172:175],v[92:95]
v_exp_f32 v81,v81
v_mfma_f32_16x16x32_bf16 v[92:95],a[176:179],v[164:167],v[92:95]
v_exp_f32 v82,v82
v_mfma_f32_16x16x32_bf16 a[96:99],a[192:195],v[112:115],a[96:99]
ds_read_b64_tr_b16 a[192:193],v2
ds_read_b64_tr_b16 a[194:195],v2 offset:4096
v_mfma_f32_16x16x32_bf16 a[96:99],a[196:199],v[120:123],a[96:99]
ds_read_b64_tr_b16 a[196:197],v2 offset:8192
ds_read_b64_tr_b16 a[198:199],v2 offset:12288
v_mfma_f32_16x16x32_bf16 a[100:103],a[200:203],v[112:115],a[100:103]
ds_read_b64_tr_b16 a[200:201],v3
ds_read_b64_tr_b16 a[202:203],v3 offset:4096
v_mfma_f32_16x16x32_bf16 a[100:103],a[204:207],v[120:123],a[100:103]
global_load_lds_dwordx4 v4,s[10:11] offset:1024 nt
ds_read_b64_tr_b16 a[204:205],v3 offset:8192
v_mfma_f32_16x16x32_bf16 a[104:107],a[208:211],v[112:115],a[104:107]
ds_read_b64_tr_b16 a[206:207],v3 offset:12288
ds_read_b64_tr_b16 a[208:209],v2 offset:256
v_mfma_f32_16x16x32_bf16 a[104:107],a[212:215],v[120:123],a[104:107]
ds_read_b64_tr_b16 a[210:211],v2 offset:4352
ds_read_b64_tr_b16 a[212:213],v2 offset:8448
v_mfma_f32_16x16x32_bf16 a[108:111],a[216:219],v[112:115],a[108:111]
ds_read_b64_tr_b16 a[214:215],v2 offset:12544
v_cvt_pk_bf16_f32 v80,v80,v81
v_mfma_f32_16x16x32_bf16 a[108:111],a[220:223],v[120:123],a[108:111]
ds_read_b64_tr_b16 a[216:217],v3 offset:256
ds_read_b64_tr_b16 a[218:219],v3 offset:4352
v_mfma_f32_16x16x32_bf16 a[112:115],a[224:227],v[112:115],a[112:115]
v_exp_f32 v83,v83
v_mfma_f32_16x16x32_bf16 a[112:115],a[228:231],v[120:123],a[112:115]
ds_read_b64_tr_b16 a[220:221],v3 offset:8448
ds_read_b64_tr_b16 a[222:223],v3 offset:12544
v_mfma_f32_16x16x32_bf16 a[116:119],a[232:235],v[112:115],a[116:119]
ds_read_b64_tr_b16 a[224:225],v2 offset:512
ds_read_b64_tr_b16 a[226:227],v2 offset:4608
v_mfma_f32_16x16x32_bf16 a[116:119],a[236:239],v[120:123],a[116:119]
ds_read_b64_tr_b16 a[228:229],v2 offset:8704
ds_read_b64_tr_b16 a[230:231],v2 offset:12800
v_mfma_f32_16x16x32_bf16 a[120:123],a[240:243],v[112:115],a[120:123]
ds_read_b64_tr_b16 a[232:233],v3 offset:512
v_cvt_pk_bf16_f32 v81,v82,v83
v_mfma_f32_16x16x32_bf16 a[120:123],a[244:247],v[120:123],a[120:123]
ds_read_b64_tr_b16 a[234:235],v3 offset:4608
ds_read_b64_tr_b16 a[236:237],v3 offset:8704
v_mfma_f32_16x16x32_bf16 a[124:127],a[248:251],v[112:115],a[124:127]
v_exp_f32 v84,v84
v_mfma_f32_16x16x32_bf16 a[124:127],a[252:255],v[120:123],a[124:127]
ds_read_b64_tr_b16 a[238:239],v3 offset:12800
ds_read_b64_tr_b16 a[240:241],v2 offset:768
v_mfma_f32_16x16x32_bf16 v[60:63],v[28:31],v[112:115],v[60:63]
ds_read_b64_tr_b16 a[242:243],v2 offset:4864
ds_read_b64_tr_b16 a[244:245],v2 offset:8960
v_mfma_f32_16x16x32_bf16 v[60:63],v[28:31],v[120:123],v[60:63]
global_load_lds_dwordx4 v5,s[10:11] offset:2048 nt
ds_read_b64_tr_b16 a[246:247],v2 offset:13056
v_mfma_f32_16x16x32_bf16 v[96:99],a[128:131],v[196:199],v[40:43]
ds_read_b64_tr_b16 a[248:249],v3 offset:768
ds_read_b64_tr_b16 a[250:251],v3 offset:4864
v_mfma_f32_16x16x32_bf16 v[96:99],a[132:135],v[204:207],v[96:99]
ds_read_b64_tr_b16 a[252:253],v3 offset:8960
ds_read_b64_tr_b16 a[254:255],v3 offset:13056
v_mfma_f32_16x16x32_bf16 v[96:99],a[136:139],v[212:215],v[96:99]
v_exp_f32 v85,v85
v_mfma_f32_16x16x32_bf16 v[96:99],a[140:143],v[220:223],v[96:99]
v_exp_f32 v86,v86
v_mfma_f32_16x16x32_bf16 v[100:103],a[156:159],v[220:223],v[40:43]
v_exp_f32 v87,v87
v_mfma_f32_16x16x32_bf16 v[100:103],a[152:155],v[212:215],v[100:103]
v_exp_f32 v88,v88
v_mfma_f32_16x16x32_bf16 v[100:103],a[148:151],v[204:207],v[100:103]
v_exp_f32 v89,v89
v_mfma_f32_16x16x32_bf16 v[100:103],a[144:147],v[196:199],v[100:103]
v_exp_f32 v90,v90
v_mfma_f32_16x16x32_bf16 v[104:107],a[160:163],v[196:199],v[40:43]
v_exp_f32 v91,v91
v_mfma_f32_16x16x32_bf16 v[104:107],a[164:167],v[204:207],v[104:107]
v_exp_f32 v92,v92
v_mfma_f32_16x16x32_bf16 v[104:107],a[168:171],v[212:215],v[104:107]
v_exp_f32 v93,v93
v_mfma_f32_16x16x32_bf16 v[104:107],a[172:175],v[220:223],v[104:107]
v_exp_f32 v94,v94
v_mfma_f32_16x16x32_bf16 v[108:111],a[188:191],v[220:223],v[40:43]
v_exp_f32 v95,v95
v_mfma_f32_16x16x32_bf16 v[108:111],a[184:187],v[212:215],v[108:111]
v_exp_f32 v96,v96
v_mfma_f32_16x16x32_bf16 v[108:111],a[180:183],v[204:207],v[108:111]
global_load_lds_dwordx4 v5,s[10:11] offset:3072
v_cvt_pk_bf16_f32 v82,v84,v85
v_mfma_f32_16x16x32_bf16 v[108:111],a[176:179],v[196:199],v[108:111]
s_waitcnt lgkmcnt(0)
v_exp_f32 v97,v97
v_mfma_f32_16x16x32_bf16 a[0:3],a[192:195],v[64:67],a[0:3]
v_exp_f32 v98,v98
v_mfma_f32_16x16x32_bf16 a[0:3],a[196:199],v[72:75],a[0:3]
v_exp_f32 v99,v99
v_mfma_f32_16x16x32_bf16 a[4:7],a[200:203],v[64:67],a[4:7]
v_exp_f32 v100,v100
v_mfma_f32_16x16x32_bf16 a[4:7],a[204:207],v[72:75],a[4:7]
v_exp_f32 v101,v101
v_mfma_f32_16x16x32_bf16 a[8:11],a[208:211],v[64:67],a[8:11]
v_exp_f32 v102,v102
v_mfma_f32_16x16x32_bf16 a[8:11],a[212:215],v[72:75],a[8:11]
v_exp_f32 v103,v103
v_mfma_f32_16x16x32_bf16 a[12:15],a[216:219],v[64:67],a[12:15]
v_exp_f32 v104,v104
v_mfma_f32_16x16x32_bf16 a[12:15],a[220:223],v[72:75],a[12:15]
v_exp_f32 v105,v105
v_mfma_f32_16x16x32_bf16 a[16:19],a[224:227],v[64:67],a[16:19]
v_exp_f32 v106,v106
v_mfma_f32_16x16x32_bf16 a[16:19],a[228:231],v[72:75],a[16:19]
v_exp_f32 v107,v107
v_mfma_f32_16x16x32_bf16 a[20:23],a[232:235],v[64:67],a[20:23]
s_getreg_b32 s92,hwreg(HW_REG_IB_STS,0,4)
s_min_u32 s4,s92,s6
s_mov_b32 s6,s92
v_mfma_f32_16x16x32_bf16 a[20:23],a[236:239],v[72:75],a[20:23]
s_cmp_gt_u32 s4,s9
s_cselect_b32 s4,15,s5
v_cvt_pk_bf16_f32 v83,v86,v87
v_mfma_f32_16x16x32_bf16 a[24:27],a[240:243],v[64:67],a[24:27]
ds_write_b128 v7,v[24:27] offset:3072
)A"
R"A(v_mfma_f32_16x16x32_bf16 a[24:27],a[244:247],v[72:75],a[24:27]
v_cvt_pk_bf16_f32 v88,v88,v89
v_cvt_pk_bf16_f32 v89,v90,v91
v_mfma_f32_16x16x32_bf16 a[28:31],a[248:251],v[64:67],a[28:31]
v_cvt_pk_bf16_f32 v90,v92,v93
v_cvt_pk_bf16_f32 v91,v94,v95
v_mfma_f32_16x16x32_bf16 a[28:31],a[252:255],v[72:75],a[28:31]
s_getpc_b64 s[92:93]
s_cmp_lg_u32 s4,0
s_cbranch_scc1 L_pace_%=
v_mfma_f32_16x16x32_bf16 v[48:51],v[28:31],v[64:67],v[48:51]
v_cvt_pk_bf16_f32 v96,v96,v97
v_cvt_pk_bf16_f32 v97,v98,v99
v_mfma_f32_16x16x32_bf16 v[48:51],v[28:31],v[72:75],v[48:51]
v_cvt_pk_bf16_f32 v98,v100,v101
v_cvt_pk_bf16_f32 v99,v102,v103
v_cvt_pk_bf16_f32 v104,v104,v105
v_cvt_pk_bf16_f32 v105,v106,v107
v_exp_f32 v108,v108
v_exp_f32 v109,v109
s_mov_b32 s70,12
L_a0_%=:
s_waitcnt vmcnt(12)
s_barrier
v_mfma_f32_16x16x32_bf16 v[112:115],a[128:131],v[228:231],v[44:47]
s_add_u32 s10,s24,s74
s_addc_u32 s11,s25,0
s_add_u32 m0,s76,16384
v_mfma_f32_16x16x32_bf16 v[112:115],a[132:135],v[236:239],v[112:115]
s_add_u32 s75,s75,16384
ds_read_b128 a[128:131],v1
v_cvt_pk_bf16_f32 v106,v108,v109
v_mfma_f32_16x16x32_bf16 v[112:115],a[136:139],v[244:247],v[112:115]
s_add_u32 s74,s74,16384
global_load_lds_dwordx4 v4,s[10:11]
v_mfma_f32_16x16x32_bf16 v[112:115],a[140:143],v[252:255],v[112:115]
s_and_b32 s74,s74,0x3ffff
s_and_b32 s69,s75,0xfffc0000
ds_read_b128 a[132:135],v1 offset:256
v_mfma_f32_16x16x32_bf16 v[116:119],a[156:159],v[252:255],v[44:47]
s_or_b32 s74,s74,s69
v_exp_f32 v110,v110
v_mfma_f32_16x16x32_bf16 v[116:119],a[152:155],v[244:247],v[116:119]
s_sub_u32 s76,s76,32768
s_and_b32 s76,s76,0x1ffff
ds_read_b128 a[136:139],v1 offset:512
v_mfma_f32_16x16x32_bf16 v[116:119],a[148:151],v[236:239],v[116:119]
v_exp_f32 v111,v111
v_mfma_f32_16x16x32_bf16 v[116:119],a[144:147],v[228:231],v[116:119]
ds_read_b128 a[140:143],v1 offset:768
v_cvt_pk_bf16_f32 v107,v110,v111
v_mfma_f32_16x16x32_bf16 v[120:123],a[160:163],v[228:231],v[44:47]
v_exp_f32 v112,v112
v_mfma_f32_16x16x32_bf16 v[120:123],a[164:167],v[236:239],v[120:123]
ds_read_b128 a[156:159],v1 offset:4864
v_add_u32 v2,s78,v2
v_mfma_f32_16x16x32_bf16 v[120:123],a[168:171],v[244:247],v[120:123]
v_exp_f32 v113,v113
v_mfma_f32_16x16x32_bf16 v[120:123],a[172:175],v[252:255],v[120:123]
ds_read_b128 a[152:155],v1 offset:4608
v_cvt_pk_bf16_f32 v112,v112,v113
v_mfma_f32_16x16x32_bf16 v[124:127],a[188:191],v[252:255],v[44:47]
v_exp_f32 v114,v114
v_mfma_f32_16x16x32_bf16 v[124:127],a[184:187],v[244:247],v[124:127]
ds_read_b128 a[148:151],v1 offset:4352
v_add_u32 v3,s78,v3
v_mfma_f32_16x16x32_bf16 v[124:127],a[180:183],v[236:239],v[124:127]
s_sub_u32 s78,s77,32768
v_exp_f32 v115,v115
v_mfma_f32_16x16x32_bf16 v[124:127],a[176:179],v[228:231],v[124:127]
s_and_b32 s78,s78,0x1ffff
ds_read_b128 a[144:147],v1 offset:4096
v_cvt_pk_bf16_f32 v113,v114,v115
v_mfma_f32_16x16x32_bf16 a[32:35],a[192:195],v[80:83],a[32:35]
s_sub_u32 s78,s78,s77
v_exp_f32 v116,v116
v_mfma_f32_16x16x32_bf16 a[32:35],a[196:199],v[88:91],a[32:35]
s_add_u32 s77,s77,s78
v_exp_f32 v117,v117
v_mfma_f32_16x16x32_bf16 a[36:39],a[200:203],v[80:83],a[36:39]
global_load_lds_dwordx4 v5,s[10:11] offset:1024
ds_read_b128 a[160:163],v1 offset:8192
v_mfma_f32_16x16x32_bf16 a[36:39],a[204:207],v[88:91],a[36:39]
v_exp_f32 v118,v118
v_mfma_f32_16x16x32_bf16 a[40:43],a[208:211],v[80:83],a[40:43]
ds_read_b128 a[164:167],v1 offset:8448
v_cvt_pk_bf16_f32 v114,v116,v117
v_mfma_f32_16x16x32_bf16 a[40:43],a[212:215],v[88:91],a[40:43]
v_exp_f32 v119,v119
v_mfma_f32_16x16x32_bf16 a[44:47],a[216:219],v[80:83],a[44:47]
ds_read_b128 a[168:171],v1 offset:8704
v_cvt_pk_bf16_f32 v115,v118,v119
v_mfma_f32_16x16x32_bf16 a[44:47],a[220:223],v[88:91],a[44:47]
v_exp_f32 v120,v120
v_mfma_f32_16x16x32_bf16 a[48:51],a[224:227],v[80:83],a[48:51]
v_exp_f32 v121,v121
v_mfma_f32_16x16x32_bf16 a[48:51],a[228:231],v[88:91],a[48:51]
ds_read_b128 a[172:175],v1 offset:8960
v_cvt_pk_bf16_f32 v120,v120,v121
v_mfma_f32_16x16x32_bf16 a[52:55],a[232:235],v[80:83],a[52:55]
v_exp_f32 v122,v122
v_mfma_f32_16x16x32_bf16 a[52:55],a[236:239],v[88:91],a[52:55]
v_exp_f32 v123,v123
v_mfma_f32_16x16x32_bf16 a[56:59],a[240:243],v[80:83],a[56:59]
ds_read_b128 a[188:191],v1 offset:13056
v_cvt_pk_bf16_f32 v121,v122,v123
v_mfma_f32_16x16x32_bf16 a[56:59],a[244:247],v[88:91],a[56:59]
v_exp_f32 v124,v124
v_mfma_f32_16x16x32_bf16 a[60:63],a[248:251],v[80:83],a[60:63]
v_exp_f32 v125,v125
v_mfma_f32_16x16x32_bf16 a[60:63],a[252:255],v[88:91],a[60:63]
ds_read_b128 a[184:187],v1 offset:12800
v_cvt_pk_bf16_f32 v122,v124,v125
v_mfma_f32_16x16x32_bf16 v[52:55],v[28:31],v[80:83],v[52:55]
v_exp_f32 v126,v126
v_mfma_f32_16x16x32_bf16 v[52:55],v[28:31],v[88:91],v[52:55]
s_waitcnt lgkmcnt(2)
v_exp_f32 v127,v127
v_mfma_f32_16x16x32_bf16 v[64:67],a[128:131],v[132:135],v[32:35]
global_load_lds_dwordx4 v4,s[10:11] offset:2048
ds_read_b128 a[180:183],v1 offset:12544
v_mfma_f32_16x16x32_bf16 v[64:67],a[132:135],v[140:143],v[64:67]
v_cvt_pk_bf16_f32 v123,v126,v127
v_mfma_f32_16x16x32_bf16 v[64:67],a[136:139],v[148:151],v[64:67]
ds_read_b128 a[176:179],v1 offset:12288
v_add_u32 v1,s78,v1
v_mfma_f32_16x16x32_bf16 v[64:67],a[140:143],v[156:159],v[64:67]
v_mfma_f32_16x16x32_bf16 v[68:71],a[156:159],v[156:159],v[32:35]
v_mfma_f32_16x16x32_bf16 v[68:71],a[152:155],v[148:151],v[68:71]
v_mfma_f32_16x16x32_bf16 v[68:71],a[148:151],v[140:143],v[68:71]
v_mfma_f32_16x16x32_bf16 v[68:71],a[144:147],v[132:135],v[68:71]
v_mfma_f32_16x16x32_bf16 v[72:75],a[160:163],v[132:135],v[32:35]
v_mfma_f32_16x16x32_bf16 v[72:75],a[164:167],v[140:143],v[72:75]
v_mfma_f32_16x16x32_bf16 v[72:75],a[168:171],v[148:151],v[72:75]
v_mfma_f32_16x16x32_bf16 v[72:75],a[172:175],v[156:159],v[72:75]
s_waitcnt lgkmcnt(0)
v_exp_f32 v64,v64
v_mfma_f32_16x16x32_bf16 v[76:79],a[188:191],v[156:159],v[32:35]
v_exp_f32 v65,v65
v_mfma_f32_16x16x32_bf16 v[76:79],a[184:187],v[148:151],v[76:79]
v_exp_f32 v66,v66
v_mfma_f32_16x16x32_bf16 v[76:79],a[180:183],v[140:143],v[76:79]
v_exp_f32 v67,v67
v_mfma_f32_16x16x32_bf16 v[76:79],a[176:179],v[132:135],v[76:79]
v_exp_f32 v68,v68
v_mfma_f32_16x16x32_bf16 a[64:67],a[192:195],v[96:99],a[64:67]
global_load_lds_dwordx4 v5,s[10:11] offset:3072
v_cvt_pk_bf16_f32 v64,v64,v65
v_mfma_f32_16x16x32_bf16 a[64:67],a[196:199],v[104:107],a[64:67]
v_exp_f32 v69,v69
v_mfma_f32_16x16x32_bf16 a[68:71],a[200:203],v[96:99],a[68:71]
v_exp_f32 v70,v70
v_mfma_f32_16x16x32_bf16 a[68:71],a[204:207],v[104:107],a[68:71]
v_exp_f32 v71,v71
v_mfma_f32_16x16x32_bf16 a[72:75],a[208:211],v[96:99],a[72:75]
v_cvt_pk_bf16_f32 v65,v66,v67
v_cvt_pk_bf16_f32 v66,v68,v69
v_mfma_f32_16x16x32_bf16 a[72:75],a[212:215],v[104:107],a[72:75]
v_exp_f32 v72,v72
v_mfma_f32_16x16x32_bf16 a[76:79],a[216:219],v[96:99],a[76:79]
v_exp_f32 v73,v73
v_mfma_f32_16x16x32_bf16 a[76:79],a[220:223],v[104:107],a[76:79]
v_cvt_pk_bf16_f32 v67,v70,v71
v_cvt_pk_bf16_f32 v72,v72,v73
v_mfma_f32_16x16x32_bf16 a[80:83],a[224:227],v[96:99],a[80:83]
v_exp_f32 v74,v74
v_mfma_f32_16x16x32_bf16 a[80:83],a[228:231],v[104:107],a[80:83]
v_exp_f32 v75,v75
v_mfma_f32_16x16x32_bf16 a[84:87],a[232:235],v[96:99],a[84:87]
v_exp_f32 v76,v76
v_mfma_f32_16x16x32_bf16 a[84:87],a[236:239],v[104:107],a[84:87]
v_exp_f32 v77,v77
v_mfma_f32_16x16x32_bf16 a[88:91],a[240:243],v[96:99],a[88:91]
s_add_u32 s10,s20,s74
s_addc_u32 s11,s21,0
v_cvt_pk_bf16_f32 v73,v74,v75
v_mfma_f32_16x16x32_bf16 a[88:91],a[244:247],v[104:107],a[88:91]
v_exp_f32 v78,v78
v_mfma_f32_16x16x32_bf16 a[92:95],a[248:251],v[96:99],a[92:95]
s_add_u32 m0,s76,0
v_exp_f32 v79,v79
v_mfma_f32_16x16x32_bf16 a[92:95],a[252:255],v[104:107],a[92:95]
v_cvt_pk_bf16_f32 v74,v76,v77
v_cvt_pk_bf16_f32 v75,v78,v79
v_mfma_f32_16x16x32_bf16 v[56:59],v[28:31],v[96:99],v[56:59]
global_load_lds_dwordx4 v4,s[10:11] nt
v_mfma_f32_16x16x32_bf16 v[56:59],v[28:31],v[104:107],v[56:59]
v_mfma_f32_16x16x32_bf16 v[80:83],a[128:131],v[164:167],v[36:39]
v_mfma_f32_16x16x32_bf16 v[80:83],a[132:135],v[172:175],v[80:83]
v_mfma_f32_16x16x32_bf16 v[80:83],a[136:139],v[180:183],v[80:83]
v_mfma_f32_16x16x32_bf16 v[80:83],a[140:143],v[188:191],v[80:83]
v_mfma_f32_16x16x32_bf16 v[84:87],a[156:159],v[188:191],v[36:39]
v_mfma_f32_16x16x32_bf16 v[84:87],a[152:155],v[180:183],v[84:87]
v_mfma_f32_16x16x32_bf16 v[84:87],a[148:151],v[172:175],v[84:87]
v_mfma_f32_16x16x32_bf16 v[84:87],a[144:147],v[164:167],v[84:87]
v_mfma_f32_16x16x32_bf16 v[88:91],a[160:163],v[164:167],v[36:39]
v_mfma_f32_16x16x32_bf16 v[88:91],a[164:167],v[172:175],v[88:91]
v_mfma_f32_16x16x32_bf16 v[88:91],a[168:171],v[180:183],v[88:91]
v_mfma_f32_16x16x32_bf16 v[88:91],a[172:175],v[188:191],v[88:91]
v_exp_f32 v80,v80
v_mfma_f32_16x16x32_bf16 v[92:95],a[188:191],v[188:191],v[36:39]
v_exp_f32 v81,v81
v_mfma_f32_16x16x32_bf16 v[92:95],a[184:187],v[180:183],v[92:95]
v_exp_f32 v82,v82
v_mfma_f32_16x16x32_bf16 v[92:95],a[180:183],v[172:175],v[92:95]
global_load_lds_dwordx4 v4,s[10:11] offset:1024 nt
v_cvt_pk_bf16_f32 v80,v80,v81
v_mfma_f32_16x16x32_bf16 v[92:95],a[176:179],v[164:167],v[92:95]
v_exp_f32 v83,v83
v_mfma_f32_16x16x32_bf16 a[96:99],a[192:195],v[112:115],a[96:99]
ds_read_b64_tr_b16 a[192:193],v2
ds_read_b64_tr_b16 a[194:195],v2 offset:4096
v_mfma_f32_16x16x32_bf16 a[96:99],a[196:199],v[120:123],a[96:99]
ds_read_b64_tr_b16 a[196:197],v2 offset:8192
v_cvt_pk_bf16_f32 v81,v82,v83
v_mfma_f32_16x16x32_bf16 a[100:103],a[200:203],v[112:115],a[100:103]
v_exp_f32 v84,v84
v_mfma_f32_16x16x32_bf16 a[100:103],a[204:207],v[120:123],a[100:103]
ds_read_b64_tr_b16 a[198:199],v2 offset:12288
ds_read_b64_tr_b16 a[200:201],v3
v_mfma_f32_16x16x32_bf16 a[104:107],a[208:211],v[112:115],a[104:107]
v_exp_f32 v85,v85
v_mfma_f32_16x16x32_bf16 a[104:107],a[212:215],v[120:123],a[104:107]
ds_read_b64_tr_b16 a[202:203],v3 offset:4096
ds_read_b64_tr_b16 a[204:205],v3 offset:8192
v_mfma_f32_16x16x32_bf16 a[108:111],a[216:219],v[112:115],a[108:111]
ds_read_b64_tr_b16 a[206:207],v3 offset:12288
v_cvt_pk_bf16_f32 v82,v84,v85
v_mfma_f32_16x16x32_bf16 a[108:111],a[220:223],v[120:123],a[108:111]
v_exp_f32 v86,v86
v_mfma_f32_16x16x32_bf16 a[112:115],a[224:227],v[112:115],a[112:115]
ds_read_b64_tr_b16 a[208:209],v2 offset:256
ds_read_b64_tr_b16 a[210:211],v2 offset:4352
v_mfma_f32_16x16x32_bf16 a[112:115],a[228:231],v[120:123],a[112:115]
v_exp_f32 v87,v87
v_mfma_f32_16x16x32_bf16 a[116:119],a[232:235],v[112:115],a[116:119]
ds_read_b64_tr_b16 a[212:213],v2 offset:8448
ds_read_b64_tr_b16 a[214:215],v2 offset:12544
v_mfma_f32_16x16x32_bf16 a[116:119],a[236:239],v[120:123],a[116:119]
ds_read_b64_tr_b16 a[216:217],v3 offset:256
v_cvt_pk_bf16_f32 v83,v86,v87
v_mfma_f32_16x16x32_bf16 a[120:123],a[240:243],v[112:115],a[120:123]
v_exp_f32 v88,v88
v_mfma_f32_16x16x32_bf16 a[120:123],a[244:247],v[120:123],a[120:123]
ds_read_b64_tr_b16 a[218:219],v3 offset:4352
ds_read_b64_tr_b16 a[220:221],v3 offset:8448
v_mfma_f32_16x16x32_bf16 a[124:127],a[248:251],v[112:115],a[124:127]
global_load_lds_dwordx4 v5,s[10:11] offset:2048 nt
ds_read_b64_tr_b16 a[222:223],v3 offset:12544
v_mfma_f32_16x16x32_bf16 a[124:127],a[252:255],v[120:123],a[124:127]
v_exp_f32 v89,v89
v_mfma_f32_16x16x32_bf16 v[60:63],v[28:31],v[112:115],v[60:63]
ds_read_b64_tr_b16 a[224:225],v2 offset:512
ds_read_b64_tr_b16 a[226:227],v2 offset:4608
v_mfma_f32_16x16x32_bf16 v[60:63],v[28:31],v[120:123],v[60:63]
ds_read_b64_tr_b16 a[228:229],v2 offset:8704
v_cvt_pk_bf16_f32 v88,v88,v89
v_mfma_f32_16x16x32_bf16 v[96:99],a[128:131],v[196:199],v[40:43]
v_exp_f32 v90,v90
v_mfma_f32_16x16x32_bf16 v[96:99],a[132:135],v[204:207],v[96:99]
ds_read_b64_tr_b16 a[230:231],v2 offset:12800
ds_read_b64_tr_b16 a[232:233],v3 offset:512
v_mfma_f32_16x16x32_bf16 v[96:99],a[136:139],v[212:215],v[96:99]
v_exp_f32 v91,v91
v_mfma_f32_16x16x32_bf16 v[96:99],a[140:143],v[220:223],v[96:99]
ds_read_b64_tr_b16 a[234:235],v3 offset:4608
ds_read_b64_tr_b16 a[236:237],v3 offset:8704
v_mfma_f32_16x16x32_bf16 v[100:103],a[156:159],v[220:223],v[40:43]
ds_read_b64_tr_b16 a[238:239],v3 offset:12800
v_cvt_pk_bf16_f32 v89,v90,v91
v_mfma_f32_16x16x32_bf16 v[100:103],a[152:155],v[212:215],v[100:103]
v_exp_f32 v92,v92
v_mfma_f32_16x16x32_bf16 v[100:103],a[148:151],v[204:207],v[100:103]
)A"
R"A(ds_read_b64_tr_b16 a[240:241],v2 offset:768
ds_read_b64_tr_b16 a[242:243],v2 offset:4864
v_mfma_f32_16x16x32_bf16 v[100:103],a[144:147],v[196:199],v[100:103]
v_exp_f32 v93,v93
v_mfma_f32_16x16x32_bf16 v[104:107],a[160:163],v[196:199],v[40:43]
ds_read_b64_tr_b16 a[244:245],v2 offset:8960
ds_read_b64_tr_b16 a[246:247],v2 offset:13056
v_mfma_f32_16x16x32_bf16 v[104:107],a[164:167],v[204:207],v[104:107]
ds_read_b64_tr_b16 a[248:249],v3 offset:768
v_cvt_pk_bf16_f32 v90,v92,v93
v_mfma_f32_16x16x32_bf16 v[104:107],a[168:171],v[212:215],v[104:107]
v_exp_f32 v94,v94
v_mfma_f32_16x16x32_bf16 v[104:107],a[172:175],v[220:223],v[104:107]
ds_read_b64_tr_b16 a[250:251],v3 offset:4864
ds_read_b64_tr_b16 a[252:253],v3 offset:8960
v_mfma_f32_16x16x32_bf16 v[108:111],a[188:191],v[220:223],v[40:43]
global_load_lds_dwordx4 v5,s[10:11] offset:3072
ds_read_b64_tr_b16 a[254:255],v3 offset:13056
v_mfma_f32_16x16x32_bf16 v[108:111],a[184:187],v[212:215],v[108:111]
v_exp_f32 v95,v95
v_mfma_f32_16x16x32_bf16 v[108:111],a[180:183],v[204:207],v[108:111]
v_exp_f32 v96,v96
v_mfma_f32_16x16x32_bf16 v[108:111],a[176:179],v[196:199],v[108:111]
s_waitcnt lgkmcnt(6)
v_exp_f32 v97,v97
v_mfma_f32_16x16x32_bf16 a[0:3],a[192:195],v[64:67],a[0:3]
v_exp_f32 v98,v98
v_mfma_f32_16x16x32_bf16 a[0:3],a[196:199],v[72:75],a[0:3]
v_exp_f32 v99,v99
v_mfma_f32_16x16x32_bf16 a[4:7],a[200:203],v[64:67],a[4:7]
v_exp_f32 v100,v100
v_mfma_f32_16x16x32_bf16 a[4:7],a[204:207],v[72:75],a[4:7]
v_exp_f32 v101,v101
v_mfma_f32_16x16x32_bf16 a[8:11],a[208:211],v[64:67],a[8:11]
v_exp_f32 v102,v102
v_mfma_f32_16x16x32_bf16 a[8:11],a[212:215],v[72:75],a[8:11]
v_exp_f32 v103,v103
v_mfma_f32_16x16x32_bf16 a[12:15],a[216:219],v[64:67],a[12:15]
v_exp_f32 v104,v104
v_mfma_f32_16x16x32_bf16 a[12:15],a[220:223],v[72:75],a[12:15]
v_exp_f32 v105,v105
v_mfma_f32_16x16x32_bf16 a[16:19],a[224:227],v[64:67],a[16:19]
v_exp_f32 v106,v106
v_mfma_f32_16x16x32_bf16 a[16:19],a[228:231],v[72:75],a[16:19]
v_exp_f32 v107,v107
v_mfma_f32_16x16x32_bf16 a[20:23],a[232:235],v[64:67],a[20:23]
s_getreg_b32 s92,hwreg(HW_REG_IB_STS,0,4)
s_min_u32 s4,s92,s6
s_mov_b32 s6,s92
v_mfma_f32_16x16x32_bf16 a[20:23],a[236:239],v[72:75],a[20:23]
s_cmp_ge_u32 s4,s9
s_cselect_b32 s4,15,s5
v_cvt_pk_bf16_f32 v91,v94,v95
v_mfma_f32_16x16x32_bf16 a[24:27],a[240:243],v[64:67],a[24:27]
s_waitcnt lgkmcnt(0)
v_cvt_pk_bf16_f32 v96,v96,v97
v_cvt_pk_bf16_f32 v97,v98,v99
v_mfma_f32_16x16x32_bf16 a[24:27],a[244:247],v[72:75],a[24:27]
v_cvt_pk_bf16_f32 v98,v100,v101
v_cvt_pk_bf16_f32 v99,v102,v103
v_mfma_f32_16x16x32_bf16 a[28:31],a[248:251],v[64:67],a[28:31]
v_cvt_pk_bf16_f32 v104,v104,v105
v_cvt_pk_bf16_f32 v105,v106,v107
v_mfma_f32_16x16x32_bf16 a[28:31],a[252:255],v[72:75],a[28:31]
s_getpc_b64 s[92:93]
s_cmp_lg_u32 s4,0
s_cbranch_scc1 L_pace_%=
v_mfma_f32_16x16x32_bf16 v[48:51],v[28:31],v[64:67],v[48:51]
v_exp_f32 v108,v108
v_mfma_f32_16x16x32_bf16 v[48:51],v[28:31],v[72:75],v[48:51]
v_exp_f32 v109,v109
s_sub_u32 s70,s70,1
s_cmp_lg_u32 s70,0
s_cbranch_scc1 L_a0_%=
s_waitcnt vmcnt(12)
s_barrier
v_mfma_f32_16x16x32_bf16 v[112:115],a[128:131],v[228:231],v[44:47]
s_add_u32 s10,s24,s74
s_addc_u32 s11,s25,0
s_add_u32 m0,s76,16384
v_mfma_f32_16x16x32_bf16 v[112:115],a[132:135],v[236:239],v[112:115]
s_add_u32 s75,s75,16384
ds_read_b128 v[32:35],v7
v_cvt_pk_bf16_f32 v106,v108,v109
v_mfma_f32_16x16x32_bf16 v[112:115],a[136:139],v[244:247],v[112:115]
s_add_u32 s74,s74,16384
global_load_lds_dwordx4 v4,s[10:11]
v_mfma_f32_16x16x32_bf16 v[112:115],a[140:143],v[252:255],v[112:115]
s_and_b32 s74,s74,0x3ffff
s_and_b32 s69,s75,0xfffc0000
ds_read_b128 a[128:131],v1
v_mfma_f32_16x16x32_bf16 v[116:119],a[156:159],v[252:255],v[44:47]
s_or_b32 s74,s74,s69
v_exp_f32 v110,v110
v_mfma_f32_16x16x32_bf16 v[116:119],a[152:155],v[244:247],v[116:119]
s_sub_u32 s76,s76,32768
s_and_b32 s76,s76,0x1ffff
ds_read_b128 a[132:135],v1 offset:256
v_mfma_f32_16x16x32_bf16 v[116:119],a[148:151],v[236:239],v[116:119]
s_min_u32 s7,s7,0
v_exp_f32 v111,v111
v_mfma_f32_16x16x32_bf16 v[116:119],a[144:147],v[228:231],v[116:119]
s_min_u32 s4,s5,0
ds_read_b128 a[136:139],v1 offset:512
v_cvt_pk_bf16_f32 v107,v110,v111
v_mfma_f32_16x16x32_bf16 v[120:123],a[160:163],v[228:231],v[44:47]
v_exp_f32 v112,v112
v_mfma_f32_16x16x32_bf16 v[120:123],a[164:167],v[236:239],v[120:123]
ds_read_b128 a[140:143],v1 offset:768
v_add_u32 v2,s78,v2
v_mfma_f32_16x16x32_bf16 v[120:123],a[168:171],v[244:247],v[120:123]
v_exp_f32 v113,v113
v_mfma_f32_16x16x32_bf16 v[120:123],a[172:175],v[252:255],v[120:123]
ds_read_b128 a[156:159],v1 offset:4864
v_cvt_pk_bf16_f32 v112,v112,v113
v_mfma_f32_16x16x32_bf16 v[124:127],a[188:191],v[252:255],v[44:47]
v_exp_f32 v114,v114
v_mfma_f32_16x16x32_bf16 v[124:127],a[184:187],v[244:247],v[124:127]
ds_read_b128 a[152:155],v1 offset:4608
v_add_u32 v3,s78,v3
v_mfma_f32_16x16x32_bf16 v[124:127],a[180:183],v[236:239],v[124:127]
s_sub_u32 s78,s77,32768
v_exp_f32 v115,v115
v_mfma_f32_16x16x32_bf16 v[124:127],a[176:179],v[228:231],v[124:127]
s_and_b32 s78,s78,0x1ffff
ds_read_b128 a[148:151],v1 offset:4352
v_cvt_pk_bf16_f32 v113,v114,v115
v_mfma_f32_16x16x32_bf16 v[52:55],v[28:31],v[80:83],v[52:55]
global_load_lds_dwordx4 v5,s[10:11] offset:1024
v_accvgpr_read_b32 v8,a0
v_mfma_f32_16x16x32_bf16 v[52:55],v[28:31],v[88:91],v[52:55]
s_sub_u32 s78,s78,s77
ds_read_b128 a[144:147],v1 offset:4096
v_accvgpr_read_b32 v9,a1
v_mfma_f32_16x16x32_bf16 a[32:35],a[192:195],v[80:83],a[32:35]
s_add_u32 s77,s77,s78
v_exp_f32 v116,v116
v_mfma_f32_16x16x32_bf16 a[32:35],a[196:199],v[88:91],a[32:35]
ds_read_b128 a[160:163],v1 offset:8192
v_accvgpr_read_b32 v10,a2
v_mfma_f32_16x16x32_bf16 a[36:39],a[200:203],v[80:83],a[36:39]
v_exp_f32 v117,v117
v_mfma_f32_16x16x32_bf16 a[36:39],a[204:207],v[88:91],a[36:39]
ds_read_b128 a[164:167],v1 offset:8448
v_cvt_pk_bf16_f32 v114,v116,v117
v_mfma_f32_16x16x32_bf16 a[40:43],a[208:211],v[80:83],a[40:43]
v_exp_f32 v118,v118
v_mfma_f32_16x16x32_bf16 a[40:43],a[212:215],v[88:91],a[40:43]
ds_read_b128 a[168:171],v1 offset:8704
v_accvgpr_read_b32 v11,a3
v_mfma_f32_16x16x32_bf16 a[44:47],a[216:219],v[80:83],a[44:47]
v_exp_f32 v119,v119
v_mfma_f32_16x16x32_bf16 a[44:47],a[220:223],v[88:91],a[44:47]
ds_read_b128 a[172:175],v1 offset:8960
v_cvt_pk_bf16_f32 v115,v118,v119
v_mfma_f32_16x16x32_bf16 a[48:51],a[224:227],v[80:83],a[48:51]
v_exp_f32 v120,v120
v_mfma_f32_16x16x32_bf16 a[48:51],a[228:231],v[88:91],a[48:51]
ds_read_b128 a[188:191],v1 offset:13056
v_accvgpr_read_b32 v12,a4
v_mfma_f32_16x16x32_bf16 a[52:55],a[232:235],v[80:83],a[52:55]
v_exp_f32 v121,v121
v_mfma_f32_16x16x32_bf16 a[52:55],a[236:239],v[88:91],a[52:55]
ds_read_b128 a[184:187],v1 offset:12800
v_cvt_pk_bf16_f32 v120,v120,v121
v_mfma_f32_16x16x32_bf16 a[56:59],a[240:243],v[80:83],a[56:59]
global_load_lds_dwordx4 v4,s[10:11] offset:2048
v_accvgpr_read_b32 v13,a5
v_mfma_f32_16x16x32_bf16 a[56:59],a[244:247],v[88:91],a[56:59]
ds_read_b128 a[180:183],v1 offset:12544
v_accvgpr_read_b32 v14,a6
v_mfma_f32_16x16x32_bf16 a[60:63],a[248:251],v[80:83],a[60:63]
v_exp_f32 v122,v122
v_mfma_f32_16x16x32_bf16 a[60:63],a[252:255],v[88:91],a[60:63]
s_waitcnt lgkmcnt(3)
ds_read_b128 a[176:179],v1 offset:12288
v_accvgpr_read_b32 v15,a7
v_mfma_f32_16x16x32_bf16 v[64:67],a[128:131],v[128:131],v[32:35]
v_exp_f32 v123,v123
v_mfma_f32_16x16x32_bf16 v[64:67],a[132:135],v[136:139],v[64:67]
ds_read_b128 v[36:39],v7 offset:1024
v_cvt_pk_bf16_f32 v121,v122,v123
v_mfma_f32_16x16x32_bf16 v[64:67],a[136:139],v[144:147],v[64:67]
v_exp_f32 v124,v124
v_mfma_f32_16x16x32_bf16 v[64:67],a[140:143],v[152:155],v[64:67]
v_exp_f32 v125,v125
v_mfma_f32_16x16x32_bf16 v[68:71],a[156:159],v[152:155],v[32:35]
v_exp_f32 v126,v126
v_mfma_f32_16x16x32_bf16 v[68:71],a[152:155],v[144:147],v[68:71]
v_exp_f32 v127,v127
v_mfma_f32_16x16x32_bf16 v[68:71],a[148:151],v[136:139],v[68:71]
v_cvt_pk_bf16_f32 v122,v124,v125
v_cvt_pk_bf16_f32 v123,v126,v127
v_mfma_f32_16x16x32_bf16 v[68:71],a[144:147],v[128:131],v[68:71]
ds_read_b128 v[40:43],v7 offset:2048
v_max_f32 v24,0x00800000,v48
v_mfma_f32_16x16x32_bf16 v[72:75],a[160:163],v[128:131],v[32:35]
v_exp_f32 v64,v64
L_pd7_%=:
s_cbranch_execz L_trt0_%=
v_mfma_f32_16x16x32_bf16 v[72:75],a[164:167],v[136:139],v[72:75]
v_exp_f32 v65,v65
v_mfma_f32_16x16x32_bf16 v[72:75],a[168:171],v[144:147],v[72:75]
s_waitcnt lgkmcnt(1)
v_exp_f32 v66,v66
v_mfma_f32_16x16x32_bf16 v[72:75],a[172:175],v[152:155],v[72:75]
global_load_lds_dwordx4 v5,s[10:11] offset:3072
v_cvt_pk_bf16_f32 v64,v64,v65
v_mfma_f32_16x16x32_bf16 v[76:79],a[188:191],v[152:155],v[32:35]
v_exp_f32 v67,v67
v_mfma_f32_16x16x32_bf16 v[76:79],a[184:187],v[144:147],v[76:79]
v_exp_f32 v68,v68
v_mfma_f32_16x16x32_bf16 v[76:79],a[180:183],v[136:139],v[76:79]
v_exp_f32 v69,v69
v_mfma_f32_16x16x32_bf16 v[76:79],a[176:179],v[128:131],v[76:79]
v_exp_f32 v70,v70
v_mfma_f32_16x16x32_bf16 v[56:59],v[28:31],v[96:99],v[56:59]
v_exp_f32 v71,v71
v_mfma_f32_16x16x32_bf16 v[56:59],v[28:31],v[104:107],v[56:59]
v_cvt_pk_bf16_f32 v65,v66,v67
v_cvt_pk_bf16_f32 v66,v68,v69
v_mfma_f32_16x16x32_bf16 a[64:67],a[192:195],v[96:99],a[64:67]
v_cvt_pk_bf16_f32 v67,v70,v71
v_accvgpr_read_b32 v16,a8
v_mfma_f32_16x16x32_bf16 a[64:67],a[196:199],v[104:107],a[64:67]
v_exp_f32 v72,v72
v_mfma_f32_16x16x32_bf16 a[68:71],a[200:203],v[96:99],a[68:71]
v_exp_f32 v73,v73
v_mfma_f32_16x16x32_bf16 a[68:71],a[204:207],v[104:107],a[68:71]
s_add_u32 s10,s20,s74
s_addc_u32 s11,s21,0
v_cvt_pk_bf16_f32 v72,v72,v73
v_mfma_f32_16x16x32_bf16 a[72:75],a[208:211],v[96:99],a[72:75]
v_exp_f32 v74,v74
v_mfma_f32_16x16x32_bf16 a[72:75],a[212:215],v[104:107],a[72:75]
s_add_u32 m0,s76,0
v_exp_f32 v75,v75
v_mfma_f32_16x16x32_bf16 a[76:79],a[216:219],v[96:99],a[76:79]
v_exp_f32 v76,v76
v_mfma_f32_16x16x32_bf16 a[76:79],a[220:223],v[104:107],a[76:79]
global_load_lds_dwordx4 v4,s[10:11] nt
v_cvt_pk_bf16_f32 v73,v74,v75
v_mfma_f32_16x16x32_bf16 a[80:83],a[224:227],v[96:99],a[80:83]
v_exp_f32 v77,v77
v_mfma_f32_16x16x32_bf16 a[80:83],a[228:231],v[104:107],a[80:83]
v_exp_f32 v78,v78
v_mfma_f32_16x16x32_bf16 a[84:87],a[232:235],v[96:99],a[84:87]
v_exp_f32 v79,v79
v_mfma_f32_16x16x32_bf16 a[84:87],a[236:239],v[104:107],a[84:87]
v_cvt_pk_bf16_f32 v74,v76,v77
v_cvt_pk_bf16_f32 v75,v78,v79
v_mfma_f32_16x16x32_bf16 a[88:91],a[240:243],v[96:99],a[88:91]
v_rcp_f32 v24,v24
v_mfma_f32_16x16x32_bf16 a[88:91],a[244:247],v[104:107],a[88:91]
v_accvgpr_read_b32 v17,a9
v_accvgpr_read_b32 v18,a10
v_mfma_f32_16x16x32_bf16 a[92:95],a[248:251],v[96:99],a[92:95]
v_accvgpr_read_b32 v19,a11
v_mul_f32 v8,v8,v24
v_mfma_f32_16x16x32_bf16 a[92:95],a[252:255],v[104:107],a[92:95]
v_mul_f32 v9,v9,v24
v_mul_f32 v10,v10,v24
v_mfma_f32_16x16x32_bf16 v[80:83],a[128:131],v[160:163],v[36:39]
v_mul_f32 v11,v11,v24
v_mul_f32 v12,v12,v24
v_mfma_f32_16x16x32_bf16 v[80:83],a[132:135],v[168:171],v[80:83]
v_mul_f32 v13,v13,v24
v_mul_f32 v14,v14,v24
v_mfma_f32_16x16x32_bf16 v[80:83],a[136:139],v[176:179],v[80:83]
v_mul_f32 v15,v15,v24
v_cvt_pk_bf16_f32 v8,v8,v9
v_mfma_f32_16x16x32_bf16 v[80:83],a[140:143],v[184:187],v[80:83]
v_cvt_pk_bf16_f32 v9,v10,v11
v_cvt_pk_bf16_f32 v10,v12,v13
v_mfma_f32_16x16x32_bf16 v[84:87],a[156:159],v[184:187],v[36:39]
v_cvt_pk_bf16_f32 v11,v14,v15
v_accvgpr_read_b32 v20,a12
v_mfma_f32_16x16x32_bf16 v[84:87],a[152:155],v[176:179],v[84:87]
ds_write_b128 v7,v[8:11] offset:6144
v_mfma_f32_16x16x32_bf16 v[84:87],a[148:151],v[168:171],v[84:87]
global_load_lds_dwordx4 v4,s[10:11] offset:1024 nt
v_accvgpr_read_b32 v21,a13
v_mfma_f32_16x16x32_bf16 v[84:87],a[144:147],v[160:163],v[84:87]
v_accvgpr_read_b32 v22,a14
v_accvgpr_read_b32 v23,a15
v_mfma_f32_16x16x32_bf16 v[88:91],a[160:163],v[160:163],v[36:39]
v_accvgpr_read_b32 v8,a16
v_accvgpr_read_b32 v9,a17
v_mfma_f32_16x16x32_bf16 v[88:91],a[164:167],v[168:171],v[88:91]
v_accvgpr_read_b32 v10,a18
v_accvgpr_read_b32 v11,a19
v_mfma_f32_16x16x32_bf16 v[88:91],a[168:171],v[176:179],v[88:91]
v_mul_f32 v16,v16,v24
v_mul_f32 v17,v17,v24
v_mfma_f32_16x16x32_bf16 v[88:91],a[172:175],v[184:187],v[88:91]
v_mul_f32 v18,v18,v24
v_mul_f32 v19,v19,v24
v_mfma_f32_16x16x32_bf16 v[92:95],a[188:191],v[184:187],v[36:39]
)A"
R"A(v_mul_f32 v20,v20,v24
v_mul_f32 v21,v21,v24
v_mfma_f32_16x16x32_bf16 v[92:95],a[184:187],v[176:179],v[92:95]
v_mul_f32 v22,v22,v24
v_mul_f32 v23,v23,v24
v_mfma_f32_16x16x32_bf16 v[92:95],a[180:183],v[168:171],v[92:95]
v_cvt_pk_bf16_f32 v16,v16,v17
v_cvt_pk_bf16_f32 v17,v18,v19
v_mfma_f32_16x16x32_bf16 v[92:95],a[176:179],v[160:163],v[92:95]
v_cvt_pk_bf16_f32 v18,v20,v21
v_cvt_pk_bf16_f32 v19,v22,v23
v_mfma_f32_16x16x32_bf16 v[60:63],v[28:31],v[112:115],v[60:63]
ds_write_b128 v7,v[16:19] offset:7168
v_mfma_f32_16x16x32_bf16 v[60:63],v[28:31],v[120:123],v[60:63]
v_accvgpr_read_b32 v12,a20
v_accvgpr_read_b32 v13,a21
v_mfma_f32_16x16x32_bf16 a[96:99],a[192:195],v[112:115],a[96:99]
ds_read_b64_tr_b16 a[192:193],v2
ds_read_b64_tr_b16 a[194:195],v2 offset:4096
v_mfma_f32_16x16x32_bf16 a[96:99],a[196:199],v[120:123],a[96:99]
ds_read_b64_tr_b16 a[196:197],v2 offset:8192
v_accvgpr_read_b32 v14,a22
v_mfma_f32_16x16x32_bf16 a[100:103],a[200:203],v[112:115],a[100:103]
global_load_lds_dwordx4 v5,s[10:11] offset:2048 nt
ds_read_b64_tr_b16 a[198:199],v2 offset:12288
v_mfma_f32_16x16x32_bf16 a[100:103],a[204:207],v[120:123],a[100:103]
ds_read_b64_tr_b16 a[200:201],v3
v_accvgpr_read_b32 v15,a23
v_mfma_f32_16x16x32_bf16 a[104:107],a[208:211],v[112:115],a[104:107]
ds_read_b64_tr_b16 a[202:203],v3 offset:4096
ds_read_b64_tr_b16 a[204:205],v3 offset:8192
v_mfma_f32_16x16x32_bf16 a[104:107],a[212:215],v[120:123],a[104:107]
s_cmp_le_u32 s5,2
s_cselect_b32 s5,s4,s5
ds_read_b64_tr_b16 a[206:207],v3 offset:12288
v_mfma_f32_16x16x32_bf16 a[108:111],a[216:219],v[112:115],a[108:111]
ds_read_b64_tr_b16 a[208:209],v2 offset:256
v_accvgpr_read_b32 v16,a24
v_mfma_f32_16x16x32_bf16 a[108:111],a[220:223],v[120:123],a[108:111]
ds_read_b64_tr_b16 a[210:211],v2 offset:4352
v_accvgpr_read_b32 v17,a25
v_mfma_f32_16x16x32_bf16 a[112:115],a[224:227],v[112:115],a[112:115]
ds_read_b64_tr_b16 a[212:213],v2 offset:8448
ds_read_b64_tr_b16 a[214:215],v2 offset:12544
v_mfma_f32_16x16x32_bf16 a[112:115],a[228:231],v[120:123],a[112:115]
ds_read_b64_tr_b16 a[216:217],v3 offset:256
v_accvgpr_read_b32 v18,a26
v_mfma_f32_16x16x32_bf16 a[116:119],a[232:235],v[112:115],a[116:119]
ds_read_b64_tr_b16 a[218:219],v3 offset:4352
v_accvgpr_read_b32 v19,a27
v_mfma_f32_16x16x32_bf16 a[116:119],a[236:239],v[120:123],a[116:119]
ds_read_b64_tr_b16 a[220:221],v3 offset:8448
v_accvgpr_read_b32 v20,a28
v_mfma_f32_16x16x32_bf16 a[120:123],a[240:243],v[112:115],a[120:123]
ds_read_b64_tr_b16 a[222:223],v3 offset:12544
ds_read_b64_tr_b16 a[224:225],v2 offset:512
v_mfma_f32_16x16x32_bf16 a[120:123],a[244:247],v[120:123],a[120:123]
ds_read_b64_tr_b16 a[226:227],v2 offset:4608
v_accvgpr_read_b32 v21,a29
v_mfma_f32_16x16x32_bf16 a[124:127],a[248:251],v[112:115],a[124:127]
ds_read_b64_tr_b16 a[228:229],v2 offset:8704
v_accvgpr_read_b32 v22,a30
v_mfma_f32_16x16x32_bf16 a[124:127],a[252:255],v[120:123],a[124:127]
s_waitcnt lgkmcnt(9)
ds_read_b64_tr_b16 a[230:231],v2 offset:12800
v_accvgpr_read_b32 v23,a31
v_mfma_f32_16x16x32_bf16 v[96:99],a[128:131],v[192:195],v[40:43]
ds_read_b64_tr_b16 a[232:233],v3 offset:512
ds_read_b64_tr_b16 a[234:235],v3 offset:4608
v_mfma_f32_16x16x32_bf16 v[96:99],a[132:135],v[200:203],v[96:99]
global_load_lds_dwordx4 v5,s[10:11] offset:3072
ds_read_b64_tr_b16 a[236:237],v3 offset:8704
v_mfma_f32_16x16x32_bf16 v[96:99],a[136:139],v[208:211],v[96:99]
ds_read_b64_tr_b16 a[238:239],v3 offset:12800
v_cmp_nlt_f32 s[54:55],v48,s73
v_mfma_f32_16x16x32_bf16 v[96:99],a[140:143],v[216:219],v[96:99]
ds_read_b64_tr_b16 a[240:241],v2 offset:768
v_cmp_nlt_f32 s[56:57],v52,s73
v_mfma_f32_16x16x32_bf16 v[100:103],a[156:159],v[216:219],v[40:43]
s_or_b64 s[54:55],s[54:55],s[56:57]
ds_read_b64_tr_b16 a[242:243],v2 offset:4864
ds_read_b64_tr_b16 a[244:245],v2 offset:8960
v_mfma_f32_16x16x32_bf16 v[100:103],a[152:155],v[208:211],v[100:103]
ds_read_b64_tr_b16 a[246:247],v2 offset:13056
v_cmp_nlt_f32 s[56:57],v56,s73
v_mfma_f32_16x16x32_bf16 v[100:103],a[148:151],v[200:203],v[100:103]
s_or_b64 s[54:55],s[54:55],s[56:57]
ds_read_b64_tr_b16 a[248:249],v3 offset:768
v_mul_f32 v8,v8,v24
v_mfma_f32_16x16x32_bf16 v[100:103],a[144:147],v[192:195],v[100:103]
ds_read_b64_tr_b16 a[250:251],v3 offset:4864
v_mul_f32 v9,v9,v24
v_mfma_f32_16x16x32_bf16 v[104:107],a[160:163],v[192:195],v[40:43]
ds_read_b64_tr_b16 a[252:253],v3 offset:8960
ds_read_b64_tr_b16 a[254:255],v3 offset:13056
v_mfma_f32_16x16x32_bf16 v[104:107],a[164:167],v[200:203],v[104:107]
v_mul_f32 v10,v10,v24
v_mul_f32 v11,v11,v24
v_mfma_f32_16x16x32_bf16 v[104:107],a[168:171],v[208:211],v[104:107]
v_mul_f32 v12,v12,v24
v_mul_f32 v13,v13,v24
v_mfma_f32_16x16x32_bf16 v[104:107],a[172:175],v[216:219],v[104:107]
s_add_u32 s10,s24,s74
s_addc_u32 s11,s25,0
v_mul_f32 v14,v14,v24
v_mfma_f32_16x16x32_bf16 v[108:111],a[188:191],v[216:219],v[40:43]
v_mul_f32 v15,v15,v24
v_cvt_pk_bf16_f32 v8,v8,v9
v_mfma_f32_16x16x32_bf16 v[108:111],a[184:187],v[208:211],v[108:111]
s_add_u32 m0,s76,16384
v_cvt_pk_bf16_f32 v9,v10,v11
v_cvt_pk_bf16_f32 v10,v12,v13
v_mfma_f32_16x16x32_bf16 v[108:111],a[180:183],v[200:203],v[108:111]
v_cvt_pk_bf16_f32 v11,v14,v15
v_mul_f32 v16,v16,v24
v_mfma_f32_16x16x32_bf16 v[108:111],a[176:179],v[192:195],v[108:111]
global_load_lds_dwordx4 v4,s[10:11]
v_mul_f32 v17,v17,v24
v_mfma_f32_16x16x32_bf16 a[0:3],a[192:195],v[64:67],0
v_mul_f32 v18,v18,v24
v_mul_f32 v19,v19,v24
v_mfma_f32_16x16x32_bf16 a[0:3],a[196:199],v[72:75],a[0:3]
v_mul_f32 v20,v20,v24
v_mul_f32 v21,v21,v24
v_mfma_f32_16x16x32_bf16 a[4:7],a[200:203],v[64:67],0
v_mul_f32 v22,v22,v24
v_mul_f32 v23,v23,v24
v_mfma_f32_16x16x32_bf16 a[4:7],a[204:207],v[72:75],a[4:7]
v_cvt_pk_bf16_f32 v16,v16,v17
v_cvt_pk_bf16_f32 v17,v18,v19
v_mfma_f32_16x16x32_bf16 a[8:11],a[208:211],v[64:67],0
s_waitcnt lgkmcnt(0)
v_cvt_pk_bf16_f32 v18,v20,v21
v_cvt_pk_bf16_f32 v19,v22,v23
v_mfma_f32_16x16x32_bf16 a[8:11],a[212:215],v[72:75],a[8:11]
v_exp_f32 v80,v80
v_mfma_f32_16x16x32_bf16 a[12:15],a[216:219],v[64:67],0
v_exp_f32 v81,v81
v_mfma_f32_16x16x32_bf16 a[12:15],a[220:223],v[72:75],a[12:15]
v_exp_f32 v82,v82
v_mfma_f32_16x16x32_bf16 a[16:19],a[224:227],v[64:67],0
v_exp_f32 v83,v83
v_mfma_f32_16x16x32_bf16 a[16:19],a[228:231],v[72:75],a[16:19]
v_exp_f32 v84,v84
v_mfma_f32_16x16x32_bf16 a[20:23],a[232:235],v[64:67],0
s_getreg_b32 s92,hwreg(HW_REG_IB_STS,0,4)
s_min_u32 s4,s92,s6
s_mov_b32 s6,s92
v_mfma_f32_16x16x32_bf16 a[20:23],a[236:239],v[72:75],a[20:23]
s_cmp_gt_u32 s4,s9
s_cselect_b32 s4,15,s5
v_cmp_nlt_f32 s[56:57],v60,s73
v_mfma_f32_16x16x32_bf16 a[24:27],a[240:243],v[64:67],0
s_or_b64 s[54:55],s[54:55],s[56:57]
v_exp_f32 v85,v85
v_mfma_f32_16x16x32_bf16 a[24:27],a[244:247],v[72:75],a[24:27]
v_exp_f32 v86,v86
v_mfma_f32_16x16x32_bf16 a[28:31],a[248:251],v[64:67],0
global_load_lds_dwordx4 v5,s[10:11] offset:1024
v_add_u32 v1,s78,v1
v_mfma_f32_16x16x32_bf16 a[28:31],a[252:255],v[72:75],a[28:31]
v_exp_f32 v87,v87
v_mfma_f32_16x16x32_bf16 v[48:51],v[28:31],v[64:67],0
s_getpc_b64 s[92:93]
s_cmp_lg_u32 s4,0
s_cbranch_scc1 L_pace_%=
v_exp_f32 v88,v88
v_exp_f32 v89,v89
v_mfma_f32_16x16x32_bf16 v[48:51],v[28:31],v[72:75],v[48:51]
ds_write_b128 v7,v[8:11]
v_max_f32 v25,0x00800000,v52
v_rcp_f32 v25,v25
v_max_f32 v26,0x00800000,v56
v_rcp_f32 v26,v26
v_cvt_pk_bf16_f32 v80,v80,v81
v_cvt_pk_bf16_f32 v81,v82,v83
v_cvt_pk_bf16_f32 v82,v84,v85
v_cvt_pk_bf16_f32 v83,v86,v87
v_cvt_pk_bf16_f32 v88,v88,v89
v_max_f32 v27,0x00800000,v60
v_rcp_f32 v27,v27
ds_write_b128 v7,v[16:19] offset:1024
ds_read_b128 v[44:47],v7 offset:3072
s_cmp_lg_u64 s[54:55],0
s_cbranch_scc1 L_slow0_%=
L_ok0_%=:
s_waitcnt vmcnt(14)
s_barrier
s_waitcnt lgkmcnt(0)
v_mfma_f32_16x16x32_bf16 v[112:115],a[128:131],v[224:227],v[44:47]
s_add_u32 s10,s24,s74
s_addc_u32 s11,s25,0
s_add_u32 m0,s76,16384
v_mfma_f32_16x16x32_bf16 v[112:115],a[132:135],v[232:235],v[112:115]
s_add_u32 s75,s75,16384
v_exp_f32 v90,v90
v_mfma_f32_16x16x32_bf16 v[112:115],a[136:139],v[240:243],v[112:115]
global_load_lds_dwordx4 v4,s[10:11] offset:2048
ds_read_b128 a[128:131],v1
v_mfma_f32_16x16x32_bf16 v[112:115],a[140:143],v[248:251],v[112:115]
s_add_u32 s74,s74,16384
v_exp_f32 v91,v91
v_mfma_f32_16x16x32_bf16 v[116:119],a[156:159],v[248:251],v[44:47]
s_and_b32 s74,s74,0x3ffff
v_exp_f32 v92,v92
v_mfma_f32_16x16x32_bf16 v[116:119],a[152:155],v[240:243],v[116:119]
s_and_b32 s69,s75,0xfffc0000
v_exp_f32 v93,v93
v_mfma_f32_16x16x32_bf16 v[116:119],a[148:151],v[232:235],v[116:119]
s_or_b32 s74,s74,s69
v_exp_f32 v94,v94
v_mfma_f32_16x16x32_bf16 v[116:119],a[144:147],v[224:227],v[116:119]
s_sub_u32 s76,s76,32768
v_exp_f32 v95,v95
v_mfma_f32_16x16x32_bf16 v[120:123],a[160:163],v[224:227],v[44:47]
s_and_b32 s76,s76,0x1ffff
v_cvt_pk_bf16_f32 v89,v90,v91
v_cvt_pk_bf16_f32 v90,v92,v93
v_mfma_f32_16x16x32_bf16 v[120:123],a[164:167],v[232:235],v[120:123]
s_add_u32 s75,s75,16384
ds_read_b128 a[132:135],v1 offset:256
v_cvt_pk_bf16_f32 v91,v94,v95
v_mfma_f32_16x16x32_bf16 v[120:123],a[168:171],v[240:243],v[120:123]
s_and_b32 s69,s75,0xfffc0000
v_exp_f32 v96,v96
v_mfma_f32_16x16x32_bf16 v[120:123],a[172:175],v[248:251],v[120:123]
s_add_u32 s75,s75,16384
ds_read_b128 a[136:139],v1 offset:512
v_accvgpr_read_b32 v16,a32
v_mfma_f32_16x16x32_bf16 v[124:127],a[188:191],v[248:251],v[44:47]
v_exp_f32 v97,v97
v_mfma_f32_16x16x32_bf16 v[124:127],a[184:187],v[240:243],v[124:127]
ds_read_b128 a[140:143],v1 offset:768
v_cvt_pk_bf16_f32 v96,v96,v97
v_mfma_f32_16x16x32_bf16 v[124:127],a[180:183],v[232:235],v[124:127]
v_exp_f32 v98,v98
v_mfma_f32_16x16x32_bf16 v[124:127],a[176:179],v[224:227],v[124:127]
ds_read_b128 a[156:159],v1 offset:4864
v_accvgpr_read_b32 v17,a33
v_mfma_f32_16x16x32_bf16 v[132:135],a[192:195],v[80:83],0
global_load_lds_dwordx4 v5,s[10:11] offset:3072
v_accvgpr_read_b32 v18,a34
v_mfma_f32_16x16x32_bf16 v[132:135],a[196:199],v[88:91],v[132:135]
ds_read_b128 a[152:155],v1 offset:4608
v_accvgpr_read_b32 v19,a35
v_mfma_f32_16x16x32_bf16 v[140:143],a[200:203],v[80:83],0
v_exp_f32 v99,v99
v_mfma_f32_16x16x32_bf16 v[140:143],a[204:207],v[88:91],v[140:143]
ds_read_b128 a[148:151],v1 offset:4352
v_cvt_pk_bf16_f32 v97,v98,v99
v_mfma_f32_16x16x32_bf16 v[148:151],a[208:211],v[80:83],0
v_exp_f32 v100,v100
v_mfma_f32_16x16x32_bf16 v[148:151],a[212:215],v[88:91],v[148:151]
ds_read_b128 a[144:147],v1 offset:4096
v_accvgpr_read_b32 v20,a36
v_mfma_f32_16x16x32_bf16 v[156:159],a[216:219],v[80:83],0
v_exp_f32 v101,v101
v_mfma_f32_16x16x32_bf16 v[156:159],a[220:223],v[88:91],v[156:159]
ds_read_b128 a[160:163],v1 offset:8192
v_cvt_pk_bf16_f32 v98,v100,v101
v_mfma_f32_16x16x32_bf16 v[164:167],a[224:227],v[80:83],0
v_exp_f32 v102,v102
v_mfma_f32_16x16x32_bf16 v[164:167],a[228:231],v[88:91],v[164:167]
ds_read_b128 a[164:167],v1 offset:8448
v_accvgpr_read_b32 v21,a37
v_mfma_f32_16x16x32_bf16 v[172:175],a[232:235],v[80:83],0
s_add_u32 s10,s20,s74
s_addc_u32 s11,s21,0
v_accvgpr_read_b32 v22,a38
v_mfma_f32_16x16x32_bf16 v[172:175],a[236:239],v[88:91],v[172:175]
ds_read_b128 a[168:171],v1 offset:8704
v_accvgpr_read_b32 v23,a39
v_mfma_f32_16x16x32_bf16 v[180:183],a[240:243],v[80:83],0
s_add_u32 m0,s76,0
v_exp_f32 v103,v103
v_mfma_f32_16x16x32_bf16 v[180:183],a[244:247],v[88:91],v[180:183]
ds_read_b128 a[172:175],v1 offset:8960
v_cvt_pk_bf16_f32 v99,v102,v103
v_mfma_f32_16x16x32_bf16 v[188:191],a[248:251],v[80:83],0
global_load_lds_dwordx4 v4,s[10:11] nt
v_mul_f32 v16,v16,v25
v_mfma_f32_16x16x32_bf16 v[188:191],a[252:255],v[88:91],v[188:191]
ds_read_b128 a[188:191],v1 offset:13056
v_mul_f32 v17,v17,v25
v_mfma_f32_16x16x32_bf16 v[52:55],v[28:31],v[80:83],0
v_exp_f32 v104,v104
v_mfma_f32_16x16x32_bf16 v[52:55],v[28:31],v[88:91],v[52:55]
s_waitcnt lgkmcnt(3)
ds_read_b128 a[184:187],v1 offset:12800
v_mul_f32 v18,v18,v25
v_mfma_f32_16x16x32_bf16 v[64:67],a[128:131],v[128:131],v[32:35]
v_exp_f32 v105,v105
v_mfma_f32_16x16x32_bf16 v[64:67],a[132:135],v[136:139],v[64:67]
ds_read_b128 a[180:183],v1 offset:12544
v_cvt_pk_bf16_f32 v104,v104,v105
v_mfma_f32_16x16x32_bf16 v[64:67],a[136:139],v[144:147],v[64:67]
v_exp_f32 v106,v106
v_mfma_f32_16x16x32_bf16 v[64:67],a[140:143],v[152:155],v[64:67]
)A"
R"A(ds_read_b128 a[176:179],v1 offset:12288
v_mul_f32 v19,v19,v25
v_mfma_f32_16x16x32_bf16 v[68:71],a[156:159],v[152:155],v[32:35]
v_exp_f32 v107,v107
v_mfma_f32_16x16x32_bf16 v[68:71],a[152:155],v[144:147],v[68:71]
v_exp_f32 v108,v108
v_mfma_f32_16x16x32_bf16 v[68:71],a[148:151],v[136:139],v[68:71]
v_exp_f32 v109,v109
v_mfma_f32_16x16x32_bf16 v[68:71],a[144:147],v[128:131],v[68:71]
v_exp_f32 v110,v110
v_mfma_f32_16x16x32_bf16 v[72:75],a[160:163],v[128:131],v[32:35]
v_exp_f32 v111,v111
v_mfma_f32_16x16x32_bf16 v[72:75],a[164:167],v[136:139],v[72:75]
s_waitcnt lgkmcnt(1)
v_cvt_pk_bf16_f32 v105,v106,v107
v_cvt_pk_bf16_f32 v106,v108,v109
v_mfma_f32_16x16x32_bf16 v[72:75],a[168:171],v[144:147],v[72:75]
global_load_lds_dwordx4 v4,s[10:11] offset:1024 nt
v_cvt_pk_bf16_f32 v107,v110,v111
v_mfma_f32_16x16x32_bf16 v[72:75],a[172:175],v[152:155],v[72:75]
v_exp_f32 v112,v112
v_mfma_f32_16x16x32_bf16 v[76:79],a[188:191],v[152:155],v[32:35]
v_exp_f32 v113,v113
v_mfma_f32_16x16x32_bf16 v[76:79],a[184:187],v[144:147],v[76:79]
v_exp_f32 v114,v114
v_mfma_f32_16x16x32_bf16 v[76:79],a[180:183],v[136:139],v[76:79]
s_waitcnt lgkmcnt(0)
v_exp_f32 v115,v115
v_mfma_f32_16x16x32_bf16 v[76:79],a[176:179],v[128:131],v[76:79]
v_exp_f32 v116,v116
v_mfma_f32_16x16x32_bf16 v[196:199],a[192:195],v[96:99],0
v_exp_f32 v117,v117
v_mfma_f32_16x16x32_bf16 v[196:199],a[196:199],v[104:107],v[196:199]
v_exp_f32 v118,v118
v_mfma_f32_16x16x32_bf16 v[204:207],a[200:203],v[96:99],0
v_exp_f32 v119,v119
v_mfma_f32_16x16x32_bf16 v[204:207],a[204:207],v[104:107],v[204:207]
v_cvt_pk_bf16_f32 v112,v112,v113
v_cvt_pk_bf16_f32 v113,v114,v115
v_mfma_f32_16x16x32_bf16 v[212:215],a[208:211],v[96:99],0
v_cvt_pk_bf16_f32 v114,v116,v117
v_cvt_pk_bf16_f32 v115,v118,v119
v_mfma_f32_16x16x32_bf16 v[212:215],a[212:215],v[104:107],v[212:215]
v_exp_f32 v120,v120
v_mfma_f32_16x16x32_bf16 v[220:223],a[216:219],v[96:99],0
v_exp_f32 v121,v121
v_mfma_f32_16x16x32_bf16 v[220:223],a[220:223],v[104:107],v[220:223]
v_exp_f32 v122,v122
v_mfma_f32_16x16x32_bf16 v[228:231],a[224:227],v[96:99],0
global_load_lds_dwordx4 v5,s[10:11] offset:2048 nt
v_cvt_pk_bf16_f32 v120,v120,v121
v_mfma_f32_16x16x32_bf16 v[228:231],a[228:231],v[104:107],v[228:231]
v_exp_f32 v123,v123
v_mfma_f32_16x16x32_bf16 v[236:239],a[232:235],v[96:99],0
v_exp_f32 v124,v124
v_mfma_f32_16x16x32_bf16 v[236:239],a[236:239],v[104:107],v[236:239]
v_exp_f32 v125,v125
v_mfma_f32_16x16x32_bf16 v[244:247],a[240:243],v[96:99],0
v_exp_f32 v126,v126
v_mfma_f32_16x16x32_bf16 v[244:247],a[244:247],v[104:107],v[244:247]
v_exp_f32 v127,v127
v_mfma_f32_16x16x32_bf16 v[252:255],a[248:251],v[96:99],0
v_cvt_pk_bf16_f32 v121,v122,v123
v_cvt_pk_bf16_f32 v122,v124,v125
v_mfma_f32_16x16x32_bf16 v[252:255],a[252:255],v[104:107],v[252:255]
v_cvt_pk_bf16_f32 v123,v126,v127
v_mul_f32 v20,v20,v25
v_mfma_f32_16x16x32_bf16 v[56:59],v[28:31],v[96:99],0
v_mul_f32 v21,v21,v25
v_mul_f32 v22,v22,v25
v_mfma_f32_16x16x32_bf16 v[56:59],v[28:31],v[104:107],v[56:59]
v_mul_f32 v23,v23,v25
v_cvt_pk_bf16_f32 v16,v16,v17
v_mfma_f32_16x16x32_bf16 v[80:83],a[128:131],v[160:163],v[36:39]
v_cvt_pk_bf16_f32 v17,v18,v19
v_cvt_pk_bf16_f32 v18,v20,v21
v_mfma_f32_16x16x32_bf16 v[80:83],a[132:135],v[168:171],v[80:83]
v_cvt_pk_bf16_f32 v19,v22,v23
v_accvgpr_read_b32 v20,a44
v_mfma_f32_16x16x32_bf16 v[80:83],a[136:139],v[176:179],v[80:83]
ds_write_b128 v7,v[16:19] offset:2048
v_mfma_f32_16x16x32_bf16 v[80:83],a[140:143],v[184:187],v[80:83]
v_accvgpr_read_b32 v21,a45
v_accvgpr_read_b32 v22,a46
v_mfma_f32_16x16x32_bf16 v[84:87],a[156:159],v[184:187],v[36:39]
v_accvgpr_read_b32 v16,a40
v_accvgpr_read_b32 v17,a41
v_mfma_f32_16x16x32_bf16 v[84:87],a[152:155],v[176:179],v[84:87]
global_load_lds_dwordx4 v5,s[10:11] offset:3072
v_accvgpr_read_b32 v18,a42
v_mfma_f32_16x16x32_bf16 v[84:87],a[148:151],v[168:171],v[84:87]
v_accvgpr_read_b32 v19,a43
v_accvgpr_read_b32 v23,a47
v_mfma_f32_16x16x32_bf16 v[84:87],a[144:147],v[160:163],v[84:87]
v_accvgpr_read_b32 v8,a48
v_accvgpr_read_b32 v9,a49
v_mfma_f32_16x16x32_bf16 v[88:91],a[160:163],v[160:163],v[36:39]
v_accvgpr_read_b32 v10,a50
v_accvgpr_read_b32 v11,a51
v_mfma_f32_16x16x32_bf16 v[88:91],a[164:167],v[168:171],v[88:91]
v_mul_f32 v16,v16,v25
v_mul_f32 v17,v17,v25
v_mfma_f32_16x16x32_bf16 v[88:91],a[168:171],v[176:179],v[88:91]
v_mul_f32 v18,v18,v25
v_mul_f32 v19,v19,v25
v_mfma_f32_16x16x32_bf16 v[88:91],a[172:175],v[184:187],v[88:91]
v_mul_f32 v20,v20,v25
v_mul_f32 v21,v21,v25
v_mfma_f32_16x16x32_bf16 v[92:95],a[188:191],v[184:187],v[36:39]
v_mul_f32 v22,v22,v25
v_mul_f32 v23,v23,v25
v_mfma_f32_16x16x32_bf16 v[92:95],a[184:187],v[176:179],v[92:95]
v_cvt_pk_bf16_f32 v16,v16,v17
v_cvt_pk_bf16_f32 v17,v18,v19
v_mfma_f32_16x16x32_bf16 v[92:95],a[180:183],v[168:171],v[92:95]
v_cvt_pk_bf16_f32 v18,v20,v21
v_cvt_pk_bf16_f32 v19,v22,v23
v_mfma_f32_16x16x32_bf16 v[92:95],a[176:179],v[160:163],v[92:95]
s_add_u32 s10,s24,s74
s_addc_u32 s11,s25,0
v_accvgpr_read_b32 v12,a52
v_mfma_f32_16x16x32_bf16 a[32:35],a[192:195],v[112:115],0
ds_write_b128 v7,v[16:19] offset:3072
v_mfma_f32_16x16x32_bf16 a[32:35],a[196:199],v[120:123],a[32:35]
s_add_u32 m0,s76,16384
v_accvgpr_read_b32 v13,a53
v_accvgpr_read_b32 v14,a54
v_mfma_f32_16x16x32_bf16 a[36:39],a[200:203],v[112:115],0
v_accvgpr_read_b32 v15,a55
v_accvgpr_read_b32 v16,a56
v_mfma_f32_16x16x32_bf16 a[36:39],a[204:207],v[120:123],a[36:39]
global_load_lds_dwordx4 v4,s[10:11]
v_accvgpr_read_b32 v17,a57
v_mfma_f32_16x16x32_bf16 a[40:43],a[208:211],v[112:115],0
v_accvgpr_read_b32 v18,a58
v_accvgpr_read_b32 v19,a59
v_mfma_f32_16x16x32_bf16 a[40:43],a[212:215],v[120:123],a[40:43]
v_accvgpr_read_b32 v20,a60
v_accvgpr_read_b32 v21,a61
v_mfma_f32_16x16x32_bf16 a[44:47],a[216:219],v[112:115],0
v_accvgpr_read_b32 v22,a62
v_accvgpr_read_b32 v23,a63
v_mfma_f32_16x16x32_bf16 a[44:47],a[220:223],v[120:123],a[44:47]
v_add_u32 v2,s78,v2
v_add_u32 v3,s78,v3
v_mfma_f32_16x16x32_bf16 a[48:51],a[224:227],v[112:115],0
s_sub_u32 s78,s77,32768
ds_read_b64_tr_b16 a[192:193],v2
ds_read_b64_tr_b16 a[194:195],v2 offset:4096
v_mfma_f32_16x16x32_bf16 a[48:51],a[228:231],v[120:123],a[48:51]
s_and_b32 s78,s78,0x1ffff
ds_read_b64_tr_b16 a[196:197],v2 offset:8192
v_mul_f32 v8,v8,v25
v_mfma_f32_16x16x32_bf16 a[52:55],a[232:235],v[112:115],0
s_sub_u32 s78,s78,s77
ds_read_b64_tr_b16 a[198:199],v2 offset:12288
v_mul_f32 v9,v9,v25
v_mfma_f32_16x16x32_bf16 a[52:55],a[236:239],v[120:123],a[52:55]
s_add_u32 s77,s77,s78
ds_read_b64_tr_b16 a[200:201],v3
v_mul_f32 v10,v10,v25
v_mfma_f32_16x16x32_bf16 a[56:59],a[240:243],v[112:115],0
ds_read_b64_tr_b16 a[202:203],v3 offset:4096
ds_read_b64_tr_b16 a[204:205],v3 offset:8192
v_mfma_f32_16x16x32_bf16 a[56:59],a[244:247],v[120:123],a[56:59]
ds_read_b64_tr_b16 a[206:207],v3 offset:12288
v_mul_f32 v11,v11,v25
v_mfma_f32_16x16x32_bf16 a[60:63],a[248:251],v[112:115],0
ds_read_b64_tr_b16 a[208:209],v2 offset:256
v_mul_f32 v12,v12,v25
v_mfma_f32_16x16x32_bf16 a[60:63],a[252:255],v[120:123],a[60:63]
ds_read_b64_tr_b16 a[210:211],v2 offset:4352
v_mul_f32 v13,v13,v25
v_mfma_f32_16x16x32_bf16 v[60:63],v[28:31],v[112:115],0
ds_read_b64_tr_b16 a[212:213],v2 offset:8448
ds_read_b64_tr_b16 a[214:215],v2 offset:12544
v_mfma_f32_16x16x32_bf16 v[60:63],v[28:31],v[120:123],v[60:63]
global_load_lds_dwordx4 v5,s[10:11] offset:1024
ds_read_b64_tr_b16 a[216:217],v3 offset:256
v_mfma_f32_16x16x32_bf16 v[96:99],a[128:131],v[192:195],v[40:43]
v_exp_f32 v64,v64
v_mfma_f32_16x16x32_bf16 v[96:99],a[132:135],v[200:203],v[96:99]
v_exp_f32 v65,v65
v_mfma_f32_16x16x32_bf16 v[96:99],a[136:139],v[208:211],v[96:99]
v_exp_f32 v66,v66
v_mfma_f32_16x16x32_bf16 v[96:99],a[140:143],v[216:219],v[96:99]
v_exp_f32 v67,v67
v_mfma_f32_16x16x32_bf16 v[100:103],a[156:159],v[216:219],v[40:43]
v_exp_f32 v68,v68
v_mfma_f32_16x16x32_bf16 v[100:103],a[152:155],v[208:211],v[100:103]
v_exp_f32 v69,v69
v_mfma_f32_16x16x32_bf16 v[100:103],a[148:151],v[200:203],v[100:103]
v_exp_f32 v70,v70
v_mfma_f32_16x16x32_bf16 v[100:103],a[144:147],v[192:195],v[100:103]
v_exp_f32 v71,v71
v_mfma_f32_16x16x32_bf16 v[104:107],a[160:163],v[192:195],v[40:43]
ds_read_b64_tr_b16 a[218:219],v3 offset:4352
v_cvt_pk_bf16_f32 v64,v64,v65
v_mfma_f32_16x16x32_bf16 v[104:107],a[164:167],v[200:203],v[104:107]
v_cvt_pk_bf16_f32 v65,v66,v67
v_cvt_pk_bf16_f32 v66,v68,v69
v_mfma_f32_16x16x32_bf16 v[104:107],a[168:171],v[208:211],v[104:107]
L_pd8_%=:
s_cbranch_execz L_trt0_%=
ds_read_b64_tr_b16 a[220:221],v3 offset:8448
v_cvt_pk_bf16_f32 v67,v70,v71
v_mfma_f32_16x16x32_bf16 v[104:107],a[172:175],v[216:219],v[104:107]
v_exp_f32 v72,v72
v_mfma_f32_16x16x32_bf16 v[108:111],a[188:191],v[216:219],v[40:43]
v_exp_f32 v73,v73
v_mfma_f32_16x16x32_bf16 v[108:111],a[184:187],v[208:211],v[108:111]
global_load_lds_dwordx4 v4,s[10:11] offset:2048
v_exp_f32 v74,v74
v_exp_f32 v75,v75
v_exp_f32 v76,v76
v_exp_f32 v77,v77
v_exp_f32 v78,v78
v_exp_f32 v79,v79
v_mfma_f32_16x16x32_bf16 v[108:111],a[180:183],v[200:203],v[108:111]
ds_read_b64_tr_b16 a[222:223],v3 offset:12544
v_cvt_pk_bf16_f32 v72,v72,v73
v_cvt_pk_bf16_f32 v73,v74,v75
v_cvt_pk_bf16_f32 v74,v76,v77
v_cvt_pk_bf16_f32 v75,v78,v79
v_mfma_f32_16x16x32_bf16 v[108:111],a[176:179],v[192:195],v[108:111]
s_waitcnt lgkmcnt(3)
ds_read_b64_tr_b16 a[224:225],v2 offset:512
ds_read_b64_tr_b16 a[226:227],v2 offset:4608
v_mfma_f32_16x16x32_bf16 a[0:3],a[192:195],v[64:67],a[0:3]
ds_read_b64_tr_b16 a[228:229],v2 offset:8704
v_mul_f32 v14,v14,v25
v_mfma_f32_16x16x32_bf16 a[0:3],a[196:199],v[72:75],a[0:3]
ds_read_b64_tr_b16 a[230:231],v2 offset:12800
v_mul_f32 v15,v15,v25
v_mfma_f32_16x16x32_bf16 a[4:7],a[200:203],v[64:67],a[4:7]
ds_read_b64_tr_b16 a[232:233],v3 offset:512
v_cvt_pk_bf16_f32 v8,v8,v9
v_mfma_f32_16x16x32_bf16 a[4:7],a[204:207],v[72:75],a[4:7]
ds_read_b64_tr_b16 a[234:235],v3 offset:4608
ds_read_b64_tr_b16 a[236:237],v3 offset:8704
v_mfma_f32_16x16x32_bf16 a[8:11],a[208:211],v[64:67],a[8:11]
ds_read_b64_tr_b16 a[238:239],v3 offset:12800
v_cvt_pk_bf16_f32 v9,v10,v11
v_mfma_f32_16x16x32_bf16 a[8:11],a[212:215],v[72:75],a[8:11]
s_waitcnt lgkmcnt(9)
ds_read_b64_tr_b16 a[240:241],v2 offset:768
v_cvt_pk_bf16_f32 v10,v12,v13
v_mfma_f32_16x16x32_bf16 a[12:15],a[216:219],v[64:67],a[12:15]
s_waitcnt lgkmcnt(9)
ds_read_b64_tr_b16 a[242:243],v2 offset:4864
v_cvt_pk_bf16_f32 v11,v14,v15
v_mfma_f32_16x16x32_bf16 a[12:15],a[220:223],v[72:75],a[12:15]
s_waitcnt lgkmcnt(8)
ds_read_b64_tr_b16 a[244:245],v2 offset:8960
ds_read_b64_tr_b16 a[246:247],v2 offset:13056
v_mfma_f32_16x16x32_bf16 a[16:19],a[224:227],v[64:67],a[16:19]
s_waitcnt lgkmcnt(8)
ds_read_b64_tr_b16 a[248:249],v3 offset:768
v_mul_f32 v16,v16,v25
v_mfma_f32_16x16x32_bf16 a[16:19],a[228:231],v[72:75],a[16:19]
s_waitcnt lgkmcnt(1)
ds_read_b64_tr_b16 a[250:251],v3 offset:4864
v_mul_f32 v17,v17,v25
v_mfma_f32_16x16x32_bf16 a[20:23],a[232:235],v[64:67],a[20:23]
s_getreg_b32 s92,hwreg(HW_REG_IB_STS,0,4)
s_min_u32 s4,s92,s6
ds_read_b64_tr_b16 a[252:253],v3 offset:8960
v_mfma_f32_16x16x32_bf16 a[20:23],a[236:239],v[72:75],a[20:23]
s_mov_b32 s6,s92
ds_read_b64_tr_b16 a[254:255],v3 offset:13056
v_mul_f32 v18,v18,v25
v_mfma_f32_16x16x32_bf16 a[24:27],a[240:243],v[64:67],a[24:27]
s_cmp_gt_u32 s4,s9
s_cselect_b32 s4,15,s5
v_mul_f32 v19,v19,v25
v_mfma_f32_16x16x32_bf16 a[24:27],a[244:247],v[72:75],a[24:27]
s_waitcnt lgkmcnt(2)
v_mul_f32 v20,v20,v25
v_mul_f32 v21,v21,v25
v_mfma_f32_16x16x32_bf16 a[28:31],a[248:251],v[64:67],a[28:31]
s_waitcnt lgkmcnt(0)
v_mul_f32 v22,v22,v25
v_mul_f32 v23,v23,v25
v_mfma_f32_16x16x32_bf16 a[28:31],a[252:255],v[72:75],a[28:31]
s_getpc_b64 s[92:93]
s_cmp_lg_u32 s4,0
s_cbranch_scc1 L_pace_%=
v_mfma_f32_16x16x32_bf16 v[48:51],v[28:31],v[64:67],v[48:51]
v_cvt_pk_bf16_f32 v12,v16,v17
v_cvt_pk_bf16_f32 v13,v18,v19
v_mfma_f32_16x16x32_bf16 v[48:51],v[28:31],v[72:75],v[48:51]
s_add_u32 s10,s24,s74
s_addc_u32 s11,s25,0
v_cvt_pk_bf16_f32 v14,v20,v21
s_waitcnt vmcnt(15)
s_barrier
v_mfma_f32_16x16x32_bf16 v[112:115],a[128:131],v[224:227],v[44:47]
s_add_u32 m0,s76,16384
v_cvt_pk_bf16_f32 v15,v22,v23
v_accvgpr_read_b32 v16,a112
v_mfma_f32_16x16x32_bf16 v[112:115],a[132:135],v[232:235],v[112:115]
)A"
R"A(s_add_u32 s74,s74,16384
v_accvgpr_read_b32 v17,a113
v_accvgpr_read_b32 v18,a114
v_mfma_f32_16x16x32_bf16 v[112:115],a[136:139],v[240:243],v[112:115]
global_load_lds_dwordx4 v5,s[10:11] offset:3072
v_accvgpr_read_b32 v19,a115
v_mfma_f32_16x16x32_bf16 v[112:115],a[140:143],v[248:251],v[112:115]
s_and_b32 s74,s74,0x3ffff
v_accvgpr_read_b32 v20,a116
v_accvgpr_read_b32 v21,a117
v_mfma_f32_16x16x32_bf16 v[116:119],a[156:159],v[248:251],v[44:47]
s_or_b32 s74,s74,s69
v_accvgpr_read_b32 v22,a118
v_accvgpr_read_b32 v23,a119
v_mfma_f32_16x16x32_bf16 v[116:119],a[152:155],v[240:243],v[116:119]
s_sub_u32 s76,s76,32768
v_mul_f32 v16,v16,v27
v_mul_f32 v17,v17,v27
v_mfma_f32_16x16x32_bf16 v[116:119],a[148:151],v[232:235],v[116:119]
s_and_b32 s76,s76,0x1ffff
v_mul_f32 v18,v18,v27
v_mul_f32 v19,v19,v27
v_mfma_f32_16x16x32_bf16 v[116:119],a[144:147],v[224:227],v[116:119]
s_and_b32 s69,s75,0xfffc0000
v_mul_f32 v20,v20,v27
v_mul_f32 v21,v21,v27
v_mfma_f32_16x16x32_bf16 v[120:123],a[160:163],v[224:227],v[44:47]
s_add_u32 s75,s75,16384
v_mul_f32 v22,v22,v27
v_mul_f32 v23,v23,v27
v_mfma_f32_16x16x32_bf16 v[120:123],a[164:167],v[232:235],v[120:123]
v_cvt_pk_bf16_f32 v16,v16,v17
v_cvt_pk_bf16_f32 v17,v18,v19
v_mfma_f32_16x16x32_bf16 v[120:123],a[168:171],v[240:243],v[120:123]
v_cvt_pk_bf16_f32 v18,v20,v21
v_cvt_pk_bf16_f32 v19,v22,v23
v_mfma_f32_16x16x32_bf16 v[120:123],a[172:175],v[248:251],v[120:123]
v_accvgpr_write_b32 a112,v16
v_accvgpr_write_b32 a113,v17
v_mfma_f32_16x16x32_bf16 v[124:127],a[188:191],v[248:251],v[44:47]
v_accvgpr_write_b32 a114,v18
v_accvgpr_write_b32 a115,v19
v_accvgpr_write_b32 a116,v8
v_accvgpr_write_b32 a117,v9
v_accvgpr_write_b32 a118,v10
v_accvgpr_write_b32 a119,v11
v_exp_f32 v80,v80
v_exp_f32 v81,v81
v_exp_f32 v82,v82
v_exp_f32 v83,v83
v_exp_f32 v84,v84
v_exp_f32 v85,v85
v_exp_f32 v86,v86
v_exp_f32 v87,v87
v_mfma_f32_16x16x32_bf16 v[124:127],a[184:187],v[240:243],v[124:127]
v_cvt_pk_bf16_f32 v80,v80,v81
v_cvt_pk_bf16_f32 v81,v82,v83
v_cvt_pk_bf16_f32 v82,v84,v85
v_cvt_pk_bf16_f32 v83,v86,v87
v_exp_f32 v88,v88
v_exp_f32 v89,v89
v_exp_f32 v90,v90
v_exp_f32 v91,v91
v_exp_f32 v92,v92
v_exp_f32 v93,v93
v_exp_f32 v94,v94
v_exp_f32 v95,v95
v_mfma_f32_16x16x32_bf16 v[124:127],a[180:183],v[232:235],v[124:127]
v_cvt_pk_bf16_f32 v88,v88,v89
v_cvt_pk_bf16_f32 v89,v90,v91
v_cvt_pk_bf16_f32 v90,v92,v93
v_cvt_pk_bf16_f32 v91,v94,v95
v_mfma_f32_16x16x32_bf16 v[124:127],a[176:179],v[224:227],v[124:127]
s_add_u32 s10,s20,s74
s_addc_u32 s11,s21,0
s_add_u32 m0,s76,0
v_mfma_f32_16x16x32_bf16 v[132:135],a[192:195],v[80:83],v[132:135]
global_load_lds_dwordx4 v4,s[10:11] nt
v_add_u32 v1,s78,v1
v_mfma_f32_16x16x32_bf16 v[132:135],a[196:199],v[88:91],v[132:135]
ds_read_b128 a[128:131],v1
v_accvgpr_read_b32 v16,a120
v_mfma_f32_16x16x32_bf16 v[140:143],a[200:203],v[80:83],v[140:143]
v_exp_f32 v96,v96
v_mfma_f32_16x16x32_bf16 v[140:143],a[204:207],v[88:91],v[140:143]
ds_read_b128 a[132:135],v1 offset:256
v_accvgpr_read_b32 v17,a121
v_mfma_f32_16x16x32_bf16 v[148:151],a[208:211],v[80:83],v[148:151]
v_exp_f32 v97,v97
v_mfma_f32_16x16x32_bf16 v[148:151],a[212:215],v[88:91],v[148:151]
ds_read_b128 a[136:139],v1 offset:512
v_cvt_pk_bf16_f32 v96,v96,v97
v_mfma_f32_16x16x32_bf16 v[156:159],a[216:219],v[80:83],v[156:159]
v_exp_f32 v98,v98
v_mfma_f32_16x16x32_bf16 v[156:159],a[220:223],v[88:91],v[156:159]
ds_read_b128 a[140:143],v1 offset:768
v_accvgpr_read_b32 v18,a122
v_mfma_f32_16x16x32_bf16 v[164:167],a[224:227],v[80:83],v[164:167]
v_exp_f32 v99,v99
v_mfma_f32_16x16x32_bf16 v[164:167],a[228:231],v[88:91],v[164:167]
ds_read_b128 a[156:159],v1 offset:4864
v_cvt_pk_bf16_f32 v97,v98,v99
v_mfma_f32_16x16x32_bf16 v[172:175],a[232:235],v[80:83],v[172:175]
v_exp_f32 v100,v100
v_mfma_f32_16x16x32_bf16 v[172:175],a[236:239],v[88:91],v[172:175]
ds_read_b128 a[152:155],v1 offset:4608
v_accvgpr_read_b32 v19,a123
v_mfma_f32_16x16x32_bf16 v[180:183],a[240:243],v[80:83],v[180:183]
v_exp_f32 v101,v101
v_mfma_f32_16x16x32_bf16 v[180:183],a[244:247],v[88:91],v[180:183]
ds_read_b128 a[148:151],v1 offset:4352
v_cvt_pk_bf16_f32 v98,v100,v101
v_mfma_f32_16x16x32_bf16 v[188:191],a[248:251],v[80:83],v[188:191]
global_load_lds_dwordx4 v4,s[10:11] offset:1024 nt
v_accvgpr_read_b32 v20,a124
v_mfma_f32_16x16x32_bf16 v[188:191],a[252:255],v[88:91],v[188:191]
ds_read_b128 a[144:147],v1 offset:4096
v_accvgpr_read_b32 v21,a125
v_mfma_f32_16x16x32_bf16 v[52:55],v[28:31],v[80:83],v[52:55]
v_exp_f32 v102,v102
v_mfma_f32_16x16x32_bf16 v[52:55],v[28:31],v[88:91],v[52:55]
s_waitcnt lgkmcnt(3)
ds_read_b128 a[160:163],v1 offset:8192
v_accvgpr_read_b32 v22,a126
v_mfma_f32_16x16x32_bf16 v[64:67],a[128:131],v[128:131],v[32:35]
v_exp_f32 v103,v103
v_mfma_f32_16x16x32_bf16 v[64:67],a[132:135],v[136:139],v[64:67]
ds_read_b128 a[164:167],v1 offset:8448
v_cvt_pk_bf16_f32 v99,v102,v103
v_mfma_f32_16x16x32_bf16 v[64:67],a[136:139],v[144:147],v[64:67]
v_exp_f32 v104,v104
v_mfma_f32_16x16x32_bf16 v[64:67],a[140:143],v[152:155],v[64:67]
ds_read_b128 a[168:171],v1 offset:8704
v_accvgpr_read_b32 v23,a127
v_mfma_f32_16x16x32_bf16 v[68:71],a[156:159],v[152:155],v[32:35]
s_waitcnt lgkmcnt(4)
v_exp_f32 v105,v105
v_mfma_f32_16x16x32_bf16 v[68:71],a[152:155],v[144:147],v[68:71]
ds_read_b128 a[172:175],v1 offset:8960
v_cvt_pk_bf16_f32 v104,v104,v105
v_mfma_f32_16x16x32_bf16 v[68:71],a[148:151],v[136:139],v[68:71]
s_waitcnt lgkmcnt(4)
v_exp_f32 v106,v106
v_mfma_f32_16x16x32_bf16 v[68:71],a[144:147],v[128:131],v[68:71]
s_waitcnt lgkmcnt(3)
ds_read_b128 a[188:191],v1 offset:13056
v_mul_f32 v16,v16,v27
v_mfma_f32_16x16x32_bf16 v[72:75],a[160:163],v[128:131],v[32:35]
s_waitcnt lgkmcnt(3)
v_exp_f32 v107,v107
v_mfma_f32_16x16x32_bf16 v[72:75],a[164:167],v[136:139],v[72:75]
s_waitcnt lgkmcnt(1)
ds_read_b128 a[184:187],v1 offset:12800
v_cvt_pk_bf16_f32 v105,v106,v107
v_mfma_f32_16x16x32_bf16 v[72:75],a[168:171],v[144:147],v[72:75]
global_load_lds_dwordx4 v5,s[10:11] offset:2048 nt
ds_read_b128 a[180:183],v1 offset:12544
v_mfma_f32_16x16x32_bf16 v[72:75],a[172:175],v[152:155],v[72:75]
s_waitcnt lgkmcnt(2)
ds_read_b128 a[176:179],v1 offset:12288
v_mul_f32 v17,v17,v27
v_mfma_f32_16x16x32_bf16 v[76:79],a[188:191],v[152:155],v[32:35]
s_waitcnt lgkmcnt(2)
v_exp_f32 v108,v108
v_mfma_f32_16x16x32_bf16 v[76:79],a[184:187],v[144:147],v[76:79]
s_waitcnt lgkmcnt(1)
v_exp_f32 v109,v109
v_exp_f32 v110,v110
v_exp_f32 v111,v111
v_mfma_f32_16x16x32_bf16 v[76:79],a[180:183],v[136:139],v[76:79]
s_waitcnt lgkmcnt(0)
v_cvt_pk_bf16_f32 v106,v108,v109
v_cvt_pk_bf16_f32 v107,v110,v111
v_mfma_f32_16x16x32_bf16 v[76:79],a[176:179],v[128:131],v[76:79]
v_mul_f32 v18,v18,v27
v_mul_f32 v19,v19,v27
v_mfma_f32_16x16x32_bf16 v[196:199],a[192:195],v[96:99],v[196:199]
v_mul_f32 v20,v20,v27
v_mul_f32 v21,v21,v27
v_mfma_f32_16x16x32_bf16 v[196:199],a[196:199],v[104:107],v[196:199]
v_mul_f32 v22,v22,v27
v_mul_f32 v23,v23,v27
v_mfma_f32_16x16x32_bf16 v[204:207],a[200:203],v[96:99],v[204:207]
v_cvt_pk_bf16_f32 v16,v16,v17
v_cvt_pk_bf16_f32 v17,v18,v19
v_mfma_f32_16x16x32_bf16 v[204:207],a[204:207],v[104:107],v[204:207]
v_cvt_pk_bf16_f32 v18,v20,v21
v_cvt_pk_bf16_f32 v19,v22,v23
v_mfma_f32_16x16x32_bf16 v[212:215],a[208:211],v[96:99],v[212:215]
v_accvgpr_write_b32 a120,v16
v_accvgpr_write_b32 a121,v17
v_mfma_f32_16x16x32_bf16 v[212:215],a[212:215],v[104:107],v[212:215]
v_accvgpr_write_b32 a122,v18
v_accvgpr_write_b32 a123,v19
v_mfma_f32_16x16x32_bf16 v[220:223],a[216:219],v[96:99],v[220:223]
v_accvgpr_write_b32 a124,v12
v_accvgpr_write_b32 a125,v13
v_mfma_f32_16x16x32_bf16 v[220:223],a[220:223],v[104:107],v[220:223]
v_accvgpr_write_b32 a126,v14
v_accvgpr_write_b32 a127,v15
v_mfma_f32_16x16x32_bf16 v[228:231],a[224:227],v[96:99],v[228:231]
global_load_lds_dwordx4 v5,s[10:11] offset:3072
v_add_u32 v2,s78,v2
v_mfma_f32_16x16x32_bf16 v[228:231],a[228:231],v[104:107],v[228:231]
v_exp_f32 v112,v112
v_mfma_f32_16x16x32_bf16 v[236:239],a[232:235],v[96:99],v[236:239]
v_exp_f32 v113,v113
v_mfma_f32_16x16x32_bf16 v[236:239],a[236:239],v[104:107],v[236:239]
v_exp_f32 v114,v114
v_mfma_f32_16x16x32_bf16 v[244:247],a[240:243],v[96:99],v[244:247]
v_exp_f32 v115,v115
v_mfma_f32_16x16x32_bf16 v[244:247],a[244:247],v[104:107],v[244:247]
v_exp_f32 v116,v116
v_mfma_f32_16x16x32_bf16 v[252:255],a[248:251],v[96:99],v[252:255]
v_exp_f32 v117,v117
v_mfma_f32_16x16x32_bf16 v[252:255],a[252:255],v[104:107],v[252:255]
v_exp_f32 v118,v118
v_mfma_f32_16x16x32_bf16 v[56:59],v[28:31],v[96:99],v[56:59]
v_exp_f32 v119,v119
v_mfma_f32_16x16x32_bf16 v[56:59],v[28:31],v[104:107],v[56:59]
v_cvt_pk_bf16_f32 v112,v112,v113
v_cvt_pk_bf16_f32 v113,v114,v115
v_mfma_f32_16x16x32_bf16 v[80:83],a[128:131],v[160:163],v[36:39]
v_cvt_pk_bf16_f32 v114,v116,v117
v_cvt_pk_bf16_f32 v115,v118,v119
v_mfma_f32_16x16x32_bf16 v[80:83],a[132:135],v[168:171],v[80:83]
s_add_u32 s10,s24,s74
s_addc_u32 s11,s25,0
v_add_u32 v3,s78,v3
v_mfma_f32_16x16x32_bf16 v[80:83],a[136:139],v[176:179],v[80:83]
s_add_u32 s74,s74,16384
v_exp_f32 v120,v120
v_mfma_f32_16x16x32_bf16 v[80:83],a[140:143],v[184:187],v[80:83]
s_add_u32 m0,s76,16384
v_exp_f32 v121,v121
v_mfma_f32_16x16x32_bf16 v[84:87],a[156:159],v[184:187],v[36:39]
s_and_b32 s74,s74,0x3ffff
v_exp_f32 v122,v122
v_mfma_f32_16x16x32_bf16 v[84:87],a[152:155],v[176:179],v[84:87]
global_load_lds_dwordx4 v4,s[10:11]
v_cvt_pk_bf16_f32 v120,v120,v121
v_mfma_f32_16x16x32_bf16 v[84:87],a[148:151],v[168:171],v[84:87]
s_or_b32 s74,s74,s69
v_exp_f32 v123,v123
v_mfma_f32_16x16x32_bf16 v[84:87],a[144:147],v[160:163],v[84:87]
s_sub_u32 s76,s76,32768
v_exp_f32 v124,v124
v_mfma_f32_16x16x32_bf16 v[88:91],a[160:163],v[160:163],v[36:39]
s_and_b32 s76,s76,0x1ffff
v_exp_f32 v125,v125
v_mfma_f32_16x16x32_bf16 v[88:91],a[164:167],v[168:171],v[88:91]
s_sub_u32 s78,s77,32768
v_exp_f32 v126,v126
v_mfma_f32_16x16x32_bf16 v[88:91],a[168:171],v[176:179],v[88:91]
s_and_b32 s78,s78,0x1ffff
v_exp_f32 v127,v127
v_mfma_f32_16x16x32_bf16 v[88:91],a[172:175],v[184:187],v[88:91]
s_sub_u32 s78,s78,s77
v_cvt_pk_bf16_f32 v121,v122,v123
v_cvt_pk_bf16_f32 v122,v124,v125
v_mfma_f32_16x16x32_bf16 v[92:95],a[188:191],v[184:187],v[36:39]
s_and_b32 s69,s75,0xfffc0000
v_cvt_pk_bf16_f32 v123,v126,v127
v_accvgpr_read_b32 v12,a100
v_mfma_f32_16x16x32_bf16 v[92:95],a[184:187],v[176:179],v[92:95]
s_add_u32 s77,s77,s78
v_exp_f32 v64,v64
v_mfma_f32_16x16x32_bf16 v[92:95],a[180:183],v[168:171],v[92:95]
s_add_u32 s75,s75,16384
v_exp_f32 v65,v65
v_mfma_f32_16x16x32_bf16 v[92:95],a[176:179],v[160:163],v[92:95]
v_exp_f32 v66,v66
v_mfma_f32_16x16x32_bf16 a[32:35],a[192:195],v[112:115],a[32:35]
ds_read_b64_tr_b16 a[192:193],v2
ds_read_b64_tr_b16 a[194:195],v2 offset:4096
v_mfma_f32_16x16x32_bf16 a[32:35],a[196:199],v[120:123],a[32:35]
ds_read_b64_tr_b16 a[196:197],v2 offset:8192
v_cvt_pk_bf16_f32 v64,v64,v65
v_mfma_f32_16x16x32_bf16 a[36:39],a[200:203],v[112:115],a[36:39]
ds_read_b64_tr_b16 a[198:199],v2 offset:12288
v_accvgpr_read_b32 v13,a101
v_mfma_f32_16x16x32_bf16 a[36:39],a[204:207],v[120:123],a[36:39]
global_load_lds_dwordx4 v5,s[10:11] offset:1024
ds_read_b64_tr_b16 a[200:201],v3
v_mfma_f32_16x16x32_bf16 a[40:43],a[208:211],v[112:115],a[40:43]
ds_read_b64_tr_b16 a[202:203],v3 offset:4096
ds_read_b64_tr_b16 a[204:205],v3 offset:8192
v_mfma_f32_16x16x32_bf16 a[40:43],a[212:215],v[120:123],a[40:43]
ds_read_b64_tr_b16 a[206:207],v3 offset:12288
v_accvgpr_read_b32 v14,a102
v_mfma_f32_16x16x32_bf16 a[44:47],a[216:219],v[112:115],a[44:47]
ds_read_b64_tr_b16 a[208:209],v2 offset:256
v_accvgpr_read_b32 v15,a103
v_mfma_f32_16x16x32_bf16 a[44:47],a[220:223],v[120:123],a[44:47]
ds_read_b64_tr_b16 a[210:211],v2 offset:4352
v_accvgpr_read_b32 v8,a96
v_mfma_f32_16x16x32_bf16 a[48:51],a[224:227],v[112:115],a[48:51]
ds_read_b64_tr_b16 a[212:213],v2 offset:8448
ds_read_b64_tr_b16 a[214:215],v2 offset:12544
v_mfma_f32_16x16x32_bf16 a[48:51],a[228:231],v[120:123],a[48:51]
v_exp_f32 v67,v67
v_mfma_f32_16x16x32_bf16 a[52:55],a[232:235],v[112:115],a[52:55]
v_exp_f32 v68,v68
v_mfma_f32_16x16x32_bf16 a[52:55],a[236:239],v[120:123],a[52:55]
v_exp_f32 v69,v69
v_mfma_f32_16x16x32_bf16 a[56:59],a[240:243],v[112:115],a[56:59]
)A"
R"A(v_exp_f32 v70,v70
v_mfma_f32_16x16x32_bf16 a[56:59],a[244:247],v[120:123],a[56:59]
v_exp_f32 v71,v71
v_mfma_f32_16x16x32_bf16 a[60:63],a[248:251],v[112:115],a[60:63]
ds_read_b64_tr_b16 a[216:217],v3 offset:256
ds_read_b64_tr_b16 a[218:219],v3 offset:4352
v_mfma_f32_16x16x32_bf16 a[60:63],a[252:255],v[120:123],a[60:63]
v_cvt_pk_bf16_f32 v65,v66,v67
v_cvt_pk_bf16_f32 v66,v68,v69
v_mfma_f32_16x16x32_bf16 v[60:63],v[28:31],v[112:115],v[60:63]
ds_read_b64_tr_b16 a[220:221],v3 offset:8448
v_cvt_pk_bf16_f32 v67,v70,v71
v_mfma_f32_16x16x32_bf16 v[60:63],v[28:31],v[120:123],v[60:63]
global_load_lds_dwordx4 v4,s[10:11] offset:2048
ds_read_b64_tr_b16 a[222:223],v3 offset:12544
v_mfma_f32_16x16x32_bf16 v[96:99],a[128:131],v[192:195],v[40:43]
v_exp_f32 v72,v72
v_mfma_f32_16x16x32_bf16 v[96:99],a[132:135],v[200:203],v[96:99]
v_exp_f32 v73,v73
v_mfma_f32_16x16x32_bf16 v[96:99],a[136:139],v[208:211],v[96:99]
v_exp_f32 v74,v74
v_mfma_f32_16x16x32_bf16 v[96:99],a[140:143],v[216:219],v[96:99]
v_exp_f32 v75,v75
v_mfma_f32_16x16x32_bf16 v[100:103],a[156:159],v[216:219],v[40:43]
v_exp_f32 v76,v76
v_mfma_f32_16x16x32_bf16 v[100:103],a[152:155],v[208:211],v[100:103]
v_exp_f32 v77,v77
v_mfma_f32_16x16x32_bf16 v[100:103],a[148:151],v[200:203],v[100:103]
v_exp_f32 v78,v78
v_mfma_f32_16x16x32_bf16 v[100:103],a[144:147],v[192:195],v[100:103]
v_exp_f32 v79,v79
v_mfma_f32_16x16x32_bf16 v[104:107],a[160:163],v[192:195],v[40:43]
v_cvt_pk_bf16_f32 v72,v72,v73
v_cvt_pk_bf16_f32 v73,v74,v75
v_mfma_f32_16x16x32_bf16 v[104:107],a[164:167],v[200:203],v[104:107]
v_cvt_pk_bf16_f32 v74,v76,v77
v_cvt_pk_bf16_f32 v75,v78,v79
v_mfma_f32_16x16x32_bf16 v[104:107],a[168:171],v[208:211],v[104:107]
ds_read_b64_tr_b16 a[224:225],v2 offset:512
ds_read_b64_tr_b16 a[226:227],v2 offset:4608
v_mfma_f32_16x16x32_bf16 v[104:107],a[172:175],v[216:219],v[104:107]
v_accvgpr_read_b32 v9,a97
v_accvgpr_read_b32 v10,a98
v_mfma_f32_16x16x32_bf16 v[108:111],a[188:191],v[216:219],v[40:43]
v_accvgpr_read_b32 v11,a99
v_mul_f32 v8,v8,v27
v_mfma_f32_16x16x32_bf16 v[108:111],a[184:187],v[208:211],v[108:111]
global_load_lds_dwordx4 v5,s[10:11] offset:3072
v_mul_f32 v9,v9,v27
v_mfma_f32_16x16x32_bf16 v[108:111],a[180:183],v[200:203],v[108:111]
v_mul_f32 v10,v10,v27
v_mul_f32 v11,v11,v27
v_mfma_f32_16x16x32_bf16 v[108:111],a[176:179],v[192:195],v[108:111]
s_waitcnt lgkmcnt(2)
v_mul_f32 v12,v12,v27
v_mul_f32 v13,v13,v27
v_mul_f32 v14,v14,v27
v_mul_f32 v15,v15,v27
v_cvt_pk_bf16_f32 v8,v8,v9
v_cvt_pk_bf16_f32 v9,v10,v11
v_cvt_pk_bf16_f32 v10,v12,v13
v_cvt_pk_bf16_f32 v11,v14,v15
v_accvgpr_write_b32 a96,v8
v_accvgpr_write_b32 a97,v9
v_accvgpr_write_b32 a98,v10
v_accvgpr_write_b32 a99,v11
v_mfma_f32_16x16x32_bf16 a[0:3],a[192:195],v[64:67],a[0:3]
ds_read_b128 a[100:103],v7 offset:2048
v_add_u32 v1,s78,v1
v_mfma_f32_16x16x32_bf16 a[0:3],a[196:199],v[72:75],a[0:3]
ds_read_b64_tr_b16 a[228:229],v2 offset:8704
v_accvgpr_read_b32 v20,a108
v_mfma_f32_16x16x32_bf16 a[4:7],a[200:203],v[64:67],a[4:7]
ds_read_b64_tr_b16 a[230:231],v2 offset:12800
v_accvgpr_read_b32 v21,a109
v_mfma_f32_16x16x32_bf16 a[4:7],a[204:207],v[72:75],a[4:7]
ds_read_b64_tr_b16 a[232:233],v3 offset:512
v_accvgpr_read_b32 v22,a110
v_mfma_f32_16x16x32_bf16 a[8:11],a[208:211],v[64:67],a[8:11]
ds_read_b64_tr_b16 a[234:235],v3 offset:4608
ds_read_b64_tr_b16 a[236:237],v3 offset:8704
v_mfma_f32_16x16x32_bf16 a[8:11],a[212:215],v[72:75],a[8:11]
ds_read_b64_tr_b16 a[238:239],v3 offset:12800
v_accvgpr_read_b32 v23,a111
v_mfma_f32_16x16x32_bf16 a[12:15],a[216:219],v[64:67],a[12:15]
ds_read_b64_tr_b16 a[240:241],v2 offset:768
v_accvgpr_read_b32 v16,a104
v_mfma_f32_16x16x32_bf16 a[12:15],a[220:223],v[72:75],a[12:15]
s_waitcnt lgkmcnt(8)
ds_read_b64_tr_b16 a[242:243],v2 offset:4864
v_accvgpr_read_b32 v17,a105
v_mfma_f32_16x16x32_bf16 a[16:19],a[224:227],v[64:67],a[16:19]
s_waitcnt lgkmcnt(6)
ds_read_b64_tr_b16 a[244:245],v2 offset:8960
ds_read_b64_tr_b16 a[246:247],v2 offset:13056
v_mfma_f32_16x16x32_bf16 a[16:19],a[228:231],v[72:75],a[16:19]
s_waitcnt lgkmcnt(0)
ds_read_b64_tr_b16 a[248:249],v3 offset:768
v_accvgpr_read_b32 v18,a106
v_mfma_f32_16x16x32_bf16 a[20:23],a[232:235],v[64:67],a[20:23]
s_getreg_b32 s92,hwreg(HW_REG_IB_STS,0,4)
s_min_u32 s4,s92,s6
ds_read_b64_tr_b16 a[250:251],v3 offset:4864
v_mfma_f32_16x16x32_bf16 a[20:23],a[236:239],v[72:75],a[20:23]
s_mov_b32 s6,s92
ds_read_b64_tr_b16 a[252:253],v3 offset:8960
ds_read_b64_tr_b16 a[254:255],v3 offset:13056
v_mfma_f32_16x16x32_bf16 a[24:27],a[240:243],v[64:67],a[24:27]
s_cmp_gt_u32 s4,s9
s_cselect_b32 s4,15,s5
v_accvgpr_read_b32 v19,a107
v_mfma_f32_16x16x32_bf16 a[24:27],a[244:247],v[72:75],a[24:27]
s_waitcnt lgkmcnt(2)
v_exp_f32 v80,v80
v_mfma_f32_16x16x32_bf16 a[28:31],a[248:251],v[64:67],a[28:31]
s_waitcnt lgkmcnt(0)
v_exp_f32 v81,v81
v_mfma_f32_16x16x32_bf16 a[28:31],a[252:255],v[72:75],a[28:31]
s_getpc_b64 s[92:93]
s_cmp_lg_u32 s4,0
s_cbranch_scc1 L_pace_%=
v_mfma_f32_16x16x32_bf16 v[48:51],v[28:31],v[64:67],v[48:51]
v_exp_f32 v82,v82
v_mfma_f32_16x16x32_bf16 v[48:51],v[28:31],v[72:75],v[48:51]
s_add_u32 s10,s20,s74
s_addc_u32 s11,s21,0
v_cvt_pk_bf16_f32 v80,v80,v81
s_waitcnt vmcnt(16)
s_barrier
v_mfma_f32_16x16x32_bf16 v[112:115],a[128:131],v[224:227],v[44:47]
s_add_u32 m0,s76,0
v_exp_f32 v83,v83
v_mfma_f32_16x16x32_bf16 v[112:115],a[132:135],v[232:235],v[112:115]
v_exp_f32 v84,v84
v_mfma_f32_16x16x32_bf16 v[112:115],a[136:139],v[240:243],v[112:115]
global_load_lds_dwordx4 v4,s[10:11] nt
v_cvt_pk_bf16_f32 v81,v82,v83
v_mfma_f32_16x16x32_bf16 v[112:115],a[140:143],v[248:251],v[112:115]
v_exp_f32 v85,v85
v_mfma_f32_16x16x32_bf16 v[116:119],a[156:159],v[248:251],v[44:47]
v_exp_f32 v86,v86
v_mfma_f32_16x16x32_bf16 v[116:119],a[152:155],v[240:243],v[116:119]
v_exp_f32 v87,v87
v_mfma_f32_16x16x32_bf16 v[116:119],a[148:151],v[232:235],v[116:119]
v_cvt_pk_bf16_f32 v82,v84,v85
v_cvt_pk_bf16_f32 v83,v86,v87
v_mfma_f32_16x16x32_bf16 v[116:119],a[144:147],v[224:227],v[116:119]
v_exp_f32 v88,v88
v_mfma_f32_16x16x32_bf16 v[120:123],a[160:163],v[224:227],v[44:47]
v_exp_f32 v89,v89
v_mfma_f32_16x16x32_bf16 v[120:123],a[164:167],v[232:235],v[120:123]
v_exp_f32 v90,v90
v_mfma_f32_16x16x32_bf16 v[120:123],a[168:171],v[240:243],v[120:123]
v_exp_f32 v91,v91
v_mfma_f32_16x16x32_bf16 v[120:123],a[172:175],v[248:251],v[120:123]
v_exp_f32 v92,v92
v_mfma_f32_16x16x32_bf16 v[124:127],a[188:191],v[248:251],v[44:47]
v_exp_f32 v93,v93
v_mfma_f32_16x16x32_bf16 v[124:127],a[184:187],v[240:243],v[124:127]
v_exp_f32 v94,v94
v_exp_f32 v95,v95
v_mfma_f32_16x16x32_bf16 v[124:127],a[180:183],v[232:235],v[124:127]
v_cvt_pk_bf16_f32 v88,v88,v89
v_cvt_pk_bf16_f32 v89,v90,v91
v_cvt_pk_bf16_f32 v90,v92,v93
v_cvt_pk_bf16_f32 v91,v94,v95
v_mfma_f32_16x16x32_bf16 v[124:127],a[176:179],v[224:227],v[124:127]
ds_read_b128 a[128:131],v1
v_mul_f32 v16,v16,v27
v_mfma_f32_16x16x32_bf16 v[132:135],a[192:195],v[80:83],v[132:135]
v_mul_f32 v17,v17,v27
v_mul_f32 v18,v18,v27
v_mfma_f32_16x16x32_bf16 v[132:135],a[196:199],v[88:91],v[132:135]
ds_read_b128 a[132:135],v1 offset:256
v_mul_f32 v19,v19,v27
v_mfma_f32_16x16x32_bf16 v[140:143],a[200:203],v[80:83],v[140:143]
global_load_lds_dwordx4 v4,s[10:11] offset:1024 nt
v_mul_f32 v20,v20,v27
v_mfma_f32_16x16x32_bf16 v[140:143],a[204:207],v[88:91],v[140:143]
ds_read_b128 a[136:139],v1 offset:512
v_mul_f32 v21,v21,v27
v_mfma_f32_16x16x32_bf16 v[148:151],a[208:211],v[80:83],v[148:151]
v_mul_f32 v22,v22,v27
v_mul_f32 v23,v23,v27
v_mfma_f32_16x16x32_bf16 v[148:151],a[212:215],v[88:91],v[148:151]
ds_read_b128 a[140:143],v1 offset:768
v_cvt_pk_bf16_f32 v16,v16,v17
v_mfma_f32_16x16x32_bf16 v[156:159],a[216:219],v[80:83],v[156:159]
v_cvt_pk_bf16_f32 v17,v18,v19
v_cvt_pk_bf16_f32 v18,v20,v21
v_mfma_f32_16x16x32_bf16 v[156:159],a[220:223],v[88:91],v[156:159]
ds_read_b128 a[156:159],v1 offset:4864
v_cvt_pk_bf16_f32 v19,v22,v23
v_mfma_f32_16x16x32_bf16 v[164:167],a[224:227],v[80:83],v[164:167]
v_accvgpr_write_b32 a104,v16
v_accvgpr_write_b32 a105,v17
v_mfma_f32_16x16x32_bf16 v[164:167],a[228:231],v[88:91],v[164:167]
ds_read_b128 a[152:155],v1 offset:4608
v_accvgpr_write_b32 a106,v18
v_mfma_f32_16x16x32_bf16 v[172:175],a[232:235],v[80:83],v[172:175]
v_accvgpr_write_b32 a107,v19
v_accvgpr_read_b32 v12,a68
v_mfma_f32_16x16x32_bf16 v[172:175],a[236:239],v[88:91],v[172:175]
ds_read_b128 a[148:151],v1 offset:4352
s_nop 0
L_pd9_%=:
s_cbranch_execz L_pd13_%=
v_accvgpr_read_b32 v13,a69
v_mfma_f32_16x16x32_bf16 v[180:183],a[240:243],v[80:83],v[180:183]
v_exp_f32 v96,v96
v_mfma_f32_16x16x32_bf16 v[180:183],a[244:247],v[88:91],v[180:183]
ds_read_b128 a[144:147],v1 offset:4096
v_accvgpr_read_b32 v14,a70
v_mfma_f32_16x16x32_bf16 v[188:191],a[248:251],v[80:83],v[188:191]
v_exp_f32 v97,v97
v_mfma_f32_16x16x32_bf16 v[188:191],a[252:255],v[88:91],v[188:191]
ds_read_b128 a[160:163],v1 offset:8192
v_cvt_pk_bf16_f32 v96,v96,v97
v_mfma_f32_16x16x32_bf16 v[52:55],v[28:31],v[80:83],v[52:55]
v_exp_f32 v98,v98
v_mfma_f32_16x16x32_bf16 v[52:55],v[28:31],v[88:91],v[52:55]
s_waitcnt lgkmcnt(3)
ds_read_b128 a[164:167],v1 offset:8448
v_accvgpr_read_b32 v15,a71
v_mfma_f32_16x16x32_bf16 v[64:67],a[128:131],v[128:131],v[32:35]
global_load_lds_dwordx4 v5,s[10:11] offset:2048 nt
v_accvgpr_read_b32 v8,a64
v_mfma_f32_16x16x32_bf16 v[64:67],a[132:135],v[136:139],v[64:67]
ds_read_b128 a[168:171],v1 offset:8704
v_accvgpr_read_b32 v9,a65
v_mfma_f32_16x16x32_bf16 v[64:67],a[136:139],v[144:147],v[64:67]
v_exp_f32 v99,v99
v_mfma_f32_16x16x32_bf16 v[64:67],a[140:143],v[152:155],v[64:67]
ds_read_b128 a[108:111],v7 offset:3072
v_cvt_pk_bf16_f32 v97,v98,v99
v_mfma_f32_16x16x32_bf16 v[68:71],a[156:159],v[152:155],v[32:35]
v_exp_f32 v100,v100
v_mfma_f32_16x16x32_bf16 v[68:71],a[152:155],v[144:147],v[68:71]
s_waitcnt lgkmcnt(3)
ds_read_b128 a[172:175],v1 offset:8960
v_accvgpr_read_b32 v10,a66
v_mfma_f32_16x16x32_bf16 v[68:71],a[148:151],v[136:139],v[68:71]
v_exp_f32 v101,v101
v_mfma_f32_16x16x32_bf16 v[68:71],a[144:147],v[128:131],v[68:71]
ds_read_b128 a[188:191],v1 offset:13056
v_cvt_pk_bf16_f32 v98,v100,v101
v_mfma_f32_16x16x32_bf16 v[72:75],a[160:163],v[128:131],v[32:35]
s_waitcnt lgkmcnt(4)
v_exp_f32 v102,v102
v_mfma_f32_16x16x32_bf16 v[72:75],a[164:167],v[136:139],v[72:75]
s_waitcnt lgkmcnt(3)
ds_read_b128 a[184:187],v1 offset:12800
v_accvgpr_read_b32 v11,a67
v_mfma_f32_16x16x32_bf16 v[72:75],a[168:171],v[144:147],v[72:75]
s_waitcnt lgkmcnt(2)
ds_read_b128 a[180:183],v1 offset:12544
v_mul_f32 v8,v8,v26
v_mfma_f32_16x16x32_bf16 v[72:75],a[172:175],v[152:155],v[72:75]
s_waitcnt lgkmcnt(2)
ds_read_b128 a[176:179],v1 offset:12288
v_mul_f32 v9,v9,v26
v_mfma_f32_16x16x32_bf16 v[76:79],a[188:191],v[152:155],v[32:35]
s_waitcnt lgkmcnt(2)
v_exp_f32 v103,v103
v_mfma_f32_16x16x32_bf16 v[76:79],a[184:187],v[144:147],v[76:79]
s_waitcnt lgkmcnt(1)
v_cvt_pk_bf16_f32 v99,v102,v103
v_exp_f32 v104,v104
v_exp_f32 v105,v105
v_exp_f32 v106,v106
v_exp_f32 v107,v107
v_exp_f32 v108,v108
v_exp_f32 v109,v109
v_exp_f32 v110,v110
v_exp_f32 v111,v111
v_mfma_f32_16x16x32_bf16 v[76:79],a[180:183],v[136:139],v[76:79]
s_waitcnt lgkmcnt(0)
v_cvt_pk_bf16_f32 v104,v104,v105
v_cvt_pk_bf16_f32 v105,v106,v107
v_cvt_pk_bf16_f32 v106,v108,v109
v_cvt_pk_bf16_f32 v107,v110,v111
v_mfma_f32_16x16x32_bf16 v[76:79],a[176:179],v[128:131],v[76:79]
v_exp_f32 v112,v112
v_mfma_f32_16x16x32_bf16 v[196:199],a[192:195],v[96:99],v[196:199]
global_load_lds_dwordx4 v5,s[10:11] offset:3072
v_mul_f32 v10,v10,v26
v_mfma_f32_16x16x32_bf16 v[196:199],a[196:199],v[104:107],v[196:199]
v_exp_f32 v113,v113
v_mfma_f32_16x16x32_bf16 v[204:207],a[200:203],v[96:99],v[204:207]
v_exp_f32 v114,v114
v_mfma_f32_16x16x32_bf16 v[204:207],a[204:207],v[104:107],v[204:207]
v_exp_f32 v115,v115
v_mfma_f32_16x16x32_bf16 v[212:215],a[208:211],v[96:99],v[212:215]
v_exp_f32 v116,v116
v_mfma_f32_16x16x32_bf16 v[212:215],a[212:215],v[104:107],v[212:215]
v_exp_f32 v117,v117
v_mfma_f32_16x16x32_bf16 v[220:223],a[216:219],v[96:99],v[220:223]
v_exp_f32 v118,v118
v_mfma_f32_16x16x32_bf16 v[220:223],a[220:223],v[104:107],v[220:223]
v_exp_f32 v119,v119
v_mfma_f32_16x16x32_bf16 v[228:231],a[224:227],v[96:99],v[228:231]
)A"
R"A(v_cvt_pk_bf16_f32 v112,v112,v113
v_cvt_pk_bf16_f32 v113,v114,v115
v_mfma_f32_16x16x32_bf16 v[228:231],a[228:231],v[104:107],v[228:231]
v_cvt_pk_bf16_f32 v114,v116,v117
v_cvt_pk_bf16_f32 v115,v118,v119
v_mfma_f32_16x16x32_bf16 v[236:239],a[232:235],v[96:99],v[236:239]
v_exp_f32 v120,v120
v_mfma_f32_16x16x32_bf16 v[236:239],a[236:239],v[104:107],v[236:239]
v_exp_f32 v121,v121
v_mfma_f32_16x16x32_bf16 v[244:247],a[240:243],v[96:99],v[244:247]
s_add_u32 s10,s24,s74
s_addc_u32 s11,s25,0
v_cvt_pk_bf16_f32 v120,v120,v121
v_mfma_f32_16x16x32_bf16 v[244:247],a[244:247],v[104:107],v[244:247]
s_add_u32 s74,s74,16384
v_exp_f32 v122,v122
v_mfma_f32_16x16x32_bf16 v[252:255],a[248:251],v[96:99],v[252:255]
s_add_u32 m0,s76,16384
v_exp_f32 v123,v123
v_mfma_f32_16x16x32_bf16 v[252:255],a[252:255],v[104:107],v[252:255]
s_and_b32 s74,s74,0x3ffff
v_exp_f32 v124,v124
v_mfma_f32_16x16x32_bf16 v[56:59],v[28:31],v[96:99],v[56:59]
global_load_lds_dwordx4 v4,s[10:11]
v_cvt_pk_bf16_f32 v121,v122,v123
v_mfma_f32_16x16x32_bf16 v[56:59],v[28:31],v[104:107],v[56:59]
s_or_b32 s74,s74,s69
v_exp_f32 v125,v125
v_mfma_f32_16x16x32_bf16 v[80:83],a[128:131],v[160:163],v[36:39]
s_sub_u32 s76,s76,32768
v_exp_f32 v126,v126
v_mfma_f32_16x16x32_bf16 v[80:83],a[132:135],v[168:171],v[80:83]
s_and_b32 s76,s76,0x1ffff
v_exp_f32 v127,v127
v_mfma_f32_16x16x32_bf16 v[80:83],a[136:139],v[176:179],v[80:83]
s_and_b32 s69,s75,0xfffc0000
v_cvt_pk_bf16_f32 v122,v124,v125
v_cvt_pk_bf16_f32 v123,v126,v127
v_mfma_f32_16x16x32_bf16 v[80:83],a[140:143],v[184:187],v[80:83]
v_mul_f32 v11,v11,v26
v_mul_f32 v12,v12,v26
v_mfma_f32_16x16x32_bf16 v[84:87],a[156:159],v[184:187],v[36:39]
v_mul_f32 v13,v13,v26
v_mul_f32 v14,v14,v26
v_mfma_f32_16x16x32_bf16 v[84:87],a[152:155],v[176:179],v[84:87]
v_mul_f32 v15,v15,v26
v_cvt_pk_bf16_f32 v8,v8,v9
v_mfma_f32_16x16x32_bf16 v[84:87],a[148:151],v[168:171],v[84:87]
v_cvt_pk_bf16_f32 v9,v10,v11
v_cvt_pk_bf16_f32 v10,v12,v13
v_mfma_f32_16x16x32_bf16 v[84:87],a[144:147],v[160:163],v[84:87]
v_cvt_pk_bf16_f32 v11,v14,v15
v_accvgpr_write_b32 a64,v8
v_mfma_f32_16x16x32_bf16 v[88:91],a[160:163],v[160:163],v[36:39]
v_accvgpr_write_b32 a65,v9
v_accvgpr_write_b32 a66,v10
v_mfma_f32_16x16x32_bf16 v[88:91],a[164:167],v[168:171],v[88:91]
ds_read_b128 a[68:71],v7 offset:6144
v_accvgpr_write_b32 a67,v11
v_mfma_f32_16x16x32_bf16 v[88:91],a[168:171],v[176:179],v[88:91]
v_add_u32 v2,s78,v2
v_add_u32 v3,s78,v3
v_mfma_f32_16x16x32_bf16 v[88:91],a[172:175],v[184:187],v[88:91]
s_sub_u32 s78,s77,32768
v_exp_f32 v64,v64
v_mfma_f32_16x16x32_bf16 v[92:95],a[188:191],v[184:187],v[36:39]
s_and_b32 s78,s78,0x1ffff
v_exp_f32 v65,v65
v_mfma_f32_16x16x32_bf16 v[92:95],a[184:187],v[176:179],v[92:95]
s_sub_u32 s78,s78,s77
v_exp_f32 v66,v66
v_mfma_f32_16x16x32_bf16 v[92:95],a[180:183],v[168:171],v[92:95]
global_load_lds_dwordx4 v5,s[10:11] offset:1024
v_cvt_pk_bf16_f32 v64,v64,v65
v_mfma_f32_16x16x32_bf16 v[92:95],a[176:179],v[160:163],v[92:95]
s_add_u32 s77,s77,s78
v_exp_f32 v67,v67
v_mfma_f32_16x16x32_bf16 a[32:35],a[192:195],v[112:115],a[32:35]
ds_read_b64_tr_b16 a[192:193],v2
ds_read_b64_tr_b16 a[194:195],v2 offset:4096
v_mfma_f32_16x16x32_bf16 a[32:35],a[196:199],v[120:123],a[32:35]
ds_read_b64_tr_b16 a[196:197],v2 offset:8192
v_cvt_pk_bf16_f32 v65,v66,v67
v_mfma_f32_16x16x32_bf16 a[36:39],a[200:203],v[112:115],a[36:39]
ds_read_b64_tr_b16 a[198:199],v2 offset:12288
v_accvgpr_read_b32 v20,a76
v_mfma_f32_16x16x32_bf16 a[36:39],a[204:207],v[120:123],a[36:39]
ds_read_b64_tr_b16 a[200:201],v3
v_accvgpr_read_b32 v21,a77
v_mfma_f32_16x16x32_bf16 a[40:43],a[208:211],v[112:115],a[40:43]
ds_read_b64_tr_b16 a[202:203],v3 offset:4096
ds_read_b64_tr_b16 a[204:205],v3 offset:8192
v_mfma_f32_16x16x32_bf16 a[40:43],a[212:215],v[120:123],a[40:43]
ds_read_b64_tr_b16 a[206:207],v3 offset:12288
v_accvgpr_read_b32 v22,a78
v_mfma_f32_16x16x32_bf16 a[44:47],a[216:219],v[112:115],a[44:47]
ds_read_b64_tr_b16 a[208:209],v2 offset:256
v_accvgpr_read_b32 v23,a79
v_mfma_f32_16x16x32_bf16 a[44:47],a[220:223],v[120:123],a[44:47]
ds_read_b64_tr_b16 a[210:211],v2 offset:4352
v_accvgpr_read_b32 v16,a72
v_mfma_f32_16x16x32_bf16 a[48:51],a[224:227],v[112:115],a[48:51]
ds_read_b64_tr_b16 a[212:213],v2 offset:8448
ds_read_b64_tr_b16 a[214:215],v2 offset:12544
v_mfma_f32_16x16x32_bf16 a[48:51],a[228:231],v[120:123],a[48:51]
v_exp_f32 v68,v68
v_mfma_f32_16x16x32_bf16 a[52:55],a[232:235],v[112:115],a[52:55]
v_exp_f32 v69,v69
v_mfma_f32_16x16x32_bf16 a[52:55],a[236:239],v[120:123],a[52:55]
v_exp_f32 v70,v70
v_mfma_f32_16x16x32_bf16 a[56:59],a[240:243],v[112:115],a[56:59]
v_exp_f32 v71,v71
v_mfma_f32_16x16x32_bf16 a[56:59],a[244:247],v[120:123],a[56:59]
ds_read_b64_tr_b16 a[216:217],v3 offset:256
ds_read_b64_tr_b16 a[218:219],v3 offset:4352
v_mfma_f32_16x16x32_bf16 a[60:63],a[248:251],v[112:115],a[60:63]
global_load_lds_dwordx4 v4,s[10:11] offset:2048
v_cvt_pk_bf16_f32 v66,v68,v69
v_mfma_f32_16x16x32_bf16 a[60:63],a[252:255],v[120:123],a[60:63]
ds_read_b64_tr_b16 a[220:221],v3 offset:8448
v_cvt_pk_bf16_f32 v67,v70,v71
v_mfma_f32_16x16x32_bf16 v[60:63],v[28:31],v[112:115],v[60:63]
v_exp_f32 v72,v72
v_mfma_f32_16x16x32_bf16 v[60:63],v[28:31],v[120:123],v[60:63]
v_exp_f32 v73,v73
v_mfma_f32_16x16x32_bf16 v[96:99],a[128:131],v[192:195],v[40:43]
v_exp_f32 v74,v74
v_mfma_f32_16x16x32_bf16 v[96:99],a[132:135],v[200:203],v[96:99]
v_exp_f32 v75,v75
v_mfma_f32_16x16x32_bf16 v[96:99],a[136:139],v[208:211],v[96:99]
v_exp_f32 v76,v76
v_mfma_f32_16x16x32_bf16 v[96:99],a[140:143],v[216:219],v[96:99]
v_exp_f32 v77,v77
v_mfma_f32_16x16x32_bf16 v[100:103],a[156:159],v[216:219],v[40:43]
v_exp_f32 v78,v78
v_mfma_f32_16x16x32_bf16 v[100:103],a[152:155],v[208:211],v[100:103]
v_exp_f32 v79,v79
v_mfma_f32_16x16x32_bf16 v[100:103],a[148:151],v[200:203],v[100:103]
ds_read_b64_tr_b16 a[222:223],v3 offset:12544
v_cvt_pk_bf16_f32 v72,v72,v73
v_mfma_f32_16x16x32_bf16 v[100:103],a[144:147],v[192:195],v[100:103]
v_cvt_pk_bf16_f32 v73,v74,v75
v_cvt_pk_bf16_f32 v74,v76,v77
v_mfma_f32_16x16x32_bf16 v[104:107],a[160:163],v[192:195],v[40:43]
ds_read_b64_tr_b16 a[224:225],v2 offset:512
v_cvt_pk_bf16_f32 v75,v78,v79
v_mfma_f32_16x16x32_bf16 v[104:107],a[164:167],v[200:203],v[104:107]
ds_read_b64_tr_b16 a[226:227],v2 offset:4608
ds_read_b64_tr_b16 a[228:229],v2 offset:8704
v_mfma_f32_16x16x32_bf16 v[104:107],a[168:171],v[208:211],v[104:107]
ds_read_b64_tr_b16 a[230:231],v2 offset:12800
v_accvgpr_read_b32 v17,a73
v_mfma_f32_16x16x32_bf16 v[104:107],a[172:175],v[216:219],v[104:107]
ds_read_b64_tr_b16 a[232:233],v3 offset:512
v_accvgpr_read_b32 v18,a74
v_mfma_f32_16x16x32_bf16 v[108:111],a[188:191],v[216:219],v[40:43]
global_load_lds_dwordx4 v5,s[10:11] offset:3072
ds_read_b64_tr_b16 a[234:235],v3 offset:4608
v_mfma_f32_16x16x32_bf16 v[108:111],a[184:187],v[208:211],v[108:111]
ds_read_b64_tr_b16 a[236:237],v3 offset:8704
ds_read_b64_tr_b16 a[238:239],v3 offset:12800
v_mfma_f32_16x16x32_bf16 v[108:111],a[180:183],v[200:203],v[108:111]
ds_read_b64_tr_b16 a[240:241],v2 offset:768
v_accvgpr_read_b32 v19,a75
v_mfma_f32_16x16x32_bf16 v[108:111],a[176:179],v[192:195],v[108:111]
s_waitcnt lgkmcnt(9)
ds_read_b64_tr_b16 a[242:243],v2 offset:4864
v_mul_f32 v16,v16,v26
v_mfma_f32_16x16x32_bf16 a[0:3],a[192:195],v[64:67],a[0:3]
ds_read_b64_tr_b16 a[244:245],v2 offset:8960
v_mul_f32 v17,v17,v26
v_mfma_f32_16x16x32_bf16 a[0:3],a[196:199],v[72:75],a[0:3]
ds_read_b64_tr_b16 a[246:247],v2 offset:13056
ds_read_b64_tr_b16 a[248:249],v3 offset:768
v_mfma_f32_16x16x32_bf16 a[4:7],a[200:203],v[64:67],a[4:7]
ds_read_b64_tr_b16 a[250:251],v3 offset:4864
v_mul_f32 v18,v18,v26
v_mfma_f32_16x16x32_bf16 a[4:7],a[204:207],v[72:75],a[4:7]
ds_read_b64_tr_b16 a[252:253],v3 offset:8960
v_mul_f32 v19,v19,v26
v_mfma_f32_16x16x32_bf16 a[8:11],a[208:211],v[64:67],a[8:11]
ds_read_b64_tr_b16 a[254:255],v3 offset:13056
v_mul_f32 v20,v20,v26
v_mfma_f32_16x16x32_bf16 a[8:11],a[212:215],v[72:75],a[8:11]
v_exp_f32 v80,v80
v_mfma_f32_16x16x32_bf16 a[12:15],a[216:219],v[64:67],a[12:15]
v_exp_f32 v81,v81
v_mfma_f32_16x16x32_bf16 a[12:15],a[220:223],v[72:75],a[12:15]
s_waitcnt lgkmcnt(6)
v_exp_f32 v82,v82
v_mfma_f32_16x16x32_bf16 a[16:19],a[224:227],v[64:67],a[16:19]
v_exp_f32 v83,v83
v_mfma_f32_16x16x32_bf16 a[16:19],a[228:231],v[72:75],a[16:19]
v_exp_f32 v84,v84
v_mfma_f32_16x16x32_bf16 a[20:23],a[232:235],v[64:67],a[20:23]
s_getreg_b32 s92,hwreg(HW_REG_IB_STS,0,4)
s_min_u32 s4,s92,s6
s_mov_b32 s6,s92
v_mfma_f32_16x16x32_bf16 a[20:23],a[236:239],v[72:75],a[20:23]
s_cmp_ge_u32 s4,s9
s_cselect_b32 s4,15,s5
v_cvt_pk_bf16_f32 v80,v80,v81
v_mfma_f32_16x16x32_bf16 a[24:27],a[240:243],v[64:67],a[24:27]
s_waitcnt lgkmcnt(0)
v_exp_f32 v85,v85
v_mfma_f32_16x16x32_bf16 a[24:27],a[244:247],v[72:75],a[24:27]
v_exp_f32 v86,v86
v_mfma_f32_16x16x32_bf16 a[28:31],a[248:251],v[64:67],a[28:31]
v_exp_f32 v87,v87
v_mfma_f32_16x16x32_bf16 a[28:31],a[252:255],v[72:75],a[28:31]
s_getpc_b64 s[92:93]
s_cmp_lg_u32 s4,0
s_cbranch_scc1 L_pace_%=
v_mfma_f32_16x16x32_bf16 v[48:51],v[28:31],v[64:67],v[48:51]
v_cvt_pk_bf16_f32 v81,v82,v83
v_cvt_pk_bf16_f32 v82,v84,v85
v_mfma_f32_16x16x32_bf16 v[48:51],v[28:31],v[72:75],v[48:51]
s_add_u32 s10,s20,s74
s_addc_u32 s11,s21,0
v_cvt_pk_bf16_f32 v83,v86,v87
s_waitcnt vmcnt(16)
s_barrier
v_mfma_f32_16x16x32_bf16 v[112:115],a[128:131],v[224:227],v[44:47]
s_add_u32 m0,s76,0
v_exp_f32 v88,v88
v_mfma_f32_16x16x32_bf16 v[112:115],a[132:135],v[232:235],v[112:115]
v_exp_f32 v89,v89
v_mfma_f32_16x16x32_bf16 v[112:115],a[136:139],v[240:243],v[112:115]
global_load_lds_dwordx4 v4,s[10:11] nt
v_cvt_pk_bf16_f32 v88,v88,v89
v_mfma_f32_16x16x32_bf16 v[112:115],a[140:143],v[248:251],v[112:115]
v_exp_f32 v90,v90
v_mfma_f32_16x16x32_bf16 v[116:119],a[156:159],v[248:251],v[44:47]
v_exp_f32 v91,v91
v_mfma_f32_16x16x32_bf16 v[116:119],a[152:155],v[240:243],v[116:119]
v_exp_f32 v92,v92
v_mfma_f32_16x16x32_bf16 v[116:119],a[148:151],v[232:235],v[116:119]
v_exp_f32 v93,v93
v_mfma_f32_16x16x32_bf16 v[116:119],a[144:147],v[224:227],v[116:119]
v_exp_f32 v94,v94
v_mfma_f32_16x16x32_bf16 v[120:123],a[160:163],v[224:227],v[44:47]
v_exp_f32 v95,v95
v_mfma_f32_16x16x32_bf16 v[120:123],a[164:167],v[232:235],v[120:123]
v_cvt_pk_bf16_f32 v89,v90,v91
v_cvt_pk_bf16_f32 v90,v92,v93
v_mfma_f32_16x16x32_bf16 v[120:123],a[168:171],v[240:243],v[120:123]
v_cvt_pk_bf16_f32 v91,v94,v95
v_mul_f32 v21,v21,v26
v_mfma_f32_16x16x32_bf16 v[120:123],a[172:175],v[248:251],v[120:123]
v_mul_f32 v22,v22,v26
v_mul_f32 v23,v23,v26
v_mfma_f32_16x16x32_bf16 v[124:127],a[188:191],v[248:251],v[44:47]
v_cvt_pk_bf16_f32 v16,v16,v17
v_cvt_pk_bf16_f32 v17,v18,v19
v_mfma_f32_16x16x32_bf16 v[124:127],a[184:187],v[240:243],v[124:127]
v_cvt_pk_bf16_f32 v18,v20,v21
v_cvt_pk_bf16_f32 v19,v22,v23
v_mfma_f32_16x16x32_bf16 v[124:127],a[180:183],v[232:235],v[124:127]
v_accvgpr_write_b32 a72,v16
v_accvgpr_write_b32 a73,v17
v_mfma_f32_16x16x32_bf16 v[124:127],a[176:179],v[224:227],v[124:127]
v_accvgpr_write_b32 a74,v18
v_accvgpr_write_b32 a75,v19
v_mfma_f32_16x16x32_bf16 v[132:135],a[192:195],v[80:83],v[132:135]
ds_read_b128 a[76:79],v7 offset:7168
v_add_u32 v1,s78,v1
v_mfma_f32_16x16x32_bf16 v[132:135],a[196:199],v[88:91],v[132:135]
v_exp_f32 v96,v96
v_mfma_f32_16x16x32_bf16 v[140:143],a[200:203],v[80:83],v[140:143]
global_load_lds_dwordx4 v4,s[10:11] offset:1024 nt
ds_read_b128 a[128:131],v1
v_mfma_f32_16x16x32_bf16 v[140:143],a[204:207],v[88:91],v[140:143]
v_exp_f32 v97,v97
v_mfma_f32_16x16x32_bf16 v[148:151],a[208:211],v[80:83],v[148:151]
ds_read_b128 a[132:135],v1 offset:256
v_cvt_pk_bf16_f32 v96,v96,v97
v_mfma_f32_16x16x32_bf16 v[148:151],a[212:215],v[88:91],v[148:151]
v_exp_f32 v98,v98
v_mfma_f32_16x16x32_bf16 v[156:159],a[216:219],v[80:83],v[156:159]
ds_read_b128 a[136:139],v1 offset:512
v_accvgpr_read_b32 v12,a84
v_mfma_f32_16x16x32_bf16 v[156:159],a[220:223],v[88:91],v[156:159]
v_exp_f32 v99,v99
v_mfma_f32_16x16x32_bf16 v[164:167],a[224:227],v[80:83],v[164:167]
ds_read_b128 a[140:143],v1 offset:768
v_cvt_pk_bf16_f32 v97,v98,v99
v_mfma_f32_16x16x32_bf16 v[164:167],a[228:231],v[88:91],v[164:167]
v_exp_f32 v100,v100
v_mfma_f32_16x16x32_bf16 v[172:175],a[232:235],v[80:83],v[172:175]
)A"
R"A(ds_read_b128 a[156:159],v1 offset:4864
v_accvgpr_read_b32 v13,a85
v_mfma_f32_16x16x32_bf16 v[172:175],a[236:239],v[88:91],v[172:175]
v_exp_f32 v101,v101
v_mfma_f32_16x16x32_bf16 v[180:183],a[240:243],v[80:83],v[180:183]
ds_read_b128 a[152:155],v1 offset:4608
v_cvt_pk_bf16_f32 v98,v100,v101
v_mfma_f32_16x16x32_bf16 v[180:183],a[244:247],v[88:91],v[180:183]
v_exp_f32 v102,v102
v_mfma_f32_16x16x32_bf16 v[188:191],a[248:251],v[80:83],v[188:191]
ds_read_b128 a[148:151],v1 offset:4352
v_accvgpr_read_b32 v14,a86
v_mfma_f32_16x16x32_bf16 v[188:191],a[252:255],v[88:91],v[188:191]
v_exp_f32 v103,v103
v_mfma_f32_16x16x32_bf16 v[52:55],v[28:31],v[80:83],v[52:55]
ds_read_b128 a[144:147],v1 offset:4096
v_cvt_pk_bf16_f32 v99,v102,v103
v_mfma_f32_16x16x32_bf16 v[52:55],v[28:31],v[88:91],v[52:55]
s_waitcnt lgkmcnt(4)
v_exp_f32 v104,v104
v_mfma_f32_16x16x32_bf16 v[64:67],a[128:131],v[128:131],v[32:35]
global_load_lds_dwordx4 v5,s[10:11] offset:2048 nt
ds_read_b128 a[160:163],v1 offset:8192
v_mfma_f32_16x16x32_bf16 v[64:67],a[132:135],v[136:139],v[64:67]
v_exp_f32 v105,v105
v_mfma_f32_16x16x32_bf16 v[64:67],a[136:139],v[144:147],v[64:67]
ds_read_b128 a[164:167],v1 offset:8448
v_cvt_pk_bf16_f32 v104,v104,v105
v_mfma_f32_16x16x32_bf16 v[64:67],a[140:143],v[152:155],v[64:67]
s_waitcnt lgkmcnt(4)
v_exp_f32 v106,v106
v_mfma_f32_16x16x32_bf16 v[68:71],a[156:159],v[152:155],v[32:35]
ds_read_b128 a[168:171],v1 offset:8704
v_accvgpr_read_b32 v15,a87
v_mfma_f32_16x16x32_bf16 v[68:71],a[152:155],v[144:147],v[68:71]
s_waitcnt lgkmcnt(4)
v_exp_f32 v107,v107
v_mfma_f32_16x16x32_bf16 v[68:71],a[148:151],v[136:139],v[68:71]
s_waitcnt lgkmcnt(3)
ds_read_b128 a[172:175],v1 offset:8960
v_cvt_pk_bf16_f32 v105,v106,v107
v_mfma_f32_16x16x32_bf16 v[68:71],a[144:147],v[128:131],v[68:71]
s_waitcnt lgkmcnt(3)
v_exp_f32 v108,v108
v_mfma_f32_16x16x32_bf16 v[72:75],a[160:163],v[128:131],v[32:35]
s_waitcnt lgkmcnt(2)
ds_read_b128 a[188:191],v1 offset:13056
v_accvgpr_read_b32 v8,a80
v_mfma_f32_16x16x32_bf16 v[72:75],a[164:167],v[136:139],v[72:75]
s_waitcnt lgkmcnt(2)
ds_read_b128 a[184:187],v1 offset:12800
v_accvgpr_read_b32 v9,a81
v_mfma_f32_16x16x32_bf16 v[72:75],a[168:171],v[144:147],v[72:75]
s_waitcnt lgkmcnt(2)
ds_read_b128 a[180:183],v1 offset:12544
v_accvgpr_read_b32 v10,a82
v_mfma_f32_16x16x32_bf16 v[72:75],a[172:175],v[152:155],v[72:75]
s_waitcnt lgkmcnt(2)
ds_read_b128 a[176:179],v1 offset:12288
v_accvgpr_read_b32 v11,a83
v_mfma_f32_16x16x32_bf16 v[76:79],a[188:191],v[152:155],v[32:35]
s_waitcnt lgkmcnt(2)
v_exp_f32 v109,v109
v_mfma_f32_16x16x32_bf16 v[76:79],a[184:187],v[144:147],v[76:79]
s_waitcnt lgkmcnt(1)
v_exp_f32 v110,v110
v_exp_f32 v111,v111
v_mfma_f32_16x16x32_bf16 v[76:79],a[180:183],v[136:139],v[76:79]
s_waitcnt lgkmcnt(0)
v_cvt_pk_bf16_f32 v106,v108,v109
v_cvt_pk_bf16_f32 v107,v110,v111
v_mfma_f32_16x16x32_bf16 v[76:79],a[176:179],v[128:131],v[76:79]
v_mul_f32 v8,v8,v26
v_mul_f32 v9,v9,v26
v_mfma_f32_16x16x32_bf16 v[196:199],a[192:195],v[96:99],v[196:199]
global_load_lds_dwordx4 v5,s[10:11] offset:3072
v_mul_f32 v10,v10,v26
v_mfma_f32_16x16x32_bf16 v[196:199],a[196:199],v[104:107],v[196:199]
v_mul_f32 v11,v11,v26
v_mul_f32 v12,v12,v26
v_mfma_f32_16x16x32_bf16 v[204:207],a[200:203],v[96:99],v[204:207]
v_mul_f32 v13,v13,v26
v_mul_f32 v14,v14,v26
v_mfma_f32_16x16x32_bf16 v[204:207],a[204:207],v[104:107],v[204:207]
v_mul_f32 v15,v15,v26
v_cvt_pk_bf16_f32 v8,v8,v9
v_mfma_f32_16x16x32_bf16 v[212:215],a[208:211],v[96:99],v[212:215]
v_cvt_pk_bf16_f32 v9,v10,v11
v_cvt_pk_bf16_f32 v10,v12,v13
v_mfma_f32_16x16x32_bf16 v[212:215],a[212:215],v[104:107],v[212:215]
v_cvt_pk_bf16_f32 v11,v14,v15
v_accvgpr_write_b32 a80,v8
v_mfma_f32_16x16x32_bf16 v[220:223],a[216:219],v[96:99],v[220:223]
v_accvgpr_write_b32 a81,v9
v_accvgpr_write_b32 a82,v10
v_mfma_f32_16x16x32_bf16 v[220:223],a[220:223],v[104:107],v[220:223]
ds_read_b128 a[84:87],v7
v_accvgpr_write_b32 a83,v11
v_mfma_f32_16x16x32_bf16 v[228:231],a[224:227],v[96:99],v[228:231]
v_exp_f32 v112,v112
v_mfma_f32_16x16x32_bf16 v[228:231],a[228:231],v[104:107],v[228:231]
v_exp_f32 v113,v113
v_mfma_f32_16x16x32_bf16 v[236:239],a[232:235],v[96:99],v[236:239]
v_exp_f32 v114,v114
v_mfma_f32_16x16x32_bf16 v[236:239],a[236:239],v[104:107],v[236:239]
v_exp_f32 v115,v115
v_mfma_f32_16x16x32_bf16 v[244:247],a[240:243],v[96:99],v[244:247]
s_add_u32 s10,s24,s74
s_addc_u32 s11,s25,0
v_cvt_pk_bf16_f32 v112,v112,v113
v_mfma_f32_16x16x32_bf16 v[244:247],a[244:247],v[104:107],v[244:247]
s_add_u32 s74,s74,16384
v_exp_f32 v116,v116
v_mfma_f32_16x16x32_bf16 v[252:255],a[248:251],v[96:99],v[252:255]
s_add_u32 m0,s76,16384
v_exp_f32 v117,v117
v_mfma_f32_16x16x32_bf16 v[252:255],a[252:255],v[104:107],v[252:255]
s_and_b32 s74,s74,0x3ffff
v_exp_f32 v118,v118
v_mfma_f32_16x16x32_bf16 v[56:59],v[28:31],v[96:99],v[56:59]
global_load_lds_dwordx4 v4,s[10:11]
v_cvt_pk_bf16_f32 v113,v114,v115
v_mfma_f32_16x16x32_bf16 v[56:59],v[28:31],v[104:107],v[56:59]
s_or_b32 s74,s74,s69
v_exp_f32 v119,v119
v_mfma_f32_16x16x32_bf16 v[80:83],a[128:131],v[160:163],v[36:39]
s_sub_u32 s76,s76,32768
v_cvt_pk_bf16_f32 v114,v116,v117
v_cvt_pk_bf16_f32 v115,v118,v119
v_mfma_f32_16x16x32_bf16 v[80:83],a[132:135],v[168:171],v[80:83]
s_and_b32 s76,s76,0x1ffff
v_exp_f32 v120,v120
v_mfma_f32_16x16x32_bf16 v[80:83],a[136:139],v[176:179],v[80:83]
v_exp_f32 v121,v121
v_mfma_f32_16x16x32_bf16 v[80:83],a[140:143],v[184:187],v[80:83]
v_exp_f32 v122,v122
v_mfma_f32_16x16x32_bf16 v[84:87],a[156:159],v[184:187],v[36:39]
v_exp_f32 v123,v123
v_mfma_f32_16x16x32_bf16 v[84:87],a[152:155],v[176:179],v[84:87]
v_exp_f32 v124,v124
v_mfma_f32_16x16x32_bf16 v[84:87],a[148:151],v[168:171],v[84:87]
v_exp_f32 v125,v125
v_mfma_f32_16x16x32_bf16 v[84:87],a[144:147],v[160:163],v[84:87]
v_exp_f32 v126,v126
v_mfma_f32_16x16x32_bf16 v[88:91],a[160:163],v[160:163],v[36:39]
v_exp_f32 v127,v127
v_mfma_f32_16x16x32_bf16 v[88:91],a[164:167],v[168:171],v[88:91]
v_cvt_pk_bf16_f32 v120,v120,v121
v_cvt_pk_bf16_f32 v121,v122,v123
v_mfma_f32_16x16x32_bf16 v[88:91],a[168:171],v[176:179],v[88:91]
v_cvt_pk_bf16_f32 v122,v124,v125
v_cvt_pk_bf16_f32 v123,v126,v127
v_mfma_f32_16x16x32_bf16 v[88:91],a[172:175],v[184:187],v[88:91]
v_add_u32 v2,s78,v2
v_add_u32 v3,s78,v3
v_mfma_f32_16x16x32_bf16 v[92:95],a[188:191],v[184:187],v[36:39]
s_sub_u32 s78,s77,32768
v_exp_f32 v64,v64
v_mfma_f32_16x16x32_bf16 v[92:95],a[184:187],v[176:179],v[92:95]
s_and_b32 s78,s78,0x1ffff
v_exp_f32 v65,v65
v_mfma_f32_16x16x32_bf16 v[92:95],a[180:183],v[168:171],v[92:95]
global_load_lds_dwordx4 v5,s[10:11] offset:1024
v_cvt_pk_bf16_f32 v64,v64,v65
v_mfma_f32_16x16x32_bf16 v[92:95],a[176:179],v[160:163],v[92:95]
s_sub_u32 s78,s78,s77
v_exp_f32 v66,v66
v_mfma_f32_16x16x32_bf16 a[32:35],a[192:195],v[112:115],a[32:35]
s_add_u32 s77,s77,s78
ds_read_b64_tr_b16 a[192:193],v2
ds_read_b64_tr_b16 a[194:195],v2 offset:4096
v_mfma_f32_16x16x32_bf16 a[32:35],a[196:199],v[120:123],a[32:35]
ds_read_b64_tr_b16 a[196:197],v2 offset:8192
v_accvgpr_read_b32 v20,a92
v_mfma_f32_16x16x32_bf16 a[36:39],a[200:203],v[112:115],a[36:39]
ds_read_b64_tr_b16 a[198:199],v2 offset:12288
v_accvgpr_read_b32 v21,a93
v_mfma_f32_16x16x32_bf16 a[36:39],a[204:207],v[120:123],a[36:39]
ds_read_b64_tr_b16 a[200:201],v3
v_accvgpr_read_b32 v22,a94
v_mfma_f32_16x16x32_bf16 a[40:43],a[208:211],v[112:115],a[40:43]
ds_read_b64_tr_b16 a[202:203],v3 offset:4096
ds_read_b64_tr_b16 a[204:205],v3 offset:8192
v_mfma_f32_16x16x32_bf16 a[40:43],a[212:215],v[120:123],a[40:43]
ds_read_b64_tr_b16 a[206:207],v3 offset:12288
v_accvgpr_read_b32 v23,a95
v_mfma_f32_16x16x32_bf16 a[44:47],a[216:219],v[112:115],a[44:47]
ds_read_b64_tr_b16 a[208:209],v2 offset:256
v_accvgpr_read_b32 v16,a88
v_mfma_f32_16x16x32_bf16 a[44:47],a[220:223],v[120:123],a[44:47]
ds_read_b64_tr_b16 a[210:211],v2 offset:4352
v_accvgpr_read_b32 v17,a89
v_mfma_f32_16x16x32_bf16 a[48:51],a[224:227],v[112:115],a[48:51]
ds_read_b64_tr_b16 a[212:213],v2 offset:8448
ds_read_b64_tr_b16 a[214:215],v2 offset:12544
v_mfma_f32_16x16x32_bf16 a[48:51],a[228:231],v[120:123],a[48:51]
v_exp_f32 v67,v67
v_mfma_f32_16x16x32_bf16 a[52:55],a[232:235],v[112:115],a[52:55]
v_exp_f32 v68,v68
v_mfma_f32_16x16x32_bf16 a[52:55],a[236:239],v[120:123],a[52:55]
v_exp_f32 v69,v69
v_mfma_f32_16x16x32_bf16 a[56:59],a[240:243],v[112:115],a[56:59]
v_exp_f32 v70,v70
v_mfma_f32_16x16x32_bf16 a[56:59],a[244:247],v[120:123],a[56:59]
v_exp_f32 v71,v71
v_mfma_f32_16x16x32_bf16 a[60:63],a[248:251],v[112:115],a[60:63]
global_load_lds_dwordx4 v4,s[10:11] offset:2048
ds_read_b64_tr_b16 a[216:217],v3 offset:256
v_mfma_f32_16x16x32_bf16 a[60:63],a[252:255],v[120:123],a[60:63]
ds_read_b64_tr_b16 a[218:219],v3 offset:4352
v_cvt_pk_bf16_f32 v65,v66,v67
v_mfma_f32_16x16x32_bf16 v[60:63],v[28:31],v[112:115],v[60:63]
v_cvt_pk_bf16_f32 v66,v68,v69
v_cvt_pk_bf16_f32 v67,v70,v71
v_mfma_f32_16x16x32_bf16 v[60:63],v[28:31],v[120:123],v[60:63]
v_exp_f32 v72,v72
v_mfma_f32_16x16x32_bf16 v[96:99],a[128:131],v[192:195],v[40:43]
v_exp_f32 v73,v73
v_mfma_f32_16x16x32_bf16 v[96:99],a[132:135],v[200:203],v[96:99]
v_exp_f32 v74,v74
L_pd10_%=:
s_cbranch_execz L_t1b_%=
v_mfma_f32_16x16x32_bf16 v[96:99],a[136:139],v[208:211],v[96:99]
v_exp_f32 v75,v75
v_mfma_f32_16x16x32_bf16 v[96:99],a[140:143],v[216:219],v[96:99]
v_exp_f32 v76,v76
v_mfma_f32_16x16x32_bf16 v[100:103],a[156:159],v[216:219],v[40:43]
v_exp_f32 v77,v77
v_mfma_f32_16x16x32_bf16 v[100:103],a[152:155],v[208:211],v[100:103]
v_exp_f32 v78,v78
v_mfma_f32_16x16x32_bf16 v[100:103],a[148:151],v[200:203],v[100:103]
v_exp_f32 v79,v79
v_mfma_f32_16x16x32_bf16 v[100:103],a[144:147],v[192:195],v[100:103]
ds_read_b64_tr_b16 a[220:221],v3 offset:8448
ds_read_b64_tr_b16 a[222:223],v3 offset:12544
v_mfma_f32_16x16x32_bf16 v[104:107],a[160:163],v[192:195],v[40:43]
v_cvt_pk_bf16_f32 v72,v72,v73
v_cvt_pk_bf16_f32 v73,v74,v75
v_mfma_f32_16x16x32_bf16 v[104:107],a[164:167],v[200:203],v[104:107]
v_cvt_pk_bf16_f32 v74,v76,v77
v_cvt_pk_bf16_f32 v75,v78,v79
v_mfma_f32_16x16x32_bf16 v[104:107],a[168:171],v[208:211],v[104:107]
ds_read_b64_tr_b16 a[224:225],v2 offset:512
ds_read_b64_tr_b16 a[226:227],v2 offset:4608
v_mfma_f32_16x16x32_bf16 v[104:107],a[172:175],v[216:219],v[104:107]
ds_read_b64_tr_b16 a[228:229],v2 offset:8704
v_accvgpr_read_b32 v18,a90
v_mfma_f32_16x16x32_bf16 v[108:111],a[188:191],v[216:219],v[40:43]
global_load_lds_dwordx4 v5,s[10:11] offset:3072
ds_read_b64_tr_b16 a[230:231],v2 offset:12800
v_mfma_f32_16x16x32_bf16 v[108:111],a[184:187],v[208:211],v[108:111]
ds_read_b64_tr_b16 a[232:233],v3 offset:512
v_accvgpr_read_b32 v19,a91
v_mfma_f32_16x16x32_bf16 v[108:111],a[180:183],v[200:203],v[108:111]
ds_read_b64_tr_b16 a[234:235],v3 offset:4608
ds_read_b64_tr_b16 a[236:237],v3 offset:8704
v_mfma_f32_16x16x32_bf16 v[108:111],a[176:179],v[192:195],v[108:111]
s_waitcnt lgkmcnt(7)
ds_read_b64_tr_b16 a[238:239],v3 offset:12800
v_mul_f32 v16,v16,v26
v_mfma_f32_16x16x32_bf16 a[0:3],a[192:195],v[64:67],a[0:3]
ds_read_b64_tr_b16 a[240:241],v2 offset:768
v_mul_f32 v17,v17,v26
v_mfma_f32_16x16x32_bf16 a[0:3],a[196:199],v[72:75],a[0:3]
ds_read_b64_tr_b16 a[242:243],v2 offset:4864
v_mul_f32 v18,v18,v26
v_mfma_f32_16x16x32_bf16 a[4:7],a[200:203],v[64:67],a[4:7]
ds_read_b64_tr_b16 a[244:245],v2 offset:8960
ds_read_b64_tr_b16 a[246:247],v2 offset:13056
v_mfma_f32_16x16x32_bf16 a[4:7],a[204:207],v[72:75],a[4:7]
ds_read_b64_tr_b16 a[248:249],v3 offset:768
v_mul_f32 v19,v19,v26
v_mfma_f32_16x16x32_bf16 a[8:11],a[208:211],v[64:67],a[8:11]
ds_read_b64_tr_b16 a[250:251],v3 offset:4864
v_mul_f32 v20,v20,v26
v_mfma_f32_16x16x32_bf16 a[8:11],a[212:215],v[72:75],a[8:11]
v_mul_f32 v21,v21,v26
v_mul_f32 v22,v22,v26
v_mul_f32 v23,v23,v26
v_cvt_pk_bf16_f32 v16,v16,v17
v_cvt_pk_bf16_f32 v17,v18,v19
v_cvt_pk_bf16_f32 v18,v20,v21
v_cvt_pk_bf16_f32 v19,v22,v23
v_accvgpr_write_b32 a88,v16
v_accvgpr_write_b32 a89,v17
v_accvgpr_write_b32 a90,v18
v_accvgpr_write_b32 a91,v19
v_mfma_f32_16x16x32_bf16 a[12:15],a[216:219],v[64:67],a[12:15]
ds_read_b128 a[92:95],v7 offset:1024
v_mov_b32 v8,v7
v_mfma_f32_16x16x32_bf16 a[12:15],a[220:223],v[72:75],a[12:15]
)A"
R"A(s_waitcnt lgkmcnt(7)
ds_read_b64_tr_b16 a[252:253],v3 offset:8960
v_add_u32 v1,s78,v1
v_mfma_f32_16x16x32_bf16 a[16:19],a[224:227],v[64:67],a[16:19]
ds_read_b64_tr_b16 a[254:255],v3 offset:13056
v_mfma_f32_16x16x32_bf16 a[16:19],a[228:231],v[72:75],a[16:19]
s_waitcnt lgkmcnt(4)
v_exp_f32 v80,v80
v_mfma_f32_16x16x32_bf16 a[20:23],a[232:235],v[64:67],a[20:23]
s_getreg_b32 s92,hwreg(HW_REG_IB_STS,0,4)
s_min_u32 s4,s92,s6
s_mov_b32 s6,s92
v_mfma_f32_16x16x32_bf16 a[20:23],a[236:239],v[72:75],a[20:23]
s_cmp_ge_u32 s4,s9
s_cselect_b32 s4,15,s5
ds_read_b128 v[24:27],v8 offset:4096
v_mfma_f32_16x16x32_bf16 a[24:27],a[240:243],v[64:67],a[24:27]
v_exp_f32 v81,v81
v_mfma_f32_16x16x32_bf16 a[24:27],a[244:247],v[72:75],a[24:27]
s_waitcnt lgkmcnt(4)
v_exp_f32 v82,v82
v_mfma_f32_16x16x32_bf16 a[28:31],a[248:251],v[64:67],a[28:31]
s_waitcnt lgkmcnt(1)
v_exp_f32 v83,v83
v_mfma_f32_16x16x32_bf16 a[28:31],a[252:255],v[72:75],a[28:31]
v_exp_f32 v84,v84
v_mfma_f32_16x16x32_bf16 v[48:51],v[28:31],v[64:67],v[48:51]
s_getpc_b64 s[92:93]
s_cmp_lg_u32 s4,0
s_cbranch_scc1 L_pace_%=
v_exp_f32 v85,v85
v_exp_f32 v86,v86
v_exp_f32 v87,v87
v_exp_f32 v88,v88
v_exp_f32 v89,v89
v_mfma_f32_16x16x32_bf16 v[48:51],v[28:31],v[72:75],v[48:51]
ds_read_b32 v7,v8 offset:5120
v_cvt_pk_bf16_f32 v80,v80,v81
v_cvt_pk_bf16_f32 v81,v82,v83
v_cvt_pk_bf16_f32 v82,v84,v85
v_cvt_pk_bf16_f32 v83,v86,v87
v_cvt_pk_bf16_f32 v88,v88,v89
s_mov_b32 s70,8
L_a1_%=:
s_waitcnt vmcnt(16)
s_barrier
v_mfma_f32_16x16x32_bf16 v[112:115],a[128:131],v[224:227],v[44:47]
s_add_u32 s10,s20,s74
s_addc_u32 s11,s21,0
s_add_u32 m0,s76,0
v_mfma_f32_16x16x32_bf16 v[112:115],a[132:135],v[232:235],v[112:115]
s_add_u32 s75,s75,16384
v_exp_f32 v90,v90
v_mfma_f32_16x16x32_bf16 v[112:115],a[136:139],v[240:243],v[112:115]
global_load_lds_dwordx4 v4,s[10:11] nt
ds_read_b128 a[128:131],v1
v_mfma_f32_16x16x32_bf16 v[112:115],a[140:143],v[248:251],v[112:115]
s_and_b32 s69,s75,0xfffc0000
v_exp_f32 v91,v91
v_mfma_f32_16x16x32_bf16 v[116:119],a[156:159],v[248:251],v[44:47]
v_exp_f32 v92,v92
v_mfma_f32_16x16x32_bf16 v[116:119],a[152:155],v[240:243],v[116:119]
v_exp_f32 v93,v93
v_mfma_f32_16x16x32_bf16 v[116:119],a[148:151],v[232:235],v[116:119]
v_exp_f32 v94,v94
v_mfma_f32_16x16x32_bf16 v[116:119],a[144:147],v[224:227],v[116:119]
v_exp_f32 v95,v95
v_mfma_f32_16x16x32_bf16 v[120:123],a[160:163],v[224:227],v[44:47]
v_cvt_pk_bf16_f32 v89,v90,v91
v_cvt_pk_bf16_f32 v90,v92,v93
v_mfma_f32_16x16x32_bf16 v[120:123],a[164:167],v[232:235],v[120:123]
ds_read_b128 a[132:135],v1 offset:256
v_cvt_pk_bf16_f32 v91,v94,v95
v_mfma_f32_16x16x32_bf16 v[120:123],a[168:171],v[240:243],v[120:123]
v_exp_f32 v96,v96
v_mfma_f32_16x16x32_bf16 v[120:123],a[172:175],v[248:251],v[120:123]
ds_read_b128 a[136:139],v1 offset:512
v_add_u32 v2,s78,v2
v_mfma_f32_16x16x32_bf16 v[124:127],a[188:191],v[248:251],v[44:47]
v_exp_f32 v97,v97
v_mfma_f32_16x16x32_bf16 v[124:127],a[184:187],v[240:243],v[124:127]
ds_read_b128 a[140:143],v1 offset:768
v_cvt_pk_bf16_f32 v96,v96,v97
v_mfma_f32_16x16x32_bf16 v[124:127],a[180:183],v[232:235],v[124:127]
v_exp_f32 v98,v98
v_mfma_f32_16x16x32_bf16 v[124:127],a[176:179],v[224:227],v[124:127]
ds_read_b128 a[156:159],v1 offset:4864
v_add_u32 v3,s78,v3
v_mfma_f32_16x16x32_bf16 v[132:135],a[192:195],v[80:83],v[132:135]
s_sub_u32 s78,s77,32768
v_exp_f32 v99,v99
v_mfma_f32_16x16x32_bf16 v[132:135],a[196:199],v[88:91],v[132:135]
s_and_b32 s78,s78,0x1ffff
ds_read_b128 a[152:155],v1 offset:4608
v_cvt_pk_bf16_f32 v97,v98,v99
v_mfma_f32_16x16x32_bf16 v[140:143],a[200:203],v[80:83],v[140:143]
s_sub_u32 s78,s78,s77
global_load_lds_dwordx4 v4,s[10:11] offset:1024 nt
v_mfma_f32_16x16x32_bf16 v[140:143],a[204:207],v[88:91],v[140:143]
s_add_u32 s77,s77,s78
v_exp_f32 v100,v100
v_mfma_f32_16x16x32_bf16 v[148:151],a[208:211],v[80:83],v[148:151]
v_exp_f32 v101,v101
v_mfma_f32_16x16x32_bf16 v[148:151],a[212:215],v[88:91],v[148:151]
ds_read_b128 a[148:151],v1 offset:4352
v_cvt_pk_bf16_f32 v98,v100,v101
v_mfma_f32_16x16x32_bf16 v[156:159],a[216:219],v[80:83],v[156:159]
v_exp_f32 v102,v102
v_mfma_f32_16x16x32_bf16 v[156:159],a[220:223],v[88:91],v[156:159]
v_exp_f32 v103,v103
v_mfma_f32_16x16x32_bf16 v[164:167],a[224:227],v[80:83],v[164:167]
ds_read_b128 a[144:147],v1 offset:4096
v_cvt_pk_bf16_f32 v99,v102,v103
v_mfma_f32_16x16x32_bf16 v[164:167],a[228:231],v[88:91],v[164:167]
v_exp_f32 v104,v104
v_mfma_f32_16x16x32_bf16 v[172:175],a[232:235],v[80:83],v[172:175]
v_exp_f32 v105,v105
v_mfma_f32_16x16x32_bf16 v[172:175],a[236:239],v[88:91],v[172:175]
ds_read_b128 a[160:163],v1 offset:8192
v_cvt_pk_bf16_f32 v104,v104,v105
v_mfma_f32_16x16x32_bf16 v[180:183],a[240:243],v[80:83],v[180:183]
v_exp_f32 v106,v106
v_mfma_f32_16x16x32_bf16 v[180:183],a[244:247],v[88:91],v[180:183]
v_exp_f32 v107,v107
v_mfma_f32_16x16x32_bf16 v[188:191],a[248:251],v[80:83],v[188:191]
ds_read_b128 a[164:167],v1 offset:8448
v_cvt_pk_bf16_f32 v105,v106,v107
v_mfma_f32_16x16x32_bf16 v[188:191],a[252:255],v[88:91],v[188:191]
v_exp_f32 v108,v108
v_mfma_f32_16x16x32_bf16 v[52:55],v[28:31],v[80:83],v[52:55]
ds_read_b128 a[168:171],v1 offset:8704
v_mfma_f32_16x16x32_bf16 v[52:55],v[28:31],v[88:91],v[52:55]
s_waitcnt lgkmcnt(3)
v_exp_f32 v109,v109
v_mfma_f32_16x16x32_bf16 v[64:67],a[128:131],v[128:131],v[32:35]
global_load_lds_dwordx4 v5,s[10:11] offset:2048 nt
ds_read_b128 a[172:175],v1 offset:8960
v_mfma_f32_16x16x32_bf16 v[64:67],a[132:135],v[136:139],v[64:67]
v_exp_f32 v110,v110
v_mfma_f32_16x16x32_bf16 v[64:67],a[136:139],v[144:147],v[64:67]
ds_read_b128 a[188:191],v1 offset:13056
v_cvt_pk_bf16_f32 v106,v108,v109
v_mfma_f32_16x16x32_bf16 v[64:67],a[140:143],v[152:155],v[64:67]
v_exp_f32 v111,v111
v_mfma_f32_16x16x32_bf16 v[68:71],a[156:159],v[152:155],v[32:35]
ds_read_b128 a[184:187],v1 offset:12800
v_cvt_pk_bf16_f32 v107,v110,v111
v_mfma_f32_16x16x32_bf16 v[68:71],a[152:155],v[144:147],v[68:71]
v_exp_f32 v112,v112
v_mfma_f32_16x16x32_bf16 v[68:71],a[148:151],v[136:139],v[68:71]
ds_read_b128 a[180:183],v1 offset:12544
v_mfma_f32_16x16x32_bf16 v[68:71],a[144:147],v[128:131],v[68:71]
s_waitcnt lgkmcnt(4)
v_exp_f32 v113,v113
v_mfma_f32_16x16x32_bf16 v[72:75],a[160:163],v[128:131],v[32:35]
ds_read_b128 a[176:179],v1 offset:12288
v_cvt_pk_bf16_f32 v112,v112,v113
v_mfma_f32_16x16x32_bf16 v[72:75],a[164:167],v[136:139],v[72:75]
v_exp_f32 v114,v114
v_mfma_f32_16x16x32_bf16 v[72:75],a[168:171],v[144:147],v[72:75]
s_waitcnt lgkmcnt(3)
v_exp_f32 v115,v115
v_mfma_f32_16x16x32_bf16 v[72:75],a[172:175],v[152:155],v[72:75]
v_exp_f32 v116,v116
v_mfma_f32_16x16x32_bf16 v[76:79],a[188:191],v[152:155],v[32:35]
s_waitcnt lgkmcnt(2)
v_exp_f32 v117,v117
v_mfma_f32_16x16x32_bf16 v[76:79],a[184:187],v[144:147],v[76:79]
s_waitcnt lgkmcnt(1)
v_exp_f32 v118,v118
v_mfma_f32_16x16x32_bf16 v[76:79],a[180:183],v[136:139],v[76:79]
s_waitcnt lgkmcnt(0)
v_exp_f32 v119,v119
v_mfma_f32_16x16x32_bf16 v[76:79],a[176:179],v[128:131],v[76:79]
v_cvt_pk_bf16_f32 v113,v114,v115
v_cvt_pk_bf16_f32 v114,v116,v117
v_mfma_f32_16x16x32_bf16 v[196:199],a[192:195],v[96:99],v[196:199]
global_load_lds_dwordx4 v5,s[10:11] offset:3072
v_cvt_pk_bf16_f32 v115,v118,v119
v_mfma_f32_16x16x32_bf16 v[196:199],a[196:199],v[104:107],v[196:199]
v_exp_f32 v120,v120
v_mfma_f32_16x16x32_bf16 v[204:207],a[200:203],v[96:99],v[204:207]
v_exp_f32 v121,v121
v_mfma_f32_16x16x32_bf16 v[204:207],a[204:207],v[104:107],v[204:207]
v_exp_f32 v122,v122
v_mfma_f32_16x16x32_bf16 v[212:215],a[208:211],v[96:99],v[212:215]
v_exp_f32 v123,v123
v_mfma_f32_16x16x32_bf16 v[212:215],a[212:215],v[104:107],v[212:215]
v_exp_f32 v124,v124
v_mfma_f32_16x16x32_bf16 v[220:223],a[216:219],v[96:99],v[220:223]
v_exp_f32 v125,v125
v_mfma_f32_16x16x32_bf16 v[220:223],a[220:223],v[104:107],v[220:223]
v_exp_f32 v126,v126
v_mfma_f32_16x16x32_bf16 v[228:231],a[224:227],v[96:99],v[228:231]
v_exp_f32 v127,v127
v_mfma_f32_16x16x32_bf16 v[228:231],a[228:231],v[104:107],v[228:231]
v_cvt_pk_bf16_f32 v120,v120,v121
v_cvt_pk_bf16_f32 v121,v122,v123
v_mfma_f32_16x16x32_bf16 v[236:239],a[232:235],v[96:99],v[236:239]
v_cvt_pk_bf16_f32 v122,v124,v125
v_cvt_pk_bf16_f32 v123,v126,v127
v_mfma_f32_16x16x32_bf16 v[236:239],a[236:239],v[104:107],v[236:239]
v_exp_f32 v64,v64
v_mfma_f32_16x16x32_bf16 v[244:247],a[240:243],v[96:99],v[244:247]
s_add_u32 s10,s24,s74
s_addc_u32 s11,s25,0
s_add_u32 s74,s74,16384
v_mfma_f32_16x16x32_bf16 v[244:247],a[244:247],v[104:107],v[244:247]
s_and_b32 s74,s74,0x3ffff
v_exp_f32 v65,v65
v_mfma_f32_16x16x32_bf16 v[252:255],a[248:251],v[96:99],v[252:255]
s_add_u32 m0,s76,16384
v_exp_f32 v66,v66
v_mfma_f32_16x16x32_bf16 v[252:255],a[252:255],v[104:107],v[252:255]
s_or_b32 s74,s74,s69
v_exp_f32 v67,v67
v_mfma_f32_16x16x32_bf16 v[56:59],v[28:31],v[96:99],v[56:59]
global_load_lds_dwordx4 v4,s[10:11]
v_cvt_pk_bf16_f32 v64,v64,v65
v_mfma_f32_16x16x32_bf16 v[56:59],v[28:31],v[104:107],v[56:59]
s_sub_u32 s76,s76,32768
v_exp_f32 v68,v68
v_mfma_f32_16x16x32_bf16 v[80:83],a[128:131],v[160:163],v[36:39]
s_and_b32 s76,s76,0x1ffff
v_exp_f32 v69,v69
v_mfma_f32_16x16x32_bf16 v[80:83],a[132:135],v[168:171],v[80:83]
v_exp_f32 v70,v70
v_mfma_f32_16x16x32_bf16 v[80:83],a[136:139],v[176:179],v[80:83]
v_exp_f32 v71,v71
v_mfma_f32_16x16x32_bf16 v[80:83],a[140:143],v[184:187],v[80:83]
v_cvt_pk_bf16_f32 v65,v66,v67
v_cvt_pk_bf16_f32 v66,v68,v69
v_mfma_f32_16x16x32_bf16 v[84:87],a[156:159],v[184:187],v[36:39]
v_cvt_pk_bf16_f32 v67,v70,v71
v_add_u32 v1,s78,v1
v_mfma_f32_16x16x32_bf16 v[84:87],a[152:155],v[176:179],v[84:87]
v_exp_f32 v72,v72
v_mfma_f32_16x16x32_bf16 v[84:87],a[148:151],v[168:171],v[84:87]
v_exp_f32 v73,v73
v_mfma_f32_16x16x32_bf16 v[84:87],a[144:147],v[160:163],v[84:87]
v_exp_f32 v74,v74
v_mfma_f32_16x16x32_bf16 v[88:91],a[160:163],v[160:163],v[36:39]
v_exp_f32 v75,v75
v_mfma_f32_16x16x32_bf16 v[88:91],a[164:167],v[168:171],v[88:91]
v_exp_f32 v76,v76
v_mfma_f32_16x16x32_bf16 v[88:91],a[168:171],v[176:179],v[88:91]
v_exp_f32 v77,v77
v_mfma_f32_16x16x32_bf16 v[88:91],a[172:175],v[184:187],v[88:91]
v_exp_f32 v78,v78
v_mfma_f32_16x16x32_bf16 v[92:95],a[188:191],v[184:187],v[36:39]
v_exp_f32 v79,v79
v_mfma_f32_16x16x32_bf16 v[92:95],a[184:187],v[176:179],v[92:95]
v_cvt_pk_bf16_f32 v72,v72,v73
v_cvt_pk_bf16_f32 v73,v74,v75
v_mfma_f32_16x16x32_bf16 v[92:95],a[180:183],v[168:171],v[92:95]
global_load_lds_dwordx4 v5,s[10:11] offset:1024
v_cvt_pk_bf16_f32 v74,v76,v77
v_mfma_f32_16x16x32_bf16 v[92:95],a[176:179],v[160:163],v[92:95]
v_exp_f32 v80,v80
v_mfma_f32_16x16x32_bf16 a[32:35],a[192:195],v[112:115],a[32:35]
ds_read_b64_tr_b16 a[192:193],v2
ds_read_b64_tr_b16 a[194:195],v2 offset:4096
v_mfma_f32_16x16x32_bf16 a[32:35],a[196:199],v[120:123],a[32:35]
ds_read_b64_tr_b16 a[196:197],v2 offset:8192
v_cvt_pk_bf16_f32 v75,v78,v79
v_mfma_f32_16x16x32_bf16 a[36:39],a[200:203],v[112:115],a[36:39]
v_exp_f32 v81,v81
v_mfma_f32_16x16x32_bf16 a[36:39],a[204:207],v[120:123],a[36:39]
ds_read_b64_tr_b16 a[198:199],v2 offset:12288
ds_read_b64_tr_b16 a[200:201],v3
v_mfma_f32_16x16x32_bf16 a[40:43],a[208:211],v[112:115],a[40:43]
ds_read_b64_tr_b16 a[202:203],v3 offset:4096
v_cvt_pk_bf16_f32 v80,v80,v81
v_mfma_f32_16x16x32_bf16 a[40:43],a[212:215],v[120:123],a[40:43]
v_exp_f32 v82,v82
v_mfma_f32_16x16x32_bf16 a[44:47],a[216:219],v[112:115],a[44:47]
ds_read_b64_tr_b16 a[204:205],v3 offset:8192
ds_read_b64_tr_b16 a[206:207],v3 offset:12288
v_mfma_f32_16x16x32_bf16 a[44:47],a[220:223],v[120:123],a[44:47]
v_exp_f32 v83,v83
v_mfma_f32_16x16x32_bf16 a[48:51],a[224:227],v[112:115],a[48:51]
ds_read_b64_tr_b16 a[208:209],v2 offset:256
ds_read_b64_tr_b16 a[210:211],v2 offset:4352
v_mfma_f32_16x16x32_bf16 a[48:51],a[228:231],v[120:123],a[48:51]
ds_read_b64_tr_b16 a[212:213],v2 offset:8448
v_cvt_pk_bf16_f32 v81,v82,v83
v_mfma_f32_16x16x32_bf16 a[52:55],a[232:235],v[112:115],a[52:55]
v_exp_f32 v84,v84
v_mfma_f32_16x16x32_bf16 a[52:55],a[236:239],v[120:123],a[52:55]
ds_read_b64_tr_b16 a[214:215],v2 offset:12544
ds_read_b64_tr_b16 a[216:217],v3 offset:256
v_mfma_f32_16x16x32_bf16 a[56:59],a[240:243],v[112:115],a[56:59]
v_exp_f32 v85,v85
v_mfma_f32_16x16x32_bf16 a[56:59],a[244:247],v[120:123],a[56:59]
)A"
R"A(ds_read_b64_tr_b16 a[218:219],v3 offset:4352
ds_read_b64_tr_b16 a[220:221],v3 offset:8448
v_mfma_f32_16x16x32_bf16 a[60:63],a[248:251],v[112:115],a[60:63]
global_load_lds_dwordx4 v4,s[10:11] offset:2048
ds_read_b64_tr_b16 a[222:223],v3 offset:12544
v_mfma_f32_16x16x32_bf16 a[60:63],a[252:255],v[120:123],a[60:63]
ds_read_b64_tr_b16 a[224:225],v2 offset:512
v_cvt_pk_bf16_f32 v82,v84,v85
v_mfma_f32_16x16x32_bf16 v[60:63],v[28:31],v[112:115],v[60:63]
v_exp_f32 v86,v86
v_mfma_f32_16x16x32_bf16 v[60:63],v[28:31],v[120:123],v[60:63]
ds_read_b64_tr_b16 a[226:227],v2 offset:4608
ds_read_b64_tr_b16 a[228:229],v2 offset:8704
v_mfma_f32_16x16x32_bf16 v[96:99],a[128:131],v[192:195],v[40:43]
v_exp_f32 v87,v87
v_mfma_f32_16x16x32_bf16 v[96:99],a[132:135],v[200:203],v[96:99]
ds_read_b64_tr_b16 a[230:231],v2 offset:12800
ds_read_b64_tr_b16 a[232:233],v3 offset:512
v_mfma_f32_16x16x32_bf16 v[96:99],a[136:139],v[208:211],v[96:99]
ds_read_b64_tr_b16 a[234:235],v3 offset:4608
v_cvt_pk_bf16_f32 v83,v86,v87
v_mfma_f32_16x16x32_bf16 v[96:99],a[140:143],v[216:219],v[96:99]
v_exp_f32 v88,v88
v_mfma_f32_16x16x32_bf16 v[100:103],a[156:159],v[216:219],v[40:43]
ds_read_b64_tr_b16 a[236:237],v3 offset:8704
ds_read_b64_tr_b16 a[238:239],v3 offset:12800
v_mfma_f32_16x16x32_bf16 v[100:103],a[152:155],v[208:211],v[100:103]
v_exp_f32 v89,v89
v_mfma_f32_16x16x32_bf16 v[100:103],a[148:151],v[200:203],v[100:103]
ds_read_b64_tr_b16 a[240:241],v2 offset:768
ds_read_b64_tr_b16 a[242:243],v2 offset:4864
v_mfma_f32_16x16x32_bf16 v[100:103],a[144:147],v[192:195],v[100:103]
ds_read_b64_tr_b16 a[244:245],v2 offset:8960
v_cvt_pk_bf16_f32 v88,v88,v89
v_mfma_f32_16x16x32_bf16 v[104:107],a[160:163],v[192:195],v[40:43]
ds_read_b64_tr_b16 a[246:247],v2 offset:13056
v_mfma_f32_16x16x32_bf16 v[104:107],a[164:167],v[200:203],v[104:107]
ds_read_b64_tr_b16 a[248:249],v3 offset:768
v_mfma_f32_16x16x32_bf16 v[104:107],a[168:171],v[208:211],v[104:107]
ds_read_b64_tr_b16 a[250:251],v3 offset:4864
ds_read_b64_tr_b16 a[252:253],v3 offset:8960
v_mfma_f32_16x16x32_bf16 v[104:107],a[172:175],v[216:219],v[104:107]
ds_read_b64_tr_b16 a[254:255],v3 offset:13056
v_mfma_f32_16x16x32_bf16 v[108:111],a[188:191],v[216:219],v[40:43]
global_load_lds_dwordx4 v5,s[10:11] offset:3072
v_mfma_f32_16x16x32_bf16 v[108:111],a[184:187],v[208:211],v[108:111]
v_mfma_f32_16x16x32_bf16 v[108:111],a[180:183],v[200:203],v[108:111]
v_mfma_f32_16x16x32_bf16 v[108:111],a[176:179],v[192:195],v[108:111]
s_waitcnt lgkmcnt(5)
v_mfma_f32_16x16x32_bf16 a[0:3],a[192:195],v[64:67],a[0:3]
v_mfma_f32_16x16x32_bf16 a[0:3],a[196:199],v[72:75],a[0:3]
v_mfma_f32_16x16x32_bf16 a[4:7],a[200:203],v[64:67],a[4:7]
v_mfma_f32_16x16x32_bf16 a[4:7],a[204:207],v[72:75],a[4:7]
v_mfma_f32_16x16x32_bf16 a[8:11],a[208:211],v[64:67],a[8:11]
v_mfma_f32_16x16x32_bf16 a[8:11],a[212:215],v[72:75],a[8:11]
v_mfma_f32_16x16x32_bf16 a[12:15],a[216:219],v[64:67],a[12:15]
v_mfma_f32_16x16x32_bf16 a[12:15],a[220:223],v[72:75],a[12:15]
v_mfma_f32_16x16x32_bf16 a[16:19],a[224:227],v[64:67],a[16:19]
v_mfma_f32_16x16x32_bf16 a[16:19],a[228:231],v[72:75],a[16:19]
v_mfma_f32_16x16x32_bf16 a[20:23],a[232:235],v[64:67],a[20:23]
s_getreg_b32 s92,hwreg(HW_REG_IB_STS,0,4)
s_min_u32 s4,s92,s6
s_mov_b32 s6,s92
v_mfma_f32_16x16x32_bf16 a[20:23],a[236:239],v[72:75],a[20:23]
s_cmp_ge_u32 s4,s9
s_cselect_b32 s4,15,s5
v_mfma_f32_16x16x32_bf16 a[24:27],a[240:243],v[64:67],a[24:27]
s_waitcnt lgkmcnt(0)
v_mfma_f32_16x16x32_bf16 a[24:27],a[244:247],v[72:75],a[24:27]
v_mfma_f32_16x16x32_bf16 a[28:31],a[248:251],v[64:67],a[28:31]
v_mfma_f32_16x16x32_bf16 a[28:31],a[252:255],v[72:75],a[28:31]
s_getpc_b64 s[92:93]
s_cmp_lg_u32 s4,0
s_cbranch_scc1 L_pace_%=
v_mfma_f32_16x16x32_bf16 v[48:51],v[28:31],v[64:67],v[48:51]
v_mfma_f32_16x16x32_bf16 v[48:51],v[28:31],v[72:75],v[48:51]
s_sub_u32 s70,s70,1
s_cmp_lg_u32 s70,0
s_cbranch_scc1 L_a1_%=
s_waitcnt vmcnt(16)
s_barrier
v_mfma_f32_16x16x32_bf16 v[112:115],a[128:131],v[224:227],v[44:47]
s_mul_i32 s86,s60,10240
s_mul_i32 s85,s63,1310720
s_mul_i32 s66,s64,320
v_mfma_f32_16x16x32_bf16 v[112:115],a[132:135],v[232:235],v[112:115]
s_add_u32 s85,s85,s66
s_add_u32 s83,s64,0
s_and_b32 s83,s83,3
v_mfma_f32_16x16x32_bf16 v[112:115],a[136:139],v[240:243],v[112:115]
s_lshl_b32 s83,s83,1
s_mul_i32 s83,s83,163840
s_add_u32 s83,s83,s85
v_mfma_f32_16x16x32_bf16 v[112:115],a[140:143],v[248:251],v[112:115]
s_cmp_eq_u32 s60,3
s_cselect_b32 s69,0x40000000,0
v_add_u32 v14,s69,v6
v_mfma_f32_16x16x32_bf16 v[116:119],a[156:159],v[248:251],v[44:47]
s_add_u32 s84,s83,20480
v_exp_f32 v90,v90
v_mfma_f32_16x16x32_bf16 v[116:119],a[152:155],v[240:243],v[116:119]
s_add_u32 m0,s86,122880
v_exp_f32 v91,v91
v_mfma_f32_16x16x32_bf16 v[116:119],a[148:151],v[232:235],v[116:119]
s_getpc_b64 s[8:9]
L_pcg_%=:
v_exp_f32 v92,v92
v_mfma_f32_16x16x32_bf16 v[116:119],a[144:147],v[224:227],v[116:119]
buffer_load_dwordx4 v24,s[48:51],s83 offen lds
v_mfma_f32_16x16x32_bf16 v[120:123],a[160:163],v[224:227],v[44:47]
s_mov_b32 s6,L_gbF_%=-L_pcg_%=
v_exp_f32 v93,v93
v_mfma_f32_16x16x32_bf16 v[120:123],a[164:167],v[232:235],v[120:123]
buffer_store_dwordx4 a[112:115],v14,s[40:43],s81 offen offset:3072 sc1
v_mfma_f32_16x16x32_bf16 v[120:123],a[168:171],v[240:243],v[120:123]
s_mov_b32 s10,L_geF_%=-L_gbF_%=
v_exp_f32 v94,v94
v_mfma_f32_16x16x32_bf16 v[120:123],a[172:175],v[248:251],v[120:123]
s_mov_b32 s11,0x20000
v_exp_f32 v95,v95
v_mfma_f32_16x16x32_bf16 v[124:127],a[188:191],v[248:251],v[44:47]
s_lshr_b32 s7,%1,3
v_cvt_pk_bf16_f32 v89,v90,v91
v_cvt_pk_bf16_f32 v90,v92,v93
v_mfma_f32_16x16x32_bf16 v[124:127],a[184:187],v[240:243],v[124:127]
s_lshl_b32 s7,s7,2
ds_read_b128 a[128:131],v1
v_cvt_pk_bf16_f32 v91,v94,v95
v_mfma_f32_16x16x32_bf16 v[124:127],a[180:183],v[232:235],v[124:127]
s_add_u32 s7,s7,s60
v_exp_f32 v96,v96
v_mfma_f32_16x16x32_bf16 v[124:127],a[176:179],v[224:227],v[124:127]
s_lshl_b32 s7,s7,7
ds_read_b128 a[132:135],v1 offset:256
v_add_u32 v2,s78,v2
v_mfma_f32_16x16x32_bf16 v[132:135],a[192:195],v[80:83],v[132:135]
s_lshl_b32 s66,s61,5
v_exp_f32 v97,v97
v_mfma_f32_16x16x32_bf16 v[132:135],a[196:199],v[88:91],v[132:135]
buffer_load_dwordx4 v25,s[48:51],s83 offen offset:1024 lds
v_mfma_f32_16x16x32_bf16 v[140:143],a[200:203],v[80:83],v[140:143]
s_lshl_b32 s67,s63,3
ds_read_b128 a[136:139],v1 offset:512
v_cvt_pk_bf16_f32 v96,v96,v97
v_mfma_f32_16x16x32_bf16 v[140:143],a[204:207],v[88:91],v[140:143]
s_add_u32 s66,s66,s67
v_exp_f32 v98,v98
v_mfma_f32_16x16x32_bf16 v[148:151],a[208:211],v[80:83],v[148:151]
s_lshl_b32 s66,s66,16
ds_read_b128 a[140:143],v1 offset:768
v_add_u32 v3,s78,v3
v_mfma_f32_16x16x32_bf16 v[148:151],a[212:215],v[88:91],v[148:151]
s_sub_u32 s78,s77,32768
v_exp_f32 v99,v99
v_mfma_f32_16x16x32_bf16 v[156:159],a[216:219],v[80:83],v[156:159]
s_and_b32 s78,s78,0x1ffff
ds_read_b128 a[156:159],v1 offset:4864
v_cvt_pk_bf16_f32 v97,v98,v99
v_mfma_f32_16x16x32_bf16 v[156:159],a[220:223],v[88:91],v[156:159]
s_sub_u32 s78,s78,s77
v_exp_f32 v100,v100
v_mfma_f32_16x16x32_bf16 v[164:167],a[224:227],v[80:83],v[164:167]
s_add_u32 s77,s77,s78
ds_read_b128 a[152:155],v1 offset:4608
v_lshlrev_b32 v4,10,v6
v_mfma_f32_16x16x32_bf16 v[164:167],a[228:231],v[88:91],v[164:167]
s_lshl_b32 s67,s60,14
v_exp_f32 v101,v101
v_mfma_f32_16x16x32_bf16 v[172:175],a[232:235],v[80:83],v[172:175]
buffer_store_dwordx4 a[116:119],v6,s[40:43],s81 offen offset:1024 sc1
v_mfma_f32_16x16x32_bf16 v[172:175],a[236:239],v[88:91],v[172:175]
s_add_u32 s66,s66,s67
ds_read_b128 a[148:151],v1 offset:4352
v_cvt_pk_bf16_f32 v98,v100,v101
v_mfma_f32_16x16x32_bf16 v[180:183],a[240:243],v[80:83],v[180:183]
buffer_load_dwordx4 v26,s[48:51],s83 offen offset:2048 lds
v_mfma_f32_16x16x32_bf16 v[180:183],a[244:247],v[88:91],v[180:183]
s_mov_b32 s30,1523712
ds_read_b128 a[144:147],v1 offset:4096
v_mov_b32 v8,s52
v_mfma_f32_16x16x32_bf16 v[188:191],a[248:251],v[80:83],v[188:191]
s_mov_b32 s31,0x20000
v_exp_f32 v102,v102
v_mfma_f32_16x16x32_bf16 v[188:191],a[252:255],v[88:91],v[188:191]
ds_read_b128 a[160:163],v1 offset:8192
v_mov_b32 v9,s53
v_mfma_f32_16x16x32_bf16 v[52:55],v[28:31],v[80:83],v[52:55]
v_exp_f32 v103,v103
v_mfma_f32_16x16x32_bf16 v[52:55],v[28:31],v[88:91],v[52:55]
s_waitcnt lgkmcnt(3)
ds_read_b128 a[164:167],v1 offset:8448
v_cvt_pk_bf16_f32 v99,v102,v103
v_mfma_f32_16x16x32_bf16 v[64:67],a[128:131],v[128:131],v[32:35]
v_exp_f32 v104,v104
v_mfma_f32_16x16x32_bf16 v[64:67],a[132:135],v[136:139],v[64:67]
ds_read_b128 a[168:171],v1 offset:8704
v_lshlrev_b32 v10,20,v6
v_mfma_f32_16x16x32_bf16 v[64:67],a[136:139],v[144:147],v[64:67]
v_exp_f32 v105,v105
v_mfma_f32_16x16x32_bf16 v[64:67],a[140:143],v[152:155],v[64:67]
buffer_load_dwordx4 v27,s[48:51],s83 offen offset:3072 lds
v_mfma_f32_16x16x32_bf16 v[68:71],a[156:159],v[152:155],v[32:35]
ds_read_b128 a[172:175],v1 offset:8960
v_cvt_pk_bf16_f32 v104,v104,v105
v_mfma_f32_16x16x32_bf16 v[68:71],a[152:155],v[144:147],v[68:71]
s_waitcnt lgkmcnt(3)
v_exp_f32 v106,v106
v_mfma_f32_16x16x32_bf16 v[68:71],a[148:151],v[136:139],v[68:71]
s_add_u32 s28,s12,s66
s_addc_u32 s29,s13,0
ds_read_b128 a[188:191],v1 offset:13056
v_mfma_f32_16x16x32_bf16 v[68:71],a[144:147],v[128:131],v[68:71]
s_and_b32 s29,s29,0xffff
v_exp_f32 v107,v107
v_mfma_f32_16x16x32_bf16 v[72:75],a[160:163],v[128:131],v[32:35]
s_waitcnt lgkmcnt(2)
ds_read_b128 a[184:187],v1 offset:12800
v_cvt_pk_bf16_f32 v105,v106,v107
v_mfma_f32_16x16x32_bf16 v[72:75],a[164:167],v[136:139],v[72:75]
buffer_store_dwordx4 a[120:123],v14,s[40:43],s82 offen offset:3072 sc1
v_mfma_f32_16x16x32_bf16 v[72:75],a[168:171],v[144:147],v[72:75]
s_waitcnt lgkmcnt(1)
ds_read_b128 a[180:183],v1 offset:12544
v_mfma_f32_16x16x32_bf16 v[72:75],a[172:175],v[152:155],v[72:75]
s_add_u32 m0,s86,126976
ds_read_b128 a[176:179],v1 offset:12288
v_add_u32 v1,s78,v1
v_mfma_f32_16x16x32_bf16 v[76:79],a[188:191],v[152:155],v[32:35]
s_waitcnt lgkmcnt(0)
v_exp_f32 v108,v108
v_mfma_f32_16x16x32_bf16 v[76:79],a[184:187],v[144:147],v[76:79]
buffer_load_dwordx4 v7,s[48:51],s83 offen lds
v_exp_f32 v109,v109
v_exp_f32 v110,v110
v_exp_f32 v111,v111
v_mfma_f32_16x16x32_bf16 v[76:79],a[180:183],v[136:139],v[76:79]
s_add_u32 s83,s64,0
v_cvt_pk_bf16_f32 v106,v108,v109
v_cvt_pk_bf16_f32 v107,v110,v111
v_mfma_f32_16x16x32_bf16 v[76:79],a[176:179],v[128:131],v[76:79]
s_and_b32 s83,s83,3
v_exp_f32 v112,v112
v_mfma_f32_16x16x32_bf16 v[196:199],a[192:195],v[96:99],v[196:199]
s_lshl_b32 s83,s83,1
v_exp_f32 v113,v113
v_mfma_f32_16x16x32_bf16 v[196:199],a[196:199],v[104:107],v[196:199]
s_or_b32 s83,s83,1
v_exp_f32 v114,v114
v_mfma_f32_16x16x32_bf16 v[204:207],a[200:203],v[96:99],v[204:207]
s_mul_i32 s83,s83,163840
v_exp_f32 v115,v115
s_nop 0
L_pd11_%=:
s_cbranch_execz L_t1c_%=
v_mfma_f32_16x16x32_bf16 v[204:207],a[204:207],v[104:107],v[204:207]
s_add_u32 s83,s83,s85
v_exp_f32 v116,v116
v_mfma_f32_16x16x32_bf16 v[212:215],a[208:211],v[96:99],v[212:215]
v_exp_f32 v117,v117
v_mfma_f32_16x16x32_bf16 v[212:215],a[212:215],v[104:107],v[212:215]
s_add_u32 m0,s86,128000
v_exp_f32 v118,v118
v_mfma_f32_16x16x32_bf16 v[220:223],a[216:219],v[96:99],v[220:223]
v_exp_f32 v119,v119
v_mfma_f32_16x16x32_bf16 v[220:223],a[220:223],v[104:107],v[220:223]
buffer_load_dwordx4 v24,s[48:51],s84 offen lds
v_mfma_f32_16x16x32_bf16 v[228:231],a[224:227],v[96:99],v[228:231]
v_cvt_pk_bf16_f32 v112,v112,v113
v_cvt_pk_bf16_f32 v113,v114,v115
v_mfma_f32_16x16x32_bf16 v[228:231],a[228:231],v[104:107],v[228:231]
v_cvt_pk_bf16_f32 v114,v116,v117
v_cvt_pk_bf16_f32 v115,v118,v119
v_mfma_f32_16x16x32_bf16 v[236:239],a[232:235],v[96:99],v[236:239]
buffer_store_dwordx4 a[124:127],v6,s[40:43],s82 offen offset:1024 sc1
v_mfma_f32_16x16x32_bf16 v[236:239],a[236:239],v[104:107],v[236:239]
v_exp_f32 v120,v120
v_mfma_f32_16x16x32_bf16 v[244:247],a[240:243],v[96:99],v[244:247]
v_exp_f32 v121,v121
v_mfma_f32_16x16x32_bf16 v[244:247],a[244:247],v[104:107],v[244:247]
v_exp_f32 v122,v122
v_mfma_f32_16x16x32_bf16 v[252:255],a[248:251],v[96:99],v[252:255]
v_exp_f32 v123,v123
v_mfma_f32_16x16x32_bf16 v[252:255],a[252:255],v[104:107],v[252:255]
v_exp_f32 v124,v124
v_mfma_f32_16x16x32_bf16 v[56:59],v[28:31],v[96:99],v[56:59]
)A"
R"A(v_exp_f32 v125,v125
v_mfma_f32_16x16x32_bf16 v[56:59],v[28:31],v[104:107],v[56:59]
buffer_load_dwordx4 v25,s[48:51],s84 offen offset:1024 lds
v_mfma_f32_16x16x32_bf16 v[80:83],a[128:131],v[160:163],v[36:39]
v_exp_f32 v126,v126
v_mfma_f32_16x16x32_bf16 v[80:83],a[132:135],v[168:171],v[80:83]
v_exp_f32 v127,v127
v_mfma_f32_16x16x32_bf16 v[80:83],a[136:139],v[176:179],v[80:83]
v_cvt_pk_bf16_f32 v120,v120,v121
v_cvt_pk_bf16_f32 v121,v122,v123
v_mfma_f32_16x16x32_bf16 v[80:83],a[140:143],v[184:187],v[80:83]
v_cvt_pk_bf16_f32 v122,v124,v125
v_cvt_pk_bf16_f32 v123,v126,v127
v_mfma_f32_16x16x32_bf16 v[84:87],a[156:159],v[184:187],v[36:39]
v_exp_f32 v64,v64
v_mfma_f32_16x16x32_bf16 v[84:87],a[152:155],v[176:179],v[84:87]
v_exp_f32 v65,v65
v_mfma_f32_16x16x32_bf16 v[84:87],a[148:151],v[168:171],v[84:87]
v_exp_f32 v66,v66
v_mfma_f32_16x16x32_bf16 v[84:87],a[144:147],v[160:163],v[84:87]
v_exp_f32 v67,v67
v_mfma_f32_16x16x32_bf16 v[88:91],a[160:163],v[160:163],v[36:39]
v_exp_f32 v68,v68
v_mfma_f32_16x16x32_bf16 v[88:91],a[164:167],v[168:171],v[88:91]
buffer_store_dwordx4 a[96:99],v14,s[40:43],s79 offen offset:3072 sc1
v_mfma_f32_16x16x32_bf16 v[88:91],a[168:171],v[176:179],v[88:91]
v_exp_f32 v69,v69
v_mfma_f32_16x16x32_bf16 v[88:91],a[172:175],v[184:187],v[88:91]
buffer_load_dwordx4 v26,s[48:51],s84 offen offset:2048 lds
v_mfma_f32_16x16x32_bf16 v[92:95],a[188:191],v[184:187],v[36:39]
v_exp_f32 v70,v70
v_mfma_f32_16x16x32_bf16 v[92:95],a[184:187],v[176:179],v[92:95]
v_exp_f32 v71,v71
v_mfma_f32_16x16x32_bf16 v[92:95],a[180:183],v[168:171],v[92:95]
v_cvt_pk_bf16_f32 v64,v64,v65
v_cvt_pk_bf16_f32 v65,v66,v67
v_mfma_f32_16x16x32_bf16 v[92:95],a[176:179],v[160:163],v[92:95]
v_cvt_pk_bf16_f32 v66,v68,v69
v_cvt_pk_bf16_f32 v67,v70,v71
v_mfma_f32_16x16x32_bf16 a[32:35],a[192:195],v[112:115],a[32:35]
ds_read_b64_tr_b16 a[192:193],v2
ds_read_b64_tr_b16 a[194:195],v2 offset:4096
v_mfma_f32_16x16x32_bf16 a[32:35],a[196:199],v[120:123],a[32:35]
v_exp_f32 v72,v72
v_mfma_f32_16x16x32_bf16 a[36:39],a[200:203],v[112:115],a[36:39]
ds_read_b64_tr_b16 a[196:197],v2 offset:8192
ds_read_b64_tr_b16 a[198:199],v2 offset:12288
v_mfma_f32_16x16x32_bf16 a[36:39],a[204:207],v[120:123],a[36:39]
buffer_load_dwordx4 v27,s[48:51],s84 offen offset:3072 lds
v_mfma_f32_16x16x32_bf16 a[40:43],a[208:211],v[112:115],a[40:43]
ds_read_b64_tr_b16 a[200:201],v3
ds_read_b64_tr_b16 a[202:203],v3 offset:4096
v_mfma_f32_16x16x32_bf16 a[40:43],a[212:215],v[120:123],a[40:43]
v_exp_f32 v73,v73
v_mfma_f32_16x16x32_bf16 a[44:47],a[216:219],v[112:115],a[44:47]
ds_read_b64_tr_b16 a[204:205],v3 offset:8192
ds_read_b64_tr_b16 a[206:207],v3 offset:12288
v_mfma_f32_16x16x32_bf16 a[44:47],a[220:223],v[120:123],a[44:47]
ds_read_b64_tr_b16 a[208:209],v2 offset:256
v_cvt_pk_bf16_f32 v72,v72,v73
v_mfma_f32_16x16x32_bf16 a[48:51],a[224:227],v[112:115],a[48:51]
v_exp_f32 v74,v74
v_mfma_f32_16x16x32_bf16 a[48:51],a[228:231],v[120:123],a[48:51]
ds_read_b64_tr_b16 a[210:211],v2 offset:4352
ds_read_b64_tr_b16 a[212:213],v2 offset:8448
v_mfma_f32_16x16x32_bf16 a[52:55],a[232:235],v[112:115],a[52:55]
buffer_store_dwordx4 a[100:103],v6,s[40:43],s79 offen offset:1024 sc1
v_mfma_f32_16x16x32_bf16 a[52:55],a[236:239],v[120:123],a[52:55]
s_add_u32 m0,s86,132096
ds_read_b64_tr_b16 a[214:215],v2 offset:12544
ds_read_b64_tr_b16 a[216:217],v3 offset:256
v_mfma_f32_16x16x32_bf16 a[56:59],a[240:243],v[112:115],a[56:59]
v_exp_f32 v75,v75
v_mfma_f32_16x16x32_bf16 a[56:59],a[244:247],v[120:123],a[56:59]
buffer_load_dwordx4 v7,s[48:51],s84 offen lds
v_mfma_f32_16x16x32_bf16 a[60:63],a[248:251],v[112:115],a[60:63]
s_add_u32 s84,s83,20480
ds_read_b64_tr_b16 a[218:219],v3 offset:4352
ds_read_b64_tr_b16 a[220:221],v3 offset:8448
v_mfma_f32_16x16x32_bf16 a[60:63],a[252:255],v[120:123],a[60:63]
v_exp_f32 v76,v76
v_mfma_f32_16x16x32_bf16 v[60:63],v[28:31],v[112:115],v[60:63]
v_exp_f32 v77,v77
v_mfma_f32_16x16x32_bf16 v[60:63],v[28:31],v[120:123],v[60:63]
v_exp_f32 v78,v78
v_mfma_f32_16x16x32_bf16 v[96:99],a[128:131],v[192:195],v[40:43]
v_exp_f32 v79,v79
v_mfma_f32_16x16x32_bf16 v[96:99],a[132:135],v[200:203],v[96:99]
ds_read_b64_tr_b16 a[222:223],v3 offset:12544
v_cvt_pk_bf16_f32 v73,v74,v75
v_mfma_f32_16x16x32_bf16 v[96:99],a[136:139],v[208:211],v[96:99]
v_cvt_pk_bf16_f32 v74,v76,v77
v_cvt_pk_bf16_f32 v75,v78,v79
v_mfma_f32_16x16x32_bf16 v[96:99],a[140:143],v[216:219],v[96:99]
ds_read_b64_tr_b16 a[224:225],v2 offset:512
ds_read_b64_tr_b16 a[226:227],v2 offset:4608
v_mfma_f32_16x16x32_bf16 v[100:103],a[156:159],v[216:219],v[40:43]
v_exp_f32 v80,v80
v_mfma_f32_16x16x32_bf16 v[100:103],a[152:155],v[208:211],v[100:103]
ds_read_b64_tr_b16 a[228:229],v2 offset:8704
ds_read_b64_tr_b16 a[230:231],v2 offset:12800
v_mfma_f32_16x16x32_bf16 v[100:103],a[148:151],v[200:203],v[100:103]
v_exp_f32 v81,v81
v_mfma_f32_16x16x32_bf16 v[100:103],a[144:147],v[192:195],v[100:103]
ds_read_b64_tr_b16 a[232:233],v3 offset:512
ds_read_b64_tr_b16 a[234:235],v3 offset:4608
v_mfma_f32_16x16x32_bf16 v[104:107],a[160:163],v[192:195],v[40:43]
ds_read_b64_tr_b16 a[236:237],v3 offset:8704
v_cvt_pk_bf16_f32 v80,v80,v81
v_mfma_f32_16x16x32_bf16 v[104:107],a[164:167],v[200:203],v[104:107]
buffer_store_dwordx4 a[104:107],v14,s[40:43],s80 offen offset:3072 sc1
v_mfma_f32_16x16x32_bf16 v[104:107],a[168:171],v[208:211],v[104:107]
ds_read_b64_tr_b16 a[238:239],v3 offset:12800
ds_read_b64_tr_b16 a[240:241],v2 offset:768
v_mfma_f32_16x16x32_bf16 v[104:107],a[172:175],v[216:219],v[104:107]
v_exp_f32 v82,v82
v_mfma_f32_16x16x32_bf16 v[108:111],a[188:191],v[216:219],v[40:43]
ds_read_b64_tr_b16 a[242:243],v2 offset:4864
ds_read_b64_tr_b16 a[244:245],v2 offset:8960
v_mfma_f32_16x16x32_bf16 v[108:111],a[184:187],v[208:211],v[108:111]
ds_read_b64_tr_b16 a[246:247],v2 offset:13056
v_add_u32 v2,s78,v2
v_mfma_f32_16x16x32_bf16 v[108:111],a[180:183],v[200:203],v[108:111]
v_exp_f32 v83,v83
v_mfma_f32_16x16x32_bf16 v[108:111],a[176:179],v[192:195],v[108:111]
s_waitcnt lgkmcnt(6)
ds_read_b64_tr_b16 a[248:249],v3 offset:768
ds_read_b64_tr_b16 a[250:251],v3 offset:4864
v_mfma_f32_16x16x32_bf16 a[0:3],a[192:195],v[64:67],a[0:3]
ds_read_b64_tr_b16 a[252:253],v3 offset:8960
v_cvt_pk_bf16_f32 v81,v82,v83
v_mfma_f32_16x16x32_bf16 a[0:3],a[196:199],v[72:75],a[0:3]
ds_read_b64_tr_b16 a[254:255],v3 offset:13056
v_add_u32 v3,s78,v3
v_mfma_f32_16x16x32_bf16 a[4:7],a[200:203],v[64:67],a[4:7]
s_sub_u32 s78,s77,32768
v_exp_f32 v84,v84
v_mfma_f32_16x16x32_bf16 a[4:7],a[204:207],v[72:75],a[4:7]
s_and_b32 s78,s78,0x1ffff
v_exp_f32 v85,v85
v_mfma_f32_16x16x32_bf16 a[8:11],a[208:211],v[64:67],a[8:11]
s_sub_u32 s78,s78,s77
v_exp_f32 v86,v86
v_mfma_f32_16x16x32_bf16 a[8:11],a[212:215],v[72:75],a[8:11]
s_add_u32 s77,s77,s78
v_exp_f32 v87,v87
v_mfma_f32_16x16x32_bf16 a[12:15],a[216:219],v[64:67],a[12:15]
v_cvt_pk_bf16_f32 v82,v84,v85
v_cvt_pk_bf16_f32 v83,v86,v87
v_mfma_f32_16x16x32_bf16 a[12:15],a[220:223],v[72:75],a[12:15]
v_exp_f32 v88,v88
v_mfma_f32_16x16x32_bf16 a[16:19],a[224:227],v[64:67],a[16:19]
v_exp_f32 v89,v89
v_mfma_f32_16x16x32_bf16 a[16:19],a[228:231],v[72:75],a[16:19]
s_waitcnt lgkmcnt(0)
v_exp_f32 v90,v90
v_mfma_f32_16x16x32_bf16 a[20:23],a[232:235],v[64:67],a[20:23]
buffer_store_dwordx4 a[108:111],v6,s[40:43],s80 offen offset:1024 sc1
v_mfma_f32_16x16x32_bf16 a[20:23],a[236:239],v[72:75],a[20:23]
v_exp_f32 v91,v91
v_mfma_f32_16x16x32_bf16 a[24:27],a[240:243],v[64:67],a[24:27]
v_exp_f32 v92,v92
v_mfma_f32_16x16x32_bf16 a[24:27],a[244:247],v[72:75],a[24:27]
v_exp_f32 v93,v93
v_mfma_f32_16x16x32_bf16 a[28:31],a[248:251],v[64:67],a[28:31]
v_exp_f32 v94,v94
v_mfma_f32_16x16x32_bf16 a[28:31],a[252:255],v[72:75],a[28:31]
v_exp_f32 v95,v95
v_mfma_f32_16x16x32_bf16 v[48:51],v[28:31],v[64:67],v[48:51]
v_cvt_pk_bf16_f32 v88,v88,v89
v_cvt_pk_bf16_f32 v89,v90,v91
v_mfma_f32_16x16x32_bf16 v[48:51],v[28:31],v[72:75],v[48:51]
v_cvt_pk_bf16_f32 v90,v92,v93
v_cvt_pk_bf16_f32 v91,v94,v95
s_waitcnt vmcnt(26)
s_barrier
v_mfma_f32_16x16x32_bf16 v[112:115],a[128:131],v[224:227],v[44:47]
v_exp_f32 v96,v96
v_mfma_f32_16x16x32_bf16 v[112:115],a[132:135],v[232:235],v[112:115]
v_exp_f32 v97,v97
v_mfma_f32_16x16x32_bf16 v[112:115],a[136:139],v[240:243],v[112:115]
ds_read_b128 a[128:131],v1
v_cvt_pk_bf16_f32 v96,v96,v97
v_mfma_f32_16x16x32_bf16 v[112:115],a[140:143],v[248:251],v[112:115]
v_exp_f32 v98,v98
v_mfma_f32_16x16x32_bf16 v[116:119],a[156:159],v[248:251],v[44:47]
v_exp_f32 v99,v99
v_mfma_f32_16x16x32_bf16 v[116:119],a[152:155],v[240:243],v[116:119]
s_add_u32 m0,s86,81920
ds_read_b128 a[132:135],v1 offset:256
v_cvt_pk_bf16_f32 v97,v98,v99
v_mfma_f32_16x16x32_bf16 v[116:119],a[148:151],v[232:235],v[116:119]
v_exp_f32 v100,v100
v_mfma_f32_16x16x32_bf16 v[116:119],a[144:147],v[224:227],v[116:119]
buffer_load_dwordx4 v24,s[48:51],s83 offen lds
v_mfma_f32_16x16x32_bf16 v[120:123],a[160:163],v[224:227],v[44:47]
v_exp_f32 v101,v101
v_mfma_f32_16x16x32_bf16 v[120:123],a[164:167],v[232:235],v[120:123]
buffer_store_dwordx4 a[64:67],v6,s[40:43],s79 offen offset:2048 sc1
v_mfma_f32_16x16x32_bf16 v[120:123],a[168:171],v[240:243],v[120:123]
ds_read_b128 a[136:139],v1 offset:512
v_cvt_pk_bf16_f32 v98,v100,v101
v_mfma_f32_16x16x32_bf16 v[120:123],a[172:175],v[248:251],v[120:123]
v_exp_f32 v102,v102
v_mfma_f32_16x16x32_bf16 v[124:127],a[188:191],v[248:251],v[44:47]
v_exp_f32 v103,v103
v_mfma_f32_16x16x32_bf16 v[124:127],a[184:187],v[240:243],v[124:127]
ds_read_b128 a[140:143],v1 offset:768
v_cvt_pk_bf16_f32 v99,v102,v103
v_mfma_f32_16x16x32_bf16 v[124:127],a[180:183],v[232:235],v[124:127]
v_exp_f32 v104,v104
v_mfma_f32_16x16x32_bf16 v[124:127],a[176:179],v[224:227],v[124:127]
v_exp_f32 v105,v105
v_mfma_f32_16x16x32_bf16 v[132:135],a[192:195],v[80:83],v[132:135]
ds_read_b128 a[156:159],v1 offset:4864
v_cvt_pk_bf16_f32 v104,v104,v105
v_mfma_f32_16x16x32_bf16 v[132:135],a[196:199],v[88:91],v[132:135]
buffer_load_dwordx4 v25,s[48:51],s83 offen offset:1024 lds
v_mfma_f32_16x16x32_bf16 v[140:143],a[200:203],v[80:83],v[140:143]
v_exp_f32 v106,v106
v_mfma_f32_16x16x32_bf16 v[140:143],a[204:207],v[88:91],v[140:143]
v_exp_f32 v107,v107
v_mfma_f32_16x16x32_bf16 v[148:151],a[208:211],v[80:83],v[148:151]
ds_read_b128 a[152:155],v1 offset:4608
v_cvt_pk_bf16_f32 v105,v106,v107
v_mfma_f32_16x16x32_bf16 v[148:151],a[212:215],v[88:91],v[148:151]
v_exp_f32 v108,v108
v_mfma_f32_16x16x32_bf16 v[156:159],a[216:219],v[80:83],v[156:159]
v_exp_f32 v109,v109
v_mfma_f32_16x16x32_bf16 v[156:159],a[220:223],v[88:91],v[156:159]
ds_read_b128 a[148:151],v1 offset:4352
v_cvt_pk_bf16_f32 v106,v108,v109
v_mfma_f32_16x16x32_bf16 v[164:167],a[224:227],v[80:83],v[164:167]
v_exp_f32 v110,v110
v_mfma_f32_16x16x32_bf16 v[164:167],a[228:231],v[88:91],v[164:167]
buffer_store_dwordx4 a[68:71],v6,s[40:43],s79 offen sc1
v_mfma_f32_16x16x32_bf16 v[172:175],a[232:235],v[80:83],v[172:175]
s_add_u32 s79,s79,65536
v_exp_f32 v111,v111
v_mfma_f32_16x16x32_bf16 v[172:175],a[236:239],v[88:91],v[172:175]
buffer_load_dwordx4 v26,s[48:51],s83 offen offset:2048 lds
v_mfma_f32_16x16x32_bf16 v[180:183],a[240:243],v[80:83],v[180:183]
ds_read_b128 a[144:147],v1 offset:4096
v_cvt_pk_bf16_f32 v107,v110,v111
v_mfma_f32_16x16x32_bf16 v[180:183],a[244:247],v[88:91],v[180:183]
v_exp_f32 v112,v112
v_mfma_f32_16x16x32_bf16 v[188:191],a[248:251],v[80:83],v[188:191]
ds_read_b128 a[160:163],v1 offset:8192
v_mfma_f32_16x16x32_bf16 v[188:191],a[252:255],v[88:91],v[188:191]
v_exp_f32 v113,v113
v_mfma_f32_16x16x32_bf16 v[52:55],v[28:31],v[80:83],v[52:55]
ds_read_b128 a[164:167],v1 offset:8448
v_cvt_pk_bf16_f32 v112,v112,v113
v_mfma_f32_16x16x32_bf16 v[52:55],v[28:31],v[88:91],v[52:55]
s_waitcnt lgkmcnt(3)
v_exp_f32 v114,v114
v_mfma_f32_16x16x32_bf16 v[64:67],a[128:131],v[128:131],v[32:35]
ds_read_b128 a[168:171],v1 offset:8704
v_mfma_f32_16x16x32_bf16 v[64:67],a[132:135],v[136:139],v[64:67]
v_exp_f32 v115,v115
v_mfma_f32_16x16x32_bf16 v[64:67],a[136:139],v[144:147],v[64:67]
ds_read_b128 a[172:175],v1 offset:8960
v_cvt_pk_bf16_f32 v113,v114,v115
v_mfma_f32_16x16x32_bf16 v[64:67],a[140:143],v[152:155],v[64:67]
buffer_load_dwordx4 v27,s[48:51],s83 offen offset:3072 lds
)A"
R"A(v_mfma_f32_16x16x32_bf16 v[68:71],a[156:159],v[152:155],v[32:35]
ds_read_b128 a[188:191],v1 offset:13056
v_mfma_f32_16x16x32_bf16 v[68:71],a[152:155],v[144:147],v[68:71]
v_exp_f32 v116,v116
v_mfma_f32_16x16x32_bf16 v[68:71],a[148:151],v[136:139],v[68:71]
s_waitcnt lgkmcnt(3)
ds_read_b128 a[184:187],v1 offset:12800
v_mfma_f32_16x16x32_bf16 v[68:71],a[144:147],v[128:131],v[68:71]
v_exp_f32 v117,v117
v_mfma_f32_16x16x32_bf16 v[72:75],a[160:163],v[128:131],v[32:35]
buffer_store_dwordx4 a[72:75],v6,s[40:43],s80 offen offset:2048 sc1
v_mfma_f32_16x16x32_bf16 v[72:75],a[164:167],v[136:139],v[72:75]
s_waitcnt lgkmcnt(3)
ds_read_b128 a[180:183],v1 offset:12544
v_cvt_pk_bf16_f32 v114,v116,v117
v_mfma_f32_16x16x32_bf16 v[72:75],a[168:171],v[144:147],v[72:75]
s_waitcnt lgkmcnt(2)
v_exp_f32 v118,v118
v_mfma_f32_16x16x32_bf16 v[72:75],a[172:175],v[152:155],v[72:75]
s_add_u32 m0,s86,86016
ds_read_b128 a[176:179],v1 offset:12288
v_add_u32 v1,s78,v1
v_mfma_f32_16x16x32_bf16 v[76:79],a[188:191],v[152:155],v[32:35]
s_waitcnt lgkmcnt(0)
v_exp_f32 v119,v119
v_mfma_f32_16x16x32_bf16 v[76:79],a[184:187],v[144:147],v[76:79]
buffer_load_dwordx4 v7,s[48:51],s83 offen lds
v_mfma_f32_16x16x32_bf16 v[76:79],a[180:183],v[136:139],v[76:79]
s_add_u32 s83,s64,1
v_exp_f32 v120,v120
v_mfma_f32_16x16x32_bf16 v[76:79],a[176:179],v[128:131],v[76:79]
s_and_b32 s83,s83,3
v_exp_f32 v121,v121
v_mfma_f32_16x16x32_bf16 v[196:199],a[192:195],v[96:99],v[196:199]
s_lshl_b32 s83,s83,1
v_cvt_pk_bf16_f32 v115,v118,v119
v_cvt_pk_bf16_f32 v120,v120,v121
v_mfma_f32_16x16x32_bf16 v[196:199],a[196:199],v[104:107],v[196:199]
s_mul_i32 s83,s83,163840
v_exp_f32 v122,v122
v_mfma_f32_16x16x32_bf16 v[204:207],a[200:203],v[96:99],v[204:207]
s_add_u32 s83,s83,s85
v_exp_f32 v123,v123
v_mfma_f32_16x16x32_bf16 v[204:207],a[204:207],v[104:107],v[204:207]
v_exp_f32 v124,v124
v_mfma_f32_16x16x32_bf16 v[212:215],a[208:211],v[96:99],v[212:215]
v_exp_f32 v125,v125
v_mfma_f32_16x16x32_bf16 v[212:215],a[212:215],v[104:107],v[212:215]
s_add_u32 m0,s86,87040
v_exp_f32 v126,v126
v_mfma_f32_16x16x32_bf16 v[220:223],a[216:219],v[96:99],v[220:223]
v_exp_f32 v127,v127
v_mfma_f32_16x16x32_bf16 v[220:223],a[220:223],v[104:107],v[220:223]
buffer_load_dwordx4 v24,s[48:51],s84 offen lds
v_mfma_f32_16x16x32_bf16 v[228:231],a[224:227],v[96:99],v[228:231]
v_cvt_pk_bf16_f32 v121,v122,v123
v_cvt_pk_bf16_f32 v122,v124,v125
v_mfma_f32_16x16x32_bf16 v[228:231],a[228:231],v[104:107],v[228:231]
buffer_store_dwordx4 a[76:79],v6,s[40:43],s80 offen sc1
v_mfma_f32_16x16x32_bf16 v[236:239],a[232:235],v[96:99],v[236:239]
s_add_u32 s80,s80,65536
v_exp_f32 v64,v64
v_mfma_f32_16x16x32_bf16 v[236:239],a[236:239],v[104:107],v[236:239]
v_exp_f32 v65,v65
v_mfma_f32_16x16x32_bf16 v[244:247],a[240:243],v[96:99],v[244:247]
v_cvt_pk_bf16_f32 v123,v126,v127
v_cvt_pk_bf16_f32 v64,v64,v65
v_mfma_f32_16x16x32_bf16 v[244:247],a[244:247],v[104:107],v[244:247]
v_exp_f32 v66,v66
v_mfma_f32_16x16x32_bf16 v[252:255],a[248:251],v[96:99],v[252:255]
v_exp_f32 v67,v67
v_mfma_f32_16x16x32_bf16 v[252:255],a[252:255],v[104:107],v[252:255]
v_exp_f32 v68,v68
v_mfma_f32_16x16x32_bf16 v[56:59],v[28:31],v[96:99],v[56:59]
v_exp_f32 v69,v69
v_mfma_f32_16x16x32_bf16 v[56:59],v[28:31],v[104:107],v[56:59]
buffer_load_dwordx4 v25,s[48:51],s84 offen offset:1024 lds
v_mfma_f32_16x16x32_bf16 v[80:83],a[128:131],v[160:163],v[36:39]
v_exp_f32 v70,v70
v_mfma_f32_16x16x32_bf16 v[80:83],a[132:135],v[168:171],v[80:83]
v_exp_f32 v71,v71
v_mfma_f32_16x16x32_bf16 v[80:83],a[136:139],v[176:179],v[80:83]
v_cvt_pk_bf16_f32 v65,v66,v67
v_cvt_pk_bf16_f32 v66,v68,v69
v_mfma_f32_16x16x32_bf16 v[80:83],a[140:143],v[184:187],v[80:83]
v_exp_f32 v72,v72
v_mfma_f32_16x16x32_bf16 v[84:87],a[156:159],v[184:187],v[36:39]
v_exp_f32 v73,v73
v_mfma_f32_16x16x32_bf16 v[84:87],a[152:155],v[176:179],v[84:87]
v_cvt_pk_bf16_f32 v67,v70,v71
v_cvt_pk_bf16_f32 v72,v72,v73
v_mfma_f32_16x16x32_bf16 v[84:87],a[148:151],v[168:171],v[84:87]
v_exp_f32 v74,v74
v_mfma_f32_16x16x32_bf16 v[84:87],a[144:147],v[160:163],v[84:87]
v_exp_f32 v75,v75
v_mfma_f32_16x16x32_bf16 v[88:91],a[160:163],v[160:163],v[36:39]
buffer_store_dwordx4 a[80:83],v6,s[40:43],s81 offen offset:2048 sc1
v_mfma_f32_16x16x32_bf16 v[88:91],a[164:167],v[168:171],v[88:91]
v_exp_f32 v76,v76
v_mfma_f32_16x16x32_bf16 v[88:91],a[168:171],v[176:179],v[88:91]
buffer_load_dwordx4 v26,s[48:51],s84 offen offset:2048 lds
v_mfma_f32_16x16x32_bf16 v[88:91],a[172:175],v[184:187],v[88:91]
v_exp_f32 v77,v77
v_mfma_f32_16x16x32_bf16 v[92:95],a[188:191],v[184:187],v[36:39]
v_exp_f32 v78,v78
v_mfma_f32_16x16x32_bf16 v[92:95],a[184:187],v[176:179],v[92:95]
v_exp_f32 v79,v79
v_mfma_f32_16x16x32_bf16 v[92:95],a[180:183],v[168:171],v[92:95]
v_cvt_pk_bf16_f32 v73,v74,v75
v_cvt_pk_bf16_f32 v74,v76,v77
v_mfma_f32_16x16x32_bf16 v[92:95],a[176:179],v[160:163],v[92:95]
v_exp_f32 v80,v80
v_mfma_f32_16x16x32_bf16 a[32:35],a[192:195],v[112:115],a[32:35]
ds_read_b64_tr_b16 a[192:193],v2
ds_read_b64_tr_b16 a[194:195],v2 offset:4096
v_mfma_f32_16x16x32_bf16 a[32:35],a[196:199],v[120:123],a[32:35]
ds_read_b64_tr_b16 a[196:197],v2 offset:8192
v_cvt_pk_bf16_f32 v75,v78,v79
v_mfma_f32_16x16x32_bf16 a[36:39],a[200:203],v[112:115],a[36:39]
v_exp_f32 v81,v81
v_mfma_f32_16x16x32_bf16 a[36:39],a[204:207],v[120:123],a[36:39]
buffer_load_dwordx4 v27,s[48:51],s84 offen offset:3072 lds
v_mfma_f32_16x16x32_bf16 a[40:43],a[208:211],v[112:115],a[40:43]
ds_read_b64_tr_b16 a[198:199],v2 offset:12288
ds_read_b64_tr_b16 a[200:201],v3
v_mfma_f32_16x16x32_bf16 a[40:43],a[212:215],v[120:123],a[40:43]
ds_read_b64_tr_b16 a[202:203],v3 offset:4096
v_cvt_pk_bf16_f32 v80,v80,v81
v_mfma_f32_16x16x32_bf16 a[44:47],a[216:219],v[112:115],a[44:47]
v_exp_f32 v82,v82
v_mfma_f32_16x16x32_bf16 a[44:47],a[220:223],v[120:123],a[44:47]
ds_read_b64_tr_b16 a[204:205],v3 offset:8192
ds_read_b64_tr_b16 a[206:207],v3 offset:12288
v_mfma_f32_16x16x32_bf16 a[48:51],a[224:227],v[112:115],a[48:51]
v_exp_f32 v83,v83
v_mfma_f32_16x16x32_bf16 a[48:51],a[228:231],v[120:123],a[48:51]
buffer_store_dwordx4 a[84:87],v6,s[40:43],s81 offen sc1
v_mfma_f32_16x16x32_bf16 a[52:55],a[232:235],v[112:115],a[52:55]
s_add_u32 s81,s81,65536
ds_read_b64_tr_b16 a[208:209],v2 offset:256
ds_read_b64_tr_b16 a[210:211],v2 offset:4352
v_mfma_f32_16x16x32_bf16 a[52:55],a[236:239],v[120:123],a[52:55]
s_add_u32 m0,s86,91136
ds_read_b64_tr_b16 a[212:213],v2 offset:8448
v_cvt_pk_bf16_f32 v81,v82,v83
v_mfma_f32_16x16x32_bf16 a[56:59],a[240:243],v[112:115],a[56:59]
v_exp_f32 v84,v84
v_mfma_f32_16x16x32_bf16 a[56:59],a[244:247],v[120:123],a[56:59]
buffer_load_dwordx4 v7,s[48:51],s84 offen lds
v_mfma_f32_16x16x32_bf16 a[60:63],a[248:251],v[112:115],a[60:63]
s_add_u32 s84,s83,20480
ds_read_b64_tr_b16 a[214:215],v2 offset:12544
ds_read_b64_tr_b16 a[216:217],v3 offset:256
v_mfma_f32_16x16x32_bf16 a[60:63],a[252:255],v[120:123],a[60:63]
v_exp_f32 v85,v85
v_mfma_f32_16x16x32_bf16 v[60:63],v[28:31],v[112:115],v[60:63]
ds_read_b64_tr_b16 a[218:219],v3 offset:4352
ds_read_b64_tr_b16 a[220:221],v3 offset:8448
v_mfma_f32_16x16x32_bf16 v[60:63],v[28:31],v[120:123],v[60:63]
ds_read_b64_tr_b16 a[222:223],v3 offset:12544
v_cvt_pk_bf16_f32 v82,v84,v85
v_mfma_f32_16x16x32_bf16 v[96:99],a[128:131],v[192:195],v[40:43]
v_exp_f32 v86,v86
v_mfma_f32_16x16x32_bf16 v[96:99],a[132:135],v[200:203],v[96:99]
ds_read_b64_tr_b16 a[224:225],v2 offset:512
ds_read_b64_tr_b16 a[226:227],v2 offset:4608
v_mfma_f32_16x16x32_bf16 v[96:99],a[136:139],v[208:211],v[96:99]
v_exp_f32 v87,v87
v_mfma_f32_16x16x32_bf16 v[96:99],a[140:143],v[216:219],v[96:99]
ds_read_b64_tr_b16 a[228:229],v2 offset:8704
ds_read_b64_tr_b16 a[230:231],v2 offset:12800
v_mfma_f32_16x16x32_bf16 v[100:103],a[156:159],v[216:219],v[40:43]
ds_read_b64_tr_b16 a[232:233],v3 offset:512
v_cvt_pk_bf16_f32 v83,v86,v87
v_mfma_f32_16x16x32_bf16 v[100:103],a[152:155],v[208:211],v[100:103]
v_exp_f32 v88,v88
v_mfma_f32_16x16x32_bf16 v[100:103],a[148:151],v[200:203],v[100:103]
ds_read_b64_tr_b16 a[234:235],v3 offset:4608
ds_read_b64_tr_b16 a[236:237],v3 offset:8704
v_mfma_f32_16x16x32_bf16 v[100:103],a[144:147],v[192:195],v[100:103]
v_exp_f32 v89,v89
v_mfma_f32_16x16x32_bf16 v[104:107],a[160:163],v[192:195],v[40:43]
buffer_store_dwordx4 a[88:91],v6,s[40:43],s82 offen offset:2048 sc1
v_mfma_f32_16x16x32_bf16 v[104:107],a[164:167],v[200:203],v[104:107]
ds_read_b64_tr_b16 a[238:239],v3 offset:12800
ds_read_b64_tr_b16 a[240:241],v2 offset:768
v_mfma_f32_16x16x32_bf16 v[104:107],a[168:171],v[208:211],v[104:107]
ds_read_b64_tr_b16 a[242:243],v2 offset:4864
v_cvt_pk_bf16_f32 v88,v88,v89
v_mfma_f32_16x16x32_bf16 v[104:107],a[172:175],v[216:219],v[104:107]
v_exp_f32 v90,v90
v_mfma_f32_16x16x32_bf16 v[108:111],a[188:191],v[216:219],v[40:43]
ds_read_b64_tr_b16 a[244:245],v2 offset:8960
ds_read_b64_tr_b16 a[246:247],v2 offset:13056
v_mfma_f32_16x16x32_bf16 v[108:111],a[184:187],v[208:211],v[108:111]
ds_read_b64_tr_b16 a[248:249],v3 offset:768
v_add_u32 v2,s78,v2
v_mfma_f32_16x16x32_bf16 v[108:111],a[180:183],v[200:203],v[108:111]
v_exp_f32 v91,v91
v_mfma_f32_16x16x32_bf16 v[108:111],a[176:179],v[192:195],v[108:111]
s_waitcnt lgkmcnt(6)
ds_read_b64_tr_b16 a[250:251],v3 offset:4864
ds_read_b64_tr_b16 a[252:253],v3 offset:8960
v_mfma_f32_16x16x32_bf16 a[0:3],a[192:195],v[64:67],a[0:3]
ds_read_b64_tr_b16 a[254:255],v3 offset:13056
v_cvt_pk_bf16_f32 v89,v90,v91
v_mfma_f32_16x16x32_bf16 a[0:3],a[196:199],v[72:75],a[0:3]
v_exp_f32 v92,v92
v_mfma_f32_16x16x32_bf16 a[4:7],a[200:203],v[64:67],a[4:7]
v_exp_f32 v93,v93
v_mfma_f32_16x16x32_bf16 a[4:7],a[204:207],v[72:75],a[4:7]
v_exp_f32 v94,v94
v_mfma_f32_16x16x32_bf16 a[8:11],a[208:211],v[64:67],a[8:11]
v_exp_f32 v95,v95
v_mfma_f32_16x16x32_bf16 a[8:11],a[212:215],v[72:75],a[8:11]
v_cvt_pk_bf16_f32 v90,v92,v93
v_cvt_pk_bf16_f32 v91,v94,v95
v_mfma_f32_16x16x32_bf16 a[12:15],a[216:219],v[64:67],a[12:15]
s_cmp_ge_u32 s63,2
s_cselect_b32 s6,L_gbS_%=-L_pcg_%=,s6
s_cselect_b32 s10,L_geS_%=-L_gbS_%=,s10
v_mfma_f32_16x16x32_bf16 a[12:15],a[220:223],v[72:75],a[12:15]
s_add_u32 s8,s8,s6
s_addc_u32 s9,s9,0
s_and_b32 s8,s8,0xffffff80
v_mfma_f32_16x16x32_bf16 a[16:19],a[224:227],v[64:67],a[16:19]
s_and_b32 s9,s9,0xffff
v_exp_f32 v96,v96
v_mfma_f32_16x16x32_bf16 a[16:19],a[228:231],v[72:75],a[16:19]
buffer_store_dwordx4 a[92:95],v6,s[40:43],s82 offen sc1
v_mfma_f32_16x16x32_bf16 a[20:23],a[232:235],v[64:67],a[20:23]
s_add_u32 s82,s82,65536
v_exp_f32 v97,v97
s_waitcnt lgkmcnt(0)
v_mfma_f32_16x16x32_bf16 a[20:23],a[236:239],v[72:75],a[20:23]
v_exp_f32 v98,v98
v_mfma_f32_16x16x32_bf16 a[24:27],a[240:243],v[64:67],a[24:27]
v_exp_f32 v99,v99
v_mfma_f32_16x16x32_bf16 a[24:27],a[244:247],v[72:75],a[24:27]
v_exp_f32 v100,v100
v_mfma_f32_16x16x32_bf16 a[28:31],a[248:251],v[64:67],a[28:31]
v_exp_f32 v101,v101
v_mfma_f32_16x16x32_bf16 a[28:31],a[252:255],v[72:75],a[28:31]
v_exp_f32 v102,v102
v_mfma_f32_16x16x32_bf16 v[48:51],v[28:31],v[64:67],v[48:51]
v_exp_f32 v103,v103
v_mfma_f32_16x16x32_bf16 v[48:51],v[28:31],v[72:75],v[48:51]
v_cvt_pk_bf16_f32 v96,v96,v97
v_cvt_pk_bf16_f32 v97,v98,v99
s_waitcnt vmcnt(36)
s_barrier
v_mfma_f32_16x16x32_bf16 v[112:115],a[128:131],v[224:227],v[44:47]
ds_read_b128 a[128:131],v1
v_cvt_pk_bf16_f32 v98,v100,v101
v_mfma_f32_16x16x32_bf16 v[112:115],a[132:135],v[232:235],v[112:115]
v_cvt_pk_bf16_f32 v99,v102,v103
v_add_u32 v3,s78,v3
v_mfma_f32_16x16x32_bf16 v[112:115],a[136:139],v[240:243],v[112:115]
s_sub_u32 s78,s77,32768
v_exp_f32 v104,v104
v_mfma_f32_16x16x32_bf16 v[112:115],a[140:143],v[248:251],v[112:115]
s_and_b32 s78,s78,0x1ffff
v_exp_f32 v105,v105
v_mfma_f32_16x16x32_bf16 v[116:119],a[156:159],v[248:251],v[44:47]
s_sub_u32 s78,s78,s77
ds_read_b128 a[132:135],v1 offset:256
v_cvt_pk_bf16_f32 v104,v104,v105
v_mfma_f32_16x16x32_bf16 v[116:119],a[152:155],v[240:243],v[116:119]
s_add_u32 m0,s86,40960
v_exp_f32 v106,v106
v_mfma_f32_16x16x32_bf16 v[116:119],a[148:151],v[232:235],v[116:119]
s_add_u32 s77,s77,s78
v_exp_f32 v107,v107
v_mfma_f32_16x16x32_bf16 v[116:119],a[144:147],v[224:227],v[116:119]
buffer_load_dwordx4 v24,s[48:51],s83 offen lds
v_mfma_f32_16x16x32_bf16 v[120:123],a[160:163],v[224:227],v[44:47]
)A"
R"A(ds_read_b128 a[136:139],v1 offset:512
v_cvt_pk_bf16_f32 v105,v106,v107
v_mfma_f32_16x16x32_bf16 v[120:123],a[164:167],v[232:235],v[120:123]
v_exp_f32 v108,v108
v_mfma_f32_16x16x32_bf16 v[120:123],a[168:171],v[240:243],v[120:123]
v_exp_f32 v109,v109
v_mfma_f32_16x16x32_bf16 v[120:123],a[172:175],v[248:251],v[120:123]
ds_read_b128 a[140:143],v1 offset:768
v_cvt_pk_bf16_f32 v106,v108,v109
v_mfma_f32_16x16x32_bf16 v[124:127],a[188:191],v[248:251],v[44:47]
v_exp_f32 v110,v110
v_mfma_f32_16x16x32_bf16 v[124:127],a[184:187],v[240:243],v[124:127]
v_exp_f32 v111,v111
v_mfma_f32_16x16x32_bf16 v[124:127],a[180:183],v[232:235],v[124:127]
ds_read_b128 a[156:159],v1 offset:4864
v_cvt_pk_bf16_f32 v107,v110,v111
s_nop 0
L_pd12_%=:
s_cbranch_execz L_trt1_%=
v_mfma_f32_16x16x32_bf16 v[124:127],a[176:179],v[224:227],v[124:127]
v_exp_f32 v112,v112
v_mfma_f32_16x16x32_bf16 v[132:135],a[192:195],v[80:83],v[132:135]
v_exp_f32 v113,v113
v_mfma_f32_16x16x32_bf16 v[132:135],a[196:199],v[88:91],v[132:135]
buffer_load_dwordx4 v25,s[48:51],s83 offen offset:1024 lds
v_mfma_f32_16x16x32_bf16 v[140:143],a[200:203],v[80:83],v[140:143]
ds_read_b128 a[152:155],v1 offset:4608
v_cvt_pk_bf16_f32 v112,v112,v113
v_mfma_f32_16x16x32_bf16 v[140:143],a[204:207],v[88:91],v[140:143]
v_exp_f32 v114,v114
v_mfma_f32_16x16x32_bf16 v[148:151],a[208:211],v[80:83],v[148:151]
v_exp_f32 v115,v115
v_mfma_f32_16x16x32_bf16 v[148:151],a[212:215],v[88:91],v[148:151]
ds_read_b128 a[148:151],v1 offset:4352
v_cvt_pk_bf16_f32 v113,v114,v115
v_mfma_f32_16x16x32_bf16 v[156:159],a[216:219],v[80:83],v[156:159]
v_exp_f32 v116,v116
v_mfma_f32_16x16x32_bf16 v[156:159],a[220:223],v[88:91],v[156:159]
v_exp_f32 v117,v117
v_mfma_f32_16x16x32_bf16 v[164:167],a[224:227],v[80:83],v[164:167]
buffer_load_dword v5,v4,s[8:11],s7 offen
v_mfma_f32_16x16x32_bf16 v[164:167],a[228:231],v[88:91],v[164:167]
ds_read_b128 a[144:147],v1 offset:4096
v_cvt_pk_bf16_f32 v114,v116,v117
v_mfma_f32_16x16x32_bf16 v[172:175],a[232:235],v[80:83],v[172:175]
v_exp_f32 v118,v118
v_mfma_f32_16x16x32_bf16 v[172:175],a[236:239],v[88:91],v[172:175]
v_exp_f32 v119,v119
v_mfma_f32_16x16x32_bf16 v[180:183],a[240:243],v[80:83],v[180:183]
ds_read_b128 a[160:163],v1 offset:8192
v_cvt_pk_bf16_f32 v115,v118,v119
v_mfma_f32_16x16x32_bf16 v[180:183],a[244:247],v[88:91],v[180:183]
v_exp_f32 v120,v120
v_mfma_f32_16x16x32_bf16 v[188:191],a[248:251],v[80:83],v[188:191]
buffer_load_dwordx4 v26,s[48:51],s83 offen offset:2048 lds
v_mfma_f32_16x16x32_bf16 v[188:191],a[252:255],v[88:91],v[188:191]
ds_read_b128 a[164:167],v1 offset:8448
v_mfma_f32_16x16x32_bf16 v[52:55],v[28:31],v[80:83],v[52:55]
v_exp_f32 v121,v121
v_mfma_f32_16x16x32_bf16 v[52:55],v[28:31],v[88:91],v[52:55]
s_waitcnt lgkmcnt(2)
ds_read_b128 a[168:171],v1 offset:8704
v_cvt_pk_bf16_f32 v120,v120,v121
v_mfma_f32_16x16x32_bf16 v[64:67],a[128:131],v[128:131],v[32:35]
v_exp_f32 v122,v122
v_mfma_f32_16x16x32_bf16 v[64:67],a[132:135],v[136:139],v[64:67]
ds_read_b128 a[172:175],v1 offset:8960
v_mfma_f32_16x16x32_bf16 v[64:67],a[136:139],v[144:147],v[64:67]
v_exp_f32 v123,v123
v_mfma_f32_16x16x32_bf16 v[64:67],a[140:143],v[152:155],v[64:67]
buffer_load_dwordx4 v27,s[48:51],s83 offen offset:3072 lds
v_mfma_f32_16x16x32_bf16 v[68:71],a[156:159],v[152:155],v[32:35]
ds_read_b128 a[188:191],v1 offset:13056
v_cvt_pk_bf16_f32 v121,v122,v123
v_mfma_f32_16x16x32_bf16 v[68:71],a[152:155],v[144:147],v[68:71]
v_exp_f32 v124,v124
v_mfma_f32_16x16x32_bf16 v[68:71],a[148:151],v[136:139],v[68:71]
ds_read_b128 a[184:187],v1 offset:12800
v_mfma_f32_16x16x32_bf16 v[68:71],a[144:147],v[128:131],v[68:71]
s_waitcnt lgkmcnt(3)
v_exp_f32 v125,v125
v_mfma_f32_16x16x32_bf16 v[72:75],a[160:163],v[128:131],v[32:35]
ds_read_b128 a[180:183],v1 offset:12544
v_cvt_pk_bf16_f32 v122,v124,v125
v_mfma_f32_16x16x32_bf16 v[72:75],a[164:167],v[136:139],v[72:75]
v_exp_f32 v126,v126
v_mfma_f32_16x16x32_bf16 v[72:75],a[168:171],v[144:147],v[72:75]
s_waitcnt lgkmcnt(2)
ds_read_b128 a[176:179],v1 offset:12288
v_add_u32 v1,s78,v1
v_mfma_f32_16x16x32_bf16 v[72:75],a[172:175],v[152:155],v[72:75]
s_add_u32 m0,s86,45056
v_exp_f32 v127,v127
v_mfma_f32_16x16x32_bf16 v[76:79],a[188:191],v[152:155],v[32:35]
s_waitcnt lgkmcnt(0)
v_cvt_pk_bf16_f32 v123,v126,v127
v_lshrrev_b32 v1,6,v6
v_mfma_f32_16x16x32_bf16 v[76:79],a[184:187],v[144:147],v[76:79]
buffer_load_dwordx4 v7,s[48:51],s83 offen lds
v_mfma_f32_16x16x32_bf16 v[76:79],a[180:183],v[136:139],v[76:79]
s_add_u32 s83,s64,2
v_exp_f32 v64,v64
v_mfma_f32_16x16x32_bf16 v[76:79],a[176:179],v[128:131],v[76:79]
s_and_b32 s83,s83,3
v_exp_f32 v65,v65
v_mfma_f32_16x16x32_bf16 v[196:199],a[192:195],v[96:99],v[196:199]
s_lshl_b32 s83,s83,1
v_exp_f32 v66,v66
v_mfma_f32_16x16x32_bf16 v[196:199],a[196:199],v[104:107],v[196:199]
s_mul_i32 s83,s83,163840
v_exp_f32 v67,v67
v_mfma_f32_16x16x32_bf16 v[204:207],a[200:203],v[96:99],v[204:207]
s_add_u32 s83,s83,s85
v_exp_f32 v68,v68
v_mfma_f32_16x16x32_bf16 v[204:207],a[204:207],v[104:107],v[204:207]
v_exp_f32 v69,v69
v_mfma_f32_16x16x32_bf16 v[212:215],a[208:211],v[96:99],v[212:215]
v_exp_f32 v70,v70
v_mfma_f32_16x16x32_bf16 v[212:215],a[212:215],v[104:107],v[212:215]
s_add_u32 m0,s86,46080
v_exp_f32 v71,v71
v_mfma_f32_16x16x32_bf16 v[220:223],a[216:219],v[96:99],v[220:223]
v_cvt_pk_bf16_f32 v64,v64,v65
v_cvt_pk_bf16_f32 v65,v66,v67
v_mfma_f32_16x16x32_bf16 v[220:223],a[220:223],v[104:107],v[220:223]
buffer_load_dwordx4 v24,s[48:51],s84 offen lds
v_mfma_f32_16x16x32_bf16 v[228:231],a[224:227],v[96:99],v[228:231]
v_cvt_pk_bf16_f32 v66,v68,v69
v_cvt_pk_bf16_f32 v67,v70,v71
v_mfma_f32_16x16x32_bf16 v[228:231],a[228:231],v[104:107],v[228:231]
v_exp_f32 v72,v72
v_mfma_f32_16x16x32_bf16 v[236:239],a[232:235],v[96:99],v[236:239]
v_exp_f32 v73,v73
v_mfma_f32_16x16x32_bf16 v[236:239],a[236:239],v[104:107],v[236:239]
v_exp_f32 v74,v74
v_mfma_f32_16x16x32_bf16 v[244:247],a[240:243],v[96:99],v[244:247]
v_exp_f32 v75,v75
v_mfma_f32_16x16x32_bf16 v[244:247],a[244:247],v[104:107],v[244:247]
v_exp_f32 v76,v76
v_mfma_f32_16x16x32_bf16 v[252:255],a[248:251],v[96:99],v[252:255]
v_exp_f32 v77,v77
v_mfma_f32_16x16x32_bf16 v[252:255],a[252:255],v[104:107],v[252:255]
v_exp_f32 v78,v78
v_mfma_f32_16x16x32_bf16 v[56:59],v[28:31],v[96:99],v[56:59]
v_exp_f32 v79,v79
v_mfma_f32_16x16x32_bf16 v[56:59],v[28:31],v[104:107],v[56:59]
buffer_load_dwordx4 v25,s[48:51],s84 offen offset:1024 lds
v_mfma_f32_16x16x32_bf16 v[80:83],a[128:131],v[160:163],v[36:39]
v_cvt_pk_bf16_f32 v72,v72,v73
v_cvt_pk_bf16_f32 v73,v74,v75
v_mfma_f32_16x16x32_bf16 v[80:83],a[132:135],v[168:171],v[80:83]
v_cvt_pk_bf16_f32 v74,v76,v77
v_cvt_pk_bf16_f32 v75,v78,v79
v_mfma_f32_16x16x32_bf16 v[80:83],a[136:139],v[176:179],v[80:83]
v_mul_u32_u24 v1,320,v1
v_mfma_f32_16x16x32_bf16 v[80:83],a[140:143],v[184:187],v[80:83]
v_mfma_f32_16x16x32_bf16 v[84:87],a[156:159],v[184:187],v[36:39]
v_mfma_f32_16x16x32_bf16 v[84:87],a[152:155],v[176:179],v[84:87]
v_mfma_f32_16x16x32_bf16 v[84:87],a[148:151],v[168:171],v[84:87]
v_mfma_f32_16x16x32_bf16 v[84:87],a[144:147],v[160:163],v[84:87]
v_mfma_f32_16x16x32_bf16 v[88:91],a[160:163],v[160:163],v[36:39]
v_mfma_f32_16x16x32_bf16 v[88:91],a[164:167],v[168:171],v[88:91]
buffer_load_dwordx4 v26,s[48:51],s84 offen offset:2048 lds
v_mfma_f32_16x16x32_bf16 v[88:91],a[168:171],v[176:179],v[88:91]
v_exp_f32 v80,v80
v_mfma_f32_16x16x32_bf16 v[88:91],a[172:175],v[184:187],v[88:91]
v_exp_f32 v81,v81
v_mfma_f32_16x16x32_bf16 v[92:95],a[188:191],v[184:187],v[36:39]
v_exp_f32 v82,v82
v_mfma_f32_16x16x32_bf16 v[92:95],a[184:187],v[176:179],v[92:95]
v_exp_f32 v83,v83
v_mfma_f32_16x16x32_bf16 v[92:95],a[180:183],v[168:171],v[92:95]
v_exp_f32 v84,v84
v_mfma_f32_16x16x32_bf16 v[92:95],a[176:179],v[160:163],v[92:95]
v_exp_f32 v85,v85
v_mfma_f32_16x16x32_bf16 a[32:35],a[192:195],v[112:115],a[32:35]
ds_read_b64_tr_b16 a[192:193],v2
ds_read_b64_tr_b16 a[194:195],v2 offset:4096
v_mfma_f32_16x16x32_bf16 a[32:35],a[196:199],v[120:123],a[32:35]
ds_read_b64_tr_b16 a[196:197],v2 offset:8192
v_cvt_pk_bf16_f32 v80,v80,v81
v_mfma_f32_16x16x32_bf16 a[36:39],a[200:203],v[112:115],a[36:39]
ds_read_b64_tr_b16 a[198:199],v2 offset:12288
v_cvt_pk_bf16_f32 v81,v82,v83
v_mfma_f32_16x16x32_bf16 a[36:39],a[204:207],v[120:123],a[36:39]
buffer_load_dwordx4 v27,s[48:51],s84 offen offset:3072 lds
v_mfma_f32_16x16x32_bf16 a[40:43],a[208:211],v[112:115],a[40:43]
ds_read_b64_tr_b16 a[200:201],v3
ds_read_b64_tr_b16 a[202:203],v3 offset:4096
v_mfma_f32_16x16x32_bf16 a[40:43],a[212:215],v[120:123],a[40:43]
ds_read_b64_tr_b16 a[204:205],v3 offset:8192
v_cvt_pk_bf16_f32 v82,v84,v85
v_mfma_f32_16x16x32_bf16 a[44:47],a[216:219],v[112:115],a[44:47]
v_exp_f32 v86,v86
v_mfma_f32_16x16x32_bf16 a[44:47],a[220:223],v[120:123],a[44:47]
ds_read_b64_tr_b16 a[206:207],v3 offset:12288
ds_read_b64_tr_b16 a[208:209],v2 offset:256
v_mfma_f32_16x16x32_bf16 a[48:51],a[224:227],v[112:115],a[48:51]
v_exp_f32 v87,v87
v_mfma_f32_16x16x32_bf16 a[48:51],a[228:231],v[120:123],a[48:51]
ds_read_b64_tr_b16 a[210:211],v2 offset:4352
ds_read_b64_tr_b16 a[212:213],v2 offset:8448
v_mfma_f32_16x16x32_bf16 a[52:55],a[232:235],v[112:115],a[52:55]
ds_read_b64_tr_b16 a[214:215],v2 offset:12544
v_cvt_pk_bf16_f32 v83,v86,v87
v_mfma_f32_16x16x32_bf16 a[52:55],a[236:239],v[120:123],a[52:55]
s_add_u32 m0,s86,50176
v_exp_f32 v88,v88
v_mfma_f32_16x16x32_bf16 a[56:59],a[240:243],v[112:115],a[56:59]
ds_read_b64_tr_b16 a[216:217],v3 offset:256
ds_read_b64_tr_b16 a[218:219],v3 offset:4352
v_mfma_f32_16x16x32_bf16 a[56:59],a[244:247],v[120:123],a[56:59]
buffer_load_dwordx4 v7,s[48:51],s84 offen lds
v_mfma_f32_16x16x32_bf16 a[60:63],a[248:251],v[112:115],a[60:63]
s_add_u32 s84,s83,20480
ds_read_b64_tr_b16 a[220:221],v3 offset:8448
ds_read_b64_tr_b16 a[222:223],v3 offset:12544
v_mfma_f32_16x16x32_bf16 a[60:63],a[252:255],v[120:123],a[60:63]
v_exp_f32 v89,v89
v_mfma_f32_16x16x32_bf16 v[60:63],v[28:31],v[112:115],v[60:63]
ds_read_b64_tr_b16 a[224:225],v2 offset:512
ds_read_b64_tr_b16 a[226:227],v2 offset:4608
v_mfma_f32_16x16x32_bf16 v[60:63],v[28:31],v[120:123],v[60:63]
ds_read_b64_tr_b16 a[228:229],v2 offset:8704
v_cvt_pk_bf16_f32 v88,v88,v89
v_mfma_f32_16x16x32_bf16 v[96:99],a[128:131],v[192:195],v[40:43]
v_exp_f32 v90,v90
v_mfma_f32_16x16x32_bf16 v[96:99],a[132:135],v[200:203],v[96:99]
ds_read_b64_tr_b16 a[230:231],v2 offset:12800
ds_read_b64_tr_b16 a[232:233],v3 offset:512
v_mfma_f32_16x16x32_bf16 v[96:99],a[136:139],v[208:211],v[96:99]
v_exp_f32 v91,v91
v_mfma_f32_16x16x32_bf16 v[96:99],a[140:143],v[216:219],v[96:99]
ds_read_b64_tr_b16 a[234:235],v3 offset:4608
ds_read_b64_tr_b16 a[236:237],v3 offset:8704
v_mfma_f32_16x16x32_bf16 v[100:103],a[156:159],v[216:219],v[40:43]
ds_read_b64_tr_b16 a[238:239],v3 offset:12800
v_cvt_pk_bf16_f32 v89,v90,v91
v_mfma_f32_16x16x32_bf16 v[100:103],a[152:155],v[208:211],v[100:103]
v_exp_f32 v92,v92
v_mfma_f32_16x16x32_bf16 v[100:103],a[148:151],v[200:203],v[100:103]
ds_read_b64_tr_b16 a[240:241],v2 offset:768
ds_read_b64_tr_b16 a[242:243],v2 offset:4864
v_mfma_f32_16x16x32_bf16 v[100:103],a[144:147],v[192:195],v[100:103]
v_exp_f32 v93,v93
v_mfma_f32_16x16x32_bf16 v[104:107],a[160:163],v[192:195],v[40:43]
ds_read_b64_tr_b16 a[244:245],v2 offset:8960
ds_read_b64_tr_b16 a[246:247],v2 offset:13056
v_mfma_f32_16x16x32_bf16 v[104:107],a[164:167],v[200:203],v[104:107]
ds_read_b64_tr_b16 a[248:249],v3 offset:768
v_cvt_pk_bf16_f32 v90,v92,v93
v_mfma_f32_16x16x32_bf16 v[104:107],a[168:171],v[208:211],v[104:107]
ds_read_b64_tr_b16 a[250:251],v3 offset:4864
v_bfe_u32 v2,v6,4,2
v_mfma_f32_16x16x32_bf16 v[104:107],a[172:175],v[216:219],v[104:107]
ds_read_b64_tr_b16 a[252:253],v3 offset:8960
v_lshl_add_u32 v1,v2,3,v1
v_mfma_f32_16x16x32_bf16 v[108:111],a[188:191],v[216:219],v[40:43]
ds_read_b64_tr_b16 a[254:255],v3 offset:13056
v_add_u32 v1,122880,v1
v_mfma_f32_16x16x32_bf16 v[108:111],a[184:187],v[208:211],v[108:111]
v_exp_f32 v94,v94
v_mfma_f32_16x16x32_bf16 v[108:111],a[180:183],v[200:203],v[108:111]
v_exp_f32 v95,v95
v_mfma_f32_16x16x32_bf16 v[108:111],a[176:179],v[192:195],v[108:111]
s_waitcnt lgkmcnt(6)
v_cvt_pk_bf16_f32 v91,v94,v95
)A"
R"A(v_bfe_u32 v2,v6,8,1
v_mfma_f32_16x16x32_bf16 a[0:3],a[192:195],v[64:67],a[0:3]
v_exp_f32 v96,v96
v_mfma_f32_16x16x32_bf16 a[0:3],a[196:199],v[72:75],a[0:3]
v_exp_f32 v97,v97
v_mfma_f32_16x16x32_bf16 a[4:7],a[200:203],v[64:67],a[4:7]
v_exp_f32 v98,v98
v_mfma_f32_16x16x32_bf16 a[4:7],a[204:207],v[72:75],a[4:7]
v_exp_f32 v99,v99
v_mfma_f32_16x16x32_bf16 a[8:11],a[208:211],v[64:67],a[8:11]
v_exp_f32 v100,v100
v_mfma_f32_16x16x32_bf16 a[8:11],a[212:215],v[72:75],a[8:11]
v_exp_f32 v101,v101
v_mfma_f32_16x16x32_bf16 a[12:15],a[216:219],v[64:67],a[12:15]
v_exp_f32 v102,v102
v_mfma_f32_16x16x32_bf16 a[12:15],a[220:223],v[72:75],a[12:15]
v_exp_f32 v103,v103
v_mfma_f32_16x16x32_bf16 a[16:19],a[224:227],v[64:67],a[16:19]
v_cvt_pk_bf16_f32 v96,v96,v97
v_cvt_pk_bf16_f32 v97,v98,v99
v_mfma_f32_16x16x32_bf16 a[16:19],a[228:231],v[72:75],a[16:19]
v_cvt_pk_bf16_f32 v98,v100,v101
v_cvt_pk_bf16_f32 v99,v102,v103
v_mfma_f32_16x16x32_bf16 a[20:23],a[232:235],v[64:67],a[20:23]
v_exp_f32 v104,v104
v_mfma_f32_16x16x32_bf16 a[20:23],a[236:239],v[72:75],a[20:23]
v_exp_f32 v105,v105
v_mfma_f32_16x16x32_bf16 a[24:27],a[240:243],v[64:67],a[24:27]
s_waitcnt lgkmcnt(0)
v_exp_f32 v106,v106
v_mfma_f32_16x16x32_bf16 a[24:27],a[244:247],v[72:75],a[24:27]
v_exp_f32 v107,v107
v_mfma_f32_16x16x32_bf16 a[28:31],a[248:251],v[64:67],a[28:31]
v_exp_f32 v108,v108
v_mfma_f32_16x16x32_bf16 a[28:31],a[252:255],v[72:75],a[28:31]
v_exp_f32 v109,v109
v_mfma_f32_16x16x32_bf16 v[48:51],v[28:31],v[64:67],v[48:51]
v_exp_f32 v110,v110
v_mfma_f32_16x16x32_bf16 v[48:51],v[28:31],v[72:75],v[48:51]
v_exp_f32 v111,v111
s_waitcnt vmcnt(11)
s_barrier
v_mfma_f32_16x16x32_bf16 v[112:115],a[128:131],v[224:227],v[44:47]
buffer_store_dwordx2 v[8:9],v10,s[44:47],s18 offen offset:256 sc1
v_mfma_f32_16x16x32_bf16 v[112:115],a[132:135],v[232:235],v[112:115]
v_cvt_pk_bf16_f32 v104,v104,v105
v_cvt_pk_bf16_f32 v105,v106,v107
v_mfma_f32_16x16x32_bf16 v[112:115],a[136:139],v[240:243],v[112:115]
v_cvt_pk_bf16_f32 v106,v108,v109
v_cvt_pk_bf16_f32 v107,v110,v111
v_mfma_f32_16x16x32_bf16 v[112:115],a[140:143],v[248:251],v[112:115]
s_add_u32 m0,s86,0
v_cmp_nlt_f32 s[54:55],v48,s73
v_max_f32 v49,0x00800000,v48
v_mfma_f32_16x16x32_bf16 v[116:119],a[156:159],v[248:251],v[44:47]
v_rcp_f32 v49,v49
v_mfma_f32_16x16x32_bf16 v[116:119],a[152:155],v[240:243],v[116:119]
v_lshl_add_u32 v1,v2,5,v1
v_lshlrev_b32 v2,6,v2
v_mfma_f32_16x16x32_bf16 v[116:119],a[148:151],v[232:235],v[116:119]
buffer_load_dwordx4 v24,s[48:51],s83 offen lds
v_mfma_f32_16x16x32_bf16 v[116:119],a[144:147],v[224:227],v[116:119]
v_exp_f32 v112,v112
v_mfma_f32_16x16x32_bf16 v[120:123],a[160:163],v[224:227],v[44:47]
v_exp_f32 v113,v113
v_mfma_f32_16x16x32_bf16 v[120:123],a[164:167],v[232:235],v[120:123]
v_exp_f32 v114,v114
v_mfma_f32_16x16x32_bf16 v[120:123],a[168:171],v[240:243],v[120:123]
v_exp_f32 v115,v115
v_mfma_f32_16x16x32_bf16 v[120:123],a[172:175],v[248:251],v[120:123]
v_exp_f32 v116,v116
v_mfma_f32_16x16x32_bf16 v[124:127],a[188:191],v[248:251],v[44:47]
buffer_load_dwordx4 v25,s[48:51],s83 offen offset:1024 lds
v_mfma_f32_16x16x32_bf16 v[124:127],a[184:187],v[240:243],v[124:127]
v_exp_f32 v117,v117
v_mfma_f32_16x16x32_bf16 v[124:127],a[180:183],v[232:235],v[124:127]
v_exp_f32 v118,v118
v_mfma_f32_16x16x32_bf16 v[124:127],a[176:179],v[224:227],v[124:127]
v_exp_f32 v119,v119
v_mfma_f32_16x16x32_bf16 v[52:55],v[28:31],v[80:83],v[52:55]
v_cvt_pk_bf16_f32 v112,v112,v113
v_cvt_pk_bf16_f32 v113,v114,v115
v_mfma_f32_16x16x32_bf16 v[52:55],v[28:31],v[88:91],v[52:55]
v_cvt_pk_bf16_f32 v114,v116,v117
v_cvt_pk_bf16_f32 v115,v118,v119
v_mfma_f32_16x16x32_bf16 v[132:135],a[192:195],v[80:83],v[132:135]
buffer_load_dwordx4 v26,s[48:51],s83 offen offset:2048 lds
v_mfma_f32_16x16x32_bf16 v[132:135],a[196:199],v[88:91],v[132:135]
v_exp_f32 v120,v120
v_mfma_f32_16x16x32_bf16 v[140:143],a[200:203],v[80:83],v[140:143]
v_exp_f32 v121,v121
v_mfma_f32_16x16x32_bf16 v[140:143],a[204:207],v[88:91],v[140:143]
v_exp_f32 v122,v122
v_mfma_f32_16x16x32_bf16 v[148:151],a[208:211],v[80:83],v[148:151]
v_exp_f32 v123,v123
v_mfma_f32_16x16x32_bf16 v[148:151],a[212:215],v[88:91],v[148:151]
v_exp_f32 v124,v124
v_mfma_f32_16x16x32_bf16 v[156:159],a[216:219],v[80:83],v[156:159]
buffer_load_dwordx4 v27,s[48:51],s83 offen offset:3072 lds
v_mfma_f32_16x16x32_bf16 v[156:159],a[220:223],v[88:91],v[156:159]
v_exp_f32 v125,v125
v_mfma_f32_16x16x32_bf16 v[164:167],a[224:227],v[80:83],v[164:167]
v_exp_f32 v126,v126
v_mfma_f32_16x16x32_bf16 v[164:167],a[228:231],v[88:91],v[164:167]
s_add_u32 m0,s86,4096
v_exp_f32 v127,v127
v_mfma_f32_16x16x32_bf16 v[172:175],a[232:235],v[80:83],v[172:175]
v_cvt_pk_bf16_f32 v120,v120,v121
v_cvt_pk_bf16_f32 v121,v122,v123
v_mfma_f32_16x16x32_bf16 v[172:175],a[236:239],v[88:91],v[172:175]
v_cvt_pk_bf16_f32 v122,v124,v125
v_cvt_pk_bf16_f32 v123,v126,v127
v_mfma_f32_16x16x32_bf16 v[180:183],a[240:243],v[80:83],v[180:183]
buffer_load_dwordx4 v7,s[48:51],s83 offen lds
v_mfma_f32_16x16x32_bf16 v[180:183],a[244:247],v[88:91],v[180:183]
v_cmp_nlt_f32 s[56:57],v52,s73
v_sub_u32 v2,v1,v2
v_mfma_f32_16x16x32_bf16 v[188:191],a[248:251],v[80:83],v[188:191]
s_or_b64 s[54:55],s[54:55],s[56:57]
v_accvgpr_read_b32 v8,a0
v_accvgpr_read_b32 v9,a1
v_mfma_f32_16x16x32_bf16 v[188:191],a[252:255],v[88:91],v[188:191]
s_add_u32 m0,s86,5120
v_accvgpr_read_b32 v10,a2
v_accvgpr_read_b32 v11,a3
v_mfma_f32_16x16x32_bf16 v[56:59],v[28:31],v[96:99],v[56:59]
v_accvgpr_read_b32 v12,a4
v_accvgpr_read_b32 v13,a5
v_mfma_f32_16x16x32_bf16 v[56:59],v[28:31],v[104:107],v[56:59]
v_accvgpr_read_b32 v14,a6
v_accvgpr_read_b32 v15,a7
v_mfma_f32_16x16x32_bf16 v[196:199],a[192:195],v[96:99],v[196:199]
buffer_load_dwordx4 v24,s[48:51],s84 offen lds
v_mfma_f32_16x16x32_bf16 v[196:199],a[196:199],v[104:107],v[196:199]
v_mul_f32 v8,v8,v49
v_mul_f32 v9,v9,v49
v_mfma_f32_16x16x32_bf16 v[204:207],a[200:203],v[96:99],v[204:207]
v_cmp_nlt_f32 s[56:57],v56,s73
v_mul_f32 v10,v10,v49
v_mfma_f32_16x16x32_bf16 v[204:207],a[204:207],v[104:107],v[204:207]
s_or_b64 s[54:55],s[54:55],s[56:57]
v_mul_f32 v11,v11,v49
v_mul_f32 v12,v12,v49
v_mfma_f32_16x16x32_bf16 v[212:215],a[208:211],v[96:99],v[212:215]
v_mul_f32 v13,v13,v49
v_mul_f32 v14,v14,v49
v_mfma_f32_16x16x32_bf16 v[212:215],a[212:215],v[104:107],v[212:215]
v_mul_f32 v15,v15,v49
v_cvt_pk_bf16_f32 v32,v8,v9
v_mfma_f32_16x16x32_bf16 v[220:223],a[216:219],v[96:99],v[220:223]
buffer_load_dwordx4 v25,s[48:51],s84 offen offset:1024 lds
v_mfma_f32_16x16x32_bf16 v[220:223],a[220:223],v[104:107],v[220:223]
v_cvt_pk_bf16_f32 v33,v10,v11
v_cvt_pk_bf16_f32 v34,v12,v13
v_mfma_f32_16x16x32_bf16 v[228:231],a[224:227],v[96:99],v[228:231]
v_cvt_pk_bf16_f32 v35,v14,v15
v_accvgpr_read_b32 v16,a8
v_mfma_f32_16x16x32_bf16 v[228:231],a[228:231],v[104:107],v[228:231]
v_accvgpr_read_b32 v17,a9
v_accvgpr_read_b32 v18,a10
v_mfma_f32_16x16x32_bf16 v[236:239],a[232:235],v[96:99],v[236:239]
v_accvgpr_read_b32 v19,a11
v_accvgpr_read_b32 v20,a12
v_mfma_f32_16x16x32_bf16 v[236:239],a[236:239],v[104:107],v[236:239]
v_accvgpr_read_b32 v21,a13
v_accvgpr_read_b32 v22,a14
v_mfma_f32_16x16x32_bf16 v[244:247],a[240:243],v[96:99],v[244:247]
buffer_load_dwordx4 v26,s[48:51],s84 offen offset:2048 lds
v_mfma_f32_16x16x32_bf16 v[244:247],a[244:247],v[104:107],v[244:247]
v_accvgpr_read_b32 v23,a15
v_mul_f32 v16,v16,v49
v_mfma_f32_16x16x32_bf16 v[252:255],a[248:251],v[96:99],v[252:255]
v_mul_f32 v17,v17,v49
v_mul_f32 v18,v18,v49
v_mfma_f32_16x16x32_bf16 v[252:255],a[252:255],v[104:107],v[252:255]
v_mul_f32 v19,v19,v49
v_mul_f32 v20,v20,v49
v_mfma_f32_16x16x32_bf16 v[60:63],v[28:31],v[112:115],v[60:63]
v_mul_f32 v21,v21,v49
v_mul_f32 v22,v22,v49
v_mfma_f32_16x16x32_bf16 v[60:63],v[28:31],v[120:123],v[60:63]
v_mul_f32 v23,v23,v49
v_cvt_pk_bf16_f32 v36,v16,v17
v_mfma_f32_16x16x32_bf16 a[32:35],a[192:195],v[112:115],a[32:35]
buffer_load_dwordx4 v27,s[48:51],s84 offen offset:3072 lds
v_mfma_f32_16x16x32_bf16 a[32:35],a[196:199],v[120:123],a[32:35]
v_cvt_pk_bf16_f32 v37,v18,v19
v_cvt_pk_bf16_f32 v38,v20,v21
v_mfma_f32_16x16x32_bf16 a[36:39],a[200:203],v[112:115],a[36:39]
v_cmp_nlt_f32 s[56:57],v60,s73
v_cvt_pk_bf16_f32 v39,v22,v23
v_mfma_f32_16x16x32_bf16 a[36:39],a[204:207],v[120:123],a[36:39]
s_add_u32 m0,s86,9216
v_accvgpr_read_b32 v8,a16
v_accvgpr_read_b32 v9,a17
v_mfma_f32_16x16x32_bf16 a[40:43],a[208:211],v[112:115],a[40:43]
s_or_b64 s[54:55],s[54:55],s[56:57]
v_accvgpr_read_b32 v10,a18
v_accvgpr_read_b32 v11,a19
v_mfma_f32_16x16x32_bf16 a[40:43],a[212:215],v[120:123],a[40:43]
v_accvgpr_read_b32 v12,a20
v_accvgpr_read_b32 v13,a21
v_mfma_f32_16x16x32_bf16 a[44:47],a[216:219],v[112:115],a[44:47]
buffer_load_dwordx4 v7,s[48:51],s84 offen lds
v_mfma_f32_16x16x32_bf16 a[44:47],a[220:223],v[120:123],a[44:47]
v_accvgpr_read_b32 v14,a22
v_accvgpr_read_b32 v15,a23
v_mfma_f32_16x16x32_bf16 a[48:51],a[224:227],v[112:115],a[48:51]
v_mul_f32 v8,v8,v49
v_mul_f32 v9,v9,v49
v_mfma_f32_16x16x32_bf16 a[48:51],a[228:231],v[120:123],a[48:51]
v_mul_f32 v10,v10,v49
v_mul_f32 v11,v11,v49
v_mfma_f32_16x16x32_bf16 a[52:55],a[232:235],v[112:115],a[52:55]
v_mul_f32 v12,v12,v49
v_mul_f32 v13,v13,v49
v_mfma_f32_16x16x32_bf16 a[52:55],a[236:239],v[120:123],a[52:55]
v_mul_f32 v14,v14,v49
v_mul_f32 v15,v15,v49
v_mfma_f32_16x16x32_bf16 a[56:59],a[240:243],v[112:115],a[56:59]
v_cvt_pk_bf16_f32 v40,v8,v9
v_cvt_pk_bf16_f32 v41,v10,v11
v_mfma_f32_16x16x32_bf16 a[56:59],a[244:247],v[120:123],a[56:59]
v_cvt_pk_bf16_f32 v42,v12,v13
v_cvt_pk_bf16_f32 v43,v14,v15
v_mfma_f32_16x16x32_bf16 a[60:63],a[248:251],v[112:115],a[60:63]
ds_read_b64_tr_b16 v[64:65],v1
ds_read_b64_tr_b16 v[66:67],v1 offset:5120
v_mfma_f32_16x16x32_bf16 a[60:63],a[252:255],v[120:123],a[60:63]
ds_read_b64_tr_b16 v[68:69],v1 offset:10240
v_max_f32 v53,0x00800000,v52
v_rcp_f32 v53,v53
v_max_f32 v57,0x00800000,v56
v_rcp_f32 v57,v57
v_max_f32 v61,0x00800000,v60
v_rcp_f32 v61,v61
ds_read_b64_tr_b16 v[70:71],v1 offset:15360
ds_read_b64_tr_b16 v[72:73],v1 offset:20480
ds_read_b64_tr_b16 v[74:75],v1 offset:25600
ds_read_b64_tr_b16 v[76:77],v1 offset:30720
ds_read_b64_tr_b16 v[78:79],v1 offset:35840
ds_read_b64_tr_b16 v[80:81],v2 offset:32
ds_read_b64_tr_b16 v[82:83],v2 offset:5152
ds_read_b64_tr_b16 v[84:85],v2 offset:10272
ds_read_b64_tr_b16 v[86:87],v2 offset:15392
ds_read_b64_tr_b16 v[88:89],v2 offset:20512
ds_read_b64_tr_b16 v[90:91],v2 offset:25632
ds_read_b64_tr_b16 v[92:93],v2 offset:30752
ds_read_b64_tr_b16 v[94:95],v2 offset:35872
s_cmp_lg_u64 s[54:55],0
s_cbranch_scc1 L_slow1_%=
L_ok1_%=:
s_cmp_ge_u32 s63,2
s_cbranch_scc1 L_GS_%=
L_gbF_%=:
s_waitcnt lgkmcnt(14)
v_mfma_f32_16x16x32_bf16 a[192:195],v[64:67],a[68:71],0
buffer_store_dwordx4 v[32:35],v6,s[40:43],s79 offen sc1
s_waitcnt lgkmcnt(12)
v_mfma_f32_16x16x32_bf16 a[192:195],v[68:71],a[76:79],a[192:195]
s_lshl_b32 s91,s63,8
v_bfe_u32 v3,v6,4,2
v_min_u32 v3,2,v3
s_waitcnt lgkmcnt(10)
v_mfma_f32_16x16x32_bf16 a[192:195],v[72:75],a[84:87],a[192:195]
s_add_u32 s87,s64,1
v_add3_u32 v3,v3,s64,1
v_and_b32 v3,3,v3
s_waitcnt lgkmcnt(8)
v_mfma_f32_16x16x32_bf16 a[192:195],v[76:79],a[92:95],a[192:195]
s_and_b32 s87,s87,3
ds_read_b64_tr_b16 v[96:97],v1 offset:64
v_lshlrev_b32 v3,6,v3
v_mfma_f32_16x16x32_bf16 a[196:199],v[76:79],a[124:127],0
s_lshl_b32 s87,s87,1
ds_read_b64_tr_b16 v[98:99],v1 offset:5184
ds_read_b64_tr_b16 v[100:101],v1 offset:10304
v_mfma_f32_16x16x32_bf16 a[196:199],v[72:75],a[116:119],a[196:199]
s_lshl_b32 s87,s87,16
ds_read_b64_tr_b16 v[102:103],v1 offset:15424
v_lshlrev_b32 v29,10,v6
v_mfma_f32_16x16x32_bf16 a[196:199],v[68:71],a[108:111],a[196:199]
s_add_u32 s88,s87,4096
ds_read_b64_tr_b16 v[104:105],v1 offset:20544
v_accvgpr_read_b32 v8,a24
v_mfma_f32_16x16x32_bf16 a[196:199],v[64:67],a[100:103],a[196:199]
s_add_u32 s89,s87,8192
ds_read_b64_tr_b16 v[106:107],v1 offset:25664
v_accvgpr_read_b32 v9,a25
v_mfma_f32_16x16x32_bf16 a[200:203],v[64:67],a[64:67],0
s_add_u32 s90,s87,12288
ds_read_b64_tr_b16 v[108:109],v1 offset:30784
ds_read_b64_tr_b16 v[110:111],v1 offset:35904
v_mfma_f32_16x16x32_bf16 a[200:203],v[68:71],a[72:75],a[200:203]
s_add_u32 m0,s86,122880
ds_read_b64_tr_b16 v[112:113],v2 offset:96
)A"
R"A(v_accvgpr_read_b32 v10,a26
v_mfma_f32_16x16x32_bf16 a[200:203],v[72:75],a[80:83],a[200:203]
s_add_u32 s83,s64,3
ds_read_b64_tr_b16 v[114:115],v2 offset:5216
v_accvgpr_read_b32 v11,a27
v_mfma_f32_16x16x32_bf16 a[200:203],v[76:79],a[88:91],a[200:203]
s_and_b32 s83,s83,3
ds_read_b64_tr_b16 v[116:117],v2 offset:10336
v_accvgpr_read_b32 v12,a28
v_mfma_f32_16x16x32_bf16 a[204:207],v[76:79],a[120:123],0
s_lshl_b32 s83,s83,1
ds_read_b64_tr_b16 v[118:119],v2 offset:15456
ds_read_b64_tr_b16 v[120:121],v2 offset:20576
v_mfma_f32_16x16x32_bf16 a[204:207],v[72:75],a[112:115],a[204:207]
s_mul_i32 s83,s83,163840
ds_read_b64_tr_b16 v[122:123],v2 offset:25696
v_accvgpr_read_b32 v13,a29
v_mfma_f32_16x16x32_bf16 a[204:207],v[68:71],a[104:107],a[204:207]
s_add_u32 s83,s83,s85
ds_read_b64_tr_b16 v[124:125],v2 offset:30816
v_accvgpr_read_b32 v14,a30
v_mfma_f32_16x16x32_bf16 a[204:207],v[64:67],a[96:99],a[204:207]
buffer_store_dwordx4 v[36:39],v6,s[40:43],s80 offen sc1
s_waitcnt lgkmcnt(9)
v_mfma_f32_16x16x32_bf16 a[208:211],v[80:83],a[68:71],0
s_add_u32 s84,s83,20480
ds_read_b64_tr_b16 v[126:127],v2 offset:35936
ds_read_b64_tr_b16 v[64:65],v1 offset:128
v_mfma_f32_16x16x32_bf16 a[208:211],v[84:87],a[76:79],a[208:211]
s_lshl_b32 s66,s61,3
ds_read_b64_tr_b16 v[66:67],v1 offset:5248
v_accvgpr_read_b32 v15,a31
v_mfma_f32_16x16x32_bf16 a[208:211],v[88:91],a[84:87],a[208:211]
s_add_u32 s66,s66,s64
ds_read_b64_tr_b16 v[68:69],v1 offset:10368
v_mul_f32 v8,v8,v49
v_mfma_f32_16x16x32_bf16 a[208:211],v[92:95],a[92:95],a[208:211]
s_mul_i32 s66,s66,262144
ds_read_b64_tr_b16 v[70:71],v1 offset:15488
v_mul_f32 v9,v9,v49
v_mfma_f32_16x16x32_bf16 a[212:215],v[92:95],a[124:127],0
s_waitcnt vmcnt(21)
buffer_load_dwordx2 v[4:5],v3,s[44:47],s91 offen offset:256 sc1
v_mfma_f32_16x16x32_bf16 a[212:215],v[88:91],a[116:119],a[212:215]
s_add_u32 s66,s66,100663296
ds_read_b64_tr_b16 v[72:73],v1 offset:20608
ds_read_b64_tr_b16 v[74:75],v1 offset:25728
L_pd13_%=:
s_cbranch_execz L_trt1_%=
v_mfma_f32_16x16x32_bf16 a[212:215],v[84:87],a[108:111],a[212:215]
s_mov_b32 s38,262144
ds_read_b64_tr_b16 v[76:77],v1 offset:30848
v_mul_f32 v10,v10,v49
v_mfma_f32_16x16x32_bf16 a[212:215],v[80:83],a[100:103],a[212:215]
s_mov_b32 s39,0x20000
ds_read_b64_tr_b16 v[78:79],v1 offset:35968
v_mul_f32 v11,v11,v49
v_mfma_f32_16x16x32_bf16 a[216:219],v[80:83],a[64:67],0
s_add_u32 s74,s63,1
v_mul_f32 v12,v12,v49
v_mul_f32 v13,v13,v49
v_mfma_f32_16x16x32_bf16 a[216:219],v[84:87],a[72:75],a[216:219]
s_and_b32 s74,s74,3
v_mul_f32 v14,v14,v49
v_mul_f32 v15,v15,v49
v_mfma_f32_16x16x32_bf16 a[216:219],v[88:91],a[80:83],a[216:219]
s_lshl_b32 s74,s74,2
v_cvt_pk_bf16_f32 v44,v8,v9
v_cvt_pk_bf16_f32 v45,v10,v11
v_mfma_f32_16x16x32_bf16 a[216:219],v[92:95],a[88:91],a[216:219]
s_add_u32 s74,s74,s60
v_cvt_pk_bf16_f32 v46,v12,v13
v_cvt_pk_bf16_f32 v47,v14,v15
v_mfma_f32_16x16x32_bf16 a[220:223],v[92:95],a[120:123],0
s_mul_i32 s74,s74,16384
ds_read_b64_tr_b16 v[92:93],v2 offset:30880
ds_read_b64_tr_b16 v[94:95],v2 offset:36000
v_mfma_f32_16x16x32_bf16 a[220:223],v[88:91],a[112:115],a[220:223]
s_add_u32 s74,s74,8192
ds_read_b64_tr_b16 v[88:89],v2 offset:20640
v_mul_f32 v132,v132,v53
v_mfma_f32_16x16x32_bf16 a[220:223],v[84:87],a[104:107],a[220:223]
buffer_store_dwordx4 v[40:43],v6,s[40:43],s81 offen sc1
v_mfma_f32_16x16x32_bf16 a[220:223],v[80:83],a[96:99],a[220:223]
s_add_u32 s75,s63,2
ds_read_b64_tr_b16 v[80:81],v2 offset:160
ds_read_b64_tr_b16 v[82:83],v2 offset:5280
v_mfma_f32_16x16x32_bf16 a[224:227],v[96:99],a[68:71],0
s_and_b32 s75,s75,3
ds_read_b64_tr_b16 v[84:85],v2 offset:10400
v_mul_f32 v133,v133,v53
v_mfma_f32_16x16x32_bf16 a[224:227],v[100:103],a[76:79],a[224:227]
s_lshl_b32 s75,s75,2
ds_read_b64_tr_b16 v[86:87],v2 offset:15520
v_mul_f32 v134,v134,v53
v_mfma_f32_16x16x32_bf16 a[224:227],v[104:107],a[84:87],a[224:227]
s_add_u32 s75,s75,s60
ds_read_b64_tr_b16 v[90:91],v2 offset:25760
v_mul_f32 v135,v135,v53
s_waitcnt lgkmcnt(8)
v_mfma_f32_16x16x32_bf16 a[224:227],v[108:111],a[92:95],a[224:227]
s_mul_i32 s75,s75,16384
v_mul_f32 v140,v140,v53
v_mul_f32 v141,v141,v53
v_mfma_f32_16x16x32_bf16 a[228:231],v[108:111],a[124:127],0
s_add_u32 s75,s75,4096
v_mul_f32 v142,v142,v53
v_mul_f32 v143,v143,v53
v_mfma_f32_16x16x32_bf16 a[228:231],v[104:107],a[116:119],a[228:231]
s_add_u32 s76,s63,3
v_cvt_pk_bf16_f32 v132,v132,v133
v_cvt_pk_bf16_f32 v133,v134,v135
v_mfma_f32_16x16x32_bf16 a[228:231],v[100:103],a[108:111],a[228:231]
s_and_b32 s76,s76,3
v_cvt_pk_bf16_f32 v134,v140,v141
v_cvt_pk_bf16_f32 v135,v142,v143
v_mfma_f32_16x16x32_bf16 a[228:231],v[96:99],a[100:103],a[228:231]
s_lshl_b32 s76,s76,2
v_mul_f32 v148,v148,v53
v_mul_f32 v149,v149,v53
v_mfma_f32_16x16x32_bf16 a[232:235],v[96:99],a[64:67],0
s_add_u32 s76,s76,s60
v_mul_f32 v150,v150,v53
v_mul_f32 v151,v151,v53
v_mfma_f32_16x16x32_bf16 a[232:235],v[100:103],a[72:75],a[232:235]
s_mul_i32 s76,s76,16384
v_mul_f32 v156,v156,v53
v_mul_f32 v157,v157,v53
v_mfma_f32_16x16x32_bf16 a[232:235],v[104:107],a[80:83],a[232:235]
s_add_u32 s76,s76,0
v_mul_f32 v158,v158,v53
v_mul_f32 v159,v159,v53
v_mfma_f32_16x16x32_bf16 a[232:235],v[108:111],a[88:91],a[232:235]
s_lshl_b32 s67,s63,10
v_cvt_pk_bf16_f32 v148,v148,v149
v_cvt_pk_bf16_f32 v149,v150,v151
v_mfma_f32_16x16x32_bf16 a[236:239],v[108:111],a[120:123],0
s_mov_b32 s26,307200
ds_read_b64_tr_b16 v[108:109],v1 offset:30912
ds_read_b64_tr_b16 v[110:111],v1 offset:36032
v_mfma_f32_16x16x32_bf16 a[236:239],v[104:107],a[112:115],a[236:239]
buffer_store_dwordx4 v[44:47],v6,s[40:43],s82 offen sc1
v_mfma_f32_16x16x32_bf16 a[236:239],v[100:103],a[104:107],a[236:239]
s_mov_b32 s27,0x20000
ds_read_b64_tr_b16 v[100:101],v1 offset:10432
ds_read_b64_tr_b16 v[102:103],v1 offset:15552
v_mfma_f32_16x16x32_bf16 a[236:239],v[96:99],a[96:99],a[236:239]
s_lshl_b32 s77,s63,2
ds_read_b64_tr_b16 v[96:97],v1 offset:192
v_cvt_pk_bf16_f32 v150,v156,v157
v_mfma_f32_16x16x32_bf16 a[240:243],v[112:115],a[68:71],0
s_add_u32 s77,s77,s60
ds_read_b64_tr_b16 v[98:99],v1 offset:5312
v_cvt_pk_bf16_f32 v151,v158,v159
v_mfma_f32_16x16x32_bf16 a[240:243],v[116:119],a[76:79],a[240:243]
s_mul_i32 s77,s77,16384
ds_read_b64_tr_b16 v[104:105],v1 offset:20672
v_mul_f32 v164,v164,v53
v_mfma_f32_16x16x32_bf16 a[240:243],v[120:123],a[84:87],a[240:243]
s_add_u32 s65,s77,0
ds_read_b64_tr_b16 v[106:107],v1 offset:25792
v_mul_f32 v165,v165,v53
v_mfma_f32_16x16x32_bf16 a[240:243],v[124:127],a[92:95],a[240:243]
s_add_u32 s68,s77,12288
v_mul_f32 v166,v166,v53
v_mul_f32 v167,v167,v53
v_mfma_f32_16x16x32_bf16 a[244:247],v[124:127],a[124:127],0
v_mul_f32 v172,v172,v53
v_mul_f32 v173,v173,v53
v_mfma_f32_16x16x32_bf16 a[244:247],v[120:123],a[116:119],a[244:247]
v_mul_f32 v174,v174,v53
v_mul_f32 v175,v175,v53
v_mfma_f32_16x16x32_bf16 a[244:247],v[116:119],a[108:111],a[244:247]
v_cvt_pk_bf16_f32 v164,v164,v165
v_cvt_pk_bf16_f32 v165,v166,v167
v_mfma_f32_16x16x32_bf16 a[244:247],v[112:115],a[100:103],a[244:247]
v_cvt_pk_bf16_f32 v166,v172,v173
v_cvt_pk_bf16_f32 v167,v174,v175
v_mfma_f32_16x16x32_bf16 a[248:251],v[112:115],a[64:67],0
v_mul_f32 v180,v180,v53
v_mul_f32 v181,v181,v53
v_mfma_f32_16x16x32_bf16 a[248:251],v[116:119],a[72:75],a[248:251]
v_mul_f32 v182,v182,v53
v_mul_f32 v183,v183,v53
v_mfma_f32_16x16x32_bf16 a[248:251],v[120:123],a[80:83],a[248:251]
v_mul_f32 v188,v188,v53
v_mul_f32 v189,v189,v53
v_mfma_f32_16x16x32_bf16 a[248:251],v[124:127],a[88:91],a[248:251]
v_mul_f32 v190,v190,v53
v_mul_f32 v191,v191,v53
v_mfma_f32_16x16x32_bf16 a[252:255],v[124:127],a[120:123],0
buffer_load_dword v30,v29,s[8:11],s7 offen
v_mfma_f32_16x16x32_bf16 a[252:255],v[120:123],a[112:115],a[252:255]
ds_read_b64_tr_b16 v[120:121],v2 offset:20704
ds_read_b64_tr_b16 v[122:123],v2 offset:25824
v_mfma_f32_16x16x32_bf16 a[252:255],v[116:119],a[104:107],a[252:255]
buffer_store_dwordx4 v[132:135],v6,s[40:43],s79 offen offset:1024 sc1
v_mfma_f32_16x16x32_bf16 a[252:255],v[112:115],a[96:99],a[252:255]
ds_read_b64_tr_b16 v[112:113],v2 offset:224
ds_read_b64_tr_b16 v[114:115],v2 offset:5344
v_mfma_f32_16x16x32_bf16 a[0:3],v[64:67],a[68:71],0
ds_read_b64_tr_b16 v[116:117],v2 offset:10464
v_cvt_pk_bf16_f32 v180,v180,v181
v_mfma_f32_16x16x32_bf16 a[0:3],v[68:71],a[76:79],a[0:3]
ds_read_b64_tr_b16 v[118:119],v2 offset:15584
v_cvt_pk_bf16_f32 v181,v182,v183
v_mfma_f32_16x16x32_bf16 a[0:3],v[72:75],a[84:87],a[0:3]
ds_read_b64_tr_b16 v[124:125],v2 offset:30944
v_cvt_pk_bf16_f32 v182,v188,v189
v_mfma_f32_16x16x32_bf16 a[0:3],v[76:79],a[92:95],a[0:3]
ds_read_b64_tr_b16 v[126:127],v2 offset:36064
v_cvt_pk_bf16_f32 v183,v190,v191
v_mfma_f32_16x16x32_bf16 a[4:7],v[76:79],a[124:127],0
v_mul_f32 v196,v196,v57
v_mul_f32 v197,v197,v57
v_mfma_f32_16x16x32_bf16 a[4:7],v[72:75],a[116:119],a[4:7]
v_mul_f32 v198,v198,v57
v_mul_f32 v199,v199,v57
v_mfma_f32_16x16x32_bf16 a[4:7],v[68:71],a[108:111],a[4:7]
v_mul_f32 v204,v204,v57
v_mul_f32 v205,v205,v57
v_mfma_f32_16x16x32_bf16 a[4:7],v[64:67],a[100:103],a[4:7]
v_mul_f32 v206,v206,v57
v_mul_f32 v207,v207,v57
v_mfma_f32_16x16x32_bf16 a[8:11],v[64:67],a[64:67],0
v_cvt_pk_bf16_f32 v196,v196,v197
v_cvt_pk_bf16_f32 v197,v198,v199
v_mfma_f32_16x16x32_bf16 a[8:11],v[68:71],a[72:75],a[8:11]
v_cvt_pk_bf16_f32 v198,v204,v205
v_cvt_pk_bf16_f32 v199,v206,v207
v_mfma_f32_16x16x32_bf16 a[8:11],v[72:75],a[80:83],a[8:11]
v_mul_f32 v212,v212,v57
v_mul_f32 v213,v213,v57
v_mfma_f32_16x16x32_bf16 a[8:11],v[76:79],a[88:91],a[8:11]
v_mul_f32 v214,v214,v57
v_mul_f32 v215,v215,v57
v_mfma_f32_16x16x32_bf16 a[12:15],v[76:79],a[120:123],0
ds_read_b64_tr_b16 v[76:77],v1 offset:30976
ds_read_b64_tr_b16 v[78:79],v1 offset:36096
v_mfma_f32_16x16x32_bf16 a[12:15],v[72:75],a[112:115],a[12:15]
buffer_store_dwordx4 v[148:151],v6,s[40:43],s80 offen offset:1024 sc1
v_mfma_f32_16x16x32_bf16 a[12:15],v[68:71],a[104:107],a[12:15]
ds_read_b64_tr_b16 v[68:69],v1 offset:10496
ds_read_b64_tr_b16 v[70:71],v1 offset:15616
v_mfma_f32_16x16x32_bf16 a[12:15],v[64:67],a[96:99],a[12:15]
s_waitcnt lgkmcnt(4)
ds_read_b64_tr_b16 v[64:65],v1 offset:256
v_mul_f32 v220,v220,v57
v_mfma_f32_16x16x32_bf16 a[16:19],v[80:83],a[68:71],0
ds_read_b64_tr_b16 v[66:67],v1 offset:5376
v_mul_f32 v221,v221,v57
v_mfma_f32_16x16x32_bf16 a[16:19],v[84:87],a[76:79],a[16:19]
ds_read_b64_tr_b16 v[72:73],v1 offset:20736
v_mul_f32 v222,v222,v57
v_mfma_f32_16x16x32_bf16 a[16:19],v[88:91],a[84:87],a[16:19]
ds_read_b64_tr_b16 v[74:75],v1 offset:25856
v_add_u32 v1,0xffff6000,v1
v_mfma_f32_16x16x32_bf16 a[16:19],v[92:95],a[92:95],a[16:19]
v_mul_f32 v223,v223,v57
v_cvt_pk_bf16_f32 v212,v212,v213
v_mfma_f32_16x16x32_bf16 a[20:23],v[92:95],a[124:127],0
v_cvt_pk_bf16_f32 v213,v214,v215
v_cvt_pk_bf16_f32 v214,v220,v221
v_mfma_f32_16x16x32_bf16 a[20:23],v[88:91],a[116:119],a[20:23]
v_cvt_pk_bf16_f32 v215,v222,v223
v_mul_f32 v228,v228,v57
v_mfma_f32_16x16x32_bf16 a[20:23],v[84:87],a[108:111],a[20:23]
v_mul_f32 v229,v229,v57
v_mul_f32 v230,v230,v57
v_mfma_f32_16x16x32_bf16 a[20:23],v[80:83],a[100:103],a[20:23]
v_mul_f32 v231,v231,v57
v_mul_f32 v236,v236,v57
v_mfma_f32_16x16x32_bf16 a[24:27],v[80:83],a[64:67],0
v_mul_f32 v237,v237,v57
v_mul_f32 v238,v238,v57
v_mfma_f32_16x16x32_bf16 a[24:27],v[84:87],a[72:75],a[24:27]
v_mul_f32 v239,v239,v57
v_cvt_pk_bf16_f32 v228,v228,v229
v_mfma_f32_16x16x32_bf16 a[24:27],v[88:91],a[80:83],a[24:27]
s_waitcnt vmcnt(5)
v_cmp_ne_u32 s[56:57],s52,v4
v_cmp_ne_u32 s[58:59],s53,v5
v_mfma_f32_16x16x32_bf16 a[24:27],v[92:95],a[88:91],a[24:27]
s_or_b64 s[56:57],s[56:57],s[58:59]
s_cbranch_scc1 L_fr1_%=
L_fo1_%=:
v_cvt_pk_bf16_f32 v229,v230,v231
v_mfma_f32_16x16x32_bf16 a[28:31],v[92:95],a[120:123],0
buffer_store_dwordx4 v[164:167],v6,s[40:43],s81 offen offset:1024 sc1
v_mfma_f32_16x16x32_bf16 a[28:31],v[88:91],a[112:115],a[28:31]
s_lshl_b32 s91,s63,8
ds_read_b64_tr_b16 v[88:89],v2 offset:20768
ds_read_b64_tr_b16 v[90:91],v2 offset:25888
v_mfma_f32_16x16x32_bf16 a[28:31],v[84:87],a[104:107],a[28:31]
buffer_load_dwordx4 a[128:131],v6,s[28:31],s87 offen sc1
v_mfma_f32_16x16x32_bf16 a[28:31],v[80:83],a[96:99],a[28:31]
ds_read_b64_tr_b16 v[80:81],v2 offset:288
ds_read_b64_tr_b16 v[82:83],v2 offset:5408
v_mfma_f32_16x16x32_bf16 v[128:131],v[96:99],a[68:71],0
)A"
R"A(ds_read_b64_tr_b16 v[84:85],v2 offset:10528
v_cvt_pk_bf16_f32 v230,v236,v237
v_mfma_f32_16x16x32_bf16 v[128:131],v[100:103],a[76:79],v[128:131]
ds_read_b64_tr_b16 v[86:87],v2 offset:15648
v_cvt_pk_bf16_f32 v231,v238,v239
v_mfma_f32_16x16x32_bf16 v[128:131],v[104:107],a[84:87],v[128:131]
ds_read_b64_tr_b16 v[92:93],v2 offset:31008
v_mul_f32 v244,v244,v57
v_mfma_f32_16x16x32_bf16 v[128:131],v[108:111],a[92:95],v[128:131]
ds_read_b64_tr_b16 v[94:95],v2 offset:36128
v_add_u32 v2,0xffff6000,v2
v_mfma_f32_16x16x32_bf16 v[136:139],v[108:111],a[124:127],0
v_mul_f32 v245,v245,v57
v_mul_f32 v246,v246,v57
v_mfma_f32_16x16x32_bf16 v[136:139],v[104:107],a[116:119],v[136:139]
v_mul_f32 v247,v247,v57
v_mul_f32 v252,v252,v57
v_mfma_f32_16x16x32_bf16 v[136:139],v[100:103],a[108:111],v[136:139]
v_mul_f32 v253,v253,v57
v_mul_f32 v254,v254,v57
v_mfma_f32_16x16x32_bf16 v[136:139],v[96:99],a[100:103],v[136:139]
v_mul_f32 v255,v255,v57
v_cvt_pk_bf16_f32 v244,v244,v245
v_mfma_f32_16x16x32_bf16 v[144:147],v[96:99],a[64:67],0
v_cvt_pk_bf16_f32 v245,v246,v247
v_cvt_pk_bf16_f32 v246,v252,v253
v_mfma_f32_16x16x32_bf16 v[144:147],v[100:103],a[72:75],v[144:147]
buffer_load_dwordx4 a[144:147],v6,s[28:31],s88 offen sc1
v_mfma_f32_16x16x32_bf16 v[144:147],v[104:107],a[80:83],v[144:147]
v_cvt_pk_bf16_f32 v247,v254,v255
v_accvgpr_read_b32 v8,a40
v_mfma_f32_16x16x32_bf16 v[144:147],v[108:111],a[88:91],v[144:147]
buffer_store_dwordx4 v[180:183],v6,s[40:43],s82 offen offset:1024 sc1
v_mfma_f32_16x16x32_bf16 v[152:155],v[108:111],a[120:123],0
ds_read_b64_tr_b16 v[108:109],v1 offset:30720
ds_read_b64_tr_b16 v[110:111],v1 offset:35840
v_mfma_f32_16x16x32_bf16 v[152:155],v[104:107],a[112:115],v[152:155]
ds_read_b64_tr_b16 v[104:105],v1 offset:20480
v_accvgpr_read_b32 v9,a41
v_mfma_f32_16x16x32_bf16 v[152:155],v[100:103],a[104:107],v[152:155]
ds_read_b64_tr_b16 v[100:101],v1 offset:10240
v_accvgpr_read_b32 v10,a42
v_mfma_f32_16x16x32_bf16 v[152:155],v[96:99],a[96:99],v[152:155]
ds_read_b64_tr_b16 v[96:97],v1
v_accvgpr_read_b32 v11,a43
v_mfma_f32_16x16x32_bf16 v[160:163],v[112:115],a[68:71],0
ds_read_b64_tr_b16 v[98:99],v1 offset:5120
ds_read_b64_tr_b16 v[102:103],v1 offset:15360
v_mfma_f32_16x16x32_bf16 v[160:163],v[116:119],a[76:79],v[160:163]
ds_read_b64_tr_b16 v[106:107],v1 offset:25600
v_accvgpr_read_b32 v12,a44
v_mfma_f32_16x16x32_bf16 v[160:163],v[120:123],a[84:87],v[160:163]
v_accvgpr_read_b32 v13,a45
v_accvgpr_read_b32 v14,a46
v_mfma_f32_16x16x32_bf16 v[160:163],v[124:127],a[92:95],v[160:163]
v_accvgpr_read_b32 v15,a47
v_mul_f32 v8,v8,v61
v_mfma_f32_16x16x32_bf16 v[168:171],v[124:127],a[124:127],0
v_mul_f32 v9,v9,v61
v_mul_f32 v10,v10,v61
v_mfma_f32_16x16x32_bf16 v[168:171],v[120:123],a[116:119],v[168:171]
v_mul_f32 v11,v11,v61
v_mul_f32 v12,v12,v61
v_mfma_f32_16x16x32_bf16 v[168:171],v[116:119],a[108:111],v[168:171]
buffer_load_dwordx4 a[160:163],v6,s[28:31],s89 offen sc1
v_mfma_f32_16x16x32_bf16 v[168:171],v[112:115],a[100:103],v[168:171]
v_mul_f32 v13,v13,v61
v_mul_f32 v14,v14,v61
v_mfma_f32_16x16x32_bf16 v[176:179],v[112:115],a[64:67],0
v_mul_f32 v15,v15,v61
v_cvt_pk_bf16_f32 v156,v8,v9
v_mfma_f32_16x16x32_bf16 v[176:179],v[116:119],a[72:75],v[176:179]
v_cvt_pk_bf16_f32 v157,v10,v11
v_cvt_pk_bf16_f32 v158,v12,v13
v_mfma_f32_16x16x32_bf16 v[176:179],v[120:123],a[80:83],v[176:179]
buffer_store_dwordx4 v[196:199],v6,s[40:43],s79 offen offset:2048 sc1
v_mfma_f32_16x16x32_bf16 v[176:179],v[124:127],a[88:91],v[176:179]
v_cvt_pk_bf16_f32 v159,v14,v15
v_accvgpr_read_b32 v8,a56
v_mfma_f32_16x16x32_bf16 v[184:187],v[124:127],a[120:123],0
v_accvgpr_read_b32 v9,a57
v_accvgpr_read_b32 v10,a58
v_mfma_f32_16x16x32_bf16 v[184:187],v[120:123],a[112:115],v[184:187]
ds_read_b64_tr_b16 v[120:121],v2 offset:20512
ds_read_b64_tr_b16 v[122:123],v2 offset:25632
v_mfma_f32_16x16x32_bf16 v[184:187],v[116:119],a[104:107],v[184:187]
ds_read_b64_tr_b16 v[116:117],v2 offset:10272
v_accvgpr_read_b32 v11,a59
v_mfma_f32_16x16x32_bf16 v[184:187],v[112:115],a[96:99],v[184:187]
s_waitcnt lgkmcnt(3)
ds_read_b64_tr_b16 v[112:113],v2 offset:32
v_accvgpr_read_b32 v12,a60
v_mfma_f32_16x16x32_bf16 v[192:195],v[64:67],a[68:71],0
ds_read_b64_tr_b16 v[114:115],v2 offset:5152
v_accvgpr_read_b32 v13,a61
v_mfma_f32_16x16x32_bf16 v[192:195],v[68:71],a[76:79],v[192:195]
ds_read_b64_tr_b16 v[118:119],v2 offset:15392
v_accvgpr_read_b32 v14,a62
v_mfma_f32_16x16x32_bf16 v[192:195],v[72:75],a[84:87],v[192:195]
v_accvgpr_read_b32 v15,a63
v_mul_f32 v8,v8,v61
v_mfma_f32_16x16x32_bf16 v[192:195],v[76:79],a[92:95],v[192:195]
buffer_load_dwordx4 a[176:179],v6,s[28:31],s90 offen sc1
v_mfma_f32_16x16x32_bf16 v[200:203],v[76:79],a[124:127],0
v_mul_f32 v9,v9,v61
v_mul_f32 v10,v10,v61
v_mfma_f32_16x16x32_bf16 v[200:203],v[72:75],a[116:119],v[200:203]
v_mul_f32 v11,v11,v61
v_mul_f32 v12,v12,v61
v_mfma_f32_16x16x32_bf16 v[200:203],v[68:71],a[108:111],v[200:203]
v_mul_f32 v13,v13,v61
v_mul_f32 v14,v14,v61
v_mfma_f32_16x16x32_bf16 v[200:203],v[64:67],a[100:103],v[200:203]
v_mul_f32 v15,v15,v61
v_cvt_pk_bf16_f32 v188,v8,v9
v_mfma_f32_16x16x32_bf16 v[208:211],v[64:67],a[64:67],0
v_cvt_pk_bf16_f32 v189,v10,v11
v_cvt_pk_bf16_f32 v190,v12,v13
v_mfma_f32_16x16x32_bf16 v[208:211],v[68:71],a[72:75],v[208:211]
buffer_store_dwordx4 v[212:215],v6,s[40:43],s80 offen offset:2048 sc1
v_mfma_f32_16x16x32_bf16 v[208:211],v[72:75],a[80:83],v[208:211]
ds_read_b64_tr_b16 v[124:125],v2 offset:30752
v_cvt_pk_bf16_f32 v191,v14,v15
v_mfma_f32_16x16x32_bf16 v[208:211],v[76:79],a[88:91],v[208:211]
ds_read_b64_tr_b16 v[126:127],v2 offset:35872
v_accvgpr_read_b32 v16,a32
v_mfma_f32_16x16x32_bf16 v[216:219],v[76:79],a[120:123],0
v_accvgpr_read_b32 v17,a33
v_accvgpr_read_b32 v18,a34
v_mfma_f32_16x16x32_bf16 v[216:219],v[72:75],a[112:115],v[216:219]
v_accvgpr_read_b32 v19,a35
v_accvgpr_read_b32 v20,a36
v_mfma_f32_16x16x32_bf16 v[216:219],v[68:71],a[104:107],v[216:219]
v_accvgpr_read_b32 v21,a37
v_accvgpr_read_b32 v22,a38
v_mfma_f32_16x16x32_bf16 v[216:219],v[64:67],a[96:99],v[216:219]
v_accvgpr_read_b32 v23,a39
v_mul_f32 v16,v16,v61
v_mfma_f32_16x16x32_bf16 v[224:227],v[80:83],a[68:71],0
buffer_load_dwordx4 a[180:183],v6,s[28:31],s90 offen offset:1024 sc1
v_mfma_f32_16x16x32_bf16 v[224:227],v[84:87],a[76:79],v[224:227]
v_mul_f32 v17,v17,v61
v_mul_f32 v18,v18,v61
v_mfma_f32_16x16x32_bf16 v[224:227],v[88:91],a[84:87],v[224:227]
v_mul_f32 v19,v19,v61
v_mul_f32 v20,v20,v61
v_mfma_f32_16x16x32_bf16 v[224:227],v[92:95],a[92:95],v[224:227]
v_mul_f32 v21,v21,v61
v_mul_f32 v22,v22,v61
v_mfma_f32_16x16x32_bf16 v[232:235],v[92:95],a[124:127],0
v_mul_f32 v23,v23,v61
v_cvt_pk_bf16_f32 v140,v16,v17
v_mfma_f32_16x16x32_bf16 v[232:235],v[88:91],a[116:119],v[232:235]
v_cvt_pk_bf16_f32 v141,v18,v19
v_cvt_pk_bf16_f32 v142,v20,v21
v_mfma_f32_16x16x32_bf16 v[232:235],v[84:87],a[108:111],v[232:235]
v_cvt_pk_bf16_f32 v143,v22,v23
v_accvgpr_read_b32 v16,a48
v_mfma_f32_16x16x32_bf16 v[232:235],v[80:83],a[100:103],v[232:235]
v_accvgpr_read_b32 v17,a49
v_accvgpr_read_b32 v18,a50
v_mfma_f32_16x16x32_bf16 v[240:243],v[80:83],a[64:67],0
buffer_store_dwordx4 v[228:231],v6,s[40:43],s81 offen offset:2048 sc1
v_mfma_f32_16x16x32_bf16 v[240:243],v[84:87],a[72:75],v[240:243]
v_accvgpr_read_b32 v19,a51
v_accvgpr_read_b32 v20,a52
v_mfma_f32_16x16x32_bf16 v[240:243],v[88:91],a[80:83],v[240:243]
v_accvgpr_read_b32 v21,a53
v_accvgpr_read_b32 v22,a54
v_mfma_f32_16x16x32_bf16 v[240:243],v[92:95],a[88:91],v[240:243]
v_accvgpr_read_b32 v23,a55
v_mul_f32 v16,v16,v61
v_mfma_f32_16x16x32_bf16 v[248:251],v[92:95],a[120:123],0
v_mul_f32 v17,v17,v61
v_mul_f32 v18,v18,v61
v_mfma_f32_16x16x32_bf16 v[248:251],v[88:91],a[112:115],v[248:251]
buffer_load_dwordx4 a[164:167],v6,s[28:31],s89 offen offset:1024 sc1
v_mfma_f32_16x16x32_bf16 v[248:251],v[84:87],a[104:107],v[248:251]
v_mul_f32 v19,v19,v61
v_mul_f32 v20,v20,v61
v_mfma_f32_16x16x32_bf16 v[248:251],v[80:83],a[96:99],v[248:251]
v_mul_f32 v21,v21,v61
v_mul_f32 v22,v22,v61
v_mfma_f32_16x16x32_bf16 a[192:195],v[96:99],v[32:35],a[192:195]
v_mul_f32 v23,v23,v61
v_cvt_pk_bf16_f32 v172,v16,v17
v_mfma_f32_16x16x32_bf16 a[192:195],v[100:103],v[36:39],a[192:195]
v_cvt_pk_bf16_f32 v173,v18,v19
v_cvt_pk_bf16_f32 v174,v20,v21
v_mfma_f32_16x16x32_bf16 a[192:195],v[104:107],v[40:43],a[192:195]
ds_read_b64_tr_b16 v[64:65],v1 offset:64
v_cvt_pk_bf16_f32 v175,v22,v23
v_mfma_f32_16x16x32_bf16 a[192:195],v[108:111],v[44:47],a[192:195]
ds_read_b64_tr_b16 v[66:67],v1 offset:5184
ds_read_b64_tr_b16 v[68:69],v1 offset:10304
v_mfma_f32_16x16x32_bf16 a[196:199],v[108:111],v[180:183],a[196:199]
s_cmp_eq_u32 s60,3
s_cselect_b32 s69,0x40000000,0
ds_read_b64_tr_b16 v[70:71],v1 offset:15424
v_mfma_f32_16x16x32_bf16 a[196:199],v[104:107],v[164:167],a[196:199]
ds_read_b64_tr_b16 v[72:73],v1 offset:20544
v_add_u32 v28,s69,v6
v_mfma_f32_16x16x32_bf16 a[196:199],v[100:103],v[148:151],a[196:199]
s_lshl_b32 s69,s63,2
ds_read_b64_tr_b16 v[74:75],v1 offset:25664
v_mov_b32 v8,s52
v_mfma_f32_16x16x32_bf16 a[196:199],v[96:99],v[132:135],a[196:199]
buffer_store_dwordx4 v[244:247],v6,s[40:43],s82 offen offset:2048 sc1
v_mfma_f32_16x16x32_bf16 a[200:203],v[96:99],v[196:199],a[200:203]
ds_read_b64_tr_b16 v[76:77],v1 offset:30784
ds_read_b64_tr_b16 v[78:79],v1 offset:35904
v_mfma_f32_16x16x32_bf16 a[200:203],v[100:103],v[212:215],a[200:203]
ds_read_b64_tr_b16 v[80:81],v2 offset:96
v_mov_b32 v9,s53
v_mfma_f32_16x16x32_bf16 a[200:203],v[104:107],v[228:231],a[200:203]
buffer_load_dwordx4 a[148:151],v6,s[28:31],s88 offen offset:1024 sc1
v_mfma_f32_16x16x32_bf16 a[200:203],v[108:111],v[244:247],a[200:203]
ds_read_b64_tr_b16 v[82:83],v2 offset:5216
ds_read_b64_tr_b16 v[84:85],v2 offset:10336
v_mfma_f32_16x16x32_bf16 a[204:207],v[108:111],v[188:191],a[204:207]
ds_read_b64_tr_b16 v[86:87],v2 offset:15456
v_lshlrev_b32 v10,20,v6
v_mfma_f32_16x16x32_bf16 a[204:207],v[104:107],v[172:175],a[204:207]
ds_read_b64_tr_b16 v[88:89],v2 offset:20576
v_bfe_u32 v3,v6,4,2
v_mfma_f32_16x16x32_bf16 a[204:207],v[100:103],v[156:159],a[204:207]
ds_read_b64_tr_b16 v[90:91],v2 offset:25696
v_min_u32 v3,2,v3
v_mfma_f32_16x16x32_bf16 a[204:207],v[96:99],v[140:143],a[204:207]
s_waitcnt lgkmcnt(8)
ds_read_b64_tr_b16 v[92:93],v2 offset:30816
ds_read_b64_tr_b16 v[94:95],v2 offset:35936
v_mfma_f32_16x16x32_bf16 a[208:211],v[112:115],v[32:35],a[208:211]
ds_read_b64_tr_b16 v[96:97],v1 offset:128
v_add3_u32 v3,v3,s64,1
v_mfma_f32_16x16x32_bf16 a[208:211],v[116:119],v[36:39],a[208:211]
ds_read_b64_tr_b16 v[98:99],v1 offset:5248
v_and_b32 v3,3,v3
v_mfma_f32_16x16x32_bf16 a[208:211],v[120:123],v[40:43],a[208:211]
ds_read_b64_tr_b16 v[100:101],v1 offset:10368
v_lshlrev_b32 v3,6,v3
v_mfma_f32_16x16x32_bf16 a[208:211],v[124:127],v[44:47],a[208:211]
ds_read_b64_tr_b16 v[102:103],v1 offset:15488
ds_read_b64_tr_b16 v[104:105],v1 offset:20608
v_mfma_f32_16x16x32_bf16 a[212:215],v[124:127],v[180:183],a[212:215]
ds_read_b64_tr_b16 v[106:107],v1 offset:25728
v_bfe_u32 v56,v6,6,2
v_mfma_f32_16x16x32_bf16 a[212:215],v[120:123],v[164:167],a[212:215]
ds_read_b64_tr_b16 v[108:109],v1 offset:30848
v_lshrrev_b32 v57,8,v6
v_mfma_f32_16x16x32_bf16 a[212:215],v[116:119],v[148:151],a[212:215]
buffer_store_dwordx4 v[140:143],v28,s[40:43],s79 offen offset:3072 sc1
v_mfma_f32_16x16x32_bf16 a[212:215],v[112:115],v[132:135],a[212:215]
ds_read_b64_tr_b16 v[110:111],v1 offset:35968
v_cmp_eq_u32 s[56:57],v56,v57
v_mfma_f32_16x16x32_bf16 a[216:219],v[112:115],v[196:199],a[216:219]
buffer_load_dwordx4 a[132:135],v6,s[28:31],s87 offen offset:1024 sc1
v_mfma_f32_16x16x32_bf16 a[216:219],v[116:119],v[212:215],a[216:219]
v_bfe_u32 v56,v6,4,1
v_lshlrev_b32 v56,4,v56
v_mfma_f32_16x16x32_bf16 a[216:219],v[120:123],v[228:231],a[216:219]
v_mov_b32 v57,0x3f80
v_lshlrev_b32 v56,v56,v57
v_mfma_f32_16x16x32_bf16 a[216:219],v[124:127],v[244:247],a[216:219]
v_cndmask_b32 v56,0,v56,s[56:57]
v_bfe_u32 v57,v6,5,1
v_mfma_f32_16x16x32_bf16 a[220:223],v[124:127],v[188:191],a[220:223]
ds_read_b64_tr_b16 v[124:125],v2 offset:30880
ds_read_b64_tr_b16 v[126:127],v2 offset:36000
v_mfma_f32_16x16x32_bf16 a[220:223],v[120:123],v[172:175],a[220:223]
)A"
R"A(ds_read_b64_tr_b16 v[120:121],v2 offset:20640
v_cmp_eq_u32 s[56:57],0,v57
v_mfma_f32_16x16x32_bf16 a[220:223],v[116:119],v[156:159],a[220:223]
s_add_u32 s36,s12,s66
s_addc_u32 s37,s13,0
ds_read_b64_tr_b16 v[116:117],v2 offset:10400
v_mfma_f32_16x16x32_bf16 a[220:223],v[112:115],v[140:143],a[220:223]
s_and_b32 s37,s37,0xffff
ds_read_b64_tr_b16 v[112:113],v2 offset:160
v_cndmask_b32 v48,0,v56,s[56:57]
v_mfma_f32_16x16x32_bf16 a[224:227],v[64:67],v[32:35],a[224:227]
s_mul_i32 s66,s61,307200
ds_read_b64_tr_b16 v[114:115],v2 offset:5280
ds_read_b64_tr_b16 v[118:119],v2 offset:15520
v_mfma_f32_16x16x32_bf16 a[224:227],v[68:71],v[36:39],a[224:227]
ds_read_b64_tr_b16 v[122:123],v2 offset:25760
v_cndmask_b32 v49,v56,0,s[56:57]
v_mfma_f32_16x16x32_bf16 a[224:227],v[72:75],v[40:43],a[224:227]
s_waitcnt lgkmcnt(8)
v_mov_b32 v58,s52
v_mov_b32 v59,s53
v_mfma_f32_16x16x32_bf16 a[224:227],v[76:79],v[44:47],a[224:227]
s_add_u32 s24,s16,s66
s_addc_u32 s25,s17,0
v_lshlrev_b32 v60,20,v6
v_mfma_f32_16x16x32_bf16 a[228:231],v[76:79],v[180:183],a[228:231]
buffer_load_dwordx4 a[136:139],v6,s[28:31],s87 offen offset:2048 sc1
v_mfma_f32_16x16x32_bf16 a[228:231],v[72:75],v[164:167],a[228:231]
s_mul_i32 s66,s60,16384
s_add_u32 s66,s66,s67
s_add_u32 s79,s66,12288
v_mfma_f32_16x16x32_bf16 a[228:231],v[68:71],v[148:151],a[228:231]
buffer_store_dwordx4 v[156:159],v28,s[40:43],s80 offen offset:3072 sc1
v_mfma_f32_16x16x32_bf16 a[228:231],v[64:67],v[132:135],a[228:231]
s_add_u32 s80,s66,77824
v_bfe_u32 v61,v6,4,1
v_add3_u32 v61,v61,s63,2
v_mfma_f32_16x16x32_bf16 a[232:235],v[64:67],v[196:199],a[232:235]
s_and_b32 s25,s25,0xffff
v_and_b32 v61,3,v61
v_lshlrev_b32 v61,8,v61
v_mfma_f32_16x16x32_bf16 a[232:235],v[68:71],v[212:215],a[232:235]
s_add_u32 s67,s77,8192
v_mov_b32 v50,0
v_mov_b32 v51,0
v_mfma_f32_16x16x32_bf16 a[232:235],v[72:75],v[228:231],a[232:235]
v_mov_b32 v52,0
v_mov_b32 v53,0
v_mfma_f32_16x16x32_bf16 a[232:235],v[76:79],v[244:247],a[232:235]
v_mov_b32 v54,v48
v_mov_b32 v55,v49
v_mfma_f32_16x16x32_bf16 a[236:239],v[76:79],v[188:191],a[236:239]
ds_read_b64_tr_b16 v[76:77],v1 offset:30912
ds_read_b64_tr_b16 v[78:79],v1 offset:36032
v_mfma_f32_16x16x32_bf16 a[236:239],v[72:75],v[172:175],a[236:239]
ds_read_b64_tr_b16 v[72:73],v1 offset:20672
v_bfe_u32 v56,v6,4,4
v_mfma_f32_16x16x32_bf16 a[236:239],v[68:71],v[156:159],a[236:239]
ds_read_b64_tr_b16 v[68:69],v1 offset:10432
v_mul_u32_u24 v56,1280,v56
v_mfma_f32_16x16x32_bf16 a[236:239],v[64:67],v[140:143],a[236:239]
ds_read_b64_tr_b16 v[64:65],v1 offset:192
v_bfe_u32 v57,v6,8,1
v_mfma_f32_16x16x32_bf16 a[240:243],v[80:83],v[32:35],a[240:243]
ds_read_b64_tr_b16 v[66:67],v1 offset:5312
ds_read_b64_tr_b16 v[70:71],v1 offset:15552
v_mfma_f32_16x16x32_bf16 a[240:243],v[84:87],v[36:39],a[240:243]
buffer_load_dwordx4 a[152:155],v6,s[28:31],s88 offen offset:2048 sc1
v_mfma_f32_16x16x32_bf16 a[240:243],v[88:91],v[40:43],a[240:243]
ds_read_b64_tr_b16 v[74:75],v1 offset:25792
v_lshl_add_u32 v56,v57,5,v56
L_pd14_%=:
s_cbranch_execz L_trt1_%=
v_mfma_f32_16x16x32_bf16 a[240:243],v[92:95],v[44:47],a[240:243]
v_lshrrev_b32 v57,9,v6
v_lshl_add_u32 v56,v57,4,v56
v_mfma_f32_16x16x32_bf16 a[244:247],v[92:95],v[180:183],a[244:247]
v_mfma_f32_16x16x32_bf16 a[244:247],v[88:91],v[164:167],a[244:247]
buffer_store_dwordx4 v[172:175],v28,s[40:43],s81 offen offset:3072 sc1
v_mfma_f32_16x16x32_bf16 a[244:247],v[84:87],v[148:151],a[244:247]
s_add_u32 s81,s66,143360
v_mfma_f32_16x16x32_bf16 a[244:247],v[80:83],v[132:135],a[244:247]
v_mfma_f32_16x16x32_bf16 a[248:251],v[80:83],v[196:199],a[248:251]
v_mfma_f32_16x16x32_bf16 a[248:251],v[84:87],v[212:215],a[248:251]
v_mfma_f32_16x16x32_bf16 a[248:251],v[88:91],v[228:231],a[248:251]
v_mfma_f32_16x16x32_bf16 a[248:251],v[92:95],v[244:247],a[248:251]
v_mfma_f32_16x16x32_bf16 a[252:255],v[92:95],v[188:191],a[252:255]
ds_read_b64_tr_b16 v[92:93],v2 offset:30944
ds_read_b64_tr_b16 v[94:95],v2 offset:36064
v_mfma_f32_16x16x32_bf16 a[252:255],v[88:91],v[172:175],a[252:255]
ds_read_b64_tr_b16 v[88:89],v2 offset:20704
v_mfma_f32_16x16x32_bf16 a[252:255],v[84:87],v[156:159],a[252:255]
buffer_load_dwordx4 a[168:171],v6,s[28:31],s89 offen offset:2048 sc1
v_mfma_f32_16x16x32_bf16 a[252:255],v[80:83],v[140:143],a[252:255]
ds_read_b64_tr_b16 v[80:81],v2 offset:224
ds_read_b64_tr_b16 v[82:83],v2 offset:5344
v_mfma_f32_16x16x32_bf16 a[0:3],v[96:99],v[32:35],a[0:3]
ds_read_b64_tr_b16 v[84:85],v2 offset:10464
v_mfma_f32_16x16x32_bf16 a[0:3],v[100:103],v[36:39],a[0:3]
ds_read_b64_tr_b16 v[86:87],v2 offset:15584
v_mfma_f32_16x16x32_bf16 a[0:3],v[104:107],v[40:43],a[0:3]
ds_read_b64_tr_b16 v[90:91],v2 offset:25824
v_mfma_f32_16x16x32_bf16 a[0:3],v[108:111],v[44:47],a[0:3]
v_mfma_f32_16x16x32_bf16 a[4:7],v[108:111],v[180:183],a[4:7]
buffer_store_dwordx4 v[188:191],v28,s[40:43],s82 offen offset:3072 sc1
v_mfma_f32_16x16x32_bf16 a[4:7],v[104:107],v[164:167],a[4:7]
s_add_u32 s82,s66,208896
s_mul_i32 s66,s64,320
v_mfma_f32_16x16x32_bf16 a[4:7],v[100:103],v[148:151],a[4:7]
v_mfma_f32_16x16x32_bf16 a[4:7],v[96:99],v[132:135],a[4:7]
v_mfma_f32_16x16x32_bf16 a[8:11],v[96:99],v[196:199],a[8:11]
v_mfma_f32_16x16x32_bf16 a[8:11],v[100:103],v[212:215],a[8:11]
v_mfma_f32_16x16x32_bf16 a[8:11],v[104:107],v[228:231],a[8:11]
v_mfma_f32_16x16x32_bf16 a[8:11],v[108:111],v[244:247],a[8:11]
buffer_load_dwordx4 a[184:187],v6,s[28:31],s90 offen offset:2048 sc1
v_mfma_f32_16x16x32_bf16 a[12:15],v[108:111],v[188:191],a[12:15]
ds_read_b64_tr_b16 v[108:109],v1 offset:30976
ds_read_b64_tr_b16 v[110:111],v1 offset:36096
v_mfma_f32_16x16x32_bf16 a[12:15],v[104:107],v[172:175],a[12:15]
ds_read_b64_tr_b16 v[104:105],v1 offset:20736
v_mfma_f32_16x16x32_bf16 a[12:15],v[100:103],v[156:159],a[12:15]
ds_read_b64_tr_b16 v[100:101],v1 offset:10496
v_mfma_f32_16x16x32_bf16 a[12:15],v[96:99],v[140:143],a[12:15]
s_waitcnt lgkmcnt(4)
ds_read_b64_tr_b16 v[96:97],v1 offset:256
v_mfma_f32_16x16x32_bf16 a[16:19],v[112:115],v[32:35],a[16:19]
ds_read_b64_tr_b16 v[98:99],v1 offset:5376
ds_read_b64_tr_b16 v[102:103],v1 offset:15616
v_mfma_f32_16x16x32_bf16 a[16:19],v[116:119],v[36:39],a[16:19]
ds_read_b64_tr_b16 v[106:107],v1 offset:25856
v_add_u32 v1,0xffff6000,v1
v_mfma_f32_16x16x32_bf16 a[16:19],v[120:123],v[40:43],a[16:19]
v_mfma_f32_16x16x32_bf16 a[16:19],v[124:127],v[44:47],a[16:19]
v_mfma_f32_16x16x32_bf16 a[20:23],v[124:127],v[180:183],a[20:23]
v_mfma_f32_16x16x32_bf16 a[20:23],v[120:123],v[164:167],a[20:23]
v_mfma_f32_16x16x32_bf16 a[20:23],v[116:119],v[148:151],a[20:23]
v_mfma_f32_16x16x32_bf16 a[20:23],v[112:115],v[132:135],a[20:23]
v_mfma_f32_16x16x32_bf16 a[24:27],v[112:115],v[196:199],a[24:27]
buffer_load_dwordx4 a[188:191],v28,s[28:31],s90 offen offset:3072 sc1
v_mfma_f32_16x16x32_bf16 a[24:27],v[116:119],v[212:215],a[24:27]
v_mfma_f32_16x16x32_bf16 a[24:27],v[120:123],v[228:231],a[24:27]
v_mfma_f32_16x16x32_bf16 a[24:27],v[124:127],v[244:247],a[24:27]
v_mfma_f32_16x16x32_bf16 a[28:31],v[124:127],v[188:191],a[28:31]
ds_read_b64_tr_b16 v[124:125],v2 offset:31008
ds_read_b64_tr_b16 v[126:127],v2 offset:36128
v_mfma_f32_16x16x32_bf16 a[28:31],v[120:123],v[172:175],a[28:31]
ds_read_b64_tr_b16 v[120:121],v2 offset:20768
v_mfma_f32_16x16x32_bf16 a[28:31],v[116:119],v[156:159],a[28:31]
ds_read_b64_tr_b16 v[116:117],v2 offset:10528
v_mfma_f32_16x16x32_bf16 a[28:31],v[112:115],v[140:143],a[28:31]
ds_read_b64_tr_b16 v[112:113],v2 offset:288
v_mfma_f32_16x16x32_bf16 v[128:131],v[64:67],v[32:35],v[128:131]
ds_read_b64_tr_b16 v[114:115],v2 offset:5408
ds_read_b64_tr_b16 v[118:119],v2 offset:15648
v_mfma_f32_16x16x32_bf16 v[128:131],v[68:71],v[36:39],v[128:131]
ds_read_b64_tr_b16 v[122:123],v2 offset:25888
v_add_u32 v2,0xffff6000,v2
v_mfma_f32_16x16x32_bf16 v[128:131],v[72:75],v[40:43],v[128:131]
v_mfma_f32_16x16x32_bf16 v[128:131],v[76:79],v[44:47],v[128:131]
v_mfma_f32_16x16x32_bf16 v[136:139],v[76:79],v[180:183],v[136:139]
v_mfma_f32_16x16x32_bf16 v[136:139],v[72:75],v[164:167],v[136:139]
buffer_load_dwordx4 a[172:175],v28,s[28:31],s89 offen offset:3072 sc1
v_mfma_f32_16x16x32_bf16 v[136:139],v[68:71],v[148:151],v[136:139]
v_mfma_f32_16x16x32_bf16 v[136:139],v[64:67],v[132:135],v[136:139]
v_mfma_f32_16x16x32_bf16 v[144:147],v[64:67],v[196:199],v[144:147]
v_mfma_f32_16x16x32_bf16 v[144:147],v[68:71],v[212:215],v[144:147]
v_mfma_f32_16x16x32_bf16 v[144:147],v[72:75],v[228:231],v[144:147]
v_mfma_f32_16x16x32_bf16 v[144:147],v[76:79],v[244:247],v[144:147]
v_mfma_f32_16x16x32_bf16 v[152:155],v[76:79],v[188:191],v[152:155]
v_mfma_f32_16x16x32_bf16 v[152:155],v[72:75],v[172:175],v[152:155]
v_mfma_f32_16x16x32_bf16 v[152:155],v[68:71],v[156:159],v[152:155]
v_mfma_f32_16x16x32_bf16 v[152:155],v[64:67],v[140:143],v[152:155]
s_mov_b64 exec,0
s_cmp_eq_u32 s60,0
s_cbranch_scc1 L_pd16_%=
s_cmp_eq_u32 s60,1
s_cbranch_scc1 L_pd17_%=
s_cmp_eq_u32 s60,2
s_cbranch_scc1 L_pd18_%=
s_branch L_trtg2F_%=
L_trtg2F_%=:
s_mov_b64 exec,-1
s_waitcnt lgkmcnt(0)
s_barrier
v_mfma_f32_16x16x32_bf16 v[160:163],v[80:83],v[32:35],v[160:163]
buffer_load_dwordx4 v24,s[48:51],s83 offen lds
v_mfma_f32_16x16x32_bf16 v[160:163],v[84:87],v[36:39],v[160:163]
ds_read_b64_tr_b16 v[64:65],v1
ds_read_b64_tr_b16 v[66:67],v1 offset:5120
v_mfma_f32_16x16x32_bf16 v[160:163],v[88:91],v[40:43],v[160:163]
ds_read_b64_tr_b16 v[68:69],v1 offset:10240
v_mfma_f32_16x16x32_bf16 v[160:163],v[92:95],v[44:47],v[160:163]
ds_read_b64_tr_b16 v[70:71],v1 offset:15360
v_mfma_f32_16x16x32_bf16 v[168:171],v[92:95],v[180:183],v[168:171]
ds_read_b64_tr_b16 v[72:73],v1 offset:20480
v_mfma_f32_16x16x32_bf16 v[168:171],v[88:91],v[164:167],v[168:171]
buffer_load_dwordx4 a[156:159],v28,s[28:31],s88 offen offset:3072 sc1
v_mfma_f32_16x16x32_bf16 v[168:171],v[84:87],v[148:151],v[168:171]
ds_read_b64_tr_b16 v[74:75],v1 offset:25600
ds_read_b64_tr_b16 v[76:77],v1 offset:30720
v_mfma_f32_16x16x32_bf16 v[168:171],v[80:83],v[132:135],v[168:171]
ds_read_b64_tr_b16 v[78:79],v1 offset:35840
v_mfma_f32_16x16x32_bf16 v[176:179],v[80:83],v[196:199],v[176:179]
v_mfma_f32_16x16x32_bf16 v[176:179],v[84:87],v[212:215],v[176:179]
v_mfma_f32_16x16x32_bf16 v[176:179],v[88:91],v[228:231],v[176:179]
v_mfma_f32_16x16x32_bf16 v[176:179],v[92:95],v[244:247],v[176:179]
buffer_load_dwordx4 v25,s[48:51],s83 offen offset:1024 lds
v_mfma_f32_16x16x32_bf16 v[184:187],v[92:95],v[188:191],v[184:187]
ds_read_b64_tr_b16 v[92:93],v2 offset:30752
ds_read_b64_tr_b16 v[94:95],v2 offset:35872
v_mfma_f32_16x16x32_bf16 v[184:187],v[88:91],v[172:175],v[184:187]
ds_read_b64_tr_b16 v[88:89],v2 offset:20512
v_mfma_f32_16x16x32_bf16 v[184:187],v[84:87],v[156:159],v[184:187]
ds_read_b64_tr_b16 v[84:85],v2 offset:10272
v_mfma_f32_16x16x32_bf16 v[184:187],v[80:83],v[140:143],v[184:187]
ds_read_b64_tr_b16 v[80:81],v2 offset:32
v_mfma_f32_16x16x32_bf16 v[192:195],v[96:99],v[32:35],v[192:195]
buffer_load_dwordx4 a[140:143],v28,s[28:31],s87 offen offset:3072 sc1
v_mfma_f32_16x16x32_bf16 v[192:195],v[100:103],v[36:39],v[192:195]
s_add_u32 s87,s64,2
s_and_b32 s87,s87,3
s_lshl_b32 s87,s87,1
v_mfma_f32_16x16x32_bf16 v[192:195],v[104:107],v[40:43],v[192:195]
s_lshl_b32 s87,s87,16
s_add_u32 s88,s87,4096
s_add_u32 s89,s87,8192
v_mfma_f32_16x16x32_bf16 v[192:195],v[108:111],v[44:47],v[192:195]
s_add_u32 s90,s87,12288
ds_read_b64_tr_b16 v[82:83],v2 offset:5152
ds_read_b64_tr_b16 v[86:87],v2 offset:15392
v_mfma_f32_16x16x32_bf16 v[200:203],v[108:111],v[180:183],v[200:203]
ds_read_b64_tr_b16 v[90:91],v2 offset:25632
v_mfma_f32_16x16x32_bf16 v[200:203],v[104:107],v[164:167],v[200:203]
buffer_load_dwordx4 a[68:71],v6,s[28:31],s87 offen sc1
v_mfma_f32_16x16x32_bf16 v[200:203],v[100:103],v[148:151],v[200:203]
v_mfma_f32_16x16x32_bf16 v[200:203],v[96:99],v[132:135],v[200:203]
v_mfma_f32_16x16x32_bf16 v[208:211],v[96:99],v[196:199],v[208:211]
v_mfma_f32_16x16x32_bf16 v[208:211],v[100:103],v[212:215],v[208:211]
v_mfma_f32_16x16x32_bf16 v[208:211],v[104:107],v[228:231],v[208:211]
buffer_load_dwordx4 a[76:79],v6,s[28:31],s88 offen sc1
v_mfma_f32_16x16x32_bf16 v[208:211],v[108:111],v[244:247],v[208:211]
v_mfma_f32_16x16x32_bf16 v[216:219],v[108:111],v[188:191],v[216:219]
)A"
R"A(ds_read_b64_tr_b16 v[108:109],v1 offset:30784
ds_read_b64_tr_b16 v[110:111],v1 offset:35904
v_mfma_f32_16x16x32_bf16 v[216:219],v[104:107],v[172:175],v[216:219]
ds_read_b64_tr_b16 v[104:105],v1 offset:20544
v_mfma_f32_16x16x32_bf16 v[216:219],v[100:103],v[156:159],v[216:219]
ds_read_b64_tr_b16 v[100:101],v1 offset:10304
v_mfma_f32_16x16x32_bf16 v[216:219],v[96:99],v[140:143],v[216:219]
buffer_load_dwordx4 a[84:87],v6,s[28:31],s89 offen sc1
v_mfma_f32_16x16x32_bf16 v[224:227],v[112:115],v[32:35],v[224:227]
ds_read_b64_tr_b16 v[96:97],v1 offset:64
ds_read_b64_tr_b16 v[98:99],v1 offset:5184
v_mfma_f32_16x16x32_bf16 v[224:227],v[116:119],v[36:39],v[224:227]
ds_read_b64_tr_b16 v[102:103],v1 offset:15424
v_mfma_f32_16x16x32_bf16 v[224:227],v[120:123],v[40:43],v[224:227]
ds_read_b64_tr_b16 v[106:107],v1 offset:25664
v_mfma_f32_16x16x32_bf16 v[224:227],v[124:127],v[44:47],v[224:227]
v_mfma_f32_16x16x32_bf16 v[232:235],v[124:127],v[180:183],v[232:235]
buffer_load_dwordx4 a[92:95],v6,s[28:31],s90 offen sc1
v_mfma_f32_16x16x32_bf16 v[232:235],v[120:123],v[164:167],v[232:235]
v_mfma_f32_16x16x32_bf16 v[232:235],v[116:119],v[148:151],v[232:235]
v_mfma_f32_16x16x32_bf16 v[232:235],v[112:115],v[132:135],v[232:235]
v_mfma_f32_16x16x32_bf16 v[240:243],v[112:115],v[196:199],v[240:243]
v_mfma_f32_16x16x32_bf16 v[240:243],v[116:119],v[212:215],v[240:243]
buffer_load_dwordx4 a[124:127],v6,s[28:31],s90 offen offset:1024 sc1
v_mfma_f32_16x16x32_bf16 v[240:243],v[120:123],v[228:231],v[240:243]
v_mfma_f32_16x16x32_bf16 v[240:243],v[124:127],v[244:247],v[240:243]
v_mfma_f32_16x16x32_bf16 v[248:251],v[124:127],v[188:191],v[248:251]
ds_read_b64_tr_b16 v[124:125],v2 offset:30816
ds_read_b64_tr_b16 v[126:127],v2 offset:35936
v_mfma_f32_16x16x32_bf16 v[248:251],v[120:123],v[172:175],v[248:251]
ds_read_b64_tr_b16 v[120:121],v2 offset:20576
v_mfma_f32_16x16x32_bf16 v[248:251],v[116:119],v[156:159],v[248:251]
buffer_load_dwordx4 a[116:119],v6,s[28:31],s89 offen offset:1024 sc1
v_mfma_f32_16x16x32_bf16 v[248:251],v[112:115],v[140:143],v[248:251]
s_waitcnt vmcnt(31) lgkmcnt(3)
ds_read_b64_tr_b16 v[112:113],v2 offset:96
ds_read_b64_tr_b16 v[114:115],v2 offset:5216
v_mfma_f32_16x16x32_bf16 a[192:195],v[64:67],a[128:131],a[192:195]
ds_read_b64_tr_b16 v[116:117],v2 offset:10336
v_mfma_f32_16x16x32_bf16 a[192:195],v[68:71],a[144:147],a[192:195]
s_waitcnt vmcnt(29)
ds_read_b64_tr_b16 v[118:119],v2 offset:15456
v_mfma_f32_16x16x32_bf16 a[192:195],v[72:75],a[160:163],a[192:195]
s_waitcnt vmcnt(25)
ds_read_b64_tr_b16 v[122:123],v2 offset:25696
v_mfma_f32_16x16x32_bf16 a[192:195],v[76:79],a[176:179],a[192:195]
buffer_load_dwordx4 a[108:111],v6,s[28:31],s88 offen offset:1024 sc1
v_mfma_f32_16x16x32_bf16 a[196:199],v[76:79],a[180:183],a[196:199]
s_waitcnt vmcnt(24)
v_mfma_f32_16x16x32_bf16 a[196:199],v[72:75],a[164:167],a[196:199]
s_waitcnt vmcnt(22)
v_mfma_f32_16x16x32_bf16 a[196:199],v[68:71],a[148:151],a[196:199]
s_waitcnt vmcnt(19)
v_mfma_f32_16x16x32_bf16 a[196:199],v[64:67],a[132:135],a[196:199]
s_waitcnt vmcnt(17)
v_mfma_f32_16x16x32_bf16 a[200:203],v[64:67],a[136:139],a[200:203]
buffer_load_dwordx4 a[100:103],v6,s[28:31],s87 offen offset:1024 sc1
v_mfma_f32_16x16x32_bf16 a[200:203],v[68:71],a[152:155],a[200:203]
s_waitcnt vmcnt(16)
v_mfma_f32_16x16x32_bf16 a[200:203],v[72:75],a[168:171],a[200:203]
s_waitcnt vmcnt(14)
v_mfma_f32_16x16x32_bf16 a[200:203],v[76:79],a[184:187],a[200:203]
s_waitcnt vmcnt(13)
v_mfma_f32_16x16x32_bf16 a[204:207],v[76:79],a[188:191],a[204:207]
s_waitcnt vmcnt(10)
ds_read_b64_tr_b16 v[76:77],v1 offset:30848
ds_read_b64_tr_b16 v[78:79],v1 offset:35968
v_mfma_f32_16x16x32_bf16 a[204:207],v[72:75],a[172:175],a[204:207]
buffer_load_dwordx4 a[64:67],v6,s[28:31],s87 offen offset:2048 sc1
v_mfma_f32_16x16x32_bf16 a[204:207],v[68:71],a[156:159],a[204:207]
s_waitcnt vmcnt(9)
ds_read_b64_tr_b16 v[68:69],v1 offset:10368
ds_read_b64_tr_b16 v[70:71],v1 offset:15488
v_mfma_f32_16x16x32_bf16 a[204:207],v[64:67],a[140:143],a[204:207]
ds_read_b64_tr_b16 v[64:65],v1 offset:128
v_mfma_f32_16x16x32_bf16 a[208:211],v[80:83],a[128:131],a[208:211]
ds_read_b64_tr_b16 v[66:67],v1 offset:5248
v_mfma_f32_16x16x32_bf16 a[208:211],v[84:87],a[144:147],a[208:211]
ds_read_b64_tr_b16 v[72:73],v1 offset:20608
v_mfma_f32_16x16x32_bf16 a[208:211],v[88:91],a[160:163],a[208:211]
ds_read_b64_tr_b16 v[74:75],v1 offset:25728
v_mfma_f32_16x16x32_bf16 a[208:211],v[92:95],a[176:179],a[208:211]
buffer_load_dwordx4 v26,s[48:51],s83 offen offset:2048 lds
v_mfma_f32_16x16x32_bf16 a[212:215],v[92:95],a[180:183],a[212:215]
v_mfma_f32_16x16x32_bf16 a[212:215],v[88:91],a[164:167],a[212:215]
v_mfma_f32_16x16x32_bf16 a[212:215],v[84:87],a[148:151],a[212:215]
v_mfma_f32_16x16x32_bf16 a[212:215],v[80:83],a[132:135],a[212:215]
v_mfma_f32_16x16x32_bf16 a[216:219],v[80:83],a[136:139],a[216:219]
buffer_load_dwordx4 a[72:75],v6,s[28:31],s88 offen offset:2048 sc1
v_mfma_f32_16x16x32_bf16 a[216:219],v[84:87],a[152:155],a[216:219]
v_mfma_f32_16x16x32_bf16 a[216:219],v[88:91],a[168:171],a[216:219]
v_mfma_f32_16x16x32_bf16 a[216:219],v[92:95],a[184:187],a[216:219]
v_mfma_f32_16x16x32_bf16 a[220:223],v[92:95],a[188:191],a[220:223]
ds_read_b64_tr_b16 v[92:93],v2 offset:30880
ds_read_b64_tr_b16 v[94:95],v2 offset:36000
v_mfma_f32_16x16x32_bf16 a[220:223],v[88:91],a[172:175],a[220:223]
ds_read_b64_tr_b16 v[88:89],v2 offset:20640
v_mfma_f32_16x16x32_bf16 a[220:223],v[84:87],a[156:159],a[220:223]
buffer_load_dwordx4 v27,s[48:51],s83 offen offset:3072 lds
v_mfma_f32_16x16x32_bf16 a[220:223],v[80:83],a[140:143],a[220:223]
s_add_u32 m0,s86,126976
ds_read_b64_tr_b16 v[80:81],v2 offset:160
ds_read_b64_tr_b16 v[82:83],v2 offset:5280
s_barrier
v_mfma_f32_16x16x32_bf16 a[224:227],v[96:99],a[128:131],a[224:227]
ds_read_b64_tr_b16 v[84:85],v2 offset:10400
v_mfma_f32_16x16x32_bf16 a[224:227],v[100:103],a[144:147],a[224:227]
ds_read_b64_tr_b16 v[86:87],v2 offset:15520
v_mfma_f32_16x16x32_bf16 a[224:227],v[104:107],a[160:163],a[224:227]
buffer_store_dwordx2 v[8:9],v10,s[44:47],s18 offen offset:1280 sc1
v_mfma_f32_16x16x32_bf16 a[224:227],v[108:111],a[176:179],a[224:227]
ds_read_b64_tr_b16 v[90:91],v2 offset:25760
v_mfma_f32_16x16x32_bf16 a[228:231],v[108:111],a[180:183],a[228:231]
v_mfma_f32_16x16x32_bf16 a[228:231],v[104:107],a[164:167],a[228:231]
v_mfma_f32_16x16x32_bf16 a[228:231],v[100:103],a[148:151],a[228:231]
v_mfma_f32_16x16x32_bf16 a[228:231],v[96:99],a[132:135],a[228:231]
buffer_load_dwordx4 a[80:83],v6,s[28:31],s89 offen offset:2048 sc1
v_mfma_f32_16x16x32_bf16 a[232:235],v[96:99],a[136:139],a[232:235]
v_mfma_f32_16x16x32_bf16 a[232:235],v[100:103],a[152:155],a[232:235]
v_mfma_f32_16x16x32_bf16 a[232:235],v[104:107],a[168:171],a[232:235]
v_mfma_f32_16x16x32_bf16 a[232:235],v[108:111],a[184:187],a[232:235]
v_mfma_f32_16x16x32_bf16 a[236:239],v[108:111],a[188:191],a[236:239]
s_waitcnt lgkmcnt(0)
ds_read_b64_tr_b16 v[108:109],v1 offset:30912
ds_read_b64_tr_b16 v[110:111],v1 offset:36032
v_mfma_f32_16x16x32_bf16 a[236:239],v[104:107],a[172:175],a[236:239]
buffer_load_dwordx4 v7,s[48:51],s83 offen lds
v_mfma_f32_16x16x32_bf16 a[236:239],v[100:103],a[156:159],a[236:239]
s_add_u32 m0,s86,128000
ds_read_b64_tr_b16 v[100:101],v1 offset:10432
ds_read_b64_tr_b16 v[102:103],v1 offset:15552
v_mfma_f32_16x16x32_bf16 a[236:239],v[96:99],a[140:143],a[236:239]
s_add_u32 s83,s64,1
s_and_b32 s83,s83,3
ds_read_b64_tr_b16 v[96:97],v1 offset:192
v_mfma_f32_16x16x32_bf16 a[240:243],v[112:115],a[128:131],a[240:243]
s_lshl_b32 s83,s83,1
s_or_b32 s83,s83,1
ds_read_b64_tr_b16 v[98:99],v1 offset:5312
v_mfma_f32_16x16x32_bf16 a[240:243],v[116:119],a[144:147],a[240:243]
s_mul_i32 s83,s83,163840
s_add_u32 s83,s83,s85
ds_read_b64_tr_b16 v[104:105],v1 offset:20672
v_mfma_f32_16x16x32_bf16 a[240:243],v[120:123],a[160:163],a[240:243]
buffer_load_dwordx4 a[88:91],v6,s[28:31],s90 offen offset:2048 sc1
v_mfma_f32_16x16x32_bf16 a[240:243],v[124:127],a[176:179],a[240:243]
ds_read_b64_tr_b16 v[106:107],v1 offset:25792
v_mfma_f32_16x16x32_bf16 a[244:247],v[124:127],a[180:183],a[244:247]
v_mfma_f32_16x16x32_bf16 a[244:247],v[120:123],a[164:167],a[244:247]
v_mfma_f32_16x16x32_bf16 a[244:247],v[116:119],a[148:151],a[244:247]
v_mfma_f32_16x16x32_bf16 a[244:247],v[112:115],a[132:135],a[244:247]
v_mfma_f32_16x16x32_bf16 a[248:251],v[112:115],a[136:139],a[248:251]
buffer_load_dwordx4 v24,s[48:51],s84 offen lds
v_mfma_f32_16x16x32_bf16 a[248:251],v[116:119],a[152:155],a[248:251]
v_mfma_f32_16x16x32_bf16 a[248:251],v[120:123],a[168:171],a[248:251]
v_mfma_f32_16x16x32_bf16 a[248:251],v[124:127],a[184:187],a[248:251]
v_mfma_f32_16x16x32_bf16 a[252:255],v[124:127],a[188:191],a[252:255]
ds_read_b64_tr_b16 v[124:125],v2 offset:30944
ds_read_b64_tr_b16 v[126:127],v2 offset:36064
v_mfma_f32_16x16x32_bf16 a[252:255],v[120:123],a[172:175],a[252:255]
buffer_load_dwordx4 a[120:123],v28,s[28:31],s90 offen offset:3072 sc1
v_mfma_f32_16x16x32_bf16 a[252:255],v[116:119],a[156:159],a[252:255]
ds_read_b64_tr_b16 v[116:117],v2 offset:10464
ds_read_b64_tr_b16 v[118:119],v2 offset:15584
v_mfma_f32_16x16x32_bf16 a[252:255],v[112:115],a[140:143],a[252:255]
ds_read_b64_tr_b16 v[112:113],v2 offset:224
v_mfma_f32_16x16x32_bf16 a[0:3],v[64:67],a[128:131],a[0:3]
ds_read_b64_tr_b16 v[114:115],v2 offset:5344
v_mfma_f32_16x16x32_bf16 a[0:3],v[68:71],a[144:147],a[0:3]
ds_read_b64_tr_b16 v[120:121],v2 offset:20704
v_mfma_f32_16x16x32_bf16 a[0:3],v[72:75],a[160:163],a[0:3]
ds_read_b64_tr_b16 v[122:123],v2 offset:25824
v_mfma_f32_16x16x32_bf16 a[0:3],v[76:79],a[176:179],a[0:3]
buffer_load_dwordx4 v25,s[48:51],s84 offen offset:1024 lds
v_mfma_f32_16x16x32_bf16 a[4:7],v[76:79],a[180:183],a[4:7]
v_mfma_f32_16x16x32_bf16 a[4:7],v[72:75],a[164:167],a[4:7]
v_mfma_f32_16x16x32_bf16 a[4:7],v[68:71],a[148:151],a[4:7]
v_mfma_f32_16x16x32_bf16 a[4:7],v[64:67],a[132:135],a[4:7]
v_mfma_f32_16x16x32_bf16 a[8:11],v[64:67],a[136:139],a[8:11]
buffer_load_dwordx4 a[112:115],v28,s[28:31],s89 offen offset:3072 sc1
v_mfma_f32_16x16x32_bf16 a[8:11],v[68:71],a[152:155],a[8:11]
v_mfma_f32_16x16x32_bf16 a[8:11],v[72:75],a[168:171],a[8:11]
v_mfma_f32_16x16x32_bf16 a[8:11],v[76:79],a[184:187],a[8:11]
v_mfma_f32_16x16x32_bf16 a[12:15],v[76:79],a[188:191],a[12:15]
ds_read_b64_tr_b16 v[76:77],v1 offset:30976
ds_read_b64_tr_b16 v[78:79],v1 offset:36096
v_mfma_f32_16x16x32_bf16 a[12:15],v[72:75],a[172:175],a[12:15]
buffer_load_dwordx4 a[104:107],v28,s[28:31],s88 offen offset:3072 sc1
v_mfma_f32_16x16x32_bf16 a[12:15],v[68:71],a[156:159],a[12:15]
ds_read_b64_tr_b16 v[68:69],v1 offset:10496
ds_read_b64_tr_b16 v[70:71],v1 offset:15616
v_mfma_f32_16x16x32_bf16 a[12:15],v[64:67],a[140:143],a[12:15]
ds_read_b64_tr_b16 v[64:65],v1 offset:256
v_mfma_f32_16x16x32_bf16 a[16:19],v[80:83],a[128:131],a[16:19]
ds_read_b64_tr_b16 v[66:67],v1 offset:5376
v_mfma_f32_16x16x32_bf16 a[16:19],v[84:87],a[144:147],a[16:19]
ds_read_b64_tr_b16 v[72:73],v1 offset:20736
v_mfma_f32_16x16x32_bf16 a[16:19],v[88:91],a[160:163],a[16:19]
ds_read_b64_tr_b16 v[74:75],v1 offset:25856
v_add_u32 v1,0xffff6000,v1
v_mfma_f32_16x16x32_bf16 a[16:19],v[92:95],a[176:179],a[16:19]
buffer_load_dwordx4 v26,s[48:51],s84 offen offset:2048 lds
v_mfma_f32_16x16x32_bf16 a[20:23],v[92:95],a[180:183],a[20:23]
v_mfma_f32_16x16x32_bf16 a[20:23],v[88:91],a[164:167],a[20:23]
v_mfma_f32_16x16x32_bf16 a[20:23],v[84:87],a[148:151],a[20:23]
v_mfma_f32_16x16x32_bf16 a[20:23],v[80:83],a[132:135],a[20:23]
v_mfma_f32_16x16x32_bf16 a[24:27],v[80:83],a[136:139],a[24:27]
buffer_load_dwordx4 a[96:99],v28,s[28:31],s87 offen offset:3072 sc1
v_mfma_f32_16x16x32_bf16 a[24:27],v[84:87],a[152:155],a[24:27]
s_add_u32 s87,s64,3
s_and_b32 s87,s87,3
s_lshl_b32 s87,s87,1
v_mfma_f32_16x16x32_bf16 a[24:27],v[88:91],a[168:171],a[24:27]
s_lshl_b32 s87,s87,16
s_add_u32 s88,s87,4096
s_add_u32 s89,s87,8192
v_mfma_f32_16x16x32_bf16 a[24:27],v[92:95],a[184:187],a[24:27]
s_add_u32 s90,s87,12288
v_mfma_f32_16x16x32_bf16 a[28:31],v[92:95],a[188:191],a[28:31]
ds_read_b64_tr_b16 v[92:93],v2 offset:31008
ds_read_b64_tr_b16 v[94:95],v2 offset:36128
v_mfma_f32_16x16x32_bf16 a[28:31],v[88:91],a[172:175],a[28:31]
)A"
R"A(s_waitcnt lgkmcnt(2)
ds_read_b64_tr_b16 v[88:89],v2 offset:20768
v_mfma_f32_16x16x32_bf16 a[28:31],v[84:87],a[156:159],a[28:31]
buffer_load_dwordx4 v27,s[48:51],s84 offen offset:3072 lds
v_mfma_f32_16x16x32_bf16 a[28:31],v[80:83],a[140:143],a[28:31]
s_add_u32 m0,s86,132096
ds_read_b64_tr_b16 v[80:81],v2 offset:288
ds_read_b64_tr_b16 v[82:83],v2 offset:5408
v_mfma_f32_16x16x32_bf16 v[128:131],v[96:99],a[128:131],v[128:131]
ds_read_b64_tr_b16 v[84:85],v2 offset:10528
v_mfma_f32_16x16x32_bf16 v[128:131],v[100:103],a[144:147],v[128:131]
ds_read_b64_tr_b16 v[86:87],v2 offset:15648
v_mfma_f32_16x16x32_bf16 v[128:131],v[104:107],a[160:163],v[128:131]
ds_read_b64_tr_b16 v[90:91],v2 offset:25888
v_add_u32 v2,0xffff6000,v2
v_mfma_f32_16x16x32_bf16 v[128:131],v[108:111],a[176:179],v[128:131]
v_mfma_f32_16x16x32_bf16 v[136:139],v[108:111],a[180:183],v[136:139]
buffer_load_dwordx4 v7,s[48:51],s84 offen lds
v_mfma_f32_16x16x32_bf16 v[136:139],v[104:107],a[164:167],v[136:139]
s_add_u32 m0,s86,81920
s_add_u32 s84,s83,20480
v_mfma_f32_16x16x32_bf16 v[136:139],v[100:103],a[148:151],v[136:139]
v_mfma_f32_16x16x32_bf16 v[136:139],v[96:99],a[132:135],v[136:139]
v_mfma_f32_16x16x32_bf16 v[144:147],v[96:99],a[136:139],v[144:147]
v_mfma_f32_16x16x32_bf16 v[144:147],v[100:103],a[152:155],v[144:147]
v_mfma_f32_16x16x32_bf16 v[144:147],v[104:107],a[168:171],v[144:147]
buffer_load_dwordx4 v24,s[48:51],s83 offen lds
v_mfma_f32_16x16x32_bf16 v[144:147],v[108:111],a[184:187],v[144:147]
v_mfma_f32_16x16x32_bf16 v[152:155],v[108:111],a[188:191],v[152:155]
ds_read_b64_tr_b16 v[108:109],v1 offset:30720
ds_read_b64_tr_b16 v[110:111],v1 offset:35840
v_mfma_f32_16x16x32_bf16 v[152:155],v[104:107],a[172:175],v[152:155]
ds_read_b64_tr_b16 v[104:105],v1 offset:20480
v_mfma_f32_16x16x32_bf16 v[152:155],v[100:103],a[156:159],v[152:155]
ds_read_b64_tr_b16 v[100:101],v1 offset:10240
v_mfma_f32_16x16x32_bf16 v[152:155],v[96:99],a[140:143],v[152:155]
ds_read_b64_tr_b16 v[96:97],v1
v_mfma_f32_16x16x32_bf16 v[160:163],v[112:115],a[128:131],v[160:163]
buffer_load_dwordx4 v25,s[48:51],s83 offen offset:1024 lds
v_mfma_f32_16x16x32_bf16 v[160:163],v[116:119],a[144:147],v[160:163]
ds_read_b64_tr_b16 v[98:99],v1 offset:5120
ds_read_b64_tr_b16 v[102:103],v1 offset:15360
v_mfma_f32_16x16x32_bf16 v[160:163],v[120:123],a[160:163],v[160:163]
ds_read_b64_tr_b16 v[106:107],v1 offset:25600
v_mfma_f32_16x16x32_bf16 v[160:163],v[124:127],a[176:179],v[160:163]
v_mfma_f32_16x16x32_bf16 v[168:171],v[124:127],a[180:183],v[168:171]
v_mfma_f32_16x16x32_bf16 v[168:171],v[120:123],a[164:167],v[168:171]
v_mfma_f32_16x16x32_bf16 v[168:171],v[116:119],a[148:151],v[168:171]
buffer_load_dwordx4 v26,s[48:51],s83 offen offset:2048 lds
v_mfma_f32_16x16x32_bf16 v[168:171],v[112:115],a[132:135],v[168:171]
v_mfma_f32_16x16x32_bf16 v[176:179],v[112:115],a[136:139],v[176:179]
v_mfma_f32_16x16x32_bf16 v[176:179],v[116:119],a[152:155],v[176:179]
v_mfma_f32_16x16x32_bf16 v[176:179],v[120:123],a[168:171],v[176:179]
v_mfma_f32_16x16x32_bf16 v[176:179],v[124:127],a[184:187],v[176:179]
v_mfma_f32_16x16x32_bf16 v[184:187],v[124:127],a[188:191],v[184:187]
buffer_load_dwordx4 v27,s[48:51],s83 offen offset:3072 lds
v_mfma_f32_16x16x32_bf16 v[184:187],v[120:123],a[172:175],v[184:187]
s_add_u32 m0,s86,86016
ds_read_b64_tr_b16 v[120:121],v2 offset:20512
ds_read_b64_tr_b16 v[122:123],v2 offset:25632
v_mfma_f32_16x16x32_bf16 v[184:187],v[116:119],a[156:159],v[184:187]
ds_read_b64_tr_b16 v[116:117],v2 offset:10272
v_mfma_f32_16x16x32_bf16 v[184:187],v[112:115],a[140:143],v[184:187]
ds_read_b64_tr_b16 v[112:113],v2 offset:32
v_mfma_f32_16x16x32_bf16 v[192:195],v[64:67],a[128:131],v[192:195]
ds_read_b64_tr_b16 v[114:115],v2 offset:5152
v_mfma_f32_16x16x32_bf16 v[192:195],v[68:71],a[144:147],v[192:195]
ds_read_b64_tr_b16 v[118:119],v2 offset:15392
ds_read_b64_tr_b16 v[124:125],v2 offset:30752
v_mfma_f32_16x16x32_bf16 v[192:195],v[72:75],a[160:163],v[192:195]
buffer_load_dwordx4 v7,s[48:51],s83 offen lds
v_mfma_f32_16x16x32_bf16 v[192:195],v[76:79],a[176:179],v[192:195]
s_add_u32 m0,s86,87040
s_add_u32 s83,s64,2
ds_read_b64_tr_b16 v[126:127],v2 offset:35872
v_mfma_f32_16x16x32_bf16 v[200:203],v[76:79],a[180:183],v[200:203]
s_and_b32 s83,s83,3
s_lshl_b32 s83,s83,1
s_or_b32 s83,s83,1
v_mfma_f32_16x16x32_bf16 v[200:203],v[72:75],a[164:167],v[200:203]
s_mul_i32 s83,s83,163840
s_add_u32 s83,s83,s85
v_mfma_f32_16x16x32_bf16 v[200:203],v[68:71],a[148:151],v[200:203]
v_mfma_f32_16x16x32_bf16 v[200:203],v[64:67],a[132:135],v[200:203]
v_mfma_f32_16x16x32_bf16 v[208:211],v[64:67],a[136:139],v[208:211]
buffer_load_dwordx4 v24,s[48:51],s84 offen lds
v_mfma_f32_16x16x32_bf16 v[208:211],v[68:71],a[152:155],v[208:211]
v_mfma_f32_16x16x32_bf16 v[208:211],v[72:75],a[168:171],v[208:211]
v_mfma_f32_16x16x32_bf16 v[208:211],v[76:79],a[184:187],v[208:211]
v_mfma_f32_16x16x32_bf16 v[216:219],v[76:79],a[188:191],v[216:219]
ds_read_b64_tr_b16 v[76:77],v1 offset:30784
ds_read_b64_tr_b16 v[78:79],v1 offset:35904
v_mfma_f32_16x16x32_bf16 v[216:219],v[72:75],a[172:175],v[216:219]
ds_read_b64_tr_b16 v[72:73],v1 offset:20544
v_mfma_f32_16x16x32_bf16 v[216:219],v[68:71],a[156:159],v[216:219]
buffer_load_dwordx2 v[4:5],v3,s[44:47],s91 offen offset:1280 sc1
v_mfma_f32_16x16x32_bf16 v[216:219],v[64:67],a[140:143],v[216:219]
s_waitcnt lgkmcnt(3)
ds_read_b64_tr_b16 v[64:65],v1 offset:64
ds_read_b64_tr_b16 v[66:67],v1 offset:5184
v_mfma_f32_16x16x32_bf16 v[224:227],v[80:83],a[128:131],v[224:227]
ds_read_b64_tr_b16 v[68:69],v1 offset:10304
v_mfma_f32_16x16x32_bf16 v[224:227],v[84:87],a[144:147],v[224:227]
ds_read_b64_tr_b16 v[70:71],v1 offset:15424
v_mfma_f32_16x16x32_bf16 v[224:227],v[88:91],a[160:163],v[224:227]
ds_read_b64_tr_b16 v[74:75],v1 offset:25664
v_mfma_f32_16x16x32_bf16 v[224:227],v[92:95],a[176:179],v[224:227]
v_mfma_f32_16x16x32_bf16 v[232:235],v[92:95],a[180:183],v[232:235]
buffer_load_dwordx4 v25,s[48:51],s84 offen offset:1024 lds
v_mfma_f32_16x16x32_bf16 v[232:235],v[88:91],a[164:167],v[232:235]
v_mfma_f32_16x16x32_bf16 v[232:235],v[84:87],a[148:151],v[232:235]
v_mfma_f32_16x16x32_bf16 v[232:235],v[80:83],a[132:135],v[232:235]
v_mfma_f32_16x16x32_bf16 v[240:243],v[80:83],a[136:139],v[240:243]
v_mfma_f32_16x16x32_bf16 v[240:243],v[84:87],a[152:155],v[240:243]
v_mfma_f32_16x16x32_bf16 v[240:243],v[88:91],a[168:171],v[240:243]
buffer_load_dwordx4 v26,s[48:51],s84 offen offset:2048 lds
v_mfma_f32_16x16x32_bf16 v[240:243],v[92:95],a[184:187],v[240:243]
v_mfma_f32_16x16x32_bf16 v[248:251],v[92:95],a[188:191],v[248:251]
ds_read_b64_tr_b16 v[92:93],v2 offset:30816
ds_read_b64_tr_b16 v[94:95],v2 offset:35936
v_mfma_f32_16x16x32_bf16 v[248:251],v[88:91],a[172:175],v[248:251]
ds_read_b64_tr_b16 v[88:89],v2 offset:20576
v_mfma_f32_16x16x32_bf16 v[248:251],v[84:87],a[156:159],v[248:251]
ds_read_b64_tr_b16 v[84:85],v2 offset:10336
v_mfma_f32_16x16x32_bf16 v[248:251],v[80:83],a[140:143],v[248:251]
s_waitcnt vmcnt(25)
ds_read_b64_tr_b16 v[80:81],v2 offset:96
v_mfma_f32_16x16x32_bf16 a[192:195],v[96:99],a[68:71],a[192:195]
buffer_load_dwordx4 v27,s[48:51],s84 offen offset:3072 lds
v_mfma_f32_16x16x32_bf16 a[192:195],v[100:103],a[76:79],a[192:195]
s_add_u32 m0,s86,91136
ds_read_b64_tr_b16 v[82:83],v2 offset:5216
ds_read_b64_tr_b16 v[86:87],v2 offset:15456
v_mfma_f32_16x16x32_bf16 a[192:195],v[104:107],a[84:87],a[192:195]
ds_read_b64_tr_b16 v[90:91],v2 offset:25696
v_mfma_f32_16x16x32_bf16 a[192:195],v[108:111],a[92:95],a[192:195]
v_mfma_f32_16x16x32_bf16 a[196:199],v[108:111],a[124:127],a[196:199]
v_mfma_f32_16x16x32_bf16 a[196:199],v[104:107],a[116:119],a[196:199]
v_mfma_f32_16x16x32_bf16 a[196:199],v[100:103],a[108:111],a[196:199]
buffer_load_dwordx4 v7,s[48:51],s84 offen lds
v_mfma_f32_16x16x32_bf16 a[196:199],v[96:99],a[100:103],a[196:199]
s_add_u32 m0,s86,40960
s_add_u32 s84,s83,20480
v_mfma_f32_16x16x32_bf16 a[200:203],v[96:99],a[64:67],a[200:203]
s_waitcnt vmcnt(25)
v_mfma_f32_16x16x32_bf16 a[200:203],v[100:103],a[72:75],a[200:203]
L_pd15_%=:
s_cbranch_execz L_trt1_%=
s_waitcnt vmcnt(22)
v_mfma_f32_16x16x32_bf16 a[200:203],v[104:107],a[80:83],a[200:203]
s_waitcnt vmcnt(18)
v_mfma_f32_16x16x32_bf16 a[200:203],v[108:111],a[88:91],a[200:203]
buffer_load_dwordx4 v[32:35],v6,s[28:31],s87 offen sc1
v_mfma_f32_16x16x32_bf16 a[204:207],v[108:111],a[120:123],a[204:207]
s_waitcnt vmcnt(17)
ds_read_b64_tr_b16 v[108:109],v1 offset:30848
ds_read_b64_tr_b16 v[110:111],v1 offset:35968
v_mfma_f32_16x16x32_bf16 a[204:207],v[104:107],a[112:115],a[204:207]
s_waitcnt vmcnt(16)
ds_read_b64_tr_b16 v[104:105],v1 offset:20608
v_mfma_f32_16x16x32_bf16 a[204:207],v[100:103],a[104:107],a[204:207]
s_waitcnt vmcnt(14)
ds_read_b64_tr_b16 v[100:101],v1 offset:10368
v_mfma_f32_16x16x32_bf16 a[204:207],v[96:99],a[96:99],a[204:207]
ds_read_b64_tr_b16 v[96:97],v1 offset:128
v_mfma_f32_16x16x32_bf16 a[208:211],v[112:115],a[68:71],a[208:211]
buffer_load_dwordx4 v[132:135],v6,s[28:31],s87 offen offset:1024 sc1
v_mfma_f32_16x16x32_bf16 a[208:211],v[116:119],a[76:79],a[208:211]
ds_read_b64_tr_b16 v[98:99],v1 offset:5248
ds_read_b64_tr_b16 v[102:103],v1 offset:15488
v_mfma_f32_16x16x32_bf16 a[208:211],v[120:123],a[84:87],a[208:211]
ds_read_b64_tr_b16 v[106:107],v1 offset:25728
v_mfma_f32_16x16x32_bf16 a[208:211],v[124:127],a[92:95],a[208:211]
v_mfma_f32_16x16x32_bf16 a[212:215],v[124:127],a[124:127],a[212:215]
v_mfma_f32_16x16x32_bf16 a[212:215],v[120:123],a[116:119],a[212:215]
buffer_load_dwordx4 v[196:199],v6,s[28:31],s87 offen offset:2048 sc1
v_mfma_f32_16x16x32_bf16 a[212:215],v[116:119],a[108:111],a[212:215]
v_mfma_f32_16x16x32_bf16 a[212:215],v[112:115],a[100:103],a[212:215]
v_mfma_f32_16x16x32_bf16 a[216:219],v[112:115],a[64:67],a[216:219]
v_mfma_f32_16x16x32_bf16 a[216:219],v[116:119],a[72:75],a[216:219]
v_mfma_f32_16x16x32_bf16 a[216:219],v[120:123],a[80:83],a[216:219]
buffer_load_dwordx4 v[36:39],v6,s[28:31],s88 offen sc1
v_mfma_f32_16x16x32_bf16 a[216:219],v[124:127],a[88:91],a[216:219]
v_mfma_f32_16x16x32_bf16 a[220:223],v[124:127],a[120:123],a[220:223]
ds_read_b64_tr_b16 v[124:125],v2 offset:30880
ds_read_b64_tr_b16 v[126:127],v2 offset:36000
v_mfma_f32_16x16x32_bf16 a[220:223],v[120:123],a[112:115],a[220:223]
ds_read_b64_tr_b16 v[120:121],v2 offset:20640
v_mfma_f32_16x16x32_bf16 a[220:223],v[116:119],a[104:107],a[220:223]
s_waitcnt lgkmcnt(3)
ds_read_b64_tr_b16 v[116:117],v2 offset:10400
v_mfma_f32_16x16x32_bf16 a[220:223],v[112:115],a[96:99],a[220:223]
buffer_load_dwordx4 v[140:143],v28,s[28:31],s87 offen offset:3072 sc1
v_mfma_f32_16x16x32_bf16 a[224:227],v[64:67],a[68:71],a[224:227]
s_add_u32 s87,s64,1
s_and_b32 s87,s87,3
s_lshl_b32 s87,s87,1
v_mfma_f32_16x16x32_bf16 a[224:227],v[68:71],a[76:79],a[224:227]
s_or_b32 s87,s87,1
s_lshl_b32 s87,s87,16
ds_read_b64_tr_b16 v[112:113],v2 offset:160
v_mfma_f32_16x16x32_bf16 a[224:227],v[72:75],a[84:87],a[224:227]
ds_read_b64_tr_b16 v[114:115],v2 offset:5280
ds_read_b64_tr_b16 v[118:119],v2 offset:15520
v_mfma_f32_16x16x32_bf16 a[224:227],v[76:79],a[92:95],a[224:227]
ds_read_b64_tr_b16 v[122:123],v2 offset:25760
v_mfma_f32_16x16x32_bf16 a[228:231],v[76:79],a[124:127],a[228:231]
buffer_load_dwordx4 v[212:215],v6,s[28:31],s88 offen offset:2048 sc1
v_mfma_f32_16x16x32_bf16 a[228:231],v[72:75],a[116:119],a[228:231]
v_mfma_f32_16x16x32_bf16 a[228:231],v[68:71],a[108:111],a[228:231]
v_mfma_f32_16x16x32_bf16 a[228:231],v[64:67],a[100:103],a[228:231]
v_mfma_f32_16x16x32_bf16 a[232:235],v[64:67],a[64:67],a[232:235]
v_mfma_f32_16x16x32_bf16 a[232:235],v[68:71],a[72:75],a[232:235]
buffer_load_dwordx4 v[148:151],v6,s[28:31],s88 offen offset:1024 sc1
v_mfma_f32_16x16x32_bf16 a[232:235],v[72:75],a[80:83],a[232:235]
s_waitcnt vmcnt(11)
v_cmp_ne_u32 s[56:57],s52,v4
v_cmp_ne_u32 s[58:59],s53,v5
v_mfma_f32_16x16x32_bf16 a[232:235],v[76:79],a[88:91],a[232:235]
s_or_b64 s[56:57],s[56:57],s[58:59]
s_cbranch_scc1 L_fr2_%=
L_fo2_%=:
v_mov_b32 v3,0
v_mfma_f32_16x16x32_bf16 a[236:239],v[76:79],a[120:123],a[236:239]
s_add_u32 s91,s64,3
ds_read_b64_tr_b16 v[76:77],v1 offset:30912
ds_read_b64_tr_b16 v[78:79],v1 offset:36032
)A"
R"A(v_mfma_f32_16x16x32_bf16 a[236:239],v[72:75],a[112:115],a[236:239]
s_and_b32 s91,s91,3
s_add_u32 s91,s91,s69
ds_read_b64_tr_b16 v[72:73],v1 offset:20672
v_mfma_f32_16x16x32_bf16 a[236:239],v[68:71],a[104:107],a[236:239]
buffer_load_dwordx4 v[40:43],v6,s[28:31],s89 offen sc1
v_mfma_f32_16x16x32_bf16 a[236:239],v[64:67],a[96:99],a[236:239]
s_add_u32 s91,s91,8
ds_read_b64_tr_b16 v[64:65],v1 offset:192
ds_read_b64_tr_b16 v[66:67],v1 offset:5312
v_mfma_f32_16x16x32_bf16 a[240:243],v[80:83],a[68:71],a[240:243]
s_lshl_b32 s91,s91,6
s_and_b32 s69,s63,s60
ds_read_b64_tr_b16 v[68:69],v1 offset:10432
v_mfma_f32_16x16x32_bf16 a[240:243],v[84:87],a[76:79],a[240:243]
s_cmp_eq_u32 s69,3
s_cselect_b32 s69,0x40000000,0
ds_read_b64_tr_b16 v[70:71],v1 offset:15552
v_mfma_f32_16x16x32_bf16 a[240:243],v[88:91],a[84:87],a[240:243]
s_add_u32 s68,s68,s69
ds_read_b64_tr_b16 v[74:75],v1 offset:25792
v_mfma_f32_16x16x32_bf16 a[240:243],v[92:95],a[92:95],a[240:243]
buffer_load_dwordx4 a[128:131],v6,s[28:31],s87 offen sc1
v_mfma_f32_16x16x32_bf16 a[244:247],v[92:95],a[124:127],a[244:247]
v_mfma_f32_16x16x32_bf16 a[244:247],v[88:91],a[116:119],a[244:247]
v_mfma_f32_16x16x32_bf16 a[244:247],v[84:87],a[108:111],a[244:247]
v_mfma_f32_16x16x32_bf16 a[244:247],v[80:83],a[100:103],a[244:247]
v_mfma_f32_16x16x32_bf16 a[248:251],v[80:83],a[64:67],a[248:251]
buffer_load_dwordx4 v[156:159],v28,s[28:31],s88 offen offset:3072 sc1
v_mfma_f32_16x16x32_bf16 a[248:251],v[84:87],a[72:75],a[248:251]
s_add_u32 s88,s87,4096
v_mfma_f32_16x16x32_bf16 a[248:251],v[88:91],a[80:83],a[248:251]
v_mfma_f32_16x16x32_bf16 a[248:251],v[92:95],a[88:91],a[248:251]
v_mfma_f32_16x16x32_bf16 a[252:255],v[92:95],a[120:123],a[252:255]
ds_read_b64_tr_b16 v[92:93],v2 offset:30944
ds_read_b64_tr_b16 v[94:95],v2 offset:36064
v_mfma_f32_16x16x32_bf16 a[252:255],v[88:91],a[112:115],a[252:255]
buffer_load_dwordx4 v[228:231],v6,s[28:31],s89 offen offset:2048 sc1
v_mfma_f32_16x16x32_bf16 a[252:255],v[84:87],a[104:107],a[252:255]
ds_read_b64_tr_b16 v[84:85],v2 offset:10464
ds_read_b64_tr_b16 v[86:87],v2 offset:15584
v_mfma_f32_16x16x32_bf16 a[252:255],v[80:83],a[96:99],a[252:255]
ds_read_b64_tr_b16 v[80:81],v2 offset:224
v_mfma_f32_16x16x32_bf16 a[0:3],v[96:99],a[68:71],a[0:3]
ds_read_b64_tr_b16 v[82:83],v2 offset:5344
v_mfma_f32_16x16x32_bf16 a[0:3],v[100:103],a[76:79],a[0:3]
ds_read_b64_tr_b16 v[88:89],v2 offset:20704
v_mfma_f32_16x16x32_bf16 a[0:3],v[104:107],a[84:87],a[0:3]
buffer_load_dwordx4 v[164:167],v6,s[28:31],s89 offen offset:1024 sc1
v_mfma_f32_16x16x32_bf16 a[0:3],v[108:111],a[92:95],a[0:3]
ds_read_b64_tr_b16 v[90:91],v2 offset:25824
v_mfma_f32_16x16x32_bf16 a[4:7],v[108:111],a[124:127],a[4:7]
v_mfma_f32_16x16x32_bf16 a[4:7],v[104:107],a[116:119],a[4:7]
v_mfma_f32_16x16x32_bf16 a[4:7],v[100:103],a[108:111],a[4:7]
v_mfma_f32_16x16x32_bf16 a[4:7],v[96:99],a[100:103],a[4:7]
buffer_load_dwordx4 v[44:47],v6,s[28:31],s90 offen sc1
v_mfma_f32_16x16x32_bf16 a[8:11],v[96:99],a[64:67],a[8:11]
v_mfma_f32_16x16x32_bf16 a[8:11],v[100:103],a[72:75],a[8:11]
v_mfma_f32_16x16x32_bf16 a[8:11],v[104:107],a[80:83],a[8:11]
v_mfma_f32_16x16x32_bf16 a[8:11],v[108:111],a[88:91],a[8:11]
v_mfma_f32_16x16x32_bf16 a[12:15],v[108:111],a[120:123],a[12:15]
buffer_load_dwordx4 a[144:147],v6,s[28:31],s88 offen sc1
v_mfma_f32_16x16x32_bf16 a[12:15],v[104:107],a[112:115],a[12:15]
ds_read_b64_tr_b16 v[104:105],v1 offset:20736
ds_read_b64_tr_b16 v[106:107],v1 offset:25856
v_mfma_f32_16x16x32_bf16 a[12:15],v[100:103],a[104:107],a[12:15]
ds_read_b64_tr_b16 v[100:101],v1 offset:10496
v_mfma_f32_16x16x32_bf16 a[12:15],v[96:99],a[96:99],a[12:15]
s_waitcnt lgkmcnt(3)
ds_read_b64_tr_b16 v[96:97],v1 offset:256
v_mfma_f32_16x16x32_bf16 a[16:19],v[112:115],a[68:71],a[16:19]
ds_read_b64_tr_b16 v[98:99],v1 offset:5376
v_mfma_f32_16x16x32_bf16 a[16:19],v[116:119],a[76:79],a[16:19]
buffer_load_dwordx4 v[172:175],v28,s[28:31],s89 offen offset:3072 sc1
v_mfma_f32_16x16x32_bf16 a[16:19],v[120:123],a[84:87],a[16:19]
s_add_u32 s89,s87,8192
ds_read_b64_tr_b16 v[102:103],v1 offset:15616
ds_read_b64_tr_b16 v[108:109],v1 offset:30976
v_mfma_f32_16x16x32_bf16 a[16:19],v[124:127],a[92:95],a[16:19]
ds_read_b64_tr_b16 v[110:111],v1 offset:36096
v_add_u32 v1,0x1e000,v1
v_mfma_f32_16x16x32_bf16 a[20:23],v[124:127],a[124:127],a[20:23]
v_mfma_f32_16x16x32_bf16 a[20:23],v[120:123],a[116:119],a[20:23]
v_mfma_f32_16x16x32_bf16 a[20:23],v[116:119],a[108:111],a[20:23]
buffer_load_dwordx4 v[180:183],v6,s[28:31],s90 offen offset:1024 sc1
v_mfma_f32_16x16x32_bf16 a[20:23],v[112:115],a[100:103],a[20:23]
v_mfma_f32_16x16x32_bf16 a[24:27],v[112:115],a[64:67],a[24:27]
v_mfma_f32_16x16x32_bf16 a[24:27],v[116:119],a[72:75],a[24:27]
v_mfma_f32_16x16x32_bf16 a[24:27],v[120:123],a[80:83],a[24:27]
v_mfma_f32_16x16x32_bf16 a[24:27],v[124:127],a[88:91],a[24:27]
buffer_load_dwordx4 a[160:163],v6,s[28:31],s89 offen sc1
v_mfma_f32_16x16x32_bf16 a[28:31],v[124:127],a[120:123],a[28:31]
ds_read_b64_tr_b16 v[124:125],v2 offset:31008
ds_read_b64_tr_b16 v[126:127],v2 offset:36128
v_mfma_f32_16x16x32_bf16 a[28:31],v[120:123],a[112:115],a[28:31]
ds_read_b64_tr_b16 v[120:121],v2 offset:20768
v_mfma_f32_16x16x32_bf16 a[28:31],v[116:119],a[104:107],a[28:31]
ds_read_b64_tr_b16 v[116:117],v2 offset:10528
v_mfma_f32_16x16x32_bf16 a[28:31],v[112:115],a[96:99],a[28:31]
ds_read_b64_tr_b16 v[112:113],v2 offset:288
v_mfma_f32_16x16x32_bf16 v[128:131],v[64:67],a[68:71],v[128:131]
buffer_load_dwordx4 v[244:247],v6,s[28:31],s90 offen offset:2048 sc1
v_mfma_f32_16x16x32_bf16 v[128:131],v[68:71],a[76:79],v[128:131]
ds_read_b64_tr_b16 v[114:115],v2 offset:5408
ds_read_b64_tr_b16 v[118:119],v2 offset:15648
v_mfma_f32_16x16x32_bf16 v[128:131],v[72:75],a[84:87],v[128:131]
ds_read_b64_tr_b16 v[122:123],v2 offset:25888
v_add_u32 v2,0x1e000,v2
v_mfma_f32_16x16x32_bf16 v[128:131],v[76:79],a[92:95],v[128:131]
v_mfma_f32_16x16x32_bf16 v[136:139],v[76:79],a[124:127],v[136:139]
v_mfma_f32_16x16x32_bf16 v[136:139],v[72:75],a[116:119],v[136:139]
buffer_load_dwordx4 v[188:191],v28,s[28:31],s90 offen offset:3072 sc1
v_mfma_f32_16x16x32_bf16 v[136:139],v[68:71],a[108:111],v[136:139]
s_add_u32 s90,s87,12288
v_mfma_f32_16x16x32_bf16 v[136:139],v[64:67],a[100:103],v[136:139]
v_mfma_f32_16x16x32_bf16 v[144:147],v[64:67],a[64:67],v[144:147]
v_mfma_f32_16x16x32_bf16 v[144:147],v[68:71],a[72:75],v[144:147]
v_mfma_f32_16x16x32_bf16 v[144:147],v[72:75],a[80:83],v[144:147]
buffer_load_dwordx4 a[176:179],v6,s[28:31],s90 offen sc1
v_mfma_f32_16x16x32_bf16 v[144:147],v[76:79],a[88:91],v[144:147]
v_mfma_f32_16x16x32_bf16 v[152:155],v[76:79],a[120:123],v[152:155]
v_mfma_f32_16x16x32_bf16 v[152:155],v[72:75],a[112:115],v[152:155]
v_mfma_f32_16x16x32_bf16 v[152:155],v[68:71],a[104:107],v[152:155]
v_mfma_f32_16x16x32_bf16 v[152:155],v[64:67],a[96:99],v[152:155]
buffer_load_dwordx4 a[180:183],v6,s[28:31],s90 offen offset:1024 sc1
s_waitcnt vmcnt(21) lgkmcnt(0)
s_barrier
v_mfma_f32_16x16x32_bf16 v[160:163],v[80:83],a[68:71],v[160:163]
ds_read_b64_tr_b16 v[64:65],v1
ds_read_b64_tr_b16 v[66:67],v1 offset:5120
v_mfma_f32_16x16x32_bf16 v[160:163],v[84:87],a[76:79],v[160:163]
ds_read_b64_tr_b16 v[68:69],v1 offset:10240
v_mfma_f32_16x16x32_bf16 v[160:163],v[88:91],a[84:87],v[160:163]
ds_read_b64_tr_b16 v[70:71],v1 offset:15360
v_mfma_f32_16x16x32_bf16 v[160:163],v[92:95],a[92:95],v[160:163]
buffer_load_dwordx4 a[164:167],v6,s[28:31],s89 offen offset:1024 sc1
v_mfma_f32_16x16x32_bf16 v[168:171],v[92:95],a[124:127],v[168:171]
ds_read_b64_tr_b16 v[72:73],v1 offset:20480
ds_read_b64_tr_b16 v[74:75],v1 offset:25600
v_mfma_f32_16x16x32_bf16 v[168:171],v[88:91],a[116:119],v[168:171]
ds_read_b64_tr_b16 v[76:77],v1 offset:30720
v_mfma_f32_16x16x32_bf16 v[168:171],v[84:87],a[108:111],v[168:171]
ds_read_b64_tr_b16 v[78:79],v1 offset:35840
v_mfma_f32_16x16x32_bf16 v[168:171],v[80:83],a[100:103],v[168:171]
v_mfma_f32_16x16x32_bf16 v[176:179],v[80:83],a[64:67],v[176:179]
v_mfma_f32_16x16x32_bf16 v[176:179],v[84:87],a[72:75],v[176:179]
buffer_load_dwordx4 v24,s[48:51],s83 offen lds
v_mfma_f32_16x16x32_bf16 v[176:179],v[88:91],a[80:83],v[176:179]
v_mfma_f32_16x16x32_bf16 v[176:179],v[92:95],a[88:91],v[176:179]
v_mfma_f32_16x16x32_bf16 v[184:187],v[92:95],a[120:123],v[184:187]
ds_read_b64_tr_b16 v[92:93],v2 offset:30752
ds_read_b64_tr_b16 v[94:95],v2 offset:35872
v_mfma_f32_16x16x32_bf16 v[184:187],v[88:91],a[112:115],v[184:187]
ds_read_b64_tr_b16 v[88:89],v2 offset:20512
v_mfma_f32_16x16x32_bf16 v[184:187],v[84:87],a[104:107],v[184:187]
buffer_load_dwordx4 a[148:151],v6,s[28:31],s88 offen offset:1024 sc1
v_mfma_f32_16x16x32_bf16 v[184:187],v[80:83],a[96:99],v[184:187]
ds_read_b64_tr_b16 v[80:81],v2 offset:32
ds_read_b64_tr_b16 v[82:83],v2 offset:5152
v_mfma_f32_16x16x32_bf16 v[192:195],v[96:99],a[68:71],v[192:195]
ds_read_b64_tr_b16 v[84:85],v2 offset:10272
v_mfma_f32_16x16x32_bf16 v[192:195],v[100:103],a[76:79],v[192:195]
ds_read_b64_tr_b16 v[86:87],v2 offset:15392
v_mfma_f32_16x16x32_bf16 v[192:195],v[104:107],a[84:87],v[192:195]
ds_read_b64_tr_b16 v[90:91],v2 offset:25632
v_mfma_f32_16x16x32_bf16 v[192:195],v[108:111],a[92:95],v[192:195]
v_mfma_f32_16x16x32_bf16 v[200:203],v[108:111],a[124:127],v[200:203]
buffer_load_dwordx4 v25,s[48:51],s83 offen offset:1024 lds
v_mfma_f32_16x16x32_bf16 v[200:203],v[104:107],a[116:119],v[200:203]
v_mfma_f32_16x16x32_bf16 v[200:203],v[100:103],a[108:111],v[200:203]
v_mfma_f32_16x16x32_bf16 v[200:203],v[96:99],a[100:103],v[200:203]
v_mfma_f32_16x16x32_bf16 v[208:211],v[96:99],a[64:67],v[208:211]
v_mfma_f32_16x16x32_bf16 v[208:211],v[100:103],a[72:75],v[208:211]
buffer_load_dwordx4 a[132:135],v6,s[28:31],s87 offen offset:1024 sc1
v_mfma_f32_16x16x32_bf16 v[208:211],v[104:107],a[80:83],v[208:211]
v_mfma_f32_16x16x32_bf16 v[208:211],v[108:111],a[88:91],v[208:211]
v_mfma_f32_16x16x32_bf16 v[216:219],v[108:111],a[120:123],v[216:219]
ds_read_b64_tr_b16 v[108:109],v1 offset:30784
ds_read_b64_tr_b16 v[110:111],v1 offset:35904
v_mfma_f32_16x16x32_bf16 v[216:219],v[104:107],a[112:115],v[216:219]
ds_read_b64_tr_b16 v[104:105],v1 offset:20544
v_mfma_f32_16x16x32_bf16 v[216:219],v[100:103],a[104:107],v[216:219]
ds_read_b64_tr_b16 v[100:101],v1 offset:10304
v_mfma_f32_16x16x32_bf16 v[216:219],v[96:99],a[96:99],v[216:219]
buffer_load_dwordx4 v26,s[48:51],s83 offen offset:2048 lds
v_mfma_f32_16x16x32_bf16 v[224:227],v[112:115],a[68:71],v[224:227]
ds_read_b64_tr_b16 v[96:97],v1 offset:64
ds_read_b64_tr_b16 v[98:99],v1 offset:5184
v_mfma_f32_16x16x32_bf16 v[224:227],v[116:119],a[76:79],v[224:227]
ds_read_b64_tr_b16 v[102:103],v1 offset:15424
v_mfma_f32_16x16x32_bf16 v[224:227],v[120:123],a[84:87],v[224:227]
ds_read_b64_tr_b16 v[106:107],v1 offset:25664
v_mfma_f32_16x16x32_bf16 v[224:227],v[124:127],a[92:95],v[224:227]
v_mfma_f32_16x16x32_bf16 v[232:235],v[124:127],a[124:127],v[232:235]
v_mfma_f32_16x16x32_bf16 v[232:235],v[120:123],a[116:119],v[232:235]
buffer_load_dwordx4 a[136:139],v6,s[28:31],s87 offen offset:2048 sc1
v_mfma_f32_16x16x32_bf16 v[232:235],v[116:119],a[108:111],v[232:235]
v_mfma_f32_16x16x32_bf16 v[232:235],v[112:115],a[100:103],v[232:235]
v_mfma_f32_16x16x32_bf16 v[240:243],v[112:115],a[64:67],v[240:243]
v_mfma_f32_16x16x32_bf16 v[240:243],v[116:119],a[72:75],v[240:243]
v_mfma_f32_16x16x32_bf16 v[240:243],v[120:123],a[80:83],v[240:243]
v_mfma_f32_16x16x32_bf16 v[240:243],v[124:127],a[88:91],v[240:243]
buffer_load_dwordx4 v27,s[48:51],s83 offen offset:3072 lds
v_mfma_f32_16x16x32_bf16 v[248:251],v[124:127],a[120:123],v[248:251]
s_add_u32 m0,s86,45056
ds_read_b64_tr_b16 v[124:125],v2 offset:30816
ds_read_b64_tr_b16 v[126:127],v2 offset:35936
v_mfma_f32_16x16x32_bf16 v[248:251],v[120:123],a[112:115],v[248:251]
ds_read_b64_tr_b16 v[120:121],v2 offset:20576
v_mfma_f32_16x16x32_bf16 v[248:251],v[116:119],a[104:107],v[248:251]
ds_read_b64_tr_b16 v[116:117],v2 offset:10336
v_mfma_f32_16x16x32_bf16 v[248:251],v[112:115],a[96:99],v[248:251]
s_waitcnt vmcnt(21) lgkmcnt(4)
ds_read_b64_tr_b16 v[112:113],v2 offset:96
v_mfma_f32_16x16x32_bf16 a[192:195],v[64:67],v[32:35],a[192:195]
s_waitcnt vmcnt(13)
)A"
R"A(ds_read_b64_tr_b16 v[114:115],v2 offset:5216
ds_read_b64_tr_b16 v[118:119],v2 offset:15456
v_mfma_f32_16x16x32_bf16 a[192:195],v[68:71],v[36:39],a[192:195]
buffer_load_dwordx4 v7,s[48:51],s83 offen lds
v_mfma_f32_16x16x32_bf16 a[192:195],v[72:75],v[40:43],a[192:195]
s_add_u32 m0,s86,46080
s_add_u32 s83,s64,3
ds_read_b64_tr_b16 v[122:123],v2 offset:25696
v_mfma_f32_16x16x32_bf16 a[192:195],v[76:79],v[44:47],a[192:195]
s_and_b32 s83,s83,3
s_lshl_b32 s83,s83,1
s_or_b32 s83,s83,17
v_mfma_f32_16x16x32_bf16 a[196:199],v[76:79],v[180:183],a[196:199]
s_mul_i32 s83,s83,163840
s_add_u32 s83,s83,s85
v_mfma_f32_16x16x32_bf16 a[196:199],v[72:75],v[164:167],a[196:199]
v_mfma_f32_16x16x32_bf16 a[196:199],v[68:71],v[148:151],a[196:199]
buffer_load_dwordx4 a[152:155],v6,s[28:31],s88 offen offset:2048 sc1
v_mfma_f32_16x16x32_bf16 a[196:199],v[64:67],v[132:135],a[196:199]
v_mfma_f32_16x16x32_bf16 a[200:203],v[64:67],v[196:199],a[200:203]
v_mfma_f32_16x16x32_bf16 a[200:203],v[68:71],v[212:215],a[200:203]
v_mfma_f32_16x16x32_bf16 a[200:203],v[72:75],v[228:231],a[200:203]
s_waitcnt vmcnt(13)
v_mfma_f32_16x16x32_bf16 a[200:203],v[76:79],v[244:247],a[200:203]
s_waitcnt vmcnt(12)
v_mfma_f32_16x16x32_bf16 a[204:207],v[76:79],v[188:191],a[204:207]
buffer_load_dwordx4 v24,s[48:51],s84 offen lds
v_mfma_f32_16x16x32_bf16 a[204:207],v[72:75],v[172:175],a[204:207]
ds_read_b64_tr_b16 v[72:73],v1 offset:20608
ds_read_b64_tr_b16 v[74:75],v1 offset:25728
v_mfma_f32_16x16x32_bf16 a[204:207],v[68:71],v[156:159],a[204:207]
ds_read_b64_tr_b16 v[68:69],v1 offset:10368
v_mfma_f32_16x16x32_bf16 a[204:207],v[64:67],v[140:143],a[204:207]
ds_read_b64_tr_b16 v[64:65],v1 offset:128
v_mfma_f32_16x16x32_bf16 a[208:211],v[80:83],v[32:35],a[208:211]
ds_read_b64_tr_b16 v[66:67],v1 offset:5248
v_mfma_f32_16x16x32_bf16 a[208:211],v[84:87],v[36:39],a[208:211]
buffer_load_dwordx4 a[168:171],v6,s[28:31],s89 offen offset:2048 sc1
v_mfma_f32_16x16x32_bf16 a[208:211],v[88:91],v[40:43],a[208:211]
ds_read_b64_tr_b16 v[70:71],v1 offset:15488
ds_read_b64_tr_b16 v[76:77],v1 offset:30848
v_mfma_f32_16x16x32_bf16 a[208:211],v[92:95],v[44:47],a[208:211]
ds_read_b64_tr_b16 v[78:79],v1 offset:35968
v_mfma_f32_16x16x32_bf16 a[212:215],v[92:95],v[180:183],a[212:215]
v_mfma_f32_16x16x32_bf16 a[212:215],v[88:91],v[164:167],a[212:215]
v_mfma_f32_16x16x32_bf16 a[212:215],v[84:87],v[148:151],a[212:215]
v_mfma_f32_16x16x32_bf16 a[212:215],v[80:83],v[132:135],a[212:215]
buffer_load_dwordx4 v25,s[48:51],s84 offen offset:1024 lds
v_mfma_f32_16x16x32_bf16 a[216:219],v[80:83],v[196:199],a[216:219]
v_mfma_f32_16x16x32_bf16 a[216:219],v[84:87],v[212:215],a[216:219]
v_mfma_f32_16x16x32_bf16 a[216:219],v[88:91],v[228:231],a[216:219]
v_mfma_f32_16x16x32_bf16 a[216:219],v[92:95],v[244:247],a[216:219]
v_mfma_f32_16x16x32_bf16 a[220:223],v[92:95],v[188:191],a[220:223]
buffer_load_dwordx4 a[184:187],v6,s[28:31],s90 offen offset:2048 sc1
v_mfma_f32_16x16x32_bf16 a[220:223],v[88:91],v[172:175],a[220:223]
ds_read_b64_tr_b16 v[88:89],v2 offset:20640
ds_read_b64_tr_b16 v[90:91],v2 offset:25760
v_mfma_f32_16x16x32_bf16 a[220:223],v[84:87],v[156:159],a[220:223]
ds_read_b64_tr_b16 v[84:85],v2 offset:10400
v_mfma_f32_16x16x32_bf16 a[220:223],v[80:83],v[140:143],a[220:223]
ds_read_b64_tr_b16 v[80:81],v2 offset:160
v_mfma_f32_16x16x32_bf16 a[224:227],v[96:99],v[32:35],a[224:227]
ds_read_b64_tr_b16 v[82:83],v2 offset:5280
v_mfma_f32_16x16x32_bf16 a[224:227],v[100:103],v[36:39],a[224:227]
ds_read_b64_tr_b16 v[86:87],v2 offset:15520
ds_read_b64_tr_b16 v[92:93],v2 offset:30880
v_mfma_f32_16x16x32_bf16 a[224:227],v[104:107],v[40:43],a[224:227]
buffer_load_dwordx4 v26,s[48:51],s84 offen offset:2048 lds
v_mfma_f32_16x16x32_bf16 a[224:227],v[108:111],v[44:47],a[224:227]
ds_read_b64_tr_b16 v[94:95],v2 offset:36000
v_mfma_f32_16x16x32_bf16 a[228:231],v[108:111],v[180:183],a[228:231]
v_mfma_f32_16x16x32_bf16 a[228:231],v[104:107],v[164:167],a[228:231]
v_mfma_f32_16x16x32_bf16 a[228:231],v[100:103],v[148:151],a[228:231]
v_mfma_f32_16x16x32_bf16 a[228:231],v[96:99],v[132:135],a[228:231]
v_mfma_f32_16x16x32_bf16 a[232:235],v[96:99],v[196:199],a[232:235]
buffer_load_dwordx4 v27,s[48:51],s84 offen offset:3072 lds
v_mfma_f32_16x16x32_bf16 a[232:235],v[100:103],v[212:215],a[232:235]
s_add_u32 m0,s86,50176
v_mfma_f32_16x16x32_bf16 a[232:235],v[104:107],v[228:231],a[232:235]
v_mfma_f32_16x16x32_bf16 a[232:235],v[108:111],v[244:247],a[232:235]
v_mfma_f32_16x16x32_bf16 a[236:239],v[108:111],v[188:191],a[236:239]
ds_read_b64_tr_b16 v[108:109],v1 offset:30912
ds_read_b64_tr_b16 v[110:111],v1 offset:36032
v_mfma_f32_16x16x32_bf16 a[236:239],v[104:107],v[172:175],a[236:239]
buffer_load_dwordx4 a[188:191],v28,s[28:31],s90 offen offset:3072 sc1
v_mfma_f32_16x16x32_bf16 a[236:239],v[100:103],v[156:159],a[236:239]
ds_read_b64_tr_b16 v[100:101],v1 offset:10432
ds_read_b64_tr_b16 v[102:103],v1 offset:15552
v_mfma_f32_16x16x32_bf16 a[236:239],v[96:99],v[140:143],a[236:239]
s_waitcnt lgkmcnt(4)
ds_read_b64_tr_b16 v[96:97],v1 offset:192
v_mfma_f32_16x16x32_bf16 a[240:243],v[112:115],v[32:35],a[240:243]
ds_read_b64_tr_b16 v[98:99],v1 offset:5312
v_mfma_f32_16x16x32_bf16 a[240:243],v[116:119],v[36:39],a[240:243]
ds_read_b64_tr_b16 v[104:105],v1 offset:20672
v_mfma_f32_16x16x32_bf16 a[240:243],v[120:123],v[40:43],a[240:243]
ds_read_b64_tr_b16 v[106:107],v1 offset:25792
v_mfma_f32_16x16x32_bf16 a[240:243],v[124:127],v[44:47],a[240:243]
buffer_load_dwordx4 v7,s[48:51],s84 offen lds
v_mfma_f32_16x16x32_bf16 a[244:247],v[124:127],v[180:183],a[244:247]
s_add_u32 m0,s86,0
s_add_u32 s84,s83,20480
v_mfma_f32_16x16x32_bf16 a[244:247],v[120:123],v[164:167],a[244:247]
v_mfma_f32_16x16x32_bf16 a[244:247],v[116:119],v[148:151],a[244:247]
v_mfma_f32_16x16x32_bf16 a[244:247],v[112:115],v[132:135],a[244:247]
v_mfma_f32_16x16x32_bf16 a[248:251],v[112:115],v[196:199],a[248:251]
buffer_load_dwordx4 a[172:175],v28,s[28:31],s89 offen offset:3072 sc1
v_mfma_f32_16x16x32_bf16 a[248:251],v[116:119],v[212:215],a[248:251]
v_mfma_f32_16x16x32_bf16 a[248:251],v[120:123],v[228:231],a[248:251]
v_mfma_f32_16x16x32_bf16 a[248:251],v[124:127],v[244:247],a[248:251]
v_mfma_f32_16x16x32_bf16 a[252:255],v[124:127],v[188:191],a[252:255]
ds_read_b64_tr_b16 v[124:125],v2 offset:30944
ds_read_b64_tr_b16 v[126:127],v2 offset:36064
v_mfma_f32_16x16x32_bf16 a[252:255],v[120:123],v[172:175],a[252:255]
ds_read_b64_tr_b16 v[120:121],v2 offset:20704
v_mfma_f32_16x16x32_bf16 a[252:255],v[116:119],v[156:159],a[252:255]
buffer_load_dwordx4 v24,s[48:51],s83 offen lds
v_mfma_f32_16x16x32_bf16 a[252:255],v[112:115],v[140:143],a[252:255]
ds_read_b64_tr_b16 v[112:113],v2 offset:224
ds_read_b64_tr_b16 v[114:115],v2 offset:5344
v_mfma_f32_16x16x32_bf16 a[0:3],v[64:67],v[32:35],a[0:3]
ds_read_b64_tr_b16 v[116:117],v2 offset:10464
v_mfma_f32_16x16x32_bf16 a[0:3],v[68:71],v[36:39],a[0:3]
ds_read_b64_tr_b16 v[118:119],v2 offset:15584
v_mfma_f32_16x16x32_bf16 a[0:3],v[72:75],v[40:43],a[0:3]
ds_read_b64_tr_b16 v[122:123],v2 offset:25824
v_mfma_f32_16x16x32_bf16 a[0:3],v[76:79],v[44:47],a[0:3]
buffer_load_dwordx4 a[156:159],v28,s[28:31],s88 offen offset:3072 sc1
v_mfma_f32_16x16x32_bf16 a[4:7],v[76:79],v[180:183],a[4:7]
v_mfma_f32_16x16x32_bf16 a[4:7],v[72:75],v[164:167],a[4:7]
v_mfma_f32_16x16x32_bf16 a[4:7],v[68:71],v[148:151],a[4:7]
v_mfma_f32_16x16x32_bf16 a[4:7],v[64:67],v[132:135],a[4:7]
v_mfma_f32_16x16x32_bf16 a[8:11],v[64:67],v[196:199],a[8:11]
v_mfma_f32_16x16x32_bf16 a[8:11],v[68:71],v[212:215],a[8:11]
buffer_load_dwordx4 v25,s[48:51],s83 offen offset:1024 lds
v_mfma_f32_16x16x32_bf16 a[8:11],v[72:75],v[228:231],a[8:11]
v_mfma_f32_16x16x32_bf16 a[8:11],v[76:79],v[244:247],a[8:11]
v_mfma_f32_16x16x32_bf16 a[12:15],v[76:79],v[188:191],a[12:15]
ds_read_b64_tr_b16 v[76:77],v1 offset:30976
ds_read_b64_tr_b16 v[78:79],v1 offset:36096
v_mfma_f32_16x16x32_bf16 a[12:15],v[72:75],v[172:175],a[12:15]
ds_read_b64_tr_b16 v[72:73],v1 offset:20736
v_mfma_f32_16x16x32_bf16 a[12:15],v[68:71],v[156:159],a[12:15]
ds_read_b64_tr_b16 v[68:69],v1 offset:10496
v_mfma_f32_16x16x32_bf16 a[12:15],v[64:67],v[140:143],a[12:15]
buffer_load_dwordx4 v26,s[48:51],s83 offen offset:2048 lds
v_mfma_f32_16x16x32_bf16 a[16:19],v[80:83],v[32:35],a[16:19]
ds_read_b64_tr_b16 v[64:65],v1 offset:256
ds_read_b64_tr_b16 v[66:67],v1 offset:5376
v_mfma_f32_16x16x32_bf16 a[16:19],v[84:87],v[36:39],a[16:19]
ds_read_b64_tr_b16 v[70:71],v1 offset:15616
v_mfma_f32_16x16x32_bf16 a[16:19],v[88:91],v[40:43],a[16:19]
ds_read_b64_tr_b16 v[74:75],v1 offset:25856
v_add_u32 v1,0xffff6000,v1
v_mfma_f32_16x16x32_bf16 a[16:19],v[92:95],v[44:47],a[16:19]
v_mfma_f32_16x16x32_bf16 a[20:23],v[92:95],v[180:183],a[20:23]
buffer_load_dwordx4 a[140:143],v28,s[28:31],s87 offen offset:3072 sc1
v_mfma_f32_16x16x32_bf16 a[20:23],v[88:91],v[164:167],a[20:23]
s_add_u32 s87,s64,2
s_and_b32 s87,s87,3
s_lshl_b32 s87,s87,1
v_mfma_f32_16x16x32_bf16 a[20:23],v[84:87],v[148:151],a[20:23]
s_or_b32 s87,s87,1
s_lshl_b32 s87,s87,16
s_add_u32 s88,s87,4096
v_mfma_f32_16x16x32_bf16 a[20:23],v[80:83],v[132:135],a[20:23]
s_add_u32 s89,s87,8192
s_add_u32 s90,s87,12288
v_mfma_f32_16x16x32_bf16 a[24:27],v[80:83],v[196:199],a[24:27]
v_mfma_f32_16x16x32_bf16 a[24:27],v[84:87],v[212:215],a[24:27]
v_mfma_f32_16x16x32_bf16 a[24:27],v[88:91],v[228:231],a[24:27]
buffer_load_dwordx4 v27,s[48:51],s83 offen offset:3072 lds
v_mfma_f32_16x16x32_bf16 a[24:27],v[92:95],v[244:247],a[24:27]
s_add_u32 m0,s86,4096
v_mfma_f32_16x16x32_bf16 a[28:31],v[92:95],v[188:191],a[28:31]
ds_read_b64_tr_b16 v[92:93],v2 offset:31008
ds_read_b64_tr_b16 v[94:95],v2 offset:36128
v_mfma_f32_16x16x32_bf16 a[28:31],v[88:91],v[172:175],a[28:31]
ds_read_b64_tr_b16 v[88:89],v2 offset:20768
v_mfma_f32_16x16x32_bf16 a[28:31],v[84:87],v[156:159],a[28:31]
ds_read_b64_tr_b16 v[84:85],v2 offset:10528
v_mfma_f32_16x16x32_bf16 a[28:31],v[80:83],v[140:143],a[28:31]
s_waitcnt lgkmcnt(4)
ds_read_b64_tr_b16 v[80:81],v2 offset:288
v_mfma_f32_16x16x32_bf16 v[128:131],v[96:99],v[32:35],v[128:131]
buffer_load_dwordx4 v7,s[48:51],s83 offen lds
v_mfma_f32_16x16x32_bf16 v[128:131],v[100:103],v[36:39],v[128:131]
s_add_u32 m0,s86,5120
ds_read_b64_tr_b16 v[82:83],v2 offset:5408
ds_read_b64_tr_b16 v[86:87],v2 offset:15648
v_mfma_f32_16x16x32_bf16 v[128:131],v[104:107],v[40:43],v[128:131]
s_add_u32 s83,s64,3
ds_read_b64_tr_b16 v[90:91],v2 offset:25888
v_add_u32 v2,0xffff6000,v2
v_mfma_f32_16x16x32_bf16 v[128:131],v[108:111],v[44:47],v[128:131]
s_and_b32 s83,s83,3
s_lshl_b32 s83,s83,1
s_or_b32 s83,s83,1
v_mfma_f32_16x16x32_bf16 v[136:139],v[108:111],v[180:183],v[136:139]
s_mul_i32 s83,s83,163840
s_add_u32 s83,s83,s85
v_mfma_f32_16x16x32_bf16 v[136:139],v[104:107],v[164:167],v[136:139]
v_mfma_f32_16x16x32_bf16 v[136:139],v[100:103],v[148:151],v[136:139]
buffer_load_dwordx4 v24,s[48:51],s84 offen lds
v_mfma_f32_16x16x32_bf16 v[136:139],v[96:99],v[132:135],v[136:139]
v_mfma_f32_16x16x32_bf16 v[144:147],v[96:99],v[196:199],v[144:147]
v_mfma_f32_16x16x32_bf16 v[144:147],v[100:103],v[212:215],v[144:147]
v_mfma_f32_16x16x32_bf16 v[144:147],v[104:107],v[228:231],v[144:147]
v_mfma_f32_16x16x32_bf16 v[144:147],v[108:111],v[244:247],v[144:147]
v_mfma_f32_16x16x32_bf16 v[152:155],v[108:111],v[188:191],v[152:155]
buffer_load_dwordx4 v25,s[48:51],s84 offen offset:1024 lds
v_mfma_f32_16x16x32_bf16 v[152:155],v[104:107],v[172:175],v[152:155]
ds_read_b64_tr_b16 v[104:105],v1 offset:20480
ds_read_b64_tr_b16 v[106:107],v1 offset:25600
v_mfma_f32_16x16x32_bf16 v[152:155],v[100:103],v[156:159],v[152:155]
ds_read_b64_tr_b16 v[100:101],v1 offset:10240
v_mfma_f32_16x16x32_bf16 v[152:155],v[96:99],v[140:143],v[152:155]
ds_read_b64_tr_b16 v[96:97],v1
v_mfma_f32_16x16x32_bf16 v[160:163],v[112:115],v[32:35],v[160:163]
ds_read_b64_tr_b16 v[98:99],v1 offset:5120
v_mfma_f32_16x16x32_bf16 v[160:163],v[116:119],v[36:39],v[160:163]
ds_read_b64_tr_b16 v[102:103],v1 offset:15360
ds_read_b64_tr_b16 v[108:109],v1 offset:30720
v_mfma_f32_16x16x32_bf16 v[160:163],v[120:123],v[40:43],v[160:163]
buffer_load_dwordx4 v26,s[48:51],s84 offen offset:2048 lds
v_mfma_f32_16x16x32_bf16 v[160:163],v[124:127],v[44:47],v[160:163]
)A"
R"A(ds_read_b64_tr_b16 v[110:111],v1 offset:35840
v_mfma_f32_16x16x32_bf16 v[168:171],v[124:127],v[180:183],v[168:171]
v_mfma_f32_16x16x32_bf16 v[168:171],v[120:123],v[164:167],v[168:171]
v_mfma_f32_16x16x32_bf16 v[168:171],v[116:119],v[148:151],v[168:171]
v_mfma_f32_16x16x32_bf16 v[168:171],v[112:115],v[132:135],v[168:171]
v_mfma_f32_16x16x32_bf16 v[176:179],v[112:115],v[196:199],v[176:179]
buffer_load_dwordx4 v27,s[48:51],s84 offen offset:3072 lds
v_mfma_f32_16x16x32_bf16 v[176:179],v[116:119],v[212:215],v[176:179]
s_add_u32 m0,s86,9216
v_mfma_f32_16x16x32_bf16 v[176:179],v[120:123],v[228:231],v[176:179]
v_mfma_f32_16x16x32_bf16 v[176:179],v[124:127],v[244:247],v[176:179]
v_mfma_f32_16x16x32_bf16 v[184:187],v[124:127],v[188:191],v[184:187]
ds_read_b64_tr_b16 v[124:125],v2 offset:30752
ds_read_b64_tr_b16 v[126:127],v2 offset:35872
v_mfma_f32_16x16x32_bf16 v[184:187],v[120:123],v[172:175],v[184:187]
ds_read_b64_tr_b16 v[120:121],v2 offset:20512
L_pd16_%=:
s_cbranch_execz L_trtg2F_%=
v_mfma_f32_16x16x32_bf16 v[184:187],v[116:119],v[156:159],v[184:187]
buffer_load_dwordx4 v7,s[48:51],s84 offen lds
v_mfma_f32_16x16x32_bf16 v[184:187],v[112:115],v[140:143],v[184:187]
s_add_u32 m0,s86,122880
ds_read_b64_tr_b16 v[112:113],v2 offset:32
ds_read_b64_tr_b16 v[114:115],v2 offset:5152
v_mfma_f32_16x16x32_bf16 v[192:195],v[64:67],v[32:35],v[192:195]
s_add_u32 s84,s83,20480
ds_read_b64_tr_b16 v[116:117],v2 offset:10272
v_mfma_f32_16x16x32_bf16 v[192:195],v[68:71],v[36:39],v[192:195]
ds_read_b64_tr_b16 v[118:119],v2 offset:15392
v_mfma_f32_16x16x32_bf16 v[192:195],v[72:75],v[40:43],v[192:195]
ds_read_b64_tr_b16 v[122:123],v2 offset:25632
v_mfma_f32_16x16x32_bf16 v[192:195],v[76:79],v[44:47],v[192:195]
buffer_load_dwordx4 a[68:71],v6,s[28:31],s87 offen sc1
v_mfma_f32_16x16x32_bf16 v[200:203],v[76:79],v[180:183],v[200:203]
v_mfma_f32_16x16x32_bf16 v[200:203],v[72:75],v[164:167],v[200:203]
v_mfma_f32_16x16x32_bf16 v[200:203],v[68:71],v[148:151],v[200:203]
v_mfma_f32_16x16x32_bf16 v[200:203],v[64:67],v[132:135],v[200:203]
v_mfma_f32_16x16x32_bf16 v[208:211],v[64:67],v[196:199],v[208:211]
buffer_load_dwordx4 a[100:103],v6,s[28:31],s87 offen offset:1024 sc1
v_mfma_f32_16x16x32_bf16 v[208:211],v[68:71],v[212:215],v[208:211]
v_mfma_f32_16x16x32_bf16 v[208:211],v[72:75],v[228:231],v[208:211]
v_mfma_f32_16x16x32_bf16 v[208:211],v[76:79],v[244:247],v[208:211]
v_mfma_f32_16x16x32_bf16 v[216:219],v[76:79],v[188:191],v[216:219]
ds_read_b64_tr_b16 v[76:77],v1 offset:30784
ds_read_b64_tr_b16 v[78:79],v1 offset:35904
v_mfma_f32_16x16x32_bf16 v[216:219],v[72:75],v[172:175],v[216:219]
buffer_load_dwordx2 v[4:5],v3,s[44:47],s91 offen offset:1280 sc1
v_mfma_f32_16x16x32_bf16 v[216:219],v[68:71],v[156:159],v[216:219]
ds_read_b64_tr_b16 v[68:69],v1 offset:10304
ds_read_b64_tr_b16 v[70:71],v1 offset:15424
v_mfma_f32_16x16x32_bf16 v[216:219],v[64:67],v[140:143],v[216:219]
s_waitcnt lgkmcnt(4)
ds_read_b64_tr_b16 v[64:65],v1 offset:64
v_mfma_f32_16x16x32_bf16 v[224:227],v[80:83],v[32:35],v[224:227]
ds_read_b64_tr_b16 v[66:67],v1 offset:5184
v_mfma_f32_16x16x32_bf16 v[224:227],v[84:87],v[36:39],v[224:227]
ds_read_b64_tr_b16 v[72:73],v1 offset:20544
v_mfma_f32_16x16x32_bf16 v[224:227],v[88:91],v[40:43],v[224:227]
buffer_load_dwordx4 a[64:67],v6,s[28:31],s87 offen offset:2048 sc1
v_mfma_f32_16x16x32_bf16 v[224:227],v[92:95],v[44:47],v[224:227]
ds_read_b64_tr_b16 v[74:75],v1 offset:25664
v_mfma_f32_16x16x32_bf16 v[232:235],v[92:95],v[180:183],v[232:235]
v_mfma_f32_16x16x32_bf16 v[232:235],v[88:91],v[164:167],v[232:235]
v_mfma_f32_16x16x32_bf16 v[232:235],v[84:87],v[148:151],v[232:235]
v_mfma_f32_16x16x32_bf16 v[232:235],v[80:83],v[132:135],v[232:235]
buffer_load_dwordx4 a[76:79],v6,s[28:31],s88 offen sc1
v_mfma_f32_16x16x32_bf16 v[240:243],v[80:83],v[196:199],v[240:243]
v_mfma_f32_16x16x32_bf16 v[240:243],v[84:87],v[212:215],v[240:243]
v_mfma_f32_16x16x32_bf16 v[240:243],v[88:91],v[228:231],v[240:243]
v_mfma_f32_16x16x32_bf16 v[240:243],v[92:95],v[244:247],v[240:243]
v_mfma_f32_16x16x32_bf16 v[248:251],v[92:95],v[188:191],v[248:251]
buffer_load_dwordx4 a[96:99],v28,s[28:31],s87 offen offset:3072 sc1
v_mfma_f32_16x16x32_bf16 v[248:251],v[88:91],v[172:175],v[248:251]
s_add_u32 s87,s64,3
ds_read_b64_tr_b16 v[88:89],v2 offset:20576
ds_read_b64_tr_b16 v[90:91],v2 offset:25696
v_mfma_f32_16x16x32_bf16 v[248:251],v[84:87],v[156:159],v[248:251]
s_and_b32 s87,s87,3
s_lshl_b32 s87,s87,1
ds_read_b64_tr_b16 v[84:85],v2 offset:10336
v_mfma_f32_16x16x32_bf16 v[248:251],v[80:83],v[140:143],v[248:251]
s_or_b32 s87,s87,17
s_lshl_b32 s87,s87,16
ds_read_b64_tr_b16 v[80:81],v2 offset:96
v_mfma_f32_16x16x32_bf16 a[192:195],v[96:99],a[128:131],a[192:195]
ds_read_b64_tr_b16 v[82:83],v2 offset:5216
v_mfma_f32_16x16x32_bf16 a[192:195],v[100:103],a[144:147],a[192:195]
buffer_load_dwordx4 a[72:75],v6,s[28:31],s88 offen offset:2048 sc1
v_mfma_f32_16x16x32_bf16 a[192:195],v[104:107],a[160:163],a[192:195]
s_waitcnt vmcnt(37)
ds_read_b64_tr_b16 v[86:87],v2 offset:15456
ds_read_b64_tr_b16 v[92:93],v2 offset:30816
v_mfma_f32_16x16x32_bf16 a[192:195],v[108:111],a[176:179],a[192:195]
ds_read_b64_tr_b16 v[94:95],v2 offset:35936
v_mfma_f32_16x16x32_bf16 a[196:199],v[108:111],a[180:183],a[196:199]
v_mfma_f32_16x16x32_bf16 a[196:199],v[104:107],a[164:167],a[196:199]
s_waitcnt vmcnt(33)
v_mfma_f32_16x16x32_bf16 a[196:199],v[100:103],a[148:151],a[196:199]
buffer_load_dwordx4 a[108:111],v6,s[28:31],s88 offen offset:1024 sc1
v_mfma_f32_16x16x32_bf16 a[196:199],v[96:99],a[132:135],a[196:199]
s_waitcnt vmcnt(32)
v_mfma_f32_16x16x32_bf16 a[200:203],v[96:99],a[136:139],a[200:203]
s_waitcnt vmcnt(29)
v_mfma_f32_16x16x32_bf16 a[200:203],v[100:103],a[152:155],a[200:203]
s_waitcnt vmcnt(27)
v_mfma_f32_16x16x32_bf16 a[200:203],v[104:107],a[168:171],a[200:203]
s_waitcnt vmcnt(22)
v_mfma_f32_16x16x32_bf16 a[200:203],v[108:111],a[184:187],a[200:203]
buffer_load_dwordx4 a[84:87],v6,s[28:31],s89 offen sc1
v_mfma_f32_16x16x32_bf16 a[204:207],v[108:111],a[188:191],a[204:207]
s_waitcnt vmcnt(21)
ds_read_b64_tr_b16 v[108:109],v1 offset:30848
ds_read_b64_tr_b16 v[110:111],v1 offset:35968
v_mfma_f32_16x16x32_bf16 a[204:207],v[104:107],a[172:175],a[204:207]
s_waitcnt vmcnt(19)
ds_read_b64_tr_b16 v[104:105],v1 offset:20608
v_mfma_f32_16x16x32_bf16 a[204:207],v[100:103],a[156:159],a[204:207]
s_waitcnt vmcnt(16)
ds_read_b64_tr_b16 v[100:101],v1 offset:10368
v_mfma_f32_16x16x32_bf16 a[204:207],v[96:99],a[140:143],a[204:207]
ds_read_b64_tr_b16 v[96:97],v1 offset:128
v_mfma_f32_16x16x32_bf16 a[208:211],v[112:115],a[128:131],a[208:211]
buffer_load_dwordx4 a[104:107],v28,s[28:31],s88 offen offset:3072 sc1
v_mfma_f32_16x16x32_bf16 a[208:211],v[116:119],a[144:147],a[208:211]
s_add_u32 s88,s87,4096
ds_read_b64_tr_b16 v[98:99],v1 offset:5248
ds_read_b64_tr_b16 v[102:103],v1 offset:15488
v_mfma_f32_16x16x32_bf16 a[208:211],v[120:123],a[160:163],a[208:211]
ds_read_b64_tr_b16 v[106:107],v1 offset:25728
v_mfma_f32_16x16x32_bf16 a[208:211],v[124:127],a[176:179],a[208:211]
v_mfma_f32_16x16x32_bf16 a[212:215],v[124:127],a[180:183],a[212:215]
v_mfma_f32_16x16x32_bf16 a[212:215],v[120:123],a[164:167],a[212:215]
buffer_load_dwordx4 a[80:83],v6,s[28:31],s89 offen offset:2048 sc1
v_mfma_f32_16x16x32_bf16 a[212:215],v[116:119],a[148:151],a[212:215]
v_mfma_f32_16x16x32_bf16 a[212:215],v[112:115],a[132:135],a[212:215]
v_mfma_f32_16x16x32_bf16 a[216:219],v[112:115],a[136:139],a[216:219]
v_mfma_f32_16x16x32_bf16 a[216:219],v[116:119],a[152:155],a[216:219]
v_mfma_f32_16x16x32_bf16 a[216:219],v[120:123],a[168:171],a[216:219]
buffer_load_dwordx4 a[116:119],v6,s[28:31],s89 offen offset:1024 sc1
v_mfma_f32_16x16x32_bf16 a[216:219],v[124:127],a[184:187],a[216:219]
v_mfma_f32_16x16x32_bf16 a[220:223],v[124:127],a[188:191],a[220:223]
ds_read_b64_tr_b16 v[124:125],v2 offset:30880
ds_read_b64_tr_b16 v[126:127],v2 offset:36000
v_mfma_f32_16x16x32_bf16 a[220:223],v[120:123],a[172:175],a[220:223]
ds_read_b64_tr_b16 v[120:121],v2 offset:20640
v_mfma_f32_16x16x32_bf16 a[220:223],v[116:119],a[156:159],a[220:223]
s_waitcnt lgkmcnt(3)
ds_read_b64_tr_b16 v[116:117],v2 offset:10400
v_mfma_f32_16x16x32_bf16 a[220:223],v[112:115],a[140:143],a[220:223]
buffer_load_dwordx4 a[92:95],v6,s[28:31],s90 offen sc1
v_mfma_f32_16x16x32_bf16 a[224:227],v[64:67],a[128:131],a[224:227]
ds_read_b64_tr_b16 v[112:113],v2 offset:160
ds_read_b64_tr_b16 v[114:115],v2 offset:5280
v_mfma_f32_16x16x32_bf16 a[224:227],v[68:71],a[144:147],a[224:227]
ds_read_b64_tr_b16 v[118:119],v2 offset:15520
v_mfma_f32_16x16x32_bf16 a[224:227],v[72:75],a[160:163],a[224:227]
ds_read_b64_tr_b16 v[122:123],v2 offset:25760
v_mfma_f32_16x16x32_bf16 a[224:227],v[76:79],a[176:179],a[224:227]
v_mfma_f32_16x16x32_bf16 a[228:231],v[76:79],a[180:183],a[228:231]
buffer_load_dwordx4 a[112:115],v28,s[28:31],s89 offen offset:3072 sc1
v_mfma_f32_16x16x32_bf16 a[228:231],v[72:75],a[164:167],a[228:231]
s_add_u32 s89,s87,8192
v_mfma_f32_16x16x32_bf16 a[228:231],v[68:71],a[148:151],a[228:231]
v_mfma_f32_16x16x32_bf16 a[228:231],v[64:67],a[132:135],a[228:231]
v_mfma_f32_16x16x32_bf16 a[232:235],v[64:67],a[136:139],a[232:235]
v_mfma_f32_16x16x32_bf16 a[232:235],v[68:71],a[152:155],a[232:235]
buffer_load_dwordx4 a[124:127],v6,s[28:31],s90 offen offset:1024 sc1
v_mfma_f32_16x16x32_bf16 a[232:235],v[72:75],a[168:171],a[232:235]
s_waitcnt vmcnt(12)
v_cmp_ne_u32 s[56:57],s52,v4
v_cmp_ne_u32 s[58:59],s53,v5
v_mfma_f32_16x16x32_bf16 a[232:235],v[76:79],a[184:187],a[232:235]
s_or_b64 s[56:57],s[56:57],s[58:59]
s_cbranch_scc1 L_fr3_%=
L_fo3_%=:
s_lshl_b32 s91,s64,6
v_mfma_f32_16x16x32_bf16 a[236:239],v[76:79],a[188:191],a[236:239]
ds_read_b64_tr_b16 v[76:77],v1 offset:30912
ds_read_b64_tr_b16 v[78:79],v1 offset:36032
v_mfma_f32_16x16x32_bf16 a[236:239],v[72:75],a[172:175],a[236:239]
ds_read_b64_tr_b16 v[72:73],v1 offset:20672
v_bfe_u32 v3,v6,4,2
v_mfma_f32_16x16x32_bf16 a[236:239],v[68:71],a[156:159],a[236:239]
buffer_load_dwordx4 v[32:35],v6,s[28:31],s87 offen sc1
v_mfma_f32_16x16x32_bf16 a[236:239],v[64:67],a[140:143],a[236:239]
ds_read_b64_tr_b16 v[64:65],v1 offset:192
ds_read_b64_tr_b16 v[66:67],v1 offset:5312
v_mfma_f32_16x16x32_bf16 a[240:243],v[80:83],a[128:131],a[240:243]
ds_read_b64_tr_b16 v[68:69],v1 offset:10432
v_min_u32 v3,2,v3
v_mfma_f32_16x16x32_bf16 a[240:243],v[84:87],a[144:147],a[240:243]
ds_read_b64_tr_b16 v[70:71],v1 offset:15552
v_add3_u32 v3,v3,s63,1
v_mfma_f32_16x16x32_bf16 a[240:243],v[88:91],a[160:163],a[240:243]
ds_read_b64_tr_b16 v[74:75],v1 offset:25792
v_and_b32 v3,3,v3
v_mfma_f32_16x16x32_bf16 a[240:243],v[92:95],a[176:179],a[240:243]
buffer_load_dwordx4 a[88:91],v6,s[28:31],s90 offen offset:2048 sc1
v_mfma_f32_16x16x32_bf16 a[244:247],v[92:95],a[180:183],a[244:247]
v_lshlrev_b32 v3,8,v3
v_mfma_f32_16x16x32_bf16 a[244:247],v[88:91],a[164:167],a[244:247]
v_mfma_f32_16x16x32_bf16 a[244:247],v[84:87],a[148:151],a[244:247]
v_mfma_f32_16x16x32_bf16 a[244:247],v[80:83],a[132:135],a[244:247]
v_mfma_f32_16x16x32_bf16 a[248:251],v[80:83],a[136:139],a[248:251]
buffer_load_dwordx4 v[36:39],v6,s[28:31],s88 offen sc1
v_mfma_f32_16x16x32_bf16 a[248:251],v[84:87],a[152:155],a[248:251]
v_mfma_f32_16x16x32_bf16 a[248:251],v[88:91],a[168:171],a[248:251]
v_mfma_f32_16x16x32_bf16 a[248:251],v[92:95],a[184:187],a[248:251]
v_mfma_f32_16x16x32_bf16 a[252:255],v[92:95],a[188:191],a[252:255]
ds_read_b64_tr_b16 v[92:93],v2 offset:30944
ds_read_b64_tr_b16 v[94:95],v2 offset:36064
v_mfma_f32_16x16x32_bf16 a[252:255],v[88:91],a[172:175],a[252:255]
buffer_load_dwordx4 a[120:123],v28,s[28:31],s90 offen offset:3072 sc1
v_mfma_f32_16x16x32_bf16 a[252:255],v[84:87],a[156:159],a[252:255]
s_add_u32 s90,s87,12288
ds_read_b64_tr_b16 v[84:85],v2 offset:10464
ds_read_b64_tr_b16 v[86:87],v2 offset:15584
v_mfma_f32_16x16x32_bf16 a[252:255],v[80:83],a[140:143],a[252:255]
ds_read_b64_tr_b16 v[80:81],v2 offset:224
v_mfma_f32_16x16x32_bf16 a[0:3],v[96:99],a[128:131],a[0:3]
ds_read_b64_tr_b16 v[82:83],v2 offset:5344
v_mfma_f32_16x16x32_bf16 a[0:3],v[100:103],a[144:147],a[0:3]
ds_read_b64_tr_b16 v[88:89],v2 offset:20704
)A"
R"A(v_mfma_f32_16x16x32_bf16 a[0:3],v[104:107],a[160:163],a[0:3]
buffer_load_dwordx4 v[40:43],v6,s[28:31],s89 offen sc1
v_mfma_f32_16x16x32_bf16 a[0:3],v[108:111],a[176:179],a[0:3]
ds_read_b64_tr_b16 v[90:91],v2 offset:25824
v_mfma_f32_16x16x32_bf16 a[4:7],v[108:111],a[180:183],a[4:7]
v_mfma_f32_16x16x32_bf16 a[4:7],v[104:107],a[164:167],a[4:7]
v_mfma_f32_16x16x32_bf16 a[4:7],v[100:103],a[148:151],a[4:7]
v_mfma_f32_16x16x32_bf16 a[4:7],v[96:99],a[132:135],a[4:7]
v_mfma_f32_16x16x32_bf16 a[8:11],v[96:99],a[136:139],a[8:11]
v_mfma_f32_16x16x32_bf16 a[8:11],v[100:103],a[152:155],a[8:11]
v_mfma_f32_16x16x32_bf16 a[8:11],v[104:107],a[168:171],a[8:11]
v_mfma_f32_16x16x32_bf16 a[8:11],v[108:111],a[184:187],a[8:11]
v_mfma_f32_16x16x32_bf16 a[12:15],v[108:111],a[188:191],a[12:15]
ds_read_b64_tr_b16 v[108:109],v1 offset:30976
ds_read_b64_tr_b16 v[110:111],v1 offset:36096
v_mfma_f32_16x16x32_bf16 a[12:15],v[104:107],a[172:175],a[12:15]
buffer_load_dwordx4 v[44:47],v6,s[28:31],s90 offen sc1
v_mfma_f32_16x16x32_bf16 a[12:15],v[100:103],a[156:159],a[12:15]
ds_read_b64_tr_b16 v[100:101],v1 offset:10496
ds_read_b64_tr_b16 v[102:103],v1 offset:15616
v_mfma_f32_16x16x32_bf16 a[12:15],v[96:99],a[140:143],a[12:15]
s_waitcnt lgkmcnt(4)
ds_read_b64_tr_b16 v[96:97],v1 offset:256
v_mfma_f32_16x16x32_bf16 a[16:19],v[112:115],a[128:131],a[16:19]
ds_read_b64_tr_b16 v[98:99],v1 offset:5376
v_mfma_f32_16x16x32_bf16 a[16:19],v[116:119],a[144:147],a[16:19]
ds_read_b64_tr_b16 v[104:105],v1 offset:20736
v_mfma_f32_16x16x32_bf16 a[16:19],v[120:123],a[160:163],a[16:19]
ds_read_b64_tr_b16 v[106:107],v1 offset:25856
v_add_u32 v1,0xffff6000,v1
v_mfma_f32_16x16x32_bf16 a[16:19],v[124:127],a[176:179],a[16:19]
v_mfma_f32_16x16x32_bf16 a[20:23],v[124:127],a[180:183],a[20:23]
v_mfma_f32_16x16x32_bf16 a[20:23],v[120:123],a[164:167],a[20:23]
v_mfma_f32_16x16x32_bf16 a[20:23],v[116:119],a[148:151],a[20:23]
v_mfma_f32_16x16x32_bf16 a[20:23],v[112:115],a[132:135],a[20:23]
v_mfma_f32_16x16x32_bf16 a[24:27],v[112:115],a[136:139],a[24:27]
buffer_load_dwordx4 v[180:183],v6,s[28:31],s90 offen offset:1024 sc1
v_mfma_f32_16x16x32_bf16 a[24:27],v[116:119],a[152:155],a[24:27]
v_mfma_f32_16x16x32_bf16 a[24:27],v[120:123],a[168:171],a[24:27]
v_mfma_f32_16x16x32_bf16 a[24:27],v[124:127],a[184:187],a[24:27]
v_mfma_f32_16x16x32_bf16 a[28:31],v[124:127],a[188:191],a[28:31]
ds_read_b64_tr_b16 v[124:125],v2 offset:31008
ds_read_b64_tr_b16 v[126:127],v2 offset:36128
v_mfma_f32_16x16x32_bf16 a[28:31],v[120:123],a[172:175],a[28:31]
ds_read_b64_tr_b16 v[120:121],v2 offset:20768
v_mfma_f32_16x16x32_bf16 a[28:31],v[116:119],a[156:159],a[28:31]
ds_read_b64_tr_b16 v[116:117],v2 offset:10528
v_mfma_f32_16x16x32_bf16 a[28:31],v[112:115],a[140:143],a[28:31]
ds_read_b64_tr_b16 v[112:113],v2 offset:288
v_mfma_f32_16x16x32_bf16 v[128:131],v[64:67],a[128:131],v[128:131]
ds_read_b64_tr_b16 v[114:115],v2 offset:5408
ds_read_b64_tr_b16 v[118:119],v2 offset:15648
v_mfma_f32_16x16x32_bf16 v[128:131],v[68:71],a[144:147],v[128:131]
ds_read_b64_tr_b16 v[122:123],v2 offset:25888
v_add_u32 v2,0xffff6000,v2
v_mfma_f32_16x16x32_bf16 v[128:131],v[72:75],a[160:163],v[128:131]
v_mfma_f32_16x16x32_bf16 v[128:131],v[76:79],a[176:179],v[128:131]
buffer_load_dwordx4 v[164:167],v6,s[28:31],s89 offen offset:1024 sc1
v_mfma_f32_16x16x32_bf16 v[136:139],v[76:79],a[180:183],v[136:139]
v_mfma_f32_16x16x32_bf16 v[136:139],v[72:75],a[164:167],v[136:139]
v_mfma_f32_16x16x32_bf16 v[136:139],v[68:71],a[148:151],v[136:139]
v_mfma_f32_16x16x32_bf16 v[136:139],v[64:67],a[132:135],v[136:139]
v_mfma_f32_16x16x32_bf16 v[144:147],v[64:67],a[136:139],v[144:147]
v_mfma_f32_16x16x32_bf16 v[144:147],v[68:71],a[152:155],v[144:147]
v_mfma_f32_16x16x32_bf16 v[144:147],v[72:75],a[168:171],v[144:147]
v_mfma_f32_16x16x32_bf16 v[144:147],v[76:79],a[184:187],v[144:147]
v_mfma_f32_16x16x32_bf16 v[152:155],v[76:79],a[188:191],v[152:155]
v_mfma_f32_16x16x32_bf16 v[152:155],v[72:75],a[172:175],v[152:155]
v_mfma_f32_16x16x32_bf16 v[152:155],v[68:71],a[156:159],v[152:155]
buffer_load_dwordx4 v[148:151],v6,s[28:31],s88 offen offset:1024 sc1
v_mfma_f32_16x16x32_bf16 v[152:155],v[64:67],a[140:143],v[152:155]
s_barrier
v_mfma_f32_16x16x32_bf16 v[160:163],v[80:83],a[128:131],v[160:163]
ds_read_b64_tr_b16 v[64:65],v1
ds_read_b64_tr_b16 v[66:67],v1 offset:5120
v_mfma_f32_16x16x32_bf16 v[160:163],v[84:87],a[144:147],v[160:163]
ds_read_b64_tr_b16 v[68:69],v1 offset:10240
v_mfma_f32_16x16x32_bf16 v[160:163],v[88:91],a[160:163],v[160:163]
ds_read_b64_tr_b16 v[70:71],v1 offset:15360
v_mfma_f32_16x16x32_bf16 v[160:163],v[92:95],a[176:179],v[160:163]
buffer_load_dwordx4 v24,s[48:51],s83 offen lds
v_mfma_f32_16x16x32_bf16 v[168:171],v[92:95],a[180:183],v[168:171]
ds_read_b64_tr_b16 v[72:73],v1 offset:20480
ds_read_b64_tr_b16 v[74:75],v1 offset:25600
v_mfma_f32_16x16x32_bf16 v[168:171],v[88:91],a[164:167],v[168:171]
ds_read_b64_tr_b16 v[76:77],v1 offset:30720
v_mfma_f32_16x16x32_bf16 v[168:171],v[84:87],a[148:151],v[168:171]
ds_read_b64_tr_b16 v[78:79],v1 offset:35840
v_mfma_f32_16x16x32_bf16 v[168:171],v[80:83],a[132:135],v[168:171]
v_mfma_f32_16x16x32_bf16 v[176:179],v[80:83],a[136:139],v[176:179]
buffer_load_dwordx4 v[132:135],v6,s[28:31],s87 offen offset:1024 sc1
v_mfma_f32_16x16x32_bf16 v[176:179],v[84:87],a[152:155],v[176:179]
v_mfma_f32_16x16x32_bf16 v[176:179],v[88:91],a[168:171],v[176:179]
v_mfma_f32_16x16x32_bf16 v[176:179],v[92:95],a[184:187],v[176:179]
v_mfma_f32_16x16x32_bf16 v[184:187],v[92:95],a[188:191],v[184:187]
ds_read_b64_tr_b16 v[92:93],v2 offset:30752
ds_read_b64_tr_b16 v[94:95],v2 offset:35872
v_mfma_f32_16x16x32_bf16 v[184:187],v[88:91],a[172:175],v[184:187]
ds_read_b64_tr_b16 v[88:89],v2 offset:20512
v_mfma_f32_16x16x32_bf16 v[184:187],v[84:87],a[156:159],v[184:187]
buffer_load_dwordx4 v25,s[48:51],s83 offen offset:1024 lds
v_mfma_f32_16x16x32_bf16 v[184:187],v[80:83],a[140:143],v[184:187]
s_waitcnt lgkmcnt(3)
ds_read_b64_tr_b16 v[80:81],v2 offset:32
ds_read_b64_tr_b16 v[82:83],v2 offset:5152
v_mfma_f32_16x16x32_bf16 v[192:195],v[96:99],a[128:131],v[192:195]
ds_read_b64_tr_b16 v[84:85],v2 offset:10272
v_mfma_f32_16x16x32_bf16 v[192:195],v[100:103],a[144:147],v[192:195]
ds_read_b64_tr_b16 v[86:87],v2 offset:15392
v_mfma_f32_16x16x32_bf16 v[192:195],v[104:107],a[160:163],v[192:195]
ds_read_b64_tr_b16 v[90:91],v2 offset:25632
v_mfma_f32_16x16x32_bf16 v[192:195],v[108:111],a[176:179],v[192:195]
v_mfma_f32_16x16x32_bf16 v[200:203],v[108:111],a[180:183],v[200:203]
buffer_load_dwordx4 v26,s[48:51],s83 offen offset:2048 lds
v_mfma_f32_16x16x32_bf16 v[200:203],v[104:107],a[164:167],v[200:203]
v_mfma_f32_16x16x32_bf16 v[200:203],v[100:103],a[148:151],v[200:203]
v_mfma_f32_16x16x32_bf16 v[200:203],v[96:99],a[132:135],v[200:203]
v_mfma_f32_16x16x32_bf16 v[208:211],v[96:99],a[136:139],v[208:211]
v_mfma_f32_16x16x32_bf16 v[208:211],v[100:103],a[152:155],v[208:211]
buffer_load_dwordx4 v[196:199],v6,s[28:31],s87 offen offset:2048 sc1
v_mfma_f32_16x16x32_bf16 v[208:211],v[104:107],a[168:171],v[208:211]
v_mfma_f32_16x16x32_bf16 v[208:211],v[108:111],a[184:187],v[208:211]
v_mfma_f32_16x16x32_bf16 v[216:219],v[108:111],a[188:191],v[216:219]
ds_read_b64_tr_b16 v[108:109],v1 offset:30784
ds_read_b64_tr_b16 v[110:111],v1 offset:35904
v_mfma_f32_16x16x32_bf16 v[216:219],v[104:107],a[172:175],v[216:219]
ds_read_b64_tr_b16 v[104:105],v1 offset:20544
v_mfma_f32_16x16x32_bf16 v[216:219],v[100:103],a[156:159],v[216:219]
ds_read_b64_tr_b16 v[100:101],v1 offset:10304
v_mfma_f32_16x16x32_bf16 v[216:219],v[96:99],a[140:143],v[216:219]
buffer_load_dwordx4 v27,s[48:51],s83 offen offset:3072 lds
v_mfma_f32_16x16x32_bf16 v[224:227],v[112:115],a[128:131],v[224:227]
s_add_u32 m0,s86,126976
ds_read_b64_tr_b16 v[96:97],v1 offset:64
ds_read_b64_tr_b16 v[98:99],v1 offset:5184
v_mfma_f32_16x16x32_bf16 v[224:227],v[116:119],a[144:147],v[224:227]
ds_read_b64_tr_b16 v[102:103],v1 offset:15424
v_mfma_f32_16x16x32_bf16 v[224:227],v[120:123],a[160:163],v[224:227]
ds_read_b64_tr_b16 v[106:107],v1 offset:25664
v_mfma_f32_16x16x32_bf16 v[224:227],v[124:127],a[176:179],v[224:227]
v_mfma_f32_16x16x32_bf16 v[232:235],v[124:127],a[180:183],v[232:235]
buffer_load_dwordx4 v[212:215],v6,s[28:31],s88 offen offset:2048 sc1
v_mfma_f32_16x16x32_bf16 v[232:235],v[120:123],a[164:167],v[232:235]
v_mfma_f32_16x16x32_bf16 v[232:235],v[116:119],a[148:151],v[232:235]
v_mfma_f32_16x16x32_bf16 v[232:235],v[112:115],a[132:135],v[232:235]
v_mfma_f32_16x16x32_bf16 v[240:243],v[112:115],a[136:139],v[240:243]
v_mfma_f32_16x16x32_bf16 v[240:243],v[116:119],a[152:155],v[240:243]
v_mfma_f32_16x16x32_bf16 v[240:243],v[120:123],a[168:171],v[240:243]
buffer_load_dwordx4 v7,s[48:51],s83 offen lds
v_mfma_f32_16x16x32_bf16 v[240:243],v[124:127],a[184:187],v[240:243]
s_add_u32 m0,s86,128000
s_mul_i32 s83,s63,20480
v_mfma_f32_16x16x32_bf16 v[248:251],v[124:127],a[188:191],v[248:251]
ds_read_b64_tr_b16 v[124:125],v2 offset:30816
ds_read_b64_tr_b16 v[126:127],v2 offset:35936
v_mfma_f32_16x16x32_bf16 v[248:251],v[120:123],a[172:175],v[248:251]
ds_read_b64_tr_b16 v[120:121],v2 offset:20576
v_mfma_f32_16x16x32_bf16 v[248:251],v[116:119],a[156:159],v[248:251]
ds_read_b64_tr_b16 v[116:117],v2 offset:10336
v_mfma_f32_16x16x32_bf16 v[248:251],v[112:115],a[140:143],v[248:251]
buffer_load_dwordx4 v[228:231],v6,s[28:31],s89 offen offset:2048 sc1
v_mfma_f32_16x16x32_bf16 a[192:195],v[64:67],a[68:71],a[192:195]
s_waitcnt vmcnt(17)
ds_read_b64_tr_b16 v[112:113],v2 offset:96
ds_read_b64_tr_b16 v[114:115],v2 offset:5216
v_mfma_f32_16x16x32_bf16 a[192:195],v[68:71],a[76:79],a[192:195]
ds_read_b64_tr_b16 v[118:119],v2 offset:15456
v_mfma_f32_16x16x32_bf16 a[192:195],v[72:75],a[84:87],a[192:195]
ds_read_b64_tr_b16 v[122:123],v2 offset:25696
v_mfma_f32_16x16x32_bf16 a[192:195],v[76:79],a[92:95],a[192:195]
v_mfma_f32_16x16x32_bf16 a[196:199],v[76:79],a[124:127],a[196:199]
v_mfma_f32_16x16x32_bf16 a[196:199],v[72:75],a[116:119],a[196:199]
buffer_load_dwordx4 v24,s[48:51],s84 offen lds
v_mfma_f32_16x16x32_bf16 a[196:199],v[68:71],a[108:111],a[196:199]
v_mfma_f32_16x16x32_bf16 a[196:199],v[64:67],a[100:103],a[196:199]
v_mfma_f32_16x16x32_bf16 a[200:203],v[64:67],a[64:67],a[200:203]
v_mfma_f32_16x16x32_bf16 a[200:203],v[68:71],a[72:75],a[200:203]
s_waitcnt vmcnt(16)
v_mfma_f32_16x16x32_bf16 a[200:203],v[72:75],a[80:83],a[200:203]
buffer_load_dwordx4 v[244:247],v6,s[28:31],s90 offen offset:2048 sc1
v_mfma_f32_16x16x32_bf16 a[200:203],v[76:79],a[88:91],a[200:203]
s_waitcnt vmcnt(16)
v_mfma_f32_16x16x32_bf16 a[204:207],v[76:79],a[120:123],a[204:207]
ds_read_b64_tr_b16 v[76:77],v1 offset:30848
ds_read_b64_tr_b16 v[78:79],v1 offset:35968
v_mfma_f32_16x16x32_bf16 a[204:207],v[72:75],a[112:115],a[204:207]
ds_read_b64_tr_b16 v[72:73],v1 offset:20608
v_mfma_f32_16x16x32_bf16 a[204:207],v[68:71],a[104:107],a[204:207]
ds_read_b64_tr_b16 v[68:69],v1 offset:10368
v_mfma_f32_16x16x32_bf16 a[204:207],v[64:67],a[96:99],a[204:207]
s_waitcnt lgkmcnt(4)
ds_read_b64_tr_b16 v[64:65],v1 offset:128
v_mfma_f32_16x16x32_bf16 a[208:211],v[80:83],a[68:71],a[208:211]
buffer_load_dwordx4 v25,s[48:51],s84 offen offset:1024 lds
v_mfma_f32_16x16x32_bf16 a[208:211],v[84:87],a[76:79],a[208:211]
ds_read_b64_tr_b16 v[66:67],v1 offset:5248
ds_read_b64_tr_b16 v[70:71],v1 offset:15488
v_mfma_f32_16x16x32_bf16 a[208:211],v[88:91],a[84:87],a[208:211]
ds_read_b64_tr_b16 v[74:75],v1 offset:25728
v_mfma_f32_16x16x32_bf16 a[208:211],v[92:95],a[92:95],a[208:211]
v_mfma_f32_16x16x32_bf16 a[212:215],v[92:95],a[124:127],a[212:215]
v_mfma_f32_16x16x32_bf16 a[212:215],v[88:91],a[116:119],a[212:215]
buffer_load_dwordx4 v[188:191],v28,s[28:31],s90 offen offset:3072 sc1
v_mfma_f32_16x16x32_bf16 a[212:215],v[84:87],a[108:111],a[212:215]
v_mfma_f32_16x16x32_bf16 a[212:215],v[80:83],a[100:103],a[212:215]
v_mfma_f32_16x16x32_bf16 a[216:219],v[80:83],a[64:67],a[216:219]
v_mfma_f32_16x16x32_bf16 a[216:219],v[84:87],a[72:75],a[216:219]
v_mfma_f32_16x16x32_bf16 a[216:219],v[88:91],a[80:83],a[216:219]
v_mfma_f32_16x16x32_bf16 a[216:219],v[92:95],a[88:91],a[216:219]
buffer_load_dwordx4 v26,s[48:51],s84 offen offset:2048 lds
)A"
R"A(v_mfma_f32_16x16x32_bf16 a[220:223],v[92:95],a[120:123],a[220:223]
ds_read_b64_tr_b16 v[92:93],v2 offset:30880
ds_read_b64_tr_b16 v[94:95],v2 offset:36000
v_mfma_f32_16x16x32_bf16 a[220:223],v[88:91],a[112:115],a[220:223]
ds_read_b64_tr_b16 v[88:89],v2 offset:20640
v_mfma_f32_16x16x32_bf16 a[220:223],v[84:87],a[104:107],a[220:223]
ds_read_b64_tr_b16 v[84:85],v2 offset:10400
v_mfma_f32_16x16x32_bf16 a[220:223],v[80:83],a[96:99],a[220:223]
ds_read_b64_tr_b16 v[80:81],v2 offset:160
v_mfma_f32_16x16x32_bf16 a[224:227],v[96:99],a[68:71],a[224:227]
ds_read_b64_tr_b16 v[82:83],v2 offset:5280
ds_read_b64_tr_b16 v[86:87],v2 offset:15520
v_mfma_f32_16x16x32_bf16 a[224:227],v[100:103],a[76:79],a[224:227]
buffer_load_dwordx4 v27,s[48:51],s84 offen offset:3072 lds
v_mfma_f32_16x16x32_bf16 a[224:227],v[104:107],a[84:87],a[224:227]
s_add_u32 m0,s86,132096
ds_read_b64_tr_b16 v[90:91],v2 offset:25760
v_mfma_f32_16x16x32_bf16 a[224:227],v[108:111],a[92:95],a[224:227]
v_mfma_f32_16x16x32_bf16 a[228:231],v[108:111],a[124:127],a[228:231]
v_mfma_f32_16x16x32_bf16 a[228:231],v[104:107],a[116:119],a[228:231]
v_mfma_f32_16x16x32_bf16 a[228:231],v[100:103],a[108:111],a[228:231]
buffer_load_dwordx4 v[172:175],v28,s[28:31],s89 offen offset:3072 sc1
v_mfma_f32_16x16x32_bf16 a[228:231],v[96:99],a[100:103],a[228:231]
v_mfma_f32_16x16x32_bf16 a[232:235],v[96:99],a[64:67],a[232:235]
v_mfma_f32_16x16x32_bf16 a[232:235],v[100:103],a[72:75],a[232:235]
v_mfma_f32_16x16x32_bf16 a[232:235],v[104:107],a[80:83],a[232:235]
v_mfma_f32_16x16x32_bf16 a[232:235],v[108:111],a[88:91],a[232:235]
v_mfma_f32_16x16x32_bf16 a[236:239],v[108:111],a[120:123],a[236:239]
buffer_load_dwordx4 v7,s[48:51],s84 offen lds
v_mfma_f32_16x16x32_bf16 a[236:239],v[104:107],a[112:115],a[236:239]
ds_read_b64_tr_b16 v[104:105],v1 offset:20672
ds_read_b64_tr_b16 v[106:107],v1 offset:25792
v_mfma_f32_16x16x32_bf16 a[236:239],v[100:103],a[104:107],a[236:239]
ds_read_b64_tr_b16 v[100:101],v1 offset:10432
v_mfma_f32_16x16x32_bf16 a[236:239],v[96:99],a[96:99],a[236:239]
ds_read_b64_tr_b16 v[96:97],v1 offset:192
v_mfma_f32_16x16x32_bf16 a[240:243],v[112:115],a[68:71],a[240:243]
ds_read_b64_tr_b16 v[98:99],v1 offset:5312
v_mfma_f32_16x16x32_bf16 a[240:243],v[116:119],a[76:79],a[240:243]
buffer_load_dwordx4 v[156:159],v28,s[28:31],s88 offen offset:3072 sc1
v_mfma_f32_16x16x32_bf16 a[240:243],v[120:123],a[84:87],a[240:243]
ds_read_b64_tr_b16 v[102:103],v1 offset:15552
ds_read_b64_tr_b16 v[108:109],v1 offset:30912
v_mfma_f32_16x16x32_bf16 a[240:243],v[124:127],a[92:95],a[240:243]
ds_read_b64_tr_b16 v[110:111],v1 offset:36032
v_add_u32 v1,0xffff6000,v1
v_mfma_f32_16x16x32_bf16 a[244:247],v[124:127],a[124:127],a[244:247]
v_mfma_f32_16x16x32_bf16 a[244:247],v[120:123],a[116:119],a[244:247]
v_mfma_f32_16x16x32_bf16 a[244:247],v[116:119],a[108:111],a[244:247]
buffer_load_dwordx4 v[140:143],v28,s[28:31],s87 offen offset:3072 sc1
v_mfma_f32_16x16x32_bf16 a[244:247],v[112:115],a[100:103],a[244:247]
s_add_u32 s87,s64,3
s_and_b32 s87,s87,3
s_lshl_b32 s87,s87,1
s_or_b32 s87,s87,1
s_lshl_b32 s87,s87,16
s_add_u32 s88,s87,4096
s_add_u32 s89,s87,8192
s_add_u32 s90,s87,12288
v_mfma_f32_16x16x32_bf16 a[248:251],v[112:115],a[64:67],a[248:251]
v_mfma_f32_16x16x32_bf16 a[248:251],v[116:119],a[72:75],a[248:251]
v_mfma_f32_16x16x32_bf16 a[248:251],v[120:123],a[80:83],a[248:251]
v_mfma_f32_16x16x32_bf16 a[248:251],v[124:127],a[88:91],a[248:251]
buffer_load_dwordx4 a[128:131],v6,s[28:31],s87 offen sc1
v_mfma_f32_16x16x32_bf16 a[252:255],v[124:127],a[120:123],a[252:255]
ds_read_b64_tr_b16 v[124:125],v2 offset:30944
ds_read_b64_tr_b16 v[126:127],v2 offset:36064
v_mfma_f32_16x16x32_bf16 a[252:255],v[120:123],a[112:115],a[252:255]
ds_read_b64_tr_b16 v[120:121],v2 offset:20704
v_mfma_f32_16x16x32_bf16 a[252:255],v[116:119],a[104:107],a[252:255]
ds_read_b64_tr_b16 v[116:117],v2 offset:10464
v_mfma_f32_16x16x32_bf16 a[252:255],v[112:115],a[96:99],a[252:255]
s_waitcnt lgkmcnt(4)
ds_read_b64_tr_b16 v[112:113],v2 offset:224
v_mfma_f32_16x16x32_bf16 a[0:3],v[64:67],a[68:71],a[0:3]
buffer_load_dwordx4 a[144:147],v6,s[28:31],s88 offen sc1
v_mfma_f32_16x16x32_bf16 a[0:3],v[68:71],a[76:79],a[0:3]
ds_read_b64_tr_b16 v[114:115],v2 offset:5344
ds_read_b64_tr_b16 v[118:119],v2 offset:15584
v_mfma_f32_16x16x32_bf16 a[0:3],v[72:75],a[84:87],a[0:3]
ds_read_b64_tr_b16 v[122:123],v2 offset:25824
v_add_u32 v2,0xffff6000,v2
v_mfma_f32_16x16x32_bf16 a[0:3],v[76:79],a[92:95],a[0:3]
v_mfma_f32_16x16x32_bf16 a[4:7],v[76:79],a[124:127],a[4:7]
v_mfma_f32_16x16x32_bf16 a[4:7],v[72:75],a[116:119],a[4:7]
buffer_load_dwordx4 a[160:163],v6,s[28:31],s89 offen sc1
v_mfma_f32_16x16x32_bf16 a[4:7],v[68:71],a[108:111],a[4:7]
v_mfma_f32_16x16x32_bf16 a[4:7],v[64:67],a[100:103],a[4:7]
v_mfma_f32_16x16x32_bf16 a[8:11],v[64:67],a[64:67],a[8:11]
v_mfma_f32_16x16x32_bf16 a[8:11],v[68:71],a[72:75],a[8:11]
v_mfma_f32_16x16x32_bf16 a[8:11],v[72:75],a[80:83],a[8:11]
buffer_load_dwordx4 a[176:179],v6,s[28:31],s90 offen sc1
v_mfma_f32_16x16x32_bf16 a[8:11],v[76:79],a[88:91],a[8:11]
v_mfma_f32_16x16x32_bf16 a[12:15],v[76:79],a[120:123],a[12:15]
ds_read_b64_tr_b16 v[76:77],v1 offset:30720
ds_read_b64_tr_b16 v[78:79],v1 offset:35840
v_mfma_f32_16x16x32_bf16 a[12:15],v[72:75],a[112:115],a[12:15]
ds_read_b64_tr_b16 v[72:73],v1 offset:20480
v_mfma_f32_16x16x32_bf16 a[12:15],v[68:71],a[104:107],a[12:15]
ds_read_b64_tr_b16 v[68:69],v1 offset:10240
v_mfma_f32_16x16x32_bf16 a[12:15],v[64:67],a[96:99],a[12:15]
buffer_load_dwordx4 a[180:183],v6,s[28:31],s90 offen offset:1024 sc1
v_mfma_f32_16x16x32_bf16 a[16:19],v[80:83],a[68:71],a[16:19]
ds_read_b64_tr_b16 v[64:65],v1
ds_read_b64_tr_b16 v[66:67],v1 offset:5120
v_mfma_f32_16x16x32_bf16 a[16:19],v[84:87],a[76:79],a[16:19]
ds_read_b64_tr_b16 v[70:71],v1 offset:15360
v_mfma_f32_16x16x32_bf16 a[16:19],v[88:91],a[84:87],a[16:19]
ds_read_b64_tr_b16 v[74:75],v1 offset:25600
v_add_u32 v1,0x1e000,v1
v_mfma_f32_16x16x32_bf16 a[16:19],v[92:95],a[92:95],a[16:19]
v_mfma_f32_16x16x32_bf16 a[20:23],v[92:95],a[124:127],a[20:23]
buffer_load_dwordx4 a[164:167],v6,s[28:31],s89 offen offset:1024 sc1
v_mfma_f32_16x16x32_bf16 a[20:23],v[88:91],a[116:119],a[20:23]
v_mfma_f32_16x16x32_bf16 a[20:23],v[84:87],a[108:111],a[20:23]
s_nop 0
L_pd17_%=:
s_cbranch_execz L_trtg2F_%=
v_mfma_f32_16x16x32_bf16 a[20:23],v[80:83],a[100:103],a[20:23]
v_mfma_f32_16x16x32_bf16 a[24:27],v[80:83],a[64:67],a[24:27]
v_mfma_f32_16x16x32_bf16 a[24:27],v[84:87],a[72:75],a[24:27]
buffer_load_dwordx4 a[148:151],v6,s[28:31],s88 offen offset:1024 sc1
v_mfma_f32_16x16x32_bf16 a[24:27],v[88:91],a[80:83],a[24:27]
v_mfma_f32_16x16x32_bf16 a[24:27],v[92:95],a[88:91],a[24:27]
v_mfma_f32_16x16x32_bf16 a[28:31],v[92:95],a[120:123],a[28:31]
ds_read_b64_tr_b16 v[92:93],v2 offset:30752
ds_read_b64_tr_b16 v[94:95],v2 offset:35872
v_mfma_f32_16x16x32_bf16 a[28:31],v[88:91],a[112:115],a[28:31]
ds_read_b64_tr_b16 v[88:89],v2 offset:20512
v_mfma_f32_16x16x32_bf16 a[28:31],v[84:87],a[104:107],a[28:31]
buffer_load_dwordx4 a[132:135],v6,s[28:31],s87 offen offset:1024 sc1
v_mfma_f32_16x16x32_bf16 a[28:31],v[80:83],a[96:99],a[28:31]
ds_read_b64_tr_b16 v[80:81],v2 offset:32
ds_read_b64_tr_b16 v[82:83],v2 offset:5152
v_mfma_f32_16x16x32_bf16 v[128:131],v[96:99],a[68:71],v[128:131]
ds_read_b64_tr_b16 v[84:85],v2 offset:10272
v_mfma_f32_16x16x32_bf16 v[128:131],v[100:103],a[76:79],v[128:131]
ds_read_b64_tr_b16 v[86:87],v2 offset:15392
v_mfma_f32_16x16x32_bf16 v[128:131],v[104:107],a[84:87],v[128:131]
ds_read_b64_tr_b16 v[90:91],v2 offset:25632
v_add_u32 v2,0x1e000,v2
v_mfma_f32_16x16x32_bf16 v[128:131],v[108:111],a[92:95],v[128:131]
buffer_load_dwordx4 a[136:139],v6,s[28:31],s87 offen offset:2048 sc1
v_mfma_f32_16x16x32_bf16 v[136:139],v[108:111],a[124:127],v[136:139]
v_mfma_f32_16x16x32_bf16 v[136:139],v[104:107],a[116:119],v[136:139]
v_mfma_f32_16x16x32_bf16 v[136:139],v[100:103],a[108:111],v[136:139]
v_mfma_f32_16x16x32_bf16 v[136:139],v[96:99],a[100:103],v[136:139]
v_mfma_f32_16x16x32_bf16 v[144:147],v[96:99],a[64:67],v[144:147]
buffer_load_dwordx4 a[152:155],v6,s[28:31],s88 offen offset:2048 sc1
v_mfma_f32_16x16x32_bf16 v[144:147],v[100:103],a[72:75],v[144:147]
v_mfma_f32_16x16x32_bf16 v[144:147],v[104:107],a[80:83],v[144:147]
v_mfma_f32_16x16x32_bf16 v[144:147],v[108:111],a[88:91],v[144:147]
v_mfma_f32_16x16x32_bf16 v[152:155],v[108:111],a[120:123],v[152:155]
v_mfma_f32_16x16x32_bf16 v[152:155],v[104:107],a[112:115],v[152:155]
buffer_load_dwordx4 a[168:171],v6,s[28:31],s89 offen offset:2048 sc1
v_mfma_f32_16x16x32_bf16 v[152:155],v[100:103],a[104:107],v[152:155]
v_mfma_f32_16x16x32_bf16 v[152:155],v[96:99],a[96:99],v[152:155]
s_waitcnt vmcnt(13)
s_barrier
s_waitcnt lgkmcnt(0)
v_mfma_f32_16x16x32_bf16 v[160:163],v[112:115],a[68:71],v[160:163]
ds_read_b64_tr_b16 v[96:97],v1
ds_read_b64_tr_b16 v[98:99],v1 offset:5120
v_mfma_f32_16x16x32_bf16 v[160:163],v[116:119],a[76:79],v[160:163]
buffer_load_dwordx4 a[184:187],v6,s[28:31],s90 offen offset:2048 sc1
v_mfma_f32_16x16x32_bf16 v[160:163],v[120:123],a[84:87],v[160:163]
ds_read_b64_tr_b16 v[100:101],v1 offset:10240
ds_read_b64_tr_b16 v[102:103],v1 offset:15360
v_mfma_f32_16x16x32_bf16 v[160:163],v[124:127],a[92:95],v[160:163]
ds_read_b64_tr_b16 v[104:105],v1 offset:20480
v_mfma_f32_16x16x32_bf16 v[168:171],v[124:127],a[124:127],v[168:171]
ds_read_b64_tr_b16 v[106:107],v1 offset:25600
v_mfma_f32_16x16x32_bf16 v[168:171],v[120:123],a[116:119],v[168:171]
ds_read_b64_tr_b16 v[108:109],v1 offset:30720
v_mfma_f32_16x16x32_bf16 v[168:171],v[116:119],a[108:111],v[168:171]
buffer_load_dwordx4 a[188:191],v28,s[28:31],s90 offen offset:3072 sc1
v_mfma_f32_16x16x32_bf16 v[168:171],v[112:115],a[100:103],v[168:171]
ds_read_b64_tr_b16 v[110:111],v1 offset:35840
v_add_u32 v1,0xfffe2000,v1
v_mfma_f32_16x16x32_bf16 v[176:179],v[112:115],a[64:67],v[176:179]
v_mfma_f32_16x16x32_bf16 v[176:179],v[116:119],a[72:75],v[176:179]
v_mfma_f32_16x16x32_bf16 v[176:179],v[120:123],a[80:83],v[176:179]
v_mfma_f32_16x16x32_bf16 v[176:179],v[124:127],a[88:91],v[176:179]
buffer_load_dwordx4 a[172:175],v28,s[28:31],s89 offen offset:3072 sc1
v_mfma_f32_16x16x32_bf16 v[184:187],v[124:127],a[120:123],v[184:187]
ds_read_b64_tr_b16 v[124:125],v2 offset:30752
ds_read_b64_tr_b16 v[126:127],v2 offset:35872
v_mfma_f32_16x16x32_bf16 v[184:187],v[120:123],a[112:115],v[184:187]
ds_read_b64_tr_b16 v[120:121],v2 offset:20512
v_mfma_f32_16x16x32_bf16 v[184:187],v[116:119],a[104:107],v[184:187]
ds_read_b64_tr_b16 v[116:117],v2 offset:10272
v_mfma_f32_16x16x32_bf16 v[184:187],v[112:115],a[96:99],v[184:187]
ds_read_b64_tr_b16 v[112:113],v2 offset:32
v_mfma_f32_16x16x32_bf16 a[192:195],v[64:67],v[32:35],a[192:195]
buffer_load_dwordx4 a[156:159],v28,s[28:31],s88 offen offset:3072 sc1
v_mfma_f32_16x16x32_bf16 a[192:195],v[68:71],v[36:39],a[192:195]
ds_read_b64_tr_b16 v[114:115],v2 offset:5152
ds_read_b64_tr_b16 v[118:119],v2 offset:15392
v_mfma_f32_16x16x32_bf16 a[192:195],v[72:75],v[40:43],a[192:195]
ds_read_b64_tr_b16 v[122:123],v2 offset:25632
v_add_u32 v2,0xfffe2000,v2
v_mfma_f32_16x16x32_bf16 a[192:195],v[76:79],v[44:47],a[192:195]
v_mfma_f32_16x16x32_bf16 a[196:199],v[76:79],v[180:183],a[196:199]
v_mfma_f32_16x16x32_bf16 a[196:199],v[72:75],v[164:167],a[196:199]
buffer_load_dwordx4 a[140:143],v28,s[28:31],s87 offen offset:3072 sc1
v_mfma_f32_16x16x32_bf16 a[196:199],v[68:71],v[148:151],a[196:199]
s_mul_i32 s87,s60,81920
s_add_u32 s87,s87,s66
s_add_u32 s83,s83,s87
v_mfma_f32_16x16x32_bf16 a[196:199],v[64:67],v[132:135],a[196:199]
s_lshl_b32 s66,s63,6
s_add_u32 s87,s87,s66
s_add_u32 s88,s87,20480
v_mfma_f32_16x16x32_bf16 a[200:203],v[64:67],v[196:199],a[200:203]
s_add_u32 s89,s87,40960
s_add_u32 s90,s87,61440
s_add_u32 s66,s77,4096
v_mfma_f32_16x16x32_bf16 a[200:203],v[68:71],v[212:215],a[200:203]
v_mfma_f32_16x16x32_bf16 a[200:203],v[72:75],v[228:231],a[200:203]
v_mfma_f32_16x16x32_bf16 a[200:203],v[76:79],v[244:247],a[200:203]
v_mfma_f32_16x16x32_bf16 a[204:207],v[76:79],v[188:191],a[204:207]
ds_read_b64_tr_b16 v[76:77],v1 offset:30784
ds_read_b64_tr_b16 v[78:79],v1 offset:35904
)A"
R"A(v_mfma_f32_16x16x32_bf16 a[204:207],v[72:75],v[172:175],a[204:207]
s_waitcnt vmcnt(17)
ds_read_b64_tr_b16 v[72:73],v1 offset:20544
v_mfma_f32_16x16x32_bf16 a[204:207],v[68:71],v[156:159],a[204:207]
s_waitcnt vmcnt(16)
ds_read_b64_tr_b16 v[68:69],v1 offset:10304
v_mfma_f32_16x16x32_bf16 a[204:207],v[64:67],v[140:143],a[204:207]
ds_read_b64_tr_b16 v[64:65],v1 offset:64
v_mfma_f32_16x16x32_bf16 a[208:211],v[80:83],v[32:35],a[208:211]
ds_read_b64_tr_b16 v[66:67],v1 offset:5184
ds_read_b64_tr_b16 v[70:71],v1 offset:15424
v_mfma_f32_16x16x32_bf16 a[208:211],v[84:87],v[36:39],a[208:211]
ds_read_b64_tr_b16 v[74:75],v1 offset:25664
v_add_u32 v1,0x1e000,v1
v_mfma_f32_16x16x32_bf16 a[208:211],v[88:91],v[40:43],a[208:211]
v_mfma_f32_16x16x32_bf16 a[208:211],v[92:95],v[44:47],a[208:211]
v_mfma_f32_16x16x32_bf16 a[212:215],v[92:95],v[180:183],a[212:215]
v_mfma_f32_16x16x32_bf16 a[212:215],v[88:91],v[164:167],a[212:215]
v_mfma_f32_16x16x32_bf16 a[212:215],v[84:87],v[148:151],a[212:215]
v_mfma_f32_16x16x32_bf16 a[212:215],v[80:83],v[132:135],a[212:215]
v_mfma_f32_16x16x32_bf16 a[216:219],v[80:83],v[196:199],a[216:219]
v_mfma_f32_16x16x32_bf16 a[216:219],v[84:87],v[212:215],a[216:219]
v_mfma_f32_16x16x32_bf16 a[216:219],v[88:91],v[228:231],a[216:219]
v_mfma_f32_16x16x32_bf16 a[216:219],v[92:95],v[244:247],a[216:219]
v_mfma_f32_16x16x32_bf16 a[220:223],v[92:95],v[188:191],a[220:223]
ds_read_b64_tr_b16 v[92:93],v2 offset:30816
ds_read_b64_tr_b16 v[94:95],v2 offset:35936
v_mfma_f32_16x16x32_bf16 a[220:223],v[88:91],v[172:175],a[220:223]
ds_read_b64_tr_b16 v[88:89],v2 offset:20576
v_mfma_f32_16x16x32_bf16 a[220:223],v[84:87],v[156:159],a[220:223]
ds_read_b64_tr_b16 v[84:85],v2 offset:10336
v_mfma_f32_16x16x32_bf16 a[220:223],v[80:83],v[140:143],a[220:223]
s_waitcnt vmcnt(15) lgkmcnt(4)
ds_read_b64_tr_b16 v[80:81],v2 offset:96
v_mfma_f32_16x16x32_bf16 a[192:195],v[96:99],a[128:131],a[192:195]
s_waitcnt vmcnt(14)
ds_read_b64_tr_b16 v[82:83],v2 offset:5216
ds_read_b64_tr_b16 v[86:87],v2 offset:15456
v_mfma_f32_16x16x32_bf16 a[192:195],v[100:103],a[144:147],a[192:195]
s_waitcnt vmcnt(13)
ds_read_b64_tr_b16 v[90:91],v2 offset:25696
v_add_u32 v2,0x1e000,v2
v_mfma_f32_16x16x32_bf16 a[192:195],v[104:107],a[160:163],a[192:195]
s_waitcnt vmcnt(12)
v_mfma_f32_16x16x32_bf16 a[192:195],v[108:111],a[176:179],a[192:195]
s_waitcnt vmcnt(11)
v_mfma_f32_16x16x32_bf16 a[196:199],v[108:111],a[180:183],a[196:199]
s_waitcnt vmcnt(10)
v_mfma_f32_16x16x32_bf16 a[196:199],v[104:107],a[164:167],a[196:199]
s_waitcnt vmcnt(9)
v_mfma_f32_16x16x32_bf16 a[196:199],v[100:103],a[148:151],a[196:199]
s_waitcnt vmcnt(8)
v_mfma_f32_16x16x32_bf16 a[196:199],v[96:99],a[132:135],a[196:199]
s_waitcnt vmcnt(7)
v_mfma_f32_16x16x32_bf16 a[200:203],v[96:99],a[136:139],a[200:203]
s_waitcnt vmcnt(6)
v_mfma_f32_16x16x32_bf16 a[200:203],v[100:103],a[152:155],a[200:203]
s_waitcnt vmcnt(5)
v_mfma_f32_16x16x32_bf16 a[200:203],v[104:107],a[168:171],a[200:203]
s_waitcnt vmcnt(4)
v_mfma_f32_16x16x32_bf16 a[200:203],v[108:111],a[184:187],a[200:203]
s_waitcnt vmcnt(3)
v_accvgpr_read_b32 v16,a192
v_accvgpr_read_b32 v17,a193
v_mfma_f32_16x16x32_bf16 a[204:207],v[108:111],a[188:191],a[204:207]
s_waitcnt vmcnt(2)
v_accvgpr_read_b32 v18,a194
v_accvgpr_read_b32 v19,a195
v_mfma_f32_16x16x32_bf16 a[204:207],v[104:107],a[172:175],a[204:207]
s_waitcnt vmcnt(1)
v_cvt_pk_bf16_f32 v16,v16,v17
v_cvt_pk_bf16_f32 v17,v18,v19
v_mfma_f32_16x16x32_bf16 a[204:207],v[100:103],a[156:159],a[204:207]
s_waitcnt vmcnt(0)
v_accvgpr_read_b32 v8,a196
v_accvgpr_read_b32 v9,a197
v_mfma_f32_16x16x32_bf16 a[204:207],v[96:99],a[140:143],a[204:207]
v_accvgpr_read_b32 v10,a198
v_accvgpr_read_b32 v11,a199
v_mfma_f32_16x16x32_bf16 a[208:211],v[112:115],a[128:131],a[208:211]
v_cvt_pk_bf16_f32 v8,v8,v9
v_cvt_pk_bf16_f32 v9,v10,v11
v_mfma_f32_16x16x32_bf16 a[208:211],v[116:119],a[144:147],a[208:211]
ds_read_b64_tr_b16 v[96:97],v1 offset:64
ds_read_b64_tr_b16 v[98:99],v1 offset:5184
v_mfma_f32_16x16x32_bf16 a[208:211],v[120:123],a[160:163],a[208:211]
ds_read_b64_tr_b16 v[100:101],v1 offset:10304
v_mfma_f32_16x16x32_bf16 a[208:211],v[124:127],a[176:179],a[208:211]
ds_read_b64_tr_b16 v[102:103],v1 offset:15424
v_mfma_f32_16x16x32_bf16 a[212:215],v[124:127],a[180:183],a[212:215]
ds_read_b64_tr_b16 v[104:105],v1 offset:20544
v_mfma_f32_16x16x32_bf16 a[212:215],v[120:123],a[164:167],a[212:215]
ds_read_b64_tr_b16 v[106:107],v1 offset:25664
ds_read_b64_tr_b16 v[108:109],v1 offset:30784
v_mfma_f32_16x16x32_bf16 a[212:215],v[116:119],a[148:151],a[212:215]
ds_read_b64_tr_b16 v[110:111],v1 offset:35904
v_add_u32 v1,0xfffe2000,v1
v_mfma_f32_16x16x32_bf16 a[212:215],v[112:115],a[132:135],a[212:215]
v_accvgpr_read_b32 v20,a208
v_accvgpr_read_b32 v21,a209
v_mfma_f32_16x16x32_bf16 a[216:219],v[112:115],a[136:139],a[216:219]
v_accvgpr_read_b32 v22,a210
v_accvgpr_read_b32 v23,a211
v_mfma_f32_16x16x32_bf16 a[216:219],v[116:119],a[152:155],a[216:219]
v_cvt_pk_bf16_f32 v18,v20,v21
v_cvt_pk_bf16_f32 v19,v22,v23
v_mfma_f32_16x16x32_bf16 a[216:219],v[120:123],a[168:171],a[216:219]
buffer_store_dwordx4 v[16:19],v6,s[36:39],s74 offen sc1
v_mfma_f32_16x16x32_bf16 a[216:219],v[124:127],a[184:187],a[216:219]
v_accvgpr_read_b32 v12,a212
v_accvgpr_read_b32 v13,a213
v_mfma_f32_16x16x32_bf16 a[220:223],v[124:127],a[188:191],a[220:223]
v_accvgpr_read_b32 v14,a214
v_accvgpr_read_b32 v15,a215
v_mfma_f32_16x16x32_bf16 a[220:223],v[120:123],a[172:175],a[220:223]
v_cvt_pk_bf16_f32 v10,v12,v13
v_cvt_pk_bf16_f32 v11,v14,v15
v_mfma_f32_16x16x32_bf16 a[220:223],v[116:119],a[156:159],a[220:223]
v_accvgpr_read_b32 v16,a200
v_accvgpr_read_b32 v17,a201
v_mfma_f32_16x16x32_bf16 a[220:223],v[112:115],a[140:143],a[220:223]
v_accvgpr_read_b32 v18,a202
v_accvgpr_read_b32 v19,a203
v_mfma_f32_16x16x32_bf16 a[224:227],v[64:67],v[32:35],a[224:227]
v_accvgpr_read_b32 v20,a216
v_accvgpr_read_b32 v21,a217
v_mfma_f32_16x16x32_bf16 a[224:227],v[68:71],v[36:39],a[224:227]
v_accvgpr_read_b32 v22,a218
v_accvgpr_read_b32 v23,a219
v_mfma_f32_16x16x32_bf16 a[224:227],v[72:75],v[40:43],a[224:227]
v_cvt_pk_bf16_f32 v16,v16,v17
v_cvt_pk_bf16_f32 v17,v18,v19
v_mfma_f32_16x16x32_bf16 a[224:227],v[76:79],v[44:47],a[224:227]
v_cvt_pk_bf16_f32 v18,v20,v21
v_cvt_pk_bf16_f32 v19,v22,v23
v_mfma_f32_16x16x32_bf16 a[228:231],v[76:79],v[180:183],a[228:231]
ds_read_b64_tr_b16 v[112:113],v2 offset:96
ds_read_b64_tr_b16 v[114:115],v2 offset:5216
v_mfma_f32_16x16x32_bf16 a[228:231],v[72:75],v[164:167],a[228:231]
buffer_store_dwordx4 v[8:11],v6,s[36:39],s74 offen offset:1024 sc1
v_mfma_f32_16x16x32_bf16 a[228:231],v[68:71],v[148:151],a[228:231]
ds_read_b64_tr_b16 v[116:117],v2 offset:10336
ds_read_b64_tr_b16 v[118:119],v2 offset:15456
v_mfma_f32_16x16x32_bf16 a[228:231],v[64:67],v[132:135],a[228:231]
ds_read_b64_tr_b16 v[120:121],v2 offset:20576
v_accvgpr_read_b32 v8,a204
v_mfma_f32_16x16x32_bf16 a[232:235],v[64:67],v[196:199],a[232:235]
ds_read_b64_tr_b16 v[122:123],v2 offset:25696
v_accvgpr_read_b32 v9,a205
v_mfma_f32_16x16x32_bf16 a[232:235],v[68:71],v[212:215],a[232:235]
ds_read_b64_tr_b16 v[124:125],v2 offset:30816
v_accvgpr_read_b32 v10,a206
v_mfma_f32_16x16x32_bf16 a[232:235],v[72:75],v[228:231],a[232:235]
ds_read_b64_tr_b16 v[126:127],v2 offset:35936
v_accvgpr_read_b32 v11,a207
v_mfma_f32_16x16x32_bf16 a[232:235],v[76:79],v[244:247],a[232:235]
v_accvgpr_read_b32 v12,a220
v_accvgpr_read_b32 v13,a221
v_mfma_f32_16x16x32_bf16 a[236:239],v[76:79],v[188:191],a[236:239]
v_accvgpr_read_b32 v14,a222
v_accvgpr_read_b32 v15,a223
v_mfma_f32_16x16x32_bf16 a[236:239],v[72:75],v[172:175],a[236:239]
v_cvt_pk_bf16_f32 v8,v8,v9
v_cvt_pk_bf16_f32 v9,v10,v11
v_mfma_f32_16x16x32_bf16 a[236:239],v[68:71],v[156:159],a[236:239]
v_cvt_pk_bf16_f32 v10,v12,v13
v_cvt_pk_bf16_f32 v11,v14,v15
v_mfma_f32_16x16x32_bf16 a[236:239],v[64:67],v[140:143],a[236:239]
s_waitcnt lgkmcnt(3)
ds_read_b64_tr_b16 v[64:65],v1 offset:128
ds_read_b64_tr_b16 v[66:67],v1 offset:5248
v_mfma_f32_16x16x32_bf16 a[240:243],v[80:83],v[32:35],a[240:243]
buffer_store_dwordx4 v[16:19],v6,s[36:39],s74 offen offset:2048 sc1
v_mfma_f32_16x16x32_bf16 a[240:243],v[84:87],v[36:39],a[240:243]
ds_read_b64_tr_b16 v[68:69],v1 offset:10368
ds_read_b64_tr_b16 v[70:71],v1 offset:15488
v_mfma_f32_16x16x32_bf16 a[240:243],v[88:91],v[40:43],a[240:243]
ds_read_b64_tr_b16 v[72:73],v1 offset:20608
v_add_u32 v2,0xfffe2000,v2
v_mfma_f32_16x16x32_bf16 a[240:243],v[92:95],v[44:47],a[240:243]
ds_read_b64_tr_b16 v[74:75],v1 offset:25728
v_mfma_f32_16x16x32_bf16 a[244:247],v[92:95],v[180:183],a[244:247]
ds_read_b64_tr_b16 v[76:77],v1 offset:30848
v_mfma_f32_16x16x32_bf16 a[244:247],v[88:91],v[164:167],a[244:247]
ds_read_b64_tr_b16 v[78:79],v1 offset:35968
v_add_u32 v1,0x1e000,v1
v_mfma_f32_16x16x32_bf16 a[244:247],v[84:87],v[148:151],a[244:247]
v_mfma_f32_16x16x32_bf16 a[244:247],v[80:83],v[132:135],a[244:247]
v_mfma_f32_16x16x32_bf16 a[248:251],v[80:83],v[196:199],a[248:251]
v_mfma_f32_16x16x32_bf16 a[248:251],v[84:87],v[212:215],a[248:251]
v_mfma_f32_16x16x32_bf16 a[248:251],v[88:91],v[228:231],a[248:251]
v_mfma_f32_16x16x32_bf16 a[248:251],v[92:95],v[244:247],a[248:251]
buffer_store_dwordx4 v[8:11],v28,s[36:39],s74 offen offset:3072 sc1
v_mfma_f32_16x16x32_bf16 a[252:255],v[92:95],v[188:191],a[252:255]
ds_read_b64_tr_b16 v[92:93],v2 offset:30880
ds_read_b64_tr_b16 v[94:95],v2 offset:36000
v_mfma_f32_16x16x32_bf16 a[252:255],v[88:91],v[172:175],a[252:255]
ds_read_b64_tr_b16 v[88:89],v2 offset:20640
v_mfma_f32_16x16x32_bf16 a[252:255],v[84:87],v[156:159],a[252:255]
ds_read_b64_tr_b16 v[84:85],v2 offset:10400
v_mfma_f32_16x16x32_bf16 a[252:255],v[80:83],v[140:143],a[252:255]
ds_read_b64_tr_b16 v[80:81],v2 offset:160
v_mfma_f32_16x16x32_bf16 a[224:227],v[96:99],a[128:131],a[224:227]
ds_read_b64_tr_b16 v[82:83],v2 offset:5280
ds_read_b64_tr_b16 v[86:87],v2 offset:15520
v_mfma_f32_16x16x32_bf16 a[224:227],v[100:103],a[144:147],a[224:227]
ds_read_b64_tr_b16 v[90:91],v2 offset:25760
v_add_u32 v2,0x1e000,v2
v_mfma_f32_16x16x32_bf16 a[224:227],v[104:107],a[160:163],a[224:227]
v_mfma_f32_16x16x32_bf16 a[224:227],v[108:111],a[176:179],a[224:227]
v_mfma_f32_16x16x32_bf16 a[228:231],v[108:111],a[180:183],a[228:231]
v_mfma_f32_16x16x32_bf16 a[228:231],v[104:107],a[164:167],a[228:231]
v_mfma_f32_16x16x32_bf16 a[228:231],v[100:103],a[148:151],a[228:231]
v_mfma_f32_16x16x32_bf16 a[228:231],v[96:99],a[132:135],a[228:231]
v_mfma_f32_16x16x32_bf16 a[232:235],v[96:99],a[136:139],a[232:235]
v_mfma_f32_16x16x32_bf16 a[232:235],v[100:103],a[152:155],a[232:235]
v_mfma_f32_16x16x32_bf16 a[232:235],v[104:107],a[168:171],a[232:235]
v_mfma_f32_16x16x32_bf16 a[232:235],v[108:111],a[184:187],a[232:235]
v_accvgpr_read_b32 v16,a224
v_accvgpr_read_b32 v17,a225
v_mfma_f32_16x16x32_bf16 a[236:239],v[108:111],a[188:191],a[236:239]
v_accvgpr_read_b32 v18,a226
v_accvgpr_read_b32 v19,a227
v_mfma_f32_16x16x32_bf16 a[236:239],v[104:107],a[172:175],a[236:239]
v_cvt_pk_bf16_f32 v16,v16,v17
v_cvt_pk_bf16_f32 v17,v18,v19
v_mfma_f32_16x16x32_bf16 a[236:239],v[100:103],a[156:159],a[236:239]
v_accvgpr_read_b32 v8,a228
v_accvgpr_read_b32 v9,a229
v_mfma_f32_16x16x32_bf16 a[236:239],v[96:99],a[140:143],a[236:239]
v_accvgpr_read_b32 v10,a230
v_accvgpr_read_b32 v11,a231
v_mfma_f32_16x16x32_bf16 a[240:243],v[112:115],a[128:131],a[240:243]
v_cvt_pk_bf16_f32 v8,v8,v9
v_cvt_pk_bf16_f32 v9,v10,v11
v_mfma_f32_16x16x32_bf16 a[240:243],v[116:119],a[144:147],a[240:243]
s_waitcnt lgkmcnt(0)
ds_read_b64_tr_b16 v[96:97],v1 offset:128
ds_read_b64_tr_b16 v[98:99],v1 offset:5248
v_mfma_f32_16x16x32_bf16 a[240:243],v[120:123],a[160:163],a[240:243]
ds_read_b64_tr_b16 v[100:101],v1 offset:10368
v_mfma_f32_16x16x32_bf16 a[240:243],v[124:127],a[176:179],a[240:243]
ds_read_b64_tr_b16 v[102:103],v1 offset:15488
v_mfma_f32_16x16x32_bf16 a[244:247],v[124:127],a[180:183],a[244:247]
ds_read_b64_tr_b16 v[104:105],v1 offset:20608
v_mfma_f32_16x16x32_bf16 a[244:247],v[120:123],a[164:167],a[244:247]
ds_read_b64_tr_b16 v[106:107],v1 offset:25728
ds_read_b64_tr_b16 v[108:109],v1 offset:30848
v_mfma_f32_16x16x32_bf16 a[244:247],v[116:119],a[148:151],a[244:247]
)A"
R"A(ds_read_b64_tr_b16 v[110:111],v1 offset:35968
v_add_u32 v1,0xfffe2000,v1
v_mfma_f32_16x16x32_bf16 a[244:247],v[112:115],a[132:135],a[244:247]
v_accvgpr_read_b32 v20,a240
v_accvgpr_read_b32 v21,a241
v_mfma_f32_16x16x32_bf16 a[248:251],v[112:115],a[136:139],a[248:251]
v_accvgpr_read_b32 v22,a242
v_accvgpr_read_b32 v23,a243
v_mfma_f32_16x16x32_bf16 a[248:251],v[116:119],a[152:155],a[248:251]
v_cvt_pk_bf16_f32 v18,v20,v21
v_cvt_pk_bf16_f32 v19,v22,v23
v_mfma_f32_16x16x32_bf16 a[248:251],v[120:123],a[168:171],a[248:251]
buffer_store_dwordx4 v[16:19],v6,s[36:39],s75 offen sc1
v_mfma_f32_16x16x32_bf16 a[248:251],v[124:127],a[184:187],a[248:251]
v_accvgpr_read_b32 v12,a244
v_accvgpr_read_b32 v13,a245
v_mfma_f32_16x16x32_bf16 a[252:255],v[124:127],a[188:191],a[252:255]
v_accvgpr_read_b32 v14,a246
v_accvgpr_read_b32 v15,a247
v_mfma_f32_16x16x32_bf16 a[252:255],v[120:123],a[172:175],a[252:255]
v_cvt_pk_bf16_f32 v10,v12,v13
v_cvt_pk_bf16_f32 v11,v14,v15
v_mfma_f32_16x16x32_bf16 a[252:255],v[116:119],a[156:159],a[252:255]
v_accvgpr_read_b32 v16,a232
v_accvgpr_read_b32 v17,a233
v_mfma_f32_16x16x32_bf16 a[252:255],v[112:115],a[140:143],a[252:255]
v_accvgpr_read_b32 v18,a234
v_accvgpr_read_b32 v19,a235
v_mfma_f32_16x16x32_bf16 a[0:3],v[64:67],v[32:35],a[0:3]
v_accvgpr_read_b32 v20,a248
v_accvgpr_read_b32 v21,a249
v_mfma_f32_16x16x32_bf16 a[0:3],v[68:71],v[36:39],a[0:3]
v_accvgpr_read_b32 v22,a250
v_accvgpr_read_b32 v23,a251
v_mfma_f32_16x16x32_bf16 a[0:3],v[72:75],v[40:43],a[0:3]
v_cvt_pk_bf16_f32 v16,v16,v17
v_cvt_pk_bf16_f32 v17,v18,v19
v_mfma_f32_16x16x32_bf16 a[0:3],v[76:79],v[44:47],a[0:3]
v_cvt_pk_bf16_f32 v18,v20,v21
v_cvt_pk_bf16_f32 v19,v22,v23
v_mfma_f32_16x16x32_bf16 a[4:7],v[76:79],v[180:183],a[4:7]
ds_read_b64_tr_b16 v[112:113],v2 offset:160
ds_read_b64_tr_b16 v[114:115],v2 offset:5280
v_mfma_f32_16x16x32_bf16 a[4:7],v[72:75],v[164:167],a[4:7]
buffer_store_dwordx4 v[8:11],v6,s[36:39],s75 offen offset:1024 sc1
v_mfma_f32_16x16x32_bf16 a[4:7],v[68:71],v[148:151],a[4:7]
ds_read_b64_tr_b16 v[116:117],v2 offset:10400
ds_read_b64_tr_b16 v[118:119],v2 offset:15520
v_mfma_f32_16x16x32_bf16 a[4:7],v[64:67],v[132:135],a[4:7]
ds_read_b64_tr_b16 v[120:121],v2 offset:20640
v_accvgpr_read_b32 v8,a236
v_mfma_f32_16x16x32_bf16 a[8:11],v[64:67],v[196:199],a[8:11]
ds_read_b64_tr_b16 v[122:123],v2 offset:25760
v_accvgpr_read_b32 v9,a237
v_mfma_f32_16x16x32_bf16 a[8:11],v[68:71],v[212:215],a[8:11]
ds_read_b64_tr_b16 v[124:125],v2 offset:30880
v_accvgpr_read_b32 v10,a238
v_mfma_f32_16x16x32_bf16 a[8:11],v[72:75],v[228:231],a[8:11]
ds_read_b64_tr_b16 v[126:127],v2 offset:36000
v_accvgpr_read_b32 v11,a239
v_mfma_f32_16x16x32_bf16 a[8:11],v[76:79],v[244:247],a[8:11]
v_accvgpr_read_b32 v12,a252
v_accvgpr_read_b32 v13,a253
v_mfma_f32_16x16x32_bf16 a[12:15],v[76:79],v[188:191],a[12:15]
v_accvgpr_read_b32 v14,a254
v_accvgpr_read_b32 v15,a255
v_mfma_f32_16x16x32_bf16 a[12:15],v[72:75],v[172:175],a[12:15]
v_cvt_pk_bf16_f32 v8,v8,v9
v_cvt_pk_bf16_f32 v9,v10,v11
v_mfma_f32_16x16x32_bf16 a[12:15],v[68:71],v[156:159],a[12:15]
v_cvt_pk_bf16_f32 v10,v12,v13
v_cvt_pk_bf16_f32 v11,v14,v15
v_mfma_f32_16x16x32_bf16 a[12:15],v[64:67],v[140:143],a[12:15]
ds_read_b64_tr_b16 v[64:65],v1 offset:192
ds_read_b64_tr_b16 v[66:67],v1 offset:5312
v_mfma_f32_16x16x32_bf16 a[16:19],v[80:83],v[32:35],a[16:19]
buffer_store_dwordx4 v[16:19],v6,s[36:39],s75 offen offset:2048 sc1
v_mfma_f32_16x16x32_bf16 a[16:19],v[84:87],v[36:39],a[16:19]
ds_read_b64_tr_b16 v[68:69],v1 offset:10432
ds_read_b64_tr_b16 v[70:71],v1 offset:15552
v_mfma_f32_16x16x32_bf16 a[16:19],v[88:91],v[40:43],a[16:19]
ds_read_b64_tr_b16 v[72:73],v1 offset:20672
v_add_u32 v2,0xfffe2000,v2
v_mfma_f32_16x16x32_bf16 a[16:19],v[92:95],v[44:47],a[16:19]
ds_read_b64_tr_b16 v[74:75],v1 offset:25792
v_mfma_f32_16x16x32_bf16 a[20:23],v[92:95],v[180:183],a[20:23]
ds_read_b64_tr_b16 v[76:77],v1 offset:30912
v_mfma_f32_16x16x32_bf16 a[20:23],v[88:91],v[164:167],a[20:23]
ds_read_b64_tr_b16 v[78:79],v1 offset:36032
v_add_u32 v1,0x1e000,v1
v_mfma_f32_16x16x32_bf16 a[20:23],v[84:87],v[148:151],a[20:23]
v_mfma_f32_16x16x32_bf16 a[20:23],v[80:83],v[132:135],a[20:23]
v_mfma_f32_16x16x32_bf16 a[24:27],v[80:83],v[196:199],a[24:27]
v_mfma_f32_16x16x32_bf16 a[24:27],v[84:87],v[212:215],a[24:27]
v_mfma_f32_16x16x32_bf16 a[24:27],v[88:91],v[228:231],a[24:27]
v_mfma_f32_16x16x32_bf16 a[24:27],v[92:95],v[244:247],a[24:27]
buffer_store_dwordx4 v[8:11],v28,s[36:39],s75 offen offset:3072 sc1
v_mfma_f32_16x16x32_bf16 a[28:31],v[92:95],v[188:191],a[28:31]
ds_read_b64_tr_b16 v[92:93],v2 offset:30944
ds_read_b64_tr_b16 v[94:95],v2 offset:36064
v_mfma_f32_16x16x32_bf16 a[28:31],v[88:91],v[172:175],a[28:31]
ds_read_b64_tr_b16 v[88:89],v2 offset:20704
v_mfma_f32_16x16x32_bf16 a[28:31],v[84:87],v[156:159],a[28:31]
ds_read_b64_tr_b16 v[84:85],v2 offset:10464
v_mfma_f32_16x16x32_bf16 a[28:31],v[80:83],v[140:143],a[28:31]
s_waitcnt lgkmcnt(4)
ds_read_b64_tr_b16 v[80:81],v2 offset:224
v_mfma_f32_16x16x32_bf16 a[0:3],v[96:99],a[128:131],a[0:3]
ds_read_b64_tr_b16 v[82:83],v2 offset:5344
ds_read_b64_tr_b16 v[86:87],v2 offset:15584
v_mfma_f32_16x16x32_bf16 a[0:3],v[100:103],a[144:147],a[0:3]
ds_read_b64_tr_b16 v[90:91],v2 offset:25824
v_add_u32 v2,0x1e000,v2
v_mfma_f32_16x16x32_bf16 a[0:3],v[104:107],a[160:163],a[0:3]
v_mfma_f32_16x16x32_bf16 a[0:3],v[108:111],a[176:179],a[0:3]
v_mfma_f32_16x16x32_bf16 a[4:7],v[108:111],a[180:183],a[4:7]
v_mfma_f32_16x16x32_bf16 a[4:7],v[104:107],a[164:167],a[4:7]
v_mfma_f32_16x16x32_bf16 a[4:7],v[100:103],a[148:151],a[4:7]
v_mfma_f32_16x16x32_bf16 a[4:7],v[96:99],a[132:135],a[4:7]
v_mfma_f32_16x16x32_bf16 a[8:11],v[96:99],a[136:139],a[8:11]
v_mfma_f32_16x16x32_bf16 a[8:11],v[100:103],a[152:155],a[8:11]
v_mfma_f32_16x16x32_bf16 a[8:11],v[104:107],a[168:171],a[8:11]
v_mfma_f32_16x16x32_bf16 a[8:11],v[108:111],a[184:187],a[8:11]
v_accvgpr_read_b32 v16,a0
v_accvgpr_read_b32 v17,a1
v_mfma_f32_16x16x32_bf16 a[12:15],v[108:111],a[188:191],a[12:15]
v_accvgpr_read_b32 v18,a2
v_accvgpr_read_b32 v19,a3
v_mfma_f32_16x16x32_bf16 a[12:15],v[104:107],a[172:175],a[12:15]
v_cvt_pk_bf16_f32 v16,v16,v17
v_cvt_pk_bf16_f32 v17,v18,v19
v_mfma_f32_16x16x32_bf16 a[12:15],v[100:103],a[156:159],a[12:15]
v_accvgpr_read_b32 v8,a4
v_accvgpr_read_b32 v9,a5
v_mfma_f32_16x16x32_bf16 a[12:15],v[96:99],a[140:143],a[12:15]
v_accvgpr_read_b32 v10,a6
v_accvgpr_read_b32 v11,a7
v_mfma_f32_16x16x32_bf16 a[16:19],v[112:115],a[128:131],a[16:19]
v_cvt_pk_bf16_f32 v8,v8,v9
v_cvt_pk_bf16_f32 v9,v10,v11
v_mfma_f32_16x16x32_bf16 a[16:19],v[116:119],a[144:147],a[16:19]
ds_read_b64_tr_b16 v[96:97],v1 offset:192
ds_read_b64_tr_b16 v[98:99],v1 offset:5312
v_mfma_f32_16x16x32_bf16 a[16:19],v[120:123],a[160:163],a[16:19]
ds_read_b64_tr_b16 v[100:101],v1 offset:10432
v_mfma_f32_16x16x32_bf16 a[16:19],v[124:127],a[176:179],a[16:19]
ds_read_b64_tr_b16 v[102:103],v1 offset:15552
v_mfma_f32_16x16x32_bf16 a[20:23],v[124:127],a[180:183],a[20:23]
ds_read_b64_tr_b16 v[104:105],v1 offset:20672
v_mfma_f32_16x16x32_bf16 a[20:23],v[120:123],a[164:167],a[20:23]
ds_read_b64_tr_b16 v[106:107],v1 offset:25792
ds_read_b64_tr_b16 v[108:109],v1 offset:30912
v_mfma_f32_16x16x32_bf16 a[20:23],v[116:119],a[148:151],a[20:23]
ds_read_b64_tr_b16 v[110:111],v1 offset:36032
v_add_u32 v1,0xfffec000,v1
v_mfma_f32_16x16x32_bf16 a[20:23],v[112:115],a[132:135],a[20:23]
v_accvgpr_read_b32 v20,a16
v_accvgpr_read_b32 v21,a17
v_mfma_f32_16x16x32_bf16 a[24:27],v[112:115],a[136:139],a[24:27]
v_accvgpr_read_b32 v22,a18
v_accvgpr_read_b32 v23,a19
v_mfma_f32_16x16x32_bf16 a[24:27],v[116:119],a[152:155],a[24:27]
v_cvt_pk_bf16_f32 v18,v20,v21
v_cvt_pk_bf16_f32 v19,v22,v23
v_mfma_f32_16x16x32_bf16 a[24:27],v[120:123],a[168:171],a[24:27]
buffer_store_dwordx4 v[16:19],v6,s[36:39],s76 offen sc1
v_mfma_f32_16x16x32_bf16 a[24:27],v[124:127],a[184:187],a[24:27]
v_accvgpr_read_b32 v12,a20
v_accvgpr_read_b32 v13,a21
v_mfma_f32_16x16x32_bf16 a[28:31],v[124:127],a[188:191],a[28:31]
v_accvgpr_read_b32 v14,a22
s_nop 0
L_pd18_%=:
s_cbranch_execz L_trtg2F_%=
v_accvgpr_read_b32 v15,a23
v_mfma_f32_16x16x32_bf16 a[28:31],v[120:123],a[172:175],a[28:31]
v_cvt_pk_bf16_f32 v10,v12,v13
v_cvt_pk_bf16_f32 v11,v14,v15
v_mfma_f32_16x16x32_bf16 a[28:31],v[116:119],a[156:159],a[28:31]
v_accvgpr_read_b32 v16,a8
v_accvgpr_read_b32 v17,a9
v_mfma_f32_16x16x32_bf16 a[28:31],v[112:115],a[140:143],a[28:31]
v_accvgpr_read_b32 v18,a10
v_accvgpr_read_b32 v19,a11
v_mfma_f32_16x16x32_bf16 v[128:131],v[64:67],v[32:35],v[128:131]
v_accvgpr_read_b32 v20,a24
v_accvgpr_read_b32 v21,a25
v_mfma_f32_16x16x32_bf16 v[128:131],v[68:71],v[36:39],v[128:131]
v_accvgpr_read_b32 v22,a26
v_accvgpr_read_b32 v23,a27
v_mfma_f32_16x16x32_bf16 v[128:131],v[72:75],v[40:43],v[128:131]
v_cvt_pk_bf16_f32 v16,v16,v17
v_cvt_pk_bf16_f32 v17,v18,v19
v_mfma_f32_16x16x32_bf16 v[128:131],v[76:79],v[44:47],v[128:131]
v_cvt_pk_bf16_f32 v18,v20,v21
v_cvt_pk_bf16_f32 v19,v22,v23
v_mfma_f32_16x16x32_bf16 v[136:139],v[76:79],v[180:183],v[136:139]
ds_read_b64_tr_b16 v[112:113],v2 offset:224
ds_read_b64_tr_b16 v[114:115],v2 offset:5344
v_mfma_f32_16x16x32_bf16 v[136:139],v[72:75],v[164:167],v[136:139]
buffer_store_dwordx4 v[8:11],v6,s[36:39],s76 offen offset:1024 sc1
v_mfma_f32_16x16x32_bf16 v[136:139],v[68:71],v[148:151],v[136:139]
ds_read_b64_tr_b16 v[116:117],v2 offset:10464
ds_read_b64_tr_b16 v[118:119],v2 offset:15584
v_mfma_f32_16x16x32_bf16 v[136:139],v[64:67],v[132:135],v[136:139]
ds_read_b64_tr_b16 v[120:121],v2 offset:20704
v_accvgpr_read_b32 v8,a12
v_mfma_f32_16x16x32_bf16 v[144:147],v[64:67],v[196:199],v[144:147]
ds_read_b64_tr_b16 v[122:123],v2 offset:25824
v_accvgpr_read_b32 v9,a13
v_mfma_f32_16x16x32_bf16 v[144:147],v[68:71],v[212:215],v[144:147]
ds_read_b64_tr_b16 v[124:125],v2 offset:30944
v_accvgpr_read_b32 v10,a14
v_mfma_f32_16x16x32_bf16 v[144:147],v[72:75],v[228:231],v[144:147]
ds_read_b64_tr_b16 v[126:127],v2 offset:36064
v_accvgpr_read_b32 v11,a15
v_mfma_f32_16x16x32_bf16 v[144:147],v[76:79],v[244:247],v[144:147]
v_accvgpr_read_b32 v12,a28
v_accvgpr_read_b32 v13,a29
v_mfma_f32_16x16x32_bf16 v[152:155],v[76:79],v[188:191],v[152:155]
v_accvgpr_read_b32 v14,a30
v_accvgpr_read_b32 v15,a31
v_mfma_f32_16x16x32_bf16 v[152:155],v[72:75],v[172:175],v[152:155]
v_cvt_pk_bf16_f32 v8,v8,v9
v_cvt_pk_bf16_f32 v9,v10,v11
v_mfma_f32_16x16x32_bf16 v[152:155],v[68:71],v[156:159],v[152:155]
v_cvt_pk_bf16_f32 v10,v12,v13
v_cvt_pk_bf16_f32 v11,v14,v15
v_mfma_f32_16x16x32_bf16 v[152:155],v[64:67],v[140:143],v[152:155]
s_waitcnt lgkmcnt(3)
ds_read_b64_tr_b16 v[64:65],v1 offset:256
ds_read_b64_tr_b16 v[66:67],v1 offset:5376
v_mfma_f32_16x16x32_bf16 v[160:163],v[80:83],v[32:35],v[160:163]
buffer_store_dwordx4 v[16:19],v6,s[36:39],s76 offen offset:2048 sc1
v_mfma_f32_16x16x32_bf16 v[160:163],v[84:87],v[36:39],v[160:163]
ds_read_b64_tr_b16 v[68:69],v1 offset:10496
ds_read_b64_tr_b16 v[70:71],v1 offset:15616
v_mfma_f32_16x16x32_bf16 v[160:163],v[88:91],v[40:43],v[160:163]
ds_read_b64_tr_b16 v[72:73],v1 offset:20736
v_add_u32 v2,0xfffec000,v2
v_mfma_f32_16x16x32_bf16 v[160:163],v[92:95],v[44:47],v[160:163]
ds_read_b64_tr_b16 v[74:75],v1 offset:25856
v_mov_b32 v20,s52
v_mfma_f32_16x16x32_bf16 v[168:171],v[92:95],v[180:183],v[168:171]
ds_read_b64_tr_b16 v[76:77],v1 offset:30976
v_mov_b32 v21,s53
v_mfma_f32_16x16x32_bf16 v[168:171],v[88:91],v[164:167],v[168:171]
ds_read_b64_tr_b16 v[78:79],v1 offset:36096
v_add_u32 v1,0xffff6000,v1
v_mfma_f32_16x16x32_bf16 v[168:171],v[84:87],v[148:151],v[168:171]
v_lshlrev_b32 v22,20,v6
v_mfma_f32_16x16x32_bf16 v[168:171],v[80:83],v[132:135],v[168:171]
v_mfma_f32_16x16x32_bf16 v[176:179],v[80:83],v[196:199],v[176:179]
v_mfma_f32_16x16x32_bf16 v[176:179],v[84:87],v[212:215],v[176:179]
v_mfma_f32_16x16x32_bf16 v[176:179],v[88:91],v[228:231],v[176:179]
v_mfma_f32_16x16x32_bf16 v[176:179],v[92:95],v[244:247],v[176:179]
buffer_store_dwordx4 v[8:11],v28,s[36:39],s76 offen offset:3072 sc1
v_mfma_f32_16x16x32_bf16 v[184:187],v[92:95],v[188:191],v[184:187]
ds_read_b64_tr_b16 v[92:93],v2 offset:31008
ds_read_b64_tr_b16 v[94:95],v2 offset:36128
)A"
R"A(v_mfma_f32_16x16x32_bf16 v[184:187],v[88:91],v[172:175],v[184:187]
ds_read_b64_tr_b16 v[88:89],v2 offset:20768
v_mfma_f32_16x16x32_bf16 v[184:187],v[84:87],v[156:159],v[184:187]
ds_read_b64_tr_b16 v[84:85],v2 offset:10528
v_mfma_f32_16x16x32_bf16 v[184:187],v[80:83],v[140:143],v[184:187]
ds_read_b64_tr_b16 v[80:81],v2 offset:288
v_mfma_f32_16x16x32_bf16 v[128:131],v[96:99],a[128:131],v[128:131]
ds_read_b64_tr_b16 v[82:83],v2 offset:5408
ds_read_b64_tr_b16 v[86:87],v2 offset:15648
v_mfma_f32_16x16x32_bf16 v[128:131],v[100:103],a[144:147],v[128:131]
ds_read_b64_tr_b16 v[90:91],v2 offset:25888
v_add_u32 v2,0xffff6000,v2
v_mfma_f32_16x16x32_bf16 v[128:131],v[104:107],a[160:163],v[128:131]
v_mfma_f32_16x16x32_bf16 v[128:131],v[108:111],a[176:179],v[128:131]
v_mfma_f32_16x16x32_bf16 v[136:139],v[108:111],a[180:183],v[136:139]
v_mfma_f32_16x16x32_bf16 v[136:139],v[104:107],a[164:167],v[136:139]
v_mfma_f32_16x16x32_bf16 v[136:139],v[100:103],a[148:151],v[136:139]
v_mfma_f32_16x16x32_bf16 v[136:139],v[96:99],a[132:135],v[136:139]
v_mfma_f32_16x16x32_bf16 v[144:147],v[96:99],a[136:139],v[144:147]
v_mfma_f32_16x16x32_bf16 v[144:147],v[100:103],a[152:155],v[144:147]
v_mfma_f32_16x16x32_bf16 v[144:147],v[104:107],a[168:171],v[144:147]
v_mfma_f32_16x16x32_bf16 v[144:147],v[108:111],a[184:187],v[144:147]
v_cvt_pk_bf16_f32 v128,v128,v129
v_cvt_pk_bf16_f32 v129,v130,v131
v_mfma_f32_16x16x32_bf16 v[152:155],v[108:111],a[188:191],v[152:155]
ds_read_b64_tr_b16 v[108:109],v1 offset:30976
ds_read_b64_tr_b16 v[110:111],v1 offset:36096
v_mfma_f32_16x16x32_bf16 v[152:155],v[104:107],a[172:175],v[152:155]
v_cvt_pk_bf16_f32 v136,v136,v137
v_cvt_pk_bf16_f32 v137,v138,v139
v_mfma_f32_16x16x32_bf16 v[152:155],v[100:103],a[156:159],v[152:155]
v_cvt_pk_bf16_f32 v144,v144,v145
v_cvt_pk_bf16_f32 v145,v146,v147
v_mfma_f32_16x16x32_bf16 v[152:155],v[96:99],a[140:143],v[152:155]
ds_read_b64_tr_b16 v[96:97],v1 offset:256
ds_read_b64_tr_b16 v[98:99],v1 offset:5376
v_mfma_f32_16x16x32_bf16 v[160:163],v[112:115],a[128:131],v[160:163]
ds_read_b64_tr_b16 v[100:101],v1 offset:10496
v_mfma_f32_16x16x32_bf16 v[160:163],v[116:119],a[144:147],v[160:163]
s_waitcnt lgkmcnt(5)
ds_read_b64_tr_b16 v[102:103],v1 offset:15616
v_mfma_f32_16x16x32_bf16 v[160:163],v[120:123],a[160:163],v[160:163]
ds_read_b64_tr_b16 v[104:105],v1 offset:20736
v_mfma_f32_16x16x32_bf16 v[160:163],v[124:127],a[176:179],v[160:163]
v_cvt_pk_bf16_f32 v152,v152,v153
v_cvt_pk_bf16_f32 v153,v154,v155
v_mfma_f32_16x16x32_bf16 v[168:171],v[124:127],a[180:183],v[168:171]
ds_read_b64_tr_b16 v[106:107],v1 offset:25856
v_add_u32 v1,0x1e000,v1
v_mfma_f32_16x16x32_bf16 v[168:171],v[120:123],a[164:167],v[168:171]
v_mfma_f32_16x16x32_bf16 v[168:171],v[116:119],a[148:151],v[168:171]
v_mfma_f32_16x16x32_bf16 v[168:171],v[112:115],a[132:135],v[168:171]
v_cvt_pk_bf16_f32 v130,v160,v161
v_cvt_pk_bf16_f32 v131,v162,v163
v_mfma_f32_16x16x32_bf16 v[176:179],v[112:115],a[136:139],v[176:179]
buffer_store_dwordx4 v[128:131],v6,s[36:39],s79 offen sc1
v_mfma_f32_16x16x32_bf16 v[176:179],v[116:119],a[152:155],v[176:179]
v_mfma_f32_16x16x32_bf16 v[176:179],v[120:123],a[168:171],v[176:179]
v_mfma_f32_16x16x32_bf16 v[176:179],v[124:127],a[184:187],v[176:179]
v_mfma_f32_16x16x32_bf16 v[184:187],v[124:127],a[188:191],v[184:187]
v_cvt_pk_bf16_f32 v138,v168,v169
v_cvt_pk_bf16_f32 v139,v170,v171
v_mfma_f32_16x16x32_bf16 v[184:187],v[120:123],a[172:175],v[184:187]
buffer_store_dwordx4 v[136:139],v6,s[36:39],s80 offen sc1
v_mfma_f32_16x16x32_bf16 v[184:187],v[116:119],a[156:159],v[184:187]
ds_read_b64_tr_b16 v[116:117],v2 offset:10528
ds_read_b64_tr_b16 v[118:119],v2 offset:15648
v_mfma_f32_16x16x32_bf16 v[184:187],v[112:115],a[140:143],v[184:187]
v_cvt_pk_bf16_f32 v146,v176,v177
v_cvt_pk_bf16_f32 v147,v178,v179
s_waitcnt vmcnt(6)
s_barrier
v_mfma_f32_16x16x32_bf16 v[192:195],v[64:67],a[68:71],v[192:195]
buffer_store_dwordx4 v[144:147],v6,s[36:39],s81 offen sc1
v_mfma_f32_16x16x32_bf16 v[192:195],v[68:71],a[76:79],v[192:195]
ds_read_b64_tr_b16 v[112:113],v2 offset:288
ds_read_b64_tr_b16 v[114:115],v2 offset:5408
v_mfma_f32_16x16x32_bf16 v[192:195],v[72:75],a[84:87],v[192:195]
buffer_store_dwordx2 v[58:59],v60,s[44:47],s18 offen offset:2312 sc1
v_mfma_f32_16x16x32_bf16 v[192:195],v[76:79],a[92:95],v[192:195]
v_cvt_pk_bf16_f32 v154,v184,v185
v_cvt_pk_bf16_f32 v155,v186,v187
v_mfma_f32_16x16x32_bf16 v[200:203],v[76:79],a[124:127],v[200:203]
buffer_store_dwordx4 v[152:155],v28,s[36:39],s82 offen sc1
v_mfma_f32_16x16x32_bf16 v[200:203],v[72:75],a[116:119],v[200:203]
ds_read_b64_tr_b16 v[120:121],v2 offset:20768
ds_read_b64_tr_b16 v[122:123],v2 offset:25888
v_mfma_f32_16x16x32_bf16 v[200:203],v[68:71],a[108:111],v[200:203]
ds_read_b64_tr_b16 v[124:125],v2 offset:31008
v_mfma_f32_16x16x32_bf16 v[200:203],v[64:67],a[100:103],v[200:203]
ds_read_b64_tr_b16 v[126:127],v2 offset:36128
v_add_u32 v2,0x1e000,v2
v_mfma_f32_16x16x32_bf16 v[208:211],v[64:67],a[64:67],v[208:211]
v_mfma_f32_16x16x32_bf16 v[208:211],v[68:71],a[72:75],v[208:211]
v_mfma_f32_16x16x32_bf16 v[208:211],v[72:75],a[80:83],v[208:211]
v_mfma_f32_16x16x32_bf16 v[208:211],v[76:79],a[88:91],v[208:211]
v_mfma_f32_16x16x32_bf16 v[216:219],v[76:79],a[120:123],v[216:219]
ds_read_b64_tr_b16 v[76:77],v1 offset:30976
ds_read_b64_tr_b16 v[78:79],v1 offset:36096
v_mfma_f32_16x16x32_bf16 v[216:219],v[72:75],a[112:115],v[216:219]
ds_read_b64_tr_b16 v[72:73],v1 offset:20736
v_mfma_f32_16x16x32_bf16 v[216:219],v[68:71],a[104:107],v[216:219]
ds_read_b64_tr_b16 v[68:69],v1 offset:10496
v_mfma_f32_16x16x32_bf16 v[216:219],v[64:67],a[96:99],v[216:219]
ds_read_b64_tr_b16 v[64:65],v1 offset:256
v_mfma_f32_16x16x32_bf16 v[224:227],v[80:83],a[68:71],v[224:227]
ds_read_b64_tr_b16 v[66:67],v1 offset:5376
ds_read_b64_tr_b16 v[70:71],v1 offset:15616
v_mfma_f32_16x16x32_bf16 v[224:227],v[84:87],a[76:79],v[224:227]
ds_read_b64_tr_b16 v[74:75],v1 offset:25856
v_mfma_f32_16x16x32_bf16 v[224:227],v[88:91],a[84:87],v[224:227]
v_mfma_f32_16x16x32_bf16 v[224:227],v[92:95],a[92:95],v[224:227]
v_mfma_f32_16x16x32_bf16 v[232:235],v[92:95],a[124:127],v[232:235]
v_mfma_f32_16x16x32_bf16 v[232:235],v[88:91],a[116:119],v[232:235]
v_mfma_f32_16x16x32_bf16 v[232:235],v[84:87],a[108:111],v[232:235]
v_mfma_f32_16x16x32_bf16 v[232:235],v[80:83],a[100:103],v[232:235]
v_mfma_f32_16x16x32_bf16 v[240:243],v[80:83],a[64:67],v[240:243]
v_mfma_f32_16x16x32_bf16 v[240:243],v[84:87],a[72:75],v[240:243]
v_mfma_f32_16x16x32_bf16 v[240:243],v[88:91],a[80:83],v[240:243]
v_mfma_f32_16x16x32_bf16 v[240:243],v[92:95],a[88:91],v[240:243]
v_mfma_f32_16x16x32_bf16 v[248:251],v[92:95],a[120:123],v[248:251]
ds_read_b64_tr_b16 v[92:93],v2 offset:31008
ds_read_b64_tr_b16 v[94:95],v2 offset:36128
v_mfma_f32_16x16x32_bf16 v[248:251],v[88:91],a[112:115],v[248:251]
ds_read_b64_tr_b16 v[88:89],v2 offset:20768
v_mfma_f32_16x16x32_bf16 v[248:251],v[84:87],a[104:107],v[248:251]
ds_read_b64_tr_b16 v[84:85],v2 offset:10528
v_mfma_f32_16x16x32_bf16 v[248:251],v[80:83],a[96:99],v[248:251]
s_waitcnt lgkmcnt(4)
ds_read_b64_tr_b16 v[80:81],v2 offset:288
v_mfma_f32_16x16x32_bf16 v[192:195],v[96:99],v[32:35],v[192:195]
ds_read_b64_tr_b16 v[82:83],v2 offset:5408
ds_read_b64_tr_b16 v[86:87],v2 offset:15648
v_mfma_f32_16x16x32_bf16 v[192:195],v[100:103],v[36:39],v[192:195]
ds_read_b64_tr_b16 v[90:91],v2 offset:25888
v_mfma_f32_16x16x32_bf16 v[192:195],v[104:107],v[40:43],v[192:195]
v_mfma_f32_16x16x32_bf16 v[192:195],v[108:111],v[44:47],v[192:195]
v_mfma_f32_16x16x32_bf16 v[200:203],v[108:111],v[180:183],v[200:203]
v_mfma_f32_16x16x32_bf16 v[200:203],v[104:107],v[164:167],v[200:203]
v_mfma_f32_16x16x32_bf16 v[200:203],v[100:103],v[148:151],v[200:203]
v_mfma_f32_16x16x32_bf16 v[200:203],v[96:99],v[132:135],v[200:203]
v_mfma_f32_16x16x32_bf16 v[208:211],v[96:99],v[196:199],v[208:211]
v_mfma_f32_16x16x32_bf16 v[208:211],v[100:103],v[212:215],v[208:211]
v_mfma_f32_16x16x32_bf16 v[208:211],v[104:107],v[228:231],v[208:211]
v_mfma_f32_16x16x32_bf16 v[208:211],v[108:111],v[244:247],v[208:211]
v_mfma_f32_16x16x32_bf16 v[216:219],v[108:111],v[188:191],v[216:219]
v_mfma_f32_16x16x32_bf16 v[216:219],v[104:107],v[172:175],v[216:219]
v_mfma_f32_16x16x32_bf16 v[216:219],v[100:103],v[156:159],v[216:219]
v_mfma_f32_16x16x32_bf16 v[216:219],v[96:99],v[140:143],v[216:219]
v_mfma_f32_16x16x32_bf16 v[224:227],v[112:115],v[32:35],v[224:227]
v_mfma_f32_16x16x32_bf16 v[224:227],v[116:119],v[36:39],v[224:227]
v_mfma_f32_16x16x32_bf16 v[224:227],v[120:123],v[40:43],v[224:227]
v_mfma_f32_16x16x32_bf16 v[224:227],v[124:127],v[44:47],v[224:227]
v_mfma_f32_16x16x32_bf16 v[232:235],v[124:127],v[180:183],v[232:235]
v_mfma_f32_16x16x32_bf16 v[232:235],v[120:123],v[164:167],v[232:235]
v_mfma_f32_16x16x32_bf16 v[232:235],v[116:119],v[148:151],v[232:235]
v_mfma_f32_16x16x32_bf16 v[232:235],v[112:115],v[132:135],v[232:235]
v_mfma_f32_16x16x32_bf16 v[240:243],v[112:115],v[196:199],v[240:243]
v_mfma_f32_16x16x32_bf16 v[240:243],v[116:119],v[212:215],v[240:243]
v_mfma_f32_16x16x32_bf16 v[240:243],v[120:123],v[228:231],v[240:243]
v_mfma_f32_16x16x32_bf16 v[240:243],v[124:127],v[244:247],v[240:243]
v_mfma_f32_16x16x32_bf16 v[248:251],v[124:127],v[188:191],v[248:251]
v_mfma_f32_16x16x32_bf16 v[248:251],v[120:123],v[172:175],v[248:251]
v_mfma_f32_16x16x32_bf16 v[248:251],v[116:119],v[156:159],v[248:251]
v_mfma_f32_16x16x32_bf16 v[248:251],v[112:115],v[140:143],v[248:251]
s_waitcnt vmcnt(0)
s_barrier
v_mfma_f32_16x16x32_bf16 v[192:195],v[64:67],a[128:131],v[192:195]
buffer_store_dwordx2 v[58:59],v60,s[44:47],s18 offen offset:2304 sc1
v_mfma_f32_16x16x32_bf16 v[192:195],v[68:71],a[144:147],v[192:195]
v_mfma_f32_16x16x32_bf16 v[192:195],v[72:75],a[160:163],v[192:195]
v_mfma_f32_16x16x32_bf16 v[192:195],v[76:79],a[176:179],v[192:195]
v_mfma_f32_16x16x32_bf16 v[200:203],v[76:79],a[180:183],v[200:203]
v_mfma_f32_16x16x32_bf16 v[200:203],v[72:75],a[164:167],v[200:203]
v_mfma_f32_16x16x32_bf16 v[200:203],v[68:71],a[148:151],v[200:203]
v_mfma_f32_16x16x32_bf16 v[200:203],v[64:67],a[132:135],v[200:203]
v_mfma_f32_16x16x32_bf16 v[208:211],v[64:67],a[136:139],v[208:211]
v_mfma_f32_16x16x32_bf16 v[208:211],v[68:71],a[152:155],v[208:211]
v_mfma_f32_16x16x32_bf16 v[208:211],v[72:75],a[168:171],v[208:211]
v_mfma_f32_16x16x32_bf16 v[208:211],v[76:79],a[184:187],v[208:211]
v_mfma_f32_16x16x32_bf16 v[216:219],v[76:79],a[188:191],v[216:219]
v_mfma_f32_16x16x32_bf16 v[216:219],v[72:75],a[172:175],v[216:219]
v_mfma_f32_16x16x32_bf16 v[216:219],v[68:71],a[156:159],v[216:219]
v_mfma_f32_16x16x32_bf16 v[216:219],v[64:67],a[140:143],v[216:219]
s_waitcnt lgkmcnt(0)
v_mfma_f32_16x16x32_bf16 v[224:227],v[80:83],a[128:131],v[224:227]
buffer_load_dwordx2 v[62:63],v61,s[44:47],s91 offen offset:2312 sc1
v_mfma_f32_16x16x32_bf16 v[224:227],v[84:87],a[144:147],v[224:227]
v_mfma_f32_16x16x32_bf16 v[224:227],v[88:91],a[160:163],v[224:227]
v_mfma_f32_16x16x32_bf16 v[224:227],v[92:95],a[176:179],v[224:227]
v_mfma_f32_16x16x32_bf16 v[232:235],v[92:95],a[180:183],v[232:235]
v_mfma_f32_16x16x32_bf16 v[232:235],v[88:91],a[164:167],v[232:235]
v_mfma_f32_16x16x32_bf16 v[232:235],v[84:87],a[148:151],v[232:235]
v_mfma_f32_16x16x32_bf16 v[232:235],v[80:83],a[132:135],v[232:235]
v_mfma_f32_16x16x32_bf16 v[240:243],v[80:83],a[136:139],v[240:243]
v_mfma_f32_16x16x32_bf16 v[240:243],v[84:87],a[152:155],v[240:243]
v_mfma_f32_16x16x32_bf16 v[240:243],v[88:91],a[168:171],v[240:243]
v_mfma_f32_16x16x32_bf16 v[240:243],v[92:95],a[184:187],v[240:243]
v_mfma_f32_16x16x32_bf16 v[248:251],v[92:95],a[188:191],v[248:251]
v_mfma_f32_16x16x32_bf16 v[248:251],v[88:91],a[172:175],v[248:251]
v_mfma_f32_16x16x32_bf16 v[248:251],v[84:87],a[156:159],v[248:251]
v_mfma_f32_16x16x32_bf16 v[248:251],v[80:83],a[140:143],v[248:251]
s_waitcnt vmcnt(0)
v_cmp_ne_u32 s[56:57],s52,v62
v_cmp_ne_u32 s[58:59],s53,v63
s_nop 0
s_or_b64 s[56:57],s[56:57],s[58:59]
s_cbranch_scc1 L_fr4_%=
L_fo4_%=:
buffer_load_dwordx4 v[64:67],v6,s[36:39],s66 offen sc1
buffer_load_dwordx4 v[68:71],v6,s[36:39],s67 offen sc1
buffer_load_dwordx4 v[72:75],v6,s[36:39],s66 offen offset:1024 sc1
)A"
R"A(buffer_load_dwordx4 v[76:79],v6,s[36:39],s67 offen offset:1024 sc1
buffer_load_dwordx4 v[80:83],v6,s[36:39],s66 offen offset:2048 sc1
buffer_load_dwordx4 v[84:87],v6,s[36:39],s67 offen offset:2048 sc1
buffer_load_dwordx4 v[88:91],v28,s[36:39],s66 offen offset:3072 sc1
buffer_load_dwordx4 v[92:95],v28,s[36:39],s67 offen offset:3072 sc1
buffer_load_dwordx2 v[4:5],v3,s[44:47],s91 offen offset:2304 sc1
s_waitcnt vmcnt(8)
v_mfma_f32_16x16x32_bf16 v[192:195],v[48:51],v[64:67],v[192:195]
s_waitcnt vmcnt(7)
v_mfma_f32_16x16x32_bf16 v[192:195],v[48:51],v[68:71],v[192:195]
v_mfma_f32_16x16x32_bf16 v[224:227],v[52:55],v[64:67],v[224:227]
v_mfma_f32_16x16x32_bf16 v[224:227],v[52:55],v[68:71],v[224:227]
s_waitcnt vmcnt(6)
v_mfma_f32_16x16x32_bf16 v[200:203],v[48:51],v[72:75],v[200:203]
s_waitcnt vmcnt(5)
v_mfma_f32_16x16x32_bf16 v[200:203],v[48:51],v[76:79],v[200:203]
v_mfma_f32_16x16x32_bf16 v[232:235],v[52:55],v[72:75],v[232:235]
v_mfma_f32_16x16x32_bf16 v[232:235],v[52:55],v[76:79],v[232:235]
s_waitcnt vmcnt(4)
v_mfma_f32_16x16x32_bf16 v[208:211],v[48:51],v[80:83],v[208:211]
s_waitcnt vmcnt(3)
v_mfma_f32_16x16x32_bf16 v[208:211],v[48:51],v[84:87],v[208:211]
v_mfma_f32_16x16x32_bf16 v[240:243],v[52:55],v[80:83],v[240:243]
v_mfma_f32_16x16x32_bf16 v[240:243],v[52:55],v[84:87],v[240:243]
s_waitcnt vmcnt(2)
v_mfma_f32_16x16x32_bf16 v[216:219],v[48:51],v[88:91],v[216:219]
s_waitcnt vmcnt(1)
v_mfma_f32_16x16x32_bf16 v[216:219],v[48:51],v[92:95],v[216:219]
v_mfma_f32_16x16x32_bf16 v[248:251],v[52:55],v[88:91],v[248:251]
v_mfma_f32_16x16x32_bf16 v[248:251],v[52:55],v[92:95],v[248:251]
s_waitcnt vmcnt(0)
v_cmp_ne_u32 s[56:57],s52,v4
v_cmp_ne_u32 s[58:59],s53,v5
s_nop 0
s_or_b64 s[56:57],s[56:57],s[58:59]
s_cbranch_scc1 L_fr5_%=
L_fo5_%=:
buffer_load_dwordx4 v[96:99],v6,s[36:39],s68 offen sc1
buffer_load_dwordx4 v[100:103],v6,s[36:39],s68 offen offset:1024 sc1
buffer_load_dwordx4 v[104:107],v6,s[36:39],s68 offen offset:2048 sc1
buffer_load_dwordx4 v[108:111],v6,s[36:39],s68 offen offset:3072 sc1
buffer_load_dwordx4 v[112:115],v6,s[36:39],s65 offen sc1
buffer_load_dwordx4 v[116:119],v6,s[36:39],s65 offen offset:1024 sc1
buffer_load_dwordx4 v[120:123],v6,s[36:39],s65 offen offset:2048 sc1
buffer_load_dwordx4 v[124:127],v28,s[36:39],s65 offen offset:3072 sc1
s_waitcnt vmcnt(7)
v_mfma_f32_16x16x32_bf16 v[128:131],v[48:51],v[96:99],0
s_waitcnt vmcnt(6)
v_mfma_f32_16x16x32_bf16 v[128:131],v[48:51],v[100:103],v[128:131]
s_waitcnt vmcnt(5)
v_mfma_f32_16x16x32_bf16 v[128:131],v[48:51],v[104:107],v[128:131]
s_waitcnt vmcnt(4)
v_mfma_f32_16x16x32_bf16 v[128:131],v[48:51],v[108:111],v[128:131]
v_mfma_f32_16x16x32_bf16 v[160:163],v[52:55],v[96:99],0
v_mfma_f32_16x16x32_bf16 v[160:163],v[52:55],v[100:103],v[160:163]
v_mfma_f32_16x16x32_bf16 v[160:163],v[52:55],v[104:107],v[160:163]
v_mfma_f32_16x16x32_bf16 v[160:163],v[52:55],v[108:111],v[160:163]
s_waitcnt vmcnt(3)
v_mfma_f32_16x16x32_bf16 v[192:195],v[48:51],v[112:115],v[192:195]
v_mfma_f32_16x16x32_bf16 v[224:227],v[52:55],v[112:115],v[224:227]
s_waitcnt vmcnt(2)
v_mfma_f32_16x16x32_bf16 v[200:203],v[48:51],v[116:119],v[200:203]
v_mfma_f32_16x16x32_bf16 v[232:235],v[52:55],v[116:119],v[232:235]
s_waitcnt vmcnt(1)
v_cvt_pk_bf16_f32 v128,v128,v129
v_cvt_pk_bf16_f32 v129,v130,v131
v_mfma_f32_16x16x32_bf16 v[208:211],v[48:51],v[120:123],v[208:211]
v_mfma_f32_16x16x32_bf16 v[240:243],v[52:55],v[120:123],v[240:243]
s_waitcnt vmcnt(0)
v_cvt_pk_bf16_f32 v130,v160,v161
v_cvt_pk_bf16_f32 v131,v162,v163
v_mfma_f32_16x16x32_bf16 v[216:219],v[48:51],v[124:127],v[216:219]
v_cvt_pk_bf16_f32 v192,v192,v193
v_cvt_pk_bf16_f32 v193,v194,v195
v_cvt_pk_bf16_f32 v194,v224,v225
v_cvt_pk_bf16_f32 v195,v226,v227
v_cvt_pk_bf16_f32 v200,v200,v201
v_cvt_pk_bf16_f32 v201,v202,v203
v_mfma_f32_16x16x32_bf16 v[248:251],v[52:55],v[124:127],v[248:251]
v_permlane16_swap_b32 v128,v130
v_permlane16_swap_b32 v129,v131
v_cvt_pk_bf16_f32 v202,v232,v233
v_cvt_pk_bf16_f32 v203,v234,v235
v_cvt_pk_bf16_f32 v208,v208,v209
v_cvt_pk_bf16_f32 v209,v210,v211
v_cvt_pk_bf16_f32 v210,v240,v241
v_cvt_pk_bf16_f32 v211,v242,v243
buffer_store_dwordx4 v[128:131],v56,s[24:27],s83 offen offset:256 sc1 nt
v_permlane16_swap_b32 v192,v194
v_permlane16_swap_b32 v193,v195
buffer_store_dwordx4 v[192:195],v56,s[24:27],s87 offen sc1 nt
v_permlane16_swap_b32 v200,v202
v_permlane16_swap_b32 v201,v203
v_cvt_pk_bf16_f32 v216,v216,v217
v_cvt_pk_bf16_f32 v217,v218,v219
v_cvt_pk_bf16_f32 v218,v248,v249
v_cvt_pk_bf16_f32 v219,v250,v251
buffer_store_dwordx4 v[200:203],v56,s[24:27],s88 offen sc1 nt
v_permlane16_swap_b32 v208,v210
v_permlane16_swap_b32 v209,v211
buffer_store_dwordx4 v[208:211],v56,s[24:27],s89 offen sc1 nt
v_permlane16_swap_b32 v216,v218
v_permlane16_swap_b32 v217,v219
buffer_store_dwordx4 v[216:219],v56,s[24:27],s90 offen sc1 nt
s_lshl_b32 s69,s62,20
s_add_u32 s69,s69,s18
buffer_store_dword v20,v22,s[44:47],s69 offen sc1
L_geF_%=:
s_branch L_end_%=
.p2align 12
L_pd19_%=:
s_cbranch_execz L_trt1_%=
L_GS_%=:
L_gbS_%=:
s_waitcnt lgkmcnt(14)
v_mfma_f32_16x16x32_bf16 a[192:195],v[64:67],a[68:71],0
buffer_store_dwordx4 v[32:35],v6,s[40:43],s79 offen sc1
s_waitcnt lgkmcnt(12)
v_mfma_f32_16x16x32_bf16 a[192:195],v[68:71],a[76:79],a[192:195]
s_lshl_b32 s91,s63,8
v_bfe_u32 v3,v6,4,2
v_min_u32 v3,2,v3
s_waitcnt lgkmcnt(10)
v_mfma_f32_16x16x32_bf16 a[192:195],v[72:75],a[84:87],a[192:195]
s_add_u32 s87,s64,1
v_add3_u32 v3,v3,s64,1
v_and_b32 v3,3,v3
s_waitcnt lgkmcnt(8)
v_mfma_f32_16x16x32_bf16 a[192:195],v[76:79],a[92:95],a[192:195]
s_and_b32 s87,s87,3
ds_read_b64_tr_b16 v[96:97],v1 offset:64
v_lshlrev_b32 v3,6,v3
v_mfma_f32_16x16x32_bf16 a[196:199],v[76:79],a[124:127],0
s_lshl_b32 s87,s87,1
ds_read_b64_tr_b16 v[98:99],v1 offset:5184
ds_read_b64_tr_b16 v[100:101],v1 offset:10304
v_mfma_f32_16x16x32_bf16 a[196:199],v[72:75],a[116:119],a[196:199]
s_lshl_b32 s87,s87,16
ds_read_b64_tr_b16 v[102:103],v1 offset:15424
v_lshlrev_b32 v29,10,v6
v_mfma_f32_16x16x32_bf16 a[196:199],v[68:71],a[108:111],a[196:199]
s_add_u32 s88,s87,4096
ds_read_b64_tr_b16 v[104:105],v1 offset:20544
v_accvgpr_read_b32 v16,a24
v_mfma_f32_16x16x32_bf16 a[196:199],v[64:67],a[100:103],a[196:199]
s_add_u32 s89,s87,8192
ds_read_b64_tr_b16 v[106:107],v1 offset:25664
v_accvgpr_read_b32 v17,a25
v_mfma_f32_16x16x32_bf16 a[200:203],v[64:67],a[64:67],0
s_add_u32 s90,s87,12288
ds_read_b64_tr_b16 v[108:109],v1 offset:30784
ds_read_b64_tr_b16 v[110:111],v1 offset:35904
v_mfma_f32_16x16x32_bf16 a[200:203],v[68:71],a[72:75],a[200:203]
s_add_u32 m0,s86,122880
ds_read_b64_tr_b16 v[112:113],v2 offset:96
v_accvgpr_read_b32 v18,a26
v_mfma_f32_16x16x32_bf16 a[200:203],v[72:75],a[80:83],a[200:203]
s_add_u32 s83,s64,3
ds_read_b64_tr_b16 v[114:115],v2 offset:5216
v_accvgpr_read_b32 v19,a27
v_mfma_f32_16x16x32_bf16 a[200:203],v[76:79],a[88:91],a[200:203]
s_and_b32 s83,s83,3
ds_read_b64_tr_b16 v[116:117],v2 offset:10336
v_accvgpr_read_b32 v20,a28
v_mfma_f32_16x16x32_bf16 a[204:207],v[76:79],a[120:123],0
s_lshl_b32 s83,s83,1
ds_read_b64_tr_b16 v[118:119],v2 offset:15456
ds_read_b64_tr_b16 v[120:121],v2 offset:20576
v_mfma_f32_16x16x32_bf16 a[204:207],v[72:75],a[112:115],a[204:207]
s_mul_i32 s83,s83,163840
ds_read_b64_tr_b16 v[122:123],v2 offset:25696
v_accvgpr_read_b32 v21,a29
v_mfma_f32_16x16x32_bf16 a[204:207],v[68:71],a[104:107],a[204:207]
s_add_u32 s83,s83,s85
ds_read_b64_tr_b16 v[124:125],v2 offset:30816
v_accvgpr_read_b32 v22,a30
v_mfma_f32_16x16x32_bf16 a[204:207],v[64:67],a[96:99],a[204:207]
buffer_store_dwordx4 v[36:39],v6,s[40:43],s80 offen sc1
s_waitcnt lgkmcnt(9)
v_mfma_f32_16x16x32_bf16 a[208:211],v[80:83],a[68:71],0
s_add_u32 s84,s83,20480
ds_read_b64_tr_b16 v[126:127],v2 offset:35936
ds_read_b64_tr_b16 v[64:65],v1 offset:128
v_mfma_f32_16x16x32_bf16 a[208:211],v[84:87],a[76:79],a[208:211]
s_lshl_b32 s66,s61,3
ds_read_b64_tr_b16 v[66:67],v1 offset:5248
v_accvgpr_read_b32 v23,a31
v_mfma_f32_16x16x32_bf16 a[208:211],v[88:91],a[84:87],a[208:211]
s_add_u32 s66,s66,s64
ds_read_b64_tr_b16 v[68:69],v1 offset:10368
v_mul_f32 v16,v16,v49
v_mfma_f32_16x16x32_bf16 a[208:211],v[92:95],a[92:95],a[208:211]
s_mul_i32 s66,s66,262144
ds_read_b64_tr_b16 v[70:71],v1 offset:15488
v_mul_f32 v17,v17,v49
v_mfma_f32_16x16x32_bf16 a[212:215],v[92:95],a[124:127],0
s_waitcnt vmcnt(21)
buffer_load_dwordx2 v[4:5],v3,s[44:47],s91 offen offset:256 sc1
v_mfma_f32_16x16x32_bf16 a[212:215],v[88:91],a[116:119],a[212:215]
s_add_u32 s66,s66,100663296
ds_read_b64_tr_b16 v[72:73],v1 offset:20608
ds_read_b64_tr_b16 v[74:75],v1 offset:25728
v_mfma_f32_16x16x32_bf16 a[212:215],v[84:87],a[108:111],a[212:215]
s_mov_b32 s38,262144
ds_read_b64_tr_b16 v[76:77],v1 offset:30848
v_mul_f32 v18,v18,v49
v_mfma_f32_16x16x32_bf16 a[212:215],v[80:83],a[100:103],a[212:215]
s_mov_b32 s39,0x20000
ds_read_b64_tr_b16 v[78:79],v1 offset:35968
v_mul_f32 v19,v19,v49
v_mfma_f32_16x16x32_bf16 a[216:219],v[80:83],a[64:67],0
s_add_u32 s74,s63,1
v_mul_f32 v20,v20,v49
v_mul_f32 v21,v21,v49
v_mfma_f32_16x16x32_bf16 a[216:219],v[84:87],a[72:75],a[216:219]
s_and_b32 s74,s74,3
v_mul_f32 v22,v22,v49
v_mul_f32 v23,v23,v49
v_mfma_f32_16x16x32_bf16 a[216:219],v[88:91],a[80:83],a[216:219]
s_lshl_b32 s74,s74,2
v_cvt_pk_bf16_f32 v44,v16,v17
v_cvt_pk_bf16_f32 v45,v18,v19
v_mfma_f32_16x16x32_bf16 a[216:219],v[92:95],a[88:91],a[216:219]
s_add_u32 s74,s74,s60
v_cvt_pk_bf16_f32 v46,v20,v21
v_cvt_pk_bf16_f32 v47,v22,v23
v_mfma_f32_16x16x32_bf16 a[220:223],v[92:95],a[120:123],0
s_mul_i32 s74,s74,16384
ds_read_b64_tr_b16 v[92:93],v2 offset:30880
ds_read_b64_tr_b16 v[94:95],v2 offset:36000
v_mfma_f32_16x16x32_bf16 a[220:223],v[88:91],a[112:115],a[220:223]
s_add_u32 s74,s74,8192
ds_read_b64_tr_b16 v[88:89],v2 offset:20640
v_mul_f32 v132,v132,v53
v_mfma_f32_16x16x32_bf16 a[220:223],v[84:87],a[104:107],a[220:223]
buffer_store_dwordx4 v[40:43],v6,s[40:43],s81 offen sc1
v_mfma_f32_16x16x32_bf16 a[220:223],v[80:83],a[96:99],a[220:223]
s_add_u32 s75,s63,2
ds_read_b64_tr_b16 v[80:81],v2 offset:160
ds_read_b64_tr_b16 v[82:83],v2 offset:5280
v_mfma_f32_16x16x32_bf16 a[224:227],v[96:99],a[68:71],0
s_and_b32 s75,s75,3
ds_read_b64_tr_b16 v[84:85],v2 offset:10400
v_mul_f32 v133,v133,v53
v_mfma_f32_16x16x32_bf16 a[224:227],v[100:103],a[76:79],a[224:227]
s_lshl_b32 s75,s75,2
ds_read_b64_tr_b16 v[86:87],v2 offset:15520
v_mul_f32 v134,v134,v53
v_mfma_f32_16x16x32_bf16 a[224:227],v[104:107],a[84:87],a[224:227]
s_add_u32 s75,s75,s60
ds_read_b64_tr_b16 v[90:91],v2 offset:25760
v_mul_f32 v135,v135,v53
s_waitcnt lgkmcnt(8)
v_mfma_f32_16x16x32_bf16 a[224:227],v[108:111],a[92:95],a[224:227]
s_mul_i32 s75,s75,16384
v_mul_f32 v140,v140,v53
v_mul_f32 v141,v141,v53
v_mfma_f32_16x16x32_bf16 a[228:231],v[108:111],a[124:127],0
s_add_u32 s75,s75,4096
v_mul_f32 v142,v142,v53
v_mul_f32 v143,v143,v53
v_mfma_f32_16x16x32_bf16 a[228:231],v[104:107],a[116:119],a[228:231]
s_add_u32 s76,s63,3
v_cvt_pk_bf16_f32 v132,v132,v133
v_cvt_pk_bf16_f32 v133,v134,v135
v_mfma_f32_16x16x32_bf16 a[228:231],v[100:103],a[108:111],a[228:231]
s_and_b32 s76,s76,3
v_cvt_pk_bf16_f32 v134,v140,v141
v_cvt_pk_bf16_f32 v135,v142,v143
v_mfma_f32_16x16x32_bf16 a[228:231],v[96:99],a[100:103],a[228:231]
s_lshl_b32 s76,s76,2
v_mul_f32 v148,v148,v53
v_mul_f32 v149,v149,v53
v_mfma_f32_16x16x32_bf16 a[232:235],v[96:99],a[64:67],0
s_add_u32 s76,s76,s60
v_mul_f32 v150,v150,v53
v_mul_f32 v151,v151,v53
v_mfma_f32_16x16x32_bf16 a[232:235],v[100:103],a[72:75],a[232:235]
s_mul_i32 s76,s76,16384
v_mul_f32 v156,v156,v53
v_mul_f32 v157,v157,v53
v_mfma_f32_16x16x32_bf16 a[232:235],v[104:107],a[80:83],a[232:235]
s_add_u32 s76,s76,0
v_mul_f32 v158,v158,v53
v_mul_f32 v159,v159,v53
v_mfma_f32_16x16x32_bf16 a[232:235],v[108:111],a[88:91],a[232:235]
s_lshl_b32 s67,s63,10
v_cvt_pk_bf16_f32 v148,v148,v149
v_cvt_pk_bf16_f32 v149,v150,v151
v_mfma_f32_16x16x32_bf16 a[236:239],v[108:111],a[120:123],0
s_mov_b32 s26,307200
ds_read_b64_tr_b16 v[108:109],v1 offset:30912
ds_read_b64_tr_b16 v[110:111],v1 offset:36032
v_mfma_f32_16x16x32_bf16 a[236:239],v[104:107],a[112:115],a[236:239]
buffer_store_dwordx4 v[44:47],v6,s[40:43],s82 offen sc1
v_mfma_f32_16x16x32_bf16 a[236:239],v[100:103],a[104:107],a[236:239]
)A"
R"A(s_mov_b32 s27,0x20000
ds_read_b64_tr_b16 v[100:101],v1 offset:10432
ds_read_b64_tr_b16 v[102:103],v1 offset:15552
v_mfma_f32_16x16x32_bf16 a[236:239],v[96:99],a[96:99],a[236:239]
s_lshl_b32 s77,s63,2
ds_read_b64_tr_b16 v[96:97],v1 offset:192
v_cvt_pk_bf16_f32 v150,v156,v157
v_mfma_f32_16x16x32_bf16 a[240:243],v[112:115],a[68:71],0
s_add_u32 s77,s77,s60
ds_read_b64_tr_b16 v[98:99],v1 offset:5312
v_cvt_pk_bf16_f32 v151,v158,v159
v_mfma_f32_16x16x32_bf16 a[240:243],v[116:119],a[76:79],a[240:243]
s_mul_i32 s77,s77,16384
ds_read_b64_tr_b16 v[104:105],v1 offset:20672
v_mul_f32 v164,v164,v53
v_mfma_f32_16x16x32_bf16 a[240:243],v[120:123],a[84:87],a[240:243]
s_add_u32 s65,s77,0
ds_read_b64_tr_b16 v[106:107],v1 offset:25792
v_mul_f32 v165,v165,v53
v_mfma_f32_16x16x32_bf16 a[240:243],v[124:127],a[92:95],a[240:243]
s_add_u32 s68,s77,12288
v_mul_f32 v166,v166,v53
v_mul_f32 v167,v167,v53
v_mfma_f32_16x16x32_bf16 a[244:247],v[124:127],a[124:127],0
v_mul_f32 v172,v172,v53
v_mul_f32 v173,v173,v53
v_mfma_f32_16x16x32_bf16 a[244:247],v[120:123],a[116:119],a[244:247]
v_mul_f32 v174,v174,v53
v_mul_f32 v175,v175,v53
v_mfma_f32_16x16x32_bf16 a[244:247],v[116:119],a[108:111],a[244:247]
v_cvt_pk_bf16_f32 v164,v164,v165
v_cvt_pk_bf16_f32 v165,v166,v167
v_mfma_f32_16x16x32_bf16 a[244:247],v[112:115],a[100:103],a[244:247]
v_cvt_pk_bf16_f32 v166,v172,v173
v_cvt_pk_bf16_f32 v167,v174,v175
v_mfma_f32_16x16x32_bf16 a[248:251],v[112:115],a[64:67],0
v_mul_f32 v180,v180,v53
v_mul_f32 v181,v181,v53
v_mfma_f32_16x16x32_bf16 a[248:251],v[116:119],a[72:75],a[248:251]
v_mul_f32 v182,v182,v53
v_mul_f32 v183,v183,v53
v_mfma_f32_16x16x32_bf16 a[248:251],v[120:123],a[80:83],a[248:251]
v_mul_f32 v188,v188,v53
v_mul_f32 v189,v189,v53
v_mfma_f32_16x16x32_bf16 a[248:251],v[124:127],a[88:91],a[248:251]
v_mul_f32 v190,v190,v53
v_mul_f32 v191,v191,v53
v_mfma_f32_16x16x32_bf16 a[252:255],v[124:127],a[120:123],0
buffer_load_dword v30,v29,s[8:11],s7 offen
v_mfma_f32_16x16x32_bf16 a[252:255],v[120:123],a[112:115],a[252:255]
ds_read_b64_tr_b16 v[120:121],v2 offset:20704
ds_read_b64_tr_b16 v[122:123],v2 offset:25824
v_mfma_f32_16x16x32_bf16 a[252:255],v[116:119],a[104:107],a[252:255]
buffer_store_dwordx4 v[132:135],v6,s[40:43],s79 offen offset:1024 sc1
v_mfma_f32_16x16x32_bf16 a[252:255],v[112:115],a[96:99],a[252:255]
ds_read_b64_tr_b16 v[112:113],v2 offset:224
ds_read_b64_tr_b16 v[114:115],v2 offset:5344
v_mfma_f32_16x16x32_bf16 a[0:3],v[64:67],a[68:71],0
ds_read_b64_tr_b16 v[116:117],v2 offset:10464
v_cvt_pk_bf16_f32 v180,v180,v181
v_mfma_f32_16x16x32_bf16 a[0:3],v[68:71],a[76:79],a[0:3]
ds_read_b64_tr_b16 v[118:119],v2 offset:15584
v_cvt_pk_bf16_f32 v181,v182,v183
v_mfma_f32_16x16x32_bf16 a[0:3],v[72:75],a[84:87],a[0:3]
ds_read_b64_tr_b16 v[124:125],v2 offset:30944
v_cvt_pk_bf16_f32 v182,v188,v189
v_mfma_f32_16x16x32_bf16 a[0:3],v[76:79],a[92:95],a[0:3]
ds_read_b64_tr_b16 v[126:127],v2 offset:36064
v_cvt_pk_bf16_f32 v183,v190,v191
v_mfma_f32_16x16x32_bf16 a[4:7],v[76:79],a[124:127],0
v_mul_f32 v196,v196,v57
v_mul_f32 v197,v197,v57
v_mfma_f32_16x16x32_bf16 a[4:7],v[72:75],a[116:119],a[4:7]
v_mul_f32 v198,v198,v57
v_mul_f32 v199,v199,v57
v_mfma_f32_16x16x32_bf16 a[4:7],v[68:71],a[108:111],a[4:7]
v_mul_f32 v204,v204,v57
v_mul_f32 v205,v205,v57
v_mfma_f32_16x16x32_bf16 a[4:7],v[64:67],a[100:103],a[4:7]
v_mul_f32 v206,v206,v57
v_mul_f32 v207,v207,v57
v_mfma_f32_16x16x32_bf16 a[8:11],v[64:67],a[64:67],0
v_cvt_pk_bf16_f32 v196,v196,v197
v_cvt_pk_bf16_f32 v197,v198,v199
v_mfma_f32_16x16x32_bf16 a[8:11],v[68:71],a[72:75],a[8:11]
v_cvt_pk_bf16_f32 v198,v204,v205
v_cvt_pk_bf16_f32 v199,v206,v207
v_mfma_f32_16x16x32_bf16 a[8:11],v[72:75],a[80:83],a[8:11]
v_mul_f32 v212,v212,v57
v_mul_f32 v213,v213,v57
v_mfma_f32_16x16x32_bf16 a[8:11],v[76:79],a[88:91],a[8:11]
v_mul_f32 v214,v214,v57
v_mul_f32 v215,v215,v57
v_mfma_f32_16x16x32_bf16 a[12:15],v[76:79],a[120:123],0
ds_read_b64_tr_b16 v[76:77],v1 offset:30976
ds_read_b64_tr_b16 v[78:79],v1 offset:36096
v_mfma_f32_16x16x32_bf16 a[12:15],v[72:75],a[112:115],a[12:15]
buffer_store_dwordx4 v[148:151],v6,s[40:43],s80 offen offset:1024 sc1
v_mfma_f32_16x16x32_bf16 a[12:15],v[68:71],a[104:107],a[12:15]
ds_read_b64_tr_b16 v[68:69],v1 offset:10496
ds_read_b64_tr_b16 v[70:71],v1 offset:15616
v_mfma_f32_16x16x32_bf16 a[12:15],v[64:67],a[96:99],a[12:15]
s_waitcnt lgkmcnt(4)
ds_read_b64_tr_b16 v[64:65],v1 offset:256
v_mul_f32 v220,v220,v57
v_mfma_f32_16x16x32_bf16 a[16:19],v[80:83],a[68:71],0
ds_read_b64_tr_b16 v[66:67],v1 offset:5376
v_mul_f32 v221,v221,v57
v_mfma_f32_16x16x32_bf16 a[16:19],v[84:87],a[76:79],a[16:19]
ds_read_b64_tr_b16 v[72:73],v1 offset:20736
v_mul_f32 v222,v222,v57
v_mfma_f32_16x16x32_bf16 a[16:19],v[88:91],a[84:87],a[16:19]
ds_read_b64_tr_b16 v[74:75],v1 offset:25856
v_add_u32 v1,0xffff6000,v1
v_mfma_f32_16x16x32_bf16 a[16:19],v[92:95],a[92:95],a[16:19]
v_mul_f32 v223,v223,v57
v_cvt_pk_bf16_f32 v212,v212,v213
v_mfma_f32_16x16x32_bf16 a[20:23],v[92:95],a[124:127],0
v_cvt_pk_bf16_f32 v213,v214,v215
v_cvt_pk_bf16_f32 v214,v220,v221
v_mfma_f32_16x16x32_bf16 a[20:23],v[88:91],a[116:119],a[20:23]
v_cvt_pk_bf16_f32 v215,v222,v223
v_mul_f32 v228,v228,v57
v_mfma_f32_16x16x32_bf16 a[20:23],v[84:87],a[108:111],a[20:23]
v_mul_f32 v229,v229,v57
v_mul_f32 v230,v230,v57
v_mfma_f32_16x16x32_bf16 a[20:23],v[80:83],a[100:103],a[20:23]
v_mul_f32 v231,v231,v57
v_mul_f32 v236,v236,v57
v_mfma_f32_16x16x32_bf16 a[24:27],v[80:83],a[64:67],0
v_mul_f32 v237,v237,v57
v_mul_f32 v238,v238,v57
v_mfma_f32_16x16x32_bf16 a[24:27],v[84:87],a[72:75],a[24:27]
v_mul_f32 v239,v239,v57
v_cvt_pk_bf16_f32 v228,v228,v229
v_mfma_f32_16x16x32_bf16 a[24:27],v[88:91],a[80:83],a[24:27]
s_waitcnt vmcnt(5)
v_cmp_ne_u32 s[56:57],s52,v4
v_cmp_ne_u32 s[58:59],s53,v5
v_mfma_f32_16x16x32_bf16 a[24:27],v[92:95],a[88:91],a[24:27]
s_or_b64 s[56:57],s[56:57],s[58:59]
s_cbranch_scc1 L_fr6_%=
L_fo6_%=:
v_cvt_pk_bf16_f32 v229,v230,v231
v_mfma_f32_16x16x32_bf16 a[28:31],v[92:95],a[120:123],0
buffer_store_dwordx4 v[164:167],v6,s[40:43],s81 offen offset:1024 sc1
v_mfma_f32_16x16x32_bf16 a[28:31],v[88:91],a[112:115],a[28:31]
s_lshl_b32 s91,s63,8
ds_read_b64_tr_b16 v[88:89],v2 offset:20768
ds_read_b64_tr_b16 v[90:91],v2 offset:25888
v_mfma_f32_16x16x32_bf16 a[28:31],v[84:87],a[104:107],a[28:31]
buffer_load_dwordx4 a[128:131],v6,s[28:31],s87 offen sc1
v_mfma_f32_16x16x32_bf16 a[28:31],v[80:83],a[96:99],a[28:31]
ds_read_b64_tr_b16 v[80:81],v2 offset:288
ds_read_b64_tr_b16 v[82:83],v2 offset:5408
v_mfma_f32_16x16x32_bf16 v[128:131],v[96:99],a[68:71],0
ds_read_b64_tr_b16 v[84:85],v2 offset:10528
v_cvt_pk_bf16_f32 v230,v236,v237
v_mfma_f32_16x16x32_bf16 v[128:131],v[100:103],a[76:79],v[128:131]
ds_read_b64_tr_b16 v[86:87],v2 offset:15648
v_cvt_pk_bf16_f32 v231,v238,v239
v_mfma_f32_16x16x32_bf16 v[128:131],v[104:107],a[84:87],v[128:131]
ds_read_b64_tr_b16 v[92:93],v2 offset:31008
v_mul_f32 v244,v244,v57
v_mfma_f32_16x16x32_bf16 v[128:131],v[108:111],a[92:95],v[128:131]
ds_read_b64_tr_b16 v[94:95],v2 offset:36128
v_add_u32 v2,0xffff6000,v2
v_mfma_f32_16x16x32_bf16 v[136:139],v[108:111],a[124:127],0
v_mul_f32 v245,v245,v57
v_mul_f32 v246,v246,v57
v_mfma_f32_16x16x32_bf16 v[136:139],v[104:107],a[116:119],v[136:139]
v_mul_f32 v247,v247,v57
v_mul_f32 v252,v252,v57
v_mfma_f32_16x16x32_bf16 v[136:139],v[100:103],a[108:111],v[136:139]
v_mul_f32 v253,v253,v57
v_mul_f32 v254,v254,v57
v_mfma_f32_16x16x32_bf16 v[136:139],v[96:99],a[100:103],v[136:139]
v_mul_f32 v255,v255,v57
v_cvt_pk_bf16_f32 v244,v244,v245
v_mfma_f32_16x16x32_bf16 v[144:147],v[96:99],a[64:67],0
v_cvt_pk_bf16_f32 v245,v246,v247
v_cvt_pk_bf16_f32 v246,v252,v253
v_mfma_f32_16x16x32_bf16 v[144:147],v[100:103],a[72:75],v[144:147]
buffer_load_dwordx4 a[144:147],v6,s[28:31],s88 offen sc1
v_mfma_f32_16x16x32_bf16 v[144:147],v[104:107],a[80:83],v[144:147]
v_cvt_pk_bf16_f32 v247,v254,v255
v_accvgpr_read_b32 v16,a40
v_mfma_f32_16x16x32_bf16 v[144:147],v[108:111],a[88:91],v[144:147]
buffer_store_dwordx4 v[180:183],v6,s[40:43],s82 offen offset:1024 sc1
v_mfma_f32_16x16x32_bf16 v[152:155],v[108:111],a[120:123],0
ds_read_b64_tr_b16 v[108:109],v1 offset:30720
ds_read_b64_tr_b16 v[110:111],v1 offset:35840
v_mfma_f32_16x16x32_bf16 v[152:155],v[104:107],a[112:115],v[152:155]
ds_read_b64_tr_b16 v[104:105],v1 offset:20480
v_accvgpr_read_b32 v17,a41
v_mfma_f32_16x16x32_bf16 v[152:155],v[100:103],a[104:107],v[152:155]
ds_read_b64_tr_b16 v[100:101],v1 offset:10240
v_accvgpr_read_b32 v18,a42
v_mfma_f32_16x16x32_bf16 v[152:155],v[96:99],a[96:99],v[152:155]
ds_read_b64_tr_b16 v[96:97],v1
v_accvgpr_read_b32 v19,a43
v_mfma_f32_16x16x32_bf16 v[160:163],v[112:115],a[68:71],0
ds_read_b64_tr_b16 v[98:99],v1 offset:5120
ds_read_b64_tr_b16 v[102:103],v1 offset:15360
v_mfma_f32_16x16x32_bf16 v[160:163],v[116:119],a[76:79],v[160:163]
ds_read_b64_tr_b16 v[106:107],v1 offset:25600
v_accvgpr_read_b32 v20,a44
v_mfma_f32_16x16x32_bf16 v[160:163],v[120:123],a[84:87],v[160:163]
v_accvgpr_read_b32 v21,a45
v_accvgpr_read_b32 v22,a46
v_mfma_f32_16x16x32_bf16 v[160:163],v[124:127],a[92:95],v[160:163]
v_accvgpr_read_b32 v23,a47
v_mul_f32 v16,v16,v61
v_mfma_f32_16x16x32_bf16 v[168:171],v[124:127],a[124:127],0
v_mul_f32 v17,v17,v61
v_mul_f32 v18,v18,v61
v_mfma_f32_16x16x32_bf16 v[168:171],v[120:123],a[116:119],v[168:171]
v_mul_f32 v19,v19,v61
v_mul_f32 v20,v20,v61
v_mfma_f32_16x16x32_bf16 v[168:171],v[116:119],a[108:111],v[168:171]
buffer_load_dwordx4 a[160:163],v6,s[28:31],s89 offen sc1
v_mfma_f32_16x16x32_bf16 v[168:171],v[112:115],a[100:103],v[168:171]
v_mul_f32 v21,v21,v61
v_mul_f32 v22,v22,v61
v_mfma_f32_16x16x32_bf16 v[176:179],v[112:115],a[64:67],0
v_mul_f32 v23,v23,v61
v_cvt_pk_bf16_f32 v156,v16,v17
v_mfma_f32_16x16x32_bf16 v[176:179],v[116:119],a[72:75],v[176:179]
v_cvt_pk_bf16_f32 v157,v18,v19
v_cvt_pk_bf16_f32 v158,v20,v21
v_mfma_f32_16x16x32_bf16 v[176:179],v[120:123],a[80:83],v[176:179]
buffer_store_dwordx4 v[196:199],v6,s[40:43],s79 offen offset:2048 sc1
v_mfma_f32_16x16x32_bf16 v[176:179],v[124:127],a[88:91],v[176:179]
v_cvt_pk_bf16_f32 v159,v22,v23
v_accvgpr_read_b32 v16,a56
v_mfma_f32_16x16x32_bf16 v[184:187],v[124:127],a[120:123],0
v_accvgpr_read_b32 v17,a57
v_accvgpr_read_b32 v18,a58
v_mfma_f32_16x16x32_bf16 v[184:187],v[120:123],a[112:115],v[184:187]
ds_read_b64_tr_b16 v[120:121],v2 offset:20512
ds_read_b64_tr_b16 v[122:123],v2 offset:25632
v_mfma_f32_16x16x32_bf16 v[184:187],v[116:119],a[104:107],v[184:187]
ds_read_b64_tr_b16 v[116:117],v2 offset:10272
v_accvgpr_read_b32 v19,a59
v_mfma_f32_16x16x32_bf16 v[184:187],v[112:115],a[96:99],v[184:187]
s_waitcnt lgkmcnt(3)
ds_read_b64_tr_b16 v[112:113],v2 offset:32
v_accvgpr_read_b32 v20,a60
v_mfma_f32_16x16x32_bf16 v[192:195],v[64:67],a[68:71],0
ds_read_b64_tr_b16 v[114:115],v2 offset:5152
v_accvgpr_read_b32 v21,a61
v_mfma_f32_16x16x32_bf16 v[192:195],v[68:71],a[76:79],v[192:195]
ds_read_b64_tr_b16 v[118:119],v2 offset:15392
v_accvgpr_read_b32 v22,a62
v_mfma_f32_16x16x32_bf16 v[192:195],v[72:75],a[84:87],v[192:195]
v_accvgpr_read_b32 v23,a63
v_mul_f32 v16,v16,v61
v_mfma_f32_16x16x32_bf16 v[192:195],v[76:79],a[92:95],v[192:195]
buffer_load_dwordx4 a[176:179],v6,s[28:31],s90 offen sc1
v_mfma_f32_16x16x32_bf16 v[200:203],v[76:79],a[124:127],0
v_mul_f32 v17,v17,v61
v_mul_f32 v18,v18,v61
v_mfma_f32_16x16x32_bf16 v[200:203],v[72:75],a[116:119],v[200:203]
v_mul_f32 v19,v19,v61
v_mul_f32 v20,v20,v61
v_mfma_f32_16x16x32_bf16 v[200:203],v[68:71],a[108:111],v[200:203]
v_mul_f32 v21,v21,v61
v_mul_f32 v22,v22,v61
v_mfma_f32_16x16x32_bf16 v[200:203],v[64:67],a[100:103],v[200:203]
v_mul_f32 v23,v23,v61
v_cvt_pk_bf16_f32 v188,v16,v17
v_mfma_f32_16x16x32_bf16 v[208:211],v[64:67],a[64:67],0
v_cvt_pk_bf16_f32 v189,v18,v19
v_cvt_pk_bf16_f32 v190,v20,v21
v_mfma_f32_16x16x32_bf16 v[208:211],v[68:71],a[72:75],v[208:211]
buffer_store_dwordx4 v[212:215],v6,s[40:43],s80 offen offset:2048 sc1
v_mfma_f32_16x16x32_bf16 v[208:211],v[72:75],a[80:83],v[208:211]
ds_read_b64_tr_b16 v[124:125],v2 offset:30752
v_cvt_pk_bf16_f32 v191,v22,v23
v_mfma_f32_16x16x32_bf16 v[208:211],v[76:79],a[88:91],v[208:211]
ds_read_b64_tr_b16 v[126:127],v2 offset:35872
)A"
R"A(v_accvgpr_read_b32 v8,a32
v_mfma_f32_16x16x32_bf16 v[216:219],v[76:79],a[120:123],0
v_accvgpr_read_b32 v9,a33
v_accvgpr_read_b32 v10,a34
v_mfma_f32_16x16x32_bf16 v[216:219],v[72:75],a[112:115],v[216:219]
v_accvgpr_read_b32 v11,a35
v_accvgpr_read_b32 v12,a36
v_mfma_f32_16x16x32_bf16 v[216:219],v[68:71],a[104:107],v[216:219]
v_accvgpr_read_b32 v13,a37
v_accvgpr_read_b32 v14,a38
v_mfma_f32_16x16x32_bf16 v[216:219],v[64:67],a[96:99],v[216:219]
v_accvgpr_read_b32 v15,a39
v_mul_f32 v8,v8,v61
v_mfma_f32_16x16x32_bf16 v[224:227],v[80:83],a[68:71],0
buffer_load_dwordx4 a[180:183],v6,s[28:31],s90 offen offset:1024 sc1
v_mfma_f32_16x16x32_bf16 v[224:227],v[84:87],a[76:79],v[224:227]
v_mul_f32 v9,v9,v61
v_mul_f32 v10,v10,v61
v_mfma_f32_16x16x32_bf16 v[224:227],v[88:91],a[84:87],v[224:227]
v_mul_f32 v11,v11,v61
v_mul_f32 v12,v12,v61
v_mfma_f32_16x16x32_bf16 v[224:227],v[92:95],a[92:95],v[224:227]
v_mul_f32 v13,v13,v61
v_mul_f32 v14,v14,v61
v_mfma_f32_16x16x32_bf16 v[232:235],v[92:95],a[124:127],0
v_mul_f32 v15,v15,v61
v_cvt_pk_bf16_f32 v140,v8,v9
v_mfma_f32_16x16x32_bf16 v[232:235],v[88:91],a[116:119],v[232:235]
v_cvt_pk_bf16_f32 v141,v10,v11
v_cvt_pk_bf16_f32 v142,v12,v13
v_mfma_f32_16x16x32_bf16 v[232:235],v[84:87],a[108:111],v[232:235]
v_cvt_pk_bf16_f32 v143,v14,v15
v_accvgpr_read_b32 v8,a48
v_mfma_f32_16x16x32_bf16 v[232:235],v[80:83],a[100:103],v[232:235]
v_accvgpr_read_b32 v9,a49
v_accvgpr_read_b32 v10,a50
v_mfma_f32_16x16x32_bf16 v[240:243],v[80:83],a[64:67],0
buffer_store_dwordx4 v[228:231],v6,s[40:43],s81 offen offset:2048 sc1
v_mfma_f32_16x16x32_bf16 v[240:243],v[84:87],a[72:75],v[240:243]
v_accvgpr_read_b32 v11,a51
v_accvgpr_read_b32 v12,a52
v_mfma_f32_16x16x32_bf16 v[240:243],v[88:91],a[80:83],v[240:243]
v_accvgpr_read_b32 v13,a53
v_accvgpr_read_b32 v14,a54
v_mfma_f32_16x16x32_bf16 v[240:243],v[92:95],a[88:91],v[240:243]
v_accvgpr_read_b32 v15,a55
v_mul_f32 v8,v8,v61
v_mfma_f32_16x16x32_bf16 v[248:251],v[92:95],a[120:123],0
v_mul_f32 v9,v9,v61
v_mul_f32 v10,v10,v61
v_mfma_f32_16x16x32_bf16 v[248:251],v[88:91],a[112:115],v[248:251]
buffer_load_dwordx4 a[164:167],v6,s[28:31],s89 offen offset:1024 sc1
v_mfma_f32_16x16x32_bf16 v[248:251],v[84:87],a[104:107],v[248:251]
v_mul_f32 v11,v11,v61
v_mul_f32 v12,v12,v61
v_mfma_f32_16x16x32_bf16 v[248:251],v[80:83],a[96:99],v[248:251]
v_mul_f32 v13,v13,v61
v_mul_f32 v14,v14,v61
v_mfma_f32_16x16x32_bf16 a[192:195],v[96:99],v[32:35],a[192:195]
v_mul_f32 v15,v15,v61
v_cvt_pk_bf16_f32 v172,v8,v9
v_mfma_f32_16x16x32_bf16 a[192:195],v[100:103],v[36:39],a[192:195]
v_cvt_pk_bf16_f32 v173,v10,v11
v_cvt_pk_bf16_f32 v174,v12,v13
v_mfma_f32_16x16x32_bf16 a[192:195],v[104:107],v[40:43],a[192:195]
ds_read_b64_tr_b16 v[64:65],v1 offset:64
v_cvt_pk_bf16_f32 v175,v14,v15
v_mfma_f32_16x16x32_bf16 a[192:195],v[108:111],v[44:47],a[192:195]
ds_read_b64_tr_b16 v[66:67],v1 offset:5184
ds_read_b64_tr_b16 v[68:69],v1 offset:10304
v_mfma_f32_16x16x32_bf16 a[196:199],v[108:111],v[180:183],a[196:199]
s_cmp_eq_u32 s60,3
s_cselect_b32 s69,0x40000000,0
ds_read_b64_tr_b16 v[70:71],v1 offset:15424
v_mfma_f32_16x16x32_bf16 a[196:199],v[104:107],v[164:167],a[196:199]
ds_read_b64_tr_b16 v[72:73],v1 offset:20544
v_add_u32 v28,s69,v6
v_mfma_f32_16x16x32_bf16 a[196:199],v[100:103],v[148:151],a[196:199]
s_and_b32 s69,s63,s60
ds_read_b64_tr_b16 v[74:75],v1 offset:25664
v_mov_b32 v8,s52
v_mfma_f32_16x16x32_bf16 a[196:199],v[96:99],v[132:135],a[196:199]
buffer_store_dwordx4 v[244:247],v6,s[40:43],s82 offen offset:2048 sc1
v_mfma_f32_16x16x32_bf16 a[200:203],v[96:99],v[196:199],a[200:203]
ds_read_b64_tr_b16 v[76:77],v1 offset:30784
ds_read_b64_tr_b16 v[78:79],v1 offset:35904
v_mfma_f32_16x16x32_bf16 a[200:203],v[100:103],v[212:215],a[200:203]
ds_read_b64_tr_b16 v[80:81],v2 offset:96
v_mov_b32 v9,s53
v_mfma_f32_16x16x32_bf16 a[200:203],v[104:107],v[228:231],a[200:203]
buffer_load_dwordx4 a[148:151],v6,s[28:31],s88 offen offset:1024 sc1
v_mfma_f32_16x16x32_bf16 a[200:203],v[108:111],v[244:247],a[200:203]
ds_read_b64_tr_b16 v[82:83],v2 offset:5216
ds_read_b64_tr_b16 v[84:85],v2 offset:10336
v_mfma_f32_16x16x32_bf16 a[204:207],v[108:111],v[188:191],a[204:207]
ds_read_b64_tr_b16 v[86:87],v2 offset:15456
v_lshlrev_b32 v10,20,v6
v_mfma_f32_16x16x32_bf16 a[204:207],v[104:107],v[172:175],a[204:207]
ds_read_b64_tr_b16 v[88:89],v2 offset:20576
v_bfe_u32 v3,v6,4,2
v_mfma_f32_16x16x32_bf16 a[204:207],v[100:103],v[156:159],a[204:207]
ds_read_b64_tr_b16 v[90:91],v2 offset:25696
v_min_u32 v3,2,v3
v_mfma_f32_16x16x32_bf16 a[204:207],v[96:99],v[140:143],a[204:207]
s_waitcnt lgkmcnt(8)
ds_read_b64_tr_b16 v[92:93],v2 offset:30816
ds_read_b64_tr_b16 v[94:95],v2 offset:35936
v_mfma_f32_16x16x32_bf16 a[208:211],v[112:115],v[32:35],a[208:211]
ds_read_b64_tr_b16 v[96:97],v1 offset:128
v_add3_u32 v3,v3,s64,1
v_mfma_f32_16x16x32_bf16 a[208:211],v[116:119],v[36:39],a[208:211]
ds_read_b64_tr_b16 v[98:99],v1 offset:5248
v_and_b32 v3,3,v3
v_mfma_f32_16x16x32_bf16 a[208:211],v[120:123],v[40:43],a[208:211]
ds_read_b64_tr_b16 v[100:101],v1 offset:10368
v_lshlrev_b32 v3,6,v3
v_mfma_f32_16x16x32_bf16 a[208:211],v[124:127],v[44:47],a[208:211]
ds_read_b64_tr_b16 v[102:103],v1 offset:15488
ds_read_b64_tr_b16 v[104:105],v1 offset:20608
v_mfma_f32_16x16x32_bf16 a[212:215],v[124:127],v[180:183],a[212:215]
ds_read_b64_tr_b16 v[106:107],v1 offset:25728
v_bfe_u32 v56,v6,6,2
v_mfma_f32_16x16x32_bf16 a[212:215],v[120:123],v[164:167],a[212:215]
ds_read_b64_tr_b16 v[108:109],v1 offset:30848
v_lshrrev_b32 v57,8,v6
v_mfma_f32_16x16x32_bf16 a[212:215],v[116:119],v[148:151],a[212:215]
buffer_store_dwordx4 v[140:143],v28,s[40:43],s79 offen offset:3072 sc1
v_mfma_f32_16x16x32_bf16 a[212:215],v[112:115],v[132:135],a[212:215]
ds_read_b64_tr_b16 v[110:111],v1 offset:35968
v_cmp_eq_u32 s[56:57],v56,v57
L_pd20_%=:
s_cbranch_execz L_trt1_%=
v_mfma_f32_16x16x32_bf16 a[216:219],v[112:115],v[196:199],a[216:219]
buffer_load_dwordx4 a[132:135],v6,s[28:31],s87 offen offset:1024 sc1
v_mfma_f32_16x16x32_bf16 a[216:219],v[116:119],v[212:215],a[216:219]
v_bfe_u32 v56,v6,4,1
v_lshlrev_b32 v56,4,v56
v_mfma_f32_16x16x32_bf16 a[216:219],v[120:123],v[228:231],a[216:219]
v_mov_b32 v57,0x3f80
v_lshlrev_b32 v56,v56,v57
v_mfma_f32_16x16x32_bf16 a[216:219],v[124:127],v[244:247],a[216:219]
v_cndmask_b32 v56,0,v56,s[56:57]
v_bfe_u32 v57,v6,5,1
v_mfma_f32_16x16x32_bf16 a[220:223],v[124:127],v[188:191],a[220:223]
ds_read_b64_tr_b16 v[124:125],v2 offset:30880
ds_read_b64_tr_b16 v[126:127],v2 offset:36000
v_mfma_f32_16x16x32_bf16 a[220:223],v[120:123],v[172:175],a[220:223]
ds_read_b64_tr_b16 v[120:121],v2 offset:20640
v_cmp_eq_u32 s[56:57],0,v57
v_mfma_f32_16x16x32_bf16 a[220:223],v[116:119],v[156:159],a[220:223]
s_add_u32 s36,s12,s66
s_addc_u32 s37,s13,0
ds_read_b64_tr_b16 v[116:117],v2 offset:10400
v_mfma_f32_16x16x32_bf16 a[220:223],v[112:115],v[140:143],a[220:223]
s_and_b32 s37,s37,0xffff
ds_read_b64_tr_b16 v[112:113],v2 offset:160
v_cndmask_b32 v48,0,v56,s[56:57]
v_mfma_f32_16x16x32_bf16 a[224:227],v[64:67],v[32:35],a[224:227]
s_mul_i32 s66,s61,307200
ds_read_b64_tr_b16 v[114:115],v2 offset:5280
ds_read_b64_tr_b16 v[118:119],v2 offset:15520
v_mfma_f32_16x16x32_bf16 a[224:227],v[68:71],v[36:39],a[224:227]
ds_read_b64_tr_b16 v[122:123],v2 offset:25760
v_cndmask_b32 v49,v56,0,s[56:57]
v_mfma_f32_16x16x32_bf16 a[224:227],v[72:75],v[40:43],a[224:227]
s_waitcnt lgkmcnt(8)
v_mov_b32 v58,s52
v_mov_b32 v59,s53
v_mfma_f32_16x16x32_bf16 a[224:227],v[76:79],v[44:47],a[224:227]
s_add_u32 s24,s16,s66
s_addc_u32 s25,s17,0
v_lshlrev_b32 v60,20,v6
v_mfma_f32_16x16x32_bf16 a[228:231],v[76:79],v[180:183],a[228:231]
buffer_load_dwordx4 a[136:139],v6,s[28:31],s87 offen offset:2048 sc1
v_mfma_f32_16x16x32_bf16 a[228:231],v[72:75],v[164:167],a[228:231]
s_mul_i32 s66,s60,16384
s_add_u32 s66,s66,s67
s_add_u32 s79,s66,12288
v_mfma_f32_16x16x32_bf16 a[228:231],v[68:71],v[148:151],a[228:231]
buffer_store_dwordx4 v[156:159],v28,s[40:43],s80 offen offset:3072 sc1
v_mfma_f32_16x16x32_bf16 a[228:231],v[64:67],v[132:135],a[228:231]
s_add_u32 s80,s66,77824
v_bfe_u32 v61,v6,4,1
v_add3_u32 v61,v61,s63,2
v_mfma_f32_16x16x32_bf16 a[232:235],v[64:67],v[196:199],a[232:235]
s_and_b32 s25,s25,0xffff
v_and_b32 v61,3,v61
v_lshlrev_b32 v61,8,v61
v_mfma_f32_16x16x32_bf16 a[232:235],v[68:71],v[212:215],a[232:235]
s_add_u32 s67,s77,8192
v_mov_b32 v50,0
v_mov_b32 v51,0
v_mfma_f32_16x16x32_bf16 a[232:235],v[72:75],v[228:231],a[232:235]
v_mov_b32 v52,0
v_mov_b32 v53,0
v_mfma_f32_16x16x32_bf16 a[232:235],v[76:79],v[244:247],a[232:235]
v_mov_b32 v54,v48
v_mov_b32 v55,v49
v_mfma_f32_16x16x32_bf16 a[236:239],v[76:79],v[188:191],a[236:239]
ds_read_b64_tr_b16 v[76:77],v1 offset:30912
ds_read_b64_tr_b16 v[78:79],v1 offset:36032
v_mfma_f32_16x16x32_bf16 a[236:239],v[72:75],v[172:175],a[236:239]
ds_read_b64_tr_b16 v[72:73],v1 offset:20672
v_bfe_u32 v56,v6,4,4
v_mfma_f32_16x16x32_bf16 a[236:239],v[68:71],v[156:159],a[236:239]
ds_read_b64_tr_b16 v[68:69],v1 offset:10432
v_mul_u32_u24 v56,1280,v56
v_mfma_f32_16x16x32_bf16 a[236:239],v[64:67],v[140:143],a[236:239]
ds_read_b64_tr_b16 v[64:65],v1 offset:192
v_bfe_u32 v57,v6,8,1
v_mfma_f32_16x16x32_bf16 a[240:243],v[80:83],v[32:35],a[240:243]
ds_read_b64_tr_b16 v[66:67],v1 offset:5312
ds_read_b64_tr_b16 v[70:71],v1 offset:15552
v_mfma_f32_16x16x32_bf16 a[240:243],v[84:87],v[36:39],a[240:243]
buffer_load_dwordx4 a[152:155],v6,s[28:31],s88 offen offset:2048 sc1
v_mfma_f32_16x16x32_bf16 a[240:243],v[88:91],v[40:43],a[240:243]
ds_read_b64_tr_b16 v[74:75],v1 offset:25792
v_lshl_add_u32 v56,v57,5,v56
v_mfma_f32_16x16x32_bf16 a[240:243],v[92:95],v[44:47],a[240:243]
v_lshrrev_b32 v57,9,v6
v_lshl_add_u32 v56,v57,4,v56
v_mfma_f32_16x16x32_bf16 a[244:247],v[92:95],v[180:183],a[244:247]
s_cmp_eq_u32 s69,3
s_cselect_b32 s69,0x40000000,0
s_add_u32 s68,s68,s69
v_mfma_f32_16x16x32_bf16 a[244:247],v[88:91],v[164:167],a[244:247]
buffer_store_dwordx4 v[172:175],v28,s[40:43],s81 offen offset:3072 sc1
v_mfma_f32_16x16x32_bf16 a[244:247],v[84:87],v[148:151],a[244:247]
s_add_u32 s81,s66,143360
v_mfma_f32_16x16x32_bf16 a[244:247],v[80:83],v[132:135],a[244:247]
v_mfma_f32_16x16x32_bf16 a[248:251],v[80:83],v[196:199],a[248:251]
v_mfma_f32_16x16x32_bf16 a[248:251],v[84:87],v[212:215],a[248:251]
v_mfma_f32_16x16x32_bf16 a[248:251],v[88:91],v[228:231],a[248:251]
v_mfma_f32_16x16x32_bf16 a[248:251],v[92:95],v[244:247],a[248:251]
v_mfma_f32_16x16x32_bf16 a[252:255],v[92:95],v[188:191],a[252:255]
ds_read_b64_tr_b16 v[92:93],v2 offset:30944
ds_read_b64_tr_b16 v[94:95],v2 offset:36064
v_mfma_f32_16x16x32_bf16 a[252:255],v[88:91],v[172:175],a[252:255]
ds_read_b64_tr_b16 v[88:89],v2 offset:20704
v_mfma_f32_16x16x32_bf16 a[252:255],v[84:87],v[156:159],a[252:255]
buffer_load_dwordx4 a[168:171],v6,s[28:31],s89 offen offset:2048 sc1
v_mfma_f32_16x16x32_bf16 a[252:255],v[80:83],v[140:143],a[252:255]
ds_read_b64_tr_b16 v[80:81],v2 offset:224
ds_read_b64_tr_b16 v[82:83],v2 offset:5344
v_mfma_f32_16x16x32_bf16 a[0:3],v[96:99],v[32:35],a[0:3]
ds_read_b64_tr_b16 v[84:85],v2 offset:10464
v_mfma_f32_16x16x32_bf16 a[0:3],v[100:103],v[36:39],a[0:3]
ds_read_b64_tr_b16 v[86:87],v2 offset:15584
v_mfma_f32_16x16x32_bf16 a[0:3],v[104:107],v[40:43],a[0:3]
ds_read_b64_tr_b16 v[90:91],v2 offset:25824
v_mfma_f32_16x16x32_bf16 a[0:3],v[108:111],v[44:47],a[0:3]
v_mfma_f32_16x16x32_bf16 a[4:7],v[108:111],v[180:183],a[4:7]
buffer_store_dwordx4 v[188:191],v28,s[40:43],s82 offen offset:3072 sc1
v_mfma_f32_16x16x32_bf16 a[4:7],v[104:107],v[164:167],a[4:7]
s_add_u32 s82,s66,208896
s_mul_i32 s66,s64,320
v_mfma_f32_16x16x32_bf16 a[4:7],v[100:103],v[148:151],a[4:7]
v_mfma_f32_16x16x32_bf16 a[4:7],v[96:99],v[132:135],a[4:7]
v_mfma_f32_16x16x32_bf16 a[8:11],v[96:99],v[196:199],a[8:11]
v_mfma_f32_16x16x32_bf16 a[8:11],v[100:103],v[212:215],a[8:11]
v_mfma_f32_16x16x32_bf16 a[8:11],v[104:107],v[228:231],a[8:11]
v_mfma_f32_16x16x32_bf16 a[8:11],v[108:111],v[244:247],a[8:11]
buffer_load_dwordx4 a[184:187],v6,s[28:31],s90 offen offset:2048 sc1
v_mfma_f32_16x16x32_bf16 a[12:15],v[108:111],v[188:191],a[12:15]
ds_read_b64_tr_b16 v[108:109],v1 offset:30976
)A"
R"A(ds_read_b64_tr_b16 v[110:111],v1 offset:36096
v_mfma_f32_16x16x32_bf16 a[12:15],v[104:107],v[172:175],a[12:15]
ds_read_b64_tr_b16 v[104:105],v1 offset:20736
v_mfma_f32_16x16x32_bf16 a[12:15],v[100:103],v[156:159],a[12:15]
ds_read_b64_tr_b16 v[100:101],v1 offset:10496
v_mfma_f32_16x16x32_bf16 a[12:15],v[96:99],v[140:143],a[12:15]
s_waitcnt lgkmcnt(4)
ds_read_b64_tr_b16 v[96:97],v1 offset:256
v_mfma_f32_16x16x32_bf16 a[16:19],v[112:115],v[32:35],a[16:19]
ds_read_b64_tr_b16 v[98:99],v1 offset:5376
ds_read_b64_tr_b16 v[102:103],v1 offset:15616
v_mfma_f32_16x16x32_bf16 a[16:19],v[116:119],v[36:39],a[16:19]
ds_read_b64_tr_b16 v[106:107],v1 offset:25856
v_add_u32 v1,0xffff6000,v1
v_mfma_f32_16x16x32_bf16 a[16:19],v[120:123],v[40:43],a[16:19]
v_mfma_f32_16x16x32_bf16 a[16:19],v[124:127],v[44:47],a[16:19]
v_mfma_f32_16x16x32_bf16 a[20:23],v[124:127],v[180:183],a[20:23]
v_mfma_f32_16x16x32_bf16 a[20:23],v[120:123],v[164:167],a[20:23]
v_mfma_f32_16x16x32_bf16 a[20:23],v[116:119],v[148:151],a[20:23]
v_mfma_f32_16x16x32_bf16 a[20:23],v[112:115],v[132:135],a[20:23]
v_mfma_f32_16x16x32_bf16 a[24:27],v[112:115],v[196:199],a[24:27]
buffer_load_dwordx4 a[188:191],v28,s[28:31],s90 offen offset:3072 sc1
v_mfma_f32_16x16x32_bf16 a[24:27],v[116:119],v[212:215],a[24:27]
v_mfma_f32_16x16x32_bf16 a[24:27],v[120:123],v[228:231],a[24:27]
v_mfma_f32_16x16x32_bf16 a[24:27],v[124:127],v[244:247],a[24:27]
v_mfma_f32_16x16x32_bf16 a[28:31],v[124:127],v[188:191],a[28:31]
ds_read_b64_tr_b16 v[124:125],v2 offset:31008
ds_read_b64_tr_b16 v[126:127],v2 offset:36128
v_mfma_f32_16x16x32_bf16 a[28:31],v[120:123],v[172:175],a[28:31]
ds_read_b64_tr_b16 v[120:121],v2 offset:20768
v_mfma_f32_16x16x32_bf16 a[28:31],v[116:119],v[156:159],a[28:31]
ds_read_b64_tr_b16 v[116:117],v2 offset:10528
v_mfma_f32_16x16x32_bf16 a[28:31],v[112:115],v[140:143],a[28:31]
ds_read_b64_tr_b16 v[112:113],v2 offset:288
v_mfma_f32_16x16x32_bf16 v[128:131],v[64:67],v[32:35],v[128:131]
ds_read_b64_tr_b16 v[114:115],v2 offset:5408
ds_read_b64_tr_b16 v[118:119],v2 offset:15648
v_mfma_f32_16x16x32_bf16 v[128:131],v[68:71],v[36:39],v[128:131]
ds_read_b64_tr_b16 v[122:123],v2 offset:25888
v_add_u32 v2,0xffff6000,v2
v_mfma_f32_16x16x32_bf16 v[128:131],v[72:75],v[40:43],v[128:131]
v_mfma_f32_16x16x32_bf16 v[128:131],v[76:79],v[44:47],v[128:131]
v_mfma_f32_16x16x32_bf16 v[136:139],v[76:79],v[180:183],v[136:139]
v_mfma_f32_16x16x32_bf16 v[136:139],v[72:75],v[164:167],v[136:139]
buffer_load_dwordx4 a[172:175],v28,s[28:31],s89 offen offset:3072 sc1
v_mfma_f32_16x16x32_bf16 v[136:139],v[68:71],v[148:151],v[136:139]
v_mfma_f32_16x16x32_bf16 v[136:139],v[64:67],v[132:135],v[136:139]
v_mfma_f32_16x16x32_bf16 v[144:147],v[64:67],v[196:199],v[144:147]
v_mfma_f32_16x16x32_bf16 v[144:147],v[68:71],v[212:215],v[144:147]
v_mfma_f32_16x16x32_bf16 v[144:147],v[72:75],v[228:231],v[144:147]
v_mfma_f32_16x16x32_bf16 v[144:147],v[76:79],v[244:247],v[144:147]
v_mfma_f32_16x16x32_bf16 v[152:155],v[76:79],v[188:191],v[152:155]
v_mfma_f32_16x16x32_bf16 v[152:155],v[72:75],v[172:175],v[152:155]
v_mfma_f32_16x16x32_bf16 v[152:155],v[68:71],v[156:159],v[152:155]
v_mfma_f32_16x16x32_bf16 v[152:155],v[64:67],v[140:143],v[152:155]
s_mov_b64 exec,0
s_cmp_eq_u32 s60,0
s_cbranch_scc1 L_pd21_%=
s_cmp_eq_u32 s60,1
s_cbranch_scc1 L_pd22_%=
s_cmp_eq_u32 s60,2
s_cbranch_scc1 L_pd23_%=
s_branch L_trtg2S_%=
L_trtg2S_%=:
s_mov_b64 exec,-1
s_waitcnt lgkmcnt(0)
s_barrier
v_mfma_f32_16x16x32_bf16 v[160:163],v[80:83],v[32:35],v[160:163]
buffer_load_dwordx4 v24,s[48:51],s83 offen lds
v_mfma_f32_16x16x32_bf16 v[160:163],v[84:87],v[36:39],v[160:163]
ds_read_b64_tr_b16 v[64:65],v1
ds_read_b64_tr_b16 v[66:67],v1 offset:5120
v_mfma_f32_16x16x32_bf16 v[160:163],v[88:91],v[40:43],v[160:163]
ds_read_b64_tr_b16 v[68:69],v1 offset:10240
v_mfma_f32_16x16x32_bf16 v[160:163],v[92:95],v[44:47],v[160:163]
ds_read_b64_tr_b16 v[70:71],v1 offset:15360
v_mfma_f32_16x16x32_bf16 v[168:171],v[92:95],v[180:183],v[168:171]
ds_read_b64_tr_b16 v[72:73],v1 offset:20480
v_mfma_f32_16x16x32_bf16 v[168:171],v[88:91],v[164:167],v[168:171]
buffer_load_dwordx4 a[156:159],v28,s[28:31],s88 offen offset:3072 sc1
v_mfma_f32_16x16x32_bf16 v[168:171],v[84:87],v[148:151],v[168:171]
ds_read_b64_tr_b16 v[74:75],v1 offset:25600
ds_read_b64_tr_b16 v[76:77],v1 offset:30720
v_mfma_f32_16x16x32_bf16 v[168:171],v[80:83],v[132:135],v[168:171]
ds_read_b64_tr_b16 v[78:79],v1 offset:35840
v_mfma_f32_16x16x32_bf16 v[176:179],v[80:83],v[196:199],v[176:179]
v_mfma_f32_16x16x32_bf16 v[176:179],v[84:87],v[212:215],v[176:179]
v_mfma_f32_16x16x32_bf16 v[176:179],v[88:91],v[228:231],v[176:179]
v_mfma_f32_16x16x32_bf16 v[176:179],v[92:95],v[244:247],v[176:179]
buffer_load_dwordx4 v25,s[48:51],s83 offen offset:1024 lds
v_mfma_f32_16x16x32_bf16 v[184:187],v[92:95],v[188:191],v[184:187]
ds_read_b64_tr_b16 v[92:93],v2 offset:30752
ds_read_b64_tr_b16 v[94:95],v2 offset:35872
v_mfma_f32_16x16x32_bf16 v[184:187],v[88:91],v[172:175],v[184:187]
ds_read_b64_tr_b16 v[88:89],v2 offset:20512
v_mfma_f32_16x16x32_bf16 v[184:187],v[84:87],v[156:159],v[184:187]
ds_read_b64_tr_b16 v[84:85],v2 offset:10272
v_mfma_f32_16x16x32_bf16 v[184:187],v[80:83],v[140:143],v[184:187]
ds_read_b64_tr_b16 v[80:81],v2 offset:32
v_mfma_f32_16x16x32_bf16 v[192:195],v[96:99],v[32:35],v[192:195]
buffer_load_dwordx4 a[140:143],v28,s[28:31],s87 offen offset:3072 sc1
v_mfma_f32_16x16x32_bf16 v[192:195],v[100:103],v[36:39],v[192:195]
s_add_u32 s87,s64,2
s_and_b32 s87,s87,3
s_lshl_b32 s87,s87,1
v_mfma_f32_16x16x32_bf16 v[192:195],v[104:107],v[40:43],v[192:195]
s_lshl_b32 s87,s87,16
s_add_u32 s88,s87,4096
s_add_u32 s89,s87,8192
v_mfma_f32_16x16x32_bf16 v[192:195],v[108:111],v[44:47],v[192:195]
s_add_u32 s90,s87,12288
ds_read_b64_tr_b16 v[82:83],v2 offset:5152
ds_read_b64_tr_b16 v[86:87],v2 offset:15392
v_mfma_f32_16x16x32_bf16 v[200:203],v[108:111],v[180:183],v[200:203]
ds_read_b64_tr_b16 v[90:91],v2 offset:25632
v_mfma_f32_16x16x32_bf16 v[200:203],v[104:107],v[164:167],v[200:203]
buffer_load_dwordx4 a[68:71],v6,s[28:31],s87 offen sc1
v_mfma_f32_16x16x32_bf16 v[200:203],v[100:103],v[148:151],v[200:203]
v_mfma_f32_16x16x32_bf16 v[200:203],v[96:99],v[132:135],v[200:203]
v_mfma_f32_16x16x32_bf16 v[208:211],v[96:99],v[196:199],v[208:211]
v_mfma_f32_16x16x32_bf16 v[208:211],v[100:103],v[212:215],v[208:211]
v_mfma_f32_16x16x32_bf16 v[208:211],v[104:107],v[228:231],v[208:211]
buffer_load_dwordx4 a[76:79],v6,s[28:31],s88 offen sc1
v_mfma_f32_16x16x32_bf16 v[208:211],v[108:111],v[244:247],v[208:211]
v_mfma_f32_16x16x32_bf16 v[216:219],v[108:111],v[188:191],v[216:219]
ds_read_b64_tr_b16 v[108:109],v1 offset:30784
ds_read_b64_tr_b16 v[110:111],v1 offset:35904
v_mfma_f32_16x16x32_bf16 v[216:219],v[104:107],v[172:175],v[216:219]
ds_read_b64_tr_b16 v[104:105],v1 offset:20544
v_mfma_f32_16x16x32_bf16 v[216:219],v[100:103],v[156:159],v[216:219]
ds_read_b64_tr_b16 v[100:101],v1 offset:10304
v_mfma_f32_16x16x32_bf16 v[216:219],v[96:99],v[140:143],v[216:219]
buffer_load_dwordx4 a[84:87],v6,s[28:31],s89 offen sc1
v_mfma_f32_16x16x32_bf16 v[224:227],v[112:115],v[32:35],v[224:227]
ds_read_b64_tr_b16 v[96:97],v1 offset:64
ds_read_b64_tr_b16 v[98:99],v1 offset:5184
v_mfma_f32_16x16x32_bf16 v[224:227],v[116:119],v[36:39],v[224:227]
ds_read_b64_tr_b16 v[102:103],v1 offset:15424
v_mfma_f32_16x16x32_bf16 v[224:227],v[120:123],v[40:43],v[224:227]
ds_read_b64_tr_b16 v[106:107],v1 offset:25664
v_mfma_f32_16x16x32_bf16 v[224:227],v[124:127],v[44:47],v[224:227]
v_mfma_f32_16x16x32_bf16 v[232:235],v[124:127],v[180:183],v[232:235]
buffer_load_dwordx4 a[92:95],v6,s[28:31],s90 offen sc1
v_mfma_f32_16x16x32_bf16 v[232:235],v[120:123],v[164:167],v[232:235]
v_mfma_f32_16x16x32_bf16 v[232:235],v[116:119],v[148:151],v[232:235]
v_mfma_f32_16x16x32_bf16 v[232:235],v[112:115],v[132:135],v[232:235]
v_mfma_f32_16x16x32_bf16 v[240:243],v[112:115],v[196:199],v[240:243]
v_mfma_f32_16x16x32_bf16 v[240:243],v[116:119],v[212:215],v[240:243]
buffer_load_dwordx4 a[124:127],v6,s[28:31],s90 offen offset:1024 sc1
v_mfma_f32_16x16x32_bf16 v[240:243],v[120:123],v[228:231],v[240:243]
v_mfma_f32_16x16x32_bf16 v[240:243],v[124:127],v[244:247],v[240:243]
v_mfma_f32_16x16x32_bf16 v[248:251],v[124:127],v[188:191],v[248:251]
ds_read_b64_tr_b16 v[124:125],v2 offset:30816
ds_read_b64_tr_b16 v[126:127],v2 offset:35936
v_mfma_f32_16x16x32_bf16 v[248:251],v[120:123],v[172:175],v[248:251]
ds_read_b64_tr_b16 v[120:121],v2 offset:20576
v_mfma_f32_16x16x32_bf16 v[248:251],v[116:119],v[156:159],v[248:251]
buffer_load_dwordx4 a[116:119],v6,s[28:31],s89 offen offset:1024 sc1
v_mfma_f32_16x16x32_bf16 v[248:251],v[112:115],v[140:143],v[248:251]
s_waitcnt vmcnt(31) lgkmcnt(3)
ds_read_b64_tr_b16 v[112:113],v2 offset:96
ds_read_b64_tr_b16 v[114:115],v2 offset:5216
v_mfma_f32_16x16x32_bf16 a[192:195],v[64:67],a[128:131],a[192:195]
ds_read_b64_tr_b16 v[116:117],v2 offset:10336
v_mfma_f32_16x16x32_bf16 a[192:195],v[68:71],a[144:147],a[192:195]
s_waitcnt vmcnt(29)
ds_read_b64_tr_b16 v[118:119],v2 offset:15456
v_mfma_f32_16x16x32_bf16 a[192:195],v[72:75],a[160:163],a[192:195]
s_waitcnt vmcnt(25)
ds_read_b64_tr_b16 v[122:123],v2 offset:25696
v_mfma_f32_16x16x32_bf16 a[192:195],v[76:79],a[176:179],a[192:195]
buffer_load_dwordx4 a[108:111],v6,s[28:31],s88 offen offset:1024 sc1
v_mfma_f32_16x16x32_bf16 a[196:199],v[76:79],a[180:183],a[196:199]
s_waitcnt vmcnt(24)
v_mfma_f32_16x16x32_bf16 a[196:199],v[72:75],a[164:167],a[196:199]
s_waitcnt vmcnt(22)
v_mfma_f32_16x16x32_bf16 a[196:199],v[68:71],a[148:151],a[196:199]
s_waitcnt vmcnt(19)
v_mfma_f32_16x16x32_bf16 a[196:199],v[64:67],a[132:135],a[196:199]
s_waitcnt vmcnt(17)
v_mfma_f32_16x16x32_bf16 a[200:203],v[64:67],a[136:139],a[200:203]
buffer_load_dwordx4 a[100:103],v6,s[28:31],s87 offen offset:1024 sc1
v_mfma_f32_16x16x32_bf16 a[200:203],v[68:71],a[152:155],a[200:203]
s_waitcnt vmcnt(16)
v_mfma_f32_16x16x32_bf16 a[200:203],v[72:75],a[168:171],a[200:203]
s_waitcnt vmcnt(14)
v_mfma_f32_16x16x32_bf16 a[200:203],v[76:79],a[184:187],a[200:203]
s_waitcnt vmcnt(13)
v_mfma_f32_16x16x32_bf16 a[204:207],v[76:79],a[188:191],a[204:207]
s_waitcnt vmcnt(10)
ds_read_b64_tr_b16 v[76:77],v1 offset:30848
ds_read_b64_tr_b16 v[78:79],v1 offset:35968
v_mfma_f32_16x16x32_bf16 a[204:207],v[72:75],a[172:175],a[204:207]
buffer_load_dwordx4 a[64:67],v6,s[28:31],s87 offen offset:2048 sc1
v_mfma_f32_16x16x32_bf16 a[204:207],v[68:71],a[156:159],a[204:207]
s_waitcnt vmcnt(9)
ds_read_b64_tr_b16 v[68:69],v1 offset:10368
ds_read_b64_tr_b16 v[70:71],v1 offset:15488
v_mfma_f32_16x16x32_bf16 a[204:207],v[64:67],a[140:143],a[204:207]
ds_read_b64_tr_b16 v[64:65],v1 offset:128
v_mfma_f32_16x16x32_bf16 a[208:211],v[80:83],a[128:131],a[208:211]
ds_read_b64_tr_b16 v[66:67],v1 offset:5248
v_mfma_f32_16x16x32_bf16 a[208:211],v[84:87],a[144:147],a[208:211]
ds_read_b64_tr_b16 v[72:73],v1 offset:20608
v_mfma_f32_16x16x32_bf16 a[208:211],v[88:91],a[160:163],a[208:211]
ds_read_b64_tr_b16 v[74:75],v1 offset:25728
v_mfma_f32_16x16x32_bf16 a[208:211],v[92:95],a[176:179],a[208:211]
buffer_load_dwordx4 v26,s[48:51],s83 offen offset:2048 lds
v_mfma_f32_16x16x32_bf16 a[212:215],v[92:95],a[180:183],a[212:215]
v_mfma_f32_16x16x32_bf16 a[212:215],v[88:91],a[164:167],a[212:215]
v_mfma_f32_16x16x32_bf16 a[212:215],v[84:87],a[148:151],a[212:215]
v_mfma_f32_16x16x32_bf16 a[212:215],v[80:83],a[132:135],a[212:215]
v_mfma_f32_16x16x32_bf16 a[216:219],v[80:83],a[136:139],a[216:219]
buffer_load_dwordx4 a[72:75],v6,s[28:31],s88 offen offset:2048 sc1
v_mfma_f32_16x16x32_bf16 a[216:219],v[84:87],a[152:155],a[216:219]
v_mfma_f32_16x16x32_bf16 a[216:219],v[88:91],a[168:171],a[216:219]
v_mfma_f32_16x16x32_bf16 a[216:219],v[92:95],a[184:187],a[216:219]
v_mfma_f32_16x16x32_bf16 a[220:223],v[92:95],a[188:191],a[220:223]
ds_read_b64_tr_b16 v[92:93],v2 offset:30880
ds_read_b64_tr_b16 v[94:95],v2 offset:36000
v_mfma_f32_16x16x32_bf16 a[220:223],v[88:91],a[172:175],a[220:223]
ds_read_b64_tr_b16 v[88:89],v2 offset:20640
v_mfma_f32_16x16x32_bf16 a[220:223],v[84:87],a[156:159],a[220:223]
buffer_load_dwordx4 v27,s[48:51],s83 offen offset:3072 lds
)A"
R"A(v_mfma_f32_16x16x32_bf16 a[220:223],v[80:83],a[140:143],a[220:223]
s_add_u32 m0,s86,126976
ds_read_b64_tr_b16 v[80:81],v2 offset:160
ds_read_b64_tr_b16 v[82:83],v2 offset:5280
s_barrier
v_mfma_f32_16x16x32_bf16 a[224:227],v[96:99],a[128:131],a[224:227]
ds_read_b64_tr_b16 v[84:85],v2 offset:10400
v_mfma_f32_16x16x32_bf16 a[224:227],v[100:103],a[144:147],a[224:227]
ds_read_b64_tr_b16 v[86:87],v2 offset:15520
v_mfma_f32_16x16x32_bf16 a[224:227],v[104:107],a[160:163],a[224:227]
buffer_store_dwordx2 v[8:9],v10,s[44:47],s18 offen offset:1280 sc1
v_mfma_f32_16x16x32_bf16 a[224:227],v[108:111],a[176:179],a[224:227]
ds_read_b64_tr_b16 v[90:91],v2 offset:25760
v_mfma_f32_16x16x32_bf16 a[228:231],v[108:111],a[180:183],a[228:231]
v_mfma_f32_16x16x32_bf16 a[228:231],v[104:107],a[164:167],a[228:231]
v_mfma_f32_16x16x32_bf16 a[228:231],v[100:103],a[148:151],a[228:231]
v_mfma_f32_16x16x32_bf16 a[228:231],v[96:99],a[132:135],a[228:231]
buffer_load_dwordx4 a[80:83],v6,s[28:31],s89 offen offset:2048 sc1
v_mfma_f32_16x16x32_bf16 a[232:235],v[96:99],a[136:139],a[232:235]
v_mfma_f32_16x16x32_bf16 a[232:235],v[100:103],a[152:155],a[232:235]
v_mfma_f32_16x16x32_bf16 a[232:235],v[104:107],a[168:171],a[232:235]
v_mfma_f32_16x16x32_bf16 a[232:235],v[108:111],a[184:187],a[232:235]
v_mfma_f32_16x16x32_bf16 a[236:239],v[108:111],a[188:191],a[236:239]
s_waitcnt lgkmcnt(0)
ds_read_b64_tr_b16 v[108:109],v1 offset:30912
ds_read_b64_tr_b16 v[110:111],v1 offset:36032
v_mfma_f32_16x16x32_bf16 a[236:239],v[104:107],a[172:175],a[236:239]
buffer_load_dwordx4 v7,s[48:51],s83 offen lds
v_mfma_f32_16x16x32_bf16 a[236:239],v[100:103],a[156:159],a[236:239]
s_add_u32 m0,s86,128000
ds_read_b64_tr_b16 v[100:101],v1 offset:10432
ds_read_b64_tr_b16 v[102:103],v1 offset:15552
v_mfma_f32_16x16x32_bf16 a[236:239],v[96:99],a[140:143],a[236:239]
s_add_u32 s83,s64,1
s_and_b32 s83,s83,3
ds_read_b64_tr_b16 v[96:97],v1 offset:192
v_mfma_f32_16x16x32_bf16 a[240:243],v[112:115],a[128:131],a[240:243]
s_lshl_b32 s83,s83,1
s_or_b32 s83,s83,1
ds_read_b64_tr_b16 v[98:99],v1 offset:5312
v_mfma_f32_16x16x32_bf16 a[240:243],v[116:119],a[144:147],a[240:243]
s_mul_i32 s83,s83,163840
s_add_u32 s83,s83,s85
ds_read_b64_tr_b16 v[104:105],v1 offset:20672
v_mfma_f32_16x16x32_bf16 a[240:243],v[120:123],a[160:163],a[240:243]
buffer_load_dwordx4 a[88:91],v6,s[28:31],s90 offen offset:2048 sc1
v_mfma_f32_16x16x32_bf16 a[240:243],v[124:127],a[176:179],a[240:243]
ds_read_b64_tr_b16 v[106:107],v1 offset:25792
v_mfma_f32_16x16x32_bf16 a[244:247],v[124:127],a[180:183],a[244:247]
v_mfma_f32_16x16x32_bf16 a[244:247],v[120:123],a[164:167],a[244:247]
v_mfma_f32_16x16x32_bf16 a[244:247],v[116:119],a[148:151],a[244:247]
v_mfma_f32_16x16x32_bf16 a[244:247],v[112:115],a[132:135],a[244:247]
v_mfma_f32_16x16x32_bf16 a[248:251],v[112:115],a[136:139],a[248:251]
buffer_load_dwordx4 v24,s[48:51],s84 offen lds
v_mfma_f32_16x16x32_bf16 a[248:251],v[116:119],a[152:155],a[248:251]
v_mfma_f32_16x16x32_bf16 a[248:251],v[120:123],a[168:171],a[248:251]
v_mfma_f32_16x16x32_bf16 a[248:251],v[124:127],a[184:187],a[248:251]
v_mfma_f32_16x16x32_bf16 a[252:255],v[124:127],a[188:191],a[252:255]
ds_read_b64_tr_b16 v[124:125],v2 offset:30944
ds_read_b64_tr_b16 v[126:127],v2 offset:36064
v_mfma_f32_16x16x32_bf16 a[252:255],v[120:123],a[172:175],a[252:255]
buffer_load_dwordx4 a[120:123],v28,s[28:31],s90 offen offset:3072 sc1
v_mfma_f32_16x16x32_bf16 a[252:255],v[116:119],a[156:159],a[252:255]
ds_read_b64_tr_b16 v[116:117],v2 offset:10464
ds_read_b64_tr_b16 v[118:119],v2 offset:15584
v_mfma_f32_16x16x32_bf16 a[252:255],v[112:115],a[140:143],a[252:255]
ds_read_b64_tr_b16 v[112:113],v2 offset:224
v_mfma_f32_16x16x32_bf16 a[0:3],v[64:67],a[128:131],a[0:3]
ds_read_b64_tr_b16 v[114:115],v2 offset:5344
v_mfma_f32_16x16x32_bf16 a[0:3],v[68:71],a[144:147],a[0:3]
ds_read_b64_tr_b16 v[120:121],v2 offset:20704
v_mfma_f32_16x16x32_bf16 a[0:3],v[72:75],a[160:163],a[0:3]
ds_read_b64_tr_b16 v[122:123],v2 offset:25824
v_mfma_f32_16x16x32_bf16 a[0:3],v[76:79],a[176:179],a[0:3]
buffer_load_dwordx4 v25,s[48:51],s84 offen offset:1024 lds
v_mfma_f32_16x16x32_bf16 a[4:7],v[76:79],a[180:183],a[4:7]
v_mfma_f32_16x16x32_bf16 a[4:7],v[72:75],a[164:167],a[4:7]
v_mfma_f32_16x16x32_bf16 a[4:7],v[68:71],a[148:151],a[4:7]
v_mfma_f32_16x16x32_bf16 a[4:7],v[64:67],a[132:135],a[4:7]
v_mfma_f32_16x16x32_bf16 a[8:11],v[64:67],a[136:139],a[8:11]
buffer_load_dwordx4 a[112:115],v28,s[28:31],s89 offen offset:3072 sc1
v_mfma_f32_16x16x32_bf16 a[8:11],v[68:71],a[152:155],a[8:11]
v_mfma_f32_16x16x32_bf16 a[8:11],v[72:75],a[168:171],a[8:11]
v_mfma_f32_16x16x32_bf16 a[8:11],v[76:79],a[184:187],a[8:11]
v_mfma_f32_16x16x32_bf16 a[12:15],v[76:79],a[188:191],a[12:15]
ds_read_b64_tr_b16 v[76:77],v1 offset:30976
ds_read_b64_tr_b16 v[78:79],v1 offset:36096
v_mfma_f32_16x16x32_bf16 a[12:15],v[72:75],a[172:175],a[12:15]
buffer_load_dwordx4 a[104:107],v28,s[28:31],s88 offen offset:3072 sc1
v_mfma_f32_16x16x32_bf16 a[12:15],v[68:71],a[156:159],a[12:15]
ds_read_b64_tr_b16 v[68:69],v1 offset:10496
ds_read_b64_tr_b16 v[70:71],v1 offset:15616
v_mfma_f32_16x16x32_bf16 a[12:15],v[64:67],a[140:143],a[12:15]
ds_read_b64_tr_b16 v[64:65],v1 offset:256
v_mfma_f32_16x16x32_bf16 a[16:19],v[80:83],a[128:131],a[16:19]
ds_read_b64_tr_b16 v[66:67],v1 offset:5376
v_mfma_f32_16x16x32_bf16 a[16:19],v[84:87],a[144:147],a[16:19]
ds_read_b64_tr_b16 v[72:73],v1 offset:20736
v_mfma_f32_16x16x32_bf16 a[16:19],v[88:91],a[160:163],a[16:19]
ds_read_b64_tr_b16 v[74:75],v1 offset:25856
v_add_u32 v1,0xffff6000,v1
v_mfma_f32_16x16x32_bf16 a[16:19],v[92:95],a[176:179],a[16:19]
buffer_load_dwordx4 v26,s[48:51],s84 offen offset:2048 lds
v_mfma_f32_16x16x32_bf16 a[20:23],v[92:95],a[180:183],a[20:23]
v_mfma_f32_16x16x32_bf16 a[20:23],v[88:91],a[164:167],a[20:23]
v_mfma_f32_16x16x32_bf16 a[20:23],v[84:87],a[148:151],a[20:23]
v_mfma_f32_16x16x32_bf16 a[20:23],v[80:83],a[132:135],a[20:23]
v_mfma_f32_16x16x32_bf16 a[24:27],v[80:83],a[136:139],a[24:27]
buffer_load_dwordx4 a[96:99],v28,s[28:31],s87 offen offset:3072 sc1
v_mfma_f32_16x16x32_bf16 a[24:27],v[84:87],a[152:155],a[24:27]
s_add_u32 s87,s64,3
s_and_b32 s87,s87,3
s_lshl_b32 s87,s87,1
v_mfma_f32_16x16x32_bf16 a[24:27],v[88:91],a[168:171],a[24:27]
s_lshl_b32 s87,s87,16
s_add_u32 s88,s87,4096
s_add_u32 s89,s87,8192
v_mfma_f32_16x16x32_bf16 a[24:27],v[92:95],a[184:187],a[24:27]
s_add_u32 s90,s87,12288
v_mfma_f32_16x16x32_bf16 a[28:31],v[92:95],a[188:191],a[28:31]
ds_read_b64_tr_b16 v[92:93],v2 offset:31008
ds_read_b64_tr_b16 v[94:95],v2 offset:36128
v_mfma_f32_16x16x32_bf16 a[28:31],v[88:91],a[172:175],a[28:31]
s_waitcnt lgkmcnt(2)
ds_read_b64_tr_b16 v[88:89],v2 offset:20768
v_mfma_f32_16x16x32_bf16 a[28:31],v[84:87],a[156:159],a[28:31]
buffer_load_dwordx4 v27,s[48:51],s84 offen offset:3072 lds
v_mfma_f32_16x16x32_bf16 a[28:31],v[80:83],a[140:143],a[28:31]
s_add_u32 m0,s86,132096
ds_read_b64_tr_b16 v[80:81],v2 offset:288
ds_read_b64_tr_b16 v[82:83],v2 offset:5408
v_mfma_f32_16x16x32_bf16 v[128:131],v[96:99],a[128:131],v[128:131]
ds_read_b64_tr_b16 v[84:85],v2 offset:10528
v_mfma_f32_16x16x32_bf16 v[128:131],v[100:103],a[144:147],v[128:131]
ds_read_b64_tr_b16 v[86:87],v2 offset:15648
v_mfma_f32_16x16x32_bf16 v[128:131],v[104:107],a[160:163],v[128:131]
ds_read_b64_tr_b16 v[90:91],v2 offset:25888
v_add_u32 v2,0xffff6000,v2
v_mfma_f32_16x16x32_bf16 v[128:131],v[108:111],a[176:179],v[128:131]
v_mfma_f32_16x16x32_bf16 v[136:139],v[108:111],a[180:183],v[136:139]
buffer_load_dwordx4 v7,s[48:51],s84 offen lds
v_mfma_f32_16x16x32_bf16 v[136:139],v[104:107],a[164:167],v[136:139]
s_add_u32 m0,s86,81920
s_add_u32 s84,s83,20480
v_mfma_f32_16x16x32_bf16 v[136:139],v[100:103],a[148:151],v[136:139]
v_mfma_f32_16x16x32_bf16 v[136:139],v[96:99],a[132:135],v[136:139]
v_mfma_f32_16x16x32_bf16 v[144:147],v[96:99],a[136:139],v[144:147]
v_mfma_f32_16x16x32_bf16 v[144:147],v[100:103],a[152:155],v[144:147]
v_mfma_f32_16x16x32_bf16 v[144:147],v[104:107],a[168:171],v[144:147]
buffer_load_dwordx4 v24,s[48:51],s83 offen lds
v_mfma_f32_16x16x32_bf16 v[144:147],v[108:111],a[184:187],v[144:147]
v_mfma_f32_16x16x32_bf16 v[152:155],v[108:111],a[188:191],v[152:155]
ds_read_b64_tr_b16 v[108:109],v1 offset:30720
ds_read_b64_tr_b16 v[110:111],v1 offset:35840
v_mfma_f32_16x16x32_bf16 v[152:155],v[104:107],a[172:175],v[152:155]
ds_read_b64_tr_b16 v[104:105],v1 offset:20480
v_mfma_f32_16x16x32_bf16 v[152:155],v[100:103],a[156:159],v[152:155]
ds_read_b64_tr_b16 v[100:101],v1 offset:10240
v_mfma_f32_16x16x32_bf16 v[152:155],v[96:99],a[140:143],v[152:155]
ds_read_b64_tr_b16 v[96:97],v1
v_mfma_f32_16x16x32_bf16 v[160:163],v[112:115],a[128:131],v[160:163]
buffer_load_dwordx4 v25,s[48:51],s83 offen offset:1024 lds
v_mfma_f32_16x16x32_bf16 v[160:163],v[116:119],a[144:147],v[160:163]
ds_read_b64_tr_b16 v[98:99],v1 offset:5120
ds_read_b64_tr_b16 v[102:103],v1 offset:15360
v_mfma_f32_16x16x32_bf16 v[160:163],v[120:123],a[160:163],v[160:163]
ds_read_b64_tr_b16 v[106:107],v1 offset:25600
v_mfma_f32_16x16x32_bf16 v[160:163],v[124:127],a[176:179],v[160:163]
v_mfma_f32_16x16x32_bf16 v[168:171],v[124:127],a[180:183],v[168:171]
v_mfma_f32_16x16x32_bf16 v[168:171],v[120:123],a[164:167],v[168:171]
v_mfma_f32_16x16x32_bf16 v[168:171],v[116:119],a[148:151],v[168:171]
buffer_load_dwordx4 v26,s[48:51],s83 offen offset:2048 lds
v_mfma_f32_16x16x32_bf16 v[168:171],v[112:115],a[132:135],v[168:171]
v_mfma_f32_16x16x32_bf16 v[176:179],v[112:115],a[136:139],v[176:179]
v_mfma_f32_16x16x32_bf16 v[176:179],v[116:119],a[152:155],v[176:179]
v_mfma_f32_16x16x32_bf16 v[176:179],v[120:123],a[168:171],v[176:179]
v_mfma_f32_16x16x32_bf16 v[176:179],v[124:127],a[184:187],v[176:179]
v_mfma_f32_16x16x32_bf16 v[184:187],v[124:127],a[188:191],v[184:187]
buffer_load_dwordx4 v27,s[48:51],s83 offen offset:3072 lds
v_mfma_f32_16x16x32_bf16 v[184:187],v[120:123],a[172:175],v[184:187]
s_add_u32 m0,s86,86016
ds_read_b64_tr_b16 v[120:121],v2 offset:20512
ds_read_b64_tr_b16 v[122:123],v2 offset:25632
v_mfma_f32_16x16x32_bf16 v[184:187],v[116:119],a[156:159],v[184:187]
ds_read_b64_tr_b16 v[116:117],v2 offset:10272
v_mfma_f32_16x16x32_bf16 v[184:187],v[112:115],a[140:143],v[184:187]
ds_read_b64_tr_b16 v[112:113],v2 offset:32
L_pd21_%=:
s_cbranch_execz L_trtg2S_%=
v_mfma_f32_16x16x32_bf16 v[192:195],v[64:67],a[128:131],v[192:195]
ds_read_b64_tr_b16 v[114:115],v2 offset:5152
v_mfma_f32_16x16x32_bf16 v[192:195],v[68:71],a[144:147],v[192:195]
ds_read_b64_tr_b16 v[118:119],v2 offset:15392
ds_read_b64_tr_b16 v[124:125],v2 offset:30752
v_mfma_f32_16x16x32_bf16 v[192:195],v[72:75],a[160:163],v[192:195]
buffer_load_dwordx4 v7,s[48:51],s83 offen lds
v_mfma_f32_16x16x32_bf16 v[192:195],v[76:79],a[176:179],v[192:195]
s_add_u32 m0,s86,87040
s_add_u32 s83,s64,2
ds_read_b64_tr_b16 v[126:127],v2 offset:35872
v_mfma_f32_16x16x32_bf16 v[200:203],v[76:79],a[180:183],v[200:203]
s_and_b32 s83,s83,3
s_lshl_b32 s83,s83,1
s_or_b32 s83,s83,1
v_mfma_f32_16x16x32_bf16 v[200:203],v[72:75],a[164:167],v[200:203]
s_mul_i32 s83,s83,163840
s_add_u32 s83,s83,s85
v_mfma_f32_16x16x32_bf16 v[200:203],v[68:71],a[148:151],v[200:203]
v_mfma_f32_16x16x32_bf16 v[200:203],v[64:67],a[132:135],v[200:203]
v_mfma_f32_16x16x32_bf16 v[208:211],v[64:67],a[136:139],v[208:211]
buffer_load_dwordx4 v24,s[48:51],s84 offen lds
v_mfma_f32_16x16x32_bf16 v[208:211],v[68:71],a[152:155],v[208:211]
v_mfma_f32_16x16x32_bf16 v[208:211],v[72:75],a[168:171],v[208:211]
v_mfma_f32_16x16x32_bf16 v[208:211],v[76:79],a[184:187],v[208:211]
v_mfma_f32_16x16x32_bf16 v[216:219],v[76:79],a[188:191],v[216:219]
ds_read_b64_tr_b16 v[76:77],v1 offset:30784
ds_read_b64_tr_b16 v[78:79],v1 offset:35904
v_mfma_f32_16x16x32_bf16 v[216:219],v[72:75],a[172:175],v[216:219]
ds_read_b64_tr_b16 v[72:73],v1 offset:20544
v_mfma_f32_16x16x32_bf16 v[216:219],v[68:71],a[156:159],v[216:219]
buffer_load_dwordx2 v[4:5],v3,s[44:47],s91 offen offset:1280 sc1
v_mfma_f32_16x16x32_bf16 v[216:219],v[64:67],a[140:143],v[216:219]
s_waitcnt lgkmcnt(3)
ds_read_b64_tr_b16 v[64:65],v1 offset:64
ds_read_b64_tr_b16 v[66:67],v1 offset:5184
)A"
R"A(v_mfma_f32_16x16x32_bf16 v[224:227],v[80:83],a[128:131],v[224:227]
ds_read_b64_tr_b16 v[68:69],v1 offset:10304
v_mfma_f32_16x16x32_bf16 v[224:227],v[84:87],a[144:147],v[224:227]
ds_read_b64_tr_b16 v[70:71],v1 offset:15424
v_mfma_f32_16x16x32_bf16 v[224:227],v[88:91],a[160:163],v[224:227]
ds_read_b64_tr_b16 v[74:75],v1 offset:25664
v_mfma_f32_16x16x32_bf16 v[224:227],v[92:95],a[176:179],v[224:227]
v_mfma_f32_16x16x32_bf16 v[232:235],v[92:95],a[180:183],v[232:235]
buffer_load_dwordx4 v25,s[48:51],s84 offen offset:1024 lds
v_mfma_f32_16x16x32_bf16 v[232:235],v[88:91],a[164:167],v[232:235]
v_mfma_f32_16x16x32_bf16 v[232:235],v[84:87],a[148:151],v[232:235]
v_mfma_f32_16x16x32_bf16 v[232:235],v[80:83],a[132:135],v[232:235]
v_mfma_f32_16x16x32_bf16 v[240:243],v[80:83],a[136:139],v[240:243]
v_mfma_f32_16x16x32_bf16 v[240:243],v[84:87],a[152:155],v[240:243]
v_mfma_f32_16x16x32_bf16 v[240:243],v[88:91],a[168:171],v[240:243]
buffer_load_dwordx4 v26,s[48:51],s84 offen offset:2048 lds
v_mfma_f32_16x16x32_bf16 v[240:243],v[92:95],a[184:187],v[240:243]
v_mfma_f32_16x16x32_bf16 v[248:251],v[92:95],a[188:191],v[248:251]
ds_read_b64_tr_b16 v[92:93],v2 offset:30816
ds_read_b64_tr_b16 v[94:95],v2 offset:35936
v_mfma_f32_16x16x32_bf16 v[248:251],v[88:91],a[172:175],v[248:251]
ds_read_b64_tr_b16 v[88:89],v2 offset:20576
v_mfma_f32_16x16x32_bf16 v[248:251],v[84:87],a[156:159],v[248:251]
ds_read_b64_tr_b16 v[84:85],v2 offset:10336
v_mfma_f32_16x16x32_bf16 v[248:251],v[80:83],a[140:143],v[248:251]
s_waitcnt vmcnt(25)
ds_read_b64_tr_b16 v[80:81],v2 offset:96
v_mfma_f32_16x16x32_bf16 a[192:195],v[96:99],a[68:71],a[192:195]
buffer_load_dwordx4 v27,s[48:51],s84 offen offset:3072 lds
v_mfma_f32_16x16x32_bf16 a[192:195],v[100:103],a[76:79],a[192:195]
s_add_u32 m0,s86,91136
ds_read_b64_tr_b16 v[82:83],v2 offset:5216
ds_read_b64_tr_b16 v[86:87],v2 offset:15456
v_mfma_f32_16x16x32_bf16 a[192:195],v[104:107],a[84:87],a[192:195]
ds_read_b64_tr_b16 v[90:91],v2 offset:25696
v_mfma_f32_16x16x32_bf16 a[192:195],v[108:111],a[92:95],a[192:195]
v_mfma_f32_16x16x32_bf16 a[196:199],v[108:111],a[124:127],a[196:199]
v_mfma_f32_16x16x32_bf16 a[196:199],v[104:107],a[116:119],a[196:199]
v_mfma_f32_16x16x32_bf16 a[196:199],v[100:103],a[108:111],a[196:199]
buffer_load_dwordx4 v7,s[48:51],s84 offen lds
v_mfma_f32_16x16x32_bf16 a[196:199],v[96:99],a[100:103],a[196:199]
s_add_u32 m0,s86,40960
s_add_u32 s84,s83,20480
v_mfma_f32_16x16x32_bf16 a[200:203],v[96:99],a[64:67],a[200:203]
s_waitcnt vmcnt(25)
v_mfma_f32_16x16x32_bf16 a[200:203],v[100:103],a[72:75],a[200:203]
s_waitcnt vmcnt(22)
v_mfma_f32_16x16x32_bf16 a[200:203],v[104:107],a[80:83],a[200:203]
s_waitcnt vmcnt(18)
v_mfma_f32_16x16x32_bf16 a[200:203],v[108:111],a[88:91],a[200:203]
buffer_load_dwordx4 v[32:35],v6,s[28:31],s87 offen sc1
v_mfma_f32_16x16x32_bf16 a[204:207],v[108:111],a[120:123],a[204:207]
s_waitcnt vmcnt(17)
ds_read_b64_tr_b16 v[108:109],v1 offset:30848
ds_read_b64_tr_b16 v[110:111],v1 offset:35968
v_mfma_f32_16x16x32_bf16 a[204:207],v[104:107],a[112:115],a[204:207]
s_waitcnt vmcnt(16)
ds_read_b64_tr_b16 v[104:105],v1 offset:20608
v_mfma_f32_16x16x32_bf16 a[204:207],v[100:103],a[104:107],a[204:207]
s_waitcnt vmcnt(14)
ds_read_b64_tr_b16 v[100:101],v1 offset:10368
v_mfma_f32_16x16x32_bf16 a[204:207],v[96:99],a[96:99],a[204:207]
ds_read_b64_tr_b16 v[96:97],v1 offset:128
v_mfma_f32_16x16x32_bf16 a[208:211],v[112:115],a[68:71],a[208:211]
buffer_load_dwordx4 v[132:135],v6,s[28:31],s87 offen offset:1024 sc1
v_mfma_f32_16x16x32_bf16 a[208:211],v[116:119],a[76:79],a[208:211]
ds_read_b64_tr_b16 v[98:99],v1 offset:5248
ds_read_b64_tr_b16 v[102:103],v1 offset:15488
v_mfma_f32_16x16x32_bf16 a[208:211],v[120:123],a[84:87],a[208:211]
ds_read_b64_tr_b16 v[106:107],v1 offset:25728
v_mfma_f32_16x16x32_bf16 a[208:211],v[124:127],a[92:95],a[208:211]
v_mfma_f32_16x16x32_bf16 a[212:215],v[124:127],a[124:127],a[212:215]
v_mfma_f32_16x16x32_bf16 a[212:215],v[120:123],a[116:119],a[212:215]
buffer_load_dwordx4 v[196:199],v6,s[28:31],s87 offen offset:2048 sc1
v_mfma_f32_16x16x32_bf16 a[212:215],v[116:119],a[108:111],a[212:215]
v_mfma_f32_16x16x32_bf16 a[212:215],v[112:115],a[100:103],a[212:215]
v_mfma_f32_16x16x32_bf16 a[216:219],v[112:115],a[64:67],a[216:219]
v_mfma_f32_16x16x32_bf16 a[216:219],v[116:119],a[72:75],a[216:219]
v_mfma_f32_16x16x32_bf16 a[216:219],v[120:123],a[80:83],a[216:219]
buffer_load_dwordx4 v[36:39],v6,s[28:31],s88 offen sc1
v_mfma_f32_16x16x32_bf16 a[216:219],v[124:127],a[88:91],a[216:219]
v_mfma_f32_16x16x32_bf16 a[220:223],v[124:127],a[120:123],a[220:223]
ds_read_b64_tr_b16 v[124:125],v2 offset:30880
ds_read_b64_tr_b16 v[126:127],v2 offset:36000
v_mfma_f32_16x16x32_bf16 a[220:223],v[120:123],a[112:115],a[220:223]
ds_read_b64_tr_b16 v[120:121],v2 offset:20640
v_mfma_f32_16x16x32_bf16 a[220:223],v[116:119],a[104:107],a[220:223]
s_waitcnt lgkmcnt(3)
ds_read_b64_tr_b16 v[116:117],v2 offset:10400
v_mfma_f32_16x16x32_bf16 a[220:223],v[112:115],a[96:99],a[220:223]
buffer_load_dwordx4 v[140:143],v28,s[28:31],s87 offen offset:3072 sc1
v_mfma_f32_16x16x32_bf16 a[224:227],v[64:67],a[68:71],a[224:227]
s_add_u32 s87,s64,1
s_and_b32 s87,s87,3
s_lshl_b32 s87,s87,1
v_mfma_f32_16x16x32_bf16 a[224:227],v[68:71],a[76:79],a[224:227]
s_or_b32 s87,s87,1
s_lshl_b32 s87,s87,16
ds_read_b64_tr_b16 v[112:113],v2 offset:160
v_mfma_f32_16x16x32_bf16 a[224:227],v[72:75],a[84:87],a[224:227]
ds_read_b64_tr_b16 v[114:115],v2 offset:5280
ds_read_b64_tr_b16 v[118:119],v2 offset:15520
v_mfma_f32_16x16x32_bf16 a[224:227],v[76:79],a[92:95],a[224:227]
ds_read_b64_tr_b16 v[122:123],v2 offset:25760
v_mfma_f32_16x16x32_bf16 a[228:231],v[76:79],a[124:127],a[228:231]
buffer_load_dwordx4 v[212:215],v6,s[28:31],s88 offen offset:2048 sc1
v_mfma_f32_16x16x32_bf16 a[228:231],v[72:75],a[116:119],a[228:231]
v_mfma_f32_16x16x32_bf16 a[228:231],v[68:71],a[108:111],a[228:231]
v_mfma_f32_16x16x32_bf16 a[228:231],v[64:67],a[100:103],a[228:231]
v_mfma_f32_16x16x32_bf16 a[232:235],v[64:67],a[64:67],a[232:235]
v_mfma_f32_16x16x32_bf16 a[232:235],v[68:71],a[72:75],a[232:235]
buffer_load_dwordx4 v[148:151],v6,s[28:31],s88 offen offset:1024 sc1
v_mfma_f32_16x16x32_bf16 a[232:235],v[72:75],a[80:83],a[232:235]
s_waitcnt vmcnt(11)
v_cmp_ne_u32 s[56:57],s52,v4
v_cmp_ne_u32 s[58:59],s53,v5
v_mfma_f32_16x16x32_bf16 a[232:235],v[76:79],a[88:91],a[232:235]
s_or_b64 s[56:57],s[56:57],s[58:59]
s_cbranch_scc1 L_fr7_%=
L_fo7_%=:
s_lshl_b32 s91,s64,6
v_mfma_f32_16x16x32_bf16 a[236:239],v[76:79],a[120:123],a[236:239]
ds_read_b64_tr_b16 v[76:77],v1 offset:30912
ds_read_b64_tr_b16 v[78:79],v1 offset:36032
v_mfma_f32_16x16x32_bf16 a[236:239],v[72:75],a[112:115],a[236:239]
ds_read_b64_tr_b16 v[72:73],v1 offset:20672
v_bfe_u32 v3,v6,4,2
v_mfma_f32_16x16x32_bf16 a[236:239],v[68:71],a[104:107],a[236:239]
buffer_load_dwordx4 v[40:43],v6,s[28:31],s89 offen sc1
v_mfma_f32_16x16x32_bf16 a[236:239],v[64:67],a[96:99],a[236:239]
ds_read_b64_tr_b16 v[64:65],v1 offset:192
ds_read_b64_tr_b16 v[66:67],v1 offset:5312
v_mfma_f32_16x16x32_bf16 a[240:243],v[80:83],a[68:71],a[240:243]
ds_read_b64_tr_b16 v[68:69],v1 offset:10432
v_min_u32 v3,2,v3
v_mfma_f32_16x16x32_bf16 a[240:243],v[84:87],a[76:79],a[240:243]
ds_read_b64_tr_b16 v[70:71],v1 offset:15552
v_add3_u32 v3,v3,s63,1
v_mfma_f32_16x16x32_bf16 a[240:243],v[88:91],a[84:87],a[240:243]
ds_read_b64_tr_b16 v[74:75],v1 offset:25792
v_and_b32 v3,3,v3
v_mfma_f32_16x16x32_bf16 a[240:243],v[92:95],a[92:95],a[240:243]
buffer_load_dwordx4 a[128:131],v6,s[28:31],s87 offen sc1
v_mfma_f32_16x16x32_bf16 a[244:247],v[92:95],a[124:127],a[244:247]
v_lshlrev_b32 v3,8,v3
v_mfma_f32_16x16x32_bf16 a[244:247],v[88:91],a[116:119],a[244:247]
v_mfma_f32_16x16x32_bf16 a[244:247],v[84:87],a[108:111],a[244:247]
v_mfma_f32_16x16x32_bf16 a[244:247],v[80:83],a[100:103],a[244:247]
v_mfma_f32_16x16x32_bf16 a[248:251],v[80:83],a[64:67],a[248:251]
buffer_load_dwordx4 v[156:159],v28,s[28:31],s88 offen offset:3072 sc1
v_mfma_f32_16x16x32_bf16 a[248:251],v[84:87],a[72:75],a[248:251]
s_add_u32 s88,s87,4096
v_mfma_f32_16x16x32_bf16 a[248:251],v[88:91],a[80:83],a[248:251]
v_mfma_f32_16x16x32_bf16 a[248:251],v[92:95],a[88:91],a[248:251]
v_mfma_f32_16x16x32_bf16 a[252:255],v[92:95],a[120:123],a[252:255]
ds_read_b64_tr_b16 v[92:93],v2 offset:30944
ds_read_b64_tr_b16 v[94:95],v2 offset:36064
v_mfma_f32_16x16x32_bf16 a[252:255],v[88:91],a[112:115],a[252:255]
buffer_load_dwordx4 v[228:231],v6,s[28:31],s89 offen offset:2048 sc1
v_mfma_f32_16x16x32_bf16 a[252:255],v[84:87],a[104:107],a[252:255]
ds_read_b64_tr_b16 v[84:85],v2 offset:10464
ds_read_b64_tr_b16 v[86:87],v2 offset:15584
v_mfma_f32_16x16x32_bf16 a[252:255],v[80:83],a[96:99],a[252:255]
ds_read_b64_tr_b16 v[80:81],v2 offset:224
v_mfma_f32_16x16x32_bf16 a[0:3],v[96:99],a[68:71],a[0:3]
ds_read_b64_tr_b16 v[82:83],v2 offset:5344
v_mfma_f32_16x16x32_bf16 a[0:3],v[100:103],a[76:79],a[0:3]
ds_read_b64_tr_b16 v[88:89],v2 offset:20704
v_mfma_f32_16x16x32_bf16 a[0:3],v[104:107],a[84:87],a[0:3]
buffer_load_dwordx4 v[164:167],v6,s[28:31],s89 offen offset:1024 sc1
v_mfma_f32_16x16x32_bf16 a[0:3],v[108:111],a[92:95],a[0:3]
ds_read_b64_tr_b16 v[90:91],v2 offset:25824
v_mfma_f32_16x16x32_bf16 a[4:7],v[108:111],a[124:127],a[4:7]
v_mfma_f32_16x16x32_bf16 a[4:7],v[104:107],a[116:119],a[4:7]
v_mfma_f32_16x16x32_bf16 a[4:7],v[100:103],a[108:111],a[4:7]
v_mfma_f32_16x16x32_bf16 a[4:7],v[96:99],a[100:103],a[4:7]
buffer_load_dwordx4 v[44:47],v6,s[28:31],s90 offen sc1
v_mfma_f32_16x16x32_bf16 a[8:11],v[96:99],a[64:67],a[8:11]
v_mfma_f32_16x16x32_bf16 a[8:11],v[100:103],a[72:75],a[8:11]
v_mfma_f32_16x16x32_bf16 a[8:11],v[104:107],a[80:83],a[8:11]
v_mfma_f32_16x16x32_bf16 a[8:11],v[108:111],a[88:91],a[8:11]
v_mfma_f32_16x16x32_bf16 a[12:15],v[108:111],a[120:123],a[12:15]
buffer_load_dwordx4 a[144:147],v6,s[28:31],s88 offen sc1
v_mfma_f32_16x16x32_bf16 a[12:15],v[104:107],a[112:115],a[12:15]
ds_read_b64_tr_b16 v[104:105],v1 offset:20736
ds_read_b64_tr_b16 v[106:107],v1 offset:25856
v_mfma_f32_16x16x32_bf16 a[12:15],v[100:103],a[104:107],a[12:15]
ds_read_b64_tr_b16 v[100:101],v1 offset:10496
v_mfma_f32_16x16x32_bf16 a[12:15],v[96:99],a[96:99],a[12:15]
s_waitcnt lgkmcnt(3)
ds_read_b64_tr_b16 v[96:97],v1 offset:256
v_mfma_f32_16x16x32_bf16 a[16:19],v[112:115],a[68:71],a[16:19]
ds_read_b64_tr_b16 v[98:99],v1 offset:5376
v_mfma_f32_16x16x32_bf16 a[16:19],v[116:119],a[76:79],a[16:19]
buffer_load_dwordx4 v[172:175],v28,s[28:31],s89 offen offset:3072 sc1
v_mfma_f32_16x16x32_bf16 a[16:19],v[120:123],a[84:87],a[16:19]
s_add_u32 s89,s87,8192
ds_read_b64_tr_b16 v[102:103],v1 offset:15616
ds_read_b64_tr_b16 v[108:109],v1 offset:30976
v_mfma_f32_16x16x32_bf16 a[16:19],v[124:127],a[92:95],a[16:19]
ds_read_b64_tr_b16 v[110:111],v1 offset:36096
v_add_u32 v1,0x1e000,v1
v_mfma_f32_16x16x32_bf16 a[20:23],v[124:127],a[124:127],a[20:23]
v_mfma_f32_16x16x32_bf16 a[20:23],v[120:123],a[116:119],a[20:23]
v_mfma_f32_16x16x32_bf16 a[20:23],v[116:119],a[108:111],a[20:23]
buffer_load_dwordx4 v[180:183],v6,s[28:31],s90 offen offset:1024 sc1
v_mfma_f32_16x16x32_bf16 a[20:23],v[112:115],a[100:103],a[20:23]
v_mfma_f32_16x16x32_bf16 a[24:27],v[112:115],a[64:67],a[24:27]
v_mfma_f32_16x16x32_bf16 a[24:27],v[116:119],a[72:75],a[24:27]
v_mfma_f32_16x16x32_bf16 a[24:27],v[120:123],a[80:83],a[24:27]
v_mfma_f32_16x16x32_bf16 a[24:27],v[124:127],a[88:91],a[24:27]
buffer_load_dwordx4 a[160:163],v6,s[28:31],s89 offen sc1
v_mfma_f32_16x16x32_bf16 a[28:31],v[124:127],a[120:123],a[28:31]
ds_read_b64_tr_b16 v[124:125],v2 offset:31008
ds_read_b64_tr_b16 v[126:127],v2 offset:36128
v_mfma_f32_16x16x32_bf16 a[28:31],v[120:123],a[112:115],a[28:31]
ds_read_b64_tr_b16 v[120:121],v2 offset:20768
v_mfma_f32_16x16x32_bf16 a[28:31],v[116:119],a[104:107],a[28:31]
ds_read_b64_tr_b16 v[116:117],v2 offset:10528
v_mfma_f32_16x16x32_bf16 a[28:31],v[112:115],a[96:99],a[28:31]
ds_read_b64_tr_b16 v[112:113],v2 offset:288
v_mfma_f32_16x16x32_bf16 v[128:131],v[64:67],a[68:71],v[128:131]
buffer_load_dwordx4 v[244:247],v6,s[28:31],s90 offen offset:2048 sc1
v_mfma_f32_16x16x32_bf16 v[128:131],v[68:71],a[76:79],v[128:131]
)A"
R"A(ds_read_b64_tr_b16 v[114:115],v2 offset:5408
ds_read_b64_tr_b16 v[118:119],v2 offset:15648
v_mfma_f32_16x16x32_bf16 v[128:131],v[72:75],a[84:87],v[128:131]
ds_read_b64_tr_b16 v[122:123],v2 offset:25888
v_add_u32 v2,0x1e000,v2
v_mfma_f32_16x16x32_bf16 v[128:131],v[76:79],a[92:95],v[128:131]
v_mfma_f32_16x16x32_bf16 v[136:139],v[76:79],a[124:127],v[136:139]
v_mfma_f32_16x16x32_bf16 v[136:139],v[72:75],a[116:119],v[136:139]
buffer_load_dwordx4 v[188:191],v28,s[28:31],s90 offen offset:3072 sc1
v_mfma_f32_16x16x32_bf16 v[136:139],v[68:71],a[108:111],v[136:139]
s_add_u32 s90,s87,12288
v_mfma_f32_16x16x32_bf16 v[136:139],v[64:67],a[100:103],v[136:139]
v_mfma_f32_16x16x32_bf16 v[144:147],v[64:67],a[64:67],v[144:147]
v_mfma_f32_16x16x32_bf16 v[144:147],v[68:71],a[72:75],v[144:147]
v_mfma_f32_16x16x32_bf16 v[144:147],v[72:75],a[80:83],v[144:147]
buffer_load_dwordx4 a[176:179],v6,s[28:31],s90 offen sc1
v_mfma_f32_16x16x32_bf16 v[144:147],v[76:79],a[88:91],v[144:147]
v_mfma_f32_16x16x32_bf16 v[152:155],v[76:79],a[120:123],v[152:155]
v_mfma_f32_16x16x32_bf16 v[152:155],v[72:75],a[112:115],v[152:155]
v_mfma_f32_16x16x32_bf16 v[152:155],v[68:71],a[104:107],v[152:155]
v_mfma_f32_16x16x32_bf16 v[152:155],v[64:67],a[96:99],v[152:155]
buffer_load_dwordx4 a[180:183],v6,s[28:31],s90 offen offset:1024 sc1
s_waitcnt vmcnt(21)
s_barrier
v_mfma_f32_16x16x32_bf16 v[160:163],v[80:83],a[68:71],v[160:163]
ds_read_b64_tr_b16 v[64:65],v1
ds_read_b64_tr_b16 v[66:67],v1 offset:5120
v_mfma_f32_16x16x32_bf16 v[160:163],v[84:87],a[76:79],v[160:163]
ds_read_b64_tr_b16 v[68:69],v1 offset:10240
v_mfma_f32_16x16x32_bf16 v[160:163],v[88:91],a[84:87],v[160:163]
ds_read_b64_tr_b16 v[70:71],v1 offset:15360
v_mfma_f32_16x16x32_bf16 v[160:163],v[92:95],a[92:95],v[160:163]
buffer_load_dwordx4 a[164:167],v6,s[28:31],s89 offen offset:1024 sc1
v_mfma_f32_16x16x32_bf16 v[168:171],v[92:95],a[124:127],v[168:171]
ds_read_b64_tr_b16 v[72:73],v1 offset:20480
ds_read_b64_tr_b16 v[74:75],v1 offset:25600
v_mfma_f32_16x16x32_bf16 v[168:171],v[88:91],a[116:119],v[168:171]
ds_read_b64_tr_b16 v[76:77],v1 offset:30720
v_mfma_f32_16x16x32_bf16 v[168:171],v[84:87],a[108:111],v[168:171]
ds_read_b64_tr_b16 v[78:79],v1 offset:35840
v_mfma_f32_16x16x32_bf16 v[168:171],v[80:83],a[100:103],v[168:171]
v_mfma_f32_16x16x32_bf16 v[176:179],v[80:83],a[64:67],v[176:179]
buffer_load_dwordx4 a[148:151],v6,s[28:31],s88 offen offset:1024 sc1
v_mfma_f32_16x16x32_bf16 v[176:179],v[84:87],a[72:75],v[176:179]
v_mfma_f32_16x16x32_bf16 v[176:179],v[88:91],a[80:83],v[176:179]
v_mfma_f32_16x16x32_bf16 v[176:179],v[92:95],a[88:91],v[176:179]
v_mfma_f32_16x16x32_bf16 v[184:187],v[92:95],a[120:123],v[184:187]
ds_read_b64_tr_b16 v[92:93],v2 offset:30752
ds_read_b64_tr_b16 v[94:95],v2 offset:35872
v_mfma_f32_16x16x32_bf16 v[184:187],v[88:91],a[112:115],v[184:187]
buffer_load_dwordx4 a[132:135],v6,s[28:31],s87 offen offset:1024 sc1
v_mfma_f32_16x16x32_bf16 v[184:187],v[84:87],a[104:107],v[184:187]
ds_read_b64_tr_b16 v[84:85],v2 offset:10272
ds_read_b64_tr_b16 v[86:87],v2 offset:15392
v_mfma_f32_16x16x32_bf16 v[184:187],v[80:83],a[96:99],v[184:187]
s_waitcnt lgkmcnt(4)
ds_read_b64_tr_b16 v[80:81],v2 offset:32
v_mfma_f32_16x16x32_bf16 v[192:195],v[96:99],a[68:71],v[192:195]
ds_read_b64_tr_b16 v[82:83],v2 offset:5152
v_mfma_f32_16x16x32_bf16 v[192:195],v[100:103],a[76:79],v[192:195]
ds_read_b64_tr_b16 v[88:89],v2 offset:20512
v_mfma_f32_16x16x32_bf16 v[192:195],v[104:107],a[84:87],v[192:195]
ds_read_b64_tr_b16 v[90:91],v2 offset:25632
v_mfma_f32_16x16x32_bf16 v[192:195],v[108:111],a[92:95],v[192:195]
buffer_load_dwordx4 v24,s[48:51],s83 offen lds
v_mfma_f32_16x16x32_bf16 v[200:203],v[108:111],a[124:127],v[200:203]
v_mfma_f32_16x16x32_bf16 v[200:203],v[104:107],a[116:119],v[200:203]
v_mfma_f32_16x16x32_bf16 v[200:203],v[100:103],a[108:111],v[200:203]
v_mfma_f32_16x16x32_bf16 v[200:203],v[96:99],a[100:103],v[200:203]
v_mfma_f32_16x16x32_bf16 v[208:211],v[96:99],a[64:67],v[208:211]
buffer_load_dwordx4 a[136:139],v6,s[28:31],s87 offen offset:2048 sc1
v_mfma_f32_16x16x32_bf16 v[208:211],v[100:103],a[72:75],v[208:211]
v_mfma_f32_16x16x32_bf16 v[208:211],v[104:107],a[80:83],v[208:211]
v_mfma_f32_16x16x32_bf16 v[208:211],v[108:111],a[88:91],v[208:211]
v_mfma_f32_16x16x32_bf16 v[216:219],v[108:111],a[120:123],v[216:219]
ds_read_b64_tr_b16 v[108:109],v1 offset:30784
ds_read_b64_tr_b16 v[110:111],v1 offset:35904
v_mfma_f32_16x16x32_bf16 v[216:219],v[104:107],a[112:115],v[216:219]
ds_read_b64_tr_b16 v[104:105],v1 offset:20544
v_mfma_f32_16x16x32_bf16 v[216:219],v[100:103],a[104:107],v[216:219]
buffer_load_dwordx4 v25,s[48:51],s83 offen offset:1024 lds
v_mfma_f32_16x16x32_bf16 v[216:219],v[96:99],a[96:99],v[216:219]
ds_read_b64_tr_b16 v[96:97],v1 offset:64
ds_read_b64_tr_b16 v[98:99],v1 offset:5184
v_mfma_f32_16x16x32_bf16 v[224:227],v[112:115],a[68:71],v[224:227]
ds_read_b64_tr_b16 v[100:101],v1 offset:10304
v_mfma_f32_16x16x32_bf16 v[224:227],v[116:119],a[76:79],v[224:227]
ds_read_b64_tr_b16 v[102:103],v1 offset:15424
v_mfma_f32_16x16x32_bf16 v[224:227],v[120:123],a[84:87],v[224:227]
ds_read_b64_tr_b16 v[106:107],v1 offset:25664
v_mfma_f32_16x16x32_bf16 v[224:227],v[124:127],a[92:95],v[224:227]
buffer_load_dwordx4 a[152:155],v6,s[28:31],s88 offen offset:2048 sc1
v_mfma_f32_16x16x32_bf16 v[232:235],v[124:127],a[124:127],v[232:235]
v_mfma_f32_16x16x32_bf16 v[232:235],v[120:123],a[116:119],v[232:235]
v_mfma_f32_16x16x32_bf16 v[232:235],v[116:119],a[108:111],v[232:235]
v_mfma_f32_16x16x32_bf16 v[232:235],v[112:115],a[100:103],v[232:235]
v_mfma_f32_16x16x32_bf16 v[240:243],v[112:115],a[64:67],v[240:243]
v_mfma_f32_16x16x32_bf16 v[240:243],v[116:119],a[72:75],v[240:243]
buffer_load_dwordx4 v26,s[48:51],s83 offen offset:2048 lds
v_mfma_f32_16x16x32_bf16 v[240:243],v[120:123],a[80:83],v[240:243]
v_mfma_f32_16x16x32_bf16 v[240:243],v[124:127],a[88:91],v[240:243]
v_mfma_f32_16x16x32_bf16 v[248:251],v[124:127],a[120:123],v[248:251]
ds_read_b64_tr_b16 v[124:125],v2 offset:30816
ds_read_b64_tr_b16 v[126:127],v2 offset:35936
v_mfma_f32_16x16x32_bf16 v[248:251],v[120:123],a[112:115],v[248:251]
ds_read_b64_tr_b16 v[120:121],v2 offset:20576
v_mfma_f32_16x16x32_bf16 v[248:251],v[116:119],a[104:107],v[248:251]
buffer_load_dwordx4 a[168:171],v6,s[28:31],s89 offen offset:2048 sc1
v_mfma_f32_16x16x32_bf16 v[248:251],v[112:115],a[96:99],v[248:251]
s_waitcnt vmcnt(22)
ds_read_b64_tr_b16 v[112:113],v2 offset:96
ds_read_b64_tr_b16 v[114:115],v2 offset:5216
v_mfma_f32_16x16x32_bf16 a[192:195],v[64:67],v[32:35],a[192:195]
ds_read_b64_tr_b16 v[116:117],v2 offset:10336
v_mfma_f32_16x16x32_bf16 a[192:195],v[68:71],v[36:39],a[192:195]
ds_read_b64_tr_b16 v[118:119],v2 offset:15456
v_mfma_f32_16x16x32_bf16 a[192:195],v[72:75],v[40:43],a[192:195]
s_waitcnt vmcnt(17)
ds_read_b64_tr_b16 v[122:123],v2 offset:25696
v_mfma_f32_16x16x32_bf16 a[192:195],v[76:79],v[44:47],a[192:195]
s_waitcnt vmcnt(14)
v_mfma_f32_16x16x32_bf16 a[196:199],v[76:79],v[180:183],a[196:199]
buffer_load_dwordx4 v27,s[48:51],s83 offen offset:3072 lds
v_mfma_f32_16x16x32_bf16 a[196:199],v[72:75],v[164:167],a[196:199]
s_add_u32 m0,s86,45056
v_mfma_f32_16x16x32_bf16 a[196:199],v[68:71],v[148:151],a[196:199]
v_mfma_f32_16x16x32_bf16 a[196:199],v[64:67],v[132:135],a[196:199]
v_mfma_f32_16x16x32_bf16 a[200:203],v[64:67],v[196:199],a[200:203]
v_mfma_f32_16x16x32_bf16 a[200:203],v[68:71],v[212:215],a[200:203]
buffer_load_dwordx4 a[184:187],v6,s[28:31],s90 offen offset:2048 sc1
v_mfma_f32_16x16x32_bf16 a[200:203],v[72:75],v[228:231],a[200:203]
s_waitcnt vmcnt(14)
v_mfma_f32_16x16x32_bf16 a[200:203],v[76:79],v[244:247],a[200:203]
s_waitcnt vmcnt(13)
v_mfma_f32_16x16x32_bf16 a[204:207],v[76:79],v[188:191],a[204:207]
ds_read_b64_tr_b16 v[76:77],v1 offset:30848
ds_read_b64_tr_b16 v[78:79],v1 offset:35968
v_mfma_f32_16x16x32_bf16 a[204:207],v[72:75],v[172:175],a[204:207]
ds_read_b64_tr_b16 v[72:73],v1 offset:20608
v_mfma_f32_16x16x32_bf16 a[204:207],v[68:71],v[156:159],a[204:207]
s_waitcnt lgkmcnt(3)
ds_read_b64_tr_b16 v[68:69],v1 offset:10368
v_mfma_f32_16x16x32_bf16 a[204:207],v[64:67],v[140:143],a[204:207]
buffer_load_dwordx4 v7,s[48:51],s83 offen lds
v_mfma_f32_16x16x32_bf16 a[208:211],v[80:83],v[32:35],a[208:211]
s_add_u32 m0,s86,46080
ds_read_b64_tr_b16 v[64:65],v1 offset:128
ds_read_b64_tr_b16 v[66:67],v1 offset:5248
v_mfma_f32_16x16x32_bf16 a[208:211],v[84:87],v[36:39],a[208:211]
s_mul_i32 s83,s63,20480
ds_read_b64_tr_b16 v[70:71],v1 offset:15488
v_mfma_f32_16x16x32_bf16 a[208:211],v[88:91],v[40:43],a[208:211]
ds_read_b64_tr_b16 v[74:75],v1 offset:25728
v_mfma_f32_16x16x32_bf16 a[208:211],v[92:95],v[44:47],a[208:211]
v_mfma_f32_16x16x32_bf16 a[212:215],v[92:95],v[180:183],a[212:215]
buffer_load_dwordx4 a[188:191],v28,s[28:31],s90 offen offset:3072 sc1
v_mfma_f32_16x16x32_bf16 a[212:215],v[88:91],v[164:167],a[212:215]
v_mfma_f32_16x16x32_bf16 a[212:215],v[84:87],v[148:151],a[212:215]
v_mfma_f32_16x16x32_bf16 a[212:215],v[80:83],v[132:135],a[212:215]
v_mfma_f32_16x16x32_bf16 a[216:219],v[80:83],v[196:199],a[216:219]
v_mfma_f32_16x16x32_bf16 a[216:219],v[84:87],v[212:215],a[216:219]
v_mfma_f32_16x16x32_bf16 a[216:219],v[88:91],v[228:231],a[216:219]
buffer_load_dwordx4 v24,s[48:51],s84 offen lds
v_mfma_f32_16x16x32_bf16 a[216:219],v[92:95],v[244:247],a[216:219]
v_mfma_f32_16x16x32_bf16 a[220:223],v[92:95],v[188:191],a[220:223]
ds_read_b64_tr_b16 v[92:93],v2 offset:30880
ds_read_b64_tr_b16 v[94:95],v2 offset:36000
v_mfma_f32_16x16x32_bf16 a[220:223],v[88:91],v[172:175],a[220:223]
ds_read_b64_tr_b16 v[88:89],v2 offset:20640
v_mfma_f32_16x16x32_bf16 a[220:223],v[84:87],v[156:159],a[220:223]
ds_read_b64_tr_b16 v[84:85],v2 offset:10400
v_mfma_f32_16x16x32_bf16 a[220:223],v[80:83],v[140:143],a[220:223]
ds_read_b64_tr_b16 v[80:81],v2 offset:160
v_mfma_f32_16x16x32_bf16 a[224:227],v[96:99],v[32:35],a[224:227]
buffer_load_dwordx4 v25,s[48:51],s84 offen offset:1024 lds
v_mfma_f32_16x16x32_bf16 a[224:227],v[100:103],v[36:39],a[224:227]
ds_read_b64_tr_b16 v[82:83],v2 offset:5280
ds_read_b64_tr_b16 v[86:87],v2 offset:15520
v_mfma_f32_16x16x32_bf16 a[224:227],v[104:107],v[40:43],a[224:227]
ds_read_b64_tr_b16 v[90:91],v2 offset:25760
v_mfma_f32_16x16x32_bf16 a[224:227],v[108:111],v[44:47],a[224:227]
v_mfma_f32_16x16x32_bf16 a[228:231],v[108:111],v[180:183],a[228:231]
v_mfma_f32_16x16x32_bf16 a[228:231],v[104:107],v[164:167],a[228:231]
buffer_load_dwordx4 a[172:175],v28,s[28:31],s89 offen offset:3072 sc1
v_mfma_f32_16x16x32_bf16 a[228:231],v[100:103],v[148:151],a[228:231]
v_mfma_f32_16x16x32_bf16 a[228:231],v[96:99],v[132:135],a[228:231]
v_mfma_f32_16x16x32_bf16 a[232:235],v[96:99],v[196:199],a[232:235]
v_mfma_f32_16x16x32_bf16 a[232:235],v[100:103],v[212:215],a[232:235]
v_mfma_f32_16x16x32_bf16 a[232:235],v[104:107],v[228:231],a[232:235]
v_mfma_f32_16x16x32_bf16 a[232:235],v[108:111],v[244:247],a[232:235]
buffer_load_dwordx4 v26,s[48:51],s84 offen offset:2048 lds
v_mfma_f32_16x16x32_bf16 a[236:239],v[108:111],v[188:191],a[236:239]
ds_read_b64_tr_b16 v[108:109],v1 offset:30912
ds_read_b64_tr_b16 v[110:111],v1 offset:36032
v_mfma_f32_16x16x32_bf16 a[236:239],v[104:107],v[172:175],a[236:239]
ds_read_b64_tr_b16 v[104:105],v1 offset:20672
v_mfma_f32_16x16x32_bf16 a[236:239],v[100:103],v[156:159],a[236:239]
ds_read_b64_tr_b16 v[100:101],v1 offset:10432
v_mfma_f32_16x16x32_bf16 a[236:239],v[96:99],v[140:143],a[236:239]
ds_read_b64_tr_b16 v[96:97],v1 offset:192
v_mfma_f32_16x16x32_bf16 a[240:243],v[112:115],v[32:35],a[240:243]
ds_read_b64_tr_b16 v[98:99],v1 offset:5312
ds_read_b64_tr_b16 v[102:103],v1 offset:15552
v_mfma_f32_16x16x32_bf16 a[240:243],v[116:119],v[36:39],a[240:243]
buffer_load_dwordx4 v27,s[48:51],s84 offen offset:3072 lds
v_mfma_f32_16x16x32_bf16 a[240:243],v[120:123],v[40:43],a[240:243]
s_add_u32 m0,s86,50176
ds_read_b64_tr_b16 v[106:107],v1 offset:25792
v_add_u32 v1,0xffff6000,v1
v_mfma_f32_16x16x32_bf16 a[240:243],v[124:127],v[44:47],a[240:243]
v_mfma_f32_16x16x32_bf16 a[244:247],v[124:127],v[180:183],a[244:247]
v_mfma_f32_16x16x32_bf16 a[244:247],v[120:123],v[164:167],a[244:247]
)A"
R"A(v_mfma_f32_16x16x32_bf16 a[244:247],v[116:119],v[148:151],a[244:247]
buffer_load_dwordx4 a[140:143],v28,s[28:31],s87 offen offset:3072 sc1
v_mfma_f32_16x16x32_bf16 a[244:247],v[112:115],v[132:135],a[244:247]
s_add_u32 s87,s64,2
s_and_b32 s87,s87,3
s_lshl_b32 s87,s87,1
s_or_b32 s87,s87,1
s_lshl_b32 s87,s87,16
s_add_u32 s89,s87,8192
s_add_u32 s90,s87,12288
v_mfma_f32_16x16x32_bf16 a[248:251],v[112:115],v[196:199],a[248:251]
v_mfma_f32_16x16x32_bf16 a[248:251],v[116:119],v[212:215],a[248:251]
v_mfma_f32_16x16x32_bf16 a[248:251],v[120:123],v[228:231],a[248:251]
v_mfma_f32_16x16x32_bf16 a[248:251],v[124:127],v[244:247],a[248:251]
buffer_load_dwordx4 a[68:71],v6,s[28:31],s87 offen sc1
v_mfma_f32_16x16x32_bf16 a[252:255],v[124:127],v[188:191],a[252:255]
ds_read_b64_tr_b16 v[124:125],v2 offset:30944
ds_read_b64_tr_b16 v[126:127],v2 offset:36064
v_mfma_f32_16x16x32_bf16 a[252:255],v[120:123],v[172:175],a[252:255]
ds_read_b64_tr_b16 v[120:121],v2 offset:20704
v_mfma_f32_16x16x32_bf16 a[252:255],v[116:119],v[156:159],a[252:255]
ds_read_b64_tr_b16 v[116:117],v2 offset:10464
v_mfma_f32_16x16x32_bf16 a[252:255],v[112:115],v[140:143],a[252:255]
s_waitcnt lgkmcnt(4)
ds_read_b64_tr_b16 v[112:113],v2 offset:224
v_mfma_f32_16x16x32_bf16 a[0:3],v[64:67],v[32:35],a[0:3]
buffer_load_dwordx4 a[156:159],v28,s[28:31],s88 offen offset:3072 sc1
v_mfma_f32_16x16x32_bf16 a[0:3],v[68:71],v[36:39],a[0:3]
s_add_u32 s88,s87,4096
ds_read_b64_tr_b16 v[114:115],v2 offset:5344
ds_read_b64_tr_b16 v[118:119],v2 offset:15584
v_mfma_f32_16x16x32_bf16 a[0:3],v[72:75],v[40:43],a[0:3]
ds_read_b64_tr_b16 v[122:123],v2 offset:25824
v_add_u32 v2,0xffff6000,v2
v_mfma_f32_16x16x32_bf16 a[0:3],v[76:79],v[44:47],a[0:3]
v_mfma_f32_16x16x32_bf16 a[4:7],v[76:79],v[180:183],a[4:7]
v_mfma_f32_16x16x32_bf16 a[4:7],v[72:75],v[164:167],a[4:7]
v_mfma_f32_16x16x32_bf16 a[4:7],v[68:71],v[148:151],a[4:7]
buffer_load_dwordx4 v7,s[48:51],s84 offen lds
v_mfma_f32_16x16x32_bf16 a[4:7],v[64:67],v[132:135],a[4:7]
v_mfma_f32_16x16x32_bf16 a[8:11],v[64:67],v[196:199],a[8:11]
v_mfma_f32_16x16x32_bf16 a[8:11],v[68:71],v[212:215],a[8:11]
v_mfma_f32_16x16x32_bf16 a[8:11],v[72:75],v[228:231],a[8:11]
v_mfma_f32_16x16x32_bf16 a[8:11],v[76:79],v[244:247],a[8:11]
buffer_load_dwordx4 a[76:79],v6,s[28:31],s88 offen sc1
v_mfma_f32_16x16x32_bf16 a[12:15],v[76:79],v[188:191],a[12:15]
ds_read_b64_tr_b16 v[76:77],v1 offset:30720
ds_read_b64_tr_b16 v[78:79],v1 offset:35840
v_mfma_f32_16x16x32_bf16 a[12:15],v[72:75],v[172:175],a[12:15]
ds_read_b64_tr_b16 v[72:73],v1 offset:20480
v_mfma_f32_16x16x32_bf16 a[12:15],v[68:71],v[156:159],a[12:15]
ds_read_b64_tr_b16 v[68:69],v1 offset:10240
v_mfma_f32_16x16x32_bf16 a[12:15],v[64:67],v[140:143],a[12:15]
ds_read_b64_tr_b16 v[64:65],v1
v_mfma_f32_16x16x32_bf16 a[16:19],v[80:83],v[32:35],a[16:19]
buffer_load_dwordx4 a[84:87],v6,s[28:31],s89 offen sc1
v_mfma_f32_16x16x32_bf16 a[16:19],v[84:87],v[36:39],a[16:19]
ds_read_b64_tr_b16 v[66:67],v1 offset:5120
ds_read_b64_tr_b16 v[70:71],v1 offset:15360
v_mfma_f32_16x16x32_bf16 a[16:19],v[88:91],v[40:43],a[16:19]
ds_read_b64_tr_b16 v[74:75],v1 offset:25600
s_nop 0
L_pd22_%=:
s_cbranch_execz L_trtg2S_%=
v_add_u32 v1,0xffff6000,v1
v_mfma_f32_16x16x32_bf16 a[16:19],v[92:95],v[44:47],a[16:19]
v_mfma_f32_16x16x32_bf16 a[20:23],v[92:95],v[180:183],a[20:23]
v_mfma_f32_16x16x32_bf16 a[20:23],v[88:91],v[164:167],a[20:23]
buffer_load_dwordx4 a[92:95],v6,s[28:31],s90 offen sc1
v_mfma_f32_16x16x32_bf16 a[20:23],v[84:87],v[148:151],a[20:23]
v_mfma_f32_16x16x32_bf16 a[20:23],v[80:83],v[132:135],a[20:23]
v_mfma_f32_16x16x32_bf16 a[24:27],v[80:83],v[196:199],a[24:27]
v_mfma_f32_16x16x32_bf16 a[24:27],v[84:87],v[212:215],a[24:27]
v_mfma_f32_16x16x32_bf16 a[24:27],v[88:91],v[228:231],a[24:27]
buffer_load_dwordx4 a[124:127],v6,s[28:31],s90 offen offset:1024 sc1
v_mfma_f32_16x16x32_bf16 a[24:27],v[92:95],v[244:247],a[24:27]
v_mfma_f32_16x16x32_bf16 a[28:31],v[92:95],v[188:191],a[28:31]
ds_read_b64_tr_b16 v[92:93],v2 offset:30752
ds_read_b64_tr_b16 v[94:95],v2 offset:35872
v_mfma_f32_16x16x32_bf16 a[28:31],v[88:91],v[172:175],a[28:31]
ds_read_b64_tr_b16 v[88:89],v2 offset:20512
v_mfma_f32_16x16x32_bf16 a[28:31],v[84:87],v[156:159],a[28:31]
ds_read_b64_tr_b16 v[84:85],v2 offset:10272
v_mfma_f32_16x16x32_bf16 a[28:31],v[80:83],v[140:143],a[28:31]
buffer_load_dwordx4 a[116:119],v6,s[28:31],s89 offen offset:1024 sc1
v_mfma_f32_16x16x32_bf16 v[128:131],v[96:99],v[32:35],v[128:131]
ds_read_b64_tr_b16 v[80:81],v2 offset:32
ds_read_b64_tr_b16 v[82:83],v2 offset:5152
v_mfma_f32_16x16x32_bf16 v[128:131],v[100:103],v[36:39],v[128:131]
ds_read_b64_tr_b16 v[86:87],v2 offset:15392
v_mfma_f32_16x16x32_bf16 v[128:131],v[104:107],v[40:43],v[128:131]
ds_read_b64_tr_b16 v[90:91],v2 offset:25632
v_add_u32 v2,0xffff6000,v2
v_mfma_f32_16x16x32_bf16 v[128:131],v[108:111],v[44:47],v[128:131]
v_mfma_f32_16x16x32_bf16 v[136:139],v[108:111],v[180:183],v[136:139]
buffer_load_dwordx4 a[108:111],v6,s[28:31],s88 offen offset:1024 sc1
v_mfma_f32_16x16x32_bf16 v[136:139],v[104:107],v[164:167],v[136:139]
v_mfma_f32_16x16x32_bf16 v[136:139],v[100:103],v[148:151],v[136:139]
v_mfma_f32_16x16x32_bf16 v[136:139],v[96:99],v[132:135],v[136:139]
v_mfma_f32_16x16x32_bf16 v[144:147],v[96:99],v[196:199],v[144:147]
v_mfma_f32_16x16x32_bf16 v[144:147],v[100:103],v[212:215],v[144:147]
buffer_load_dwordx4 a[100:103],v6,s[28:31],s87 offen offset:1024 sc1
v_mfma_f32_16x16x32_bf16 v[144:147],v[104:107],v[228:231],v[144:147]
v_mfma_f32_16x16x32_bf16 v[144:147],v[108:111],v[244:247],v[144:147]
v_mfma_f32_16x16x32_bf16 v[152:155],v[108:111],v[188:191],v[152:155]
v_mfma_f32_16x16x32_bf16 v[152:155],v[104:107],v[172:175],v[152:155]
v_mfma_f32_16x16x32_bf16 v[152:155],v[100:103],v[156:159],v[152:155]
buffer_load_dwordx4 a[64:67],v6,s[28:31],s87 offen offset:2048 sc1
v_mfma_f32_16x16x32_bf16 v[152:155],v[96:99],v[140:143],v[152:155]
s_waitcnt vmcnt(8)
s_barrier
s_waitcnt lgkmcnt(0)
v_mfma_f32_16x16x32_bf16 v[160:163],v[112:115],v[32:35],v[160:163]
ds_read_b64_tr_b16 v[96:97],v1
ds_read_b64_tr_b16 v[98:99],v1 offset:5120
v_mfma_f32_16x16x32_bf16 v[160:163],v[116:119],v[36:39],v[160:163]
ds_read_b64_tr_b16 v[100:101],v1 offset:10240
v_mfma_f32_16x16x32_bf16 v[160:163],v[120:123],v[40:43],v[160:163]
buffer_load_dwordx4 a[72:75],v6,s[28:31],s88 offen offset:2048 sc1
v_mfma_f32_16x16x32_bf16 v[160:163],v[124:127],v[44:47],v[160:163]
ds_read_b64_tr_b16 v[102:103],v1 offset:15360
ds_read_b64_tr_b16 v[104:105],v1 offset:20480
v_mfma_f32_16x16x32_bf16 v[168:171],v[124:127],v[180:183],v[168:171]
ds_read_b64_tr_b16 v[106:107],v1 offset:25600
v_mfma_f32_16x16x32_bf16 v[168:171],v[120:123],v[164:167],v[168:171]
ds_read_b64_tr_b16 v[108:109],v1 offset:30720
v_mfma_f32_16x16x32_bf16 v[168:171],v[116:119],v[148:151],v[168:171]
ds_read_b64_tr_b16 v[110:111],v1 offset:35840
v_add_u32 v1,0xa000,v1
v_mfma_f32_16x16x32_bf16 v[168:171],v[112:115],v[132:135],v[168:171]
buffer_load_dwordx4 a[80:83],v6,s[28:31],s89 offen offset:2048 sc1
v_mfma_f32_16x16x32_bf16 v[176:179],v[112:115],v[196:199],v[176:179]
v_mfma_f32_16x16x32_bf16 v[176:179],v[116:119],v[212:215],v[176:179]
v_mfma_f32_16x16x32_bf16 v[176:179],v[120:123],v[228:231],v[176:179]
v_mfma_f32_16x16x32_bf16 v[176:179],v[124:127],v[244:247],v[176:179]
v_mfma_f32_16x16x32_bf16 v[184:187],v[124:127],v[188:191],v[184:187]
buffer_load_dwordx4 a[88:91],v6,s[28:31],s90 offen offset:2048 sc1
v_mfma_f32_16x16x32_bf16 v[184:187],v[120:123],v[172:175],v[184:187]
ds_read_b64_tr_b16 v[120:121],v2 offset:20512
ds_read_b64_tr_b16 v[122:123],v2 offset:25632
v_mfma_f32_16x16x32_bf16 v[184:187],v[116:119],v[156:159],v[184:187]
ds_read_b64_tr_b16 v[116:117],v2 offset:10272
v_mfma_f32_16x16x32_bf16 v[184:187],v[112:115],v[140:143],v[184:187]
ds_read_b64_tr_b16 v[112:113],v2 offset:32
v_mfma_f32_16x16x32_bf16 a[192:195],v[64:67],a[128:131],a[192:195]
ds_read_b64_tr_b16 v[114:115],v2 offset:5152
v_mfma_f32_16x16x32_bf16 a[192:195],v[68:71],a[144:147],a[192:195]
buffer_load_dwordx4 a[120:123],v28,s[28:31],s90 offen offset:3072 sc1
v_mfma_f32_16x16x32_bf16 a[192:195],v[72:75],a[160:163],a[192:195]
ds_read_b64_tr_b16 v[118:119],v2 offset:15392
ds_read_b64_tr_b16 v[124:125],v2 offset:30752
v_mfma_f32_16x16x32_bf16 a[192:195],v[76:79],a[176:179],a[192:195]
ds_read_b64_tr_b16 v[126:127],v2 offset:35872
v_add_u32 v2,0xa000,v2
v_mfma_f32_16x16x32_bf16 a[196:199],v[76:79],a[180:183],a[196:199]
v_mfma_f32_16x16x32_bf16 a[196:199],v[72:75],a[164:167],a[196:199]
v_mfma_f32_16x16x32_bf16 a[196:199],v[68:71],a[148:151],a[196:199]
buffer_load_dwordx4 a[112:115],v28,s[28:31],s89 offen offset:3072 sc1
v_mfma_f32_16x16x32_bf16 a[196:199],v[64:67],a[132:135],a[196:199]
v_mfma_f32_16x16x32_bf16 a[200:203],v[64:67],a[136:139],a[200:203]
v_mfma_f32_16x16x32_bf16 a[200:203],v[68:71],a[152:155],a[200:203]
v_mfma_f32_16x16x32_bf16 a[200:203],v[72:75],a[168:171],a[200:203]
v_mfma_f32_16x16x32_bf16 a[200:203],v[76:79],a[184:187],a[200:203]
buffer_load_dwordx4 a[104:107],v28,s[28:31],s88 offen offset:3072 sc1
v_mfma_f32_16x16x32_bf16 a[204:207],v[76:79],a[188:191],a[204:207]
ds_read_b64_tr_b16 v[76:77],v1 offset:30784
ds_read_b64_tr_b16 v[78:79],v1 offset:35904
v_mfma_f32_16x16x32_bf16 a[204:207],v[72:75],a[172:175],a[204:207]
ds_read_b64_tr_b16 v[72:73],v1 offset:20544
v_mfma_f32_16x16x32_bf16 a[204:207],v[68:71],a[156:159],a[204:207]
ds_read_b64_tr_b16 v[68:69],v1 offset:10304
v_mfma_f32_16x16x32_bf16 a[204:207],v[64:67],a[140:143],a[204:207]
ds_read_b64_tr_b16 v[64:65],v1 offset:64
v_mfma_f32_16x16x32_bf16 a[208:211],v[80:83],a[128:131],a[208:211]
buffer_load_dwordx4 a[96:99],v28,s[28:31],s87 offen offset:3072 sc1
v_mfma_f32_16x16x32_bf16 a[208:211],v[84:87],a[144:147],a[208:211]
s_mul_i32 s87,s60,81920
ds_read_b64_tr_b16 v[66:67],v1 offset:5184
ds_read_b64_tr_b16 v[70:71],v1 offset:15424
v_mfma_f32_16x16x32_bf16 a[208:211],v[88:91],a[160:163],a[208:211]
s_add_u32 s87,s87,s66
ds_read_b64_tr_b16 v[74:75],v1 offset:25664
v_add_u32 v1,0xffff6000,v1
v_mfma_f32_16x16x32_bf16 a[208:211],v[92:95],a[176:179],a[208:211]
s_add_u32 s83,s83,s87
s_lshl_b32 s66,s63,6
s_add_u32 s87,s87,s66
v_mfma_f32_16x16x32_bf16 a[212:215],v[92:95],a[180:183],a[212:215]
s_add_u32 s88,s87,20480
s_add_u32 s89,s87,40960
s_add_u32 s90,s87,61440
v_mfma_f32_16x16x32_bf16 a[212:215],v[88:91],a[164:167],a[212:215]
s_add_u32 s66,s77,4096
v_mfma_f32_16x16x32_bf16 a[212:215],v[84:87],a[148:151],a[212:215]
v_mfma_f32_16x16x32_bf16 a[212:215],v[80:83],a[132:135],a[212:215]
v_mfma_f32_16x16x32_bf16 a[216:219],v[80:83],a[136:139],a[216:219]
v_mfma_f32_16x16x32_bf16 a[216:219],v[84:87],a[152:155],a[216:219]
v_mfma_f32_16x16x32_bf16 a[216:219],v[88:91],a[168:171],a[216:219]
v_mfma_f32_16x16x32_bf16 a[216:219],v[92:95],a[184:187],a[216:219]
v_mfma_f32_16x16x32_bf16 a[220:223],v[92:95],a[188:191],a[220:223]
ds_read_b64_tr_b16 v[92:93],v2 offset:30816
ds_read_b64_tr_b16 v[94:95],v2 offset:35936
v_mfma_f32_16x16x32_bf16 a[220:223],v[88:91],a[172:175],a[220:223]
ds_read_b64_tr_b16 v[88:89],v2 offset:20576
v_mfma_f32_16x16x32_bf16 a[220:223],v[84:87],a[156:159],a[220:223]
ds_read_b64_tr_b16 v[84:85],v2 offset:10336
v_mfma_f32_16x16x32_bf16 a[220:223],v[80:83],a[140:143],a[220:223]
s_waitcnt lgkmcnt(4)
ds_read_b64_tr_b16 v[80:81],v2 offset:96
v_mfma_f32_16x16x32_bf16 a[192:195],v[96:99],a[68:71],a[192:195]
s_waitcnt vmcnt(14)
ds_read_b64_tr_b16 v[82:83],v2 offset:5216
ds_read_b64_tr_b16 v[86:87],v2 offset:15456
v_mfma_f32_16x16x32_bf16 a[192:195],v[100:103],a[76:79],a[192:195]
s_waitcnt vmcnt(13)
ds_read_b64_tr_b16 v[90:91],v2 offset:25696
v_add_u32 v2,0xffff6000,v2
v_mfma_f32_16x16x32_bf16 a[192:195],v[104:107],a[84:87],a[192:195]
s_waitcnt vmcnt(12)
v_mfma_f32_16x16x32_bf16 a[192:195],v[108:111],a[92:95],a[192:195]
s_waitcnt vmcnt(11)
v_mfma_f32_16x16x32_bf16 a[196:199],v[108:111],a[124:127],a[196:199]
s_waitcnt vmcnt(10)
v_mfma_f32_16x16x32_bf16 a[196:199],v[104:107],a[116:119],a[196:199]
s_waitcnt vmcnt(9)
v_mfma_f32_16x16x32_bf16 a[196:199],v[100:103],a[108:111],a[196:199]
s_waitcnt vmcnt(8)
v_mfma_f32_16x16x32_bf16 a[196:199],v[96:99],a[100:103],a[196:199]
)A"
R"A(s_waitcnt vmcnt(7)
v_mfma_f32_16x16x32_bf16 a[200:203],v[96:99],a[64:67],a[200:203]
s_waitcnt vmcnt(6)
v_mfma_f32_16x16x32_bf16 a[200:203],v[100:103],a[72:75],a[200:203]
s_waitcnt vmcnt(5)
v_mfma_f32_16x16x32_bf16 a[200:203],v[104:107],a[80:83],a[200:203]
s_waitcnt vmcnt(4)
v_mfma_f32_16x16x32_bf16 a[200:203],v[108:111],a[88:91],a[200:203]
s_waitcnt vmcnt(3)
v_accvgpr_read_b32 v8,a192
v_accvgpr_read_b32 v9,a193
v_mfma_f32_16x16x32_bf16 a[204:207],v[108:111],a[120:123],a[204:207]
s_waitcnt vmcnt(2)
v_accvgpr_read_b32 v10,a194
v_accvgpr_read_b32 v11,a195
v_mfma_f32_16x16x32_bf16 a[204:207],v[104:107],a[112:115],a[204:207]
s_waitcnt vmcnt(1)
v_cvt_pk_bf16_f32 v8,v8,v9
v_cvt_pk_bf16_f32 v9,v10,v11
v_mfma_f32_16x16x32_bf16 a[204:207],v[100:103],a[104:107],a[204:207]
s_waitcnt vmcnt(0)
v_accvgpr_read_b32 v16,a196
v_accvgpr_read_b32 v17,a197
v_mfma_f32_16x16x32_bf16 a[204:207],v[96:99],a[96:99],a[204:207]
v_accvgpr_read_b32 v18,a198
v_accvgpr_read_b32 v19,a199
v_mfma_f32_16x16x32_bf16 a[208:211],v[112:115],a[68:71],a[208:211]
v_cvt_pk_bf16_f32 v16,v16,v17
v_cvt_pk_bf16_f32 v17,v18,v19
v_mfma_f32_16x16x32_bf16 a[208:211],v[116:119],a[76:79],a[208:211]
ds_read_b64_tr_b16 v[96:97],v1 offset:64
ds_read_b64_tr_b16 v[98:99],v1 offset:5184
v_mfma_f32_16x16x32_bf16 a[208:211],v[120:123],a[84:87],a[208:211]
ds_read_b64_tr_b16 v[100:101],v1 offset:10304
v_mfma_f32_16x16x32_bf16 a[208:211],v[124:127],a[92:95],a[208:211]
ds_read_b64_tr_b16 v[102:103],v1 offset:15424
v_mfma_f32_16x16x32_bf16 a[212:215],v[124:127],a[124:127],a[212:215]
ds_read_b64_tr_b16 v[104:105],v1 offset:20544
v_mfma_f32_16x16x32_bf16 a[212:215],v[120:123],a[116:119],a[212:215]
ds_read_b64_tr_b16 v[106:107],v1 offset:25664
ds_read_b64_tr_b16 v[108:109],v1 offset:30784
v_mfma_f32_16x16x32_bf16 a[212:215],v[116:119],a[108:111],a[212:215]
ds_read_b64_tr_b16 v[110:111],v1 offset:35904
v_add_u32 v1,0xa000,v1
v_mfma_f32_16x16x32_bf16 a[212:215],v[112:115],a[100:103],a[212:215]
v_accvgpr_read_b32 v12,a208
v_accvgpr_read_b32 v13,a209
v_mfma_f32_16x16x32_bf16 a[216:219],v[112:115],a[64:67],a[216:219]
v_accvgpr_read_b32 v14,a210
v_accvgpr_read_b32 v15,a211
v_mfma_f32_16x16x32_bf16 a[216:219],v[116:119],a[72:75],a[216:219]
v_cvt_pk_bf16_f32 v10,v12,v13
v_cvt_pk_bf16_f32 v11,v14,v15
v_mfma_f32_16x16x32_bf16 a[216:219],v[120:123],a[80:83],a[216:219]
buffer_store_dwordx4 v[8:11],v6,s[36:39],s74 offen sc1
v_mfma_f32_16x16x32_bf16 a[216:219],v[124:127],a[88:91],a[216:219]
v_accvgpr_read_b32 v20,a212
v_accvgpr_read_b32 v21,a213
v_mfma_f32_16x16x32_bf16 a[220:223],v[124:127],a[120:123],a[220:223]
v_accvgpr_read_b32 v22,a214
v_accvgpr_read_b32 v23,a215
v_mfma_f32_16x16x32_bf16 a[220:223],v[120:123],a[112:115],a[220:223]
v_cvt_pk_bf16_f32 v18,v20,v21
v_cvt_pk_bf16_f32 v19,v22,v23
v_mfma_f32_16x16x32_bf16 a[220:223],v[116:119],a[104:107],a[220:223]
v_accvgpr_read_b32 v8,a200
v_accvgpr_read_b32 v9,a201
v_mfma_f32_16x16x32_bf16 a[220:223],v[112:115],a[96:99],a[220:223]
v_accvgpr_read_b32 v10,a202
v_accvgpr_read_b32 v11,a203
v_mfma_f32_16x16x32_bf16 a[224:227],v[64:67],a[128:131],a[224:227]
v_accvgpr_read_b32 v12,a216
v_accvgpr_read_b32 v13,a217
v_mfma_f32_16x16x32_bf16 a[224:227],v[68:71],a[144:147],a[224:227]
v_accvgpr_read_b32 v14,a218
v_accvgpr_read_b32 v15,a219
v_mfma_f32_16x16x32_bf16 a[224:227],v[72:75],a[160:163],a[224:227]
v_cvt_pk_bf16_f32 v8,v8,v9
v_cvt_pk_bf16_f32 v9,v10,v11
v_mfma_f32_16x16x32_bf16 a[224:227],v[76:79],a[176:179],a[224:227]
v_cvt_pk_bf16_f32 v10,v12,v13
v_cvt_pk_bf16_f32 v11,v14,v15
v_mfma_f32_16x16x32_bf16 a[228:231],v[76:79],a[180:183],a[228:231]
ds_read_b64_tr_b16 v[112:113],v2 offset:96
ds_read_b64_tr_b16 v[114:115],v2 offset:5216
v_mfma_f32_16x16x32_bf16 a[228:231],v[72:75],a[164:167],a[228:231]
buffer_store_dwordx4 v[16:19],v6,s[36:39],s74 offen offset:1024 sc1
v_mfma_f32_16x16x32_bf16 a[228:231],v[68:71],a[148:151],a[228:231]
ds_read_b64_tr_b16 v[116:117],v2 offset:10336
ds_read_b64_tr_b16 v[118:119],v2 offset:15456
v_mfma_f32_16x16x32_bf16 a[228:231],v[64:67],a[132:135],a[228:231]
ds_read_b64_tr_b16 v[120:121],v2 offset:20576
v_accvgpr_read_b32 v16,a204
v_mfma_f32_16x16x32_bf16 a[232:235],v[64:67],a[136:139],a[232:235]
ds_read_b64_tr_b16 v[122:123],v2 offset:25696
v_accvgpr_read_b32 v17,a205
v_mfma_f32_16x16x32_bf16 a[232:235],v[68:71],a[152:155],a[232:235]
ds_read_b64_tr_b16 v[124:125],v2 offset:30816
v_accvgpr_read_b32 v18,a206
v_mfma_f32_16x16x32_bf16 a[232:235],v[72:75],a[168:171],a[232:235]
ds_read_b64_tr_b16 v[126:127],v2 offset:35936
v_accvgpr_read_b32 v19,a207
v_mfma_f32_16x16x32_bf16 a[232:235],v[76:79],a[184:187],a[232:235]
v_accvgpr_read_b32 v20,a220
v_accvgpr_read_b32 v21,a221
v_mfma_f32_16x16x32_bf16 a[236:239],v[76:79],a[188:191],a[236:239]
v_accvgpr_read_b32 v22,a222
v_accvgpr_read_b32 v23,a223
v_mfma_f32_16x16x32_bf16 a[236:239],v[72:75],a[172:175],a[236:239]
v_cvt_pk_bf16_f32 v16,v16,v17
v_cvt_pk_bf16_f32 v17,v18,v19
v_mfma_f32_16x16x32_bf16 a[236:239],v[68:71],a[156:159],a[236:239]
v_cvt_pk_bf16_f32 v18,v20,v21
v_cvt_pk_bf16_f32 v19,v22,v23
v_mfma_f32_16x16x32_bf16 a[236:239],v[64:67],a[140:143],a[236:239]
s_waitcnt lgkmcnt(3)
ds_read_b64_tr_b16 v[64:65],v1 offset:128
ds_read_b64_tr_b16 v[66:67],v1 offset:5248
v_mfma_f32_16x16x32_bf16 a[240:243],v[80:83],a[128:131],a[240:243]
buffer_store_dwordx4 v[8:11],v6,s[36:39],s74 offen offset:2048 sc1
v_mfma_f32_16x16x32_bf16 a[240:243],v[84:87],a[144:147],a[240:243]
ds_read_b64_tr_b16 v[68:69],v1 offset:10368
ds_read_b64_tr_b16 v[70:71],v1 offset:15488
v_mfma_f32_16x16x32_bf16 a[240:243],v[88:91],a[160:163],a[240:243]
ds_read_b64_tr_b16 v[72:73],v1 offset:20608
v_add_u32 v2,0xa000,v2
v_mfma_f32_16x16x32_bf16 a[240:243],v[92:95],a[176:179],a[240:243]
ds_read_b64_tr_b16 v[74:75],v1 offset:25728
v_mfma_f32_16x16x32_bf16 a[244:247],v[92:95],a[180:183],a[244:247]
ds_read_b64_tr_b16 v[76:77],v1 offset:30848
v_mfma_f32_16x16x32_bf16 a[244:247],v[88:91],a[164:167],a[244:247]
ds_read_b64_tr_b16 v[78:79],v1 offset:35968
v_add_u32 v1,0xffff6000,v1
v_mfma_f32_16x16x32_bf16 a[244:247],v[84:87],a[148:151],a[244:247]
v_mfma_f32_16x16x32_bf16 a[244:247],v[80:83],a[132:135],a[244:247]
v_mfma_f32_16x16x32_bf16 a[248:251],v[80:83],a[136:139],a[248:251]
v_mfma_f32_16x16x32_bf16 a[248:251],v[84:87],a[152:155],a[248:251]
v_mfma_f32_16x16x32_bf16 a[248:251],v[88:91],a[168:171],a[248:251]
v_mfma_f32_16x16x32_bf16 a[248:251],v[92:95],a[184:187],a[248:251]
buffer_store_dwordx4 v[16:19],v28,s[36:39],s74 offen offset:3072 sc1
v_mfma_f32_16x16x32_bf16 a[252:255],v[92:95],a[188:191],a[252:255]
ds_read_b64_tr_b16 v[92:93],v2 offset:30880
ds_read_b64_tr_b16 v[94:95],v2 offset:36000
v_mfma_f32_16x16x32_bf16 a[252:255],v[88:91],a[172:175],a[252:255]
ds_read_b64_tr_b16 v[88:89],v2 offset:20640
v_mfma_f32_16x16x32_bf16 a[252:255],v[84:87],a[156:159],a[252:255]
ds_read_b64_tr_b16 v[84:85],v2 offset:10400
v_mfma_f32_16x16x32_bf16 a[252:255],v[80:83],a[140:143],a[252:255]
ds_read_b64_tr_b16 v[80:81],v2 offset:160
v_mfma_f32_16x16x32_bf16 a[224:227],v[96:99],a[68:71],a[224:227]
ds_read_b64_tr_b16 v[82:83],v2 offset:5280
ds_read_b64_tr_b16 v[86:87],v2 offset:15520
v_mfma_f32_16x16x32_bf16 a[224:227],v[100:103],a[76:79],a[224:227]
ds_read_b64_tr_b16 v[90:91],v2 offset:25760
v_add_u32 v2,0xffff6000,v2
v_mfma_f32_16x16x32_bf16 a[224:227],v[104:107],a[84:87],a[224:227]
v_mfma_f32_16x16x32_bf16 a[224:227],v[108:111],a[92:95],a[224:227]
v_mfma_f32_16x16x32_bf16 a[228:231],v[108:111],a[124:127],a[228:231]
v_mfma_f32_16x16x32_bf16 a[228:231],v[104:107],a[116:119],a[228:231]
v_mfma_f32_16x16x32_bf16 a[228:231],v[100:103],a[108:111],a[228:231]
v_mfma_f32_16x16x32_bf16 a[228:231],v[96:99],a[100:103],a[228:231]
v_mfma_f32_16x16x32_bf16 a[232:235],v[96:99],a[64:67],a[232:235]
v_mfma_f32_16x16x32_bf16 a[232:235],v[100:103],a[72:75],a[232:235]
v_mfma_f32_16x16x32_bf16 a[232:235],v[104:107],a[80:83],a[232:235]
v_mfma_f32_16x16x32_bf16 a[232:235],v[108:111],a[88:91],a[232:235]
v_accvgpr_read_b32 v8,a224
v_accvgpr_read_b32 v9,a225
v_mfma_f32_16x16x32_bf16 a[236:239],v[108:111],a[120:123],a[236:239]
v_accvgpr_read_b32 v10,a226
v_accvgpr_read_b32 v11,a227
v_mfma_f32_16x16x32_bf16 a[236:239],v[104:107],a[112:115],a[236:239]
v_cvt_pk_bf16_f32 v8,v8,v9
v_cvt_pk_bf16_f32 v9,v10,v11
v_mfma_f32_16x16x32_bf16 a[236:239],v[100:103],a[104:107],a[236:239]
v_accvgpr_read_b32 v16,a228
v_accvgpr_read_b32 v17,a229
v_mfma_f32_16x16x32_bf16 a[236:239],v[96:99],a[96:99],a[236:239]
v_accvgpr_read_b32 v18,a230
v_accvgpr_read_b32 v19,a231
v_mfma_f32_16x16x32_bf16 a[240:243],v[112:115],a[68:71],a[240:243]
v_cvt_pk_bf16_f32 v16,v16,v17
v_cvt_pk_bf16_f32 v17,v18,v19
v_mfma_f32_16x16x32_bf16 a[240:243],v[116:119],a[76:79],a[240:243]
s_waitcnt lgkmcnt(0)
ds_read_b64_tr_b16 v[96:97],v1 offset:128
ds_read_b64_tr_b16 v[98:99],v1 offset:5248
v_mfma_f32_16x16x32_bf16 a[240:243],v[120:123],a[84:87],a[240:243]
ds_read_b64_tr_b16 v[100:101],v1 offset:10368
v_mfma_f32_16x16x32_bf16 a[240:243],v[124:127],a[92:95],a[240:243]
ds_read_b64_tr_b16 v[102:103],v1 offset:15488
v_mfma_f32_16x16x32_bf16 a[244:247],v[124:127],a[124:127],a[244:247]
ds_read_b64_tr_b16 v[104:105],v1 offset:20608
v_mfma_f32_16x16x32_bf16 a[244:247],v[120:123],a[116:119],a[244:247]
ds_read_b64_tr_b16 v[106:107],v1 offset:25728
ds_read_b64_tr_b16 v[108:109],v1 offset:30848
v_mfma_f32_16x16x32_bf16 a[244:247],v[116:119],a[108:111],a[244:247]
ds_read_b64_tr_b16 v[110:111],v1 offset:35968
v_add_u32 v1,0xa000,v1
v_mfma_f32_16x16x32_bf16 a[244:247],v[112:115],a[100:103],a[244:247]
v_accvgpr_read_b32 v12,a240
v_accvgpr_read_b32 v13,a241
v_mfma_f32_16x16x32_bf16 a[248:251],v[112:115],a[64:67],a[248:251]
v_accvgpr_read_b32 v14,a242
v_accvgpr_read_b32 v15,a243
v_mfma_f32_16x16x32_bf16 a[248:251],v[116:119],a[72:75],a[248:251]
v_cvt_pk_bf16_f32 v10,v12,v13
v_cvt_pk_bf16_f32 v11,v14,v15
v_mfma_f32_16x16x32_bf16 a[248:251],v[120:123],a[80:83],a[248:251]
buffer_store_dwordx4 v[8:11],v6,s[36:39],s75 offen sc1
v_mfma_f32_16x16x32_bf16 a[248:251],v[124:127],a[88:91],a[248:251]
v_accvgpr_read_b32 v20,a244
v_accvgpr_read_b32 v21,a245
v_mfma_f32_16x16x32_bf16 a[252:255],v[124:127],a[120:123],a[252:255]
v_accvgpr_read_b32 v22,a246
v_accvgpr_read_b32 v23,a247
v_mfma_f32_16x16x32_bf16 a[252:255],v[120:123],a[112:115],a[252:255]
v_cvt_pk_bf16_f32 v18,v20,v21
v_cvt_pk_bf16_f32 v19,v22,v23
v_mfma_f32_16x16x32_bf16 a[252:255],v[116:119],a[104:107],a[252:255]
v_accvgpr_read_b32 v8,a232
v_accvgpr_read_b32 v9,a233
v_mfma_f32_16x16x32_bf16 a[252:255],v[112:115],a[96:99],a[252:255]
v_accvgpr_read_b32 v10,a234
v_accvgpr_read_b32 v11,a235
v_mfma_f32_16x16x32_bf16 a[0:3],v[64:67],a[128:131],a[0:3]
v_accvgpr_read_b32 v12,a248
v_accvgpr_read_b32 v13,a249
v_mfma_f32_16x16x32_bf16 a[0:3],v[68:71],a[144:147],a[0:3]
v_accvgpr_read_b32 v14,a250
v_accvgpr_read_b32 v15,a251
v_mfma_f32_16x16x32_bf16 a[0:3],v[72:75],a[160:163],a[0:3]
v_cvt_pk_bf16_f32 v8,v8,v9
v_cvt_pk_bf16_f32 v9,v10,v11
v_mfma_f32_16x16x32_bf16 a[0:3],v[76:79],a[176:179],a[0:3]
v_cvt_pk_bf16_f32 v10,v12,v13
v_cvt_pk_bf16_f32 v11,v14,v15
v_mfma_f32_16x16x32_bf16 a[4:7],v[76:79],a[180:183],a[4:7]
ds_read_b64_tr_b16 v[112:113],v2 offset:160
ds_read_b64_tr_b16 v[114:115],v2 offset:5280
v_mfma_f32_16x16x32_bf16 a[4:7],v[72:75],a[164:167],a[4:7]
buffer_store_dwordx4 v[16:19],v6,s[36:39],s75 offen offset:1024 sc1
v_mfma_f32_16x16x32_bf16 a[4:7],v[68:71],a[148:151],a[4:7]
ds_read_b64_tr_b16 v[116:117],v2 offset:10400
ds_read_b64_tr_b16 v[118:119],v2 offset:15520
v_mfma_f32_16x16x32_bf16 a[4:7],v[64:67],a[132:135],a[4:7]
ds_read_b64_tr_b16 v[120:121],v2 offset:20640
v_accvgpr_read_b32 v16,a236
v_mfma_f32_16x16x32_bf16 a[8:11],v[64:67],a[136:139],a[8:11]
ds_read_b64_tr_b16 v[122:123],v2 offset:25760
v_accvgpr_read_b32 v17,a237
v_mfma_f32_16x16x32_bf16 a[8:11],v[68:71],a[152:155],a[8:11]
ds_read_b64_tr_b16 v[124:125],v2 offset:30880
v_accvgpr_read_b32 v18,a238
v_mfma_f32_16x16x32_bf16 a[8:11],v[72:75],a[168:171],a[8:11]
ds_read_b64_tr_b16 v[126:127],v2 offset:36000
v_accvgpr_read_b32 v19,a239
v_mfma_f32_16x16x32_bf16 a[8:11],v[76:79],a[184:187],a[8:11]
v_accvgpr_read_b32 v20,a252
)A"
R"A(v_accvgpr_read_b32 v21,a253
v_mfma_f32_16x16x32_bf16 a[12:15],v[76:79],a[188:191],a[12:15]
v_accvgpr_read_b32 v22,a254
v_accvgpr_read_b32 v23,a255
v_mfma_f32_16x16x32_bf16 a[12:15],v[72:75],a[172:175],a[12:15]
v_cvt_pk_bf16_f32 v16,v16,v17
v_cvt_pk_bf16_f32 v17,v18,v19
v_mfma_f32_16x16x32_bf16 a[12:15],v[68:71],a[156:159],a[12:15]
v_cvt_pk_bf16_f32 v18,v20,v21
v_cvt_pk_bf16_f32 v19,v22,v23
v_mfma_f32_16x16x32_bf16 a[12:15],v[64:67],a[140:143],a[12:15]
ds_read_b64_tr_b16 v[64:65],v1 offset:192
ds_read_b64_tr_b16 v[66:67],v1 offset:5312
v_mfma_f32_16x16x32_bf16 a[16:19],v[80:83],a[128:131],a[16:19]
buffer_store_dwordx4 v[8:11],v6,s[36:39],s75 offen offset:2048 sc1
v_mfma_f32_16x16x32_bf16 a[16:19],v[84:87],a[144:147],a[16:19]
ds_read_b64_tr_b16 v[68:69],v1 offset:10432
ds_read_b64_tr_b16 v[70:71],v1 offset:15552
v_mfma_f32_16x16x32_bf16 a[16:19],v[88:91],a[160:163],a[16:19]
ds_read_b64_tr_b16 v[72:73],v1 offset:20672
v_add_u32 v2,0xa000,v2
v_mfma_f32_16x16x32_bf16 a[16:19],v[92:95],a[176:179],a[16:19]
ds_read_b64_tr_b16 v[74:75],v1 offset:25792
v_mfma_f32_16x16x32_bf16 a[20:23],v[92:95],a[180:183],a[20:23]
ds_read_b64_tr_b16 v[76:77],v1 offset:30912
v_mfma_f32_16x16x32_bf16 a[20:23],v[88:91],a[164:167],a[20:23]
ds_read_b64_tr_b16 v[78:79],v1 offset:36032
v_add_u32 v1,0xffff6000,v1
v_mfma_f32_16x16x32_bf16 a[20:23],v[84:87],a[148:151],a[20:23]
v_mfma_f32_16x16x32_bf16 a[20:23],v[80:83],a[132:135],a[20:23]
v_mfma_f32_16x16x32_bf16 a[24:27],v[80:83],a[136:139],a[24:27]
v_mfma_f32_16x16x32_bf16 a[24:27],v[84:87],a[152:155],a[24:27]
v_mfma_f32_16x16x32_bf16 a[24:27],v[88:91],a[168:171],a[24:27]
v_mfma_f32_16x16x32_bf16 a[24:27],v[92:95],a[184:187],a[24:27]
buffer_store_dwordx4 v[16:19],v28,s[36:39],s75 offen offset:3072 sc1
v_mfma_f32_16x16x32_bf16 a[28:31],v[92:95],a[188:191],a[28:31]
ds_read_b64_tr_b16 v[92:93],v2 offset:30944
ds_read_b64_tr_b16 v[94:95],v2 offset:36064
v_mfma_f32_16x16x32_bf16 a[28:31],v[88:91],a[172:175],a[28:31]
ds_read_b64_tr_b16 v[88:89],v2 offset:20704
v_mfma_f32_16x16x32_bf16 a[28:31],v[84:87],a[156:159],a[28:31]
ds_read_b64_tr_b16 v[84:85],v2 offset:10464
v_mfma_f32_16x16x32_bf16 a[28:31],v[80:83],a[140:143],a[28:31]
s_waitcnt lgkmcnt(4)
ds_read_b64_tr_b16 v[80:81],v2 offset:224
v_mfma_f32_16x16x32_bf16 a[0:3],v[96:99],a[68:71],a[0:3]
ds_read_b64_tr_b16 v[82:83],v2 offset:5344
ds_read_b64_tr_b16 v[86:87],v2 offset:15584
v_mfma_f32_16x16x32_bf16 a[0:3],v[100:103],a[76:79],a[0:3]
ds_read_b64_tr_b16 v[90:91],v2 offset:25824
v_add_u32 v2,0xffff6000,v2
v_mfma_f32_16x16x32_bf16 a[0:3],v[104:107],a[84:87],a[0:3]
v_mfma_f32_16x16x32_bf16 a[0:3],v[108:111],a[92:95],a[0:3]
v_mfma_f32_16x16x32_bf16 a[4:7],v[108:111],a[124:127],a[4:7]
v_mfma_f32_16x16x32_bf16 a[4:7],v[104:107],a[116:119],a[4:7]
v_mfma_f32_16x16x32_bf16 a[4:7],v[100:103],a[108:111],a[4:7]
v_mfma_f32_16x16x32_bf16 a[4:7],v[96:99],a[100:103],a[4:7]
v_mfma_f32_16x16x32_bf16 a[8:11],v[96:99],a[64:67],a[8:11]
v_mfma_f32_16x16x32_bf16 a[8:11],v[100:103],a[72:75],a[8:11]
v_mfma_f32_16x16x32_bf16 a[8:11],v[104:107],a[80:83],a[8:11]
v_mfma_f32_16x16x32_bf16 a[8:11],v[108:111],a[88:91],a[8:11]
v_accvgpr_read_b32 v8,a0
v_accvgpr_read_b32 v9,a1
v_mfma_f32_16x16x32_bf16 a[12:15],v[108:111],a[120:123],a[12:15]
v_accvgpr_read_b32 v10,a2
v_accvgpr_read_b32 v11,a3
v_mfma_f32_16x16x32_bf16 a[12:15],v[104:107],a[112:115],a[12:15]
v_cvt_pk_bf16_f32 v8,v8,v9
v_cvt_pk_bf16_f32 v9,v10,v11
v_mfma_f32_16x16x32_bf16 a[12:15],v[100:103],a[104:107],a[12:15]
v_accvgpr_read_b32 v16,a4
v_accvgpr_read_b32 v17,a5
v_mfma_f32_16x16x32_bf16 a[12:15],v[96:99],a[96:99],a[12:15]
v_accvgpr_read_b32 v18,a6
v_accvgpr_read_b32 v19,a7
v_mfma_f32_16x16x32_bf16 a[16:19],v[112:115],a[68:71],a[16:19]
v_cvt_pk_bf16_f32 v16,v16,v17
v_cvt_pk_bf16_f32 v17,v18,v19
v_mfma_f32_16x16x32_bf16 a[16:19],v[116:119],a[76:79],a[16:19]
ds_read_b64_tr_b16 v[96:97],v1 offset:192
ds_read_b64_tr_b16 v[98:99],v1 offset:5312
v_mfma_f32_16x16x32_bf16 a[16:19],v[120:123],a[84:87],a[16:19]
ds_read_b64_tr_b16 v[100:101],v1 offset:10432
v_mfma_f32_16x16x32_bf16 a[16:19],v[124:127],a[92:95],a[16:19]
ds_read_b64_tr_b16 v[102:103],v1 offset:15552
v_mfma_f32_16x16x32_bf16 a[20:23],v[124:127],a[124:127],a[20:23]
ds_read_b64_tr_b16 v[104:105],v1 offset:20672
v_mfma_f32_16x16x32_bf16 a[20:23],v[120:123],a[116:119],a[20:23]
ds_read_b64_tr_b16 v[106:107],v1 offset:25792
ds_read_b64_tr_b16 v[108:109],v1 offset:30912
v_mfma_f32_16x16x32_bf16 a[20:23],v[116:119],a[108:111],a[20:23]
ds_read_b64_tr_b16 v[110:111],v1 offset:36032
v_add_u32 v1,0x14000,v1
v_mfma_f32_16x16x32_bf16 a[20:23],v[112:115],a[100:103],a[20:23]
v_accvgpr_read_b32 v12,a16
v_accvgpr_read_b32 v13,a17
v_mfma_f32_16x16x32_bf16 a[24:27],v[112:115],a[64:67],a[24:27]
v_accvgpr_read_b32 v14,a18
v_accvgpr_read_b32 v15,a19
v_mfma_f32_16x16x32_bf16 a[24:27],v[116:119],a[72:75],a[24:27]
v_cvt_pk_bf16_f32 v10,v12,v13
v_cvt_pk_bf16_f32 v11,v14,v15
s_nop 0
L_pd23_%=:
s_cbranch_execz L_trtg2S_%=
v_mfma_f32_16x16x32_bf16 a[24:27],v[120:123],a[80:83],a[24:27]
buffer_store_dwordx4 v[8:11],v6,s[36:39],s76 offen sc1
v_mfma_f32_16x16x32_bf16 a[24:27],v[124:127],a[88:91],a[24:27]
v_accvgpr_read_b32 v20,a20
v_accvgpr_read_b32 v21,a21
v_mfma_f32_16x16x32_bf16 a[28:31],v[124:127],a[120:123],a[28:31]
v_accvgpr_read_b32 v22,a22
v_accvgpr_read_b32 v23,a23
v_mfma_f32_16x16x32_bf16 a[28:31],v[120:123],a[112:115],a[28:31]
v_cvt_pk_bf16_f32 v18,v20,v21
v_cvt_pk_bf16_f32 v19,v22,v23
v_mfma_f32_16x16x32_bf16 a[28:31],v[116:119],a[104:107],a[28:31]
v_accvgpr_read_b32 v8,a8
v_accvgpr_read_b32 v9,a9
v_mfma_f32_16x16x32_bf16 a[28:31],v[112:115],a[96:99],a[28:31]
v_accvgpr_read_b32 v10,a10
v_accvgpr_read_b32 v11,a11
v_mfma_f32_16x16x32_bf16 v[128:131],v[64:67],a[128:131],v[128:131]
v_accvgpr_read_b32 v12,a24
v_accvgpr_read_b32 v13,a25
v_mfma_f32_16x16x32_bf16 v[128:131],v[68:71],a[144:147],v[128:131]
v_accvgpr_read_b32 v14,a26
v_accvgpr_read_b32 v15,a27
v_mfma_f32_16x16x32_bf16 v[128:131],v[72:75],a[160:163],v[128:131]
v_cvt_pk_bf16_f32 v8,v8,v9
v_cvt_pk_bf16_f32 v9,v10,v11
v_mfma_f32_16x16x32_bf16 v[128:131],v[76:79],a[176:179],v[128:131]
v_cvt_pk_bf16_f32 v10,v12,v13
v_cvt_pk_bf16_f32 v11,v14,v15
v_mfma_f32_16x16x32_bf16 v[136:139],v[76:79],a[180:183],v[136:139]
ds_read_b64_tr_b16 v[112:113],v2 offset:224
ds_read_b64_tr_b16 v[114:115],v2 offset:5344
v_mfma_f32_16x16x32_bf16 v[136:139],v[72:75],a[164:167],v[136:139]
buffer_store_dwordx4 v[16:19],v6,s[36:39],s76 offen offset:1024 sc1
v_mfma_f32_16x16x32_bf16 v[136:139],v[68:71],a[148:151],v[136:139]
ds_read_b64_tr_b16 v[116:117],v2 offset:10464
ds_read_b64_tr_b16 v[118:119],v2 offset:15584
v_mfma_f32_16x16x32_bf16 v[136:139],v[64:67],a[132:135],v[136:139]
ds_read_b64_tr_b16 v[120:121],v2 offset:20704
v_accvgpr_read_b32 v16,a12
v_mfma_f32_16x16x32_bf16 v[144:147],v[64:67],a[136:139],v[144:147]
ds_read_b64_tr_b16 v[122:123],v2 offset:25824
v_accvgpr_read_b32 v17,a13
v_mfma_f32_16x16x32_bf16 v[144:147],v[68:71],a[152:155],v[144:147]
ds_read_b64_tr_b16 v[124:125],v2 offset:30944
v_accvgpr_read_b32 v18,a14
v_mfma_f32_16x16x32_bf16 v[144:147],v[72:75],a[168:171],v[144:147]
ds_read_b64_tr_b16 v[126:127],v2 offset:36064
v_accvgpr_read_b32 v19,a15
v_mfma_f32_16x16x32_bf16 v[144:147],v[76:79],a[184:187],v[144:147]
v_accvgpr_read_b32 v20,a28
v_accvgpr_read_b32 v21,a29
v_mfma_f32_16x16x32_bf16 v[152:155],v[76:79],a[188:191],v[152:155]
v_accvgpr_read_b32 v22,a30
v_accvgpr_read_b32 v23,a31
v_mfma_f32_16x16x32_bf16 v[152:155],v[72:75],a[172:175],v[152:155]
v_cvt_pk_bf16_f32 v16,v16,v17
v_cvt_pk_bf16_f32 v17,v18,v19
v_mfma_f32_16x16x32_bf16 v[152:155],v[68:71],a[156:159],v[152:155]
v_cvt_pk_bf16_f32 v18,v20,v21
v_cvt_pk_bf16_f32 v19,v22,v23
v_mfma_f32_16x16x32_bf16 v[152:155],v[64:67],a[140:143],v[152:155]
s_waitcnt lgkmcnt(3)
ds_read_b64_tr_b16 v[64:65],v1 offset:256
ds_read_b64_tr_b16 v[66:67],v1 offset:5376
v_mfma_f32_16x16x32_bf16 v[160:163],v[80:83],a[128:131],v[160:163]
buffer_store_dwordx4 v[8:11],v6,s[36:39],s76 offen offset:2048 sc1
v_mfma_f32_16x16x32_bf16 v[160:163],v[84:87],a[144:147],v[160:163]
ds_read_b64_tr_b16 v[68:69],v1 offset:10496
ds_read_b64_tr_b16 v[70:71],v1 offset:15616
v_mfma_f32_16x16x32_bf16 v[160:163],v[88:91],a[160:163],v[160:163]
ds_read_b64_tr_b16 v[72:73],v1 offset:20736
v_add_u32 v2,0x14000,v2
v_mfma_f32_16x16x32_bf16 v[160:163],v[92:95],a[176:179],v[160:163]
ds_read_b64_tr_b16 v[74:75],v1 offset:25856
v_mov_b32 v20,s52
v_mfma_f32_16x16x32_bf16 v[168:171],v[92:95],a[180:183],v[168:171]
ds_read_b64_tr_b16 v[76:77],v1 offset:30976
v_mov_b32 v21,s53
v_mfma_f32_16x16x32_bf16 v[168:171],v[88:91],a[164:167],v[168:171]
ds_read_b64_tr_b16 v[78:79],v1 offset:36096
v_add_u32 v1,0xffff6000,v1
v_mfma_f32_16x16x32_bf16 v[168:171],v[84:87],a[148:151],v[168:171]
v_lshlrev_b32 v22,20,v6
v_mfma_f32_16x16x32_bf16 v[168:171],v[80:83],a[132:135],v[168:171]
v_mfma_f32_16x16x32_bf16 v[176:179],v[80:83],a[136:139],v[176:179]
v_mfma_f32_16x16x32_bf16 v[176:179],v[84:87],a[152:155],v[176:179]
v_mfma_f32_16x16x32_bf16 v[176:179],v[88:91],a[168:171],v[176:179]
v_mfma_f32_16x16x32_bf16 v[176:179],v[92:95],a[184:187],v[176:179]
buffer_store_dwordx4 v[16:19],v28,s[36:39],s76 offen offset:3072 sc1
v_mfma_f32_16x16x32_bf16 v[184:187],v[92:95],a[188:191],v[184:187]
ds_read_b64_tr_b16 v[92:93],v2 offset:31008
ds_read_b64_tr_b16 v[94:95],v2 offset:36128
v_mfma_f32_16x16x32_bf16 v[184:187],v[88:91],a[172:175],v[184:187]
ds_read_b64_tr_b16 v[88:89],v2 offset:20768
v_mfma_f32_16x16x32_bf16 v[184:187],v[84:87],a[156:159],v[184:187]
ds_read_b64_tr_b16 v[84:85],v2 offset:10528
v_mfma_f32_16x16x32_bf16 v[184:187],v[80:83],a[140:143],v[184:187]
ds_read_b64_tr_b16 v[80:81],v2 offset:288
v_mfma_f32_16x16x32_bf16 v[128:131],v[96:99],a[68:71],v[128:131]
ds_read_b64_tr_b16 v[82:83],v2 offset:5408
ds_read_b64_tr_b16 v[86:87],v2 offset:15648
v_mfma_f32_16x16x32_bf16 v[128:131],v[100:103],a[76:79],v[128:131]
ds_read_b64_tr_b16 v[90:91],v2 offset:25888
v_add_u32 v2,0xffff6000,v2
v_mfma_f32_16x16x32_bf16 v[128:131],v[104:107],a[84:87],v[128:131]
v_mfma_f32_16x16x32_bf16 v[128:131],v[108:111],a[92:95],v[128:131]
v_mfma_f32_16x16x32_bf16 v[136:139],v[108:111],a[124:127],v[136:139]
v_mfma_f32_16x16x32_bf16 v[136:139],v[104:107],a[116:119],v[136:139]
v_mfma_f32_16x16x32_bf16 v[136:139],v[100:103],a[108:111],v[136:139]
v_mfma_f32_16x16x32_bf16 v[136:139],v[96:99],a[100:103],v[136:139]
v_mfma_f32_16x16x32_bf16 v[144:147],v[96:99],a[64:67],v[144:147]
v_mfma_f32_16x16x32_bf16 v[144:147],v[100:103],a[72:75],v[144:147]
v_mfma_f32_16x16x32_bf16 v[144:147],v[104:107],a[80:83],v[144:147]
v_mfma_f32_16x16x32_bf16 v[144:147],v[108:111],a[88:91],v[144:147]
v_cvt_pk_bf16_f32 v128,v128,v129
v_cvt_pk_bf16_f32 v129,v130,v131
v_mfma_f32_16x16x32_bf16 v[152:155],v[108:111],a[120:123],v[152:155]
ds_read_b64_tr_b16 v[108:109],v1 offset:30976
ds_read_b64_tr_b16 v[110:111],v1 offset:36096
v_mfma_f32_16x16x32_bf16 v[152:155],v[104:107],a[112:115],v[152:155]
v_cvt_pk_bf16_f32 v136,v136,v137
v_cvt_pk_bf16_f32 v137,v138,v139
v_mfma_f32_16x16x32_bf16 v[152:155],v[100:103],a[104:107],v[152:155]
v_cvt_pk_bf16_f32 v144,v144,v145
v_cvt_pk_bf16_f32 v145,v146,v147
v_mfma_f32_16x16x32_bf16 v[152:155],v[96:99],a[96:99],v[152:155]
ds_read_b64_tr_b16 v[96:97],v1 offset:256
ds_read_b64_tr_b16 v[98:99],v1 offset:5376
v_mfma_f32_16x16x32_bf16 v[160:163],v[112:115],a[68:71],v[160:163]
ds_read_b64_tr_b16 v[100:101],v1 offset:10496
v_mfma_f32_16x16x32_bf16 v[160:163],v[116:119],a[76:79],v[160:163]
s_waitcnt lgkmcnt(5)
ds_read_b64_tr_b16 v[102:103],v1 offset:15616
v_mfma_f32_16x16x32_bf16 v[160:163],v[120:123],a[84:87],v[160:163]
ds_read_b64_tr_b16 v[104:105],v1 offset:20736
v_mfma_f32_16x16x32_bf16 v[160:163],v[124:127],a[92:95],v[160:163]
v_cvt_pk_bf16_f32 v152,v152,v153
v_cvt_pk_bf16_f32 v153,v154,v155
v_mfma_f32_16x16x32_bf16 v[168:171],v[124:127],a[124:127],v[168:171]
ds_read_b64_tr_b16 v[106:107],v1 offset:25856
v_add_u32 v1,0xffff6000,v1
v_mfma_f32_16x16x32_bf16 v[168:171],v[120:123],a[116:119],v[168:171]
v_mfma_f32_16x16x32_bf16 v[168:171],v[116:119],a[108:111],v[168:171]
v_mfma_f32_16x16x32_bf16 v[168:171],v[112:115],a[100:103],v[168:171]
)A"
R"A(v_cvt_pk_bf16_f32 v130,v160,v161
v_cvt_pk_bf16_f32 v131,v162,v163
v_mfma_f32_16x16x32_bf16 v[176:179],v[112:115],a[64:67],v[176:179]
buffer_store_dwordx4 v[128:131],v6,s[36:39],s79 offen sc1
v_mfma_f32_16x16x32_bf16 v[176:179],v[116:119],a[72:75],v[176:179]
v_mfma_f32_16x16x32_bf16 v[176:179],v[120:123],a[80:83],v[176:179]
v_mfma_f32_16x16x32_bf16 v[176:179],v[124:127],a[88:91],v[176:179]
v_mfma_f32_16x16x32_bf16 v[184:187],v[124:127],a[120:123],v[184:187]
v_cvt_pk_bf16_f32 v138,v168,v169
v_cvt_pk_bf16_f32 v139,v170,v171
v_mfma_f32_16x16x32_bf16 v[184:187],v[120:123],a[112:115],v[184:187]
buffer_store_dwordx4 v[136:139],v6,s[36:39],s80 offen sc1
v_mfma_f32_16x16x32_bf16 v[184:187],v[116:119],a[104:107],v[184:187]
ds_read_b64_tr_b16 v[116:117],v2 offset:10528
ds_read_b64_tr_b16 v[118:119],v2 offset:15648
v_mfma_f32_16x16x32_bf16 v[184:187],v[112:115],a[96:99],v[184:187]
v_cvt_pk_bf16_f32 v146,v176,v177
v_cvt_pk_bf16_f32 v147,v178,v179
s_waitcnt vmcnt(6)
s_barrier
v_mfma_f32_16x16x32_bf16 v[192:195],v[64:67],v[32:35],v[192:195]
buffer_store_dwordx4 v[144:147],v6,s[36:39],s81 offen sc1
v_mfma_f32_16x16x32_bf16 v[192:195],v[68:71],v[36:39],v[192:195]
ds_read_b64_tr_b16 v[112:113],v2 offset:288
ds_read_b64_tr_b16 v[114:115],v2 offset:5408
v_mfma_f32_16x16x32_bf16 v[192:195],v[72:75],v[40:43],v[192:195]
buffer_store_dwordx2 v[58:59],v60,s[44:47],s18 offen offset:2312 sc1
v_mfma_f32_16x16x32_bf16 v[192:195],v[76:79],v[44:47],v[192:195]
v_cvt_pk_bf16_f32 v154,v184,v185
v_cvt_pk_bf16_f32 v155,v186,v187
v_mfma_f32_16x16x32_bf16 v[200:203],v[76:79],v[180:183],v[200:203]
buffer_store_dwordx4 v[152:155],v28,s[36:39],s82 offen sc1
v_mfma_f32_16x16x32_bf16 v[200:203],v[72:75],v[164:167],v[200:203]
ds_read_b64_tr_b16 v[120:121],v2 offset:20768
ds_read_b64_tr_b16 v[122:123],v2 offset:25888
v_mfma_f32_16x16x32_bf16 v[200:203],v[68:71],v[148:151],v[200:203]
ds_read_b64_tr_b16 v[124:125],v2 offset:31008
v_mfma_f32_16x16x32_bf16 v[200:203],v[64:67],v[132:135],v[200:203]
ds_read_b64_tr_b16 v[126:127],v2 offset:36128
v_add_u32 v2,0xffff6000,v2
v_mfma_f32_16x16x32_bf16 v[208:211],v[64:67],v[196:199],v[208:211]
v_mfma_f32_16x16x32_bf16 v[208:211],v[68:71],v[212:215],v[208:211]
v_mfma_f32_16x16x32_bf16 v[208:211],v[72:75],v[228:231],v[208:211]
v_mfma_f32_16x16x32_bf16 v[208:211],v[76:79],v[244:247],v[208:211]
v_mfma_f32_16x16x32_bf16 v[216:219],v[76:79],v[188:191],v[216:219]
ds_read_b64_tr_b16 v[76:77],v1 offset:30976
ds_read_b64_tr_b16 v[78:79],v1 offset:36096
v_mfma_f32_16x16x32_bf16 v[216:219],v[72:75],v[172:175],v[216:219]
ds_read_b64_tr_b16 v[72:73],v1 offset:20736
v_mfma_f32_16x16x32_bf16 v[216:219],v[68:71],v[156:159],v[216:219]
ds_read_b64_tr_b16 v[68:69],v1 offset:10496
v_mfma_f32_16x16x32_bf16 v[216:219],v[64:67],v[140:143],v[216:219]
ds_read_b64_tr_b16 v[64:65],v1 offset:256
v_mfma_f32_16x16x32_bf16 v[224:227],v[80:83],v[32:35],v[224:227]
ds_read_b64_tr_b16 v[66:67],v1 offset:5376
ds_read_b64_tr_b16 v[70:71],v1 offset:15616
v_mfma_f32_16x16x32_bf16 v[224:227],v[84:87],v[36:39],v[224:227]
ds_read_b64_tr_b16 v[74:75],v1 offset:25856
v_mfma_f32_16x16x32_bf16 v[224:227],v[88:91],v[40:43],v[224:227]
v_mfma_f32_16x16x32_bf16 v[224:227],v[92:95],v[44:47],v[224:227]
v_mfma_f32_16x16x32_bf16 v[232:235],v[92:95],v[180:183],v[232:235]
v_mfma_f32_16x16x32_bf16 v[232:235],v[88:91],v[164:167],v[232:235]
v_mfma_f32_16x16x32_bf16 v[232:235],v[84:87],v[148:151],v[232:235]
v_mfma_f32_16x16x32_bf16 v[232:235],v[80:83],v[132:135],v[232:235]
v_mfma_f32_16x16x32_bf16 v[240:243],v[80:83],v[196:199],v[240:243]
v_mfma_f32_16x16x32_bf16 v[240:243],v[84:87],v[212:215],v[240:243]
v_mfma_f32_16x16x32_bf16 v[240:243],v[88:91],v[228:231],v[240:243]
v_mfma_f32_16x16x32_bf16 v[240:243],v[92:95],v[244:247],v[240:243]
v_mfma_f32_16x16x32_bf16 v[248:251],v[92:95],v[188:191],v[248:251]
ds_read_b64_tr_b16 v[92:93],v2 offset:31008
ds_read_b64_tr_b16 v[94:95],v2 offset:36128
v_mfma_f32_16x16x32_bf16 v[248:251],v[88:91],v[172:175],v[248:251]
ds_read_b64_tr_b16 v[88:89],v2 offset:20768
v_mfma_f32_16x16x32_bf16 v[248:251],v[84:87],v[156:159],v[248:251]
ds_read_b64_tr_b16 v[84:85],v2 offset:10528
v_mfma_f32_16x16x32_bf16 v[248:251],v[80:83],v[140:143],v[248:251]
s_waitcnt lgkmcnt(4)
ds_read_b64_tr_b16 v[80:81],v2 offset:288
v_mfma_f32_16x16x32_bf16 v[192:195],v[96:99],a[128:131],v[192:195]
ds_read_b64_tr_b16 v[82:83],v2 offset:5408
ds_read_b64_tr_b16 v[86:87],v2 offset:15648
v_mfma_f32_16x16x32_bf16 v[192:195],v[100:103],a[144:147],v[192:195]
ds_read_b64_tr_b16 v[90:91],v2 offset:25888
v_mfma_f32_16x16x32_bf16 v[192:195],v[104:107],a[160:163],v[192:195]
v_mfma_f32_16x16x32_bf16 v[192:195],v[108:111],a[176:179],v[192:195]
v_mfma_f32_16x16x32_bf16 v[200:203],v[108:111],a[180:183],v[200:203]
v_mfma_f32_16x16x32_bf16 v[200:203],v[104:107],a[164:167],v[200:203]
v_mfma_f32_16x16x32_bf16 v[200:203],v[100:103],a[148:151],v[200:203]
v_mfma_f32_16x16x32_bf16 v[200:203],v[96:99],a[132:135],v[200:203]
v_mfma_f32_16x16x32_bf16 v[208:211],v[96:99],a[136:139],v[208:211]
v_mfma_f32_16x16x32_bf16 v[208:211],v[100:103],a[152:155],v[208:211]
v_mfma_f32_16x16x32_bf16 v[208:211],v[104:107],a[168:171],v[208:211]
v_mfma_f32_16x16x32_bf16 v[208:211],v[108:111],a[184:187],v[208:211]
v_mfma_f32_16x16x32_bf16 v[216:219],v[108:111],a[188:191],v[216:219]
v_mfma_f32_16x16x32_bf16 v[216:219],v[104:107],a[172:175],v[216:219]
v_mfma_f32_16x16x32_bf16 v[216:219],v[100:103],a[156:159],v[216:219]
v_mfma_f32_16x16x32_bf16 v[216:219],v[96:99],a[140:143],v[216:219]
v_mfma_f32_16x16x32_bf16 v[224:227],v[112:115],a[128:131],v[224:227]
v_mfma_f32_16x16x32_bf16 v[224:227],v[116:119],a[144:147],v[224:227]
v_mfma_f32_16x16x32_bf16 v[224:227],v[120:123],a[160:163],v[224:227]
v_mfma_f32_16x16x32_bf16 v[224:227],v[124:127],a[176:179],v[224:227]
v_mfma_f32_16x16x32_bf16 v[232:235],v[124:127],a[180:183],v[232:235]
v_mfma_f32_16x16x32_bf16 v[232:235],v[120:123],a[164:167],v[232:235]
v_mfma_f32_16x16x32_bf16 v[232:235],v[116:119],a[148:151],v[232:235]
v_mfma_f32_16x16x32_bf16 v[232:235],v[112:115],a[132:135],v[232:235]
v_mfma_f32_16x16x32_bf16 v[240:243],v[112:115],a[136:139],v[240:243]
v_mfma_f32_16x16x32_bf16 v[240:243],v[116:119],a[152:155],v[240:243]
v_mfma_f32_16x16x32_bf16 v[240:243],v[120:123],a[168:171],v[240:243]
v_mfma_f32_16x16x32_bf16 v[240:243],v[124:127],a[184:187],v[240:243]
v_mfma_f32_16x16x32_bf16 v[248:251],v[124:127],a[188:191],v[248:251]
v_mfma_f32_16x16x32_bf16 v[248:251],v[120:123],a[172:175],v[248:251]
v_mfma_f32_16x16x32_bf16 v[248:251],v[116:119],a[156:159],v[248:251]
v_mfma_f32_16x16x32_bf16 v[248:251],v[112:115],a[140:143],v[248:251]
s_waitcnt vmcnt(0)
s_barrier
v_mfma_f32_16x16x32_bf16 v[192:195],v[64:67],a[68:71],v[192:195]
buffer_store_dwordx2 v[58:59],v60,s[44:47],s18 offen offset:2304 sc1
v_mfma_f32_16x16x32_bf16 v[192:195],v[68:71],a[76:79],v[192:195]
v_mfma_f32_16x16x32_bf16 v[192:195],v[72:75],a[84:87],v[192:195]
v_mfma_f32_16x16x32_bf16 v[192:195],v[76:79],a[92:95],v[192:195]
v_mfma_f32_16x16x32_bf16 v[200:203],v[76:79],a[124:127],v[200:203]
v_mfma_f32_16x16x32_bf16 v[200:203],v[72:75],a[116:119],v[200:203]
v_mfma_f32_16x16x32_bf16 v[200:203],v[68:71],a[108:111],v[200:203]
v_mfma_f32_16x16x32_bf16 v[200:203],v[64:67],a[100:103],v[200:203]
v_mfma_f32_16x16x32_bf16 v[208:211],v[64:67],a[64:67],v[208:211]
v_mfma_f32_16x16x32_bf16 v[208:211],v[68:71],a[72:75],v[208:211]
v_mfma_f32_16x16x32_bf16 v[208:211],v[72:75],a[80:83],v[208:211]
v_mfma_f32_16x16x32_bf16 v[208:211],v[76:79],a[88:91],v[208:211]
v_mfma_f32_16x16x32_bf16 v[216:219],v[76:79],a[120:123],v[216:219]
v_mfma_f32_16x16x32_bf16 v[216:219],v[72:75],a[112:115],v[216:219]
v_mfma_f32_16x16x32_bf16 v[216:219],v[68:71],a[104:107],v[216:219]
v_mfma_f32_16x16x32_bf16 v[216:219],v[64:67],a[96:99],v[216:219]
s_waitcnt lgkmcnt(0)
v_mfma_f32_16x16x32_bf16 v[224:227],v[80:83],a[68:71],v[224:227]
buffer_load_dwordx2 v[62:63],v61,s[44:47],s91 offen offset:2312 sc1
v_mfma_f32_16x16x32_bf16 v[224:227],v[84:87],a[76:79],v[224:227]
v_mfma_f32_16x16x32_bf16 v[224:227],v[88:91],a[84:87],v[224:227]
v_mfma_f32_16x16x32_bf16 v[224:227],v[92:95],a[92:95],v[224:227]
v_mfma_f32_16x16x32_bf16 v[232:235],v[92:95],a[124:127],v[232:235]
v_mfma_f32_16x16x32_bf16 v[232:235],v[88:91],a[116:119],v[232:235]
v_mfma_f32_16x16x32_bf16 v[232:235],v[84:87],a[108:111],v[232:235]
v_mfma_f32_16x16x32_bf16 v[232:235],v[80:83],a[100:103],v[232:235]
v_mfma_f32_16x16x32_bf16 v[240:243],v[80:83],a[64:67],v[240:243]
v_mfma_f32_16x16x32_bf16 v[240:243],v[84:87],a[72:75],v[240:243]
v_mfma_f32_16x16x32_bf16 v[240:243],v[88:91],a[80:83],v[240:243]
v_mfma_f32_16x16x32_bf16 v[240:243],v[92:95],a[88:91],v[240:243]
v_mfma_f32_16x16x32_bf16 v[248:251],v[92:95],a[120:123],v[248:251]
v_mfma_f32_16x16x32_bf16 v[248:251],v[88:91],a[112:115],v[248:251]
v_mfma_f32_16x16x32_bf16 v[248:251],v[84:87],a[104:107],v[248:251]
v_mfma_f32_16x16x32_bf16 v[248:251],v[80:83],a[96:99],v[248:251]
s_waitcnt vmcnt(0)
v_cmp_ne_u32 s[56:57],s52,v62
v_cmp_ne_u32 s[58:59],s53,v63
s_nop 0
s_or_b64 s[56:57],s[56:57],s[58:59]
s_cbranch_scc1 L_fr8_%=
L_fo8_%=:
buffer_load_dwordx4 v[64:67],v6,s[36:39],s66 offen sc1
buffer_load_dwordx4 v[68:71],v6,s[36:39],s67 offen sc1
buffer_load_dwordx4 v[72:75],v6,s[36:39],s66 offen offset:1024 sc1
buffer_load_dwordx4 v[76:79],v6,s[36:39],s67 offen offset:1024 sc1
buffer_load_dwordx4 v[80:83],v6,s[36:39],s66 offen offset:2048 sc1
buffer_load_dwordx4 v[84:87],v6,s[36:39],s67 offen offset:2048 sc1
buffer_load_dwordx4 v[88:91],v28,s[36:39],s66 offen offset:3072 sc1
buffer_load_dwordx4 v[92:95],v28,s[36:39],s67 offen offset:3072 sc1
buffer_load_dwordx2 v[4:5],v3,s[44:47],s91 offen offset:2304 sc1
s_waitcnt vmcnt(8)
v_mfma_f32_16x16x32_bf16 v[192:195],v[48:51],v[64:67],v[192:195]
s_waitcnt vmcnt(7)
v_mfma_f32_16x16x32_bf16 v[192:195],v[48:51],v[68:71],v[192:195]
v_mfma_f32_16x16x32_bf16 v[224:227],v[52:55],v[64:67],v[224:227]
v_mfma_f32_16x16x32_bf16 v[224:227],v[52:55],v[68:71],v[224:227]
s_waitcnt vmcnt(6)
v_mfma_f32_16x16x32_bf16 v[200:203],v[48:51],v[72:75],v[200:203]
s_waitcnt vmcnt(5)
v_mfma_f32_16x16x32_bf16 v[200:203],v[48:51],v[76:79],v[200:203]
v_mfma_f32_16x16x32_bf16 v[232:235],v[52:55],v[72:75],v[232:235]
v_mfma_f32_16x16x32_bf16 v[232:235],v[52:55],v[76:79],v[232:235]
s_waitcnt vmcnt(4)
v_mfma_f32_16x16x32_bf16 v[208:211],v[48:51],v[80:83],v[208:211]
s_waitcnt vmcnt(3)
v_mfma_f32_16x16x32_bf16 v[208:211],v[48:51],v[84:87],v[208:211]
v_mfma_f32_16x16x32_bf16 v[240:243],v[52:55],v[80:83],v[240:243]
v_mfma_f32_16x16x32_bf16 v[240:243],v[52:55],v[84:87],v[240:243]
s_waitcnt vmcnt(2)
v_mfma_f32_16x16x32_bf16 v[216:219],v[48:51],v[88:91],v[216:219]
s_waitcnt vmcnt(1)
v_mfma_f32_16x16x32_bf16 v[216:219],v[48:51],v[92:95],v[216:219]
v_mfma_f32_16x16x32_bf16 v[248:251],v[52:55],v[88:91],v[248:251]
v_mfma_f32_16x16x32_bf16 v[248:251],v[52:55],v[92:95],v[248:251]
s_waitcnt vmcnt(0)
v_cmp_ne_u32 s[56:57],s52,v4
v_cmp_ne_u32 s[58:59],s53,v5
s_nop 0
s_or_b64 s[56:57],s[56:57],s[58:59]
s_cbranch_scc1 L_fr9_%=
L_fo9_%=:
buffer_load_dwordx4 v[96:99],v6,s[36:39],s68 offen sc1
buffer_load_dwordx4 v[100:103],v6,s[36:39],s68 offen offset:1024 sc1
buffer_load_dwordx4 v[104:107],v6,s[36:39],s68 offen offset:2048 sc1
buffer_load_dwordx4 v[108:111],v6,s[36:39],s68 offen offset:3072 sc1
buffer_load_dwordx4 v[112:115],v6,s[36:39],s65 offen sc1
buffer_load_dwordx4 v[116:119],v6,s[36:39],s65 offen offset:1024 sc1
buffer_load_dwordx4 v[120:123],v6,s[36:39],s65 offen offset:2048 sc1
buffer_load_dwordx4 v[124:127],v28,s[36:39],s65 offen offset:3072 sc1
s_waitcnt vmcnt(7)
v_mfma_f32_16x16x32_bf16 v[128:131],v[48:51],v[96:99],0
s_waitcnt vmcnt(6)
v_mfma_f32_16x16x32_bf16 v[128:131],v[48:51],v[100:103],v[128:131]
s_waitcnt vmcnt(5)
v_mfma_f32_16x16x32_bf16 v[128:131],v[48:51],v[104:107],v[128:131]
s_waitcnt vmcnt(4)
v_mfma_f32_16x16x32_bf16 v[128:131],v[48:51],v[108:111],v[128:131]
v_mfma_f32_16x16x32_bf16 v[160:163],v[52:55],v[96:99],0
v_mfma_f32_16x16x32_bf16 v[160:163],v[52:55],v[100:103],v[160:163]
v_mfma_f32_16x16x32_bf16 v[160:163],v[52:55],v[104:107],v[160:163]
)A"
R"A(v_mfma_f32_16x16x32_bf16 v[160:163],v[52:55],v[108:111],v[160:163]
s_waitcnt vmcnt(3)
v_mfma_f32_16x16x32_bf16 v[192:195],v[48:51],v[112:115],v[192:195]
v_mfma_f32_16x16x32_bf16 v[224:227],v[52:55],v[112:115],v[224:227]
s_waitcnt vmcnt(2)
v_mfma_f32_16x16x32_bf16 v[200:203],v[48:51],v[116:119],v[200:203]
v_mfma_f32_16x16x32_bf16 v[232:235],v[52:55],v[116:119],v[232:235]
s_waitcnt vmcnt(1)
v_cvt_pk_bf16_f32 v128,v128,v129
v_cvt_pk_bf16_f32 v129,v130,v131
v_mfma_f32_16x16x32_bf16 v[208:211],v[48:51],v[120:123],v[208:211]
v_mfma_f32_16x16x32_bf16 v[240:243],v[52:55],v[120:123],v[240:243]
s_waitcnt vmcnt(0)
v_cvt_pk_bf16_f32 v130,v160,v161
v_cvt_pk_bf16_f32 v131,v162,v163
v_mfma_f32_16x16x32_bf16 v[216:219],v[48:51],v[124:127],v[216:219]
v_cvt_pk_bf16_f32 v192,v192,v193
v_cvt_pk_bf16_f32 v193,v194,v195
v_cvt_pk_bf16_f32 v194,v224,v225
v_cvt_pk_bf16_f32 v195,v226,v227
v_cvt_pk_bf16_f32 v200,v200,v201
v_cvt_pk_bf16_f32 v201,v202,v203
v_mfma_f32_16x16x32_bf16 v[248:251],v[52:55],v[124:127],v[248:251]
v_permlane16_swap_b32 v128,v130
v_permlane16_swap_b32 v129,v131
v_cvt_pk_bf16_f32 v202,v232,v233
v_cvt_pk_bf16_f32 v203,v234,v235
v_cvt_pk_bf16_f32 v208,v208,v209
v_cvt_pk_bf16_f32 v209,v210,v211
v_cvt_pk_bf16_f32 v210,v240,v241
v_cvt_pk_bf16_f32 v211,v242,v243
buffer_store_dwordx4 v[128:131],v56,s[24:27],s83 offen offset:256 sc1 nt
v_permlane16_swap_b32 v192,v194
v_permlane16_swap_b32 v193,v195
buffer_store_dwordx4 v[192:195],v56,s[24:27],s87 offen sc1 nt
v_permlane16_swap_b32 v200,v202
v_permlane16_swap_b32 v201,v203
v_cvt_pk_bf16_f32 v216,v216,v217
v_cvt_pk_bf16_f32 v217,v218,v219
v_cvt_pk_bf16_f32 v218,v248,v249
v_cvt_pk_bf16_f32 v219,v250,v251
buffer_store_dwordx4 v[200:203],v56,s[24:27],s88 offen sc1 nt
v_permlane16_swap_b32 v208,v210
v_permlane16_swap_b32 v209,v211
buffer_store_dwordx4 v[208:211],v56,s[24:27],s89 offen sc1 nt
v_permlane16_swap_b32 v216,v218
v_permlane16_swap_b32 v217,v219
buffer_store_dwordx4 v[216:219],v56,s[24:27],s90 offen sc1 nt
s_lshl_b32 s69,s62,20
s_add_u32 s69,s69,s18
buffer_store_dword v20,v22,s[44:47],s69 offen sc1
L_geS_%=:
s_branch L_end_%=
L_hot_%=:
L_fr1_%=:
s_mov_b32 s19,65536
L_fl1_%=:
buffer_load_dwordx2 v[4:5],v3,s[44:47],s91 offen offset:256 sc1
s_waitcnt vmcnt(0)
s_sub_u32 s19,s19,1
s_cmp_eq_u32 s19,0
s_cbranch_scc1 L_fo1_%=
v_cmp_ne_u32 s[56:57],s52,v4
v_cmp_ne_u32 s[58:59],s53,v5
s_nop 1
s_or_b64 s[56:57],s[56:57],s[58:59]
s_cbranch_scc1 L_fl1_%=
s_branch L_fo1_%=
L_fr2_%=:
s_mov_b32 s19,65536
L_fl2_%=:
buffer_load_dwordx2 v[4:5],v3,s[44:47],s91 offen offset:1280 sc1
s_waitcnt vmcnt(0)
s_sub_u32 s19,s19,1
s_cmp_eq_u32 s19,0
s_cbranch_scc1 L_fo2_%=
v_cmp_ne_u32 s[56:57],s52,v4
v_cmp_ne_u32 s[58:59],s53,v5
s_nop 1
s_or_b64 s[56:57],s[56:57],s[58:59]
s_cbranch_scc1 L_fl2_%=
s_branch L_fo2_%=
L_fr3_%=:
s_mov_b32 s19,65536
L_fl3_%=:
buffer_load_dwordx2 v[4:5],v3,s[44:47],s91 offen offset:1280 sc1
s_waitcnt vmcnt(0)
s_sub_u32 s19,s19,1
s_cmp_eq_u32 s19,0
s_cbranch_scc1 L_fo3_%=
v_cmp_ne_u32 s[56:57],s52,v4
v_cmp_ne_u32 s[58:59],s53,v5
s_nop 1
s_or_b64 s[56:57],s[56:57],s[58:59]
s_cbranch_scc1 L_fl3_%=
s_branch L_fo3_%=
L_fr4_%=:
s_mov_b32 s19,65536
L_fl4_%=:
buffer_load_dwordx2 v[62:63],v61,s[44:47],s91 offen offset:2312 sc1
s_waitcnt vmcnt(0)
s_sub_u32 s19,s19,1
s_cmp_eq_u32 s19,0
s_cbranch_scc1 L_fo4_%=
v_cmp_ne_u32 s[56:57],s52,v62
v_cmp_ne_u32 s[58:59],s53,v63
s_nop 1
s_or_b64 s[56:57],s[56:57],s[58:59]
s_cbranch_scc1 L_fl4_%=
s_branch L_fo4_%=
L_fr5_%=:
s_mov_b32 s19,65536
L_fl5_%=:
buffer_load_dwordx2 v[4:5],v3,s[44:47],s91 offen offset:2304 sc1
s_waitcnt vmcnt(0)
s_sub_u32 s19,s19,1
s_cmp_eq_u32 s19,0
s_cbranch_scc1 L_fo5_%=
v_cmp_ne_u32 s[56:57],s52,v4
v_cmp_ne_u32 s[58:59],s53,v5
s_nop 1
s_or_b64 s[56:57],s[56:57],s[58:59]
s_cbranch_scc1 L_fl5_%=
s_branch L_fo5_%=
L_fr6_%=:
s_mov_b32 s19,65536
L_fl6_%=:
buffer_load_dwordx2 v[4:5],v3,s[44:47],s91 offen offset:256 sc1
s_waitcnt vmcnt(0)
s_sub_u32 s19,s19,1
s_cmp_eq_u32 s19,0
s_cbranch_scc1 L_fo6_%=
v_cmp_ne_u32 s[56:57],s52,v4
v_cmp_ne_u32 s[58:59],s53,v5
s_nop 1
s_or_b64 s[56:57],s[56:57],s[58:59]
s_cbranch_scc1 L_fl6_%=
s_branch L_fo6_%=
L_fr7_%=:
s_mov_b32 s19,65536
L_fl7_%=:
buffer_load_dwordx2 v[4:5],v3,s[44:47],s91 offen offset:1280 sc1
s_waitcnt vmcnt(0)
s_sub_u32 s19,s19,1
s_cmp_eq_u32 s19,0
s_cbranch_scc1 L_fo7_%=
v_cmp_ne_u32 s[56:57],s52,v4
v_cmp_ne_u32 s[58:59],s53,v5
s_nop 1
s_or_b64 s[56:57],s[56:57],s[58:59]
s_cbranch_scc1 L_fl7_%=
s_branch L_fo7_%=
L_fr8_%=:
s_mov_b32 s19,65536
L_fl8_%=:
buffer_load_dwordx2 v[62:63],v61,s[44:47],s91 offen offset:2312 sc1
s_waitcnt vmcnt(0)
s_sub_u32 s19,s19,1
s_cmp_eq_u32 s19,0
s_cbranch_scc1 L_fo8_%=
v_cmp_ne_u32 s[56:57],s52,v62
v_cmp_ne_u32 s[58:59],s53,v63
s_nop 1
s_or_b64 s[56:57],s[56:57],s[58:59]
s_cbranch_scc1 L_fl8_%=
s_branch L_fo8_%=
L_fr9_%=:
s_mov_b32 s19,65536
L_fl9_%=:
buffer_load_dwordx2 v[4:5],v3,s[44:47],s91 offen offset:2304 sc1
s_waitcnt vmcnt(0)
s_sub_u32 s19,s19,1
s_cmp_eq_u32 s19,0
s_cbranch_scc1 L_fo9_%=
v_cmp_ne_u32 s[56:57],s52,v4
v_cmp_ne_u32 s[58:59],s53,v5
s_nop 1
s_or_b64 s[56:57],s[56:57],s[58:59]
s_cbranch_scc1 L_fl9_%=
s_branch L_fo9_%=
L_slow0_%=:
s_waitcnt vmcnt(0) lgkmcnt(0)
s_nop 7
v_accvgpr_write_b32 a160,v32
v_accvgpr_write_b32 a161,v33
v_accvgpr_write_b32 a162,v34
v_accvgpr_write_b32 a163,v35
v_accvgpr_write_b32 a164,v36
v_accvgpr_write_b32 a165,v37
v_accvgpr_write_b32 a166,v38
v_accvgpr_write_b32 a167,v39
v_accvgpr_write_b32 a168,v40
v_accvgpr_write_b32 a169,v41
v_accvgpr_write_b32 a170,v42
v_accvgpr_write_b32 a171,v43
v_accvgpr_write_b32 a172,v44
v_accvgpr_write_b32 a173,v45
v_accvgpr_write_b32 a174,v46
v_accvgpr_write_b32 a175,v47
s_waitcnt vmcnt(0) lgkmcnt(0)
s_nop 7
v_bfe_u32 v11,v6,4,4
v_lshrrev_b32 v12,8,v6
v_lshlrev_b32 v8,8,v11
v_lshl_add_u32 v8,v12,4,v8
v_lshlrev_b32 v9,10,v12
v_lshl_add_u32 v9,v11,1,v9
v_add_u32 v10,4096,v9
v_mov_b32 v48,0xff800000
v_mov_b32 v52,0xff800000
v_mov_b32 v56,0xff800000
v_mov_b32 v60,0xff800000
s_mov_b32 s65,0
s_mov_b32 s66,64
L_s10_%=:
buffer_load_dwordx4 a[128:131],v8,s[20:23],s65 offen
buffer_load_dwordx4 a[132:135],v8,s[20:23],s65 offen offset:64
buffer_load_dwordx4 a[136:139],v8,s[20:23],s65 offen offset:128
buffer_load_dwordx4 a[140:143],v8,s[20:23],s65 offen offset:192
s_waitcnt vmcnt(0)
v_mfma_f32_16x16x32_bf16 v[64:67],a[128:131],v[132:135],0
v_mfma_f32_16x16x32_bf16 v[64:67],a[132:135],v[140:143],v[64:67]
v_mfma_f32_16x16x32_bf16 v[64:67],a[136:139],v[148:151],v[64:67]
v_mfma_f32_16x16x32_bf16 v[64:67],a[140:143],v[156:159],v[64:67]
v_mfma_f32_16x16x32_bf16 v[80:83],a[128:131],v[164:167],0
v_mfma_f32_16x16x32_bf16 v[80:83],a[132:135],v[172:175],v[80:83]
v_mfma_f32_16x16x32_bf16 v[80:83],a[136:139],v[180:183],v[80:83]
v_mfma_f32_16x16x32_bf16 v[80:83],a[140:143],v[188:191],v[80:83]
v_mfma_f32_16x16x32_bf16 v[96:99],a[128:131],v[196:199],0
v_mfma_f32_16x16x32_bf16 v[96:99],a[132:135],v[204:207],v[96:99]
v_mfma_f32_16x16x32_bf16 v[96:99],a[136:139],v[212:215],v[96:99]
v_mfma_f32_16x16x32_bf16 v[96:99],a[140:143],v[220:223],v[96:99]
v_mfma_f32_16x16x32_bf16 v[112:115],a[128:131],v[228:231],0
v_mfma_f32_16x16x32_bf16 v[112:115],a[132:135],v[236:239],v[112:115]
v_mfma_f32_16x16x32_bf16 v[112:115],a[136:139],v[244:247],v[112:115]
v_mfma_f32_16x16x32_bf16 v[112:115],a[140:143],v[252:255],v[112:115]
s_nop 7
v_max3_f32 v48,v48,v64,v65
v_max3_f32 v48,v48,v66,v67
v_max3_f32 v52,v52,v80,v81
v_max3_f32 v52,v52,v82,v83
v_max3_f32 v56,v56,v96,v97
v_max3_f32 v56,v56,v98,v99
v_max3_f32 v60,v60,v112,v113
v_max3_f32 v60,v60,v114,v115
s_add_u32 s65,s65,4096
s_sub_u32 s66,s66,1
s_cmp_lg_u32 s66,0
s_cbranch_scc1 L_s10_%=
v_mov_b32 v11,v48
s_nop 1
v_permlane32_swap_b32 v48,v11
v_max_f32 v48,v48,v11
v_mov_b32 v11,v48
s_nop 1
v_permlane16_swap_b32 v48,v11
v_max_f32 v48,v48,v11
v_sub_f32 v32,0,v48
v_sub_f32 v33,0,v48
v_sub_f32 v34,0,v48
v_sub_f32 v35,0,v48
v_mov_b32 v11,v52
s_nop 1
v_permlane32_swap_b32 v52,v11
v_max_f32 v52,v52,v11
v_mov_b32 v11,v52
s_nop 1
v_permlane16_swap_b32 v52,v11
v_max_f32 v52,v52,v11
v_sub_f32 v36,0,v52
v_sub_f32 v37,0,v52
v_sub_f32 v38,0,v52
v_sub_f32 v39,0,v52
v_mov_b32 v11,v56
s_nop 1
v_permlane32_swap_b32 v56,v11
v_max_f32 v56,v56,v11
v_mov_b32 v11,v56
s_nop 1
v_permlane16_swap_b32 v56,v11
v_max_f32 v56,v56,v11
v_sub_f32 v40,0,v56
v_sub_f32 v41,0,v56
v_sub_f32 v42,0,v56
v_sub_f32 v43,0,v56
v_mov_b32 v11,v60
s_nop 1
v_permlane32_swap_b32 v60,v11
v_max_f32 v60,v60,v11
v_mov_b32 v11,v60
s_nop 1
v_permlane16_swap_b32 v60,v11
v_max_f32 v60,v60,v11
v_max_f32 v60,s72,v60
v_sub_f32 v44,0,v60
v_sub_f32 v45,0,v60
v_sub_f32 v46,0,v60
v_sub_f32 v47,0,v60
v_mov_b32 v48,0
v_mov_b32 v49,0
v_mov_b32 v50,0
v_mov_b32 v51,0
v_mov_b32 v52,0
v_mov_b32 v53,0
v_mov_b32 v54,0
v_mov_b32 v55,0
v_mov_b32 v56,0
v_mov_b32 v57,0
v_mov_b32 v58,0
v_mov_b32 v59,0
v_mov_b32 v60,0
v_mov_b32 v61,0
v_mov_b32 v62,0
v_mov_b32 v63,0
v_accvgpr_write_b32 a0,0
v_accvgpr_write_b32 a1,0
v_accvgpr_write_b32 a2,0
v_accvgpr_write_b32 a3,0
v_accvgpr_write_b32 a4,0
v_accvgpr_write_b32 a5,0
v_accvgpr_write_b32 a6,0
v_accvgpr_write_b32 a7,0
v_accvgpr_write_b32 a8,0
v_accvgpr_write_b32 a9,0
v_accvgpr_write_b32 a10,0
v_accvgpr_write_b32 a11,0
v_accvgpr_write_b32 a12,0
v_accvgpr_write_b32 a13,0
v_accvgpr_write_b32 a14,0
v_accvgpr_write_b32 a15,0
v_accvgpr_write_b32 a16,0
v_accvgpr_write_b32 a17,0
v_accvgpr_write_b32 a18,0
v_accvgpr_write_b32 a19,0
v_accvgpr_write_b32 a20,0
v_accvgpr_write_b32 a21,0
v_accvgpr_write_b32 a22,0
v_accvgpr_write_b32 a23,0
v_accvgpr_write_b32 a24,0
v_accvgpr_write_b32 a25,0
v_accvgpr_write_b32 a26,0
v_accvgpr_write_b32 a27,0
v_accvgpr_write_b32 a28,0
v_accvgpr_write_b32 a29,0
v_accvgpr_write_b32 a30,0
v_accvgpr_write_b32 a31,0
v_accvgpr_write_b32 a32,0
v_accvgpr_write_b32 a33,0
v_accvgpr_write_b32 a34,0
v_accvgpr_write_b32 a35,0
v_accvgpr_write_b32 a36,0
v_accvgpr_write_b32 a37,0
v_accvgpr_write_b32 a38,0
v_accvgpr_write_b32 a39,0
v_accvgpr_write_b32 a40,0
v_accvgpr_write_b32 a41,0
v_accvgpr_write_b32 a42,0
v_accvgpr_write_b32 a43,0
v_accvgpr_write_b32 a44,0
v_accvgpr_write_b32 a45,0
v_accvgpr_write_b32 a46,0
v_accvgpr_write_b32 a47,0
v_accvgpr_write_b32 a48,0
v_accvgpr_write_b32 a49,0
v_accvgpr_write_b32 a50,0
v_accvgpr_write_b32 a51,0
v_accvgpr_write_b32 a52,0
v_accvgpr_write_b32 a53,0
v_accvgpr_write_b32 a54,0
v_accvgpr_write_b32 a55,0
v_accvgpr_write_b32 a56,0
v_accvgpr_write_b32 a57,0
v_accvgpr_write_b32 a58,0
v_accvgpr_write_b32 a59,0
v_accvgpr_write_b32 a60,0
v_accvgpr_write_b32 a61,0
v_accvgpr_write_b32 a62,0
v_accvgpr_write_b32 a63,0
v_accvgpr_write_b32 a64,0
v_accvgpr_write_b32 a65,0
v_accvgpr_write_b32 a66,0
v_accvgpr_write_b32 a67,0
v_accvgpr_write_b32 a68,0
v_accvgpr_write_b32 a69,0
v_accvgpr_write_b32 a70,0
v_accvgpr_write_b32 a71,0
v_accvgpr_write_b32 a72,0
v_accvgpr_write_b32 a73,0
v_accvgpr_write_b32 a74,0
v_accvgpr_write_b32 a75,0
v_accvgpr_write_b32 a76,0
v_accvgpr_write_b32 a77,0
v_accvgpr_write_b32 a78,0
v_accvgpr_write_b32 a79,0
v_accvgpr_write_b32 a80,0
v_accvgpr_write_b32 a81,0
v_accvgpr_write_b32 a82,0
v_accvgpr_write_b32 a83,0
v_accvgpr_write_b32 a84,0
v_accvgpr_write_b32 a85,0
v_accvgpr_write_b32 a86,0
v_accvgpr_write_b32 a87,0
v_accvgpr_write_b32 a88,0
v_accvgpr_write_b32 a89,0
v_accvgpr_write_b32 a90,0
v_accvgpr_write_b32 a91,0
v_accvgpr_write_b32 a92,0
v_accvgpr_write_b32 a93,0
v_accvgpr_write_b32 a94,0
v_accvgpr_write_b32 a95,0
v_accvgpr_write_b32 a96,0
v_accvgpr_write_b32 a97,0
v_accvgpr_write_b32 a98,0
v_accvgpr_write_b32 a99,0
v_accvgpr_write_b32 a100,0
v_accvgpr_write_b32 a101,0
v_accvgpr_write_b32 a102,0
v_accvgpr_write_b32 a103,0
v_accvgpr_write_b32 a104,0
v_accvgpr_write_b32 a105,0
v_accvgpr_write_b32 a106,0
v_accvgpr_write_b32 a107,0
v_accvgpr_write_b32 a108,0
v_accvgpr_write_b32 a109,0
v_accvgpr_write_b32 a110,0
v_accvgpr_write_b32 a111,0
v_accvgpr_write_b32 a112,0
v_accvgpr_write_b32 a113,0
v_accvgpr_write_b32 a114,0
v_accvgpr_write_b32 a115,0
v_accvgpr_write_b32 a116,0
v_accvgpr_write_b32 a117,0
v_accvgpr_write_b32 a118,0
v_accvgpr_write_b32 a119,0
v_accvgpr_write_b32 a120,0
v_accvgpr_write_b32 a121,0
v_accvgpr_write_b32 a122,0
v_accvgpr_write_b32 a123,0
)A"
R"A(v_accvgpr_write_b32 a124,0
v_accvgpr_write_b32 a125,0
v_accvgpr_write_b32 a126,0
v_accvgpr_write_b32 a127,0
s_mov_b32 s65,0
s_mov_b32 s66,32
L_s20_%=:
s_add_u32 s67,s65,4096
buffer_load_dwordx4 a[128:131],v8,s[20:23],s65 offen
buffer_load_dwordx4 a[132:135],v8,s[20:23],s65 offen offset:64
buffer_load_dwordx4 a[136:139],v8,s[20:23],s65 offen offset:128
buffer_load_dwordx4 a[140:143],v8,s[20:23],s65 offen offset:192
buffer_load_dwordx4 a[144:147],v8,s[20:23],s67 offen
buffer_load_dwordx4 a[148:151],v8,s[20:23],s67 offen offset:64
buffer_load_dwordx4 a[152:155],v8,s[20:23],s67 offen offset:128
buffer_load_dwordx4 a[156:159],v8,s[20:23],s67 offen offset:192
s_waitcnt vmcnt(0)
v_mfma_f32_16x16x32_bf16 v[64:67],a[128:131],v[132:135],v[32:35]
v_mfma_f32_16x16x32_bf16 v[64:67],a[132:135],v[140:143],v[64:67]
v_mfma_f32_16x16x32_bf16 v[64:67],a[136:139],v[148:151],v[64:67]
v_mfma_f32_16x16x32_bf16 v[64:67],a[140:143],v[156:159],v[64:67]
v_mfma_f32_16x16x32_bf16 v[68:71],a[144:147],v[132:135],v[32:35]
v_mfma_f32_16x16x32_bf16 v[68:71],a[148:151],v[140:143],v[68:71]
v_mfma_f32_16x16x32_bf16 v[68:71],a[152:155],v[148:151],v[68:71]
v_mfma_f32_16x16x32_bf16 v[68:71],a[156:159],v[156:159],v[68:71]
v_mfma_f32_16x16x32_bf16 v[80:83],a[128:131],v[164:167],v[36:39]
v_mfma_f32_16x16x32_bf16 v[80:83],a[132:135],v[172:175],v[80:83]
v_mfma_f32_16x16x32_bf16 v[80:83],a[136:139],v[180:183],v[80:83]
v_mfma_f32_16x16x32_bf16 v[80:83],a[140:143],v[188:191],v[80:83]
v_mfma_f32_16x16x32_bf16 v[84:87],a[144:147],v[164:167],v[36:39]
v_mfma_f32_16x16x32_bf16 v[84:87],a[148:151],v[172:175],v[84:87]
v_mfma_f32_16x16x32_bf16 v[84:87],a[152:155],v[180:183],v[84:87]
v_mfma_f32_16x16x32_bf16 v[84:87],a[156:159],v[188:191],v[84:87]
v_mfma_f32_16x16x32_bf16 v[96:99],a[128:131],v[196:199],v[40:43]
v_mfma_f32_16x16x32_bf16 v[96:99],a[132:135],v[204:207],v[96:99]
v_mfma_f32_16x16x32_bf16 v[96:99],a[136:139],v[212:215],v[96:99]
v_mfma_f32_16x16x32_bf16 v[96:99],a[140:143],v[220:223],v[96:99]
v_mfma_f32_16x16x32_bf16 v[100:103],a[144:147],v[196:199],v[40:43]
v_mfma_f32_16x16x32_bf16 v[100:103],a[148:151],v[204:207],v[100:103]
v_mfma_f32_16x16x32_bf16 v[100:103],a[152:155],v[212:215],v[100:103]
v_mfma_f32_16x16x32_bf16 v[100:103],a[156:159],v[220:223],v[100:103]
v_mfma_f32_16x16x32_bf16 v[112:115],a[128:131],v[228:231],v[44:47]
v_mfma_f32_16x16x32_bf16 v[112:115],a[132:135],v[236:239],v[112:115]
v_mfma_f32_16x16x32_bf16 v[112:115],a[136:139],v[244:247],v[112:115]
v_mfma_f32_16x16x32_bf16 v[112:115],a[140:143],v[252:255],v[112:115]
v_mfma_f32_16x16x32_bf16 v[116:119],a[144:147],v[228:231],v[44:47]
v_mfma_f32_16x16x32_bf16 v[116:119],a[148:151],v[236:239],v[116:119]
v_mfma_f32_16x16x32_bf16 v[116:119],a[152:155],v[244:247],v[116:119]
v_mfma_f32_16x16x32_bf16 v[116:119],a[156:159],v[252:255],v[116:119]
s_nop 7
v_exp_f32 v64,v64
v_exp_f32 v65,v65
v_exp_f32 v66,v66
v_exp_f32 v67,v67
v_exp_f32 v68,v68
v_exp_f32 v69,v69
v_exp_f32 v70,v70
v_exp_f32 v71,v71
s_nop 0
v_cvt_pk_bf16_f32 v64,v64,v65
v_cvt_pk_bf16_f32 v65,v66,v67
v_cvt_pk_bf16_f32 v66,v68,v69
v_cvt_pk_bf16_f32 v67,v70,v71
v_exp_f32 v80,v80
v_exp_f32 v81,v81
v_exp_f32 v82,v82
v_exp_f32 v83,v83
v_exp_f32 v84,v84
v_exp_f32 v85,v85
v_exp_f32 v86,v86
v_exp_f32 v87,v87
s_nop 0
v_cvt_pk_bf16_f32 v80,v80,v81
v_cvt_pk_bf16_f32 v81,v82,v83
v_cvt_pk_bf16_f32 v82,v84,v85
v_cvt_pk_bf16_f32 v83,v86,v87
v_exp_f32 v96,v96
v_exp_f32 v97,v97
v_exp_f32 v98,v98
v_exp_f32 v99,v99
v_exp_f32 v100,v100
v_exp_f32 v101,v101
v_exp_f32 v102,v102
v_exp_f32 v103,v103
s_nop 0
v_cvt_pk_bf16_f32 v96,v96,v97
v_cvt_pk_bf16_f32 v97,v98,v99
v_cvt_pk_bf16_f32 v98,v100,v101
v_cvt_pk_bf16_f32 v99,v102,v103
v_exp_f32 v112,v112
v_exp_f32 v113,v113
v_exp_f32 v114,v114
v_exp_f32 v115,v115
v_exp_f32 v116,v116
v_exp_f32 v117,v117
v_exp_f32 v118,v118
v_exp_f32 v119,v119
s_nop 0
v_cvt_pk_bf16_f32 v112,v112,v113
v_cvt_pk_bf16_f32 v113,v114,v115
v_cvt_pk_bf16_f32 v114,v116,v117
v_cvt_pk_bf16_f32 v115,v118,v119
buffer_load_ushort v72,v9,s[24:27],s65 offen
buffer_load_ushort v73,v9,s[24:27],s65 offen offset:256
buffer_load_ushort v74,v9,s[24:27],s65 offen offset:512
buffer_load_ushort v75,v9,s[24:27],s65 offen offset:768
buffer_load_ushort v76,v10,s[24:27],s65 offen
buffer_load_ushort v77,v10,s[24:27],s65 offen offset:256
buffer_load_ushort v78,v10,s[24:27],s65 offen offset:512
buffer_load_ushort v79,v10,s[24:27],s65 offen offset:768
s_waitcnt vmcnt(0)
v_lshl_or_b32 v88,v73,16,v72
v_lshl_or_b32 v89,v75,16,v74
v_lshl_or_b32 v90,v77,16,v76
v_lshl_or_b32 v91,v79,16,v78
s_nop 1
v_mfma_f32_16x16x32_bf16 a[0:3],v[88:91],v[64:67],a[0:3]
v_mfma_f32_16x16x32_bf16 a[32:35],v[88:91],v[80:83],a[32:35]
v_mfma_f32_16x16x32_bf16 a[64:67],v[88:91],v[96:99],a[64:67]
v_mfma_f32_16x16x32_bf16 a[96:99],v[88:91],v[112:115],a[96:99]
buffer_load_ushort v72,v9,s[24:27],s65 offen offset:32
buffer_load_ushort v73,v9,s[24:27],s65 offen offset:288
buffer_load_ushort v74,v9,s[24:27],s65 offen offset:544
buffer_load_ushort v75,v9,s[24:27],s65 offen offset:800
buffer_load_ushort v76,v10,s[24:27],s65 offen offset:32
buffer_load_ushort v77,v10,s[24:27],s65 offen offset:288
buffer_load_ushort v78,v10,s[24:27],s65 offen offset:544
buffer_load_ushort v79,v10,s[24:27],s65 offen offset:800
s_waitcnt vmcnt(0)
v_lshl_or_b32 v88,v73,16,v72
v_lshl_or_b32 v89,v75,16,v74
v_lshl_or_b32 v90,v77,16,v76
v_lshl_or_b32 v91,v79,16,v78
s_nop 1
v_mfma_f32_16x16x32_bf16 a[4:7],v[88:91],v[64:67],a[4:7]
v_mfma_f32_16x16x32_bf16 a[36:39],v[88:91],v[80:83],a[36:39]
v_mfma_f32_16x16x32_bf16 a[68:71],v[88:91],v[96:99],a[68:71]
v_mfma_f32_16x16x32_bf16 a[100:103],v[88:91],v[112:115],a[100:103]
buffer_load_ushort v72,v9,s[24:27],s65 offen offset:64
buffer_load_ushort v73,v9,s[24:27],s65 offen offset:320
buffer_load_ushort v74,v9,s[24:27],s65 offen offset:576
buffer_load_ushort v75,v9,s[24:27],s65 offen offset:832
buffer_load_ushort v76,v10,s[24:27],s65 offen offset:64
buffer_load_ushort v77,v10,s[24:27],s65 offen offset:320
buffer_load_ushort v78,v10,s[24:27],s65 offen offset:576
buffer_load_ushort v79,v10,s[24:27],s65 offen offset:832
s_waitcnt vmcnt(0)
v_lshl_or_b32 v88,v73,16,v72
v_lshl_or_b32 v89,v75,16,v74
v_lshl_or_b32 v90,v77,16,v76
v_lshl_or_b32 v91,v79,16,v78
s_nop 1
v_mfma_f32_16x16x32_bf16 a[8:11],v[88:91],v[64:67],a[8:11]
v_mfma_f32_16x16x32_bf16 a[40:43],v[88:91],v[80:83],a[40:43]
v_mfma_f32_16x16x32_bf16 a[72:75],v[88:91],v[96:99],a[72:75]
v_mfma_f32_16x16x32_bf16 a[104:107],v[88:91],v[112:115],a[104:107]
buffer_load_ushort v72,v9,s[24:27],s65 offen offset:96
buffer_load_ushort v73,v9,s[24:27],s65 offen offset:352
buffer_load_ushort v74,v9,s[24:27],s65 offen offset:608
buffer_load_ushort v75,v9,s[24:27],s65 offen offset:864
buffer_load_ushort v76,v10,s[24:27],s65 offen offset:96
buffer_load_ushort v77,v10,s[24:27],s65 offen offset:352
buffer_load_ushort v78,v10,s[24:27],s65 offen offset:608
buffer_load_ushort v79,v10,s[24:27],s65 offen offset:864
s_waitcnt vmcnt(0)
v_lshl_or_b32 v88,v73,16,v72
v_lshl_or_b32 v89,v75,16,v74
v_lshl_or_b32 v90,v77,16,v76
v_lshl_or_b32 v91,v79,16,v78
s_nop 1
v_mfma_f32_16x16x32_bf16 a[12:15],v[88:91],v[64:67],a[12:15]
v_mfma_f32_16x16x32_bf16 a[44:47],v[88:91],v[80:83],a[44:47]
v_mfma_f32_16x16x32_bf16 a[76:79],v[88:91],v[96:99],a[76:79]
v_mfma_f32_16x16x32_bf16 a[108:111],v[88:91],v[112:115],a[108:111]
buffer_load_ushort v72,v9,s[24:27],s65 offen offset:128
buffer_load_ushort v73,v9,s[24:27],s65 offen offset:384
buffer_load_ushort v74,v9,s[24:27],s65 offen offset:640
buffer_load_ushort v75,v9,s[24:27],s65 offen offset:896
buffer_load_ushort v76,v10,s[24:27],s65 offen offset:128
buffer_load_ushort v77,v10,s[24:27],s65 offen offset:384
buffer_load_ushort v78,v10,s[24:27],s65 offen offset:640
buffer_load_ushort v79,v10,s[24:27],s65 offen offset:896
s_waitcnt vmcnt(0)
v_lshl_or_b32 v88,v73,16,v72
v_lshl_or_b32 v89,v75,16,v74
v_lshl_or_b32 v90,v77,16,v76
v_lshl_or_b32 v91,v79,16,v78
s_nop 1
v_mfma_f32_16x16x32_bf16 a[16:19],v[88:91],v[64:67],a[16:19]
v_mfma_f32_16x16x32_bf16 a[48:51],v[88:91],v[80:83],a[48:51]
v_mfma_f32_16x16x32_bf16 a[80:83],v[88:91],v[96:99],a[80:83]
v_mfma_f32_16x16x32_bf16 a[112:115],v[88:91],v[112:115],a[112:115]
buffer_load_ushort v72,v9,s[24:27],s65 offen offset:160
buffer_load_ushort v73,v9,s[24:27],s65 offen offset:416
buffer_load_ushort v74,v9,s[24:27],s65 offen offset:672
buffer_load_ushort v75,v9,s[24:27],s65 offen offset:928
buffer_load_ushort v76,v10,s[24:27],s65 offen offset:160
buffer_load_ushort v77,v10,s[24:27],s65 offen offset:416
buffer_load_ushort v78,v10,s[24:27],s65 offen offset:672
buffer_load_ushort v79,v10,s[24:27],s65 offen offset:928
s_waitcnt vmcnt(0)
v_lshl_or_b32 v88,v73,16,v72
v_lshl_or_b32 v89,v75,16,v74
v_lshl_or_b32 v90,v77,16,v76
v_lshl_or_b32 v91,v79,16,v78
s_nop 1
v_mfma_f32_16x16x32_bf16 a[20:23],v[88:91],v[64:67],a[20:23]
v_mfma_f32_16x16x32_bf16 a[52:55],v[88:91],v[80:83],a[52:55]
v_mfma_f32_16x16x32_bf16 a[84:87],v[88:91],v[96:99],a[84:87]
v_mfma_f32_16x16x32_bf16 a[116:119],v[88:91],v[112:115],a[116:119]
buffer_load_ushort v72,v9,s[24:27],s65 offen offset:192
buffer_load_ushort v73,v9,s[24:27],s65 offen offset:448
buffer_load_ushort v74,v9,s[24:27],s65 offen offset:704
buffer_load_ushort v75,v9,s[24:27],s65 offen offset:960
buffer_load_ushort v76,v10,s[24:27],s65 offen offset:192
buffer_load_ushort v77,v10,s[24:27],s65 offen offset:448
buffer_load_ushort v78,v10,s[24:27],s65 offen offset:704
buffer_load_ushort v79,v10,s[24:27],s65 offen offset:960
s_waitcnt vmcnt(0)
v_lshl_or_b32 v88,v73,16,v72
v_lshl_or_b32 v89,v75,16,v74
v_lshl_or_b32 v90,v77,16,v76
v_lshl_or_b32 v91,v79,16,v78
s_nop 1
v_mfma_f32_16x16x32_bf16 a[24:27],v[88:91],v[64:67],a[24:27]
v_mfma_f32_16x16x32_bf16 a[56:59],v[88:91],v[80:83],a[56:59]
v_mfma_f32_16x16x32_bf16 a[88:91],v[88:91],v[96:99],a[88:91]
v_mfma_f32_16x16x32_bf16 a[120:123],v[88:91],v[112:115],a[120:123]
buffer_load_ushort v72,v9,s[24:27],s65 offen offset:224
buffer_load_ushort v73,v9,s[24:27],s65 offen offset:480
buffer_load_ushort v74,v9,s[24:27],s65 offen offset:736
buffer_load_ushort v75,v9,s[24:27],s65 offen offset:992
buffer_load_ushort v76,v10,s[24:27],s65 offen offset:224
buffer_load_ushort v77,v10,s[24:27],s65 offen offset:480
buffer_load_ushort v78,v10,s[24:27],s65 offen offset:736
buffer_load_ushort v79,v10,s[24:27],s65 offen offset:992
s_waitcnt vmcnt(0)
v_lshl_or_b32 v88,v73,16,v72
v_lshl_or_b32 v89,v75,16,v74
v_lshl_or_b32 v90,v77,16,v76
v_lshl_or_b32 v91,v79,16,v78
s_nop 1
v_mfma_f32_16x16x32_bf16 a[28:31],v[88:91],v[64:67],a[28:31]
v_mfma_f32_16x16x32_bf16 a[60:63],v[88:91],v[80:83],a[60:63]
v_mfma_f32_16x16x32_bf16 a[92:95],v[88:91],v[96:99],a[92:95]
v_mfma_f32_16x16x32_bf16 a[124:127],v[88:91],v[112:115],a[124:127]
v_mfma_f32_16x16x32_bf16 v[48:51],v[28:31],v[64:67],v[48:51]
v_mfma_f32_16x16x32_bf16 v[52:55],v[28:31],v[80:83],v[52:55]
v_mfma_f32_16x16x32_bf16 v[56:59],v[28:31],v[96:99],v[56:59]
v_mfma_f32_16x16x32_bf16 v[60:63],v[28:31],v[112:115],v[60:63]
s_add_u32 s65,s65,8192
s_sub_u32 s66,s66,1
s_cmp_lg_u32 s66,0
s_cbranch_scc1 L_s20_%=
s_nop 7
v_max_f32 v24,0x00800000,v48
v_rcp_f32 v24,v24
v_max_f32 v25,0x00800000,v52
v_rcp_f32 v25,v25
v_max_f32 v26,0x00800000,v56
v_rcp_f32 v26,v26
v_max_f32 v27,0x00800000,v60
v_rcp_f32 v27,v27
v_accvgpr_read_b32 v8,a0
v_accvgpr_read_b32 v9,a1
v_accvgpr_read_b32 v10,a2
v_accvgpr_read_b32 v11,a3
v_accvgpr_read_b32 v12,a4
v_accvgpr_read_b32 v13,a5
v_accvgpr_read_b32 v14,a6
v_accvgpr_read_b32 v15,a7
v_mul_f32 v8,v8,v24
v_mul_f32 v9,v9,v24
v_mul_f32 v10,v10,v24
v_mul_f32 v11,v11,v24
v_mul_f32 v12,v12,v24
v_mul_f32 v13,v13,v24
v_mul_f32 v14,v14,v24
v_mul_f32 v15,v15,v24
v_cvt_pk_bf16_f32 v8,v8,v9
v_cvt_pk_bf16_f32 v9,v10,v11
v_cvt_pk_bf16_f32 v10,v12,v13
v_cvt_pk_bf16_f32 v11,v14,v15
ds_write_b128 v7,v[8:11] offset:6144
v_accvgpr_read_b32 v16,a8
v_accvgpr_read_b32 v17,a9
v_accvgpr_read_b32 v18,a10
v_accvgpr_read_b32 v19,a11
v_accvgpr_read_b32 v20,a12
v_accvgpr_read_b32 v21,a13
v_accvgpr_read_b32 v22,a14
v_accvgpr_read_b32 v23,a15
v_mul_f32 v16,v16,v24
v_mul_f32 v17,v17,v24
v_mul_f32 v18,v18,v24
v_mul_f32 v19,v19,v24
)A"
R"A(v_mul_f32 v20,v20,v24
v_mul_f32 v21,v21,v24
v_mul_f32 v22,v22,v24
v_mul_f32 v23,v23,v24
v_cvt_pk_bf16_f32 v16,v16,v17
v_cvt_pk_bf16_f32 v17,v18,v19
v_cvt_pk_bf16_f32 v18,v20,v21
v_cvt_pk_bf16_f32 v19,v22,v23
ds_write_b128 v7,v[16:19] offset:7168
v_accvgpr_read_b32 v8,a16
v_accvgpr_read_b32 v9,a17
v_accvgpr_read_b32 v10,a18
v_accvgpr_read_b32 v11,a19
v_accvgpr_read_b32 v12,a20
v_accvgpr_read_b32 v13,a21
v_accvgpr_read_b32 v14,a22
v_accvgpr_read_b32 v15,a23
v_mul_f32 v8,v8,v24
v_mul_f32 v9,v9,v24
v_mul_f32 v10,v10,v24
v_mul_f32 v11,v11,v24
v_mul_f32 v12,v12,v24
v_mul_f32 v13,v13,v24
v_mul_f32 v14,v14,v24
v_mul_f32 v15,v15,v24
v_cvt_pk_bf16_f32 v8,v8,v9
v_cvt_pk_bf16_f32 v9,v10,v11
v_cvt_pk_bf16_f32 v10,v12,v13
v_cvt_pk_bf16_f32 v11,v14,v15
ds_write_b128 v7,v[8:11]
v_accvgpr_read_b32 v16,a24
v_accvgpr_read_b32 v17,a25
v_accvgpr_read_b32 v18,a26
v_accvgpr_read_b32 v19,a27
v_accvgpr_read_b32 v20,a28
v_accvgpr_read_b32 v21,a29
v_accvgpr_read_b32 v22,a30
v_accvgpr_read_b32 v23,a31
v_mul_f32 v16,v16,v24
v_mul_f32 v17,v17,v24
v_mul_f32 v18,v18,v24
v_mul_f32 v19,v19,v24
v_mul_f32 v20,v20,v24
v_mul_f32 v21,v21,v24
v_mul_f32 v22,v22,v24
v_mul_f32 v23,v23,v24
v_cvt_pk_bf16_f32 v16,v16,v17
v_cvt_pk_bf16_f32 v17,v18,v19
v_cvt_pk_bf16_f32 v18,v20,v21
v_cvt_pk_bf16_f32 v19,v22,v23
ds_write_b128 v7,v[16:19] offset:1024
v_accvgpr_read_b32 v32,a160
v_accvgpr_read_b32 v33,a161
v_accvgpr_read_b32 v34,a162
v_accvgpr_read_b32 v35,a163
v_accvgpr_read_b32 v36,a164
v_accvgpr_read_b32 v37,a165
v_accvgpr_read_b32 v38,a166
v_accvgpr_read_b32 v39,a167
v_accvgpr_read_b32 v40,a168
v_accvgpr_read_b32 v41,a169
v_accvgpr_read_b32 v42,a170
v_accvgpr_read_b32 v43,a171
v_accvgpr_read_b32 v44,a172
v_accvgpr_read_b32 v45,a173
v_accvgpr_read_b32 v46,a174
v_accvgpr_read_b32 v47,a175
s_waitcnt lgkmcnt(0)
v_subrev_u32 v8,s78,v1
ds_read_b128 a[128:131],v8
ds_read_b128 a[132:135],v8 offset:256
ds_read_b128 a[136:139],v8 offset:512
ds_read_b128 a[140:143],v8 offset:768
ds_read_b128 a[144:147],v8 offset:4096
ds_read_b128 a[148:151],v8 offset:4352
ds_read_b128 a[152:155],v8 offset:4608
ds_read_b128 a[156:159],v8 offset:4864
ds_read_b128 a[160:163],v8 offset:8192
ds_read_b128 a[164:167],v8 offset:8448
ds_read_b128 a[168:171],v8 offset:8704
ds_read_b128 a[172:175],v8 offset:8960
ds_read_b128 a[176:179],v8 offset:12288
ds_read_b128 a[180:183],v8 offset:12544
ds_read_b128 a[184:187],v8 offset:12800
ds_read_b128 a[188:191],v8 offset:13056
s_waitcnt lgkmcnt(15)
v_mfma_f32_16x16x32_bf16 v[64:67],a[128:131],v[128:131],v[32:35]
s_waitcnt lgkmcnt(14)
v_mfma_f32_16x16x32_bf16 v[64:67],a[132:135],v[136:139],v[64:67]
s_waitcnt lgkmcnt(13)
v_mfma_f32_16x16x32_bf16 v[64:67],a[136:139],v[144:147],v[64:67]
s_waitcnt lgkmcnt(12)
v_mfma_f32_16x16x32_bf16 v[64:67],a[140:143],v[152:155],v[64:67]
s_waitcnt lgkmcnt(8)
v_mfma_f32_16x16x32_bf16 v[68:71],a[156:159],v[152:155],v[32:35]
v_mfma_f32_16x16x32_bf16 v[68:71],a[152:155],v[144:147],v[68:71]
v_mfma_f32_16x16x32_bf16 v[68:71],a[148:151],v[136:139],v[68:71]
v_mfma_f32_16x16x32_bf16 v[68:71],a[144:147],v[128:131],v[68:71]
s_waitcnt lgkmcnt(0)
v_mfma_f32_16x16x32_bf16 v[72:75],a[160:163],v[128:131],v[32:35]
v_mfma_f32_16x16x32_bf16 v[72:75],a[164:167],v[136:139],v[72:75]
v_mfma_f32_16x16x32_bf16 v[72:75],a[168:171],v[144:147],v[72:75]
v_mfma_f32_16x16x32_bf16 v[72:75],a[172:175],v[152:155],v[72:75]
v_mfma_f32_16x16x32_bf16 v[76:79],a[188:191],v[152:155],v[32:35]
v_mfma_f32_16x16x32_bf16 v[76:79],a[184:187],v[144:147],v[76:79]
v_mfma_f32_16x16x32_bf16 v[76:79],a[180:183],v[136:139],v[76:79]
v_mfma_f32_16x16x32_bf16 v[76:79],a[176:179],v[128:131],v[76:79]
v_mfma_f32_16x16x32_bf16 v[80:83],a[128:131],v[160:163],v[36:39]
v_mfma_f32_16x16x32_bf16 v[80:83],a[132:135],v[168:171],v[80:83]
v_mfma_f32_16x16x32_bf16 v[80:83],a[136:139],v[176:179],v[80:83]
v_mfma_f32_16x16x32_bf16 v[80:83],a[140:143],v[184:187],v[80:83]
v_mfma_f32_16x16x32_bf16 v[84:87],a[156:159],v[184:187],v[36:39]
v_mfma_f32_16x16x32_bf16 v[84:87],a[152:155],v[176:179],v[84:87]
v_mfma_f32_16x16x32_bf16 v[84:87],a[148:151],v[168:171],v[84:87]
v_mfma_f32_16x16x32_bf16 v[84:87],a[144:147],v[160:163],v[84:87]
v_mfma_f32_16x16x32_bf16 v[88:91],a[160:163],v[160:163],v[36:39]
v_mfma_f32_16x16x32_bf16 v[88:91],a[164:167],v[168:171],v[88:91]
v_mfma_f32_16x16x32_bf16 v[88:91],a[168:171],v[176:179],v[88:91]
v_mfma_f32_16x16x32_bf16 v[88:91],a[172:175],v[184:187],v[88:91]
v_mfma_f32_16x16x32_bf16 v[92:95],a[188:191],v[184:187],v[36:39]
v_mfma_f32_16x16x32_bf16 v[92:95],a[184:187],v[176:179],v[92:95]
v_mfma_f32_16x16x32_bf16 v[92:95],a[180:183],v[168:171],v[92:95]
v_mfma_f32_16x16x32_bf16 v[92:95],a[176:179],v[160:163],v[92:95]
v_mfma_f32_16x16x32_bf16 v[96:99],a[128:131],v[192:195],v[40:43]
v_mfma_f32_16x16x32_bf16 v[96:99],a[132:135],v[200:203],v[96:99]
v_mfma_f32_16x16x32_bf16 v[96:99],a[136:139],v[208:211],v[96:99]
v_mfma_f32_16x16x32_bf16 v[96:99],a[140:143],v[216:219],v[96:99]
v_mfma_f32_16x16x32_bf16 v[100:103],a[156:159],v[216:219],v[40:43]
v_mfma_f32_16x16x32_bf16 v[100:103],a[152:155],v[208:211],v[100:103]
v_mfma_f32_16x16x32_bf16 v[100:103],a[148:151],v[200:203],v[100:103]
v_mfma_f32_16x16x32_bf16 v[100:103],a[144:147],v[192:195],v[100:103]
v_mfma_f32_16x16x32_bf16 v[104:107],a[160:163],v[192:195],v[40:43]
v_mfma_f32_16x16x32_bf16 v[104:107],a[164:167],v[200:203],v[104:107]
v_mfma_f32_16x16x32_bf16 v[104:107],a[168:171],v[208:211],v[104:107]
v_mfma_f32_16x16x32_bf16 v[104:107],a[172:175],v[216:219],v[104:107]
v_mfma_f32_16x16x32_bf16 v[108:111],a[188:191],v[216:219],v[40:43]
v_mfma_f32_16x16x32_bf16 v[108:111],a[184:187],v[208:211],v[108:111]
v_mfma_f32_16x16x32_bf16 v[108:111],a[180:183],v[200:203],v[108:111]
v_mfma_f32_16x16x32_bf16 v[108:111],a[176:179],v[192:195],v[108:111]
v_exp_f32 v64,v64
v_exp_f32 v65,v65
s_nop 0
v_cvt_pk_bf16_f32 v64,v64,v65
v_exp_f32 v66,v66
v_exp_f32 v67,v67
s_nop 0
v_cvt_pk_bf16_f32 v65,v66,v67
v_exp_f32 v68,v68
v_exp_f32 v69,v69
s_nop 0
v_cvt_pk_bf16_f32 v66,v68,v69
v_exp_f32 v70,v70
v_exp_f32 v71,v71
s_nop 0
v_cvt_pk_bf16_f32 v67,v70,v71
v_exp_f32 v72,v72
v_exp_f32 v73,v73
s_nop 0
v_cvt_pk_bf16_f32 v72,v72,v73
v_exp_f32 v74,v74
v_exp_f32 v75,v75
s_nop 0
v_cvt_pk_bf16_f32 v73,v74,v75
v_exp_f32 v76,v76
v_exp_f32 v77,v77
s_nop 0
v_cvt_pk_bf16_f32 v74,v76,v77
v_exp_f32 v78,v78
v_exp_f32 v79,v79
s_nop 0
v_cvt_pk_bf16_f32 v75,v78,v79
v_exp_f32 v80,v80
v_exp_f32 v81,v81
s_nop 0
v_cvt_pk_bf16_f32 v80,v80,v81
v_exp_f32 v82,v82
v_exp_f32 v83,v83
s_nop 0
v_cvt_pk_bf16_f32 v81,v82,v83
v_exp_f32 v84,v84
v_exp_f32 v85,v85
s_nop 0
v_cvt_pk_bf16_f32 v82,v84,v85
v_exp_f32 v86,v86
v_exp_f32 v87,v87
s_nop 0
v_cvt_pk_bf16_f32 v83,v86,v87
v_exp_f32 v88,v88
v_exp_f32 v89,v89
s_nop 0
v_cvt_pk_bf16_f32 v88,v88,v89
v_mfma_f32_16x16x32_bf16 a[0:3],a[192:195],v[64:67],0
v_mfma_f32_16x16x32_bf16 a[0:3],a[196:199],v[72:75],a[0:3]
v_mfma_f32_16x16x32_bf16 a[4:7],a[200:203],v[64:67],0
v_mfma_f32_16x16x32_bf16 a[4:7],a[204:207],v[72:75],a[4:7]
v_mfma_f32_16x16x32_bf16 a[8:11],a[208:211],v[64:67],0
v_mfma_f32_16x16x32_bf16 a[8:11],a[212:215],v[72:75],a[8:11]
v_mfma_f32_16x16x32_bf16 a[12:15],a[216:219],v[64:67],0
v_mfma_f32_16x16x32_bf16 a[12:15],a[220:223],v[72:75],a[12:15]
v_mfma_f32_16x16x32_bf16 a[16:19],a[224:227],v[64:67],0
v_mfma_f32_16x16x32_bf16 a[16:19],a[228:231],v[72:75],a[16:19]
v_mfma_f32_16x16x32_bf16 a[20:23],a[232:235],v[64:67],0
v_mfma_f32_16x16x32_bf16 a[20:23],a[236:239],v[72:75],a[20:23]
v_mfma_f32_16x16x32_bf16 a[24:27],a[240:243],v[64:67],0
v_mfma_f32_16x16x32_bf16 a[24:27],a[244:247],v[72:75],a[24:27]
v_mfma_f32_16x16x32_bf16 a[28:31],a[248:251],v[64:67],0
v_mfma_f32_16x16x32_bf16 a[28:31],a[252:255],v[72:75],a[28:31]
v_mfma_f32_16x16x32_bf16 v[48:51],v[28:31],v[64:67],0
v_mfma_f32_16x16x32_bf16 v[48:51],v[28:31],v[72:75],v[48:51]
s_waitcnt vmcnt(0) lgkmcnt(0)
s_nop 7
s_branch L_ok0_%=
L_slow1_%=:
s_waitcnt vmcnt(0) lgkmcnt(0)
s_nop 7
v_bfe_u32 v11,v6,4,4
v_lshrrev_b32 v12,8,v6
v_lshlrev_b32 v8,8,v11
v_lshl_add_u32 v8,v12,4,v8
v_lshlrev_b32 v9,10,v12
v_lshl_add_u32 v9,v11,1,v9
v_add_u32 v10,4096,v9
v_mov_b32 v48,0xff800000
v_mov_b32 v52,0xff800000
v_mov_b32 v56,0xff800000
v_mov_b32 v60,0xff800000
s_mov_b32 s65,262144
s_mov_b32 s66,64
L_s11_%=:
buffer_load_dwordx4 a[128:131],v8,s[20:23],s65 offen
buffer_load_dwordx4 a[132:135],v8,s[20:23],s65 offen offset:64
buffer_load_dwordx4 a[136:139],v8,s[20:23],s65 offen offset:128
buffer_load_dwordx4 a[140:143],v8,s[20:23],s65 offen offset:192
s_waitcnt vmcnt(0)
v_mfma_f32_16x16x32_bf16 v[64:67],a[128:131],v[128:131],0
v_mfma_f32_16x16x32_bf16 v[64:67],a[132:135],v[136:139],v[64:67]
v_mfma_f32_16x16x32_bf16 v[64:67],a[136:139],v[144:147],v[64:67]
v_mfma_f32_16x16x32_bf16 v[64:67],a[140:143],v[152:155],v[64:67]
v_mfma_f32_16x16x32_bf16 v[80:83],a[128:131],v[160:163],0
v_mfma_f32_16x16x32_bf16 v[80:83],a[132:135],v[168:171],v[80:83]
v_mfma_f32_16x16x32_bf16 v[80:83],a[136:139],v[176:179],v[80:83]
v_mfma_f32_16x16x32_bf16 v[80:83],a[140:143],v[184:187],v[80:83]
v_mfma_f32_16x16x32_bf16 v[96:99],a[128:131],v[192:195],0
v_mfma_f32_16x16x32_bf16 v[96:99],a[132:135],v[200:203],v[96:99]
v_mfma_f32_16x16x32_bf16 v[96:99],a[136:139],v[208:211],v[96:99]
v_mfma_f32_16x16x32_bf16 v[96:99],a[140:143],v[216:219],v[96:99]
v_mfma_f32_16x16x32_bf16 v[112:115],a[128:131],v[224:227],0
v_mfma_f32_16x16x32_bf16 v[112:115],a[132:135],v[232:235],v[112:115]
v_mfma_f32_16x16x32_bf16 v[112:115],a[136:139],v[240:243],v[112:115]
v_mfma_f32_16x16x32_bf16 v[112:115],a[140:143],v[248:251],v[112:115]
s_nop 7
v_max3_f32 v48,v48,v64,v65
v_max3_f32 v48,v48,v66,v67
v_max3_f32 v52,v52,v80,v81
v_max3_f32 v52,v52,v82,v83
v_max3_f32 v56,v56,v96,v97
v_max3_f32 v56,v56,v98,v99
v_max3_f32 v60,v60,v112,v113
v_max3_f32 v60,v60,v114,v115
s_add_u32 s65,s65,4096
s_sub_u32 s66,s66,1
s_cmp_lg_u32 s66,0
s_cbranch_scc1 L_s11_%=
v_mov_b32 v11,v48
s_nop 1
v_permlane32_swap_b32 v48,v11
v_max_f32 v48,v48,v11
v_mov_b32 v11,v48
s_nop 1
v_permlane16_swap_b32 v48,v11
v_max_f32 v48,v48,v11
v_sub_f32 v32,0,v48
v_sub_f32 v33,0,v48
v_sub_f32 v34,0,v48
v_sub_f32 v35,0,v48
v_mov_b32 v11,v52
s_nop 1
v_permlane32_swap_b32 v52,v11
v_max_f32 v52,v52,v11
v_mov_b32 v11,v52
s_nop 1
v_permlane16_swap_b32 v52,v11
v_max_f32 v52,v52,v11
v_sub_f32 v36,0,v52
v_sub_f32 v37,0,v52
v_sub_f32 v38,0,v52
v_sub_f32 v39,0,v52
v_mov_b32 v11,v56
s_nop 1
v_permlane32_swap_b32 v56,v11
v_max_f32 v56,v56,v11
v_mov_b32 v11,v56
s_nop 1
v_permlane16_swap_b32 v56,v11
v_max_f32 v56,v56,v11
v_sub_f32 v40,0,v56
v_sub_f32 v41,0,v56
v_sub_f32 v42,0,v56
v_sub_f32 v43,0,v56
v_mov_b32 v11,v60
s_nop 1
v_permlane32_swap_b32 v60,v11
v_max_f32 v60,v60,v11
v_mov_b32 v11,v60
s_nop 1
v_permlane16_swap_b32 v60,v11
v_max_f32 v60,v60,v11
v_max_f32 v60,s72,v60
v_sub_f32 v44,0,v60
v_sub_f32 v45,0,v60
v_sub_f32 v46,0,v60
v_sub_f32 v47,0,v60
v_mov_b32 v48,0
v_mov_b32 v49,0
v_mov_b32 v50,0
v_mov_b32 v51,0
v_mov_b32 v52,0
v_mov_b32 v53,0
v_mov_b32 v54,0
v_mov_b32 v55,0
v_mov_b32 v56,0
v_mov_b32 v57,0
v_mov_b32 v58,0
v_mov_b32 v59,0
v_mov_b32 v60,0
v_mov_b32 v61,0
v_mov_b32 v62,0
v_mov_b32 v63,0
v_accvgpr_write_b32 a0,0
v_accvgpr_write_b32 a1,0
v_accvgpr_write_b32 a2,0
v_accvgpr_write_b32 a3,0
v_accvgpr_write_b32 a4,0
v_accvgpr_write_b32 a5,0
v_accvgpr_write_b32 a6,0
v_accvgpr_write_b32 a7,0
v_accvgpr_write_b32 a8,0
v_accvgpr_write_b32 a9,0
v_accvgpr_write_b32 a10,0
v_accvgpr_write_b32 a11,0
v_accvgpr_write_b32 a12,0
v_accvgpr_write_b32 a13,0
v_accvgpr_write_b32 a14,0
v_accvgpr_write_b32 a15,0
v_accvgpr_write_b32 a16,0
v_accvgpr_write_b32 a17,0
v_accvgpr_write_b32 a18,0
v_accvgpr_write_b32 a19,0
v_accvgpr_write_b32 a20,0
v_accvgpr_write_b32 a21,0
v_accvgpr_write_b32 a22,0
v_accvgpr_write_b32 a23,0
v_accvgpr_write_b32 a24,0
v_accvgpr_write_b32 a25,0
v_accvgpr_write_b32 a26,0
v_accvgpr_write_b32 a27,0
v_accvgpr_write_b32 a28,0
v_accvgpr_write_b32 a29,0
v_accvgpr_write_b32 a30,0
v_accvgpr_write_b32 a31,0
v_mov_b32 v132,0
v_mov_b32 v133,0
v_mov_b32 v134,0
v_mov_b32 v135,0
v_mov_b32 v140,0
v_mov_b32 v141,0
v_mov_b32 v142,0
v_mov_b32 v143,0
v_mov_b32 v148,0
v_mov_b32 v149,0
v_mov_b32 v150,0
v_mov_b32 v151,0
)A"
R"A(v_mov_b32 v156,0
v_mov_b32 v157,0
v_mov_b32 v158,0
v_mov_b32 v159,0
v_mov_b32 v164,0
v_mov_b32 v165,0
v_mov_b32 v166,0
v_mov_b32 v167,0
v_mov_b32 v172,0
v_mov_b32 v173,0
v_mov_b32 v174,0
v_mov_b32 v175,0
v_mov_b32 v180,0
v_mov_b32 v181,0
v_mov_b32 v182,0
v_mov_b32 v183,0
v_mov_b32 v188,0
v_mov_b32 v189,0
v_mov_b32 v190,0
v_mov_b32 v191,0
v_mov_b32 v196,0
v_mov_b32 v197,0
v_mov_b32 v198,0
v_mov_b32 v199,0
v_mov_b32 v204,0
v_mov_b32 v205,0
v_mov_b32 v206,0
v_mov_b32 v207,0
v_mov_b32 v212,0
v_mov_b32 v213,0
v_mov_b32 v214,0
v_mov_b32 v215,0
v_mov_b32 v220,0
v_mov_b32 v221,0
v_mov_b32 v222,0
v_mov_b32 v223,0
v_mov_b32 v228,0
v_mov_b32 v229,0
v_mov_b32 v230,0
v_mov_b32 v231,0
v_mov_b32 v236,0
v_mov_b32 v237,0
v_mov_b32 v238,0
v_mov_b32 v239,0
v_mov_b32 v244,0
v_mov_b32 v245,0
v_mov_b32 v246,0
v_mov_b32 v247,0
v_mov_b32 v252,0
v_mov_b32 v253,0
v_mov_b32 v254,0
v_mov_b32 v255,0
v_accvgpr_write_b32 a32,0
v_accvgpr_write_b32 a33,0
v_accvgpr_write_b32 a34,0
v_accvgpr_write_b32 a35,0
v_accvgpr_write_b32 a36,0
v_accvgpr_write_b32 a37,0
v_accvgpr_write_b32 a38,0
v_accvgpr_write_b32 a39,0
v_accvgpr_write_b32 a40,0
v_accvgpr_write_b32 a41,0
v_accvgpr_write_b32 a42,0
v_accvgpr_write_b32 a43,0
v_accvgpr_write_b32 a44,0
v_accvgpr_write_b32 a45,0
v_accvgpr_write_b32 a46,0
v_accvgpr_write_b32 a47,0
v_accvgpr_write_b32 a48,0
v_accvgpr_write_b32 a49,0
v_accvgpr_write_b32 a50,0
v_accvgpr_write_b32 a51,0
v_accvgpr_write_b32 a52,0
v_accvgpr_write_b32 a53,0
v_accvgpr_write_b32 a54,0
v_accvgpr_write_b32 a55,0
v_accvgpr_write_b32 a56,0
v_accvgpr_write_b32 a57,0
v_accvgpr_write_b32 a58,0
v_accvgpr_write_b32 a59,0
v_accvgpr_write_b32 a60,0
v_accvgpr_write_b32 a61,0
v_accvgpr_write_b32 a62,0
v_accvgpr_write_b32 a63,0
s_mov_b32 s65,262144
s_mov_b32 s66,32
L_s21_%=:
s_add_u32 s67,s65,4096
buffer_load_dwordx4 a[128:131],v8,s[20:23],s65 offen
buffer_load_dwordx4 a[132:135],v8,s[20:23],s65 offen offset:64
buffer_load_dwordx4 a[136:139],v8,s[20:23],s65 offen offset:128
buffer_load_dwordx4 a[140:143],v8,s[20:23],s65 offen offset:192
buffer_load_dwordx4 a[144:147],v8,s[20:23],s67 offen
buffer_load_dwordx4 a[148:151],v8,s[20:23],s67 offen offset:64
buffer_load_dwordx4 a[152:155],v8,s[20:23],s67 offen offset:128
buffer_load_dwordx4 a[156:159],v8,s[20:23],s67 offen offset:192
s_waitcnt vmcnt(0)
v_mfma_f32_16x16x32_bf16 v[64:67],a[128:131],v[128:131],v[32:35]
v_mfma_f32_16x16x32_bf16 v[64:67],a[132:135],v[136:139],v[64:67]
v_mfma_f32_16x16x32_bf16 v[64:67],a[136:139],v[144:147],v[64:67]
v_mfma_f32_16x16x32_bf16 v[64:67],a[140:143],v[152:155],v[64:67]
v_mfma_f32_16x16x32_bf16 v[68:71],a[144:147],v[128:131],v[32:35]
v_mfma_f32_16x16x32_bf16 v[68:71],a[148:151],v[136:139],v[68:71]
v_mfma_f32_16x16x32_bf16 v[68:71],a[152:155],v[144:147],v[68:71]
v_mfma_f32_16x16x32_bf16 v[68:71],a[156:159],v[152:155],v[68:71]
v_mfma_f32_16x16x32_bf16 v[80:83],a[128:131],v[160:163],v[36:39]
v_mfma_f32_16x16x32_bf16 v[80:83],a[132:135],v[168:171],v[80:83]
v_mfma_f32_16x16x32_bf16 v[80:83],a[136:139],v[176:179],v[80:83]
v_mfma_f32_16x16x32_bf16 v[80:83],a[140:143],v[184:187],v[80:83]
v_mfma_f32_16x16x32_bf16 v[84:87],a[144:147],v[160:163],v[36:39]
v_mfma_f32_16x16x32_bf16 v[84:87],a[148:151],v[168:171],v[84:87]
v_mfma_f32_16x16x32_bf16 v[84:87],a[152:155],v[176:179],v[84:87]
v_mfma_f32_16x16x32_bf16 v[84:87],a[156:159],v[184:187],v[84:87]
v_mfma_f32_16x16x32_bf16 v[96:99],a[128:131],v[192:195],v[40:43]
v_mfma_f32_16x16x32_bf16 v[96:99],a[132:135],v[200:203],v[96:99]
v_mfma_f32_16x16x32_bf16 v[96:99],a[136:139],v[208:211],v[96:99]
v_mfma_f32_16x16x32_bf16 v[96:99],a[140:143],v[216:219],v[96:99]
v_mfma_f32_16x16x32_bf16 v[100:103],a[144:147],v[192:195],v[40:43]
v_mfma_f32_16x16x32_bf16 v[100:103],a[148:151],v[200:203],v[100:103]
v_mfma_f32_16x16x32_bf16 v[100:103],a[152:155],v[208:211],v[100:103]
v_mfma_f32_16x16x32_bf16 v[100:103],a[156:159],v[216:219],v[100:103]
v_mfma_f32_16x16x32_bf16 v[112:115],a[128:131],v[224:227],v[44:47]
v_mfma_f32_16x16x32_bf16 v[112:115],a[132:135],v[232:235],v[112:115]
v_mfma_f32_16x16x32_bf16 v[112:115],a[136:139],v[240:243],v[112:115]
v_mfma_f32_16x16x32_bf16 v[112:115],a[140:143],v[248:251],v[112:115]
v_mfma_f32_16x16x32_bf16 v[116:119],a[144:147],v[224:227],v[44:47]
v_mfma_f32_16x16x32_bf16 v[116:119],a[148:151],v[232:235],v[116:119]
v_mfma_f32_16x16x32_bf16 v[116:119],a[152:155],v[240:243],v[116:119]
v_mfma_f32_16x16x32_bf16 v[116:119],a[156:159],v[248:251],v[116:119]
s_nop 7
v_exp_f32 v64,v64
v_exp_f32 v65,v65
v_exp_f32 v66,v66
v_exp_f32 v67,v67
v_exp_f32 v68,v68
v_exp_f32 v69,v69
v_exp_f32 v70,v70
v_exp_f32 v71,v71
s_nop 0
v_cvt_pk_bf16_f32 v64,v64,v65
v_cvt_pk_bf16_f32 v65,v66,v67
v_cvt_pk_bf16_f32 v66,v68,v69
v_cvt_pk_bf16_f32 v67,v70,v71
v_exp_f32 v80,v80
v_exp_f32 v81,v81
v_exp_f32 v82,v82
v_exp_f32 v83,v83
v_exp_f32 v84,v84
v_exp_f32 v85,v85
v_exp_f32 v86,v86
v_exp_f32 v87,v87
s_nop 0
v_cvt_pk_bf16_f32 v80,v80,v81
v_cvt_pk_bf16_f32 v81,v82,v83
v_cvt_pk_bf16_f32 v82,v84,v85
v_cvt_pk_bf16_f32 v83,v86,v87
v_exp_f32 v96,v96
v_exp_f32 v97,v97
v_exp_f32 v98,v98
v_exp_f32 v99,v99
v_exp_f32 v100,v100
v_exp_f32 v101,v101
v_exp_f32 v102,v102
v_exp_f32 v103,v103
s_nop 0
v_cvt_pk_bf16_f32 v96,v96,v97
v_cvt_pk_bf16_f32 v97,v98,v99
v_cvt_pk_bf16_f32 v98,v100,v101
v_cvt_pk_bf16_f32 v99,v102,v103
v_exp_f32 v112,v112
v_exp_f32 v113,v113
v_exp_f32 v114,v114
v_exp_f32 v115,v115
v_exp_f32 v116,v116
v_exp_f32 v117,v117
v_exp_f32 v118,v118
v_exp_f32 v119,v119
s_nop 0
v_cvt_pk_bf16_f32 v112,v112,v113
v_cvt_pk_bf16_f32 v113,v114,v115
v_cvt_pk_bf16_f32 v114,v116,v117
v_cvt_pk_bf16_f32 v115,v118,v119
buffer_load_ushort v72,v9,s[24:27],s65 offen
buffer_load_ushort v73,v9,s[24:27],s65 offen offset:256
buffer_load_ushort v74,v9,s[24:27],s65 offen offset:512
buffer_load_ushort v75,v9,s[24:27],s65 offen offset:768
buffer_load_ushort v76,v10,s[24:27],s65 offen
buffer_load_ushort v77,v10,s[24:27],s65 offen offset:256
buffer_load_ushort v78,v10,s[24:27],s65 offen offset:512
buffer_load_ushort v79,v10,s[24:27],s65 offen offset:768
s_waitcnt vmcnt(0)
v_lshl_or_b32 v88,v73,16,v72
v_lshl_or_b32 v89,v75,16,v74
v_lshl_or_b32 v90,v77,16,v76
v_lshl_or_b32 v91,v79,16,v78
s_nop 1
v_mfma_f32_16x16x32_bf16 a[0:3],v[88:91],v[64:67],a[0:3]
v_mfma_f32_16x16x32_bf16 v[132:135],v[88:91],v[80:83],v[132:135]
v_mfma_f32_16x16x32_bf16 v[196:199],v[88:91],v[96:99],v[196:199]
v_mfma_f32_16x16x32_bf16 a[32:35],v[88:91],v[112:115],a[32:35]
buffer_load_ushort v72,v9,s[24:27],s65 offen offset:32
buffer_load_ushort v73,v9,s[24:27],s65 offen offset:288
buffer_load_ushort v74,v9,s[24:27],s65 offen offset:544
buffer_load_ushort v75,v9,s[24:27],s65 offen offset:800
buffer_load_ushort v76,v10,s[24:27],s65 offen offset:32
buffer_load_ushort v77,v10,s[24:27],s65 offen offset:288
buffer_load_ushort v78,v10,s[24:27],s65 offen offset:544
buffer_load_ushort v79,v10,s[24:27],s65 offen offset:800
s_waitcnt vmcnt(0)
v_lshl_or_b32 v88,v73,16,v72
v_lshl_or_b32 v89,v75,16,v74
v_lshl_or_b32 v90,v77,16,v76
v_lshl_or_b32 v91,v79,16,v78
s_nop 1
v_mfma_f32_16x16x32_bf16 a[4:7],v[88:91],v[64:67],a[4:7]
v_mfma_f32_16x16x32_bf16 v[140:143],v[88:91],v[80:83],v[140:143]
v_mfma_f32_16x16x32_bf16 v[204:207],v[88:91],v[96:99],v[204:207]
v_mfma_f32_16x16x32_bf16 a[36:39],v[88:91],v[112:115],a[36:39]
buffer_load_ushort v72,v9,s[24:27],s65 offen offset:64
buffer_load_ushort v73,v9,s[24:27],s65 offen offset:320
buffer_load_ushort v74,v9,s[24:27],s65 offen offset:576
buffer_load_ushort v75,v9,s[24:27],s65 offen offset:832
buffer_load_ushort v76,v10,s[24:27],s65 offen offset:64
buffer_load_ushort v77,v10,s[24:27],s65 offen offset:320
buffer_load_ushort v78,v10,s[24:27],s65 offen offset:576
buffer_load_ushort v79,v10,s[24:27],s65 offen offset:832
s_waitcnt vmcnt(0)
v_lshl_or_b32 v88,v73,16,v72
v_lshl_or_b32 v89,v75,16,v74
v_lshl_or_b32 v90,v77,16,v76
v_lshl_or_b32 v91,v79,16,v78
s_nop 1
v_mfma_f32_16x16x32_bf16 a[8:11],v[88:91],v[64:67],a[8:11]
v_mfma_f32_16x16x32_bf16 v[148:151],v[88:91],v[80:83],v[148:151]
v_mfma_f32_16x16x32_bf16 v[212:215],v[88:91],v[96:99],v[212:215]
v_mfma_f32_16x16x32_bf16 a[40:43],v[88:91],v[112:115],a[40:43]
buffer_load_ushort v72,v9,s[24:27],s65 offen offset:96
buffer_load_ushort v73,v9,s[24:27],s65 offen offset:352
buffer_load_ushort v74,v9,s[24:27],s65 offen offset:608
buffer_load_ushort v75,v9,s[24:27],s65 offen offset:864
buffer_load_ushort v76,v10,s[24:27],s65 offen offset:96
buffer_load_ushort v77,v10,s[24:27],s65 offen offset:352
buffer_load_ushort v78,v10,s[24:27],s65 offen offset:608
buffer_load_ushort v79,v10,s[24:27],s65 offen offset:864
s_waitcnt vmcnt(0)
v_lshl_or_b32 v88,v73,16,v72
v_lshl_or_b32 v89,v75,16,v74
v_lshl_or_b32 v90,v77,16,v76
v_lshl_or_b32 v91,v79,16,v78
s_nop 1
v_mfma_f32_16x16x32_bf16 a[12:15],v[88:91],v[64:67],a[12:15]
v_mfma_f32_16x16x32_bf16 v[156:159],v[88:91],v[80:83],v[156:159]
v_mfma_f32_16x16x32_bf16 v[220:223],v[88:91],v[96:99],v[220:223]
v_mfma_f32_16x16x32_bf16 a[44:47],v[88:91],v[112:115],a[44:47]
buffer_load_ushort v72,v9,s[24:27],s65 offen offset:128
buffer_load_ushort v73,v9,s[24:27],s65 offen offset:384
buffer_load_ushort v74,v9,s[24:27],s65 offen offset:640
buffer_load_ushort v75,v9,s[24:27],s65 offen offset:896
buffer_load_ushort v76,v10,s[24:27],s65 offen offset:128
buffer_load_ushort v77,v10,s[24:27],s65 offen offset:384
buffer_load_ushort v78,v10,s[24:27],s65 offen offset:640
buffer_load_ushort v79,v10,s[24:27],s65 offen offset:896
s_waitcnt vmcnt(0)
v_lshl_or_b32 v88,v73,16,v72
v_lshl_or_b32 v89,v75,16,v74
v_lshl_or_b32 v90,v77,16,v76
v_lshl_or_b32 v91,v79,16,v78
s_nop 1
v_mfma_f32_16x16x32_bf16 a[16:19],v[88:91],v[64:67],a[16:19]
v_mfma_f32_16x16x32_bf16 v[164:167],v[88:91],v[80:83],v[164:167]
v_mfma_f32_16x16x32_bf16 v[228:231],v[88:91],v[96:99],v[228:231]
v_mfma_f32_16x16x32_bf16 a[48:51],v[88:91],v[112:115],a[48:51]
buffer_load_ushort v72,v9,s[24:27],s65 offen offset:160
buffer_load_ushort v73,v9,s[24:27],s65 offen offset:416
buffer_load_ushort v74,v9,s[24:27],s65 offen offset:672
buffer_load_ushort v75,v9,s[24:27],s65 offen offset:928
buffer_load_ushort v76,v10,s[24:27],s65 offen offset:160
buffer_load_ushort v77,v10,s[24:27],s65 offen offset:416
buffer_load_ushort v78,v10,s[24:27],s65 offen offset:672
buffer_load_ushort v79,v10,s[24:27],s65 offen offset:928
s_waitcnt vmcnt(0)
v_lshl_or_b32 v88,v73,16,v72
v_lshl_or_b32 v89,v75,16,v74
v_lshl_or_b32 v90,v77,16,v76
v_lshl_or_b32 v91,v79,16,v78
s_nop 1
v_mfma_f32_16x16x32_bf16 a[20:23],v[88:91],v[64:67],a[20:23]
v_mfma_f32_16x16x32_bf16 v[172:175],v[88:91],v[80:83],v[172:175]
v_mfma_f32_16x16x32_bf16 v[236:239],v[88:91],v[96:99],v[236:239]
v_mfma_f32_16x16x32_bf16 a[52:55],v[88:91],v[112:115],a[52:55]
buffer_load_ushort v72,v9,s[24:27],s65 offen offset:192
buffer_load_ushort v73,v9,s[24:27],s65 offen offset:448
buffer_load_ushort v74,v9,s[24:27],s65 offen offset:704
buffer_load_ushort v75,v9,s[24:27],s65 offen offset:960
buffer_load_ushort v76,v10,s[24:27],s65 offen offset:192
buffer_load_ushort v77,v10,s[24:27],s65 offen offset:448
buffer_load_ushort v78,v10,s[24:27],s65 offen offset:704
buffer_load_ushort v79,v10,s[24:27],s65 offen offset:960
s_waitcnt vmcnt(0)
v_lshl_or_b32 v88,v73,16,v72
v_lshl_or_b32 v89,v75,16,v74
v_lshl_or_b32 v90,v77,16,v76
v_lshl_or_b32 v91,v79,16,v78
s_nop 1
v_mfma_f32_16x16x32_bf16 a[24:27],v[88:91],v[64:67],a[24:27]
v_mfma_f32_16x16x32_bf16 v[180:183],v[88:91],v[80:83],v[180:183]
v_mfma_f32_16x16x32_bf16 v[244:247],v[88:91],v[96:99],v[244:247]
v_mfma_f32_16x16x32_bf16 a[56:59],v[88:91],v[112:115],a[56:59]
buffer_load_ushort v72,v9,s[24:27],s65 offen offset:224
buffer_load_ushort v73,v9,s[24:27],s65 offen offset:480
buffer_load_ushort v74,v9,s[24:27],s65 offen offset:736
buffer_load_ushort v75,v9,s[24:27],s65 offen offset:992
buffer_load_ushort v76,v10,s[24:27],s65 offen offset:224
buffer_load_ushort v77,v10,s[24:27],s65 offen offset:480
buffer_load_ushort v78,v10,s[24:27],s65 offen offset:736
buffer_load_ushort v79,v10,s[24:27],s65 offen offset:992
s_waitcnt vmcnt(0)
v_lshl_or_b32 v88,v73,16,v72
v_lshl_or_b32 v89,v75,16,v74
v_lshl_or_b32 v90,v77,16,v76
)A"
R"A(v_lshl_or_b32 v91,v79,16,v78
s_nop 1
v_mfma_f32_16x16x32_bf16 a[28:31],v[88:91],v[64:67],a[28:31]
v_mfma_f32_16x16x32_bf16 v[188:191],v[88:91],v[80:83],v[188:191]
v_mfma_f32_16x16x32_bf16 v[252:255],v[88:91],v[96:99],v[252:255]
v_mfma_f32_16x16x32_bf16 a[60:63],v[88:91],v[112:115],a[60:63]
v_mfma_f32_16x16x32_bf16 v[48:51],v[28:31],v[64:67],v[48:51]
v_mfma_f32_16x16x32_bf16 v[52:55],v[28:31],v[80:83],v[52:55]
v_mfma_f32_16x16x32_bf16 v[56:59],v[28:31],v[96:99],v[56:59]
v_mfma_f32_16x16x32_bf16 v[60:63],v[28:31],v[112:115],v[60:63]
s_add_u32 s65,s65,8192
s_sub_u32 s66,s66,1
s_cmp_lg_u32 s66,0
s_cbranch_scc1 L_s21_%=
s_nop 7
v_max_f32 v49,0x00800000,v48
v_rcp_f32 v49,v49
v_max_f32 v53,0x00800000,v52
v_rcp_f32 v53,v53
v_max_f32 v57,0x00800000,v56
v_rcp_f32 v57,v57
v_max_f32 v61,0x00800000,v60
v_rcp_f32 v61,v61
v_accvgpr_read_b32 v16,a0
v_accvgpr_read_b32 v17,a1
v_accvgpr_read_b32 v18,a2
v_accvgpr_read_b32 v19,a3
v_accvgpr_read_b32 v20,a4
v_accvgpr_read_b32 v21,a5
v_accvgpr_read_b32 v22,a6
v_accvgpr_read_b32 v23,a7
v_mul_f32 v16,v16,v49
v_mul_f32 v17,v17,v49
v_mul_f32 v18,v18,v49
v_mul_f32 v19,v19,v49
v_mul_f32 v20,v20,v49
v_mul_f32 v21,v21,v49
v_mul_f32 v22,v22,v49
v_mul_f32 v23,v23,v49
v_cvt_pk_bf16_f32 v32,v16,v17
v_cvt_pk_bf16_f32 v33,v18,v19
v_cvt_pk_bf16_f32 v34,v20,v21
v_cvt_pk_bf16_f32 v35,v22,v23
v_accvgpr_read_b32 v8,a8
v_accvgpr_read_b32 v9,a9
v_accvgpr_read_b32 v10,a10
v_accvgpr_read_b32 v11,a11
v_accvgpr_read_b32 v12,a12
v_accvgpr_read_b32 v13,a13
v_accvgpr_read_b32 v14,a14
v_accvgpr_read_b32 v15,a15
v_mul_f32 v8,v8,v49
v_mul_f32 v9,v9,v49
v_mul_f32 v10,v10,v49
v_mul_f32 v11,v11,v49
v_mul_f32 v12,v12,v49
v_mul_f32 v13,v13,v49
v_mul_f32 v14,v14,v49
v_mul_f32 v15,v15,v49
v_cvt_pk_bf16_f32 v36,v8,v9
v_cvt_pk_bf16_f32 v37,v10,v11
v_cvt_pk_bf16_f32 v38,v12,v13
v_cvt_pk_bf16_f32 v39,v14,v15
v_accvgpr_read_b32 v16,a16
v_accvgpr_read_b32 v17,a17
v_accvgpr_read_b32 v18,a18
v_accvgpr_read_b32 v19,a19
v_accvgpr_read_b32 v20,a20
v_accvgpr_read_b32 v21,a21
v_accvgpr_read_b32 v22,a22
v_accvgpr_read_b32 v23,a23
v_mul_f32 v16,v16,v49
v_mul_f32 v17,v17,v49
v_mul_f32 v18,v18,v49
v_mul_f32 v19,v19,v49
v_mul_f32 v20,v20,v49
v_mul_f32 v21,v21,v49
v_mul_f32 v22,v22,v49
v_mul_f32 v23,v23,v49
v_cvt_pk_bf16_f32 v40,v16,v17
v_cvt_pk_bf16_f32 v41,v18,v19
v_cvt_pk_bf16_f32 v42,v20,v21
v_cvt_pk_bf16_f32 v43,v22,v23
ds_read_b64_tr_b16 v[64:65],v1
ds_read_b64_tr_b16 v[66:67],v1 offset:5120
ds_read_b64_tr_b16 v[68:69],v1 offset:10240
ds_read_b64_tr_b16 v[70:71],v1 offset:15360
ds_read_b64_tr_b16 v[72:73],v1 offset:20480
ds_read_b64_tr_b16 v[74:75],v1 offset:25600
ds_read_b64_tr_b16 v[76:77],v1 offset:30720
ds_read_b64_tr_b16 v[78:79],v1 offset:35840
ds_read_b64_tr_b16 v[80:81],v2 offset:32
ds_read_b64_tr_b16 v[82:83],v2 offset:5152
ds_read_b64_tr_b16 v[84:85],v2 offset:10272
ds_read_b64_tr_b16 v[86:87],v2 offset:15392
ds_read_b64_tr_b16 v[88:89],v2 offset:20512
ds_read_b64_tr_b16 v[90:91],v2 offset:25632
ds_read_b64_tr_b16 v[92:93],v2 offset:30752
ds_read_b64_tr_b16 v[94:95],v2 offset:35872
s_waitcnt vmcnt(0) lgkmcnt(0)
s_nop 7
s_branch L_ok1_%=
L_end_%=:
)A"
    :: "s"(ka), "s"(blk), "s"(wid)
    : "v0", "v1", "v2", "v3", "v4", "v5", "v6", "v7", "v8", "v9", "v10", "v11", "v12", "v13", "v14", "v15", "v16", "v17", "v18", "v19", "v20", "v21", "v22", "v23", "v24", "v25", "v26", "v27", "v28", "v29", "v30", "v31", "v32", "v33", "v34", "v35", "v36", "v37", "v38", "v39", "v40", "v41", "v42", "v43", "v44", "v45", "v46", "v47", "v48", "v49", "v50", "v51", "v52", "v53", "v54", "v55", "v56", "v57", "v58", "v59", "v60", "v61", "v62", "v63", "v64", "v65", "v66", "v67", "v68", "v69", "v70", "v71", "v72", "v73", "v74", "v75", "v76", "v77", "v78", "v79", "v80", "v81", "v82", "v83", "v84", "v85", "v86", "v87", "v88", "v89", "v90", "v91", "v92", "v93", "v94", "v95", "v96", "v97", "v98", "v99", "v100", "v101", "v102", "v103", "v104", "v105", "v106", "v107", "v108", "v109", "v110", "v111", "v112", "v113", "v114", "v115", "v116", "v117", "v118", "v119", "v120", "v121", "v122", "v123", "v124", "v125", "v126", "v127", "v128", "v129", "v130", "v131", "v132", "v133", "v134", "v135", "v136", "v137", "v138", "v139", "v140", "v141", "v142", "v143", "v144", "v145", "v146", "v147", "v148", "v149", "v150", "v151", "v152", "v153", "v154", "v155", "v156", "v157", "v158", "v159", "v160", "v161", "v162", "v163", "v164", "v165", "v166", "v167", "v168", "v169", "v170", "v171", "v172", "v173", "v174", "v175", "v176", "v177", "v178", "v179", "v180", "v181", "v182", "v183", "v184", "v185", "v186", "v187", "v188", "v189", "v190", "v191", "v192", "v193", "v194", "v195", "v196", "v197", "v198", "v199", "v200", "v201", "v202", "v203", "v204", "v205", "v206", "v207", "v208", "v209", "v210", "v211", "v212", "v213", "v214", "v215", "v216", "v217", "v218", "v219", "v220", "v221", "v222", "v223", "v224", "v225", "v226", "v227", "v228", "v229", "v230", "v231", "v232", "v233", "v234", "v235", "v236", "v237", "v238", "v239", "v240", "v241", "v242", "v243", "v244", "v245", "v246", "v247", "v248", "v249", "v250", "v251", "v252", "v253", "v254", "v255", "a0", "a1", "a2", "a3", "a4", "a5", "a6", "a7", "a8", "a9", "a10", "a11", "a12", "a13", "a14", "a15", "a16", "a17", "a18", "a19", "a20", "a21", "a22", "a23", "a24", "a25", "a26", "a27", "a28", "a29", "a30", "a31", "a32", "a33", "a34", "a35", "a36", "a37", "a38", "a39", "a40", "a41", "a42", "a43", "a44", "a45", "a46", "a47", "a48", "a49", "a50", "a51", "a52", "a53", "a54", "a55", "a56", "a57", "a58", "a59", "a60", "a61", "a62", "a63", "a64", "a65", "a66", "a67", "a68", "a69", "a70", "a71", "a72", "a73", "a74", "a75", "a76", "a77", "a78", "a79", "a80", "a81", "a82", "a83", "a84", "a85", "a86", "a87", "a88", "a89", "a90", "a91", "a92", "a93", "a94", "a95", "a96", "a97", "a98", "a99", "a100", "a101", "a102", "a103", "a104", "a105", "a106", "a107", "a108", "a109", "a110", "a111", "a112", "a113", "a114", "a115", "a116", "a117", "a118", "a119", "a120", "a121", "a122", "a123", "a124", "a125", "a126", "a127", "a128", "a129", "a130", "a131", "a132", "a133", "a134", "a135", "a136", "a137", "a138", "a139", "a140", "a141", "a142", "a143", "a144", "a145", "a146", "a147", "a148", "a149", "a150", "a151", "a152", "a153", "a154", "a155", "a156", "a157", "a158", "a159", "a160", "a161", "a162", "a163", "a164", "a165", "a166", "a167", "a168", "a169", "a170", "a171", "a172", "a173", "a174", "a175", "a176", "a177", "a178", "a179", "a180", "a181", "a182", "a183", "a184", "a185", "a186", "a187", "a188", "a189", "a190", "a191", "a192", "a193", "a194", "a195", "a196", "a197", "a198", "a199", "a200", "a201", "a202", "a203", "a204", "a205", "a206", "a207", "a208", "a209", "a210", "a211", "a212", "a213", "a214", "a215", "a216", "a217", "a218", "a219", "a220", "a221", "a222", "a223", "a224", "a225", "a226", "a227", "a228", "a229", "a230", "a231", "a232", "a233", "a234", "a235", "a236", "a237", "a238", "a239", "a240", "a241", "a242", "a243", "a244", "a245", "a246", "a247", "a248", "a249", "a250", "a251", "a252", "a253", "a254", "a255", "s4", "s5", "s6", "s7", "s8", "s9", "s10", "s11", "s12", "s13", "s14", "s15", "s16", "s17", "s18", "s19", "s20", "s21", "s22", "s23", "s24", "s25", "s26", "s27", "s28", "s29", "s30", "s31", "s36", "s37", "s38", "s39", "s40", "s41", "s42", "s43", "s44", "s45", "s46", "s47", "s48", "s49", "s50", "s51", "s52", "s53", "s54", "s55", "s56", "s57", "s58", "s59", "s60", "s61", "s62", "s63", "s64", "s65", "s66", "s67", "s68", "s69", "s70", "s71", "s72", "s73", "s74", "s75", "s76", "s77", "s78", "s79", "s80", "s81", "s82", "s83", "s84", "s85", "s86", "s87", "s88", "s89", "s90", "s91", "s92", "s93", "s94", "s95", "s96", "s97", "s98", "s99", "vcc", "scc", "memory");
}

extern "C" void kernel_launch(void* const* d_in, const int* in_sizes, int n_in,
                              void* d_out, int out_size, void* d_ws, size_t ws_size,
                              hipStream_t stream) {
  (void)hipFuncSetAttribute((const void*)qattn_kernel, hipFuncAttributeMaxDynamicSharedMemorySize, LDS_ATT);
  qattn_kernel<<<256, 256, LDS_ATT, stream>>>(d_in[0], d_in[1], d_in[2], d_in[3], d_ws, d_in[4], d_out);
}
```

```cpp
#include <hip/hip_runtime.h>
#include <hip/hip_bf16.h>

constexpr int LDS_ATT = 163840;

__global__ __launch_bounds__(256, 1) __attribute__((aligned(4096))) void qattn_kernel(const void* __restrict__ x, const void* __restrict__ kc, const void* __restrict__ vc,
                                                       const void* __restrict__ wq, void* __restrict__ ws, const void* __restrict__ wo,
                                                       void* __restrict__ out) {
  const unsigned blk = blockIdx.x;
  const unsigned wid = __builtin_amdgcn_readfirstlane(threadIdx.x >> 6);
  auto ka = __builtin_amdgcn_kernarg_segment_ptr();
  asm volatile(
R"A(s_load_dwordx8 s[4:11],%0,0x0
s_load_dwordx4 s[12:15],%0,0x20
s_load_dwordx2 s[16:17],%0,0x30
v_mbcnt_lo_u32_b32 v23,-1,0
v_mbcnt_hi_u32_b32 v23,-1,v23
s_mov_b32 s60,%2
s_and_b32 s66,%1,7
s_lshr_b32 s67,%1,3
s_getpc_b64 s[20:21]
L_pc_%=:
s_and_b32 s20,s20,0xffffff80
s_and_b32 s21,s21,0xffff
s_mov_b32 s22,L_hot_%=-L_pc_%=
s_mov_b32 s23,0x20000
s_lshl_b32 s68,s67,2
s_add_u32 s68,s68,s60
s_mul_i32 s68,s68,896
v_lshlrev_b32 v67,7,v23
s_mov_b64 exec,127
buffer_load_dword v8,v67,s[20:23],s68 offen
s_mov_b64 exec,-1
s_lshr_b32 s61,s66,2
s_lshl_b32 s61,s61,3
s_lshr_b32 s68,s67,2
s_add_u32 s61,s61,s68
s_and_b32 s62,s66,1
s_lshl_b32 s62,s62,2
s_and_b32 s68,s66,2
s_add_u32 s62,s62,s68
s_lshl_b32 s62,s62,1
s_and_b32 s68,s67,3
s_add_u32 s62,s62,s68
s_lshl_b32 s83,s62,9
s_mov_b32 s84,0
s_lshl_b32 s89,s60,13
s_add_u32 s85,s89,65536
s_add_u32 s88,s89,131072
v_and_b32 v20,3,v23
v_bfe_u32 v21,v23,2,2
v_lshrrev_b32 v22,4,v23
v_lshrrev_b32 v75,3,v23
s_lshl_b32 s66,s60,6
v_add_u32 v75,s66,v75
v_mul_u32_u24 v75,1280,v75
v_and_b32 v76,7,v23
v_or_b32 v77,0,v22
v_xor_b32 v77,v77,v76
v_lshl_add_u32 v77,v77,4,v75
v_add_u32 v32,0,v77
v_add_u32 v34,18432,v77
v_add_u32 v36,40960,v77
v_add_u32 v38,59392,v77
v_or_b32 v77,4,v22
v_xor_b32 v77,v77,v76
v_lshl_add_u32 v77,v77,4,v75
v_add_u32 v33,9216,v77
v_add_u32 v35,27648,v77
v_add_u32 v37,50176,v77
v_add_u32 v39,68608,v77
s_lshl_b32 s66,s60,17
v_lshlrev_b32 v75,13,v21
v_lshl_add_u32 v75,v22,6,v75
v_lshl_add_u32 v75,v20,4,v75
v_add_u32 v75,s66,v75
v_add_u32 v40,0,v75
v_add_u32 v41,256,v75
v_add_u32 v42,32768,v75
v_add_u32 v43,33024,v75
v_add_u32 v44,65536,v75
v_add_u32 v45,65792,v75
v_add_u32 v46,98304,v75
v_add_u32 v47,98560,v75
s_mul_i32 s66,s61,307200
s_waitcnt lgkmcnt(0)
s_add_u32 s28,s4,s66
s_addc_u32 s29,s5,0
s_and_b32 s29,s29,0xffff
s_mov_b32 s30,307200
s_mov_b32 s31,0x20000
s_mov_b32 s36,s10
s_and_b32 s37,s11,0xffff
s_mov_b32 s38,5242880
s_mov_b32 s39,0x20000
s_add_u32 m0,s85,0
s_nop 0
buffer_load_dwordx4 v32,s[28:31],s84 offen lds
buffer_load_dwordx4 v33,s[28:31],s84 offen offset:1024 lds
buffer_load_dwordx4 v34,s[28:31],s84 offen offset:2048 lds
buffer_load_dwordx4 v35,s[28:31],s84 offen offset:3072 lds
s_add_u32 m0,s85,4096
s_nop 0
buffer_load_dwordx4 v36,s[28:31],s84 offen lds
buffer_load_dwordx4 v37,s[28:31],s84 offen offset:1024 lds
buffer_load_dwordx4 v38,s[28:31],s84 offen offset:2048 lds
buffer_load_dwordx4 v39,s[28:31],s84 offen offset:3072 lds
s_add_u32 s84,s84,128
s_xor_b32 s85,s85,98304
s_add_u32 m0,s88,0
s_nop 0
buffer_load_dwordx4 v40,s[36:39],s83 offen lds
s_add_u32 m0,s88,1024
s_nop 0
buffer_load_dwordx4 v41,s[36:39],s83 offen lds
s_add_u32 m0,s88,2048
s_nop 0
buffer_load_dwordx4 v42,s[36:39],s83 offen lds
s_add_u32 m0,s88,3072
s_nop 0
buffer_load_dwordx4 v43,s[36:39],s83 offen lds
s_add_u32 m0,s88,4096
s_nop 0
buffer_load_dwordx4 v44,s[36:39],s83 offen lds
s_add_u32 m0,s88,5120
s_nop 0
buffer_load_dwordx4 v45,s[36:39],s83 offen lds
s_add_u32 m0,s88,6144
s_nop 0
buffer_load_dwordx4 v46,s[36:39],s83 offen lds
s_add_u32 m0,s88,7168
s_nop 0
buffer_load_dwordx4 v47,s[36:39],s83 offen lds
s_add_u32 s83,s83,524288
s_lshl_b32 s67,s61,21
s_add_u32 s68,s67,101711872
s_add_u32 s44,s12,s68
s_addc_u32 s45,s13,0
s_and_b32 s45,s45,0xffff
s_mov_b32 s46,4096
s_mov_b32 s47,0x20000
v_mov_b32 v10,0
buffer_load_dword v9,v10,s[44:47],0 offen sc1
v_lshlrev_b32 v6,4,v23
v_lshlrev_b32 v72,12,v22
v_lshl_add_u32 v72,v21,6,v72
v_lshl_add_u32 v72,v20,4,v72
v_subrev_u32 v51,s89,v72
v_add_u32 v50,131072,v72
v_and_b32 v74,15,v23
v_lshrrev_b32 v76,1,v74
v_lshlrev_b32 v75,7,v74
s_add_u32 s66,s89,65536
v_add_u32 v75,s66,v75
v_or_b32 v77,0,v22
v_xor_b32 v77,v77,v76
v_lshl_add_u32 v48,v77,4,v75
v_or_b32 v77,4,v22
v_xor_b32 v77,v77,v76
v_lshl_add_u32 v49,v77,4,v75
s_add_u32 s86,s89,131072
s_add_u32 s87,s89,98304
s_mov_b64 exec,0
s_cmp_eq_u32 s60,0
s_cbranch_scc1 L_pd1_%=
s_cmp_eq_u32 s60,1
s_cbranch_scc1 L_pd2_%=
s_cmp_eq_u32 s60,2
s_cbranch_scc1 L_pd3_%=
s_branch L_pd4_%=
L_trt0_%=:
s_mov_b64 exec,-1
s_mov_b32 s88,s87
s_add_u32 m0,s88,0
s_nop 0
buffer_load_dwordx4 v40,s[36:39],s83 offen lds
s_add_u32 m0,s88,1024
s_nop 0
buffer_load_dwordx4 v41,s[36:39],s83 offen lds
s_add_u32 m0,s88,2048
s_nop 0
buffer_load_dwordx4 v42,s[36:39],s83 offen lds
s_add_u32 m0,s88,3072
s_nop 0
buffer_load_dwordx4 v43,s[36:39],s83 offen lds
s_add_u32 m0,s88,4096
s_nop 0
buffer_load_dwordx4 v44,s[36:39],s83 offen lds
s_add_u32 m0,s88,5120
s_nop 0
buffer_load_dwordx4 v45,s[36:39],s83 offen lds
s_add_u32 m0,s88,6144
s_nop 0
buffer_load_dwordx4 v46,s[36:39],s83 offen lds
s_add_u32 m0,s88,7168
s_nop 0
buffer_load_dwordx4 v47,s[36:39],s83 offen lds
s_add_u32 s83,s83,524288
s_add_u32 s88,s89,0
s_mov_b64 exec,0
s_cmp_eq_u32 s60,0
s_cbranch_scc1 L_pd9_%=
s_cmp_eq_u32 s60,1
s_cbranch_scc1 L_pd10_%=
s_cmp_eq_u32 s60,2
s_cbranch_scc1 L_pd11_%=
s_branch L_pd12_%=
L_t1b_%=:
s_cmp_ge_u32 s62,8
s_cbranch_scc1 L_pd19_%=
s_branch L_pd14_%=
L_t1c_%=:
s_cmp_ge_u32 s62,8
s_cbranch_scc1 L_pd20_%=
s_branch L_pd15_%=
s_nop 0
s_nop 0
s_nop 0
s_nop 0
s_nop 0
s_nop 0
L_trt1_%=:
s_mov_b64 exec,-1
s_branch L_pacx_%=
L_pace_%=:
s_sub_u32 s5,s4,1
s_max_i32 s5,s5,s7
L_pacl_%=:
s_sub_u32 s4,s4,1
s_cmp_eq_u32 s4,0
s_cbranch_scc1 L_pace2_%=
s_nop 2
s_branch L_pacl_%=
L_pace2_%=:
s_setpc_b64 s[92:93]
L_pacx_%=:
s_waitcnt vmcnt(17)
ds_read_b128 a[192:195],v48
ds_read_b128 a[196:199],v49
ds_read_b128 a[200:203],v48 offset:2048
ds_read_b128 a[204:207],v49 offset:2048
ds_read_b128 a[208:211],v48 offset:4096
ds_read_b128 a[212:215],v49 offset:4096
ds_read_b128 a[216:219],v48 offset:6144
ds_read_b128 a[220:223],v49 offset:6144
s_waitcnt vmcnt(9)
s_barrier
ds_read_b64_tr_b16 a[64:65],v50
ds_read_b64_tr_b16 a[66:67],v50 offset:2048
ds_read_b64_tr_b16 a[68:69],v50 offset:16384
ds_read_b64_tr_b16 a[70:71],v50 offset:18432
ds_read_b64_tr_b16 a[72:73],v50 offset:8
ds_read_b64_tr_b16 a[74:75],v50 offset:2056
ds_read_b64_tr_b16 a[76:77],v50 offset:16392
ds_read_b64_tr_b16 a[78:79],v50 offset:18440
s_waitcnt lgkmcnt(6)
v_mfma_f32_16x16x32_bf16 a[0:3],a[64:67],a[192:195],0
s_add_u32 m0,s85,0
ds_read_b64_tr_b16 a[80:81],v50 offset:256
v_xor_b32 v48,98304,v48
s_waitcnt lgkmcnt(5)
v_mfma_f32_16x16x32_bf16 a[0:3],a[68:71],a[196:199],a[0:3]
buffer_load_dwordx4 v32,s[28:31],s84 offen lds
v_mfma_f32_16x16x32_bf16 a[16:19],a[68:71],a[204:207],0
s_mov_b32 s69,s86
ds_read_b64_tr_b16 a[82:83],v50 offset:2304
v_xor_b32 v49,98304,v49
v_mfma_f32_16x16x32_bf16 a[16:19],a[64:67],a[200:203],a[16:19]
s_mov_b32 s86,s87
s_mov_b32 s87,s88
ds_read_b64_tr_b16 a[84:85],v50 offset:16640
v_mfma_f32_16x16x32_bf16 a[32:35],a[64:67],a[208:211],0
ds_read_b64_tr_b16 a[86:87],v50 offset:18688
v_mfma_f32_16x16x32_bf16 a[32:35],a[68:71],a[212:215],a[32:35]
ds_read_b64_tr_b16 a[88:89],v50 offset:264
v_mfma_f32_16x16x32_bf16 a[48:51],a[68:71],a[220:223],0
buffer_load_dwordx4 v33,s[28:31],s84 offen offset:1024 lds
v_mfma_f32_16x16x32_bf16 a[48:51],a[64:67],a[216:219],a[48:51]
s_waitcnt lgkmcnt(5)
ds_read_b64_tr_b16 a[90:91],v50 offset:2312
v_mfma_f32_16x16x32_bf16 a[4:7],a[72:75],a[192:195],0
s_lshl_b32 s67,s60,12
ds_read_b64_tr_b16 a[92:93],v50 offset:16648
v_cmp_lt_u32 vcc,6,v23
v_mfma_f32_16x16x32_bf16 a[4:7],a[76:79],a[196:199],a[4:7]
s_lshl_b32 s65,s61,5
ds_read_b64_tr_b16 a[94:95],v50 offset:18696
v_lshlrev_b32 v19,3,v6
v_mfma_f32_16x16x32_bf16 a[20:23],a[76:79],a[204:207],0
s_lshl_b32 s66,s62,1
v_cndmask_b32_e64 v19,v19,-1,vcc
v_lshrrev_b32 v18,1,v19
v_mfma_f32_16x16x32_bf16 a[20:23],a[72:75],a[200:203],a[20:23]
buffer_load_dwordx4 v34,s[28:31],s84 offen offset:2048 lds
v_mfma_f32_16x16x32_bf16 a[36:39],a[72:75],a[208:211],0
s_add_u32 s65,s65,s66
ds_read_b64_tr_b16 a[96:97],v50 offset:512
v_min_u32 v19,v19,v19
v_mfma_f32_16x16x32_bf16 a[36:39],a[76:79],a[212:215],a[36:39]
s_lshl_b32 s66,s65,18
ds_read_b64_tr_b16 a[98:99],v50 offset:2560
v_cndmask_b32_e64 v19,v19,v18,vcc
v_mfma_f32_16x16x32_bf16 a[52:55],a[76:79],a[220:223],0
s_lshr_b32 s63,s62,2
ds_read_b64_tr_b16 a[100:101],v50 offset:16896
v_lshlrev_b32 v11,8,v21
v_mfma_f32_16x16x32_bf16 a[52:55],a[72:75],a[216:219],a[52:55]
s_mov_b32 s22,524288
ds_read_b64_tr_b16 a[102:103],v50 offset:18944
v_lshl_add_u32 v11,v22,6,v11
s_waitcnt lgkmcnt(6)
v_mfma_f32_16x16x32_bf16 a[8:11],a[80:83],a[192:195],0
buffer_load_dwordx4 v35,s[28:31],s84 offen offset:3072 lds
v_mfma_f32_16x16x32_bf16 a[8:11],a[84:87],a[196:199],a[8:11]
s_mov_b32 s23,0x20000
ds_read_b64_tr_b16 a[104:105],v50 offset:520
v_add_u32 v11,s67,v11
v_mfma_f32_16x16x32_bf16 a[24:27],a[84:87],a[204:207],0
s_mov_b32 s26,524288
ds_read_b64_tr_b16 a[106:107],v50 offset:2568
v_lshl_add_u32 v4,v20,4,v11
v_mfma_f32_16x16x32_bf16 a[24:27],a[80:83],a[200:203],a[24:27]
s_add_u32 m0,s85,4096
s_xor_b32 s85,s85,98304
ds_read_b64_tr_b16 a[108:109],v50 offset:16904
v_mfma_f32_16x16x32_bf16 a[40:43],a[80:83],a[208:211],0
s_mov_b32 s27,0x20000
v_xor_b32 v12,2,v20
v_lshl_add_u32 v5,v12,4,v11
v_mfma_f32_16x16x32_bf16 a[40:43],a[84:87],a[212:215],a[40:43]
buffer_load_dwordx4 v36,s[28:31],s84 offen lds
v_mfma_f32_16x16x32_bf16 a[56:59],a[84:87],a[220:223],0
s_mov_b32 s42,131072
ds_read_b64_tr_b16 a[110:111],v50 offset:18952
v_lshlrev_b32 v11,10,v21
v_mfma_f32_16x16x32_bf16 a[56:59],a[80:83],a[216:219],a[56:59]
s_mov_b32 s43,0x20000
ds_read_b64_tr_b16 a[112:113],v50 offset:768
v_lshl_add_u32 v11,v20,6,v11
v_mfma_f32_16x16x32_bf16 a[12:15],a[88:91],a[192:195],0
s_mov_b32 s48,s14
ds_read_b64_tr_b16 a[114:115],v50 offset:2816
v_and_b32 v12,2,v21
s_waitcnt lgkmcnt(6)
v_mfma_f32_16x16x32_bf16 a[12:15],a[92:95],a[196:199],a[12:15]
s_and_b32 s49,s15,0xffff
ds_read_b64_tr_b16 a[116:117],v50 offset:17152
v_xor_b32 v12,v12,v22
v_mfma_f32_16x16x32_bf16 a[28:31],a[92:95],a[204:207],0
buffer_load_dwordx4 v37,s[28:31],s84 offen offset:1024 lds
v_mfma_f32_16x16x32_bf16 a[28:31],a[88:91],a[200:203],a[28:31]
s_mov_b32 s50,5242880
ds_read_b64_tr_b16 a[118:119],v50 offset:19200
v_lshl_add_u32 v11,v12,4,v11
v_mfma_f32_16x16x32_bf16 a[44:47],a[88:91],a[208:211],0
s_mov_b32 s51,0x20000
ds_read_b64_tr_b16 a[120:121],v50 offset:776
v_add_u32 v1,98304,v11
v_mfma_f32_16x16x32_bf16 a[44:47],a[92:95],a[212:215],a[44:47]
s_add_u32 s76,s67,98304
ds_read_b64_tr_b16 a[122:123],v50 offset:2824
v_lshlrev_b32 v11,10,v22
v_mfma_f32_16x16x32_bf16 a[60:63],a[92:95],a[220:223],0
s_mul_i32 s74,s61,5
v_lshl_add_u32 v11,v21,6,v11
v_lshl_add_u32 v11,v20,3,v11
v_mfma_f32_16x16x32_bf16 a[60:63],a[88:91],a[216:219],a[60:63]
buffer_load_dwordx4 v38,s[28:31],s84 offen offset:2048 lds
v_mfma_f32_16x16x32_bf16 v[64:67],a[96:99],a[192:195],0
s_add_u32 s74,s74,s62
ds_read_b64_tr_b16 a[124:125],v50 offset:17160
v_add_u32 v11,114688,v11
v_mfma_f32_16x16x32_bf16 v[64:67],a[100:103],a[196:199],v[64:67]
s_and_b32 s74,s74,15
ds_read_b64_tr_b16 a[126:127],v50 offset:19208
v_and_b32 v12,1,v22
v_mfma_f32_16x16x32_bf16 v[80:83],a[100:103],a[204:207],0
s_lshl_b32 s74,s74,14
ds_read_b64_tr_b16 a[64:65],v50 offset:1024
v_lshl_add_u32 v2,v12,5,v11
v_mfma_f32_16x16x32_bf16 v[80:83],a[96:99],a[200:203],v[80:83]
s_mov_b32 s75,0
ds_read_b64_tr_b16 a[66:67],v50 offset:3072
v_xor_b32 v12,1,v12
v_mfma_f32_16x16x32_bf16 v[96:99],a[96:99],a[208:211],0
buffer_load_dwordx4 v39,s[28:31],s84 offen offset:3072 lds
v_mfma_f32_16x16x32_bf16 v[96:99],a[100:103],a[212:215],v[96:99]
s_add_u32 s84,s84,128
ds_read_b64_tr_b16 a[68:69],v50 offset:17408
v_lshl_add_u32 v3,v12,5,v11
v_mfma_f32_16x16x32_bf16 v[112:115],a[100:103],a[220:223],0
s_mov_b32 s77,98304
ds_read_b64_tr_b16 a[70:71],v50 offset:19456
v_add_u32 v11,0,v23
v_mfma_f32_16x16x32_bf16 v[112:115],a[96:99],a[216:219],v[112:115]
s_add_u32 m0,s88,0
ds_read_b64_tr_b16 a[72:73],v50 offset:1032
v_mul_u32_u24 v12,3277,v11
s_waitcnt lgkmcnt(7)
v_mfma_f32_16x16x32_bf16 v[68:71],a[104:107],a[192:195],0
s_mov_b32 s71,0x3e0293ee
v_lshrrev_b32 v12,16,v12
v_mul_u32_u24 v13,20,v12
v_mfma_f32_16x16x32_bf16 v[68:71],a[108:111],a[196:199],v[68:71]
buffer_load_dwordx4 v40,s[36:39],s83 offen lds
v_mfma_f32_16x16x32_bf16 v[84:87],a[108:111],a[204:207],0
s_mov_b32 s73,0x53800000
ds_read_b64_tr_b16 a[74:75],v50 offset:3080
v_sub_u32 v13,v11,v13
)A"
R"A(v_mfma_f32_16x16x32_bf16 v[84:87],a[104:107],a[200:203],v[84:87]
s_lshl_b32 s79,s60,14
ds_read_b64_tr_b16 a[76:77],v50 offset:17416
v_bfe_u32 v14,v12,2,1
v_mfma_f32_16x16x32_bf16 v[100:103],a[104:107],a[208:211],0
s_add_u32 m0,s88,1024
ds_read_b64_tr_b16 a[78:79],v50 offset:19464
v_lshlrev_b32 v14,1,v14
v_mfma_f32_16x16x32_bf16 v[100:103],a[108:111],a[212:215],v[100:103]
s_add_u32 s80,s79,4096
ds_read_b64_tr_b16 a[80:81],v50 offset:1280
v_xor_b32 v13,v13,v14
v_mfma_f32_16x16x32_bf16 v[116:119],a[108:111],a[220:223],0
buffer_load_dwordx4 v41,s[36:39],s83 offen lds
v_mfma_f32_16x16x32_bf16 v[116:119],a[104:107],a[216:219],v[116:119]
s_add_u32 s81,s79,8192
ds_read_b64_tr_b16 a[82:83],v50 offset:3328
v_lshrrev_b32 v14,2,v13
v_mfma_f32_16x16x32_bf16 v[72:75],a[112:115],a[192:195],0
s_add_u32 s82,s79,12288
ds_read_b64_tr_b16 a[84:85],v50 offset:17664
v_cmp_eq_u32 vcc,3,v14
v_mfma_f32_16x16x32_bf16 v[72:75],a[116:119],a[196:199],v[72:75]
s_add_u32 m0,s88,2048
ds_read_b64_tr_b16 a[86:87],v50 offset:19712
v_min_u32 v15,3,v14
v_mfma_f32_16x16x32_bf16 v[88:91],a[116:119],a[204:207],0
s_add_u32 s20,s6,s66
s_addc_u32 s21,s7,0
v_and_b32 v13,3,v13
v_mfma_f32_16x16x32_bf16 v[88:91],a[112:115],a[200:203],v[88:91]
buffer_load_dwordx4 v42,s[36:39],s83 offen lds
v_mfma_f32_16x16x32_bf16 v[104:107],a[112:115],a[208:211],0
s_add_u32 s24,s8,s66
s_addc_u32 s25,s9,0
ds_read_b64_tr_b16 a[88:89],v50 offset:1288
v_mfma_f32_16x16x32_bf16 v[104:107],a[116:119],a[212:215],v[104:107]
s_lshl_b32 s66,s65,16
s_add_u32 s65,s63,1
ds_read_b64_tr_b16 a[90:91],v50 offset:3336
v_mfma_f32_16x16x32_bf16 v[120:123],a[116:119],a[220:223],0
s_add_u32 m0,s88,3072
ds_read_b64_tr_b16 a[92:93],v50 offset:17672
v_add_u32 v15,s65,v15
v_mfma_f32_16x16x32_bf16 v[120:123],a[112:115],a[216:219],v[120:123]
s_add_u32 s40,s12,s66
s_addc_u32 s41,s13,0
ds_read_b64_tr_b16 a[94:95],v50 offset:19720
v_mfma_f32_16x16x32_bf16 v[76:79],a[120:123],a[192:195],0
buffer_load_dwordx4 v43,s[36:39],s83 offen lds
s_waitcnt lgkmcnt(5)
v_mfma_f32_16x16x32_bf16 v[76:79],a[124:127],a[196:199],v[76:79]
s_mul_i32 s66,s60,40960
ds_read_b64_tr_b16 a[96:97],v50 offset:1536
v_and_b32 v15,3,v15
v_mfma_f32_16x16x32_bf16 v[92:95],a[124:127],a[204:207],0
s_and_b32 s21,s21,0xffff
ds_read_b64_tr_b16 a[98:99],v50 offset:3584
v_cndmask_b32_e64 v15,v15,4,vcc
v_mfma_f32_16x16x32_bf16 v[92:95],a[120:123],a[200:203],v[92:95]
s_add_u32 m0,s88,4096
ds_read_b64_tr_b16 a[100:101],v50 offset:17920
v_lshl_add_u32 v13,v15,2,v13
v_mfma_f32_16x16x32_bf16 v[108:111],a[120:123],a[208:211],0
s_and_b32 s25,s25,0xffff
v_mul_u32_u24 v12,1280,v12
v_lshl_add_u32 v12,v13,4,v12
v_mfma_f32_16x16x32_bf16 v[108:111],a[124:127],a[212:215],v[108:111]
buffer_load_dwordx4 v44,s[36:39],s83 offen lds
v_mfma_f32_16x16x32_bf16 v[124:127],a[124:127],a[220:223],0
s_and_b32 s41,s41,0xffff
ds_read_b64_tr_b16 a[102:103],v50 offset:19968
v_add_u32 v12,s66,v12
v_mfma_f32_16x16x32_bf16 v[124:127],a[120:123],a[216:219],v[124:127]
s_and_b32 s64,s62,3
ds_read_b64_tr_b16 a[104:105],v50 offset:1544
v_subrev_u32 v24,0,v12
v_mfma_f32_16x16x32_bf16 v[128:131],a[64:67],a[192:195],0
s_add_u32 m0,s88,5120
ds_read_b64_tr_b16 a[106:107],v50 offset:3592
v_add_u32 v11,64,v23
v_mfma_f32_16x16x32_bf16 v[128:131],a[68:71],a[196:199],v[128:131]
s_mov_b32 s72,0xff800000
ds_read_b64_tr_b16 a[108:109],v50 offset:17928
v_mul_u32_u24 v12,3277,v11
v_mfma_f32_16x16x32_bf16 v[160:163],a[68:71],a[204:207],0
buffer_load_dwordx4 v45,s[36:39],s83 offen lds
v_mfma_f32_16x16x32_bf16 v[160:163],a[64:67],a[200:203],v[160:163]
s_lshl_b32 s18,s60,24
ds_read_b64_tr_b16 a[110:111],v50 offset:19976
v_lshrrev_b32 v12,16,v12
v_mfma_f32_16x16x32_bf16 v[192:195],a[64:67],a[208:211],0
s_lshl_b32 s68,s62,6
ds_read_b64_tr_b16 a[112:113],v50 offset:1792
v_mul_u32_u24 v13,20,v12
v_mfma_f32_16x16x32_bf16 v[192:195],a[68:71],a[212:215],v[192:195]
s_add_u32 m0,s88,6144
ds_read_b64_tr_b16 a[114:115],v50 offset:3840
v_sub_u32 v13,v11,v13
v_mfma_f32_16x16x32_bf16 v[224:227],a[68:71],a[220:223],0
s_add_u32 s18,s18,s68
v_bfe_u32 v14,v12,2,1
v_lshlrev_b32 v14,1,v14
v_mfma_f32_16x16x32_bf16 v[224:227],a[64:67],a[216:219],v[224:227]
buffer_load_dwordx4 v46,s[36:39],s83 offen lds
v_mfma_f32_16x16x32_bf16 v[132:135],a[72:75],a[192:195],0
s_lshl_b32 s90,%1,2
ds_read_b64_tr_b16 a[116:117],v50 offset:18176
v_xor_b32 v13,v13,v14
v_mfma_f32_16x16x32_bf16 v[132:135],a[76:79],a[196:199],v[132:135]
s_add_u32 s90,s90,s60
ds_read_b64_tr_b16 a[118:119],v50 offset:20224
v_lshrrev_b32 v14,2,v13
v_mfma_f32_16x16x32_bf16 v[164:167],a[76:79],a[204:207],0
s_add_u32 m0,s88,7168
ds_read_b64_tr_b16 a[120:121],v50 offset:1800
v_cmp_eq_u32 vcc,3,v14
v_mfma_f32_16x16x32_bf16 v[164:167],a[72:75],a[200:203],v[164:167]
s_mov_b32 s88,s69
ds_read_b64_tr_b16 a[122:123],v50 offset:3848
v_min_u32 v15,3,v14
v_mfma_f32_16x16x32_bf16 v[196:199],a[72:75],a[208:211],0
buffer_load_dwordx4 v47,s[36:39],s83 offen lds
v_mfma_f32_16x16x32_bf16 v[196:199],a[76:79],a[212:215],v[196:199]
s_add_u32 s83,s83,524288
ds_read_b64_tr_b16 a[124:125],v50 offset:18184
v_add_u32 v15,s65,v15
v_mfma_f32_16x16x32_bf16 v[228:231],a[76:79],a[220:223],0
s_mul_i32 s90,s90,5120
ds_read_b64_tr_b16 a[126:127],v50 offset:20232
v_add_u32 v50,s86,v51
v_mfma_f32_16x16x32_bf16 v[228:231],a[72:75],a[216:219],v[228:231]
s_add_u32 m0,s85,0
v_and_b32 v15,3,v15
v_cndmask_b32_e64 v15,v15,4,vcc
v_mfma_f32_16x16x32_bf16 v[136:139],a[80:83],a[192:195],0
s_mul_i32 s58,s61,307200
v_and_b32 v13,3,v13
v_lshl_add_u32 v13,v15,2,v13
s_waitcnt lgkmcnt(5)
v_mfma_f32_16x16x32_bf16 v[136:139],a[84:87],a[196:199],v[136:139]
buffer_load_dwordx4 v32,s[28:31],s84 offen lds
v_mfma_f32_16x16x32_bf16 v[168:171],a[84:87],a[204:207],0
v_mul_u32_u24 v12,1280,v12
v_lshl_add_u32 v12,v13,4,v12
v_mfma_f32_16x16x32_bf16 v[168:171],a[80:83],a[200:203],v[168:171]
v_add_u32 v12,s66,v12
v_subrev_u32 v25,1024,v12
v_mfma_f32_16x16x32_bf16 v[200:203],a[80:83],a[208:211],0
v_add_u32 v11,128,v23
v_mul_u32_u24 v12,3277,v11
v_mfma_f32_16x16x32_bf16 v[200:203],a[84:87],a[212:215],v[200:203]
v_lshrrev_b32 v12,16,v12
v_mul_u32_u24 v13,20,v12
v_mfma_f32_16x16x32_bf16 v[232:235],a[84:87],a[220:223],0
buffer_load_dwordx4 v33,s[28:31],s84 offen offset:1024 lds
v_mfma_f32_16x16x32_bf16 v[232:235],a[80:83],a[216:219],v[232:235]
v_sub_u32 v13,v11,v13
v_bfe_u32 v14,v12,2,1
v_mfma_f32_16x16x32_bf16 v[140:143],a[88:91],a[192:195],0
v_lshlrev_b32 v14,1,v14
v_xor_b32 v13,v13,v14
v_mfma_f32_16x16x32_bf16 v[140:143],a[92:95],a[196:199],v[140:143]
v_lshrrev_b32 v14,2,v13
v_cmp_eq_u32 vcc,3,v14
v_mfma_f32_16x16x32_bf16 v[172:175],a[92:95],a[204:207],0
v_min_u32 v15,3,v14
v_add_u32 v15,s65,v15
v_mfma_f32_16x16x32_bf16 v[172:175],a[88:91],a[200:203],v[172:175]
buffer_load_dwordx4 v34,s[28:31],s84 offen offset:2048 lds
v_mfma_f32_16x16x32_bf16 v[204:207],a[88:91],a[208:211],0
v_and_b32 v15,3,v15
v_cndmask_b32_e64 v15,v15,4,vcc
v_mfma_f32_16x16x32_bf16 v[204:207],a[92:95],a[212:215],v[204:207]
v_and_b32 v13,3,v13
v_lshl_add_u32 v13,v15,2,v13
v_mfma_f32_16x16x32_bf16 v[236:239],a[92:95],a[220:223],0
v_mul_u32_u24 v12,1280,v12
v_lshl_add_u32 v12,v13,4,v12
v_mfma_f32_16x16x32_bf16 v[236:239],a[88:91],a[216:219],v[236:239]
v_add_u32 v12,s66,v12
v_subrev_u32 v26,2048,v12
s_waitcnt vmcnt(19) lgkmcnt(0)
s_barrier
v_mfma_f32_16x16x32_bf16 v[144:147],a[96:99],a[192:195],0
buffer_load_dwordx4 v35,s[28:31],s84 offen offset:3072 lds
v_mfma_f32_16x16x32_bf16 v[144:147],a[100:103],a[196:199],v[144:147]
s_waitcnt vmcnt(12)
ds_read_b128 a[224:227],v48
v_add_u32 v11,192,v23
v_mfma_f32_16x16x32_bf16 v[176:179],a[100:103],a[204:207],0
s_add_u32 m0,s85,4096
ds_read_b128 a[228:231],v49
v_mul_u32_u24 v12,3277,v11
v_mfma_f32_16x16x32_bf16 v[176:179],a[96:99],a[200:203],v[176:179]
s_xor_b32 s85,s85,98304
ds_read_b128 a[232:235],v48 offset:2048
v_lshrrev_b32 v12,16,v12
v_mfma_f32_16x16x32_bf16 v[208:211],a[96:99],a[208:211],0
ds_read_b128 a[236:239],v49 offset:2048
v_mul_u32_u24 v13,20,v12
v_mfma_f32_16x16x32_bf16 v[208:211],a[100:103],a[212:215],v[208:211]
buffer_load_dwordx4 v36,s[28:31],s84 offen lds
v_mfma_f32_16x16x32_bf16 v[240:243],a[100:103],a[220:223],0
ds_read_b128 a[240:243],v48 offset:4096
v_sub_u32 v13,v11,v13
v_mfma_f32_16x16x32_bf16 v[240:243],a[96:99],a[216:219],v[240:243]
ds_read_b128 a[244:247],v49 offset:4096
v_bfe_u32 v14,v12,2,1
v_mfma_f32_16x16x32_bf16 v[148:151],a[104:107],a[192:195],0
ds_read_b128 a[248:251],v48 offset:6144
v_lshlrev_b32 v14,1,v14
v_mfma_f32_16x16x32_bf16 v[148:151],a[108:111],a[196:199],v[148:151]
v_xor_b32 v13,v13,v14
v_lshrrev_b32 v14,2,v13
v_mfma_f32_16x16x32_bf16 v[180:183],a[108:111],a[204:207],0
buffer_load_dwordx4 v37,s[28:31],s84 offen offset:1024 lds
v_mfma_f32_16x16x32_bf16 v[180:183],a[104:107],a[200:203],v[180:183]
ds_read_b128 a[252:255],v49 offset:6144
v_cmp_eq_u32 vcc,3,v14
v_mfma_f32_16x16x32_bf16 v[212:215],a[104:107],a[208:211],0
ds_read_b64_tr_b16 a[64:65],v50
v_min_u32 v15,3,v14
v_mfma_f32_16x16x32_bf16 v[212:215],a[108:111],a[212:215],v[212:215]
ds_read_b64_tr_b16 a[66:67],v50 offset:2048
v_add_u32 v15,s65,v15
v_mfma_f32_16x16x32_bf16 v[244:247],a[108:111],a[220:223],0
ds_read_b64_tr_b16 a[68:69],v50 offset:16384
v_and_b32 v15,3,v15
v_mfma_f32_16x16x32_bf16 v[244:247],a[104:107],a[216:219],v[244:247]
buffer_load_dwordx4 v38,s[28:31],s84 offen offset:2048 lds
v_mfma_f32_16x16x32_bf16 v[152:155],a[112:115],a[192:195],0
ds_read_b64_tr_b16 a[70:71],v50 offset:18432
v_cndmask_b32_e64 v15,v15,4,vcc
v_mfma_f32_16x16x32_bf16 v[152:155],a[116:119],a[196:199],v[152:155]
ds_read_b64_tr_b16 a[72:73],v50 offset:8
v_and_b32 v13,3,v13
v_mfma_f32_16x16x32_bf16 v[184:187],a[116:119],a[204:207],0
L_pd1_%=:
s_cbranch_execz L_pd5_%=
ds_read_b64_tr_b16 a[74:75],v50 offset:2056
v_lshl_add_u32 v13,v15,2,v13
v_mfma_f32_16x16x32_bf16 v[184:187],a[112:115],a[200:203],v[184:187]
v_mul_u32_u24 v12,1280,v12
v_lshl_add_u32 v12,v13,4,v12
v_mfma_f32_16x16x32_bf16 v[216:219],a[112:115],a[208:211],0
buffer_load_dwordx4 v39,s[28:31],s84 offen offset:3072 lds
v_mfma_f32_16x16x32_bf16 v[216:219],a[116:119],a[212:215],v[216:219]
s_add_u32 s84,s84,128
ds_read_b64_tr_b16 a[76:77],v50 offset:16392
v_add_u32 v12,s66,v12
v_mfma_f32_16x16x32_bf16 v[248:251],a[116:119],a[220:223],0
ds_read_b64_tr_b16 a[78:79],v50 offset:18440
v_subrev_u32 v27,3072,v12
v_mfma_f32_16x16x32_bf16 v[248:251],a[112:115],a[216:219],v[248:251]
ds_read_b64_tr_b16 a[80:81],v50 offset:256
v_add_u32 v11,256,v23
v_mfma_f32_16x16x32_bf16 v[156:159],a[120:123],a[192:195],0
ds_read_b64_tr_b16 a[82:83],v50 offset:2304
v_mul_u32_u24 v12,3277,v11
v_mfma_f32_16x16x32_bf16 v[156:159],a[124:127],a[196:199],v[156:159]
v_lshrrev_b32 v12,16,v12
v_mul_u32_u24 v13,20,v12
v_mfma_f32_16x16x32_bf16 v[188:191],a[124:127],a[204:207],0
ds_read_b64_tr_b16 a[84:85],v50 offset:16640
v_sub_u32 v13,v11,v13
v_mfma_f32_16x16x32_bf16 v[188:191],a[120:123],a[200:203],v[188:191]
ds_read_b64_tr_b16 a[86:87],v50 offset:18688
v_bfe_u32 v14,v12,2,1
v_mfma_f32_16x16x32_bf16 v[220:223],a[120:123],a[208:211],0
ds_read_b64_tr_b16 a[88:89],v50 offset:264
v_lshlrev_b32 v14,1,v14
v_mfma_f32_16x16x32_bf16 v[220:223],a[124:127],a[212:215],v[220:223]
v_xor_b32 v13,v13,v14
v_lshrrev_b32 v14,2,v13
v_cmp_eq_u32 vcc,3,v14
v_mfma_f32_16x16x32_bf16 v[252:255],a[124:127],a[220:223],0
ds_read_b64_tr_b16 a[90:91],v50 offset:2312
v_mov_b32 v17,0
v_readfirstlane_b32 s52,v9
v_mfma_f32_16x16x32_bf16 v[252:255],a[120:123],a[216:219],v[252:255]
s_cmp_eq_u32 s60,3
s_cselect_b32 s72,0x7149f2ca,s72
s_add_u32 s56,s40,52224
s_addc_u32 s57,s41,0
s_add_u32 s58,s16,s58
s_addc_u32 s59,s17,0
s_cmp_eq_u32 s60,1
s_cselect_b64 s[56:57],s[58:59],s[56:57]
s_cmp_eq_u32 s60,2
s_cselect_b64 s[56:57],s[20:21],s[56:57]
s_cmp_eq_u32 s60,3
s_cselect_b64 s[56:57],s[24:25],s[56:57]
s_add_u32 s52,s52,1
s_xor_b32 s53,s52,0x9e3779b9
ds_read_b64_tr_b16 a[92:93],v50 offset:16648
v_mov_b32 v28,0x3f803f80
v_mov_b32 v29,0x3f803f80
v_mov_b32 v30,0x3f803f80
v_mov_b32 v31,0x3f803f80
v_min_u32 v15,3,v14
v_add_u32 v15,s65,v15
v_and_b32 v15,3,v15
v_cndmask_b32_e64 v15,v15,4,vcc
v_and_b32 v13,3,v13
v_lshl_add_u32 v13,v15,2,v13
v_mul_u32_u24 v12,1280,v12
)A"
R"A(v_lshl_add_u32 v12,v13,4,v12
v_add_u32 v12,s66,v12
v_subrev_u32 v7,0,v12
ds_read_b64_tr_b16 a[94:95],v50 offset:18696
global_load_dword a184,v17,s[56:57] nt
s_mov_b32 s70,3
s_memrealtime s[8:9]
s_memtime s[4:5]
L_qp_%=:
s_waitcnt lgkmcnt(6)
v_mfma_f32_16x16x32_bf16 a[0:3],a[64:67],a[224:227],a[0:3]
s_add_u32 m0,s88,0
ds_read_b64_tr_b16 a[96:97],v50 offset:512
v_xor_b32 v48,98304,v48
v_mfma_f32_16x16x32_bf16 a[0:3],a[68:71],a[228:231],a[0:3]
buffer_load_dwordx4 v40,s[36:39],s83 offen lds
v_mfma_f32_16x16x32_bf16 a[16:19],a[68:71],a[236:239],a[16:19]
s_mov_b32 s69,s86
ds_read_b64_tr_b16 a[98:99],v50 offset:2560
v_xor_b32 v49,98304,v49
v_mfma_f32_16x16x32_bf16 a[16:19],a[64:67],a[232:235],a[16:19]
s_mov_b32 s86,s87
s_mov_b32 s87,s88
ds_read_b64_tr_b16 a[100:101],v50 offset:16896
v_mfma_f32_16x16x32_bf16 a[32:35],a[64:67],a[240:243],a[32:35]
ds_read_b64_tr_b16 a[102:103],v50 offset:18944
v_mfma_f32_16x16x32_bf16 a[32:35],a[68:71],a[244:247],a[32:35]
ds_read_b64_tr_b16 a[104:105],v50 offset:520
v_mfma_f32_16x16x32_bf16 a[48:51],a[68:71],a[252:255],a[48:51]
s_add_u32 m0,s88,1024
v_mfma_f32_16x16x32_bf16 a[48:51],a[64:67],a[248:251],a[48:51]
ds_read_b64_tr_b16 a[106:107],v50 offset:2568
v_mfma_f32_16x16x32_bf16 a[4:7],a[72:75],a[224:227],a[4:7]
buffer_load_dwordx4 v41,s[36:39],s83 offen lds
v_mfma_f32_16x16x32_bf16 a[4:7],a[76:79],a[228:231],a[4:7]
ds_read_b64_tr_b16 a[108:109],v50 offset:16904
v_mfma_f32_16x16x32_bf16 a[20:23],a[76:79],a[236:239],a[20:23]
ds_read_b64_tr_b16 a[110:111],v50 offset:18952
v_mfma_f32_16x16x32_bf16 a[20:23],a[72:75],a[232:235],a[20:23]
ds_read_b64_tr_b16 a[112:113],v50 offset:768
v_mfma_f32_16x16x32_bf16 a[36:39],a[72:75],a[240:243],a[36:39]
ds_read_b64_tr_b16 a[114:115],v50 offset:2816
v_mfma_f32_16x16x32_bf16 a[36:39],a[76:79],a[244:247],a[36:39]
s_add_u32 m0,s88,2048
v_mfma_f32_16x16x32_bf16 a[52:55],a[76:79],a[252:255],a[52:55]
s_waitcnt lgkmcnt(4)
ds_read_b64_tr_b16 a[116:117],v50 offset:17152
v_mfma_f32_16x16x32_bf16 a[52:55],a[72:75],a[248:251],a[52:55]
buffer_load_dwordx4 v42,s[36:39],s83 offen lds
v_mfma_f32_16x16x32_bf16 a[8:11],a[80:83],a[224:227],a[8:11]
s_waitcnt lgkmcnt(5)
ds_read_b64_tr_b16 a[118:119],v50 offset:19200
v_mfma_f32_16x16x32_bf16 a[8:11],a[84:87],a[228:231],a[8:11]
ds_read_b64_tr_b16 a[120:121],v50 offset:776
v_mfma_f32_16x16x32_bf16 a[24:27],a[84:87],a[236:239],a[24:27]
ds_read_b64_tr_b16 a[122:123],v50 offset:2824
v_mfma_f32_16x16x32_bf16 a[24:27],a[80:83],a[232:235],a[24:27]
ds_read_b64_tr_b16 a[124:125],v50 offset:17160
v_mfma_f32_16x16x32_bf16 a[40:43],a[80:83],a[240:243],a[40:43]
s_add_u32 m0,s88,3072
v_mfma_f32_16x16x32_bf16 a[40:43],a[84:87],a[244:247],a[40:43]
ds_read_b64_tr_b16 a[126:127],v50 offset:19208
v_mfma_f32_16x16x32_bf16 a[56:59],a[84:87],a[252:255],a[56:59]
buffer_load_dwordx4 v43,s[36:39],s83 offen lds
v_mfma_f32_16x16x32_bf16 a[56:59],a[80:83],a[248:251],a[56:59]
ds_read_b64_tr_b16 a[64:65],v50 offset:1024
v_mfma_f32_16x16x32_bf16 a[12:15],a[88:91],a[224:227],a[12:15]
ds_read_b64_tr_b16 a[66:67],v50 offset:3072
v_mfma_f32_16x16x32_bf16 a[12:15],a[92:95],a[228:231],a[12:15]
ds_read_b64_tr_b16 a[68:69],v50 offset:17408
v_mfma_f32_16x16x32_bf16 a[28:31],a[92:95],a[236:239],a[28:31]
ds_read_b64_tr_b16 a[70:71],v50 offset:19456
v_mfma_f32_16x16x32_bf16 a[28:31],a[88:91],a[232:235],a[28:31]
s_add_u32 m0,s88,4096
v_mfma_f32_16x16x32_bf16 a[44:47],a[88:91],a[240:243],a[44:47]
ds_read_b64_tr_b16 a[72:73],v50 offset:1032
v_mfma_f32_16x16x32_bf16 a[44:47],a[92:95],a[244:247],a[44:47]
buffer_load_dwordx4 v44,s[36:39],s83 offen lds
v_mfma_f32_16x16x32_bf16 a[60:63],a[92:95],a[252:255],a[60:63]
ds_read_b64_tr_b16 a[74:75],v50 offset:3080
v_mfma_f32_16x16x32_bf16 a[60:63],a[88:91],a[248:251],a[60:63]
ds_read_b64_tr_b16 a[76:77],v50 offset:17416
v_mfma_f32_16x16x32_bf16 v[64:67],a[96:99],a[224:227],v[64:67]
ds_read_b64_tr_b16 a[78:79],v50 offset:19464
v_mfma_f32_16x16x32_bf16 v[64:67],a[100:103],a[228:231],v[64:67]
ds_read_b64_tr_b16 a[80:81],v50 offset:1280
v_mfma_f32_16x16x32_bf16 v[80:83],a[100:103],a[236:239],v[80:83]
s_add_u32 m0,s88,5120
v_mfma_f32_16x16x32_bf16 v[80:83],a[96:99],a[232:235],v[80:83]
ds_read_b64_tr_b16 a[82:83],v50 offset:3328
v_mfma_f32_16x16x32_bf16 v[96:99],a[96:99],a[240:243],v[96:99]
buffer_load_dwordx4 v45,s[36:39],s83 offen lds
v_mfma_f32_16x16x32_bf16 v[96:99],a[100:103],a[244:247],v[96:99]
ds_read_b64_tr_b16 a[84:85],v50 offset:17664
v_mfma_f32_16x16x32_bf16 v[112:115],a[100:103],a[252:255],v[112:115]
ds_read_b64_tr_b16 a[86:87],v50 offset:19712
v_mfma_f32_16x16x32_bf16 v[112:115],a[96:99],a[248:251],v[112:115]
ds_read_b64_tr_b16 a[88:89],v50 offset:1288
v_mfma_f32_16x16x32_bf16 v[68:71],a[104:107],a[224:227],v[68:71]
s_waitcnt lgkmcnt(5)
ds_read_b64_tr_b16 a[90:91],v50 offset:3336
v_mfma_f32_16x16x32_bf16 v[68:71],a[108:111],a[228:231],v[68:71]
s_add_u32 m0,s88,6144
v_mfma_f32_16x16x32_bf16 v[84:87],a[108:111],a[236:239],v[84:87]
ds_read_b64_tr_b16 a[92:93],v50 offset:17672
v_mfma_f32_16x16x32_bf16 v[84:87],a[104:107],a[232:235],v[84:87]
buffer_load_dwordx4 v46,s[36:39],s83 offen lds
v_mfma_f32_16x16x32_bf16 v[100:103],a[104:107],a[240:243],v[100:103]
ds_read_b64_tr_b16 a[94:95],v50 offset:19720
v_mfma_f32_16x16x32_bf16 v[100:103],a[108:111],a[244:247],v[100:103]
ds_read_b64_tr_b16 a[96:97],v50 offset:1536
v_mfma_f32_16x16x32_bf16 v[116:119],a[108:111],a[252:255],v[116:119]
ds_read_b64_tr_b16 a[98:99],v50 offset:3584
v_mfma_f32_16x16x32_bf16 v[116:119],a[104:107],a[248:251],v[116:119]
ds_read_b64_tr_b16 a[100:101],v50 offset:17920
v_mfma_f32_16x16x32_bf16 v[72:75],a[112:115],a[224:227],v[72:75]
s_add_u32 m0,s88,7168
s_mov_b32 s88,s69
s_mov_b32 s69,s86
v_mfma_f32_16x16x32_bf16 v[72:75],a[116:119],a[228:231],v[72:75]
ds_read_b64_tr_b16 a[102:103],v50 offset:19968
v_mfma_f32_16x16x32_bf16 v[88:91],a[116:119],a[236:239],v[88:91]
buffer_load_dwordx4 v47,s[36:39],s83 offen lds
v_mfma_f32_16x16x32_bf16 v[88:91],a[112:115],a[232:235],v[88:91]
s_add_u32 s83,s83,524288
ds_read_b64_tr_b16 a[104:105],v50 offset:1544
v_mfma_f32_16x16x32_bf16 v[104:107],a[112:115],a[240:243],v[104:107]
ds_read_b64_tr_b16 a[106:107],v50 offset:3592
v_mfma_f32_16x16x32_bf16 v[104:107],a[116:119],a[244:247],v[104:107]
ds_read_b64_tr_b16 a[108:109],v50 offset:17928
v_mfma_f32_16x16x32_bf16 v[120:123],a[116:119],a[252:255],v[120:123]
ds_read_b64_tr_b16 a[110:111],v50 offset:19976
v_mfma_f32_16x16x32_bf16 v[120:123],a[112:115],a[248:251],v[120:123]
s_add_u32 m0,s85,0
v_mfma_f32_16x16x32_bf16 v[76:79],a[120:123],a[224:227],v[76:79]
ds_read_b64_tr_b16 a[112:113],v50 offset:1792
v_mfma_f32_16x16x32_bf16 v[76:79],a[124:127],a[228:231],v[76:79]
buffer_load_dwordx4 v32,s[28:31],s84 offen lds
v_mfma_f32_16x16x32_bf16 v[92:95],a[124:127],a[236:239],v[92:95]
ds_read_b64_tr_b16 a[114:115],v50 offset:3840
v_mfma_f32_16x16x32_bf16 v[92:95],a[120:123],a[232:235],v[92:95]
ds_read_b64_tr_b16 a[116:117],v50 offset:18176
v_mfma_f32_16x16x32_bf16 v[108:111],a[120:123],a[240:243],v[108:111]
ds_read_b64_tr_b16 a[118:119],v50 offset:20224
v_mfma_f32_16x16x32_bf16 v[108:111],a[124:127],a[244:247],v[108:111]
s_cmp_eq_u32 s70,1
s_cselect_b32 s65,256,0
s_cselect_b32 s66,524288,0
v_mfma_f32_16x16x32_bf16 v[124:127],a[124:127],a[252:255],v[124:127]
ds_read_b64_tr_b16 a[124:125],v50 offset:18184
v_add_u32 v40,s65,v40
v_mfma_f32_16x16x32_bf16 v[124:127],a[120:123],a[248:251],v[124:127]
ds_read_b64_tr_b16 a[120:121],v50 offset:1800
v_add_u32 v41,s66,v41
v_mfma_f32_16x16x32_bf16 v[128:131],a[64:67],a[224:227],v[128:131]
buffer_load_dwordx4 v33,s[28:31],s84 offen offset:1024 lds
v_mfma_f32_16x16x32_bf16 v[128:131],a[68:71],a[228:231],v[128:131]
ds_read_b64_tr_b16 a[122:123],v50 offset:3848
v_add_u32 v42,s65,v42
v_mfma_f32_16x16x32_bf16 v[160:163],a[68:71],a[236:239],v[160:163]
ds_read_b64_tr_b16 a[126:127],v50 offset:20232
v_add_u32 v50,s86,v51
v_mfma_f32_16x16x32_bf16 v[160:163],a[64:67],a[232:235],v[160:163]
s_mov_b32 s86,s87
v_add_u32 v43,s66,v43
v_add_u32 v44,s65,v44
v_mfma_f32_16x16x32_bf16 v[192:195],a[64:67],a[240:243],v[192:195]
s_mov_b32 s87,s88
v_add_u32 v45,s66,v45
v_add_u32 v46,s65,v46
v_mfma_f32_16x16x32_bf16 v[192:195],a[68:71],a[244:247],v[192:195]
v_add_u32 v47,s66,v47
v_mfma_f32_16x16x32_bf16 v[224:227],a[68:71],a[252:255],v[224:227]
v_mfma_f32_16x16x32_bf16 v[224:227],a[64:67],a[248:251],v[224:227]
buffer_load_dwordx4 v34,s[28:31],s84 offen offset:2048 lds
v_mfma_f32_16x16x32_bf16 v[132:135],a[72:75],a[224:227],v[132:135]
v_mfma_f32_16x16x32_bf16 v[132:135],a[76:79],a[228:231],v[132:135]
v_mfma_f32_16x16x32_bf16 v[164:167],a[76:79],a[236:239],v[164:167]
v_mfma_f32_16x16x32_bf16 v[164:167],a[72:75],a[232:235],v[164:167]
v_mfma_f32_16x16x32_bf16 v[196:199],a[72:75],a[240:243],v[196:199]
v_mfma_f32_16x16x32_bf16 v[196:199],a[76:79],a[244:247],v[196:199]
v_mfma_f32_16x16x32_bf16 v[228:231],a[76:79],a[252:255],v[228:231]
buffer_load_dwordx4 v35,s[28:31],s84 offen offset:3072 lds
v_mfma_f32_16x16x32_bf16 v[228:231],a[72:75],a[248:251],v[228:231]
s_waitcnt lgkmcnt(0)
v_mfma_f32_16x16x32_bf16 v[136:139],a[80:83],a[224:227],v[136:139]
v_mfma_f32_16x16x32_bf16 v[136:139],a[84:87],a[228:231],v[136:139]
v_mfma_f32_16x16x32_bf16 v[168:171],a[84:87],a[236:239],v[168:171]
v_mfma_f32_16x16x32_bf16 v[168:171],a[80:83],a[232:235],v[168:171]
s_add_u32 m0,s85,4096
s_xor_b32 s85,s85,98304
v_mfma_f32_16x16x32_bf16 v[200:203],a[80:83],a[240:243],v[200:203]
v_mfma_f32_16x16x32_bf16 v[200:203],a[84:87],a[244:247],v[200:203]
buffer_load_dwordx4 v36,s[28:31],s84 offen lds
v_mfma_f32_16x16x32_bf16 v[232:235],a[84:87],a[252:255],v[232:235]
v_mfma_f32_16x16x32_bf16 v[232:235],a[80:83],a[248:251],v[232:235]
v_mfma_f32_16x16x32_bf16 v[140:143],a[88:91],a[224:227],v[140:143]
v_mfma_f32_16x16x32_bf16 v[140:143],a[92:95],a[228:231],v[140:143]
v_mfma_f32_16x16x32_bf16 v[172:175],a[92:95],a[236:239],v[172:175]
v_mfma_f32_16x16x32_bf16 v[172:175],a[88:91],a[232:235],v[172:175]
v_mfma_f32_16x16x32_bf16 v[204:207],a[88:91],a[240:243],v[204:207]
buffer_load_dwordx4 v37,s[28:31],s84 offen offset:1024 lds
v_mfma_f32_16x16x32_bf16 v[204:207],a[92:95],a[244:247],v[204:207]
v_mfma_f32_16x16x32_bf16 v[236:239],a[92:95],a[252:255],v[236:239]
v_mfma_f32_16x16x32_bf16 v[236:239],a[88:91],a[248:251],v[236:239]
s_waitcnt vmcnt(23)
s_barrier
v_mfma_f32_16x16x32_bf16 v[144:147],a[96:99],a[224:227],v[144:147]
s_waitcnt vmcnt(15)
ds_read_b128 a[192:195],v48
v_mfma_f32_16x16x32_bf16 v[144:147],a[100:103],a[228:231],v[144:147]
ds_read_b128 a[196:199],v49
v_mfma_f32_16x16x32_bf16 v[176:179],a[100:103],a[236:239],v[176:179]
buffer_load_dwordx4 v38,s[28:31],s84 offen offset:2048 lds
v_mfma_f32_16x16x32_bf16 v[176:179],a[96:99],a[232:235],v[176:179]
ds_read_b128 a[200:203],v48 offset:2048
v_mfma_f32_16x16x32_bf16 v[208:211],a[96:99],a[240:243],v[208:211]
ds_read_b128 a[204:207],v49 offset:2048
v_mfma_f32_16x16x32_bf16 v[208:211],a[100:103],a[244:247],v[208:211]
ds_read_b128 a[208:211],v48 offset:4096
v_mfma_f32_16x16x32_bf16 v[240:243],a[100:103],a[252:255],v[240:243]
ds_read_b128 a[212:215],v49 offset:4096
v_mfma_f32_16x16x32_bf16 v[240:243],a[96:99],a[248:251],v[240:243]
v_mfma_f32_16x16x32_bf16 v[148:151],a[104:107],a[224:227],v[148:151]
ds_read_b128 a[216:219],v48 offset:6144
v_xor_b32 v48,98304,v48
v_mfma_f32_16x16x32_bf16 v[148:151],a[108:111],a[228:231],v[148:151]
buffer_load_dwordx4 v39,s[28:31],s84 offen offset:3072 lds
v_mfma_f32_16x16x32_bf16 v[180:183],a[108:111],a[236:239],v[180:183]
s_add_u32 s84,s84,128
ds_read_b128 a[220:223],v49 offset:6144
v_xor_b32 v49,98304,v49
v_mfma_f32_16x16x32_bf16 v[180:183],a[104:107],a[232:235],v[180:183]
ds_read_b64_tr_b16 a[64:65],v50
v_mfma_f32_16x16x32_bf16 v[212:215],a[104:107],a[240:243],v[212:215]
ds_read_b64_tr_b16 a[66:67],v50 offset:2048
v_mfma_f32_16x16x32_bf16 v[212:215],a[108:111],a[244:247],v[212:215]
ds_read_b64_tr_b16 a[68:69],v50 offset:16384
v_mfma_f32_16x16x32_bf16 v[244:247],a[108:111],a[252:255],v[244:247]
v_mfma_f32_16x16x32_bf16 v[244:247],a[104:107],a[248:251],v[244:247]
ds_read_b64_tr_b16 a[70:71],v50 offset:18432
v_mfma_f32_16x16x32_bf16 v[152:155],a[112:115],a[224:227],v[152:155]
)A"
R"A(buffer_load_dword v9,v19,s[48:51],s90 offen
v_mfma_f32_16x16x32_bf16 v[152:155],a[116:119],a[228:231],v[152:155]
s_add_u32 s90,s90,896
ds_read_b64_tr_b16 a[72:73],v50 offset:8
v_mfma_f32_16x16x32_bf16 v[184:187],a[116:119],a[236:239],v[184:187]
ds_read_b64_tr_b16 a[74:75],v50 offset:2056
v_mfma_f32_16x16x32_bf16 v[184:187],a[112:115],a[232:235],v[184:187]
ds_read_b64_tr_b16 a[76:77],v50 offset:16392
v_mfma_f32_16x16x32_bf16 v[216:219],a[112:115],a[240:243],v[216:219]
ds_read_b64_tr_b16 a[78:79],v50 offset:18440
v_mfma_f32_16x16x32_bf16 v[216:219],a[116:119],a[244:247],v[216:219]
v_mfma_f32_16x16x32_bf16 v[248:251],a[116:119],a[252:255],v[248:251]
ds_read_b64_tr_b16 a[80:81],v50 offset:256
v_mfma_f32_16x16x32_bf16 v[248:251],a[112:115],a[248:251],v[248:251]
ds_read_b64_tr_b16 a[82:83],v50 offset:2304
v_mfma_f32_16x16x32_bf16 v[156:159],a[120:123],a[224:227],v[156:159]
ds_read_b64_tr_b16 a[84:85],v50 offset:16640
v_mfma_f32_16x16x32_bf16 v[156:159],a[124:127],a[228:231],v[156:159]
v_mfma_f32_16x16x32_bf16 v[188:191],a[124:127],a[236:239],v[188:191]
ds_read_b64_tr_b16 a[86:87],v50 offset:18688
v_mfma_f32_16x16x32_bf16 v[188:191],a[120:123],a[232:235],v[188:191]
ds_read_b64_tr_b16 a[88:89],v50 offset:264
v_mfma_f32_16x16x32_bf16 v[220:223],a[120:123],a[240:243],v[220:223]
ds_read_b64_tr_b16 a[90:91],v50 offset:2312
v_mfma_f32_16x16x32_bf16 v[220:223],a[124:127],a[244:247],v[220:223]
v_mfma_f32_16x16x32_bf16 v[252:255],a[124:127],a[252:255],v[252:255]
s_waitcnt lgkmcnt(4)
ds_read_b64_tr_b16 a[92:93],v50 offset:16648
v_mfma_f32_16x16x32_bf16 v[252:255],a[120:123],a[248:251],v[252:255]
s_add_u32 m0,s88,0
ds_read_b64_tr_b16 a[94:95],v50 offset:18696
v_mfma_f32_16x16x32_bf16 a[0:3],a[64:67],a[192:195],a[0:3]
ds_read_b64_tr_b16 a[96:97],v50 offset:512
v_mfma_f32_16x16x32_bf16 a[0:3],a[68:71],a[196:199],a[0:3]
buffer_load_dwordx4 v40,s[36:39],s83 offen lds
v_mfma_f32_16x16x32_bf16 a[16:19],a[68:71],a[204:207],a[16:19]
ds_read_b64_tr_b16 a[98:99],v50 offset:2560
v_mfma_f32_16x16x32_bf16 a[16:19],a[64:67],a[200:203],a[16:19]
ds_read_b64_tr_b16 a[100:101],v50 offset:16896
v_mfma_f32_16x16x32_bf16 a[32:35],a[64:67],a[208:211],a[32:35]
ds_read_b64_tr_b16 a[102:103],v50 offset:18944
v_mfma_f32_16x16x32_bf16 a[32:35],a[68:71],a[212:215],a[32:35]
v_mfma_f32_16x16x32_bf16 a[48:51],a[68:71],a[220:223],a[48:51]
s_add_u32 m0,s88,1024
ds_read_b64_tr_b16 a[104:105],v50 offset:520
v_mfma_f32_16x16x32_bf16 a[48:51],a[64:67],a[216:219],a[48:51]
ds_read_b64_tr_b16 a[106:107],v50 offset:2568
v_mfma_f32_16x16x32_bf16 a[4:7],a[72:75],a[192:195],a[4:7]
buffer_load_dwordx4 v41,s[36:39],s83 offen lds
v_mfma_f32_16x16x32_bf16 a[4:7],a[76:79],a[196:199],a[4:7]
ds_read_b64_tr_b16 a[108:109],v50 offset:16904
v_mfma_f32_16x16x32_bf16 a[20:23],a[76:79],a[204:207],a[20:23]
ds_read_b64_tr_b16 a[110:111],v50 offset:18952
v_mfma_f32_16x16x32_bf16 a[20:23],a[72:75],a[200:203],a[20:23]
ds_read_b64_tr_b16 a[112:113],v50 offset:768
v_mfma_f32_16x16x32_bf16 a[36:39],a[72:75],a[208:211],a[36:39]
ds_read_b64_tr_b16 a[114:115],v50 offset:2816
v_mfma_f32_16x16x32_bf16 a[36:39],a[76:79],a[212:215],a[36:39]
s_add_u32 m0,s88,2048
v_mfma_f32_16x16x32_bf16 a[52:55],a[76:79],a[220:223],a[52:55]
ds_read_b64_tr_b16 a[116:117],v50 offset:17152
v_mfma_f32_16x16x32_bf16 a[52:55],a[72:75],a[216:219],a[52:55]
buffer_load_dwordx4 v42,s[36:39],s83 offen lds
v_mfma_f32_16x16x32_bf16 a[8:11],a[80:83],a[192:195],a[8:11]
s_waitcnt lgkmcnt(5)
ds_read_b64_tr_b16 a[118:119],v50 offset:19200
v_mfma_f32_16x16x32_bf16 a[8:11],a[84:87],a[196:199],a[8:11]
ds_read_b64_tr_b16 a[120:121],v50 offset:776
v_mfma_f32_16x16x32_bf16 a[24:27],a[84:87],a[204:207],a[24:27]
ds_read_b64_tr_b16 a[122:123],v50 offset:2824
v_mfma_f32_16x16x32_bf16 a[24:27],a[80:83],a[200:203],a[24:27]
ds_read_b64_tr_b16 a[124:125],v50 offset:17160
v_mfma_f32_16x16x32_bf16 a[40:43],a[80:83],a[208:211],a[40:43]
s_add_u32 m0,s88,3072
v_mfma_f32_16x16x32_bf16 a[40:43],a[84:87],a[212:215],a[40:43]
ds_read_b64_tr_b16 a[126:127],v50 offset:19208
v_mfma_f32_16x16x32_bf16 a[56:59],a[84:87],a[220:223],a[56:59]
buffer_load_dwordx4 v43,s[36:39],s83 offen lds
v_mfma_f32_16x16x32_bf16 a[56:59],a[80:83],a[216:219],a[56:59]
ds_read_b64_tr_b16 a[64:65],v50 offset:1024
v_mfma_f32_16x16x32_bf16 a[12:15],a[88:91],a[192:195],a[12:15]
ds_read_b64_tr_b16 a[66:67],v50 offset:3072
v_mfma_f32_16x16x32_bf16 a[12:15],a[92:95],a[196:199],a[12:15]
ds_read_b64_tr_b16 a[68:69],v50 offset:17408
v_mfma_f32_16x16x32_bf16 a[28:31],a[92:95],a[204:207],a[28:31]
ds_read_b64_tr_b16 a[70:71],v50 offset:19456
v_mfma_f32_16x16x32_bf16 a[28:31],a[88:91],a[200:203],a[28:31]
s_add_u32 m0,s88,4096
v_mfma_f32_16x16x32_bf16 a[44:47],a[88:91],a[208:211],a[44:47]
ds_read_b64_tr_b16 a[72:73],v50 offset:1032
v_mfma_f32_16x16x32_bf16 a[44:47],a[92:95],a[212:215],a[44:47]
buffer_load_dwordx4 v44,s[36:39],s83 offen lds
v_mfma_f32_16x16x32_bf16 a[60:63],a[92:95],a[220:223],a[60:63]
ds_read_b64_tr_b16 a[74:75],v50 offset:3080
v_mfma_f32_16x16x32_bf16 a[60:63],a[88:91],a[216:219],a[60:63]
ds_read_b64_tr_b16 a[76:77],v50 offset:17416
v_mfma_f32_16x16x32_bf16 v[64:67],a[96:99],a[192:195],v[64:67]
ds_read_b64_tr_b16 a[78:79],v50 offset:19464
v_mfma_f32_16x16x32_bf16 v[64:67],a[100:103],a[196:199],v[64:67]
ds_read_b64_tr_b16 a[80:81],v50 offset:1280
v_mfma_f32_16x16x32_bf16 v[80:83],a[100:103],a[204:207],v[80:83]
s_add_u32 m0,s88,5120
v_mfma_f32_16x16x32_bf16 v[80:83],a[96:99],a[200:203],v[80:83]
ds_read_b64_tr_b16 a[82:83],v50 offset:3328
v_mfma_f32_16x16x32_bf16 v[96:99],a[96:99],a[208:211],v[96:99]
buffer_load_dwordx4 v45,s[36:39],s83 offen lds
v_mfma_f32_16x16x32_bf16 v[96:99],a[100:103],a[212:215],v[96:99]
ds_read_b64_tr_b16 a[84:85],v50 offset:17664
v_mfma_f32_16x16x32_bf16 v[112:115],a[100:103],a[220:223],v[112:115]
ds_read_b64_tr_b16 a[86:87],v50 offset:19712
v_mfma_f32_16x16x32_bf16 v[112:115],a[96:99],a[216:219],v[112:115]
ds_read_b64_tr_b16 a[88:89],v50 offset:1288
v_mfma_f32_16x16x32_bf16 v[68:71],a[104:107],a[192:195],v[68:71]
s_waitcnt lgkmcnt(5)
ds_read_b64_tr_b16 a[90:91],v50 offset:3336
v_mfma_f32_16x16x32_bf16 v[68:71],a[108:111],a[196:199],v[68:71]
s_add_u32 m0,s88,6144
v_mfma_f32_16x16x32_bf16 v[84:87],a[108:111],a[204:207],v[84:87]
ds_read_b64_tr_b16 a[92:93],v50 offset:17672
v_mfma_f32_16x16x32_bf16 v[84:87],a[104:107],a[200:203],v[84:87]
buffer_load_dwordx4 v46,s[36:39],s83 offen lds
v_mfma_f32_16x16x32_bf16 v[100:103],a[104:107],a[208:211],v[100:103]
ds_read_b64_tr_b16 a[94:95],v50 offset:19720
v_mfma_f32_16x16x32_bf16 v[100:103],a[108:111],a[212:215],v[100:103]
ds_read_b64_tr_b16 a[96:97],v50 offset:1536
v_mfma_f32_16x16x32_bf16 v[116:119],a[108:111],a[220:223],v[116:119]
ds_read_b64_tr_b16 a[98:99],v50 offset:3584
v_mfma_f32_16x16x32_bf16 v[116:119],a[104:107],a[216:219],v[116:119]
ds_read_b64_tr_b16 a[100:101],v50 offset:17920
v_mfma_f32_16x16x32_bf16 v[72:75],a[112:115],a[192:195],v[72:75]
s_add_u32 m0,s88,7168
s_mov_b32 s88,s69
v_mfma_f32_16x16x32_bf16 v[72:75],a[116:119],a[196:199],v[72:75]
ds_read_b64_tr_b16 a[102:103],v50 offset:19968
v_mfma_f32_16x16x32_bf16 v[88:91],a[116:119],a[204:207],v[88:91]
buffer_load_dwordx4 v47,s[36:39],s83 offen lds
v_mfma_f32_16x16x32_bf16 v[88:91],a[112:115],a[200:203],v[88:91]
s_add_u32 s83,s83,524288
ds_read_b64_tr_b16 a[104:105],v50 offset:1544
v_mfma_f32_16x16x32_bf16 v[104:107],a[112:115],a[208:211],v[104:107]
ds_read_b64_tr_b16 a[106:107],v50 offset:3592
v_mfma_f32_16x16x32_bf16 v[104:107],a[116:119],a[212:215],v[104:107]
ds_read_b64_tr_b16 a[108:109],v50 offset:17928
v_mfma_f32_16x16x32_bf16 v[120:123],a[116:119],a[220:223],v[120:123]
ds_read_b64_tr_b16 a[110:111],v50 offset:19976
v_mfma_f32_16x16x32_bf16 v[120:123],a[112:115],a[216:219],v[120:123]
s_add_u32 m0,s85,0
v_mfma_f32_16x16x32_bf16 v[76:79],a[120:123],a[192:195],v[76:79]
ds_read_b64_tr_b16 a[112:113],v50 offset:1792
v_mfma_f32_16x16x32_bf16 v[76:79],a[124:127],a[196:199],v[76:79]
buffer_load_dwordx4 v32,s[28:31],s84 offen lds
v_mfma_f32_16x16x32_bf16 v[92:95],a[124:127],a[204:207],v[92:95]
ds_read_b64_tr_b16 a[114:115],v50 offset:3840
v_mfma_f32_16x16x32_bf16 v[92:95],a[120:123],a[200:203],v[92:95]
ds_read_b64_tr_b16 a[116:117],v50 offset:18176
v_mfma_f32_16x16x32_bf16 v[108:111],a[120:123],a[208:211],v[108:111]
ds_read_b64_tr_b16 a[118:119],v50 offset:20224
v_mfma_f32_16x16x32_bf16 v[108:111],a[124:127],a[212:215],v[108:111]
v_mfma_f32_16x16x32_bf16 v[124:127],a[124:127],a[220:223],v[124:127]
ds_read_b64_tr_b16 a[124:125],v50 offset:18184
v_mfma_f32_16x16x32_bf16 v[124:127],a[120:123],a[216:219],v[124:127]
ds_read_b64_tr_b16 a[120:121],v50 offset:1800
v_mfma_f32_16x16x32_bf16 v[128:131],a[64:67],a[192:195],v[128:131]
buffer_load_dwordx4 v33,s[28:31],s84 offen offset:1024 lds
v_mfma_f32_16x16x32_bf16 v[128:131],a[68:71],a[196:199],v[128:131]
ds_read_b64_tr_b16 a[122:123],v50 offset:3848
v_mfma_f32_16x16x32_bf16 v[160:163],a[68:71],a[204:207],v[160:163]
ds_read_b64_tr_b16 a[126:127],v50 offset:20232
v_add_u32 v50,s86,v51
v_mfma_f32_16x16x32_bf16 v[160:163],a[64:67],a[200:203],v[160:163]
v_mfma_f32_16x16x32_bf16 v[192:195],a[64:67],a[208:211],v[192:195]
v_mfma_f32_16x16x32_bf16 v[192:195],a[68:71],a[212:215],v[192:195]
v_mfma_f32_16x16x32_bf16 v[224:227],a[68:71],a[220:223],v[224:227]
v_mfma_f32_16x16x32_bf16 v[224:227],a[64:67],a[216:219],v[224:227]
buffer_load_dwordx4 v34,s[28:31],s84 offen offset:2048 lds
v_mfma_f32_16x16x32_bf16 v[132:135],a[72:75],a[192:195],v[132:135]
v_mfma_f32_16x16x32_bf16 v[132:135],a[76:79],a[196:199],v[132:135]
v_mfma_f32_16x16x32_bf16 v[164:167],a[76:79],a[204:207],v[164:167]
v_mfma_f32_16x16x32_bf16 v[164:167],a[72:75],a[200:203],v[164:167]
v_mfma_f32_16x16x32_bf16 v[196:199],a[72:75],a[208:211],v[196:199]
v_mfma_f32_16x16x32_bf16 v[196:199],a[76:79],a[212:215],v[196:199]
v_mfma_f32_16x16x32_bf16 v[228:231],a[76:79],a[220:223],v[228:231]
buffer_load_dwordx4 v35,s[28:31],s84 offen offset:3072 lds
v_mfma_f32_16x16x32_bf16 v[228:231],a[72:75],a[216:219],v[228:231]
s_waitcnt lgkmcnt(0)
v_mfma_f32_16x16x32_bf16 v[136:139],a[80:83],a[192:195],v[136:139]
v_mfma_f32_16x16x32_bf16 v[136:139],a[84:87],a[196:199],v[136:139]
v_mfma_f32_16x16x32_bf16 v[168:171],a[84:87],a[204:207],v[168:171]
v_mfma_f32_16x16x32_bf16 v[168:171],a[80:83],a[200:203],v[168:171]
s_add_u32 m0,s85,4096
s_xor_b32 s85,s85,98304
v_mfma_f32_16x16x32_bf16 v[200:203],a[80:83],a[208:211],v[200:203]
v_mfma_f32_16x16x32_bf16 v[200:203],a[84:87],a[212:215],v[200:203]
buffer_load_dwordx4 v36,s[28:31],s84 offen lds
v_mfma_f32_16x16x32_bf16 v[232:235],a[84:87],a[220:223],v[232:235]
v_mfma_f32_16x16x32_bf16 v[232:235],a[80:83],a[216:219],v[232:235]
v_mfma_f32_16x16x32_bf16 v[140:143],a[88:91],a[192:195],v[140:143]
v_mfma_f32_16x16x32_bf16 v[140:143],a[92:95],a[196:199],v[140:143]
v_mfma_f32_16x16x32_bf16 v[172:175],a[92:95],a[204:207],v[172:175]
v_mfma_f32_16x16x32_bf16 v[172:175],a[88:91],a[200:203],v[172:175]
v_mfma_f32_16x16x32_bf16 v[204:207],a[88:91],a[208:211],v[204:207]
buffer_load_dwordx4 v37,s[28:31],s84 offen offset:1024 lds
v_mfma_f32_16x16x32_bf16 v[204:207],a[92:95],a[212:215],v[204:207]
v_mfma_f32_16x16x32_bf16 v[236:239],a[92:95],a[220:223],v[236:239]
v_mfma_f32_16x16x32_bf16 v[236:239],a[88:91],a[216:219],v[236:239]
s_waitcnt vmcnt(23)
s_barrier
v_mfma_f32_16x16x32_bf16 v[144:147],a[96:99],a[192:195],v[144:147]
s_waitcnt vmcnt(15)
ds_read_b128 a[224:227],v48
v_mfma_f32_16x16x32_bf16 v[144:147],a[100:103],a[196:199],v[144:147]
ds_read_b128 a[228:231],v49
v_mfma_f32_16x16x32_bf16 v[176:179],a[100:103],a[204:207],v[176:179]
buffer_load_dwordx4 v38,s[28:31],s84 offen offset:2048 lds
v_mfma_f32_16x16x32_bf16 v[176:179],a[96:99],a[200:203],v[176:179]
ds_read_b128 a[232:235],v48 offset:2048
v_mfma_f32_16x16x32_bf16 v[208:211],a[96:99],a[208:211],v[208:211]
ds_read_b128 a[236:239],v49 offset:2048
v_mfma_f32_16x16x32_bf16 v[208:211],a[100:103],a[212:215],v[208:211]
ds_read_b128 a[240:243],v48 offset:4096
v_mfma_f32_16x16x32_bf16 v[240:243],a[100:103],a[220:223],v[240:243]
ds_read_b128 a[244:247],v49 offset:4096
v_mfma_f32_16x16x32_bf16 v[240:243],a[96:99],a[216:219],v[240:243]
)A"
R"A(v_mfma_f32_16x16x32_bf16 v[148:151],a[104:107],a[192:195],v[148:151]
ds_read_b128 a[248:251],v48 offset:6144
v_mfma_f32_16x16x32_bf16 v[148:151],a[108:111],a[196:199],v[148:151]
buffer_load_dwordx4 v39,s[28:31],s84 offen offset:3072 lds
v_mfma_f32_16x16x32_bf16 v[180:183],a[108:111],a[204:207],v[180:183]
s_add_u32 s84,s84,128
ds_read_b128 a[252:255],v49 offset:6144
v_mfma_f32_16x16x32_bf16 v[180:183],a[104:107],a[200:203],v[180:183]
ds_read_b64_tr_b16 a[64:65],v50
v_mfma_f32_16x16x32_bf16 v[212:215],a[104:107],a[208:211],v[212:215]
ds_read_b64_tr_b16 a[66:67],v50 offset:2048
v_mfma_f32_16x16x32_bf16 v[212:215],a[108:111],a[212:215],v[212:215]
ds_read_b64_tr_b16 a[68:69],v50 offset:16384
s_nop 0
L_pd2_%=:
s_cbranch_execz L_pd6_%=
v_mfma_f32_16x16x32_bf16 v[244:247],a[108:111],a[220:223],v[244:247]
v_mfma_f32_16x16x32_bf16 v[244:247],a[104:107],a[216:219],v[244:247]
ds_read_b64_tr_b16 a[70:71],v50 offset:18432
v_mfma_f32_16x16x32_bf16 v[152:155],a[112:115],a[192:195],v[152:155]
buffer_load_dword v8,v19,s[48:51],s90 offen
v_mfma_f32_16x16x32_bf16 v[152:155],a[116:119],a[196:199],v[152:155]
s_add_u32 s90,s90,896
ds_read_b64_tr_b16 a[72:73],v50 offset:8
v_mfma_f32_16x16x32_bf16 v[184:187],a[116:119],a[204:207],v[184:187]
ds_read_b64_tr_b16 a[74:75],v50 offset:2056
v_mfma_f32_16x16x32_bf16 v[184:187],a[112:115],a[200:203],v[184:187]
ds_read_b64_tr_b16 a[76:77],v50 offset:16392
v_mfma_f32_16x16x32_bf16 v[216:219],a[112:115],a[208:211],v[216:219]
ds_read_b64_tr_b16 a[78:79],v50 offset:18440
v_mfma_f32_16x16x32_bf16 v[216:219],a[116:119],a[212:215],v[216:219]
v_mfma_f32_16x16x32_bf16 v[248:251],a[116:119],a[220:223],v[248:251]
ds_read_b64_tr_b16 a[80:81],v50 offset:256
v_mfma_f32_16x16x32_bf16 v[248:251],a[112:115],a[216:219],v[248:251]
ds_read_b64_tr_b16 a[82:83],v50 offset:2304
v_mfma_f32_16x16x32_bf16 v[156:159],a[120:123],a[192:195],v[156:159]
ds_read_b64_tr_b16 a[84:85],v50 offset:16640
v_mfma_f32_16x16x32_bf16 v[156:159],a[124:127],a[196:199],v[156:159]
v_mfma_f32_16x16x32_bf16 v[188:191],a[124:127],a[204:207],v[188:191]
ds_read_b64_tr_b16 a[86:87],v50 offset:18688
v_mfma_f32_16x16x32_bf16 v[188:191],a[120:123],a[200:203],v[188:191]
ds_read_b64_tr_b16 a[88:89],v50 offset:264
v_mfma_f32_16x16x32_bf16 v[220:223],a[120:123],a[208:211],v[220:223]
ds_read_b64_tr_b16 a[90:91],v50 offset:2312
v_mfma_f32_16x16x32_bf16 v[220:223],a[124:127],a[212:215],v[220:223]
v_mfma_f32_16x16x32_bf16 v[252:255],a[124:127],a[220:223],v[252:255]
ds_read_b64_tr_b16 a[92:93],v50 offset:16648
v_mfma_f32_16x16x32_bf16 v[252:255],a[120:123],a[216:219],v[252:255]
ds_read_b64_tr_b16 a[94:95],v50 offset:18696
s_sub_u32 s70,s70,1
s_cmp_lg_u32 s70,0
s_cbranch_scc1 L_qp_%=
s_waitcnt lgkmcnt(6)
v_mfma_f32_16x16x32_bf16 a[0:3],a[64:67],a[224:227],a[0:3]
s_memrealtime s[92:93]
s_memtime s[56:57]
s_add_u32 m0,s85,0
v_mfma_f32_16x16x32_bf16 a[0:3],a[68:71],a[228:231],a[0:3]
buffer_load_dwordx4 v32,s[28:31],s84 offen lds
v_mfma_f32_16x16x32_bf16 a[16:19],a[68:71],a[236:239],a[16:19]
s_sub_u32 s83,s83,524288
ds_read_b64_tr_b16 a[96:97],v50 offset:512
v_subrev_u32 v40,256,v40
v_mfma_f32_16x16x32_bf16 a[16:19],a[64:67],a[232:235],a[16:19]
s_mov_b32 s69,s86
ds_read_b64_tr_b16 a[98:99],v50 offset:2560
v_subrev_u32 v41,256,v41
v_mfma_f32_16x16x32_bf16 a[32:35],a[64:67],a[240:243],a[32:35]
s_mov_b32 s86,s87
ds_read_b64_tr_b16 a[100:101],v50 offset:16896
v_subrev_u32 v42,256,v42
v_mfma_f32_16x16x32_bf16 a[32:35],a[68:71],a[244:247],a[32:35]
s_mov_b32 s87,s88
ds_read_b64_tr_b16 a[102:103],v50 offset:18944
v_subrev_u32 v43,256,v43
v_mfma_f32_16x16x32_bf16 a[48:51],a[68:71],a[252:255],a[48:51]
s_add_u32 s75,s75,16384
v_subrev_u32 v44,256,v44
v_subrev_u32 v45,256,v45
v_mfma_f32_16x16x32_bf16 a[48:51],a[64:67],a[248:251],a[48:51]
buffer_load_dwordx4 v33,s[28:31],s84 offen offset:1024 lds
v_mfma_f32_16x16x32_bf16 a[4:7],a[72:75],a[224:227],a[4:7]
s_sub_u32 s78,s77,32768
ds_read_b64_tr_b16 a[104:105],v50 offset:520
v_subrev_u32 v46,256,v46
v_mfma_f32_16x16x32_bf16 a[4:7],a[76:79],a[228:231],a[4:7]
s_and_b32 s78,s78,0x1ffff
ds_read_b64_tr_b16 a[106:107],v50 offset:2568
v_subrev_u32 v47,256,v47
v_mfma_f32_16x16x32_bf16 a[20:23],a[76:79],a[236:239],a[20:23]
s_sub_u32 s78,s78,s77
ds_read_b64_tr_b16 a[108:109],v50 offset:16904
v_xor_b32 v48,98304,v48
v_mfma_f32_16x16x32_bf16 a[20:23],a[72:75],a[232:235],a[20:23]
s_mov_b32 s65,262144
ds_read_b64_tr_b16 a[110:111],v50 offset:18952
v_xor_b32 v49,98304,v49
v_mfma_f32_16x16x32_bf16 a[36:39],a[72:75],a[240:243],a[36:39]
s_add_u32 s77,s77,s78
s_mov_b32 s6,0
v_add_u32 v52,s87,v51
v_mfma_f32_16x16x32_bf16 a[36:39],a[76:79],a[244:247],a[36:39]
buffer_load_dwordx4 v34,s[28:31],s84 offen offset:2048 lds
v_mfma_f32_16x16x32_bf16 a[52:55],a[76:79],a[252:255],a[52:55]
ds_read_b64_tr_b16 a[112:113],v50 offset:768
v_mfma_f32_16x16x32_bf16 a[52:55],a[72:75],a[248:251],a[52:55]
ds_read_b64_tr_b16 a[114:115],v50 offset:2816
v_mfma_f32_16x16x32_bf16 a[8:11],a[80:83],a[224:227],a[8:11]
s_waitcnt lgkmcnt(5)
ds_read_b64_tr_b16 a[116:117],v50 offset:17152
v_mfma_f32_16x16x32_bf16 a[8:11],a[84:87],a[228:231],a[8:11]
ds_read_b64_tr_b16 a[118:119],v50 offset:19200
v_mfma_f32_16x16x32_bf16 a[24:27],a[84:87],a[236:239],a[24:27]
v_mfma_f32_16x16x32_bf16 a[24:27],a[80:83],a[232:235],a[24:27]
buffer_load_dwordx4 v35,s[28:31],s84 offen offset:3072 lds
v_mfma_f32_16x16x32_bf16 a[40:43],a[80:83],a[240:243],a[40:43]
ds_read_b64_tr_b16 a[120:121],v50 offset:776
v_mfma_f32_16x16x32_bf16 a[40:43],a[84:87],a[244:247],a[40:43]
ds_read_b64_tr_b16 a[122:123],v50 offset:2824
v_mfma_f32_16x16x32_bf16 a[56:59],a[84:87],a[252:255],a[56:59]
ds_read_b64_tr_b16 a[124:125],v50 offset:17160
v_mfma_f32_16x16x32_bf16 a[56:59],a[80:83],a[248:251],a[56:59]
s_add_u32 m0,s85,4096
s_xor_b32 s85,s85,98304
ds_read_b64_tr_b16 a[126:127],v50 offset:19208
v_mfma_f32_16x16x32_bf16 a[12:15],a[88:91],a[224:227],a[12:15]
v_mfma_f32_16x16x32_bf16 a[12:15],a[92:95],a[228:231],a[12:15]
buffer_load_dwordx4 v36,s[28:31],s84 offen lds
v_mfma_f32_16x16x32_bf16 a[28:31],a[92:95],a[236:239],a[28:31]
ds_read_b64_tr_b16 a[64:65],v50 offset:1024
v_mfma_f32_16x16x32_bf16 a[28:31],a[88:91],a[232:235],a[28:31]
ds_read_b64_tr_b16 a[66:67],v50 offset:3072
v_mfma_f32_16x16x32_bf16 a[44:47],a[88:91],a[240:243],a[44:47]
ds_read_b64_tr_b16 a[68:69],v50 offset:17408
v_mfma_f32_16x16x32_bf16 a[44:47],a[92:95],a[244:247],a[44:47]
ds_read_b64_tr_b16 a[70:71],v50 offset:19456
v_mfma_f32_16x16x32_bf16 a[60:63],a[92:95],a[252:255],a[60:63]
v_mfma_f32_16x16x32_bf16 a[60:63],a[88:91],a[248:251],a[60:63]
buffer_load_dwordx4 v37,s[28:31],s84 offen offset:1024 lds
v_mfma_f32_16x16x32_bf16 v[64:67],a[96:99],a[224:227],v[64:67]
ds_read_b64_tr_b16 a[72:73],v50 offset:1032
v_mfma_f32_16x16x32_bf16 v[64:67],a[100:103],a[228:231],v[64:67]
ds_read_b64_tr_b16 a[74:75],v50 offset:3080
v_mfma_f32_16x16x32_bf16 v[80:83],a[100:103],a[236:239],v[80:83]
ds_read_b64_tr_b16 a[76:77],v50 offset:17416
v_mfma_f32_16x16x32_bf16 v[80:83],a[96:99],a[232:235],v[80:83]
ds_read_b64_tr_b16 a[78:79],v50 offset:19464
v_mfma_f32_16x16x32_bf16 v[96:99],a[96:99],a[240:243],v[96:99]
v_mfma_f32_16x16x32_bf16 v[96:99],a[100:103],a[244:247],v[96:99]
buffer_load_dwordx4 v38,s[28:31],s84 offen offset:2048 lds
v_mfma_f32_16x16x32_bf16 v[112:115],a[100:103],a[252:255],v[112:115]
ds_read_b64_tr_b16 a[80:81],v50 offset:1280
v_mfma_f32_16x16x32_bf16 v[112:115],a[96:99],a[248:251],v[112:115]
s_waitcnt lgkmcnt(5)
ds_read_b64_tr_b16 a[82:83],v50 offset:3328
v_mfma_f32_16x16x32_bf16 v[68:71],a[104:107],a[224:227],v[68:71]
ds_read_b64_tr_b16 a[84:85],v50 offset:17664
v_mfma_f32_16x16x32_bf16 v[68:71],a[108:111],a[228:231],v[68:71]
ds_read_b64_tr_b16 a[86:87],v50 offset:19712
v_mfma_f32_16x16x32_bf16 v[84:87],a[108:111],a[236:239],v[84:87]
v_mfma_f32_16x16x32_bf16 v[84:87],a[104:107],a[232:235],v[84:87]
buffer_load_dwordx4 v39,s[28:31],s84 offen offset:3072 lds
v_mfma_f32_16x16x32_bf16 v[100:103],a[104:107],a[240:243],v[100:103]
s_add_u32 s84,s84,128
ds_read_b64_tr_b16 a[88:89],v50 offset:1288
v_mfma_f32_16x16x32_bf16 v[100:103],a[108:111],a[244:247],v[100:103]
ds_read_b64_tr_b16 a[90:91],v50 offset:3336
v_mfma_f32_16x16x32_bf16 v[116:119],a[108:111],a[252:255],v[116:119]
ds_read_b64_tr_b16 a[92:93],v50 offset:17672
v_mfma_f32_16x16x32_bf16 v[116:119],a[104:107],a[248:251],v[116:119]
s_add_u32 m0,s88,0
ds_read_b64_tr_b16 a[94:95],v50 offset:19720
v_mfma_f32_16x16x32_bf16 v[72:75],a[112:115],a[224:227],v[72:75]
v_mfma_f32_16x16x32_bf16 v[72:75],a[116:119],a[228:231],v[72:75]
buffer_load_dwordx4 v40,s[36:39],s83 offen lds
v_mfma_f32_16x16x32_bf16 v[88:91],a[116:119],a[236:239],v[88:91]
ds_read_b64_tr_b16 a[96:97],v50 offset:1536
v_mfma_f32_16x16x32_bf16 v[88:91],a[112:115],a[232:235],v[88:91]
ds_read_b64_tr_b16 a[98:99],v50 offset:3584
v_mfma_f32_16x16x32_bf16 v[104:107],a[112:115],a[240:243],v[104:107]
ds_read_b64_tr_b16 a[100:101],v50 offset:17920
v_mfma_f32_16x16x32_bf16 v[104:107],a[116:119],a[244:247],v[104:107]
s_add_u32 m0,s88,1024
ds_read_b64_tr_b16 a[102:103],v50 offset:19968
v_mfma_f32_16x16x32_bf16 v[120:123],a[116:119],a[252:255],v[120:123]
v_mfma_f32_16x16x32_bf16 v[120:123],a[112:115],a[248:251],v[120:123]
buffer_load_dwordx4 v41,s[36:39],s83 offen lds
v_mfma_f32_16x16x32_bf16 v[76:79],a[120:123],a[224:227],v[76:79]
ds_read_b64_tr_b16 a[104:105],v50 offset:1544
v_mfma_f32_16x16x32_bf16 v[76:79],a[124:127],a[228:231],v[76:79]
ds_read_b64_tr_b16 a[106:107],v50 offset:3592
v_mfma_f32_16x16x32_bf16 v[92:95],a[124:127],a[236:239],v[92:95]
ds_read_b64_tr_b16 a[108:109],v50 offset:17928
v_mfma_f32_16x16x32_bf16 v[92:95],a[120:123],a[232:235],v[92:95]
s_add_u32 m0,s88,2048
ds_read_b64_tr_b16 a[110:111],v50 offset:19976
v_mfma_f32_16x16x32_bf16 v[108:111],a[120:123],a[240:243],v[108:111]
v_mfma_f32_16x16x32_bf16 v[108:111],a[124:127],a[244:247],v[108:111]
buffer_load_dwordx4 v42,s[36:39],s83 offen lds
v_mfma_f32_16x16x32_bf16 v[124:127],a[124:127],a[252:255],v[124:127]
ds_read_b64_tr_b16 a[112:113],v50 offset:1792
v_mfma_f32_16x16x32_bf16 v[124:127],a[120:123],a[248:251],v[124:127]
ds_read_b64_tr_b16 a[114:115],v50 offset:3840
v_mfma_f32_16x16x32_bf16 v[128:131],a[64:67],a[224:227],v[128:131]
ds_read_b64_tr_b16 a[116:117],v50 offset:18176
v_mfma_f32_16x16x32_bf16 v[128:131],a[68:71],a[228:231],v[128:131]
s_add_u32 m0,s88,3072
ds_read_b64_tr_b16 a[118:119],v50 offset:20224
v_mfma_f32_16x16x32_bf16 v[160:163],a[68:71],a[236:239],v[160:163]
v_mfma_f32_16x16x32_bf16 v[160:163],a[64:67],a[232:235],v[160:163]
buffer_load_dwordx4 v43,s[36:39],s83 offen lds
v_mfma_f32_16x16x32_bf16 v[192:195],a[64:67],a[240:243],v[192:195]
ds_read_b64_tr_b16 a[120:121],v50 offset:1800
v_mfma_f32_16x16x32_bf16 v[192:195],a[68:71],a[244:247],v[192:195]
ds_read_b64_tr_b16 a[122:123],v50 offset:3848
v_mfma_f32_16x16x32_bf16 v[224:227],a[68:71],a[252:255],v[224:227]
s_waitcnt lgkmcnt(4)
ds_read_b64_tr_b16 a[124:125],v50 offset:18184
v_mfma_f32_16x16x32_bf16 v[224:227],a[64:67],a[248:251],v[224:227]
s_add_u32 m0,s88,4096
ds_read_b64_tr_b16 a[126:127],v50 offset:20232
v_add_u32 v50,s86,v51
v_mfma_f32_16x16x32_bf16 v[132:135],a[72:75],a[224:227],v[132:135]
v_mfma_f32_16x16x32_bf16 v[132:135],a[76:79],a[228:231],v[132:135]
buffer_load_dwordx4 v44,s[36:39],s83 offen lds
v_mfma_f32_16x16x32_bf16 v[164:167],a[76:79],a[236:239],v[164:167]
v_mfma_f32_16x16x32_bf16 v[164:167],a[72:75],a[232:235],v[164:167]
v_mfma_f32_16x16x32_bf16 v[196:199],a[72:75],a[240:243],v[196:199]
v_mfma_f32_16x16x32_bf16 v[196:199],a[76:79],a[244:247],v[196:199]
s_add_u32 m0,s88,5120
v_mfma_f32_16x16x32_bf16 v[228:231],a[76:79],a[252:255],v[228:231]
v_mfma_f32_16x16x32_bf16 v[228:231],a[72:75],a[248:251],v[228:231]
buffer_load_dwordx4 v45,s[36:39],s83 offen lds
v_mfma_f32_16x16x32_bf16 v[136:139],a[80:83],a[224:227],v[136:139]
v_mfma_f32_16x16x32_bf16 v[136:139],a[84:87],a[228:231],v[136:139]
v_mfma_f32_16x16x32_bf16 v[168:171],a[84:87],a[236:239],v[168:171]
v_mfma_f32_16x16x32_bf16 v[168:171],a[80:83],a[232:235],v[168:171]
s_add_u32 m0,s88,6144
v_mfma_f32_16x16x32_bf16 v[200:203],a[80:83],a[240:243],v[200:203]
)A"
R"A(v_mfma_f32_16x16x32_bf16 v[200:203],a[84:87],a[244:247],v[200:203]
buffer_load_dwordx4 v46,s[36:39],s83 offen lds
v_mfma_f32_16x16x32_bf16 v[232:235],a[84:87],a[252:255],v[232:235]
v_mfma_f32_16x16x32_bf16 v[232:235],a[80:83],a[248:251],v[232:235]
v_mfma_f32_16x16x32_bf16 v[140:143],a[88:91],a[224:227],v[140:143]
v_mfma_f32_16x16x32_bf16 v[140:143],a[92:95],a[228:231],v[140:143]
s_add_u32 m0,s88,7168
s_mov_b32 s88,s69
s_and_b32 s69,s75,0xfffc0000
v_mfma_f32_16x16x32_bf16 v[172:175],a[92:95],a[236:239],v[172:175]
s_add_u32 s75,s75,16384
v_mfma_f32_16x16x32_bf16 v[172:175],a[88:91],a[232:235],v[172:175]
buffer_load_dwordx4 v47,s[36:39],s83 offen lds
v_mfma_f32_16x16x32_bf16 v[204:207],a[88:91],a[240:243],v[204:207]
s_waitcnt lgkmcnt(0)
s_add_u32 s83,s83,524288
s_sub_u32 s92,s92,s8
v_mfma_f32_16x16x32_bf16 v[204:207],a[92:95],a[244:247],v[204:207]
s_sub_u32 s56,s56,s4
s_mov_b32 s93,782
s_mul_i32 s92,s92,12600
v_mfma_f32_16x16x32_bf16 v[236:239],a[92:95],a[252:255],v[236:239]
s_cmp_ge_u32 s63,2
s_cselect_b32 s93,823,s93
s_mul_i32 s93,s93,s56
v_mfma_f32_16x16x32_bf16 v[236:239],a[88:91],a[248:251],v[236:239]
s_add_u32 s10,s20,s74
s_addc_u32 s11,s21,0
s_waitcnt vmcnt(25)
s_barrier
v_mfma_f32_16x16x32_bf16 v[144:147],a[96:99],a[224:227],v[144:147]
s_add_u32 m0,s76,0
s_waitcnt vmcnt(17)
ds_read_b128 a[192:195],v48
v_mfma_f32_16x16x32_bf16 v[144:147],a[100:103],a[228:231],v[144:147]
ds_read_b128 a[196:199],v49
v_mfma_f32_16x16x32_bf16 v[176:179],a[100:103],a[236:239],v[176:179]
global_load_lds_dwordx4 v4,s[10:11] nt
ds_read_b128 a[200:203],v48 offset:2048
v_mfma_f32_16x16x32_bf16 v[176:179],a[96:99],a[232:235],v[176:179]
ds_read_b128 a[204:207],v49 offset:2048
v_mfma_f32_16x16x32_bf16 v[208:211],a[96:99],a[240:243],v[208:211]
v_mfma_f32_16x16x32_bf16 v[208:211],a[100:103],a[244:247],v[208:211]
ds_read_b128 a[208:211],v48 offset:4096
v_mfma_f32_16x16x32_bf16 v[240:243],a[100:103],a[252:255],v[240:243]
ds_read_b128 a[212:215],v49 offset:4096
v_mfma_f32_16x16x32_bf16 v[240:243],a[96:99],a[248:251],v[240:243]
ds_read_b128 a[216:219],v48 offset:6144
v_xor_b32 v48,98304,v48
v_mfma_f32_16x16x32_bf16 v[148:151],a[104:107],a[224:227],v[148:151]
v_mfma_f32_16x16x32_bf16 v[148:151],a[108:111],a[228:231],v[148:151]
ds_read_b128 a[220:223],v49 offset:6144
v_xor_b32 v49,98304,v49
v_mfma_f32_16x16x32_bf16 v[180:183],a[108:111],a[236:239],v[180:183]
ds_read_b64_tr_b16 a[64:65],v50
v_mfma_f32_16x16x32_bf16 v[180:183],a[104:107],a[232:235],v[180:183]
ds_read_b64_tr_b16 a[66:67],v50 offset:2048
v_mfma_f32_16x16x32_bf16 v[212:215],a[104:107],a[240:243],v[212:215]
v_mfma_f32_16x16x32_bf16 v[212:215],a[108:111],a[244:247],v[212:215]
ds_read_b64_tr_b16 a[68:69],v50 offset:16384
v_mfma_f32_16x16x32_bf16 v[244:247],a[108:111],a[252:255],v[244:247]
ds_read_b64_tr_b16 a[70:71],v50 offset:18432
v_mfma_f32_16x16x32_bf16 v[244:247],a[104:107],a[248:251],v[244:247]
ds_read_b64_tr_b16 a[72:73],v50 offset:8
v_mfma_f32_16x16x32_bf16 v[152:155],a[112:115],a[224:227],v[152:155]
v_mfma_f32_16x16x32_bf16 v[152:155],a[116:119],a[228:231],v[152:155]
ds_read_b64_tr_b16 a[74:75],v50 offset:2056
v_mfma_f32_16x16x32_bf16 v[184:187],a[116:119],a[236:239],v[184:187]
global_load_lds_dwordx4 v4,s[10:11] offset:1024 nt
ds_read_b64_tr_b16 a[76:77],v50 offset:16392
v_mfma_f32_16x16x32_bf16 v[184:187],a[112:115],a[232:235],v[184:187]
ds_read_b64_tr_b16 a[78:79],v50 offset:18440
v_mfma_f32_16x16x32_bf16 v[216:219],a[112:115],a[240:243],v[216:219]
v_mfma_f32_16x16x32_bf16 v[216:219],a[116:119],a[244:247],v[216:219]
ds_read_b64_tr_b16 a[80:81],v50 offset:1024
v_mfma_f32_16x16x32_bf16 v[248:251],a[116:119],a[252:255],v[248:251]
ds_read_b64_tr_b16 a[82:83],v50 offset:3072
v_mfma_f32_16x16x32_bf16 v[248:251],a[112:115],a[248:251],v[248:251]
ds_read_b64_tr_b16 a[84:85],v50 offset:17408
v_mfma_f32_16x16x32_bf16 v[156:159],a[120:123],a[224:227],v[156:159]
v_mfma_f32_16x16x32_bf16 v[156:159],a[124:127],a[228:231],v[156:159]
ds_read_b64_tr_b16 a[86:87],v50 offset:19456
v_mfma_f32_16x16x32_bf16 v[188:191],a[124:127],a[236:239],v[188:191]
ds_read_b64_tr_b16 a[88:89],v50 offset:1032
v_mfma_f32_16x16x32_bf16 v[188:191],a[120:123],a[232:235],v[188:191]
s_cmp_lt_u32 s92,s93
s_cselect_b32 s5,6,0
s_cselect_b32 s7,3,0
s_cselect_b32 s9,8,10
ds_read_b64_tr_b16 a[90:91],v50 offset:3080
v_mfma_f32_16x16x32_bf16 v[220:223],a[120:123],a[240:243],v[220:223]
s_mov_b32 s4,s5
v_mfma_f32_16x16x32_bf16 v[220:223],a[124:127],a[244:247],v[220:223]
ds_read_b64_tr_b16 a[92:93],v50 offset:17416
v_mfma_f32_16x16x32_bf16 v[252:255],a[124:127],a[252:255],v[252:255]
ds_read_b64_tr_b16 a[94:95],v50 offset:19464
v_mfma_f32_16x16x32_bf16 v[252:255],a[120:123],a[248:251],v[252:255]
s_waitcnt vmcnt(10) lgkmcnt(5)
ds_read_b128 a[224:227],v48
v_mfma_f32_16x16x32_bf16 v[128:131],a[64:67],a[192:195],v[128:131]
v_mfma_f32_16x16x32_bf16 v[128:131],a[68:71],a[196:199],v[128:131]
ds_read_b128 a[228:231],v49
v_mfma_f32_16x16x32_bf16 v[160:163],a[68:71],a[204:207],v[160:163]
global_load_lds_dwordx4 v5,s[10:11] offset:2048 nt
ds_read_b128 a[236:239],v49 offset:2048
v_mfma_f32_16x16x32_bf16 v[160:163],a[64:67],a[200:203],v[160:163]
ds_read_b128 a[232:235],v48 offset:2048
v_mfma_f32_16x16x32_bf16 v[192:195],a[64:67],a[208:211],v[192:195]
v_mfma_f32_16x16x32_bf16 v[192:195],a[68:71],a[212:215],v[192:195]
ds_read_b128 a[240:243],v48 offset:4096
v_mfma_f32_16x16x32_bf16 v[224:227],a[68:71],a[220:223],v[224:227]
ds_read_b128 a[244:247],v49 offset:4096
v_mfma_f32_16x16x32_bf16 v[224:227],a[64:67],a[216:219],v[224:227]
ds_read_b128 a[252:255],v49 offset:6144
v_mfma_f32_16x16x32_bf16 v[132:135],a[72:75],a[192:195],v[132:135]
v_mfma_f32_16x16x32_bf16 v[132:135],a[76:79],a[196:199],v[132:135]
ds_read_b128 a[248:251],v48 offset:6144
v_mfma_f32_16x16x32_bf16 v[164:167],a[76:79],a[204:207],v[164:167]
ds_read_b64_tr_b16 a[96:97],v50 offset:256
v_mfma_f32_16x16x32_bf16 v[164:167],a[72:75],a[200:203],v[164:167]
ds_read_b64_tr_b16 a[98:99],v50 offset:2304
v_mfma_f32_16x16x32_bf16 v[196:199],a[72:75],a[208:211],v[196:199]
v_mfma_f32_16x16x32_bf16 v[196:199],a[76:79],a[212:215],v[196:199]
ds_read_b64_tr_b16 a[100:101],v50 offset:16640
v_mfma_f32_16x16x32_bf16 v[228:231],a[76:79],a[220:223],v[228:231]
ds_read_b64_tr_b16 a[102:103],v50 offset:18688
v_mfma_f32_16x16x32_bf16 v[228:231],a[72:75],a[216:219],v[228:231]
s_waitcnt lgkmcnt(5)
ds_read_b64_tr_b16 a[104:105],v50 offset:1280
v_mfma_f32_16x16x32_bf16 v[128:131],a[80:83],a[224:227],v[128:131]
v_mfma_f32_16x16x32_bf16 v[128:131],a[84:87],a[228:231],v[128:131]
ds_read_b64_tr_b16 a[106:107],v50 offset:3328
v_mfma_f32_16x16x32_bf16 v[160:163],a[84:87],a[236:239],v[160:163]
global_load_lds_dwordx4 v5,s[10:11] offset:3072
ds_read_b64_tr_b16 a[108:109],v50 offset:17664
v_mfma_f32_16x16x32_bf16 v[160:163],a[80:83],a[232:235],v[160:163]
ds_read_b64_tr_b16 a[110:111],v50 offset:19712
v_mfma_f32_16x16x32_bf16 v[192:195],a[80:83],a[240:243],v[192:195]
v_mfma_f32_16x16x32_bf16 v[192:195],a[84:87],a[244:247],v[192:195]
ds_read_b64_tr_b16 a[112:113],v50 offset:264
v_mul_f32 v128,s71,v128
v_mfma_f32_16x16x32_bf16 v[224:227],a[84:87],a[252:255],v[224:227]
s_waitcnt lgkmcnt(5)
ds_read_b64_tr_b16 a[114:115],v50 offset:2312
v_mul_f32 v129,s71,v129
v_mfma_f32_16x16x32_bf16 v[224:227],a[80:83],a[248:251],v[224:227]
ds_read_b64_tr_b16 a[116:117],v50 offset:16648
v_mul_f32 v130,s71,v130
v_mfma_f32_16x16x32_bf16 v[132:135],a[88:91],a[224:227],v[132:135]
v_mul_f32 v131,s71,v131
v_cvt_pk_bf16_f32 v128,v128,v129
v_mfma_f32_16x16x32_bf16 v[132:135],a[92:95],a[228:231],v[132:135]
ds_read_b64_tr_b16 a[118:119],v50 offset:18696
v_cvt_pk_bf16_f32 v129,v130,v131
v_mfma_f32_16x16x32_bf16 v[164:167],a[92:95],a[236:239],v[164:167]
ds_read_b64_tr_b16 a[120:121],v50 offset:1288
v_mul_f32 v192,s71,v192
v_mfma_f32_16x16x32_bf16 v[164:167],a[88:91],a[232:235],v[164:167]
ds_read_b64_tr_b16 a[122:123],v50 offset:3336
v_mul_f32 v193,s71,v193
v_mfma_f32_16x16x32_bf16 v[196:199],a[88:91],a[240:243],v[196:199]
v_mul_f32 v132,s71,v132
v_mul_f32 v133,s71,v133
v_mfma_f32_16x16x32_bf16 v[196:199],a[92:95],a[244:247],v[196:199]
ds_read_b64_tr_b16 a[124:125],v50 offset:17672
v_mul_f32 v134,s71,v134
v_mfma_f32_16x16x32_bf16 v[228:231],a[92:95],a[252:255],v[228:231]
s_add_u32 s10,s24,s74
s_addc_u32 s11,s25,0
ds_read_b64_tr_b16 a[126:127],v50 offset:19720
v_mfma_f32_16x16x32_bf16 v[228:231],a[88:91],a[248:251],v[228:231]
s_add_u32 s74,s74,16384
s_and_b32 s74,s74,0x3ffff
ds_read_b64_tr_b16 a[128:129],v50 offset:512
v_mfma_f32_16x16x32_bf16 v[136:139],a[96:99],a[192:195],v[136:139]
s_add_u32 m0,s76,16384
s_or_b32 s74,s74,s69
s_sub_u32 s76,s76,32768
v_mfma_f32_16x16x32_bf16 v[136:139],a[100:103],a[196:199],v[136:139]
s_and_b32 s76,s76,0x1ffff
s_and_b32 s69,s75,0xfffc0000
ds_read_b64_tr_b16 a[130:131],v50 offset:2560
s_waitcnt lgkmcnt(6)
v_mfma_f32_16x16x32_bf16 v[136:139],a[104:107],a[224:227],v[136:139]
global_load_lds_dwordx4 v4,s[10:11]
ds_read_b64_tr_b16 a[132:133],v50 offset:16896
v_mfma_f32_16x16x32_bf16 v[136:139],a[108:111],a[228:231],v[136:139]
s_add_u32 s75,s75,16384
ds_read_b64_tr_b16 a[134:135],v50 offset:18944
v_mul_f32 v135,s71,v135
v_mfma_f32_16x16x32_bf16 v[168:171],a[108:111],a[236:239],v[168:171]
v_cvt_pk_bf16_f32 v130,v132,v133
v_cvt_pk_bf16_f32 v131,v134,v135
v_mfma_f32_16x16x32_bf16 v[168:171],a[104:107],a[232:235],v[168:171]
ds_read_b64_tr_b16 a[136:137],v50 offset:1536
v_mul_f32 v194,s71,v194
v_mfma_f32_16x16x32_bf16 v[168:171],a[100:103],a[204:207],v[168:171]
ds_read_b64_tr_b16 a[138:139],v50 offset:3584
v_mul_f32 v195,s71,v195
v_mfma_f32_16x16x32_bf16 v[168:171],a[96:99],a[200:203],v[168:171]
ds_read_b64_tr_b16 a[140:141],v50 offset:17920
v_mul_f32 v196,s71,v196
v_mfma_f32_16x16x32_bf16 v[200:203],a[96:99],a[208:211],v[200:203]
v_mul_f32 v197,s71,v197
v_mul_f32 v198,s71,v198
v_mfma_f32_16x16x32_bf16 v[200:203],a[100:103],a[212:215],v[200:203]
ds_read_b64_tr_b16 a[142:143],v50 offset:19968
v_mul_f32 v199,s71,v199
v_mfma_f32_16x16x32_bf16 v[200:203],a[104:107],a[240:243],v[200:203]
ds_read_b64_tr_b16 a[144:145],v50 offset:520
v_cvt_pk_bf16_f32 v192,v192,v193
v_mfma_f32_16x16x32_bf16 v[200:203],a[108:111],a[244:247],v[200:203]
ds_read_b64_tr_b16 a[146:147],v50 offset:2568
v_cvt_pk_bf16_f32 v193,v194,v195
v_mfma_f32_16x16x32_bf16 v[232:235],a[108:111],a[252:255],v[232:235]
v_cvt_pk_bf16_f32 v194,v196,v197
v_cvt_pk_bf16_f32 v195,v198,v199
v_mfma_f32_16x16x32_bf16 v[232:235],a[104:107],a[248:251],v[232:235]
ds_read_b64_tr_b16 a[148:149],v50 offset:16904
v_mul_f32 v136,s71,v136
v_mfma_f32_16x16x32_bf16 v[232:235],a[100:103],a[220:223],v[232:235]
ds_read_b64_tr_b16 a[150:151],v50 offset:18952
v_mul_f32 v137,s71,v137
v_mfma_f32_16x16x32_bf16 v[232:235],a[96:99],a[216:219],v[232:235]
ds_read_b64_tr_b16 a[152:153],v50 offset:1544
v_mul_f32 v138,s71,v138
v_mfma_f32_16x16x32_bf16 v[140:143],a[112:115],a[192:195],v[140:143]
v_mul_f32 v139,s71,v139
v_cvt_pk_bf16_f32 v136,v136,v137
v_mfma_f32_16x16x32_bf16 v[140:143],a[116:119],a[196:199],v[140:143]
s_waitcnt lgkmcnt(5)
ds_read_b64_tr_b16 a[154:155],v50 offset:3592
v_cvt_pk_bf16_f32 v137,v138,v139
v_mfma_f32_16x16x32_bf16 v[140:143],a[120:123],a[224:227],v[140:143]
global_load_lds_dwordx4 v5,s[10:11] offset:1024
ds_read_b64_tr_b16 a[156:157],v50 offset:17928
v_mfma_f32_16x16x32_bf16 v[140:143],a[124:127],a[228:231],v[140:143]
ds_read_b64_tr_b16 a[158:159],v50 offset:19976
v_mul_f32 v160,s71,v160
v_mfma_f32_16x16x32_bf16 v[172:175],a[124:127],a[236:239],v[172:175]
v_mul_f32 v161,s71,v161
v_mul_f32 v162,s71,v162
v_mfma_f32_16x16x32_bf16 v[172:175],a[120:123],a[232:235],v[172:175]
ds_read_b64_tr_b16 a[160:161],v50 offset:768
v_mul_f32 v163,s71,v163
v_mfma_f32_16x16x32_bf16 v[172:175],a[116:119],a[204:207],v[172:175]
ds_read_b64_tr_b16 a[162:163],v50 offset:2816
v_mul_f32 v140,s71,v140
v_mfma_f32_16x16x32_bf16 v[172:175],a[112:115],a[200:203],v[172:175]
ds_read_b64_tr_b16 a[164:165],v50 offset:17152
v_mul_f32 v141,s71,v141
v_mfma_f32_16x16x32_bf16 v[204:207],a[112:115],a[208:211],v[204:207]
v_mul_f32 v142,s71,v142
v_mul_f32 v143,s71,v143
v_mfma_f32_16x16x32_bf16 v[204:207],a[116:119],a[212:215],v[204:207]
)A"
R"A(ds_read_b64_tr_b16 a[166:167],v50 offset:19200
v_cvt_pk_bf16_f32 v138,v140,v141
v_mfma_f32_16x16x32_bf16 v[204:207],a[120:123],a[240:243],v[204:207]
ds_read_b64_tr_b16 a[168:169],v50 offset:1792
v_cvt_pk_bf16_f32 v139,v142,v143
v_mfma_f32_16x16x32_bf16 v[204:207],a[124:127],a[244:247],v[204:207]
ds_read_b64_tr_b16 a[170:171],v50 offset:3840
v_mul_f32 v164,s71,v164
v_mfma_f32_16x16x32_bf16 v[236:239],a[124:127],a[252:255],v[236:239]
v_mul_f32 v165,s71,v165
v_mul_f32 v166,s71,v166
v_mfma_f32_16x16x32_bf16 v[236:239],a[120:123],a[248:251],v[236:239]
ds_read_b64_tr_b16 a[172:173],v50 offset:18176
v_mul_f32 v167,s71,v167
v_mfma_f32_16x16x32_bf16 v[236:239],a[116:119],a[220:223],v[236:239]
ds_read_b64_tr_b16 a[174:175],v50 offset:20224
v_cvt_pk_bf16_f32 v160,v160,v161
v_mfma_f32_16x16x32_bf16 v[236:239],a[112:115],a[216:219],v[236:239]
ds_read_b64_tr_b16 a[176:177],v50 offset:776
v_cvt_pk_bf16_f32 v161,v162,v163
v_mfma_f32_16x16x32_bf16 v[144:147],a[128:131],a[192:195],v[144:147]
v_cvt_pk_bf16_f32 v162,v164,v165
v_cvt_pk_bf16_f32 v163,v166,v167
v_mfma_f32_16x16x32_bf16 v[144:147],a[132:135],a[196:199],v[144:147]
ds_read_b64_tr_b16 a[178:179],v50 offset:2824
v_mul_f32 v224,s71,v224
v_mfma_f32_16x16x32_bf16 v[144:147],a[136:139],a[224:227],v[144:147]
global_load_lds_dwordx4 v4,s[10:11] offset:2048
ds_read_b64_tr_b16 a[180:181],v50 offset:17160
v_mfma_f32_16x16x32_bf16 v[144:147],a[140:143],a[228:231],v[144:147]
ds_read_b64_tr_b16 a[182:183],v50 offset:19208
v_mul_f32 v225,s71,v225
v_mfma_f32_16x16x32_bf16 v[176:179],a[140:143],a[236:239],v[176:179]
v_mul_f32 v226,s71,v226
v_mul_f32 v227,s71,v227
v_mfma_f32_16x16x32_bf16 v[176:179],a[136:139],a[232:235],v[176:179]
ds_read_b64_tr_b16 a[184:185],v50 offset:1800
v_mul_f32 v228,s71,v228
v_mfma_f32_16x16x32_bf16 v[176:179],a[132:135],a[204:207],v[176:179]
ds_read_b64_tr_b16 a[186:187],v50 offset:3848
v_mul_f32 v144,s71,v144
v_mfma_f32_16x16x32_bf16 v[176:179],a[128:131],a[200:203],v[176:179]
ds_read_b64_tr_b16 a[188:189],v50 offset:18184
v_mul_f32 v145,s71,v145
v_mfma_f32_16x16x32_bf16 v[208:211],a[128:131],a[208:211],v[208:211]
v_mul_f32 v146,s71,v146
v_mul_f32 v147,s71,v147
v_mfma_f32_16x16x32_bf16 v[208:211],a[132:135],a[212:215],v[208:211]
ds_read_b64_tr_b16 a[190:191],v50 offset:20232
v_cvt_pk_bf16_f32 v144,v144,v145
v_mfma_f32_16x16x32_bf16 v[208:211],a[136:139],a[240:243],v[208:211]
v_cvt_pk_bf16_f32 v145,v146,v147
v_mul_f32 v229,s71,v229
v_mfma_f32_16x16x32_bf16 v[208:211],a[140:143],a[244:247],v[208:211]
v_mul_f32 v230,s71,v230
v_mul_f32 v231,s71,v231
v_mfma_f32_16x16x32_bf16 v[240:243],a[140:143],a[252:255],v[240:243]
v_cvt_pk_bf16_f32 v224,v224,v225
v_cvt_pk_bf16_f32 v225,v226,v227
v_mfma_f32_16x16x32_bf16 v[240:243],a[136:139],a[248:251],v[240:243]
v_cvt_pk_bf16_f32 v226,v228,v229
v_cvt_pk_bf16_f32 v227,v230,v231
v_mfma_f32_16x16x32_bf16 v[240:243],a[132:135],a[220:223],v[240:243]
v_mul_f32 v168,s71,v168
v_mul_f32 v169,s71,v169
v_mfma_f32_16x16x32_bf16 v[240:243],a[128:131],a[216:219],v[240:243]
v_mul_f32 v170,s71,v170
v_mul_f32 v171,s71,v171
s_waitcnt vmcnt(7)
s_barrier
s_waitcnt lgkmcnt(1)
v_mfma_f32_16x16x32_bf16 v[148:151],a[144:147],a[192:195],v[148:151]
ds_read_b64_tr_b16 a[64:65],v52
v_mul_f32 v172,s71,v172
v_mfma_f32_16x16x32_bf16 v[148:151],a[148:151],a[196:199],v[148:151]
global_load_lds_dwordx4 v5,s[10:11] offset:3072
ds_read_b64_tr_b16 a[66:67],v52 offset:2048
v_mfma_f32_16x16x32_bf16 v[148:151],a[152:155],a[224:227],v[148:151]
ds_read_b64_tr_b16 a[68:69],v52 offset:16384
v_mul_f32 v173,s71,v173
v_mfma_f32_16x16x32_bf16 v[148:151],a[156:159],a[228:231],v[148:151]
L_pd3_%=:
s_cbranch_execz L_pd7_%=
ds_read_b64_tr_b16 a[70:71],v52 offset:18432
v_mul_f32 v174,s71,v174
v_mfma_f32_16x16x32_bf16 v[180:183],a[156:159],a[236:239],v[180:183]
v_mul_f32 v175,s71,v175
v_cvt_pk_bf16_f32 v168,v168,v169
v_mfma_f32_16x16x32_bf16 v[180:183],a[152:155],a[232:235],v[180:183]
ds_read_b64_tr_b16 a[72:73],v52 offset:1024
v_cvt_pk_bf16_f32 v169,v170,v171
v_mfma_f32_16x16x32_bf16 v[180:183],a[148:151],a[204:207],v[180:183]
ds_read_b64_tr_b16 a[74:75],v52 offset:3072
v_mul_f32 v148,s71,v148
v_mfma_f32_16x16x32_bf16 v[180:183],a[144:147],a[200:203],v[180:183]
ds_read_b64_tr_b16 a[76:77],v52 offset:17408
v_mul_f32 v149,s71,v149
v_mfma_f32_16x16x32_bf16 v[212:215],a[144:147],a[208:211],v[212:215]
v_mul_f32 v150,s71,v150
v_mul_f32 v151,s71,v151
v_mfma_f32_16x16x32_bf16 v[212:215],a[148:151],a[212:215],v[212:215]
ds_read_b64_tr_b16 a[78:79],v52 offset:19456
v_cvt_pk_bf16_f32 v146,v148,v149
v_mfma_f32_16x16x32_bf16 v[212:215],a[152:155],a[240:243],v[212:215]
ds_read_b64_tr_b16 a[80:81],v52 offset:8
v_cvt_pk_bf16_f32 v147,v150,v151
v_mfma_f32_16x16x32_bf16 v[212:215],a[156:159],a[244:247],v[212:215]
ds_read_b64_tr_b16 a[82:83],v52 offset:2056
v_cvt_pk_bf16_f32 v170,v172,v173
v_mfma_f32_16x16x32_bf16 v[244:247],a[156:159],a[252:255],v[244:247]
v_cvt_pk_bf16_f32 v171,v174,v175
v_mul_f32 v176,s71,v176
v_mfma_f32_16x16x32_bf16 v[244:247],a[152:155],a[248:251],v[244:247]
ds_read_b64_tr_b16 a[84:85],v52 offset:16392
v_mul_f32 v177,s71,v177
v_mfma_f32_16x16x32_bf16 v[244:247],a[148:151],a[220:223],v[244:247]
s_add_u32 s10,s20,s74
s_addc_u32 s11,s21,0
ds_read_b64_tr_b16 a[86:87],v52 offset:18440
v_mfma_f32_16x16x32_bf16 v[244:247],a[144:147],a[216:219],v[244:247]
ds_read_b64_tr_b16 a[88:89],v52 offset:1032
v_mul_f32 v178,s71,v178
v_mfma_f32_16x16x32_bf16 v[152:155],a[160:163],a[192:195],v[152:155]
s_add_u32 m0,s76,0
v_mul_f32 v179,s71,v179
v_mul_f32 v180,s71,v180
v_mfma_f32_16x16x32_bf16 v[152:155],a[164:167],a[196:199],v[152:155]
ds_read_b64_tr_b16 a[90:91],v52 offset:3080
v_mul_f32 v181,s71,v181
v_mfma_f32_16x16x32_bf16 v[152:155],a[168:171],a[224:227],v[152:155]
global_load_lds_dwordx4 v4,s[10:11] nt
ds_read_b64_tr_b16 a[92:93],v52 offset:17416
v_mfma_f32_16x16x32_bf16 v[152:155],a[172:175],a[228:231],v[152:155]
ds_read_b64_tr_b16 a[94:95],v52 offset:19464
v_mul_f32 v182,s71,v182
v_mfma_f32_16x16x32_bf16 v[184:187],a[172:175],a[236:239],v[184:187]
v_mul_f32 v183,s71,v183
v_cvt_pk_bf16_f32 v176,v176,v177
v_mfma_f32_16x16x32_bf16 v[184:187],a[168:171],a[232:235],v[184:187]
ds_read_b64_tr_b16 a[96:97],v52 offset:256
v_cvt_pk_bf16_f32 v177,v178,v179
v_mfma_f32_16x16x32_bf16 v[184:187],a[164:167],a[204:207],v[184:187]
ds_read_b64_tr_b16 a[98:99],v52 offset:2304
v_mul_f32 v152,s71,v152
v_mfma_f32_16x16x32_bf16 v[184:187],a[160:163],a[200:203],v[184:187]
ds_read_b64_tr_b16 a[100:101],v52 offset:16640
v_mul_f32 v153,s71,v153
v_mfma_f32_16x16x32_bf16 v[216:219],a[160:163],a[208:211],v[216:219]
v_mul_f32 v154,s71,v154
v_mul_f32 v155,s71,v155
v_mfma_f32_16x16x32_bf16 v[216:219],a[164:167],a[212:215],v[216:219]
ds_read_b64_tr_b16 a[102:103],v52 offset:18688
v_cvt_pk_bf16_f32 v152,v152,v153
v_mfma_f32_16x16x32_bf16 v[216:219],a[168:171],a[240:243],v[216:219]
ds_read_b64_tr_b16 a[104:105],v52 offset:1280
v_cvt_pk_bf16_f32 v153,v154,v155
v_mfma_f32_16x16x32_bf16 v[216:219],a[172:175],a[244:247],v[216:219]
ds_read_b64_tr_b16 a[106:107],v52 offset:3328
v_cvt_pk_bf16_f32 v178,v180,v181
v_mfma_f32_16x16x32_bf16 v[248:251],a[172:175],a[252:255],v[248:251]
v_cvt_pk_bf16_f32 v179,v182,v183
v_mul_f32 v184,s71,v184
v_mfma_f32_16x16x32_bf16 v[248:251],a[168:171],a[248:251],v[248:251]
ds_read_b64_tr_b16 a[108:109],v52 offset:17664
v_mul_f32 v185,s71,v185
v_mfma_f32_16x16x32_bf16 v[248:251],a[164:167],a[220:223],v[248:251]
ds_read_b64_tr_b16 a[110:111],v52 offset:19712
v_mul_f32 v186,s71,v186
v_mfma_f32_16x16x32_bf16 v[248:251],a[160:163],a[216:219],v[248:251]
ds_read_b64_tr_b16 a[112:113],v52 offset:264
v_mul_f32 v187,s71,v187
v_mfma_f32_16x16x32_bf16 v[156:159],a[176:179],a[192:195],v[156:159]
v_cvt_pk_bf16_f32 v184,v184,v185
v_cvt_pk_bf16_f32 v185,v186,v187
v_mfma_f32_16x16x32_bf16 v[156:159],a[180:183],a[196:199],v[156:159]
global_load_lds_dwordx4 v4,s[10:11] offset:1024 nt
ds_read_b64_tr_b16 a[114:115],v52 offset:2312
v_mfma_f32_16x16x32_bf16 v[156:159],a[184:187],a[224:227],v[156:159]
s_waitcnt lgkmcnt(5)
ds_read_b64_tr_b16 a[116:117],v52 offset:16648
v_mul_f32 v200,s71,v200
v_mfma_f32_16x16x32_bf16 v[156:159],a[188:191],a[228:231],v[156:159]
ds_read_b64_tr_b16 a[118:119],v52 offset:18696
v_mul_f32 v201,s71,v201
v_mfma_f32_16x16x32_bf16 v[188:191],a[188:191],a[236:239],v[188:191]
v_mul_f32 v202,s71,v202
v_mul_f32 v203,s71,v203
v_mfma_f32_16x16x32_bf16 v[188:191],a[184:187],a[232:235],v[188:191]
ds_read_b64_tr_b16 a[120:121],v52 offset:1288
v_mul_f32 v204,s71,v204
v_mfma_f32_16x16x32_bf16 v[188:191],a[180:183],a[204:207],v[188:191]
ds_read_b64_tr_b16 a[122:123],v52 offset:3336
v_mul_f32 v156,s71,v156
v_mfma_f32_16x16x32_bf16 v[188:191],a[176:179],a[200:203],v[188:191]
ds_read_b64_tr_b16 a[124:125],v52 offset:17672
v_mul_f32 v157,s71,v157
v_mfma_f32_16x16x32_bf16 v[220:223],a[176:179],a[208:211],v[220:223]
v_mul_f32 v158,s71,v158
v_mul_f32 v159,s71,v159
v_mfma_f32_16x16x32_bf16 v[220:223],a[180:183],a[212:215],v[220:223]
ds_read_b64_tr_b16 a[126:127],v52 offset:19720
v_cvt_pk_bf16_f32 v154,v156,v157
v_mfma_f32_16x16x32_bf16 v[220:223],a[184:187],a[240:243],v[220:223]
v_cvt_pk_bf16_f32 v155,v158,v159
v_mul_f32 v188,s71,v188
v_mfma_f32_16x16x32_bf16 v[220:223],a[188:191],a[244:247],v[220:223]
v_mul_f32 v189,s71,v189
v_mul_f32 v190,s71,v190
v_mfma_f32_16x16x32_bf16 v[252:255],a[188:191],a[252:255],v[252:255]
v_mul_f32 v191,s71,v191
v_cvt_pk_bf16_f32 v186,v188,v189
v_mfma_f32_16x16x32_bf16 v[252:255],a[184:187],a[248:251],v[252:255]
v_cvt_pk_bf16_f32 v187,v190,v191
v_mul_f32 v205,s71,v205
v_mfma_f32_16x16x32_bf16 v[252:255],a[180:183],a[220:223],v[252:255]
v_mul_f32 v206,s71,v206
v_mul_f32 v207,s71,v207
v_mfma_f32_16x16x32_bf16 v[252:255],a[176:179],a[216:219],v[252:255]
v_cvt_pk_bf16_f32 v200,v200,v201
v_cvt_pk_bf16_f32 v201,v202,v203
v_mfma_f32_16x16x32_bf16 a[0:3],a[64:67],a[192:195],a[0:3]
global_load_lds_dwordx4 v5,s[10:11] offset:2048 nt
v_cvt_pk_bf16_f32 v202,v204,v205
v_mfma_f32_16x16x32_bf16 a[0:3],a[68:71],a[196:199],a[0:3]
v_cvt_pk_bf16_f32 v203,v206,v207
v_mul_f32 v208,s71,v208
v_mfma_f32_16x16x32_bf16 a[0:3],a[72:75],a[224:227],a[0:3]
v_mul_f32 v209,s71,v209
v_mul_f32 v210,s71,v210
v_mfma_f32_16x16x32_bf16 a[0:3],a[76:79],a[228:231],a[0:3]
v_mul_f32 v211,s71,v211
v_mul_f32 v212,s71,v212
v_mfma_f32_16x16x32_bf16 a[16:19],a[76:79],a[236:239],a[16:19]
v_mul_f32 v213,s71,v213
v_mul_f32 v214,s71,v214
v_mfma_f32_16x16x32_bf16 a[16:19],a[72:75],a[232:235],a[16:19]
v_mul_f32 v215,s71,v215
v_cvt_pk_bf16_f32 v208,v208,v209
v_mfma_f32_16x16x32_bf16 a[16:19],a[68:71],a[204:207],a[16:19]
v_accvgpr_read_b32 v8,a0
v_accvgpr_read_b32 v9,a1
v_mfma_f32_16x16x32_bf16 a[16:19],a[64:67],a[200:203],a[16:19]
v_accvgpr_read_b32 v10,a2
v_accvgpr_read_b32 v11,a3
v_mfma_f32_16x16x32_bf16 a[32:35],a[64:67],a[208:211],a[32:35]
v_mul_f32 v8,s71,v8
v_mul_f32 v9,s71,v9
v_mfma_f32_16x16x32_bf16 a[32:35],a[68:71],a[212:215],a[32:35]
v_mul_f32 v10,s71,v10
v_mul_f32 v11,s71,v11
v_mfma_f32_16x16x32_bf16 a[32:35],a[72:75],a[240:243],a[32:35]
v_cvt_pk_bf16_f32 v132,v8,v9
v_cvt_pk_bf16_f32 v133,v10,v11
v_mfma_f32_16x16x32_bf16 a[32:35],a[76:79],a[244:247],a[32:35]
v_accvgpr_read_b32 v16,a16
v_accvgpr_read_b32 v17,a17
v_mfma_f32_16x16x32_bf16 a[48:51],a[76:79],a[252:255],a[48:51]
ds_read_b64_tr_b16 a[76:77],v52 offset:17920
v_accvgpr_read_b32 v18,a18
v_mfma_f32_16x16x32_bf16 a[48:51],a[72:75],a[248:251],a[48:51]
ds_read_b64_tr_b16 a[72:73],v52 offset:1536
v_accvgpr_read_b32 v19,a19
v_mfma_f32_16x16x32_bf16 a[48:51],a[68:71],a[220:223],a[48:51]
ds_read_b64_tr_b16 a[68:69],v52 offset:16896
v_accvgpr_read_b32 v8,a32
v_mfma_f32_16x16x32_bf16 a[48:51],a[64:67],a[216:219],a[48:51]
global_load_lds_dwordx4 v5,s[10:11] offset:3072
ds_read_b64_tr_b16 a[64:65],v52 offset:512
v_mfma_f32_16x16x32_bf16 a[4:7],a[80:83],a[192:195],a[4:7]
v_accvgpr_read_b32 v9,a33
v_accvgpr_read_b32 v10,a34
v_mfma_f32_16x16x32_bf16 a[4:7],a[84:87],a[196:199],a[4:7]
ds_read_b64_tr_b16 a[66:67],v52 offset:2560
v_accvgpr_read_b32 v11,a35
v_mfma_f32_16x16x32_bf16 a[4:7],a[88:91],a[224:227],a[4:7]
ds_read_b64_tr_b16 a[70:71],v52 offset:18944
)A"
R"A(v_mul_f32 v8,s71,v8
v_mfma_f32_16x16x32_bf16 a[4:7],a[92:95],a[228:231],a[4:7]
ds_read_b64_tr_b16 a[74:75],v52 offset:3584
v_mul_f32 v9,s71,v9
v_mfma_f32_16x16x32_bf16 a[20:23],a[92:95],a[236:239],a[20:23]
v_mul_f32 v10,s71,v10
v_mul_f32 v11,s71,v11
v_mfma_f32_16x16x32_bf16 a[20:23],a[88:91],a[232:235],a[20:23]
ds_read_b64_tr_b16 a[78:79],v52 offset:19968
v_cvt_pk_bf16_f32 v196,v8,v9
v_mfma_f32_16x16x32_bf16 a[20:23],a[84:87],a[204:207],a[20:23]
v_accvgpr_read_b32 v12,a4
v_accvgpr_read_b32 v13,a5
v_mfma_f32_16x16x32_bf16 a[20:23],a[80:83],a[200:203],a[20:23]
v_accvgpr_read_b32 v14,a6
v_accvgpr_read_b32 v15,a7
v_mfma_f32_16x16x32_bf16 a[36:39],a[80:83],a[208:211],a[36:39]
v_mul_f32 v12,s71,v12
v_mul_f32 v13,s71,v13
v_mfma_f32_16x16x32_bf16 a[36:39],a[84:87],a[212:215],a[36:39]
v_mul_f32 v14,s71,v14
v_mul_f32 v15,s71,v15
v_mfma_f32_16x16x32_bf16 a[36:39],a[88:91],a[240:243],a[36:39]
s_add_u32 s10,s24,s74
s_addc_u32 s11,s25,0
s_add_u32 s74,s74,16384
v_mfma_f32_16x16x32_bf16 a[36:39],a[92:95],a[244:247],a[36:39]
s_and_b32 s74,s74,0x3ffff
s_or_b32 s74,s74,s69
v_cvt_pk_bf16_f32 v134,v12,v13
v_mfma_f32_16x16x32_bf16 a[52:55],a[92:95],a[252:255],a[52:55]
s_add_u32 m0,s76,16384
s_sub_u32 s76,s76,32768
ds_read_b64_tr_b16 a[92:93],v52 offset:17928
v_mfma_f32_16x16x32_bf16 a[52:55],a[88:91],a[248:251],a[52:55]
s_and_b32 s76,s76,0x1ffff
ds_read_b64_tr_b16 a[88:89],v52 offset:1544
v_cvt_pk_bf16_f32 v135,v14,v15
v_mfma_f32_16x16x32_bf16 a[52:55],a[84:87],a[220:223],a[52:55]
global_load_lds_dwordx4 v4,s[10:11]
ds_read_b64_tr_b16 a[84:85],v52 offset:16904
v_mfma_f32_16x16x32_bf16 a[52:55],a[80:83],a[216:219],a[52:55]
s_and_b32 s69,s75,0xfffc0000
ds_read_b64_tr_b16 a[80:81],v52 offset:520
v_accvgpr_read_b32 v12,a36
v_mfma_f32_16x16x32_bf16 a[8:11],a[96:99],a[192:195],a[8:11]
s_add_u32 s75,s75,16384
v_accvgpr_read_b32 v13,a37
v_accvgpr_read_b32 v14,a38
v_mfma_f32_16x16x32_bf16 a[8:11],a[100:103],a[196:199],a[8:11]
s_waitcnt lgkmcnt(4)
ds_read_b64_tr_b16 a[82:83],v52 offset:2568
v_accvgpr_read_b32 v15,a39
v_mfma_f32_16x16x32_bf16 a[8:11],a[104:107],a[224:227],a[8:11]
ds_read_b64_tr_b16 a[86:87],v52 offset:18952
v_mul_f32 v12,s71,v12
v_mfma_f32_16x16x32_bf16 a[8:11],a[108:111],a[228:231],a[8:11]
ds_read_b64_tr_b16 a[90:91],v52 offset:3592
v_mul_f32 v13,s71,v13
v_mfma_f32_16x16x32_bf16 a[24:27],a[108:111],a[236:239],a[24:27]
v_mul_f32 v14,s71,v14
v_mul_f32 v15,s71,v15
v_mfma_f32_16x16x32_bf16 a[24:27],a[104:107],a[232:235],a[24:27]
ds_read_b64_tr_b16 a[94:95],v52 offset:19976
v_cvt_pk_bf16_f32 v197,v10,v11
v_mfma_f32_16x16x32_bf16 a[24:27],a[100:103],a[204:207],a[24:27]
v_cvt_pk_bf16_f32 v198,v12,v13
v_cvt_pk_bf16_f32 v199,v14,v15
v_mfma_f32_16x16x32_bf16 a[24:27],a[96:99],a[200:203],a[24:27]
v_accvgpr_read_b32 v8,a8
v_accvgpr_read_b32 v9,a9
v_mfma_f32_16x16x32_bf16 a[40:43],a[96:99],a[208:211],a[40:43]
v_accvgpr_read_b32 v10,a10
v_accvgpr_read_b32 v11,a11
v_mfma_f32_16x16x32_bf16 a[40:43],a[100:103],a[212:215],a[40:43]
v_mul_f32 v8,s71,v8
v_mul_f32 v9,s71,v9
v_mfma_f32_16x16x32_bf16 a[40:43],a[104:107],a[240:243],a[40:43]
v_mul_f32 v10,s71,v10
v_mul_f32 v11,s71,v11
v_mfma_f32_16x16x32_bf16 a[40:43],a[108:111],a[244:247],a[40:43]
v_cvt_pk_bf16_f32 v140,v8,v9
v_cvt_pk_bf16_f32 v141,v10,v11
v_mfma_f32_16x16x32_bf16 a[56:59],a[108:111],a[252:255],a[56:59]
ds_read_b64_tr_b16 a[108:109],v52 offset:18176
v_accvgpr_read_b32 v20,a20
v_mfma_f32_16x16x32_bf16 a[56:59],a[104:107],a[248:251],a[56:59]
global_load_lds_dwordx4 v5,s[10:11] offset:1024
ds_read_b64_tr_b16 a[104:105],v52 offset:1792
v_mfma_f32_16x16x32_bf16 a[56:59],a[100:103],a[220:223],a[56:59]
ds_read_b64_tr_b16 a[100:101],v52 offset:17152
v_accvgpr_read_b32 v21,a21
v_mfma_f32_16x16x32_bf16 a[56:59],a[96:99],a[216:219],a[56:59]
ds_read_b64_tr_b16 a[96:97],v52 offset:768
v_accvgpr_read_b32 v22,a22
v_mfma_f32_16x16x32_bf16 a[12:15],a[112:115],a[192:195],a[12:15]
v_accvgpr_read_b32 v23,a23
v_mul_f32 v16,s71,v16
v_mfma_f32_16x16x32_bf16 a[12:15],a[116:119],a[196:199],a[12:15]
ds_read_b64_tr_b16 a[98:99],v52 offset:2816
v_mul_f32 v17,s71,v17
v_mfma_f32_16x16x32_bf16 a[12:15],a[120:123],a[224:227],a[12:15]
ds_read_b64_tr_b16 a[102:103],v52 offset:19200
v_mul_f32 v18,s71,v18
v_mfma_f32_16x16x32_bf16 a[12:15],a[124:127],a[228:231],a[12:15]
ds_read_b64_tr_b16 a[106:107],v52 offset:3840
v_mul_f32 v19,s71,v19
v_mfma_f32_16x16x32_bf16 a[28:31],a[124:127],a[236:239],a[28:31]
v_mul_f32 v20,s71,v20
v_mul_f32 v21,s71,v21
v_mfma_f32_16x16x32_bf16 a[28:31],a[120:123],a[232:235],a[28:31]
ds_read_b64_tr_b16 a[110:111],v52 offset:20224
v_mul_f32 v22,s71,v22
v_mfma_f32_16x16x32_bf16 a[28:31],a[116:119],a[204:207],a[28:31]
v_accvgpr_read_b32 v12,a12
v_accvgpr_read_b32 v13,a13
v_mfma_f32_16x16x32_bf16 a[28:31],a[112:115],a[200:203],a[28:31]
v_accvgpr_read_b32 v14,a14
v_accvgpr_read_b32 v15,a15
v_mfma_f32_16x16x32_bf16 a[44:47],a[112:115],a[208:211],a[44:47]
v_mul_f32 v12,s71,v12
v_mul_f32 v13,s71,v13
v_mfma_f32_16x16x32_bf16 a[44:47],a[116:119],a[212:215],a[44:47]
v_mul_f32 v14,s71,v14
v_mul_f32 v15,s71,v15
v_mfma_f32_16x16x32_bf16 a[44:47],a[120:123],a[240:243],a[44:47]
v_cvt_pk_bf16_f32 v142,v12,v13
v_cvt_pk_bf16_f32 v143,v14,v15
v_mfma_f32_16x16x32_bf16 a[44:47],a[124:127],a[244:247],a[44:47]
v_mul_f32 v23,s71,v23
v_cvt_pk_bf16_f32 v164,v16,v17
v_mfma_f32_16x16x32_bf16 a[60:63],a[124:127],a[252:255],a[60:63]
global_load_lds_dwordx4 v4,s[10:11] offset:2048
ds_read_b64_tr_b16 a[124:125],v52 offset:18184
v_mfma_f32_16x16x32_bf16 a[60:63],a[120:123],a[248:251],a[60:63]
ds_read_b64_tr_b16 a[120:121],v52 offset:1800
v_cvt_pk_bf16_f32 v165,v18,v19
v_mfma_f32_16x16x32_bf16 a[60:63],a[116:119],a[220:223],a[60:63]
ds_read_b64_tr_b16 a[116:117],v52 offset:17160
v_cvt_pk_bf16_f32 v166,v20,v21
v_mfma_f32_16x16x32_bf16 a[60:63],a[112:115],a[216:219],a[60:63]
ds_read_b64_tr_b16 a[112:113],v52 offset:776
v_cvt_pk_bf16_f32 v167,v22,v23
v_mfma_f32_16x16x32_bf16 v[64:67],a[64:67],a[192:195],v[64:67]
v_accvgpr_read_b32 v16,a48
v_accvgpr_read_b32 v17,a49
v_mfma_f32_16x16x32_bf16 v[64:67],a[68:71],a[196:199],v[64:67]
ds_read_b64_tr_b16 a[114:115],v52 offset:2824
v_accvgpr_read_b32 v18,a50
v_mfma_f32_16x16x32_bf16 v[64:67],a[72:75],a[224:227],v[64:67]
ds_read_b64_tr_b16 a[118:119],v52 offset:19208
v_accvgpr_read_b32 v19,a51
v_mfma_f32_16x16x32_bf16 v[64:67],a[76:79],a[228:231],v[64:67]
ds_read_b64_tr_b16 a[122:123],v52 offset:3848
v_accvgpr_read_b32 v20,a52
v_mfma_f32_16x16x32_bf16 v[80:83],a[76:79],a[236:239],v[80:83]
v_accvgpr_read_b32 v21,a53
v_accvgpr_read_b32 v22,a54
v_mfma_f32_16x16x32_bf16 v[80:83],a[72:75],a[232:235],v[80:83]
ds_read_b64_tr_b16 a[126:127],v52 offset:20232
v_accvgpr_read_b32 v23,a55
v_mfma_f32_16x16x32_bf16 v[80:83],a[68:71],a[204:207],v[80:83]
v_mul_f32 v64,s71,v64
v_mul_f32 v65,s71,v65
v_mfma_f32_16x16x32_bf16 v[80:83],a[64:67],a[200:203],v[80:83]
v_mul_f32 v66,s71,v66
v_mul_f32 v67,s71,v67
v_mfma_f32_16x16x32_bf16 v[96:99],a[64:67],a[208:211],v[96:99]
v_cvt_pk_bf16_f32 v148,v64,v65
v_cvt_pk_bf16_f32 v149,v66,v67
v_mfma_f32_16x16x32_bf16 v[96:99],a[68:71],a[212:215],v[96:99]
v_mul_f32 v16,s71,v16
v_mul_f32 v17,s71,v17
v_mfma_f32_16x16x32_bf16 v[96:99],a[72:75],a[240:243],v[96:99]
v_mul_f32 v18,s71,v18
v_mul_f32 v19,s71,v19
v_mfma_f32_16x16x32_bf16 v[96:99],a[76:79],a[244:247],v[96:99]
global_load_lds_dwordx4 v5,s[10:11] offset:3072
v_mul_f32 v20,s71,v20
v_mfma_f32_16x16x32_bf16 v[112:115],a[76:79],a[252:255],v[112:115]
v_mul_f32 v21,s71,v21
v_mul_f32 v22,s71,v22
v_mfma_f32_16x16x32_bf16 v[112:115],a[72:75],a[248:251],v[112:115]
v_mul_f32 v23,s71,v23
v_cvt_pk_bf16_f32 v228,v16,v17
v_mfma_f32_16x16x32_bf16 v[112:115],a[68:71],a[220:223],v[112:115]
v_cvt_pk_bf16_f32 v229,v18,v19
v_cvt_pk_bf16_f32 v230,v20,v21
v_mfma_f32_16x16x32_bf16 v[112:115],a[64:67],a[216:219],v[112:115]
v_cvt_pk_bf16_f32 v231,v22,v23
v_accvgpr_read_b32 v16,a24
s_waitcnt vmcnt(8) lgkmcnt(0)
s_barrier
v_mfma_f32_16x16x32_bf16 v[68:71],a[80:83],a[192:195],v[68:71]
ds_read_b128 a[128:131],v1
v_accvgpr_read_b32 v17,a25
v_mfma_f32_16x16x32_bf16 v[68:71],a[84:87],a[196:199],v[68:71]
ds_read_b128 a[132:135],v1 offset:256
v_accvgpr_read_b32 v18,a26
v_mfma_f32_16x16x32_bf16 v[68:71],a[88:91],a[224:227],v[68:71]
ds_read_b128 a[136:139],v1 offset:512
v_accvgpr_read_b32 v19,a27
v_mfma_f32_16x16x32_bf16 v[68:71],a[92:95],a[228:231],v[68:71]
ds_read_b128 a[140:143],v1 offset:768
v_accvgpr_read_b32 v20,a28
v_mfma_f32_16x16x32_bf16 v[84:87],a[92:95],a[236:239],v[84:87]
v_accvgpr_read_b32 v21,a29
v_accvgpr_read_b32 v22,a30
v_mfma_f32_16x16x32_bf16 v[84:87],a[88:91],a[232:235],v[84:87]
v_accvgpr_read_b32 v23,a31
v_mul_f32 v16,s71,v16
v_mfma_f32_16x16x32_bf16 v[84:87],a[84:87],a[204:207],v[84:87]
s_add_u32 s10,s20,s74
s_addc_u32 s11,s21,0
v_mul_f32 v68,s71,v68
v_mfma_f32_16x16x32_bf16 v[84:87],a[80:83],a[200:203],v[84:87]
v_mul_f32 v69,s71,v69
v_mul_f32 v70,s71,v70
v_mfma_f32_16x16x32_bf16 v[100:103],a[80:83],a[208:211],v[100:103]
s_add_u32 m0,s76,0
v_mul_f32 v71,s71,v71
v_cvt_pk_bf16_f32 v150,v68,v69
v_mfma_f32_16x16x32_bf16 v[100:103],a[84:87],a[212:215],v[100:103]
v_cvt_pk_bf16_f32 v151,v70,v71
v_mul_f32 v17,s71,v17
v_mfma_f32_16x16x32_bf16 v[100:103],a[88:91],a[240:243],v[100:103]
global_load_lds_dwordx4 v4,s[10:11] nt
v_mul_f32 v18,s71,v18
v_mfma_f32_16x16x32_bf16 v[100:103],a[92:95],a[244:247],v[100:103]
v_mul_f32 v19,s71,v19
v_mul_f32 v20,s71,v20
v_mfma_f32_16x16x32_bf16 v[116:119],a[92:95],a[252:255],v[116:119]
v_mul_f32 v21,s71,v21
v_mul_f32 v22,s71,v22
v_mfma_f32_16x16x32_bf16 v[116:119],a[88:91],a[248:251],v[116:119]
v_mul_f32 v23,s71,v23
v_cvt_pk_bf16_f32 v172,v16,v17
v_mfma_f32_16x16x32_bf16 v[116:119],a[84:87],a[220:223],v[116:119]
v_cvt_pk_bf16_f32 v173,v18,v19
v_cvt_pk_bf16_f32 v174,v20,v21
v_mfma_f32_16x16x32_bf16 v[116:119],a[80:83],a[216:219],v[116:119]
v_cvt_pk_bf16_f32 v175,v22,v23
v_mul_f32 v80,s71,v80
v_mfma_f32_16x16x32_bf16 v[72:75],a[96:99],a[192:195],v[72:75]
v_mul_f32 v81,s71,v81
v_mul_f32 v82,s71,v82
v_mfma_f32_16x16x32_bf16 v[72:75],a[100:103],a[196:199],v[72:75]
v_mul_f32 v83,s71,v83
v_mul_f32 v84,s71,v84
v_mfma_f32_16x16x32_bf16 v[72:75],a[104:107],a[224:227],v[72:75]
v_mul_f32 v85,s71,v85
v_mul_f32 v86,s71,v86
v_mfma_f32_16x16x32_bf16 v[72:75],a[108:111],a[228:231],v[72:75]
v_mul_f32 v87,s71,v87
v_cvt_pk_bf16_f32 v180,v80,v81
v_mfma_f32_16x16x32_bf16 v[88:91],a[108:111],a[236:239],v[88:91]
v_cvt_pk_bf16_f32 v181,v82,v83
v_cvt_pk_bf16_f32 v182,v84,v85
v_mfma_f32_16x16x32_bf16 v[88:91],a[104:107],a[232:235],v[88:91]
v_cvt_pk_bf16_f32 v183,v86,v87
v_accvgpr_read_b32 v8,a40
v_mfma_f32_16x16x32_bf16 v[88:91],a[100:103],a[204:207],v[88:91]
v_mul_f32 v72,s71,v72
v_mul_f32 v73,s71,v73
v_mfma_f32_16x16x32_bf16 v[88:91],a[96:99],a[200:203],v[88:91]
v_mul_f32 v74,s71,v74
v_mul_f32 v75,s71,v75
v_mfma_f32_16x16x32_bf16 v[104:107],a[96:99],a[208:211],v[104:107]
global_load_lds_dwordx4 v4,s[10:11] offset:1024 nt
v_cvt_pk_bf16_f32 v156,v72,v73
v_mfma_f32_16x16x32_bf16 v[104:107],a[100:103],a[212:215],v[104:107]
v_cvt_pk_bf16_f32 v157,v74,v75
v_accvgpr_read_b32 v9,a41
v_mfma_f32_16x16x32_bf16 v[104:107],a[104:107],a[240:243],v[104:107]
v_mul_f32 v88,s71,v88
v_mul_f32 v89,s71,v89
v_mfma_f32_16x16x32_bf16 v[104:107],a[108:111],a[244:247],v[104:107]
v_mul_f32 v90,s71,v90
v_mul_f32 v91,s71,v91
v_mfma_f32_16x16x32_bf16 v[120:123],a[108:111],a[252:255],v[120:123]
v_cvt_pk_bf16_f32 v188,v88,v89
v_cvt_pk_bf16_f32 v189,v90,v91
v_mfma_f32_16x16x32_bf16 v[120:123],a[104:107],a[248:251],v[120:123]
v_accvgpr_read_b32 v10,a42
v_accvgpr_read_b32 v11,a43
v_mfma_f32_16x16x32_bf16 v[120:123],a[100:103],a[220:223],v[120:123]
v_accvgpr_read_b32 v12,a44
v_accvgpr_read_b32 v13,a45
v_mfma_f32_16x16x32_bf16 v[120:123],a[96:99],a[216:219],v[120:123]
v_accvgpr_read_b32 v14,a46
v_accvgpr_read_b32 v15,a47
v_mfma_f32_16x16x32_bf16 v[76:79],a[112:115],a[192:195],v[76:79]
v_mul_f32 v8,s71,v8
v_mul_f32 v9,s71,v9
v_mfma_f32_16x16x32_bf16 v[76:79],a[116:119],a[196:199],v[76:79]
v_mul_f32 v10,s71,v10
v_mul_f32 v11,s71,v11
v_mfma_f32_16x16x32_bf16 v[76:79],a[120:123],a[224:227],v[76:79]
v_mul_f32 v12,s71,v12
v_mul_f32 v13,s71,v13
)A"
R"A(v_mfma_f32_16x16x32_bf16 v[76:79],a[124:127],a[228:231],v[76:79]
v_mul_f32 v14,s71,v14
v_mul_f32 v15,s71,v15
v_mfma_f32_16x16x32_bf16 v[92:95],a[124:127],a[236:239],v[92:95]
v_cvt_pk_bf16_f32 v204,v8,v9
v_cvt_pk_bf16_f32 v205,v10,v11
v_mfma_f32_16x16x32_bf16 v[92:95],a[120:123],a[232:235],v[92:95]
v_cvt_pk_bf16_f32 v206,v12,v13
v_cvt_pk_bf16_f32 v207,v14,v15
v_mfma_f32_16x16x32_bf16 v[92:95],a[116:119],a[204:207],v[92:95]
global_load_lds_dwordx4 v5,s[10:11] offset:2048 nt
v_mul_f32 v76,s71,v76
v_mfma_f32_16x16x32_bf16 v[92:95],a[112:115],a[200:203],v[92:95]
v_mul_f32 v77,s71,v77
v_mul_f32 v78,s71,v78
v_mfma_f32_16x16x32_bf16 v[108:111],a[112:115],a[208:211],v[108:111]
v_mul_f32 v79,s71,v79
v_cvt_pk_bf16_f32 v158,v76,v77
v_mfma_f32_16x16x32_bf16 v[108:111],a[116:119],a[212:215],v[108:111]
ds_read_b128 a[156:159],v1 offset:4864
v_cvt_pk_bf16_f32 v159,v78,v79
v_mfma_f32_16x16x32_bf16 v[108:111],a[120:123],a[240:243],v[108:111]
v_mul_f32 v92,s71,v92
v_mul_f32 v93,s71,v93
v_mfma_f32_16x16x32_bf16 v[108:111],a[124:127],a[244:247],v[108:111]
v_mul_f32 v94,s71,v94
v_mul_f32 v95,s71,v95
v_mfma_f32_16x16x32_bf16 v[124:127],a[124:127],a[252:255],v[124:127]
v_cvt_pk_bf16_f32 v190,v92,v93
v_cvt_pk_bf16_f32 v191,v94,v95
v_mfma_f32_16x16x32_bf16 v[124:127],a[120:123],a[248:251],v[124:127]
v_cvt_pk_bf16_f32 v209,v210,v211
v_cvt_pk_bf16_f32 v210,v212,v213
v_mfma_f32_16x16x32_bf16 v[124:127],a[116:119],a[220:223],v[124:127]
ds_read_b128 a[152:155],v1 offset:4608
v_cvt_pk_bf16_f32 v211,v214,v215
v_mfma_f32_16x16x32_bf16 v[124:127],a[112:115],a[216:219],v[124:127]
v_mul_f32 v96,s71,v96
v_mul_f32 v97,s71,v97
s_waitcnt lgkmcnt(2)
v_mfma_f32_16x16x32_bf16 v[64:67],a[128:131],v[132:135],0
v_mul_f32 v98,s71,v98
v_mul_f32 v99,s71,v99
v_mfma_f32_16x16x32_bf16 v[64:67],a[132:135],v[140:143],v[64:67]
v_mul_f32 v100,s71,v100
v_mul_f32 v101,s71,v101
v_mfma_f32_16x16x32_bf16 v[64:67],a[136:139],v[148:151],v[64:67]
v_mul_f32 v102,s71,v102
v_mul_f32 v103,s71,v103
v_mfma_f32_16x16x32_bf16 v[64:67],a[140:143],v[156:159],v[64:67]
global_load_lds_dwordx4 v5,s[10:11] offset:3072
v_cvt_pk_bf16_f32 v212,v96,v97
v_mfma_f32_16x16x32_bf16 v[80:83],a[128:131],v[164:167],0
v_cvt_pk_bf16_f32 v213,v98,v99
v_cvt_pk_bf16_f32 v214,v100,v101
v_mfma_f32_16x16x32_bf16 v[80:83],a[132:135],v[172:175],v[80:83]
v_cvt_pk_bf16_f32 v215,v102,v103
v_mul_f32 v216,s71,v216
v_mfma_f32_16x16x32_bf16 v[80:83],a[136:139],v[180:183],v[80:83]
v_max3_f32 v64,v64,v65,v66
v_max_f32 v64,v64,v67
v_mov_b32 v65,v64
v_mfma_f32_16x16x32_bf16 v[80:83],a[140:143],v[188:191],v[80:83]
v_mul_f32 v217,s71,v217
v_mul_f32 v218,s71,v218
v_mfma_f32_16x16x32_bf16 v[96:99],a[128:131],v[196:199],0
v_permlane32_swap_b32 v64,v65
v_max_f32 v64,v64,v65
v_mov_b32 v65,v64
v_mul_f32 v219,s71,v219
v_mul_f32 v220,s71,v220
v_mul_f32 v221,s71,v221
v_mul_f32 v222,s71,v222
v_mul_f32 v223,s71,v223
v_cvt_pk_bf16_f32 v216,v216,v217
v_cvt_pk_bf16_f32 v217,v218,v219
v_cvt_pk_bf16_f32 v218,v220,v221
v_cvt_pk_bf16_f32 v219,v222,v223
v_mul_f32 v104,s71,v104
v_mul_f32 v105,s71,v105
v_mul_f32 v106,s71,v106
v_mul_f32 v107,s71,v107
v_mul_f32 v108,s71,v108
v_mul_f32 v109,s71,v109
v_mul_f32 v110,s71,v110
v_mul_f32 v111,s71,v111
v_cvt_pk_bf16_f32 v220,v104,v105
v_cvt_pk_bf16_f32 v221,v106,v107
v_cvt_pk_bf16_f32 v222,v108,v109
v_cvt_pk_bf16_f32 v223,v110,v111
v_mfma_f32_16x16x32_bf16 v[96:99],a[132:135],v[204:207],v[96:99]
ds_read_b128 a[148:151],v1 offset:4352
v_max3_f32 v80,v80,v81,v82
v_mfma_f32_16x16x32_bf16 v[96:99],a[136:139],v[212:215],v[96:99]
ds_read_b128 a[144:147],v1 offset:4096
v_permlane16_swap_b32 v64,v65
v_max_f32 v64,v64,v65
v_sub_f32 v35,0,v64
v_sub_f32 v34,0,v64
v_sub_f32 v33,0,v64
v_sub_f32 v32,0,v64
v_mfma_f32_16x16x32_bf16 v[96:99],a[140:143],v[220:223],v[96:99]
ds_read_b128 a[160:163],v1 offset:8192
v_max_f32 v80,v80,v83
v_mfma_f32_16x16x32_bf16 v[64:67],a[128:131],v[132:135],v[32:35]
s_waitcnt lgkmcnt(3)
ds_read_b128 a[164:167],v1 offset:8448
v_mov_b32 v81,v80
v_mfma_f32_16x16x32_bf16 v[64:67],a[132:135],v[140:143],v[64:67]
s_add_u32 s10,s24,s74
s_addc_u32 s11,s25,0
v_mul_f32 v232,s71,v232
v_mfma_f32_16x16x32_bf16 v[64:67],a[136:139],v[148:151],v[64:67]
L_pd4_%=:
s_cbranch_execz L_pd8_%=
s_add_u32 s74,s74,16384
ds_read_b128 a[168:171],v1 offset:8704
v_max3_f32 v96,v96,v97,v98
v_mfma_f32_16x16x32_bf16 v[64:67],a[140:143],v[156:159],v[64:67]
s_add_u32 m0,s76,16384
ds_read_b128 a[172:175],v1 offset:8960
v_max_f32 v96,v96,v99
v_mfma_f32_16x16x32_bf16 v[68:71],a[156:159],v[156:159],v[32:35]
s_and_b32 s74,s74,0x3ffff
ds_read_b128 a[188:191],v1 offset:13056
v_mov_b32 v97,v96
v_mfma_f32_16x16x32_bf16 v[68:71],a[152:155],v[148:151],v[68:71]
global_load_lds_dwordx4 v4,s[10:11]
v_mul_f32 v233,s71,v233
s_waitcnt lgkmcnt(6)
v_mfma_f32_16x16x32_bf16 v[68:71],a[148:151],v[140:143],v[68:71]
s_or_b32 s74,s74,s69
ds_read_b128 a[184:187],v1 offset:12800
v_mul_f32 v234,s71,v234
s_waitcnt lgkmcnt(6)
v_mfma_f32_16x16x32_bf16 v[68:71],a[144:147],v[132:135],v[68:71]
s_sub_u32 s76,s76,32768
ds_read_b128 a[180:183],v1 offset:12544
v_mul_f32 v235,s71,v235
s_waitcnt lgkmcnt(6)
v_mfma_f32_16x16x32_bf16 v[72:75],a[160:163],v[132:135],v[32:35]
s_and_b32 s76,s76,0x1ffff
ds_read_b128 a[176:179],v1 offset:12288
v_mul_f32 v236,s71,v236
s_waitcnt lgkmcnt(6)
v_mfma_f32_16x16x32_bf16 v[72:75],a[164:167],v[140:143],v[72:75]
s_and_b32 s69,s75,0xfffc0000
v_permlane32_swap_b32 v80,v81
s_waitcnt lgkmcnt(5)
v_mfma_f32_16x16x32_bf16 v[72:75],a[168:171],v[148:151],v[72:75]
s_add_u32 s75,s75,16384
v_max_f32 v80,v80,v81
v_mov_b32 v81,v80
s_waitcnt lgkmcnt(4)
v_mfma_f32_16x16x32_bf16 v[72:75],a[172:175],v[156:159],v[72:75]
s_waitcnt lgkmcnt(3)
v_mul_f32 v237,s71,v237
v_mul_f32 v238,s71,v238
v_mfma_f32_16x16x32_bf16 v[76:79],a[188:191],v[156:159],v[32:35]
s_waitcnt lgkmcnt(2)
v_permlane16_swap_b32 v80,v81
v_mfma_f32_16x16x32_bf16 v[76:79],a[184:187],v[148:151],v[76:79]
s_waitcnt lgkmcnt(1)
v_max_f32 v80,v80,v81
v_sub_f32 v39,0,v80
v_sub_f32 v38,0,v80
v_sub_f32 v37,0,v80
v_sub_f32 v36,0,v80
v_mfma_f32_16x16x32_bf16 v[76:79],a[180:183],v[140:143],v[76:79]
s_waitcnt lgkmcnt(0)
v_mul_f32 v239,s71,v239
v_cvt_pk_bf16_f32 v232,v232,v233
v_mfma_f32_16x16x32_bf16 v[76:79],a[176:179],v[132:135],v[76:79]
v_cvt_pk_bf16_f32 v233,v234,v235
v_cvt_pk_bf16_f32 v234,v236,v237
v_mfma_f32_16x16x32_bf16 v[80:83],a[128:131],v[164:167],v[36:39]
v_cvt_pk_bf16_f32 v235,v238,v239
v_accvgpr_read_b32 v16,a56
v_mfma_f32_16x16x32_bf16 v[80:83],a[132:135],v[172:175],v[80:83]
v_accvgpr_read_b32 v17,a57
v_accvgpr_read_b32 v18,a58
v_mfma_f32_16x16x32_bf16 v[80:83],a[136:139],v[180:183],v[80:83]
v_accvgpr_read_b32 v19,a59
v_accvgpr_read_b32 v20,a60
v_mfma_f32_16x16x32_bf16 v[80:83],a[140:143],v[188:191],v[80:83]
global_load_lds_dwordx4 v5,s[10:11] offset:1024
v_accvgpr_read_b32 v21,a61
v_mfma_f32_16x16x32_bf16 v[84:87],a[156:159],v[188:191],v[36:39]
v_accvgpr_read_b32 v22,a62
v_accvgpr_read_b32 v23,a63
v_mfma_f32_16x16x32_bf16 v[84:87],a[152:155],v[180:183],v[84:87]
v_mul_f32 v16,s71,v16
v_mul_f32 v17,s71,v17
v_mfma_f32_16x16x32_bf16 v[84:87],a[148:151],v[172:175],v[84:87]
v_mul_f32 v18,s71,v18
v_mul_f32 v19,s71,v19
v_mfma_f32_16x16x32_bf16 v[84:87],a[144:147],v[164:167],v[84:87]
v_mul_f32 v20,s71,v20
v_mul_f32 v21,s71,v21
v_mfma_f32_16x16x32_bf16 v[88:91],a[160:163],v[164:167],v[36:39]
v_mul_f32 v22,s71,v22
v_mul_f32 v23,s71,v23
v_mfma_f32_16x16x32_bf16 v[88:91],a[164:167],v[172:175],v[88:91]
v_cvt_pk_bf16_f32 v236,v16,v17
v_cvt_pk_bf16_f32 v237,v18,v19
v_mfma_f32_16x16x32_bf16 v[88:91],a[168:171],v[180:183],v[88:91]
v_cvt_pk_bf16_f32 v238,v20,v21
v_cvt_pk_bf16_f32 v239,v22,v23
v_mfma_f32_16x16x32_bf16 v[88:91],a[172:175],v[188:191],v[88:91]
v_mul_f32 v240,s71,v240
v_mul_f32 v241,s71,v241
v_mfma_f32_16x16x32_bf16 v[92:95],a[188:191],v[188:191],v[36:39]
v_mul_f32 v242,s71,v242
v_mul_f32 v243,s71,v243
v_mfma_f32_16x16x32_bf16 v[92:95],a[184:187],v[180:183],v[92:95]
v_mul_f32 v244,s71,v244
v_mul_f32 v245,s71,v245
v_mfma_f32_16x16x32_bf16 v[92:95],a[180:183],v[172:175],v[92:95]
v_mul_f32 v246,s71,v246
v_mul_f32 v247,s71,v247
v_mfma_f32_16x16x32_bf16 v[92:95],a[176:179],v[164:167],v[92:95]
v_cvt_pk_bf16_f32 v240,v240,v241
v_cvt_pk_bf16_f32 v241,v242,v243
v_cvt_pk_bf16_f32 v242,v244,v245
v_cvt_pk_bf16_f32 v243,v246,v247
v_mul_f32 v112,s71,v112
v_mul_f32 v113,s71,v113
v_mul_f32 v114,s71,v114
v_mul_f32 v115,s71,v115
v_mul_f32 v116,s71,v116
v_mul_f32 v117,s71,v117
v_mul_f32 v118,s71,v118
v_mul_f32 v119,s71,v119
v_cvt_pk_bf16_f32 v244,v112,v113
v_cvt_pk_bf16_f32 v245,v114,v115
v_cvt_pk_bf16_f32 v246,v116,v117
v_cvt_pk_bf16_f32 v247,v118,v119
v_permlane32_swap_b32 v96,v97
v_max_f32 v96,v96,v97
v_mov_b32 v97,v96
v_mfma_f32_16x16x32_bf16 v[112:115],a[128:131],v[228:231],0
v_mul_f32 v248,s71,v248
v_mul_f32 v249,s71,v249
v_mul_f32 v250,s71,v250
v_mul_f32 v251,s71,v251
v_mul_f32 v252,s71,v252
v_mul_f32 v253,s71,v253
v_mul_f32 v254,s71,v254
v_mul_f32 v255,s71,v255
v_cvt_pk_bf16_f32 v248,v248,v249
v_cvt_pk_bf16_f32 v249,v250,v251
v_cvt_pk_bf16_f32 v250,v252,v253
v_cvt_pk_bf16_f32 v251,v254,v255
v_mul_f32 v120,s71,v120
v_mul_f32 v121,s71,v121
v_mul_f32 v122,s71,v122
v_mul_f32 v123,s71,v123
v_mul_f32 v124,s71,v124
v_mul_f32 v125,s71,v125
v_mul_f32 v126,s71,v126
v_mul_f32 v127,s71,v127
v_cvt_pk_bf16_f32 v252,v120,v121
v_cvt_pk_bf16_f32 v253,v122,v123
v_cvt_pk_bf16_f32 v254,v124,v125
v_cvt_pk_bf16_f32 v255,v126,v127
v_mfma_f32_16x16x32_bf16 v[112:115],a[132:135],v[236:239],v[112:115]
global_load_lds_dwordx4 v4,s[10:11] offset:2048
v_permlane16_swap_b32 v96,v97
v_max_f32 v96,v96,v97
v_sub_f32 v43,0,v96
v_sub_f32 v42,0,v96
v_sub_f32 v41,0,v96
v_sub_f32 v40,0,v96
v_mfma_f32_16x16x32_bf16 v[112:115],a[136:139],v[244:247],v[112:115]
ds_read_b64_tr_b16 a[192:193],v2
ds_read_b64_tr_b16 a[194:195],v2 offset:4096
v_mfma_f32_16x16x32_bf16 v[112:115],a[140:143],v[252:255],v[112:115]
ds_read_b64_tr_b16 a[196:197],v2 offset:8192
ds_read_b64_tr_b16 a[198:199],v2 offset:12288
v_mfma_f32_16x16x32_bf16 v[96:99],a[128:131],v[196:199],v[40:43]
ds_read_b64_tr_b16 a[200:201],v3
ds_read_b64_tr_b16 a[202:203],v3 offset:4096
v_mfma_f32_16x16x32_bf16 v[96:99],a[132:135],v[204:207],v[96:99]
ds_read_b64_tr_b16 a[204:205],v3 offset:8192
ds_read_b64_tr_b16 a[206:207],v3 offset:12288
v_mfma_f32_16x16x32_bf16 v[96:99],a[136:139],v[212:215],v[96:99]
ds_read_b64_tr_b16 a[208:209],v2 offset:256
v_max3_f32 v112,v112,v113,v114
v_mfma_f32_16x16x32_bf16 v[96:99],a[140:143],v[220:223],v[96:99]
ds_read_b64_tr_b16 a[210:211],v2 offset:4352
ds_read_b64_tr_b16 a[212:213],v2 offset:8448
v_mfma_f32_16x16x32_bf16 v[100:103],a[156:159],v[220:223],v[40:43]
ds_read_b64_tr_b16 a[214:215],v2 offset:12544
v_max_f32 v112,v112,v115
v_mfma_f32_16x16x32_bf16 v[100:103],a[152:155],v[212:215],v[100:103]
ds_read_b64_tr_b16 a[216:217],v3 offset:256
ds_read_b64_tr_b16 a[218:219],v3 offset:4352
v_mfma_f32_16x16x32_bf16 v[100:103],a[148:151],v[204:207],v[100:103]
v_exp_f32 v64,v64
v_mfma_f32_16x16x32_bf16 v[100:103],a[144:147],v[196:199],v[100:103]
v_exp_f32 v65,v65
v_mfma_f32_16x16x32_bf16 v[104:107],a[160:163],v[196:199],v[40:43]
v_exp_f32 v66,v66
v_mfma_f32_16x16x32_bf16 v[104:107],a[164:167],v[204:207],v[104:107]
v_exp_f32 v67,v67
v_mfma_f32_16x16x32_bf16 v[104:107],a[168:171],v[212:215],v[104:107]
v_exp_f32 v68,v68
v_mfma_f32_16x16x32_bf16 v[104:107],a[172:175],v[220:223],v[104:107]
global_load_lds_dwordx4 v5,s[10:11] offset:3072
ds_read_b64_tr_b16 a[220:221],v3 offset:8448
v_mfma_f32_16x16x32_bf16 v[108:111],a[188:191],v[220:223],v[40:43]
v_exp_f32 v69,v69
v_exp_f32 v70,v70
v_exp_f32 v71,v71
v_mfma_f32_16x16x32_bf16 v[108:111],a[184:187],v[212:215],v[108:111]
ds_read_b64_tr_b16 a[222:223],v3 offset:12544
v_cvt_pk_bf16_f32 v64,v64,v65
v_cvt_pk_bf16_f32 v65,v66,v67
v_cvt_pk_bf16_f32 v66,v68,v69
v_cvt_pk_bf16_f32 v67,v70,v71
v_exp_f32 v72,v72
v_exp_f32 v73,v73
v_exp_f32 v74,v74
v_exp_f32 v75,v75
v_exp_f32 v76,v76
v_exp_f32 v77,v77
v_exp_f32 v78,v78
v_exp_f32 v79,v79
v_mfma_f32_16x16x32_bf16 v[108:111],a[180:183],v[204:207],v[108:111]
ds_read_b64_tr_b16 a[224:225],v2 offset:512
ds_read_b64_tr_b16 a[226:227],v2 offset:4608
v_cvt_pk_bf16_f32 v72,v72,v73
)A"
R"A(v_cvt_pk_bf16_f32 v73,v74,v75
v_cvt_pk_bf16_f32 v74,v76,v77
v_cvt_pk_bf16_f32 v75,v78,v79
v_mfma_f32_16x16x32_bf16 v[108:111],a[176:179],v[196:199],v[108:111]
s_waitcnt lgkmcnt(4)
ds_read_b64_tr_b16 a[228:229],v2 offset:8704
ds_read_b64_tr_b16 a[230:231],v2 offset:12800
v_mfma_f32_16x16x32_bf16 a[0:3],a[192:195],v[64:67],0
ds_read_b64_tr_b16 a[232:233],v3 offset:512
ds_read_b64_tr_b16 a[234:235],v3 offset:4608
v_mfma_f32_16x16x32_bf16 a[0:3],a[196:199],v[72:75],a[0:3]
ds_read_b64_tr_b16 a[236:237],v3 offset:8704
ds_read_b64_tr_b16 a[238:239],v3 offset:12800
v_mfma_f32_16x16x32_bf16 a[4:7],a[200:203],v[64:67],0
ds_read_b64_tr_b16 a[240:241],v2 offset:768
v_mov_b32 v113,v112
v_mfma_f32_16x16x32_bf16 a[4:7],a[204:207],v[72:75],a[4:7]
ds_read_b64_tr_b16 a[242:243],v2 offset:4864
ds_read_b64_tr_b16 a[244:245],v2 offset:8960
v_mfma_f32_16x16x32_bf16 a[8:11],a[208:211],v[64:67],0
ds_read_b64_tr_b16 a[246:247],v2 offset:13056
v_add_u32 v1,s78,v1
v_mfma_f32_16x16x32_bf16 a[8:11],a[212:215],v[72:75],a[8:11]
ds_read_b64_tr_b16 a[248:249],v3 offset:768
ds_read_b64_tr_b16 a[250:251],v3 offset:4864
v_mfma_f32_16x16x32_bf16 a[12:15],a[216:219],v[64:67],0
s_waitcnt lgkmcnt(12)
ds_read_b64_tr_b16 a[252:253],v3 offset:8960
v_add_u32 v2,s78,v2
v_mfma_f32_16x16x32_bf16 a[12:15],a[220:223],v[72:75],a[12:15]
s_waitcnt lgkmcnt(6)
ds_read_b64_tr_b16 a[254:255],v3 offset:13056
v_add_u32 v3,s78,v3
v_mfma_f32_16x16x32_bf16 a[16:19],a[224:227],v[64:67],0
s_sub_u32 s78,s77,32768
v_permlane32_swap_b32 v112,v113
v_mfma_f32_16x16x32_bf16 a[16:19],a[228:231],v[72:75],a[16:19]
s_and_b32 s78,s78,0x1ffff
v_max_f32 v112,v112,v113
v_mov_b32 v113,v112
v_mfma_f32_16x16x32_bf16 a[20:23],a[232:235],v[64:67],0
s_sub_u32 s78,s78,s77
v_exp_f32 v80,v80
v_mfma_f32_16x16x32_bf16 a[20:23],a[236:239],v[72:75],a[20:23]
s_add_u32 s77,s77,s78
v_permlane16_swap_b32 v112,v113
s_waitcnt lgkmcnt(5)
v_mfma_f32_16x16x32_bf16 a[24:27],a[240:243],v[64:67],0
s_waitcnt lgkmcnt(4)
v_max_f32 v112,v112,v113
v_max_f32 v112,s72,v112
v_mfma_f32_16x16x32_bf16 a[24:27],a[244:247],v[72:75],a[24:27]
s_waitcnt lgkmcnt(2)
v_sub_f32 v47,0,v112
v_sub_f32 v46,0,v112
v_mfma_f32_16x16x32_bf16 a[28:31],a[248:251],v[64:67],0
s_waitcnt lgkmcnt(0)
v_sub_f32 v45,0,v112
v_sub_f32 v44,0,v112
v_mfma_f32_16x16x32_bf16 a[28:31],a[252:255],v[72:75],a[28:31]
v_exp_f32 v81,v81
v_mfma_f32_16x16x32_bf16 v[48:51],v[28:31],v[64:67],0
v_exp_f32 v82,v82
v_mfma_f32_16x16x32_bf16 v[48:51],v[28:31],v[72:75],v[48:51]
v_exp_f32 v83,v83
s_waitcnt vmcnt(8)
s_barrier
v_mfma_f32_16x16x32_bf16 v[112:115],a[128:131],v[228:231],v[44:47]
s_add_u32 s10,s20,s74
s_addc_u32 s11,s21,0
s_add_u32 m0,s76,0
v_mfma_f32_16x16x32_bf16 v[112:115],a[132:135],v[236:239],v[112:115]
v_exp_f32 v84,v84
v_mfma_f32_16x16x32_bf16 v[112:115],a[136:139],v[244:247],v[112:115]
global_load_lds_dwordx4 v4,s[10:11] nt
v_cvt_pk_bf16_f32 v80,v80,v81
v_mfma_f32_16x16x32_bf16 v[112:115],a[140:143],v[252:255],v[112:115]
v_exp_f32 v85,v85
v_mfma_f32_16x16x32_bf16 v[116:119],a[156:159],v[252:255],v[44:47]
v_exp_f32 v86,v86
v_mfma_f32_16x16x32_bf16 v[116:119],a[152:155],v[244:247],v[116:119]
v_exp_f32 v87,v87
v_mfma_f32_16x16x32_bf16 v[116:119],a[148:151],v[236:239],v[116:119]
v_cvt_pk_bf16_f32 v81,v82,v83
v_cvt_pk_bf16_f32 v82,v84,v85
v_mfma_f32_16x16x32_bf16 v[116:119],a[144:147],v[228:231],v[116:119]
ds_read_b128 a[128:131],v1
v_cvt_pk_bf16_f32 v83,v86,v87
v_mfma_f32_16x16x32_bf16 v[120:123],a[160:163],v[228:231],v[44:47]
v_exp_f32 v88,v88
v_mfma_f32_16x16x32_bf16 v[120:123],a[164:167],v[236:239],v[120:123]
v_exp_f32 v89,v89
v_mfma_f32_16x16x32_bf16 v[120:123],a[168:171],v[244:247],v[120:123]
v_exp_f32 v90,v90
v_mfma_f32_16x16x32_bf16 v[120:123],a[172:175],v[252:255],v[120:123]
v_exp_f32 v91,v91
v_mfma_f32_16x16x32_bf16 v[124:127],a[188:191],v[252:255],v[44:47]
v_exp_f32 v92,v92
v_mfma_f32_16x16x32_bf16 v[124:127],a[184:187],v[244:247],v[124:127]
v_exp_f32 v93,v93
v_exp_f32 v94,v94
v_exp_f32 v95,v95
v_mfma_f32_16x16x32_bf16 v[124:127],a[180:183],v[236:239],v[124:127]
v_cvt_pk_bf16_f32 v88,v88,v89
v_cvt_pk_bf16_f32 v89,v90,v91
v_cvt_pk_bf16_f32 v90,v92,v93
v_cvt_pk_bf16_f32 v91,v94,v95
v_mfma_f32_16x16x32_bf16 v[124:127],a[176:179],v[228:231],v[124:127]
v_exp_f32 v96,v96
v_mfma_f32_16x16x32_bf16 a[32:35],a[192:195],v[80:83],0
global_load_lds_dwordx4 v4,s[10:11] offset:1024 nt
ds_read_b128 a[132:135],v1 offset:256
v_mfma_f32_16x16x32_bf16 a[32:35],a[196:199],v[88:91],a[32:35]
v_exp_f32 v97,v97
v_mfma_f32_16x16x32_bf16 a[36:39],a[200:203],v[80:83],0
ds_read_b128 a[136:139],v1 offset:512
v_cvt_pk_bf16_f32 v96,v96,v97
v_mfma_f32_16x16x32_bf16 a[36:39],a[204:207],v[88:91],a[36:39]
v_exp_f32 v98,v98
v_mfma_f32_16x16x32_bf16 a[40:43],a[208:211],v[80:83],0
v_exp_f32 v99,v99
v_mfma_f32_16x16x32_bf16 a[40:43],a[212:215],v[88:91],a[40:43]
ds_read_b128 a[140:143],v1 offset:768
v_cvt_pk_bf16_f32 v97,v98,v99
v_mfma_f32_16x16x32_bf16 a[44:47],a[216:219],v[80:83],0
v_exp_f32 v100,v100
v_mfma_f32_16x16x32_bf16 a[44:47],a[220:223],v[88:91],a[44:47]
v_exp_f32 v101,v101
v_mfma_f32_16x16x32_bf16 a[48:51],a[224:227],v[80:83],0
ds_read_b128 a[156:159],v1 offset:4864
v_cvt_pk_bf16_f32 v98,v100,v101
v_mfma_f32_16x16x32_bf16 a[48:51],a[228:231],v[88:91],a[48:51]
v_exp_f32 v102,v102
v_mfma_f32_16x16x32_bf16 a[52:55],a[232:235],v[80:83],0
v_exp_f32 v103,v103
v_mfma_f32_16x16x32_bf16 a[52:55],a[236:239],v[88:91],a[52:55]
ds_read_b128 a[152:155],v1 offset:4608
v_cvt_pk_bf16_f32 v99,v102,v103
v_mfma_f32_16x16x32_bf16 a[56:59],a[240:243],v[80:83],0
ds_read_b128 a[148:151],v1 offset:4352
v_mfma_f32_16x16x32_bf16 a[56:59],a[244:247],v[88:91],a[56:59]
ds_read_b128 a[144:147],v1 offset:4096
v_mfma_f32_16x16x32_bf16 a[60:63],a[248:251],v[80:83],0
global_load_lds_dwordx4 v5,s[10:11] offset:2048 nt
ds_read_b128 a[160:163],v1 offset:8192
v_mfma_f32_16x16x32_bf16 a[60:63],a[252:255],v[88:91],a[60:63]
v_exp_f32 v104,v104
v_mfma_f32_16x16x32_bf16 v[52:55],v[28:31],v[80:83],0
ds_read_b128 a[164:167],v1 offset:8448
v_mfma_f32_16x16x32_bf16 v[52:55],v[28:31],v[88:91],v[52:55]
s_waitcnt lgkmcnt(5)
ds_read_b128 a[168:171],v1 offset:8704
v_mfma_f32_16x16x32_bf16 v[64:67],a[128:131],v[132:135],v[32:35]
ds_read_b128 a[172:175],v1 offset:8960
v_mfma_f32_16x16x32_bf16 v[64:67],a[132:135],v[140:143],v[64:67]
v_exp_f32 v105,v105
v_mfma_f32_16x16x32_bf16 v[64:67],a[136:139],v[148:151],v[64:67]
ds_read_b128 a[188:191],v1 offset:13056
v_cvt_pk_bf16_f32 v104,v104,v105
v_mfma_f32_16x16x32_bf16 v[64:67],a[140:143],v[156:159],v[64:67]
ds_read_b128 a[184:187],v1 offset:12800
v_mfma_f32_16x16x32_bf16 v[68:71],a[156:159],v[156:159],v[32:35]
s_waitcnt lgkmcnt(5)
ds_read_b128 a[180:183],v1 offset:12544
v_mfma_f32_16x16x32_bf16 v[68:71],a[152:155],v[148:151],v[68:71]
v_exp_f32 v106,v106
v_mfma_f32_16x16x32_bf16 v[68:71],a[148:151],v[140:143],v[68:71]
ds_read_b128 a[176:179],v1 offset:12288
v_add_u32 v1,s78,v1
v_mfma_f32_16x16x32_bf16 v[68:71],a[144:147],v[132:135],v[68:71]
v_exp_f32 v107,v107
v_mfma_f32_16x16x32_bf16 v[72:75],a[160:163],v[132:135],v[32:35]
s_waitcnt lgkmcnt(4)
v_exp_f32 v108,v108
v_mfma_f32_16x16x32_bf16 v[72:75],a[164:167],v[140:143],v[72:75]
v_exp_f32 v109,v109
v_mfma_f32_16x16x32_bf16 v[72:75],a[168:171],v[148:151],v[72:75]
global_load_lds_dwordx4 v5,s[10:11] offset:3072
v_cvt_pk_bf16_f32 v105,v106,v107
v_mfma_f32_16x16x32_bf16 v[72:75],a[172:175],v[156:159],v[72:75]
s_waitcnt lgkmcnt(2)
v_exp_f32 v110,v110
v_mfma_f32_16x16x32_bf16 v[76:79],a[188:191],v[156:159],v[32:35]
v_exp_f32 v111,v111
v_mfma_f32_16x16x32_bf16 v[76:79],a[184:187],v[148:151],v[76:79]
s_waitcnt lgkmcnt(1)
v_cvt_pk_bf16_f32 v106,v108,v109
v_cvt_pk_bf16_f32 v107,v110,v111
v_mfma_f32_16x16x32_bf16 v[76:79],a[180:183],v[140:143],v[76:79]
s_waitcnt lgkmcnt(0)
v_exp_f32 v112,v112
v_mfma_f32_16x16x32_bf16 v[76:79],a[176:179],v[132:135],v[76:79]
v_exp_f32 v113,v113
v_mfma_f32_16x16x32_bf16 a[64:67],a[192:195],v[96:99],0
v_exp_f32 v114,v114
v_mfma_f32_16x16x32_bf16 a[64:67],a[196:199],v[104:107],a[64:67]
v_exp_f32 v115,v115
v_mfma_f32_16x16x32_bf16 a[68:71],a[200:203],v[96:99],0
v_exp_f32 v116,v116
v_mfma_f32_16x16x32_bf16 a[68:71],a[204:207],v[104:107],a[68:71]
v_exp_f32 v117,v117
v_mfma_f32_16x16x32_bf16 a[72:75],a[208:211],v[96:99],0
s_add_u32 s10,s24,s74
s_addc_u32 s11,s25,0
s_add_u32 s74,s74,16384
s_and_b32 s74,s74,0x3ffff
s_or_b32 s74,s74,s69
s_lshl_b32 s69,s60,13
s_add_u32 s69,s69,131072
v_add_u32 v8,s69,v6
v_mfma_f32_16x16x32_bf16 a[72:75],a[212:215],v[104:107],a[72:75]
s_lshl_b32 s69,s60,13
ds_write_b128 v8,v[24:27] offset:4096
v_mfma_f32_16x16x32_bf16 a[76:79],a[216:219],v[96:99],0
s_add_u32 m0,s76,16384
ds_write_b32 v8,v7 offset:5120
v_cvt_pk_bf16_f32 v112,v112,v113
v_mfma_f32_16x16x32_bf16 a[76:79],a[220:223],v[104:107],a[76:79]
s_sub_u32 s76,s76,32768
v_bfe_u32 v24,v6,4,4
v_cvt_pk_bf16_f32 v113,v114,v115
v_mfma_f32_16x16x32_bf16 a[80:83],a[224:227],v[96:99],0
global_load_lds_dwordx4 v4,s[10:11]
v_lshrrev_b32 v7,8,v6
v_lshlrev_b32 v7,4,v7
v_lshl_add_u32 v7,v24,8,v7
v_mfma_f32_16x16x32_bf16 a[80:83],a[228:231],v[104:107],a[80:83]
s_and_b32 s76,s76,0x1ffff
v_exp_f32 v118,v118
v_mfma_f32_16x16x32_bf16 a[84:87],a[232:235],v[96:99],0
s_add_u32 s69,s69,131072
v_exp_f32 v119,v119
v_mfma_f32_16x16x32_bf16 a[84:87],a[236:239],v[104:107],a[84:87]
v_cvt_pk_bf16_f32 v114,v116,v117
v_cvt_pk_bf16_f32 v115,v118,v119
v_mfma_f32_16x16x32_bf16 a[88:91],a[240:243],v[96:99],0
v_exp_f32 v120,v120
v_mfma_f32_16x16x32_bf16 a[88:91],a[244:247],v[104:107],a[88:91]
buffer_load_dwordx4 v[8:11],v7,s[20:23],s65 offen nt
v_mfma_f32_16x16x32_bf16 a[92:95],a[248:251],v[96:99],0
v_exp_f32 v121,v121
v_mfma_f32_16x16x32_bf16 a[92:95],a[252:255],v[104:107],a[92:95]
v_exp_f32 v122,v122
v_mfma_f32_16x16x32_bf16 v[56:59],v[28:31],v[96:99],0
v_exp_f32 v123,v123
v_mfma_f32_16x16x32_bf16 v[56:59],v[28:31],v[104:107],v[56:59]
v_exp_f32 v124,v124
v_mfma_f32_16x16x32_bf16 v[80:83],a[128:131],v[164:167],v[36:39]
buffer_load_dwordx4 v[12:15],v7,s[20:23],s65 offen offset:64 nt
v_mfma_f32_16x16x32_bf16 v[80:83],a[132:135],v[172:175],v[80:83]
v_exp_f32 v125,v125
v_mfma_f32_16x16x32_bf16 v[80:83],a[136:139],v[180:183],v[80:83]
v_exp_f32 v126,v126
v_mfma_f32_16x16x32_bf16 v[80:83],a[140:143],v[188:191],v[80:83]
v_exp_f32 v127,v127
v_mfma_f32_16x16x32_bf16 v[84:87],a[156:159],v[188:191],v[36:39]
v_cvt_pk_bf16_f32 v120,v120,v121
v_cvt_pk_bf16_f32 v121,v122,v123
v_mfma_f32_16x16x32_bf16 v[84:87],a[152:155],v[180:183],v[84:87]
buffer_load_dwordx4 v[16:19],v7,s[20:23],s65 offen offset:128 nt
v_mfma_f32_16x16x32_bf16 v[84:87],a[148:151],v[172:175],v[84:87]
v_cvt_pk_bf16_f32 v122,v124,v125
v_cvt_pk_bf16_f32 v123,v126,v127
v_mfma_f32_16x16x32_bf16 v[84:87],a[144:147],v[164:167],v[84:87]
v_exp_f32 v64,v64
v_mfma_f32_16x16x32_bf16 v[88:91],a[160:163],v[164:167],v[36:39]
v_exp_f32 v65,v65
v_mfma_f32_16x16x32_bf16 v[88:91],a[164:167],v[172:175],v[88:91]
v_exp_f32 v66,v66
v_mfma_f32_16x16x32_bf16 v[88:91],a[168:171],v[180:183],v[88:91]
v_exp_f32 v67,v67
v_mfma_f32_16x16x32_bf16 v[88:91],a[172:175],v[188:191],v[88:91]
global_load_lds_dwordx4 v5,s[10:11] offset:1024
v_cvt_pk_bf16_f32 v64,v64,v65
v_mfma_f32_16x16x32_bf16 v[92:95],a[188:191],v[188:191],v[36:39]
v_exp_f32 v68,v68
v_mfma_f32_16x16x32_bf16 v[92:95],a[184:187],v[180:183],v[92:95]
v_exp_f32 v69,v69
v_mfma_f32_16x16x32_bf16 v[92:95],a[180:183],v[172:175],v[92:95]
v_exp_f32 v70,v70
v_mfma_f32_16x16x32_bf16 v[92:95],a[176:179],v[164:167],v[92:95]
v_exp_f32 v71,v71
v_mfma_f32_16x16x32_bf16 a[96:99],a[192:195],v[112:115],0
buffer_load_dwordx4 v[20:23],v7,s[20:23],s65 offen offset:192 nt
v_mfma_f32_16x16x32_bf16 a[96:99],a[196:199],v[120:123],a[96:99]
ds_read_b64_tr_b16 a[192:193],v2
ds_read_b64_tr_b16 a[194:195],v2 offset:4096
v_mfma_f32_16x16x32_bf16 a[100:103],a[200:203],v[112:115],0
ds_read_b64_tr_b16 a[196:197],v2 offset:8192
ds_read_b64_tr_b16 a[198:199],v2 offset:12288
v_mfma_f32_16x16x32_bf16 a[100:103],a[204:207],v[120:123],a[100:103]
ds_read_b64_tr_b16 a[200:201],v3
ds_read_b64_tr_b16 a[202:203],v3 offset:4096
v_mfma_f32_16x16x32_bf16 a[104:107],a[208:211],v[112:115],0
ds_read_b64_tr_b16 a[204:205],v3 offset:8192
)A"
R"A(ds_read_b64_tr_b16 a[206:207],v3 offset:12288
v_mfma_f32_16x16x32_bf16 a[104:107],a[212:215],v[120:123],a[104:107]
ds_read_b64_tr_b16 a[208:209],v2 offset:256
v_cvt_pk_bf16_f32 v65,v66,v67
v_mfma_f32_16x16x32_bf16 a[108:111],a[216:219],v[112:115],0
global_load_lds_dwordx4 v4,s[10:11] offset:2048
ds_read_b64_tr_b16 a[210:211],v2 offset:4352
v_mfma_f32_16x16x32_bf16 a[108:111],a[220:223],v[120:123],a[108:111]
ds_read_b64_tr_b16 a[212:213],v2 offset:8448
ds_read_b64_tr_b16 a[214:215],v2 offset:12544
v_mfma_f32_16x16x32_bf16 a[112:115],a[224:227],v[112:115],0
ds_read_b64_tr_b16 a[216:217],v3 offset:256
ds_read_b64_tr_b16 a[218:219],v3 offset:4352
v_mfma_f32_16x16x32_bf16 a[112:115],a[228:231],v[120:123],a[112:115]
v_cvt_pk_bf16_f32 v66,v68,v69
v_cvt_pk_bf16_f32 v67,v70,v71
v_mfma_f32_16x16x32_bf16 a[116:119],a[232:235],v[112:115],0
v_exp_f32 v72,v72
v_mfma_f32_16x16x32_bf16 a[116:119],a[236:239],v[120:123],a[116:119]
v_exp_f32 v73,v73
v_mfma_f32_16x16x32_bf16 a[120:123],a[240:243],v[112:115],0
v_exp_f32 v74,v74
v_mfma_f32_16x16x32_bf16 a[120:123],a[244:247],v[120:123],a[120:123]
v_exp_f32 v75,v75
v_mfma_f32_16x16x32_bf16 a[124:127],a[248:251],v[112:115],0
v_exp_f32 v76,v76
v_mfma_f32_16x16x32_bf16 a[124:127],a[252:255],v[120:123],a[124:127]
v_exp_f32 v77,v77
v_mfma_f32_16x16x32_bf16 v[60:63],v[28:31],v[112:115],0
v_exp_f32 v78,v78
v_mfma_f32_16x16x32_bf16 v[60:63],v[28:31],v[120:123],v[60:63]
global_load_lds_dwordx4 v5,s[10:11] offset:3072
ds_read_b64_tr_b16 a[220:221],v3 offset:8448
v_mfma_f32_16x16x32_bf16 v[96:99],a[128:131],v[196:199],v[40:43]
v_exp_f32 v79,v79
v_mfma_f32_16x16x32_bf16 v[96:99],a[132:135],v[204:207],v[96:99]
ds_read_b64_tr_b16 a[222:223],v3 offset:12544
v_cvt_pk_bf16_f32 v72,v72,v73
v_mfma_f32_16x16x32_bf16 v[96:99],a[136:139],v[212:215],v[96:99]
v_cvt_pk_bf16_f32 v73,v74,v75
v_cvt_pk_bf16_f32 v74,v76,v77
v_mfma_f32_16x16x32_bf16 v[96:99],a[140:143],v[220:223],v[96:99]
ds_read_b64_tr_b16 a[224:225],v2 offset:512
v_cvt_pk_bf16_f32 v75,v78,v79
v_mfma_f32_16x16x32_bf16 v[100:103],a[156:159],v[220:223],v[40:43]
ds_read_b64_tr_b16 a[226:227],v2 offset:4608
ds_read_b64_tr_b16 a[228:229],v2 offset:8704
v_mfma_f32_16x16x32_bf16 v[100:103],a[152:155],v[212:215],v[100:103]
ds_read_b64_tr_b16 a[230:231],v2 offset:12800
ds_read_b64_tr_b16 a[232:233],v3 offset:512
v_mfma_f32_16x16x32_bf16 v[100:103],a[148:151],v[204:207],v[100:103]
ds_read_b64_tr_b16 a[234:235],v3 offset:4608
ds_read_b64_tr_b16 a[236:237],v3 offset:8704
v_mfma_f32_16x16x32_bf16 v[100:103],a[144:147],v[196:199],v[100:103]
ds_read_b64_tr_b16 a[238:239],v3 offset:12800
ds_read_b64_tr_b16 a[240:241],v2 offset:768
v_mfma_f32_16x16x32_bf16 v[104:107],a[160:163],v[196:199],v[40:43]
ds_read_b64_tr_b16 a[242:243],v2 offset:4864
v_add_u32 v7,s69,v6
v_mfma_f32_16x16x32_bf16 v[104:107],a[164:167],v[204:207],v[104:107]
s_add_u32 s10,s20,s74
s_addc_u32 s11,s21,0
ds_read_b64_tr_b16 a[244:245],v2 offset:8960
v_mfma_f32_16x16x32_bf16 v[104:107],a[168:171],v[212:215],v[104:107]
s_and_b32 s69,s75,0xfffc0000
ds_read_b64_tr_b16 a[246:247],v2 offset:13056
ds_read_b64_tr_b16 a[248:249],v3 offset:768
v_mfma_f32_16x16x32_bf16 v[104:107],a[172:175],v[220:223],v[104:107]
s_add_u32 m0,s76,0
ds_read_b64_tr_b16 a[250:251],v3 offset:4864
ds_read_b64_tr_b16 a[252:253],v3 offset:8960
v_mfma_f32_16x16x32_bf16 v[108:111],a[188:191],v[220:223],v[40:43]
s_add_u32 s75,s75,16384
ds_read_b64_tr_b16 a[254:255],v3 offset:13056
v_add_u32 v2,s78,v2
v_mfma_f32_16x16x32_bf16 v[108:111],a[184:187],v[212:215],v[108:111]
global_load_lds_dwordx4 v4,s[10:11] nt
v_add_u32 v3,s78,v3
v_mfma_f32_16x16x32_bf16 v[108:111],a[180:183],v[204:207],v[108:111]
s_sub_u32 s78,s77,32768
v_exp_f32 v80,v80
v_mfma_f32_16x16x32_bf16 v[108:111],a[176:179],v[196:199],v[108:111]
s_and_b32 s78,s78,0x1ffff
v_exp_f32 v81,v81
s_waitcnt lgkmcnt(7)
v_mfma_f32_16x16x32_bf16 a[0:3],a[192:195],v[64:67],a[0:3]
s_sub_u32 s78,s78,s77
v_exp_f32 v82,v82
v_mfma_f32_16x16x32_bf16 a[0:3],a[196:199],v[72:75],a[0:3]
s_add_u32 s77,s77,s78
v_exp_f32 v83,v83
v_mfma_f32_16x16x32_bf16 a[4:7],a[200:203],v[64:67],a[4:7]
v_exp_f32 v84,v84
v_mfma_f32_16x16x32_bf16 a[4:7],a[204:207],v[72:75],a[4:7]
v_exp_f32 v85,v85
v_mfma_f32_16x16x32_bf16 a[8:11],a[208:211],v[64:67],a[8:11]
v_exp_f32 v86,v86
v_mfma_f32_16x16x32_bf16 a[8:11],a[212:215],v[72:75],a[8:11]
v_exp_f32 v87,v87
v_mfma_f32_16x16x32_bf16 a[12:15],a[216:219],v[64:67],a[12:15]
v_cvt_pk_bf16_f32 v80,v80,v81
v_cvt_pk_bf16_f32 v81,v82,v83
v_mfma_f32_16x16x32_bf16 a[12:15],a[220:223],v[72:75],a[12:15]
v_cvt_pk_bf16_f32 v82,v84,v85
v_cvt_pk_bf16_f32 v83,v86,v87
v_mfma_f32_16x16x32_bf16 a[16:19],a[224:227],v[64:67],a[16:19]
v_exp_f32 v88,v88
v_mfma_f32_16x16x32_bf16 a[16:19],a[228:231],v[72:75],a[16:19]
v_exp_f32 v89,v89
v_mfma_f32_16x16x32_bf16 a[20:23],a[232:235],v[64:67],a[20:23]
L_pd5_%=:
s_cbranch_execz L_trt0_%=
v_exp_f32 v90,v90
v_mfma_f32_16x16x32_bf16 a[20:23],a[236:239],v[72:75],a[20:23]
s_waitcnt lgkmcnt(0)
v_exp_f32 v91,v91
v_mfma_f32_16x16x32_bf16 a[24:27],a[240:243],v[64:67],a[24:27]
v_exp_f32 v92,v92
v_mfma_f32_16x16x32_bf16 a[24:27],a[244:247],v[72:75],a[24:27]
v_exp_f32 v93,v93
v_mfma_f32_16x16x32_bf16 a[28:31],a[248:251],v[64:67],a[28:31]
v_exp_f32 v94,v94
v_mfma_f32_16x16x32_bf16 a[28:31],a[252:255],v[72:75],a[28:31]
s_getpc_b64 s[92:93]
s_cmp_lg_u32 s4,0
s_cbranch_scc1 L_pace_%=
v_mfma_f32_16x16x32_bf16 v[48:51],v[28:31],v[64:67],v[48:51]
v_exp_f32 v95,v95
v_mfma_f32_16x16x32_bf16 v[48:51],v[28:31],v[72:75],v[48:51]
s_add_u32 s10,s20,s74
s_addc_u32 s11,s21,0
v_cvt_pk_bf16_f32 v88,v88,v89
s_waitcnt vmcnt(13)
s_barrier
v_mfma_f32_16x16x32_bf16 v[112:115],a[128:131],v[228:231],v[44:47]
s_add_u32 m0,s76,0
v_cvt_pk_bf16_f32 v89,v90,v91
v_cvt_pk_bf16_f32 v90,v92,v93
v_mfma_f32_16x16x32_bf16 v[112:115],a[132:135],v[236:239],v[112:115]
ds_read_b128 a[128:131],v1
v_cvt_pk_bf16_f32 v91,v94,v95
v_mfma_f32_16x16x32_bf16 v[112:115],a[136:139],v[244:247],v[112:115]
global_load_lds_dwordx4 v4,s[10:11] offset:1024 nt
ds_read_b128 a[132:135],v1 offset:256
v_mfma_f32_16x16x32_bf16 v[112:115],a[140:143],v[252:255],v[112:115]
v_exp_f32 v96,v96
v_mfma_f32_16x16x32_bf16 v[116:119],a[156:159],v[252:255],v[44:47]
v_exp_f32 v97,v97
v_mfma_f32_16x16x32_bf16 v[116:119],a[152:155],v[244:247],v[116:119]
ds_read_b128 a[136:139],v1 offset:512
v_cvt_pk_bf16_f32 v96,v96,v97
v_mfma_f32_16x16x32_bf16 v[116:119],a[148:151],v[236:239],v[116:119]
v_exp_f32 v98,v98
v_mfma_f32_16x16x32_bf16 v[116:119],a[144:147],v[228:231],v[116:119]
v_exp_f32 v99,v99
v_mfma_f32_16x16x32_bf16 v[120:123],a[160:163],v[228:231],v[44:47]
ds_read_b128 a[140:143],v1 offset:768
v_cvt_pk_bf16_f32 v97,v98,v99
v_mfma_f32_16x16x32_bf16 v[120:123],a[164:167],v[236:239],v[120:123]
v_exp_f32 v100,v100
v_mfma_f32_16x16x32_bf16 v[120:123],a[168:171],v[244:247],v[120:123]
v_exp_f32 v101,v101
v_mfma_f32_16x16x32_bf16 v[120:123],a[172:175],v[252:255],v[120:123]
ds_read_b128 a[156:159],v1 offset:4864
v_cvt_pk_bf16_f32 v98,v100,v101
v_mfma_f32_16x16x32_bf16 v[124:127],a[188:191],v[252:255],v[44:47]
v_exp_f32 v102,v102
v_mfma_f32_16x16x32_bf16 v[124:127],a[184:187],v[244:247],v[124:127]
v_exp_f32 v103,v103
v_mfma_f32_16x16x32_bf16 v[124:127],a[180:183],v[236:239],v[124:127]
ds_read_b128 a[152:155],v1 offset:4608
v_cvt_pk_bf16_f32 v99,v102,v103
v_mfma_f32_16x16x32_bf16 v[124:127],a[176:179],v[228:231],v[124:127]
v_exp_f32 v104,v104
v_mfma_f32_16x16x32_bf16 a[32:35],a[192:195],v[80:83],a[32:35]
global_load_lds_dwordx4 v5,s[10:11] offset:2048 nt
ds_read_b128 a[148:151],v1 offset:4352
v_mfma_f32_16x16x32_bf16 a[32:35],a[196:199],v[88:91],a[32:35]
v_exp_f32 v105,v105
v_mfma_f32_16x16x32_bf16 a[36:39],a[200:203],v[80:83],a[36:39]
ds_read_b128 a[144:147],v1 offset:4096
v_cvt_pk_bf16_f32 v104,v104,v105
v_mfma_f32_16x16x32_bf16 a[36:39],a[204:207],v[88:91],a[36:39]
v_exp_f32 v106,v106
v_mfma_f32_16x16x32_bf16 a[40:43],a[208:211],v[80:83],a[40:43]
v_exp_f32 v107,v107
v_mfma_f32_16x16x32_bf16 a[40:43],a[212:215],v[88:91],a[40:43]
ds_read_b128 a[160:163],v1 offset:8192
v_cvt_pk_bf16_f32 v105,v106,v107
v_mfma_f32_16x16x32_bf16 a[44:47],a[216:219],v[80:83],a[44:47]
v_exp_f32 v108,v108
v_mfma_f32_16x16x32_bf16 a[44:47],a[220:223],v[88:91],a[44:47]
v_exp_f32 v109,v109
v_mfma_f32_16x16x32_bf16 a[48:51],a[224:227],v[80:83],a[48:51]
ds_read_b128 a[164:167],v1 offset:8448
v_cvt_pk_bf16_f32 v106,v108,v109
v_mfma_f32_16x16x32_bf16 a[48:51],a[228:231],v[88:91],a[48:51]
v_exp_f32 v110,v110
v_mfma_f32_16x16x32_bf16 a[52:55],a[232:235],v[80:83],a[52:55]
v_exp_f32 v111,v111
v_mfma_f32_16x16x32_bf16 a[52:55],a[236:239],v[88:91],a[52:55]
ds_read_b128 a[168:171],v1 offset:8704
v_cvt_pk_bf16_f32 v107,v110,v111
v_mfma_f32_16x16x32_bf16 a[56:59],a[240:243],v[80:83],a[56:59]
v_exp_f32 v112,v112
v_mfma_f32_16x16x32_bf16 a[56:59],a[244:247],v[88:91],a[56:59]
v_exp_f32 v113,v113
v_mfma_f32_16x16x32_bf16 a[60:63],a[248:251],v[80:83],a[60:63]
global_load_lds_dwordx4 v5,s[10:11] offset:3072
ds_read_b128 a[172:175],v1 offset:8960
v_mfma_f32_16x16x32_bf16 a[60:63],a[252:255],v[88:91],a[60:63]
ds_read_b128 a[188:191],v1 offset:13056
v_cvt_pk_bf16_f32 v112,v112,v113
v_mfma_f32_16x16x32_bf16 v[52:55],v[28:31],v[80:83],v[52:55]
v_exp_f32 v114,v114
v_mfma_f32_16x16x32_bf16 v[52:55],v[28:31],v[88:91],v[52:55]
s_waitcnt lgkmcnt(3)
v_exp_f32 v115,v115
v_mfma_f32_16x16x32_bf16 v[64:67],a[128:131],v[132:135],v[32:35]
ds_read_b128 a[184:187],v1 offset:12800
v_cvt_pk_bf16_f32 v113,v114,v115
v_mfma_f32_16x16x32_bf16 v[64:67],a[132:135],v[140:143],v[64:67]
v_exp_f32 v116,v116
v_mfma_f32_16x16x32_bf16 v[64:67],a[136:139],v[148:151],v[64:67]
ds_read_b128 a[180:183],v1 offset:12544
v_mfma_f32_16x16x32_bf16 v[64:67],a[140:143],v[156:159],v[64:67]
ds_read_b128 a[176:179],v1 offset:12288
v_add_u32 v1,s78,v1
v_mfma_f32_16x16x32_bf16 v[68:71],a[156:159],v[156:159],v[32:35]
v_exp_f32 v117,v117
v_mfma_f32_16x16x32_bf16 v[68:71],a[152:155],v[148:151],v[68:71]
v_exp_f32 v118,v118
v_mfma_f32_16x16x32_bf16 v[68:71],a[148:151],v[140:143],v[68:71]
s_add_u32 s10,s24,s74
s_addc_u32 s11,s25,0
v_cvt_pk_bf16_f32 v114,v116,v117
v_mfma_f32_16x16x32_bf16 v[68:71],a[144:147],v[132:135],v[68:71]
s_add_u32 s74,s74,16384
v_exp_f32 v119,v119
v_mfma_f32_16x16x32_bf16 v[72:75],a[160:163],v[132:135],v[32:35]
s_add_u32 m0,s76,16384
v_exp_f32 v120,v120
v_mfma_f32_16x16x32_bf16 v[72:75],a[164:167],v[140:143],v[72:75]
s_and_b32 s74,s74,0x3ffff
v_exp_f32 v121,v121
s_waitcnt lgkmcnt(2)
v_mfma_f32_16x16x32_bf16 v[72:75],a[168:171],v[148:151],v[72:75]
global_load_lds_dwordx4 v4,s[10:11]
v_cvt_pk_bf16_f32 v115,v118,v119
v_mfma_f32_16x16x32_bf16 v[72:75],a[172:175],v[156:159],v[72:75]
s_or_b32 s74,s74,s69
v_exp_f32 v122,v122
v_mfma_f32_16x16x32_bf16 v[76:79],a[188:191],v[156:159],v[32:35]
s_sub_u32 s76,s76,32768
v_exp_f32 v123,v123
v_mfma_f32_16x16x32_bf16 v[76:79],a[184:187],v[148:151],v[76:79]
s_and_b32 s76,s76,0x1ffff
v_exp_f32 v124,v124
s_waitcnt lgkmcnt(0)
v_mfma_f32_16x16x32_bf16 v[76:79],a[180:183],v[140:143],v[76:79]
s_and_b32 s69,s75,0xfffc0000
v_exp_f32 v125,v125
v_mfma_f32_16x16x32_bf16 v[76:79],a[176:179],v[132:135],v[76:79]
v_exp_f32 v126,v126
v_mfma_f32_16x16x32_bf16 a[64:67],a[192:195],v[96:99],a[64:67]
v_exp_f32 v127,v127
v_mfma_f32_16x16x32_bf16 a[64:67],a[196:199],v[104:107],a[64:67]
v_cvt_pk_bf16_f32 v120,v120,v121
v_cvt_pk_bf16_f32 v121,v122,v123
v_mfma_f32_16x16x32_bf16 a[68:71],a[200:203],v[96:99],a[68:71]
v_cvt_pk_bf16_f32 v122,v124,v125
v_cvt_pk_bf16_f32 v123,v126,v127
v_mfma_f32_16x16x32_bf16 a[68:71],a[204:207],v[104:107],a[68:71]
v_exp_f32 v64,v64
v_mfma_f32_16x16x32_bf16 a[72:75],a[208:211],v[96:99],a[72:75]
v_exp_f32 v65,v65
v_mfma_f32_16x16x32_bf16 a[72:75],a[212:215],v[104:107],a[72:75]
v_exp_f32 v66,v66
v_mfma_f32_16x16x32_bf16 a[76:79],a[216:219],v[96:99],a[76:79]
v_exp_f32 v67,v67
v_mfma_f32_16x16x32_bf16 a[76:79],a[220:223],v[104:107],a[76:79]
v_exp_f32 v68,v68
v_mfma_f32_16x16x32_bf16 a[80:83],a[224:227],v[96:99],a[80:83]
global_load_lds_dwordx4 v5,s[10:11] offset:1024
v_cvt_pk_bf16_f32 v64,v64,v65
v_mfma_f32_16x16x32_bf16 a[80:83],a[228:231],v[104:107],a[80:83]
v_exp_f32 v69,v69
)A"
R"A(v_mfma_f32_16x16x32_bf16 a[84:87],a[232:235],v[96:99],a[84:87]
v_exp_f32 v70,v70
v_mfma_f32_16x16x32_bf16 a[84:87],a[236:239],v[104:107],a[84:87]
v_exp_f32 v71,v71
v_mfma_f32_16x16x32_bf16 a[88:91],a[240:243],v[96:99],a[88:91]
v_cvt_pk_bf16_f32 v65,v66,v67
v_cvt_pk_bf16_f32 v66,v68,v69
v_mfma_f32_16x16x32_bf16 a[88:91],a[244:247],v[104:107],a[88:91]
v_exp_f32 v72,v72
v_mfma_f32_16x16x32_bf16 a[92:95],a[248:251],v[96:99],a[92:95]
v_exp_f32 v73,v73
v_mfma_f32_16x16x32_bf16 a[92:95],a[252:255],v[104:107],a[92:95]
v_cvt_pk_bf16_f32 v67,v70,v71
v_cvt_pk_bf16_f32 v72,v72,v73
v_mfma_f32_16x16x32_bf16 v[56:59],v[28:31],v[96:99],v[56:59]
v_exp_f32 v74,v74
v_mfma_f32_16x16x32_bf16 v[56:59],v[28:31],v[104:107],v[56:59]
v_exp_f32 v75,v75
v_mfma_f32_16x16x32_bf16 v[80:83],a[128:131],v[164:167],v[36:39]
v_exp_f32 v76,v76
v_mfma_f32_16x16x32_bf16 v[80:83],a[132:135],v[172:175],v[80:83]
v_exp_f32 v77,v77
v_mfma_f32_16x16x32_bf16 v[80:83],a[136:139],v[180:183],v[80:83]
v_exp_f32 v78,v78
v_mfma_f32_16x16x32_bf16 v[80:83],a[140:143],v[188:191],v[80:83]
v_exp_f32 v79,v79
v_mfma_f32_16x16x32_bf16 v[84:87],a[156:159],v[188:191],v[36:39]
v_cvt_pk_bf16_f32 v73,v74,v75
v_cvt_pk_bf16_f32 v74,v76,v77
v_mfma_f32_16x16x32_bf16 v[84:87],a[152:155],v[180:183],v[84:87]
global_load_lds_dwordx4 v4,s[10:11] offset:2048
v_cvt_pk_bf16_f32 v75,v78,v79
v_mfma_f32_16x16x32_bf16 v[84:87],a[148:151],v[172:175],v[84:87]
v_exp_f32 v80,v80
v_mfma_f32_16x16x32_bf16 v[84:87],a[144:147],v[164:167],v[84:87]
v_exp_f32 v81,v81
v_mfma_f32_16x16x32_bf16 v[88:91],a[160:163],v[164:167],v[36:39]
v_exp_f32 v82,v82
v_mfma_f32_16x16x32_bf16 v[88:91],a[164:167],v[172:175],v[88:91]
v_exp_f32 v83,v83
v_mfma_f32_16x16x32_bf16 v[88:91],a[168:171],v[180:183],v[88:91]
v_cvt_pk_bf16_f32 v80,v80,v81
v_cvt_pk_bf16_f32 v81,v82,v83
v_mfma_f32_16x16x32_bf16 v[88:91],a[172:175],v[188:191],v[88:91]
v_exp_f32 v84,v84
v_mfma_f32_16x16x32_bf16 v[92:95],a[188:191],v[188:191],v[36:39]
v_exp_f32 v85,v85
v_mfma_f32_16x16x32_bf16 v[92:95],a[184:187],v[180:183],v[92:95]
v_exp_f32 v86,v86
v_mfma_f32_16x16x32_bf16 v[92:95],a[180:183],v[172:175],v[92:95]
v_exp_f32 v87,v87
v_mfma_f32_16x16x32_bf16 v[92:95],a[176:179],v[164:167],v[92:95]
v_cvt_pk_bf16_f32 v82,v84,v85
v_cvt_pk_bf16_f32 v83,v86,v87
v_mfma_f32_16x16x32_bf16 a[96:99],a[192:195],v[112:115],a[96:99]
ds_read_b64_tr_b16 a[192:193],v2
ds_read_b64_tr_b16 a[194:195],v2 offset:4096
v_mfma_f32_16x16x32_bf16 a[96:99],a[196:199],v[120:123],a[96:99]
ds_read_b64_tr_b16 a[196:197],v2 offset:8192
ds_read_b64_tr_b16 a[198:199],v2 offset:12288
v_mfma_f32_16x16x32_bf16 a[100:103],a[200:203],v[112:115],a[100:103]
ds_read_b64_tr_b16 a[200:201],v3
ds_read_b64_tr_b16 a[202:203],v3 offset:4096
v_mfma_f32_16x16x32_bf16 a[100:103],a[204:207],v[120:123],a[100:103]
global_load_lds_dwordx4 v5,s[10:11] offset:3072
ds_read_b64_tr_b16 a[204:205],v3 offset:8192
v_mfma_f32_16x16x32_bf16 a[104:107],a[208:211],v[112:115],a[104:107]
ds_read_b64_tr_b16 a[206:207],v3 offset:12288
ds_read_b64_tr_b16 a[208:209],v2 offset:256
v_mfma_f32_16x16x32_bf16 a[104:107],a[212:215],v[120:123],a[104:107]
ds_read_b64_tr_b16 a[210:211],v2 offset:4352
ds_read_b64_tr_b16 a[212:213],v2 offset:8448
v_mfma_f32_16x16x32_bf16 a[108:111],a[216:219],v[112:115],a[108:111]
v_exp_f32 v88,v88
v_mfma_f32_16x16x32_bf16 a[108:111],a[220:223],v[120:123],a[108:111]
ds_read_b64_tr_b16 a[214:215],v2 offset:12544
ds_read_b64_tr_b16 a[216:217],v3 offset:256
v_mfma_f32_16x16x32_bf16 a[112:115],a[224:227],v[112:115],a[112:115]
ds_read_b64_tr_b16 a[218:219],v3 offset:4352
ds_read_b64_tr_b16 a[220:221],v3 offset:8448
v_mfma_f32_16x16x32_bf16 a[112:115],a[228:231],v[120:123],a[112:115]
ds_read_b64_tr_b16 a[222:223],v3 offset:12544
ds_read_b64_tr_b16 a[224:225],v2 offset:512
v_mfma_f32_16x16x32_bf16 a[116:119],a[232:235],v[112:115],a[116:119]
v_exp_f32 v89,v89
v_mfma_f32_16x16x32_bf16 a[116:119],a[236:239],v[120:123],a[116:119]
ds_read_b64_tr_b16 a[226:227],v2 offset:4608
ds_read_b64_tr_b16 a[228:229],v2 offset:8704
v_mfma_f32_16x16x32_bf16 a[120:123],a[240:243],v[112:115],a[120:123]
ds_read_b64_tr_b16 a[230:231],v2 offset:12800
ds_read_b64_tr_b16 a[232:233],v3 offset:512
v_mfma_f32_16x16x32_bf16 a[120:123],a[244:247],v[120:123],a[120:123]
s_add_u32 s10,s20,s74
s_addc_u32 s11,s21,0
ds_read_b64_tr_b16 a[234:235],v3 offset:4608
v_mfma_f32_16x16x32_bf16 a[124:127],a[248:251],v[112:115],a[124:127]
ds_read_b64_tr_b16 a[236:237],v3 offset:8704
ds_read_b64_tr_b16 a[238:239],v3 offset:12800
v_mfma_f32_16x16x32_bf16 a[124:127],a[252:255],v[120:123],a[124:127]
s_add_u32 m0,s76,0
ds_read_b64_tr_b16 a[240:241],v2 offset:768
ds_read_b64_tr_b16 a[242:243],v2 offset:4864
v_mfma_f32_16x16x32_bf16 v[60:63],v[28:31],v[112:115],v[60:63]
ds_read_b64_tr_b16 a[244:245],v2 offset:8960
v_cvt_pk_bf16_f32 v88,v88,v89
v_mfma_f32_16x16x32_bf16 v[60:63],v[28:31],v[120:123],v[60:63]
global_load_lds_dwordx4 v4,s[10:11] nt
ds_read_b64_tr_b16 a[246:247],v2 offset:13056
v_mfma_f32_16x16x32_bf16 v[96:99],a[128:131],v[196:199],v[40:43]
ds_read_b64_tr_b16 a[248:249],v3 offset:768
ds_read_b64_tr_b16 a[250:251],v3 offset:4864
v_mfma_f32_16x16x32_bf16 v[96:99],a[132:135],v[204:207],v[96:99]
ds_read_b64_tr_b16 a[252:253],v3 offset:8960
ds_read_b64_tr_b16 a[254:255],v3 offset:13056
v_mfma_f32_16x16x32_bf16 v[96:99],a[136:139],v[212:215],v[96:99]
v_exp_f32 v90,v90
v_mfma_f32_16x16x32_bf16 v[96:99],a[140:143],v[220:223],v[96:99]
v_exp_f32 v91,v91
v_mfma_f32_16x16x32_bf16 v[100:103],a[156:159],v[220:223],v[40:43]
v_exp_f32 v92,v92
v_mfma_f32_16x16x32_bf16 v[100:103],a[152:155],v[212:215],v[100:103]
v_exp_f32 v93,v93
v_mfma_f32_16x16x32_bf16 v[100:103],a[148:151],v[204:207],v[100:103]
v_exp_f32 v94,v94
v_mfma_f32_16x16x32_bf16 v[100:103],a[144:147],v[196:199],v[100:103]
v_exp_f32 v95,v95
v_mfma_f32_16x16x32_bf16 v[104:107],a[160:163],v[196:199],v[40:43]
v_cvt_pk_bf16_f32 v89,v90,v91
v_cvt_pk_bf16_f32 v90,v92,v93
v_mfma_f32_16x16x32_bf16 v[104:107],a[164:167],v[204:207],v[104:107]
v_cvt_pk_bf16_f32 v91,v94,v95
v_add_u32 v2,s78,v2
v_mfma_f32_16x16x32_bf16 v[104:107],a[168:171],v[212:215],v[104:107]
v_exp_f32 v96,v96
v_mfma_f32_16x16x32_bf16 v[104:107],a[172:175],v[220:223],v[104:107]
v_exp_f32 v97,v97
v_mfma_f32_16x16x32_bf16 v[108:111],a[188:191],v[220:223],v[40:43]
v_exp_f32 v98,v98
v_mfma_f32_16x16x32_bf16 v[108:111],a[184:187],v[212:215],v[108:111]
global_load_lds_dwordx4 v4,s[10:11] offset:1024 nt
v_cvt_pk_bf16_f32 v96,v96,v97
v_mfma_f32_16x16x32_bf16 v[108:111],a[180:183],v[204:207],v[108:111]
v_exp_f32 v99,v99
v_mfma_f32_16x16x32_bf16 v[108:111],a[176:179],v[196:199],v[108:111]
s_waitcnt lgkmcnt(0)
v_exp_f32 v100,v100
v_mfma_f32_16x16x32_bf16 a[0:3],a[192:195],v[64:67],a[0:3]
v_exp_f32 v101,v101
v_mfma_f32_16x16x32_bf16 a[0:3],a[196:199],v[72:75],a[0:3]
v_exp_f32 v102,v102
v_mfma_f32_16x16x32_bf16 a[4:7],a[200:203],v[64:67],a[4:7]
v_exp_f32 v103,v103
v_mfma_f32_16x16x32_bf16 a[4:7],a[204:207],v[72:75],a[4:7]
v_cvt_pk_bf16_f32 v97,v98,v99
v_cvt_pk_bf16_f32 v98,v100,v101
v_mfma_f32_16x16x32_bf16 a[8:11],a[208:211],v[64:67],a[8:11]
v_cvt_pk_bf16_f32 v99,v102,v103
v_add_u32 v3,s78,v3
v_mfma_f32_16x16x32_bf16 a[8:11],a[212:215],v[72:75],a[8:11]
s_sub_u32 s78,s77,32768
v_exp_f32 v104,v104
v_mfma_f32_16x16x32_bf16 a[12:15],a[216:219],v[64:67],a[12:15]
s_and_b32 s78,s78,0x1ffff
v_exp_f32 v105,v105
v_mfma_f32_16x16x32_bf16 a[12:15],a[220:223],v[72:75],a[12:15]
s_sub_u32 s78,s78,s77
v_exp_f32 v106,v106
v_mfma_f32_16x16x32_bf16 a[16:19],a[224:227],v[64:67],a[16:19]
s_add_u32 s77,s77,s78
v_exp_f32 v107,v107
v_mfma_f32_16x16x32_bf16 a[16:19],a[228:231],v[72:75],a[16:19]
v_exp_f32 v108,v108
v_mfma_f32_16x16x32_bf16 a[20:23],a[232:235],v[64:67],a[20:23]
s_getreg_b32 s92,hwreg(HW_REG_IB_STS,0,4)
s_min_u32 s4,s92,s6
s_mov_b32 s6,s92
v_mfma_f32_16x16x32_bf16 a[20:23],a[236:239],v[72:75],a[20:23]
s_cmp_gt_u32 s4,s9
s_cselect_b32 s4,15,s5
v_cvt_pk_bf16_f32 v104,v104,v105
v_mfma_f32_16x16x32_bf16 a[24:27],a[240:243],v[64:67],a[24:27]
v_exp_f32 v109,v109
v_mfma_f32_16x16x32_bf16 a[24:27],a[244:247],v[72:75],a[24:27]
v_exp_f32 v110,v110
v_mfma_f32_16x16x32_bf16 a[28:31],a[248:251],v[64:67],a[28:31]
v_exp_f32 v111,v111
v_mfma_f32_16x16x32_bf16 a[28:31],a[252:255],v[72:75],a[28:31]
s_getpc_b64 s[92:93]
s_cmp_lg_u32 s4,0
s_cbranch_scc1 L_pace_%=
v_mfma_f32_16x16x32_bf16 v[48:51],v[28:31],v[64:67],v[48:51]
v_cvt_pk_bf16_f32 v105,v106,v107
v_cvt_pk_bf16_f32 v106,v108,v109
v_mfma_f32_16x16x32_bf16 v[48:51],v[28:31],v[72:75],v[48:51]
s_add_u32 s10,s20,s74
s_addc_u32 s11,s21,0
v_cvt_pk_bf16_f32 v107,v110,v111
s_waitcnt vmcnt(10)
s_barrier
v_mfma_f32_16x16x32_bf16 v[24:27],v[8:11],v[128:131],0
s_add_u32 m0,s76,0
v_mfma_f32_16x16x32_bf16 v[24:27],v[12:15],v[136:139],v[24:27]
v_mfma_f32_16x16x32_bf16 v[24:27],v[16:19],v[144:147],v[24:27]
global_load_lds_dwordx4 v5,s[10:11] offset:2048 nt
v_mfma_f32_16x16x32_bf16 v[24:27],v[20:23],v[152:155],v[24:27]
v_mfma_f32_16x16x32_bf16 v[112:115],a[128:131],v[228:231],v[44:47]
ds_read_b128 a[128:131],v1
v_mfma_f32_16x16x32_bf16 v[112:115],a[132:135],v[236:239],v[112:115]
ds_read_b128 a[132:135],v1 offset:256
v_mfma_f32_16x16x32_bf16 v[112:115],a[136:139],v[244:247],v[112:115]
ds_read_b128 a[136:139],v1 offset:512
v_mfma_f32_16x16x32_bf16 v[112:115],a[140:143],v[252:255],v[112:115]
ds_read_b128 a[140:143],v1 offset:768
v_mfma_f32_16x16x32_bf16 v[116:119],a[156:159],v[252:255],v[44:47]
v_max3_f32 v24,v24,v25,v26
v_max_f32 v24,v24,v27
v_mfma_f32_16x16x32_bf16 v[116:119],a[152:155],v[244:247],v[116:119]
ds_read_b128 a[156:159],v1 offset:4864
v_mov_b32 v25,v24
v_mfma_f32_16x16x32_bf16 v[116:119],a[148:151],v[236:239],v[116:119]
v_exp_f32 v112,v112
v_mfma_f32_16x16x32_bf16 v[116:119],a[144:147],v[228:231],v[116:119]
v_permlane32_swap_b32 v24,v25
v_mfma_f32_16x16x32_bf16 v[120:123],a[160:163],v[228:231],v[44:47]
v_max_f32 v24,v24,v25
v_mov_b32 v25,v24
v_mfma_f32_16x16x32_bf16 v[120:123],a[164:167],v[236:239],v[120:123]
v_exp_f32 v113,v113
v_mfma_f32_16x16x32_bf16 v[120:123],a[168:171],v[244:247],v[120:123]
v_permlane16_swap_b32 v24,v25
v_mfma_f32_16x16x32_bf16 v[120:123],a[172:175],v[252:255],v[120:123]
v_max_f32 v24,v24,v25
v_sub_f32 v27,0,v24
v_mfma_f32_16x16x32_bf16 v[124:127],a[188:191],v[252:255],v[44:47]
global_load_lds_dwordx4 v5,s[10:11] offset:3072
v_sub_f32 v26,0,v24
v_sub_f32 v25,0,v24
v_sub_f32 v24,0,v24
v_mfma_f32_16x16x32_bf16 v[124:127],a[184:187],v[244:247],v[124:127]
ds_write_b128 v7,v[24:27]
v_mfma_f32_16x16x32_bf16 v[124:127],a[180:183],v[236:239],v[124:127]
ds_read_b128 a[152:155],v1 offset:4608
v_cvt_pk_bf16_f32 v112,v112,v113
v_mfma_f32_16x16x32_bf16 v[124:127],a[176:179],v[228:231],v[124:127]
v_exp_f32 v114,v114
v_mfma_f32_16x16x32_bf16 v[24:27],v[8:11],v[160:163],0
v_exp_f32 v115,v115
v_mfma_f32_16x16x32_bf16 v[24:27],v[12:15],v[168:171],v[24:27]
ds_read_b128 a[148:151],v1 offset:4352
v_cvt_pk_bf16_f32 v113,v114,v115
v_mfma_f32_16x16x32_bf16 v[24:27],v[16:19],v[176:179],v[24:27]
v_exp_f32 v116,v116
v_mfma_f32_16x16x32_bf16 v[24:27],v[20:23],v[184:187],v[24:27]
v_exp_f32 v117,v117
v_mfma_f32_16x16x32_bf16 a[32:35],a[192:195],v[80:83],a[32:35]
ds_read_b128 a[144:147],v1 offset:4096
v_cvt_pk_bf16_f32 v114,v116,v117
v_mfma_f32_16x16x32_bf16 a[32:35],a[196:199],v[88:91],a[32:35]
v_exp_f32 v118,v118
v_mfma_f32_16x16x32_bf16 a[36:39],a[200:203],v[80:83],a[36:39]
s_add_u32 s10,s24,s74
s_addc_u32 s11,s25,0
ds_read_b128 a[160:163],v1 offset:8192
v_mfma_f32_16x16x32_bf16 a[36:39],a[204:207],v[88:91],a[36:39]
s_add_u32 s74,s74,16384
v_max3_f32 v24,v24,v25,v26
v_max_f32 v24,v24,v27
v_mfma_f32_16x16x32_bf16 a[40:43],a[208:211],v[80:83],a[40:43]
s_add_u32 m0,s76,16384
ds_read_b128 a[164:167],v1 offset:8448
v_mov_b32 v25,v24
v_mfma_f32_16x16x32_bf16 a[40:43],a[212:215],v[88:91],a[40:43]
s_and_b32 s74,s74,0x3ffff
v_exp_f32 v119,v119
v_mfma_f32_16x16x32_bf16 a[44:47],a[216:219],v[80:83],a[44:47]
global_load_lds_dwordx4 v4,s[10:11]
ds_read_b128 a[168:171],v1 offset:8704
v_mfma_f32_16x16x32_bf16 a[44:47],a[220:223],v[88:91],a[44:47]
s_or_b32 s74,s74,s69
v_permlane32_swap_b32 v24,v25
v_mfma_f32_16x16x32_bf16 a[48:51],a[224:227],v[80:83],a[48:51]
)A"
R"A(s_sub_u32 s76,s76,32768
v_max_f32 v24,v24,v25
v_mov_b32 v25,v24
v_mfma_f32_16x16x32_bf16 a[48:51],a[228:231],v[88:91],a[48:51]
s_and_b32 s76,s76,0x1ffff
ds_read_b128 a[172:175],v1 offset:8960
v_cvt_pk_bf16_f32 v115,v118,v119
v_mfma_f32_16x16x32_bf16 a[52:55],a[232:235],v[80:83],a[52:55]
v_permlane16_swap_b32 v24,v25
v_mfma_f32_16x16x32_bf16 a[52:55],a[236:239],v[88:91],a[52:55]
v_max_f32 v24,v24,v25
v_sub_f32 v27,0,v24
v_mfma_f32_16x16x32_bf16 a[56:59],a[240:243],v[80:83],a[56:59]
v_sub_f32 v26,0,v24
v_sub_f32 v25,0,v24
v_mfma_f32_16x16x32_bf16 a[56:59],a[244:247],v[88:91],a[56:59]
ds_read_b128 a[188:191],v1 offset:13056
v_sub_f32 v24,0,v24
v_mfma_f32_16x16x32_bf16 a[60:63],a[248:251],v[80:83],a[60:63]
ds_write_b128 v7,v[24:27] offset:1024
v_mfma_f32_16x16x32_bf16 a[60:63],a[252:255],v[88:91],a[60:63]
v_exp_f32 v120,v120
v_mfma_f32_16x16x32_bf16 v[52:55],v[28:31],v[80:83],v[52:55]
v_exp_f32 v121,v121
v_mfma_f32_16x16x32_bf16 v[52:55],v[28:31],v[88:91],v[52:55]
ds_read_b128 a[184:187],v1 offset:12800
v_cvt_pk_bf16_f32 v120,v120,v121
v_mfma_f32_16x16x32_bf16 v[24:27],v[8:11],v[192:195],0
v_exp_f32 v122,v122
v_mfma_f32_16x16x32_bf16 v[24:27],v[12:15],v[200:203],v[24:27]
v_exp_f32 v123,v123
v_mfma_f32_16x16x32_bf16 v[24:27],v[16:19],v[208:211],v[24:27]
s_waitcnt lgkmcnt(2)
ds_read_b128 a[180:183],v1 offset:12544
v_cvt_pk_bf16_f32 v121,v122,v123
v_mfma_f32_16x16x32_bf16 v[24:27],v[20:23],v[216:219],v[24:27]
global_load_lds_dwordx4 v5,s[10:11] offset:1024
ds_read_b128 a[176:179],v1 offset:12288
v_mfma_f32_16x16x32_bf16 v[64:67],a[128:131],v[132:135],v[32:35]
v_exp_f32 v124,v124
v_mfma_f32_16x16x32_bf16 v[64:67],a[132:135],v[140:143],v[64:67]
v_exp_f32 v125,v125
v_mfma_f32_16x16x32_bf16 v[64:67],a[136:139],v[148:151],v[64:67]
v_exp_f32 v126,v126
v_mfma_f32_16x16x32_bf16 v[64:67],a[140:143],v[156:159],v[64:67]
v_max3_f32 v24,v24,v25,v26
v_max_f32 v24,v24,v27
v_mfma_f32_16x16x32_bf16 v[68:71],a[156:159],v[156:159],v[32:35]
v_mov_b32 v25,v24
v_cvt_pk_bf16_f32 v122,v124,v125
v_mfma_f32_16x16x32_bf16 v[68:71],a[152:155],v[148:151],v[68:71]
v_exp_f32 v127,v127
v_mfma_f32_16x16x32_bf16 v[68:71],a[148:151],v[140:143],v[68:71]
v_permlane32_swap_b32 v24,v25
v_mfma_f32_16x16x32_bf16 v[68:71],a[144:147],v[132:135],v[68:71]
v_max_f32 v24,v24,v25
v_mov_b32 v25,v24
v_mfma_f32_16x16x32_bf16 v[72:75],a[160:163],v[132:135],v[32:35]
v_cvt_pk_bf16_f32 v123,v126,v127
v_add_u32 v1,s78,v1
v_mfma_f32_16x16x32_bf16 v[72:75],a[164:167],v[140:143],v[72:75]
v_permlane16_swap_b32 v24,v25
v_mfma_f32_16x16x32_bf16 v[72:75],a[168:171],v[148:151],v[72:75]
v_max_f32 v24,v24,v25
v_sub_f32 v27,0,v24
v_mfma_f32_16x16x32_bf16 v[72:75],a[172:175],v[156:159],v[72:75]
v_sub_f32 v26,0,v24
v_sub_f32 v25,0,v24
v_mfma_f32_16x16x32_bf16 v[76:79],a[188:191],v[156:159],v[32:35]
s_waitcnt lgkmcnt(0)
v_exp_f32 v64,v64
v_mfma_f32_16x16x32_bf16 v[76:79],a[184:187],v[148:151],v[76:79]
global_load_lds_dwordx4 v4,s[10:11] offset:2048
v_sub_f32 v24,0,v24
v_mfma_f32_16x16x32_bf16 v[76:79],a[180:183],v[140:143],v[76:79]
ds_write_b128 v7,v[24:27] offset:2048
v_mfma_f32_16x16x32_bf16 v[76:79],a[176:179],v[132:135],v[76:79]
v_exp_f32 v65,v65
v_mfma_f32_16x16x32_bf16 a[64:67],a[192:195],v[96:99],a[64:67]
v_exp_f32 v66,v66
v_mfma_f32_16x16x32_bf16 a[64:67],a[196:199],v[104:107],a[64:67]
v_exp_f32 v67,v67
v_mfma_f32_16x16x32_bf16 a[68:71],a[200:203],v[96:99],a[68:71]
v_exp_f32 v68,v68
v_mfma_f32_16x16x32_bf16 a[68:71],a[204:207],v[104:107],a[68:71]
v_exp_f32 v69,v69
v_mfma_f32_16x16x32_bf16 a[72:75],a[208:211],v[96:99],a[72:75]
v_exp_f32 v70,v70
v_mfma_f32_16x16x32_bf16 a[72:75],a[212:215],v[104:107],a[72:75]
v_exp_f32 v71,v71
v_mfma_f32_16x16x32_bf16 a[76:79],a[216:219],v[96:99],a[76:79]
v_cvt_pk_bf16_f32 v64,v64,v65
v_cvt_pk_bf16_f32 v65,v66,v67
v_mfma_f32_16x16x32_bf16 a[76:79],a[220:223],v[104:107],a[76:79]
v_cvt_pk_bf16_f32 v66,v68,v69
v_cvt_pk_bf16_f32 v67,v70,v71
v_mfma_f32_16x16x32_bf16 a[80:83],a[224:227],v[96:99],a[80:83]
v_exp_f32 v72,v72
v_mfma_f32_16x16x32_bf16 a[80:83],a[228:231],v[104:107],a[80:83]
v_exp_f32 v73,v73
v_mfma_f32_16x16x32_bf16 a[84:87],a[232:235],v[96:99],a[84:87]
v_exp_f32 v74,v74
v_mfma_f32_16x16x32_bf16 a[84:87],a[236:239],v[104:107],a[84:87]
v_exp_f32 v75,v75
v_mfma_f32_16x16x32_bf16 a[88:91],a[240:243],v[96:99],a[88:91]
global_load_lds_dwordx4 v5,s[10:11] offset:3072
v_cvt_pk_bf16_f32 v72,v72,v73
v_mfma_f32_16x16x32_bf16 a[88:91],a[244:247],v[104:107],a[88:91]
v_exp_f32 v76,v76
v_mfma_f32_16x16x32_bf16 a[92:95],a[248:251],v[96:99],a[92:95]
v_exp_f32 v77,v77
v_mfma_f32_16x16x32_bf16 a[92:95],a[252:255],v[104:107],a[92:95]
v_exp_f32 v78,v78
v_mfma_f32_16x16x32_bf16 v[56:59],v[28:31],v[96:99],v[56:59]
v_exp_f32 v79,v79
v_mfma_f32_16x16x32_bf16 v[56:59],v[28:31],v[104:107],v[56:59]
v_cvt_pk_bf16_f32 v73,v74,v75
v_cvt_pk_bf16_f32 v74,v76,v77
v_mfma_f32_16x16x32_bf16 v[24:27],v[8:11],v[224:227],0
v_cvt_pk_bf16_f32 v75,v78,v79
v_mfma_f32_16x16x32_bf16 v[24:27],v[12:15],v[232:235],v[24:27]
v_mfma_f32_16x16x32_bf16 v[24:27],v[16:19],v[240:243],v[24:27]
v_mfma_f32_16x16x32_bf16 v[24:27],v[20:23],v[248:251],v[24:27]
v_mfma_f32_16x16x32_bf16 v[80:83],a[128:131],v[164:167],v[36:39]
s_add_u32 s10,s20,s74
s_addc_u32 s11,s21,0
v_mfma_f32_16x16x32_bf16 v[80:83],a[132:135],v[172:175],v[80:83]
v_mfma_f32_16x16x32_bf16 v[80:83],a[136:139],v[180:183],v[80:83]
s_add_u32 m0,s76,0
v_mfma_f32_16x16x32_bf16 v[80:83],a[140:143],v[188:191],v[80:83]
v_mfma_f32_16x16x32_bf16 v[84:87],a[156:159],v[188:191],v[36:39]
global_load_lds_dwordx4 v4,s[10:11] nt
v_max3_f32 v24,v24,v25,v26
v_mfma_f32_16x16x32_bf16 v[84:87],a[152:155],v[180:183],v[84:87]
v_max_f32 v24,v24,v27
v_mov_b32 v25,v24
v_mfma_f32_16x16x32_bf16 v[84:87],a[148:151],v[172:175],v[84:87]
v_mfma_f32_16x16x32_bf16 v[84:87],a[144:147],v[164:167],v[84:87]
v_permlane32_swap_b32 v24,v25
v_mfma_f32_16x16x32_bf16 v[88:91],a[160:163],v[164:167],v[36:39]
v_max_f32 v24,v24,v25
v_mov_b32 v25,v24
v_mfma_f32_16x16x32_bf16 v[88:91],a[164:167],v[172:175],v[88:91]
v_exp_f32 v80,v80
v_mfma_f32_16x16x32_bf16 v[88:91],a[168:171],v[180:183],v[88:91]
v_permlane16_swap_b32 v24,v25
v_mfma_f32_16x16x32_bf16 v[88:91],a[172:175],v[188:191],v[88:91]
v_max_f32 v24,v24,v25
v_max_f32 v24,s72,v24
v_mfma_f32_16x16x32_bf16 v[92:95],a[188:191],v[188:191],v[36:39]
v_sub_f32 v27,0,v24
v_sub_f32 v26,0,v24
v_mfma_f32_16x16x32_bf16 v[92:95],a[184:187],v[180:183],v[92:95]
v_sub_f32 v25,0,v24
v_sub_f32 v24,0,v24
v_mfma_f32_16x16x32_bf16 v[92:95],a[180:183],v[172:175],v[92:95]
v_exp_f32 v81,v81
v_mfma_f32_16x16x32_bf16 v[92:95],a[176:179],v[164:167],v[92:95]
v_exp_f32 v82,v82
v_mfma_f32_16x16x32_bf16 a[96:99],a[192:195],v[112:115],a[96:99]
ds_read_b64_tr_b16 a[192:193],v2
s_nop 0
L_pd6_%=:
s_cbranch_execz L_trt0_%=
ds_read_b64_tr_b16 a[194:195],v2 offset:4096
v_mfma_f32_16x16x32_bf16 a[96:99],a[196:199],v[120:123],a[96:99]
ds_read_b64_tr_b16 a[196:197],v2 offset:8192
ds_read_b64_tr_b16 a[198:199],v2 offset:12288
v_mfma_f32_16x16x32_bf16 a[100:103],a[200:203],v[112:115],a[100:103]
ds_read_b64_tr_b16 a[200:201],v3
ds_read_b64_tr_b16 a[202:203],v3 offset:4096
v_mfma_f32_16x16x32_bf16 a[100:103],a[204:207],v[120:123],a[100:103]
global_load_lds_dwordx4 v4,s[10:11] offset:1024 nt
ds_read_b64_tr_b16 a[204:205],v3 offset:8192
v_mfma_f32_16x16x32_bf16 a[104:107],a[208:211],v[112:115],a[104:107]
ds_read_b64_tr_b16 a[206:207],v3 offset:12288
ds_read_b64_tr_b16 a[208:209],v2 offset:256
v_mfma_f32_16x16x32_bf16 a[104:107],a[212:215],v[120:123],a[104:107]
ds_read_b64_tr_b16 a[210:211],v2 offset:4352
ds_read_b64_tr_b16 a[212:213],v2 offset:8448
v_mfma_f32_16x16x32_bf16 a[108:111],a[216:219],v[112:115],a[108:111]
ds_read_b64_tr_b16 a[214:215],v2 offset:12544
v_cvt_pk_bf16_f32 v80,v80,v81
v_mfma_f32_16x16x32_bf16 a[108:111],a[220:223],v[120:123],a[108:111]
ds_read_b64_tr_b16 a[216:217],v3 offset:256
ds_read_b64_tr_b16 a[218:219],v3 offset:4352
v_mfma_f32_16x16x32_bf16 a[112:115],a[224:227],v[112:115],a[112:115]
v_exp_f32 v83,v83
v_mfma_f32_16x16x32_bf16 a[112:115],a[228:231],v[120:123],a[112:115]
ds_read_b64_tr_b16 a[220:221],v3 offset:8448
ds_read_b64_tr_b16 a[222:223],v3 offset:12544
v_mfma_f32_16x16x32_bf16 a[116:119],a[232:235],v[112:115],a[116:119]
ds_read_b64_tr_b16 a[224:225],v2 offset:512
ds_read_b64_tr_b16 a[226:227],v2 offset:4608
v_mfma_f32_16x16x32_bf16 a[116:119],a[236:239],v[120:123],a[116:119]
ds_read_b64_tr_b16 a[228:229],v2 offset:8704
ds_read_b64_tr_b16 a[230:231],v2 offset:12800
v_mfma_f32_16x16x32_bf16 a[120:123],a[240:243],v[112:115],a[120:123]
ds_read_b64_tr_b16 a[232:233],v3 offset:512
v_cvt_pk_bf16_f32 v81,v82,v83
v_mfma_f32_16x16x32_bf16 a[120:123],a[244:247],v[120:123],a[120:123]
ds_read_b64_tr_b16 a[234:235],v3 offset:4608
ds_read_b64_tr_b16 a[236:237],v3 offset:8704
v_mfma_f32_16x16x32_bf16 a[124:127],a[248:251],v[112:115],a[124:127]
v_exp_f32 v84,v84
v_mfma_f32_16x16x32_bf16 a[124:127],a[252:255],v[120:123],a[124:127]
ds_read_b64_tr_b16 a[238:239],v3 offset:12800
ds_read_b64_tr_b16 a[240:241],v2 offset:768
v_mfma_f32_16x16x32_bf16 v[60:63],v[28:31],v[112:115],v[60:63]
ds_read_b64_tr_b16 a[242:243],v2 offset:4864
ds_read_b64_tr_b16 a[244:245],v2 offset:8960
v_mfma_f32_16x16x32_bf16 v[60:63],v[28:31],v[120:123],v[60:63]
global_load_lds_dwordx4 v5,s[10:11] offset:2048 nt
ds_read_b64_tr_b16 a[246:247],v2 offset:13056
v_mfma_f32_16x16x32_bf16 v[96:99],a[128:131],v[196:199],v[40:43]
ds_read_b64_tr_b16 a[248:249],v3 offset:768
ds_read_b64_tr_b16 a[250:251],v3 offset:4864
v_mfma_f32_16x16x32_bf16 v[96:99],a[132:135],v[204:207],v[96:99]
ds_read_b64_tr_b16 a[252:253],v3 offset:8960
ds_read_b64_tr_b16 a[254:255],v3 offset:13056
v_mfma_f32_16x16x32_bf16 v[96:99],a[136:139],v[212:215],v[96:99]
v_exp_f32 v85,v85
v_mfma_f32_16x16x32_bf16 v[96:99],a[140:143],v[220:223],v[96:99]
v_exp_f32 v86,v86
v_mfma_f32_16x16x32_bf16 v[100:103],a[156:159],v[220:223],v[40:43]
v_exp_f32 v87,v87
v_mfma_f32_16x16x32_bf16 v[100:103],a[152:155],v[212:215],v[100:103]
v_exp_f32 v88,v88
v_mfma_f32_16x16x32_bf16 v[100:103],a[148:151],v[204:207],v[100:103]
v_exp_f32 v89,v89
v_mfma_f32_16x16x32_bf16 v[100:103],a[144:147],v[196:199],v[100:103]
v_exp_f32 v90,v90
v_mfma_f32_16x16x32_bf16 v[104:107],a[160:163],v[196:199],v[40:43]
v_exp_f32 v91,v91
v_mfma_f32_16x16x32_bf16 v[104:107],a[164:167],v[204:207],v[104:107]
v_exp_f32 v92,v92
v_mfma_f32_16x16x32_bf16 v[104:107],a[168:171],v[212:215],v[104:107]
v_exp_f32 v93,v93
v_mfma_f32_16x16x32_bf16 v[104:107],a[172:175],v[220:223],v[104:107]
v_exp_f32 v94,v94
v_mfma_f32_16x16x32_bf16 v[108:111],a[188:191],v[220:223],v[40:43]
v_exp_f32 v95,v95
v_mfma_f32_16x16x32_bf16 v[108:111],a[184:187],v[212:215],v[108:111]
v_exp_f32 v96,v96
v_mfma_f32_16x16x32_bf16 v[108:111],a[180:183],v[204:207],v[108:111]
global_load_lds_dwordx4 v5,s[10:11] offset:3072
v_cvt_pk_bf16_f32 v82,v84,v85
v_mfma_f32_16x16x32_bf16 v[108:111],a[176:179],v[196:199],v[108:111]
s_waitcnt lgkmcnt(0)
v_exp_f32 v97,v97
v_mfma_f32_16x16x32_bf16 a[0:3],a[192:195],v[64:67],a[0:3]
v_exp_f32 v98,v98
v_mfma_f32_16x16x32_bf16 a[0:3],a[196:199],v[72:75],a[0:3]
v_exp_f32 v99,v99
v_mfma_f32_16x16x32_bf16 a[4:7],a[200:203],v[64:67],a[4:7]
v_exp_f32 v100,v100
v_mfma_f32_16x16x32_bf16 a[4:7],a[204:207],v[72:75],a[4:7]
v_exp_f32 v101,v101
v_mfma_f32_16x16x32_bf16 a[8:11],a[208:211],v[64:67],a[8:11]
v_exp_f32 v102,v102
v_mfma_f32_16x16x32_bf16 a[8:11],a[212:215],v[72:75],a[8:11]
v_exp_f32 v103,v103
v_mfma_f32_16x16x32_bf16 a[12:15],a[216:219],v[64:67],a[12:15]
ds_write_b128 v7,v[24:27] offset:3072
v_mfma_f32_16x16x32_bf16 a[12:15],a[220:223],v[72:75],a[12:15]
v_cvt_pk_bf16_f32 v83,v86,v87
v_cvt_pk_bf16_f32 v88,v88,v89
v_mfma_f32_16x16x32_bf16 a[16:19],a[224:227],v[64:67],a[16:19]
v_cvt_pk_bf16_f32 v89,v90,v91
v_cvt_pk_bf16_f32 v90,v92,v93
v_mfma_f32_16x16x32_bf16 a[16:19],a[228:231],v[72:75],a[16:19]
v_cvt_pk_bf16_f32 v91,v94,v95
v_cvt_pk_bf16_f32 v96,v96,v97
v_mfma_f32_16x16x32_bf16 a[20:23],a[232:235],v[64:67],a[20:23]
s_getreg_b32 s92,hwreg(HW_REG_IB_STS,0,4)
s_min_u32 s4,s92,s6
s_mov_b32 s6,s92
v_mfma_f32_16x16x32_bf16 a[20:23],a[236:239],v[72:75],a[20:23]
s_cmp_gt_u32 s4,s9
)A"
R"A(s_cselect_b32 s4,15,s5
v_cvt_pk_bf16_f32 v97,v98,v99
v_mfma_f32_16x16x32_bf16 a[24:27],a[240:243],v[64:67],a[24:27]
v_cvt_pk_bf16_f32 v98,v100,v101
v_cvt_pk_bf16_f32 v99,v102,v103
v_mfma_f32_16x16x32_bf16 a[24:27],a[244:247],v[72:75],a[24:27]
v_mfma_f32_16x16x32_bf16 a[28:31],a[248:251],v[64:67],a[28:31]
v_mfma_f32_16x16x32_bf16 a[28:31],a[252:255],v[72:75],a[28:31]
s_getpc_b64 s[92:93]
s_cmp_lg_u32 s4,0
s_cbranch_scc1 L_pace_%=
v_mfma_f32_16x16x32_bf16 v[48:51],v[28:31],v[64:67],v[48:51]
v_mfma_f32_16x16x32_bf16 v[48:51],v[28:31],v[72:75],v[48:51]
s_mov_b32 s70,11
L_a0_%=:
s_waitcnt vmcnt(12)
s_barrier
v_mfma_f32_16x16x32_bf16 v[112:115],a[128:131],v[228:231],v[44:47]
s_add_u32 s10,s24,s74
s_addc_u32 s11,s25,0
s_add_u32 m0,s76,16384
v_mfma_f32_16x16x32_bf16 v[112:115],a[132:135],v[236:239],v[112:115]
s_add_u32 s75,s75,16384
s_add_u32 s74,s74,16384
ds_read_b128 a[128:131],v1
v_mfma_f32_16x16x32_bf16 v[112:115],a[136:139],v[244:247],v[112:115]
s_and_b32 s74,s74,0x3ffff
global_load_lds_dwordx4 v4,s[10:11]
v_mfma_f32_16x16x32_bf16 v[112:115],a[140:143],v[252:255],v[112:115]
s_and_b32 s69,s75,0xfffc0000
s_or_b32 s74,s74,s69
ds_read_b128 a[132:135],v1 offset:256
v_mfma_f32_16x16x32_bf16 v[116:119],a[156:159],v[252:255],v[44:47]
s_sub_u32 s76,s76,32768
s_and_b32 s76,s76,0x1ffff
v_mfma_f32_16x16x32_bf16 v[116:119],a[152:155],v[244:247],v[116:119]
ds_read_b128 a[136:139],v1 offset:512
v_mfma_f32_16x16x32_bf16 v[116:119],a[148:151],v[236:239],v[116:119]
v_mfma_f32_16x16x32_bf16 v[116:119],a[144:147],v[228:231],v[116:119]
ds_read_b128 a[140:143],v1 offset:768
v_mfma_f32_16x16x32_bf16 v[120:123],a[160:163],v[228:231],v[44:47]
v_mfma_f32_16x16x32_bf16 v[120:123],a[164:167],v[236:239],v[120:123]
ds_read_b128 a[156:159],v1 offset:4864
v_mfma_f32_16x16x32_bf16 v[120:123],a[168:171],v[244:247],v[120:123]
v_mfma_f32_16x16x32_bf16 v[120:123],a[172:175],v[252:255],v[120:123]
ds_read_b128 a[152:155],v1 offset:4608
v_mfma_f32_16x16x32_bf16 v[124:127],a[188:191],v[252:255],v[44:47]
v_exp_f32 v104,v104
v_mfma_f32_16x16x32_bf16 v[124:127],a[184:187],v[244:247],v[124:127]
ds_read_b128 a[148:151],v1 offset:4352
v_add_u32 v2,s78,v2
v_mfma_f32_16x16x32_bf16 v[124:127],a[180:183],v[236:239],v[124:127]
v_exp_f32 v105,v105
v_mfma_f32_16x16x32_bf16 v[124:127],a[176:179],v[228:231],v[124:127]
ds_read_b128 a[144:147],v1 offset:4096
v_cvt_pk_bf16_f32 v104,v104,v105
v_mfma_f32_16x16x32_bf16 a[32:35],a[192:195],v[80:83],a[32:35]
v_exp_f32 v106,v106
v_mfma_f32_16x16x32_bf16 a[32:35],a[196:199],v[88:91],a[32:35]
ds_read_b128 a[160:163],v1 offset:8192
v_add_u32 v3,s78,v3
v_mfma_f32_16x16x32_bf16 a[36:39],a[200:203],v[80:83],a[36:39]
s_sub_u32 s78,s77,32768
global_load_lds_dwordx4 v5,s[10:11] offset:1024
v_mfma_f32_16x16x32_bf16 a[36:39],a[204:207],v[88:91],a[36:39]
s_and_b32 s78,s78,0x1ffff
v_exp_f32 v107,v107
v_mfma_f32_16x16x32_bf16 a[40:43],a[208:211],v[80:83],a[40:43]
s_sub_u32 s78,s78,s77
ds_read_b128 a[164:167],v1 offset:8448
v_cvt_pk_bf16_f32 v105,v106,v107
v_mfma_f32_16x16x32_bf16 a[40:43],a[212:215],v[88:91],a[40:43]
s_add_u32 s77,s77,s78
v_exp_f32 v108,v108
v_mfma_f32_16x16x32_bf16 a[44:47],a[216:219],v[80:83],a[44:47]
v_exp_f32 v109,v109
v_mfma_f32_16x16x32_bf16 a[44:47],a[220:223],v[88:91],a[44:47]
ds_read_b128 a[168:171],v1 offset:8704
v_cvt_pk_bf16_f32 v106,v108,v109
v_mfma_f32_16x16x32_bf16 a[48:51],a[224:227],v[80:83],a[48:51]
v_exp_f32 v110,v110
v_mfma_f32_16x16x32_bf16 a[48:51],a[228:231],v[88:91],a[48:51]
v_exp_f32 v111,v111
v_mfma_f32_16x16x32_bf16 a[52:55],a[232:235],v[80:83],a[52:55]
ds_read_b128 a[172:175],v1 offset:8960
v_cvt_pk_bf16_f32 v107,v110,v111
v_mfma_f32_16x16x32_bf16 a[52:55],a[236:239],v[88:91],a[52:55]
v_exp_f32 v112,v112
v_mfma_f32_16x16x32_bf16 a[56:59],a[240:243],v[80:83],a[56:59]
v_exp_f32 v113,v113
v_mfma_f32_16x16x32_bf16 a[56:59],a[244:247],v[88:91],a[56:59]
ds_read_b128 a[188:191],v1 offset:13056
v_cvt_pk_bf16_f32 v112,v112,v113
v_mfma_f32_16x16x32_bf16 a[60:63],a[248:251],v[80:83],a[60:63]
v_exp_f32 v114,v114
v_mfma_f32_16x16x32_bf16 a[60:63],a[252:255],v[88:91],a[60:63]
v_exp_f32 v115,v115
v_mfma_f32_16x16x32_bf16 v[52:55],v[28:31],v[80:83],v[52:55]
ds_read_b128 a[184:187],v1 offset:12800
v_cvt_pk_bf16_f32 v113,v114,v115
v_mfma_f32_16x16x32_bf16 v[52:55],v[28:31],v[88:91],v[52:55]
s_waitcnt lgkmcnt(3)
v_exp_f32 v116,v116
v_mfma_f32_16x16x32_bf16 v[64:67],a[128:131],v[132:135],v[32:35]
global_load_lds_dwordx4 v4,s[10:11] offset:2048
ds_read_b128 a[180:183],v1 offset:12544
v_mfma_f32_16x16x32_bf16 v[64:67],a[132:135],v[140:143],v[64:67]
v_exp_f32 v117,v117
v_mfma_f32_16x16x32_bf16 v[64:67],a[136:139],v[148:151],v[64:67]
ds_read_b128 a[176:179],v1 offset:12288
v_cvt_pk_bf16_f32 v114,v116,v117
v_mfma_f32_16x16x32_bf16 v[64:67],a[140:143],v[156:159],v[64:67]
v_exp_f32 v118,v118
v_mfma_f32_16x16x32_bf16 v[68:71],a[156:159],v[156:159],v[32:35]
v_exp_f32 v119,v119
v_mfma_f32_16x16x32_bf16 v[68:71],a[152:155],v[148:151],v[68:71]
v_cvt_pk_bf16_f32 v115,v118,v119
v_add_u32 v1,s78,v1
v_mfma_f32_16x16x32_bf16 v[68:71],a[148:151],v[140:143],v[68:71]
v_exp_f32 v120,v120
v_mfma_f32_16x16x32_bf16 v[68:71],a[144:147],v[132:135],v[68:71]
v_exp_f32 v121,v121
v_mfma_f32_16x16x32_bf16 v[72:75],a[160:163],v[132:135],v[32:35]
v_exp_f32 v122,v122
v_mfma_f32_16x16x32_bf16 v[72:75],a[164:167],v[140:143],v[72:75]
v_exp_f32 v123,v123
v_mfma_f32_16x16x32_bf16 v[72:75],a[168:171],v[148:151],v[72:75]
s_waitcnt lgkmcnt(0)
v_exp_f32 v124,v124
v_mfma_f32_16x16x32_bf16 v[72:75],a[172:175],v[156:159],v[72:75]
v_exp_f32 v125,v125
v_mfma_f32_16x16x32_bf16 v[76:79],a[188:191],v[156:159],v[32:35]
v_exp_f32 v126,v126
v_mfma_f32_16x16x32_bf16 v[76:79],a[184:187],v[148:151],v[76:79]
v_exp_f32 v127,v127
v_mfma_f32_16x16x32_bf16 v[76:79],a[180:183],v[140:143],v[76:79]
v_cvt_pk_bf16_f32 v120,v120,v121
v_cvt_pk_bf16_f32 v121,v122,v123
v_mfma_f32_16x16x32_bf16 v[76:79],a[176:179],v[132:135],v[76:79]
v_cvt_pk_bf16_f32 v122,v124,v125
v_cvt_pk_bf16_f32 v123,v126,v127
v_mfma_f32_16x16x32_bf16 a[64:67],a[192:195],v[96:99],a[64:67]
global_load_lds_dwordx4 v5,s[10:11] offset:3072
v_mfma_f32_16x16x32_bf16 a[64:67],a[196:199],v[104:107],a[64:67]
v_exp_f32 v64,v64
v_mfma_f32_16x16x32_bf16 a[68:71],a[200:203],v[96:99],a[68:71]
v_exp_f32 v65,v65
v_mfma_f32_16x16x32_bf16 a[68:71],a[204:207],v[104:107],a[68:71]
v_exp_f32 v66,v66
v_mfma_f32_16x16x32_bf16 a[72:75],a[208:211],v[96:99],a[72:75]
v_exp_f32 v67,v67
v_mfma_f32_16x16x32_bf16 a[72:75],a[212:215],v[104:107],a[72:75]
v_exp_f32 v68,v68
v_mfma_f32_16x16x32_bf16 a[76:79],a[216:219],v[96:99],a[76:79]
v_exp_f32 v69,v69
v_mfma_f32_16x16x32_bf16 a[76:79],a[220:223],v[104:107],a[76:79]
v_exp_f32 v70,v70
v_mfma_f32_16x16x32_bf16 a[80:83],a[224:227],v[96:99],a[80:83]
v_exp_f32 v71,v71
v_mfma_f32_16x16x32_bf16 a[80:83],a[228:231],v[104:107],a[80:83]
v_cvt_pk_bf16_f32 v64,v64,v65
v_cvt_pk_bf16_f32 v65,v66,v67
v_mfma_f32_16x16x32_bf16 a[84:87],a[232:235],v[96:99],a[84:87]
v_cvt_pk_bf16_f32 v66,v68,v69
v_cvt_pk_bf16_f32 v67,v70,v71
v_mfma_f32_16x16x32_bf16 a[84:87],a[236:239],v[104:107],a[84:87]
v_exp_f32 v72,v72
v_mfma_f32_16x16x32_bf16 a[88:91],a[240:243],v[96:99],a[88:91]
s_add_u32 s10,s20,s74
s_addc_u32 s11,s21,0
v_mfma_f32_16x16x32_bf16 a[88:91],a[244:247],v[104:107],a[88:91]
v_exp_f32 v73,v73
v_mfma_f32_16x16x32_bf16 a[92:95],a[248:251],v[96:99],a[92:95]
s_add_u32 m0,s76,0
v_exp_f32 v74,v74
v_mfma_f32_16x16x32_bf16 a[92:95],a[252:255],v[104:107],a[92:95]
v_exp_f32 v75,v75
v_mfma_f32_16x16x32_bf16 v[56:59],v[28:31],v[96:99],v[56:59]
global_load_lds_dwordx4 v4,s[10:11] nt
v_cvt_pk_bf16_f32 v72,v72,v73
v_mfma_f32_16x16x32_bf16 v[56:59],v[28:31],v[104:107],v[56:59]
v_exp_f32 v76,v76
v_mfma_f32_16x16x32_bf16 v[80:83],a[128:131],v[164:167],v[36:39]
v_exp_f32 v77,v77
v_mfma_f32_16x16x32_bf16 v[80:83],a[132:135],v[172:175],v[80:83]
v_exp_f32 v78,v78
v_mfma_f32_16x16x32_bf16 v[80:83],a[136:139],v[180:183],v[80:83]
v_exp_f32 v79,v79
v_mfma_f32_16x16x32_bf16 v[80:83],a[140:143],v[188:191],v[80:83]
v_cvt_pk_bf16_f32 v73,v74,v75
v_cvt_pk_bf16_f32 v74,v76,v77
v_mfma_f32_16x16x32_bf16 v[84:87],a[156:159],v[188:191],v[36:39]
v_cvt_pk_bf16_f32 v75,v78,v79
v_mfma_f32_16x16x32_bf16 v[84:87],a[152:155],v[180:183],v[84:87]
v_mfma_f32_16x16x32_bf16 v[84:87],a[148:151],v[172:175],v[84:87]
v_mfma_f32_16x16x32_bf16 v[84:87],a[144:147],v[164:167],v[84:87]
v_mfma_f32_16x16x32_bf16 v[88:91],a[160:163],v[164:167],v[36:39]
v_exp_f32 v80,v80
v_mfma_f32_16x16x32_bf16 v[88:91],a[164:167],v[172:175],v[88:91]
v_exp_f32 v81,v81
v_mfma_f32_16x16x32_bf16 v[88:91],a[168:171],v[180:183],v[88:91]
v_exp_f32 v82,v82
v_mfma_f32_16x16x32_bf16 v[88:91],a[172:175],v[188:191],v[88:91]
v_exp_f32 v83,v83
v_mfma_f32_16x16x32_bf16 v[92:95],a[188:191],v[188:191],v[36:39]
v_exp_f32 v84,v84
v_mfma_f32_16x16x32_bf16 v[92:95],a[184:187],v[180:183],v[92:95]
v_exp_f32 v85,v85
v_mfma_f32_16x16x32_bf16 v[92:95],a[180:183],v[172:175],v[92:95]
global_load_lds_dwordx4 v4,s[10:11] offset:1024 nt
v_cvt_pk_bf16_f32 v80,v80,v81
v_mfma_f32_16x16x32_bf16 v[92:95],a[176:179],v[164:167],v[92:95]
v_exp_f32 v86,v86
v_mfma_f32_16x16x32_bf16 a[96:99],a[192:195],v[112:115],a[96:99]
ds_read_b64_tr_b16 a[192:193],v2
ds_read_b64_tr_b16 a[194:195],v2 offset:4096
v_mfma_f32_16x16x32_bf16 a[96:99],a[196:199],v[120:123],a[96:99]
ds_read_b64_tr_b16 a[196:197],v2 offset:8192
v_cvt_pk_bf16_f32 v81,v82,v83
v_mfma_f32_16x16x32_bf16 a[100:103],a[200:203],v[112:115],a[100:103]
ds_read_b64_tr_b16 a[198:199],v2 offset:12288
v_cvt_pk_bf16_f32 v82,v84,v85
v_mfma_f32_16x16x32_bf16 a[100:103],a[204:207],v[120:123],a[100:103]
v_exp_f32 v87,v87
v_mfma_f32_16x16x32_bf16 a[104:107],a[208:211],v[112:115],a[104:107]
ds_read_b64_tr_b16 a[200:201],v3
ds_read_b64_tr_b16 a[202:203],v3 offset:4096
v_mfma_f32_16x16x32_bf16 a[104:107],a[212:215],v[120:123],a[104:107]
ds_read_b64_tr_b16 a[204:205],v3 offset:8192
v_cvt_pk_bf16_f32 v83,v86,v87
v_mfma_f32_16x16x32_bf16 a[108:111],a[216:219],v[112:115],a[108:111]
v_exp_f32 v88,v88
v_mfma_f32_16x16x32_bf16 a[108:111],a[220:223],v[120:123],a[108:111]
ds_read_b64_tr_b16 a[206:207],v3 offset:12288
ds_read_b64_tr_b16 a[208:209],v2 offset:256
v_mfma_f32_16x16x32_bf16 a[112:115],a[224:227],v[112:115],a[112:115]
v_exp_f32 v89,v89
v_mfma_f32_16x16x32_bf16 a[112:115],a[228:231],v[120:123],a[112:115]
ds_read_b64_tr_b16 a[210:211],v2 offset:4352
ds_read_b64_tr_b16 a[212:213],v2 offset:8448
v_mfma_f32_16x16x32_bf16 a[116:119],a[232:235],v[112:115],a[116:119]
ds_read_b64_tr_b16 a[214:215],v2 offset:12544
v_cvt_pk_bf16_f32 v88,v88,v89
v_mfma_f32_16x16x32_bf16 a[116:119],a[236:239],v[120:123],a[116:119]
v_exp_f32 v90,v90
v_mfma_f32_16x16x32_bf16 a[120:123],a[240:243],v[112:115],a[120:123]
ds_read_b64_tr_b16 a[216:217],v3 offset:256
ds_read_b64_tr_b16 a[218:219],v3 offset:4352
v_mfma_f32_16x16x32_bf16 a[120:123],a[244:247],v[120:123],a[120:123]
v_exp_f32 v91,v91
v_mfma_f32_16x16x32_bf16 a[124:127],a[248:251],v[112:115],a[124:127]
global_load_lds_dwordx4 v5,s[10:11] offset:2048 nt
ds_read_b64_tr_b16 a[220:221],v3 offset:8448
v_mfma_f32_16x16x32_bf16 a[124:127],a[252:255],v[120:123],a[124:127]
ds_read_b64_tr_b16 a[222:223],v3 offset:12544
ds_read_b64_tr_b16 a[224:225],v2 offset:512
v_mfma_f32_16x16x32_bf16 v[60:63],v[28:31],v[112:115],v[60:63]
ds_read_b64_tr_b16 a[226:227],v2 offset:4608
v_cvt_pk_bf16_f32 v89,v90,v91
v_mfma_f32_16x16x32_bf16 v[60:63],v[28:31],v[120:123],v[60:63]
v_exp_f32 v92,v92
v_mfma_f32_16x16x32_bf16 v[96:99],a[128:131],v[196:199],v[40:43]
ds_read_b64_tr_b16 a[228:229],v2 offset:8704
ds_read_b64_tr_b16 a[230:231],v2 offset:12800
v_mfma_f32_16x16x32_bf16 v[96:99],a[132:135],v[204:207],v[96:99]
v_exp_f32 v93,v93
v_mfma_f32_16x16x32_bf16 v[96:99],a[136:139],v[212:215],v[96:99]
ds_read_b64_tr_b16 a[232:233],v3 offset:512
ds_read_b64_tr_b16 a[234:235],v3 offset:4608
v_mfma_f32_16x16x32_bf16 v[96:99],a[140:143],v[220:223],v[96:99]
ds_read_b64_tr_b16 a[236:237],v3 offset:8704
v_cvt_pk_bf16_f32 v90,v92,v93
v_mfma_f32_16x16x32_bf16 v[100:103],a[156:159],v[220:223],v[40:43]
v_exp_f32 v94,v94
v_mfma_f32_16x16x32_bf16 v[100:103],a[152:155],v[212:215],v[100:103]
)A"
R"A(ds_read_b64_tr_b16 a[238:239],v3 offset:12800
ds_read_b64_tr_b16 a[240:241],v2 offset:768
v_mfma_f32_16x16x32_bf16 v[100:103],a[148:151],v[204:207],v[100:103]
v_exp_f32 v95,v95
v_mfma_f32_16x16x32_bf16 v[100:103],a[144:147],v[196:199],v[100:103]
ds_read_b64_tr_b16 a[242:243],v2 offset:4864
ds_read_b64_tr_b16 a[244:245],v2 offset:8960
v_mfma_f32_16x16x32_bf16 v[104:107],a[160:163],v[196:199],v[40:43]
ds_read_b64_tr_b16 a[246:247],v2 offset:13056
v_cvt_pk_bf16_f32 v91,v94,v95
v_mfma_f32_16x16x32_bf16 v[104:107],a[164:167],v[204:207],v[104:107]
v_exp_f32 v96,v96
v_mfma_f32_16x16x32_bf16 v[104:107],a[168:171],v[212:215],v[104:107]
ds_read_b64_tr_b16 a[248:249],v3 offset:768
ds_read_b64_tr_b16 a[250:251],v3 offset:4864
v_mfma_f32_16x16x32_bf16 v[104:107],a[172:175],v[220:223],v[104:107]
v_exp_f32 v97,v97
v_mfma_f32_16x16x32_bf16 v[108:111],a[188:191],v[220:223],v[40:43]
global_load_lds_dwordx4 v5,s[10:11] offset:3072
ds_read_b64_tr_b16 a[252:253],v3 offset:8960
v_mfma_f32_16x16x32_bf16 v[108:111],a[184:187],v[212:215],v[108:111]
ds_read_b64_tr_b16 a[254:255],v3 offset:13056
v_cvt_pk_bf16_f32 v96,v96,v97
v_mfma_f32_16x16x32_bf16 v[108:111],a[180:183],v[204:207],v[108:111]
v_exp_f32 v98,v98
v_mfma_f32_16x16x32_bf16 v[108:111],a[176:179],v[196:199],v[108:111]
s_waitcnt lgkmcnt(5)
v_exp_f32 v99,v99
v_mfma_f32_16x16x32_bf16 a[0:3],a[192:195],v[64:67],a[0:3]
v_exp_f32 v100,v100
v_mfma_f32_16x16x32_bf16 a[0:3],a[196:199],v[72:75],a[0:3]
v_exp_f32 v101,v101
v_mfma_f32_16x16x32_bf16 a[4:7],a[200:203],v[64:67],a[4:7]
v_exp_f32 v102,v102
v_mfma_f32_16x16x32_bf16 a[4:7],a[204:207],v[72:75],a[4:7]
v_exp_f32 v103,v103
v_mfma_f32_16x16x32_bf16 a[8:11],a[208:211],v[64:67],a[8:11]
v_cvt_pk_bf16_f32 v97,v98,v99
v_cvt_pk_bf16_f32 v98,v100,v101
v_mfma_f32_16x16x32_bf16 a[8:11],a[212:215],v[72:75],a[8:11]
v_cvt_pk_bf16_f32 v99,v102,v103
v_mfma_f32_16x16x32_bf16 a[12:15],a[216:219],v[64:67],a[12:15]
v_mfma_f32_16x16x32_bf16 a[12:15],a[220:223],v[72:75],a[12:15]
v_mfma_f32_16x16x32_bf16 a[16:19],a[224:227],v[64:67],a[16:19]
v_mfma_f32_16x16x32_bf16 a[16:19],a[228:231],v[72:75],a[16:19]
v_mfma_f32_16x16x32_bf16 a[20:23],a[232:235],v[64:67],a[20:23]
s_getreg_b32 s92,hwreg(HW_REG_IB_STS,0,4)
s_min_u32 s4,s92,s6
s_mov_b32 s6,s92
v_mfma_f32_16x16x32_bf16 a[20:23],a[236:239],v[72:75],a[20:23]
s_cmp_ge_u32 s4,s9
s_cselect_b32 s4,15,s5
v_mfma_f32_16x16x32_bf16 a[24:27],a[240:243],v[64:67],a[24:27]
s_waitcnt lgkmcnt(0)
v_mfma_f32_16x16x32_bf16 a[24:27],a[244:247],v[72:75],a[24:27]
v_mfma_f32_16x16x32_bf16 a[28:31],a[248:251],v[64:67],a[28:31]
v_mfma_f32_16x16x32_bf16 a[28:31],a[252:255],v[72:75],a[28:31]
s_getpc_b64 s[92:93]
s_cmp_lg_u32 s4,0
s_cbranch_scc1 L_pace_%=
v_mfma_f32_16x16x32_bf16 v[48:51],v[28:31],v[64:67],v[48:51]
v_mfma_f32_16x16x32_bf16 v[48:51],v[28:31],v[72:75],v[48:51]
s_sub_u32 s70,s70,1
s_cbranch_scc0 L_a0_%=
s_waitcnt vmcnt(12)
s_barrier
v_mfma_f32_16x16x32_bf16 v[112:115],a[128:131],v[228:231],v[44:47]
s_add_u32 s10,s24,s74
s_addc_u32 s11,s25,0
s_add_u32 m0,s76,16384
v_mfma_f32_16x16x32_bf16 v[112:115],a[132:135],v[236:239],v[112:115]
s_add_u32 s75,s75,16384
s_add_u32 s74,s74,16384
ds_read_b128 v[32:35],v7
v_mfma_f32_16x16x32_bf16 v[112:115],a[136:139],v[244:247],v[112:115]
s_and_b32 s74,s74,0x3ffff
global_load_lds_dwordx4 v4,s[10:11]
v_mfma_f32_16x16x32_bf16 v[112:115],a[140:143],v[252:255],v[112:115]
s_and_b32 s69,s75,0xfffc0000
s_or_b32 s74,s74,s69
ds_read_b128 a[128:131],v1
v_mfma_f32_16x16x32_bf16 v[116:119],a[156:159],v[252:255],v[44:47]
s_sub_u32 s76,s76,32768
v_exp_f32 v104,v104
v_mfma_f32_16x16x32_bf16 v[116:119],a[152:155],v[244:247],v[116:119]
s_and_b32 s76,s76,0x1ffff
ds_read_b128 a[132:135],v1 offset:256
v_add_u32 v2,s78,v2
v_mfma_f32_16x16x32_bf16 v[116:119],a[148:151],v[236:239],v[116:119]
s_min_u32 s7,s7,1
v_exp_f32 v105,v105
v_mfma_f32_16x16x32_bf16 v[116:119],a[144:147],v[228:231],v[116:119]
s_min_u32 s4,s5,1
ds_read_b128 a[136:139],v1 offset:512
v_cvt_pk_bf16_f32 v104,v104,v105
v_mfma_f32_16x16x32_bf16 v[120:123],a[160:163],v[228:231],v[44:47]
v_exp_f32 v106,v106
v_mfma_f32_16x16x32_bf16 v[120:123],a[164:167],v[236:239],v[120:123]
ds_read_b128 a[140:143],v1 offset:768
v_add_u32 v3,s78,v3
v_mfma_f32_16x16x32_bf16 v[120:123],a[168:171],v[244:247],v[120:123]
s_sub_u32 s78,s77,32768
v_exp_f32 v107,v107
v_mfma_f32_16x16x32_bf16 v[120:123],a[172:175],v[252:255],v[120:123]
s_and_b32 s78,s78,0x1ffff
ds_read_b128 a[156:159],v1 offset:4864
v_cvt_pk_bf16_f32 v105,v106,v107
v_mfma_f32_16x16x32_bf16 v[124:127],a[188:191],v[252:255],v[44:47]
s_sub_u32 s78,s78,s77
v_exp_f32 v108,v108
v_mfma_f32_16x16x32_bf16 v[124:127],a[184:187],v[244:247],v[124:127]
s_add_u32 s77,s77,s78
ds_read_b128 a[152:155],v1 offset:4608
v_accvgpr_read_b32 v8,a0
v_mfma_f32_16x16x32_bf16 v[124:127],a[180:183],v[236:239],v[124:127]
v_exp_f32 v109,v109
v_mfma_f32_16x16x32_bf16 v[124:127],a[176:179],v[228:231],v[124:127]
ds_read_b128 a[148:151],v1 offset:4352
v_cvt_pk_bf16_f32 v106,v108,v109
v_mfma_f32_16x16x32_bf16 v[52:55],v[28:31],v[80:83],v[52:55]
global_load_lds_dwordx4 v5,s[10:11] offset:1024
v_accvgpr_read_b32 v9,a1
v_mfma_f32_16x16x32_bf16 v[52:55],v[28:31],v[88:91],v[52:55]
ds_read_b128 a[144:147],v1 offset:4096
v_accvgpr_read_b32 v10,a2
v_mfma_f32_16x16x32_bf16 a[32:35],a[192:195],v[80:83],a[32:35]
v_exp_f32 v110,v110
v_mfma_f32_16x16x32_bf16 a[32:35],a[196:199],v[88:91],a[32:35]
ds_read_b128 a[160:163],v1 offset:8192
v_accvgpr_read_b32 v11,a3
v_mfma_f32_16x16x32_bf16 a[36:39],a[200:203],v[80:83],a[36:39]
v_exp_f32 v111,v111
v_mfma_f32_16x16x32_bf16 a[36:39],a[204:207],v[88:91],a[36:39]
ds_read_b128 a[164:167],v1 offset:8448
v_cvt_pk_bf16_f32 v107,v110,v111
v_mfma_f32_16x16x32_bf16 a[40:43],a[208:211],v[80:83],a[40:43]
v_exp_f32 v112,v112
v_mfma_f32_16x16x32_bf16 a[40:43],a[212:215],v[88:91],a[40:43]
ds_read_b128 a[168:171],v1 offset:8704
v_accvgpr_read_b32 v12,a4
v_mfma_f32_16x16x32_bf16 a[44:47],a[216:219],v[80:83],a[44:47]
v_exp_f32 v113,v113
v_mfma_f32_16x16x32_bf16 a[44:47],a[220:223],v[88:91],a[44:47]
ds_read_b128 a[172:175],v1 offset:8960
v_cvt_pk_bf16_f32 v112,v112,v113
v_mfma_f32_16x16x32_bf16 a[48:51],a[224:227],v[80:83],a[48:51]
v_exp_f32 v114,v114
v_mfma_f32_16x16x32_bf16 a[48:51],a[228:231],v[88:91],a[48:51]
ds_read_b128 a[188:191],v1 offset:13056
v_accvgpr_read_b32 v13,a5
v_mfma_f32_16x16x32_bf16 a[52:55],a[232:235],v[80:83],a[52:55]
v_exp_f32 v115,v115
v_mfma_f32_16x16x32_bf16 a[52:55],a[236:239],v[88:91],a[52:55]
ds_read_b128 a[184:187],v1 offset:12800
v_cvt_pk_bf16_f32 v113,v114,v115
v_mfma_f32_16x16x32_bf16 a[56:59],a[240:243],v[80:83],a[56:59]
global_load_lds_dwordx4 v4,s[10:11] offset:2048
v_accvgpr_read_b32 v14,a6
v_mfma_f32_16x16x32_bf16 a[56:59],a[244:247],v[88:91],a[56:59]
ds_read_b128 a[180:183],v1 offset:12544
v_accvgpr_read_b32 v15,a7
v_mfma_f32_16x16x32_bf16 a[60:63],a[248:251],v[80:83],a[60:63]
v_exp_f32 v116,v116
v_mfma_f32_16x16x32_bf16 a[60:63],a[252:255],v[88:91],a[60:63]
s_waitcnt lgkmcnt(3)
ds_read_b128 a[176:179],v1 offset:12288
v_max_f32 v24,0x00800000,v48
v_mfma_f32_16x16x32_bf16 v[64:67],a[128:131],v[128:131],v[32:35]
v_exp_f32 v117,v117
v_mfma_f32_16x16x32_bf16 v[64:67],a[132:135],v[136:139],v[64:67]
ds_read_b128 v[36:39],v7 offset:1024
v_cvt_pk_bf16_f32 v114,v116,v117
v_mfma_f32_16x16x32_bf16 v[64:67],a[136:139],v[144:147],v[64:67]
v_exp_f32 v118,v118
v_mfma_f32_16x16x32_bf16 v[64:67],a[140:143],v[152:155],v[64:67]
v_exp_f32 v119,v119
v_mfma_f32_16x16x32_bf16 v[68:71],a[156:159],v[152:155],v[32:35]
ds_read_b128 v[40:43],v7 offset:2048
v_cvt_pk_bf16_f32 v115,v118,v119
v_mfma_f32_16x16x32_bf16 v[68:71],a[152:155],v[144:147],v[68:71]
v_exp_f32 v120,v120
v_mfma_f32_16x16x32_bf16 v[68:71],a[148:151],v[136:139],v[68:71]
v_exp_f32 v121,v121
v_mfma_f32_16x16x32_bf16 v[68:71],a[144:147],v[128:131],v[68:71]
v_exp_f32 v122,v122
v_mfma_f32_16x16x32_bf16 v[72:75],a[160:163],v[128:131],v[32:35]
v_exp_f32 v123,v123
v_mfma_f32_16x16x32_bf16 v[72:75],a[164:167],v[136:139],v[72:75]
v_exp_f32 v124,v124
v_mfma_f32_16x16x32_bf16 v[72:75],a[168:171],v[144:147],v[72:75]
s_waitcnt lgkmcnt(1)
v_exp_f32 v125,v125
v_mfma_f32_16x16x32_bf16 v[72:75],a[172:175],v[152:155],v[72:75]
global_load_lds_dwordx4 v5,s[10:11] offset:3072
v_cvt_pk_bf16_f32 v120,v120,v121
v_mfma_f32_16x16x32_bf16 v[76:79],a[188:191],v[152:155],v[32:35]
v_exp_f32 v126,v126
v_mfma_f32_16x16x32_bf16 v[76:79],a[184:187],v[144:147],v[76:79]
v_exp_f32 v127,v127
v_mfma_f32_16x16x32_bf16 v[76:79],a[180:183],v[136:139],v[76:79]
v_cvt_pk_bf16_f32 v121,v122,v123
v_cvt_pk_bf16_f32 v122,v124,v125
L_pd7_%=:
s_cbranch_execz L_trt0_%=
v_mfma_f32_16x16x32_bf16 v[76:79],a[176:179],v[128:131],v[76:79]
v_cvt_pk_bf16_f32 v123,v126,v127
v_accvgpr_read_b32 v16,a8
v_mfma_f32_16x16x32_bf16 v[56:59],v[28:31],v[96:99],v[56:59]
v_exp_f32 v64,v64
v_mfma_f32_16x16x32_bf16 v[56:59],v[28:31],v[104:107],v[56:59]
v_exp_f32 v65,v65
v_mfma_f32_16x16x32_bf16 a[64:67],a[192:195],v[96:99],a[64:67]
v_exp_f32 v66,v66
v_mfma_f32_16x16x32_bf16 a[64:67],a[196:199],v[104:107],a[64:67]
v_exp_f32 v67,v67
v_mfma_f32_16x16x32_bf16 a[68:71],a[200:203],v[96:99],a[68:71]
v_exp_f32 v68,v68
v_mfma_f32_16x16x32_bf16 a[68:71],a[204:207],v[104:107],a[68:71]
s_add_u32 s10,s20,s74
s_addc_u32 s11,s21,0
v_cvt_pk_bf16_f32 v64,v64,v65
v_mfma_f32_16x16x32_bf16 a[72:75],a[208:211],v[96:99],a[72:75]
v_exp_f32 v69,v69
v_mfma_f32_16x16x32_bf16 a[72:75],a[212:215],v[104:107],a[72:75]
s_add_u32 m0,s76,0
v_exp_f32 v70,v70
v_mfma_f32_16x16x32_bf16 a[76:79],a[216:219],v[96:99],a[76:79]
v_exp_f32 v71,v71
v_mfma_f32_16x16x32_bf16 a[76:79],a[220:223],v[104:107],a[76:79]
global_load_lds_dwordx4 v4,s[10:11] nt
v_cvt_pk_bf16_f32 v65,v66,v67
v_mfma_f32_16x16x32_bf16 a[80:83],a[224:227],v[96:99],a[80:83]
v_cvt_pk_bf16_f32 v66,v68,v69
v_cvt_pk_bf16_f32 v67,v70,v71
v_mfma_f32_16x16x32_bf16 a[80:83],a[228:231],v[104:107],a[80:83]
v_exp_f32 v72,v72
v_mfma_f32_16x16x32_bf16 a[84:87],a[232:235],v[96:99],a[84:87]
v_exp_f32 v73,v73
v_mfma_f32_16x16x32_bf16 a[84:87],a[236:239],v[104:107],a[84:87]
v_exp_f32 v74,v74
v_mfma_f32_16x16x32_bf16 a[88:91],a[240:243],v[96:99],a[88:91]
v_exp_f32 v75,v75
v_mfma_f32_16x16x32_bf16 a[88:91],a[244:247],v[104:107],a[88:91]
v_exp_f32 v76,v76
v_mfma_f32_16x16x32_bf16 a[92:95],a[248:251],v[96:99],a[92:95]
v_exp_f32 v77,v77
v_mfma_f32_16x16x32_bf16 a[92:95],a[252:255],v[104:107],a[92:95]
v_exp_f32 v78,v78
v_mfma_f32_16x16x32_bf16 v[80:83],a[128:131],v[160:163],v[36:39]
v_exp_f32 v79,v79
v_mfma_f32_16x16x32_bf16 v[80:83],a[132:135],v[168:171],v[80:83]
v_cvt_pk_bf16_f32 v72,v72,v73
v_cvt_pk_bf16_f32 v73,v74,v75
v_mfma_f32_16x16x32_bf16 v[80:83],a[136:139],v[176:179],v[80:83]
v_cvt_pk_bf16_f32 v74,v76,v77
v_cvt_pk_bf16_f32 v75,v78,v79
v_mfma_f32_16x16x32_bf16 v[80:83],a[140:143],v[184:187],v[80:83]
v_rcp_f32 v24,v24
v_mfma_f32_16x16x32_bf16 v[84:87],a[156:159],v[184:187],v[36:39]
v_accvgpr_read_b32 v17,a9
v_accvgpr_read_b32 v18,a10
v_mfma_f32_16x16x32_bf16 v[84:87],a[152:155],v[176:179],v[84:87]
v_accvgpr_read_b32 v19,a11
v_mul_f32 v8,v8,v24
v_mfma_f32_16x16x32_bf16 v[84:87],a[148:151],v[168:171],v[84:87]
global_load_lds_dwordx4 v4,s[10:11] offset:1024 nt
v_mul_f32 v9,v9,v24
v_mfma_f32_16x16x32_bf16 v[84:87],a[144:147],v[160:163],v[84:87]
v_mul_f32 v10,v10,v24
v_mul_f32 v11,v11,v24
v_mfma_f32_16x16x32_bf16 v[88:91],a[160:163],v[160:163],v[36:39]
v_mul_f32 v12,v12,v24
v_mul_f32 v13,v13,v24
v_mfma_f32_16x16x32_bf16 v[88:91],a[164:167],v[168:171],v[88:91]
v_mul_f32 v14,v14,v24
v_mul_f32 v15,v15,v24
v_mfma_f32_16x16x32_bf16 v[88:91],a[168:171],v[176:179],v[88:91]
v_cvt_pk_bf16_f32 v8,v8,v9
v_cvt_pk_bf16_f32 v9,v10,v11
v_mfma_f32_16x16x32_bf16 v[88:91],a[172:175],v[184:187],v[88:91]
v_cvt_pk_bf16_f32 v10,v12,v13
v_cvt_pk_bf16_f32 v11,v14,v15
v_mfma_f32_16x16x32_bf16 v[92:95],a[188:191],v[184:187],v[36:39]
ds_write_b128 v7,v[8:11] offset:6144
v_mfma_f32_16x16x32_bf16 v[92:95],a[184:187],v[176:179],v[92:95]
v_accvgpr_read_b32 v20,a12
v_accvgpr_read_b32 v21,a13
v_mfma_f32_16x16x32_bf16 v[92:95],a[180:183],v[168:171],v[92:95]
v_accvgpr_read_b32 v22,a14
v_accvgpr_read_b32 v23,a15
v_mfma_f32_16x16x32_bf16 v[92:95],a[176:179],v[160:163],v[92:95]
)A"
R"A(v_accvgpr_read_b32 v8,a16
v_accvgpr_read_b32 v9,a17
v_mfma_f32_16x16x32_bf16 v[60:63],v[28:31],v[112:115],v[60:63]
v_accvgpr_read_b32 v10,a18
v_accvgpr_read_b32 v11,a19
v_mfma_f32_16x16x32_bf16 v[60:63],v[28:31],v[120:123],v[60:63]
v_mul_f32 v16,v16,v24
v_mul_f32 v17,v17,v24
v_mfma_f32_16x16x32_bf16 a[96:99],a[192:195],v[112:115],a[96:99]
ds_read_b64_tr_b16 a[192:193],v2
ds_read_b64_tr_b16 a[194:195],v2 offset:4096
v_mfma_f32_16x16x32_bf16 a[96:99],a[196:199],v[120:123],a[96:99]
ds_read_b64_tr_b16 a[196:197],v2 offset:8192
v_mul_f32 v18,v18,v24
v_mfma_f32_16x16x32_bf16 a[100:103],a[200:203],v[112:115],a[100:103]
global_load_lds_dwordx4 v5,s[10:11] offset:2048 nt
ds_read_b64_tr_b16 a[198:199],v2 offset:12288
v_mfma_f32_16x16x32_bf16 a[100:103],a[204:207],v[120:123],a[100:103]
ds_read_b64_tr_b16 a[200:201],v3
v_mul_f32 v19,v19,v24
v_mfma_f32_16x16x32_bf16 a[104:107],a[208:211],v[112:115],a[104:107]
ds_read_b64_tr_b16 a[202:203],v3 offset:4096
ds_read_b64_tr_b16 a[204:205],v3 offset:8192
v_mfma_f32_16x16x32_bf16 a[104:107],a[212:215],v[120:123],a[104:107]
ds_read_b64_tr_b16 a[206:207],v3 offset:12288
v_mul_f32 v20,v20,v24
v_mfma_f32_16x16x32_bf16 a[108:111],a[216:219],v[112:115],a[108:111]
ds_read_b64_tr_b16 a[208:209],v2 offset:256
v_mul_f32 v21,v21,v24
v_mfma_f32_16x16x32_bf16 a[108:111],a[220:223],v[120:123],a[108:111]
ds_read_b64_tr_b16 a[210:211],v2 offset:4352
v_mul_f32 v22,v22,v24
v_mfma_f32_16x16x32_bf16 a[112:115],a[224:227],v[112:115],a[112:115]
ds_read_b64_tr_b16 a[212:213],v2 offset:8448
ds_read_b64_tr_b16 a[214:215],v2 offset:12544
v_mfma_f32_16x16x32_bf16 a[112:115],a[228:231],v[120:123],a[112:115]
ds_read_b64_tr_b16 a[216:217],v3 offset:256
v_mul_f32 v23,v23,v24
v_mfma_f32_16x16x32_bf16 a[116:119],a[232:235],v[112:115],a[116:119]
ds_read_b64_tr_b16 a[218:219],v3 offset:4352
v_cvt_pk_bf16_f32 v16,v16,v17
v_mfma_f32_16x16x32_bf16 a[116:119],a[236:239],v[120:123],a[116:119]
ds_read_b64_tr_b16 a[220:221],v3 offset:8448
v_cvt_pk_bf16_f32 v17,v18,v19
v_mfma_f32_16x16x32_bf16 a[120:123],a[240:243],v[112:115],a[120:123]
ds_read_b64_tr_b16 a[222:223],v3 offset:12544
ds_read_b64_tr_b16 a[224:225],v2 offset:512
v_mfma_f32_16x16x32_bf16 a[120:123],a[244:247],v[120:123],a[120:123]
ds_read_b64_tr_b16 a[226:227],v2 offset:4608
v_cvt_pk_bf16_f32 v18,v20,v21
v_mfma_f32_16x16x32_bf16 a[124:127],a[248:251],v[112:115],a[124:127]
ds_read_b64_tr_b16 a[228:229],v2 offset:8704
v_cvt_pk_bf16_f32 v19,v22,v23
v_mfma_f32_16x16x32_bf16 a[124:127],a[252:255],v[120:123],a[124:127]
s_waitcnt lgkmcnt(9)
ds_read_b64_tr_b16 a[230:231],v2 offset:12800
v_accvgpr_read_b32 v12,a20
v_mfma_f32_16x16x32_bf16 v[96:99],a[128:131],v[192:195],v[40:43]
ds_read_b64_tr_b16 a[232:233],v3 offset:512
ds_read_b64_tr_b16 a[234:235],v3 offset:4608
v_mfma_f32_16x16x32_bf16 v[96:99],a[132:135],v[200:203],v[96:99]
global_load_lds_dwordx4 v5,s[10:11] offset:3072
ds_read_b64_tr_b16 a[236:237],v3 offset:8704
v_mfma_f32_16x16x32_bf16 v[96:99],a[136:139],v[208:211],v[96:99]
ds_read_b64_tr_b16 a[238:239],v3 offset:12800
v_accvgpr_read_b32 v13,a21
v_mfma_f32_16x16x32_bf16 v[96:99],a[140:143],v[216:219],v[96:99]
ds_read_b64_tr_b16 a[240:241],v2 offset:768
v_accvgpr_read_b32 v14,a22
v_mfma_f32_16x16x32_bf16 v[100:103],a[156:159],v[216:219],v[40:43]
ds_read_b64_tr_b16 a[242:243],v2 offset:4864
ds_read_b64_tr_b16 a[244:245],v2 offset:8960
v_mfma_f32_16x16x32_bf16 v[100:103],a[152:155],v[208:211],v[100:103]
ds_read_b64_tr_b16 a[246:247],v2 offset:13056
v_accvgpr_read_b32 v15,a23
v_mfma_f32_16x16x32_bf16 v[100:103],a[148:151],v[200:203],v[100:103]
s_cmp_le_u32 s5,3
s_cselect_b32 s5,s4,s5
ds_read_b64_tr_b16 a[248:249],v3 offset:768
v_mfma_f32_16x16x32_bf16 v[100:103],a[144:147],v[192:195],v[100:103]
ds_read_b64_tr_b16 a[250:251],v3 offset:4864
v_accvgpr_read_b32 v20,a28
v_mfma_f32_16x16x32_bf16 v[104:107],a[160:163],v[192:195],v[40:43]
ds_read_b64_tr_b16 a[252:253],v3 offset:8960
ds_read_b64_tr_b16 a[254:255],v3 offset:13056
v_mfma_f32_16x16x32_bf16 v[104:107],a[164:167],v[200:203],v[104:107]
v_accvgpr_read_b32 v21,a29
v_accvgpr_read_b32 v22,a30
v_mfma_f32_16x16x32_bf16 v[104:107],a[168:171],v[208:211],v[104:107]
ds_write_b128 v7,v[16:19] offset:7168
v_mfma_f32_16x16x32_bf16 v[104:107],a[172:175],v[216:219],v[104:107]
s_add_u32 s10,s24,s74
s_addc_u32 s11,s25,0
v_accvgpr_read_b32 v23,a31
v_mfma_f32_16x16x32_bf16 v[108:111],a[188:191],v[216:219],v[40:43]
v_accvgpr_read_b32 v16,a24
v_accvgpr_read_b32 v17,a25
v_mfma_f32_16x16x32_bf16 v[108:111],a[184:187],v[208:211],v[108:111]
s_add_u32 m0,s76,16384
v_accvgpr_read_b32 v18,a26
v_accvgpr_read_b32 v19,a27
v_mfma_f32_16x16x32_bf16 v[108:111],a[180:183],v[200:203],v[108:111]
v_cmp_nlt_f32 s[54:55],v48,s73
v_cmp_nlt_f32 s[56:57],v52,s73
v_mfma_f32_16x16x32_bf16 v[108:111],a[176:179],v[192:195],v[108:111]
s_or_b64 s[54:55],s[54:55],s[56:57]
global_load_lds_dwordx4 v4,s[10:11]
v_mfma_f32_16x16x32_bf16 a[0:3],a[192:195],v[64:67],0
v_cmp_nlt_f32 s[56:57],v56,s73
v_mul_f32 v8,v8,v24
v_mfma_f32_16x16x32_bf16 a[0:3],a[196:199],v[72:75],a[0:3]
s_or_b64 s[54:55],s[54:55],s[56:57]
v_mul_f32 v9,v9,v24
v_mul_f32 v10,v10,v24
v_mfma_f32_16x16x32_bf16 a[4:7],a[200:203],v[64:67],0
v_mul_f32 v11,v11,v24
v_mul_f32 v12,v12,v24
v_mfma_f32_16x16x32_bf16 a[4:7],a[204:207],v[72:75],a[4:7]
v_mul_f32 v13,v13,v24
v_mul_f32 v14,v14,v24
v_mfma_f32_16x16x32_bf16 a[8:11],a[208:211],v[64:67],0
s_waitcnt lgkmcnt(0)
v_mul_f32 v15,v15,v24
v_cvt_pk_bf16_f32 v8,v8,v9
v_mfma_f32_16x16x32_bf16 a[8:11],a[212:215],v[72:75],a[8:11]
v_cvt_pk_bf16_f32 v9,v10,v11
v_cvt_pk_bf16_f32 v10,v12,v13
v_mfma_f32_16x16x32_bf16 a[12:15],a[216:219],v[64:67],0
v_cvt_pk_bf16_f32 v11,v14,v15
v_mul_f32 v16,v16,v24
v_mfma_f32_16x16x32_bf16 a[12:15],a[220:223],v[72:75],a[12:15]
v_mul_f32 v17,v17,v24
v_mul_f32 v18,v18,v24
v_mfma_f32_16x16x32_bf16 a[16:19],a[224:227],v[64:67],0
v_mul_f32 v19,v19,v24
v_mul_f32 v20,v20,v24
v_mfma_f32_16x16x32_bf16 a[16:19],a[228:231],v[72:75],a[16:19]
v_mul_f32 v21,v21,v24
v_mul_f32 v22,v22,v24
v_mfma_f32_16x16x32_bf16 a[20:23],a[232:235],v[64:67],0
s_getreg_b32 s92,hwreg(HW_REG_IB_STS,0,4)
s_min_u32 s4,s92,s6
s_mov_b32 s6,s92
v_mfma_f32_16x16x32_bf16 a[20:23],a[236:239],v[72:75],a[20:23]
s_cmp_gt_u32 s4,s9
s_cselect_b32 s4,15,s5
v_mul_f32 v23,v23,v24
v_mfma_f32_16x16x32_bf16 a[24:27],a[240:243],v[64:67],0
v_cvt_pk_bf16_f32 v16,v16,v17
v_cvt_pk_bf16_f32 v17,v18,v19
v_mfma_f32_16x16x32_bf16 a[24:27],a[244:247],v[72:75],a[24:27]
v_cvt_pk_bf16_f32 v18,v20,v21
v_cvt_pk_bf16_f32 v19,v22,v23
v_mfma_f32_16x16x32_bf16 a[28:31],a[248:251],v[64:67],0
global_load_lds_dwordx4 v5,s[10:11] offset:1024
v_cmp_nlt_f32 s[56:57],v60,s73
v_mfma_f32_16x16x32_bf16 a[28:31],a[252:255],v[72:75],a[28:31]
s_or_b64 s[54:55],s[54:55],s[56:57]
v_exp_f32 v80,v80
v_mfma_f32_16x16x32_bf16 v[48:51],v[28:31],v[64:67],0
s_getpc_b64 s[92:93]
s_cmp_lg_u32 s4,0
s_cbranch_scc1 L_pace_%=
v_exp_f32 v81,v81
v_exp_f32 v82,v82
v_exp_f32 v83,v83
v_exp_f32 v84,v84
v_exp_f32 v85,v85
v_exp_f32 v86,v86
v_exp_f32 v87,v87
v_exp_f32 v88,v88
v_exp_f32 v89,v89
v_mfma_f32_16x16x32_bf16 v[48:51],v[28:31],v[72:75],v[48:51]
ds_write_b128 v7,v[8:11]
v_add_u32 v1,s78,v1
v_max_f32 v25,0x00800000,v52
v_rcp_f32 v25,v25
v_max_f32 v26,0x00800000,v56
v_rcp_f32 v26,v26
v_cvt_pk_bf16_f32 v80,v80,v81
v_cvt_pk_bf16_f32 v81,v82,v83
v_cvt_pk_bf16_f32 v82,v84,v85
v_cvt_pk_bf16_f32 v83,v86,v87
v_cvt_pk_bf16_f32 v88,v88,v89
v_max_f32 v27,0x00800000,v60
v_rcp_f32 v27,v27
ds_write_b128 v7,v[16:19] offset:1024
ds_read_b128 v[44:47],v7 offset:3072
s_cmp_lg_u64 s[54:55],0
s_cbranch_scc1 L_slow0_%=
L_ok0_%=:
s_waitcnt vmcnt(14)
s_barrier
s_waitcnt lgkmcnt(0)
v_mfma_f32_16x16x32_bf16 v[112:115],a[128:131],v[224:227],v[44:47]
s_add_u32 s10,s24,s74
s_addc_u32 s11,s25,0
s_add_u32 m0,s76,16384
v_mfma_f32_16x16x32_bf16 v[112:115],a[132:135],v[232:235],v[112:115]
s_add_u32 s75,s75,16384
v_exp_f32 v90,v90
v_mfma_f32_16x16x32_bf16 v[112:115],a[136:139],v[240:243],v[112:115]
global_load_lds_dwordx4 v4,s[10:11] offset:2048
ds_read_b128 a[128:131],v1
v_mfma_f32_16x16x32_bf16 v[112:115],a[140:143],v[248:251],v[112:115]
s_add_u32 s74,s74,16384
v_exp_f32 v91,v91
v_mfma_f32_16x16x32_bf16 v[116:119],a[156:159],v[248:251],v[44:47]
s_and_b32 s74,s74,0x3ffff
v_exp_f32 v92,v92
v_mfma_f32_16x16x32_bf16 v[116:119],a[152:155],v[240:243],v[116:119]
s_and_b32 s69,s75,0xfffc0000
v_exp_f32 v93,v93
v_mfma_f32_16x16x32_bf16 v[116:119],a[148:151],v[232:235],v[116:119]
s_or_b32 s74,s74,s69
v_exp_f32 v94,v94
v_mfma_f32_16x16x32_bf16 v[116:119],a[144:147],v[224:227],v[116:119]
s_sub_u32 s76,s76,32768
v_exp_f32 v95,v95
v_mfma_f32_16x16x32_bf16 v[120:123],a[160:163],v[224:227],v[44:47]
s_and_b32 s76,s76,0x1ffff
v_cvt_pk_bf16_f32 v89,v90,v91
v_cvt_pk_bf16_f32 v90,v92,v93
v_mfma_f32_16x16x32_bf16 v[120:123],a[164:167],v[232:235],v[120:123]
s_add_u32 s75,s75,16384
ds_read_b128 a[132:135],v1 offset:256
v_cvt_pk_bf16_f32 v91,v94,v95
v_mfma_f32_16x16x32_bf16 v[120:123],a[168:171],v[240:243],v[120:123]
s_and_b32 s69,s75,0xfffc0000
v_exp_f32 v96,v96
v_mfma_f32_16x16x32_bf16 v[120:123],a[172:175],v[248:251],v[120:123]
s_add_u32 s75,s75,16384
ds_read_b128 a[136:139],v1 offset:512
v_accvgpr_read_b32 v16,a32
v_mfma_f32_16x16x32_bf16 v[124:127],a[188:191],v[248:251],v[44:47]
v_exp_f32 v97,v97
v_mfma_f32_16x16x32_bf16 v[124:127],a[184:187],v[240:243],v[124:127]
ds_read_b128 a[140:143],v1 offset:768
v_cvt_pk_bf16_f32 v96,v96,v97
v_mfma_f32_16x16x32_bf16 v[124:127],a[180:183],v[232:235],v[124:127]
v_exp_f32 v98,v98
v_mfma_f32_16x16x32_bf16 v[124:127],a[176:179],v[224:227],v[124:127]
ds_read_b128 a[156:159],v1 offset:4864
v_accvgpr_read_b32 v17,a33
v_mfma_f32_16x16x32_bf16 v[132:135],a[192:195],v[80:83],0
global_load_lds_dwordx4 v5,s[10:11] offset:3072
v_accvgpr_read_b32 v18,a34
v_mfma_f32_16x16x32_bf16 v[132:135],a[196:199],v[88:91],v[132:135]
ds_read_b128 a[152:155],v1 offset:4608
v_accvgpr_read_b32 v19,a35
v_mfma_f32_16x16x32_bf16 v[140:143],a[200:203],v[80:83],0
v_exp_f32 v99,v99
v_mfma_f32_16x16x32_bf16 v[140:143],a[204:207],v[88:91],v[140:143]
ds_read_b128 a[148:151],v1 offset:4352
v_cvt_pk_bf16_f32 v97,v98,v99
v_mfma_f32_16x16x32_bf16 v[148:151],a[208:211],v[80:83],0
v_exp_f32 v100,v100
v_mfma_f32_16x16x32_bf16 v[148:151],a[212:215],v[88:91],v[148:151]
ds_read_b128 a[144:147],v1 offset:4096
v_accvgpr_read_b32 v20,a36
v_mfma_f32_16x16x32_bf16 v[156:159],a[216:219],v[80:83],0
v_exp_f32 v101,v101
v_mfma_f32_16x16x32_bf16 v[156:159],a[220:223],v[88:91],v[156:159]
ds_read_b128 a[160:163],v1 offset:8192
v_cvt_pk_bf16_f32 v98,v100,v101
v_mfma_f32_16x16x32_bf16 v[164:167],a[224:227],v[80:83],0
v_exp_f32 v102,v102
v_mfma_f32_16x16x32_bf16 v[164:167],a[228:231],v[88:91],v[164:167]
ds_read_b128 a[164:167],v1 offset:8448
v_accvgpr_read_b32 v21,a37
v_mfma_f32_16x16x32_bf16 v[172:175],a[232:235],v[80:83],0
s_add_u32 s10,s20,s74
s_addc_u32 s11,s21,0
v_accvgpr_read_b32 v22,a38
v_mfma_f32_16x16x32_bf16 v[172:175],a[236:239],v[88:91],v[172:175]
ds_read_b128 a[168:171],v1 offset:8704
v_accvgpr_read_b32 v23,a39
v_mfma_f32_16x16x32_bf16 v[180:183],a[240:243],v[80:83],0
s_add_u32 m0,s76,0
v_exp_f32 v103,v103
v_mfma_f32_16x16x32_bf16 v[180:183],a[244:247],v[88:91],v[180:183]
ds_read_b128 a[172:175],v1 offset:8960
v_cvt_pk_bf16_f32 v99,v102,v103
v_mfma_f32_16x16x32_bf16 v[188:191],a[248:251],v[80:83],0
global_load_lds_dwordx4 v4,s[10:11] nt
v_mul_f32 v16,v16,v25
v_mfma_f32_16x16x32_bf16 v[188:191],a[252:255],v[88:91],v[188:191]
ds_read_b128 a[188:191],v1 offset:13056
v_mul_f32 v17,v17,v25
v_mfma_f32_16x16x32_bf16 v[52:55],v[28:31],v[80:83],0
v_exp_f32 v104,v104
v_mfma_f32_16x16x32_bf16 v[52:55],v[28:31],v[88:91],v[52:55]
s_waitcnt lgkmcnt(3)
ds_read_b128 a[184:187],v1 offset:12800
v_mul_f32 v18,v18,v25
v_mfma_f32_16x16x32_bf16 v[64:67],a[128:131],v[128:131],v[32:35]
v_exp_f32 v105,v105
v_mfma_f32_16x16x32_bf16 v[64:67],a[132:135],v[136:139],v[64:67]
ds_read_b128 a[180:183],v1 offset:12544
v_cvt_pk_bf16_f32 v104,v104,v105
v_mfma_f32_16x16x32_bf16 v[64:67],a[136:139],v[144:147],v[64:67]
v_exp_f32 v106,v106
v_mfma_f32_16x16x32_bf16 v[64:67],a[140:143],v[152:155],v[64:67]
)A"
R"A(ds_read_b128 a[176:179],v1 offset:12288
v_mul_f32 v19,v19,v25
v_mfma_f32_16x16x32_bf16 v[68:71],a[156:159],v[152:155],v[32:35]
v_exp_f32 v107,v107
v_mfma_f32_16x16x32_bf16 v[68:71],a[152:155],v[144:147],v[68:71]
v_exp_f32 v108,v108
v_mfma_f32_16x16x32_bf16 v[68:71],a[148:151],v[136:139],v[68:71]
v_exp_f32 v109,v109
v_mfma_f32_16x16x32_bf16 v[68:71],a[144:147],v[128:131],v[68:71]
v_exp_f32 v110,v110
v_mfma_f32_16x16x32_bf16 v[72:75],a[160:163],v[128:131],v[32:35]
v_exp_f32 v111,v111
v_mfma_f32_16x16x32_bf16 v[72:75],a[164:167],v[136:139],v[72:75]
s_waitcnt lgkmcnt(1)
v_cvt_pk_bf16_f32 v105,v106,v107
v_cvt_pk_bf16_f32 v106,v108,v109
v_mfma_f32_16x16x32_bf16 v[72:75],a[168:171],v[144:147],v[72:75]
global_load_lds_dwordx4 v4,s[10:11] offset:1024 nt
v_cvt_pk_bf16_f32 v107,v110,v111
v_mfma_f32_16x16x32_bf16 v[72:75],a[172:175],v[152:155],v[72:75]
v_exp_f32 v112,v112
v_mfma_f32_16x16x32_bf16 v[76:79],a[188:191],v[152:155],v[32:35]
v_exp_f32 v113,v113
v_mfma_f32_16x16x32_bf16 v[76:79],a[184:187],v[144:147],v[76:79]
v_exp_f32 v114,v114
v_mfma_f32_16x16x32_bf16 v[76:79],a[180:183],v[136:139],v[76:79]
s_waitcnt lgkmcnt(0)
v_exp_f32 v115,v115
v_mfma_f32_16x16x32_bf16 v[76:79],a[176:179],v[128:131],v[76:79]
v_exp_f32 v116,v116
v_mfma_f32_16x16x32_bf16 v[196:199],a[192:195],v[96:99],0
v_exp_f32 v117,v117
v_mfma_f32_16x16x32_bf16 v[196:199],a[196:199],v[104:107],v[196:199]
v_exp_f32 v118,v118
v_mfma_f32_16x16x32_bf16 v[204:207],a[200:203],v[96:99],0
v_exp_f32 v119,v119
v_mfma_f32_16x16x32_bf16 v[204:207],a[204:207],v[104:107],v[204:207]
v_cvt_pk_bf16_f32 v112,v112,v113
v_cvt_pk_bf16_f32 v113,v114,v115
v_mfma_f32_16x16x32_bf16 v[212:215],a[208:211],v[96:99],0
v_cvt_pk_bf16_f32 v114,v116,v117
v_cvt_pk_bf16_f32 v115,v118,v119
v_mfma_f32_16x16x32_bf16 v[212:215],a[212:215],v[104:107],v[212:215]
v_exp_f32 v120,v120
v_mfma_f32_16x16x32_bf16 v[220:223],a[216:219],v[96:99],0
v_exp_f32 v121,v121
v_mfma_f32_16x16x32_bf16 v[220:223],a[220:223],v[104:107],v[220:223]
v_exp_f32 v122,v122
v_mfma_f32_16x16x32_bf16 v[228:231],a[224:227],v[96:99],0
global_load_lds_dwordx4 v5,s[10:11] offset:2048 nt
v_cvt_pk_bf16_f32 v120,v120,v121
v_mfma_f32_16x16x32_bf16 v[228:231],a[228:231],v[104:107],v[228:231]
v_exp_f32 v123,v123
v_mfma_f32_16x16x32_bf16 v[236:239],a[232:235],v[96:99],0
v_exp_f32 v124,v124
v_mfma_f32_16x16x32_bf16 v[236:239],a[236:239],v[104:107],v[236:239]
v_exp_f32 v125,v125
v_mfma_f32_16x16x32_bf16 v[244:247],a[240:243],v[96:99],0
v_exp_f32 v126,v126
v_mfma_f32_16x16x32_bf16 v[244:247],a[244:247],v[104:107],v[244:247]
v_exp_f32 v127,v127
v_mfma_f32_16x16x32_bf16 v[252:255],a[248:251],v[96:99],0
v_cvt_pk_bf16_f32 v121,v122,v123
v_cvt_pk_bf16_f32 v122,v124,v125
v_mfma_f32_16x16x32_bf16 v[252:255],a[252:255],v[104:107],v[252:255]
v_cvt_pk_bf16_f32 v123,v126,v127
v_mul_f32 v20,v20,v25
v_mfma_f32_16x16x32_bf16 v[56:59],v[28:31],v[96:99],0
v_mul_f32 v21,v21,v25
v_mul_f32 v22,v22,v25
v_mfma_f32_16x16x32_bf16 v[56:59],v[28:31],v[104:107],v[56:59]
v_mul_f32 v23,v23,v25
v_cvt_pk_bf16_f32 v16,v16,v17
v_mfma_f32_16x16x32_bf16 v[80:83],a[128:131],v[160:163],v[36:39]
v_cvt_pk_bf16_f32 v17,v18,v19
v_cvt_pk_bf16_f32 v18,v20,v21
v_mfma_f32_16x16x32_bf16 v[80:83],a[132:135],v[168:171],v[80:83]
v_cvt_pk_bf16_f32 v19,v22,v23
v_accvgpr_read_b32 v20,a44
v_mfma_f32_16x16x32_bf16 v[80:83],a[136:139],v[176:179],v[80:83]
ds_write_b128 v7,v[16:19] offset:2048
v_mfma_f32_16x16x32_bf16 v[80:83],a[140:143],v[184:187],v[80:83]
v_accvgpr_read_b32 v21,a45
v_accvgpr_read_b32 v22,a46
v_mfma_f32_16x16x32_bf16 v[84:87],a[156:159],v[184:187],v[36:39]
v_accvgpr_read_b32 v16,a40
v_accvgpr_read_b32 v17,a41
v_mfma_f32_16x16x32_bf16 v[84:87],a[152:155],v[176:179],v[84:87]
global_load_lds_dwordx4 v5,s[10:11] offset:3072
v_accvgpr_read_b32 v18,a42
v_mfma_f32_16x16x32_bf16 v[84:87],a[148:151],v[168:171],v[84:87]
v_accvgpr_read_b32 v19,a43
v_accvgpr_read_b32 v23,a47
v_mfma_f32_16x16x32_bf16 v[84:87],a[144:147],v[160:163],v[84:87]
v_accvgpr_read_b32 v8,a48
v_accvgpr_read_b32 v9,a49
v_mfma_f32_16x16x32_bf16 v[88:91],a[160:163],v[160:163],v[36:39]
v_accvgpr_read_b32 v10,a50
v_accvgpr_read_b32 v11,a51
v_mfma_f32_16x16x32_bf16 v[88:91],a[164:167],v[168:171],v[88:91]
v_mul_f32 v16,v16,v25
v_mul_f32 v17,v17,v25
v_mfma_f32_16x16x32_bf16 v[88:91],a[168:171],v[176:179],v[88:91]
v_mul_f32 v18,v18,v25
v_mul_f32 v19,v19,v25
v_mfma_f32_16x16x32_bf16 v[88:91],a[172:175],v[184:187],v[88:91]
v_mul_f32 v20,v20,v25
v_mul_f32 v21,v21,v25
v_mfma_f32_16x16x32_bf16 v[92:95],a[188:191],v[184:187],v[36:39]
v_mul_f32 v22,v22,v25
v_mul_f32 v23,v23,v25
v_mfma_f32_16x16x32_bf16 v[92:95],a[184:187],v[176:179],v[92:95]
v_cvt_pk_bf16_f32 v16,v16,v17
v_cvt_pk_bf16_f32 v17,v18,v19
v_mfma_f32_16x16x32_bf16 v[92:95],a[180:183],v[168:171],v[92:95]
v_cvt_pk_bf16_f32 v18,v20,v21
v_cvt_pk_bf16_f32 v19,v22,v23
v_mfma_f32_16x16x32_bf16 v[92:95],a[176:179],v[160:163],v[92:95]
s_add_u32 s10,s24,s74
s_addc_u32 s11,s25,0
v_accvgpr_read_b32 v12,a52
v_mfma_f32_16x16x32_bf16 a[32:35],a[192:195],v[112:115],0
ds_write_b128 v7,v[16:19] offset:3072
v_mfma_f32_16x16x32_bf16 a[32:35],a[196:199],v[120:123],a[32:35]
s_add_u32 m0,s76,16384
v_accvgpr_read_b32 v13,a53
v_accvgpr_read_b32 v14,a54
v_mfma_f32_16x16x32_bf16 a[36:39],a[200:203],v[112:115],0
v_accvgpr_read_b32 v15,a55
v_accvgpr_read_b32 v16,a56
v_mfma_f32_16x16x32_bf16 a[36:39],a[204:207],v[120:123],a[36:39]
global_load_lds_dwordx4 v4,s[10:11]
v_accvgpr_read_b32 v17,a57
v_mfma_f32_16x16x32_bf16 a[40:43],a[208:211],v[112:115],0
v_accvgpr_read_b32 v18,a58
v_accvgpr_read_b32 v19,a59
v_mfma_f32_16x16x32_bf16 a[40:43],a[212:215],v[120:123],a[40:43]
v_accvgpr_read_b32 v20,a60
v_accvgpr_read_b32 v21,a61
v_mfma_f32_16x16x32_bf16 a[44:47],a[216:219],v[112:115],0
v_accvgpr_read_b32 v22,a62
v_accvgpr_read_b32 v23,a63
v_mfma_f32_16x16x32_bf16 a[44:47],a[220:223],v[120:123],a[44:47]
v_add_u32 v2,s78,v2
v_add_u32 v3,s78,v3
v_mfma_f32_16x16x32_bf16 a[48:51],a[224:227],v[112:115],0
s_sub_u32 s78,s77,32768
ds_read_b64_tr_b16 a[192:193],v2
ds_read_b64_tr_b16 a[194:195],v2 offset:4096
v_mfma_f32_16x16x32_bf16 a[48:51],a[228:231],v[120:123],a[48:51]
s_and_b32 s78,s78,0x1ffff
ds_read_b64_tr_b16 a[196:197],v2 offset:8192
v_mul_f32 v8,v8,v25
v_mfma_f32_16x16x32_bf16 a[52:55],a[232:235],v[112:115],0
s_sub_u32 s78,s78,s77
ds_read_b64_tr_b16 a[198:199],v2 offset:12288
v_mul_f32 v9,v9,v25
v_mfma_f32_16x16x32_bf16 a[52:55],a[236:239],v[120:123],a[52:55]
s_add_u32 s77,s77,s78
ds_read_b64_tr_b16 a[200:201],v3
v_mul_f32 v10,v10,v25
v_mfma_f32_16x16x32_bf16 a[56:59],a[240:243],v[112:115],0
ds_read_b64_tr_b16 a[202:203],v3 offset:4096
ds_read_b64_tr_b16 a[204:205],v3 offset:8192
v_mfma_f32_16x16x32_bf16 a[56:59],a[244:247],v[120:123],a[56:59]
ds_read_b64_tr_b16 a[206:207],v3 offset:12288
v_mul_f32 v11,v11,v25
v_mfma_f32_16x16x32_bf16 a[60:63],a[248:251],v[112:115],0
ds_read_b64_tr_b16 a[208:209],v2 offset:256
v_mul_f32 v12,v12,v25
v_mfma_f32_16x16x32_bf16 a[60:63],a[252:255],v[120:123],a[60:63]
ds_read_b64_tr_b16 a[210:211],v2 offset:4352
v_mul_f32 v13,v13,v25
v_mfma_f32_16x16x32_bf16 v[60:63],v[28:31],v[112:115],0
ds_read_b64_tr_b16 a[212:213],v2 offset:8448
ds_read_b64_tr_b16 a[214:215],v2 offset:12544
v_mfma_f32_16x16x32_bf16 v[60:63],v[28:31],v[120:123],v[60:63]
global_load_lds_dwordx4 v5,s[10:11] offset:1024
ds_read_b64_tr_b16 a[216:217],v3 offset:256
v_mfma_f32_16x16x32_bf16 v[96:99],a[128:131],v[192:195],v[40:43]
v_exp_f32 v64,v64
v_mfma_f32_16x16x32_bf16 v[96:99],a[132:135],v[200:203],v[96:99]
v_exp_f32 v65,v65
v_mfma_f32_16x16x32_bf16 v[96:99],a[136:139],v[208:211],v[96:99]
v_exp_f32 v66,v66
v_mfma_f32_16x16x32_bf16 v[96:99],a[140:143],v[216:219],v[96:99]
v_exp_f32 v67,v67
v_mfma_f32_16x16x32_bf16 v[100:103],a[156:159],v[216:219],v[40:43]
v_exp_f32 v68,v68
v_mfma_f32_16x16x32_bf16 v[100:103],a[152:155],v[208:211],v[100:103]
v_exp_f32 v69,v69
v_mfma_f32_16x16x32_bf16 v[100:103],a[148:151],v[200:203],v[100:103]
v_exp_f32 v70,v70
v_mfma_f32_16x16x32_bf16 v[100:103],a[144:147],v[192:195],v[100:103]
v_exp_f32 v71,v71
v_mfma_f32_16x16x32_bf16 v[104:107],a[160:163],v[192:195],v[40:43]
ds_read_b64_tr_b16 a[218:219],v3 offset:4352
v_cvt_pk_bf16_f32 v64,v64,v65
v_mfma_f32_16x16x32_bf16 v[104:107],a[164:167],v[200:203],v[104:107]
v_cvt_pk_bf16_f32 v65,v66,v67
v_cvt_pk_bf16_f32 v66,v68,v69
v_mfma_f32_16x16x32_bf16 v[104:107],a[168:171],v[208:211],v[104:107]
ds_read_b64_tr_b16 a[220:221],v3 offset:8448
v_cvt_pk_bf16_f32 v67,v70,v71
v_mfma_f32_16x16x32_bf16 v[104:107],a[172:175],v[216:219],v[104:107]
v_exp_f32 v72,v72
v_mfma_f32_16x16x32_bf16 v[108:111],a[188:191],v[216:219],v[40:43]
v_exp_f32 v73,v73
v_mfma_f32_16x16x32_bf16 v[108:111],a[184:187],v[208:211],v[108:111]
L_pd8_%=:
s_cbranch_execz L_trt0_%=
global_load_lds_dwordx4 v4,s[10:11] offset:2048
v_exp_f32 v74,v74
v_exp_f32 v75,v75
v_exp_f32 v76,v76
v_exp_f32 v77,v77
v_exp_f32 v78,v78
v_exp_f32 v79,v79
v_mfma_f32_16x16x32_bf16 v[108:111],a[180:183],v[200:203],v[108:111]
ds_read_b64_tr_b16 a[222:223],v3 offset:12544
v_cvt_pk_bf16_f32 v72,v72,v73
v_cvt_pk_bf16_f32 v73,v74,v75
v_cvt_pk_bf16_f32 v74,v76,v77
v_cvt_pk_bf16_f32 v75,v78,v79
v_mfma_f32_16x16x32_bf16 v[108:111],a[176:179],v[192:195],v[108:111]
s_waitcnt lgkmcnt(3)
ds_read_b64_tr_b16 a[224:225],v2 offset:512
ds_read_b64_tr_b16 a[226:227],v2 offset:4608
v_mfma_f32_16x16x32_bf16 a[0:3],a[192:195],v[64:67],a[0:3]
ds_read_b64_tr_b16 a[228:229],v2 offset:8704
v_mul_f32 v14,v14,v25
v_mfma_f32_16x16x32_bf16 a[0:3],a[196:199],v[72:75],a[0:3]
ds_read_b64_tr_b16 a[230:231],v2 offset:12800
v_mul_f32 v15,v15,v25
v_mfma_f32_16x16x32_bf16 a[4:7],a[200:203],v[64:67],a[4:7]
ds_read_b64_tr_b16 a[232:233],v3 offset:512
v_cvt_pk_bf16_f32 v8,v8,v9
v_mfma_f32_16x16x32_bf16 a[4:7],a[204:207],v[72:75],a[4:7]
ds_read_b64_tr_b16 a[234:235],v3 offset:4608
ds_read_b64_tr_b16 a[236:237],v3 offset:8704
v_mfma_f32_16x16x32_bf16 a[8:11],a[208:211],v[64:67],a[8:11]
ds_read_b64_tr_b16 a[238:239],v3 offset:12800
v_cvt_pk_bf16_f32 v9,v10,v11
v_mfma_f32_16x16x32_bf16 a[8:11],a[212:215],v[72:75],a[8:11]
s_waitcnt lgkmcnt(9)
ds_read_b64_tr_b16 a[240:241],v2 offset:768
v_cvt_pk_bf16_f32 v10,v12,v13
v_mfma_f32_16x16x32_bf16 a[12:15],a[216:219],v[64:67],a[12:15]
s_waitcnt lgkmcnt(9)
ds_read_b64_tr_b16 a[242:243],v2 offset:4864
v_cvt_pk_bf16_f32 v11,v14,v15
v_mfma_f32_16x16x32_bf16 a[12:15],a[220:223],v[72:75],a[12:15]
s_waitcnt lgkmcnt(8)
ds_read_b64_tr_b16 a[244:245],v2 offset:8960
ds_read_b64_tr_b16 a[246:247],v2 offset:13056
v_mfma_f32_16x16x32_bf16 a[16:19],a[224:227],v[64:67],a[16:19]
s_waitcnt lgkmcnt(8)
ds_read_b64_tr_b16 a[248:249],v3 offset:768
v_mul_f32 v16,v16,v25
v_mfma_f32_16x16x32_bf16 a[16:19],a[228:231],v[72:75],a[16:19]
s_waitcnt lgkmcnt(1)
ds_read_b64_tr_b16 a[250:251],v3 offset:4864
v_mul_f32 v17,v17,v25
v_mfma_f32_16x16x32_bf16 a[20:23],a[232:235],v[64:67],a[20:23]
s_getreg_b32 s92,hwreg(HW_REG_IB_STS,0,4)
s_min_u32 s4,s92,s6
ds_read_b64_tr_b16 a[252:253],v3 offset:8960
v_mfma_f32_16x16x32_bf16 a[20:23],a[236:239],v[72:75],a[20:23]
s_mov_b32 s6,s92
ds_read_b64_tr_b16 a[254:255],v3 offset:13056
v_mul_f32 v18,v18,v25
v_mfma_f32_16x16x32_bf16 a[24:27],a[240:243],v[64:67],a[24:27]
s_cmp_gt_u32 s4,s9
s_cselect_b32 s4,15,s5
v_mul_f32 v19,v19,v25
v_mfma_f32_16x16x32_bf16 a[24:27],a[244:247],v[72:75],a[24:27]
s_waitcnt lgkmcnt(2)
v_mul_f32 v20,v20,v25
v_mul_f32 v21,v21,v25
v_mfma_f32_16x16x32_bf16 a[28:31],a[248:251],v[64:67],a[28:31]
s_waitcnt lgkmcnt(0)
v_mul_f32 v22,v22,v25
v_mul_f32 v23,v23,v25
v_mfma_f32_16x16x32_bf16 a[28:31],a[252:255],v[72:75],a[28:31]
s_getpc_b64 s[92:93]
s_cmp_lg_u32 s4,0
s_cbranch_scc1 L_pace_%=
v_mfma_f32_16x16x32_bf16 v[48:51],v[28:31],v[64:67],v[48:51]
v_cvt_pk_bf16_f32 v12,v16,v17
v_cvt_pk_bf16_f32 v13,v18,v19
v_mfma_f32_16x16x32_bf16 v[48:51],v[28:31],v[72:75],v[48:51]
s_add_u32 s10,s24,s74
s_addc_u32 s11,s25,0
v_cvt_pk_bf16_f32 v14,v20,v21
s_waitcnt vmcnt(15)
s_barrier
v_mfma_f32_16x16x32_bf16 v[112:115],a[128:131],v[224:227],v[44:47]
s_add_u32 m0,s76,16384
v_cvt_pk_bf16_f32 v15,v22,v23
v_accvgpr_read_b32 v16,a112
v_mfma_f32_16x16x32_bf16 v[112:115],a[132:135],v[232:235],v[112:115]
)A"
R"A(s_add_u32 s74,s74,16384
v_accvgpr_read_b32 v17,a113
v_accvgpr_read_b32 v18,a114
v_mfma_f32_16x16x32_bf16 v[112:115],a[136:139],v[240:243],v[112:115]
global_load_lds_dwordx4 v5,s[10:11] offset:3072
v_accvgpr_read_b32 v19,a115
v_mfma_f32_16x16x32_bf16 v[112:115],a[140:143],v[248:251],v[112:115]
s_and_b32 s74,s74,0x3ffff
v_accvgpr_read_b32 v20,a116
v_accvgpr_read_b32 v21,a117
v_mfma_f32_16x16x32_bf16 v[116:119],a[156:159],v[248:251],v[44:47]
s_or_b32 s74,s74,s69
v_accvgpr_read_b32 v22,a118
v_accvgpr_read_b32 v23,a119
v_mfma_f32_16x16x32_bf16 v[116:119],a[152:155],v[240:243],v[116:119]
s_sub_u32 s76,s76,32768
v_mul_f32 v16,v16,v27
v_mul_f32 v17,v17,v27
v_mfma_f32_16x16x32_bf16 v[116:119],a[148:151],v[232:235],v[116:119]
s_and_b32 s76,s76,0x1ffff
v_mul_f32 v18,v18,v27
v_mul_f32 v19,v19,v27
v_mfma_f32_16x16x32_bf16 v[116:119],a[144:147],v[224:227],v[116:119]
s_and_b32 s69,s75,0xfffc0000
v_mul_f32 v20,v20,v27
v_mul_f32 v21,v21,v27
v_mfma_f32_16x16x32_bf16 v[120:123],a[160:163],v[224:227],v[44:47]
s_add_u32 s75,s75,16384
v_mul_f32 v22,v22,v27
v_mul_f32 v23,v23,v27
v_mfma_f32_16x16x32_bf16 v[120:123],a[164:167],v[232:235],v[120:123]
v_cvt_pk_bf16_f32 v16,v16,v17
v_cvt_pk_bf16_f32 v17,v18,v19
v_mfma_f32_16x16x32_bf16 v[120:123],a[168:171],v[240:243],v[120:123]
v_cvt_pk_bf16_f32 v18,v20,v21
v_cvt_pk_bf16_f32 v19,v22,v23
v_mfma_f32_16x16x32_bf16 v[120:123],a[172:175],v[248:251],v[120:123]
v_accvgpr_write_b32 a112,v16
v_accvgpr_write_b32 a113,v17
v_mfma_f32_16x16x32_bf16 v[124:127],a[188:191],v[248:251],v[44:47]
v_accvgpr_write_b32 a114,v18
v_accvgpr_write_b32 a115,v19
v_accvgpr_write_b32 a116,v8
v_accvgpr_write_b32 a117,v9
v_accvgpr_write_b32 a118,v10
v_accvgpr_write_b32 a119,v11
v_exp_f32 v80,v80
v_exp_f32 v81,v81
v_exp_f32 v82,v82
v_exp_f32 v83,v83
v_exp_f32 v84,v84
v_exp_f32 v85,v85
v_exp_f32 v86,v86
v_exp_f32 v87,v87
v_mfma_f32_16x16x32_bf16 v[124:127],a[184:187],v[240:243],v[124:127]
v_cvt_pk_bf16_f32 v80,v80,v81
v_cvt_pk_bf16_f32 v81,v82,v83
v_cvt_pk_bf16_f32 v82,v84,v85
v_cvt_pk_bf16_f32 v83,v86,v87
v_exp_f32 v88,v88
v_exp_f32 v89,v89
v_exp_f32 v90,v90
v_exp_f32 v91,v91
v_exp_f32 v92,v92
v_exp_f32 v93,v93
v_exp_f32 v94,v94
v_exp_f32 v95,v95
v_mfma_f32_16x16x32_bf16 v[124:127],a[180:183],v[232:235],v[124:127]
v_cvt_pk_bf16_f32 v88,v88,v89
v_cvt_pk_bf16_f32 v89,v90,v91
v_cvt_pk_bf16_f32 v90,v92,v93
v_cvt_pk_bf16_f32 v91,v94,v95
v_mfma_f32_16x16x32_bf16 v[124:127],a[176:179],v[224:227],v[124:127]
s_add_u32 s10,s20,s74
s_addc_u32 s11,s21,0
s_add_u32 m0,s76,0
v_mfma_f32_16x16x32_bf16 v[132:135],a[192:195],v[80:83],v[132:135]
global_load_lds_dwordx4 v4,s[10:11] nt
v_add_u32 v1,s78,v1
v_mfma_f32_16x16x32_bf16 v[132:135],a[196:199],v[88:91],v[132:135]
ds_read_b128 a[128:131],v1
v_accvgpr_read_b32 v16,a120
v_mfma_f32_16x16x32_bf16 v[140:143],a[200:203],v[80:83],v[140:143]
v_exp_f32 v96,v96
v_mfma_f32_16x16x32_bf16 v[140:143],a[204:207],v[88:91],v[140:143]
ds_read_b128 a[132:135],v1 offset:256
v_accvgpr_read_b32 v17,a121
v_mfma_f32_16x16x32_bf16 v[148:151],a[208:211],v[80:83],v[148:151]
v_exp_f32 v97,v97
v_mfma_f32_16x16x32_bf16 v[148:151],a[212:215],v[88:91],v[148:151]
ds_read_b128 a[136:139],v1 offset:512
v_cvt_pk_bf16_f32 v96,v96,v97
v_mfma_f32_16x16x32_bf16 v[156:159],a[216:219],v[80:83],v[156:159]
v_exp_f32 v98,v98
v_mfma_f32_16x16x32_bf16 v[156:159],a[220:223],v[88:91],v[156:159]
ds_read_b128 a[140:143],v1 offset:768
v_accvgpr_read_b32 v18,a122
v_mfma_f32_16x16x32_bf16 v[164:167],a[224:227],v[80:83],v[164:167]
v_exp_f32 v99,v99
v_mfma_f32_16x16x32_bf16 v[164:167],a[228:231],v[88:91],v[164:167]
ds_read_b128 a[156:159],v1 offset:4864
v_cvt_pk_bf16_f32 v97,v98,v99
v_mfma_f32_16x16x32_bf16 v[172:175],a[232:235],v[80:83],v[172:175]
v_exp_f32 v100,v100
v_mfma_f32_16x16x32_bf16 v[172:175],a[236:239],v[88:91],v[172:175]
ds_read_b128 a[152:155],v1 offset:4608
v_accvgpr_read_b32 v19,a123
v_mfma_f32_16x16x32_bf16 v[180:183],a[240:243],v[80:83],v[180:183]
v_exp_f32 v101,v101
v_mfma_f32_16x16x32_bf16 v[180:183],a[244:247],v[88:91],v[180:183]
ds_read_b128 a[148:151],v1 offset:4352
v_cvt_pk_bf16_f32 v98,v100,v101
v_mfma_f32_16x16x32_bf16 v[188:191],a[248:251],v[80:83],v[188:191]
global_load_lds_dwordx4 v4,s[10:11] offset:1024 nt
v_accvgpr_read_b32 v20,a124
v_mfma_f32_16x16x32_bf16 v[188:191],a[252:255],v[88:91],v[188:191]
ds_read_b128 a[144:147],v1 offset:4096
v_accvgpr_read_b32 v21,a125
v_mfma_f32_16x16x32_bf16 v[52:55],v[28:31],v[80:83],v[52:55]
v_exp_f32 v102,v102
v_mfma_f32_16x16x32_bf16 v[52:55],v[28:31],v[88:91],v[52:55]
s_waitcnt lgkmcnt(3)
ds_read_b128 a[160:163],v1 offset:8192
v_accvgpr_read_b32 v22,a126
v_mfma_f32_16x16x32_bf16 v[64:67],a[128:131],v[128:131],v[32:35]
v_exp_f32 v103,v103
v_mfma_f32_16x16x32_bf16 v[64:67],a[132:135],v[136:139],v[64:67]
ds_read_b128 a[164:167],v1 offset:8448
v_cvt_pk_bf16_f32 v99,v102,v103
v_mfma_f32_16x16x32_bf16 v[64:67],a[136:139],v[144:147],v[64:67]
v_exp_f32 v104,v104
v_mfma_f32_16x16x32_bf16 v[64:67],a[140:143],v[152:155],v[64:67]
ds_read_b128 a[168:171],v1 offset:8704
v_accvgpr_read_b32 v23,a127
v_mfma_f32_16x16x32_bf16 v[68:71],a[156:159],v[152:155],v[32:35]
s_waitcnt lgkmcnt(4)
v_exp_f32 v105,v105
v_mfma_f32_16x16x32_bf16 v[68:71],a[152:155],v[144:147],v[68:71]
ds_read_b128 a[172:175],v1 offset:8960
v_cvt_pk_bf16_f32 v104,v104,v105
v_mfma_f32_16x16x32_bf16 v[68:71],a[148:151],v[136:139],v[68:71]
s_waitcnt lgkmcnt(4)
v_exp_f32 v106,v106
v_mfma_f32_16x16x32_bf16 v[68:71],a[144:147],v[128:131],v[68:71]
s_waitcnt lgkmcnt(3)
ds_read_b128 a[188:191],v1 offset:13056
v_mul_f32 v16,v16,v27
v_mfma_f32_16x16x32_bf16 v[72:75],a[160:163],v[128:131],v[32:35]
s_waitcnt lgkmcnt(3)
v_exp_f32 v107,v107
v_mfma_f32_16x16x32_bf16 v[72:75],a[164:167],v[136:139],v[72:75]
s_waitcnt lgkmcnt(1)
ds_read_b128 a[184:187],v1 offset:12800
v_cvt_pk_bf16_f32 v105,v106,v107
v_mfma_f32_16x16x32_bf16 v[72:75],a[168:171],v[144:147],v[72:75]
global_load_lds_dwordx4 v5,s[10:11] offset:2048 nt
ds_read_b128 a[180:183],v1 offset:12544
v_mfma_f32_16x16x32_bf16 v[72:75],a[172:175],v[152:155],v[72:75]
s_waitcnt lgkmcnt(2)
ds_read_b128 a[176:179],v1 offset:12288
v_mul_f32 v17,v17,v27
v_mfma_f32_16x16x32_bf16 v[76:79],a[188:191],v[152:155],v[32:35]
s_waitcnt lgkmcnt(2)
v_exp_f32 v108,v108
v_mfma_f32_16x16x32_bf16 v[76:79],a[184:187],v[144:147],v[76:79]
s_waitcnt lgkmcnt(1)
v_exp_f32 v109,v109
v_exp_f32 v110,v110
v_exp_f32 v111,v111
v_mfma_f32_16x16x32_bf16 v[76:79],a[180:183],v[136:139],v[76:79]
s_waitcnt lgkmcnt(0)
v_cvt_pk_bf16_f32 v106,v108,v109
v_cvt_pk_bf16_f32 v107,v110,v111
v_mfma_f32_16x16x32_bf16 v[76:79],a[176:179],v[128:131],v[76:79]
v_mul_f32 v18,v18,v27
v_mul_f32 v19,v19,v27
v_mfma_f32_16x16x32_bf16 v[196:199],a[192:195],v[96:99],v[196:199]
v_mul_f32 v20,v20,v27
v_mul_f32 v21,v21,v27
v_mfma_f32_16x16x32_bf16 v[196:199],a[196:199],v[104:107],v[196:199]
v_mul_f32 v22,v22,v27
v_mul_f32 v23,v23,v27
v_mfma_f32_16x16x32_bf16 v[204:207],a[200:203],v[96:99],v[204:207]
v_cvt_pk_bf16_f32 v16,v16,v17
v_cvt_pk_bf16_f32 v17,v18,v19
v_mfma_f32_16x16x32_bf16 v[204:207],a[204:207],v[104:107],v[204:207]
v_cvt_pk_bf16_f32 v18,v20,v21
v_cvt_pk_bf16_f32 v19,v22,v23
v_mfma_f32_16x16x32_bf16 v[212:215],a[208:211],v[96:99],v[212:215]
v_accvgpr_write_b32 a120,v16
v_accvgpr_write_b32 a121,v17
v_mfma_f32_16x16x32_bf16 v[212:215],a[212:215],v[104:107],v[212:215]
v_accvgpr_write_b32 a122,v18
v_accvgpr_write_b32 a123,v19
v_mfma_f32_16x16x32_bf16 v[220:223],a[216:219],v[96:99],v[220:223]
v_accvgpr_write_b32 a124,v12
v_accvgpr_write_b32 a125,v13
v_mfma_f32_16x16x32_bf16 v[220:223],a[220:223],v[104:107],v[220:223]
v_accvgpr_write_b32 a126,v14
v_accvgpr_write_b32 a127,v15
v_mfma_f32_16x16x32_bf16 v[228:231],a[224:227],v[96:99],v[228:231]
global_load_lds_dwordx4 v5,s[10:11] offset:3072
v_add_u32 v2,s78,v2
v_mfma_f32_16x16x32_bf16 v[228:231],a[228:231],v[104:107],v[228:231]
v_exp_f32 v112,v112
v_mfma_f32_16x16x32_bf16 v[236:239],a[232:235],v[96:99],v[236:239]
v_exp_f32 v113,v113
v_mfma_f32_16x16x32_bf16 v[236:239],a[236:239],v[104:107],v[236:239]
v_exp_f32 v114,v114
v_mfma_f32_16x16x32_bf16 v[244:247],a[240:243],v[96:99],v[244:247]
v_exp_f32 v115,v115
v_mfma_f32_16x16x32_bf16 v[244:247],a[244:247],v[104:107],v[244:247]
v_exp_f32 v116,v116
v_mfma_f32_16x16x32_bf16 v[252:255],a[248:251],v[96:99],v[252:255]
v_exp_f32 v117,v117
v_mfma_f32_16x16x32_bf16 v[252:255],a[252:255],v[104:107],v[252:255]
v_exp_f32 v118,v118
v_mfma_f32_16x16x32_bf16 v[56:59],v[28:31],v[96:99],v[56:59]
v_exp_f32 v119,v119
v_mfma_f32_16x16x32_bf16 v[56:59],v[28:31],v[104:107],v[56:59]
v_cvt_pk_bf16_f32 v112,v112,v113
v_cvt_pk_bf16_f32 v113,v114,v115
v_mfma_f32_16x16x32_bf16 v[80:83],a[128:131],v[160:163],v[36:39]
v_cvt_pk_bf16_f32 v114,v116,v117
v_cvt_pk_bf16_f32 v115,v118,v119
v_mfma_f32_16x16x32_bf16 v[80:83],a[132:135],v[168:171],v[80:83]
s_add_u32 s10,s24,s74
s_addc_u32 s11,s25,0
v_add_u32 v3,s78,v3
v_mfma_f32_16x16x32_bf16 v[80:83],a[136:139],v[176:179],v[80:83]
s_add_u32 s74,s74,16384
v_exp_f32 v120,v120
v_mfma_f32_16x16x32_bf16 v[80:83],a[140:143],v[184:187],v[80:83]
s_add_u32 m0,s76,16384
v_exp_f32 v121,v121
v_mfma_f32_16x16x32_bf16 v[84:87],a[156:159],v[184:187],v[36:39]
s_and_b32 s74,s74,0x3ffff
v_exp_f32 v122,v122
v_mfma_f32_16x16x32_bf16 v[84:87],a[152:155],v[176:179],v[84:87]
global_load_lds_dwordx4 v4,s[10:11]
v_cvt_pk_bf16_f32 v120,v120,v121
v_mfma_f32_16x16x32_bf16 v[84:87],a[148:151],v[168:171],v[84:87]
s_or_b32 s74,s74,s69
v_exp_f32 v123,v123
v_mfma_f32_16x16x32_bf16 v[84:87],a[144:147],v[160:163],v[84:87]
s_sub_u32 s76,s76,32768
v_exp_f32 v124,v124
v_mfma_f32_16x16x32_bf16 v[88:91],a[160:163],v[160:163],v[36:39]
s_and_b32 s76,s76,0x1ffff
v_exp_f32 v125,v125
v_mfma_f32_16x16x32_bf16 v[88:91],a[164:167],v[168:171],v[88:91]
s_sub_u32 s78,s77,32768
v_exp_f32 v126,v126
v_mfma_f32_16x16x32_bf16 v[88:91],a[168:171],v[176:179],v[88:91]
s_and_b32 s78,s78,0x1ffff
v_exp_f32 v127,v127
v_mfma_f32_16x16x32_bf16 v[88:91],a[172:175],v[184:187],v[88:91]
s_sub_u32 s78,s78,s77
v_cvt_pk_bf16_f32 v121,v122,v123
v_cvt_pk_bf16_f32 v122,v124,v125
v_mfma_f32_16x16x32_bf16 v[92:95],a[188:191],v[184:187],v[36:39]
s_and_b32 s69,s75,0xfffc0000
v_cvt_pk_bf16_f32 v123,v126,v127
v_accvgpr_read_b32 v12,a100
v_mfma_f32_16x16x32_bf16 v[92:95],a[184:187],v[176:179],v[92:95]
s_add_u32 s77,s77,s78
v_exp_f32 v64,v64
v_mfma_f32_16x16x32_bf16 v[92:95],a[180:183],v[168:171],v[92:95]
s_add_u32 s75,s75,16384
v_exp_f32 v65,v65
v_mfma_f32_16x16x32_bf16 v[92:95],a[176:179],v[160:163],v[92:95]
v_exp_f32 v66,v66
v_mfma_f32_16x16x32_bf16 a[32:35],a[192:195],v[112:115],a[32:35]
ds_read_b64_tr_b16 a[192:193],v2
ds_read_b64_tr_b16 a[194:195],v2 offset:4096
v_mfma_f32_16x16x32_bf16 a[32:35],a[196:199],v[120:123],a[32:35]
ds_read_b64_tr_b16 a[196:197],v2 offset:8192
v_cvt_pk_bf16_f32 v64,v64,v65
v_mfma_f32_16x16x32_bf16 a[36:39],a[200:203],v[112:115],a[36:39]
ds_read_b64_tr_b16 a[198:199],v2 offset:12288
v_accvgpr_read_b32 v13,a101
v_mfma_f32_16x16x32_bf16 a[36:39],a[204:207],v[120:123],a[36:39]
global_load_lds_dwordx4 v5,s[10:11] offset:1024
ds_read_b64_tr_b16 a[200:201],v3
v_mfma_f32_16x16x32_bf16 a[40:43],a[208:211],v[112:115],a[40:43]
ds_read_b64_tr_b16 a[202:203],v3 offset:4096
ds_read_b64_tr_b16 a[204:205],v3 offset:8192
v_mfma_f32_16x16x32_bf16 a[40:43],a[212:215],v[120:123],a[40:43]
ds_read_b64_tr_b16 a[206:207],v3 offset:12288
v_accvgpr_read_b32 v14,a102
v_mfma_f32_16x16x32_bf16 a[44:47],a[216:219],v[112:115],a[44:47]
ds_read_b64_tr_b16 a[208:209],v2 offset:256
v_accvgpr_read_b32 v15,a103
v_mfma_f32_16x16x32_bf16 a[44:47],a[220:223],v[120:123],a[44:47]
ds_read_b64_tr_b16 a[210:211],v2 offset:4352
v_accvgpr_read_b32 v8,a96
v_mfma_f32_16x16x32_bf16 a[48:51],a[224:227],v[112:115],a[48:51]
ds_read_b64_tr_b16 a[212:213],v2 offset:8448
ds_read_b64_tr_b16 a[214:215],v2 offset:12544
v_mfma_f32_16x16x32_bf16 a[48:51],a[228:231],v[120:123],a[48:51]
v_exp_f32 v67,v67
v_mfma_f32_16x16x32_bf16 a[52:55],a[232:235],v[112:115],a[52:55]
v_exp_f32 v68,v68
v_mfma_f32_16x16x32_bf16 a[52:55],a[236:239],v[120:123],a[52:55]
v_exp_f32 v69,v69
v_mfma_f32_16x16x32_bf16 a[56:59],a[240:243],v[112:115],a[56:59]
)A"
R"A(v_exp_f32 v70,v70
v_mfma_f32_16x16x32_bf16 a[56:59],a[244:247],v[120:123],a[56:59]
v_exp_f32 v71,v71
v_mfma_f32_16x16x32_bf16 a[60:63],a[248:251],v[112:115],a[60:63]
ds_read_b64_tr_b16 a[216:217],v3 offset:256
ds_read_b64_tr_b16 a[218:219],v3 offset:4352
v_mfma_f32_16x16x32_bf16 a[60:63],a[252:255],v[120:123],a[60:63]
v_cvt_pk_bf16_f32 v65,v66,v67
v_cvt_pk_bf16_f32 v66,v68,v69
v_mfma_f32_16x16x32_bf16 v[60:63],v[28:31],v[112:115],v[60:63]
ds_read_b64_tr_b16 a[220:221],v3 offset:8448
v_cvt_pk_bf16_f32 v67,v70,v71
v_mfma_f32_16x16x32_bf16 v[60:63],v[28:31],v[120:123],v[60:63]
global_load_lds_dwordx4 v4,s[10:11] offset:2048
ds_read_b64_tr_b16 a[222:223],v3 offset:12544
v_mfma_f32_16x16x32_bf16 v[96:99],a[128:131],v[192:195],v[40:43]
v_exp_f32 v72,v72
v_mfma_f32_16x16x32_bf16 v[96:99],a[132:135],v[200:203],v[96:99]
v_exp_f32 v73,v73
v_mfma_f32_16x16x32_bf16 v[96:99],a[136:139],v[208:211],v[96:99]
v_exp_f32 v74,v74
v_mfma_f32_16x16x32_bf16 v[96:99],a[140:143],v[216:219],v[96:99]
v_exp_f32 v75,v75
v_mfma_f32_16x16x32_bf16 v[100:103],a[156:159],v[216:219],v[40:43]
v_exp_f32 v76,v76
v_mfma_f32_16x16x32_bf16 v[100:103],a[152:155],v[208:211],v[100:103]
v_exp_f32 v77,v77
v_mfma_f32_16x16x32_bf16 v[100:103],a[148:151],v[200:203],v[100:103]
v_exp_f32 v78,v78
v_mfma_f32_16x16x32_bf16 v[100:103],a[144:147],v[192:195],v[100:103]
v_exp_f32 v79,v79
v_mfma_f32_16x16x32_bf16 v[104:107],a[160:163],v[192:195],v[40:43]
v_cvt_pk_bf16_f32 v72,v72,v73
v_cvt_pk_bf16_f32 v73,v74,v75
v_mfma_f32_16x16x32_bf16 v[104:107],a[164:167],v[200:203],v[104:107]
v_cvt_pk_bf16_f32 v74,v76,v77
v_cvt_pk_bf16_f32 v75,v78,v79
v_mfma_f32_16x16x32_bf16 v[104:107],a[168:171],v[208:211],v[104:107]
ds_read_b64_tr_b16 a[224:225],v2 offset:512
ds_read_b64_tr_b16 a[226:227],v2 offset:4608
v_mfma_f32_16x16x32_bf16 v[104:107],a[172:175],v[216:219],v[104:107]
v_accvgpr_read_b32 v9,a97
v_accvgpr_read_b32 v10,a98
v_mfma_f32_16x16x32_bf16 v[108:111],a[188:191],v[216:219],v[40:43]
v_accvgpr_read_b32 v11,a99
v_mul_f32 v8,v8,v27
v_mfma_f32_16x16x32_bf16 v[108:111],a[184:187],v[208:211],v[108:111]
global_load_lds_dwordx4 v5,s[10:11] offset:3072
v_mul_f32 v9,v9,v27
v_mfma_f32_16x16x32_bf16 v[108:111],a[180:183],v[200:203],v[108:111]
v_mul_f32 v10,v10,v27
v_mul_f32 v11,v11,v27
v_mfma_f32_16x16x32_bf16 v[108:111],a[176:179],v[192:195],v[108:111]
s_waitcnt lgkmcnt(2)
v_mul_f32 v12,v12,v27
v_mul_f32 v13,v13,v27
v_mul_f32 v14,v14,v27
v_mul_f32 v15,v15,v27
v_cvt_pk_bf16_f32 v8,v8,v9
v_cvt_pk_bf16_f32 v9,v10,v11
v_cvt_pk_bf16_f32 v10,v12,v13
v_cvt_pk_bf16_f32 v11,v14,v15
v_accvgpr_write_b32 a96,v8
v_accvgpr_write_b32 a97,v9
v_accvgpr_write_b32 a98,v10
v_accvgpr_write_b32 a99,v11
v_mfma_f32_16x16x32_bf16 a[0:3],a[192:195],v[64:67],a[0:3]
ds_read_b128 a[100:103],v7 offset:2048
v_add_u32 v1,s78,v1
v_mfma_f32_16x16x32_bf16 a[0:3],a[196:199],v[72:75],a[0:3]
ds_read_b64_tr_b16 a[228:229],v2 offset:8704
v_accvgpr_read_b32 v20,a108
v_mfma_f32_16x16x32_bf16 a[4:7],a[200:203],v[64:67],a[4:7]
ds_read_b64_tr_b16 a[230:231],v2 offset:12800
v_accvgpr_read_b32 v21,a109
v_mfma_f32_16x16x32_bf16 a[4:7],a[204:207],v[72:75],a[4:7]
ds_read_b64_tr_b16 a[232:233],v3 offset:512
v_accvgpr_read_b32 v22,a110
v_mfma_f32_16x16x32_bf16 a[8:11],a[208:211],v[64:67],a[8:11]
ds_read_b64_tr_b16 a[234:235],v3 offset:4608
ds_read_b64_tr_b16 a[236:237],v3 offset:8704
v_mfma_f32_16x16x32_bf16 a[8:11],a[212:215],v[72:75],a[8:11]
ds_read_b64_tr_b16 a[238:239],v3 offset:12800
v_accvgpr_read_b32 v23,a111
v_mfma_f32_16x16x32_bf16 a[12:15],a[216:219],v[64:67],a[12:15]
ds_read_b64_tr_b16 a[240:241],v2 offset:768
v_accvgpr_read_b32 v16,a104
v_mfma_f32_16x16x32_bf16 a[12:15],a[220:223],v[72:75],a[12:15]
s_waitcnt lgkmcnt(8)
ds_read_b64_tr_b16 a[242:243],v2 offset:4864
v_accvgpr_read_b32 v17,a105
v_mfma_f32_16x16x32_bf16 a[16:19],a[224:227],v[64:67],a[16:19]
s_waitcnt lgkmcnt(6)
ds_read_b64_tr_b16 a[244:245],v2 offset:8960
ds_read_b64_tr_b16 a[246:247],v2 offset:13056
v_mfma_f32_16x16x32_bf16 a[16:19],a[228:231],v[72:75],a[16:19]
s_waitcnt lgkmcnt(0)
ds_read_b64_tr_b16 a[248:249],v3 offset:768
v_accvgpr_read_b32 v18,a106
v_mfma_f32_16x16x32_bf16 a[20:23],a[232:235],v[64:67],a[20:23]
s_getreg_b32 s92,hwreg(HW_REG_IB_STS,0,4)
s_min_u32 s4,s92,s6
ds_read_b64_tr_b16 a[250:251],v3 offset:4864
v_mfma_f32_16x16x32_bf16 a[20:23],a[236:239],v[72:75],a[20:23]
s_mov_b32 s6,s92
ds_read_b64_tr_b16 a[252:253],v3 offset:8960
ds_read_b64_tr_b16 a[254:255],v3 offset:13056
v_mfma_f32_16x16x32_bf16 a[24:27],a[240:243],v[64:67],a[24:27]
s_cmp_gt_u32 s4,s9
s_cselect_b32 s4,15,s5
v_accvgpr_read_b32 v19,a107
v_mfma_f32_16x16x32_bf16 a[24:27],a[244:247],v[72:75],a[24:27]
s_waitcnt lgkmcnt(2)
v_exp_f32 v80,v80
v_mfma_f32_16x16x32_bf16 a[28:31],a[248:251],v[64:67],a[28:31]
s_waitcnt lgkmcnt(0)
v_exp_f32 v81,v81
v_mfma_f32_16x16x32_bf16 a[28:31],a[252:255],v[72:75],a[28:31]
s_getpc_b64 s[92:93]
s_cmp_lg_u32 s4,0
s_cbranch_scc1 L_pace_%=
v_mfma_f32_16x16x32_bf16 v[48:51],v[28:31],v[64:67],v[48:51]
v_exp_f32 v82,v82
v_mfma_f32_16x16x32_bf16 v[48:51],v[28:31],v[72:75],v[48:51]
s_add_u32 s10,s20,s74
s_addc_u32 s11,s21,0
v_cvt_pk_bf16_f32 v80,v80,v81
s_waitcnt vmcnt(16)
s_barrier
v_mfma_f32_16x16x32_bf16 v[112:115],a[128:131],v[224:227],v[44:47]
s_add_u32 m0,s76,0
v_exp_f32 v83,v83
v_mfma_f32_16x16x32_bf16 v[112:115],a[132:135],v[232:235],v[112:115]
v_exp_f32 v84,v84
v_mfma_f32_16x16x32_bf16 v[112:115],a[136:139],v[240:243],v[112:115]
global_load_lds_dwordx4 v4,s[10:11] nt
v_cvt_pk_bf16_f32 v81,v82,v83
v_mfma_f32_16x16x32_bf16 v[112:115],a[140:143],v[248:251],v[112:115]
v_exp_f32 v85,v85
v_mfma_f32_16x16x32_bf16 v[116:119],a[156:159],v[248:251],v[44:47]
v_exp_f32 v86,v86
v_mfma_f32_16x16x32_bf16 v[116:119],a[152:155],v[240:243],v[116:119]
v_exp_f32 v87,v87
v_mfma_f32_16x16x32_bf16 v[116:119],a[148:151],v[232:235],v[116:119]
v_cvt_pk_bf16_f32 v82,v84,v85
v_cvt_pk_bf16_f32 v83,v86,v87
v_mfma_f32_16x16x32_bf16 v[116:119],a[144:147],v[224:227],v[116:119]
v_exp_f32 v88,v88
v_mfma_f32_16x16x32_bf16 v[120:123],a[160:163],v[224:227],v[44:47]
v_exp_f32 v89,v89
v_mfma_f32_16x16x32_bf16 v[120:123],a[164:167],v[232:235],v[120:123]
v_exp_f32 v90,v90
v_mfma_f32_16x16x32_bf16 v[120:123],a[168:171],v[240:243],v[120:123]
v_exp_f32 v91,v91
v_mfma_f32_16x16x32_bf16 v[120:123],a[172:175],v[248:251],v[120:123]
v_exp_f32 v92,v92
v_mfma_f32_16x16x32_bf16 v[124:127],a[188:191],v[248:251],v[44:47]
v_exp_f32 v93,v93
v_mfma_f32_16x16x32_bf16 v[124:127],a[184:187],v[240:243],v[124:127]
v_exp_f32 v94,v94
v_exp_f32 v95,v95
v_mfma_f32_16x16x32_bf16 v[124:127],a[180:183],v[232:235],v[124:127]
v_cvt_pk_bf16_f32 v88,v88,v89
v_cvt_pk_bf16_f32 v89,v90,v91
v_cvt_pk_bf16_f32 v90,v92,v93
v_cvt_pk_bf16_f32 v91,v94,v95
v_mfma_f32_16x16x32_bf16 v[124:127],a[176:179],v[224:227],v[124:127]
ds_read_b128 a[128:131],v1
v_mul_f32 v16,v16,v27
v_mfma_f32_16x16x32_bf16 v[132:135],a[192:195],v[80:83],v[132:135]
v_mul_f32 v17,v17,v27
v_mul_f32 v18,v18,v27
v_mfma_f32_16x16x32_bf16 v[132:135],a[196:199],v[88:91],v[132:135]
ds_read_b128 a[132:135],v1 offset:256
v_mul_f32 v19,v19,v27
v_mfma_f32_16x16x32_bf16 v[140:143],a[200:203],v[80:83],v[140:143]
global_load_lds_dwordx4 v4,s[10:11] offset:1024 nt
v_mul_f32 v20,v20,v27
v_mfma_f32_16x16x32_bf16 v[140:143],a[204:207],v[88:91],v[140:143]
ds_read_b128 a[136:139],v1 offset:512
v_mul_f32 v21,v21,v27
v_mfma_f32_16x16x32_bf16 v[148:151],a[208:211],v[80:83],v[148:151]
v_mul_f32 v22,v22,v27
v_mul_f32 v23,v23,v27
v_mfma_f32_16x16x32_bf16 v[148:151],a[212:215],v[88:91],v[148:151]
ds_read_b128 a[140:143],v1 offset:768
v_cvt_pk_bf16_f32 v16,v16,v17
v_mfma_f32_16x16x32_bf16 v[156:159],a[216:219],v[80:83],v[156:159]
v_cvt_pk_bf16_f32 v17,v18,v19
v_cvt_pk_bf16_f32 v18,v20,v21
v_mfma_f32_16x16x32_bf16 v[156:159],a[220:223],v[88:91],v[156:159]
ds_read_b128 a[156:159],v1 offset:4864
v_cvt_pk_bf16_f32 v19,v22,v23
v_mfma_f32_16x16x32_bf16 v[164:167],a[224:227],v[80:83],v[164:167]
v_accvgpr_write_b32 a104,v16
v_accvgpr_write_b32 a105,v17
v_mfma_f32_16x16x32_bf16 v[164:167],a[228:231],v[88:91],v[164:167]
ds_read_b128 a[152:155],v1 offset:4608
v_accvgpr_write_b32 a106,v18
v_mfma_f32_16x16x32_bf16 v[172:175],a[232:235],v[80:83],v[172:175]
v_accvgpr_write_b32 a107,v19
v_accvgpr_read_b32 v12,a68
v_mfma_f32_16x16x32_bf16 v[172:175],a[236:239],v[88:91],v[172:175]
ds_read_b128 a[148:151],v1 offset:4352
v_accvgpr_read_b32 v13,a69
v_mfma_f32_16x16x32_bf16 v[180:183],a[240:243],v[80:83],v[180:183]
v_exp_f32 v96,v96
v_mfma_f32_16x16x32_bf16 v[180:183],a[244:247],v[88:91],v[180:183]
ds_read_b128 a[144:147],v1 offset:4096
v_accvgpr_read_b32 v14,a70
v_mfma_f32_16x16x32_bf16 v[188:191],a[248:251],v[80:83],v[188:191]
L_pd9_%=:
s_cbranch_execz L_pd13_%=
v_exp_f32 v97,v97
v_mfma_f32_16x16x32_bf16 v[188:191],a[252:255],v[88:91],v[188:191]
ds_read_b128 a[160:163],v1 offset:8192
v_cvt_pk_bf16_f32 v96,v96,v97
v_mfma_f32_16x16x32_bf16 v[52:55],v[28:31],v[80:83],v[52:55]
v_exp_f32 v98,v98
v_mfma_f32_16x16x32_bf16 v[52:55],v[28:31],v[88:91],v[52:55]
s_waitcnt lgkmcnt(3)
ds_read_b128 a[164:167],v1 offset:8448
v_accvgpr_read_b32 v15,a71
v_mfma_f32_16x16x32_bf16 v[64:67],a[128:131],v[128:131],v[32:35]
global_load_lds_dwordx4 v5,s[10:11] offset:2048 nt
v_accvgpr_read_b32 v8,a64
v_mfma_f32_16x16x32_bf16 v[64:67],a[132:135],v[136:139],v[64:67]
ds_read_b128 a[168:171],v1 offset:8704
v_accvgpr_read_b32 v9,a65
v_mfma_f32_16x16x32_bf16 v[64:67],a[136:139],v[144:147],v[64:67]
v_exp_f32 v99,v99
v_mfma_f32_16x16x32_bf16 v[64:67],a[140:143],v[152:155],v[64:67]
ds_read_b128 a[108:111],v7 offset:3072
v_cvt_pk_bf16_f32 v97,v98,v99
v_mfma_f32_16x16x32_bf16 v[68:71],a[156:159],v[152:155],v[32:35]
v_exp_f32 v100,v100
v_mfma_f32_16x16x32_bf16 v[68:71],a[152:155],v[144:147],v[68:71]
s_waitcnt lgkmcnt(3)
ds_read_b128 a[172:175],v1 offset:8960
v_accvgpr_read_b32 v10,a66
v_mfma_f32_16x16x32_bf16 v[68:71],a[148:151],v[136:139],v[68:71]
v_exp_f32 v101,v101
v_mfma_f32_16x16x32_bf16 v[68:71],a[144:147],v[128:131],v[68:71]
ds_read_b128 a[188:191],v1 offset:13056
v_cvt_pk_bf16_f32 v98,v100,v101
v_mfma_f32_16x16x32_bf16 v[72:75],a[160:163],v[128:131],v[32:35]
s_waitcnt lgkmcnt(4)
v_exp_f32 v102,v102
v_mfma_f32_16x16x32_bf16 v[72:75],a[164:167],v[136:139],v[72:75]
s_waitcnt lgkmcnt(3)
ds_read_b128 a[184:187],v1 offset:12800
v_accvgpr_read_b32 v11,a67
v_mfma_f32_16x16x32_bf16 v[72:75],a[168:171],v[144:147],v[72:75]
s_waitcnt lgkmcnt(2)
ds_read_b128 a[180:183],v1 offset:12544
v_mul_f32 v8,v8,v26
v_mfma_f32_16x16x32_bf16 v[72:75],a[172:175],v[152:155],v[72:75]
s_waitcnt lgkmcnt(2)
ds_read_b128 a[176:179],v1 offset:12288
v_mul_f32 v9,v9,v26
v_mfma_f32_16x16x32_bf16 v[76:79],a[188:191],v[152:155],v[32:35]
s_waitcnt lgkmcnt(2)
v_exp_f32 v103,v103
v_mfma_f32_16x16x32_bf16 v[76:79],a[184:187],v[144:147],v[76:79]
s_waitcnt lgkmcnt(1)
v_cvt_pk_bf16_f32 v99,v102,v103
v_exp_f32 v104,v104
v_exp_f32 v105,v105
v_exp_f32 v106,v106
v_exp_f32 v107,v107
v_exp_f32 v108,v108
v_exp_f32 v109,v109
v_exp_f32 v110,v110
v_exp_f32 v111,v111
v_mfma_f32_16x16x32_bf16 v[76:79],a[180:183],v[136:139],v[76:79]
s_waitcnt lgkmcnt(0)
v_cvt_pk_bf16_f32 v104,v104,v105
v_cvt_pk_bf16_f32 v105,v106,v107
v_cvt_pk_bf16_f32 v106,v108,v109
v_cvt_pk_bf16_f32 v107,v110,v111
v_mfma_f32_16x16x32_bf16 v[76:79],a[176:179],v[128:131],v[76:79]
v_exp_f32 v112,v112
v_mfma_f32_16x16x32_bf16 v[196:199],a[192:195],v[96:99],v[196:199]
global_load_lds_dwordx4 v5,s[10:11] offset:3072
v_mul_f32 v10,v10,v26
v_mfma_f32_16x16x32_bf16 v[196:199],a[196:199],v[104:107],v[196:199]
v_exp_f32 v113,v113
v_mfma_f32_16x16x32_bf16 v[204:207],a[200:203],v[96:99],v[204:207]
v_exp_f32 v114,v114
v_mfma_f32_16x16x32_bf16 v[204:207],a[204:207],v[104:107],v[204:207]
v_exp_f32 v115,v115
v_mfma_f32_16x16x32_bf16 v[212:215],a[208:211],v[96:99],v[212:215]
v_exp_f32 v116,v116
v_mfma_f32_16x16x32_bf16 v[212:215],a[212:215],v[104:107],v[212:215]
v_exp_f32 v117,v117
v_mfma_f32_16x16x32_bf16 v[220:223],a[216:219],v[96:99],v[220:223]
v_exp_f32 v118,v118
v_mfma_f32_16x16x32_bf16 v[220:223],a[220:223],v[104:107],v[220:223]
v_exp_f32 v119,v119
v_mfma_f32_16x16x32_bf16 v[228:231],a[224:227],v[96:99],v[228:231]
)A"
R"A(v_cvt_pk_bf16_f32 v112,v112,v113
v_cvt_pk_bf16_f32 v113,v114,v115
v_mfma_f32_16x16x32_bf16 v[228:231],a[228:231],v[104:107],v[228:231]
v_cvt_pk_bf16_f32 v114,v116,v117
v_cvt_pk_bf16_f32 v115,v118,v119
v_mfma_f32_16x16x32_bf16 v[236:239],a[232:235],v[96:99],v[236:239]
v_exp_f32 v120,v120
v_mfma_f32_16x16x32_bf16 v[236:239],a[236:239],v[104:107],v[236:239]
v_exp_f32 v121,v121
v_mfma_f32_16x16x32_bf16 v[244:247],a[240:243],v[96:99],v[244:247]
s_add_u32 s10,s24,s74
s_addc_u32 s11,s25,0
v_cvt_pk_bf16_f32 v120,v120,v121
v_mfma_f32_16x16x32_bf16 v[244:247],a[244:247],v[104:107],v[244:247]
s_add_u32 s74,s74,16384
v_exp_f32 v122,v122
v_mfma_f32_16x16x32_bf16 v[252:255],a[248:251],v[96:99],v[252:255]
s_add_u32 m0,s76,16384
v_exp_f32 v123,v123
v_mfma_f32_16x16x32_bf16 v[252:255],a[252:255],v[104:107],v[252:255]
s_and_b32 s74,s74,0x3ffff
v_exp_f32 v124,v124
v_mfma_f32_16x16x32_bf16 v[56:59],v[28:31],v[96:99],v[56:59]
global_load_lds_dwordx4 v4,s[10:11]
v_cvt_pk_bf16_f32 v121,v122,v123
v_mfma_f32_16x16x32_bf16 v[56:59],v[28:31],v[104:107],v[56:59]
s_or_b32 s74,s74,s69
v_exp_f32 v125,v125
v_mfma_f32_16x16x32_bf16 v[80:83],a[128:131],v[160:163],v[36:39]
s_sub_u32 s76,s76,32768
v_exp_f32 v126,v126
v_mfma_f32_16x16x32_bf16 v[80:83],a[132:135],v[168:171],v[80:83]
s_and_b32 s76,s76,0x1ffff
v_exp_f32 v127,v127
v_mfma_f32_16x16x32_bf16 v[80:83],a[136:139],v[176:179],v[80:83]
s_and_b32 s69,s75,0xfffc0000
v_cvt_pk_bf16_f32 v122,v124,v125
v_cvt_pk_bf16_f32 v123,v126,v127
v_mfma_f32_16x16x32_bf16 v[80:83],a[140:143],v[184:187],v[80:83]
v_mul_f32 v11,v11,v26
v_mul_f32 v12,v12,v26
v_mfma_f32_16x16x32_bf16 v[84:87],a[156:159],v[184:187],v[36:39]
v_mul_f32 v13,v13,v26
v_mul_f32 v14,v14,v26
v_mfma_f32_16x16x32_bf16 v[84:87],a[152:155],v[176:179],v[84:87]
v_mul_f32 v15,v15,v26
v_cvt_pk_bf16_f32 v8,v8,v9
v_mfma_f32_16x16x32_bf16 v[84:87],a[148:151],v[168:171],v[84:87]
v_cvt_pk_bf16_f32 v9,v10,v11
v_cvt_pk_bf16_f32 v10,v12,v13
v_mfma_f32_16x16x32_bf16 v[84:87],a[144:147],v[160:163],v[84:87]
v_cvt_pk_bf16_f32 v11,v14,v15
v_accvgpr_write_b32 a64,v8
v_mfma_f32_16x16x32_bf16 v[88:91],a[160:163],v[160:163],v[36:39]
v_accvgpr_write_b32 a65,v9
v_accvgpr_write_b32 a66,v10
v_mfma_f32_16x16x32_bf16 v[88:91],a[164:167],v[168:171],v[88:91]
ds_read_b128 a[68:71],v7 offset:6144
v_accvgpr_write_b32 a67,v11
v_mfma_f32_16x16x32_bf16 v[88:91],a[168:171],v[176:179],v[88:91]
v_add_u32 v2,s78,v2
v_add_u32 v3,s78,v3
v_mfma_f32_16x16x32_bf16 v[88:91],a[172:175],v[184:187],v[88:91]
s_sub_u32 s78,s77,32768
v_exp_f32 v64,v64
v_mfma_f32_16x16x32_bf16 v[92:95],a[188:191],v[184:187],v[36:39]
s_and_b32 s78,s78,0x1ffff
v_exp_f32 v65,v65
v_mfma_f32_16x16x32_bf16 v[92:95],a[184:187],v[176:179],v[92:95]
s_sub_u32 s78,s78,s77
v_exp_f32 v66,v66
v_mfma_f32_16x16x32_bf16 v[92:95],a[180:183],v[168:171],v[92:95]
global_load_lds_dwordx4 v5,s[10:11] offset:1024
v_cvt_pk_bf16_f32 v64,v64,v65
v_mfma_f32_16x16x32_bf16 v[92:95],a[176:179],v[160:163],v[92:95]
s_add_u32 s77,s77,s78
v_exp_f32 v67,v67
v_mfma_f32_16x16x32_bf16 a[32:35],a[192:195],v[112:115],a[32:35]
ds_read_b64_tr_b16 a[192:193],v2
ds_read_b64_tr_b16 a[194:195],v2 offset:4096
v_mfma_f32_16x16x32_bf16 a[32:35],a[196:199],v[120:123],a[32:35]
ds_read_b64_tr_b16 a[196:197],v2 offset:8192
v_cvt_pk_bf16_f32 v65,v66,v67
v_mfma_f32_16x16x32_bf16 a[36:39],a[200:203],v[112:115],a[36:39]
ds_read_b64_tr_b16 a[198:199],v2 offset:12288
v_accvgpr_read_b32 v20,a76
v_mfma_f32_16x16x32_bf16 a[36:39],a[204:207],v[120:123],a[36:39]
ds_read_b64_tr_b16 a[200:201],v3
v_accvgpr_read_b32 v21,a77
v_mfma_f32_16x16x32_bf16 a[40:43],a[208:211],v[112:115],a[40:43]
ds_read_b64_tr_b16 a[202:203],v3 offset:4096
ds_read_b64_tr_b16 a[204:205],v3 offset:8192
v_mfma_f32_16x16x32_bf16 a[40:43],a[212:215],v[120:123],a[40:43]
ds_read_b64_tr_b16 a[206:207],v3 offset:12288
v_accvgpr_read_b32 v22,a78
v_mfma_f32_16x16x32_bf16 a[44:47],a[216:219],v[112:115],a[44:47]
ds_read_b64_tr_b16 a[208:209],v2 offset:256
v_accvgpr_read_b32 v23,a79
v_mfma_f32_16x16x32_bf16 a[44:47],a[220:223],v[120:123],a[44:47]
ds_read_b64_tr_b16 a[210:211],v2 offset:4352
v_accvgpr_read_b32 v16,a72
v_mfma_f32_16x16x32_bf16 a[48:51],a[224:227],v[112:115],a[48:51]
ds_read_b64_tr_b16 a[212:213],v2 offset:8448
ds_read_b64_tr_b16 a[214:215],v2 offset:12544
v_mfma_f32_16x16x32_bf16 a[48:51],a[228:231],v[120:123],a[48:51]
v_exp_f32 v68,v68
v_mfma_f32_16x16x32_bf16 a[52:55],a[232:235],v[112:115],a[52:55]
v_exp_f32 v69,v69
v_mfma_f32_16x16x32_bf16 a[52:55],a[236:239],v[120:123],a[52:55]
v_exp_f32 v70,v70
v_mfma_f32_16x16x32_bf16 a[56:59],a[240:243],v[112:115],a[56:59]
v_exp_f32 v71,v71
v_mfma_f32_16x16x32_bf16 a[56:59],a[244:247],v[120:123],a[56:59]
ds_read_b64_tr_b16 a[216:217],v3 offset:256
ds_read_b64_tr_b16 a[218:219],v3 offset:4352
v_mfma_f32_16x16x32_bf16 a[60:63],a[248:251],v[112:115],a[60:63]
global_load_lds_dwordx4 v4,s[10:11] offset:2048
v_cvt_pk_bf16_f32 v66,v68,v69
v_mfma_f32_16x16x32_bf16 a[60:63],a[252:255],v[120:123],a[60:63]
ds_read_b64_tr_b16 a[220:221],v3 offset:8448
v_cvt_pk_bf16_f32 v67,v70,v71
v_mfma_f32_16x16x32_bf16 v[60:63],v[28:31],v[112:115],v[60:63]
v_exp_f32 v72,v72
v_mfma_f32_16x16x32_bf16 v[60:63],v[28:31],v[120:123],v[60:63]
v_exp_f32 v73,v73
v_mfma_f32_16x16x32_bf16 v[96:99],a[128:131],v[192:195],v[40:43]
v_exp_f32 v74,v74
v_mfma_f32_16x16x32_bf16 v[96:99],a[132:135],v[200:203],v[96:99]
v_exp_f32 v75,v75
v_mfma_f32_16x16x32_bf16 v[96:99],a[136:139],v[208:211],v[96:99]
v_exp_f32 v76,v76
v_mfma_f32_16x16x32_bf16 v[96:99],a[140:143],v[216:219],v[96:99]
v_exp_f32 v77,v77
v_mfma_f32_16x16x32_bf16 v[100:103],a[156:159],v[216:219],v[40:43]
v_exp_f32 v78,v78
v_mfma_f32_16x16x32_bf16 v[100:103],a[152:155],v[208:211],v[100:103]
v_exp_f32 v79,v79
v_mfma_f32_16x16x32_bf16 v[100:103],a[148:151],v[200:203],v[100:103]
ds_read_b64_tr_b16 a[222:223],v3 offset:12544
v_cvt_pk_bf16_f32 v72,v72,v73
v_mfma_f32_16x16x32_bf16 v[100:103],a[144:147],v[192:195],v[100:103]
v_cvt_pk_bf16_f32 v73,v74,v75
v_cvt_pk_bf16_f32 v74,v76,v77
v_mfma_f32_16x16x32_bf16 v[104:107],a[160:163],v[192:195],v[40:43]
ds_read_b64_tr_b16 a[224:225],v2 offset:512
v_cvt_pk_bf16_f32 v75,v78,v79
v_mfma_f32_16x16x32_bf16 v[104:107],a[164:167],v[200:203],v[104:107]
ds_read_b64_tr_b16 a[226:227],v2 offset:4608
ds_read_b64_tr_b16 a[228:229],v2 offset:8704
v_mfma_f32_16x16x32_bf16 v[104:107],a[168:171],v[208:211],v[104:107]
ds_read_b64_tr_b16 a[230:231],v2 offset:12800
v_accvgpr_read_b32 v17,a73
v_mfma_f32_16x16x32_bf16 v[104:107],a[172:175],v[216:219],v[104:107]
ds_read_b64_tr_b16 a[232:233],v3 offset:512
v_accvgpr_read_b32 v18,a74
v_mfma_f32_16x16x32_bf16 v[108:111],a[188:191],v[216:219],v[40:43]
global_load_lds_dwordx4 v5,s[10:11] offset:3072
ds_read_b64_tr_b16 a[234:235],v3 offset:4608
v_mfma_f32_16x16x32_bf16 v[108:111],a[184:187],v[208:211],v[108:111]
ds_read_b64_tr_b16 a[236:237],v3 offset:8704
ds_read_b64_tr_b16 a[238:239],v3 offset:12800
v_mfma_f32_16x16x32_bf16 v[108:111],a[180:183],v[200:203],v[108:111]
ds_read_b64_tr_b16 a[240:241],v2 offset:768
v_accvgpr_read_b32 v19,a75
v_mfma_f32_16x16x32_bf16 v[108:111],a[176:179],v[192:195],v[108:111]
s_waitcnt lgkmcnt(9)
ds_read_b64_tr_b16 a[242:243],v2 offset:4864
v_mul_f32 v16,v16,v26
v_mfma_f32_16x16x32_bf16 a[0:3],a[192:195],v[64:67],a[0:3]
ds_read_b64_tr_b16 a[244:245],v2 offset:8960
v_mul_f32 v17,v17,v26
v_mfma_f32_16x16x32_bf16 a[0:3],a[196:199],v[72:75],a[0:3]
ds_read_b64_tr_b16 a[246:247],v2 offset:13056
ds_read_b64_tr_b16 a[248:249],v3 offset:768
v_mfma_f32_16x16x32_bf16 a[4:7],a[200:203],v[64:67],a[4:7]
ds_read_b64_tr_b16 a[250:251],v3 offset:4864
v_mul_f32 v18,v18,v26
v_mfma_f32_16x16x32_bf16 a[4:7],a[204:207],v[72:75],a[4:7]
ds_read_b64_tr_b16 a[252:253],v3 offset:8960
v_mul_f32 v19,v19,v26
v_mfma_f32_16x16x32_bf16 a[8:11],a[208:211],v[64:67],a[8:11]
ds_read_b64_tr_b16 a[254:255],v3 offset:13056
v_mul_f32 v20,v20,v26
v_mfma_f32_16x16x32_bf16 a[8:11],a[212:215],v[72:75],a[8:11]
v_exp_f32 v80,v80
v_mfma_f32_16x16x32_bf16 a[12:15],a[216:219],v[64:67],a[12:15]
v_exp_f32 v81,v81
v_mfma_f32_16x16x32_bf16 a[12:15],a[220:223],v[72:75],a[12:15]
s_waitcnt lgkmcnt(6)
v_exp_f32 v82,v82
v_mfma_f32_16x16x32_bf16 a[16:19],a[224:227],v[64:67],a[16:19]
v_exp_f32 v83,v83
v_mfma_f32_16x16x32_bf16 a[16:19],a[228:231],v[72:75],a[16:19]
v_exp_f32 v84,v84
v_mfma_f32_16x16x32_bf16 a[20:23],a[232:235],v[64:67],a[20:23]
s_getreg_b32 s92,hwreg(HW_REG_IB_STS,0,4)
s_min_u32 s4,s92,s6
s_mov_b32 s6,s92
v_mfma_f32_16x16x32_bf16 a[20:23],a[236:239],v[72:75],a[20:23]
s_cmp_ge_u32 s4,s9
s_cselect_b32 s4,15,s5
v_cvt_pk_bf16_f32 v80,v80,v81
v_mfma_f32_16x16x32_bf16 a[24:27],a[240:243],v[64:67],a[24:27]
s_waitcnt lgkmcnt(0)
v_exp_f32 v85,v85
v_mfma_f32_16x16x32_bf16 a[24:27],a[244:247],v[72:75],a[24:27]
v_exp_f32 v86,v86
v_mfma_f32_16x16x32_bf16 a[28:31],a[248:251],v[64:67],a[28:31]
v_exp_f32 v87,v87
v_mfma_f32_16x16x32_bf16 a[28:31],a[252:255],v[72:75],a[28:31]
s_getpc_b64 s[92:93]
s_cmp_lg_u32 s4,0
s_cbranch_scc1 L_pace_%=
v_mfma_f32_16x16x32_bf16 v[48:51],v[28:31],v[64:67],v[48:51]
v_cvt_pk_bf16_f32 v81,v82,v83
v_cvt_pk_bf16_f32 v82,v84,v85
v_mfma_f32_16x16x32_bf16 v[48:51],v[28:31],v[72:75],v[48:51]
s_add_u32 s10,s20,s74
s_addc_u32 s11,s21,0
v_cvt_pk_bf16_f32 v83,v86,v87
s_waitcnt vmcnt(16)
s_barrier
v_mfma_f32_16x16x32_bf16 v[112:115],a[128:131],v[224:227],v[44:47]
s_add_u32 m0,s76,0
v_exp_f32 v88,v88
v_mfma_f32_16x16x32_bf16 v[112:115],a[132:135],v[232:235],v[112:115]
v_exp_f32 v89,v89
v_mfma_f32_16x16x32_bf16 v[112:115],a[136:139],v[240:243],v[112:115]
global_load_lds_dwordx4 v4,s[10:11] nt
v_cvt_pk_bf16_f32 v88,v88,v89
v_mfma_f32_16x16x32_bf16 v[112:115],a[140:143],v[248:251],v[112:115]
v_exp_f32 v90,v90
v_mfma_f32_16x16x32_bf16 v[116:119],a[156:159],v[248:251],v[44:47]
v_exp_f32 v91,v91
v_mfma_f32_16x16x32_bf16 v[116:119],a[152:155],v[240:243],v[116:119]
v_exp_f32 v92,v92
v_mfma_f32_16x16x32_bf16 v[116:119],a[148:151],v[232:235],v[116:119]
v_exp_f32 v93,v93
v_mfma_f32_16x16x32_bf16 v[116:119],a[144:147],v[224:227],v[116:119]
v_exp_f32 v94,v94
v_mfma_f32_16x16x32_bf16 v[120:123],a[160:163],v[224:227],v[44:47]
v_exp_f32 v95,v95
v_mfma_f32_16x16x32_bf16 v[120:123],a[164:167],v[232:235],v[120:123]
v_cvt_pk_bf16_f32 v89,v90,v91
v_cvt_pk_bf16_f32 v90,v92,v93
v_mfma_f32_16x16x32_bf16 v[120:123],a[168:171],v[240:243],v[120:123]
v_cvt_pk_bf16_f32 v91,v94,v95
v_mul_f32 v21,v21,v26
v_mfma_f32_16x16x32_bf16 v[120:123],a[172:175],v[248:251],v[120:123]
v_mul_f32 v22,v22,v26
v_mul_f32 v23,v23,v26
v_mfma_f32_16x16x32_bf16 v[124:127],a[188:191],v[248:251],v[44:47]
v_cvt_pk_bf16_f32 v16,v16,v17
v_cvt_pk_bf16_f32 v17,v18,v19
v_mfma_f32_16x16x32_bf16 v[124:127],a[184:187],v[240:243],v[124:127]
v_cvt_pk_bf16_f32 v18,v20,v21
v_cvt_pk_bf16_f32 v19,v22,v23
v_mfma_f32_16x16x32_bf16 v[124:127],a[180:183],v[232:235],v[124:127]
v_accvgpr_write_b32 a72,v16
v_accvgpr_write_b32 a73,v17
v_mfma_f32_16x16x32_bf16 v[124:127],a[176:179],v[224:227],v[124:127]
v_accvgpr_write_b32 a74,v18
v_accvgpr_write_b32 a75,v19
v_mfma_f32_16x16x32_bf16 v[132:135],a[192:195],v[80:83],v[132:135]
ds_read_b128 a[76:79],v7 offset:7168
v_add_u32 v1,s78,v1
v_mfma_f32_16x16x32_bf16 v[132:135],a[196:199],v[88:91],v[132:135]
v_exp_f32 v96,v96
v_mfma_f32_16x16x32_bf16 v[140:143],a[200:203],v[80:83],v[140:143]
global_load_lds_dwordx4 v4,s[10:11] offset:1024 nt
ds_read_b128 a[128:131],v1
v_mfma_f32_16x16x32_bf16 v[140:143],a[204:207],v[88:91],v[140:143]
v_exp_f32 v97,v97
v_mfma_f32_16x16x32_bf16 v[148:151],a[208:211],v[80:83],v[148:151]
ds_read_b128 a[132:135],v1 offset:256
v_cvt_pk_bf16_f32 v96,v96,v97
v_mfma_f32_16x16x32_bf16 v[148:151],a[212:215],v[88:91],v[148:151]
v_exp_f32 v98,v98
v_mfma_f32_16x16x32_bf16 v[156:159],a[216:219],v[80:83],v[156:159]
ds_read_b128 a[136:139],v1 offset:512
v_accvgpr_read_b32 v12,a84
v_mfma_f32_16x16x32_bf16 v[156:159],a[220:223],v[88:91],v[156:159]
v_exp_f32 v99,v99
v_mfma_f32_16x16x32_bf16 v[164:167],a[224:227],v[80:83],v[164:167]
ds_read_b128 a[140:143],v1 offset:768
v_cvt_pk_bf16_f32 v97,v98,v99
v_mfma_f32_16x16x32_bf16 v[164:167],a[228:231],v[88:91],v[164:167]
v_exp_f32 v100,v100
v_mfma_f32_16x16x32_bf16 v[172:175],a[232:235],v[80:83],v[172:175]
)A"
R"A(ds_read_b128 a[156:159],v1 offset:4864
v_accvgpr_read_b32 v13,a85
v_mfma_f32_16x16x32_bf16 v[172:175],a[236:239],v[88:91],v[172:175]
v_exp_f32 v101,v101
v_mfma_f32_16x16x32_bf16 v[180:183],a[240:243],v[80:83],v[180:183]
ds_read_b128 a[152:155],v1 offset:4608
v_cvt_pk_bf16_f32 v98,v100,v101
v_mfma_f32_16x16x32_bf16 v[180:183],a[244:247],v[88:91],v[180:183]
v_exp_f32 v102,v102
v_mfma_f32_16x16x32_bf16 v[188:191],a[248:251],v[80:83],v[188:191]
ds_read_b128 a[148:151],v1 offset:4352
v_accvgpr_read_b32 v14,a86
v_mfma_f32_16x16x32_bf16 v[188:191],a[252:255],v[88:91],v[188:191]
v_exp_f32 v103,v103
v_mfma_f32_16x16x32_bf16 v[52:55],v[28:31],v[80:83],v[52:55]
ds_read_b128 a[144:147],v1 offset:4096
v_cvt_pk_bf16_f32 v99,v102,v103
v_mfma_f32_16x16x32_bf16 v[52:55],v[28:31],v[88:91],v[52:55]
s_waitcnt lgkmcnt(4)
v_exp_f32 v104,v104
v_mfma_f32_16x16x32_bf16 v[64:67],a[128:131],v[128:131],v[32:35]
global_load_lds_dwordx4 v5,s[10:11] offset:2048 nt
ds_read_b128 a[160:163],v1 offset:8192
v_mfma_f32_16x16x32_bf16 v[64:67],a[132:135],v[136:139],v[64:67]
v_exp_f32 v105,v105
v_mfma_f32_16x16x32_bf16 v[64:67],a[136:139],v[144:147],v[64:67]
ds_read_b128 a[164:167],v1 offset:8448
v_cvt_pk_bf16_f32 v104,v104,v105
v_mfma_f32_16x16x32_bf16 v[64:67],a[140:143],v[152:155],v[64:67]
s_waitcnt lgkmcnt(4)
v_exp_f32 v106,v106
v_mfma_f32_16x16x32_bf16 v[68:71],a[156:159],v[152:155],v[32:35]
ds_read_b128 a[168:171],v1 offset:8704
v_accvgpr_read_b32 v15,a87
v_mfma_f32_16x16x32_bf16 v[68:71],a[152:155],v[144:147],v[68:71]
s_waitcnt lgkmcnt(4)
v_exp_f32 v107,v107
v_mfma_f32_16x16x32_bf16 v[68:71],a[148:151],v[136:139],v[68:71]
s_waitcnt lgkmcnt(3)
ds_read_b128 a[172:175],v1 offset:8960
v_cvt_pk_bf16_f32 v105,v106,v107
v_mfma_f32_16x16x32_bf16 v[68:71],a[144:147],v[128:131],v[68:71]
s_waitcnt lgkmcnt(3)
v_exp_f32 v108,v108
v_mfma_f32_16x16x32_bf16 v[72:75],a[160:163],v[128:131],v[32:35]
s_waitcnt lgkmcnt(2)
ds_read_b128 a[188:191],v1 offset:13056
v_accvgpr_read_b32 v8,a80
v_mfma_f32_16x16x32_bf16 v[72:75],a[164:167],v[136:139],v[72:75]
s_waitcnt lgkmcnt(2)
ds_read_b128 a[184:187],v1 offset:12800
v_accvgpr_read_b32 v9,a81
v_mfma_f32_16x16x32_bf16 v[72:75],a[168:171],v[144:147],v[72:75]
s_waitcnt lgkmcnt(2)
ds_read_b128 a[180:183],v1 offset:12544
v_accvgpr_read_b32 v10,a82
v_mfma_f32_16x16x32_bf16 v[72:75],a[172:175],v[152:155],v[72:75]
s_waitcnt lgkmcnt(2)
ds_read_b128 a[176:179],v1 offset:12288
v_accvgpr_read_b32 v11,a83
v_mfma_f32_16x16x32_bf16 v[76:79],a[188:191],v[152:155],v[32:35]
s_waitcnt lgkmcnt(2)
v_exp_f32 v109,v109
v_mfma_f32_16x16x32_bf16 v[76:79],a[184:187],v[144:147],v[76:79]
s_waitcnt lgkmcnt(1)
v_exp_f32 v110,v110
v_exp_f32 v111,v111
v_mfma_f32_16x16x32_bf16 v[76:79],a[180:183],v[136:139],v[76:79]
s_waitcnt lgkmcnt(0)
v_cvt_pk_bf16_f32 v106,v108,v109
v_cvt_pk_bf16_f32 v107,v110,v111
v_mfma_f32_16x16x32_bf16 v[76:79],a[176:179],v[128:131],v[76:79]
v_mul_f32 v8,v8,v26
v_mul_f32 v9,v9,v26
v_mfma_f32_16x16x32_bf16 v[196:199],a[192:195],v[96:99],v[196:199]
global_load_lds_dwordx4 v5,s[10:11] offset:3072
v_mul_f32 v10,v10,v26
v_mfma_f32_16x16x32_bf16 v[196:199],a[196:199],v[104:107],v[196:199]
v_mul_f32 v11,v11,v26
v_mul_f32 v12,v12,v26
v_mfma_f32_16x16x32_bf16 v[204:207],a[200:203],v[96:99],v[204:207]
v_mul_f32 v13,v13,v26
v_mul_f32 v14,v14,v26
v_mfma_f32_16x16x32_bf16 v[204:207],a[204:207],v[104:107],v[204:207]
v_mul_f32 v15,v15,v26
v_cvt_pk_bf16_f32 v8,v8,v9
v_mfma_f32_16x16x32_bf16 v[212:215],a[208:211],v[96:99],v[212:215]
v_cvt_pk_bf16_f32 v9,v10,v11
v_cvt_pk_bf16_f32 v10,v12,v13
v_mfma_f32_16x16x32_bf16 v[212:215],a[212:215],v[104:107],v[212:215]
v_cvt_pk_bf16_f32 v11,v14,v15
v_accvgpr_write_b32 a80,v8
v_mfma_f32_16x16x32_bf16 v[220:223],a[216:219],v[96:99],v[220:223]
v_accvgpr_write_b32 a81,v9
v_accvgpr_write_b32 a82,v10
v_mfma_f32_16x16x32_bf16 v[220:223],a[220:223],v[104:107],v[220:223]
ds_read_b128 a[84:87],v7
v_accvgpr_write_b32 a83,v11
v_mfma_f32_16x16x32_bf16 v[228:231],a[224:227],v[96:99],v[228:231]
v_exp_f32 v112,v112
v_mfma_f32_16x16x32_bf16 v[228:231],a[228:231],v[104:107],v[228:231]
v_exp_f32 v113,v113
v_mfma_f32_16x16x32_bf16 v[236:239],a[232:235],v[96:99],v[236:239]
v_exp_f32 v114,v114
v_mfma_f32_16x16x32_bf16 v[236:239],a[236:239],v[104:107],v[236:239]
v_exp_f32 v115,v115
v_mfma_f32_16x16x32_bf16 v[244:247],a[240:243],v[96:99],v[244:247]
s_add_u32 s10,s24,s74
s_addc_u32 s11,s25,0
v_cvt_pk_bf16_f32 v112,v112,v113
v_mfma_f32_16x16x32_bf16 v[244:247],a[244:247],v[104:107],v[244:247]
s_add_u32 s74,s74,16384
v_exp_f32 v116,v116
v_mfma_f32_16x16x32_bf16 v[252:255],a[248:251],v[96:99],v[252:255]
s_add_u32 m0,s76,16384
v_exp_f32 v117,v117
v_mfma_f32_16x16x32_bf16 v[252:255],a[252:255],v[104:107],v[252:255]
s_and_b32 s74,s74,0x3ffff
v_exp_f32 v118,v118
v_mfma_f32_16x16x32_bf16 v[56:59],v[28:31],v[96:99],v[56:59]
global_load_lds_dwordx4 v4,s[10:11]
v_cvt_pk_bf16_f32 v113,v114,v115
v_mfma_f32_16x16x32_bf16 v[56:59],v[28:31],v[104:107],v[56:59]
s_or_b32 s74,s74,s69
v_exp_f32 v119,v119
v_mfma_f32_16x16x32_bf16 v[80:83],a[128:131],v[160:163],v[36:39]
s_sub_u32 s76,s76,32768
v_cvt_pk_bf16_f32 v114,v116,v117
v_cvt_pk_bf16_f32 v115,v118,v119
v_mfma_f32_16x16x32_bf16 v[80:83],a[132:135],v[168:171],v[80:83]
s_and_b32 s76,s76,0x1ffff
v_exp_f32 v120,v120
v_mfma_f32_16x16x32_bf16 v[80:83],a[136:139],v[176:179],v[80:83]
v_exp_f32 v121,v121
v_mfma_f32_16x16x32_bf16 v[80:83],a[140:143],v[184:187],v[80:83]
v_exp_f32 v122,v122
v_mfma_f32_16x16x32_bf16 v[84:87],a[156:159],v[184:187],v[36:39]
v_exp_f32 v123,v123
v_mfma_f32_16x16x32_bf16 v[84:87],a[152:155],v[176:179],v[84:87]
v_exp_f32 v124,v124
v_mfma_f32_16x16x32_bf16 v[84:87],a[148:151],v[168:171],v[84:87]
v_exp_f32 v125,v125
v_mfma_f32_16x16x32_bf16 v[84:87],a[144:147],v[160:163],v[84:87]
v_exp_f32 v126,v126
v_mfma_f32_16x16x32_bf16 v[88:91],a[160:163],v[160:163],v[36:39]
v_exp_f32 v127,v127
v_mfma_f32_16x16x32_bf16 v[88:91],a[164:167],v[168:171],v[88:91]
v_cvt_pk_bf16_f32 v120,v120,v121
v_cvt_pk_bf16_f32 v121,v122,v123
v_mfma_f32_16x16x32_bf16 v[88:91],a[168:171],v[176:179],v[88:91]
v_cvt_pk_bf16_f32 v122,v124,v125
v_cvt_pk_bf16_f32 v123,v126,v127
v_mfma_f32_16x16x32_bf16 v[88:91],a[172:175],v[184:187],v[88:91]
v_add_u32 v2,s78,v2
v_add_u32 v3,s78,v3
v_mfma_f32_16x16x32_bf16 v[92:95],a[188:191],v[184:187],v[36:39]
s_sub_u32 s78,s77,32768
v_exp_f32 v64,v64
v_mfma_f32_16x16x32_bf16 v[92:95],a[184:187],v[176:179],v[92:95]
s_and_b32 s78,s78,0x1ffff
v_exp_f32 v65,v65
v_mfma_f32_16x16x32_bf16 v[92:95],a[180:183],v[168:171],v[92:95]
global_load_lds_dwordx4 v5,s[10:11] offset:1024
v_cvt_pk_bf16_f32 v64,v64,v65
v_mfma_f32_16x16x32_bf16 v[92:95],a[176:179],v[160:163],v[92:95]
s_sub_u32 s78,s78,s77
v_exp_f32 v66,v66
v_mfma_f32_16x16x32_bf16 a[32:35],a[192:195],v[112:115],a[32:35]
s_add_u32 s77,s77,s78
ds_read_b64_tr_b16 a[192:193],v2
ds_read_b64_tr_b16 a[194:195],v2 offset:4096
v_mfma_f32_16x16x32_bf16 a[32:35],a[196:199],v[120:123],a[32:35]
ds_read_b64_tr_b16 a[196:197],v2 offset:8192
v_accvgpr_read_b32 v20,a92
v_mfma_f32_16x16x32_bf16 a[36:39],a[200:203],v[112:115],a[36:39]
ds_read_b64_tr_b16 a[198:199],v2 offset:12288
v_accvgpr_read_b32 v21,a93
v_mfma_f32_16x16x32_bf16 a[36:39],a[204:207],v[120:123],a[36:39]
ds_read_b64_tr_b16 a[200:201],v3
v_accvgpr_read_b32 v22,a94
v_mfma_f32_16x16x32_bf16 a[40:43],a[208:211],v[112:115],a[40:43]
ds_read_b64_tr_b16 a[202:203],v3 offset:4096
ds_read_b64_tr_b16 a[204:205],v3 offset:8192
v_mfma_f32_16x16x32_bf16 a[40:43],a[212:215],v[120:123],a[40:43]
ds_read_b64_tr_b16 a[206:207],v3 offset:12288
v_accvgpr_read_b32 v23,a95
v_mfma_f32_16x16x32_bf16 a[44:47],a[216:219],v[112:115],a[44:47]
ds_read_b64_tr_b16 a[208:209],v2 offset:256
v_accvgpr_read_b32 v16,a88
v_mfma_f32_16x16x32_bf16 a[44:47],a[220:223],v[120:123],a[44:47]
ds_read_b64_tr_b16 a[210:211],v2 offset:4352
v_accvgpr_read_b32 v17,a89
v_mfma_f32_16x16x32_bf16 a[48:51],a[224:227],v[112:115],a[48:51]
ds_read_b64_tr_b16 a[212:213],v2 offset:8448
ds_read_b64_tr_b16 a[214:215],v2 offset:12544
v_mfma_f32_16x16x32_bf16 a[48:51],a[228:231],v[120:123],a[48:51]
v_exp_f32 v67,v67
v_mfma_f32_16x16x32_bf16 a[52:55],a[232:235],v[112:115],a[52:55]
v_exp_f32 v68,v68
v_mfma_f32_16x16x32_bf16 a[52:55],a[236:239],v[120:123],a[52:55]
v_exp_f32 v69,v69
v_mfma_f32_16x16x32_bf16 a[56:59],a[240:243],v[112:115],a[56:59]
v_exp_f32 v70,v70
v_mfma_f32_16x16x32_bf16 a[56:59],a[244:247],v[120:123],a[56:59]
v_exp_f32 v71,v71
v_mfma_f32_16x16x32_bf16 a[60:63],a[248:251],v[112:115],a[60:63]
global_load_lds_dwordx4 v4,s[10:11] offset:2048
ds_read_b64_tr_b16 a[216:217],v3 offset:256
v_mfma_f32_16x16x32_bf16 a[60:63],a[252:255],v[120:123],a[60:63]
ds_read_b64_tr_b16 a[218:219],v3 offset:4352
v_cvt_pk_bf16_f32 v65,v66,v67
v_mfma_f32_16x16x32_bf16 v[60:63],v[28:31],v[112:115],v[60:63]
v_cvt_pk_bf16_f32 v66,v68,v69
v_cvt_pk_bf16_f32 v67,v70,v71
v_mfma_f32_16x16x32_bf16 v[60:63],v[28:31],v[120:123],v[60:63]
v_exp_f32 v72,v72
v_mfma_f32_16x16x32_bf16 v[96:99],a[128:131],v[192:195],v[40:43]
v_exp_f32 v73,v73
v_mfma_f32_16x16x32_bf16 v[96:99],a[132:135],v[200:203],v[96:99]
v_exp_f32 v74,v74
v_mfma_f32_16x16x32_bf16 v[96:99],a[136:139],v[208:211],v[96:99]
v_exp_f32 v75,v75
v_mfma_f32_16x16x32_bf16 v[96:99],a[140:143],v[216:219],v[96:99]
v_exp_f32 v76,v76
v_mfma_f32_16x16x32_bf16 v[100:103],a[156:159],v[216:219],v[40:43]
v_exp_f32 v77,v77
v_mfma_f32_16x16x32_bf16 v[100:103],a[152:155],v[208:211],v[100:103]
v_exp_f32 v78,v78
s_nop 0
L_pd10_%=:
s_cbranch_execz L_t1b_%=
v_mfma_f32_16x16x32_bf16 v[100:103],a[148:151],v[200:203],v[100:103]
v_exp_f32 v79,v79
v_mfma_f32_16x16x32_bf16 v[100:103],a[144:147],v[192:195],v[100:103]
ds_read_b64_tr_b16 a[220:221],v3 offset:8448
ds_read_b64_tr_b16 a[222:223],v3 offset:12544
v_mfma_f32_16x16x32_bf16 v[104:107],a[160:163],v[192:195],v[40:43]
v_cvt_pk_bf16_f32 v72,v72,v73
v_cvt_pk_bf16_f32 v73,v74,v75
v_mfma_f32_16x16x32_bf16 v[104:107],a[164:167],v[200:203],v[104:107]
v_cvt_pk_bf16_f32 v74,v76,v77
v_cvt_pk_bf16_f32 v75,v78,v79
v_mfma_f32_16x16x32_bf16 v[104:107],a[168:171],v[208:211],v[104:107]
ds_read_b64_tr_b16 a[224:225],v2 offset:512
ds_read_b64_tr_b16 a[226:227],v2 offset:4608
v_mfma_f32_16x16x32_bf16 v[104:107],a[172:175],v[216:219],v[104:107]
ds_read_b64_tr_b16 a[228:229],v2 offset:8704
v_accvgpr_read_b32 v18,a90
v_mfma_f32_16x16x32_bf16 v[108:111],a[188:191],v[216:219],v[40:43]
global_load_lds_dwordx4 v5,s[10:11] offset:3072
ds_read_b64_tr_b16 a[230:231],v2 offset:12800
v_mfma_f32_16x16x32_bf16 v[108:111],a[184:187],v[208:211],v[108:111]
ds_read_b64_tr_b16 a[232:233],v3 offset:512
v_accvgpr_read_b32 v19,a91
v_mfma_f32_16x16x32_bf16 v[108:111],a[180:183],v[200:203],v[108:111]
ds_read_b64_tr_b16 a[234:235],v3 offset:4608
ds_read_b64_tr_b16 a[236:237],v3 offset:8704
v_mfma_f32_16x16x32_bf16 v[108:111],a[176:179],v[192:195],v[108:111]
s_waitcnt lgkmcnt(7)
ds_read_b64_tr_b16 a[238:239],v3 offset:12800
v_mul_f32 v16,v16,v26
v_mfma_f32_16x16x32_bf16 a[0:3],a[192:195],v[64:67],a[0:3]
ds_read_b64_tr_b16 a[240:241],v2 offset:768
v_mul_f32 v17,v17,v26
v_mfma_f32_16x16x32_bf16 a[0:3],a[196:199],v[72:75],a[0:3]
ds_read_b64_tr_b16 a[242:243],v2 offset:4864
v_mul_f32 v18,v18,v26
v_mfma_f32_16x16x32_bf16 a[4:7],a[200:203],v[64:67],a[4:7]
ds_read_b64_tr_b16 a[244:245],v2 offset:8960
ds_read_b64_tr_b16 a[246:247],v2 offset:13056
v_mfma_f32_16x16x32_bf16 a[4:7],a[204:207],v[72:75],a[4:7]
ds_read_b64_tr_b16 a[248:249],v3 offset:768
v_mul_f32 v19,v19,v26
v_mfma_f32_16x16x32_bf16 a[8:11],a[208:211],v[64:67],a[8:11]
ds_read_b64_tr_b16 a[250:251],v3 offset:4864
v_mul_f32 v20,v20,v26
v_mfma_f32_16x16x32_bf16 a[8:11],a[212:215],v[72:75],a[8:11]
v_mul_f32 v21,v21,v26
v_mul_f32 v22,v22,v26
v_mul_f32 v23,v23,v26
v_cvt_pk_bf16_f32 v16,v16,v17
v_cvt_pk_bf16_f32 v17,v18,v19
v_cvt_pk_bf16_f32 v18,v20,v21
v_cvt_pk_bf16_f32 v19,v22,v23
v_accvgpr_write_b32 a88,v16
v_accvgpr_write_b32 a89,v17
v_accvgpr_write_b32 a90,v18
v_accvgpr_write_b32 a91,v19
v_mfma_f32_16x16x32_bf16 a[12:15],a[216:219],v[64:67],a[12:15]
ds_read_b128 a[92:95],v7 offset:1024
v_mov_b32 v8,v7
v_mfma_f32_16x16x32_bf16 a[12:15],a[220:223],v[72:75],a[12:15]
)A"
R"A(s_waitcnt lgkmcnt(7)
ds_read_b64_tr_b16 a[252:253],v3 offset:8960
v_add_u32 v1,s78,v1
v_mfma_f32_16x16x32_bf16 a[16:19],a[224:227],v[64:67],a[16:19]
ds_read_b64_tr_b16 a[254:255],v3 offset:13056
v_mfma_f32_16x16x32_bf16 a[16:19],a[228:231],v[72:75],a[16:19]
s_waitcnt lgkmcnt(4)
v_exp_f32 v80,v80
v_mfma_f32_16x16x32_bf16 a[20:23],a[232:235],v[64:67],a[20:23]
s_getreg_b32 s92,hwreg(HW_REG_IB_STS,0,4)
s_min_u32 s4,s92,s6
s_mov_b32 s6,s92
v_mfma_f32_16x16x32_bf16 a[20:23],a[236:239],v[72:75],a[20:23]
s_cmp_ge_u32 s4,s9
s_cselect_b32 s4,15,s5
ds_read_b128 v[24:27],v8 offset:4096
v_mfma_f32_16x16x32_bf16 a[24:27],a[240:243],v[64:67],a[24:27]
v_exp_f32 v81,v81
v_mfma_f32_16x16x32_bf16 a[24:27],a[244:247],v[72:75],a[24:27]
s_waitcnt lgkmcnt(4)
v_exp_f32 v82,v82
v_mfma_f32_16x16x32_bf16 a[28:31],a[248:251],v[64:67],a[28:31]
s_waitcnt lgkmcnt(1)
v_exp_f32 v83,v83
v_mfma_f32_16x16x32_bf16 a[28:31],a[252:255],v[72:75],a[28:31]
v_exp_f32 v84,v84
v_mfma_f32_16x16x32_bf16 v[48:51],v[28:31],v[64:67],v[48:51]
s_getpc_b64 s[92:93]
s_cmp_lg_u32 s4,0
s_cbranch_scc1 L_pace_%=
v_exp_f32 v85,v85
v_exp_f32 v86,v86
v_exp_f32 v87,v87
v_exp_f32 v88,v88
v_exp_f32 v89,v89
v_exp_f32 v90,v90
v_exp_f32 v91,v91
v_exp_f32 v92,v92
v_exp_f32 v93,v93
v_exp_f32 v94,v94
v_exp_f32 v95,v95
v_mfma_f32_16x16x32_bf16 v[48:51],v[28:31],v[72:75],v[48:51]
ds_read_b32 v7,v8 offset:5120
v_cvt_pk_bf16_f32 v80,v80,v81
v_cvt_pk_bf16_f32 v81,v82,v83
v_cvt_pk_bf16_f32 v82,v84,v85
v_cvt_pk_bf16_f32 v83,v86,v87
v_cvt_pk_bf16_f32 v88,v88,v89
v_cvt_pk_bf16_f32 v89,v90,v91
v_cvt_pk_bf16_f32 v90,v92,v93
v_cvt_pk_bf16_f32 v91,v94,v95
s_mov_b32 s70,7
L_a1_%=:
s_waitcnt vmcnt(16)
s_barrier
v_mfma_f32_16x16x32_bf16 v[112:115],a[128:131],v[224:227],v[44:47]
s_add_u32 s10,s20,s74
s_addc_u32 s11,s21,0
s_add_u32 m0,s76,0
v_mfma_f32_16x16x32_bf16 v[112:115],a[132:135],v[232:235],v[112:115]
s_add_u32 s75,s75,16384
s_and_b32 s69,s75,0xfffc0000
ds_read_b128 a[128:131],v1
v_mfma_f32_16x16x32_bf16 v[112:115],a[136:139],v[240:243],v[112:115]
global_load_lds_dwordx4 v4,s[10:11] nt
v_mfma_f32_16x16x32_bf16 v[112:115],a[140:143],v[248:251],v[112:115]
ds_read_b128 a[132:135],v1 offset:256
v_mfma_f32_16x16x32_bf16 v[116:119],a[156:159],v[248:251],v[44:47]
v_mfma_f32_16x16x32_bf16 v[116:119],a[152:155],v[240:243],v[116:119]
ds_read_b128 a[136:139],v1 offset:512
v_mfma_f32_16x16x32_bf16 v[116:119],a[148:151],v[232:235],v[116:119]
v_mfma_f32_16x16x32_bf16 v[116:119],a[144:147],v[224:227],v[116:119]
ds_read_b128 a[140:143],v1 offset:768
v_mfma_f32_16x16x32_bf16 v[120:123],a[160:163],v[224:227],v[44:47]
v_mfma_f32_16x16x32_bf16 v[120:123],a[164:167],v[232:235],v[120:123]
ds_read_b128 a[156:159],v1 offset:4864
v_mfma_f32_16x16x32_bf16 v[120:123],a[168:171],v[240:243],v[120:123]
v_mfma_f32_16x16x32_bf16 v[120:123],a[172:175],v[248:251],v[120:123]
ds_read_b128 a[152:155],v1 offset:4608
v_mfma_f32_16x16x32_bf16 v[124:127],a[188:191],v[248:251],v[44:47]
v_exp_f32 v96,v96
v_mfma_f32_16x16x32_bf16 v[124:127],a[184:187],v[240:243],v[124:127]
ds_read_b128 a[148:151],v1 offset:4352
v_add_u32 v2,s78,v2
v_mfma_f32_16x16x32_bf16 v[124:127],a[180:183],v[232:235],v[124:127]
v_exp_f32 v97,v97
v_mfma_f32_16x16x32_bf16 v[124:127],a[176:179],v[224:227],v[124:127]
ds_read_b128 a[144:147],v1 offset:4096
v_cvt_pk_bf16_f32 v96,v96,v97
v_mfma_f32_16x16x32_bf16 v[132:135],a[192:195],v[80:83],v[132:135]
v_exp_f32 v98,v98
v_mfma_f32_16x16x32_bf16 v[132:135],a[196:199],v[88:91],v[132:135]
ds_read_b128 a[160:163],v1 offset:8192
v_add_u32 v3,s78,v3
v_mfma_f32_16x16x32_bf16 v[140:143],a[200:203],v[80:83],v[140:143]
s_sub_u32 s78,s77,32768
global_load_lds_dwordx4 v4,s[10:11] offset:1024 nt
v_mfma_f32_16x16x32_bf16 v[140:143],a[204:207],v[88:91],v[140:143]
s_and_b32 s78,s78,0x1ffff
v_exp_f32 v99,v99
v_mfma_f32_16x16x32_bf16 v[148:151],a[208:211],v[80:83],v[148:151]
s_sub_u32 s78,s78,s77
ds_read_b128 a[164:167],v1 offset:8448
v_cvt_pk_bf16_f32 v97,v98,v99
v_mfma_f32_16x16x32_bf16 v[148:151],a[212:215],v[88:91],v[148:151]
s_add_u32 s77,s77,s78
v_exp_f32 v100,v100
v_mfma_f32_16x16x32_bf16 v[156:159],a[216:219],v[80:83],v[156:159]
v_exp_f32 v101,v101
v_mfma_f32_16x16x32_bf16 v[156:159],a[220:223],v[88:91],v[156:159]
ds_read_b128 a[168:171],v1 offset:8704
v_cvt_pk_bf16_f32 v98,v100,v101
v_mfma_f32_16x16x32_bf16 v[164:167],a[224:227],v[80:83],v[164:167]
v_exp_f32 v102,v102
v_mfma_f32_16x16x32_bf16 v[164:167],a[228:231],v[88:91],v[164:167]
v_exp_f32 v103,v103
v_mfma_f32_16x16x32_bf16 v[172:175],a[232:235],v[80:83],v[172:175]
ds_read_b128 a[172:175],v1 offset:8960
v_cvt_pk_bf16_f32 v99,v102,v103
v_mfma_f32_16x16x32_bf16 v[172:175],a[236:239],v[88:91],v[172:175]
v_exp_f32 v104,v104
v_mfma_f32_16x16x32_bf16 v[180:183],a[240:243],v[80:83],v[180:183]
v_exp_f32 v105,v105
v_mfma_f32_16x16x32_bf16 v[180:183],a[244:247],v[88:91],v[180:183]
ds_read_b128 a[188:191],v1 offset:13056
v_cvt_pk_bf16_f32 v104,v104,v105
v_mfma_f32_16x16x32_bf16 v[188:191],a[248:251],v[80:83],v[188:191]
v_exp_f32 v106,v106
v_mfma_f32_16x16x32_bf16 v[188:191],a[252:255],v[88:91],v[188:191]
v_exp_f32 v107,v107
v_mfma_f32_16x16x32_bf16 v[52:55],v[28:31],v[80:83],v[52:55]
ds_read_b128 a[184:187],v1 offset:12800
v_cvt_pk_bf16_f32 v105,v106,v107
v_mfma_f32_16x16x32_bf16 v[52:55],v[28:31],v[88:91],v[52:55]
s_waitcnt lgkmcnt(3)
v_exp_f32 v108,v108
v_mfma_f32_16x16x32_bf16 v[64:67],a[128:131],v[128:131],v[32:35]
global_load_lds_dwordx4 v5,s[10:11] offset:2048 nt
ds_read_b128 a[180:183],v1 offset:12544
v_mfma_f32_16x16x32_bf16 v[64:67],a[132:135],v[136:139],v[64:67]
v_exp_f32 v109,v109
v_mfma_f32_16x16x32_bf16 v[64:67],a[136:139],v[144:147],v[64:67]
ds_read_b128 a[176:179],v1 offset:12288
v_cvt_pk_bf16_f32 v106,v108,v109
v_mfma_f32_16x16x32_bf16 v[64:67],a[140:143],v[152:155],v[64:67]
v_exp_f32 v110,v110
v_mfma_f32_16x16x32_bf16 v[68:71],a[156:159],v[152:155],v[32:35]
v_exp_f32 v111,v111
v_mfma_f32_16x16x32_bf16 v[68:71],a[152:155],v[144:147],v[68:71]
v_cvt_pk_bf16_f32 v107,v110,v111
v_add_u32 v1,s78,v1
v_mfma_f32_16x16x32_bf16 v[68:71],a[148:151],v[136:139],v[68:71]
v_exp_f32 v112,v112
v_mfma_f32_16x16x32_bf16 v[68:71],a[144:147],v[128:131],v[68:71]
v_exp_f32 v113,v113
v_mfma_f32_16x16x32_bf16 v[72:75],a[160:163],v[128:131],v[32:35]
v_exp_f32 v114,v114
v_mfma_f32_16x16x32_bf16 v[72:75],a[164:167],v[136:139],v[72:75]
v_exp_f32 v115,v115
v_mfma_f32_16x16x32_bf16 v[72:75],a[168:171],v[144:147],v[72:75]
s_waitcnt lgkmcnt(0)
v_exp_f32 v116,v116
v_mfma_f32_16x16x32_bf16 v[72:75],a[172:175],v[152:155],v[72:75]
v_exp_f32 v117,v117
v_mfma_f32_16x16x32_bf16 v[76:79],a[188:191],v[152:155],v[32:35]
v_exp_f32 v118,v118
v_mfma_f32_16x16x32_bf16 v[76:79],a[184:187],v[144:147],v[76:79]
v_exp_f32 v119,v119
v_mfma_f32_16x16x32_bf16 v[76:79],a[180:183],v[136:139],v[76:79]
v_cvt_pk_bf16_f32 v112,v112,v113
v_cvt_pk_bf16_f32 v113,v114,v115
v_mfma_f32_16x16x32_bf16 v[76:79],a[176:179],v[128:131],v[76:79]
v_cvt_pk_bf16_f32 v114,v116,v117
v_cvt_pk_bf16_f32 v115,v118,v119
v_mfma_f32_16x16x32_bf16 v[196:199],a[192:195],v[96:99],v[196:199]
global_load_lds_dwordx4 v5,s[10:11] offset:3072
v_mfma_f32_16x16x32_bf16 v[196:199],a[196:199],v[104:107],v[196:199]
v_exp_f32 v120,v120
v_mfma_f32_16x16x32_bf16 v[204:207],a[200:203],v[96:99],v[204:207]
v_exp_f32 v121,v121
v_mfma_f32_16x16x32_bf16 v[204:207],a[204:207],v[104:107],v[204:207]
v_exp_f32 v122,v122
v_mfma_f32_16x16x32_bf16 v[212:215],a[208:211],v[96:99],v[212:215]
v_exp_f32 v123,v123
v_mfma_f32_16x16x32_bf16 v[212:215],a[212:215],v[104:107],v[212:215]
v_exp_f32 v124,v124
v_mfma_f32_16x16x32_bf16 v[220:223],a[216:219],v[96:99],v[220:223]
v_exp_f32 v125,v125
v_mfma_f32_16x16x32_bf16 v[220:223],a[220:223],v[104:107],v[220:223]
v_exp_f32 v126,v126
v_mfma_f32_16x16x32_bf16 v[228:231],a[224:227],v[96:99],v[228:231]
v_exp_f32 v127,v127
v_mfma_f32_16x16x32_bf16 v[228:231],a[228:231],v[104:107],v[228:231]
v_cvt_pk_bf16_f32 v120,v120,v121
v_cvt_pk_bf16_f32 v121,v122,v123
v_mfma_f32_16x16x32_bf16 v[236:239],a[232:235],v[96:99],v[236:239]
v_cvt_pk_bf16_f32 v122,v124,v125
v_cvt_pk_bf16_f32 v123,v126,v127
v_mfma_f32_16x16x32_bf16 v[236:239],a[236:239],v[104:107],v[236:239]
v_exp_f32 v64,v64
v_mfma_f32_16x16x32_bf16 v[244:247],a[240:243],v[96:99],v[244:247]
s_add_u32 s10,s24,s74
s_addc_u32 s11,s25,0
s_add_u32 s74,s74,16384
v_mfma_f32_16x16x32_bf16 v[244:247],a[244:247],v[104:107],v[244:247]
s_and_b32 s74,s74,0x3ffff
v_exp_f32 v65,v65
v_mfma_f32_16x16x32_bf16 v[252:255],a[248:251],v[96:99],v[252:255]
s_add_u32 m0,s76,16384
v_exp_f32 v66,v66
v_mfma_f32_16x16x32_bf16 v[252:255],a[252:255],v[104:107],v[252:255]
s_or_b32 s74,s74,s69
v_exp_f32 v67,v67
v_mfma_f32_16x16x32_bf16 v[56:59],v[28:31],v[96:99],v[56:59]
global_load_lds_dwordx4 v4,s[10:11]
v_cvt_pk_bf16_f32 v64,v64,v65
v_mfma_f32_16x16x32_bf16 v[56:59],v[28:31],v[104:107],v[56:59]
s_sub_u32 s76,s76,32768
v_exp_f32 v68,v68
v_mfma_f32_16x16x32_bf16 v[80:83],a[128:131],v[160:163],v[36:39]
s_and_b32 s76,s76,0x1ffff
v_exp_f32 v69,v69
v_mfma_f32_16x16x32_bf16 v[80:83],a[132:135],v[168:171],v[80:83]
v_exp_f32 v70,v70
v_mfma_f32_16x16x32_bf16 v[80:83],a[136:139],v[176:179],v[80:83]
v_exp_f32 v71,v71
v_mfma_f32_16x16x32_bf16 v[80:83],a[140:143],v[184:187],v[80:83]
v_cvt_pk_bf16_f32 v65,v66,v67
v_cvt_pk_bf16_f32 v66,v68,v69
v_mfma_f32_16x16x32_bf16 v[84:87],a[156:159],v[184:187],v[36:39]
v_exp_f32 v72,v72
v_mfma_f32_16x16x32_bf16 v[84:87],a[152:155],v[176:179],v[84:87]
v_exp_f32 v73,v73
v_mfma_f32_16x16x32_bf16 v[84:87],a[148:151],v[168:171],v[84:87]
v_cvt_pk_bf16_f32 v67,v70,v71
v_cvt_pk_bf16_f32 v72,v72,v73
v_mfma_f32_16x16x32_bf16 v[84:87],a[144:147],v[160:163],v[84:87]
v_exp_f32 v74,v74
v_mfma_f32_16x16x32_bf16 v[88:91],a[160:163],v[160:163],v[36:39]
v_exp_f32 v75,v75
v_mfma_f32_16x16x32_bf16 v[88:91],a[164:167],v[168:171],v[88:91]
v_exp_f32 v76,v76
v_mfma_f32_16x16x32_bf16 v[88:91],a[168:171],v[176:179],v[88:91]
v_exp_f32 v77,v77
v_mfma_f32_16x16x32_bf16 v[88:91],a[172:175],v[184:187],v[88:91]
v_exp_f32 v78,v78
v_mfma_f32_16x16x32_bf16 v[92:95],a[188:191],v[184:187],v[36:39]
v_exp_f32 v79,v79
v_mfma_f32_16x16x32_bf16 v[92:95],a[184:187],v[176:179],v[92:95]
v_cvt_pk_bf16_f32 v73,v74,v75
v_cvt_pk_bf16_f32 v74,v76,v77
v_mfma_f32_16x16x32_bf16 v[92:95],a[180:183],v[168:171],v[92:95]
global_load_lds_dwordx4 v5,s[10:11] offset:1024
v_cvt_pk_bf16_f32 v75,v78,v79
v_mfma_f32_16x16x32_bf16 v[92:95],a[176:179],v[160:163],v[92:95]
v_exp_f32 v80,v80
v_mfma_f32_16x16x32_bf16 a[32:35],a[192:195],v[112:115],a[32:35]
ds_read_b64_tr_b16 a[192:193],v2
ds_read_b64_tr_b16 a[194:195],v2 offset:4096
v_mfma_f32_16x16x32_bf16 a[32:35],a[196:199],v[120:123],a[32:35]
v_exp_f32 v81,v81
v_mfma_f32_16x16x32_bf16 a[36:39],a[200:203],v[112:115],a[36:39]
ds_read_b64_tr_b16 a[196:197],v2 offset:8192
ds_read_b64_tr_b16 a[198:199],v2 offset:12288
v_mfma_f32_16x16x32_bf16 a[36:39],a[204:207],v[120:123],a[36:39]
ds_read_b64_tr_b16 a[200:201],v3
v_cvt_pk_bf16_f32 v80,v80,v81
v_mfma_f32_16x16x32_bf16 a[40:43],a[208:211],v[112:115],a[40:43]
v_exp_f32 v82,v82
v_mfma_f32_16x16x32_bf16 a[40:43],a[212:215],v[120:123],a[40:43]
ds_read_b64_tr_b16 a[202:203],v3 offset:4096
ds_read_b64_tr_b16 a[204:205],v3 offset:8192
v_mfma_f32_16x16x32_bf16 a[44:47],a[216:219],v[112:115],a[44:47]
v_exp_f32 v83,v83
v_mfma_f32_16x16x32_bf16 a[44:47],a[220:223],v[120:123],a[44:47]
ds_read_b64_tr_b16 a[206:207],v3 offset:12288
ds_read_b64_tr_b16 a[208:209],v2 offset:256
v_mfma_f32_16x16x32_bf16 a[48:51],a[224:227],v[112:115],a[48:51]
ds_read_b64_tr_b16 a[210:211],v2 offset:4352
v_cvt_pk_bf16_f32 v81,v82,v83
v_mfma_f32_16x16x32_bf16 a[48:51],a[228:231],v[120:123],a[48:51]
v_exp_f32 v84,v84
v_mfma_f32_16x16x32_bf16 a[52:55],a[232:235],v[112:115],a[52:55]
ds_read_b64_tr_b16 a[212:213],v2 offset:8448
ds_read_b64_tr_b16 a[214:215],v2 offset:12544
v_mfma_f32_16x16x32_bf16 a[52:55],a[236:239],v[120:123],a[52:55]
v_exp_f32 v85,v85
v_mfma_f32_16x16x32_bf16 a[56:59],a[240:243],v[112:115],a[56:59]
ds_read_b64_tr_b16 a[216:217],v3 offset:256
ds_read_b64_tr_b16 a[218:219],v3 offset:4352
v_mfma_f32_16x16x32_bf16 a[56:59],a[244:247],v[120:123],a[56:59]
ds_read_b64_tr_b16 a[220:221],v3 offset:8448
)A"
R"A(v_cvt_pk_bf16_f32 v82,v84,v85
v_mfma_f32_16x16x32_bf16 a[60:63],a[248:251],v[112:115],a[60:63]
global_load_lds_dwordx4 v4,s[10:11] offset:2048
ds_read_b64_tr_b16 a[222:223],v3 offset:12544
v_mfma_f32_16x16x32_bf16 a[60:63],a[252:255],v[120:123],a[60:63]
v_exp_f32 v86,v86
v_mfma_f32_16x16x32_bf16 v[60:63],v[28:31],v[112:115],v[60:63]
ds_read_b64_tr_b16 a[224:225],v2 offset:512
ds_read_b64_tr_b16 a[226:227],v2 offset:4608
v_mfma_f32_16x16x32_bf16 v[60:63],v[28:31],v[120:123],v[60:63]
v_exp_f32 v87,v87
v_mfma_f32_16x16x32_bf16 v[96:99],a[128:131],v[192:195],v[40:43]
ds_read_b64_tr_b16 a[228:229],v2 offset:8704
ds_read_b64_tr_b16 a[230:231],v2 offset:12800
v_mfma_f32_16x16x32_bf16 v[96:99],a[132:135],v[200:203],v[96:99]
ds_read_b64_tr_b16 a[232:233],v3 offset:512
v_cvt_pk_bf16_f32 v83,v86,v87
v_mfma_f32_16x16x32_bf16 v[96:99],a[136:139],v[208:211],v[96:99]
v_exp_f32 v88,v88
v_mfma_f32_16x16x32_bf16 v[96:99],a[140:143],v[216:219],v[96:99]
ds_read_b64_tr_b16 a[234:235],v3 offset:4608
ds_read_b64_tr_b16 a[236:237],v3 offset:8704
v_mfma_f32_16x16x32_bf16 v[100:103],a[156:159],v[216:219],v[40:43]
v_exp_f32 v89,v89
v_mfma_f32_16x16x32_bf16 v[100:103],a[152:155],v[208:211],v[100:103]
ds_read_b64_tr_b16 a[238:239],v3 offset:12800
ds_read_b64_tr_b16 a[240:241],v2 offset:768
v_mfma_f32_16x16x32_bf16 v[100:103],a[148:151],v[200:203],v[100:103]
ds_read_b64_tr_b16 a[242:243],v2 offset:4864
v_cvt_pk_bf16_f32 v88,v88,v89
v_mfma_f32_16x16x32_bf16 v[100:103],a[144:147],v[192:195],v[100:103]
v_exp_f32 v90,v90
v_mfma_f32_16x16x32_bf16 v[104:107],a[160:163],v[192:195],v[40:43]
ds_read_b64_tr_b16 a[244:245],v2 offset:8960
ds_read_b64_tr_b16 a[246:247],v2 offset:13056
v_mfma_f32_16x16x32_bf16 v[104:107],a[164:167],v[200:203],v[104:107]
v_exp_f32 v91,v91
v_mfma_f32_16x16x32_bf16 v[104:107],a[168:171],v[208:211],v[104:107]
ds_read_b64_tr_b16 a[248:249],v3 offset:768
ds_read_b64_tr_b16 a[250:251],v3 offset:4864
v_mfma_f32_16x16x32_bf16 v[104:107],a[172:175],v[216:219],v[104:107]
ds_read_b64_tr_b16 a[252:253],v3 offset:8960
v_cvt_pk_bf16_f32 v89,v90,v91
v_mfma_f32_16x16x32_bf16 v[108:111],a[188:191],v[216:219],v[40:43]
global_load_lds_dwordx4 v5,s[10:11] offset:3072
ds_read_b64_tr_b16 a[254:255],v3 offset:13056
v_mfma_f32_16x16x32_bf16 v[108:111],a[184:187],v[208:211],v[108:111]
v_exp_f32 v92,v92
v_mfma_f32_16x16x32_bf16 v[108:111],a[180:183],v[200:203],v[108:111]
v_exp_f32 v93,v93
v_mfma_f32_16x16x32_bf16 v[108:111],a[176:179],v[192:195],v[108:111]
s_waitcnt lgkmcnt(6)
v_exp_f32 v94,v94
v_mfma_f32_16x16x32_bf16 a[0:3],a[192:195],v[64:67],a[0:3]
v_exp_f32 v95,v95
v_mfma_f32_16x16x32_bf16 a[0:3],a[196:199],v[72:75],a[0:3]
v_cvt_pk_bf16_f32 v90,v92,v93
v_cvt_pk_bf16_f32 v91,v94,v95
v_mfma_f32_16x16x32_bf16 a[4:7],a[200:203],v[64:67],a[4:7]
v_mfma_f32_16x16x32_bf16 a[4:7],a[204:207],v[72:75],a[4:7]
v_mfma_f32_16x16x32_bf16 a[8:11],a[208:211],v[64:67],a[8:11]
v_mfma_f32_16x16x32_bf16 a[8:11],a[212:215],v[72:75],a[8:11]
v_mfma_f32_16x16x32_bf16 a[12:15],a[216:219],v[64:67],a[12:15]
v_mfma_f32_16x16x32_bf16 a[12:15],a[220:223],v[72:75],a[12:15]
v_mfma_f32_16x16x32_bf16 a[16:19],a[224:227],v[64:67],a[16:19]
v_mfma_f32_16x16x32_bf16 a[16:19],a[228:231],v[72:75],a[16:19]
v_mfma_f32_16x16x32_bf16 a[20:23],a[232:235],v[64:67],a[20:23]
s_getreg_b32 s92,hwreg(HW_REG_IB_STS,0,4)
s_min_u32 s4,s92,s6
s_mov_b32 s6,s92
v_mfma_f32_16x16x32_bf16 a[20:23],a[236:239],v[72:75],a[20:23]
s_cmp_ge_u32 s4,s9
s_cselect_b32 s4,15,s5
v_mfma_f32_16x16x32_bf16 a[24:27],a[240:243],v[64:67],a[24:27]
s_waitcnt lgkmcnt(0)
v_mfma_f32_16x16x32_bf16 a[24:27],a[244:247],v[72:75],a[24:27]
v_mfma_f32_16x16x32_bf16 a[28:31],a[248:251],v[64:67],a[28:31]
v_mfma_f32_16x16x32_bf16 a[28:31],a[252:255],v[72:75],a[28:31]
s_getpc_b64 s[92:93]
s_cmp_lg_u32 s4,0
s_cbranch_scc1 L_pace_%=
v_mfma_f32_16x16x32_bf16 v[48:51],v[28:31],v[64:67],v[48:51]
v_mfma_f32_16x16x32_bf16 v[48:51],v[28:31],v[72:75],v[48:51]
s_sub_u32 s70,s70,1
s_cbranch_scc0 L_a1_%=
s_waitcnt vmcnt(16)
s_barrier
v_mfma_f32_16x16x32_bf16 v[112:115],a[128:131],v[224:227],v[44:47]
s_mul_i32 s86,s60,10240
s_mul_i32 s85,s63,1310720
s_mul_i32 s66,s64,320
v_mfma_f32_16x16x32_bf16 v[112:115],a[132:135],v[232:235],v[112:115]
s_add_u32 s85,s85,s66
s_add_u32 s83,s64,0
s_and_b32 s83,s83,3
v_mfma_f32_16x16x32_bf16 v[112:115],a[136:139],v[240:243],v[112:115]
s_lshl_b32 s83,s83,1
s_mul_i32 s83,s83,163840
s_add_u32 s83,s83,s85
v_mfma_f32_16x16x32_bf16 v[112:115],a[140:143],v[248:251],v[112:115]
s_cmp_eq_u32 s60,3
s_cselect_b32 s69,0x40000000,0
v_add_u32 v14,s69,v6
v_mfma_f32_16x16x32_bf16 v[116:119],a[156:159],v[248:251],v[44:47]
s_add_u32 s84,s83,20480
ds_read_b128 a[128:131],v1
v_add_u32 v2,s78,v2
v_mfma_f32_16x16x32_bf16 v[116:119],a[152:155],v[240:243],v[116:119]
s_add_u32 m0,s86,122880
v_exp_f32 v96,v96
v_mfma_f32_16x16x32_bf16 v[116:119],a[148:151],v[232:235],v[116:119]
s_getpc_b64 s[8:9]
L_pcg_%=:
ds_read_b128 a[132:135],v1 offset:256
v_add_u32 v3,s78,v3
v_mfma_f32_16x16x32_bf16 v[116:119],a[144:147],v[224:227],v[116:119]
buffer_load_dwordx4 v24,s[48:51],s83 offen lds
v_mfma_f32_16x16x32_bf16 v[120:123],a[160:163],v[224:227],v[44:47]
s_sub_u32 s78,s77,32768
s_and_b32 s78,s78,0x1ffff
ds_read_b128 a[136:139],v1 offset:512
v_mfma_f32_16x16x32_bf16 v[120:123],a[164:167],v[232:235],v[120:123]
buffer_store_dwordx4 a[112:115],v14,s[40:43],s81 offen offset:3072 sc1
v_mfma_f32_16x16x32_bf16 v[120:123],a[168:171],v[240:243],v[120:123]
s_sub_u32 s78,s78,s77
ds_read_b128 a[140:143],v1 offset:768
v_lshlrev_b32 v4,10,v6
v_mfma_f32_16x16x32_bf16 v[120:123],a[172:175],v[248:251],v[120:123]
s_add_u32 s77,s77,s78
v_exp_f32 v97,v97
v_mfma_f32_16x16x32_bf16 v[124:127],a[188:191],v[248:251],v[44:47]
s_mov_b32 s6,L_gbF_%=-L_pcg_%=
ds_read_b128 a[156:159],v1 offset:4864
v_cvt_pk_bf16_f32 v96,v96,v97
v_mfma_f32_16x16x32_bf16 v[124:127],a[184:187],v[240:243],v[124:127]
s_mov_b32 s10,L_geF_%=-L_gbF_%=
v_exp_f32 v98,v98
v_mfma_f32_16x16x32_bf16 v[124:127],a[180:183],v[232:235],v[124:127]
s_mov_b32 s11,0x20000
s_lshr_b32 s7,%1,3
ds_read_b128 a[152:155],v1 offset:4608
v_mfma_f32_16x16x32_bf16 v[124:127],a[176:179],v[224:227],v[124:127]
s_lshl_b32 s7,s7,2
v_exp_f32 v99,v99
v_mfma_f32_16x16x32_bf16 v[132:135],a[192:195],v[80:83],v[132:135]
s_add_u32 s7,s7,s60
ds_read_b128 a[148:151],v1 offset:4352
v_cvt_pk_bf16_f32 v97,v98,v99
v_mfma_f32_16x16x32_bf16 v[132:135],a[196:199],v[88:91],v[132:135]
buffer_load_dwordx4 v25,s[48:51],s83 offen offset:1024 lds
v_mfma_f32_16x16x32_bf16 v[140:143],a[200:203],v[80:83],v[140:143]
s_lshl_b32 s7,s7,7
ds_read_b128 a[144:147],v1 offset:4096
v_mov_b32 v8,s52
v_mfma_f32_16x16x32_bf16 v[140:143],a[204:207],v[88:91],v[140:143]
s_lshl_b32 s66,s61,5
v_exp_f32 v100,v100
v_mfma_f32_16x16x32_bf16 v[148:151],a[208:211],v[80:83],v[148:151]
s_lshl_b32 s67,s63,3
ds_read_b128 a[160:163],v1 offset:8192
v_mov_b32 v9,s53
v_mfma_f32_16x16x32_bf16 v[148:151],a[212:215],v[88:91],v[148:151]
s_add_u32 s66,s66,s67
v_exp_f32 v101,v101
v_mfma_f32_16x16x32_bf16 v[156:159],a[216:219],v[80:83],v[156:159]
s_lshl_b32 s66,s66,16
ds_read_b128 a[164:167],v1 offset:8448
v_cvt_pk_bf16_f32 v98,v100,v101
v_mfma_f32_16x16x32_bf16 v[156:159],a[220:223],v[88:91],v[156:159]
s_lshl_b32 s67,s60,14
v_exp_f32 v102,v102
v_mfma_f32_16x16x32_bf16 v[164:167],a[224:227],v[80:83],v[164:167]
s_add_u32 s66,s66,s67
ds_read_b128 a[168:171],v1 offset:8704
v_lshlrev_b32 v10,20,v6
v_mfma_f32_16x16x32_bf16 v[164:167],a[228:231],v[88:91],v[164:167]
s_mov_b32 s30,1523712
v_exp_f32 v103,v103
v_mfma_f32_16x16x32_bf16 v[172:175],a[232:235],v[80:83],v[172:175]
buffer_store_dwordx4 a[116:119],v6,s[40:43],s81 offen offset:1024 sc1
v_mfma_f32_16x16x32_bf16 v[172:175],a[236:239],v[88:91],v[172:175]
s_mov_b32 s31,0x20000
ds_read_b128 a[172:175],v1 offset:8960
v_cvt_pk_bf16_f32 v99,v102,v103
v_mfma_f32_16x16x32_bf16 v[180:183],a[240:243],v[80:83],v[180:183]
buffer_load_dwordx4 v26,s[48:51],s83 offen offset:2048 lds
v_mfma_f32_16x16x32_bf16 v[180:183],a[244:247],v[88:91],v[180:183]
v_exp_f32 v104,v104
v_mfma_f32_16x16x32_bf16 v[188:191],a[248:251],v[80:83],v[188:191]
v_exp_f32 v105,v105
v_mfma_f32_16x16x32_bf16 v[188:191],a[252:255],v[88:91],v[188:191]
ds_read_b128 a[188:191],v1 offset:13056
v_cvt_pk_bf16_f32 v104,v104,v105
v_mfma_f32_16x16x32_bf16 v[52:55],v[28:31],v[80:83],v[52:55]
v_exp_f32 v106,v106
v_mfma_f32_16x16x32_bf16 v[52:55],v[28:31],v[88:91],v[52:55]
s_waitcnt lgkmcnt(2)
v_exp_f32 v107,v107
v_mfma_f32_16x16x32_bf16 v[64:67],a[128:131],v[128:131],v[32:35]
ds_read_b128 a[184:187],v1 offset:12800
v_cvt_pk_bf16_f32 v105,v106,v107
v_mfma_f32_16x16x32_bf16 v[64:67],a[132:135],v[136:139],v[64:67]
v_exp_f32 v108,v108
v_mfma_f32_16x16x32_bf16 v[64:67],a[136:139],v[144:147],v[64:67]
s_add_u32 s28,s12,s66
s_addc_u32 s29,s13,0
ds_read_b128 a[180:183],v1 offset:12544
v_mfma_f32_16x16x32_bf16 v[64:67],a[140:143],v[152:155],v[64:67]
buffer_load_dwordx4 v27,s[48:51],s83 offen offset:3072 lds
v_mfma_f32_16x16x32_bf16 v[68:71],a[156:159],v[152:155],v[32:35]
s_and_b32 s29,s29,0xffff
ds_read_b128 a[176:179],v1 offset:12288
v_add_u32 v1,s78,v1
v_mfma_f32_16x16x32_bf16 v[68:71],a[152:155],v[144:147],v[68:71]
v_exp_f32 v109,v109
v_mfma_f32_16x16x32_bf16 v[68:71],a[148:151],v[136:139],v[68:71]
v_exp_f32 v110,v110
v_mfma_f32_16x16x32_bf16 v[68:71],a[144:147],v[128:131],v[68:71]
v_exp_f32 v111,v111
v_mfma_f32_16x16x32_bf16 v[72:75],a[160:163],v[128:131],v[32:35]
v_cvt_pk_bf16_f32 v106,v108,v109
v_cvt_pk_bf16_f32 v107,v110,v111
v_mfma_f32_16x16x32_bf16 v[72:75],a[164:167],v[136:139],v[72:75]
buffer_store_dwordx4 a[120:123],v14,s[40:43],s82 offen offset:3072 sc1
v_mfma_f32_16x16x32_bf16 v[72:75],a[168:171],v[144:147],v[72:75]
s_waitcnt lgkmcnt(1)
v_exp_f32 v112,v112
v_mfma_f32_16x16x32_bf16 v[72:75],a[172:175],v[152:155],v[72:75]
s_add_u32 m0,s86,126976
v_exp_f32 v113,v113
v_mfma_f32_16x16x32_bf16 v[76:79],a[188:191],v[152:155],v[32:35]
s_waitcnt lgkmcnt(0)
v_exp_f32 v114,v114
v_mfma_f32_16x16x32_bf16 v[76:79],a[184:187],v[144:147],v[76:79]
buffer_load_dwordx4 v7,s[48:51],s83 offen lds
v_mfma_f32_16x16x32_bf16 v[76:79],a[180:183],v[136:139],v[76:79]
s_add_u32 s83,s64,0
v_exp_f32 v115,v115
v_mfma_f32_16x16x32_bf16 v[76:79],a[176:179],v[128:131],v[76:79]
s_and_b32 s83,s83,3
v_exp_f32 v116,v116
v_mfma_f32_16x16x32_bf16 v[196:199],a[192:195],v[96:99],v[196:199]
s_lshl_b32 s83,s83,1
v_exp_f32 v117,v117
v_mfma_f32_16x16x32_bf16 v[196:199],a[196:199],v[104:107],v[196:199]
s_or_b32 s83,s83,1
v_exp_f32 v118,v118
v_mfma_f32_16x16x32_bf16 v[204:207],a[200:203],v[96:99],v[204:207]
s_mul_i32 s83,s83,163840
v_exp_f32 v119,v119
v_mfma_f32_16x16x32_bf16 v[204:207],a[204:207],v[104:107],v[204:207]
s_add_u32 s83,s83,s85
v_cvt_pk_bf16_f32 v112,v112,v113
v_cvt_pk_bf16_f32 v113,v114,v115
v_mfma_f32_16x16x32_bf16 v[212:215],a[208:211],v[96:99],v[212:215]
v_cvt_pk_bf16_f32 v114,v116,v117
v_cvt_pk_bf16_f32 v115,v118,v119
v_mfma_f32_16x16x32_bf16 v[212:215],a[212:215],v[104:107],v[212:215]
s_nop 0
L_pd11_%=:
s_cbranch_execz L_t1c_%=
s_add_u32 m0,s86,128000
v_exp_f32 v120,v120
v_mfma_f32_16x16x32_bf16 v[220:223],a[216:219],v[96:99],v[220:223]
v_exp_f32 v121,v121
v_mfma_f32_16x16x32_bf16 v[220:223],a[220:223],v[104:107],v[220:223]
buffer_load_dwordx4 v24,s[48:51],s84 offen lds
v_mfma_f32_16x16x32_bf16 v[228:231],a[224:227],v[96:99],v[228:231]
v_exp_f32 v122,v122
v_mfma_f32_16x16x32_bf16 v[228:231],a[228:231],v[104:107],v[228:231]
v_exp_f32 v123,v123
v_mfma_f32_16x16x32_bf16 v[236:239],a[232:235],v[96:99],v[236:239]
buffer_store_dwordx4 a[124:127],v6,s[40:43],s82 offen offset:1024 sc1
v_mfma_f32_16x16x32_bf16 v[236:239],a[236:239],v[104:107],v[236:239]
v_exp_f32 v124,v124
v_mfma_f32_16x16x32_bf16 v[244:247],a[240:243],v[96:99],v[244:247]
v_exp_f32 v125,v125
v_mfma_f32_16x16x32_bf16 v[244:247],a[244:247],v[104:107],v[244:247]
v_exp_f32 v126,v126
v_mfma_f32_16x16x32_bf16 v[252:255],a[248:251],v[96:99],v[252:255]
v_exp_f32 v127,v127
v_mfma_f32_16x16x32_bf16 v[252:255],a[252:255],v[104:107],v[252:255]
v_cvt_pk_bf16_f32 v120,v120,v121
v_cvt_pk_bf16_f32 v121,v122,v123
v_mfma_f32_16x16x32_bf16 v[56:59],v[28:31],v[96:99],v[56:59]
)A"
R"A(v_cvt_pk_bf16_f32 v122,v124,v125
v_cvt_pk_bf16_f32 v123,v126,v127
v_mfma_f32_16x16x32_bf16 v[56:59],v[28:31],v[104:107],v[56:59]
buffer_load_dwordx4 v25,s[48:51],s84 offen offset:1024 lds
v_mfma_f32_16x16x32_bf16 v[80:83],a[128:131],v[160:163],v[36:39]
v_exp_f32 v64,v64
v_mfma_f32_16x16x32_bf16 v[80:83],a[132:135],v[168:171],v[80:83]
v_exp_f32 v65,v65
v_mfma_f32_16x16x32_bf16 v[80:83],a[136:139],v[176:179],v[80:83]
v_exp_f32 v66,v66
v_mfma_f32_16x16x32_bf16 v[80:83],a[140:143],v[184:187],v[80:83]
v_exp_f32 v67,v67
v_mfma_f32_16x16x32_bf16 v[84:87],a[156:159],v[184:187],v[36:39]
v_exp_f32 v68,v68
v_mfma_f32_16x16x32_bf16 v[84:87],a[152:155],v[176:179],v[84:87]
v_exp_f32 v69,v69
v_mfma_f32_16x16x32_bf16 v[84:87],a[148:151],v[168:171],v[84:87]
v_exp_f32 v70,v70
v_mfma_f32_16x16x32_bf16 v[84:87],a[144:147],v[160:163],v[84:87]
v_exp_f32 v71,v71
v_mfma_f32_16x16x32_bf16 v[88:91],a[160:163],v[160:163],v[36:39]
v_cvt_pk_bf16_f32 v64,v64,v65
v_cvt_pk_bf16_f32 v65,v66,v67
v_mfma_f32_16x16x32_bf16 v[88:91],a[164:167],v[168:171],v[88:91]
buffer_store_dwordx4 a[96:99],v14,s[40:43],s79 offen offset:3072 sc1
v_mfma_f32_16x16x32_bf16 v[88:91],a[168:171],v[176:179],v[88:91]
v_cvt_pk_bf16_f32 v66,v68,v69
v_cvt_pk_bf16_f32 v67,v70,v71
v_mfma_f32_16x16x32_bf16 v[88:91],a[172:175],v[184:187],v[88:91]
buffer_load_dwordx4 v26,s[48:51],s84 offen offset:2048 lds
v_mfma_f32_16x16x32_bf16 v[92:95],a[188:191],v[184:187],v[36:39]
v_exp_f32 v72,v72
v_mfma_f32_16x16x32_bf16 v[92:95],a[184:187],v[176:179],v[92:95]
v_exp_f32 v73,v73
v_mfma_f32_16x16x32_bf16 v[92:95],a[180:183],v[168:171],v[92:95]
v_exp_f32 v74,v74
v_mfma_f32_16x16x32_bf16 v[92:95],a[176:179],v[160:163],v[92:95]
v_exp_f32 v75,v75
v_mfma_f32_16x16x32_bf16 a[32:35],a[192:195],v[112:115],a[32:35]
ds_read_b64_tr_b16 a[192:193],v2
ds_read_b64_tr_b16 a[194:195],v2 offset:4096
v_mfma_f32_16x16x32_bf16 a[32:35],a[196:199],v[120:123],a[32:35]
ds_read_b64_tr_b16 a[196:197],v2 offset:8192
v_cvt_pk_bf16_f32 v72,v72,v73
v_mfma_f32_16x16x32_bf16 a[36:39],a[200:203],v[112:115],a[36:39]
ds_read_b64_tr_b16 a[198:199],v2 offset:12288
v_cvt_pk_bf16_f32 v73,v74,v75
v_mfma_f32_16x16x32_bf16 a[36:39],a[204:207],v[120:123],a[36:39]
buffer_load_dwordx4 v27,s[48:51],s84 offen offset:3072 lds
v_mfma_f32_16x16x32_bf16 a[40:43],a[208:211],v[112:115],a[40:43]
ds_read_b64_tr_b16 a[200:201],v3
ds_read_b64_tr_b16 a[202:203],v3 offset:4096
v_mfma_f32_16x16x32_bf16 a[40:43],a[212:215],v[120:123],a[40:43]
v_exp_f32 v76,v76
v_mfma_f32_16x16x32_bf16 a[44:47],a[216:219],v[112:115],a[44:47]
ds_read_b64_tr_b16 a[204:205],v3 offset:8192
ds_read_b64_tr_b16 a[206:207],v3 offset:12288
v_mfma_f32_16x16x32_bf16 a[44:47],a[220:223],v[120:123],a[44:47]
v_exp_f32 v77,v77
v_mfma_f32_16x16x32_bf16 a[48:51],a[224:227],v[112:115],a[48:51]
ds_read_b64_tr_b16 a[208:209],v2 offset:256
ds_read_b64_tr_b16 a[210:211],v2 offset:4352
v_mfma_f32_16x16x32_bf16 a[48:51],a[228:231],v[120:123],a[48:51]
ds_read_b64_tr_b16 a[212:213],v2 offset:8448
v_cvt_pk_bf16_f32 v74,v76,v77
v_mfma_f32_16x16x32_bf16 a[52:55],a[232:235],v[112:115],a[52:55]
buffer_store_dwordx4 a[100:103],v6,s[40:43],s79 offen offset:1024 sc1
v_mfma_f32_16x16x32_bf16 a[52:55],a[236:239],v[120:123],a[52:55]
s_add_u32 m0,s86,132096
ds_read_b64_tr_b16 a[214:215],v2 offset:12544
ds_read_b64_tr_b16 a[216:217],v3 offset:256
v_mfma_f32_16x16x32_bf16 a[56:59],a[240:243],v[112:115],a[56:59]
v_exp_f32 v78,v78
v_mfma_f32_16x16x32_bf16 a[56:59],a[244:247],v[120:123],a[56:59]
buffer_load_dwordx4 v7,s[48:51],s84 offen lds
v_mfma_f32_16x16x32_bf16 a[60:63],a[248:251],v[112:115],a[60:63]
s_add_u32 s84,s83,20480
ds_read_b64_tr_b16 a[218:219],v3 offset:4352
ds_read_b64_tr_b16 a[220:221],v3 offset:8448
v_mfma_f32_16x16x32_bf16 a[60:63],a[252:255],v[120:123],a[60:63]
v_exp_f32 v79,v79
v_mfma_f32_16x16x32_bf16 v[60:63],v[28:31],v[112:115],v[60:63]
ds_read_b64_tr_b16 a[222:223],v3 offset:12544
v_cvt_pk_bf16_f32 v75,v78,v79
v_mfma_f32_16x16x32_bf16 v[60:63],v[28:31],v[120:123],v[60:63]
ds_read_b64_tr_b16 a[224:225],v2 offset:512
ds_read_b64_tr_b16 a[226:227],v2 offset:4608
v_mfma_f32_16x16x32_bf16 v[96:99],a[128:131],v[192:195],v[40:43]
v_exp_f32 v80,v80
v_mfma_f32_16x16x32_bf16 v[96:99],a[132:135],v[200:203],v[96:99]
ds_read_b64_tr_b16 a[228:229],v2 offset:8704
ds_read_b64_tr_b16 a[230:231],v2 offset:12800
v_mfma_f32_16x16x32_bf16 v[96:99],a[136:139],v[208:211],v[96:99]
v_exp_f32 v81,v81
v_mfma_f32_16x16x32_bf16 v[96:99],a[140:143],v[216:219],v[96:99]
ds_read_b64_tr_b16 a[232:233],v3 offset:512
ds_read_b64_tr_b16 a[234:235],v3 offset:4608
v_mfma_f32_16x16x32_bf16 v[100:103],a[156:159],v[216:219],v[40:43]
ds_read_b64_tr_b16 a[236:237],v3 offset:8704
v_cvt_pk_bf16_f32 v80,v80,v81
v_mfma_f32_16x16x32_bf16 v[100:103],a[152:155],v[208:211],v[100:103]
v_exp_f32 v82,v82
v_mfma_f32_16x16x32_bf16 v[100:103],a[148:151],v[200:203],v[100:103]
ds_read_b64_tr_b16 a[238:239],v3 offset:12800
ds_read_b64_tr_b16 a[240:241],v2 offset:768
v_mfma_f32_16x16x32_bf16 v[100:103],a[144:147],v[192:195],v[100:103]
v_exp_f32 v83,v83
v_mfma_f32_16x16x32_bf16 v[104:107],a[160:163],v[192:195],v[40:43]
ds_read_b64_tr_b16 a[242:243],v2 offset:4864
ds_read_b64_tr_b16 a[244:245],v2 offset:8960
v_mfma_f32_16x16x32_bf16 v[104:107],a[164:167],v[200:203],v[104:107]
buffer_store_dwordx4 a[104:107],v14,s[40:43],s80 offen offset:3072 sc1
v_mfma_f32_16x16x32_bf16 v[104:107],a[168:171],v[208:211],v[104:107]
ds_read_b64_tr_b16 a[246:247],v2 offset:13056
ds_read_b64_tr_b16 a[248:249],v3 offset:768
v_mfma_f32_16x16x32_bf16 v[104:107],a[172:175],v[216:219],v[104:107]
ds_read_b64_tr_b16 a[250:251],v3 offset:4864
v_cvt_pk_bf16_f32 v81,v82,v83
v_mfma_f32_16x16x32_bf16 v[108:111],a[188:191],v[216:219],v[40:43]
ds_read_b64_tr_b16 a[252:253],v3 offset:8960
v_add_u32 v2,s78,v2
v_mfma_f32_16x16x32_bf16 v[108:111],a[184:187],v[208:211],v[108:111]
s_waitcnt lgkmcnt(6)
ds_read_b64_tr_b16 a[254:255],v3 offset:13056
v_add_u32 v3,s78,v3
v_mfma_f32_16x16x32_bf16 v[108:111],a[180:183],v[200:203],v[108:111]
s_sub_u32 s78,s77,32768
v_exp_f32 v84,v84
v_mfma_f32_16x16x32_bf16 v[108:111],a[176:179],v[192:195],v[108:111]
s_and_b32 s78,s78,0x1ffff
v_exp_f32 v85,v85
v_mfma_f32_16x16x32_bf16 a[0:3],a[192:195],v[64:67],a[0:3]
s_sub_u32 s78,s78,s77
v_exp_f32 v86,v86
v_mfma_f32_16x16x32_bf16 a[0:3],a[196:199],v[72:75],a[0:3]
s_add_u32 s77,s77,s78
v_exp_f32 v87,v87
v_mfma_f32_16x16x32_bf16 a[4:7],a[200:203],v[64:67],a[4:7]
v_cvt_pk_bf16_f32 v82,v84,v85
v_cvt_pk_bf16_f32 v83,v86,v87
v_mfma_f32_16x16x32_bf16 a[4:7],a[204:207],v[72:75],a[4:7]
v_exp_f32 v88,v88
v_mfma_f32_16x16x32_bf16 a[8:11],a[208:211],v[64:67],a[8:11]
v_exp_f32 v89,v89
v_mfma_f32_16x16x32_bf16 a[8:11],a[212:215],v[72:75],a[8:11]
v_exp_f32 v90,v90
v_mfma_f32_16x16x32_bf16 a[12:15],a[216:219],v[64:67],a[12:15]
v_exp_f32 v91,v91
v_mfma_f32_16x16x32_bf16 a[12:15],a[220:223],v[72:75],a[12:15]
v_exp_f32 v92,v92
v_mfma_f32_16x16x32_bf16 a[16:19],a[224:227],v[64:67],a[16:19]
v_exp_f32 v93,v93
v_mfma_f32_16x16x32_bf16 a[16:19],a[228:231],v[72:75],a[16:19]
v_exp_f32 v94,v94
v_mfma_f32_16x16x32_bf16 a[20:23],a[232:235],v[64:67],a[20:23]
buffer_store_dwordx4 a[108:111],v6,s[40:43],s80 offen offset:1024 sc1
v_mfma_f32_16x16x32_bf16 a[20:23],a[236:239],v[72:75],a[20:23]
s_waitcnt lgkmcnt(0)
v_exp_f32 v95,v95
v_mfma_f32_16x16x32_bf16 a[24:27],a[240:243],v[64:67],a[24:27]
v_cvt_pk_bf16_f32 v88,v88,v89
v_cvt_pk_bf16_f32 v89,v90,v91
v_mfma_f32_16x16x32_bf16 a[24:27],a[244:247],v[72:75],a[24:27]
v_cvt_pk_bf16_f32 v90,v92,v93
v_cvt_pk_bf16_f32 v91,v94,v95
v_mfma_f32_16x16x32_bf16 a[28:31],a[248:251],v[64:67],a[28:31]
v_exp_f32 v96,v96
v_mfma_f32_16x16x32_bf16 a[28:31],a[252:255],v[72:75],a[28:31]
v_exp_f32 v97,v97
v_mfma_f32_16x16x32_bf16 v[48:51],v[28:31],v[64:67],v[48:51]
v_exp_f32 v98,v98
v_mfma_f32_16x16x32_bf16 v[48:51],v[28:31],v[72:75],v[48:51]
v_exp_f32 v99,v99
s_waitcnt vmcnt(26)
s_barrier
v_mfma_f32_16x16x32_bf16 v[112:115],a[128:131],v[224:227],v[44:47]
ds_read_b128 a[128:131],v1
v_cvt_pk_bf16_f32 v96,v96,v97
v_mfma_f32_16x16x32_bf16 v[112:115],a[132:135],v[232:235],v[112:115]
v_exp_f32 v100,v100
v_mfma_f32_16x16x32_bf16 v[112:115],a[136:139],v[240:243],v[112:115]
ds_read_b128 a[132:135],v1 offset:256
v_cvt_pk_bf16_f32 v97,v98,v99
v_mfma_f32_16x16x32_bf16 v[112:115],a[140:143],v[248:251],v[112:115]
v_exp_f32 v101,v101
v_mfma_f32_16x16x32_bf16 v[116:119],a[156:159],v[248:251],v[44:47]
ds_read_b128 a[136:139],v1 offset:512
v_cvt_pk_bf16_f32 v98,v100,v101
v_mfma_f32_16x16x32_bf16 v[116:119],a[152:155],v[240:243],v[116:119]
s_add_u32 m0,s86,81920
v_exp_f32 v102,v102
v_mfma_f32_16x16x32_bf16 v[116:119],a[148:151],v[232:235],v[116:119]
v_exp_f32 v103,v103
v_mfma_f32_16x16x32_bf16 v[116:119],a[144:147],v[224:227],v[116:119]
buffer_load_dwordx4 v24,s[48:51],s83 offen lds
v_mfma_f32_16x16x32_bf16 v[120:123],a[160:163],v[224:227],v[44:47]
ds_read_b128 a[140:143],v1 offset:768
v_cvt_pk_bf16_f32 v99,v102,v103
v_mfma_f32_16x16x32_bf16 v[120:123],a[164:167],v[232:235],v[120:123]
buffer_store_dwordx4 a[64:67],v6,s[40:43],s79 offen offset:2048 sc1
v_mfma_f32_16x16x32_bf16 v[120:123],a[168:171],v[240:243],v[120:123]
v_exp_f32 v104,v104
v_mfma_f32_16x16x32_bf16 v[120:123],a[172:175],v[248:251],v[120:123]
v_exp_f32 v105,v105
v_mfma_f32_16x16x32_bf16 v[124:127],a[188:191],v[248:251],v[44:47]
ds_read_b128 a[156:159],v1 offset:4864
v_cvt_pk_bf16_f32 v104,v104,v105
v_mfma_f32_16x16x32_bf16 v[124:127],a[184:187],v[240:243],v[124:127]
v_exp_f32 v106,v106
v_mfma_f32_16x16x32_bf16 v[124:127],a[180:183],v[232:235],v[124:127]
v_exp_f32 v107,v107
v_mfma_f32_16x16x32_bf16 v[124:127],a[176:179],v[224:227],v[124:127]
ds_read_b128 a[152:155],v1 offset:4608
v_cvt_pk_bf16_f32 v105,v106,v107
v_mfma_f32_16x16x32_bf16 v[132:135],a[192:195],v[80:83],v[132:135]
v_exp_f32 v108,v108
v_mfma_f32_16x16x32_bf16 v[132:135],a[196:199],v[88:91],v[132:135]
buffer_load_dwordx4 v25,s[48:51],s83 offen offset:1024 lds
v_mfma_f32_16x16x32_bf16 v[140:143],a[200:203],v[80:83],v[140:143]
v_exp_f32 v109,v109
v_mfma_f32_16x16x32_bf16 v[140:143],a[204:207],v[88:91],v[140:143]
ds_read_b128 a[148:151],v1 offset:4352
v_cvt_pk_bf16_f32 v106,v108,v109
v_mfma_f32_16x16x32_bf16 v[148:151],a[208:211],v[80:83],v[148:151]
v_exp_f32 v110,v110
v_mfma_f32_16x16x32_bf16 v[148:151],a[212:215],v[88:91],v[148:151]
v_exp_f32 v111,v111
v_mfma_f32_16x16x32_bf16 v[156:159],a[216:219],v[80:83],v[156:159]
ds_read_b128 a[144:147],v1 offset:4096
v_cvt_pk_bf16_f32 v107,v110,v111
v_mfma_f32_16x16x32_bf16 v[156:159],a[220:223],v[88:91],v[156:159]
v_exp_f32 v112,v112
v_mfma_f32_16x16x32_bf16 v[164:167],a[224:227],v[80:83],v[164:167]
v_exp_f32 v113,v113
v_mfma_f32_16x16x32_bf16 v[164:167],a[228:231],v[88:91],v[164:167]
buffer_store_dwordx4 a[68:71],v6,s[40:43],s79 offen sc1
v_mfma_f32_16x16x32_bf16 v[172:175],a[232:235],v[80:83],v[172:175]
s_add_u32 s79,s79,65536
ds_read_b128 a[160:163],v1 offset:8192
v_cvt_pk_bf16_f32 v112,v112,v113
v_mfma_f32_16x16x32_bf16 v[172:175],a[236:239],v[88:91],v[172:175]
buffer_load_dwordx4 v26,s[48:51],s83 offen offset:2048 lds
v_mfma_f32_16x16x32_bf16 v[180:183],a[240:243],v[80:83],v[180:183]
v_exp_f32 v114,v114
v_mfma_f32_16x16x32_bf16 v[180:183],a[244:247],v[88:91],v[180:183]
v_exp_f32 v115,v115
v_mfma_f32_16x16x32_bf16 v[188:191],a[248:251],v[80:83],v[188:191]
ds_read_b128 a[164:167],v1 offset:8448
v_cvt_pk_bf16_f32 v113,v114,v115
v_mfma_f32_16x16x32_bf16 v[188:191],a[252:255],v[88:91],v[188:191]
v_exp_f32 v116,v116
v_mfma_f32_16x16x32_bf16 v[52:55],v[28:31],v[80:83],v[52:55]
ds_read_b128 a[168:171],v1 offset:8704
v_mfma_f32_16x16x32_bf16 v[52:55],v[28:31],v[88:91],v[52:55]
s_waitcnt lgkmcnt(3)
v_exp_f32 v117,v117
v_mfma_f32_16x16x32_bf16 v[64:67],a[128:131],v[128:131],v[32:35]
ds_read_b128 a[172:175],v1 offset:8960
v_cvt_pk_bf16_f32 v114,v116,v117
v_mfma_f32_16x16x32_bf16 v[64:67],a[132:135],v[136:139],v[64:67]
v_exp_f32 v118,v118
v_mfma_f32_16x16x32_bf16 v[64:67],a[136:139],v[144:147],v[64:67]
ds_read_b128 a[188:191],v1 offset:13056
v_mfma_f32_16x16x32_bf16 v[64:67],a[140:143],v[152:155],v[64:67]
buffer_load_dwordx4 v27,s[48:51],s83 offen offset:3072 lds
)A"
R"A(v_mfma_f32_16x16x32_bf16 v[68:71],a[156:159],v[152:155],v[32:35]
ds_read_b128 a[184:187],v1 offset:12800
v_mfma_f32_16x16x32_bf16 v[68:71],a[152:155],v[144:147],v[68:71]
v_exp_f32 v119,v119
v_mfma_f32_16x16x32_bf16 v[68:71],a[148:151],v[136:139],v[68:71]
ds_read_b128 a[180:183],v1 offset:12544
v_cvt_pk_bf16_f32 v115,v118,v119
v_mfma_f32_16x16x32_bf16 v[68:71],a[144:147],v[128:131],v[68:71]
s_waitcnt lgkmcnt(4)
v_exp_f32 v120,v120
v_mfma_f32_16x16x32_bf16 v[72:75],a[160:163],v[128:131],v[32:35]
buffer_store_dwordx4 a[72:75],v6,s[40:43],s80 offen offset:2048 sc1
v_mfma_f32_16x16x32_bf16 v[72:75],a[164:167],v[136:139],v[72:75]
ds_read_b128 a[176:179],v1 offset:12288
v_add_u32 v1,s78,v1
v_mfma_f32_16x16x32_bf16 v[72:75],a[168:171],v[144:147],v[72:75]
s_waitcnt lgkmcnt(3)
v_exp_f32 v121,v121
v_mfma_f32_16x16x32_bf16 v[72:75],a[172:175],v[152:155],v[72:75]
s_add_u32 m0,s86,86016
v_exp_f32 v122,v122
v_mfma_f32_16x16x32_bf16 v[76:79],a[188:191],v[152:155],v[32:35]
s_waitcnt lgkmcnt(0)
v_exp_f32 v123,v123
v_mfma_f32_16x16x32_bf16 v[76:79],a[184:187],v[144:147],v[76:79]
buffer_load_dwordx4 v7,s[48:51],s83 offen lds
v_mfma_f32_16x16x32_bf16 v[76:79],a[180:183],v[136:139],v[76:79]
s_add_u32 s83,s64,1
v_exp_f32 v124,v124
v_mfma_f32_16x16x32_bf16 v[76:79],a[176:179],v[128:131],v[76:79]
s_and_b32 s83,s83,3
v_exp_f32 v125,v125
v_mfma_f32_16x16x32_bf16 v[196:199],a[192:195],v[96:99],v[196:199]
s_lshl_b32 s83,s83,1
v_exp_f32 v126,v126
v_mfma_f32_16x16x32_bf16 v[196:199],a[196:199],v[104:107],v[196:199]
s_mul_i32 s83,s83,163840
v_exp_f32 v127,v127
v_mfma_f32_16x16x32_bf16 v[204:207],a[200:203],v[96:99],v[204:207]
s_add_u32 s83,s83,s85
v_cvt_pk_bf16_f32 v120,v120,v121
v_cvt_pk_bf16_f32 v121,v122,v123
v_mfma_f32_16x16x32_bf16 v[204:207],a[204:207],v[104:107],v[204:207]
v_cvt_pk_bf16_f32 v122,v124,v125
v_cvt_pk_bf16_f32 v123,v126,v127
v_mfma_f32_16x16x32_bf16 v[212:215],a[208:211],v[96:99],v[212:215]
v_exp_f32 v64,v64
v_mfma_f32_16x16x32_bf16 v[212:215],a[212:215],v[104:107],v[212:215]
s_add_u32 m0,s86,87040
v_exp_f32 v65,v65
v_mfma_f32_16x16x32_bf16 v[220:223],a[216:219],v[96:99],v[220:223]
v_exp_f32 v66,v66
v_mfma_f32_16x16x32_bf16 v[220:223],a[220:223],v[104:107],v[220:223]
buffer_load_dwordx4 v24,s[48:51],s84 offen lds
v_mfma_f32_16x16x32_bf16 v[228:231],a[224:227],v[96:99],v[228:231]
v_exp_f32 v67,v67
v_mfma_f32_16x16x32_bf16 v[228:231],a[228:231],v[104:107],v[228:231]
buffer_store_dwordx4 a[76:79],v6,s[40:43],s80 offen sc1
v_mfma_f32_16x16x32_bf16 v[236:239],a[232:235],v[96:99],v[236:239]
s_add_u32 s80,s80,65536
v_exp_f32 v68,v68
v_mfma_f32_16x16x32_bf16 v[236:239],a[236:239],v[104:107],v[236:239]
v_exp_f32 v69,v69
v_mfma_f32_16x16x32_bf16 v[244:247],a[240:243],v[96:99],v[244:247]
v_exp_f32 v70,v70
v_mfma_f32_16x16x32_bf16 v[244:247],a[244:247],v[104:107],v[244:247]
v_exp_f32 v71,v71
v_mfma_f32_16x16x32_bf16 v[252:255],a[248:251],v[96:99],v[252:255]
v_cvt_pk_bf16_f32 v64,v64,v65
v_cvt_pk_bf16_f32 v65,v66,v67
v_mfma_f32_16x16x32_bf16 v[252:255],a[252:255],v[104:107],v[252:255]
v_cvt_pk_bf16_f32 v66,v68,v69
v_cvt_pk_bf16_f32 v67,v70,v71
v_mfma_f32_16x16x32_bf16 v[56:59],v[28:31],v[96:99],v[56:59]
v_exp_f32 v72,v72
v_mfma_f32_16x16x32_bf16 v[56:59],v[28:31],v[104:107],v[56:59]
buffer_load_dwordx4 v25,s[48:51],s84 offen offset:1024 lds
v_mfma_f32_16x16x32_bf16 v[80:83],a[128:131],v[160:163],v[36:39]
v_exp_f32 v73,v73
v_mfma_f32_16x16x32_bf16 v[80:83],a[132:135],v[168:171],v[80:83]
v_exp_f32 v74,v74
v_mfma_f32_16x16x32_bf16 v[80:83],a[136:139],v[176:179],v[80:83]
v_exp_f32 v75,v75
v_mfma_f32_16x16x32_bf16 v[80:83],a[140:143],v[184:187],v[80:83]
v_exp_f32 v76,v76
v_mfma_f32_16x16x32_bf16 v[84:87],a[156:159],v[184:187],v[36:39]
v_exp_f32 v77,v77
v_mfma_f32_16x16x32_bf16 v[84:87],a[152:155],v[176:179],v[84:87]
v_exp_f32 v78,v78
v_mfma_f32_16x16x32_bf16 v[84:87],a[148:151],v[168:171],v[84:87]
v_exp_f32 v79,v79
v_mfma_f32_16x16x32_bf16 v[84:87],a[144:147],v[160:163],v[84:87]
v_cvt_pk_bf16_f32 v72,v72,v73
v_cvt_pk_bf16_f32 v73,v74,v75
v_mfma_f32_16x16x32_bf16 v[88:91],a[160:163],v[160:163],v[36:39]
buffer_store_dwordx4 a[80:83],v6,s[40:43],s81 offen offset:2048 sc1
v_mfma_f32_16x16x32_bf16 v[88:91],a[164:167],v[168:171],v[88:91]
v_cvt_pk_bf16_f32 v74,v76,v77
v_cvt_pk_bf16_f32 v75,v78,v79
v_mfma_f32_16x16x32_bf16 v[88:91],a[168:171],v[176:179],v[88:91]
buffer_load_dwordx4 v26,s[48:51],s84 offen offset:2048 lds
v_mfma_f32_16x16x32_bf16 v[88:91],a[172:175],v[184:187],v[88:91]
v_exp_f32 v80,v80
v_mfma_f32_16x16x32_bf16 v[92:95],a[188:191],v[184:187],v[36:39]
v_exp_f32 v81,v81
v_mfma_f32_16x16x32_bf16 v[92:95],a[184:187],v[176:179],v[92:95]
v_exp_f32 v82,v82
v_mfma_f32_16x16x32_bf16 v[92:95],a[180:183],v[168:171],v[92:95]
v_exp_f32 v83,v83
v_mfma_f32_16x16x32_bf16 v[92:95],a[176:179],v[160:163],v[92:95]
v_exp_f32 v84,v84
v_mfma_f32_16x16x32_bf16 a[32:35],a[192:195],v[112:115],a[32:35]
ds_read_b64_tr_b16 a[192:193],v2
ds_read_b64_tr_b16 a[194:195],v2 offset:4096
v_mfma_f32_16x16x32_bf16 a[32:35],a[196:199],v[120:123],a[32:35]
ds_read_b64_tr_b16 a[196:197],v2 offset:8192
v_cvt_pk_bf16_f32 v80,v80,v81
v_mfma_f32_16x16x32_bf16 a[36:39],a[200:203],v[112:115],a[36:39]
ds_read_b64_tr_b16 a[198:199],v2 offset:12288
v_cvt_pk_bf16_f32 v81,v82,v83
v_mfma_f32_16x16x32_bf16 a[36:39],a[204:207],v[120:123],a[36:39]
buffer_load_dwordx4 v27,s[48:51],s84 offen offset:3072 lds
v_mfma_f32_16x16x32_bf16 a[40:43],a[208:211],v[112:115],a[40:43]
ds_read_b64_tr_b16 a[200:201],v3
ds_read_b64_tr_b16 a[202:203],v3 offset:4096
v_mfma_f32_16x16x32_bf16 a[40:43],a[212:215],v[120:123],a[40:43]
v_exp_f32 v85,v85
v_mfma_f32_16x16x32_bf16 a[44:47],a[216:219],v[112:115],a[44:47]
ds_read_b64_tr_b16 a[204:205],v3 offset:8192
ds_read_b64_tr_b16 a[206:207],v3 offset:12288
v_mfma_f32_16x16x32_bf16 a[44:47],a[220:223],v[120:123],a[44:47]
ds_read_b64_tr_b16 a[208:209],v2 offset:256
v_cvt_pk_bf16_f32 v82,v84,v85
v_mfma_f32_16x16x32_bf16 a[48:51],a[224:227],v[112:115],a[48:51]
v_exp_f32 v86,v86
v_mfma_f32_16x16x32_bf16 a[48:51],a[228:231],v[120:123],a[48:51]
buffer_store_dwordx4 a[84:87],v6,s[40:43],s81 offen sc1
v_mfma_f32_16x16x32_bf16 a[52:55],a[232:235],v[112:115],a[52:55]
s_add_u32 s81,s81,65536
ds_read_b64_tr_b16 a[210:211],v2 offset:4352
ds_read_b64_tr_b16 a[212:213],v2 offset:8448
v_mfma_f32_16x16x32_bf16 a[52:55],a[236:239],v[120:123],a[52:55]
s_add_u32 m0,s86,91136
v_exp_f32 v87,v87
v_mfma_f32_16x16x32_bf16 a[56:59],a[240:243],v[112:115],a[56:59]
ds_read_b64_tr_b16 a[214:215],v2 offset:12544
ds_read_b64_tr_b16 a[216:217],v3 offset:256
v_mfma_f32_16x16x32_bf16 a[56:59],a[244:247],v[120:123],a[56:59]
buffer_load_dwordx4 v7,s[48:51],s84 offen lds
v_mfma_f32_16x16x32_bf16 a[60:63],a[248:251],v[112:115],a[60:63]
s_add_u32 s84,s83,20480
ds_read_b64_tr_b16 a[218:219],v3 offset:4352
ds_read_b64_tr_b16 a[220:221],v3 offset:8448
v_mfma_f32_16x16x32_bf16 a[60:63],a[252:255],v[120:123],a[60:63]
ds_read_b64_tr_b16 a[222:223],v3 offset:12544
v_cvt_pk_bf16_f32 v83,v86,v87
v_mfma_f32_16x16x32_bf16 v[60:63],v[28:31],v[112:115],v[60:63]
v_exp_f32 v88,v88
v_mfma_f32_16x16x32_bf16 v[60:63],v[28:31],v[120:123],v[60:63]
ds_read_b64_tr_b16 a[224:225],v2 offset:512
ds_read_b64_tr_b16 a[226:227],v2 offset:4608
v_mfma_f32_16x16x32_bf16 v[96:99],a[128:131],v[192:195],v[40:43]
v_exp_f32 v89,v89
v_mfma_f32_16x16x32_bf16 v[96:99],a[132:135],v[200:203],v[96:99]
ds_read_b64_tr_b16 a[228:229],v2 offset:8704
ds_read_b64_tr_b16 a[230:231],v2 offset:12800
v_mfma_f32_16x16x32_bf16 v[96:99],a[136:139],v[208:211],v[96:99]
ds_read_b64_tr_b16 a[232:233],v3 offset:512
v_cvt_pk_bf16_f32 v88,v88,v89
v_mfma_f32_16x16x32_bf16 v[96:99],a[140:143],v[216:219],v[96:99]
v_exp_f32 v90,v90
v_mfma_f32_16x16x32_bf16 v[100:103],a[156:159],v[216:219],v[40:43]
ds_read_b64_tr_b16 a[234:235],v3 offset:4608
ds_read_b64_tr_b16 a[236:237],v3 offset:8704
v_mfma_f32_16x16x32_bf16 v[100:103],a[152:155],v[208:211],v[100:103]
v_exp_f32 v91,v91
v_mfma_f32_16x16x32_bf16 v[100:103],a[148:151],v[200:203],v[100:103]
ds_read_b64_tr_b16 a[238:239],v3 offset:12800
ds_read_b64_tr_b16 a[240:241],v2 offset:768
v_mfma_f32_16x16x32_bf16 v[100:103],a[144:147],v[192:195],v[100:103]
ds_read_b64_tr_b16 a[242:243],v2 offset:4864
v_cvt_pk_bf16_f32 v89,v90,v91
v_mfma_f32_16x16x32_bf16 v[104:107],a[160:163],v[192:195],v[40:43]
buffer_store_dwordx4 a[88:91],v6,s[40:43],s82 offen offset:2048 sc1
v_mfma_f32_16x16x32_bf16 v[104:107],a[164:167],v[200:203],v[104:107]
ds_read_b64_tr_b16 a[244:245],v2 offset:8960
ds_read_b64_tr_b16 a[246:247],v2 offset:13056
v_mfma_f32_16x16x32_bf16 v[104:107],a[168:171],v[208:211],v[104:107]
ds_read_b64_tr_b16 a[248:249],v3 offset:768
v_add_u32 v2,s78,v2
v_mfma_f32_16x16x32_bf16 v[104:107],a[172:175],v[216:219],v[104:107]
v_exp_f32 v92,v92
v_mfma_f32_16x16x32_bf16 v[108:111],a[188:191],v[216:219],v[40:43]
ds_read_b64_tr_b16 a[250:251],v3 offset:4864
ds_read_b64_tr_b16 a[252:253],v3 offset:8960
v_mfma_f32_16x16x32_bf16 v[108:111],a[184:187],v[208:211],v[108:111]
s_waitcnt lgkmcnt(5)
ds_read_b64_tr_b16 a[254:255],v3 offset:13056
v_add_u32 v3,s78,v3
v_mfma_f32_16x16x32_bf16 v[108:111],a[180:183],v[200:203],v[108:111]
s_sub_u32 s78,s77,32768
v_exp_f32 v93,v93
v_mfma_f32_16x16x32_bf16 v[108:111],a[176:179],v[192:195],v[108:111]
s_and_b32 s78,s78,0x1ffff
v_exp_f32 v94,v94
v_mfma_f32_16x16x32_bf16 a[0:3],a[192:195],v[64:67],a[0:3]
s_sub_u32 s78,s78,s77
v_exp_f32 v95,v95
v_mfma_f32_16x16x32_bf16 a[0:3],a[196:199],v[72:75],a[0:3]
s_add_u32 s77,s77,s78
v_cvt_pk_bf16_f32 v90,v92,v93
v_cvt_pk_bf16_f32 v91,v94,v95
v_mfma_f32_16x16x32_bf16 a[4:7],a[200:203],v[64:67],a[4:7]
s_cmp_ge_u32 s63,2
s_cselect_b32 s6,L_gbS_%=-L_pcg_%=,s6
s_cselect_b32 s10,L_geS_%=-L_gbS_%=,s10
v_mfma_f32_16x16x32_bf16 a[4:7],a[204:207],v[72:75],a[4:7]
s_add_u32 s8,s8,s6
s_addc_u32 s9,s9,0
s_and_b32 s8,s8,0xffffff80
v_mfma_f32_16x16x32_bf16 a[8:11],a[208:211],v[64:67],a[8:11]
s_and_b32 s9,s9,0xffff
v_exp_f32 v96,v96
v_mfma_f32_16x16x32_bf16 a[8:11],a[212:215],v[72:75],a[8:11]
v_exp_f32 v97,v97
v_mfma_f32_16x16x32_bf16 a[12:15],a[216:219],v[64:67],a[12:15]
v_exp_f32 v98,v98
v_mfma_f32_16x16x32_bf16 a[12:15],a[220:223],v[72:75],a[12:15]
v_exp_f32 v99,v99
v_mfma_f32_16x16x32_bf16 a[16:19],a[224:227],v[64:67],a[16:19]
v_exp_f32 v100,v100
v_mfma_f32_16x16x32_bf16 a[16:19],a[228:231],v[72:75],a[16:19]
buffer_store_dwordx4 a[92:95],v6,s[40:43],s82 offen sc1
v_mfma_f32_16x16x32_bf16 a[20:23],a[232:235],v[64:67],a[20:23]
s_add_u32 s82,s82,65536
v_exp_f32 v101,v101
v_mfma_f32_16x16x32_bf16 a[20:23],a[236:239],v[72:75],a[20:23]
v_exp_f32 v102,v102
v_mfma_f32_16x16x32_bf16 a[24:27],a[240:243],v[64:67],a[24:27]
s_waitcnt lgkmcnt(0)
v_exp_f32 v103,v103
v_mfma_f32_16x16x32_bf16 a[24:27],a[244:247],v[72:75],a[24:27]
v_cvt_pk_bf16_f32 v96,v96,v97
v_cvt_pk_bf16_f32 v97,v98,v99
v_mfma_f32_16x16x32_bf16 a[28:31],a[248:251],v[64:67],a[28:31]
v_cvt_pk_bf16_f32 v98,v100,v101
v_cvt_pk_bf16_f32 v99,v102,v103
v_mfma_f32_16x16x32_bf16 a[28:31],a[252:255],v[72:75],a[28:31]
v_exp_f32 v104,v104
v_mfma_f32_16x16x32_bf16 v[48:51],v[28:31],v[64:67],v[48:51]
v_exp_f32 v105,v105
v_mfma_f32_16x16x32_bf16 v[48:51],v[28:31],v[72:75],v[48:51]
v_exp_f32 v106,v106
s_waitcnt vmcnt(36)
s_barrier
v_mfma_f32_16x16x32_bf16 v[112:115],a[128:131],v[224:227],v[44:47]
ds_read_b128 a[128:131],v1
v_cvt_pk_bf16_f32 v104,v104,v105
v_mfma_f32_16x16x32_bf16 v[112:115],a[132:135],v[232:235],v[112:115]
v_exp_f32 v107,v107
v_mfma_f32_16x16x32_bf16 v[112:115],a[136:139],v[240:243],v[112:115]
ds_read_b128 a[132:135],v1 offset:256
v_cvt_pk_bf16_f32 v105,v106,v107
v_mfma_f32_16x16x32_bf16 v[112:115],a[140:143],v[248:251],v[112:115]
v_exp_f32 v108,v108
v_mfma_f32_16x16x32_bf16 v[116:119],a[156:159],v[248:251],v[44:47]
v_exp_f32 v109,v109
v_mfma_f32_16x16x32_bf16 v[116:119],a[152:155],v[240:243],v[116:119]
s_add_u32 m0,s86,40960
ds_read_b128 a[136:139],v1 offset:512
v_cvt_pk_bf16_f32 v106,v108,v109
v_mfma_f32_16x16x32_bf16 v[116:119],a[148:151],v[232:235],v[116:119]
v_exp_f32 v110,v110
v_mfma_f32_16x16x32_bf16 v[116:119],a[144:147],v[224:227],v[116:119]
buffer_load_dwordx4 v24,s[48:51],s83 offen lds
v_mfma_f32_16x16x32_bf16 v[120:123],a[160:163],v[224:227],v[44:47]
)A"
R"A(v_exp_f32 v111,v111
v_mfma_f32_16x16x32_bf16 v[120:123],a[164:167],v[232:235],v[120:123]
ds_read_b128 a[140:143],v1 offset:768
v_cvt_pk_bf16_f32 v107,v110,v111
v_mfma_f32_16x16x32_bf16 v[120:123],a[168:171],v[240:243],v[120:123]
v_exp_f32 v112,v112
v_mfma_f32_16x16x32_bf16 v[120:123],a[172:175],v[248:251],v[120:123]
v_exp_f32 v113,v113
v_mfma_f32_16x16x32_bf16 v[124:127],a[188:191],v[248:251],v[44:47]
ds_read_b128 a[156:159],v1 offset:4864
v_cvt_pk_bf16_f32 v112,v112,v113
v_mfma_f32_16x16x32_bf16 v[124:127],a[184:187],v[240:243],v[124:127]
v_exp_f32 v114,v114
v_mfma_f32_16x16x32_bf16 v[124:127],a[180:183],v[232:235],v[124:127]
v_exp_f32 v115,v115
v_mfma_f32_16x16x32_bf16 v[124:127],a[176:179],v[224:227],v[124:127]
ds_read_b128 a[152:155],v1 offset:4608
v_cvt_pk_bf16_f32 v113,v114,v115
v_mfma_f32_16x16x32_bf16 v[132:135],a[192:195],v[80:83],v[132:135]
v_exp_f32 v116,v116
v_mfma_f32_16x16x32_bf16 v[132:135],a[196:199],v[88:91],v[132:135]
buffer_load_dwordx4 v25,s[48:51],s83 offen offset:1024 lds
v_mfma_f32_16x16x32_bf16 v[140:143],a[200:203],v[80:83],v[140:143]
L_pd12_%=:
s_cbranch_execz L_trt1_%=
v_exp_f32 v117,v117
v_mfma_f32_16x16x32_bf16 v[140:143],a[204:207],v[88:91],v[140:143]
ds_read_b128 a[148:151],v1 offset:4352
v_cvt_pk_bf16_f32 v114,v116,v117
v_mfma_f32_16x16x32_bf16 v[148:151],a[208:211],v[80:83],v[148:151]
v_exp_f32 v118,v118
v_mfma_f32_16x16x32_bf16 v[148:151],a[212:215],v[88:91],v[148:151]
v_exp_f32 v119,v119
v_mfma_f32_16x16x32_bf16 v[156:159],a[216:219],v[80:83],v[156:159]
ds_read_b128 a[144:147],v1 offset:4096
v_cvt_pk_bf16_f32 v115,v118,v119
v_mfma_f32_16x16x32_bf16 v[156:159],a[220:223],v[88:91],v[156:159]
v_exp_f32 v120,v120
v_mfma_f32_16x16x32_bf16 v[164:167],a[224:227],v[80:83],v[164:167]
buffer_load_dword v5,v4,s[8:11],s7 offen
v_mfma_f32_16x16x32_bf16 v[164:167],a[228:231],v[88:91],v[164:167]
v_exp_f32 v121,v121
v_mfma_f32_16x16x32_bf16 v[172:175],a[232:235],v[80:83],v[172:175]
ds_read_b128 a[160:163],v1 offset:8192
v_cvt_pk_bf16_f32 v120,v120,v121
v_mfma_f32_16x16x32_bf16 v[172:175],a[236:239],v[88:91],v[172:175]
v_exp_f32 v122,v122
v_mfma_f32_16x16x32_bf16 v[180:183],a[240:243],v[80:83],v[180:183]
v_exp_f32 v123,v123
v_mfma_f32_16x16x32_bf16 v[180:183],a[244:247],v[88:91],v[180:183]
ds_read_b128 a[164:167],v1 offset:8448
v_cvt_pk_bf16_f32 v121,v122,v123
v_mfma_f32_16x16x32_bf16 v[188:191],a[248:251],v[80:83],v[188:191]
buffer_load_dwordx4 v26,s[48:51],s83 offen offset:2048 lds
v_mfma_f32_16x16x32_bf16 v[188:191],a[252:255],v[88:91],v[188:191]
v_exp_f32 v124,v124
v_mfma_f32_16x16x32_bf16 v[52:55],v[28:31],v[80:83],v[52:55]
ds_read_b128 a[168:171],v1 offset:8704
v_mfma_f32_16x16x32_bf16 v[52:55],v[28:31],v[88:91],v[52:55]
s_waitcnt lgkmcnt(3)
v_exp_f32 v125,v125
v_mfma_f32_16x16x32_bf16 v[64:67],a[128:131],v[128:131],v[32:35]
ds_read_b128 a[172:175],v1 offset:8960
v_cvt_pk_bf16_f32 v122,v124,v125
v_mfma_f32_16x16x32_bf16 v[64:67],a[132:135],v[136:139],v[64:67]
v_exp_f32 v126,v126
v_mfma_f32_16x16x32_bf16 v[64:67],a[136:139],v[144:147],v[64:67]
ds_read_b128 a[188:191],v1 offset:13056
v_mfma_f32_16x16x32_bf16 v[64:67],a[140:143],v[152:155],v[64:67]
buffer_load_dwordx4 v27,s[48:51],s83 offen offset:3072 lds
v_mfma_f32_16x16x32_bf16 v[68:71],a[156:159],v[152:155],v[32:35]
ds_read_b128 a[184:187],v1 offset:12800
v_mfma_f32_16x16x32_bf16 v[68:71],a[152:155],v[144:147],v[68:71]
v_exp_f32 v127,v127
v_mfma_f32_16x16x32_bf16 v[68:71],a[148:151],v[136:139],v[68:71]
ds_read_b128 a[180:183],v1 offset:12544
v_cvt_pk_bf16_f32 v123,v126,v127
v_mfma_f32_16x16x32_bf16 v[68:71],a[144:147],v[128:131],v[68:71]
s_waitcnt lgkmcnt(4)
v_exp_f32 v64,v64
v_mfma_f32_16x16x32_bf16 v[72:75],a[160:163],v[128:131],v[32:35]
ds_read_b128 a[176:179],v1 offset:12288
v_add_u32 v1,s78,v1
v_mfma_f32_16x16x32_bf16 v[72:75],a[164:167],v[136:139],v[72:75]
v_exp_f32 v65,v65
v_mfma_f32_16x16x32_bf16 v[72:75],a[168:171],v[144:147],v[72:75]
s_waitcnt lgkmcnt(3)
v_exp_f32 v66,v66
v_mfma_f32_16x16x32_bf16 v[72:75],a[172:175],v[152:155],v[72:75]
s_add_u32 m0,s86,45056
v_exp_f32 v67,v67
v_mfma_f32_16x16x32_bf16 v[76:79],a[188:191],v[152:155],v[32:35]
s_waitcnt lgkmcnt(0)
v_exp_f32 v68,v68
v_mfma_f32_16x16x32_bf16 v[76:79],a[184:187],v[144:147],v[76:79]
buffer_load_dwordx4 v7,s[48:51],s83 offen lds
v_mfma_f32_16x16x32_bf16 v[76:79],a[180:183],v[136:139],v[76:79]
s_add_u32 s83,s64,2
v_exp_f32 v69,v69
v_mfma_f32_16x16x32_bf16 v[76:79],a[176:179],v[128:131],v[76:79]
s_and_b32 s83,s83,3
v_exp_f32 v70,v70
v_mfma_f32_16x16x32_bf16 v[196:199],a[192:195],v[96:99],v[196:199]
s_lshl_b32 s83,s83,1
v_exp_f32 v71,v71
v_mfma_f32_16x16x32_bf16 v[196:199],a[196:199],v[104:107],v[196:199]
s_mul_i32 s83,s83,163840
v_cvt_pk_bf16_f32 v64,v64,v65
v_cvt_pk_bf16_f32 v65,v66,v67
v_mfma_f32_16x16x32_bf16 v[204:207],a[200:203],v[96:99],v[204:207]
s_add_u32 s83,s83,s85
v_cvt_pk_bf16_f32 v66,v68,v69
v_cvt_pk_bf16_f32 v67,v70,v71
v_mfma_f32_16x16x32_bf16 v[204:207],a[204:207],v[104:107],v[204:207]
v_exp_f32 v72,v72
v_mfma_f32_16x16x32_bf16 v[212:215],a[208:211],v[96:99],v[212:215]
v_exp_f32 v73,v73
v_mfma_f32_16x16x32_bf16 v[212:215],a[212:215],v[104:107],v[212:215]
s_add_u32 m0,s86,46080
v_exp_f32 v74,v74
v_mfma_f32_16x16x32_bf16 v[220:223],a[216:219],v[96:99],v[220:223]
v_exp_f32 v75,v75
v_mfma_f32_16x16x32_bf16 v[220:223],a[220:223],v[104:107],v[220:223]
buffer_load_dwordx4 v24,s[48:51],s84 offen lds
v_mfma_f32_16x16x32_bf16 v[228:231],a[224:227],v[96:99],v[228:231]
v_exp_f32 v76,v76
v_mfma_f32_16x16x32_bf16 v[228:231],a[228:231],v[104:107],v[228:231]
v_exp_f32 v77,v77
v_mfma_f32_16x16x32_bf16 v[236:239],a[232:235],v[96:99],v[236:239]
v_exp_f32 v78,v78
v_mfma_f32_16x16x32_bf16 v[236:239],a[236:239],v[104:107],v[236:239]
v_exp_f32 v79,v79
v_mfma_f32_16x16x32_bf16 v[244:247],a[240:243],v[96:99],v[244:247]
v_cvt_pk_bf16_f32 v72,v72,v73
v_cvt_pk_bf16_f32 v73,v74,v75
v_mfma_f32_16x16x32_bf16 v[244:247],a[244:247],v[104:107],v[244:247]
v_cvt_pk_bf16_f32 v74,v76,v77
v_cvt_pk_bf16_f32 v75,v78,v79
v_mfma_f32_16x16x32_bf16 v[252:255],a[248:251],v[96:99],v[252:255]
v_lshrrev_b32 v1,6,v6
v_mul_u32_u24 v1,320,v1
v_mfma_f32_16x16x32_bf16 v[252:255],a[252:255],v[104:107],v[252:255]
v_mfma_f32_16x16x32_bf16 v[56:59],v[28:31],v[96:99],v[56:59]
v_mfma_f32_16x16x32_bf16 v[56:59],v[28:31],v[104:107],v[56:59]
buffer_load_dwordx4 v25,s[48:51],s84 offen offset:1024 lds
v_mfma_f32_16x16x32_bf16 v[80:83],a[128:131],v[160:163],v[36:39]
v_mfma_f32_16x16x32_bf16 v[80:83],a[132:135],v[168:171],v[80:83]
v_mfma_f32_16x16x32_bf16 v[80:83],a[136:139],v[176:179],v[80:83]
v_mfma_f32_16x16x32_bf16 v[80:83],a[140:143],v[184:187],v[80:83]
v_mfma_f32_16x16x32_bf16 v[84:87],a[156:159],v[184:187],v[36:39]
v_mfma_f32_16x16x32_bf16 v[84:87],a[152:155],v[176:179],v[84:87]
v_mfma_f32_16x16x32_bf16 v[84:87],a[148:151],v[168:171],v[84:87]
v_mfma_f32_16x16x32_bf16 v[84:87],a[144:147],v[160:163],v[84:87]
v_mfma_f32_16x16x32_bf16 v[88:91],a[160:163],v[160:163],v[36:39]
v_mfma_f32_16x16x32_bf16 v[88:91],a[164:167],v[168:171],v[88:91]
buffer_load_dwordx4 v26,s[48:51],s84 offen offset:2048 lds
v_mfma_f32_16x16x32_bf16 v[88:91],a[168:171],v[176:179],v[88:91]
v_exp_f32 v80,v80
v_mfma_f32_16x16x32_bf16 v[88:91],a[172:175],v[184:187],v[88:91]
v_exp_f32 v81,v81
v_mfma_f32_16x16x32_bf16 v[92:95],a[188:191],v[184:187],v[36:39]
v_exp_f32 v82,v82
v_mfma_f32_16x16x32_bf16 v[92:95],a[184:187],v[176:179],v[92:95]
v_exp_f32 v83,v83
v_mfma_f32_16x16x32_bf16 v[92:95],a[180:183],v[168:171],v[92:95]
v_exp_f32 v84,v84
v_mfma_f32_16x16x32_bf16 v[92:95],a[176:179],v[160:163],v[92:95]
v_exp_f32 v85,v85
v_mfma_f32_16x16x32_bf16 a[32:35],a[192:195],v[112:115],a[32:35]
ds_read_b64_tr_b16 a[192:193],v2
ds_read_b64_tr_b16 a[194:195],v2 offset:4096
v_mfma_f32_16x16x32_bf16 a[32:35],a[196:199],v[120:123],a[32:35]
ds_read_b64_tr_b16 a[196:197],v2 offset:8192
v_cvt_pk_bf16_f32 v80,v80,v81
v_mfma_f32_16x16x32_bf16 a[36:39],a[200:203],v[112:115],a[36:39]
ds_read_b64_tr_b16 a[198:199],v2 offset:12288
v_cvt_pk_bf16_f32 v81,v82,v83
v_mfma_f32_16x16x32_bf16 a[36:39],a[204:207],v[120:123],a[36:39]
buffer_load_dwordx4 v27,s[48:51],s84 offen offset:3072 lds
v_mfma_f32_16x16x32_bf16 a[40:43],a[208:211],v[112:115],a[40:43]
ds_read_b64_tr_b16 a[200:201],v3
ds_read_b64_tr_b16 a[202:203],v3 offset:4096
v_mfma_f32_16x16x32_bf16 a[40:43],a[212:215],v[120:123],a[40:43]
ds_read_b64_tr_b16 a[204:205],v3 offset:8192
v_cvt_pk_bf16_f32 v82,v84,v85
v_mfma_f32_16x16x32_bf16 a[44:47],a[216:219],v[112:115],a[44:47]
v_exp_f32 v86,v86
v_mfma_f32_16x16x32_bf16 a[44:47],a[220:223],v[120:123],a[44:47]
ds_read_b64_tr_b16 a[206:207],v3 offset:12288
ds_read_b64_tr_b16 a[208:209],v2 offset:256
v_mfma_f32_16x16x32_bf16 a[48:51],a[224:227],v[112:115],a[48:51]
v_exp_f32 v87,v87
v_mfma_f32_16x16x32_bf16 a[48:51],a[228:231],v[120:123],a[48:51]
ds_read_b64_tr_b16 a[210:211],v2 offset:4352
ds_read_b64_tr_b16 a[212:213],v2 offset:8448
v_mfma_f32_16x16x32_bf16 a[52:55],a[232:235],v[112:115],a[52:55]
ds_read_b64_tr_b16 a[214:215],v2 offset:12544
v_cvt_pk_bf16_f32 v83,v86,v87
v_mfma_f32_16x16x32_bf16 a[52:55],a[236:239],v[120:123],a[52:55]
s_add_u32 m0,s86,50176
v_exp_f32 v88,v88
v_mfma_f32_16x16x32_bf16 a[56:59],a[240:243],v[112:115],a[56:59]
ds_read_b64_tr_b16 a[216:217],v3 offset:256
ds_read_b64_tr_b16 a[218:219],v3 offset:4352
v_mfma_f32_16x16x32_bf16 a[56:59],a[244:247],v[120:123],a[56:59]
buffer_load_dwordx4 v7,s[48:51],s84 offen lds
v_mfma_f32_16x16x32_bf16 a[60:63],a[248:251],v[112:115],a[60:63]
s_add_u32 s84,s83,20480
ds_read_b64_tr_b16 a[220:221],v3 offset:8448
ds_read_b64_tr_b16 a[222:223],v3 offset:12544
v_mfma_f32_16x16x32_bf16 a[60:63],a[252:255],v[120:123],a[60:63]
v_exp_f32 v89,v89
v_mfma_f32_16x16x32_bf16 v[60:63],v[28:31],v[112:115],v[60:63]
ds_read_b64_tr_b16 a[224:225],v2 offset:512
ds_read_b64_tr_b16 a[226:227],v2 offset:4608
v_mfma_f32_16x16x32_bf16 v[60:63],v[28:31],v[120:123],v[60:63]
ds_read_b64_tr_b16 a[228:229],v2 offset:8704
v_cvt_pk_bf16_f32 v88,v88,v89
v_mfma_f32_16x16x32_bf16 v[96:99],a[128:131],v[192:195],v[40:43]
v_exp_f32 v90,v90
v_mfma_f32_16x16x32_bf16 v[96:99],a[132:135],v[200:203],v[96:99]
ds_read_b64_tr_b16 a[230:231],v2 offset:12800
ds_read_b64_tr_b16 a[232:233],v3 offset:512
v_mfma_f32_16x16x32_bf16 v[96:99],a[136:139],v[208:211],v[96:99]
v_exp_f32 v91,v91
v_mfma_f32_16x16x32_bf16 v[96:99],a[140:143],v[216:219],v[96:99]
ds_read_b64_tr_b16 a[234:235],v3 offset:4608
ds_read_b64_tr_b16 a[236:237],v3 offset:8704
v_mfma_f32_16x16x32_bf16 v[100:103],a[156:159],v[216:219],v[40:43]
ds_read_b64_tr_b16 a[238:239],v3 offset:12800
v_cvt_pk_bf16_f32 v89,v90,v91
v_mfma_f32_16x16x32_bf16 v[100:103],a[152:155],v[208:211],v[100:103]
v_exp_f32 v92,v92
v_mfma_f32_16x16x32_bf16 v[100:103],a[148:151],v[200:203],v[100:103]
ds_read_b64_tr_b16 a[240:241],v2 offset:768
ds_read_b64_tr_b16 a[242:243],v2 offset:4864
v_mfma_f32_16x16x32_bf16 v[100:103],a[144:147],v[192:195],v[100:103]
v_exp_f32 v93,v93
v_mfma_f32_16x16x32_bf16 v[104:107],a[160:163],v[192:195],v[40:43]
ds_read_b64_tr_b16 a[244:245],v2 offset:8960
ds_read_b64_tr_b16 a[246:247],v2 offset:13056
v_mfma_f32_16x16x32_bf16 v[104:107],a[164:167],v[200:203],v[104:107]
ds_read_b64_tr_b16 a[248:249],v3 offset:768
v_cvt_pk_bf16_f32 v90,v92,v93
v_mfma_f32_16x16x32_bf16 v[104:107],a[168:171],v[208:211],v[104:107]
ds_read_b64_tr_b16 a[250:251],v3 offset:4864
v_bfe_u32 v2,v6,4,2
v_mfma_f32_16x16x32_bf16 v[104:107],a[172:175],v[216:219],v[104:107]
ds_read_b64_tr_b16 a[252:253],v3 offset:8960
v_lshl_add_u32 v1,v2,3,v1
v_mfma_f32_16x16x32_bf16 v[108:111],a[188:191],v[216:219],v[40:43]
ds_read_b64_tr_b16 a[254:255],v3 offset:13056
v_add_u32 v1,122880,v1
v_mfma_f32_16x16x32_bf16 v[108:111],a[184:187],v[208:211],v[108:111]
v_exp_f32 v94,v94
v_mfma_f32_16x16x32_bf16 v[108:111],a[180:183],v[200:203],v[108:111]
v_exp_f32 v95,v95
v_mfma_f32_16x16x32_bf16 v[108:111],a[176:179],v[192:195],v[108:111]
s_waitcnt lgkmcnt(6)
v_cvt_pk_bf16_f32 v91,v94,v95
v_bfe_u32 v2,v6,8,1
v_mfma_f32_16x16x32_bf16 a[0:3],a[192:195],v[64:67],a[0:3]
v_exp_f32 v96,v96
v_mfma_f32_16x16x32_bf16 a[0:3],a[196:199],v[72:75],a[0:3]
)A"
R"A(v_exp_f32 v97,v97
v_mfma_f32_16x16x32_bf16 a[4:7],a[200:203],v[64:67],a[4:7]
v_exp_f32 v98,v98
v_mfma_f32_16x16x32_bf16 a[4:7],a[204:207],v[72:75],a[4:7]
v_exp_f32 v99,v99
v_mfma_f32_16x16x32_bf16 a[8:11],a[208:211],v[64:67],a[8:11]
v_exp_f32 v100,v100
v_mfma_f32_16x16x32_bf16 a[8:11],a[212:215],v[72:75],a[8:11]
v_exp_f32 v101,v101
v_mfma_f32_16x16x32_bf16 a[12:15],a[216:219],v[64:67],a[12:15]
v_exp_f32 v102,v102
v_mfma_f32_16x16x32_bf16 a[12:15],a[220:223],v[72:75],a[12:15]
v_exp_f32 v103,v103
v_mfma_f32_16x16x32_bf16 a[16:19],a[224:227],v[64:67],a[16:19]
v_cvt_pk_bf16_f32 v96,v96,v97
v_cvt_pk_bf16_f32 v97,v98,v99
v_mfma_f32_16x16x32_bf16 a[16:19],a[228:231],v[72:75],a[16:19]
v_cvt_pk_bf16_f32 v98,v100,v101
v_cvt_pk_bf16_f32 v99,v102,v103
v_mfma_f32_16x16x32_bf16 a[20:23],a[232:235],v[64:67],a[20:23]
v_exp_f32 v104,v104
v_mfma_f32_16x16x32_bf16 a[20:23],a[236:239],v[72:75],a[20:23]
v_exp_f32 v105,v105
v_mfma_f32_16x16x32_bf16 a[24:27],a[240:243],v[64:67],a[24:27]
s_waitcnt lgkmcnt(0)
v_exp_f32 v106,v106
v_mfma_f32_16x16x32_bf16 a[24:27],a[244:247],v[72:75],a[24:27]
v_exp_f32 v107,v107
v_mfma_f32_16x16x32_bf16 a[28:31],a[248:251],v[64:67],a[28:31]
v_exp_f32 v108,v108
v_mfma_f32_16x16x32_bf16 a[28:31],a[252:255],v[72:75],a[28:31]
v_exp_f32 v109,v109
v_mfma_f32_16x16x32_bf16 v[48:51],v[28:31],v[64:67],v[48:51]
v_exp_f32 v110,v110
v_mfma_f32_16x16x32_bf16 v[48:51],v[28:31],v[72:75],v[48:51]
v_exp_f32 v111,v111
s_waitcnt vmcnt(11)
s_barrier
v_mfma_f32_16x16x32_bf16 v[112:115],a[128:131],v[224:227],v[44:47]
buffer_store_dwordx2 v[8:9],v10,s[44:47],s18 offen offset:256 sc1
v_mfma_f32_16x16x32_bf16 v[112:115],a[132:135],v[232:235],v[112:115]
v_cvt_pk_bf16_f32 v104,v104,v105
v_cvt_pk_bf16_f32 v105,v106,v107
v_mfma_f32_16x16x32_bf16 v[112:115],a[136:139],v[240:243],v[112:115]
v_cvt_pk_bf16_f32 v106,v108,v109
v_cvt_pk_bf16_f32 v107,v110,v111
v_mfma_f32_16x16x32_bf16 v[112:115],a[140:143],v[248:251],v[112:115]
s_add_u32 m0,s86,0
v_cmp_nlt_f32 s[54:55],v48,s73
v_max_f32 v49,0x00800000,v48
v_mfma_f32_16x16x32_bf16 v[116:119],a[156:159],v[248:251],v[44:47]
v_rcp_f32 v49,v49
v_mfma_f32_16x16x32_bf16 v[116:119],a[152:155],v[240:243],v[116:119]
v_lshl_add_u32 v1,v2,5,v1
v_lshlrev_b32 v2,6,v2
v_mfma_f32_16x16x32_bf16 v[116:119],a[148:151],v[232:235],v[116:119]
buffer_load_dwordx4 v24,s[48:51],s83 offen lds
v_mfma_f32_16x16x32_bf16 v[116:119],a[144:147],v[224:227],v[116:119]
v_exp_f32 v112,v112
v_mfma_f32_16x16x32_bf16 v[120:123],a[160:163],v[224:227],v[44:47]
v_exp_f32 v113,v113
v_mfma_f32_16x16x32_bf16 v[120:123],a[164:167],v[232:235],v[120:123]
v_exp_f32 v114,v114
v_mfma_f32_16x16x32_bf16 v[120:123],a[168:171],v[240:243],v[120:123]
v_exp_f32 v115,v115
v_mfma_f32_16x16x32_bf16 v[120:123],a[172:175],v[248:251],v[120:123]
v_exp_f32 v116,v116
v_mfma_f32_16x16x32_bf16 v[124:127],a[188:191],v[248:251],v[44:47]
buffer_load_dwordx4 v25,s[48:51],s83 offen offset:1024 lds
v_mfma_f32_16x16x32_bf16 v[124:127],a[184:187],v[240:243],v[124:127]
v_exp_f32 v117,v117
v_mfma_f32_16x16x32_bf16 v[124:127],a[180:183],v[232:235],v[124:127]
v_exp_f32 v118,v118
v_mfma_f32_16x16x32_bf16 v[124:127],a[176:179],v[224:227],v[124:127]
v_exp_f32 v119,v119
v_mfma_f32_16x16x32_bf16 v[52:55],v[28:31],v[80:83],v[52:55]
v_cvt_pk_bf16_f32 v112,v112,v113
v_cvt_pk_bf16_f32 v113,v114,v115
v_mfma_f32_16x16x32_bf16 v[52:55],v[28:31],v[88:91],v[52:55]
v_cvt_pk_bf16_f32 v114,v116,v117
v_cvt_pk_bf16_f32 v115,v118,v119
v_mfma_f32_16x16x32_bf16 v[132:135],a[192:195],v[80:83],v[132:135]
buffer_load_dwordx4 v26,s[48:51],s83 offen offset:2048 lds
v_mfma_f32_16x16x32_bf16 v[132:135],a[196:199],v[88:91],v[132:135]
v_exp_f32 v120,v120
v_mfma_f32_16x16x32_bf16 v[140:143],a[200:203],v[80:83],v[140:143]
v_exp_f32 v121,v121
v_mfma_f32_16x16x32_bf16 v[140:143],a[204:207],v[88:91],v[140:143]
v_exp_f32 v122,v122
v_mfma_f32_16x16x32_bf16 v[148:151],a[208:211],v[80:83],v[148:151]
v_exp_f32 v123,v123
v_mfma_f32_16x16x32_bf16 v[148:151],a[212:215],v[88:91],v[148:151]
v_exp_f32 v124,v124
v_mfma_f32_16x16x32_bf16 v[156:159],a[216:219],v[80:83],v[156:159]
buffer_load_dwordx4 v27,s[48:51],s83 offen offset:3072 lds
v_mfma_f32_16x16x32_bf16 v[156:159],a[220:223],v[88:91],v[156:159]
v_exp_f32 v125,v125
v_mfma_f32_16x16x32_bf16 v[164:167],a[224:227],v[80:83],v[164:167]
v_exp_f32 v126,v126
v_mfma_f32_16x16x32_bf16 v[164:167],a[228:231],v[88:91],v[164:167]
s_add_u32 m0,s86,4096
v_exp_f32 v127,v127
v_mfma_f32_16x16x32_bf16 v[172:175],a[232:235],v[80:83],v[172:175]
v_cvt_pk_bf16_f32 v120,v120,v121
v_cvt_pk_bf16_f32 v121,v122,v123
v_mfma_f32_16x16x32_bf16 v[172:175],a[236:239],v[88:91],v[172:175]
v_cvt_pk_bf16_f32 v122,v124,v125
v_cvt_pk_bf16_f32 v123,v126,v127
v_mfma_f32_16x16x32_bf16 v[180:183],a[240:243],v[80:83],v[180:183]
buffer_load_dwordx4 v7,s[48:51],s83 offen lds
v_mfma_f32_16x16x32_bf16 v[180:183],a[244:247],v[88:91],v[180:183]
v_cmp_nlt_f32 s[56:57],v52,s73
v_sub_u32 v2,v1,v2
v_mfma_f32_16x16x32_bf16 v[188:191],a[248:251],v[80:83],v[188:191]
s_or_b64 s[54:55],s[54:55],s[56:57]
v_accvgpr_read_b32 v8,a0
v_accvgpr_read_b32 v9,a1
v_mfma_f32_16x16x32_bf16 v[188:191],a[252:255],v[88:91],v[188:191]
s_add_u32 m0,s86,5120
v_accvgpr_read_b32 v10,a2
v_accvgpr_read_b32 v11,a3
v_mfma_f32_16x16x32_bf16 v[56:59],v[28:31],v[96:99],v[56:59]
v_accvgpr_read_b32 v12,a4
v_accvgpr_read_b32 v13,a5
v_mfma_f32_16x16x32_bf16 v[56:59],v[28:31],v[104:107],v[56:59]
v_accvgpr_read_b32 v14,a6
v_accvgpr_read_b32 v15,a7
v_mfma_f32_16x16x32_bf16 v[196:199],a[192:195],v[96:99],v[196:199]
buffer_load_dwordx4 v24,s[48:51],s84 offen lds
v_mfma_f32_16x16x32_bf16 v[196:199],a[196:199],v[104:107],v[196:199]
v_mul_f32 v8,v8,v49
v_mul_f32 v9,v9,v49
v_mfma_f32_16x16x32_bf16 v[204:207],a[200:203],v[96:99],v[204:207]
v_cmp_nlt_f32 s[56:57],v56,s73
v_mul_f32 v10,v10,v49
v_mfma_f32_16x16x32_bf16 v[204:207],a[204:207],v[104:107],v[204:207]
s_or_b64 s[54:55],s[54:55],s[56:57]
v_mul_f32 v11,v11,v49
v_mul_f32 v12,v12,v49
v_mfma_f32_16x16x32_bf16 v[212:215],a[208:211],v[96:99],v[212:215]
v_mul_f32 v13,v13,v49
v_mul_f32 v14,v14,v49
v_mfma_f32_16x16x32_bf16 v[212:215],a[212:215],v[104:107],v[212:215]
v_mul_f32 v15,v15,v49
v_cvt_pk_bf16_f32 v32,v8,v9
v_mfma_f32_16x16x32_bf16 v[220:223],a[216:219],v[96:99],v[220:223]
buffer_load_dwordx4 v25,s[48:51],s84 offen offset:1024 lds
v_mfma_f32_16x16x32_bf16 v[220:223],a[220:223],v[104:107],v[220:223]
v_cvt_pk_bf16_f32 v33,v10,v11
v_cvt_pk_bf16_f32 v34,v12,v13
v_mfma_f32_16x16x32_bf16 v[228:231],a[224:227],v[96:99],v[228:231]
v_cvt_pk_bf16_f32 v35,v14,v15
v_accvgpr_read_b32 v16,a8
v_mfma_f32_16x16x32_bf16 v[228:231],a[228:231],v[104:107],v[228:231]
v_accvgpr_read_b32 v17,a9
v_accvgpr_read_b32 v18,a10
v_mfma_f32_16x16x32_bf16 v[236:239],a[232:235],v[96:99],v[236:239]
v_accvgpr_read_b32 v19,a11
v_accvgpr_read_b32 v20,a12
v_mfma_f32_16x16x32_bf16 v[236:239],a[236:239],v[104:107],v[236:239]
v_accvgpr_read_b32 v21,a13
v_accvgpr_read_b32 v22,a14
v_mfma_f32_16x16x32_bf16 v[244:247],a[240:243],v[96:99],v[244:247]
buffer_load_dwordx4 v26,s[48:51],s84 offen offset:2048 lds
v_mfma_f32_16x16x32_bf16 v[244:247],a[244:247],v[104:107],v[244:247]
v_accvgpr_read_b32 v23,a15
v_mul_f32 v16,v16,v49
v_mfma_f32_16x16x32_bf16 v[252:255],a[248:251],v[96:99],v[252:255]
v_mul_f32 v17,v17,v49
v_mul_f32 v18,v18,v49
v_mfma_f32_16x16x32_bf16 v[252:255],a[252:255],v[104:107],v[252:255]
v_mul_f32 v19,v19,v49
v_mul_f32 v20,v20,v49
v_mfma_f32_16x16x32_bf16 v[60:63],v[28:31],v[112:115],v[60:63]
v_mul_f32 v21,v21,v49
v_mul_f32 v22,v22,v49
v_mfma_f32_16x16x32_bf16 v[60:63],v[28:31],v[120:123],v[60:63]
v_mul_f32 v23,v23,v49
v_cvt_pk_bf16_f32 v36,v16,v17
v_mfma_f32_16x16x32_bf16 a[32:35],a[192:195],v[112:115],a[32:35]
buffer_load_dwordx4 v27,s[48:51],s84 offen offset:3072 lds
v_mfma_f32_16x16x32_bf16 a[32:35],a[196:199],v[120:123],a[32:35]
v_cvt_pk_bf16_f32 v37,v18,v19
v_cvt_pk_bf16_f32 v38,v20,v21
v_mfma_f32_16x16x32_bf16 a[36:39],a[200:203],v[112:115],a[36:39]
v_cmp_nlt_f32 s[56:57],v60,s73
v_cvt_pk_bf16_f32 v39,v22,v23
v_mfma_f32_16x16x32_bf16 a[36:39],a[204:207],v[120:123],a[36:39]
s_add_u32 m0,s86,9216
v_accvgpr_read_b32 v8,a16
v_accvgpr_read_b32 v9,a17
v_mfma_f32_16x16x32_bf16 a[40:43],a[208:211],v[112:115],a[40:43]
s_or_b64 s[54:55],s[54:55],s[56:57]
v_accvgpr_read_b32 v10,a18
v_accvgpr_read_b32 v11,a19
v_mfma_f32_16x16x32_bf16 a[40:43],a[212:215],v[120:123],a[40:43]
v_accvgpr_read_b32 v12,a20
v_accvgpr_read_b32 v13,a21
v_mfma_f32_16x16x32_bf16 a[44:47],a[216:219],v[112:115],a[44:47]
buffer_load_dwordx4 v7,s[48:51],s84 offen lds
v_mfma_f32_16x16x32_bf16 a[44:47],a[220:223],v[120:123],a[44:47]
v_accvgpr_read_b32 v14,a22
v_accvgpr_read_b32 v15,a23
v_mfma_f32_16x16x32_bf16 a[48:51],a[224:227],v[112:115],a[48:51]
v_mul_f32 v8,v8,v49
v_mul_f32 v9,v9,v49
v_mfma_f32_16x16x32_bf16 a[48:51],a[228:231],v[120:123],a[48:51]
v_mul_f32 v10,v10,v49
v_mul_f32 v11,v11,v49
v_mfma_f32_16x16x32_bf16 a[52:55],a[232:235],v[112:115],a[52:55]
v_mul_f32 v12,v12,v49
v_mul_f32 v13,v13,v49
v_mfma_f32_16x16x32_bf16 a[52:55],a[236:239],v[120:123],a[52:55]
v_mul_f32 v14,v14,v49
v_mul_f32 v15,v15,v49
v_mfma_f32_16x16x32_bf16 a[56:59],a[240:243],v[112:115],a[56:59]
v_cvt_pk_bf16_f32 v40,v8,v9
v_cvt_pk_bf16_f32 v41,v10,v11
v_mfma_f32_16x16x32_bf16 a[56:59],a[244:247],v[120:123],a[56:59]
v_cvt_pk_bf16_f32 v42,v12,v13
v_cvt_pk_bf16_f32 v43,v14,v15
v_mfma_f32_16x16x32_bf16 a[60:63],a[248:251],v[112:115],a[60:63]
ds_read_b64_tr_b16 v[64:65],v1
ds_read_b64_tr_b16 v[66:67],v1 offset:5120
v_mfma_f32_16x16x32_bf16 a[60:63],a[252:255],v[120:123],a[60:63]
ds_read_b64_tr_b16 v[68:69],v1 offset:10240
v_max_f32 v53,0x00800000,v52
v_rcp_f32 v53,v53
v_max_f32 v57,0x00800000,v56
v_rcp_f32 v57,v57
v_max_f32 v61,0x00800000,v60
v_rcp_f32 v61,v61
ds_read_b64_tr_b16 v[70:71],v1 offset:15360
ds_read_b64_tr_b16 v[72:73],v1 offset:20480
ds_read_b64_tr_b16 v[74:75],v1 offset:25600
ds_read_b64_tr_b16 v[76:77],v1 offset:30720
ds_read_b64_tr_b16 v[78:79],v1 offset:35840
ds_read_b64_tr_b16 v[80:81],v2 offset:32
ds_read_b64_tr_b16 v[82:83],v2 offset:5152
ds_read_b64_tr_b16 v[84:85],v2 offset:10272
ds_read_b64_tr_b16 v[86:87],v2 offset:15392
ds_read_b64_tr_b16 v[88:89],v2 offset:20512
ds_read_b64_tr_b16 v[90:91],v2 offset:25632
ds_read_b64_tr_b16 v[92:93],v2 offset:30752
ds_read_b64_tr_b16 v[94:95],v2 offset:35872
s_cmp_lg_u64 s[54:55],0
s_cbranch_scc1 L_slow1_%=
L_ok1_%=:
s_cmp_ge_u32 s63,2
s_cbranch_scc1 L_GS_%=
L_gbF_%=:
s_waitcnt lgkmcnt(14)
v_mfma_f32_16x16x32_bf16 a[192:195],v[64:67],a[68:71],0
buffer_store_dwordx4 v[32:35],v6,s[40:43],s79 offen sc1
s_waitcnt lgkmcnt(12)
v_mfma_f32_16x16x32_bf16 a[192:195],v[68:71],a[76:79],a[192:195]
s_lshl_b32 s91,s63,8
v_bfe_u32 v3,v6,4,2
v_min_u32 v3,2,v3
s_waitcnt lgkmcnt(10)
v_mfma_f32_16x16x32_bf16 a[192:195],v[72:75],a[84:87],a[192:195]
s_add_u32 s87,s64,1
v_add3_u32 v3,v3,s64,1
v_and_b32 v3,3,v3
s_waitcnt lgkmcnt(8)
v_mfma_f32_16x16x32_bf16 a[192:195],v[76:79],a[92:95],a[192:195]
s_and_b32 s87,s87,3
ds_read_b64_tr_b16 v[96:97],v1 offset:64
v_lshlrev_b32 v3,6,v3
v_mfma_f32_16x16x32_bf16 a[196:199],v[76:79],a[124:127],0
s_lshl_b32 s87,s87,1
ds_read_b64_tr_b16 v[98:99],v1 offset:5184
ds_read_b64_tr_b16 v[100:101],v1 offset:10304
v_mfma_f32_16x16x32_bf16 a[196:199],v[72:75],a[116:119],a[196:199]
s_lshl_b32 s87,s87,16
ds_read_b64_tr_b16 v[102:103],v1 offset:15424
v_lshlrev_b32 v29,10,v6
v_mfma_f32_16x16x32_bf16 a[196:199],v[68:71],a[108:111],a[196:199]
s_add_u32 s88,s87,4096
ds_read_b64_tr_b16 v[104:105],v1 offset:20544
v_accvgpr_read_b32 v8,a24
v_mfma_f32_16x16x32_bf16 a[196:199],v[64:67],a[100:103],a[196:199]
s_add_u32 s89,s87,8192
ds_read_b64_tr_b16 v[106:107],v1 offset:25664
v_accvgpr_read_b32 v9,a25
v_mfma_f32_16x16x32_bf16 a[200:203],v[64:67],a[64:67],0
s_add_u32 s90,s87,12288
ds_read_b64_tr_b16 v[108:109],v1 offset:30784
ds_read_b64_tr_b16 v[110:111],v1 offset:35904
v_mfma_f32_16x16x32_bf16 a[200:203],v[68:71],a[72:75],a[200:203]
s_add_u32 m0,s86,122880
ds_read_b64_tr_b16 v[112:113],v2 offset:96
v_accvgpr_read_b32 v10,a26
v_mfma_f32_16x16x32_bf16 a[200:203],v[72:75],a[80:83],a[200:203]
s_add_u32 s83,s64,3
ds_read_b64_tr_b16 v[114:115],v2 offset:5216
)A"
R"A(v_accvgpr_read_b32 v11,a27
v_mfma_f32_16x16x32_bf16 a[200:203],v[76:79],a[88:91],a[200:203]
s_and_b32 s83,s83,3
ds_read_b64_tr_b16 v[116:117],v2 offset:10336
v_accvgpr_read_b32 v12,a28
v_mfma_f32_16x16x32_bf16 a[204:207],v[76:79],a[120:123],0
s_lshl_b32 s83,s83,1
ds_read_b64_tr_b16 v[118:119],v2 offset:15456
ds_read_b64_tr_b16 v[120:121],v2 offset:20576
v_mfma_f32_16x16x32_bf16 a[204:207],v[72:75],a[112:115],a[204:207]
s_mul_i32 s83,s83,163840
ds_read_b64_tr_b16 v[122:123],v2 offset:25696
v_accvgpr_read_b32 v13,a29
v_mfma_f32_16x16x32_bf16 a[204:207],v[68:71],a[104:107],a[204:207]
s_add_u32 s83,s83,s85
ds_read_b64_tr_b16 v[124:125],v2 offset:30816
v_accvgpr_read_b32 v14,a30
v_mfma_f32_16x16x32_bf16 a[204:207],v[64:67],a[96:99],a[204:207]
buffer_store_dwordx4 v[36:39],v6,s[40:43],s80 offen sc1
s_waitcnt lgkmcnt(9)
v_mfma_f32_16x16x32_bf16 a[208:211],v[80:83],a[68:71],0
s_add_u32 s84,s83,20480
ds_read_b64_tr_b16 v[126:127],v2 offset:35936
ds_read_b64_tr_b16 v[64:65],v1 offset:128
v_mfma_f32_16x16x32_bf16 a[208:211],v[84:87],a[76:79],a[208:211]
s_lshl_b32 s66,s61,3
ds_read_b64_tr_b16 v[66:67],v1 offset:5248
v_accvgpr_read_b32 v15,a31
v_mfma_f32_16x16x32_bf16 a[208:211],v[88:91],a[84:87],a[208:211]
s_add_u32 s66,s66,s64
ds_read_b64_tr_b16 v[68:69],v1 offset:10368
v_mul_f32 v8,v8,v49
v_mfma_f32_16x16x32_bf16 a[208:211],v[92:95],a[92:95],a[208:211]
s_mul_i32 s66,s66,262144
ds_read_b64_tr_b16 v[70:71],v1 offset:15488
v_mul_f32 v9,v9,v49
v_mfma_f32_16x16x32_bf16 a[212:215],v[92:95],a[124:127],0
s_waitcnt vmcnt(21)
buffer_load_dwordx2 v[4:5],v3,s[44:47],s91 offen offset:256 sc1
v_mfma_f32_16x16x32_bf16 a[212:215],v[88:91],a[116:119],a[212:215]
s_add_u32 s66,s66,100663296
ds_read_b64_tr_b16 v[72:73],v1 offset:20608
ds_read_b64_tr_b16 v[74:75],v1 offset:25728
v_mfma_f32_16x16x32_bf16 a[212:215],v[84:87],a[108:111],a[212:215]
s_mov_b32 s38,262144
ds_read_b64_tr_b16 v[76:77],v1 offset:30848
v_mul_f32 v10,v10,v49
v_mfma_f32_16x16x32_bf16 a[212:215],v[80:83],a[100:103],a[212:215]
s_mov_b32 s39,0x20000
ds_read_b64_tr_b16 v[78:79],v1 offset:35968
v_mul_f32 v11,v11,v49
v_mfma_f32_16x16x32_bf16 a[216:219],v[80:83],a[64:67],0
s_add_u32 s74,s63,1
v_mul_f32 v12,v12,v49
v_mul_f32 v13,v13,v49
v_mfma_f32_16x16x32_bf16 a[216:219],v[84:87],a[72:75],a[216:219]
L_pd13_%=:
s_cbranch_execz L_trt1_%=
s_and_b32 s74,s74,3
v_mul_f32 v14,v14,v49
v_mul_f32 v15,v15,v49
v_mfma_f32_16x16x32_bf16 a[216:219],v[88:91],a[80:83],a[216:219]
s_lshl_b32 s74,s74,2
v_cvt_pk_bf16_f32 v44,v8,v9
v_cvt_pk_bf16_f32 v45,v10,v11
v_mfma_f32_16x16x32_bf16 a[216:219],v[92:95],a[88:91],a[216:219]
s_add_u32 s74,s74,s60
v_cvt_pk_bf16_f32 v46,v12,v13
v_cvt_pk_bf16_f32 v47,v14,v15
v_mfma_f32_16x16x32_bf16 a[220:223],v[92:95],a[120:123],0
s_mul_i32 s74,s74,16384
ds_read_b64_tr_b16 v[92:93],v2 offset:30880
ds_read_b64_tr_b16 v[94:95],v2 offset:36000
v_mfma_f32_16x16x32_bf16 a[220:223],v[88:91],a[112:115],a[220:223]
s_add_u32 s74,s74,8192
ds_read_b64_tr_b16 v[88:89],v2 offset:20640
v_mul_f32 v132,v132,v53
v_mfma_f32_16x16x32_bf16 a[220:223],v[84:87],a[104:107],a[220:223]
buffer_store_dwordx4 v[40:43],v6,s[40:43],s81 offen sc1
v_mfma_f32_16x16x32_bf16 a[220:223],v[80:83],a[96:99],a[220:223]
s_add_u32 s75,s63,2
ds_read_b64_tr_b16 v[80:81],v2 offset:160
ds_read_b64_tr_b16 v[82:83],v2 offset:5280
v_mfma_f32_16x16x32_bf16 a[224:227],v[96:99],a[68:71],0
s_and_b32 s75,s75,3
ds_read_b64_tr_b16 v[84:85],v2 offset:10400
v_mul_f32 v133,v133,v53
v_mfma_f32_16x16x32_bf16 a[224:227],v[100:103],a[76:79],a[224:227]
s_lshl_b32 s75,s75,2
ds_read_b64_tr_b16 v[86:87],v2 offset:15520
v_mul_f32 v134,v134,v53
v_mfma_f32_16x16x32_bf16 a[224:227],v[104:107],a[84:87],a[224:227]
s_add_u32 s75,s75,s60
ds_read_b64_tr_b16 v[90:91],v2 offset:25760
v_mul_f32 v135,v135,v53
s_waitcnt lgkmcnt(8)
v_mfma_f32_16x16x32_bf16 a[224:227],v[108:111],a[92:95],a[224:227]
s_mul_i32 s75,s75,16384
v_mul_f32 v140,v140,v53
v_mul_f32 v141,v141,v53
v_mfma_f32_16x16x32_bf16 a[228:231],v[108:111],a[124:127],0
s_add_u32 s75,s75,4096
v_mul_f32 v142,v142,v53
v_mul_f32 v143,v143,v53
v_mfma_f32_16x16x32_bf16 a[228:231],v[104:107],a[116:119],a[228:231]
s_add_u32 s76,s63,3
v_cvt_pk_bf16_f32 v132,v132,v133
v_cvt_pk_bf16_f32 v133,v134,v135
v_mfma_f32_16x16x32_bf16 a[228:231],v[100:103],a[108:111],a[228:231]
s_and_b32 s76,s76,3
v_cvt_pk_bf16_f32 v134,v140,v141
v_cvt_pk_bf16_f32 v135,v142,v143
v_mfma_f32_16x16x32_bf16 a[228:231],v[96:99],a[100:103],a[228:231]
s_lshl_b32 s76,s76,2
v_mul_f32 v148,v148,v53
v_mul_f32 v149,v149,v53
v_mfma_f32_16x16x32_bf16 a[232:235],v[96:99],a[64:67],0
s_add_u32 s76,s76,s60
v_mul_f32 v150,v150,v53
v_mul_f32 v151,v151,v53
v_mfma_f32_16x16x32_bf16 a[232:235],v[100:103],a[72:75],a[232:235]
s_mul_i32 s76,s76,16384
v_mul_f32 v156,v156,v53
v_mul_f32 v157,v157,v53
v_mfma_f32_16x16x32_bf16 a[232:235],v[104:107],a[80:83],a[232:235]
s_add_u32 s76,s76,0
v_mul_f32 v158,v158,v53
v_mul_f32 v159,v159,v53
v_mfma_f32_16x16x32_bf16 a[232:235],v[108:111],a[88:91],a[232:235]
s_lshl_b32 s67,s63,10
v_cvt_pk_bf16_f32 v148,v148,v149
v_cvt_pk_bf16_f32 v149,v150,v151
v_mfma_f32_16x16x32_bf16 a[236:239],v[108:111],a[120:123],0
s_mov_b32 s26,307200
ds_read_b64_tr_b16 v[108:109],v1 offset:30912
ds_read_b64_tr_b16 v[110:111],v1 offset:36032
v_mfma_f32_16x16x32_bf16 a[236:239],v[104:107],a[112:115],a[236:239]
buffer_store_dwordx4 v[44:47],v6,s[40:43],s82 offen sc1
v_mfma_f32_16x16x32_bf16 a[236:239],v[100:103],a[104:107],a[236:239]
s_mov_b32 s27,0x20000
ds_read_b64_tr_b16 v[100:101],v1 offset:10432
ds_read_b64_tr_b16 v[102:103],v1 offset:15552
v_mfma_f32_16x16x32_bf16 a[236:239],v[96:99],a[96:99],a[236:239]
s_lshl_b32 s77,s63,2
ds_read_b64_tr_b16 v[96:97],v1 offset:192
v_cvt_pk_bf16_f32 v150,v156,v157
v_mfma_f32_16x16x32_bf16 a[240:243],v[112:115],a[68:71],0
s_add_u32 s77,s77,s60
ds_read_b64_tr_b16 v[98:99],v1 offset:5312
v_cvt_pk_bf16_f32 v151,v158,v159
v_mfma_f32_16x16x32_bf16 a[240:243],v[116:119],a[76:79],a[240:243]
s_mul_i32 s77,s77,16384
ds_read_b64_tr_b16 v[104:105],v1 offset:20672
v_mul_f32 v164,v164,v53
v_mfma_f32_16x16x32_bf16 a[240:243],v[120:123],a[84:87],a[240:243]
s_add_u32 s65,s77,0
ds_read_b64_tr_b16 v[106:107],v1 offset:25792
v_mul_f32 v165,v165,v53
v_mfma_f32_16x16x32_bf16 a[240:243],v[124:127],a[92:95],a[240:243]
s_add_u32 s68,s77,12288
v_mul_f32 v166,v166,v53
v_mul_f32 v167,v167,v53
v_mfma_f32_16x16x32_bf16 a[244:247],v[124:127],a[124:127],0
v_mul_f32 v172,v172,v53
v_mul_f32 v173,v173,v53
v_mfma_f32_16x16x32_bf16 a[244:247],v[120:123],a[116:119],a[244:247]
v_mul_f32 v174,v174,v53
v_mul_f32 v175,v175,v53
v_mfma_f32_16x16x32_bf16 a[244:247],v[116:119],a[108:111],a[244:247]
v_cvt_pk_bf16_f32 v164,v164,v165
v_cvt_pk_bf16_f32 v165,v166,v167
v_mfma_f32_16x16x32_bf16 a[244:247],v[112:115],a[100:103],a[244:247]
v_cvt_pk_bf16_f32 v166,v172,v173
v_cvt_pk_bf16_f32 v167,v174,v175
v_mfma_f32_16x16x32_bf16 a[248:251],v[112:115],a[64:67],0
v_mul_f32 v180,v180,v53
v_mul_f32 v181,v181,v53
v_mfma_f32_16x16x32_bf16 a[248:251],v[116:119],a[72:75],a[248:251]
v_mul_f32 v182,v182,v53
v_mul_f32 v183,v183,v53
v_mfma_f32_16x16x32_bf16 a[248:251],v[120:123],a[80:83],a[248:251]
v_mul_f32 v188,v188,v53
v_mul_f32 v189,v189,v53
v_mfma_f32_16x16x32_bf16 a[248:251],v[124:127],a[88:91],a[248:251]
v_mul_f32 v190,v190,v53
v_mul_f32 v191,v191,v53
v_mfma_f32_16x16x32_bf16 a[252:255],v[124:127],a[120:123],0
buffer_load_dword v30,v29,s[8:11],s7 offen
v_mfma_f32_16x16x32_bf16 a[252:255],v[120:123],a[112:115],a[252:255]
ds_read_b64_tr_b16 v[120:121],v2 offset:20704
ds_read_b64_tr_b16 v[122:123],v2 offset:25824
v_mfma_f32_16x16x32_bf16 a[252:255],v[116:119],a[104:107],a[252:255]
buffer_store_dwordx4 v[132:135],v6,s[40:43],s79 offen offset:1024 sc1
v_mfma_f32_16x16x32_bf16 a[252:255],v[112:115],a[96:99],a[252:255]
ds_read_b64_tr_b16 v[112:113],v2 offset:224
ds_read_b64_tr_b16 v[114:115],v2 offset:5344
v_mfma_f32_16x16x32_bf16 a[0:3],v[64:67],a[68:71],0
ds_read_b64_tr_b16 v[116:117],v2 offset:10464
v_cvt_pk_bf16_f32 v180,v180,v181
v_mfma_f32_16x16x32_bf16 a[0:3],v[68:71],a[76:79],a[0:3]
ds_read_b64_tr_b16 v[118:119],v2 offset:15584
v_cvt_pk_bf16_f32 v181,v182,v183
v_mfma_f32_16x16x32_bf16 a[0:3],v[72:75],a[84:87],a[0:3]
ds_read_b64_tr_b16 v[124:125],v2 offset:30944
v_cvt_pk_bf16_f32 v182,v188,v189
v_mfma_f32_16x16x32_bf16 a[0:3],v[76:79],a[92:95],a[0:3]
ds_read_b64_tr_b16 v[126:127],v2 offset:36064
v_cvt_pk_bf16_f32 v183,v190,v191
v_mfma_f32_16x16x32_bf16 a[4:7],v[76:79],a[124:127],0
v_mul_f32 v196,v196,v57
v_mul_f32 v197,v197,v57
v_mfma_f32_16x16x32_bf16 a[4:7],v[72:75],a[116:119],a[4:7]
v_mul_f32 v198,v198,v57
v_mul_f32 v199,v199,v57
v_mfma_f32_16x16x32_bf16 a[4:7],v[68:71],a[108:111],a[4:7]
v_mul_f32 v204,v204,v57
v_mul_f32 v205,v205,v57
v_mfma_f32_16x16x32_bf16 a[4:7],v[64:67],a[100:103],a[4:7]
v_mul_f32 v206,v206,v57
v_mul_f32 v207,v207,v57
v_mfma_f32_16x16x32_bf16 a[8:11],v[64:67],a[64:67],0
v_cvt_pk_bf16_f32 v196,v196,v197
v_cvt_pk_bf16_f32 v197,v198,v199
v_mfma_f32_16x16x32_bf16 a[8:11],v[68:71],a[72:75],a[8:11]
v_cvt_pk_bf16_f32 v198,v204,v205
v_cvt_pk_bf16_f32 v199,v206,v207
v_mfma_f32_16x16x32_bf16 a[8:11],v[72:75],a[80:83],a[8:11]
v_mul_f32 v212,v212,v57
v_mul_f32 v213,v213,v57
v_mfma_f32_16x16x32_bf16 a[8:11],v[76:79],a[88:91],a[8:11]
v_mul_f32 v214,v214,v57
v_mul_f32 v215,v215,v57
v_mfma_f32_16x16x32_bf16 a[12:15],v[76:79],a[120:123],0
ds_read_b64_tr_b16 v[76:77],v1 offset:30976
ds_read_b64_tr_b16 v[78:79],v1 offset:36096
v_mfma_f32_16x16x32_bf16 a[12:15],v[72:75],a[112:115],a[12:15]
buffer_store_dwordx4 v[148:151],v6,s[40:43],s80 offen offset:1024 sc1
v_mfma_f32_16x16x32_bf16 a[12:15],v[68:71],a[104:107],a[12:15]
ds_read_b64_tr_b16 v[68:69],v1 offset:10496
ds_read_b64_tr_b16 v[70:71],v1 offset:15616
v_mfma_f32_16x16x32_bf16 a[12:15],v[64:67],a[96:99],a[12:15]
s_waitcnt lgkmcnt(4)
ds_read_b64_tr_b16 v[64:65],v1 offset:256
v_mul_f32 v220,v220,v57
v_mfma_f32_16x16x32_bf16 a[16:19],v[80:83],a[68:71],0
ds_read_b64_tr_b16 v[66:67],v1 offset:5376
v_mul_f32 v221,v221,v57
v_mfma_f32_16x16x32_bf16 a[16:19],v[84:87],a[76:79],a[16:19]
ds_read_b64_tr_b16 v[72:73],v1 offset:20736
v_mul_f32 v222,v222,v57
v_mfma_f32_16x16x32_bf16 a[16:19],v[88:91],a[84:87],a[16:19]
ds_read_b64_tr_b16 v[74:75],v1 offset:25856
v_add_u32 v1,0xffff6000,v1
v_mfma_f32_16x16x32_bf16 a[16:19],v[92:95],a[92:95],a[16:19]
v_mul_f32 v223,v223,v57
v_cvt_pk_bf16_f32 v212,v212,v213
v_mfma_f32_16x16x32_bf16 a[20:23],v[92:95],a[124:127],0
v_cvt_pk_bf16_f32 v213,v214,v215
v_cvt_pk_bf16_f32 v214,v220,v221
v_mfma_f32_16x16x32_bf16 a[20:23],v[88:91],a[116:119],a[20:23]
v_cvt_pk_bf16_f32 v215,v222,v223
v_mul_f32 v228,v228,v57
v_mfma_f32_16x16x32_bf16 a[20:23],v[84:87],a[108:111],a[20:23]
v_mul_f32 v229,v229,v57
v_mul_f32 v230,v230,v57
v_mfma_f32_16x16x32_bf16 a[20:23],v[80:83],a[100:103],a[20:23]
v_mul_f32 v231,v231,v57
v_mul_f32 v236,v236,v57
v_mfma_f32_16x16x32_bf16 a[24:27],v[80:83],a[64:67],0
v_mul_f32 v237,v237,v57
v_mul_f32 v238,v238,v57
v_mfma_f32_16x16x32_bf16 a[24:27],v[84:87],a[72:75],a[24:27]
v_mul_f32 v239,v239,v57
v_cvt_pk_bf16_f32 v228,v228,v229
v_mfma_f32_16x16x32_bf16 a[24:27],v[88:91],a[80:83],a[24:27]
s_waitcnt vmcnt(5)
v_cmp_ne_u32 s[56:57],s52,v4
v_cmp_ne_u32 s[58:59],s53,v5
v_mfma_f32_16x16x32_bf16 a[24:27],v[92:95],a[88:91],a[24:27]
s_or_b64 s[56:57],s[56:57],s[58:59]
s_cbranch_scc1 L_fr1_%=
L_fo1_%=:
v_cvt_pk_bf16_f32 v229,v230,v231
v_mfma_f32_16x16x32_bf16 a[28:31],v[92:95],a[120:123],0
buffer_store_dwordx4 v[164:167],v6,s[40:43],s81 offen offset:1024 sc1
v_mfma_f32_16x16x32_bf16 a[28:31],v[88:91],a[112:115],a[28:31]
s_lshl_b32 s91,s63,8
ds_read_b64_tr_b16 v[88:89],v2 offset:20768
ds_read_b64_tr_b16 v[90:91],v2 offset:25888
v_mfma_f32_16x16x32_bf16 a[28:31],v[84:87],a[104:107],a[28:31]
buffer_load_dwordx4 a[128:131],v6,s[28:31],s87 offen sc1
v_mfma_f32_16x16x32_bf16 a[28:31],v[80:83],a[96:99],a[28:31]
ds_read_b64_tr_b16 v[80:81],v2 offset:288
ds_read_b64_tr_b16 v[82:83],v2 offset:5408
v_mfma_f32_16x16x32_bf16 v[128:131],v[96:99],a[68:71],0
ds_read_b64_tr_b16 v[84:85],v2 offset:10528
v_cvt_pk_bf16_f32 v230,v236,v237
v_mfma_f32_16x16x32_bf16 v[128:131],v[100:103],a[76:79],v[128:131]
)A"
R"A(ds_read_b64_tr_b16 v[86:87],v2 offset:15648
v_cvt_pk_bf16_f32 v231,v238,v239
v_mfma_f32_16x16x32_bf16 v[128:131],v[104:107],a[84:87],v[128:131]
ds_read_b64_tr_b16 v[92:93],v2 offset:31008
v_mul_f32 v244,v244,v57
v_mfma_f32_16x16x32_bf16 v[128:131],v[108:111],a[92:95],v[128:131]
ds_read_b64_tr_b16 v[94:95],v2 offset:36128
v_add_u32 v2,0xffff6000,v2
v_mfma_f32_16x16x32_bf16 v[136:139],v[108:111],a[124:127],0
v_mul_f32 v245,v245,v57
v_mul_f32 v246,v246,v57
v_mfma_f32_16x16x32_bf16 v[136:139],v[104:107],a[116:119],v[136:139]
v_mul_f32 v247,v247,v57
v_mul_f32 v252,v252,v57
v_mfma_f32_16x16x32_bf16 v[136:139],v[100:103],a[108:111],v[136:139]
v_mul_f32 v253,v253,v57
v_mul_f32 v254,v254,v57
v_mfma_f32_16x16x32_bf16 v[136:139],v[96:99],a[100:103],v[136:139]
v_mul_f32 v255,v255,v57
v_cvt_pk_bf16_f32 v244,v244,v245
v_mfma_f32_16x16x32_bf16 v[144:147],v[96:99],a[64:67],0
v_cvt_pk_bf16_f32 v245,v246,v247
v_cvt_pk_bf16_f32 v246,v252,v253
v_mfma_f32_16x16x32_bf16 v[144:147],v[100:103],a[72:75],v[144:147]
buffer_load_dwordx4 a[144:147],v6,s[28:31],s88 offen sc1
v_mfma_f32_16x16x32_bf16 v[144:147],v[104:107],a[80:83],v[144:147]
v_cvt_pk_bf16_f32 v247,v254,v255
v_accvgpr_read_b32 v8,a40
v_mfma_f32_16x16x32_bf16 v[144:147],v[108:111],a[88:91],v[144:147]
buffer_store_dwordx4 v[180:183],v6,s[40:43],s82 offen offset:1024 sc1
v_mfma_f32_16x16x32_bf16 v[152:155],v[108:111],a[120:123],0
ds_read_b64_tr_b16 v[108:109],v1 offset:30720
ds_read_b64_tr_b16 v[110:111],v1 offset:35840
v_mfma_f32_16x16x32_bf16 v[152:155],v[104:107],a[112:115],v[152:155]
ds_read_b64_tr_b16 v[104:105],v1 offset:20480
v_accvgpr_read_b32 v9,a41
v_mfma_f32_16x16x32_bf16 v[152:155],v[100:103],a[104:107],v[152:155]
ds_read_b64_tr_b16 v[100:101],v1 offset:10240
v_accvgpr_read_b32 v10,a42
v_mfma_f32_16x16x32_bf16 v[152:155],v[96:99],a[96:99],v[152:155]
ds_read_b64_tr_b16 v[96:97],v1
v_accvgpr_read_b32 v11,a43
v_mfma_f32_16x16x32_bf16 v[160:163],v[112:115],a[68:71],0
ds_read_b64_tr_b16 v[98:99],v1 offset:5120
ds_read_b64_tr_b16 v[102:103],v1 offset:15360
v_mfma_f32_16x16x32_bf16 v[160:163],v[116:119],a[76:79],v[160:163]
ds_read_b64_tr_b16 v[106:107],v1 offset:25600
v_accvgpr_read_b32 v12,a44
v_mfma_f32_16x16x32_bf16 v[160:163],v[120:123],a[84:87],v[160:163]
v_accvgpr_read_b32 v13,a45
v_accvgpr_read_b32 v14,a46
v_mfma_f32_16x16x32_bf16 v[160:163],v[124:127],a[92:95],v[160:163]
v_accvgpr_read_b32 v15,a47
v_mul_f32 v8,v8,v61
v_mfma_f32_16x16x32_bf16 v[168:171],v[124:127],a[124:127],0
v_mul_f32 v9,v9,v61
v_mul_f32 v10,v10,v61
v_mfma_f32_16x16x32_bf16 v[168:171],v[120:123],a[116:119],v[168:171]
v_mul_f32 v11,v11,v61
v_mul_f32 v12,v12,v61
v_mfma_f32_16x16x32_bf16 v[168:171],v[116:119],a[108:111],v[168:171]
buffer_load_dwordx4 a[160:163],v6,s[28:31],s89 offen sc1
v_mfma_f32_16x16x32_bf16 v[168:171],v[112:115],a[100:103],v[168:171]
v_mul_f32 v13,v13,v61
v_mul_f32 v14,v14,v61
v_mfma_f32_16x16x32_bf16 v[176:179],v[112:115],a[64:67],0
v_mul_f32 v15,v15,v61
v_cvt_pk_bf16_f32 v156,v8,v9
v_mfma_f32_16x16x32_bf16 v[176:179],v[116:119],a[72:75],v[176:179]
v_cvt_pk_bf16_f32 v157,v10,v11
v_cvt_pk_bf16_f32 v158,v12,v13
v_mfma_f32_16x16x32_bf16 v[176:179],v[120:123],a[80:83],v[176:179]
buffer_store_dwordx4 v[196:199],v6,s[40:43],s79 offen offset:2048 sc1
v_mfma_f32_16x16x32_bf16 v[176:179],v[124:127],a[88:91],v[176:179]
v_cvt_pk_bf16_f32 v159,v14,v15
v_accvgpr_read_b32 v8,a56
v_mfma_f32_16x16x32_bf16 v[184:187],v[124:127],a[120:123],0
v_accvgpr_read_b32 v9,a57
v_accvgpr_read_b32 v10,a58
v_mfma_f32_16x16x32_bf16 v[184:187],v[120:123],a[112:115],v[184:187]
ds_read_b64_tr_b16 v[120:121],v2 offset:20512
ds_read_b64_tr_b16 v[122:123],v2 offset:25632
v_mfma_f32_16x16x32_bf16 v[184:187],v[116:119],a[104:107],v[184:187]
ds_read_b64_tr_b16 v[116:117],v2 offset:10272
v_accvgpr_read_b32 v11,a59
v_mfma_f32_16x16x32_bf16 v[184:187],v[112:115],a[96:99],v[184:187]
s_waitcnt lgkmcnt(3)
ds_read_b64_tr_b16 v[112:113],v2 offset:32
v_accvgpr_read_b32 v12,a60
v_mfma_f32_16x16x32_bf16 v[192:195],v[64:67],a[68:71],0
ds_read_b64_tr_b16 v[114:115],v2 offset:5152
v_accvgpr_read_b32 v13,a61
v_mfma_f32_16x16x32_bf16 v[192:195],v[68:71],a[76:79],v[192:195]
ds_read_b64_tr_b16 v[118:119],v2 offset:15392
v_accvgpr_read_b32 v14,a62
v_mfma_f32_16x16x32_bf16 v[192:195],v[72:75],a[84:87],v[192:195]
v_accvgpr_read_b32 v15,a63
v_mul_f32 v8,v8,v61
v_mfma_f32_16x16x32_bf16 v[192:195],v[76:79],a[92:95],v[192:195]
buffer_load_dwordx4 a[176:179],v6,s[28:31],s90 offen sc1
v_mfma_f32_16x16x32_bf16 v[200:203],v[76:79],a[124:127],0
v_mul_f32 v9,v9,v61
v_mul_f32 v10,v10,v61
v_mfma_f32_16x16x32_bf16 v[200:203],v[72:75],a[116:119],v[200:203]
v_mul_f32 v11,v11,v61
v_mul_f32 v12,v12,v61
v_mfma_f32_16x16x32_bf16 v[200:203],v[68:71],a[108:111],v[200:203]
v_mul_f32 v13,v13,v61
v_mul_f32 v14,v14,v61
v_mfma_f32_16x16x32_bf16 v[200:203],v[64:67],a[100:103],v[200:203]
v_mul_f32 v15,v15,v61
v_cvt_pk_bf16_f32 v188,v8,v9
v_mfma_f32_16x16x32_bf16 v[208:211],v[64:67],a[64:67],0
v_cvt_pk_bf16_f32 v189,v10,v11
v_cvt_pk_bf16_f32 v190,v12,v13
v_mfma_f32_16x16x32_bf16 v[208:211],v[68:71],a[72:75],v[208:211]
buffer_store_dwordx4 v[212:215],v6,s[40:43],s80 offen offset:2048 sc1
v_mfma_f32_16x16x32_bf16 v[208:211],v[72:75],a[80:83],v[208:211]
ds_read_b64_tr_b16 v[124:125],v2 offset:30752
v_cvt_pk_bf16_f32 v191,v14,v15
v_mfma_f32_16x16x32_bf16 v[208:211],v[76:79],a[88:91],v[208:211]
ds_read_b64_tr_b16 v[126:127],v2 offset:35872
v_accvgpr_read_b32 v16,a32
v_mfma_f32_16x16x32_bf16 v[216:219],v[76:79],a[120:123],0
v_accvgpr_read_b32 v17,a33
v_accvgpr_read_b32 v18,a34
v_mfma_f32_16x16x32_bf16 v[216:219],v[72:75],a[112:115],v[216:219]
v_accvgpr_read_b32 v19,a35
v_accvgpr_read_b32 v20,a36
v_mfma_f32_16x16x32_bf16 v[216:219],v[68:71],a[104:107],v[216:219]
v_accvgpr_read_b32 v21,a37
v_accvgpr_read_b32 v22,a38
v_mfma_f32_16x16x32_bf16 v[216:219],v[64:67],a[96:99],v[216:219]
v_accvgpr_read_b32 v23,a39
v_mul_f32 v16,v16,v61
v_mfma_f32_16x16x32_bf16 v[224:227],v[80:83],a[68:71],0
buffer_load_dwordx4 a[180:183],v6,s[28:31],s90 offen offset:1024 sc1
v_mfma_f32_16x16x32_bf16 v[224:227],v[84:87],a[76:79],v[224:227]
v_mul_f32 v17,v17,v61
v_mul_f32 v18,v18,v61
v_mfma_f32_16x16x32_bf16 v[224:227],v[88:91],a[84:87],v[224:227]
v_mul_f32 v19,v19,v61
v_mul_f32 v20,v20,v61
v_mfma_f32_16x16x32_bf16 v[224:227],v[92:95],a[92:95],v[224:227]
v_mul_f32 v21,v21,v61
v_mul_f32 v22,v22,v61
v_mfma_f32_16x16x32_bf16 v[232:235],v[92:95],a[124:127],0
v_mul_f32 v23,v23,v61
v_cvt_pk_bf16_f32 v140,v16,v17
v_mfma_f32_16x16x32_bf16 v[232:235],v[88:91],a[116:119],v[232:235]
v_cvt_pk_bf16_f32 v141,v18,v19
v_cvt_pk_bf16_f32 v142,v20,v21
v_mfma_f32_16x16x32_bf16 v[232:235],v[84:87],a[108:111],v[232:235]
v_cvt_pk_bf16_f32 v143,v22,v23
v_accvgpr_read_b32 v16,a48
v_mfma_f32_16x16x32_bf16 v[232:235],v[80:83],a[100:103],v[232:235]
v_accvgpr_read_b32 v17,a49
v_accvgpr_read_b32 v18,a50
v_mfma_f32_16x16x32_bf16 v[240:243],v[80:83],a[64:67],0
buffer_store_dwordx4 v[228:231],v6,s[40:43],s81 offen offset:2048 sc1
v_mfma_f32_16x16x32_bf16 v[240:243],v[84:87],a[72:75],v[240:243]
v_accvgpr_read_b32 v19,a51
v_accvgpr_read_b32 v20,a52
v_mfma_f32_16x16x32_bf16 v[240:243],v[88:91],a[80:83],v[240:243]
v_accvgpr_read_b32 v21,a53
v_accvgpr_read_b32 v22,a54
v_mfma_f32_16x16x32_bf16 v[240:243],v[92:95],a[88:91],v[240:243]
v_accvgpr_read_b32 v23,a55
v_mul_f32 v16,v16,v61
v_mfma_f32_16x16x32_bf16 v[248:251],v[92:95],a[120:123],0
v_mul_f32 v17,v17,v61
v_mul_f32 v18,v18,v61
v_mfma_f32_16x16x32_bf16 v[248:251],v[88:91],a[112:115],v[248:251]
buffer_load_dwordx4 a[164:167],v6,s[28:31],s89 offen offset:1024 sc1
v_mfma_f32_16x16x32_bf16 v[248:251],v[84:87],a[104:107],v[248:251]
v_mul_f32 v19,v19,v61
v_mul_f32 v20,v20,v61
v_mfma_f32_16x16x32_bf16 v[248:251],v[80:83],a[96:99],v[248:251]
v_mul_f32 v21,v21,v61
v_mul_f32 v22,v22,v61
v_mfma_f32_16x16x32_bf16 a[192:195],v[96:99],v[32:35],a[192:195]
v_mul_f32 v23,v23,v61
v_cvt_pk_bf16_f32 v172,v16,v17
v_mfma_f32_16x16x32_bf16 a[192:195],v[100:103],v[36:39],a[192:195]
v_cvt_pk_bf16_f32 v173,v18,v19
v_cvt_pk_bf16_f32 v174,v20,v21
v_mfma_f32_16x16x32_bf16 a[192:195],v[104:107],v[40:43],a[192:195]
ds_read_b64_tr_b16 v[64:65],v1 offset:64
v_cvt_pk_bf16_f32 v175,v22,v23
v_mfma_f32_16x16x32_bf16 a[192:195],v[108:111],v[44:47],a[192:195]
ds_read_b64_tr_b16 v[66:67],v1 offset:5184
ds_read_b64_tr_b16 v[68:69],v1 offset:10304
v_mfma_f32_16x16x32_bf16 a[196:199],v[108:111],v[180:183],a[196:199]
s_cmp_eq_u32 s60,3
s_cselect_b32 s69,0x40000000,0
ds_read_b64_tr_b16 v[70:71],v1 offset:15424
v_mfma_f32_16x16x32_bf16 a[196:199],v[104:107],v[164:167],a[196:199]
ds_read_b64_tr_b16 v[72:73],v1 offset:20544
v_add_u32 v28,s69,v6
v_mfma_f32_16x16x32_bf16 a[196:199],v[100:103],v[148:151],a[196:199]
s_lshl_b32 s69,s63,2
ds_read_b64_tr_b16 v[74:75],v1 offset:25664
v_mov_b32 v8,s52
v_mfma_f32_16x16x32_bf16 a[196:199],v[96:99],v[132:135],a[196:199]
buffer_store_dwordx4 v[244:247],v6,s[40:43],s82 offen offset:2048 sc1
v_mfma_f32_16x16x32_bf16 a[200:203],v[96:99],v[196:199],a[200:203]
ds_read_b64_tr_b16 v[76:77],v1 offset:30784
ds_read_b64_tr_b16 v[78:79],v1 offset:35904
v_mfma_f32_16x16x32_bf16 a[200:203],v[100:103],v[212:215],a[200:203]
ds_read_b64_tr_b16 v[80:81],v2 offset:96
v_mov_b32 v9,s53
v_mfma_f32_16x16x32_bf16 a[200:203],v[104:107],v[228:231],a[200:203]
buffer_load_dwordx4 a[148:151],v6,s[28:31],s88 offen offset:1024 sc1
v_mfma_f32_16x16x32_bf16 a[200:203],v[108:111],v[244:247],a[200:203]
ds_read_b64_tr_b16 v[82:83],v2 offset:5216
ds_read_b64_tr_b16 v[84:85],v2 offset:10336
v_mfma_f32_16x16x32_bf16 a[204:207],v[108:111],v[188:191],a[204:207]
ds_read_b64_tr_b16 v[86:87],v2 offset:15456
v_lshlrev_b32 v10,20,v6
v_mfma_f32_16x16x32_bf16 a[204:207],v[104:107],v[172:175],a[204:207]
ds_read_b64_tr_b16 v[88:89],v2 offset:20576
v_bfe_u32 v3,v6,4,2
v_mfma_f32_16x16x32_bf16 a[204:207],v[100:103],v[156:159],a[204:207]
ds_read_b64_tr_b16 v[90:91],v2 offset:25696
v_min_u32 v3,2,v3
v_mfma_f32_16x16x32_bf16 a[204:207],v[96:99],v[140:143],a[204:207]
s_waitcnt lgkmcnt(8)
ds_read_b64_tr_b16 v[92:93],v2 offset:30816
ds_read_b64_tr_b16 v[94:95],v2 offset:35936
v_mfma_f32_16x16x32_bf16 a[208:211],v[112:115],v[32:35],a[208:211]
ds_read_b64_tr_b16 v[96:97],v1 offset:128
v_add3_u32 v3,v3,s64,1
v_mfma_f32_16x16x32_bf16 a[208:211],v[116:119],v[36:39],a[208:211]
ds_read_b64_tr_b16 v[98:99],v1 offset:5248
v_and_b32 v3,3,v3
v_mfma_f32_16x16x32_bf16 a[208:211],v[120:123],v[40:43],a[208:211]
ds_read_b64_tr_b16 v[100:101],v1 offset:10368
v_lshlrev_b32 v3,6,v3
v_mfma_f32_16x16x32_bf16 a[208:211],v[124:127],v[44:47],a[208:211]
ds_read_b64_tr_b16 v[102:103],v1 offset:15488
ds_read_b64_tr_b16 v[104:105],v1 offset:20608
v_mfma_f32_16x16x32_bf16 a[212:215],v[124:127],v[180:183],a[212:215]
ds_read_b64_tr_b16 v[106:107],v1 offset:25728
v_bfe_u32 v56,v6,6,2
v_mfma_f32_16x16x32_bf16 a[212:215],v[120:123],v[164:167],a[212:215]
ds_read_b64_tr_b16 v[108:109],v1 offset:30848
v_lshrrev_b32 v57,8,v6
v_mfma_f32_16x16x32_bf16 a[212:215],v[116:119],v[148:151],a[212:215]
buffer_store_dwordx4 v[140:143],v28,s[40:43],s79 offen offset:3072 sc1
v_mfma_f32_16x16x32_bf16 a[212:215],v[112:115],v[132:135],a[212:215]
ds_read_b64_tr_b16 v[110:111],v1 offset:35968
v_cmp_eq_u32 s[56:57],v56,v57
v_mfma_f32_16x16x32_bf16 a[216:219],v[112:115],v[196:199],a[216:219]
buffer_load_dwordx4 a[132:135],v6,s[28:31],s87 offen offset:1024 sc1
v_mfma_f32_16x16x32_bf16 a[216:219],v[116:119],v[212:215],a[216:219]
v_bfe_u32 v56,v6,4,1
v_lshlrev_b32 v56,4,v56
v_mfma_f32_16x16x32_bf16 a[216:219],v[120:123],v[228:231],a[216:219]
v_mov_b32 v57,0x3f80
v_lshlrev_b32 v56,v56,v57
v_mfma_f32_16x16x32_bf16 a[216:219],v[124:127],v[244:247],a[216:219]
v_cndmask_b32 v56,0,v56,s[56:57]
v_bfe_u32 v57,v6,5,1
v_mfma_f32_16x16x32_bf16 a[220:223],v[124:127],v[188:191],a[220:223]
ds_read_b64_tr_b16 v[124:125],v2 offset:30880
ds_read_b64_tr_b16 v[126:127],v2 offset:36000
v_mfma_f32_16x16x32_bf16 a[220:223],v[120:123],v[172:175],a[220:223]
ds_read_b64_tr_b16 v[120:121],v2 offset:20640
v_cmp_eq_u32 s[56:57],0,v57
v_mfma_f32_16x16x32_bf16 a[220:223],v[116:119],v[156:159],a[220:223]
)A"
R"A(s_add_u32 s36,s12,s66
s_addc_u32 s37,s13,0
ds_read_b64_tr_b16 v[116:117],v2 offset:10400
v_mfma_f32_16x16x32_bf16 a[220:223],v[112:115],v[140:143],a[220:223]
s_and_b32 s37,s37,0xffff
ds_read_b64_tr_b16 v[112:113],v2 offset:160
v_cndmask_b32 v48,0,v56,s[56:57]
v_mfma_f32_16x16x32_bf16 a[224:227],v[64:67],v[32:35],a[224:227]
s_mul_i32 s66,s61,307200
ds_read_b64_tr_b16 v[114:115],v2 offset:5280
ds_read_b64_tr_b16 v[118:119],v2 offset:15520
v_mfma_f32_16x16x32_bf16 a[224:227],v[68:71],v[36:39],a[224:227]
ds_read_b64_tr_b16 v[122:123],v2 offset:25760
v_cndmask_b32 v49,v56,0,s[56:57]
v_mfma_f32_16x16x32_bf16 a[224:227],v[72:75],v[40:43],a[224:227]
s_waitcnt lgkmcnt(8)
v_mov_b32 v58,s52
v_mov_b32 v59,s53
v_mfma_f32_16x16x32_bf16 a[224:227],v[76:79],v[44:47],a[224:227]
s_add_u32 s24,s16,s66
s_addc_u32 s25,s17,0
v_lshlrev_b32 v60,20,v6
v_mfma_f32_16x16x32_bf16 a[228:231],v[76:79],v[180:183],a[228:231]
buffer_load_dwordx4 a[136:139],v6,s[28:31],s87 offen offset:2048 sc1
v_mfma_f32_16x16x32_bf16 a[228:231],v[72:75],v[164:167],a[228:231]
s_mul_i32 s66,s60,16384
s_add_u32 s66,s66,s67
s_add_u32 s79,s66,12288
v_mfma_f32_16x16x32_bf16 a[228:231],v[68:71],v[148:151],a[228:231]
buffer_store_dwordx4 v[156:159],v28,s[40:43],s80 offen offset:3072 sc1
v_mfma_f32_16x16x32_bf16 a[228:231],v[64:67],v[132:135],a[228:231]
s_add_u32 s80,s66,77824
v_bfe_u32 v61,v6,4,1
v_add3_u32 v61,v61,s63,2
v_mfma_f32_16x16x32_bf16 a[232:235],v[64:67],v[196:199],a[232:235]
s_and_b32 s25,s25,0xffff
v_and_b32 v61,3,v61
v_lshlrev_b32 v61,8,v61
v_mfma_f32_16x16x32_bf16 a[232:235],v[68:71],v[212:215],a[232:235]
s_add_u32 s67,s77,8192
v_mov_b32 v50,0
v_mov_b32 v51,0
v_mfma_f32_16x16x32_bf16 a[232:235],v[72:75],v[228:231],a[232:235]
v_mov_b32 v52,0
v_mov_b32 v53,0
v_mfma_f32_16x16x32_bf16 a[232:235],v[76:79],v[244:247],a[232:235]
v_mov_b32 v54,v48
v_mov_b32 v55,v49
v_mfma_f32_16x16x32_bf16 a[236:239],v[76:79],v[188:191],a[236:239]
ds_read_b64_tr_b16 v[76:77],v1 offset:30912
ds_read_b64_tr_b16 v[78:79],v1 offset:36032
v_mfma_f32_16x16x32_bf16 a[236:239],v[72:75],v[172:175],a[236:239]
ds_read_b64_tr_b16 v[72:73],v1 offset:20672
v_bfe_u32 v56,v6,4,4
v_mfma_f32_16x16x32_bf16 a[236:239],v[68:71],v[156:159],a[236:239]
ds_read_b64_tr_b16 v[68:69],v1 offset:10432
v_mul_u32_u24 v56,1280,v56
v_mfma_f32_16x16x32_bf16 a[236:239],v[64:67],v[140:143],a[236:239]
ds_read_b64_tr_b16 v[64:65],v1 offset:192
v_bfe_u32 v57,v6,8,1
v_mfma_f32_16x16x32_bf16 a[240:243],v[80:83],v[32:35],a[240:243]
ds_read_b64_tr_b16 v[66:67],v1 offset:5312
ds_read_b64_tr_b16 v[70:71],v1 offset:15552
v_mfma_f32_16x16x32_bf16 a[240:243],v[84:87],v[36:39],a[240:243]
buffer_load_dwordx4 a[152:155],v6,s[28:31],s88 offen offset:2048 sc1
v_mfma_f32_16x16x32_bf16 a[240:243],v[88:91],v[40:43],a[240:243]
ds_read_b64_tr_b16 v[74:75],v1 offset:25792
v_lshl_add_u32 v56,v57,5,v56
v_mfma_f32_16x16x32_bf16 a[240:243],v[92:95],v[44:47],a[240:243]
v_lshrrev_b32 v57,9,v6
v_lshl_add_u32 v56,v57,4,v56
v_mfma_f32_16x16x32_bf16 a[244:247],v[92:95],v[180:183],a[244:247]
v_mfma_f32_16x16x32_bf16 a[244:247],v[88:91],v[164:167],a[244:247]
buffer_store_dwordx4 v[172:175],v28,s[40:43],s81 offen offset:3072 sc1
v_mfma_f32_16x16x32_bf16 a[244:247],v[84:87],v[148:151],a[244:247]
s_add_u32 s81,s66,143360
v_mfma_f32_16x16x32_bf16 a[244:247],v[80:83],v[132:135],a[244:247]
v_mfma_f32_16x16x32_bf16 a[248:251],v[80:83],v[196:199],a[248:251]
v_mfma_f32_16x16x32_bf16 a[248:251],v[84:87],v[212:215],a[248:251]
L_pd14_%=:
s_cbranch_execz L_trt1_%=
v_mfma_f32_16x16x32_bf16 a[248:251],v[88:91],v[228:231],a[248:251]
v_mfma_f32_16x16x32_bf16 a[248:251],v[92:95],v[244:247],a[248:251]
v_mfma_f32_16x16x32_bf16 a[252:255],v[92:95],v[188:191],a[252:255]
ds_read_b64_tr_b16 v[92:93],v2 offset:30944
ds_read_b64_tr_b16 v[94:95],v2 offset:36064
v_mfma_f32_16x16x32_bf16 a[252:255],v[88:91],v[172:175],a[252:255]
ds_read_b64_tr_b16 v[88:89],v2 offset:20704
v_mfma_f32_16x16x32_bf16 a[252:255],v[84:87],v[156:159],a[252:255]
buffer_load_dwordx4 a[168:171],v6,s[28:31],s89 offen offset:2048 sc1
v_mfma_f32_16x16x32_bf16 a[252:255],v[80:83],v[140:143],a[252:255]
ds_read_b64_tr_b16 v[80:81],v2 offset:224
ds_read_b64_tr_b16 v[82:83],v2 offset:5344
v_mfma_f32_16x16x32_bf16 a[0:3],v[96:99],v[32:35],a[0:3]
ds_read_b64_tr_b16 v[84:85],v2 offset:10464
v_mfma_f32_16x16x32_bf16 a[0:3],v[100:103],v[36:39],a[0:3]
ds_read_b64_tr_b16 v[86:87],v2 offset:15584
v_mfma_f32_16x16x32_bf16 a[0:3],v[104:107],v[40:43],a[0:3]
ds_read_b64_tr_b16 v[90:91],v2 offset:25824
v_mfma_f32_16x16x32_bf16 a[0:3],v[108:111],v[44:47],a[0:3]
v_mfma_f32_16x16x32_bf16 a[4:7],v[108:111],v[180:183],a[4:7]
buffer_store_dwordx4 v[188:191],v28,s[40:43],s82 offen offset:3072 sc1
v_mfma_f32_16x16x32_bf16 a[4:7],v[104:107],v[164:167],a[4:7]
s_add_u32 s82,s66,208896
s_mul_i32 s66,s64,320
v_mfma_f32_16x16x32_bf16 a[4:7],v[100:103],v[148:151],a[4:7]
v_mfma_f32_16x16x32_bf16 a[4:7],v[96:99],v[132:135],a[4:7]
v_mfma_f32_16x16x32_bf16 a[8:11],v[96:99],v[196:199],a[8:11]
v_mfma_f32_16x16x32_bf16 a[8:11],v[100:103],v[212:215],a[8:11]
v_mfma_f32_16x16x32_bf16 a[8:11],v[104:107],v[228:231],a[8:11]
v_mfma_f32_16x16x32_bf16 a[8:11],v[108:111],v[244:247],a[8:11]
buffer_load_dwordx4 a[184:187],v6,s[28:31],s90 offen offset:2048 sc1
v_mfma_f32_16x16x32_bf16 a[12:15],v[108:111],v[188:191],a[12:15]
ds_read_b64_tr_b16 v[108:109],v1 offset:30976
ds_read_b64_tr_b16 v[110:111],v1 offset:36096
v_mfma_f32_16x16x32_bf16 a[12:15],v[104:107],v[172:175],a[12:15]
ds_read_b64_tr_b16 v[104:105],v1 offset:20736
v_mfma_f32_16x16x32_bf16 a[12:15],v[100:103],v[156:159],a[12:15]
ds_read_b64_tr_b16 v[100:101],v1 offset:10496
v_mfma_f32_16x16x32_bf16 a[12:15],v[96:99],v[140:143],a[12:15]
s_waitcnt lgkmcnt(4)
ds_read_b64_tr_b16 v[96:97],v1 offset:256
v_mfma_f32_16x16x32_bf16 a[16:19],v[112:115],v[32:35],a[16:19]
ds_read_b64_tr_b16 v[98:99],v1 offset:5376
ds_read_b64_tr_b16 v[102:103],v1 offset:15616
v_mfma_f32_16x16x32_bf16 a[16:19],v[116:119],v[36:39],a[16:19]
ds_read_b64_tr_b16 v[106:107],v1 offset:25856
v_add_u32 v1,0xffff6000,v1
v_mfma_f32_16x16x32_bf16 a[16:19],v[120:123],v[40:43],a[16:19]
v_mfma_f32_16x16x32_bf16 a[16:19],v[124:127],v[44:47],a[16:19]
v_mfma_f32_16x16x32_bf16 a[20:23],v[124:127],v[180:183],a[20:23]
v_mfma_f32_16x16x32_bf16 a[20:23],v[120:123],v[164:167],a[20:23]
v_mfma_f32_16x16x32_bf16 a[20:23],v[116:119],v[148:151],a[20:23]
v_mfma_f32_16x16x32_bf16 a[20:23],v[112:115],v[132:135],a[20:23]
v_mfma_f32_16x16x32_bf16 a[24:27],v[112:115],v[196:199],a[24:27]
buffer_load_dwordx4 a[188:191],v28,s[28:31],s90 offen offset:3072 sc1
v_mfma_f32_16x16x32_bf16 a[24:27],v[116:119],v[212:215],a[24:27]
v_mfma_f32_16x16x32_bf16 a[24:27],v[120:123],v[228:231],a[24:27]
v_mfma_f32_16x16x32_bf16 a[24:27],v[124:127],v[244:247],a[24:27]
v_mfma_f32_16x16x32_bf16 a[28:31],v[124:127],v[188:191],a[28:31]
ds_read_b64_tr_b16 v[124:125],v2 offset:31008
ds_read_b64_tr_b16 v[126:127],v2 offset:36128
v_mfma_f32_16x16x32_bf16 a[28:31],v[120:123],v[172:175],a[28:31]
ds_read_b64_tr_b16 v[120:121],v2 offset:20768
v_mfma_f32_16x16x32_bf16 a[28:31],v[116:119],v[156:159],a[28:31]
ds_read_b64_tr_b16 v[116:117],v2 offset:10528
v_mfma_f32_16x16x32_bf16 a[28:31],v[112:115],v[140:143],a[28:31]
ds_read_b64_tr_b16 v[112:113],v2 offset:288
v_mfma_f32_16x16x32_bf16 v[128:131],v[64:67],v[32:35],v[128:131]
ds_read_b64_tr_b16 v[114:115],v2 offset:5408
ds_read_b64_tr_b16 v[118:119],v2 offset:15648
v_mfma_f32_16x16x32_bf16 v[128:131],v[68:71],v[36:39],v[128:131]
ds_read_b64_tr_b16 v[122:123],v2 offset:25888
v_add_u32 v2,0xffff6000,v2
v_mfma_f32_16x16x32_bf16 v[128:131],v[72:75],v[40:43],v[128:131]
v_mfma_f32_16x16x32_bf16 v[128:131],v[76:79],v[44:47],v[128:131]
v_mfma_f32_16x16x32_bf16 v[136:139],v[76:79],v[180:183],v[136:139]
v_mfma_f32_16x16x32_bf16 v[136:139],v[72:75],v[164:167],v[136:139]
buffer_load_dwordx4 a[172:175],v28,s[28:31],s89 offen offset:3072 sc1
v_mfma_f32_16x16x32_bf16 v[136:139],v[68:71],v[148:151],v[136:139]
v_mfma_f32_16x16x32_bf16 v[136:139],v[64:67],v[132:135],v[136:139]
v_mfma_f32_16x16x32_bf16 v[144:147],v[64:67],v[196:199],v[144:147]
v_mfma_f32_16x16x32_bf16 v[144:147],v[68:71],v[212:215],v[144:147]
v_mfma_f32_16x16x32_bf16 v[144:147],v[72:75],v[228:231],v[144:147]
v_mfma_f32_16x16x32_bf16 v[144:147],v[76:79],v[244:247],v[144:147]
v_mfma_f32_16x16x32_bf16 v[152:155],v[76:79],v[188:191],v[152:155]
v_mfma_f32_16x16x32_bf16 v[152:155],v[72:75],v[172:175],v[152:155]
v_mfma_f32_16x16x32_bf16 v[152:155],v[68:71],v[156:159],v[152:155]
v_mfma_f32_16x16x32_bf16 v[152:155],v[64:67],v[140:143],v[152:155]
s_mov_b64 exec,0
s_cmp_eq_u32 s60,0
s_cbranch_scc1 L_pd16_%=
s_cmp_eq_u32 s60,1
s_cbranch_scc1 L_pd17_%=
s_cmp_eq_u32 s60,2
s_cbranch_scc1 L_pd18_%=
s_branch L_trtg2F_%=
L_trtg2F_%=:
s_mov_b64 exec,-1
s_waitcnt lgkmcnt(0)
s_barrier
v_mfma_f32_16x16x32_bf16 v[160:163],v[80:83],v[32:35],v[160:163]
buffer_load_dwordx4 v24,s[48:51],s83 offen lds
v_mfma_f32_16x16x32_bf16 v[160:163],v[84:87],v[36:39],v[160:163]
ds_read_b64_tr_b16 v[64:65],v1
ds_read_b64_tr_b16 v[66:67],v1 offset:5120
v_mfma_f32_16x16x32_bf16 v[160:163],v[88:91],v[40:43],v[160:163]
ds_read_b64_tr_b16 v[68:69],v1 offset:10240
v_mfma_f32_16x16x32_bf16 v[160:163],v[92:95],v[44:47],v[160:163]
ds_read_b64_tr_b16 v[70:71],v1 offset:15360
v_mfma_f32_16x16x32_bf16 v[168:171],v[92:95],v[180:183],v[168:171]
ds_read_b64_tr_b16 v[72:73],v1 offset:20480
v_mfma_f32_16x16x32_bf16 v[168:171],v[88:91],v[164:167],v[168:171]
buffer_load_dwordx4 a[156:159],v28,s[28:31],s88 offen offset:3072 sc1
v_mfma_f32_16x16x32_bf16 v[168:171],v[84:87],v[148:151],v[168:171]
ds_read_b64_tr_b16 v[74:75],v1 offset:25600
ds_read_b64_tr_b16 v[76:77],v1 offset:30720
v_mfma_f32_16x16x32_bf16 v[168:171],v[80:83],v[132:135],v[168:171]
ds_read_b64_tr_b16 v[78:79],v1 offset:35840
v_mfma_f32_16x16x32_bf16 v[176:179],v[80:83],v[196:199],v[176:179]
v_mfma_f32_16x16x32_bf16 v[176:179],v[84:87],v[212:215],v[176:179]
v_mfma_f32_16x16x32_bf16 v[176:179],v[88:91],v[228:231],v[176:179]
v_mfma_f32_16x16x32_bf16 v[176:179],v[92:95],v[244:247],v[176:179]
buffer_load_dwordx4 v25,s[48:51],s83 offen offset:1024 lds
v_mfma_f32_16x16x32_bf16 v[184:187],v[92:95],v[188:191],v[184:187]
ds_read_b64_tr_b16 v[92:93],v2 offset:30752
ds_read_b64_tr_b16 v[94:95],v2 offset:35872
v_mfma_f32_16x16x32_bf16 v[184:187],v[88:91],v[172:175],v[184:187]
ds_read_b64_tr_b16 v[88:89],v2 offset:20512
v_mfma_f32_16x16x32_bf16 v[184:187],v[84:87],v[156:159],v[184:187]
ds_read_b64_tr_b16 v[84:85],v2 offset:10272
v_mfma_f32_16x16x32_bf16 v[184:187],v[80:83],v[140:143],v[184:187]
ds_read_b64_tr_b16 v[80:81],v2 offset:32
v_mfma_f32_16x16x32_bf16 v[192:195],v[96:99],v[32:35],v[192:195]
buffer_load_dwordx4 a[140:143],v28,s[28:31],s87 offen offset:3072 sc1
v_mfma_f32_16x16x32_bf16 v[192:195],v[100:103],v[36:39],v[192:195]
s_add_u32 s87,s64,2
s_and_b32 s87,s87,3
s_lshl_b32 s87,s87,1
v_mfma_f32_16x16x32_bf16 v[192:195],v[104:107],v[40:43],v[192:195]
s_lshl_b32 s87,s87,16
s_add_u32 s88,s87,4096
s_add_u32 s89,s87,8192
v_mfma_f32_16x16x32_bf16 v[192:195],v[108:111],v[44:47],v[192:195]
s_add_u32 s90,s87,12288
ds_read_b64_tr_b16 v[82:83],v2 offset:5152
ds_read_b64_tr_b16 v[86:87],v2 offset:15392
v_mfma_f32_16x16x32_bf16 v[200:203],v[108:111],v[180:183],v[200:203]
ds_read_b64_tr_b16 v[90:91],v2 offset:25632
v_mfma_f32_16x16x32_bf16 v[200:203],v[104:107],v[164:167],v[200:203]
buffer_load_dwordx4 a[68:71],v6,s[28:31],s87 offen sc1
v_mfma_f32_16x16x32_bf16 v[200:203],v[100:103],v[148:151],v[200:203]
v_mfma_f32_16x16x32_bf16 v[200:203],v[96:99],v[132:135],v[200:203]
v_mfma_f32_16x16x32_bf16 v[208:211],v[96:99],v[196:199],v[208:211]
v_mfma_f32_16x16x32_bf16 v[208:211],v[100:103],v[212:215],v[208:211]
v_mfma_f32_16x16x32_bf16 v[208:211],v[104:107],v[228:231],v[208:211]
buffer_load_dwordx4 a[76:79],v6,s[28:31],s88 offen sc1
v_mfma_f32_16x16x32_bf16 v[208:211],v[108:111],v[244:247],v[208:211]
v_mfma_f32_16x16x32_bf16 v[216:219],v[108:111],v[188:191],v[216:219]
ds_read_b64_tr_b16 v[108:109],v1 offset:30784
ds_read_b64_tr_b16 v[110:111],v1 offset:35904
v_mfma_f32_16x16x32_bf16 v[216:219],v[104:107],v[172:175],v[216:219]
)A"
R"A(ds_read_b64_tr_b16 v[104:105],v1 offset:20544
v_mfma_f32_16x16x32_bf16 v[216:219],v[100:103],v[156:159],v[216:219]
ds_read_b64_tr_b16 v[100:101],v1 offset:10304
v_mfma_f32_16x16x32_bf16 v[216:219],v[96:99],v[140:143],v[216:219]
buffer_load_dwordx4 a[84:87],v6,s[28:31],s89 offen sc1
v_mfma_f32_16x16x32_bf16 v[224:227],v[112:115],v[32:35],v[224:227]
ds_read_b64_tr_b16 v[96:97],v1 offset:64
ds_read_b64_tr_b16 v[98:99],v1 offset:5184
v_mfma_f32_16x16x32_bf16 v[224:227],v[116:119],v[36:39],v[224:227]
ds_read_b64_tr_b16 v[102:103],v1 offset:15424
v_mfma_f32_16x16x32_bf16 v[224:227],v[120:123],v[40:43],v[224:227]
ds_read_b64_tr_b16 v[106:107],v1 offset:25664
v_mfma_f32_16x16x32_bf16 v[224:227],v[124:127],v[44:47],v[224:227]
v_mfma_f32_16x16x32_bf16 v[232:235],v[124:127],v[180:183],v[232:235]
buffer_load_dwordx4 a[92:95],v6,s[28:31],s90 offen sc1
v_mfma_f32_16x16x32_bf16 v[232:235],v[120:123],v[164:167],v[232:235]
v_mfma_f32_16x16x32_bf16 v[232:235],v[116:119],v[148:151],v[232:235]
v_mfma_f32_16x16x32_bf16 v[232:235],v[112:115],v[132:135],v[232:235]
v_mfma_f32_16x16x32_bf16 v[240:243],v[112:115],v[196:199],v[240:243]
v_mfma_f32_16x16x32_bf16 v[240:243],v[116:119],v[212:215],v[240:243]
buffer_load_dwordx4 a[124:127],v6,s[28:31],s90 offen offset:1024 sc1
v_mfma_f32_16x16x32_bf16 v[240:243],v[120:123],v[228:231],v[240:243]
v_mfma_f32_16x16x32_bf16 v[240:243],v[124:127],v[244:247],v[240:243]
v_mfma_f32_16x16x32_bf16 v[248:251],v[124:127],v[188:191],v[248:251]
ds_read_b64_tr_b16 v[124:125],v2 offset:30816
ds_read_b64_tr_b16 v[126:127],v2 offset:35936
v_mfma_f32_16x16x32_bf16 v[248:251],v[120:123],v[172:175],v[248:251]
ds_read_b64_tr_b16 v[120:121],v2 offset:20576
v_mfma_f32_16x16x32_bf16 v[248:251],v[116:119],v[156:159],v[248:251]
buffer_load_dwordx4 a[116:119],v6,s[28:31],s89 offen offset:1024 sc1
v_mfma_f32_16x16x32_bf16 v[248:251],v[112:115],v[140:143],v[248:251]
s_waitcnt vmcnt(31) lgkmcnt(3)
ds_read_b64_tr_b16 v[112:113],v2 offset:96
ds_read_b64_tr_b16 v[114:115],v2 offset:5216
v_mfma_f32_16x16x32_bf16 a[192:195],v[64:67],a[128:131],a[192:195]
ds_read_b64_tr_b16 v[116:117],v2 offset:10336
v_mfma_f32_16x16x32_bf16 a[192:195],v[68:71],a[144:147],a[192:195]
s_waitcnt vmcnt(29)
ds_read_b64_tr_b16 v[118:119],v2 offset:15456
v_mfma_f32_16x16x32_bf16 a[192:195],v[72:75],a[160:163],a[192:195]
s_waitcnt vmcnt(25)
ds_read_b64_tr_b16 v[122:123],v2 offset:25696
v_mfma_f32_16x16x32_bf16 a[192:195],v[76:79],a[176:179],a[192:195]
buffer_load_dwordx4 a[108:111],v6,s[28:31],s88 offen offset:1024 sc1
v_mfma_f32_16x16x32_bf16 a[196:199],v[76:79],a[180:183],a[196:199]
s_waitcnt vmcnt(24)
v_mfma_f32_16x16x32_bf16 a[196:199],v[72:75],a[164:167],a[196:199]
s_waitcnt vmcnt(22)
v_mfma_f32_16x16x32_bf16 a[196:199],v[68:71],a[148:151],a[196:199]
s_waitcnt vmcnt(19)
v_mfma_f32_16x16x32_bf16 a[196:199],v[64:67],a[132:135],a[196:199]
s_waitcnt vmcnt(17)
v_mfma_f32_16x16x32_bf16 a[200:203],v[64:67],a[136:139],a[200:203]
buffer_load_dwordx4 a[100:103],v6,s[28:31],s87 offen offset:1024 sc1
v_mfma_f32_16x16x32_bf16 a[200:203],v[68:71],a[152:155],a[200:203]
s_waitcnt vmcnt(16)
v_mfma_f32_16x16x32_bf16 a[200:203],v[72:75],a[168:171],a[200:203]
s_waitcnt vmcnt(14)
v_mfma_f32_16x16x32_bf16 a[200:203],v[76:79],a[184:187],a[200:203]
s_waitcnt vmcnt(13)
v_mfma_f32_16x16x32_bf16 a[204:207],v[76:79],a[188:191],a[204:207]
s_waitcnt vmcnt(10)
ds_read_b64_tr_b16 v[76:77],v1 offset:30848
ds_read_b64_tr_b16 v[78:79],v1 offset:35968
v_mfma_f32_16x16x32_bf16 a[204:207],v[72:75],a[172:175],a[204:207]
buffer_load_dwordx4 a[64:67],v6,s[28:31],s87 offen offset:2048 sc1
v_mfma_f32_16x16x32_bf16 a[204:207],v[68:71],a[156:159],a[204:207]
s_waitcnt vmcnt(9)
ds_read_b64_tr_b16 v[68:69],v1 offset:10368
ds_read_b64_tr_b16 v[70:71],v1 offset:15488
v_mfma_f32_16x16x32_bf16 a[204:207],v[64:67],a[140:143],a[204:207]
ds_read_b64_tr_b16 v[64:65],v1 offset:128
v_mfma_f32_16x16x32_bf16 a[208:211],v[80:83],a[128:131],a[208:211]
ds_read_b64_tr_b16 v[66:67],v1 offset:5248
v_mfma_f32_16x16x32_bf16 a[208:211],v[84:87],a[144:147],a[208:211]
ds_read_b64_tr_b16 v[72:73],v1 offset:20608
v_mfma_f32_16x16x32_bf16 a[208:211],v[88:91],a[160:163],a[208:211]
ds_read_b64_tr_b16 v[74:75],v1 offset:25728
v_mfma_f32_16x16x32_bf16 a[208:211],v[92:95],a[176:179],a[208:211]
buffer_load_dwordx4 v26,s[48:51],s83 offen offset:2048 lds
v_mfma_f32_16x16x32_bf16 a[212:215],v[92:95],a[180:183],a[212:215]
v_mfma_f32_16x16x32_bf16 a[212:215],v[88:91],a[164:167],a[212:215]
v_mfma_f32_16x16x32_bf16 a[212:215],v[84:87],a[148:151],a[212:215]
v_mfma_f32_16x16x32_bf16 a[212:215],v[80:83],a[132:135],a[212:215]
v_mfma_f32_16x16x32_bf16 a[216:219],v[80:83],a[136:139],a[216:219]
buffer_load_dwordx4 a[72:75],v6,s[28:31],s88 offen offset:2048 sc1
v_mfma_f32_16x16x32_bf16 a[216:219],v[84:87],a[152:155],a[216:219]
v_mfma_f32_16x16x32_bf16 a[216:219],v[88:91],a[168:171],a[216:219]
v_mfma_f32_16x16x32_bf16 a[216:219],v[92:95],a[184:187],a[216:219]
v_mfma_f32_16x16x32_bf16 a[220:223],v[92:95],a[188:191],a[220:223]
ds_read_b64_tr_b16 v[92:93],v2 offset:30880
ds_read_b64_tr_b16 v[94:95],v2 offset:36000
v_mfma_f32_16x16x32_bf16 a[220:223],v[88:91],a[172:175],a[220:223]
ds_read_b64_tr_b16 v[88:89],v2 offset:20640
v_mfma_f32_16x16x32_bf16 a[220:223],v[84:87],a[156:159],a[220:223]
buffer_load_dwordx4 v27,s[48:51],s83 offen offset:3072 lds
v_mfma_f32_16x16x32_bf16 a[220:223],v[80:83],a[140:143],a[220:223]
s_add_u32 m0,s86,126976
ds_read_b64_tr_b16 v[80:81],v2 offset:160
ds_read_b64_tr_b16 v[82:83],v2 offset:5280
s_barrier
v_mfma_f32_16x16x32_bf16 a[224:227],v[96:99],a[128:131],a[224:227]
ds_read_b64_tr_b16 v[84:85],v2 offset:10400
v_mfma_f32_16x16x32_bf16 a[224:227],v[100:103],a[144:147],a[224:227]
ds_read_b64_tr_b16 v[86:87],v2 offset:15520
v_mfma_f32_16x16x32_bf16 a[224:227],v[104:107],a[160:163],a[224:227]
buffer_store_dwordx2 v[8:9],v10,s[44:47],s18 offen offset:1280 sc1
v_mfma_f32_16x16x32_bf16 a[224:227],v[108:111],a[176:179],a[224:227]
ds_read_b64_tr_b16 v[90:91],v2 offset:25760
v_mfma_f32_16x16x32_bf16 a[228:231],v[108:111],a[180:183],a[228:231]
v_mfma_f32_16x16x32_bf16 a[228:231],v[104:107],a[164:167],a[228:231]
v_mfma_f32_16x16x32_bf16 a[228:231],v[100:103],a[148:151],a[228:231]
v_mfma_f32_16x16x32_bf16 a[228:231],v[96:99],a[132:135],a[228:231]
buffer_load_dwordx4 a[80:83],v6,s[28:31],s89 offen offset:2048 sc1
v_mfma_f32_16x16x32_bf16 a[232:235],v[96:99],a[136:139],a[232:235]
v_mfma_f32_16x16x32_bf16 a[232:235],v[100:103],a[152:155],a[232:235]
v_mfma_f32_16x16x32_bf16 a[232:235],v[104:107],a[168:171],a[232:235]
v_mfma_f32_16x16x32_bf16 a[232:235],v[108:111],a[184:187],a[232:235]
v_mfma_f32_16x16x32_bf16 a[236:239],v[108:111],a[188:191],a[236:239]
s_waitcnt lgkmcnt(0)
ds_read_b64_tr_b16 v[108:109],v1 offset:30912
ds_read_b64_tr_b16 v[110:111],v1 offset:36032
v_mfma_f32_16x16x32_bf16 a[236:239],v[104:107],a[172:175],a[236:239]
buffer_load_dwordx4 v7,s[48:51],s83 offen lds
v_mfma_f32_16x16x32_bf16 a[236:239],v[100:103],a[156:159],a[236:239]
s_add_u32 m0,s86,128000
ds_read_b64_tr_b16 v[100:101],v1 offset:10432
ds_read_b64_tr_b16 v[102:103],v1 offset:15552
v_mfma_f32_16x16x32_bf16 a[236:239],v[96:99],a[140:143],a[236:239]
s_add_u32 s83,s64,1
s_and_b32 s83,s83,3
ds_read_b64_tr_b16 v[96:97],v1 offset:192
v_mfma_f32_16x16x32_bf16 a[240:243],v[112:115],a[128:131],a[240:243]
s_lshl_b32 s83,s83,1
s_or_b32 s83,s83,1
ds_read_b64_tr_b16 v[98:99],v1 offset:5312
v_mfma_f32_16x16x32_bf16 a[240:243],v[116:119],a[144:147],a[240:243]
s_mul_i32 s83,s83,163840
s_add_u32 s83,s83,s85
ds_read_b64_tr_b16 v[104:105],v1 offset:20672
v_mfma_f32_16x16x32_bf16 a[240:243],v[120:123],a[160:163],a[240:243]
buffer_load_dwordx4 a[88:91],v6,s[28:31],s90 offen offset:2048 sc1
v_mfma_f32_16x16x32_bf16 a[240:243],v[124:127],a[176:179],a[240:243]
ds_read_b64_tr_b16 v[106:107],v1 offset:25792
v_mfma_f32_16x16x32_bf16 a[244:247],v[124:127],a[180:183],a[244:247]
v_mfma_f32_16x16x32_bf16 a[244:247],v[120:123],a[164:167],a[244:247]
v_mfma_f32_16x16x32_bf16 a[244:247],v[116:119],a[148:151],a[244:247]
v_mfma_f32_16x16x32_bf16 a[244:247],v[112:115],a[132:135],a[244:247]
v_mfma_f32_16x16x32_bf16 a[248:251],v[112:115],a[136:139],a[248:251]
buffer_load_dwordx4 v24,s[48:51],s84 offen lds
v_mfma_f32_16x16x32_bf16 a[248:251],v[116:119],a[152:155],a[248:251]
v_mfma_f32_16x16x32_bf16 a[248:251],v[120:123],a[168:171],a[248:251]
v_mfma_f32_16x16x32_bf16 a[248:251],v[124:127],a[184:187],a[248:251]
v_mfma_f32_16x16x32_bf16 a[252:255],v[124:127],a[188:191],a[252:255]
ds_read_b64_tr_b16 v[124:125],v2 offset:30944
ds_read_b64_tr_b16 v[126:127],v2 offset:36064
v_mfma_f32_16x16x32_bf16 a[252:255],v[120:123],a[172:175],a[252:255]
buffer_load_dwordx4 a[120:123],v28,s[28:31],s90 offen offset:3072 sc1
v_mfma_f32_16x16x32_bf16 a[252:255],v[116:119],a[156:159],a[252:255]
ds_read_b64_tr_b16 v[116:117],v2 offset:10464
ds_read_b64_tr_b16 v[118:119],v2 offset:15584
v_mfma_f32_16x16x32_bf16 a[252:255],v[112:115],a[140:143],a[252:255]
ds_read_b64_tr_b16 v[112:113],v2 offset:224
v_mfma_f32_16x16x32_bf16 a[0:3],v[64:67],a[128:131],a[0:3]
ds_read_b64_tr_b16 v[114:115],v2 offset:5344
v_mfma_f32_16x16x32_bf16 a[0:3],v[68:71],a[144:147],a[0:3]
ds_read_b64_tr_b16 v[120:121],v2 offset:20704
v_mfma_f32_16x16x32_bf16 a[0:3],v[72:75],a[160:163],a[0:3]
ds_read_b64_tr_b16 v[122:123],v2 offset:25824
v_mfma_f32_16x16x32_bf16 a[0:3],v[76:79],a[176:179],a[0:3]
buffer_load_dwordx4 v25,s[48:51],s84 offen offset:1024 lds
v_mfma_f32_16x16x32_bf16 a[4:7],v[76:79],a[180:183],a[4:7]
v_mfma_f32_16x16x32_bf16 a[4:7],v[72:75],a[164:167],a[4:7]
v_mfma_f32_16x16x32_bf16 a[4:7],v[68:71],a[148:151],a[4:7]
v_mfma_f32_16x16x32_bf16 a[4:7],v[64:67],a[132:135],a[4:7]
v_mfma_f32_16x16x32_bf16 a[8:11],v[64:67],a[136:139],a[8:11]
buffer_load_dwordx4 a[112:115],v28,s[28:31],s89 offen offset:3072 sc1
v_mfma_f32_16x16x32_bf16 a[8:11],v[68:71],a[152:155],a[8:11]
v_mfma_f32_16x16x32_bf16 a[8:11],v[72:75],a[168:171],a[8:11]
v_mfma_f32_16x16x32_bf16 a[8:11],v[76:79],a[184:187],a[8:11]
v_mfma_f32_16x16x32_bf16 a[12:15],v[76:79],a[188:191],a[12:15]
ds_read_b64_tr_b16 v[76:77],v1 offset:30976
ds_read_b64_tr_b16 v[78:79],v1 offset:36096
v_mfma_f32_16x16x32_bf16 a[12:15],v[72:75],a[172:175],a[12:15]
buffer_load_dwordx4 a[104:107],v28,s[28:31],s88 offen offset:3072 sc1
v_mfma_f32_16x16x32_bf16 a[12:15],v[68:71],a[156:159],a[12:15]
ds_read_b64_tr_b16 v[68:69],v1 offset:10496
ds_read_b64_tr_b16 v[70:71],v1 offset:15616
v_mfma_f32_16x16x32_bf16 a[12:15],v[64:67],a[140:143],a[12:15]
ds_read_b64_tr_b16 v[64:65],v1 offset:256
v_mfma_f32_16x16x32_bf16 a[16:19],v[80:83],a[128:131],a[16:19]
ds_read_b64_tr_b16 v[66:67],v1 offset:5376
v_mfma_f32_16x16x32_bf16 a[16:19],v[84:87],a[144:147],a[16:19]
ds_read_b64_tr_b16 v[72:73],v1 offset:20736
v_mfma_f32_16x16x32_bf16 a[16:19],v[88:91],a[160:163],a[16:19]
ds_read_b64_tr_b16 v[74:75],v1 offset:25856
v_add_u32 v1,0xffff6000,v1
v_mfma_f32_16x16x32_bf16 a[16:19],v[92:95],a[176:179],a[16:19]
buffer_load_dwordx4 v26,s[48:51],s84 offen offset:2048 lds
v_mfma_f32_16x16x32_bf16 a[20:23],v[92:95],a[180:183],a[20:23]
v_mfma_f32_16x16x32_bf16 a[20:23],v[88:91],a[164:167],a[20:23]
v_mfma_f32_16x16x32_bf16 a[20:23],v[84:87],a[148:151],a[20:23]
v_mfma_f32_16x16x32_bf16 a[20:23],v[80:83],a[132:135],a[20:23]
v_mfma_f32_16x16x32_bf16 a[24:27],v[80:83],a[136:139],a[24:27]
buffer_load_dwordx4 a[96:99],v28,s[28:31],s87 offen offset:3072 sc1
v_mfma_f32_16x16x32_bf16 a[24:27],v[84:87],a[152:155],a[24:27]
s_add_u32 s87,s64,3
s_and_b32 s87,s87,3
s_lshl_b32 s87,s87,1
v_mfma_f32_16x16x32_bf16 a[24:27],v[88:91],a[168:171],a[24:27]
s_lshl_b32 s87,s87,16
s_add_u32 s88,s87,4096
s_add_u32 s89,s87,8192
v_mfma_f32_16x16x32_bf16 a[24:27],v[92:95],a[184:187],a[24:27]
s_add_u32 s90,s87,12288
v_mfma_f32_16x16x32_bf16 a[28:31],v[92:95],a[188:191],a[28:31]
ds_read_b64_tr_b16 v[92:93],v2 offset:31008
ds_read_b64_tr_b16 v[94:95],v2 offset:36128
v_mfma_f32_16x16x32_bf16 a[28:31],v[88:91],a[172:175],a[28:31]
s_waitcnt lgkmcnt(2)
ds_read_b64_tr_b16 v[88:89],v2 offset:20768
v_mfma_f32_16x16x32_bf16 a[28:31],v[84:87],a[156:159],a[28:31]
)A"
R"A(buffer_load_dwordx4 v27,s[48:51],s84 offen offset:3072 lds
v_mfma_f32_16x16x32_bf16 a[28:31],v[80:83],a[140:143],a[28:31]
s_add_u32 m0,s86,132096
ds_read_b64_tr_b16 v[80:81],v2 offset:288
ds_read_b64_tr_b16 v[82:83],v2 offset:5408
v_mfma_f32_16x16x32_bf16 v[128:131],v[96:99],a[128:131],v[128:131]
ds_read_b64_tr_b16 v[84:85],v2 offset:10528
v_mfma_f32_16x16x32_bf16 v[128:131],v[100:103],a[144:147],v[128:131]
ds_read_b64_tr_b16 v[86:87],v2 offset:15648
v_mfma_f32_16x16x32_bf16 v[128:131],v[104:107],a[160:163],v[128:131]
ds_read_b64_tr_b16 v[90:91],v2 offset:25888
v_add_u32 v2,0xffff6000,v2
v_mfma_f32_16x16x32_bf16 v[128:131],v[108:111],a[176:179],v[128:131]
v_mfma_f32_16x16x32_bf16 v[136:139],v[108:111],a[180:183],v[136:139]
buffer_load_dwordx4 v7,s[48:51],s84 offen lds
v_mfma_f32_16x16x32_bf16 v[136:139],v[104:107],a[164:167],v[136:139]
s_add_u32 m0,s86,81920
s_add_u32 s84,s83,20480
v_mfma_f32_16x16x32_bf16 v[136:139],v[100:103],a[148:151],v[136:139]
v_mfma_f32_16x16x32_bf16 v[136:139],v[96:99],a[132:135],v[136:139]
v_mfma_f32_16x16x32_bf16 v[144:147],v[96:99],a[136:139],v[144:147]
v_mfma_f32_16x16x32_bf16 v[144:147],v[100:103],a[152:155],v[144:147]
v_mfma_f32_16x16x32_bf16 v[144:147],v[104:107],a[168:171],v[144:147]
buffer_load_dwordx4 v24,s[48:51],s83 offen lds
v_mfma_f32_16x16x32_bf16 v[144:147],v[108:111],a[184:187],v[144:147]
v_mfma_f32_16x16x32_bf16 v[152:155],v[108:111],a[188:191],v[152:155]
ds_read_b64_tr_b16 v[108:109],v1 offset:30720
ds_read_b64_tr_b16 v[110:111],v1 offset:35840
v_mfma_f32_16x16x32_bf16 v[152:155],v[104:107],a[172:175],v[152:155]
ds_read_b64_tr_b16 v[104:105],v1 offset:20480
v_mfma_f32_16x16x32_bf16 v[152:155],v[100:103],a[156:159],v[152:155]
ds_read_b64_tr_b16 v[100:101],v1 offset:10240
v_mfma_f32_16x16x32_bf16 v[152:155],v[96:99],a[140:143],v[152:155]
ds_read_b64_tr_b16 v[96:97],v1
v_mfma_f32_16x16x32_bf16 v[160:163],v[112:115],a[128:131],v[160:163]
buffer_load_dwordx4 v25,s[48:51],s83 offen offset:1024 lds
v_mfma_f32_16x16x32_bf16 v[160:163],v[116:119],a[144:147],v[160:163]
ds_read_b64_tr_b16 v[98:99],v1 offset:5120
ds_read_b64_tr_b16 v[102:103],v1 offset:15360
v_mfma_f32_16x16x32_bf16 v[160:163],v[120:123],a[160:163],v[160:163]
ds_read_b64_tr_b16 v[106:107],v1 offset:25600
v_mfma_f32_16x16x32_bf16 v[160:163],v[124:127],a[176:179],v[160:163]
v_mfma_f32_16x16x32_bf16 v[168:171],v[124:127],a[180:183],v[168:171]
v_mfma_f32_16x16x32_bf16 v[168:171],v[120:123],a[164:167],v[168:171]
v_mfma_f32_16x16x32_bf16 v[168:171],v[116:119],a[148:151],v[168:171]
buffer_load_dwordx4 v26,s[48:51],s83 offen offset:2048 lds
v_mfma_f32_16x16x32_bf16 v[168:171],v[112:115],a[132:135],v[168:171]
v_mfma_f32_16x16x32_bf16 v[176:179],v[112:115],a[136:139],v[176:179]
v_mfma_f32_16x16x32_bf16 v[176:179],v[116:119],a[152:155],v[176:179]
v_mfma_f32_16x16x32_bf16 v[176:179],v[120:123],a[168:171],v[176:179]
v_mfma_f32_16x16x32_bf16 v[176:179],v[124:127],a[184:187],v[176:179]
v_mfma_f32_16x16x32_bf16 v[184:187],v[124:127],a[188:191],v[184:187]
buffer_load_dwordx4 v27,s[48:51],s83 offen offset:3072 lds
v_mfma_f32_16x16x32_bf16 v[184:187],v[120:123],a[172:175],v[184:187]
s_add_u32 m0,s86,86016
ds_read_b64_tr_b16 v[120:121],v2 offset:20512
ds_read_b64_tr_b16 v[122:123],v2 offset:25632
v_mfma_f32_16x16x32_bf16 v[184:187],v[116:119],a[156:159],v[184:187]
ds_read_b64_tr_b16 v[116:117],v2 offset:10272
v_mfma_f32_16x16x32_bf16 v[184:187],v[112:115],a[140:143],v[184:187]
ds_read_b64_tr_b16 v[112:113],v2 offset:32
v_mfma_f32_16x16x32_bf16 v[192:195],v[64:67],a[128:131],v[192:195]
ds_read_b64_tr_b16 v[114:115],v2 offset:5152
v_mfma_f32_16x16x32_bf16 v[192:195],v[68:71],a[144:147],v[192:195]
ds_read_b64_tr_b16 v[118:119],v2 offset:15392
ds_read_b64_tr_b16 v[124:125],v2 offset:30752
v_mfma_f32_16x16x32_bf16 v[192:195],v[72:75],a[160:163],v[192:195]
buffer_load_dwordx4 v7,s[48:51],s83 offen lds
v_mfma_f32_16x16x32_bf16 v[192:195],v[76:79],a[176:179],v[192:195]
s_add_u32 m0,s86,87040
s_add_u32 s83,s64,2
ds_read_b64_tr_b16 v[126:127],v2 offset:35872
v_mfma_f32_16x16x32_bf16 v[200:203],v[76:79],a[180:183],v[200:203]
s_and_b32 s83,s83,3
s_lshl_b32 s83,s83,1
s_or_b32 s83,s83,1
v_mfma_f32_16x16x32_bf16 v[200:203],v[72:75],a[164:167],v[200:203]
s_mul_i32 s83,s83,163840
s_add_u32 s83,s83,s85
v_mfma_f32_16x16x32_bf16 v[200:203],v[68:71],a[148:151],v[200:203]
v_mfma_f32_16x16x32_bf16 v[200:203],v[64:67],a[132:135],v[200:203]
v_mfma_f32_16x16x32_bf16 v[208:211],v[64:67],a[136:139],v[208:211]
buffer_load_dwordx4 v24,s[48:51],s84 offen lds
v_mfma_f32_16x16x32_bf16 v[208:211],v[68:71],a[152:155],v[208:211]
v_mfma_f32_16x16x32_bf16 v[208:211],v[72:75],a[168:171],v[208:211]
v_mfma_f32_16x16x32_bf16 v[208:211],v[76:79],a[184:187],v[208:211]
v_mfma_f32_16x16x32_bf16 v[216:219],v[76:79],a[188:191],v[216:219]
ds_read_b64_tr_b16 v[76:77],v1 offset:30784
ds_read_b64_tr_b16 v[78:79],v1 offset:35904
v_mfma_f32_16x16x32_bf16 v[216:219],v[72:75],a[172:175],v[216:219]
ds_read_b64_tr_b16 v[72:73],v1 offset:20544
v_mfma_f32_16x16x32_bf16 v[216:219],v[68:71],a[156:159],v[216:219]
buffer_load_dwordx2 v[4:5],v3,s[44:47],s91 offen offset:1280 sc1
v_mfma_f32_16x16x32_bf16 v[216:219],v[64:67],a[140:143],v[216:219]
s_waitcnt lgkmcnt(3)
ds_read_b64_tr_b16 v[64:65],v1 offset:64
ds_read_b64_tr_b16 v[66:67],v1 offset:5184
v_mfma_f32_16x16x32_bf16 v[224:227],v[80:83],a[128:131],v[224:227]
ds_read_b64_tr_b16 v[68:69],v1 offset:10304
v_mfma_f32_16x16x32_bf16 v[224:227],v[84:87],a[144:147],v[224:227]
ds_read_b64_tr_b16 v[70:71],v1 offset:15424
v_mfma_f32_16x16x32_bf16 v[224:227],v[88:91],a[160:163],v[224:227]
ds_read_b64_tr_b16 v[74:75],v1 offset:25664
v_mfma_f32_16x16x32_bf16 v[224:227],v[92:95],a[176:179],v[224:227]
v_mfma_f32_16x16x32_bf16 v[232:235],v[92:95],a[180:183],v[232:235]
buffer_load_dwordx4 v25,s[48:51],s84 offen offset:1024 lds
v_mfma_f32_16x16x32_bf16 v[232:235],v[88:91],a[164:167],v[232:235]
v_mfma_f32_16x16x32_bf16 v[232:235],v[84:87],a[148:151],v[232:235]
v_mfma_f32_16x16x32_bf16 v[232:235],v[80:83],a[132:135],v[232:235]
v_mfma_f32_16x16x32_bf16 v[240:243],v[80:83],a[136:139],v[240:243]
v_mfma_f32_16x16x32_bf16 v[240:243],v[84:87],a[152:155],v[240:243]
v_mfma_f32_16x16x32_bf16 v[240:243],v[88:91],a[168:171],v[240:243]
buffer_load_dwordx4 v26,s[48:51],s84 offen offset:2048 lds
v_mfma_f32_16x16x32_bf16 v[240:243],v[92:95],a[184:187],v[240:243]
v_mfma_f32_16x16x32_bf16 v[248:251],v[92:95],a[188:191],v[248:251]
ds_read_b64_tr_b16 v[92:93],v2 offset:30816
ds_read_b64_tr_b16 v[94:95],v2 offset:35936
v_mfma_f32_16x16x32_bf16 v[248:251],v[88:91],a[172:175],v[248:251]
ds_read_b64_tr_b16 v[88:89],v2 offset:20576
v_mfma_f32_16x16x32_bf16 v[248:251],v[84:87],a[156:159],v[248:251]
ds_read_b64_tr_b16 v[84:85],v2 offset:10336
v_mfma_f32_16x16x32_bf16 v[248:251],v[80:83],a[140:143],v[248:251]
s_waitcnt vmcnt(25)
ds_read_b64_tr_b16 v[80:81],v2 offset:96
v_mfma_f32_16x16x32_bf16 a[192:195],v[96:99],a[68:71],a[192:195]
buffer_load_dwordx4 v27,s[48:51],s84 offen offset:3072 lds
v_mfma_f32_16x16x32_bf16 a[192:195],v[100:103],a[76:79],a[192:195]
s_add_u32 m0,s86,91136
ds_read_b64_tr_b16 v[82:83],v2 offset:5216
ds_read_b64_tr_b16 v[86:87],v2 offset:15456
v_mfma_f32_16x16x32_bf16 a[192:195],v[104:107],a[84:87],a[192:195]
ds_read_b64_tr_b16 v[90:91],v2 offset:25696
v_mfma_f32_16x16x32_bf16 a[192:195],v[108:111],a[92:95],a[192:195]
v_mfma_f32_16x16x32_bf16 a[196:199],v[108:111],a[124:127],a[196:199]
v_mfma_f32_16x16x32_bf16 a[196:199],v[104:107],a[116:119],a[196:199]
v_mfma_f32_16x16x32_bf16 a[196:199],v[100:103],a[108:111],a[196:199]
buffer_load_dwordx4 v7,s[48:51],s84 offen lds
v_mfma_f32_16x16x32_bf16 a[196:199],v[96:99],a[100:103],a[196:199]
s_add_u32 m0,s86,40960
s_add_u32 s84,s83,20480
v_mfma_f32_16x16x32_bf16 a[200:203],v[96:99],a[64:67],a[200:203]
s_waitcnt vmcnt(25)
v_mfma_f32_16x16x32_bf16 a[200:203],v[100:103],a[72:75],a[200:203]
s_waitcnt vmcnt(22)
v_mfma_f32_16x16x32_bf16 a[200:203],v[104:107],a[80:83],a[200:203]
s_waitcnt vmcnt(18)
v_mfma_f32_16x16x32_bf16 a[200:203],v[108:111],a[88:91],a[200:203]
buffer_load_dwordx4 v[32:35],v6,s[28:31],s87 offen sc1
v_mfma_f32_16x16x32_bf16 a[204:207],v[108:111],a[120:123],a[204:207]
s_waitcnt vmcnt(17)
ds_read_b64_tr_b16 v[108:109],v1 offset:30848
ds_read_b64_tr_b16 v[110:111],v1 offset:35968
v_mfma_f32_16x16x32_bf16 a[204:207],v[104:107],a[112:115],a[204:207]
s_waitcnt vmcnt(16)
ds_read_b64_tr_b16 v[104:105],v1 offset:20608
s_nop 0
L_pd15_%=:
s_cbranch_execz L_trt1_%=
v_mfma_f32_16x16x32_bf16 a[204:207],v[100:103],a[104:107],a[204:207]
s_waitcnt vmcnt(14)
ds_read_b64_tr_b16 v[100:101],v1 offset:10368
v_mfma_f32_16x16x32_bf16 a[204:207],v[96:99],a[96:99],a[204:207]
ds_read_b64_tr_b16 v[96:97],v1 offset:128
v_mfma_f32_16x16x32_bf16 a[208:211],v[112:115],a[68:71],a[208:211]
buffer_load_dwordx4 v[132:135],v6,s[28:31],s87 offen offset:1024 sc1
v_mfma_f32_16x16x32_bf16 a[208:211],v[116:119],a[76:79],a[208:211]
ds_read_b64_tr_b16 v[98:99],v1 offset:5248
ds_read_b64_tr_b16 v[102:103],v1 offset:15488
v_mfma_f32_16x16x32_bf16 a[208:211],v[120:123],a[84:87],a[208:211]
ds_read_b64_tr_b16 v[106:107],v1 offset:25728
v_mfma_f32_16x16x32_bf16 a[208:211],v[124:127],a[92:95],a[208:211]
v_mfma_f32_16x16x32_bf16 a[212:215],v[124:127],a[124:127],a[212:215]
v_mfma_f32_16x16x32_bf16 a[212:215],v[120:123],a[116:119],a[212:215]
buffer_load_dwordx4 v[196:199],v6,s[28:31],s87 offen offset:2048 sc1
v_mfma_f32_16x16x32_bf16 a[212:215],v[116:119],a[108:111],a[212:215]
v_mfma_f32_16x16x32_bf16 a[212:215],v[112:115],a[100:103],a[212:215]
v_mfma_f32_16x16x32_bf16 a[216:219],v[112:115],a[64:67],a[216:219]
v_mfma_f32_16x16x32_bf16 a[216:219],v[116:119],a[72:75],a[216:219]
v_mfma_f32_16x16x32_bf16 a[216:219],v[120:123],a[80:83],a[216:219]
buffer_load_dwordx4 v[36:39],v6,s[28:31],s88 offen sc1
v_mfma_f32_16x16x32_bf16 a[216:219],v[124:127],a[88:91],a[216:219]
v_mfma_f32_16x16x32_bf16 a[220:223],v[124:127],a[120:123],a[220:223]
ds_read_b64_tr_b16 v[124:125],v2 offset:30880
ds_read_b64_tr_b16 v[126:127],v2 offset:36000
v_mfma_f32_16x16x32_bf16 a[220:223],v[120:123],a[112:115],a[220:223]
ds_read_b64_tr_b16 v[120:121],v2 offset:20640
v_mfma_f32_16x16x32_bf16 a[220:223],v[116:119],a[104:107],a[220:223]
s_waitcnt lgkmcnt(3)
ds_read_b64_tr_b16 v[116:117],v2 offset:10400
v_mfma_f32_16x16x32_bf16 a[220:223],v[112:115],a[96:99],a[220:223]
buffer_load_dwordx4 v[140:143],v28,s[28:31],s87 offen offset:3072 sc1
v_mfma_f32_16x16x32_bf16 a[224:227],v[64:67],a[68:71],a[224:227]
s_add_u32 s87,s64,1
s_and_b32 s87,s87,3
s_lshl_b32 s87,s87,1
v_mfma_f32_16x16x32_bf16 a[224:227],v[68:71],a[76:79],a[224:227]
s_or_b32 s87,s87,1
s_lshl_b32 s87,s87,16
ds_read_b64_tr_b16 v[112:113],v2 offset:160
v_mfma_f32_16x16x32_bf16 a[224:227],v[72:75],a[84:87],a[224:227]
ds_read_b64_tr_b16 v[114:115],v2 offset:5280
ds_read_b64_tr_b16 v[118:119],v2 offset:15520
v_mfma_f32_16x16x32_bf16 a[224:227],v[76:79],a[92:95],a[224:227]
ds_read_b64_tr_b16 v[122:123],v2 offset:25760
v_mfma_f32_16x16x32_bf16 a[228:231],v[76:79],a[124:127],a[228:231]
buffer_load_dwordx4 v[212:215],v6,s[28:31],s88 offen offset:2048 sc1
v_mfma_f32_16x16x32_bf16 a[228:231],v[72:75],a[116:119],a[228:231]
v_mfma_f32_16x16x32_bf16 a[228:231],v[68:71],a[108:111],a[228:231]
v_mfma_f32_16x16x32_bf16 a[228:231],v[64:67],a[100:103],a[228:231]
v_mfma_f32_16x16x32_bf16 a[232:235],v[64:67],a[64:67],a[232:235]
v_mfma_f32_16x16x32_bf16 a[232:235],v[68:71],a[72:75],a[232:235]
buffer_load_dwordx4 v[148:151],v6,s[28:31],s88 offen offset:1024 sc1
v_mfma_f32_16x16x32_bf16 a[232:235],v[72:75],a[80:83],a[232:235]
s_waitcnt vmcnt(11)
v_cmp_ne_u32 s[56:57],s52,v4
v_cmp_ne_u32 s[58:59],s53,v5
v_mfma_f32_16x16x32_bf16 a[232:235],v[76:79],a[88:91],a[232:235]
s_or_b64 s[56:57],s[56:57],s[58:59]
s_cbranch_scc1 L_fr2_%=
L_fo2_%=:
v_mov_b32 v3,0
v_mfma_f32_16x16x32_bf16 a[236:239],v[76:79],a[120:123],a[236:239]
s_add_u32 s91,s64,3
ds_read_b64_tr_b16 v[76:77],v1 offset:30912
ds_read_b64_tr_b16 v[78:79],v1 offset:36032
v_mfma_f32_16x16x32_bf16 a[236:239],v[72:75],a[112:115],a[236:239]
s_and_b32 s91,s91,3
s_add_u32 s91,s91,s69
ds_read_b64_tr_b16 v[72:73],v1 offset:20672
)A"
R"A(v_mfma_f32_16x16x32_bf16 a[236:239],v[68:71],a[104:107],a[236:239]
buffer_load_dwordx4 v[40:43],v6,s[28:31],s89 offen sc1
v_mfma_f32_16x16x32_bf16 a[236:239],v[64:67],a[96:99],a[236:239]
s_add_u32 s91,s91,8
ds_read_b64_tr_b16 v[64:65],v1 offset:192
ds_read_b64_tr_b16 v[66:67],v1 offset:5312
v_mfma_f32_16x16x32_bf16 a[240:243],v[80:83],a[68:71],a[240:243]
s_lshl_b32 s91,s91,6
s_and_b32 s69,s63,s60
ds_read_b64_tr_b16 v[68:69],v1 offset:10432
v_mfma_f32_16x16x32_bf16 a[240:243],v[84:87],a[76:79],a[240:243]
s_cmp_eq_u32 s69,3
s_cselect_b32 s69,0x40000000,0
ds_read_b64_tr_b16 v[70:71],v1 offset:15552
v_mfma_f32_16x16x32_bf16 a[240:243],v[88:91],a[84:87],a[240:243]
s_add_u32 s68,s68,s69
ds_read_b64_tr_b16 v[74:75],v1 offset:25792
v_mfma_f32_16x16x32_bf16 a[240:243],v[92:95],a[92:95],a[240:243]
buffer_load_dwordx4 a[128:131],v6,s[28:31],s87 offen sc1
v_mfma_f32_16x16x32_bf16 a[244:247],v[92:95],a[124:127],a[244:247]
v_mfma_f32_16x16x32_bf16 a[244:247],v[88:91],a[116:119],a[244:247]
v_mfma_f32_16x16x32_bf16 a[244:247],v[84:87],a[108:111],a[244:247]
v_mfma_f32_16x16x32_bf16 a[244:247],v[80:83],a[100:103],a[244:247]
v_mfma_f32_16x16x32_bf16 a[248:251],v[80:83],a[64:67],a[248:251]
buffer_load_dwordx4 v[156:159],v28,s[28:31],s88 offen offset:3072 sc1
v_mfma_f32_16x16x32_bf16 a[248:251],v[84:87],a[72:75],a[248:251]
s_add_u32 s88,s87,4096
v_mfma_f32_16x16x32_bf16 a[248:251],v[88:91],a[80:83],a[248:251]
v_mfma_f32_16x16x32_bf16 a[248:251],v[92:95],a[88:91],a[248:251]
v_mfma_f32_16x16x32_bf16 a[252:255],v[92:95],a[120:123],a[252:255]
ds_read_b64_tr_b16 v[92:93],v2 offset:30944
ds_read_b64_tr_b16 v[94:95],v2 offset:36064
v_mfma_f32_16x16x32_bf16 a[252:255],v[88:91],a[112:115],a[252:255]
buffer_load_dwordx4 v[228:231],v6,s[28:31],s89 offen offset:2048 sc1
v_mfma_f32_16x16x32_bf16 a[252:255],v[84:87],a[104:107],a[252:255]
ds_read_b64_tr_b16 v[84:85],v2 offset:10464
ds_read_b64_tr_b16 v[86:87],v2 offset:15584
v_mfma_f32_16x16x32_bf16 a[252:255],v[80:83],a[96:99],a[252:255]
ds_read_b64_tr_b16 v[80:81],v2 offset:224
v_mfma_f32_16x16x32_bf16 a[0:3],v[96:99],a[68:71],a[0:3]
ds_read_b64_tr_b16 v[82:83],v2 offset:5344
v_mfma_f32_16x16x32_bf16 a[0:3],v[100:103],a[76:79],a[0:3]
ds_read_b64_tr_b16 v[88:89],v2 offset:20704
v_mfma_f32_16x16x32_bf16 a[0:3],v[104:107],a[84:87],a[0:3]
buffer_load_dwordx4 v[164:167],v6,s[28:31],s89 offen offset:1024 sc1
v_mfma_f32_16x16x32_bf16 a[0:3],v[108:111],a[92:95],a[0:3]
ds_read_b64_tr_b16 v[90:91],v2 offset:25824
v_mfma_f32_16x16x32_bf16 a[4:7],v[108:111],a[124:127],a[4:7]
v_mfma_f32_16x16x32_bf16 a[4:7],v[104:107],a[116:119],a[4:7]
v_mfma_f32_16x16x32_bf16 a[4:7],v[100:103],a[108:111],a[4:7]
v_mfma_f32_16x16x32_bf16 a[4:7],v[96:99],a[100:103],a[4:7]
buffer_load_dwordx4 v[44:47],v6,s[28:31],s90 offen sc1
v_mfma_f32_16x16x32_bf16 a[8:11],v[96:99],a[64:67],a[8:11]
v_mfma_f32_16x16x32_bf16 a[8:11],v[100:103],a[72:75],a[8:11]
v_mfma_f32_16x16x32_bf16 a[8:11],v[104:107],a[80:83],a[8:11]
v_mfma_f32_16x16x32_bf16 a[8:11],v[108:111],a[88:91],a[8:11]
v_mfma_f32_16x16x32_bf16 a[12:15],v[108:111],a[120:123],a[12:15]
buffer_load_dwordx4 a[144:147],v6,s[28:31],s88 offen sc1
v_mfma_f32_16x16x32_bf16 a[12:15],v[104:107],a[112:115],a[12:15]
ds_read_b64_tr_b16 v[104:105],v1 offset:20736
ds_read_b64_tr_b16 v[106:107],v1 offset:25856
v_mfma_f32_16x16x32_bf16 a[12:15],v[100:103],a[104:107],a[12:15]
ds_read_b64_tr_b16 v[100:101],v1 offset:10496
v_mfma_f32_16x16x32_bf16 a[12:15],v[96:99],a[96:99],a[12:15]
s_waitcnt lgkmcnt(3)
ds_read_b64_tr_b16 v[96:97],v1 offset:256
v_mfma_f32_16x16x32_bf16 a[16:19],v[112:115],a[68:71],a[16:19]
ds_read_b64_tr_b16 v[98:99],v1 offset:5376
v_mfma_f32_16x16x32_bf16 a[16:19],v[116:119],a[76:79],a[16:19]
buffer_load_dwordx4 v[172:175],v28,s[28:31],s89 offen offset:3072 sc1
v_mfma_f32_16x16x32_bf16 a[16:19],v[120:123],a[84:87],a[16:19]
s_add_u32 s89,s87,8192
ds_read_b64_tr_b16 v[102:103],v1 offset:15616
ds_read_b64_tr_b16 v[108:109],v1 offset:30976
v_mfma_f32_16x16x32_bf16 a[16:19],v[124:127],a[92:95],a[16:19]
ds_read_b64_tr_b16 v[110:111],v1 offset:36096
v_add_u32 v1,0x1e000,v1
v_mfma_f32_16x16x32_bf16 a[20:23],v[124:127],a[124:127],a[20:23]
v_mfma_f32_16x16x32_bf16 a[20:23],v[120:123],a[116:119],a[20:23]
v_mfma_f32_16x16x32_bf16 a[20:23],v[116:119],a[108:111],a[20:23]
buffer_load_dwordx4 v[180:183],v6,s[28:31],s90 offen offset:1024 sc1
v_mfma_f32_16x16x32_bf16 a[20:23],v[112:115],a[100:103],a[20:23]
v_mfma_f32_16x16x32_bf16 a[24:27],v[112:115],a[64:67],a[24:27]
v_mfma_f32_16x16x32_bf16 a[24:27],v[116:119],a[72:75],a[24:27]
v_mfma_f32_16x16x32_bf16 a[24:27],v[120:123],a[80:83],a[24:27]
v_mfma_f32_16x16x32_bf16 a[24:27],v[124:127],a[88:91],a[24:27]
buffer_load_dwordx4 a[160:163],v6,s[28:31],s89 offen sc1
v_mfma_f32_16x16x32_bf16 a[28:31],v[124:127],a[120:123],a[28:31]
ds_read_b64_tr_b16 v[124:125],v2 offset:31008
ds_read_b64_tr_b16 v[126:127],v2 offset:36128
v_mfma_f32_16x16x32_bf16 a[28:31],v[120:123],a[112:115],a[28:31]
ds_read_b64_tr_b16 v[120:121],v2 offset:20768
v_mfma_f32_16x16x32_bf16 a[28:31],v[116:119],a[104:107],a[28:31]
ds_read_b64_tr_b16 v[116:117],v2 offset:10528
v_mfma_f32_16x16x32_bf16 a[28:31],v[112:115],a[96:99],a[28:31]
ds_read_b64_tr_b16 v[112:113],v2 offset:288
v_mfma_f32_16x16x32_bf16 v[128:131],v[64:67],a[68:71],v[128:131]
buffer_load_dwordx4 v[244:247],v6,s[28:31],s90 offen offset:2048 sc1
v_mfma_f32_16x16x32_bf16 v[128:131],v[68:71],a[76:79],v[128:131]
ds_read_b64_tr_b16 v[114:115],v2 offset:5408
ds_read_b64_tr_b16 v[118:119],v2 offset:15648
v_mfma_f32_16x16x32_bf16 v[128:131],v[72:75],a[84:87],v[128:131]
ds_read_b64_tr_b16 v[122:123],v2 offset:25888
v_add_u32 v2,0x1e000,v2
v_mfma_f32_16x16x32_bf16 v[128:131],v[76:79],a[92:95],v[128:131]
v_mfma_f32_16x16x32_bf16 v[136:139],v[76:79],a[124:127],v[136:139]
v_mfma_f32_16x16x32_bf16 v[136:139],v[72:75],a[116:119],v[136:139]
buffer_load_dwordx4 v[188:191],v28,s[28:31],s90 offen offset:3072 sc1
v_mfma_f32_16x16x32_bf16 v[136:139],v[68:71],a[108:111],v[136:139]
s_add_u32 s90,s87,12288
v_mfma_f32_16x16x32_bf16 v[136:139],v[64:67],a[100:103],v[136:139]
v_mfma_f32_16x16x32_bf16 v[144:147],v[64:67],a[64:67],v[144:147]
v_mfma_f32_16x16x32_bf16 v[144:147],v[68:71],a[72:75],v[144:147]
v_mfma_f32_16x16x32_bf16 v[144:147],v[72:75],a[80:83],v[144:147]
buffer_load_dwordx4 a[176:179],v6,s[28:31],s90 offen sc1
v_mfma_f32_16x16x32_bf16 v[144:147],v[76:79],a[88:91],v[144:147]
v_mfma_f32_16x16x32_bf16 v[152:155],v[76:79],a[120:123],v[152:155]
v_mfma_f32_16x16x32_bf16 v[152:155],v[72:75],a[112:115],v[152:155]
v_mfma_f32_16x16x32_bf16 v[152:155],v[68:71],a[104:107],v[152:155]
v_mfma_f32_16x16x32_bf16 v[152:155],v[64:67],a[96:99],v[152:155]
buffer_load_dwordx4 a[180:183],v6,s[28:31],s90 offen offset:1024 sc1
s_waitcnt vmcnt(21) lgkmcnt(0)
s_barrier
v_mfma_f32_16x16x32_bf16 v[160:163],v[80:83],a[68:71],v[160:163]
ds_read_b64_tr_b16 v[64:65],v1
ds_read_b64_tr_b16 v[66:67],v1 offset:5120
v_mfma_f32_16x16x32_bf16 v[160:163],v[84:87],a[76:79],v[160:163]
ds_read_b64_tr_b16 v[68:69],v1 offset:10240
v_mfma_f32_16x16x32_bf16 v[160:163],v[88:91],a[84:87],v[160:163]
ds_read_b64_tr_b16 v[70:71],v1 offset:15360
v_mfma_f32_16x16x32_bf16 v[160:163],v[92:95],a[92:95],v[160:163]
buffer_load_dwordx4 a[164:167],v6,s[28:31],s89 offen offset:1024 sc1
v_mfma_f32_16x16x32_bf16 v[168:171],v[92:95],a[124:127],v[168:171]
ds_read_b64_tr_b16 v[72:73],v1 offset:20480
ds_read_b64_tr_b16 v[74:75],v1 offset:25600
v_mfma_f32_16x16x32_bf16 v[168:171],v[88:91],a[116:119],v[168:171]
ds_read_b64_tr_b16 v[76:77],v1 offset:30720
v_mfma_f32_16x16x32_bf16 v[168:171],v[84:87],a[108:111],v[168:171]
ds_read_b64_tr_b16 v[78:79],v1 offset:35840
v_mfma_f32_16x16x32_bf16 v[168:171],v[80:83],a[100:103],v[168:171]
v_mfma_f32_16x16x32_bf16 v[176:179],v[80:83],a[64:67],v[176:179]
v_mfma_f32_16x16x32_bf16 v[176:179],v[84:87],a[72:75],v[176:179]
buffer_load_dwordx4 v24,s[48:51],s83 offen lds
v_mfma_f32_16x16x32_bf16 v[176:179],v[88:91],a[80:83],v[176:179]
v_mfma_f32_16x16x32_bf16 v[176:179],v[92:95],a[88:91],v[176:179]
v_mfma_f32_16x16x32_bf16 v[184:187],v[92:95],a[120:123],v[184:187]
ds_read_b64_tr_b16 v[92:93],v2 offset:30752
ds_read_b64_tr_b16 v[94:95],v2 offset:35872
v_mfma_f32_16x16x32_bf16 v[184:187],v[88:91],a[112:115],v[184:187]
ds_read_b64_tr_b16 v[88:89],v2 offset:20512
v_mfma_f32_16x16x32_bf16 v[184:187],v[84:87],a[104:107],v[184:187]
buffer_load_dwordx4 a[148:151],v6,s[28:31],s88 offen offset:1024 sc1
v_mfma_f32_16x16x32_bf16 v[184:187],v[80:83],a[96:99],v[184:187]
ds_read_b64_tr_b16 v[80:81],v2 offset:32
ds_read_b64_tr_b16 v[82:83],v2 offset:5152
v_mfma_f32_16x16x32_bf16 v[192:195],v[96:99],a[68:71],v[192:195]
ds_read_b64_tr_b16 v[84:85],v2 offset:10272
v_mfma_f32_16x16x32_bf16 v[192:195],v[100:103],a[76:79],v[192:195]
ds_read_b64_tr_b16 v[86:87],v2 offset:15392
v_mfma_f32_16x16x32_bf16 v[192:195],v[104:107],a[84:87],v[192:195]
ds_read_b64_tr_b16 v[90:91],v2 offset:25632
v_mfma_f32_16x16x32_bf16 v[192:195],v[108:111],a[92:95],v[192:195]
v_mfma_f32_16x16x32_bf16 v[200:203],v[108:111],a[124:127],v[200:203]
buffer_load_dwordx4 v25,s[48:51],s83 offen offset:1024 lds
v_mfma_f32_16x16x32_bf16 v[200:203],v[104:107],a[116:119],v[200:203]
v_mfma_f32_16x16x32_bf16 v[200:203],v[100:103],a[108:111],v[200:203]
v_mfma_f32_16x16x32_bf16 v[200:203],v[96:99],a[100:103],v[200:203]
v_mfma_f32_16x16x32_bf16 v[208:211],v[96:99],a[64:67],v[208:211]
v_mfma_f32_16x16x32_bf16 v[208:211],v[100:103],a[72:75],v[208:211]
buffer_load_dwordx4 a[132:135],v6,s[28:31],s87 offen offset:1024 sc1
v_mfma_f32_16x16x32_bf16 v[208:211],v[104:107],a[80:83],v[208:211]
v_mfma_f32_16x16x32_bf16 v[208:211],v[108:111],a[88:91],v[208:211]
v_mfma_f32_16x16x32_bf16 v[216:219],v[108:111],a[120:123],v[216:219]
ds_read_b64_tr_b16 v[108:109],v1 offset:30784
ds_read_b64_tr_b16 v[110:111],v1 offset:35904
v_mfma_f32_16x16x32_bf16 v[216:219],v[104:107],a[112:115],v[216:219]
ds_read_b64_tr_b16 v[104:105],v1 offset:20544
v_mfma_f32_16x16x32_bf16 v[216:219],v[100:103],a[104:107],v[216:219]
ds_read_b64_tr_b16 v[100:101],v1 offset:10304
v_mfma_f32_16x16x32_bf16 v[216:219],v[96:99],a[96:99],v[216:219]
buffer_load_dwordx4 v26,s[48:51],s83 offen offset:2048 lds
v_mfma_f32_16x16x32_bf16 v[224:227],v[112:115],a[68:71],v[224:227]
ds_read_b64_tr_b16 v[96:97],v1 offset:64
ds_read_b64_tr_b16 v[98:99],v1 offset:5184
v_mfma_f32_16x16x32_bf16 v[224:227],v[116:119],a[76:79],v[224:227]
ds_read_b64_tr_b16 v[102:103],v1 offset:15424
v_mfma_f32_16x16x32_bf16 v[224:227],v[120:123],a[84:87],v[224:227]
ds_read_b64_tr_b16 v[106:107],v1 offset:25664
v_mfma_f32_16x16x32_bf16 v[224:227],v[124:127],a[92:95],v[224:227]
v_mfma_f32_16x16x32_bf16 v[232:235],v[124:127],a[124:127],v[232:235]
v_mfma_f32_16x16x32_bf16 v[232:235],v[120:123],a[116:119],v[232:235]
buffer_load_dwordx4 a[136:139],v6,s[28:31],s87 offen offset:2048 sc1
v_mfma_f32_16x16x32_bf16 v[232:235],v[116:119],a[108:111],v[232:235]
v_mfma_f32_16x16x32_bf16 v[232:235],v[112:115],a[100:103],v[232:235]
v_mfma_f32_16x16x32_bf16 v[240:243],v[112:115],a[64:67],v[240:243]
v_mfma_f32_16x16x32_bf16 v[240:243],v[116:119],a[72:75],v[240:243]
v_mfma_f32_16x16x32_bf16 v[240:243],v[120:123],a[80:83],v[240:243]
v_mfma_f32_16x16x32_bf16 v[240:243],v[124:127],a[88:91],v[240:243]
buffer_load_dwordx4 v27,s[48:51],s83 offen offset:3072 lds
v_mfma_f32_16x16x32_bf16 v[248:251],v[124:127],a[120:123],v[248:251]
s_add_u32 m0,s86,45056
ds_read_b64_tr_b16 v[124:125],v2 offset:30816
ds_read_b64_tr_b16 v[126:127],v2 offset:35936
v_mfma_f32_16x16x32_bf16 v[248:251],v[120:123],a[112:115],v[248:251]
ds_read_b64_tr_b16 v[120:121],v2 offset:20576
v_mfma_f32_16x16x32_bf16 v[248:251],v[116:119],a[104:107],v[248:251]
ds_read_b64_tr_b16 v[116:117],v2 offset:10336
v_mfma_f32_16x16x32_bf16 v[248:251],v[112:115],a[96:99],v[248:251]
s_waitcnt vmcnt(21) lgkmcnt(4)
ds_read_b64_tr_b16 v[112:113],v2 offset:96
v_mfma_f32_16x16x32_bf16 a[192:195],v[64:67],v[32:35],a[192:195]
s_waitcnt vmcnt(13)
ds_read_b64_tr_b16 v[114:115],v2 offset:5216
ds_read_b64_tr_b16 v[118:119],v2 offset:15456
v_mfma_f32_16x16x32_bf16 a[192:195],v[68:71],v[36:39],a[192:195]
)A"
R"A(buffer_load_dwordx4 v7,s[48:51],s83 offen lds
v_mfma_f32_16x16x32_bf16 a[192:195],v[72:75],v[40:43],a[192:195]
s_add_u32 m0,s86,46080
s_add_u32 s83,s64,3
ds_read_b64_tr_b16 v[122:123],v2 offset:25696
v_mfma_f32_16x16x32_bf16 a[192:195],v[76:79],v[44:47],a[192:195]
s_and_b32 s83,s83,3
s_lshl_b32 s83,s83,1
s_or_b32 s83,s83,17
v_mfma_f32_16x16x32_bf16 a[196:199],v[76:79],v[180:183],a[196:199]
s_mul_i32 s83,s83,163840
s_add_u32 s83,s83,s85
v_mfma_f32_16x16x32_bf16 a[196:199],v[72:75],v[164:167],a[196:199]
v_mfma_f32_16x16x32_bf16 a[196:199],v[68:71],v[148:151],a[196:199]
buffer_load_dwordx4 a[152:155],v6,s[28:31],s88 offen offset:2048 sc1
v_mfma_f32_16x16x32_bf16 a[196:199],v[64:67],v[132:135],a[196:199]
v_mfma_f32_16x16x32_bf16 a[200:203],v[64:67],v[196:199],a[200:203]
v_mfma_f32_16x16x32_bf16 a[200:203],v[68:71],v[212:215],a[200:203]
v_mfma_f32_16x16x32_bf16 a[200:203],v[72:75],v[228:231],a[200:203]
s_waitcnt vmcnt(13)
v_mfma_f32_16x16x32_bf16 a[200:203],v[76:79],v[244:247],a[200:203]
s_waitcnt vmcnt(12)
v_mfma_f32_16x16x32_bf16 a[204:207],v[76:79],v[188:191],a[204:207]
buffer_load_dwordx4 v24,s[48:51],s84 offen lds
v_mfma_f32_16x16x32_bf16 a[204:207],v[72:75],v[172:175],a[204:207]
ds_read_b64_tr_b16 v[72:73],v1 offset:20608
ds_read_b64_tr_b16 v[74:75],v1 offset:25728
v_mfma_f32_16x16x32_bf16 a[204:207],v[68:71],v[156:159],a[204:207]
ds_read_b64_tr_b16 v[68:69],v1 offset:10368
v_mfma_f32_16x16x32_bf16 a[204:207],v[64:67],v[140:143],a[204:207]
ds_read_b64_tr_b16 v[64:65],v1 offset:128
v_mfma_f32_16x16x32_bf16 a[208:211],v[80:83],v[32:35],a[208:211]
ds_read_b64_tr_b16 v[66:67],v1 offset:5248
v_mfma_f32_16x16x32_bf16 a[208:211],v[84:87],v[36:39],a[208:211]
buffer_load_dwordx4 a[168:171],v6,s[28:31],s89 offen offset:2048 sc1
v_mfma_f32_16x16x32_bf16 a[208:211],v[88:91],v[40:43],a[208:211]
ds_read_b64_tr_b16 v[70:71],v1 offset:15488
ds_read_b64_tr_b16 v[76:77],v1 offset:30848
v_mfma_f32_16x16x32_bf16 a[208:211],v[92:95],v[44:47],a[208:211]
ds_read_b64_tr_b16 v[78:79],v1 offset:35968
v_mfma_f32_16x16x32_bf16 a[212:215],v[92:95],v[180:183],a[212:215]
v_mfma_f32_16x16x32_bf16 a[212:215],v[88:91],v[164:167],a[212:215]
v_mfma_f32_16x16x32_bf16 a[212:215],v[84:87],v[148:151],a[212:215]
v_mfma_f32_16x16x32_bf16 a[212:215],v[80:83],v[132:135],a[212:215]
buffer_load_dwordx4 v25,s[48:51],s84 offen offset:1024 lds
v_mfma_f32_16x16x32_bf16 a[216:219],v[80:83],v[196:199],a[216:219]
v_mfma_f32_16x16x32_bf16 a[216:219],v[84:87],v[212:215],a[216:219]
v_mfma_f32_16x16x32_bf16 a[216:219],v[88:91],v[228:231],a[216:219]
v_mfma_f32_16x16x32_bf16 a[216:219],v[92:95],v[244:247],a[216:219]
v_mfma_f32_16x16x32_bf16 a[220:223],v[92:95],v[188:191],a[220:223]
buffer_load_dwordx4 a[184:187],v6,s[28:31],s90 offen offset:2048 sc1
v_mfma_f32_16x16x32_bf16 a[220:223],v[88:91],v[172:175],a[220:223]
ds_read_b64_tr_b16 v[88:89],v2 offset:20640
ds_read_b64_tr_b16 v[90:91],v2 offset:25760
v_mfma_f32_16x16x32_bf16 a[220:223],v[84:87],v[156:159],a[220:223]
ds_read_b64_tr_b16 v[84:85],v2 offset:10400
v_mfma_f32_16x16x32_bf16 a[220:223],v[80:83],v[140:143],a[220:223]
ds_read_b64_tr_b16 v[80:81],v2 offset:160
v_mfma_f32_16x16x32_bf16 a[224:227],v[96:99],v[32:35],a[224:227]
ds_read_b64_tr_b16 v[82:83],v2 offset:5280
v_mfma_f32_16x16x32_bf16 a[224:227],v[100:103],v[36:39],a[224:227]
ds_read_b64_tr_b16 v[86:87],v2 offset:15520
ds_read_b64_tr_b16 v[92:93],v2 offset:30880
v_mfma_f32_16x16x32_bf16 a[224:227],v[104:107],v[40:43],a[224:227]
buffer_load_dwordx4 v26,s[48:51],s84 offen offset:2048 lds
v_mfma_f32_16x16x32_bf16 a[224:227],v[108:111],v[44:47],a[224:227]
ds_read_b64_tr_b16 v[94:95],v2 offset:36000
v_mfma_f32_16x16x32_bf16 a[228:231],v[108:111],v[180:183],a[228:231]
v_mfma_f32_16x16x32_bf16 a[228:231],v[104:107],v[164:167],a[228:231]
v_mfma_f32_16x16x32_bf16 a[228:231],v[100:103],v[148:151],a[228:231]
v_mfma_f32_16x16x32_bf16 a[228:231],v[96:99],v[132:135],a[228:231]
v_mfma_f32_16x16x32_bf16 a[232:235],v[96:99],v[196:199],a[232:235]
buffer_load_dwordx4 v27,s[48:51],s84 offen offset:3072 lds
v_mfma_f32_16x16x32_bf16 a[232:235],v[100:103],v[212:215],a[232:235]
s_add_u32 m0,s86,50176
v_mfma_f32_16x16x32_bf16 a[232:235],v[104:107],v[228:231],a[232:235]
v_mfma_f32_16x16x32_bf16 a[232:235],v[108:111],v[244:247],a[232:235]
v_mfma_f32_16x16x32_bf16 a[236:239],v[108:111],v[188:191],a[236:239]
ds_read_b64_tr_b16 v[108:109],v1 offset:30912
ds_read_b64_tr_b16 v[110:111],v1 offset:36032
v_mfma_f32_16x16x32_bf16 a[236:239],v[104:107],v[172:175],a[236:239]
buffer_load_dwordx4 a[188:191],v28,s[28:31],s90 offen offset:3072 sc1
v_mfma_f32_16x16x32_bf16 a[236:239],v[100:103],v[156:159],a[236:239]
ds_read_b64_tr_b16 v[100:101],v1 offset:10432
ds_read_b64_tr_b16 v[102:103],v1 offset:15552
v_mfma_f32_16x16x32_bf16 a[236:239],v[96:99],v[140:143],a[236:239]
s_waitcnt lgkmcnt(4)
ds_read_b64_tr_b16 v[96:97],v1 offset:192
v_mfma_f32_16x16x32_bf16 a[240:243],v[112:115],v[32:35],a[240:243]
ds_read_b64_tr_b16 v[98:99],v1 offset:5312
v_mfma_f32_16x16x32_bf16 a[240:243],v[116:119],v[36:39],a[240:243]
ds_read_b64_tr_b16 v[104:105],v1 offset:20672
v_mfma_f32_16x16x32_bf16 a[240:243],v[120:123],v[40:43],a[240:243]
ds_read_b64_tr_b16 v[106:107],v1 offset:25792
v_mfma_f32_16x16x32_bf16 a[240:243],v[124:127],v[44:47],a[240:243]
buffer_load_dwordx4 v7,s[48:51],s84 offen lds
v_mfma_f32_16x16x32_bf16 a[244:247],v[124:127],v[180:183],a[244:247]
s_add_u32 m0,s86,0
s_add_u32 s84,s83,20480
v_mfma_f32_16x16x32_bf16 a[244:247],v[120:123],v[164:167],a[244:247]
v_mfma_f32_16x16x32_bf16 a[244:247],v[116:119],v[148:151],a[244:247]
v_mfma_f32_16x16x32_bf16 a[244:247],v[112:115],v[132:135],a[244:247]
v_mfma_f32_16x16x32_bf16 a[248:251],v[112:115],v[196:199],a[248:251]
buffer_load_dwordx4 a[172:175],v28,s[28:31],s89 offen offset:3072 sc1
v_mfma_f32_16x16x32_bf16 a[248:251],v[116:119],v[212:215],a[248:251]
v_mfma_f32_16x16x32_bf16 a[248:251],v[120:123],v[228:231],a[248:251]
v_mfma_f32_16x16x32_bf16 a[248:251],v[124:127],v[244:247],a[248:251]
v_mfma_f32_16x16x32_bf16 a[252:255],v[124:127],v[188:191],a[252:255]
ds_read_b64_tr_b16 v[124:125],v2 offset:30944
ds_read_b64_tr_b16 v[126:127],v2 offset:36064
v_mfma_f32_16x16x32_bf16 a[252:255],v[120:123],v[172:175],a[252:255]
ds_read_b64_tr_b16 v[120:121],v2 offset:20704
v_mfma_f32_16x16x32_bf16 a[252:255],v[116:119],v[156:159],a[252:255]
buffer_load_dwordx4 v24,s[48:51],s83 offen lds
v_mfma_f32_16x16x32_bf16 a[252:255],v[112:115],v[140:143],a[252:255]
ds_read_b64_tr_b16 v[112:113],v2 offset:224
ds_read_b64_tr_b16 v[114:115],v2 offset:5344
v_mfma_f32_16x16x32_bf16 a[0:3],v[64:67],v[32:35],a[0:3]
ds_read_b64_tr_b16 v[116:117],v2 offset:10464
v_mfma_f32_16x16x32_bf16 a[0:3],v[68:71],v[36:39],a[0:3]
ds_read_b64_tr_b16 v[118:119],v2 offset:15584
v_mfma_f32_16x16x32_bf16 a[0:3],v[72:75],v[40:43],a[0:3]
ds_read_b64_tr_b16 v[122:123],v2 offset:25824
v_mfma_f32_16x16x32_bf16 a[0:3],v[76:79],v[44:47],a[0:3]
buffer_load_dwordx4 a[156:159],v28,s[28:31],s88 offen offset:3072 sc1
v_mfma_f32_16x16x32_bf16 a[4:7],v[76:79],v[180:183],a[4:7]
v_mfma_f32_16x16x32_bf16 a[4:7],v[72:75],v[164:167],a[4:7]
v_mfma_f32_16x16x32_bf16 a[4:7],v[68:71],v[148:151],a[4:7]
v_mfma_f32_16x16x32_bf16 a[4:7],v[64:67],v[132:135],a[4:7]
v_mfma_f32_16x16x32_bf16 a[8:11],v[64:67],v[196:199],a[8:11]
v_mfma_f32_16x16x32_bf16 a[8:11],v[68:71],v[212:215],a[8:11]
buffer_load_dwordx4 v25,s[48:51],s83 offen offset:1024 lds
v_mfma_f32_16x16x32_bf16 a[8:11],v[72:75],v[228:231],a[8:11]
v_mfma_f32_16x16x32_bf16 a[8:11],v[76:79],v[244:247],a[8:11]
v_mfma_f32_16x16x32_bf16 a[12:15],v[76:79],v[188:191],a[12:15]
ds_read_b64_tr_b16 v[76:77],v1 offset:30976
ds_read_b64_tr_b16 v[78:79],v1 offset:36096
v_mfma_f32_16x16x32_bf16 a[12:15],v[72:75],v[172:175],a[12:15]
ds_read_b64_tr_b16 v[72:73],v1 offset:20736
v_mfma_f32_16x16x32_bf16 a[12:15],v[68:71],v[156:159],a[12:15]
ds_read_b64_tr_b16 v[68:69],v1 offset:10496
v_mfma_f32_16x16x32_bf16 a[12:15],v[64:67],v[140:143],a[12:15]
buffer_load_dwordx4 v26,s[48:51],s83 offen offset:2048 lds
v_mfma_f32_16x16x32_bf16 a[16:19],v[80:83],v[32:35],a[16:19]
ds_read_b64_tr_b16 v[64:65],v1 offset:256
ds_read_b64_tr_b16 v[66:67],v1 offset:5376
v_mfma_f32_16x16x32_bf16 a[16:19],v[84:87],v[36:39],a[16:19]
ds_read_b64_tr_b16 v[70:71],v1 offset:15616
v_mfma_f32_16x16x32_bf16 a[16:19],v[88:91],v[40:43],a[16:19]
ds_read_b64_tr_b16 v[74:75],v1 offset:25856
v_add_u32 v1,0xffff6000,v1
v_mfma_f32_16x16x32_bf16 a[16:19],v[92:95],v[44:47],a[16:19]
v_mfma_f32_16x16x32_bf16 a[20:23],v[92:95],v[180:183],a[20:23]
buffer_load_dwordx4 a[140:143],v28,s[28:31],s87 offen offset:3072 sc1
v_mfma_f32_16x16x32_bf16 a[20:23],v[88:91],v[164:167],a[20:23]
s_add_u32 s87,s64,2
s_and_b32 s87,s87,3
s_lshl_b32 s87,s87,1
v_mfma_f32_16x16x32_bf16 a[20:23],v[84:87],v[148:151],a[20:23]
s_or_b32 s87,s87,1
s_lshl_b32 s87,s87,16
s_add_u32 s88,s87,4096
v_mfma_f32_16x16x32_bf16 a[20:23],v[80:83],v[132:135],a[20:23]
s_add_u32 s89,s87,8192
s_add_u32 s90,s87,12288
v_mfma_f32_16x16x32_bf16 a[24:27],v[80:83],v[196:199],a[24:27]
v_mfma_f32_16x16x32_bf16 a[24:27],v[84:87],v[212:215],a[24:27]
v_mfma_f32_16x16x32_bf16 a[24:27],v[88:91],v[228:231],a[24:27]
buffer_load_dwordx4 v27,s[48:51],s83 offen offset:3072 lds
v_mfma_f32_16x16x32_bf16 a[24:27],v[92:95],v[244:247],a[24:27]
s_add_u32 m0,s86,4096
v_mfma_f32_16x16x32_bf16 a[28:31],v[92:95],v[188:191],a[28:31]
ds_read_b64_tr_b16 v[92:93],v2 offset:31008
ds_read_b64_tr_b16 v[94:95],v2 offset:36128
v_mfma_f32_16x16x32_bf16 a[28:31],v[88:91],v[172:175],a[28:31]
ds_read_b64_tr_b16 v[88:89],v2 offset:20768
v_mfma_f32_16x16x32_bf16 a[28:31],v[84:87],v[156:159],a[28:31]
ds_read_b64_tr_b16 v[84:85],v2 offset:10528
v_mfma_f32_16x16x32_bf16 a[28:31],v[80:83],v[140:143],a[28:31]
s_waitcnt lgkmcnt(4)
ds_read_b64_tr_b16 v[80:81],v2 offset:288
v_mfma_f32_16x16x32_bf16 v[128:131],v[96:99],v[32:35],v[128:131]
buffer_load_dwordx4 v7,s[48:51],s83 offen lds
v_mfma_f32_16x16x32_bf16 v[128:131],v[100:103],v[36:39],v[128:131]
s_add_u32 m0,s86,5120
ds_read_b64_tr_b16 v[82:83],v2 offset:5408
ds_read_b64_tr_b16 v[86:87],v2 offset:15648
v_mfma_f32_16x16x32_bf16 v[128:131],v[104:107],v[40:43],v[128:131]
s_add_u32 s83,s64,3
ds_read_b64_tr_b16 v[90:91],v2 offset:25888
v_add_u32 v2,0xffff6000,v2
v_mfma_f32_16x16x32_bf16 v[128:131],v[108:111],v[44:47],v[128:131]
s_and_b32 s83,s83,3
s_lshl_b32 s83,s83,1
s_or_b32 s83,s83,1
v_mfma_f32_16x16x32_bf16 v[136:139],v[108:111],v[180:183],v[136:139]
s_mul_i32 s83,s83,163840
s_add_u32 s83,s83,s85
v_mfma_f32_16x16x32_bf16 v[136:139],v[104:107],v[164:167],v[136:139]
v_mfma_f32_16x16x32_bf16 v[136:139],v[100:103],v[148:151],v[136:139]
buffer_load_dwordx4 v24,s[48:51],s84 offen lds
v_mfma_f32_16x16x32_bf16 v[136:139],v[96:99],v[132:135],v[136:139]
v_mfma_f32_16x16x32_bf16 v[144:147],v[96:99],v[196:199],v[144:147]
v_mfma_f32_16x16x32_bf16 v[144:147],v[100:103],v[212:215],v[144:147]
v_mfma_f32_16x16x32_bf16 v[144:147],v[104:107],v[228:231],v[144:147]
v_mfma_f32_16x16x32_bf16 v[144:147],v[108:111],v[244:247],v[144:147]
v_mfma_f32_16x16x32_bf16 v[152:155],v[108:111],v[188:191],v[152:155]
buffer_load_dwordx4 v25,s[48:51],s84 offen offset:1024 lds
v_mfma_f32_16x16x32_bf16 v[152:155],v[104:107],v[172:175],v[152:155]
ds_read_b64_tr_b16 v[104:105],v1 offset:20480
ds_read_b64_tr_b16 v[106:107],v1 offset:25600
v_mfma_f32_16x16x32_bf16 v[152:155],v[100:103],v[156:159],v[152:155]
ds_read_b64_tr_b16 v[100:101],v1 offset:10240
v_mfma_f32_16x16x32_bf16 v[152:155],v[96:99],v[140:143],v[152:155]
ds_read_b64_tr_b16 v[96:97],v1
v_mfma_f32_16x16x32_bf16 v[160:163],v[112:115],v[32:35],v[160:163]
ds_read_b64_tr_b16 v[98:99],v1 offset:5120
v_mfma_f32_16x16x32_bf16 v[160:163],v[116:119],v[36:39],v[160:163]
ds_read_b64_tr_b16 v[102:103],v1 offset:15360
ds_read_b64_tr_b16 v[108:109],v1 offset:30720
v_mfma_f32_16x16x32_bf16 v[160:163],v[120:123],v[40:43],v[160:163]
buffer_load_dwordx4 v26,s[48:51],s84 offen offset:2048 lds
v_mfma_f32_16x16x32_bf16 v[160:163],v[124:127],v[44:47],v[160:163]
ds_read_b64_tr_b16 v[110:111],v1 offset:35840
v_mfma_f32_16x16x32_bf16 v[168:171],v[124:127],v[180:183],v[168:171]
)A"
R"A(v_mfma_f32_16x16x32_bf16 v[168:171],v[120:123],v[164:167],v[168:171]
v_mfma_f32_16x16x32_bf16 v[168:171],v[116:119],v[148:151],v[168:171]
v_mfma_f32_16x16x32_bf16 v[168:171],v[112:115],v[132:135],v[168:171]
v_mfma_f32_16x16x32_bf16 v[176:179],v[112:115],v[196:199],v[176:179]
buffer_load_dwordx4 v27,s[48:51],s84 offen offset:3072 lds
v_mfma_f32_16x16x32_bf16 v[176:179],v[116:119],v[212:215],v[176:179]
s_add_u32 m0,s86,9216
v_mfma_f32_16x16x32_bf16 v[176:179],v[120:123],v[228:231],v[176:179]
v_mfma_f32_16x16x32_bf16 v[176:179],v[124:127],v[244:247],v[176:179]
v_mfma_f32_16x16x32_bf16 v[184:187],v[124:127],v[188:191],v[184:187]
ds_read_b64_tr_b16 v[124:125],v2 offset:30752
ds_read_b64_tr_b16 v[126:127],v2 offset:35872
v_mfma_f32_16x16x32_bf16 v[184:187],v[120:123],v[172:175],v[184:187]
ds_read_b64_tr_b16 v[120:121],v2 offset:20512
v_mfma_f32_16x16x32_bf16 v[184:187],v[116:119],v[156:159],v[184:187]
buffer_load_dwordx4 v7,s[48:51],s84 offen lds
v_mfma_f32_16x16x32_bf16 v[184:187],v[112:115],v[140:143],v[184:187]
s_add_u32 m0,s86,122880
ds_read_b64_tr_b16 v[112:113],v2 offset:32
ds_read_b64_tr_b16 v[114:115],v2 offset:5152
v_mfma_f32_16x16x32_bf16 v[192:195],v[64:67],v[32:35],v[192:195]
s_add_u32 s84,s83,20480
ds_read_b64_tr_b16 v[116:117],v2 offset:10272
v_mfma_f32_16x16x32_bf16 v[192:195],v[68:71],v[36:39],v[192:195]
L_pd16_%=:
s_cbranch_execz L_trtg2F_%=
ds_read_b64_tr_b16 v[118:119],v2 offset:15392
v_mfma_f32_16x16x32_bf16 v[192:195],v[72:75],v[40:43],v[192:195]
ds_read_b64_tr_b16 v[122:123],v2 offset:25632
v_mfma_f32_16x16x32_bf16 v[192:195],v[76:79],v[44:47],v[192:195]
buffer_load_dwordx4 a[68:71],v6,s[28:31],s87 offen sc1
v_mfma_f32_16x16x32_bf16 v[200:203],v[76:79],v[180:183],v[200:203]
v_mfma_f32_16x16x32_bf16 v[200:203],v[72:75],v[164:167],v[200:203]
v_mfma_f32_16x16x32_bf16 v[200:203],v[68:71],v[148:151],v[200:203]
v_mfma_f32_16x16x32_bf16 v[200:203],v[64:67],v[132:135],v[200:203]
v_mfma_f32_16x16x32_bf16 v[208:211],v[64:67],v[196:199],v[208:211]
buffer_load_dwordx4 a[100:103],v6,s[28:31],s87 offen offset:1024 sc1
v_mfma_f32_16x16x32_bf16 v[208:211],v[68:71],v[212:215],v[208:211]
v_mfma_f32_16x16x32_bf16 v[208:211],v[72:75],v[228:231],v[208:211]
v_mfma_f32_16x16x32_bf16 v[208:211],v[76:79],v[244:247],v[208:211]
v_mfma_f32_16x16x32_bf16 v[216:219],v[76:79],v[188:191],v[216:219]
ds_read_b64_tr_b16 v[76:77],v1 offset:30784
ds_read_b64_tr_b16 v[78:79],v1 offset:35904
v_mfma_f32_16x16x32_bf16 v[216:219],v[72:75],v[172:175],v[216:219]
buffer_load_dwordx2 v[4:5],v3,s[44:47],s91 offen offset:1280 sc1
v_mfma_f32_16x16x32_bf16 v[216:219],v[68:71],v[156:159],v[216:219]
ds_read_b64_tr_b16 v[68:69],v1 offset:10304
ds_read_b64_tr_b16 v[70:71],v1 offset:15424
v_mfma_f32_16x16x32_bf16 v[216:219],v[64:67],v[140:143],v[216:219]
s_waitcnt lgkmcnt(4)
ds_read_b64_tr_b16 v[64:65],v1 offset:64
v_mfma_f32_16x16x32_bf16 v[224:227],v[80:83],v[32:35],v[224:227]
ds_read_b64_tr_b16 v[66:67],v1 offset:5184
v_mfma_f32_16x16x32_bf16 v[224:227],v[84:87],v[36:39],v[224:227]
ds_read_b64_tr_b16 v[72:73],v1 offset:20544
v_mfma_f32_16x16x32_bf16 v[224:227],v[88:91],v[40:43],v[224:227]
buffer_load_dwordx4 a[64:67],v6,s[28:31],s87 offen offset:2048 sc1
v_mfma_f32_16x16x32_bf16 v[224:227],v[92:95],v[44:47],v[224:227]
ds_read_b64_tr_b16 v[74:75],v1 offset:25664
v_mfma_f32_16x16x32_bf16 v[232:235],v[92:95],v[180:183],v[232:235]
v_mfma_f32_16x16x32_bf16 v[232:235],v[88:91],v[164:167],v[232:235]
v_mfma_f32_16x16x32_bf16 v[232:235],v[84:87],v[148:151],v[232:235]
v_mfma_f32_16x16x32_bf16 v[232:235],v[80:83],v[132:135],v[232:235]
buffer_load_dwordx4 a[76:79],v6,s[28:31],s88 offen sc1
v_mfma_f32_16x16x32_bf16 v[240:243],v[80:83],v[196:199],v[240:243]
v_mfma_f32_16x16x32_bf16 v[240:243],v[84:87],v[212:215],v[240:243]
v_mfma_f32_16x16x32_bf16 v[240:243],v[88:91],v[228:231],v[240:243]
v_mfma_f32_16x16x32_bf16 v[240:243],v[92:95],v[244:247],v[240:243]
v_mfma_f32_16x16x32_bf16 v[248:251],v[92:95],v[188:191],v[248:251]
buffer_load_dwordx4 a[96:99],v28,s[28:31],s87 offen offset:3072 sc1
v_mfma_f32_16x16x32_bf16 v[248:251],v[88:91],v[172:175],v[248:251]
s_add_u32 s87,s64,3
ds_read_b64_tr_b16 v[88:89],v2 offset:20576
ds_read_b64_tr_b16 v[90:91],v2 offset:25696
v_mfma_f32_16x16x32_bf16 v[248:251],v[84:87],v[156:159],v[248:251]
s_and_b32 s87,s87,3
s_lshl_b32 s87,s87,1
ds_read_b64_tr_b16 v[84:85],v2 offset:10336
v_mfma_f32_16x16x32_bf16 v[248:251],v[80:83],v[140:143],v[248:251]
s_or_b32 s87,s87,17
s_lshl_b32 s87,s87,16
ds_read_b64_tr_b16 v[80:81],v2 offset:96
v_mfma_f32_16x16x32_bf16 a[192:195],v[96:99],a[128:131],a[192:195]
ds_read_b64_tr_b16 v[82:83],v2 offset:5216
v_mfma_f32_16x16x32_bf16 a[192:195],v[100:103],a[144:147],a[192:195]
buffer_load_dwordx4 a[72:75],v6,s[28:31],s88 offen offset:2048 sc1
v_mfma_f32_16x16x32_bf16 a[192:195],v[104:107],a[160:163],a[192:195]
s_waitcnt vmcnt(37)
ds_read_b64_tr_b16 v[86:87],v2 offset:15456
ds_read_b64_tr_b16 v[92:93],v2 offset:30816
v_mfma_f32_16x16x32_bf16 a[192:195],v[108:111],a[176:179],a[192:195]
ds_read_b64_tr_b16 v[94:95],v2 offset:35936
v_mfma_f32_16x16x32_bf16 a[196:199],v[108:111],a[180:183],a[196:199]
v_mfma_f32_16x16x32_bf16 a[196:199],v[104:107],a[164:167],a[196:199]
s_waitcnt vmcnt(33)
v_mfma_f32_16x16x32_bf16 a[196:199],v[100:103],a[148:151],a[196:199]
buffer_load_dwordx4 a[108:111],v6,s[28:31],s88 offen offset:1024 sc1
v_mfma_f32_16x16x32_bf16 a[196:199],v[96:99],a[132:135],a[196:199]
s_waitcnt vmcnt(32)
v_mfma_f32_16x16x32_bf16 a[200:203],v[96:99],a[136:139],a[200:203]
s_waitcnt vmcnt(29)
v_mfma_f32_16x16x32_bf16 a[200:203],v[100:103],a[152:155],a[200:203]
s_waitcnt vmcnt(27)
v_mfma_f32_16x16x32_bf16 a[200:203],v[104:107],a[168:171],a[200:203]
s_waitcnt vmcnt(22)
v_mfma_f32_16x16x32_bf16 a[200:203],v[108:111],a[184:187],a[200:203]
buffer_load_dwordx4 a[84:87],v6,s[28:31],s89 offen sc1
v_mfma_f32_16x16x32_bf16 a[204:207],v[108:111],a[188:191],a[204:207]
s_waitcnt vmcnt(21)
ds_read_b64_tr_b16 v[108:109],v1 offset:30848
ds_read_b64_tr_b16 v[110:111],v1 offset:35968
v_mfma_f32_16x16x32_bf16 a[204:207],v[104:107],a[172:175],a[204:207]
s_waitcnt vmcnt(19)
ds_read_b64_tr_b16 v[104:105],v1 offset:20608
v_mfma_f32_16x16x32_bf16 a[204:207],v[100:103],a[156:159],a[204:207]
s_waitcnt vmcnt(16)
ds_read_b64_tr_b16 v[100:101],v1 offset:10368
v_mfma_f32_16x16x32_bf16 a[204:207],v[96:99],a[140:143],a[204:207]
ds_read_b64_tr_b16 v[96:97],v1 offset:128
v_mfma_f32_16x16x32_bf16 a[208:211],v[112:115],a[128:131],a[208:211]
buffer_load_dwordx4 a[104:107],v28,s[28:31],s88 offen offset:3072 sc1
v_mfma_f32_16x16x32_bf16 a[208:211],v[116:119],a[144:147],a[208:211]
s_add_u32 s88,s87,4096
ds_read_b64_tr_b16 v[98:99],v1 offset:5248
ds_read_b64_tr_b16 v[102:103],v1 offset:15488
v_mfma_f32_16x16x32_bf16 a[208:211],v[120:123],a[160:163],a[208:211]
ds_read_b64_tr_b16 v[106:107],v1 offset:25728
v_mfma_f32_16x16x32_bf16 a[208:211],v[124:127],a[176:179],a[208:211]
v_mfma_f32_16x16x32_bf16 a[212:215],v[124:127],a[180:183],a[212:215]
v_mfma_f32_16x16x32_bf16 a[212:215],v[120:123],a[164:167],a[212:215]
buffer_load_dwordx4 a[80:83],v6,s[28:31],s89 offen offset:2048 sc1
v_mfma_f32_16x16x32_bf16 a[212:215],v[116:119],a[148:151],a[212:215]
v_mfma_f32_16x16x32_bf16 a[212:215],v[112:115],a[132:135],a[212:215]
v_mfma_f32_16x16x32_bf16 a[216:219],v[112:115],a[136:139],a[216:219]
v_mfma_f32_16x16x32_bf16 a[216:219],v[116:119],a[152:155],a[216:219]
v_mfma_f32_16x16x32_bf16 a[216:219],v[120:123],a[168:171],a[216:219]
buffer_load_dwordx4 a[116:119],v6,s[28:31],s89 offen offset:1024 sc1
v_mfma_f32_16x16x32_bf16 a[216:219],v[124:127],a[184:187],a[216:219]
v_mfma_f32_16x16x32_bf16 a[220:223],v[124:127],a[188:191],a[220:223]
ds_read_b64_tr_b16 v[124:125],v2 offset:30880
ds_read_b64_tr_b16 v[126:127],v2 offset:36000
v_mfma_f32_16x16x32_bf16 a[220:223],v[120:123],a[172:175],a[220:223]
ds_read_b64_tr_b16 v[120:121],v2 offset:20640
v_mfma_f32_16x16x32_bf16 a[220:223],v[116:119],a[156:159],a[220:223]
s_waitcnt lgkmcnt(3)
ds_read_b64_tr_b16 v[116:117],v2 offset:10400
v_mfma_f32_16x16x32_bf16 a[220:223],v[112:115],a[140:143],a[220:223]
buffer_load_dwordx4 a[92:95],v6,s[28:31],s90 offen sc1
v_mfma_f32_16x16x32_bf16 a[224:227],v[64:67],a[128:131],a[224:227]
ds_read_b64_tr_b16 v[112:113],v2 offset:160
ds_read_b64_tr_b16 v[114:115],v2 offset:5280
v_mfma_f32_16x16x32_bf16 a[224:227],v[68:71],a[144:147],a[224:227]
ds_read_b64_tr_b16 v[118:119],v2 offset:15520
v_mfma_f32_16x16x32_bf16 a[224:227],v[72:75],a[160:163],a[224:227]
ds_read_b64_tr_b16 v[122:123],v2 offset:25760
v_mfma_f32_16x16x32_bf16 a[224:227],v[76:79],a[176:179],a[224:227]
v_mfma_f32_16x16x32_bf16 a[228:231],v[76:79],a[180:183],a[228:231]
buffer_load_dwordx4 a[112:115],v28,s[28:31],s89 offen offset:3072 sc1
v_mfma_f32_16x16x32_bf16 a[228:231],v[72:75],a[164:167],a[228:231]
s_add_u32 s89,s87,8192
v_mfma_f32_16x16x32_bf16 a[228:231],v[68:71],a[148:151],a[228:231]
v_mfma_f32_16x16x32_bf16 a[228:231],v[64:67],a[132:135],a[228:231]
v_mfma_f32_16x16x32_bf16 a[232:235],v[64:67],a[136:139],a[232:235]
v_mfma_f32_16x16x32_bf16 a[232:235],v[68:71],a[152:155],a[232:235]
buffer_load_dwordx4 a[124:127],v6,s[28:31],s90 offen offset:1024 sc1
v_mfma_f32_16x16x32_bf16 a[232:235],v[72:75],a[168:171],a[232:235]
s_waitcnt vmcnt(12)
v_cmp_ne_u32 s[56:57],s52,v4
v_cmp_ne_u32 s[58:59],s53,v5
v_mfma_f32_16x16x32_bf16 a[232:235],v[76:79],a[184:187],a[232:235]
s_or_b64 s[56:57],s[56:57],s[58:59]
s_cbranch_scc1 L_fr3_%=
L_fo3_%=:
s_lshl_b32 s91,s64,6
v_mfma_f32_16x16x32_bf16 a[236:239],v[76:79],a[188:191],a[236:239]
ds_read_b64_tr_b16 v[76:77],v1 offset:30912
ds_read_b64_tr_b16 v[78:79],v1 offset:36032
v_mfma_f32_16x16x32_bf16 a[236:239],v[72:75],a[172:175],a[236:239]
ds_read_b64_tr_b16 v[72:73],v1 offset:20672
v_bfe_u32 v3,v6,4,2
v_mfma_f32_16x16x32_bf16 a[236:239],v[68:71],a[156:159],a[236:239]
buffer_load_dwordx4 v[32:35],v6,s[28:31],s87 offen sc1
v_mfma_f32_16x16x32_bf16 a[236:239],v[64:67],a[140:143],a[236:239]
ds_read_b64_tr_b16 v[64:65],v1 offset:192
ds_read_b64_tr_b16 v[66:67],v1 offset:5312
v_mfma_f32_16x16x32_bf16 a[240:243],v[80:83],a[128:131],a[240:243]
ds_read_b64_tr_b16 v[68:69],v1 offset:10432
v_min_u32 v3,2,v3
v_mfma_f32_16x16x32_bf16 a[240:243],v[84:87],a[144:147],a[240:243]
ds_read_b64_tr_b16 v[70:71],v1 offset:15552
v_add3_u32 v3,v3,s63,1
v_mfma_f32_16x16x32_bf16 a[240:243],v[88:91],a[160:163],a[240:243]
ds_read_b64_tr_b16 v[74:75],v1 offset:25792
v_and_b32 v3,3,v3
v_mfma_f32_16x16x32_bf16 a[240:243],v[92:95],a[176:179],a[240:243]
buffer_load_dwordx4 a[88:91],v6,s[28:31],s90 offen offset:2048 sc1
v_mfma_f32_16x16x32_bf16 a[244:247],v[92:95],a[180:183],a[244:247]
v_lshlrev_b32 v3,8,v3
v_mfma_f32_16x16x32_bf16 a[244:247],v[88:91],a[164:167],a[244:247]
v_mfma_f32_16x16x32_bf16 a[244:247],v[84:87],a[148:151],a[244:247]
v_mfma_f32_16x16x32_bf16 a[244:247],v[80:83],a[132:135],a[244:247]
v_mfma_f32_16x16x32_bf16 a[248:251],v[80:83],a[136:139],a[248:251]
buffer_load_dwordx4 v[36:39],v6,s[28:31],s88 offen sc1
v_mfma_f32_16x16x32_bf16 a[248:251],v[84:87],a[152:155],a[248:251]
v_mfma_f32_16x16x32_bf16 a[248:251],v[88:91],a[168:171],a[248:251]
v_mfma_f32_16x16x32_bf16 a[248:251],v[92:95],a[184:187],a[248:251]
v_mfma_f32_16x16x32_bf16 a[252:255],v[92:95],a[188:191],a[252:255]
ds_read_b64_tr_b16 v[92:93],v2 offset:30944
ds_read_b64_tr_b16 v[94:95],v2 offset:36064
v_mfma_f32_16x16x32_bf16 a[252:255],v[88:91],a[172:175],a[252:255]
buffer_load_dwordx4 a[120:123],v28,s[28:31],s90 offen offset:3072 sc1
v_mfma_f32_16x16x32_bf16 a[252:255],v[84:87],a[156:159],a[252:255]
s_add_u32 s90,s87,12288
ds_read_b64_tr_b16 v[84:85],v2 offset:10464
ds_read_b64_tr_b16 v[86:87],v2 offset:15584
v_mfma_f32_16x16x32_bf16 a[252:255],v[80:83],a[140:143],a[252:255]
ds_read_b64_tr_b16 v[80:81],v2 offset:224
v_mfma_f32_16x16x32_bf16 a[0:3],v[96:99],a[128:131],a[0:3]
ds_read_b64_tr_b16 v[82:83],v2 offset:5344
v_mfma_f32_16x16x32_bf16 a[0:3],v[100:103],a[144:147],a[0:3]
ds_read_b64_tr_b16 v[88:89],v2 offset:20704
v_mfma_f32_16x16x32_bf16 a[0:3],v[104:107],a[160:163],a[0:3]
buffer_load_dwordx4 v[40:43],v6,s[28:31],s89 offen sc1
)A"
R"A(v_mfma_f32_16x16x32_bf16 a[0:3],v[108:111],a[176:179],a[0:3]
ds_read_b64_tr_b16 v[90:91],v2 offset:25824
v_mfma_f32_16x16x32_bf16 a[4:7],v[108:111],a[180:183],a[4:7]
v_mfma_f32_16x16x32_bf16 a[4:7],v[104:107],a[164:167],a[4:7]
v_mfma_f32_16x16x32_bf16 a[4:7],v[100:103],a[148:151],a[4:7]
v_mfma_f32_16x16x32_bf16 a[4:7],v[96:99],a[132:135],a[4:7]
v_mfma_f32_16x16x32_bf16 a[8:11],v[96:99],a[136:139],a[8:11]
v_mfma_f32_16x16x32_bf16 a[8:11],v[100:103],a[152:155],a[8:11]
v_mfma_f32_16x16x32_bf16 a[8:11],v[104:107],a[168:171],a[8:11]
v_mfma_f32_16x16x32_bf16 a[8:11],v[108:111],a[184:187],a[8:11]
v_mfma_f32_16x16x32_bf16 a[12:15],v[108:111],a[188:191],a[12:15]
ds_read_b64_tr_b16 v[108:109],v1 offset:30976
ds_read_b64_tr_b16 v[110:111],v1 offset:36096
v_mfma_f32_16x16x32_bf16 a[12:15],v[104:107],a[172:175],a[12:15]
buffer_load_dwordx4 v[44:47],v6,s[28:31],s90 offen sc1
v_mfma_f32_16x16x32_bf16 a[12:15],v[100:103],a[156:159],a[12:15]
ds_read_b64_tr_b16 v[100:101],v1 offset:10496
ds_read_b64_tr_b16 v[102:103],v1 offset:15616
v_mfma_f32_16x16x32_bf16 a[12:15],v[96:99],a[140:143],a[12:15]
s_waitcnt lgkmcnt(4)
ds_read_b64_tr_b16 v[96:97],v1 offset:256
v_mfma_f32_16x16x32_bf16 a[16:19],v[112:115],a[128:131],a[16:19]
ds_read_b64_tr_b16 v[98:99],v1 offset:5376
v_mfma_f32_16x16x32_bf16 a[16:19],v[116:119],a[144:147],a[16:19]
ds_read_b64_tr_b16 v[104:105],v1 offset:20736
v_mfma_f32_16x16x32_bf16 a[16:19],v[120:123],a[160:163],a[16:19]
ds_read_b64_tr_b16 v[106:107],v1 offset:25856
v_add_u32 v1,0xffff6000,v1
v_mfma_f32_16x16x32_bf16 a[16:19],v[124:127],a[176:179],a[16:19]
v_mfma_f32_16x16x32_bf16 a[20:23],v[124:127],a[180:183],a[20:23]
v_mfma_f32_16x16x32_bf16 a[20:23],v[120:123],a[164:167],a[20:23]
v_mfma_f32_16x16x32_bf16 a[20:23],v[116:119],a[148:151],a[20:23]
v_mfma_f32_16x16x32_bf16 a[20:23],v[112:115],a[132:135],a[20:23]
v_mfma_f32_16x16x32_bf16 a[24:27],v[112:115],a[136:139],a[24:27]
buffer_load_dwordx4 v[180:183],v6,s[28:31],s90 offen offset:1024 sc1
v_mfma_f32_16x16x32_bf16 a[24:27],v[116:119],a[152:155],a[24:27]
v_mfma_f32_16x16x32_bf16 a[24:27],v[120:123],a[168:171],a[24:27]
v_mfma_f32_16x16x32_bf16 a[24:27],v[124:127],a[184:187],a[24:27]
v_mfma_f32_16x16x32_bf16 a[28:31],v[124:127],a[188:191],a[28:31]
ds_read_b64_tr_b16 v[124:125],v2 offset:31008
ds_read_b64_tr_b16 v[126:127],v2 offset:36128
v_mfma_f32_16x16x32_bf16 a[28:31],v[120:123],a[172:175],a[28:31]
ds_read_b64_tr_b16 v[120:121],v2 offset:20768
v_mfma_f32_16x16x32_bf16 a[28:31],v[116:119],a[156:159],a[28:31]
ds_read_b64_tr_b16 v[116:117],v2 offset:10528
v_mfma_f32_16x16x32_bf16 a[28:31],v[112:115],a[140:143],a[28:31]
ds_read_b64_tr_b16 v[112:113],v2 offset:288
v_mfma_f32_16x16x32_bf16 v[128:131],v[64:67],a[128:131],v[128:131]
ds_read_b64_tr_b16 v[114:115],v2 offset:5408
ds_read_b64_tr_b16 v[118:119],v2 offset:15648
v_mfma_f32_16x16x32_bf16 v[128:131],v[68:71],a[144:147],v[128:131]
ds_read_b64_tr_b16 v[122:123],v2 offset:25888
v_add_u32 v2,0xffff6000,v2
v_mfma_f32_16x16x32_bf16 v[128:131],v[72:75],a[160:163],v[128:131]
v_mfma_f32_16x16x32_bf16 v[128:131],v[76:79],a[176:179],v[128:131]
buffer_load_dwordx4 v[164:167],v6,s[28:31],s89 offen offset:1024 sc1
v_mfma_f32_16x16x32_bf16 v[136:139],v[76:79],a[180:183],v[136:139]
v_mfma_f32_16x16x32_bf16 v[136:139],v[72:75],a[164:167],v[136:139]
v_mfma_f32_16x16x32_bf16 v[136:139],v[68:71],a[148:151],v[136:139]
v_mfma_f32_16x16x32_bf16 v[136:139],v[64:67],a[132:135],v[136:139]
v_mfma_f32_16x16x32_bf16 v[144:147],v[64:67],a[136:139],v[144:147]
v_mfma_f32_16x16x32_bf16 v[144:147],v[68:71],a[152:155],v[144:147]
v_mfma_f32_16x16x32_bf16 v[144:147],v[72:75],a[168:171],v[144:147]
v_mfma_f32_16x16x32_bf16 v[144:147],v[76:79],a[184:187],v[144:147]
v_mfma_f32_16x16x32_bf16 v[152:155],v[76:79],a[188:191],v[152:155]
v_mfma_f32_16x16x32_bf16 v[152:155],v[72:75],a[172:175],v[152:155]
v_mfma_f32_16x16x32_bf16 v[152:155],v[68:71],a[156:159],v[152:155]
buffer_load_dwordx4 v[148:151],v6,s[28:31],s88 offen offset:1024 sc1
v_mfma_f32_16x16x32_bf16 v[152:155],v[64:67],a[140:143],v[152:155]
s_barrier
v_mfma_f32_16x16x32_bf16 v[160:163],v[80:83],a[128:131],v[160:163]
ds_read_b64_tr_b16 v[64:65],v1
ds_read_b64_tr_b16 v[66:67],v1 offset:5120
v_mfma_f32_16x16x32_bf16 v[160:163],v[84:87],a[144:147],v[160:163]
ds_read_b64_tr_b16 v[68:69],v1 offset:10240
v_mfma_f32_16x16x32_bf16 v[160:163],v[88:91],a[160:163],v[160:163]
ds_read_b64_tr_b16 v[70:71],v1 offset:15360
v_mfma_f32_16x16x32_bf16 v[160:163],v[92:95],a[176:179],v[160:163]
buffer_load_dwordx4 v24,s[48:51],s83 offen lds
v_mfma_f32_16x16x32_bf16 v[168:171],v[92:95],a[180:183],v[168:171]
ds_read_b64_tr_b16 v[72:73],v1 offset:20480
ds_read_b64_tr_b16 v[74:75],v1 offset:25600
v_mfma_f32_16x16x32_bf16 v[168:171],v[88:91],a[164:167],v[168:171]
ds_read_b64_tr_b16 v[76:77],v1 offset:30720
v_mfma_f32_16x16x32_bf16 v[168:171],v[84:87],a[148:151],v[168:171]
ds_read_b64_tr_b16 v[78:79],v1 offset:35840
v_mfma_f32_16x16x32_bf16 v[168:171],v[80:83],a[132:135],v[168:171]
v_mfma_f32_16x16x32_bf16 v[176:179],v[80:83],a[136:139],v[176:179]
buffer_load_dwordx4 v[132:135],v6,s[28:31],s87 offen offset:1024 sc1
v_mfma_f32_16x16x32_bf16 v[176:179],v[84:87],a[152:155],v[176:179]
v_mfma_f32_16x16x32_bf16 v[176:179],v[88:91],a[168:171],v[176:179]
v_mfma_f32_16x16x32_bf16 v[176:179],v[92:95],a[184:187],v[176:179]
v_mfma_f32_16x16x32_bf16 v[184:187],v[92:95],a[188:191],v[184:187]
ds_read_b64_tr_b16 v[92:93],v2 offset:30752
ds_read_b64_tr_b16 v[94:95],v2 offset:35872
v_mfma_f32_16x16x32_bf16 v[184:187],v[88:91],a[172:175],v[184:187]
ds_read_b64_tr_b16 v[88:89],v2 offset:20512
v_mfma_f32_16x16x32_bf16 v[184:187],v[84:87],a[156:159],v[184:187]
buffer_load_dwordx4 v25,s[48:51],s83 offen offset:1024 lds
v_mfma_f32_16x16x32_bf16 v[184:187],v[80:83],a[140:143],v[184:187]
s_waitcnt lgkmcnt(3)
ds_read_b64_tr_b16 v[80:81],v2 offset:32
ds_read_b64_tr_b16 v[82:83],v2 offset:5152
v_mfma_f32_16x16x32_bf16 v[192:195],v[96:99],a[128:131],v[192:195]
ds_read_b64_tr_b16 v[84:85],v2 offset:10272
v_mfma_f32_16x16x32_bf16 v[192:195],v[100:103],a[144:147],v[192:195]
ds_read_b64_tr_b16 v[86:87],v2 offset:15392
v_mfma_f32_16x16x32_bf16 v[192:195],v[104:107],a[160:163],v[192:195]
ds_read_b64_tr_b16 v[90:91],v2 offset:25632
v_mfma_f32_16x16x32_bf16 v[192:195],v[108:111],a[176:179],v[192:195]
v_mfma_f32_16x16x32_bf16 v[200:203],v[108:111],a[180:183],v[200:203]
buffer_load_dwordx4 v26,s[48:51],s83 offen offset:2048 lds
v_mfma_f32_16x16x32_bf16 v[200:203],v[104:107],a[164:167],v[200:203]
v_mfma_f32_16x16x32_bf16 v[200:203],v[100:103],a[148:151],v[200:203]
v_mfma_f32_16x16x32_bf16 v[200:203],v[96:99],a[132:135],v[200:203]
v_mfma_f32_16x16x32_bf16 v[208:211],v[96:99],a[136:139],v[208:211]
v_mfma_f32_16x16x32_bf16 v[208:211],v[100:103],a[152:155],v[208:211]
buffer_load_dwordx4 v[196:199],v6,s[28:31],s87 offen offset:2048 sc1
v_mfma_f32_16x16x32_bf16 v[208:211],v[104:107],a[168:171],v[208:211]
v_mfma_f32_16x16x32_bf16 v[208:211],v[108:111],a[184:187],v[208:211]
v_mfma_f32_16x16x32_bf16 v[216:219],v[108:111],a[188:191],v[216:219]
ds_read_b64_tr_b16 v[108:109],v1 offset:30784
ds_read_b64_tr_b16 v[110:111],v1 offset:35904
v_mfma_f32_16x16x32_bf16 v[216:219],v[104:107],a[172:175],v[216:219]
ds_read_b64_tr_b16 v[104:105],v1 offset:20544
v_mfma_f32_16x16x32_bf16 v[216:219],v[100:103],a[156:159],v[216:219]
ds_read_b64_tr_b16 v[100:101],v1 offset:10304
v_mfma_f32_16x16x32_bf16 v[216:219],v[96:99],a[140:143],v[216:219]
buffer_load_dwordx4 v27,s[48:51],s83 offen offset:3072 lds
v_mfma_f32_16x16x32_bf16 v[224:227],v[112:115],a[128:131],v[224:227]
s_add_u32 m0,s86,126976
ds_read_b64_tr_b16 v[96:97],v1 offset:64
ds_read_b64_tr_b16 v[98:99],v1 offset:5184
v_mfma_f32_16x16x32_bf16 v[224:227],v[116:119],a[144:147],v[224:227]
ds_read_b64_tr_b16 v[102:103],v1 offset:15424
v_mfma_f32_16x16x32_bf16 v[224:227],v[120:123],a[160:163],v[224:227]
ds_read_b64_tr_b16 v[106:107],v1 offset:25664
v_mfma_f32_16x16x32_bf16 v[224:227],v[124:127],a[176:179],v[224:227]
v_mfma_f32_16x16x32_bf16 v[232:235],v[124:127],a[180:183],v[232:235]
buffer_load_dwordx4 v[212:215],v6,s[28:31],s88 offen offset:2048 sc1
v_mfma_f32_16x16x32_bf16 v[232:235],v[120:123],a[164:167],v[232:235]
v_mfma_f32_16x16x32_bf16 v[232:235],v[116:119],a[148:151],v[232:235]
v_mfma_f32_16x16x32_bf16 v[232:235],v[112:115],a[132:135],v[232:235]
v_mfma_f32_16x16x32_bf16 v[240:243],v[112:115],a[136:139],v[240:243]
v_mfma_f32_16x16x32_bf16 v[240:243],v[116:119],a[152:155],v[240:243]
v_mfma_f32_16x16x32_bf16 v[240:243],v[120:123],a[168:171],v[240:243]
buffer_load_dwordx4 v7,s[48:51],s83 offen lds
v_mfma_f32_16x16x32_bf16 v[240:243],v[124:127],a[184:187],v[240:243]
s_add_u32 m0,s86,128000
s_mul_i32 s83,s63,20480
v_mfma_f32_16x16x32_bf16 v[248:251],v[124:127],a[188:191],v[248:251]
ds_read_b64_tr_b16 v[124:125],v2 offset:30816
ds_read_b64_tr_b16 v[126:127],v2 offset:35936
v_mfma_f32_16x16x32_bf16 v[248:251],v[120:123],a[172:175],v[248:251]
ds_read_b64_tr_b16 v[120:121],v2 offset:20576
v_mfma_f32_16x16x32_bf16 v[248:251],v[116:119],a[156:159],v[248:251]
ds_read_b64_tr_b16 v[116:117],v2 offset:10336
v_mfma_f32_16x16x32_bf16 v[248:251],v[112:115],a[140:143],v[248:251]
buffer_load_dwordx4 v[228:231],v6,s[28:31],s89 offen offset:2048 sc1
v_mfma_f32_16x16x32_bf16 a[192:195],v[64:67],a[68:71],a[192:195]
s_waitcnt vmcnt(17)
ds_read_b64_tr_b16 v[112:113],v2 offset:96
ds_read_b64_tr_b16 v[114:115],v2 offset:5216
v_mfma_f32_16x16x32_bf16 a[192:195],v[68:71],a[76:79],a[192:195]
ds_read_b64_tr_b16 v[118:119],v2 offset:15456
v_mfma_f32_16x16x32_bf16 a[192:195],v[72:75],a[84:87],a[192:195]
ds_read_b64_tr_b16 v[122:123],v2 offset:25696
v_mfma_f32_16x16x32_bf16 a[192:195],v[76:79],a[92:95],a[192:195]
v_mfma_f32_16x16x32_bf16 a[196:199],v[76:79],a[124:127],a[196:199]
v_mfma_f32_16x16x32_bf16 a[196:199],v[72:75],a[116:119],a[196:199]
buffer_load_dwordx4 v24,s[48:51],s84 offen lds
v_mfma_f32_16x16x32_bf16 a[196:199],v[68:71],a[108:111],a[196:199]
v_mfma_f32_16x16x32_bf16 a[196:199],v[64:67],a[100:103],a[196:199]
v_mfma_f32_16x16x32_bf16 a[200:203],v[64:67],a[64:67],a[200:203]
v_mfma_f32_16x16x32_bf16 a[200:203],v[68:71],a[72:75],a[200:203]
s_waitcnt vmcnt(16)
v_mfma_f32_16x16x32_bf16 a[200:203],v[72:75],a[80:83],a[200:203]
buffer_load_dwordx4 v[244:247],v6,s[28:31],s90 offen offset:2048 sc1
v_mfma_f32_16x16x32_bf16 a[200:203],v[76:79],a[88:91],a[200:203]
s_waitcnt vmcnt(16)
v_mfma_f32_16x16x32_bf16 a[204:207],v[76:79],a[120:123],a[204:207]
ds_read_b64_tr_b16 v[76:77],v1 offset:30848
ds_read_b64_tr_b16 v[78:79],v1 offset:35968
v_mfma_f32_16x16x32_bf16 a[204:207],v[72:75],a[112:115],a[204:207]
ds_read_b64_tr_b16 v[72:73],v1 offset:20608
v_mfma_f32_16x16x32_bf16 a[204:207],v[68:71],a[104:107],a[204:207]
ds_read_b64_tr_b16 v[68:69],v1 offset:10368
v_mfma_f32_16x16x32_bf16 a[204:207],v[64:67],a[96:99],a[204:207]
s_waitcnt lgkmcnt(4)
ds_read_b64_tr_b16 v[64:65],v1 offset:128
v_mfma_f32_16x16x32_bf16 a[208:211],v[80:83],a[68:71],a[208:211]
buffer_load_dwordx4 v25,s[48:51],s84 offen offset:1024 lds
v_mfma_f32_16x16x32_bf16 a[208:211],v[84:87],a[76:79],a[208:211]
ds_read_b64_tr_b16 v[66:67],v1 offset:5248
ds_read_b64_tr_b16 v[70:71],v1 offset:15488
v_mfma_f32_16x16x32_bf16 a[208:211],v[88:91],a[84:87],a[208:211]
ds_read_b64_tr_b16 v[74:75],v1 offset:25728
v_mfma_f32_16x16x32_bf16 a[208:211],v[92:95],a[92:95],a[208:211]
v_mfma_f32_16x16x32_bf16 a[212:215],v[92:95],a[124:127],a[212:215]
v_mfma_f32_16x16x32_bf16 a[212:215],v[88:91],a[116:119],a[212:215]
buffer_load_dwordx4 v[188:191],v28,s[28:31],s90 offen offset:3072 sc1
v_mfma_f32_16x16x32_bf16 a[212:215],v[84:87],a[108:111],a[212:215]
v_mfma_f32_16x16x32_bf16 a[212:215],v[80:83],a[100:103],a[212:215]
v_mfma_f32_16x16x32_bf16 a[216:219],v[80:83],a[64:67],a[216:219]
v_mfma_f32_16x16x32_bf16 a[216:219],v[84:87],a[72:75],a[216:219]
v_mfma_f32_16x16x32_bf16 a[216:219],v[88:91],a[80:83],a[216:219]
v_mfma_f32_16x16x32_bf16 a[216:219],v[92:95],a[88:91],a[216:219]
buffer_load_dwordx4 v26,s[48:51],s84 offen offset:2048 lds
v_mfma_f32_16x16x32_bf16 a[220:223],v[92:95],a[120:123],a[220:223]
)A"
R"A(ds_read_b64_tr_b16 v[92:93],v2 offset:30880
ds_read_b64_tr_b16 v[94:95],v2 offset:36000
v_mfma_f32_16x16x32_bf16 a[220:223],v[88:91],a[112:115],a[220:223]
ds_read_b64_tr_b16 v[88:89],v2 offset:20640
v_mfma_f32_16x16x32_bf16 a[220:223],v[84:87],a[104:107],a[220:223]
ds_read_b64_tr_b16 v[84:85],v2 offset:10400
v_mfma_f32_16x16x32_bf16 a[220:223],v[80:83],a[96:99],a[220:223]
ds_read_b64_tr_b16 v[80:81],v2 offset:160
v_mfma_f32_16x16x32_bf16 a[224:227],v[96:99],a[68:71],a[224:227]
ds_read_b64_tr_b16 v[82:83],v2 offset:5280
ds_read_b64_tr_b16 v[86:87],v2 offset:15520
v_mfma_f32_16x16x32_bf16 a[224:227],v[100:103],a[76:79],a[224:227]
buffer_load_dwordx4 v27,s[48:51],s84 offen offset:3072 lds
v_mfma_f32_16x16x32_bf16 a[224:227],v[104:107],a[84:87],a[224:227]
s_add_u32 m0,s86,132096
ds_read_b64_tr_b16 v[90:91],v2 offset:25760
v_mfma_f32_16x16x32_bf16 a[224:227],v[108:111],a[92:95],a[224:227]
v_mfma_f32_16x16x32_bf16 a[228:231],v[108:111],a[124:127],a[228:231]
v_mfma_f32_16x16x32_bf16 a[228:231],v[104:107],a[116:119],a[228:231]
v_mfma_f32_16x16x32_bf16 a[228:231],v[100:103],a[108:111],a[228:231]
buffer_load_dwordx4 v[172:175],v28,s[28:31],s89 offen offset:3072 sc1
v_mfma_f32_16x16x32_bf16 a[228:231],v[96:99],a[100:103],a[228:231]
v_mfma_f32_16x16x32_bf16 a[232:235],v[96:99],a[64:67],a[232:235]
v_mfma_f32_16x16x32_bf16 a[232:235],v[100:103],a[72:75],a[232:235]
v_mfma_f32_16x16x32_bf16 a[232:235],v[104:107],a[80:83],a[232:235]
v_mfma_f32_16x16x32_bf16 a[232:235],v[108:111],a[88:91],a[232:235]
v_mfma_f32_16x16x32_bf16 a[236:239],v[108:111],a[120:123],a[236:239]
buffer_load_dwordx4 v7,s[48:51],s84 offen lds
v_mfma_f32_16x16x32_bf16 a[236:239],v[104:107],a[112:115],a[236:239]
ds_read_b64_tr_b16 v[104:105],v1 offset:20672
ds_read_b64_tr_b16 v[106:107],v1 offset:25792
v_mfma_f32_16x16x32_bf16 a[236:239],v[100:103],a[104:107],a[236:239]
ds_read_b64_tr_b16 v[100:101],v1 offset:10432
v_mfma_f32_16x16x32_bf16 a[236:239],v[96:99],a[96:99],a[236:239]
ds_read_b64_tr_b16 v[96:97],v1 offset:192
v_mfma_f32_16x16x32_bf16 a[240:243],v[112:115],a[68:71],a[240:243]
ds_read_b64_tr_b16 v[98:99],v1 offset:5312
v_mfma_f32_16x16x32_bf16 a[240:243],v[116:119],a[76:79],a[240:243]
buffer_load_dwordx4 v[156:159],v28,s[28:31],s88 offen offset:3072 sc1
v_mfma_f32_16x16x32_bf16 a[240:243],v[120:123],a[84:87],a[240:243]
ds_read_b64_tr_b16 v[102:103],v1 offset:15552
ds_read_b64_tr_b16 v[108:109],v1 offset:30912
v_mfma_f32_16x16x32_bf16 a[240:243],v[124:127],a[92:95],a[240:243]
ds_read_b64_tr_b16 v[110:111],v1 offset:36032
v_add_u32 v1,0xffff6000,v1
v_mfma_f32_16x16x32_bf16 a[244:247],v[124:127],a[124:127],a[244:247]
v_mfma_f32_16x16x32_bf16 a[244:247],v[120:123],a[116:119],a[244:247]
v_mfma_f32_16x16x32_bf16 a[244:247],v[116:119],a[108:111],a[244:247]
buffer_load_dwordx4 v[140:143],v28,s[28:31],s87 offen offset:3072 sc1
v_mfma_f32_16x16x32_bf16 a[244:247],v[112:115],a[100:103],a[244:247]
s_add_u32 s87,s64,3
s_and_b32 s87,s87,3
s_lshl_b32 s87,s87,1
s_or_b32 s87,s87,1
s_lshl_b32 s87,s87,16
s_add_u32 s88,s87,4096
s_add_u32 s89,s87,8192
s_add_u32 s90,s87,12288
v_mfma_f32_16x16x32_bf16 a[248:251],v[112:115],a[64:67],a[248:251]
v_mfma_f32_16x16x32_bf16 a[248:251],v[116:119],a[72:75],a[248:251]
v_mfma_f32_16x16x32_bf16 a[248:251],v[120:123],a[80:83],a[248:251]
v_mfma_f32_16x16x32_bf16 a[248:251],v[124:127],a[88:91],a[248:251]
buffer_load_dwordx4 a[128:131],v6,s[28:31],s87 offen sc1
v_mfma_f32_16x16x32_bf16 a[252:255],v[124:127],a[120:123],a[252:255]
ds_read_b64_tr_b16 v[124:125],v2 offset:30944
ds_read_b64_tr_b16 v[126:127],v2 offset:36064
v_mfma_f32_16x16x32_bf16 a[252:255],v[120:123],a[112:115],a[252:255]
ds_read_b64_tr_b16 v[120:121],v2 offset:20704
v_mfma_f32_16x16x32_bf16 a[252:255],v[116:119],a[104:107],a[252:255]
ds_read_b64_tr_b16 v[116:117],v2 offset:10464
v_mfma_f32_16x16x32_bf16 a[252:255],v[112:115],a[96:99],a[252:255]
s_waitcnt lgkmcnt(4)
ds_read_b64_tr_b16 v[112:113],v2 offset:224
v_mfma_f32_16x16x32_bf16 a[0:3],v[64:67],a[68:71],a[0:3]
buffer_load_dwordx4 a[144:147],v6,s[28:31],s88 offen sc1
v_mfma_f32_16x16x32_bf16 a[0:3],v[68:71],a[76:79],a[0:3]
ds_read_b64_tr_b16 v[114:115],v2 offset:5344
ds_read_b64_tr_b16 v[118:119],v2 offset:15584
v_mfma_f32_16x16x32_bf16 a[0:3],v[72:75],a[84:87],a[0:3]
ds_read_b64_tr_b16 v[122:123],v2 offset:25824
v_add_u32 v2,0xffff6000,v2
v_mfma_f32_16x16x32_bf16 a[0:3],v[76:79],a[92:95],a[0:3]
v_mfma_f32_16x16x32_bf16 a[4:7],v[76:79],a[124:127],a[4:7]
v_mfma_f32_16x16x32_bf16 a[4:7],v[72:75],a[116:119],a[4:7]
buffer_load_dwordx4 a[160:163],v6,s[28:31],s89 offen sc1
v_mfma_f32_16x16x32_bf16 a[4:7],v[68:71],a[108:111],a[4:7]
v_mfma_f32_16x16x32_bf16 a[4:7],v[64:67],a[100:103],a[4:7]
v_mfma_f32_16x16x32_bf16 a[8:11],v[64:67],a[64:67],a[8:11]
v_mfma_f32_16x16x32_bf16 a[8:11],v[68:71],a[72:75],a[8:11]
v_mfma_f32_16x16x32_bf16 a[8:11],v[72:75],a[80:83],a[8:11]
buffer_load_dwordx4 a[176:179],v6,s[28:31],s90 offen sc1
v_mfma_f32_16x16x32_bf16 a[8:11],v[76:79],a[88:91],a[8:11]
v_mfma_f32_16x16x32_bf16 a[12:15],v[76:79],a[120:123],a[12:15]
ds_read_b64_tr_b16 v[76:77],v1 offset:30720
ds_read_b64_tr_b16 v[78:79],v1 offset:35840
v_mfma_f32_16x16x32_bf16 a[12:15],v[72:75],a[112:115],a[12:15]
ds_read_b64_tr_b16 v[72:73],v1 offset:20480
v_mfma_f32_16x16x32_bf16 a[12:15],v[68:71],a[104:107],a[12:15]
ds_read_b64_tr_b16 v[68:69],v1 offset:10240
v_mfma_f32_16x16x32_bf16 a[12:15],v[64:67],a[96:99],a[12:15]
buffer_load_dwordx4 a[180:183],v6,s[28:31],s90 offen offset:1024 sc1
v_mfma_f32_16x16x32_bf16 a[16:19],v[80:83],a[68:71],a[16:19]
ds_read_b64_tr_b16 v[64:65],v1
ds_read_b64_tr_b16 v[66:67],v1 offset:5120
v_mfma_f32_16x16x32_bf16 a[16:19],v[84:87],a[76:79],a[16:19]
ds_read_b64_tr_b16 v[70:71],v1 offset:15360
v_mfma_f32_16x16x32_bf16 a[16:19],v[88:91],a[84:87],a[16:19]
ds_read_b64_tr_b16 v[74:75],v1 offset:25600
v_add_u32 v1,0x1e000,v1
v_mfma_f32_16x16x32_bf16 a[16:19],v[92:95],a[92:95],a[16:19]
v_mfma_f32_16x16x32_bf16 a[20:23],v[92:95],a[124:127],a[20:23]
buffer_load_dwordx4 a[164:167],v6,s[28:31],s89 offen offset:1024 sc1
v_mfma_f32_16x16x32_bf16 a[20:23],v[88:91],a[116:119],a[20:23]
v_mfma_f32_16x16x32_bf16 a[20:23],v[84:87],a[108:111],a[20:23]
v_mfma_f32_16x16x32_bf16 a[20:23],v[80:83],a[100:103],a[20:23]
v_mfma_f32_16x16x32_bf16 a[24:27],v[80:83],a[64:67],a[24:27]
v_mfma_f32_16x16x32_bf16 a[24:27],v[84:87],a[72:75],a[24:27]
buffer_load_dwordx4 a[148:151],v6,s[28:31],s88 offen offset:1024 sc1
v_mfma_f32_16x16x32_bf16 a[24:27],v[88:91],a[80:83],a[24:27]
v_mfma_f32_16x16x32_bf16 a[24:27],v[92:95],a[88:91],a[24:27]
v_mfma_f32_16x16x32_bf16 a[28:31],v[92:95],a[120:123],a[28:31]
ds_read_b64_tr_b16 v[92:93],v2 offset:30752
ds_read_b64_tr_b16 v[94:95],v2 offset:35872
v_mfma_f32_16x16x32_bf16 a[28:31],v[88:91],a[112:115],a[28:31]
s_nop 0
L_pd17_%=:
s_cbranch_execz L_trtg2F_%=
ds_read_b64_tr_b16 v[88:89],v2 offset:20512
v_mfma_f32_16x16x32_bf16 a[28:31],v[84:87],a[104:107],a[28:31]
buffer_load_dwordx4 a[132:135],v6,s[28:31],s87 offen offset:1024 sc1
v_mfma_f32_16x16x32_bf16 a[28:31],v[80:83],a[96:99],a[28:31]
ds_read_b64_tr_b16 v[80:81],v2 offset:32
ds_read_b64_tr_b16 v[82:83],v2 offset:5152
v_mfma_f32_16x16x32_bf16 v[128:131],v[96:99],a[68:71],v[128:131]
ds_read_b64_tr_b16 v[84:85],v2 offset:10272
v_mfma_f32_16x16x32_bf16 v[128:131],v[100:103],a[76:79],v[128:131]
ds_read_b64_tr_b16 v[86:87],v2 offset:15392
v_mfma_f32_16x16x32_bf16 v[128:131],v[104:107],a[84:87],v[128:131]
ds_read_b64_tr_b16 v[90:91],v2 offset:25632
v_add_u32 v2,0x1e000,v2
v_mfma_f32_16x16x32_bf16 v[128:131],v[108:111],a[92:95],v[128:131]
buffer_load_dwordx4 a[136:139],v6,s[28:31],s87 offen offset:2048 sc1
v_mfma_f32_16x16x32_bf16 v[136:139],v[108:111],a[124:127],v[136:139]
v_mfma_f32_16x16x32_bf16 v[136:139],v[104:107],a[116:119],v[136:139]
v_mfma_f32_16x16x32_bf16 v[136:139],v[100:103],a[108:111],v[136:139]
v_mfma_f32_16x16x32_bf16 v[136:139],v[96:99],a[100:103],v[136:139]
v_mfma_f32_16x16x32_bf16 v[144:147],v[96:99],a[64:67],v[144:147]
buffer_load_dwordx4 a[152:155],v6,s[28:31],s88 offen offset:2048 sc1
v_mfma_f32_16x16x32_bf16 v[144:147],v[100:103],a[72:75],v[144:147]
v_mfma_f32_16x16x32_bf16 v[144:147],v[104:107],a[80:83],v[144:147]
v_mfma_f32_16x16x32_bf16 v[144:147],v[108:111],a[88:91],v[144:147]
v_mfma_f32_16x16x32_bf16 v[152:155],v[108:111],a[120:123],v[152:155]
v_mfma_f32_16x16x32_bf16 v[152:155],v[104:107],a[112:115],v[152:155]
buffer_load_dwordx4 a[168:171],v6,s[28:31],s89 offen offset:2048 sc1
v_mfma_f32_16x16x32_bf16 v[152:155],v[100:103],a[104:107],v[152:155]
v_mfma_f32_16x16x32_bf16 v[152:155],v[96:99],a[96:99],v[152:155]
s_waitcnt vmcnt(13)
s_barrier
s_waitcnt lgkmcnt(0)
v_mfma_f32_16x16x32_bf16 v[160:163],v[112:115],a[68:71],v[160:163]
ds_read_b64_tr_b16 v[96:97],v1
ds_read_b64_tr_b16 v[98:99],v1 offset:5120
v_mfma_f32_16x16x32_bf16 v[160:163],v[116:119],a[76:79],v[160:163]
buffer_load_dwordx4 a[184:187],v6,s[28:31],s90 offen offset:2048 sc1
v_mfma_f32_16x16x32_bf16 v[160:163],v[120:123],a[84:87],v[160:163]
ds_read_b64_tr_b16 v[100:101],v1 offset:10240
ds_read_b64_tr_b16 v[102:103],v1 offset:15360
v_mfma_f32_16x16x32_bf16 v[160:163],v[124:127],a[92:95],v[160:163]
ds_read_b64_tr_b16 v[104:105],v1 offset:20480
v_mfma_f32_16x16x32_bf16 v[168:171],v[124:127],a[124:127],v[168:171]
ds_read_b64_tr_b16 v[106:107],v1 offset:25600
v_mfma_f32_16x16x32_bf16 v[168:171],v[120:123],a[116:119],v[168:171]
ds_read_b64_tr_b16 v[108:109],v1 offset:30720
v_mfma_f32_16x16x32_bf16 v[168:171],v[116:119],a[108:111],v[168:171]
buffer_load_dwordx4 a[188:191],v28,s[28:31],s90 offen offset:3072 sc1
v_mfma_f32_16x16x32_bf16 v[168:171],v[112:115],a[100:103],v[168:171]
ds_read_b64_tr_b16 v[110:111],v1 offset:35840
v_add_u32 v1,0xfffe2000,v1
v_mfma_f32_16x16x32_bf16 v[176:179],v[112:115],a[64:67],v[176:179]
v_mfma_f32_16x16x32_bf16 v[176:179],v[116:119],a[72:75],v[176:179]
v_mfma_f32_16x16x32_bf16 v[176:179],v[120:123],a[80:83],v[176:179]
v_mfma_f32_16x16x32_bf16 v[176:179],v[124:127],a[88:91],v[176:179]
buffer_load_dwordx4 a[172:175],v28,s[28:31],s89 offen offset:3072 sc1
v_mfma_f32_16x16x32_bf16 v[184:187],v[124:127],a[120:123],v[184:187]
ds_read_b64_tr_b16 v[124:125],v2 offset:30752
ds_read_b64_tr_b16 v[126:127],v2 offset:35872
v_mfma_f32_16x16x32_bf16 v[184:187],v[120:123],a[112:115],v[184:187]
ds_read_b64_tr_b16 v[120:121],v2 offset:20512
v_mfma_f32_16x16x32_bf16 v[184:187],v[116:119],a[104:107],v[184:187]
ds_read_b64_tr_b16 v[116:117],v2 offset:10272
v_mfma_f32_16x16x32_bf16 v[184:187],v[112:115],a[96:99],v[184:187]
ds_read_b64_tr_b16 v[112:113],v2 offset:32
v_mfma_f32_16x16x32_bf16 a[192:195],v[64:67],v[32:35],a[192:195]
buffer_load_dwordx4 a[156:159],v28,s[28:31],s88 offen offset:3072 sc1
v_mfma_f32_16x16x32_bf16 a[192:195],v[68:71],v[36:39],a[192:195]
ds_read_b64_tr_b16 v[114:115],v2 offset:5152
ds_read_b64_tr_b16 v[118:119],v2 offset:15392
v_mfma_f32_16x16x32_bf16 a[192:195],v[72:75],v[40:43],a[192:195]
ds_read_b64_tr_b16 v[122:123],v2 offset:25632
v_add_u32 v2,0xfffe2000,v2
v_mfma_f32_16x16x32_bf16 a[192:195],v[76:79],v[44:47],a[192:195]
v_mfma_f32_16x16x32_bf16 a[196:199],v[76:79],v[180:183],a[196:199]
v_mfma_f32_16x16x32_bf16 a[196:199],v[72:75],v[164:167],a[196:199]
buffer_load_dwordx4 a[140:143],v28,s[28:31],s87 offen offset:3072 sc1
v_mfma_f32_16x16x32_bf16 a[196:199],v[68:71],v[148:151],a[196:199]
s_mul_i32 s87,s60,81920
s_add_u32 s87,s87,s66
s_add_u32 s83,s83,s87
v_mfma_f32_16x16x32_bf16 a[196:199],v[64:67],v[132:135],a[196:199]
s_lshl_b32 s66,s63,6
s_add_u32 s87,s87,s66
s_add_u32 s88,s87,20480
v_mfma_f32_16x16x32_bf16 a[200:203],v[64:67],v[196:199],a[200:203]
s_add_u32 s89,s87,40960
s_add_u32 s90,s87,61440
s_add_u32 s66,s77,4096
v_mfma_f32_16x16x32_bf16 a[200:203],v[68:71],v[212:215],a[200:203]
v_mfma_f32_16x16x32_bf16 a[200:203],v[72:75],v[228:231],a[200:203]
v_mfma_f32_16x16x32_bf16 a[200:203],v[76:79],v[244:247],a[200:203]
v_mfma_f32_16x16x32_bf16 a[204:207],v[76:79],v[188:191],a[204:207]
ds_read_b64_tr_b16 v[76:77],v1 offset:30784
ds_read_b64_tr_b16 v[78:79],v1 offset:35904
v_mfma_f32_16x16x32_bf16 a[204:207],v[72:75],v[172:175],a[204:207]
)A"
R"A(s_waitcnt vmcnt(17)
ds_read_b64_tr_b16 v[72:73],v1 offset:20544
v_mfma_f32_16x16x32_bf16 a[204:207],v[68:71],v[156:159],a[204:207]
s_waitcnt vmcnt(16)
ds_read_b64_tr_b16 v[68:69],v1 offset:10304
v_mfma_f32_16x16x32_bf16 a[204:207],v[64:67],v[140:143],a[204:207]
ds_read_b64_tr_b16 v[64:65],v1 offset:64
v_mfma_f32_16x16x32_bf16 a[208:211],v[80:83],v[32:35],a[208:211]
ds_read_b64_tr_b16 v[66:67],v1 offset:5184
ds_read_b64_tr_b16 v[70:71],v1 offset:15424
v_mfma_f32_16x16x32_bf16 a[208:211],v[84:87],v[36:39],a[208:211]
ds_read_b64_tr_b16 v[74:75],v1 offset:25664
v_add_u32 v1,0x1e000,v1
v_mfma_f32_16x16x32_bf16 a[208:211],v[88:91],v[40:43],a[208:211]
v_mfma_f32_16x16x32_bf16 a[208:211],v[92:95],v[44:47],a[208:211]
v_mfma_f32_16x16x32_bf16 a[212:215],v[92:95],v[180:183],a[212:215]
v_mfma_f32_16x16x32_bf16 a[212:215],v[88:91],v[164:167],a[212:215]
v_mfma_f32_16x16x32_bf16 a[212:215],v[84:87],v[148:151],a[212:215]
v_mfma_f32_16x16x32_bf16 a[212:215],v[80:83],v[132:135],a[212:215]
v_mfma_f32_16x16x32_bf16 a[216:219],v[80:83],v[196:199],a[216:219]
v_mfma_f32_16x16x32_bf16 a[216:219],v[84:87],v[212:215],a[216:219]
v_mfma_f32_16x16x32_bf16 a[216:219],v[88:91],v[228:231],a[216:219]
v_mfma_f32_16x16x32_bf16 a[216:219],v[92:95],v[244:247],a[216:219]
v_mfma_f32_16x16x32_bf16 a[220:223],v[92:95],v[188:191],a[220:223]
ds_read_b64_tr_b16 v[92:93],v2 offset:30816
ds_read_b64_tr_b16 v[94:95],v2 offset:35936
v_mfma_f32_16x16x32_bf16 a[220:223],v[88:91],v[172:175],a[220:223]
ds_read_b64_tr_b16 v[88:89],v2 offset:20576
v_mfma_f32_16x16x32_bf16 a[220:223],v[84:87],v[156:159],a[220:223]
ds_read_b64_tr_b16 v[84:85],v2 offset:10336
v_mfma_f32_16x16x32_bf16 a[220:223],v[80:83],v[140:143],a[220:223]
s_waitcnt vmcnt(15) lgkmcnt(4)
ds_read_b64_tr_b16 v[80:81],v2 offset:96
v_mfma_f32_16x16x32_bf16 a[192:195],v[96:99],a[128:131],a[192:195]
s_waitcnt vmcnt(14)
ds_read_b64_tr_b16 v[82:83],v2 offset:5216
ds_read_b64_tr_b16 v[86:87],v2 offset:15456
v_mfma_f32_16x16x32_bf16 a[192:195],v[100:103],a[144:147],a[192:195]
s_waitcnt vmcnt(13)
ds_read_b64_tr_b16 v[90:91],v2 offset:25696
v_add_u32 v2,0x1e000,v2
v_mfma_f32_16x16x32_bf16 a[192:195],v[104:107],a[160:163],a[192:195]
s_waitcnt vmcnt(12)
v_mfma_f32_16x16x32_bf16 a[192:195],v[108:111],a[176:179],a[192:195]
s_waitcnt vmcnt(11)
v_mfma_f32_16x16x32_bf16 a[196:199],v[108:111],a[180:183],a[196:199]
s_waitcnt vmcnt(10)
v_mfma_f32_16x16x32_bf16 a[196:199],v[104:107],a[164:167],a[196:199]
s_waitcnt vmcnt(9)
v_mfma_f32_16x16x32_bf16 a[196:199],v[100:103],a[148:151],a[196:199]
s_waitcnt vmcnt(8)
v_mfma_f32_16x16x32_bf16 a[196:199],v[96:99],a[132:135],a[196:199]
s_waitcnt vmcnt(7)
v_mfma_f32_16x16x32_bf16 a[200:203],v[96:99],a[136:139],a[200:203]
s_waitcnt vmcnt(6)
v_mfma_f32_16x16x32_bf16 a[200:203],v[100:103],a[152:155],a[200:203]
s_waitcnt vmcnt(5)
v_mfma_f32_16x16x32_bf16 a[200:203],v[104:107],a[168:171],a[200:203]
s_waitcnt vmcnt(4)
v_mfma_f32_16x16x32_bf16 a[200:203],v[108:111],a[184:187],a[200:203]
s_waitcnt vmcnt(3)
v_accvgpr_read_b32 v16,a192
v_accvgpr_read_b32 v17,a193
v_mfma_f32_16x16x32_bf16 a[204:207],v[108:111],a[188:191],a[204:207]
s_waitcnt vmcnt(2)
v_accvgpr_read_b32 v18,a194
v_accvgpr_read_b32 v19,a195
v_mfma_f32_16x16x32_bf16 a[204:207],v[104:107],a[172:175],a[204:207]
s_waitcnt vmcnt(1)
v_cvt_pk_bf16_f32 v16,v16,v17
v_cvt_pk_bf16_f32 v17,v18,v19
v_mfma_f32_16x16x32_bf16 a[204:207],v[100:103],a[156:159],a[204:207]
s_waitcnt vmcnt(0)
v_accvgpr_read_b32 v8,a196
v_accvgpr_read_b32 v9,a197
v_mfma_f32_16x16x32_bf16 a[204:207],v[96:99],a[140:143],a[204:207]
v_accvgpr_read_b32 v10,a198
v_accvgpr_read_b32 v11,a199
v_mfma_f32_16x16x32_bf16 a[208:211],v[112:115],a[128:131],a[208:211]
v_cvt_pk_bf16_f32 v8,v8,v9
v_cvt_pk_bf16_f32 v9,v10,v11
v_mfma_f32_16x16x32_bf16 a[208:211],v[116:119],a[144:147],a[208:211]
ds_read_b64_tr_b16 v[96:97],v1 offset:64
ds_read_b64_tr_b16 v[98:99],v1 offset:5184
v_mfma_f32_16x16x32_bf16 a[208:211],v[120:123],a[160:163],a[208:211]
ds_read_b64_tr_b16 v[100:101],v1 offset:10304
v_mfma_f32_16x16x32_bf16 a[208:211],v[124:127],a[176:179],a[208:211]
ds_read_b64_tr_b16 v[102:103],v1 offset:15424
v_mfma_f32_16x16x32_bf16 a[212:215],v[124:127],a[180:183],a[212:215]
ds_read_b64_tr_b16 v[104:105],v1 offset:20544
v_mfma_f32_16x16x32_bf16 a[212:215],v[120:123],a[164:167],a[212:215]
ds_read_b64_tr_b16 v[106:107],v1 offset:25664
ds_read_b64_tr_b16 v[108:109],v1 offset:30784
v_mfma_f32_16x16x32_bf16 a[212:215],v[116:119],a[148:151],a[212:215]
ds_read_b64_tr_b16 v[110:111],v1 offset:35904
v_add_u32 v1,0xfffe2000,v1
v_mfma_f32_16x16x32_bf16 a[212:215],v[112:115],a[132:135],a[212:215]
v_accvgpr_read_b32 v20,a208
v_accvgpr_read_b32 v21,a209
v_mfma_f32_16x16x32_bf16 a[216:219],v[112:115],a[136:139],a[216:219]
v_accvgpr_read_b32 v22,a210
v_accvgpr_read_b32 v23,a211
v_mfma_f32_16x16x32_bf16 a[216:219],v[116:119],a[152:155],a[216:219]
v_cvt_pk_bf16_f32 v18,v20,v21
v_cvt_pk_bf16_f32 v19,v22,v23
v_mfma_f32_16x16x32_bf16 a[216:219],v[120:123],a[168:171],a[216:219]
buffer_store_dwordx4 v[16:19],v6,s[36:39],s74 offen sc1
v_mfma_f32_16x16x32_bf16 a[216:219],v[124:127],a[184:187],a[216:219]
v_accvgpr_read_b32 v12,a212
v_accvgpr_read_b32 v13,a213
v_mfma_f32_16x16x32_bf16 a[220:223],v[124:127],a[188:191],a[220:223]
v_accvgpr_read_b32 v14,a214
v_accvgpr_read_b32 v15,a215
v_mfma_f32_16x16x32_bf16 a[220:223],v[120:123],a[172:175],a[220:223]
v_cvt_pk_bf16_f32 v10,v12,v13
v_cvt_pk_bf16_f32 v11,v14,v15
v_mfma_f32_16x16x32_bf16 a[220:223],v[116:119],a[156:159],a[220:223]
v_accvgpr_read_b32 v16,a200
v_accvgpr_read_b32 v17,a201
v_mfma_f32_16x16x32_bf16 a[220:223],v[112:115],a[140:143],a[220:223]
v_accvgpr_read_b32 v18,a202
v_accvgpr_read_b32 v19,a203
v_mfma_f32_16x16x32_bf16 a[224:227],v[64:67],v[32:35],a[224:227]
v_accvgpr_read_b32 v20,a216
v_accvgpr_read_b32 v21,a217
v_mfma_f32_16x16x32_bf16 a[224:227],v[68:71],v[36:39],a[224:227]
v_accvgpr_read_b32 v22,a218
v_accvgpr_read_b32 v23,a219
v_mfma_f32_16x16x32_bf16 a[224:227],v[72:75],v[40:43],a[224:227]
v_cvt_pk_bf16_f32 v16,v16,v17
v_cvt_pk_bf16_f32 v17,v18,v19
v_mfma_f32_16x16x32_bf16 a[224:227],v[76:79],v[44:47],a[224:227]
v_cvt_pk_bf16_f32 v18,v20,v21
v_cvt_pk_bf16_f32 v19,v22,v23
v_mfma_f32_16x16x32_bf16 a[228:231],v[76:79],v[180:183],a[228:231]
ds_read_b64_tr_b16 v[112:113],v2 offset:96
ds_read_b64_tr_b16 v[114:115],v2 offset:5216
v_mfma_f32_16x16x32_bf16 a[228:231],v[72:75],v[164:167],a[228:231]
buffer_store_dwordx4 v[8:11],v6,s[36:39],s74 offen offset:1024 sc1
v_mfma_f32_16x16x32_bf16 a[228:231],v[68:71],v[148:151],a[228:231]
ds_read_b64_tr_b16 v[116:117],v2 offset:10336
ds_read_b64_tr_b16 v[118:119],v2 offset:15456
v_mfma_f32_16x16x32_bf16 a[228:231],v[64:67],v[132:135],a[228:231]
ds_read_b64_tr_b16 v[120:121],v2 offset:20576
v_accvgpr_read_b32 v8,a204
v_mfma_f32_16x16x32_bf16 a[232:235],v[64:67],v[196:199],a[232:235]
ds_read_b64_tr_b16 v[122:123],v2 offset:25696
v_accvgpr_read_b32 v9,a205
v_mfma_f32_16x16x32_bf16 a[232:235],v[68:71],v[212:215],a[232:235]
ds_read_b64_tr_b16 v[124:125],v2 offset:30816
v_accvgpr_read_b32 v10,a206
v_mfma_f32_16x16x32_bf16 a[232:235],v[72:75],v[228:231],a[232:235]
ds_read_b64_tr_b16 v[126:127],v2 offset:35936
v_accvgpr_read_b32 v11,a207
v_mfma_f32_16x16x32_bf16 a[232:235],v[76:79],v[244:247],a[232:235]
v_accvgpr_read_b32 v12,a220
v_accvgpr_read_b32 v13,a221
v_mfma_f32_16x16x32_bf16 a[236:239],v[76:79],v[188:191],a[236:239]
v_accvgpr_read_b32 v14,a222
v_accvgpr_read_b32 v15,a223
v_mfma_f32_16x16x32_bf16 a[236:239],v[72:75],v[172:175],a[236:239]
v_cvt_pk_bf16_f32 v8,v8,v9
v_cvt_pk_bf16_f32 v9,v10,v11
v_mfma_f32_16x16x32_bf16 a[236:239],v[68:71],v[156:159],a[236:239]
v_cvt_pk_bf16_f32 v10,v12,v13
v_cvt_pk_bf16_f32 v11,v14,v15
v_mfma_f32_16x16x32_bf16 a[236:239],v[64:67],v[140:143],a[236:239]
s_waitcnt lgkmcnt(3)
ds_read_b64_tr_b16 v[64:65],v1 offset:128
ds_read_b64_tr_b16 v[66:67],v1 offset:5248
v_mfma_f32_16x16x32_bf16 a[240:243],v[80:83],v[32:35],a[240:243]
buffer_store_dwordx4 v[16:19],v6,s[36:39],s74 offen offset:2048 sc1
v_mfma_f32_16x16x32_bf16 a[240:243],v[84:87],v[36:39],a[240:243]
ds_read_b64_tr_b16 v[68:69],v1 offset:10368
ds_read_b64_tr_b16 v[70:71],v1 offset:15488
v_mfma_f32_16x16x32_bf16 a[240:243],v[88:91],v[40:43],a[240:243]
ds_read_b64_tr_b16 v[72:73],v1 offset:20608
v_add_u32 v2,0xfffe2000,v2
v_mfma_f32_16x16x32_bf16 a[240:243],v[92:95],v[44:47],a[240:243]
ds_read_b64_tr_b16 v[74:75],v1 offset:25728
v_mfma_f32_16x16x32_bf16 a[244:247],v[92:95],v[180:183],a[244:247]
ds_read_b64_tr_b16 v[76:77],v1 offset:30848
v_mfma_f32_16x16x32_bf16 a[244:247],v[88:91],v[164:167],a[244:247]
ds_read_b64_tr_b16 v[78:79],v1 offset:35968
v_add_u32 v1,0x1e000,v1
v_mfma_f32_16x16x32_bf16 a[244:247],v[84:87],v[148:151],a[244:247]
v_mfma_f32_16x16x32_bf16 a[244:247],v[80:83],v[132:135],a[244:247]
v_mfma_f32_16x16x32_bf16 a[248:251],v[80:83],v[196:199],a[248:251]
v_mfma_f32_16x16x32_bf16 a[248:251],v[84:87],v[212:215],a[248:251]
v_mfma_f32_16x16x32_bf16 a[248:251],v[88:91],v[228:231],a[248:251]
v_mfma_f32_16x16x32_bf16 a[248:251],v[92:95],v[244:247],a[248:251]
buffer_store_dwordx4 v[8:11],v28,s[36:39],s74 offen offset:3072 sc1
v_mfma_f32_16x16x32_bf16 a[252:255],v[92:95],v[188:191],a[252:255]
ds_read_b64_tr_b16 v[92:93],v2 offset:30880
ds_read_b64_tr_b16 v[94:95],v2 offset:36000
v_mfma_f32_16x16x32_bf16 a[252:255],v[88:91],v[172:175],a[252:255]
ds_read_b64_tr_b16 v[88:89],v2 offset:20640
v_mfma_f32_16x16x32_bf16 a[252:255],v[84:87],v[156:159],a[252:255]
ds_read_b64_tr_b16 v[84:85],v2 offset:10400
v_mfma_f32_16x16x32_bf16 a[252:255],v[80:83],v[140:143],a[252:255]
ds_read_b64_tr_b16 v[80:81],v2 offset:160
v_mfma_f32_16x16x32_bf16 a[224:227],v[96:99],a[128:131],a[224:227]
ds_read_b64_tr_b16 v[82:83],v2 offset:5280
ds_read_b64_tr_b16 v[86:87],v2 offset:15520
v_mfma_f32_16x16x32_bf16 a[224:227],v[100:103],a[144:147],a[224:227]
ds_read_b64_tr_b16 v[90:91],v2 offset:25760
v_add_u32 v2,0x1e000,v2
v_mfma_f32_16x16x32_bf16 a[224:227],v[104:107],a[160:163],a[224:227]
v_mfma_f32_16x16x32_bf16 a[224:227],v[108:111],a[176:179],a[224:227]
v_mfma_f32_16x16x32_bf16 a[228:231],v[108:111],a[180:183],a[228:231]
v_mfma_f32_16x16x32_bf16 a[228:231],v[104:107],a[164:167],a[228:231]
v_mfma_f32_16x16x32_bf16 a[228:231],v[100:103],a[148:151],a[228:231]
v_mfma_f32_16x16x32_bf16 a[228:231],v[96:99],a[132:135],a[228:231]
v_mfma_f32_16x16x32_bf16 a[232:235],v[96:99],a[136:139],a[232:235]
v_mfma_f32_16x16x32_bf16 a[232:235],v[100:103],a[152:155],a[232:235]
v_mfma_f32_16x16x32_bf16 a[232:235],v[104:107],a[168:171],a[232:235]
v_mfma_f32_16x16x32_bf16 a[232:235],v[108:111],a[184:187],a[232:235]
v_accvgpr_read_b32 v16,a224
v_accvgpr_read_b32 v17,a225
v_mfma_f32_16x16x32_bf16 a[236:239],v[108:111],a[188:191],a[236:239]
v_accvgpr_read_b32 v18,a226
v_accvgpr_read_b32 v19,a227
v_mfma_f32_16x16x32_bf16 a[236:239],v[104:107],a[172:175],a[236:239]
v_cvt_pk_bf16_f32 v16,v16,v17
v_cvt_pk_bf16_f32 v17,v18,v19
v_mfma_f32_16x16x32_bf16 a[236:239],v[100:103],a[156:159],a[236:239]
v_accvgpr_read_b32 v8,a228
v_accvgpr_read_b32 v9,a229
v_mfma_f32_16x16x32_bf16 a[236:239],v[96:99],a[140:143],a[236:239]
v_accvgpr_read_b32 v10,a230
v_accvgpr_read_b32 v11,a231
v_mfma_f32_16x16x32_bf16 a[240:243],v[112:115],a[128:131],a[240:243]
v_cvt_pk_bf16_f32 v8,v8,v9
v_cvt_pk_bf16_f32 v9,v10,v11
v_mfma_f32_16x16x32_bf16 a[240:243],v[116:119],a[144:147],a[240:243]
s_waitcnt lgkmcnt(0)
ds_read_b64_tr_b16 v[96:97],v1 offset:128
ds_read_b64_tr_b16 v[98:99],v1 offset:5248
v_mfma_f32_16x16x32_bf16 a[240:243],v[120:123],a[160:163],a[240:243]
ds_read_b64_tr_b16 v[100:101],v1 offset:10368
v_mfma_f32_16x16x32_bf16 a[240:243],v[124:127],a[176:179],a[240:243]
ds_read_b64_tr_b16 v[102:103],v1 offset:15488
v_mfma_f32_16x16x32_bf16 a[244:247],v[124:127],a[180:183],a[244:247]
ds_read_b64_tr_b16 v[104:105],v1 offset:20608
v_mfma_f32_16x16x32_bf16 a[244:247],v[120:123],a[164:167],a[244:247]
ds_read_b64_tr_b16 v[106:107],v1 offset:25728
ds_read_b64_tr_b16 v[108:109],v1 offset:30848
v_mfma_f32_16x16x32_bf16 a[244:247],v[116:119],a[148:151],a[244:247]
ds_read_b64_tr_b16 v[110:111],v1 offset:35968
)A"
R"A(v_add_u32 v1,0xfffe2000,v1
v_mfma_f32_16x16x32_bf16 a[244:247],v[112:115],a[132:135],a[244:247]
v_accvgpr_read_b32 v20,a240
v_accvgpr_read_b32 v21,a241
v_mfma_f32_16x16x32_bf16 a[248:251],v[112:115],a[136:139],a[248:251]
v_accvgpr_read_b32 v22,a242
v_accvgpr_read_b32 v23,a243
v_mfma_f32_16x16x32_bf16 a[248:251],v[116:119],a[152:155],a[248:251]
v_cvt_pk_bf16_f32 v18,v20,v21
v_cvt_pk_bf16_f32 v19,v22,v23
v_mfma_f32_16x16x32_bf16 a[248:251],v[120:123],a[168:171],a[248:251]
buffer_store_dwordx4 v[16:19],v6,s[36:39],s75 offen sc1
v_mfma_f32_16x16x32_bf16 a[248:251],v[124:127],a[184:187],a[248:251]
v_accvgpr_read_b32 v12,a244
v_accvgpr_read_b32 v13,a245
v_mfma_f32_16x16x32_bf16 a[252:255],v[124:127],a[188:191],a[252:255]
v_accvgpr_read_b32 v14,a246
v_accvgpr_read_b32 v15,a247
v_mfma_f32_16x16x32_bf16 a[252:255],v[120:123],a[172:175],a[252:255]
v_cvt_pk_bf16_f32 v10,v12,v13
v_cvt_pk_bf16_f32 v11,v14,v15
v_mfma_f32_16x16x32_bf16 a[252:255],v[116:119],a[156:159],a[252:255]
v_accvgpr_read_b32 v16,a232
v_accvgpr_read_b32 v17,a233
v_mfma_f32_16x16x32_bf16 a[252:255],v[112:115],a[140:143],a[252:255]
v_accvgpr_read_b32 v18,a234
v_accvgpr_read_b32 v19,a235
v_mfma_f32_16x16x32_bf16 a[0:3],v[64:67],v[32:35],a[0:3]
v_accvgpr_read_b32 v20,a248
v_accvgpr_read_b32 v21,a249
v_mfma_f32_16x16x32_bf16 a[0:3],v[68:71],v[36:39],a[0:3]
v_accvgpr_read_b32 v22,a250
v_accvgpr_read_b32 v23,a251
v_mfma_f32_16x16x32_bf16 a[0:3],v[72:75],v[40:43],a[0:3]
v_cvt_pk_bf16_f32 v16,v16,v17
v_cvt_pk_bf16_f32 v17,v18,v19
v_mfma_f32_16x16x32_bf16 a[0:3],v[76:79],v[44:47],a[0:3]
v_cvt_pk_bf16_f32 v18,v20,v21
v_cvt_pk_bf16_f32 v19,v22,v23
v_mfma_f32_16x16x32_bf16 a[4:7],v[76:79],v[180:183],a[4:7]
ds_read_b64_tr_b16 v[112:113],v2 offset:160
ds_read_b64_tr_b16 v[114:115],v2 offset:5280
v_mfma_f32_16x16x32_bf16 a[4:7],v[72:75],v[164:167],a[4:7]
buffer_store_dwordx4 v[8:11],v6,s[36:39],s75 offen offset:1024 sc1
v_mfma_f32_16x16x32_bf16 a[4:7],v[68:71],v[148:151],a[4:7]
ds_read_b64_tr_b16 v[116:117],v2 offset:10400
ds_read_b64_tr_b16 v[118:119],v2 offset:15520
v_mfma_f32_16x16x32_bf16 a[4:7],v[64:67],v[132:135],a[4:7]
ds_read_b64_tr_b16 v[120:121],v2 offset:20640
v_accvgpr_read_b32 v8,a236
v_mfma_f32_16x16x32_bf16 a[8:11],v[64:67],v[196:199],a[8:11]
ds_read_b64_tr_b16 v[122:123],v2 offset:25760
v_accvgpr_read_b32 v9,a237
v_mfma_f32_16x16x32_bf16 a[8:11],v[68:71],v[212:215],a[8:11]
ds_read_b64_tr_b16 v[124:125],v2 offset:30880
v_accvgpr_read_b32 v10,a238
v_mfma_f32_16x16x32_bf16 a[8:11],v[72:75],v[228:231],a[8:11]
ds_read_b64_tr_b16 v[126:127],v2 offset:36000
v_accvgpr_read_b32 v11,a239
v_mfma_f32_16x16x32_bf16 a[8:11],v[76:79],v[244:247],a[8:11]
v_accvgpr_read_b32 v12,a252
v_accvgpr_read_b32 v13,a253
v_mfma_f32_16x16x32_bf16 a[12:15],v[76:79],v[188:191],a[12:15]
v_accvgpr_read_b32 v14,a254
v_accvgpr_read_b32 v15,a255
v_mfma_f32_16x16x32_bf16 a[12:15],v[72:75],v[172:175],a[12:15]
v_cvt_pk_bf16_f32 v8,v8,v9
v_cvt_pk_bf16_f32 v9,v10,v11
v_mfma_f32_16x16x32_bf16 a[12:15],v[68:71],v[156:159],a[12:15]
v_cvt_pk_bf16_f32 v10,v12,v13
v_cvt_pk_bf16_f32 v11,v14,v15
v_mfma_f32_16x16x32_bf16 a[12:15],v[64:67],v[140:143],a[12:15]
ds_read_b64_tr_b16 v[64:65],v1 offset:192
ds_read_b64_tr_b16 v[66:67],v1 offset:5312
v_mfma_f32_16x16x32_bf16 a[16:19],v[80:83],v[32:35],a[16:19]
buffer_store_dwordx4 v[16:19],v6,s[36:39],s75 offen offset:2048 sc1
v_mfma_f32_16x16x32_bf16 a[16:19],v[84:87],v[36:39],a[16:19]
ds_read_b64_tr_b16 v[68:69],v1 offset:10432
ds_read_b64_tr_b16 v[70:71],v1 offset:15552
v_mfma_f32_16x16x32_bf16 a[16:19],v[88:91],v[40:43],a[16:19]
ds_read_b64_tr_b16 v[72:73],v1 offset:20672
v_add_u32 v2,0xfffe2000,v2
v_mfma_f32_16x16x32_bf16 a[16:19],v[92:95],v[44:47],a[16:19]
ds_read_b64_tr_b16 v[74:75],v1 offset:25792
v_mfma_f32_16x16x32_bf16 a[20:23],v[92:95],v[180:183],a[20:23]
ds_read_b64_tr_b16 v[76:77],v1 offset:30912
v_mfma_f32_16x16x32_bf16 a[20:23],v[88:91],v[164:167],a[20:23]
ds_read_b64_tr_b16 v[78:79],v1 offset:36032
v_add_u32 v1,0x1e000,v1
v_mfma_f32_16x16x32_bf16 a[20:23],v[84:87],v[148:151],a[20:23]
v_mfma_f32_16x16x32_bf16 a[20:23],v[80:83],v[132:135],a[20:23]
v_mfma_f32_16x16x32_bf16 a[24:27],v[80:83],v[196:199],a[24:27]
v_mfma_f32_16x16x32_bf16 a[24:27],v[84:87],v[212:215],a[24:27]
v_mfma_f32_16x16x32_bf16 a[24:27],v[88:91],v[228:231],a[24:27]
v_mfma_f32_16x16x32_bf16 a[24:27],v[92:95],v[244:247],a[24:27]
buffer_store_dwordx4 v[8:11],v28,s[36:39],s75 offen offset:3072 sc1
v_mfma_f32_16x16x32_bf16 a[28:31],v[92:95],v[188:191],a[28:31]
ds_read_b64_tr_b16 v[92:93],v2 offset:30944
ds_read_b64_tr_b16 v[94:95],v2 offset:36064
v_mfma_f32_16x16x32_bf16 a[28:31],v[88:91],v[172:175],a[28:31]
ds_read_b64_tr_b16 v[88:89],v2 offset:20704
v_mfma_f32_16x16x32_bf16 a[28:31],v[84:87],v[156:159],a[28:31]
ds_read_b64_tr_b16 v[84:85],v2 offset:10464
v_mfma_f32_16x16x32_bf16 a[28:31],v[80:83],v[140:143],a[28:31]
s_waitcnt lgkmcnt(4)
ds_read_b64_tr_b16 v[80:81],v2 offset:224
v_mfma_f32_16x16x32_bf16 a[0:3],v[96:99],a[128:131],a[0:3]
ds_read_b64_tr_b16 v[82:83],v2 offset:5344
ds_read_b64_tr_b16 v[86:87],v2 offset:15584
v_mfma_f32_16x16x32_bf16 a[0:3],v[100:103],a[144:147],a[0:3]
ds_read_b64_tr_b16 v[90:91],v2 offset:25824
v_add_u32 v2,0x1e000,v2
v_mfma_f32_16x16x32_bf16 a[0:3],v[104:107],a[160:163],a[0:3]
v_mfma_f32_16x16x32_bf16 a[0:3],v[108:111],a[176:179],a[0:3]
v_mfma_f32_16x16x32_bf16 a[4:7],v[108:111],a[180:183],a[4:7]
v_mfma_f32_16x16x32_bf16 a[4:7],v[104:107],a[164:167],a[4:7]
v_mfma_f32_16x16x32_bf16 a[4:7],v[100:103],a[148:151],a[4:7]
v_mfma_f32_16x16x32_bf16 a[4:7],v[96:99],a[132:135],a[4:7]
v_mfma_f32_16x16x32_bf16 a[8:11],v[96:99],a[136:139],a[8:11]
v_mfma_f32_16x16x32_bf16 a[8:11],v[100:103],a[152:155],a[8:11]
v_mfma_f32_16x16x32_bf16 a[8:11],v[104:107],a[168:171],a[8:11]
v_mfma_f32_16x16x32_bf16 a[8:11],v[108:111],a[184:187],a[8:11]
v_accvgpr_read_b32 v16,a0
v_accvgpr_read_b32 v17,a1
v_mfma_f32_16x16x32_bf16 a[12:15],v[108:111],a[188:191],a[12:15]
v_accvgpr_read_b32 v18,a2
v_accvgpr_read_b32 v19,a3
v_mfma_f32_16x16x32_bf16 a[12:15],v[104:107],a[172:175],a[12:15]
v_cvt_pk_bf16_f32 v16,v16,v17
v_cvt_pk_bf16_f32 v17,v18,v19
v_mfma_f32_16x16x32_bf16 a[12:15],v[100:103],a[156:159],a[12:15]
v_accvgpr_read_b32 v8,a4
v_accvgpr_read_b32 v9,a5
v_mfma_f32_16x16x32_bf16 a[12:15],v[96:99],a[140:143],a[12:15]
v_accvgpr_read_b32 v10,a6
v_accvgpr_read_b32 v11,a7
v_mfma_f32_16x16x32_bf16 a[16:19],v[112:115],a[128:131],a[16:19]
v_cvt_pk_bf16_f32 v8,v8,v9
v_cvt_pk_bf16_f32 v9,v10,v11
v_mfma_f32_16x16x32_bf16 a[16:19],v[116:119],a[144:147],a[16:19]
ds_read_b64_tr_b16 v[96:97],v1 offset:192
ds_read_b64_tr_b16 v[98:99],v1 offset:5312
v_mfma_f32_16x16x32_bf16 a[16:19],v[120:123],a[160:163],a[16:19]
ds_read_b64_tr_b16 v[100:101],v1 offset:10432
v_mfma_f32_16x16x32_bf16 a[16:19],v[124:127],a[176:179],a[16:19]
ds_read_b64_tr_b16 v[102:103],v1 offset:15552
v_mfma_f32_16x16x32_bf16 a[20:23],v[124:127],a[180:183],a[20:23]
ds_read_b64_tr_b16 v[104:105],v1 offset:20672
v_mfma_f32_16x16x32_bf16 a[20:23],v[120:123],a[164:167],a[20:23]
ds_read_b64_tr_b16 v[106:107],v1 offset:25792
ds_read_b64_tr_b16 v[108:109],v1 offset:30912
v_mfma_f32_16x16x32_bf16 a[20:23],v[116:119],a[148:151],a[20:23]
ds_read_b64_tr_b16 v[110:111],v1 offset:36032
v_add_u32 v1,0xfffec000,v1
v_mfma_f32_16x16x32_bf16 a[20:23],v[112:115],a[132:135],a[20:23]
v_accvgpr_read_b32 v20,a16
v_accvgpr_read_b32 v21,a17
v_mfma_f32_16x16x32_bf16 a[24:27],v[112:115],a[136:139],a[24:27]
v_accvgpr_read_b32 v22,a18
v_accvgpr_read_b32 v23,a19
v_mfma_f32_16x16x32_bf16 a[24:27],v[116:119],a[152:155],a[24:27]
v_cvt_pk_bf16_f32 v18,v20,v21
v_cvt_pk_bf16_f32 v19,v22,v23
v_mfma_f32_16x16x32_bf16 a[24:27],v[120:123],a[168:171],a[24:27]
buffer_store_dwordx4 v[16:19],v6,s[36:39],s76 offen sc1
v_mfma_f32_16x16x32_bf16 a[24:27],v[124:127],a[184:187],a[24:27]
v_accvgpr_read_b32 v12,a20
v_accvgpr_read_b32 v13,a21
v_mfma_f32_16x16x32_bf16 a[28:31],v[124:127],a[188:191],a[28:31]
v_accvgpr_read_b32 v14,a22
v_accvgpr_read_b32 v15,a23
v_mfma_f32_16x16x32_bf16 a[28:31],v[120:123],a[172:175],a[28:31]
v_cvt_pk_bf16_f32 v10,v12,v13
v_cvt_pk_bf16_f32 v11,v14,v15
v_mfma_f32_16x16x32_bf16 a[28:31],v[116:119],a[156:159],a[28:31]
v_accvgpr_read_b32 v16,a8
v_accvgpr_read_b32 v17,a9
v_mfma_f32_16x16x32_bf16 a[28:31],v[112:115],a[140:143],a[28:31]
v_accvgpr_read_b32 v18,a10
v_accvgpr_read_b32 v19,a11
s_nop 0
L_pd18_%=:
s_cbranch_execz L_trtg2F_%=
v_mfma_f32_16x16x32_bf16 v[128:131],v[64:67],v[32:35],v[128:131]
v_accvgpr_read_b32 v20,a24
v_accvgpr_read_b32 v21,a25
v_mfma_f32_16x16x32_bf16 v[128:131],v[68:71],v[36:39],v[128:131]
v_accvgpr_read_b32 v22,a26
v_accvgpr_read_b32 v23,a27
v_mfma_f32_16x16x32_bf16 v[128:131],v[72:75],v[40:43],v[128:131]
v_cvt_pk_bf16_f32 v16,v16,v17
v_cvt_pk_bf16_f32 v17,v18,v19
v_mfma_f32_16x16x32_bf16 v[128:131],v[76:79],v[44:47],v[128:131]
v_cvt_pk_bf16_f32 v18,v20,v21
v_cvt_pk_bf16_f32 v19,v22,v23
v_mfma_f32_16x16x32_bf16 v[136:139],v[76:79],v[180:183],v[136:139]
ds_read_b64_tr_b16 v[112:113],v2 offset:224
ds_read_b64_tr_b16 v[114:115],v2 offset:5344
v_mfma_f32_16x16x32_bf16 v[136:139],v[72:75],v[164:167],v[136:139]
buffer_store_dwordx4 v[8:11],v6,s[36:39],s76 offen offset:1024 sc1
v_mfma_f32_16x16x32_bf16 v[136:139],v[68:71],v[148:151],v[136:139]
ds_read_b64_tr_b16 v[116:117],v2 offset:10464
ds_read_b64_tr_b16 v[118:119],v2 offset:15584
v_mfma_f32_16x16x32_bf16 v[136:139],v[64:67],v[132:135],v[136:139]
ds_read_b64_tr_b16 v[120:121],v2 offset:20704
v_accvgpr_read_b32 v8,a12
v_mfma_f32_16x16x32_bf16 v[144:147],v[64:67],v[196:199],v[144:147]
ds_read_b64_tr_b16 v[122:123],v2 offset:25824
v_accvgpr_read_b32 v9,a13
v_mfma_f32_16x16x32_bf16 v[144:147],v[68:71],v[212:215],v[144:147]
ds_read_b64_tr_b16 v[124:125],v2 offset:30944
v_accvgpr_read_b32 v10,a14
v_mfma_f32_16x16x32_bf16 v[144:147],v[72:75],v[228:231],v[144:147]
ds_read_b64_tr_b16 v[126:127],v2 offset:36064
v_accvgpr_read_b32 v11,a15
v_mfma_f32_16x16x32_bf16 v[144:147],v[76:79],v[244:247],v[144:147]
v_accvgpr_read_b32 v12,a28
v_accvgpr_read_b32 v13,a29
v_mfma_f32_16x16x32_bf16 v[152:155],v[76:79],v[188:191],v[152:155]
v_accvgpr_read_b32 v14,a30
v_accvgpr_read_b32 v15,a31
v_mfma_f32_16x16x32_bf16 v[152:155],v[72:75],v[172:175],v[152:155]
v_cvt_pk_bf16_f32 v8,v8,v9
v_cvt_pk_bf16_f32 v9,v10,v11
v_mfma_f32_16x16x32_bf16 v[152:155],v[68:71],v[156:159],v[152:155]
v_cvt_pk_bf16_f32 v10,v12,v13
v_cvt_pk_bf16_f32 v11,v14,v15
v_mfma_f32_16x16x32_bf16 v[152:155],v[64:67],v[140:143],v[152:155]
s_waitcnt lgkmcnt(3)
ds_read_b64_tr_b16 v[64:65],v1 offset:256
ds_read_b64_tr_b16 v[66:67],v1 offset:5376
v_mfma_f32_16x16x32_bf16 v[160:163],v[80:83],v[32:35],v[160:163]
buffer_store_dwordx4 v[16:19],v6,s[36:39],s76 offen offset:2048 sc1
v_mfma_f32_16x16x32_bf16 v[160:163],v[84:87],v[36:39],v[160:163]
ds_read_b64_tr_b16 v[68:69],v1 offset:10496
ds_read_b64_tr_b16 v[70:71],v1 offset:15616
v_mfma_f32_16x16x32_bf16 v[160:163],v[88:91],v[40:43],v[160:163]
ds_read_b64_tr_b16 v[72:73],v1 offset:20736
v_add_u32 v2,0xfffec000,v2
v_mfma_f32_16x16x32_bf16 v[160:163],v[92:95],v[44:47],v[160:163]
ds_read_b64_tr_b16 v[74:75],v1 offset:25856
v_mov_b32 v20,s52
v_mfma_f32_16x16x32_bf16 v[168:171],v[92:95],v[180:183],v[168:171]
ds_read_b64_tr_b16 v[76:77],v1 offset:30976
v_mov_b32 v21,s53
v_mfma_f32_16x16x32_bf16 v[168:171],v[88:91],v[164:167],v[168:171]
ds_read_b64_tr_b16 v[78:79],v1 offset:36096
v_add_u32 v1,0xffff6000,v1
v_mfma_f32_16x16x32_bf16 v[168:171],v[84:87],v[148:151],v[168:171]
v_lshlrev_b32 v22,20,v6
v_mfma_f32_16x16x32_bf16 v[168:171],v[80:83],v[132:135],v[168:171]
v_mfma_f32_16x16x32_bf16 v[176:179],v[80:83],v[196:199],v[176:179]
v_mfma_f32_16x16x32_bf16 v[176:179],v[84:87],v[212:215],v[176:179]
v_mfma_f32_16x16x32_bf16 v[176:179],v[88:91],v[228:231],v[176:179]
v_mfma_f32_16x16x32_bf16 v[176:179],v[92:95],v[244:247],v[176:179]
buffer_store_dwordx4 v[8:11],v28,s[36:39],s76 offen offset:3072 sc1
v_mfma_f32_16x16x32_bf16 v[184:187],v[92:95],v[188:191],v[184:187]
ds_read_b64_tr_b16 v[92:93],v2 offset:31008
ds_read_b64_tr_b16 v[94:95],v2 offset:36128
v_mfma_f32_16x16x32_bf16 v[184:187],v[88:91],v[172:175],v[184:187]
)A"
R"A(ds_read_b64_tr_b16 v[88:89],v2 offset:20768
v_mfma_f32_16x16x32_bf16 v[184:187],v[84:87],v[156:159],v[184:187]
ds_read_b64_tr_b16 v[84:85],v2 offset:10528
v_mfma_f32_16x16x32_bf16 v[184:187],v[80:83],v[140:143],v[184:187]
ds_read_b64_tr_b16 v[80:81],v2 offset:288
v_mfma_f32_16x16x32_bf16 v[128:131],v[96:99],a[128:131],v[128:131]
ds_read_b64_tr_b16 v[82:83],v2 offset:5408
ds_read_b64_tr_b16 v[86:87],v2 offset:15648
v_mfma_f32_16x16x32_bf16 v[128:131],v[100:103],a[144:147],v[128:131]
ds_read_b64_tr_b16 v[90:91],v2 offset:25888
v_add_u32 v2,0xffff6000,v2
v_mfma_f32_16x16x32_bf16 v[128:131],v[104:107],a[160:163],v[128:131]
v_mfma_f32_16x16x32_bf16 v[128:131],v[108:111],a[176:179],v[128:131]
v_mfma_f32_16x16x32_bf16 v[136:139],v[108:111],a[180:183],v[136:139]
v_mfma_f32_16x16x32_bf16 v[136:139],v[104:107],a[164:167],v[136:139]
v_mfma_f32_16x16x32_bf16 v[136:139],v[100:103],a[148:151],v[136:139]
v_mfma_f32_16x16x32_bf16 v[136:139],v[96:99],a[132:135],v[136:139]
v_mfma_f32_16x16x32_bf16 v[144:147],v[96:99],a[136:139],v[144:147]
v_mfma_f32_16x16x32_bf16 v[144:147],v[100:103],a[152:155],v[144:147]
v_mfma_f32_16x16x32_bf16 v[144:147],v[104:107],a[168:171],v[144:147]
v_mfma_f32_16x16x32_bf16 v[144:147],v[108:111],a[184:187],v[144:147]
v_cvt_pk_bf16_f32 v128,v128,v129
v_cvt_pk_bf16_f32 v129,v130,v131
v_mfma_f32_16x16x32_bf16 v[152:155],v[108:111],a[188:191],v[152:155]
ds_read_b64_tr_b16 v[108:109],v1 offset:30976
ds_read_b64_tr_b16 v[110:111],v1 offset:36096
v_mfma_f32_16x16x32_bf16 v[152:155],v[104:107],a[172:175],v[152:155]
v_cvt_pk_bf16_f32 v136,v136,v137
v_cvt_pk_bf16_f32 v137,v138,v139
v_mfma_f32_16x16x32_bf16 v[152:155],v[100:103],a[156:159],v[152:155]
v_cvt_pk_bf16_f32 v144,v144,v145
v_cvt_pk_bf16_f32 v145,v146,v147
v_mfma_f32_16x16x32_bf16 v[152:155],v[96:99],a[140:143],v[152:155]
ds_read_b64_tr_b16 v[96:97],v1 offset:256
ds_read_b64_tr_b16 v[98:99],v1 offset:5376
v_mfma_f32_16x16x32_bf16 v[160:163],v[112:115],a[128:131],v[160:163]
ds_read_b64_tr_b16 v[100:101],v1 offset:10496
v_mfma_f32_16x16x32_bf16 v[160:163],v[116:119],a[144:147],v[160:163]
s_waitcnt lgkmcnt(5)
ds_read_b64_tr_b16 v[102:103],v1 offset:15616
v_mfma_f32_16x16x32_bf16 v[160:163],v[120:123],a[160:163],v[160:163]
ds_read_b64_tr_b16 v[104:105],v1 offset:20736
v_mfma_f32_16x16x32_bf16 v[160:163],v[124:127],a[176:179],v[160:163]
v_cvt_pk_bf16_f32 v152,v152,v153
v_cvt_pk_bf16_f32 v153,v154,v155
v_mfma_f32_16x16x32_bf16 v[168:171],v[124:127],a[180:183],v[168:171]
ds_read_b64_tr_b16 v[106:107],v1 offset:25856
v_add_u32 v1,0x1e000,v1
v_mfma_f32_16x16x32_bf16 v[168:171],v[120:123],a[164:167],v[168:171]
v_mfma_f32_16x16x32_bf16 v[168:171],v[116:119],a[148:151],v[168:171]
v_mfma_f32_16x16x32_bf16 v[168:171],v[112:115],a[132:135],v[168:171]
v_cvt_pk_bf16_f32 v130,v160,v161
v_cvt_pk_bf16_f32 v131,v162,v163
v_mfma_f32_16x16x32_bf16 v[176:179],v[112:115],a[136:139],v[176:179]
buffer_store_dwordx4 v[128:131],v6,s[36:39],s79 offen sc1
v_mfma_f32_16x16x32_bf16 v[176:179],v[116:119],a[152:155],v[176:179]
v_mfma_f32_16x16x32_bf16 v[176:179],v[120:123],a[168:171],v[176:179]
v_mfma_f32_16x16x32_bf16 v[176:179],v[124:127],a[184:187],v[176:179]
v_mfma_f32_16x16x32_bf16 v[184:187],v[124:127],a[188:191],v[184:187]
v_cvt_pk_bf16_f32 v138,v168,v169
v_cvt_pk_bf16_f32 v139,v170,v171
v_mfma_f32_16x16x32_bf16 v[184:187],v[120:123],a[172:175],v[184:187]
buffer_store_dwordx4 v[136:139],v6,s[36:39],s80 offen sc1
v_mfma_f32_16x16x32_bf16 v[184:187],v[116:119],a[156:159],v[184:187]
ds_read_b64_tr_b16 v[116:117],v2 offset:10528
ds_read_b64_tr_b16 v[118:119],v2 offset:15648
v_mfma_f32_16x16x32_bf16 v[184:187],v[112:115],a[140:143],v[184:187]
v_cvt_pk_bf16_f32 v146,v176,v177
v_cvt_pk_bf16_f32 v147,v178,v179
s_waitcnt vmcnt(6)
s_barrier
v_mfma_f32_16x16x32_bf16 v[192:195],v[64:67],a[68:71],v[192:195]
buffer_store_dwordx4 v[144:147],v6,s[36:39],s81 offen sc1
v_mfma_f32_16x16x32_bf16 v[192:195],v[68:71],a[76:79],v[192:195]
ds_read_b64_tr_b16 v[112:113],v2 offset:288
ds_read_b64_tr_b16 v[114:115],v2 offset:5408
v_mfma_f32_16x16x32_bf16 v[192:195],v[72:75],a[84:87],v[192:195]
buffer_store_dwordx2 v[58:59],v60,s[44:47],s18 offen offset:2312 sc1
v_mfma_f32_16x16x32_bf16 v[192:195],v[76:79],a[92:95],v[192:195]
v_cvt_pk_bf16_f32 v154,v184,v185
v_cvt_pk_bf16_f32 v155,v186,v187
v_mfma_f32_16x16x32_bf16 v[200:203],v[76:79],a[124:127],v[200:203]
buffer_store_dwordx4 v[152:155],v28,s[36:39],s82 offen sc1
v_mfma_f32_16x16x32_bf16 v[200:203],v[72:75],a[116:119],v[200:203]
ds_read_b64_tr_b16 v[120:121],v2 offset:20768
ds_read_b64_tr_b16 v[122:123],v2 offset:25888
v_mfma_f32_16x16x32_bf16 v[200:203],v[68:71],a[108:111],v[200:203]
ds_read_b64_tr_b16 v[124:125],v2 offset:31008
v_mfma_f32_16x16x32_bf16 v[200:203],v[64:67],a[100:103],v[200:203]
ds_read_b64_tr_b16 v[126:127],v2 offset:36128
v_add_u32 v2,0x1e000,v2
v_mfma_f32_16x16x32_bf16 v[208:211],v[64:67],a[64:67],v[208:211]
v_mfma_f32_16x16x32_bf16 v[208:211],v[68:71],a[72:75],v[208:211]
v_mfma_f32_16x16x32_bf16 v[208:211],v[72:75],a[80:83],v[208:211]
v_mfma_f32_16x16x32_bf16 v[208:211],v[76:79],a[88:91],v[208:211]
v_mfma_f32_16x16x32_bf16 v[216:219],v[76:79],a[120:123],v[216:219]
ds_read_b64_tr_b16 v[76:77],v1 offset:30976
ds_read_b64_tr_b16 v[78:79],v1 offset:36096
v_mfma_f32_16x16x32_bf16 v[216:219],v[72:75],a[112:115],v[216:219]
ds_read_b64_tr_b16 v[72:73],v1 offset:20736
v_mfma_f32_16x16x32_bf16 v[216:219],v[68:71],a[104:107],v[216:219]
ds_read_b64_tr_b16 v[68:69],v1 offset:10496
v_mfma_f32_16x16x32_bf16 v[216:219],v[64:67],a[96:99],v[216:219]
ds_read_b64_tr_b16 v[64:65],v1 offset:256
v_mfma_f32_16x16x32_bf16 v[224:227],v[80:83],a[68:71],v[224:227]
ds_read_b64_tr_b16 v[66:67],v1 offset:5376
ds_read_b64_tr_b16 v[70:71],v1 offset:15616
v_mfma_f32_16x16x32_bf16 v[224:227],v[84:87],a[76:79],v[224:227]
ds_read_b64_tr_b16 v[74:75],v1 offset:25856
v_mfma_f32_16x16x32_bf16 v[224:227],v[88:91],a[84:87],v[224:227]
v_mfma_f32_16x16x32_bf16 v[224:227],v[92:95],a[92:95],v[224:227]
v_mfma_f32_16x16x32_bf16 v[232:235],v[92:95],a[124:127],v[232:235]
v_mfma_f32_16x16x32_bf16 v[232:235],v[88:91],a[116:119],v[232:235]
v_mfma_f32_16x16x32_bf16 v[232:235],v[84:87],a[108:111],v[232:235]
v_mfma_f32_16x16x32_bf16 v[232:235],v[80:83],a[100:103],v[232:235]
v_mfma_f32_16x16x32_bf16 v[240:243],v[80:83],a[64:67],v[240:243]
v_mfma_f32_16x16x32_bf16 v[240:243],v[84:87],a[72:75],v[240:243]
v_mfma_f32_16x16x32_bf16 v[240:243],v[88:91],a[80:83],v[240:243]
v_mfma_f32_16x16x32_bf16 v[240:243],v[92:95],a[88:91],v[240:243]
v_mfma_f32_16x16x32_bf16 v[248:251],v[92:95],a[120:123],v[248:251]
ds_read_b64_tr_b16 v[92:93],v2 offset:31008
ds_read_b64_tr_b16 v[94:95],v2 offset:36128
v_mfma_f32_16x16x32_bf16 v[248:251],v[88:91],a[112:115],v[248:251]
ds_read_b64_tr_b16 v[88:89],v2 offset:20768
v_mfma_f32_16x16x32_bf16 v[248:251],v[84:87],a[104:107],v[248:251]
ds_read_b64_tr_b16 v[84:85],v2 offset:10528
v_mfma_f32_16x16x32_bf16 v[248:251],v[80:83],a[96:99],v[248:251]
s_waitcnt lgkmcnt(4)
ds_read_b64_tr_b16 v[80:81],v2 offset:288
v_mfma_f32_16x16x32_bf16 v[192:195],v[96:99],v[32:35],v[192:195]
ds_read_b64_tr_b16 v[82:83],v2 offset:5408
ds_read_b64_tr_b16 v[86:87],v2 offset:15648
v_mfma_f32_16x16x32_bf16 v[192:195],v[100:103],v[36:39],v[192:195]
ds_read_b64_tr_b16 v[90:91],v2 offset:25888
v_mfma_f32_16x16x32_bf16 v[192:195],v[104:107],v[40:43],v[192:195]
v_mfma_f32_16x16x32_bf16 v[192:195],v[108:111],v[44:47],v[192:195]
v_mfma_f32_16x16x32_bf16 v[200:203],v[108:111],v[180:183],v[200:203]
v_mfma_f32_16x16x32_bf16 v[200:203],v[104:107],v[164:167],v[200:203]
v_mfma_f32_16x16x32_bf16 v[200:203],v[100:103],v[148:151],v[200:203]
v_mfma_f32_16x16x32_bf16 v[200:203],v[96:99],v[132:135],v[200:203]
v_mfma_f32_16x16x32_bf16 v[208:211],v[96:99],v[196:199],v[208:211]
v_mfma_f32_16x16x32_bf16 v[208:211],v[100:103],v[212:215],v[208:211]
v_mfma_f32_16x16x32_bf16 v[208:211],v[104:107],v[228:231],v[208:211]
v_mfma_f32_16x16x32_bf16 v[208:211],v[108:111],v[244:247],v[208:211]
v_mfma_f32_16x16x32_bf16 v[216:219],v[108:111],v[188:191],v[216:219]
v_mfma_f32_16x16x32_bf16 v[216:219],v[104:107],v[172:175],v[216:219]
v_mfma_f32_16x16x32_bf16 v[216:219],v[100:103],v[156:159],v[216:219]
v_mfma_f32_16x16x32_bf16 v[216:219],v[96:99],v[140:143],v[216:219]
v_mfma_f32_16x16x32_bf16 v[224:227],v[112:115],v[32:35],v[224:227]
v_mfma_f32_16x16x32_bf16 v[224:227],v[116:119],v[36:39],v[224:227]
v_mfma_f32_16x16x32_bf16 v[224:227],v[120:123],v[40:43],v[224:227]
v_mfma_f32_16x16x32_bf16 v[224:227],v[124:127],v[44:47],v[224:227]
v_mfma_f32_16x16x32_bf16 v[232:235],v[124:127],v[180:183],v[232:235]
v_mfma_f32_16x16x32_bf16 v[232:235],v[120:123],v[164:167],v[232:235]
v_mfma_f32_16x16x32_bf16 v[232:235],v[116:119],v[148:151],v[232:235]
v_mfma_f32_16x16x32_bf16 v[232:235],v[112:115],v[132:135],v[232:235]
v_mfma_f32_16x16x32_bf16 v[240:243],v[112:115],v[196:199],v[240:243]
v_mfma_f32_16x16x32_bf16 v[240:243],v[116:119],v[212:215],v[240:243]
v_mfma_f32_16x16x32_bf16 v[240:243],v[120:123],v[228:231],v[240:243]
v_mfma_f32_16x16x32_bf16 v[240:243],v[124:127],v[244:247],v[240:243]
v_mfma_f32_16x16x32_bf16 v[248:251],v[124:127],v[188:191],v[248:251]
v_mfma_f32_16x16x32_bf16 v[248:251],v[120:123],v[172:175],v[248:251]
v_mfma_f32_16x16x32_bf16 v[248:251],v[116:119],v[156:159],v[248:251]
v_mfma_f32_16x16x32_bf16 v[248:251],v[112:115],v[140:143],v[248:251]
s_waitcnt vmcnt(0)
s_barrier
v_mfma_f32_16x16x32_bf16 v[192:195],v[64:67],a[128:131],v[192:195]
buffer_store_dwordx2 v[58:59],v60,s[44:47],s18 offen offset:2304 sc1
v_mfma_f32_16x16x32_bf16 v[192:195],v[68:71],a[144:147],v[192:195]
v_mfma_f32_16x16x32_bf16 v[192:195],v[72:75],a[160:163],v[192:195]
v_mfma_f32_16x16x32_bf16 v[192:195],v[76:79],a[176:179],v[192:195]
v_mfma_f32_16x16x32_bf16 v[200:203],v[76:79],a[180:183],v[200:203]
v_mfma_f32_16x16x32_bf16 v[200:203],v[72:75],a[164:167],v[200:203]
v_mfma_f32_16x16x32_bf16 v[200:203],v[68:71],a[148:151],v[200:203]
v_mfma_f32_16x16x32_bf16 v[200:203],v[64:67],a[132:135],v[200:203]
v_mfma_f32_16x16x32_bf16 v[208:211],v[64:67],a[136:139],v[208:211]
v_mfma_f32_16x16x32_bf16 v[208:211],v[68:71],a[152:155],v[208:211]
v_mfma_f32_16x16x32_bf16 v[208:211],v[72:75],a[168:171],v[208:211]
v_mfma_f32_16x16x32_bf16 v[208:211],v[76:79],a[184:187],v[208:211]
v_mfma_f32_16x16x32_bf16 v[216:219],v[76:79],a[188:191],v[216:219]
v_mfma_f32_16x16x32_bf16 v[216:219],v[72:75],a[172:175],v[216:219]
v_mfma_f32_16x16x32_bf16 v[216:219],v[68:71],a[156:159],v[216:219]
v_mfma_f32_16x16x32_bf16 v[216:219],v[64:67],a[140:143],v[216:219]
s_waitcnt lgkmcnt(0)
v_mfma_f32_16x16x32_bf16 v[224:227],v[80:83],a[128:131],v[224:227]
buffer_load_dwordx2 v[62:63],v61,s[44:47],s91 offen offset:2312 sc1
v_mfma_f32_16x16x32_bf16 v[224:227],v[84:87],a[144:147],v[224:227]
v_mfma_f32_16x16x32_bf16 v[224:227],v[88:91],a[160:163],v[224:227]
v_mfma_f32_16x16x32_bf16 v[224:227],v[92:95],a[176:179],v[224:227]
v_mfma_f32_16x16x32_bf16 v[232:235],v[92:95],a[180:183],v[232:235]
v_mfma_f32_16x16x32_bf16 v[232:235],v[88:91],a[164:167],v[232:235]
v_mfma_f32_16x16x32_bf16 v[232:235],v[84:87],a[148:151],v[232:235]
v_mfma_f32_16x16x32_bf16 v[232:235],v[80:83],a[132:135],v[232:235]
v_mfma_f32_16x16x32_bf16 v[240:243],v[80:83],a[136:139],v[240:243]
v_mfma_f32_16x16x32_bf16 v[240:243],v[84:87],a[152:155],v[240:243]
v_mfma_f32_16x16x32_bf16 v[240:243],v[88:91],a[168:171],v[240:243]
v_mfma_f32_16x16x32_bf16 v[240:243],v[92:95],a[184:187],v[240:243]
v_mfma_f32_16x16x32_bf16 v[248:251],v[92:95],a[188:191],v[248:251]
v_mfma_f32_16x16x32_bf16 v[248:251],v[88:91],a[172:175],v[248:251]
v_mfma_f32_16x16x32_bf16 v[248:251],v[84:87],a[156:159],v[248:251]
v_mfma_f32_16x16x32_bf16 v[248:251],v[80:83],a[140:143],v[248:251]
s_waitcnt vmcnt(0)
v_cmp_ne_u32 s[56:57],s52,v62
v_cmp_ne_u32 s[58:59],s53,v63
s_nop 0
s_or_b64 s[56:57],s[56:57],s[58:59]
s_cbranch_scc1 L_fr4_%=
L_fo4_%=:
buffer_load_dwordx4 v[64:67],v6,s[36:39],s66 offen sc1
buffer_load_dwordx4 v[68:71],v6,s[36:39],s67 offen sc1
buffer_load_dwordx4 v[72:75],v6,s[36:39],s66 offen offset:1024 sc1
buffer_load_dwordx4 v[76:79],v6,s[36:39],s67 offen offset:1024 sc1
)A"
R"A(buffer_load_dwordx4 v[80:83],v6,s[36:39],s66 offen offset:2048 sc1
buffer_load_dwordx4 v[84:87],v6,s[36:39],s67 offen offset:2048 sc1
buffer_load_dwordx4 v[88:91],v28,s[36:39],s66 offen offset:3072 sc1
buffer_load_dwordx4 v[92:95],v28,s[36:39],s67 offen offset:3072 sc1
buffer_load_dwordx2 v[4:5],v3,s[44:47],s91 offen offset:2304 sc1
s_waitcnt vmcnt(8)
v_mfma_f32_16x16x32_bf16 v[192:195],v[48:51],v[64:67],v[192:195]
s_waitcnt vmcnt(7)
v_mfma_f32_16x16x32_bf16 v[192:195],v[48:51],v[68:71],v[192:195]
v_mfma_f32_16x16x32_bf16 v[224:227],v[52:55],v[64:67],v[224:227]
v_mfma_f32_16x16x32_bf16 v[224:227],v[52:55],v[68:71],v[224:227]
s_waitcnt vmcnt(6)
v_mfma_f32_16x16x32_bf16 v[200:203],v[48:51],v[72:75],v[200:203]
s_waitcnt vmcnt(5)
v_mfma_f32_16x16x32_bf16 v[200:203],v[48:51],v[76:79],v[200:203]
v_mfma_f32_16x16x32_bf16 v[232:235],v[52:55],v[72:75],v[232:235]
v_mfma_f32_16x16x32_bf16 v[232:235],v[52:55],v[76:79],v[232:235]
s_waitcnt vmcnt(4)
v_mfma_f32_16x16x32_bf16 v[208:211],v[48:51],v[80:83],v[208:211]
s_waitcnt vmcnt(3)
v_mfma_f32_16x16x32_bf16 v[208:211],v[48:51],v[84:87],v[208:211]
v_mfma_f32_16x16x32_bf16 v[240:243],v[52:55],v[80:83],v[240:243]
v_mfma_f32_16x16x32_bf16 v[240:243],v[52:55],v[84:87],v[240:243]
s_waitcnt vmcnt(2)
v_mfma_f32_16x16x32_bf16 v[216:219],v[48:51],v[88:91],v[216:219]
s_waitcnt vmcnt(1)
v_mfma_f32_16x16x32_bf16 v[216:219],v[48:51],v[92:95],v[216:219]
v_mfma_f32_16x16x32_bf16 v[248:251],v[52:55],v[88:91],v[248:251]
v_mfma_f32_16x16x32_bf16 v[248:251],v[52:55],v[92:95],v[248:251]
s_waitcnt vmcnt(0)
v_cmp_ne_u32 s[56:57],s52,v4
v_cmp_ne_u32 s[58:59],s53,v5
s_nop 0
s_or_b64 s[56:57],s[56:57],s[58:59]
s_cbranch_scc1 L_fr5_%=
L_fo5_%=:
buffer_load_dwordx4 v[96:99],v6,s[36:39],s68 offen sc1
buffer_load_dwordx4 v[100:103],v6,s[36:39],s68 offen offset:1024 sc1
buffer_load_dwordx4 v[104:107],v6,s[36:39],s68 offen offset:2048 sc1
buffer_load_dwordx4 v[108:111],v6,s[36:39],s68 offen offset:3072 sc1
buffer_load_dwordx4 v[112:115],v6,s[36:39],s65 offen sc1
buffer_load_dwordx4 v[116:119],v6,s[36:39],s65 offen offset:1024 sc1
buffer_load_dwordx4 v[120:123],v6,s[36:39],s65 offen offset:2048 sc1
buffer_load_dwordx4 v[124:127],v28,s[36:39],s65 offen offset:3072 sc1
s_waitcnt vmcnt(7)
v_mfma_f32_16x16x32_bf16 v[128:131],v[48:51],v[96:99],0
s_waitcnt vmcnt(6)
v_mfma_f32_16x16x32_bf16 v[128:131],v[48:51],v[100:103],v[128:131]
s_waitcnt vmcnt(5)
v_mfma_f32_16x16x32_bf16 v[128:131],v[48:51],v[104:107],v[128:131]
s_waitcnt vmcnt(4)
v_mfma_f32_16x16x32_bf16 v[128:131],v[48:51],v[108:111],v[128:131]
v_mfma_f32_16x16x32_bf16 v[160:163],v[52:55],v[96:99],0
v_mfma_f32_16x16x32_bf16 v[160:163],v[52:55],v[100:103],v[160:163]
v_mfma_f32_16x16x32_bf16 v[160:163],v[52:55],v[104:107],v[160:163]
v_mfma_f32_16x16x32_bf16 v[160:163],v[52:55],v[108:111],v[160:163]
s_waitcnt vmcnt(3)
v_mfma_f32_16x16x32_bf16 v[192:195],v[48:51],v[112:115],v[192:195]
v_mfma_f32_16x16x32_bf16 v[224:227],v[52:55],v[112:115],v[224:227]
s_waitcnt vmcnt(2)
v_mfma_f32_16x16x32_bf16 v[200:203],v[48:51],v[116:119],v[200:203]
v_mfma_f32_16x16x32_bf16 v[232:235],v[52:55],v[116:119],v[232:235]
s_waitcnt vmcnt(1)
v_cvt_pk_bf16_f32 v128,v128,v129
v_cvt_pk_bf16_f32 v129,v130,v131
v_mfma_f32_16x16x32_bf16 v[208:211],v[48:51],v[120:123],v[208:211]
v_mfma_f32_16x16x32_bf16 v[240:243],v[52:55],v[120:123],v[240:243]
s_waitcnt vmcnt(0)
v_cvt_pk_bf16_f32 v130,v160,v161
v_cvt_pk_bf16_f32 v131,v162,v163
v_mfma_f32_16x16x32_bf16 v[216:219],v[48:51],v[124:127],v[216:219]
v_cvt_pk_bf16_f32 v192,v192,v193
v_cvt_pk_bf16_f32 v193,v194,v195
v_cvt_pk_bf16_f32 v194,v224,v225
v_cvt_pk_bf16_f32 v195,v226,v227
v_cvt_pk_bf16_f32 v200,v200,v201
v_cvt_pk_bf16_f32 v201,v202,v203
v_mfma_f32_16x16x32_bf16 v[248:251],v[52:55],v[124:127],v[248:251]
v_permlane16_swap_b32 v128,v130
v_permlane16_swap_b32 v129,v131
v_cvt_pk_bf16_f32 v202,v232,v233
v_cvt_pk_bf16_f32 v203,v234,v235
v_cvt_pk_bf16_f32 v208,v208,v209
v_cvt_pk_bf16_f32 v209,v210,v211
v_cvt_pk_bf16_f32 v210,v240,v241
v_cvt_pk_bf16_f32 v211,v242,v243
buffer_store_dwordx4 v[128:131],v56,s[24:27],s83 offen offset:256 sc1 nt
v_permlane16_swap_b32 v192,v194
v_permlane16_swap_b32 v193,v195
buffer_store_dwordx4 v[192:195],v56,s[24:27],s87 offen sc1 nt
v_permlane16_swap_b32 v200,v202
v_permlane16_swap_b32 v201,v203
v_cvt_pk_bf16_f32 v216,v216,v217
v_cvt_pk_bf16_f32 v217,v218,v219
v_cvt_pk_bf16_f32 v218,v248,v249
v_cvt_pk_bf16_f32 v219,v250,v251
buffer_store_dwordx4 v[200:203],v56,s[24:27],s88 offen sc1 nt
v_permlane16_swap_b32 v208,v210
v_permlane16_swap_b32 v209,v211
buffer_store_dwordx4 v[208:211],v56,s[24:27],s89 offen sc1 nt
v_permlane16_swap_b32 v216,v218
v_permlane16_swap_b32 v217,v219
buffer_store_dwordx4 v[216:219],v56,s[24:27],s90 offen sc1 nt
s_lshl_b32 s69,s62,20
s_add_u32 s69,s69,s18
buffer_store_dword v20,v22,s[44:47],s69 offen sc1
L_geF_%=:
s_branch L_end_%=
.p2align 12
L_pd19_%=:
s_cbranch_execz L_trt1_%=
L_GS_%=:
L_gbS_%=:
s_waitcnt lgkmcnt(14)
v_mfma_f32_16x16x32_bf16 a[192:195],v[64:67],a[68:71],0
buffer_store_dwordx4 v[32:35],v6,s[40:43],s79 offen sc1
s_waitcnt lgkmcnt(12)
v_mfma_f32_16x16x32_bf16 a[192:195],v[68:71],a[76:79],a[192:195]
s_lshl_b32 s91,s63,8
v_bfe_u32 v3,v6,4,2
v_min_u32 v3,2,v3
s_waitcnt lgkmcnt(10)
v_mfma_f32_16x16x32_bf16 a[192:195],v[72:75],a[84:87],a[192:195]
s_add_u32 s87,s64,1
v_add3_u32 v3,v3,s64,1
v_and_b32 v3,3,v3
s_waitcnt lgkmcnt(8)
v_mfma_f32_16x16x32_bf16 a[192:195],v[76:79],a[92:95],a[192:195]
s_and_b32 s87,s87,3
ds_read_b64_tr_b16 v[96:97],v1 offset:64
v_lshlrev_b32 v3,6,v3
v_mfma_f32_16x16x32_bf16 a[196:199],v[76:79],a[124:127],0
s_lshl_b32 s87,s87,1
ds_read_b64_tr_b16 v[98:99],v1 offset:5184
ds_read_b64_tr_b16 v[100:101],v1 offset:10304
v_mfma_f32_16x16x32_bf16 a[196:199],v[72:75],a[116:119],a[196:199]
s_lshl_b32 s87,s87,16
ds_read_b64_tr_b16 v[102:103],v1 offset:15424
v_lshlrev_b32 v29,10,v6
v_mfma_f32_16x16x32_bf16 a[196:199],v[68:71],a[108:111],a[196:199]
s_add_u32 s88,s87,4096
ds_read_b64_tr_b16 v[104:105],v1 offset:20544
v_accvgpr_read_b32 v16,a24
v_mfma_f32_16x16x32_bf16 a[196:199],v[64:67],a[100:103],a[196:199]
s_add_u32 s89,s87,8192
ds_read_b64_tr_b16 v[106:107],v1 offset:25664
v_accvgpr_read_b32 v17,a25
v_mfma_f32_16x16x32_bf16 a[200:203],v[64:67],a[64:67],0
s_add_u32 s90,s87,12288
ds_read_b64_tr_b16 v[108:109],v1 offset:30784
ds_read_b64_tr_b16 v[110:111],v1 offset:35904
v_mfma_f32_16x16x32_bf16 a[200:203],v[68:71],a[72:75],a[200:203]
s_add_u32 m0,s86,122880
ds_read_b64_tr_b16 v[112:113],v2 offset:96
v_accvgpr_read_b32 v18,a26
v_mfma_f32_16x16x32_bf16 a[200:203],v[72:75],a[80:83],a[200:203]
s_add_u32 s83,s64,3
ds_read_b64_tr_b16 v[114:115],v2 offset:5216
v_accvgpr_read_b32 v19,a27
v_mfma_f32_16x16x32_bf16 a[200:203],v[76:79],a[88:91],a[200:203]
s_and_b32 s83,s83,3
ds_read_b64_tr_b16 v[116:117],v2 offset:10336
v_accvgpr_read_b32 v20,a28
v_mfma_f32_16x16x32_bf16 a[204:207],v[76:79],a[120:123],0
s_lshl_b32 s83,s83,1
ds_read_b64_tr_b16 v[118:119],v2 offset:15456
ds_read_b64_tr_b16 v[120:121],v2 offset:20576
v_mfma_f32_16x16x32_bf16 a[204:207],v[72:75],a[112:115],a[204:207]
s_mul_i32 s83,s83,163840
ds_read_b64_tr_b16 v[122:123],v2 offset:25696
v_accvgpr_read_b32 v21,a29
v_mfma_f32_16x16x32_bf16 a[204:207],v[68:71],a[104:107],a[204:207]
s_add_u32 s83,s83,s85
ds_read_b64_tr_b16 v[124:125],v2 offset:30816
v_accvgpr_read_b32 v22,a30
v_mfma_f32_16x16x32_bf16 a[204:207],v[64:67],a[96:99],a[204:207]
buffer_store_dwordx4 v[36:39],v6,s[40:43],s80 offen sc1
s_waitcnt lgkmcnt(9)
v_mfma_f32_16x16x32_bf16 a[208:211],v[80:83],a[68:71],0
s_add_u32 s84,s83,20480
ds_read_b64_tr_b16 v[126:127],v2 offset:35936
ds_read_b64_tr_b16 v[64:65],v1 offset:128
v_mfma_f32_16x16x32_bf16 a[208:211],v[84:87],a[76:79],a[208:211]
s_lshl_b32 s66,s61,3
ds_read_b64_tr_b16 v[66:67],v1 offset:5248
v_accvgpr_read_b32 v23,a31
v_mfma_f32_16x16x32_bf16 a[208:211],v[88:91],a[84:87],a[208:211]
s_add_u32 s66,s66,s64
ds_read_b64_tr_b16 v[68:69],v1 offset:10368
v_mul_f32 v16,v16,v49
v_mfma_f32_16x16x32_bf16 a[208:211],v[92:95],a[92:95],a[208:211]
s_mul_i32 s66,s66,262144
ds_read_b64_tr_b16 v[70:71],v1 offset:15488
v_mul_f32 v17,v17,v49
v_mfma_f32_16x16x32_bf16 a[212:215],v[92:95],a[124:127],0
s_waitcnt vmcnt(21)
buffer_load_dwordx2 v[4:5],v3,s[44:47],s91 offen offset:256 sc1
v_mfma_f32_16x16x32_bf16 a[212:215],v[88:91],a[116:119],a[212:215]
s_add_u32 s66,s66,100663296
ds_read_b64_tr_b16 v[72:73],v1 offset:20608
ds_read_b64_tr_b16 v[74:75],v1 offset:25728
v_mfma_f32_16x16x32_bf16 a[212:215],v[84:87],a[108:111],a[212:215]
s_mov_b32 s38,262144
ds_read_b64_tr_b16 v[76:77],v1 offset:30848
v_mul_f32 v18,v18,v49
v_mfma_f32_16x16x32_bf16 a[212:215],v[80:83],a[100:103],a[212:215]
s_mov_b32 s39,0x20000
ds_read_b64_tr_b16 v[78:79],v1 offset:35968
v_mul_f32 v19,v19,v49
v_mfma_f32_16x16x32_bf16 a[216:219],v[80:83],a[64:67],0
s_add_u32 s74,s63,1
v_mul_f32 v20,v20,v49
v_mul_f32 v21,v21,v49
v_mfma_f32_16x16x32_bf16 a[216:219],v[84:87],a[72:75],a[216:219]
s_and_b32 s74,s74,3
v_mul_f32 v22,v22,v49
v_mul_f32 v23,v23,v49
v_mfma_f32_16x16x32_bf16 a[216:219],v[88:91],a[80:83],a[216:219]
s_lshl_b32 s74,s74,2
v_cvt_pk_bf16_f32 v44,v16,v17
v_cvt_pk_bf16_f32 v45,v18,v19
v_mfma_f32_16x16x32_bf16 a[216:219],v[92:95],a[88:91],a[216:219]
s_add_u32 s74,s74,s60
v_cvt_pk_bf16_f32 v46,v20,v21
v_cvt_pk_bf16_f32 v47,v22,v23
v_mfma_f32_16x16x32_bf16 a[220:223],v[92:95],a[120:123],0
s_mul_i32 s74,s74,16384
ds_read_b64_tr_b16 v[92:93],v2 offset:30880
ds_read_b64_tr_b16 v[94:95],v2 offset:36000
v_mfma_f32_16x16x32_bf16 a[220:223],v[88:91],a[112:115],a[220:223]
s_add_u32 s74,s74,8192
ds_read_b64_tr_b16 v[88:89],v2 offset:20640
v_mul_f32 v132,v132,v53
v_mfma_f32_16x16x32_bf16 a[220:223],v[84:87],a[104:107],a[220:223]
buffer_store_dwordx4 v[40:43],v6,s[40:43],s81 offen sc1
v_mfma_f32_16x16x32_bf16 a[220:223],v[80:83],a[96:99],a[220:223]
s_add_u32 s75,s63,2
ds_read_b64_tr_b16 v[80:81],v2 offset:160
ds_read_b64_tr_b16 v[82:83],v2 offset:5280
v_mfma_f32_16x16x32_bf16 a[224:227],v[96:99],a[68:71],0
s_and_b32 s75,s75,3
ds_read_b64_tr_b16 v[84:85],v2 offset:10400
v_mul_f32 v133,v133,v53
v_mfma_f32_16x16x32_bf16 a[224:227],v[100:103],a[76:79],a[224:227]
s_lshl_b32 s75,s75,2
ds_read_b64_tr_b16 v[86:87],v2 offset:15520
v_mul_f32 v134,v134,v53
v_mfma_f32_16x16x32_bf16 a[224:227],v[104:107],a[84:87],a[224:227]
s_add_u32 s75,s75,s60
ds_read_b64_tr_b16 v[90:91],v2 offset:25760
v_mul_f32 v135,v135,v53
s_waitcnt lgkmcnt(8)
v_mfma_f32_16x16x32_bf16 a[224:227],v[108:111],a[92:95],a[224:227]
s_mul_i32 s75,s75,16384
v_mul_f32 v140,v140,v53
v_mul_f32 v141,v141,v53
v_mfma_f32_16x16x32_bf16 a[228:231],v[108:111],a[124:127],0
s_add_u32 s75,s75,4096
v_mul_f32 v142,v142,v53
v_mul_f32 v143,v143,v53
v_mfma_f32_16x16x32_bf16 a[228:231],v[104:107],a[116:119],a[228:231]
s_add_u32 s76,s63,3
v_cvt_pk_bf16_f32 v132,v132,v133
v_cvt_pk_bf16_f32 v133,v134,v135
v_mfma_f32_16x16x32_bf16 a[228:231],v[100:103],a[108:111],a[228:231]
s_and_b32 s76,s76,3
v_cvt_pk_bf16_f32 v134,v140,v141
v_cvt_pk_bf16_f32 v135,v142,v143
v_mfma_f32_16x16x32_bf16 a[228:231],v[96:99],a[100:103],a[228:231]
s_lshl_b32 s76,s76,2
v_mul_f32 v148,v148,v53
v_mul_f32 v149,v149,v53
v_mfma_f32_16x16x32_bf16 a[232:235],v[96:99],a[64:67],0
s_add_u32 s76,s76,s60
v_mul_f32 v150,v150,v53
v_mul_f32 v151,v151,v53
v_mfma_f32_16x16x32_bf16 a[232:235],v[100:103],a[72:75],a[232:235]
s_mul_i32 s76,s76,16384
v_mul_f32 v156,v156,v53
v_mul_f32 v157,v157,v53
v_mfma_f32_16x16x32_bf16 a[232:235],v[104:107],a[80:83],a[232:235]
s_add_u32 s76,s76,0
v_mul_f32 v158,v158,v53
v_mul_f32 v159,v159,v53
v_mfma_f32_16x16x32_bf16 a[232:235],v[108:111],a[88:91],a[232:235]
s_lshl_b32 s67,s63,10
v_cvt_pk_bf16_f32 v148,v148,v149
v_cvt_pk_bf16_f32 v149,v150,v151
v_mfma_f32_16x16x32_bf16 a[236:239],v[108:111],a[120:123],0
s_mov_b32 s26,307200
ds_read_b64_tr_b16 v[108:109],v1 offset:30912
ds_read_b64_tr_b16 v[110:111],v1 offset:36032
v_mfma_f32_16x16x32_bf16 a[236:239],v[104:107],a[112:115],a[236:239]
buffer_store_dwordx4 v[44:47],v6,s[40:43],s82 offen sc1
v_mfma_f32_16x16x32_bf16 a[236:239],v[100:103],a[104:107],a[236:239]
s_mov_b32 s27,0x20000
)A"
R"A(ds_read_b64_tr_b16 v[100:101],v1 offset:10432
ds_read_b64_tr_b16 v[102:103],v1 offset:15552
v_mfma_f32_16x16x32_bf16 a[236:239],v[96:99],a[96:99],a[236:239]
s_lshl_b32 s77,s63,2
ds_read_b64_tr_b16 v[96:97],v1 offset:192
v_cvt_pk_bf16_f32 v150,v156,v157
v_mfma_f32_16x16x32_bf16 a[240:243],v[112:115],a[68:71],0
s_add_u32 s77,s77,s60
ds_read_b64_tr_b16 v[98:99],v1 offset:5312
v_cvt_pk_bf16_f32 v151,v158,v159
v_mfma_f32_16x16x32_bf16 a[240:243],v[116:119],a[76:79],a[240:243]
s_mul_i32 s77,s77,16384
ds_read_b64_tr_b16 v[104:105],v1 offset:20672
v_mul_f32 v164,v164,v53
v_mfma_f32_16x16x32_bf16 a[240:243],v[120:123],a[84:87],a[240:243]
s_add_u32 s65,s77,0
ds_read_b64_tr_b16 v[106:107],v1 offset:25792
v_mul_f32 v165,v165,v53
v_mfma_f32_16x16x32_bf16 a[240:243],v[124:127],a[92:95],a[240:243]
s_add_u32 s68,s77,12288
v_mul_f32 v166,v166,v53
v_mul_f32 v167,v167,v53
v_mfma_f32_16x16x32_bf16 a[244:247],v[124:127],a[124:127],0
v_mul_f32 v172,v172,v53
v_mul_f32 v173,v173,v53
v_mfma_f32_16x16x32_bf16 a[244:247],v[120:123],a[116:119],a[244:247]
v_mul_f32 v174,v174,v53
v_mul_f32 v175,v175,v53
v_mfma_f32_16x16x32_bf16 a[244:247],v[116:119],a[108:111],a[244:247]
v_cvt_pk_bf16_f32 v164,v164,v165
v_cvt_pk_bf16_f32 v165,v166,v167
v_mfma_f32_16x16x32_bf16 a[244:247],v[112:115],a[100:103],a[244:247]
v_cvt_pk_bf16_f32 v166,v172,v173
v_cvt_pk_bf16_f32 v167,v174,v175
v_mfma_f32_16x16x32_bf16 a[248:251],v[112:115],a[64:67],0
v_mul_f32 v180,v180,v53
v_mul_f32 v181,v181,v53
v_mfma_f32_16x16x32_bf16 a[248:251],v[116:119],a[72:75],a[248:251]
v_mul_f32 v182,v182,v53
v_mul_f32 v183,v183,v53
v_mfma_f32_16x16x32_bf16 a[248:251],v[120:123],a[80:83],a[248:251]
v_mul_f32 v188,v188,v53
v_mul_f32 v189,v189,v53
v_mfma_f32_16x16x32_bf16 a[248:251],v[124:127],a[88:91],a[248:251]
v_mul_f32 v190,v190,v53
v_mul_f32 v191,v191,v53
v_mfma_f32_16x16x32_bf16 a[252:255],v[124:127],a[120:123],0
buffer_load_dword v30,v29,s[8:11],s7 offen
v_mfma_f32_16x16x32_bf16 a[252:255],v[120:123],a[112:115],a[252:255]
ds_read_b64_tr_b16 v[120:121],v2 offset:20704
ds_read_b64_tr_b16 v[122:123],v2 offset:25824
v_mfma_f32_16x16x32_bf16 a[252:255],v[116:119],a[104:107],a[252:255]
buffer_store_dwordx4 v[132:135],v6,s[40:43],s79 offen offset:1024 sc1
v_mfma_f32_16x16x32_bf16 a[252:255],v[112:115],a[96:99],a[252:255]
ds_read_b64_tr_b16 v[112:113],v2 offset:224
ds_read_b64_tr_b16 v[114:115],v2 offset:5344
v_mfma_f32_16x16x32_bf16 a[0:3],v[64:67],a[68:71],0
ds_read_b64_tr_b16 v[116:117],v2 offset:10464
v_cvt_pk_bf16_f32 v180,v180,v181
v_mfma_f32_16x16x32_bf16 a[0:3],v[68:71],a[76:79],a[0:3]
ds_read_b64_tr_b16 v[118:119],v2 offset:15584
v_cvt_pk_bf16_f32 v181,v182,v183
v_mfma_f32_16x16x32_bf16 a[0:3],v[72:75],a[84:87],a[0:3]
ds_read_b64_tr_b16 v[124:125],v2 offset:30944
v_cvt_pk_bf16_f32 v182,v188,v189
v_mfma_f32_16x16x32_bf16 a[0:3],v[76:79],a[92:95],a[0:3]
ds_read_b64_tr_b16 v[126:127],v2 offset:36064
v_cvt_pk_bf16_f32 v183,v190,v191
v_mfma_f32_16x16x32_bf16 a[4:7],v[76:79],a[124:127],0
v_mul_f32 v196,v196,v57
v_mul_f32 v197,v197,v57
v_mfma_f32_16x16x32_bf16 a[4:7],v[72:75],a[116:119],a[4:7]
v_mul_f32 v198,v198,v57
v_mul_f32 v199,v199,v57
v_mfma_f32_16x16x32_bf16 a[4:7],v[68:71],a[108:111],a[4:7]
v_mul_f32 v204,v204,v57
v_mul_f32 v205,v205,v57
v_mfma_f32_16x16x32_bf16 a[4:7],v[64:67],a[100:103],a[4:7]
v_mul_f32 v206,v206,v57
v_mul_f32 v207,v207,v57
v_mfma_f32_16x16x32_bf16 a[8:11],v[64:67],a[64:67],0
v_cvt_pk_bf16_f32 v196,v196,v197
v_cvt_pk_bf16_f32 v197,v198,v199
v_mfma_f32_16x16x32_bf16 a[8:11],v[68:71],a[72:75],a[8:11]
v_cvt_pk_bf16_f32 v198,v204,v205
v_cvt_pk_bf16_f32 v199,v206,v207
v_mfma_f32_16x16x32_bf16 a[8:11],v[72:75],a[80:83],a[8:11]
v_mul_f32 v212,v212,v57
v_mul_f32 v213,v213,v57
v_mfma_f32_16x16x32_bf16 a[8:11],v[76:79],a[88:91],a[8:11]
v_mul_f32 v214,v214,v57
v_mul_f32 v215,v215,v57
v_mfma_f32_16x16x32_bf16 a[12:15],v[76:79],a[120:123],0
ds_read_b64_tr_b16 v[76:77],v1 offset:30976
ds_read_b64_tr_b16 v[78:79],v1 offset:36096
v_mfma_f32_16x16x32_bf16 a[12:15],v[72:75],a[112:115],a[12:15]
buffer_store_dwordx4 v[148:151],v6,s[40:43],s80 offen offset:1024 sc1
v_mfma_f32_16x16x32_bf16 a[12:15],v[68:71],a[104:107],a[12:15]
ds_read_b64_tr_b16 v[68:69],v1 offset:10496
ds_read_b64_tr_b16 v[70:71],v1 offset:15616
v_mfma_f32_16x16x32_bf16 a[12:15],v[64:67],a[96:99],a[12:15]
s_waitcnt lgkmcnt(4)
ds_read_b64_tr_b16 v[64:65],v1 offset:256
v_mul_f32 v220,v220,v57
v_mfma_f32_16x16x32_bf16 a[16:19],v[80:83],a[68:71],0
ds_read_b64_tr_b16 v[66:67],v1 offset:5376
v_mul_f32 v221,v221,v57
v_mfma_f32_16x16x32_bf16 a[16:19],v[84:87],a[76:79],a[16:19]
ds_read_b64_tr_b16 v[72:73],v1 offset:20736
v_mul_f32 v222,v222,v57
v_mfma_f32_16x16x32_bf16 a[16:19],v[88:91],a[84:87],a[16:19]
ds_read_b64_tr_b16 v[74:75],v1 offset:25856
v_add_u32 v1,0xffff6000,v1
v_mfma_f32_16x16x32_bf16 a[16:19],v[92:95],a[92:95],a[16:19]
v_mul_f32 v223,v223,v57
v_cvt_pk_bf16_f32 v212,v212,v213
v_mfma_f32_16x16x32_bf16 a[20:23],v[92:95],a[124:127],0
v_cvt_pk_bf16_f32 v213,v214,v215
v_cvt_pk_bf16_f32 v214,v220,v221
v_mfma_f32_16x16x32_bf16 a[20:23],v[88:91],a[116:119],a[20:23]
v_cvt_pk_bf16_f32 v215,v222,v223
v_mul_f32 v228,v228,v57
v_mfma_f32_16x16x32_bf16 a[20:23],v[84:87],a[108:111],a[20:23]
v_mul_f32 v229,v229,v57
v_mul_f32 v230,v230,v57
v_mfma_f32_16x16x32_bf16 a[20:23],v[80:83],a[100:103],a[20:23]
v_mul_f32 v231,v231,v57
v_mul_f32 v236,v236,v57
v_mfma_f32_16x16x32_bf16 a[24:27],v[80:83],a[64:67],0
v_mul_f32 v237,v237,v57
v_mul_f32 v238,v238,v57
v_mfma_f32_16x16x32_bf16 a[24:27],v[84:87],a[72:75],a[24:27]
v_mul_f32 v239,v239,v57
v_cvt_pk_bf16_f32 v228,v228,v229
v_mfma_f32_16x16x32_bf16 a[24:27],v[88:91],a[80:83],a[24:27]
s_waitcnt vmcnt(5)
v_cmp_ne_u32 s[56:57],s52,v4
v_cmp_ne_u32 s[58:59],s53,v5
v_mfma_f32_16x16x32_bf16 a[24:27],v[92:95],a[88:91],a[24:27]
s_or_b64 s[56:57],s[56:57],s[58:59]
s_cbranch_scc1 L_fr6_%=
L_fo6_%=:
v_cvt_pk_bf16_f32 v229,v230,v231
v_mfma_f32_16x16x32_bf16 a[28:31],v[92:95],a[120:123],0
buffer_store_dwordx4 v[164:167],v6,s[40:43],s81 offen offset:1024 sc1
v_mfma_f32_16x16x32_bf16 a[28:31],v[88:91],a[112:115],a[28:31]
s_lshl_b32 s91,s63,8
ds_read_b64_tr_b16 v[88:89],v2 offset:20768
ds_read_b64_tr_b16 v[90:91],v2 offset:25888
v_mfma_f32_16x16x32_bf16 a[28:31],v[84:87],a[104:107],a[28:31]
buffer_load_dwordx4 a[128:131],v6,s[28:31],s87 offen sc1
v_mfma_f32_16x16x32_bf16 a[28:31],v[80:83],a[96:99],a[28:31]
ds_read_b64_tr_b16 v[80:81],v2 offset:288
ds_read_b64_tr_b16 v[82:83],v2 offset:5408
v_mfma_f32_16x16x32_bf16 v[128:131],v[96:99],a[68:71],0
ds_read_b64_tr_b16 v[84:85],v2 offset:10528
v_cvt_pk_bf16_f32 v230,v236,v237
v_mfma_f32_16x16x32_bf16 v[128:131],v[100:103],a[76:79],v[128:131]
ds_read_b64_tr_b16 v[86:87],v2 offset:15648
v_cvt_pk_bf16_f32 v231,v238,v239
v_mfma_f32_16x16x32_bf16 v[128:131],v[104:107],a[84:87],v[128:131]
ds_read_b64_tr_b16 v[92:93],v2 offset:31008
v_mul_f32 v244,v244,v57
v_mfma_f32_16x16x32_bf16 v[128:131],v[108:111],a[92:95],v[128:131]
ds_read_b64_tr_b16 v[94:95],v2 offset:36128
v_add_u32 v2,0xffff6000,v2
v_mfma_f32_16x16x32_bf16 v[136:139],v[108:111],a[124:127],0
v_mul_f32 v245,v245,v57
v_mul_f32 v246,v246,v57
v_mfma_f32_16x16x32_bf16 v[136:139],v[104:107],a[116:119],v[136:139]
v_mul_f32 v247,v247,v57
v_mul_f32 v252,v252,v57
v_mfma_f32_16x16x32_bf16 v[136:139],v[100:103],a[108:111],v[136:139]
v_mul_f32 v253,v253,v57
v_mul_f32 v254,v254,v57
v_mfma_f32_16x16x32_bf16 v[136:139],v[96:99],a[100:103],v[136:139]
v_mul_f32 v255,v255,v57
v_cvt_pk_bf16_f32 v244,v244,v245
v_mfma_f32_16x16x32_bf16 v[144:147],v[96:99],a[64:67],0
v_cvt_pk_bf16_f32 v245,v246,v247
v_cvt_pk_bf16_f32 v246,v252,v253
v_mfma_f32_16x16x32_bf16 v[144:147],v[100:103],a[72:75],v[144:147]
buffer_load_dwordx4 a[144:147],v6,s[28:31],s88 offen sc1
v_mfma_f32_16x16x32_bf16 v[144:147],v[104:107],a[80:83],v[144:147]
v_cvt_pk_bf16_f32 v247,v254,v255
v_accvgpr_read_b32 v16,a40
v_mfma_f32_16x16x32_bf16 v[144:147],v[108:111],a[88:91],v[144:147]
buffer_store_dwordx4 v[180:183],v6,s[40:43],s82 offen offset:1024 sc1
v_mfma_f32_16x16x32_bf16 v[152:155],v[108:111],a[120:123],0
ds_read_b64_tr_b16 v[108:109],v1 offset:30720
ds_read_b64_tr_b16 v[110:111],v1 offset:35840
v_mfma_f32_16x16x32_bf16 v[152:155],v[104:107],a[112:115],v[152:155]
ds_read_b64_tr_b16 v[104:105],v1 offset:20480
v_accvgpr_read_b32 v17,a41
v_mfma_f32_16x16x32_bf16 v[152:155],v[100:103],a[104:107],v[152:155]
ds_read_b64_tr_b16 v[100:101],v1 offset:10240
v_accvgpr_read_b32 v18,a42
v_mfma_f32_16x16x32_bf16 v[152:155],v[96:99],a[96:99],v[152:155]
ds_read_b64_tr_b16 v[96:97],v1
v_accvgpr_read_b32 v19,a43
v_mfma_f32_16x16x32_bf16 v[160:163],v[112:115],a[68:71],0
ds_read_b64_tr_b16 v[98:99],v1 offset:5120
ds_read_b64_tr_b16 v[102:103],v1 offset:15360
v_mfma_f32_16x16x32_bf16 v[160:163],v[116:119],a[76:79],v[160:163]
ds_read_b64_tr_b16 v[106:107],v1 offset:25600
v_accvgpr_read_b32 v20,a44
v_mfma_f32_16x16x32_bf16 v[160:163],v[120:123],a[84:87],v[160:163]
v_accvgpr_read_b32 v21,a45
v_accvgpr_read_b32 v22,a46
v_mfma_f32_16x16x32_bf16 v[160:163],v[124:127],a[92:95],v[160:163]
v_accvgpr_read_b32 v23,a47
v_mul_f32 v16,v16,v61
v_mfma_f32_16x16x32_bf16 v[168:171],v[124:127],a[124:127],0
v_mul_f32 v17,v17,v61
v_mul_f32 v18,v18,v61
v_mfma_f32_16x16x32_bf16 v[168:171],v[120:123],a[116:119],v[168:171]
v_mul_f32 v19,v19,v61
v_mul_f32 v20,v20,v61
v_mfma_f32_16x16x32_bf16 v[168:171],v[116:119],a[108:111],v[168:171]
buffer_load_dwordx4 a[160:163],v6,s[28:31],s89 offen sc1
v_mfma_f32_16x16x32_bf16 v[168:171],v[112:115],a[100:103],v[168:171]
v_mul_f32 v21,v21,v61
v_mul_f32 v22,v22,v61
v_mfma_f32_16x16x32_bf16 v[176:179],v[112:115],a[64:67],0
v_mul_f32 v23,v23,v61
v_cvt_pk_bf16_f32 v156,v16,v17
v_mfma_f32_16x16x32_bf16 v[176:179],v[116:119],a[72:75],v[176:179]
v_cvt_pk_bf16_f32 v157,v18,v19
v_cvt_pk_bf16_f32 v158,v20,v21
v_mfma_f32_16x16x32_bf16 v[176:179],v[120:123],a[80:83],v[176:179]
buffer_store_dwordx4 v[196:199],v6,s[40:43],s79 offen offset:2048 sc1
v_mfma_f32_16x16x32_bf16 v[176:179],v[124:127],a[88:91],v[176:179]
v_cvt_pk_bf16_f32 v159,v22,v23
v_accvgpr_read_b32 v16,a56
v_mfma_f32_16x16x32_bf16 v[184:187],v[124:127],a[120:123],0
v_accvgpr_read_b32 v17,a57
v_accvgpr_read_b32 v18,a58
v_mfma_f32_16x16x32_bf16 v[184:187],v[120:123],a[112:115],v[184:187]
ds_read_b64_tr_b16 v[120:121],v2 offset:20512
ds_read_b64_tr_b16 v[122:123],v2 offset:25632
v_mfma_f32_16x16x32_bf16 v[184:187],v[116:119],a[104:107],v[184:187]
ds_read_b64_tr_b16 v[116:117],v2 offset:10272
v_accvgpr_read_b32 v19,a59
v_mfma_f32_16x16x32_bf16 v[184:187],v[112:115],a[96:99],v[184:187]
s_waitcnt lgkmcnt(3)
ds_read_b64_tr_b16 v[112:113],v2 offset:32
v_accvgpr_read_b32 v20,a60
v_mfma_f32_16x16x32_bf16 v[192:195],v[64:67],a[68:71],0
ds_read_b64_tr_b16 v[114:115],v2 offset:5152
v_accvgpr_read_b32 v21,a61
v_mfma_f32_16x16x32_bf16 v[192:195],v[68:71],a[76:79],v[192:195]
ds_read_b64_tr_b16 v[118:119],v2 offset:15392
v_accvgpr_read_b32 v22,a62
v_mfma_f32_16x16x32_bf16 v[192:195],v[72:75],a[84:87],v[192:195]
v_accvgpr_read_b32 v23,a63
v_mul_f32 v16,v16,v61
v_mfma_f32_16x16x32_bf16 v[192:195],v[76:79],a[92:95],v[192:195]
buffer_load_dwordx4 a[176:179],v6,s[28:31],s90 offen sc1
v_mfma_f32_16x16x32_bf16 v[200:203],v[76:79],a[124:127],0
v_mul_f32 v17,v17,v61
v_mul_f32 v18,v18,v61
v_mfma_f32_16x16x32_bf16 v[200:203],v[72:75],a[116:119],v[200:203]
v_mul_f32 v19,v19,v61
v_mul_f32 v20,v20,v61
v_mfma_f32_16x16x32_bf16 v[200:203],v[68:71],a[108:111],v[200:203]
v_mul_f32 v21,v21,v61
v_mul_f32 v22,v22,v61
v_mfma_f32_16x16x32_bf16 v[200:203],v[64:67],a[100:103],v[200:203]
v_mul_f32 v23,v23,v61
v_cvt_pk_bf16_f32 v188,v16,v17
v_mfma_f32_16x16x32_bf16 v[208:211],v[64:67],a[64:67],0
v_cvt_pk_bf16_f32 v189,v18,v19
v_cvt_pk_bf16_f32 v190,v20,v21
v_mfma_f32_16x16x32_bf16 v[208:211],v[68:71],a[72:75],v[208:211]
buffer_store_dwordx4 v[212:215],v6,s[40:43],s80 offen offset:2048 sc1
v_mfma_f32_16x16x32_bf16 v[208:211],v[72:75],a[80:83],v[208:211]
ds_read_b64_tr_b16 v[124:125],v2 offset:30752
v_cvt_pk_bf16_f32 v191,v22,v23
v_mfma_f32_16x16x32_bf16 v[208:211],v[76:79],a[88:91],v[208:211]
ds_read_b64_tr_b16 v[126:127],v2 offset:35872
)A"
R"A(v_accvgpr_read_b32 v8,a32
v_mfma_f32_16x16x32_bf16 v[216:219],v[76:79],a[120:123],0
v_accvgpr_read_b32 v9,a33
v_accvgpr_read_b32 v10,a34
v_mfma_f32_16x16x32_bf16 v[216:219],v[72:75],a[112:115],v[216:219]
v_accvgpr_read_b32 v11,a35
v_accvgpr_read_b32 v12,a36
v_mfma_f32_16x16x32_bf16 v[216:219],v[68:71],a[104:107],v[216:219]
v_accvgpr_read_b32 v13,a37
v_accvgpr_read_b32 v14,a38
v_mfma_f32_16x16x32_bf16 v[216:219],v[64:67],a[96:99],v[216:219]
v_accvgpr_read_b32 v15,a39
v_mul_f32 v8,v8,v61
v_mfma_f32_16x16x32_bf16 v[224:227],v[80:83],a[68:71],0
buffer_load_dwordx4 a[180:183],v6,s[28:31],s90 offen offset:1024 sc1
v_mfma_f32_16x16x32_bf16 v[224:227],v[84:87],a[76:79],v[224:227]
v_mul_f32 v9,v9,v61
v_mul_f32 v10,v10,v61
v_mfma_f32_16x16x32_bf16 v[224:227],v[88:91],a[84:87],v[224:227]
v_mul_f32 v11,v11,v61
v_mul_f32 v12,v12,v61
v_mfma_f32_16x16x32_bf16 v[224:227],v[92:95],a[92:95],v[224:227]
v_mul_f32 v13,v13,v61
v_mul_f32 v14,v14,v61
v_mfma_f32_16x16x32_bf16 v[232:235],v[92:95],a[124:127],0
v_mul_f32 v15,v15,v61
v_cvt_pk_bf16_f32 v140,v8,v9
v_mfma_f32_16x16x32_bf16 v[232:235],v[88:91],a[116:119],v[232:235]
v_cvt_pk_bf16_f32 v141,v10,v11
v_cvt_pk_bf16_f32 v142,v12,v13
v_mfma_f32_16x16x32_bf16 v[232:235],v[84:87],a[108:111],v[232:235]
v_cvt_pk_bf16_f32 v143,v14,v15
v_accvgpr_read_b32 v8,a48
v_mfma_f32_16x16x32_bf16 v[232:235],v[80:83],a[100:103],v[232:235]
v_accvgpr_read_b32 v9,a49
v_accvgpr_read_b32 v10,a50
v_mfma_f32_16x16x32_bf16 v[240:243],v[80:83],a[64:67],0
buffer_store_dwordx4 v[228:231],v6,s[40:43],s81 offen offset:2048 sc1
v_mfma_f32_16x16x32_bf16 v[240:243],v[84:87],a[72:75],v[240:243]
v_accvgpr_read_b32 v11,a51
v_accvgpr_read_b32 v12,a52
v_mfma_f32_16x16x32_bf16 v[240:243],v[88:91],a[80:83],v[240:243]
v_accvgpr_read_b32 v13,a53
v_accvgpr_read_b32 v14,a54
v_mfma_f32_16x16x32_bf16 v[240:243],v[92:95],a[88:91],v[240:243]
v_accvgpr_read_b32 v15,a55
v_mul_f32 v8,v8,v61
v_mfma_f32_16x16x32_bf16 v[248:251],v[92:95],a[120:123],0
v_mul_f32 v9,v9,v61
v_mul_f32 v10,v10,v61
v_mfma_f32_16x16x32_bf16 v[248:251],v[88:91],a[112:115],v[248:251]
buffer_load_dwordx4 a[164:167],v6,s[28:31],s89 offen offset:1024 sc1
v_mfma_f32_16x16x32_bf16 v[248:251],v[84:87],a[104:107],v[248:251]
v_mul_f32 v11,v11,v61
v_mul_f32 v12,v12,v61
v_mfma_f32_16x16x32_bf16 v[248:251],v[80:83],a[96:99],v[248:251]
v_mul_f32 v13,v13,v61
v_mul_f32 v14,v14,v61
v_mfma_f32_16x16x32_bf16 a[192:195],v[96:99],v[32:35],a[192:195]
v_mul_f32 v15,v15,v61
v_cvt_pk_bf16_f32 v172,v8,v9
v_mfma_f32_16x16x32_bf16 a[192:195],v[100:103],v[36:39],a[192:195]
v_cvt_pk_bf16_f32 v173,v10,v11
v_cvt_pk_bf16_f32 v174,v12,v13
v_mfma_f32_16x16x32_bf16 a[192:195],v[104:107],v[40:43],a[192:195]
ds_read_b64_tr_b16 v[64:65],v1 offset:64
v_cvt_pk_bf16_f32 v175,v14,v15
v_mfma_f32_16x16x32_bf16 a[192:195],v[108:111],v[44:47],a[192:195]
ds_read_b64_tr_b16 v[66:67],v1 offset:5184
ds_read_b64_tr_b16 v[68:69],v1 offset:10304
v_mfma_f32_16x16x32_bf16 a[196:199],v[108:111],v[180:183],a[196:199]
s_cmp_eq_u32 s60,3
s_cselect_b32 s69,0x40000000,0
ds_read_b64_tr_b16 v[70:71],v1 offset:15424
v_mfma_f32_16x16x32_bf16 a[196:199],v[104:107],v[164:167],a[196:199]
ds_read_b64_tr_b16 v[72:73],v1 offset:20544
v_add_u32 v28,s69,v6
v_mfma_f32_16x16x32_bf16 a[196:199],v[100:103],v[148:151],a[196:199]
s_and_b32 s69,s63,s60
ds_read_b64_tr_b16 v[74:75],v1 offset:25664
v_mov_b32 v8,s52
v_mfma_f32_16x16x32_bf16 a[196:199],v[96:99],v[132:135],a[196:199]
buffer_store_dwordx4 v[244:247],v6,s[40:43],s82 offen offset:2048 sc1
v_mfma_f32_16x16x32_bf16 a[200:203],v[96:99],v[196:199],a[200:203]
ds_read_b64_tr_b16 v[76:77],v1 offset:30784
ds_read_b64_tr_b16 v[78:79],v1 offset:35904
v_mfma_f32_16x16x32_bf16 a[200:203],v[100:103],v[212:215],a[200:203]
ds_read_b64_tr_b16 v[80:81],v2 offset:96
v_mov_b32 v9,s53
v_mfma_f32_16x16x32_bf16 a[200:203],v[104:107],v[228:231],a[200:203]
buffer_load_dwordx4 a[148:151],v6,s[28:31],s88 offen offset:1024 sc1
v_mfma_f32_16x16x32_bf16 a[200:203],v[108:111],v[244:247],a[200:203]
ds_read_b64_tr_b16 v[82:83],v2 offset:5216
ds_read_b64_tr_b16 v[84:85],v2 offset:10336
v_mfma_f32_16x16x32_bf16 a[204:207],v[108:111],v[188:191],a[204:207]
ds_read_b64_tr_b16 v[86:87],v2 offset:15456
v_lshlrev_b32 v10,20,v6
v_mfma_f32_16x16x32_bf16 a[204:207],v[104:107],v[172:175],a[204:207]
ds_read_b64_tr_b16 v[88:89],v2 offset:20576
v_bfe_u32 v3,v6,4,2
v_mfma_f32_16x16x32_bf16 a[204:207],v[100:103],v[156:159],a[204:207]
ds_read_b64_tr_b16 v[90:91],v2 offset:25696
v_min_u32 v3,2,v3
v_mfma_f32_16x16x32_bf16 a[204:207],v[96:99],v[140:143],a[204:207]
s_waitcnt lgkmcnt(8)
ds_read_b64_tr_b16 v[92:93],v2 offset:30816
ds_read_b64_tr_b16 v[94:95],v2 offset:35936
v_mfma_f32_16x16x32_bf16 a[208:211],v[112:115],v[32:35],a[208:211]
ds_read_b64_tr_b16 v[96:97],v1 offset:128
v_add3_u32 v3,v3,s64,1
v_mfma_f32_16x16x32_bf16 a[208:211],v[116:119],v[36:39],a[208:211]
ds_read_b64_tr_b16 v[98:99],v1 offset:5248
v_and_b32 v3,3,v3
v_mfma_f32_16x16x32_bf16 a[208:211],v[120:123],v[40:43],a[208:211]
ds_read_b64_tr_b16 v[100:101],v1 offset:10368
v_lshlrev_b32 v3,6,v3
v_mfma_f32_16x16x32_bf16 a[208:211],v[124:127],v[44:47],a[208:211]
ds_read_b64_tr_b16 v[102:103],v1 offset:15488
ds_read_b64_tr_b16 v[104:105],v1 offset:20608
v_mfma_f32_16x16x32_bf16 a[212:215],v[124:127],v[180:183],a[212:215]
ds_read_b64_tr_b16 v[106:107],v1 offset:25728
v_bfe_u32 v56,v6,6,2
v_mfma_f32_16x16x32_bf16 a[212:215],v[120:123],v[164:167],a[212:215]
ds_read_b64_tr_b16 v[108:109],v1 offset:30848
v_lshrrev_b32 v57,8,v6
v_mfma_f32_16x16x32_bf16 a[212:215],v[116:119],v[148:151],a[212:215]
buffer_store_dwordx4 v[140:143],v28,s[40:43],s79 offen offset:3072 sc1
v_mfma_f32_16x16x32_bf16 a[212:215],v[112:115],v[132:135],a[212:215]
ds_read_b64_tr_b16 v[110:111],v1 offset:35968
v_cmp_eq_u32 s[56:57],v56,v57
L_pd20_%=:
s_cbranch_execz L_trt1_%=
v_mfma_f32_16x16x32_bf16 a[216:219],v[112:115],v[196:199],a[216:219]
buffer_load_dwordx4 a[132:135],v6,s[28:31],s87 offen offset:1024 sc1
v_mfma_f32_16x16x32_bf16 a[216:219],v[116:119],v[212:215],a[216:219]
v_bfe_u32 v56,v6,4,1
v_lshlrev_b32 v56,4,v56
v_mfma_f32_16x16x32_bf16 a[216:219],v[120:123],v[228:231],a[216:219]
v_mov_b32 v57,0x3f80
v_lshlrev_b32 v56,v56,v57
v_mfma_f32_16x16x32_bf16 a[216:219],v[124:127],v[244:247],a[216:219]
v_cndmask_b32 v56,0,v56,s[56:57]
v_bfe_u32 v57,v6,5,1
v_mfma_f32_16x16x32_bf16 a[220:223],v[124:127],v[188:191],a[220:223]
ds_read_b64_tr_b16 v[124:125],v2 offset:30880
ds_read_b64_tr_b16 v[126:127],v2 offset:36000
v_mfma_f32_16x16x32_bf16 a[220:223],v[120:123],v[172:175],a[220:223]
ds_read_b64_tr_b16 v[120:121],v2 offset:20640
v_cmp_eq_u32 s[56:57],0,v57
v_mfma_f32_16x16x32_bf16 a[220:223],v[116:119],v[156:159],a[220:223]
s_add_u32 s36,s12,s66
s_addc_u32 s37,s13,0
ds_read_b64_tr_b16 v[116:117],v2 offset:10400
v_mfma_f32_16x16x32_bf16 a[220:223],v[112:115],v[140:143],a[220:223]
s_and_b32 s37,s37,0xffff
ds_read_b64_tr_b16 v[112:113],v2 offset:160
v_cndmask_b32 v48,0,v56,s[56:57]
v_mfma_f32_16x16x32_bf16 a[224:227],v[64:67],v[32:35],a[224:227]
s_mul_i32 s66,s61,307200
ds_read_b64_tr_b16 v[114:115],v2 offset:5280
ds_read_b64_tr_b16 v[118:119],v2 offset:15520
v_mfma_f32_16x16x32_bf16 a[224:227],v[68:71],v[36:39],a[224:227]
ds_read_b64_tr_b16 v[122:123],v2 offset:25760
v_cndmask_b32 v49,v56,0,s[56:57]
v_mfma_f32_16x16x32_bf16 a[224:227],v[72:75],v[40:43],a[224:227]
s_waitcnt lgkmcnt(8)
v_mov_b32 v58,s52
v_mov_b32 v59,s53
v_mfma_f32_16x16x32_bf16 a[224:227],v[76:79],v[44:47],a[224:227]
s_add_u32 s24,s16,s66
s_addc_u32 s25,s17,0
v_lshlrev_b32 v60,20,v6
v_mfma_f32_16x16x32_bf16 a[228:231],v[76:79],v[180:183],a[228:231]
buffer_load_dwordx4 a[136:139],v6,s[28:31],s87 offen offset:2048 sc1
v_mfma_f32_16x16x32_bf16 a[228:231],v[72:75],v[164:167],a[228:231]
s_mul_i32 s66,s60,16384
s_add_u32 s66,s66,s67
s_add_u32 s79,s66,12288
v_mfma_f32_16x16x32_bf16 a[228:231],v[68:71],v[148:151],a[228:231]
buffer_store_dwordx4 v[156:159],v28,s[40:43],s80 offen offset:3072 sc1
v_mfma_f32_16x16x32_bf16 a[228:231],v[64:67],v[132:135],a[228:231]
s_add_u32 s80,s66,77824
v_bfe_u32 v61,v6,4,1
v_add3_u32 v61,v61,s63,2
v_mfma_f32_16x16x32_bf16 a[232:235],v[64:67],v[196:199],a[232:235]
s_and_b32 s25,s25,0xffff
v_and_b32 v61,3,v61
v_lshlrev_b32 v61,8,v61
v_mfma_f32_16x16x32_bf16 a[232:235],v[68:71],v[212:215],a[232:235]
s_add_u32 s67,s77,8192
v_mov_b32 v50,0
v_mov_b32 v51,0
v_mfma_f32_16x16x32_bf16 a[232:235],v[72:75],v[228:231],a[232:235]
v_mov_b32 v52,0
v_mov_b32 v53,0
v_mfma_f32_16x16x32_bf16 a[232:235],v[76:79],v[244:247],a[232:235]
v_mov_b32 v54,v48
v_mov_b32 v55,v49
v_mfma_f32_16x16x32_bf16 a[236:239],v[76:79],v[188:191],a[236:239]
ds_read_b64_tr_b16 v[76:77],v1 offset:30912
ds_read_b64_tr_b16 v[78:79],v1 offset:36032
v_mfma_f32_16x16x32_bf16 a[236:239],v[72:75],v[172:175],a[236:239]
ds_read_b64_tr_b16 v[72:73],v1 offset:20672
v_bfe_u32 v56,v6,4,4
v_mfma_f32_16x16x32_bf16 a[236:239],v[68:71],v[156:159],a[236:239]
ds_read_b64_tr_b16 v[68:69],v1 offset:10432
v_mul_u32_u24 v56,1280,v56
v_mfma_f32_16x16x32_bf16 a[236:239],v[64:67],v[140:143],a[236:239]
ds_read_b64_tr_b16 v[64:65],v1 offset:192
v_bfe_u32 v57,v6,8,1
v_mfma_f32_16x16x32_bf16 a[240:243],v[80:83],v[32:35],a[240:243]
ds_read_b64_tr_b16 v[66:67],v1 offset:5312
ds_read_b64_tr_b16 v[70:71],v1 offset:15552
v_mfma_f32_16x16x32_bf16 a[240:243],v[84:87],v[36:39],a[240:243]
buffer_load_dwordx4 a[152:155],v6,s[28:31],s88 offen offset:2048 sc1
v_mfma_f32_16x16x32_bf16 a[240:243],v[88:91],v[40:43],a[240:243]
ds_read_b64_tr_b16 v[74:75],v1 offset:25792
v_lshl_add_u32 v56,v57,5,v56
v_mfma_f32_16x16x32_bf16 a[240:243],v[92:95],v[44:47],a[240:243]
v_lshrrev_b32 v57,9,v6
v_lshl_add_u32 v56,v57,4,v56
v_mfma_f32_16x16x32_bf16 a[244:247],v[92:95],v[180:183],a[244:247]
s_cmp_eq_u32 s69,3
s_cselect_b32 s69,0x40000000,0
s_add_u32 s68,s68,s69
v_mfma_f32_16x16x32_bf16 a[244:247],v[88:91],v[164:167],a[244:247]
buffer_store_dwordx4 v[172:175],v28,s[40:43],s81 offen offset:3072 sc1
v_mfma_f32_16x16x32_bf16 a[244:247],v[84:87],v[148:151],a[244:247]
s_add_u32 s81,s66,143360
v_mfma_f32_16x16x32_bf16 a[244:247],v[80:83],v[132:135],a[244:247]
v_mfma_f32_16x16x32_bf16 a[248:251],v[80:83],v[196:199],a[248:251]
v_mfma_f32_16x16x32_bf16 a[248:251],v[84:87],v[212:215],a[248:251]
v_mfma_f32_16x16x32_bf16 a[248:251],v[88:91],v[228:231],a[248:251]
v_mfma_f32_16x16x32_bf16 a[248:251],v[92:95],v[244:247],a[248:251]
v_mfma_f32_16x16x32_bf16 a[252:255],v[92:95],v[188:191],a[252:255]
ds_read_b64_tr_b16 v[92:93],v2 offset:30944
ds_read_b64_tr_b16 v[94:95],v2 offset:36064
v_mfma_f32_16x16x32_bf16 a[252:255],v[88:91],v[172:175],a[252:255]
ds_read_b64_tr_b16 v[88:89],v2 offset:20704
v_mfma_f32_16x16x32_bf16 a[252:255],v[84:87],v[156:159],a[252:255]
buffer_load_dwordx4 a[168:171],v6,s[28:31],s89 offen offset:2048 sc1
v_mfma_f32_16x16x32_bf16 a[252:255],v[80:83],v[140:143],a[252:255]
ds_read_b64_tr_b16 v[80:81],v2 offset:224
ds_read_b64_tr_b16 v[82:83],v2 offset:5344
v_mfma_f32_16x16x32_bf16 a[0:3],v[96:99],v[32:35],a[0:3]
ds_read_b64_tr_b16 v[84:85],v2 offset:10464
v_mfma_f32_16x16x32_bf16 a[0:3],v[100:103],v[36:39],a[0:3]
ds_read_b64_tr_b16 v[86:87],v2 offset:15584
v_mfma_f32_16x16x32_bf16 a[0:3],v[104:107],v[40:43],a[0:3]
ds_read_b64_tr_b16 v[90:91],v2 offset:25824
v_mfma_f32_16x16x32_bf16 a[0:3],v[108:111],v[44:47],a[0:3]
v_mfma_f32_16x16x32_bf16 a[4:7],v[108:111],v[180:183],a[4:7]
buffer_store_dwordx4 v[188:191],v28,s[40:43],s82 offen offset:3072 sc1
v_mfma_f32_16x16x32_bf16 a[4:7],v[104:107],v[164:167],a[4:7]
s_add_u32 s82,s66,208896
s_mul_i32 s66,s64,320
v_mfma_f32_16x16x32_bf16 a[4:7],v[100:103],v[148:151],a[4:7]
v_mfma_f32_16x16x32_bf16 a[4:7],v[96:99],v[132:135],a[4:7]
v_mfma_f32_16x16x32_bf16 a[8:11],v[96:99],v[196:199],a[8:11]
v_mfma_f32_16x16x32_bf16 a[8:11],v[100:103],v[212:215],a[8:11]
v_mfma_f32_16x16x32_bf16 a[8:11],v[104:107],v[228:231],a[8:11]
v_mfma_f32_16x16x32_bf16 a[8:11],v[108:111],v[244:247],a[8:11]
buffer_load_dwordx4 a[184:187],v6,s[28:31],s90 offen offset:2048 sc1
v_mfma_f32_16x16x32_bf16 a[12:15],v[108:111],v[188:191],a[12:15]
ds_read_b64_tr_b16 v[108:109],v1 offset:30976
)A"
R"A(ds_read_b64_tr_b16 v[110:111],v1 offset:36096
v_mfma_f32_16x16x32_bf16 a[12:15],v[104:107],v[172:175],a[12:15]
ds_read_b64_tr_b16 v[104:105],v1 offset:20736
v_mfma_f32_16x16x32_bf16 a[12:15],v[100:103],v[156:159],a[12:15]
ds_read_b64_tr_b16 v[100:101],v1 offset:10496
v_mfma_f32_16x16x32_bf16 a[12:15],v[96:99],v[140:143],a[12:15]
s_waitcnt lgkmcnt(4)
ds_read_b64_tr_b16 v[96:97],v1 offset:256
v_mfma_f32_16x16x32_bf16 a[16:19],v[112:115],v[32:35],a[16:19]
ds_read_b64_tr_b16 v[98:99],v1 offset:5376
ds_read_b64_tr_b16 v[102:103],v1 offset:15616
v_mfma_f32_16x16x32_bf16 a[16:19],v[116:119],v[36:39],a[16:19]
ds_read_b64_tr_b16 v[106:107],v1 offset:25856
v_add_u32 v1,0xffff6000,v1
v_mfma_f32_16x16x32_bf16 a[16:19],v[120:123],v[40:43],a[16:19]
v_mfma_f32_16x16x32_bf16 a[16:19],v[124:127],v[44:47],a[16:19]
v_mfma_f32_16x16x32_bf16 a[20:23],v[124:127],v[180:183],a[20:23]
v_mfma_f32_16x16x32_bf16 a[20:23],v[120:123],v[164:167],a[20:23]
v_mfma_f32_16x16x32_bf16 a[20:23],v[116:119],v[148:151],a[20:23]
v_mfma_f32_16x16x32_bf16 a[20:23],v[112:115],v[132:135],a[20:23]
v_mfma_f32_16x16x32_bf16 a[24:27],v[112:115],v[196:199],a[24:27]
buffer_load_dwordx4 a[188:191],v28,s[28:31],s90 offen offset:3072 sc1
v_mfma_f32_16x16x32_bf16 a[24:27],v[116:119],v[212:215],a[24:27]
v_mfma_f32_16x16x32_bf16 a[24:27],v[120:123],v[228:231],a[24:27]
v_mfma_f32_16x16x32_bf16 a[24:27],v[124:127],v[244:247],a[24:27]
v_mfma_f32_16x16x32_bf16 a[28:31],v[124:127],v[188:191],a[28:31]
ds_read_b64_tr_b16 v[124:125],v2 offset:31008
ds_read_b64_tr_b16 v[126:127],v2 offset:36128
v_mfma_f32_16x16x32_bf16 a[28:31],v[120:123],v[172:175],a[28:31]
ds_read_b64_tr_b16 v[120:121],v2 offset:20768
v_mfma_f32_16x16x32_bf16 a[28:31],v[116:119],v[156:159],a[28:31]
ds_read_b64_tr_b16 v[116:117],v2 offset:10528
v_mfma_f32_16x16x32_bf16 a[28:31],v[112:115],v[140:143],a[28:31]
ds_read_b64_tr_b16 v[112:113],v2 offset:288
v_mfma_f32_16x16x32_bf16 v[128:131],v[64:67],v[32:35],v[128:131]
ds_read_b64_tr_b16 v[114:115],v2 offset:5408
ds_read_b64_tr_b16 v[118:119],v2 offset:15648
v_mfma_f32_16x16x32_bf16 v[128:131],v[68:71],v[36:39],v[128:131]
ds_read_b64_tr_b16 v[122:123],v2 offset:25888
v_add_u32 v2,0xffff6000,v2
v_mfma_f32_16x16x32_bf16 v[128:131],v[72:75],v[40:43],v[128:131]
v_mfma_f32_16x16x32_bf16 v[128:131],v[76:79],v[44:47],v[128:131]
v_mfma_f32_16x16x32_bf16 v[136:139],v[76:79],v[180:183],v[136:139]
v_mfma_f32_16x16x32_bf16 v[136:139],v[72:75],v[164:167],v[136:139]
buffer_load_dwordx4 a[172:175],v28,s[28:31],s89 offen offset:3072 sc1
v_mfma_f32_16x16x32_bf16 v[136:139],v[68:71],v[148:151],v[136:139]
v_mfma_f32_16x16x32_bf16 v[136:139],v[64:67],v[132:135],v[136:139]
v_mfma_f32_16x16x32_bf16 v[144:147],v[64:67],v[196:199],v[144:147]
v_mfma_f32_16x16x32_bf16 v[144:147],v[68:71],v[212:215],v[144:147]
v_mfma_f32_16x16x32_bf16 v[144:147],v[72:75],v[228:231],v[144:147]
v_mfma_f32_16x16x32_bf16 v[144:147],v[76:79],v[244:247],v[144:147]
v_mfma_f32_16x16x32_bf16 v[152:155],v[76:79],v[188:191],v[152:155]
v_mfma_f32_16x16x32_bf16 v[152:155],v[72:75],v[172:175],v[152:155]
v_mfma_f32_16x16x32_bf16 v[152:155],v[68:71],v[156:159],v[152:155]
v_mfma_f32_16x16x32_bf16 v[152:155],v[64:67],v[140:143],v[152:155]
s_mov_b64 exec,0
s_cmp_eq_u32 s60,0
s_cbranch_scc1 L_pd21_%=
s_cmp_eq_u32 s60,1
s_cbranch_scc1 L_pd22_%=
s_cmp_eq_u32 s60,2
s_cbranch_scc1 L_pd23_%=
s_branch L_trtg2S_%=
L_trtg2S_%=:
s_mov_b64 exec,-1
s_waitcnt lgkmcnt(0)
s_barrier
v_mfma_f32_16x16x32_bf16 v[160:163],v[80:83],v[32:35],v[160:163]
buffer_load_dwordx4 v24,s[48:51],s83 offen lds
v_mfma_f32_16x16x32_bf16 v[160:163],v[84:87],v[36:39],v[160:163]
ds_read_b64_tr_b16 v[64:65],v1
ds_read_b64_tr_b16 v[66:67],v1 offset:5120
v_mfma_f32_16x16x32_bf16 v[160:163],v[88:91],v[40:43],v[160:163]
ds_read_b64_tr_b16 v[68:69],v1 offset:10240
v_mfma_f32_16x16x32_bf16 v[160:163],v[92:95],v[44:47],v[160:163]
ds_read_b64_tr_b16 v[70:71],v1 offset:15360
v_mfma_f32_16x16x32_bf16 v[168:171],v[92:95],v[180:183],v[168:171]
ds_read_b64_tr_b16 v[72:73],v1 offset:20480
v_mfma_f32_16x16x32_bf16 v[168:171],v[88:91],v[164:167],v[168:171]
buffer_load_dwordx4 a[156:159],v28,s[28:31],s88 offen offset:3072 sc1
v_mfma_f32_16x16x32_bf16 v[168:171],v[84:87],v[148:151],v[168:171]
ds_read_b64_tr_b16 v[74:75],v1 offset:25600
ds_read_b64_tr_b16 v[76:77],v1 offset:30720
v_mfma_f32_16x16x32_bf16 v[168:171],v[80:83],v[132:135],v[168:171]
ds_read_b64_tr_b16 v[78:79],v1 offset:35840
v_mfma_f32_16x16x32_bf16 v[176:179],v[80:83],v[196:199],v[176:179]
v_mfma_f32_16x16x32_bf16 v[176:179],v[84:87],v[212:215],v[176:179]
v_mfma_f32_16x16x32_bf16 v[176:179],v[88:91],v[228:231],v[176:179]
v_mfma_f32_16x16x32_bf16 v[176:179],v[92:95],v[244:247],v[176:179]
buffer_load_dwordx4 v25,s[48:51],s83 offen offset:1024 lds
v_mfma_f32_16x16x32_bf16 v[184:187],v[92:95],v[188:191],v[184:187]
ds_read_b64_tr_b16 v[92:93],v2 offset:30752
ds_read_b64_tr_b16 v[94:95],v2 offset:35872
v_mfma_f32_16x16x32_bf16 v[184:187],v[88:91],v[172:175],v[184:187]
ds_read_b64_tr_b16 v[88:89],v2 offset:20512
v_mfma_f32_16x16x32_bf16 v[184:187],v[84:87],v[156:159],v[184:187]
ds_read_b64_tr_b16 v[84:85],v2 offset:10272
v_mfma_f32_16x16x32_bf16 v[184:187],v[80:83],v[140:143],v[184:187]
ds_read_b64_tr_b16 v[80:81],v2 offset:32
v_mfma_f32_16x16x32_bf16 v[192:195],v[96:99],v[32:35],v[192:195]
buffer_load_dwordx4 a[140:143],v28,s[28:31],s87 offen offset:3072 sc1
v_mfma_f32_16x16x32_bf16 v[192:195],v[100:103],v[36:39],v[192:195]
s_add_u32 s87,s64,2
s_and_b32 s87,s87,3
s_lshl_b32 s87,s87,1
v_mfma_f32_16x16x32_bf16 v[192:195],v[104:107],v[40:43],v[192:195]
s_lshl_b32 s87,s87,16
s_add_u32 s88,s87,4096
s_add_u32 s89,s87,8192
v_mfma_f32_16x16x32_bf16 v[192:195],v[108:111],v[44:47],v[192:195]
s_add_u32 s90,s87,12288
ds_read_b64_tr_b16 v[82:83],v2 offset:5152
ds_read_b64_tr_b16 v[86:87],v2 offset:15392
v_mfma_f32_16x16x32_bf16 v[200:203],v[108:111],v[180:183],v[200:203]
ds_read_b64_tr_b16 v[90:91],v2 offset:25632
v_mfma_f32_16x16x32_bf16 v[200:203],v[104:107],v[164:167],v[200:203]
buffer_load_dwordx4 a[68:71],v6,s[28:31],s87 offen sc1
v_mfma_f32_16x16x32_bf16 v[200:203],v[100:103],v[148:151],v[200:203]
v_mfma_f32_16x16x32_bf16 v[200:203],v[96:99],v[132:135],v[200:203]
v_mfma_f32_16x16x32_bf16 v[208:211],v[96:99],v[196:199],v[208:211]
v_mfma_f32_16x16x32_bf16 v[208:211],v[100:103],v[212:215],v[208:211]
v_mfma_f32_16x16x32_bf16 v[208:211],v[104:107],v[228:231],v[208:211]
buffer_load_dwordx4 a[76:79],v6,s[28:31],s88 offen sc1
v_mfma_f32_16x16x32_bf16 v[208:211],v[108:111],v[244:247],v[208:211]
v_mfma_f32_16x16x32_bf16 v[216:219],v[108:111],v[188:191],v[216:219]
ds_read_b64_tr_b16 v[108:109],v1 offset:30784
ds_read_b64_tr_b16 v[110:111],v1 offset:35904
v_mfma_f32_16x16x32_bf16 v[216:219],v[104:107],v[172:175],v[216:219]
ds_read_b64_tr_b16 v[104:105],v1 offset:20544
v_mfma_f32_16x16x32_bf16 v[216:219],v[100:103],v[156:159],v[216:219]
ds_read_b64_tr_b16 v[100:101],v1 offset:10304
v_mfma_f32_16x16x32_bf16 v[216:219],v[96:99],v[140:143],v[216:219]
buffer_load_dwordx4 a[84:87],v6,s[28:31],s89 offen sc1
v_mfma_f32_16x16x32_bf16 v[224:227],v[112:115],v[32:35],v[224:227]
ds_read_b64_tr_b16 v[96:97],v1 offset:64
ds_read_b64_tr_b16 v[98:99],v1 offset:5184
v_mfma_f32_16x16x32_bf16 v[224:227],v[116:119],v[36:39],v[224:227]
ds_read_b64_tr_b16 v[102:103],v1 offset:15424
v_mfma_f32_16x16x32_bf16 v[224:227],v[120:123],v[40:43],v[224:227]
ds_read_b64_tr_b16 v[106:107],v1 offset:25664
v_mfma_f32_16x16x32_bf16 v[224:227],v[124:127],v[44:47],v[224:227]
v_mfma_f32_16x16x32_bf16 v[232:235],v[124:127],v[180:183],v[232:235]
buffer_load_dwordx4 a[92:95],v6,s[28:31],s90 offen sc1
v_mfma_f32_16x16x32_bf16 v[232:235],v[120:123],v[164:167],v[232:235]
v_mfma_f32_16x16x32_bf16 v[232:235],v[116:119],v[148:151],v[232:235]
v_mfma_f32_16x16x32_bf16 v[232:235],v[112:115],v[132:135],v[232:235]
v_mfma_f32_16x16x32_bf16 v[240:243],v[112:115],v[196:199],v[240:243]
v_mfma_f32_16x16x32_bf16 v[240:243],v[116:119],v[212:215],v[240:243]
buffer_load_dwordx4 a[124:127],v6,s[28:31],s90 offen offset:1024 sc1
v_mfma_f32_16x16x32_bf16 v[240:243],v[120:123],v[228:231],v[240:243]
v_mfma_f32_16x16x32_bf16 v[240:243],v[124:127],v[244:247],v[240:243]
v_mfma_f32_16x16x32_bf16 v[248:251],v[124:127],v[188:191],v[248:251]
ds_read_b64_tr_b16 v[124:125],v2 offset:30816
ds_read_b64_tr_b16 v[126:127],v2 offset:35936
v_mfma_f32_16x16x32_bf16 v[248:251],v[120:123],v[172:175],v[248:251]
ds_read_b64_tr_b16 v[120:121],v2 offset:20576
v_mfma_f32_16x16x32_bf16 v[248:251],v[116:119],v[156:159],v[248:251]
buffer_load_dwordx4 a[116:119],v6,s[28:31],s89 offen offset:1024 sc1
v_mfma_f32_16x16x32_bf16 v[248:251],v[112:115],v[140:143],v[248:251]
s_waitcnt vmcnt(31) lgkmcnt(3)
ds_read_b64_tr_b16 v[112:113],v2 offset:96
ds_read_b64_tr_b16 v[114:115],v2 offset:5216
v_mfma_f32_16x16x32_bf16 a[192:195],v[64:67],a[128:131],a[192:195]
ds_read_b64_tr_b16 v[116:117],v2 offset:10336
v_mfma_f32_16x16x32_bf16 a[192:195],v[68:71],a[144:147],a[192:195]
s_waitcnt vmcnt(29)
ds_read_b64_tr_b16 v[118:119],v2 offset:15456
v_mfma_f32_16x16x32_bf16 a[192:195],v[72:75],a[160:163],a[192:195]
s_waitcnt vmcnt(25)
ds_read_b64_tr_b16 v[122:123],v2 offset:25696
v_mfma_f32_16x16x32_bf16 a[192:195],v[76:79],a[176:179],a[192:195]
buffer_load_dwordx4 a[108:111],v6,s[28:31],s88 offen offset:1024 sc1
v_mfma_f32_16x16x32_bf16 a[196:199],v[76:79],a[180:183],a[196:199]
s_waitcnt vmcnt(24)
v_mfma_f32_16x16x32_bf16 a[196:199],v[72:75],a[164:167],a[196:199]
s_waitcnt vmcnt(22)
v_mfma_f32_16x16x32_bf16 a[196:199],v[68:71],a[148:151],a[196:199]
s_waitcnt vmcnt(19)
v_mfma_f32_16x16x32_bf16 a[196:199],v[64:67],a[132:135],a[196:199]
s_waitcnt vmcnt(17)
v_mfma_f32_16x16x32_bf16 a[200:203],v[64:67],a[136:139],a[200:203]
buffer_load_dwordx4 a[100:103],v6,s[28:31],s87 offen offset:1024 sc1
v_mfma_f32_16x16x32_bf16 a[200:203],v[68:71],a[152:155],a[200:203]
s_waitcnt vmcnt(16)
v_mfma_f32_16x16x32_bf16 a[200:203],v[72:75],a[168:171],a[200:203]
s_waitcnt vmcnt(14)
v_mfma_f32_16x16x32_bf16 a[200:203],v[76:79],a[184:187],a[200:203]
s_waitcnt vmcnt(13)
v_mfma_f32_16x16x32_bf16 a[204:207],v[76:79],a[188:191],a[204:207]
s_waitcnt vmcnt(10)
ds_read_b64_tr_b16 v[76:77],v1 offset:30848
ds_read_b64_tr_b16 v[78:79],v1 offset:35968
v_mfma_f32_16x16x32_bf16 a[204:207],v[72:75],a[172:175],a[204:207]
buffer_load_dwordx4 a[64:67],v6,s[28:31],s87 offen offset:2048 sc1
v_mfma_f32_16x16x32_bf16 a[204:207],v[68:71],a[156:159],a[204:207]
s_waitcnt vmcnt(9)
ds_read_b64_tr_b16 v[68:69],v1 offset:10368
ds_read_b64_tr_b16 v[70:71],v1 offset:15488
v_mfma_f32_16x16x32_bf16 a[204:207],v[64:67],a[140:143],a[204:207]
ds_read_b64_tr_b16 v[64:65],v1 offset:128
v_mfma_f32_16x16x32_bf16 a[208:211],v[80:83],a[128:131],a[208:211]
ds_read_b64_tr_b16 v[66:67],v1 offset:5248
v_mfma_f32_16x16x32_bf16 a[208:211],v[84:87],a[144:147],a[208:211]
ds_read_b64_tr_b16 v[72:73],v1 offset:20608
v_mfma_f32_16x16x32_bf16 a[208:211],v[88:91],a[160:163],a[208:211]
ds_read_b64_tr_b16 v[74:75],v1 offset:25728
v_mfma_f32_16x16x32_bf16 a[208:211],v[92:95],a[176:179],a[208:211]
buffer_load_dwordx4 v26,s[48:51],s83 offen offset:2048 lds
v_mfma_f32_16x16x32_bf16 a[212:215],v[92:95],a[180:183],a[212:215]
v_mfma_f32_16x16x32_bf16 a[212:215],v[88:91],a[164:167],a[212:215]
v_mfma_f32_16x16x32_bf16 a[212:215],v[84:87],a[148:151],a[212:215]
v_mfma_f32_16x16x32_bf16 a[212:215],v[80:83],a[132:135],a[212:215]
v_mfma_f32_16x16x32_bf16 a[216:219],v[80:83],a[136:139],a[216:219]
buffer_load_dwordx4 a[72:75],v6,s[28:31],s88 offen offset:2048 sc1
v_mfma_f32_16x16x32_bf16 a[216:219],v[84:87],a[152:155],a[216:219]
v_mfma_f32_16x16x32_bf16 a[216:219],v[88:91],a[168:171],a[216:219]
v_mfma_f32_16x16x32_bf16 a[216:219],v[92:95],a[184:187],a[216:219]
v_mfma_f32_16x16x32_bf16 a[220:223],v[92:95],a[188:191],a[220:223]
ds_read_b64_tr_b16 v[92:93],v2 offset:30880
ds_read_b64_tr_b16 v[94:95],v2 offset:36000
v_mfma_f32_16x16x32_bf16 a[220:223],v[88:91],a[172:175],a[220:223]
ds_read_b64_tr_b16 v[88:89],v2 offset:20640
v_mfma_f32_16x16x32_bf16 a[220:223],v[84:87],a[156:159],a[220:223]
buffer_load_dwordx4 v27,s[48:51],s83 offen offset:3072 lds
)A"
R"A(v_mfma_f32_16x16x32_bf16 a[220:223],v[80:83],a[140:143],a[220:223]
s_add_u32 m0,s86,126976
ds_read_b64_tr_b16 v[80:81],v2 offset:160
ds_read_b64_tr_b16 v[82:83],v2 offset:5280
s_barrier
v_mfma_f32_16x16x32_bf16 a[224:227],v[96:99],a[128:131],a[224:227]
ds_read_b64_tr_b16 v[84:85],v2 offset:10400
v_mfma_f32_16x16x32_bf16 a[224:227],v[100:103],a[144:147],a[224:227]
ds_read_b64_tr_b16 v[86:87],v2 offset:15520
v_mfma_f32_16x16x32_bf16 a[224:227],v[104:107],a[160:163],a[224:227]
buffer_store_dwordx2 v[8:9],v10,s[44:47],s18 offen offset:1280 sc1
v_mfma_f32_16x16x32_bf16 a[224:227],v[108:111],a[176:179],a[224:227]
ds_read_b64_tr_b16 v[90:91],v2 offset:25760
v_mfma_f32_16x16x32_bf16 a[228:231],v[108:111],a[180:183],a[228:231]
v_mfma_f32_16x16x32_bf16 a[228:231],v[104:107],a[164:167],a[228:231]
v_mfma_f32_16x16x32_bf16 a[228:231],v[100:103],a[148:151],a[228:231]
v_mfma_f32_16x16x32_bf16 a[228:231],v[96:99],a[132:135],a[228:231]
buffer_load_dwordx4 a[80:83],v6,s[28:31],s89 offen offset:2048 sc1
v_mfma_f32_16x16x32_bf16 a[232:235],v[96:99],a[136:139],a[232:235]
v_mfma_f32_16x16x32_bf16 a[232:235],v[100:103],a[152:155],a[232:235]
v_mfma_f32_16x16x32_bf16 a[232:235],v[104:107],a[168:171],a[232:235]
v_mfma_f32_16x16x32_bf16 a[232:235],v[108:111],a[184:187],a[232:235]
v_mfma_f32_16x16x32_bf16 a[236:239],v[108:111],a[188:191],a[236:239]
s_waitcnt lgkmcnt(0)
ds_read_b64_tr_b16 v[108:109],v1 offset:30912
ds_read_b64_tr_b16 v[110:111],v1 offset:36032
v_mfma_f32_16x16x32_bf16 a[236:239],v[104:107],a[172:175],a[236:239]
buffer_load_dwordx4 v7,s[48:51],s83 offen lds
v_mfma_f32_16x16x32_bf16 a[236:239],v[100:103],a[156:159],a[236:239]
s_add_u32 m0,s86,128000
ds_read_b64_tr_b16 v[100:101],v1 offset:10432
ds_read_b64_tr_b16 v[102:103],v1 offset:15552
v_mfma_f32_16x16x32_bf16 a[236:239],v[96:99],a[140:143],a[236:239]
s_add_u32 s83,s64,1
s_and_b32 s83,s83,3
ds_read_b64_tr_b16 v[96:97],v1 offset:192
v_mfma_f32_16x16x32_bf16 a[240:243],v[112:115],a[128:131],a[240:243]
s_lshl_b32 s83,s83,1
s_or_b32 s83,s83,1
ds_read_b64_tr_b16 v[98:99],v1 offset:5312
v_mfma_f32_16x16x32_bf16 a[240:243],v[116:119],a[144:147],a[240:243]
s_mul_i32 s83,s83,163840
s_add_u32 s83,s83,s85
ds_read_b64_tr_b16 v[104:105],v1 offset:20672
v_mfma_f32_16x16x32_bf16 a[240:243],v[120:123],a[160:163],a[240:243]
buffer_load_dwordx4 a[88:91],v6,s[28:31],s90 offen offset:2048 sc1
v_mfma_f32_16x16x32_bf16 a[240:243],v[124:127],a[176:179],a[240:243]
ds_read_b64_tr_b16 v[106:107],v1 offset:25792
v_mfma_f32_16x16x32_bf16 a[244:247],v[124:127],a[180:183],a[244:247]
v_mfma_f32_16x16x32_bf16 a[244:247],v[120:123],a[164:167],a[244:247]
v_mfma_f32_16x16x32_bf16 a[244:247],v[116:119],a[148:151],a[244:247]
v_mfma_f32_16x16x32_bf16 a[244:247],v[112:115],a[132:135],a[244:247]
v_mfma_f32_16x16x32_bf16 a[248:251],v[112:115],a[136:139],a[248:251]
buffer_load_dwordx4 v24,s[48:51],s84 offen lds
v_mfma_f32_16x16x32_bf16 a[248:251],v[116:119],a[152:155],a[248:251]
v_mfma_f32_16x16x32_bf16 a[248:251],v[120:123],a[168:171],a[248:251]
v_mfma_f32_16x16x32_bf16 a[248:251],v[124:127],a[184:187],a[248:251]
v_mfma_f32_16x16x32_bf16 a[252:255],v[124:127],a[188:191],a[252:255]
ds_read_b64_tr_b16 v[124:125],v2 offset:30944
ds_read_b64_tr_b16 v[126:127],v2 offset:36064
v_mfma_f32_16x16x32_bf16 a[252:255],v[120:123],a[172:175],a[252:255]
buffer_load_dwordx4 a[120:123],v28,s[28:31],s90 offen offset:3072 sc1
v_mfma_f32_16x16x32_bf16 a[252:255],v[116:119],a[156:159],a[252:255]
ds_read_b64_tr_b16 v[116:117],v2 offset:10464
ds_read_b64_tr_b16 v[118:119],v2 offset:15584
v_mfma_f32_16x16x32_bf16 a[252:255],v[112:115],a[140:143],a[252:255]
ds_read_b64_tr_b16 v[112:113],v2 offset:224
v_mfma_f32_16x16x32_bf16 a[0:3],v[64:67],a[128:131],a[0:3]
ds_read_b64_tr_b16 v[114:115],v2 offset:5344
v_mfma_f32_16x16x32_bf16 a[0:3],v[68:71],a[144:147],a[0:3]
ds_read_b64_tr_b16 v[120:121],v2 offset:20704
v_mfma_f32_16x16x32_bf16 a[0:3],v[72:75],a[160:163],a[0:3]
ds_read_b64_tr_b16 v[122:123],v2 offset:25824
v_mfma_f32_16x16x32_bf16 a[0:3],v[76:79],a[176:179],a[0:3]
buffer_load_dwordx4 v25,s[48:51],s84 offen offset:1024 lds
v_mfma_f32_16x16x32_bf16 a[4:7],v[76:79],a[180:183],a[4:7]
v_mfma_f32_16x16x32_bf16 a[4:7],v[72:75],a[164:167],a[4:7]
v_mfma_f32_16x16x32_bf16 a[4:7],v[68:71],a[148:151],a[4:7]
v_mfma_f32_16x16x32_bf16 a[4:7],v[64:67],a[132:135],a[4:7]
v_mfma_f32_16x16x32_bf16 a[8:11],v[64:67],a[136:139],a[8:11]
buffer_load_dwordx4 a[112:115],v28,s[28:31],s89 offen offset:3072 sc1
v_mfma_f32_16x16x32_bf16 a[8:11],v[68:71],a[152:155],a[8:11]
v_mfma_f32_16x16x32_bf16 a[8:11],v[72:75],a[168:171],a[8:11]
v_mfma_f32_16x16x32_bf16 a[8:11],v[76:79],a[184:187],a[8:11]
v_mfma_f32_16x16x32_bf16 a[12:15],v[76:79],a[188:191],a[12:15]
ds_read_b64_tr_b16 v[76:77],v1 offset:30976
ds_read_b64_tr_b16 v[78:79],v1 offset:36096
v_mfma_f32_16x16x32_bf16 a[12:15],v[72:75],a[172:175],a[12:15]
buffer_load_dwordx4 a[104:107],v28,s[28:31],s88 offen offset:3072 sc1
v_mfma_f32_16x16x32_bf16 a[12:15],v[68:71],a[156:159],a[12:15]
ds_read_b64_tr_b16 v[68:69],v1 offset:10496
ds_read_b64_tr_b16 v[70:71],v1 offset:15616
v_mfma_f32_16x16x32_bf16 a[12:15],v[64:67],a[140:143],a[12:15]
ds_read_b64_tr_b16 v[64:65],v1 offset:256
v_mfma_f32_16x16x32_bf16 a[16:19],v[80:83],a[128:131],a[16:19]
ds_read_b64_tr_b16 v[66:67],v1 offset:5376
v_mfma_f32_16x16x32_bf16 a[16:19],v[84:87],a[144:147],a[16:19]
ds_read_b64_tr_b16 v[72:73],v1 offset:20736
v_mfma_f32_16x16x32_bf16 a[16:19],v[88:91],a[160:163],a[16:19]
ds_read_b64_tr_b16 v[74:75],v1 offset:25856
v_add_u32 v1,0xffff6000,v1
v_mfma_f32_16x16x32_bf16 a[16:19],v[92:95],a[176:179],a[16:19]
buffer_load_dwordx4 v26,s[48:51],s84 offen offset:2048 lds
v_mfma_f32_16x16x32_bf16 a[20:23],v[92:95],a[180:183],a[20:23]
v_mfma_f32_16x16x32_bf16 a[20:23],v[88:91],a[164:167],a[20:23]
v_mfma_f32_16x16x32_bf16 a[20:23],v[84:87],a[148:151],a[20:23]
v_mfma_f32_16x16x32_bf16 a[20:23],v[80:83],a[132:135],a[20:23]
v_mfma_f32_16x16x32_bf16 a[24:27],v[80:83],a[136:139],a[24:27]
buffer_load_dwordx4 a[96:99],v28,s[28:31],s87 offen offset:3072 sc1
v_mfma_f32_16x16x32_bf16 a[24:27],v[84:87],a[152:155],a[24:27]
s_add_u32 s87,s64,3
s_and_b32 s87,s87,3
s_lshl_b32 s87,s87,1
v_mfma_f32_16x16x32_bf16 a[24:27],v[88:91],a[168:171],a[24:27]
s_lshl_b32 s87,s87,16
s_add_u32 s88,s87,4096
s_add_u32 s89,s87,8192
v_mfma_f32_16x16x32_bf16 a[24:27],v[92:95],a[184:187],a[24:27]
s_add_u32 s90,s87,12288
v_mfma_f32_16x16x32_bf16 a[28:31],v[92:95],a[188:191],a[28:31]
ds_read_b64_tr_b16 v[92:93],v2 offset:31008
ds_read_b64_tr_b16 v[94:95],v2 offset:36128
v_mfma_f32_16x16x32_bf16 a[28:31],v[88:91],a[172:175],a[28:31]
s_waitcnt lgkmcnt(2)
ds_read_b64_tr_b16 v[88:89],v2 offset:20768
v_mfma_f32_16x16x32_bf16 a[28:31],v[84:87],a[156:159],a[28:31]
buffer_load_dwordx4 v27,s[48:51],s84 offen offset:3072 lds
v_mfma_f32_16x16x32_bf16 a[28:31],v[80:83],a[140:143],a[28:31]
s_add_u32 m0,s86,132096
ds_read_b64_tr_b16 v[80:81],v2 offset:288
ds_read_b64_tr_b16 v[82:83],v2 offset:5408
v_mfma_f32_16x16x32_bf16 v[128:131],v[96:99],a[128:131],v[128:131]
ds_read_b64_tr_b16 v[84:85],v2 offset:10528
v_mfma_f32_16x16x32_bf16 v[128:131],v[100:103],a[144:147],v[128:131]
ds_read_b64_tr_b16 v[86:87],v2 offset:15648
v_mfma_f32_16x16x32_bf16 v[128:131],v[104:107],a[160:163],v[128:131]
ds_read_b64_tr_b16 v[90:91],v2 offset:25888
v_add_u32 v2,0xffff6000,v2
v_mfma_f32_16x16x32_bf16 v[128:131],v[108:111],a[176:179],v[128:131]
v_mfma_f32_16x16x32_bf16 v[136:139],v[108:111],a[180:183],v[136:139]
buffer_load_dwordx4 v7,s[48:51],s84 offen lds
v_mfma_f32_16x16x32_bf16 v[136:139],v[104:107],a[164:167],v[136:139]
s_add_u32 m0,s86,81920
s_add_u32 s84,s83,20480
v_mfma_f32_16x16x32_bf16 v[136:139],v[100:103],a[148:151],v[136:139]
v_mfma_f32_16x16x32_bf16 v[136:139],v[96:99],a[132:135],v[136:139]
v_mfma_f32_16x16x32_bf16 v[144:147],v[96:99],a[136:139],v[144:147]
v_mfma_f32_16x16x32_bf16 v[144:147],v[100:103],a[152:155],v[144:147]
v_mfma_f32_16x16x32_bf16 v[144:147],v[104:107],a[168:171],v[144:147]
buffer_load_dwordx4 v24,s[48:51],s83 offen lds
v_mfma_f32_16x16x32_bf16 v[144:147],v[108:111],a[184:187],v[144:147]
v_mfma_f32_16x16x32_bf16 v[152:155],v[108:111],a[188:191],v[152:155]
ds_read_b64_tr_b16 v[108:109],v1 offset:30720
ds_read_b64_tr_b16 v[110:111],v1 offset:35840
v_mfma_f32_16x16x32_bf16 v[152:155],v[104:107],a[172:175],v[152:155]
ds_read_b64_tr_b16 v[104:105],v1 offset:20480
v_mfma_f32_16x16x32_bf16 v[152:155],v[100:103],a[156:159],v[152:155]
ds_read_b64_tr_b16 v[100:101],v1 offset:10240
v_mfma_f32_16x16x32_bf16 v[152:155],v[96:99],a[140:143],v[152:155]
ds_read_b64_tr_b16 v[96:97],v1
v_mfma_f32_16x16x32_bf16 v[160:163],v[112:115],a[128:131],v[160:163]
buffer_load_dwordx4 v25,s[48:51],s83 offen offset:1024 lds
v_mfma_f32_16x16x32_bf16 v[160:163],v[116:119],a[144:147],v[160:163]
ds_read_b64_tr_b16 v[98:99],v1 offset:5120
ds_read_b64_tr_b16 v[102:103],v1 offset:15360
v_mfma_f32_16x16x32_bf16 v[160:163],v[120:123],a[160:163],v[160:163]
ds_read_b64_tr_b16 v[106:107],v1 offset:25600
v_mfma_f32_16x16x32_bf16 v[160:163],v[124:127],a[176:179],v[160:163]
v_mfma_f32_16x16x32_bf16 v[168:171],v[124:127],a[180:183],v[168:171]
v_mfma_f32_16x16x32_bf16 v[168:171],v[120:123],a[164:167],v[168:171]
v_mfma_f32_16x16x32_bf16 v[168:171],v[116:119],a[148:151],v[168:171]
buffer_load_dwordx4 v26,s[48:51],s83 offen offset:2048 lds
v_mfma_f32_16x16x32_bf16 v[168:171],v[112:115],a[132:135],v[168:171]
v_mfma_f32_16x16x32_bf16 v[176:179],v[112:115],a[136:139],v[176:179]
v_mfma_f32_16x16x32_bf16 v[176:179],v[116:119],a[152:155],v[176:179]
v_mfma_f32_16x16x32_bf16 v[176:179],v[120:123],a[168:171],v[176:179]
v_mfma_f32_16x16x32_bf16 v[176:179],v[124:127],a[184:187],v[176:179]
v_mfma_f32_16x16x32_bf16 v[184:187],v[124:127],a[188:191],v[184:187]
buffer_load_dwordx4 v27,s[48:51],s83 offen offset:3072 lds
v_mfma_f32_16x16x32_bf16 v[184:187],v[120:123],a[172:175],v[184:187]
s_add_u32 m0,s86,86016
ds_read_b64_tr_b16 v[120:121],v2 offset:20512
ds_read_b64_tr_b16 v[122:123],v2 offset:25632
v_mfma_f32_16x16x32_bf16 v[184:187],v[116:119],a[156:159],v[184:187]
ds_read_b64_tr_b16 v[116:117],v2 offset:10272
v_mfma_f32_16x16x32_bf16 v[184:187],v[112:115],a[140:143],v[184:187]
ds_read_b64_tr_b16 v[112:113],v2 offset:32
L_pd21_%=:
s_cbranch_execz L_trtg2S_%=
v_mfma_f32_16x16x32_bf16 v[192:195],v[64:67],a[128:131],v[192:195]
ds_read_b64_tr_b16 v[114:115],v2 offset:5152
v_mfma_f32_16x16x32_bf16 v[192:195],v[68:71],a[144:147],v[192:195]
ds_read_b64_tr_b16 v[118:119],v2 offset:15392
ds_read_b64_tr_b16 v[124:125],v2 offset:30752
v_mfma_f32_16x16x32_bf16 v[192:195],v[72:75],a[160:163],v[192:195]
buffer_load_dwordx4 v7,s[48:51],s83 offen lds
v_mfma_f32_16x16x32_bf16 v[192:195],v[76:79],a[176:179],v[192:195]
s_add_u32 m0,s86,87040
s_add_u32 s83,s64,2
ds_read_b64_tr_b16 v[126:127],v2 offset:35872
v_mfma_f32_16x16x32_bf16 v[200:203],v[76:79],a[180:183],v[200:203]
s_and_b32 s83,s83,3
s_lshl_b32 s83,s83,1
s_or_b32 s83,s83,1
v_mfma_f32_16x16x32_bf16 v[200:203],v[72:75],a[164:167],v[200:203]
s_mul_i32 s83,s83,163840
s_add_u32 s83,s83,s85
v_mfma_f32_16x16x32_bf16 v[200:203],v[68:71],a[148:151],v[200:203]
v_mfma_f32_16x16x32_bf16 v[200:203],v[64:67],a[132:135],v[200:203]
v_mfma_f32_16x16x32_bf16 v[208:211],v[64:67],a[136:139],v[208:211]
buffer_load_dwordx4 v24,s[48:51],s84 offen lds
v_mfma_f32_16x16x32_bf16 v[208:211],v[68:71],a[152:155],v[208:211]
v_mfma_f32_16x16x32_bf16 v[208:211],v[72:75],a[168:171],v[208:211]
v_mfma_f32_16x16x32_bf16 v[208:211],v[76:79],a[184:187],v[208:211]
v_mfma_f32_16x16x32_bf16 v[216:219],v[76:79],a[188:191],v[216:219]
ds_read_b64_tr_b16 v[76:77],v1 offset:30784
ds_read_b64_tr_b16 v[78:79],v1 offset:35904
v_mfma_f32_16x16x32_bf16 v[216:219],v[72:75],a[172:175],v[216:219]
ds_read_b64_tr_b16 v[72:73],v1 offset:20544
v_mfma_f32_16x16x32_bf16 v[216:219],v[68:71],a[156:159],v[216:219]
buffer_load_dwordx2 v[4:5],v3,s[44:47],s91 offen offset:1280 sc1
v_mfma_f32_16x16x32_bf16 v[216:219],v[64:67],a[140:143],v[216:219]
s_waitcnt lgkmcnt(3)
ds_read_b64_tr_b16 v[64:65],v1 offset:64
ds_read_b64_tr_b16 v[66:67],v1 offset:5184
)A"
R"A(v_mfma_f32_16x16x32_bf16 v[224:227],v[80:83],a[128:131],v[224:227]
ds_read_b64_tr_b16 v[68:69],v1 offset:10304
v_mfma_f32_16x16x32_bf16 v[224:227],v[84:87],a[144:147],v[224:227]
ds_read_b64_tr_b16 v[70:71],v1 offset:15424
v_mfma_f32_16x16x32_bf16 v[224:227],v[88:91],a[160:163],v[224:227]
ds_read_b64_tr_b16 v[74:75],v1 offset:25664
v_mfma_f32_16x16x32_bf16 v[224:227],v[92:95],a[176:179],v[224:227]
v_mfma_f32_16x16x32_bf16 v[232:235],v[92:95],a[180:183],v[232:235]
buffer_load_dwordx4 v25,s[48:51],s84 offen offset:1024 lds
v_mfma_f32_16x16x32_bf16 v[232:235],v[88:91],a[164:167],v[232:235]
v_mfma_f32_16x16x32_bf16 v[232:235],v[84:87],a[148:151],v[232:235]
v_mfma_f32_16x16x32_bf16 v[232:235],v[80:83],a[132:135],v[232:235]
v_mfma_f32_16x16x32_bf16 v[240:243],v[80:83],a[136:139],v[240:243]
v_mfma_f32_16x16x32_bf16 v[240:243],v[84:87],a[152:155],v[240:243]
v_mfma_f32_16x16x32_bf16 v[240:243],v[88:91],a[168:171],v[240:243]
buffer_load_dwordx4 v26,s[48:51],s84 offen offset:2048 lds
v_mfma_f32_16x16x32_bf16 v[240:243],v[92:95],a[184:187],v[240:243]
v_mfma_f32_16x16x32_bf16 v[248:251],v[92:95],a[188:191],v[248:251]
ds_read_b64_tr_b16 v[92:93],v2 offset:30816
ds_read_b64_tr_b16 v[94:95],v2 offset:35936
v_mfma_f32_16x16x32_bf16 v[248:251],v[88:91],a[172:175],v[248:251]
ds_read_b64_tr_b16 v[88:89],v2 offset:20576
v_mfma_f32_16x16x32_bf16 v[248:251],v[84:87],a[156:159],v[248:251]
ds_read_b64_tr_b16 v[84:85],v2 offset:10336
v_mfma_f32_16x16x32_bf16 v[248:251],v[80:83],a[140:143],v[248:251]
s_waitcnt vmcnt(25)
ds_read_b64_tr_b16 v[80:81],v2 offset:96
v_mfma_f32_16x16x32_bf16 a[192:195],v[96:99],a[68:71],a[192:195]
buffer_load_dwordx4 v27,s[48:51],s84 offen offset:3072 lds
v_mfma_f32_16x16x32_bf16 a[192:195],v[100:103],a[76:79],a[192:195]
s_add_u32 m0,s86,91136
ds_read_b64_tr_b16 v[82:83],v2 offset:5216
ds_read_b64_tr_b16 v[86:87],v2 offset:15456
v_mfma_f32_16x16x32_bf16 a[192:195],v[104:107],a[84:87],a[192:195]
ds_read_b64_tr_b16 v[90:91],v2 offset:25696
v_mfma_f32_16x16x32_bf16 a[192:195],v[108:111],a[92:95],a[192:195]
v_mfma_f32_16x16x32_bf16 a[196:199],v[108:111],a[124:127],a[196:199]
v_mfma_f32_16x16x32_bf16 a[196:199],v[104:107],a[116:119],a[196:199]
v_mfma_f32_16x16x32_bf16 a[196:199],v[100:103],a[108:111],a[196:199]
buffer_load_dwordx4 v7,s[48:51],s84 offen lds
v_mfma_f32_16x16x32_bf16 a[196:199],v[96:99],a[100:103],a[196:199]
s_add_u32 m0,s86,40960
s_add_u32 s84,s83,20480
v_mfma_f32_16x16x32_bf16 a[200:203],v[96:99],a[64:67],a[200:203]
s_waitcnt vmcnt(25)
v_mfma_f32_16x16x32_bf16 a[200:203],v[100:103],a[72:75],a[200:203]
s_waitcnt vmcnt(22)
v_mfma_f32_16x16x32_bf16 a[200:203],v[104:107],a[80:83],a[200:203]
s_waitcnt vmcnt(18)
v_mfma_f32_16x16x32_bf16 a[200:203],v[108:111],a[88:91],a[200:203]
buffer_load_dwordx4 v[32:35],v6,s[28:31],s87 offen sc1
v_mfma_f32_16x16x32_bf16 a[204:207],v[108:111],a[120:123],a[204:207]
s_waitcnt vmcnt(17)
ds_read_b64_tr_b16 v[108:109],v1 offset:30848
ds_read_b64_tr_b16 v[110:111],v1 offset:35968
v_mfma_f32_16x16x32_bf16 a[204:207],v[104:107],a[112:115],a[204:207]
s_waitcnt vmcnt(16)
ds_read_b64_tr_b16 v[104:105],v1 offset:20608
v_mfma_f32_16x16x32_bf16 a[204:207],v[100:103],a[104:107],a[204:207]
s_waitcnt vmcnt(14)
ds_read_b64_tr_b16 v[100:101],v1 offset:10368
v_mfma_f32_16x16x32_bf16 a[204:207],v[96:99],a[96:99],a[204:207]
ds_read_b64_tr_b16 v[96:97],v1 offset:128
v_mfma_f32_16x16x32_bf16 a[208:211],v[112:115],a[68:71],a[208:211]
buffer_load_dwordx4 v[132:135],v6,s[28:31],s87 offen offset:1024 sc1
v_mfma_f32_16x16x32_bf16 a[208:211],v[116:119],a[76:79],a[208:211]
ds_read_b64_tr_b16 v[98:99],v1 offset:5248
ds_read_b64_tr_b16 v[102:103],v1 offset:15488
v_mfma_f32_16x16x32_bf16 a[208:211],v[120:123],a[84:87],a[208:211]
ds_read_b64_tr_b16 v[106:107],v1 offset:25728
v_mfma_f32_16x16x32_bf16 a[208:211],v[124:127],a[92:95],a[208:211]
v_mfma_f32_16x16x32_bf16 a[212:215],v[124:127],a[124:127],a[212:215]
v_mfma_f32_16x16x32_bf16 a[212:215],v[120:123],a[116:119],a[212:215]
buffer_load_dwordx4 v[196:199],v6,s[28:31],s87 offen offset:2048 sc1
v_mfma_f32_16x16x32_bf16 a[212:215],v[116:119],a[108:111],a[212:215]
v_mfma_f32_16x16x32_bf16 a[212:215],v[112:115],a[100:103],a[212:215]
v_mfma_f32_16x16x32_bf16 a[216:219],v[112:115],a[64:67],a[216:219]
v_mfma_f32_16x16x32_bf16 a[216:219],v[116:119],a[72:75],a[216:219]
v_mfma_f32_16x16x32_bf16 a[216:219],v[120:123],a[80:83],a[216:219]
buffer_load_dwordx4 v[36:39],v6,s[28:31],s88 offen sc1
v_mfma_f32_16x16x32_bf16 a[216:219],v[124:127],a[88:91],a[216:219]
v_mfma_f32_16x16x32_bf16 a[220:223],v[124:127],a[120:123],a[220:223]
ds_read_b64_tr_b16 v[124:125],v2 offset:30880
ds_read_b64_tr_b16 v[126:127],v2 offset:36000
v_mfma_f32_16x16x32_bf16 a[220:223],v[120:123],a[112:115],a[220:223]
ds_read_b64_tr_b16 v[120:121],v2 offset:20640
v_mfma_f32_16x16x32_bf16 a[220:223],v[116:119],a[104:107],a[220:223]
s_waitcnt lgkmcnt(3)
ds_read_b64_tr_b16 v[116:117],v2 offset:10400
v_mfma_f32_16x16x32_bf16 a[220:223],v[112:115],a[96:99],a[220:223]
buffer_load_dwordx4 v[140:143],v28,s[28:31],s87 offen offset:3072 sc1
v_mfma_f32_16x16x32_bf16 a[224:227],v[64:67],a[68:71],a[224:227]
s_add_u32 s87,s64,1
s_and_b32 s87,s87,3
s_lshl_b32 s87,s87,1
v_mfma_f32_16x16x32_bf16 a[224:227],v[68:71],a[76:79],a[224:227]
s_or_b32 s87,s87,1
s_lshl_b32 s87,s87,16
ds_read_b64_tr_b16 v[112:113],v2 offset:160
v_mfma_f32_16x16x32_bf16 a[224:227],v[72:75],a[84:87],a[224:227]
ds_read_b64_tr_b16 v[114:115],v2 offset:5280
ds_read_b64_tr_b16 v[118:119],v2 offset:15520
v_mfma_f32_16x16x32_bf16 a[224:227],v[76:79],a[92:95],a[224:227]
ds_read_b64_tr_b16 v[122:123],v2 offset:25760
v_mfma_f32_16x16x32_bf16 a[228:231],v[76:79],a[124:127],a[228:231]
buffer_load_dwordx4 v[212:215],v6,s[28:31],s88 offen offset:2048 sc1
v_mfma_f32_16x16x32_bf16 a[228:231],v[72:75],a[116:119],a[228:231]
v_mfma_f32_16x16x32_bf16 a[228:231],v[68:71],a[108:111],a[228:231]
v_mfma_f32_16x16x32_bf16 a[228:231],v[64:67],a[100:103],a[228:231]
v_mfma_f32_16x16x32_bf16 a[232:235],v[64:67],a[64:67],a[232:235]
v_mfma_f32_16x16x32_bf16 a[232:235],v[68:71],a[72:75],a[232:235]
buffer_load_dwordx4 v[148:151],v6,s[28:31],s88 offen offset:1024 sc1
v_mfma_f32_16x16x32_bf16 a[232:235],v[72:75],a[80:83],a[232:235]
s_waitcnt vmcnt(11)
v_cmp_ne_u32 s[56:57],s52,v4
v_cmp_ne_u32 s[58:59],s53,v5
v_mfma_f32_16x16x32_bf16 a[232:235],v[76:79],a[88:91],a[232:235]
s_or_b64 s[56:57],s[56:57],s[58:59]
s_cbranch_scc1 L_fr7_%=
L_fo7_%=:
s_lshl_b32 s91,s64,6
v_mfma_f32_16x16x32_bf16 a[236:239],v[76:79],a[120:123],a[236:239]
ds_read_b64_tr_b16 v[76:77],v1 offset:30912
ds_read_b64_tr_b16 v[78:79],v1 offset:36032
v_mfma_f32_16x16x32_bf16 a[236:239],v[72:75],a[112:115],a[236:239]
ds_read_b64_tr_b16 v[72:73],v1 offset:20672
v_bfe_u32 v3,v6,4,2
v_mfma_f32_16x16x32_bf16 a[236:239],v[68:71],a[104:107],a[236:239]
buffer_load_dwordx4 v[40:43],v6,s[28:31],s89 offen sc1
v_mfma_f32_16x16x32_bf16 a[236:239],v[64:67],a[96:99],a[236:239]
ds_read_b64_tr_b16 v[64:65],v1 offset:192
ds_read_b64_tr_b16 v[66:67],v1 offset:5312
v_mfma_f32_16x16x32_bf16 a[240:243],v[80:83],a[68:71],a[240:243]
ds_read_b64_tr_b16 v[68:69],v1 offset:10432
v_min_u32 v3,2,v3
v_mfma_f32_16x16x32_bf16 a[240:243],v[84:87],a[76:79],a[240:243]
ds_read_b64_tr_b16 v[70:71],v1 offset:15552
v_add3_u32 v3,v3,s63,1
v_mfma_f32_16x16x32_bf16 a[240:243],v[88:91],a[84:87],a[240:243]
ds_read_b64_tr_b16 v[74:75],v1 offset:25792
v_and_b32 v3,3,v3
v_mfma_f32_16x16x32_bf16 a[240:243],v[92:95],a[92:95],a[240:243]
buffer_load_dwordx4 a[128:131],v6,s[28:31],s87 offen sc1
v_mfma_f32_16x16x32_bf16 a[244:247],v[92:95],a[124:127],a[244:247]
v_lshlrev_b32 v3,8,v3
v_mfma_f32_16x16x32_bf16 a[244:247],v[88:91],a[116:119],a[244:247]
v_mfma_f32_16x16x32_bf16 a[244:247],v[84:87],a[108:111],a[244:247]
v_mfma_f32_16x16x32_bf16 a[244:247],v[80:83],a[100:103],a[244:247]
v_mfma_f32_16x16x32_bf16 a[248:251],v[80:83],a[64:67],a[248:251]
buffer_load_dwordx4 v[156:159],v28,s[28:31],s88 offen offset:3072 sc1
v_mfma_f32_16x16x32_bf16 a[248:251],v[84:87],a[72:75],a[248:251]
s_add_u32 s88,s87,4096
v_mfma_f32_16x16x32_bf16 a[248:251],v[88:91],a[80:83],a[248:251]
v_mfma_f32_16x16x32_bf16 a[248:251],v[92:95],a[88:91],a[248:251]
v_mfma_f32_16x16x32_bf16 a[252:255],v[92:95],a[120:123],a[252:255]
ds_read_b64_tr_b16 v[92:93],v2 offset:30944
ds_read_b64_tr_b16 v[94:95],v2 offset:36064
v_mfma_f32_16x16x32_bf16 a[252:255],v[88:91],a[112:115],a[252:255]
buffer_load_dwordx4 v[228:231],v6,s[28:31],s89 offen offset:2048 sc1
v_mfma_f32_16x16x32_bf16 a[252:255],v[84:87],a[104:107],a[252:255]
ds_read_b64_tr_b16 v[84:85],v2 offset:10464
ds_read_b64_tr_b16 v[86:87],v2 offset:15584
v_mfma_f32_16x16x32_bf16 a[252:255],v[80:83],a[96:99],a[252:255]
ds_read_b64_tr_b16 v[80:81],v2 offset:224
v_mfma_f32_16x16x32_bf16 a[0:3],v[96:99],a[68:71],a[0:3]
ds_read_b64_tr_b16 v[82:83],v2 offset:5344
v_mfma_f32_16x16x32_bf16 a[0:3],v[100:103],a[76:79],a[0:3]
ds_read_b64_tr_b16 v[88:89],v2 offset:20704
v_mfma_f32_16x16x32_bf16 a[0:3],v[104:107],a[84:87],a[0:3]
buffer_load_dwordx4 v[164:167],v6,s[28:31],s89 offen offset:1024 sc1
v_mfma_f32_16x16x32_bf16 a[0:3],v[108:111],a[92:95],a[0:3]
ds_read_b64_tr_b16 v[90:91],v2 offset:25824
v_mfma_f32_16x16x32_bf16 a[4:7],v[108:111],a[124:127],a[4:7]
v_mfma_f32_16x16x32_bf16 a[4:7],v[104:107],a[116:119],a[4:7]
v_mfma_f32_16x16x32_bf16 a[4:7],v[100:103],a[108:111],a[4:7]
v_mfma_f32_16x16x32_bf16 a[4:7],v[96:99],a[100:103],a[4:7]
buffer_load_dwordx4 v[44:47],v6,s[28:31],s90 offen sc1
v_mfma_f32_16x16x32_bf16 a[8:11],v[96:99],a[64:67],a[8:11]
v_mfma_f32_16x16x32_bf16 a[8:11],v[100:103],a[72:75],a[8:11]
v_mfma_f32_16x16x32_bf16 a[8:11],v[104:107],a[80:83],a[8:11]
v_mfma_f32_16x16x32_bf16 a[8:11],v[108:111],a[88:91],a[8:11]
v_mfma_f32_16x16x32_bf16 a[12:15],v[108:111],a[120:123],a[12:15]
buffer_load_dwordx4 a[144:147],v6,s[28:31],s88 offen sc1
v_mfma_f32_16x16x32_bf16 a[12:15],v[104:107],a[112:115],a[12:15]
ds_read_b64_tr_b16 v[104:105],v1 offset:20736
ds_read_b64_tr_b16 v[106:107],v1 offset:25856
v_mfma_f32_16x16x32_bf16 a[12:15],v[100:103],a[104:107],a[12:15]
ds_read_b64_tr_b16 v[100:101],v1 offset:10496
v_mfma_f32_16x16x32_bf16 a[12:15],v[96:99],a[96:99],a[12:15]
s_waitcnt lgkmcnt(3)
ds_read_b64_tr_b16 v[96:97],v1 offset:256
v_mfma_f32_16x16x32_bf16 a[16:19],v[112:115],a[68:71],a[16:19]
ds_read_b64_tr_b16 v[98:99],v1 offset:5376
v_mfma_f32_16x16x32_bf16 a[16:19],v[116:119],a[76:79],a[16:19]
buffer_load_dwordx4 v[172:175],v28,s[28:31],s89 offen offset:3072 sc1
v_mfma_f32_16x16x32_bf16 a[16:19],v[120:123],a[84:87],a[16:19]
s_add_u32 s89,s87,8192
ds_read_b64_tr_b16 v[102:103],v1 offset:15616
ds_read_b64_tr_b16 v[108:109],v1 offset:30976
v_mfma_f32_16x16x32_bf16 a[16:19],v[124:127],a[92:95],a[16:19]
ds_read_b64_tr_b16 v[110:111],v1 offset:36096
v_add_u32 v1,0x1e000,v1
v_mfma_f32_16x16x32_bf16 a[20:23],v[124:127],a[124:127],a[20:23]
v_mfma_f32_16x16x32_bf16 a[20:23],v[120:123],a[116:119],a[20:23]
v_mfma_f32_16x16x32_bf16 a[20:23],v[116:119],a[108:111],a[20:23]
buffer_load_dwordx4 v[180:183],v6,s[28:31],s90 offen offset:1024 sc1
v_mfma_f32_16x16x32_bf16 a[20:23],v[112:115],a[100:103],a[20:23]
v_mfma_f32_16x16x32_bf16 a[24:27],v[112:115],a[64:67],a[24:27]
v_mfma_f32_16x16x32_bf16 a[24:27],v[116:119],a[72:75],a[24:27]
v_mfma_f32_16x16x32_bf16 a[24:27],v[120:123],a[80:83],a[24:27]
v_mfma_f32_16x16x32_bf16 a[24:27],v[124:127],a[88:91],a[24:27]
buffer_load_dwordx4 a[160:163],v6,s[28:31],s89 offen sc1
v_mfma_f32_16x16x32_bf16 a[28:31],v[124:127],a[120:123],a[28:31]
ds_read_b64_tr_b16 v[124:125],v2 offset:31008
ds_read_b64_tr_b16 v[126:127],v2 offset:36128
v_mfma_f32_16x16x32_bf16 a[28:31],v[120:123],a[112:115],a[28:31]
ds_read_b64_tr_b16 v[120:121],v2 offset:20768
v_mfma_f32_16x16x32_bf16 a[28:31],v[116:119],a[104:107],a[28:31]
ds_read_b64_tr_b16 v[116:117],v2 offset:10528
v_mfma_f32_16x16x32_bf16 a[28:31],v[112:115],a[96:99],a[28:31]
ds_read_b64_tr_b16 v[112:113],v2 offset:288
v_mfma_f32_16x16x32_bf16 v[128:131],v[64:67],a[68:71],v[128:131]
buffer_load_dwordx4 v[244:247],v6,s[28:31],s90 offen offset:2048 sc1
v_mfma_f32_16x16x32_bf16 v[128:131],v[68:71],a[76:79],v[128:131]
)A"
R"A(ds_read_b64_tr_b16 v[114:115],v2 offset:5408
ds_read_b64_tr_b16 v[118:119],v2 offset:15648
v_mfma_f32_16x16x32_bf16 v[128:131],v[72:75],a[84:87],v[128:131]
ds_read_b64_tr_b16 v[122:123],v2 offset:25888
v_add_u32 v2,0x1e000,v2
v_mfma_f32_16x16x32_bf16 v[128:131],v[76:79],a[92:95],v[128:131]
v_mfma_f32_16x16x32_bf16 v[136:139],v[76:79],a[124:127],v[136:139]
v_mfma_f32_16x16x32_bf16 v[136:139],v[72:75],a[116:119],v[136:139]
buffer_load_dwordx4 v[188:191],v28,s[28:31],s90 offen offset:3072 sc1
v_mfma_f32_16x16x32_bf16 v[136:139],v[68:71],a[108:111],v[136:139]
s_add_u32 s90,s87,12288
v_mfma_f32_16x16x32_bf16 v[136:139],v[64:67],a[100:103],v[136:139]
v_mfma_f32_16x16x32_bf16 v[144:147],v[64:67],a[64:67],v[144:147]
v_mfma_f32_16x16x32_bf16 v[144:147],v[68:71],a[72:75],v[144:147]
v_mfma_f32_16x16x32_bf16 v[144:147],v[72:75],a[80:83],v[144:147]
buffer_load_dwordx4 a[176:179],v6,s[28:31],s90 offen sc1
v_mfma_f32_16x16x32_bf16 v[144:147],v[76:79],a[88:91],v[144:147]
v_mfma_f32_16x16x32_bf16 v[152:155],v[76:79],a[120:123],v[152:155]
v_mfma_f32_16x16x32_bf16 v[152:155],v[72:75],a[112:115],v[152:155]
v_mfma_f32_16x16x32_bf16 v[152:155],v[68:71],a[104:107],v[152:155]
v_mfma_f32_16x16x32_bf16 v[152:155],v[64:67],a[96:99],v[152:155]
buffer_load_dwordx4 a[180:183],v6,s[28:31],s90 offen offset:1024 sc1
s_waitcnt vmcnt(21)
s_barrier
v_mfma_f32_16x16x32_bf16 v[160:163],v[80:83],a[68:71],v[160:163]
ds_read_b64_tr_b16 v[64:65],v1
ds_read_b64_tr_b16 v[66:67],v1 offset:5120
v_mfma_f32_16x16x32_bf16 v[160:163],v[84:87],a[76:79],v[160:163]
ds_read_b64_tr_b16 v[68:69],v1 offset:10240
v_mfma_f32_16x16x32_bf16 v[160:163],v[88:91],a[84:87],v[160:163]
ds_read_b64_tr_b16 v[70:71],v1 offset:15360
v_mfma_f32_16x16x32_bf16 v[160:163],v[92:95],a[92:95],v[160:163]
buffer_load_dwordx4 a[164:167],v6,s[28:31],s89 offen offset:1024 sc1
v_mfma_f32_16x16x32_bf16 v[168:171],v[92:95],a[124:127],v[168:171]
ds_read_b64_tr_b16 v[72:73],v1 offset:20480
ds_read_b64_tr_b16 v[74:75],v1 offset:25600
v_mfma_f32_16x16x32_bf16 v[168:171],v[88:91],a[116:119],v[168:171]
ds_read_b64_tr_b16 v[76:77],v1 offset:30720
v_mfma_f32_16x16x32_bf16 v[168:171],v[84:87],a[108:111],v[168:171]
ds_read_b64_tr_b16 v[78:79],v1 offset:35840
v_mfma_f32_16x16x32_bf16 v[168:171],v[80:83],a[100:103],v[168:171]
v_mfma_f32_16x16x32_bf16 v[176:179],v[80:83],a[64:67],v[176:179]
buffer_load_dwordx4 a[148:151],v6,s[28:31],s88 offen offset:1024 sc1
v_mfma_f32_16x16x32_bf16 v[176:179],v[84:87],a[72:75],v[176:179]
v_mfma_f32_16x16x32_bf16 v[176:179],v[88:91],a[80:83],v[176:179]
v_mfma_f32_16x16x32_bf16 v[176:179],v[92:95],a[88:91],v[176:179]
v_mfma_f32_16x16x32_bf16 v[184:187],v[92:95],a[120:123],v[184:187]
ds_read_b64_tr_b16 v[92:93],v2 offset:30752
ds_read_b64_tr_b16 v[94:95],v2 offset:35872
v_mfma_f32_16x16x32_bf16 v[184:187],v[88:91],a[112:115],v[184:187]
buffer_load_dwordx4 a[132:135],v6,s[28:31],s87 offen offset:1024 sc1
v_mfma_f32_16x16x32_bf16 v[184:187],v[84:87],a[104:107],v[184:187]
ds_read_b64_tr_b16 v[84:85],v2 offset:10272
ds_read_b64_tr_b16 v[86:87],v2 offset:15392
v_mfma_f32_16x16x32_bf16 v[184:187],v[80:83],a[96:99],v[184:187]
s_waitcnt lgkmcnt(4)
ds_read_b64_tr_b16 v[80:81],v2 offset:32
v_mfma_f32_16x16x32_bf16 v[192:195],v[96:99],a[68:71],v[192:195]
ds_read_b64_tr_b16 v[82:83],v2 offset:5152
v_mfma_f32_16x16x32_bf16 v[192:195],v[100:103],a[76:79],v[192:195]
ds_read_b64_tr_b16 v[88:89],v2 offset:20512
v_mfma_f32_16x16x32_bf16 v[192:195],v[104:107],a[84:87],v[192:195]
ds_read_b64_tr_b16 v[90:91],v2 offset:25632
v_mfma_f32_16x16x32_bf16 v[192:195],v[108:111],a[92:95],v[192:195]
buffer_load_dwordx4 v24,s[48:51],s83 offen lds
v_mfma_f32_16x16x32_bf16 v[200:203],v[108:111],a[124:127],v[200:203]
v_mfma_f32_16x16x32_bf16 v[200:203],v[104:107],a[116:119],v[200:203]
v_mfma_f32_16x16x32_bf16 v[200:203],v[100:103],a[108:111],v[200:203]
v_mfma_f32_16x16x32_bf16 v[200:203],v[96:99],a[100:103],v[200:203]
v_mfma_f32_16x16x32_bf16 v[208:211],v[96:99],a[64:67],v[208:211]
buffer_load_dwordx4 a[136:139],v6,s[28:31],s87 offen offset:2048 sc1
v_mfma_f32_16x16x32_bf16 v[208:211],v[100:103],a[72:75],v[208:211]
v_mfma_f32_16x16x32_bf16 v[208:211],v[104:107],a[80:83],v[208:211]
v_mfma_f32_16x16x32_bf16 v[208:211],v[108:111],a[88:91],v[208:211]
v_mfma_f32_16x16x32_bf16 v[216:219],v[108:111],a[120:123],v[216:219]
ds_read_b64_tr_b16 v[108:109],v1 offset:30784
ds_read_b64_tr_b16 v[110:111],v1 offset:35904
v_mfma_f32_16x16x32_bf16 v[216:219],v[104:107],a[112:115],v[216:219]
ds_read_b64_tr_b16 v[104:105],v1 offset:20544
v_mfma_f32_16x16x32_bf16 v[216:219],v[100:103],a[104:107],v[216:219]
buffer_load_dwordx4 v25,s[48:51],s83 offen offset:1024 lds
v_mfma_f32_16x16x32_bf16 v[216:219],v[96:99],a[96:99],v[216:219]
ds_read_b64_tr_b16 v[96:97],v1 offset:64
ds_read_b64_tr_b16 v[98:99],v1 offset:5184
v_mfma_f32_16x16x32_bf16 v[224:227],v[112:115],a[68:71],v[224:227]
ds_read_b64_tr_b16 v[100:101],v1 offset:10304
v_mfma_f32_16x16x32_bf16 v[224:227],v[116:119],a[76:79],v[224:227]
ds_read_b64_tr_b16 v[102:103],v1 offset:15424
v_mfma_f32_16x16x32_bf16 v[224:227],v[120:123],a[84:87],v[224:227]
ds_read_b64_tr_b16 v[106:107],v1 offset:25664
v_mfma_f32_16x16x32_bf16 v[224:227],v[124:127],a[92:95],v[224:227]
buffer_load_dwordx4 a[152:155],v6,s[28:31],s88 offen offset:2048 sc1
v_mfma_f32_16x16x32_bf16 v[232:235],v[124:127],a[124:127],v[232:235]
v_mfma_f32_16x16x32_bf16 v[232:235],v[120:123],a[116:119],v[232:235]
v_mfma_f32_16x16x32_bf16 v[232:235],v[116:119],a[108:111],v[232:235]
v_mfma_f32_16x16x32_bf16 v[232:235],v[112:115],a[100:103],v[232:235]
v_mfma_f32_16x16x32_bf16 v[240:243],v[112:115],a[64:67],v[240:243]
v_mfma_f32_16x16x32_bf16 v[240:243],v[116:119],a[72:75],v[240:243]
buffer_load_dwordx4 v26,s[48:51],s83 offen offset:2048 lds
v_mfma_f32_16x16x32_bf16 v[240:243],v[120:123],a[80:83],v[240:243]
v_mfma_f32_16x16x32_bf16 v[240:243],v[124:127],a[88:91],v[240:243]
v_mfma_f32_16x16x32_bf16 v[248:251],v[124:127],a[120:123],v[248:251]
ds_read_b64_tr_b16 v[124:125],v2 offset:30816
ds_read_b64_tr_b16 v[126:127],v2 offset:35936
v_mfma_f32_16x16x32_bf16 v[248:251],v[120:123],a[112:115],v[248:251]
ds_read_b64_tr_b16 v[120:121],v2 offset:20576
v_mfma_f32_16x16x32_bf16 v[248:251],v[116:119],a[104:107],v[248:251]
buffer_load_dwordx4 a[168:171],v6,s[28:31],s89 offen offset:2048 sc1
v_mfma_f32_16x16x32_bf16 v[248:251],v[112:115],a[96:99],v[248:251]
s_waitcnt vmcnt(22)
ds_read_b64_tr_b16 v[112:113],v2 offset:96
ds_read_b64_tr_b16 v[114:115],v2 offset:5216
v_mfma_f32_16x16x32_bf16 a[192:195],v[64:67],v[32:35],a[192:195]
ds_read_b64_tr_b16 v[116:117],v2 offset:10336
v_mfma_f32_16x16x32_bf16 a[192:195],v[68:71],v[36:39],a[192:195]
ds_read_b64_tr_b16 v[118:119],v2 offset:15456
v_mfma_f32_16x16x32_bf16 a[192:195],v[72:75],v[40:43],a[192:195]
s_waitcnt vmcnt(17)
ds_read_b64_tr_b16 v[122:123],v2 offset:25696
v_mfma_f32_16x16x32_bf16 a[192:195],v[76:79],v[44:47],a[192:195]
s_waitcnt vmcnt(14)
v_mfma_f32_16x16x32_bf16 a[196:199],v[76:79],v[180:183],a[196:199]
buffer_load_dwordx4 v27,s[48:51],s83 offen offset:3072 lds
v_mfma_f32_16x16x32_bf16 a[196:199],v[72:75],v[164:167],a[196:199]
s_add_u32 m0,s86,45056
v_mfma_f32_16x16x32_bf16 a[196:199],v[68:71],v[148:151],a[196:199]
v_mfma_f32_16x16x32_bf16 a[196:199],v[64:67],v[132:135],a[196:199]
v_mfma_f32_16x16x32_bf16 a[200:203],v[64:67],v[196:199],a[200:203]
v_mfma_f32_16x16x32_bf16 a[200:203],v[68:71],v[212:215],a[200:203]
buffer_load_dwordx4 a[184:187],v6,s[28:31],s90 offen offset:2048 sc1
v_mfma_f32_16x16x32_bf16 a[200:203],v[72:75],v[228:231],a[200:203]
s_waitcnt vmcnt(14)
v_mfma_f32_16x16x32_bf16 a[200:203],v[76:79],v[244:247],a[200:203]
s_waitcnt vmcnt(13)
v_mfma_f32_16x16x32_bf16 a[204:207],v[76:79],v[188:191],a[204:207]
ds_read_b64_tr_b16 v[76:77],v1 offset:30848
ds_read_b64_tr_b16 v[78:79],v1 offset:35968
v_mfma_f32_16x16x32_bf16 a[204:207],v[72:75],v[172:175],a[204:207]
ds_read_b64_tr_b16 v[72:73],v1 offset:20608
v_mfma_f32_16x16x32_bf16 a[204:207],v[68:71],v[156:159],a[204:207]
s_waitcnt lgkmcnt(3)
ds_read_b64_tr_b16 v[68:69],v1 offset:10368
v_mfma_f32_16x16x32_bf16 a[204:207],v[64:67],v[140:143],a[204:207]
buffer_load_dwordx4 v7,s[48:51],s83 offen lds
v_mfma_f32_16x16x32_bf16 a[208:211],v[80:83],v[32:35],a[208:211]
s_add_u32 m0,s86,46080
ds_read_b64_tr_b16 v[64:65],v1 offset:128
ds_read_b64_tr_b16 v[66:67],v1 offset:5248
v_mfma_f32_16x16x32_bf16 a[208:211],v[84:87],v[36:39],a[208:211]
s_mul_i32 s83,s63,20480
ds_read_b64_tr_b16 v[70:71],v1 offset:15488
v_mfma_f32_16x16x32_bf16 a[208:211],v[88:91],v[40:43],a[208:211]
ds_read_b64_tr_b16 v[74:75],v1 offset:25728
v_mfma_f32_16x16x32_bf16 a[208:211],v[92:95],v[44:47],a[208:211]
v_mfma_f32_16x16x32_bf16 a[212:215],v[92:95],v[180:183],a[212:215]
buffer_load_dwordx4 a[188:191],v28,s[28:31],s90 offen offset:3072 sc1
v_mfma_f32_16x16x32_bf16 a[212:215],v[88:91],v[164:167],a[212:215]
v_mfma_f32_16x16x32_bf16 a[212:215],v[84:87],v[148:151],a[212:215]
v_mfma_f32_16x16x32_bf16 a[212:215],v[80:83],v[132:135],a[212:215]
v_mfma_f32_16x16x32_bf16 a[216:219],v[80:83],v[196:199],a[216:219]
v_mfma_f32_16x16x32_bf16 a[216:219],v[84:87],v[212:215],a[216:219]
v_mfma_f32_16x16x32_bf16 a[216:219],v[88:91],v[228:231],a[216:219]
buffer_load_dwordx4 v24,s[48:51],s84 offen lds
v_mfma_f32_16x16x32_bf16 a[216:219],v[92:95],v[244:247],a[216:219]
v_mfma_f32_16x16x32_bf16 a[220:223],v[92:95],v[188:191],a[220:223]
ds_read_b64_tr_b16 v[92:93],v2 offset:30880
ds_read_b64_tr_b16 v[94:95],v2 offset:36000
v_mfma_f32_16x16x32_bf16 a[220:223],v[88:91],v[172:175],a[220:223]
ds_read_b64_tr_b16 v[88:89],v2 offset:20640
v_mfma_f32_16x16x32_bf16 a[220:223],v[84:87],v[156:159],a[220:223]
ds_read_b64_tr_b16 v[84:85],v2 offset:10400
v_mfma_f32_16x16x32_bf16 a[220:223],v[80:83],v[140:143],a[220:223]
ds_read_b64_tr_b16 v[80:81],v2 offset:160
v_mfma_f32_16x16x32_bf16 a[224:227],v[96:99],v[32:35],a[224:227]
buffer_load_dwordx4 v25,s[48:51],s84 offen offset:1024 lds
v_mfma_f32_16x16x32_bf16 a[224:227],v[100:103],v[36:39],a[224:227]
ds_read_b64_tr_b16 v[82:83],v2 offset:5280
ds_read_b64_tr_b16 v[86:87],v2 offset:15520
v_mfma_f32_16x16x32_bf16 a[224:227],v[104:107],v[40:43],a[224:227]
ds_read_b64_tr_b16 v[90:91],v2 offset:25760
v_mfma_f32_16x16x32_bf16 a[224:227],v[108:111],v[44:47],a[224:227]
v_mfma_f32_16x16x32_bf16 a[228:231],v[108:111],v[180:183],a[228:231]
v_mfma_f32_16x16x32_bf16 a[228:231],v[104:107],v[164:167],a[228:231]
buffer_load_dwordx4 a[172:175],v28,s[28:31],s89 offen offset:3072 sc1
v_mfma_f32_16x16x32_bf16 a[228:231],v[100:103],v[148:151],a[228:231]
v_mfma_f32_16x16x32_bf16 a[228:231],v[96:99],v[132:135],a[228:231]
v_mfma_f32_16x16x32_bf16 a[232:235],v[96:99],v[196:199],a[232:235]
v_mfma_f32_16x16x32_bf16 a[232:235],v[100:103],v[212:215],a[232:235]
v_mfma_f32_16x16x32_bf16 a[232:235],v[104:107],v[228:231],a[232:235]
v_mfma_f32_16x16x32_bf16 a[232:235],v[108:111],v[244:247],a[232:235]
buffer_load_dwordx4 v26,s[48:51],s84 offen offset:2048 lds
v_mfma_f32_16x16x32_bf16 a[236:239],v[108:111],v[188:191],a[236:239]
ds_read_b64_tr_b16 v[108:109],v1 offset:30912
ds_read_b64_tr_b16 v[110:111],v1 offset:36032
v_mfma_f32_16x16x32_bf16 a[236:239],v[104:107],v[172:175],a[236:239]
ds_read_b64_tr_b16 v[104:105],v1 offset:20672
v_mfma_f32_16x16x32_bf16 a[236:239],v[100:103],v[156:159],a[236:239]
ds_read_b64_tr_b16 v[100:101],v1 offset:10432
v_mfma_f32_16x16x32_bf16 a[236:239],v[96:99],v[140:143],a[236:239]
ds_read_b64_tr_b16 v[96:97],v1 offset:192
v_mfma_f32_16x16x32_bf16 a[240:243],v[112:115],v[32:35],a[240:243]
ds_read_b64_tr_b16 v[98:99],v1 offset:5312
ds_read_b64_tr_b16 v[102:103],v1 offset:15552
v_mfma_f32_16x16x32_bf16 a[240:243],v[116:119],v[36:39],a[240:243]
buffer_load_dwordx4 v27,s[48:51],s84 offen offset:3072 lds
v_mfma_f32_16x16x32_bf16 a[240:243],v[120:123],v[40:43],a[240:243]
s_add_u32 m0,s86,50176
ds_read_b64_tr_b16 v[106:107],v1 offset:25792
v_add_u32 v1,0xffff6000,v1
v_mfma_f32_16x16x32_bf16 a[240:243],v[124:127],v[44:47],a[240:243]
v_mfma_f32_16x16x32_bf16 a[244:247],v[124:127],v[180:183],a[244:247]
v_mfma_f32_16x16x32_bf16 a[244:247],v[120:123],v[164:167],a[244:247]
)A"
R"A(v_mfma_f32_16x16x32_bf16 a[244:247],v[116:119],v[148:151],a[244:247]
buffer_load_dwordx4 a[140:143],v28,s[28:31],s87 offen offset:3072 sc1
v_mfma_f32_16x16x32_bf16 a[244:247],v[112:115],v[132:135],a[244:247]
s_add_u32 s87,s64,2
s_and_b32 s87,s87,3
s_lshl_b32 s87,s87,1
s_or_b32 s87,s87,1
s_lshl_b32 s87,s87,16
s_add_u32 s89,s87,8192
s_add_u32 s90,s87,12288
v_mfma_f32_16x16x32_bf16 a[248:251],v[112:115],v[196:199],a[248:251]
v_mfma_f32_16x16x32_bf16 a[248:251],v[116:119],v[212:215],a[248:251]
v_mfma_f32_16x16x32_bf16 a[248:251],v[120:123],v[228:231],a[248:251]
v_mfma_f32_16x16x32_bf16 a[248:251],v[124:127],v[244:247],a[248:251]
buffer_load_dwordx4 a[68:71],v6,s[28:31],s87 offen sc1
v_mfma_f32_16x16x32_bf16 a[252:255],v[124:127],v[188:191],a[252:255]
ds_read_b64_tr_b16 v[124:125],v2 offset:30944
ds_read_b64_tr_b16 v[126:127],v2 offset:36064
v_mfma_f32_16x16x32_bf16 a[252:255],v[120:123],v[172:175],a[252:255]
ds_read_b64_tr_b16 v[120:121],v2 offset:20704
v_mfma_f32_16x16x32_bf16 a[252:255],v[116:119],v[156:159],a[252:255]
ds_read_b64_tr_b16 v[116:117],v2 offset:10464
v_mfma_f32_16x16x32_bf16 a[252:255],v[112:115],v[140:143],a[252:255]
s_waitcnt lgkmcnt(4)
ds_read_b64_tr_b16 v[112:113],v2 offset:224
v_mfma_f32_16x16x32_bf16 a[0:3],v[64:67],v[32:35],a[0:3]
buffer_load_dwordx4 a[156:159],v28,s[28:31],s88 offen offset:3072 sc1
v_mfma_f32_16x16x32_bf16 a[0:3],v[68:71],v[36:39],a[0:3]
s_add_u32 s88,s87,4096
ds_read_b64_tr_b16 v[114:115],v2 offset:5344
ds_read_b64_tr_b16 v[118:119],v2 offset:15584
v_mfma_f32_16x16x32_bf16 a[0:3],v[72:75],v[40:43],a[0:3]
ds_read_b64_tr_b16 v[122:123],v2 offset:25824
v_add_u32 v2,0xffff6000,v2
v_mfma_f32_16x16x32_bf16 a[0:3],v[76:79],v[44:47],a[0:3]
v_mfma_f32_16x16x32_bf16 a[4:7],v[76:79],v[180:183],a[4:7]
v_mfma_f32_16x16x32_bf16 a[4:7],v[72:75],v[164:167],a[4:7]
v_mfma_f32_16x16x32_bf16 a[4:7],v[68:71],v[148:151],a[4:7]
buffer_load_dwordx4 v7,s[48:51],s84 offen lds
v_mfma_f32_16x16x32_bf16 a[4:7],v[64:67],v[132:135],a[4:7]
v_mfma_f32_16x16x32_bf16 a[8:11],v[64:67],v[196:199],a[8:11]
v_mfma_f32_16x16x32_bf16 a[8:11],v[68:71],v[212:215],a[8:11]
v_mfma_f32_16x16x32_bf16 a[8:11],v[72:75],v[228:231],a[8:11]
v_mfma_f32_16x16x32_bf16 a[8:11],v[76:79],v[244:247],a[8:11]
buffer_load_dwordx4 a[76:79],v6,s[28:31],s88 offen sc1
v_mfma_f32_16x16x32_bf16 a[12:15],v[76:79],v[188:191],a[12:15]
ds_read_b64_tr_b16 v[76:77],v1 offset:30720
ds_read_b64_tr_b16 v[78:79],v1 offset:35840
v_mfma_f32_16x16x32_bf16 a[12:15],v[72:75],v[172:175],a[12:15]
ds_read_b64_tr_b16 v[72:73],v1 offset:20480
v_mfma_f32_16x16x32_bf16 a[12:15],v[68:71],v[156:159],a[12:15]
ds_read_b64_tr_b16 v[68:69],v1 offset:10240
v_mfma_f32_16x16x32_bf16 a[12:15],v[64:67],v[140:143],a[12:15]
ds_read_b64_tr_b16 v[64:65],v1
v_mfma_f32_16x16x32_bf16 a[16:19],v[80:83],v[32:35],a[16:19]
buffer_load_dwordx4 a[84:87],v6,s[28:31],s89 offen sc1
v_mfma_f32_16x16x32_bf16 a[16:19],v[84:87],v[36:39],a[16:19]
ds_read_b64_tr_b16 v[66:67],v1 offset:5120
ds_read_b64_tr_b16 v[70:71],v1 offset:15360
v_mfma_f32_16x16x32_bf16 a[16:19],v[88:91],v[40:43],a[16:19]
ds_read_b64_tr_b16 v[74:75],v1 offset:25600
s_nop 0
L_pd22_%=:
s_cbranch_execz L_trtg2S_%=
v_add_u32 v1,0xffff6000,v1
v_mfma_f32_16x16x32_bf16 a[16:19],v[92:95],v[44:47],a[16:19]
v_mfma_f32_16x16x32_bf16 a[20:23],v[92:95],v[180:183],a[20:23]
v_mfma_f32_16x16x32_bf16 a[20:23],v[88:91],v[164:167],a[20:23]
buffer_load_dwordx4 a[92:95],v6,s[28:31],s90 offen sc1
v_mfma_f32_16x16x32_bf16 a[20:23],v[84:87],v[148:151],a[20:23]
v_mfma_f32_16x16x32_bf16 a[20:23],v[80:83],v[132:135],a[20:23]
v_mfma_f32_16x16x32_bf16 a[24:27],v[80:83],v[196:199],a[24:27]
v_mfma_f32_16x16x32_bf16 a[24:27],v[84:87],v[212:215],a[24:27]
v_mfma_f32_16x16x32_bf16 a[24:27],v[88:91],v[228:231],a[24:27]
buffer_load_dwordx4 a[124:127],v6,s[28:31],s90 offen offset:1024 sc1
v_mfma_f32_16x16x32_bf16 a[24:27],v[92:95],v[244:247],a[24:27]
v_mfma_f32_16x16x32_bf16 a[28:31],v[92:95],v[188:191],a[28:31]
ds_read_b64_tr_b16 v[92:93],v2 offset:30752
ds_read_b64_tr_b16 v[94:95],v2 offset:35872
v_mfma_f32_16x16x32_bf16 a[28:31],v[88:91],v[172:175],a[28:31]
ds_read_b64_tr_b16 v[88:89],v2 offset:20512
v_mfma_f32_16x16x32_bf16 a[28:31],v[84:87],v[156:159],a[28:31]
ds_read_b64_tr_b16 v[84:85],v2 offset:10272
v_mfma_f32_16x16x32_bf16 a[28:31],v[80:83],v[140:143],a[28:31]
buffer_load_dwordx4 a[116:119],v6,s[28:31],s89 offen offset:1024 sc1
v_mfma_f32_16x16x32_bf16 v[128:131],v[96:99],v[32:35],v[128:131]
ds_read_b64_tr_b16 v[80:81],v2 offset:32
ds_read_b64_tr_b16 v[82:83],v2 offset:5152
v_mfma_f32_16x16x32_bf16 v[128:131],v[100:103],v[36:39],v[128:131]
ds_read_b64_tr_b16 v[86:87],v2 offset:15392
v_mfma_f32_16x16x32_bf16 v[128:131],v[104:107],v[40:43],v[128:131]
ds_read_b64_tr_b16 v[90:91],v2 offset:25632
v_add_u32 v2,0xffff6000,v2
v_mfma_f32_16x16x32_bf16 v[128:131],v[108:111],v[44:47],v[128:131]
v_mfma_f32_16x16x32_bf16 v[136:139],v[108:111],v[180:183],v[136:139]
buffer_load_dwordx4 a[108:111],v6,s[28:31],s88 offen offset:1024 sc1
v_mfma_f32_16x16x32_bf16 v[136:139],v[104:107],v[164:167],v[136:139]
v_mfma_f32_16x16x32_bf16 v[136:139],v[100:103],v[148:151],v[136:139]
v_mfma_f32_16x16x32_bf16 v[136:139],v[96:99],v[132:135],v[136:139]
v_mfma_f32_16x16x32_bf16 v[144:147],v[96:99],v[196:199],v[144:147]
v_mfma_f32_16x16x32_bf16 v[144:147],v[100:103],v[212:215],v[144:147]
buffer_load_dwordx4 a[100:103],v6,s[28:31],s87 offen offset:1024 sc1
v_mfma_f32_16x16x32_bf16 v[144:147],v[104:107],v[228:231],v[144:147]
v_mfma_f32_16x16x32_bf16 v[144:147],v[108:111],v[244:247],v[144:147]
v_mfma_f32_16x16x32_bf16 v[152:155],v[108:111],v[188:191],v[152:155]
v_mfma_f32_16x16x32_bf16 v[152:155],v[104:107],v[172:175],v[152:155]
v_mfma_f32_16x16x32_bf16 v[152:155],v[100:103],v[156:159],v[152:155]
buffer_load_dwordx4 a[64:67],v6,s[28:31],s87 offen offset:2048 sc1
v_mfma_f32_16x16x32_bf16 v[152:155],v[96:99],v[140:143],v[152:155]
s_waitcnt vmcnt(8)
s_barrier
s_waitcnt lgkmcnt(0)
v_mfma_f32_16x16x32_bf16 v[160:163],v[112:115],v[32:35],v[160:163]
ds_read_b64_tr_b16 v[96:97],v1
ds_read_b64_tr_b16 v[98:99],v1 offset:5120
v_mfma_f32_16x16x32_bf16 v[160:163],v[116:119],v[36:39],v[160:163]
ds_read_b64_tr_b16 v[100:101],v1 offset:10240
v_mfma_f32_16x16x32_bf16 v[160:163],v[120:123],v[40:43],v[160:163]
buffer_load_dwordx4 a[72:75],v6,s[28:31],s88 offen offset:2048 sc1
v_mfma_f32_16x16x32_bf16 v[160:163],v[124:127],v[44:47],v[160:163]
ds_read_b64_tr_b16 v[102:103],v1 offset:15360
ds_read_b64_tr_b16 v[104:105],v1 offset:20480
v_mfma_f32_16x16x32_bf16 v[168:171],v[124:127],v[180:183],v[168:171]
ds_read_b64_tr_b16 v[106:107],v1 offset:25600
v_mfma_f32_16x16x32_bf16 v[168:171],v[120:123],v[164:167],v[168:171]
ds_read_b64_tr_b16 v[108:109],v1 offset:30720
v_mfma_f32_16x16x32_bf16 v[168:171],v[116:119],v[148:151],v[168:171]
ds_read_b64_tr_b16 v[110:111],v1 offset:35840
v_add_u32 v1,0xa000,v1
v_mfma_f32_16x16x32_bf16 v[168:171],v[112:115],v[132:135],v[168:171]
buffer_load_dwordx4 a[80:83],v6,s[28:31],s89 offen offset:2048 sc1
v_mfma_f32_16x16x32_bf16 v[176:179],v[112:115],v[196:199],v[176:179]
v_mfma_f32_16x16x32_bf16 v[176:179],v[116:119],v[212:215],v[176:179]
v_mfma_f32_16x16x32_bf16 v[176:179],v[120:123],v[228:231],v[176:179]
v_mfma_f32_16x16x32_bf16 v[176:179],v[124:127],v[244:247],v[176:179]
v_mfma_f32_16x16x32_bf16 v[184:187],v[124:127],v[188:191],v[184:187]
buffer_load_dwordx4 a[88:91],v6,s[28:31],s90 offen offset:2048 sc1
v_mfma_f32_16x16x32_bf16 v[184:187],v[120:123],v[172:175],v[184:187]
ds_read_b64_tr_b16 v[120:121],v2 offset:20512
ds_read_b64_tr_b16 v[122:123],v2 offset:25632
v_mfma_f32_16x16x32_bf16 v[184:187],v[116:119],v[156:159],v[184:187]
ds_read_b64_tr_b16 v[116:117],v2 offset:10272
v_mfma_f32_16x16x32_bf16 v[184:187],v[112:115],v[140:143],v[184:187]
ds_read_b64_tr_b16 v[112:113],v2 offset:32
v_mfma_f32_16x16x32_bf16 a[192:195],v[64:67],a[128:131],a[192:195]
ds_read_b64_tr_b16 v[114:115],v2 offset:5152
v_mfma_f32_16x16x32_bf16 a[192:195],v[68:71],a[144:147],a[192:195]
buffer_load_dwordx4 a[120:123],v28,s[28:31],s90 offen offset:3072 sc1
v_mfma_f32_16x16x32_bf16 a[192:195],v[72:75],a[160:163],a[192:195]
ds_read_b64_tr_b16 v[118:119],v2 offset:15392
ds_read_b64_tr_b16 v[124:125],v2 offset:30752
v_mfma_f32_16x16x32_bf16 a[192:195],v[76:79],a[176:179],a[192:195]
ds_read_b64_tr_b16 v[126:127],v2 offset:35872
v_add_u32 v2,0xa000,v2
v_mfma_f32_16x16x32_bf16 a[196:199],v[76:79],a[180:183],a[196:199]
v_mfma_f32_16x16x32_bf16 a[196:199],v[72:75],a[164:167],a[196:199]
v_mfma_f32_16x16x32_bf16 a[196:199],v[68:71],a[148:151],a[196:199]
buffer_load_dwordx4 a[112:115],v28,s[28:31],s89 offen offset:3072 sc1
v_mfma_f32_16x16x32_bf16 a[196:199],v[64:67],a[132:135],a[196:199]
v_mfma_f32_16x16x32_bf16 a[200:203],v[64:67],a[136:139],a[200:203]
v_mfma_f32_16x16x32_bf16 a[200:203],v[68:71],a[152:155],a[200:203]
v_mfma_f32_16x16x32_bf16 a[200:203],v[72:75],a[168:171],a[200:203]
v_mfma_f32_16x16x32_bf16 a[200:203],v[76:79],a[184:187],a[200:203]
buffer_load_dwordx4 a[104:107],v28,s[28:31],s88 offen offset:3072 sc1
v_mfma_f32_16x16x32_bf16 a[204:207],v[76:79],a[188:191],a[204:207]
ds_read_b64_tr_b16 v[76:77],v1 offset:30784
ds_read_b64_tr_b16 v[78:79],v1 offset:35904
v_mfma_f32_16x16x32_bf16 a[204:207],v[72:75],a[172:175],a[204:207]
ds_read_b64_tr_b16 v[72:73],v1 offset:20544
v_mfma_f32_16x16x32_bf16 a[204:207],v[68:71],a[156:159],a[204:207]
ds_read_b64_tr_b16 v[68:69],v1 offset:10304
v_mfma_f32_16x16x32_bf16 a[204:207],v[64:67],a[140:143],a[204:207]
ds_read_b64_tr_b16 v[64:65],v1 offset:64
v_mfma_f32_16x16x32_bf16 a[208:211],v[80:83],a[128:131],a[208:211]
buffer_load_dwordx4 a[96:99],v28,s[28:31],s87 offen offset:3072 sc1
v_mfma_f32_16x16x32_bf16 a[208:211],v[84:87],a[144:147],a[208:211]
s_mul_i32 s87,s60,81920
ds_read_b64_tr_b16 v[66:67],v1 offset:5184
ds_read_b64_tr_b16 v[70:71],v1 offset:15424
v_mfma_f32_16x16x32_bf16 a[208:211],v[88:91],a[160:163],a[208:211]
s_add_u32 s87,s87,s66
ds_read_b64_tr_b16 v[74:75],v1 offset:25664
v_add_u32 v1,0xffff6000,v1
v_mfma_f32_16x16x32_bf16 a[208:211],v[92:95],a[176:179],a[208:211]
s_add_u32 s83,s83,s87
s_lshl_b32 s66,s63,6
s_add_u32 s87,s87,s66
v_mfma_f32_16x16x32_bf16 a[212:215],v[92:95],a[180:183],a[212:215]
s_add_u32 s88,s87,20480
s_add_u32 s89,s87,40960
s_add_u32 s90,s87,61440
v_mfma_f32_16x16x32_bf16 a[212:215],v[88:91],a[164:167],a[212:215]
s_add_u32 s66,s77,4096
v_mfma_f32_16x16x32_bf16 a[212:215],v[84:87],a[148:151],a[212:215]
v_mfma_f32_16x16x32_bf16 a[212:215],v[80:83],a[132:135],a[212:215]
v_mfma_f32_16x16x32_bf16 a[216:219],v[80:83],a[136:139],a[216:219]
v_mfma_f32_16x16x32_bf16 a[216:219],v[84:87],a[152:155],a[216:219]
v_mfma_f32_16x16x32_bf16 a[216:219],v[88:91],a[168:171],a[216:219]
v_mfma_f32_16x16x32_bf16 a[216:219],v[92:95],a[184:187],a[216:219]
v_mfma_f32_16x16x32_bf16 a[220:223],v[92:95],a[188:191],a[220:223]
ds_read_b64_tr_b16 v[92:93],v2 offset:30816
ds_read_b64_tr_b16 v[94:95],v2 offset:35936
v_mfma_f32_16x16x32_bf16 a[220:223],v[88:91],a[172:175],a[220:223]
ds_read_b64_tr_b16 v[88:89],v2 offset:20576
v_mfma_f32_16x16x32_bf16 a[220:223],v[84:87],a[156:159],a[220:223]
ds_read_b64_tr_b16 v[84:85],v2 offset:10336
v_mfma_f32_16x16x32_bf16 a[220:223],v[80:83],a[140:143],a[220:223]
s_waitcnt lgkmcnt(4)
ds_read_b64_tr_b16 v[80:81],v2 offset:96
v_mfma_f32_16x16x32_bf16 a[192:195],v[96:99],a[68:71],a[192:195]
s_waitcnt vmcnt(14)
ds_read_b64_tr_b16 v[82:83],v2 offset:5216
ds_read_b64_tr_b16 v[86:87],v2 offset:15456
v_mfma_f32_16x16x32_bf16 a[192:195],v[100:103],a[76:79],a[192:195]
s_waitcnt vmcnt(13)
ds_read_b64_tr_b16 v[90:91],v2 offset:25696
v_add_u32 v2,0xffff6000,v2
v_mfma_f32_16x16x32_bf16 a[192:195],v[104:107],a[84:87],a[192:195]
s_waitcnt vmcnt(12)
v_mfma_f32_16x16x32_bf16 a[192:195],v[108:111],a[92:95],a[192:195]
s_waitcnt vmcnt(11)
v_mfma_f32_16x16x32_bf16 a[196:199],v[108:111],a[124:127],a[196:199]
s_waitcnt vmcnt(10)
v_mfma_f32_16x16x32_bf16 a[196:199],v[104:107],a[116:119],a[196:199]
s_waitcnt vmcnt(9)
v_mfma_f32_16x16x32_bf16 a[196:199],v[100:103],a[108:111],a[196:199]
s_waitcnt vmcnt(8)
v_mfma_f32_16x16x32_bf16 a[196:199],v[96:99],a[100:103],a[196:199]
)A"
R"A(s_waitcnt vmcnt(7)
v_mfma_f32_16x16x32_bf16 a[200:203],v[96:99],a[64:67],a[200:203]
s_waitcnt vmcnt(6)
v_mfma_f32_16x16x32_bf16 a[200:203],v[100:103],a[72:75],a[200:203]
s_waitcnt vmcnt(5)
v_mfma_f32_16x16x32_bf16 a[200:203],v[104:107],a[80:83],a[200:203]
s_waitcnt vmcnt(4)
v_mfma_f32_16x16x32_bf16 a[200:203],v[108:111],a[88:91],a[200:203]
s_waitcnt vmcnt(3)
v_accvgpr_read_b32 v8,a192
v_accvgpr_read_b32 v9,a193
v_mfma_f32_16x16x32_bf16 a[204:207],v[108:111],a[120:123],a[204:207]
s_waitcnt vmcnt(2)
v_accvgpr_read_b32 v10,a194
v_accvgpr_read_b32 v11,a195
v_mfma_f32_16x16x32_bf16 a[204:207],v[104:107],a[112:115],a[204:207]
s_waitcnt vmcnt(1)
v_cvt_pk_bf16_f32 v8,v8,v9
v_cvt_pk_bf16_f32 v9,v10,v11
v_mfma_f32_16x16x32_bf16 a[204:207],v[100:103],a[104:107],a[204:207]
s_waitcnt vmcnt(0)
v_accvgpr_read_b32 v16,a196
v_accvgpr_read_b32 v17,a197
v_mfma_f32_16x16x32_bf16 a[204:207],v[96:99],a[96:99],a[204:207]
v_accvgpr_read_b32 v18,a198
v_accvgpr_read_b32 v19,a199
v_mfma_f32_16x16x32_bf16 a[208:211],v[112:115],a[68:71],a[208:211]
v_cvt_pk_bf16_f32 v16,v16,v17
v_cvt_pk_bf16_f32 v17,v18,v19
v_mfma_f32_16x16x32_bf16 a[208:211],v[116:119],a[76:79],a[208:211]
ds_read_b64_tr_b16 v[96:97],v1 offset:64
ds_read_b64_tr_b16 v[98:99],v1 offset:5184
v_mfma_f32_16x16x32_bf16 a[208:211],v[120:123],a[84:87],a[208:211]
ds_read_b64_tr_b16 v[100:101],v1 offset:10304
v_mfma_f32_16x16x32_bf16 a[208:211],v[124:127],a[92:95],a[208:211]
ds_read_b64_tr_b16 v[102:103],v1 offset:15424
v_mfma_f32_16x16x32_bf16 a[212:215],v[124:127],a[124:127],a[212:215]
ds_read_b64_tr_b16 v[104:105],v1 offset:20544
v_mfma_f32_16x16x32_bf16 a[212:215],v[120:123],a[116:119],a[212:215]
ds_read_b64_tr_b16 v[106:107],v1 offset:25664
ds_read_b64_tr_b16 v[108:109],v1 offset:30784
v_mfma_f32_16x16x32_bf16 a[212:215],v[116:119],a[108:111],a[212:215]
ds_read_b64_tr_b16 v[110:111],v1 offset:35904
v_add_u32 v1,0xa000,v1
v_mfma_f32_16x16x32_bf16 a[212:215],v[112:115],a[100:103],a[212:215]
v_accvgpr_read_b32 v12,a208
v_accvgpr_read_b32 v13,a209
v_mfma_f32_16x16x32_bf16 a[216:219],v[112:115],a[64:67],a[216:219]
v_accvgpr_read_b32 v14,a210
v_accvgpr_read_b32 v15,a211
v_mfma_f32_16x16x32_bf16 a[216:219],v[116:119],a[72:75],a[216:219]
v_cvt_pk_bf16_f32 v10,v12,v13
v_cvt_pk_bf16_f32 v11,v14,v15
v_mfma_f32_16x16x32_bf16 a[216:219],v[120:123],a[80:83],a[216:219]
buffer_store_dwordx4 v[8:11],v6,s[36:39],s74 offen sc1
v_mfma_f32_16x16x32_bf16 a[216:219],v[124:127],a[88:91],a[216:219]
v_accvgpr_read_b32 v20,a212
v_accvgpr_read_b32 v21,a213
v_mfma_f32_16x16x32_bf16 a[220:223],v[124:127],a[120:123],a[220:223]
v_accvgpr_read_b32 v22,a214
v_accvgpr_read_b32 v23,a215
v_mfma_f32_16x16x32_bf16 a[220:223],v[120:123],a[112:115],a[220:223]
v_cvt_pk_bf16_f32 v18,v20,v21
v_cvt_pk_bf16_f32 v19,v22,v23
v_mfma_f32_16x16x32_bf16 a[220:223],v[116:119],a[104:107],a[220:223]
v_accvgpr_read_b32 v8,a200
v_accvgpr_read_b32 v9,a201
v_mfma_f32_16x16x32_bf16 a[220:223],v[112:115],a[96:99],a[220:223]
v_accvgpr_read_b32 v10,a202
v_accvgpr_read_b32 v11,a203
v_mfma_f32_16x16x32_bf16 a[224:227],v[64:67],a[128:131],a[224:227]
v_accvgpr_read_b32 v12,a216
v_accvgpr_read_b32 v13,a217
v_mfma_f32_16x16x32_bf16 a[224:227],v[68:71],a[144:147],a[224:227]
v_accvgpr_read_b32 v14,a218
v_accvgpr_read_b32 v15,a219
v_mfma_f32_16x16x32_bf16 a[224:227],v[72:75],a[160:163],a[224:227]
v_cvt_pk_bf16_f32 v8,v8,v9
v_cvt_pk_bf16_f32 v9,v10,v11
v_mfma_f32_16x16x32_bf16 a[224:227],v[76:79],a[176:179],a[224:227]
v_cvt_pk_bf16_f32 v10,v12,v13
v_cvt_pk_bf16_f32 v11,v14,v15
v_mfma_f32_16x16x32_bf16 a[228:231],v[76:79],a[180:183],a[228:231]
ds_read_b64_tr_b16 v[112:113],v2 offset:96
ds_read_b64_tr_b16 v[114:115],v2 offset:5216
v_mfma_f32_16x16x32_bf16 a[228:231],v[72:75],a[164:167],a[228:231]
buffer_store_dwordx4 v[16:19],v6,s[36:39],s74 offen offset:1024 sc1
v_mfma_f32_16x16x32_bf16 a[228:231],v[68:71],a[148:151],a[228:231]
ds_read_b64_tr_b16 v[116:117],v2 offset:10336
ds_read_b64_tr_b16 v[118:119],v2 offset:15456
v_mfma_f32_16x16x32_bf16 a[228:231],v[64:67],a[132:135],a[228:231]
ds_read_b64_tr_b16 v[120:121],v2 offset:20576
v_accvgpr_read_b32 v16,a204
v_mfma_f32_16x16x32_bf16 a[232:235],v[64:67],a[136:139],a[232:235]
ds_read_b64_tr_b16 v[122:123],v2 offset:25696
v_accvgpr_read_b32 v17,a205
v_mfma_f32_16x16x32_bf16 a[232:235],v[68:71],a[152:155],a[232:235]
ds_read_b64_tr_b16 v[124:125],v2 offset:30816
v_accvgpr_read_b32 v18,a206
v_mfma_f32_16x16x32_bf16 a[232:235],v[72:75],a[168:171],a[232:235]
ds_read_b64_tr_b16 v[126:127],v2 offset:35936
v_accvgpr_read_b32 v19,a207
v_mfma_f32_16x16x32_bf16 a[232:235],v[76:79],a[184:187],a[232:235]
v_accvgpr_read_b32 v20,a220
v_accvgpr_read_b32 v21,a221
v_mfma_f32_16x16x32_bf16 a[236:239],v[76:79],a[188:191],a[236:239]
v_accvgpr_read_b32 v22,a222
v_accvgpr_read_b32 v23,a223
v_mfma_f32_16x16x32_bf16 a[236:239],v[72:75],a[172:175],a[236:239]
v_cvt_pk_bf16_f32 v16,v16,v17
v_cvt_pk_bf16_f32 v17,v18,v19
v_mfma_f32_16x16x32_bf16 a[236:239],v[68:71],a[156:159],a[236:239]
v_cvt_pk_bf16_f32 v18,v20,v21
v_cvt_pk_bf16_f32 v19,v22,v23
v_mfma_f32_16x16x32_bf16 a[236:239],v[64:67],a[140:143],a[236:239]
s_waitcnt lgkmcnt(3)
ds_read_b64_tr_b16 v[64:65],v1 offset:128
ds_read_b64_tr_b16 v[66:67],v1 offset:5248
v_mfma_f32_16x16x32_bf16 a[240:243],v[80:83],a[128:131],a[240:243]
buffer_store_dwordx4 v[8:11],v6,s[36:39],s74 offen offset:2048 sc1
v_mfma_f32_16x16x32_bf16 a[240:243],v[84:87],a[144:147],a[240:243]
ds_read_b64_tr_b16 v[68:69],v1 offset:10368
ds_read_b64_tr_b16 v[70:71],v1 offset:15488
v_mfma_f32_16x16x32_bf16 a[240:243],v[88:91],a[160:163],a[240:243]
ds_read_b64_tr_b16 v[72:73],v1 offset:20608
v_add_u32 v2,0xa000,v2
v_mfma_f32_16x16x32_bf16 a[240:243],v[92:95],a[176:179],a[240:243]
ds_read_b64_tr_b16 v[74:75],v1 offset:25728
v_mfma_f32_16x16x32_bf16 a[244:247],v[92:95],a[180:183],a[244:247]
ds_read_b64_tr_b16 v[76:77],v1 offset:30848
v_mfma_f32_16x16x32_bf16 a[244:247],v[88:91],a[164:167],a[244:247]
ds_read_b64_tr_b16 v[78:79],v1 offset:35968
v_add_u32 v1,0xffff6000,v1
v_mfma_f32_16x16x32_bf16 a[244:247],v[84:87],a[148:151],a[244:247]
v_mfma_f32_16x16x32_bf16 a[244:247],v[80:83],a[132:135],a[244:247]
v_mfma_f32_16x16x32_bf16 a[248:251],v[80:83],a[136:139],a[248:251]
v_mfma_f32_16x16x32_bf16 a[248:251],v[84:87],a[152:155],a[248:251]
v_mfma_f32_16x16x32_bf16 a[248:251],v[88:91],a[168:171],a[248:251]
v_mfma_f32_16x16x32_bf16 a[248:251],v[92:95],a[184:187],a[248:251]
buffer_store_dwordx4 v[16:19],v28,s[36:39],s74 offen offset:3072 sc1
v_mfma_f32_16x16x32_bf16 a[252:255],v[92:95],a[188:191],a[252:255]
ds_read_b64_tr_b16 v[92:93],v2 offset:30880
ds_read_b64_tr_b16 v[94:95],v2 offset:36000
v_mfma_f32_16x16x32_bf16 a[252:255],v[88:91],a[172:175],a[252:255]
ds_read_b64_tr_b16 v[88:89],v2 offset:20640
v_mfma_f32_16x16x32_bf16 a[252:255],v[84:87],a[156:159],a[252:255]
ds_read_b64_tr_b16 v[84:85],v2 offset:10400
v_mfma_f32_16x16x32_bf16 a[252:255],v[80:83],a[140:143],a[252:255]
ds_read_b64_tr_b16 v[80:81],v2 offset:160
v_mfma_f32_16x16x32_bf16 a[224:227],v[96:99],a[68:71],a[224:227]
ds_read_b64_tr_b16 v[82:83],v2 offset:5280
ds_read_b64_tr_b16 v[86:87],v2 offset:15520
v_mfma_f32_16x16x32_bf16 a[224:227],v[100:103],a[76:79],a[224:227]
ds_read_b64_tr_b16 v[90:91],v2 offset:25760
v_add_u32 v2,0xffff6000,v2
v_mfma_f32_16x16x32_bf16 a[224:227],v[104:107],a[84:87],a[224:227]
v_mfma_f32_16x16x32_bf16 a[224:227],v[108:111],a[92:95],a[224:227]
v_mfma_f32_16x16x32_bf16 a[228:231],v[108:111],a[124:127],a[228:231]
v_mfma_f32_16x16x32_bf16 a[228:231],v[104:107],a[116:119],a[228:231]
v_mfma_f32_16x16x32_bf16 a[228:231],v[100:103],a[108:111],a[228:231]
v_mfma_f32_16x16x32_bf16 a[228:231],v[96:99],a[100:103],a[228:231]
v_mfma_f32_16x16x32_bf16 a[232:235],v[96:99],a[64:67],a[232:235]
v_mfma_f32_16x16x32_bf16 a[232:235],v[100:103],a[72:75],a[232:235]
v_mfma_f32_16x16x32_bf16 a[232:235],v[104:107],a[80:83],a[232:235]
v_mfma_f32_16x16x32_bf16 a[232:235],v[108:111],a[88:91],a[232:235]
v_accvgpr_read_b32 v8,a224
v_accvgpr_read_b32 v9,a225
v_mfma_f32_16x16x32_bf16 a[236:239],v[108:111],a[120:123],a[236:239]
v_accvgpr_read_b32 v10,a226
v_accvgpr_read_b32 v11,a227
v_mfma_f32_16x16x32_bf16 a[236:239],v[104:107],a[112:115],a[236:239]
v_cvt_pk_bf16_f32 v8,v8,v9
v_cvt_pk_bf16_f32 v9,v10,v11
v_mfma_f32_16x16x32_bf16 a[236:239],v[100:103],a[104:107],a[236:239]
v_accvgpr_read_b32 v16,a228
v_accvgpr_read_b32 v17,a229
v_mfma_f32_16x16x32_bf16 a[236:239],v[96:99],a[96:99],a[236:239]
v_accvgpr_read_b32 v18,a230
v_accvgpr_read_b32 v19,a231
v_mfma_f32_16x16x32_bf16 a[240:243],v[112:115],a[68:71],a[240:243]
v_cvt_pk_bf16_f32 v16,v16,v17
v_cvt_pk_bf16_f32 v17,v18,v19
v_mfma_f32_16x16x32_bf16 a[240:243],v[116:119],a[76:79],a[240:243]
s_waitcnt lgkmcnt(0)
ds_read_b64_tr_b16 v[96:97],v1 offset:128
ds_read_b64_tr_b16 v[98:99],v1 offset:5248
v_mfma_f32_16x16x32_bf16 a[240:243],v[120:123],a[84:87],a[240:243]
ds_read_b64_tr_b16 v[100:101],v1 offset:10368
v_mfma_f32_16x16x32_bf16 a[240:243],v[124:127],a[92:95],a[240:243]
ds_read_b64_tr_b16 v[102:103],v1 offset:15488
v_mfma_f32_16x16x32_bf16 a[244:247],v[124:127],a[124:127],a[244:247]
ds_read_b64_tr_b16 v[104:105],v1 offset:20608
v_mfma_f32_16x16x32_bf16 a[244:247],v[120:123],a[116:119],a[244:247]
ds_read_b64_tr_b16 v[106:107],v1 offset:25728
ds_read_b64_tr_b16 v[108:109],v1 offset:30848
v_mfma_f32_16x16x32_bf16 a[244:247],v[116:119],a[108:111],a[244:247]
ds_read_b64_tr_b16 v[110:111],v1 offset:35968
v_add_u32 v1,0xa000,v1
v_mfma_f32_16x16x32_bf16 a[244:247],v[112:115],a[100:103],a[244:247]
v_accvgpr_read_b32 v12,a240
v_accvgpr_read_b32 v13,a241
v_mfma_f32_16x16x32_bf16 a[248:251],v[112:115],a[64:67],a[248:251]
v_accvgpr_read_b32 v14,a242
v_accvgpr_read_b32 v15,a243
v_mfma_f32_16x16x32_bf16 a[248:251],v[116:119],a[72:75],a[248:251]
v_cvt_pk_bf16_f32 v10,v12,v13
v_cvt_pk_bf16_f32 v11,v14,v15
v_mfma_f32_16x16x32_bf16 a[248:251],v[120:123],a[80:83],a[248:251]
buffer_store_dwordx4 v[8:11],v6,s[36:39],s75 offen sc1
v_mfma_f32_16x16x32_bf16 a[248:251],v[124:127],a[88:91],a[248:251]
v_accvgpr_read_b32 v20,a244
v_accvgpr_read_b32 v21,a245
v_mfma_f32_16x16x32_bf16 a[252:255],v[124:127],a[120:123],a[252:255]
v_accvgpr_read_b32 v22,a246
v_accvgpr_read_b32 v23,a247
v_mfma_f32_16x16x32_bf16 a[252:255],v[120:123],a[112:115],a[252:255]
v_cvt_pk_bf16_f32 v18,v20,v21
v_cvt_pk_bf16_f32 v19,v22,v23
v_mfma_f32_16x16x32_bf16 a[252:255],v[116:119],a[104:107],a[252:255]
v_accvgpr_read_b32 v8,a232
v_accvgpr_read_b32 v9,a233
v_mfma_f32_16x16x32_bf16 a[252:255],v[112:115],a[96:99],a[252:255]
v_accvgpr_read_b32 v10,a234
v_accvgpr_read_b32 v11,a235
v_mfma_f32_16x16x32_bf16 a[0:3],v[64:67],a[128:131],a[0:3]
v_accvgpr_read_b32 v12,a248
v_accvgpr_read_b32 v13,a249
v_mfma_f32_16x16x32_bf16 a[0:3],v[68:71],a[144:147],a[0:3]
v_accvgpr_read_b32 v14,a250
v_accvgpr_read_b32 v15,a251
v_mfma_f32_16x16x32_bf16 a[0:3],v[72:75],a[160:163],a[0:3]
v_cvt_pk_bf16_f32 v8,v8,v9
v_cvt_pk_bf16_f32 v9,v10,v11
v_mfma_f32_16x16x32_bf16 a[0:3],v[76:79],a[176:179],a[0:3]
v_cvt_pk_bf16_f32 v10,v12,v13
v_cvt_pk_bf16_f32 v11,v14,v15
v_mfma_f32_16x16x32_bf16 a[4:7],v[76:79],a[180:183],a[4:7]
ds_read_b64_tr_b16 v[112:113],v2 offset:160
ds_read_b64_tr_b16 v[114:115],v2 offset:5280
v_mfma_f32_16x16x32_bf16 a[4:7],v[72:75],a[164:167],a[4:7]
buffer_store_dwordx4 v[16:19],v6,s[36:39],s75 offen offset:1024 sc1
v_mfma_f32_16x16x32_bf16 a[4:7],v[68:71],a[148:151],a[4:7]
ds_read_b64_tr_b16 v[116:117],v2 offset:10400
ds_read_b64_tr_b16 v[118:119],v2 offset:15520
v_mfma_f32_16x16x32_bf16 a[4:7],v[64:67],a[132:135],a[4:7]
ds_read_b64_tr_b16 v[120:121],v2 offset:20640
v_accvgpr_read_b32 v16,a236
v_mfma_f32_16x16x32_bf16 a[8:11],v[64:67],a[136:139],a[8:11]
ds_read_b64_tr_b16 v[122:123],v2 offset:25760
v_accvgpr_read_b32 v17,a237
v_mfma_f32_16x16x32_bf16 a[8:11],v[68:71],a[152:155],a[8:11]
ds_read_b64_tr_b16 v[124:125],v2 offset:30880
v_accvgpr_read_b32 v18,a238
v_mfma_f32_16x16x32_bf16 a[8:11],v[72:75],a[168:171],a[8:11]
ds_read_b64_tr_b16 v[126:127],v2 offset:36000
v_accvgpr_read_b32 v19,a239
v_mfma_f32_16x16x32_bf16 a[8:11],v[76:79],a[184:187],a[8:11]
v_accvgpr_read_b32 v20,a252
)A"
R"A(v_accvgpr_read_b32 v21,a253
v_mfma_f32_16x16x32_bf16 a[12:15],v[76:79],a[188:191],a[12:15]
v_accvgpr_read_b32 v22,a254
v_accvgpr_read_b32 v23,a255
v_mfma_f32_16x16x32_bf16 a[12:15],v[72:75],a[172:175],a[12:15]
v_cvt_pk_bf16_f32 v16,v16,v17
v_cvt_pk_bf16_f32 v17,v18,v19
v_mfma_f32_16x16x32_bf16 a[12:15],v[68:71],a[156:159],a[12:15]
v_cvt_pk_bf16_f32 v18,v20,v21
v_cvt_pk_bf16_f32 v19,v22,v23
v_mfma_f32_16x16x32_bf16 a[12:15],v[64:67],a[140:143],a[12:15]
ds_read_b64_tr_b16 v[64:65],v1 offset:192
ds_read_b64_tr_b16 v[66:67],v1 offset:5312
v_mfma_f32_16x16x32_bf16 a[16:19],v[80:83],a[128:131],a[16:19]
buffer_store_dwordx4 v[8:11],v6,s[36:39],s75 offen offset:2048 sc1
v_mfma_f32_16x16x32_bf16 a[16:19],v[84:87],a[144:147],a[16:19]
ds_read_b64_tr_b16 v[68:69],v1 offset:10432
ds_read_b64_tr_b16 v[70:71],v1 offset:15552
v_mfma_f32_16x16x32_bf16 a[16:19],v[88:91],a[160:163],a[16:19]
ds_read_b64_tr_b16 v[72:73],v1 offset:20672
v_add_u32 v2,0xa000,v2
v_mfma_f32_16x16x32_bf16 a[16:19],v[92:95],a[176:179],a[16:19]
ds_read_b64_tr_b16 v[74:75],v1 offset:25792
v_mfma_f32_16x16x32_bf16 a[20:23],v[92:95],a[180:183],a[20:23]
ds_read_b64_tr_b16 v[76:77],v1 offset:30912
v_mfma_f32_16x16x32_bf16 a[20:23],v[88:91],a[164:167],a[20:23]
ds_read_b64_tr_b16 v[78:79],v1 offset:36032
v_add_u32 v1,0xffff6000,v1
v_mfma_f32_16x16x32_bf16 a[20:23],v[84:87],a[148:151],a[20:23]
v_mfma_f32_16x16x32_bf16 a[20:23],v[80:83],a[132:135],a[20:23]
v_mfma_f32_16x16x32_bf16 a[24:27],v[80:83],a[136:139],a[24:27]
v_mfma_f32_16x16x32_bf16 a[24:27],v[84:87],a[152:155],a[24:27]
v_mfma_f32_16x16x32_bf16 a[24:27],v[88:91],a[168:171],a[24:27]
v_mfma_f32_16x16x32_bf16 a[24:27],v[92:95],a[184:187],a[24:27]
buffer_store_dwordx4 v[16:19],v28,s[36:39],s75 offen offset:3072 sc1
v_mfma_f32_16x16x32_bf16 a[28:31],v[92:95],a[188:191],a[28:31]
ds_read_b64_tr_b16 v[92:93],v2 offset:30944
ds_read_b64_tr_b16 v[94:95],v2 offset:36064
v_mfma_f32_16x16x32_bf16 a[28:31],v[88:91],a[172:175],a[28:31]
ds_read_b64_tr_b16 v[88:89],v2 offset:20704
v_mfma_f32_16x16x32_bf16 a[28:31],v[84:87],a[156:159],a[28:31]
ds_read_b64_tr_b16 v[84:85],v2 offset:10464
v_mfma_f32_16x16x32_bf16 a[28:31],v[80:83],a[140:143],a[28:31]
s_waitcnt lgkmcnt(4)
ds_read_b64_tr_b16 v[80:81],v2 offset:224
v_mfma_f32_16x16x32_bf16 a[0:3],v[96:99],a[68:71],a[0:3]
ds_read_b64_tr_b16 v[82:83],v2 offset:5344
ds_read_b64_tr_b16 v[86:87],v2 offset:15584
v_mfma_f32_16x16x32_bf16 a[0:3],v[100:103],a[76:79],a[0:3]
ds_read_b64_tr_b16 v[90:91],v2 offset:25824
v_add_u32 v2,0xffff6000,v2
v_mfma_f32_16x16x32_bf16 a[0:3],v[104:107],a[84:87],a[0:3]
v_mfma_f32_16x16x32_bf16 a[0:3],v[108:111],a[92:95],a[0:3]
v_mfma_f32_16x16x32_bf16 a[4:7],v[108:111],a[124:127],a[4:7]
v_mfma_f32_16x16x32_bf16 a[4:7],v[104:107],a[116:119],a[4:7]
v_mfma_f32_16x16x32_bf16 a[4:7],v[100:103],a[108:111],a[4:7]
v_mfma_f32_16x16x32_bf16 a[4:7],v[96:99],a[100:103],a[4:7]
v_mfma_f32_16x16x32_bf16 a[8:11],v[96:99],a[64:67],a[8:11]
v_mfma_f32_16x16x32_bf16 a[8:11],v[100:103],a[72:75],a[8:11]
v_mfma_f32_16x16x32_bf16 a[8:11],v[104:107],a[80:83],a[8:11]
v_mfma_f32_16x16x32_bf16 a[8:11],v[108:111],a[88:91],a[8:11]
v_accvgpr_read_b32 v8,a0
v_accvgpr_read_b32 v9,a1
v_mfma_f32_16x16x32_bf16 a[12:15],v[108:111],a[120:123],a[12:15]
v_accvgpr_read_b32 v10,a2
v_accvgpr_read_b32 v11,a3
v_mfma_f32_16x16x32_bf16 a[12:15],v[104:107],a[112:115],a[12:15]
v_cvt_pk_bf16_f32 v8,v8,v9
v_cvt_pk_bf16_f32 v9,v10,v11
v_mfma_f32_16x16x32_bf16 a[12:15],v[100:103],a[104:107],a[12:15]
v_accvgpr_read_b32 v16,a4
v_accvgpr_read_b32 v17,a5
v_mfma_f32_16x16x32_bf16 a[12:15],v[96:99],a[96:99],a[12:15]
v_accvgpr_read_b32 v18,a6
v_accvgpr_read_b32 v19,a7
v_mfma_f32_16x16x32_bf16 a[16:19],v[112:115],a[68:71],a[16:19]
v_cvt_pk_bf16_f32 v16,v16,v17
v_cvt_pk_bf16_f32 v17,v18,v19
v_mfma_f32_16x16x32_bf16 a[16:19],v[116:119],a[76:79],a[16:19]
ds_read_b64_tr_b16 v[96:97],v1 offset:192
ds_read_b64_tr_b16 v[98:99],v1 offset:5312
v_mfma_f32_16x16x32_bf16 a[16:19],v[120:123],a[84:87],a[16:19]
ds_read_b64_tr_b16 v[100:101],v1 offset:10432
v_mfma_f32_16x16x32_bf16 a[16:19],v[124:127],a[92:95],a[16:19]
ds_read_b64_tr_b16 v[102:103],v1 offset:15552
v_mfma_f32_16x16x32_bf16 a[20:23],v[124:127],a[124:127],a[20:23]
ds_read_b64_tr_b16 v[104:105],v1 offset:20672
v_mfma_f32_16x16x32_bf16 a[20:23],v[120:123],a[116:119],a[20:23]
ds_read_b64_tr_b16 v[106:107],v1 offset:25792
ds_read_b64_tr_b16 v[108:109],v1 offset:30912
v_mfma_f32_16x16x32_bf16 a[20:23],v[116:119],a[108:111],a[20:23]
ds_read_b64_tr_b16 v[110:111],v1 offset:36032
v_add_u32 v1,0x14000,v1
v_mfma_f32_16x16x32_bf16 a[20:23],v[112:115],a[100:103],a[20:23]
v_accvgpr_read_b32 v12,a16
v_accvgpr_read_b32 v13,a17
v_mfma_f32_16x16x32_bf16 a[24:27],v[112:115],a[64:67],a[24:27]
v_accvgpr_read_b32 v14,a18
v_accvgpr_read_b32 v15,a19
v_mfma_f32_16x16x32_bf16 a[24:27],v[116:119],a[72:75],a[24:27]
v_cvt_pk_bf16_f32 v10,v12,v13
v_cvt_pk_bf16_f32 v11,v14,v15
s_nop 0
L_pd23_%=:
s_cbranch_execz L_trtg2S_%=
v_mfma_f32_16x16x32_bf16 a[24:27],v[120:123],a[80:83],a[24:27]
buffer_store_dwordx4 v[8:11],v6,s[36:39],s76 offen sc1
v_mfma_f32_16x16x32_bf16 a[24:27],v[124:127],a[88:91],a[24:27]
v_accvgpr_read_b32 v20,a20
v_accvgpr_read_b32 v21,a21
v_mfma_f32_16x16x32_bf16 a[28:31],v[124:127],a[120:123],a[28:31]
v_accvgpr_read_b32 v22,a22
v_accvgpr_read_b32 v23,a23
v_mfma_f32_16x16x32_bf16 a[28:31],v[120:123],a[112:115],a[28:31]
v_cvt_pk_bf16_f32 v18,v20,v21
v_cvt_pk_bf16_f32 v19,v22,v23
v_mfma_f32_16x16x32_bf16 a[28:31],v[116:119],a[104:107],a[28:31]
v_accvgpr_read_b32 v8,a8
v_accvgpr_read_b32 v9,a9
v_mfma_f32_16x16x32_bf16 a[28:31],v[112:115],a[96:99],a[28:31]
v_accvgpr_read_b32 v10,a10
v_accvgpr_read_b32 v11,a11
v_mfma_f32_16x16x32_bf16 v[128:131],v[64:67],a[128:131],v[128:131]
v_accvgpr_read_b32 v12,a24
v_accvgpr_read_b32 v13,a25
v_mfma_f32_16x16x32_bf16 v[128:131],v[68:71],a[144:147],v[128:131]
v_accvgpr_read_b32 v14,a26
v_accvgpr_read_b32 v15,a27
v_mfma_f32_16x16x32_bf16 v[128:131],v[72:75],a[160:163],v[128:131]
v_cvt_pk_bf16_f32 v8,v8,v9
v_cvt_pk_bf16_f32 v9,v10,v11
v_mfma_f32_16x16x32_bf16 v[128:131],v[76:79],a[176:179],v[128:131]
v_cvt_pk_bf16_f32 v10,v12,v13
v_cvt_pk_bf16_f32 v11,v14,v15
v_mfma_f32_16x16x32_bf16 v[136:139],v[76:79],a[180:183],v[136:139]
ds_read_b64_tr_b16 v[112:113],v2 offset:224
ds_read_b64_tr_b16 v[114:115],v2 offset:5344
v_mfma_f32_16x16x32_bf16 v[136:139],v[72:75],a[164:167],v[136:139]
buffer_store_dwordx4 v[16:19],v6,s[36:39],s76 offen offset:1024 sc1
v_mfma_f32_16x16x32_bf16 v[136:139],v[68:71],a[148:151],v[136:139]
ds_read_b64_tr_b16 v[116:117],v2 offset:10464
ds_read_b64_tr_b16 v[118:119],v2 offset:15584
v_mfma_f32_16x16x32_bf16 v[136:139],v[64:67],a[132:135],v[136:139]
ds_read_b64_tr_b16 v[120:121],v2 offset:20704
v_accvgpr_read_b32 v16,a12
v_mfma_f32_16x16x32_bf16 v[144:147],v[64:67],a[136:139],v[144:147]
ds_read_b64_tr_b16 v[122:123],v2 offset:25824
v_accvgpr_read_b32 v17,a13
v_mfma_f32_16x16x32_bf16 v[144:147],v[68:71],a[152:155],v[144:147]
ds_read_b64_tr_b16 v[124:125],v2 offset:30944
v_accvgpr_read_b32 v18,a14
v_mfma_f32_16x16x32_bf16 v[144:147],v[72:75],a[168:171],v[144:147]
ds_read_b64_tr_b16 v[126:127],v2 offset:36064
v_accvgpr_read_b32 v19,a15
v_mfma_f32_16x16x32_bf16 v[144:147],v[76:79],a[184:187],v[144:147]
v_accvgpr_read_b32 v20,a28
v_accvgpr_read_b32 v21,a29
v_mfma_f32_16x16x32_bf16 v[152:155],v[76:79],a[188:191],v[152:155]
v_accvgpr_read_b32 v22,a30
v_accvgpr_read_b32 v23,a31
v_mfma_f32_16x16x32_bf16 v[152:155],v[72:75],a[172:175],v[152:155]
v_cvt_pk_bf16_f32 v16,v16,v17
v_cvt_pk_bf16_f32 v17,v18,v19
v_mfma_f32_16x16x32_bf16 v[152:155],v[68:71],a[156:159],v[152:155]
v_cvt_pk_bf16_f32 v18,v20,v21
v_cvt_pk_bf16_f32 v19,v22,v23
v_mfma_f32_16x16x32_bf16 v[152:155],v[64:67],a[140:143],v[152:155]
s_waitcnt lgkmcnt(3)
ds_read_b64_tr_b16 v[64:65],v1 offset:256
ds_read_b64_tr_b16 v[66:67],v1 offset:5376
v_mfma_f32_16x16x32_bf16 v[160:163],v[80:83],a[128:131],v[160:163]
buffer_store_dwordx4 v[8:11],v6,s[36:39],s76 offen offset:2048 sc1
v_mfma_f32_16x16x32_bf16 v[160:163],v[84:87],a[144:147],v[160:163]
ds_read_b64_tr_b16 v[68:69],v1 offset:10496
ds_read_b64_tr_b16 v[70:71],v1 offset:15616
v_mfma_f32_16x16x32_bf16 v[160:163],v[88:91],a[160:163],v[160:163]
ds_read_b64_tr_b16 v[72:73],v1 offset:20736
v_add_u32 v2,0x14000,v2
v_mfma_f32_16x16x32_bf16 v[160:163],v[92:95],a[176:179],v[160:163]
ds_read_b64_tr_b16 v[74:75],v1 offset:25856
v_mov_b32 v20,s52
v_mfma_f32_16x16x32_bf16 v[168:171],v[92:95],a[180:183],v[168:171]
ds_read_b64_tr_b16 v[76:77],v1 offset:30976
v_mov_b32 v21,s53
v_mfma_f32_16x16x32_bf16 v[168:171],v[88:91],a[164:167],v[168:171]
ds_read_b64_tr_b16 v[78:79],v1 offset:36096
v_add_u32 v1,0xffff6000,v1
v_mfma_f32_16x16x32_bf16 v[168:171],v[84:87],a[148:151],v[168:171]
v_lshlrev_b32 v22,20,v6
v_mfma_f32_16x16x32_bf16 v[168:171],v[80:83],a[132:135],v[168:171]
v_mfma_f32_16x16x32_bf16 v[176:179],v[80:83],a[136:139],v[176:179]
v_mfma_f32_16x16x32_bf16 v[176:179],v[84:87],a[152:155],v[176:179]
v_mfma_f32_16x16x32_bf16 v[176:179],v[88:91],a[168:171],v[176:179]
v_mfma_f32_16x16x32_bf16 v[176:179],v[92:95],a[184:187],v[176:179]
buffer_store_dwordx4 v[16:19],v28,s[36:39],s76 offen offset:3072 sc1
v_mfma_f32_16x16x32_bf16 v[184:187],v[92:95],a[188:191],v[184:187]
ds_read_b64_tr_b16 v[92:93],v2 offset:31008
ds_read_b64_tr_b16 v[94:95],v2 offset:36128
v_mfma_f32_16x16x32_bf16 v[184:187],v[88:91],a[172:175],v[184:187]
ds_read_b64_tr_b16 v[88:89],v2 offset:20768
v_mfma_f32_16x16x32_bf16 v[184:187],v[84:87],a[156:159],v[184:187]
ds_read_b64_tr_b16 v[84:85],v2 offset:10528
v_mfma_f32_16x16x32_bf16 v[184:187],v[80:83],a[140:143],v[184:187]
ds_read_b64_tr_b16 v[80:81],v2 offset:288
v_mfma_f32_16x16x32_bf16 v[128:131],v[96:99],a[68:71],v[128:131]
ds_read_b64_tr_b16 v[82:83],v2 offset:5408
ds_read_b64_tr_b16 v[86:87],v2 offset:15648
v_mfma_f32_16x16x32_bf16 v[128:131],v[100:103],a[76:79],v[128:131]
ds_read_b64_tr_b16 v[90:91],v2 offset:25888
v_add_u32 v2,0xffff6000,v2
v_mfma_f32_16x16x32_bf16 v[128:131],v[104:107],a[84:87],v[128:131]
v_mfma_f32_16x16x32_bf16 v[128:131],v[108:111],a[92:95],v[128:131]
v_mfma_f32_16x16x32_bf16 v[136:139],v[108:111],a[124:127],v[136:139]
v_mfma_f32_16x16x32_bf16 v[136:139],v[104:107],a[116:119],v[136:139]
v_mfma_f32_16x16x32_bf16 v[136:139],v[100:103],a[108:111],v[136:139]
v_mfma_f32_16x16x32_bf16 v[136:139],v[96:99],a[100:103],v[136:139]
v_mfma_f32_16x16x32_bf16 v[144:147],v[96:99],a[64:67],v[144:147]
v_mfma_f32_16x16x32_bf16 v[144:147],v[100:103],a[72:75],v[144:147]
v_mfma_f32_16x16x32_bf16 v[144:147],v[104:107],a[80:83],v[144:147]
v_mfma_f32_16x16x32_bf16 v[144:147],v[108:111],a[88:91],v[144:147]
v_cvt_pk_bf16_f32 v128,v128,v129
v_cvt_pk_bf16_f32 v129,v130,v131
v_mfma_f32_16x16x32_bf16 v[152:155],v[108:111],a[120:123],v[152:155]
ds_read_b64_tr_b16 v[108:109],v1 offset:30976
ds_read_b64_tr_b16 v[110:111],v1 offset:36096
v_mfma_f32_16x16x32_bf16 v[152:155],v[104:107],a[112:115],v[152:155]
v_cvt_pk_bf16_f32 v136,v136,v137
v_cvt_pk_bf16_f32 v137,v138,v139
v_mfma_f32_16x16x32_bf16 v[152:155],v[100:103],a[104:107],v[152:155]
v_cvt_pk_bf16_f32 v144,v144,v145
v_cvt_pk_bf16_f32 v145,v146,v147
v_mfma_f32_16x16x32_bf16 v[152:155],v[96:99],a[96:99],v[152:155]
ds_read_b64_tr_b16 v[96:97],v1 offset:256
ds_read_b64_tr_b16 v[98:99],v1 offset:5376
v_mfma_f32_16x16x32_bf16 v[160:163],v[112:115],a[68:71],v[160:163]
ds_read_b64_tr_b16 v[100:101],v1 offset:10496
v_mfma_f32_16x16x32_bf16 v[160:163],v[116:119],a[76:79],v[160:163]
s_waitcnt lgkmcnt(5)
ds_read_b64_tr_b16 v[102:103],v1 offset:15616
v_mfma_f32_16x16x32_bf16 v[160:163],v[120:123],a[84:87],v[160:163]
ds_read_b64_tr_b16 v[104:105],v1 offset:20736
v_mfma_f32_16x16x32_bf16 v[160:163],v[124:127],a[92:95],v[160:163]
v_cvt_pk_bf16_f32 v152,v152,v153
v_cvt_pk_bf16_f32 v153,v154,v155
v_mfma_f32_16x16x32_bf16 v[168:171],v[124:127],a[124:127],v[168:171]
ds_read_b64_tr_b16 v[106:107],v1 offset:25856
v_add_u32 v1,0xffff6000,v1
v_mfma_f32_16x16x32_bf16 v[168:171],v[120:123],a[116:119],v[168:171]
v_mfma_f32_16x16x32_bf16 v[168:171],v[116:119],a[108:111],v[168:171]
v_mfma_f32_16x16x32_bf16 v[168:171],v[112:115],a[100:103],v[168:171]
)A"
R"A(v_cvt_pk_bf16_f32 v130,v160,v161
v_cvt_pk_bf16_f32 v131,v162,v163
v_mfma_f32_16x16x32_bf16 v[176:179],v[112:115],a[64:67],v[176:179]
buffer_store_dwordx4 v[128:131],v6,s[36:39],s79 offen sc1
v_mfma_f32_16x16x32_bf16 v[176:179],v[116:119],a[72:75],v[176:179]
v_mfma_f32_16x16x32_bf16 v[176:179],v[120:123],a[80:83],v[176:179]
v_mfma_f32_16x16x32_bf16 v[176:179],v[124:127],a[88:91],v[176:179]
v_mfma_f32_16x16x32_bf16 v[184:187],v[124:127],a[120:123],v[184:187]
v_cvt_pk_bf16_f32 v138,v168,v169
v_cvt_pk_bf16_f32 v139,v170,v171
v_mfma_f32_16x16x32_bf16 v[184:187],v[120:123],a[112:115],v[184:187]
buffer_store_dwordx4 v[136:139],v6,s[36:39],s80 offen sc1
v_mfma_f32_16x16x32_bf16 v[184:187],v[116:119],a[104:107],v[184:187]
ds_read_b64_tr_b16 v[116:117],v2 offset:10528
ds_read_b64_tr_b16 v[118:119],v2 offset:15648
v_mfma_f32_16x16x32_bf16 v[184:187],v[112:115],a[96:99],v[184:187]
v_cvt_pk_bf16_f32 v146,v176,v177
v_cvt_pk_bf16_f32 v147,v178,v179
s_waitcnt vmcnt(6)
s_barrier
v_mfma_f32_16x16x32_bf16 v[192:195],v[64:67],v[32:35],v[192:195]
buffer_store_dwordx4 v[144:147],v6,s[36:39],s81 offen sc1
v_mfma_f32_16x16x32_bf16 v[192:195],v[68:71],v[36:39],v[192:195]
ds_read_b64_tr_b16 v[112:113],v2 offset:288
ds_read_b64_tr_b16 v[114:115],v2 offset:5408
v_mfma_f32_16x16x32_bf16 v[192:195],v[72:75],v[40:43],v[192:195]
buffer_store_dwordx2 v[58:59],v60,s[44:47],s18 offen offset:2312 sc1
v_mfma_f32_16x16x32_bf16 v[192:195],v[76:79],v[44:47],v[192:195]
v_cvt_pk_bf16_f32 v154,v184,v185
v_cvt_pk_bf16_f32 v155,v186,v187
v_mfma_f32_16x16x32_bf16 v[200:203],v[76:79],v[180:183],v[200:203]
buffer_store_dwordx4 v[152:155],v28,s[36:39],s82 offen sc1
v_mfma_f32_16x16x32_bf16 v[200:203],v[72:75],v[164:167],v[200:203]
ds_read_b64_tr_b16 v[120:121],v2 offset:20768
ds_read_b64_tr_b16 v[122:123],v2 offset:25888
v_mfma_f32_16x16x32_bf16 v[200:203],v[68:71],v[148:151],v[200:203]
ds_read_b64_tr_b16 v[124:125],v2 offset:31008
v_mfma_f32_16x16x32_bf16 v[200:203],v[64:67],v[132:135],v[200:203]
ds_read_b64_tr_b16 v[126:127],v2 offset:36128
v_add_u32 v2,0xffff6000,v2
v_mfma_f32_16x16x32_bf16 v[208:211],v[64:67],v[196:199],v[208:211]
v_mfma_f32_16x16x32_bf16 v[208:211],v[68:71],v[212:215],v[208:211]
v_mfma_f32_16x16x32_bf16 v[208:211],v[72:75],v[228:231],v[208:211]
v_mfma_f32_16x16x32_bf16 v[208:211],v[76:79],v[244:247],v[208:211]
v_mfma_f32_16x16x32_bf16 v[216:219],v[76:79],v[188:191],v[216:219]
ds_read_b64_tr_b16 v[76:77],v1 offset:30976
ds_read_b64_tr_b16 v[78:79],v1 offset:36096
v_mfma_f32_16x16x32_bf16 v[216:219],v[72:75],v[172:175],v[216:219]
ds_read_b64_tr_b16 v[72:73],v1 offset:20736
v_mfma_f32_16x16x32_bf16 v[216:219],v[68:71],v[156:159],v[216:219]
ds_read_b64_tr_b16 v[68:69],v1 offset:10496
v_mfma_f32_16x16x32_bf16 v[216:219],v[64:67],v[140:143],v[216:219]
ds_read_b64_tr_b16 v[64:65],v1 offset:256
v_mfma_f32_16x16x32_bf16 v[224:227],v[80:83],v[32:35],v[224:227]
ds_read_b64_tr_b16 v[66:67],v1 offset:5376
ds_read_b64_tr_b16 v[70:71],v1 offset:15616
v_mfma_f32_16x16x32_bf16 v[224:227],v[84:87],v[36:39],v[224:227]
ds_read_b64_tr_b16 v[74:75],v1 offset:25856
v_mfma_f32_16x16x32_bf16 v[224:227],v[88:91],v[40:43],v[224:227]
v_mfma_f32_16x16x32_bf16 v[224:227],v[92:95],v[44:47],v[224:227]
v_mfma_f32_16x16x32_bf16 v[232:235],v[92:95],v[180:183],v[232:235]
v_mfma_f32_16x16x32_bf16 v[232:235],v[88:91],v[164:167],v[232:235]
v_mfma_f32_16x16x32_bf16 v[232:235],v[84:87],v[148:151],v[232:235]
v_mfma_f32_16x16x32_bf16 v[232:235],v[80:83],v[132:135],v[232:235]
v_mfma_f32_16x16x32_bf16 v[240:243],v[80:83],v[196:199],v[240:243]
v_mfma_f32_16x16x32_bf16 v[240:243],v[84:87],v[212:215],v[240:243]
v_mfma_f32_16x16x32_bf16 v[240:243],v[88:91],v[228:231],v[240:243]
v_mfma_f32_16x16x32_bf16 v[240:243],v[92:95],v[244:247],v[240:243]
v_mfma_f32_16x16x32_bf16 v[248:251],v[92:95],v[188:191],v[248:251]
ds_read_b64_tr_b16 v[92:93],v2 offset:31008
ds_read_b64_tr_b16 v[94:95],v2 offset:36128
v_mfma_f32_16x16x32_bf16 v[248:251],v[88:91],v[172:175],v[248:251]
ds_read_b64_tr_b16 v[88:89],v2 offset:20768
v_mfma_f32_16x16x32_bf16 v[248:251],v[84:87],v[156:159],v[248:251]
ds_read_b64_tr_b16 v[84:85],v2 offset:10528
v_mfma_f32_16x16x32_bf16 v[248:251],v[80:83],v[140:143],v[248:251]
s_waitcnt lgkmcnt(4)
ds_read_b64_tr_b16 v[80:81],v2 offset:288
v_mfma_f32_16x16x32_bf16 v[192:195],v[96:99],a[128:131],v[192:195]
ds_read_b64_tr_b16 v[82:83],v2 offset:5408
ds_read_b64_tr_b16 v[86:87],v2 offset:15648
v_mfma_f32_16x16x32_bf16 v[192:195],v[100:103],a[144:147],v[192:195]
ds_read_b64_tr_b16 v[90:91],v2 offset:25888
v_mfma_f32_16x16x32_bf16 v[192:195],v[104:107],a[160:163],v[192:195]
v_mfma_f32_16x16x32_bf16 v[192:195],v[108:111],a[176:179],v[192:195]
v_mfma_f32_16x16x32_bf16 v[200:203],v[108:111],a[180:183],v[200:203]
v_mfma_f32_16x16x32_bf16 v[200:203],v[104:107],a[164:167],v[200:203]
v_mfma_f32_16x16x32_bf16 v[200:203],v[100:103],a[148:151],v[200:203]
v_mfma_f32_16x16x32_bf16 v[200:203],v[96:99],a[132:135],v[200:203]
v_mfma_f32_16x16x32_bf16 v[208:211],v[96:99],a[136:139],v[208:211]
v_mfma_f32_16x16x32_bf16 v[208:211],v[100:103],a[152:155],v[208:211]
v_mfma_f32_16x16x32_bf16 v[208:211],v[104:107],a[168:171],v[208:211]
v_mfma_f32_16x16x32_bf16 v[208:211],v[108:111],a[184:187],v[208:211]
v_mfma_f32_16x16x32_bf16 v[216:219],v[108:111],a[188:191],v[216:219]
v_mfma_f32_16x16x32_bf16 v[216:219],v[104:107],a[172:175],v[216:219]
v_mfma_f32_16x16x32_bf16 v[216:219],v[100:103],a[156:159],v[216:219]
v_mfma_f32_16x16x32_bf16 v[216:219],v[96:99],a[140:143],v[216:219]
v_mfma_f32_16x16x32_bf16 v[224:227],v[112:115],a[128:131],v[224:227]
v_mfma_f32_16x16x32_bf16 v[224:227],v[116:119],a[144:147],v[224:227]
v_mfma_f32_16x16x32_bf16 v[224:227],v[120:123],a[160:163],v[224:227]
v_mfma_f32_16x16x32_bf16 v[224:227],v[124:127],a[176:179],v[224:227]
v_mfma_f32_16x16x32_bf16 v[232:235],v[124:127],a[180:183],v[232:235]
v_mfma_f32_16x16x32_bf16 v[232:235],v[120:123],a[164:167],v[232:235]
v_mfma_f32_16x16x32_bf16 v[232:235],v[116:119],a[148:151],v[232:235]
v_mfma_f32_16x16x32_bf16 v[232:235],v[112:115],a[132:135],v[232:235]
v_mfma_f32_16x16x32_bf16 v[240:243],v[112:115],a[136:139],v[240:243]
v_mfma_f32_16x16x32_bf16 v[240:243],v[116:119],a[152:155],v[240:243]
v_mfma_f32_16x16x32_bf16 v[240:243],v[120:123],a[168:171],v[240:243]
v_mfma_f32_16x16x32_bf16 v[240:243],v[124:127],a[184:187],v[240:243]
v_mfma_f32_16x16x32_bf16 v[248:251],v[124:127],a[188:191],v[248:251]
v_mfma_f32_16x16x32_bf16 v[248:251],v[120:123],a[172:175],v[248:251]
v_mfma_f32_16x16x32_bf16 v[248:251],v[116:119],a[156:159],v[248:251]
v_mfma_f32_16x16x32_bf16 v[248:251],v[112:115],a[140:143],v[248:251]
s_waitcnt vmcnt(0)
s_barrier
v_mfma_f32_16x16x32_bf16 v[192:195],v[64:67],a[68:71],v[192:195]
buffer_store_dwordx2 v[58:59],v60,s[44:47],s18 offen offset:2304 sc1
v_mfma_f32_16x16x32_bf16 v[192:195],v[68:71],a[76:79],v[192:195]
v_mfma_f32_16x16x32_bf16 v[192:195],v[72:75],a[84:87],v[192:195]
v_mfma_f32_16x16x32_bf16 v[192:195],v[76:79],a[92:95],v[192:195]
v_mfma_f32_16x16x32_bf16 v[200:203],v[76:79],a[124:127],v[200:203]
v_mfma_f32_16x16x32_bf16 v[200:203],v[72:75],a[116:119],v[200:203]
v_mfma_f32_16x16x32_bf16 v[200:203],v[68:71],a[108:111],v[200:203]
v_mfma_f32_16x16x32_bf16 v[200:203],v[64:67],a[100:103],v[200:203]
v_mfma_f32_16x16x32_bf16 v[208:211],v[64:67],a[64:67],v[208:211]
v_mfma_f32_16x16x32_bf16 v[208:211],v[68:71],a[72:75],v[208:211]
v_mfma_f32_16x16x32_bf16 v[208:211],v[72:75],a[80:83],v[208:211]
v_mfma_f32_16x16x32_bf16 v[208:211],v[76:79],a[88:91],v[208:211]
v_mfma_f32_16x16x32_bf16 v[216:219],v[76:79],a[120:123],v[216:219]
v_mfma_f32_16x16x32_bf16 v[216:219],v[72:75],a[112:115],v[216:219]
v_mfma_f32_16x16x32_bf16 v[216:219],v[68:71],a[104:107],v[216:219]
v_mfma_f32_16x16x32_bf16 v[216:219],v[64:67],a[96:99],v[216:219]
s_waitcnt lgkmcnt(0)
v_mfma_f32_16x16x32_bf16 v[224:227],v[80:83],a[68:71],v[224:227]
buffer_load_dwordx2 v[62:63],v61,s[44:47],s91 offen offset:2312 sc1
v_mfma_f32_16x16x32_bf16 v[224:227],v[84:87],a[76:79],v[224:227]
v_mfma_f32_16x16x32_bf16 v[224:227],v[88:91],a[84:87],v[224:227]
v_mfma_f32_16x16x32_bf16 v[224:227],v[92:95],a[92:95],v[224:227]
v_mfma_f32_16x16x32_bf16 v[232:235],v[92:95],a[124:127],v[232:235]
v_mfma_f32_16x16x32_bf16 v[232:235],v[88:91],a[116:119],v[232:235]
v_mfma_f32_16x16x32_bf16 v[232:235],v[84:87],a[108:111],v[232:235]
v_mfma_f32_16x16x32_bf16 v[232:235],v[80:83],a[100:103],v[232:235]
v_mfma_f32_16x16x32_bf16 v[240:243],v[80:83],a[64:67],v[240:243]
v_mfma_f32_16x16x32_bf16 v[240:243],v[84:87],a[72:75],v[240:243]
v_mfma_f32_16x16x32_bf16 v[240:243],v[88:91],a[80:83],v[240:243]
v_mfma_f32_16x16x32_bf16 v[240:243],v[92:95],a[88:91],v[240:243]
v_mfma_f32_16x16x32_bf16 v[248:251],v[92:95],a[120:123],v[248:251]
v_mfma_f32_16x16x32_bf16 v[248:251],v[88:91],a[112:115],v[248:251]
v_mfma_f32_16x16x32_bf16 v[248:251],v[84:87],a[104:107],v[248:251]
v_mfma_f32_16x16x32_bf16 v[248:251],v[80:83],a[96:99],v[248:251]
s_waitcnt vmcnt(0)
v_cmp_ne_u32 s[56:57],s52,v62
v_cmp_ne_u32 s[58:59],s53,v63
s_nop 0
s_or_b64 s[56:57],s[56:57],s[58:59]
s_cbranch_scc1 L_fr8_%=
L_fo8_%=:
buffer_load_dwordx4 v[64:67],v6,s[36:39],s66 offen sc1
buffer_load_dwordx4 v[68:71],v6,s[36:39],s67 offen sc1
buffer_load_dwordx4 v[72:75],v6,s[36:39],s66 offen offset:1024 sc1
buffer_load_dwordx4 v[76:79],v6,s[36:39],s67 offen offset:1024 sc1
buffer_load_dwordx4 v[80:83],v6,s[36:39],s66 offen offset:2048 sc1
buffer_load_dwordx4 v[84:87],v6,s[36:39],s67 offen offset:2048 sc1
buffer_load_dwordx4 v[88:91],v28,s[36:39],s66 offen offset:3072 sc1
buffer_load_dwordx4 v[92:95],v28,s[36:39],s67 offen offset:3072 sc1
buffer_load_dwordx2 v[4:5],v3,s[44:47],s91 offen offset:2304 sc1
s_waitcnt vmcnt(8)
v_mfma_f32_16x16x32_bf16 v[192:195],v[48:51],v[64:67],v[192:195]
s_waitcnt vmcnt(7)
v_mfma_f32_16x16x32_bf16 v[192:195],v[48:51],v[68:71],v[192:195]
v_mfma_f32_16x16x32_bf16 v[224:227],v[52:55],v[64:67],v[224:227]
v_mfma_f32_16x16x32_bf16 v[224:227],v[52:55],v[68:71],v[224:227]
s_waitcnt vmcnt(6)
v_mfma_f32_16x16x32_bf16 v[200:203],v[48:51],v[72:75],v[200:203]
s_waitcnt vmcnt(5)
v_mfma_f32_16x16x32_bf16 v[200:203],v[48:51],v[76:79],v[200:203]
v_mfma_f32_16x16x32_bf16 v[232:235],v[52:55],v[72:75],v[232:235]
v_mfma_f32_16x16x32_bf16 v[232:235],v[52:55],v[76:79],v[232:235]
s_waitcnt vmcnt(4)
v_mfma_f32_16x16x32_bf16 v[208:211],v[48:51],v[80:83],v[208:211]
s_waitcnt vmcnt(3)
v_mfma_f32_16x16x32_bf16 v[208:211],v[48:51],v[84:87],v[208:211]
v_mfma_f32_16x16x32_bf16 v[240:243],v[52:55],v[80:83],v[240:243]
v_mfma_f32_16x16x32_bf16 v[240:243],v[52:55],v[84:87],v[240:243]
s_waitcnt vmcnt(2)
v_mfma_f32_16x16x32_bf16 v[216:219],v[48:51],v[88:91],v[216:219]
s_waitcnt vmcnt(1)
v_mfma_f32_16x16x32_bf16 v[216:219],v[48:51],v[92:95],v[216:219]
v_mfma_f32_16x16x32_bf16 v[248:251],v[52:55],v[88:91],v[248:251]
v_mfma_f32_16x16x32_bf16 v[248:251],v[52:55],v[92:95],v[248:251]
s_waitcnt vmcnt(0)
v_cmp_ne_u32 s[56:57],s52,v4
v_cmp_ne_u32 s[58:59],s53,v5
s_nop 0
s_or_b64 s[56:57],s[56:57],s[58:59]
s_cbranch_scc1 L_fr9_%=
L_fo9_%=:
buffer_load_dwordx4 v[96:99],v6,s[36:39],s68 offen sc1
buffer_load_dwordx4 v[100:103],v6,s[36:39],s68 offen offset:1024 sc1
buffer_load_dwordx4 v[104:107],v6,s[36:39],s68 offen offset:2048 sc1
buffer_load_dwordx4 v[108:111],v6,s[36:39],s68 offen offset:3072 sc1
buffer_load_dwordx4 v[112:115],v6,s[36:39],s65 offen sc1
buffer_load_dwordx4 v[116:119],v6,s[36:39],s65 offen offset:1024 sc1
buffer_load_dwordx4 v[120:123],v6,s[36:39],s65 offen offset:2048 sc1
buffer_load_dwordx4 v[124:127],v28,s[36:39],s65 offen offset:3072 sc1
s_waitcnt vmcnt(7)
v_mfma_f32_16x16x32_bf16 v[128:131],v[48:51],v[96:99],0
s_waitcnt vmcnt(6)
v_mfma_f32_16x16x32_bf16 v[128:131],v[48:51],v[100:103],v[128:131]
s_waitcnt vmcnt(5)
v_mfma_f32_16x16x32_bf16 v[128:131],v[48:51],v[104:107],v[128:131]
s_waitcnt vmcnt(4)
v_mfma_f32_16x16x32_bf16 v[128:131],v[48:51],v[108:111],v[128:131]
v_mfma_f32_16x16x32_bf16 v[160:163],v[52:55],v[96:99],0
v_mfma_f32_16x16x32_bf16 v[160:163],v[52:55],v[100:103],v[160:163]
v_mfma_f32_16x16x32_bf16 v[160:163],v[52:55],v[104:107],v[160:163]
)A"
R"A(v_mfma_f32_16x16x32_bf16 v[160:163],v[52:55],v[108:111],v[160:163]
s_waitcnt vmcnt(3)
v_mfma_f32_16x16x32_bf16 v[192:195],v[48:51],v[112:115],v[192:195]
v_mfma_f32_16x16x32_bf16 v[224:227],v[52:55],v[112:115],v[224:227]
s_waitcnt vmcnt(2)
v_mfma_f32_16x16x32_bf16 v[200:203],v[48:51],v[116:119],v[200:203]
v_mfma_f32_16x16x32_bf16 v[232:235],v[52:55],v[116:119],v[232:235]
s_waitcnt vmcnt(1)
v_cvt_pk_bf16_f32 v128,v128,v129
v_cvt_pk_bf16_f32 v129,v130,v131
v_mfma_f32_16x16x32_bf16 v[208:211],v[48:51],v[120:123],v[208:211]
v_mfma_f32_16x16x32_bf16 v[240:243],v[52:55],v[120:123],v[240:243]
s_waitcnt vmcnt(0)
v_cvt_pk_bf16_f32 v130,v160,v161
v_cvt_pk_bf16_f32 v131,v162,v163
v_mfma_f32_16x16x32_bf16 v[216:219],v[48:51],v[124:127],v[216:219]
v_cvt_pk_bf16_f32 v192,v192,v193
v_cvt_pk_bf16_f32 v193,v194,v195
v_cvt_pk_bf16_f32 v194,v224,v225
v_cvt_pk_bf16_f32 v195,v226,v227
v_cvt_pk_bf16_f32 v200,v200,v201
v_cvt_pk_bf16_f32 v201,v202,v203
v_mfma_f32_16x16x32_bf16 v[248:251],v[52:55],v[124:127],v[248:251]
v_permlane16_swap_b32 v128,v130
v_permlane16_swap_b32 v129,v131
v_cvt_pk_bf16_f32 v202,v232,v233
v_cvt_pk_bf16_f32 v203,v234,v235
v_cvt_pk_bf16_f32 v208,v208,v209
v_cvt_pk_bf16_f32 v209,v210,v211
v_cvt_pk_bf16_f32 v210,v240,v241
v_cvt_pk_bf16_f32 v211,v242,v243
buffer_store_dwordx4 v[128:131],v56,s[24:27],s83 offen offset:256 sc1 nt
v_permlane16_swap_b32 v192,v194
v_permlane16_swap_b32 v193,v195
buffer_store_dwordx4 v[192:195],v56,s[24:27],s87 offen sc1 nt
v_permlane16_swap_b32 v200,v202
v_permlane16_swap_b32 v201,v203
v_cvt_pk_bf16_f32 v216,v216,v217
v_cvt_pk_bf16_f32 v217,v218,v219
v_cvt_pk_bf16_f32 v218,v248,v249
v_cvt_pk_bf16_f32 v219,v250,v251
buffer_store_dwordx4 v[200:203],v56,s[24:27],s88 offen sc1 nt
v_permlane16_swap_b32 v208,v210
v_permlane16_swap_b32 v209,v211
buffer_store_dwordx4 v[208:211],v56,s[24:27],s89 offen sc1 nt
v_permlane16_swap_b32 v216,v218
v_permlane16_swap_b32 v217,v219
buffer_store_dwordx4 v[216:219],v56,s[24:27],s90 offen sc1 nt
s_lshl_b32 s69,s62,20
s_add_u32 s69,s69,s18
buffer_store_dword v20,v22,s[44:47],s69 offen sc1
L_geS_%=:
s_branch L_end_%=
L_hot_%=:
L_fr1_%=:
s_mov_b32 s19,65536
L_fl1_%=:
buffer_load_dwordx2 v[4:5],v3,s[44:47],s91 offen offset:256 sc1
s_waitcnt vmcnt(0)
s_sub_u32 s19,s19,1
s_cmp_eq_u32 s19,0
s_cbranch_scc1 L_fo1_%=
v_cmp_ne_u32 s[56:57],s52,v4
v_cmp_ne_u32 s[58:59],s53,v5
s_nop 1
s_or_b64 s[56:57],s[56:57],s[58:59]
s_cbranch_scc1 L_fl1_%=
s_branch L_fo1_%=
L_fr2_%=:
s_mov_b32 s19,65536
L_fl2_%=:
buffer_load_dwordx2 v[4:5],v3,s[44:47],s91 offen offset:1280 sc1
s_waitcnt vmcnt(0)
s_sub_u32 s19,s19,1
s_cmp_eq_u32 s19,0
s_cbranch_scc1 L_fo2_%=
v_cmp_ne_u32 s[56:57],s52,v4
v_cmp_ne_u32 s[58:59],s53,v5
s_nop 1
s_or_b64 s[56:57],s[56:57],s[58:59]
s_cbranch_scc1 L_fl2_%=
s_branch L_fo2_%=
L_fr3_%=:
s_mov_b32 s19,65536
L_fl3_%=:
buffer_load_dwordx2 v[4:5],v3,s[44:47],s91 offen offset:1280 sc1
s_waitcnt vmcnt(0)
s_sub_u32 s19,s19,1
s_cmp_eq_u32 s19,0
s_cbranch_scc1 L_fo3_%=
v_cmp_ne_u32 s[56:57],s52,v4
v_cmp_ne_u32 s[58:59],s53,v5
s_nop 1
s_or_b64 s[56:57],s[56:57],s[58:59]
s_cbranch_scc1 L_fl3_%=
s_branch L_fo3_%=
L_fr4_%=:
s_mov_b32 s19,65536
L_fl4_%=:
buffer_load_dwordx2 v[62:63],v61,s[44:47],s91 offen offset:2312 sc1
s_waitcnt vmcnt(0)
s_sub_u32 s19,s19,1
s_cmp_eq_u32 s19,0
s_cbranch_scc1 L_fo4_%=
v_cmp_ne_u32 s[56:57],s52,v62
v_cmp_ne_u32 s[58:59],s53,v63
s_nop 1
s_or_b64 s[56:57],s[56:57],s[58:59]
s_cbranch_scc1 L_fl4_%=
s_branch L_fo4_%=
L_fr5_%=:
s_mov_b32 s19,65536
L_fl5_%=:
buffer_load_dwordx2 v[4:5],v3,s[44:47],s91 offen offset:2304 sc1
s_waitcnt vmcnt(0)
s_sub_u32 s19,s19,1
s_cmp_eq_u32 s19,0
s_cbranch_scc1 L_fo5_%=
v_cmp_ne_u32 s[56:57],s52,v4
v_cmp_ne_u32 s[58:59],s53,v5
s_nop 1
s_or_b64 s[56:57],s[56:57],s[58:59]
s_cbranch_scc1 L_fl5_%=
s_branch L_fo5_%=
L_fr6_%=:
s_mov_b32 s19,65536
L_fl6_%=:
buffer_load_dwordx2 v[4:5],v3,s[44:47],s91 offen offset:256 sc1
s_waitcnt vmcnt(0)
s_sub_u32 s19,s19,1
s_cmp_eq_u32 s19,0
s_cbranch_scc1 L_fo6_%=
v_cmp_ne_u32 s[56:57],s52,v4
v_cmp_ne_u32 s[58:59],s53,v5
s_nop 1
s_or_b64 s[56:57],s[56:57],s[58:59]
s_cbranch_scc1 L_fl6_%=
s_branch L_fo6_%=
L_fr7_%=:
s_mov_b32 s19,65536
L_fl7_%=:
buffer_load_dwordx2 v[4:5],v3,s[44:47],s91 offen offset:1280 sc1
s_waitcnt vmcnt(0)
s_sub_u32 s19,s19,1
s_cmp_eq_u32 s19,0
s_cbranch_scc1 L_fo7_%=
v_cmp_ne_u32 s[56:57],s52,v4
v_cmp_ne_u32 s[58:59],s53,v5
s_nop 1
s_or_b64 s[56:57],s[56:57],s[58:59]
s_cbranch_scc1 L_fl7_%=
s_branch L_fo7_%=
L_fr8_%=:
s_mov_b32 s19,65536
L_fl8_%=:
buffer_load_dwordx2 v[62:63],v61,s[44:47],s91 offen offset:2312 sc1
s_waitcnt vmcnt(0)
s_sub_u32 s19,s19,1
s_cmp_eq_u32 s19,0
s_cbranch_scc1 L_fo8_%=
v_cmp_ne_u32 s[56:57],s52,v62
v_cmp_ne_u32 s[58:59],s53,v63
s_nop 1
s_or_b64 s[56:57],s[56:57],s[58:59]
s_cbranch_scc1 L_fl8_%=
s_branch L_fo8_%=
L_fr9_%=:
s_mov_b32 s19,65536
L_fl9_%=:
buffer_load_dwordx2 v[4:5],v3,s[44:47],s91 offen offset:2304 sc1
s_waitcnt vmcnt(0)
s_sub_u32 s19,s19,1
s_cmp_eq_u32 s19,0
s_cbranch_scc1 L_fo9_%=
v_cmp_ne_u32 s[56:57],s52,v4
v_cmp_ne_u32 s[58:59],s53,v5
s_nop 1
s_or_b64 s[56:57],s[56:57],s[58:59]
s_cbranch_scc1 L_fl9_%=
s_branch L_fo9_%=
L_slow0_%=:
s_waitcnt vmcnt(0) lgkmcnt(0)
s_nop 7
v_accvgpr_write_b32 a160,v32
v_accvgpr_write_b32 a161,v33
v_accvgpr_write_b32 a162,v34
v_accvgpr_write_b32 a163,v35
v_accvgpr_write_b32 a164,v36
v_accvgpr_write_b32 a165,v37
v_accvgpr_write_b32 a166,v38
v_accvgpr_write_b32 a167,v39
v_accvgpr_write_b32 a168,v40
v_accvgpr_write_b32 a169,v41
v_accvgpr_write_b32 a170,v42
v_accvgpr_write_b32 a171,v43
v_accvgpr_write_b32 a172,v44
v_accvgpr_write_b32 a173,v45
v_accvgpr_write_b32 a174,v46
v_accvgpr_write_b32 a175,v47
s_waitcnt vmcnt(0) lgkmcnt(0)
s_nop 7
v_bfe_u32 v11,v6,4,4
v_lshrrev_b32 v12,8,v6
v_lshlrev_b32 v8,8,v11
v_lshl_add_u32 v8,v12,4,v8
v_lshlrev_b32 v9,10,v12
v_lshl_add_u32 v9,v11,1,v9
v_add_u32 v10,4096,v9
v_mov_b32 v48,0xff800000
v_mov_b32 v52,0xff800000
v_mov_b32 v56,0xff800000
v_mov_b32 v60,0xff800000
s_mov_b32 s65,0
s_mov_b32 s66,64
L_s10_%=:
buffer_load_dwordx4 a[128:131],v8,s[20:23],s65 offen
buffer_load_dwordx4 a[132:135],v8,s[20:23],s65 offen offset:64
buffer_load_dwordx4 a[136:139],v8,s[20:23],s65 offen offset:128
buffer_load_dwordx4 a[140:143],v8,s[20:23],s65 offen offset:192
s_waitcnt vmcnt(0)
v_mfma_f32_16x16x32_bf16 v[64:67],a[128:131],v[132:135],0
v_mfma_f32_16x16x32_bf16 v[64:67],a[132:135],v[140:143],v[64:67]
v_mfma_f32_16x16x32_bf16 v[64:67],a[136:139],v[148:151],v[64:67]
v_mfma_f32_16x16x32_bf16 v[64:67],a[140:143],v[156:159],v[64:67]
v_mfma_f32_16x16x32_bf16 v[80:83],a[128:131],v[164:167],0
v_mfma_f32_16x16x32_bf16 v[80:83],a[132:135],v[172:175],v[80:83]
v_mfma_f32_16x16x32_bf16 v[80:83],a[136:139],v[180:183],v[80:83]
v_mfma_f32_16x16x32_bf16 v[80:83],a[140:143],v[188:191],v[80:83]
v_mfma_f32_16x16x32_bf16 v[96:99],a[128:131],v[196:199],0
v_mfma_f32_16x16x32_bf16 v[96:99],a[132:135],v[204:207],v[96:99]
v_mfma_f32_16x16x32_bf16 v[96:99],a[136:139],v[212:215],v[96:99]
v_mfma_f32_16x16x32_bf16 v[96:99],a[140:143],v[220:223],v[96:99]
v_mfma_f32_16x16x32_bf16 v[112:115],a[128:131],v[228:231],0
v_mfma_f32_16x16x32_bf16 v[112:115],a[132:135],v[236:239],v[112:115]
v_mfma_f32_16x16x32_bf16 v[112:115],a[136:139],v[244:247],v[112:115]
v_mfma_f32_16x16x32_bf16 v[112:115],a[140:143],v[252:255],v[112:115]
s_nop 7
v_max3_f32 v48,v48,v64,v65
v_max3_f32 v48,v48,v66,v67
v_max3_f32 v52,v52,v80,v81
v_max3_f32 v52,v52,v82,v83
v_max3_f32 v56,v56,v96,v97
v_max3_f32 v56,v56,v98,v99
v_max3_f32 v60,v60,v112,v113
v_max3_f32 v60,v60,v114,v115
s_add_u32 s65,s65,4096
s_sub_u32 s66,s66,1
s_cmp_lg_u32 s66,0
s_cbranch_scc1 L_s10_%=
v_mov_b32 v11,v48
s_nop 1
v_permlane32_swap_b32 v48,v11
v_max_f32 v48,v48,v11
v_mov_b32 v11,v48
s_nop 1
v_permlane16_swap_b32 v48,v11
v_max_f32 v48,v48,v11
v_sub_f32 v32,0,v48
v_sub_f32 v33,0,v48
v_sub_f32 v34,0,v48
v_sub_f32 v35,0,v48
v_mov_b32 v11,v52
s_nop 1
v_permlane32_swap_b32 v52,v11
v_max_f32 v52,v52,v11
v_mov_b32 v11,v52
s_nop 1
v_permlane16_swap_b32 v52,v11
v_max_f32 v52,v52,v11
v_sub_f32 v36,0,v52
v_sub_f32 v37,0,v52
v_sub_f32 v38,0,v52
v_sub_f32 v39,0,v52
v_mov_b32 v11,v56
s_nop 1
v_permlane32_swap_b32 v56,v11
v_max_f32 v56,v56,v11
v_mov_b32 v11,v56
s_nop 1
v_permlane16_swap_b32 v56,v11
v_max_f32 v56,v56,v11
v_sub_f32 v40,0,v56
v_sub_f32 v41,0,v56
v_sub_f32 v42,0,v56
v_sub_f32 v43,0,v56
v_mov_b32 v11,v60
s_nop 1
v_permlane32_swap_b32 v60,v11
v_max_f32 v60,v60,v11
v_mov_b32 v11,v60
s_nop 1
v_permlane16_swap_b32 v60,v11
v_max_f32 v60,v60,v11
v_max_f32 v60,s72,v60
v_sub_f32 v44,0,v60
v_sub_f32 v45,0,v60
v_sub_f32 v46,0,v60
v_sub_f32 v47,0,v60
v_mov_b32 v48,0
v_mov_b32 v49,0
v_mov_b32 v50,0
v_mov_b32 v51,0
v_mov_b32 v52,0
v_mov_b32 v53,0
v_mov_b32 v54,0
v_mov_b32 v55,0
v_mov_b32 v56,0
v_mov_b32 v57,0
v_mov_b32 v58,0
v_mov_b32 v59,0
v_mov_b32 v60,0
v_mov_b32 v61,0
v_mov_b32 v62,0
v_mov_b32 v63,0
v_accvgpr_write_b32 a0,0
v_accvgpr_write_b32 a1,0
v_accvgpr_write_b32 a2,0
v_accvgpr_write_b32 a3,0
v_accvgpr_write_b32 a4,0
v_accvgpr_write_b32 a5,0
v_accvgpr_write_b32 a6,0
v_accvgpr_write_b32 a7,0
v_accvgpr_write_b32 a8,0
v_accvgpr_write_b32 a9,0
v_accvgpr_write_b32 a10,0
v_accvgpr_write_b32 a11,0
v_accvgpr_write_b32 a12,0
v_accvgpr_write_b32 a13,0
v_accvgpr_write_b32 a14,0
v_accvgpr_write_b32 a15,0
v_accvgpr_write_b32 a16,0
v_accvgpr_write_b32 a17,0
v_accvgpr_write_b32 a18,0
v_accvgpr_write_b32 a19,0
v_accvgpr_write_b32 a20,0
v_accvgpr_write_b32 a21,0
v_accvgpr_write_b32 a22,0
v_accvgpr_write_b32 a23,0
v_accvgpr_write_b32 a24,0
v_accvgpr_write_b32 a25,0
v_accvgpr_write_b32 a26,0
v_accvgpr_write_b32 a27,0
v_accvgpr_write_b32 a28,0
v_accvgpr_write_b32 a29,0
v_accvgpr_write_b32 a30,0
v_accvgpr_write_b32 a31,0
v_accvgpr_write_b32 a32,0
v_accvgpr_write_b32 a33,0
v_accvgpr_write_b32 a34,0
v_accvgpr_write_b32 a35,0
v_accvgpr_write_b32 a36,0
v_accvgpr_write_b32 a37,0
v_accvgpr_write_b32 a38,0
v_accvgpr_write_b32 a39,0
v_accvgpr_write_b32 a40,0
v_accvgpr_write_b32 a41,0
v_accvgpr_write_b32 a42,0
v_accvgpr_write_b32 a43,0
v_accvgpr_write_b32 a44,0
v_accvgpr_write_b32 a45,0
v_accvgpr_write_b32 a46,0
v_accvgpr_write_b32 a47,0
v_accvgpr_write_b32 a48,0
v_accvgpr_write_b32 a49,0
v_accvgpr_write_b32 a50,0
v_accvgpr_write_b32 a51,0
v_accvgpr_write_b32 a52,0
v_accvgpr_write_b32 a53,0
v_accvgpr_write_b32 a54,0
v_accvgpr_write_b32 a55,0
v_accvgpr_write_b32 a56,0
v_accvgpr_write_b32 a57,0
v_accvgpr_write_b32 a58,0
v_accvgpr_write_b32 a59,0
v_accvgpr_write_b32 a60,0
v_accvgpr_write_b32 a61,0
v_accvgpr_write_b32 a62,0
v_accvgpr_write_b32 a63,0
v_accvgpr_write_b32 a64,0
v_accvgpr_write_b32 a65,0
v_accvgpr_write_b32 a66,0
v_accvgpr_write_b32 a67,0
v_accvgpr_write_b32 a68,0
v_accvgpr_write_b32 a69,0
v_accvgpr_write_b32 a70,0
v_accvgpr_write_b32 a71,0
v_accvgpr_write_b32 a72,0
v_accvgpr_write_b32 a73,0
v_accvgpr_write_b32 a74,0
v_accvgpr_write_b32 a75,0
v_accvgpr_write_b32 a76,0
v_accvgpr_write_b32 a77,0
v_accvgpr_write_b32 a78,0
v_accvgpr_write_b32 a79,0
v_accvgpr_write_b32 a80,0
v_accvgpr_write_b32 a81,0
v_accvgpr_write_b32 a82,0
v_accvgpr_write_b32 a83,0
v_accvgpr_write_b32 a84,0
v_accvgpr_write_b32 a85,0
v_accvgpr_write_b32 a86,0
v_accvgpr_write_b32 a87,0
v_accvgpr_write_b32 a88,0
v_accvgpr_write_b32 a89,0
v_accvgpr_write_b32 a90,0
v_accvgpr_write_b32 a91,0
v_accvgpr_write_b32 a92,0
v_accvgpr_write_b32 a93,0
v_accvgpr_write_b32 a94,0
v_accvgpr_write_b32 a95,0
v_accvgpr_write_b32 a96,0
v_accvgpr_write_b32 a97,0
v_accvgpr_write_b32 a98,0
v_accvgpr_write_b32 a99,0
v_accvgpr_write_b32 a100,0
v_accvgpr_write_b32 a101,0
v_accvgpr_write_b32 a102,0
v_accvgpr_write_b32 a103,0
v_accvgpr_write_b32 a104,0
v_accvgpr_write_b32 a105,0
v_accvgpr_write_b32 a106,0
v_accvgpr_write_b32 a107,0
v_accvgpr_write_b32 a108,0
v_accvgpr_write_b32 a109,0
v_accvgpr_write_b32 a110,0
v_accvgpr_write_b32 a111,0
v_accvgpr_write_b32 a112,0
v_accvgpr_write_b32 a113,0
v_accvgpr_write_b32 a114,0
v_accvgpr_write_b32 a115,0
v_accvgpr_write_b32 a116,0
v_accvgpr_write_b32 a117,0
v_accvgpr_write_b32 a118,0
v_accvgpr_write_b32 a119,0
v_accvgpr_write_b32 a120,0
v_accvgpr_write_b32 a121,0
v_accvgpr_write_b32 a122,0
v_accvgpr_write_b32 a123,0
)A"
R"A(v_accvgpr_write_b32 a124,0
v_accvgpr_write_b32 a125,0
v_accvgpr_write_b32 a126,0
v_accvgpr_write_b32 a127,0
s_mov_b32 s65,0
s_mov_b32 s66,32
L_s20_%=:
s_add_u32 s67,s65,4096
buffer_load_dwordx4 a[128:131],v8,s[20:23],s65 offen
buffer_load_dwordx4 a[132:135],v8,s[20:23],s65 offen offset:64
buffer_load_dwordx4 a[136:139],v8,s[20:23],s65 offen offset:128
buffer_load_dwordx4 a[140:143],v8,s[20:23],s65 offen offset:192
buffer_load_dwordx4 a[144:147],v8,s[20:23],s67 offen
buffer_load_dwordx4 a[148:151],v8,s[20:23],s67 offen offset:64
buffer_load_dwordx4 a[152:155],v8,s[20:23],s67 offen offset:128
buffer_load_dwordx4 a[156:159],v8,s[20:23],s67 offen offset:192
s_waitcnt vmcnt(0)
v_mfma_f32_16x16x32_bf16 v[64:67],a[128:131],v[132:135],v[32:35]
v_mfma_f32_16x16x32_bf16 v[64:67],a[132:135],v[140:143],v[64:67]
v_mfma_f32_16x16x32_bf16 v[64:67],a[136:139],v[148:151],v[64:67]
v_mfma_f32_16x16x32_bf16 v[64:67],a[140:143],v[156:159],v[64:67]
v_mfma_f32_16x16x32_bf16 v[68:71],a[144:147],v[132:135],v[32:35]
v_mfma_f32_16x16x32_bf16 v[68:71],a[148:151],v[140:143],v[68:71]
v_mfma_f32_16x16x32_bf16 v[68:71],a[152:155],v[148:151],v[68:71]
v_mfma_f32_16x16x32_bf16 v[68:71],a[156:159],v[156:159],v[68:71]
v_mfma_f32_16x16x32_bf16 v[80:83],a[128:131],v[164:167],v[36:39]
v_mfma_f32_16x16x32_bf16 v[80:83],a[132:135],v[172:175],v[80:83]
v_mfma_f32_16x16x32_bf16 v[80:83],a[136:139],v[180:183],v[80:83]
v_mfma_f32_16x16x32_bf16 v[80:83],a[140:143],v[188:191],v[80:83]
v_mfma_f32_16x16x32_bf16 v[84:87],a[144:147],v[164:167],v[36:39]
v_mfma_f32_16x16x32_bf16 v[84:87],a[148:151],v[172:175],v[84:87]
v_mfma_f32_16x16x32_bf16 v[84:87],a[152:155],v[180:183],v[84:87]
v_mfma_f32_16x16x32_bf16 v[84:87],a[156:159],v[188:191],v[84:87]
v_mfma_f32_16x16x32_bf16 v[96:99],a[128:131],v[196:199],v[40:43]
v_mfma_f32_16x16x32_bf16 v[96:99],a[132:135],v[204:207],v[96:99]
v_mfma_f32_16x16x32_bf16 v[96:99],a[136:139],v[212:215],v[96:99]
v_mfma_f32_16x16x32_bf16 v[96:99],a[140:143],v[220:223],v[96:99]
v_mfma_f32_16x16x32_bf16 v[100:103],a[144:147],v[196:199],v[40:43]
v_mfma_f32_16x16x32_bf16 v[100:103],a[148:151],v[204:207],v[100:103]
v_mfma_f32_16x16x32_bf16 v[100:103],a[152:155],v[212:215],v[100:103]
v_mfma_f32_16x16x32_bf16 v[100:103],a[156:159],v[220:223],v[100:103]
v_mfma_f32_16x16x32_bf16 v[112:115],a[128:131],v[228:231],v[44:47]
v_mfma_f32_16x16x32_bf16 v[112:115],a[132:135],v[236:239],v[112:115]
v_mfma_f32_16x16x32_bf16 v[112:115],a[136:139],v[244:247],v[112:115]
v_mfma_f32_16x16x32_bf16 v[112:115],a[140:143],v[252:255],v[112:115]
v_mfma_f32_16x16x32_bf16 v[116:119],a[144:147],v[228:231],v[44:47]
v_mfma_f32_16x16x32_bf16 v[116:119],a[148:151],v[236:239],v[116:119]
v_mfma_f32_16x16x32_bf16 v[116:119],a[152:155],v[244:247],v[116:119]
v_mfma_f32_16x16x32_bf16 v[116:119],a[156:159],v[252:255],v[116:119]
s_nop 7
v_exp_f32 v64,v64
v_exp_f32 v65,v65
v_exp_f32 v66,v66
v_exp_f32 v67,v67
v_exp_f32 v68,v68
v_exp_f32 v69,v69
v_exp_f32 v70,v70
v_exp_f32 v71,v71
s_nop 0
v_cvt_pk_bf16_f32 v64,v64,v65
v_cvt_pk_bf16_f32 v65,v66,v67
v_cvt_pk_bf16_f32 v66,v68,v69
v_cvt_pk_bf16_f32 v67,v70,v71
v_exp_f32 v80,v80
v_exp_f32 v81,v81
v_exp_f32 v82,v82
v_exp_f32 v83,v83
v_exp_f32 v84,v84
v_exp_f32 v85,v85
v_exp_f32 v86,v86
v_exp_f32 v87,v87
s_nop 0
v_cvt_pk_bf16_f32 v80,v80,v81
v_cvt_pk_bf16_f32 v81,v82,v83
v_cvt_pk_bf16_f32 v82,v84,v85
v_cvt_pk_bf16_f32 v83,v86,v87
v_exp_f32 v96,v96
v_exp_f32 v97,v97
v_exp_f32 v98,v98
v_exp_f32 v99,v99
v_exp_f32 v100,v100
v_exp_f32 v101,v101
v_exp_f32 v102,v102
v_exp_f32 v103,v103
s_nop 0
v_cvt_pk_bf16_f32 v96,v96,v97
v_cvt_pk_bf16_f32 v97,v98,v99
v_cvt_pk_bf16_f32 v98,v100,v101
v_cvt_pk_bf16_f32 v99,v102,v103
v_exp_f32 v112,v112
v_exp_f32 v113,v113
v_exp_f32 v114,v114
v_exp_f32 v115,v115
v_exp_f32 v116,v116
v_exp_f32 v117,v117
v_exp_f32 v118,v118
v_exp_f32 v119,v119
s_nop 0
v_cvt_pk_bf16_f32 v112,v112,v113
v_cvt_pk_bf16_f32 v113,v114,v115
v_cvt_pk_bf16_f32 v114,v116,v117
v_cvt_pk_bf16_f32 v115,v118,v119
buffer_load_ushort v72,v9,s[24:27],s65 offen
buffer_load_ushort v73,v9,s[24:27],s65 offen offset:256
buffer_load_ushort v74,v9,s[24:27],s65 offen offset:512
buffer_load_ushort v75,v9,s[24:27],s65 offen offset:768
buffer_load_ushort v76,v10,s[24:27],s65 offen
buffer_load_ushort v77,v10,s[24:27],s65 offen offset:256
buffer_load_ushort v78,v10,s[24:27],s65 offen offset:512
buffer_load_ushort v79,v10,s[24:27],s65 offen offset:768
s_waitcnt vmcnt(0)
v_lshl_or_b32 v88,v73,16,v72
v_lshl_or_b32 v89,v75,16,v74
v_lshl_or_b32 v90,v77,16,v76
v_lshl_or_b32 v91,v79,16,v78
s_nop 1
v_mfma_f32_16x16x32_bf16 a[0:3],v[88:91],v[64:67],a[0:3]
v_mfma_f32_16x16x32_bf16 a[32:35],v[88:91],v[80:83],a[32:35]
v_mfma_f32_16x16x32_bf16 a[64:67],v[88:91],v[96:99],a[64:67]
v_mfma_f32_16x16x32_bf16 a[96:99],v[88:91],v[112:115],a[96:99]
buffer_load_ushort v72,v9,s[24:27],s65 offen offset:32
buffer_load_ushort v73,v9,s[24:27],s65 offen offset:288
buffer_load_ushort v74,v9,s[24:27],s65 offen offset:544
buffer_load_ushort v75,v9,s[24:27],s65 offen offset:800
buffer_load_ushort v76,v10,s[24:27],s65 offen offset:32
buffer_load_ushort v77,v10,s[24:27],s65 offen offset:288
buffer_load_ushort v78,v10,s[24:27],s65 offen offset:544
buffer_load_ushort v79,v10,s[24:27],s65 offen offset:800
s_waitcnt vmcnt(0)
v_lshl_or_b32 v88,v73,16,v72
v_lshl_or_b32 v89,v75,16,v74
v_lshl_or_b32 v90,v77,16,v76
v_lshl_or_b32 v91,v79,16,v78
s_nop 1
v_mfma_f32_16x16x32_bf16 a[4:7],v[88:91],v[64:67],a[4:7]
v_mfma_f32_16x16x32_bf16 a[36:39],v[88:91],v[80:83],a[36:39]
v_mfma_f32_16x16x32_bf16 a[68:71],v[88:91],v[96:99],a[68:71]
v_mfma_f32_16x16x32_bf16 a[100:103],v[88:91],v[112:115],a[100:103]
buffer_load_ushort v72,v9,s[24:27],s65 offen offset:64
buffer_load_ushort v73,v9,s[24:27],s65 offen offset:320
buffer_load_ushort v74,v9,s[24:27],s65 offen offset:576
buffer_load_ushort v75,v9,s[24:27],s65 offen offset:832
buffer_load_ushort v76,v10,s[24:27],s65 offen offset:64
buffer_load_ushort v77,v10,s[24:27],s65 offen offset:320
buffer_load_ushort v78,v10,s[24:27],s65 offen offset:576
buffer_load_ushort v79,v10,s[24:27],s65 offen offset:832
s_waitcnt vmcnt(0)
v_lshl_or_b32 v88,v73,16,v72
v_lshl_or_b32 v89,v75,16,v74
v_lshl_or_b32 v90,v77,16,v76
v_lshl_or_b32 v91,v79,16,v78
s_nop 1
v_mfma_f32_16x16x32_bf16 a[8:11],v[88:91],v[64:67],a[8:11]
v_mfma_f32_16x16x32_bf16 a[40:43],v[88:91],v[80:83],a[40:43]
v_mfma_f32_16x16x32_bf16 a[72:75],v[88:91],v[96:99],a[72:75]
v_mfma_f32_16x16x32_bf16 a[104:107],v[88:91],v[112:115],a[104:107]
buffer_load_ushort v72,v9,s[24:27],s65 offen offset:96
buffer_load_ushort v73,v9,s[24:27],s65 offen offset:352
buffer_load_ushort v74,v9,s[24:27],s65 offen offset:608
buffer_load_ushort v75,v9,s[24:27],s65 offen offset:864
buffer_load_ushort v76,v10,s[24:27],s65 offen offset:96
buffer_load_ushort v77,v10,s[24:27],s65 offen offset:352
buffer_load_ushort v78,v10,s[24:27],s65 offen offset:608
buffer_load_ushort v79,v10,s[24:27],s65 offen offset:864
s_waitcnt vmcnt(0)
v_lshl_or_b32 v88,v73,16,v72
v_lshl_or_b32 v89,v75,16,v74
v_lshl_or_b32 v90,v77,16,v76
v_lshl_or_b32 v91,v79,16,v78
s_nop 1
v_mfma_f32_16x16x32_bf16 a[12:15],v[88:91],v[64:67],a[12:15]
v_mfma_f32_16x16x32_bf16 a[44:47],v[88:91],v[80:83],a[44:47]
v_mfma_f32_16x16x32_bf16 a[76:79],v[88:91],v[96:99],a[76:79]
v_mfma_f32_16x16x32_bf16 a[108:111],v[88:91],v[112:115],a[108:111]
buffer_load_ushort v72,v9,s[24:27],s65 offen offset:128
buffer_load_ushort v73,v9,s[24:27],s65 offen offset:384
buffer_load_ushort v74,v9,s[24:27],s65 offen offset:640
buffer_load_ushort v75,v9,s[24:27],s65 offen offset:896
buffer_load_ushort v76,v10,s[24:27],s65 offen offset:128
buffer_load_ushort v77,v10,s[24:27],s65 offen offset:384
buffer_load_ushort v78,v10,s[24:27],s65 offen offset:640
buffer_load_ushort v79,v10,s[24:27],s65 offen offset:896
s_waitcnt vmcnt(0)
v_lshl_or_b32 v88,v73,16,v72
v_lshl_or_b32 v89,v75,16,v74
v_lshl_or_b32 v90,v77,16,v76
v_lshl_or_b32 v91,v79,16,v78
s_nop 1
v_mfma_f32_16x16x32_bf16 a[16:19],v[88:91],v[64:67],a[16:19]
v_mfma_f32_16x16x32_bf16 a[48:51],v[88:91],v[80:83],a[48:51]
v_mfma_f32_16x16x32_bf16 a[80:83],v[88:91],v[96:99],a[80:83]
v_mfma_f32_16x16x32_bf16 a[112:115],v[88:91],v[112:115],a[112:115]
buffer_load_ushort v72,v9,s[24:27],s65 offen offset:160
buffer_load_ushort v73,v9,s[24:27],s65 offen offset:416
buffer_load_ushort v74,v9,s[24:27],s65 offen offset:672
buffer_load_ushort v75,v9,s[24:27],s65 offen offset:928
buffer_load_ushort v76,v10,s[24:27],s65 offen offset:160
buffer_load_ushort v77,v10,s[24:27],s65 offen offset:416
buffer_load_ushort v78,v10,s[24:27],s65 offen offset:672
buffer_load_ushort v79,v10,s[24:27],s65 offen offset:928
s_waitcnt vmcnt(0)
v_lshl_or_b32 v88,v73,16,v72
v_lshl_or_b32 v89,v75,16,v74
v_lshl_or_b32 v90,v77,16,v76
v_lshl_or_b32 v91,v79,16,v78
s_nop 1
v_mfma_f32_16x16x32_bf16 a[20:23],v[88:91],v[64:67],a[20:23]
v_mfma_f32_16x16x32_bf16 a[52:55],v[88:91],v[80:83],a[52:55]
v_mfma_f32_16x16x32_bf16 a[84:87],v[88:91],v[96:99],a[84:87]
v_mfma_f32_16x16x32_bf16 a[116:119],v[88:91],v[112:115],a[116:119]
buffer_load_ushort v72,v9,s[24:27],s65 offen offset:192
buffer_load_ushort v73,v9,s[24:27],s65 offen offset:448
buffer_load_ushort v74,v9,s[24:27],s65 offen offset:704
buffer_load_ushort v75,v9,s[24:27],s65 offen offset:960
buffer_load_ushort v76,v10,s[24:27],s65 offen offset:192
buffer_load_ushort v77,v10,s[24:27],s65 offen offset:448
buffer_load_ushort v78,v10,s[24:27],s65 offen offset:704
buffer_load_ushort v79,v10,s[24:27],s65 offen offset:960
s_waitcnt vmcnt(0)
v_lshl_or_b32 v88,v73,16,v72
v_lshl_or_b32 v89,v75,16,v74
v_lshl_or_b32 v90,v77,16,v76
v_lshl_or_b32 v91,v79,16,v78
s_nop 1
v_mfma_f32_16x16x32_bf16 a[24:27],v[88:91],v[64:67],a[24:27]
v_mfma_f32_16x16x32_bf16 a[56:59],v[88:91],v[80:83],a[56:59]
v_mfma_f32_16x16x32_bf16 a[88:91],v[88:91],v[96:99],a[88:91]
v_mfma_f32_16x16x32_bf16 a[120:123],v[88:91],v[112:115],a[120:123]
buffer_load_ushort v72,v9,s[24:27],s65 offen offset:224
buffer_load_ushort v73,v9,s[24:27],s65 offen offset:480
buffer_load_ushort v74,v9,s[24:27],s65 offen offset:736
buffer_load_ushort v75,v9,s[24:27],s65 offen offset:992
buffer_load_ushort v76,v10,s[24:27],s65 offen offset:224
buffer_load_ushort v77,v10,s[24:27],s65 offen offset:480
buffer_load_ushort v78,v10,s[24:27],s65 offen offset:736
buffer_load_ushort v79,v10,s[24:27],s65 offen offset:992
s_waitcnt vmcnt(0)
v_lshl_or_b32 v88,v73,16,v72
v_lshl_or_b32 v89,v75,16,v74
v_lshl_or_b32 v90,v77,16,v76
v_lshl_or_b32 v91,v79,16,v78
s_nop 1
v_mfma_f32_16x16x32_bf16 a[28:31],v[88:91],v[64:67],a[28:31]
v_mfma_f32_16x16x32_bf16 a[60:63],v[88:91],v[80:83],a[60:63]
v_mfma_f32_16x16x32_bf16 a[92:95],v[88:91],v[96:99],a[92:95]
v_mfma_f32_16x16x32_bf16 a[124:127],v[88:91],v[112:115],a[124:127]
v_mfma_f32_16x16x32_bf16 v[48:51],v[28:31],v[64:67],v[48:51]
v_mfma_f32_16x16x32_bf16 v[52:55],v[28:31],v[80:83],v[52:55]
v_mfma_f32_16x16x32_bf16 v[56:59],v[28:31],v[96:99],v[56:59]
v_mfma_f32_16x16x32_bf16 v[60:63],v[28:31],v[112:115],v[60:63]
s_add_u32 s65,s65,8192
s_sub_u32 s66,s66,1
s_cmp_lg_u32 s66,0
s_cbranch_scc1 L_s20_%=
s_nop 7
v_max_f32 v24,0x00800000,v48
v_rcp_f32 v24,v24
v_max_f32 v25,0x00800000,v52
v_rcp_f32 v25,v25
v_max_f32 v26,0x00800000,v56
v_rcp_f32 v26,v26
v_max_f32 v27,0x00800000,v60
v_rcp_f32 v27,v27
v_accvgpr_read_b32 v8,a0
v_accvgpr_read_b32 v9,a1
v_accvgpr_read_b32 v10,a2
v_accvgpr_read_b32 v11,a3
v_accvgpr_read_b32 v12,a4
v_accvgpr_read_b32 v13,a5
v_accvgpr_read_b32 v14,a6
v_accvgpr_read_b32 v15,a7
v_mul_f32 v8,v8,v24
v_mul_f32 v9,v9,v24
v_mul_f32 v10,v10,v24
v_mul_f32 v11,v11,v24
v_mul_f32 v12,v12,v24
v_mul_f32 v13,v13,v24
v_mul_f32 v14,v14,v24
v_mul_f32 v15,v15,v24
v_cvt_pk_bf16_f32 v8,v8,v9
v_cvt_pk_bf16_f32 v9,v10,v11
v_cvt_pk_bf16_f32 v10,v12,v13
v_cvt_pk_bf16_f32 v11,v14,v15
ds_write_b128 v7,v[8:11] offset:6144
v_accvgpr_read_b32 v16,a8
v_accvgpr_read_b32 v17,a9
v_accvgpr_read_b32 v18,a10
v_accvgpr_read_b32 v19,a11
v_accvgpr_read_b32 v20,a12
v_accvgpr_read_b32 v21,a13
v_accvgpr_read_b32 v22,a14
v_accvgpr_read_b32 v23,a15
v_mul_f32 v16,v16,v24
v_mul_f32 v17,v17,v24
v_mul_f32 v18,v18,v24
v_mul_f32 v19,v19,v24
)A"
R"A(v_mul_f32 v20,v20,v24
v_mul_f32 v21,v21,v24
v_mul_f32 v22,v22,v24
v_mul_f32 v23,v23,v24
v_cvt_pk_bf16_f32 v16,v16,v17
v_cvt_pk_bf16_f32 v17,v18,v19
v_cvt_pk_bf16_f32 v18,v20,v21
v_cvt_pk_bf16_f32 v19,v22,v23
ds_write_b128 v7,v[16:19] offset:7168
v_accvgpr_read_b32 v8,a16
v_accvgpr_read_b32 v9,a17
v_accvgpr_read_b32 v10,a18
v_accvgpr_read_b32 v11,a19
v_accvgpr_read_b32 v12,a20
v_accvgpr_read_b32 v13,a21
v_accvgpr_read_b32 v14,a22
v_accvgpr_read_b32 v15,a23
v_mul_f32 v8,v8,v24
v_mul_f32 v9,v9,v24
v_mul_f32 v10,v10,v24
v_mul_f32 v11,v11,v24
v_mul_f32 v12,v12,v24
v_mul_f32 v13,v13,v24
v_mul_f32 v14,v14,v24
v_mul_f32 v15,v15,v24
v_cvt_pk_bf16_f32 v8,v8,v9
v_cvt_pk_bf16_f32 v9,v10,v11
v_cvt_pk_bf16_f32 v10,v12,v13
v_cvt_pk_bf16_f32 v11,v14,v15
ds_write_b128 v7,v[8:11]
v_accvgpr_read_b32 v16,a24
v_accvgpr_read_b32 v17,a25
v_accvgpr_read_b32 v18,a26
v_accvgpr_read_b32 v19,a27
v_accvgpr_read_b32 v20,a28
v_accvgpr_read_b32 v21,a29
v_accvgpr_read_b32 v22,a30
v_accvgpr_read_b32 v23,a31
v_mul_f32 v16,v16,v24
v_mul_f32 v17,v17,v24
v_mul_f32 v18,v18,v24
v_mul_f32 v19,v19,v24
v_mul_f32 v20,v20,v24
v_mul_f32 v21,v21,v24
v_mul_f32 v22,v22,v24
v_mul_f32 v23,v23,v24
v_cvt_pk_bf16_f32 v16,v16,v17
v_cvt_pk_bf16_f32 v17,v18,v19
v_cvt_pk_bf16_f32 v18,v20,v21
v_cvt_pk_bf16_f32 v19,v22,v23
ds_write_b128 v7,v[16:19] offset:1024
v_accvgpr_read_b32 v32,a160
v_accvgpr_read_b32 v33,a161
v_accvgpr_read_b32 v34,a162
v_accvgpr_read_b32 v35,a163
v_accvgpr_read_b32 v36,a164
v_accvgpr_read_b32 v37,a165
v_accvgpr_read_b32 v38,a166
v_accvgpr_read_b32 v39,a167
v_accvgpr_read_b32 v40,a168
v_accvgpr_read_b32 v41,a169
v_accvgpr_read_b32 v42,a170
v_accvgpr_read_b32 v43,a171
v_accvgpr_read_b32 v44,a172
v_accvgpr_read_b32 v45,a173
v_accvgpr_read_b32 v46,a174
v_accvgpr_read_b32 v47,a175
s_waitcnt lgkmcnt(0)
v_subrev_u32 v8,s78,v1
ds_read_b128 a[128:131],v8
ds_read_b128 a[132:135],v8 offset:256
ds_read_b128 a[136:139],v8 offset:512
ds_read_b128 a[140:143],v8 offset:768
ds_read_b128 a[144:147],v8 offset:4096
ds_read_b128 a[148:151],v8 offset:4352
ds_read_b128 a[152:155],v8 offset:4608
ds_read_b128 a[156:159],v8 offset:4864
ds_read_b128 a[160:163],v8 offset:8192
ds_read_b128 a[164:167],v8 offset:8448
ds_read_b128 a[168:171],v8 offset:8704
ds_read_b128 a[172:175],v8 offset:8960
ds_read_b128 a[176:179],v8 offset:12288
ds_read_b128 a[180:183],v8 offset:12544
ds_read_b128 a[184:187],v8 offset:12800
ds_read_b128 a[188:191],v8 offset:13056
s_waitcnt lgkmcnt(15)
v_mfma_f32_16x16x32_bf16 v[64:67],a[128:131],v[128:131],v[32:35]
s_waitcnt lgkmcnt(14)
v_mfma_f32_16x16x32_bf16 v[64:67],a[132:135],v[136:139],v[64:67]
s_waitcnt lgkmcnt(13)
v_mfma_f32_16x16x32_bf16 v[64:67],a[136:139],v[144:147],v[64:67]
s_waitcnt lgkmcnt(12)
v_mfma_f32_16x16x32_bf16 v[64:67],a[140:143],v[152:155],v[64:67]
s_waitcnt lgkmcnt(8)
v_mfma_f32_16x16x32_bf16 v[68:71],a[156:159],v[152:155],v[32:35]
v_mfma_f32_16x16x32_bf16 v[68:71],a[152:155],v[144:147],v[68:71]
v_mfma_f32_16x16x32_bf16 v[68:71],a[148:151],v[136:139],v[68:71]
v_mfma_f32_16x16x32_bf16 v[68:71],a[144:147],v[128:131],v[68:71]
s_waitcnt lgkmcnt(0)
v_mfma_f32_16x16x32_bf16 v[72:75],a[160:163],v[128:131],v[32:35]
v_mfma_f32_16x16x32_bf16 v[72:75],a[164:167],v[136:139],v[72:75]
v_mfma_f32_16x16x32_bf16 v[72:75],a[168:171],v[144:147],v[72:75]
v_mfma_f32_16x16x32_bf16 v[72:75],a[172:175],v[152:155],v[72:75]
v_mfma_f32_16x16x32_bf16 v[76:79],a[188:191],v[152:155],v[32:35]
v_mfma_f32_16x16x32_bf16 v[76:79],a[184:187],v[144:147],v[76:79]
v_mfma_f32_16x16x32_bf16 v[76:79],a[180:183],v[136:139],v[76:79]
v_mfma_f32_16x16x32_bf16 v[76:79],a[176:179],v[128:131],v[76:79]
v_mfma_f32_16x16x32_bf16 v[80:83],a[128:131],v[160:163],v[36:39]
v_mfma_f32_16x16x32_bf16 v[80:83],a[132:135],v[168:171],v[80:83]
v_mfma_f32_16x16x32_bf16 v[80:83],a[136:139],v[176:179],v[80:83]
v_mfma_f32_16x16x32_bf16 v[80:83],a[140:143],v[184:187],v[80:83]
v_mfma_f32_16x16x32_bf16 v[84:87],a[156:159],v[184:187],v[36:39]
v_mfma_f32_16x16x32_bf16 v[84:87],a[152:155],v[176:179],v[84:87]
v_mfma_f32_16x16x32_bf16 v[84:87],a[148:151],v[168:171],v[84:87]
v_mfma_f32_16x16x32_bf16 v[84:87],a[144:147],v[160:163],v[84:87]
v_mfma_f32_16x16x32_bf16 v[88:91],a[160:163],v[160:163],v[36:39]
v_mfma_f32_16x16x32_bf16 v[88:91],a[164:167],v[168:171],v[88:91]
v_mfma_f32_16x16x32_bf16 v[88:91],a[168:171],v[176:179],v[88:91]
v_mfma_f32_16x16x32_bf16 v[88:91],a[172:175],v[184:187],v[88:91]
v_mfma_f32_16x16x32_bf16 v[92:95],a[188:191],v[184:187],v[36:39]
v_mfma_f32_16x16x32_bf16 v[92:95],a[184:187],v[176:179],v[92:95]
v_mfma_f32_16x16x32_bf16 v[92:95],a[180:183],v[168:171],v[92:95]
v_mfma_f32_16x16x32_bf16 v[92:95],a[176:179],v[160:163],v[92:95]
v_mfma_f32_16x16x32_bf16 v[96:99],a[128:131],v[192:195],v[40:43]
v_mfma_f32_16x16x32_bf16 v[96:99],a[132:135],v[200:203],v[96:99]
v_mfma_f32_16x16x32_bf16 v[96:99],a[136:139],v[208:211],v[96:99]
v_mfma_f32_16x16x32_bf16 v[96:99],a[140:143],v[216:219],v[96:99]
v_mfma_f32_16x16x32_bf16 v[100:103],a[156:159],v[216:219],v[40:43]
v_mfma_f32_16x16x32_bf16 v[100:103],a[152:155],v[208:211],v[100:103]
v_mfma_f32_16x16x32_bf16 v[100:103],a[148:151],v[200:203],v[100:103]
v_mfma_f32_16x16x32_bf16 v[100:103],a[144:147],v[192:195],v[100:103]
v_mfma_f32_16x16x32_bf16 v[104:107],a[160:163],v[192:195],v[40:43]
v_mfma_f32_16x16x32_bf16 v[104:107],a[164:167],v[200:203],v[104:107]
v_mfma_f32_16x16x32_bf16 v[104:107],a[168:171],v[208:211],v[104:107]
v_mfma_f32_16x16x32_bf16 v[104:107],a[172:175],v[216:219],v[104:107]
v_mfma_f32_16x16x32_bf16 v[108:111],a[188:191],v[216:219],v[40:43]
v_mfma_f32_16x16x32_bf16 v[108:111],a[184:187],v[208:211],v[108:111]
v_mfma_f32_16x16x32_bf16 v[108:111],a[180:183],v[200:203],v[108:111]
v_mfma_f32_16x16x32_bf16 v[108:111],a[176:179],v[192:195],v[108:111]
v_exp_f32 v64,v64
v_exp_f32 v65,v65
s_nop 0
v_cvt_pk_bf16_f32 v64,v64,v65
v_exp_f32 v66,v66
v_exp_f32 v67,v67
s_nop 0
v_cvt_pk_bf16_f32 v65,v66,v67
v_exp_f32 v68,v68
v_exp_f32 v69,v69
s_nop 0
v_cvt_pk_bf16_f32 v66,v68,v69
v_exp_f32 v70,v70
v_exp_f32 v71,v71
s_nop 0
v_cvt_pk_bf16_f32 v67,v70,v71
v_exp_f32 v72,v72
v_exp_f32 v73,v73
s_nop 0
v_cvt_pk_bf16_f32 v72,v72,v73
v_exp_f32 v74,v74
v_exp_f32 v75,v75
s_nop 0
v_cvt_pk_bf16_f32 v73,v74,v75
v_exp_f32 v76,v76
v_exp_f32 v77,v77
s_nop 0
v_cvt_pk_bf16_f32 v74,v76,v77
v_exp_f32 v78,v78
v_exp_f32 v79,v79
s_nop 0
v_cvt_pk_bf16_f32 v75,v78,v79
v_exp_f32 v80,v80
v_exp_f32 v81,v81
s_nop 0
v_cvt_pk_bf16_f32 v80,v80,v81
v_exp_f32 v82,v82
v_exp_f32 v83,v83
s_nop 0
v_cvt_pk_bf16_f32 v81,v82,v83
v_exp_f32 v84,v84
v_exp_f32 v85,v85
s_nop 0
v_cvt_pk_bf16_f32 v82,v84,v85
v_exp_f32 v86,v86
v_exp_f32 v87,v87
s_nop 0
v_cvt_pk_bf16_f32 v83,v86,v87
v_exp_f32 v88,v88
v_exp_f32 v89,v89
s_nop 0
v_cvt_pk_bf16_f32 v88,v88,v89
v_mfma_f32_16x16x32_bf16 a[0:3],a[192:195],v[64:67],0
v_mfma_f32_16x16x32_bf16 a[0:3],a[196:199],v[72:75],a[0:3]
v_mfma_f32_16x16x32_bf16 a[4:7],a[200:203],v[64:67],0
v_mfma_f32_16x16x32_bf16 a[4:7],a[204:207],v[72:75],a[4:7]
v_mfma_f32_16x16x32_bf16 a[8:11],a[208:211],v[64:67],0
v_mfma_f32_16x16x32_bf16 a[8:11],a[212:215],v[72:75],a[8:11]
v_mfma_f32_16x16x32_bf16 a[12:15],a[216:219],v[64:67],0
v_mfma_f32_16x16x32_bf16 a[12:15],a[220:223],v[72:75],a[12:15]
v_mfma_f32_16x16x32_bf16 a[16:19],a[224:227],v[64:67],0
v_mfma_f32_16x16x32_bf16 a[16:19],a[228:231],v[72:75],a[16:19]
v_mfma_f32_16x16x32_bf16 a[20:23],a[232:235],v[64:67],0
v_mfma_f32_16x16x32_bf16 a[20:23],a[236:239],v[72:75],a[20:23]
v_mfma_f32_16x16x32_bf16 a[24:27],a[240:243],v[64:67],0
v_mfma_f32_16x16x32_bf16 a[24:27],a[244:247],v[72:75],a[24:27]
v_mfma_f32_16x16x32_bf16 a[28:31],a[248:251],v[64:67],0
v_mfma_f32_16x16x32_bf16 a[28:31],a[252:255],v[72:75],a[28:31]
v_mfma_f32_16x16x32_bf16 v[48:51],v[28:31],v[64:67],0
v_mfma_f32_16x16x32_bf16 v[48:51],v[28:31],v[72:75],v[48:51]
s_waitcnt vmcnt(0) lgkmcnt(0)
s_nop 7
s_branch L_ok0_%=
L_slow1_%=:
s_waitcnt vmcnt(0) lgkmcnt(0)
s_nop 7
v_bfe_u32 v11,v6,4,4
v_lshrrev_b32 v12,8,v6
v_lshlrev_b32 v8,8,v11
v_lshl_add_u32 v8,v12,4,v8
v_lshlrev_b32 v9,10,v12
v_lshl_add_u32 v9,v11,1,v9
v_add_u32 v10,4096,v9
v_mov_b32 v48,0xff800000
v_mov_b32 v52,0xff800000
v_mov_b32 v56,0xff800000
v_mov_b32 v60,0xff800000
s_mov_b32 s65,262144
s_mov_b32 s66,64
L_s11_%=:
buffer_load_dwordx4 a[128:131],v8,s[20:23],s65 offen
buffer_load_dwordx4 a[132:135],v8,s[20:23],s65 offen offset:64
buffer_load_dwordx4 a[136:139],v8,s[20:23],s65 offen offset:128
buffer_load_dwordx4 a[140:143],v8,s[20:23],s65 offen offset:192
s_waitcnt vmcnt(0)
v_mfma_f32_16x16x32_bf16 v[64:67],a[128:131],v[128:131],0
v_mfma_f32_16x16x32_bf16 v[64:67],a[132:135],v[136:139],v[64:67]
v_mfma_f32_16x16x32_bf16 v[64:67],a[136:139],v[144:147],v[64:67]
v_mfma_f32_16x16x32_bf16 v[64:67],a[140:143],v[152:155],v[64:67]
v_mfma_f32_16x16x32_bf16 v[80:83],a[128:131],v[160:163],0
v_mfma_f32_16x16x32_bf16 v[80:83],a[132:135],v[168:171],v[80:83]
v_mfma_f32_16x16x32_bf16 v[80:83],a[136:139],v[176:179],v[80:83]
v_mfma_f32_16x16x32_bf16 v[80:83],a[140:143],v[184:187],v[80:83]
v_mfma_f32_16x16x32_bf16 v[96:99],a[128:131],v[192:195],0
v_mfma_f32_16x16x32_bf16 v[96:99],a[132:135],v[200:203],v[96:99]
v_mfma_f32_16x16x32_bf16 v[96:99],a[136:139],v[208:211],v[96:99]
v_mfma_f32_16x16x32_bf16 v[96:99],a[140:143],v[216:219],v[96:99]
v_mfma_f32_16x16x32_bf16 v[112:115],a[128:131],v[224:227],0
v_mfma_f32_16x16x32_bf16 v[112:115],a[132:135],v[232:235],v[112:115]
v_mfma_f32_16x16x32_bf16 v[112:115],a[136:139],v[240:243],v[112:115]
v_mfma_f32_16x16x32_bf16 v[112:115],a[140:143],v[248:251],v[112:115]
s_nop 7
v_max3_f32 v48,v48,v64,v65
v_max3_f32 v48,v48,v66,v67
v_max3_f32 v52,v52,v80,v81
v_max3_f32 v52,v52,v82,v83
v_max3_f32 v56,v56,v96,v97
v_max3_f32 v56,v56,v98,v99
v_max3_f32 v60,v60,v112,v113
v_max3_f32 v60,v60,v114,v115
s_add_u32 s65,s65,4096
s_sub_u32 s66,s66,1
s_cmp_lg_u32 s66,0
s_cbranch_scc1 L_s11_%=
v_mov_b32 v11,v48
s_nop 1
v_permlane32_swap_b32 v48,v11
v_max_f32 v48,v48,v11
v_mov_b32 v11,v48
s_nop 1
v_permlane16_swap_b32 v48,v11
v_max_f32 v48,v48,v11
v_sub_f32 v32,0,v48
v_sub_f32 v33,0,v48
v_sub_f32 v34,0,v48
v_sub_f32 v35,0,v48
v_mov_b32 v11,v52
s_nop 1
v_permlane32_swap_b32 v52,v11
v_max_f32 v52,v52,v11
v_mov_b32 v11,v52
s_nop 1
v_permlane16_swap_b32 v52,v11
v_max_f32 v52,v52,v11
v_sub_f32 v36,0,v52
v_sub_f32 v37,0,v52
v_sub_f32 v38,0,v52
v_sub_f32 v39,0,v52
v_mov_b32 v11,v56
s_nop 1
v_permlane32_swap_b32 v56,v11
v_max_f32 v56,v56,v11
v_mov_b32 v11,v56
s_nop 1
v_permlane16_swap_b32 v56,v11
v_max_f32 v56,v56,v11
v_sub_f32 v40,0,v56
v_sub_f32 v41,0,v56
v_sub_f32 v42,0,v56
v_sub_f32 v43,0,v56
v_mov_b32 v11,v60
s_nop 1
v_permlane32_swap_b32 v60,v11
v_max_f32 v60,v60,v11
v_mov_b32 v11,v60
s_nop 1
v_permlane16_swap_b32 v60,v11
v_max_f32 v60,v60,v11
v_max_f32 v60,s72,v60
v_sub_f32 v44,0,v60
v_sub_f32 v45,0,v60
v_sub_f32 v46,0,v60
v_sub_f32 v47,0,v60
v_mov_b32 v48,0
v_mov_b32 v49,0
v_mov_b32 v50,0
v_mov_b32 v51,0
v_mov_b32 v52,0
v_mov_b32 v53,0
v_mov_b32 v54,0
v_mov_b32 v55,0
v_mov_b32 v56,0
v_mov_b32 v57,0
v_mov_b32 v58,0
v_mov_b32 v59,0
v_mov_b32 v60,0
v_mov_b32 v61,0
v_mov_b32 v62,0
v_mov_b32 v63,0
v_accvgpr_write_b32 a0,0
v_accvgpr_write_b32 a1,0
v_accvgpr_write_b32 a2,0
v_accvgpr_write_b32 a3,0
v_accvgpr_write_b32 a4,0
v_accvgpr_write_b32 a5,0
v_accvgpr_write_b32 a6,0
v_accvgpr_write_b32 a7,0
v_accvgpr_write_b32 a8,0
v_accvgpr_write_b32 a9,0
v_accvgpr_write_b32 a10,0
v_accvgpr_write_b32 a11,0
v_accvgpr_write_b32 a12,0
v_accvgpr_write_b32 a13,0
v_accvgpr_write_b32 a14,0
v_accvgpr_write_b32 a15,0
v_accvgpr_write_b32 a16,0
v_accvgpr_write_b32 a17,0
v_accvgpr_write_b32 a18,0
v_accvgpr_write_b32 a19,0
v_accvgpr_write_b32 a20,0
v_accvgpr_write_b32 a21,0
v_accvgpr_write_b32 a22,0
v_accvgpr_write_b32 a23,0
v_accvgpr_write_b32 a24,0
v_accvgpr_write_b32 a25,0
v_accvgpr_write_b32 a26,0
v_accvgpr_write_b32 a27,0
v_accvgpr_write_b32 a28,0
v_accvgpr_write_b32 a29,0
v_accvgpr_write_b32 a30,0
v_accvgpr_write_b32 a31,0
v_mov_b32 v132,0
v_mov_b32 v133,0
v_mov_b32 v134,0
v_mov_b32 v135,0
v_mov_b32 v140,0
v_mov_b32 v141,0
v_mov_b32 v142,0
v_mov_b32 v143,0
v_mov_b32 v148,0
v_mov_b32 v149,0
v_mov_b32 v150,0
v_mov_b32 v151,0
)A"
R"A(v_mov_b32 v156,0
v_mov_b32 v157,0
v_mov_b32 v158,0
v_mov_b32 v159,0
v_mov_b32 v164,0
v_mov_b32 v165,0
v_mov_b32 v166,0
v_mov_b32 v167,0
v_mov_b32 v172,0
v_mov_b32 v173,0
v_mov_b32 v174,0
v_mov_b32 v175,0
v_mov_b32 v180,0
v_mov_b32 v181,0
v_mov_b32 v182,0
v_mov_b32 v183,0
v_mov_b32 v188,0
v_mov_b32 v189,0
v_mov_b32 v190,0
v_mov_b32 v191,0
v_mov_b32 v196,0
v_mov_b32 v197,0
v_mov_b32 v198,0
v_mov_b32 v199,0
v_mov_b32 v204,0
v_mov_b32 v205,0
v_mov_b32 v206,0
v_mov_b32 v207,0
v_mov_b32 v212,0
v_mov_b32 v213,0
v_mov_b32 v214,0
v_mov_b32 v215,0
v_mov_b32 v220,0
v_mov_b32 v221,0
v_mov_b32 v222,0
v_mov_b32 v223,0
v_mov_b32 v228,0
v_mov_b32 v229,0
v_mov_b32 v230,0
v_mov_b32 v231,0
v_mov_b32 v236,0
v_mov_b32 v237,0
v_mov_b32 v238,0
v_mov_b32 v239,0
v_mov_b32 v244,0
v_mov_b32 v245,0
v_mov_b32 v246,0
v_mov_b32 v247,0
v_mov_b32 v252,0
v_mov_b32 v253,0
v_mov_b32 v254,0
v_mov_b32 v255,0
v_accvgpr_write_b32 a32,0
v_accvgpr_write_b32 a33,0
v_accvgpr_write_b32 a34,0
v_accvgpr_write_b32 a35,0
v_accvgpr_write_b32 a36,0
v_accvgpr_write_b32 a37,0
v_accvgpr_write_b32 a38,0
v_accvgpr_write_b32 a39,0
v_accvgpr_write_b32 a40,0
v_accvgpr_write_b32 a41,0
v_accvgpr_write_b32 a42,0
v_accvgpr_write_b32 a43,0
v_accvgpr_write_b32 a44,0
v_accvgpr_write_b32 a45,0
v_accvgpr_write_b32 a46,0
v_accvgpr_write_b32 a47,0
v_accvgpr_write_b32 a48,0
v_accvgpr_write_b32 a49,0
v_accvgpr_write_b32 a50,0
v_accvgpr_write_b32 a51,0
v_accvgpr_write_b32 a52,0
v_accvgpr_write_b32 a53,0
v_accvgpr_write_b32 a54,0
v_accvgpr_write_b32 a55,0
v_accvgpr_write_b32 a56,0
v_accvgpr_write_b32 a57,0
v_accvgpr_write_b32 a58,0
v_accvgpr_write_b32 a59,0
v_accvgpr_write_b32 a60,0
v_accvgpr_write_b32 a61,0
v_accvgpr_write_b32 a62,0
v_accvgpr_write_b32 a63,0
s_mov_b32 s65,262144
s_mov_b32 s66,32
L_s21_%=:
s_add_u32 s67,s65,4096
buffer_load_dwordx4 a[128:131],v8,s[20:23],s65 offen
buffer_load_dwordx4 a[132:135],v8,s[20:23],s65 offen offset:64
buffer_load_dwordx4 a[136:139],v8,s[20:23],s65 offen offset:128
buffer_load_dwordx4 a[140:143],v8,s[20:23],s65 offen offset:192
buffer_load_dwordx4 a[144:147],v8,s[20:23],s67 offen
buffer_load_dwordx4 a[148:151],v8,s[20:23],s67 offen offset:64
buffer_load_dwordx4 a[152:155],v8,s[20:23],s67 offen offset:128
buffer_load_dwordx4 a[156:159],v8,s[20:23],s67 offen offset:192
s_waitcnt vmcnt(0)
v_mfma_f32_16x16x32_bf16 v[64:67],a[128:131],v[128:131],v[32:35]
v_mfma_f32_16x16x32_bf16 v[64:67],a[132:135],v[136:139],v[64:67]
v_mfma_f32_16x16x32_bf16 v[64:67],a[136:139],v[144:147],v[64:67]
v_mfma_f32_16x16x32_bf16 v[64:67],a[140:143],v[152:155],v[64:67]
v_mfma_f32_16x16x32_bf16 v[68:71],a[144:147],v[128:131],v[32:35]
v_mfma_f32_16x16x32_bf16 v[68:71],a[148:151],v[136:139],v[68:71]
v_mfma_f32_16x16x32_bf16 v[68:71],a[152:155],v[144:147],v[68:71]
v_mfma_f32_16x16x32_bf16 v[68:71],a[156:159],v[152:155],v[68:71]
v_mfma_f32_16x16x32_bf16 v[80:83],a[128:131],v[160:163],v[36:39]
v_mfma_f32_16x16x32_bf16 v[80:83],a[132:135],v[168:171],v[80:83]
v_mfma_f32_16x16x32_bf16 v[80:83],a[136:139],v[176:179],v[80:83]
v_mfma_f32_16x16x32_bf16 v[80:83],a[140:143],v[184:187],v[80:83]
v_mfma_f32_16x16x32_bf16 v[84:87],a[144:147],v[160:163],v[36:39]
v_mfma_f32_16x16x32_bf16 v[84:87],a[148:151],v[168:171],v[84:87]
v_mfma_f32_16x16x32_bf16 v[84:87],a[152:155],v[176:179],v[84:87]
v_mfma_f32_16x16x32_bf16 v[84:87],a[156:159],v[184:187],v[84:87]
v_mfma_f32_16x16x32_bf16 v[96:99],a[128:131],v[192:195],v[40:43]
v_mfma_f32_16x16x32_bf16 v[96:99],a[132:135],v[200:203],v[96:99]
v_mfma_f32_16x16x32_bf16 v[96:99],a[136:139],v[208:211],v[96:99]
v_mfma_f32_16x16x32_bf16 v[96:99],a[140:143],v[216:219],v[96:99]
v_mfma_f32_16x16x32_bf16 v[100:103],a[144:147],v[192:195],v[40:43]
v_mfma_f32_16x16x32_bf16 v[100:103],a[148:151],v[200:203],v[100:103]
v_mfma_f32_16x16x32_bf16 v[100:103],a[152:155],v[208:211],v[100:103]
v_mfma_f32_16x16x32_bf16 v[100:103],a[156:159],v[216:219],v[100:103]
v_mfma_f32_16x16x32_bf16 v[112:115],a[128:131],v[224:227],v[44:47]
v_mfma_f32_16x16x32_bf16 v[112:115],a[132:135],v[232:235],v[112:115]
v_mfma_f32_16x16x32_bf16 v[112:115],a[136:139],v[240:243],v[112:115]
v_mfma_f32_16x16x32_bf16 v[112:115],a[140:143],v[248:251],v[112:115]
v_mfma_f32_16x16x32_bf16 v[116:119],a[144:147],v[224:227],v[44:47]
v_mfma_f32_16x16x32_bf16 v[116:119],a[148:151],v[232:235],v[116:119]
v_mfma_f32_16x16x32_bf16 v[116:119],a[152:155],v[240:243],v[116:119]
v_mfma_f32_16x16x32_bf16 v[116:119],a[156:159],v[248:251],v[116:119]
s_nop 7
v_exp_f32 v64,v64
v_exp_f32 v65,v65
v_exp_f32 v66,v66
v_exp_f32 v67,v67
v_exp_f32 v68,v68
v_exp_f32 v69,v69
v_exp_f32 v70,v70
v_exp_f32 v71,v71
s_nop 0
v_cvt_pk_bf16_f32 v64,v64,v65
v_cvt_pk_bf16_f32 v65,v66,v67
v_cvt_pk_bf16_f32 v66,v68,v69
v_cvt_pk_bf16_f32 v67,v70,v71
v_exp_f32 v80,v80
v_exp_f32 v81,v81
v_exp_f32 v82,v82
v_exp_f32 v83,v83
v_exp_f32 v84,v84
v_exp_f32 v85,v85
v_exp_f32 v86,v86
v_exp_f32 v87,v87
s_nop 0
v_cvt_pk_bf16_f32 v80,v80,v81
v_cvt_pk_bf16_f32 v81,v82,v83
v_cvt_pk_bf16_f32 v82,v84,v85
v_cvt_pk_bf16_f32 v83,v86,v87
v_exp_f32 v96,v96
v_exp_f32 v97,v97
v_exp_f32 v98,v98
v_exp_f32 v99,v99
v_exp_f32 v100,v100
v_exp_f32 v101,v101
v_exp_f32 v102,v102
v_exp_f32 v103,v103
s_nop 0
v_cvt_pk_bf16_f32 v96,v96,v97
v_cvt_pk_bf16_f32 v97,v98,v99
v_cvt_pk_bf16_f32 v98,v100,v101
v_cvt_pk_bf16_f32 v99,v102,v103
v_exp_f32 v112,v112
v_exp_f32 v113,v113
v_exp_f32 v114,v114
v_exp_f32 v115,v115
v_exp_f32 v116,v116
v_exp_f32 v117,v117
v_exp_f32 v118,v118
v_exp_f32 v119,v119
s_nop 0
v_cvt_pk_bf16_f32 v112,v112,v113
v_cvt_pk_bf16_f32 v113,v114,v115
v_cvt_pk_bf16_f32 v114,v116,v117
v_cvt_pk_bf16_f32 v115,v118,v119
buffer_load_ushort v72,v9,s[24:27],s65 offen
buffer_load_ushort v73,v9,s[24:27],s65 offen offset:256
buffer_load_ushort v74,v9,s[24:27],s65 offen offset:512
buffer_load_ushort v75,v9,s[24:27],s65 offen offset:768
buffer_load_ushort v76,v10,s[24:27],s65 offen
buffer_load_ushort v77,v10,s[24:27],s65 offen offset:256
buffer_load_ushort v78,v10,s[24:27],s65 offen offset:512
buffer_load_ushort v79,v10,s[24:27],s65 offen offset:768
s_waitcnt vmcnt(0)
v_lshl_or_b32 v88,v73,16,v72
v_lshl_or_b32 v89,v75,16,v74
v_lshl_or_b32 v90,v77,16,v76
v_lshl_or_b32 v91,v79,16,v78
s_nop 1
v_mfma_f32_16x16x32_bf16 a[0:3],v[88:91],v[64:67],a[0:3]
v_mfma_f32_16x16x32_bf16 v[132:135],v[88:91],v[80:83],v[132:135]
v_mfma_f32_16x16x32_bf16 v[196:199],v[88:91],v[96:99],v[196:199]
v_mfma_f32_16x16x32_bf16 a[32:35],v[88:91],v[112:115],a[32:35]
buffer_load_ushort v72,v9,s[24:27],s65 offen offset:32
buffer_load_ushort v73,v9,s[24:27],s65 offen offset:288
buffer_load_ushort v74,v9,s[24:27],s65 offen offset:544
buffer_load_ushort v75,v9,s[24:27],s65 offen offset:800
buffer_load_ushort v76,v10,s[24:27],s65 offen offset:32
buffer_load_ushort v77,v10,s[24:27],s65 offen offset:288
buffer_load_ushort v78,v10,s[24:27],s65 offen offset:544
buffer_load_ushort v79,v10,s[24:27],s65 offen offset:800
s_waitcnt vmcnt(0)
v_lshl_or_b32 v88,v73,16,v72
v_lshl_or_b32 v89,v75,16,v74
v_lshl_or_b32 v90,v77,16,v76
v_lshl_or_b32 v91,v79,16,v78
s_nop 1
v_mfma_f32_16x16x32_bf16 a[4:7],v[88:91],v[64:67],a[4:7]
v_mfma_f32_16x16x32_bf16 v[140:143],v[88:91],v[80:83],v[140:143]
v_mfma_f32_16x16x32_bf16 v[204:207],v[88:91],v[96:99],v[204:207]
v_mfma_f32_16x16x32_bf16 a[36:39],v[88:91],v[112:115],a[36:39]
buffer_load_ushort v72,v9,s[24:27],s65 offen offset:64
buffer_load_ushort v73,v9,s[24:27],s65 offen offset:320
buffer_load_ushort v74,v9,s[24:27],s65 offen offset:576
buffer_load_ushort v75,v9,s[24:27],s65 offen offset:832
buffer_load_ushort v76,v10,s[24:27],s65 offen offset:64
buffer_load_ushort v77,v10,s[24:27],s65 offen offset:320
buffer_load_ushort v78,v10,s[24:27],s65 offen offset:576
buffer_load_ushort v79,v10,s[24:27],s65 offen offset:832
s_waitcnt vmcnt(0)
v_lshl_or_b32 v88,v73,16,v72
v_lshl_or_b32 v89,v75,16,v74
v_lshl_or_b32 v90,v77,16,v76
v_lshl_or_b32 v91,v79,16,v78
s_nop 1
v_mfma_f32_16x16x32_bf16 a[8:11],v[88:91],v[64:67],a[8:11]
v_mfma_f32_16x16x32_bf16 v[148:151],v[88:91],v[80:83],v[148:151]
v_mfma_f32_16x16x32_bf16 v[212:215],v[88:91],v[96:99],v[212:215]
v_mfma_f32_16x16x32_bf16 a[40:43],v[88:91],v[112:115],a[40:43]
buffer_load_ushort v72,v9,s[24:27],s65 offen offset:96
buffer_load_ushort v73,v9,s[24:27],s65 offen offset:352
buffer_load_ushort v74,v9,s[24:27],s65 offen offset:608
buffer_load_ushort v75,v9,s[24:27],s65 offen offset:864
buffer_load_ushort v76,v10,s[24:27],s65 offen offset:96
buffer_load_ushort v77,v10,s[24:27],s65 offen offset:352
buffer_load_ushort v78,v10,s[24:27],s65 offen offset:608
buffer_load_ushort v79,v10,s[24:27],s65 offen offset:864
s_waitcnt vmcnt(0)
v_lshl_or_b32 v88,v73,16,v72
v_lshl_or_b32 v89,v75,16,v74
v_lshl_or_b32 v90,v77,16,v76
v_lshl_or_b32 v91,v79,16,v78
s_nop 1
v_mfma_f32_16x16x32_bf16 a[12:15],v[88:91],v[64:67],a[12:15]
v_mfma_f32_16x16x32_bf16 v[156:159],v[88:91],v[80:83],v[156:159]
v_mfma_f32_16x16x32_bf16 v[220:223],v[88:91],v[96:99],v[220:223]
v_mfma_f32_16x16x32_bf16 a[44:47],v[88:91],v[112:115],a[44:47]
buffer_load_ushort v72,v9,s[24:27],s65 offen offset:128
buffer_load_ushort v73,v9,s[24:27],s65 offen offset:384
buffer_load_ushort v74,v9,s[24:27],s65 offen offset:640
buffer_load_ushort v75,v9,s[24:27],s65 offen offset:896
buffer_load_ushort v76,v10,s[24:27],s65 offen offset:128
buffer_load_ushort v77,v10,s[24:27],s65 offen offset:384
buffer_load_ushort v78,v10,s[24:27],s65 offen offset:640
buffer_load_ushort v79,v10,s[24:27],s65 offen offset:896
s_waitcnt vmcnt(0)
v_lshl_or_b32 v88,v73,16,v72
v_lshl_or_b32 v89,v75,16,v74
v_lshl_or_b32 v90,v77,16,v76
v_lshl_or_b32 v91,v79,16,v78
s_nop 1
v_mfma_f32_16x16x32_bf16 a[16:19],v[88:91],v[64:67],a[16:19]
v_mfma_f32_16x16x32_bf16 v[164:167],v[88:91],v[80:83],v[164:167]
v_mfma_f32_16x16x32_bf16 v[228:231],v[88:91],v[96:99],v[228:231]
v_mfma_f32_16x16x32_bf16 a[48:51],v[88:91],v[112:115],a[48:51]
buffer_load_ushort v72,v9,s[24:27],s65 offen offset:160
buffer_load_ushort v73,v9,s[24:27],s65 offen offset:416
buffer_load_ushort v74,v9,s[24:27],s65 offen offset:672
buffer_load_ushort v75,v9,s[24:27],s65 offen offset:928
buffer_load_ushort v76,v10,s[24:27],s65 offen offset:160
buffer_load_ushort v77,v10,s[24:27],s65 offen offset:416
buffer_load_ushort v78,v10,s[24:27],s65 offen offset:672
buffer_load_ushort v79,v10,s[24:27],s65 offen offset:928
s_waitcnt vmcnt(0)
v_lshl_or_b32 v88,v73,16,v72
v_lshl_or_b32 v89,v75,16,v74
v_lshl_or_b32 v90,v77,16,v76
v_lshl_or_b32 v91,v79,16,v78
s_nop 1
v_mfma_f32_16x16x32_bf16 a[20:23],v[88:91],v[64:67],a[20:23]
v_mfma_f32_16x16x32_bf16 v[172:175],v[88:91],v[80:83],v[172:175]
v_mfma_f32_16x16x32_bf16 v[236:239],v[88:91],v[96:99],v[236:239]
v_mfma_f32_16x16x32_bf16 a[52:55],v[88:91],v[112:115],a[52:55]
buffer_load_ushort v72,v9,s[24:27],s65 offen offset:192
buffer_load_ushort v73,v9,s[24:27],s65 offen offset:448
buffer_load_ushort v74,v9,s[24:27],s65 offen offset:704
buffer_load_ushort v75,v9,s[24:27],s65 offen offset:960
buffer_load_ushort v76,v10,s[24:27],s65 offen offset:192
buffer_load_ushort v77,v10,s[24:27],s65 offen offset:448
buffer_load_ushort v78,v10,s[24:27],s65 offen offset:704
buffer_load_ushort v79,v10,s[24:27],s65 offen offset:960
s_waitcnt vmcnt(0)
v_lshl_or_b32 v88,v73,16,v72
v_lshl_or_b32 v89,v75,16,v74
v_lshl_or_b32 v90,v77,16,v76
v_lshl_or_b32 v91,v79,16,v78
s_nop 1
v_mfma_f32_16x16x32_bf16 a[24:27],v[88:91],v[64:67],a[24:27]
v_mfma_f32_16x16x32_bf16 v[180:183],v[88:91],v[80:83],v[180:183]
v_mfma_f32_16x16x32_bf16 v[244:247],v[88:91],v[96:99],v[244:247]
v_mfma_f32_16x16x32_bf16 a[56:59],v[88:91],v[112:115],a[56:59]
buffer_load_ushort v72,v9,s[24:27],s65 offen offset:224
buffer_load_ushort v73,v9,s[24:27],s65 offen offset:480
buffer_load_ushort v74,v9,s[24:27],s65 offen offset:736
buffer_load_ushort v75,v9,s[24:27],s65 offen offset:992
buffer_load_ushort v76,v10,s[24:27],s65 offen offset:224
buffer_load_ushort v77,v10,s[24:27],s65 offen offset:480
buffer_load_ushort v78,v10,s[24:27],s65 offen offset:736
buffer_load_ushort v79,v10,s[24:27],s65 offen offset:992
s_waitcnt vmcnt(0)
v_lshl_or_b32 v88,v73,16,v72
v_lshl_or_b32 v89,v75,16,v74
v_lshl_or_b32 v90,v77,16,v76
)A"
R"A(v_lshl_or_b32 v91,v79,16,v78
s_nop 1
v_mfma_f32_16x16x32_bf16 a[28:31],v[88:91],v[64:67],a[28:31]
v_mfma_f32_16x16x32_bf16 v[188:191],v[88:91],v[80:83],v[188:191]
v_mfma_f32_16x16x32_bf16 v[252:255],v[88:91],v[96:99],v[252:255]
v_mfma_f32_16x16x32_bf16 a[60:63],v[88:91],v[112:115],a[60:63]
v_mfma_f32_16x16x32_bf16 v[48:51],v[28:31],v[64:67],v[48:51]
v_mfma_f32_16x16x32_bf16 v[52:55],v[28:31],v[80:83],v[52:55]
v_mfma_f32_16x16x32_bf16 v[56:59],v[28:31],v[96:99],v[56:59]
v_mfma_f32_16x16x32_bf16 v[60:63],v[28:31],v[112:115],v[60:63]
s_add_u32 s65,s65,8192
s_sub_u32 s66,s66,1
s_cmp_lg_u32 s66,0
s_cbranch_scc1 L_s21_%=
s_nop 7
v_max_f32 v49,0x00800000,v48
v_rcp_f32 v49,v49
v_max_f32 v53,0x00800000,v52
v_rcp_f32 v53,v53
v_max_f32 v57,0x00800000,v56
v_rcp_f32 v57,v57
v_max_f32 v61,0x00800000,v60
v_rcp_f32 v61,v61
v_accvgpr_read_b32 v16,a0
v_accvgpr_read_b32 v17,a1
v_accvgpr_read_b32 v18,a2
v_accvgpr_read_b32 v19,a3
v_accvgpr_read_b32 v20,a4
v_accvgpr_read_b32 v21,a5
v_accvgpr_read_b32 v22,a6
v_accvgpr_read_b32 v23,a7
v_mul_f32 v16,v16,v49
v_mul_f32 v17,v17,v49
v_mul_f32 v18,v18,v49
v_mul_f32 v19,v19,v49
v_mul_f32 v20,v20,v49
v_mul_f32 v21,v21,v49
v_mul_f32 v22,v22,v49
v_mul_f32 v23,v23,v49
v_cvt_pk_bf16_f32 v32,v16,v17
v_cvt_pk_bf16_f32 v33,v18,v19
v_cvt_pk_bf16_f32 v34,v20,v21
v_cvt_pk_bf16_f32 v35,v22,v23
v_accvgpr_read_b32 v8,a8
v_accvgpr_read_b32 v9,a9
v_accvgpr_read_b32 v10,a10
v_accvgpr_read_b32 v11,a11
v_accvgpr_read_b32 v12,a12
v_accvgpr_read_b32 v13,a13
v_accvgpr_read_b32 v14,a14
v_accvgpr_read_b32 v15,a15
v_mul_f32 v8,v8,v49
v_mul_f32 v9,v9,v49
v_mul_f32 v10,v10,v49
v_mul_f32 v11,v11,v49
v_mul_f32 v12,v12,v49
v_mul_f32 v13,v13,v49
v_mul_f32 v14,v14,v49
v_mul_f32 v15,v15,v49
v_cvt_pk_bf16_f32 v36,v8,v9
v_cvt_pk_bf16_f32 v37,v10,v11
v_cvt_pk_bf16_f32 v38,v12,v13
v_cvt_pk_bf16_f32 v39,v14,v15
v_accvgpr_read_b32 v16,a16
v_accvgpr_read_b32 v17,a17
v_accvgpr_read_b32 v18,a18
v_accvgpr_read_b32 v19,a19
v_accvgpr_read_b32 v20,a20
v_accvgpr_read_b32 v21,a21
v_accvgpr_read_b32 v22,a22
v_accvgpr_read_b32 v23,a23
v_mul_f32 v16,v16,v49
v_mul_f32 v17,v17,v49
v_mul_f32 v18,v18,v49
v_mul_f32 v19,v19,v49
v_mul_f32 v20,v20,v49
v_mul_f32 v21,v21,v49
v_mul_f32 v22,v22,v49
v_mul_f32 v23,v23,v49
v_cvt_pk_bf16_f32 v40,v16,v17
v_cvt_pk_bf16_f32 v41,v18,v19
v_cvt_pk_bf16_f32 v42,v20,v21
v_cvt_pk_bf16_f32 v43,v22,v23
ds_read_b64_tr_b16 v[64:65],v1
ds_read_b64_tr_b16 v[66:67],v1 offset:5120
ds_read_b64_tr_b16 v[68:69],v1 offset:10240
ds_read_b64_tr_b16 v[70:71],v1 offset:15360
ds_read_b64_tr_b16 v[72:73],v1 offset:20480
ds_read_b64_tr_b16 v[74:75],v1 offset:25600
ds_read_b64_tr_b16 v[76:77],v1 offset:30720
ds_read_b64_tr_b16 v[78:79],v1 offset:35840
ds_read_b64_tr_b16 v[80:81],v2 offset:32
ds_read_b64_tr_b16 v[82:83],v2 offset:5152
ds_read_b64_tr_b16 v[84:85],v2 offset:10272
ds_read_b64_tr_b16 v[86:87],v2 offset:15392
ds_read_b64_tr_b16 v[88:89],v2 offset:20512
ds_read_b64_tr_b16 v[90:91],v2 offset:25632
ds_read_b64_tr_b16 v[92:93],v2 offset:30752
ds_read_b64_tr_b16 v[94:95],v2 offset:35872
s_waitcnt vmcnt(0) lgkmcnt(0)
s_nop 7
s_branch L_ok1_%=
L_end_%=:
)A"
    :: "s"(ka), "s"(blk), "s"(wid)
    : "v0", "v1", "v2", "v3", "v4", "v5", "v6", "v7", "v8", "v9", "v10", "v11", "v12", "v13", "v14", "v15", "v16", "v17", "v18", "v19", "v20", "v21", "v22", "v23", "v24", "v25", "v26", "v27", "v28", "v29", "v30", "v31", "v32", "v33", "v34", "v35", "v36", "v37", "v38", "v39", "v40", "v41", "v42", "v43", "v44", "v45", "v46", "v47", "v48", "v49", "v50", "v51", "v52", "v53", "v54", "v55", "v56", "v57", "v58", "v59", "v60", "v61", "v62", "v63", "v64", "v65", "v66", "v67", "v68", "v69", "v70", "v71", "v72", "v73", "v74", "v75", "v76", "v77", "v78", "v79", "v80", "v81", "v82", "v83", "v84", "v85", "v86", "v87", "v88", "v89", "v90", "v91", "v92", "v93", "v94", "v95", "v96", "v97", "v98", "v99", "v100", "v101", "v102", "v103", "v104", "v105", "v106", "v107", "v108", "v109", "v110", "v111", "v112", "v113", "v114", "v115", "v116", "v117", "v118", "v119", "v120", "v121", "v122", "v123", "v124", "v125", "v126", "v127", "v128", "v129", "v130", "v131", "v132", "v133", "v134", "v135", "v136", "v137", "v138", "v139", "v140", "v141", "v142", "v143", "v144", "v145", "v146", "v147", "v148", "v149", "v150", "v151", "v152", "v153", "v154", "v155", "v156", "v157", "v158", "v159", "v160", "v161", "v162", "v163", "v164", "v165", "v166", "v167", "v168", "v169", "v170", "v171", "v172", "v173", "v174", "v175", "v176", "v177", "v178", "v179", "v180", "v181", "v182", "v183", "v184", "v185", "v186", "v187", "v188", "v189", "v190", "v191", "v192", "v193", "v194", "v195", "v196", "v197", "v198", "v199", "v200", "v201", "v202", "v203", "v204", "v205", "v206", "v207", "v208", "v209", "v210", "v211", "v212", "v213", "v214", "v215", "v216", "v217", "v218", "v219", "v220", "v221", "v222", "v223", "v224", "v225", "v226", "v227", "v228", "v229", "v230", "v231", "v232", "v233", "v234", "v235", "v236", "v237", "v238", "v239", "v240", "v241", "v242", "v243", "v244", "v245", "v246", "v247", "v248", "v249", "v250", "v251", "v252", "v253", "v254", "v255", "a0", "a1", "a2", "a3", "a4", "a5", "a6", "a7", "a8", "a9", "a10", "a11", "a12", "a13", "a14", "a15", "a16", "a17", "a18", "a19", "a20", "a21", "a22", "a23", "a24", "a25", "a26", "a27", "a28", "a29", "a30", "a31", "a32", "a33", "a34", "a35", "a36", "a37", "a38", "a39", "a40", "a41", "a42", "a43", "a44", "a45", "a46", "a47", "a48", "a49", "a50", "a51", "a52", "a53", "a54", "a55", "a56", "a57", "a58", "a59", "a60", "a61", "a62", "a63", "a64", "a65", "a66", "a67", "a68", "a69", "a70", "a71", "a72", "a73", "a74", "a75", "a76", "a77", "a78", "a79", "a80", "a81", "a82", "a83", "a84", "a85", "a86", "a87", "a88", "a89", "a90", "a91", "a92", "a93", "a94", "a95", "a96", "a97", "a98", "a99", "a100", "a101", "a102", "a103", "a104", "a105", "a106", "a107", "a108", "a109", "a110", "a111", "a112", "a113", "a114", "a115", "a116", "a117", "a118", "a119", "a120", "a121", "a122", "a123", "a124", "a125", "a126", "a127", "a128", "a129", "a130", "a131", "a132", "a133", "a134", "a135", "a136", "a137", "a138", "a139", "a140", "a141", "a142", "a143", "a144", "a145", "a146", "a147", "a148", "a149", "a150", "a151", "a152", "a153", "a154", "a155", "a156", "a157", "a158", "a159", "a160", "a161", "a162", "a163", "a164", "a165", "a166", "a167", "a168", "a169", "a170", "a171", "a172", "a173", "a174", "a175", "a176", "a177", "a178", "a179", "a180", "a181", "a182", "a183", "a184", "a185", "a186", "a187", "a188", "a189", "a190", "a191", "a192", "a193", "a194", "a195", "a196", "a197", "a198", "a199", "a200", "a201", "a202", "a203", "a204", "a205", "a206", "a207", "a208", "a209", "a210", "a211", "a212", "a213", "a214", "a215", "a216", "a217", "a218", "a219", "a220", "a221", "a222", "a223", "a224", "a225", "a226", "a227", "a228", "a229", "a230", "a231", "a232", "a233", "a234", "a235", "a236", "a237", "a238", "a239", "a240", "a241", "a242", "a243", "a244", "a245", "a246", "a247", "a248", "a249", "a250", "a251", "a252", "a253", "a254", "a255", "s4", "s5", "s6", "s7", "s8", "s9", "s10", "s11", "s12", "s13", "s14", "s15", "s16", "s17", "s18", "s19", "s20", "s21", "s22", "s23", "s24", "s25", "s26", "s27", "s28", "s29", "s30", "s31", "s36", "s37", "s38", "s39", "s40", "s41", "s42", "s43", "s44", "s45", "s46", "s47", "s48", "s49", "s50", "s51", "s52", "s53", "s54", "s55", "s56", "s57", "s58", "s59", "s60", "s61", "s62", "s63", "s64", "s65", "s66", "s67", "s68", "s69", "s70", "s71", "s72", "s73", "s74", "s75", "s76", "s77", "s78", "s79", "s80", "s81", "s82", "s83", "s84", "s85", "s86", "s87", "s88", "s89", "s90", "s91", "s92", "s93", "s94", "s95", "s96", "s97", "s98", "s99", "vcc", "scc", "memory");
}

extern "C" void kernel_launch(void* const* d_in, const int* in_sizes, int n_in,
                              void* d_out, int out_size, void* d_ws, size_t ws_size,
                              hipStream_t stream) {
  (void)hipFuncSetAttribute((const void*)qattn_kernel, hipFuncAttributeMaxDynamicSharedMemorySize, LDS_ATT);
  qattn_kernel<<<256, 256, LDS_ATT, stream>>>(d_in[0], d_in[1], d_in[2], d_in[3], d_ws, d_in[4], d_out);
}
```

```cpp
#include <hip/hip_runtime.h>
#include <hip/hip_bf16.h>

constexpr int LDS_ATT = 163840;

__global__ __launch_bounds__(256, 1) __attribute__((aligned(4096))) void qattn_kernel(const void* __restrict__ x, const void* __restrict__ kc, const void* __restrict__ vc,
                                                       const void* __restrict__ wq, void* __restrict__ ws, const void* __restrict__ wo,
                                                       void* __restrict__ out) {
  const unsigned blk = blockIdx.x;
  const unsigned wid = __builtin_amdgcn_readfirstlane(threadIdx.x >> 6);
  auto ka = __builtin_amdgcn_kernarg_segment_ptr();
  asm volatile(
R"A(s_load_dwordx8 s[4:11],%0,0x0
s_load_dwordx4 s[12:15],%0,0x20
s_load_dwordx2 s[16:17],%0,0x30
v_mbcnt_lo_u32_b32 v23,-1,0
v_mbcnt_hi_u32_b32 v23,-1,v23
s_mov_b32 s60,%2
s_and_b32 s66,%1,7
s_lshr_b32 s67,%1,3
s_getpc_b64 s[20:21]
L_pc_%=:
s_and_b32 s20,s20,0xffffff80
s_and_b32 s21,s21,0xffff
s_mov_b32 s22,L_hot_%=-L_pc_%=
s_mov_b32 s23,0x20000
s_lshl_b32 s68,s67,2
s_add_u32 s68,s68,s60
s_mul_i32 s68,s68,896
v_lshlrev_b32 v67,7,v23
s_mov_b64 exec,127
buffer_load_dword v8,v67,s[20:23],s68 offen
s_mov_b64 exec,-1
s_lshr_b32 s61,s66,2
s_lshl_b32 s61,s61,3
s_lshr_b32 s68,s67,2
s_add_u32 s61,s61,s68
s_and_b32 s62,s66,1
s_lshl_b32 s62,s62,2
s_and_b32 s68,s66,2
s_add_u32 s62,s62,s68
s_lshl_b32 s62,s62,1
s_and_b32 s68,s67,3
s_add_u32 s62,s62,s68
s_lshl_b32 s83,s62,9
s_mov_b32 s84,0
s_lshl_b32 s89,s60,13
s_add_u32 s85,s89,65536
s_add_u32 s88,s89,131072
v_and_b32 v20,3,v23
v_bfe_u32 v21,v23,2,2
v_lshrrev_b32 v22,4,v23
v_lshrrev_b32 v75,3,v23
s_lshl_b32 s66,s60,6
v_add_u32 v75,s66,v75
v_mul_u32_u24 v75,1280,v75
v_and_b32 v76,7,v23
v_or_b32 v77,0,v22
v_xor_b32 v77,v77,v76
v_lshl_add_u32 v77,v77,4,v75
v_add_u32 v32,0,v77
v_add_u32 v34,18432,v77
v_add_u32 v36,40960,v77
v_add_u32 v38,59392,v77
v_or_b32 v77,4,v22
v_xor_b32 v77,v77,v76
v_lshl_add_u32 v77,v77,4,v75
v_add_u32 v33,9216,v77
v_add_u32 v35,27648,v77
v_add_u32 v37,50176,v77
v_add_u32 v39,68608,v77
s_lshl_b32 s66,s60,17
v_lshlrev_b32 v75,13,v21
v_lshl_add_u32 v75,v22,6,v75
v_lshl_add_u32 v75,v20,4,v75
v_add_u32 v75,s66,v75
v_add_u32 v40,0,v75
v_add_u32 v41,256,v75
v_add_u32 v42,32768,v75
v_add_u32 v43,33024,v75
v_add_u32 v44,65536,v75
v_add_u32 v45,65792,v75
v_add_u32 v46,98304,v75
v_add_u32 v47,98560,v75
s_mul_i32 s66,s61,307200
s_waitcnt lgkmcnt(0)
s_add_u32 s28,s4,s66
s_addc_u32 s29,s5,0
s_and_b32 s29,s29,0xffff
s_mov_b32 s30,307200
s_mov_b32 s31,0x20000
s_mov_b32 s36,s10
s_and_b32 s37,s11,0xffff
s_mov_b32 s38,5242880
s_mov_b32 s39,0x20000
s_add_u32 m0,s85,0
s_nop 0
buffer_load_dwordx4 v32,s[28:31],s84 offen lds
buffer_load_dwordx4 v33,s[28:31],s84 offen offset:1024 lds
buffer_load_dwordx4 v34,s[28:31],s84 offen offset:2048 lds
buffer_load_dwordx4 v35,s[28:31],s84 offen offset:3072 lds
s_add_u32 m0,s85,4096
s_nop 0
buffer_load_dwordx4 v36,s[28:31],s84 offen lds
buffer_load_dwordx4 v37,s[28:31],s84 offen offset:1024 lds
buffer_load_dwordx4 v38,s[28:31],s84 offen offset:2048 lds
buffer_load_dwordx4 v39,s[28:31],s84 offen offset:3072 lds
s_add_u32 s84,s84,128
s_xor_b32 s85,s85,98304
s_add_u32 m0,s88,0
s_nop 0
buffer_load_dwordx4 v40,s[36:39],s83 offen lds
s_add_u32 m0,s88,1024
s_nop 0
buffer_load_dwordx4 v41,s[36:39],s83 offen lds
s_add_u32 m0,s88,2048
s_nop 0
buffer_load_dwordx4 v42,s[36:39],s83 offen lds
s_add_u32 m0,s88,3072
s_nop 0
buffer_load_dwordx4 v43,s[36:39],s83 offen lds
s_add_u32 m0,s88,4096
s_nop 0
buffer_load_dwordx4 v44,s[36:39],s83 offen lds
s_add_u32 m0,s88,5120
s_nop 0
buffer_load_dwordx4 v45,s[36:39],s83 offen lds
s_add_u32 m0,s88,6144
s_nop 0
buffer_load_dwordx4 v46,s[36:39],s83 offen lds
s_add_u32 m0,s88,7168
s_nop 0
buffer_load_dwordx4 v47,s[36:39],s83 offen lds
s_add_u32 s83,s83,524288
s_lshl_b32 s67,s61,21
s_add_u32 s68,s67,101711872
s_add_u32 s44,s12,s68
s_addc_u32 s45,s13,0
s_and_b32 s45,s45,0xffff
s_mov_b32 s46,4096
s_mov_b32 s47,0x20000
v_mov_b32 v10,0
buffer_load_dword v9,v10,s[44:47],0 offen sc1
v_lshlrev_b32 v6,4,v23
v_lshlrev_b32 v72,12,v22
v_lshl_add_u32 v72,v21,6,v72
v_lshl_add_u32 v72,v20,4,v72
v_subrev_u32 v51,s89,v72
v_add_u32 v50,131072,v72
v_and_b32 v74,15,v23
v_lshrrev_b32 v76,1,v74
v_lshlrev_b32 v75,7,v74
s_add_u32 s66,s89,65536
v_add_u32 v75,s66,v75
v_or_b32 v77,0,v22
v_xor_b32 v77,v77,v76
v_lshl_add_u32 v48,v77,4,v75
v_or_b32 v77,4,v22
v_xor_b32 v77,v77,v76
v_lshl_add_u32 v49,v77,4,v75
s_add_u32 s86,s89,131072
s_add_u32 s87,s89,98304
s_mov_b64 exec,0
s_cmp_eq_u32 s60,0
s_cbranch_scc1 L_pd1_%=
s_cmp_eq_u32 s60,1
s_cbranch_scc1 L_pd2_%=
s_cmp_eq_u32 s60,2
s_cbranch_scc1 L_pd3_%=
s_branch L_pd4_%=
L_trt0_%=:
s_mov_b64 exec,-1
s_mov_b32 s88,s87
s_add_u32 m0,s88,0
s_nop 0
buffer_load_dwordx4 v40,s[36:39],s83 offen lds
s_add_u32 m0,s88,1024
s_nop 0
buffer_load_dwordx4 v41,s[36:39],s83 offen lds
s_add_u32 m0,s88,2048
s_nop 0
buffer_load_dwordx4 v42,s[36:39],s83 offen lds
s_add_u32 m0,s88,3072
s_nop 0
buffer_load_dwordx4 v43,s[36:39],s83 offen lds
s_add_u32 m0,s88,4096
s_nop 0
buffer_load_dwordx4 v44,s[36:39],s83 offen lds
s_add_u32 m0,s88,5120
s_nop 0
buffer_load_dwordx4 v45,s[36:39],s83 offen lds
s_add_u32 m0,s88,6144
s_nop 0
buffer_load_dwordx4 v46,s[36:39],s83 offen lds
s_add_u32 m0,s88,7168
s_nop 0
buffer_load_dwordx4 v47,s[36:39],s83 offen lds
s_add_u32 s83,s83,524288
s_add_u32 s88,s89,0
s_mov_b64 exec,0
s_cmp_eq_u32 s60,0
s_cbranch_scc1 L_pd9_%=
s_cmp_eq_u32 s60,1
s_cbranch_scc1 L_pd10_%=
s_cmp_eq_u32 s60,2
s_cbranch_scc1 L_pd11_%=
s_branch L_pd12_%=
L_t1b_%=:
s_cmp_ge_u32 s62,8
s_cbranch_scc1 L_pd19_%=
s_branch L_pd14_%=
L_t1c_%=:
s_cmp_ge_u32 s62,8
s_cbranch_scc1 L_pd20_%=
s_branch L_pd15_%=
s_nop 0
s_nop 0
s_nop 0
s_nop 0
s_nop 0
s_nop 0
L_trt1_%=:
s_mov_b64 exec,-1
s_branch L_pacx_%=
L_pace_%=:
s_cmp_ge_u32 s4,8
s_cselect_b32 s5,3,1
s_sub_u32 s5,s4,s5
s_max_i32 s5,s5,s7
L_pacl_%=:
s_sub_u32 s4,s4,1
s_cmp_eq_u32 s4,0
s_cbranch_scc1 L_pace2_%=
s_nop 2
s_branch L_pacl_%=
L_pace2_%=:
s_setpc_b64 s[92:93]
L_pacx_%=:
s_waitcnt vmcnt(17)
ds_read_b128 a[192:195],v48
ds_read_b128 a[196:199],v49
ds_read_b128 a[200:203],v48 offset:2048
ds_read_b128 a[204:207],v49 offset:2048
ds_read_b128 a[208:211],v48 offset:4096
ds_read_b128 a[212:215],v49 offset:4096
ds_read_b128 a[216:219],v48 offset:6144
ds_read_b128 a[220:223],v49 offset:6144
s_waitcnt vmcnt(9)
s_barrier
ds_read_b64_tr_b16 a[64:65],v50
ds_read_b64_tr_b16 a[66:67],v50 offset:2048
ds_read_b64_tr_b16 a[68:69],v50 offset:16384
ds_read_b64_tr_b16 a[70:71],v50 offset:18432
ds_read_b64_tr_b16 a[72:73],v50 offset:8
ds_read_b64_tr_b16 a[74:75],v50 offset:2056
ds_read_b64_tr_b16 a[76:77],v50 offset:16392
ds_read_b64_tr_b16 a[78:79],v50 offset:18440
s_waitcnt lgkmcnt(6)
v_mfma_f32_16x16x32_bf16 a[0:3],a[64:67],a[192:195],0
s_add_u32 m0,s85,0
ds_read_b64_tr_b16 a[80:81],v50 offset:256
v_xor_b32 v48,98304,v48
s_waitcnt lgkmcnt(5)
v_mfma_f32_16x16x32_bf16 a[0:3],a[68:71],a[196:199],a[0:3]
buffer_load_dwordx4 v32,s[28:31],s84 offen lds
v_mfma_f32_16x16x32_bf16 a[16:19],a[68:71],a[204:207],0
s_mov_b32 s69,s86
ds_read_b64_tr_b16 a[82:83],v50 offset:2304
v_xor_b32 v49,98304,v49
v_mfma_f32_16x16x32_bf16 a[16:19],a[64:67],a[200:203],a[16:19]
s_mov_b32 s86,s87
s_mov_b32 s87,s88
ds_read_b64_tr_b16 a[84:85],v50 offset:16640
v_mfma_f32_16x16x32_bf16 a[32:35],a[64:67],a[208:211],0
ds_read_b64_tr_b16 a[86:87],v50 offset:18688
v_mfma_f32_16x16x32_bf16 a[32:35],a[68:71],a[212:215],a[32:35]
ds_read_b64_tr_b16 a[88:89],v50 offset:264
v_mfma_f32_16x16x32_bf16 a[48:51],a[68:71],a[220:223],0
buffer_load_dwordx4 v33,s[28:31],s84 offen offset:1024 lds
v_mfma_f32_16x16x32_bf16 a[48:51],a[64:67],a[216:219],a[48:51]
s_waitcnt lgkmcnt(5)
ds_read_b64_tr_b16 a[90:91],v50 offset:2312
v_mfma_f32_16x16x32_bf16 a[4:7],a[72:75],a[192:195],0
s_lshl_b32 s67,s60,12
ds_read_b64_tr_b16 a[92:93],v50 offset:16648
v_cmp_lt_u32 vcc,6,v23
v_mfma_f32_16x16x32_bf16 a[4:7],a[76:79],a[196:199],a[4:7]
s_lshl_b32 s65,s61,5
ds_read_b64_tr_b16 a[94:95],v50 offset:18696
v_lshlrev_b32 v19,3,v6
v_mfma_f32_16x16x32_bf16 a[20:23],a[76:79],a[204:207],0
s_lshl_b32 s66,s62,1
v_cndmask_b32_e64 v19,v19,-1,vcc
v_lshrrev_b32 v18,1,v19
v_mfma_f32_16x16x32_bf16 a[20:23],a[72:75],a[200:203],a[20:23]
buffer_load_dwordx4 v34,s[28:31],s84 offen offset:2048 lds
v_mfma_f32_16x16x32_bf16 a[36:39],a[72:75],a[208:211],0
s_add_u32 s65,s65,s66
ds_read_b64_tr_b16 a[96:97],v50 offset:512
v_min_u32 v19,v19,v19
v_mfma_f32_16x16x32_bf16 a[36:39],a[76:79],a[212:215],a[36:39]
s_lshl_b32 s66,s65,18
ds_read_b64_tr_b16 a[98:99],v50 offset:2560
v_cndmask_b32_e64 v19,v19,v18,vcc
v_mfma_f32_16x16x32_bf16 a[52:55],a[76:79],a[220:223],0
s_lshr_b32 s63,s62,2
ds_read_b64_tr_b16 a[100:101],v50 offset:16896
v_lshlrev_b32 v11,8,v21
v_mfma_f32_16x16x32_bf16 a[52:55],a[72:75],a[216:219],a[52:55]
s_mov_b32 s22,524288
ds_read_b64_tr_b16 a[102:103],v50 offset:18944
v_lshl_add_u32 v11,v22,6,v11
s_waitcnt lgkmcnt(6)
v_mfma_f32_16x16x32_bf16 a[8:11],a[80:83],a[192:195],0
buffer_load_dwordx4 v35,s[28:31],s84 offen offset:3072 lds
v_mfma_f32_16x16x32_bf16 a[8:11],a[84:87],a[196:199],a[8:11]
s_mov_b32 s23,0x20000
ds_read_b64_tr_b16 a[104:105],v50 offset:520
v_add_u32 v11,s67,v11
v_mfma_f32_16x16x32_bf16 a[24:27],a[84:87],a[204:207],0
s_mov_b32 s26,524288
ds_read_b64_tr_b16 a[106:107],v50 offset:2568
v_lshl_add_u32 v4,v20,4,v11
v_mfma_f32_16x16x32_bf16 a[24:27],a[80:83],a[200:203],a[24:27]
s_add_u32 m0,s85,4096
s_xor_b32 s85,s85,98304
ds_read_b64_tr_b16 a[108:109],v50 offset:16904
v_mfma_f32_16x16x32_bf16 a[40:43],a[80:83],a[208:211],0
s_mov_b32 s27,0x20000
v_xor_b32 v12,2,v20
v_lshl_add_u32 v5,v12,4,v11
v_mfma_f32_16x16x32_bf16 a[40:43],a[84:87],a[212:215],a[40:43]
buffer_load_dwordx4 v36,s[28:31],s84 offen lds
v_mfma_f32_16x16x32_bf16 a[56:59],a[84:87],a[220:223],0
s_mov_b32 s42,131072
ds_read_b64_tr_b16 a[110:111],v50 offset:18952
v_lshlrev_b32 v11,10,v21
v_mfma_f32_16x16x32_bf16 a[56:59],a[80:83],a[216:219],a[56:59]
s_mov_b32 s43,0x20000
ds_read_b64_tr_b16 a[112:113],v50 offset:768
v_lshl_add_u32 v11,v20,6,v11
v_mfma_f32_16x16x32_bf16 a[12:15],a[88:91],a[192:195],0
s_mov_b32 s48,s14
ds_read_b64_tr_b16 a[114:115],v50 offset:2816
v_and_b32 v12,2,v21
s_waitcnt lgkmcnt(6)
v_mfma_f32_16x16x32_bf16 a[12:15],a[92:95],a[196:199],a[12:15]
s_and_b32 s49,s15,0xffff
ds_read_b64_tr_b16 a[116:117],v50 offset:17152
v_xor_b32 v12,v12,v22
v_mfma_f32_16x16x32_bf16 a[28:31],a[92:95],a[204:207],0
buffer_load_dwordx4 v37,s[28:31],s84 offen offset:1024 lds
v_mfma_f32_16x16x32_bf16 a[28:31],a[88:91],a[200:203],a[28:31]
s_mov_b32 s50,5242880
ds_read_b64_tr_b16 a[118:119],v50 offset:19200
v_lshl_add_u32 v11,v12,4,v11
v_mfma_f32_16x16x32_bf16 a[44:47],a[88:91],a[208:211],0
s_mov_b32 s51,0x20000
ds_read_b64_tr_b16 a[120:121],v50 offset:776
v_add_u32 v1,98304,v11
v_mfma_f32_16x16x32_bf16 a[44:47],a[92:95],a[212:215],a[44:47]
s_add_u32 s76,s67,98304
ds_read_b64_tr_b16 a[122:123],v50 offset:2824
v_lshlrev_b32 v11,10,v22
v_mfma_f32_16x16x32_bf16 a[60:63],a[92:95],a[220:223],0
s_mul_i32 s74,s61,5
v_lshl_add_u32 v11,v21,6,v11
v_lshl_add_u32 v11,v20,3,v11
v_mfma_f32_16x16x32_bf16 a[60:63],a[88:91],a[216:219],a[60:63]
buffer_load_dwordx4 v38,s[28:31],s84 offen offset:2048 lds
v_mfma_f32_16x16x32_bf16 v[64:67],a[96:99],a[192:195],0
s_add_u32 s74,s74,s62
ds_read_b64_tr_b16 a[124:125],v50 offset:17160
v_add_u32 v11,114688,v11
v_mfma_f32_16x16x32_bf16 v[64:67],a[100:103],a[196:199],v[64:67]
s_and_b32 s74,s74,15
ds_read_b64_tr_b16 a[126:127],v50 offset:19208
v_and_b32 v12,1,v22
v_mfma_f32_16x16x32_bf16 v[80:83],a[100:103],a[204:207],0
s_lshl_b32 s74,s74,14
ds_read_b64_tr_b16 a[64:65],v50 offset:1024
v_lshl_add_u32 v2,v12,5,v11
v_mfma_f32_16x16x32_bf16 v[80:83],a[96:99],a[200:203],v[80:83]
s_mov_b32 s75,0
ds_read_b64_tr_b16 a[66:67],v50 offset:3072
v_xor_b32 v12,1,v12
v_mfma_f32_16x16x32_bf16 v[96:99],a[96:99],a[208:211],0
buffer_load_dwordx4 v39,s[28:31],s84 offen offset:3072 lds
v_mfma_f32_16x16x32_bf16 v[96:99],a[100:103],a[212:215],v[96:99]
s_add_u32 s84,s84,128
ds_read_b64_tr_b16 a[68:69],v50 offset:17408
v_lshl_add_u32 v3,v12,5,v11
v_mfma_f32_16x16x32_bf16 v[112:115],a[100:103],a[220:223],0
s_mov_b32 s77,98304
ds_read_b64_tr_b16 a[70:71],v50 offset:19456
v_add_u32 v11,0,v23
v_mfma_f32_16x16x32_bf16 v[112:115],a[96:99],a[216:219],v[112:115]
s_add_u32 m0,s88,0
ds_read_b64_tr_b16 a[72:73],v50 offset:1032
v_mul_u32_u24 v12,3277,v11
s_waitcnt lgkmcnt(7)
v_mfma_f32_16x16x32_bf16 v[68:71],a[104:107],a[192:195],0
s_mov_b32 s71,0x3e0293ee
v_lshrrev_b32 v12,16,v12
v_mul_u32_u24 v13,20,v12
v_mfma_f32_16x16x32_bf16 v[68:71],a[108:111],a[196:199],v[68:71]
buffer_load_dwordx4 v40,s[36:39],s83 offen lds
v_mfma_f32_16x16x32_bf16 v[84:87],a[108:111],a[204:207],0
s_mov_b32 s73,0x53800000
ds_read_b64_tr_b16 a[74:75],v50 offset:3080
)A"
R"A(v_sub_u32 v13,v11,v13
v_mfma_f32_16x16x32_bf16 v[84:87],a[104:107],a[200:203],v[84:87]
s_lshl_b32 s79,s60,14
ds_read_b64_tr_b16 a[76:77],v50 offset:17416
v_bfe_u32 v14,v12,2,1
v_mfma_f32_16x16x32_bf16 v[100:103],a[104:107],a[208:211],0
s_add_u32 m0,s88,1024
ds_read_b64_tr_b16 a[78:79],v50 offset:19464
v_lshlrev_b32 v14,1,v14
v_mfma_f32_16x16x32_bf16 v[100:103],a[108:111],a[212:215],v[100:103]
s_add_u32 s80,s79,4096
ds_read_b64_tr_b16 a[80:81],v50 offset:1280
v_xor_b32 v13,v13,v14
v_mfma_f32_16x16x32_bf16 v[116:119],a[108:111],a[220:223],0
buffer_load_dwordx4 v41,s[36:39],s83 offen lds
v_mfma_f32_16x16x32_bf16 v[116:119],a[104:107],a[216:219],v[116:119]
s_add_u32 s81,s79,8192
ds_read_b64_tr_b16 a[82:83],v50 offset:3328
v_lshrrev_b32 v14,2,v13
v_mfma_f32_16x16x32_bf16 v[72:75],a[112:115],a[192:195],0
s_add_u32 s82,s79,12288
ds_read_b64_tr_b16 a[84:85],v50 offset:17664
v_cmp_eq_u32 vcc,3,v14
v_mfma_f32_16x16x32_bf16 v[72:75],a[116:119],a[196:199],v[72:75]
s_add_u32 m0,s88,2048
ds_read_b64_tr_b16 a[86:87],v50 offset:19712
v_min_u32 v15,3,v14
v_mfma_f32_16x16x32_bf16 v[88:91],a[116:119],a[204:207],0
s_add_u32 s20,s6,s66
s_addc_u32 s21,s7,0
v_and_b32 v13,3,v13
v_mfma_f32_16x16x32_bf16 v[88:91],a[112:115],a[200:203],v[88:91]
buffer_load_dwordx4 v42,s[36:39],s83 offen lds
v_mfma_f32_16x16x32_bf16 v[104:107],a[112:115],a[208:211],0
s_add_u32 s24,s8,s66
s_addc_u32 s25,s9,0
ds_read_b64_tr_b16 a[88:89],v50 offset:1288
v_mfma_f32_16x16x32_bf16 v[104:107],a[116:119],a[212:215],v[104:107]
s_lshl_b32 s66,s65,16
s_add_u32 s65,s63,1
ds_read_b64_tr_b16 a[90:91],v50 offset:3336
v_mfma_f32_16x16x32_bf16 v[120:123],a[116:119],a[220:223],0
s_add_u32 m0,s88,3072
ds_read_b64_tr_b16 a[92:93],v50 offset:17672
v_add_u32 v15,s65,v15
v_mfma_f32_16x16x32_bf16 v[120:123],a[112:115],a[216:219],v[120:123]
s_add_u32 s40,s12,s66
s_addc_u32 s41,s13,0
ds_read_b64_tr_b16 a[94:95],v50 offset:19720
v_mfma_f32_16x16x32_bf16 v[76:79],a[120:123],a[192:195],0
buffer_load_dwordx4 v43,s[36:39],s83 offen lds
s_waitcnt lgkmcnt(5)
v_mfma_f32_16x16x32_bf16 v[76:79],a[124:127],a[196:199],v[76:79]
s_mul_i32 s66,s60,40960
ds_read_b64_tr_b16 a[96:97],v50 offset:1536
v_and_b32 v15,3,v15
v_mfma_f32_16x16x32_bf16 v[92:95],a[124:127],a[204:207],0
s_and_b32 s21,s21,0xffff
ds_read_b64_tr_b16 a[98:99],v50 offset:3584
v_cndmask_b32_e64 v15,v15,4,vcc
v_mfma_f32_16x16x32_bf16 v[92:95],a[120:123],a[200:203],v[92:95]
s_add_u32 m0,s88,4096
ds_read_b64_tr_b16 a[100:101],v50 offset:17920
v_lshl_add_u32 v13,v15,2,v13
v_mfma_f32_16x16x32_bf16 v[108:111],a[120:123],a[208:211],0
s_and_b32 s25,s25,0xffff
v_mul_u32_u24 v12,1280,v12
v_lshl_add_u32 v12,v13,4,v12
v_mfma_f32_16x16x32_bf16 v[108:111],a[124:127],a[212:215],v[108:111]
buffer_load_dwordx4 v44,s[36:39],s83 offen lds
v_mfma_f32_16x16x32_bf16 v[124:127],a[124:127],a[220:223],0
s_and_b32 s41,s41,0xffff
ds_read_b64_tr_b16 a[102:103],v50 offset:19968
v_add_u32 v12,s66,v12
v_mfma_f32_16x16x32_bf16 v[124:127],a[120:123],a[216:219],v[124:127]
s_and_b32 s64,s62,3
ds_read_b64_tr_b16 a[104:105],v50 offset:1544
v_subrev_u32 v24,0,v12
v_mfma_f32_16x16x32_bf16 v[128:131],a[64:67],a[192:195],0
s_add_u32 m0,s88,5120
ds_read_b64_tr_b16 a[106:107],v50 offset:3592
v_add_u32 v11,64,v23
v_mfma_f32_16x16x32_bf16 v[128:131],a[68:71],a[196:199],v[128:131]
s_mov_b32 s72,0xff800000
ds_read_b64_tr_b16 a[108:109],v50 offset:17928
v_mul_u32_u24 v12,3277,v11
v_mfma_f32_16x16x32_bf16 v[160:163],a[68:71],a[204:207],0
buffer_load_dwordx4 v45,s[36:39],s83 offen lds
v_mfma_f32_16x16x32_bf16 v[160:163],a[64:67],a[200:203],v[160:163]
s_lshl_b32 s18,s60,24
ds_read_b64_tr_b16 a[110:111],v50 offset:19976
v_lshrrev_b32 v12,16,v12
v_mfma_f32_16x16x32_bf16 v[192:195],a[64:67],a[208:211],0
s_lshl_b32 s68,s62,6
ds_read_b64_tr_b16 a[112:113],v50 offset:1792
v_mul_u32_u24 v13,20,v12
v_mfma_f32_16x16x32_bf16 v[192:195],a[68:71],a[212:215],v[192:195]
s_add_u32 m0,s88,6144
ds_read_b64_tr_b16 a[114:115],v50 offset:3840
v_sub_u32 v13,v11,v13
v_mfma_f32_16x16x32_bf16 v[224:227],a[68:71],a[220:223],0
s_add_u32 s18,s18,s68
v_bfe_u32 v14,v12,2,1
v_lshlrev_b32 v14,1,v14
v_mfma_f32_16x16x32_bf16 v[224:227],a[64:67],a[216:219],v[224:227]
buffer_load_dwordx4 v46,s[36:39],s83 offen lds
v_mfma_f32_16x16x32_bf16 v[132:135],a[72:75],a[192:195],0
s_lshl_b32 s90,%1,2
ds_read_b64_tr_b16 a[116:117],v50 offset:18176
v_xor_b32 v13,v13,v14
v_mfma_f32_16x16x32_bf16 v[132:135],a[76:79],a[196:199],v[132:135]
s_add_u32 s90,s90,s60
ds_read_b64_tr_b16 a[118:119],v50 offset:20224
v_lshrrev_b32 v14,2,v13
v_mfma_f32_16x16x32_bf16 v[164:167],a[76:79],a[204:207],0
s_add_u32 m0,s88,7168
ds_read_b64_tr_b16 a[120:121],v50 offset:1800
v_cmp_eq_u32 vcc,3,v14
v_mfma_f32_16x16x32_bf16 v[164:167],a[72:75],a[200:203],v[164:167]
s_mov_b32 s88,s69
ds_read_b64_tr_b16 a[122:123],v50 offset:3848
v_min_u32 v15,3,v14
v_mfma_f32_16x16x32_bf16 v[196:199],a[72:75],a[208:211],0
buffer_load_dwordx4 v47,s[36:39],s83 offen lds
v_mfma_f32_16x16x32_bf16 v[196:199],a[76:79],a[212:215],v[196:199]
s_add_u32 s83,s83,524288
ds_read_b64_tr_b16 a[124:125],v50 offset:18184
v_add_u32 v15,s65,v15
v_mfma_f32_16x16x32_bf16 v[228:231],a[76:79],a[220:223],0
s_mul_i32 s90,s90,5120
ds_read_b64_tr_b16 a[126:127],v50 offset:20232
v_add_u32 v50,s86,v51
v_mfma_f32_16x16x32_bf16 v[228:231],a[72:75],a[216:219],v[228:231]
s_add_u32 m0,s85,0
v_and_b32 v15,3,v15
v_cndmask_b32_e64 v15,v15,4,vcc
v_mfma_f32_16x16x32_bf16 v[136:139],a[80:83],a[192:195],0
s_mul_i32 s58,s61,307200
v_and_b32 v13,3,v13
v_lshl_add_u32 v13,v15,2,v13
s_waitcnt lgkmcnt(5)
v_mfma_f32_16x16x32_bf16 v[136:139],a[84:87],a[196:199],v[136:139]
buffer_load_dwordx4 v32,s[28:31],s84 offen lds
v_mfma_f32_16x16x32_bf16 v[168:171],a[84:87],a[204:207],0
v_mul_u32_u24 v12,1280,v12
v_lshl_add_u32 v12,v13,4,v12
v_mfma_f32_16x16x32_bf16 v[168:171],a[80:83],a[200:203],v[168:171]
v_add_u32 v12,s66,v12
v_subrev_u32 v25,1024,v12
v_mfma_f32_16x16x32_bf16 v[200:203],a[80:83],a[208:211],0
v_add_u32 v11,128,v23
v_mul_u32_u24 v12,3277,v11
v_mfma_f32_16x16x32_bf16 v[200:203],a[84:87],a[212:215],v[200:203]
v_lshrrev_b32 v12,16,v12
v_mul_u32_u24 v13,20,v12
v_mfma_f32_16x16x32_bf16 v[232:235],a[84:87],a[220:223],0
buffer_load_dwordx4 v33,s[28:31],s84 offen offset:1024 lds
v_mfma_f32_16x16x32_bf16 v[232:235],a[80:83],a[216:219],v[232:235]
v_sub_u32 v13,v11,v13
v_bfe_u32 v14,v12,2,1
v_mfma_f32_16x16x32_bf16 v[140:143],a[88:91],a[192:195],0
v_lshlrev_b32 v14,1,v14
v_xor_b32 v13,v13,v14
v_mfma_f32_16x16x32_bf16 v[140:143],a[92:95],a[196:199],v[140:143]
v_lshrrev_b32 v14,2,v13
v_cmp_eq_u32 vcc,3,v14
v_mfma_f32_16x16x32_bf16 v[172:175],a[92:95],a[204:207],0
v_min_u32 v15,3,v14
v_add_u32 v15,s65,v15
v_mfma_f32_16x16x32_bf16 v[172:175],a[88:91],a[200:203],v[172:175]
buffer_load_dwordx4 v34,s[28:31],s84 offen offset:2048 lds
v_mfma_f32_16x16x32_bf16 v[204:207],a[88:91],a[208:211],0
v_and_b32 v15,3,v15
v_cndmask_b32_e64 v15,v15,4,vcc
v_mfma_f32_16x16x32_bf16 v[204:207],a[92:95],a[212:215],v[204:207]
v_and_b32 v13,3,v13
v_lshl_add_u32 v13,v15,2,v13
v_mfma_f32_16x16x32_bf16 v[236:239],a[92:95],a[220:223],0
v_mul_u32_u24 v12,1280,v12
v_lshl_add_u32 v12,v13,4,v12
v_mfma_f32_16x16x32_bf16 v[236:239],a[88:91],a[216:219],v[236:239]
v_add_u32 v12,s66,v12
v_subrev_u32 v26,2048,v12
s_waitcnt vmcnt(19) lgkmcnt(0)
s_barrier
v_mfma_f32_16x16x32_bf16 v[144:147],a[96:99],a[192:195],0
buffer_load_dwordx4 v35,s[28:31],s84 offen offset:3072 lds
v_mfma_f32_16x16x32_bf16 v[144:147],a[100:103],a[196:199],v[144:147]
s_waitcnt vmcnt(12)
ds_read_b128 a[224:227],v48
v_add_u32 v11,192,v23
v_mfma_f32_16x16x32_bf16 v[176:179],a[100:103],a[204:207],0
s_add_u32 m0,s85,4096
ds_read_b128 a[228:231],v49
v_mul_u32_u24 v12,3277,v11
v_mfma_f32_16x16x32_bf16 v[176:179],a[96:99],a[200:203],v[176:179]
s_xor_b32 s85,s85,98304
ds_read_b128 a[232:235],v48 offset:2048
v_lshrrev_b32 v12,16,v12
v_mfma_f32_16x16x32_bf16 v[208:211],a[96:99],a[208:211],0
ds_read_b128 a[236:239],v49 offset:2048
v_mul_u32_u24 v13,20,v12
v_mfma_f32_16x16x32_bf16 v[208:211],a[100:103],a[212:215],v[208:211]
buffer_load_dwordx4 v36,s[28:31],s84 offen lds
v_mfma_f32_16x16x32_bf16 v[240:243],a[100:103],a[220:223],0
ds_read_b128 a[240:243],v48 offset:4096
v_sub_u32 v13,v11,v13
v_mfma_f32_16x16x32_bf16 v[240:243],a[96:99],a[216:219],v[240:243]
ds_read_b128 a[244:247],v49 offset:4096
v_bfe_u32 v14,v12,2,1
v_mfma_f32_16x16x32_bf16 v[148:151],a[104:107],a[192:195],0
ds_read_b128 a[248:251],v48 offset:6144
v_lshlrev_b32 v14,1,v14
v_mfma_f32_16x16x32_bf16 v[148:151],a[108:111],a[196:199],v[148:151]
v_xor_b32 v13,v13,v14
v_lshrrev_b32 v14,2,v13
v_mfma_f32_16x16x32_bf16 v[180:183],a[108:111],a[204:207],0
buffer_load_dwordx4 v37,s[28:31],s84 offen offset:1024 lds
v_mfma_f32_16x16x32_bf16 v[180:183],a[104:107],a[200:203],v[180:183]
ds_read_b128 a[252:255],v49 offset:6144
v_cmp_eq_u32 vcc,3,v14
v_mfma_f32_16x16x32_bf16 v[212:215],a[104:107],a[208:211],0
ds_read_b64_tr_b16 a[64:65],v50
v_min_u32 v15,3,v14
v_mfma_f32_16x16x32_bf16 v[212:215],a[108:111],a[212:215],v[212:215]
ds_read_b64_tr_b16 a[66:67],v50 offset:2048
v_add_u32 v15,s65,v15
v_mfma_f32_16x16x32_bf16 v[244:247],a[108:111],a[220:223],0
ds_read_b64_tr_b16 a[68:69],v50 offset:16384
v_and_b32 v15,3,v15
v_mfma_f32_16x16x32_bf16 v[244:247],a[104:107],a[216:219],v[244:247]
buffer_load_dwordx4 v38,s[28:31],s84 offen offset:2048 lds
v_mfma_f32_16x16x32_bf16 v[152:155],a[112:115],a[192:195],0
ds_read_b64_tr_b16 a[70:71],v50 offset:18432
v_cndmask_b32_e64 v15,v15,4,vcc
v_mfma_f32_16x16x32_bf16 v[152:155],a[116:119],a[196:199],v[152:155]
ds_read_b64_tr_b16 a[72:73],v50 offset:8
v_and_b32 v13,3,v13
L_pd1_%=:
s_cbranch_execz L_pd5_%=
v_mfma_f32_16x16x32_bf16 v[184:187],a[116:119],a[204:207],0
ds_read_b64_tr_b16 a[74:75],v50 offset:2056
v_lshl_add_u32 v13,v15,2,v13
v_mfma_f32_16x16x32_bf16 v[184:187],a[112:115],a[200:203],v[184:187]
v_mul_u32_u24 v12,1280,v12
v_lshl_add_u32 v12,v13,4,v12
v_mfma_f32_16x16x32_bf16 v[216:219],a[112:115],a[208:211],0
buffer_load_dwordx4 v39,s[28:31],s84 offen offset:3072 lds
v_mfma_f32_16x16x32_bf16 v[216:219],a[116:119],a[212:215],v[216:219]
s_add_u32 s84,s84,128
ds_read_b64_tr_b16 a[76:77],v50 offset:16392
v_add_u32 v12,s66,v12
v_mfma_f32_16x16x32_bf16 v[248:251],a[116:119],a[220:223],0
ds_read_b64_tr_b16 a[78:79],v50 offset:18440
v_subrev_u32 v27,3072,v12
v_mfma_f32_16x16x32_bf16 v[248:251],a[112:115],a[216:219],v[248:251]
ds_read_b64_tr_b16 a[80:81],v50 offset:256
v_add_u32 v11,256,v23
v_mfma_f32_16x16x32_bf16 v[156:159],a[120:123],a[192:195],0
ds_read_b64_tr_b16 a[82:83],v50 offset:2304
v_mul_u32_u24 v12,3277,v11
v_mfma_f32_16x16x32_bf16 v[156:159],a[124:127],a[196:199],v[156:159]
v_lshrrev_b32 v12,16,v12
v_mul_u32_u24 v13,20,v12
v_mfma_f32_16x16x32_bf16 v[188:191],a[124:127],a[204:207],0
ds_read_b64_tr_b16 a[84:85],v50 offset:16640
v_sub_u32 v13,v11,v13
v_mfma_f32_16x16x32_bf16 v[188:191],a[120:123],a[200:203],v[188:191]
ds_read_b64_tr_b16 a[86:87],v50 offset:18688
v_bfe_u32 v14,v12,2,1
v_mfma_f32_16x16x32_bf16 v[220:223],a[120:123],a[208:211],0
ds_read_b64_tr_b16 a[88:89],v50 offset:264
v_lshlrev_b32 v14,1,v14
v_mfma_f32_16x16x32_bf16 v[220:223],a[124:127],a[212:215],v[220:223]
v_xor_b32 v13,v13,v14
v_lshrrev_b32 v14,2,v13
v_cmp_eq_u32 vcc,3,v14
v_mfma_f32_16x16x32_bf16 v[252:255],a[124:127],a[220:223],0
ds_read_b64_tr_b16 a[90:91],v50 offset:2312
v_mov_b32 v17,0
v_readfirstlane_b32 s52,v9
v_mfma_f32_16x16x32_bf16 v[252:255],a[120:123],a[216:219],v[252:255]
s_cmp_eq_u32 s60,3
s_cselect_b32 s72,0x7149f2ca,s72
s_add_u32 s56,s40,52224
s_addc_u32 s57,s41,0
s_add_u32 s58,s16,s58
s_addc_u32 s59,s17,0
s_cmp_eq_u32 s60,1
s_cselect_b64 s[56:57],s[58:59],s[56:57]
s_cmp_eq_u32 s60,2
s_cselect_b64 s[56:57],s[20:21],s[56:57]
s_cmp_eq_u32 s60,3
s_cselect_b64 s[56:57],s[24:25],s[56:57]
s_add_u32 s52,s52,1
s_xor_b32 s53,s52,0x9e3779b9
ds_read_b64_tr_b16 a[92:93],v50 offset:16648
v_mov_b32 v28,0x3f803f80
v_mov_b32 v29,0x3f803f80
v_mov_b32 v30,0x3f803f80
v_mov_b32 v31,0x3f803f80
v_min_u32 v15,3,v14
v_add_u32 v15,s65,v15
v_and_b32 v15,3,v15
v_cndmask_b32_e64 v15,v15,4,vcc
v_and_b32 v13,3,v13
v_lshl_add_u32 v13,v15,2,v13
)A"
R"A(v_mul_u32_u24 v12,1280,v12
v_lshl_add_u32 v12,v13,4,v12
v_add_u32 v12,s66,v12
v_subrev_u32 v7,0,v12
ds_read_b64_tr_b16 a[94:95],v50 offset:18696
global_load_dword a184,v17,s[56:57] nt
s_mov_b32 s70,3
s_memrealtime s[8:9]
s_memtime s[4:5]
L_qp_%=:
s_waitcnt lgkmcnt(6)
v_mfma_f32_16x16x32_bf16 a[0:3],a[64:67],a[224:227],a[0:3]
s_add_u32 m0,s88,0
ds_read_b64_tr_b16 a[96:97],v50 offset:512
v_xor_b32 v48,98304,v48
v_mfma_f32_16x16x32_bf16 a[0:3],a[68:71],a[228:231],a[0:3]
buffer_load_dwordx4 v40,s[36:39],s83 offen lds
v_mfma_f32_16x16x32_bf16 a[16:19],a[68:71],a[236:239],a[16:19]
s_mov_b32 s69,s86
ds_read_b64_tr_b16 a[98:99],v50 offset:2560
v_xor_b32 v49,98304,v49
v_mfma_f32_16x16x32_bf16 a[16:19],a[64:67],a[232:235],a[16:19]
s_mov_b32 s86,s87
s_mov_b32 s87,s88
ds_read_b64_tr_b16 a[100:101],v50 offset:16896
v_mfma_f32_16x16x32_bf16 a[32:35],a[64:67],a[240:243],a[32:35]
ds_read_b64_tr_b16 a[102:103],v50 offset:18944
v_mfma_f32_16x16x32_bf16 a[32:35],a[68:71],a[244:247],a[32:35]
ds_read_b64_tr_b16 a[104:105],v50 offset:520
v_mfma_f32_16x16x32_bf16 a[48:51],a[68:71],a[252:255],a[48:51]
s_add_u32 m0,s88,1024
v_mfma_f32_16x16x32_bf16 a[48:51],a[64:67],a[248:251],a[48:51]
ds_read_b64_tr_b16 a[106:107],v50 offset:2568
v_mfma_f32_16x16x32_bf16 a[4:7],a[72:75],a[224:227],a[4:7]
buffer_load_dwordx4 v41,s[36:39],s83 offen lds
v_mfma_f32_16x16x32_bf16 a[4:7],a[76:79],a[228:231],a[4:7]
ds_read_b64_tr_b16 a[108:109],v50 offset:16904
v_mfma_f32_16x16x32_bf16 a[20:23],a[76:79],a[236:239],a[20:23]
ds_read_b64_tr_b16 a[110:111],v50 offset:18952
v_mfma_f32_16x16x32_bf16 a[20:23],a[72:75],a[232:235],a[20:23]
ds_read_b64_tr_b16 a[112:113],v50 offset:768
v_mfma_f32_16x16x32_bf16 a[36:39],a[72:75],a[240:243],a[36:39]
ds_read_b64_tr_b16 a[114:115],v50 offset:2816
v_mfma_f32_16x16x32_bf16 a[36:39],a[76:79],a[244:247],a[36:39]
s_add_u32 m0,s88,2048
v_mfma_f32_16x16x32_bf16 a[52:55],a[76:79],a[252:255],a[52:55]
s_waitcnt lgkmcnt(4)
ds_read_b64_tr_b16 a[116:117],v50 offset:17152
v_mfma_f32_16x16x32_bf16 a[52:55],a[72:75],a[248:251],a[52:55]
buffer_load_dwordx4 v42,s[36:39],s83 offen lds
v_mfma_f32_16x16x32_bf16 a[8:11],a[80:83],a[224:227],a[8:11]
s_waitcnt lgkmcnt(5)
ds_read_b64_tr_b16 a[118:119],v50 offset:19200
v_mfma_f32_16x16x32_bf16 a[8:11],a[84:87],a[228:231],a[8:11]
ds_read_b64_tr_b16 a[120:121],v50 offset:776
v_mfma_f32_16x16x32_bf16 a[24:27],a[84:87],a[236:239],a[24:27]
ds_read_b64_tr_b16 a[122:123],v50 offset:2824
v_mfma_f32_16x16x32_bf16 a[24:27],a[80:83],a[232:235],a[24:27]
ds_read_b64_tr_b16 a[124:125],v50 offset:17160
v_mfma_f32_16x16x32_bf16 a[40:43],a[80:83],a[240:243],a[40:43]
s_add_u32 m0,s88,3072
v_mfma_f32_16x16x32_bf16 a[40:43],a[84:87],a[244:247],a[40:43]
ds_read_b64_tr_b16 a[126:127],v50 offset:19208
v_mfma_f32_16x16x32_bf16 a[56:59],a[84:87],a[252:255],a[56:59]
buffer_load_dwordx4 v43,s[36:39],s83 offen lds
v_mfma_f32_16x16x32_bf16 a[56:59],a[80:83],a[248:251],a[56:59]
ds_read_b64_tr_b16 a[64:65],v50 offset:1024
v_mfma_f32_16x16x32_bf16 a[12:15],a[88:91],a[224:227],a[12:15]
ds_read_b64_tr_b16 a[66:67],v50 offset:3072
v_mfma_f32_16x16x32_bf16 a[12:15],a[92:95],a[228:231],a[12:15]
ds_read_b64_tr_b16 a[68:69],v50 offset:17408
v_mfma_f32_16x16x32_bf16 a[28:31],a[92:95],a[236:239],a[28:31]
ds_read_b64_tr_b16 a[70:71],v50 offset:19456
v_mfma_f32_16x16x32_bf16 a[28:31],a[88:91],a[232:235],a[28:31]
s_add_u32 m0,s88,4096
v_mfma_f32_16x16x32_bf16 a[44:47],a[88:91],a[240:243],a[44:47]
ds_read_b64_tr_b16 a[72:73],v50 offset:1032
v_mfma_f32_16x16x32_bf16 a[44:47],a[92:95],a[244:247],a[44:47]
buffer_load_dwordx4 v44,s[36:39],s83 offen lds
v_mfma_f32_16x16x32_bf16 a[60:63],a[92:95],a[252:255],a[60:63]
ds_read_b64_tr_b16 a[74:75],v50 offset:3080
v_mfma_f32_16x16x32_bf16 a[60:63],a[88:91],a[248:251],a[60:63]
ds_read_b64_tr_b16 a[76:77],v50 offset:17416
v_mfma_f32_16x16x32_bf16 v[64:67],a[96:99],a[224:227],v[64:67]
ds_read_b64_tr_b16 a[78:79],v50 offset:19464
v_mfma_f32_16x16x32_bf16 v[64:67],a[100:103],a[228:231],v[64:67]
ds_read_b64_tr_b16 a[80:81],v50 offset:1280
v_mfma_f32_16x16x32_bf16 v[80:83],a[100:103],a[236:239],v[80:83]
s_add_u32 m0,s88,5120
v_mfma_f32_16x16x32_bf16 v[80:83],a[96:99],a[232:235],v[80:83]
ds_read_b64_tr_b16 a[82:83],v50 offset:3328
v_mfma_f32_16x16x32_bf16 v[96:99],a[96:99],a[240:243],v[96:99]
buffer_load_dwordx4 v45,s[36:39],s83 offen lds
v_mfma_f32_16x16x32_bf16 v[96:99],a[100:103],a[244:247],v[96:99]
ds_read_b64_tr_b16 a[84:85],v50 offset:17664
v_mfma_f32_16x16x32_bf16 v[112:115],a[100:103],a[252:255],v[112:115]
ds_read_b64_tr_b16 a[86:87],v50 offset:19712
v_mfma_f32_16x16x32_bf16 v[112:115],a[96:99],a[248:251],v[112:115]
ds_read_b64_tr_b16 a[88:89],v50 offset:1288
v_mfma_f32_16x16x32_bf16 v[68:71],a[104:107],a[224:227],v[68:71]
s_waitcnt lgkmcnt(5)
ds_read_b64_tr_b16 a[90:91],v50 offset:3336
v_mfma_f32_16x16x32_bf16 v[68:71],a[108:111],a[228:231],v[68:71]
s_add_u32 m0,s88,6144
v_mfma_f32_16x16x32_bf16 v[84:87],a[108:111],a[236:239],v[84:87]
ds_read_b64_tr_b16 a[92:93],v50 offset:17672
v_mfma_f32_16x16x32_bf16 v[84:87],a[104:107],a[232:235],v[84:87]
buffer_load_dwordx4 v46,s[36:39],s83 offen lds
v_mfma_f32_16x16x32_bf16 v[100:103],a[104:107],a[240:243],v[100:103]
ds_read_b64_tr_b16 a[94:95],v50 offset:19720
v_mfma_f32_16x16x32_bf16 v[100:103],a[108:111],a[244:247],v[100:103]
ds_read_b64_tr_b16 a[96:97],v50 offset:1536
v_mfma_f32_16x16x32_bf16 v[116:119],a[108:111],a[252:255],v[116:119]
ds_read_b64_tr_b16 a[98:99],v50 offset:3584
v_mfma_f32_16x16x32_bf16 v[116:119],a[104:107],a[248:251],v[116:119]
ds_read_b64_tr_b16 a[100:101],v50 offset:17920
v_mfma_f32_16x16x32_bf16 v[72:75],a[112:115],a[224:227],v[72:75]
s_add_u32 m0,s88,7168
s_mov_b32 s88,s69
s_mov_b32 s69,s86
v_mfma_f32_16x16x32_bf16 v[72:75],a[116:119],a[228:231],v[72:75]
ds_read_b64_tr_b16 a[102:103],v50 offset:19968
v_mfma_f32_16x16x32_bf16 v[88:91],a[116:119],a[236:239],v[88:91]
buffer_load_dwordx4 v47,s[36:39],s83 offen lds
v_mfma_f32_16x16x32_bf16 v[88:91],a[112:115],a[232:235],v[88:91]
s_add_u32 s83,s83,524288
ds_read_b64_tr_b16 a[104:105],v50 offset:1544
v_mfma_f32_16x16x32_bf16 v[104:107],a[112:115],a[240:243],v[104:107]
ds_read_b64_tr_b16 a[106:107],v50 offset:3592
v_mfma_f32_16x16x32_bf16 v[104:107],a[116:119],a[244:247],v[104:107]
ds_read_b64_tr_b16 a[108:109],v50 offset:17928
v_mfma_f32_16x16x32_bf16 v[120:123],a[116:119],a[252:255],v[120:123]
ds_read_b64_tr_b16 a[110:111],v50 offset:19976
v_mfma_f32_16x16x32_bf16 v[120:123],a[112:115],a[248:251],v[120:123]
s_add_u32 m0,s85,0
v_mfma_f32_16x16x32_bf16 v[76:79],a[120:123],a[224:227],v[76:79]
ds_read_b64_tr_b16 a[112:113],v50 offset:1792
v_mfma_f32_16x16x32_bf16 v[76:79],a[124:127],a[228:231],v[76:79]
buffer_load_dwordx4 v32,s[28:31],s84 offen lds
v_mfma_f32_16x16x32_bf16 v[92:95],a[124:127],a[236:239],v[92:95]
ds_read_b64_tr_b16 a[114:115],v50 offset:3840
v_mfma_f32_16x16x32_bf16 v[92:95],a[120:123],a[232:235],v[92:95]
ds_read_b64_tr_b16 a[116:117],v50 offset:18176
v_mfma_f32_16x16x32_bf16 v[108:111],a[120:123],a[240:243],v[108:111]
ds_read_b64_tr_b16 a[118:119],v50 offset:20224
v_mfma_f32_16x16x32_bf16 v[108:111],a[124:127],a[244:247],v[108:111]
s_cmp_eq_u32 s70,1
s_cselect_b32 s65,256,0
s_cselect_b32 s66,524288,0
v_mfma_f32_16x16x32_bf16 v[124:127],a[124:127],a[252:255],v[124:127]
ds_read_b64_tr_b16 a[124:125],v50 offset:18184
v_add_u32 v40,s65,v40
v_mfma_f32_16x16x32_bf16 v[124:127],a[120:123],a[248:251],v[124:127]
ds_read_b64_tr_b16 a[120:121],v50 offset:1800
v_add_u32 v41,s66,v41
v_mfma_f32_16x16x32_bf16 v[128:131],a[64:67],a[224:227],v[128:131]
buffer_load_dwordx4 v33,s[28:31],s84 offen offset:1024 lds
v_mfma_f32_16x16x32_bf16 v[128:131],a[68:71],a[228:231],v[128:131]
ds_read_b64_tr_b16 a[122:123],v50 offset:3848
v_add_u32 v42,s65,v42
v_mfma_f32_16x16x32_bf16 v[160:163],a[68:71],a[236:239],v[160:163]
ds_read_b64_tr_b16 a[126:127],v50 offset:20232
v_add_u32 v50,s86,v51
v_mfma_f32_16x16x32_bf16 v[160:163],a[64:67],a[232:235],v[160:163]
s_mov_b32 s86,s87
v_add_u32 v43,s66,v43
v_add_u32 v44,s65,v44
v_mfma_f32_16x16x32_bf16 v[192:195],a[64:67],a[240:243],v[192:195]
s_mov_b32 s87,s88
v_add_u32 v45,s66,v45
v_add_u32 v46,s65,v46
v_mfma_f32_16x16x32_bf16 v[192:195],a[68:71],a[244:247],v[192:195]
v_add_u32 v47,s66,v47
v_mfma_f32_16x16x32_bf16 v[224:227],a[68:71],a[252:255],v[224:227]
v_mfma_f32_16x16x32_bf16 v[224:227],a[64:67],a[248:251],v[224:227]
buffer_load_dwordx4 v34,s[28:31],s84 offen offset:2048 lds
v_mfma_f32_16x16x32_bf16 v[132:135],a[72:75],a[224:227],v[132:135]
v_mfma_f32_16x16x32_bf16 v[132:135],a[76:79],a[228:231],v[132:135]
v_mfma_f32_16x16x32_bf16 v[164:167],a[76:79],a[236:239],v[164:167]
v_mfma_f32_16x16x32_bf16 v[164:167],a[72:75],a[232:235],v[164:167]
v_mfma_f32_16x16x32_bf16 v[196:199],a[72:75],a[240:243],v[196:199]
v_mfma_f32_16x16x32_bf16 v[196:199],a[76:79],a[244:247],v[196:199]
v_mfma_f32_16x16x32_bf16 v[228:231],a[76:79],a[252:255],v[228:231]
buffer_load_dwordx4 v35,s[28:31],s84 offen offset:3072 lds
v_mfma_f32_16x16x32_bf16 v[228:231],a[72:75],a[248:251],v[228:231]
s_waitcnt lgkmcnt(0)
v_mfma_f32_16x16x32_bf16 v[136:139],a[80:83],a[224:227],v[136:139]
v_mfma_f32_16x16x32_bf16 v[136:139],a[84:87],a[228:231],v[136:139]
v_mfma_f32_16x16x32_bf16 v[168:171],a[84:87],a[236:239],v[168:171]
v_mfma_f32_16x16x32_bf16 v[168:171],a[80:83],a[232:235],v[168:171]
s_add_u32 m0,s85,4096
s_xor_b32 s85,s85,98304
v_mfma_f32_16x16x32_bf16 v[200:203],a[80:83],a[240:243],v[200:203]
v_mfma_f32_16x16x32_bf16 v[200:203],a[84:87],a[244:247],v[200:203]
buffer_load_dwordx4 v36,s[28:31],s84 offen lds
v_mfma_f32_16x16x32_bf16 v[232:235],a[84:87],a[252:255],v[232:235]
v_mfma_f32_16x16x32_bf16 v[232:235],a[80:83],a[248:251],v[232:235]
v_mfma_f32_16x16x32_bf16 v[140:143],a[88:91],a[224:227],v[140:143]
v_mfma_f32_16x16x32_bf16 v[140:143],a[92:95],a[228:231],v[140:143]
v_mfma_f32_16x16x32_bf16 v[172:175],a[92:95],a[236:239],v[172:175]
v_mfma_f32_16x16x32_bf16 v[172:175],a[88:91],a[232:235],v[172:175]
v_mfma_f32_16x16x32_bf16 v[204:207],a[88:91],a[240:243],v[204:207]
buffer_load_dwordx4 v37,s[28:31],s84 offen offset:1024 lds
v_mfma_f32_16x16x32_bf16 v[204:207],a[92:95],a[244:247],v[204:207]
v_mfma_f32_16x16x32_bf16 v[236:239],a[92:95],a[252:255],v[236:239]
v_mfma_f32_16x16x32_bf16 v[236:239],a[88:91],a[248:251],v[236:239]
s_waitcnt vmcnt(23)
s_barrier
v_mfma_f32_16x16x32_bf16 v[144:147],a[96:99],a[224:227],v[144:147]
s_waitcnt vmcnt(15)
ds_read_b128 a[192:195],v48
v_mfma_f32_16x16x32_bf16 v[144:147],a[100:103],a[228:231],v[144:147]
ds_read_b128 a[196:199],v49
v_mfma_f32_16x16x32_bf16 v[176:179],a[100:103],a[236:239],v[176:179]
buffer_load_dwordx4 v38,s[28:31],s84 offen offset:2048 lds
v_mfma_f32_16x16x32_bf16 v[176:179],a[96:99],a[232:235],v[176:179]
ds_read_b128 a[200:203],v48 offset:2048
v_mfma_f32_16x16x32_bf16 v[208:211],a[96:99],a[240:243],v[208:211]
ds_read_b128 a[204:207],v49 offset:2048
v_mfma_f32_16x16x32_bf16 v[208:211],a[100:103],a[244:247],v[208:211]
ds_read_b128 a[208:211],v48 offset:4096
v_mfma_f32_16x16x32_bf16 v[240:243],a[100:103],a[252:255],v[240:243]
ds_read_b128 a[212:215],v49 offset:4096
v_mfma_f32_16x16x32_bf16 v[240:243],a[96:99],a[248:251],v[240:243]
v_mfma_f32_16x16x32_bf16 v[148:151],a[104:107],a[224:227],v[148:151]
ds_read_b128 a[216:219],v48 offset:6144
v_xor_b32 v48,98304,v48
v_mfma_f32_16x16x32_bf16 v[148:151],a[108:111],a[228:231],v[148:151]
buffer_load_dwordx4 v39,s[28:31],s84 offen offset:3072 lds
v_mfma_f32_16x16x32_bf16 v[180:183],a[108:111],a[236:239],v[180:183]
s_add_u32 s84,s84,128
ds_read_b128 a[220:223],v49 offset:6144
v_xor_b32 v49,98304,v49
v_mfma_f32_16x16x32_bf16 v[180:183],a[104:107],a[232:235],v[180:183]
ds_read_b64_tr_b16 a[64:65],v50
v_mfma_f32_16x16x32_bf16 v[212:215],a[104:107],a[240:243],v[212:215]
ds_read_b64_tr_b16 a[66:67],v50 offset:2048
v_mfma_f32_16x16x32_bf16 v[212:215],a[108:111],a[244:247],v[212:215]
ds_read_b64_tr_b16 a[68:69],v50 offset:16384
v_mfma_f32_16x16x32_bf16 v[244:247],a[108:111],a[252:255],v[244:247]
v_mfma_f32_16x16x32_bf16 v[244:247],a[104:107],a[248:251],v[244:247]
ds_read_b64_tr_b16 a[70:71],v50 offset:18432
)A"
R"A(v_mfma_f32_16x16x32_bf16 v[152:155],a[112:115],a[224:227],v[152:155]
buffer_load_dword v9,v19,s[48:51],s90 offen
v_mfma_f32_16x16x32_bf16 v[152:155],a[116:119],a[228:231],v[152:155]
s_add_u32 s90,s90,896
ds_read_b64_tr_b16 a[72:73],v50 offset:8
v_mfma_f32_16x16x32_bf16 v[184:187],a[116:119],a[236:239],v[184:187]
ds_read_b64_tr_b16 a[74:75],v50 offset:2056
v_mfma_f32_16x16x32_bf16 v[184:187],a[112:115],a[232:235],v[184:187]
ds_read_b64_tr_b16 a[76:77],v50 offset:16392
v_mfma_f32_16x16x32_bf16 v[216:219],a[112:115],a[240:243],v[216:219]
ds_read_b64_tr_b16 a[78:79],v50 offset:18440
v_mfma_f32_16x16x32_bf16 v[216:219],a[116:119],a[244:247],v[216:219]
v_mfma_f32_16x16x32_bf16 v[248:251],a[116:119],a[252:255],v[248:251]
ds_read_b64_tr_b16 a[80:81],v50 offset:256
v_mfma_f32_16x16x32_bf16 v[248:251],a[112:115],a[248:251],v[248:251]
ds_read_b64_tr_b16 a[82:83],v50 offset:2304
v_mfma_f32_16x16x32_bf16 v[156:159],a[120:123],a[224:227],v[156:159]
ds_read_b64_tr_b16 a[84:85],v50 offset:16640
v_mfma_f32_16x16x32_bf16 v[156:159],a[124:127],a[228:231],v[156:159]
v_mfma_f32_16x16x32_bf16 v[188:191],a[124:127],a[236:239],v[188:191]
ds_read_b64_tr_b16 a[86:87],v50 offset:18688
v_mfma_f32_16x16x32_bf16 v[188:191],a[120:123],a[232:235],v[188:191]
ds_read_b64_tr_b16 a[88:89],v50 offset:264
v_mfma_f32_16x16x32_bf16 v[220:223],a[120:123],a[240:243],v[220:223]
ds_read_b64_tr_b16 a[90:91],v50 offset:2312
v_mfma_f32_16x16x32_bf16 v[220:223],a[124:127],a[244:247],v[220:223]
v_mfma_f32_16x16x32_bf16 v[252:255],a[124:127],a[252:255],v[252:255]
s_waitcnt lgkmcnt(4)
ds_read_b64_tr_b16 a[92:93],v50 offset:16648
v_mfma_f32_16x16x32_bf16 v[252:255],a[120:123],a[248:251],v[252:255]
s_add_u32 m0,s88,0
ds_read_b64_tr_b16 a[94:95],v50 offset:18696
v_mfma_f32_16x16x32_bf16 a[0:3],a[64:67],a[192:195],a[0:3]
ds_read_b64_tr_b16 a[96:97],v50 offset:512
v_mfma_f32_16x16x32_bf16 a[0:3],a[68:71],a[196:199],a[0:3]
buffer_load_dwordx4 v40,s[36:39],s83 offen lds
v_mfma_f32_16x16x32_bf16 a[16:19],a[68:71],a[204:207],a[16:19]
ds_read_b64_tr_b16 a[98:99],v50 offset:2560
v_mfma_f32_16x16x32_bf16 a[16:19],a[64:67],a[200:203],a[16:19]
ds_read_b64_tr_b16 a[100:101],v50 offset:16896
v_mfma_f32_16x16x32_bf16 a[32:35],a[64:67],a[208:211],a[32:35]
ds_read_b64_tr_b16 a[102:103],v50 offset:18944
v_mfma_f32_16x16x32_bf16 a[32:35],a[68:71],a[212:215],a[32:35]
v_mfma_f32_16x16x32_bf16 a[48:51],a[68:71],a[220:223],a[48:51]
s_add_u32 m0,s88,1024
ds_read_b64_tr_b16 a[104:105],v50 offset:520
v_mfma_f32_16x16x32_bf16 a[48:51],a[64:67],a[216:219],a[48:51]
ds_read_b64_tr_b16 a[106:107],v50 offset:2568
v_mfma_f32_16x16x32_bf16 a[4:7],a[72:75],a[192:195],a[4:7]
buffer_load_dwordx4 v41,s[36:39],s83 offen lds
v_mfma_f32_16x16x32_bf16 a[4:7],a[76:79],a[196:199],a[4:7]
ds_read_b64_tr_b16 a[108:109],v50 offset:16904
v_mfma_f32_16x16x32_bf16 a[20:23],a[76:79],a[204:207],a[20:23]
ds_read_b64_tr_b16 a[110:111],v50 offset:18952
v_mfma_f32_16x16x32_bf16 a[20:23],a[72:75],a[200:203],a[20:23]
ds_read_b64_tr_b16 a[112:113],v50 offset:768
v_mfma_f32_16x16x32_bf16 a[36:39],a[72:75],a[208:211],a[36:39]
ds_read_b64_tr_b16 a[114:115],v50 offset:2816
v_mfma_f32_16x16x32_bf16 a[36:39],a[76:79],a[212:215],a[36:39]
s_add_u32 m0,s88,2048
v_mfma_f32_16x16x32_bf16 a[52:55],a[76:79],a[220:223],a[52:55]
ds_read_b64_tr_b16 a[116:117],v50 offset:17152
v_mfma_f32_16x16x32_bf16 a[52:55],a[72:75],a[216:219],a[52:55]
buffer_load_dwordx4 v42,s[36:39],s83 offen lds
v_mfma_f32_16x16x32_bf16 a[8:11],a[80:83],a[192:195],a[8:11]
s_waitcnt lgkmcnt(5)
ds_read_b64_tr_b16 a[118:119],v50 offset:19200
v_mfma_f32_16x16x32_bf16 a[8:11],a[84:87],a[196:199],a[8:11]
ds_read_b64_tr_b16 a[120:121],v50 offset:776
v_mfma_f32_16x16x32_bf16 a[24:27],a[84:87],a[204:207],a[24:27]
ds_read_b64_tr_b16 a[122:123],v50 offset:2824
v_mfma_f32_16x16x32_bf16 a[24:27],a[80:83],a[200:203],a[24:27]
ds_read_b64_tr_b16 a[124:125],v50 offset:17160
v_mfma_f32_16x16x32_bf16 a[40:43],a[80:83],a[208:211],a[40:43]
s_add_u32 m0,s88,3072
v_mfma_f32_16x16x32_bf16 a[40:43],a[84:87],a[212:215],a[40:43]
ds_read_b64_tr_b16 a[126:127],v50 offset:19208
v_mfma_f32_16x16x32_bf16 a[56:59],a[84:87],a[220:223],a[56:59]
buffer_load_dwordx4 v43,s[36:39],s83 offen lds
v_mfma_f32_16x16x32_bf16 a[56:59],a[80:83],a[216:219],a[56:59]
ds_read_b64_tr_b16 a[64:65],v50 offset:1024
v_mfma_f32_16x16x32_bf16 a[12:15],a[88:91],a[192:195],a[12:15]
ds_read_b64_tr_b16 a[66:67],v50 offset:3072
v_mfma_f32_16x16x32_bf16 a[12:15],a[92:95],a[196:199],a[12:15]
ds_read_b64_tr_b16 a[68:69],v50 offset:17408
v_mfma_f32_16x16x32_bf16 a[28:31],a[92:95],a[204:207],a[28:31]
ds_read_b64_tr_b16 a[70:71],v50 offset:19456
v_mfma_f32_16x16x32_bf16 a[28:31],a[88:91],a[200:203],a[28:31]
s_add_u32 m0,s88,4096
v_mfma_f32_16x16x32_bf16 a[44:47],a[88:91],a[208:211],a[44:47]
ds_read_b64_tr_b16 a[72:73],v50 offset:1032
v_mfma_f32_16x16x32_bf16 a[44:47],a[92:95],a[212:215],a[44:47]
buffer_load_dwordx4 v44,s[36:39],s83 offen lds
v_mfma_f32_16x16x32_bf16 a[60:63],a[92:95],a[220:223],a[60:63]
ds_read_b64_tr_b16 a[74:75],v50 offset:3080
v_mfma_f32_16x16x32_bf16 a[60:63],a[88:91],a[216:219],a[60:63]
ds_read_b64_tr_b16 a[76:77],v50 offset:17416
v_mfma_f32_16x16x32_bf16 v[64:67],a[96:99],a[192:195],v[64:67]
ds_read_b64_tr_b16 a[78:79],v50 offset:19464
v_mfma_f32_16x16x32_bf16 v[64:67],a[100:103],a[196:199],v[64:67]
ds_read_b64_tr_b16 a[80:81],v50 offset:1280
v_mfma_f32_16x16x32_bf16 v[80:83],a[100:103],a[204:207],v[80:83]
s_add_u32 m0,s88,5120
v_mfma_f32_16x16x32_bf16 v[80:83],a[96:99],a[200:203],v[80:83]
ds_read_b64_tr_b16 a[82:83],v50 offset:3328
v_mfma_f32_16x16x32_bf16 v[96:99],a[96:99],a[208:211],v[96:99]
buffer_load_dwordx4 v45,s[36:39],s83 offen lds
v_mfma_f32_16x16x32_bf16 v[96:99],a[100:103],a[212:215],v[96:99]
ds_read_b64_tr_b16 a[84:85],v50 offset:17664
v_mfma_f32_16x16x32_bf16 v[112:115],a[100:103],a[220:223],v[112:115]
ds_read_b64_tr_b16 a[86:87],v50 offset:19712
v_mfma_f32_16x16x32_bf16 v[112:115],a[96:99],a[216:219],v[112:115]
ds_read_b64_tr_b16 a[88:89],v50 offset:1288
v_mfma_f32_16x16x32_bf16 v[68:71],a[104:107],a[192:195],v[68:71]
s_waitcnt lgkmcnt(5)
ds_read_b64_tr_b16 a[90:91],v50 offset:3336
v_mfma_f32_16x16x32_bf16 v[68:71],a[108:111],a[196:199],v[68:71]
s_add_u32 m0,s88,6144
v_mfma_f32_16x16x32_bf16 v[84:87],a[108:111],a[204:207],v[84:87]
ds_read_b64_tr_b16 a[92:93],v50 offset:17672
v_mfma_f32_16x16x32_bf16 v[84:87],a[104:107],a[200:203],v[84:87]
buffer_load_dwordx4 v46,s[36:39],s83 offen lds
v_mfma_f32_16x16x32_bf16 v[100:103],a[104:107],a[208:211],v[100:103]
ds_read_b64_tr_b16 a[94:95],v50 offset:19720
v_mfma_f32_16x16x32_bf16 v[100:103],a[108:111],a[212:215],v[100:103]
ds_read_b64_tr_b16 a[96:97],v50 offset:1536
v_mfma_f32_16x16x32_bf16 v[116:119],a[108:111],a[220:223],v[116:119]
ds_read_b64_tr_b16 a[98:99],v50 offset:3584
v_mfma_f32_16x16x32_bf16 v[116:119],a[104:107],a[216:219],v[116:119]
ds_read_b64_tr_b16 a[100:101],v50 offset:17920
v_mfma_f32_16x16x32_bf16 v[72:75],a[112:115],a[192:195],v[72:75]
s_add_u32 m0,s88,7168
s_mov_b32 s88,s69
v_mfma_f32_16x16x32_bf16 v[72:75],a[116:119],a[196:199],v[72:75]
ds_read_b64_tr_b16 a[102:103],v50 offset:19968
v_mfma_f32_16x16x32_bf16 v[88:91],a[116:119],a[204:207],v[88:91]
buffer_load_dwordx4 v47,s[36:39],s83 offen lds
v_mfma_f32_16x16x32_bf16 v[88:91],a[112:115],a[200:203],v[88:91]
s_add_u32 s83,s83,524288
ds_read_b64_tr_b16 a[104:105],v50 offset:1544
v_mfma_f32_16x16x32_bf16 v[104:107],a[112:115],a[208:211],v[104:107]
ds_read_b64_tr_b16 a[106:107],v50 offset:3592
v_mfma_f32_16x16x32_bf16 v[104:107],a[116:119],a[212:215],v[104:107]
ds_read_b64_tr_b16 a[108:109],v50 offset:17928
v_mfma_f32_16x16x32_bf16 v[120:123],a[116:119],a[220:223],v[120:123]
ds_read_b64_tr_b16 a[110:111],v50 offset:19976
v_mfma_f32_16x16x32_bf16 v[120:123],a[112:115],a[216:219],v[120:123]
s_add_u32 m0,s85,0
v_mfma_f32_16x16x32_bf16 v[76:79],a[120:123],a[192:195],v[76:79]
ds_read_b64_tr_b16 a[112:113],v50 offset:1792
v_mfma_f32_16x16x32_bf16 v[76:79],a[124:127],a[196:199],v[76:79]
buffer_load_dwordx4 v32,s[28:31],s84 offen lds
v_mfma_f32_16x16x32_bf16 v[92:95],a[124:127],a[204:207],v[92:95]
ds_read_b64_tr_b16 a[114:115],v50 offset:3840
v_mfma_f32_16x16x32_bf16 v[92:95],a[120:123],a[200:203],v[92:95]
ds_read_b64_tr_b16 a[116:117],v50 offset:18176
v_mfma_f32_16x16x32_bf16 v[108:111],a[120:123],a[208:211],v[108:111]
ds_read_b64_tr_b16 a[118:119],v50 offset:20224
v_mfma_f32_16x16x32_bf16 v[108:111],a[124:127],a[212:215],v[108:111]
v_mfma_f32_16x16x32_bf16 v[124:127],a[124:127],a[220:223],v[124:127]
ds_read_b64_tr_b16 a[124:125],v50 offset:18184
v_mfma_f32_16x16x32_bf16 v[124:127],a[120:123],a[216:219],v[124:127]
ds_read_b64_tr_b16 a[120:121],v50 offset:1800
v_mfma_f32_16x16x32_bf16 v[128:131],a[64:67],a[192:195],v[128:131]
buffer_load_dwordx4 v33,s[28:31],s84 offen offset:1024 lds
v_mfma_f32_16x16x32_bf16 v[128:131],a[68:71],a[196:199],v[128:131]
ds_read_b64_tr_b16 a[122:123],v50 offset:3848
v_mfma_f32_16x16x32_bf16 v[160:163],a[68:71],a[204:207],v[160:163]
ds_read_b64_tr_b16 a[126:127],v50 offset:20232
v_add_u32 v50,s86,v51
v_mfma_f32_16x16x32_bf16 v[160:163],a[64:67],a[200:203],v[160:163]
v_mfma_f32_16x16x32_bf16 v[192:195],a[64:67],a[208:211],v[192:195]
v_mfma_f32_16x16x32_bf16 v[192:195],a[68:71],a[212:215],v[192:195]
v_mfma_f32_16x16x32_bf16 v[224:227],a[68:71],a[220:223],v[224:227]
v_mfma_f32_16x16x32_bf16 v[224:227],a[64:67],a[216:219],v[224:227]
buffer_load_dwordx4 v34,s[28:31],s84 offen offset:2048 lds
v_mfma_f32_16x16x32_bf16 v[132:135],a[72:75],a[192:195],v[132:135]
v_mfma_f32_16x16x32_bf16 v[132:135],a[76:79],a[196:199],v[132:135]
v_mfma_f32_16x16x32_bf16 v[164:167],a[76:79],a[204:207],v[164:167]
v_mfma_f32_16x16x32_bf16 v[164:167],a[72:75],a[200:203],v[164:167]
v_mfma_f32_16x16x32_bf16 v[196:199],a[72:75],a[208:211],v[196:199]
v_mfma_f32_16x16x32_bf16 v[196:199],a[76:79],a[212:215],v[196:199]
v_mfma_f32_16x16x32_bf16 v[228:231],a[76:79],a[220:223],v[228:231]
buffer_load_dwordx4 v35,s[28:31],s84 offen offset:3072 lds
v_mfma_f32_16x16x32_bf16 v[228:231],a[72:75],a[216:219],v[228:231]
s_waitcnt lgkmcnt(0)
v_mfma_f32_16x16x32_bf16 v[136:139],a[80:83],a[192:195],v[136:139]
v_mfma_f32_16x16x32_bf16 v[136:139],a[84:87],a[196:199],v[136:139]
v_mfma_f32_16x16x32_bf16 v[168:171],a[84:87],a[204:207],v[168:171]
v_mfma_f32_16x16x32_bf16 v[168:171],a[80:83],a[200:203],v[168:171]
s_add_u32 m0,s85,4096
s_xor_b32 s85,s85,98304
v_mfma_f32_16x16x32_bf16 v[200:203],a[80:83],a[208:211],v[200:203]
v_mfma_f32_16x16x32_bf16 v[200:203],a[84:87],a[212:215],v[200:203]
buffer_load_dwordx4 v36,s[28:31],s84 offen lds
v_mfma_f32_16x16x32_bf16 v[232:235],a[84:87],a[220:223],v[232:235]
v_mfma_f32_16x16x32_bf16 v[232:235],a[80:83],a[216:219],v[232:235]
v_mfma_f32_16x16x32_bf16 v[140:143],a[88:91],a[192:195],v[140:143]
v_mfma_f32_16x16x32_bf16 v[140:143],a[92:95],a[196:199],v[140:143]
v_mfma_f32_16x16x32_bf16 v[172:175],a[92:95],a[204:207],v[172:175]
v_mfma_f32_16x16x32_bf16 v[172:175],a[88:91],a[200:203],v[172:175]
v_mfma_f32_16x16x32_bf16 v[204:207],a[88:91],a[208:211],v[204:207]
buffer_load_dwordx4 v37,s[28:31],s84 offen offset:1024 lds
v_mfma_f32_16x16x32_bf16 v[204:207],a[92:95],a[212:215],v[204:207]
v_mfma_f32_16x16x32_bf16 v[236:239],a[92:95],a[220:223],v[236:239]
v_mfma_f32_16x16x32_bf16 v[236:239],a[88:91],a[216:219],v[236:239]
s_waitcnt vmcnt(23)
s_barrier
v_mfma_f32_16x16x32_bf16 v[144:147],a[96:99],a[192:195],v[144:147]
s_waitcnt vmcnt(15)
ds_read_b128 a[224:227],v48
v_mfma_f32_16x16x32_bf16 v[144:147],a[100:103],a[196:199],v[144:147]
ds_read_b128 a[228:231],v49
v_mfma_f32_16x16x32_bf16 v[176:179],a[100:103],a[204:207],v[176:179]
buffer_load_dwordx4 v38,s[28:31],s84 offen offset:2048 lds
v_mfma_f32_16x16x32_bf16 v[176:179],a[96:99],a[200:203],v[176:179]
ds_read_b128 a[232:235],v48 offset:2048
v_mfma_f32_16x16x32_bf16 v[208:211],a[96:99],a[208:211],v[208:211]
ds_read_b128 a[236:239],v49 offset:2048
v_mfma_f32_16x16x32_bf16 v[208:211],a[100:103],a[212:215],v[208:211]
ds_read_b128 a[240:243],v48 offset:4096
v_mfma_f32_16x16x32_bf16 v[240:243],a[100:103],a[220:223],v[240:243]
)A"
R"A(ds_read_b128 a[244:247],v49 offset:4096
v_mfma_f32_16x16x32_bf16 v[240:243],a[96:99],a[216:219],v[240:243]
v_mfma_f32_16x16x32_bf16 v[148:151],a[104:107],a[192:195],v[148:151]
ds_read_b128 a[248:251],v48 offset:6144
v_mfma_f32_16x16x32_bf16 v[148:151],a[108:111],a[196:199],v[148:151]
buffer_load_dwordx4 v39,s[28:31],s84 offen offset:3072 lds
v_mfma_f32_16x16x32_bf16 v[180:183],a[108:111],a[204:207],v[180:183]
s_add_u32 s84,s84,128
ds_read_b128 a[252:255],v49 offset:6144
v_mfma_f32_16x16x32_bf16 v[180:183],a[104:107],a[200:203],v[180:183]
ds_read_b64_tr_b16 a[64:65],v50
v_mfma_f32_16x16x32_bf16 v[212:215],a[104:107],a[208:211],v[212:215]
ds_read_b64_tr_b16 a[66:67],v50 offset:2048
v_mfma_f32_16x16x32_bf16 v[212:215],a[108:111],a[212:215],v[212:215]
s_nop 0
L_pd2_%=:
s_cbranch_execz L_pd6_%=
ds_read_b64_tr_b16 a[68:69],v50 offset:16384
v_mfma_f32_16x16x32_bf16 v[244:247],a[108:111],a[220:223],v[244:247]
v_mfma_f32_16x16x32_bf16 v[244:247],a[104:107],a[216:219],v[244:247]
ds_read_b64_tr_b16 a[70:71],v50 offset:18432
v_mfma_f32_16x16x32_bf16 v[152:155],a[112:115],a[192:195],v[152:155]
buffer_load_dword v8,v19,s[48:51],s90 offen
v_mfma_f32_16x16x32_bf16 v[152:155],a[116:119],a[196:199],v[152:155]
s_add_u32 s90,s90,896
ds_read_b64_tr_b16 a[72:73],v50 offset:8
v_mfma_f32_16x16x32_bf16 v[184:187],a[116:119],a[204:207],v[184:187]
ds_read_b64_tr_b16 a[74:75],v50 offset:2056
v_mfma_f32_16x16x32_bf16 v[184:187],a[112:115],a[200:203],v[184:187]
ds_read_b64_tr_b16 a[76:77],v50 offset:16392
v_mfma_f32_16x16x32_bf16 v[216:219],a[112:115],a[208:211],v[216:219]
ds_read_b64_tr_b16 a[78:79],v50 offset:18440
v_mfma_f32_16x16x32_bf16 v[216:219],a[116:119],a[212:215],v[216:219]
v_mfma_f32_16x16x32_bf16 v[248:251],a[116:119],a[220:223],v[248:251]
ds_read_b64_tr_b16 a[80:81],v50 offset:256
v_mfma_f32_16x16x32_bf16 v[248:251],a[112:115],a[216:219],v[248:251]
ds_read_b64_tr_b16 a[82:83],v50 offset:2304
v_mfma_f32_16x16x32_bf16 v[156:159],a[120:123],a[192:195],v[156:159]
ds_read_b64_tr_b16 a[84:85],v50 offset:16640
v_mfma_f32_16x16x32_bf16 v[156:159],a[124:127],a[196:199],v[156:159]
v_mfma_f32_16x16x32_bf16 v[188:191],a[124:127],a[204:207],v[188:191]
ds_read_b64_tr_b16 a[86:87],v50 offset:18688
v_mfma_f32_16x16x32_bf16 v[188:191],a[120:123],a[200:203],v[188:191]
ds_read_b64_tr_b16 a[88:89],v50 offset:264
v_mfma_f32_16x16x32_bf16 v[220:223],a[120:123],a[208:211],v[220:223]
ds_read_b64_tr_b16 a[90:91],v50 offset:2312
v_mfma_f32_16x16x32_bf16 v[220:223],a[124:127],a[212:215],v[220:223]
v_mfma_f32_16x16x32_bf16 v[252:255],a[124:127],a[220:223],v[252:255]
ds_read_b64_tr_b16 a[92:93],v50 offset:16648
v_mfma_f32_16x16x32_bf16 v[252:255],a[120:123],a[216:219],v[252:255]
ds_read_b64_tr_b16 a[94:95],v50 offset:18696
s_sub_u32 s70,s70,1
s_cmp_lg_u32 s70,0
s_cbranch_scc1 L_qp_%=
s_waitcnt lgkmcnt(6)
v_mfma_f32_16x16x32_bf16 a[0:3],a[64:67],a[224:227],a[0:3]
s_memrealtime s[92:93]
s_memtime s[56:57]
s_add_u32 m0,s85,0
v_mfma_f32_16x16x32_bf16 a[0:3],a[68:71],a[228:231],a[0:3]
buffer_load_dwordx4 v32,s[28:31],s84 offen lds
v_mfma_f32_16x16x32_bf16 a[16:19],a[68:71],a[236:239],a[16:19]
s_sub_u32 s83,s83,524288
ds_read_b64_tr_b16 a[96:97],v50 offset:512
v_subrev_u32 v40,256,v40
v_mfma_f32_16x16x32_bf16 a[16:19],a[64:67],a[232:235],a[16:19]
s_mov_b32 s69,s86
ds_read_b64_tr_b16 a[98:99],v50 offset:2560
v_subrev_u32 v41,256,v41
v_mfma_f32_16x16x32_bf16 a[32:35],a[64:67],a[240:243],a[32:35]
s_mov_b32 s86,s87
ds_read_b64_tr_b16 a[100:101],v50 offset:16896
v_subrev_u32 v42,256,v42
v_mfma_f32_16x16x32_bf16 a[32:35],a[68:71],a[244:247],a[32:35]
s_mov_b32 s87,s88
ds_read_b64_tr_b16 a[102:103],v50 offset:18944
v_subrev_u32 v43,256,v43
v_mfma_f32_16x16x32_bf16 a[48:51],a[68:71],a[252:255],a[48:51]
s_add_u32 s75,s75,16384
v_subrev_u32 v44,256,v44
v_subrev_u32 v45,256,v45
v_mfma_f32_16x16x32_bf16 a[48:51],a[64:67],a[248:251],a[48:51]
buffer_load_dwordx4 v33,s[28:31],s84 offen offset:1024 lds
v_mfma_f32_16x16x32_bf16 a[4:7],a[72:75],a[224:227],a[4:7]
s_sub_u32 s78,s77,32768
ds_read_b64_tr_b16 a[104:105],v50 offset:520
v_subrev_u32 v46,256,v46
v_mfma_f32_16x16x32_bf16 a[4:7],a[76:79],a[228:231],a[4:7]
s_and_b32 s78,s78,0x1ffff
ds_read_b64_tr_b16 a[106:107],v50 offset:2568
v_subrev_u32 v47,256,v47
v_mfma_f32_16x16x32_bf16 a[20:23],a[76:79],a[236:239],a[20:23]
s_sub_u32 s78,s78,s77
ds_read_b64_tr_b16 a[108:109],v50 offset:16904
v_xor_b32 v48,98304,v48
v_mfma_f32_16x16x32_bf16 a[20:23],a[72:75],a[232:235],a[20:23]
s_mov_b32 s65,262144
ds_read_b64_tr_b16 a[110:111],v50 offset:18952
v_xor_b32 v49,98304,v49
v_mfma_f32_16x16x32_bf16 a[36:39],a[72:75],a[240:243],a[36:39]
s_add_u32 s77,s77,s78
s_mov_b32 s6,0
v_add_u32 v52,s87,v51
v_mfma_f32_16x16x32_bf16 a[36:39],a[76:79],a[244:247],a[36:39]
buffer_load_dwordx4 v34,s[28:31],s84 offen offset:2048 lds
v_mfma_f32_16x16x32_bf16 a[52:55],a[76:79],a[252:255],a[52:55]
ds_read_b64_tr_b16 a[112:113],v50 offset:768
v_mfma_f32_16x16x32_bf16 a[52:55],a[72:75],a[248:251],a[52:55]
ds_read_b64_tr_b16 a[114:115],v50 offset:2816
v_mfma_f32_16x16x32_bf16 a[8:11],a[80:83],a[224:227],a[8:11]
s_waitcnt lgkmcnt(5)
ds_read_b64_tr_b16 a[116:117],v50 offset:17152
v_mfma_f32_16x16x32_bf16 a[8:11],a[84:87],a[228:231],a[8:11]
ds_read_b64_tr_b16 a[118:119],v50 offset:19200
v_mfma_f32_16x16x32_bf16 a[24:27],a[84:87],a[236:239],a[24:27]
v_mfma_f32_16x16x32_bf16 a[24:27],a[80:83],a[232:235],a[24:27]
buffer_load_dwordx4 v35,s[28:31],s84 offen offset:3072 lds
v_mfma_f32_16x16x32_bf16 a[40:43],a[80:83],a[240:243],a[40:43]
ds_read_b64_tr_b16 a[120:121],v50 offset:776
v_mfma_f32_16x16x32_bf16 a[40:43],a[84:87],a[244:247],a[40:43]
ds_read_b64_tr_b16 a[122:123],v50 offset:2824
v_mfma_f32_16x16x32_bf16 a[56:59],a[84:87],a[252:255],a[56:59]
ds_read_b64_tr_b16 a[124:125],v50 offset:17160
v_mfma_f32_16x16x32_bf16 a[56:59],a[80:83],a[248:251],a[56:59]
s_add_u32 m0,s85,4096
s_xor_b32 s85,s85,98304
ds_read_b64_tr_b16 a[126:127],v50 offset:19208
v_mfma_f32_16x16x32_bf16 a[12:15],a[88:91],a[224:227],a[12:15]
v_mfma_f32_16x16x32_bf16 a[12:15],a[92:95],a[228:231],a[12:15]
buffer_load_dwordx4 v36,s[28:31],s84 offen lds
v_mfma_f32_16x16x32_bf16 a[28:31],a[92:95],a[236:239],a[28:31]
ds_read_b64_tr_b16 a[64:65],v50 offset:1024
v_mfma_f32_16x16x32_bf16 a[28:31],a[88:91],a[232:235],a[28:31]
ds_read_b64_tr_b16 a[66:67],v50 offset:3072
v_mfma_f32_16x16x32_bf16 a[44:47],a[88:91],a[240:243],a[44:47]
ds_read_b64_tr_b16 a[68:69],v50 offset:17408
v_mfma_f32_16x16x32_bf16 a[44:47],a[92:95],a[244:247],a[44:47]
ds_read_b64_tr_b16 a[70:71],v50 offset:19456
v_mfma_f32_16x16x32_bf16 a[60:63],a[92:95],a[252:255],a[60:63]
v_mfma_f32_16x16x32_bf16 a[60:63],a[88:91],a[248:251],a[60:63]
buffer_load_dwordx4 v37,s[28:31],s84 offen offset:1024 lds
v_mfma_f32_16x16x32_bf16 v[64:67],a[96:99],a[224:227],v[64:67]
ds_read_b64_tr_b16 a[72:73],v50 offset:1032
v_mfma_f32_16x16x32_bf16 v[64:67],a[100:103],a[228:231],v[64:67]
ds_read_b64_tr_b16 a[74:75],v50 offset:3080
v_mfma_f32_16x16x32_bf16 v[80:83],a[100:103],a[236:239],v[80:83]
ds_read_b64_tr_b16 a[76:77],v50 offset:17416
v_mfma_f32_16x16x32_bf16 v[80:83],a[96:99],a[232:235],v[80:83]
ds_read_b64_tr_b16 a[78:79],v50 offset:19464
v_mfma_f32_16x16x32_bf16 v[96:99],a[96:99],a[240:243],v[96:99]
v_mfma_f32_16x16x32_bf16 v[96:99],a[100:103],a[244:247],v[96:99]
buffer_load_dwordx4 v38,s[28:31],s84 offen offset:2048 lds
v_mfma_f32_16x16x32_bf16 v[112:115],a[100:103],a[252:255],v[112:115]
ds_read_b64_tr_b16 a[80:81],v50 offset:1280
v_mfma_f32_16x16x32_bf16 v[112:115],a[96:99],a[248:251],v[112:115]
s_waitcnt lgkmcnt(5)
ds_read_b64_tr_b16 a[82:83],v50 offset:3328
v_mfma_f32_16x16x32_bf16 v[68:71],a[104:107],a[224:227],v[68:71]
ds_read_b64_tr_b16 a[84:85],v50 offset:17664
v_mfma_f32_16x16x32_bf16 v[68:71],a[108:111],a[228:231],v[68:71]
ds_read_b64_tr_b16 a[86:87],v50 offset:19712
v_mfma_f32_16x16x32_bf16 v[84:87],a[108:111],a[236:239],v[84:87]
v_mfma_f32_16x16x32_bf16 v[84:87],a[104:107],a[232:235],v[84:87]
buffer_load_dwordx4 v39,s[28:31],s84 offen offset:3072 lds
v_mfma_f32_16x16x32_bf16 v[100:103],a[104:107],a[240:243],v[100:103]
s_add_u32 s84,s84,128
ds_read_b64_tr_b16 a[88:89],v50 offset:1288
v_mfma_f32_16x16x32_bf16 v[100:103],a[108:111],a[244:247],v[100:103]
ds_read_b64_tr_b16 a[90:91],v50 offset:3336
v_mfma_f32_16x16x32_bf16 v[116:119],a[108:111],a[252:255],v[116:119]
ds_read_b64_tr_b16 a[92:93],v50 offset:17672
v_mfma_f32_16x16x32_bf16 v[116:119],a[104:107],a[248:251],v[116:119]
s_add_u32 m0,s88,0
ds_read_b64_tr_b16 a[94:95],v50 offset:19720
v_mfma_f32_16x16x32_bf16 v[72:75],a[112:115],a[224:227],v[72:75]
v_mfma_f32_16x16x32_bf16 v[72:75],a[116:119],a[228:231],v[72:75]
buffer_load_dwordx4 v40,s[36:39],s83 offen lds
v_mfma_f32_16x16x32_bf16 v[88:91],a[116:119],a[236:239],v[88:91]
ds_read_b64_tr_b16 a[96:97],v50 offset:1536
v_mfma_f32_16x16x32_bf16 v[88:91],a[112:115],a[232:235],v[88:91]
ds_read_b64_tr_b16 a[98:99],v50 offset:3584
v_mfma_f32_16x16x32_bf16 v[104:107],a[112:115],a[240:243],v[104:107]
ds_read_b64_tr_b16 a[100:101],v50 offset:17920
v_mfma_f32_16x16x32_bf16 v[104:107],a[116:119],a[244:247],v[104:107]
s_add_u32 m0,s88,1024
ds_read_b64_tr_b16 a[102:103],v50 offset:19968
v_mfma_f32_16x16x32_bf16 v[120:123],a[116:119],a[252:255],v[120:123]
v_mfma_f32_16x16x32_bf16 v[120:123],a[112:115],a[248:251],v[120:123]
buffer_load_dwordx4 v41,s[36:39],s83 offen lds
v_mfma_f32_16x16x32_bf16 v[76:79],a[120:123],a[224:227],v[76:79]
ds_read_b64_tr_b16 a[104:105],v50 offset:1544
v_mfma_f32_16x16x32_bf16 v[76:79],a[124:127],a[228:231],v[76:79]
ds_read_b64_tr_b16 a[106:107],v50 offset:3592
v_mfma_f32_16x16x32_bf16 v[92:95],a[124:127],a[236:239],v[92:95]
ds_read_b64_tr_b16 a[108:109],v50 offset:17928
v_mfma_f32_16x16x32_bf16 v[92:95],a[120:123],a[232:235],v[92:95]
s_add_u32 m0,s88,2048
ds_read_b64_tr_b16 a[110:111],v50 offset:19976
v_mfma_f32_16x16x32_bf16 v[108:111],a[120:123],a[240:243],v[108:111]
v_mfma_f32_16x16x32_bf16 v[108:111],a[124:127],a[244:247],v[108:111]
buffer_load_dwordx4 v42,s[36:39],s83 offen lds
v_mfma_f32_16x16x32_bf16 v[124:127],a[124:127],a[252:255],v[124:127]
ds_read_b64_tr_b16 a[112:113],v50 offset:1792
v_mfma_f32_16x16x32_bf16 v[124:127],a[120:123],a[248:251],v[124:127]
ds_read_b64_tr_b16 a[114:115],v50 offset:3840
v_mfma_f32_16x16x32_bf16 v[128:131],a[64:67],a[224:227],v[128:131]
ds_read_b64_tr_b16 a[116:117],v50 offset:18176
v_mfma_f32_16x16x32_bf16 v[128:131],a[68:71],a[228:231],v[128:131]
s_add_u32 m0,s88,3072
ds_read_b64_tr_b16 a[118:119],v50 offset:20224
v_mfma_f32_16x16x32_bf16 v[160:163],a[68:71],a[236:239],v[160:163]
v_mfma_f32_16x16x32_bf16 v[160:163],a[64:67],a[232:235],v[160:163]
buffer_load_dwordx4 v43,s[36:39],s83 offen lds
v_mfma_f32_16x16x32_bf16 v[192:195],a[64:67],a[240:243],v[192:195]
ds_read_b64_tr_b16 a[120:121],v50 offset:1800
v_mfma_f32_16x16x32_bf16 v[192:195],a[68:71],a[244:247],v[192:195]
ds_read_b64_tr_b16 a[122:123],v50 offset:3848
v_mfma_f32_16x16x32_bf16 v[224:227],a[68:71],a[252:255],v[224:227]
s_waitcnt lgkmcnt(4)
ds_read_b64_tr_b16 a[124:125],v50 offset:18184
v_mfma_f32_16x16x32_bf16 v[224:227],a[64:67],a[248:251],v[224:227]
s_add_u32 m0,s88,4096
ds_read_b64_tr_b16 a[126:127],v50 offset:20232
v_add_u32 v50,s86,v51
v_mfma_f32_16x16x32_bf16 v[132:135],a[72:75],a[224:227],v[132:135]
v_mfma_f32_16x16x32_bf16 v[132:135],a[76:79],a[228:231],v[132:135]
buffer_load_dwordx4 v44,s[36:39],s83 offen lds
v_mfma_f32_16x16x32_bf16 v[164:167],a[76:79],a[236:239],v[164:167]
v_mfma_f32_16x16x32_bf16 v[164:167],a[72:75],a[232:235],v[164:167]
v_mfma_f32_16x16x32_bf16 v[196:199],a[72:75],a[240:243],v[196:199]
v_mfma_f32_16x16x32_bf16 v[196:199],a[76:79],a[244:247],v[196:199]
s_add_u32 m0,s88,5120
v_mfma_f32_16x16x32_bf16 v[228:231],a[76:79],a[252:255],v[228:231]
v_mfma_f32_16x16x32_bf16 v[228:231],a[72:75],a[248:251],v[228:231]
buffer_load_dwordx4 v45,s[36:39],s83 offen lds
v_mfma_f32_16x16x32_bf16 v[136:139],a[80:83],a[224:227],v[136:139]
v_mfma_f32_16x16x32_bf16 v[136:139],a[84:87],a[228:231],v[136:139]
v_mfma_f32_16x16x32_bf16 v[168:171],a[84:87],a[236:239],v[168:171]
v_mfma_f32_16x16x32_bf16 v[168:171],a[80:83],a[232:235],v[168:171]
)A"
R"A(s_add_u32 m0,s88,6144
v_mfma_f32_16x16x32_bf16 v[200:203],a[80:83],a[240:243],v[200:203]
v_mfma_f32_16x16x32_bf16 v[200:203],a[84:87],a[244:247],v[200:203]
buffer_load_dwordx4 v46,s[36:39],s83 offen lds
v_mfma_f32_16x16x32_bf16 v[232:235],a[84:87],a[252:255],v[232:235]
v_mfma_f32_16x16x32_bf16 v[232:235],a[80:83],a[248:251],v[232:235]
v_mfma_f32_16x16x32_bf16 v[140:143],a[88:91],a[224:227],v[140:143]
v_mfma_f32_16x16x32_bf16 v[140:143],a[92:95],a[228:231],v[140:143]
s_add_u32 m0,s88,7168
s_mov_b32 s88,s69
s_and_b32 s69,s75,0xfffc0000
v_mfma_f32_16x16x32_bf16 v[172:175],a[92:95],a[236:239],v[172:175]
s_add_u32 s75,s75,16384
v_mfma_f32_16x16x32_bf16 v[172:175],a[88:91],a[232:235],v[172:175]
buffer_load_dwordx4 v47,s[36:39],s83 offen lds
v_mfma_f32_16x16x32_bf16 v[204:207],a[88:91],a[240:243],v[204:207]
s_waitcnt lgkmcnt(0)
s_add_u32 s83,s83,524288
s_sub_u32 s92,s92,s8
v_mfma_f32_16x16x32_bf16 v[204:207],a[92:95],a[244:247],v[204:207]
s_sub_u32 s56,s56,s4
s_mov_b32 s93,782
s_mul_i32 s92,s92,12600
v_mfma_f32_16x16x32_bf16 v[236:239],a[92:95],a[252:255],v[236:239]
s_cmp_ge_u32 s63,2
s_cselect_b32 s93,823,s93
s_mul_i32 s93,s93,s56
v_mfma_f32_16x16x32_bf16 v[236:239],a[88:91],a[248:251],v[236:239]
s_add_u32 s10,s20,s74
s_addc_u32 s11,s21,0
s_waitcnt vmcnt(25)
s_barrier
v_mfma_f32_16x16x32_bf16 v[144:147],a[96:99],a[224:227],v[144:147]
s_add_u32 m0,s76,0
s_waitcnt vmcnt(17)
ds_read_b128 a[192:195],v48
v_mfma_f32_16x16x32_bf16 v[144:147],a[100:103],a[228:231],v[144:147]
ds_read_b128 a[196:199],v49
v_mfma_f32_16x16x32_bf16 v[176:179],a[100:103],a[236:239],v[176:179]
global_load_lds_dwordx4 v4,s[10:11] nt
ds_read_b128 a[200:203],v48 offset:2048
v_mfma_f32_16x16x32_bf16 v[176:179],a[96:99],a[232:235],v[176:179]
ds_read_b128 a[204:207],v49 offset:2048
v_mfma_f32_16x16x32_bf16 v[208:211],a[96:99],a[240:243],v[208:211]
v_mfma_f32_16x16x32_bf16 v[208:211],a[100:103],a[244:247],v[208:211]
ds_read_b128 a[208:211],v48 offset:4096
v_mfma_f32_16x16x32_bf16 v[240:243],a[100:103],a[252:255],v[240:243]
ds_read_b128 a[212:215],v49 offset:4096
v_mfma_f32_16x16x32_bf16 v[240:243],a[96:99],a[248:251],v[240:243]
ds_read_b128 a[216:219],v48 offset:6144
v_xor_b32 v48,98304,v48
v_mfma_f32_16x16x32_bf16 v[148:151],a[104:107],a[224:227],v[148:151]
v_mfma_f32_16x16x32_bf16 v[148:151],a[108:111],a[228:231],v[148:151]
ds_read_b128 a[220:223],v49 offset:6144
v_xor_b32 v49,98304,v49
v_mfma_f32_16x16x32_bf16 v[180:183],a[108:111],a[236:239],v[180:183]
ds_read_b64_tr_b16 a[64:65],v50
v_mfma_f32_16x16x32_bf16 v[180:183],a[104:107],a[232:235],v[180:183]
ds_read_b64_tr_b16 a[66:67],v50 offset:2048
v_mfma_f32_16x16x32_bf16 v[212:215],a[104:107],a[240:243],v[212:215]
v_mfma_f32_16x16x32_bf16 v[212:215],a[108:111],a[244:247],v[212:215]
ds_read_b64_tr_b16 a[68:69],v50 offset:16384
v_mfma_f32_16x16x32_bf16 v[244:247],a[108:111],a[252:255],v[244:247]
ds_read_b64_tr_b16 a[70:71],v50 offset:18432
v_mfma_f32_16x16x32_bf16 v[244:247],a[104:107],a[248:251],v[244:247]
ds_read_b64_tr_b16 a[72:73],v50 offset:8
v_mfma_f32_16x16x32_bf16 v[152:155],a[112:115],a[224:227],v[152:155]
v_mfma_f32_16x16x32_bf16 v[152:155],a[116:119],a[228:231],v[152:155]
ds_read_b64_tr_b16 a[74:75],v50 offset:2056
v_mfma_f32_16x16x32_bf16 v[184:187],a[116:119],a[236:239],v[184:187]
global_load_lds_dwordx4 v4,s[10:11] offset:1024 nt
ds_read_b64_tr_b16 a[76:77],v50 offset:16392
v_mfma_f32_16x16x32_bf16 v[184:187],a[112:115],a[232:235],v[184:187]
ds_read_b64_tr_b16 a[78:79],v50 offset:18440
v_mfma_f32_16x16x32_bf16 v[216:219],a[112:115],a[240:243],v[216:219]
v_mfma_f32_16x16x32_bf16 v[216:219],a[116:119],a[244:247],v[216:219]
ds_read_b64_tr_b16 a[80:81],v50 offset:1024
v_mfma_f32_16x16x32_bf16 v[248:251],a[116:119],a[252:255],v[248:251]
ds_read_b64_tr_b16 a[82:83],v50 offset:3072
v_mfma_f32_16x16x32_bf16 v[248:251],a[112:115],a[248:251],v[248:251]
ds_read_b64_tr_b16 a[84:85],v50 offset:17408
v_mfma_f32_16x16x32_bf16 v[156:159],a[120:123],a[224:227],v[156:159]
v_mfma_f32_16x16x32_bf16 v[156:159],a[124:127],a[228:231],v[156:159]
ds_read_b64_tr_b16 a[86:87],v50 offset:19456
v_mfma_f32_16x16x32_bf16 v[188:191],a[124:127],a[236:239],v[188:191]
ds_read_b64_tr_b16 a[88:89],v50 offset:1032
v_mfma_f32_16x16x32_bf16 v[188:191],a[120:123],a[232:235],v[188:191]
s_cmp_lt_u32 s92,s93
s_cselect_b32 s5,6,0
s_cselect_b32 s7,3,0
s_cselect_b32 s9,8,10
ds_read_b64_tr_b16 a[90:91],v50 offset:3080
v_mfma_f32_16x16x32_bf16 v[220:223],a[120:123],a[240:243],v[220:223]
s_mov_b32 s4,s5
v_mfma_f32_16x16x32_bf16 v[220:223],a[124:127],a[244:247],v[220:223]
ds_read_b64_tr_b16 a[92:93],v50 offset:17416
v_mfma_f32_16x16x32_bf16 v[252:255],a[124:127],a[252:255],v[252:255]
ds_read_b64_tr_b16 a[94:95],v50 offset:19464
v_mfma_f32_16x16x32_bf16 v[252:255],a[120:123],a[248:251],v[252:255]
s_waitcnt vmcnt(10) lgkmcnt(5)
ds_read_b128 a[224:227],v48
v_mfma_f32_16x16x32_bf16 v[128:131],a[64:67],a[192:195],v[128:131]
v_mfma_f32_16x16x32_bf16 v[128:131],a[68:71],a[196:199],v[128:131]
ds_read_b128 a[228:231],v49
v_mfma_f32_16x16x32_bf16 v[160:163],a[68:71],a[204:207],v[160:163]
global_load_lds_dwordx4 v5,s[10:11] offset:2048 nt
ds_read_b128 a[236:239],v49 offset:2048
v_mfma_f32_16x16x32_bf16 v[160:163],a[64:67],a[200:203],v[160:163]
ds_read_b128 a[232:235],v48 offset:2048
v_mfma_f32_16x16x32_bf16 v[192:195],a[64:67],a[208:211],v[192:195]
v_mfma_f32_16x16x32_bf16 v[192:195],a[68:71],a[212:215],v[192:195]
ds_read_b128 a[240:243],v48 offset:4096
v_mfma_f32_16x16x32_bf16 v[224:227],a[68:71],a[220:223],v[224:227]
ds_read_b128 a[244:247],v49 offset:4096
v_mfma_f32_16x16x32_bf16 v[224:227],a[64:67],a[216:219],v[224:227]
ds_read_b128 a[252:255],v49 offset:6144
v_mfma_f32_16x16x32_bf16 v[132:135],a[72:75],a[192:195],v[132:135]
v_mfma_f32_16x16x32_bf16 v[132:135],a[76:79],a[196:199],v[132:135]
ds_read_b128 a[248:251],v48 offset:6144
v_mfma_f32_16x16x32_bf16 v[164:167],a[76:79],a[204:207],v[164:167]
ds_read_b64_tr_b16 a[96:97],v50 offset:256
v_mfma_f32_16x16x32_bf16 v[164:167],a[72:75],a[200:203],v[164:167]
ds_read_b64_tr_b16 a[98:99],v50 offset:2304
v_mfma_f32_16x16x32_bf16 v[196:199],a[72:75],a[208:211],v[196:199]
v_mfma_f32_16x16x32_bf16 v[196:199],a[76:79],a[212:215],v[196:199]
ds_read_b64_tr_b16 a[100:101],v50 offset:16640
v_mfma_f32_16x16x32_bf16 v[228:231],a[76:79],a[220:223],v[228:231]
ds_read_b64_tr_b16 a[102:103],v50 offset:18688
v_mfma_f32_16x16x32_bf16 v[228:231],a[72:75],a[216:219],v[228:231]
s_waitcnt lgkmcnt(5)
ds_read_b64_tr_b16 a[104:105],v50 offset:1280
v_mfma_f32_16x16x32_bf16 v[128:131],a[80:83],a[224:227],v[128:131]
v_mfma_f32_16x16x32_bf16 v[128:131],a[84:87],a[228:231],v[128:131]
ds_read_b64_tr_b16 a[106:107],v50 offset:3328
v_mfma_f32_16x16x32_bf16 v[160:163],a[84:87],a[236:239],v[160:163]
global_load_lds_dwordx4 v5,s[10:11] offset:3072
ds_read_b64_tr_b16 a[108:109],v50 offset:17664
v_mfma_f32_16x16x32_bf16 v[160:163],a[80:83],a[232:235],v[160:163]
ds_read_b64_tr_b16 a[110:111],v50 offset:19712
v_mfma_f32_16x16x32_bf16 v[192:195],a[80:83],a[240:243],v[192:195]
v_mfma_f32_16x16x32_bf16 v[192:195],a[84:87],a[244:247],v[192:195]
ds_read_b64_tr_b16 a[112:113],v50 offset:264
v_mul_f32 v128,s71,v128
v_mfma_f32_16x16x32_bf16 v[224:227],a[84:87],a[252:255],v[224:227]
s_waitcnt lgkmcnt(5)
ds_read_b64_tr_b16 a[114:115],v50 offset:2312
v_mul_f32 v129,s71,v129
v_mfma_f32_16x16x32_bf16 v[224:227],a[80:83],a[248:251],v[224:227]
ds_read_b64_tr_b16 a[116:117],v50 offset:16648
v_mul_f32 v130,s71,v130
v_mfma_f32_16x16x32_bf16 v[132:135],a[88:91],a[224:227],v[132:135]
v_mul_f32 v131,s71,v131
v_cvt_pk_bf16_f32 v128,v128,v129
v_mfma_f32_16x16x32_bf16 v[132:135],a[92:95],a[228:231],v[132:135]
ds_read_b64_tr_b16 a[118:119],v50 offset:18696
v_cvt_pk_bf16_f32 v129,v130,v131
v_mfma_f32_16x16x32_bf16 v[164:167],a[92:95],a[236:239],v[164:167]
ds_read_b64_tr_b16 a[120:121],v50 offset:1288
v_mul_f32 v192,s71,v192
v_mfma_f32_16x16x32_bf16 v[164:167],a[88:91],a[232:235],v[164:167]
ds_read_b64_tr_b16 a[122:123],v50 offset:3336
v_mul_f32 v193,s71,v193
v_mfma_f32_16x16x32_bf16 v[196:199],a[88:91],a[240:243],v[196:199]
v_mul_f32 v132,s71,v132
v_mul_f32 v133,s71,v133
v_mfma_f32_16x16x32_bf16 v[196:199],a[92:95],a[244:247],v[196:199]
ds_read_b64_tr_b16 a[124:125],v50 offset:17672
v_mul_f32 v134,s71,v134
v_mfma_f32_16x16x32_bf16 v[228:231],a[92:95],a[252:255],v[228:231]
s_add_u32 s10,s24,s74
s_addc_u32 s11,s25,0
ds_read_b64_tr_b16 a[126:127],v50 offset:19720
v_mfma_f32_16x16x32_bf16 v[228:231],a[88:91],a[248:251],v[228:231]
s_add_u32 s74,s74,16384
s_and_b32 s74,s74,0x3ffff
ds_read_b64_tr_b16 a[128:129],v50 offset:512
v_mfma_f32_16x16x32_bf16 v[136:139],a[96:99],a[192:195],v[136:139]
s_add_u32 m0,s76,16384
s_or_b32 s74,s74,s69
s_sub_u32 s76,s76,32768
v_mfma_f32_16x16x32_bf16 v[136:139],a[100:103],a[196:199],v[136:139]
s_and_b32 s76,s76,0x1ffff
s_and_b32 s69,s75,0xfffc0000
ds_read_b64_tr_b16 a[130:131],v50 offset:2560
s_waitcnt lgkmcnt(6)
v_mfma_f32_16x16x32_bf16 v[136:139],a[104:107],a[224:227],v[136:139]
global_load_lds_dwordx4 v4,s[10:11]
ds_read_b64_tr_b16 a[132:133],v50 offset:16896
v_mfma_f32_16x16x32_bf16 v[136:139],a[108:111],a[228:231],v[136:139]
s_add_u32 s75,s75,16384
ds_read_b64_tr_b16 a[134:135],v50 offset:18944
v_mul_f32 v135,s71,v135
v_mfma_f32_16x16x32_bf16 v[168:171],a[108:111],a[236:239],v[168:171]
v_cvt_pk_bf16_f32 v130,v132,v133
v_cvt_pk_bf16_f32 v131,v134,v135
v_mfma_f32_16x16x32_bf16 v[168:171],a[104:107],a[232:235],v[168:171]
ds_read_b64_tr_b16 a[136:137],v50 offset:1536
v_mul_f32 v194,s71,v194
v_mfma_f32_16x16x32_bf16 v[168:171],a[100:103],a[204:207],v[168:171]
ds_read_b64_tr_b16 a[138:139],v50 offset:3584
v_mul_f32 v195,s71,v195
v_mfma_f32_16x16x32_bf16 v[168:171],a[96:99],a[200:203],v[168:171]
ds_read_b64_tr_b16 a[140:141],v50 offset:17920
v_mul_f32 v196,s71,v196
v_mfma_f32_16x16x32_bf16 v[200:203],a[96:99],a[208:211],v[200:203]
v_mul_f32 v197,s71,v197
v_mul_f32 v198,s71,v198
v_mfma_f32_16x16x32_bf16 v[200:203],a[100:103],a[212:215],v[200:203]
ds_read_b64_tr_b16 a[142:143],v50 offset:19968
v_mul_f32 v199,s71,v199
v_mfma_f32_16x16x32_bf16 v[200:203],a[104:107],a[240:243],v[200:203]
ds_read_b64_tr_b16 a[144:145],v50 offset:520
v_cvt_pk_bf16_f32 v192,v192,v193
v_mfma_f32_16x16x32_bf16 v[200:203],a[108:111],a[244:247],v[200:203]
ds_read_b64_tr_b16 a[146:147],v50 offset:2568
v_cvt_pk_bf16_f32 v193,v194,v195
v_mfma_f32_16x16x32_bf16 v[232:235],a[108:111],a[252:255],v[232:235]
v_cvt_pk_bf16_f32 v194,v196,v197
v_cvt_pk_bf16_f32 v195,v198,v199
v_mfma_f32_16x16x32_bf16 v[232:235],a[104:107],a[248:251],v[232:235]
ds_read_b64_tr_b16 a[148:149],v50 offset:16904
v_mul_f32 v136,s71,v136
v_mfma_f32_16x16x32_bf16 v[232:235],a[100:103],a[220:223],v[232:235]
ds_read_b64_tr_b16 a[150:151],v50 offset:18952
v_mul_f32 v137,s71,v137
v_mfma_f32_16x16x32_bf16 v[232:235],a[96:99],a[216:219],v[232:235]
ds_read_b64_tr_b16 a[152:153],v50 offset:1544
v_mul_f32 v138,s71,v138
v_mfma_f32_16x16x32_bf16 v[140:143],a[112:115],a[192:195],v[140:143]
v_mul_f32 v139,s71,v139
v_cvt_pk_bf16_f32 v136,v136,v137
v_mfma_f32_16x16x32_bf16 v[140:143],a[116:119],a[196:199],v[140:143]
s_waitcnt lgkmcnt(5)
ds_read_b64_tr_b16 a[154:155],v50 offset:3592
v_cvt_pk_bf16_f32 v137,v138,v139
v_mfma_f32_16x16x32_bf16 v[140:143],a[120:123],a[224:227],v[140:143]
global_load_lds_dwordx4 v5,s[10:11] offset:1024
ds_read_b64_tr_b16 a[156:157],v50 offset:17928
v_mfma_f32_16x16x32_bf16 v[140:143],a[124:127],a[228:231],v[140:143]
ds_read_b64_tr_b16 a[158:159],v50 offset:19976
v_mul_f32 v160,s71,v160
v_mfma_f32_16x16x32_bf16 v[172:175],a[124:127],a[236:239],v[172:175]
v_mul_f32 v161,s71,v161
v_mul_f32 v162,s71,v162
v_mfma_f32_16x16x32_bf16 v[172:175],a[120:123],a[232:235],v[172:175]
ds_read_b64_tr_b16 a[160:161],v50 offset:768
v_mul_f32 v163,s71,v163
v_mfma_f32_16x16x32_bf16 v[172:175],a[116:119],a[204:207],v[172:175]
ds_read_b64_tr_b16 a[162:163],v50 offset:2816
v_mul_f32 v140,s71,v140
v_mfma_f32_16x16x32_bf16 v[172:175],a[112:115],a[200:203],v[172:175]
ds_read_b64_tr_b16 a[164:165],v50 offset:17152
v_mul_f32 v141,s71,v141
v_mfma_f32_16x16x32_bf16 v[204:207],a[112:115],a[208:211],v[204:207]
)A"
R"A(v_mul_f32 v142,s71,v142
v_mul_f32 v143,s71,v143
v_mfma_f32_16x16x32_bf16 v[204:207],a[116:119],a[212:215],v[204:207]
ds_read_b64_tr_b16 a[166:167],v50 offset:19200
v_cvt_pk_bf16_f32 v138,v140,v141
v_mfma_f32_16x16x32_bf16 v[204:207],a[120:123],a[240:243],v[204:207]
ds_read_b64_tr_b16 a[168:169],v50 offset:1792
v_cvt_pk_bf16_f32 v139,v142,v143
v_mfma_f32_16x16x32_bf16 v[204:207],a[124:127],a[244:247],v[204:207]
ds_read_b64_tr_b16 a[170:171],v50 offset:3840
v_mul_f32 v164,s71,v164
v_mfma_f32_16x16x32_bf16 v[236:239],a[124:127],a[252:255],v[236:239]
v_mul_f32 v165,s71,v165
v_mul_f32 v166,s71,v166
v_mfma_f32_16x16x32_bf16 v[236:239],a[120:123],a[248:251],v[236:239]
ds_read_b64_tr_b16 a[172:173],v50 offset:18176
v_mul_f32 v167,s71,v167
v_mfma_f32_16x16x32_bf16 v[236:239],a[116:119],a[220:223],v[236:239]
ds_read_b64_tr_b16 a[174:175],v50 offset:20224
v_cvt_pk_bf16_f32 v160,v160,v161
v_mfma_f32_16x16x32_bf16 v[236:239],a[112:115],a[216:219],v[236:239]
ds_read_b64_tr_b16 a[176:177],v50 offset:776
v_cvt_pk_bf16_f32 v161,v162,v163
v_mfma_f32_16x16x32_bf16 v[144:147],a[128:131],a[192:195],v[144:147]
v_cvt_pk_bf16_f32 v162,v164,v165
v_cvt_pk_bf16_f32 v163,v166,v167
v_mfma_f32_16x16x32_bf16 v[144:147],a[132:135],a[196:199],v[144:147]
ds_read_b64_tr_b16 a[178:179],v50 offset:2824
v_mul_f32 v224,s71,v224
v_mfma_f32_16x16x32_bf16 v[144:147],a[136:139],a[224:227],v[144:147]
global_load_lds_dwordx4 v4,s[10:11] offset:2048
ds_read_b64_tr_b16 a[180:181],v50 offset:17160
v_mfma_f32_16x16x32_bf16 v[144:147],a[140:143],a[228:231],v[144:147]
ds_read_b64_tr_b16 a[182:183],v50 offset:19208
v_mul_f32 v225,s71,v225
v_mfma_f32_16x16x32_bf16 v[176:179],a[140:143],a[236:239],v[176:179]
v_mul_f32 v226,s71,v226
v_mul_f32 v227,s71,v227
v_mfma_f32_16x16x32_bf16 v[176:179],a[136:139],a[232:235],v[176:179]
ds_read_b64_tr_b16 a[184:185],v50 offset:1800
v_mul_f32 v228,s71,v228
v_mfma_f32_16x16x32_bf16 v[176:179],a[132:135],a[204:207],v[176:179]
ds_read_b64_tr_b16 a[186:187],v50 offset:3848
v_mul_f32 v144,s71,v144
v_mfma_f32_16x16x32_bf16 v[176:179],a[128:131],a[200:203],v[176:179]
ds_read_b64_tr_b16 a[188:189],v50 offset:18184
v_mul_f32 v145,s71,v145
v_mfma_f32_16x16x32_bf16 v[208:211],a[128:131],a[208:211],v[208:211]
v_mul_f32 v146,s71,v146
v_mul_f32 v147,s71,v147
v_mfma_f32_16x16x32_bf16 v[208:211],a[132:135],a[212:215],v[208:211]
ds_read_b64_tr_b16 a[190:191],v50 offset:20232
v_cvt_pk_bf16_f32 v144,v144,v145
v_mfma_f32_16x16x32_bf16 v[208:211],a[136:139],a[240:243],v[208:211]
v_cvt_pk_bf16_f32 v145,v146,v147
v_mul_f32 v229,s71,v229
v_mfma_f32_16x16x32_bf16 v[208:211],a[140:143],a[244:247],v[208:211]
v_mul_f32 v230,s71,v230
v_mul_f32 v231,s71,v231
v_mfma_f32_16x16x32_bf16 v[240:243],a[140:143],a[252:255],v[240:243]
v_cvt_pk_bf16_f32 v224,v224,v225
v_cvt_pk_bf16_f32 v225,v226,v227
v_mfma_f32_16x16x32_bf16 v[240:243],a[136:139],a[248:251],v[240:243]
v_cvt_pk_bf16_f32 v226,v228,v229
v_cvt_pk_bf16_f32 v227,v230,v231
v_mfma_f32_16x16x32_bf16 v[240:243],a[132:135],a[220:223],v[240:243]
v_mul_f32 v168,s71,v168
v_mul_f32 v169,s71,v169
v_mfma_f32_16x16x32_bf16 v[240:243],a[128:131],a[216:219],v[240:243]
v_mul_f32 v170,s71,v170
v_mul_f32 v171,s71,v171
s_waitcnt vmcnt(7)
s_barrier
s_waitcnt lgkmcnt(1)
v_mfma_f32_16x16x32_bf16 v[148:151],a[144:147],a[192:195],v[148:151]
ds_read_b64_tr_b16 a[64:65],v52
v_mul_f32 v172,s71,v172
v_mfma_f32_16x16x32_bf16 v[148:151],a[148:151],a[196:199],v[148:151]
global_load_lds_dwordx4 v5,s[10:11] offset:3072
ds_read_b64_tr_b16 a[66:67],v52 offset:2048
v_mfma_f32_16x16x32_bf16 v[148:151],a[152:155],a[224:227],v[148:151]
ds_read_b64_tr_b16 a[68:69],v52 offset:16384
v_mul_f32 v173,s71,v173
L_pd3_%=:
s_cbranch_execz L_pd7_%=
v_mfma_f32_16x16x32_bf16 v[148:151],a[156:159],a[228:231],v[148:151]
ds_read_b64_tr_b16 a[70:71],v52 offset:18432
v_mul_f32 v174,s71,v174
v_mfma_f32_16x16x32_bf16 v[180:183],a[156:159],a[236:239],v[180:183]
v_mul_f32 v175,s71,v175
v_cvt_pk_bf16_f32 v168,v168,v169
v_mfma_f32_16x16x32_bf16 v[180:183],a[152:155],a[232:235],v[180:183]
ds_read_b64_tr_b16 a[72:73],v52 offset:1024
v_cvt_pk_bf16_f32 v169,v170,v171
v_mfma_f32_16x16x32_bf16 v[180:183],a[148:151],a[204:207],v[180:183]
ds_read_b64_tr_b16 a[74:75],v52 offset:3072
v_mul_f32 v148,s71,v148
v_mfma_f32_16x16x32_bf16 v[180:183],a[144:147],a[200:203],v[180:183]
ds_read_b64_tr_b16 a[76:77],v52 offset:17408
v_mul_f32 v149,s71,v149
v_mfma_f32_16x16x32_bf16 v[212:215],a[144:147],a[208:211],v[212:215]
v_mul_f32 v150,s71,v150
v_mul_f32 v151,s71,v151
v_mfma_f32_16x16x32_bf16 v[212:215],a[148:151],a[212:215],v[212:215]
ds_read_b64_tr_b16 a[78:79],v52 offset:19456
v_cvt_pk_bf16_f32 v146,v148,v149
v_mfma_f32_16x16x32_bf16 v[212:215],a[152:155],a[240:243],v[212:215]
ds_read_b64_tr_b16 a[80:81],v52 offset:8
v_cvt_pk_bf16_f32 v147,v150,v151
v_mfma_f32_16x16x32_bf16 v[212:215],a[156:159],a[244:247],v[212:215]
ds_read_b64_tr_b16 a[82:83],v52 offset:2056
v_cvt_pk_bf16_f32 v170,v172,v173
v_mfma_f32_16x16x32_bf16 v[244:247],a[156:159],a[252:255],v[244:247]
v_cvt_pk_bf16_f32 v171,v174,v175
v_mul_f32 v176,s71,v176
v_mfma_f32_16x16x32_bf16 v[244:247],a[152:155],a[248:251],v[244:247]
ds_read_b64_tr_b16 a[84:85],v52 offset:16392
v_mul_f32 v177,s71,v177
v_mfma_f32_16x16x32_bf16 v[244:247],a[148:151],a[220:223],v[244:247]
s_add_u32 s10,s20,s74
s_addc_u32 s11,s21,0
ds_read_b64_tr_b16 a[86:87],v52 offset:18440
v_mfma_f32_16x16x32_bf16 v[244:247],a[144:147],a[216:219],v[244:247]
ds_read_b64_tr_b16 a[88:89],v52 offset:1032
v_mul_f32 v178,s71,v178
v_mfma_f32_16x16x32_bf16 v[152:155],a[160:163],a[192:195],v[152:155]
s_add_u32 m0,s76,0
v_mul_f32 v179,s71,v179
v_mul_f32 v180,s71,v180
v_mfma_f32_16x16x32_bf16 v[152:155],a[164:167],a[196:199],v[152:155]
ds_read_b64_tr_b16 a[90:91],v52 offset:3080
v_mul_f32 v181,s71,v181
v_mfma_f32_16x16x32_bf16 v[152:155],a[168:171],a[224:227],v[152:155]
global_load_lds_dwordx4 v4,s[10:11] nt
ds_read_b64_tr_b16 a[92:93],v52 offset:17416
v_mfma_f32_16x16x32_bf16 v[152:155],a[172:175],a[228:231],v[152:155]
ds_read_b64_tr_b16 a[94:95],v52 offset:19464
v_mul_f32 v182,s71,v182
v_mfma_f32_16x16x32_bf16 v[184:187],a[172:175],a[236:239],v[184:187]
v_mul_f32 v183,s71,v183
v_cvt_pk_bf16_f32 v176,v176,v177
v_mfma_f32_16x16x32_bf16 v[184:187],a[168:171],a[232:235],v[184:187]
ds_read_b64_tr_b16 a[96:97],v52 offset:256
v_cvt_pk_bf16_f32 v177,v178,v179
v_mfma_f32_16x16x32_bf16 v[184:187],a[164:167],a[204:207],v[184:187]
ds_read_b64_tr_b16 a[98:99],v52 offset:2304
v_mul_f32 v152,s71,v152
v_mfma_f32_16x16x32_bf16 v[184:187],a[160:163],a[200:203],v[184:187]
ds_read_b64_tr_b16 a[100:101],v52 offset:16640
v_mul_f32 v153,s71,v153
v_mfma_f32_16x16x32_bf16 v[216:219],a[160:163],a[208:211],v[216:219]
v_mul_f32 v154,s71,v154
v_mul_f32 v155,s71,v155
v_mfma_f32_16x16x32_bf16 v[216:219],a[164:167],a[212:215],v[216:219]
ds_read_b64_tr_b16 a[102:103],v52 offset:18688
v_cvt_pk_bf16_f32 v152,v152,v153
v_mfma_f32_16x16x32_bf16 v[216:219],a[168:171],a[240:243],v[216:219]
ds_read_b64_tr_b16 a[104:105],v52 offset:1280
v_cvt_pk_bf16_f32 v153,v154,v155
v_mfma_f32_16x16x32_bf16 v[216:219],a[172:175],a[244:247],v[216:219]
ds_read_b64_tr_b16 a[106:107],v52 offset:3328
v_cvt_pk_bf16_f32 v178,v180,v181
v_mfma_f32_16x16x32_bf16 v[248:251],a[172:175],a[252:255],v[248:251]
v_cvt_pk_bf16_f32 v179,v182,v183
v_mul_f32 v184,s71,v184
v_mfma_f32_16x16x32_bf16 v[248:251],a[168:171],a[248:251],v[248:251]
ds_read_b64_tr_b16 a[108:109],v52 offset:17664
v_mul_f32 v185,s71,v185
v_mfma_f32_16x16x32_bf16 v[248:251],a[164:167],a[220:223],v[248:251]
ds_read_b64_tr_b16 a[110:111],v52 offset:19712
v_mul_f32 v186,s71,v186
v_mfma_f32_16x16x32_bf16 v[248:251],a[160:163],a[216:219],v[248:251]
ds_read_b64_tr_b16 a[112:113],v52 offset:264
v_mul_f32 v187,s71,v187
v_mfma_f32_16x16x32_bf16 v[156:159],a[176:179],a[192:195],v[156:159]
v_cvt_pk_bf16_f32 v184,v184,v185
v_cvt_pk_bf16_f32 v185,v186,v187
v_mfma_f32_16x16x32_bf16 v[156:159],a[180:183],a[196:199],v[156:159]
global_load_lds_dwordx4 v4,s[10:11] offset:1024 nt
ds_read_b64_tr_b16 a[114:115],v52 offset:2312
v_mfma_f32_16x16x32_bf16 v[156:159],a[184:187],a[224:227],v[156:159]
s_waitcnt lgkmcnt(5)
ds_read_b64_tr_b16 a[116:117],v52 offset:16648
v_mul_f32 v200,s71,v200
v_mfma_f32_16x16x32_bf16 v[156:159],a[188:191],a[228:231],v[156:159]
ds_read_b64_tr_b16 a[118:119],v52 offset:18696
v_mul_f32 v201,s71,v201
v_mfma_f32_16x16x32_bf16 v[188:191],a[188:191],a[236:239],v[188:191]
v_mul_f32 v202,s71,v202
v_mul_f32 v203,s71,v203
v_mfma_f32_16x16x32_bf16 v[188:191],a[184:187],a[232:235],v[188:191]
ds_read_b64_tr_b16 a[120:121],v52 offset:1288
v_mul_f32 v204,s71,v204
v_mfma_f32_16x16x32_bf16 v[188:191],a[180:183],a[204:207],v[188:191]
ds_read_b64_tr_b16 a[122:123],v52 offset:3336
v_mul_f32 v156,s71,v156
v_mfma_f32_16x16x32_bf16 v[188:191],a[176:179],a[200:203],v[188:191]
ds_read_b64_tr_b16 a[124:125],v52 offset:17672
v_mul_f32 v157,s71,v157
v_mfma_f32_16x16x32_bf16 v[220:223],a[176:179],a[208:211],v[220:223]
v_mul_f32 v158,s71,v158
v_mul_f32 v159,s71,v159
v_mfma_f32_16x16x32_bf16 v[220:223],a[180:183],a[212:215],v[220:223]
ds_read_b64_tr_b16 a[126:127],v52 offset:19720
v_cvt_pk_bf16_f32 v154,v156,v157
v_mfma_f32_16x16x32_bf16 v[220:223],a[184:187],a[240:243],v[220:223]
v_cvt_pk_bf16_f32 v155,v158,v159
v_mul_f32 v188,s71,v188
v_mfma_f32_16x16x32_bf16 v[220:223],a[188:191],a[244:247],v[220:223]
v_mul_f32 v189,s71,v189
v_mul_f32 v190,s71,v190
v_mfma_f32_16x16x32_bf16 v[252:255],a[188:191],a[252:255],v[252:255]
v_mul_f32 v191,s71,v191
v_cvt_pk_bf16_f32 v186,v188,v189
v_mfma_f32_16x16x32_bf16 v[252:255],a[184:187],a[248:251],v[252:255]
v_cvt_pk_bf16_f32 v187,v190,v191
v_mul_f32 v205,s71,v205
v_mfma_f32_16x16x32_bf16 v[252:255],a[180:183],a[220:223],v[252:255]
v_mul_f32 v206,s71,v206
v_mul_f32 v207,s71,v207
v_mfma_f32_16x16x32_bf16 v[252:255],a[176:179],a[216:219],v[252:255]
v_cvt_pk_bf16_f32 v200,v200,v201
v_cvt_pk_bf16_f32 v201,v202,v203
v_mfma_f32_16x16x32_bf16 a[0:3],a[64:67],a[192:195],a[0:3]
global_load_lds_dwordx4 v5,s[10:11] offset:2048 nt
v_cvt_pk_bf16_f32 v202,v204,v205
v_mfma_f32_16x16x32_bf16 a[0:3],a[68:71],a[196:199],a[0:3]
v_cvt_pk_bf16_f32 v203,v206,v207
v_mul_f32 v208,s71,v208
v_mfma_f32_16x16x32_bf16 a[0:3],a[72:75],a[224:227],a[0:3]
v_mul_f32 v209,s71,v209
v_mul_f32 v210,s71,v210
v_mfma_f32_16x16x32_bf16 a[0:3],a[76:79],a[228:231],a[0:3]
v_mul_f32 v211,s71,v211
v_mul_f32 v212,s71,v212
v_mfma_f32_16x16x32_bf16 a[16:19],a[76:79],a[236:239],a[16:19]
v_mul_f32 v213,s71,v213
v_mul_f32 v214,s71,v214
v_mfma_f32_16x16x32_bf16 a[16:19],a[72:75],a[232:235],a[16:19]
v_mul_f32 v215,s71,v215
v_cvt_pk_bf16_f32 v208,v208,v209
v_mfma_f32_16x16x32_bf16 a[16:19],a[68:71],a[204:207],a[16:19]
v_accvgpr_read_b32 v8,a0
v_accvgpr_read_b32 v9,a1
v_mfma_f32_16x16x32_bf16 a[16:19],a[64:67],a[200:203],a[16:19]
v_accvgpr_read_b32 v10,a2
v_accvgpr_read_b32 v11,a3
v_mfma_f32_16x16x32_bf16 a[32:35],a[64:67],a[208:211],a[32:35]
v_mul_f32 v8,s71,v8
v_mul_f32 v9,s71,v9
v_mfma_f32_16x16x32_bf16 a[32:35],a[68:71],a[212:215],a[32:35]
v_mul_f32 v10,s71,v10
v_mul_f32 v11,s71,v11
v_mfma_f32_16x16x32_bf16 a[32:35],a[72:75],a[240:243],a[32:35]
v_cvt_pk_bf16_f32 v132,v8,v9
v_cvt_pk_bf16_f32 v133,v10,v11
v_mfma_f32_16x16x32_bf16 a[32:35],a[76:79],a[244:247],a[32:35]
v_accvgpr_read_b32 v16,a16
v_accvgpr_read_b32 v17,a17
v_mfma_f32_16x16x32_bf16 a[48:51],a[76:79],a[252:255],a[48:51]
ds_read_b64_tr_b16 a[76:77],v52 offset:17920
v_accvgpr_read_b32 v18,a18
v_mfma_f32_16x16x32_bf16 a[48:51],a[72:75],a[248:251],a[48:51]
ds_read_b64_tr_b16 a[72:73],v52 offset:1536
v_accvgpr_read_b32 v19,a19
v_mfma_f32_16x16x32_bf16 a[48:51],a[68:71],a[220:223],a[48:51]
ds_read_b64_tr_b16 a[68:69],v52 offset:16896
v_accvgpr_read_b32 v8,a32
v_mfma_f32_16x16x32_bf16 a[48:51],a[64:67],a[216:219],a[48:51]
global_load_lds_dwordx4 v5,s[10:11] offset:3072
ds_read_b64_tr_b16 a[64:65],v52 offset:512
v_mfma_f32_16x16x32_bf16 a[4:7],a[80:83],a[192:195],a[4:7]
v_accvgpr_read_b32 v9,a33
v_accvgpr_read_b32 v10,a34
v_mfma_f32_16x16x32_bf16 a[4:7],a[84:87],a[196:199],a[4:7]
ds_read_b64_tr_b16 a[66:67],v52 offset:2560
v_accvgpr_read_b32 v11,a35
)A"
R"A(v_mfma_f32_16x16x32_bf16 a[4:7],a[88:91],a[224:227],a[4:7]
ds_read_b64_tr_b16 a[70:71],v52 offset:18944
v_mul_f32 v8,s71,v8
v_mfma_f32_16x16x32_bf16 a[4:7],a[92:95],a[228:231],a[4:7]
ds_read_b64_tr_b16 a[74:75],v52 offset:3584
v_mul_f32 v9,s71,v9
v_mfma_f32_16x16x32_bf16 a[20:23],a[92:95],a[236:239],a[20:23]
v_mul_f32 v10,s71,v10
v_mul_f32 v11,s71,v11
v_mfma_f32_16x16x32_bf16 a[20:23],a[88:91],a[232:235],a[20:23]
ds_read_b64_tr_b16 a[78:79],v52 offset:19968
v_cvt_pk_bf16_f32 v196,v8,v9
v_mfma_f32_16x16x32_bf16 a[20:23],a[84:87],a[204:207],a[20:23]
v_accvgpr_read_b32 v12,a4
v_accvgpr_read_b32 v13,a5
v_mfma_f32_16x16x32_bf16 a[20:23],a[80:83],a[200:203],a[20:23]
v_accvgpr_read_b32 v14,a6
v_accvgpr_read_b32 v15,a7
v_mfma_f32_16x16x32_bf16 a[36:39],a[80:83],a[208:211],a[36:39]
v_mul_f32 v12,s71,v12
v_mul_f32 v13,s71,v13
v_mfma_f32_16x16x32_bf16 a[36:39],a[84:87],a[212:215],a[36:39]
v_mul_f32 v14,s71,v14
v_mul_f32 v15,s71,v15
v_mfma_f32_16x16x32_bf16 a[36:39],a[88:91],a[240:243],a[36:39]
s_add_u32 s10,s24,s74
s_addc_u32 s11,s25,0
s_add_u32 s74,s74,16384
v_mfma_f32_16x16x32_bf16 a[36:39],a[92:95],a[244:247],a[36:39]
s_and_b32 s74,s74,0x3ffff
s_or_b32 s74,s74,s69
v_cvt_pk_bf16_f32 v134,v12,v13
v_mfma_f32_16x16x32_bf16 a[52:55],a[92:95],a[252:255],a[52:55]
s_add_u32 m0,s76,16384
s_sub_u32 s76,s76,32768
ds_read_b64_tr_b16 a[92:93],v52 offset:17928
v_mfma_f32_16x16x32_bf16 a[52:55],a[88:91],a[248:251],a[52:55]
s_and_b32 s76,s76,0x1ffff
ds_read_b64_tr_b16 a[88:89],v52 offset:1544
v_cvt_pk_bf16_f32 v135,v14,v15
v_mfma_f32_16x16x32_bf16 a[52:55],a[84:87],a[220:223],a[52:55]
global_load_lds_dwordx4 v4,s[10:11]
ds_read_b64_tr_b16 a[84:85],v52 offset:16904
v_mfma_f32_16x16x32_bf16 a[52:55],a[80:83],a[216:219],a[52:55]
s_and_b32 s69,s75,0xfffc0000
ds_read_b64_tr_b16 a[80:81],v52 offset:520
v_accvgpr_read_b32 v12,a36
v_mfma_f32_16x16x32_bf16 a[8:11],a[96:99],a[192:195],a[8:11]
s_add_u32 s75,s75,16384
v_accvgpr_read_b32 v13,a37
v_accvgpr_read_b32 v14,a38
v_mfma_f32_16x16x32_bf16 a[8:11],a[100:103],a[196:199],a[8:11]
s_waitcnt lgkmcnt(4)
ds_read_b64_tr_b16 a[82:83],v52 offset:2568
v_accvgpr_read_b32 v15,a39
v_mfma_f32_16x16x32_bf16 a[8:11],a[104:107],a[224:227],a[8:11]
ds_read_b64_tr_b16 a[86:87],v52 offset:18952
v_mul_f32 v12,s71,v12
v_mfma_f32_16x16x32_bf16 a[8:11],a[108:111],a[228:231],a[8:11]
ds_read_b64_tr_b16 a[90:91],v52 offset:3592
v_mul_f32 v13,s71,v13
v_mfma_f32_16x16x32_bf16 a[24:27],a[108:111],a[236:239],a[24:27]
v_mul_f32 v14,s71,v14
v_mul_f32 v15,s71,v15
v_mfma_f32_16x16x32_bf16 a[24:27],a[104:107],a[232:235],a[24:27]
ds_read_b64_tr_b16 a[94:95],v52 offset:19976
v_cvt_pk_bf16_f32 v197,v10,v11
v_mfma_f32_16x16x32_bf16 a[24:27],a[100:103],a[204:207],a[24:27]
v_cvt_pk_bf16_f32 v198,v12,v13
v_cvt_pk_bf16_f32 v199,v14,v15
v_mfma_f32_16x16x32_bf16 a[24:27],a[96:99],a[200:203],a[24:27]
v_accvgpr_read_b32 v8,a8
v_accvgpr_read_b32 v9,a9
v_mfma_f32_16x16x32_bf16 a[40:43],a[96:99],a[208:211],a[40:43]
v_accvgpr_read_b32 v10,a10
v_accvgpr_read_b32 v11,a11
v_mfma_f32_16x16x32_bf16 a[40:43],a[100:103],a[212:215],a[40:43]
v_mul_f32 v8,s71,v8
v_mul_f32 v9,s71,v9
v_mfma_f32_16x16x32_bf16 a[40:43],a[104:107],a[240:243],a[40:43]
v_mul_f32 v10,s71,v10
v_mul_f32 v11,s71,v11
v_mfma_f32_16x16x32_bf16 a[40:43],a[108:111],a[244:247],a[40:43]
v_cvt_pk_bf16_f32 v140,v8,v9
v_cvt_pk_bf16_f32 v141,v10,v11
v_mfma_f32_16x16x32_bf16 a[56:59],a[108:111],a[252:255],a[56:59]
ds_read_b64_tr_b16 a[108:109],v52 offset:18176
v_accvgpr_read_b32 v20,a20
v_mfma_f32_16x16x32_bf16 a[56:59],a[104:107],a[248:251],a[56:59]
global_load_lds_dwordx4 v5,s[10:11] offset:1024
ds_read_b64_tr_b16 a[104:105],v52 offset:1792
v_mfma_f32_16x16x32_bf16 a[56:59],a[100:103],a[220:223],a[56:59]
ds_read_b64_tr_b16 a[100:101],v52 offset:17152
v_accvgpr_read_b32 v21,a21
v_mfma_f32_16x16x32_bf16 a[56:59],a[96:99],a[216:219],a[56:59]
ds_read_b64_tr_b16 a[96:97],v52 offset:768
v_accvgpr_read_b32 v22,a22
v_mfma_f32_16x16x32_bf16 a[12:15],a[112:115],a[192:195],a[12:15]
v_accvgpr_read_b32 v23,a23
v_mul_f32 v16,s71,v16
v_mfma_f32_16x16x32_bf16 a[12:15],a[116:119],a[196:199],a[12:15]
ds_read_b64_tr_b16 a[98:99],v52 offset:2816
v_mul_f32 v17,s71,v17
v_mfma_f32_16x16x32_bf16 a[12:15],a[120:123],a[224:227],a[12:15]
ds_read_b64_tr_b16 a[102:103],v52 offset:19200
v_mul_f32 v18,s71,v18
v_mfma_f32_16x16x32_bf16 a[12:15],a[124:127],a[228:231],a[12:15]
ds_read_b64_tr_b16 a[106:107],v52 offset:3840
v_mul_f32 v19,s71,v19
v_mfma_f32_16x16x32_bf16 a[28:31],a[124:127],a[236:239],a[28:31]
v_mul_f32 v20,s71,v20
v_mul_f32 v21,s71,v21
v_mfma_f32_16x16x32_bf16 a[28:31],a[120:123],a[232:235],a[28:31]
ds_read_b64_tr_b16 a[110:111],v52 offset:20224
v_mul_f32 v22,s71,v22
v_mfma_f32_16x16x32_bf16 a[28:31],a[116:119],a[204:207],a[28:31]
v_accvgpr_read_b32 v12,a12
v_accvgpr_read_b32 v13,a13
v_mfma_f32_16x16x32_bf16 a[28:31],a[112:115],a[200:203],a[28:31]
v_accvgpr_read_b32 v14,a14
v_accvgpr_read_b32 v15,a15
v_mfma_f32_16x16x32_bf16 a[44:47],a[112:115],a[208:211],a[44:47]
v_mul_f32 v12,s71,v12
v_mul_f32 v13,s71,v13
v_mfma_f32_16x16x32_bf16 a[44:47],a[116:119],a[212:215],a[44:47]
v_mul_f32 v14,s71,v14
v_mul_f32 v15,s71,v15
v_mfma_f32_16x16x32_bf16 a[44:47],a[120:123],a[240:243],a[44:47]
v_cvt_pk_bf16_f32 v142,v12,v13
v_cvt_pk_bf16_f32 v143,v14,v15
v_mfma_f32_16x16x32_bf16 a[44:47],a[124:127],a[244:247],a[44:47]
v_mul_f32 v23,s71,v23
v_cvt_pk_bf16_f32 v164,v16,v17
v_mfma_f32_16x16x32_bf16 a[60:63],a[124:127],a[252:255],a[60:63]
global_load_lds_dwordx4 v4,s[10:11] offset:2048
ds_read_b64_tr_b16 a[124:125],v52 offset:18184
v_mfma_f32_16x16x32_bf16 a[60:63],a[120:123],a[248:251],a[60:63]
ds_read_b64_tr_b16 a[120:121],v52 offset:1800
v_cvt_pk_bf16_f32 v165,v18,v19
v_mfma_f32_16x16x32_bf16 a[60:63],a[116:119],a[220:223],a[60:63]
ds_read_b64_tr_b16 a[116:117],v52 offset:17160
v_cvt_pk_bf16_f32 v166,v20,v21
v_mfma_f32_16x16x32_bf16 a[60:63],a[112:115],a[216:219],a[60:63]
ds_read_b64_tr_b16 a[112:113],v52 offset:776
v_cvt_pk_bf16_f32 v167,v22,v23
v_mfma_f32_16x16x32_bf16 v[64:67],a[64:67],a[192:195],v[64:67]
v_accvgpr_read_b32 v16,a48
v_accvgpr_read_b32 v17,a49
v_mfma_f32_16x16x32_bf16 v[64:67],a[68:71],a[196:199],v[64:67]
ds_read_b64_tr_b16 a[114:115],v52 offset:2824
v_accvgpr_read_b32 v18,a50
v_mfma_f32_16x16x32_bf16 v[64:67],a[72:75],a[224:227],v[64:67]
ds_read_b64_tr_b16 a[118:119],v52 offset:19208
v_accvgpr_read_b32 v19,a51
v_mfma_f32_16x16x32_bf16 v[64:67],a[76:79],a[228:231],v[64:67]
ds_read_b64_tr_b16 a[122:123],v52 offset:3848
v_accvgpr_read_b32 v20,a52
v_mfma_f32_16x16x32_bf16 v[80:83],a[76:79],a[236:239],v[80:83]
v_accvgpr_read_b32 v21,a53
v_accvgpr_read_b32 v22,a54
v_mfma_f32_16x16x32_bf16 v[80:83],a[72:75],a[232:235],v[80:83]
ds_read_b64_tr_b16 a[126:127],v52 offset:20232
v_accvgpr_read_b32 v23,a55
v_mfma_f32_16x16x32_bf16 v[80:83],a[68:71],a[204:207],v[80:83]
v_mul_f32 v64,s71,v64
v_mul_f32 v65,s71,v65
v_mfma_f32_16x16x32_bf16 v[80:83],a[64:67],a[200:203],v[80:83]
v_mul_f32 v66,s71,v66
v_mul_f32 v67,s71,v67
v_mfma_f32_16x16x32_bf16 v[96:99],a[64:67],a[208:211],v[96:99]
v_cvt_pk_bf16_f32 v148,v64,v65
v_cvt_pk_bf16_f32 v149,v66,v67
v_mfma_f32_16x16x32_bf16 v[96:99],a[68:71],a[212:215],v[96:99]
v_mul_f32 v16,s71,v16
v_mul_f32 v17,s71,v17
v_mfma_f32_16x16x32_bf16 v[96:99],a[72:75],a[240:243],v[96:99]
v_mul_f32 v18,s71,v18
v_mul_f32 v19,s71,v19
v_mfma_f32_16x16x32_bf16 v[96:99],a[76:79],a[244:247],v[96:99]
global_load_lds_dwordx4 v5,s[10:11] offset:3072
v_mul_f32 v20,s71,v20
v_mfma_f32_16x16x32_bf16 v[112:115],a[76:79],a[252:255],v[112:115]
v_mul_f32 v21,s71,v21
v_mul_f32 v22,s71,v22
v_mfma_f32_16x16x32_bf16 v[112:115],a[72:75],a[248:251],v[112:115]
v_mul_f32 v23,s71,v23
v_cvt_pk_bf16_f32 v228,v16,v17
v_mfma_f32_16x16x32_bf16 v[112:115],a[68:71],a[220:223],v[112:115]
v_cvt_pk_bf16_f32 v229,v18,v19
v_cvt_pk_bf16_f32 v230,v20,v21
v_mfma_f32_16x16x32_bf16 v[112:115],a[64:67],a[216:219],v[112:115]
v_cvt_pk_bf16_f32 v231,v22,v23
v_accvgpr_read_b32 v16,a24
s_waitcnt vmcnt(8) lgkmcnt(0)
s_barrier
v_mfma_f32_16x16x32_bf16 v[68:71],a[80:83],a[192:195],v[68:71]
ds_read_b128 a[128:131],v1
v_accvgpr_read_b32 v17,a25
v_mfma_f32_16x16x32_bf16 v[68:71],a[84:87],a[196:199],v[68:71]
ds_read_b128 a[132:135],v1 offset:256
v_accvgpr_read_b32 v18,a26
v_mfma_f32_16x16x32_bf16 v[68:71],a[88:91],a[224:227],v[68:71]
ds_read_b128 a[136:139],v1 offset:512
v_accvgpr_read_b32 v19,a27
v_mfma_f32_16x16x32_bf16 v[68:71],a[92:95],a[228:231],v[68:71]
ds_read_b128 a[140:143],v1 offset:768
v_accvgpr_read_b32 v20,a28
v_mfma_f32_16x16x32_bf16 v[84:87],a[92:95],a[236:239],v[84:87]
v_accvgpr_read_b32 v21,a29
v_accvgpr_read_b32 v22,a30
v_mfma_f32_16x16x32_bf16 v[84:87],a[88:91],a[232:235],v[84:87]
v_accvgpr_read_b32 v23,a31
v_mul_f32 v16,s71,v16
v_mfma_f32_16x16x32_bf16 v[84:87],a[84:87],a[204:207],v[84:87]
s_add_u32 s10,s20,s74
s_addc_u32 s11,s21,0
v_mul_f32 v68,s71,v68
v_mfma_f32_16x16x32_bf16 v[84:87],a[80:83],a[200:203],v[84:87]
v_mul_f32 v69,s71,v69
v_mul_f32 v70,s71,v70
v_mfma_f32_16x16x32_bf16 v[100:103],a[80:83],a[208:211],v[100:103]
s_add_u32 m0,s76,0
v_mul_f32 v71,s71,v71
v_cvt_pk_bf16_f32 v150,v68,v69
v_mfma_f32_16x16x32_bf16 v[100:103],a[84:87],a[212:215],v[100:103]
v_cvt_pk_bf16_f32 v151,v70,v71
v_mul_f32 v17,s71,v17
v_mfma_f32_16x16x32_bf16 v[100:103],a[88:91],a[240:243],v[100:103]
global_load_lds_dwordx4 v4,s[10:11] nt
v_mul_f32 v18,s71,v18
v_mfma_f32_16x16x32_bf16 v[100:103],a[92:95],a[244:247],v[100:103]
v_mul_f32 v19,s71,v19
v_mul_f32 v20,s71,v20
v_mfma_f32_16x16x32_bf16 v[116:119],a[92:95],a[252:255],v[116:119]
v_mul_f32 v21,s71,v21
v_mul_f32 v22,s71,v22
v_mfma_f32_16x16x32_bf16 v[116:119],a[88:91],a[248:251],v[116:119]
v_mul_f32 v23,s71,v23
v_cvt_pk_bf16_f32 v172,v16,v17
v_mfma_f32_16x16x32_bf16 v[116:119],a[84:87],a[220:223],v[116:119]
v_cvt_pk_bf16_f32 v173,v18,v19
v_cvt_pk_bf16_f32 v174,v20,v21
v_mfma_f32_16x16x32_bf16 v[116:119],a[80:83],a[216:219],v[116:119]
v_cvt_pk_bf16_f32 v175,v22,v23
v_mul_f32 v80,s71,v80
v_mfma_f32_16x16x32_bf16 v[72:75],a[96:99],a[192:195],v[72:75]
v_mul_f32 v81,s71,v81
v_mul_f32 v82,s71,v82
v_mfma_f32_16x16x32_bf16 v[72:75],a[100:103],a[196:199],v[72:75]
v_mul_f32 v83,s71,v83
v_mul_f32 v84,s71,v84
v_mfma_f32_16x16x32_bf16 v[72:75],a[104:107],a[224:227],v[72:75]
v_mul_f32 v85,s71,v85
v_mul_f32 v86,s71,v86
v_mfma_f32_16x16x32_bf16 v[72:75],a[108:111],a[228:231],v[72:75]
v_mul_f32 v87,s71,v87
v_cvt_pk_bf16_f32 v180,v80,v81
v_mfma_f32_16x16x32_bf16 v[88:91],a[108:111],a[236:239],v[88:91]
v_cvt_pk_bf16_f32 v181,v82,v83
v_cvt_pk_bf16_f32 v182,v84,v85
v_mfma_f32_16x16x32_bf16 v[88:91],a[104:107],a[232:235],v[88:91]
v_cvt_pk_bf16_f32 v183,v86,v87
v_accvgpr_read_b32 v8,a40
v_mfma_f32_16x16x32_bf16 v[88:91],a[100:103],a[204:207],v[88:91]
v_mul_f32 v72,s71,v72
v_mul_f32 v73,s71,v73
v_mfma_f32_16x16x32_bf16 v[88:91],a[96:99],a[200:203],v[88:91]
v_mul_f32 v74,s71,v74
v_mul_f32 v75,s71,v75
v_mfma_f32_16x16x32_bf16 v[104:107],a[96:99],a[208:211],v[104:107]
global_load_lds_dwordx4 v4,s[10:11] offset:1024 nt
v_cvt_pk_bf16_f32 v156,v72,v73
v_mfma_f32_16x16x32_bf16 v[104:107],a[100:103],a[212:215],v[104:107]
v_cvt_pk_bf16_f32 v157,v74,v75
v_accvgpr_read_b32 v9,a41
v_mfma_f32_16x16x32_bf16 v[104:107],a[104:107],a[240:243],v[104:107]
v_mul_f32 v88,s71,v88
v_mul_f32 v89,s71,v89
v_mfma_f32_16x16x32_bf16 v[104:107],a[108:111],a[244:247],v[104:107]
v_mul_f32 v90,s71,v90
v_mul_f32 v91,s71,v91
v_mfma_f32_16x16x32_bf16 v[120:123],a[108:111],a[252:255],v[120:123]
v_cvt_pk_bf16_f32 v188,v88,v89
v_cvt_pk_bf16_f32 v189,v90,v91
v_mfma_f32_16x16x32_bf16 v[120:123],a[104:107],a[248:251],v[120:123]
v_accvgpr_read_b32 v10,a42
v_accvgpr_read_b32 v11,a43
v_mfma_f32_16x16x32_bf16 v[120:123],a[100:103],a[220:223],v[120:123]
v_accvgpr_read_b32 v12,a44
v_accvgpr_read_b32 v13,a45
v_mfma_f32_16x16x32_bf16 v[120:123],a[96:99],a[216:219],v[120:123]
v_accvgpr_read_b32 v14,a46
v_accvgpr_read_b32 v15,a47
v_mfma_f32_16x16x32_bf16 v[76:79],a[112:115],a[192:195],v[76:79]
v_mul_f32 v8,s71,v8
v_mul_f32 v9,s71,v9
v_mfma_f32_16x16x32_bf16 v[76:79],a[116:119],a[196:199],v[76:79]
v_mul_f32 v10,s71,v10
v_mul_f32 v11,s71,v11
v_mfma_f32_16x16x32_bf16 v[76:79],a[120:123],a[224:227],v[76:79]
)A"
R"A(v_mul_f32 v12,s71,v12
v_mul_f32 v13,s71,v13
v_mfma_f32_16x16x32_bf16 v[76:79],a[124:127],a[228:231],v[76:79]
v_mul_f32 v14,s71,v14
v_mul_f32 v15,s71,v15
v_mfma_f32_16x16x32_bf16 v[92:95],a[124:127],a[236:239],v[92:95]
v_cvt_pk_bf16_f32 v204,v8,v9
v_cvt_pk_bf16_f32 v205,v10,v11
v_mfma_f32_16x16x32_bf16 v[92:95],a[120:123],a[232:235],v[92:95]
v_cvt_pk_bf16_f32 v206,v12,v13
v_cvt_pk_bf16_f32 v207,v14,v15
v_mfma_f32_16x16x32_bf16 v[92:95],a[116:119],a[204:207],v[92:95]
global_load_lds_dwordx4 v5,s[10:11] offset:2048 nt
v_mul_f32 v76,s71,v76
v_mfma_f32_16x16x32_bf16 v[92:95],a[112:115],a[200:203],v[92:95]
v_mul_f32 v77,s71,v77
v_mul_f32 v78,s71,v78
v_mfma_f32_16x16x32_bf16 v[108:111],a[112:115],a[208:211],v[108:111]
v_mul_f32 v79,s71,v79
v_cvt_pk_bf16_f32 v158,v76,v77
v_mfma_f32_16x16x32_bf16 v[108:111],a[116:119],a[212:215],v[108:111]
ds_read_b128 a[156:159],v1 offset:4864
v_cvt_pk_bf16_f32 v159,v78,v79
v_mfma_f32_16x16x32_bf16 v[108:111],a[120:123],a[240:243],v[108:111]
v_mul_f32 v92,s71,v92
v_mul_f32 v93,s71,v93
v_mfma_f32_16x16x32_bf16 v[108:111],a[124:127],a[244:247],v[108:111]
v_mul_f32 v94,s71,v94
v_mul_f32 v95,s71,v95
v_mfma_f32_16x16x32_bf16 v[124:127],a[124:127],a[252:255],v[124:127]
v_cvt_pk_bf16_f32 v190,v92,v93
v_cvt_pk_bf16_f32 v191,v94,v95
v_mfma_f32_16x16x32_bf16 v[124:127],a[120:123],a[248:251],v[124:127]
v_cvt_pk_bf16_f32 v209,v210,v211
v_cvt_pk_bf16_f32 v210,v212,v213
v_mfma_f32_16x16x32_bf16 v[124:127],a[116:119],a[220:223],v[124:127]
ds_read_b128 a[152:155],v1 offset:4608
v_cvt_pk_bf16_f32 v211,v214,v215
v_mfma_f32_16x16x32_bf16 v[124:127],a[112:115],a[216:219],v[124:127]
v_mul_f32 v96,s71,v96
v_mul_f32 v97,s71,v97
s_waitcnt lgkmcnt(2)
v_mfma_f32_16x16x32_bf16 v[64:67],a[128:131],v[132:135],0
v_mul_f32 v98,s71,v98
v_mul_f32 v99,s71,v99
v_mfma_f32_16x16x32_bf16 v[64:67],a[132:135],v[140:143],v[64:67]
v_mul_f32 v100,s71,v100
v_mul_f32 v101,s71,v101
v_mfma_f32_16x16x32_bf16 v[64:67],a[136:139],v[148:151],v[64:67]
v_mul_f32 v102,s71,v102
v_mul_f32 v103,s71,v103
v_mfma_f32_16x16x32_bf16 v[64:67],a[140:143],v[156:159],v[64:67]
global_load_lds_dwordx4 v5,s[10:11] offset:3072
v_cvt_pk_bf16_f32 v212,v96,v97
v_mfma_f32_16x16x32_bf16 v[80:83],a[128:131],v[164:167],0
v_cvt_pk_bf16_f32 v213,v98,v99
v_cvt_pk_bf16_f32 v214,v100,v101
v_mfma_f32_16x16x32_bf16 v[80:83],a[132:135],v[172:175],v[80:83]
v_cvt_pk_bf16_f32 v215,v102,v103
v_mul_f32 v216,s71,v216
v_mfma_f32_16x16x32_bf16 v[80:83],a[136:139],v[180:183],v[80:83]
v_max3_f32 v64,v64,v65,v66
v_max_f32 v64,v64,v67
v_mov_b32 v65,v64
v_mfma_f32_16x16x32_bf16 v[80:83],a[140:143],v[188:191],v[80:83]
v_mul_f32 v217,s71,v217
v_mul_f32 v218,s71,v218
v_mfma_f32_16x16x32_bf16 v[96:99],a[128:131],v[196:199],0
v_permlane32_swap_b32 v64,v65
v_max_f32 v64,v64,v65
v_mov_b32 v65,v64
v_mul_f32 v219,s71,v219
v_mul_f32 v220,s71,v220
v_mul_f32 v221,s71,v221
v_mul_f32 v222,s71,v222
v_mul_f32 v223,s71,v223
v_cvt_pk_bf16_f32 v216,v216,v217
v_cvt_pk_bf16_f32 v217,v218,v219
v_cvt_pk_bf16_f32 v218,v220,v221
v_cvt_pk_bf16_f32 v219,v222,v223
v_mul_f32 v104,s71,v104
v_mul_f32 v105,s71,v105
v_mul_f32 v106,s71,v106
v_mul_f32 v107,s71,v107
v_mul_f32 v108,s71,v108
v_mul_f32 v109,s71,v109
v_mul_f32 v110,s71,v110
v_mul_f32 v111,s71,v111
v_cvt_pk_bf16_f32 v220,v104,v105
v_cvt_pk_bf16_f32 v221,v106,v107
v_cvt_pk_bf16_f32 v222,v108,v109
v_cvt_pk_bf16_f32 v223,v110,v111
v_mfma_f32_16x16x32_bf16 v[96:99],a[132:135],v[204:207],v[96:99]
ds_read_b128 a[148:151],v1 offset:4352
v_max3_f32 v80,v80,v81,v82
v_mfma_f32_16x16x32_bf16 v[96:99],a[136:139],v[212:215],v[96:99]
ds_read_b128 a[144:147],v1 offset:4096
v_permlane16_swap_b32 v64,v65
v_max_f32 v64,v64,v65
v_sub_f32 v35,0,v64
v_sub_f32 v34,0,v64
v_sub_f32 v33,0,v64
v_sub_f32 v32,0,v64
v_mfma_f32_16x16x32_bf16 v[96:99],a[140:143],v[220:223],v[96:99]
ds_read_b128 a[160:163],v1 offset:8192
v_max_f32 v80,v80,v83
v_mfma_f32_16x16x32_bf16 v[64:67],a[128:131],v[132:135],v[32:35]
s_waitcnt lgkmcnt(3)
ds_read_b128 a[164:167],v1 offset:8448
v_mov_b32 v81,v80
v_mfma_f32_16x16x32_bf16 v[64:67],a[132:135],v[140:143],v[64:67]
s_add_u32 s10,s24,s74
s_addc_u32 s11,s25,0
v_mul_f32 v232,s71,v232
L_pd4_%=:
s_cbranch_execz L_pd8_%=
v_mfma_f32_16x16x32_bf16 v[64:67],a[136:139],v[148:151],v[64:67]
s_add_u32 s74,s74,16384
ds_read_b128 a[168:171],v1 offset:8704
v_max3_f32 v96,v96,v97,v98
v_mfma_f32_16x16x32_bf16 v[64:67],a[140:143],v[156:159],v[64:67]
s_add_u32 m0,s76,16384
ds_read_b128 a[172:175],v1 offset:8960
v_max_f32 v96,v96,v99
v_mfma_f32_16x16x32_bf16 v[68:71],a[156:159],v[156:159],v[32:35]
s_and_b32 s74,s74,0x3ffff
ds_read_b128 a[188:191],v1 offset:13056
v_mov_b32 v97,v96
v_mfma_f32_16x16x32_bf16 v[68:71],a[152:155],v[148:151],v[68:71]
global_load_lds_dwordx4 v4,s[10:11]
v_mul_f32 v233,s71,v233
s_waitcnt lgkmcnt(6)
v_mfma_f32_16x16x32_bf16 v[68:71],a[148:151],v[140:143],v[68:71]
s_or_b32 s74,s74,s69
ds_read_b128 a[184:187],v1 offset:12800
v_mul_f32 v234,s71,v234
s_waitcnt lgkmcnt(6)
v_mfma_f32_16x16x32_bf16 v[68:71],a[144:147],v[132:135],v[68:71]
s_sub_u32 s76,s76,32768
ds_read_b128 a[180:183],v1 offset:12544
v_mul_f32 v235,s71,v235
s_waitcnt lgkmcnt(6)
v_mfma_f32_16x16x32_bf16 v[72:75],a[160:163],v[132:135],v[32:35]
s_and_b32 s76,s76,0x1ffff
ds_read_b128 a[176:179],v1 offset:12288
v_mul_f32 v236,s71,v236
s_waitcnt lgkmcnt(6)
v_mfma_f32_16x16x32_bf16 v[72:75],a[164:167],v[140:143],v[72:75]
s_and_b32 s69,s75,0xfffc0000
v_permlane32_swap_b32 v80,v81
s_waitcnt lgkmcnt(5)
v_mfma_f32_16x16x32_bf16 v[72:75],a[168:171],v[148:151],v[72:75]
s_add_u32 s75,s75,16384
v_max_f32 v80,v80,v81
v_mov_b32 v81,v80
s_waitcnt lgkmcnt(4)
v_mfma_f32_16x16x32_bf16 v[72:75],a[172:175],v[156:159],v[72:75]
s_waitcnt lgkmcnt(3)
v_mul_f32 v237,s71,v237
v_mul_f32 v238,s71,v238
v_mfma_f32_16x16x32_bf16 v[76:79],a[188:191],v[156:159],v[32:35]
s_waitcnt lgkmcnt(2)
v_permlane16_swap_b32 v80,v81
v_mfma_f32_16x16x32_bf16 v[76:79],a[184:187],v[148:151],v[76:79]
s_waitcnt lgkmcnt(1)
v_max_f32 v80,v80,v81
v_sub_f32 v39,0,v80
v_sub_f32 v38,0,v80
v_sub_f32 v37,0,v80
v_sub_f32 v36,0,v80
v_mfma_f32_16x16x32_bf16 v[76:79],a[180:183],v[140:143],v[76:79]
s_waitcnt lgkmcnt(0)
v_mul_f32 v239,s71,v239
v_cvt_pk_bf16_f32 v232,v232,v233
v_mfma_f32_16x16x32_bf16 v[76:79],a[176:179],v[132:135],v[76:79]
v_cvt_pk_bf16_f32 v233,v234,v235
v_cvt_pk_bf16_f32 v234,v236,v237
v_mfma_f32_16x16x32_bf16 v[80:83],a[128:131],v[164:167],v[36:39]
v_cvt_pk_bf16_f32 v235,v238,v239
v_accvgpr_read_b32 v16,a56
v_mfma_f32_16x16x32_bf16 v[80:83],a[132:135],v[172:175],v[80:83]
v_accvgpr_read_b32 v17,a57
v_accvgpr_read_b32 v18,a58
v_mfma_f32_16x16x32_bf16 v[80:83],a[136:139],v[180:183],v[80:83]
v_accvgpr_read_b32 v19,a59
v_accvgpr_read_b32 v20,a60
v_mfma_f32_16x16x32_bf16 v[80:83],a[140:143],v[188:191],v[80:83]
global_load_lds_dwordx4 v5,s[10:11] offset:1024
v_accvgpr_read_b32 v21,a61
v_mfma_f32_16x16x32_bf16 v[84:87],a[156:159],v[188:191],v[36:39]
v_accvgpr_read_b32 v22,a62
v_accvgpr_read_b32 v23,a63
v_mfma_f32_16x16x32_bf16 v[84:87],a[152:155],v[180:183],v[84:87]
v_mul_f32 v16,s71,v16
v_mul_f32 v17,s71,v17
v_mfma_f32_16x16x32_bf16 v[84:87],a[148:151],v[172:175],v[84:87]
v_mul_f32 v18,s71,v18
v_mul_f32 v19,s71,v19
v_mfma_f32_16x16x32_bf16 v[84:87],a[144:147],v[164:167],v[84:87]
v_mul_f32 v20,s71,v20
v_mul_f32 v21,s71,v21
v_mfma_f32_16x16x32_bf16 v[88:91],a[160:163],v[164:167],v[36:39]
v_mul_f32 v22,s71,v22
v_mul_f32 v23,s71,v23
v_mfma_f32_16x16x32_bf16 v[88:91],a[164:167],v[172:175],v[88:91]
v_cvt_pk_bf16_f32 v236,v16,v17
v_cvt_pk_bf16_f32 v237,v18,v19
v_mfma_f32_16x16x32_bf16 v[88:91],a[168:171],v[180:183],v[88:91]
v_cvt_pk_bf16_f32 v238,v20,v21
v_cvt_pk_bf16_f32 v239,v22,v23
v_mfma_f32_16x16x32_bf16 v[88:91],a[172:175],v[188:191],v[88:91]
v_mul_f32 v240,s71,v240
v_mul_f32 v241,s71,v241
v_mfma_f32_16x16x32_bf16 v[92:95],a[188:191],v[188:191],v[36:39]
v_mul_f32 v242,s71,v242
v_mul_f32 v243,s71,v243
v_mfma_f32_16x16x32_bf16 v[92:95],a[184:187],v[180:183],v[92:95]
v_mul_f32 v244,s71,v244
v_mul_f32 v245,s71,v245
v_mfma_f32_16x16x32_bf16 v[92:95],a[180:183],v[172:175],v[92:95]
v_mul_f32 v246,s71,v246
v_mul_f32 v247,s71,v247
v_mfma_f32_16x16x32_bf16 v[92:95],a[176:179],v[164:167],v[92:95]
v_cvt_pk_bf16_f32 v240,v240,v241
v_cvt_pk_bf16_f32 v241,v242,v243
v_cvt_pk_bf16_f32 v242,v244,v245
v_cvt_pk_bf16_f32 v243,v246,v247
v_mul_f32 v112,s71,v112
v_mul_f32 v113,s71,v113
v_mul_f32 v114,s71,v114
v_mul_f32 v115,s71,v115
v_mul_f32 v116,s71,v116
v_mul_f32 v117,s71,v117
v_mul_f32 v118,s71,v118
v_mul_f32 v119,s71,v119
v_cvt_pk_bf16_f32 v244,v112,v113
v_cvt_pk_bf16_f32 v245,v114,v115
v_cvt_pk_bf16_f32 v246,v116,v117
v_cvt_pk_bf16_f32 v247,v118,v119
v_permlane32_swap_b32 v96,v97
v_max_f32 v96,v96,v97
v_mov_b32 v97,v96
v_mfma_f32_16x16x32_bf16 v[112:115],a[128:131],v[228:231],0
v_mul_f32 v248,s71,v248
v_mul_f32 v249,s71,v249
v_mul_f32 v250,s71,v250
v_mul_f32 v251,s71,v251
v_mul_f32 v252,s71,v252
v_mul_f32 v253,s71,v253
v_mul_f32 v254,s71,v254
v_mul_f32 v255,s71,v255
v_cvt_pk_bf16_f32 v248,v248,v249
v_cvt_pk_bf16_f32 v249,v250,v251
v_cvt_pk_bf16_f32 v250,v252,v253
v_cvt_pk_bf16_f32 v251,v254,v255
v_mul_f32 v120,s71,v120
v_mul_f32 v121,s71,v121
v_mul_f32 v122,s71,v122
v_mul_f32 v123,s71,v123
v_mul_f32 v124,s71,v124
v_mul_f32 v125,s71,v125
v_mul_f32 v126,s71,v126
v_mul_f32 v127,s71,v127
v_cvt_pk_bf16_f32 v252,v120,v121
v_cvt_pk_bf16_f32 v253,v122,v123
v_cvt_pk_bf16_f32 v254,v124,v125
v_cvt_pk_bf16_f32 v255,v126,v127
v_mfma_f32_16x16x32_bf16 v[112:115],a[132:135],v[236:239],v[112:115]
global_load_lds_dwordx4 v4,s[10:11] offset:2048
v_permlane16_swap_b32 v96,v97
v_max_f32 v96,v96,v97
v_sub_f32 v43,0,v96
v_sub_f32 v42,0,v96
v_sub_f32 v41,0,v96
v_sub_f32 v40,0,v96
v_mfma_f32_16x16x32_bf16 v[112:115],a[136:139],v[244:247],v[112:115]
ds_read_b64_tr_b16 a[192:193],v2
ds_read_b64_tr_b16 a[194:195],v2 offset:4096
v_mfma_f32_16x16x32_bf16 v[112:115],a[140:143],v[252:255],v[112:115]
ds_read_b64_tr_b16 a[196:197],v2 offset:8192
ds_read_b64_tr_b16 a[198:199],v2 offset:12288
v_mfma_f32_16x16x32_bf16 v[96:99],a[128:131],v[196:199],v[40:43]
ds_read_b64_tr_b16 a[200:201],v3
ds_read_b64_tr_b16 a[202:203],v3 offset:4096
v_mfma_f32_16x16x32_bf16 v[96:99],a[132:135],v[204:207],v[96:99]
ds_read_b64_tr_b16 a[204:205],v3 offset:8192
ds_read_b64_tr_b16 a[206:207],v3 offset:12288
v_mfma_f32_16x16x32_bf16 v[96:99],a[136:139],v[212:215],v[96:99]
ds_read_b64_tr_b16 a[208:209],v2 offset:256
v_max3_f32 v112,v112,v113,v114
v_mfma_f32_16x16x32_bf16 v[96:99],a[140:143],v[220:223],v[96:99]
ds_read_b64_tr_b16 a[210:211],v2 offset:4352
ds_read_b64_tr_b16 a[212:213],v2 offset:8448
v_mfma_f32_16x16x32_bf16 v[100:103],a[156:159],v[220:223],v[40:43]
ds_read_b64_tr_b16 a[214:215],v2 offset:12544
v_max_f32 v112,v112,v115
v_mfma_f32_16x16x32_bf16 v[100:103],a[152:155],v[212:215],v[100:103]
ds_read_b64_tr_b16 a[216:217],v3 offset:256
ds_read_b64_tr_b16 a[218:219],v3 offset:4352
v_mfma_f32_16x16x32_bf16 v[100:103],a[148:151],v[204:207],v[100:103]
v_exp_f32 v64,v64
v_mfma_f32_16x16x32_bf16 v[100:103],a[144:147],v[196:199],v[100:103]
v_exp_f32 v65,v65
v_mfma_f32_16x16x32_bf16 v[104:107],a[160:163],v[196:199],v[40:43]
v_exp_f32 v66,v66
v_mfma_f32_16x16x32_bf16 v[104:107],a[164:167],v[204:207],v[104:107]
v_exp_f32 v67,v67
v_mfma_f32_16x16x32_bf16 v[104:107],a[168:171],v[212:215],v[104:107]
v_exp_f32 v68,v68
v_mfma_f32_16x16x32_bf16 v[104:107],a[172:175],v[220:223],v[104:107]
global_load_lds_dwordx4 v5,s[10:11] offset:3072
ds_read_b64_tr_b16 a[220:221],v3 offset:8448
v_mfma_f32_16x16x32_bf16 v[108:111],a[188:191],v[220:223],v[40:43]
v_exp_f32 v69,v69
v_exp_f32 v70,v70
v_exp_f32 v71,v71
v_mfma_f32_16x16x32_bf16 v[108:111],a[184:187],v[212:215],v[108:111]
ds_read_b64_tr_b16 a[222:223],v3 offset:12544
v_cvt_pk_bf16_f32 v64,v64,v65
v_cvt_pk_bf16_f32 v65,v66,v67
v_cvt_pk_bf16_f32 v66,v68,v69
v_cvt_pk_bf16_f32 v67,v70,v71
v_exp_f32 v72,v72
v_exp_f32 v73,v73
v_exp_f32 v74,v74
v_exp_f32 v75,v75
v_exp_f32 v76,v76
v_exp_f32 v77,v77
v_exp_f32 v78,v78
v_exp_f32 v79,v79
v_mfma_f32_16x16x32_bf16 v[108:111],a[180:183],v[204:207],v[108:111]
ds_read_b64_tr_b16 a[224:225],v2 offset:512
ds_read_b64_tr_b16 a[226:227],v2 offset:4608
)A"
R"A(v_cvt_pk_bf16_f32 v72,v72,v73
v_cvt_pk_bf16_f32 v73,v74,v75
v_cvt_pk_bf16_f32 v74,v76,v77
v_cvt_pk_bf16_f32 v75,v78,v79
v_mfma_f32_16x16x32_bf16 v[108:111],a[176:179],v[196:199],v[108:111]
s_waitcnt lgkmcnt(4)
ds_read_b64_tr_b16 a[228:229],v2 offset:8704
ds_read_b64_tr_b16 a[230:231],v2 offset:12800
v_mfma_f32_16x16x32_bf16 a[0:3],a[192:195],v[64:67],0
ds_read_b64_tr_b16 a[232:233],v3 offset:512
ds_read_b64_tr_b16 a[234:235],v3 offset:4608
v_mfma_f32_16x16x32_bf16 a[0:3],a[196:199],v[72:75],a[0:3]
ds_read_b64_tr_b16 a[236:237],v3 offset:8704
ds_read_b64_tr_b16 a[238:239],v3 offset:12800
v_mfma_f32_16x16x32_bf16 a[4:7],a[200:203],v[64:67],0
ds_read_b64_tr_b16 a[240:241],v2 offset:768
v_mov_b32 v113,v112
v_mfma_f32_16x16x32_bf16 a[4:7],a[204:207],v[72:75],a[4:7]
ds_read_b64_tr_b16 a[242:243],v2 offset:4864
ds_read_b64_tr_b16 a[244:245],v2 offset:8960
v_mfma_f32_16x16x32_bf16 a[8:11],a[208:211],v[64:67],0
ds_read_b64_tr_b16 a[246:247],v2 offset:13056
v_add_u32 v1,s78,v1
v_mfma_f32_16x16x32_bf16 a[8:11],a[212:215],v[72:75],a[8:11]
ds_read_b64_tr_b16 a[248:249],v3 offset:768
ds_read_b64_tr_b16 a[250:251],v3 offset:4864
v_mfma_f32_16x16x32_bf16 a[12:15],a[216:219],v[64:67],0
s_waitcnt lgkmcnt(12)
ds_read_b64_tr_b16 a[252:253],v3 offset:8960
v_add_u32 v2,s78,v2
v_mfma_f32_16x16x32_bf16 a[12:15],a[220:223],v[72:75],a[12:15]
s_waitcnt lgkmcnt(6)
ds_read_b64_tr_b16 a[254:255],v3 offset:13056
v_add_u32 v3,s78,v3
v_mfma_f32_16x16x32_bf16 a[16:19],a[224:227],v[64:67],0
s_sub_u32 s78,s77,32768
v_permlane32_swap_b32 v112,v113
v_mfma_f32_16x16x32_bf16 a[16:19],a[228:231],v[72:75],a[16:19]
s_and_b32 s78,s78,0x1ffff
v_max_f32 v112,v112,v113
v_mov_b32 v113,v112
v_mfma_f32_16x16x32_bf16 a[20:23],a[232:235],v[64:67],0
s_sub_u32 s78,s78,s77
v_exp_f32 v80,v80
v_mfma_f32_16x16x32_bf16 a[20:23],a[236:239],v[72:75],a[20:23]
s_add_u32 s77,s77,s78
v_permlane16_swap_b32 v112,v113
s_waitcnt lgkmcnt(5)
v_mfma_f32_16x16x32_bf16 a[24:27],a[240:243],v[64:67],0
s_waitcnt lgkmcnt(4)
v_max_f32 v112,v112,v113
v_max_f32 v112,s72,v112
v_mfma_f32_16x16x32_bf16 a[24:27],a[244:247],v[72:75],a[24:27]
s_waitcnt lgkmcnt(2)
v_sub_f32 v47,0,v112
v_sub_f32 v46,0,v112
v_mfma_f32_16x16x32_bf16 a[28:31],a[248:251],v[64:67],0
s_waitcnt lgkmcnt(0)
v_sub_f32 v45,0,v112
v_sub_f32 v44,0,v112
v_mfma_f32_16x16x32_bf16 a[28:31],a[252:255],v[72:75],a[28:31]
v_exp_f32 v81,v81
v_mfma_f32_16x16x32_bf16 v[48:51],v[28:31],v[64:67],0
v_exp_f32 v82,v82
v_mfma_f32_16x16x32_bf16 v[48:51],v[28:31],v[72:75],v[48:51]
v_exp_f32 v83,v83
s_waitcnt vmcnt(8)
s_barrier
v_mfma_f32_16x16x32_bf16 v[112:115],a[128:131],v[228:231],v[44:47]
s_add_u32 s10,s20,s74
s_addc_u32 s11,s21,0
s_add_u32 m0,s76,0
v_mfma_f32_16x16x32_bf16 v[112:115],a[132:135],v[236:239],v[112:115]
v_exp_f32 v84,v84
v_mfma_f32_16x16x32_bf16 v[112:115],a[136:139],v[244:247],v[112:115]
global_load_lds_dwordx4 v4,s[10:11] nt
v_cvt_pk_bf16_f32 v80,v80,v81
v_mfma_f32_16x16x32_bf16 v[112:115],a[140:143],v[252:255],v[112:115]
v_exp_f32 v85,v85
v_mfma_f32_16x16x32_bf16 v[116:119],a[156:159],v[252:255],v[44:47]
v_exp_f32 v86,v86
v_mfma_f32_16x16x32_bf16 v[116:119],a[152:155],v[244:247],v[116:119]
v_exp_f32 v87,v87
v_mfma_f32_16x16x32_bf16 v[116:119],a[148:151],v[236:239],v[116:119]
v_cvt_pk_bf16_f32 v81,v82,v83
v_cvt_pk_bf16_f32 v82,v84,v85
v_mfma_f32_16x16x32_bf16 v[116:119],a[144:147],v[228:231],v[116:119]
ds_read_b128 a[128:131],v1
v_cvt_pk_bf16_f32 v83,v86,v87
v_mfma_f32_16x16x32_bf16 v[120:123],a[160:163],v[228:231],v[44:47]
v_exp_f32 v88,v88
v_mfma_f32_16x16x32_bf16 v[120:123],a[164:167],v[236:239],v[120:123]
v_exp_f32 v89,v89
v_mfma_f32_16x16x32_bf16 v[120:123],a[168:171],v[244:247],v[120:123]
v_exp_f32 v90,v90
v_mfma_f32_16x16x32_bf16 v[120:123],a[172:175],v[252:255],v[120:123]
v_exp_f32 v91,v91
v_mfma_f32_16x16x32_bf16 v[124:127],a[188:191],v[252:255],v[44:47]
v_exp_f32 v92,v92
v_mfma_f32_16x16x32_bf16 v[124:127],a[184:187],v[244:247],v[124:127]
v_exp_f32 v93,v93
v_exp_f32 v94,v94
v_exp_f32 v95,v95
v_mfma_f32_16x16x32_bf16 v[124:127],a[180:183],v[236:239],v[124:127]
v_cvt_pk_bf16_f32 v88,v88,v89
v_cvt_pk_bf16_f32 v89,v90,v91
v_cvt_pk_bf16_f32 v90,v92,v93
v_cvt_pk_bf16_f32 v91,v94,v95
v_mfma_f32_16x16x32_bf16 v[124:127],a[176:179],v[228:231],v[124:127]
v_exp_f32 v96,v96
v_mfma_f32_16x16x32_bf16 a[32:35],a[192:195],v[80:83],0
global_load_lds_dwordx4 v4,s[10:11] offset:1024 nt
ds_read_b128 a[132:135],v1 offset:256
v_mfma_f32_16x16x32_bf16 a[32:35],a[196:199],v[88:91],a[32:35]
v_exp_f32 v97,v97
v_mfma_f32_16x16x32_bf16 a[36:39],a[200:203],v[80:83],0
ds_read_b128 a[136:139],v1 offset:512
v_cvt_pk_bf16_f32 v96,v96,v97
v_mfma_f32_16x16x32_bf16 a[36:39],a[204:207],v[88:91],a[36:39]
v_exp_f32 v98,v98
v_mfma_f32_16x16x32_bf16 a[40:43],a[208:211],v[80:83],0
v_exp_f32 v99,v99
v_mfma_f32_16x16x32_bf16 a[40:43],a[212:215],v[88:91],a[40:43]
ds_read_b128 a[140:143],v1 offset:768
v_cvt_pk_bf16_f32 v97,v98,v99
v_mfma_f32_16x16x32_bf16 a[44:47],a[216:219],v[80:83],0
v_exp_f32 v100,v100
v_mfma_f32_16x16x32_bf16 a[44:47],a[220:223],v[88:91],a[44:47]
v_exp_f32 v101,v101
v_mfma_f32_16x16x32_bf16 a[48:51],a[224:227],v[80:83],0
ds_read_b128 a[156:159],v1 offset:4864
v_cvt_pk_bf16_f32 v98,v100,v101
v_mfma_f32_16x16x32_bf16 a[48:51],a[228:231],v[88:91],a[48:51]
v_exp_f32 v102,v102
v_mfma_f32_16x16x32_bf16 a[52:55],a[232:235],v[80:83],0
v_exp_f32 v103,v103
v_mfma_f32_16x16x32_bf16 a[52:55],a[236:239],v[88:91],a[52:55]
ds_read_b128 a[152:155],v1 offset:4608
v_cvt_pk_bf16_f32 v99,v102,v103
v_mfma_f32_16x16x32_bf16 a[56:59],a[240:243],v[80:83],0
ds_read_b128 a[148:151],v1 offset:4352
v_mfma_f32_16x16x32_bf16 a[56:59],a[244:247],v[88:91],a[56:59]
ds_read_b128 a[144:147],v1 offset:4096
v_mfma_f32_16x16x32_bf16 a[60:63],a[248:251],v[80:83],0
global_load_lds_dwordx4 v5,s[10:11] offset:2048 nt
ds_read_b128 a[160:163],v1 offset:8192
v_mfma_f32_16x16x32_bf16 a[60:63],a[252:255],v[88:91],a[60:63]
v_exp_f32 v104,v104
v_mfma_f32_16x16x32_bf16 v[52:55],v[28:31],v[80:83],0
ds_read_b128 a[164:167],v1 offset:8448
v_mfma_f32_16x16x32_bf16 v[52:55],v[28:31],v[88:91],v[52:55]
s_waitcnt lgkmcnt(5)
ds_read_b128 a[168:171],v1 offset:8704
v_mfma_f32_16x16x32_bf16 v[64:67],a[128:131],v[132:135],v[32:35]
ds_read_b128 a[172:175],v1 offset:8960
v_mfma_f32_16x16x32_bf16 v[64:67],a[132:135],v[140:143],v[64:67]
v_exp_f32 v105,v105
v_mfma_f32_16x16x32_bf16 v[64:67],a[136:139],v[148:151],v[64:67]
ds_read_b128 a[188:191],v1 offset:13056
v_cvt_pk_bf16_f32 v104,v104,v105
v_mfma_f32_16x16x32_bf16 v[64:67],a[140:143],v[156:159],v[64:67]
ds_read_b128 a[184:187],v1 offset:12800
v_mfma_f32_16x16x32_bf16 v[68:71],a[156:159],v[156:159],v[32:35]
s_waitcnt lgkmcnt(5)
ds_read_b128 a[180:183],v1 offset:12544
v_mfma_f32_16x16x32_bf16 v[68:71],a[152:155],v[148:151],v[68:71]
v_exp_f32 v106,v106
v_mfma_f32_16x16x32_bf16 v[68:71],a[148:151],v[140:143],v[68:71]
ds_read_b128 a[176:179],v1 offset:12288
v_add_u32 v1,s78,v1
v_mfma_f32_16x16x32_bf16 v[68:71],a[144:147],v[132:135],v[68:71]
v_exp_f32 v107,v107
v_mfma_f32_16x16x32_bf16 v[72:75],a[160:163],v[132:135],v[32:35]
s_waitcnt lgkmcnt(4)
v_exp_f32 v108,v108
v_mfma_f32_16x16x32_bf16 v[72:75],a[164:167],v[140:143],v[72:75]
v_exp_f32 v109,v109
v_mfma_f32_16x16x32_bf16 v[72:75],a[168:171],v[148:151],v[72:75]
global_load_lds_dwordx4 v5,s[10:11] offset:3072
v_cvt_pk_bf16_f32 v105,v106,v107
v_mfma_f32_16x16x32_bf16 v[72:75],a[172:175],v[156:159],v[72:75]
s_waitcnt lgkmcnt(2)
v_exp_f32 v110,v110
v_mfma_f32_16x16x32_bf16 v[76:79],a[188:191],v[156:159],v[32:35]
v_exp_f32 v111,v111
v_mfma_f32_16x16x32_bf16 v[76:79],a[184:187],v[148:151],v[76:79]
s_waitcnt lgkmcnt(1)
v_cvt_pk_bf16_f32 v106,v108,v109
v_cvt_pk_bf16_f32 v107,v110,v111
v_mfma_f32_16x16x32_bf16 v[76:79],a[180:183],v[140:143],v[76:79]
s_waitcnt lgkmcnt(0)
v_exp_f32 v112,v112
v_mfma_f32_16x16x32_bf16 v[76:79],a[176:179],v[132:135],v[76:79]
v_exp_f32 v113,v113
v_mfma_f32_16x16x32_bf16 a[64:67],a[192:195],v[96:99],0
v_exp_f32 v114,v114
v_mfma_f32_16x16x32_bf16 a[64:67],a[196:199],v[104:107],a[64:67]
v_exp_f32 v115,v115
v_mfma_f32_16x16x32_bf16 a[68:71],a[200:203],v[96:99],0
v_exp_f32 v116,v116
v_mfma_f32_16x16x32_bf16 a[68:71],a[204:207],v[104:107],a[68:71]
v_exp_f32 v117,v117
v_mfma_f32_16x16x32_bf16 a[72:75],a[208:211],v[96:99],0
s_add_u32 s10,s24,s74
s_addc_u32 s11,s25,0
s_add_u32 s74,s74,16384
s_and_b32 s74,s74,0x3ffff
s_or_b32 s74,s74,s69
s_lshl_b32 s69,s60,13
s_add_u32 s69,s69,131072
v_add_u32 v8,s69,v6
v_mfma_f32_16x16x32_bf16 a[72:75],a[212:215],v[104:107],a[72:75]
s_lshl_b32 s69,s60,13
ds_write_b128 v8,v[24:27] offset:4096
v_mfma_f32_16x16x32_bf16 a[76:79],a[216:219],v[96:99],0
s_add_u32 m0,s76,16384
ds_write_b32 v8,v7 offset:5120
v_cvt_pk_bf16_f32 v112,v112,v113
v_mfma_f32_16x16x32_bf16 a[76:79],a[220:223],v[104:107],a[76:79]
s_sub_u32 s76,s76,32768
v_bfe_u32 v24,v6,4,4
v_cvt_pk_bf16_f32 v113,v114,v115
v_mfma_f32_16x16x32_bf16 a[80:83],a[224:227],v[96:99],0
global_load_lds_dwordx4 v4,s[10:11]
v_lshrrev_b32 v7,8,v6
v_lshlrev_b32 v7,4,v7
v_lshl_add_u32 v7,v24,8,v7
v_mfma_f32_16x16x32_bf16 a[80:83],a[228:231],v[104:107],a[80:83]
s_and_b32 s76,s76,0x1ffff
v_exp_f32 v118,v118
v_mfma_f32_16x16x32_bf16 a[84:87],a[232:235],v[96:99],0
s_add_u32 s69,s69,131072
v_exp_f32 v119,v119
v_mfma_f32_16x16x32_bf16 a[84:87],a[236:239],v[104:107],a[84:87]
v_cvt_pk_bf16_f32 v114,v116,v117
v_cvt_pk_bf16_f32 v115,v118,v119
v_mfma_f32_16x16x32_bf16 a[88:91],a[240:243],v[96:99],0
v_exp_f32 v120,v120
v_mfma_f32_16x16x32_bf16 a[88:91],a[244:247],v[104:107],a[88:91]
buffer_load_dwordx4 v[8:11],v7,s[20:23],s65 offen nt
v_mfma_f32_16x16x32_bf16 a[92:95],a[248:251],v[96:99],0
v_exp_f32 v121,v121
v_mfma_f32_16x16x32_bf16 a[92:95],a[252:255],v[104:107],a[92:95]
v_exp_f32 v122,v122
v_mfma_f32_16x16x32_bf16 v[56:59],v[28:31],v[96:99],0
v_exp_f32 v123,v123
v_mfma_f32_16x16x32_bf16 v[56:59],v[28:31],v[104:107],v[56:59]
v_exp_f32 v124,v124
v_mfma_f32_16x16x32_bf16 v[80:83],a[128:131],v[164:167],v[36:39]
buffer_load_dwordx4 v[12:15],v7,s[20:23],s65 offen offset:64 nt
v_mfma_f32_16x16x32_bf16 v[80:83],a[132:135],v[172:175],v[80:83]
v_exp_f32 v125,v125
v_mfma_f32_16x16x32_bf16 v[80:83],a[136:139],v[180:183],v[80:83]
v_exp_f32 v126,v126
v_mfma_f32_16x16x32_bf16 v[80:83],a[140:143],v[188:191],v[80:83]
v_exp_f32 v127,v127
v_mfma_f32_16x16x32_bf16 v[84:87],a[156:159],v[188:191],v[36:39]
v_cvt_pk_bf16_f32 v120,v120,v121
v_cvt_pk_bf16_f32 v121,v122,v123
v_mfma_f32_16x16x32_bf16 v[84:87],a[152:155],v[180:183],v[84:87]
buffer_load_dwordx4 v[16:19],v7,s[20:23],s65 offen offset:128 nt
v_mfma_f32_16x16x32_bf16 v[84:87],a[148:151],v[172:175],v[84:87]
v_cvt_pk_bf16_f32 v122,v124,v125
v_cvt_pk_bf16_f32 v123,v126,v127
v_mfma_f32_16x16x32_bf16 v[84:87],a[144:147],v[164:167],v[84:87]
v_exp_f32 v64,v64
v_mfma_f32_16x16x32_bf16 v[88:91],a[160:163],v[164:167],v[36:39]
v_exp_f32 v65,v65
v_mfma_f32_16x16x32_bf16 v[88:91],a[164:167],v[172:175],v[88:91]
v_exp_f32 v66,v66
v_mfma_f32_16x16x32_bf16 v[88:91],a[168:171],v[180:183],v[88:91]
v_exp_f32 v67,v67
v_mfma_f32_16x16x32_bf16 v[88:91],a[172:175],v[188:191],v[88:91]
global_load_lds_dwordx4 v5,s[10:11] offset:1024
v_cvt_pk_bf16_f32 v64,v64,v65
v_mfma_f32_16x16x32_bf16 v[92:95],a[188:191],v[188:191],v[36:39]
v_exp_f32 v68,v68
v_mfma_f32_16x16x32_bf16 v[92:95],a[184:187],v[180:183],v[92:95]
v_exp_f32 v69,v69
v_mfma_f32_16x16x32_bf16 v[92:95],a[180:183],v[172:175],v[92:95]
v_exp_f32 v70,v70
v_mfma_f32_16x16x32_bf16 v[92:95],a[176:179],v[164:167],v[92:95]
v_exp_f32 v71,v71
v_mfma_f32_16x16x32_bf16 a[96:99],a[192:195],v[112:115],0
buffer_load_dwordx4 v[20:23],v7,s[20:23],s65 offen offset:192 nt
v_mfma_f32_16x16x32_bf16 a[96:99],a[196:199],v[120:123],a[96:99]
ds_read_b64_tr_b16 a[192:193],v2
ds_read_b64_tr_b16 a[194:195],v2 offset:4096
v_mfma_f32_16x16x32_bf16 a[100:103],a[200:203],v[112:115],0
ds_read_b64_tr_b16 a[196:197],v2 offset:8192
ds_read_b64_tr_b16 a[198:199],v2 offset:12288
v_mfma_f32_16x16x32_bf16 a[100:103],a[204:207],v[120:123],a[100:103]
ds_read_b64_tr_b16 a[200:201],v3
ds_read_b64_tr_b16 a[202:203],v3 offset:4096
v_mfma_f32_16x16x32_bf16 a[104:107],a[208:211],v[112:115],0
)A"
R"A(ds_read_b64_tr_b16 a[204:205],v3 offset:8192
ds_read_b64_tr_b16 a[206:207],v3 offset:12288
v_mfma_f32_16x16x32_bf16 a[104:107],a[212:215],v[120:123],a[104:107]
ds_read_b64_tr_b16 a[208:209],v2 offset:256
v_cvt_pk_bf16_f32 v65,v66,v67
v_mfma_f32_16x16x32_bf16 a[108:111],a[216:219],v[112:115],0
global_load_lds_dwordx4 v4,s[10:11] offset:2048
ds_read_b64_tr_b16 a[210:211],v2 offset:4352
v_mfma_f32_16x16x32_bf16 a[108:111],a[220:223],v[120:123],a[108:111]
ds_read_b64_tr_b16 a[212:213],v2 offset:8448
ds_read_b64_tr_b16 a[214:215],v2 offset:12544
v_mfma_f32_16x16x32_bf16 a[112:115],a[224:227],v[112:115],0
ds_read_b64_tr_b16 a[216:217],v3 offset:256
ds_read_b64_tr_b16 a[218:219],v3 offset:4352
v_mfma_f32_16x16x32_bf16 a[112:115],a[228:231],v[120:123],a[112:115]
v_cvt_pk_bf16_f32 v66,v68,v69
v_cvt_pk_bf16_f32 v67,v70,v71
v_mfma_f32_16x16x32_bf16 a[116:119],a[232:235],v[112:115],0
v_exp_f32 v72,v72
v_mfma_f32_16x16x32_bf16 a[116:119],a[236:239],v[120:123],a[116:119]
v_exp_f32 v73,v73
v_mfma_f32_16x16x32_bf16 a[120:123],a[240:243],v[112:115],0
v_exp_f32 v74,v74
v_mfma_f32_16x16x32_bf16 a[120:123],a[244:247],v[120:123],a[120:123]
v_exp_f32 v75,v75
v_mfma_f32_16x16x32_bf16 a[124:127],a[248:251],v[112:115],0
v_exp_f32 v76,v76
v_mfma_f32_16x16x32_bf16 a[124:127],a[252:255],v[120:123],a[124:127]
v_exp_f32 v77,v77
v_mfma_f32_16x16x32_bf16 v[60:63],v[28:31],v[112:115],0
v_exp_f32 v78,v78
v_mfma_f32_16x16x32_bf16 v[60:63],v[28:31],v[120:123],v[60:63]
global_load_lds_dwordx4 v5,s[10:11] offset:3072
ds_read_b64_tr_b16 a[220:221],v3 offset:8448
v_mfma_f32_16x16x32_bf16 v[96:99],a[128:131],v[196:199],v[40:43]
v_exp_f32 v79,v79
v_mfma_f32_16x16x32_bf16 v[96:99],a[132:135],v[204:207],v[96:99]
ds_read_b64_tr_b16 a[222:223],v3 offset:12544
v_cvt_pk_bf16_f32 v72,v72,v73
v_mfma_f32_16x16x32_bf16 v[96:99],a[136:139],v[212:215],v[96:99]
v_cvt_pk_bf16_f32 v73,v74,v75
v_cvt_pk_bf16_f32 v74,v76,v77
v_mfma_f32_16x16x32_bf16 v[96:99],a[140:143],v[220:223],v[96:99]
ds_read_b64_tr_b16 a[224:225],v2 offset:512
v_cvt_pk_bf16_f32 v75,v78,v79
v_mfma_f32_16x16x32_bf16 v[100:103],a[156:159],v[220:223],v[40:43]
ds_read_b64_tr_b16 a[226:227],v2 offset:4608
ds_read_b64_tr_b16 a[228:229],v2 offset:8704
v_mfma_f32_16x16x32_bf16 v[100:103],a[152:155],v[212:215],v[100:103]
ds_read_b64_tr_b16 a[230:231],v2 offset:12800
ds_read_b64_tr_b16 a[232:233],v3 offset:512
v_mfma_f32_16x16x32_bf16 v[100:103],a[148:151],v[204:207],v[100:103]
ds_read_b64_tr_b16 a[234:235],v3 offset:4608
ds_read_b64_tr_b16 a[236:237],v3 offset:8704
v_mfma_f32_16x16x32_bf16 v[100:103],a[144:147],v[196:199],v[100:103]
ds_read_b64_tr_b16 a[238:239],v3 offset:12800
ds_read_b64_tr_b16 a[240:241],v2 offset:768
v_mfma_f32_16x16x32_bf16 v[104:107],a[160:163],v[196:199],v[40:43]
ds_read_b64_tr_b16 a[242:243],v2 offset:4864
v_add_u32 v7,s69,v6
v_mfma_f32_16x16x32_bf16 v[104:107],a[164:167],v[204:207],v[104:107]
s_add_u32 s10,s20,s74
s_addc_u32 s11,s21,0
ds_read_b64_tr_b16 a[244:245],v2 offset:8960
v_mfma_f32_16x16x32_bf16 v[104:107],a[168:171],v[212:215],v[104:107]
s_and_b32 s69,s75,0xfffc0000
ds_read_b64_tr_b16 a[246:247],v2 offset:13056
ds_read_b64_tr_b16 a[248:249],v3 offset:768
v_mfma_f32_16x16x32_bf16 v[104:107],a[172:175],v[220:223],v[104:107]
s_add_u32 m0,s76,0
ds_read_b64_tr_b16 a[250:251],v3 offset:4864
ds_read_b64_tr_b16 a[252:253],v3 offset:8960
v_mfma_f32_16x16x32_bf16 v[108:111],a[188:191],v[220:223],v[40:43]
s_add_u32 s75,s75,16384
ds_read_b64_tr_b16 a[254:255],v3 offset:13056
v_add_u32 v2,s78,v2
v_mfma_f32_16x16x32_bf16 v[108:111],a[184:187],v[212:215],v[108:111]
global_load_lds_dwordx4 v4,s[10:11] nt
v_add_u32 v3,s78,v3
v_mfma_f32_16x16x32_bf16 v[108:111],a[180:183],v[204:207],v[108:111]
s_sub_u32 s78,s77,32768
v_exp_f32 v80,v80
v_mfma_f32_16x16x32_bf16 v[108:111],a[176:179],v[196:199],v[108:111]
s_and_b32 s78,s78,0x1ffff
v_exp_f32 v81,v81
s_waitcnt lgkmcnt(7)
v_mfma_f32_16x16x32_bf16 a[0:3],a[192:195],v[64:67],a[0:3]
s_sub_u32 s78,s78,s77
v_exp_f32 v82,v82
v_mfma_f32_16x16x32_bf16 a[0:3],a[196:199],v[72:75],a[0:3]
s_add_u32 s77,s77,s78
v_exp_f32 v83,v83
v_mfma_f32_16x16x32_bf16 a[4:7],a[200:203],v[64:67],a[4:7]
v_exp_f32 v84,v84
v_mfma_f32_16x16x32_bf16 a[4:7],a[204:207],v[72:75],a[4:7]
v_exp_f32 v85,v85
v_mfma_f32_16x16x32_bf16 a[8:11],a[208:211],v[64:67],a[8:11]
v_exp_f32 v86,v86
v_mfma_f32_16x16x32_bf16 a[8:11],a[212:215],v[72:75],a[8:11]
v_exp_f32 v87,v87
v_mfma_f32_16x16x32_bf16 a[12:15],a[216:219],v[64:67],a[12:15]
v_cvt_pk_bf16_f32 v80,v80,v81
v_cvt_pk_bf16_f32 v81,v82,v83
v_mfma_f32_16x16x32_bf16 a[12:15],a[220:223],v[72:75],a[12:15]
v_cvt_pk_bf16_f32 v82,v84,v85
v_cvt_pk_bf16_f32 v83,v86,v87
v_mfma_f32_16x16x32_bf16 a[16:19],a[224:227],v[64:67],a[16:19]
v_exp_f32 v88,v88
v_mfma_f32_16x16x32_bf16 a[16:19],a[228:231],v[72:75],a[16:19]
v_exp_f32 v89,v89
L_pd5_%=:
s_cbranch_execz L_trt0_%=
v_mfma_f32_16x16x32_bf16 a[20:23],a[232:235],v[64:67],a[20:23]
v_exp_f32 v90,v90
v_mfma_f32_16x16x32_bf16 a[20:23],a[236:239],v[72:75],a[20:23]
s_waitcnt lgkmcnt(0)
v_exp_f32 v91,v91
v_mfma_f32_16x16x32_bf16 a[24:27],a[240:243],v[64:67],a[24:27]
v_exp_f32 v92,v92
v_mfma_f32_16x16x32_bf16 a[24:27],a[244:247],v[72:75],a[24:27]
v_exp_f32 v93,v93
v_mfma_f32_16x16x32_bf16 a[28:31],a[248:251],v[64:67],a[28:31]
v_exp_f32 v94,v94
v_mfma_f32_16x16x32_bf16 a[28:31],a[252:255],v[72:75],a[28:31]
s_getpc_b64 s[92:93]
s_cmp_lg_u32 s4,0
s_cbranch_scc1 L_pace_%=
v_mfma_f32_16x16x32_bf16 v[48:51],v[28:31],v[64:67],v[48:51]
v_exp_f32 v95,v95
v_mfma_f32_16x16x32_bf16 v[48:51],v[28:31],v[72:75],v[48:51]
s_add_u32 s10,s20,s74
s_addc_u32 s11,s21,0
v_cvt_pk_bf16_f32 v88,v88,v89
s_waitcnt vmcnt(13)
s_barrier
v_mfma_f32_16x16x32_bf16 v[112:115],a[128:131],v[228:231],v[44:47]
s_add_u32 m0,s76,0
v_cvt_pk_bf16_f32 v89,v90,v91
v_cvt_pk_bf16_f32 v90,v92,v93
v_mfma_f32_16x16x32_bf16 v[112:115],a[132:135],v[236:239],v[112:115]
ds_read_b128 a[128:131],v1
v_cvt_pk_bf16_f32 v91,v94,v95
v_mfma_f32_16x16x32_bf16 v[112:115],a[136:139],v[244:247],v[112:115]
global_load_lds_dwordx4 v4,s[10:11] offset:1024 nt
ds_read_b128 a[132:135],v1 offset:256
v_mfma_f32_16x16x32_bf16 v[112:115],a[140:143],v[252:255],v[112:115]
v_exp_f32 v96,v96
v_mfma_f32_16x16x32_bf16 v[116:119],a[156:159],v[252:255],v[44:47]
v_exp_f32 v97,v97
v_mfma_f32_16x16x32_bf16 v[116:119],a[152:155],v[244:247],v[116:119]
ds_read_b128 a[136:139],v1 offset:512
v_cvt_pk_bf16_f32 v96,v96,v97
v_mfma_f32_16x16x32_bf16 v[116:119],a[148:151],v[236:239],v[116:119]
v_exp_f32 v98,v98
v_mfma_f32_16x16x32_bf16 v[116:119],a[144:147],v[228:231],v[116:119]
v_exp_f32 v99,v99
v_mfma_f32_16x16x32_bf16 v[120:123],a[160:163],v[228:231],v[44:47]
ds_read_b128 a[140:143],v1 offset:768
v_cvt_pk_bf16_f32 v97,v98,v99
v_mfma_f32_16x16x32_bf16 v[120:123],a[164:167],v[236:239],v[120:123]
v_exp_f32 v100,v100
v_mfma_f32_16x16x32_bf16 v[120:123],a[168:171],v[244:247],v[120:123]
v_exp_f32 v101,v101
v_mfma_f32_16x16x32_bf16 v[120:123],a[172:175],v[252:255],v[120:123]
ds_read_b128 a[156:159],v1 offset:4864
v_cvt_pk_bf16_f32 v98,v100,v101
v_mfma_f32_16x16x32_bf16 v[124:127],a[188:191],v[252:255],v[44:47]
v_exp_f32 v102,v102
v_mfma_f32_16x16x32_bf16 v[124:127],a[184:187],v[244:247],v[124:127]
v_exp_f32 v103,v103
v_mfma_f32_16x16x32_bf16 v[124:127],a[180:183],v[236:239],v[124:127]
ds_read_b128 a[152:155],v1 offset:4608
v_cvt_pk_bf16_f32 v99,v102,v103
v_mfma_f32_16x16x32_bf16 v[124:127],a[176:179],v[228:231],v[124:127]
v_exp_f32 v104,v104
v_mfma_f32_16x16x32_bf16 a[32:35],a[192:195],v[80:83],a[32:35]
global_load_lds_dwordx4 v5,s[10:11] offset:2048 nt
ds_read_b128 a[148:151],v1 offset:4352
v_mfma_f32_16x16x32_bf16 a[32:35],a[196:199],v[88:91],a[32:35]
v_exp_f32 v105,v105
v_mfma_f32_16x16x32_bf16 a[36:39],a[200:203],v[80:83],a[36:39]
ds_read_b128 a[144:147],v1 offset:4096
v_cvt_pk_bf16_f32 v104,v104,v105
v_mfma_f32_16x16x32_bf16 a[36:39],a[204:207],v[88:91],a[36:39]
v_exp_f32 v106,v106
v_mfma_f32_16x16x32_bf16 a[40:43],a[208:211],v[80:83],a[40:43]
v_exp_f32 v107,v107
v_mfma_f32_16x16x32_bf16 a[40:43],a[212:215],v[88:91],a[40:43]
ds_read_b128 a[160:163],v1 offset:8192
v_cvt_pk_bf16_f32 v105,v106,v107
v_mfma_f32_16x16x32_bf16 a[44:47],a[216:219],v[80:83],a[44:47]
v_exp_f32 v108,v108
v_mfma_f32_16x16x32_bf16 a[44:47],a[220:223],v[88:91],a[44:47]
v_exp_f32 v109,v109
v_mfma_f32_16x16x32_bf16 a[48:51],a[224:227],v[80:83],a[48:51]
ds_read_b128 a[164:167],v1 offset:8448
v_cvt_pk_bf16_f32 v106,v108,v109
v_mfma_f32_16x16x32_bf16 a[48:51],a[228:231],v[88:91],a[48:51]
v_exp_f32 v110,v110
v_mfma_f32_16x16x32_bf16 a[52:55],a[232:235],v[80:83],a[52:55]
v_exp_f32 v111,v111
v_mfma_f32_16x16x32_bf16 a[52:55],a[236:239],v[88:91],a[52:55]
ds_read_b128 a[168:171],v1 offset:8704
v_cvt_pk_bf16_f32 v107,v110,v111
v_mfma_f32_16x16x32_bf16 a[56:59],a[240:243],v[80:83],a[56:59]
v_exp_f32 v112,v112
v_mfma_f32_16x16x32_bf16 a[56:59],a[244:247],v[88:91],a[56:59]
v_exp_f32 v113,v113
v_mfma_f32_16x16x32_bf16 a[60:63],a[248:251],v[80:83],a[60:63]
global_load_lds_dwordx4 v5,s[10:11] offset:3072
ds_read_b128 a[172:175],v1 offset:8960
v_mfma_f32_16x16x32_bf16 a[60:63],a[252:255],v[88:91],a[60:63]
ds_read_b128 a[188:191],v1 offset:13056
v_cvt_pk_bf16_f32 v112,v112,v113
v_mfma_f32_16x16x32_bf16 v[52:55],v[28:31],v[80:83],v[52:55]
v_exp_f32 v114,v114
v_mfma_f32_16x16x32_bf16 v[52:55],v[28:31],v[88:91],v[52:55]
s_waitcnt lgkmcnt(3)
v_exp_f32 v115,v115
v_mfma_f32_16x16x32_bf16 v[64:67],a[128:131],v[132:135],v[32:35]
ds_read_b128 a[184:187],v1 offset:12800
v_cvt_pk_bf16_f32 v113,v114,v115
v_mfma_f32_16x16x32_bf16 v[64:67],a[132:135],v[140:143],v[64:67]
v_exp_f32 v116,v116
v_mfma_f32_16x16x32_bf16 v[64:67],a[136:139],v[148:151],v[64:67]
ds_read_b128 a[180:183],v1 offset:12544
v_mfma_f32_16x16x32_bf16 v[64:67],a[140:143],v[156:159],v[64:67]
ds_read_b128 a[176:179],v1 offset:12288
v_add_u32 v1,s78,v1
v_mfma_f32_16x16x32_bf16 v[68:71],a[156:159],v[156:159],v[32:35]
v_exp_f32 v117,v117
v_mfma_f32_16x16x32_bf16 v[68:71],a[152:155],v[148:151],v[68:71]
v_exp_f32 v118,v118
v_mfma_f32_16x16x32_bf16 v[68:71],a[148:151],v[140:143],v[68:71]
s_add_u32 s10,s24,s74
s_addc_u32 s11,s25,0
v_cvt_pk_bf16_f32 v114,v116,v117
v_mfma_f32_16x16x32_bf16 v[68:71],a[144:147],v[132:135],v[68:71]
s_add_u32 s74,s74,16384
v_exp_f32 v119,v119
v_mfma_f32_16x16x32_bf16 v[72:75],a[160:163],v[132:135],v[32:35]
s_add_u32 m0,s76,16384
v_exp_f32 v120,v120
v_mfma_f32_16x16x32_bf16 v[72:75],a[164:167],v[140:143],v[72:75]
s_and_b32 s74,s74,0x3ffff
v_exp_f32 v121,v121
s_waitcnt lgkmcnt(2)
v_mfma_f32_16x16x32_bf16 v[72:75],a[168:171],v[148:151],v[72:75]
global_load_lds_dwordx4 v4,s[10:11]
v_cvt_pk_bf16_f32 v115,v118,v119
v_mfma_f32_16x16x32_bf16 v[72:75],a[172:175],v[156:159],v[72:75]
s_or_b32 s74,s74,s69
v_exp_f32 v122,v122
v_mfma_f32_16x16x32_bf16 v[76:79],a[188:191],v[156:159],v[32:35]
s_sub_u32 s76,s76,32768
v_exp_f32 v123,v123
v_mfma_f32_16x16x32_bf16 v[76:79],a[184:187],v[148:151],v[76:79]
s_and_b32 s76,s76,0x1ffff
v_exp_f32 v124,v124
s_waitcnt lgkmcnt(0)
v_mfma_f32_16x16x32_bf16 v[76:79],a[180:183],v[140:143],v[76:79]
s_and_b32 s69,s75,0xfffc0000
v_exp_f32 v125,v125
v_mfma_f32_16x16x32_bf16 v[76:79],a[176:179],v[132:135],v[76:79]
v_exp_f32 v126,v126
v_mfma_f32_16x16x32_bf16 a[64:67],a[192:195],v[96:99],a[64:67]
v_exp_f32 v127,v127
v_mfma_f32_16x16x32_bf16 a[64:67],a[196:199],v[104:107],a[64:67]
v_cvt_pk_bf16_f32 v120,v120,v121
v_cvt_pk_bf16_f32 v121,v122,v123
v_mfma_f32_16x16x32_bf16 a[68:71],a[200:203],v[96:99],a[68:71]
v_cvt_pk_bf16_f32 v122,v124,v125
v_cvt_pk_bf16_f32 v123,v126,v127
v_mfma_f32_16x16x32_bf16 a[68:71],a[204:207],v[104:107],a[68:71]
v_exp_f32 v64,v64
v_mfma_f32_16x16x32_bf16 a[72:75],a[208:211],v[96:99],a[72:75]
v_exp_f32 v65,v65
v_mfma_f32_16x16x32_bf16 a[72:75],a[212:215],v[104:107],a[72:75]
v_exp_f32 v66,v66
v_mfma_f32_16x16x32_bf16 a[76:79],a[216:219],v[96:99],a[76:79]
v_exp_f32 v67,v67
v_mfma_f32_16x16x32_bf16 a[76:79],a[220:223],v[104:107],a[76:79]
v_exp_f32 v68,v68
v_mfma_f32_16x16x32_bf16 a[80:83],a[224:227],v[96:99],a[80:83]
global_load_lds_dwordx4 v5,s[10:11] offset:1024
v_cvt_pk_bf16_f32 v64,v64,v65
v_mfma_f32_16x16x32_bf16 a[80:83],a[228:231],v[104:107],a[80:83]
)A"
R"A(v_exp_f32 v69,v69
v_mfma_f32_16x16x32_bf16 a[84:87],a[232:235],v[96:99],a[84:87]
v_exp_f32 v70,v70
v_mfma_f32_16x16x32_bf16 a[84:87],a[236:239],v[104:107],a[84:87]
v_exp_f32 v71,v71
v_mfma_f32_16x16x32_bf16 a[88:91],a[240:243],v[96:99],a[88:91]
v_cvt_pk_bf16_f32 v65,v66,v67
v_cvt_pk_bf16_f32 v66,v68,v69
v_mfma_f32_16x16x32_bf16 a[88:91],a[244:247],v[104:107],a[88:91]
v_exp_f32 v72,v72
v_mfma_f32_16x16x32_bf16 a[92:95],a[248:251],v[96:99],a[92:95]
v_exp_f32 v73,v73
v_mfma_f32_16x16x32_bf16 a[92:95],a[252:255],v[104:107],a[92:95]
v_cvt_pk_bf16_f32 v67,v70,v71
v_cvt_pk_bf16_f32 v72,v72,v73
v_mfma_f32_16x16x32_bf16 v[56:59],v[28:31],v[96:99],v[56:59]
v_exp_f32 v74,v74
v_mfma_f32_16x16x32_bf16 v[56:59],v[28:31],v[104:107],v[56:59]
v_exp_f32 v75,v75
v_mfma_f32_16x16x32_bf16 v[80:83],a[128:131],v[164:167],v[36:39]
v_exp_f32 v76,v76
v_mfma_f32_16x16x32_bf16 v[80:83],a[132:135],v[172:175],v[80:83]
v_exp_f32 v77,v77
v_mfma_f32_16x16x32_bf16 v[80:83],a[136:139],v[180:183],v[80:83]
v_exp_f32 v78,v78
v_mfma_f32_16x16x32_bf16 v[80:83],a[140:143],v[188:191],v[80:83]
v_exp_f32 v79,v79
v_mfma_f32_16x16x32_bf16 v[84:87],a[156:159],v[188:191],v[36:39]
v_cvt_pk_bf16_f32 v73,v74,v75
v_cvt_pk_bf16_f32 v74,v76,v77
v_mfma_f32_16x16x32_bf16 v[84:87],a[152:155],v[180:183],v[84:87]
global_load_lds_dwordx4 v4,s[10:11] offset:2048
v_cvt_pk_bf16_f32 v75,v78,v79
v_mfma_f32_16x16x32_bf16 v[84:87],a[148:151],v[172:175],v[84:87]
v_exp_f32 v80,v80
v_mfma_f32_16x16x32_bf16 v[84:87],a[144:147],v[164:167],v[84:87]
v_exp_f32 v81,v81
v_mfma_f32_16x16x32_bf16 v[88:91],a[160:163],v[164:167],v[36:39]
v_exp_f32 v82,v82
v_mfma_f32_16x16x32_bf16 v[88:91],a[164:167],v[172:175],v[88:91]
v_exp_f32 v83,v83
v_mfma_f32_16x16x32_bf16 v[88:91],a[168:171],v[180:183],v[88:91]
v_cvt_pk_bf16_f32 v80,v80,v81
v_cvt_pk_bf16_f32 v81,v82,v83
v_mfma_f32_16x16x32_bf16 v[88:91],a[172:175],v[188:191],v[88:91]
v_exp_f32 v84,v84
v_mfma_f32_16x16x32_bf16 v[92:95],a[188:191],v[188:191],v[36:39]
v_exp_f32 v85,v85
v_mfma_f32_16x16x32_bf16 v[92:95],a[184:187],v[180:183],v[92:95]
v_exp_f32 v86,v86
v_mfma_f32_16x16x32_bf16 v[92:95],a[180:183],v[172:175],v[92:95]
v_exp_f32 v87,v87
v_mfma_f32_16x16x32_bf16 v[92:95],a[176:179],v[164:167],v[92:95]
v_cvt_pk_bf16_f32 v82,v84,v85
v_cvt_pk_bf16_f32 v83,v86,v87
v_mfma_f32_16x16x32_bf16 a[96:99],a[192:195],v[112:115],a[96:99]
ds_read_b64_tr_b16 a[192:193],v2
ds_read_b64_tr_b16 a[194:195],v2 offset:4096
v_mfma_f32_16x16x32_bf16 a[96:99],a[196:199],v[120:123],a[96:99]
ds_read_b64_tr_b16 a[196:197],v2 offset:8192
ds_read_b64_tr_b16 a[198:199],v2 offset:12288
v_mfma_f32_16x16x32_bf16 a[100:103],a[200:203],v[112:115],a[100:103]
ds_read_b64_tr_b16 a[200:201],v3
ds_read_b64_tr_b16 a[202:203],v3 offset:4096
v_mfma_f32_16x16x32_bf16 a[100:103],a[204:207],v[120:123],a[100:103]
global_load_lds_dwordx4 v5,s[10:11] offset:3072
ds_read_b64_tr_b16 a[204:205],v3 offset:8192
v_mfma_f32_16x16x32_bf16 a[104:107],a[208:211],v[112:115],a[104:107]
ds_read_b64_tr_b16 a[206:207],v3 offset:12288
ds_read_b64_tr_b16 a[208:209],v2 offset:256
v_mfma_f32_16x16x32_bf16 a[104:107],a[212:215],v[120:123],a[104:107]
ds_read_b64_tr_b16 a[210:211],v2 offset:4352
ds_read_b64_tr_b16 a[212:213],v2 offset:8448
v_mfma_f32_16x16x32_bf16 a[108:111],a[216:219],v[112:115],a[108:111]
v_exp_f32 v88,v88
v_mfma_f32_16x16x32_bf16 a[108:111],a[220:223],v[120:123],a[108:111]
ds_read_b64_tr_b16 a[214:215],v2 offset:12544
ds_read_b64_tr_b16 a[216:217],v3 offset:256
v_mfma_f32_16x16x32_bf16 a[112:115],a[224:227],v[112:115],a[112:115]
ds_read_b64_tr_b16 a[218:219],v3 offset:4352
ds_read_b64_tr_b16 a[220:221],v3 offset:8448
v_mfma_f32_16x16x32_bf16 a[112:115],a[228:231],v[120:123],a[112:115]
ds_read_b64_tr_b16 a[222:223],v3 offset:12544
ds_read_b64_tr_b16 a[224:225],v2 offset:512
v_mfma_f32_16x16x32_bf16 a[116:119],a[232:235],v[112:115],a[116:119]
v_exp_f32 v89,v89
v_mfma_f32_16x16x32_bf16 a[116:119],a[236:239],v[120:123],a[116:119]
ds_read_b64_tr_b16 a[226:227],v2 offset:4608
ds_read_b64_tr_b16 a[228:229],v2 offset:8704
v_mfma_f32_16x16x32_bf16 a[120:123],a[240:243],v[112:115],a[120:123]
ds_read_b64_tr_b16 a[230:231],v2 offset:12800
ds_read_b64_tr_b16 a[232:233],v3 offset:512
v_mfma_f32_16x16x32_bf16 a[120:123],a[244:247],v[120:123],a[120:123]
s_add_u32 s10,s20,s74
s_addc_u32 s11,s21,0
ds_read_b64_tr_b16 a[234:235],v3 offset:4608
v_mfma_f32_16x16x32_bf16 a[124:127],a[248:251],v[112:115],a[124:127]
ds_read_b64_tr_b16 a[236:237],v3 offset:8704
ds_read_b64_tr_b16 a[238:239],v3 offset:12800
v_mfma_f32_16x16x32_bf16 a[124:127],a[252:255],v[120:123],a[124:127]
s_add_u32 m0,s76,0
ds_read_b64_tr_b16 a[240:241],v2 offset:768
ds_read_b64_tr_b16 a[242:243],v2 offset:4864
v_mfma_f32_16x16x32_bf16 v[60:63],v[28:31],v[112:115],v[60:63]
ds_read_b64_tr_b16 a[244:245],v2 offset:8960
v_cvt_pk_bf16_f32 v88,v88,v89
v_mfma_f32_16x16x32_bf16 v[60:63],v[28:31],v[120:123],v[60:63]
global_load_lds_dwordx4 v4,s[10:11] nt
ds_read_b64_tr_b16 a[246:247],v2 offset:13056
v_mfma_f32_16x16x32_bf16 v[96:99],a[128:131],v[196:199],v[40:43]
ds_read_b64_tr_b16 a[248:249],v3 offset:768
ds_read_b64_tr_b16 a[250:251],v3 offset:4864
v_mfma_f32_16x16x32_bf16 v[96:99],a[132:135],v[204:207],v[96:99]
ds_read_b64_tr_b16 a[252:253],v3 offset:8960
ds_read_b64_tr_b16 a[254:255],v3 offset:13056
v_mfma_f32_16x16x32_bf16 v[96:99],a[136:139],v[212:215],v[96:99]
v_exp_f32 v90,v90
v_mfma_f32_16x16x32_bf16 v[96:99],a[140:143],v[220:223],v[96:99]
v_exp_f32 v91,v91
v_mfma_f32_16x16x32_bf16 v[100:103],a[156:159],v[220:223],v[40:43]
v_exp_f32 v92,v92
v_mfma_f32_16x16x32_bf16 v[100:103],a[152:155],v[212:215],v[100:103]
v_exp_f32 v93,v93
v_mfma_f32_16x16x32_bf16 v[100:103],a[148:151],v[204:207],v[100:103]
v_exp_f32 v94,v94
v_mfma_f32_16x16x32_bf16 v[100:103],a[144:147],v[196:199],v[100:103]
v_exp_f32 v95,v95
v_mfma_f32_16x16x32_bf16 v[104:107],a[160:163],v[196:199],v[40:43]
v_cvt_pk_bf16_f32 v89,v90,v91
v_cvt_pk_bf16_f32 v90,v92,v93
v_mfma_f32_16x16x32_bf16 v[104:107],a[164:167],v[204:207],v[104:107]
v_cvt_pk_bf16_f32 v91,v94,v95
v_add_u32 v2,s78,v2
v_mfma_f32_16x16x32_bf16 v[104:107],a[168:171],v[212:215],v[104:107]
v_exp_f32 v96,v96
v_mfma_f32_16x16x32_bf16 v[104:107],a[172:175],v[220:223],v[104:107]
v_exp_f32 v97,v97
v_mfma_f32_16x16x32_bf16 v[108:111],a[188:191],v[220:223],v[40:43]
v_exp_f32 v98,v98
v_mfma_f32_16x16x32_bf16 v[108:111],a[184:187],v[212:215],v[108:111]
global_load_lds_dwordx4 v4,s[10:11] offset:1024 nt
v_cvt_pk_bf16_f32 v96,v96,v97
v_mfma_f32_16x16x32_bf16 v[108:111],a[180:183],v[204:207],v[108:111]
v_exp_f32 v99,v99
v_mfma_f32_16x16x32_bf16 v[108:111],a[176:179],v[196:199],v[108:111]
s_waitcnt lgkmcnt(0)
v_exp_f32 v100,v100
v_mfma_f32_16x16x32_bf16 a[0:3],a[192:195],v[64:67],a[0:3]
v_exp_f32 v101,v101
v_mfma_f32_16x16x32_bf16 a[0:3],a[196:199],v[72:75],a[0:3]
v_exp_f32 v102,v102
v_mfma_f32_16x16x32_bf16 a[4:7],a[200:203],v[64:67],a[4:7]
v_exp_f32 v103,v103
v_mfma_f32_16x16x32_bf16 a[4:7],a[204:207],v[72:75],a[4:7]
v_cvt_pk_bf16_f32 v97,v98,v99
v_cvt_pk_bf16_f32 v98,v100,v101
v_mfma_f32_16x16x32_bf16 a[8:11],a[208:211],v[64:67],a[8:11]
v_cvt_pk_bf16_f32 v99,v102,v103
v_add_u32 v3,s78,v3
v_mfma_f32_16x16x32_bf16 a[8:11],a[212:215],v[72:75],a[8:11]
s_sub_u32 s78,s77,32768
v_exp_f32 v104,v104
v_mfma_f32_16x16x32_bf16 a[12:15],a[216:219],v[64:67],a[12:15]
s_and_b32 s78,s78,0x1ffff
v_exp_f32 v105,v105
v_mfma_f32_16x16x32_bf16 a[12:15],a[220:223],v[72:75],a[12:15]
s_sub_u32 s78,s78,s77
v_exp_f32 v106,v106
v_mfma_f32_16x16x32_bf16 a[16:19],a[224:227],v[64:67],a[16:19]
s_add_u32 s77,s77,s78
v_exp_f32 v107,v107
v_mfma_f32_16x16x32_bf16 a[16:19],a[228:231],v[72:75],a[16:19]
v_exp_f32 v108,v108
v_mfma_f32_16x16x32_bf16 a[20:23],a[232:235],v[64:67],a[20:23]
s_getreg_b32 s92,hwreg(HW_REG_IB_STS,0,4)
s_min_u32 s4,s92,s6
s_mov_b32 s6,s92
v_mfma_f32_16x16x32_bf16 a[20:23],a[236:239],v[72:75],a[20:23]
s_cmp_gt_u32 s4,s9
s_cselect_b32 s4,15,s5
v_cvt_pk_bf16_f32 v104,v104,v105
v_mfma_f32_16x16x32_bf16 a[24:27],a[240:243],v[64:67],a[24:27]
v_exp_f32 v109,v109
v_mfma_f32_16x16x32_bf16 a[24:27],a[244:247],v[72:75],a[24:27]
v_exp_f32 v110,v110
v_mfma_f32_16x16x32_bf16 a[28:31],a[248:251],v[64:67],a[28:31]
v_exp_f32 v111,v111
v_mfma_f32_16x16x32_bf16 a[28:31],a[252:255],v[72:75],a[28:31]
s_getpc_b64 s[92:93]
s_cmp_lg_u32 s4,0
s_cbranch_scc1 L_pace_%=
v_mfma_f32_16x16x32_bf16 v[48:51],v[28:31],v[64:67],v[48:51]
v_cvt_pk_bf16_f32 v105,v106,v107
v_cvt_pk_bf16_f32 v106,v108,v109
v_mfma_f32_16x16x32_bf16 v[48:51],v[28:31],v[72:75],v[48:51]
s_add_u32 s10,s20,s74
s_addc_u32 s11,s21,0
v_cvt_pk_bf16_f32 v107,v110,v111
s_waitcnt vmcnt(10)
s_barrier
v_mfma_f32_16x16x32_bf16 v[24:27],v[8:11],v[128:131],0
s_add_u32 m0,s76,0
v_mfma_f32_16x16x32_bf16 v[24:27],v[12:15],v[136:139],v[24:27]
v_mfma_f32_16x16x32_bf16 v[24:27],v[16:19],v[144:147],v[24:27]
global_load_lds_dwordx4 v5,s[10:11] offset:2048 nt
v_mfma_f32_16x16x32_bf16 v[24:27],v[20:23],v[152:155],v[24:27]
v_mfma_f32_16x16x32_bf16 v[112:115],a[128:131],v[228:231],v[44:47]
ds_read_b128 a[128:131],v1
v_mfma_f32_16x16x32_bf16 v[112:115],a[132:135],v[236:239],v[112:115]
ds_read_b128 a[132:135],v1 offset:256
v_mfma_f32_16x16x32_bf16 v[112:115],a[136:139],v[244:247],v[112:115]
ds_read_b128 a[136:139],v1 offset:512
v_mfma_f32_16x16x32_bf16 v[112:115],a[140:143],v[252:255],v[112:115]
ds_read_b128 a[140:143],v1 offset:768
v_mfma_f32_16x16x32_bf16 v[116:119],a[156:159],v[252:255],v[44:47]
v_max3_f32 v24,v24,v25,v26
v_max_f32 v24,v24,v27
v_mfma_f32_16x16x32_bf16 v[116:119],a[152:155],v[244:247],v[116:119]
ds_read_b128 a[156:159],v1 offset:4864
v_mov_b32 v25,v24
v_mfma_f32_16x16x32_bf16 v[116:119],a[148:151],v[236:239],v[116:119]
v_exp_f32 v112,v112
v_mfma_f32_16x16x32_bf16 v[116:119],a[144:147],v[228:231],v[116:119]
v_permlane32_swap_b32 v24,v25
v_mfma_f32_16x16x32_bf16 v[120:123],a[160:163],v[228:231],v[44:47]
v_max_f32 v24,v24,v25
v_mov_b32 v25,v24
v_mfma_f32_16x16x32_bf16 v[120:123],a[164:167],v[236:239],v[120:123]
v_exp_f32 v113,v113
v_mfma_f32_16x16x32_bf16 v[120:123],a[168:171],v[244:247],v[120:123]
v_permlane16_swap_b32 v24,v25
v_mfma_f32_16x16x32_bf16 v[120:123],a[172:175],v[252:255],v[120:123]
v_max_f32 v24,v24,v25
v_sub_f32 v27,0,v24
v_mfma_f32_16x16x32_bf16 v[124:127],a[188:191],v[252:255],v[44:47]
global_load_lds_dwordx4 v5,s[10:11] offset:3072
v_sub_f32 v26,0,v24
v_sub_f32 v25,0,v24
v_sub_f32 v24,0,v24
v_mfma_f32_16x16x32_bf16 v[124:127],a[184:187],v[244:247],v[124:127]
ds_write_b128 v7,v[24:27]
v_mfma_f32_16x16x32_bf16 v[124:127],a[180:183],v[236:239],v[124:127]
ds_read_b128 a[152:155],v1 offset:4608
v_cvt_pk_bf16_f32 v112,v112,v113
v_mfma_f32_16x16x32_bf16 v[124:127],a[176:179],v[228:231],v[124:127]
v_exp_f32 v114,v114
v_mfma_f32_16x16x32_bf16 v[24:27],v[8:11],v[160:163],0
v_exp_f32 v115,v115
v_mfma_f32_16x16x32_bf16 v[24:27],v[12:15],v[168:171],v[24:27]
ds_read_b128 a[148:151],v1 offset:4352
v_cvt_pk_bf16_f32 v113,v114,v115
v_mfma_f32_16x16x32_bf16 v[24:27],v[16:19],v[176:179],v[24:27]
v_exp_f32 v116,v116
v_mfma_f32_16x16x32_bf16 v[24:27],v[20:23],v[184:187],v[24:27]
v_exp_f32 v117,v117
v_mfma_f32_16x16x32_bf16 a[32:35],a[192:195],v[80:83],a[32:35]
ds_read_b128 a[144:147],v1 offset:4096
v_cvt_pk_bf16_f32 v114,v116,v117
v_mfma_f32_16x16x32_bf16 a[32:35],a[196:199],v[88:91],a[32:35]
v_exp_f32 v118,v118
v_mfma_f32_16x16x32_bf16 a[36:39],a[200:203],v[80:83],a[36:39]
s_add_u32 s10,s24,s74
s_addc_u32 s11,s25,0
ds_read_b128 a[160:163],v1 offset:8192
v_mfma_f32_16x16x32_bf16 a[36:39],a[204:207],v[88:91],a[36:39]
s_add_u32 s74,s74,16384
v_max3_f32 v24,v24,v25,v26
v_max_f32 v24,v24,v27
v_mfma_f32_16x16x32_bf16 a[40:43],a[208:211],v[80:83],a[40:43]
s_add_u32 m0,s76,16384
ds_read_b128 a[164:167],v1 offset:8448
v_mov_b32 v25,v24
v_mfma_f32_16x16x32_bf16 a[40:43],a[212:215],v[88:91],a[40:43]
s_and_b32 s74,s74,0x3ffff
v_exp_f32 v119,v119
v_mfma_f32_16x16x32_bf16 a[44:47],a[216:219],v[80:83],a[44:47]
global_load_lds_dwordx4 v4,s[10:11]
ds_read_b128 a[168:171],v1 offset:8704
v_mfma_f32_16x16x32_bf16 a[44:47],a[220:223],v[88:91],a[44:47]
s_or_b32 s74,s74,s69
v_permlane32_swap_b32 v24,v25
v_mfma_f32_16x16x32_bf16 a[48:51],a[224:227],v[80:83],a[48:51]
)A"
R"A(s_sub_u32 s76,s76,32768
v_max_f32 v24,v24,v25
v_mov_b32 v25,v24
v_mfma_f32_16x16x32_bf16 a[48:51],a[228:231],v[88:91],a[48:51]
s_and_b32 s76,s76,0x1ffff
ds_read_b128 a[172:175],v1 offset:8960
v_cvt_pk_bf16_f32 v115,v118,v119
v_mfma_f32_16x16x32_bf16 a[52:55],a[232:235],v[80:83],a[52:55]
v_permlane16_swap_b32 v24,v25
v_mfma_f32_16x16x32_bf16 a[52:55],a[236:239],v[88:91],a[52:55]
v_max_f32 v24,v24,v25
v_sub_f32 v27,0,v24
v_mfma_f32_16x16x32_bf16 a[56:59],a[240:243],v[80:83],a[56:59]
v_sub_f32 v26,0,v24
v_sub_f32 v25,0,v24
v_mfma_f32_16x16x32_bf16 a[56:59],a[244:247],v[88:91],a[56:59]
ds_read_b128 a[188:191],v1 offset:13056
v_sub_f32 v24,0,v24
v_mfma_f32_16x16x32_bf16 a[60:63],a[248:251],v[80:83],a[60:63]
ds_write_b128 v7,v[24:27] offset:1024
v_mfma_f32_16x16x32_bf16 a[60:63],a[252:255],v[88:91],a[60:63]
v_exp_f32 v120,v120
v_mfma_f32_16x16x32_bf16 v[52:55],v[28:31],v[80:83],v[52:55]
v_exp_f32 v121,v121
v_mfma_f32_16x16x32_bf16 v[52:55],v[28:31],v[88:91],v[52:55]
ds_read_b128 a[184:187],v1 offset:12800
v_cvt_pk_bf16_f32 v120,v120,v121
v_mfma_f32_16x16x32_bf16 v[24:27],v[8:11],v[192:195],0
v_exp_f32 v122,v122
v_mfma_f32_16x16x32_bf16 v[24:27],v[12:15],v[200:203],v[24:27]
v_exp_f32 v123,v123
v_mfma_f32_16x16x32_bf16 v[24:27],v[16:19],v[208:211],v[24:27]
s_waitcnt lgkmcnt(2)
ds_read_b128 a[180:183],v1 offset:12544
v_cvt_pk_bf16_f32 v121,v122,v123
v_mfma_f32_16x16x32_bf16 v[24:27],v[20:23],v[216:219],v[24:27]
global_load_lds_dwordx4 v5,s[10:11] offset:1024
ds_read_b128 a[176:179],v1 offset:12288
v_mfma_f32_16x16x32_bf16 v[64:67],a[128:131],v[132:135],v[32:35]
v_exp_f32 v124,v124
v_mfma_f32_16x16x32_bf16 v[64:67],a[132:135],v[140:143],v[64:67]
v_exp_f32 v125,v125
v_mfma_f32_16x16x32_bf16 v[64:67],a[136:139],v[148:151],v[64:67]
v_exp_f32 v126,v126
v_mfma_f32_16x16x32_bf16 v[64:67],a[140:143],v[156:159],v[64:67]
v_max3_f32 v24,v24,v25,v26
v_max_f32 v24,v24,v27
v_mfma_f32_16x16x32_bf16 v[68:71],a[156:159],v[156:159],v[32:35]
v_mov_b32 v25,v24
v_cvt_pk_bf16_f32 v122,v124,v125
v_mfma_f32_16x16x32_bf16 v[68:71],a[152:155],v[148:151],v[68:71]
v_exp_f32 v127,v127
v_mfma_f32_16x16x32_bf16 v[68:71],a[148:151],v[140:143],v[68:71]
v_permlane32_swap_b32 v24,v25
v_mfma_f32_16x16x32_bf16 v[68:71],a[144:147],v[132:135],v[68:71]
v_max_f32 v24,v24,v25
v_mov_b32 v25,v24
v_mfma_f32_16x16x32_bf16 v[72:75],a[160:163],v[132:135],v[32:35]
v_cvt_pk_bf16_f32 v123,v126,v127
v_add_u32 v1,s78,v1
v_mfma_f32_16x16x32_bf16 v[72:75],a[164:167],v[140:143],v[72:75]
v_permlane16_swap_b32 v24,v25
v_mfma_f32_16x16x32_bf16 v[72:75],a[168:171],v[148:151],v[72:75]
v_max_f32 v24,v24,v25
v_sub_f32 v27,0,v24
v_mfma_f32_16x16x32_bf16 v[72:75],a[172:175],v[156:159],v[72:75]
v_sub_f32 v26,0,v24
v_sub_f32 v25,0,v24
v_mfma_f32_16x16x32_bf16 v[76:79],a[188:191],v[156:159],v[32:35]
s_waitcnt lgkmcnt(0)
v_exp_f32 v64,v64
v_mfma_f32_16x16x32_bf16 v[76:79],a[184:187],v[148:151],v[76:79]
global_load_lds_dwordx4 v4,s[10:11] offset:2048
v_sub_f32 v24,0,v24
v_mfma_f32_16x16x32_bf16 v[76:79],a[180:183],v[140:143],v[76:79]
ds_write_b128 v7,v[24:27] offset:2048
v_mfma_f32_16x16x32_bf16 v[76:79],a[176:179],v[132:135],v[76:79]
v_exp_f32 v65,v65
v_mfma_f32_16x16x32_bf16 a[64:67],a[192:195],v[96:99],a[64:67]
v_exp_f32 v66,v66
v_mfma_f32_16x16x32_bf16 a[64:67],a[196:199],v[104:107],a[64:67]
v_exp_f32 v67,v67
v_mfma_f32_16x16x32_bf16 a[68:71],a[200:203],v[96:99],a[68:71]
v_exp_f32 v68,v68
v_mfma_f32_16x16x32_bf16 a[68:71],a[204:207],v[104:107],a[68:71]
v_exp_f32 v69,v69
v_mfma_f32_16x16x32_bf16 a[72:75],a[208:211],v[96:99],a[72:75]
v_exp_f32 v70,v70
v_mfma_f32_16x16x32_bf16 a[72:75],a[212:215],v[104:107],a[72:75]
v_exp_f32 v71,v71
v_mfma_f32_16x16x32_bf16 a[76:79],a[216:219],v[96:99],a[76:79]
v_cvt_pk_bf16_f32 v64,v64,v65
v_cvt_pk_bf16_f32 v65,v66,v67
v_mfma_f32_16x16x32_bf16 a[76:79],a[220:223],v[104:107],a[76:79]
v_cvt_pk_bf16_f32 v66,v68,v69
v_cvt_pk_bf16_f32 v67,v70,v71
v_mfma_f32_16x16x32_bf16 a[80:83],a[224:227],v[96:99],a[80:83]
v_exp_f32 v72,v72
v_mfma_f32_16x16x32_bf16 a[80:83],a[228:231],v[104:107],a[80:83]
v_exp_f32 v73,v73
v_mfma_f32_16x16x32_bf16 a[84:87],a[232:235],v[96:99],a[84:87]
v_exp_f32 v74,v74
v_mfma_f32_16x16x32_bf16 a[84:87],a[236:239],v[104:107],a[84:87]
v_exp_f32 v75,v75
v_mfma_f32_16x16x32_bf16 a[88:91],a[240:243],v[96:99],a[88:91]
global_load_lds_dwordx4 v5,s[10:11] offset:3072
v_cvt_pk_bf16_f32 v72,v72,v73
v_mfma_f32_16x16x32_bf16 a[88:91],a[244:247],v[104:107],a[88:91]
v_exp_f32 v76,v76
v_mfma_f32_16x16x32_bf16 a[92:95],a[248:251],v[96:99],a[92:95]
v_exp_f32 v77,v77
v_mfma_f32_16x16x32_bf16 a[92:95],a[252:255],v[104:107],a[92:95]
v_exp_f32 v78,v78
v_mfma_f32_16x16x32_bf16 v[56:59],v[28:31],v[96:99],v[56:59]
v_exp_f32 v79,v79
v_mfma_f32_16x16x32_bf16 v[56:59],v[28:31],v[104:107],v[56:59]
v_cvt_pk_bf16_f32 v73,v74,v75
v_cvt_pk_bf16_f32 v74,v76,v77
v_mfma_f32_16x16x32_bf16 v[24:27],v[8:11],v[224:227],0
v_cvt_pk_bf16_f32 v75,v78,v79
v_mfma_f32_16x16x32_bf16 v[24:27],v[12:15],v[232:235],v[24:27]
v_mfma_f32_16x16x32_bf16 v[24:27],v[16:19],v[240:243],v[24:27]
v_mfma_f32_16x16x32_bf16 v[24:27],v[20:23],v[248:251],v[24:27]
v_mfma_f32_16x16x32_bf16 v[80:83],a[128:131],v[164:167],v[36:39]
s_add_u32 s10,s20,s74
s_addc_u32 s11,s21,0
v_mfma_f32_16x16x32_bf16 v[80:83],a[132:135],v[172:175],v[80:83]
v_mfma_f32_16x16x32_bf16 v[80:83],a[136:139],v[180:183],v[80:83]
s_add_u32 m0,s76,0
v_mfma_f32_16x16x32_bf16 v[80:83],a[140:143],v[188:191],v[80:83]
v_mfma_f32_16x16x32_bf16 v[84:87],a[156:159],v[188:191],v[36:39]
global_load_lds_dwordx4 v4,s[10:11] nt
v_max3_f32 v24,v24,v25,v26
v_mfma_f32_16x16x32_bf16 v[84:87],a[152:155],v[180:183],v[84:87]
v_max_f32 v24,v24,v27
v_mov_b32 v25,v24
v_mfma_f32_16x16x32_bf16 v[84:87],a[148:151],v[172:175],v[84:87]
v_mfma_f32_16x16x32_bf16 v[84:87],a[144:147],v[164:167],v[84:87]
v_permlane32_swap_b32 v24,v25
v_mfma_f32_16x16x32_bf16 v[88:91],a[160:163],v[164:167],v[36:39]
v_max_f32 v24,v24,v25
v_mov_b32 v25,v24
v_mfma_f32_16x16x32_bf16 v[88:91],a[164:167],v[172:175],v[88:91]
v_exp_f32 v80,v80
v_mfma_f32_16x16x32_bf16 v[88:91],a[168:171],v[180:183],v[88:91]
v_permlane16_swap_b32 v24,v25
v_mfma_f32_16x16x32_bf16 v[88:91],a[172:175],v[188:191],v[88:91]
v_max_f32 v24,v24,v25
v_max_f32 v24,s72,v24
v_mfma_f32_16x16x32_bf16 v[92:95],a[188:191],v[188:191],v[36:39]
v_sub_f32 v27,0,v24
v_sub_f32 v26,0,v24
v_mfma_f32_16x16x32_bf16 v[92:95],a[184:187],v[180:183],v[92:95]
v_sub_f32 v25,0,v24
v_sub_f32 v24,0,v24
v_mfma_f32_16x16x32_bf16 v[92:95],a[180:183],v[172:175],v[92:95]
v_exp_f32 v81,v81
v_mfma_f32_16x16x32_bf16 v[92:95],a[176:179],v[164:167],v[92:95]
v_exp_f32 v82,v82
v_mfma_f32_16x16x32_bf16 a[96:99],a[192:195],v[112:115],a[96:99]
s_nop 0
L_pd6_%=:
s_cbranch_execz L_trt0_%=
ds_read_b64_tr_b16 a[192:193],v2
ds_read_b64_tr_b16 a[194:195],v2 offset:4096
v_mfma_f32_16x16x32_bf16 a[96:99],a[196:199],v[120:123],a[96:99]
ds_read_b64_tr_b16 a[196:197],v2 offset:8192
ds_read_b64_tr_b16 a[198:199],v2 offset:12288
v_mfma_f32_16x16x32_bf16 a[100:103],a[200:203],v[112:115],a[100:103]
ds_read_b64_tr_b16 a[200:201],v3
ds_read_b64_tr_b16 a[202:203],v3 offset:4096
v_mfma_f32_16x16x32_bf16 a[100:103],a[204:207],v[120:123],a[100:103]
global_load_lds_dwordx4 v4,s[10:11] offset:1024 nt
ds_read_b64_tr_b16 a[204:205],v3 offset:8192
v_mfma_f32_16x16x32_bf16 a[104:107],a[208:211],v[112:115],a[104:107]
ds_read_b64_tr_b16 a[206:207],v3 offset:12288
ds_read_b64_tr_b16 a[208:209],v2 offset:256
v_mfma_f32_16x16x32_bf16 a[104:107],a[212:215],v[120:123],a[104:107]
ds_read_b64_tr_b16 a[210:211],v2 offset:4352
ds_read_b64_tr_b16 a[212:213],v2 offset:8448
v_mfma_f32_16x16x32_bf16 a[108:111],a[216:219],v[112:115],a[108:111]
ds_read_b64_tr_b16 a[214:215],v2 offset:12544
v_cvt_pk_bf16_f32 v80,v80,v81
v_mfma_f32_16x16x32_bf16 a[108:111],a[220:223],v[120:123],a[108:111]
ds_read_b64_tr_b16 a[216:217],v3 offset:256
ds_read_b64_tr_b16 a[218:219],v3 offset:4352
v_mfma_f32_16x16x32_bf16 a[112:115],a[224:227],v[112:115],a[112:115]
v_exp_f32 v83,v83
v_mfma_f32_16x16x32_bf16 a[112:115],a[228:231],v[120:123],a[112:115]
ds_read_b64_tr_b16 a[220:221],v3 offset:8448
ds_read_b64_tr_b16 a[222:223],v3 offset:12544
v_mfma_f32_16x16x32_bf16 a[116:119],a[232:235],v[112:115],a[116:119]
ds_read_b64_tr_b16 a[224:225],v2 offset:512
ds_read_b64_tr_b16 a[226:227],v2 offset:4608
v_mfma_f32_16x16x32_bf16 a[116:119],a[236:239],v[120:123],a[116:119]
ds_read_b64_tr_b16 a[228:229],v2 offset:8704
ds_read_b64_tr_b16 a[230:231],v2 offset:12800
v_mfma_f32_16x16x32_bf16 a[120:123],a[240:243],v[112:115],a[120:123]
ds_read_b64_tr_b16 a[232:233],v3 offset:512
v_cvt_pk_bf16_f32 v81,v82,v83
v_mfma_f32_16x16x32_bf16 a[120:123],a[244:247],v[120:123],a[120:123]
ds_read_b64_tr_b16 a[234:235],v3 offset:4608
ds_read_b64_tr_b16 a[236:237],v3 offset:8704
v_mfma_f32_16x16x32_bf16 a[124:127],a[248:251],v[112:115],a[124:127]
v_exp_f32 v84,v84
v_mfma_f32_16x16x32_bf16 a[124:127],a[252:255],v[120:123],a[124:127]
ds_read_b64_tr_b16 a[238:239],v3 offset:12800
ds_read_b64_tr_b16 a[240:241],v2 offset:768
v_mfma_f32_16x16x32_bf16 v[60:63],v[28:31],v[112:115],v[60:63]
ds_read_b64_tr_b16 a[242:243],v2 offset:4864
ds_read_b64_tr_b16 a[244:245],v2 offset:8960
v_mfma_f32_16x16x32_bf16 v[60:63],v[28:31],v[120:123],v[60:63]
global_load_lds_dwordx4 v5,s[10:11] offset:2048 nt
ds_read_b64_tr_b16 a[246:247],v2 offset:13056
v_mfma_f32_16x16x32_bf16 v[96:99],a[128:131],v[196:199],v[40:43]
ds_read_b64_tr_b16 a[248:249],v3 offset:768
ds_read_b64_tr_b16 a[250:251],v3 offset:4864
v_mfma_f32_16x16x32_bf16 v[96:99],a[132:135],v[204:207],v[96:99]
ds_read_b64_tr_b16 a[252:253],v3 offset:8960
ds_read_b64_tr_b16 a[254:255],v3 offset:13056
v_mfma_f32_16x16x32_bf16 v[96:99],a[136:139],v[212:215],v[96:99]
v_exp_f32 v85,v85
v_mfma_f32_16x16x32_bf16 v[96:99],a[140:143],v[220:223],v[96:99]
v_exp_f32 v86,v86
v_mfma_f32_16x16x32_bf16 v[100:103],a[156:159],v[220:223],v[40:43]
v_exp_f32 v87,v87
v_mfma_f32_16x16x32_bf16 v[100:103],a[152:155],v[212:215],v[100:103]
v_exp_f32 v88,v88
v_mfma_f32_16x16x32_bf16 v[100:103],a[148:151],v[204:207],v[100:103]
v_exp_f32 v89,v89
v_mfma_f32_16x16x32_bf16 v[100:103],a[144:147],v[196:199],v[100:103]
v_exp_f32 v90,v90
v_mfma_f32_16x16x32_bf16 v[104:107],a[160:163],v[196:199],v[40:43]
v_exp_f32 v91,v91
v_mfma_f32_16x16x32_bf16 v[104:107],a[164:167],v[204:207],v[104:107]
v_exp_f32 v92,v92
v_mfma_f32_16x16x32_bf16 v[104:107],a[168:171],v[212:215],v[104:107]
v_exp_f32 v93,v93
v_mfma_f32_16x16x32_bf16 v[104:107],a[172:175],v[220:223],v[104:107]
v_exp_f32 v94,v94
v_mfma_f32_16x16x32_bf16 v[108:111],a[188:191],v[220:223],v[40:43]
v_exp_f32 v95,v95
v_mfma_f32_16x16x32_bf16 v[108:111],a[184:187],v[212:215],v[108:111]
v_exp_f32 v96,v96
v_mfma_f32_16x16x32_bf16 v[108:111],a[180:183],v[204:207],v[108:111]
global_load_lds_dwordx4 v5,s[10:11] offset:3072
v_cvt_pk_bf16_f32 v82,v84,v85
v_mfma_f32_16x16x32_bf16 v[108:111],a[176:179],v[196:199],v[108:111]
s_waitcnt lgkmcnt(0)
v_exp_f32 v97,v97
v_mfma_f32_16x16x32_bf16 a[0:3],a[192:195],v[64:67],a[0:3]
v_exp_f32 v98,v98
v_mfma_f32_16x16x32_bf16 a[0:3],a[196:199],v[72:75],a[0:3]
v_exp_f32 v99,v99
v_mfma_f32_16x16x32_bf16 a[4:7],a[200:203],v[64:67],a[4:7]
v_exp_f32 v100,v100
v_mfma_f32_16x16x32_bf16 a[4:7],a[204:207],v[72:75],a[4:7]
v_exp_f32 v101,v101
v_mfma_f32_16x16x32_bf16 a[8:11],a[208:211],v[64:67],a[8:11]
v_exp_f32 v102,v102
v_mfma_f32_16x16x32_bf16 a[8:11],a[212:215],v[72:75],a[8:11]
v_exp_f32 v103,v103
v_mfma_f32_16x16x32_bf16 a[12:15],a[216:219],v[64:67],a[12:15]
ds_write_b128 v7,v[24:27] offset:3072
v_mfma_f32_16x16x32_bf16 a[12:15],a[220:223],v[72:75],a[12:15]
v_cvt_pk_bf16_f32 v83,v86,v87
v_cvt_pk_bf16_f32 v88,v88,v89
v_mfma_f32_16x16x32_bf16 a[16:19],a[224:227],v[64:67],a[16:19]
v_cvt_pk_bf16_f32 v89,v90,v91
v_cvt_pk_bf16_f32 v90,v92,v93
v_mfma_f32_16x16x32_bf16 a[16:19],a[228:231],v[72:75],a[16:19]
v_cvt_pk_bf16_f32 v91,v94,v95
v_cvt_pk_bf16_f32 v96,v96,v97
v_mfma_f32_16x16x32_bf16 a[20:23],a[232:235],v[64:67],a[20:23]
s_getreg_b32 s92,hwreg(HW_REG_IB_STS,0,4)
s_min_u32 s4,s92,s6
s_mov_b32 s6,s92
v_mfma_f32_16x16x32_bf16 a[20:23],a[236:239],v[72:75],a[20:23]
s_cmp_gt_u32 s4,s9
)A"
R"A(s_cselect_b32 s4,15,s5
v_cvt_pk_bf16_f32 v97,v98,v99
v_mfma_f32_16x16x32_bf16 a[24:27],a[240:243],v[64:67],a[24:27]
v_cvt_pk_bf16_f32 v98,v100,v101
v_cvt_pk_bf16_f32 v99,v102,v103
v_mfma_f32_16x16x32_bf16 a[24:27],a[244:247],v[72:75],a[24:27]
v_mfma_f32_16x16x32_bf16 a[28:31],a[248:251],v[64:67],a[28:31]
v_mfma_f32_16x16x32_bf16 a[28:31],a[252:255],v[72:75],a[28:31]
s_getpc_b64 s[92:93]
s_cmp_lg_u32 s4,0
s_cbranch_scc1 L_pace_%=
v_mfma_f32_16x16x32_bf16 v[48:51],v[28:31],v[64:67],v[48:51]
v_mfma_f32_16x16x32_bf16 v[48:51],v[28:31],v[72:75],v[48:51]
s_mov_b32 s70,11
L_a0_%=:
s_waitcnt vmcnt(12)
s_barrier
v_mfma_f32_16x16x32_bf16 v[112:115],a[128:131],v[228:231],v[44:47]
s_add_u32 s10,s24,s74
s_addc_u32 s11,s25,0
s_add_u32 m0,s76,16384
v_mfma_f32_16x16x32_bf16 v[112:115],a[132:135],v[236:239],v[112:115]
s_add_u32 s75,s75,16384
s_add_u32 s74,s74,16384
ds_read_b128 a[128:131],v1
v_mfma_f32_16x16x32_bf16 v[112:115],a[136:139],v[244:247],v[112:115]
s_and_b32 s74,s74,0x3ffff
global_load_lds_dwordx4 v4,s[10:11]
v_mfma_f32_16x16x32_bf16 v[112:115],a[140:143],v[252:255],v[112:115]
s_and_b32 s69,s75,0xfffc0000
s_or_b32 s74,s74,s69
ds_read_b128 a[132:135],v1 offset:256
v_mfma_f32_16x16x32_bf16 v[116:119],a[156:159],v[252:255],v[44:47]
s_sub_u32 s76,s76,32768
s_and_b32 s76,s76,0x1ffff
v_mfma_f32_16x16x32_bf16 v[116:119],a[152:155],v[244:247],v[116:119]
ds_read_b128 a[136:139],v1 offset:512
v_mfma_f32_16x16x32_bf16 v[116:119],a[148:151],v[236:239],v[116:119]
v_mfma_f32_16x16x32_bf16 v[116:119],a[144:147],v[228:231],v[116:119]
ds_read_b128 a[140:143],v1 offset:768
v_mfma_f32_16x16x32_bf16 v[120:123],a[160:163],v[228:231],v[44:47]
v_mfma_f32_16x16x32_bf16 v[120:123],a[164:167],v[236:239],v[120:123]
ds_read_b128 a[156:159],v1 offset:4864
v_mfma_f32_16x16x32_bf16 v[120:123],a[168:171],v[244:247],v[120:123]
v_mfma_f32_16x16x32_bf16 v[120:123],a[172:175],v[252:255],v[120:123]
ds_read_b128 a[152:155],v1 offset:4608
v_mfma_f32_16x16x32_bf16 v[124:127],a[188:191],v[252:255],v[44:47]
v_exp_f32 v104,v104
v_mfma_f32_16x16x32_bf16 v[124:127],a[184:187],v[244:247],v[124:127]
ds_read_b128 a[148:151],v1 offset:4352
v_add_u32 v2,s78,v2
v_mfma_f32_16x16x32_bf16 v[124:127],a[180:183],v[236:239],v[124:127]
v_exp_f32 v105,v105
v_mfma_f32_16x16x32_bf16 v[124:127],a[176:179],v[228:231],v[124:127]
ds_read_b128 a[144:147],v1 offset:4096
v_cvt_pk_bf16_f32 v104,v104,v105
v_mfma_f32_16x16x32_bf16 a[32:35],a[192:195],v[80:83],a[32:35]
v_exp_f32 v106,v106
v_mfma_f32_16x16x32_bf16 a[32:35],a[196:199],v[88:91],a[32:35]
ds_read_b128 a[160:163],v1 offset:8192
v_add_u32 v3,s78,v3
v_mfma_f32_16x16x32_bf16 a[36:39],a[200:203],v[80:83],a[36:39]
s_sub_u32 s78,s77,32768
global_load_lds_dwordx4 v5,s[10:11] offset:1024
v_mfma_f32_16x16x32_bf16 a[36:39],a[204:207],v[88:91],a[36:39]
s_and_b32 s78,s78,0x1ffff
v_exp_f32 v107,v107
v_mfma_f32_16x16x32_bf16 a[40:43],a[208:211],v[80:83],a[40:43]
s_sub_u32 s78,s78,s77
ds_read_b128 a[164:167],v1 offset:8448
v_cvt_pk_bf16_f32 v105,v106,v107
v_mfma_f32_16x16x32_bf16 a[40:43],a[212:215],v[88:91],a[40:43]
s_add_u32 s77,s77,s78
v_exp_f32 v108,v108
v_mfma_f32_16x16x32_bf16 a[44:47],a[216:219],v[80:83],a[44:47]
v_exp_f32 v109,v109
v_mfma_f32_16x16x32_bf16 a[44:47],a[220:223],v[88:91],a[44:47]
ds_read_b128 a[168:171],v1 offset:8704
v_cvt_pk_bf16_f32 v106,v108,v109
v_mfma_f32_16x16x32_bf16 a[48:51],a[224:227],v[80:83],a[48:51]
v_exp_f32 v110,v110
v_mfma_f32_16x16x32_bf16 a[48:51],a[228:231],v[88:91],a[48:51]
v_exp_f32 v111,v111
v_mfma_f32_16x16x32_bf16 a[52:55],a[232:235],v[80:83],a[52:55]
ds_read_b128 a[172:175],v1 offset:8960
v_cvt_pk_bf16_f32 v107,v110,v111
v_mfma_f32_16x16x32_bf16 a[52:55],a[236:239],v[88:91],a[52:55]
v_exp_f32 v112,v112
v_mfma_f32_16x16x32_bf16 a[56:59],a[240:243],v[80:83],a[56:59]
v_exp_f32 v113,v113
v_mfma_f32_16x16x32_bf16 a[56:59],a[244:247],v[88:91],a[56:59]
ds_read_b128 a[188:191],v1 offset:13056
v_cvt_pk_bf16_f32 v112,v112,v113
v_mfma_f32_16x16x32_bf16 a[60:63],a[248:251],v[80:83],a[60:63]
v_exp_f32 v114,v114
v_mfma_f32_16x16x32_bf16 a[60:63],a[252:255],v[88:91],a[60:63]
v_exp_f32 v115,v115
v_mfma_f32_16x16x32_bf16 v[52:55],v[28:31],v[80:83],v[52:55]
ds_read_b128 a[184:187],v1 offset:12800
v_cvt_pk_bf16_f32 v113,v114,v115
v_mfma_f32_16x16x32_bf16 v[52:55],v[28:31],v[88:91],v[52:55]
s_waitcnt lgkmcnt(3)
v_exp_f32 v116,v116
v_mfma_f32_16x16x32_bf16 v[64:67],a[128:131],v[132:135],v[32:35]
global_load_lds_dwordx4 v4,s[10:11] offset:2048
ds_read_b128 a[180:183],v1 offset:12544
v_mfma_f32_16x16x32_bf16 v[64:67],a[132:135],v[140:143],v[64:67]
v_exp_f32 v117,v117
v_mfma_f32_16x16x32_bf16 v[64:67],a[136:139],v[148:151],v[64:67]
ds_read_b128 a[176:179],v1 offset:12288
v_cvt_pk_bf16_f32 v114,v116,v117
v_mfma_f32_16x16x32_bf16 v[64:67],a[140:143],v[156:159],v[64:67]
v_exp_f32 v118,v118
v_mfma_f32_16x16x32_bf16 v[68:71],a[156:159],v[156:159],v[32:35]
v_exp_f32 v119,v119
v_mfma_f32_16x16x32_bf16 v[68:71],a[152:155],v[148:151],v[68:71]
v_cvt_pk_bf16_f32 v115,v118,v119
v_add_u32 v1,s78,v1
v_mfma_f32_16x16x32_bf16 v[68:71],a[148:151],v[140:143],v[68:71]
v_exp_f32 v120,v120
v_mfma_f32_16x16x32_bf16 v[68:71],a[144:147],v[132:135],v[68:71]
v_exp_f32 v121,v121
v_mfma_f32_16x16x32_bf16 v[72:75],a[160:163],v[132:135],v[32:35]
v_exp_f32 v122,v122
v_mfma_f32_16x16x32_bf16 v[72:75],a[164:167],v[140:143],v[72:75]
v_exp_f32 v123,v123
v_mfma_f32_16x16x32_bf16 v[72:75],a[168:171],v[148:151],v[72:75]
s_waitcnt lgkmcnt(0)
v_exp_f32 v124,v124
v_mfma_f32_16x16x32_bf16 v[72:75],a[172:175],v[156:159],v[72:75]
v_exp_f32 v125,v125
v_mfma_f32_16x16x32_bf16 v[76:79],a[188:191],v[156:159],v[32:35]
v_exp_f32 v126,v126
v_mfma_f32_16x16x32_bf16 v[76:79],a[184:187],v[148:151],v[76:79]
v_exp_f32 v127,v127
v_mfma_f32_16x16x32_bf16 v[76:79],a[180:183],v[140:143],v[76:79]
v_cvt_pk_bf16_f32 v120,v120,v121
v_cvt_pk_bf16_f32 v121,v122,v123
v_mfma_f32_16x16x32_bf16 v[76:79],a[176:179],v[132:135],v[76:79]
v_cvt_pk_bf16_f32 v122,v124,v125
v_cvt_pk_bf16_f32 v123,v126,v127
v_mfma_f32_16x16x32_bf16 a[64:67],a[192:195],v[96:99],a[64:67]
global_load_lds_dwordx4 v5,s[10:11] offset:3072
v_mfma_f32_16x16x32_bf16 a[64:67],a[196:199],v[104:107],a[64:67]
v_exp_f32 v64,v64
v_mfma_f32_16x16x32_bf16 a[68:71],a[200:203],v[96:99],a[68:71]
v_exp_f32 v65,v65
v_mfma_f32_16x16x32_bf16 a[68:71],a[204:207],v[104:107],a[68:71]
v_exp_f32 v66,v66
v_mfma_f32_16x16x32_bf16 a[72:75],a[208:211],v[96:99],a[72:75]
v_exp_f32 v67,v67
v_mfma_f32_16x16x32_bf16 a[72:75],a[212:215],v[104:107],a[72:75]
v_exp_f32 v68,v68
v_mfma_f32_16x16x32_bf16 a[76:79],a[216:219],v[96:99],a[76:79]
v_exp_f32 v69,v69
v_mfma_f32_16x16x32_bf16 a[76:79],a[220:223],v[104:107],a[76:79]
v_exp_f32 v70,v70
v_mfma_f32_16x16x32_bf16 a[80:83],a[224:227],v[96:99],a[80:83]
v_exp_f32 v71,v71
v_mfma_f32_16x16x32_bf16 a[80:83],a[228:231],v[104:107],a[80:83]
v_cvt_pk_bf16_f32 v64,v64,v65
v_cvt_pk_bf16_f32 v65,v66,v67
v_mfma_f32_16x16x32_bf16 a[84:87],a[232:235],v[96:99],a[84:87]
v_cvt_pk_bf16_f32 v66,v68,v69
v_cvt_pk_bf16_f32 v67,v70,v71
v_mfma_f32_16x16x32_bf16 a[84:87],a[236:239],v[104:107],a[84:87]
v_exp_f32 v72,v72
v_mfma_f32_16x16x32_bf16 a[88:91],a[240:243],v[96:99],a[88:91]
s_add_u32 s10,s20,s74
s_addc_u32 s11,s21,0
v_mfma_f32_16x16x32_bf16 a[88:91],a[244:247],v[104:107],a[88:91]
v_exp_f32 v73,v73
v_mfma_f32_16x16x32_bf16 a[92:95],a[248:251],v[96:99],a[92:95]
s_add_u32 m0,s76,0
v_exp_f32 v74,v74
v_mfma_f32_16x16x32_bf16 a[92:95],a[252:255],v[104:107],a[92:95]
v_exp_f32 v75,v75
v_mfma_f32_16x16x32_bf16 v[56:59],v[28:31],v[96:99],v[56:59]
global_load_lds_dwordx4 v4,s[10:11] nt
v_cvt_pk_bf16_f32 v72,v72,v73
v_mfma_f32_16x16x32_bf16 v[56:59],v[28:31],v[104:107],v[56:59]
v_exp_f32 v76,v76
v_mfma_f32_16x16x32_bf16 v[80:83],a[128:131],v[164:167],v[36:39]
v_exp_f32 v77,v77
v_mfma_f32_16x16x32_bf16 v[80:83],a[132:135],v[172:175],v[80:83]
v_exp_f32 v78,v78
v_mfma_f32_16x16x32_bf16 v[80:83],a[136:139],v[180:183],v[80:83]
v_exp_f32 v79,v79
v_mfma_f32_16x16x32_bf16 v[80:83],a[140:143],v[188:191],v[80:83]
v_cvt_pk_bf16_f32 v73,v74,v75
v_cvt_pk_bf16_f32 v74,v76,v77
v_mfma_f32_16x16x32_bf16 v[84:87],a[156:159],v[188:191],v[36:39]
v_cvt_pk_bf16_f32 v75,v78,v79
v_mfma_f32_16x16x32_bf16 v[84:87],a[152:155],v[180:183],v[84:87]
v_mfma_f32_16x16x32_bf16 v[84:87],a[148:151],v[172:175],v[84:87]
v_mfma_f32_16x16x32_bf16 v[84:87],a[144:147],v[164:167],v[84:87]
v_mfma_f32_16x16x32_bf16 v[88:91],a[160:163],v[164:167],v[36:39]
v_exp_f32 v80,v80
v_mfma_f32_16x16x32_bf16 v[88:91],a[164:167],v[172:175],v[88:91]
v_exp_f32 v81,v81
v_mfma_f32_16x16x32_bf16 v[88:91],a[168:171],v[180:183],v[88:91]
v_exp_f32 v82,v82
v_mfma_f32_16x16x32_bf16 v[88:91],a[172:175],v[188:191],v[88:91]
v_exp_f32 v83,v83
v_mfma_f32_16x16x32_bf16 v[92:95],a[188:191],v[188:191],v[36:39]
v_exp_f32 v84,v84
v_mfma_f32_16x16x32_bf16 v[92:95],a[184:187],v[180:183],v[92:95]
v_exp_f32 v85,v85
v_mfma_f32_16x16x32_bf16 v[92:95],a[180:183],v[172:175],v[92:95]
global_load_lds_dwordx4 v4,s[10:11] offset:1024 nt
v_cvt_pk_bf16_f32 v80,v80,v81
v_mfma_f32_16x16x32_bf16 v[92:95],a[176:179],v[164:167],v[92:95]
v_exp_f32 v86,v86
v_mfma_f32_16x16x32_bf16 a[96:99],a[192:195],v[112:115],a[96:99]
ds_read_b64_tr_b16 a[192:193],v2
ds_read_b64_tr_b16 a[194:195],v2 offset:4096
v_mfma_f32_16x16x32_bf16 a[96:99],a[196:199],v[120:123],a[96:99]
ds_read_b64_tr_b16 a[196:197],v2 offset:8192
v_cvt_pk_bf16_f32 v81,v82,v83
v_mfma_f32_16x16x32_bf16 a[100:103],a[200:203],v[112:115],a[100:103]
ds_read_b64_tr_b16 a[198:199],v2 offset:12288
v_cvt_pk_bf16_f32 v82,v84,v85
v_mfma_f32_16x16x32_bf16 a[100:103],a[204:207],v[120:123],a[100:103]
v_exp_f32 v87,v87
v_mfma_f32_16x16x32_bf16 a[104:107],a[208:211],v[112:115],a[104:107]
ds_read_b64_tr_b16 a[200:201],v3
ds_read_b64_tr_b16 a[202:203],v3 offset:4096
v_mfma_f32_16x16x32_bf16 a[104:107],a[212:215],v[120:123],a[104:107]
ds_read_b64_tr_b16 a[204:205],v3 offset:8192
v_cvt_pk_bf16_f32 v83,v86,v87
v_mfma_f32_16x16x32_bf16 a[108:111],a[216:219],v[112:115],a[108:111]
v_exp_f32 v88,v88
v_mfma_f32_16x16x32_bf16 a[108:111],a[220:223],v[120:123],a[108:111]
ds_read_b64_tr_b16 a[206:207],v3 offset:12288
ds_read_b64_tr_b16 a[208:209],v2 offset:256
v_mfma_f32_16x16x32_bf16 a[112:115],a[224:227],v[112:115],a[112:115]
v_exp_f32 v89,v89
v_mfma_f32_16x16x32_bf16 a[112:115],a[228:231],v[120:123],a[112:115]
ds_read_b64_tr_b16 a[210:211],v2 offset:4352
ds_read_b64_tr_b16 a[212:213],v2 offset:8448
v_mfma_f32_16x16x32_bf16 a[116:119],a[232:235],v[112:115],a[116:119]
ds_read_b64_tr_b16 a[214:215],v2 offset:12544
v_cvt_pk_bf16_f32 v88,v88,v89
v_mfma_f32_16x16x32_bf16 a[116:119],a[236:239],v[120:123],a[116:119]
v_exp_f32 v90,v90
v_mfma_f32_16x16x32_bf16 a[120:123],a[240:243],v[112:115],a[120:123]
ds_read_b64_tr_b16 a[216:217],v3 offset:256
ds_read_b64_tr_b16 a[218:219],v3 offset:4352
v_mfma_f32_16x16x32_bf16 a[120:123],a[244:247],v[120:123],a[120:123]
v_exp_f32 v91,v91
v_mfma_f32_16x16x32_bf16 a[124:127],a[248:251],v[112:115],a[124:127]
global_load_lds_dwordx4 v5,s[10:11] offset:2048 nt
ds_read_b64_tr_b16 a[220:221],v3 offset:8448
v_mfma_f32_16x16x32_bf16 a[124:127],a[252:255],v[120:123],a[124:127]
ds_read_b64_tr_b16 a[222:223],v3 offset:12544
ds_read_b64_tr_b16 a[224:225],v2 offset:512
v_mfma_f32_16x16x32_bf16 v[60:63],v[28:31],v[112:115],v[60:63]
ds_read_b64_tr_b16 a[226:227],v2 offset:4608
v_cvt_pk_bf16_f32 v89,v90,v91
v_mfma_f32_16x16x32_bf16 v[60:63],v[28:31],v[120:123],v[60:63]
v_exp_f32 v92,v92
v_mfma_f32_16x16x32_bf16 v[96:99],a[128:131],v[196:199],v[40:43]
ds_read_b64_tr_b16 a[228:229],v2 offset:8704
ds_read_b64_tr_b16 a[230:231],v2 offset:12800
v_mfma_f32_16x16x32_bf16 v[96:99],a[132:135],v[204:207],v[96:99]
v_exp_f32 v93,v93
v_mfma_f32_16x16x32_bf16 v[96:99],a[136:139],v[212:215],v[96:99]
ds_read_b64_tr_b16 a[232:233],v3 offset:512
ds_read_b64_tr_b16 a[234:235],v3 offset:4608
v_mfma_f32_16x16x32_bf16 v[96:99],a[140:143],v[220:223],v[96:99]
ds_read_b64_tr_b16 a[236:237],v3 offset:8704
v_cvt_pk_bf16_f32 v90,v92,v93
v_mfma_f32_16x16x32_bf16 v[100:103],a[156:159],v[220:223],v[40:43]
v_exp_f32 v94,v94
v_mfma_f32_16x16x32_bf16 v[100:103],a[152:155],v[212:215],v[100:103]
)A"
R"A(ds_read_b64_tr_b16 a[238:239],v3 offset:12800
ds_read_b64_tr_b16 a[240:241],v2 offset:768
v_mfma_f32_16x16x32_bf16 v[100:103],a[148:151],v[204:207],v[100:103]
v_exp_f32 v95,v95
v_mfma_f32_16x16x32_bf16 v[100:103],a[144:147],v[196:199],v[100:103]
ds_read_b64_tr_b16 a[242:243],v2 offset:4864
ds_read_b64_tr_b16 a[244:245],v2 offset:8960
v_mfma_f32_16x16x32_bf16 v[104:107],a[160:163],v[196:199],v[40:43]
ds_read_b64_tr_b16 a[246:247],v2 offset:13056
v_cvt_pk_bf16_f32 v91,v94,v95
v_mfma_f32_16x16x32_bf16 v[104:107],a[164:167],v[204:207],v[104:107]
v_exp_f32 v96,v96
v_mfma_f32_16x16x32_bf16 v[104:107],a[168:171],v[212:215],v[104:107]
ds_read_b64_tr_b16 a[248:249],v3 offset:768
ds_read_b64_tr_b16 a[250:251],v3 offset:4864
v_mfma_f32_16x16x32_bf16 v[104:107],a[172:175],v[220:223],v[104:107]
v_exp_f32 v97,v97
v_mfma_f32_16x16x32_bf16 v[108:111],a[188:191],v[220:223],v[40:43]
global_load_lds_dwordx4 v5,s[10:11] offset:3072
ds_read_b64_tr_b16 a[252:253],v3 offset:8960
v_mfma_f32_16x16x32_bf16 v[108:111],a[184:187],v[212:215],v[108:111]
ds_read_b64_tr_b16 a[254:255],v3 offset:13056
v_cvt_pk_bf16_f32 v96,v96,v97
v_mfma_f32_16x16x32_bf16 v[108:111],a[180:183],v[204:207],v[108:111]
v_exp_f32 v98,v98
v_mfma_f32_16x16x32_bf16 v[108:111],a[176:179],v[196:199],v[108:111]
s_waitcnt lgkmcnt(5)
v_exp_f32 v99,v99
v_mfma_f32_16x16x32_bf16 a[0:3],a[192:195],v[64:67],a[0:3]
v_exp_f32 v100,v100
v_mfma_f32_16x16x32_bf16 a[0:3],a[196:199],v[72:75],a[0:3]
v_exp_f32 v101,v101
v_mfma_f32_16x16x32_bf16 a[4:7],a[200:203],v[64:67],a[4:7]
v_exp_f32 v102,v102
v_mfma_f32_16x16x32_bf16 a[4:7],a[204:207],v[72:75],a[4:7]
v_exp_f32 v103,v103
v_mfma_f32_16x16x32_bf16 a[8:11],a[208:211],v[64:67],a[8:11]
v_cvt_pk_bf16_f32 v97,v98,v99
v_cvt_pk_bf16_f32 v98,v100,v101
v_mfma_f32_16x16x32_bf16 a[8:11],a[212:215],v[72:75],a[8:11]
v_cvt_pk_bf16_f32 v99,v102,v103
v_mfma_f32_16x16x32_bf16 a[12:15],a[216:219],v[64:67],a[12:15]
v_mfma_f32_16x16x32_bf16 a[12:15],a[220:223],v[72:75],a[12:15]
v_mfma_f32_16x16x32_bf16 a[16:19],a[224:227],v[64:67],a[16:19]
v_mfma_f32_16x16x32_bf16 a[16:19],a[228:231],v[72:75],a[16:19]
v_mfma_f32_16x16x32_bf16 a[20:23],a[232:235],v[64:67],a[20:23]
s_getreg_b32 s92,hwreg(HW_REG_IB_STS,0,4)
s_min_u32 s4,s92,s6
s_mov_b32 s6,s92
v_mfma_f32_16x16x32_bf16 a[20:23],a[236:239],v[72:75],a[20:23]
s_cmp_ge_u32 s4,s9
s_cselect_b32 s4,15,s5
v_mfma_f32_16x16x32_bf16 a[24:27],a[240:243],v[64:67],a[24:27]
s_waitcnt lgkmcnt(0)
v_mfma_f32_16x16x32_bf16 a[24:27],a[244:247],v[72:75],a[24:27]
v_mfma_f32_16x16x32_bf16 a[28:31],a[248:251],v[64:67],a[28:31]
v_mfma_f32_16x16x32_bf16 a[28:31],a[252:255],v[72:75],a[28:31]
s_getpc_b64 s[92:93]
s_cmp_lg_u32 s4,0
s_cbranch_scc1 L_pace_%=
v_mfma_f32_16x16x32_bf16 v[48:51],v[28:31],v[64:67],v[48:51]
v_mfma_f32_16x16x32_bf16 v[48:51],v[28:31],v[72:75],v[48:51]
s_sub_u32 s70,s70,1
s_cbranch_scc0 L_a0_%=
L_nx0_%=:
s_waitcnt vmcnt(12)
s_barrier
v_mfma_f32_16x16x32_bf16 v[112:115],a[128:131],v[228:231],v[44:47]
s_add_u32 s10,s24,s74
s_addc_u32 s11,s25,0
s_add_u32 m0,s76,16384
v_mfma_f32_16x16x32_bf16 v[112:115],a[132:135],v[236:239],v[112:115]
s_add_u32 s75,s75,16384
s_add_u32 s74,s74,16384
ds_read_b128 v[32:35],v7
v_mfma_f32_16x16x32_bf16 v[112:115],a[136:139],v[244:247],v[112:115]
s_and_b32 s74,s74,0x3ffff
global_load_lds_dwordx4 v4,s[10:11]
v_mfma_f32_16x16x32_bf16 v[112:115],a[140:143],v[252:255],v[112:115]
s_and_b32 s69,s75,0xfffc0000
s_or_b32 s74,s74,s69
ds_read_b128 a[128:131],v1
v_mfma_f32_16x16x32_bf16 v[116:119],a[156:159],v[252:255],v[44:47]
s_sub_u32 s76,s76,32768
v_exp_f32 v104,v104
v_mfma_f32_16x16x32_bf16 v[116:119],a[152:155],v[244:247],v[116:119]
s_and_b32 s76,s76,0x1ffff
ds_read_b128 a[132:135],v1 offset:256
v_add_u32 v2,s78,v2
v_mfma_f32_16x16x32_bf16 v[116:119],a[148:151],v[236:239],v[116:119]
s_min_u32 s7,s7,1
v_exp_f32 v105,v105
v_mfma_f32_16x16x32_bf16 v[116:119],a[144:147],v[228:231],v[116:119]
s_min_u32 s4,s5,1
ds_read_b128 a[136:139],v1 offset:512
v_cvt_pk_bf16_f32 v104,v104,v105
v_mfma_f32_16x16x32_bf16 v[120:123],a[160:163],v[228:231],v[44:47]
v_exp_f32 v106,v106
v_mfma_f32_16x16x32_bf16 v[120:123],a[164:167],v[236:239],v[120:123]
ds_read_b128 a[140:143],v1 offset:768
v_add_u32 v3,s78,v3
v_mfma_f32_16x16x32_bf16 v[120:123],a[168:171],v[244:247],v[120:123]
s_sub_u32 s78,s77,32768
v_exp_f32 v107,v107
v_mfma_f32_16x16x32_bf16 v[120:123],a[172:175],v[252:255],v[120:123]
s_and_b32 s78,s78,0x1ffff
ds_read_b128 a[156:159],v1 offset:4864
v_cvt_pk_bf16_f32 v105,v106,v107
v_mfma_f32_16x16x32_bf16 v[124:127],a[188:191],v[252:255],v[44:47]
s_sub_u32 s78,s78,s77
v_exp_f32 v108,v108
v_mfma_f32_16x16x32_bf16 v[124:127],a[184:187],v[244:247],v[124:127]
s_add_u32 s77,s77,s78
ds_read_b128 a[152:155],v1 offset:4608
v_accvgpr_read_b32 v8,a0
v_mfma_f32_16x16x32_bf16 v[124:127],a[180:183],v[236:239],v[124:127]
v_exp_f32 v109,v109
v_mfma_f32_16x16x32_bf16 v[124:127],a[176:179],v[228:231],v[124:127]
ds_read_b128 a[148:151],v1 offset:4352
v_cvt_pk_bf16_f32 v106,v108,v109
v_mfma_f32_16x16x32_bf16 v[52:55],v[28:31],v[80:83],v[52:55]
global_load_lds_dwordx4 v5,s[10:11] offset:1024
v_accvgpr_read_b32 v9,a1
v_mfma_f32_16x16x32_bf16 v[52:55],v[28:31],v[88:91],v[52:55]
ds_read_b128 a[144:147],v1 offset:4096
v_accvgpr_read_b32 v10,a2
v_mfma_f32_16x16x32_bf16 a[32:35],a[192:195],v[80:83],a[32:35]
v_exp_f32 v110,v110
v_mfma_f32_16x16x32_bf16 a[32:35],a[196:199],v[88:91],a[32:35]
ds_read_b128 a[160:163],v1 offset:8192
v_accvgpr_read_b32 v11,a3
v_mfma_f32_16x16x32_bf16 a[36:39],a[200:203],v[80:83],a[36:39]
v_exp_f32 v111,v111
v_mfma_f32_16x16x32_bf16 a[36:39],a[204:207],v[88:91],a[36:39]
ds_read_b128 a[164:167],v1 offset:8448
v_cvt_pk_bf16_f32 v107,v110,v111
v_mfma_f32_16x16x32_bf16 a[40:43],a[208:211],v[80:83],a[40:43]
v_exp_f32 v112,v112
v_mfma_f32_16x16x32_bf16 a[40:43],a[212:215],v[88:91],a[40:43]
ds_read_b128 a[168:171],v1 offset:8704
v_accvgpr_read_b32 v12,a4
v_mfma_f32_16x16x32_bf16 a[44:47],a[216:219],v[80:83],a[44:47]
v_exp_f32 v113,v113
v_mfma_f32_16x16x32_bf16 a[44:47],a[220:223],v[88:91],a[44:47]
ds_read_b128 a[172:175],v1 offset:8960
v_cvt_pk_bf16_f32 v112,v112,v113
v_mfma_f32_16x16x32_bf16 a[48:51],a[224:227],v[80:83],a[48:51]
v_exp_f32 v114,v114
v_mfma_f32_16x16x32_bf16 a[48:51],a[228:231],v[88:91],a[48:51]
ds_read_b128 a[188:191],v1 offset:13056
v_accvgpr_read_b32 v13,a5
v_mfma_f32_16x16x32_bf16 a[52:55],a[232:235],v[80:83],a[52:55]
v_exp_f32 v115,v115
v_mfma_f32_16x16x32_bf16 a[52:55],a[236:239],v[88:91],a[52:55]
ds_read_b128 a[184:187],v1 offset:12800
v_cvt_pk_bf16_f32 v113,v114,v115
v_mfma_f32_16x16x32_bf16 a[56:59],a[240:243],v[80:83],a[56:59]
global_load_lds_dwordx4 v4,s[10:11] offset:2048
v_accvgpr_read_b32 v14,a6
v_mfma_f32_16x16x32_bf16 a[56:59],a[244:247],v[88:91],a[56:59]
ds_read_b128 a[180:183],v1 offset:12544
v_accvgpr_read_b32 v15,a7
v_mfma_f32_16x16x32_bf16 a[60:63],a[248:251],v[80:83],a[60:63]
v_exp_f32 v116,v116
v_mfma_f32_16x16x32_bf16 a[60:63],a[252:255],v[88:91],a[60:63]
s_waitcnt lgkmcnt(3)
ds_read_b128 a[176:179],v1 offset:12288
v_max_f32 v24,0x00800000,v48
v_mfma_f32_16x16x32_bf16 v[64:67],a[128:131],v[128:131],v[32:35]
v_exp_f32 v117,v117
v_mfma_f32_16x16x32_bf16 v[64:67],a[132:135],v[136:139],v[64:67]
ds_read_b128 v[36:39],v7 offset:1024
v_cvt_pk_bf16_f32 v114,v116,v117
v_mfma_f32_16x16x32_bf16 v[64:67],a[136:139],v[144:147],v[64:67]
v_exp_f32 v118,v118
v_mfma_f32_16x16x32_bf16 v[64:67],a[140:143],v[152:155],v[64:67]
v_exp_f32 v119,v119
v_mfma_f32_16x16x32_bf16 v[68:71],a[156:159],v[152:155],v[32:35]
ds_read_b128 v[40:43],v7 offset:2048
v_cvt_pk_bf16_f32 v115,v118,v119
v_mfma_f32_16x16x32_bf16 v[68:71],a[152:155],v[144:147],v[68:71]
v_exp_f32 v120,v120
v_mfma_f32_16x16x32_bf16 v[68:71],a[148:151],v[136:139],v[68:71]
v_exp_f32 v121,v121
v_mfma_f32_16x16x32_bf16 v[68:71],a[144:147],v[128:131],v[68:71]
v_exp_f32 v122,v122
v_mfma_f32_16x16x32_bf16 v[72:75],a[160:163],v[128:131],v[32:35]
v_exp_f32 v123,v123
v_mfma_f32_16x16x32_bf16 v[72:75],a[164:167],v[136:139],v[72:75]
v_exp_f32 v124,v124
v_mfma_f32_16x16x32_bf16 v[72:75],a[168:171],v[144:147],v[72:75]
s_waitcnt lgkmcnt(1)
v_exp_f32 v125,v125
v_mfma_f32_16x16x32_bf16 v[72:75],a[172:175],v[152:155],v[72:75]
global_load_lds_dwordx4 v5,s[10:11] offset:3072
v_cvt_pk_bf16_f32 v120,v120,v121
v_mfma_f32_16x16x32_bf16 v[76:79],a[188:191],v[152:155],v[32:35]
v_exp_f32 v126,v126
v_mfma_f32_16x16x32_bf16 v[76:79],a[184:187],v[144:147],v[76:79]
v_exp_f32 v127,v127
v_mfma_f32_16x16x32_bf16 v[76:79],a[180:183],v[136:139],v[76:79]
v_cvt_pk_bf16_f32 v121,v122,v123
L_pd7_%=:
s_cbranch_execz L_trt0_%=
v_cvt_pk_bf16_f32 v122,v124,v125
v_mfma_f32_16x16x32_bf16 v[76:79],a[176:179],v[128:131],v[76:79]
v_cvt_pk_bf16_f32 v123,v126,v127
v_accvgpr_read_b32 v16,a8
v_mfma_f32_16x16x32_bf16 v[56:59],v[28:31],v[96:99],v[56:59]
v_exp_f32 v64,v64
v_mfma_f32_16x16x32_bf16 v[56:59],v[28:31],v[104:107],v[56:59]
v_exp_f32 v65,v65
v_mfma_f32_16x16x32_bf16 a[64:67],a[192:195],v[96:99],a[64:67]
v_exp_f32 v66,v66
v_mfma_f32_16x16x32_bf16 a[64:67],a[196:199],v[104:107],a[64:67]
v_exp_f32 v67,v67
v_mfma_f32_16x16x32_bf16 a[68:71],a[200:203],v[96:99],a[68:71]
v_exp_f32 v68,v68
v_mfma_f32_16x16x32_bf16 a[68:71],a[204:207],v[104:107],a[68:71]
s_add_u32 s10,s20,s74
s_addc_u32 s11,s21,0
v_cvt_pk_bf16_f32 v64,v64,v65
v_mfma_f32_16x16x32_bf16 a[72:75],a[208:211],v[96:99],a[72:75]
v_exp_f32 v69,v69
v_mfma_f32_16x16x32_bf16 a[72:75],a[212:215],v[104:107],a[72:75]
s_add_u32 m0,s76,0
v_exp_f32 v70,v70
v_mfma_f32_16x16x32_bf16 a[76:79],a[216:219],v[96:99],a[76:79]
v_exp_f32 v71,v71
v_mfma_f32_16x16x32_bf16 a[76:79],a[220:223],v[104:107],a[76:79]
global_load_lds_dwordx4 v4,s[10:11] nt
v_cvt_pk_bf16_f32 v65,v66,v67
v_mfma_f32_16x16x32_bf16 a[80:83],a[224:227],v[96:99],a[80:83]
v_cvt_pk_bf16_f32 v66,v68,v69
v_cvt_pk_bf16_f32 v67,v70,v71
v_mfma_f32_16x16x32_bf16 a[80:83],a[228:231],v[104:107],a[80:83]
v_exp_f32 v72,v72
v_mfma_f32_16x16x32_bf16 a[84:87],a[232:235],v[96:99],a[84:87]
v_exp_f32 v73,v73
v_mfma_f32_16x16x32_bf16 a[84:87],a[236:239],v[104:107],a[84:87]
v_exp_f32 v74,v74
v_mfma_f32_16x16x32_bf16 a[88:91],a[240:243],v[96:99],a[88:91]
v_exp_f32 v75,v75
v_mfma_f32_16x16x32_bf16 a[88:91],a[244:247],v[104:107],a[88:91]
v_exp_f32 v76,v76
v_mfma_f32_16x16x32_bf16 a[92:95],a[248:251],v[96:99],a[92:95]
v_exp_f32 v77,v77
v_mfma_f32_16x16x32_bf16 a[92:95],a[252:255],v[104:107],a[92:95]
v_exp_f32 v78,v78
v_mfma_f32_16x16x32_bf16 v[80:83],a[128:131],v[160:163],v[36:39]
v_exp_f32 v79,v79
v_mfma_f32_16x16x32_bf16 v[80:83],a[132:135],v[168:171],v[80:83]
v_cvt_pk_bf16_f32 v72,v72,v73
v_cvt_pk_bf16_f32 v73,v74,v75
v_mfma_f32_16x16x32_bf16 v[80:83],a[136:139],v[176:179],v[80:83]
v_cvt_pk_bf16_f32 v74,v76,v77
v_cvt_pk_bf16_f32 v75,v78,v79
v_mfma_f32_16x16x32_bf16 v[80:83],a[140:143],v[184:187],v[80:83]
v_rcp_f32 v24,v24
v_mfma_f32_16x16x32_bf16 v[84:87],a[156:159],v[184:187],v[36:39]
v_accvgpr_read_b32 v17,a9
v_accvgpr_read_b32 v18,a10
v_mfma_f32_16x16x32_bf16 v[84:87],a[152:155],v[176:179],v[84:87]
v_accvgpr_read_b32 v19,a11
v_mul_f32 v8,v8,v24
v_mfma_f32_16x16x32_bf16 v[84:87],a[148:151],v[168:171],v[84:87]
global_load_lds_dwordx4 v4,s[10:11] offset:1024 nt
v_mul_f32 v9,v9,v24
v_mfma_f32_16x16x32_bf16 v[84:87],a[144:147],v[160:163],v[84:87]
v_mul_f32 v10,v10,v24
v_mul_f32 v11,v11,v24
v_mfma_f32_16x16x32_bf16 v[88:91],a[160:163],v[160:163],v[36:39]
v_mul_f32 v12,v12,v24
v_mul_f32 v13,v13,v24
v_mfma_f32_16x16x32_bf16 v[88:91],a[164:167],v[168:171],v[88:91]
v_mul_f32 v14,v14,v24
v_mul_f32 v15,v15,v24
v_mfma_f32_16x16x32_bf16 v[88:91],a[168:171],v[176:179],v[88:91]
v_cvt_pk_bf16_f32 v8,v8,v9
v_cvt_pk_bf16_f32 v9,v10,v11
v_mfma_f32_16x16x32_bf16 v[88:91],a[172:175],v[184:187],v[88:91]
v_cvt_pk_bf16_f32 v10,v12,v13
v_cvt_pk_bf16_f32 v11,v14,v15
v_mfma_f32_16x16x32_bf16 v[92:95],a[188:191],v[184:187],v[36:39]
ds_write_b128 v7,v[8:11] offset:6144
v_mfma_f32_16x16x32_bf16 v[92:95],a[184:187],v[176:179],v[92:95]
v_accvgpr_read_b32 v20,a12
v_accvgpr_read_b32 v21,a13
v_mfma_f32_16x16x32_bf16 v[92:95],a[180:183],v[168:171],v[92:95]
v_accvgpr_read_b32 v22,a14
v_accvgpr_read_b32 v23,a15
)A"
R"A(v_mfma_f32_16x16x32_bf16 v[92:95],a[176:179],v[160:163],v[92:95]
v_accvgpr_read_b32 v8,a16
v_accvgpr_read_b32 v9,a17
v_mfma_f32_16x16x32_bf16 v[60:63],v[28:31],v[112:115],v[60:63]
v_accvgpr_read_b32 v10,a18
v_accvgpr_read_b32 v11,a19
v_mfma_f32_16x16x32_bf16 v[60:63],v[28:31],v[120:123],v[60:63]
v_mul_f32 v16,v16,v24
v_mul_f32 v17,v17,v24
v_mfma_f32_16x16x32_bf16 a[96:99],a[192:195],v[112:115],a[96:99]
ds_read_b64_tr_b16 a[192:193],v2
ds_read_b64_tr_b16 a[194:195],v2 offset:4096
v_mfma_f32_16x16x32_bf16 a[96:99],a[196:199],v[120:123],a[96:99]
ds_read_b64_tr_b16 a[196:197],v2 offset:8192
v_mul_f32 v18,v18,v24
v_mfma_f32_16x16x32_bf16 a[100:103],a[200:203],v[112:115],a[100:103]
global_load_lds_dwordx4 v5,s[10:11] offset:2048 nt
ds_read_b64_tr_b16 a[198:199],v2 offset:12288
v_mfma_f32_16x16x32_bf16 a[100:103],a[204:207],v[120:123],a[100:103]
ds_read_b64_tr_b16 a[200:201],v3
v_mul_f32 v19,v19,v24
v_mfma_f32_16x16x32_bf16 a[104:107],a[208:211],v[112:115],a[104:107]
ds_read_b64_tr_b16 a[202:203],v3 offset:4096
ds_read_b64_tr_b16 a[204:205],v3 offset:8192
v_mfma_f32_16x16x32_bf16 a[104:107],a[212:215],v[120:123],a[104:107]
ds_read_b64_tr_b16 a[206:207],v3 offset:12288
v_mul_f32 v20,v20,v24
v_mfma_f32_16x16x32_bf16 a[108:111],a[216:219],v[112:115],a[108:111]
ds_read_b64_tr_b16 a[208:209],v2 offset:256
v_mul_f32 v21,v21,v24
v_mfma_f32_16x16x32_bf16 a[108:111],a[220:223],v[120:123],a[108:111]
ds_read_b64_tr_b16 a[210:211],v2 offset:4352
v_mul_f32 v22,v22,v24
v_mfma_f32_16x16x32_bf16 a[112:115],a[224:227],v[112:115],a[112:115]
ds_read_b64_tr_b16 a[212:213],v2 offset:8448
ds_read_b64_tr_b16 a[214:215],v2 offset:12544
v_mfma_f32_16x16x32_bf16 a[112:115],a[228:231],v[120:123],a[112:115]
ds_read_b64_tr_b16 a[216:217],v3 offset:256
v_mul_f32 v23,v23,v24
v_mfma_f32_16x16x32_bf16 a[116:119],a[232:235],v[112:115],a[116:119]
ds_read_b64_tr_b16 a[218:219],v3 offset:4352
v_cvt_pk_bf16_f32 v16,v16,v17
v_mfma_f32_16x16x32_bf16 a[116:119],a[236:239],v[120:123],a[116:119]
ds_read_b64_tr_b16 a[220:221],v3 offset:8448
v_cvt_pk_bf16_f32 v17,v18,v19
v_mfma_f32_16x16x32_bf16 a[120:123],a[240:243],v[112:115],a[120:123]
ds_read_b64_tr_b16 a[222:223],v3 offset:12544
ds_read_b64_tr_b16 a[224:225],v2 offset:512
v_mfma_f32_16x16x32_bf16 a[120:123],a[244:247],v[120:123],a[120:123]
ds_read_b64_tr_b16 a[226:227],v2 offset:4608
v_cvt_pk_bf16_f32 v18,v20,v21
v_mfma_f32_16x16x32_bf16 a[124:127],a[248:251],v[112:115],a[124:127]
ds_read_b64_tr_b16 a[228:229],v2 offset:8704
v_cvt_pk_bf16_f32 v19,v22,v23
v_mfma_f32_16x16x32_bf16 a[124:127],a[252:255],v[120:123],a[124:127]
s_waitcnt lgkmcnt(9)
ds_read_b64_tr_b16 a[230:231],v2 offset:12800
v_accvgpr_read_b32 v12,a20
v_mfma_f32_16x16x32_bf16 v[96:99],a[128:131],v[192:195],v[40:43]
ds_read_b64_tr_b16 a[232:233],v3 offset:512
ds_read_b64_tr_b16 a[234:235],v3 offset:4608
v_mfma_f32_16x16x32_bf16 v[96:99],a[132:135],v[200:203],v[96:99]
global_load_lds_dwordx4 v5,s[10:11] offset:3072
ds_read_b64_tr_b16 a[236:237],v3 offset:8704
v_mfma_f32_16x16x32_bf16 v[96:99],a[136:139],v[208:211],v[96:99]
ds_read_b64_tr_b16 a[238:239],v3 offset:12800
v_accvgpr_read_b32 v13,a21
v_mfma_f32_16x16x32_bf16 v[96:99],a[140:143],v[216:219],v[96:99]
ds_read_b64_tr_b16 a[240:241],v2 offset:768
v_accvgpr_read_b32 v14,a22
v_mfma_f32_16x16x32_bf16 v[100:103],a[156:159],v[216:219],v[40:43]
ds_read_b64_tr_b16 a[242:243],v2 offset:4864
ds_read_b64_tr_b16 a[244:245],v2 offset:8960
v_mfma_f32_16x16x32_bf16 v[100:103],a[152:155],v[208:211],v[100:103]
ds_read_b64_tr_b16 a[246:247],v2 offset:13056
v_accvgpr_read_b32 v15,a23
v_mfma_f32_16x16x32_bf16 v[100:103],a[148:151],v[200:203],v[100:103]
s_cmp_le_u32 s5,3
s_cselect_b32 s5,s4,s5
ds_read_b64_tr_b16 a[248:249],v3 offset:768
v_mfma_f32_16x16x32_bf16 v[100:103],a[144:147],v[192:195],v[100:103]
ds_read_b64_tr_b16 a[250:251],v3 offset:4864
v_accvgpr_read_b32 v20,a28
v_mfma_f32_16x16x32_bf16 v[104:107],a[160:163],v[192:195],v[40:43]
ds_read_b64_tr_b16 a[252:253],v3 offset:8960
ds_read_b64_tr_b16 a[254:255],v3 offset:13056
v_mfma_f32_16x16x32_bf16 v[104:107],a[164:167],v[200:203],v[104:107]
v_accvgpr_read_b32 v21,a29
v_accvgpr_read_b32 v22,a30
v_mfma_f32_16x16x32_bf16 v[104:107],a[168:171],v[208:211],v[104:107]
ds_write_b128 v7,v[16:19] offset:7168
v_mfma_f32_16x16x32_bf16 v[104:107],a[172:175],v[216:219],v[104:107]
s_add_u32 s10,s24,s74
s_addc_u32 s11,s25,0
v_accvgpr_read_b32 v23,a31
v_mfma_f32_16x16x32_bf16 v[108:111],a[188:191],v[216:219],v[40:43]
v_accvgpr_read_b32 v16,a24
v_accvgpr_read_b32 v17,a25
v_mfma_f32_16x16x32_bf16 v[108:111],a[184:187],v[208:211],v[108:111]
s_add_u32 m0,s76,16384
v_accvgpr_read_b32 v18,a26
v_accvgpr_read_b32 v19,a27
v_mfma_f32_16x16x32_bf16 v[108:111],a[180:183],v[200:203],v[108:111]
v_cmp_nlt_f32 s[54:55],v48,s73
v_cmp_nlt_f32 s[56:57],v52,s73
v_mfma_f32_16x16x32_bf16 v[108:111],a[176:179],v[192:195],v[108:111]
s_or_b64 s[54:55],s[54:55],s[56:57]
global_load_lds_dwordx4 v4,s[10:11]
v_mfma_f32_16x16x32_bf16 a[0:3],a[192:195],v[64:67],0
v_cmp_nlt_f32 s[56:57],v56,s73
v_mul_f32 v8,v8,v24
v_mfma_f32_16x16x32_bf16 a[0:3],a[196:199],v[72:75],a[0:3]
s_or_b64 s[54:55],s[54:55],s[56:57]
v_mul_f32 v9,v9,v24
v_mul_f32 v10,v10,v24
v_mfma_f32_16x16x32_bf16 a[4:7],a[200:203],v[64:67],0
v_mul_f32 v11,v11,v24
v_mul_f32 v12,v12,v24
v_mfma_f32_16x16x32_bf16 a[4:7],a[204:207],v[72:75],a[4:7]
v_mul_f32 v13,v13,v24
v_mul_f32 v14,v14,v24
v_mfma_f32_16x16x32_bf16 a[8:11],a[208:211],v[64:67],0
s_waitcnt lgkmcnt(0)
v_mul_f32 v15,v15,v24
v_cvt_pk_bf16_f32 v8,v8,v9
v_mfma_f32_16x16x32_bf16 a[8:11],a[212:215],v[72:75],a[8:11]
v_cvt_pk_bf16_f32 v9,v10,v11
v_cvt_pk_bf16_f32 v10,v12,v13
v_mfma_f32_16x16x32_bf16 a[12:15],a[216:219],v[64:67],0
v_cvt_pk_bf16_f32 v11,v14,v15
v_mul_f32 v16,v16,v24
v_mfma_f32_16x16x32_bf16 a[12:15],a[220:223],v[72:75],a[12:15]
v_mul_f32 v17,v17,v24
v_mul_f32 v18,v18,v24
v_mfma_f32_16x16x32_bf16 a[16:19],a[224:227],v[64:67],0
v_mul_f32 v19,v19,v24
v_mul_f32 v20,v20,v24
v_mfma_f32_16x16x32_bf16 a[16:19],a[228:231],v[72:75],a[16:19]
v_mul_f32 v21,v21,v24
v_mul_f32 v22,v22,v24
v_mfma_f32_16x16x32_bf16 a[20:23],a[232:235],v[64:67],0
s_getreg_b32 s92,hwreg(HW_REG_IB_STS,0,4)
s_min_u32 s4,s92,s6
s_mov_b32 s6,s92
v_mfma_f32_16x16x32_bf16 a[20:23],a[236:239],v[72:75],a[20:23]
s_cmp_gt_u32 s4,s9
s_cselect_b32 s4,15,s5
v_mul_f32 v23,v23,v24
v_mfma_f32_16x16x32_bf16 a[24:27],a[240:243],v[64:67],0
v_cvt_pk_bf16_f32 v16,v16,v17
v_cvt_pk_bf16_f32 v17,v18,v19
v_mfma_f32_16x16x32_bf16 a[24:27],a[244:247],v[72:75],a[24:27]
v_cvt_pk_bf16_f32 v18,v20,v21
v_cvt_pk_bf16_f32 v19,v22,v23
v_mfma_f32_16x16x32_bf16 a[28:31],a[248:251],v[64:67],0
global_load_lds_dwordx4 v5,s[10:11] offset:1024
v_cmp_nlt_f32 s[56:57],v60,s73
v_mfma_f32_16x16x32_bf16 a[28:31],a[252:255],v[72:75],a[28:31]
s_or_b64 s[54:55],s[54:55],s[56:57]
v_exp_f32 v80,v80
v_mfma_f32_16x16x32_bf16 v[48:51],v[28:31],v[64:67],0
s_getpc_b64 s[92:93]
s_cmp_lg_u32 s4,0
s_cbranch_scc1 L_pace_%=
v_exp_f32 v81,v81
v_exp_f32 v82,v82
v_exp_f32 v83,v83
v_exp_f32 v84,v84
v_exp_f32 v85,v85
v_exp_f32 v86,v86
v_exp_f32 v87,v87
v_exp_f32 v88,v88
v_exp_f32 v89,v89
v_mfma_f32_16x16x32_bf16 v[48:51],v[28:31],v[72:75],v[48:51]
ds_write_b128 v7,v[8:11]
v_add_u32 v1,s78,v1
v_max_f32 v25,0x00800000,v52
v_rcp_f32 v25,v25
v_max_f32 v26,0x00800000,v56
v_rcp_f32 v26,v26
v_cvt_pk_bf16_f32 v80,v80,v81
v_cvt_pk_bf16_f32 v81,v82,v83
v_cvt_pk_bf16_f32 v82,v84,v85
v_cvt_pk_bf16_f32 v83,v86,v87
v_cvt_pk_bf16_f32 v88,v88,v89
v_max_f32 v27,0x00800000,v60
v_rcp_f32 v27,v27
ds_write_b128 v7,v[16:19] offset:1024
ds_read_b128 v[44:47],v7 offset:3072
s_cmp_lg_u64 s[54:55],0
s_cbranch_scc1 L_slow0_%=
L_ok0_%=:
s_waitcnt vmcnt(14)
s_barrier
s_waitcnt lgkmcnt(0)
v_mfma_f32_16x16x32_bf16 v[112:115],a[128:131],v[224:227],v[44:47]
s_add_u32 s10,s24,s74
s_addc_u32 s11,s25,0
s_add_u32 m0,s76,16384
v_mfma_f32_16x16x32_bf16 v[112:115],a[132:135],v[232:235],v[112:115]
s_add_u32 s75,s75,16384
v_exp_f32 v90,v90
v_mfma_f32_16x16x32_bf16 v[112:115],a[136:139],v[240:243],v[112:115]
global_load_lds_dwordx4 v4,s[10:11] offset:2048
ds_read_b128 a[128:131],v1
v_mfma_f32_16x16x32_bf16 v[112:115],a[140:143],v[248:251],v[112:115]
s_add_u32 s74,s74,16384
v_exp_f32 v91,v91
v_mfma_f32_16x16x32_bf16 v[116:119],a[156:159],v[248:251],v[44:47]
s_and_b32 s74,s74,0x3ffff
v_exp_f32 v92,v92
v_mfma_f32_16x16x32_bf16 v[116:119],a[152:155],v[240:243],v[116:119]
s_and_b32 s69,s75,0xfffc0000
v_exp_f32 v93,v93
v_mfma_f32_16x16x32_bf16 v[116:119],a[148:151],v[232:235],v[116:119]
s_or_b32 s74,s74,s69
v_exp_f32 v94,v94
v_mfma_f32_16x16x32_bf16 v[116:119],a[144:147],v[224:227],v[116:119]
s_sub_u32 s76,s76,32768
v_exp_f32 v95,v95
v_mfma_f32_16x16x32_bf16 v[120:123],a[160:163],v[224:227],v[44:47]
s_and_b32 s76,s76,0x1ffff
v_cvt_pk_bf16_f32 v89,v90,v91
v_cvt_pk_bf16_f32 v90,v92,v93
v_mfma_f32_16x16x32_bf16 v[120:123],a[164:167],v[232:235],v[120:123]
s_add_u32 s75,s75,16384
ds_read_b128 a[132:135],v1 offset:256
v_cvt_pk_bf16_f32 v91,v94,v95
v_mfma_f32_16x16x32_bf16 v[120:123],a[168:171],v[240:243],v[120:123]
s_and_b32 s69,s75,0xfffc0000
v_exp_f32 v96,v96
v_mfma_f32_16x16x32_bf16 v[120:123],a[172:175],v[248:251],v[120:123]
s_add_u32 s75,s75,16384
ds_read_b128 a[136:139],v1 offset:512
v_accvgpr_read_b32 v16,a32
v_mfma_f32_16x16x32_bf16 v[124:127],a[188:191],v[248:251],v[44:47]
v_exp_f32 v97,v97
v_mfma_f32_16x16x32_bf16 v[124:127],a[184:187],v[240:243],v[124:127]
ds_read_b128 a[140:143],v1 offset:768
v_cvt_pk_bf16_f32 v96,v96,v97
v_mfma_f32_16x16x32_bf16 v[124:127],a[180:183],v[232:235],v[124:127]
v_exp_f32 v98,v98
v_mfma_f32_16x16x32_bf16 v[124:127],a[176:179],v[224:227],v[124:127]
ds_read_b128 a[156:159],v1 offset:4864
v_accvgpr_read_b32 v17,a33
v_mfma_f32_16x16x32_bf16 v[132:135],a[192:195],v[80:83],0
global_load_lds_dwordx4 v5,s[10:11] offset:3072
v_accvgpr_read_b32 v18,a34
v_mfma_f32_16x16x32_bf16 v[132:135],a[196:199],v[88:91],v[132:135]
ds_read_b128 a[152:155],v1 offset:4608
v_accvgpr_read_b32 v19,a35
v_mfma_f32_16x16x32_bf16 v[140:143],a[200:203],v[80:83],0
v_exp_f32 v99,v99
v_mfma_f32_16x16x32_bf16 v[140:143],a[204:207],v[88:91],v[140:143]
ds_read_b128 a[148:151],v1 offset:4352
v_cvt_pk_bf16_f32 v97,v98,v99
v_mfma_f32_16x16x32_bf16 v[148:151],a[208:211],v[80:83],0
v_exp_f32 v100,v100
v_mfma_f32_16x16x32_bf16 v[148:151],a[212:215],v[88:91],v[148:151]
ds_read_b128 a[144:147],v1 offset:4096
v_accvgpr_read_b32 v20,a36
v_mfma_f32_16x16x32_bf16 v[156:159],a[216:219],v[80:83],0
v_exp_f32 v101,v101
v_mfma_f32_16x16x32_bf16 v[156:159],a[220:223],v[88:91],v[156:159]
ds_read_b128 a[160:163],v1 offset:8192
v_cvt_pk_bf16_f32 v98,v100,v101
v_mfma_f32_16x16x32_bf16 v[164:167],a[224:227],v[80:83],0
v_exp_f32 v102,v102
v_mfma_f32_16x16x32_bf16 v[164:167],a[228:231],v[88:91],v[164:167]
ds_read_b128 a[164:167],v1 offset:8448
v_accvgpr_read_b32 v21,a37
v_mfma_f32_16x16x32_bf16 v[172:175],a[232:235],v[80:83],0
s_add_u32 s10,s20,s74
s_addc_u32 s11,s21,0
v_accvgpr_read_b32 v22,a38
v_mfma_f32_16x16x32_bf16 v[172:175],a[236:239],v[88:91],v[172:175]
ds_read_b128 a[168:171],v1 offset:8704
v_accvgpr_read_b32 v23,a39
v_mfma_f32_16x16x32_bf16 v[180:183],a[240:243],v[80:83],0
s_add_u32 m0,s76,0
v_exp_f32 v103,v103
v_mfma_f32_16x16x32_bf16 v[180:183],a[244:247],v[88:91],v[180:183]
ds_read_b128 a[172:175],v1 offset:8960
v_cvt_pk_bf16_f32 v99,v102,v103
v_mfma_f32_16x16x32_bf16 v[188:191],a[248:251],v[80:83],0
global_load_lds_dwordx4 v4,s[10:11] nt
v_mul_f32 v16,v16,v25
v_mfma_f32_16x16x32_bf16 v[188:191],a[252:255],v[88:91],v[188:191]
ds_read_b128 a[188:191],v1 offset:13056
v_mul_f32 v17,v17,v25
v_mfma_f32_16x16x32_bf16 v[52:55],v[28:31],v[80:83],0
v_exp_f32 v104,v104
v_mfma_f32_16x16x32_bf16 v[52:55],v[28:31],v[88:91],v[52:55]
s_waitcnt lgkmcnt(3)
ds_read_b128 a[184:187],v1 offset:12800
v_mul_f32 v18,v18,v25
v_mfma_f32_16x16x32_bf16 v[64:67],a[128:131],v[128:131],v[32:35]
v_exp_f32 v105,v105
v_mfma_f32_16x16x32_bf16 v[64:67],a[132:135],v[136:139],v[64:67]
ds_read_b128 a[180:183],v1 offset:12544
v_cvt_pk_bf16_f32 v104,v104,v105
v_mfma_f32_16x16x32_bf16 v[64:67],a[136:139],v[144:147],v[64:67]
v_exp_f32 v106,v106
)A"
R"A(v_mfma_f32_16x16x32_bf16 v[64:67],a[140:143],v[152:155],v[64:67]
ds_read_b128 a[176:179],v1 offset:12288
v_mul_f32 v19,v19,v25
v_mfma_f32_16x16x32_bf16 v[68:71],a[156:159],v[152:155],v[32:35]
v_exp_f32 v107,v107
v_mfma_f32_16x16x32_bf16 v[68:71],a[152:155],v[144:147],v[68:71]
v_exp_f32 v108,v108
v_mfma_f32_16x16x32_bf16 v[68:71],a[148:151],v[136:139],v[68:71]
v_exp_f32 v109,v109
v_mfma_f32_16x16x32_bf16 v[68:71],a[144:147],v[128:131],v[68:71]
v_exp_f32 v110,v110
v_mfma_f32_16x16x32_bf16 v[72:75],a[160:163],v[128:131],v[32:35]
v_exp_f32 v111,v111
v_mfma_f32_16x16x32_bf16 v[72:75],a[164:167],v[136:139],v[72:75]
s_waitcnt lgkmcnt(1)
v_cvt_pk_bf16_f32 v105,v106,v107
v_cvt_pk_bf16_f32 v106,v108,v109
v_mfma_f32_16x16x32_bf16 v[72:75],a[168:171],v[144:147],v[72:75]
global_load_lds_dwordx4 v4,s[10:11] offset:1024 nt
v_cvt_pk_bf16_f32 v107,v110,v111
v_mfma_f32_16x16x32_bf16 v[72:75],a[172:175],v[152:155],v[72:75]
v_exp_f32 v112,v112
v_mfma_f32_16x16x32_bf16 v[76:79],a[188:191],v[152:155],v[32:35]
v_exp_f32 v113,v113
v_mfma_f32_16x16x32_bf16 v[76:79],a[184:187],v[144:147],v[76:79]
v_exp_f32 v114,v114
v_mfma_f32_16x16x32_bf16 v[76:79],a[180:183],v[136:139],v[76:79]
s_waitcnt lgkmcnt(0)
v_exp_f32 v115,v115
v_mfma_f32_16x16x32_bf16 v[76:79],a[176:179],v[128:131],v[76:79]
v_exp_f32 v116,v116
v_mfma_f32_16x16x32_bf16 v[196:199],a[192:195],v[96:99],0
v_exp_f32 v117,v117
v_mfma_f32_16x16x32_bf16 v[196:199],a[196:199],v[104:107],v[196:199]
v_exp_f32 v118,v118
v_mfma_f32_16x16x32_bf16 v[204:207],a[200:203],v[96:99],0
v_exp_f32 v119,v119
v_mfma_f32_16x16x32_bf16 v[204:207],a[204:207],v[104:107],v[204:207]
v_cvt_pk_bf16_f32 v112,v112,v113
v_cvt_pk_bf16_f32 v113,v114,v115
v_mfma_f32_16x16x32_bf16 v[212:215],a[208:211],v[96:99],0
v_cvt_pk_bf16_f32 v114,v116,v117
v_cvt_pk_bf16_f32 v115,v118,v119
v_mfma_f32_16x16x32_bf16 v[212:215],a[212:215],v[104:107],v[212:215]
v_exp_f32 v120,v120
v_mfma_f32_16x16x32_bf16 v[220:223],a[216:219],v[96:99],0
v_exp_f32 v121,v121
v_mfma_f32_16x16x32_bf16 v[220:223],a[220:223],v[104:107],v[220:223]
v_exp_f32 v122,v122
v_mfma_f32_16x16x32_bf16 v[228:231],a[224:227],v[96:99],0
global_load_lds_dwordx4 v5,s[10:11] offset:2048 nt
v_cvt_pk_bf16_f32 v120,v120,v121
v_mfma_f32_16x16x32_bf16 v[228:231],a[228:231],v[104:107],v[228:231]
v_exp_f32 v123,v123
v_mfma_f32_16x16x32_bf16 v[236:239],a[232:235],v[96:99],0
v_exp_f32 v124,v124
v_mfma_f32_16x16x32_bf16 v[236:239],a[236:239],v[104:107],v[236:239]
v_exp_f32 v125,v125
v_mfma_f32_16x16x32_bf16 v[244:247],a[240:243],v[96:99],0
v_exp_f32 v126,v126
v_mfma_f32_16x16x32_bf16 v[244:247],a[244:247],v[104:107],v[244:247]
v_exp_f32 v127,v127
v_mfma_f32_16x16x32_bf16 v[252:255],a[248:251],v[96:99],0
v_cvt_pk_bf16_f32 v121,v122,v123
v_cvt_pk_bf16_f32 v122,v124,v125
v_mfma_f32_16x16x32_bf16 v[252:255],a[252:255],v[104:107],v[252:255]
v_cvt_pk_bf16_f32 v123,v126,v127
v_mul_f32 v20,v20,v25
v_mfma_f32_16x16x32_bf16 v[56:59],v[28:31],v[96:99],0
v_mul_f32 v21,v21,v25
v_mul_f32 v22,v22,v25
v_mfma_f32_16x16x32_bf16 v[56:59],v[28:31],v[104:107],v[56:59]
v_mul_f32 v23,v23,v25
v_cvt_pk_bf16_f32 v16,v16,v17
v_mfma_f32_16x16x32_bf16 v[80:83],a[128:131],v[160:163],v[36:39]
v_cvt_pk_bf16_f32 v17,v18,v19
v_cvt_pk_bf16_f32 v18,v20,v21
v_mfma_f32_16x16x32_bf16 v[80:83],a[132:135],v[168:171],v[80:83]
v_cvt_pk_bf16_f32 v19,v22,v23
v_accvgpr_read_b32 v20,a44
v_mfma_f32_16x16x32_bf16 v[80:83],a[136:139],v[176:179],v[80:83]
ds_write_b128 v7,v[16:19] offset:2048
v_mfma_f32_16x16x32_bf16 v[80:83],a[140:143],v[184:187],v[80:83]
v_accvgpr_read_b32 v21,a45
v_accvgpr_read_b32 v22,a46
v_mfma_f32_16x16x32_bf16 v[84:87],a[156:159],v[184:187],v[36:39]
v_accvgpr_read_b32 v16,a40
v_accvgpr_read_b32 v17,a41
v_mfma_f32_16x16x32_bf16 v[84:87],a[152:155],v[176:179],v[84:87]
global_load_lds_dwordx4 v5,s[10:11] offset:3072
v_accvgpr_read_b32 v18,a42
v_mfma_f32_16x16x32_bf16 v[84:87],a[148:151],v[168:171],v[84:87]
v_accvgpr_read_b32 v19,a43
v_accvgpr_read_b32 v23,a47
v_mfma_f32_16x16x32_bf16 v[84:87],a[144:147],v[160:163],v[84:87]
v_accvgpr_read_b32 v8,a48
v_accvgpr_read_b32 v9,a49
v_mfma_f32_16x16x32_bf16 v[88:91],a[160:163],v[160:163],v[36:39]
v_accvgpr_read_b32 v10,a50
v_accvgpr_read_b32 v11,a51
v_mfma_f32_16x16x32_bf16 v[88:91],a[164:167],v[168:171],v[88:91]
v_mul_f32 v16,v16,v25
v_mul_f32 v17,v17,v25
v_mfma_f32_16x16x32_bf16 v[88:91],a[168:171],v[176:179],v[88:91]
v_mul_f32 v18,v18,v25
v_mul_f32 v19,v19,v25
v_mfma_f32_16x16x32_bf16 v[88:91],a[172:175],v[184:187],v[88:91]
v_mul_f32 v20,v20,v25
v_mul_f32 v21,v21,v25
v_mfma_f32_16x16x32_bf16 v[92:95],a[188:191],v[184:187],v[36:39]
v_mul_f32 v22,v22,v25
v_mul_f32 v23,v23,v25
v_mfma_f32_16x16x32_bf16 v[92:95],a[184:187],v[176:179],v[92:95]
v_cvt_pk_bf16_f32 v16,v16,v17
v_cvt_pk_bf16_f32 v17,v18,v19
v_mfma_f32_16x16x32_bf16 v[92:95],a[180:183],v[168:171],v[92:95]
v_cvt_pk_bf16_f32 v18,v20,v21
v_cvt_pk_bf16_f32 v19,v22,v23
v_mfma_f32_16x16x32_bf16 v[92:95],a[176:179],v[160:163],v[92:95]
s_add_u32 s10,s24,s74
s_addc_u32 s11,s25,0
v_accvgpr_read_b32 v12,a52
v_mfma_f32_16x16x32_bf16 a[32:35],a[192:195],v[112:115],0
ds_write_b128 v7,v[16:19] offset:3072
v_mfma_f32_16x16x32_bf16 a[32:35],a[196:199],v[120:123],a[32:35]
s_add_u32 m0,s76,16384
v_accvgpr_read_b32 v13,a53
v_accvgpr_read_b32 v14,a54
v_mfma_f32_16x16x32_bf16 a[36:39],a[200:203],v[112:115],0
v_accvgpr_read_b32 v15,a55
v_accvgpr_read_b32 v16,a56
v_mfma_f32_16x16x32_bf16 a[36:39],a[204:207],v[120:123],a[36:39]
global_load_lds_dwordx4 v4,s[10:11]
v_accvgpr_read_b32 v17,a57
v_mfma_f32_16x16x32_bf16 a[40:43],a[208:211],v[112:115],0
v_accvgpr_read_b32 v18,a58
v_accvgpr_read_b32 v19,a59
v_mfma_f32_16x16x32_bf16 a[40:43],a[212:215],v[120:123],a[40:43]
v_accvgpr_read_b32 v20,a60
v_accvgpr_read_b32 v21,a61
v_mfma_f32_16x16x32_bf16 a[44:47],a[216:219],v[112:115],0
v_accvgpr_read_b32 v22,a62
v_accvgpr_read_b32 v23,a63
v_mfma_f32_16x16x32_bf16 a[44:47],a[220:223],v[120:123],a[44:47]
v_add_u32 v2,s78,v2
v_add_u32 v3,s78,v3
v_mfma_f32_16x16x32_bf16 a[48:51],a[224:227],v[112:115],0
s_sub_u32 s78,s77,32768
ds_read_b64_tr_b16 a[192:193],v2
ds_read_b64_tr_b16 a[194:195],v2 offset:4096
v_mfma_f32_16x16x32_bf16 a[48:51],a[228:231],v[120:123],a[48:51]
s_and_b32 s78,s78,0x1ffff
ds_read_b64_tr_b16 a[196:197],v2 offset:8192
v_mul_f32 v8,v8,v25
v_mfma_f32_16x16x32_bf16 a[52:55],a[232:235],v[112:115],0
s_sub_u32 s78,s78,s77
ds_read_b64_tr_b16 a[198:199],v2 offset:12288
v_mul_f32 v9,v9,v25
v_mfma_f32_16x16x32_bf16 a[52:55],a[236:239],v[120:123],a[52:55]
s_add_u32 s77,s77,s78
ds_read_b64_tr_b16 a[200:201],v3
v_mul_f32 v10,v10,v25
v_mfma_f32_16x16x32_bf16 a[56:59],a[240:243],v[112:115],0
ds_read_b64_tr_b16 a[202:203],v3 offset:4096
ds_read_b64_tr_b16 a[204:205],v3 offset:8192
v_mfma_f32_16x16x32_bf16 a[56:59],a[244:247],v[120:123],a[56:59]
ds_read_b64_tr_b16 a[206:207],v3 offset:12288
v_mul_f32 v11,v11,v25
v_mfma_f32_16x16x32_bf16 a[60:63],a[248:251],v[112:115],0
ds_read_b64_tr_b16 a[208:209],v2 offset:256
v_mul_f32 v12,v12,v25
v_mfma_f32_16x16x32_bf16 a[60:63],a[252:255],v[120:123],a[60:63]
ds_read_b64_tr_b16 a[210:211],v2 offset:4352
v_mul_f32 v13,v13,v25
v_mfma_f32_16x16x32_bf16 v[60:63],v[28:31],v[112:115],0
ds_read_b64_tr_b16 a[212:213],v2 offset:8448
ds_read_b64_tr_b16 a[214:215],v2 offset:12544
v_mfma_f32_16x16x32_bf16 v[60:63],v[28:31],v[120:123],v[60:63]
global_load_lds_dwordx4 v5,s[10:11] offset:1024
ds_read_b64_tr_b16 a[216:217],v3 offset:256
v_mfma_f32_16x16x32_bf16 v[96:99],a[128:131],v[192:195],v[40:43]
v_exp_f32 v64,v64
v_mfma_f32_16x16x32_bf16 v[96:99],a[132:135],v[200:203],v[96:99]
v_exp_f32 v65,v65
v_mfma_f32_16x16x32_bf16 v[96:99],a[136:139],v[208:211],v[96:99]
v_exp_f32 v66,v66
v_mfma_f32_16x16x32_bf16 v[96:99],a[140:143],v[216:219],v[96:99]
v_exp_f32 v67,v67
v_mfma_f32_16x16x32_bf16 v[100:103],a[156:159],v[216:219],v[40:43]
v_exp_f32 v68,v68
v_mfma_f32_16x16x32_bf16 v[100:103],a[152:155],v[208:211],v[100:103]
v_exp_f32 v69,v69
v_mfma_f32_16x16x32_bf16 v[100:103],a[148:151],v[200:203],v[100:103]
v_exp_f32 v70,v70
v_mfma_f32_16x16x32_bf16 v[100:103],a[144:147],v[192:195],v[100:103]
v_exp_f32 v71,v71
v_mfma_f32_16x16x32_bf16 v[104:107],a[160:163],v[192:195],v[40:43]
ds_read_b64_tr_b16 a[218:219],v3 offset:4352
v_cvt_pk_bf16_f32 v64,v64,v65
v_mfma_f32_16x16x32_bf16 v[104:107],a[164:167],v[200:203],v[104:107]
v_cvt_pk_bf16_f32 v65,v66,v67
v_cvt_pk_bf16_f32 v66,v68,v69
v_mfma_f32_16x16x32_bf16 v[104:107],a[168:171],v[208:211],v[104:107]
ds_read_b64_tr_b16 a[220:221],v3 offset:8448
v_cvt_pk_bf16_f32 v67,v70,v71
v_mfma_f32_16x16x32_bf16 v[104:107],a[172:175],v[216:219],v[104:107]
v_exp_f32 v72,v72
v_mfma_f32_16x16x32_bf16 v[108:111],a[188:191],v[216:219],v[40:43]
v_exp_f32 v73,v73
L_pd8_%=:
s_cbranch_execz L_trt0_%=
v_mfma_f32_16x16x32_bf16 v[108:111],a[184:187],v[208:211],v[108:111]
global_load_lds_dwordx4 v4,s[10:11] offset:2048
v_exp_f32 v74,v74
v_exp_f32 v75,v75
v_exp_f32 v76,v76
v_exp_f32 v77,v77
v_exp_f32 v78,v78
v_exp_f32 v79,v79
v_mfma_f32_16x16x32_bf16 v[108:111],a[180:183],v[200:203],v[108:111]
ds_read_b64_tr_b16 a[222:223],v3 offset:12544
v_cvt_pk_bf16_f32 v72,v72,v73
v_cvt_pk_bf16_f32 v73,v74,v75
v_cvt_pk_bf16_f32 v74,v76,v77
v_cvt_pk_bf16_f32 v75,v78,v79
v_mfma_f32_16x16x32_bf16 v[108:111],a[176:179],v[192:195],v[108:111]
s_waitcnt lgkmcnt(3)
ds_read_b64_tr_b16 a[224:225],v2 offset:512
ds_read_b64_tr_b16 a[226:227],v2 offset:4608
v_mfma_f32_16x16x32_bf16 a[0:3],a[192:195],v[64:67],a[0:3]
ds_read_b64_tr_b16 a[228:229],v2 offset:8704
v_mul_f32 v14,v14,v25
v_mfma_f32_16x16x32_bf16 a[0:3],a[196:199],v[72:75],a[0:3]
ds_read_b64_tr_b16 a[230:231],v2 offset:12800
v_mul_f32 v15,v15,v25
v_mfma_f32_16x16x32_bf16 a[4:7],a[200:203],v[64:67],a[4:7]
ds_read_b64_tr_b16 a[232:233],v3 offset:512
v_cvt_pk_bf16_f32 v8,v8,v9
v_mfma_f32_16x16x32_bf16 a[4:7],a[204:207],v[72:75],a[4:7]
ds_read_b64_tr_b16 a[234:235],v3 offset:4608
ds_read_b64_tr_b16 a[236:237],v3 offset:8704
v_mfma_f32_16x16x32_bf16 a[8:11],a[208:211],v[64:67],a[8:11]
ds_read_b64_tr_b16 a[238:239],v3 offset:12800
v_cvt_pk_bf16_f32 v9,v10,v11
v_mfma_f32_16x16x32_bf16 a[8:11],a[212:215],v[72:75],a[8:11]
s_waitcnt lgkmcnt(9)
ds_read_b64_tr_b16 a[240:241],v2 offset:768
v_cvt_pk_bf16_f32 v10,v12,v13
v_mfma_f32_16x16x32_bf16 a[12:15],a[216:219],v[64:67],a[12:15]
s_waitcnt lgkmcnt(9)
ds_read_b64_tr_b16 a[242:243],v2 offset:4864
v_cvt_pk_bf16_f32 v11,v14,v15
v_mfma_f32_16x16x32_bf16 a[12:15],a[220:223],v[72:75],a[12:15]
s_waitcnt lgkmcnt(8)
ds_read_b64_tr_b16 a[244:245],v2 offset:8960
ds_read_b64_tr_b16 a[246:247],v2 offset:13056
v_mfma_f32_16x16x32_bf16 a[16:19],a[224:227],v[64:67],a[16:19]
s_waitcnt lgkmcnt(8)
ds_read_b64_tr_b16 a[248:249],v3 offset:768
v_mul_f32 v16,v16,v25
v_mfma_f32_16x16x32_bf16 a[16:19],a[228:231],v[72:75],a[16:19]
s_waitcnt lgkmcnt(1)
ds_read_b64_tr_b16 a[250:251],v3 offset:4864
v_mul_f32 v17,v17,v25
v_mfma_f32_16x16x32_bf16 a[20:23],a[232:235],v[64:67],a[20:23]
s_getreg_b32 s92,hwreg(HW_REG_IB_STS,0,4)
s_min_u32 s4,s92,s6
ds_read_b64_tr_b16 a[252:253],v3 offset:8960
v_mfma_f32_16x16x32_bf16 a[20:23],a[236:239],v[72:75],a[20:23]
s_mov_b32 s6,s92
ds_read_b64_tr_b16 a[254:255],v3 offset:13056
v_mul_f32 v18,v18,v25
v_mfma_f32_16x16x32_bf16 a[24:27],a[240:243],v[64:67],a[24:27]
s_cmp_gt_u32 s4,s9
s_cselect_b32 s4,15,s5
v_mul_f32 v19,v19,v25
v_mfma_f32_16x16x32_bf16 a[24:27],a[244:247],v[72:75],a[24:27]
s_waitcnt lgkmcnt(2)
v_mul_f32 v20,v20,v25
v_mul_f32 v21,v21,v25
v_mfma_f32_16x16x32_bf16 a[28:31],a[248:251],v[64:67],a[28:31]
s_waitcnt lgkmcnt(0)
v_mul_f32 v22,v22,v25
v_mul_f32 v23,v23,v25
v_mfma_f32_16x16x32_bf16 a[28:31],a[252:255],v[72:75],a[28:31]
s_getpc_b64 s[92:93]
s_cmp_lg_u32 s4,0
s_cbranch_scc1 L_pace_%=
v_mfma_f32_16x16x32_bf16 v[48:51],v[28:31],v[64:67],v[48:51]
v_cvt_pk_bf16_f32 v12,v16,v17
v_cvt_pk_bf16_f32 v13,v18,v19
v_mfma_f32_16x16x32_bf16 v[48:51],v[28:31],v[72:75],v[48:51]
s_add_u32 s10,s24,s74
s_addc_u32 s11,s25,0
v_cvt_pk_bf16_f32 v14,v20,v21
s_waitcnt vmcnt(15)
s_barrier
v_mfma_f32_16x16x32_bf16 v[112:115],a[128:131],v[224:227],v[44:47]
s_add_u32 m0,s76,16384
v_cvt_pk_bf16_f32 v15,v22,v23
v_accvgpr_read_b32 v16,a112
)A"
R"A(v_mfma_f32_16x16x32_bf16 v[112:115],a[132:135],v[232:235],v[112:115]
s_add_u32 s74,s74,16384
v_accvgpr_read_b32 v17,a113
v_accvgpr_read_b32 v18,a114
v_mfma_f32_16x16x32_bf16 v[112:115],a[136:139],v[240:243],v[112:115]
global_load_lds_dwordx4 v5,s[10:11] offset:3072
v_accvgpr_read_b32 v19,a115
v_mfma_f32_16x16x32_bf16 v[112:115],a[140:143],v[248:251],v[112:115]
s_and_b32 s74,s74,0x3ffff
v_accvgpr_read_b32 v20,a116
v_accvgpr_read_b32 v21,a117
v_mfma_f32_16x16x32_bf16 v[116:119],a[156:159],v[248:251],v[44:47]
s_or_b32 s74,s74,s69
v_accvgpr_read_b32 v22,a118
v_accvgpr_read_b32 v23,a119
v_mfma_f32_16x16x32_bf16 v[116:119],a[152:155],v[240:243],v[116:119]
s_sub_u32 s76,s76,32768
v_mul_f32 v16,v16,v27
v_mul_f32 v17,v17,v27
v_mfma_f32_16x16x32_bf16 v[116:119],a[148:151],v[232:235],v[116:119]
s_and_b32 s76,s76,0x1ffff
v_mul_f32 v18,v18,v27
v_mul_f32 v19,v19,v27
v_mfma_f32_16x16x32_bf16 v[116:119],a[144:147],v[224:227],v[116:119]
s_and_b32 s69,s75,0xfffc0000
v_mul_f32 v20,v20,v27
v_mul_f32 v21,v21,v27
v_mfma_f32_16x16x32_bf16 v[120:123],a[160:163],v[224:227],v[44:47]
s_add_u32 s75,s75,16384
v_mul_f32 v22,v22,v27
v_mul_f32 v23,v23,v27
v_mfma_f32_16x16x32_bf16 v[120:123],a[164:167],v[232:235],v[120:123]
v_cvt_pk_bf16_f32 v16,v16,v17
v_cvt_pk_bf16_f32 v17,v18,v19
v_mfma_f32_16x16x32_bf16 v[120:123],a[168:171],v[240:243],v[120:123]
v_cvt_pk_bf16_f32 v18,v20,v21
v_cvt_pk_bf16_f32 v19,v22,v23
v_mfma_f32_16x16x32_bf16 v[120:123],a[172:175],v[248:251],v[120:123]
v_accvgpr_write_b32 a112,v16
v_accvgpr_write_b32 a113,v17
v_mfma_f32_16x16x32_bf16 v[124:127],a[188:191],v[248:251],v[44:47]
v_accvgpr_write_b32 a114,v18
v_accvgpr_write_b32 a115,v19
v_accvgpr_write_b32 a116,v8
v_accvgpr_write_b32 a117,v9
v_accvgpr_write_b32 a118,v10
v_accvgpr_write_b32 a119,v11
v_exp_f32 v80,v80
v_exp_f32 v81,v81
v_exp_f32 v82,v82
v_exp_f32 v83,v83
v_exp_f32 v84,v84
v_exp_f32 v85,v85
v_exp_f32 v86,v86
v_exp_f32 v87,v87
v_mfma_f32_16x16x32_bf16 v[124:127],a[184:187],v[240:243],v[124:127]
v_cvt_pk_bf16_f32 v80,v80,v81
v_cvt_pk_bf16_f32 v81,v82,v83
v_cvt_pk_bf16_f32 v82,v84,v85
v_cvt_pk_bf16_f32 v83,v86,v87
v_exp_f32 v88,v88
v_exp_f32 v89,v89
v_exp_f32 v90,v90
v_exp_f32 v91,v91
v_exp_f32 v92,v92
v_exp_f32 v93,v93
v_exp_f32 v94,v94
v_exp_f32 v95,v95
v_mfma_f32_16x16x32_bf16 v[124:127],a[180:183],v[232:235],v[124:127]
v_cvt_pk_bf16_f32 v88,v88,v89
v_cvt_pk_bf16_f32 v89,v90,v91
v_cvt_pk_bf16_f32 v90,v92,v93
v_cvt_pk_bf16_f32 v91,v94,v95
v_mfma_f32_16x16x32_bf16 v[124:127],a[176:179],v[224:227],v[124:127]
s_add_u32 s10,s20,s74
s_addc_u32 s11,s21,0
s_add_u32 m0,s76,0
v_mfma_f32_16x16x32_bf16 v[132:135],a[192:195],v[80:83],v[132:135]
global_load_lds_dwordx4 v4,s[10:11] nt
v_add_u32 v1,s78,v1
v_mfma_f32_16x16x32_bf16 v[132:135],a[196:199],v[88:91],v[132:135]
ds_read_b128 a[128:131],v1
v_accvgpr_read_b32 v16,a120
v_mfma_f32_16x16x32_bf16 v[140:143],a[200:203],v[80:83],v[140:143]
v_exp_f32 v96,v96
v_mfma_f32_16x16x32_bf16 v[140:143],a[204:207],v[88:91],v[140:143]
ds_read_b128 a[132:135],v1 offset:256
v_accvgpr_read_b32 v17,a121
v_mfma_f32_16x16x32_bf16 v[148:151],a[208:211],v[80:83],v[148:151]
v_exp_f32 v97,v97
v_mfma_f32_16x16x32_bf16 v[148:151],a[212:215],v[88:91],v[148:151]
ds_read_b128 a[136:139],v1 offset:512
v_cvt_pk_bf16_f32 v96,v96,v97
v_mfma_f32_16x16x32_bf16 v[156:159],a[216:219],v[80:83],v[156:159]
v_exp_f32 v98,v98
v_mfma_f32_16x16x32_bf16 v[156:159],a[220:223],v[88:91],v[156:159]
ds_read_b128 a[140:143],v1 offset:768
v_accvgpr_read_b32 v18,a122
v_mfma_f32_16x16x32_bf16 v[164:167],a[224:227],v[80:83],v[164:167]
v_exp_f32 v99,v99
v_mfma_f32_16x16x32_bf16 v[164:167],a[228:231],v[88:91],v[164:167]
ds_read_b128 a[156:159],v1 offset:4864
v_cvt_pk_bf16_f32 v97,v98,v99
v_mfma_f32_16x16x32_bf16 v[172:175],a[232:235],v[80:83],v[172:175]
v_exp_f32 v100,v100
v_mfma_f32_16x16x32_bf16 v[172:175],a[236:239],v[88:91],v[172:175]
ds_read_b128 a[152:155],v1 offset:4608
v_accvgpr_read_b32 v19,a123
v_mfma_f32_16x16x32_bf16 v[180:183],a[240:243],v[80:83],v[180:183]
v_exp_f32 v101,v101
v_mfma_f32_16x16x32_bf16 v[180:183],a[244:247],v[88:91],v[180:183]
ds_read_b128 a[148:151],v1 offset:4352
v_cvt_pk_bf16_f32 v98,v100,v101
v_mfma_f32_16x16x32_bf16 v[188:191],a[248:251],v[80:83],v[188:191]
global_load_lds_dwordx4 v4,s[10:11] offset:1024 nt
v_accvgpr_read_b32 v20,a124
v_mfma_f32_16x16x32_bf16 v[188:191],a[252:255],v[88:91],v[188:191]
ds_read_b128 a[144:147],v1 offset:4096
v_accvgpr_read_b32 v21,a125
v_mfma_f32_16x16x32_bf16 v[52:55],v[28:31],v[80:83],v[52:55]
v_exp_f32 v102,v102
v_mfma_f32_16x16x32_bf16 v[52:55],v[28:31],v[88:91],v[52:55]
s_waitcnt lgkmcnt(3)
ds_read_b128 a[160:163],v1 offset:8192
v_accvgpr_read_b32 v22,a126
v_mfma_f32_16x16x32_bf16 v[64:67],a[128:131],v[128:131],v[32:35]
v_exp_f32 v103,v103
v_mfma_f32_16x16x32_bf16 v[64:67],a[132:135],v[136:139],v[64:67]
ds_read_b128 a[164:167],v1 offset:8448
v_cvt_pk_bf16_f32 v99,v102,v103
v_mfma_f32_16x16x32_bf16 v[64:67],a[136:139],v[144:147],v[64:67]
v_exp_f32 v104,v104
v_mfma_f32_16x16x32_bf16 v[64:67],a[140:143],v[152:155],v[64:67]
ds_read_b128 a[168:171],v1 offset:8704
v_accvgpr_read_b32 v23,a127
v_mfma_f32_16x16x32_bf16 v[68:71],a[156:159],v[152:155],v[32:35]
s_waitcnt lgkmcnt(4)
v_exp_f32 v105,v105
v_mfma_f32_16x16x32_bf16 v[68:71],a[152:155],v[144:147],v[68:71]
ds_read_b128 a[172:175],v1 offset:8960
v_cvt_pk_bf16_f32 v104,v104,v105
v_mfma_f32_16x16x32_bf16 v[68:71],a[148:151],v[136:139],v[68:71]
s_waitcnt lgkmcnt(4)
v_exp_f32 v106,v106
v_mfma_f32_16x16x32_bf16 v[68:71],a[144:147],v[128:131],v[68:71]
s_waitcnt lgkmcnt(3)
ds_read_b128 a[188:191],v1 offset:13056
v_mul_f32 v16,v16,v27
v_mfma_f32_16x16x32_bf16 v[72:75],a[160:163],v[128:131],v[32:35]
s_waitcnt lgkmcnt(3)
v_exp_f32 v107,v107
v_mfma_f32_16x16x32_bf16 v[72:75],a[164:167],v[136:139],v[72:75]
s_waitcnt lgkmcnt(1)
ds_read_b128 a[184:187],v1 offset:12800
v_cvt_pk_bf16_f32 v105,v106,v107
v_mfma_f32_16x16x32_bf16 v[72:75],a[168:171],v[144:147],v[72:75]
global_load_lds_dwordx4 v5,s[10:11] offset:2048 nt
ds_read_b128 a[180:183],v1 offset:12544
v_mfma_f32_16x16x32_bf16 v[72:75],a[172:175],v[152:155],v[72:75]
s_waitcnt lgkmcnt(2)
ds_read_b128 a[176:179],v1 offset:12288
v_mul_f32 v17,v17,v27
v_mfma_f32_16x16x32_bf16 v[76:79],a[188:191],v[152:155],v[32:35]
s_waitcnt lgkmcnt(2)
v_exp_f32 v108,v108
v_mfma_f32_16x16x32_bf16 v[76:79],a[184:187],v[144:147],v[76:79]
s_waitcnt lgkmcnt(1)
v_exp_f32 v109,v109
v_exp_f32 v110,v110
v_exp_f32 v111,v111
v_mfma_f32_16x16x32_bf16 v[76:79],a[180:183],v[136:139],v[76:79]
s_waitcnt lgkmcnt(0)
v_cvt_pk_bf16_f32 v106,v108,v109
v_cvt_pk_bf16_f32 v107,v110,v111
v_mfma_f32_16x16x32_bf16 v[76:79],a[176:179],v[128:131],v[76:79]
v_mul_f32 v18,v18,v27
v_mul_f32 v19,v19,v27
v_mfma_f32_16x16x32_bf16 v[196:199],a[192:195],v[96:99],v[196:199]
v_mul_f32 v20,v20,v27
v_mul_f32 v21,v21,v27
v_mfma_f32_16x16x32_bf16 v[196:199],a[196:199],v[104:107],v[196:199]
v_mul_f32 v22,v22,v27
v_mul_f32 v23,v23,v27
v_mfma_f32_16x16x32_bf16 v[204:207],a[200:203],v[96:99],v[204:207]
v_cvt_pk_bf16_f32 v16,v16,v17
v_cvt_pk_bf16_f32 v17,v18,v19
v_mfma_f32_16x16x32_bf16 v[204:207],a[204:207],v[104:107],v[204:207]
v_cvt_pk_bf16_f32 v18,v20,v21
v_cvt_pk_bf16_f32 v19,v22,v23
v_mfma_f32_16x16x32_bf16 v[212:215],a[208:211],v[96:99],v[212:215]
v_accvgpr_write_b32 a120,v16
v_accvgpr_write_b32 a121,v17
v_mfma_f32_16x16x32_bf16 v[212:215],a[212:215],v[104:107],v[212:215]
v_accvgpr_write_b32 a122,v18
v_accvgpr_write_b32 a123,v19
v_mfma_f32_16x16x32_bf16 v[220:223],a[216:219],v[96:99],v[220:223]
v_accvgpr_write_b32 a124,v12
v_accvgpr_write_b32 a125,v13
v_mfma_f32_16x16x32_bf16 v[220:223],a[220:223],v[104:107],v[220:223]
v_accvgpr_write_b32 a126,v14
v_accvgpr_write_b32 a127,v15
v_mfma_f32_16x16x32_bf16 v[228:231],a[224:227],v[96:99],v[228:231]
global_load_lds_dwordx4 v5,s[10:11] offset:3072
v_add_u32 v2,s78,v2
v_mfma_f32_16x16x32_bf16 v[228:231],a[228:231],v[104:107],v[228:231]
v_exp_f32 v112,v112
v_mfma_f32_16x16x32_bf16 v[236:239],a[232:235],v[96:99],v[236:239]
v_exp_f32 v113,v113
v_mfma_f32_16x16x32_bf16 v[236:239],a[236:239],v[104:107],v[236:239]
v_exp_f32 v114,v114
v_mfma_f32_16x16x32_bf16 v[244:247],a[240:243],v[96:99],v[244:247]
v_exp_f32 v115,v115
v_mfma_f32_16x16x32_bf16 v[244:247],a[244:247],v[104:107],v[244:247]
v_exp_f32 v116,v116
v_mfma_f32_16x16x32_bf16 v[252:255],a[248:251],v[96:99],v[252:255]
v_exp_f32 v117,v117
v_mfma_f32_16x16x32_bf16 v[252:255],a[252:255],v[104:107],v[252:255]
v_exp_f32 v118,v118
v_mfma_f32_16x16x32_bf16 v[56:59],v[28:31],v[96:99],v[56:59]
v_exp_f32 v119,v119
v_mfma_f32_16x16x32_bf16 v[56:59],v[28:31],v[104:107],v[56:59]
v_cvt_pk_bf16_f32 v112,v112,v113
v_cvt_pk_bf16_f32 v113,v114,v115
v_mfma_f32_16x16x32_bf16 v[80:83],a[128:131],v[160:163],v[36:39]
v_cvt_pk_bf16_f32 v114,v116,v117
v_cvt_pk_bf16_f32 v115,v118,v119
v_mfma_f32_16x16x32_bf16 v[80:83],a[132:135],v[168:171],v[80:83]
s_add_u32 s10,s24,s74
s_addc_u32 s11,s25,0
v_add_u32 v3,s78,v3
v_mfma_f32_16x16x32_bf16 v[80:83],a[136:139],v[176:179],v[80:83]
s_add_u32 s74,s74,16384
v_exp_f32 v120,v120
v_mfma_f32_16x16x32_bf16 v[80:83],a[140:143],v[184:187],v[80:83]
s_add_u32 m0,s76,16384
v_exp_f32 v121,v121
v_mfma_f32_16x16x32_bf16 v[84:87],a[156:159],v[184:187],v[36:39]
s_and_b32 s74,s74,0x3ffff
v_exp_f32 v122,v122
v_mfma_f32_16x16x32_bf16 v[84:87],a[152:155],v[176:179],v[84:87]
global_load_lds_dwordx4 v4,s[10:11]
v_cvt_pk_bf16_f32 v120,v120,v121
v_mfma_f32_16x16x32_bf16 v[84:87],a[148:151],v[168:171],v[84:87]
s_or_b32 s74,s74,s69
v_exp_f32 v123,v123
v_mfma_f32_16x16x32_bf16 v[84:87],a[144:147],v[160:163],v[84:87]
s_sub_u32 s76,s76,32768
v_exp_f32 v124,v124
v_mfma_f32_16x16x32_bf16 v[88:91],a[160:163],v[160:163],v[36:39]
s_and_b32 s76,s76,0x1ffff
v_exp_f32 v125,v125
v_mfma_f32_16x16x32_bf16 v[88:91],a[164:167],v[168:171],v[88:91]
s_sub_u32 s78,s77,32768
v_exp_f32 v126,v126
v_mfma_f32_16x16x32_bf16 v[88:91],a[168:171],v[176:179],v[88:91]
s_and_b32 s78,s78,0x1ffff
v_exp_f32 v127,v127
v_mfma_f32_16x16x32_bf16 v[88:91],a[172:175],v[184:187],v[88:91]
s_sub_u32 s78,s78,s77
v_cvt_pk_bf16_f32 v121,v122,v123
v_cvt_pk_bf16_f32 v122,v124,v125
v_mfma_f32_16x16x32_bf16 v[92:95],a[188:191],v[184:187],v[36:39]
s_and_b32 s69,s75,0xfffc0000
v_cvt_pk_bf16_f32 v123,v126,v127
v_accvgpr_read_b32 v12,a100
v_mfma_f32_16x16x32_bf16 v[92:95],a[184:187],v[176:179],v[92:95]
s_add_u32 s77,s77,s78
v_exp_f32 v64,v64
v_mfma_f32_16x16x32_bf16 v[92:95],a[180:183],v[168:171],v[92:95]
s_add_u32 s75,s75,16384
v_exp_f32 v65,v65
v_mfma_f32_16x16x32_bf16 v[92:95],a[176:179],v[160:163],v[92:95]
v_exp_f32 v66,v66
v_mfma_f32_16x16x32_bf16 a[32:35],a[192:195],v[112:115],a[32:35]
ds_read_b64_tr_b16 a[192:193],v2
ds_read_b64_tr_b16 a[194:195],v2 offset:4096
v_mfma_f32_16x16x32_bf16 a[32:35],a[196:199],v[120:123],a[32:35]
ds_read_b64_tr_b16 a[196:197],v2 offset:8192
v_cvt_pk_bf16_f32 v64,v64,v65
v_mfma_f32_16x16x32_bf16 a[36:39],a[200:203],v[112:115],a[36:39]
ds_read_b64_tr_b16 a[198:199],v2 offset:12288
v_accvgpr_read_b32 v13,a101
v_mfma_f32_16x16x32_bf16 a[36:39],a[204:207],v[120:123],a[36:39]
global_load_lds_dwordx4 v5,s[10:11] offset:1024
ds_read_b64_tr_b16 a[200:201],v3
v_mfma_f32_16x16x32_bf16 a[40:43],a[208:211],v[112:115],a[40:43]
ds_read_b64_tr_b16 a[202:203],v3 offset:4096
ds_read_b64_tr_b16 a[204:205],v3 offset:8192
v_mfma_f32_16x16x32_bf16 a[40:43],a[212:215],v[120:123],a[40:43]
ds_read_b64_tr_b16 a[206:207],v3 offset:12288
v_accvgpr_read_b32 v14,a102
v_mfma_f32_16x16x32_bf16 a[44:47],a[216:219],v[112:115],a[44:47]
ds_read_b64_tr_b16 a[208:209],v2 offset:256
v_accvgpr_read_b32 v15,a103
v_mfma_f32_16x16x32_bf16 a[44:47],a[220:223],v[120:123],a[44:47]
ds_read_b64_tr_b16 a[210:211],v2 offset:4352
v_accvgpr_read_b32 v8,a96
v_mfma_f32_16x16x32_bf16 a[48:51],a[224:227],v[112:115],a[48:51]
ds_read_b64_tr_b16 a[212:213],v2 offset:8448
ds_read_b64_tr_b16 a[214:215],v2 offset:12544
v_mfma_f32_16x16x32_bf16 a[48:51],a[228:231],v[120:123],a[48:51]
v_exp_f32 v67,v67
v_mfma_f32_16x16x32_bf16 a[52:55],a[232:235],v[112:115],a[52:55]
v_exp_f32 v68,v68
v_mfma_f32_16x16x32_bf16 a[52:55],a[236:239],v[120:123],a[52:55]
v_exp_f32 v69,v69
)A"
R"A(v_mfma_f32_16x16x32_bf16 a[56:59],a[240:243],v[112:115],a[56:59]
v_exp_f32 v70,v70
v_mfma_f32_16x16x32_bf16 a[56:59],a[244:247],v[120:123],a[56:59]
v_exp_f32 v71,v71
v_mfma_f32_16x16x32_bf16 a[60:63],a[248:251],v[112:115],a[60:63]
ds_read_b64_tr_b16 a[216:217],v3 offset:256
ds_read_b64_tr_b16 a[218:219],v3 offset:4352
v_mfma_f32_16x16x32_bf16 a[60:63],a[252:255],v[120:123],a[60:63]
v_cvt_pk_bf16_f32 v65,v66,v67
v_cvt_pk_bf16_f32 v66,v68,v69
v_mfma_f32_16x16x32_bf16 v[60:63],v[28:31],v[112:115],v[60:63]
ds_read_b64_tr_b16 a[220:221],v3 offset:8448
v_cvt_pk_bf16_f32 v67,v70,v71
v_mfma_f32_16x16x32_bf16 v[60:63],v[28:31],v[120:123],v[60:63]
global_load_lds_dwordx4 v4,s[10:11] offset:2048
ds_read_b64_tr_b16 a[222:223],v3 offset:12544
v_mfma_f32_16x16x32_bf16 v[96:99],a[128:131],v[192:195],v[40:43]
v_exp_f32 v72,v72
v_mfma_f32_16x16x32_bf16 v[96:99],a[132:135],v[200:203],v[96:99]
v_exp_f32 v73,v73
v_mfma_f32_16x16x32_bf16 v[96:99],a[136:139],v[208:211],v[96:99]
v_exp_f32 v74,v74
v_mfma_f32_16x16x32_bf16 v[96:99],a[140:143],v[216:219],v[96:99]
v_exp_f32 v75,v75
v_mfma_f32_16x16x32_bf16 v[100:103],a[156:159],v[216:219],v[40:43]
v_exp_f32 v76,v76
v_mfma_f32_16x16x32_bf16 v[100:103],a[152:155],v[208:211],v[100:103]
v_exp_f32 v77,v77
v_mfma_f32_16x16x32_bf16 v[100:103],a[148:151],v[200:203],v[100:103]
v_exp_f32 v78,v78
v_mfma_f32_16x16x32_bf16 v[100:103],a[144:147],v[192:195],v[100:103]
v_exp_f32 v79,v79
v_mfma_f32_16x16x32_bf16 v[104:107],a[160:163],v[192:195],v[40:43]
v_cvt_pk_bf16_f32 v72,v72,v73
v_cvt_pk_bf16_f32 v73,v74,v75
v_mfma_f32_16x16x32_bf16 v[104:107],a[164:167],v[200:203],v[104:107]
v_cvt_pk_bf16_f32 v74,v76,v77
v_cvt_pk_bf16_f32 v75,v78,v79
v_mfma_f32_16x16x32_bf16 v[104:107],a[168:171],v[208:211],v[104:107]
ds_read_b64_tr_b16 a[224:225],v2 offset:512
ds_read_b64_tr_b16 a[226:227],v2 offset:4608
v_mfma_f32_16x16x32_bf16 v[104:107],a[172:175],v[216:219],v[104:107]
v_accvgpr_read_b32 v9,a97
v_accvgpr_read_b32 v10,a98
v_mfma_f32_16x16x32_bf16 v[108:111],a[188:191],v[216:219],v[40:43]
v_accvgpr_read_b32 v11,a99
v_mul_f32 v8,v8,v27
v_mfma_f32_16x16x32_bf16 v[108:111],a[184:187],v[208:211],v[108:111]
global_load_lds_dwordx4 v5,s[10:11] offset:3072
v_mul_f32 v9,v9,v27
v_mfma_f32_16x16x32_bf16 v[108:111],a[180:183],v[200:203],v[108:111]
v_mul_f32 v10,v10,v27
v_mul_f32 v11,v11,v27
v_mfma_f32_16x16x32_bf16 v[108:111],a[176:179],v[192:195],v[108:111]
s_waitcnt lgkmcnt(2)
v_mul_f32 v12,v12,v27
v_mul_f32 v13,v13,v27
v_mul_f32 v14,v14,v27
v_mul_f32 v15,v15,v27
v_cvt_pk_bf16_f32 v8,v8,v9
v_cvt_pk_bf16_f32 v9,v10,v11
v_cvt_pk_bf16_f32 v10,v12,v13
v_cvt_pk_bf16_f32 v11,v14,v15
v_accvgpr_write_b32 a96,v8
v_accvgpr_write_b32 a97,v9
v_accvgpr_write_b32 a98,v10
v_accvgpr_write_b32 a99,v11
v_mfma_f32_16x16x32_bf16 a[0:3],a[192:195],v[64:67],a[0:3]
ds_read_b128 a[100:103],v7 offset:2048
v_add_u32 v1,s78,v1
v_mfma_f32_16x16x32_bf16 a[0:3],a[196:199],v[72:75],a[0:3]
ds_read_b64_tr_b16 a[228:229],v2 offset:8704
v_accvgpr_read_b32 v20,a108
v_mfma_f32_16x16x32_bf16 a[4:7],a[200:203],v[64:67],a[4:7]
ds_read_b64_tr_b16 a[230:231],v2 offset:12800
v_accvgpr_read_b32 v21,a109
v_mfma_f32_16x16x32_bf16 a[4:7],a[204:207],v[72:75],a[4:7]
ds_read_b64_tr_b16 a[232:233],v3 offset:512
v_accvgpr_read_b32 v22,a110
v_mfma_f32_16x16x32_bf16 a[8:11],a[208:211],v[64:67],a[8:11]
ds_read_b64_tr_b16 a[234:235],v3 offset:4608
ds_read_b64_tr_b16 a[236:237],v3 offset:8704
v_mfma_f32_16x16x32_bf16 a[8:11],a[212:215],v[72:75],a[8:11]
ds_read_b64_tr_b16 a[238:239],v3 offset:12800
v_accvgpr_read_b32 v23,a111
v_mfma_f32_16x16x32_bf16 a[12:15],a[216:219],v[64:67],a[12:15]
ds_read_b64_tr_b16 a[240:241],v2 offset:768
v_accvgpr_read_b32 v16,a104
v_mfma_f32_16x16x32_bf16 a[12:15],a[220:223],v[72:75],a[12:15]
s_waitcnt lgkmcnt(8)
ds_read_b64_tr_b16 a[242:243],v2 offset:4864
v_accvgpr_read_b32 v17,a105
v_mfma_f32_16x16x32_bf16 a[16:19],a[224:227],v[64:67],a[16:19]
s_waitcnt lgkmcnt(6)
ds_read_b64_tr_b16 a[244:245],v2 offset:8960
ds_read_b64_tr_b16 a[246:247],v2 offset:13056
v_mfma_f32_16x16x32_bf16 a[16:19],a[228:231],v[72:75],a[16:19]
s_waitcnt lgkmcnt(0)
ds_read_b64_tr_b16 a[248:249],v3 offset:768
v_accvgpr_read_b32 v18,a106
v_mfma_f32_16x16x32_bf16 a[20:23],a[232:235],v[64:67],a[20:23]
s_getreg_b32 s92,hwreg(HW_REG_IB_STS,0,4)
s_min_u32 s4,s92,s6
ds_read_b64_tr_b16 a[250:251],v3 offset:4864
v_mfma_f32_16x16x32_bf16 a[20:23],a[236:239],v[72:75],a[20:23]
s_mov_b32 s6,s92
ds_read_b64_tr_b16 a[252:253],v3 offset:8960
ds_read_b64_tr_b16 a[254:255],v3 offset:13056
v_mfma_f32_16x16x32_bf16 a[24:27],a[240:243],v[64:67],a[24:27]
s_cmp_gt_u32 s4,s9
s_cselect_b32 s4,15,s5
v_accvgpr_read_b32 v19,a107
v_mfma_f32_16x16x32_bf16 a[24:27],a[244:247],v[72:75],a[24:27]
s_waitcnt lgkmcnt(2)
v_exp_f32 v80,v80
v_mfma_f32_16x16x32_bf16 a[28:31],a[248:251],v[64:67],a[28:31]
s_waitcnt lgkmcnt(0)
v_exp_f32 v81,v81
v_mfma_f32_16x16x32_bf16 a[28:31],a[252:255],v[72:75],a[28:31]
s_getpc_b64 s[92:93]
s_cmp_lg_u32 s4,0
s_cbranch_scc1 L_pace_%=
v_mfma_f32_16x16x32_bf16 v[48:51],v[28:31],v[64:67],v[48:51]
v_exp_f32 v82,v82
v_mfma_f32_16x16x32_bf16 v[48:51],v[28:31],v[72:75],v[48:51]
s_add_u32 s10,s20,s74
s_addc_u32 s11,s21,0
v_cvt_pk_bf16_f32 v80,v80,v81
s_waitcnt vmcnt(16)
s_barrier
v_mfma_f32_16x16x32_bf16 v[112:115],a[128:131],v[224:227],v[44:47]
s_add_u32 m0,s76,0
v_exp_f32 v83,v83
v_mfma_f32_16x16x32_bf16 v[112:115],a[132:135],v[232:235],v[112:115]
v_exp_f32 v84,v84
v_mfma_f32_16x16x32_bf16 v[112:115],a[136:139],v[240:243],v[112:115]
global_load_lds_dwordx4 v4,s[10:11] nt
v_cvt_pk_bf16_f32 v81,v82,v83
v_mfma_f32_16x16x32_bf16 v[112:115],a[140:143],v[248:251],v[112:115]
v_exp_f32 v85,v85
v_mfma_f32_16x16x32_bf16 v[116:119],a[156:159],v[248:251],v[44:47]
v_exp_f32 v86,v86
v_mfma_f32_16x16x32_bf16 v[116:119],a[152:155],v[240:243],v[116:119]
v_exp_f32 v87,v87
v_mfma_f32_16x16x32_bf16 v[116:119],a[148:151],v[232:235],v[116:119]
v_cvt_pk_bf16_f32 v82,v84,v85
v_cvt_pk_bf16_f32 v83,v86,v87
v_mfma_f32_16x16x32_bf16 v[116:119],a[144:147],v[224:227],v[116:119]
v_exp_f32 v88,v88
v_mfma_f32_16x16x32_bf16 v[120:123],a[160:163],v[224:227],v[44:47]
v_exp_f32 v89,v89
v_mfma_f32_16x16x32_bf16 v[120:123],a[164:167],v[232:235],v[120:123]
v_exp_f32 v90,v90
v_mfma_f32_16x16x32_bf16 v[120:123],a[168:171],v[240:243],v[120:123]
v_exp_f32 v91,v91
v_mfma_f32_16x16x32_bf16 v[120:123],a[172:175],v[248:251],v[120:123]
v_exp_f32 v92,v92
v_mfma_f32_16x16x32_bf16 v[124:127],a[188:191],v[248:251],v[44:47]
v_exp_f32 v93,v93
v_mfma_f32_16x16x32_bf16 v[124:127],a[184:187],v[240:243],v[124:127]
v_exp_f32 v94,v94
v_exp_f32 v95,v95
v_mfma_f32_16x16x32_bf16 v[124:127],a[180:183],v[232:235],v[124:127]
v_cvt_pk_bf16_f32 v88,v88,v89
v_cvt_pk_bf16_f32 v89,v90,v91
v_cvt_pk_bf16_f32 v90,v92,v93
v_cvt_pk_bf16_f32 v91,v94,v95
v_mfma_f32_16x16x32_bf16 v[124:127],a[176:179],v[224:227],v[124:127]
ds_read_b128 a[128:131],v1
v_mul_f32 v16,v16,v27
v_mfma_f32_16x16x32_bf16 v[132:135],a[192:195],v[80:83],v[132:135]
v_mul_f32 v17,v17,v27
v_mul_f32 v18,v18,v27
v_mfma_f32_16x16x32_bf16 v[132:135],a[196:199],v[88:91],v[132:135]
ds_read_b128 a[132:135],v1 offset:256
v_mul_f32 v19,v19,v27
v_mfma_f32_16x16x32_bf16 v[140:143],a[200:203],v[80:83],v[140:143]
global_load_lds_dwordx4 v4,s[10:11] offset:1024 nt
v_mul_f32 v20,v20,v27
v_mfma_f32_16x16x32_bf16 v[140:143],a[204:207],v[88:91],v[140:143]
ds_read_b128 a[136:139],v1 offset:512
v_mul_f32 v21,v21,v27
v_mfma_f32_16x16x32_bf16 v[148:151],a[208:211],v[80:83],v[148:151]
v_mul_f32 v22,v22,v27
v_mul_f32 v23,v23,v27
v_mfma_f32_16x16x32_bf16 v[148:151],a[212:215],v[88:91],v[148:151]
ds_read_b128 a[140:143],v1 offset:768
v_cvt_pk_bf16_f32 v16,v16,v17
v_mfma_f32_16x16x32_bf16 v[156:159],a[216:219],v[80:83],v[156:159]
v_cvt_pk_bf16_f32 v17,v18,v19
v_cvt_pk_bf16_f32 v18,v20,v21
v_mfma_f32_16x16x32_bf16 v[156:159],a[220:223],v[88:91],v[156:159]
ds_read_b128 a[156:159],v1 offset:4864
v_cvt_pk_bf16_f32 v19,v22,v23
v_mfma_f32_16x16x32_bf16 v[164:167],a[224:227],v[80:83],v[164:167]
v_accvgpr_write_b32 a104,v16
v_accvgpr_write_b32 a105,v17
v_mfma_f32_16x16x32_bf16 v[164:167],a[228:231],v[88:91],v[164:167]
ds_read_b128 a[152:155],v1 offset:4608
v_accvgpr_write_b32 a106,v18
v_mfma_f32_16x16x32_bf16 v[172:175],a[232:235],v[80:83],v[172:175]
v_accvgpr_write_b32 a107,v19
v_accvgpr_read_b32 v12,a68
v_mfma_f32_16x16x32_bf16 v[172:175],a[236:239],v[88:91],v[172:175]
ds_read_b128 a[148:151],v1 offset:4352
v_accvgpr_read_b32 v13,a69
v_mfma_f32_16x16x32_bf16 v[180:183],a[240:243],v[80:83],v[180:183]
v_exp_f32 v96,v96
v_mfma_f32_16x16x32_bf16 v[180:183],a[244:247],v[88:91],v[180:183]
ds_read_b128 a[144:147],v1 offset:4096
v_accvgpr_read_b32 v14,a70
L_pd9_%=:
s_cbranch_execz L_pd13_%=
v_mfma_f32_16x16x32_bf16 v[188:191],a[248:251],v[80:83],v[188:191]
v_exp_f32 v97,v97
v_mfma_f32_16x16x32_bf16 v[188:191],a[252:255],v[88:91],v[188:191]
ds_read_b128 a[160:163],v1 offset:8192
v_cvt_pk_bf16_f32 v96,v96,v97
v_mfma_f32_16x16x32_bf16 v[52:55],v[28:31],v[80:83],v[52:55]
v_exp_f32 v98,v98
v_mfma_f32_16x16x32_bf16 v[52:55],v[28:31],v[88:91],v[52:55]
s_waitcnt lgkmcnt(3)
ds_read_b128 a[164:167],v1 offset:8448
v_accvgpr_read_b32 v15,a71
v_mfma_f32_16x16x32_bf16 v[64:67],a[128:131],v[128:131],v[32:35]
global_load_lds_dwordx4 v5,s[10:11] offset:2048 nt
v_accvgpr_read_b32 v8,a64
v_mfma_f32_16x16x32_bf16 v[64:67],a[132:135],v[136:139],v[64:67]
ds_read_b128 a[168:171],v1 offset:8704
v_accvgpr_read_b32 v9,a65
v_mfma_f32_16x16x32_bf16 v[64:67],a[136:139],v[144:147],v[64:67]
v_exp_f32 v99,v99
v_mfma_f32_16x16x32_bf16 v[64:67],a[140:143],v[152:155],v[64:67]
ds_read_b128 a[108:111],v7 offset:3072
v_cvt_pk_bf16_f32 v97,v98,v99
v_mfma_f32_16x16x32_bf16 v[68:71],a[156:159],v[152:155],v[32:35]
v_exp_f32 v100,v100
v_mfma_f32_16x16x32_bf16 v[68:71],a[152:155],v[144:147],v[68:71]
s_waitcnt lgkmcnt(3)
ds_read_b128 a[172:175],v1 offset:8960
v_accvgpr_read_b32 v10,a66
v_mfma_f32_16x16x32_bf16 v[68:71],a[148:151],v[136:139],v[68:71]
v_exp_f32 v101,v101
v_mfma_f32_16x16x32_bf16 v[68:71],a[144:147],v[128:131],v[68:71]
ds_read_b128 a[188:191],v1 offset:13056
v_cvt_pk_bf16_f32 v98,v100,v101
v_mfma_f32_16x16x32_bf16 v[72:75],a[160:163],v[128:131],v[32:35]
s_waitcnt lgkmcnt(4)
v_exp_f32 v102,v102
v_mfma_f32_16x16x32_bf16 v[72:75],a[164:167],v[136:139],v[72:75]
s_waitcnt lgkmcnt(3)
ds_read_b128 a[184:187],v1 offset:12800
v_accvgpr_read_b32 v11,a67
v_mfma_f32_16x16x32_bf16 v[72:75],a[168:171],v[144:147],v[72:75]
s_waitcnt lgkmcnt(2)
ds_read_b128 a[180:183],v1 offset:12544
v_mul_f32 v8,v8,v26
v_mfma_f32_16x16x32_bf16 v[72:75],a[172:175],v[152:155],v[72:75]
s_waitcnt lgkmcnt(2)
ds_read_b128 a[176:179],v1 offset:12288
v_mul_f32 v9,v9,v26
v_mfma_f32_16x16x32_bf16 v[76:79],a[188:191],v[152:155],v[32:35]
s_waitcnt lgkmcnt(2)
v_exp_f32 v103,v103
v_mfma_f32_16x16x32_bf16 v[76:79],a[184:187],v[144:147],v[76:79]
s_waitcnt lgkmcnt(1)
v_cvt_pk_bf16_f32 v99,v102,v103
v_exp_f32 v104,v104
v_exp_f32 v105,v105
v_exp_f32 v106,v106
v_exp_f32 v107,v107
v_exp_f32 v108,v108
v_exp_f32 v109,v109
v_exp_f32 v110,v110
v_exp_f32 v111,v111
v_mfma_f32_16x16x32_bf16 v[76:79],a[180:183],v[136:139],v[76:79]
s_waitcnt lgkmcnt(0)
v_cvt_pk_bf16_f32 v104,v104,v105
v_cvt_pk_bf16_f32 v105,v106,v107
v_cvt_pk_bf16_f32 v106,v108,v109
v_cvt_pk_bf16_f32 v107,v110,v111
v_mfma_f32_16x16x32_bf16 v[76:79],a[176:179],v[128:131],v[76:79]
v_exp_f32 v112,v112
v_mfma_f32_16x16x32_bf16 v[196:199],a[192:195],v[96:99],v[196:199]
global_load_lds_dwordx4 v5,s[10:11] offset:3072
v_mul_f32 v10,v10,v26
v_mfma_f32_16x16x32_bf16 v[196:199],a[196:199],v[104:107],v[196:199]
v_exp_f32 v113,v113
v_mfma_f32_16x16x32_bf16 v[204:207],a[200:203],v[96:99],v[204:207]
v_exp_f32 v114,v114
v_mfma_f32_16x16x32_bf16 v[204:207],a[204:207],v[104:107],v[204:207]
v_exp_f32 v115,v115
v_mfma_f32_16x16x32_bf16 v[212:215],a[208:211],v[96:99],v[212:215]
v_exp_f32 v116,v116
v_mfma_f32_16x16x32_bf16 v[212:215],a[212:215],v[104:107],v[212:215]
v_exp_f32 v117,v117
v_mfma_f32_16x16x32_bf16 v[220:223],a[216:219],v[96:99],v[220:223]
v_exp_f32 v118,v118
v_mfma_f32_16x16x32_bf16 v[220:223],a[220:223],v[104:107],v[220:223]
v_exp_f32 v119,v119
)A"
R"A(v_mfma_f32_16x16x32_bf16 v[228:231],a[224:227],v[96:99],v[228:231]
v_cvt_pk_bf16_f32 v112,v112,v113
v_cvt_pk_bf16_f32 v113,v114,v115
v_mfma_f32_16x16x32_bf16 v[228:231],a[228:231],v[104:107],v[228:231]
v_cvt_pk_bf16_f32 v114,v116,v117
v_cvt_pk_bf16_f32 v115,v118,v119
v_mfma_f32_16x16x32_bf16 v[236:239],a[232:235],v[96:99],v[236:239]
v_exp_f32 v120,v120
v_mfma_f32_16x16x32_bf16 v[236:239],a[236:239],v[104:107],v[236:239]
v_exp_f32 v121,v121
v_mfma_f32_16x16x32_bf16 v[244:247],a[240:243],v[96:99],v[244:247]
s_add_u32 s10,s24,s74
s_addc_u32 s11,s25,0
v_cvt_pk_bf16_f32 v120,v120,v121
v_mfma_f32_16x16x32_bf16 v[244:247],a[244:247],v[104:107],v[244:247]
s_add_u32 s74,s74,16384
v_exp_f32 v122,v122
v_mfma_f32_16x16x32_bf16 v[252:255],a[248:251],v[96:99],v[252:255]
s_add_u32 m0,s76,16384
v_exp_f32 v123,v123
v_mfma_f32_16x16x32_bf16 v[252:255],a[252:255],v[104:107],v[252:255]
s_and_b32 s74,s74,0x3ffff
v_exp_f32 v124,v124
v_mfma_f32_16x16x32_bf16 v[56:59],v[28:31],v[96:99],v[56:59]
global_load_lds_dwordx4 v4,s[10:11]
v_cvt_pk_bf16_f32 v121,v122,v123
v_mfma_f32_16x16x32_bf16 v[56:59],v[28:31],v[104:107],v[56:59]
s_or_b32 s74,s74,s69
v_exp_f32 v125,v125
v_mfma_f32_16x16x32_bf16 v[80:83],a[128:131],v[160:163],v[36:39]
s_sub_u32 s76,s76,32768
v_exp_f32 v126,v126
v_mfma_f32_16x16x32_bf16 v[80:83],a[132:135],v[168:171],v[80:83]
s_and_b32 s76,s76,0x1ffff
v_exp_f32 v127,v127
v_mfma_f32_16x16x32_bf16 v[80:83],a[136:139],v[176:179],v[80:83]
s_and_b32 s69,s75,0xfffc0000
v_cvt_pk_bf16_f32 v122,v124,v125
v_cvt_pk_bf16_f32 v123,v126,v127
v_mfma_f32_16x16x32_bf16 v[80:83],a[140:143],v[184:187],v[80:83]
v_mul_f32 v11,v11,v26
v_mul_f32 v12,v12,v26
v_mfma_f32_16x16x32_bf16 v[84:87],a[156:159],v[184:187],v[36:39]
v_mul_f32 v13,v13,v26
v_mul_f32 v14,v14,v26
v_mfma_f32_16x16x32_bf16 v[84:87],a[152:155],v[176:179],v[84:87]
v_mul_f32 v15,v15,v26
v_cvt_pk_bf16_f32 v8,v8,v9
v_mfma_f32_16x16x32_bf16 v[84:87],a[148:151],v[168:171],v[84:87]
v_cvt_pk_bf16_f32 v9,v10,v11
v_cvt_pk_bf16_f32 v10,v12,v13
v_mfma_f32_16x16x32_bf16 v[84:87],a[144:147],v[160:163],v[84:87]
v_cvt_pk_bf16_f32 v11,v14,v15
v_accvgpr_write_b32 a64,v8
v_mfma_f32_16x16x32_bf16 v[88:91],a[160:163],v[160:163],v[36:39]
v_accvgpr_write_b32 a65,v9
v_accvgpr_write_b32 a66,v10
v_mfma_f32_16x16x32_bf16 v[88:91],a[164:167],v[168:171],v[88:91]
ds_read_b128 a[68:71],v7 offset:6144
v_accvgpr_write_b32 a67,v11
v_mfma_f32_16x16x32_bf16 v[88:91],a[168:171],v[176:179],v[88:91]
v_add_u32 v2,s78,v2
v_add_u32 v3,s78,v3
v_mfma_f32_16x16x32_bf16 v[88:91],a[172:175],v[184:187],v[88:91]
s_sub_u32 s78,s77,32768
v_exp_f32 v64,v64
v_mfma_f32_16x16x32_bf16 v[92:95],a[188:191],v[184:187],v[36:39]
s_and_b32 s78,s78,0x1ffff
v_exp_f32 v65,v65
v_mfma_f32_16x16x32_bf16 v[92:95],a[184:187],v[176:179],v[92:95]
s_sub_u32 s78,s78,s77
v_exp_f32 v66,v66
v_mfma_f32_16x16x32_bf16 v[92:95],a[180:183],v[168:171],v[92:95]
global_load_lds_dwordx4 v5,s[10:11] offset:1024
v_cvt_pk_bf16_f32 v64,v64,v65
v_mfma_f32_16x16x32_bf16 v[92:95],a[176:179],v[160:163],v[92:95]
s_add_u32 s77,s77,s78
v_exp_f32 v67,v67
v_mfma_f32_16x16x32_bf16 a[32:35],a[192:195],v[112:115],a[32:35]
ds_read_b64_tr_b16 a[192:193],v2
ds_read_b64_tr_b16 a[194:195],v2 offset:4096
v_mfma_f32_16x16x32_bf16 a[32:35],a[196:199],v[120:123],a[32:35]
ds_read_b64_tr_b16 a[196:197],v2 offset:8192
v_cvt_pk_bf16_f32 v65,v66,v67
v_mfma_f32_16x16x32_bf16 a[36:39],a[200:203],v[112:115],a[36:39]
ds_read_b64_tr_b16 a[198:199],v2 offset:12288
v_accvgpr_read_b32 v20,a76
v_mfma_f32_16x16x32_bf16 a[36:39],a[204:207],v[120:123],a[36:39]
ds_read_b64_tr_b16 a[200:201],v3
v_accvgpr_read_b32 v21,a77
v_mfma_f32_16x16x32_bf16 a[40:43],a[208:211],v[112:115],a[40:43]
ds_read_b64_tr_b16 a[202:203],v3 offset:4096
ds_read_b64_tr_b16 a[204:205],v3 offset:8192
v_mfma_f32_16x16x32_bf16 a[40:43],a[212:215],v[120:123],a[40:43]
ds_read_b64_tr_b16 a[206:207],v3 offset:12288
v_accvgpr_read_b32 v22,a78
v_mfma_f32_16x16x32_bf16 a[44:47],a[216:219],v[112:115],a[44:47]
ds_read_b64_tr_b16 a[208:209],v2 offset:256
v_accvgpr_read_b32 v23,a79
v_mfma_f32_16x16x32_bf16 a[44:47],a[220:223],v[120:123],a[44:47]
ds_read_b64_tr_b16 a[210:211],v2 offset:4352
v_accvgpr_read_b32 v16,a72
v_mfma_f32_16x16x32_bf16 a[48:51],a[224:227],v[112:115],a[48:51]
ds_read_b64_tr_b16 a[212:213],v2 offset:8448
ds_read_b64_tr_b16 a[214:215],v2 offset:12544
v_mfma_f32_16x16x32_bf16 a[48:51],a[228:231],v[120:123],a[48:51]
v_exp_f32 v68,v68
v_mfma_f32_16x16x32_bf16 a[52:55],a[232:235],v[112:115],a[52:55]
v_exp_f32 v69,v69
v_mfma_f32_16x16x32_bf16 a[52:55],a[236:239],v[120:123],a[52:55]
v_exp_f32 v70,v70
v_mfma_f32_16x16x32_bf16 a[56:59],a[240:243],v[112:115],a[56:59]
v_exp_f32 v71,v71
v_mfma_f32_16x16x32_bf16 a[56:59],a[244:247],v[120:123],a[56:59]
ds_read_b64_tr_b16 a[216:217],v3 offset:256
ds_read_b64_tr_b16 a[218:219],v3 offset:4352
v_mfma_f32_16x16x32_bf16 a[60:63],a[248:251],v[112:115],a[60:63]
global_load_lds_dwordx4 v4,s[10:11] offset:2048
v_cvt_pk_bf16_f32 v66,v68,v69
v_mfma_f32_16x16x32_bf16 a[60:63],a[252:255],v[120:123],a[60:63]
ds_read_b64_tr_b16 a[220:221],v3 offset:8448
v_cvt_pk_bf16_f32 v67,v70,v71
v_mfma_f32_16x16x32_bf16 v[60:63],v[28:31],v[112:115],v[60:63]
v_exp_f32 v72,v72
v_mfma_f32_16x16x32_bf16 v[60:63],v[28:31],v[120:123],v[60:63]
v_exp_f32 v73,v73
v_mfma_f32_16x16x32_bf16 v[96:99],a[128:131],v[192:195],v[40:43]
v_exp_f32 v74,v74
v_mfma_f32_16x16x32_bf16 v[96:99],a[132:135],v[200:203],v[96:99]
v_exp_f32 v75,v75
v_mfma_f32_16x16x32_bf16 v[96:99],a[136:139],v[208:211],v[96:99]
v_exp_f32 v76,v76
v_mfma_f32_16x16x32_bf16 v[96:99],a[140:143],v[216:219],v[96:99]
v_exp_f32 v77,v77
v_mfma_f32_16x16x32_bf16 v[100:103],a[156:159],v[216:219],v[40:43]
v_exp_f32 v78,v78
v_mfma_f32_16x16x32_bf16 v[100:103],a[152:155],v[208:211],v[100:103]
v_exp_f32 v79,v79
v_mfma_f32_16x16x32_bf16 v[100:103],a[148:151],v[200:203],v[100:103]
ds_read_b64_tr_b16 a[222:223],v3 offset:12544
v_cvt_pk_bf16_f32 v72,v72,v73
v_mfma_f32_16x16x32_bf16 v[100:103],a[144:147],v[192:195],v[100:103]
v_cvt_pk_bf16_f32 v73,v74,v75
v_cvt_pk_bf16_f32 v74,v76,v77
v_mfma_f32_16x16x32_bf16 v[104:107],a[160:163],v[192:195],v[40:43]
ds_read_b64_tr_b16 a[224:225],v2 offset:512
v_cvt_pk_bf16_f32 v75,v78,v79
v_mfma_f32_16x16x32_bf16 v[104:107],a[164:167],v[200:203],v[104:107]
ds_read_b64_tr_b16 a[226:227],v2 offset:4608
ds_read_b64_tr_b16 a[228:229],v2 offset:8704
v_mfma_f32_16x16x32_bf16 v[104:107],a[168:171],v[208:211],v[104:107]
ds_read_b64_tr_b16 a[230:231],v2 offset:12800
v_accvgpr_read_b32 v17,a73
v_mfma_f32_16x16x32_bf16 v[104:107],a[172:175],v[216:219],v[104:107]
ds_read_b64_tr_b16 a[232:233],v3 offset:512
v_accvgpr_read_b32 v18,a74
v_mfma_f32_16x16x32_bf16 v[108:111],a[188:191],v[216:219],v[40:43]
global_load_lds_dwordx4 v5,s[10:11] offset:3072
ds_read_b64_tr_b16 a[234:235],v3 offset:4608
v_mfma_f32_16x16x32_bf16 v[108:111],a[184:187],v[208:211],v[108:111]
ds_read_b64_tr_b16 a[236:237],v3 offset:8704
ds_read_b64_tr_b16 a[238:239],v3 offset:12800
v_mfma_f32_16x16x32_bf16 v[108:111],a[180:183],v[200:203],v[108:111]
ds_read_b64_tr_b16 a[240:241],v2 offset:768
v_accvgpr_read_b32 v19,a75
v_mfma_f32_16x16x32_bf16 v[108:111],a[176:179],v[192:195],v[108:111]
s_waitcnt lgkmcnt(9)
ds_read_b64_tr_b16 a[242:243],v2 offset:4864
v_mul_f32 v16,v16,v26
v_mfma_f32_16x16x32_bf16 a[0:3],a[192:195],v[64:67],a[0:3]
ds_read_b64_tr_b16 a[244:245],v2 offset:8960
v_mul_f32 v17,v17,v26
v_mfma_f32_16x16x32_bf16 a[0:3],a[196:199],v[72:75],a[0:3]
ds_read_b64_tr_b16 a[246:247],v2 offset:13056
ds_read_b64_tr_b16 a[248:249],v3 offset:768
v_mfma_f32_16x16x32_bf16 a[4:7],a[200:203],v[64:67],a[4:7]
ds_read_b64_tr_b16 a[250:251],v3 offset:4864
v_mul_f32 v18,v18,v26
v_mfma_f32_16x16x32_bf16 a[4:7],a[204:207],v[72:75],a[4:7]
ds_read_b64_tr_b16 a[252:253],v3 offset:8960
v_mul_f32 v19,v19,v26
v_mfma_f32_16x16x32_bf16 a[8:11],a[208:211],v[64:67],a[8:11]
ds_read_b64_tr_b16 a[254:255],v3 offset:13056
v_mul_f32 v20,v20,v26
v_mfma_f32_16x16x32_bf16 a[8:11],a[212:215],v[72:75],a[8:11]
v_exp_f32 v80,v80
v_mfma_f32_16x16x32_bf16 a[12:15],a[216:219],v[64:67],a[12:15]
v_exp_f32 v81,v81
v_mfma_f32_16x16x32_bf16 a[12:15],a[220:223],v[72:75],a[12:15]
s_waitcnt lgkmcnt(6)
v_exp_f32 v82,v82
v_mfma_f32_16x16x32_bf16 a[16:19],a[224:227],v[64:67],a[16:19]
v_exp_f32 v83,v83
v_mfma_f32_16x16x32_bf16 a[16:19],a[228:231],v[72:75],a[16:19]
v_exp_f32 v84,v84
v_mfma_f32_16x16x32_bf16 a[20:23],a[232:235],v[64:67],a[20:23]
s_getreg_b32 s92,hwreg(HW_REG_IB_STS,0,4)
s_min_u32 s4,s92,s6
s_mov_b32 s6,s92
v_mfma_f32_16x16x32_bf16 a[20:23],a[236:239],v[72:75],a[20:23]
s_cmp_ge_u32 s4,s9
s_cselect_b32 s4,15,s5
v_cvt_pk_bf16_f32 v80,v80,v81
v_mfma_f32_16x16x32_bf16 a[24:27],a[240:243],v[64:67],a[24:27]
s_waitcnt lgkmcnt(0)
v_exp_f32 v85,v85
v_mfma_f32_16x16x32_bf16 a[24:27],a[244:247],v[72:75],a[24:27]
v_exp_f32 v86,v86
v_mfma_f32_16x16x32_bf16 a[28:31],a[248:251],v[64:67],a[28:31]
v_exp_f32 v87,v87
v_mfma_f32_16x16x32_bf16 a[28:31],a[252:255],v[72:75],a[28:31]
s_getpc_b64 s[92:93]
s_cmp_lg_u32 s4,0
s_cbranch_scc1 L_pace_%=
v_mfma_f32_16x16x32_bf16 v[48:51],v[28:31],v[64:67],v[48:51]
v_cvt_pk_bf16_f32 v81,v82,v83
v_cvt_pk_bf16_f32 v82,v84,v85
v_mfma_f32_16x16x32_bf16 v[48:51],v[28:31],v[72:75],v[48:51]
s_add_u32 s10,s20,s74
s_addc_u32 s11,s21,0
v_cvt_pk_bf16_f32 v83,v86,v87
s_waitcnt vmcnt(16)
s_barrier
v_mfma_f32_16x16x32_bf16 v[112:115],a[128:131],v[224:227],v[44:47]
s_add_u32 m0,s76,0
v_exp_f32 v88,v88
v_mfma_f32_16x16x32_bf16 v[112:115],a[132:135],v[232:235],v[112:115]
v_exp_f32 v89,v89
v_mfma_f32_16x16x32_bf16 v[112:115],a[136:139],v[240:243],v[112:115]
global_load_lds_dwordx4 v4,s[10:11] nt
v_cvt_pk_bf16_f32 v88,v88,v89
v_mfma_f32_16x16x32_bf16 v[112:115],a[140:143],v[248:251],v[112:115]
v_exp_f32 v90,v90
v_mfma_f32_16x16x32_bf16 v[116:119],a[156:159],v[248:251],v[44:47]
v_exp_f32 v91,v91
v_mfma_f32_16x16x32_bf16 v[116:119],a[152:155],v[240:243],v[116:119]
v_exp_f32 v92,v92
v_mfma_f32_16x16x32_bf16 v[116:119],a[148:151],v[232:235],v[116:119]
v_exp_f32 v93,v93
v_mfma_f32_16x16x32_bf16 v[116:119],a[144:147],v[224:227],v[116:119]
v_exp_f32 v94,v94
v_mfma_f32_16x16x32_bf16 v[120:123],a[160:163],v[224:227],v[44:47]
v_exp_f32 v95,v95
v_mfma_f32_16x16x32_bf16 v[120:123],a[164:167],v[232:235],v[120:123]
v_cvt_pk_bf16_f32 v89,v90,v91
v_cvt_pk_bf16_f32 v90,v92,v93
v_mfma_f32_16x16x32_bf16 v[120:123],a[168:171],v[240:243],v[120:123]
v_cvt_pk_bf16_f32 v91,v94,v95
v_mul_f32 v21,v21,v26
v_mfma_f32_16x16x32_bf16 v[120:123],a[172:175],v[248:251],v[120:123]
v_mul_f32 v22,v22,v26
v_mul_f32 v23,v23,v26
v_mfma_f32_16x16x32_bf16 v[124:127],a[188:191],v[248:251],v[44:47]
v_cvt_pk_bf16_f32 v16,v16,v17
v_cvt_pk_bf16_f32 v17,v18,v19
v_mfma_f32_16x16x32_bf16 v[124:127],a[184:187],v[240:243],v[124:127]
v_cvt_pk_bf16_f32 v18,v20,v21
v_cvt_pk_bf16_f32 v19,v22,v23
v_mfma_f32_16x16x32_bf16 v[124:127],a[180:183],v[232:235],v[124:127]
v_accvgpr_write_b32 a72,v16
v_accvgpr_write_b32 a73,v17
v_mfma_f32_16x16x32_bf16 v[124:127],a[176:179],v[224:227],v[124:127]
v_accvgpr_write_b32 a74,v18
v_accvgpr_write_b32 a75,v19
v_mfma_f32_16x16x32_bf16 v[132:135],a[192:195],v[80:83],v[132:135]
ds_read_b128 a[76:79],v7 offset:7168
v_add_u32 v1,s78,v1
v_mfma_f32_16x16x32_bf16 v[132:135],a[196:199],v[88:91],v[132:135]
v_exp_f32 v96,v96
v_mfma_f32_16x16x32_bf16 v[140:143],a[200:203],v[80:83],v[140:143]
global_load_lds_dwordx4 v4,s[10:11] offset:1024 nt
ds_read_b128 a[128:131],v1
v_mfma_f32_16x16x32_bf16 v[140:143],a[204:207],v[88:91],v[140:143]
v_exp_f32 v97,v97
v_mfma_f32_16x16x32_bf16 v[148:151],a[208:211],v[80:83],v[148:151]
ds_read_b128 a[132:135],v1 offset:256
v_cvt_pk_bf16_f32 v96,v96,v97
v_mfma_f32_16x16x32_bf16 v[148:151],a[212:215],v[88:91],v[148:151]
v_exp_f32 v98,v98
v_mfma_f32_16x16x32_bf16 v[156:159],a[216:219],v[80:83],v[156:159]
ds_read_b128 a[136:139],v1 offset:512
v_accvgpr_read_b32 v12,a84
v_mfma_f32_16x16x32_bf16 v[156:159],a[220:223],v[88:91],v[156:159]
v_exp_f32 v99,v99
v_mfma_f32_16x16x32_bf16 v[164:167],a[224:227],v[80:83],v[164:167]
ds_read_b128 a[140:143],v1 offset:768
v_cvt_pk_bf16_f32 v97,v98,v99
v_mfma_f32_16x16x32_bf16 v[164:167],a[228:231],v[88:91],v[164:167]
)A"
R"A(v_exp_f32 v100,v100
v_mfma_f32_16x16x32_bf16 v[172:175],a[232:235],v[80:83],v[172:175]
ds_read_b128 a[156:159],v1 offset:4864
v_accvgpr_read_b32 v13,a85
v_mfma_f32_16x16x32_bf16 v[172:175],a[236:239],v[88:91],v[172:175]
v_exp_f32 v101,v101
v_mfma_f32_16x16x32_bf16 v[180:183],a[240:243],v[80:83],v[180:183]
ds_read_b128 a[152:155],v1 offset:4608
v_cvt_pk_bf16_f32 v98,v100,v101
v_mfma_f32_16x16x32_bf16 v[180:183],a[244:247],v[88:91],v[180:183]
v_exp_f32 v102,v102
v_mfma_f32_16x16x32_bf16 v[188:191],a[248:251],v[80:83],v[188:191]
ds_read_b128 a[148:151],v1 offset:4352
v_accvgpr_read_b32 v14,a86
v_mfma_f32_16x16x32_bf16 v[188:191],a[252:255],v[88:91],v[188:191]
v_exp_f32 v103,v103
v_mfma_f32_16x16x32_bf16 v[52:55],v[28:31],v[80:83],v[52:55]
ds_read_b128 a[144:147],v1 offset:4096
v_cvt_pk_bf16_f32 v99,v102,v103
v_mfma_f32_16x16x32_bf16 v[52:55],v[28:31],v[88:91],v[52:55]
s_waitcnt lgkmcnt(4)
v_exp_f32 v104,v104
v_mfma_f32_16x16x32_bf16 v[64:67],a[128:131],v[128:131],v[32:35]
global_load_lds_dwordx4 v5,s[10:11] offset:2048 nt
ds_read_b128 a[160:163],v1 offset:8192
v_mfma_f32_16x16x32_bf16 v[64:67],a[132:135],v[136:139],v[64:67]
v_exp_f32 v105,v105
v_mfma_f32_16x16x32_bf16 v[64:67],a[136:139],v[144:147],v[64:67]
ds_read_b128 a[164:167],v1 offset:8448
v_cvt_pk_bf16_f32 v104,v104,v105
v_mfma_f32_16x16x32_bf16 v[64:67],a[140:143],v[152:155],v[64:67]
s_waitcnt lgkmcnt(4)
v_exp_f32 v106,v106
v_mfma_f32_16x16x32_bf16 v[68:71],a[156:159],v[152:155],v[32:35]
ds_read_b128 a[168:171],v1 offset:8704
v_accvgpr_read_b32 v15,a87
v_mfma_f32_16x16x32_bf16 v[68:71],a[152:155],v[144:147],v[68:71]
s_waitcnt lgkmcnt(4)
v_exp_f32 v107,v107
v_mfma_f32_16x16x32_bf16 v[68:71],a[148:151],v[136:139],v[68:71]
s_waitcnt lgkmcnt(3)
ds_read_b128 a[172:175],v1 offset:8960
v_cvt_pk_bf16_f32 v105,v106,v107
v_mfma_f32_16x16x32_bf16 v[68:71],a[144:147],v[128:131],v[68:71]
s_waitcnt lgkmcnt(3)
v_exp_f32 v108,v108
v_mfma_f32_16x16x32_bf16 v[72:75],a[160:163],v[128:131],v[32:35]
s_waitcnt lgkmcnt(2)
ds_read_b128 a[188:191],v1 offset:13056
v_accvgpr_read_b32 v8,a80
v_mfma_f32_16x16x32_bf16 v[72:75],a[164:167],v[136:139],v[72:75]
s_waitcnt lgkmcnt(2)
ds_read_b128 a[184:187],v1 offset:12800
v_accvgpr_read_b32 v9,a81
v_mfma_f32_16x16x32_bf16 v[72:75],a[168:171],v[144:147],v[72:75]
s_waitcnt lgkmcnt(2)
ds_read_b128 a[180:183],v1 offset:12544
v_accvgpr_read_b32 v10,a82
v_mfma_f32_16x16x32_bf16 v[72:75],a[172:175],v[152:155],v[72:75]
s_waitcnt lgkmcnt(2)
ds_read_b128 a[176:179],v1 offset:12288
v_accvgpr_read_b32 v11,a83
v_mfma_f32_16x16x32_bf16 v[76:79],a[188:191],v[152:155],v[32:35]
s_waitcnt lgkmcnt(2)
v_exp_f32 v109,v109
v_mfma_f32_16x16x32_bf16 v[76:79],a[184:187],v[144:147],v[76:79]
s_waitcnt lgkmcnt(1)
v_exp_f32 v110,v110
v_exp_f32 v111,v111
v_mfma_f32_16x16x32_bf16 v[76:79],a[180:183],v[136:139],v[76:79]
s_waitcnt lgkmcnt(0)
v_cvt_pk_bf16_f32 v106,v108,v109
v_cvt_pk_bf16_f32 v107,v110,v111
v_mfma_f32_16x16x32_bf16 v[76:79],a[176:179],v[128:131],v[76:79]
v_mul_f32 v8,v8,v26
v_mul_f32 v9,v9,v26
v_mfma_f32_16x16x32_bf16 v[196:199],a[192:195],v[96:99],v[196:199]
global_load_lds_dwordx4 v5,s[10:11] offset:3072
v_mul_f32 v10,v10,v26
v_mfma_f32_16x16x32_bf16 v[196:199],a[196:199],v[104:107],v[196:199]
v_mul_f32 v11,v11,v26
v_mul_f32 v12,v12,v26
v_mfma_f32_16x16x32_bf16 v[204:207],a[200:203],v[96:99],v[204:207]
v_mul_f32 v13,v13,v26
v_mul_f32 v14,v14,v26
v_mfma_f32_16x16x32_bf16 v[204:207],a[204:207],v[104:107],v[204:207]
v_mul_f32 v15,v15,v26
v_cvt_pk_bf16_f32 v8,v8,v9
v_mfma_f32_16x16x32_bf16 v[212:215],a[208:211],v[96:99],v[212:215]
v_cvt_pk_bf16_f32 v9,v10,v11
v_cvt_pk_bf16_f32 v10,v12,v13
v_mfma_f32_16x16x32_bf16 v[212:215],a[212:215],v[104:107],v[212:215]
v_cvt_pk_bf16_f32 v11,v14,v15
v_accvgpr_write_b32 a80,v8
v_mfma_f32_16x16x32_bf16 v[220:223],a[216:219],v[96:99],v[220:223]
v_accvgpr_write_b32 a81,v9
v_accvgpr_write_b32 a82,v10
v_mfma_f32_16x16x32_bf16 v[220:223],a[220:223],v[104:107],v[220:223]
ds_read_b128 a[84:87],v7
v_accvgpr_write_b32 a83,v11
v_mfma_f32_16x16x32_bf16 v[228:231],a[224:227],v[96:99],v[228:231]
v_exp_f32 v112,v112
v_mfma_f32_16x16x32_bf16 v[228:231],a[228:231],v[104:107],v[228:231]
v_exp_f32 v113,v113
v_mfma_f32_16x16x32_bf16 v[236:239],a[232:235],v[96:99],v[236:239]
v_exp_f32 v114,v114
v_mfma_f32_16x16x32_bf16 v[236:239],a[236:239],v[104:107],v[236:239]
v_exp_f32 v115,v115
v_mfma_f32_16x16x32_bf16 v[244:247],a[240:243],v[96:99],v[244:247]
s_add_u32 s10,s24,s74
s_addc_u32 s11,s25,0
v_cvt_pk_bf16_f32 v112,v112,v113
v_mfma_f32_16x16x32_bf16 v[244:247],a[244:247],v[104:107],v[244:247]
s_add_u32 s74,s74,16384
v_exp_f32 v116,v116
v_mfma_f32_16x16x32_bf16 v[252:255],a[248:251],v[96:99],v[252:255]
s_add_u32 m0,s76,16384
v_exp_f32 v117,v117
v_mfma_f32_16x16x32_bf16 v[252:255],a[252:255],v[104:107],v[252:255]
s_and_b32 s74,s74,0x3ffff
v_exp_f32 v118,v118
v_mfma_f32_16x16x32_bf16 v[56:59],v[28:31],v[96:99],v[56:59]
global_load_lds_dwordx4 v4,s[10:11]
v_cvt_pk_bf16_f32 v113,v114,v115
v_mfma_f32_16x16x32_bf16 v[56:59],v[28:31],v[104:107],v[56:59]
s_or_b32 s74,s74,s69
v_exp_f32 v119,v119
v_mfma_f32_16x16x32_bf16 v[80:83],a[128:131],v[160:163],v[36:39]
s_sub_u32 s76,s76,32768
v_cvt_pk_bf16_f32 v114,v116,v117
v_cvt_pk_bf16_f32 v115,v118,v119
v_mfma_f32_16x16x32_bf16 v[80:83],a[132:135],v[168:171],v[80:83]
s_and_b32 s76,s76,0x1ffff
v_exp_f32 v120,v120
v_mfma_f32_16x16x32_bf16 v[80:83],a[136:139],v[176:179],v[80:83]
v_exp_f32 v121,v121
v_mfma_f32_16x16x32_bf16 v[80:83],a[140:143],v[184:187],v[80:83]
v_exp_f32 v122,v122
v_mfma_f32_16x16x32_bf16 v[84:87],a[156:159],v[184:187],v[36:39]
v_exp_f32 v123,v123
v_mfma_f32_16x16x32_bf16 v[84:87],a[152:155],v[176:179],v[84:87]
v_exp_f32 v124,v124
v_mfma_f32_16x16x32_bf16 v[84:87],a[148:151],v[168:171],v[84:87]
v_exp_f32 v125,v125
v_mfma_f32_16x16x32_bf16 v[84:87],a[144:147],v[160:163],v[84:87]
v_exp_f32 v126,v126
v_mfma_f32_16x16x32_bf16 v[88:91],a[160:163],v[160:163],v[36:39]
v_exp_f32 v127,v127
v_mfma_f32_16x16x32_bf16 v[88:91],a[164:167],v[168:171],v[88:91]
v_cvt_pk_bf16_f32 v120,v120,v121
v_cvt_pk_bf16_f32 v121,v122,v123
v_mfma_f32_16x16x32_bf16 v[88:91],a[168:171],v[176:179],v[88:91]
v_cvt_pk_bf16_f32 v122,v124,v125
v_cvt_pk_bf16_f32 v123,v126,v127
v_mfma_f32_16x16x32_bf16 v[88:91],a[172:175],v[184:187],v[88:91]
v_add_u32 v2,s78,v2
v_add_u32 v3,s78,v3
v_mfma_f32_16x16x32_bf16 v[92:95],a[188:191],v[184:187],v[36:39]
s_sub_u32 s78,s77,32768
v_exp_f32 v64,v64
v_mfma_f32_16x16x32_bf16 v[92:95],a[184:187],v[176:179],v[92:95]
s_and_b32 s78,s78,0x1ffff
v_exp_f32 v65,v65
v_mfma_f32_16x16x32_bf16 v[92:95],a[180:183],v[168:171],v[92:95]
global_load_lds_dwordx4 v5,s[10:11] offset:1024
v_cvt_pk_bf16_f32 v64,v64,v65
v_mfma_f32_16x16x32_bf16 v[92:95],a[176:179],v[160:163],v[92:95]
s_sub_u32 s78,s78,s77
v_exp_f32 v66,v66
v_mfma_f32_16x16x32_bf16 a[32:35],a[192:195],v[112:115],a[32:35]
s_add_u32 s77,s77,s78
ds_read_b64_tr_b16 a[192:193],v2
ds_read_b64_tr_b16 a[194:195],v2 offset:4096
v_mfma_f32_16x16x32_bf16 a[32:35],a[196:199],v[120:123],a[32:35]
ds_read_b64_tr_b16 a[196:197],v2 offset:8192
v_accvgpr_read_b32 v20,a92
v_mfma_f32_16x16x32_bf16 a[36:39],a[200:203],v[112:115],a[36:39]
ds_read_b64_tr_b16 a[198:199],v2 offset:12288
v_accvgpr_read_b32 v21,a93
v_mfma_f32_16x16x32_bf16 a[36:39],a[204:207],v[120:123],a[36:39]
ds_read_b64_tr_b16 a[200:201],v3
v_accvgpr_read_b32 v22,a94
v_mfma_f32_16x16x32_bf16 a[40:43],a[208:211],v[112:115],a[40:43]
ds_read_b64_tr_b16 a[202:203],v3 offset:4096
ds_read_b64_tr_b16 a[204:205],v3 offset:8192
v_mfma_f32_16x16x32_bf16 a[40:43],a[212:215],v[120:123],a[40:43]
ds_read_b64_tr_b16 a[206:207],v3 offset:12288
v_accvgpr_read_b32 v23,a95
v_mfma_f32_16x16x32_bf16 a[44:47],a[216:219],v[112:115],a[44:47]
ds_read_b64_tr_b16 a[208:209],v2 offset:256
v_accvgpr_read_b32 v16,a88
v_mfma_f32_16x16x32_bf16 a[44:47],a[220:223],v[120:123],a[44:47]
ds_read_b64_tr_b16 a[210:211],v2 offset:4352
v_accvgpr_read_b32 v17,a89
v_mfma_f32_16x16x32_bf16 a[48:51],a[224:227],v[112:115],a[48:51]
ds_read_b64_tr_b16 a[212:213],v2 offset:8448
ds_read_b64_tr_b16 a[214:215],v2 offset:12544
v_mfma_f32_16x16x32_bf16 a[48:51],a[228:231],v[120:123],a[48:51]
v_exp_f32 v67,v67
v_mfma_f32_16x16x32_bf16 a[52:55],a[232:235],v[112:115],a[52:55]
v_exp_f32 v68,v68
v_mfma_f32_16x16x32_bf16 a[52:55],a[236:239],v[120:123],a[52:55]
v_exp_f32 v69,v69
v_mfma_f32_16x16x32_bf16 a[56:59],a[240:243],v[112:115],a[56:59]
v_exp_f32 v70,v70
v_mfma_f32_16x16x32_bf16 a[56:59],a[244:247],v[120:123],a[56:59]
v_exp_f32 v71,v71
v_mfma_f32_16x16x32_bf16 a[60:63],a[248:251],v[112:115],a[60:63]
global_load_lds_dwordx4 v4,s[10:11] offset:2048
ds_read_b64_tr_b16 a[216:217],v3 offset:256
v_mfma_f32_16x16x32_bf16 a[60:63],a[252:255],v[120:123],a[60:63]
ds_read_b64_tr_b16 a[218:219],v3 offset:4352
v_cvt_pk_bf16_f32 v65,v66,v67
v_mfma_f32_16x16x32_bf16 v[60:63],v[28:31],v[112:115],v[60:63]
v_cvt_pk_bf16_f32 v66,v68,v69
v_cvt_pk_bf16_f32 v67,v70,v71
v_mfma_f32_16x16x32_bf16 v[60:63],v[28:31],v[120:123],v[60:63]
v_exp_f32 v72,v72
v_mfma_f32_16x16x32_bf16 v[96:99],a[128:131],v[192:195],v[40:43]
v_exp_f32 v73,v73
v_mfma_f32_16x16x32_bf16 v[96:99],a[132:135],v[200:203],v[96:99]
v_exp_f32 v74,v74
v_mfma_f32_16x16x32_bf16 v[96:99],a[136:139],v[208:211],v[96:99]
v_exp_f32 v75,v75
v_mfma_f32_16x16x32_bf16 v[96:99],a[140:143],v[216:219],v[96:99]
v_exp_f32 v76,v76
v_mfma_f32_16x16x32_bf16 v[100:103],a[156:159],v[216:219],v[40:43]
v_exp_f32 v77,v77
v_mfma_f32_16x16x32_bf16 v[100:103],a[152:155],v[208:211],v[100:103]
L_pd10_%=:
s_cbranch_execz L_t1b_%=
v_exp_f32 v78,v78
v_mfma_f32_16x16x32_bf16 v[100:103],a[148:151],v[200:203],v[100:103]
v_exp_f32 v79,v79
v_mfma_f32_16x16x32_bf16 v[100:103],a[144:147],v[192:195],v[100:103]
ds_read_b64_tr_b16 a[220:221],v3 offset:8448
ds_read_b64_tr_b16 a[222:223],v3 offset:12544
v_mfma_f32_16x16x32_bf16 v[104:107],a[160:163],v[192:195],v[40:43]
v_cvt_pk_bf16_f32 v72,v72,v73
v_cvt_pk_bf16_f32 v73,v74,v75
v_mfma_f32_16x16x32_bf16 v[104:107],a[164:167],v[200:203],v[104:107]
v_cvt_pk_bf16_f32 v74,v76,v77
v_cvt_pk_bf16_f32 v75,v78,v79
v_mfma_f32_16x16x32_bf16 v[104:107],a[168:171],v[208:211],v[104:107]
ds_read_b64_tr_b16 a[224:225],v2 offset:512
ds_read_b64_tr_b16 a[226:227],v2 offset:4608
v_mfma_f32_16x16x32_bf16 v[104:107],a[172:175],v[216:219],v[104:107]
ds_read_b64_tr_b16 a[228:229],v2 offset:8704
v_accvgpr_read_b32 v18,a90
v_mfma_f32_16x16x32_bf16 v[108:111],a[188:191],v[216:219],v[40:43]
global_load_lds_dwordx4 v5,s[10:11] offset:3072
ds_read_b64_tr_b16 a[230:231],v2 offset:12800
v_mfma_f32_16x16x32_bf16 v[108:111],a[184:187],v[208:211],v[108:111]
ds_read_b64_tr_b16 a[232:233],v3 offset:512
v_accvgpr_read_b32 v19,a91
v_mfma_f32_16x16x32_bf16 v[108:111],a[180:183],v[200:203],v[108:111]
ds_read_b64_tr_b16 a[234:235],v3 offset:4608
ds_read_b64_tr_b16 a[236:237],v3 offset:8704
v_mfma_f32_16x16x32_bf16 v[108:111],a[176:179],v[192:195],v[108:111]
s_waitcnt lgkmcnt(7)
ds_read_b64_tr_b16 a[238:239],v3 offset:12800
v_mul_f32 v16,v16,v26
v_mfma_f32_16x16x32_bf16 a[0:3],a[192:195],v[64:67],a[0:3]
ds_read_b64_tr_b16 a[240:241],v2 offset:768
v_mul_f32 v17,v17,v26
v_mfma_f32_16x16x32_bf16 a[0:3],a[196:199],v[72:75],a[0:3]
ds_read_b64_tr_b16 a[242:243],v2 offset:4864
v_mul_f32 v18,v18,v26
v_mfma_f32_16x16x32_bf16 a[4:7],a[200:203],v[64:67],a[4:7]
ds_read_b64_tr_b16 a[244:245],v2 offset:8960
ds_read_b64_tr_b16 a[246:247],v2 offset:13056
v_mfma_f32_16x16x32_bf16 a[4:7],a[204:207],v[72:75],a[4:7]
ds_read_b64_tr_b16 a[248:249],v3 offset:768
v_mul_f32 v19,v19,v26
v_mfma_f32_16x16x32_bf16 a[8:11],a[208:211],v[64:67],a[8:11]
ds_read_b64_tr_b16 a[250:251],v3 offset:4864
v_mul_f32 v20,v20,v26
v_mfma_f32_16x16x32_bf16 a[8:11],a[212:215],v[72:75],a[8:11]
v_mul_f32 v21,v21,v26
v_mul_f32 v22,v22,v26
v_mul_f32 v23,v23,v26
v_cvt_pk_bf16_f32 v16,v16,v17
v_cvt_pk_bf16_f32 v17,v18,v19
v_cvt_pk_bf16_f32 v18,v20,v21
v_cvt_pk_bf16_f32 v19,v22,v23
v_accvgpr_write_b32 a88,v16
v_accvgpr_write_b32 a89,v17
v_accvgpr_write_b32 a90,v18
v_accvgpr_write_b32 a91,v19
v_mfma_f32_16x16x32_bf16 a[12:15],a[216:219],v[64:67],a[12:15]
ds_read_b128 a[92:95],v7 offset:1024
)A"
R"A(v_mov_b32 v8,v7
v_mfma_f32_16x16x32_bf16 a[12:15],a[220:223],v[72:75],a[12:15]
s_waitcnt lgkmcnt(7)
ds_read_b64_tr_b16 a[252:253],v3 offset:8960
v_add_u32 v1,s78,v1
v_mfma_f32_16x16x32_bf16 a[16:19],a[224:227],v[64:67],a[16:19]
ds_read_b64_tr_b16 a[254:255],v3 offset:13056
v_mfma_f32_16x16x32_bf16 a[16:19],a[228:231],v[72:75],a[16:19]
s_waitcnt lgkmcnt(4)
v_exp_f32 v80,v80
v_mfma_f32_16x16x32_bf16 a[20:23],a[232:235],v[64:67],a[20:23]
s_getreg_b32 s92,hwreg(HW_REG_IB_STS,0,4)
s_min_u32 s4,s92,s6
s_mov_b32 s6,s92
v_mfma_f32_16x16x32_bf16 a[20:23],a[236:239],v[72:75],a[20:23]
s_cmp_ge_u32 s4,s9
s_cselect_b32 s4,15,s5
ds_read_b128 v[24:27],v8 offset:4096
v_mfma_f32_16x16x32_bf16 a[24:27],a[240:243],v[64:67],a[24:27]
v_exp_f32 v81,v81
v_mfma_f32_16x16x32_bf16 a[24:27],a[244:247],v[72:75],a[24:27]
s_waitcnt lgkmcnt(4)
v_exp_f32 v82,v82
v_mfma_f32_16x16x32_bf16 a[28:31],a[248:251],v[64:67],a[28:31]
s_waitcnt lgkmcnt(1)
v_exp_f32 v83,v83
v_mfma_f32_16x16x32_bf16 a[28:31],a[252:255],v[72:75],a[28:31]
v_exp_f32 v84,v84
v_mfma_f32_16x16x32_bf16 v[48:51],v[28:31],v[64:67],v[48:51]
s_getpc_b64 s[92:93]
s_cmp_lg_u32 s4,0
s_cbranch_scc1 L_pace_%=
v_exp_f32 v85,v85
v_exp_f32 v86,v86
v_exp_f32 v87,v87
v_exp_f32 v88,v88
v_exp_f32 v89,v89
v_exp_f32 v90,v90
v_exp_f32 v91,v91
v_exp_f32 v92,v92
v_exp_f32 v93,v93
v_exp_f32 v94,v94
v_exp_f32 v95,v95
v_mfma_f32_16x16x32_bf16 v[48:51],v[28:31],v[72:75],v[48:51]
ds_read_b32 v7,v8 offset:5120
v_cvt_pk_bf16_f32 v80,v80,v81
v_cvt_pk_bf16_f32 v81,v82,v83
v_cvt_pk_bf16_f32 v82,v84,v85
v_cvt_pk_bf16_f32 v83,v86,v87
v_cvt_pk_bf16_f32 v88,v88,v89
v_cvt_pk_bf16_f32 v89,v90,v91
v_cvt_pk_bf16_f32 v90,v92,v93
v_cvt_pk_bf16_f32 v91,v94,v95
s_mov_b32 s70,7
L_a1_%=:
s_waitcnt vmcnt(16)
s_barrier
v_mfma_f32_16x16x32_bf16 v[112:115],a[128:131],v[224:227],v[44:47]
s_add_u32 s10,s20,s74
s_addc_u32 s11,s21,0
s_add_u32 m0,s76,0
v_mfma_f32_16x16x32_bf16 v[112:115],a[132:135],v[232:235],v[112:115]
s_add_u32 s75,s75,16384
s_and_b32 s69,s75,0xfffc0000
ds_read_b128 a[128:131],v1
v_mfma_f32_16x16x32_bf16 v[112:115],a[136:139],v[240:243],v[112:115]
global_load_lds_dwordx4 v4,s[10:11] nt
v_mfma_f32_16x16x32_bf16 v[112:115],a[140:143],v[248:251],v[112:115]
ds_read_b128 a[132:135],v1 offset:256
v_mfma_f32_16x16x32_bf16 v[116:119],a[156:159],v[248:251],v[44:47]
v_mfma_f32_16x16x32_bf16 v[116:119],a[152:155],v[240:243],v[116:119]
ds_read_b128 a[136:139],v1 offset:512
v_mfma_f32_16x16x32_bf16 v[116:119],a[148:151],v[232:235],v[116:119]
v_mfma_f32_16x16x32_bf16 v[116:119],a[144:147],v[224:227],v[116:119]
ds_read_b128 a[140:143],v1 offset:768
v_mfma_f32_16x16x32_bf16 v[120:123],a[160:163],v[224:227],v[44:47]
v_mfma_f32_16x16x32_bf16 v[120:123],a[164:167],v[232:235],v[120:123]
ds_read_b128 a[156:159],v1 offset:4864
v_mfma_f32_16x16x32_bf16 v[120:123],a[168:171],v[240:243],v[120:123]
v_mfma_f32_16x16x32_bf16 v[120:123],a[172:175],v[248:251],v[120:123]
ds_read_b128 a[152:155],v1 offset:4608
v_mfma_f32_16x16x32_bf16 v[124:127],a[188:191],v[248:251],v[44:47]
v_exp_f32 v96,v96
v_mfma_f32_16x16x32_bf16 v[124:127],a[184:187],v[240:243],v[124:127]
ds_read_b128 a[148:151],v1 offset:4352
v_add_u32 v2,s78,v2
v_mfma_f32_16x16x32_bf16 v[124:127],a[180:183],v[232:235],v[124:127]
v_exp_f32 v97,v97
v_mfma_f32_16x16x32_bf16 v[124:127],a[176:179],v[224:227],v[124:127]
ds_read_b128 a[144:147],v1 offset:4096
v_cvt_pk_bf16_f32 v96,v96,v97
v_mfma_f32_16x16x32_bf16 v[132:135],a[192:195],v[80:83],v[132:135]
v_exp_f32 v98,v98
v_mfma_f32_16x16x32_bf16 v[132:135],a[196:199],v[88:91],v[132:135]
ds_read_b128 a[160:163],v1 offset:8192
v_add_u32 v3,s78,v3
v_mfma_f32_16x16x32_bf16 v[140:143],a[200:203],v[80:83],v[140:143]
s_sub_u32 s78,s77,32768
global_load_lds_dwordx4 v4,s[10:11] offset:1024 nt
v_mfma_f32_16x16x32_bf16 v[140:143],a[204:207],v[88:91],v[140:143]
s_and_b32 s78,s78,0x1ffff
v_exp_f32 v99,v99
v_mfma_f32_16x16x32_bf16 v[148:151],a[208:211],v[80:83],v[148:151]
s_sub_u32 s78,s78,s77
ds_read_b128 a[164:167],v1 offset:8448
v_cvt_pk_bf16_f32 v97,v98,v99
v_mfma_f32_16x16x32_bf16 v[148:151],a[212:215],v[88:91],v[148:151]
s_add_u32 s77,s77,s78
v_exp_f32 v100,v100
v_mfma_f32_16x16x32_bf16 v[156:159],a[216:219],v[80:83],v[156:159]
v_exp_f32 v101,v101
v_mfma_f32_16x16x32_bf16 v[156:159],a[220:223],v[88:91],v[156:159]
ds_read_b128 a[168:171],v1 offset:8704
v_cvt_pk_bf16_f32 v98,v100,v101
v_mfma_f32_16x16x32_bf16 v[164:167],a[224:227],v[80:83],v[164:167]
v_exp_f32 v102,v102
v_mfma_f32_16x16x32_bf16 v[164:167],a[228:231],v[88:91],v[164:167]
v_exp_f32 v103,v103
v_mfma_f32_16x16x32_bf16 v[172:175],a[232:235],v[80:83],v[172:175]
ds_read_b128 a[172:175],v1 offset:8960
v_cvt_pk_bf16_f32 v99,v102,v103
v_mfma_f32_16x16x32_bf16 v[172:175],a[236:239],v[88:91],v[172:175]
v_exp_f32 v104,v104
v_mfma_f32_16x16x32_bf16 v[180:183],a[240:243],v[80:83],v[180:183]
v_exp_f32 v105,v105
v_mfma_f32_16x16x32_bf16 v[180:183],a[244:247],v[88:91],v[180:183]
ds_read_b128 a[188:191],v1 offset:13056
v_cvt_pk_bf16_f32 v104,v104,v105
v_mfma_f32_16x16x32_bf16 v[188:191],a[248:251],v[80:83],v[188:191]
v_exp_f32 v106,v106
v_mfma_f32_16x16x32_bf16 v[188:191],a[252:255],v[88:91],v[188:191]
v_exp_f32 v107,v107
v_mfma_f32_16x16x32_bf16 v[52:55],v[28:31],v[80:83],v[52:55]
ds_read_b128 a[184:187],v1 offset:12800
v_cvt_pk_bf16_f32 v105,v106,v107
v_mfma_f32_16x16x32_bf16 v[52:55],v[28:31],v[88:91],v[52:55]
s_waitcnt lgkmcnt(3)
v_exp_f32 v108,v108
v_mfma_f32_16x16x32_bf16 v[64:67],a[128:131],v[128:131],v[32:35]
global_load_lds_dwordx4 v5,s[10:11] offset:2048 nt
ds_read_b128 a[180:183],v1 offset:12544
v_mfma_f32_16x16x32_bf16 v[64:67],a[132:135],v[136:139],v[64:67]
v_exp_f32 v109,v109
v_mfma_f32_16x16x32_bf16 v[64:67],a[136:139],v[144:147],v[64:67]
ds_read_b128 a[176:179],v1 offset:12288
v_cvt_pk_bf16_f32 v106,v108,v109
v_mfma_f32_16x16x32_bf16 v[64:67],a[140:143],v[152:155],v[64:67]
v_exp_f32 v110,v110
v_mfma_f32_16x16x32_bf16 v[68:71],a[156:159],v[152:155],v[32:35]
v_exp_f32 v111,v111
v_mfma_f32_16x16x32_bf16 v[68:71],a[152:155],v[144:147],v[68:71]
v_cvt_pk_bf16_f32 v107,v110,v111
v_add_u32 v1,s78,v1
v_mfma_f32_16x16x32_bf16 v[68:71],a[148:151],v[136:139],v[68:71]
v_exp_f32 v112,v112
v_mfma_f32_16x16x32_bf16 v[68:71],a[144:147],v[128:131],v[68:71]
v_exp_f32 v113,v113
v_mfma_f32_16x16x32_bf16 v[72:75],a[160:163],v[128:131],v[32:35]
v_exp_f32 v114,v114
v_mfma_f32_16x16x32_bf16 v[72:75],a[164:167],v[136:139],v[72:75]
v_exp_f32 v115,v115
v_mfma_f32_16x16x32_bf16 v[72:75],a[168:171],v[144:147],v[72:75]
s_waitcnt lgkmcnt(0)
v_exp_f32 v116,v116
v_mfma_f32_16x16x32_bf16 v[72:75],a[172:175],v[152:155],v[72:75]
v_exp_f32 v117,v117
v_mfma_f32_16x16x32_bf16 v[76:79],a[188:191],v[152:155],v[32:35]
v_exp_f32 v118,v118
v_mfma_f32_16x16x32_bf16 v[76:79],a[184:187],v[144:147],v[76:79]
v_exp_f32 v119,v119
v_mfma_f32_16x16x32_bf16 v[76:79],a[180:183],v[136:139],v[76:79]
v_cvt_pk_bf16_f32 v112,v112,v113
v_cvt_pk_bf16_f32 v113,v114,v115
v_mfma_f32_16x16x32_bf16 v[76:79],a[176:179],v[128:131],v[76:79]
v_cvt_pk_bf16_f32 v114,v116,v117
v_cvt_pk_bf16_f32 v115,v118,v119
v_mfma_f32_16x16x32_bf16 v[196:199],a[192:195],v[96:99],v[196:199]
global_load_lds_dwordx4 v5,s[10:11] offset:3072
v_mfma_f32_16x16x32_bf16 v[196:199],a[196:199],v[104:107],v[196:199]
v_exp_f32 v120,v120
v_mfma_f32_16x16x32_bf16 v[204:207],a[200:203],v[96:99],v[204:207]
v_exp_f32 v121,v121
v_mfma_f32_16x16x32_bf16 v[204:207],a[204:207],v[104:107],v[204:207]
v_exp_f32 v122,v122
v_mfma_f32_16x16x32_bf16 v[212:215],a[208:211],v[96:99],v[212:215]
v_exp_f32 v123,v123
v_mfma_f32_16x16x32_bf16 v[212:215],a[212:215],v[104:107],v[212:215]
v_exp_f32 v124,v124
v_mfma_f32_16x16x32_bf16 v[220:223],a[216:219],v[96:99],v[220:223]
v_exp_f32 v125,v125
v_mfma_f32_16x16x32_bf16 v[220:223],a[220:223],v[104:107],v[220:223]
v_exp_f32 v126,v126
v_mfma_f32_16x16x32_bf16 v[228:231],a[224:227],v[96:99],v[228:231]
v_exp_f32 v127,v127
v_mfma_f32_16x16x32_bf16 v[228:231],a[228:231],v[104:107],v[228:231]
v_cvt_pk_bf16_f32 v120,v120,v121
v_cvt_pk_bf16_f32 v121,v122,v123
v_mfma_f32_16x16x32_bf16 v[236:239],a[232:235],v[96:99],v[236:239]
v_cvt_pk_bf16_f32 v122,v124,v125
v_cvt_pk_bf16_f32 v123,v126,v127
v_mfma_f32_16x16x32_bf16 v[236:239],a[236:239],v[104:107],v[236:239]
v_exp_f32 v64,v64
v_mfma_f32_16x16x32_bf16 v[244:247],a[240:243],v[96:99],v[244:247]
s_add_u32 s10,s24,s74
s_addc_u32 s11,s25,0
s_add_u32 s74,s74,16384
v_mfma_f32_16x16x32_bf16 v[244:247],a[244:247],v[104:107],v[244:247]
s_and_b32 s74,s74,0x3ffff
v_exp_f32 v65,v65
v_mfma_f32_16x16x32_bf16 v[252:255],a[248:251],v[96:99],v[252:255]
s_add_u32 m0,s76,16384
v_exp_f32 v66,v66
v_mfma_f32_16x16x32_bf16 v[252:255],a[252:255],v[104:107],v[252:255]
s_or_b32 s74,s74,s69
v_exp_f32 v67,v67
v_mfma_f32_16x16x32_bf16 v[56:59],v[28:31],v[96:99],v[56:59]
global_load_lds_dwordx4 v4,s[10:11]
v_cvt_pk_bf16_f32 v64,v64,v65
v_mfma_f32_16x16x32_bf16 v[56:59],v[28:31],v[104:107],v[56:59]
s_sub_u32 s76,s76,32768
v_exp_f32 v68,v68
v_mfma_f32_16x16x32_bf16 v[80:83],a[128:131],v[160:163],v[36:39]
s_and_b32 s76,s76,0x1ffff
v_exp_f32 v69,v69
v_mfma_f32_16x16x32_bf16 v[80:83],a[132:135],v[168:171],v[80:83]
v_exp_f32 v70,v70
v_mfma_f32_16x16x32_bf16 v[80:83],a[136:139],v[176:179],v[80:83]
v_exp_f32 v71,v71
v_mfma_f32_16x16x32_bf16 v[80:83],a[140:143],v[184:187],v[80:83]
v_cvt_pk_bf16_f32 v65,v66,v67
v_cvt_pk_bf16_f32 v66,v68,v69
v_mfma_f32_16x16x32_bf16 v[84:87],a[156:159],v[184:187],v[36:39]
v_exp_f32 v72,v72
v_mfma_f32_16x16x32_bf16 v[84:87],a[152:155],v[176:179],v[84:87]
v_exp_f32 v73,v73
v_mfma_f32_16x16x32_bf16 v[84:87],a[148:151],v[168:171],v[84:87]
v_cvt_pk_bf16_f32 v67,v70,v71
v_cvt_pk_bf16_f32 v72,v72,v73
v_mfma_f32_16x16x32_bf16 v[84:87],a[144:147],v[160:163],v[84:87]
v_exp_f32 v74,v74
v_mfma_f32_16x16x32_bf16 v[88:91],a[160:163],v[160:163],v[36:39]
v_exp_f32 v75,v75
v_mfma_f32_16x16x32_bf16 v[88:91],a[164:167],v[168:171],v[88:91]
v_exp_f32 v76,v76
v_mfma_f32_16x16x32_bf16 v[88:91],a[168:171],v[176:179],v[88:91]
v_exp_f32 v77,v77
v_mfma_f32_16x16x32_bf16 v[88:91],a[172:175],v[184:187],v[88:91]
v_exp_f32 v78,v78
v_mfma_f32_16x16x32_bf16 v[92:95],a[188:191],v[184:187],v[36:39]
v_exp_f32 v79,v79
v_mfma_f32_16x16x32_bf16 v[92:95],a[184:187],v[176:179],v[92:95]
v_cvt_pk_bf16_f32 v73,v74,v75
v_cvt_pk_bf16_f32 v74,v76,v77
v_mfma_f32_16x16x32_bf16 v[92:95],a[180:183],v[168:171],v[92:95]
global_load_lds_dwordx4 v5,s[10:11] offset:1024
v_cvt_pk_bf16_f32 v75,v78,v79
v_mfma_f32_16x16x32_bf16 v[92:95],a[176:179],v[160:163],v[92:95]
v_exp_f32 v80,v80
v_mfma_f32_16x16x32_bf16 a[32:35],a[192:195],v[112:115],a[32:35]
ds_read_b64_tr_b16 a[192:193],v2
ds_read_b64_tr_b16 a[194:195],v2 offset:4096
v_mfma_f32_16x16x32_bf16 a[32:35],a[196:199],v[120:123],a[32:35]
v_exp_f32 v81,v81
v_mfma_f32_16x16x32_bf16 a[36:39],a[200:203],v[112:115],a[36:39]
ds_read_b64_tr_b16 a[196:197],v2 offset:8192
ds_read_b64_tr_b16 a[198:199],v2 offset:12288
v_mfma_f32_16x16x32_bf16 a[36:39],a[204:207],v[120:123],a[36:39]
ds_read_b64_tr_b16 a[200:201],v3
v_cvt_pk_bf16_f32 v80,v80,v81
v_mfma_f32_16x16x32_bf16 a[40:43],a[208:211],v[112:115],a[40:43]
v_exp_f32 v82,v82
v_mfma_f32_16x16x32_bf16 a[40:43],a[212:215],v[120:123],a[40:43]
ds_read_b64_tr_b16 a[202:203],v3 offset:4096
ds_read_b64_tr_b16 a[204:205],v3 offset:8192
v_mfma_f32_16x16x32_bf16 a[44:47],a[216:219],v[112:115],a[44:47]
v_exp_f32 v83,v83
v_mfma_f32_16x16x32_bf16 a[44:47],a[220:223],v[120:123],a[44:47]
ds_read_b64_tr_b16 a[206:207],v3 offset:12288
ds_read_b64_tr_b16 a[208:209],v2 offset:256
v_mfma_f32_16x16x32_bf16 a[48:51],a[224:227],v[112:115],a[48:51]
ds_read_b64_tr_b16 a[210:211],v2 offset:4352
v_cvt_pk_bf16_f32 v81,v82,v83
v_mfma_f32_16x16x32_bf16 a[48:51],a[228:231],v[120:123],a[48:51]
v_exp_f32 v84,v84
v_mfma_f32_16x16x32_bf16 a[52:55],a[232:235],v[112:115],a[52:55]
ds_read_b64_tr_b16 a[212:213],v2 offset:8448
ds_read_b64_tr_b16 a[214:215],v2 offset:12544
v_mfma_f32_16x16x32_bf16 a[52:55],a[236:239],v[120:123],a[52:55]
v_exp_f32 v85,v85
v_mfma_f32_16x16x32_bf16 a[56:59],a[240:243],v[112:115],a[56:59]
ds_read_b64_tr_b16 a[216:217],v3 offset:256
ds_read_b64_tr_b16 a[218:219],v3 offset:4352
)A"
R"A(v_mfma_f32_16x16x32_bf16 a[56:59],a[244:247],v[120:123],a[56:59]
ds_read_b64_tr_b16 a[220:221],v3 offset:8448
v_cvt_pk_bf16_f32 v82,v84,v85
v_mfma_f32_16x16x32_bf16 a[60:63],a[248:251],v[112:115],a[60:63]
global_load_lds_dwordx4 v4,s[10:11] offset:2048
ds_read_b64_tr_b16 a[222:223],v3 offset:12544
v_mfma_f32_16x16x32_bf16 a[60:63],a[252:255],v[120:123],a[60:63]
v_exp_f32 v86,v86
v_mfma_f32_16x16x32_bf16 v[60:63],v[28:31],v[112:115],v[60:63]
ds_read_b64_tr_b16 a[224:225],v2 offset:512
ds_read_b64_tr_b16 a[226:227],v2 offset:4608
v_mfma_f32_16x16x32_bf16 v[60:63],v[28:31],v[120:123],v[60:63]
v_exp_f32 v87,v87
v_mfma_f32_16x16x32_bf16 v[96:99],a[128:131],v[192:195],v[40:43]
ds_read_b64_tr_b16 a[228:229],v2 offset:8704
ds_read_b64_tr_b16 a[230:231],v2 offset:12800
v_mfma_f32_16x16x32_bf16 v[96:99],a[132:135],v[200:203],v[96:99]
ds_read_b64_tr_b16 a[232:233],v3 offset:512
v_cvt_pk_bf16_f32 v83,v86,v87
v_mfma_f32_16x16x32_bf16 v[96:99],a[136:139],v[208:211],v[96:99]
v_exp_f32 v88,v88
v_mfma_f32_16x16x32_bf16 v[96:99],a[140:143],v[216:219],v[96:99]
ds_read_b64_tr_b16 a[234:235],v3 offset:4608
ds_read_b64_tr_b16 a[236:237],v3 offset:8704
v_mfma_f32_16x16x32_bf16 v[100:103],a[156:159],v[216:219],v[40:43]
v_exp_f32 v89,v89
v_mfma_f32_16x16x32_bf16 v[100:103],a[152:155],v[208:211],v[100:103]
ds_read_b64_tr_b16 a[238:239],v3 offset:12800
ds_read_b64_tr_b16 a[240:241],v2 offset:768
v_mfma_f32_16x16x32_bf16 v[100:103],a[148:151],v[200:203],v[100:103]
ds_read_b64_tr_b16 a[242:243],v2 offset:4864
v_cvt_pk_bf16_f32 v88,v88,v89
v_mfma_f32_16x16x32_bf16 v[100:103],a[144:147],v[192:195],v[100:103]
v_exp_f32 v90,v90
v_mfma_f32_16x16x32_bf16 v[104:107],a[160:163],v[192:195],v[40:43]
ds_read_b64_tr_b16 a[244:245],v2 offset:8960
ds_read_b64_tr_b16 a[246:247],v2 offset:13056
v_mfma_f32_16x16x32_bf16 v[104:107],a[164:167],v[200:203],v[104:107]
v_exp_f32 v91,v91
v_mfma_f32_16x16x32_bf16 v[104:107],a[168:171],v[208:211],v[104:107]
ds_read_b64_tr_b16 a[248:249],v3 offset:768
ds_read_b64_tr_b16 a[250:251],v3 offset:4864
v_mfma_f32_16x16x32_bf16 v[104:107],a[172:175],v[216:219],v[104:107]
ds_read_b64_tr_b16 a[252:253],v3 offset:8960
v_cvt_pk_bf16_f32 v89,v90,v91
v_mfma_f32_16x16x32_bf16 v[108:111],a[188:191],v[216:219],v[40:43]
global_load_lds_dwordx4 v5,s[10:11] offset:3072
ds_read_b64_tr_b16 a[254:255],v3 offset:13056
v_mfma_f32_16x16x32_bf16 v[108:111],a[184:187],v[208:211],v[108:111]
v_exp_f32 v92,v92
v_mfma_f32_16x16x32_bf16 v[108:111],a[180:183],v[200:203],v[108:111]
v_exp_f32 v93,v93
v_mfma_f32_16x16x32_bf16 v[108:111],a[176:179],v[192:195],v[108:111]
s_waitcnt lgkmcnt(6)
v_exp_f32 v94,v94
v_mfma_f32_16x16x32_bf16 a[0:3],a[192:195],v[64:67],a[0:3]
v_exp_f32 v95,v95
v_mfma_f32_16x16x32_bf16 a[0:3],a[196:199],v[72:75],a[0:3]
v_cvt_pk_bf16_f32 v90,v92,v93
v_cvt_pk_bf16_f32 v91,v94,v95
v_mfma_f32_16x16x32_bf16 a[4:7],a[200:203],v[64:67],a[4:7]
v_mfma_f32_16x16x32_bf16 a[4:7],a[204:207],v[72:75],a[4:7]
v_mfma_f32_16x16x32_bf16 a[8:11],a[208:211],v[64:67],a[8:11]
v_mfma_f32_16x16x32_bf16 a[8:11],a[212:215],v[72:75],a[8:11]
v_mfma_f32_16x16x32_bf16 a[12:15],a[216:219],v[64:67],a[12:15]
v_mfma_f32_16x16x32_bf16 a[12:15],a[220:223],v[72:75],a[12:15]
v_mfma_f32_16x16x32_bf16 a[16:19],a[224:227],v[64:67],a[16:19]
v_mfma_f32_16x16x32_bf16 a[16:19],a[228:231],v[72:75],a[16:19]
v_mfma_f32_16x16x32_bf16 a[20:23],a[232:235],v[64:67],a[20:23]
s_getreg_b32 s92,hwreg(HW_REG_IB_STS,0,4)
s_min_u32 s4,s92,s6
s_mov_b32 s6,s92
v_mfma_f32_16x16x32_bf16 a[20:23],a[236:239],v[72:75],a[20:23]
s_cmp_ge_u32 s4,s9
s_cselect_b32 s4,15,s5
v_mfma_f32_16x16x32_bf16 a[24:27],a[240:243],v[64:67],a[24:27]
s_waitcnt lgkmcnt(0)
v_mfma_f32_16x16x32_bf16 a[24:27],a[244:247],v[72:75],a[24:27]
v_mfma_f32_16x16x32_bf16 a[28:31],a[248:251],v[64:67],a[28:31]
v_mfma_f32_16x16x32_bf16 a[28:31],a[252:255],v[72:75],a[28:31]
s_getpc_b64 s[92:93]
s_cmp_lg_u32 s4,0
s_cbranch_scc1 L_pace_%=
v_mfma_f32_16x16x32_bf16 v[48:51],v[28:31],v[64:67],v[48:51]
v_mfma_f32_16x16x32_bf16 v[48:51],v[28:31],v[72:75],v[48:51]
s_sub_u32 s70,s70,1
s_cbranch_scc0 L_a1_%=
L_nx1_%=:
s_waitcnt vmcnt(16)
s_barrier
v_mfma_f32_16x16x32_bf16 v[112:115],a[128:131],v[224:227],v[44:47]
s_mul_i32 s86,s60,10240
s_mul_i32 s85,s63,1310720
s_mul_i32 s66,s64,320
v_mfma_f32_16x16x32_bf16 v[112:115],a[132:135],v[232:235],v[112:115]
s_add_u32 s85,s85,s66
s_add_u32 s83,s64,0
s_and_b32 s83,s83,3
v_mfma_f32_16x16x32_bf16 v[112:115],a[136:139],v[240:243],v[112:115]
s_lshl_b32 s83,s83,1
s_mul_i32 s83,s83,163840
s_add_u32 s83,s83,s85
v_mfma_f32_16x16x32_bf16 v[112:115],a[140:143],v[248:251],v[112:115]
s_cmp_eq_u32 s60,3
s_cselect_b32 s69,0x40000000,0
v_add_u32 v14,s69,v6
v_mfma_f32_16x16x32_bf16 v[116:119],a[156:159],v[248:251],v[44:47]
s_add_u32 s84,s83,20480
ds_read_b128 a[128:131],v1
v_add_u32 v2,s78,v2
v_mfma_f32_16x16x32_bf16 v[116:119],a[152:155],v[240:243],v[116:119]
s_add_u32 m0,s86,122880
v_exp_f32 v96,v96
v_mfma_f32_16x16x32_bf16 v[116:119],a[148:151],v[232:235],v[116:119]
s_getpc_b64 s[8:9]
L_pcg_%=:
ds_read_b128 a[132:135],v1 offset:256
v_add_u32 v3,s78,v3
v_mfma_f32_16x16x32_bf16 v[116:119],a[144:147],v[224:227],v[116:119]
buffer_load_dwordx4 v24,s[48:51],s83 offen lds
v_mfma_f32_16x16x32_bf16 v[120:123],a[160:163],v[224:227],v[44:47]
s_sub_u32 s78,s77,32768
s_and_b32 s78,s78,0x1ffff
ds_read_b128 a[136:139],v1 offset:512
v_mfma_f32_16x16x32_bf16 v[120:123],a[164:167],v[232:235],v[120:123]
buffer_store_dwordx4 a[112:115],v14,s[40:43],s81 offen offset:3072 sc1
v_mfma_f32_16x16x32_bf16 v[120:123],a[168:171],v[240:243],v[120:123]
s_sub_u32 s78,s78,s77
ds_read_b128 a[140:143],v1 offset:768
v_lshlrev_b32 v4,10,v6
v_mfma_f32_16x16x32_bf16 v[120:123],a[172:175],v[248:251],v[120:123]
s_add_u32 s77,s77,s78
v_exp_f32 v97,v97
v_mfma_f32_16x16x32_bf16 v[124:127],a[188:191],v[248:251],v[44:47]
s_mov_b32 s6,L_gbF_%=-L_pcg_%=
ds_read_b128 a[156:159],v1 offset:4864
v_cvt_pk_bf16_f32 v96,v96,v97
v_mfma_f32_16x16x32_bf16 v[124:127],a[184:187],v[240:243],v[124:127]
s_mov_b32 s10,L_geF_%=-L_gbF_%=
v_exp_f32 v98,v98
v_mfma_f32_16x16x32_bf16 v[124:127],a[180:183],v[232:235],v[124:127]
s_mov_b32 s11,0x20000
s_lshr_b32 s7,%1,3
ds_read_b128 a[152:155],v1 offset:4608
v_mfma_f32_16x16x32_bf16 v[124:127],a[176:179],v[224:227],v[124:127]
s_lshl_b32 s7,s7,2
v_exp_f32 v99,v99
v_mfma_f32_16x16x32_bf16 v[132:135],a[192:195],v[80:83],v[132:135]
s_add_u32 s7,s7,s60
ds_read_b128 a[148:151],v1 offset:4352
v_cvt_pk_bf16_f32 v97,v98,v99
v_mfma_f32_16x16x32_bf16 v[132:135],a[196:199],v[88:91],v[132:135]
buffer_load_dwordx4 v25,s[48:51],s83 offen offset:1024 lds
v_mfma_f32_16x16x32_bf16 v[140:143],a[200:203],v[80:83],v[140:143]
s_lshl_b32 s7,s7,7
ds_read_b128 a[144:147],v1 offset:4096
v_mov_b32 v8,s52
v_mfma_f32_16x16x32_bf16 v[140:143],a[204:207],v[88:91],v[140:143]
s_lshl_b32 s66,s61,5
v_exp_f32 v100,v100
v_mfma_f32_16x16x32_bf16 v[148:151],a[208:211],v[80:83],v[148:151]
s_lshl_b32 s67,s63,3
ds_read_b128 a[160:163],v1 offset:8192
v_mov_b32 v9,s53
v_mfma_f32_16x16x32_bf16 v[148:151],a[212:215],v[88:91],v[148:151]
s_add_u32 s66,s66,s67
v_exp_f32 v101,v101
v_mfma_f32_16x16x32_bf16 v[156:159],a[216:219],v[80:83],v[156:159]
s_lshl_b32 s66,s66,16
ds_read_b128 a[164:167],v1 offset:8448
v_cvt_pk_bf16_f32 v98,v100,v101
v_mfma_f32_16x16x32_bf16 v[156:159],a[220:223],v[88:91],v[156:159]
s_lshl_b32 s67,s60,14
v_exp_f32 v102,v102
v_mfma_f32_16x16x32_bf16 v[164:167],a[224:227],v[80:83],v[164:167]
s_add_u32 s66,s66,s67
ds_read_b128 a[168:171],v1 offset:8704
v_lshlrev_b32 v10,20,v6
v_mfma_f32_16x16x32_bf16 v[164:167],a[228:231],v[88:91],v[164:167]
s_mov_b32 s30,1523712
v_exp_f32 v103,v103
v_mfma_f32_16x16x32_bf16 v[172:175],a[232:235],v[80:83],v[172:175]
buffer_store_dwordx4 a[116:119],v6,s[40:43],s81 offen offset:1024 sc1
v_mfma_f32_16x16x32_bf16 v[172:175],a[236:239],v[88:91],v[172:175]
s_mov_b32 s31,0x20000
ds_read_b128 a[172:175],v1 offset:8960
v_cvt_pk_bf16_f32 v99,v102,v103
v_mfma_f32_16x16x32_bf16 v[180:183],a[240:243],v[80:83],v[180:183]
buffer_load_dwordx4 v26,s[48:51],s83 offen offset:2048 lds
v_mfma_f32_16x16x32_bf16 v[180:183],a[244:247],v[88:91],v[180:183]
v_exp_f32 v104,v104
v_mfma_f32_16x16x32_bf16 v[188:191],a[248:251],v[80:83],v[188:191]
v_exp_f32 v105,v105
v_mfma_f32_16x16x32_bf16 v[188:191],a[252:255],v[88:91],v[188:191]
ds_read_b128 a[188:191],v1 offset:13056
v_cvt_pk_bf16_f32 v104,v104,v105
v_mfma_f32_16x16x32_bf16 v[52:55],v[28:31],v[80:83],v[52:55]
v_exp_f32 v106,v106
v_mfma_f32_16x16x32_bf16 v[52:55],v[28:31],v[88:91],v[52:55]
s_waitcnt lgkmcnt(2)
v_exp_f32 v107,v107
v_mfma_f32_16x16x32_bf16 v[64:67],a[128:131],v[128:131],v[32:35]
ds_read_b128 a[184:187],v1 offset:12800
v_cvt_pk_bf16_f32 v105,v106,v107
v_mfma_f32_16x16x32_bf16 v[64:67],a[132:135],v[136:139],v[64:67]
v_exp_f32 v108,v108
v_mfma_f32_16x16x32_bf16 v[64:67],a[136:139],v[144:147],v[64:67]
s_add_u32 s28,s12,s66
s_addc_u32 s29,s13,0
ds_read_b128 a[180:183],v1 offset:12544
v_mfma_f32_16x16x32_bf16 v[64:67],a[140:143],v[152:155],v[64:67]
buffer_load_dwordx4 v27,s[48:51],s83 offen offset:3072 lds
v_mfma_f32_16x16x32_bf16 v[68:71],a[156:159],v[152:155],v[32:35]
s_and_b32 s29,s29,0xffff
ds_read_b128 a[176:179],v1 offset:12288
v_add_u32 v1,s78,v1
v_mfma_f32_16x16x32_bf16 v[68:71],a[152:155],v[144:147],v[68:71]
v_exp_f32 v109,v109
v_mfma_f32_16x16x32_bf16 v[68:71],a[148:151],v[136:139],v[68:71]
v_exp_f32 v110,v110
v_mfma_f32_16x16x32_bf16 v[68:71],a[144:147],v[128:131],v[68:71]
v_exp_f32 v111,v111
v_mfma_f32_16x16x32_bf16 v[72:75],a[160:163],v[128:131],v[32:35]
v_cvt_pk_bf16_f32 v106,v108,v109
v_cvt_pk_bf16_f32 v107,v110,v111
v_mfma_f32_16x16x32_bf16 v[72:75],a[164:167],v[136:139],v[72:75]
buffer_store_dwordx4 a[120:123],v14,s[40:43],s82 offen offset:3072 sc1
v_mfma_f32_16x16x32_bf16 v[72:75],a[168:171],v[144:147],v[72:75]
s_waitcnt lgkmcnt(1)
v_exp_f32 v112,v112
v_mfma_f32_16x16x32_bf16 v[72:75],a[172:175],v[152:155],v[72:75]
s_add_u32 m0,s86,126976
v_exp_f32 v113,v113
v_mfma_f32_16x16x32_bf16 v[76:79],a[188:191],v[152:155],v[32:35]
s_waitcnt lgkmcnt(0)
v_exp_f32 v114,v114
v_mfma_f32_16x16x32_bf16 v[76:79],a[184:187],v[144:147],v[76:79]
buffer_load_dwordx4 v7,s[48:51],s83 offen lds
v_mfma_f32_16x16x32_bf16 v[76:79],a[180:183],v[136:139],v[76:79]
s_add_u32 s83,s64,0
v_exp_f32 v115,v115
v_mfma_f32_16x16x32_bf16 v[76:79],a[176:179],v[128:131],v[76:79]
s_and_b32 s83,s83,3
v_exp_f32 v116,v116
v_mfma_f32_16x16x32_bf16 v[196:199],a[192:195],v[96:99],v[196:199]
s_lshl_b32 s83,s83,1
v_exp_f32 v117,v117
v_mfma_f32_16x16x32_bf16 v[196:199],a[196:199],v[104:107],v[196:199]
s_or_b32 s83,s83,1
v_exp_f32 v118,v118
v_mfma_f32_16x16x32_bf16 v[204:207],a[200:203],v[96:99],v[204:207]
s_mul_i32 s83,s83,163840
v_exp_f32 v119,v119
v_mfma_f32_16x16x32_bf16 v[204:207],a[204:207],v[104:107],v[204:207]
s_add_u32 s83,s83,s85
v_cvt_pk_bf16_f32 v112,v112,v113
v_cvt_pk_bf16_f32 v113,v114,v115
v_mfma_f32_16x16x32_bf16 v[212:215],a[208:211],v[96:99],v[212:215]
v_cvt_pk_bf16_f32 v114,v116,v117
v_cvt_pk_bf16_f32 v115,v118,v119
v_mfma_f32_16x16x32_bf16 v[212:215],a[212:215],v[104:107],v[212:215]
L_pd11_%=:
s_cbranch_execz L_t1c_%=
s_add_u32 m0,s86,128000
v_exp_f32 v120,v120
v_mfma_f32_16x16x32_bf16 v[220:223],a[216:219],v[96:99],v[220:223]
v_exp_f32 v121,v121
v_mfma_f32_16x16x32_bf16 v[220:223],a[220:223],v[104:107],v[220:223]
buffer_load_dwordx4 v24,s[48:51],s84 offen lds
v_mfma_f32_16x16x32_bf16 v[228:231],a[224:227],v[96:99],v[228:231]
v_exp_f32 v122,v122
v_mfma_f32_16x16x32_bf16 v[228:231],a[228:231],v[104:107],v[228:231]
v_exp_f32 v123,v123
v_mfma_f32_16x16x32_bf16 v[236:239],a[232:235],v[96:99],v[236:239]
buffer_store_dwordx4 a[124:127],v6,s[40:43],s82 offen offset:1024 sc1
v_mfma_f32_16x16x32_bf16 v[236:239],a[236:239],v[104:107],v[236:239]
v_exp_f32 v124,v124
v_mfma_f32_16x16x32_bf16 v[244:247],a[240:243],v[96:99],v[244:247]
v_exp_f32 v125,v125
v_mfma_f32_16x16x32_bf16 v[244:247],a[244:247],v[104:107],v[244:247]
v_exp_f32 v126,v126
v_mfma_f32_16x16x32_bf16 v[252:255],a[248:251],v[96:99],v[252:255]
v_exp_f32 v127,v127
v_mfma_f32_16x16x32_bf16 v[252:255],a[252:255],v[104:107],v[252:255]
)A"
R"A(v_cvt_pk_bf16_f32 v120,v120,v121
v_cvt_pk_bf16_f32 v121,v122,v123
v_mfma_f32_16x16x32_bf16 v[56:59],v[28:31],v[96:99],v[56:59]
v_cvt_pk_bf16_f32 v122,v124,v125
v_cvt_pk_bf16_f32 v123,v126,v127
v_mfma_f32_16x16x32_bf16 v[56:59],v[28:31],v[104:107],v[56:59]
buffer_load_dwordx4 v25,s[48:51],s84 offen offset:1024 lds
v_mfma_f32_16x16x32_bf16 v[80:83],a[128:131],v[160:163],v[36:39]
v_exp_f32 v64,v64
v_mfma_f32_16x16x32_bf16 v[80:83],a[132:135],v[168:171],v[80:83]
v_exp_f32 v65,v65
v_mfma_f32_16x16x32_bf16 v[80:83],a[136:139],v[176:179],v[80:83]
v_exp_f32 v66,v66
v_mfma_f32_16x16x32_bf16 v[80:83],a[140:143],v[184:187],v[80:83]
v_exp_f32 v67,v67
v_mfma_f32_16x16x32_bf16 v[84:87],a[156:159],v[184:187],v[36:39]
v_exp_f32 v68,v68
v_mfma_f32_16x16x32_bf16 v[84:87],a[152:155],v[176:179],v[84:87]
v_exp_f32 v69,v69
v_mfma_f32_16x16x32_bf16 v[84:87],a[148:151],v[168:171],v[84:87]
v_exp_f32 v70,v70
v_mfma_f32_16x16x32_bf16 v[84:87],a[144:147],v[160:163],v[84:87]
v_exp_f32 v71,v71
v_mfma_f32_16x16x32_bf16 v[88:91],a[160:163],v[160:163],v[36:39]
v_cvt_pk_bf16_f32 v64,v64,v65
v_cvt_pk_bf16_f32 v65,v66,v67
v_mfma_f32_16x16x32_bf16 v[88:91],a[164:167],v[168:171],v[88:91]
buffer_store_dwordx4 a[96:99],v14,s[40:43],s79 offen offset:3072 sc1
v_mfma_f32_16x16x32_bf16 v[88:91],a[168:171],v[176:179],v[88:91]
v_cvt_pk_bf16_f32 v66,v68,v69
v_cvt_pk_bf16_f32 v67,v70,v71
v_mfma_f32_16x16x32_bf16 v[88:91],a[172:175],v[184:187],v[88:91]
buffer_load_dwordx4 v26,s[48:51],s84 offen offset:2048 lds
v_mfma_f32_16x16x32_bf16 v[92:95],a[188:191],v[184:187],v[36:39]
v_exp_f32 v72,v72
v_mfma_f32_16x16x32_bf16 v[92:95],a[184:187],v[176:179],v[92:95]
v_exp_f32 v73,v73
v_mfma_f32_16x16x32_bf16 v[92:95],a[180:183],v[168:171],v[92:95]
v_exp_f32 v74,v74
v_mfma_f32_16x16x32_bf16 v[92:95],a[176:179],v[160:163],v[92:95]
v_exp_f32 v75,v75
v_mfma_f32_16x16x32_bf16 a[32:35],a[192:195],v[112:115],a[32:35]
ds_read_b64_tr_b16 a[192:193],v2
ds_read_b64_tr_b16 a[194:195],v2 offset:4096
v_mfma_f32_16x16x32_bf16 a[32:35],a[196:199],v[120:123],a[32:35]
ds_read_b64_tr_b16 a[196:197],v2 offset:8192
v_cvt_pk_bf16_f32 v72,v72,v73
v_mfma_f32_16x16x32_bf16 a[36:39],a[200:203],v[112:115],a[36:39]
ds_read_b64_tr_b16 a[198:199],v2 offset:12288
v_cvt_pk_bf16_f32 v73,v74,v75
v_mfma_f32_16x16x32_bf16 a[36:39],a[204:207],v[120:123],a[36:39]
buffer_load_dwordx4 v27,s[48:51],s84 offen offset:3072 lds
v_mfma_f32_16x16x32_bf16 a[40:43],a[208:211],v[112:115],a[40:43]
ds_read_b64_tr_b16 a[200:201],v3
ds_read_b64_tr_b16 a[202:203],v3 offset:4096
v_mfma_f32_16x16x32_bf16 a[40:43],a[212:215],v[120:123],a[40:43]
v_exp_f32 v76,v76
v_mfma_f32_16x16x32_bf16 a[44:47],a[216:219],v[112:115],a[44:47]
ds_read_b64_tr_b16 a[204:205],v3 offset:8192
ds_read_b64_tr_b16 a[206:207],v3 offset:12288
v_mfma_f32_16x16x32_bf16 a[44:47],a[220:223],v[120:123],a[44:47]
v_exp_f32 v77,v77
v_mfma_f32_16x16x32_bf16 a[48:51],a[224:227],v[112:115],a[48:51]
ds_read_b64_tr_b16 a[208:209],v2 offset:256
ds_read_b64_tr_b16 a[210:211],v2 offset:4352
v_mfma_f32_16x16x32_bf16 a[48:51],a[228:231],v[120:123],a[48:51]
ds_read_b64_tr_b16 a[212:213],v2 offset:8448
v_cvt_pk_bf16_f32 v74,v76,v77
v_mfma_f32_16x16x32_bf16 a[52:55],a[232:235],v[112:115],a[52:55]
buffer_store_dwordx4 a[100:103],v6,s[40:43],s79 offen offset:1024 sc1
v_mfma_f32_16x16x32_bf16 a[52:55],a[236:239],v[120:123],a[52:55]
s_add_u32 m0,s86,132096
ds_read_b64_tr_b16 a[214:215],v2 offset:12544
ds_read_b64_tr_b16 a[216:217],v3 offset:256
v_mfma_f32_16x16x32_bf16 a[56:59],a[240:243],v[112:115],a[56:59]
v_exp_f32 v78,v78
v_mfma_f32_16x16x32_bf16 a[56:59],a[244:247],v[120:123],a[56:59]
buffer_load_dwordx4 v7,s[48:51],s84 offen lds
v_mfma_f32_16x16x32_bf16 a[60:63],a[248:251],v[112:115],a[60:63]
s_add_u32 s84,s83,20480
ds_read_b64_tr_b16 a[218:219],v3 offset:4352
ds_read_b64_tr_b16 a[220:221],v3 offset:8448
v_mfma_f32_16x16x32_bf16 a[60:63],a[252:255],v[120:123],a[60:63]
v_exp_f32 v79,v79
v_mfma_f32_16x16x32_bf16 v[60:63],v[28:31],v[112:115],v[60:63]
ds_read_b64_tr_b16 a[222:223],v3 offset:12544
v_cvt_pk_bf16_f32 v75,v78,v79
v_mfma_f32_16x16x32_bf16 v[60:63],v[28:31],v[120:123],v[60:63]
ds_read_b64_tr_b16 a[224:225],v2 offset:512
ds_read_b64_tr_b16 a[226:227],v2 offset:4608
v_mfma_f32_16x16x32_bf16 v[96:99],a[128:131],v[192:195],v[40:43]
v_exp_f32 v80,v80
v_mfma_f32_16x16x32_bf16 v[96:99],a[132:135],v[200:203],v[96:99]
ds_read_b64_tr_b16 a[228:229],v2 offset:8704
ds_read_b64_tr_b16 a[230:231],v2 offset:12800
v_mfma_f32_16x16x32_bf16 v[96:99],a[136:139],v[208:211],v[96:99]
v_exp_f32 v81,v81
v_mfma_f32_16x16x32_bf16 v[96:99],a[140:143],v[216:219],v[96:99]
ds_read_b64_tr_b16 a[232:233],v3 offset:512
ds_read_b64_tr_b16 a[234:235],v3 offset:4608
v_mfma_f32_16x16x32_bf16 v[100:103],a[156:159],v[216:219],v[40:43]
ds_read_b64_tr_b16 a[236:237],v3 offset:8704
v_cvt_pk_bf16_f32 v80,v80,v81
v_mfma_f32_16x16x32_bf16 v[100:103],a[152:155],v[208:211],v[100:103]
v_exp_f32 v82,v82
v_mfma_f32_16x16x32_bf16 v[100:103],a[148:151],v[200:203],v[100:103]
ds_read_b64_tr_b16 a[238:239],v3 offset:12800
ds_read_b64_tr_b16 a[240:241],v2 offset:768
v_mfma_f32_16x16x32_bf16 v[100:103],a[144:147],v[192:195],v[100:103]
v_exp_f32 v83,v83
v_mfma_f32_16x16x32_bf16 v[104:107],a[160:163],v[192:195],v[40:43]
ds_read_b64_tr_b16 a[242:243],v2 offset:4864
ds_read_b64_tr_b16 a[244:245],v2 offset:8960
v_mfma_f32_16x16x32_bf16 v[104:107],a[164:167],v[200:203],v[104:107]
buffer_store_dwordx4 a[104:107],v14,s[40:43],s80 offen offset:3072 sc1
v_mfma_f32_16x16x32_bf16 v[104:107],a[168:171],v[208:211],v[104:107]
ds_read_b64_tr_b16 a[246:247],v2 offset:13056
ds_read_b64_tr_b16 a[248:249],v3 offset:768
v_mfma_f32_16x16x32_bf16 v[104:107],a[172:175],v[216:219],v[104:107]
ds_read_b64_tr_b16 a[250:251],v3 offset:4864
v_cvt_pk_bf16_f32 v81,v82,v83
v_mfma_f32_16x16x32_bf16 v[108:111],a[188:191],v[216:219],v[40:43]
ds_read_b64_tr_b16 a[252:253],v3 offset:8960
v_add_u32 v2,s78,v2
v_mfma_f32_16x16x32_bf16 v[108:111],a[184:187],v[208:211],v[108:111]
s_waitcnt lgkmcnt(6)
ds_read_b64_tr_b16 a[254:255],v3 offset:13056
v_add_u32 v3,s78,v3
v_mfma_f32_16x16x32_bf16 v[108:111],a[180:183],v[200:203],v[108:111]
s_sub_u32 s78,s77,32768
v_exp_f32 v84,v84
v_mfma_f32_16x16x32_bf16 v[108:111],a[176:179],v[192:195],v[108:111]
s_and_b32 s78,s78,0x1ffff
v_exp_f32 v85,v85
v_mfma_f32_16x16x32_bf16 a[0:3],a[192:195],v[64:67],a[0:3]
s_sub_u32 s78,s78,s77
v_exp_f32 v86,v86
v_mfma_f32_16x16x32_bf16 a[0:3],a[196:199],v[72:75],a[0:3]
s_add_u32 s77,s77,s78
v_exp_f32 v87,v87
v_mfma_f32_16x16x32_bf16 a[4:7],a[200:203],v[64:67],a[4:7]
v_cvt_pk_bf16_f32 v82,v84,v85
v_cvt_pk_bf16_f32 v83,v86,v87
v_mfma_f32_16x16x32_bf16 a[4:7],a[204:207],v[72:75],a[4:7]
v_exp_f32 v88,v88
v_mfma_f32_16x16x32_bf16 a[8:11],a[208:211],v[64:67],a[8:11]
v_exp_f32 v89,v89
v_mfma_f32_16x16x32_bf16 a[8:11],a[212:215],v[72:75],a[8:11]
v_exp_f32 v90,v90
v_mfma_f32_16x16x32_bf16 a[12:15],a[216:219],v[64:67],a[12:15]
v_exp_f32 v91,v91
v_mfma_f32_16x16x32_bf16 a[12:15],a[220:223],v[72:75],a[12:15]
v_exp_f32 v92,v92
v_mfma_f32_16x16x32_bf16 a[16:19],a[224:227],v[64:67],a[16:19]
v_exp_f32 v93,v93
v_mfma_f32_16x16x32_bf16 a[16:19],a[228:231],v[72:75],a[16:19]
v_exp_f32 v94,v94
v_mfma_f32_16x16x32_bf16 a[20:23],a[232:235],v[64:67],a[20:23]
buffer_store_dwordx4 a[108:111],v6,s[40:43],s80 offen offset:1024 sc1
v_mfma_f32_16x16x32_bf16 a[20:23],a[236:239],v[72:75],a[20:23]
s_waitcnt lgkmcnt(0)
v_exp_f32 v95,v95
v_mfma_f32_16x16x32_bf16 a[24:27],a[240:243],v[64:67],a[24:27]
v_cvt_pk_bf16_f32 v88,v88,v89
v_cvt_pk_bf16_f32 v89,v90,v91
v_mfma_f32_16x16x32_bf16 a[24:27],a[244:247],v[72:75],a[24:27]
v_cvt_pk_bf16_f32 v90,v92,v93
v_cvt_pk_bf16_f32 v91,v94,v95
v_mfma_f32_16x16x32_bf16 a[28:31],a[248:251],v[64:67],a[28:31]
v_exp_f32 v96,v96
v_mfma_f32_16x16x32_bf16 a[28:31],a[252:255],v[72:75],a[28:31]
v_exp_f32 v97,v97
v_mfma_f32_16x16x32_bf16 v[48:51],v[28:31],v[64:67],v[48:51]
v_exp_f32 v98,v98
v_mfma_f32_16x16x32_bf16 v[48:51],v[28:31],v[72:75],v[48:51]
v_exp_f32 v99,v99
s_waitcnt vmcnt(26)
s_barrier
v_mfma_f32_16x16x32_bf16 v[112:115],a[128:131],v[224:227],v[44:47]
ds_read_b128 a[128:131],v1
v_cvt_pk_bf16_f32 v96,v96,v97
v_mfma_f32_16x16x32_bf16 v[112:115],a[132:135],v[232:235],v[112:115]
v_exp_f32 v100,v100
v_mfma_f32_16x16x32_bf16 v[112:115],a[136:139],v[240:243],v[112:115]
ds_read_b128 a[132:135],v1 offset:256
v_cvt_pk_bf16_f32 v97,v98,v99
v_mfma_f32_16x16x32_bf16 v[112:115],a[140:143],v[248:251],v[112:115]
v_exp_f32 v101,v101
v_mfma_f32_16x16x32_bf16 v[116:119],a[156:159],v[248:251],v[44:47]
ds_read_b128 a[136:139],v1 offset:512
v_cvt_pk_bf16_f32 v98,v100,v101
v_mfma_f32_16x16x32_bf16 v[116:119],a[152:155],v[240:243],v[116:119]
s_add_u32 m0,s86,81920
v_exp_f32 v102,v102
v_mfma_f32_16x16x32_bf16 v[116:119],a[148:151],v[232:235],v[116:119]
v_exp_f32 v103,v103
v_mfma_f32_16x16x32_bf16 v[116:119],a[144:147],v[224:227],v[116:119]
buffer_load_dwordx4 v24,s[48:51],s83 offen lds
v_mfma_f32_16x16x32_bf16 v[120:123],a[160:163],v[224:227],v[44:47]
ds_read_b128 a[140:143],v1 offset:768
v_cvt_pk_bf16_f32 v99,v102,v103
v_mfma_f32_16x16x32_bf16 v[120:123],a[164:167],v[232:235],v[120:123]
buffer_store_dwordx4 a[64:67],v6,s[40:43],s79 offen offset:2048 sc1
v_mfma_f32_16x16x32_bf16 v[120:123],a[168:171],v[240:243],v[120:123]
v_exp_f32 v104,v104
v_mfma_f32_16x16x32_bf16 v[120:123],a[172:175],v[248:251],v[120:123]
v_exp_f32 v105,v105
v_mfma_f32_16x16x32_bf16 v[124:127],a[188:191],v[248:251],v[44:47]
ds_read_b128 a[156:159],v1 offset:4864
v_cvt_pk_bf16_f32 v104,v104,v105
v_mfma_f32_16x16x32_bf16 v[124:127],a[184:187],v[240:243],v[124:127]
v_exp_f32 v106,v106
v_mfma_f32_16x16x32_bf16 v[124:127],a[180:183],v[232:235],v[124:127]
v_exp_f32 v107,v107
v_mfma_f32_16x16x32_bf16 v[124:127],a[176:179],v[224:227],v[124:127]
ds_read_b128 a[152:155],v1 offset:4608
v_cvt_pk_bf16_f32 v105,v106,v107
v_mfma_f32_16x16x32_bf16 v[132:135],a[192:195],v[80:83],v[132:135]
v_exp_f32 v108,v108
v_mfma_f32_16x16x32_bf16 v[132:135],a[196:199],v[88:91],v[132:135]
buffer_load_dwordx4 v25,s[48:51],s83 offen offset:1024 lds
v_mfma_f32_16x16x32_bf16 v[140:143],a[200:203],v[80:83],v[140:143]
v_exp_f32 v109,v109
v_mfma_f32_16x16x32_bf16 v[140:143],a[204:207],v[88:91],v[140:143]
ds_read_b128 a[148:151],v1 offset:4352
v_cvt_pk_bf16_f32 v106,v108,v109
v_mfma_f32_16x16x32_bf16 v[148:151],a[208:211],v[80:83],v[148:151]
v_exp_f32 v110,v110
v_mfma_f32_16x16x32_bf16 v[148:151],a[212:215],v[88:91],v[148:151]
v_exp_f32 v111,v111
v_mfma_f32_16x16x32_bf16 v[156:159],a[216:219],v[80:83],v[156:159]
ds_read_b128 a[144:147],v1 offset:4096
v_cvt_pk_bf16_f32 v107,v110,v111
v_mfma_f32_16x16x32_bf16 v[156:159],a[220:223],v[88:91],v[156:159]
v_exp_f32 v112,v112
v_mfma_f32_16x16x32_bf16 v[164:167],a[224:227],v[80:83],v[164:167]
v_exp_f32 v113,v113
v_mfma_f32_16x16x32_bf16 v[164:167],a[228:231],v[88:91],v[164:167]
buffer_store_dwordx4 a[68:71],v6,s[40:43],s79 offen sc1
v_mfma_f32_16x16x32_bf16 v[172:175],a[232:235],v[80:83],v[172:175]
s_add_u32 s79,s79,65536
ds_read_b128 a[160:163],v1 offset:8192
v_cvt_pk_bf16_f32 v112,v112,v113
v_mfma_f32_16x16x32_bf16 v[172:175],a[236:239],v[88:91],v[172:175]
buffer_load_dwordx4 v26,s[48:51],s83 offen offset:2048 lds
v_mfma_f32_16x16x32_bf16 v[180:183],a[240:243],v[80:83],v[180:183]
v_exp_f32 v114,v114
v_mfma_f32_16x16x32_bf16 v[180:183],a[244:247],v[88:91],v[180:183]
v_exp_f32 v115,v115
v_mfma_f32_16x16x32_bf16 v[188:191],a[248:251],v[80:83],v[188:191]
ds_read_b128 a[164:167],v1 offset:8448
v_cvt_pk_bf16_f32 v113,v114,v115
v_mfma_f32_16x16x32_bf16 v[188:191],a[252:255],v[88:91],v[188:191]
v_exp_f32 v116,v116
v_mfma_f32_16x16x32_bf16 v[52:55],v[28:31],v[80:83],v[52:55]
ds_read_b128 a[168:171],v1 offset:8704
v_mfma_f32_16x16x32_bf16 v[52:55],v[28:31],v[88:91],v[52:55]
s_waitcnt lgkmcnt(3)
v_exp_f32 v117,v117
v_mfma_f32_16x16x32_bf16 v[64:67],a[128:131],v[128:131],v[32:35]
ds_read_b128 a[172:175],v1 offset:8960
v_cvt_pk_bf16_f32 v114,v116,v117
v_mfma_f32_16x16x32_bf16 v[64:67],a[132:135],v[136:139],v[64:67]
v_exp_f32 v118,v118
v_mfma_f32_16x16x32_bf16 v[64:67],a[136:139],v[144:147],v[64:67]
ds_read_b128 a[188:191],v1 offset:13056
)A"
R"A(v_mfma_f32_16x16x32_bf16 v[64:67],a[140:143],v[152:155],v[64:67]
buffer_load_dwordx4 v27,s[48:51],s83 offen offset:3072 lds
v_mfma_f32_16x16x32_bf16 v[68:71],a[156:159],v[152:155],v[32:35]
ds_read_b128 a[184:187],v1 offset:12800
v_mfma_f32_16x16x32_bf16 v[68:71],a[152:155],v[144:147],v[68:71]
v_exp_f32 v119,v119
v_mfma_f32_16x16x32_bf16 v[68:71],a[148:151],v[136:139],v[68:71]
ds_read_b128 a[180:183],v1 offset:12544
v_cvt_pk_bf16_f32 v115,v118,v119
v_mfma_f32_16x16x32_bf16 v[68:71],a[144:147],v[128:131],v[68:71]
s_waitcnt lgkmcnt(4)
v_exp_f32 v120,v120
v_mfma_f32_16x16x32_bf16 v[72:75],a[160:163],v[128:131],v[32:35]
buffer_store_dwordx4 a[72:75],v6,s[40:43],s80 offen offset:2048 sc1
v_mfma_f32_16x16x32_bf16 v[72:75],a[164:167],v[136:139],v[72:75]
ds_read_b128 a[176:179],v1 offset:12288
v_add_u32 v1,s78,v1
v_mfma_f32_16x16x32_bf16 v[72:75],a[168:171],v[144:147],v[72:75]
s_waitcnt lgkmcnt(3)
v_exp_f32 v121,v121
v_mfma_f32_16x16x32_bf16 v[72:75],a[172:175],v[152:155],v[72:75]
s_add_u32 m0,s86,86016
v_exp_f32 v122,v122
v_mfma_f32_16x16x32_bf16 v[76:79],a[188:191],v[152:155],v[32:35]
s_waitcnt lgkmcnt(0)
v_exp_f32 v123,v123
v_mfma_f32_16x16x32_bf16 v[76:79],a[184:187],v[144:147],v[76:79]
buffer_load_dwordx4 v7,s[48:51],s83 offen lds
v_mfma_f32_16x16x32_bf16 v[76:79],a[180:183],v[136:139],v[76:79]
s_add_u32 s83,s64,1
v_exp_f32 v124,v124
v_mfma_f32_16x16x32_bf16 v[76:79],a[176:179],v[128:131],v[76:79]
s_and_b32 s83,s83,3
v_exp_f32 v125,v125
v_mfma_f32_16x16x32_bf16 v[196:199],a[192:195],v[96:99],v[196:199]
s_lshl_b32 s83,s83,1
v_exp_f32 v126,v126
v_mfma_f32_16x16x32_bf16 v[196:199],a[196:199],v[104:107],v[196:199]
s_mul_i32 s83,s83,163840
v_exp_f32 v127,v127
v_mfma_f32_16x16x32_bf16 v[204:207],a[200:203],v[96:99],v[204:207]
s_add_u32 s83,s83,s85
v_cvt_pk_bf16_f32 v120,v120,v121
v_cvt_pk_bf16_f32 v121,v122,v123
v_mfma_f32_16x16x32_bf16 v[204:207],a[204:207],v[104:107],v[204:207]
v_cvt_pk_bf16_f32 v122,v124,v125
v_cvt_pk_bf16_f32 v123,v126,v127
v_mfma_f32_16x16x32_bf16 v[212:215],a[208:211],v[96:99],v[212:215]
v_exp_f32 v64,v64
v_mfma_f32_16x16x32_bf16 v[212:215],a[212:215],v[104:107],v[212:215]
s_add_u32 m0,s86,87040
v_exp_f32 v65,v65
v_mfma_f32_16x16x32_bf16 v[220:223],a[216:219],v[96:99],v[220:223]
v_exp_f32 v66,v66
v_mfma_f32_16x16x32_bf16 v[220:223],a[220:223],v[104:107],v[220:223]
buffer_load_dwordx4 v24,s[48:51],s84 offen lds
v_mfma_f32_16x16x32_bf16 v[228:231],a[224:227],v[96:99],v[228:231]
v_exp_f32 v67,v67
v_mfma_f32_16x16x32_bf16 v[228:231],a[228:231],v[104:107],v[228:231]
buffer_store_dwordx4 a[76:79],v6,s[40:43],s80 offen sc1
v_mfma_f32_16x16x32_bf16 v[236:239],a[232:235],v[96:99],v[236:239]
s_add_u32 s80,s80,65536
v_exp_f32 v68,v68
v_mfma_f32_16x16x32_bf16 v[236:239],a[236:239],v[104:107],v[236:239]
v_exp_f32 v69,v69
v_mfma_f32_16x16x32_bf16 v[244:247],a[240:243],v[96:99],v[244:247]
v_exp_f32 v70,v70
v_mfma_f32_16x16x32_bf16 v[244:247],a[244:247],v[104:107],v[244:247]
v_exp_f32 v71,v71
v_mfma_f32_16x16x32_bf16 v[252:255],a[248:251],v[96:99],v[252:255]
v_cvt_pk_bf16_f32 v64,v64,v65
v_cvt_pk_bf16_f32 v65,v66,v67
v_mfma_f32_16x16x32_bf16 v[252:255],a[252:255],v[104:107],v[252:255]
v_cvt_pk_bf16_f32 v66,v68,v69
v_cvt_pk_bf16_f32 v67,v70,v71
v_mfma_f32_16x16x32_bf16 v[56:59],v[28:31],v[96:99],v[56:59]
v_exp_f32 v72,v72
v_mfma_f32_16x16x32_bf16 v[56:59],v[28:31],v[104:107],v[56:59]
buffer_load_dwordx4 v25,s[48:51],s84 offen offset:1024 lds
v_mfma_f32_16x16x32_bf16 v[80:83],a[128:131],v[160:163],v[36:39]
v_exp_f32 v73,v73
v_mfma_f32_16x16x32_bf16 v[80:83],a[132:135],v[168:171],v[80:83]
v_exp_f32 v74,v74
v_mfma_f32_16x16x32_bf16 v[80:83],a[136:139],v[176:179],v[80:83]
v_exp_f32 v75,v75
v_mfma_f32_16x16x32_bf16 v[80:83],a[140:143],v[184:187],v[80:83]
v_exp_f32 v76,v76
v_mfma_f32_16x16x32_bf16 v[84:87],a[156:159],v[184:187],v[36:39]
v_exp_f32 v77,v77
v_mfma_f32_16x16x32_bf16 v[84:87],a[152:155],v[176:179],v[84:87]
v_exp_f32 v78,v78
v_mfma_f32_16x16x32_bf16 v[84:87],a[148:151],v[168:171],v[84:87]
v_exp_f32 v79,v79
v_mfma_f32_16x16x32_bf16 v[84:87],a[144:147],v[160:163],v[84:87]
v_cvt_pk_bf16_f32 v72,v72,v73
v_cvt_pk_bf16_f32 v73,v74,v75
v_mfma_f32_16x16x32_bf16 v[88:91],a[160:163],v[160:163],v[36:39]
buffer_store_dwordx4 a[80:83],v6,s[40:43],s81 offen offset:2048 sc1
v_mfma_f32_16x16x32_bf16 v[88:91],a[164:167],v[168:171],v[88:91]
v_cvt_pk_bf16_f32 v74,v76,v77
v_cvt_pk_bf16_f32 v75,v78,v79
v_mfma_f32_16x16x32_bf16 v[88:91],a[168:171],v[176:179],v[88:91]
buffer_load_dwordx4 v26,s[48:51],s84 offen offset:2048 lds
v_mfma_f32_16x16x32_bf16 v[88:91],a[172:175],v[184:187],v[88:91]
v_exp_f32 v80,v80
v_mfma_f32_16x16x32_bf16 v[92:95],a[188:191],v[184:187],v[36:39]
v_exp_f32 v81,v81
v_mfma_f32_16x16x32_bf16 v[92:95],a[184:187],v[176:179],v[92:95]
v_exp_f32 v82,v82
v_mfma_f32_16x16x32_bf16 v[92:95],a[180:183],v[168:171],v[92:95]
v_exp_f32 v83,v83
v_mfma_f32_16x16x32_bf16 v[92:95],a[176:179],v[160:163],v[92:95]
v_exp_f32 v84,v84
v_mfma_f32_16x16x32_bf16 a[32:35],a[192:195],v[112:115],a[32:35]
ds_read_b64_tr_b16 a[192:193],v2
ds_read_b64_tr_b16 a[194:195],v2 offset:4096
v_mfma_f32_16x16x32_bf16 a[32:35],a[196:199],v[120:123],a[32:35]
ds_read_b64_tr_b16 a[196:197],v2 offset:8192
v_cvt_pk_bf16_f32 v80,v80,v81
v_mfma_f32_16x16x32_bf16 a[36:39],a[200:203],v[112:115],a[36:39]
ds_read_b64_tr_b16 a[198:199],v2 offset:12288
v_cvt_pk_bf16_f32 v81,v82,v83
v_mfma_f32_16x16x32_bf16 a[36:39],a[204:207],v[120:123],a[36:39]
buffer_load_dwordx4 v27,s[48:51],s84 offen offset:3072 lds
v_mfma_f32_16x16x32_bf16 a[40:43],a[208:211],v[112:115],a[40:43]
ds_read_b64_tr_b16 a[200:201],v3
ds_read_b64_tr_b16 a[202:203],v3 offset:4096
v_mfma_f32_16x16x32_bf16 a[40:43],a[212:215],v[120:123],a[40:43]
v_exp_f32 v85,v85
v_mfma_f32_16x16x32_bf16 a[44:47],a[216:219],v[112:115],a[44:47]
ds_read_b64_tr_b16 a[204:205],v3 offset:8192
ds_read_b64_tr_b16 a[206:207],v3 offset:12288
v_mfma_f32_16x16x32_bf16 a[44:47],a[220:223],v[120:123],a[44:47]
ds_read_b64_tr_b16 a[208:209],v2 offset:256
v_cvt_pk_bf16_f32 v82,v84,v85
v_mfma_f32_16x16x32_bf16 a[48:51],a[224:227],v[112:115],a[48:51]
v_exp_f32 v86,v86
v_mfma_f32_16x16x32_bf16 a[48:51],a[228:231],v[120:123],a[48:51]
buffer_store_dwordx4 a[84:87],v6,s[40:43],s81 offen sc1
v_mfma_f32_16x16x32_bf16 a[52:55],a[232:235],v[112:115],a[52:55]
s_add_u32 s81,s81,65536
ds_read_b64_tr_b16 a[210:211],v2 offset:4352
ds_read_b64_tr_b16 a[212:213],v2 offset:8448
v_mfma_f32_16x16x32_bf16 a[52:55],a[236:239],v[120:123],a[52:55]
s_add_u32 m0,s86,91136
v_exp_f32 v87,v87
v_mfma_f32_16x16x32_bf16 a[56:59],a[240:243],v[112:115],a[56:59]
ds_read_b64_tr_b16 a[214:215],v2 offset:12544
ds_read_b64_tr_b16 a[216:217],v3 offset:256
v_mfma_f32_16x16x32_bf16 a[56:59],a[244:247],v[120:123],a[56:59]
buffer_load_dwordx4 v7,s[48:51],s84 offen lds
v_mfma_f32_16x16x32_bf16 a[60:63],a[248:251],v[112:115],a[60:63]
s_add_u32 s84,s83,20480
ds_read_b64_tr_b16 a[218:219],v3 offset:4352
ds_read_b64_tr_b16 a[220:221],v3 offset:8448
v_mfma_f32_16x16x32_bf16 a[60:63],a[252:255],v[120:123],a[60:63]
ds_read_b64_tr_b16 a[222:223],v3 offset:12544
v_cvt_pk_bf16_f32 v83,v86,v87
v_mfma_f32_16x16x32_bf16 v[60:63],v[28:31],v[112:115],v[60:63]
v_exp_f32 v88,v88
v_mfma_f32_16x16x32_bf16 v[60:63],v[28:31],v[120:123],v[60:63]
ds_read_b64_tr_b16 a[224:225],v2 offset:512
ds_read_b64_tr_b16 a[226:227],v2 offset:4608
v_mfma_f32_16x16x32_bf16 v[96:99],a[128:131],v[192:195],v[40:43]
v_exp_f32 v89,v89
v_mfma_f32_16x16x32_bf16 v[96:99],a[132:135],v[200:203],v[96:99]
ds_read_b64_tr_b16 a[228:229],v2 offset:8704
ds_read_b64_tr_b16 a[230:231],v2 offset:12800
v_mfma_f32_16x16x32_bf16 v[96:99],a[136:139],v[208:211],v[96:99]
ds_read_b64_tr_b16 a[232:233],v3 offset:512
v_cvt_pk_bf16_f32 v88,v88,v89
v_mfma_f32_16x16x32_bf16 v[96:99],a[140:143],v[216:219],v[96:99]
v_exp_f32 v90,v90
v_mfma_f32_16x16x32_bf16 v[100:103],a[156:159],v[216:219],v[40:43]
ds_read_b64_tr_b16 a[234:235],v3 offset:4608
ds_read_b64_tr_b16 a[236:237],v3 offset:8704
v_mfma_f32_16x16x32_bf16 v[100:103],a[152:155],v[208:211],v[100:103]
v_exp_f32 v91,v91
v_mfma_f32_16x16x32_bf16 v[100:103],a[148:151],v[200:203],v[100:103]
ds_read_b64_tr_b16 a[238:239],v3 offset:12800
ds_read_b64_tr_b16 a[240:241],v2 offset:768
v_mfma_f32_16x16x32_bf16 v[100:103],a[144:147],v[192:195],v[100:103]
ds_read_b64_tr_b16 a[242:243],v2 offset:4864
v_cvt_pk_bf16_f32 v89,v90,v91
v_mfma_f32_16x16x32_bf16 v[104:107],a[160:163],v[192:195],v[40:43]
buffer_store_dwordx4 a[88:91],v6,s[40:43],s82 offen offset:2048 sc1
v_mfma_f32_16x16x32_bf16 v[104:107],a[164:167],v[200:203],v[104:107]
ds_read_b64_tr_b16 a[244:245],v2 offset:8960
ds_read_b64_tr_b16 a[246:247],v2 offset:13056
v_mfma_f32_16x16x32_bf16 v[104:107],a[168:171],v[208:211],v[104:107]
ds_read_b64_tr_b16 a[248:249],v3 offset:768
v_add_u32 v2,s78,v2
v_mfma_f32_16x16x32_bf16 v[104:107],a[172:175],v[216:219],v[104:107]
v_exp_f32 v92,v92
v_mfma_f32_16x16x32_bf16 v[108:111],a[188:191],v[216:219],v[40:43]
ds_read_b64_tr_b16 a[250:251],v3 offset:4864
ds_read_b64_tr_b16 a[252:253],v3 offset:8960
v_mfma_f32_16x16x32_bf16 v[108:111],a[184:187],v[208:211],v[108:111]
s_waitcnt lgkmcnt(5)
ds_read_b64_tr_b16 a[254:255],v3 offset:13056
v_add_u32 v3,s78,v3
v_mfma_f32_16x16x32_bf16 v[108:111],a[180:183],v[200:203],v[108:111]
s_sub_u32 s78,s77,32768
v_exp_f32 v93,v93
v_mfma_f32_16x16x32_bf16 v[108:111],a[176:179],v[192:195],v[108:111]
s_and_b32 s78,s78,0x1ffff
v_exp_f32 v94,v94
v_mfma_f32_16x16x32_bf16 a[0:3],a[192:195],v[64:67],a[0:3]
s_sub_u32 s78,s78,s77
v_exp_f32 v95,v95
v_mfma_f32_16x16x32_bf16 a[0:3],a[196:199],v[72:75],a[0:3]
s_add_u32 s77,s77,s78
v_cvt_pk_bf16_f32 v90,v92,v93
v_cvt_pk_bf16_f32 v91,v94,v95
v_mfma_f32_16x16x32_bf16 a[4:7],a[200:203],v[64:67],a[4:7]
s_cmp_ge_u32 s63,2
s_cselect_b32 s6,L_gbS_%=-L_pcg_%=,s6
s_cselect_b32 s10,L_geS_%=-L_gbS_%=,s10
v_mfma_f32_16x16x32_bf16 a[4:7],a[204:207],v[72:75],a[4:7]
s_add_u32 s8,s8,s6
s_addc_u32 s9,s9,0
s_and_b32 s8,s8,0xffffff80
v_mfma_f32_16x16x32_bf16 a[8:11],a[208:211],v[64:67],a[8:11]
s_and_b32 s9,s9,0xffff
v_exp_f32 v96,v96
v_mfma_f32_16x16x32_bf16 a[8:11],a[212:215],v[72:75],a[8:11]
v_exp_f32 v97,v97
v_mfma_f32_16x16x32_bf16 a[12:15],a[216:219],v[64:67],a[12:15]
v_exp_f32 v98,v98
v_mfma_f32_16x16x32_bf16 a[12:15],a[220:223],v[72:75],a[12:15]
v_exp_f32 v99,v99
v_mfma_f32_16x16x32_bf16 a[16:19],a[224:227],v[64:67],a[16:19]
v_exp_f32 v100,v100
v_mfma_f32_16x16x32_bf16 a[16:19],a[228:231],v[72:75],a[16:19]
buffer_store_dwordx4 a[92:95],v6,s[40:43],s82 offen sc1
v_mfma_f32_16x16x32_bf16 a[20:23],a[232:235],v[64:67],a[20:23]
s_add_u32 s82,s82,65536
v_exp_f32 v101,v101
v_mfma_f32_16x16x32_bf16 a[20:23],a[236:239],v[72:75],a[20:23]
v_exp_f32 v102,v102
v_mfma_f32_16x16x32_bf16 a[24:27],a[240:243],v[64:67],a[24:27]
s_waitcnt lgkmcnt(0)
v_exp_f32 v103,v103
v_mfma_f32_16x16x32_bf16 a[24:27],a[244:247],v[72:75],a[24:27]
v_cvt_pk_bf16_f32 v96,v96,v97
v_cvt_pk_bf16_f32 v97,v98,v99
v_mfma_f32_16x16x32_bf16 a[28:31],a[248:251],v[64:67],a[28:31]
v_cvt_pk_bf16_f32 v98,v100,v101
v_cvt_pk_bf16_f32 v99,v102,v103
v_mfma_f32_16x16x32_bf16 a[28:31],a[252:255],v[72:75],a[28:31]
v_exp_f32 v104,v104
v_mfma_f32_16x16x32_bf16 v[48:51],v[28:31],v[64:67],v[48:51]
v_exp_f32 v105,v105
v_mfma_f32_16x16x32_bf16 v[48:51],v[28:31],v[72:75],v[48:51]
v_exp_f32 v106,v106
s_waitcnt vmcnt(36)
s_barrier
v_mfma_f32_16x16x32_bf16 v[112:115],a[128:131],v[224:227],v[44:47]
ds_read_b128 a[128:131],v1
v_cvt_pk_bf16_f32 v104,v104,v105
v_mfma_f32_16x16x32_bf16 v[112:115],a[132:135],v[232:235],v[112:115]
v_exp_f32 v107,v107
v_mfma_f32_16x16x32_bf16 v[112:115],a[136:139],v[240:243],v[112:115]
ds_read_b128 a[132:135],v1 offset:256
v_cvt_pk_bf16_f32 v105,v106,v107
v_mfma_f32_16x16x32_bf16 v[112:115],a[140:143],v[248:251],v[112:115]
v_exp_f32 v108,v108
v_mfma_f32_16x16x32_bf16 v[116:119],a[156:159],v[248:251],v[44:47]
v_exp_f32 v109,v109
v_mfma_f32_16x16x32_bf16 v[116:119],a[152:155],v[240:243],v[116:119]
s_add_u32 m0,s86,40960
ds_read_b128 a[136:139],v1 offset:512
v_cvt_pk_bf16_f32 v106,v108,v109
v_mfma_f32_16x16x32_bf16 v[116:119],a[148:151],v[232:235],v[116:119]
v_exp_f32 v110,v110
)A"
R"A(v_mfma_f32_16x16x32_bf16 v[116:119],a[144:147],v[224:227],v[116:119]
buffer_load_dwordx4 v24,s[48:51],s83 offen lds
v_mfma_f32_16x16x32_bf16 v[120:123],a[160:163],v[224:227],v[44:47]
v_exp_f32 v111,v111
v_mfma_f32_16x16x32_bf16 v[120:123],a[164:167],v[232:235],v[120:123]
ds_read_b128 a[140:143],v1 offset:768
v_cvt_pk_bf16_f32 v107,v110,v111
v_mfma_f32_16x16x32_bf16 v[120:123],a[168:171],v[240:243],v[120:123]
v_exp_f32 v112,v112
v_mfma_f32_16x16x32_bf16 v[120:123],a[172:175],v[248:251],v[120:123]
v_exp_f32 v113,v113
v_mfma_f32_16x16x32_bf16 v[124:127],a[188:191],v[248:251],v[44:47]
ds_read_b128 a[156:159],v1 offset:4864
v_cvt_pk_bf16_f32 v112,v112,v113
v_mfma_f32_16x16x32_bf16 v[124:127],a[184:187],v[240:243],v[124:127]
v_exp_f32 v114,v114
v_mfma_f32_16x16x32_bf16 v[124:127],a[180:183],v[232:235],v[124:127]
v_exp_f32 v115,v115
v_mfma_f32_16x16x32_bf16 v[124:127],a[176:179],v[224:227],v[124:127]
ds_read_b128 a[152:155],v1 offset:4608
v_cvt_pk_bf16_f32 v113,v114,v115
v_mfma_f32_16x16x32_bf16 v[132:135],a[192:195],v[80:83],v[132:135]
v_exp_f32 v116,v116
v_mfma_f32_16x16x32_bf16 v[132:135],a[196:199],v[88:91],v[132:135]
buffer_load_dwordx4 v25,s[48:51],s83 offen offset:1024 lds
v_mfma_f32_16x16x32_bf16 v[140:143],a[200:203],v[80:83],v[140:143]
L_pd12_%=:
s_cbranch_execz L_trt1_%=
v_exp_f32 v117,v117
v_mfma_f32_16x16x32_bf16 v[140:143],a[204:207],v[88:91],v[140:143]
ds_read_b128 a[148:151],v1 offset:4352
v_cvt_pk_bf16_f32 v114,v116,v117
v_mfma_f32_16x16x32_bf16 v[148:151],a[208:211],v[80:83],v[148:151]
v_exp_f32 v118,v118
v_mfma_f32_16x16x32_bf16 v[148:151],a[212:215],v[88:91],v[148:151]
v_exp_f32 v119,v119
v_mfma_f32_16x16x32_bf16 v[156:159],a[216:219],v[80:83],v[156:159]
ds_read_b128 a[144:147],v1 offset:4096
v_cvt_pk_bf16_f32 v115,v118,v119
v_mfma_f32_16x16x32_bf16 v[156:159],a[220:223],v[88:91],v[156:159]
v_exp_f32 v120,v120
v_mfma_f32_16x16x32_bf16 v[164:167],a[224:227],v[80:83],v[164:167]
buffer_load_dword v5,v4,s[8:11],s7 offen
v_mfma_f32_16x16x32_bf16 v[164:167],a[228:231],v[88:91],v[164:167]
v_exp_f32 v121,v121
v_mfma_f32_16x16x32_bf16 v[172:175],a[232:235],v[80:83],v[172:175]
ds_read_b128 a[160:163],v1 offset:8192
v_cvt_pk_bf16_f32 v120,v120,v121
v_mfma_f32_16x16x32_bf16 v[172:175],a[236:239],v[88:91],v[172:175]
v_exp_f32 v122,v122
v_mfma_f32_16x16x32_bf16 v[180:183],a[240:243],v[80:83],v[180:183]
v_exp_f32 v123,v123
v_mfma_f32_16x16x32_bf16 v[180:183],a[244:247],v[88:91],v[180:183]
ds_read_b128 a[164:167],v1 offset:8448
v_cvt_pk_bf16_f32 v121,v122,v123
v_mfma_f32_16x16x32_bf16 v[188:191],a[248:251],v[80:83],v[188:191]
buffer_load_dwordx4 v26,s[48:51],s83 offen offset:2048 lds
v_mfma_f32_16x16x32_bf16 v[188:191],a[252:255],v[88:91],v[188:191]
v_exp_f32 v124,v124
v_mfma_f32_16x16x32_bf16 v[52:55],v[28:31],v[80:83],v[52:55]
ds_read_b128 a[168:171],v1 offset:8704
v_mfma_f32_16x16x32_bf16 v[52:55],v[28:31],v[88:91],v[52:55]
s_waitcnt lgkmcnt(3)
v_exp_f32 v125,v125
v_mfma_f32_16x16x32_bf16 v[64:67],a[128:131],v[128:131],v[32:35]
ds_read_b128 a[172:175],v1 offset:8960
v_cvt_pk_bf16_f32 v122,v124,v125
v_mfma_f32_16x16x32_bf16 v[64:67],a[132:135],v[136:139],v[64:67]
v_exp_f32 v126,v126
v_mfma_f32_16x16x32_bf16 v[64:67],a[136:139],v[144:147],v[64:67]
ds_read_b128 a[188:191],v1 offset:13056
v_mfma_f32_16x16x32_bf16 v[64:67],a[140:143],v[152:155],v[64:67]
buffer_load_dwordx4 v27,s[48:51],s83 offen offset:3072 lds
v_mfma_f32_16x16x32_bf16 v[68:71],a[156:159],v[152:155],v[32:35]
ds_read_b128 a[184:187],v1 offset:12800
v_mfma_f32_16x16x32_bf16 v[68:71],a[152:155],v[144:147],v[68:71]
v_exp_f32 v127,v127
v_mfma_f32_16x16x32_bf16 v[68:71],a[148:151],v[136:139],v[68:71]
ds_read_b128 a[180:183],v1 offset:12544
v_cvt_pk_bf16_f32 v123,v126,v127
v_mfma_f32_16x16x32_bf16 v[68:71],a[144:147],v[128:131],v[68:71]
s_waitcnt lgkmcnt(4)
v_exp_f32 v64,v64
v_mfma_f32_16x16x32_bf16 v[72:75],a[160:163],v[128:131],v[32:35]
ds_read_b128 a[176:179],v1 offset:12288
v_add_u32 v1,s78,v1
v_mfma_f32_16x16x32_bf16 v[72:75],a[164:167],v[136:139],v[72:75]
v_exp_f32 v65,v65
v_mfma_f32_16x16x32_bf16 v[72:75],a[168:171],v[144:147],v[72:75]
s_waitcnt lgkmcnt(3)
v_exp_f32 v66,v66
v_mfma_f32_16x16x32_bf16 v[72:75],a[172:175],v[152:155],v[72:75]
s_add_u32 m0,s86,45056
v_exp_f32 v67,v67
v_mfma_f32_16x16x32_bf16 v[76:79],a[188:191],v[152:155],v[32:35]
s_waitcnt lgkmcnt(0)
v_exp_f32 v68,v68
v_mfma_f32_16x16x32_bf16 v[76:79],a[184:187],v[144:147],v[76:79]
buffer_load_dwordx4 v7,s[48:51],s83 offen lds
v_mfma_f32_16x16x32_bf16 v[76:79],a[180:183],v[136:139],v[76:79]
s_add_u32 s83,s64,2
v_exp_f32 v69,v69
v_mfma_f32_16x16x32_bf16 v[76:79],a[176:179],v[128:131],v[76:79]
s_and_b32 s83,s83,3
v_exp_f32 v70,v70
v_mfma_f32_16x16x32_bf16 v[196:199],a[192:195],v[96:99],v[196:199]
s_lshl_b32 s83,s83,1
v_exp_f32 v71,v71
v_mfma_f32_16x16x32_bf16 v[196:199],a[196:199],v[104:107],v[196:199]
s_mul_i32 s83,s83,163840
v_cvt_pk_bf16_f32 v64,v64,v65
v_cvt_pk_bf16_f32 v65,v66,v67
v_mfma_f32_16x16x32_bf16 v[204:207],a[200:203],v[96:99],v[204:207]
s_add_u32 s83,s83,s85
v_cvt_pk_bf16_f32 v66,v68,v69
v_cvt_pk_bf16_f32 v67,v70,v71
v_mfma_f32_16x16x32_bf16 v[204:207],a[204:207],v[104:107],v[204:207]
v_exp_f32 v72,v72
v_mfma_f32_16x16x32_bf16 v[212:215],a[208:211],v[96:99],v[212:215]
v_exp_f32 v73,v73
v_mfma_f32_16x16x32_bf16 v[212:215],a[212:215],v[104:107],v[212:215]
s_add_u32 m0,s86,46080
v_exp_f32 v74,v74
v_mfma_f32_16x16x32_bf16 v[220:223],a[216:219],v[96:99],v[220:223]
v_exp_f32 v75,v75
v_mfma_f32_16x16x32_bf16 v[220:223],a[220:223],v[104:107],v[220:223]
buffer_load_dwordx4 v24,s[48:51],s84 offen lds
v_mfma_f32_16x16x32_bf16 v[228:231],a[224:227],v[96:99],v[228:231]
v_exp_f32 v76,v76
v_mfma_f32_16x16x32_bf16 v[228:231],a[228:231],v[104:107],v[228:231]
v_exp_f32 v77,v77
v_mfma_f32_16x16x32_bf16 v[236:239],a[232:235],v[96:99],v[236:239]
v_exp_f32 v78,v78
v_mfma_f32_16x16x32_bf16 v[236:239],a[236:239],v[104:107],v[236:239]
v_exp_f32 v79,v79
v_mfma_f32_16x16x32_bf16 v[244:247],a[240:243],v[96:99],v[244:247]
v_cvt_pk_bf16_f32 v72,v72,v73
v_cvt_pk_bf16_f32 v73,v74,v75
v_mfma_f32_16x16x32_bf16 v[244:247],a[244:247],v[104:107],v[244:247]
v_cvt_pk_bf16_f32 v74,v76,v77
v_cvt_pk_bf16_f32 v75,v78,v79
v_mfma_f32_16x16x32_bf16 v[252:255],a[248:251],v[96:99],v[252:255]
v_lshrrev_b32 v1,6,v6
v_mul_u32_u24 v1,320,v1
v_mfma_f32_16x16x32_bf16 v[252:255],a[252:255],v[104:107],v[252:255]
v_mfma_f32_16x16x32_bf16 v[56:59],v[28:31],v[96:99],v[56:59]
v_mfma_f32_16x16x32_bf16 v[56:59],v[28:31],v[104:107],v[56:59]
buffer_load_dwordx4 v25,s[48:51],s84 offen offset:1024 lds
v_mfma_f32_16x16x32_bf16 v[80:83],a[128:131],v[160:163],v[36:39]
v_mfma_f32_16x16x32_bf16 v[80:83],a[132:135],v[168:171],v[80:83]
v_mfma_f32_16x16x32_bf16 v[80:83],a[136:139],v[176:179],v[80:83]
v_mfma_f32_16x16x32_bf16 v[80:83],a[140:143],v[184:187],v[80:83]
v_mfma_f32_16x16x32_bf16 v[84:87],a[156:159],v[184:187],v[36:39]
v_mfma_f32_16x16x32_bf16 v[84:87],a[152:155],v[176:179],v[84:87]
v_mfma_f32_16x16x32_bf16 v[84:87],a[148:151],v[168:171],v[84:87]
v_mfma_f32_16x16x32_bf16 v[84:87],a[144:147],v[160:163],v[84:87]
v_mfma_f32_16x16x32_bf16 v[88:91],a[160:163],v[160:163],v[36:39]
v_mfma_f32_16x16x32_bf16 v[88:91],a[164:167],v[168:171],v[88:91]
buffer_load_dwordx4 v26,s[48:51],s84 offen offset:2048 lds
v_mfma_f32_16x16x32_bf16 v[88:91],a[168:171],v[176:179],v[88:91]
v_exp_f32 v80,v80
v_mfma_f32_16x16x32_bf16 v[88:91],a[172:175],v[184:187],v[88:91]
v_exp_f32 v81,v81
v_mfma_f32_16x16x32_bf16 v[92:95],a[188:191],v[184:187],v[36:39]
v_exp_f32 v82,v82
v_mfma_f32_16x16x32_bf16 v[92:95],a[184:187],v[176:179],v[92:95]
v_exp_f32 v83,v83
v_mfma_f32_16x16x32_bf16 v[92:95],a[180:183],v[168:171],v[92:95]
v_exp_f32 v84,v84
v_mfma_f32_16x16x32_bf16 v[92:95],a[176:179],v[160:163],v[92:95]
v_exp_f32 v85,v85
v_mfma_f32_16x16x32_bf16 a[32:35],a[192:195],v[112:115],a[32:35]
ds_read_b64_tr_b16 a[192:193],v2
ds_read_b64_tr_b16 a[194:195],v2 offset:4096
v_mfma_f32_16x16x32_bf16 a[32:35],a[196:199],v[120:123],a[32:35]
ds_read_b64_tr_b16 a[196:197],v2 offset:8192
v_cvt_pk_bf16_f32 v80,v80,v81
v_mfma_f32_16x16x32_bf16 a[36:39],a[200:203],v[112:115],a[36:39]
ds_read_b64_tr_b16 a[198:199],v2 offset:12288
v_cvt_pk_bf16_f32 v81,v82,v83
v_mfma_f32_16x16x32_bf16 a[36:39],a[204:207],v[120:123],a[36:39]
buffer_load_dwordx4 v27,s[48:51],s84 offen offset:3072 lds
v_mfma_f32_16x16x32_bf16 a[40:43],a[208:211],v[112:115],a[40:43]
ds_read_b64_tr_b16 a[200:201],v3
ds_read_b64_tr_b16 a[202:203],v3 offset:4096
v_mfma_f32_16x16x32_bf16 a[40:43],a[212:215],v[120:123],a[40:43]
ds_read_b64_tr_b16 a[204:205],v3 offset:8192
v_cvt_pk_bf16_f32 v82,v84,v85
v_mfma_f32_16x16x32_bf16 a[44:47],a[216:219],v[112:115],a[44:47]
v_exp_f32 v86,v86
v_mfma_f32_16x16x32_bf16 a[44:47],a[220:223],v[120:123],a[44:47]
ds_read_b64_tr_b16 a[206:207],v3 offset:12288
ds_read_b64_tr_b16 a[208:209],v2 offset:256
v_mfma_f32_16x16x32_bf16 a[48:51],a[224:227],v[112:115],a[48:51]
v_exp_f32 v87,v87
v_mfma_f32_16x16x32_bf16 a[48:51],a[228:231],v[120:123],a[48:51]
ds_read_b64_tr_b16 a[210:211],v2 offset:4352
ds_read_b64_tr_b16 a[212:213],v2 offset:8448
v_mfma_f32_16x16x32_bf16 a[52:55],a[232:235],v[112:115],a[52:55]
ds_read_b64_tr_b16 a[214:215],v2 offset:12544
v_cvt_pk_bf16_f32 v83,v86,v87
v_mfma_f32_16x16x32_bf16 a[52:55],a[236:239],v[120:123],a[52:55]
s_add_u32 m0,s86,50176
v_exp_f32 v88,v88
v_mfma_f32_16x16x32_bf16 a[56:59],a[240:243],v[112:115],a[56:59]
ds_read_b64_tr_b16 a[216:217],v3 offset:256
ds_read_b64_tr_b16 a[218:219],v3 offset:4352
v_mfma_f32_16x16x32_bf16 a[56:59],a[244:247],v[120:123],a[56:59]
buffer_load_dwordx4 v7,s[48:51],s84 offen lds
v_mfma_f32_16x16x32_bf16 a[60:63],a[248:251],v[112:115],a[60:63]
s_add_u32 s84,s83,20480
ds_read_b64_tr_b16 a[220:221],v3 offset:8448
ds_read_b64_tr_b16 a[222:223],v3 offset:12544
v_mfma_f32_16x16x32_bf16 a[60:63],a[252:255],v[120:123],a[60:63]
v_exp_f32 v89,v89
v_mfma_f32_16x16x32_bf16 v[60:63],v[28:31],v[112:115],v[60:63]
ds_read_b64_tr_b16 a[224:225],v2 offset:512
ds_read_b64_tr_b16 a[226:227],v2 offset:4608
v_mfma_f32_16x16x32_bf16 v[60:63],v[28:31],v[120:123],v[60:63]
ds_read_b64_tr_b16 a[228:229],v2 offset:8704
v_cvt_pk_bf16_f32 v88,v88,v89
v_mfma_f32_16x16x32_bf16 v[96:99],a[128:131],v[192:195],v[40:43]
v_exp_f32 v90,v90
v_mfma_f32_16x16x32_bf16 v[96:99],a[132:135],v[200:203],v[96:99]
ds_read_b64_tr_b16 a[230:231],v2 offset:12800
ds_read_b64_tr_b16 a[232:233],v3 offset:512
v_mfma_f32_16x16x32_bf16 v[96:99],a[136:139],v[208:211],v[96:99]
v_exp_f32 v91,v91
v_mfma_f32_16x16x32_bf16 v[96:99],a[140:143],v[216:219],v[96:99]
ds_read_b64_tr_b16 a[234:235],v3 offset:4608
ds_read_b64_tr_b16 a[236:237],v3 offset:8704
v_mfma_f32_16x16x32_bf16 v[100:103],a[156:159],v[216:219],v[40:43]
ds_read_b64_tr_b16 a[238:239],v3 offset:12800
v_cvt_pk_bf16_f32 v89,v90,v91
v_mfma_f32_16x16x32_bf16 v[100:103],a[152:155],v[208:211],v[100:103]
v_exp_f32 v92,v92
v_mfma_f32_16x16x32_bf16 v[100:103],a[148:151],v[200:203],v[100:103]
ds_read_b64_tr_b16 a[240:241],v2 offset:768
ds_read_b64_tr_b16 a[242:243],v2 offset:4864
v_mfma_f32_16x16x32_bf16 v[100:103],a[144:147],v[192:195],v[100:103]
v_exp_f32 v93,v93
v_mfma_f32_16x16x32_bf16 v[104:107],a[160:163],v[192:195],v[40:43]
ds_read_b64_tr_b16 a[244:245],v2 offset:8960
ds_read_b64_tr_b16 a[246:247],v2 offset:13056
v_mfma_f32_16x16x32_bf16 v[104:107],a[164:167],v[200:203],v[104:107]
ds_read_b64_tr_b16 a[248:249],v3 offset:768
v_cvt_pk_bf16_f32 v90,v92,v93
v_mfma_f32_16x16x32_bf16 v[104:107],a[168:171],v[208:211],v[104:107]
ds_read_b64_tr_b16 a[250:251],v3 offset:4864
v_bfe_u32 v2,v6,4,2
v_mfma_f32_16x16x32_bf16 v[104:107],a[172:175],v[216:219],v[104:107]
ds_read_b64_tr_b16 a[252:253],v3 offset:8960
v_lshl_add_u32 v1,v2,3,v1
v_mfma_f32_16x16x32_bf16 v[108:111],a[188:191],v[216:219],v[40:43]
ds_read_b64_tr_b16 a[254:255],v3 offset:13056
v_add_u32 v1,122880,v1
v_mfma_f32_16x16x32_bf16 v[108:111],a[184:187],v[208:211],v[108:111]
v_exp_f32 v94,v94
v_mfma_f32_16x16x32_bf16 v[108:111],a[180:183],v[200:203],v[108:111]
v_exp_f32 v95,v95
v_mfma_f32_16x16x32_bf16 v[108:111],a[176:179],v[192:195],v[108:111]
s_waitcnt lgkmcnt(6)
)A"
R"A(v_cvt_pk_bf16_f32 v91,v94,v95
v_bfe_u32 v2,v6,8,1
v_mfma_f32_16x16x32_bf16 a[0:3],a[192:195],v[64:67],a[0:3]
v_exp_f32 v96,v96
v_mfma_f32_16x16x32_bf16 a[0:3],a[196:199],v[72:75],a[0:3]
v_exp_f32 v97,v97
v_mfma_f32_16x16x32_bf16 a[4:7],a[200:203],v[64:67],a[4:7]
v_exp_f32 v98,v98
v_mfma_f32_16x16x32_bf16 a[4:7],a[204:207],v[72:75],a[4:7]
v_exp_f32 v99,v99
v_mfma_f32_16x16x32_bf16 a[8:11],a[208:211],v[64:67],a[8:11]
v_exp_f32 v100,v100
v_mfma_f32_16x16x32_bf16 a[8:11],a[212:215],v[72:75],a[8:11]
v_exp_f32 v101,v101
v_mfma_f32_16x16x32_bf16 a[12:15],a[216:219],v[64:67],a[12:15]
v_exp_f32 v102,v102
v_mfma_f32_16x16x32_bf16 a[12:15],a[220:223],v[72:75],a[12:15]
v_exp_f32 v103,v103
v_mfma_f32_16x16x32_bf16 a[16:19],a[224:227],v[64:67],a[16:19]
v_cvt_pk_bf16_f32 v96,v96,v97
v_cvt_pk_bf16_f32 v97,v98,v99
v_mfma_f32_16x16x32_bf16 a[16:19],a[228:231],v[72:75],a[16:19]
v_cvt_pk_bf16_f32 v98,v100,v101
v_cvt_pk_bf16_f32 v99,v102,v103
v_mfma_f32_16x16x32_bf16 a[20:23],a[232:235],v[64:67],a[20:23]
v_exp_f32 v104,v104
v_mfma_f32_16x16x32_bf16 a[20:23],a[236:239],v[72:75],a[20:23]
v_exp_f32 v105,v105
v_mfma_f32_16x16x32_bf16 a[24:27],a[240:243],v[64:67],a[24:27]
s_waitcnt lgkmcnt(0)
v_exp_f32 v106,v106
v_mfma_f32_16x16x32_bf16 a[24:27],a[244:247],v[72:75],a[24:27]
v_exp_f32 v107,v107
v_mfma_f32_16x16x32_bf16 a[28:31],a[248:251],v[64:67],a[28:31]
v_exp_f32 v108,v108
v_mfma_f32_16x16x32_bf16 a[28:31],a[252:255],v[72:75],a[28:31]
v_exp_f32 v109,v109
v_mfma_f32_16x16x32_bf16 v[48:51],v[28:31],v[64:67],v[48:51]
v_exp_f32 v110,v110
v_mfma_f32_16x16x32_bf16 v[48:51],v[28:31],v[72:75],v[48:51]
v_exp_f32 v111,v111
s_waitcnt vmcnt(11)
s_barrier
v_mfma_f32_16x16x32_bf16 v[112:115],a[128:131],v[224:227],v[44:47]
buffer_store_dwordx2 v[8:9],v10,s[44:47],s18 offen offset:256 sc1
v_mfma_f32_16x16x32_bf16 v[112:115],a[132:135],v[232:235],v[112:115]
v_cvt_pk_bf16_f32 v104,v104,v105
v_cvt_pk_bf16_f32 v105,v106,v107
v_mfma_f32_16x16x32_bf16 v[112:115],a[136:139],v[240:243],v[112:115]
v_cvt_pk_bf16_f32 v106,v108,v109
v_cvt_pk_bf16_f32 v107,v110,v111
v_mfma_f32_16x16x32_bf16 v[112:115],a[140:143],v[248:251],v[112:115]
s_add_u32 m0,s86,0
v_cmp_nlt_f32 s[54:55],v48,s73
v_max_f32 v49,0x00800000,v48
v_mfma_f32_16x16x32_bf16 v[116:119],a[156:159],v[248:251],v[44:47]
v_rcp_f32 v49,v49
v_mfma_f32_16x16x32_bf16 v[116:119],a[152:155],v[240:243],v[116:119]
v_lshl_add_u32 v1,v2,5,v1
v_lshlrev_b32 v2,6,v2
v_mfma_f32_16x16x32_bf16 v[116:119],a[148:151],v[232:235],v[116:119]
buffer_load_dwordx4 v24,s[48:51],s83 offen lds
v_mfma_f32_16x16x32_bf16 v[116:119],a[144:147],v[224:227],v[116:119]
v_exp_f32 v112,v112
v_mfma_f32_16x16x32_bf16 v[120:123],a[160:163],v[224:227],v[44:47]
v_exp_f32 v113,v113
v_mfma_f32_16x16x32_bf16 v[120:123],a[164:167],v[232:235],v[120:123]
v_exp_f32 v114,v114
v_mfma_f32_16x16x32_bf16 v[120:123],a[168:171],v[240:243],v[120:123]
v_exp_f32 v115,v115
v_mfma_f32_16x16x32_bf16 v[120:123],a[172:175],v[248:251],v[120:123]
v_exp_f32 v116,v116
v_mfma_f32_16x16x32_bf16 v[124:127],a[188:191],v[248:251],v[44:47]
buffer_load_dwordx4 v25,s[48:51],s83 offen offset:1024 lds
v_mfma_f32_16x16x32_bf16 v[124:127],a[184:187],v[240:243],v[124:127]
v_exp_f32 v117,v117
v_mfma_f32_16x16x32_bf16 v[124:127],a[180:183],v[232:235],v[124:127]
v_exp_f32 v118,v118
v_mfma_f32_16x16x32_bf16 v[124:127],a[176:179],v[224:227],v[124:127]
v_exp_f32 v119,v119
v_mfma_f32_16x16x32_bf16 v[52:55],v[28:31],v[80:83],v[52:55]
v_cvt_pk_bf16_f32 v112,v112,v113
v_cvt_pk_bf16_f32 v113,v114,v115
v_mfma_f32_16x16x32_bf16 v[52:55],v[28:31],v[88:91],v[52:55]
v_cvt_pk_bf16_f32 v114,v116,v117
v_cvt_pk_bf16_f32 v115,v118,v119
v_mfma_f32_16x16x32_bf16 v[132:135],a[192:195],v[80:83],v[132:135]
buffer_load_dwordx4 v26,s[48:51],s83 offen offset:2048 lds
v_mfma_f32_16x16x32_bf16 v[132:135],a[196:199],v[88:91],v[132:135]
v_exp_f32 v120,v120
v_mfma_f32_16x16x32_bf16 v[140:143],a[200:203],v[80:83],v[140:143]
v_exp_f32 v121,v121
v_mfma_f32_16x16x32_bf16 v[140:143],a[204:207],v[88:91],v[140:143]
v_exp_f32 v122,v122
v_mfma_f32_16x16x32_bf16 v[148:151],a[208:211],v[80:83],v[148:151]
v_exp_f32 v123,v123
v_mfma_f32_16x16x32_bf16 v[148:151],a[212:215],v[88:91],v[148:151]
v_exp_f32 v124,v124
v_mfma_f32_16x16x32_bf16 v[156:159],a[216:219],v[80:83],v[156:159]
buffer_load_dwordx4 v27,s[48:51],s83 offen offset:3072 lds
v_mfma_f32_16x16x32_bf16 v[156:159],a[220:223],v[88:91],v[156:159]
v_exp_f32 v125,v125
v_mfma_f32_16x16x32_bf16 v[164:167],a[224:227],v[80:83],v[164:167]
v_exp_f32 v126,v126
v_mfma_f32_16x16x32_bf16 v[164:167],a[228:231],v[88:91],v[164:167]
s_add_u32 m0,s86,4096
v_exp_f32 v127,v127
v_mfma_f32_16x16x32_bf16 v[172:175],a[232:235],v[80:83],v[172:175]
v_cvt_pk_bf16_f32 v120,v120,v121
v_cvt_pk_bf16_f32 v121,v122,v123
v_mfma_f32_16x16x32_bf16 v[172:175],a[236:239],v[88:91],v[172:175]
v_cvt_pk_bf16_f32 v122,v124,v125
v_cvt_pk_bf16_f32 v123,v126,v127
v_mfma_f32_16x16x32_bf16 v[180:183],a[240:243],v[80:83],v[180:183]
buffer_load_dwordx4 v7,s[48:51],s83 offen lds
v_mfma_f32_16x16x32_bf16 v[180:183],a[244:247],v[88:91],v[180:183]
v_cmp_nlt_f32 s[56:57],v52,s73
v_sub_u32 v2,v1,v2
v_mfma_f32_16x16x32_bf16 v[188:191],a[248:251],v[80:83],v[188:191]
s_or_b64 s[54:55],s[54:55],s[56:57]
v_accvgpr_read_b32 v8,a0
v_accvgpr_read_b32 v9,a1
v_mfma_f32_16x16x32_bf16 v[188:191],a[252:255],v[88:91],v[188:191]
s_add_u32 m0,s86,5120
v_accvgpr_read_b32 v10,a2
v_accvgpr_read_b32 v11,a3
v_mfma_f32_16x16x32_bf16 v[56:59],v[28:31],v[96:99],v[56:59]
v_accvgpr_read_b32 v12,a4
v_accvgpr_read_b32 v13,a5
v_mfma_f32_16x16x32_bf16 v[56:59],v[28:31],v[104:107],v[56:59]
v_accvgpr_read_b32 v14,a6
v_accvgpr_read_b32 v15,a7
v_mfma_f32_16x16x32_bf16 v[196:199],a[192:195],v[96:99],v[196:199]
buffer_load_dwordx4 v24,s[48:51],s84 offen lds
v_mfma_f32_16x16x32_bf16 v[196:199],a[196:199],v[104:107],v[196:199]
v_mul_f32 v8,v8,v49
v_mul_f32 v9,v9,v49
v_mfma_f32_16x16x32_bf16 v[204:207],a[200:203],v[96:99],v[204:207]
v_cmp_nlt_f32 s[56:57],v56,s73
v_mul_f32 v10,v10,v49
v_mfma_f32_16x16x32_bf16 v[204:207],a[204:207],v[104:107],v[204:207]
s_or_b64 s[54:55],s[54:55],s[56:57]
v_mul_f32 v11,v11,v49
v_mul_f32 v12,v12,v49
v_mfma_f32_16x16x32_bf16 v[212:215],a[208:211],v[96:99],v[212:215]
v_mul_f32 v13,v13,v49
v_mul_f32 v14,v14,v49
v_mfma_f32_16x16x32_bf16 v[212:215],a[212:215],v[104:107],v[212:215]
v_mul_f32 v15,v15,v49
v_cvt_pk_bf16_f32 v32,v8,v9
v_mfma_f32_16x16x32_bf16 v[220:223],a[216:219],v[96:99],v[220:223]
buffer_load_dwordx4 v25,s[48:51],s84 offen offset:1024 lds
v_mfma_f32_16x16x32_bf16 v[220:223],a[220:223],v[104:107],v[220:223]
v_cvt_pk_bf16_f32 v33,v10,v11
v_cvt_pk_bf16_f32 v34,v12,v13
v_mfma_f32_16x16x32_bf16 v[228:231],a[224:227],v[96:99],v[228:231]
v_cvt_pk_bf16_f32 v35,v14,v15
v_accvgpr_read_b32 v16,a8
v_mfma_f32_16x16x32_bf16 v[228:231],a[228:231],v[104:107],v[228:231]
v_accvgpr_read_b32 v17,a9
v_accvgpr_read_b32 v18,a10
v_mfma_f32_16x16x32_bf16 v[236:239],a[232:235],v[96:99],v[236:239]
v_accvgpr_read_b32 v19,a11
v_accvgpr_read_b32 v20,a12
v_mfma_f32_16x16x32_bf16 v[236:239],a[236:239],v[104:107],v[236:239]
v_accvgpr_read_b32 v21,a13
v_accvgpr_read_b32 v22,a14
v_mfma_f32_16x16x32_bf16 v[244:247],a[240:243],v[96:99],v[244:247]
buffer_load_dwordx4 v26,s[48:51],s84 offen offset:2048 lds
v_mfma_f32_16x16x32_bf16 v[244:247],a[244:247],v[104:107],v[244:247]
v_accvgpr_read_b32 v23,a15
v_mul_f32 v16,v16,v49
v_mfma_f32_16x16x32_bf16 v[252:255],a[248:251],v[96:99],v[252:255]
v_mul_f32 v17,v17,v49
v_mul_f32 v18,v18,v49
v_mfma_f32_16x16x32_bf16 v[252:255],a[252:255],v[104:107],v[252:255]
v_mul_f32 v19,v19,v49
v_mul_f32 v20,v20,v49
v_mfma_f32_16x16x32_bf16 v[60:63],v[28:31],v[112:115],v[60:63]
v_mul_f32 v21,v21,v49
v_mul_f32 v22,v22,v49
v_mfma_f32_16x16x32_bf16 v[60:63],v[28:31],v[120:123],v[60:63]
v_mul_f32 v23,v23,v49
v_cvt_pk_bf16_f32 v36,v16,v17
v_mfma_f32_16x16x32_bf16 a[32:35],a[192:195],v[112:115],a[32:35]
buffer_load_dwordx4 v27,s[48:51],s84 offen offset:3072 lds
v_mfma_f32_16x16x32_bf16 a[32:35],a[196:199],v[120:123],a[32:35]
v_cvt_pk_bf16_f32 v37,v18,v19
v_cvt_pk_bf16_f32 v38,v20,v21
v_mfma_f32_16x16x32_bf16 a[36:39],a[200:203],v[112:115],a[36:39]
v_cmp_nlt_f32 s[56:57],v60,s73
v_cvt_pk_bf16_f32 v39,v22,v23
v_mfma_f32_16x16x32_bf16 a[36:39],a[204:207],v[120:123],a[36:39]
s_add_u32 m0,s86,9216
v_accvgpr_read_b32 v8,a16
v_accvgpr_read_b32 v9,a17
v_mfma_f32_16x16x32_bf16 a[40:43],a[208:211],v[112:115],a[40:43]
s_or_b64 s[54:55],s[54:55],s[56:57]
v_accvgpr_read_b32 v10,a18
v_accvgpr_read_b32 v11,a19
v_mfma_f32_16x16x32_bf16 a[40:43],a[212:215],v[120:123],a[40:43]
v_accvgpr_read_b32 v12,a20
v_accvgpr_read_b32 v13,a21
v_mfma_f32_16x16x32_bf16 a[44:47],a[216:219],v[112:115],a[44:47]
buffer_load_dwordx4 v7,s[48:51],s84 offen lds
v_mfma_f32_16x16x32_bf16 a[44:47],a[220:223],v[120:123],a[44:47]
v_accvgpr_read_b32 v14,a22
v_accvgpr_read_b32 v15,a23
v_mfma_f32_16x16x32_bf16 a[48:51],a[224:227],v[112:115],a[48:51]
v_mul_f32 v8,v8,v49
v_mul_f32 v9,v9,v49
v_mfma_f32_16x16x32_bf16 a[48:51],a[228:231],v[120:123],a[48:51]
v_mul_f32 v10,v10,v49
v_mul_f32 v11,v11,v49
v_mfma_f32_16x16x32_bf16 a[52:55],a[232:235],v[112:115],a[52:55]
v_mul_f32 v12,v12,v49
v_mul_f32 v13,v13,v49
v_mfma_f32_16x16x32_bf16 a[52:55],a[236:239],v[120:123],a[52:55]
v_mul_f32 v14,v14,v49
v_mul_f32 v15,v15,v49
v_mfma_f32_16x16x32_bf16 a[56:59],a[240:243],v[112:115],a[56:59]
v_cvt_pk_bf16_f32 v40,v8,v9
v_cvt_pk_bf16_f32 v41,v10,v11
v_mfma_f32_16x16x32_bf16 a[56:59],a[244:247],v[120:123],a[56:59]
v_cvt_pk_bf16_f32 v42,v12,v13
v_cvt_pk_bf16_f32 v43,v14,v15
v_mfma_f32_16x16x32_bf16 a[60:63],a[248:251],v[112:115],a[60:63]
ds_read_b64_tr_b16 v[64:65],v1
ds_read_b64_tr_b16 v[66:67],v1 offset:5120
v_mfma_f32_16x16x32_bf16 a[60:63],a[252:255],v[120:123],a[60:63]
ds_read_b64_tr_b16 v[68:69],v1 offset:10240
v_max_f32 v53,0x00800000,v52
v_rcp_f32 v53,v53
v_max_f32 v57,0x00800000,v56
v_rcp_f32 v57,v57
v_max_f32 v61,0x00800000,v60
v_rcp_f32 v61,v61
ds_read_b64_tr_b16 v[70:71],v1 offset:15360
ds_read_b64_tr_b16 v[72:73],v1 offset:20480
ds_read_b64_tr_b16 v[74:75],v1 offset:25600
ds_read_b64_tr_b16 v[76:77],v1 offset:30720
ds_read_b64_tr_b16 v[78:79],v1 offset:35840
ds_read_b64_tr_b16 v[80:81],v2 offset:32
ds_read_b64_tr_b16 v[82:83],v2 offset:5152
ds_read_b64_tr_b16 v[84:85],v2 offset:10272
ds_read_b64_tr_b16 v[86:87],v2 offset:15392
ds_read_b64_tr_b16 v[88:89],v2 offset:20512
ds_read_b64_tr_b16 v[90:91],v2 offset:25632
ds_read_b64_tr_b16 v[92:93],v2 offset:30752
ds_read_b64_tr_b16 v[94:95],v2 offset:35872
s_cmp_lg_u64 s[54:55],0
s_cbranch_scc1 L_slow1_%=
L_ok1_%=:
s_cmp_ge_u32 s63,2
s_cbranch_scc1 L_GS_%=
L_gbF_%=:
s_waitcnt lgkmcnt(14)
v_mfma_f32_16x16x32_bf16 a[192:195],v[64:67],a[68:71],0
buffer_store_dwordx4 v[32:35],v6,s[40:43],s79 offen sc1
s_waitcnt lgkmcnt(12)
v_mfma_f32_16x16x32_bf16 a[192:195],v[68:71],a[76:79],a[192:195]
s_lshl_b32 s91,s63,8
v_bfe_u32 v3,v6,4,2
v_min_u32 v3,2,v3
s_waitcnt lgkmcnt(10)
v_mfma_f32_16x16x32_bf16 a[192:195],v[72:75],a[84:87],a[192:195]
s_add_u32 s87,s64,1
v_add3_u32 v3,v3,s64,1
v_and_b32 v3,3,v3
s_waitcnt lgkmcnt(8)
v_mfma_f32_16x16x32_bf16 a[192:195],v[76:79],a[92:95],a[192:195]
s_and_b32 s87,s87,3
ds_read_b64_tr_b16 v[96:97],v1 offset:64
v_lshlrev_b32 v3,6,v3
v_mfma_f32_16x16x32_bf16 a[196:199],v[76:79],a[124:127],0
s_lshl_b32 s87,s87,1
ds_read_b64_tr_b16 v[98:99],v1 offset:5184
ds_read_b64_tr_b16 v[100:101],v1 offset:10304
v_mfma_f32_16x16x32_bf16 a[196:199],v[72:75],a[116:119],a[196:199]
s_lshl_b32 s87,s87,16
ds_read_b64_tr_b16 v[102:103],v1 offset:15424
v_lshlrev_b32 v29,10,v6
v_mfma_f32_16x16x32_bf16 a[196:199],v[68:71],a[108:111],a[196:199]
s_add_u32 s88,s87,4096
ds_read_b64_tr_b16 v[104:105],v1 offset:20544
v_accvgpr_read_b32 v8,a24
v_mfma_f32_16x16x32_bf16 a[196:199],v[64:67],a[100:103],a[196:199]
s_add_u32 s89,s87,8192
ds_read_b64_tr_b16 v[106:107],v1 offset:25664
v_accvgpr_read_b32 v9,a25
v_mfma_f32_16x16x32_bf16 a[200:203],v[64:67],a[64:67],0
s_add_u32 s90,s87,12288
ds_read_b64_tr_b16 v[108:109],v1 offset:30784
ds_read_b64_tr_b16 v[110:111],v1 offset:35904
v_mfma_f32_16x16x32_bf16 a[200:203],v[68:71],a[72:75],a[200:203]
)A"
R"A(s_add_u32 m0,s86,122880
ds_read_b64_tr_b16 v[112:113],v2 offset:96
v_accvgpr_read_b32 v10,a26
v_mfma_f32_16x16x32_bf16 a[200:203],v[72:75],a[80:83],a[200:203]
s_add_u32 s83,s64,3
ds_read_b64_tr_b16 v[114:115],v2 offset:5216
v_accvgpr_read_b32 v11,a27
v_mfma_f32_16x16x32_bf16 a[200:203],v[76:79],a[88:91],a[200:203]
s_and_b32 s83,s83,3
ds_read_b64_tr_b16 v[116:117],v2 offset:10336
v_accvgpr_read_b32 v12,a28
v_mfma_f32_16x16x32_bf16 a[204:207],v[76:79],a[120:123],0
s_lshl_b32 s83,s83,1
ds_read_b64_tr_b16 v[118:119],v2 offset:15456
ds_read_b64_tr_b16 v[120:121],v2 offset:20576
v_mfma_f32_16x16x32_bf16 a[204:207],v[72:75],a[112:115],a[204:207]
s_mul_i32 s83,s83,163840
ds_read_b64_tr_b16 v[122:123],v2 offset:25696
v_accvgpr_read_b32 v13,a29
v_mfma_f32_16x16x32_bf16 a[204:207],v[68:71],a[104:107],a[204:207]
s_add_u32 s83,s83,s85
ds_read_b64_tr_b16 v[124:125],v2 offset:30816
v_accvgpr_read_b32 v14,a30
v_mfma_f32_16x16x32_bf16 a[204:207],v[64:67],a[96:99],a[204:207]
buffer_store_dwordx4 v[36:39],v6,s[40:43],s80 offen sc1
s_waitcnt lgkmcnt(9)
v_mfma_f32_16x16x32_bf16 a[208:211],v[80:83],a[68:71],0
s_add_u32 s84,s83,20480
ds_read_b64_tr_b16 v[126:127],v2 offset:35936
ds_read_b64_tr_b16 v[64:65],v1 offset:128
v_mfma_f32_16x16x32_bf16 a[208:211],v[84:87],a[76:79],a[208:211]
s_lshl_b32 s66,s61,3
ds_read_b64_tr_b16 v[66:67],v1 offset:5248
v_accvgpr_read_b32 v15,a31
v_mfma_f32_16x16x32_bf16 a[208:211],v[88:91],a[84:87],a[208:211]
s_add_u32 s66,s66,s64
ds_read_b64_tr_b16 v[68:69],v1 offset:10368
v_mul_f32 v8,v8,v49
v_mfma_f32_16x16x32_bf16 a[208:211],v[92:95],a[92:95],a[208:211]
s_mul_i32 s66,s66,262144
ds_read_b64_tr_b16 v[70:71],v1 offset:15488
v_mul_f32 v9,v9,v49
v_mfma_f32_16x16x32_bf16 a[212:215],v[92:95],a[124:127],0
s_waitcnt vmcnt(21)
buffer_load_dwordx2 v[4:5],v3,s[44:47],s91 offen offset:256 sc1
v_mfma_f32_16x16x32_bf16 a[212:215],v[88:91],a[116:119],a[212:215]
s_add_u32 s66,s66,100663296
ds_read_b64_tr_b16 v[72:73],v1 offset:20608
ds_read_b64_tr_b16 v[74:75],v1 offset:25728
v_mfma_f32_16x16x32_bf16 a[212:215],v[84:87],a[108:111],a[212:215]
s_mov_b32 s38,262144
ds_read_b64_tr_b16 v[76:77],v1 offset:30848
v_mul_f32 v10,v10,v49
v_mfma_f32_16x16x32_bf16 a[212:215],v[80:83],a[100:103],a[212:215]
s_mov_b32 s39,0x20000
ds_read_b64_tr_b16 v[78:79],v1 offset:35968
v_mul_f32 v11,v11,v49
v_mfma_f32_16x16x32_bf16 a[216:219],v[80:83],a[64:67],0
s_add_u32 s74,s63,1
v_mul_f32 v12,v12,v49
v_mul_f32 v13,v13,v49
v_mfma_f32_16x16x32_bf16 a[216:219],v[84:87],a[72:75],a[216:219]
L_pd13_%=:
s_cbranch_execz L_trt1_%=
s_and_b32 s74,s74,3
v_mul_f32 v14,v14,v49
v_mul_f32 v15,v15,v49
v_mfma_f32_16x16x32_bf16 a[216:219],v[88:91],a[80:83],a[216:219]
s_lshl_b32 s74,s74,2
v_cvt_pk_bf16_f32 v44,v8,v9
v_cvt_pk_bf16_f32 v45,v10,v11
v_mfma_f32_16x16x32_bf16 a[216:219],v[92:95],a[88:91],a[216:219]
s_add_u32 s74,s74,s60
v_cvt_pk_bf16_f32 v46,v12,v13
v_cvt_pk_bf16_f32 v47,v14,v15
v_mfma_f32_16x16x32_bf16 a[220:223],v[92:95],a[120:123],0
s_mul_i32 s74,s74,16384
ds_read_b64_tr_b16 v[92:93],v2 offset:30880
ds_read_b64_tr_b16 v[94:95],v2 offset:36000
v_mfma_f32_16x16x32_bf16 a[220:223],v[88:91],a[112:115],a[220:223]
s_add_u32 s74,s74,8192
ds_read_b64_tr_b16 v[88:89],v2 offset:20640
v_mul_f32 v132,v132,v53
v_mfma_f32_16x16x32_bf16 a[220:223],v[84:87],a[104:107],a[220:223]
buffer_store_dwordx4 v[40:43],v6,s[40:43],s81 offen sc1
v_mfma_f32_16x16x32_bf16 a[220:223],v[80:83],a[96:99],a[220:223]
s_add_u32 s75,s63,2
ds_read_b64_tr_b16 v[80:81],v2 offset:160
ds_read_b64_tr_b16 v[82:83],v2 offset:5280
v_mfma_f32_16x16x32_bf16 a[224:227],v[96:99],a[68:71],0
s_and_b32 s75,s75,3
ds_read_b64_tr_b16 v[84:85],v2 offset:10400
v_mul_f32 v133,v133,v53
v_mfma_f32_16x16x32_bf16 a[224:227],v[100:103],a[76:79],a[224:227]
s_lshl_b32 s75,s75,2
ds_read_b64_tr_b16 v[86:87],v2 offset:15520
v_mul_f32 v134,v134,v53
v_mfma_f32_16x16x32_bf16 a[224:227],v[104:107],a[84:87],a[224:227]
s_add_u32 s75,s75,s60
ds_read_b64_tr_b16 v[90:91],v2 offset:25760
v_mul_f32 v135,v135,v53
s_waitcnt lgkmcnt(8)
v_mfma_f32_16x16x32_bf16 a[224:227],v[108:111],a[92:95],a[224:227]
s_mul_i32 s75,s75,16384
v_mul_f32 v140,v140,v53
v_mul_f32 v141,v141,v53
v_mfma_f32_16x16x32_bf16 a[228:231],v[108:111],a[124:127],0
s_add_u32 s75,s75,4096
v_mul_f32 v142,v142,v53
v_mul_f32 v143,v143,v53
v_mfma_f32_16x16x32_bf16 a[228:231],v[104:107],a[116:119],a[228:231]
s_add_u32 s76,s63,3
v_cvt_pk_bf16_f32 v132,v132,v133
v_cvt_pk_bf16_f32 v133,v134,v135
v_mfma_f32_16x16x32_bf16 a[228:231],v[100:103],a[108:111],a[228:231]
s_and_b32 s76,s76,3
v_cvt_pk_bf16_f32 v134,v140,v141
v_cvt_pk_bf16_f32 v135,v142,v143
v_mfma_f32_16x16x32_bf16 a[228:231],v[96:99],a[100:103],a[228:231]
s_lshl_b32 s76,s76,2
v_mul_f32 v148,v148,v53
v_mul_f32 v149,v149,v53
v_mfma_f32_16x16x32_bf16 a[232:235],v[96:99],a[64:67],0
s_add_u32 s76,s76,s60
v_mul_f32 v150,v150,v53
v_mul_f32 v151,v151,v53
v_mfma_f32_16x16x32_bf16 a[232:235],v[100:103],a[72:75],a[232:235]
s_mul_i32 s76,s76,16384
v_mul_f32 v156,v156,v53
v_mul_f32 v157,v157,v53
v_mfma_f32_16x16x32_bf16 a[232:235],v[104:107],a[80:83],a[232:235]
s_add_u32 s76,s76,0
v_mul_f32 v158,v158,v53
v_mul_f32 v159,v159,v53
v_mfma_f32_16x16x32_bf16 a[232:235],v[108:111],a[88:91],a[232:235]
s_lshl_b32 s67,s63,10
v_cvt_pk_bf16_f32 v148,v148,v149
v_cvt_pk_bf16_f32 v149,v150,v151
v_mfma_f32_16x16x32_bf16 a[236:239],v[108:111],a[120:123],0
s_mov_b32 s26,307200
ds_read_b64_tr_b16 v[108:109],v1 offset:30912
ds_read_b64_tr_b16 v[110:111],v1 offset:36032
v_mfma_f32_16x16x32_bf16 a[236:239],v[104:107],a[112:115],a[236:239]
buffer_store_dwordx4 v[44:47],v6,s[40:43],s82 offen sc1
v_mfma_f32_16x16x32_bf16 a[236:239],v[100:103],a[104:107],a[236:239]
s_mov_b32 s27,0x20000
ds_read_b64_tr_b16 v[100:101],v1 offset:10432
ds_read_b64_tr_b16 v[102:103],v1 offset:15552
v_mfma_f32_16x16x32_bf16 a[236:239],v[96:99],a[96:99],a[236:239]
s_lshl_b32 s77,s63,2
ds_read_b64_tr_b16 v[96:97],v1 offset:192
v_cvt_pk_bf16_f32 v150,v156,v157
v_mfma_f32_16x16x32_bf16 a[240:243],v[112:115],a[68:71],0
s_add_u32 s77,s77,s60
ds_read_b64_tr_b16 v[98:99],v1 offset:5312
v_cvt_pk_bf16_f32 v151,v158,v159
v_mfma_f32_16x16x32_bf16 a[240:243],v[116:119],a[76:79],a[240:243]
s_mul_i32 s77,s77,16384
ds_read_b64_tr_b16 v[104:105],v1 offset:20672
v_mul_f32 v164,v164,v53
v_mfma_f32_16x16x32_bf16 a[240:243],v[120:123],a[84:87],a[240:243]
s_add_u32 s65,s77,0
ds_read_b64_tr_b16 v[106:107],v1 offset:25792
v_mul_f32 v165,v165,v53
v_mfma_f32_16x16x32_bf16 a[240:243],v[124:127],a[92:95],a[240:243]
s_add_u32 s68,s77,12288
v_mul_f32 v166,v166,v53
v_mul_f32 v167,v167,v53
v_mfma_f32_16x16x32_bf16 a[244:247],v[124:127],a[124:127],0
v_mul_f32 v172,v172,v53
v_mul_f32 v173,v173,v53
v_mfma_f32_16x16x32_bf16 a[244:247],v[120:123],a[116:119],a[244:247]
v_mul_f32 v174,v174,v53
v_mul_f32 v175,v175,v53
v_mfma_f32_16x16x32_bf16 a[244:247],v[116:119],a[108:111],a[244:247]
v_cvt_pk_bf16_f32 v164,v164,v165
v_cvt_pk_bf16_f32 v165,v166,v167
v_mfma_f32_16x16x32_bf16 a[244:247],v[112:115],a[100:103],a[244:247]
v_cvt_pk_bf16_f32 v166,v172,v173
v_cvt_pk_bf16_f32 v167,v174,v175
v_mfma_f32_16x16x32_bf16 a[248:251],v[112:115],a[64:67],0
v_mul_f32 v180,v180,v53
v_mul_f32 v181,v181,v53
v_mfma_f32_16x16x32_bf16 a[248:251],v[116:119],a[72:75],a[248:251]
v_mul_f32 v182,v182,v53
v_mul_f32 v183,v183,v53
v_mfma_f32_16x16x32_bf16 a[248:251],v[120:123],a[80:83],a[248:251]
v_mul_f32 v188,v188,v53
v_mul_f32 v189,v189,v53
v_mfma_f32_16x16x32_bf16 a[248:251],v[124:127],a[88:91],a[248:251]
v_mul_f32 v190,v190,v53
v_mul_f32 v191,v191,v53
v_mfma_f32_16x16x32_bf16 a[252:255],v[124:127],a[120:123],0
buffer_load_dword v30,v29,s[8:11],s7 offen
v_mfma_f32_16x16x32_bf16 a[252:255],v[120:123],a[112:115],a[252:255]
ds_read_b64_tr_b16 v[120:121],v2 offset:20704
ds_read_b64_tr_b16 v[122:123],v2 offset:25824
v_mfma_f32_16x16x32_bf16 a[252:255],v[116:119],a[104:107],a[252:255]
buffer_store_dwordx4 v[132:135],v6,s[40:43],s79 offen offset:1024 sc1
v_mfma_f32_16x16x32_bf16 a[252:255],v[112:115],a[96:99],a[252:255]
ds_read_b64_tr_b16 v[112:113],v2 offset:224
ds_read_b64_tr_b16 v[114:115],v2 offset:5344
v_mfma_f32_16x16x32_bf16 a[0:3],v[64:67],a[68:71],0
ds_read_b64_tr_b16 v[116:117],v2 offset:10464
v_cvt_pk_bf16_f32 v180,v180,v181
v_mfma_f32_16x16x32_bf16 a[0:3],v[68:71],a[76:79],a[0:3]
ds_read_b64_tr_b16 v[118:119],v2 offset:15584
v_cvt_pk_bf16_f32 v181,v182,v183
v_mfma_f32_16x16x32_bf16 a[0:3],v[72:75],a[84:87],a[0:3]
ds_read_b64_tr_b16 v[124:125],v2 offset:30944
v_cvt_pk_bf16_f32 v182,v188,v189
v_mfma_f32_16x16x32_bf16 a[0:3],v[76:79],a[92:95],a[0:3]
ds_read_b64_tr_b16 v[126:127],v2 offset:36064
v_cvt_pk_bf16_f32 v183,v190,v191
v_mfma_f32_16x16x32_bf16 a[4:7],v[76:79],a[124:127],0
v_mul_f32 v196,v196,v57
v_mul_f32 v197,v197,v57
v_mfma_f32_16x16x32_bf16 a[4:7],v[72:75],a[116:119],a[4:7]
v_mul_f32 v198,v198,v57
v_mul_f32 v199,v199,v57
v_mfma_f32_16x16x32_bf16 a[4:7],v[68:71],a[108:111],a[4:7]
v_mul_f32 v204,v204,v57
v_mul_f32 v205,v205,v57
v_mfma_f32_16x16x32_bf16 a[4:7],v[64:67],a[100:103],a[4:7]
v_mul_f32 v206,v206,v57
v_mul_f32 v207,v207,v57
v_mfma_f32_16x16x32_bf16 a[8:11],v[64:67],a[64:67],0
v_cvt_pk_bf16_f32 v196,v196,v197
v_cvt_pk_bf16_f32 v197,v198,v199
v_mfma_f32_16x16x32_bf16 a[8:11],v[68:71],a[72:75],a[8:11]
v_cvt_pk_bf16_f32 v198,v204,v205
v_cvt_pk_bf16_f32 v199,v206,v207
v_mfma_f32_16x16x32_bf16 a[8:11],v[72:75],a[80:83],a[8:11]
v_mul_f32 v212,v212,v57
v_mul_f32 v213,v213,v57
v_mfma_f32_16x16x32_bf16 a[8:11],v[76:79],a[88:91],a[8:11]
v_mul_f32 v214,v214,v57
v_mul_f32 v215,v215,v57
v_mfma_f32_16x16x32_bf16 a[12:15],v[76:79],a[120:123],0
ds_read_b64_tr_b16 v[76:77],v1 offset:30976
ds_read_b64_tr_b16 v[78:79],v1 offset:36096
v_mfma_f32_16x16x32_bf16 a[12:15],v[72:75],a[112:115],a[12:15]
buffer_store_dwordx4 v[148:151],v6,s[40:43],s80 offen offset:1024 sc1
v_mfma_f32_16x16x32_bf16 a[12:15],v[68:71],a[104:107],a[12:15]
ds_read_b64_tr_b16 v[68:69],v1 offset:10496
ds_read_b64_tr_b16 v[70:71],v1 offset:15616
v_mfma_f32_16x16x32_bf16 a[12:15],v[64:67],a[96:99],a[12:15]
s_waitcnt lgkmcnt(4)
ds_read_b64_tr_b16 v[64:65],v1 offset:256
v_mul_f32 v220,v220,v57
v_mfma_f32_16x16x32_bf16 a[16:19],v[80:83],a[68:71],0
ds_read_b64_tr_b16 v[66:67],v1 offset:5376
v_mul_f32 v221,v221,v57
v_mfma_f32_16x16x32_bf16 a[16:19],v[84:87],a[76:79],a[16:19]
ds_read_b64_tr_b16 v[72:73],v1 offset:20736
v_mul_f32 v222,v222,v57
v_mfma_f32_16x16x32_bf16 a[16:19],v[88:91],a[84:87],a[16:19]
ds_read_b64_tr_b16 v[74:75],v1 offset:25856
v_add_u32 v1,0xffff6000,v1
v_mfma_f32_16x16x32_bf16 a[16:19],v[92:95],a[92:95],a[16:19]
v_mul_f32 v223,v223,v57
v_cvt_pk_bf16_f32 v212,v212,v213
v_mfma_f32_16x16x32_bf16 a[20:23],v[92:95],a[124:127],0
v_cvt_pk_bf16_f32 v213,v214,v215
v_cvt_pk_bf16_f32 v214,v220,v221
v_mfma_f32_16x16x32_bf16 a[20:23],v[88:91],a[116:119],a[20:23]
v_cvt_pk_bf16_f32 v215,v222,v223
v_mul_f32 v228,v228,v57
v_mfma_f32_16x16x32_bf16 a[20:23],v[84:87],a[108:111],a[20:23]
v_mul_f32 v229,v229,v57
v_mul_f32 v230,v230,v57
v_mfma_f32_16x16x32_bf16 a[20:23],v[80:83],a[100:103],a[20:23]
v_mul_f32 v231,v231,v57
v_mul_f32 v236,v236,v57
v_mfma_f32_16x16x32_bf16 a[24:27],v[80:83],a[64:67],0
v_mul_f32 v237,v237,v57
v_mul_f32 v238,v238,v57
v_mfma_f32_16x16x32_bf16 a[24:27],v[84:87],a[72:75],a[24:27]
v_mul_f32 v239,v239,v57
v_cvt_pk_bf16_f32 v228,v228,v229
v_mfma_f32_16x16x32_bf16 a[24:27],v[88:91],a[80:83],a[24:27]
s_waitcnt vmcnt(5)
v_cmp_ne_u32 s[56:57],s52,v4
v_cmp_ne_u32 s[58:59],s53,v5
v_mfma_f32_16x16x32_bf16 a[24:27],v[92:95],a[88:91],a[24:27]
s_or_b64 s[56:57],s[56:57],s[58:59]
s_cbranch_scc1 L_fr1_%=
L_fo1_%=:
v_cvt_pk_bf16_f32 v229,v230,v231
v_mfma_f32_16x16x32_bf16 a[28:31],v[92:95],a[120:123],0
buffer_store_dwordx4 v[164:167],v6,s[40:43],s81 offen offset:1024 sc1
v_mfma_f32_16x16x32_bf16 a[28:31],v[88:91],a[112:115],a[28:31]
s_lshl_b32 s91,s63,8
ds_read_b64_tr_b16 v[88:89],v2 offset:20768
ds_read_b64_tr_b16 v[90:91],v2 offset:25888
v_mfma_f32_16x16x32_bf16 a[28:31],v[84:87],a[104:107],a[28:31]
buffer_load_dwordx4 a[128:131],v6,s[28:31],s87 offen sc1
v_mfma_f32_16x16x32_bf16 a[28:31],v[80:83],a[96:99],a[28:31]
ds_read_b64_tr_b16 v[80:81],v2 offset:288
)A"
R"A(ds_read_b64_tr_b16 v[82:83],v2 offset:5408
v_mfma_f32_16x16x32_bf16 v[128:131],v[96:99],a[68:71],0
ds_read_b64_tr_b16 v[84:85],v2 offset:10528
v_cvt_pk_bf16_f32 v230,v236,v237
v_mfma_f32_16x16x32_bf16 v[128:131],v[100:103],a[76:79],v[128:131]
ds_read_b64_tr_b16 v[86:87],v2 offset:15648
v_cvt_pk_bf16_f32 v231,v238,v239
v_mfma_f32_16x16x32_bf16 v[128:131],v[104:107],a[84:87],v[128:131]
ds_read_b64_tr_b16 v[92:93],v2 offset:31008
v_mul_f32 v244,v244,v57
v_mfma_f32_16x16x32_bf16 v[128:131],v[108:111],a[92:95],v[128:131]
ds_read_b64_tr_b16 v[94:95],v2 offset:36128
v_add_u32 v2,0xffff6000,v2
v_mfma_f32_16x16x32_bf16 v[136:139],v[108:111],a[124:127],0
v_mul_f32 v245,v245,v57
v_mul_f32 v246,v246,v57
v_mfma_f32_16x16x32_bf16 v[136:139],v[104:107],a[116:119],v[136:139]
v_mul_f32 v247,v247,v57
v_mul_f32 v252,v252,v57
v_mfma_f32_16x16x32_bf16 v[136:139],v[100:103],a[108:111],v[136:139]
v_mul_f32 v253,v253,v57
v_mul_f32 v254,v254,v57
v_mfma_f32_16x16x32_bf16 v[136:139],v[96:99],a[100:103],v[136:139]
v_mul_f32 v255,v255,v57
v_cvt_pk_bf16_f32 v244,v244,v245
v_mfma_f32_16x16x32_bf16 v[144:147],v[96:99],a[64:67],0
v_cvt_pk_bf16_f32 v245,v246,v247
v_cvt_pk_bf16_f32 v246,v252,v253
v_mfma_f32_16x16x32_bf16 v[144:147],v[100:103],a[72:75],v[144:147]
buffer_load_dwordx4 a[144:147],v6,s[28:31],s88 offen sc1
v_mfma_f32_16x16x32_bf16 v[144:147],v[104:107],a[80:83],v[144:147]
v_cvt_pk_bf16_f32 v247,v254,v255
v_accvgpr_read_b32 v8,a40
v_mfma_f32_16x16x32_bf16 v[144:147],v[108:111],a[88:91],v[144:147]
buffer_store_dwordx4 v[180:183],v6,s[40:43],s82 offen offset:1024 sc1
v_mfma_f32_16x16x32_bf16 v[152:155],v[108:111],a[120:123],0
ds_read_b64_tr_b16 v[108:109],v1 offset:30720
ds_read_b64_tr_b16 v[110:111],v1 offset:35840
v_mfma_f32_16x16x32_bf16 v[152:155],v[104:107],a[112:115],v[152:155]
ds_read_b64_tr_b16 v[104:105],v1 offset:20480
v_accvgpr_read_b32 v9,a41
v_mfma_f32_16x16x32_bf16 v[152:155],v[100:103],a[104:107],v[152:155]
ds_read_b64_tr_b16 v[100:101],v1 offset:10240
v_accvgpr_read_b32 v10,a42
v_mfma_f32_16x16x32_bf16 v[152:155],v[96:99],a[96:99],v[152:155]
ds_read_b64_tr_b16 v[96:97],v1
v_accvgpr_read_b32 v11,a43
v_mfma_f32_16x16x32_bf16 v[160:163],v[112:115],a[68:71],0
ds_read_b64_tr_b16 v[98:99],v1 offset:5120
ds_read_b64_tr_b16 v[102:103],v1 offset:15360
v_mfma_f32_16x16x32_bf16 v[160:163],v[116:119],a[76:79],v[160:163]
ds_read_b64_tr_b16 v[106:107],v1 offset:25600
v_accvgpr_read_b32 v12,a44
v_mfma_f32_16x16x32_bf16 v[160:163],v[120:123],a[84:87],v[160:163]
v_accvgpr_read_b32 v13,a45
v_accvgpr_read_b32 v14,a46
v_mfma_f32_16x16x32_bf16 v[160:163],v[124:127],a[92:95],v[160:163]
v_accvgpr_read_b32 v15,a47
v_mul_f32 v8,v8,v61
v_mfma_f32_16x16x32_bf16 v[168:171],v[124:127],a[124:127],0
v_mul_f32 v9,v9,v61
v_mul_f32 v10,v10,v61
v_mfma_f32_16x16x32_bf16 v[168:171],v[120:123],a[116:119],v[168:171]
v_mul_f32 v11,v11,v61
v_mul_f32 v12,v12,v61
v_mfma_f32_16x16x32_bf16 v[168:171],v[116:119],a[108:111],v[168:171]
buffer_load_dwordx4 a[160:163],v6,s[28:31],s89 offen sc1
v_mfma_f32_16x16x32_bf16 v[168:171],v[112:115],a[100:103],v[168:171]
v_mul_f32 v13,v13,v61
v_mul_f32 v14,v14,v61
v_mfma_f32_16x16x32_bf16 v[176:179],v[112:115],a[64:67],0
v_mul_f32 v15,v15,v61
v_cvt_pk_bf16_f32 v156,v8,v9
v_mfma_f32_16x16x32_bf16 v[176:179],v[116:119],a[72:75],v[176:179]
v_cvt_pk_bf16_f32 v157,v10,v11
v_cvt_pk_bf16_f32 v158,v12,v13
v_mfma_f32_16x16x32_bf16 v[176:179],v[120:123],a[80:83],v[176:179]
buffer_store_dwordx4 v[196:199],v6,s[40:43],s79 offen offset:2048 sc1
v_mfma_f32_16x16x32_bf16 v[176:179],v[124:127],a[88:91],v[176:179]
v_cvt_pk_bf16_f32 v159,v14,v15
v_accvgpr_read_b32 v8,a56
v_mfma_f32_16x16x32_bf16 v[184:187],v[124:127],a[120:123],0
v_accvgpr_read_b32 v9,a57
v_accvgpr_read_b32 v10,a58
v_mfma_f32_16x16x32_bf16 v[184:187],v[120:123],a[112:115],v[184:187]
ds_read_b64_tr_b16 v[120:121],v2 offset:20512
ds_read_b64_tr_b16 v[122:123],v2 offset:25632
v_mfma_f32_16x16x32_bf16 v[184:187],v[116:119],a[104:107],v[184:187]
ds_read_b64_tr_b16 v[116:117],v2 offset:10272
v_accvgpr_read_b32 v11,a59
v_mfma_f32_16x16x32_bf16 v[184:187],v[112:115],a[96:99],v[184:187]
s_waitcnt lgkmcnt(3)
ds_read_b64_tr_b16 v[112:113],v2 offset:32
v_accvgpr_read_b32 v12,a60
v_mfma_f32_16x16x32_bf16 v[192:195],v[64:67],a[68:71],0
ds_read_b64_tr_b16 v[114:115],v2 offset:5152
v_accvgpr_read_b32 v13,a61
v_mfma_f32_16x16x32_bf16 v[192:195],v[68:71],a[76:79],v[192:195]
ds_read_b64_tr_b16 v[118:119],v2 offset:15392
v_accvgpr_read_b32 v14,a62
v_mfma_f32_16x16x32_bf16 v[192:195],v[72:75],a[84:87],v[192:195]
v_accvgpr_read_b32 v15,a63
v_mul_f32 v8,v8,v61
v_mfma_f32_16x16x32_bf16 v[192:195],v[76:79],a[92:95],v[192:195]
buffer_load_dwordx4 a[176:179],v6,s[28:31],s90 offen sc1
v_mfma_f32_16x16x32_bf16 v[200:203],v[76:79],a[124:127],0
v_mul_f32 v9,v9,v61
v_mul_f32 v10,v10,v61
v_mfma_f32_16x16x32_bf16 v[200:203],v[72:75],a[116:119],v[200:203]
v_mul_f32 v11,v11,v61
v_mul_f32 v12,v12,v61
v_mfma_f32_16x16x32_bf16 v[200:203],v[68:71],a[108:111],v[200:203]
v_mul_f32 v13,v13,v61
v_mul_f32 v14,v14,v61
v_mfma_f32_16x16x32_bf16 v[200:203],v[64:67],a[100:103],v[200:203]
v_mul_f32 v15,v15,v61
v_cvt_pk_bf16_f32 v188,v8,v9
v_mfma_f32_16x16x32_bf16 v[208:211],v[64:67],a[64:67],0
v_cvt_pk_bf16_f32 v189,v10,v11
v_cvt_pk_bf16_f32 v190,v12,v13
v_mfma_f32_16x16x32_bf16 v[208:211],v[68:71],a[72:75],v[208:211]
buffer_store_dwordx4 v[212:215],v6,s[40:43],s80 offen offset:2048 sc1
v_mfma_f32_16x16x32_bf16 v[208:211],v[72:75],a[80:83],v[208:211]
ds_read_b64_tr_b16 v[124:125],v2 offset:30752
v_cvt_pk_bf16_f32 v191,v14,v15
v_mfma_f32_16x16x32_bf16 v[208:211],v[76:79],a[88:91],v[208:211]
ds_read_b64_tr_b16 v[126:127],v2 offset:35872
v_accvgpr_read_b32 v16,a32
v_mfma_f32_16x16x32_bf16 v[216:219],v[76:79],a[120:123],0
v_accvgpr_read_b32 v17,a33
v_accvgpr_read_b32 v18,a34
v_mfma_f32_16x16x32_bf16 v[216:219],v[72:75],a[112:115],v[216:219]
v_accvgpr_read_b32 v19,a35
v_accvgpr_read_b32 v20,a36
v_mfma_f32_16x16x32_bf16 v[216:219],v[68:71],a[104:107],v[216:219]
v_accvgpr_read_b32 v21,a37
v_accvgpr_read_b32 v22,a38
v_mfma_f32_16x16x32_bf16 v[216:219],v[64:67],a[96:99],v[216:219]
v_accvgpr_read_b32 v23,a39
v_mul_f32 v16,v16,v61
v_mfma_f32_16x16x32_bf16 v[224:227],v[80:83],a[68:71],0
buffer_load_dwordx4 a[180:183],v6,s[28:31],s90 offen offset:1024 sc1
v_mfma_f32_16x16x32_bf16 v[224:227],v[84:87],a[76:79],v[224:227]
v_mul_f32 v17,v17,v61
v_mul_f32 v18,v18,v61
v_mfma_f32_16x16x32_bf16 v[224:227],v[88:91],a[84:87],v[224:227]
v_mul_f32 v19,v19,v61
v_mul_f32 v20,v20,v61
v_mfma_f32_16x16x32_bf16 v[224:227],v[92:95],a[92:95],v[224:227]
v_mul_f32 v21,v21,v61
v_mul_f32 v22,v22,v61
v_mfma_f32_16x16x32_bf16 v[232:235],v[92:95],a[124:127],0
v_mul_f32 v23,v23,v61
v_cvt_pk_bf16_f32 v140,v16,v17
v_mfma_f32_16x16x32_bf16 v[232:235],v[88:91],a[116:119],v[232:235]
v_cvt_pk_bf16_f32 v141,v18,v19
v_cvt_pk_bf16_f32 v142,v20,v21
v_mfma_f32_16x16x32_bf16 v[232:235],v[84:87],a[108:111],v[232:235]
v_cvt_pk_bf16_f32 v143,v22,v23
v_accvgpr_read_b32 v16,a48
v_mfma_f32_16x16x32_bf16 v[232:235],v[80:83],a[100:103],v[232:235]
v_accvgpr_read_b32 v17,a49
v_accvgpr_read_b32 v18,a50
v_mfma_f32_16x16x32_bf16 v[240:243],v[80:83],a[64:67],0
buffer_store_dwordx4 v[228:231],v6,s[40:43],s81 offen offset:2048 sc1
v_mfma_f32_16x16x32_bf16 v[240:243],v[84:87],a[72:75],v[240:243]
v_accvgpr_read_b32 v19,a51
v_accvgpr_read_b32 v20,a52
v_mfma_f32_16x16x32_bf16 v[240:243],v[88:91],a[80:83],v[240:243]
v_accvgpr_read_b32 v21,a53
v_accvgpr_read_b32 v22,a54
v_mfma_f32_16x16x32_bf16 v[240:243],v[92:95],a[88:91],v[240:243]
v_accvgpr_read_b32 v23,a55
v_mul_f32 v16,v16,v61
v_mfma_f32_16x16x32_bf16 v[248:251],v[92:95],a[120:123],0
v_mul_f32 v17,v17,v61
v_mul_f32 v18,v18,v61
v_mfma_f32_16x16x32_bf16 v[248:251],v[88:91],a[112:115],v[248:251]
buffer_load_dwordx4 a[164:167],v6,s[28:31],s89 offen offset:1024 sc1
v_mfma_f32_16x16x32_bf16 v[248:251],v[84:87],a[104:107],v[248:251]
v_mul_f32 v19,v19,v61
v_mul_f32 v20,v20,v61
v_mfma_f32_16x16x32_bf16 v[248:251],v[80:83],a[96:99],v[248:251]
v_mul_f32 v21,v21,v61
v_mul_f32 v22,v22,v61
v_mfma_f32_16x16x32_bf16 a[192:195],v[96:99],v[32:35],a[192:195]
v_mul_f32 v23,v23,v61
v_cvt_pk_bf16_f32 v172,v16,v17
v_mfma_f32_16x16x32_bf16 a[192:195],v[100:103],v[36:39],a[192:195]
v_cvt_pk_bf16_f32 v173,v18,v19
v_cvt_pk_bf16_f32 v174,v20,v21
v_mfma_f32_16x16x32_bf16 a[192:195],v[104:107],v[40:43],a[192:195]
ds_read_b64_tr_b16 v[64:65],v1 offset:64
v_cvt_pk_bf16_f32 v175,v22,v23
v_mfma_f32_16x16x32_bf16 a[192:195],v[108:111],v[44:47],a[192:195]
ds_read_b64_tr_b16 v[66:67],v1 offset:5184
ds_read_b64_tr_b16 v[68:69],v1 offset:10304
v_mfma_f32_16x16x32_bf16 a[196:199],v[108:111],v[180:183],a[196:199]
s_cmp_eq_u32 s60,3
s_cselect_b32 s69,0x40000000,0
ds_read_b64_tr_b16 v[70:71],v1 offset:15424
v_mfma_f32_16x16x32_bf16 a[196:199],v[104:107],v[164:167],a[196:199]
ds_read_b64_tr_b16 v[72:73],v1 offset:20544
v_add_u32 v28,s69,v6
v_mfma_f32_16x16x32_bf16 a[196:199],v[100:103],v[148:151],a[196:199]
s_lshl_b32 s69,s63,2
ds_read_b64_tr_b16 v[74:75],v1 offset:25664
v_mov_b32 v8,s52
v_mfma_f32_16x16x32_bf16 a[196:199],v[96:99],v[132:135],a[196:199]
buffer_store_dwordx4 v[244:247],v6,s[40:43],s82 offen offset:2048 sc1
v_mfma_f32_16x16x32_bf16 a[200:203],v[96:99],v[196:199],a[200:203]
ds_read_b64_tr_b16 v[76:77],v1 offset:30784
ds_read_b64_tr_b16 v[78:79],v1 offset:35904
v_mfma_f32_16x16x32_bf16 a[200:203],v[100:103],v[212:215],a[200:203]
ds_read_b64_tr_b16 v[80:81],v2 offset:96
v_mov_b32 v9,s53
v_mfma_f32_16x16x32_bf16 a[200:203],v[104:107],v[228:231],a[200:203]
buffer_load_dwordx4 a[148:151],v6,s[28:31],s88 offen offset:1024 sc1
v_mfma_f32_16x16x32_bf16 a[200:203],v[108:111],v[244:247],a[200:203]
ds_read_b64_tr_b16 v[82:83],v2 offset:5216
ds_read_b64_tr_b16 v[84:85],v2 offset:10336
v_mfma_f32_16x16x32_bf16 a[204:207],v[108:111],v[188:191],a[204:207]
ds_read_b64_tr_b16 v[86:87],v2 offset:15456
v_lshlrev_b32 v10,20,v6
v_mfma_f32_16x16x32_bf16 a[204:207],v[104:107],v[172:175],a[204:207]
ds_read_b64_tr_b16 v[88:89],v2 offset:20576
v_bfe_u32 v3,v6,4,2
v_mfma_f32_16x16x32_bf16 a[204:207],v[100:103],v[156:159],a[204:207]
ds_read_b64_tr_b16 v[90:91],v2 offset:25696
v_min_u32 v3,2,v3
v_mfma_f32_16x16x32_bf16 a[204:207],v[96:99],v[140:143],a[204:207]
s_waitcnt lgkmcnt(8)
ds_read_b64_tr_b16 v[92:93],v2 offset:30816
ds_read_b64_tr_b16 v[94:95],v2 offset:35936
v_mfma_f32_16x16x32_bf16 a[208:211],v[112:115],v[32:35],a[208:211]
ds_read_b64_tr_b16 v[96:97],v1 offset:128
v_add3_u32 v3,v3,s64,1
v_mfma_f32_16x16x32_bf16 a[208:211],v[116:119],v[36:39],a[208:211]
ds_read_b64_tr_b16 v[98:99],v1 offset:5248
v_and_b32 v3,3,v3
v_mfma_f32_16x16x32_bf16 a[208:211],v[120:123],v[40:43],a[208:211]
ds_read_b64_tr_b16 v[100:101],v1 offset:10368
v_lshlrev_b32 v3,6,v3
v_mfma_f32_16x16x32_bf16 a[208:211],v[124:127],v[44:47],a[208:211]
ds_read_b64_tr_b16 v[102:103],v1 offset:15488
ds_read_b64_tr_b16 v[104:105],v1 offset:20608
v_mfma_f32_16x16x32_bf16 a[212:215],v[124:127],v[180:183],a[212:215]
ds_read_b64_tr_b16 v[106:107],v1 offset:25728
v_bfe_u32 v56,v6,6,2
v_mfma_f32_16x16x32_bf16 a[212:215],v[120:123],v[164:167],a[212:215]
ds_read_b64_tr_b16 v[108:109],v1 offset:30848
v_lshrrev_b32 v57,8,v6
v_mfma_f32_16x16x32_bf16 a[212:215],v[116:119],v[148:151],a[212:215]
buffer_store_dwordx4 v[140:143],v28,s[40:43],s79 offen offset:3072 sc1
v_mfma_f32_16x16x32_bf16 a[212:215],v[112:115],v[132:135],a[212:215]
ds_read_b64_tr_b16 v[110:111],v1 offset:35968
v_cmp_eq_u32 s[56:57],v56,v57
v_mfma_f32_16x16x32_bf16 a[216:219],v[112:115],v[196:199],a[216:219]
buffer_load_dwordx4 a[132:135],v6,s[28:31],s87 offen offset:1024 sc1
v_mfma_f32_16x16x32_bf16 a[216:219],v[116:119],v[212:215],a[216:219]
v_bfe_u32 v56,v6,4,1
v_lshlrev_b32 v56,4,v56
v_mfma_f32_16x16x32_bf16 a[216:219],v[120:123],v[228:231],a[216:219]
v_mov_b32 v57,0x3f80
v_lshlrev_b32 v56,v56,v57
v_mfma_f32_16x16x32_bf16 a[216:219],v[124:127],v[244:247],a[216:219]
v_cndmask_b32 v56,0,v56,s[56:57]
v_bfe_u32 v57,v6,5,1
v_mfma_f32_16x16x32_bf16 a[220:223],v[124:127],v[188:191],a[220:223]
ds_read_b64_tr_b16 v[124:125],v2 offset:30880
ds_read_b64_tr_b16 v[126:127],v2 offset:36000
)A"
R"A(v_mfma_f32_16x16x32_bf16 a[220:223],v[120:123],v[172:175],a[220:223]
ds_read_b64_tr_b16 v[120:121],v2 offset:20640
v_cmp_eq_u32 s[56:57],0,v57
v_mfma_f32_16x16x32_bf16 a[220:223],v[116:119],v[156:159],a[220:223]
s_add_u32 s36,s12,s66
s_addc_u32 s37,s13,0
ds_read_b64_tr_b16 v[116:117],v2 offset:10400
v_mfma_f32_16x16x32_bf16 a[220:223],v[112:115],v[140:143],a[220:223]
s_and_b32 s37,s37,0xffff
ds_read_b64_tr_b16 v[112:113],v2 offset:160
v_cndmask_b32 v48,0,v56,s[56:57]
v_mfma_f32_16x16x32_bf16 a[224:227],v[64:67],v[32:35],a[224:227]
s_mul_i32 s66,s61,307200
ds_read_b64_tr_b16 v[114:115],v2 offset:5280
ds_read_b64_tr_b16 v[118:119],v2 offset:15520
v_mfma_f32_16x16x32_bf16 a[224:227],v[68:71],v[36:39],a[224:227]
ds_read_b64_tr_b16 v[122:123],v2 offset:25760
v_cndmask_b32 v49,v56,0,s[56:57]
v_mfma_f32_16x16x32_bf16 a[224:227],v[72:75],v[40:43],a[224:227]
s_waitcnt lgkmcnt(8)
v_mov_b32 v58,s52
v_mov_b32 v59,s53
v_mfma_f32_16x16x32_bf16 a[224:227],v[76:79],v[44:47],a[224:227]
s_add_u32 s24,s16,s66
s_addc_u32 s25,s17,0
v_lshlrev_b32 v60,20,v6
v_mfma_f32_16x16x32_bf16 a[228:231],v[76:79],v[180:183],a[228:231]
buffer_load_dwordx4 a[136:139],v6,s[28:31],s87 offen offset:2048 sc1
v_mfma_f32_16x16x32_bf16 a[228:231],v[72:75],v[164:167],a[228:231]
s_mul_i32 s66,s60,16384
s_add_u32 s66,s66,s67
s_add_u32 s79,s66,12288
v_mfma_f32_16x16x32_bf16 a[228:231],v[68:71],v[148:151],a[228:231]
buffer_store_dwordx4 v[156:159],v28,s[40:43],s80 offen offset:3072 sc1
v_mfma_f32_16x16x32_bf16 a[228:231],v[64:67],v[132:135],a[228:231]
s_add_u32 s80,s66,77824
v_bfe_u32 v61,v6,4,1
v_add3_u32 v61,v61,s63,2
v_mfma_f32_16x16x32_bf16 a[232:235],v[64:67],v[196:199],a[232:235]
s_and_b32 s25,s25,0xffff
v_and_b32 v61,3,v61
v_lshlrev_b32 v61,8,v61
v_mfma_f32_16x16x32_bf16 a[232:235],v[68:71],v[212:215],a[232:235]
s_add_u32 s67,s77,8192
v_mov_b32 v50,0
v_mov_b32 v51,0
v_mfma_f32_16x16x32_bf16 a[232:235],v[72:75],v[228:231],a[232:235]
v_mov_b32 v52,0
v_mov_b32 v53,0
v_mfma_f32_16x16x32_bf16 a[232:235],v[76:79],v[244:247],a[232:235]
v_mov_b32 v54,v48
v_mov_b32 v55,v49
v_mfma_f32_16x16x32_bf16 a[236:239],v[76:79],v[188:191],a[236:239]
ds_read_b64_tr_b16 v[76:77],v1 offset:30912
ds_read_b64_tr_b16 v[78:79],v1 offset:36032
v_mfma_f32_16x16x32_bf16 a[236:239],v[72:75],v[172:175],a[236:239]
ds_read_b64_tr_b16 v[72:73],v1 offset:20672
v_bfe_u32 v56,v6,4,4
v_mfma_f32_16x16x32_bf16 a[236:239],v[68:71],v[156:159],a[236:239]
ds_read_b64_tr_b16 v[68:69],v1 offset:10432
v_mul_u32_u24 v56,1280,v56
v_mfma_f32_16x16x32_bf16 a[236:239],v[64:67],v[140:143],a[236:239]
ds_read_b64_tr_b16 v[64:65],v1 offset:192
v_bfe_u32 v57,v6,8,1
v_mfma_f32_16x16x32_bf16 a[240:243],v[80:83],v[32:35],a[240:243]
ds_read_b64_tr_b16 v[66:67],v1 offset:5312
ds_read_b64_tr_b16 v[70:71],v1 offset:15552
v_mfma_f32_16x16x32_bf16 a[240:243],v[84:87],v[36:39],a[240:243]
buffer_load_dwordx4 a[152:155],v6,s[28:31],s88 offen offset:2048 sc1
v_mfma_f32_16x16x32_bf16 a[240:243],v[88:91],v[40:43],a[240:243]
ds_read_b64_tr_b16 v[74:75],v1 offset:25792
v_lshl_add_u32 v56,v57,5,v56
v_mfma_f32_16x16x32_bf16 a[240:243],v[92:95],v[44:47],a[240:243]
v_lshrrev_b32 v57,9,v6
v_lshl_add_u32 v56,v57,4,v56
v_mfma_f32_16x16x32_bf16 a[244:247],v[92:95],v[180:183],a[244:247]
v_mfma_f32_16x16x32_bf16 a[244:247],v[88:91],v[164:167],a[244:247]
buffer_store_dwordx4 v[172:175],v28,s[40:43],s81 offen offset:3072 sc1
v_mfma_f32_16x16x32_bf16 a[244:247],v[84:87],v[148:151],a[244:247]
s_add_u32 s81,s66,143360
v_mfma_f32_16x16x32_bf16 a[244:247],v[80:83],v[132:135],a[244:247]
v_mfma_f32_16x16x32_bf16 a[248:251],v[80:83],v[196:199],a[248:251]
v_mfma_f32_16x16x32_bf16 a[248:251],v[84:87],v[212:215],a[248:251]
L_pd14_%=:
s_cbranch_execz L_trt1_%=
v_mfma_f32_16x16x32_bf16 a[248:251],v[88:91],v[228:231],a[248:251]
v_mfma_f32_16x16x32_bf16 a[248:251],v[92:95],v[244:247],a[248:251]
v_mfma_f32_16x16x32_bf16 a[252:255],v[92:95],v[188:191],a[252:255]
ds_read_b64_tr_b16 v[92:93],v2 offset:30944
ds_read_b64_tr_b16 v[94:95],v2 offset:36064
v_mfma_f32_16x16x32_bf16 a[252:255],v[88:91],v[172:175],a[252:255]
ds_read_b64_tr_b16 v[88:89],v2 offset:20704
v_mfma_f32_16x16x32_bf16 a[252:255],v[84:87],v[156:159],a[252:255]
buffer_load_dwordx4 a[168:171],v6,s[28:31],s89 offen offset:2048 sc1
v_mfma_f32_16x16x32_bf16 a[252:255],v[80:83],v[140:143],a[252:255]
ds_read_b64_tr_b16 v[80:81],v2 offset:224
ds_read_b64_tr_b16 v[82:83],v2 offset:5344
v_mfma_f32_16x16x32_bf16 a[0:3],v[96:99],v[32:35],a[0:3]
ds_read_b64_tr_b16 v[84:85],v2 offset:10464
v_mfma_f32_16x16x32_bf16 a[0:3],v[100:103],v[36:39],a[0:3]
ds_read_b64_tr_b16 v[86:87],v2 offset:15584
v_mfma_f32_16x16x32_bf16 a[0:3],v[104:107],v[40:43],a[0:3]
ds_read_b64_tr_b16 v[90:91],v2 offset:25824
v_mfma_f32_16x16x32_bf16 a[0:3],v[108:111],v[44:47],a[0:3]
v_mfma_f32_16x16x32_bf16 a[4:7],v[108:111],v[180:183],a[4:7]
buffer_store_dwordx4 v[188:191],v28,s[40:43],s82 offen offset:3072 sc1
v_mfma_f32_16x16x32_bf16 a[4:7],v[104:107],v[164:167],a[4:7]
s_add_u32 s82,s66,208896
s_mul_i32 s66,s64,320
v_mfma_f32_16x16x32_bf16 a[4:7],v[100:103],v[148:151],a[4:7]
v_mfma_f32_16x16x32_bf16 a[4:7],v[96:99],v[132:135],a[4:7]
v_mfma_f32_16x16x32_bf16 a[8:11],v[96:99],v[196:199],a[8:11]
v_mfma_f32_16x16x32_bf16 a[8:11],v[100:103],v[212:215],a[8:11]
v_mfma_f32_16x16x32_bf16 a[8:11],v[104:107],v[228:231],a[8:11]
v_mfma_f32_16x16x32_bf16 a[8:11],v[108:111],v[244:247],a[8:11]
buffer_load_dwordx4 a[184:187],v6,s[28:31],s90 offen offset:2048 sc1
v_mfma_f32_16x16x32_bf16 a[12:15],v[108:111],v[188:191],a[12:15]
ds_read_b64_tr_b16 v[108:109],v1 offset:30976
ds_read_b64_tr_b16 v[110:111],v1 offset:36096
v_mfma_f32_16x16x32_bf16 a[12:15],v[104:107],v[172:175],a[12:15]
ds_read_b64_tr_b16 v[104:105],v1 offset:20736
v_mfma_f32_16x16x32_bf16 a[12:15],v[100:103],v[156:159],a[12:15]
ds_read_b64_tr_b16 v[100:101],v1 offset:10496
v_mfma_f32_16x16x32_bf16 a[12:15],v[96:99],v[140:143],a[12:15]
s_waitcnt lgkmcnt(4)
ds_read_b64_tr_b16 v[96:97],v1 offset:256
v_mfma_f32_16x16x32_bf16 a[16:19],v[112:115],v[32:35],a[16:19]
ds_read_b64_tr_b16 v[98:99],v1 offset:5376
ds_read_b64_tr_b16 v[102:103],v1 offset:15616
v_mfma_f32_16x16x32_bf16 a[16:19],v[116:119],v[36:39],a[16:19]
ds_read_b64_tr_b16 v[106:107],v1 offset:25856
v_add_u32 v1,0xffff6000,v1
v_mfma_f32_16x16x32_bf16 a[16:19],v[120:123],v[40:43],a[16:19]
v_mfma_f32_16x16x32_bf16 a[16:19],v[124:127],v[44:47],a[16:19]
v_mfma_f32_16x16x32_bf16 a[20:23],v[124:127],v[180:183],a[20:23]
v_mfma_f32_16x16x32_bf16 a[20:23],v[120:123],v[164:167],a[20:23]
v_mfma_f32_16x16x32_bf16 a[20:23],v[116:119],v[148:151],a[20:23]
v_mfma_f32_16x16x32_bf16 a[20:23],v[112:115],v[132:135],a[20:23]
v_mfma_f32_16x16x32_bf16 a[24:27],v[112:115],v[196:199],a[24:27]
buffer_load_dwordx4 a[188:191],v28,s[28:31],s90 offen offset:3072 sc1
v_mfma_f32_16x16x32_bf16 a[24:27],v[116:119],v[212:215],a[24:27]
v_mfma_f32_16x16x32_bf16 a[24:27],v[120:123],v[228:231],a[24:27]
v_mfma_f32_16x16x32_bf16 a[24:27],v[124:127],v[244:247],a[24:27]
v_mfma_f32_16x16x32_bf16 a[28:31],v[124:127],v[188:191],a[28:31]
ds_read_b64_tr_b16 v[124:125],v2 offset:31008
ds_read_b64_tr_b16 v[126:127],v2 offset:36128
v_mfma_f32_16x16x32_bf16 a[28:31],v[120:123],v[172:175],a[28:31]
ds_read_b64_tr_b16 v[120:121],v2 offset:20768
v_mfma_f32_16x16x32_bf16 a[28:31],v[116:119],v[156:159],a[28:31]
ds_read_b64_tr_b16 v[116:117],v2 offset:10528
v_mfma_f32_16x16x32_bf16 a[28:31],v[112:115],v[140:143],a[28:31]
ds_read_b64_tr_b16 v[112:113],v2 offset:288
v_mfma_f32_16x16x32_bf16 v[128:131],v[64:67],v[32:35],v[128:131]
ds_read_b64_tr_b16 v[114:115],v2 offset:5408
ds_read_b64_tr_b16 v[118:119],v2 offset:15648
v_mfma_f32_16x16x32_bf16 v[128:131],v[68:71],v[36:39],v[128:131]
ds_read_b64_tr_b16 v[122:123],v2 offset:25888
v_add_u32 v2,0xffff6000,v2
v_mfma_f32_16x16x32_bf16 v[128:131],v[72:75],v[40:43],v[128:131]
v_mfma_f32_16x16x32_bf16 v[128:131],v[76:79],v[44:47],v[128:131]
v_mfma_f32_16x16x32_bf16 v[136:139],v[76:79],v[180:183],v[136:139]
v_mfma_f32_16x16x32_bf16 v[136:139],v[72:75],v[164:167],v[136:139]
buffer_load_dwordx4 a[172:175],v28,s[28:31],s89 offen offset:3072 sc1
v_mfma_f32_16x16x32_bf16 v[136:139],v[68:71],v[148:151],v[136:139]
v_mfma_f32_16x16x32_bf16 v[136:139],v[64:67],v[132:135],v[136:139]
v_mfma_f32_16x16x32_bf16 v[144:147],v[64:67],v[196:199],v[144:147]
v_mfma_f32_16x16x32_bf16 v[144:147],v[68:71],v[212:215],v[144:147]
v_mfma_f32_16x16x32_bf16 v[144:147],v[72:75],v[228:231],v[144:147]
v_mfma_f32_16x16x32_bf16 v[144:147],v[76:79],v[244:247],v[144:147]
v_mfma_f32_16x16x32_bf16 v[152:155],v[76:79],v[188:191],v[152:155]
v_mfma_f32_16x16x32_bf16 v[152:155],v[72:75],v[172:175],v[152:155]
v_mfma_f32_16x16x32_bf16 v[152:155],v[68:71],v[156:159],v[152:155]
v_mfma_f32_16x16x32_bf16 v[152:155],v[64:67],v[140:143],v[152:155]
s_mov_b64 exec,0
s_cmp_eq_u32 s60,0
s_cbranch_scc1 L_pd16_%=
s_cmp_eq_u32 s60,1
s_cbranch_scc1 L_pd17_%=
s_cmp_eq_u32 s60,2
s_cbranch_scc1 L_pd18_%=
s_branch L_trtg2F_%=
L_trtg2F_%=:
s_mov_b64 exec,-1
s_waitcnt lgkmcnt(0)
s_barrier
v_mfma_f32_16x16x32_bf16 v[160:163],v[80:83],v[32:35],v[160:163]
buffer_load_dwordx4 v24,s[48:51],s83 offen lds
v_mfma_f32_16x16x32_bf16 v[160:163],v[84:87],v[36:39],v[160:163]
ds_read_b64_tr_b16 v[64:65],v1
ds_read_b64_tr_b16 v[66:67],v1 offset:5120
v_mfma_f32_16x16x32_bf16 v[160:163],v[88:91],v[40:43],v[160:163]
ds_read_b64_tr_b16 v[68:69],v1 offset:10240
v_mfma_f32_16x16x32_bf16 v[160:163],v[92:95],v[44:47],v[160:163]
ds_read_b64_tr_b16 v[70:71],v1 offset:15360
v_mfma_f32_16x16x32_bf16 v[168:171],v[92:95],v[180:183],v[168:171]
ds_read_b64_tr_b16 v[72:73],v1 offset:20480
v_mfma_f32_16x16x32_bf16 v[168:171],v[88:91],v[164:167],v[168:171]
buffer_load_dwordx4 a[156:159],v28,s[28:31],s88 offen offset:3072 sc1
v_mfma_f32_16x16x32_bf16 v[168:171],v[84:87],v[148:151],v[168:171]
ds_read_b64_tr_b16 v[74:75],v1 offset:25600
ds_read_b64_tr_b16 v[76:77],v1 offset:30720
v_mfma_f32_16x16x32_bf16 v[168:171],v[80:83],v[132:135],v[168:171]
ds_read_b64_tr_b16 v[78:79],v1 offset:35840
v_mfma_f32_16x16x32_bf16 v[176:179],v[80:83],v[196:199],v[176:179]
v_mfma_f32_16x16x32_bf16 v[176:179],v[84:87],v[212:215],v[176:179]
v_mfma_f32_16x16x32_bf16 v[176:179],v[88:91],v[228:231],v[176:179]
v_mfma_f32_16x16x32_bf16 v[176:179],v[92:95],v[244:247],v[176:179]
buffer_load_dwordx4 v25,s[48:51],s83 offen offset:1024 lds
v_mfma_f32_16x16x32_bf16 v[184:187],v[92:95],v[188:191],v[184:187]
ds_read_b64_tr_b16 v[92:93],v2 offset:30752
ds_read_b64_tr_b16 v[94:95],v2 offset:35872
v_mfma_f32_16x16x32_bf16 v[184:187],v[88:91],v[172:175],v[184:187]
ds_read_b64_tr_b16 v[88:89],v2 offset:20512
v_mfma_f32_16x16x32_bf16 v[184:187],v[84:87],v[156:159],v[184:187]
ds_read_b64_tr_b16 v[84:85],v2 offset:10272
v_mfma_f32_16x16x32_bf16 v[184:187],v[80:83],v[140:143],v[184:187]
ds_read_b64_tr_b16 v[80:81],v2 offset:32
v_mfma_f32_16x16x32_bf16 v[192:195],v[96:99],v[32:35],v[192:195]
buffer_load_dwordx4 a[140:143],v28,s[28:31],s87 offen offset:3072 sc1
v_mfma_f32_16x16x32_bf16 v[192:195],v[100:103],v[36:39],v[192:195]
s_add_u32 s87,s64,2
s_and_b32 s87,s87,3
s_lshl_b32 s87,s87,1
v_mfma_f32_16x16x32_bf16 v[192:195],v[104:107],v[40:43],v[192:195]
s_lshl_b32 s87,s87,16
s_add_u32 s88,s87,4096
s_add_u32 s89,s87,8192
v_mfma_f32_16x16x32_bf16 v[192:195],v[108:111],v[44:47],v[192:195]
s_add_u32 s90,s87,12288
ds_read_b64_tr_b16 v[82:83],v2 offset:5152
ds_read_b64_tr_b16 v[86:87],v2 offset:15392
v_mfma_f32_16x16x32_bf16 v[200:203],v[108:111],v[180:183],v[200:203]
ds_read_b64_tr_b16 v[90:91],v2 offset:25632
v_mfma_f32_16x16x32_bf16 v[200:203],v[104:107],v[164:167],v[200:203]
buffer_load_dwordx4 a[68:71],v6,s[28:31],s87 offen sc1
v_mfma_f32_16x16x32_bf16 v[200:203],v[100:103],v[148:151],v[200:203]
v_mfma_f32_16x16x32_bf16 v[200:203],v[96:99],v[132:135],v[200:203]
v_mfma_f32_16x16x32_bf16 v[208:211],v[96:99],v[196:199],v[208:211]
v_mfma_f32_16x16x32_bf16 v[208:211],v[100:103],v[212:215],v[208:211]
v_mfma_f32_16x16x32_bf16 v[208:211],v[104:107],v[228:231],v[208:211]
buffer_load_dwordx4 a[76:79],v6,s[28:31],s88 offen sc1
v_mfma_f32_16x16x32_bf16 v[208:211],v[108:111],v[244:247],v[208:211]
)A"
R"A(v_mfma_f32_16x16x32_bf16 v[216:219],v[108:111],v[188:191],v[216:219]
ds_read_b64_tr_b16 v[108:109],v1 offset:30784
ds_read_b64_tr_b16 v[110:111],v1 offset:35904
v_mfma_f32_16x16x32_bf16 v[216:219],v[104:107],v[172:175],v[216:219]
ds_read_b64_tr_b16 v[104:105],v1 offset:20544
v_mfma_f32_16x16x32_bf16 v[216:219],v[100:103],v[156:159],v[216:219]
ds_read_b64_tr_b16 v[100:101],v1 offset:10304
v_mfma_f32_16x16x32_bf16 v[216:219],v[96:99],v[140:143],v[216:219]
buffer_load_dwordx4 a[84:87],v6,s[28:31],s89 offen sc1
v_mfma_f32_16x16x32_bf16 v[224:227],v[112:115],v[32:35],v[224:227]
ds_read_b64_tr_b16 v[96:97],v1 offset:64
ds_read_b64_tr_b16 v[98:99],v1 offset:5184
v_mfma_f32_16x16x32_bf16 v[224:227],v[116:119],v[36:39],v[224:227]
ds_read_b64_tr_b16 v[102:103],v1 offset:15424
v_mfma_f32_16x16x32_bf16 v[224:227],v[120:123],v[40:43],v[224:227]
ds_read_b64_tr_b16 v[106:107],v1 offset:25664
v_mfma_f32_16x16x32_bf16 v[224:227],v[124:127],v[44:47],v[224:227]
v_mfma_f32_16x16x32_bf16 v[232:235],v[124:127],v[180:183],v[232:235]
buffer_load_dwordx4 a[92:95],v6,s[28:31],s90 offen sc1
v_mfma_f32_16x16x32_bf16 v[232:235],v[120:123],v[164:167],v[232:235]
v_mfma_f32_16x16x32_bf16 v[232:235],v[116:119],v[148:151],v[232:235]
v_mfma_f32_16x16x32_bf16 v[232:235],v[112:115],v[132:135],v[232:235]
v_mfma_f32_16x16x32_bf16 v[240:243],v[112:115],v[196:199],v[240:243]
v_mfma_f32_16x16x32_bf16 v[240:243],v[116:119],v[212:215],v[240:243]
buffer_load_dwordx4 a[124:127],v6,s[28:31],s90 offen offset:1024 sc1
v_mfma_f32_16x16x32_bf16 v[240:243],v[120:123],v[228:231],v[240:243]
v_mfma_f32_16x16x32_bf16 v[240:243],v[124:127],v[244:247],v[240:243]
v_mfma_f32_16x16x32_bf16 v[248:251],v[124:127],v[188:191],v[248:251]
ds_read_b64_tr_b16 v[124:125],v2 offset:30816
ds_read_b64_tr_b16 v[126:127],v2 offset:35936
v_mfma_f32_16x16x32_bf16 v[248:251],v[120:123],v[172:175],v[248:251]
ds_read_b64_tr_b16 v[120:121],v2 offset:20576
v_mfma_f32_16x16x32_bf16 v[248:251],v[116:119],v[156:159],v[248:251]
buffer_load_dwordx4 a[116:119],v6,s[28:31],s89 offen offset:1024 sc1
v_mfma_f32_16x16x32_bf16 v[248:251],v[112:115],v[140:143],v[248:251]
s_waitcnt vmcnt(31) lgkmcnt(3)
ds_read_b64_tr_b16 v[112:113],v2 offset:96
ds_read_b64_tr_b16 v[114:115],v2 offset:5216
v_mfma_f32_16x16x32_bf16 a[192:195],v[64:67],a[128:131],a[192:195]
ds_read_b64_tr_b16 v[116:117],v2 offset:10336
v_mfma_f32_16x16x32_bf16 a[192:195],v[68:71],a[144:147],a[192:195]
s_waitcnt vmcnt(29)
ds_read_b64_tr_b16 v[118:119],v2 offset:15456
v_mfma_f32_16x16x32_bf16 a[192:195],v[72:75],a[160:163],a[192:195]
s_waitcnt vmcnt(25)
ds_read_b64_tr_b16 v[122:123],v2 offset:25696
v_mfma_f32_16x16x32_bf16 a[192:195],v[76:79],a[176:179],a[192:195]
buffer_load_dwordx4 a[108:111],v6,s[28:31],s88 offen offset:1024 sc1
v_mfma_f32_16x16x32_bf16 a[196:199],v[76:79],a[180:183],a[196:199]
s_waitcnt vmcnt(24)
v_mfma_f32_16x16x32_bf16 a[196:199],v[72:75],a[164:167],a[196:199]
s_waitcnt vmcnt(22)
v_mfma_f32_16x16x32_bf16 a[196:199],v[68:71],a[148:151],a[196:199]
s_waitcnt vmcnt(19)
v_mfma_f32_16x16x32_bf16 a[196:199],v[64:67],a[132:135],a[196:199]
s_waitcnt vmcnt(17)
v_mfma_f32_16x16x32_bf16 a[200:203],v[64:67],a[136:139],a[200:203]
buffer_load_dwordx4 a[100:103],v6,s[28:31],s87 offen offset:1024 sc1
v_mfma_f32_16x16x32_bf16 a[200:203],v[68:71],a[152:155],a[200:203]
s_waitcnt vmcnt(16)
v_mfma_f32_16x16x32_bf16 a[200:203],v[72:75],a[168:171],a[200:203]
s_waitcnt vmcnt(14)
v_mfma_f32_16x16x32_bf16 a[200:203],v[76:79],a[184:187],a[200:203]
s_waitcnt vmcnt(13)
v_mfma_f32_16x16x32_bf16 a[204:207],v[76:79],a[188:191],a[204:207]
s_waitcnt vmcnt(10)
ds_read_b64_tr_b16 v[76:77],v1 offset:30848
ds_read_b64_tr_b16 v[78:79],v1 offset:35968
v_mfma_f32_16x16x32_bf16 a[204:207],v[72:75],a[172:175],a[204:207]
buffer_load_dwordx4 a[64:67],v6,s[28:31],s87 offen offset:2048 sc1
v_mfma_f32_16x16x32_bf16 a[204:207],v[68:71],a[156:159],a[204:207]
s_waitcnt vmcnt(9)
ds_read_b64_tr_b16 v[68:69],v1 offset:10368
ds_read_b64_tr_b16 v[70:71],v1 offset:15488
v_mfma_f32_16x16x32_bf16 a[204:207],v[64:67],a[140:143],a[204:207]
ds_read_b64_tr_b16 v[64:65],v1 offset:128
v_mfma_f32_16x16x32_bf16 a[208:211],v[80:83],a[128:131],a[208:211]
ds_read_b64_tr_b16 v[66:67],v1 offset:5248
v_mfma_f32_16x16x32_bf16 a[208:211],v[84:87],a[144:147],a[208:211]
ds_read_b64_tr_b16 v[72:73],v1 offset:20608
v_mfma_f32_16x16x32_bf16 a[208:211],v[88:91],a[160:163],a[208:211]
ds_read_b64_tr_b16 v[74:75],v1 offset:25728
v_mfma_f32_16x16x32_bf16 a[208:211],v[92:95],a[176:179],a[208:211]
buffer_load_dwordx4 v26,s[48:51],s83 offen offset:2048 lds
v_mfma_f32_16x16x32_bf16 a[212:215],v[92:95],a[180:183],a[212:215]
v_mfma_f32_16x16x32_bf16 a[212:215],v[88:91],a[164:167],a[212:215]
v_mfma_f32_16x16x32_bf16 a[212:215],v[84:87],a[148:151],a[212:215]
v_mfma_f32_16x16x32_bf16 a[212:215],v[80:83],a[132:135],a[212:215]
v_mfma_f32_16x16x32_bf16 a[216:219],v[80:83],a[136:139],a[216:219]
buffer_load_dwordx4 a[72:75],v6,s[28:31],s88 offen offset:2048 sc1
v_mfma_f32_16x16x32_bf16 a[216:219],v[84:87],a[152:155],a[216:219]
v_mfma_f32_16x16x32_bf16 a[216:219],v[88:91],a[168:171],a[216:219]
v_mfma_f32_16x16x32_bf16 a[216:219],v[92:95],a[184:187],a[216:219]
v_mfma_f32_16x16x32_bf16 a[220:223],v[92:95],a[188:191],a[220:223]
ds_read_b64_tr_b16 v[92:93],v2 offset:30880
ds_read_b64_tr_b16 v[94:95],v2 offset:36000
v_mfma_f32_16x16x32_bf16 a[220:223],v[88:91],a[172:175],a[220:223]
ds_read_b64_tr_b16 v[88:89],v2 offset:20640
v_mfma_f32_16x16x32_bf16 a[220:223],v[84:87],a[156:159],a[220:223]
buffer_load_dwordx4 v27,s[48:51],s83 offen offset:3072 lds
v_mfma_f32_16x16x32_bf16 a[220:223],v[80:83],a[140:143],a[220:223]
s_add_u32 m0,s86,126976
ds_read_b64_tr_b16 v[80:81],v2 offset:160
ds_read_b64_tr_b16 v[82:83],v2 offset:5280
s_barrier
v_mfma_f32_16x16x32_bf16 a[224:227],v[96:99],a[128:131],a[224:227]
ds_read_b64_tr_b16 v[84:85],v2 offset:10400
v_mfma_f32_16x16x32_bf16 a[224:227],v[100:103],a[144:147],a[224:227]
ds_read_b64_tr_b16 v[86:87],v2 offset:15520
v_mfma_f32_16x16x32_bf16 a[224:227],v[104:107],a[160:163],a[224:227]
buffer_store_dwordx2 v[8:9],v10,s[44:47],s18 offen offset:1280 sc1
v_mfma_f32_16x16x32_bf16 a[224:227],v[108:111],a[176:179],a[224:227]
ds_read_b64_tr_b16 v[90:91],v2 offset:25760
v_mfma_f32_16x16x32_bf16 a[228:231],v[108:111],a[180:183],a[228:231]
v_mfma_f32_16x16x32_bf16 a[228:231],v[104:107],a[164:167],a[228:231]
v_mfma_f32_16x16x32_bf16 a[228:231],v[100:103],a[148:151],a[228:231]
v_mfma_f32_16x16x32_bf16 a[228:231],v[96:99],a[132:135],a[228:231]
buffer_load_dwordx4 a[80:83],v6,s[28:31],s89 offen offset:2048 sc1
v_mfma_f32_16x16x32_bf16 a[232:235],v[96:99],a[136:139],a[232:235]
v_mfma_f32_16x16x32_bf16 a[232:235],v[100:103],a[152:155],a[232:235]
v_mfma_f32_16x16x32_bf16 a[232:235],v[104:107],a[168:171],a[232:235]
v_mfma_f32_16x16x32_bf16 a[232:235],v[108:111],a[184:187],a[232:235]
v_mfma_f32_16x16x32_bf16 a[236:239],v[108:111],a[188:191],a[236:239]
s_waitcnt lgkmcnt(0)
ds_read_b64_tr_b16 v[108:109],v1 offset:30912
ds_read_b64_tr_b16 v[110:111],v1 offset:36032
v_mfma_f32_16x16x32_bf16 a[236:239],v[104:107],a[172:175],a[236:239]
buffer_load_dwordx4 v7,s[48:51],s83 offen lds
v_mfma_f32_16x16x32_bf16 a[236:239],v[100:103],a[156:159],a[236:239]
s_add_u32 m0,s86,128000
ds_read_b64_tr_b16 v[100:101],v1 offset:10432
ds_read_b64_tr_b16 v[102:103],v1 offset:15552
v_mfma_f32_16x16x32_bf16 a[236:239],v[96:99],a[140:143],a[236:239]
s_add_u32 s83,s64,1
s_and_b32 s83,s83,3
ds_read_b64_tr_b16 v[96:97],v1 offset:192
v_mfma_f32_16x16x32_bf16 a[240:243],v[112:115],a[128:131],a[240:243]
s_lshl_b32 s83,s83,1
s_or_b32 s83,s83,1
ds_read_b64_tr_b16 v[98:99],v1 offset:5312
v_mfma_f32_16x16x32_bf16 a[240:243],v[116:119],a[144:147],a[240:243]
s_mul_i32 s83,s83,163840
s_add_u32 s83,s83,s85
ds_read_b64_tr_b16 v[104:105],v1 offset:20672
v_mfma_f32_16x16x32_bf16 a[240:243],v[120:123],a[160:163],a[240:243]
buffer_load_dwordx4 a[88:91],v6,s[28:31],s90 offen offset:2048 sc1
v_mfma_f32_16x16x32_bf16 a[240:243],v[124:127],a[176:179],a[240:243]
ds_read_b64_tr_b16 v[106:107],v1 offset:25792
v_mfma_f32_16x16x32_bf16 a[244:247],v[124:127],a[180:183],a[244:247]
v_mfma_f32_16x16x32_bf16 a[244:247],v[120:123],a[164:167],a[244:247]
v_mfma_f32_16x16x32_bf16 a[244:247],v[116:119],a[148:151],a[244:247]
v_mfma_f32_16x16x32_bf16 a[244:247],v[112:115],a[132:135],a[244:247]
v_mfma_f32_16x16x32_bf16 a[248:251],v[112:115],a[136:139],a[248:251]
buffer_load_dwordx4 v24,s[48:51],s84 offen lds
v_mfma_f32_16x16x32_bf16 a[248:251],v[116:119],a[152:155],a[248:251]
v_mfma_f32_16x16x32_bf16 a[248:251],v[120:123],a[168:171],a[248:251]
v_mfma_f32_16x16x32_bf16 a[248:251],v[124:127],a[184:187],a[248:251]
v_mfma_f32_16x16x32_bf16 a[252:255],v[124:127],a[188:191],a[252:255]
ds_read_b64_tr_b16 v[124:125],v2 offset:30944
ds_read_b64_tr_b16 v[126:127],v2 offset:36064
v_mfma_f32_16x16x32_bf16 a[252:255],v[120:123],a[172:175],a[252:255]
buffer_load_dwordx4 a[120:123],v28,s[28:31],s90 offen offset:3072 sc1
v_mfma_f32_16x16x32_bf16 a[252:255],v[116:119],a[156:159],a[252:255]
ds_read_b64_tr_b16 v[116:117],v2 offset:10464
ds_read_b64_tr_b16 v[118:119],v2 offset:15584
v_mfma_f32_16x16x32_bf16 a[252:255],v[112:115],a[140:143],a[252:255]
ds_read_b64_tr_b16 v[112:113],v2 offset:224
v_mfma_f32_16x16x32_bf16 a[0:3],v[64:67],a[128:131],a[0:3]
ds_read_b64_tr_b16 v[114:115],v2 offset:5344
v_mfma_f32_16x16x32_bf16 a[0:3],v[68:71],a[144:147],a[0:3]
ds_read_b64_tr_b16 v[120:121],v2 offset:20704
v_mfma_f32_16x16x32_bf16 a[0:3],v[72:75],a[160:163],a[0:3]
ds_read_b64_tr_b16 v[122:123],v2 offset:25824
v_mfma_f32_16x16x32_bf16 a[0:3],v[76:79],a[176:179],a[0:3]
buffer_load_dwordx4 v25,s[48:51],s84 offen offset:1024 lds
v_mfma_f32_16x16x32_bf16 a[4:7],v[76:79],a[180:183],a[4:7]
v_mfma_f32_16x16x32_bf16 a[4:7],v[72:75],a[164:167],a[4:7]
v_mfma_f32_16x16x32_bf16 a[4:7],v[68:71],a[148:151],a[4:7]
v_mfma_f32_16x16x32_bf16 a[4:7],v[64:67],a[132:135],a[4:7]
v_mfma_f32_16x16x32_bf16 a[8:11],v[64:67],a[136:139],a[8:11]
buffer_load_dwordx4 a[112:115],v28,s[28:31],s89 offen offset:3072 sc1
v_mfma_f32_16x16x32_bf16 a[8:11],v[68:71],a[152:155],a[8:11]
v_mfma_f32_16x16x32_bf16 a[8:11],v[72:75],a[168:171],a[8:11]
v_mfma_f32_16x16x32_bf16 a[8:11],v[76:79],a[184:187],a[8:11]
v_mfma_f32_16x16x32_bf16 a[12:15],v[76:79],a[188:191],a[12:15]
ds_read_b64_tr_b16 v[76:77],v1 offset:30976
ds_read_b64_tr_b16 v[78:79],v1 offset:36096
v_mfma_f32_16x16x32_bf16 a[12:15],v[72:75],a[172:175],a[12:15]
buffer_load_dwordx4 a[104:107],v28,s[28:31],s88 offen offset:3072 sc1
v_mfma_f32_16x16x32_bf16 a[12:15],v[68:71],a[156:159],a[12:15]
ds_read_b64_tr_b16 v[68:69],v1 offset:10496
ds_read_b64_tr_b16 v[70:71],v1 offset:15616
v_mfma_f32_16x16x32_bf16 a[12:15],v[64:67],a[140:143],a[12:15]
ds_read_b64_tr_b16 v[64:65],v1 offset:256
v_mfma_f32_16x16x32_bf16 a[16:19],v[80:83],a[128:131],a[16:19]
ds_read_b64_tr_b16 v[66:67],v1 offset:5376
v_mfma_f32_16x16x32_bf16 a[16:19],v[84:87],a[144:147],a[16:19]
ds_read_b64_tr_b16 v[72:73],v1 offset:20736
v_mfma_f32_16x16x32_bf16 a[16:19],v[88:91],a[160:163],a[16:19]
ds_read_b64_tr_b16 v[74:75],v1 offset:25856
v_add_u32 v1,0xffff6000,v1
v_mfma_f32_16x16x32_bf16 a[16:19],v[92:95],a[176:179],a[16:19]
buffer_load_dwordx4 v26,s[48:51],s84 offen offset:2048 lds
v_mfma_f32_16x16x32_bf16 a[20:23],v[92:95],a[180:183],a[20:23]
v_mfma_f32_16x16x32_bf16 a[20:23],v[88:91],a[164:167],a[20:23]
v_mfma_f32_16x16x32_bf16 a[20:23],v[84:87],a[148:151],a[20:23]
v_mfma_f32_16x16x32_bf16 a[20:23],v[80:83],a[132:135],a[20:23]
v_mfma_f32_16x16x32_bf16 a[24:27],v[80:83],a[136:139],a[24:27]
buffer_load_dwordx4 a[96:99],v28,s[28:31],s87 offen offset:3072 sc1
v_mfma_f32_16x16x32_bf16 a[24:27],v[84:87],a[152:155],a[24:27]
s_add_u32 s87,s64,3
s_and_b32 s87,s87,3
s_lshl_b32 s87,s87,1
v_mfma_f32_16x16x32_bf16 a[24:27],v[88:91],a[168:171],a[24:27]
s_lshl_b32 s87,s87,16
s_add_u32 s88,s87,4096
s_add_u32 s89,s87,8192
v_mfma_f32_16x16x32_bf16 a[24:27],v[92:95],a[184:187],a[24:27]
s_add_u32 s90,s87,12288
v_mfma_f32_16x16x32_bf16 a[28:31],v[92:95],a[188:191],a[28:31]
ds_read_b64_tr_b16 v[92:93],v2 offset:31008
)A"
R"A(ds_read_b64_tr_b16 v[94:95],v2 offset:36128
v_mfma_f32_16x16x32_bf16 a[28:31],v[88:91],a[172:175],a[28:31]
s_waitcnt lgkmcnt(2)
ds_read_b64_tr_b16 v[88:89],v2 offset:20768
v_mfma_f32_16x16x32_bf16 a[28:31],v[84:87],a[156:159],a[28:31]
buffer_load_dwordx4 v27,s[48:51],s84 offen offset:3072 lds
v_mfma_f32_16x16x32_bf16 a[28:31],v[80:83],a[140:143],a[28:31]
s_add_u32 m0,s86,132096
ds_read_b64_tr_b16 v[80:81],v2 offset:288
ds_read_b64_tr_b16 v[82:83],v2 offset:5408
v_mfma_f32_16x16x32_bf16 v[128:131],v[96:99],a[128:131],v[128:131]
ds_read_b64_tr_b16 v[84:85],v2 offset:10528
v_mfma_f32_16x16x32_bf16 v[128:131],v[100:103],a[144:147],v[128:131]
ds_read_b64_tr_b16 v[86:87],v2 offset:15648
v_mfma_f32_16x16x32_bf16 v[128:131],v[104:107],a[160:163],v[128:131]
ds_read_b64_tr_b16 v[90:91],v2 offset:25888
v_add_u32 v2,0xffff6000,v2
v_mfma_f32_16x16x32_bf16 v[128:131],v[108:111],a[176:179],v[128:131]
v_mfma_f32_16x16x32_bf16 v[136:139],v[108:111],a[180:183],v[136:139]
buffer_load_dwordx4 v7,s[48:51],s84 offen lds
v_mfma_f32_16x16x32_bf16 v[136:139],v[104:107],a[164:167],v[136:139]
s_add_u32 m0,s86,81920
s_add_u32 s84,s83,20480
v_mfma_f32_16x16x32_bf16 v[136:139],v[100:103],a[148:151],v[136:139]
v_mfma_f32_16x16x32_bf16 v[136:139],v[96:99],a[132:135],v[136:139]
v_mfma_f32_16x16x32_bf16 v[144:147],v[96:99],a[136:139],v[144:147]
v_mfma_f32_16x16x32_bf16 v[144:147],v[100:103],a[152:155],v[144:147]
v_mfma_f32_16x16x32_bf16 v[144:147],v[104:107],a[168:171],v[144:147]
buffer_load_dwordx4 v24,s[48:51],s83 offen lds
v_mfma_f32_16x16x32_bf16 v[144:147],v[108:111],a[184:187],v[144:147]
v_mfma_f32_16x16x32_bf16 v[152:155],v[108:111],a[188:191],v[152:155]
ds_read_b64_tr_b16 v[108:109],v1 offset:30720
ds_read_b64_tr_b16 v[110:111],v1 offset:35840
v_mfma_f32_16x16x32_bf16 v[152:155],v[104:107],a[172:175],v[152:155]
ds_read_b64_tr_b16 v[104:105],v1 offset:20480
v_mfma_f32_16x16x32_bf16 v[152:155],v[100:103],a[156:159],v[152:155]
ds_read_b64_tr_b16 v[100:101],v1 offset:10240
v_mfma_f32_16x16x32_bf16 v[152:155],v[96:99],a[140:143],v[152:155]
ds_read_b64_tr_b16 v[96:97],v1
v_mfma_f32_16x16x32_bf16 v[160:163],v[112:115],a[128:131],v[160:163]
buffer_load_dwordx4 v25,s[48:51],s83 offen offset:1024 lds
v_mfma_f32_16x16x32_bf16 v[160:163],v[116:119],a[144:147],v[160:163]
ds_read_b64_tr_b16 v[98:99],v1 offset:5120
ds_read_b64_tr_b16 v[102:103],v1 offset:15360
v_mfma_f32_16x16x32_bf16 v[160:163],v[120:123],a[160:163],v[160:163]
ds_read_b64_tr_b16 v[106:107],v1 offset:25600
v_mfma_f32_16x16x32_bf16 v[160:163],v[124:127],a[176:179],v[160:163]
v_mfma_f32_16x16x32_bf16 v[168:171],v[124:127],a[180:183],v[168:171]
v_mfma_f32_16x16x32_bf16 v[168:171],v[120:123],a[164:167],v[168:171]
v_mfma_f32_16x16x32_bf16 v[168:171],v[116:119],a[148:151],v[168:171]
buffer_load_dwordx4 v26,s[48:51],s83 offen offset:2048 lds
v_mfma_f32_16x16x32_bf16 v[168:171],v[112:115],a[132:135],v[168:171]
v_mfma_f32_16x16x32_bf16 v[176:179],v[112:115],a[136:139],v[176:179]
v_mfma_f32_16x16x32_bf16 v[176:179],v[116:119],a[152:155],v[176:179]
v_mfma_f32_16x16x32_bf16 v[176:179],v[120:123],a[168:171],v[176:179]
v_mfma_f32_16x16x32_bf16 v[176:179],v[124:127],a[184:187],v[176:179]
v_mfma_f32_16x16x32_bf16 v[184:187],v[124:127],a[188:191],v[184:187]
buffer_load_dwordx4 v27,s[48:51],s83 offen offset:3072 lds
v_mfma_f32_16x16x32_bf16 v[184:187],v[120:123],a[172:175],v[184:187]
s_add_u32 m0,s86,86016
ds_read_b64_tr_b16 v[120:121],v2 offset:20512
ds_read_b64_tr_b16 v[122:123],v2 offset:25632
v_mfma_f32_16x16x32_bf16 v[184:187],v[116:119],a[156:159],v[184:187]
ds_read_b64_tr_b16 v[116:117],v2 offset:10272
v_mfma_f32_16x16x32_bf16 v[184:187],v[112:115],a[140:143],v[184:187]
ds_read_b64_tr_b16 v[112:113],v2 offset:32
v_mfma_f32_16x16x32_bf16 v[192:195],v[64:67],a[128:131],v[192:195]
ds_read_b64_tr_b16 v[114:115],v2 offset:5152
v_mfma_f32_16x16x32_bf16 v[192:195],v[68:71],a[144:147],v[192:195]
ds_read_b64_tr_b16 v[118:119],v2 offset:15392
ds_read_b64_tr_b16 v[124:125],v2 offset:30752
v_mfma_f32_16x16x32_bf16 v[192:195],v[72:75],a[160:163],v[192:195]
buffer_load_dwordx4 v7,s[48:51],s83 offen lds
v_mfma_f32_16x16x32_bf16 v[192:195],v[76:79],a[176:179],v[192:195]
s_add_u32 m0,s86,87040
s_add_u32 s83,s64,2
ds_read_b64_tr_b16 v[126:127],v2 offset:35872
v_mfma_f32_16x16x32_bf16 v[200:203],v[76:79],a[180:183],v[200:203]
s_and_b32 s83,s83,3
s_lshl_b32 s83,s83,1
s_or_b32 s83,s83,1
v_mfma_f32_16x16x32_bf16 v[200:203],v[72:75],a[164:167],v[200:203]
s_mul_i32 s83,s83,163840
s_add_u32 s83,s83,s85
v_mfma_f32_16x16x32_bf16 v[200:203],v[68:71],a[148:151],v[200:203]
v_mfma_f32_16x16x32_bf16 v[200:203],v[64:67],a[132:135],v[200:203]
v_mfma_f32_16x16x32_bf16 v[208:211],v[64:67],a[136:139],v[208:211]
buffer_load_dwordx4 v24,s[48:51],s84 offen lds
v_mfma_f32_16x16x32_bf16 v[208:211],v[68:71],a[152:155],v[208:211]
v_mfma_f32_16x16x32_bf16 v[208:211],v[72:75],a[168:171],v[208:211]
v_mfma_f32_16x16x32_bf16 v[208:211],v[76:79],a[184:187],v[208:211]
v_mfma_f32_16x16x32_bf16 v[216:219],v[76:79],a[188:191],v[216:219]
ds_read_b64_tr_b16 v[76:77],v1 offset:30784
ds_read_b64_tr_b16 v[78:79],v1 offset:35904
v_mfma_f32_16x16x32_bf16 v[216:219],v[72:75],a[172:175],v[216:219]
ds_read_b64_tr_b16 v[72:73],v1 offset:20544
v_mfma_f32_16x16x32_bf16 v[216:219],v[68:71],a[156:159],v[216:219]
buffer_load_dwordx2 v[4:5],v3,s[44:47],s91 offen offset:1280 sc1
v_mfma_f32_16x16x32_bf16 v[216:219],v[64:67],a[140:143],v[216:219]
s_waitcnt lgkmcnt(3)
ds_read_b64_tr_b16 v[64:65],v1 offset:64
ds_read_b64_tr_b16 v[66:67],v1 offset:5184
v_mfma_f32_16x16x32_bf16 v[224:227],v[80:83],a[128:131],v[224:227]
ds_read_b64_tr_b16 v[68:69],v1 offset:10304
v_mfma_f32_16x16x32_bf16 v[224:227],v[84:87],a[144:147],v[224:227]
ds_read_b64_tr_b16 v[70:71],v1 offset:15424
v_mfma_f32_16x16x32_bf16 v[224:227],v[88:91],a[160:163],v[224:227]
ds_read_b64_tr_b16 v[74:75],v1 offset:25664
v_mfma_f32_16x16x32_bf16 v[224:227],v[92:95],a[176:179],v[224:227]
v_mfma_f32_16x16x32_bf16 v[232:235],v[92:95],a[180:183],v[232:235]
buffer_load_dwordx4 v25,s[48:51],s84 offen offset:1024 lds
v_mfma_f32_16x16x32_bf16 v[232:235],v[88:91],a[164:167],v[232:235]
v_mfma_f32_16x16x32_bf16 v[232:235],v[84:87],a[148:151],v[232:235]
v_mfma_f32_16x16x32_bf16 v[232:235],v[80:83],a[132:135],v[232:235]
v_mfma_f32_16x16x32_bf16 v[240:243],v[80:83],a[136:139],v[240:243]
v_mfma_f32_16x16x32_bf16 v[240:243],v[84:87],a[152:155],v[240:243]
v_mfma_f32_16x16x32_bf16 v[240:243],v[88:91],a[168:171],v[240:243]
buffer_load_dwordx4 v26,s[48:51],s84 offen offset:2048 lds
v_mfma_f32_16x16x32_bf16 v[240:243],v[92:95],a[184:187],v[240:243]
v_mfma_f32_16x16x32_bf16 v[248:251],v[92:95],a[188:191],v[248:251]
ds_read_b64_tr_b16 v[92:93],v2 offset:30816
ds_read_b64_tr_b16 v[94:95],v2 offset:35936
v_mfma_f32_16x16x32_bf16 v[248:251],v[88:91],a[172:175],v[248:251]
ds_read_b64_tr_b16 v[88:89],v2 offset:20576
v_mfma_f32_16x16x32_bf16 v[248:251],v[84:87],a[156:159],v[248:251]
ds_read_b64_tr_b16 v[84:85],v2 offset:10336
v_mfma_f32_16x16x32_bf16 v[248:251],v[80:83],a[140:143],v[248:251]
s_waitcnt vmcnt(25)
ds_read_b64_tr_b16 v[80:81],v2 offset:96
v_mfma_f32_16x16x32_bf16 a[192:195],v[96:99],a[68:71],a[192:195]
buffer_load_dwordx4 v27,s[48:51],s84 offen offset:3072 lds
v_mfma_f32_16x16x32_bf16 a[192:195],v[100:103],a[76:79],a[192:195]
s_add_u32 m0,s86,91136
ds_read_b64_tr_b16 v[82:83],v2 offset:5216
ds_read_b64_tr_b16 v[86:87],v2 offset:15456
v_mfma_f32_16x16x32_bf16 a[192:195],v[104:107],a[84:87],a[192:195]
ds_read_b64_tr_b16 v[90:91],v2 offset:25696
v_mfma_f32_16x16x32_bf16 a[192:195],v[108:111],a[92:95],a[192:195]
v_mfma_f32_16x16x32_bf16 a[196:199],v[108:111],a[124:127],a[196:199]
v_mfma_f32_16x16x32_bf16 a[196:199],v[104:107],a[116:119],a[196:199]
v_mfma_f32_16x16x32_bf16 a[196:199],v[100:103],a[108:111],a[196:199]
buffer_load_dwordx4 v7,s[48:51],s84 offen lds
v_mfma_f32_16x16x32_bf16 a[196:199],v[96:99],a[100:103],a[196:199]
s_add_u32 m0,s86,40960
s_add_u32 s84,s83,20480
v_mfma_f32_16x16x32_bf16 a[200:203],v[96:99],a[64:67],a[200:203]
s_waitcnt vmcnt(25)
v_mfma_f32_16x16x32_bf16 a[200:203],v[100:103],a[72:75],a[200:203]
s_waitcnt vmcnt(22)
v_mfma_f32_16x16x32_bf16 a[200:203],v[104:107],a[80:83],a[200:203]
s_waitcnt vmcnt(18)
v_mfma_f32_16x16x32_bf16 a[200:203],v[108:111],a[88:91],a[200:203]
buffer_load_dwordx4 v[32:35],v6,s[28:31],s87 offen sc1
v_mfma_f32_16x16x32_bf16 a[204:207],v[108:111],a[120:123],a[204:207]
s_waitcnt vmcnt(17)
ds_read_b64_tr_b16 v[108:109],v1 offset:30848
ds_read_b64_tr_b16 v[110:111],v1 offset:35968
v_mfma_f32_16x16x32_bf16 a[204:207],v[104:107],a[112:115],a[204:207]
s_waitcnt vmcnt(16)
ds_read_b64_tr_b16 v[104:105],v1 offset:20608
s_nop 0
L_pd15_%=:
s_cbranch_execz L_trt1_%=
v_mfma_f32_16x16x32_bf16 a[204:207],v[100:103],a[104:107],a[204:207]
s_waitcnt vmcnt(14)
ds_read_b64_tr_b16 v[100:101],v1 offset:10368
v_mfma_f32_16x16x32_bf16 a[204:207],v[96:99],a[96:99],a[204:207]
ds_read_b64_tr_b16 v[96:97],v1 offset:128
v_mfma_f32_16x16x32_bf16 a[208:211],v[112:115],a[68:71],a[208:211]
buffer_load_dwordx4 v[132:135],v6,s[28:31],s87 offen offset:1024 sc1
v_mfma_f32_16x16x32_bf16 a[208:211],v[116:119],a[76:79],a[208:211]
ds_read_b64_tr_b16 v[98:99],v1 offset:5248
ds_read_b64_tr_b16 v[102:103],v1 offset:15488
v_mfma_f32_16x16x32_bf16 a[208:211],v[120:123],a[84:87],a[208:211]
ds_read_b64_tr_b16 v[106:107],v1 offset:25728
v_mfma_f32_16x16x32_bf16 a[208:211],v[124:127],a[92:95],a[208:211]
v_mfma_f32_16x16x32_bf16 a[212:215],v[124:127],a[124:127],a[212:215]
v_mfma_f32_16x16x32_bf16 a[212:215],v[120:123],a[116:119],a[212:215]
buffer_load_dwordx4 v[196:199],v6,s[28:31],s87 offen offset:2048 sc1
v_mfma_f32_16x16x32_bf16 a[212:215],v[116:119],a[108:111],a[212:215]
v_mfma_f32_16x16x32_bf16 a[212:215],v[112:115],a[100:103],a[212:215]
v_mfma_f32_16x16x32_bf16 a[216:219],v[112:115],a[64:67],a[216:219]
v_mfma_f32_16x16x32_bf16 a[216:219],v[116:119],a[72:75],a[216:219]
v_mfma_f32_16x16x32_bf16 a[216:219],v[120:123],a[80:83],a[216:219]
buffer_load_dwordx4 v[36:39],v6,s[28:31],s88 offen sc1
v_mfma_f32_16x16x32_bf16 a[216:219],v[124:127],a[88:91],a[216:219]
v_mfma_f32_16x16x32_bf16 a[220:223],v[124:127],a[120:123],a[220:223]
ds_read_b64_tr_b16 v[124:125],v2 offset:30880
ds_read_b64_tr_b16 v[126:127],v2 offset:36000
v_mfma_f32_16x16x32_bf16 a[220:223],v[120:123],a[112:115],a[220:223]
ds_read_b64_tr_b16 v[120:121],v2 offset:20640
v_mfma_f32_16x16x32_bf16 a[220:223],v[116:119],a[104:107],a[220:223]
s_waitcnt lgkmcnt(3)
ds_read_b64_tr_b16 v[116:117],v2 offset:10400
v_mfma_f32_16x16x32_bf16 a[220:223],v[112:115],a[96:99],a[220:223]
buffer_load_dwordx4 v[140:143],v28,s[28:31],s87 offen offset:3072 sc1
v_mfma_f32_16x16x32_bf16 a[224:227],v[64:67],a[68:71],a[224:227]
s_add_u32 s87,s64,1
s_and_b32 s87,s87,3
s_lshl_b32 s87,s87,1
v_mfma_f32_16x16x32_bf16 a[224:227],v[68:71],a[76:79],a[224:227]
s_or_b32 s87,s87,1
s_lshl_b32 s87,s87,16
ds_read_b64_tr_b16 v[112:113],v2 offset:160
v_mfma_f32_16x16x32_bf16 a[224:227],v[72:75],a[84:87],a[224:227]
ds_read_b64_tr_b16 v[114:115],v2 offset:5280
ds_read_b64_tr_b16 v[118:119],v2 offset:15520
v_mfma_f32_16x16x32_bf16 a[224:227],v[76:79],a[92:95],a[224:227]
ds_read_b64_tr_b16 v[122:123],v2 offset:25760
v_mfma_f32_16x16x32_bf16 a[228:231],v[76:79],a[124:127],a[228:231]
buffer_load_dwordx4 v[212:215],v6,s[28:31],s88 offen offset:2048 sc1
v_mfma_f32_16x16x32_bf16 a[228:231],v[72:75],a[116:119],a[228:231]
v_mfma_f32_16x16x32_bf16 a[228:231],v[68:71],a[108:111],a[228:231]
v_mfma_f32_16x16x32_bf16 a[228:231],v[64:67],a[100:103],a[228:231]
v_mfma_f32_16x16x32_bf16 a[232:235],v[64:67],a[64:67],a[232:235]
v_mfma_f32_16x16x32_bf16 a[232:235],v[68:71],a[72:75],a[232:235]
buffer_load_dwordx4 v[148:151],v6,s[28:31],s88 offen offset:1024 sc1
v_mfma_f32_16x16x32_bf16 a[232:235],v[72:75],a[80:83],a[232:235]
s_waitcnt vmcnt(11)
v_cmp_ne_u32 s[56:57],s52,v4
v_cmp_ne_u32 s[58:59],s53,v5
v_mfma_f32_16x16x32_bf16 a[232:235],v[76:79],a[88:91],a[232:235]
s_or_b64 s[56:57],s[56:57],s[58:59]
s_cbranch_scc1 L_fr2_%=
L_fo2_%=:
v_mov_b32 v3,0
v_mfma_f32_16x16x32_bf16 a[236:239],v[76:79],a[120:123],a[236:239]
)A"
R"A(s_add_u32 s91,s64,3
ds_read_b64_tr_b16 v[76:77],v1 offset:30912
ds_read_b64_tr_b16 v[78:79],v1 offset:36032
v_mfma_f32_16x16x32_bf16 a[236:239],v[72:75],a[112:115],a[236:239]
s_and_b32 s91,s91,3
s_add_u32 s91,s91,s69
ds_read_b64_tr_b16 v[72:73],v1 offset:20672
v_mfma_f32_16x16x32_bf16 a[236:239],v[68:71],a[104:107],a[236:239]
buffer_load_dwordx4 v[40:43],v6,s[28:31],s89 offen sc1
v_mfma_f32_16x16x32_bf16 a[236:239],v[64:67],a[96:99],a[236:239]
s_add_u32 s91,s91,8
ds_read_b64_tr_b16 v[64:65],v1 offset:192
ds_read_b64_tr_b16 v[66:67],v1 offset:5312
v_mfma_f32_16x16x32_bf16 a[240:243],v[80:83],a[68:71],a[240:243]
s_lshl_b32 s91,s91,6
s_and_b32 s69,s63,s60
ds_read_b64_tr_b16 v[68:69],v1 offset:10432
v_mfma_f32_16x16x32_bf16 a[240:243],v[84:87],a[76:79],a[240:243]
s_cmp_eq_u32 s69,3
s_cselect_b32 s69,0x40000000,0
ds_read_b64_tr_b16 v[70:71],v1 offset:15552
v_mfma_f32_16x16x32_bf16 a[240:243],v[88:91],a[84:87],a[240:243]
s_add_u32 s68,s68,s69
ds_read_b64_tr_b16 v[74:75],v1 offset:25792
v_mfma_f32_16x16x32_bf16 a[240:243],v[92:95],a[92:95],a[240:243]
buffer_load_dwordx4 a[128:131],v6,s[28:31],s87 offen sc1
v_mfma_f32_16x16x32_bf16 a[244:247],v[92:95],a[124:127],a[244:247]
v_mfma_f32_16x16x32_bf16 a[244:247],v[88:91],a[116:119],a[244:247]
v_mfma_f32_16x16x32_bf16 a[244:247],v[84:87],a[108:111],a[244:247]
v_mfma_f32_16x16x32_bf16 a[244:247],v[80:83],a[100:103],a[244:247]
v_mfma_f32_16x16x32_bf16 a[248:251],v[80:83],a[64:67],a[248:251]
buffer_load_dwordx4 v[156:159],v28,s[28:31],s88 offen offset:3072 sc1
v_mfma_f32_16x16x32_bf16 a[248:251],v[84:87],a[72:75],a[248:251]
s_add_u32 s88,s87,4096
v_mfma_f32_16x16x32_bf16 a[248:251],v[88:91],a[80:83],a[248:251]
v_mfma_f32_16x16x32_bf16 a[248:251],v[92:95],a[88:91],a[248:251]
v_mfma_f32_16x16x32_bf16 a[252:255],v[92:95],a[120:123],a[252:255]
ds_read_b64_tr_b16 v[92:93],v2 offset:30944
ds_read_b64_tr_b16 v[94:95],v2 offset:36064
v_mfma_f32_16x16x32_bf16 a[252:255],v[88:91],a[112:115],a[252:255]
buffer_load_dwordx4 v[228:231],v6,s[28:31],s89 offen offset:2048 sc1
v_mfma_f32_16x16x32_bf16 a[252:255],v[84:87],a[104:107],a[252:255]
ds_read_b64_tr_b16 v[84:85],v2 offset:10464
ds_read_b64_tr_b16 v[86:87],v2 offset:15584
v_mfma_f32_16x16x32_bf16 a[252:255],v[80:83],a[96:99],a[252:255]
ds_read_b64_tr_b16 v[80:81],v2 offset:224
v_mfma_f32_16x16x32_bf16 a[0:3],v[96:99],a[68:71],a[0:3]
ds_read_b64_tr_b16 v[82:83],v2 offset:5344
v_mfma_f32_16x16x32_bf16 a[0:3],v[100:103],a[76:79],a[0:3]
ds_read_b64_tr_b16 v[88:89],v2 offset:20704
v_mfma_f32_16x16x32_bf16 a[0:3],v[104:107],a[84:87],a[0:3]
buffer_load_dwordx4 v[164:167],v6,s[28:31],s89 offen offset:1024 sc1
v_mfma_f32_16x16x32_bf16 a[0:3],v[108:111],a[92:95],a[0:3]
ds_read_b64_tr_b16 v[90:91],v2 offset:25824
v_mfma_f32_16x16x32_bf16 a[4:7],v[108:111],a[124:127],a[4:7]
v_mfma_f32_16x16x32_bf16 a[4:7],v[104:107],a[116:119],a[4:7]
v_mfma_f32_16x16x32_bf16 a[4:7],v[100:103],a[108:111],a[4:7]
v_mfma_f32_16x16x32_bf16 a[4:7],v[96:99],a[100:103],a[4:7]
buffer_load_dwordx4 v[44:47],v6,s[28:31],s90 offen sc1
v_mfma_f32_16x16x32_bf16 a[8:11],v[96:99],a[64:67],a[8:11]
v_mfma_f32_16x16x32_bf16 a[8:11],v[100:103],a[72:75],a[8:11]
v_mfma_f32_16x16x32_bf16 a[8:11],v[104:107],a[80:83],a[8:11]
v_mfma_f32_16x16x32_bf16 a[8:11],v[108:111],a[88:91],a[8:11]
v_mfma_f32_16x16x32_bf16 a[12:15],v[108:111],a[120:123],a[12:15]
buffer_load_dwordx4 a[144:147],v6,s[28:31],s88 offen sc1
v_mfma_f32_16x16x32_bf16 a[12:15],v[104:107],a[112:115],a[12:15]
ds_read_b64_tr_b16 v[104:105],v1 offset:20736
ds_read_b64_tr_b16 v[106:107],v1 offset:25856
v_mfma_f32_16x16x32_bf16 a[12:15],v[100:103],a[104:107],a[12:15]
ds_read_b64_tr_b16 v[100:101],v1 offset:10496
v_mfma_f32_16x16x32_bf16 a[12:15],v[96:99],a[96:99],a[12:15]
s_waitcnt lgkmcnt(3)
ds_read_b64_tr_b16 v[96:97],v1 offset:256
v_mfma_f32_16x16x32_bf16 a[16:19],v[112:115],a[68:71],a[16:19]
ds_read_b64_tr_b16 v[98:99],v1 offset:5376
v_mfma_f32_16x16x32_bf16 a[16:19],v[116:119],a[76:79],a[16:19]
buffer_load_dwordx4 v[172:175],v28,s[28:31],s89 offen offset:3072 sc1
v_mfma_f32_16x16x32_bf16 a[16:19],v[120:123],a[84:87],a[16:19]
s_add_u32 s89,s87,8192
ds_read_b64_tr_b16 v[102:103],v1 offset:15616
ds_read_b64_tr_b16 v[108:109],v1 offset:30976
v_mfma_f32_16x16x32_bf16 a[16:19],v[124:127],a[92:95],a[16:19]
ds_read_b64_tr_b16 v[110:111],v1 offset:36096
v_add_u32 v1,0x1e000,v1
v_mfma_f32_16x16x32_bf16 a[20:23],v[124:127],a[124:127],a[20:23]
v_mfma_f32_16x16x32_bf16 a[20:23],v[120:123],a[116:119],a[20:23]
v_mfma_f32_16x16x32_bf16 a[20:23],v[116:119],a[108:111],a[20:23]
buffer_load_dwordx4 v[180:183],v6,s[28:31],s90 offen offset:1024 sc1
v_mfma_f32_16x16x32_bf16 a[20:23],v[112:115],a[100:103],a[20:23]
v_mfma_f32_16x16x32_bf16 a[24:27],v[112:115],a[64:67],a[24:27]
v_mfma_f32_16x16x32_bf16 a[24:27],v[116:119],a[72:75],a[24:27]
v_mfma_f32_16x16x32_bf16 a[24:27],v[120:123],a[80:83],a[24:27]
v_mfma_f32_16x16x32_bf16 a[24:27],v[124:127],a[88:91],a[24:27]
buffer_load_dwordx4 a[160:163],v6,s[28:31],s89 offen sc1
v_mfma_f32_16x16x32_bf16 a[28:31],v[124:127],a[120:123],a[28:31]
ds_read_b64_tr_b16 v[124:125],v2 offset:31008
ds_read_b64_tr_b16 v[126:127],v2 offset:36128
v_mfma_f32_16x16x32_bf16 a[28:31],v[120:123],a[112:115],a[28:31]
ds_read_b64_tr_b16 v[120:121],v2 offset:20768
v_mfma_f32_16x16x32_bf16 a[28:31],v[116:119],a[104:107],a[28:31]
ds_read_b64_tr_b16 v[116:117],v2 offset:10528
v_mfma_f32_16x16x32_bf16 a[28:31],v[112:115],a[96:99],a[28:31]
ds_read_b64_tr_b16 v[112:113],v2 offset:288
v_mfma_f32_16x16x32_bf16 v[128:131],v[64:67],a[68:71],v[128:131]
buffer_load_dwordx4 v[244:247],v6,s[28:31],s90 offen offset:2048 sc1
v_mfma_f32_16x16x32_bf16 v[128:131],v[68:71],a[76:79],v[128:131]
ds_read_b64_tr_b16 v[114:115],v2 offset:5408
ds_read_b64_tr_b16 v[118:119],v2 offset:15648
v_mfma_f32_16x16x32_bf16 v[128:131],v[72:75],a[84:87],v[128:131]
ds_read_b64_tr_b16 v[122:123],v2 offset:25888
v_add_u32 v2,0x1e000,v2
v_mfma_f32_16x16x32_bf16 v[128:131],v[76:79],a[92:95],v[128:131]
v_mfma_f32_16x16x32_bf16 v[136:139],v[76:79],a[124:127],v[136:139]
v_mfma_f32_16x16x32_bf16 v[136:139],v[72:75],a[116:119],v[136:139]
buffer_load_dwordx4 v[188:191],v28,s[28:31],s90 offen offset:3072 sc1
v_mfma_f32_16x16x32_bf16 v[136:139],v[68:71],a[108:111],v[136:139]
s_add_u32 s90,s87,12288
v_mfma_f32_16x16x32_bf16 v[136:139],v[64:67],a[100:103],v[136:139]
v_mfma_f32_16x16x32_bf16 v[144:147],v[64:67],a[64:67],v[144:147]
v_mfma_f32_16x16x32_bf16 v[144:147],v[68:71],a[72:75],v[144:147]
v_mfma_f32_16x16x32_bf16 v[144:147],v[72:75],a[80:83],v[144:147]
buffer_load_dwordx4 a[176:179],v6,s[28:31],s90 offen sc1
v_mfma_f32_16x16x32_bf16 v[144:147],v[76:79],a[88:91],v[144:147]
v_mfma_f32_16x16x32_bf16 v[152:155],v[76:79],a[120:123],v[152:155]
v_mfma_f32_16x16x32_bf16 v[152:155],v[72:75],a[112:115],v[152:155]
v_mfma_f32_16x16x32_bf16 v[152:155],v[68:71],a[104:107],v[152:155]
v_mfma_f32_16x16x32_bf16 v[152:155],v[64:67],a[96:99],v[152:155]
buffer_load_dwordx4 a[180:183],v6,s[28:31],s90 offen offset:1024 sc1
s_waitcnt vmcnt(21) lgkmcnt(0)
s_barrier
v_mfma_f32_16x16x32_bf16 v[160:163],v[80:83],a[68:71],v[160:163]
ds_read_b64_tr_b16 v[64:65],v1
ds_read_b64_tr_b16 v[66:67],v1 offset:5120
v_mfma_f32_16x16x32_bf16 v[160:163],v[84:87],a[76:79],v[160:163]
ds_read_b64_tr_b16 v[68:69],v1 offset:10240
v_mfma_f32_16x16x32_bf16 v[160:163],v[88:91],a[84:87],v[160:163]
ds_read_b64_tr_b16 v[70:71],v1 offset:15360
v_mfma_f32_16x16x32_bf16 v[160:163],v[92:95],a[92:95],v[160:163]
buffer_load_dwordx4 a[164:167],v6,s[28:31],s89 offen offset:1024 sc1
v_mfma_f32_16x16x32_bf16 v[168:171],v[92:95],a[124:127],v[168:171]
ds_read_b64_tr_b16 v[72:73],v1 offset:20480
ds_read_b64_tr_b16 v[74:75],v1 offset:25600
v_mfma_f32_16x16x32_bf16 v[168:171],v[88:91],a[116:119],v[168:171]
ds_read_b64_tr_b16 v[76:77],v1 offset:30720
v_mfma_f32_16x16x32_bf16 v[168:171],v[84:87],a[108:111],v[168:171]
ds_read_b64_tr_b16 v[78:79],v1 offset:35840
v_mfma_f32_16x16x32_bf16 v[168:171],v[80:83],a[100:103],v[168:171]
v_mfma_f32_16x16x32_bf16 v[176:179],v[80:83],a[64:67],v[176:179]
v_mfma_f32_16x16x32_bf16 v[176:179],v[84:87],a[72:75],v[176:179]
buffer_load_dwordx4 v24,s[48:51],s83 offen lds
v_mfma_f32_16x16x32_bf16 v[176:179],v[88:91],a[80:83],v[176:179]
v_mfma_f32_16x16x32_bf16 v[176:179],v[92:95],a[88:91],v[176:179]
v_mfma_f32_16x16x32_bf16 v[184:187],v[92:95],a[120:123],v[184:187]
ds_read_b64_tr_b16 v[92:93],v2 offset:30752
ds_read_b64_tr_b16 v[94:95],v2 offset:35872
v_mfma_f32_16x16x32_bf16 v[184:187],v[88:91],a[112:115],v[184:187]
ds_read_b64_tr_b16 v[88:89],v2 offset:20512
v_mfma_f32_16x16x32_bf16 v[184:187],v[84:87],a[104:107],v[184:187]
buffer_load_dwordx4 a[148:151],v6,s[28:31],s88 offen offset:1024 sc1
v_mfma_f32_16x16x32_bf16 v[184:187],v[80:83],a[96:99],v[184:187]
ds_read_b64_tr_b16 v[80:81],v2 offset:32
ds_read_b64_tr_b16 v[82:83],v2 offset:5152
v_mfma_f32_16x16x32_bf16 v[192:195],v[96:99],a[68:71],v[192:195]
ds_read_b64_tr_b16 v[84:85],v2 offset:10272
v_mfma_f32_16x16x32_bf16 v[192:195],v[100:103],a[76:79],v[192:195]
ds_read_b64_tr_b16 v[86:87],v2 offset:15392
v_mfma_f32_16x16x32_bf16 v[192:195],v[104:107],a[84:87],v[192:195]
ds_read_b64_tr_b16 v[90:91],v2 offset:25632
v_mfma_f32_16x16x32_bf16 v[192:195],v[108:111],a[92:95],v[192:195]
v_mfma_f32_16x16x32_bf16 v[200:203],v[108:111],a[124:127],v[200:203]
buffer_load_dwordx4 v25,s[48:51],s83 offen offset:1024 lds
v_mfma_f32_16x16x32_bf16 v[200:203],v[104:107],a[116:119],v[200:203]
v_mfma_f32_16x16x32_bf16 v[200:203],v[100:103],a[108:111],v[200:203]
v_mfma_f32_16x16x32_bf16 v[200:203],v[96:99],a[100:103],v[200:203]
v_mfma_f32_16x16x32_bf16 v[208:211],v[96:99],a[64:67],v[208:211]
v_mfma_f32_16x16x32_bf16 v[208:211],v[100:103],a[72:75],v[208:211]
buffer_load_dwordx4 a[132:135],v6,s[28:31],s87 offen offset:1024 sc1
v_mfma_f32_16x16x32_bf16 v[208:211],v[104:107],a[80:83],v[208:211]
v_mfma_f32_16x16x32_bf16 v[208:211],v[108:111],a[88:91],v[208:211]
v_mfma_f32_16x16x32_bf16 v[216:219],v[108:111],a[120:123],v[216:219]
ds_read_b64_tr_b16 v[108:109],v1 offset:30784
ds_read_b64_tr_b16 v[110:111],v1 offset:35904
v_mfma_f32_16x16x32_bf16 v[216:219],v[104:107],a[112:115],v[216:219]
ds_read_b64_tr_b16 v[104:105],v1 offset:20544
v_mfma_f32_16x16x32_bf16 v[216:219],v[100:103],a[104:107],v[216:219]
ds_read_b64_tr_b16 v[100:101],v1 offset:10304
v_mfma_f32_16x16x32_bf16 v[216:219],v[96:99],a[96:99],v[216:219]
buffer_load_dwordx4 v26,s[48:51],s83 offen offset:2048 lds
v_mfma_f32_16x16x32_bf16 v[224:227],v[112:115],a[68:71],v[224:227]
ds_read_b64_tr_b16 v[96:97],v1 offset:64
ds_read_b64_tr_b16 v[98:99],v1 offset:5184
v_mfma_f32_16x16x32_bf16 v[224:227],v[116:119],a[76:79],v[224:227]
ds_read_b64_tr_b16 v[102:103],v1 offset:15424
v_mfma_f32_16x16x32_bf16 v[224:227],v[120:123],a[84:87],v[224:227]
ds_read_b64_tr_b16 v[106:107],v1 offset:25664
v_mfma_f32_16x16x32_bf16 v[224:227],v[124:127],a[92:95],v[224:227]
v_mfma_f32_16x16x32_bf16 v[232:235],v[124:127],a[124:127],v[232:235]
v_mfma_f32_16x16x32_bf16 v[232:235],v[120:123],a[116:119],v[232:235]
buffer_load_dwordx4 a[136:139],v6,s[28:31],s87 offen offset:2048 sc1
v_mfma_f32_16x16x32_bf16 v[232:235],v[116:119],a[108:111],v[232:235]
v_mfma_f32_16x16x32_bf16 v[232:235],v[112:115],a[100:103],v[232:235]
v_mfma_f32_16x16x32_bf16 v[240:243],v[112:115],a[64:67],v[240:243]
v_mfma_f32_16x16x32_bf16 v[240:243],v[116:119],a[72:75],v[240:243]
v_mfma_f32_16x16x32_bf16 v[240:243],v[120:123],a[80:83],v[240:243]
v_mfma_f32_16x16x32_bf16 v[240:243],v[124:127],a[88:91],v[240:243]
buffer_load_dwordx4 v27,s[48:51],s83 offen offset:3072 lds
v_mfma_f32_16x16x32_bf16 v[248:251],v[124:127],a[120:123],v[248:251]
s_add_u32 m0,s86,45056
ds_read_b64_tr_b16 v[124:125],v2 offset:30816
ds_read_b64_tr_b16 v[126:127],v2 offset:35936
v_mfma_f32_16x16x32_bf16 v[248:251],v[120:123],a[112:115],v[248:251]
ds_read_b64_tr_b16 v[120:121],v2 offset:20576
v_mfma_f32_16x16x32_bf16 v[248:251],v[116:119],a[104:107],v[248:251]
ds_read_b64_tr_b16 v[116:117],v2 offset:10336
v_mfma_f32_16x16x32_bf16 v[248:251],v[112:115],a[96:99],v[248:251]
s_waitcnt vmcnt(21) lgkmcnt(4)
ds_read_b64_tr_b16 v[112:113],v2 offset:96
)A"
R"A(v_mfma_f32_16x16x32_bf16 a[192:195],v[64:67],v[32:35],a[192:195]
s_waitcnt vmcnt(13)
ds_read_b64_tr_b16 v[114:115],v2 offset:5216
ds_read_b64_tr_b16 v[118:119],v2 offset:15456
v_mfma_f32_16x16x32_bf16 a[192:195],v[68:71],v[36:39],a[192:195]
buffer_load_dwordx4 v7,s[48:51],s83 offen lds
v_mfma_f32_16x16x32_bf16 a[192:195],v[72:75],v[40:43],a[192:195]
s_add_u32 m0,s86,46080
s_add_u32 s83,s64,3
ds_read_b64_tr_b16 v[122:123],v2 offset:25696
v_mfma_f32_16x16x32_bf16 a[192:195],v[76:79],v[44:47],a[192:195]
s_and_b32 s83,s83,3
s_lshl_b32 s83,s83,1
s_or_b32 s83,s83,17
v_mfma_f32_16x16x32_bf16 a[196:199],v[76:79],v[180:183],a[196:199]
s_mul_i32 s83,s83,163840
s_add_u32 s83,s83,s85
v_mfma_f32_16x16x32_bf16 a[196:199],v[72:75],v[164:167],a[196:199]
v_mfma_f32_16x16x32_bf16 a[196:199],v[68:71],v[148:151],a[196:199]
buffer_load_dwordx4 a[152:155],v6,s[28:31],s88 offen offset:2048 sc1
v_mfma_f32_16x16x32_bf16 a[196:199],v[64:67],v[132:135],a[196:199]
v_mfma_f32_16x16x32_bf16 a[200:203],v[64:67],v[196:199],a[200:203]
v_mfma_f32_16x16x32_bf16 a[200:203],v[68:71],v[212:215],a[200:203]
v_mfma_f32_16x16x32_bf16 a[200:203],v[72:75],v[228:231],a[200:203]
s_waitcnt vmcnt(13)
v_mfma_f32_16x16x32_bf16 a[200:203],v[76:79],v[244:247],a[200:203]
s_waitcnt vmcnt(12)
v_mfma_f32_16x16x32_bf16 a[204:207],v[76:79],v[188:191],a[204:207]
buffer_load_dwordx4 v24,s[48:51],s84 offen lds
v_mfma_f32_16x16x32_bf16 a[204:207],v[72:75],v[172:175],a[204:207]
ds_read_b64_tr_b16 v[72:73],v1 offset:20608
ds_read_b64_tr_b16 v[74:75],v1 offset:25728
v_mfma_f32_16x16x32_bf16 a[204:207],v[68:71],v[156:159],a[204:207]
ds_read_b64_tr_b16 v[68:69],v1 offset:10368
v_mfma_f32_16x16x32_bf16 a[204:207],v[64:67],v[140:143],a[204:207]
ds_read_b64_tr_b16 v[64:65],v1 offset:128
v_mfma_f32_16x16x32_bf16 a[208:211],v[80:83],v[32:35],a[208:211]
ds_read_b64_tr_b16 v[66:67],v1 offset:5248
v_mfma_f32_16x16x32_bf16 a[208:211],v[84:87],v[36:39],a[208:211]
buffer_load_dwordx4 a[168:171],v6,s[28:31],s89 offen offset:2048 sc1
v_mfma_f32_16x16x32_bf16 a[208:211],v[88:91],v[40:43],a[208:211]
ds_read_b64_tr_b16 v[70:71],v1 offset:15488
ds_read_b64_tr_b16 v[76:77],v1 offset:30848
v_mfma_f32_16x16x32_bf16 a[208:211],v[92:95],v[44:47],a[208:211]
ds_read_b64_tr_b16 v[78:79],v1 offset:35968
v_mfma_f32_16x16x32_bf16 a[212:215],v[92:95],v[180:183],a[212:215]
v_mfma_f32_16x16x32_bf16 a[212:215],v[88:91],v[164:167],a[212:215]
v_mfma_f32_16x16x32_bf16 a[212:215],v[84:87],v[148:151],a[212:215]
v_mfma_f32_16x16x32_bf16 a[212:215],v[80:83],v[132:135],a[212:215]
buffer_load_dwordx4 v25,s[48:51],s84 offen offset:1024 lds
v_mfma_f32_16x16x32_bf16 a[216:219],v[80:83],v[196:199],a[216:219]
v_mfma_f32_16x16x32_bf16 a[216:219],v[84:87],v[212:215],a[216:219]
v_mfma_f32_16x16x32_bf16 a[216:219],v[88:91],v[228:231],a[216:219]
v_mfma_f32_16x16x32_bf16 a[216:219],v[92:95],v[244:247],a[216:219]
v_mfma_f32_16x16x32_bf16 a[220:223],v[92:95],v[188:191],a[220:223]
buffer_load_dwordx4 a[184:187],v6,s[28:31],s90 offen offset:2048 sc1
v_mfma_f32_16x16x32_bf16 a[220:223],v[88:91],v[172:175],a[220:223]
ds_read_b64_tr_b16 v[88:89],v2 offset:20640
ds_read_b64_tr_b16 v[90:91],v2 offset:25760
v_mfma_f32_16x16x32_bf16 a[220:223],v[84:87],v[156:159],a[220:223]
ds_read_b64_tr_b16 v[84:85],v2 offset:10400
v_mfma_f32_16x16x32_bf16 a[220:223],v[80:83],v[140:143],a[220:223]
ds_read_b64_tr_b16 v[80:81],v2 offset:160
v_mfma_f32_16x16x32_bf16 a[224:227],v[96:99],v[32:35],a[224:227]
ds_read_b64_tr_b16 v[82:83],v2 offset:5280
v_mfma_f32_16x16x32_bf16 a[224:227],v[100:103],v[36:39],a[224:227]
ds_read_b64_tr_b16 v[86:87],v2 offset:15520
ds_read_b64_tr_b16 v[92:93],v2 offset:30880
v_mfma_f32_16x16x32_bf16 a[224:227],v[104:107],v[40:43],a[224:227]
buffer_load_dwordx4 v26,s[48:51],s84 offen offset:2048 lds
v_mfma_f32_16x16x32_bf16 a[224:227],v[108:111],v[44:47],a[224:227]
ds_read_b64_tr_b16 v[94:95],v2 offset:36000
v_mfma_f32_16x16x32_bf16 a[228:231],v[108:111],v[180:183],a[228:231]
v_mfma_f32_16x16x32_bf16 a[228:231],v[104:107],v[164:167],a[228:231]
v_mfma_f32_16x16x32_bf16 a[228:231],v[100:103],v[148:151],a[228:231]
v_mfma_f32_16x16x32_bf16 a[228:231],v[96:99],v[132:135],a[228:231]
v_mfma_f32_16x16x32_bf16 a[232:235],v[96:99],v[196:199],a[232:235]
buffer_load_dwordx4 v27,s[48:51],s84 offen offset:3072 lds
v_mfma_f32_16x16x32_bf16 a[232:235],v[100:103],v[212:215],a[232:235]
s_add_u32 m0,s86,50176
v_mfma_f32_16x16x32_bf16 a[232:235],v[104:107],v[228:231],a[232:235]
v_mfma_f32_16x16x32_bf16 a[232:235],v[108:111],v[244:247],a[232:235]
v_mfma_f32_16x16x32_bf16 a[236:239],v[108:111],v[188:191],a[236:239]
ds_read_b64_tr_b16 v[108:109],v1 offset:30912
ds_read_b64_tr_b16 v[110:111],v1 offset:36032
v_mfma_f32_16x16x32_bf16 a[236:239],v[104:107],v[172:175],a[236:239]
buffer_load_dwordx4 a[188:191],v28,s[28:31],s90 offen offset:3072 sc1
v_mfma_f32_16x16x32_bf16 a[236:239],v[100:103],v[156:159],a[236:239]
ds_read_b64_tr_b16 v[100:101],v1 offset:10432
ds_read_b64_tr_b16 v[102:103],v1 offset:15552
v_mfma_f32_16x16x32_bf16 a[236:239],v[96:99],v[140:143],a[236:239]
s_waitcnt lgkmcnt(4)
ds_read_b64_tr_b16 v[96:97],v1 offset:192
v_mfma_f32_16x16x32_bf16 a[240:243],v[112:115],v[32:35],a[240:243]
ds_read_b64_tr_b16 v[98:99],v1 offset:5312
v_mfma_f32_16x16x32_bf16 a[240:243],v[116:119],v[36:39],a[240:243]
ds_read_b64_tr_b16 v[104:105],v1 offset:20672
v_mfma_f32_16x16x32_bf16 a[240:243],v[120:123],v[40:43],a[240:243]
ds_read_b64_tr_b16 v[106:107],v1 offset:25792
v_mfma_f32_16x16x32_bf16 a[240:243],v[124:127],v[44:47],a[240:243]
buffer_load_dwordx4 v7,s[48:51],s84 offen lds
v_mfma_f32_16x16x32_bf16 a[244:247],v[124:127],v[180:183],a[244:247]
s_add_u32 m0,s86,0
s_add_u32 s84,s83,20480
v_mfma_f32_16x16x32_bf16 a[244:247],v[120:123],v[164:167],a[244:247]
v_mfma_f32_16x16x32_bf16 a[244:247],v[116:119],v[148:151],a[244:247]
v_mfma_f32_16x16x32_bf16 a[244:247],v[112:115],v[132:135],a[244:247]
v_mfma_f32_16x16x32_bf16 a[248:251],v[112:115],v[196:199],a[248:251]
buffer_load_dwordx4 a[172:175],v28,s[28:31],s89 offen offset:3072 sc1
v_mfma_f32_16x16x32_bf16 a[248:251],v[116:119],v[212:215],a[248:251]
v_mfma_f32_16x16x32_bf16 a[248:251],v[120:123],v[228:231],a[248:251]
v_mfma_f32_16x16x32_bf16 a[248:251],v[124:127],v[244:247],a[248:251]
v_mfma_f32_16x16x32_bf16 a[252:255],v[124:127],v[188:191],a[252:255]
ds_read_b64_tr_b16 v[124:125],v2 offset:30944
ds_read_b64_tr_b16 v[126:127],v2 offset:36064
v_mfma_f32_16x16x32_bf16 a[252:255],v[120:123],v[172:175],a[252:255]
ds_read_b64_tr_b16 v[120:121],v2 offset:20704
v_mfma_f32_16x16x32_bf16 a[252:255],v[116:119],v[156:159],a[252:255]
buffer_load_dwordx4 v24,s[48:51],s83 offen lds
v_mfma_f32_16x16x32_bf16 a[252:255],v[112:115],v[140:143],a[252:255]
ds_read_b64_tr_b16 v[112:113],v2 offset:224
ds_read_b64_tr_b16 v[114:115],v2 offset:5344
v_mfma_f32_16x16x32_bf16 a[0:3],v[64:67],v[32:35],a[0:3]
ds_read_b64_tr_b16 v[116:117],v2 offset:10464
v_mfma_f32_16x16x32_bf16 a[0:3],v[68:71],v[36:39],a[0:3]
ds_read_b64_tr_b16 v[118:119],v2 offset:15584
v_mfma_f32_16x16x32_bf16 a[0:3],v[72:75],v[40:43],a[0:3]
ds_read_b64_tr_b16 v[122:123],v2 offset:25824
v_mfma_f32_16x16x32_bf16 a[0:3],v[76:79],v[44:47],a[0:3]
buffer_load_dwordx4 a[156:159],v28,s[28:31],s88 offen offset:3072 sc1
v_mfma_f32_16x16x32_bf16 a[4:7],v[76:79],v[180:183],a[4:7]
v_mfma_f32_16x16x32_bf16 a[4:7],v[72:75],v[164:167],a[4:7]
v_mfma_f32_16x16x32_bf16 a[4:7],v[68:71],v[148:151],a[4:7]
v_mfma_f32_16x16x32_bf16 a[4:7],v[64:67],v[132:135],a[4:7]
v_mfma_f32_16x16x32_bf16 a[8:11],v[64:67],v[196:199],a[8:11]
v_mfma_f32_16x16x32_bf16 a[8:11],v[68:71],v[212:215],a[8:11]
buffer_load_dwordx4 v25,s[48:51],s83 offen offset:1024 lds
v_mfma_f32_16x16x32_bf16 a[8:11],v[72:75],v[228:231],a[8:11]
v_mfma_f32_16x16x32_bf16 a[8:11],v[76:79],v[244:247],a[8:11]
v_mfma_f32_16x16x32_bf16 a[12:15],v[76:79],v[188:191],a[12:15]
ds_read_b64_tr_b16 v[76:77],v1 offset:30976
ds_read_b64_tr_b16 v[78:79],v1 offset:36096
v_mfma_f32_16x16x32_bf16 a[12:15],v[72:75],v[172:175],a[12:15]
ds_read_b64_tr_b16 v[72:73],v1 offset:20736
v_mfma_f32_16x16x32_bf16 a[12:15],v[68:71],v[156:159],a[12:15]
ds_read_b64_tr_b16 v[68:69],v1 offset:10496
v_mfma_f32_16x16x32_bf16 a[12:15],v[64:67],v[140:143],a[12:15]
buffer_load_dwordx4 v26,s[48:51],s83 offen offset:2048 lds
v_mfma_f32_16x16x32_bf16 a[16:19],v[80:83],v[32:35],a[16:19]
ds_read_b64_tr_b16 v[64:65],v1 offset:256
ds_read_b64_tr_b16 v[66:67],v1 offset:5376
v_mfma_f32_16x16x32_bf16 a[16:19],v[84:87],v[36:39],a[16:19]
ds_read_b64_tr_b16 v[70:71],v1 offset:15616
v_mfma_f32_16x16x32_bf16 a[16:19],v[88:91],v[40:43],a[16:19]
ds_read_b64_tr_b16 v[74:75],v1 offset:25856
v_add_u32 v1,0xffff6000,v1
v_mfma_f32_16x16x32_bf16 a[16:19],v[92:95],v[44:47],a[16:19]
v_mfma_f32_16x16x32_bf16 a[20:23],v[92:95],v[180:183],a[20:23]
buffer_load_dwordx4 a[140:143],v28,s[28:31],s87 offen offset:3072 sc1
v_mfma_f32_16x16x32_bf16 a[20:23],v[88:91],v[164:167],a[20:23]
s_add_u32 s87,s64,2
s_and_b32 s87,s87,3
s_lshl_b32 s87,s87,1
v_mfma_f32_16x16x32_bf16 a[20:23],v[84:87],v[148:151],a[20:23]
s_or_b32 s87,s87,1
s_lshl_b32 s87,s87,16
s_add_u32 s88,s87,4096
v_mfma_f32_16x16x32_bf16 a[20:23],v[80:83],v[132:135],a[20:23]
s_add_u32 s89,s87,8192
s_add_u32 s90,s87,12288
v_mfma_f32_16x16x32_bf16 a[24:27],v[80:83],v[196:199],a[24:27]
v_mfma_f32_16x16x32_bf16 a[24:27],v[84:87],v[212:215],a[24:27]
v_mfma_f32_16x16x32_bf16 a[24:27],v[88:91],v[228:231],a[24:27]
buffer_load_dwordx4 v27,s[48:51],s83 offen offset:3072 lds
v_mfma_f32_16x16x32_bf16 a[24:27],v[92:95],v[244:247],a[24:27]
s_add_u32 m0,s86,4096
v_mfma_f32_16x16x32_bf16 a[28:31],v[92:95],v[188:191],a[28:31]
ds_read_b64_tr_b16 v[92:93],v2 offset:31008
ds_read_b64_tr_b16 v[94:95],v2 offset:36128
v_mfma_f32_16x16x32_bf16 a[28:31],v[88:91],v[172:175],a[28:31]
ds_read_b64_tr_b16 v[88:89],v2 offset:20768
v_mfma_f32_16x16x32_bf16 a[28:31],v[84:87],v[156:159],a[28:31]
ds_read_b64_tr_b16 v[84:85],v2 offset:10528
v_mfma_f32_16x16x32_bf16 a[28:31],v[80:83],v[140:143],a[28:31]
s_waitcnt lgkmcnt(4)
ds_read_b64_tr_b16 v[80:81],v2 offset:288
v_mfma_f32_16x16x32_bf16 v[128:131],v[96:99],v[32:35],v[128:131]
buffer_load_dwordx4 v7,s[48:51],s83 offen lds
v_mfma_f32_16x16x32_bf16 v[128:131],v[100:103],v[36:39],v[128:131]
s_add_u32 m0,s86,5120
ds_read_b64_tr_b16 v[82:83],v2 offset:5408
ds_read_b64_tr_b16 v[86:87],v2 offset:15648
v_mfma_f32_16x16x32_bf16 v[128:131],v[104:107],v[40:43],v[128:131]
s_add_u32 s83,s64,3
ds_read_b64_tr_b16 v[90:91],v2 offset:25888
v_add_u32 v2,0xffff6000,v2
v_mfma_f32_16x16x32_bf16 v[128:131],v[108:111],v[44:47],v[128:131]
s_and_b32 s83,s83,3
s_lshl_b32 s83,s83,1
s_or_b32 s83,s83,1
v_mfma_f32_16x16x32_bf16 v[136:139],v[108:111],v[180:183],v[136:139]
s_mul_i32 s83,s83,163840
s_add_u32 s83,s83,s85
v_mfma_f32_16x16x32_bf16 v[136:139],v[104:107],v[164:167],v[136:139]
v_mfma_f32_16x16x32_bf16 v[136:139],v[100:103],v[148:151],v[136:139]
buffer_load_dwordx4 v24,s[48:51],s84 offen lds
v_mfma_f32_16x16x32_bf16 v[136:139],v[96:99],v[132:135],v[136:139]
v_mfma_f32_16x16x32_bf16 v[144:147],v[96:99],v[196:199],v[144:147]
v_mfma_f32_16x16x32_bf16 v[144:147],v[100:103],v[212:215],v[144:147]
v_mfma_f32_16x16x32_bf16 v[144:147],v[104:107],v[228:231],v[144:147]
v_mfma_f32_16x16x32_bf16 v[144:147],v[108:111],v[244:247],v[144:147]
v_mfma_f32_16x16x32_bf16 v[152:155],v[108:111],v[188:191],v[152:155]
buffer_load_dwordx4 v25,s[48:51],s84 offen offset:1024 lds
v_mfma_f32_16x16x32_bf16 v[152:155],v[104:107],v[172:175],v[152:155]
ds_read_b64_tr_b16 v[104:105],v1 offset:20480
ds_read_b64_tr_b16 v[106:107],v1 offset:25600
v_mfma_f32_16x16x32_bf16 v[152:155],v[100:103],v[156:159],v[152:155]
ds_read_b64_tr_b16 v[100:101],v1 offset:10240
v_mfma_f32_16x16x32_bf16 v[152:155],v[96:99],v[140:143],v[152:155]
ds_read_b64_tr_b16 v[96:97],v1
v_mfma_f32_16x16x32_bf16 v[160:163],v[112:115],v[32:35],v[160:163]
ds_read_b64_tr_b16 v[98:99],v1 offset:5120
v_mfma_f32_16x16x32_bf16 v[160:163],v[116:119],v[36:39],v[160:163]
ds_read_b64_tr_b16 v[102:103],v1 offset:15360
ds_read_b64_tr_b16 v[108:109],v1 offset:30720
v_mfma_f32_16x16x32_bf16 v[160:163],v[120:123],v[40:43],v[160:163]
)A"
R"A(buffer_load_dwordx4 v26,s[48:51],s84 offen offset:2048 lds
v_mfma_f32_16x16x32_bf16 v[160:163],v[124:127],v[44:47],v[160:163]
ds_read_b64_tr_b16 v[110:111],v1 offset:35840
v_mfma_f32_16x16x32_bf16 v[168:171],v[124:127],v[180:183],v[168:171]
v_mfma_f32_16x16x32_bf16 v[168:171],v[120:123],v[164:167],v[168:171]
v_mfma_f32_16x16x32_bf16 v[168:171],v[116:119],v[148:151],v[168:171]
v_mfma_f32_16x16x32_bf16 v[168:171],v[112:115],v[132:135],v[168:171]
v_mfma_f32_16x16x32_bf16 v[176:179],v[112:115],v[196:199],v[176:179]
buffer_load_dwordx4 v27,s[48:51],s84 offen offset:3072 lds
v_mfma_f32_16x16x32_bf16 v[176:179],v[116:119],v[212:215],v[176:179]
s_add_u32 m0,s86,9216
v_mfma_f32_16x16x32_bf16 v[176:179],v[120:123],v[228:231],v[176:179]
v_mfma_f32_16x16x32_bf16 v[176:179],v[124:127],v[244:247],v[176:179]
v_mfma_f32_16x16x32_bf16 v[184:187],v[124:127],v[188:191],v[184:187]
ds_read_b64_tr_b16 v[124:125],v2 offset:30752
ds_read_b64_tr_b16 v[126:127],v2 offset:35872
v_mfma_f32_16x16x32_bf16 v[184:187],v[120:123],v[172:175],v[184:187]
ds_read_b64_tr_b16 v[120:121],v2 offset:20512
v_mfma_f32_16x16x32_bf16 v[184:187],v[116:119],v[156:159],v[184:187]
buffer_load_dwordx4 v7,s[48:51],s84 offen lds
v_mfma_f32_16x16x32_bf16 v[184:187],v[112:115],v[140:143],v[184:187]
s_add_u32 m0,s86,122880
ds_read_b64_tr_b16 v[112:113],v2 offset:32
ds_read_b64_tr_b16 v[114:115],v2 offset:5152
v_mfma_f32_16x16x32_bf16 v[192:195],v[64:67],v[32:35],v[192:195]
s_add_u32 s84,s83,20480
ds_read_b64_tr_b16 v[116:117],v2 offset:10272
v_mfma_f32_16x16x32_bf16 v[192:195],v[68:71],v[36:39],v[192:195]
L_pd16_%=:
s_cbranch_execz L_trtg2F_%=
ds_read_b64_tr_b16 v[118:119],v2 offset:15392
v_mfma_f32_16x16x32_bf16 v[192:195],v[72:75],v[40:43],v[192:195]
ds_read_b64_tr_b16 v[122:123],v2 offset:25632
v_mfma_f32_16x16x32_bf16 v[192:195],v[76:79],v[44:47],v[192:195]
buffer_load_dwordx4 a[68:71],v6,s[28:31],s87 offen sc1
v_mfma_f32_16x16x32_bf16 v[200:203],v[76:79],v[180:183],v[200:203]
v_mfma_f32_16x16x32_bf16 v[200:203],v[72:75],v[164:167],v[200:203]
v_mfma_f32_16x16x32_bf16 v[200:203],v[68:71],v[148:151],v[200:203]
v_mfma_f32_16x16x32_bf16 v[200:203],v[64:67],v[132:135],v[200:203]
v_mfma_f32_16x16x32_bf16 v[208:211],v[64:67],v[196:199],v[208:211]
buffer_load_dwordx4 a[100:103],v6,s[28:31],s87 offen offset:1024 sc1
v_mfma_f32_16x16x32_bf16 v[208:211],v[68:71],v[212:215],v[208:211]
v_mfma_f32_16x16x32_bf16 v[208:211],v[72:75],v[228:231],v[208:211]
v_mfma_f32_16x16x32_bf16 v[208:211],v[76:79],v[244:247],v[208:211]
v_mfma_f32_16x16x32_bf16 v[216:219],v[76:79],v[188:191],v[216:219]
ds_read_b64_tr_b16 v[76:77],v1 offset:30784
ds_read_b64_tr_b16 v[78:79],v1 offset:35904
v_mfma_f32_16x16x32_bf16 v[216:219],v[72:75],v[172:175],v[216:219]
buffer_load_dwordx2 v[4:5],v3,s[44:47],s91 offen offset:1280 sc1
v_mfma_f32_16x16x32_bf16 v[216:219],v[68:71],v[156:159],v[216:219]
ds_read_b64_tr_b16 v[68:69],v1 offset:10304
ds_read_b64_tr_b16 v[70:71],v1 offset:15424
v_mfma_f32_16x16x32_bf16 v[216:219],v[64:67],v[140:143],v[216:219]
s_waitcnt lgkmcnt(4)
ds_read_b64_tr_b16 v[64:65],v1 offset:64
v_mfma_f32_16x16x32_bf16 v[224:227],v[80:83],v[32:35],v[224:227]
ds_read_b64_tr_b16 v[66:67],v1 offset:5184
v_mfma_f32_16x16x32_bf16 v[224:227],v[84:87],v[36:39],v[224:227]
ds_read_b64_tr_b16 v[72:73],v1 offset:20544
v_mfma_f32_16x16x32_bf16 v[224:227],v[88:91],v[40:43],v[224:227]
buffer_load_dwordx4 a[64:67],v6,s[28:31],s87 offen offset:2048 sc1
v_mfma_f32_16x16x32_bf16 v[224:227],v[92:95],v[44:47],v[224:227]
ds_read_b64_tr_b16 v[74:75],v1 offset:25664
v_mfma_f32_16x16x32_bf16 v[232:235],v[92:95],v[180:183],v[232:235]
v_mfma_f32_16x16x32_bf16 v[232:235],v[88:91],v[164:167],v[232:235]
v_mfma_f32_16x16x32_bf16 v[232:235],v[84:87],v[148:151],v[232:235]
v_mfma_f32_16x16x32_bf16 v[232:235],v[80:83],v[132:135],v[232:235]
buffer_load_dwordx4 a[76:79],v6,s[28:31],s88 offen sc1
v_mfma_f32_16x16x32_bf16 v[240:243],v[80:83],v[196:199],v[240:243]
v_mfma_f32_16x16x32_bf16 v[240:243],v[84:87],v[212:215],v[240:243]
v_mfma_f32_16x16x32_bf16 v[240:243],v[88:91],v[228:231],v[240:243]
v_mfma_f32_16x16x32_bf16 v[240:243],v[92:95],v[244:247],v[240:243]
v_mfma_f32_16x16x32_bf16 v[248:251],v[92:95],v[188:191],v[248:251]
buffer_load_dwordx4 a[96:99],v28,s[28:31],s87 offen offset:3072 sc1
v_mfma_f32_16x16x32_bf16 v[248:251],v[88:91],v[172:175],v[248:251]
s_add_u32 s87,s64,3
ds_read_b64_tr_b16 v[88:89],v2 offset:20576
ds_read_b64_tr_b16 v[90:91],v2 offset:25696
v_mfma_f32_16x16x32_bf16 v[248:251],v[84:87],v[156:159],v[248:251]
s_and_b32 s87,s87,3
s_lshl_b32 s87,s87,1
ds_read_b64_tr_b16 v[84:85],v2 offset:10336
v_mfma_f32_16x16x32_bf16 v[248:251],v[80:83],v[140:143],v[248:251]
s_or_b32 s87,s87,17
s_lshl_b32 s87,s87,16
ds_read_b64_tr_b16 v[80:81],v2 offset:96
v_mfma_f32_16x16x32_bf16 a[192:195],v[96:99],a[128:131],a[192:195]
ds_read_b64_tr_b16 v[82:83],v2 offset:5216
v_mfma_f32_16x16x32_bf16 a[192:195],v[100:103],a[144:147],a[192:195]
buffer_load_dwordx4 a[72:75],v6,s[28:31],s88 offen offset:2048 sc1
v_mfma_f32_16x16x32_bf16 a[192:195],v[104:107],a[160:163],a[192:195]
s_waitcnt vmcnt(37)
ds_read_b64_tr_b16 v[86:87],v2 offset:15456
ds_read_b64_tr_b16 v[92:93],v2 offset:30816
v_mfma_f32_16x16x32_bf16 a[192:195],v[108:111],a[176:179],a[192:195]
ds_read_b64_tr_b16 v[94:95],v2 offset:35936
v_mfma_f32_16x16x32_bf16 a[196:199],v[108:111],a[180:183],a[196:199]
v_mfma_f32_16x16x32_bf16 a[196:199],v[104:107],a[164:167],a[196:199]
s_waitcnt vmcnt(33)
v_mfma_f32_16x16x32_bf16 a[196:199],v[100:103],a[148:151],a[196:199]
buffer_load_dwordx4 a[108:111],v6,s[28:31],s88 offen offset:1024 sc1
v_mfma_f32_16x16x32_bf16 a[196:199],v[96:99],a[132:135],a[196:199]
s_waitcnt vmcnt(32)
v_mfma_f32_16x16x32_bf16 a[200:203],v[96:99],a[136:139],a[200:203]
s_waitcnt vmcnt(29)
v_mfma_f32_16x16x32_bf16 a[200:203],v[100:103],a[152:155],a[200:203]
s_waitcnt vmcnt(27)
v_mfma_f32_16x16x32_bf16 a[200:203],v[104:107],a[168:171],a[200:203]
s_waitcnt vmcnt(22)
v_mfma_f32_16x16x32_bf16 a[200:203],v[108:111],a[184:187],a[200:203]
buffer_load_dwordx4 a[84:87],v6,s[28:31],s89 offen sc1
v_mfma_f32_16x16x32_bf16 a[204:207],v[108:111],a[188:191],a[204:207]
s_waitcnt vmcnt(21)
ds_read_b64_tr_b16 v[108:109],v1 offset:30848
ds_read_b64_tr_b16 v[110:111],v1 offset:35968
v_mfma_f32_16x16x32_bf16 a[204:207],v[104:107],a[172:175],a[204:207]
s_waitcnt vmcnt(19)
ds_read_b64_tr_b16 v[104:105],v1 offset:20608
v_mfma_f32_16x16x32_bf16 a[204:207],v[100:103],a[156:159],a[204:207]
s_waitcnt vmcnt(16)
ds_read_b64_tr_b16 v[100:101],v1 offset:10368
v_mfma_f32_16x16x32_bf16 a[204:207],v[96:99],a[140:143],a[204:207]
ds_read_b64_tr_b16 v[96:97],v1 offset:128
v_mfma_f32_16x16x32_bf16 a[208:211],v[112:115],a[128:131],a[208:211]
buffer_load_dwordx4 a[104:107],v28,s[28:31],s88 offen offset:3072 sc1
v_mfma_f32_16x16x32_bf16 a[208:211],v[116:119],a[144:147],a[208:211]
s_add_u32 s88,s87,4096
ds_read_b64_tr_b16 v[98:99],v1 offset:5248
ds_read_b64_tr_b16 v[102:103],v1 offset:15488
v_mfma_f32_16x16x32_bf16 a[208:211],v[120:123],a[160:163],a[208:211]
ds_read_b64_tr_b16 v[106:107],v1 offset:25728
v_mfma_f32_16x16x32_bf16 a[208:211],v[124:127],a[176:179],a[208:211]
v_mfma_f32_16x16x32_bf16 a[212:215],v[124:127],a[180:183],a[212:215]
v_mfma_f32_16x16x32_bf16 a[212:215],v[120:123],a[164:167],a[212:215]
buffer_load_dwordx4 a[80:83],v6,s[28:31],s89 offen offset:2048 sc1
v_mfma_f32_16x16x32_bf16 a[212:215],v[116:119],a[148:151],a[212:215]
v_mfma_f32_16x16x32_bf16 a[212:215],v[112:115],a[132:135],a[212:215]
v_mfma_f32_16x16x32_bf16 a[216:219],v[112:115],a[136:139],a[216:219]
v_mfma_f32_16x16x32_bf16 a[216:219],v[116:119],a[152:155],a[216:219]
v_mfma_f32_16x16x32_bf16 a[216:219],v[120:123],a[168:171],a[216:219]
buffer_load_dwordx4 a[116:119],v6,s[28:31],s89 offen offset:1024 sc1
v_mfma_f32_16x16x32_bf16 a[216:219],v[124:127],a[184:187],a[216:219]
v_mfma_f32_16x16x32_bf16 a[220:223],v[124:127],a[188:191],a[220:223]
ds_read_b64_tr_b16 v[124:125],v2 offset:30880
ds_read_b64_tr_b16 v[126:127],v2 offset:36000
v_mfma_f32_16x16x32_bf16 a[220:223],v[120:123],a[172:175],a[220:223]
ds_read_b64_tr_b16 v[120:121],v2 offset:20640
v_mfma_f32_16x16x32_bf16 a[220:223],v[116:119],a[156:159],a[220:223]
s_waitcnt lgkmcnt(3)
ds_read_b64_tr_b16 v[116:117],v2 offset:10400
v_mfma_f32_16x16x32_bf16 a[220:223],v[112:115],a[140:143],a[220:223]
buffer_load_dwordx4 a[92:95],v6,s[28:31],s90 offen sc1
v_mfma_f32_16x16x32_bf16 a[224:227],v[64:67],a[128:131],a[224:227]
ds_read_b64_tr_b16 v[112:113],v2 offset:160
ds_read_b64_tr_b16 v[114:115],v2 offset:5280
v_mfma_f32_16x16x32_bf16 a[224:227],v[68:71],a[144:147],a[224:227]
ds_read_b64_tr_b16 v[118:119],v2 offset:15520
v_mfma_f32_16x16x32_bf16 a[224:227],v[72:75],a[160:163],a[224:227]
ds_read_b64_tr_b16 v[122:123],v2 offset:25760
v_mfma_f32_16x16x32_bf16 a[224:227],v[76:79],a[176:179],a[224:227]
v_mfma_f32_16x16x32_bf16 a[228:231],v[76:79],a[180:183],a[228:231]
buffer_load_dwordx4 a[112:115],v28,s[28:31],s89 offen offset:3072 sc1
v_mfma_f32_16x16x32_bf16 a[228:231],v[72:75],a[164:167],a[228:231]
s_add_u32 s89,s87,8192
v_mfma_f32_16x16x32_bf16 a[228:231],v[68:71],a[148:151],a[228:231]
v_mfma_f32_16x16x32_bf16 a[228:231],v[64:67],a[132:135],a[228:231]
v_mfma_f32_16x16x32_bf16 a[232:235],v[64:67],a[136:139],a[232:235]
v_mfma_f32_16x16x32_bf16 a[232:235],v[68:71],a[152:155],a[232:235]
buffer_load_dwordx4 a[124:127],v6,s[28:31],s90 offen offset:1024 sc1
v_mfma_f32_16x16x32_bf16 a[232:235],v[72:75],a[168:171],a[232:235]
s_waitcnt vmcnt(12)
v_cmp_ne_u32 s[56:57],s52,v4
v_cmp_ne_u32 s[58:59],s53,v5
v_mfma_f32_16x16x32_bf16 a[232:235],v[76:79],a[184:187],a[232:235]
s_or_b64 s[56:57],s[56:57],s[58:59]
s_cbranch_scc1 L_fr3_%=
L_fo3_%=:
s_lshl_b32 s91,s64,6
v_mfma_f32_16x16x32_bf16 a[236:239],v[76:79],a[188:191],a[236:239]
ds_read_b64_tr_b16 v[76:77],v1 offset:30912
ds_read_b64_tr_b16 v[78:79],v1 offset:36032
v_mfma_f32_16x16x32_bf16 a[236:239],v[72:75],a[172:175],a[236:239]
ds_read_b64_tr_b16 v[72:73],v1 offset:20672
v_bfe_u32 v3,v6,4,2
v_mfma_f32_16x16x32_bf16 a[236:239],v[68:71],a[156:159],a[236:239]
buffer_load_dwordx4 v[32:35],v6,s[28:31],s87 offen sc1
v_mfma_f32_16x16x32_bf16 a[236:239],v[64:67],a[140:143],a[236:239]
ds_read_b64_tr_b16 v[64:65],v1 offset:192
ds_read_b64_tr_b16 v[66:67],v1 offset:5312
v_mfma_f32_16x16x32_bf16 a[240:243],v[80:83],a[128:131],a[240:243]
ds_read_b64_tr_b16 v[68:69],v1 offset:10432
v_min_u32 v3,2,v3
v_mfma_f32_16x16x32_bf16 a[240:243],v[84:87],a[144:147],a[240:243]
ds_read_b64_tr_b16 v[70:71],v1 offset:15552
v_add3_u32 v3,v3,s63,1
v_mfma_f32_16x16x32_bf16 a[240:243],v[88:91],a[160:163],a[240:243]
ds_read_b64_tr_b16 v[74:75],v1 offset:25792
v_and_b32 v3,3,v3
v_mfma_f32_16x16x32_bf16 a[240:243],v[92:95],a[176:179],a[240:243]
buffer_load_dwordx4 a[88:91],v6,s[28:31],s90 offen offset:2048 sc1
v_mfma_f32_16x16x32_bf16 a[244:247],v[92:95],a[180:183],a[244:247]
v_lshlrev_b32 v3,8,v3
v_mfma_f32_16x16x32_bf16 a[244:247],v[88:91],a[164:167],a[244:247]
v_mfma_f32_16x16x32_bf16 a[244:247],v[84:87],a[148:151],a[244:247]
v_mfma_f32_16x16x32_bf16 a[244:247],v[80:83],a[132:135],a[244:247]
v_mfma_f32_16x16x32_bf16 a[248:251],v[80:83],a[136:139],a[248:251]
buffer_load_dwordx4 v[36:39],v6,s[28:31],s88 offen sc1
v_mfma_f32_16x16x32_bf16 a[248:251],v[84:87],a[152:155],a[248:251]
v_mfma_f32_16x16x32_bf16 a[248:251],v[88:91],a[168:171],a[248:251]
v_mfma_f32_16x16x32_bf16 a[248:251],v[92:95],a[184:187],a[248:251]
v_mfma_f32_16x16x32_bf16 a[252:255],v[92:95],a[188:191],a[252:255]
ds_read_b64_tr_b16 v[92:93],v2 offset:30944
ds_read_b64_tr_b16 v[94:95],v2 offset:36064
v_mfma_f32_16x16x32_bf16 a[252:255],v[88:91],a[172:175],a[252:255]
buffer_load_dwordx4 a[120:123],v28,s[28:31],s90 offen offset:3072 sc1
v_mfma_f32_16x16x32_bf16 a[252:255],v[84:87],a[156:159],a[252:255]
s_add_u32 s90,s87,12288
ds_read_b64_tr_b16 v[84:85],v2 offset:10464
ds_read_b64_tr_b16 v[86:87],v2 offset:15584
v_mfma_f32_16x16x32_bf16 a[252:255],v[80:83],a[140:143],a[252:255]
ds_read_b64_tr_b16 v[80:81],v2 offset:224
v_mfma_f32_16x16x32_bf16 a[0:3],v[96:99],a[128:131],a[0:3]
ds_read_b64_tr_b16 v[82:83],v2 offset:5344
)A"
R"A(v_mfma_f32_16x16x32_bf16 a[0:3],v[100:103],a[144:147],a[0:3]
ds_read_b64_tr_b16 v[88:89],v2 offset:20704
v_mfma_f32_16x16x32_bf16 a[0:3],v[104:107],a[160:163],a[0:3]
buffer_load_dwordx4 v[40:43],v6,s[28:31],s89 offen sc1
v_mfma_f32_16x16x32_bf16 a[0:3],v[108:111],a[176:179],a[0:3]
ds_read_b64_tr_b16 v[90:91],v2 offset:25824
v_mfma_f32_16x16x32_bf16 a[4:7],v[108:111],a[180:183],a[4:7]
v_mfma_f32_16x16x32_bf16 a[4:7],v[104:107],a[164:167],a[4:7]
v_mfma_f32_16x16x32_bf16 a[4:7],v[100:103],a[148:151],a[4:7]
v_mfma_f32_16x16x32_bf16 a[4:7],v[96:99],a[132:135],a[4:7]
v_mfma_f32_16x16x32_bf16 a[8:11],v[96:99],a[136:139],a[8:11]
v_mfma_f32_16x16x32_bf16 a[8:11],v[100:103],a[152:155],a[8:11]
v_mfma_f32_16x16x32_bf16 a[8:11],v[104:107],a[168:171],a[8:11]
v_mfma_f32_16x16x32_bf16 a[8:11],v[108:111],a[184:187],a[8:11]
v_mfma_f32_16x16x32_bf16 a[12:15],v[108:111],a[188:191],a[12:15]
ds_read_b64_tr_b16 v[108:109],v1 offset:30976
ds_read_b64_tr_b16 v[110:111],v1 offset:36096
v_mfma_f32_16x16x32_bf16 a[12:15],v[104:107],a[172:175],a[12:15]
buffer_load_dwordx4 v[44:47],v6,s[28:31],s90 offen sc1
v_mfma_f32_16x16x32_bf16 a[12:15],v[100:103],a[156:159],a[12:15]
ds_read_b64_tr_b16 v[100:101],v1 offset:10496
ds_read_b64_tr_b16 v[102:103],v1 offset:15616
v_mfma_f32_16x16x32_bf16 a[12:15],v[96:99],a[140:143],a[12:15]
s_waitcnt lgkmcnt(4)
ds_read_b64_tr_b16 v[96:97],v1 offset:256
v_mfma_f32_16x16x32_bf16 a[16:19],v[112:115],a[128:131],a[16:19]
ds_read_b64_tr_b16 v[98:99],v1 offset:5376
v_mfma_f32_16x16x32_bf16 a[16:19],v[116:119],a[144:147],a[16:19]
ds_read_b64_tr_b16 v[104:105],v1 offset:20736
v_mfma_f32_16x16x32_bf16 a[16:19],v[120:123],a[160:163],a[16:19]
ds_read_b64_tr_b16 v[106:107],v1 offset:25856
v_add_u32 v1,0xffff6000,v1
v_mfma_f32_16x16x32_bf16 a[16:19],v[124:127],a[176:179],a[16:19]
v_mfma_f32_16x16x32_bf16 a[20:23],v[124:127],a[180:183],a[20:23]
v_mfma_f32_16x16x32_bf16 a[20:23],v[120:123],a[164:167],a[20:23]
v_mfma_f32_16x16x32_bf16 a[20:23],v[116:119],a[148:151],a[20:23]
v_mfma_f32_16x16x32_bf16 a[20:23],v[112:115],a[132:135],a[20:23]
v_mfma_f32_16x16x32_bf16 a[24:27],v[112:115],a[136:139],a[24:27]
buffer_load_dwordx4 v[180:183],v6,s[28:31],s90 offen offset:1024 sc1
v_mfma_f32_16x16x32_bf16 a[24:27],v[116:119],a[152:155],a[24:27]
v_mfma_f32_16x16x32_bf16 a[24:27],v[120:123],a[168:171],a[24:27]
v_mfma_f32_16x16x32_bf16 a[24:27],v[124:127],a[184:187],a[24:27]
v_mfma_f32_16x16x32_bf16 a[28:31],v[124:127],a[188:191],a[28:31]
ds_read_b64_tr_b16 v[124:125],v2 offset:31008
ds_read_b64_tr_b16 v[126:127],v2 offset:36128
v_mfma_f32_16x16x32_bf16 a[28:31],v[120:123],a[172:175],a[28:31]
ds_read_b64_tr_b16 v[120:121],v2 offset:20768
v_mfma_f32_16x16x32_bf16 a[28:31],v[116:119],a[156:159],a[28:31]
ds_read_b64_tr_b16 v[116:117],v2 offset:10528
v_mfma_f32_16x16x32_bf16 a[28:31],v[112:115],a[140:143],a[28:31]
ds_read_b64_tr_b16 v[112:113],v2 offset:288
v_mfma_f32_16x16x32_bf16 v[128:131],v[64:67],a[128:131],v[128:131]
ds_read_b64_tr_b16 v[114:115],v2 offset:5408
ds_read_b64_tr_b16 v[118:119],v2 offset:15648
v_mfma_f32_16x16x32_bf16 v[128:131],v[68:71],a[144:147],v[128:131]
ds_read_b64_tr_b16 v[122:123],v2 offset:25888
v_add_u32 v2,0xffff6000,v2
v_mfma_f32_16x16x32_bf16 v[128:131],v[72:75],a[160:163],v[128:131]
v_mfma_f32_16x16x32_bf16 v[128:131],v[76:79],a[176:179],v[128:131]
buffer_load_dwordx4 v[164:167],v6,s[28:31],s89 offen offset:1024 sc1
v_mfma_f32_16x16x32_bf16 v[136:139],v[76:79],a[180:183],v[136:139]
v_mfma_f32_16x16x32_bf16 v[136:139],v[72:75],a[164:167],v[136:139]
v_mfma_f32_16x16x32_bf16 v[136:139],v[68:71],a[148:151],v[136:139]
v_mfma_f32_16x16x32_bf16 v[136:139],v[64:67],a[132:135],v[136:139]
v_mfma_f32_16x16x32_bf16 v[144:147],v[64:67],a[136:139],v[144:147]
v_mfma_f32_16x16x32_bf16 v[144:147],v[68:71],a[152:155],v[144:147]
v_mfma_f32_16x16x32_bf16 v[144:147],v[72:75],a[168:171],v[144:147]
v_mfma_f32_16x16x32_bf16 v[144:147],v[76:79],a[184:187],v[144:147]
v_mfma_f32_16x16x32_bf16 v[152:155],v[76:79],a[188:191],v[152:155]
v_mfma_f32_16x16x32_bf16 v[152:155],v[72:75],a[172:175],v[152:155]
v_mfma_f32_16x16x32_bf16 v[152:155],v[68:71],a[156:159],v[152:155]
buffer_load_dwordx4 v[148:151],v6,s[28:31],s88 offen offset:1024 sc1
v_mfma_f32_16x16x32_bf16 v[152:155],v[64:67],a[140:143],v[152:155]
s_barrier
v_mfma_f32_16x16x32_bf16 v[160:163],v[80:83],a[128:131],v[160:163]
ds_read_b64_tr_b16 v[64:65],v1
ds_read_b64_tr_b16 v[66:67],v1 offset:5120
v_mfma_f32_16x16x32_bf16 v[160:163],v[84:87],a[144:147],v[160:163]
ds_read_b64_tr_b16 v[68:69],v1 offset:10240
v_mfma_f32_16x16x32_bf16 v[160:163],v[88:91],a[160:163],v[160:163]
ds_read_b64_tr_b16 v[70:71],v1 offset:15360
v_mfma_f32_16x16x32_bf16 v[160:163],v[92:95],a[176:179],v[160:163]
buffer_load_dwordx4 v24,s[48:51],s83 offen lds
v_mfma_f32_16x16x32_bf16 v[168:171],v[92:95],a[180:183],v[168:171]
ds_read_b64_tr_b16 v[72:73],v1 offset:20480
ds_read_b64_tr_b16 v[74:75],v1 offset:25600
v_mfma_f32_16x16x32_bf16 v[168:171],v[88:91],a[164:167],v[168:171]
ds_read_b64_tr_b16 v[76:77],v1 offset:30720
v_mfma_f32_16x16x32_bf16 v[168:171],v[84:87],a[148:151],v[168:171]
ds_read_b64_tr_b16 v[78:79],v1 offset:35840
v_mfma_f32_16x16x32_bf16 v[168:171],v[80:83],a[132:135],v[168:171]
v_mfma_f32_16x16x32_bf16 v[176:179],v[80:83],a[136:139],v[176:179]
buffer_load_dwordx4 v[132:135],v6,s[28:31],s87 offen offset:1024 sc1
v_mfma_f32_16x16x32_bf16 v[176:179],v[84:87],a[152:155],v[176:179]
v_mfma_f32_16x16x32_bf16 v[176:179],v[88:91],a[168:171],v[176:179]
v_mfma_f32_16x16x32_bf16 v[176:179],v[92:95],a[184:187],v[176:179]
v_mfma_f32_16x16x32_bf16 v[184:187],v[92:95],a[188:191],v[184:187]
ds_read_b64_tr_b16 v[92:93],v2 offset:30752
ds_read_b64_tr_b16 v[94:95],v2 offset:35872
v_mfma_f32_16x16x32_bf16 v[184:187],v[88:91],a[172:175],v[184:187]
ds_read_b64_tr_b16 v[88:89],v2 offset:20512
v_mfma_f32_16x16x32_bf16 v[184:187],v[84:87],a[156:159],v[184:187]
buffer_load_dwordx4 v25,s[48:51],s83 offen offset:1024 lds
v_mfma_f32_16x16x32_bf16 v[184:187],v[80:83],a[140:143],v[184:187]
s_waitcnt lgkmcnt(3)
ds_read_b64_tr_b16 v[80:81],v2 offset:32
ds_read_b64_tr_b16 v[82:83],v2 offset:5152
v_mfma_f32_16x16x32_bf16 v[192:195],v[96:99],a[128:131],v[192:195]
ds_read_b64_tr_b16 v[84:85],v2 offset:10272
v_mfma_f32_16x16x32_bf16 v[192:195],v[100:103],a[144:147],v[192:195]
ds_read_b64_tr_b16 v[86:87],v2 offset:15392
v_mfma_f32_16x16x32_bf16 v[192:195],v[104:107],a[160:163],v[192:195]
ds_read_b64_tr_b16 v[90:91],v2 offset:25632
v_mfma_f32_16x16x32_bf16 v[192:195],v[108:111],a[176:179],v[192:195]
v_mfma_f32_16x16x32_bf16 v[200:203],v[108:111],a[180:183],v[200:203]
buffer_load_dwordx4 v26,s[48:51],s83 offen offset:2048 lds
v_mfma_f32_16x16x32_bf16 v[200:203],v[104:107],a[164:167],v[200:203]
v_mfma_f32_16x16x32_bf16 v[200:203],v[100:103],a[148:151],v[200:203]
v_mfma_f32_16x16x32_bf16 v[200:203],v[96:99],a[132:135],v[200:203]
v_mfma_f32_16x16x32_bf16 v[208:211],v[96:99],a[136:139],v[208:211]
v_mfma_f32_16x16x32_bf16 v[208:211],v[100:103],a[152:155],v[208:211]
buffer_load_dwordx4 v[196:199],v6,s[28:31],s87 offen offset:2048 sc1
v_mfma_f32_16x16x32_bf16 v[208:211],v[104:107],a[168:171],v[208:211]
v_mfma_f32_16x16x32_bf16 v[208:211],v[108:111],a[184:187],v[208:211]
v_mfma_f32_16x16x32_bf16 v[216:219],v[108:111],a[188:191],v[216:219]
ds_read_b64_tr_b16 v[108:109],v1 offset:30784
ds_read_b64_tr_b16 v[110:111],v1 offset:35904
v_mfma_f32_16x16x32_bf16 v[216:219],v[104:107],a[172:175],v[216:219]
ds_read_b64_tr_b16 v[104:105],v1 offset:20544
v_mfma_f32_16x16x32_bf16 v[216:219],v[100:103],a[156:159],v[216:219]
ds_read_b64_tr_b16 v[100:101],v1 offset:10304
v_mfma_f32_16x16x32_bf16 v[216:219],v[96:99],a[140:143],v[216:219]
buffer_load_dwordx4 v27,s[48:51],s83 offen offset:3072 lds
v_mfma_f32_16x16x32_bf16 v[224:227],v[112:115],a[128:131],v[224:227]
s_add_u32 m0,s86,126976
ds_read_b64_tr_b16 v[96:97],v1 offset:64
ds_read_b64_tr_b16 v[98:99],v1 offset:5184
v_mfma_f32_16x16x32_bf16 v[224:227],v[116:119],a[144:147],v[224:227]
ds_read_b64_tr_b16 v[102:103],v1 offset:15424
v_mfma_f32_16x16x32_bf16 v[224:227],v[120:123],a[160:163],v[224:227]
ds_read_b64_tr_b16 v[106:107],v1 offset:25664
v_mfma_f32_16x16x32_bf16 v[224:227],v[124:127],a[176:179],v[224:227]
v_mfma_f32_16x16x32_bf16 v[232:235],v[124:127],a[180:183],v[232:235]
buffer_load_dwordx4 v[212:215],v6,s[28:31],s88 offen offset:2048 sc1
v_mfma_f32_16x16x32_bf16 v[232:235],v[120:123],a[164:167],v[232:235]
v_mfma_f32_16x16x32_bf16 v[232:235],v[116:119],a[148:151],v[232:235]
v_mfma_f32_16x16x32_bf16 v[232:235],v[112:115],a[132:135],v[232:235]
v_mfma_f32_16x16x32_bf16 v[240:243],v[112:115],a[136:139],v[240:243]
v_mfma_f32_16x16x32_bf16 v[240:243],v[116:119],a[152:155],v[240:243]
v_mfma_f32_16x16x32_bf16 v[240:243],v[120:123],a[168:171],v[240:243]
buffer_load_dwordx4 v7,s[48:51],s83 offen lds
v_mfma_f32_16x16x32_bf16 v[240:243],v[124:127],a[184:187],v[240:243]
s_add_u32 m0,s86,128000
s_mul_i32 s83,s63,20480
v_mfma_f32_16x16x32_bf16 v[248:251],v[124:127],a[188:191],v[248:251]
ds_read_b64_tr_b16 v[124:125],v2 offset:30816
ds_read_b64_tr_b16 v[126:127],v2 offset:35936
v_mfma_f32_16x16x32_bf16 v[248:251],v[120:123],a[172:175],v[248:251]
ds_read_b64_tr_b16 v[120:121],v2 offset:20576
v_mfma_f32_16x16x32_bf16 v[248:251],v[116:119],a[156:159],v[248:251]
ds_read_b64_tr_b16 v[116:117],v2 offset:10336
v_mfma_f32_16x16x32_bf16 v[248:251],v[112:115],a[140:143],v[248:251]
buffer_load_dwordx4 v[228:231],v6,s[28:31],s89 offen offset:2048 sc1
v_mfma_f32_16x16x32_bf16 a[192:195],v[64:67],a[68:71],a[192:195]
s_waitcnt vmcnt(17)
ds_read_b64_tr_b16 v[112:113],v2 offset:96
ds_read_b64_tr_b16 v[114:115],v2 offset:5216
v_mfma_f32_16x16x32_bf16 a[192:195],v[68:71],a[76:79],a[192:195]
ds_read_b64_tr_b16 v[118:119],v2 offset:15456
v_mfma_f32_16x16x32_bf16 a[192:195],v[72:75],a[84:87],a[192:195]
ds_read_b64_tr_b16 v[122:123],v2 offset:25696
v_mfma_f32_16x16x32_bf16 a[192:195],v[76:79],a[92:95],a[192:195]
v_mfma_f32_16x16x32_bf16 a[196:199],v[76:79],a[124:127],a[196:199]
v_mfma_f32_16x16x32_bf16 a[196:199],v[72:75],a[116:119],a[196:199]
buffer_load_dwordx4 v24,s[48:51],s84 offen lds
v_mfma_f32_16x16x32_bf16 a[196:199],v[68:71],a[108:111],a[196:199]
v_mfma_f32_16x16x32_bf16 a[196:199],v[64:67],a[100:103],a[196:199]
v_mfma_f32_16x16x32_bf16 a[200:203],v[64:67],a[64:67],a[200:203]
v_mfma_f32_16x16x32_bf16 a[200:203],v[68:71],a[72:75],a[200:203]
s_waitcnt vmcnt(16)
v_mfma_f32_16x16x32_bf16 a[200:203],v[72:75],a[80:83],a[200:203]
buffer_load_dwordx4 v[244:247],v6,s[28:31],s90 offen offset:2048 sc1
v_mfma_f32_16x16x32_bf16 a[200:203],v[76:79],a[88:91],a[200:203]
s_waitcnt vmcnt(16)
v_mfma_f32_16x16x32_bf16 a[204:207],v[76:79],a[120:123],a[204:207]
ds_read_b64_tr_b16 v[76:77],v1 offset:30848
ds_read_b64_tr_b16 v[78:79],v1 offset:35968
v_mfma_f32_16x16x32_bf16 a[204:207],v[72:75],a[112:115],a[204:207]
ds_read_b64_tr_b16 v[72:73],v1 offset:20608
v_mfma_f32_16x16x32_bf16 a[204:207],v[68:71],a[104:107],a[204:207]
ds_read_b64_tr_b16 v[68:69],v1 offset:10368
v_mfma_f32_16x16x32_bf16 a[204:207],v[64:67],a[96:99],a[204:207]
s_waitcnt lgkmcnt(4)
ds_read_b64_tr_b16 v[64:65],v1 offset:128
v_mfma_f32_16x16x32_bf16 a[208:211],v[80:83],a[68:71],a[208:211]
buffer_load_dwordx4 v25,s[48:51],s84 offen offset:1024 lds
v_mfma_f32_16x16x32_bf16 a[208:211],v[84:87],a[76:79],a[208:211]
ds_read_b64_tr_b16 v[66:67],v1 offset:5248
ds_read_b64_tr_b16 v[70:71],v1 offset:15488
v_mfma_f32_16x16x32_bf16 a[208:211],v[88:91],a[84:87],a[208:211]
ds_read_b64_tr_b16 v[74:75],v1 offset:25728
v_mfma_f32_16x16x32_bf16 a[208:211],v[92:95],a[92:95],a[208:211]
v_mfma_f32_16x16x32_bf16 a[212:215],v[92:95],a[124:127],a[212:215]
v_mfma_f32_16x16x32_bf16 a[212:215],v[88:91],a[116:119],a[212:215]
buffer_load_dwordx4 v[188:191],v28,s[28:31],s90 offen offset:3072 sc1
v_mfma_f32_16x16x32_bf16 a[212:215],v[84:87],a[108:111],a[212:215]
v_mfma_f32_16x16x32_bf16 a[212:215],v[80:83],a[100:103],a[212:215]
v_mfma_f32_16x16x32_bf16 a[216:219],v[80:83],a[64:67],a[216:219]
v_mfma_f32_16x16x32_bf16 a[216:219],v[84:87],a[72:75],a[216:219]
v_mfma_f32_16x16x32_bf16 a[216:219],v[88:91],a[80:83],a[216:219]
)A"
R"A(v_mfma_f32_16x16x32_bf16 a[216:219],v[92:95],a[88:91],a[216:219]
buffer_load_dwordx4 v26,s[48:51],s84 offen offset:2048 lds
v_mfma_f32_16x16x32_bf16 a[220:223],v[92:95],a[120:123],a[220:223]
ds_read_b64_tr_b16 v[92:93],v2 offset:30880
ds_read_b64_tr_b16 v[94:95],v2 offset:36000
v_mfma_f32_16x16x32_bf16 a[220:223],v[88:91],a[112:115],a[220:223]
ds_read_b64_tr_b16 v[88:89],v2 offset:20640
v_mfma_f32_16x16x32_bf16 a[220:223],v[84:87],a[104:107],a[220:223]
ds_read_b64_tr_b16 v[84:85],v2 offset:10400
v_mfma_f32_16x16x32_bf16 a[220:223],v[80:83],a[96:99],a[220:223]
ds_read_b64_tr_b16 v[80:81],v2 offset:160
v_mfma_f32_16x16x32_bf16 a[224:227],v[96:99],a[68:71],a[224:227]
ds_read_b64_tr_b16 v[82:83],v2 offset:5280
ds_read_b64_tr_b16 v[86:87],v2 offset:15520
v_mfma_f32_16x16x32_bf16 a[224:227],v[100:103],a[76:79],a[224:227]
buffer_load_dwordx4 v27,s[48:51],s84 offen offset:3072 lds
v_mfma_f32_16x16x32_bf16 a[224:227],v[104:107],a[84:87],a[224:227]
s_add_u32 m0,s86,132096
ds_read_b64_tr_b16 v[90:91],v2 offset:25760
v_mfma_f32_16x16x32_bf16 a[224:227],v[108:111],a[92:95],a[224:227]
v_mfma_f32_16x16x32_bf16 a[228:231],v[108:111],a[124:127],a[228:231]
v_mfma_f32_16x16x32_bf16 a[228:231],v[104:107],a[116:119],a[228:231]
v_mfma_f32_16x16x32_bf16 a[228:231],v[100:103],a[108:111],a[228:231]
buffer_load_dwordx4 v[172:175],v28,s[28:31],s89 offen offset:3072 sc1
v_mfma_f32_16x16x32_bf16 a[228:231],v[96:99],a[100:103],a[228:231]
v_mfma_f32_16x16x32_bf16 a[232:235],v[96:99],a[64:67],a[232:235]
v_mfma_f32_16x16x32_bf16 a[232:235],v[100:103],a[72:75],a[232:235]
v_mfma_f32_16x16x32_bf16 a[232:235],v[104:107],a[80:83],a[232:235]
v_mfma_f32_16x16x32_bf16 a[232:235],v[108:111],a[88:91],a[232:235]
v_mfma_f32_16x16x32_bf16 a[236:239],v[108:111],a[120:123],a[236:239]
buffer_load_dwordx4 v7,s[48:51],s84 offen lds
v_mfma_f32_16x16x32_bf16 a[236:239],v[104:107],a[112:115],a[236:239]
ds_read_b64_tr_b16 v[104:105],v1 offset:20672
ds_read_b64_tr_b16 v[106:107],v1 offset:25792
v_mfma_f32_16x16x32_bf16 a[236:239],v[100:103],a[104:107],a[236:239]
ds_read_b64_tr_b16 v[100:101],v1 offset:10432
v_mfma_f32_16x16x32_bf16 a[236:239],v[96:99],a[96:99],a[236:239]
ds_read_b64_tr_b16 v[96:97],v1 offset:192
v_mfma_f32_16x16x32_bf16 a[240:243],v[112:115],a[68:71],a[240:243]
ds_read_b64_tr_b16 v[98:99],v1 offset:5312
v_mfma_f32_16x16x32_bf16 a[240:243],v[116:119],a[76:79],a[240:243]
buffer_load_dwordx4 v[156:159],v28,s[28:31],s88 offen offset:3072 sc1
v_mfma_f32_16x16x32_bf16 a[240:243],v[120:123],a[84:87],a[240:243]
ds_read_b64_tr_b16 v[102:103],v1 offset:15552
ds_read_b64_tr_b16 v[108:109],v1 offset:30912
v_mfma_f32_16x16x32_bf16 a[240:243],v[124:127],a[92:95],a[240:243]
ds_read_b64_tr_b16 v[110:111],v1 offset:36032
v_add_u32 v1,0xffff6000,v1
v_mfma_f32_16x16x32_bf16 a[244:247],v[124:127],a[124:127],a[244:247]
v_mfma_f32_16x16x32_bf16 a[244:247],v[120:123],a[116:119],a[244:247]
v_mfma_f32_16x16x32_bf16 a[244:247],v[116:119],a[108:111],a[244:247]
buffer_load_dwordx4 v[140:143],v28,s[28:31],s87 offen offset:3072 sc1
v_mfma_f32_16x16x32_bf16 a[244:247],v[112:115],a[100:103],a[244:247]
s_add_u32 s87,s64,3
s_and_b32 s87,s87,3
s_lshl_b32 s87,s87,1
s_or_b32 s87,s87,1
s_lshl_b32 s87,s87,16
s_add_u32 s88,s87,4096
s_add_u32 s89,s87,8192
s_add_u32 s90,s87,12288
v_mfma_f32_16x16x32_bf16 a[248:251],v[112:115],a[64:67],a[248:251]
v_mfma_f32_16x16x32_bf16 a[248:251],v[116:119],a[72:75],a[248:251]
v_mfma_f32_16x16x32_bf16 a[248:251],v[120:123],a[80:83],a[248:251]
v_mfma_f32_16x16x32_bf16 a[248:251],v[124:127],a[88:91],a[248:251]
buffer_load_dwordx4 a[128:131],v6,s[28:31],s87 offen sc1
v_mfma_f32_16x16x32_bf16 a[252:255],v[124:127],a[120:123],a[252:255]
ds_read_b64_tr_b16 v[124:125],v2 offset:30944
ds_read_b64_tr_b16 v[126:127],v2 offset:36064
v_mfma_f32_16x16x32_bf16 a[252:255],v[120:123],a[112:115],a[252:255]
ds_read_b64_tr_b16 v[120:121],v2 offset:20704
v_mfma_f32_16x16x32_bf16 a[252:255],v[116:119],a[104:107],a[252:255]
ds_read_b64_tr_b16 v[116:117],v2 offset:10464
v_mfma_f32_16x16x32_bf16 a[252:255],v[112:115],a[96:99],a[252:255]
s_waitcnt lgkmcnt(4)
ds_read_b64_tr_b16 v[112:113],v2 offset:224
v_mfma_f32_16x16x32_bf16 a[0:3],v[64:67],a[68:71],a[0:3]
buffer_load_dwordx4 a[144:147],v6,s[28:31],s88 offen sc1
v_mfma_f32_16x16x32_bf16 a[0:3],v[68:71],a[76:79],a[0:3]
ds_read_b64_tr_b16 v[114:115],v2 offset:5344
ds_read_b64_tr_b16 v[118:119],v2 offset:15584
v_mfma_f32_16x16x32_bf16 a[0:3],v[72:75],a[84:87],a[0:3]
ds_read_b64_tr_b16 v[122:123],v2 offset:25824
v_add_u32 v2,0xffff6000,v2
v_mfma_f32_16x16x32_bf16 a[0:3],v[76:79],a[92:95],a[0:3]
v_mfma_f32_16x16x32_bf16 a[4:7],v[76:79],a[124:127],a[4:7]
v_mfma_f32_16x16x32_bf16 a[4:7],v[72:75],a[116:119],a[4:7]
buffer_load_dwordx4 a[160:163],v6,s[28:31],s89 offen sc1
v_mfma_f32_16x16x32_bf16 a[4:7],v[68:71],a[108:111],a[4:7]
v_mfma_f32_16x16x32_bf16 a[4:7],v[64:67],a[100:103],a[4:7]
v_mfma_f32_16x16x32_bf16 a[8:11],v[64:67],a[64:67],a[8:11]
v_mfma_f32_16x16x32_bf16 a[8:11],v[68:71],a[72:75],a[8:11]
v_mfma_f32_16x16x32_bf16 a[8:11],v[72:75],a[80:83],a[8:11]
buffer_load_dwordx4 a[176:179],v6,s[28:31],s90 offen sc1
v_mfma_f32_16x16x32_bf16 a[8:11],v[76:79],a[88:91],a[8:11]
v_mfma_f32_16x16x32_bf16 a[12:15],v[76:79],a[120:123],a[12:15]
ds_read_b64_tr_b16 v[76:77],v1 offset:30720
ds_read_b64_tr_b16 v[78:79],v1 offset:35840
v_mfma_f32_16x16x32_bf16 a[12:15],v[72:75],a[112:115],a[12:15]
ds_read_b64_tr_b16 v[72:73],v1 offset:20480
v_mfma_f32_16x16x32_bf16 a[12:15],v[68:71],a[104:107],a[12:15]
ds_read_b64_tr_b16 v[68:69],v1 offset:10240
v_mfma_f32_16x16x32_bf16 a[12:15],v[64:67],a[96:99],a[12:15]
buffer_load_dwordx4 a[180:183],v6,s[28:31],s90 offen offset:1024 sc1
v_mfma_f32_16x16x32_bf16 a[16:19],v[80:83],a[68:71],a[16:19]
ds_read_b64_tr_b16 v[64:65],v1
ds_read_b64_tr_b16 v[66:67],v1 offset:5120
v_mfma_f32_16x16x32_bf16 a[16:19],v[84:87],a[76:79],a[16:19]
ds_read_b64_tr_b16 v[70:71],v1 offset:15360
v_mfma_f32_16x16x32_bf16 a[16:19],v[88:91],a[84:87],a[16:19]
ds_read_b64_tr_b16 v[74:75],v1 offset:25600
v_add_u32 v1,0x1e000,v1
v_mfma_f32_16x16x32_bf16 a[16:19],v[92:95],a[92:95],a[16:19]
v_mfma_f32_16x16x32_bf16 a[20:23],v[92:95],a[124:127],a[20:23]
buffer_load_dwordx4 a[164:167],v6,s[28:31],s89 offen offset:1024 sc1
v_mfma_f32_16x16x32_bf16 a[20:23],v[88:91],a[116:119],a[20:23]
v_mfma_f32_16x16x32_bf16 a[20:23],v[84:87],a[108:111],a[20:23]
v_mfma_f32_16x16x32_bf16 a[20:23],v[80:83],a[100:103],a[20:23]
v_mfma_f32_16x16x32_bf16 a[24:27],v[80:83],a[64:67],a[24:27]
v_mfma_f32_16x16x32_bf16 a[24:27],v[84:87],a[72:75],a[24:27]
buffer_load_dwordx4 a[148:151],v6,s[28:31],s88 offen offset:1024 sc1
v_mfma_f32_16x16x32_bf16 a[24:27],v[88:91],a[80:83],a[24:27]
v_mfma_f32_16x16x32_bf16 a[24:27],v[92:95],a[88:91],a[24:27]
v_mfma_f32_16x16x32_bf16 a[28:31],v[92:95],a[120:123],a[28:31]
ds_read_b64_tr_b16 v[92:93],v2 offset:30752
ds_read_b64_tr_b16 v[94:95],v2 offset:35872
v_mfma_f32_16x16x32_bf16 a[28:31],v[88:91],a[112:115],a[28:31]
s_nop 0
L_pd17_%=:
s_cbranch_execz L_trtg2F_%=
ds_read_b64_tr_b16 v[88:89],v2 offset:20512
v_mfma_f32_16x16x32_bf16 a[28:31],v[84:87],a[104:107],a[28:31]
buffer_load_dwordx4 a[132:135],v6,s[28:31],s87 offen offset:1024 sc1
v_mfma_f32_16x16x32_bf16 a[28:31],v[80:83],a[96:99],a[28:31]
ds_read_b64_tr_b16 v[80:81],v2 offset:32
ds_read_b64_tr_b16 v[82:83],v2 offset:5152
v_mfma_f32_16x16x32_bf16 v[128:131],v[96:99],a[68:71],v[128:131]
ds_read_b64_tr_b16 v[84:85],v2 offset:10272
v_mfma_f32_16x16x32_bf16 v[128:131],v[100:103],a[76:79],v[128:131]
ds_read_b64_tr_b16 v[86:87],v2 offset:15392
v_mfma_f32_16x16x32_bf16 v[128:131],v[104:107],a[84:87],v[128:131]
ds_read_b64_tr_b16 v[90:91],v2 offset:25632
v_add_u32 v2,0x1e000,v2
v_mfma_f32_16x16x32_bf16 v[128:131],v[108:111],a[92:95],v[128:131]
buffer_load_dwordx4 a[136:139],v6,s[28:31],s87 offen offset:2048 sc1
v_mfma_f32_16x16x32_bf16 v[136:139],v[108:111],a[124:127],v[136:139]
v_mfma_f32_16x16x32_bf16 v[136:139],v[104:107],a[116:119],v[136:139]
v_mfma_f32_16x16x32_bf16 v[136:139],v[100:103],a[108:111],v[136:139]
v_mfma_f32_16x16x32_bf16 v[136:139],v[96:99],a[100:103],v[136:139]
v_mfma_f32_16x16x32_bf16 v[144:147],v[96:99],a[64:67],v[144:147]
buffer_load_dwordx4 a[152:155],v6,s[28:31],s88 offen offset:2048 sc1
v_mfma_f32_16x16x32_bf16 v[144:147],v[100:103],a[72:75],v[144:147]
v_mfma_f32_16x16x32_bf16 v[144:147],v[104:107],a[80:83],v[144:147]
v_mfma_f32_16x16x32_bf16 v[144:147],v[108:111],a[88:91],v[144:147]
v_mfma_f32_16x16x32_bf16 v[152:155],v[108:111],a[120:123],v[152:155]
v_mfma_f32_16x16x32_bf16 v[152:155],v[104:107],a[112:115],v[152:155]
buffer_load_dwordx4 a[168:171],v6,s[28:31],s89 offen offset:2048 sc1
v_mfma_f32_16x16x32_bf16 v[152:155],v[100:103],a[104:107],v[152:155]
v_mfma_f32_16x16x32_bf16 v[152:155],v[96:99],a[96:99],v[152:155]
s_waitcnt vmcnt(13)
s_barrier
s_waitcnt lgkmcnt(0)
v_mfma_f32_16x16x32_bf16 v[160:163],v[112:115],a[68:71],v[160:163]
ds_read_b64_tr_b16 v[96:97],v1
ds_read_b64_tr_b16 v[98:99],v1 offset:5120
v_mfma_f32_16x16x32_bf16 v[160:163],v[116:119],a[76:79],v[160:163]
buffer_load_dwordx4 a[184:187],v6,s[28:31],s90 offen offset:2048 sc1
v_mfma_f32_16x16x32_bf16 v[160:163],v[120:123],a[84:87],v[160:163]
ds_read_b64_tr_b16 v[100:101],v1 offset:10240
ds_read_b64_tr_b16 v[102:103],v1 offset:15360
v_mfma_f32_16x16x32_bf16 v[160:163],v[124:127],a[92:95],v[160:163]
ds_read_b64_tr_b16 v[104:105],v1 offset:20480
v_mfma_f32_16x16x32_bf16 v[168:171],v[124:127],a[124:127],v[168:171]
ds_read_b64_tr_b16 v[106:107],v1 offset:25600
v_mfma_f32_16x16x32_bf16 v[168:171],v[120:123],a[116:119],v[168:171]
ds_read_b64_tr_b16 v[108:109],v1 offset:30720
v_mfma_f32_16x16x32_bf16 v[168:171],v[116:119],a[108:111],v[168:171]
buffer_load_dwordx4 a[188:191],v28,s[28:31],s90 offen offset:3072 sc1
v_mfma_f32_16x16x32_bf16 v[168:171],v[112:115],a[100:103],v[168:171]
ds_read_b64_tr_b16 v[110:111],v1 offset:35840
v_add_u32 v1,0xfffe2000,v1
v_mfma_f32_16x16x32_bf16 v[176:179],v[112:115],a[64:67],v[176:179]
v_mfma_f32_16x16x32_bf16 v[176:179],v[116:119],a[72:75],v[176:179]
v_mfma_f32_16x16x32_bf16 v[176:179],v[120:123],a[80:83],v[176:179]
v_mfma_f32_16x16x32_bf16 v[176:179],v[124:127],a[88:91],v[176:179]
buffer_load_dwordx4 a[172:175],v28,s[28:31],s89 offen offset:3072 sc1
v_mfma_f32_16x16x32_bf16 v[184:187],v[124:127],a[120:123],v[184:187]
ds_read_b64_tr_b16 v[124:125],v2 offset:30752
ds_read_b64_tr_b16 v[126:127],v2 offset:35872
v_mfma_f32_16x16x32_bf16 v[184:187],v[120:123],a[112:115],v[184:187]
ds_read_b64_tr_b16 v[120:121],v2 offset:20512
v_mfma_f32_16x16x32_bf16 v[184:187],v[116:119],a[104:107],v[184:187]
ds_read_b64_tr_b16 v[116:117],v2 offset:10272
v_mfma_f32_16x16x32_bf16 v[184:187],v[112:115],a[96:99],v[184:187]
ds_read_b64_tr_b16 v[112:113],v2 offset:32
v_mfma_f32_16x16x32_bf16 a[192:195],v[64:67],v[32:35],a[192:195]
buffer_load_dwordx4 a[156:159],v28,s[28:31],s88 offen offset:3072 sc1
v_mfma_f32_16x16x32_bf16 a[192:195],v[68:71],v[36:39],a[192:195]
ds_read_b64_tr_b16 v[114:115],v2 offset:5152
ds_read_b64_tr_b16 v[118:119],v2 offset:15392
v_mfma_f32_16x16x32_bf16 a[192:195],v[72:75],v[40:43],a[192:195]
ds_read_b64_tr_b16 v[122:123],v2 offset:25632
v_add_u32 v2,0xfffe2000,v2
v_mfma_f32_16x16x32_bf16 a[192:195],v[76:79],v[44:47],a[192:195]
v_mfma_f32_16x16x32_bf16 a[196:199],v[76:79],v[180:183],a[196:199]
v_mfma_f32_16x16x32_bf16 a[196:199],v[72:75],v[164:167],a[196:199]
buffer_load_dwordx4 a[140:143],v28,s[28:31],s87 offen offset:3072 sc1
v_mfma_f32_16x16x32_bf16 a[196:199],v[68:71],v[148:151],a[196:199]
s_mul_i32 s87,s60,81920
s_add_u32 s87,s87,s66
s_add_u32 s83,s83,s87
v_mfma_f32_16x16x32_bf16 a[196:199],v[64:67],v[132:135],a[196:199]
s_lshl_b32 s66,s63,6
s_add_u32 s87,s87,s66
s_add_u32 s88,s87,20480
v_mfma_f32_16x16x32_bf16 a[200:203],v[64:67],v[196:199],a[200:203]
s_add_u32 s89,s87,40960
s_add_u32 s90,s87,61440
s_add_u32 s66,s77,4096
v_mfma_f32_16x16x32_bf16 a[200:203],v[68:71],v[212:215],a[200:203]
v_mfma_f32_16x16x32_bf16 a[200:203],v[72:75],v[228:231],a[200:203]
v_mfma_f32_16x16x32_bf16 a[200:203],v[76:79],v[244:247],a[200:203]
v_mfma_f32_16x16x32_bf16 a[204:207],v[76:79],v[188:191],a[204:207]
)A"
R"A(ds_read_b64_tr_b16 v[76:77],v1 offset:30784
ds_read_b64_tr_b16 v[78:79],v1 offset:35904
v_mfma_f32_16x16x32_bf16 a[204:207],v[72:75],v[172:175],a[204:207]
s_waitcnt vmcnt(17)
ds_read_b64_tr_b16 v[72:73],v1 offset:20544
v_mfma_f32_16x16x32_bf16 a[204:207],v[68:71],v[156:159],a[204:207]
s_waitcnt vmcnt(16)
ds_read_b64_tr_b16 v[68:69],v1 offset:10304
v_mfma_f32_16x16x32_bf16 a[204:207],v[64:67],v[140:143],a[204:207]
ds_read_b64_tr_b16 v[64:65],v1 offset:64
v_mfma_f32_16x16x32_bf16 a[208:211],v[80:83],v[32:35],a[208:211]
ds_read_b64_tr_b16 v[66:67],v1 offset:5184
ds_read_b64_tr_b16 v[70:71],v1 offset:15424
v_mfma_f32_16x16x32_bf16 a[208:211],v[84:87],v[36:39],a[208:211]
ds_read_b64_tr_b16 v[74:75],v1 offset:25664
v_add_u32 v1,0x1e000,v1
v_mfma_f32_16x16x32_bf16 a[208:211],v[88:91],v[40:43],a[208:211]
v_mfma_f32_16x16x32_bf16 a[208:211],v[92:95],v[44:47],a[208:211]
v_mfma_f32_16x16x32_bf16 a[212:215],v[92:95],v[180:183],a[212:215]
v_mfma_f32_16x16x32_bf16 a[212:215],v[88:91],v[164:167],a[212:215]
v_mfma_f32_16x16x32_bf16 a[212:215],v[84:87],v[148:151],a[212:215]
v_mfma_f32_16x16x32_bf16 a[212:215],v[80:83],v[132:135],a[212:215]
v_mfma_f32_16x16x32_bf16 a[216:219],v[80:83],v[196:199],a[216:219]
v_mfma_f32_16x16x32_bf16 a[216:219],v[84:87],v[212:215],a[216:219]
v_mfma_f32_16x16x32_bf16 a[216:219],v[88:91],v[228:231],a[216:219]
v_mfma_f32_16x16x32_bf16 a[216:219],v[92:95],v[244:247],a[216:219]
v_mfma_f32_16x16x32_bf16 a[220:223],v[92:95],v[188:191],a[220:223]
ds_read_b64_tr_b16 v[92:93],v2 offset:30816
ds_read_b64_tr_b16 v[94:95],v2 offset:35936
v_mfma_f32_16x16x32_bf16 a[220:223],v[88:91],v[172:175],a[220:223]
ds_read_b64_tr_b16 v[88:89],v2 offset:20576
v_mfma_f32_16x16x32_bf16 a[220:223],v[84:87],v[156:159],a[220:223]
ds_read_b64_tr_b16 v[84:85],v2 offset:10336
v_mfma_f32_16x16x32_bf16 a[220:223],v[80:83],v[140:143],a[220:223]
s_waitcnt vmcnt(15) lgkmcnt(4)
ds_read_b64_tr_b16 v[80:81],v2 offset:96
v_mfma_f32_16x16x32_bf16 a[192:195],v[96:99],a[128:131],a[192:195]
s_waitcnt vmcnt(14)
ds_read_b64_tr_b16 v[82:83],v2 offset:5216
ds_read_b64_tr_b16 v[86:87],v2 offset:15456
v_mfma_f32_16x16x32_bf16 a[192:195],v[100:103],a[144:147],a[192:195]
s_waitcnt vmcnt(13)
ds_read_b64_tr_b16 v[90:91],v2 offset:25696
v_add_u32 v2,0x1e000,v2
v_mfma_f32_16x16x32_bf16 a[192:195],v[104:107],a[160:163],a[192:195]
s_waitcnt vmcnt(12)
v_mfma_f32_16x16x32_bf16 a[192:195],v[108:111],a[176:179],a[192:195]
s_waitcnt vmcnt(11)
v_mfma_f32_16x16x32_bf16 a[196:199],v[108:111],a[180:183],a[196:199]
s_waitcnt vmcnt(10)
v_mfma_f32_16x16x32_bf16 a[196:199],v[104:107],a[164:167],a[196:199]
s_waitcnt vmcnt(9)
v_mfma_f32_16x16x32_bf16 a[196:199],v[100:103],a[148:151],a[196:199]
s_waitcnt vmcnt(8)
v_mfma_f32_16x16x32_bf16 a[196:199],v[96:99],a[132:135],a[196:199]
s_waitcnt vmcnt(7)
v_mfma_f32_16x16x32_bf16 a[200:203],v[96:99],a[136:139],a[200:203]
s_waitcnt vmcnt(6)
v_mfma_f32_16x16x32_bf16 a[200:203],v[100:103],a[152:155],a[200:203]
s_waitcnt vmcnt(5)
v_mfma_f32_16x16x32_bf16 a[200:203],v[104:107],a[168:171],a[200:203]
s_waitcnt vmcnt(4)
v_mfma_f32_16x16x32_bf16 a[200:203],v[108:111],a[184:187],a[200:203]
s_waitcnt vmcnt(3)
v_accvgpr_read_b32 v16,a192
v_accvgpr_read_b32 v17,a193
v_mfma_f32_16x16x32_bf16 a[204:207],v[108:111],a[188:191],a[204:207]
s_waitcnt vmcnt(2)
v_accvgpr_read_b32 v18,a194
v_accvgpr_read_b32 v19,a195
v_mfma_f32_16x16x32_bf16 a[204:207],v[104:107],a[172:175],a[204:207]
s_waitcnt vmcnt(1)
v_cvt_pk_bf16_f32 v16,v16,v17
v_cvt_pk_bf16_f32 v17,v18,v19
v_mfma_f32_16x16x32_bf16 a[204:207],v[100:103],a[156:159],a[204:207]
s_waitcnt vmcnt(0)
v_accvgpr_read_b32 v8,a196
v_accvgpr_read_b32 v9,a197
v_mfma_f32_16x16x32_bf16 a[204:207],v[96:99],a[140:143],a[204:207]
v_accvgpr_read_b32 v10,a198
v_accvgpr_read_b32 v11,a199
v_mfma_f32_16x16x32_bf16 a[208:211],v[112:115],a[128:131],a[208:211]
v_cvt_pk_bf16_f32 v8,v8,v9
v_cvt_pk_bf16_f32 v9,v10,v11
v_mfma_f32_16x16x32_bf16 a[208:211],v[116:119],a[144:147],a[208:211]
ds_read_b64_tr_b16 v[96:97],v1 offset:64
ds_read_b64_tr_b16 v[98:99],v1 offset:5184
v_mfma_f32_16x16x32_bf16 a[208:211],v[120:123],a[160:163],a[208:211]
ds_read_b64_tr_b16 v[100:101],v1 offset:10304
v_mfma_f32_16x16x32_bf16 a[208:211],v[124:127],a[176:179],a[208:211]
ds_read_b64_tr_b16 v[102:103],v1 offset:15424
v_mfma_f32_16x16x32_bf16 a[212:215],v[124:127],a[180:183],a[212:215]
ds_read_b64_tr_b16 v[104:105],v1 offset:20544
v_mfma_f32_16x16x32_bf16 a[212:215],v[120:123],a[164:167],a[212:215]
ds_read_b64_tr_b16 v[106:107],v1 offset:25664
ds_read_b64_tr_b16 v[108:109],v1 offset:30784
v_mfma_f32_16x16x32_bf16 a[212:215],v[116:119],a[148:151],a[212:215]
ds_read_b64_tr_b16 v[110:111],v1 offset:35904
v_add_u32 v1,0xfffe2000,v1
v_mfma_f32_16x16x32_bf16 a[212:215],v[112:115],a[132:135],a[212:215]
v_accvgpr_read_b32 v20,a208
v_accvgpr_read_b32 v21,a209
v_mfma_f32_16x16x32_bf16 a[216:219],v[112:115],a[136:139],a[216:219]
v_accvgpr_read_b32 v22,a210
v_accvgpr_read_b32 v23,a211
v_mfma_f32_16x16x32_bf16 a[216:219],v[116:119],a[152:155],a[216:219]
v_cvt_pk_bf16_f32 v18,v20,v21
v_cvt_pk_bf16_f32 v19,v22,v23
v_mfma_f32_16x16x32_bf16 a[216:219],v[120:123],a[168:171],a[216:219]
buffer_store_dwordx4 v[16:19],v6,s[36:39],s74 offen sc1
v_mfma_f32_16x16x32_bf16 a[216:219],v[124:127],a[184:187],a[216:219]
v_accvgpr_read_b32 v12,a212
v_accvgpr_read_b32 v13,a213
v_mfma_f32_16x16x32_bf16 a[220:223],v[124:127],a[188:191],a[220:223]
v_accvgpr_read_b32 v14,a214
v_accvgpr_read_b32 v15,a215
v_mfma_f32_16x16x32_bf16 a[220:223],v[120:123],a[172:175],a[220:223]
v_cvt_pk_bf16_f32 v10,v12,v13
v_cvt_pk_bf16_f32 v11,v14,v15
v_mfma_f32_16x16x32_bf16 a[220:223],v[116:119],a[156:159],a[220:223]
v_accvgpr_read_b32 v16,a200
v_accvgpr_read_b32 v17,a201
v_mfma_f32_16x16x32_bf16 a[220:223],v[112:115],a[140:143],a[220:223]
v_accvgpr_read_b32 v18,a202
v_accvgpr_read_b32 v19,a203
v_mfma_f32_16x16x32_bf16 a[224:227],v[64:67],v[32:35],a[224:227]
v_accvgpr_read_b32 v20,a216
v_accvgpr_read_b32 v21,a217
v_mfma_f32_16x16x32_bf16 a[224:227],v[68:71],v[36:39],a[224:227]
v_accvgpr_read_b32 v22,a218
v_accvgpr_read_b32 v23,a219
v_mfma_f32_16x16x32_bf16 a[224:227],v[72:75],v[40:43],a[224:227]
v_cvt_pk_bf16_f32 v16,v16,v17
v_cvt_pk_bf16_f32 v17,v18,v19
v_mfma_f32_16x16x32_bf16 a[224:227],v[76:79],v[44:47],a[224:227]
v_cvt_pk_bf16_f32 v18,v20,v21
v_cvt_pk_bf16_f32 v19,v22,v23
v_mfma_f32_16x16x32_bf16 a[228:231],v[76:79],v[180:183],a[228:231]
ds_read_b64_tr_b16 v[112:113],v2 offset:96
ds_read_b64_tr_b16 v[114:115],v2 offset:5216
v_mfma_f32_16x16x32_bf16 a[228:231],v[72:75],v[164:167],a[228:231]
buffer_store_dwordx4 v[8:11],v6,s[36:39],s74 offen offset:1024 sc1
v_mfma_f32_16x16x32_bf16 a[228:231],v[68:71],v[148:151],a[228:231]
ds_read_b64_tr_b16 v[116:117],v2 offset:10336
ds_read_b64_tr_b16 v[118:119],v2 offset:15456
v_mfma_f32_16x16x32_bf16 a[228:231],v[64:67],v[132:135],a[228:231]
ds_read_b64_tr_b16 v[120:121],v2 offset:20576
v_accvgpr_read_b32 v8,a204
v_mfma_f32_16x16x32_bf16 a[232:235],v[64:67],v[196:199],a[232:235]
ds_read_b64_tr_b16 v[122:123],v2 offset:25696
v_accvgpr_read_b32 v9,a205
v_mfma_f32_16x16x32_bf16 a[232:235],v[68:71],v[212:215],a[232:235]
ds_read_b64_tr_b16 v[124:125],v2 offset:30816
v_accvgpr_read_b32 v10,a206
v_mfma_f32_16x16x32_bf16 a[232:235],v[72:75],v[228:231],a[232:235]
ds_read_b64_tr_b16 v[126:127],v2 offset:35936
v_accvgpr_read_b32 v11,a207
v_mfma_f32_16x16x32_bf16 a[232:235],v[76:79],v[244:247],a[232:235]
v_accvgpr_read_b32 v12,a220
v_accvgpr_read_b32 v13,a221
v_mfma_f32_16x16x32_bf16 a[236:239],v[76:79],v[188:191],a[236:239]
v_accvgpr_read_b32 v14,a222
v_accvgpr_read_b32 v15,a223
v_mfma_f32_16x16x32_bf16 a[236:239],v[72:75],v[172:175],a[236:239]
v_cvt_pk_bf16_f32 v8,v8,v9
v_cvt_pk_bf16_f32 v9,v10,v11
v_mfma_f32_16x16x32_bf16 a[236:239],v[68:71],v[156:159],a[236:239]
v_cvt_pk_bf16_f32 v10,v12,v13
v_cvt_pk_bf16_f32 v11,v14,v15
v_mfma_f32_16x16x32_bf16 a[236:239],v[64:67],v[140:143],a[236:239]
s_waitcnt lgkmcnt(3)
ds_read_b64_tr_b16 v[64:65],v1 offset:128
ds_read_b64_tr_b16 v[66:67],v1 offset:5248
v_mfma_f32_16x16x32_bf16 a[240:243],v[80:83],v[32:35],a[240:243]
buffer_store_dwordx4 v[16:19],v6,s[36:39],s74 offen offset:2048 sc1
v_mfma_f32_16x16x32_bf16 a[240:243],v[84:87],v[36:39],a[240:243]
ds_read_b64_tr_b16 v[68:69],v1 offset:10368
ds_read_b64_tr_b16 v[70:71],v1 offset:15488
v_mfma_f32_16x16x32_bf16 a[240:243],v[88:91],v[40:43],a[240:243]
ds_read_b64_tr_b16 v[72:73],v1 offset:20608
v_add_u32 v2,0xfffe2000,v2
v_mfma_f32_16x16x32_bf16 a[240:243],v[92:95],v[44:47],a[240:243]
ds_read_b64_tr_b16 v[74:75],v1 offset:25728
v_mfma_f32_16x16x32_bf16 a[244:247],v[92:95],v[180:183],a[244:247]
ds_read_b64_tr_b16 v[76:77],v1 offset:30848
v_mfma_f32_16x16x32_bf16 a[244:247],v[88:91],v[164:167],a[244:247]
ds_read_b64_tr_b16 v[78:79],v1 offset:35968
v_add_u32 v1,0x1e000,v1
v_mfma_f32_16x16x32_bf16 a[244:247],v[84:87],v[148:151],a[244:247]
v_mfma_f32_16x16x32_bf16 a[244:247],v[80:83],v[132:135],a[244:247]
v_mfma_f32_16x16x32_bf16 a[248:251],v[80:83],v[196:199],a[248:251]
v_mfma_f32_16x16x32_bf16 a[248:251],v[84:87],v[212:215],a[248:251]
v_mfma_f32_16x16x32_bf16 a[248:251],v[88:91],v[228:231],a[248:251]
v_mfma_f32_16x16x32_bf16 a[248:251],v[92:95],v[244:247],a[248:251]
buffer_store_dwordx4 v[8:11],v28,s[36:39],s74 offen offset:3072 sc1
v_mfma_f32_16x16x32_bf16 a[252:255],v[92:95],v[188:191],a[252:255]
ds_read_b64_tr_b16 v[92:93],v2 offset:30880
ds_read_b64_tr_b16 v[94:95],v2 offset:36000
v_mfma_f32_16x16x32_bf16 a[252:255],v[88:91],v[172:175],a[252:255]
ds_read_b64_tr_b16 v[88:89],v2 offset:20640
v_mfma_f32_16x16x32_bf16 a[252:255],v[84:87],v[156:159],a[252:255]
ds_read_b64_tr_b16 v[84:85],v2 offset:10400
v_mfma_f32_16x16x32_bf16 a[252:255],v[80:83],v[140:143],a[252:255]
ds_read_b64_tr_b16 v[80:81],v2 offset:160
v_mfma_f32_16x16x32_bf16 a[224:227],v[96:99],a[128:131],a[224:227]
ds_read_b64_tr_b16 v[82:83],v2 offset:5280
ds_read_b64_tr_b16 v[86:87],v2 offset:15520
v_mfma_f32_16x16x32_bf16 a[224:227],v[100:103],a[144:147],a[224:227]
ds_read_b64_tr_b16 v[90:91],v2 offset:25760
v_add_u32 v2,0x1e000,v2
v_mfma_f32_16x16x32_bf16 a[224:227],v[104:107],a[160:163],a[224:227]
v_mfma_f32_16x16x32_bf16 a[224:227],v[108:111],a[176:179],a[224:227]
v_mfma_f32_16x16x32_bf16 a[228:231],v[108:111],a[180:183],a[228:231]
v_mfma_f32_16x16x32_bf16 a[228:231],v[104:107],a[164:167],a[228:231]
v_mfma_f32_16x16x32_bf16 a[228:231],v[100:103],a[148:151],a[228:231]
v_mfma_f32_16x16x32_bf16 a[228:231],v[96:99],a[132:135],a[228:231]
v_mfma_f32_16x16x32_bf16 a[232:235],v[96:99],a[136:139],a[232:235]
v_mfma_f32_16x16x32_bf16 a[232:235],v[100:103],a[152:155],a[232:235]
v_mfma_f32_16x16x32_bf16 a[232:235],v[104:107],a[168:171],a[232:235]
v_mfma_f32_16x16x32_bf16 a[232:235],v[108:111],a[184:187],a[232:235]
v_accvgpr_read_b32 v16,a224
v_accvgpr_read_b32 v17,a225
v_mfma_f32_16x16x32_bf16 a[236:239],v[108:111],a[188:191],a[236:239]
v_accvgpr_read_b32 v18,a226
v_accvgpr_read_b32 v19,a227
v_mfma_f32_16x16x32_bf16 a[236:239],v[104:107],a[172:175],a[236:239]
v_cvt_pk_bf16_f32 v16,v16,v17
v_cvt_pk_bf16_f32 v17,v18,v19
v_mfma_f32_16x16x32_bf16 a[236:239],v[100:103],a[156:159],a[236:239]
v_accvgpr_read_b32 v8,a228
v_accvgpr_read_b32 v9,a229
v_mfma_f32_16x16x32_bf16 a[236:239],v[96:99],a[140:143],a[236:239]
v_accvgpr_read_b32 v10,a230
v_accvgpr_read_b32 v11,a231
v_mfma_f32_16x16x32_bf16 a[240:243],v[112:115],a[128:131],a[240:243]
v_cvt_pk_bf16_f32 v8,v8,v9
v_cvt_pk_bf16_f32 v9,v10,v11
v_mfma_f32_16x16x32_bf16 a[240:243],v[116:119],a[144:147],a[240:243]
s_waitcnt lgkmcnt(0)
ds_read_b64_tr_b16 v[96:97],v1 offset:128
ds_read_b64_tr_b16 v[98:99],v1 offset:5248
v_mfma_f32_16x16x32_bf16 a[240:243],v[120:123],a[160:163],a[240:243]
ds_read_b64_tr_b16 v[100:101],v1 offset:10368
v_mfma_f32_16x16x32_bf16 a[240:243],v[124:127],a[176:179],a[240:243]
ds_read_b64_tr_b16 v[102:103],v1 offset:15488
v_mfma_f32_16x16x32_bf16 a[244:247],v[124:127],a[180:183],a[244:247]
ds_read_b64_tr_b16 v[104:105],v1 offset:20608
v_mfma_f32_16x16x32_bf16 a[244:247],v[120:123],a[164:167],a[244:247]
ds_read_b64_tr_b16 v[106:107],v1 offset:25728
)A"
R"A(ds_read_b64_tr_b16 v[108:109],v1 offset:30848
v_mfma_f32_16x16x32_bf16 a[244:247],v[116:119],a[148:151],a[244:247]
ds_read_b64_tr_b16 v[110:111],v1 offset:35968
v_add_u32 v1,0xfffe2000,v1
v_mfma_f32_16x16x32_bf16 a[244:247],v[112:115],a[132:135],a[244:247]
v_accvgpr_read_b32 v20,a240
v_accvgpr_read_b32 v21,a241
v_mfma_f32_16x16x32_bf16 a[248:251],v[112:115],a[136:139],a[248:251]
v_accvgpr_read_b32 v22,a242
v_accvgpr_read_b32 v23,a243
v_mfma_f32_16x16x32_bf16 a[248:251],v[116:119],a[152:155],a[248:251]
v_cvt_pk_bf16_f32 v18,v20,v21
v_cvt_pk_bf16_f32 v19,v22,v23
v_mfma_f32_16x16x32_bf16 a[248:251],v[120:123],a[168:171],a[248:251]
buffer_store_dwordx4 v[16:19],v6,s[36:39],s75 offen sc1
v_mfma_f32_16x16x32_bf16 a[248:251],v[124:127],a[184:187],a[248:251]
v_accvgpr_read_b32 v12,a244
v_accvgpr_read_b32 v13,a245
v_mfma_f32_16x16x32_bf16 a[252:255],v[124:127],a[188:191],a[252:255]
v_accvgpr_read_b32 v14,a246
v_accvgpr_read_b32 v15,a247
v_mfma_f32_16x16x32_bf16 a[252:255],v[120:123],a[172:175],a[252:255]
v_cvt_pk_bf16_f32 v10,v12,v13
v_cvt_pk_bf16_f32 v11,v14,v15
v_mfma_f32_16x16x32_bf16 a[252:255],v[116:119],a[156:159],a[252:255]
v_accvgpr_read_b32 v16,a232
v_accvgpr_read_b32 v17,a233
v_mfma_f32_16x16x32_bf16 a[252:255],v[112:115],a[140:143],a[252:255]
v_accvgpr_read_b32 v18,a234
v_accvgpr_read_b32 v19,a235
v_mfma_f32_16x16x32_bf16 a[0:3],v[64:67],v[32:35],a[0:3]
v_accvgpr_read_b32 v20,a248
v_accvgpr_read_b32 v21,a249
v_mfma_f32_16x16x32_bf16 a[0:3],v[68:71],v[36:39],a[0:3]
v_accvgpr_read_b32 v22,a250
v_accvgpr_read_b32 v23,a251
v_mfma_f32_16x16x32_bf16 a[0:3],v[72:75],v[40:43],a[0:3]
v_cvt_pk_bf16_f32 v16,v16,v17
v_cvt_pk_bf16_f32 v17,v18,v19
v_mfma_f32_16x16x32_bf16 a[0:3],v[76:79],v[44:47],a[0:3]
v_cvt_pk_bf16_f32 v18,v20,v21
v_cvt_pk_bf16_f32 v19,v22,v23
v_mfma_f32_16x16x32_bf16 a[4:7],v[76:79],v[180:183],a[4:7]
ds_read_b64_tr_b16 v[112:113],v2 offset:160
ds_read_b64_tr_b16 v[114:115],v2 offset:5280
v_mfma_f32_16x16x32_bf16 a[4:7],v[72:75],v[164:167],a[4:7]
buffer_store_dwordx4 v[8:11],v6,s[36:39],s75 offen offset:1024 sc1
v_mfma_f32_16x16x32_bf16 a[4:7],v[68:71],v[148:151],a[4:7]
ds_read_b64_tr_b16 v[116:117],v2 offset:10400
ds_read_b64_tr_b16 v[118:119],v2 offset:15520
v_mfma_f32_16x16x32_bf16 a[4:7],v[64:67],v[132:135],a[4:7]
ds_read_b64_tr_b16 v[120:121],v2 offset:20640
v_accvgpr_read_b32 v8,a236
v_mfma_f32_16x16x32_bf16 a[8:11],v[64:67],v[196:199],a[8:11]
ds_read_b64_tr_b16 v[122:123],v2 offset:25760
v_accvgpr_read_b32 v9,a237
v_mfma_f32_16x16x32_bf16 a[8:11],v[68:71],v[212:215],a[8:11]
ds_read_b64_tr_b16 v[124:125],v2 offset:30880
v_accvgpr_read_b32 v10,a238
v_mfma_f32_16x16x32_bf16 a[8:11],v[72:75],v[228:231],a[8:11]
ds_read_b64_tr_b16 v[126:127],v2 offset:36000
v_accvgpr_read_b32 v11,a239
v_mfma_f32_16x16x32_bf16 a[8:11],v[76:79],v[244:247],a[8:11]
v_accvgpr_read_b32 v12,a252
v_accvgpr_read_b32 v13,a253
v_mfma_f32_16x16x32_bf16 a[12:15],v[76:79],v[188:191],a[12:15]
v_accvgpr_read_b32 v14,a254
v_accvgpr_read_b32 v15,a255
v_mfma_f32_16x16x32_bf16 a[12:15],v[72:75],v[172:175],a[12:15]
v_cvt_pk_bf16_f32 v8,v8,v9
v_cvt_pk_bf16_f32 v9,v10,v11
v_mfma_f32_16x16x32_bf16 a[12:15],v[68:71],v[156:159],a[12:15]
v_cvt_pk_bf16_f32 v10,v12,v13
v_cvt_pk_bf16_f32 v11,v14,v15
v_mfma_f32_16x16x32_bf16 a[12:15],v[64:67],v[140:143],a[12:15]
ds_read_b64_tr_b16 v[64:65],v1 offset:192
ds_read_b64_tr_b16 v[66:67],v1 offset:5312
v_mfma_f32_16x16x32_bf16 a[16:19],v[80:83],v[32:35],a[16:19]
buffer_store_dwordx4 v[16:19],v6,s[36:39],s75 offen offset:2048 sc1
v_mfma_f32_16x16x32_bf16 a[16:19],v[84:87],v[36:39],a[16:19]
ds_read_b64_tr_b16 v[68:69],v1 offset:10432
ds_read_b64_tr_b16 v[70:71],v1 offset:15552
v_mfma_f32_16x16x32_bf16 a[16:19],v[88:91],v[40:43],a[16:19]
ds_read_b64_tr_b16 v[72:73],v1 offset:20672
v_add_u32 v2,0xfffe2000,v2
v_mfma_f32_16x16x32_bf16 a[16:19],v[92:95],v[44:47],a[16:19]
ds_read_b64_tr_b16 v[74:75],v1 offset:25792
v_mfma_f32_16x16x32_bf16 a[20:23],v[92:95],v[180:183],a[20:23]
ds_read_b64_tr_b16 v[76:77],v1 offset:30912
v_mfma_f32_16x16x32_bf16 a[20:23],v[88:91],v[164:167],a[20:23]
ds_read_b64_tr_b16 v[78:79],v1 offset:36032
v_add_u32 v1,0x1e000,v1
v_mfma_f32_16x16x32_bf16 a[20:23],v[84:87],v[148:151],a[20:23]
v_mfma_f32_16x16x32_bf16 a[20:23],v[80:83],v[132:135],a[20:23]
v_mfma_f32_16x16x32_bf16 a[24:27],v[80:83],v[196:199],a[24:27]
v_mfma_f32_16x16x32_bf16 a[24:27],v[84:87],v[212:215],a[24:27]
v_mfma_f32_16x16x32_bf16 a[24:27],v[88:91],v[228:231],a[24:27]
v_mfma_f32_16x16x32_bf16 a[24:27],v[92:95],v[244:247],a[24:27]
buffer_store_dwordx4 v[8:11],v28,s[36:39],s75 offen offset:3072 sc1
v_mfma_f32_16x16x32_bf16 a[28:31],v[92:95],v[188:191],a[28:31]
ds_read_b64_tr_b16 v[92:93],v2 offset:30944
ds_read_b64_tr_b16 v[94:95],v2 offset:36064
v_mfma_f32_16x16x32_bf16 a[28:31],v[88:91],v[172:175],a[28:31]
ds_read_b64_tr_b16 v[88:89],v2 offset:20704
v_mfma_f32_16x16x32_bf16 a[28:31],v[84:87],v[156:159],a[28:31]
ds_read_b64_tr_b16 v[84:85],v2 offset:10464
v_mfma_f32_16x16x32_bf16 a[28:31],v[80:83],v[140:143],a[28:31]
s_waitcnt lgkmcnt(4)
ds_read_b64_tr_b16 v[80:81],v2 offset:224
v_mfma_f32_16x16x32_bf16 a[0:3],v[96:99],a[128:131],a[0:3]
ds_read_b64_tr_b16 v[82:83],v2 offset:5344
ds_read_b64_tr_b16 v[86:87],v2 offset:15584
v_mfma_f32_16x16x32_bf16 a[0:3],v[100:103],a[144:147],a[0:3]
ds_read_b64_tr_b16 v[90:91],v2 offset:25824
v_add_u32 v2,0x1e000,v2
v_mfma_f32_16x16x32_bf16 a[0:3],v[104:107],a[160:163],a[0:3]
v_mfma_f32_16x16x32_bf16 a[0:3],v[108:111],a[176:179],a[0:3]
v_mfma_f32_16x16x32_bf16 a[4:7],v[108:111],a[180:183],a[4:7]
v_mfma_f32_16x16x32_bf16 a[4:7],v[104:107],a[164:167],a[4:7]
v_mfma_f32_16x16x32_bf16 a[4:7],v[100:103],a[148:151],a[4:7]
v_mfma_f32_16x16x32_bf16 a[4:7],v[96:99],a[132:135],a[4:7]
v_mfma_f32_16x16x32_bf16 a[8:11],v[96:99],a[136:139],a[8:11]
v_mfma_f32_16x16x32_bf16 a[8:11],v[100:103],a[152:155],a[8:11]
v_mfma_f32_16x16x32_bf16 a[8:11],v[104:107],a[168:171],a[8:11]
v_mfma_f32_16x16x32_bf16 a[8:11],v[108:111],a[184:187],a[8:11]
v_accvgpr_read_b32 v16,a0
v_accvgpr_read_b32 v17,a1
v_mfma_f32_16x16x32_bf16 a[12:15],v[108:111],a[188:191],a[12:15]
v_accvgpr_read_b32 v18,a2
v_accvgpr_read_b32 v19,a3
v_mfma_f32_16x16x32_bf16 a[12:15],v[104:107],a[172:175],a[12:15]
v_cvt_pk_bf16_f32 v16,v16,v17
v_cvt_pk_bf16_f32 v17,v18,v19
v_mfma_f32_16x16x32_bf16 a[12:15],v[100:103],a[156:159],a[12:15]
v_accvgpr_read_b32 v8,a4
v_accvgpr_read_b32 v9,a5
v_mfma_f32_16x16x32_bf16 a[12:15],v[96:99],a[140:143],a[12:15]
v_accvgpr_read_b32 v10,a6
v_accvgpr_read_b32 v11,a7
v_mfma_f32_16x16x32_bf16 a[16:19],v[112:115],a[128:131],a[16:19]
v_cvt_pk_bf16_f32 v8,v8,v9
v_cvt_pk_bf16_f32 v9,v10,v11
v_mfma_f32_16x16x32_bf16 a[16:19],v[116:119],a[144:147],a[16:19]
ds_read_b64_tr_b16 v[96:97],v1 offset:192
ds_read_b64_tr_b16 v[98:99],v1 offset:5312
v_mfma_f32_16x16x32_bf16 a[16:19],v[120:123],a[160:163],a[16:19]
ds_read_b64_tr_b16 v[100:101],v1 offset:10432
v_mfma_f32_16x16x32_bf16 a[16:19],v[124:127],a[176:179],a[16:19]
ds_read_b64_tr_b16 v[102:103],v1 offset:15552
v_mfma_f32_16x16x32_bf16 a[20:23],v[124:127],a[180:183],a[20:23]
ds_read_b64_tr_b16 v[104:105],v1 offset:20672
v_mfma_f32_16x16x32_bf16 a[20:23],v[120:123],a[164:167],a[20:23]
ds_read_b64_tr_b16 v[106:107],v1 offset:25792
ds_read_b64_tr_b16 v[108:109],v1 offset:30912
v_mfma_f32_16x16x32_bf16 a[20:23],v[116:119],a[148:151],a[20:23]
ds_read_b64_tr_b16 v[110:111],v1 offset:36032
v_add_u32 v1,0xfffec000,v1
v_mfma_f32_16x16x32_bf16 a[20:23],v[112:115],a[132:135],a[20:23]
v_accvgpr_read_b32 v20,a16
v_accvgpr_read_b32 v21,a17
v_mfma_f32_16x16x32_bf16 a[24:27],v[112:115],a[136:139],a[24:27]
v_accvgpr_read_b32 v22,a18
v_accvgpr_read_b32 v23,a19
v_mfma_f32_16x16x32_bf16 a[24:27],v[116:119],a[152:155],a[24:27]
v_cvt_pk_bf16_f32 v18,v20,v21
v_cvt_pk_bf16_f32 v19,v22,v23
v_mfma_f32_16x16x32_bf16 a[24:27],v[120:123],a[168:171],a[24:27]
buffer_store_dwordx4 v[16:19],v6,s[36:39],s76 offen sc1
v_mfma_f32_16x16x32_bf16 a[24:27],v[124:127],a[184:187],a[24:27]
v_accvgpr_read_b32 v12,a20
v_accvgpr_read_b32 v13,a21
v_mfma_f32_16x16x32_bf16 a[28:31],v[124:127],a[188:191],a[28:31]
v_accvgpr_read_b32 v14,a22
v_accvgpr_read_b32 v15,a23
v_mfma_f32_16x16x32_bf16 a[28:31],v[120:123],a[172:175],a[28:31]
v_cvt_pk_bf16_f32 v10,v12,v13
v_cvt_pk_bf16_f32 v11,v14,v15
v_mfma_f32_16x16x32_bf16 a[28:31],v[116:119],a[156:159],a[28:31]
v_accvgpr_read_b32 v16,a8
v_accvgpr_read_b32 v17,a9
v_mfma_f32_16x16x32_bf16 a[28:31],v[112:115],a[140:143],a[28:31]
v_accvgpr_read_b32 v18,a10
v_accvgpr_read_b32 v19,a11
s_nop 0
L_pd18_%=:
s_cbranch_execz L_trtg2F_%=
v_mfma_f32_16x16x32_bf16 v[128:131],v[64:67],v[32:35],v[128:131]
v_accvgpr_read_b32 v20,a24
v_accvgpr_read_b32 v21,a25
v_mfma_f32_16x16x32_bf16 v[128:131],v[68:71],v[36:39],v[128:131]
v_accvgpr_read_b32 v22,a26
v_accvgpr_read_b32 v23,a27
v_mfma_f32_16x16x32_bf16 v[128:131],v[72:75],v[40:43],v[128:131]
v_cvt_pk_bf16_f32 v16,v16,v17
v_cvt_pk_bf16_f32 v17,v18,v19
v_mfma_f32_16x16x32_bf16 v[128:131],v[76:79],v[44:47],v[128:131]
v_cvt_pk_bf16_f32 v18,v20,v21
v_cvt_pk_bf16_f32 v19,v22,v23
v_mfma_f32_16x16x32_bf16 v[136:139],v[76:79],v[180:183],v[136:139]
ds_read_b64_tr_b16 v[112:113],v2 offset:224
ds_read_b64_tr_b16 v[114:115],v2 offset:5344
v_mfma_f32_16x16x32_bf16 v[136:139],v[72:75],v[164:167],v[136:139]
buffer_store_dwordx4 v[8:11],v6,s[36:39],s76 offen offset:1024 sc1
v_mfma_f32_16x16x32_bf16 v[136:139],v[68:71],v[148:151],v[136:139]
ds_read_b64_tr_b16 v[116:117],v2 offset:10464
ds_read_b64_tr_b16 v[118:119],v2 offset:15584
v_mfma_f32_16x16x32_bf16 v[136:139],v[64:67],v[132:135],v[136:139]
ds_read_b64_tr_b16 v[120:121],v2 offset:20704
v_accvgpr_read_b32 v8,a12
v_mfma_f32_16x16x32_bf16 v[144:147],v[64:67],v[196:199],v[144:147]
ds_read_b64_tr_b16 v[122:123],v2 offset:25824
v_accvgpr_read_b32 v9,a13
v_mfma_f32_16x16x32_bf16 v[144:147],v[68:71],v[212:215],v[144:147]
ds_read_b64_tr_b16 v[124:125],v2 offset:30944
v_accvgpr_read_b32 v10,a14
v_mfma_f32_16x16x32_bf16 v[144:147],v[72:75],v[228:231],v[144:147]
ds_read_b64_tr_b16 v[126:127],v2 offset:36064
v_accvgpr_read_b32 v11,a15
v_mfma_f32_16x16x32_bf16 v[144:147],v[76:79],v[244:247],v[144:147]
v_accvgpr_read_b32 v12,a28
v_accvgpr_read_b32 v13,a29
v_mfma_f32_16x16x32_bf16 v[152:155],v[76:79],v[188:191],v[152:155]
v_accvgpr_read_b32 v14,a30
v_accvgpr_read_b32 v15,a31
v_mfma_f32_16x16x32_bf16 v[152:155],v[72:75],v[172:175],v[152:155]
v_cvt_pk_bf16_f32 v8,v8,v9
v_cvt_pk_bf16_f32 v9,v10,v11
v_mfma_f32_16x16x32_bf16 v[152:155],v[68:71],v[156:159],v[152:155]
v_cvt_pk_bf16_f32 v10,v12,v13
v_cvt_pk_bf16_f32 v11,v14,v15
v_mfma_f32_16x16x32_bf16 v[152:155],v[64:67],v[140:143],v[152:155]
s_waitcnt lgkmcnt(3)
ds_read_b64_tr_b16 v[64:65],v1 offset:256
ds_read_b64_tr_b16 v[66:67],v1 offset:5376
v_mfma_f32_16x16x32_bf16 v[160:163],v[80:83],v[32:35],v[160:163]
buffer_store_dwordx4 v[16:19],v6,s[36:39],s76 offen offset:2048 sc1
v_mfma_f32_16x16x32_bf16 v[160:163],v[84:87],v[36:39],v[160:163]
ds_read_b64_tr_b16 v[68:69],v1 offset:10496
ds_read_b64_tr_b16 v[70:71],v1 offset:15616
v_mfma_f32_16x16x32_bf16 v[160:163],v[88:91],v[40:43],v[160:163]
ds_read_b64_tr_b16 v[72:73],v1 offset:20736
v_add_u32 v2,0xfffec000,v2
v_mfma_f32_16x16x32_bf16 v[160:163],v[92:95],v[44:47],v[160:163]
ds_read_b64_tr_b16 v[74:75],v1 offset:25856
v_mov_b32 v20,s52
v_mfma_f32_16x16x32_bf16 v[168:171],v[92:95],v[180:183],v[168:171]
ds_read_b64_tr_b16 v[76:77],v1 offset:30976
v_mov_b32 v21,s53
v_mfma_f32_16x16x32_bf16 v[168:171],v[88:91],v[164:167],v[168:171]
ds_read_b64_tr_b16 v[78:79],v1 offset:36096
v_add_u32 v1,0xffff6000,v1
v_mfma_f32_16x16x32_bf16 v[168:171],v[84:87],v[148:151],v[168:171]
v_lshlrev_b32 v22,20,v6
v_mfma_f32_16x16x32_bf16 v[168:171],v[80:83],v[132:135],v[168:171]
v_mfma_f32_16x16x32_bf16 v[176:179],v[80:83],v[196:199],v[176:179]
v_mfma_f32_16x16x32_bf16 v[176:179],v[84:87],v[212:215],v[176:179]
v_mfma_f32_16x16x32_bf16 v[176:179],v[88:91],v[228:231],v[176:179]
v_mfma_f32_16x16x32_bf16 v[176:179],v[92:95],v[244:247],v[176:179]
buffer_store_dwordx4 v[8:11],v28,s[36:39],s76 offen offset:3072 sc1
v_mfma_f32_16x16x32_bf16 v[184:187],v[92:95],v[188:191],v[184:187]
)A"
R"A(ds_read_b64_tr_b16 v[92:93],v2 offset:31008
ds_read_b64_tr_b16 v[94:95],v2 offset:36128
v_mfma_f32_16x16x32_bf16 v[184:187],v[88:91],v[172:175],v[184:187]
ds_read_b64_tr_b16 v[88:89],v2 offset:20768
v_mfma_f32_16x16x32_bf16 v[184:187],v[84:87],v[156:159],v[184:187]
ds_read_b64_tr_b16 v[84:85],v2 offset:10528
v_mfma_f32_16x16x32_bf16 v[184:187],v[80:83],v[140:143],v[184:187]
ds_read_b64_tr_b16 v[80:81],v2 offset:288
v_mfma_f32_16x16x32_bf16 v[128:131],v[96:99],a[128:131],v[128:131]
ds_read_b64_tr_b16 v[82:83],v2 offset:5408
ds_read_b64_tr_b16 v[86:87],v2 offset:15648
v_mfma_f32_16x16x32_bf16 v[128:131],v[100:103],a[144:147],v[128:131]
ds_read_b64_tr_b16 v[90:91],v2 offset:25888
v_add_u32 v2,0xffff6000,v2
v_mfma_f32_16x16x32_bf16 v[128:131],v[104:107],a[160:163],v[128:131]
v_mfma_f32_16x16x32_bf16 v[128:131],v[108:111],a[176:179],v[128:131]
v_mfma_f32_16x16x32_bf16 v[136:139],v[108:111],a[180:183],v[136:139]
v_mfma_f32_16x16x32_bf16 v[136:139],v[104:107],a[164:167],v[136:139]
v_mfma_f32_16x16x32_bf16 v[136:139],v[100:103],a[148:151],v[136:139]
v_mfma_f32_16x16x32_bf16 v[136:139],v[96:99],a[132:135],v[136:139]
v_mfma_f32_16x16x32_bf16 v[144:147],v[96:99],a[136:139],v[144:147]
v_mfma_f32_16x16x32_bf16 v[144:147],v[100:103],a[152:155],v[144:147]
v_mfma_f32_16x16x32_bf16 v[144:147],v[104:107],a[168:171],v[144:147]
v_mfma_f32_16x16x32_bf16 v[144:147],v[108:111],a[184:187],v[144:147]
v_cvt_pk_bf16_f32 v128,v128,v129
v_cvt_pk_bf16_f32 v129,v130,v131
v_mfma_f32_16x16x32_bf16 v[152:155],v[108:111],a[188:191],v[152:155]
ds_read_b64_tr_b16 v[108:109],v1 offset:30976
ds_read_b64_tr_b16 v[110:111],v1 offset:36096
v_mfma_f32_16x16x32_bf16 v[152:155],v[104:107],a[172:175],v[152:155]
v_cvt_pk_bf16_f32 v136,v136,v137
v_cvt_pk_bf16_f32 v137,v138,v139
v_mfma_f32_16x16x32_bf16 v[152:155],v[100:103],a[156:159],v[152:155]
v_cvt_pk_bf16_f32 v144,v144,v145
v_cvt_pk_bf16_f32 v145,v146,v147
v_mfma_f32_16x16x32_bf16 v[152:155],v[96:99],a[140:143],v[152:155]
ds_read_b64_tr_b16 v[96:97],v1 offset:256
ds_read_b64_tr_b16 v[98:99],v1 offset:5376
v_mfma_f32_16x16x32_bf16 v[160:163],v[112:115],a[128:131],v[160:163]
ds_read_b64_tr_b16 v[100:101],v1 offset:10496
v_mfma_f32_16x16x32_bf16 v[160:163],v[116:119],a[144:147],v[160:163]
s_waitcnt lgkmcnt(5)
ds_read_b64_tr_b16 v[102:103],v1 offset:15616
v_mfma_f32_16x16x32_bf16 v[160:163],v[120:123],a[160:163],v[160:163]
ds_read_b64_tr_b16 v[104:105],v1 offset:20736
v_mfma_f32_16x16x32_bf16 v[160:163],v[124:127],a[176:179],v[160:163]
v_cvt_pk_bf16_f32 v152,v152,v153
v_cvt_pk_bf16_f32 v153,v154,v155
v_mfma_f32_16x16x32_bf16 v[168:171],v[124:127],a[180:183],v[168:171]
ds_read_b64_tr_b16 v[106:107],v1 offset:25856
v_add_u32 v1,0x1e000,v1
v_mfma_f32_16x16x32_bf16 v[168:171],v[120:123],a[164:167],v[168:171]
v_mfma_f32_16x16x32_bf16 v[168:171],v[116:119],a[148:151],v[168:171]
v_mfma_f32_16x16x32_bf16 v[168:171],v[112:115],a[132:135],v[168:171]
v_cvt_pk_bf16_f32 v130,v160,v161
v_cvt_pk_bf16_f32 v131,v162,v163
v_mfma_f32_16x16x32_bf16 v[176:179],v[112:115],a[136:139],v[176:179]
buffer_store_dwordx4 v[128:131],v6,s[36:39],s79 offen sc1
v_mfma_f32_16x16x32_bf16 v[176:179],v[116:119],a[152:155],v[176:179]
v_mfma_f32_16x16x32_bf16 v[176:179],v[120:123],a[168:171],v[176:179]
v_mfma_f32_16x16x32_bf16 v[176:179],v[124:127],a[184:187],v[176:179]
v_mfma_f32_16x16x32_bf16 v[184:187],v[124:127],a[188:191],v[184:187]
v_cvt_pk_bf16_f32 v138,v168,v169
v_cvt_pk_bf16_f32 v139,v170,v171
v_mfma_f32_16x16x32_bf16 v[184:187],v[120:123],a[172:175],v[184:187]
buffer_store_dwordx4 v[136:139],v6,s[36:39],s80 offen sc1
v_mfma_f32_16x16x32_bf16 v[184:187],v[116:119],a[156:159],v[184:187]
ds_read_b64_tr_b16 v[116:117],v2 offset:10528
ds_read_b64_tr_b16 v[118:119],v2 offset:15648
v_mfma_f32_16x16x32_bf16 v[184:187],v[112:115],a[140:143],v[184:187]
v_cvt_pk_bf16_f32 v146,v176,v177
v_cvt_pk_bf16_f32 v147,v178,v179
s_waitcnt vmcnt(6)
s_barrier
v_mfma_f32_16x16x32_bf16 v[192:195],v[64:67],a[68:71],v[192:195]
buffer_store_dwordx4 v[144:147],v6,s[36:39],s81 offen sc1
v_mfma_f32_16x16x32_bf16 v[192:195],v[68:71],a[76:79],v[192:195]
ds_read_b64_tr_b16 v[112:113],v2 offset:288
ds_read_b64_tr_b16 v[114:115],v2 offset:5408
v_mfma_f32_16x16x32_bf16 v[192:195],v[72:75],a[84:87],v[192:195]
buffer_store_dwordx2 v[58:59],v60,s[44:47],s18 offen offset:2312 sc1
v_mfma_f32_16x16x32_bf16 v[192:195],v[76:79],a[92:95],v[192:195]
v_cvt_pk_bf16_f32 v154,v184,v185
v_cvt_pk_bf16_f32 v155,v186,v187
v_mfma_f32_16x16x32_bf16 v[200:203],v[76:79],a[124:127],v[200:203]
buffer_store_dwordx4 v[152:155],v28,s[36:39],s82 offen sc1
v_mfma_f32_16x16x32_bf16 v[200:203],v[72:75],a[116:119],v[200:203]
ds_read_b64_tr_b16 v[120:121],v2 offset:20768
ds_read_b64_tr_b16 v[122:123],v2 offset:25888
v_mfma_f32_16x16x32_bf16 v[200:203],v[68:71],a[108:111],v[200:203]
ds_read_b64_tr_b16 v[124:125],v2 offset:31008
v_mfma_f32_16x16x32_bf16 v[200:203],v[64:67],a[100:103],v[200:203]
ds_read_b64_tr_b16 v[126:127],v2 offset:36128
v_add_u32 v2,0x1e000,v2
v_mfma_f32_16x16x32_bf16 v[208:211],v[64:67],a[64:67],v[208:211]
v_mfma_f32_16x16x32_bf16 v[208:211],v[68:71],a[72:75],v[208:211]
v_mfma_f32_16x16x32_bf16 v[208:211],v[72:75],a[80:83],v[208:211]
v_mfma_f32_16x16x32_bf16 v[208:211],v[76:79],a[88:91],v[208:211]
v_mfma_f32_16x16x32_bf16 v[216:219],v[76:79],a[120:123],v[216:219]
ds_read_b64_tr_b16 v[76:77],v1 offset:30976
ds_read_b64_tr_b16 v[78:79],v1 offset:36096
v_mfma_f32_16x16x32_bf16 v[216:219],v[72:75],a[112:115],v[216:219]
ds_read_b64_tr_b16 v[72:73],v1 offset:20736
v_mfma_f32_16x16x32_bf16 v[216:219],v[68:71],a[104:107],v[216:219]
ds_read_b64_tr_b16 v[68:69],v1 offset:10496
v_mfma_f32_16x16x32_bf16 v[216:219],v[64:67],a[96:99],v[216:219]
ds_read_b64_tr_b16 v[64:65],v1 offset:256
v_mfma_f32_16x16x32_bf16 v[224:227],v[80:83],a[68:71],v[224:227]
ds_read_b64_tr_b16 v[66:67],v1 offset:5376
ds_read_b64_tr_b16 v[70:71],v1 offset:15616
v_mfma_f32_16x16x32_bf16 v[224:227],v[84:87],a[76:79],v[224:227]
ds_read_b64_tr_b16 v[74:75],v1 offset:25856
v_mfma_f32_16x16x32_bf16 v[224:227],v[88:91],a[84:87],v[224:227]
v_mfma_f32_16x16x32_bf16 v[224:227],v[92:95],a[92:95],v[224:227]
v_mfma_f32_16x16x32_bf16 v[232:235],v[92:95],a[124:127],v[232:235]
v_mfma_f32_16x16x32_bf16 v[232:235],v[88:91],a[116:119],v[232:235]
v_mfma_f32_16x16x32_bf16 v[232:235],v[84:87],a[108:111],v[232:235]
v_mfma_f32_16x16x32_bf16 v[232:235],v[80:83],a[100:103],v[232:235]
v_mfma_f32_16x16x32_bf16 v[240:243],v[80:83],a[64:67],v[240:243]
v_mfma_f32_16x16x32_bf16 v[240:243],v[84:87],a[72:75],v[240:243]
v_mfma_f32_16x16x32_bf16 v[240:243],v[88:91],a[80:83],v[240:243]
v_mfma_f32_16x16x32_bf16 v[240:243],v[92:95],a[88:91],v[240:243]
v_mfma_f32_16x16x32_bf16 v[248:251],v[92:95],a[120:123],v[248:251]
ds_read_b64_tr_b16 v[92:93],v2 offset:31008
ds_read_b64_tr_b16 v[94:95],v2 offset:36128
v_mfma_f32_16x16x32_bf16 v[248:251],v[88:91],a[112:115],v[248:251]
ds_read_b64_tr_b16 v[88:89],v2 offset:20768
v_mfma_f32_16x16x32_bf16 v[248:251],v[84:87],a[104:107],v[248:251]
ds_read_b64_tr_b16 v[84:85],v2 offset:10528
v_mfma_f32_16x16x32_bf16 v[248:251],v[80:83],a[96:99],v[248:251]
s_waitcnt lgkmcnt(4)
ds_read_b64_tr_b16 v[80:81],v2 offset:288
v_mfma_f32_16x16x32_bf16 v[192:195],v[96:99],v[32:35],v[192:195]
ds_read_b64_tr_b16 v[82:83],v2 offset:5408
ds_read_b64_tr_b16 v[86:87],v2 offset:15648
v_mfma_f32_16x16x32_bf16 v[192:195],v[100:103],v[36:39],v[192:195]
ds_read_b64_tr_b16 v[90:91],v2 offset:25888
v_mfma_f32_16x16x32_bf16 v[192:195],v[104:107],v[40:43],v[192:195]
v_mfma_f32_16x16x32_bf16 v[192:195],v[108:111],v[44:47],v[192:195]
v_mfma_f32_16x16x32_bf16 v[200:203],v[108:111],v[180:183],v[200:203]
v_mfma_f32_16x16x32_bf16 v[200:203],v[104:107],v[164:167],v[200:203]
v_mfma_f32_16x16x32_bf16 v[200:203],v[100:103],v[148:151],v[200:203]
v_mfma_f32_16x16x32_bf16 v[200:203],v[96:99],v[132:135],v[200:203]
v_mfma_f32_16x16x32_bf16 v[208:211],v[96:99],v[196:199],v[208:211]
v_mfma_f32_16x16x32_bf16 v[208:211],v[100:103],v[212:215],v[208:211]
v_mfma_f32_16x16x32_bf16 v[208:211],v[104:107],v[228:231],v[208:211]
v_mfma_f32_16x16x32_bf16 v[208:211],v[108:111],v[244:247],v[208:211]
v_mfma_f32_16x16x32_bf16 v[216:219],v[108:111],v[188:191],v[216:219]
v_mfma_f32_16x16x32_bf16 v[216:219],v[104:107],v[172:175],v[216:219]
v_mfma_f32_16x16x32_bf16 v[216:219],v[100:103],v[156:159],v[216:219]
v_mfma_f32_16x16x32_bf16 v[216:219],v[96:99],v[140:143],v[216:219]
v_mfma_f32_16x16x32_bf16 v[224:227],v[112:115],v[32:35],v[224:227]
v_mfma_f32_16x16x32_bf16 v[224:227],v[116:119],v[36:39],v[224:227]
v_mfma_f32_16x16x32_bf16 v[224:227],v[120:123],v[40:43],v[224:227]
v_mfma_f32_16x16x32_bf16 v[224:227],v[124:127],v[44:47],v[224:227]
v_mfma_f32_16x16x32_bf16 v[232:235],v[124:127],v[180:183],v[232:235]
v_mfma_f32_16x16x32_bf16 v[232:235],v[120:123],v[164:167],v[232:235]
v_mfma_f32_16x16x32_bf16 v[232:235],v[116:119],v[148:151],v[232:235]
v_mfma_f32_16x16x32_bf16 v[232:235],v[112:115],v[132:135],v[232:235]
v_mfma_f32_16x16x32_bf16 v[240:243],v[112:115],v[196:199],v[240:243]
v_mfma_f32_16x16x32_bf16 v[240:243],v[116:119],v[212:215],v[240:243]
v_mfma_f32_16x16x32_bf16 v[240:243],v[120:123],v[228:231],v[240:243]
v_mfma_f32_16x16x32_bf16 v[240:243],v[124:127],v[244:247],v[240:243]
v_mfma_f32_16x16x32_bf16 v[248:251],v[124:127],v[188:191],v[248:251]
v_mfma_f32_16x16x32_bf16 v[248:251],v[120:123],v[172:175],v[248:251]
v_mfma_f32_16x16x32_bf16 v[248:251],v[116:119],v[156:159],v[248:251]
v_mfma_f32_16x16x32_bf16 v[248:251],v[112:115],v[140:143],v[248:251]
v_mfma_f32_16x16x32_bf16 v[192:195],v[64:67],a[128:131],v[192:195]
v_mfma_f32_16x16x32_bf16 v[192:195],v[68:71],a[144:147],v[192:195]
v_mfma_f32_16x16x32_bf16 v[192:195],v[72:75],a[160:163],v[192:195]
v_mfma_f32_16x16x32_bf16 v[192:195],v[76:79],a[176:179],v[192:195]
v_mfma_f32_16x16x32_bf16 v[200:203],v[76:79],a[180:183],v[200:203]
v_mfma_f32_16x16x32_bf16 v[200:203],v[72:75],a[164:167],v[200:203]
v_mfma_f32_16x16x32_bf16 v[200:203],v[68:71],a[148:151],v[200:203]
v_mfma_f32_16x16x32_bf16 v[200:203],v[64:67],a[132:135],v[200:203]
v_mfma_f32_16x16x32_bf16 v[208:211],v[64:67],a[136:139],v[208:211]
v_mfma_f32_16x16x32_bf16 v[208:211],v[68:71],a[152:155],v[208:211]
v_mfma_f32_16x16x32_bf16 v[208:211],v[72:75],a[168:171],v[208:211]
v_mfma_f32_16x16x32_bf16 v[208:211],v[76:79],a[184:187],v[208:211]
v_mfma_f32_16x16x32_bf16 v[216:219],v[76:79],a[188:191],v[216:219]
v_mfma_f32_16x16x32_bf16 v[216:219],v[72:75],a[172:175],v[216:219]
v_mfma_f32_16x16x32_bf16 v[216:219],v[68:71],a[156:159],v[216:219]
v_mfma_f32_16x16x32_bf16 v[216:219],v[64:67],a[140:143],v[216:219]
s_waitcnt lgkmcnt(0)
v_mfma_f32_16x16x32_bf16 v[224:227],v[80:83],a[128:131],v[224:227]
buffer_load_dwordx2 v[62:63],v61,s[44:47],s91 offen offset:2312 sc1
v_mfma_f32_16x16x32_bf16 v[224:227],v[84:87],a[144:147],v[224:227]
v_mfma_f32_16x16x32_bf16 v[224:227],v[88:91],a[160:163],v[224:227]
v_mfma_f32_16x16x32_bf16 v[224:227],v[92:95],a[176:179],v[224:227]
v_mfma_f32_16x16x32_bf16 v[232:235],v[92:95],a[180:183],v[232:235]
v_mfma_f32_16x16x32_bf16 v[232:235],v[88:91],a[164:167],v[232:235]
v_mfma_f32_16x16x32_bf16 v[232:235],v[84:87],a[148:151],v[232:235]
v_mfma_f32_16x16x32_bf16 v[232:235],v[80:83],a[132:135],v[232:235]
v_mfma_f32_16x16x32_bf16 v[240:243],v[80:83],a[136:139],v[240:243]
v_mfma_f32_16x16x32_bf16 v[240:243],v[84:87],a[152:155],v[240:243]
v_mfma_f32_16x16x32_bf16 v[240:243],v[88:91],a[168:171],v[240:243]
v_mfma_f32_16x16x32_bf16 v[240:243],v[92:95],a[184:187],v[240:243]
v_mfma_f32_16x16x32_bf16 v[248:251],v[92:95],a[188:191],v[248:251]
v_mfma_f32_16x16x32_bf16 v[248:251],v[88:91],a[172:175],v[248:251]
v_mfma_f32_16x16x32_bf16 v[248:251],v[84:87],a[156:159],v[248:251]
v_mfma_f32_16x16x32_bf16 v[248:251],v[80:83],a[140:143],v[248:251]
s_waitcnt vmcnt(1)
s_barrier
buffer_store_dwordx2 v[20:21],v22,s[44:47],s18 offen offset:2304 sc1
s_waitcnt vmcnt(1)
v_cmp_ne_u32 s[56:57],s52,v62
v_cmp_ne_u32 s[58:59],s53,v63
s_nop 0
s_or_b64 s[56:57],s[56:57],s[58:59]
s_cbranch_scc1 L_fr4_%=
L_fo4_%=:
buffer_load_dwordx4 v[64:67],v6,s[36:39],s66 offen sc1
buffer_load_dwordx4 v[68:71],v6,s[36:39],s67 offen sc1
)A"
R"A(buffer_load_dwordx4 v[72:75],v6,s[36:39],s66 offen offset:1024 sc1
buffer_load_dwordx4 v[76:79],v6,s[36:39],s67 offen offset:1024 sc1
buffer_load_dwordx4 v[80:83],v6,s[36:39],s66 offen offset:2048 sc1
buffer_load_dwordx4 v[84:87],v6,s[36:39],s67 offen offset:2048 sc1
buffer_load_dwordx4 v[88:91],v28,s[36:39],s66 offen offset:3072 sc1
buffer_load_dwordx4 v[92:95],v28,s[36:39],s67 offen offset:3072 sc1
buffer_load_dwordx2 v[4:5],v3,s[44:47],s91 offen offset:2304 sc1
s_waitcnt vmcnt(8)
v_mfma_f32_16x16x32_bf16 v[192:195],v[48:51],v[64:67],v[192:195]
s_waitcnt vmcnt(7)
v_mfma_f32_16x16x32_bf16 v[192:195],v[48:51],v[68:71],v[192:195]
v_mfma_f32_16x16x32_bf16 v[224:227],v[52:55],v[64:67],v[224:227]
v_mfma_f32_16x16x32_bf16 v[224:227],v[52:55],v[68:71],v[224:227]
s_waitcnt vmcnt(6)
v_mfma_f32_16x16x32_bf16 v[200:203],v[48:51],v[72:75],v[200:203]
s_waitcnt vmcnt(5)
v_mfma_f32_16x16x32_bf16 v[200:203],v[48:51],v[76:79],v[200:203]
v_mfma_f32_16x16x32_bf16 v[232:235],v[52:55],v[72:75],v[232:235]
v_mfma_f32_16x16x32_bf16 v[232:235],v[52:55],v[76:79],v[232:235]
s_waitcnt vmcnt(4)
v_mfma_f32_16x16x32_bf16 v[208:211],v[48:51],v[80:83],v[208:211]
s_waitcnt vmcnt(3)
v_mfma_f32_16x16x32_bf16 v[208:211],v[48:51],v[84:87],v[208:211]
v_mfma_f32_16x16x32_bf16 v[240:243],v[52:55],v[80:83],v[240:243]
v_mfma_f32_16x16x32_bf16 v[240:243],v[52:55],v[84:87],v[240:243]
s_waitcnt vmcnt(2)
v_mfma_f32_16x16x32_bf16 v[216:219],v[48:51],v[88:91],v[216:219]
s_waitcnt vmcnt(1)
v_mfma_f32_16x16x32_bf16 v[216:219],v[48:51],v[92:95],v[216:219]
v_mfma_f32_16x16x32_bf16 v[248:251],v[52:55],v[88:91],v[248:251]
v_mfma_f32_16x16x32_bf16 v[248:251],v[52:55],v[92:95],v[248:251]
s_waitcnt vmcnt(0)
v_cmp_ne_u32 s[56:57],s52,v4
v_cmp_ne_u32 s[58:59],s53,v5
s_nop 0
s_or_b64 s[56:57],s[56:57],s[58:59]
s_cbranch_scc1 L_fr5_%=
L_fo5_%=:
buffer_load_dwordx4 v[96:99],v6,s[36:39],s68 offen sc1
buffer_load_dwordx4 v[100:103],v6,s[36:39],s68 offen offset:1024 sc1
buffer_load_dwordx4 v[104:107],v6,s[36:39],s68 offen offset:2048 sc1
buffer_load_dwordx4 v[108:111],v6,s[36:39],s68 offen offset:3072 sc1
buffer_load_dwordx4 v[112:115],v6,s[36:39],s65 offen sc1
buffer_load_dwordx4 v[116:119],v6,s[36:39],s65 offen offset:1024 sc1
buffer_load_dwordx4 v[120:123],v6,s[36:39],s65 offen offset:2048 sc1
buffer_load_dwordx4 v[124:127],v28,s[36:39],s65 offen offset:3072 sc1
s_waitcnt vmcnt(7)
v_mfma_f32_16x16x32_bf16 v[128:131],v[48:51],v[96:99],0
s_waitcnt vmcnt(6)
v_mfma_f32_16x16x32_bf16 v[128:131],v[48:51],v[100:103],v[128:131]
s_waitcnt vmcnt(5)
v_mfma_f32_16x16x32_bf16 v[128:131],v[48:51],v[104:107],v[128:131]
s_waitcnt vmcnt(4)
v_mfma_f32_16x16x32_bf16 v[128:131],v[48:51],v[108:111],v[128:131]
v_mfma_f32_16x16x32_bf16 v[160:163],v[52:55],v[96:99],0
v_mfma_f32_16x16x32_bf16 v[160:163],v[52:55],v[100:103],v[160:163]
v_mfma_f32_16x16x32_bf16 v[160:163],v[52:55],v[104:107],v[160:163]
v_mfma_f32_16x16x32_bf16 v[160:163],v[52:55],v[108:111],v[160:163]
s_waitcnt vmcnt(3)
v_mfma_f32_16x16x32_bf16 v[192:195],v[48:51],v[112:115],v[192:195]
v_mfma_f32_16x16x32_bf16 v[224:227],v[52:55],v[112:115],v[224:227]
s_waitcnt vmcnt(2)
v_mfma_f32_16x16x32_bf16 v[200:203],v[48:51],v[116:119],v[200:203]
v_mfma_f32_16x16x32_bf16 v[232:235],v[52:55],v[116:119],v[232:235]
s_waitcnt vmcnt(1)
v_cvt_pk_bf16_f32 v128,v128,v129
v_cvt_pk_bf16_f32 v129,v130,v131
v_mfma_f32_16x16x32_bf16 v[208:211],v[48:51],v[120:123],v[208:211]
v_mfma_f32_16x16x32_bf16 v[240:243],v[52:55],v[120:123],v[240:243]
s_waitcnt vmcnt(0)
v_cvt_pk_bf16_f32 v130,v160,v161
v_cvt_pk_bf16_f32 v131,v162,v163
v_mfma_f32_16x16x32_bf16 v[216:219],v[48:51],v[124:127],v[216:219]
v_cvt_pk_bf16_f32 v192,v192,v193
v_cvt_pk_bf16_f32 v193,v194,v195
v_cvt_pk_bf16_f32 v194,v224,v225
v_cvt_pk_bf16_f32 v195,v226,v227
v_cvt_pk_bf16_f32 v200,v200,v201
v_cvt_pk_bf16_f32 v201,v202,v203
v_mfma_f32_16x16x32_bf16 v[248:251],v[52:55],v[124:127],v[248:251]
v_permlane16_swap_b32 v128,v130
v_permlane16_swap_b32 v129,v131
v_cvt_pk_bf16_f32 v202,v232,v233
v_cvt_pk_bf16_f32 v203,v234,v235
v_cvt_pk_bf16_f32 v208,v208,v209
v_cvt_pk_bf16_f32 v209,v210,v211
v_cvt_pk_bf16_f32 v210,v240,v241
v_cvt_pk_bf16_f32 v211,v242,v243
buffer_store_dwordx4 v[128:131],v56,s[24:27],s83 offen offset:256 sc1 nt
v_permlane16_swap_b32 v192,v194
v_permlane16_swap_b32 v193,v195
buffer_store_dwordx4 v[192:195],v56,s[24:27],s87 offen sc1 nt
v_permlane16_swap_b32 v200,v202
v_permlane16_swap_b32 v201,v203
v_cvt_pk_bf16_f32 v216,v216,v217
v_cvt_pk_bf16_f32 v217,v218,v219
v_cvt_pk_bf16_f32 v218,v248,v249
v_cvt_pk_bf16_f32 v219,v250,v251
buffer_store_dwordx4 v[200:203],v56,s[24:27],s88 offen sc1 nt
v_permlane16_swap_b32 v208,v210
v_permlane16_swap_b32 v209,v211
buffer_store_dwordx4 v[208:211],v56,s[24:27],s89 offen sc1 nt
v_permlane16_swap_b32 v216,v218
v_permlane16_swap_b32 v217,v219
buffer_store_dwordx4 v[216:219],v56,s[24:27],s90 offen sc1 nt
s_lshl_b32 s69,s62,20
s_add_u32 s69,s69,s18
buffer_store_dword v20,v22,s[44:47],s69 offen sc1
L_geF_%=:
s_branch L_end_%=
.p2align 12
L_pd19_%=:
s_cbranch_execz L_trt1_%=
L_GS_%=:
L_gbS_%=:
s_waitcnt lgkmcnt(14)
v_mfma_f32_16x16x32_bf16 a[192:195],v[64:67],a[68:71],0
buffer_store_dwordx4 v[32:35],v6,s[40:43],s79 offen sc1
s_waitcnt lgkmcnt(12)
v_mfma_f32_16x16x32_bf16 a[192:195],v[68:71],a[76:79],a[192:195]
s_lshl_b32 s91,s63,8
v_bfe_u32 v3,v6,4,2
v_min_u32 v3,2,v3
s_waitcnt lgkmcnt(10)
v_mfma_f32_16x16x32_bf16 a[192:195],v[72:75],a[84:87],a[192:195]
s_add_u32 s87,s64,1
v_add3_u32 v3,v3,s64,1
v_and_b32 v3,3,v3
s_waitcnt lgkmcnt(8)
v_mfma_f32_16x16x32_bf16 a[192:195],v[76:79],a[92:95],a[192:195]
s_and_b32 s87,s87,3
ds_read_b64_tr_b16 v[96:97],v1 offset:64
v_lshlrev_b32 v3,6,v3
v_mfma_f32_16x16x32_bf16 a[196:199],v[76:79],a[124:127],0
s_lshl_b32 s87,s87,1
ds_read_b64_tr_b16 v[98:99],v1 offset:5184
ds_read_b64_tr_b16 v[100:101],v1 offset:10304
v_mfma_f32_16x16x32_bf16 a[196:199],v[72:75],a[116:119],a[196:199]
s_lshl_b32 s87,s87,16
ds_read_b64_tr_b16 v[102:103],v1 offset:15424
v_lshlrev_b32 v29,10,v6
v_mfma_f32_16x16x32_bf16 a[196:199],v[68:71],a[108:111],a[196:199]
s_add_u32 s88,s87,4096
ds_read_b64_tr_b16 v[104:105],v1 offset:20544
v_accvgpr_read_b32 v16,a24
v_mfma_f32_16x16x32_bf16 a[196:199],v[64:67],a[100:103],a[196:199]
s_add_u32 s89,s87,8192
ds_read_b64_tr_b16 v[106:107],v1 offset:25664
v_accvgpr_read_b32 v17,a25
v_mfma_f32_16x16x32_bf16 a[200:203],v[64:67],a[64:67],0
s_add_u32 s90,s87,12288
ds_read_b64_tr_b16 v[108:109],v1 offset:30784
ds_read_b64_tr_b16 v[110:111],v1 offset:35904
v_mfma_f32_16x16x32_bf16 a[200:203],v[68:71],a[72:75],a[200:203]
s_add_u32 m0,s86,122880
ds_read_b64_tr_b16 v[112:113],v2 offset:96
v_accvgpr_read_b32 v18,a26
v_mfma_f32_16x16x32_bf16 a[200:203],v[72:75],a[80:83],a[200:203]
s_add_u32 s83,s64,3
ds_read_b64_tr_b16 v[114:115],v2 offset:5216
v_accvgpr_read_b32 v19,a27
v_mfma_f32_16x16x32_bf16 a[200:203],v[76:79],a[88:91],a[200:203]
s_and_b32 s83,s83,3
ds_read_b64_tr_b16 v[116:117],v2 offset:10336
v_accvgpr_read_b32 v20,a28
v_mfma_f32_16x16x32_bf16 a[204:207],v[76:79],a[120:123],0
s_lshl_b32 s83,s83,1
ds_read_b64_tr_b16 v[118:119],v2 offset:15456
ds_read_b64_tr_b16 v[120:121],v2 offset:20576
v_mfma_f32_16x16x32_bf16 a[204:207],v[72:75],a[112:115],a[204:207]
s_mul_i32 s83,s83,163840
ds_read_b64_tr_b16 v[122:123],v2 offset:25696
v_accvgpr_read_b32 v21,a29
v_mfma_f32_16x16x32_bf16 a[204:207],v[68:71],a[104:107],a[204:207]
s_add_u32 s83,s83,s85
ds_read_b64_tr_b16 v[124:125],v2 offset:30816
v_accvgpr_read_b32 v22,a30
v_mfma_f32_16x16x32_bf16 a[204:207],v[64:67],a[96:99],a[204:207]
buffer_store_dwordx4 v[36:39],v6,s[40:43],s80 offen sc1
s_waitcnt lgkmcnt(9)
v_mfma_f32_16x16x32_bf16 a[208:211],v[80:83],a[68:71],0
s_add_u32 s84,s83,20480
ds_read_b64_tr_b16 v[126:127],v2 offset:35936
ds_read_b64_tr_b16 v[64:65],v1 offset:128
v_mfma_f32_16x16x32_bf16 a[208:211],v[84:87],a[76:79],a[208:211]
s_lshl_b32 s66,s61,3
ds_read_b64_tr_b16 v[66:67],v1 offset:5248
v_accvgpr_read_b32 v23,a31
v_mfma_f32_16x16x32_bf16 a[208:211],v[88:91],a[84:87],a[208:211]
s_add_u32 s66,s66,s64
ds_read_b64_tr_b16 v[68:69],v1 offset:10368
v_mul_f32 v16,v16,v49
v_mfma_f32_16x16x32_bf16 a[208:211],v[92:95],a[92:95],a[208:211]
s_mul_i32 s66,s66,262144
ds_read_b64_tr_b16 v[70:71],v1 offset:15488
v_mul_f32 v17,v17,v49
v_mfma_f32_16x16x32_bf16 a[212:215],v[92:95],a[124:127],0
s_waitcnt vmcnt(21)
buffer_load_dwordx2 v[4:5],v3,s[44:47],s91 offen offset:256 sc1
v_mfma_f32_16x16x32_bf16 a[212:215],v[88:91],a[116:119],a[212:215]
s_add_u32 s66,s66,100663296
ds_read_b64_tr_b16 v[72:73],v1 offset:20608
ds_read_b64_tr_b16 v[74:75],v1 offset:25728
v_mfma_f32_16x16x32_bf16 a[212:215],v[84:87],a[108:111],a[212:215]
s_mov_b32 s38,262144
ds_read_b64_tr_b16 v[76:77],v1 offset:30848
v_mul_f32 v18,v18,v49
v_mfma_f32_16x16x32_bf16 a[212:215],v[80:83],a[100:103],a[212:215]
s_mov_b32 s39,0x20000
ds_read_b64_tr_b16 v[78:79],v1 offset:35968
v_mul_f32 v19,v19,v49
v_mfma_f32_16x16x32_bf16 a[216:219],v[80:83],a[64:67],0
s_add_u32 s74,s63,1
v_mul_f32 v20,v20,v49
v_mul_f32 v21,v21,v49
v_mfma_f32_16x16x32_bf16 a[216:219],v[84:87],a[72:75],a[216:219]
s_and_b32 s74,s74,3
v_mul_f32 v22,v22,v49
v_mul_f32 v23,v23,v49
v_mfma_f32_16x16x32_bf16 a[216:219],v[88:91],a[80:83],a[216:219]
s_lshl_b32 s74,s74,2
v_cvt_pk_bf16_f32 v44,v16,v17
v_cvt_pk_bf16_f32 v45,v18,v19
v_mfma_f32_16x16x32_bf16 a[216:219],v[92:95],a[88:91],a[216:219]
s_add_u32 s74,s74,s60
v_cvt_pk_bf16_f32 v46,v20,v21
v_cvt_pk_bf16_f32 v47,v22,v23
v_mfma_f32_16x16x32_bf16 a[220:223],v[92:95],a[120:123],0
s_mul_i32 s74,s74,16384
ds_read_b64_tr_b16 v[92:93],v2 offset:30880
ds_read_b64_tr_b16 v[94:95],v2 offset:36000
v_mfma_f32_16x16x32_bf16 a[220:223],v[88:91],a[112:115],a[220:223]
s_add_u32 s74,s74,8192
ds_read_b64_tr_b16 v[88:89],v2 offset:20640
v_mul_f32 v132,v132,v53
v_mfma_f32_16x16x32_bf16 a[220:223],v[84:87],a[104:107],a[220:223]
buffer_store_dwordx4 v[40:43],v6,s[40:43],s81 offen sc1
v_mfma_f32_16x16x32_bf16 a[220:223],v[80:83],a[96:99],a[220:223]
s_add_u32 s75,s63,2
ds_read_b64_tr_b16 v[80:81],v2 offset:160
ds_read_b64_tr_b16 v[82:83],v2 offset:5280
v_mfma_f32_16x16x32_bf16 a[224:227],v[96:99],a[68:71],0
s_and_b32 s75,s75,3
ds_read_b64_tr_b16 v[84:85],v2 offset:10400
v_mul_f32 v133,v133,v53
v_mfma_f32_16x16x32_bf16 a[224:227],v[100:103],a[76:79],a[224:227]
s_lshl_b32 s75,s75,2
ds_read_b64_tr_b16 v[86:87],v2 offset:15520
v_mul_f32 v134,v134,v53
v_mfma_f32_16x16x32_bf16 a[224:227],v[104:107],a[84:87],a[224:227]
s_add_u32 s75,s75,s60
ds_read_b64_tr_b16 v[90:91],v2 offset:25760
v_mul_f32 v135,v135,v53
s_waitcnt lgkmcnt(8)
v_mfma_f32_16x16x32_bf16 a[224:227],v[108:111],a[92:95],a[224:227]
s_mul_i32 s75,s75,16384
v_mul_f32 v140,v140,v53
v_mul_f32 v141,v141,v53
v_mfma_f32_16x16x32_bf16 a[228:231],v[108:111],a[124:127],0
s_add_u32 s75,s75,4096
v_mul_f32 v142,v142,v53
v_mul_f32 v143,v143,v53
v_mfma_f32_16x16x32_bf16 a[228:231],v[104:107],a[116:119],a[228:231]
s_add_u32 s76,s63,3
v_cvt_pk_bf16_f32 v132,v132,v133
v_cvt_pk_bf16_f32 v133,v134,v135
v_mfma_f32_16x16x32_bf16 a[228:231],v[100:103],a[108:111],a[228:231]
s_and_b32 s76,s76,3
v_cvt_pk_bf16_f32 v134,v140,v141
v_cvt_pk_bf16_f32 v135,v142,v143
v_mfma_f32_16x16x32_bf16 a[228:231],v[96:99],a[100:103],a[228:231]
s_lshl_b32 s76,s76,2
v_mul_f32 v148,v148,v53
v_mul_f32 v149,v149,v53
v_mfma_f32_16x16x32_bf16 a[232:235],v[96:99],a[64:67],0
s_add_u32 s76,s76,s60
v_mul_f32 v150,v150,v53
v_mul_f32 v151,v151,v53
v_mfma_f32_16x16x32_bf16 a[232:235],v[100:103],a[72:75],a[232:235]
s_mul_i32 s76,s76,16384
v_mul_f32 v156,v156,v53
v_mul_f32 v157,v157,v53
v_mfma_f32_16x16x32_bf16 a[232:235],v[104:107],a[80:83],a[232:235]
s_add_u32 s76,s76,0
v_mul_f32 v158,v158,v53
v_mul_f32 v159,v159,v53
v_mfma_f32_16x16x32_bf16 a[232:235],v[108:111],a[88:91],a[232:235]
s_lshl_b32 s67,s63,10
v_cvt_pk_bf16_f32 v148,v148,v149
v_cvt_pk_bf16_f32 v149,v150,v151
v_mfma_f32_16x16x32_bf16 a[236:239],v[108:111],a[120:123],0
s_mov_b32 s26,307200
ds_read_b64_tr_b16 v[108:109],v1 offset:30912
ds_read_b64_tr_b16 v[110:111],v1 offset:36032
v_mfma_f32_16x16x32_bf16 a[236:239],v[104:107],a[112:115],a[236:239]
buffer_store_dwordx4 v[44:47],v6,s[40:43],s82 offen sc1
)A"
R"A(v_mfma_f32_16x16x32_bf16 a[236:239],v[100:103],a[104:107],a[236:239]
s_mov_b32 s27,0x20000
ds_read_b64_tr_b16 v[100:101],v1 offset:10432
ds_read_b64_tr_b16 v[102:103],v1 offset:15552
v_mfma_f32_16x16x32_bf16 a[236:239],v[96:99],a[96:99],a[236:239]
s_lshl_b32 s77,s63,2
ds_read_b64_tr_b16 v[96:97],v1 offset:192
v_cvt_pk_bf16_f32 v150,v156,v157
v_mfma_f32_16x16x32_bf16 a[240:243],v[112:115],a[68:71],0
s_add_u32 s77,s77,s60
ds_read_b64_tr_b16 v[98:99],v1 offset:5312
v_cvt_pk_bf16_f32 v151,v158,v159
v_mfma_f32_16x16x32_bf16 a[240:243],v[116:119],a[76:79],a[240:243]
s_mul_i32 s77,s77,16384
ds_read_b64_tr_b16 v[104:105],v1 offset:20672
v_mul_f32 v164,v164,v53
v_mfma_f32_16x16x32_bf16 a[240:243],v[120:123],a[84:87],a[240:243]
s_add_u32 s65,s77,0
ds_read_b64_tr_b16 v[106:107],v1 offset:25792
v_mul_f32 v165,v165,v53
v_mfma_f32_16x16x32_bf16 a[240:243],v[124:127],a[92:95],a[240:243]
s_add_u32 s68,s77,12288
v_mul_f32 v166,v166,v53
v_mul_f32 v167,v167,v53
v_mfma_f32_16x16x32_bf16 a[244:247],v[124:127],a[124:127],0
v_mul_f32 v172,v172,v53
v_mul_f32 v173,v173,v53
v_mfma_f32_16x16x32_bf16 a[244:247],v[120:123],a[116:119],a[244:247]
v_mul_f32 v174,v174,v53
v_mul_f32 v175,v175,v53
v_mfma_f32_16x16x32_bf16 a[244:247],v[116:119],a[108:111],a[244:247]
v_cvt_pk_bf16_f32 v164,v164,v165
v_cvt_pk_bf16_f32 v165,v166,v167
v_mfma_f32_16x16x32_bf16 a[244:247],v[112:115],a[100:103],a[244:247]
v_cvt_pk_bf16_f32 v166,v172,v173
v_cvt_pk_bf16_f32 v167,v174,v175
v_mfma_f32_16x16x32_bf16 a[248:251],v[112:115],a[64:67],0
v_mul_f32 v180,v180,v53
v_mul_f32 v181,v181,v53
v_mfma_f32_16x16x32_bf16 a[248:251],v[116:119],a[72:75],a[248:251]
v_mul_f32 v182,v182,v53
v_mul_f32 v183,v183,v53
v_mfma_f32_16x16x32_bf16 a[248:251],v[120:123],a[80:83],a[248:251]
v_mul_f32 v188,v188,v53
v_mul_f32 v189,v189,v53
v_mfma_f32_16x16x32_bf16 a[248:251],v[124:127],a[88:91],a[248:251]
v_mul_f32 v190,v190,v53
v_mul_f32 v191,v191,v53
v_mfma_f32_16x16x32_bf16 a[252:255],v[124:127],a[120:123],0
buffer_load_dword v30,v29,s[8:11],s7 offen
v_mfma_f32_16x16x32_bf16 a[252:255],v[120:123],a[112:115],a[252:255]
ds_read_b64_tr_b16 v[120:121],v2 offset:20704
ds_read_b64_tr_b16 v[122:123],v2 offset:25824
v_mfma_f32_16x16x32_bf16 a[252:255],v[116:119],a[104:107],a[252:255]
buffer_store_dwordx4 v[132:135],v6,s[40:43],s79 offen offset:1024 sc1
v_mfma_f32_16x16x32_bf16 a[252:255],v[112:115],a[96:99],a[252:255]
ds_read_b64_tr_b16 v[112:113],v2 offset:224
ds_read_b64_tr_b16 v[114:115],v2 offset:5344
v_mfma_f32_16x16x32_bf16 a[0:3],v[64:67],a[68:71],0
ds_read_b64_tr_b16 v[116:117],v2 offset:10464
v_cvt_pk_bf16_f32 v180,v180,v181
v_mfma_f32_16x16x32_bf16 a[0:3],v[68:71],a[76:79],a[0:3]
ds_read_b64_tr_b16 v[118:119],v2 offset:15584
v_cvt_pk_bf16_f32 v181,v182,v183
v_mfma_f32_16x16x32_bf16 a[0:3],v[72:75],a[84:87],a[0:3]
ds_read_b64_tr_b16 v[124:125],v2 offset:30944
v_cvt_pk_bf16_f32 v182,v188,v189
v_mfma_f32_16x16x32_bf16 a[0:3],v[76:79],a[92:95],a[0:3]
ds_read_b64_tr_b16 v[126:127],v2 offset:36064
v_cvt_pk_bf16_f32 v183,v190,v191
v_mfma_f32_16x16x32_bf16 a[4:7],v[76:79],a[124:127],0
v_mul_f32 v196,v196,v57
v_mul_f32 v197,v197,v57
v_mfma_f32_16x16x32_bf16 a[4:7],v[72:75],a[116:119],a[4:7]
v_mul_f32 v198,v198,v57
v_mul_f32 v199,v199,v57
v_mfma_f32_16x16x32_bf16 a[4:7],v[68:71],a[108:111],a[4:7]
v_mul_f32 v204,v204,v57
v_mul_f32 v205,v205,v57
v_mfma_f32_16x16x32_bf16 a[4:7],v[64:67],a[100:103],a[4:7]
v_mul_f32 v206,v206,v57
v_mul_f32 v207,v207,v57
v_mfma_f32_16x16x32_bf16 a[8:11],v[64:67],a[64:67],0
v_cvt_pk_bf16_f32 v196,v196,v197
v_cvt_pk_bf16_f32 v197,v198,v199
v_mfma_f32_16x16x32_bf16 a[8:11],v[68:71],a[72:75],a[8:11]
v_cvt_pk_bf16_f32 v198,v204,v205
v_cvt_pk_bf16_f32 v199,v206,v207
v_mfma_f32_16x16x32_bf16 a[8:11],v[72:75],a[80:83],a[8:11]
v_mul_f32 v212,v212,v57
v_mul_f32 v213,v213,v57
v_mfma_f32_16x16x32_bf16 a[8:11],v[76:79],a[88:91],a[8:11]
v_mul_f32 v214,v214,v57
v_mul_f32 v215,v215,v57
v_mfma_f32_16x16x32_bf16 a[12:15],v[76:79],a[120:123],0
ds_read_b64_tr_b16 v[76:77],v1 offset:30976
ds_read_b64_tr_b16 v[78:79],v1 offset:36096
v_mfma_f32_16x16x32_bf16 a[12:15],v[72:75],a[112:115],a[12:15]
buffer_store_dwordx4 v[148:151],v6,s[40:43],s80 offen offset:1024 sc1
v_mfma_f32_16x16x32_bf16 a[12:15],v[68:71],a[104:107],a[12:15]
ds_read_b64_tr_b16 v[68:69],v1 offset:10496
ds_read_b64_tr_b16 v[70:71],v1 offset:15616
v_mfma_f32_16x16x32_bf16 a[12:15],v[64:67],a[96:99],a[12:15]
s_waitcnt lgkmcnt(4)
ds_read_b64_tr_b16 v[64:65],v1 offset:256
v_mul_f32 v220,v220,v57
v_mfma_f32_16x16x32_bf16 a[16:19],v[80:83],a[68:71],0
ds_read_b64_tr_b16 v[66:67],v1 offset:5376
v_mul_f32 v221,v221,v57
v_mfma_f32_16x16x32_bf16 a[16:19],v[84:87],a[76:79],a[16:19]
ds_read_b64_tr_b16 v[72:73],v1 offset:20736
v_mul_f32 v222,v222,v57
v_mfma_f32_16x16x32_bf16 a[16:19],v[88:91],a[84:87],a[16:19]
ds_read_b64_tr_b16 v[74:75],v1 offset:25856
v_add_u32 v1,0xffff6000,v1
v_mfma_f32_16x16x32_bf16 a[16:19],v[92:95],a[92:95],a[16:19]
v_mul_f32 v223,v223,v57
v_cvt_pk_bf16_f32 v212,v212,v213
v_mfma_f32_16x16x32_bf16 a[20:23],v[92:95],a[124:127],0
v_cvt_pk_bf16_f32 v213,v214,v215
v_cvt_pk_bf16_f32 v214,v220,v221
v_mfma_f32_16x16x32_bf16 a[20:23],v[88:91],a[116:119],a[20:23]
v_cvt_pk_bf16_f32 v215,v222,v223
v_mul_f32 v228,v228,v57
v_mfma_f32_16x16x32_bf16 a[20:23],v[84:87],a[108:111],a[20:23]
v_mul_f32 v229,v229,v57
v_mul_f32 v230,v230,v57
v_mfma_f32_16x16x32_bf16 a[20:23],v[80:83],a[100:103],a[20:23]
v_mul_f32 v231,v231,v57
v_mul_f32 v236,v236,v57
v_mfma_f32_16x16x32_bf16 a[24:27],v[80:83],a[64:67],0
v_mul_f32 v237,v237,v57
v_mul_f32 v238,v238,v57
v_mfma_f32_16x16x32_bf16 a[24:27],v[84:87],a[72:75],a[24:27]
v_mul_f32 v239,v239,v57
v_cvt_pk_bf16_f32 v228,v228,v229
v_mfma_f32_16x16x32_bf16 a[24:27],v[88:91],a[80:83],a[24:27]
s_waitcnt vmcnt(5)
v_cmp_ne_u32 s[56:57],s52,v4
v_cmp_ne_u32 s[58:59],s53,v5
v_mfma_f32_16x16x32_bf16 a[24:27],v[92:95],a[88:91],a[24:27]
s_or_b64 s[56:57],s[56:57],s[58:59]
s_cbranch_scc1 L_fr6_%=
L_fo6_%=:
v_cvt_pk_bf16_f32 v229,v230,v231
v_mfma_f32_16x16x32_bf16 a[28:31],v[92:95],a[120:123],0
buffer_store_dwordx4 v[164:167],v6,s[40:43],s81 offen offset:1024 sc1
v_mfma_f32_16x16x32_bf16 a[28:31],v[88:91],a[112:115],a[28:31]
s_lshl_b32 s91,s63,8
ds_read_b64_tr_b16 v[88:89],v2 offset:20768
ds_read_b64_tr_b16 v[90:91],v2 offset:25888
v_mfma_f32_16x16x32_bf16 a[28:31],v[84:87],a[104:107],a[28:31]
buffer_load_dwordx4 a[128:131],v6,s[28:31],s87 offen sc1
v_mfma_f32_16x16x32_bf16 a[28:31],v[80:83],a[96:99],a[28:31]
ds_read_b64_tr_b16 v[80:81],v2 offset:288
ds_read_b64_tr_b16 v[82:83],v2 offset:5408
v_mfma_f32_16x16x32_bf16 v[128:131],v[96:99],a[68:71],0
ds_read_b64_tr_b16 v[84:85],v2 offset:10528
v_cvt_pk_bf16_f32 v230,v236,v237
v_mfma_f32_16x16x32_bf16 v[128:131],v[100:103],a[76:79],v[128:131]
ds_read_b64_tr_b16 v[86:87],v2 offset:15648
v_cvt_pk_bf16_f32 v231,v238,v239
v_mfma_f32_16x16x32_bf16 v[128:131],v[104:107],a[84:87],v[128:131]
ds_read_b64_tr_b16 v[92:93],v2 offset:31008
v_mul_f32 v244,v244,v57
v_mfma_f32_16x16x32_bf16 v[128:131],v[108:111],a[92:95],v[128:131]
ds_read_b64_tr_b16 v[94:95],v2 offset:36128
v_add_u32 v2,0xffff6000,v2
v_mfma_f32_16x16x32_bf16 v[136:139],v[108:111],a[124:127],0
v_mul_f32 v245,v245,v57
v_mul_f32 v246,v246,v57
v_mfma_f32_16x16x32_bf16 v[136:139],v[104:107],a[116:119],v[136:139]
v_mul_f32 v247,v247,v57
v_mul_f32 v252,v252,v57
v_mfma_f32_16x16x32_bf16 v[136:139],v[100:103],a[108:111],v[136:139]
v_mul_f32 v253,v253,v57
v_mul_f32 v254,v254,v57
v_mfma_f32_16x16x32_bf16 v[136:139],v[96:99],a[100:103],v[136:139]
v_mul_f32 v255,v255,v57
v_cvt_pk_bf16_f32 v244,v244,v245
v_mfma_f32_16x16x32_bf16 v[144:147],v[96:99],a[64:67],0
v_cvt_pk_bf16_f32 v245,v246,v247
v_cvt_pk_bf16_f32 v246,v252,v253
v_mfma_f32_16x16x32_bf16 v[144:147],v[100:103],a[72:75],v[144:147]
buffer_load_dwordx4 a[144:147],v6,s[28:31],s88 offen sc1
v_mfma_f32_16x16x32_bf16 v[144:147],v[104:107],a[80:83],v[144:147]
v_cvt_pk_bf16_f32 v247,v254,v255
v_accvgpr_read_b32 v16,a40
v_mfma_f32_16x16x32_bf16 v[144:147],v[108:111],a[88:91],v[144:147]
buffer_store_dwordx4 v[180:183],v6,s[40:43],s82 offen offset:1024 sc1
v_mfma_f32_16x16x32_bf16 v[152:155],v[108:111],a[120:123],0
ds_read_b64_tr_b16 v[108:109],v1 offset:30720
ds_read_b64_tr_b16 v[110:111],v1 offset:35840
v_mfma_f32_16x16x32_bf16 v[152:155],v[104:107],a[112:115],v[152:155]
ds_read_b64_tr_b16 v[104:105],v1 offset:20480
v_accvgpr_read_b32 v17,a41
v_mfma_f32_16x16x32_bf16 v[152:155],v[100:103],a[104:107],v[152:155]
ds_read_b64_tr_b16 v[100:101],v1 offset:10240
v_accvgpr_read_b32 v18,a42
v_mfma_f32_16x16x32_bf16 v[152:155],v[96:99],a[96:99],v[152:155]
ds_read_b64_tr_b16 v[96:97],v1
v_accvgpr_read_b32 v19,a43
v_mfma_f32_16x16x32_bf16 v[160:163],v[112:115],a[68:71],0
ds_read_b64_tr_b16 v[98:99],v1 offset:5120
ds_read_b64_tr_b16 v[102:103],v1 offset:15360
v_mfma_f32_16x16x32_bf16 v[160:163],v[116:119],a[76:79],v[160:163]
ds_read_b64_tr_b16 v[106:107],v1 offset:25600
v_accvgpr_read_b32 v20,a44
v_mfma_f32_16x16x32_bf16 v[160:163],v[120:123],a[84:87],v[160:163]
v_accvgpr_read_b32 v21,a45
v_accvgpr_read_b32 v22,a46
v_mfma_f32_16x16x32_bf16 v[160:163],v[124:127],a[92:95],v[160:163]
v_accvgpr_read_b32 v23,a47
v_mul_f32 v16,v16,v61
v_mfma_f32_16x16x32_bf16 v[168:171],v[124:127],a[124:127],0
v_mul_f32 v17,v17,v61
v_mul_f32 v18,v18,v61
v_mfma_f32_16x16x32_bf16 v[168:171],v[120:123],a[116:119],v[168:171]
v_mul_f32 v19,v19,v61
v_mul_f32 v20,v20,v61
v_mfma_f32_16x16x32_bf16 v[168:171],v[116:119],a[108:111],v[168:171]
buffer_load_dwordx4 a[160:163],v6,s[28:31],s89 offen sc1
v_mfma_f32_16x16x32_bf16 v[168:171],v[112:115],a[100:103],v[168:171]
v_mul_f32 v21,v21,v61
v_mul_f32 v22,v22,v61
v_mfma_f32_16x16x32_bf16 v[176:179],v[112:115],a[64:67],0
v_mul_f32 v23,v23,v61
v_cvt_pk_bf16_f32 v156,v16,v17
v_mfma_f32_16x16x32_bf16 v[176:179],v[116:119],a[72:75],v[176:179]
v_cvt_pk_bf16_f32 v157,v18,v19
v_cvt_pk_bf16_f32 v158,v20,v21
v_mfma_f32_16x16x32_bf16 v[176:179],v[120:123],a[80:83],v[176:179]
buffer_store_dwordx4 v[196:199],v6,s[40:43],s79 offen offset:2048 sc1
v_mfma_f32_16x16x32_bf16 v[176:179],v[124:127],a[88:91],v[176:179]
v_cvt_pk_bf16_f32 v159,v22,v23
v_accvgpr_read_b32 v16,a56
v_mfma_f32_16x16x32_bf16 v[184:187],v[124:127],a[120:123],0
v_accvgpr_read_b32 v17,a57
v_accvgpr_read_b32 v18,a58
v_mfma_f32_16x16x32_bf16 v[184:187],v[120:123],a[112:115],v[184:187]
ds_read_b64_tr_b16 v[120:121],v2 offset:20512
ds_read_b64_tr_b16 v[122:123],v2 offset:25632
v_mfma_f32_16x16x32_bf16 v[184:187],v[116:119],a[104:107],v[184:187]
ds_read_b64_tr_b16 v[116:117],v2 offset:10272
v_accvgpr_read_b32 v19,a59
v_mfma_f32_16x16x32_bf16 v[184:187],v[112:115],a[96:99],v[184:187]
s_waitcnt lgkmcnt(3)
ds_read_b64_tr_b16 v[112:113],v2 offset:32
v_accvgpr_read_b32 v20,a60
v_mfma_f32_16x16x32_bf16 v[192:195],v[64:67],a[68:71],0
ds_read_b64_tr_b16 v[114:115],v2 offset:5152
v_accvgpr_read_b32 v21,a61
v_mfma_f32_16x16x32_bf16 v[192:195],v[68:71],a[76:79],v[192:195]
ds_read_b64_tr_b16 v[118:119],v2 offset:15392
v_accvgpr_read_b32 v22,a62
v_mfma_f32_16x16x32_bf16 v[192:195],v[72:75],a[84:87],v[192:195]
v_accvgpr_read_b32 v23,a63
v_mul_f32 v16,v16,v61
v_mfma_f32_16x16x32_bf16 v[192:195],v[76:79],a[92:95],v[192:195]
buffer_load_dwordx4 a[176:179],v6,s[28:31],s90 offen sc1
v_mfma_f32_16x16x32_bf16 v[200:203],v[76:79],a[124:127],0
v_mul_f32 v17,v17,v61
v_mul_f32 v18,v18,v61
v_mfma_f32_16x16x32_bf16 v[200:203],v[72:75],a[116:119],v[200:203]
v_mul_f32 v19,v19,v61
v_mul_f32 v20,v20,v61
v_mfma_f32_16x16x32_bf16 v[200:203],v[68:71],a[108:111],v[200:203]
v_mul_f32 v21,v21,v61
v_mul_f32 v22,v22,v61
v_mfma_f32_16x16x32_bf16 v[200:203],v[64:67],a[100:103],v[200:203]
v_mul_f32 v23,v23,v61
v_cvt_pk_bf16_f32 v188,v16,v17
v_mfma_f32_16x16x32_bf16 v[208:211],v[64:67],a[64:67],0
v_cvt_pk_bf16_f32 v189,v18,v19
v_cvt_pk_bf16_f32 v190,v20,v21
v_mfma_f32_16x16x32_bf16 v[208:211],v[68:71],a[72:75],v[208:211]
buffer_store_dwordx4 v[212:215],v6,s[40:43],s80 offen offset:2048 sc1
v_mfma_f32_16x16x32_bf16 v[208:211],v[72:75],a[80:83],v[208:211]
ds_read_b64_tr_b16 v[124:125],v2 offset:30752
v_cvt_pk_bf16_f32 v191,v22,v23
v_mfma_f32_16x16x32_bf16 v[208:211],v[76:79],a[88:91],v[208:211]
)A"
R"A(ds_read_b64_tr_b16 v[126:127],v2 offset:35872
v_accvgpr_read_b32 v8,a32
v_mfma_f32_16x16x32_bf16 v[216:219],v[76:79],a[120:123],0
v_accvgpr_read_b32 v9,a33
v_accvgpr_read_b32 v10,a34
v_mfma_f32_16x16x32_bf16 v[216:219],v[72:75],a[112:115],v[216:219]
v_accvgpr_read_b32 v11,a35
v_accvgpr_read_b32 v12,a36
v_mfma_f32_16x16x32_bf16 v[216:219],v[68:71],a[104:107],v[216:219]
v_accvgpr_read_b32 v13,a37
v_accvgpr_read_b32 v14,a38
v_mfma_f32_16x16x32_bf16 v[216:219],v[64:67],a[96:99],v[216:219]
v_accvgpr_read_b32 v15,a39
v_mul_f32 v8,v8,v61
v_mfma_f32_16x16x32_bf16 v[224:227],v[80:83],a[68:71],0
buffer_load_dwordx4 a[180:183],v6,s[28:31],s90 offen offset:1024 sc1
v_mfma_f32_16x16x32_bf16 v[224:227],v[84:87],a[76:79],v[224:227]
v_mul_f32 v9,v9,v61
v_mul_f32 v10,v10,v61
v_mfma_f32_16x16x32_bf16 v[224:227],v[88:91],a[84:87],v[224:227]
v_mul_f32 v11,v11,v61
v_mul_f32 v12,v12,v61
v_mfma_f32_16x16x32_bf16 v[224:227],v[92:95],a[92:95],v[224:227]
v_mul_f32 v13,v13,v61
v_mul_f32 v14,v14,v61
v_mfma_f32_16x16x32_bf16 v[232:235],v[92:95],a[124:127],0
v_mul_f32 v15,v15,v61
v_cvt_pk_bf16_f32 v140,v8,v9
v_mfma_f32_16x16x32_bf16 v[232:235],v[88:91],a[116:119],v[232:235]
v_cvt_pk_bf16_f32 v141,v10,v11
v_cvt_pk_bf16_f32 v142,v12,v13
v_mfma_f32_16x16x32_bf16 v[232:235],v[84:87],a[108:111],v[232:235]
v_cvt_pk_bf16_f32 v143,v14,v15
v_accvgpr_read_b32 v8,a48
v_mfma_f32_16x16x32_bf16 v[232:235],v[80:83],a[100:103],v[232:235]
v_accvgpr_read_b32 v9,a49
v_accvgpr_read_b32 v10,a50
v_mfma_f32_16x16x32_bf16 v[240:243],v[80:83],a[64:67],0
buffer_store_dwordx4 v[228:231],v6,s[40:43],s81 offen offset:2048 sc1
v_mfma_f32_16x16x32_bf16 v[240:243],v[84:87],a[72:75],v[240:243]
v_accvgpr_read_b32 v11,a51
v_accvgpr_read_b32 v12,a52
v_mfma_f32_16x16x32_bf16 v[240:243],v[88:91],a[80:83],v[240:243]
v_accvgpr_read_b32 v13,a53
v_accvgpr_read_b32 v14,a54
v_mfma_f32_16x16x32_bf16 v[240:243],v[92:95],a[88:91],v[240:243]
v_accvgpr_read_b32 v15,a55
v_mul_f32 v8,v8,v61
v_mfma_f32_16x16x32_bf16 v[248:251],v[92:95],a[120:123],0
v_mul_f32 v9,v9,v61
v_mul_f32 v10,v10,v61
v_mfma_f32_16x16x32_bf16 v[248:251],v[88:91],a[112:115],v[248:251]
buffer_load_dwordx4 a[164:167],v6,s[28:31],s89 offen offset:1024 sc1
v_mfma_f32_16x16x32_bf16 v[248:251],v[84:87],a[104:107],v[248:251]
v_mul_f32 v11,v11,v61
v_mul_f32 v12,v12,v61
v_mfma_f32_16x16x32_bf16 v[248:251],v[80:83],a[96:99],v[248:251]
v_mul_f32 v13,v13,v61
v_mul_f32 v14,v14,v61
v_mfma_f32_16x16x32_bf16 a[192:195],v[96:99],v[32:35],a[192:195]
v_mul_f32 v15,v15,v61
v_cvt_pk_bf16_f32 v172,v8,v9
v_mfma_f32_16x16x32_bf16 a[192:195],v[100:103],v[36:39],a[192:195]
v_cvt_pk_bf16_f32 v173,v10,v11
v_cvt_pk_bf16_f32 v174,v12,v13
v_mfma_f32_16x16x32_bf16 a[192:195],v[104:107],v[40:43],a[192:195]
ds_read_b64_tr_b16 v[64:65],v1 offset:64
v_cvt_pk_bf16_f32 v175,v14,v15
v_mfma_f32_16x16x32_bf16 a[192:195],v[108:111],v[44:47],a[192:195]
ds_read_b64_tr_b16 v[66:67],v1 offset:5184
ds_read_b64_tr_b16 v[68:69],v1 offset:10304
v_mfma_f32_16x16x32_bf16 a[196:199],v[108:111],v[180:183],a[196:199]
s_cmp_eq_u32 s60,3
s_cselect_b32 s69,0x40000000,0
ds_read_b64_tr_b16 v[70:71],v1 offset:15424
v_mfma_f32_16x16x32_bf16 a[196:199],v[104:107],v[164:167],a[196:199]
ds_read_b64_tr_b16 v[72:73],v1 offset:20544
v_add_u32 v28,s69,v6
v_mfma_f32_16x16x32_bf16 a[196:199],v[100:103],v[148:151],a[196:199]
s_and_b32 s69,s63,s60
ds_read_b64_tr_b16 v[74:75],v1 offset:25664
v_mov_b32 v8,s52
v_mfma_f32_16x16x32_bf16 a[196:199],v[96:99],v[132:135],a[196:199]
buffer_store_dwordx4 v[244:247],v6,s[40:43],s82 offen offset:2048 sc1
v_mfma_f32_16x16x32_bf16 a[200:203],v[96:99],v[196:199],a[200:203]
ds_read_b64_tr_b16 v[76:77],v1 offset:30784
ds_read_b64_tr_b16 v[78:79],v1 offset:35904
v_mfma_f32_16x16x32_bf16 a[200:203],v[100:103],v[212:215],a[200:203]
ds_read_b64_tr_b16 v[80:81],v2 offset:96
v_mov_b32 v9,s53
v_mfma_f32_16x16x32_bf16 a[200:203],v[104:107],v[228:231],a[200:203]
buffer_load_dwordx4 a[148:151],v6,s[28:31],s88 offen offset:1024 sc1
v_mfma_f32_16x16x32_bf16 a[200:203],v[108:111],v[244:247],a[200:203]
ds_read_b64_tr_b16 v[82:83],v2 offset:5216
ds_read_b64_tr_b16 v[84:85],v2 offset:10336
v_mfma_f32_16x16x32_bf16 a[204:207],v[108:111],v[188:191],a[204:207]
ds_read_b64_tr_b16 v[86:87],v2 offset:15456
v_lshlrev_b32 v10,20,v6
v_mfma_f32_16x16x32_bf16 a[204:207],v[104:107],v[172:175],a[204:207]
ds_read_b64_tr_b16 v[88:89],v2 offset:20576
v_bfe_u32 v3,v6,4,2
v_mfma_f32_16x16x32_bf16 a[204:207],v[100:103],v[156:159],a[204:207]
ds_read_b64_tr_b16 v[90:91],v2 offset:25696
v_min_u32 v3,2,v3
v_mfma_f32_16x16x32_bf16 a[204:207],v[96:99],v[140:143],a[204:207]
s_waitcnt lgkmcnt(8)
ds_read_b64_tr_b16 v[92:93],v2 offset:30816
ds_read_b64_tr_b16 v[94:95],v2 offset:35936
v_mfma_f32_16x16x32_bf16 a[208:211],v[112:115],v[32:35],a[208:211]
ds_read_b64_tr_b16 v[96:97],v1 offset:128
v_add3_u32 v3,v3,s64,1
v_mfma_f32_16x16x32_bf16 a[208:211],v[116:119],v[36:39],a[208:211]
ds_read_b64_tr_b16 v[98:99],v1 offset:5248
v_and_b32 v3,3,v3
v_mfma_f32_16x16x32_bf16 a[208:211],v[120:123],v[40:43],a[208:211]
ds_read_b64_tr_b16 v[100:101],v1 offset:10368
v_lshlrev_b32 v3,6,v3
v_mfma_f32_16x16x32_bf16 a[208:211],v[124:127],v[44:47],a[208:211]
ds_read_b64_tr_b16 v[102:103],v1 offset:15488
ds_read_b64_tr_b16 v[104:105],v1 offset:20608
v_mfma_f32_16x16x32_bf16 a[212:215],v[124:127],v[180:183],a[212:215]
ds_read_b64_tr_b16 v[106:107],v1 offset:25728
v_bfe_u32 v56,v6,6,2
v_mfma_f32_16x16x32_bf16 a[212:215],v[120:123],v[164:167],a[212:215]
ds_read_b64_tr_b16 v[108:109],v1 offset:30848
v_lshrrev_b32 v57,8,v6
v_mfma_f32_16x16x32_bf16 a[212:215],v[116:119],v[148:151],a[212:215]
buffer_store_dwordx4 v[140:143],v28,s[40:43],s79 offen offset:3072 sc1
v_mfma_f32_16x16x32_bf16 a[212:215],v[112:115],v[132:135],a[212:215]
ds_read_b64_tr_b16 v[110:111],v1 offset:35968
v_cmp_eq_u32 s[56:57],v56,v57
L_pd20_%=:
s_cbranch_execz L_trt1_%=
v_mfma_f32_16x16x32_bf16 a[216:219],v[112:115],v[196:199],a[216:219]
buffer_load_dwordx4 a[132:135],v6,s[28:31],s87 offen offset:1024 sc1
v_mfma_f32_16x16x32_bf16 a[216:219],v[116:119],v[212:215],a[216:219]
v_bfe_u32 v56,v6,4,1
v_lshlrev_b32 v56,4,v56
v_mfma_f32_16x16x32_bf16 a[216:219],v[120:123],v[228:231],a[216:219]
v_mov_b32 v57,0x3f80
v_lshlrev_b32 v56,v56,v57
v_mfma_f32_16x16x32_bf16 a[216:219],v[124:127],v[244:247],a[216:219]
v_cndmask_b32 v56,0,v56,s[56:57]
v_bfe_u32 v57,v6,5,1
v_mfma_f32_16x16x32_bf16 a[220:223],v[124:127],v[188:191],a[220:223]
ds_read_b64_tr_b16 v[124:125],v2 offset:30880
ds_read_b64_tr_b16 v[126:127],v2 offset:36000
v_mfma_f32_16x16x32_bf16 a[220:223],v[120:123],v[172:175],a[220:223]
ds_read_b64_tr_b16 v[120:121],v2 offset:20640
v_cmp_eq_u32 s[56:57],0,v57
v_mfma_f32_16x16x32_bf16 a[220:223],v[116:119],v[156:159],a[220:223]
s_add_u32 s36,s12,s66
s_addc_u32 s37,s13,0
ds_read_b64_tr_b16 v[116:117],v2 offset:10400
v_mfma_f32_16x16x32_bf16 a[220:223],v[112:115],v[140:143],a[220:223]
s_and_b32 s37,s37,0xffff
ds_read_b64_tr_b16 v[112:113],v2 offset:160
v_cndmask_b32 v48,0,v56,s[56:57]
v_mfma_f32_16x16x32_bf16 a[224:227],v[64:67],v[32:35],a[224:227]
s_mul_i32 s66,s61,307200
ds_read_b64_tr_b16 v[114:115],v2 offset:5280
ds_read_b64_tr_b16 v[118:119],v2 offset:15520
v_mfma_f32_16x16x32_bf16 a[224:227],v[68:71],v[36:39],a[224:227]
ds_read_b64_tr_b16 v[122:123],v2 offset:25760
v_cndmask_b32 v49,v56,0,s[56:57]
v_mfma_f32_16x16x32_bf16 a[224:227],v[72:75],v[40:43],a[224:227]
s_waitcnt lgkmcnt(8)
v_mov_b32 v58,s52
v_mov_b32 v59,s53
v_mfma_f32_16x16x32_bf16 a[224:227],v[76:79],v[44:47],a[224:227]
s_add_u32 s24,s16,s66
s_addc_u32 s25,s17,0
v_lshlrev_b32 v60,20,v6
v_mfma_f32_16x16x32_bf16 a[228:231],v[76:79],v[180:183],a[228:231]
buffer_load_dwordx4 a[136:139],v6,s[28:31],s87 offen offset:2048 sc1
v_mfma_f32_16x16x32_bf16 a[228:231],v[72:75],v[164:167],a[228:231]
s_mul_i32 s66,s60,16384
s_add_u32 s66,s66,s67
s_add_u32 s79,s66,12288
v_mfma_f32_16x16x32_bf16 a[228:231],v[68:71],v[148:151],a[228:231]
buffer_store_dwordx4 v[156:159],v28,s[40:43],s80 offen offset:3072 sc1
v_mfma_f32_16x16x32_bf16 a[228:231],v[64:67],v[132:135],a[228:231]
s_add_u32 s80,s66,77824
v_bfe_u32 v61,v6,4,1
v_add3_u32 v61,v61,s63,2
v_mfma_f32_16x16x32_bf16 a[232:235],v[64:67],v[196:199],a[232:235]
s_and_b32 s25,s25,0xffff
v_and_b32 v61,3,v61
v_lshlrev_b32 v61,8,v61
v_mfma_f32_16x16x32_bf16 a[232:235],v[68:71],v[212:215],a[232:235]
s_add_u32 s67,s77,8192
v_mov_b32 v50,0
v_mov_b32 v51,0
v_mfma_f32_16x16x32_bf16 a[232:235],v[72:75],v[228:231],a[232:235]
v_mov_b32 v52,0
v_mov_b32 v53,0
v_mfma_f32_16x16x32_bf16 a[232:235],v[76:79],v[244:247],a[232:235]
v_mov_b32 v54,v48
v_mov_b32 v55,v49
v_mfma_f32_16x16x32_bf16 a[236:239],v[76:79],v[188:191],a[236:239]
ds_read_b64_tr_b16 v[76:77],v1 offset:30912
ds_read_b64_tr_b16 v[78:79],v1 offset:36032
v_mfma_f32_16x16x32_bf16 a[236:239],v[72:75],v[172:175],a[236:239]
ds_read_b64_tr_b16 v[72:73],v1 offset:20672
v_bfe_u32 v56,v6,4,4
v_mfma_f32_16x16x32_bf16 a[236:239],v[68:71],v[156:159],a[236:239]
ds_read_b64_tr_b16 v[68:69],v1 offset:10432
v_mul_u32_u24 v56,1280,v56
v_mfma_f32_16x16x32_bf16 a[236:239],v[64:67],v[140:143],a[236:239]
ds_read_b64_tr_b16 v[64:65],v1 offset:192
v_bfe_u32 v57,v6,8,1
v_mfma_f32_16x16x32_bf16 a[240:243],v[80:83],v[32:35],a[240:243]
ds_read_b64_tr_b16 v[66:67],v1 offset:5312
ds_read_b64_tr_b16 v[70:71],v1 offset:15552
v_mfma_f32_16x16x32_bf16 a[240:243],v[84:87],v[36:39],a[240:243]
buffer_load_dwordx4 a[152:155],v6,s[28:31],s88 offen offset:2048 sc1
v_mfma_f32_16x16x32_bf16 a[240:243],v[88:91],v[40:43],a[240:243]
ds_read_b64_tr_b16 v[74:75],v1 offset:25792
v_lshl_add_u32 v56,v57,5,v56
v_mfma_f32_16x16x32_bf16 a[240:243],v[92:95],v[44:47],a[240:243]
v_lshrrev_b32 v57,9,v6
v_lshl_add_u32 v56,v57,4,v56
v_mfma_f32_16x16x32_bf16 a[244:247],v[92:95],v[180:183],a[244:247]
s_cmp_eq_u32 s69,3
s_cselect_b32 s69,0x40000000,0
s_add_u32 s68,s68,s69
v_mfma_f32_16x16x32_bf16 a[244:247],v[88:91],v[164:167],a[244:247]
buffer_store_dwordx4 v[172:175],v28,s[40:43],s81 offen offset:3072 sc1
v_mfma_f32_16x16x32_bf16 a[244:247],v[84:87],v[148:151],a[244:247]
s_add_u32 s81,s66,143360
v_mfma_f32_16x16x32_bf16 a[244:247],v[80:83],v[132:135],a[244:247]
v_mfma_f32_16x16x32_bf16 a[248:251],v[80:83],v[196:199],a[248:251]
v_mfma_f32_16x16x32_bf16 a[248:251],v[84:87],v[212:215],a[248:251]
v_mfma_f32_16x16x32_bf16 a[248:251],v[88:91],v[228:231],a[248:251]
v_mfma_f32_16x16x32_bf16 a[248:251],v[92:95],v[244:247],a[248:251]
v_mfma_f32_16x16x32_bf16 a[252:255],v[92:95],v[188:191],a[252:255]
ds_read_b64_tr_b16 v[92:93],v2 offset:30944
ds_read_b64_tr_b16 v[94:95],v2 offset:36064
v_mfma_f32_16x16x32_bf16 a[252:255],v[88:91],v[172:175],a[252:255]
ds_read_b64_tr_b16 v[88:89],v2 offset:20704
v_mfma_f32_16x16x32_bf16 a[252:255],v[84:87],v[156:159],a[252:255]
buffer_load_dwordx4 a[168:171],v6,s[28:31],s89 offen offset:2048 sc1
v_mfma_f32_16x16x32_bf16 a[252:255],v[80:83],v[140:143],a[252:255]
ds_read_b64_tr_b16 v[80:81],v2 offset:224
ds_read_b64_tr_b16 v[82:83],v2 offset:5344
v_mfma_f32_16x16x32_bf16 a[0:3],v[96:99],v[32:35],a[0:3]
ds_read_b64_tr_b16 v[84:85],v2 offset:10464
v_mfma_f32_16x16x32_bf16 a[0:3],v[100:103],v[36:39],a[0:3]
ds_read_b64_tr_b16 v[86:87],v2 offset:15584
v_mfma_f32_16x16x32_bf16 a[0:3],v[104:107],v[40:43],a[0:3]
ds_read_b64_tr_b16 v[90:91],v2 offset:25824
v_mfma_f32_16x16x32_bf16 a[0:3],v[108:111],v[44:47],a[0:3]
v_mfma_f32_16x16x32_bf16 a[4:7],v[108:111],v[180:183],a[4:7]
buffer_store_dwordx4 v[188:191],v28,s[40:43],s82 offen offset:3072 sc1
v_mfma_f32_16x16x32_bf16 a[4:7],v[104:107],v[164:167],a[4:7]
s_add_u32 s82,s66,208896
s_mul_i32 s66,s64,320
v_mfma_f32_16x16x32_bf16 a[4:7],v[100:103],v[148:151],a[4:7]
v_mfma_f32_16x16x32_bf16 a[4:7],v[96:99],v[132:135],a[4:7]
v_mfma_f32_16x16x32_bf16 a[8:11],v[96:99],v[196:199],a[8:11]
v_mfma_f32_16x16x32_bf16 a[8:11],v[100:103],v[212:215],a[8:11]
v_mfma_f32_16x16x32_bf16 a[8:11],v[104:107],v[228:231],a[8:11]
v_mfma_f32_16x16x32_bf16 a[8:11],v[108:111],v[244:247],a[8:11]
buffer_load_dwordx4 a[184:187],v6,s[28:31],s90 offen offset:2048 sc1
v_mfma_f32_16x16x32_bf16 a[12:15],v[108:111],v[188:191],a[12:15]
)A"
R"A(ds_read_b64_tr_b16 v[108:109],v1 offset:30976
ds_read_b64_tr_b16 v[110:111],v1 offset:36096
v_mfma_f32_16x16x32_bf16 a[12:15],v[104:107],v[172:175],a[12:15]
ds_read_b64_tr_b16 v[104:105],v1 offset:20736
v_mfma_f32_16x16x32_bf16 a[12:15],v[100:103],v[156:159],a[12:15]
ds_read_b64_tr_b16 v[100:101],v1 offset:10496
v_mfma_f32_16x16x32_bf16 a[12:15],v[96:99],v[140:143],a[12:15]
s_waitcnt lgkmcnt(4)
ds_read_b64_tr_b16 v[96:97],v1 offset:256
v_mfma_f32_16x16x32_bf16 a[16:19],v[112:115],v[32:35],a[16:19]
ds_read_b64_tr_b16 v[98:99],v1 offset:5376
ds_read_b64_tr_b16 v[102:103],v1 offset:15616
v_mfma_f32_16x16x32_bf16 a[16:19],v[116:119],v[36:39],a[16:19]
ds_read_b64_tr_b16 v[106:107],v1 offset:25856
v_add_u32 v1,0xffff6000,v1
v_mfma_f32_16x16x32_bf16 a[16:19],v[120:123],v[40:43],a[16:19]
v_mfma_f32_16x16x32_bf16 a[16:19],v[124:127],v[44:47],a[16:19]
v_mfma_f32_16x16x32_bf16 a[20:23],v[124:127],v[180:183],a[20:23]
v_mfma_f32_16x16x32_bf16 a[20:23],v[120:123],v[164:167],a[20:23]
v_mfma_f32_16x16x32_bf16 a[20:23],v[116:119],v[148:151],a[20:23]
v_mfma_f32_16x16x32_bf16 a[20:23],v[112:115],v[132:135],a[20:23]
v_mfma_f32_16x16x32_bf16 a[24:27],v[112:115],v[196:199],a[24:27]
buffer_load_dwordx4 a[188:191],v28,s[28:31],s90 offen offset:3072 sc1
v_mfma_f32_16x16x32_bf16 a[24:27],v[116:119],v[212:215],a[24:27]
v_mfma_f32_16x16x32_bf16 a[24:27],v[120:123],v[228:231],a[24:27]
v_mfma_f32_16x16x32_bf16 a[24:27],v[124:127],v[244:247],a[24:27]
v_mfma_f32_16x16x32_bf16 a[28:31],v[124:127],v[188:191],a[28:31]
ds_read_b64_tr_b16 v[124:125],v2 offset:31008
ds_read_b64_tr_b16 v[126:127],v2 offset:36128
v_mfma_f32_16x16x32_bf16 a[28:31],v[120:123],v[172:175],a[28:31]
ds_read_b64_tr_b16 v[120:121],v2 offset:20768
v_mfma_f32_16x16x32_bf16 a[28:31],v[116:119],v[156:159],a[28:31]
ds_read_b64_tr_b16 v[116:117],v2 offset:10528
v_mfma_f32_16x16x32_bf16 a[28:31],v[112:115],v[140:143],a[28:31]
ds_read_b64_tr_b16 v[112:113],v2 offset:288
v_mfma_f32_16x16x32_bf16 v[128:131],v[64:67],v[32:35],v[128:131]
ds_read_b64_tr_b16 v[114:115],v2 offset:5408
ds_read_b64_tr_b16 v[118:119],v2 offset:15648
v_mfma_f32_16x16x32_bf16 v[128:131],v[68:71],v[36:39],v[128:131]
ds_read_b64_tr_b16 v[122:123],v2 offset:25888
v_add_u32 v2,0xffff6000,v2
v_mfma_f32_16x16x32_bf16 v[128:131],v[72:75],v[40:43],v[128:131]
v_mfma_f32_16x16x32_bf16 v[128:131],v[76:79],v[44:47],v[128:131]
v_mfma_f32_16x16x32_bf16 v[136:139],v[76:79],v[180:183],v[136:139]
v_mfma_f32_16x16x32_bf16 v[136:139],v[72:75],v[164:167],v[136:139]
buffer_load_dwordx4 a[172:175],v28,s[28:31],s89 offen offset:3072 sc1
v_mfma_f32_16x16x32_bf16 v[136:139],v[68:71],v[148:151],v[136:139]
v_mfma_f32_16x16x32_bf16 v[136:139],v[64:67],v[132:135],v[136:139]
v_mfma_f32_16x16x32_bf16 v[144:147],v[64:67],v[196:199],v[144:147]
v_mfma_f32_16x16x32_bf16 v[144:147],v[68:71],v[212:215],v[144:147]
v_mfma_f32_16x16x32_bf16 v[144:147],v[72:75],v[228:231],v[144:147]
v_mfma_f32_16x16x32_bf16 v[144:147],v[76:79],v[244:247],v[144:147]
v_mfma_f32_16x16x32_bf16 v[152:155],v[76:79],v[188:191],v[152:155]
v_mfma_f32_16x16x32_bf16 v[152:155],v[72:75],v[172:175],v[152:155]
v_mfma_f32_16x16x32_bf16 v[152:155],v[68:71],v[156:159],v[152:155]
v_mfma_f32_16x16x32_bf16 v[152:155],v[64:67],v[140:143],v[152:155]
s_mov_b64 exec,0
s_cmp_eq_u32 s60,0
s_cbranch_scc1 L_pd21_%=
s_cmp_eq_u32 s60,1
s_cbranch_scc1 L_pd22_%=
s_cmp_eq_u32 s60,2
s_cbranch_scc1 L_pd23_%=
s_branch L_trtg2S_%=
L_trtg2S_%=:
s_mov_b64 exec,-1
s_waitcnt lgkmcnt(0)
s_barrier
v_mfma_f32_16x16x32_bf16 v[160:163],v[80:83],v[32:35],v[160:163]
buffer_load_dwordx4 v24,s[48:51],s83 offen lds
v_mfma_f32_16x16x32_bf16 v[160:163],v[84:87],v[36:39],v[160:163]
ds_read_b64_tr_b16 v[64:65],v1
ds_read_b64_tr_b16 v[66:67],v1 offset:5120
v_mfma_f32_16x16x32_bf16 v[160:163],v[88:91],v[40:43],v[160:163]
ds_read_b64_tr_b16 v[68:69],v1 offset:10240
v_mfma_f32_16x16x32_bf16 v[160:163],v[92:95],v[44:47],v[160:163]
ds_read_b64_tr_b16 v[70:71],v1 offset:15360
v_mfma_f32_16x16x32_bf16 v[168:171],v[92:95],v[180:183],v[168:171]
ds_read_b64_tr_b16 v[72:73],v1 offset:20480
v_mfma_f32_16x16x32_bf16 v[168:171],v[88:91],v[164:167],v[168:171]
buffer_load_dwordx4 a[156:159],v28,s[28:31],s88 offen offset:3072 sc1
v_mfma_f32_16x16x32_bf16 v[168:171],v[84:87],v[148:151],v[168:171]
ds_read_b64_tr_b16 v[74:75],v1 offset:25600
ds_read_b64_tr_b16 v[76:77],v1 offset:30720
v_mfma_f32_16x16x32_bf16 v[168:171],v[80:83],v[132:135],v[168:171]
ds_read_b64_tr_b16 v[78:79],v1 offset:35840
v_mfma_f32_16x16x32_bf16 v[176:179],v[80:83],v[196:199],v[176:179]
v_mfma_f32_16x16x32_bf16 v[176:179],v[84:87],v[212:215],v[176:179]
v_mfma_f32_16x16x32_bf16 v[176:179],v[88:91],v[228:231],v[176:179]
v_mfma_f32_16x16x32_bf16 v[176:179],v[92:95],v[244:247],v[176:179]
buffer_load_dwordx4 v25,s[48:51],s83 offen offset:1024 lds
v_mfma_f32_16x16x32_bf16 v[184:187],v[92:95],v[188:191],v[184:187]
ds_read_b64_tr_b16 v[92:93],v2 offset:30752
ds_read_b64_tr_b16 v[94:95],v2 offset:35872
v_mfma_f32_16x16x32_bf16 v[184:187],v[88:91],v[172:175],v[184:187]
ds_read_b64_tr_b16 v[88:89],v2 offset:20512
v_mfma_f32_16x16x32_bf16 v[184:187],v[84:87],v[156:159],v[184:187]
ds_read_b64_tr_b16 v[84:85],v2 offset:10272
v_mfma_f32_16x16x32_bf16 v[184:187],v[80:83],v[140:143],v[184:187]
ds_read_b64_tr_b16 v[80:81],v2 offset:32
v_mfma_f32_16x16x32_bf16 v[192:195],v[96:99],v[32:35],v[192:195]
buffer_load_dwordx4 a[140:143],v28,s[28:31],s87 offen offset:3072 sc1
v_mfma_f32_16x16x32_bf16 v[192:195],v[100:103],v[36:39],v[192:195]
s_add_u32 s87,s64,2
s_and_b32 s87,s87,3
s_lshl_b32 s87,s87,1
v_mfma_f32_16x16x32_bf16 v[192:195],v[104:107],v[40:43],v[192:195]
s_lshl_b32 s87,s87,16
s_add_u32 s88,s87,4096
s_add_u32 s89,s87,8192
v_mfma_f32_16x16x32_bf16 v[192:195],v[108:111],v[44:47],v[192:195]
s_add_u32 s90,s87,12288
ds_read_b64_tr_b16 v[82:83],v2 offset:5152
ds_read_b64_tr_b16 v[86:87],v2 offset:15392
v_mfma_f32_16x16x32_bf16 v[200:203],v[108:111],v[180:183],v[200:203]
ds_read_b64_tr_b16 v[90:91],v2 offset:25632
v_mfma_f32_16x16x32_bf16 v[200:203],v[104:107],v[164:167],v[200:203]
buffer_load_dwordx4 a[68:71],v6,s[28:31],s87 offen sc1
v_mfma_f32_16x16x32_bf16 v[200:203],v[100:103],v[148:151],v[200:203]
v_mfma_f32_16x16x32_bf16 v[200:203],v[96:99],v[132:135],v[200:203]
v_mfma_f32_16x16x32_bf16 v[208:211],v[96:99],v[196:199],v[208:211]
v_mfma_f32_16x16x32_bf16 v[208:211],v[100:103],v[212:215],v[208:211]
v_mfma_f32_16x16x32_bf16 v[208:211],v[104:107],v[228:231],v[208:211]
buffer_load_dwordx4 a[76:79],v6,s[28:31],s88 offen sc1
v_mfma_f32_16x16x32_bf16 v[208:211],v[108:111],v[244:247],v[208:211]
v_mfma_f32_16x16x32_bf16 v[216:219],v[108:111],v[188:191],v[216:219]
ds_read_b64_tr_b16 v[108:109],v1 offset:30784
ds_read_b64_tr_b16 v[110:111],v1 offset:35904
v_mfma_f32_16x16x32_bf16 v[216:219],v[104:107],v[172:175],v[216:219]
ds_read_b64_tr_b16 v[104:105],v1 offset:20544
v_mfma_f32_16x16x32_bf16 v[216:219],v[100:103],v[156:159],v[216:219]
ds_read_b64_tr_b16 v[100:101],v1 offset:10304
v_mfma_f32_16x16x32_bf16 v[216:219],v[96:99],v[140:143],v[216:219]
buffer_load_dwordx4 a[84:87],v6,s[28:31],s89 offen sc1
v_mfma_f32_16x16x32_bf16 v[224:227],v[112:115],v[32:35],v[224:227]
ds_read_b64_tr_b16 v[96:97],v1 offset:64
ds_read_b64_tr_b16 v[98:99],v1 offset:5184
v_mfma_f32_16x16x32_bf16 v[224:227],v[116:119],v[36:39],v[224:227]
ds_read_b64_tr_b16 v[102:103],v1 offset:15424
v_mfma_f32_16x16x32_bf16 v[224:227],v[120:123],v[40:43],v[224:227]
ds_read_b64_tr_b16 v[106:107],v1 offset:25664
v_mfma_f32_16x16x32_bf16 v[224:227],v[124:127],v[44:47],v[224:227]
v_mfma_f32_16x16x32_bf16 v[232:235],v[124:127],v[180:183],v[232:235]
buffer_load_dwordx4 a[92:95],v6,s[28:31],s90 offen sc1
v_mfma_f32_16x16x32_bf16 v[232:235],v[120:123],v[164:167],v[232:235]
v_mfma_f32_16x16x32_bf16 v[232:235],v[116:119],v[148:151],v[232:235]
v_mfma_f32_16x16x32_bf16 v[232:235],v[112:115],v[132:135],v[232:235]
v_mfma_f32_16x16x32_bf16 v[240:243],v[112:115],v[196:199],v[240:243]
v_mfma_f32_16x16x32_bf16 v[240:243],v[116:119],v[212:215],v[240:243]
buffer_load_dwordx4 a[124:127],v6,s[28:31],s90 offen offset:1024 sc1
v_mfma_f32_16x16x32_bf16 v[240:243],v[120:123],v[228:231],v[240:243]
v_mfma_f32_16x16x32_bf16 v[240:243],v[124:127],v[244:247],v[240:243]
v_mfma_f32_16x16x32_bf16 v[248:251],v[124:127],v[188:191],v[248:251]
ds_read_b64_tr_b16 v[124:125],v2 offset:30816
ds_read_b64_tr_b16 v[126:127],v2 offset:35936
v_mfma_f32_16x16x32_bf16 v[248:251],v[120:123],v[172:175],v[248:251]
ds_read_b64_tr_b16 v[120:121],v2 offset:20576
v_mfma_f32_16x16x32_bf16 v[248:251],v[116:119],v[156:159],v[248:251]
buffer_load_dwordx4 a[116:119],v6,s[28:31],s89 offen offset:1024 sc1
v_mfma_f32_16x16x32_bf16 v[248:251],v[112:115],v[140:143],v[248:251]
s_waitcnt vmcnt(31) lgkmcnt(3)
ds_read_b64_tr_b16 v[112:113],v2 offset:96
ds_read_b64_tr_b16 v[114:115],v2 offset:5216
v_mfma_f32_16x16x32_bf16 a[192:195],v[64:67],a[128:131],a[192:195]
ds_read_b64_tr_b16 v[116:117],v2 offset:10336
v_mfma_f32_16x16x32_bf16 a[192:195],v[68:71],a[144:147],a[192:195]
s_waitcnt vmcnt(29)
ds_read_b64_tr_b16 v[118:119],v2 offset:15456
v_mfma_f32_16x16x32_bf16 a[192:195],v[72:75],a[160:163],a[192:195]
s_waitcnt vmcnt(25)
ds_read_b64_tr_b16 v[122:123],v2 offset:25696
v_mfma_f32_16x16x32_bf16 a[192:195],v[76:79],a[176:179],a[192:195]
buffer_load_dwordx4 a[108:111],v6,s[28:31],s88 offen offset:1024 sc1
v_mfma_f32_16x16x32_bf16 a[196:199],v[76:79],a[180:183],a[196:199]
s_waitcnt vmcnt(24)
v_mfma_f32_16x16x32_bf16 a[196:199],v[72:75],a[164:167],a[196:199]
s_waitcnt vmcnt(22)
v_mfma_f32_16x16x32_bf16 a[196:199],v[68:71],a[148:151],a[196:199]
s_waitcnt vmcnt(19)
v_mfma_f32_16x16x32_bf16 a[196:199],v[64:67],a[132:135],a[196:199]
s_waitcnt vmcnt(17)
v_mfma_f32_16x16x32_bf16 a[200:203],v[64:67],a[136:139],a[200:203]
buffer_load_dwordx4 a[100:103],v6,s[28:31],s87 offen offset:1024 sc1
v_mfma_f32_16x16x32_bf16 a[200:203],v[68:71],a[152:155],a[200:203]
s_waitcnt vmcnt(16)
v_mfma_f32_16x16x32_bf16 a[200:203],v[72:75],a[168:171],a[200:203]
s_waitcnt vmcnt(14)
v_mfma_f32_16x16x32_bf16 a[200:203],v[76:79],a[184:187],a[200:203]
s_waitcnt vmcnt(13)
v_mfma_f32_16x16x32_bf16 a[204:207],v[76:79],a[188:191],a[204:207]
s_waitcnt vmcnt(10)
ds_read_b64_tr_b16 v[76:77],v1 offset:30848
ds_read_b64_tr_b16 v[78:79],v1 offset:35968
v_mfma_f32_16x16x32_bf16 a[204:207],v[72:75],a[172:175],a[204:207]
buffer_load_dwordx4 a[64:67],v6,s[28:31],s87 offen offset:2048 sc1
v_mfma_f32_16x16x32_bf16 a[204:207],v[68:71],a[156:159],a[204:207]
s_waitcnt vmcnt(9)
ds_read_b64_tr_b16 v[68:69],v1 offset:10368
ds_read_b64_tr_b16 v[70:71],v1 offset:15488
v_mfma_f32_16x16x32_bf16 a[204:207],v[64:67],a[140:143],a[204:207]
ds_read_b64_tr_b16 v[64:65],v1 offset:128
v_mfma_f32_16x16x32_bf16 a[208:211],v[80:83],a[128:131],a[208:211]
ds_read_b64_tr_b16 v[66:67],v1 offset:5248
v_mfma_f32_16x16x32_bf16 a[208:211],v[84:87],a[144:147],a[208:211]
ds_read_b64_tr_b16 v[72:73],v1 offset:20608
v_mfma_f32_16x16x32_bf16 a[208:211],v[88:91],a[160:163],a[208:211]
ds_read_b64_tr_b16 v[74:75],v1 offset:25728
v_mfma_f32_16x16x32_bf16 a[208:211],v[92:95],a[176:179],a[208:211]
buffer_load_dwordx4 v26,s[48:51],s83 offen offset:2048 lds
v_mfma_f32_16x16x32_bf16 a[212:215],v[92:95],a[180:183],a[212:215]
v_mfma_f32_16x16x32_bf16 a[212:215],v[88:91],a[164:167],a[212:215]
v_mfma_f32_16x16x32_bf16 a[212:215],v[84:87],a[148:151],a[212:215]
v_mfma_f32_16x16x32_bf16 a[212:215],v[80:83],a[132:135],a[212:215]
v_mfma_f32_16x16x32_bf16 a[216:219],v[80:83],a[136:139],a[216:219]
buffer_load_dwordx4 a[72:75],v6,s[28:31],s88 offen offset:2048 sc1
v_mfma_f32_16x16x32_bf16 a[216:219],v[84:87],a[152:155],a[216:219]
v_mfma_f32_16x16x32_bf16 a[216:219],v[88:91],a[168:171],a[216:219]
v_mfma_f32_16x16x32_bf16 a[216:219],v[92:95],a[184:187],a[216:219]
v_mfma_f32_16x16x32_bf16 a[220:223],v[92:95],a[188:191],a[220:223]
ds_read_b64_tr_b16 v[92:93],v2 offset:30880
ds_read_b64_tr_b16 v[94:95],v2 offset:36000
v_mfma_f32_16x16x32_bf16 a[220:223],v[88:91],a[172:175],a[220:223]
ds_read_b64_tr_b16 v[88:89],v2 offset:20640
v_mfma_f32_16x16x32_bf16 a[220:223],v[84:87],a[156:159],a[220:223]
)A"
R"A(buffer_load_dwordx4 v27,s[48:51],s83 offen offset:3072 lds
v_mfma_f32_16x16x32_bf16 a[220:223],v[80:83],a[140:143],a[220:223]
s_add_u32 m0,s86,126976
ds_read_b64_tr_b16 v[80:81],v2 offset:160
ds_read_b64_tr_b16 v[82:83],v2 offset:5280
s_barrier
v_mfma_f32_16x16x32_bf16 a[224:227],v[96:99],a[128:131],a[224:227]
ds_read_b64_tr_b16 v[84:85],v2 offset:10400
v_mfma_f32_16x16x32_bf16 a[224:227],v[100:103],a[144:147],a[224:227]
ds_read_b64_tr_b16 v[86:87],v2 offset:15520
v_mfma_f32_16x16x32_bf16 a[224:227],v[104:107],a[160:163],a[224:227]
buffer_store_dwordx2 v[8:9],v10,s[44:47],s18 offen offset:1280 sc1
v_mfma_f32_16x16x32_bf16 a[224:227],v[108:111],a[176:179],a[224:227]
ds_read_b64_tr_b16 v[90:91],v2 offset:25760
v_mfma_f32_16x16x32_bf16 a[228:231],v[108:111],a[180:183],a[228:231]
v_mfma_f32_16x16x32_bf16 a[228:231],v[104:107],a[164:167],a[228:231]
v_mfma_f32_16x16x32_bf16 a[228:231],v[100:103],a[148:151],a[228:231]
v_mfma_f32_16x16x32_bf16 a[228:231],v[96:99],a[132:135],a[228:231]
buffer_load_dwordx4 a[80:83],v6,s[28:31],s89 offen offset:2048 sc1
v_mfma_f32_16x16x32_bf16 a[232:235],v[96:99],a[136:139],a[232:235]
v_mfma_f32_16x16x32_bf16 a[232:235],v[100:103],a[152:155],a[232:235]
v_mfma_f32_16x16x32_bf16 a[232:235],v[104:107],a[168:171],a[232:235]
v_mfma_f32_16x16x32_bf16 a[232:235],v[108:111],a[184:187],a[232:235]
v_mfma_f32_16x16x32_bf16 a[236:239],v[108:111],a[188:191],a[236:239]
s_waitcnt lgkmcnt(0)
ds_read_b64_tr_b16 v[108:109],v1 offset:30912
ds_read_b64_tr_b16 v[110:111],v1 offset:36032
v_mfma_f32_16x16x32_bf16 a[236:239],v[104:107],a[172:175],a[236:239]
buffer_load_dwordx4 v7,s[48:51],s83 offen lds
v_mfma_f32_16x16x32_bf16 a[236:239],v[100:103],a[156:159],a[236:239]
s_add_u32 m0,s86,128000
ds_read_b64_tr_b16 v[100:101],v1 offset:10432
ds_read_b64_tr_b16 v[102:103],v1 offset:15552
v_mfma_f32_16x16x32_bf16 a[236:239],v[96:99],a[140:143],a[236:239]
s_add_u32 s83,s64,1
s_and_b32 s83,s83,3
ds_read_b64_tr_b16 v[96:97],v1 offset:192
v_mfma_f32_16x16x32_bf16 a[240:243],v[112:115],a[128:131],a[240:243]
s_lshl_b32 s83,s83,1
s_or_b32 s83,s83,1
ds_read_b64_tr_b16 v[98:99],v1 offset:5312
v_mfma_f32_16x16x32_bf16 a[240:243],v[116:119],a[144:147],a[240:243]
s_mul_i32 s83,s83,163840
s_add_u32 s83,s83,s85
ds_read_b64_tr_b16 v[104:105],v1 offset:20672
v_mfma_f32_16x16x32_bf16 a[240:243],v[120:123],a[160:163],a[240:243]
buffer_load_dwordx4 a[88:91],v6,s[28:31],s90 offen offset:2048 sc1
v_mfma_f32_16x16x32_bf16 a[240:243],v[124:127],a[176:179],a[240:243]
ds_read_b64_tr_b16 v[106:107],v1 offset:25792
v_mfma_f32_16x16x32_bf16 a[244:247],v[124:127],a[180:183],a[244:247]
v_mfma_f32_16x16x32_bf16 a[244:247],v[120:123],a[164:167],a[244:247]
v_mfma_f32_16x16x32_bf16 a[244:247],v[116:119],a[148:151],a[244:247]
v_mfma_f32_16x16x32_bf16 a[244:247],v[112:115],a[132:135],a[244:247]
v_mfma_f32_16x16x32_bf16 a[248:251],v[112:115],a[136:139],a[248:251]
buffer_load_dwordx4 v24,s[48:51],s84 offen lds
v_mfma_f32_16x16x32_bf16 a[248:251],v[116:119],a[152:155],a[248:251]
v_mfma_f32_16x16x32_bf16 a[248:251],v[120:123],a[168:171],a[248:251]
v_mfma_f32_16x16x32_bf16 a[248:251],v[124:127],a[184:187],a[248:251]
v_mfma_f32_16x16x32_bf16 a[252:255],v[124:127],a[188:191],a[252:255]
ds_read_b64_tr_b16 v[124:125],v2 offset:30944
ds_read_b64_tr_b16 v[126:127],v2 offset:36064
v_mfma_f32_16x16x32_bf16 a[252:255],v[120:123],a[172:175],a[252:255]
buffer_load_dwordx4 a[120:123],v28,s[28:31],s90 offen offset:3072 sc1
v_mfma_f32_16x16x32_bf16 a[252:255],v[116:119],a[156:159],a[252:255]
ds_read_b64_tr_b16 v[116:117],v2 offset:10464
ds_read_b64_tr_b16 v[118:119],v2 offset:15584
v_mfma_f32_16x16x32_bf16 a[252:255],v[112:115],a[140:143],a[252:255]
ds_read_b64_tr_b16 v[112:113],v2 offset:224
v_mfma_f32_16x16x32_bf16 a[0:3],v[64:67],a[128:131],a[0:3]
ds_read_b64_tr_b16 v[114:115],v2 offset:5344
v_mfma_f32_16x16x32_bf16 a[0:3],v[68:71],a[144:147],a[0:3]
ds_read_b64_tr_b16 v[120:121],v2 offset:20704
v_mfma_f32_16x16x32_bf16 a[0:3],v[72:75],a[160:163],a[0:3]
ds_read_b64_tr_b16 v[122:123],v2 offset:25824
v_mfma_f32_16x16x32_bf16 a[0:3],v[76:79],a[176:179],a[0:3]
buffer_load_dwordx4 v25,s[48:51],s84 offen offset:1024 lds
v_mfma_f32_16x16x32_bf16 a[4:7],v[76:79],a[180:183],a[4:7]
v_mfma_f32_16x16x32_bf16 a[4:7],v[72:75],a[164:167],a[4:7]
v_mfma_f32_16x16x32_bf16 a[4:7],v[68:71],a[148:151],a[4:7]
v_mfma_f32_16x16x32_bf16 a[4:7],v[64:67],a[132:135],a[4:7]
v_mfma_f32_16x16x32_bf16 a[8:11],v[64:67],a[136:139],a[8:11]
buffer_load_dwordx4 a[112:115],v28,s[28:31],s89 offen offset:3072 sc1
v_mfma_f32_16x16x32_bf16 a[8:11],v[68:71],a[152:155],a[8:11]
v_mfma_f32_16x16x32_bf16 a[8:11],v[72:75],a[168:171],a[8:11]
v_mfma_f32_16x16x32_bf16 a[8:11],v[76:79],a[184:187],a[8:11]
v_mfma_f32_16x16x32_bf16 a[12:15],v[76:79],a[188:191],a[12:15]
ds_read_b64_tr_b16 v[76:77],v1 offset:30976
ds_read_b64_tr_b16 v[78:79],v1 offset:36096
v_mfma_f32_16x16x32_bf16 a[12:15],v[72:75],a[172:175],a[12:15]
buffer_load_dwordx4 a[104:107],v28,s[28:31],s88 offen offset:3072 sc1
v_mfma_f32_16x16x32_bf16 a[12:15],v[68:71],a[156:159],a[12:15]
ds_read_b64_tr_b16 v[68:69],v1 offset:10496
ds_read_b64_tr_b16 v[70:71],v1 offset:15616
v_mfma_f32_16x16x32_bf16 a[12:15],v[64:67],a[140:143],a[12:15]
ds_read_b64_tr_b16 v[64:65],v1 offset:256
v_mfma_f32_16x16x32_bf16 a[16:19],v[80:83],a[128:131],a[16:19]
ds_read_b64_tr_b16 v[66:67],v1 offset:5376
v_mfma_f32_16x16x32_bf16 a[16:19],v[84:87],a[144:147],a[16:19]
ds_read_b64_tr_b16 v[72:73],v1 offset:20736
v_mfma_f32_16x16x32_bf16 a[16:19],v[88:91],a[160:163],a[16:19]
ds_read_b64_tr_b16 v[74:75],v1 offset:25856
v_add_u32 v1,0xffff6000,v1
v_mfma_f32_16x16x32_bf16 a[16:19],v[92:95],a[176:179],a[16:19]
buffer_load_dwordx4 v26,s[48:51],s84 offen offset:2048 lds
v_mfma_f32_16x16x32_bf16 a[20:23],v[92:95],a[180:183],a[20:23]
v_mfma_f32_16x16x32_bf16 a[20:23],v[88:91],a[164:167],a[20:23]
v_mfma_f32_16x16x32_bf16 a[20:23],v[84:87],a[148:151],a[20:23]
v_mfma_f32_16x16x32_bf16 a[20:23],v[80:83],a[132:135],a[20:23]
v_mfma_f32_16x16x32_bf16 a[24:27],v[80:83],a[136:139],a[24:27]
buffer_load_dwordx4 a[96:99],v28,s[28:31],s87 offen offset:3072 sc1
v_mfma_f32_16x16x32_bf16 a[24:27],v[84:87],a[152:155],a[24:27]
s_add_u32 s87,s64,3
s_and_b32 s87,s87,3
s_lshl_b32 s87,s87,1
v_mfma_f32_16x16x32_bf16 a[24:27],v[88:91],a[168:171],a[24:27]
s_lshl_b32 s87,s87,16
s_add_u32 s88,s87,4096
s_add_u32 s89,s87,8192
v_mfma_f32_16x16x32_bf16 a[24:27],v[92:95],a[184:187],a[24:27]
s_add_u32 s90,s87,12288
v_mfma_f32_16x16x32_bf16 a[28:31],v[92:95],a[188:191],a[28:31]
ds_read_b64_tr_b16 v[92:93],v2 offset:31008
ds_read_b64_tr_b16 v[94:95],v2 offset:36128
v_mfma_f32_16x16x32_bf16 a[28:31],v[88:91],a[172:175],a[28:31]
s_waitcnt lgkmcnt(2)
ds_read_b64_tr_b16 v[88:89],v2 offset:20768
v_mfma_f32_16x16x32_bf16 a[28:31],v[84:87],a[156:159],a[28:31]
buffer_load_dwordx4 v27,s[48:51],s84 offen offset:3072 lds
v_mfma_f32_16x16x32_bf16 a[28:31],v[80:83],a[140:143],a[28:31]
s_add_u32 m0,s86,132096
ds_read_b64_tr_b16 v[80:81],v2 offset:288
ds_read_b64_tr_b16 v[82:83],v2 offset:5408
v_mfma_f32_16x16x32_bf16 v[128:131],v[96:99],a[128:131],v[128:131]
ds_read_b64_tr_b16 v[84:85],v2 offset:10528
v_mfma_f32_16x16x32_bf16 v[128:131],v[100:103],a[144:147],v[128:131]
ds_read_b64_tr_b16 v[86:87],v2 offset:15648
v_mfma_f32_16x16x32_bf16 v[128:131],v[104:107],a[160:163],v[128:131]
ds_read_b64_tr_b16 v[90:91],v2 offset:25888
v_add_u32 v2,0xffff6000,v2
v_mfma_f32_16x16x32_bf16 v[128:131],v[108:111],a[176:179],v[128:131]
v_mfma_f32_16x16x32_bf16 v[136:139],v[108:111],a[180:183],v[136:139]
buffer_load_dwordx4 v7,s[48:51],s84 offen lds
v_mfma_f32_16x16x32_bf16 v[136:139],v[104:107],a[164:167],v[136:139]
s_add_u32 m0,s86,81920
s_add_u32 s84,s83,20480
v_mfma_f32_16x16x32_bf16 v[136:139],v[100:103],a[148:151],v[136:139]
v_mfma_f32_16x16x32_bf16 v[136:139],v[96:99],a[132:135],v[136:139]
v_mfma_f32_16x16x32_bf16 v[144:147],v[96:99],a[136:139],v[144:147]
v_mfma_f32_16x16x32_bf16 v[144:147],v[100:103],a[152:155],v[144:147]
v_mfma_f32_16x16x32_bf16 v[144:147],v[104:107],a[168:171],v[144:147]
buffer_load_dwordx4 v24,s[48:51],s83 offen lds
v_mfma_f32_16x16x32_bf16 v[144:147],v[108:111],a[184:187],v[144:147]
v_mfma_f32_16x16x32_bf16 v[152:155],v[108:111],a[188:191],v[152:155]
ds_read_b64_tr_b16 v[108:109],v1 offset:30720
ds_read_b64_tr_b16 v[110:111],v1 offset:35840
v_mfma_f32_16x16x32_bf16 v[152:155],v[104:107],a[172:175],v[152:155]
ds_read_b64_tr_b16 v[104:105],v1 offset:20480
v_mfma_f32_16x16x32_bf16 v[152:155],v[100:103],a[156:159],v[152:155]
ds_read_b64_tr_b16 v[100:101],v1 offset:10240
v_mfma_f32_16x16x32_bf16 v[152:155],v[96:99],a[140:143],v[152:155]
ds_read_b64_tr_b16 v[96:97],v1
v_mfma_f32_16x16x32_bf16 v[160:163],v[112:115],a[128:131],v[160:163]
buffer_load_dwordx4 v25,s[48:51],s83 offen offset:1024 lds
v_mfma_f32_16x16x32_bf16 v[160:163],v[116:119],a[144:147],v[160:163]
ds_read_b64_tr_b16 v[98:99],v1 offset:5120
ds_read_b64_tr_b16 v[102:103],v1 offset:15360
v_mfma_f32_16x16x32_bf16 v[160:163],v[120:123],a[160:163],v[160:163]
ds_read_b64_tr_b16 v[106:107],v1 offset:25600
v_mfma_f32_16x16x32_bf16 v[160:163],v[124:127],a[176:179],v[160:163]
v_mfma_f32_16x16x32_bf16 v[168:171],v[124:127],a[180:183],v[168:171]
v_mfma_f32_16x16x32_bf16 v[168:171],v[120:123],a[164:167],v[168:171]
v_mfma_f32_16x16x32_bf16 v[168:171],v[116:119],a[148:151],v[168:171]
buffer_load_dwordx4 v26,s[48:51],s83 offen offset:2048 lds
v_mfma_f32_16x16x32_bf16 v[168:171],v[112:115],a[132:135],v[168:171]
v_mfma_f32_16x16x32_bf16 v[176:179],v[112:115],a[136:139],v[176:179]
v_mfma_f32_16x16x32_bf16 v[176:179],v[116:119],a[152:155],v[176:179]
v_mfma_f32_16x16x32_bf16 v[176:179],v[120:123],a[168:171],v[176:179]
v_mfma_f32_16x16x32_bf16 v[176:179],v[124:127],a[184:187],v[176:179]
v_mfma_f32_16x16x32_bf16 v[184:187],v[124:127],a[188:191],v[184:187]
buffer_load_dwordx4 v27,s[48:51],s83 offen offset:3072 lds
v_mfma_f32_16x16x32_bf16 v[184:187],v[120:123],a[172:175],v[184:187]
s_add_u32 m0,s86,86016
ds_read_b64_tr_b16 v[120:121],v2 offset:20512
ds_read_b64_tr_b16 v[122:123],v2 offset:25632
v_mfma_f32_16x16x32_bf16 v[184:187],v[116:119],a[156:159],v[184:187]
ds_read_b64_tr_b16 v[116:117],v2 offset:10272
v_mfma_f32_16x16x32_bf16 v[184:187],v[112:115],a[140:143],v[184:187]
ds_read_b64_tr_b16 v[112:113],v2 offset:32
L_pd21_%=:
s_cbranch_execz L_trtg2S_%=
v_mfma_f32_16x16x32_bf16 v[192:195],v[64:67],a[128:131],v[192:195]
ds_read_b64_tr_b16 v[114:115],v2 offset:5152
v_mfma_f32_16x16x32_bf16 v[192:195],v[68:71],a[144:147],v[192:195]
ds_read_b64_tr_b16 v[118:119],v2 offset:15392
ds_read_b64_tr_b16 v[124:125],v2 offset:30752
v_mfma_f32_16x16x32_bf16 v[192:195],v[72:75],a[160:163],v[192:195]
buffer_load_dwordx4 v7,s[48:51],s83 offen lds
v_mfma_f32_16x16x32_bf16 v[192:195],v[76:79],a[176:179],v[192:195]
s_add_u32 m0,s86,87040
s_add_u32 s83,s64,2
ds_read_b64_tr_b16 v[126:127],v2 offset:35872
v_mfma_f32_16x16x32_bf16 v[200:203],v[76:79],a[180:183],v[200:203]
s_and_b32 s83,s83,3
s_lshl_b32 s83,s83,1
s_or_b32 s83,s83,1
v_mfma_f32_16x16x32_bf16 v[200:203],v[72:75],a[164:167],v[200:203]
s_mul_i32 s83,s83,163840
s_add_u32 s83,s83,s85
v_mfma_f32_16x16x32_bf16 v[200:203],v[68:71],a[148:151],v[200:203]
v_mfma_f32_16x16x32_bf16 v[200:203],v[64:67],a[132:135],v[200:203]
v_mfma_f32_16x16x32_bf16 v[208:211],v[64:67],a[136:139],v[208:211]
buffer_load_dwordx4 v24,s[48:51],s84 offen lds
v_mfma_f32_16x16x32_bf16 v[208:211],v[68:71],a[152:155],v[208:211]
v_mfma_f32_16x16x32_bf16 v[208:211],v[72:75],a[168:171],v[208:211]
v_mfma_f32_16x16x32_bf16 v[208:211],v[76:79],a[184:187],v[208:211]
v_mfma_f32_16x16x32_bf16 v[216:219],v[76:79],a[188:191],v[216:219]
ds_read_b64_tr_b16 v[76:77],v1 offset:30784
ds_read_b64_tr_b16 v[78:79],v1 offset:35904
v_mfma_f32_16x16x32_bf16 v[216:219],v[72:75],a[172:175],v[216:219]
ds_read_b64_tr_b16 v[72:73],v1 offset:20544
v_mfma_f32_16x16x32_bf16 v[216:219],v[68:71],a[156:159],v[216:219]
buffer_load_dwordx2 v[4:5],v3,s[44:47],s91 offen offset:1280 sc1
v_mfma_f32_16x16x32_bf16 v[216:219],v[64:67],a[140:143],v[216:219]
s_waitcnt lgkmcnt(3)
ds_read_b64_tr_b16 v[64:65],v1 offset:64
)A"
R"A(ds_read_b64_tr_b16 v[66:67],v1 offset:5184
v_mfma_f32_16x16x32_bf16 v[224:227],v[80:83],a[128:131],v[224:227]
ds_read_b64_tr_b16 v[68:69],v1 offset:10304
v_mfma_f32_16x16x32_bf16 v[224:227],v[84:87],a[144:147],v[224:227]
ds_read_b64_tr_b16 v[70:71],v1 offset:15424
v_mfma_f32_16x16x32_bf16 v[224:227],v[88:91],a[160:163],v[224:227]
ds_read_b64_tr_b16 v[74:75],v1 offset:25664
v_mfma_f32_16x16x32_bf16 v[224:227],v[92:95],a[176:179],v[224:227]
v_mfma_f32_16x16x32_bf16 v[232:235],v[92:95],a[180:183],v[232:235]
buffer_load_dwordx4 v25,s[48:51],s84 offen offset:1024 lds
v_mfma_f32_16x16x32_bf16 v[232:235],v[88:91],a[164:167],v[232:235]
v_mfma_f32_16x16x32_bf16 v[232:235],v[84:87],a[148:151],v[232:235]
v_mfma_f32_16x16x32_bf16 v[232:235],v[80:83],a[132:135],v[232:235]
v_mfma_f32_16x16x32_bf16 v[240:243],v[80:83],a[136:139],v[240:243]
v_mfma_f32_16x16x32_bf16 v[240:243],v[84:87],a[152:155],v[240:243]
v_mfma_f32_16x16x32_bf16 v[240:243],v[88:91],a[168:171],v[240:243]
buffer_load_dwordx4 v26,s[48:51],s84 offen offset:2048 lds
v_mfma_f32_16x16x32_bf16 v[240:243],v[92:95],a[184:187],v[240:243]
v_mfma_f32_16x16x32_bf16 v[248:251],v[92:95],a[188:191],v[248:251]
ds_read_b64_tr_b16 v[92:93],v2 offset:30816
ds_read_b64_tr_b16 v[94:95],v2 offset:35936
v_mfma_f32_16x16x32_bf16 v[248:251],v[88:91],a[172:175],v[248:251]
ds_read_b64_tr_b16 v[88:89],v2 offset:20576
v_mfma_f32_16x16x32_bf16 v[248:251],v[84:87],a[156:159],v[248:251]
ds_read_b64_tr_b16 v[84:85],v2 offset:10336
v_mfma_f32_16x16x32_bf16 v[248:251],v[80:83],a[140:143],v[248:251]
s_waitcnt vmcnt(25)
ds_read_b64_tr_b16 v[80:81],v2 offset:96
v_mfma_f32_16x16x32_bf16 a[192:195],v[96:99],a[68:71],a[192:195]
buffer_load_dwordx4 v27,s[48:51],s84 offen offset:3072 lds
v_mfma_f32_16x16x32_bf16 a[192:195],v[100:103],a[76:79],a[192:195]
s_add_u32 m0,s86,91136
ds_read_b64_tr_b16 v[82:83],v2 offset:5216
ds_read_b64_tr_b16 v[86:87],v2 offset:15456
v_mfma_f32_16x16x32_bf16 a[192:195],v[104:107],a[84:87],a[192:195]
ds_read_b64_tr_b16 v[90:91],v2 offset:25696
v_mfma_f32_16x16x32_bf16 a[192:195],v[108:111],a[92:95],a[192:195]
v_mfma_f32_16x16x32_bf16 a[196:199],v[108:111],a[124:127],a[196:199]
v_mfma_f32_16x16x32_bf16 a[196:199],v[104:107],a[116:119],a[196:199]
v_mfma_f32_16x16x32_bf16 a[196:199],v[100:103],a[108:111],a[196:199]
buffer_load_dwordx4 v7,s[48:51],s84 offen lds
v_mfma_f32_16x16x32_bf16 a[196:199],v[96:99],a[100:103],a[196:199]
s_add_u32 m0,s86,40960
s_add_u32 s84,s83,20480
v_mfma_f32_16x16x32_bf16 a[200:203],v[96:99],a[64:67],a[200:203]
s_waitcnt vmcnt(25)
v_mfma_f32_16x16x32_bf16 a[200:203],v[100:103],a[72:75],a[200:203]
s_waitcnt vmcnt(22)
v_mfma_f32_16x16x32_bf16 a[200:203],v[104:107],a[80:83],a[200:203]
s_waitcnt vmcnt(18)
v_mfma_f32_16x16x32_bf16 a[200:203],v[108:111],a[88:91],a[200:203]
buffer_load_dwordx4 v[32:35],v6,s[28:31],s87 offen sc1
v_mfma_f32_16x16x32_bf16 a[204:207],v[108:111],a[120:123],a[204:207]
s_waitcnt vmcnt(17)
ds_read_b64_tr_b16 v[108:109],v1 offset:30848
ds_read_b64_tr_b16 v[110:111],v1 offset:35968
v_mfma_f32_16x16x32_bf16 a[204:207],v[104:107],a[112:115],a[204:207]
s_waitcnt vmcnt(16)
ds_read_b64_tr_b16 v[104:105],v1 offset:20608
v_mfma_f32_16x16x32_bf16 a[204:207],v[100:103],a[104:107],a[204:207]
s_waitcnt vmcnt(14)
ds_read_b64_tr_b16 v[100:101],v1 offset:10368
v_mfma_f32_16x16x32_bf16 a[204:207],v[96:99],a[96:99],a[204:207]
ds_read_b64_tr_b16 v[96:97],v1 offset:128
v_mfma_f32_16x16x32_bf16 a[208:211],v[112:115],a[68:71],a[208:211]
buffer_load_dwordx4 v[132:135],v6,s[28:31],s87 offen offset:1024 sc1
v_mfma_f32_16x16x32_bf16 a[208:211],v[116:119],a[76:79],a[208:211]
ds_read_b64_tr_b16 v[98:99],v1 offset:5248
ds_read_b64_tr_b16 v[102:103],v1 offset:15488
v_mfma_f32_16x16x32_bf16 a[208:211],v[120:123],a[84:87],a[208:211]
ds_read_b64_tr_b16 v[106:107],v1 offset:25728
v_mfma_f32_16x16x32_bf16 a[208:211],v[124:127],a[92:95],a[208:211]
v_mfma_f32_16x16x32_bf16 a[212:215],v[124:127],a[124:127],a[212:215]
v_mfma_f32_16x16x32_bf16 a[212:215],v[120:123],a[116:119],a[212:215]
buffer_load_dwordx4 v[196:199],v6,s[28:31],s87 offen offset:2048 sc1
v_mfma_f32_16x16x32_bf16 a[212:215],v[116:119],a[108:111],a[212:215]
v_mfma_f32_16x16x32_bf16 a[212:215],v[112:115],a[100:103],a[212:215]
v_mfma_f32_16x16x32_bf16 a[216:219],v[112:115],a[64:67],a[216:219]
v_mfma_f32_16x16x32_bf16 a[216:219],v[116:119],a[72:75],a[216:219]
v_mfma_f32_16x16x32_bf16 a[216:219],v[120:123],a[80:83],a[216:219]
buffer_load_dwordx4 v[36:39],v6,s[28:31],s88 offen sc1
v_mfma_f32_16x16x32_bf16 a[216:219],v[124:127],a[88:91],a[216:219]
v_mfma_f32_16x16x32_bf16 a[220:223],v[124:127],a[120:123],a[220:223]
ds_read_b64_tr_b16 v[124:125],v2 offset:30880
ds_read_b64_tr_b16 v[126:127],v2 offset:36000
v_mfma_f32_16x16x32_bf16 a[220:223],v[120:123],a[112:115],a[220:223]
ds_read_b64_tr_b16 v[120:121],v2 offset:20640
v_mfma_f32_16x16x32_bf16 a[220:223],v[116:119],a[104:107],a[220:223]
s_waitcnt lgkmcnt(3)
ds_read_b64_tr_b16 v[116:117],v2 offset:10400
v_mfma_f32_16x16x32_bf16 a[220:223],v[112:115],a[96:99],a[220:223]
buffer_load_dwordx4 v[140:143],v28,s[28:31],s87 offen offset:3072 sc1
v_mfma_f32_16x16x32_bf16 a[224:227],v[64:67],a[68:71],a[224:227]
s_add_u32 s87,s64,1
s_and_b32 s87,s87,3
s_lshl_b32 s87,s87,1
v_mfma_f32_16x16x32_bf16 a[224:227],v[68:71],a[76:79],a[224:227]
s_or_b32 s87,s87,1
s_lshl_b32 s87,s87,16
ds_read_b64_tr_b16 v[112:113],v2 offset:160
v_mfma_f32_16x16x32_bf16 a[224:227],v[72:75],a[84:87],a[224:227]
ds_read_b64_tr_b16 v[114:115],v2 offset:5280
ds_read_b64_tr_b16 v[118:119],v2 offset:15520
v_mfma_f32_16x16x32_bf16 a[224:227],v[76:79],a[92:95],a[224:227]
ds_read_b64_tr_b16 v[122:123],v2 offset:25760
v_mfma_f32_16x16x32_bf16 a[228:231],v[76:79],a[124:127],a[228:231]
buffer_load_dwordx4 v[212:215],v6,s[28:31],s88 offen offset:2048 sc1
v_mfma_f32_16x16x32_bf16 a[228:231],v[72:75],a[116:119],a[228:231]
v_mfma_f32_16x16x32_bf16 a[228:231],v[68:71],a[108:111],a[228:231]
v_mfma_f32_16x16x32_bf16 a[228:231],v[64:67],a[100:103],a[228:231]
v_mfma_f32_16x16x32_bf16 a[232:235],v[64:67],a[64:67],a[232:235]
v_mfma_f32_16x16x32_bf16 a[232:235],v[68:71],a[72:75],a[232:235]
buffer_load_dwordx4 v[148:151],v6,s[28:31],s88 offen offset:1024 sc1
v_mfma_f32_16x16x32_bf16 a[232:235],v[72:75],a[80:83],a[232:235]
s_waitcnt vmcnt(11)
v_cmp_ne_u32 s[56:57],s52,v4
v_cmp_ne_u32 s[58:59],s53,v5
v_mfma_f32_16x16x32_bf16 a[232:235],v[76:79],a[88:91],a[232:235]
s_or_b64 s[56:57],s[56:57],s[58:59]
s_cbranch_scc1 L_fr7_%=
L_fo7_%=:
s_lshl_b32 s91,s64,6
v_mfma_f32_16x16x32_bf16 a[236:239],v[76:79],a[120:123],a[236:239]
ds_read_b64_tr_b16 v[76:77],v1 offset:30912
ds_read_b64_tr_b16 v[78:79],v1 offset:36032
v_mfma_f32_16x16x32_bf16 a[236:239],v[72:75],a[112:115],a[236:239]
ds_read_b64_tr_b16 v[72:73],v1 offset:20672
v_bfe_u32 v3,v6,4,2
v_mfma_f32_16x16x32_bf16 a[236:239],v[68:71],a[104:107],a[236:239]
buffer_load_dwordx4 v[40:43],v6,s[28:31],s89 offen sc1
v_mfma_f32_16x16x32_bf16 a[236:239],v[64:67],a[96:99],a[236:239]
ds_read_b64_tr_b16 v[64:65],v1 offset:192
ds_read_b64_tr_b16 v[66:67],v1 offset:5312
v_mfma_f32_16x16x32_bf16 a[240:243],v[80:83],a[68:71],a[240:243]
ds_read_b64_tr_b16 v[68:69],v1 offset:10432
v_min_u32 v3,2,v3
v_mfma_f32_16x16x32_bf16 a[240:243],v[84:87],a[76:79],a[240:243]
ds_read_b64_tr_b16 v[70:71],v1 offset:15552
v_add3_u32 v3,v3,s63,1
v_mfma_f32_16x16x32_bf16 a[240:243],v[88:91],a[84:87],a[240:243]
ds_read_b64_tr_b16 v[74:75],v1 offset:25792
v_and_b32 v3,3,v3
v_mfma_f32_16x16x32_bf16 a[240:243],v[92:95],a[92:95],a[240:243]
buffer_load_dwordx4 a[128:131],v6,s[28:31],s87 offen sc1
v_mfma_f32_16x16x32_bf16 a[244:247],v[92:95],a[124:127],a[244:247]
v_lshlrev_b32 v3,8,v3
v_mfma_f32_16x16x32_bf16 a[244:247],v[88:91],a[116:119],a[244:247]
v_mfma_f32_16x16x32_bf16 a[244:247],v[84:87],a[108:111],a[244:247]
v_mfma_f32_16x16x32_bf16 a[244:247],v[80:83],a[100:103],a[244:247]
v_mfma_f32_16x16x32_bf16 a[248:251],v[80:83],a[64:67],a[248:251]
buffer_load_dwordx4 v[156:159],v28,s[28:31],s88 offen offset:3072 sc1
v_mfma_f32_16x16x32_bf16 a[248:251],v[84:87],a[72:75],a[248:251]
s_add_u32 s88,s87,4096
v_mfma_f32_16x16x32_bf16 a[248:251],v[88:91],a[80:83],a[248:251]
v_mfma_f32_16x16x32_bf16 a[248:251],v[92:95],a[88:91],a[248:251]
v_mfma_f32_16x16x32_bf16 a[252:255],v[92:95],a[120:123],a[252:255]
ds_read_b64_tr_b16 v[92:93],v2 offset:30944
ds_read_b64_tr_b16 v[94:95],v2 offset:36064
v_mfma_f32_16x16x32_bf16 a[252:255],v[88:91],a[112:115],a[252:255]
buffer_load_dwordx4 v[228:231],v6,s[28:31],s89 offen offset:2048 sc1
v_mfma_f32_16x16x32_bf16 a[252:255],v[84:87],a[104:107],a[252:255]
ds_read_b64_tr_b16 v[84:85],v2 offset:10464
ds_read_b64_tr_b16 v[86:87],v2 offset:15584
v_mfma_f32_16x16x32_bf16 a[252:255],v[80:83],a[96:99],a[252:255]
ds_read_b64_tr_b16 v[80:81],v2 offset:224
v_mfma_f32_16x16x32_bf16 a[0:3],v[96:99],a[68:71],a[0:3]
ds_read_b64_tr_b16 v[82:83],v2 offset:5344
v_mfma_f32_16x16x32_bf16 a[0:3],v[100:103],a[76:79],a[0:3]
ds_read_b64_tr_b16 v[88:89],v2 offset:20704
v_mfma_f32_16x16x32_bf16 a[0:3],v[104:107],a[84:87],a[0:3]
buffer_load_dwordx4 v[164:167],v6,s[28:31],s89 offen offset:1024 sc1
v_mfma_f32_16x16x32_bf16 a[0:3],v[108:111],a[92:95],a[0:3]
ds_read_b64_tr_b16 v[90:91],v2 offset:25824
v_mfma_f32_16x16x32_bf16 a[4:7],v[108:111],a[124:127],a[4:7]
v_mfma_f32_16x16x32_bf16 a[4:7],v[104:107],a[116:119],a[4:7]
v_mfma_f32_16x16x32_bf16 a[4:7],v[100:103],a[108:111],a[4:7]
v_mfma_f32_16x16x32_bf16 a[4:7],v[96:99],a[100:103],a[4:7]
buffer_load_dwordx4 v[44:47],v6,s[28:31],s90 offen sc1
v_mfma_f32_16x16x32_bf16 a[8:11],v[96:99],a[64:67],a[8:11]
v_mfma_f32_16x16x32_bf16 a[8:11],v[100:103],a[72:75],a[8:11]
v_mfma_f32_16x16x32_bf16 a[8:11],v[104:107],a[80:83],a[8:11]
v_mfma_f32_16x16x32_bf16 a[8:11],v[108:111],a[88:91],a[8:11]
v_mfma_f32_16x16x32_bf16 a[12:15],v[108:111],a[120:123],a[12:15]
buffer_load_dwordx4 a[144:147],v6,s[28:31],s88 offen sc1
v_mfma_f32_16x16x32_bf16 a[12:15],v[104:107],a[112:115],a[12:15]
ds_read_b64_tr_b16 v[104:105],v1 offset:20736
ds_read_b64_tr_b16 v[106:107],v1 offset:25856
v_mfma_f32_16x16x32_bf16 a[12:15],v[100:103],a[104:107],a[12:15]
ds_read_b64_tr_b16 v[100:101],v1 offset:10496
v_mfma_f32_16x16x32_bf16 a[12:15],v[96:99],a[96:99],a[12:15]
s_waitcnt lgkmcnt(3)
ds_read_b64_tr_b16 v[96:97],v1 offset:256
v_mfma_f32_16x16x32_bf16 a[16:19],v[112:115],a[68:71],a[16:19]
ds_read_b64_tr_b16 v[98:99],v1 offset:5376
v_mfma_f32_16x16x32_bf16 a[16:19],v[116:119],a[76:79],a[16:19]
buffer_load_dwordx4 v[172:175],v28,s[28:31],s89 offen offset:3072 sc1
v_mfma_f32_16x16x32_bf16 a[16:19],v[120:123],a[84:87],a[16:19]
s_add_u32 s89,s87,8192
ds_read_b64_tr_b16 v[102:103],v1 offset:15616
ds_read_b64_tr_b16 v[108:109],v1 offset:30976
v_mfma_f32_16x16x32_bf16 a[16:19],v[124:127],a[92:95],a[16:19]
ds_read_b64_tr_b16 v[110:111],v1 offset:36096
v_add_u32 v1,0x1e000,v1
v_mfma_f32_16x16x32_bf16 a[20:23],v[124:127],a[124:127],a[20:23]
v_mfma_f32_16x16x32_bf16 a[20:23],v[120:123],a[116:119],a[20:23]
v_mfma_f32_16x16x32_bf16 a[20:23],v[116:119],a[108:111],a[20:23]
buffer_load_dwordx4 v[180:183],v6,s[28:31],s90 offen offset:1024 sc1
v_mfma_f32_16x16x32_bf16 a[20:23],v[112:115],a[100:103],a[20:23]
v_mfma_f32_16x16x32_bf16 a[24:27],v[112:115],a[64:67],a[24:27]
v_mfma_f32_16x16x32_bf16 a[24:27],v[116:119],a[72:75],a[24:27]
v_mfma_f32_16x16x32_bf16 a[24:27],v[120:123],a[80:83],a[24:27]
v_mfma_f32_16x16x32_bf16 a[24:27],v[124:127],a[88:91],a[24:27]
buffer_load_dwordx4 a[160:163],v6,s[28:31],s89 offen sc1
v_mfma_f32_16x16x32_bf16 a[28:31],v[124:127],a[120:123],a[28:31]
ds_read_b64_tr_b16 v[124:125],v2 offset:31008
ds_read_b64_tr_b16 v[126:127],v2 offset:36128
v_mfma_f32_16x16x32_bf16 a[28:31],v[120:123],a[112:115],a[28:31]
ds_read_b64_tr_b16 v[120:121],v2 offset:20768
v_mfma_f32_16x16x32_bf16 a[28:31],v[116:119],a[104:107],a[28:31]
ds_read_b64_tr_b16 v[116:117],v2 offset:10528
v_mfma_f32_16x16x32_bf16 a[28:31],v[112:115],a[96:99],a[28:31]
ds_read_b64_tr_b16 v[112:113],v2 offset:288
v_mfma_f32_16x16x32_bf16 v[128:131],v[64:67],a[68:71],v[128:131]
buffer_load_dwordx4 v[244:247],v6,s[28:31],s90 offen offset:2048 sc1
)A"
R"A(v_mfma_f32_16x16x32_bf16 v[128:131],v[68:71],a[76:79],v[128:131]
ds_read_b64_tr_b16 v[114:115],v2 offset:5408
ds_read_b64_tr_b16 v[118:119],v2 offset:15648
v_mfma_f32_16x16x32_bf16 v[128:131],v[72:75],a[84:87],v[128:131]
ds_read_b64_tr_b16 v[122:123],v2 offset:25888
v_add_u32 v2,0x1e000,v2
v_mfma_f32_16x16x32_bf16 v[128:131],v[76:79],a[92:95],v[128:131]
v_mfma_f32_16x16x32_bf16 v[136:139],v[76:79],a[124:127],v[136:139]
v_mfma_f32_16x16x32_bf16 v[136:139],v[72:75],a[116:119],v[136:139]
buffer_load_dwordx4 v[188:191],v28,s[28:31],s90 offen offset:3072 sc1
v_mfma_f32_16x16x32_bf16 v[136:139],v[68:71],a[108:111],v[136:139]
s_add_u32 s90,s87,12288
v_mfma_f32_16x16x32_bf16 v[136:139],v[64:67],a[100:103],v[136:139]
v_mfma_f32_16x16x32_bf16 v[144:147],v[64:67],a[64:67],v[144:147]
v_mfma_f32_16x16x32_bf16 v[144:147],v[68:71],a[72:75],v[144:147]
v_mfma_f32_16x16x32_bf16 v[144:147],v[72:75],a[80:83],v[144:147]
buffer_load_dwordx4 a[176:179],v6,s[28:31],s90 offen sc1
v_mfma_f32_16x16x32_bf16 v[144:147],v[76:79],a[88:91],v[144:147]
v_mfma_f32_16x16x32_bf16 v[152:155],v[76:79],a[120:123],v[152:155]
v_mfma_f32_16x16x32_bf16 v[152:155],v[72:75],a[112:115],v[152:155]
v_mfma_f32_16x16x32_bf16 v[152:155],v[68:71],a[104:107],v[152:155]
v_mfma_f32_16x16x32_bf16 v[152:155],v[64:67],a[96:99],v[152:155]
buffer_load_dwordx4 a[180:183],v6,s[28:31],s90 offen offset:1024 sc1
s_waitcnt vmcnt(21)
s_barrier
v_mfma_f32_16x16x32_bf16 v[160:163],v[80:83],a[68:71],v[160:163]
ds_read_b64_tr_b16 v[64:65],v1
ds_read_b64_tr_b16 v[66:67],v1 offset:5120
v_mfma_f32_16x16x32_bf16 v[160:163],v[84:87],a[76:79],v[160:163]
ds_read_b64_tr_b16 v[68:69],v1 offset:10240
v_mfma_f32_16x16x32_bf16 v[160:163],v[88:91],a[84:87],v[160:163]
ds_read_b64_tr_b16 v[70:71],v1 offset:15360
v_mfma_f32_16x16x32_bf16 v[160:163],v[92:95],a[92:95],v[160:163]
buffer_load_dwordx4 a[164:167],v6,s[28:31],s89 offen offset:1024 sc1
v_mfma_f32_16x16x32_bf16 v[168:171],v[92:95],a[124:127],v[168:171]
ds_read_b64_tr_b16 v[72:73],v1 offset:20480
ds_read_b64_tr_b16 v[74:75],v1 offset:25600
v_mfma_f32_16x16x32_bf16 v[168:171],v[88:91],a[116:119],v[168:171]
ds_read_b64_tr_b16 v[76:77],v1 offset:30720
v_mfma_f32_16x16x32_bf16 v[168:171],v[84:87],a[108:111],v[168:171]
ds_read_b64_tr_b16 v[78:79],v1 offset:35840
v_mfma_f32_16x16x32_bf16 v[168:171],v[80:83],a[100:103],v[168:171]
v_mfma_f32_16x16x32_bf16 v[176:179],v[80:83],a[64:67],v[176:179]
buffer_load_dwordx4 a[148:151],v6,s[28:31],s88 offen offset:1024 sc1
v_mfma_f32_16x16x32_bf16 v[176:179],v[84:87],a[72:75],v[176:179]
v_mfma_f32_16x16x32_bf16 v[176:179],v[88:91],a[80:83],v[176:179]
v_mfma_f32_16x16x32_bf16 v[176:179],v[92:95],a[88:91],v[176:179]
v_mfma_f32_16x16x32_bf16 v[184:187],v[92:95],a[120:123],v[184:187]
ds_read_b64_tr_b16 v[92:93],v2 offset:30752
ds_read_b64_tr_b16 v[94:95],v2 offset:35872
v_mfma_f32_16x16x32_bf16 v[184:187],v[88:91],a[112:115],v[184:187]
buffer_load_dwordx4 a[132:135],v6,s[28:31],s87 offen offset:1024 sc1
v_mfma_f32_16x16x32_bf16 v[184:187],v[84:87],a[104:107],v[184:187]
ds_read_b64_tr_b16 v[84:85],v2 offset:10272
ds_read_b64_tr_b16 v[86:87],v2 offset:15392
v_mfma_f32_16x16x32_bf16 v[184:187],v[80:83],a[96:99],v[184:187]
s_waitcnt lgkmcnt(4)
ds_read_b64_tr_b16 v[80:81],v2 offset:32
v_mfma_f32_16x16x32_bf16 v[192:195],v[96:99],a[68:71],v[192:195]
ds_read_b64_tr_b16 v[82:83],v2 offset:5152
v_mfma_f32_16x16x32_bf16 v[192:195],v[100:103],a[76:79],v[192:195]
ds_read_b64_tr_b16 v[88:89],v2 offset:20512
v_mfma_f32_16x16x32_bf16 v[192:195],v[104:107],a[84:87],v[192:195]
ds_read_b64_tr_b16 v[90:91],v2 offset:25632
v_mfma_f32_16x16x32_bf16 v[192:195],v[108:111],a[92:95],v[192:195]
buffer_load_dwordx4 v24,s[48:51],s83 offen lds
v_mfma_f32_16x16x32_bf16 v[200:203],v[108:111],a[124:127],v[200:203]
v_mfma_f32_16x16x32_bf16 v[200:203],v[104:107],a[116:119],v[200:203]
v_mfma_f32_16x16x32_bf16 v[200:203],v[100:103],a[108:111],v[200:203]
v_mfma_f32_16x16x32_bf16 v[200:203],v[96:99],a[100:103],v[200:203]
v_mfma_f32_16x16x32_bf16 v[208:211],v[96:99],a[64:67],v[208:211]
buffer_load_dwordx4 a[136:139],v6,s[28:31],s87 offen offset:2048 sc1
v_mfma_f32_16x16x32_bf16 v[208:211],v[100:103],a[72:75],v[208:211]
v_mfma_f32_16x16x32_bf16 v[208:211],v[104:107],a[80:83],v[208:211]
v_mfma_f32_16x16x32_bf16 v[208:211],v[108:111],a[88:91],v[208:211]
v_mfma_f32_16x16x32_bf16 v[216:219],v[108:111],a[120:123],v[216:219]
ds_read_b64_tr_b16 v[108:109],v1 offset:30784
ds_read_b64_tr_b16 v[110:111],v1 offset:35904
v_mfma_f32_16x16x32_bf16 v[216:219],v[104:107],a[112:115],v[216:219]
ds_read_b64_tr_b16 v[104:105],v1 offset:20544
v_mfma_f32_16x16x32_bf16 v[216:219],v[100:103],a[104:107],v[216:219]
buffer_load_dwordx4 v25,s[48:51],s83 offen offset:1024 lds
v_mfma_f32_16x16x32_bf16 v[216:219],v[96:99],a[96:99],v[216:219]
ds_read_b64_tr_b16 v[96:97],v1 offset:64
ds_read_b64_tr_b16 v[98:99],v1 offset:5184
v_mfma_f32_16x16x32_bf16 v[224:227],v[112:115],a[68:71],v[224:227]
ds_read_b64_tr_b16 v[100:101],v1 offset:10304
v_mfma_f32_16x16x32_bf16 v[224:227],v[116:119],a[76:79],v[224:227]
ds_read_b64_tr_b16 v[102:103],v1 offset:15424
v_mfma_f32_16x16x32_bf16 v[224:227],v[120:123],a[84:87],v[224:227]
ds_read_b64_tr_b16 v[106:107],v1 offset:25664
v_mfma_f32_16x16x32_bf16 v[224:227],v[124:127],a[92:95],v[224:227]
buffer_load_dwordx4 a[152:155],v6,s[28:31],s88 offen offset:2048 sc1
v_mfma_f32_16x16x32_bf16 v[232:235],v[124:127],a[124:127],v[232:235]
v_mfma_f32_16x16x32_bf16 v[232:235],v[120:123],a[116:119],v[232:235]
v_mfma_f32_16x16x32_bf16 v[232:235],v[116:119],a[108:111],v[232:235]
v_mfma_f32_16x16x32_bf16 v[232:235],v[112:115],a[100:103],v[232:235]
v_mfma_f32_16x16x32_bf16 v[240:243],v[112:115],a[64:67],v[240:243]
v_mfma_f32_16x16x32_bf16 v[240:243],v[116:119],a[72:75],v[240:243]
buffer_load_dwordx4 v26,s[48:51],s83 offen offset:2048 lds
v_mfma_f32_16x16x32_bf16 v[240:243],v[120:123],a[80:83],v[240:243]
v_mfma_f32_16x16x32_bf16 v[240:243],v[124:127],a[88:91],v[240:243]
v_mfma_f32_16x16x32_bf16 v[248:251],v[124:127],a[120:123],v[248:251]
ds_read_b64_tr_b16 v[124:125],v2 offset:30816
ds_read_b64_tr_b16 v[126:127],v2 offset:35936
v_mfma_f32_16x16x32_bf16 v[248:251],v[120:123],a[112:115],v[248:251]
ds_read_b64_tr_b16 v[120:121],v2 offset:20576
v_mfma_f32_16x16x32_bf16 v[248:251],v[116:119],a[104:107],v[248:251]
buffer_load_dwordx4 a[168:171],v6,s[28:31],s89 offen offset:2048 sc1
v_mfma_f32_16x16x32_bf16 v[248:251],v[112:115],a[96:99],v[248:251]
s_waitcnt vmcnt(22)
ds_read_b64_tr_b16 v[112:113],v2 offset:96
ds_read_b64_tr_b16 v[114:115],v2 offset:5216
v_mfma_f32_16x16x32_bf16 a[192:195],v[64:67],v[32:35],a[192:195]
ds_read_b64_tr_b16 v[116:117],v2 offset:10336
v_mfma_f32_16x16x32_bf16 a[192:195],v[68:71],v[36:39],a[192:195]
ds_read_b64_tr_b16 v[118:119],v2 offset:15456
v_mfma_f32_16x16x32_bf16 a[192:195],v[72:75],v[40:43],a[192:195]
s_waitcnt vmcnt(17)
ds_read_b64_tr_b16 v[122:123],v2 offset:25696
v_mfma_f32_16x16x32_bf16 a[192:195],v[76:79],v[44:47],a[192:195]
s_waitcnt vmcnt(14)
v_mfma_f32_16x16x32_bf16 a[196:199],v[76:79],v[180:183],a[196:199]
buffer_load_dwordx4 v27,s[48:51],s83 offen offset:3072 lds
v_mfma_f32_16x16x32_bf16 a[196:199],v[72:75],v[164:167],a[196:199]
s_add_u32 m0,s86,45056
v_mfma_f32_16x16x32_bf16 a[196:199],v[68:71],v[148:151],a[196:199]
v_mfma_f32_16x16x32_bf16 a[196:199],v[64:67],v[132:135],a[196:199]
v_mfma_f32_16x16x32_bf16 a[200:203],v[64:67],v[196:199],a[200:203]
v_mfma_f32_16x16x32_bf16 a[200:203],v[68:71],v[212:215],a[200:203]
buffer_load_dwordx4 a[184:187],v6,s[28:31],s90 offen offset:2048 sc1
v_mfma_f32_16x16x32_bf16 a[200:203],v[72:75],v[228:231],a[200:203]
s_waitcnt vmcnt(14)
v_mfma_f32_16x16x32_bf16 a[200:203],v[76:79],v[244:247],a[200:203]
s_waitcnt vmcnt(13)
v_mfma_f32_16x16x32_bf16 a[204:207],v[76:79],v[188:191],a[204:207]
ds_read_b64_tr_b16 v[76:77],v1 offset:30848
ds_read_b64_tr_b16 v[78:79],v1 offset:35968
v_mfma_f32_16x16x32_bf16 a[204:207],v[72:75],v[172:175],a[204:207]
ds_read_b64_tr_b16 v[72:73],v1 offset:20608
v_mfma_f32_16x16x32_bf16 a[204:207],v[68:71],v[156:159],a[204:207]
s_waitcnt lgkmcnt(3)
ds_read_b64_tr_b16 v[68:69],v1 offset:10368
v_mfma_f32_16x16x32_bf16 a[204:207],v[64:67],v[140:143],a[204:207]
buffer_load_dwordx4 v7,s[48:51],s83 offen lds
v_mfma_f32_16x16x32_bf16 a[208:211],v[80:83],v[32:35],a[208:211]
s_add_u32 m0,s86,46080
ds_read_b64_tr_b16 v[64:65],v1 offset:128
ds_read_b64_tr_b16 v[66:67],v1 offset:5248
v_mfma_f32_16x16x32_bf16 a[208:211],v[84:87],v[36:39],a[208:211]
s_mul_i32 s83,s63,20480
ds_read_b64_tr_b16 v[70:71],v1 offset:15488
v_mfma_f32_16x16x32_bf16 a[208:211],v[88:91],v[40:43],a[208:211]
ds_read_b64_tr_b16 v[74:75],v1 offset:25728
v_mfma_f32_16x16x32_bf16 a[208:211],v[92:95],v[44:47],a[208:211]
v_mfma_f32_16x16x32_bf16 a[212:215],v[92:95],v[180:183],a[212:215]
buffer_load_dwordx4 a[188:191],v28,s[28:31],s90 offen offset:3072 sc1
v_mfma_f32_16x16x32_bf16 a[212:215],v[88:91],v[164:167],a[212:215]
v_mfma_f32_16x16x32_bf16 a[212:215],v[84:87],v[148:151],a[212:215]
v_mfma_f32_16x16x32_bf16 a[212:215],v[80:83],v[132:135],a[212:215]
v_mfma_f32_16x16x32_bf16 a[216:219],v[80:83],v[196:199],a[216:219]
v_mfma_f32_16x16x32_bf16 a[216:219],v[84:87],v[212:215],a[216:219]
v_mfma_f32_16x16x32_bf16 a[216:219],v[88:91],v[228:231],a[216:219]
buffer_load_dwordx4 v24,s[48:51],s84 offen lds
v_mfma_f32_16x16x32_bf16 a[216:219],v[92:95],v[244:247],a[216:219]
v_mfma_f32_16x16x32_bf16 a[220:223],v[92:95],v[188:191],a[220:223]
ds_read_b64_tr_b16 v[92:93],v2 offset:30880
ds_read_b64_tr_b16 v[94:95],v2 offset:36000
v_mfma_f32_16x16x32_bf16 a[220:223],v[88:91],v[172:175],a[220:223]
ds_read_b64_tr_b16 v[88:89],v2 offset:20640
v_mfma_f32_16x16x32_bf16 a[220:223],v[84:87],v[156:159],a[220:223]
ds_read_b64_tr_b16 v[84:85],v2 offset:10400
v_mfma_f32_16x16x32_bf16 a[220:223],v[80:83],v[140:143],a[220:223]
ds_read_b64_tr_b16 v[80:81],v2 offset:160
v_mfma_f32_16x16x32_bf16 a[224:227],v[96:99],v[32:35],a[224:227]
buffer_load_dwordx4 v25,s[48:51],s84 offen offset:1024 lds
v_mfma_f32_16x16x32_bf16 a[224:227],v[100:103],v[36:39],a[224:227]
ds_read_b64_tr_b16 v[82:83],v2 offset:5280
ds_read_b64_tr_b16 v[86:87],v2 offset:15520
v_mfma_f32_16x16x32_bf16 a[224:227],v[104:107],v[40:43],a[224:227]
ds_read_b64_tr_b16 v[90:91],v2 offset:25760
v_mfma_f32_16x16x32_bf16 a[224:227],v[108:111],v[44:47],a[224:227]
v_mfma_f32_16x16x32_bf16 a[228:231],v[108:111],v[180:183],a[228:231]
v_mfma_f32_16x16x32_bf16 a[228:231],v[104:107],v[164:167],a[228:231]
buffer_load_dwordx4 a[172:175],v28,s[28:31],s89 offen offset:3072 sc1
v_mfma_f32_16x16x32_bf16 a[228:231],v[100:103],v[148:151],a[228:231]
v_mfma_f32_16x16x32_bf16 a[228:231],v[96:99],v[132:135],a[228:231]
v_mfma_f32_16x16x32_bf16 a[232:235],v[96:99],v[196:199],a[232:235]
v_mfma_f32_16x16x32_bf16 a[232:235],v[100:103],v[212:215],a[232:235]
v_mfma_f32_16x16x32_bf16 a[232:235],v[104:107],v[228:231],a[232:235]
v_mfma_f32_16x16x32_bf16 a[232:235],v[108:111],v[244:247],a[232:235]
buffer_load_dwordx4 v26,s[48:51],s84 offen offset:2048 lds
v_mfma_f32_16x16x32_bf16 a[236:239],v[108:111],v[188:191],a[236:239]
ds_read_b64_tr_b16 v[108:109],v1 offset:30912
ds_read_b64_tr_b16 v[110:111],v1 offset:36032
v_mfma_f32_16x16x32_bf16 a[236:239],v[104:107],v[172:175],a[236:239]
ds_read_b64_tr_b16 v[104:105],v1 offset:20672
v_mfma_f32_16x16x32_bf16 a[236:239],v[100:103],v[156:159],a[236:239]
ds_read_b64_tr_b16 v[100:101],v1 offset:10432
v_mfma_f32_16x16x32_bf16 a[236:239],v[96:99],v[140:143],a[236:239]
ds_read_b64_tr_b16 v[96:97],v1 offset:192
v_mfma_f32_16x16x32_bf16 a[240:243],v[112:115],v[32:35],a[240:243]
ds_read_b64_tr_b16 v[98:99],v1 offset:5312
ds_read_b64_tr_b16 v[102:103],v1 offset:15552
v_mfma_f32_16x16x32_bf16 a[240:243],v[116:119],v[36:39],a[240:243]
buffer_load_dwordx4 v27,s[48:51],s84 offen offset:3072 lds
v_mfma_f32_16x16x32_bf16 a[240:243],v[120:123],v[40:43],a[240:243]
s_add_u32 m0,s86,50176
ds_read_b64_tr_b16 v[106:107],v1 offset:25792
v_add_u32 v1,0xffff6000,v1
v_mfma_f32_16x16x32_bf16 a[240:243],v[124:127],v[44:47],a[240:243]
v_mfma_f32_16x16x32_bf16 a[244:247],v[124:127],v[180:183],a[244:247]
)A"
R"A(v_mfma_f32_16x16x32_bf16 a[244:247],v[120:123],v[164:167],a[244:247]
v_mfma_f32_16x16x32_bf16 a[244:247],v[116:119],v[148:151],a[244:247]
buffer_load_dwordx4 a[140:143],v28,s[28:31],s87 offen offset:3072 sc1
v_mfma_f32_16x16x32_bf16 a[244:247],v[112:115],v[132:135],a[244:247]
s_add_u32 s87,s64,2
s_and_b32 s87,s87,3
s_lshl_b32 s87,s87,1
s_or_b32 s87,s87,1
s_lshl_b32 s87,s87,16
s_add_u32 s89,s87,8192
s_add_u32 s90,s87,12288
v_mfma_f32_16x16x32_bf16 a[248:251],v[112:115],v[196:199],a[248:251]
v_mfma_f32_16x16x32_bf16 a[248:251],v[116:119],v[212:215],a[248:251]
v_mfma_f32_16x16x32_bf16 a[248:251],v[120:123],v[228:231],a[248:251]
v_mfma_f32_16x16x32_bf16 a[248:251],v[124:127],v[244:247],a[248:251]
buffer_load_dwordx4 a[68:71],v6,s[28:31],s87 offen sc1
v_mfma_f32_16x16x32_bf16 a[252:255],v[124:127],v[188:191],a[252:255]
ds_read_b64_tr_b16 v[124:125],v2 offset:30944
ds_read_b64_tr_b16 v[126:127],v2 offset:36064
v_mfma_f32_16x16x32_bf16 a[252:255],v[120:123],v[172:175],a[252:255]
ds_read_b64_tr_b16 v[120:121],v2 offset:20704
v_mfma_f32_16x16x32_bf16 a[252:255],v[116:119],v[156:159],a[252:255]
ds_read_b64_tr_b16 v[116:117],v2 offset:10464
v_mfma_f32_16x16x32_bf16 a[252:255],v[112:115],v[140:143],a[252:255]
s_waitcnt lgkmcnt(4)
ds_read_b64_tr_b16 v[112:113],v2 offset:224
v_mfma_f32_16x16x32_bf16 a[0:3],v[64:67],v[32:35],a[0:3]
buffer_load_dwordx4 a[156:159],v28,s[28:31],s88 offen offset:3072 sc1
v_mfma_f32_16x16x32_bf16 a[0:3],v[68:71],v[36:39],a[0:3]
s_add_u32 s88,s87,4096
ds_read_b64_tr_b16 v[114:115],v2 offset:5344
ds_read_b64_tr_b16 v[118:119],v2 offset:15584
v_mfma_f32_16x16x32_bf16 a[0:3],v[72:75],v[40:43],a[0:3]
ds_read_b64_tr_b16 v[122:123],v2 offset:25824
v_add_u32 v2,0xffff6000,v2
v_mfma_f32_16x16x32_bf16 a[0:3],v[76:79],v[44:47],a[0:3]
v_mfma_f32_16x16x32_bf16 a[4:7],v[76:79],v[180:183],a[4:7]
v_mfma_f32_16x16x32_bf16 a[4:7],v[72:75],v[164:167],a[4:7]
v_mfma_f32_16x16x32_bf16 a[4:7],v[68:71],v[148:151],a[4:7]
buffer_load_dwordx4 v7,s[48:51],s84 offen lds
v_mfma_f32_16x16x32_bf16 a[4:7],v[64:67],v[132:135],a[4:7]
v_mfma_f32_16x16x32_bf16 a[8:11],v[64:67],v[196:199],a[8:11]
v_mfma_f32_16x16x32_bf16 a[8:11],v[68:71],v[212:215],a[8:11]
v_mfma_f32_16x16x32_bf16 a[8:11],v[72:75],v[228:231],a[8:11]
v_mfma_f32_16x16x32_bf16 a[8:11],v[76:79],v[244:247],a[8:11]
buffer_load_dwordx4 a[76:79],v6,s[28:31],s88 offen sc1
v_mfma_f32_16x16x32_bf16 a[12:15],v[76:79],v[188:191],a[12:15]
ds_read_b64_tr_b16 v[76:77],v1 offset:30720
ds_read_b64_tr_b16 v[78:79],v1 offset:35840
v_mfma_f32_16x16x32_bf16 a[12:15],v[72:75],v[172:175],a[12:15]
ds_read_b64_tr_b16 v[72:73],v1 offset:20480
v_mfma_f32_16x16x32_bf16 a[12:15],v[68:71],v[156:159],a[12:15]
ds_read_b64_tr_b16 v[68:69],v1 offset:10240
v_mfma_f32_16x16x32_bf16 a[12:15],v[64:67],v[140:143],a[12:15]
ds_read_b64_tr_b16 v[64:65],v1
v_mfma_f32_16x16x32_bf16 a[16:19],v[80:83],v[32:35],a[16:19]
buffer_load_dwordx4 a[84:87],v6,s[28:31],s89 offen sc1
v_mfma_f32_16x16x32_bf16 a[16:19],v[84:87],v[36:39],a[16:19]
ds_read_b64_tr_b16 v[66:67],v1 offset:5120
ds_read_b64_tr_b16 v[70:71],v1 offset:15360
v_mfma_f32_16x16x32_bf16 a[16:19],v[88:91],v[40:43],a[16:19]
ds_read_b64_tr_b16 v[74:75],v1 offset:25600
s_nop 0
L_pd22_%=:
s_cbranch_execz L_trtg2S_%=
v_add_u32 v1,0xffff6000,v1
v_mfma_f32_16x16x32_bf16 a[16:19],v[92:95],v[44:47],a[16:19]
v_mfma_f32_16x16x32_bf16 a[20:23],v[92:95],v[180:183],a[20:23]
v_mfma_f32_16x16x32_bf16 a[20:23],v[88:91],v[164:167],a[20:23]
buffer_load_dwordx4 a[92:95],v6,s[28:31],s90 offen sc1
v_mfma_f32_16x16x32_bf16 a[20:23],v[84:87],v[148:151],a[20:23]
v_mfma_f32_16x16x32_bf16 a[20:23],v[80:83],v[132:135],a[20:23]
v_mfma_f32_16x16x32_bf16 a[24:27],v[80:83],v[196:199],a[24:27]
v_mfma_f32_16x16x32_bf16 a[24:27],v[84:87],v[212:215],a[24:27]
v_mfma_f32_16x16x32_bf16 a[24:27],v[88:91],v[228:231],a[24:27]
buffer_load_dwordx4 a[124:127],v6,s[28:31],s90 offen offset:1024 sc1
v_mfma_f32_16x16x32_bf16 a[24:27],v[92:95],v[244:247],a[24:27]
v_mfma_f32_16x16x32_bf16 a[28:31],v[92:95],v[188:191],a[28:31]
ds_read_b64_tr_b16 v[92:93],v2 offset:30752
ds_read_b64_tr_b16 v[94:95],v2 offset:35872
v_mfma_f32_16x16x32_bf16 a[28:31],v[88:91],v[172:175],a[28:31]
ds_read_b64_tr_b16 v[88:89],v2 offset:20512
v_mfma_f32_16x16x32_bf16 a[28:31],v[84:87],v[156:159],a[28:31]
ds_read_b64_tr_b16 v[84:85],v2 offset:10272
v_mfma_f32_16x16x32_bf16 a[28:31],v[80:83],v[140:143],a[28:31]
buffer_load_dwordx4 a[116:119],v6,s[28:31],s89 offen offset:1024 sc1
v_mfma_f32_16x16x32_bf16 v[128:131],v[96:99],v[32:35],v[128:131]
ds_read_b64_tr_b16 v[80:81],v2 offset:32
ds_read_b64_tr_b16 v[82:83],v2 offset:5152
v_mfma_f32_16x16x32_bf16 v[128:131],v[100:103],v[36:39],v[128:131]
ds_read_b64_tr_b16 v[86:87],v2 offset:15392
v_mfma_f32_16x16x32_bf16 v[128:131],v[104:107],v[40:43],v[128:131]
ds_read_b64_tr_b16 v[90:91],v2 offset:25632
v_add_u32 v2,0xffff6000,v2
v_mfma_f32_16x16x32_bf16 v[128:131],v[108:111],v[44:47],v[128:131]
v_mfma_f32_16x16x32_bf16 v[136:139],v[108:111],v[180:183],v[136:139]
buffer_load_dwordx4 a[108:111],v6,s[28:31],s88 offen offset:1024 sc1
v_mfma_f32_16x16x32_bf16 v[136:139],v[104:107],v[164:167],v[136:139]
v_mfma_f32_16x16x32_bf16 v[136:139],v[100:103],v[148:151],v[136:139]
v_mfma_f32_16x16x32_bf16 v[136:139],v[96:99],v[132:135],v[136:139]
v_mfma_f32_16x16x32_bf16 v[144:147],v[96:99],v[196:199],v[144:147]
v_mfma_f32_16x16x32_bf16 v[144:147],v[100:103],v[212:215],v[144:147]
buffer_load_dwordx4 a[100:103],v6,s[28:31],s87 offen offset:1024 sc1
v_mfma_f32_16x16x32_bf16 v[144:147],v[104:107],v[228:231],v[144:147]
v_mfma_f32_16x16x32_bf16 v[144:147],v[108:111],v[244:247],v[144:147]
v_mfma_f32_16x16x32_bf16 v[152:155],v[108:111],v[188:191],v[152:155]
v_mfma_f32_16x16x32_bf16 v[152:155],v[104:107],v[172:175],v[152:155]
v_mfma_f32_16x16x32_bf16 v[152:155],v[100:103],v[156:159],v[152:155]
buffer_load_dwordx4 a[64:67],v6,s[28:31],s87 offen offset:2048 sc1
v_mfma_f32_16x16x32_bf16 v[152:155],v[96:99],v[140:143],v[152:155]
s_waitcnt vmcnt(8)
s_barrier
s_waitcnt lgkmcnt(0)
v_mfma_f32_16x16x32_bf16 v[160:163],v[112:115],v[32:35],v[160:163]
ds_read_b64_tr_b16 v[96:97],v1
ds_read_b64_tr_b16 v[98:99],v1 offset:5120
v_mfma_f32_16x16x32_bf16 v[160:163],v[116:119],v[36:39],v[160:163]
ds_read_b64_tr_b16 v[100:101],v1 offset:10240
v_mfma_f32_16x16x32_bf16 v[160:163],v[120:123],v[40:43],v[160:163]
buffer_load_dwordx4 a[72:75],v6,s[28:31],s88 offen offset:2048 sc1
v_mfma_f32_16x16x32_bf16 v[160:163],v[124:127],v[44:47],v[160:163]
ds_read_b64_tr_b16 v[102:103],v1 offset:15360
ds_read_b64_tr_b16 v[104:105],v1 offset:20480
v_mfma_f32_16x16x32_bf16 v[168:171],v[124:127],v[180:183],v[168:171]
ds_read_b64_tr_b16 v[106:107],v1 offset:25600
v_mfma_f32_16x16x32_bf16 v[168:171],v[120:123],v[164:167],v[168:171]
ds_read_b64_tr_b16 v[108:109],v1 offset:30720
v_mfma_f32_16x16x32_bf16 v[168:171],v[116:119],v[148:151],v[168:171]
ds_read_b64_tr_b16 v[110:111],v1 offset:35840
v_add_u32 v1,0xa000,v1
v_mfma_f32_16x16x32_bf16 v[168:171],v[112:115],v[132:135],v[168:171]
buffer_load_dwordx4 a[80:83],v6,s[28:31],s89 offen offset:2048 sc1
v_mfma_f32_16x16x32_bf16 v[176:179],v[112:115],v[196:199],v[176:179]
v_mfma_f32_16x16x32_bf16 v[176:179],v[116:119],v[212:215],v[176:179]
v_mfma_f32_16x16x32_bf16 v[176:179],v[120:123],v[228:231],v[176:179]
v_mfma_f32_16x16x32_bf16 v[176:179],v[124:127],v[244:247],v[176:179]
v_mfma_f32_16x16x32_bf16 v[184:187],v[124:127],v[188:191],v[184:187]
buffer_load_dwordx4 a[88:91],v6,s[28:31],s90 offen offset:2048 sc1
v_mfma_f32_16x16x32_bf16 v[184:187],v[120:123],v[172:175],v[184:187]
ds_read_b64_tr_b16 v[120:121],v2 offset:20512
ds_read_b64_tr_b16 v[122:123],v2 offset:25632
v_mfma_f32_16x16x32_bf16 v[184:187],v[116:119],v[156:159],v[184:187]
ds_read_b64_tr_b16 v[116:117],v2 offset:10272
v_mfma_f32_16x16x32_bf16 v[184:187],v[112:115],v[140:143],v[184:187]
ds_read_b64_tr_b16 v[112:113],v2 offset:32
v_mfma_f32_16x16x32_bf16 a[192:195],v[64:67],a[128:131],a[192:195]
ds_read_b64_tr_b16 v[114:115],v2 offset:5152
v_mfma_f32_16x16x32_bf16 a[192:195],v[68:71],a[144:147],a[192:195]
buffer_load_dwordx4 a[120:123],v28,s[28:31],s90 offen offset:3072 sc1
v_mfma_f32_16x16x32_bf16 a[192:195],v[72:75],a[160:163],a[192:195]
ds_read_b64_tr_b16 v[118:119],v2 offset:15392
ds_read_b64_tr_b16 v[124:125],v2 offset:30752
v_mfma_f32_16x16x32_bf16 a[192:195],v[76:79],a[176:179],a[192:195]
ds_read_b64_tr_b16 v[126:127],v2 offset:35872
v_add_u32 v2,0xa000,v2
v_mfma_f32_16x16x32_bf16 a[196:199],v[76:79],a[180:183],a[196:199]
v_mfma_f32_16x16x32_bf16 a[196:199],v[72:75],a[164:167],a[196:199]
v_mfma_f32_16x16x32_bf16 a[196:199],v[68:71],a[148:151],a[196:199]
buffer_load_dwordx4 a[112:115],v28,s[28:31],s89 offen offset:3072 sc1
v_mfma_f32_16x16x32_bf16 a[196:199],v[64:67],a[132:135],a[196:199]
v_mfma_f32_16x16x32_bf16 a[200:203],v[64:67],a[136:139],a[200:203]
v_mfma_f32_16x16x32_bf16 a[200:203],v[68:71],a[152:155],a[200:203]
v_mfma_f32_16x16x32_bf16 a[200:203],v[72:75],a[168:171],a[200:203]
v_mfma_f32_16x16x32_bf16 a[200:203],v[76:79],a[184:187],a[200:203]
buffer_load_dwordx4 a[104:107],v28,s[28:31],s88 offen offset:3072 sc1
v_mfma_f32_16x16x32_bf16 a[204:207],v[76:79],a[188:191],a[204:207]
ds_read_b64_tr_b16 v[76:77],v1 offset:30784
ds_read_b64_tr_b16 v[78:79],v1 offset:35904
v_mfma_f32_16x16x32_bf16 a[204:207],v[72:75],a[172:175],a[204:207]
ds_read_b64_tr_b16 v[72:73],v1 offset:20544
v_mfma_f32_16x16x32_bf16 a[204:207],v[68:71],a[156:159],a[204:207]
ds_read_b64_tr_b16 v[68:69],v1 offset:10304
v_mfma_f32_16x16x32_bf16 a[204:207],v[64:67],a[140:143],a[204:207]
ds_read_b64_tr_b16 v[64:65],v1 offset:64
v_mfma_f32_16x16x32_bf16 a[208:211],v[80:83],a[128:131],a[208:211]
buffer_load_dwordx4 a[96:99],v28,s[28:31],s87 offen offset:3072 sc1
v_mfma_f32_16x16x32_bf16 a[208:211],v[84:87],a[144:147],a[208:211]
s_mul_i32 s87,s60,81920
ds_read_b64_tr_b16 v[66:67],v1 offset:5184
ds_read_b64_tr_b16 v[70:71],v1 offset:15424
v_mfma_f32_16x16x32_bf16 a[208:211],v[88:91],a[160:163],a[208:211]
s_add_u32 s87,s87,s66
ds_read_b64_tr_b16 v[74:75],v1 offset:25664
v_add_u32 v1,0xffff6000,v1
v_mfma_f32_16x16x32_bf16 a[208:211],v[92:95],a[176:179],a[208:211]
s_add_u32 s83,s83,s87
s_lshl_b32 s66,s63,6
s_add_u32 s87,s87,s66
v_mfma_f32_16x16x32_bf16 a[212:215],v[92:95],a[180:183],a[212:215]
s_add_u32 s88,s87,20480
s_add_u32 s89,s87,40960
s_add_u32 s90,s87,61440
v_mfma_f32_16x16x32_bf16 a[212:215],v[88:91],a[164:167],a[212:215]
s_add_u32 s66,s77,4096
v_mfma_f32_16x16x32_bf16 a[212:215],v[84:87],a[148:151],a[212:215]
v_mfma_f32_16x16x32_bf16 a[212:215],v[80:83],a[132:135],a[212:215]
v_mfma_f32_16x16x32_bf16 a[216:219],v[80:83],a[136:139],a[216:219]
v_mfma_f32_16x16x32_bf16 a[216:219],v[84:87],a[152:155],a[216:219]
v_mfma_f32_16x16x32_bf16 a[216:219],v[88:91],a[168:171],a[216:219]
v_mfma_f32_16x16x32_bf16 a[216:219],v[92:95],a[184:187],a[216:219]
v_mfma_f32_16x16x32_bf16 a[220:223],v[92:95],a[188:191],a[220:223]
ds_read_b64_tr_b16 v[92:93],v2 offset:30816
ds_read_b64_tr_b16 v[94:95],v2 offset:35936
v_mfma_f32_16x16x32_bf16 a[220:223],v[88:91],a[172:175],a[220:223]
ds_read_b64_tr_b16 v[88:89],v2 offset:20576
v_mfma_f32_16x16x32_bf16 a[220:223],v[84:87],a[156:159],a[220:223]
ds_read_b64_tr_b16 v[84:85],v2 offset:10336
v_mfma_f32_16x16x32_bf16 a[220:223],v[80:83],a[140:143],a[220:223]
s_waitcnt lgkmcnt(4)
ds_read_b64_tr_b16 v[80:81],v2 offset:96
v_mfma_f32_16x16x32_bf16 a[192:195],v[96:99],a[68:71],a[192:195]
s_waitcnt vmcnt(14)
ds_read_b64_tr_b16 v[82:83],v2 offset:5216
ds_read_b64_tr_b16 v[86:87],v2 offset:15456
v_mfma_f32_16x16x32_bf16 a[192:195],v[100:103],a[76:79],a[192:195]
s_waitcnt vmcnt(13)
ds_read_b64_tr_b16 v[90:91],v2 offset:25696
v_add_u32 v2,0xffff6000,v2
v_mfma_f32_16x16x32_bf16 a[192:195],v[104:107],a[84:87],a[192:195]
s_waitcnt vmcnt(12)
v_mfma_f32_16x16x32_bf16 a[192:195],v[108:111],a[92:95],a[192:195]
s_waitcnt vmcnt(11)
v_mfma_f32_16x16x32_bf16 a[196:199],v[108:111],a[124:127],a[196:199]
s_waitcnt vmcnt(10)
v_mfma_f32_16x16x32_bf16 a[196:199],v[104:107],a[116:119],a[196:199]
s_waitcnt vmcnt(9)
v_mfma_f32_16x16x32_bf16 a[196:199],v[100:103],a[108:111],a[196:199]
)A"
R"A(s_waitcnt vmcnt(8)
v_mfma_f32_16x16x32_bf16 a[196:199],v[96:99],a[100:103],a[196:199]
s_waitcnt vmcnt(7)
v_mfma_f32_16x16x32_bf16 a[200:203],v[96:99],a[64:67],a[200:203]
s_waitcnt vmcnt(6)
v_mfma_f32_16x16x32_bf16 a[200:203],v[100:103],a[72:75],a[200:203]
s_waitcnt vmcnt(5)
v_mfma_f32_16x16x32_bf16 a[200:203],v[104:107],a[80:83],a[200:203]
s_waitcnt vmcnt(4)
v_mfma_f32_16x16x32_bf16 a[200:203],v[108:111],a[88:91],a[200:203]
s_waitcnt vmcnt(3)
v_accvgpr_read_b32 v8,a192
v_accvgpr_read_b32 v9,a193
v_mfma_f32_16x16x32_bf16 a[204:207],v[108:111],a[120:123],a[204:207]
s_waitcnt vmcnt(2)
v_accvgpr_read_b32 v10,a194
v_accvgpr_read_b32 v11,a195
v_mfma_f32_16x16x32_bf16 a[204:207],v[104:107],a[112:115],a[204:207]
s_waitcnt vmcnt(1)
v_cvt_pk_bf16_f32 v8,v8,v9
v_cvt_pk_bf16_f32 v9,v10,v11
v_mfma_f32_16x16x32_bf16 a[204:207],v[100:103],a[104:107],a[204:207]
s_waitcnt vmcnt(0)
v_accvgpr_read_b32 v16,a196
v_accvgpr_read_b32 v17,a197
v_mfma_f32_16x16x32_bf16 a[204:207],v[96:99],a[96:99],a[204:207]
v_accvgpr_read_b32 v18,a198
v_accvgpr_read_b32 v19,a199
v_mfma_f32_16x16x32_bf16 a[208:211],v[112:115],a[68:71],a[208:211]
v_cvt_pk_bf16_f32 v16,v16,v17
v_cvt_pk_bf16_f32 v17,v18,v19
v_mfma_f32_16x16x32_bf16 a[208:211],v[116:119],a[76:79],a[208:211]
ds_read_b64_tr_b16 v[96:97],v1 offset:64
ds_read_b64_tr_b16 v[98:99],v1 offset:5184
v_mfma_f32_16x16x32_bf16 a[208:211],v[120:123],a[84:87],a[208:211]
ds_read_b64_tr_b16 v[100:101],v1 offset:10304
v_mfma_f32_16x16x32_bf16 a[208:211],v[124:127],a[92:95],a[208:211]
ds_read_b64_tr_b16 v[102:103],v1 offset:15424
v_mfma_f32_16x16x32_bf16 a[212:215],v[124:127],a[124:127],a[212:215]
ds_read_b64_tr_b16 v[104:105],v1 offset:20544
v_mfma_f32_16x16x32_bf16 a[212:215],v[120:123],a[116:119],a[212:215]
ds_read_b64_tr_b16 v[106:107],v1 offset:25664
ds_read_b64_tr_b16 v[108:109],v1 offset:30784
v_mfma_f32_16x16x32_bf16 a[212:215],v[116:119],a[108:111],a[212:215]
ds_read_b64_tr_b16 v[110:111],v1 offset:35904
v_add_u32 v1,0xa000,v1
v_mfma_f32_16x16x32_bf16 a[212:215],v[112:115],a[100:103],a[212:215]
v_accvgpr_read_b32 v12,a208
v_accvgpr_read_b32 v13,a209
v_mfma_f32_16x16x32_bf16 a[216:219],v[112:115],a[64:67],a[216:219]
v_accvgpr_read_b32 v14,a210
v_accvgpr_read_b32 v15,a211
v_mfma_f32_16x16x32_bf16 a[216:219],v[116:119],a[72:75],a[216:219]
v_cvt_pk_bf16_f32 v10,v12,v13
v_cvt_pk_bf16_f32 v11,v14,v15
v_mfma_f32_16x16x32_bf16 a[216:219],v[120:123],a[80:83],a[216:219]
buffer_store_dwordx4 v[8:11],v6,s[36:39],s74 offen sc1
v_mfma_f32_16x16x32_bf16 a[216:219],v[124:127],a[88:91],a[216:219]
v_accvgpr_read_b32 v20,a212
v_accvgpr_read_b32 v21,a213
v_mfma_f32_16x16x32_bf16 a[220:223],v[124:127],a[120:123],a[220:223]
v_accvgpr_read_b32 v22,a214
v_accvgpr_read_b32 v23,a215
v_mfma_f32_16x16x32_bf16 a[220:223],v[120:123],a[112:115],a[220:223]
v_cvt_pk_bf16_f32 v18,v20,v21
v_cvt_pk_bf16_f32 v19,v22,v23
v_mfma_f32_16x16x32_bf16 a[220:223],v[116:119],a[104:107],a[220:223]
v_accvgpr_read_b32 v8,a200
v_accvgpr_read_b32 v9,a201
v_mfma_f32_16x16x32_bf16 a[220:223],v[112:115],a[96:99],a[220:223]
v_accvgpr_read_b32 v10,a202
v_accvgpr_read_b32 v11,a203
v_mfma_f32_16x16x32_bf16 a[224:227],v[64:67],a[128:131],a[224:227]
v_accvgpr_read_b32 v12,a216
v_accvgpr_read_b32 v13,a217
v_mfma_f32_16x16x32_bf16 a[224:227],v[68:71],a[144:147],a[224:227]
v_accvgpr_read_b32 v14,a218
v_accvgpr_read_b32 v15,a219
v_mfma_f32_16x16x32_bf16 a[224:227],v[72:75],a[160:163],a[224:227]
v_cvt_pk_bf16_f32 v8,v8,v9
v_cvt_pk_bf16_f32 v9,v10,v11
v_mfma_f32_16x16x32_bf16 a[224:227],v[76:79],a[176:179],a[224:227]
v_cvt_pk_bf16_f32 v10,v12,v13
v_cvt_pk_bf16_f32 v11,v14,v15
v_mfma_f32_16x16x32_bf16 a[228:231],v[76:79],a[180:183],a[228:231]
ds_read_b64_tr_b16 v[112:113],v2 offset:96
ds_read_b64_tr_b16 v[114:115],v2 offset:5216
v_mfma_f32_16x16x32_bf16 a[228:231],v[72:75],a[164:167],a[228:231]
buffer_store_dwordx4 v[16:19],v6,s[36:39],s74 offen offset:1024 sc1
v_mfma_f32_16x16x32_bf16 a[228:231],v[68:71],a[148:151],a[228:231]
ds_read_b64_tr_b16 v[116:117],v2 offset:10336
ds_read_b64_tr_b16 v[118:119],v2 offset:15456
v_mfma_f32_16x16x32_bf16 a[228:231],v[64:67],a[132:135],a[228:231]
ds_read_b64_tr_b16 v[120:121],v2 offset:20576
v_accvgpr_read_b32 v16,a204
v_mfma_f32_16x16x32_bf16 a[232:235],v[64:67],a[136:139],a[232:235]
ds_read_b64_tr_b16 v[122:123],v2 offset:25696
v_accvgpr_read_b32 v17,a205
v_mfma_f32_16x16x32_bf16 a[232:235],v[68:71],a[152:155],a[232:235]
ds_read_b64_tr_b16 v[124:125],v2 offset:30816
v_accvgpr_read_b32 v18,a206
v_mfma_f32_16x16x32_bf16 a[232:235],v[72:75],a[168:171],a[232:235]
ds_read_b64_tr_b16 v[126:127],v2 offset:35936
v_accvgpr_read_b32 v19,a207
v_mfma_f32_16x16x32_bf16 a[232:235],v[76:79],a[184:187],a[232:235]
v_accvgpr_read_b32 v20,a220
v_accvgpr_read_b32 v21,a221
v_mfma_f32_16x16x32_bf16 a[236:239],v[76:79],a[188:191],a[236:239]
v_accvgpr_read_b32 v22,a222
v_accvgpr_read_b32 v23,a223
v_mfma_f32_16x16x32_bf16 a[236:239],v[72:75],a[172:175],a[236:239]
v_cvt_pk_bf16_f32 v16,v16,v17
v_cvt_pk_bf16_f32 v17,v18,v19
v_mfma_f32_16x16x32_bf16 a[236:239],v[68:71],a[156:159],a[236:239]
v_cvt_pk_bf16_f32 v18,v20,v21
v_cvt_pk_bf16_f32 v19,v22,v23
v_mfma_f32_16x16x32_bf16 a[236:239],v[64:67],a[140:143],a[236:239]
s_waitcnt lgkmcnt(3)
ds_read_b64_tr_b16 v[64:65],v1 offset:128
ds_read_b64_tr_b16 v[66:67],v1 offset:5248
v_mfma_f32_16x16x32_bf16 a[240:243],v[80:83],a[128:131],a[240:243]
buffer_store_dwordx4 v[8:11],v6,s[36:39],s74 offen offset:2048 sc1
v_mfma_f32_16x16x32_bf16 a[240:243],v[84:87],a[144:147],a[240:243]
ds_read_b64_tr_b16 v[68:69],v1 offset:10368
ds_read_b64_tr_b16 v[70:71],v1 offset:15488
v_mfma_f32_16x16x32_bf16 a[240:243],v[88:91],a[160:163],a[240:243]
ds_read_b64_tr_b16 v[72:73],v1 offset:20608
v_add_u32 v2,0xa000,v2
v_mfma_f32_16x16x32_bf16 a[240:243],v[92:95],a[176:179],a[240:243]
ds_read_b64_tr_b16 v[74:75],v1 offset:25728
v_mfma_f32_16x16x32_bf16 a[244:247],v[92:95],a[180:183],a[244:247]
ds_read_b64_tr_b16 v[76:77],v1 offset:30848
v_mfma_f32_16x16x32_bf16 a[244:247],v[88:91],a[164:167],a[244:247]
ds_read_b64_tr_b16 v[78:79],v1 offset:35968
v_add_u32 v1,0xffff6000,v1
v_mfma_f32_16x16x32_bf16 a[244:247],v[84:87],a[148:151],a[244:247]
v_mfma_f32_16x16x32_bf16 a[244:247],v[80:83],a[132:135],a[244:247]
v_mfma_f32_16x16x32_bf16 a[248:251],v[80:83],a[136:139],a[248:251]
v_mfma_f32_16x16x32_bf16 a[248:251],v[84:87],a[152:155],a[248:251]
v_mfma_f32_16x16x32_bf16 a[248:251],v[88:91],a[168:171],a[248:251]
v_mfma_f32_16x16x32_bf16 a[248:251],v[92:95],a[184:187],a[248:251]
buffer_store_dwordx4 v[16:19],v28,s[36:39],s74 offen offset:3072 sc1
v_mfma_f32_16x16x32_bf16 a[252:255],v[92:95],a[188:191],a[252:255]
ds_read_b64_tr_b16 v[92:93],v2 offset:30880
ds_read_b64_tr_b16 v[94:95],v2 offset:36000
v_mfma_f32_16x16x32_bf16 a[252:255],v[88:91],a[172:175],a[252:255]
ds_read_b64_tr_b16 v[88:89],v2 offset:20640
v_mfma_f32_16x16x32_bf16 a[252:255],v[84:87],a[156:159],a[252:255]
ds_read_b64_tr_b16 v[84:85],v2 offset:10400
v_mfma_f32_16x16x32_bf16 a[252:255],v[80:83],a[140:143],a[252:255]
ds_read_b64_tr_b16 v[80:81],v2 offset:160
v_mfma_f32_16x16x32_bf16 a[224:227],v[96:99],a[68:71],a[224:227]
ds_read_b64_tr_b16 v[82:83],v2 offset:5280
ds_read_b64_tr_b16 v[86:87],v2 offset:15520
v_mfma_f32_16x16x32_bf16 a[224:227],v[100:103],a[76:79],a[224:227]
ds_read_b64_tr_b16 v[90:91],v2 offset:25760
v_add_u32 v2,0xffff6000,v2
v_mfma_f32_16x16x32_bf16 a[224:227],v[104:107],a[84:87],a[224:227]
v_mfma_f32_16x16x32_bf16 a[224:227],v[108:111],a[92:95],a[224:227]
v_mfma_f32_16x16x32_bf16 a[228:231],v[108:111],a[124:127],a[228:231]
v_mfma_f32_16x16x32_bf16 a[228:231],v[104:107],a[116:119],a[228:231]
v_mfma_f32_16x16x32_bf16 a[228:231],v[100:103],a[108:111],a[228:231]
v_mfma_f32_16x16x32_bf16 a[228:231],v[96:99],a[100:103],a[228:231]
v_mfma_f32_16x16x32_bf16 a[232:235],v[96:99],a[64:67],a[232:235]
v_mfma_f32_16x16x32_bf16 a[232:235],v[100:103],a[72:75],a[232:235]
v_mfma_f32_16x16x32_bf16 a[232:235],v[104:107],a[80:83],a[232:235]
v_mfma_f32_16x16x32_bf16 a[232:235],v[108:111],a[88:91],a[232:235]
v_accvgpr_read_b32 v8,a224
v_accvgpr_read_b32 v9,a225
v_mfma_f32_16x16x32_bf16 a[236:239],v[108:111],a[120:123],a[236:239]
v_accvgpr_read_b32 v10,a226
v_accvgpr_read_b32 v11,a227
v_mfma_f32_16x16x32_bf16 a[236:239],v[104:107],a[112:115],a[236:239]
v_cvt_pk_bf16_f32 v8,v8,v9
v_cvt_pk_bf16_f32 v9,v10,v11
v_mfma_f32_16x16x32_bf16 a[236:239],v[100:103],a[104:107],a[236:239]
v_accvgpr_read_b32 v16,a228
v_accvgpr_read_b32 v17,a229
v_mfma_f32_16x16x32_bf16 a[236:239],v[96:99],a[96:99],a[236:239]
v_accvgpr_read_b32 v18,a230
v_accvgpr_read_b32 v19,a231
v_mfma_f32_16x16x32_bf16 a[240:243],v[112:115],a[68:71],a[240:243]
v_cvt_pk_bf16_f32 v16,v16,v17
v_cvt_pk_bf16_f32 v17,v18,v19
v_mfma_f32_16x16x32_bf16 a[240:243],v[116:119],a[76:79],a[240:243]
s_waitcnt lgkmcnt(0)
ds_read_b64_tr_b16 v[96:97],v1 offset:128
ds_read_b64_tr_b16 v[98:99],v1 offset:5248
v_mfma_f32_16x16x32_bf16 a[240:243],v[120:123],a[84:87],a[240:243]
ds_read_b64_tr_b16 v[100:101],v1 offset:10368
v_mfma_f32_16x16x32_bf16 a[240:243],v[124:127],a[92:95],a[240:243]
ds_read_b64_tr_b16 v[102:103],v1 offset:15488
v_mfma_f32_16x16x32_bf16 a[244:247],v[124:127],a[124:127],a[244:247]
ds_read_b64_tr_b16 v[104:105],v1 offset:20608
v_mfma_f32_16x16x32_bf16 a[244:247],v[120:123],a[116:119],a[244:247]
ds_read_b64_tr_b16 v[106:107],v1 offset:25728
ds_read_b64_tr_b16 v[108:109],v1 offset:30848
v_mfma_f32_16x16x32_bf16 a[244:247],v[116:119],a[108:111],a[244:247]
ds_read_b64_tr_b16 v[110:111],v1 offset:35968
v_add_u32 v1,0xa000,v1
v_mfma_f32_16x16x32_bf16 a[244:247],v[112:115],a[100:103],a[244:247]
v_accvgpr_read_b32 v12,a240
v_accvgpr_read_b32 v13,a241
v_mfma_f32_16x16x32_bf16 a[248:251],v[112:115],a[64:67],a[248:251]
v_accvgpr_read_b32 v14,a242
v_accvgpr_read_b32 v15,a243
v_mfma_f32_16x16x32_bf16 a[248:251],v[116:119],a[72:75],a[248:251]
v_cvt_pk_bf16_f32 v10,v12,v13
v_cvt_pk_bf16_f32 v11,v14,v15
v_mfma_f32_16x16x32_bf16 a[248:251],v[120:123],a[80:83],a[248:251]
buffer_store_dwordx4 v[8:11],v6,s[36:39],s75 offen sc1
v_mfma_f32_16x16x32_bf16 a[248:251],v[124:127],a[88:91],a[248:251]
v_accvgpr_read_b32 v20,a244
v_accvgpr_read_b32 v21,a245
v_mfma_f32_16x16x32_bf16 a[252:255],v[124:127],a[120:123],a[252:255]
v_accvgpr_read_b32 v22,a246
v_accvgpr_read_b32 v23,a247
v_mfma_f32_16x16x32_bf16 a[252:255],v[120:123],a[112:115],a[252:255]
v_cvt_pk_bf16_f32 v18,v20,v21
v_cvt_pk_bf16_f32 v19,v22,v23
v_mfma_f32_16x16x32_bf16 a[252:255],v[116:119],a[104:107],a[252:255]
v_accvgpr_read_b32 v8,a232
v_accvgpr_read_b32 v9,a233
v_mfma_f32_16x16x32_bf16 a[252:255],v[112:115],a[96:99],a[252:255]
v_accvgpr_read_b32 v10,a234
v_accvgpr_read_b32 v11,a235
v_mfma_f32_16x16x32_bf16 a[0:3],v[64:67],a[128:131],a[0:3]
v_accvgpr_read_b32 v12,a248
v_accvgpr_read_b32 v13,a249
v_mfma_f32_16x16x32_bf16 a[0:3],v[68:71],a[144:147],a[0:3]
v_accvgpr_read_b32 v14,a250
v_accvgpr_read_b32 v15,a251
v_mfma_f32_16x16x32_bf16 a[0:3],v[72:75],a[160:163],a[0:3]
v_cvt_pk_bf16_f32 v8,v8,v9
v_cvt_pk_bf16_f32 v9,v10,v11
v_mfma_f32_16x16x32_bf16 a[0:3],v[76:79],a[176:179],a[0:3]
v_cvt_pk_bf16_f32 v10,v12,v13
v_cvt_pk_bf16_f32 v11,v14,v15
v_mfma_f32_16x16x32_bf16 a[4:7],v[76:79],a[180:183],a[4:7]
ds_read_b64_tr_b16 v[112:113],v2 offset:160
ds_read_b64_tr_b16 v[114:115],v2 offset:5280
v_mfma_f32_16x16x32_bf16 a[4:7],v[72:75],a[164:167],a[4:7]
buffer_store_dwordx4 v[16:19],v6,s[36:39],s75 offen offset:1024 sc1
v_mfma_f32_16x16x32_bf16 a[4:7],v[68:71],a[148:151],a[4:7]
ds_read_b64_tr_b16 v[116:117],v2 offset:10400
ds_read_b64_tr_b16 v[118:119],v2 offset:15520
v_mfma_f32_16x16x32_bf16 a[4:7],v[64:67],a[132:135],a[4:7]
ds_read_b64_tr_b16 v[120:121],v2 offset:20640
v_accvgpr_read_b32 v16,a236
v_mfma_f32_16x16x32_bf16 a[8:11],v[64:67],a[136:139],a[8:11]
ds_read_b64_tr_b16 v[122:123],v2 offset:25760
v_accvgpr_read_b32 v17,a237
v_mfma_f32_16x16x32_bf16 a[8:11],v[68:71],a[152:155],a[8:11]
ds_read_b64_tr_b16 v[124:125],v2 offset:30880
v_accvgpr_read_b32 v18,a238
v_mfma_f32_16x16x32_bf16 a[8:11],v[72:75],a[168:171],a[8:11]
ds_read_b64_tr_b16 v[126:127],v2 offset:36000
v_accvgpr_read_b32 v19,a239
)A"
R"A(v_mfma_f32_16x16x32_bf16 a[8:11],v[76:79],a[184:187],a[8:11]
v_accvgpr_read_b32 v20,a252
v_accvgpr_read_b32 v21,a253
v_mfma_f32_16x16x32_bf16 a[12:15],v[76:79],a[188:191],a[12:15]
v_accvgpr_read_b32 v22,a254
v_accvgpr_read_b32 v23,a255
v_mfma_f32_16x16x32_bf16 a[12:15],v[72:75],a[172:175],a[12:15]
v_cvt_pk_bf16_f32 v16,v16,v17
v_cvt_pk_bf16_f32 v17,v18,v19
v_mfma_f32_16x16x32_bf16 a[12:15],v[68:71],a[156:159],a[12:15]
v_cvt_pk_bf16_f32 v18,v20,v21
v_cvt_pk_bf16_f32 v19,v22,v23
v_mfma_f32_16x16x32_bf16 a[12:15],v[64:67],a[140:143],a[12:15]
ds_read_b64_tr_b16 v[64:65],v1 offset:192
ds_read_b64_tr_b16 v[66:67],v1 offset:5312
v_mfma_f32_16x16x32_bf16 a[16:19],v[80:83],a[128:131],a[16:19]
buffer_store_dwordx4 v[8:11],v6,s[36:39],s75 offen offset:2048 sc1
v_mfma_f32_16x16x32_bf16 a[16:19],v[84:87],a[144:147],a[16:19]
ds_read_b64_tr_b16 v[68:69],v1 offset:10432
ds_read_b64_tr_b16 v[70:71],v1 offset:15552
v_mfma_f32_16x16x32_bf16 a[16:19],v[88:91],a[160:163],a[16:19]
ds_read_b64_tr_b16 v[72:73],v1 offset:20672
v_add_u32 v2,0xa000,v2
v_mfma_f32_16x16x32_bf16 a[16:19],v[92:95],a[176:179],a[16:19]
ds_read_b64_tr_b16 v[74:75],v1 offset:25792
v_mfma_f32_16x16x32_bf16 a[20:23],v[92:95],a[180:183],a[20:23]
ds_read_b64_tr_b16 v[76:77],v1 offset:30912
v_mfma_f32_16x16x32_bf16 a[20:23],v[88:91],a[164:167],a[20:23]
ds_read_b64_tr_b16 v[78:79],v1 offset:36032
v_add_u32 v1,0xffff6000,v1
v_mfma_f32_16x16x32_bf16 a[20:23],v[84:87],a[148:151],a[20:23]
v_mfma_f32_16x16x32_bf16 a[20:23],v[80:83],a[132:135],a[20:23]
v_mfma_f32_16x16x32_bf16 a[24:27],v[80:83],a[136:139],a[24:27]
v_mfma_f32_16x16x32_bf16 a[24:27],v[84:87],a[152:155],a[24:27]
v_mfma_f32_16x16x32_bf16 a[24:27],v[88:91],a[168:171],a[24:27]
v_mfma_f32_16x16x32_bf16 a[24:27],v[92:95],a[184:187],a[24:27]
buffer_store_dwordx4 v[16:19],v28,s[36:39],s75 offen offset:3072 sc1
v_mfma_f32_16x16x32_bf16 a[28:31],v[92:95],a[188:191],a[28:31]
ds_read_b64_tr_b16 v[92:93],v2 offset:30944
ds_read_b64_tr_b16 v[94:95],v2 offset:36064
v_mfma_f32_16x16x32_bf16 a[28:31],v[88:91],a[172:175],a[28:31]
ds_read_b64_tr_b16 v[88:89],v2 offset:20704
v_mfma_f32_16x16x32_bf16 a[28:31],v[84:87],a[156:159],a[28:31]
ds_read_b64_tr_b16 v[84:85],v2 offset:10464
v_mfma_f32_16x16x32_bf16 a[28:31],v[80:83],a[140:143],a[28:31]
s_waitcnt lgkmcnt(4)
ds_read_b64_tr_b16 v[80:81],v2 offset:224
v_mfma_f32_16x16x32_bf16 a[0:3],v[96:99],a[68:71],a[0:3]
ds_read_b64_tr_b16 v[82:83],v2 offset:5344
ds_read_b64_tr_b16 v[86:87],v2 offset:15584
v_mfma_f32_16x16x32_bf16 a[0:3],v[100:103],a[76:79],a[0:3]
ds_read_b64_tr_b16 v[90:91],v2 offset:25824
v_add_u32 v2,0xffff6000,v2
v_mfma_f32_16x16x32_bf16 a[0:3],v[104:107],a[84:87],a[0:3]
v_mfma_f32_16x16x32_bf16 a[0:3],v[108:111],a[92:95],a[0:3]
v_mfma_f32_16x16x32_bf16 a[4:7],v[108:111],a[124:127],a[4:7]
v_mfma_f32_16x16x32_bf16 a[4:7],v[104:107],a[116:119],a[4:7]
v_mfma_f32_16x16x32_bf16 a[4:7],v[100:103],a[108:111],a[4:7]
v_mfma_f32_16x16x32_bf16 a[4:7],v[96:99],a[100:103],a[4:7]
v_mfma_f32_16x16x32_bf16 a[8:11],v[96:99],a[64:67],a[8:11]
v_mfma_f32_16x16x32_bf16 a[8:11],v[100:103],a[72:75],a[8:11]
v_mfma_f32_16x16x32_bf16 a[8:11],v[104:107],a[80:83],a[8:11]
v_mfma_f32_16x16x32_bf16 a[8:11],v[108:111],a[88:91],a[8:11]
v_accvgpr_read_b32 v8,a0
v_accvgpr_read_b32 v9,a1
v_mfma_f32_16x16x32_bf16 a[12:15],v[108:111],a[120:123],a[12:15]
v_accvgpr_read_b32 v10,a2
v_accvgpr_read_b32 v11,a3
v_mfma_f32_16x16x32_bf16 a[12:15],v[104:107],a[112:115],a[12:15]
v_cvt_pk_bf16_f32 v8,v8,v9
v_cvt_pk_bf16_f32 v9,v10,v11
v_mfma_f32_16x16x32_bf16 a[12:15],v[100:103],a[104:107],a[12:15]
v_accvgpr_read_b32 v16,a4
v_accvgpr_read_b32 v17,a5
v_mfma_f32_16x16x32_bf16 a[12:15],v[96:99],a[96:99],a[12:15]
v_accvgpr_read_b32 v18,a6
v_accvgpr_read_b32 v19,a7
v_mfma_f32_16x16x32_bf16 a[16:19],v[112:115],a[68:71],a[16:19]
v_cvt_pk_bf16_f32 v16,v16,v17
v_cvt_pk_bf16_f32 v17,v18,v19
v_mfma_f32_16x16x32_bf16 a[16:19],v[116:119],a[76:79],a[16:19]
ds_read_b64_tr_b16 v[96:97],v1 offset:192
ds_read_b64_tr_b16 v[98:99],v1 offset:5312
v_mfma_f32_16x16x32_bf16 a[16:19],v[120:123],a[84:87],a[16:19]
ds_read_b64_tr_b16 v[100:101],v1 offset:10432
v_mfma_f32_16x16x32_bf16 a[16:19],v[124:127],a[92:95],a[16:19]
ds_read_b64_tr_b16 v[102:103],v1 offset:15552
v_mfma_f32_16x16x32_bf16 a[20:23],v[124:127],a[124:127],a[20:23]
ds_read_b64_tr_b16 v[104:105],v1 offset:20672
v_mfma_f32_16x16x32_bf16 a[20:23],v[120:123],a[116:119],a[20:23]
ds_read_b64_tr_b16 v[106:107],v1 offset:25792
ds_read_b64_tr_b16 v[108:109],v1 offset:30912
v_mfma_f32_16x16x32_bf16 a[20:23],v[116:119],a[108:111],a[20:23]
ds_read_b64_tr_b16 v[110:111],v1 offset:36032
v_add_u32 v1,0x14000,v1
v_mfma_f32_16x16x32_bf16 a[20:23],v[112:115],a[100:103],a[20:23]
v_accvgpr_read_b32 v12,a16
v_accvgpr_read_b32 v13,a17
v_mfma_f32_16x16x32_bf16 a[24:27],v[112:115],a[64:67],a[24:27]
v_accvgpr_read_b32 v14,a18
v_accvgpr_read_b32 v15,a19
v_mfma_f32_16x16x32_bf16 a[24:27],v[116:119],a[72:75],a[24:27]
v_cvt_pk_bf16_f32 v10,v12,v13
v_cvt_pk_bf16_f32 v11,v14,v15
s_nop 0
L_pd23_%=:
s_cbranch_execz L_trtg2S_%=
v_mfma_f32_16x16x32_bf16 a[24:27],v[120:123],a[80:83],a[24:27]
buffer_store_dwordx4 v[8:11],v6,s[36:39],s76 offen sc1
v_mfma_f32_16x16x32_bf16 a[24:27],v[124:127],a[88:91],a[24:27]
v_accvgpr_read_b32 v20,a20
v_accvgpr_read_b32 v21,a21
v_mfma_f32_16x16x32_bf16 a[28:31],v[124:127],a[120:123],a[28:31]
v_accvgpr_read_b32 v22,a22
v_accvgpr_read_b32 v23,a23
v_mfma_f32_16x16x32_bf16 a[28:31],v[120:123],a[112:115],a[28:31]
v_cvt_pk_bf16_f32 v18,v20,v21
v_cvt_pk_bf16_f32 v19,v22,v23
v_mfma_f32_16x16x32_bf16 a[28:31],v[116:119],a[104:107],a[28:31]
v_accvgpr_read_b32 v8,a8
v_accvgpr_read_b32 v9,a9
v_mfma_f32_16x16x32_bf16 a[28:31],v[112:115],a[96:99],a[28:31]
v_accvgpr_read_b32 v10,a10
v_accvgpr_read_b32 v11,a11
v_mfma_f32_16x16x32_bf16 v[128:131],v[64:67],a[128:131],v[128:131]
v_accvgpr_read_b32 v12,a24
v_accvgpr_read_b32 v13,a25
v_mfma_f32_16x16x32_bf16 v[128:131],v[68:71],a[144:147],v[128:131]
v_accvgpr_read_b32 v14,a26
v_accvgpr_read_b32 v15,a27
v_mfma_f32_16x16x32_bf16 v[128:131],v[72:75],a[160:163],v[128:131]
v_cvt_pk_bf16_f32 v8,v8,v9
v_cvt_pk_bf16_f32 v9,v10,v11
v_mfma_f32_16x16x32_bf16 v[128:131],v[76:79],a[176:179],v[128:131]
v_cvt_pk_bf16_f32 v10,v12,v13
v_cvt_pk_bf16_f32 v11,v14,v15
v_mfma_f32_16x16x32_bf16 v[136:139],v[76:79],a[180:183],v[136:139]
ds_read_b64_tr_b16 v[112:113],v2 offset:224
ds_read_b64_tr_b16 v[114:115],v2 offset:5344
v_mfma_f32_16x16x32_bf16 v[136:139],v[72:75],a[164:167],v[136:139]
buffer_store_dwordx4 v[16:19],v6,s[36:39],s76 offen offset:1024 sc1
v_mfma_f32_16x16x32_bf16 v[136:139],v[68:71],a[148:151],v[136:139]
ds_read_b64_tr_b16 v[116:117],v2 offset:10464
ds_read_b64_tr_b16 v[118:119],v2 offset:15584
v_mfma_f32_16x16x32_bf16 v[136:139],v[64:67],a[132:135],v[136:139]
ds_read_b64_tr_b16 v[120:121],v2 offset:20704
v_accvgpr_read_b32 v16,a12
v_mfma_f32_16x16x32_bf16 v[144:147],v[64:67],a[136:139],v[144:147]
ds_read_b64_tr_b16 v[122:123],v2 offset:25824
v_accvgpr_read_b32 v17,a13
v_mfma_f32_16x16x32_bf16 v[144:147],v[68:71],a[152:155],v[144:147]
ds_read_b64_tr_b16 v[124:125],v2 offset:30944
v_accvgpr_read_b32 v18,a14
v_mfma_f32_16x16x32_bf16 v[144:147],v[72:75],a[168:171],v[144:147]
ds_read_b64_tr_b16 v[126:127],v2 offset:36064
v_accvgpr_read_b32 v19,a15
v_mfma_f32_16x16x32_bf16 v[144:147],v[76:79],a[184:187],v[144:147]
v_accvgpr_read_b32 v20,a28
v_accvgpr_read_b32 v21,a29
v_mfma_f32_16x16x32_bf16 v[152:155],v[76:79],a[188:191],v[152:155]
v_accvgpr_read_b32 v22,a30
v_accvgpr_read_b32 v23,a31
v_mfma_f32_16x16x32_bf16 v[152:155],v[72:75],a[172:175],v[152:155]
v_cvt_pk_bf16_f32 v16,v16,v17
v_cvt_pk_bf16_f32 v17,v18,v19
v_mfma_f32_16x16x32_bf16 v[152:155],v[68:71],a[156:159],v[152:155]
v_cvt_pk_bf16_f32 v18,v20,v21
v_cvt_pk_bf16_f32 v19,v22,v23
v_mfma_f32_16x16x32_bf16 v[152:155],v[64:67],a[140:143],v[152:155]
s_waitcnt lgkmcnt(3)
ds_read_b64_tr_b16 v[64:65],v1 offset:256
ds_read_b64_tr_b16 v[66:67],v1 offset:5376
v_mfma_f32_16x16x32_bf16 v[160:163],v[80:83],a[128:131],v[160:163]
buffer_store_dwordx4 v[8:11],v6,s[36:39],s76 offen offset:2048 sc1
v_mfma_f32_16x16x32_bf16 v[160:163],v[84:87],a[144:147],v[160:163]
ds_read_b64_tr_b16 v[68:69],v1 offset:10496
ds_read_b64_tr_b16 v[70:71],v1 offset:15616
v_mfma_f32_16x16x32_bf16 v[160:163],v[88:91],a[160:163],v[160:163]
ds_read_b64_tr_b16 v[72:73],v1 offset:20736
v_add_u32 v2,0x14000,v2
v_mfma_f32_16x16x32_bf16 v[160:163],v[92:95],a[176:179],v[160:163]
ds_read_b64_tr_b16 v[74:75],v1 offset:25856
v_mov_b32 v20,s52
v_mfma_f32_16x16x32_bf16 v[168:171],v[92:95],a[180:183],v[168:171]
ds_read_b64_tr_b16 v[76:77],v1 offset:30976
v_mov_b32 v21,s53
v_mfma_f32_16x16x32_bf16 v[168:171],v[88:91],a[164:167],v[168:171]
ds_read_b64_tr_b16 v[78:79],v1 offset:36096
v_add_u32 v1,0xffff6000,v1
v_mfma_f32_16x16x32_bf16 v[168:171],v[84:87],a[148:151],v[168:171]
v_lshlrev_b32 v22,20,v6
v_mfma_f32_16x16x32_bf16 v[168:171],v[80:83],a[132:135],v[168:171]
v_mfma_f32_16x16x32_bf16 v[176:179],v[80:83],a[136:139],v[176:179]
v_mfma_f32_16x16x32_bf16 v[176:179],v[84:87],a[152:155],v[176:179]
v_mfma_f32_16x16x32_bf16 v[176:179],v[88:91],a[168:171],v[176:179]
v_mfma_f32_16x16x32_bf16 v[176:179],v[92:95],a[184:187],v[176:179]
buffer_store_dwordx4 v[16:19],v28,s[36:39],s76 offen offset:3072 sc1
v_mfma_f32_16x16x32_bf16 v[184:187],v[92:95],a[188:191],v[184:187]
ds_read_b64_tr_b16 v[92:93],v2 offset:31008
ds_read_b64_tr_b16 v[94:95],v2 offset:36128
v_mfma_f32_16x16x32_bf16 v[184:187],v[88:91],a[172:175],v[184:187]
ds_read_b64_tr_b16 v[88:89],v2 offset:20768
v_mfma_f32_16x16x32_bf16 v[184:187],v[84:87],a[156:159],v[184:187]
ds_read_b64_tr_b16 v[84:85],v2 offset:10528
v_mfma_f32_16x16x32_bf16 v[184:187],v[80:83],a[140:143],v[184:187]
ds_read_b64_tr_b16 v[80:81],v2 offset:288
v_mfma_f32_16x16x32_bf16 v[128:131],v[96:99],a[68:71],v[128:131]
ds_read_b64_tr_b16 v[82:83],v2 offset:5408
ds_read_b64_tr_b16 v[86:87],v2 offset:15648
v_mfma_f32_16x16x32_bf16 v[128:131],v[100:103],a[76:79],v[128:131]
ds_read_b64_tr_b16 v[90:91],v2 offset:25888
v_add_u32 v2,0xffff6000,v2
v_mfma_f32_16x16x32_bf16 v[128:131],v[104:107],a[84:87],v[128:131]
v_mfma_f32_16x16x32_bf16 v[128:131],v[108:111],a[92:95],v[128:131]
v_mfma_f32_16x16x32_bf16 v[136:139],v[108:111],a[124:127],v[136:139]
v_mfma_f32_16x16x32_bf16 v[136:139],v[104:107],a[116:119],v[136:139]
v_mfma_f32_16x16x32_bf16 v[136:139],v[100:103],a[108:111],v[136:139]
v_mfma_f32_16x16x32_bf16 v[136:139],v[96:99],a[100:103],v[136:139]
v_mfma_f32_16x16x32_bf16 v[144:147],v[96:99],a[64:67],v[144:147]
v_mfma_f32_16x16x32_bf16 v[144:147],v[100:103],a[72:75],v[144:147]
v_mfma_f32_16x16x32_bf16 v[144:147],v[104:107],a[80:83],v[144:147]
v_mfma_f32_16x16x32_bf16 v[144:147],v[108:111],a[88:91],v[144:147]
v_cvt_pk_bf16_f32 v128,v128,v129
v_cvt_pk_bf16_f32 v129,v130,v131
v_mfma_f32_16x16x32_bf16 v[152:155],v[108:111],a[120:123],v[152:155]
ds_read_b64_tr_b16 v[108:109],v1 offset:30976
ds_read_b64_tr_b16 v[110:111],v1 offset:36096
v_mfma_f32_16x16x32_bf16 v[152:155],v[104:107],a[112:115],v[152:155]
v_cvt_pk_bf16_f32 v136,v136,v137
v_cvt_pk_bf16_f32 v137,v138,v139
v_mfma_f32_16x16x32_bf16 v[152:155],v[100:103],a[104:107],v[152:155]
v_cvt_pk_bf16_f32 v144,v144,v145
v_cvt_pk_bf16_f32 v145,v146,v147
v_mfma_f32_16x16x32_bf16 v[152:155],v[96:99],a[96:99],v[152:155]
ds_read_b64_tr_b16 v[96:97],v1 offset:256
ds_read_b64_tr_b16 v[98:99],v1 offset:5376
v_mfma_f32_16x16x32_bf16 v[160:163],v[112:115],a[68:71],v[160:163]
ds_read_b64_tr_b16 v[100:101],v1 offset:10496
v_mfma_f32_16x16x32_bf16 v[160:163],v[116:119],a[76:79],v[160:163]
s_waitcnt lgkmcnt(5)
ds_read_b64_tr_b16 v[102:103],v1 offset:15616
v_mfma_f32_16x16x32_bf16 v[160:163],v[120:123],a[84:87],v[160:163]
ds_read_b64_tr_b16 v[104:105],v1 offset:20736
v_mfma_f32_16x16x32_bf16 v[160:163],v[124:127],a[92:95],v[160:163]
v_cvt_pk_bf16_f32 v152,v152,v153
v_cvt_pk_bf16_f32 v153,v154,v155
v_mfma_f32_16x16x32_bf16 v[168:171],v[124:127],a[124:127],v[168:171]
ds_read_b64_tr_b16 v[106:107],v1 offset:25856
v_add_u32 v1,0xffff6000,v1
v_mfma_f32_16x16x32_bf16 v[168:171],v[120:123],a[116:119],v[168:171]
)A"
R"A(v_mfma_f32_16x16x32_bf16 v[168:171],v[116:119],a[108:111],v[168:171]
v_mfma_f32_16x16x32_bf16 v[168:171],v[112:115],a[100:103],v[168:171]
v_cvt_pk_bf16_f32 v130,v160,v161
v_cvt_pk_bf16_f32 v131,v162,v163
v_mfma_f32_16x16x32_bf16 v[176:179],v[112:115],a[64:67],v[176:179]
buffer_store_dwordx4 v[128:131],v6,s[36:39],s79 offen sc1
v_mfma_f32_16x16x32_bf16 v[176:179],v[116:119],a[72:75],v[176:179]
v_mfma_f32_16x16x32_bf16 v[176:179],v[120:123],a[80:83],v[176:179]
v_mfma_f32_16x16x32_bf16 v[176:179],v[124:127],a[88:91],v[176:179]
v_mfma_f32_16x16x32_bf16 v[184:187],v[124:127],a[120:123],v[184:187]
v_cvt_pk_bf16_f32 v138,v168,v169
v_cvt_pk_bf16_f32 v139,v170,v171
v_mfma_f32_16x16x32_bf16 v[184:187],v[120:123],a[112:115],v[184:187]
buffer_store_dwordx4 v[136:139],v6,s[36:39],s80 offen sc1
v_mfma_f32_16x16x32_bf16 v[184:187],v[116:119],a[104:107],v[184:187]
ds_read_b64_tr_b16 v[116:117],v2 offset:10528
ds_read_b64_tr_b16 v[118:119],v2 offset:15648
v_mfma_f32_16x16x32_bf16 v[184:187],v[112:115],a[96:99],v[184:187]
v_cvt_pk_bf16_f32 v146,v176,v177
v_cvt_pk_bf16_f32 v147,v178,v179
s_waitcnt vmcnt(6)
s_barrier
v_mfma_f32_16x16x32_bf16 v[192:195],v[64:67],v[32:35],v[192:195]
buffer_store_dwordx4 v[144:147],v6,s[36:39],s81 offen sc1
v_mfma_f32_16x16x32_bf16 v[192:195],v[68:71],v[36:39],v[192:195]
ds_read_b64_tr_b16 v[112:113],v2 offset:288
ds_read_b64_tr_b16 v[114:115],v2 offset:5408
v_mfma_f32_16x16x32_bf16 v[192:195],v[72:75],v[40:43],v[192:195]
buffer_store_dwordx2 v[58:59],v60,s[44:47],s18 offen offset:2312 sc1
v_mfma_f32_16x16x32_bf16 v[192:195],v[76:79],v[44:47],v[192:195]
v_cvt_pk_bf16_f32 v154,v184,v185
v_cvt_pk_bf16_f32 v155,v186,v187
v_mfma_f32_16x16x32_bf16 v[200:203],v[76:79],v[180:183],v[200:203]
buffer_store_dwordx4 v[152:155],v28,s[36:39],s82 offen sc1
v_mfma_f32_16x16x32_bf16 v[200:203],v[72:75],v[164:167],v[200:203]
ds_read_b64_tr_b16 v[120:121],v2 offset:20768
ds_read_b64_tr_b16 v[122:123],v2 offset:25888
v_mfma_f32_16x16x32_bf16 v[200:203],v[68:71],v[148:151],v[200:203]
ds_read_b64_tr_b16 v[124:125],v2 offset:31008
v_mfma_f32_16x16x32_bf16 v[200:203],v[64:67],v[132:135],v[200:203]
ds_read_b64_tr_b16 v[126:127],v2 offset:36128
v_add_u32 v2,0xffff6000,v2
v_mfma_f32_16x16x32_bf16 v[208:211],v[64:67],v[196:199],v[208:211]
v_mfma_f32_16x16x32_bf16 v[208:211],v[68:71],v[212:215],v[208:211]
v_mfma_f32_16x16x32_bf16 v[208:211],v[72:75],v[228:231],v[208:211]
v_mfma_f32_16x16x32_bf16 v[208:211],v[76:79],v[244:247],v[208:211]
v_mfma_f32_16x16x32_bf16 v[216:219],v[76:79],v[188:191],v[216:219]
ds_read_b64_tr_b16 v[76:77],v1 offset:30976
ds_read_b64_tr_b16 v[78:79],v1 offset:36096
v_mfma_f32_16x16x32_bf16 v[216:219],v[72:75],v[172:175],v[216:219]
ds_read_b64_tr_b16 v[72:73],v1 offset:20736
v_mfma_f32_16x16x32_bf16 v[216:219],v[68:71],v[156:159],v[216:219]
ds_read_b64_tr_b16 v[68:69],v1 offset:10496
v_mfma_f32_16x16x32_bf16 v[216:219],v[64:67],v[140:143],v[216:219]
ds_read_b64_tr_b16 v[64:65],v1 offset:256
v_mfma_f32_16x16x32_bf16 v[224:227],v[80:83],v[32:35],v[224:227]
ds_read_b64_tr_b16 v[66:67],v1 offset:5376
ds_read_b64_tr_b16 v[70:71],v1 offset:15616
v_mfma_f32_16x16x32_bf16 v[224:227],v[84:87],v[36:39],v[224:227]
ds_read_b64_tr_b16 v[74:75],v1 offset:25856
v_mfma_f32_16x16x32_bf16 v[224:227],v[88:91],v[40:43],v[224:227]
v_mfma_f32_16x16x32_bf16 v[224:227],v[92:95],v[44:47],v[224:227]
v_mfma_f32_16x16x32_bf16 v[232:235],v[92:95],v[180:183],v[232:235]
v_mfma_f32_16x16x32_bf16 v[232:235],v[88:91],v[164:167],v[232:235]
v_mfma_f32_16x16x32_bf16 v[232:235],v[84:87],v[148:151],v[232:235]
v_mfma_f32_16x16x32_bf16 v[232:235],v[80:83],v[132:135],v[232:235]
v_mfma_f32_16x16x32_bf16 v[240:243],v[80:83],v[196:199],v[240:243]
v_mfma_f32_16x16x32_bf16 v[240:243],v[84:87],v[212:215],v[240:243]
v_mfma_f32_16x16x32_bf16 v[240:243],v[88:91],v[228:231],v[240:243]
v_mfma_f32_16x16x32_bf16 v[240:243],v[92:95],v[244:247],v[240:243]
v_mfma_f32_16x16x32_bf16 v[248:251],v[92:95],v[188:191],v[248:251]
ds_read_b64_tr_b16 v[92:93],v2 offset:31008
ds_read_b64_tr_b16 v[94:95],v2 offset:36128
v_mfma_f32_16x16x32_bf16 v[248:251],v[88:91],v[172:175],v[248:251]
ds_read_b64_tr_b16 v[88:89],v2 offset:20768
v_mfma_f32_16x16x32_bf16 v[248:251],v[84:87],v[156:159],v[248:251]
ds_read_b64_tr_b16 v[84:85],v2 offset:10528
v_mfma_f32_16x16x32_bf16 v[248:251],v[80:83],v[140:143],v[248:251]
s_waitcnt lgkmcnt(4)
ds_read_b64_tr_b16 v[80:81],v2 offset:288
v_mfma_f32_16x16x32_bf16 v[192:195],v[96:99],a[128:131],v[192:195]
ds_read_b64_tr_b16 v[82:83],v2 offset:5408
ds_read_b64_tr_b16 v[86:87],v2 offset:15648
v_mfma_f32_16x16x32_bf16 v[192:195],v[100:103],a[144:147],v[192:195]
ds_read_b64_tr_b16 v[90:91],v2 offset:25888
v_mfma_f32_16x16x32_bf16 v[192:195],v[104:107],a[160:163],v[192:195]
v_mfma_f32_16x16x32_bf16 v[192:195],v[108:111],a[176:179],v[192:195]
v_mfma_f32_16x16x32_bf16 v[200:203],v[108:111],a[180:183],v[200:203]
v_mfma_f32_16x16x32_bf16 v[200:203],v[104:107],a[164:167],v[200:203]
v_mfma_f32_16x16x32_bf16 v[200:203],v[100:103],a[148:151],v[200:203]
v_mfma_f32_16x16x32_bf16 v[200:203],v[96:99],a[132:135],v[200:203]
v_mfma_f32_16x16x32_bf16 v[208:211],v[96:99],a[136:139],v[208:211]
v_mfma_f32_16x16x32_bf16 v[208:211],v[100:103],a[152:155],v[208:211]
v_mfma_f32_16x16x32_bf16 v[208:211],v[104:107],a[168:171],v[208:211]
v_mfma_f32_16x16x32_bf16 v[208:211],v[108:111],a[184:187],v[208:211]
v_mfma_f32_16x16x32_bf16 v[216:219],v[108:111],a[188:191],v[216:219]
v_mfma_f32_16x16x32_bf16 v[216:219],v[104:107],a[172:175],v[216:219]
v_mfma_f32_16x16x32_bf16 v[216:219],v[100:103],a[156:159],v[216:219]
v_mfma_f32_16x16x32_bf16 v[216:219],v[96:99],a[140:143],v[216:219]
v_mfma_f32_16x16x32_bf16 v[224:227],v[112:115],a[128:131],v[224:227]
v_mfma_f32_16x16x32_bf16 v[224:227],v[116:119],a[144:147],v[224:227]
v_mfma_f32_16x16x32_bf16 v[224:227],v[120:123],a[160:163],v[224:227]
v_mfma_f32_16x16x32_bf16 v[224:227],v[124:127],a[176:179],v[224:227]
v_mfma_f32_16x16x32_bf16 v[232:235],v[124:127],a[180:183],v[232:235]
v_mfma_f32_16x16x32_bf16 v[232:235],v[120:123],a[164:167],v[232:235]
v_mfma_f32_16x16x32_bf16 v[232:235],v[116:119],a[148:151],v[232:235]
v_mfma_f32_16x16x32_bf16 v[232:235],v[112:115],a[132:135],v[232:235]
v_mfma_f32_16x16x32_bf16 v[240:243],v[112:115],a[136:139],v[240:243]
v_mfma_f32_16x16x32_bf16 v[240:243],v[116:119],a[152:155],v[240:243]
v_mfma_f32_16x16x32_bf16 v[240:243],v[120:123],a[168:171],v[240:243]
v_mfma_f32_16x16x32_bf16 v[240:243],v[124:127],a[184:187],v[240:243]
v_mfma_f32_16x16x32_bf16 v[248:251],v[124:127],a[188:191],v[248:251]
v_mfma_f32_16x16x32_bf16 v[248:251],v[120:123],a[172:175],v[248:251]
v_mfma_f32_16x16x32_bf16 v[248:251],v[116:119],a[156:159],v[248:251]
v_mfma_f32_16x16x32_bf16 v[248:251],v[112:115],a[140:143],v[248:251]
v_mfma_f32_16x16x32_bf16 v[192:195],v[64:67],a[68:71],v[192:195]
v_mfma_f32_16x16x32_bf16 v[192:195],v[68:71],a[76:79],v[192:195]
v_mfma_f32_16x16x32_bf16 v[192:195],v[72:75],a[84:87],v[192:195]
v_mfma_f32_16x16x32_bf16 v[192:195],v[76:79],a[92:95],v[192:195]
v_mfma_f32_16x16x32_bf16 v[200:203],v[76:79],a[124:127],v[200:203]
v_mfma_f32_16x16x32_bf16 v[200:203],v[72:75],a[116:119],v[200:203]
v_mfma_f32_16x16x32_bf16 v[200:203],v[68:71],a[108:111],v[200:203]
v_mfma_f32_16x16x32_bf16 v[200:203],v[64:67],a[100:103],v[200:203]
v_mfma_f32_16x16x32_bf16 v[208:211],v[64:67],a[64:67],v[208:211]
v_mfma_f32_16x16x32_bf16 v[208:211],v[68:71],a[72:75],v[208:211]
v_mfma_f32_16x16x32_bf16 v[208:211],v[72:75],a[80:83],v[208:211]
v_mfma_f32_16x16x32_bf16 v[208:211],v[76:79],a[88:91],v[208:211]
v_mfma_f32_16x16x32_bf16 v[216:219],v[76:79],a[120:123],v[216:219]
v_mfma_f32_16x16x32_bf16 v[216:219],v[72:75],a[112:115],v[216:219]
v_mfma_f32_16x16x32_bf16 v[216:219],v[68:71],a[104:107],v[216:219]
v_mfma_f32_16x16x32_bf16 v[216:219],v[64:67],a[96:99],v[216:219]
s_waitcnt lgkmcnt(0)
v_mfma_f32_16x16x32_bf16 v[224:227],v[80:83],a[68:71],v[224:227]
buffer_load_dwordx2 v[62:63],v61,s[44:47],s91 offen offset:2312 sc1
v_mfma_f32_16x16x32_bf16 v[224:227],v[84:87],a[76:79],v[224:227]
v_mfma_f32_16x16x32_bf16 v[224:227],v[88:91],a[84:87],v[224:227]
v_mfma_f32_16x16x32_bf16 v[224:227],v[92:95],a[92:95],v[224:227]
v_mfma_f32_16x16x32_bf16 v[232:235],v[92:95],a[124:127],v[232:235]
v_mfma_f32_16x16x32_bf16 v[232:235],v[88:91],a[116:119],v[232:235]
v_mfma_f32_16x16x32_bf16 v[232:235],v[84:87],a[108:111],v[232:235]
v_mfma_f32_16x16x32_bf16 v[232:235],v[80:83],a[100:103],v[232:235]
v_mfma_f32_16x16x32_bf16 v[240:243],v[80:83],a[64:67],v[240:243]
v_mfma_f32_16x16x32_bf16 v[240:243],v[84:87],a[72:75],v[240:243]
v_mfma_f32_16x16x32_bf16 v[240:243],v[88:91],a[80:83],v[240:243]
v_mfma_f32_16x16x32_bf16 v[240:243],v[92:95],a[88:91],v[240:243]
v_mfma_f32_16x16x32_bf16 v[248:251],v[92:95],a[120:123],v[248:251]
v_mfma_f32_16x16x32_bf16 v[248:251],v[88:91],a[112:115],v[248:251]
v_mfma_f32_16x16x32_bf16 v[248:251],v[84:87],a[104:107],v[248:251]
v_mfma_f32_16x16x32_bf16 v[248:251],v[80:83],a[96:99],v[248:251]
s_waitcnt vmcnt(1)
s_barrier
buffer_store_dwordx2 v[20:21],v22,s[44:47],s18 offen offset:2304 sc1
s_waitcnt vmcnt(1)
v_cmp_ne_u32 s[56:57],s52,v62
v_cmp_ne_u32 s[58:59],s53,v63
s_nop 0
s_or_b64 s[56:57],s[56:57],s[58:59]
s_cbranch_scc1 L_fr8_%=
L_fo8_%=:
buffer_load_dwordx4 v[64:67],v6,s[36:39],s66 offen sc1
buffer_load_dwordx4 v[68:71],v6,s[36:39],s67 offen sc1
buffer_load_dwordx4 v[72:75],v6,s[36:39],s66 offen offset:1024 sc1
buffer_load_dwordx4 v[76:79],v6,s[36:39],s67 offen offset:1024 sc1
buffer_load_dwordx4 v[80:83],v6,s[36:39],s66 offen offset:2048 sc1
buffer_load_dwordx4 v[84:87],v6,s[36:39],s67 offen offset:2048 sc1
buffer_load_dwordx4 v[88:91],v28,s[36:39],s66 offen offset:3072 sc1
buffer_load_dwordx4 v[92:95],v28,s[36:39],s67 offen offset:3072 sc1
buffer_load_dwordx2 v[4:5],v3,s[44:47],s91 offen offset:2304 sc1
s_waitcnt vmcnt(8)
v_mfma_f32_16x16x32_bf16 v[192:195],v[48:51],v[64:67],v[192:195]
s_waitcnt vmcnt(7)
v_mfma_f32_16x16x32_bf16 v[192:195],v[48:51],v[68:71],v[192:195]
v_mfma_f32_16x16x32_bf16 v[224:227],v[52:55],v[64:67],v[224:227]
v_mfma_f32_16x16x32_bf16 v[224:227],v[52:55],v[68:71],v[224:227]
s_waitcnt vmcnt(6)
v_mfma_f32_16x16x32_bf16 v[200:203],v[48:51],v[72:75],v[200:203]
s_waitcnt vmcnt(5)
v_mfma_f32_16x16x32_bf16 v[200:203],v[48:51],v[76:79],v[200:203]
v_mfma_f32_16x16x32_bf16 v[232:235],v[52:55],v[72:75],v[232:235]
v_mfma_f32_16x16x32_bf16 v[232:235],v[52:55],v[76:79],v[232:235]
s_waitcnt vmcnt(4)
v_mfma_f32_16x16x32_bf16 v[208:211],v[48:51],v[80:83],v[208:211]
s_waitcnt vmcnt(3)
v_mfma_f32_16x16x32_bf16 v[208:211],v[48:51],v[84:87],v[208:211]
v_mfma_f32_16x16x32_bf16 v[240:243],v[52:55],v[80:83],v[240:243]
v_mfma_f32_16x16x32_bf16 v[240:243],v[52:55],v[84:87],v[240:243]
s_waitcnt vmcnt(2)
v_mfma_f32_16x16x32_bf16 v[216:219],v[48:51],v[88:91],v[216:219]
s_waitcnt vmcnt(1)
v_mfma_f32_16x16x32_bf16 v[216:219],v[48:51],v[92:95],v[216:219]
v_mfma_f32_16x16x32_bf16 v[248:251],v[52:55],v[88:91],v[248:251]
v_mfma_f32_16x16x32_bf16 v[248:251],v[52:55],v[92:95],v[248:251]
s_waitcnt vmcnt(0)
v_cmp_ne_u32 s[56:57],s52,v4
v_cmp_ne_u32 s[58:59],s53,v5
s_nop 0
s_or_b64 s[56:57],s[56:57],s[58:59]
s_cbranch_scc1 L_fr9_%=
L_fo9_%=:
buffer_load_dwordx4 v[96:99],v6,s[36:39],s68 offen sc1
buffer_load_dwordx4 v[100:103],v6,s[36:39],s68 offen offset:1024 sc1
buffer_load_dwordx4 v[104:107],v6,s[36:39],s68 offen offset:2048 sc1
buffer_load_dwordx4 v[108:111],v6,s[36:39],s68 offen offset:3072 sc1
buffer_load_dwordx4 v[112:115],v6,s[36:39],s65 offen sc1
buffer_load_dwordx4 v[116:119],v6,s[36:39],s65 offen offset:1024 sc1
buffer_load_dwordx4 v[120:123],v6,s[36:39],s65 offen offset:2048 sc1
buffer_load_dwordx4 v[124:127],v28,s[36:39],s65 offen offset:3072 sc1
s_waitcnt vmcnt(7)
v_mfma_f32_16x16x32_bf16 v[128:131],v[48:51],v[96:99],0
s_waitcnt vmcnt(6)
v_mfma_f32_16x16x32_bf16 v[128:131],v[48:51],v[100:103],v[128:131]
s_waitcnt vmcnt(5)
v_mfma_f32_16x16x32_bf16 v[128:131],v[48:51],v[104:107],v[128:131]
s_waitcnt vmcnt(4)
v_mfma_f32_16x16x32_bf16 v[128:131],v[48:51],v[108:111],v[128:131]
v_mfma_f32_16x16x32_bf16 v[160:163],v[52:55],v[96:99],0
)A"
R"A(v_mfma_f32_16x16x32_bf16 v[160:163],v[52:55],v[100:103],v[160:163]
v_mfma_f32_16x16x32_bf16 v[160:163],v[52:55],v[104:107],v[160:163]
v_mfma_f32_16x16x32_bf16 v[160:163],v[52:55],v[108:111],v[160:163]
s_waitcnt vmcnt(3)
v_mfma_f32_16x16x32_bf16 v[192:195],v[48:51],v[112:115],v[192:195]
v_mfma_f32_16x16x32_bf16 v[224:227],v[52:55],v[112:115],v[224:227]
s_waitcnt vmcnt(2)
v_mfma_f32_16x16x32_bf16 v[200:203],v[48:51],v[116:119],v[200:203]
v_mfma_f32_16x16x32_bf16 v[232:235],v[52:55],v[116:119],v[232:235]
s_waitcnt vmcnt(1)
v_cvt_pk_bf16_f32 v128,v128,v129
v_cvt_pk_bf16_f32 v129,v130,v131
v_mfma_f32_16x16x32_bf16 v[208:211],v[48:51],v[120:123],v[208:211]
v_mfma_f32_16x16x32_bf16 v[240:243],v[52:55],v[120:123],v[240:243]
s_waitcnt vmcnt(0)
v_cvt_pk_bf16_f32 v130,v160,v161
v_cvt_pk_bf16_f32 v131,v162,v163
v_mfma_f32_16x16x32_bf16 v[216:219],v[48:51],v[124:127],v[216:219]
v_cvt_pk_bf16_f32 v192,v192,v193
v_cvt_pk_bf16_f32 v193,v194,v195
v_cvt_pk_bf16_f32 v194,v224,v225
v_cvt_pk_bf16_f32 v195,v226,v227
v_cvt_pk_bf16_f32 v200,v200,v201
v_cvt_pk_bf16_f32 v201,v202,v203
v_mfma_f32_16x16x32_bf16 v[248:251],v[52:55],v[124:127],v[248:251]
v_permlane16_swap_b32 v128,v130
v_permlane16_swap_b32 v129,v131
v_cvt_pk_bf16_f32 v202,v232,v233
v_cvt_pk_bf16_f32 v203,v234,v235
v_cvt_pk_bf16_f32 v208,v208,v209
v_cvt_pk_bf16_f32 v209,v210,v211
v_cvt_pk_bf16_f32 v210,v240,v241
v_cvt_pk_bf16_f32 v211,v242,v243
buffer_store_dwordx4 v[128:131],v56,s[24:27],s83 offen offset:256 sc1 nt
v_permlane16_swap_b32 v192,v194
v_permlane16_swap_b32 v193,v195
buffer_store_dwordx4 v[192:195],v56,s[24:27],s87 offen sc1 nt
v_permlane16_swap_b32 v200,v202
v_permlane16_swap_b32 v201,v203
v_cvt_pk_bf16_f32 v216,v216,v217
v_cvt_pk_bf16_f32 v217,v218,v219
v_cvt_pk_bf16_f32 v218,v248,v249
v_cvt_pk_bf16_f32 v219,v250,v251
buffer_store_dwordx4 v[200:203],v56,s[24:27],s88 offen sc1 nt
v_permlane16_swap_b32 v208,v210
v_permlane16_swap_b32 v209,v211
buffer_store_dwordx4 v[208:211],v56,s[24:27],s89 offen sc1 nt
v_permlane16_swap_b32 v216,v218
v_permlane16_swap_b32 v217,v219
buffer_store_dwordx4 v[216:219],v56,s[24:27],s90 offen sc1 nt
s_lshl_b32 s69,s62,20
s_add_u32 s69,s69,s18
buffer_store_dword v20,v22,s[44:47],s69 offen sc1
L_geS_%=:
s_branch L_end_%=
L_hot_%=:
L_fr1_%=:
s_mov_b32 s19,65536
L_fl1_%=:
s_sleep 6
buffer_load_dwordx2 v[4:5],v3,s[44:47],s91 offen offset:256 sc1
s_waitcnt vmcnt(0)
s_sub_u32 s19,s19,1
s_cmp_eq_u32 s19,0
s_cbranch_scc1 L_fo1_%=
v_cmp_ne_u32 s[56:57],s52,v4
v_cmp_ne_u32 s[58:59],s53,v5
s_nop 1
s_or_b64 s[56:57],s[56:57],s[58:59]
s_cbranch_scc1 L_fl1_%=
s_branch L_fo1_%=
L_fr2_%=:
s_mov_b32 s19,65536
L_fl2_%=:
s_sleep 6
buffer_load_dwordx2 v[4:5],v3,s[44:47],s91 offen offset:1280 sc1
s_waitcnt vmcnt(0)
s_sub_u32 s19,s19,1
s_cmp_eq_u32 s19,0
s_cbranch_scc1 L_fo2_%=
v_cmp_ne_u32 s[56:57],s52,v4
v_cmp_ne_u32 s[58:59],s53,v5
s_nop 1
s_or_b64 s[56:57],s[56:57],s[58:59]
s_cbranch_scc1 L_fl2_%=
s_branch L_fo2_%=
L_fr3_%=:
s_mov_b32 s19,65536
L_fl3_%=:
s_sleep 6
buffer_load_dwordx2 v[4:5],v3,s[44:47],s91 offen offset:1280 sc1
s_waitcnt vmcnt(0)
s_sub_u32 s19,s19,1
s_cmp_eq_u32 s19,0
s_cbranch_scc1 L_fo3_%=
v_cmp_ne_u32 s[56:57],s52,v4
v_cmp_ne_u32 s[58:59],s53,v5
s_nop 1
s_or_b64 s[56:57],s[56:57],s[58:59]
s_cbranch_scc1 L_fl3_%=
s_branch L_fo3_%=
L_fr4_%=:
s_mov_b32 s19,65536
L_fl4_%=:
s_sleep 6
buffer_load_dwordx2 v[62:63],v61,s[44:47],s91 offen offset:2312 sc1
s_waitcnt vmcnt(0)
s_sub_u32 s19,s19,1
s_cmp_eq_u32 s19,0
s_cbranch_scc1 L_fo4_%=
v_cmp_ne_u32 s[56:57],s52,v62
v_cmp_ne_u32 s[58:59],s53,v63
s_nop 1
s_or_b64 s[56:57],s[56:57],s[58:59]
s_cbranch_scc1 L_fl4_%=
s_branch L_fo4_%=
L_fr5_%=:
s_mov_b32 s19,65536
L_fl5_%=:
s_sleep 6
buffer_load_dwordx2 v[4:5],v3,s[44:47],s91 offen offset:2304 sc1
s_waitcnt vmcnt(0)
s_sub_u32 s19,s19,1
s_cmp_eq_u32 s19,0
s_cbranch_scc1 L_fo5_%=
v_cmp_ne_u32 s[56:57],s52,v4
v_cmp_ne_u32 s[58:59],s53,v5
s_nop 1
s_or_b64 s[56:57],s[56:57],s[58:59]
s_cbranch_scc1 L_fl5_%=
s_branch L_fo5_%=
L_fr6_%=:
s_mov_b32 s19,65536
L_fl6_%=:
s_sleep 6
buffer_load_dwordx2 v[4:5],v3,s[44:47],s91 offen offset:256 sc1
s_waitcnt vmcnt(0)
s_sub_u32 s19,s19,1
s_cmp_eq_u32 s19,0
s_cbranch_scc1 L_fo6_%=
v_cmp_ne_u32 s[56:57],s52,v4
v_cmp_ne_u32 s[58:59],s53,v5
s_nop 1
s_or_b64 s[56:57],s[56:57],s[58:59]
s_cbranch_scc1 L_fl6_%=
s_branch L_fo6_%=
L_fr7_%=:
s_mov_b32 s19,65536
L_fl7_%=:
s_sleep 6
buffer_load_dwordx2 v[4:5],v3,s[44:47],s91 offen offset:1280 sc1
s_waitcnt vmcnt(0)
s_sub_u32 s19,s19,1
s_cmp_eq_u32 s19,0
s_cbranch_scc1 L_fo7_%=
v_cmp_ne_u32 s[56:57],s52,v4
v_cmp_ne_u32 s[58:59],s53,v5
s_nop 1
s_or_b64 s[56:57],s[56:57],s[58:59]
s_cbranch_scc1 L_fl7_%=
s_branch L_fo7_%=
L_fr8_%=:
s_mov_b32 s19,65536
L_fl8_%=:
s_sleep 6
buffer_load_dwordx2 v[62:63],v61,s[44:47],s91 offen offset:2312 sc1
s_waitcnt vmcnt(0)
s_sub_u32 s19,s19,1
s_cmp_eq_u32 s19,0
s_cbranch_scc1 L_fo8_%=
v_cmp_ne_u32 s[56:57],s52,v62
v_cmp_ne_u32 s[58:59],s53,v63
s_nop 1
s_or_b64 s[56:57],s[56:57],s[58:59]
s_cbranch_scc1 L_fl8_%=
s_branch L_fo8_%=
L_fr9_%=:
s_mov_b32 s19,65536
L_fl9_%=:
s_sleep 6
buffer_load_dwordx2 v[4:5],v3,s[44:47],s91 offen offset:2304 sc1
s_waitcnt vmcnt(0)
s_sub_u32 s19,s19,1
s_cmp_eq_u32 s19,0
s_cbranch_scc1 L_fo9_%=
v_cmp_ne_u32 s[56:57],s52,v4
v_cmp_ne_u32 s[58:59],s53,v5
s_nop 1
s_or_b64 s[56:57],s[56:57],s[58:59]
s_cbranch_scc1 L_fl9_%=
s_branch L_fo9_%=
L_slow0_%=:
s_waitcnt vmcnt(0) lgkmcnt(0)
s_nop 7
v_accvgpr_write_b32 a160,v32
v_accvgpr_write_b32 a161,v33
v_accvgpr_write_b32 a162,v34
v_accvgpr_write_b32 a163,v35
v_accvgpr_write_b32 a164,v36
v_accvgpr_write_b32 a165,v37
v_accvgpr_write_b32 a166,v38
v_accvgpr_write_b32 a167,v39
v_accvgpr_write_b32 a168,v40
v_accvgpr_write_b32 a169,v41
v_accvgpr_write_b32 a170,v42
v_accvgpr_write_b32 a171,v43
v_accvgpr_write_b32 a172,v44
v_accvgpr_write_b32 a173,v45
v_accvgpr_write_b32 a174,v46
v_accvgpr_write_b32 a175,v47
s_waitcnt vmcnt(0) lgkmcnt(0)
s_nop 7
v_bfe_u32 v11,v6,4,4
v_lshrrev_b32 v12,8,v6
v_lshlrev_b32 v8,8,v11
v_lshl_add_u32 v8,v12,4,v8
v_lshlrev_b32 v9,10,v12
v_lshl_add_u32 v9,v11,1,v9
v_add_u32 v10,4096,v9
v_mov_b32 v48,0xff800000
v_mov_b32 v52,0xff800000
v_mov_b32 v56,0xff800000
v_mov_b32 v60,0xff800000
s_mov_b32 s65,0
s_mov_b32 s66,64
L_s10_%=:
buffer_load_dwordx4 a[128:131],v8,s[20:23],s65 offen
buffer_load_dwordx4 a[132:135],v8,s[20:23],s65 offen offset:64
buffer_load_dwordx4 a[136:139],v8,s[20:23],s65 offen offset:128
buffer_load_dwordx4 a[140:143],v8,s[20:23],s65 offen offset:192
s_waitcnt vmcnt(0)
v_mfma_f32_16x16x32_bf16 v[64:67],a[128:131],v[132:135],0
v_mfma_f32_16x16x32_bf16 v[64:67],a[132:135],v[140:143],v[64:67]
v_mfma_f32_16x16x32_bf16 v[64:67],a[136:139],v[148:151],v[64:67]
v_mfma_f32_16x16x32_bf16 v[64:67],a[140:143],v[156:159],v[64:67]
v_mfma_f32_16x16x32_bf16 v[80:83],a[128:131],v[164:167],0
v_mfma_f32_16x16x32_bf16 v[80:83],a[132:135],v[172:175],v[80:83]
v_mfma_f32_16x16x32_bf16 v[80:83],a[136:139],v[180:183],v[80:83]
v_mfma_f32_16x16x32_bf16 v[80:83],a[140:143],v[188:191],v[80:83]
v_mfma_f32_16x16x32_bf16 v[96:99],a[128:131],v[196:199],0
v_mfma_f32_16x16x32_bf16 v[96:99],a[132:135],v[204:207],v[96:99]
v_mfma_f32_16x16x32_bf16 v[96:99],a[136:139],v[212:215],v[96:99]
v_mfma_f32_16x16x32_bf16 v[96:99],a[140:143],v[220:223],v[96:99]
v_mfma_f32_16x16x32_bf16 v[112:115],a[128:131],v[228:231],0
v_mfma_f32_16x16x32_bf16 v[112:115],a[132:135],v[236:239],v[112:115]
v_mfma_f32_16x16x32_bf16 v[112:115],a[136:139],v[244:247],v[112:115]
v_mfma_f32_16x16x32_bf16 v[112:115],a[140:143],v[252:255],v[112:115]
s_nop 7
v_max3_f32 v48,v48,v64,v65
v_max3_f32 v48,v48,v66,v67
v_max3_f32 v52,v52,v80,v81
v_max3_f32 v52,v52,v82,v83
v_max3_f32 v56,v56,v96,v97
v_max3_f32 v56,v56,v98,v99
v_max3_f32 v60,v60,v112,v113
v_max3_f32 v60,v60,v114,v115
s_add_u32 s65,s65,4096
s_sub_u32 s66,s66,1
s_cmp_lg_u32 s66,0
s_cbranch_scc1 L_s10_%=
v_mov_b32 v11,v48
s_nop 1
v_permlane32_swap_b32 v48,v11
v_max_f32 v48,v48,v11
v_mov_b32 v11,v48
s_nop 1
v_permlane16_swap_b32 v48,v11
v_max_f32 v48,v48,v11
v_sub_f32 v32,0,v48
v_sub_f32 v33,0,v48
v_sub_f32 v34,0,v48
v_sub_f32 v35,0,v48
v_mov_b32 v11,v52
s_nop 1
v_permlane32_swap_b32 v52,v11
v_max_f32 v52,v52,v11
v_mov_b32 v11,v52
s_nop 1
v_permlane16_swap_b32 v52,v11
v_max_f32 v52,v52,v11
v_sub_f32 v36,0,v52
v_sub_f32 v37,0,v52
v_sub_f32 v38,0,v52
v_sub_f32 v39,0,v52
v_mov_b32 v11,v56
s_nop 1
v_permlane32_swap_b32 v56,v11
v_max_f32 v56,v56,v11
v_mov_b32 v11,v56
s_nop 1
v_permlane16_swap_b32 v56,v11
v_max_f32 v56,v56,v11
v_sub_f32 v40,0,v56
v_sub_f32 v41,0,v56
v_sub_f32 v42,0,v56
v_sub_f32 v43,0,v56
v_mov_b32 v11,v60
s_nop 1
v_permlane32_swap_b32 v60,v11
v_max_f32 v60,v60,v11
v_mov_b32 v11,v60
s_nop 1
v_permlane16_swap_b32 v60,v11
v_max_f32 v60,v60,v11
v_max_f32 v60,s72,v60
v_sub_f32 v44,0,v60
v_sub_f32 v45,0,v60
v_sub_f32 v46,0,v60
v_sub_f32 v47,0,v60
v_mov_b32 v48,0
v_mov_b32 v49,0
v_mov_b32 v50,0
v_mov_b32 v51,0
v_mov_b32 v52,0
v_mov_b32 v53,0
v_mov_b32 v54,0
v_mov_b32 v55,0
v_mov_b32 v56,0
v_mov_b32 v57,0
v_mov_b32 v58,0
v_mov_b32 v59,0
v_mov_b32 v60,0
v_mov_b32 v61,0
v_mov_b32 v62,0
v_mov_b32 v63,0
v_accvgpr_write_b32 a0,0
v_accvgpr_write_b32 a1,0
v_accvgpr_write_b32 a2,0
v_accvgpr_write_b32 a3,0
v_accvgpr_write_b32 a4,0
v_accvgpr_write_b32 a5,0
v_accvgpr_write_b32 a6,0
v_accvgpr_write_b32 a7,0
v_accvgpr_write_b32 a8,0
v_accvgpr_write_b32 a9,0
v_accvgpr_write_b32 a10,0
v_accvgpr_write_b32 a11,0
v_accvgpr_write_b32 a12,0
v_accvgpr_write_b32 a13,0
v_accvgpr_write_b32 a14,0
v_accvgpr_write_b32 a15,0
v_accvgpr_write_b32 a16,0
v_accvgpr_write_b32 a17,0
v_accvgpr_write_b32 a18,0
v_accvgpr_write_b32 a19,0
v_accvgpr_write_b32 a20,0
v_accvgpr_write_b32 a21,0
v_accvgpr_write_b32 a22,0
v_accvgpr_write_b32 a23,0
v_accvgpr_write_b32 a24,0
v_accvgpr_write_b32 a25,0
v_accvgpr_write_b32 a26,0
v_accvgpr_write_b32 a27,0
v_accvgpr_write_b32 a28,0
v_accvgpr_write_b32 a29,0
v_accvgpr_write_b32 a30,0
v_accvgpr_write_b32 a31,0
v_accvgpr_write_b32 a32,0
v_accvgpr_write_b32 a33,0
v_accvgpr_write_b32 a34,0
v_accvgpr_write_b32 a35,0
v_accvgpr_write_b32 a36,0
v_accvgpr_write_b32 a37,0
v_accvgpr_write_b32 a38,0
v_accvgpr_write_b32 a39,0
v_accvgpr_write_b32 a40,0
v_accvgpr_write_b32 a41,0
v_accvgpr_write_b32 a42,0
v_accvgpr_write_b32 a43,0
v_accvgpr_write_b32 a44,0
v_accvgpr_write_b32 a45,0
v_accvgpr_write_b32 a46,0
v_accvgpr_write_b32 a47,0
v_accvgpr_write_b32 a48,0
v_accvgpr_write_b32 a49,0
v_accvgpr_write_b32 a50,0
v_accvgpr_write_b32 a51,0
v_accvgpr_write_b32 a52,0
v_accvgpr_write_b32 a53,0
v_accvgpr_write_b32 a54,0
v_accvgpr_write_b32 a55,0
v_accvgpr_write_b32 a56,0
v_accvgpr_write_b32 a57,0
v_accvgpr_write_b32 a58,0
v_accvgpr_write_b32 a59,0
v_accvgpr_write_b32 a60,0
v_accvgpr_write_b32 a61,0
v_accvgpr_write_b32 a62,0
v_accvgpr_write_b32 a63,0
v_accvgpr_write_b32 a64,0
v_accvgpr_write_b32 a65,0
v_accvgpr_write_b32 a66,0
v_accvgpr_write_b32 a67,0
v_accvgpr_write_b32 a68,0
v_accvgpr_write_b32 a69,0
v_accvgpr_write_b32 a70,0
v_accvgpr_write_b32 a71,0
v_accvgpr_write_b32 a72,0
v_accvgpr_write_b32 a73,0
v_accvgpr_write_b32 a74,0
v_accvgpr_write_b32 a75,0
v_accvgpr_write_b32 a76,0
v_accvgpr_write_b32 a77,0
v_accvgpr_write_b32 a78,0
v_accvgpr_write_b32 a79,0
v_accvgpr_write_b32 a80,0
v_accvgpr_write_b32 a81,0
v_accvgpr_write_b32 a82,0
v_accvgpr_write_b32 a83,0
v_accvgpr_write_b32 a84,0
v_accvgpr_write_b32 a85,0
v_accvgpr_write_b32 a86,0
v_accvgpr_write_b32 a87,0
v_accvgpr_write_b32 a88,0
v_accvgpr_write_b32 a89,0
v_accvgpr_write_b32 a90,0
v_accvgpr_write_b32 a91,0
v_accvgpr_write_b32 a92,0
v_accvgpr_write_b32 a93,0
v_accvgpr_write_b32 a94,0
v_accvgpr_write_b32 a95,0
v_accvgpr_write_b32 a96,0
v_accvgpr_write_b32 a97,0
v_accvgpr_write_b32 a98,0
v_accvgpr_write_b32 a99,0
v_accvgpr_write_b32 a100,0
v_accvgpr_write_b32 a101,0
v_accvgpr_write_b32 a102,0
v_accvgpr_write_b32 a103,0
v_accvgpr_write_b32 a104,0
v_accvgpr_write_b32 a105,0
v_accvgpr_write_b32 a106,0
v_accvgpr_write_b32 a107,0
v_accvgpr_write_b32 a108,0
v_accvgpr_write_b32 a109,0
v_accvgpr_write_b32 a110,0
v_accvgpr_write_b32 a111,0
v_accvgpr_write_b32 a112,0
v_accvgpr_write_b32 a113,0
v_accvgpr_write_b32 a114,0
)A"
R"A(v_accvgpr_write_b32 a115,0
v_accvgpr_write_b32 a116,0
v_accvgpr_write_b32 a117,0
v_accvgpr_write_b32 a118,0
v_accvgpr_write_b32 a119,0
v_accvgpr_write_b32 a120,0
v_accvgpr_write_b32 a121,0
v_accvgpr_write_b32 a122,0
v_accvgpr_write_b32 a123,0
v_accvgpr_write_b32 a124,0
v_accvgpr_write_b32 a125,0
v_accvgpr_write_b32 a126,0
v_accvgpr_write_b32 a127,0
s_mov_b32 s65,0
s_mov_b32 s66,32
L_s20_%=:
s_add_u32 s67,s65,4096
buffer_load_dwordx4 a[128:131],v8,s[20:23],s65 offen
buffer_load_dwordx4 a[132:135],v8,s[20:23],s65 offen offset:64
buffer_load_dwordx4 a[136:139],v8,s[20:23],s65 offen offset:128
buffer_load_dwordx4 a[140:143],v8,s[20:23],s65 offen offset:192
buffer_load_dwordx4 a[144:147],v8,s[20:23],s67 offen
buffer_load_dwordx4 a[148:151],v8,s[20:23],s67 offen offset:64
buffer_load_dwordx4 a[152:155],v8,s[20:23],s67 offen offset:128
buffer_load_dwordx4 a[156:159],v8,s[20:23],s67 offen offset:192
s_waitcnt vmcnt(0)
v_mfma_f32_16x16x32_bf16 v[64:67],a[128:131],v[132:135],v[32:35]
v_mfma_f32_16x16x32_bf16 v[64:67],a[132:135],v[140:143],v[64:67]
v_mfma_f32_16x16x32_bf16 v[64:67],a[136:139],v[148:151],v[64:67]
v_mfma_f32_16x16x32_bf16 v[64:67],a[140:143],v[156:159],v[64:67]
v_mfma_f32_16x16x32_bf16 v[68:71],a[144:147],v[132:135],v[32:35]
v_mfma_f32_16x16x32_bf16 v[68:71],a[148:151],v[140:143],v[68:71]
v_mfma_f32_16x16x32_bf16 v[68:71],a[152:155],v[148:151],v[68:71]
v_mfma_f32_16x16x32_bf16 v[68:71],a[156:159],v[156:159],v[68:71]
v_mfma_f32_16x16x32_bf16 v[80:83],a[128:131],v[164:167],v[36:39]
v_mfma_f32_16x16x32_bf16 v[80:83],a[132:135],v[172:175],v[80:83]
v_mfma_f32_16x16x32_bf16 v[80:83],a[136:139],v[180:183],v[80:83]
v_mfma_f32_16x16x32_bf16 v[80:83],a[140:143],v[188:191],v[80:83]
v_mfma_f32_16x16x32_bf16 v[84:87],a[144:147],v[164:167],v[36:39]
v_mfma_f32_16x16x32_bf16 v[84:87],a[148:151],v[172:175],v[84:87]
v_mfma_f32_16x16x32_bf16 v[84:87],a[152:155],v[180:183],v[84:87]
v_mfma_f32_16x16x32_bf16 v[84:87],a[156:159],v[188:191],v[84:87]
v_mfma_f32_16x16x32_bf16 v[96:99],a[128:131],v[196:199],v[40:43]
v_mfma_f32_16x16x32_bf16 v[96:99],a[132:135],v[204:207],v[96:99]
v_mfma_f32_16x16x32_bf16 v[96:99],a[136:139],v[212:215],v[96:99]
v_mfma_f32_16x16x32_bf16 v[96:99],a[140:143],v[220:223],v[96:99]
v_mfma_f32_16x16x32_bf16 v[100:103],a[144:147],v[196:199],v[40:43]
v_mfma_f32_16x16x32_bf16 v[100:103],a[148:151],v[204:207],v[100:103]
v_mfma_f32_16x16x32_bf16 v[100:103],a[152:155],v[212:215],v[100:103]
v_mfma_f32_16x16x32_bf16 v[100:103],a[156:159],v[220:223],v[100:103]
v_mfma_f32_16x16x32_bf16 v[112:115],a[128:131],v[228:231],v[44:47]
v_mfma_f32_16x16x32_bf16 v[112:115],a[132:135],v[236:239],v[112:115]
v_mfma_f32_16x16x32_bf16 v[112:115],a[136:139],v[244:247],v[112:115]
v_mfma_f32_16x16x32_bf16 v[112:115],a[140:143],v[252:255],v[112:115]
v_mfma_f32_16x16x32_bf16 v[116:119],a[144:147],v[228:231],v[44:47]
v_mfma_f32_16x16x32_bf16 v[116:119],a[148:151],v[236:239],v[116:119]
v_mfma_f32_16x16x32_bf16 v[116:119],a[152:155],v[244:247],v[116:119]
v_mfma_f32_16x16x32_bf16 v[116:119],a[156:159],v[252:255],v[116:119]
s_nop 7
v_exp_f32 v64,v64
v_exp_f32 v65,v65
v_exp_f32 v66,v66
v_exp_f32 v67,v67
v_exp_f32 v68,v68
v_exp_f32 v69,v69
v_exp_f32 v70,v70
v_exp_f32 v71,v71
s_nop 0
v_cvt_pk_bf16_f32 v64,v64,v65
v_cvt_pk_bf16_f32 v65,v66,v67
v_cvt_pk_bf16_f32 v66,v68,v69
v_cvt_pk_bf16_f32 v67,v70,v71
v_exp_f32 v80,v80
v_exp_f32 v81,v81
v_exp_f32 v82,v82
v_exp_f32 v83,v83
v_exp_f32 v84,v84
v_exp_f32 v85,v85
v_exp_f32 v86,v86
v_exp_f32 v87,v87
s_nop 0
v_cvt_pk_bf16_f32 v80,v80,v81
v_cvt_pk_bf16_f32 v81,v82,v83
v_cvt_pk_bf16_f32 v82,v84,v85
v_cvt_pk_bf16_f32 v83,v86,v87
v_exp_f32 v96,v96
v_exp_f32 v97,v97
v_exp_f32 v98,v98
v_exp_f32 v99,v99
v_exp_f32 v100,v100
v_exp_f32 v101,v101
v_exp_f32 v102,v102
v_exp_f32 v103,v103
s_nop 0
v_cvt_pk_bf16_f32 v96,v96,v97
v_cvt_pk_bf16_f32 v97,v98,v99
v_cvt_pk_bf16_f32 v98,v100,v101
v_cvt_pk_bf16_f32 v99,v102,v103
v_exp_f32 v112,v112
v_exp_f32 v113,v113
v_exp_f32 v114,v114
v_exp_f32 v115,v115
v_exp_f32 v116,v116
v_exp_f32 v117,v117
v_exp_f32 v118,v118
v_exp_f32 v119,v119
s_nop 0
v_cvt_pk_bf16_f32 v112,v112,v113
v_cvt_pk_bf16_f32 v113,v114,v115
v_cvt_pk_bf16_f32 v114,v116,v117
v_cvt_pk_bf16_f32 v115,v118,v119
buffer_load_ushort v72,v9,s[24:27],s65 offen
buffer_load_ushort v73,v9,s[24:27],s65 offen offset:256
buffer_load_ushort v74,v9,s[24:27],s65 offen offset:512
buffer_load_ushort v75,v9,s[24:27],s65 offen offset:768
buffer_load_ushort v76,v10,s[24:27],s65 offen
buffer_load_ushort v77,v10,s[24:27],s65 offen offset:256
buffer_load_ushort v78,v10,s[24:27],s65 offen offset:512
buffer_load_ushort v79,v10,s[24:27],s65 offen offset:768
s_waitcnt vmcnt(0)
v_lshl_or_b32 v88,v73,16,v72
v_lshl_or_b32 v89,v75,16,v74
v_lshl_or_b32 v90,v77,16,v76
v_lshl_or_b32 v91,v79,16,v78
s_nop 1
v_mfma_f32_16x16x32_bf16 a[0:3],v[88:91],v[64:67],a[0:3]
v_mfma_f32_16x16x32_bf16 a[32:35],v[88:91],v[80:83],a[32:35]
v_mfma_f32_16x16x32_bf16 a[64:67],v[88:91],v[96:99],a[64:67]
v_mfma_f32_16x16x32_bf16 a[96:99],v[88:91],v[112:115],a[96:99]
buffer_load_ushort v72,v9,s[24:27],s65 offen offset:32
buffer_load_ushort v73,v9,s[24:27],s65 offen offset:288
buffer_load_ushort v74,v9,s[24:27],s65 offen offset:544
buffer_load_ushort v75,v9,s[24:27],s65 offen offset:800
buffer_load_ushort v76,v10,s[24:27],s65 offen offset:32
buffer_load_ushort v77,v10,s[24:27],s65 offen offset:288
buffer_load_ushort v78,v10,s[24:27],s65 offen offset:544
buffer_load_ushort v79,v10,s[24:27],s65 offen offset:800
s_waitcnt vmcnt(0)
v_lshl_or_b32 v88,v73,16,v72
v_lshl_or_b32 v89,v75,16,v74
v_lshl_or_b32 v90,v77,16,v76
v_lshl_or_b32 v91,v79,16,v78
s_nop 1
v_mfma_f32_16x16x32_bf16 a[4:7],v[88:91],v[64:67],a[4:7]
v_mfma_f32_16x16x32_bf16 a[36:39],v[88:91],v[80:83],a[36:39]
v_mfma_f32_16x16x32_bf16 a[68:71],v[88:91],v[96:99],a[68:71]
v_mfma_f32_16x16x32_bf16 a[100:103],v[88:91],v[112:115],a[100:103]
buffer_load_ushort v72,v9,s[24:27],s65 offen offset:64
buffer_load_ushort v73,v9,s[24:27],s65 offen offset:320
buffer_load_ushort v74,v9,s[24:27],s65 offen offset:576
buffer_load_ushort v75,v9,s[24:27],s65 offen offset:832
buffer_load_ushort v76,v10,s[24:27],s65 offen offset:64
buffer_load_ushort v77,v10,s[24:27],s65 offen offset:320
buffer_load_ushort v78,v10,s[24:27],s65 offen offset:576
buffer_load_ushort v79,v10,s[24:27],s65 offen offset:832
s_waitcnt vmcnt(0)
v_lshl_or_b32 v88,v73,16,v72
v_lshl_or_b32 v89,v75,16,v74
v_lshl_or_b32 v90,v77,16,v76
v_lshl_or_b32 v91,v79,16,v78
s_nop 1
v_mfma_f32_16x16x32_bf16 a[8:11],v[88:91],v[64:67],a[8:11]
v_mfma_f32_16x16x32_bf16 a[40:43],v[88:91],v[80:83],a[40:43]
v_mfma_f32_16x16x32_bf16 a[72:75],v[88:91],v[96:99],a[72:75]
v_mfma_f32_16x16x32_bf16 a[104:107],v[88:91],v[112:115],a[104:107]
buffer_load_ushort v72,v9,s[24:27],s65 offen offset:96
buffer_load_ushort v73,v9,s[24:27],s65 offen offset:352
buffer_load_ushort v74,v9,s[24:27],s65 offen offset:608
buffer_load_ushort v75,v9,s[24:27],s65 offen offset:864
buffer_load_ushort v76,v10,s[24:27],s65 offen offset:96
buffer_load_ushort v77,v10,s[24:27],s65 offen offset:352
buffer_load_ushort v78,v10,s[24:27],s65 offen offset:608
buffer_load_ushort v79,v10,s[24:27],s65 offen offset:864
s_waitcnt vmcnt(0)
v_lshl_or_b32 v88,v73,16,v72
v_lshl_or_b32 v89,v75,16,v74
v_lshl_or_b32 v90,v77,16,v76
v_lshl_or_b32 v91,v79,16,v78
s_nop 1
v_mfma_f32_16x16x32_bf16 a[12:15],v[88:91],v[64:67],a[12:15]
v_mfma_f32_16x16x32_bf16 a[44:47],v[88:91],v[80:83],a[44:47]
v_mfma_f32_16x16x32_bf16 a[76:79],v[88:91],v[96:99],a[76:79]
v_mfma_f32_16x16x32_bf16 a[108:111],v[88:91],v[112:115],a[108:111]
buffer_load_ushort v72,v9,s[24:27],s65 offen offset:128
buffer_load_ushort v73,v9,s[24:27],s65 offen offset:384
buffer_load_ushort v74,v9,s[24:27],s65 offen offset:640
buffer_load_ushort v75,v9,s[24:27],s65 offen offset:896
buffer_load_ushort v76,v10,s[24:27],s65 offen offset:128
buffer_load_ushort v77,v10,s[24:27],s65 offen offset:384
buffer_load_ushort v78,v10,s[24:27],s65 offen offset:640
buffer_load_ushort v79,v10,s[24:27],s65 offen offset:896
s_waitcnt vmcnt(0)
v_lshl_or_b32 v88,v73,16,v72
v_lshl_or_b32 v89,v75,16,v74
v_lshl_or_b32 v90,v77,16,v76
v_lshl_or_b32 v91,v79,16,v78
s_nop 1
v_mfma_f32_16x16x32_bf16 a[16:19],v[88:91],v[64:67],a[16:19]
v_mfma_f32_16x16x32_bf16 a[48:51],v[88:91],v[80:83],a[48:51]
v_mfma_f32_16x16x32_bf16 a[80:83],v[88:91],v[96:99],a[80:83]
v_mfma_f32_16x16x32_bf16 a[112:115],v[88:91],v[112:115],a[112:115]
buffer_load_ushort v72,v9,s[24:27],s65 offen offset:160
buffer_load_ushort v73,v9,s[24:27],s65 offen offset:416
buffer_load_ushort v74,v9,s[24:27],s65 offen offset:672
buffer_load_ushort v75,v9,s[24:27],s65 offen offset:928
buffer_load_ushort v76,v10,s[24:27],s65 offen offset:160
buffer_load_ushort v77,v10,s[24:27],s65 offen offset:416
buffer_load_ushort v78,v10,s[24:27],s65 offen offset:672
buffer_load_ushort v79,v10,s[24:27],s65 offen offset:928
s_waitcnt vmcnt(0)
v_lshl_or_b32 v88,v73,16,v72
v_lshl_or_b32 v89,v75,16,v74
v_lshl_or_b32 v90,v77,16,v76
v_lshl_or_b32 v91,v79,16,v78
s_nop 1
v_mfma_f32_16x16x32_bf16 a[20:23],v[88:91],v[64:67],a[20:23]
v_mfma_f32_16x16x32_bf16 a[52:55],v[88:91],v[80:83],a[52:55]
v_mfma_f32_16x16x32_bf16 a[84:87],v[88:91],v[96:99],a[84:87]
v_mfma_f32_16x16x32_bf16 a[116:119],v[88:91],v[112:115],a[116:119]
buffer_load_ushort v72,v9,s[24:27],s65 offen offset:192
buffer_load_ushort v73,v9,s[24:27],s65 offen offset:448
buffer_load_ushort v74,v9,s[24:27],s65 offen offset:704
buffer_load_ushort v75,v9,s[24:27],s65 offen offset:960
buffer_load_ushort v76,v10,s[24:27],s65 offen offset:192
buffer_load_ushort v77,v10,s[24:27],s65 offen offset:448
buffer_load_ushort v78,v10,s[24:27],s65 offen offset:704
buffer_load_ushort v79,v10,s[24:27],s65 offen offset:960
s_waitcnt vmcnt(0)
v_lshl_or_b32 v88,v73,16,v72
v_lshl_or_b32 v89,v75,16,v74
v_lshl_or_b32 v90,v77,16,v76
v_lshl_or_b32 v91,v79,16,v78
s_nop 1
v_mfma_f32_16x16x32_bf16 a[24:27],v[88:91],v[64:67],a[24:27]
v_mfma_f32_16x16x32_bf16 a[56:59],v[88:91],v[80:83],a[56:59]
v_mfma_f32_16x16x32_bf16 a[88:91],v[88:91],v[96:99],a[88:91]
v_mfma_f32_16x16x32_bf16 a[120:123],v[88:91],v[112:115],a[120:123]
buffer_load_ushort v72,v9,s[24:27],s65 offen offset:224
buffer_load_ushort v73,v9,s[24:27],s65 offen offset:480
buffer_load_ushort v74,v9,s[24:27],s65 offen offset:736
buffer_load_ushort v75,v9,s[24:27],s65 offen offset:992
buffer_load_ushort v76,v10,s[24:27],s65 offen offset:224
buffer_load_ushort v77,v10,s[24:27],s65 offen offset:480
buffer_load_ushort v78,v10,s[24:27],s65 offen offset:736
buffer_load_ushort v79,v10,s[24:27],s65 offen offset:992
s_waitcnt vmcnt(0)
v_lshl_or_b32 v88,v73,16,v72
v_lshl_or_b32 v89,v75,16,v74
v_lshl_or_b32 v90,v77,16,v76
v_lshl_or_b32 v91,v79,16,v78
s_nop 1
v_mfma_f32_16x16x32_bf16 a[28:31],v[88:91],v[64:67],a[28:31]
v_mfma_f32_16x16x32_bf16 a[60:63],v[88:91],v[80:83],a[60:63]
v_mfma_f32_16x16x32_bf16 a[92:95],v[88:91],v[96:99],a[92:95]
v_mfma_f32_16x16x32_bf16 a[124:127],v[88:91],v[112:115],a[124:127]
v_mfma_f32_16x16x32_bf16 v[48:51],v[28:31],v[64:67],v[48:51]
v_mfma_f32_16x16x32_bf16 v[52:55],v[28:31],v[80:83],v[52:55]
v_mfma_f32_16x16x32_bf16 v[56:59],v[28:31],v[96:99],v[56:59]
v_mfma_f32_16x16x32_bf16 v[60:63],v[28:31],v[112:115],v[60:63]
s_add_u32 s65,s65,8192
s_sub_u32 s66,s66,1
s_cmp_lg_u32 s66,0
s_cbranch_scc1 L_s20_%=
s_nop 7
v_max_f32 v24,0x00800000,v48
v_rcp_f32 v24,v24
v_max_f32 v25,0x00800000,v52
v_rcp_f32 v25,v25
v_max_f32 v26,0x00800000,v56
v_rcp_f32 v26,v26
v_max_f32 v27,0x00800000,v60
v_rcp_f32 v27,v27
v_accvgpr_read_b32 v8,a0
v_accvgpr_read_b32 v9,a1
v_accvgpr_read_b32 v10,a2
v_accvgpr_read_b32 v11,a3
v_accvgpr_read_b32 v12,a4
v_accvgpr_read_b32 v13,a5
v_accvgpr_read_b32 v14,a6
v_accvgpr_read_b32 v15,a7
v_mul_f32 v8,v8,v24
v_mul_f32 v9,v9,v24
v_mul_f32 v10,v10,v24
v_mul_f32 v11,v11,v24
v_mul_f32 v12,v12,v24
v_mul_f32 v13,v13,v24
v_mul_f32 v14,v14,v24
v_mul_f32 v15,v15,v24
v_cvt_pk_bf16_f32 v8,v8,v9
v_cvt_pk_bf16_f32 v9,v10,v11
v_cvt_pk_bf16_f32 v10,v12,v13
v_cvt_pk_bf16_f32 v11,v14,v15
ds_write_b128 v7,v[8:11] offset:6144
v_accvgpr_read_b32 v16,a8
v_accvgpr_read_b32 v17,a9
)A"
R"A(v_accvgpr_read_b32 v18,a10
v_accvgpr_read_b32 v19,a11
v_accvgpr_read_b32 v20,a12
v_accvgpr_read_b32 v21,a13
v_accvgpr_read_b32 v22,a14
v_accvgpr_read_b32 v23,a15
v_mul_f32 v16,v16,v24
v_mul_f32 v17,v17,v24
v_mul_f32 v18,v18,v24
v_mul_f32 v19,v19,v24
v_mul_f32 v20,v20,v24
v_mul_f32 v21,v21,v24
v_mul_f32 v22,v22,v24
v_mul_f32 v23,v23,v24
v_cvt_pk_bf16_f32 v16,v16,v17
v_cvt_pk_bf16_f32 v17,v18,v19
v_cvt_pk_bf16_f32 v18,v20,v21
v_cvt_pk_bf16_f32 v19,v22,v23
ds_write_b128 v7,v[16:19] offset:7168
v_accvgpr_read_b32 v8,a16
v_accvgpr_read_b32 v9,a17
v_accvgpr_read_b32 v10,a18
v_accvgpr_read_b32 v11,a19
v_accvgpr_read_b32 v12,a20
v_accvgpr_read_b32 v13,a21
v_accvgpr_read_b32 v14,a22
v_accvgpr_read_b32 v15,a23
v_mul_f32 v8,v8,v24
v_mul_f32 v9,v9,v24
v_mul_f32 v10,v10,v24
v_mul_f32 v11,v11,v24
v_mul_f32 v12,v12,v24
v_mul_f32 v13,v13,v24
v_mul_f32 v14,v14,v24
v_mul_f32 v15,v15,v24
v_cvt_pk_bf16_f32 v8,v8,v9
v_cvt_pk_bf16_f32 v9,v10,v11
v_cvt_pk_bf16_f32 v10,v12,v13
v_cvt_pk_bf16_f32 v11,v14,v15
ds_write_b128 v7,v[8:11]
v_accvgpr_read_b32 v16,a24
v_accvgpr_read_b32 v17,a25
v_accvgpr_read_b32 v18,a26
v_accvgpr_read_b32 v19,a27
v_accvgpr_read_b32 v20,a28
v_accvgpr_read_b32 v21,a29
v_accvgpr_read_b32 v22,a30
v_accvgpr_read_b32 v23,a31
v_mul_f32 v16,v16,v24
v_mul_f32 v17,v17,v24
v_mul_f32 v18,v18,v24
v_mul_f32 v19,v19,v24
v_mul_f32 v20,v20,v24
v_mul_f32 v21,v21,v24
v_mul_f32 v22,v22,v24
v_mul_f32 v23,v23,v24
v_cvt_pk_bf16_f32 v16,v16,v17
v_cvt_pk_bf16_f32 v17,v18,v19
v_cvt_pk_bf16_f32 v18,v20,v21
v_cvt_pk_bf16_f32 v19,v22,v23
ds_write_b128 v7,v[16:19] offset:1024
v_accvgpr_read_b32 v32,a160
v_accvgpr_read_b32 v33,a161
v_accvgpr_read_b32 v34,a162
v_accvgpr_read_b32 v35,a163
v_accvgpr_read_b32 v36,a164
v_accvgpr_read_b32 v37,a165
v_accvgpr_read_b32 v38,a166
v_accvgpr_read_b32 v39,a167
v_accvgpr_read_b32 v40,a168
v_accvgpr_read_b32 v41,a169
v_accvgpr_read_b32 v42,a170
v_accvgpr_read_b32 v43,a171
v_accvgpr_read_b32 v44,a172
v_accvgpr_read_b32 v45,a173
v_accvgpr_read_b32 v46,a174
v_accvgpr_read_b32 v47,a175
s_waitcnt lgkmcnt(0)
v_subrev_u32 v8,s78,v1
ds_read_b128 a[128:131],v8
ds_read_b128 a[132:135],v8 offset:256
ds_read_b128 a[136:139],v8 offset:512
ds_read_b128 a[140:143],v8 offset:768
ds_read_b128 a[144:147],v8 offset:4096
ds_read_b128 a[148:151],v8 offset:4352
ds_read_b128 a[152:155],v8 offset:4608
ds_read_b128 a[156:159],v8 offset:4864
ds_read_b128 a[160:163],v8 offset:8192
ds_read_b128 a[164:167],v8 offset:8448
ds_read_b128 a[168:171],v8 offset:8704
ds_read_b128 a[172:175],v8 offset:8960
ds_read_b128 a[176:179],v8 offset:12288
ds_read_b128 a[180:183],v8 offset:12544
ds_read_b128 a[184:187],v8 offset:12800
ds_read_b128 a[188:191],v8 offset:13056
s_waitcnt lgkmcnt(15)
v_mfma_f32_16x16x32_bf16 v[64:67],a[128:131],v[128:131],v[32:35]
s_waitcnt lgkmcnt(14)
v_mfma_f32_16x16x32_bf16 v[64:67],a[132:135],v[136:139],v[64:67]
s_waitcnt lgkmcnt(13)
v_mfma_f32_16x16x32_bf16 v[64:67],a[136:139],v[144:147],v[64:67]
s_waitcnt lgkmcnt(12)
v_mfma_f32_16x16x32_bf16 v[64:67],a[140:143],v[152:155],v[64:67]
s_waitcnt lgkmcnt(8)
v_mfma_f32_16x16x32_bf16 v[68:71],a[156:159],v[152:155],v[32:35]
v_mfma_f32_16x16x32_bf16 v[68:71],a[152:155],v[144:147],v[68:71]
v_mfma_f32_16x16x32_bf16 v[68:71],a[148:151],v[136:139],v[68:71]
v_mfma_f32_16x16x32_bf16 v[68:71],a[144:147],v[128:131],v[68:71]
s_waitcnt lgkmcnt(0)
v_mfma_f32_16x16x32_bf16 v[72:75],a[160:163],v[128:131],v[32:35]
v_mfma_f32_16x16x32_bf16 v[72:75],a[164:167],v[136:139],v[72:75]
v_mfma_f32_16x16x32_bf16 v[72:75],a[168:171],v[144:147],v[72:75]
v_mfma_f32_16x16x32_bf16 v[72:75],a[172:175],v[152:155],v[72:75]
v_mfma_f32_16x16x32_bf16 v[76:79],a[188:191],v[152:155],v[32:35]
v_mfma_f32_16x16x32_bf16 v[76:79],a[184:187],v[144:147],v[76:79]
v_mfma_f32_16x16x32_bf16 v[76:79],a[180:183],v[136:139],v[76:79]
v_mfma_f32_16x16x32_bf16 v[76:79],a[176:179],v[128:131],v[76:79]
v_mfma_f32_16x16x32_bf16 v[80:83],a[128:131],v[160:163],v[36:39]
v_mfma_f32_16x16x32_bf16 v[80:83],a[132:135],v[168:171],v[80:83]
v_mfma_f32_16x16x32_bf16 v[80:83],a[136:139],v[176:179],v[80:83]
v_mfma_f32_16x16x32_bf16 v[80:83],a[140:143],v[184:187],v[80:83]
v_mfma_f32_16x16x32_bf16 v[84:87],a[156:159],v[184:187],v[36:39]
v_mfma_f32_16x16x32_bf16 v[84:87],a[152:155],v[176:179],v[84:87]
v_mfma_f32_16x16x32_bf16 v[84:87],a[148:151],v[168:171],v[84:87]
v_mfma_f32_16x16x32_bf16 v[84:87],a[144:147],v[160:163],v[84:87]
v_mfma_f32_16x16x32_bf16 v[88:91],a[160:163],v[160:163],v[36:39]
v_mfma_f32_16x16x32_bf16 v[88:91],a[164:167],v[168:171],v[88:91]
v_mfma_f32_16x16x32_bf16 v[88:91],a[168:171],v[176:179],v[88:91]
v_mfma_f32_16x16x32_bf16 v[88:91],a[172:175],v[184:187],v[88:91]
v_mfma_f32_16x16x32_bf16 v[92:95],a[188:191],v[184:187],v[36:39]
v_mfma_f32_16x16x32_bf16 v[92:95],a[184:187],v[176:179],v[92:95]
v_mfma_f32_16x16x32_bf16 v[92:95],a[180:183],v[168:171],v[92:95]
v_mfma_f32_16x16x32_bf16 v[92:95],a[176:179],v[160:163],v[92:95]
v_mfma_f32_16x16x32_bf16 v[96:99],a[128:131],v[192:195],v[40:43]
v_mfma_f32_16x16x32_bf16 v[96:99],a[132:135],v[200:203],v[96:99]
v_mfma_f32_16x16x32_bf16 v[96:99],a[136:139],v[208:211],v[96:99]
v_mfma_f32_16x16x32_bf16 v[96:99],a[140:143],v[216:219],v[96:99]
v_mfma_f32_16x16x32_bf16 v[100:103],a[156:159],v[216:219],v[40:43]
v_mfma_f32_16x16x32_bf16 v[100:103],a[152:155],v[208:211],v[100:103]
v_mfma_f32_16x16x32_bf16 v[100:103],a[148:151],v[200:203],v[100:103]
v_mfma_f32_16x16x32_bf16 v[100:103],a[144:147],v[192:195],v[100:103]
v_mfma_f32_16x16x32_bf16 v[104:107],a[160:163],v[192:195],v[40:43]
v_mfma_f32_16x16x32_bf16 v[104:107],a[164:167],v[200:203],v[104:107]
v_mfma_f32_16x16x32_bf16 v[104:107],a[168:171],v[208:211],v[104:107]
v_mfma_f32_16x16x32_bf16 v[104:107],a[172:175],v[216:219],v[104:107]
v_mfma_f32_16x16x32_bf16 v[108:111],a[188:191],v[216:219],v[40:43]
v_mfma_f32_16x16x32_bf16 v[108:111],a[184:187],v[208:211],v[108:111]
v_mfma_f32_16x16x32_bf16 v[108:111],a[180:183],v[200:203],v[108:111]
v_mfma_f32_16x16x32_bf16 v[108:111],a[176:179],v[192:195],v[108:111]
v_exp_f32 v64,v64
v_exp_f32 v65,v65
s_nop 0
v_cvt_pk_bf16_f32 v64,v64,v65
v_exp_f32 v66,v66
v_exp_f32 v67,v67
s_nop 0
v_cvt_pk_bf16_f32 v65,v66,v67
v_exp_f32 v68,v68
v_exp_f32 v69,v69
s_nop 0
v_cvt_pk_bf16_f32 v66,v68,v69
v_exp_f32 v70,v70
v_exp_f32 v71,v71
s_nop 0
v_cvt_pk_bf16_f32 v67,v70,v71
v_exp_f32 v72,v72
v_exp_f32 v73,v73
s_nop 0
v_cvt_pk_bf16_f32 v72,v72,v73
v_exp_f32 v74,v74
v_exp_f32 v75,v75
s_nop 0
v_cvt_pk_bf16_f32 v73,v74,v75
v_exp_f32 v76,v76
v_exp_f32 v77,v77
s_nop 0
v_cvt_pk_bf16_f32 v74,v76,v77
v_exp_f32 v78,v78
v_exp_f32 v79,v79
s_nop 0
v_cvt_pk_bf16_f32 v75,v78,v79
v_exp_f32 v80,v80
v_exp_f32 v81,v81
s_nop 0
v_cvt_pk_bf16_f32 v80,v80,v81
v_exp_f32 v82,v82
v_exp_f32 v83,v83
s_nop 0
v_cvt_pk_bf16_f32 v81,v82,v83
v_exp_f32 v84,v84
v_exp_f32 v85,v85
s_nop 0
v_cvt_pk_bf16_f32 v82,v84,v85
v_exp_f32 v86,v86
v_exp_f32 v87,v87
s_nop 0
v_cvt_pk_bf16_f32 v83,v86,v87
v_exp_f32 v88,v88
v_exp_f32 v89,v89
s_nop 0
v_cvt_pk_bf16_f32 v88,v88,v89
v_mfma_f32_16x16x32_bf16 a[0:3],a[192:195],v[64:67],0
v_mfma_f32_16x16x32_bf16 a[0:3],a[196:199],v[72:75],a[0:3]
v_mfma_f32_16x16x32_bf16 a[4:7],a[200:203],v[64:67],0
v_mfma_f32_16x16x32_bf16 a[4:7],a[204:207],v[72:75],a[4:7]
v_mfma_f32_16x16x32_bf16 a[8:11],a[208:211],v[64:67],0
v_mfma_f32_16x16x32_bf16 a[8:11],a[212:215],v[72:75],a[8:11]
v_mfma_f32_16x16x32_bf16 a[12:15],a[216:219],v[64:67],0
v_mfma_f32_16x16x32_bf16 a[12:15],a[220:223],v[72:75],a[12:15]
v_mfma_f32_16x16x32_bf16 a[16:19],a[224:227],v[64:67],0
v_mfma_f32_16x16x32_bf16 a[16:19],a[228:231],v[72:75],a[16:19]
v_mfma_f32_16x16x32_bf16 a[20:23],a[232:235],v[64:67],0
v_mfma_f32_16x16x32_bf16 a[20:23],a[236:239],v[72:75],a[20:23]
v_mfma_f32_16x16x32_bf16 a[24:27],a[240:243],v[64:67],0
v_mfma_f32_16x16x32_bf16 a[24:27],a[244:247],v[72:75],a[24:27]
v_mfma_f32_16x16x32_bf16 a[28:31],a[248:251],v[64:67],0
v_mfma_f32_16x16x32_bf16 a[28:31],a[252:255],v[72:75],a[28:31]
v_mfma_f32_16x16x32_bf16 v[48:51],v[28:31],v[64:67],0
v_mfma_f32_16x16x32_bf16 v[48:51],v[28:31],v[72:75],v[48:51]
s_waitcnt vmcnt(0) lgkmcnt(0)
s_nop 7
s_branch L_ok0_%=
L_slow1_%=:
s_waitcnt vmcnt(0) lgkmcnt(0)
s_nop 7
v_bfe_u32 v11,v6,4,4
v_lshrrev_b32 v12,8,v6
v_lshlrev_b32 v8,8,v11
v_lshl_add_u32 v8,v12,4,v8
v_lshlrev_b32 v9,10,v12
v_lshl_add_u32 v9,v11,1,v9
v_add_u32 v10,4096,v9
v_mov_b32 v48,0xff800000
v_mov_b32 v52,0xff800000
v_mov_b32 v56,0xff800000
v_mov_b32 v60,0xff800000
s_mov_b32 s65,262144
s_mov_b32 s66,64
L_s11_%=:
buffer_load_dwordx4 a[128:131],v8,s[20:23],s65 offen
buffer_load_dwordx4 a[132:135],v8,s[20:23],s65 offen offset:64
buffer_load_dwordx4 a[136:139],v8,s[20:23],s65 offen offset:128
buffer_load_dwordx4 a[140:143],v8,s[20:23],s65 offen offset:192
s_waitcnt vmcnt(0)
v_mfma_f32_16x16x32_bf16 v[64:67],a[128:131],v[128:131],0
v_mfma_f32_16x16x32_bf16 v[64:67],a[132:135],v[136:139],v[64:67]
v_mfma_f32_16x16x32_bf16 v[64:67],a[136:139],v[144:147],v[64:67]
v_mfma_f32_16x16x32_bf16 v[64:67],a[140:143],v[152:155],v[64:67]
v_mfma_f32_16x16x32_bf16 v[80:83],a[128:131],v[160:163],0
v_mfma_f32_16x16x32_bf16 v[80:83],a[132:135],v[168:171],v[80:83]
v_mfma_f32_16x16x32_bf16 v[80:83],a[136:139],v[176:179],v[80:83]
v_mfma_f32_16x16x32_bf16 v[80:83],a[140:143],v[184:187],v[80:83]
v_mfma_f32_16x16x32_bf16 v[96:99],a[128:131],v[192:195],0
v_mfma_f32_16x16x32_bf16 v[96:99],a[132:135],v[200:203],v[96:99]
v_mfma_f32_16x16x32_bf16 v[96:99],a[136:139],v[208:211],v[96:99]
v_mfma_f32_16x16x32_bf16 v[96:99],a[140:143],v[216:219],v[96:99]
v_mfma_f32_16x16x32_bf16 v[112:115],a[128:131],v[224:227],0
v_mfma_f32_16x16x32_bf16 v[112:115],a[132:135],v[232:235],v[112:115]
v_mfma_f32_16x16x32_bf16 v[112:115],a[136:139],v[240:243],v[112:115]
v_mfma_f32_16x16x32_bf16 v[112:115],a[140:143],v[248:251],v[112:115]
s_nop 7
v_max3_f32 v48,v48,v64,v65
v_max3_f32 v48,v48,v66,v67
v_max3_f32 v52,v52,v80,v81
v_max3_f32 v52,v52,v82,v83
v_max3_f32 v56,v56,v96,v97
v_max3_f32 v56,v56,v98,v99
v_max3_f32 v60,v60,v112,v113
v_max3_f32 v60,v60,v114,v115
s_add_u32 s65,s65,4096
s_sub_u32 s66,s66,1
s_cmp_lg_u32 s66,0
s_cbranch_scc1 L_s11_%=
v_mov_b32 v11,v48
s_nop 1
v_permlane32_swap_b32 v48,v11
v_max_f32 v48,v48,v11
v_mov_b32 v11,v48
s_nop 1
v_permlane16_swap_b32 v48,v11
v_max_f32 v48,v48,v11
v_sub_f32 v32,0,v48
v_sub_f32 v33,0,v48
v_sub_f32 v34,0,v48
v_sub_f32 v35,0,v48
v_mov_b32 v11,v52
s_nop 1
v_permlane32_swap_b32 v52,v11
v_max_f32 v52,v52,v11
v_mov_b32 v11,v52
s_nop 1
v_permlane16_swap_b32 v52,v11
v_max_f32 v52,v52,v11
v_sub_f32 v36,0,v52
v_sub_f32 v37,0,v52
v_sub_f32 v38,0,v52
v_sub_f32 v39,0,v52
v_mov_b32 v11,v56
s_nop 1
v_permlane32_swap_b32 v56,v11
v_max_f32 v56,v56,v11
v_mov_b32 v11,v56
s_nop 1
v_permlane16_swap_b32 v56,v11
v_max_f32 v56,v56,v11
v_sub_f32 v40,0,v56
v_sub_f32 v41,0,v56
v_sub_f32 v42,0,v56
v_sub_f32 v43,0,v56
v_mov_b32 v11,v60
s_nop 1
v_permlane32_swap_b32 v60,v11
v_max_f32 v60,v60,v11
v_mov_b32 v11,v60
s_nop 1
v_permlane16_swap_b32 v60,v11
v_max_f32 v60,v60,v11
v_max_f32 v60,s72,v60
v_sub_f32 v44,0,v60
v_sub_f32 v45,0,v60
v_sub_f32 v46,0,v60
v_sub_f32 v47,0,v60
v_mov_b32 v48,0
v_mov_b32 v49,0
v_mov_b32 v50,0
v_mov_b32 v51,0
v_mov_b32 v52,0
v_mov_b32 v53,0
v_mov_b32 v54,0
v_mov_b32 v55,0
v_mov_b32 v56,0
v_mov_b32 v57,0
v_mov_b32 v58,0
v_mov_b32 v59,0
v_mov_b32 v60,0
v_mov_b32 v61,0
v_mov_b32 v62,0
v_mov_b32 v63,0
v_accvgpr_write_b32 a0,0
v_accvgpr_write_b32 a1,0
v_accvgpr_write_b32 a2,0
v_accvgpr_write_b32 a3,0
v_accvgpr_write_b32 a4,0
v_accvgpr_write_b32 a5,0
v_accvgpr_write_b32 a6,0
v_accvgpr_write_b32 a7,0
v_accvgpr_write_b32 a8,0
v_accvgpr_write_b32 a9,0
v_accvgpr_write_b32 a10,0
v_accvgpr_write_b32 a11,0
v_accvgpr_write_b32 a12,0
v_accvgpr_write_b32 a13,0
v_accvgpr_write_b32 a14,0
v_accvgpr_write_b32 a15,0
v_accvgpr_write_b32 a16,0
v_accvgpr_write_b32 a17,0
v_accvgpr_write_b32 a18,0
v_accvgpr_write_b32 a19,0
v_accvgpr_write_b32 a20,0
v_accvgpr_write_b32 a21,0
v_accvgpr_write_b32 a22,0
v_accvgpr_write_b32 a23,0
v_accvgpr_write_b32 a24,0
v_accvgpr_write_b32 a25,0
v_accvgpr_write_b32 a26,0
v_accvgpr_write_b32 a27,0
v_accvgpr_write_b32 a28,0
v_accvgpr_write_b32 a29,0
v_accvgpr_write_b32 a30,0
)A"
R"A(v_accvgpr_write_b32 a31,0
v_mov_b32 v132,0
v_mov_b32 v133,0
v_mov_b32 v134,0
v_mov_b32 v135,0
v_mov_b32 v140,0
v_mov_b32 v141,0
v_mov_b32 v142,0
v_mov_b32 v143,0
v_mov_b32 v148,0
v_mov_b32 v149,0
v_mov_b32 v150,0
v_mov_b32 v151,0
v_mov_b32 v156,0
v_mov_b32 v157,0
v_mov_b32 v158,0
v_mov_b32 v159,0
v_mov_b32 v164,0
v_mov_b32 v165,0
v_mov_b32 v166,0
v_mov_b32 v167,0
v_mov_b32 v172,0
v_mov_b32 v173,0
v_mov_b32 v174,0
v_mov_b32 v175,0
v_mov_b32 v180,0
v_mov_b32 v181,0
v_mov_b32 v182,0
v_mov_b32 v183,0
v_mov_b32 v188,0
v_mov_b32 v189,0
v_mov_b32 v190,0
v_mov_b32 v191,0
v_mov_b32 v196,0
v_mov_b32 v197,0
v_mov_b32 v198,0
v_mov_b32 v199,0
v_mov_b32 v204,0
v_mov_b32 v205,0
v_mov_b32 v206,0
v_mov_b32 v207,0
v_mov_b32 v212,0
v_mov_b32 v213,0
v_mov_b32 v214,0
v_mov_b32 v215,0
v_mov_b32 v220,0
v_mov_b32 v221,0
v_mov_b32 v222,0
v_mov_b32 v223,0
v_mov_b32 v228,0
v_mov_b32 v229,0
v_mov_b32 v230,0
v_mov_b32 v231,0
v_mov_b32 v236,0
v_mov_b32 v237,0
v_mov_b32 v238,0
v_mov_b32 v239,0
v_mov_b32 v244,0
v_mov_b32 v245,0
v_mov_b32 v246,0
v_mov_b32 v247,0
v_mov_b32 v252,0
v_mov_b32 v253,0
v_mov_b32 v254,0
v_mov_b32 v255,0
v_accvgpr_write_b32 a32,0
v_accvgpr_write_b32 a33,0
v_accvgpr_write_b32 a34,0
v_accvgpr_write_b32 a35,0
v_accvgpr_write_b32 a36,0
v_accvgpr_write_b32 a37,0
v_accvgpr_write_b32 a38,0
v_accvgpr_write_b32 a39,0
v_accvgpr_write_b32 a40,0
v_accvgpr_write_b32 a41,0
v_accvgpr_write_b32 a42,0
v_accvgpr_write_b32 a43,0
v_accvgpr_write_b32 a44,0
v_accvgpr_write_b32 a45,0
v_accvgpr_write_b32 a46,0
v_accvgpr_write_b32 a47,0
v_accvgpr_write_b32 a48,0
v_accvgpr_write_b32 a49,0
v_accvgpr_write_b32 a50,0
v_accvgpr_write_b32 a51,0
v_accvgpr_write_b32 a52,0
v_accvgpr_write_b32 a53,0
v_accvgpr_write_b32 a54,0
v_accvgpr_write_b32 a55,0
v_accvgpr_write_b32 a56,0
v_accvgpr_write_b32 a57,0
v_accvgpr_write_b32 a58,0
v_accvgpr_write_b32 a59,0
v_accvgpr_write_b32 a60,0
v_accvgpr_write_b32 a61,0
v_accvgpr_write_b32 a62,0
v_accvgpr_write_b32 a63,0
s_mov_b32 s65,262144
s_mov_b32 s66,32
L_s21_%=:
s_add_u32 s67,s65,4096
buffer_load_dwordx4 a[128:131],v8,s[20:23],s65 offen
buffer_load_dwordx4 a[132:135],v8,s[20:23],s65 offen offset:64
buffer_load_dwordx4 a[136:139],v8,s[20:23],s65 offen offset:128
buffer_load_dwordx4 a[140:143],v8,s[20:23],s65 offen offset:192
buffer_load_dwordx4 a[144:147],v8,s[20:23],s67 offen
buffer_load_dwordx4 a[148:151],v8,s[20:23],s67 offen offset:64
buffer_load_dwordx4 a[152:155],v8,s[20:23],s67 offen offset:128
buffer_load_dwordx4 a[156:159],v8,s[20:23],s67 offen offset:192
s_waitcnt vmcnt(0)
v_mfma_f32_16x16x32_bf16 v[64:67],a[128:131],v[128:131],v[32:35]
v_mfma_f32_16x16x32_bf16 v[64:67],a[132:135],v[136:139],v[64:67]
v_mfma_f32_16x16x32_bf16 v[64:67],a[136:139],v[144:147],v[64:67]
v_mfma_f32_16x16x32_bf16 v[64:67],a[140:143],v[152:155],v[64:67]
v_mfma_f32_16x16x32_bf16 v[68:71],a[144:147],v[128:131],v[32:35]
v_mfma_f32_16x16x32_bf16 v[68:71],a[148:151],v[136:139],v[68:71]
v_mfma_f32_16x16x32_bf16 v[68:71],a[152:155],v[144:147],v[68:71]
v_mfma_f32_16x16x32_bf16 v[68:71],a[156:159],v[152:155],v[68:71]
v_mfma_f32_16x16x32_bf16 v[80:83],a[128:131],v[160:163],v[36:39]
v_mfma_f32_16x16x32_bf16 v[80:83],a[132:135],v[168:171],v[80:83]
v_mfma_f32_16x16x32_bf16 v[80:83],a[136:139],v[176:179],v[80:83]
v_mfma_f32_16x16x32_bf16 v[80:83],a[140:143],v[184:187],v[80:83]
v_mfma_f32_16x16x32_bf16 v[84:87],a[144:147],v[160:163],v[36:39]
v_mfma_f32_16x16x32_bf16 v[84:87],a[148:151],v[168:171],v[84:87]
v_mfma_f32_16x16x32_bf16 v[84:87],a[152:155],v[176:179],v[84:87]
v_mfma_f32_16x16x32_bf16 v[84:87],a[156:159],v[184:187],v[84:87]
v_mfma_f32_16x16x32_bf16 v[96:99],a[128:131],v[192:195],v[40:43]
v_mfma_f32_16x16x32_bf16 v[96:99],a[132:135],v[200:203],v[96:99]
v_mfma_f32_16x16x32_bf16 v[96:99],a[136:139],v[208:211],v[96:99]
v_mfma_f32_16x16x32_bf16 v[96:99],a[140:143],v[216:219],v[96:99]
v_mfma_f32_16x16x32_bf16 v[100:103],a[144:147],v[192:195],v[40:43]
v_mfma_f32_16x16x32_bf16 v[100:103],a[148:151],v[200:203],v[100:103]
v_mfma_f32_16x16x32_bf16 v[100:103],a[152:155],v[208:211],v[100:103]
v_mfma_f32_16x16x32_bf16 v[100:103],a[156:159],v[216:219],v[100:103]
v_mfma_f32_16x16x32_bf16 v[112:115],a[128:131],v[224:227],v[44:47]
v_mfma_f32_16x16x32_bf16 v[112:115],a[132:135],v[232:235],v[112:115]
v_mfma_f32_16x16x32_bf16 v[112:115],a[136:139],v[240:243],v[112:115]
v_mfma_f32_16x16x32_bf16 v[112:115],a[140:143],v[248:251],v[112:115]
v_mfma_f32_16x16x32_bf16 v[116:119],a[144:147],v[224:227],v[44:47]
v_mfma_f32_16x16x32_bf16 v[116:119],a[148:151],v[232:235],v[116:119]
v_mfma_f32_16x16x32_bf16 v[116:119],a[152:155],v[240:243],v[116:119]
v_mfma_f32_16x16x32_bf16 v[116:119],a[156:159],v[248:251],v[116:119]
s_nop 7
v_exp_f32 v64,v64
v_exp_f32 v65,v65
v_exp_f32 v66,v66
v_exp_f32 v67,v67
v_exp_f32 v68,v68
v_exp_f32 v69,v69
v_exp_f32 v70,v70
v_exp_f32 v71,v71
s_nop 0
v_cvt_pk_bf16_f32 v64,v64,v65
v_cvt_pk_bf16_f32 v65,v66,v67
v_cvt_pk_bf16_f32 v66,v68,v69
v_cvt_pk_bf16_f32 v67,v70,v71
v_exp_f32 v80,v80
v_exp_f32 v81,v81
v_exp_f32 v82,v82
v_exp_f32 v83,v83
v_exp_f32 v84,v84
v_exp_f32 v85,v85
v_exp_f32 v86,v86
v_exp_f32 v87,v87
s_nop 0
v_cvt_pk_bf16_f32 v80,v80,v81
v_cvt_pk_bf16_f32 v81,v82,v83
v_cvt_pk_bf16_f32 v82,v84,v85
v_cvt_pk_bf16_f32 v83,v86,v87
v_exp_f32 v96,v96
v_exp_f32 v97,v97
v_exp_f32 v98,v98
v_exp_f32 v99,v99
v_exp_f32 v100,v100
v_exp_f32 v101,v101
v_exp_f32 v102,v102
v_exp_f32 v103,v103
s_nop 0
v_cvt_pk_bf16_f32 v96,v96,v97
v_cvt_pk_bf16_f32 v97,v98,v99
v_cvt_pk_bf16_f32 v98,v100,v101
v_cvt_pk_bf16_f32 v99,v102,v103
v_exp_f32 v112,v112
v_exp_f32 v113,v113
v_exp_f32 v114,v114
v_exp_f32 v115,v115
v_exp_f32 v116,v116
v_exp_f32 v117,v117
v_exp_f32 v118,v118
v_exp_f32 v119,v119
s_nop 0
v_cvt_pk_bf16_f32 v112,v112,v113
v_cvt_pk_bf16_f32 v113,v114,v115
v_cvt_pk_bf16_f32 v114,v116,v117
v_cvt_pk_bf16_f32 v115,v118,v119
buffer_load_ushort v72,v9,s[24:27],s65 offen
buffer_load_ushort v73,v9,s[24:27],s65 offen offset:256
buffer_load_ushort v74,v9,s[24:27],s65 offen offset:512
buffer_load_ushort v75,v9,s[24:27],s65 offen offset:768
buffer_load_ushort v76,v10,s[24:27],s65 offen
buffer_load_ushort v77,v10,s[24:27],s65 offen offset:256
buffer_load_ushort v78,v10,s[24:27],s65 offen offset:512
buffer_load_ushort v79,v10,s[24:27],s65 offen offset:768
s_waitcnt vmcnt(0)
v_lshl_or_b32 v88,v73,16,v72
v_lshl_or_b32 v89,v75,16,v74
v_lshl_or_b32 v90,v77,16,v76
v_lshl_or_b32 v91,v79,16,v78
s_nop 1
v_mfma_f32_16x16x32_bf16 a[0:3],v[88:91],v[64:67],a[0:3]
v_mfma_f32_16x16x32_bf16 v[132:135],v[88:91],v[80:83],v[132:135]
v_mfma_f32_16x16x32_bf16 v[196:199],v[88:91],v[96:99],v[196:199]
v_mfma_f32_16x16x32_bf16 a[32:35],v[88:91],v[112:115],a[32:35]
buffer_load_ushort v72,v9,s[24:27],s65 offen offset:32
buffer_load_ushort v73,v9,s[24:27],s65 offen offset:288
buffer_load_ushort v74,v9,s[24:27],s65 offen offset:544
buffer_load_ushort v75,v9,s[24:27],s65 offen offset:800
buffer_load_ushort v76,v10,s[24:27],s65 offen offset:32
buffer_load_ushort v77,v10,s[24:27],s65 offen offset:288
buffer_load_ushort v78,v10,s[24:27],s65 offen offset:544
buffer_load_ushort v79,v10,s[24:27],s65 offen offset:800
s_waitcnt vmcnt(0)
v_lshl_or_b32 v88,v73,16,v72
v_lshl_or_b32 v89,v75,16,v74
v_lshl_or_b32 v90,v77,16,v76
v_lshl_or_b32 v91,v79,16,v78
s_nop 1
v_mfma_f32_16x16x32_bf16 a[4:7],v[88:91],v[64:67],a[4:7]
v_mfma_f32_16x16x32_bf16 v[140:143],v[88:91],v[80:83],v[140:143]
v_mfma_f32_16x16x32_bf16 v[204:207],v[88:91],v[96:99],v[204:207]
v_mfma_f32_16x16x32_bf16 a[36:39],v[88:91],v[112:115],a[36:39]
buffer_load_ushort v72,v9,s[24:27],s65 offen offset:64
buffer_load_ushort v73,v9,s[24:27],s65 offen offset:320
buffer_load_ushort v74,v9,s[24:27],s65 offen offset:576
buffer_load_ushort v75,v9,s[24:27],s65 offen offset:832
buffer_load_ushort v76,v10,s[24:27],s65 offen offset:64
buffer_load_ushort v77,v10,s[24:27],s65 offen offset:320
buffer_load_ushort v78,v10,s[24:27],s65 offen offset:576
buffer_load_ushort v79,v10,s[24:27],s65 offen offset:832
s_waitcnt vmcnt(0)
v_lshl_or_b32 v88,v73,16,v72
v_lshl_or_b32 v89,v75,16,v74
v_lshl_or_b32 v90,v77,16,v76
v_lshl_or_b32 v91,v79,16,v78
s_nop 1
v_mfma_f32_16x16x32_bf16 a[8:11],v[88:91],v[64:67],a[8:11]
v_mfma_f32_16x16x32_bf16 v[148:151],v[88:91],v[80:83],v[148:151]
v_mfma_f32_16x16x32_bf16 v[212:215],v[88:91],v[96:99],v[212:215]
v_mfma_f32_16x16x32_bf16 a[40:43],v[88:91],v[112:115],a[40:43]
buffer_load_ushort v72,v9,s[24:27],s65 offen offset:96
buffer_load_ushort v73,v9,s[24:27],s65 offen offset:352
buffer_load_ushort v74,v9,s[24:27],s65 offen offset:608
buffer_load_ushort v75,v9,s[24:27],s65 offen offset:864
buffer_load_ushort v76,v10,s[24:27],s65 offen offset:96
buffer_load_ushort v77,v10,s[24:27],s65 offen offset:352
buffer_load_ushort v78,v10,s[24:27],s65 offen offset:608
buffer_load_ushort v79,v10,s[24:27],s65 offen offset:864
s_waitcnt vmcnt(0)
v_lshl_or_b32 v88,v73,16,v72
v_lshl_or_b32 v89,v75,16,v74
v_lshl_or_b32 v90,v77,16,v76
v_lshl_or_b32 v91,v79,16,v78
s_nop 1
v_mfma_f32_16x16x32_bf16 a[12:15],v[88:91],v[64:67],a[12:15]
v_mfma_f32_16x16x32_bf16 v[156:159],v[88:91],v[80:83],v[156:159]
v_mfma_f32_16x16x32_bf16 v[220:223],v[88:91],v[96:99],v[220:223]
v_mfma_f32_16x16x32_bf16 a[44:47],v[88:91],v[112:115],a[44:47]
buffer_load_ushort v72,v9,s[24:27],s65 offen offset:128
buffer_load_ushort v73,v9,s[24:27],s65 offen offset:384
buffer_load_ushort v74,v9,s[24:27],s65 offen offset:640
buffer_load_ushort v75,v9,s[24:27],s65 offen offset:896
buffer_load_ushort v76,v10,s[24:27],s65 offen offset:128
buffer_load_ushort v77,v10,s[24:27],s65 offen offset:384
buffer_load_ushort v78,v10,s[24:27],s65 offen offset:640
buffer_load_ushort v79,v10,s[24:27],s65 offen offset:896
s_waitcnt vmcnt(0)
v_lshl_or_b32 v88,v73,16,v72
v_lshl_or_b32 v89,v75,16,v74
v_lshl_or_b32 v90,v77,16,v76
v_lshl_or_b32 v91,v79,16,v78
s_nop 1
v_mfma_f32_16x16x32_bf16 a[16:19],v[88:91],v[64:67],a[16:19]
v_mfma_f32_16x16x32_bf16 v[164:167],v[88:91],v[80:83],v[164:167]
v_mfma_f32_16x16x32_bf16 v[228:231],v[88:91],v[96:99],v[228:231]
v_mfma_f32_16x16x32_bf16 a[48:51],v[88:91],v[112:115],a[48:51]
buffer_load_ushort v72,v9,s[24:27],s65 offen offset:160
buffer_load_ushort v73,v9,s[24:27],s65 offen offset:416
buffer_load_ushort v74,v9,s[24:27],s65 offen offset:672
buffer_load_ushort v75,v9,s[24:27],s65 offen offset:928
buffer_load_ushort v76,v10,s[24:27],s65 offen offset:160
buffer_load_ushort v77,v10,s[24:27],s65 offen offset:416
buffer_load_ushort v78,v10,s[24:27],s65 offen offset:672
buffer_load_ushort v79,v10,s[24:27],s65 offen offset:928
s_waitcnt vmcnt(0)
v_lshl_or_b32 v88,v73,16,v72
v_lshl_or_b32 v89,v75,16,v74
v_lshl_or_b32 v90,v77,16,v76
v_lshl_or_b32 v91,v79,16,v78
s_nop 1
v_mfma_f32_16x16x32_bf16 a[20:23],v[88:91],v[64:67],a[20:23]
v_mfma_f32_16x16x32_bf16 v[172:175],v[88:91],v[80:83],v[172:175]
v_mfma_f32_16x16x32_bf16 v[236:239],v[88:91],v[96:99],v[236:239]
v_mfma_f32_16x16x32_bf16 a[52:55],v[88:91],v[112:115],a[52:55]
buffer_load_ushort v72,v9,s[24:27],s65 offen offset:192
buffer_load_ushort v73,v9,s[24:27],s65 offen offset:448
buffer_load_ushort v74,v9,s[24:27],s65 offen offset:704
buffer_load_ushort v75,v9,s[24:27],s65 offen offset:960
buffer_load_ushort v76,v10,s[24:27],s65 offen offset:192
buffer_load_ushort v77,v10,s[24:27],s65 offen offset:448
buffer_load_ushort v78,v10,s[24:27],s65 offen offset:704
buffer_load_ushort v79,v10,s[24:27],s65 offen offset:960
s_waitcnt vmcnt(0)
v_lshl_or_b32 v88,v73,16,v72
v_lshl_or_b32 v89,v75,16,v74
v_lshl_or_b32 v90,v77,16,v76
v_lshl_or_b32 v91,v79,16,v78
s_nop 1
v_mfma_f32_16x16x32_bf16 a[24:27],v[88:91],v[64:67],a[24:27]
v_mfma_f32_16x16x32_bf16 v[180:183],v[88:91],v[80:83],v[180:183]
v_mfma_f32_16x16x32_bf16 v[244:247],v[88:91],v[96:99],v[244:247]
v_mfma_f32_16x16x32_bf16 a[56:59],v[88:91],v[112:115],a[56:59]
buffer_load_ushort v72,v9,s[24:27],s65 offen offset:224
buffer_load_ushort v73,v9,s[24:27],s65 offen offset:480
buffer_load_ushort v74,v9,s[24:27],s65 offen offset:736
buffer_load_ushort v75,v9,s[24:27],s65 offen offset:992
buffer_load_ushort v76,v10,s[24:27],s65 offen offset:224
buffer_load_ushort v77,v10,s[24:27],s65 offen offset:480
)A"
R"A(buffer_load_ushort v78,v10,s[24:27],s65 offen offset:736
buffer_load_ushort v79,v10,s[24:27],s65 offen offset:992
s_waitcnt vmcnt(0)
v_lshl_or_b32 v88,v73,16,v72
v_lshl_or_b32 v89,v75,16,v74
v_lshl_or_b32 v90,v77,16,v76
v_lshl_or_b32 v91,v79,16,v78
s_nop 1
v_mfma_f32_16x16x32_bf16 a[28:31],v[88:91],v[64:67],a[28:31]
v_mfma_f32_16x16x32_bf16 v[188:191],v[88:91],v[80:83],v[188:191]
v_mfma_f32_16x16x32_bf16 v[252:255],v[88:91],v[96:99],v[252:255]
v_mfma_f32_16x16x32_bf16 a[60:63],v[88:91],v[112:115],a[60:63]
v_mfma_f32_16x16x32_bf16 v[48:51],v[28:31],v[64:67],v[48:51]
v_mfma_f32_16x16x32_bf16 v[52:55],v[28:31],v[80:83],v[52:55]
v_mfma_f32_16x16x32_bf16 v[56:59],v[28:31],v[96:99],v[56:59]
v_mfma_f32_16x16x32_bf16 v[60:63],v[28:31],v[112:115],v[60:63]
s_add_u32 s65,s65,8192
s_sub_u32 s66,s66,1
s_cmp_lg_u32 s66,0
s_cbranch_scc1 L_s21_%=
s_nop 7
v_max_f32 v49,0x00800000,v48
v_rcp_f32 v49,v49
v_max_f32 v53,0x00800000,v52
v_rcp_f32 v53,v53
v_max_f32 v57,0x00800000,v56
v_rcp_f32 v57,v57
v_max_f32 v61,0x00800000,v60
v_rcp_f32 v61,v61
v_accvgpr_read_b32 v16,a0
v_accvgpr_read_b32 v17,a1
v_accvgpr_read_b32 v18,a2
v_accvgpr_read_b32 v19,a3
v_accvgpr_read_b32 v20,a4
v_accvgpr_read_b32 v21,a5
v_accvgpr_read_b32 v22,a6
v_accvgpr_read_b32 v23,a7
v_mul_f32 v16,v16,v49
v_mul_f32 v17,v17,v49
v_mul_f32 v18,v18,v49
v_mul_f32 v19,v19,v49
v_mul_f32 v20,v20,v49
v_mul_f32 v21,v21,v49
v_mul_f32 v22,v22,v49
v_mul_f32 v23,v23,v49
v_cvt_pk_bf16_f32 v32,v16,v17
v_cvt_pk_bf16_f32 v33,v18,v19
v_cvt_pk_bf16_f32 v34,v20,v21
v_cvt_pk_bf16_f32 v35,v22,v23
v_accvgpr_read_b32 v8,a8
v_accvgpr_read_b32 v9,a9
v_accvgpr_read_b32 v10,a10
v_accvgpr_read_b32 v11,a11
v_accvgpr_read_b32 v12,a12
v_accvgpr_read_b32 v13,a13
v_accvgpr_read_b32 v14,a14
v_accvgpr_read_b32 v15,a15
v_mul_f32 v8,v8,v49
v_mul_f32 v9,v9,v49
v_mul_f32 v10,v10,v49
v_mul_f32 v11,v11,v49
v_mul_f32 v12,v12,v49
v_mul_f32 v13,v13,v49
v_mul_f32 v14,v14,v49
v_mul_f32 v15,v15,v49
v_cvt_pk_bf16_f32 v36,v8,v9
v_cvt_pk_bf16_f32 v37,v10,v11
v_cvt_pk_bf16_f32 v38,v12,v13
v_cvt_pk_bf16_f32 v39,v14,v15
v_accvgpr_read_b32 v16,a16
v_accvgpr_read_b32 v17,a17
v_accvgpr_read_b32 v18,a18
v_accvgpr_read_b32 v19,a19
v_accvgpr_read_b32 v20,a20
v_accvgpr_read_b32 v21,a21
v_accvgpr_read_b32 v22,a22
v_accvgpr_read_b32 v23,a23
v_mul_f32 v16,v16,v49
v_mul_f32 v17,v17,v49
v_mul_f32 v18,v18,v49
v_mul_f32 v19,v19,v49
v_mul_f32 v20,v20,v49
v_mul_f32 v21,v21,v49
v_mul_f32 v22,v22,v49
v_mul_f32 v23,v23,v49
v_cvt_pk_bf16_f32 v40,v16,v17
v_cvt_pk_bf16_f32 v41,v18,v19
v_cvt_pk_bf16_f32 v42,v20,v21
v_cvt_pk_bf16_f32 v43,v22,v23
ds_read_b64_tr_b16 v[64:65],v1
ds_read_b64_tr_b16 v[66:67],v1 offset:5120
ds_read_b64_tr_b16 v[68:69],v1 offset:10240
ds_read_b64_tr_b16 v[70:71],v1 offset:15360
ds_read_b64_tr_b16 v[72:73],v1 offset:20480
ds_read_b64_tr_b16 v[74:75],v1 offset:25600
ds_read_b64_tr_b16 v[76:77],v1 offset:30720
ds_read_b64_tr_b16 v[78:79],v1 offset:35840
ds_read_b64_tr_b16 v[80:81],v2 offset:32
ds_read_b64_tr_b16 v[82:83],v2 offset:5152
ds_read_b64_tr_b16 v[84:85],v2 offset:10272
ds_read_b64_tr_b16 v[86:87],v2 offset:15392
ds_read_b64_tr_b16 v[88:89],v2 offset:20512
ds_read_b64_tr_b16 v[90:91],v2 offset:25632
ds_read_b64_tr_b16 v[92:93],v2 offset:30752
ds_read_b64_tr_b16 v[94:95],v2 offset:35872
s_waitcnt vmcnt(0) lgkmcnt(0)
s_nop 7
s_branch L_ok1_%=
L_end_%=:
)A"
    :: "s"(ka), "s"(blk), "s"(wid)
    : "v0", "v1", "v2", "v3", "v4", "v5", "v6", "v7", "v8", "v9", "v10", "v11", "v12", "v13", "v14", "v15", "v16", "v17", "v18", "v19", "v20", "v21", "v22", "v23", "v24", "v25", "v26", "v27", "v28", "v29", "v30", "v31", "v32", "v33", "v34", "v35", "v36", "v37", "v38", "v39", "v40", "v41", "v42", "v43", "v44", "v45", "v46", "v47", "v48", "v49", "v50", "v51", "v52", "v53", "v54", "v55", "v56", "v57", "v58", "v59", "v60", "v61", "v62", "v63", "v64", "v65", "v66", "v67", "v68", "v69", "v70", "v71", "v72", "v73", "v74", "v75", "v76", "v77", "v78", "v79", "v80", "v81", "v82", "v83", "v84", "v85", "v86", "v87", "v88", "v89", "v90", "v91", "v92", "v93", "v94", "v95", "v96", "v97", "v98", "v99", "v100", "v101", "v102", "v103", "v104", "v105", "v106", "v107", "v108", "v109", "v110", "v111", "v112", "v113", "v114", "v115", "v116", "v117", "v118", "v119", "v120", "v121", "v122", "v123", "v124", "v125", "v126", "v127", "v128", "v129", "v130", "v131", "v132", "v133", "v134", "v135", "v136", "v137", "v138", "v139", "v140", "v141", "v142", "v143", "v144", "v145", "v146", "v147", "v148", "v149", "v150", "v151", "v152", "v153", "v154", "v155", "v156", "v157", "v158", "v159", "v160", "v161", "v162", "v163", "v164", "v165", "v166", "v167", "v168", "v169", "v170", "v171", "v172", "v173", "v174", "v175", "v176", "v177", "v178", "v179", "v180", "v181", "v182", "v183", "v184", "v185", "v186", "v187", "v188", "v189", "v190", "v191", "v192", "v193", "v194", "v195", "v196", "v197", "v198", "v199", "v200", "v201", "v202", "v203", "v204", "v205", "v206", "v207", "v208", "v209", "v210", "v211", "v212", "v213", "v214", "v215", "v216", "v217", "v218", "v219", "v220", "v221", "v222", "v223", "v224", "v225", "v226", "v227", "v228", "v229", "v230", "v231", "v232", "v233", "v234", "v235", "v236", "v237", "v238", "v239", "v240", "v241", "v242", "v243", "v244", "v245", "v246", "v247", "v248", "v249", "v250", "v251", "v252", "v253", "v254", "v255", "a0", "a1", "a2", "a3", "a4", "a5", "a6", "a7", "a8", "a9", "a10", "a11", "a12", "a13", "a14", "a15", "a16", "a17", "a18", "a19", "a20", "a21", "a22", "a23", "a24", "a25", "a26", "a27", "a28", "a29", "a30", "a31", "a32", "a33", "a34", "a35", "a36", "a37", "a38", "a39", "a40", "a41", "a42", "a43", "a44", "a45", "a46", "a47", "a48", "a49", "a50", "a51", "a52", "a53", "a54", "a55", "a56", "a57", "a58", "a59", "a60", "a61", "a62", "a63", "a64", "a65", "a66", "a67", "a68", "a69", "a70", "a71", "a72", "a73", "a74", "a75", "a76", "a77", "a78", "a79", "a80", "a81", "a82", "a83", "a84", "a85", "a86", "a87", "a88", "a89", "a90", "a91", "a92", "a93", "a94", "a95", "a96", "a97", "a98", "a99", "a100", "a101", "a102", "a103", "a104", "a105", "a106", "a107", "a108", "a109", "a110", "a111", "a112", "a113", "a114", "a115", "a116", "a117", "a118", "a119", "a120", "a121", "a122", "a123", "a124", "a125", "a126", "a127", "a128", "a129", "a130", "a131", "a132", "a133", "a134", "a135", "a136", "a137", "a138", "a139", "a140", "a141", "a142", "a143", "a144", "a145", "a146", "a147", "a148", "a149", "a150", "a151", "a152", "a153", "a154", "a155", "a156", "a157", "a158", "a159", "a160", "a161", "a162", "a163", "a164", "a165", "a166", "a167", "a168", "a169", "a170", "a171", "a172", "a173", "a174", "a175", "a176", "a177", "a178", "a179", "a180", "a181", "a182", "a183", "a184", "a185", "a186", "a187", "a188", "a189", "a190", "a191", "a192", "a193", "a194", "a195", "a196", "a197", "a198", "a199", "a200", "a201", "a202", "a203", "a204", "a205", "a206", "a207", "a208", "a209", "a210", "a211", "a212", "a213", "a214", "a215", "a216", "a217", "a218", "a219", "a220", "a221", "a222", "a223", "a224", "a225", "a226", "a227", "a228", "a229", "a230", "a231", "a232", "a233", "a234", "a235", "a236", "a237", "a238", "a239", "a240", "a241", "a242", "a243", "a244", "a245", "a246", "a247", "a248", "a249", "a250", "a251", "a252", "a253", "a254", "a255", "s4", "s5", "s6", "s7", "s8", "s9", "s10", "s11", "s12", "s13", "s14", "s15", "s16", "s17", "s18", "s19", "s20", "s21", "s22", "s23", "s24", "s25", "s26", "s27", "s28", "s29", "s30", "s31", "s36", "s37", "s38", "s39", "s40", "s41", "s42", "s43", "s44", "s45", "s46", "s47", "s48", "s49", "s50", "s51", "s52", "s53", "s54", "s55", "s56", "s57", "s58", "s59", "s60", "s61", "s62", "s63", "s64", "s65", "s66", "s67", "s68", "s69", "s70", "s71", "s72", "s73", "s74", "s75", "s76", "s77", "s78", "s79", "s80", "s81", "s82", "s83", "s84", "s85", "s86", "s87", "s88", "s89", "s90", "s91", "s92", "s93", "s94", "s95", "s96", "s97", "s98", "s99", "vcc", "scc", "memory");
}

extern "C" void kernel_launch(void* const* d_in, const int* in_sizes, int n_in,
                              void* d_out, int out_size, void* d_ws, size_t ws_size,
                              hipStream_t stream) {
  (void)hipFuncSetAttribute((const void*)qattn_kernel, hipFuncAttributeMaxDynamicSharedMemorySize, LDS_ATT);
  qattn_kernel<<<256, 256, LDS_ATT, stream>>>(d_in[0], d_in[1], d_in[2], d_in[3], d_ws, d_in[4], d_out);
}
```

```cpp
#include <hip/hip_runtime.h>
#include <hip/hip_bf16.h>

constexpr int LDS_ATT = 163840;

__global__ __launch_bounds__(256, 1) __attribute__((aligned(4096))) void qattn_kernel(const void* __restrict__ x, const void* __restrict__ kc, const void* __restrict__ vc,
                                                       const void* __restrict__ wq, void* __restrict__ ws, const void* __restrict__ wo,
                                                       void* __restrict__ out) {
  const unsigned blk = blockIdx.x;
  const unsigned wid = __builtin_amdgcn_readfirstlane(threadIdx.x >> 6);
  auto ka = __builtin_amdgcn_kernarg_segment_ptr();
  asm volatile(
R"A(s_load_dwordx8 s[4:11],%0,0x0
s_load_dwordx4 s[12:15],%0,0x20
s_load_dwordx2 s[16:17],%0,0x30
v_mbcnt_lo_u32_b32 v23,-1,0
v_mbcnt_hi_u32_b32 v23,-1,v23
s_mov_b32 s60,%2
s_and_b32 s66,%1,7
s_lshr_b32 s67,%1,3
s_getpc_b64 s[20:21]
L_pc_%=:
s_and_b32 s20,s20,0xffffff80
s_and_b32 s21,s21,0xffff
s_mov_b32 s22,L_hot_%=-L_pc_%=
s_mov_b32 s23,0x20000
s_lshl_b32 s68,s67,2
s_add_u32 s68,s68,s60
s_mul_i32 s68,s68,896
v_lshlrev_b32 v67,7,v23
s_mov_b64 exec,127
buffer_load_dword v8,v67,s[20:23],s68 offen
s_mov_b64 exec,-1
s_lshr_b32 s61,s66,2
s_lshl_b32 s61,s61,3
s_lshr_b32 s68,s67,2
s_add_u32 s61,s61,s68
s_and_b32 s62,s66,1
s_lshl_b32 s62,s62,2
s_and_b32 s68,s66,2
s_add_u32 s62,s62,s68
s_lshl_b32 s62,s62,1
s_and_b32 s68,s67,3
s_add_u32 s62,s62,s68
s_lshl_b32 s83,s62,9
s_mov_b32 s84,0
s_lshl_b32 s89,s60,13
s_add_u32 s85,s89,65536
s_add_u32 s88,s89,131072
v_and_b32 v20,3,v23
v_bfe_u32 v21,v23,2,2
v_lshrrev_b32 v22,4,v23
v_lshrrev_b32 v75,3,v23
s_lshl_b32 s66,s60,6
v_add_u32 v75,s66,v75
v_mul_u32_u24 v75,1280,v75
v_and_b32 v76,7,v23
v_or_b32 v77,0,v22
v_xor_b32 v77,v77,v76
v_lshl_add_u32 v77,v77,4,v75
v_add_u32 v32,0,v77
v_add_u32 v34,18432,v77
v_add_u32 v36,40960,v77
v_add_u32 v38,59392,v77
v_or_b32 v77,4,v22
v_xor_b32 v77,v77,v76
v_lshl_add_u32 v77,v77,4,v75
v_add_u32 v33,9216,v77
v_add_u32 v35,27648,v77
v_add_u32 v37,50176,v77
v_add_u32 v39,68608,v77
s_lshl_b32 s66,s60,17
v_lshlrev_b32 v75,13,v21
v_lshl_add_u32 v75,v22,6,v75
v_lshl_add_u32 v75,v20,4,v75
v_add_u32 v75,s66,v75
v_add_u32 v40,0,v75
v_add_u32 v41,256,v75
v_add_u32 v42,32768,v75
v_add_u32 v43,33024,v75
v_add_u32 v44,65536,v75
v_add_u32 v45,65792,v75
v_add_u32 v46,98304,v75
v_add_u32 v47,98560,v75
s_mul_i32 s66,s61,307200
s_waitcnt lgkmcnt(0)
s_add_u32 s28,s4,s66
s_addc_u32 s29,s5,0
s_and_b32 s29,s29,0xffff
s_mov_b32 s30,307200
s_mov_b32 s31,0x20000
s_mov_b32 s36,s10
s_and_b32 s37,s11,0xffff
s_mov_b32 s38,5242880
s_mov_b32 s39,0x20000
s_add_u32 m0,s85,0
s_nop 0
buffer_load_dwordx4 v32,s[28:31],s84 offen lds
buffer_load_dwordx4 v33,s[28:31],s84 offen offset:1024 lds
buffer_load_dwordx4 v34,s[28:31],s84 offen offset:2048 lds
buffer_load_dwordx4 v35,s[28:31],s84 offen offset:3072 lds
s_add_u32 m0,s85,4096
s_nop 0
buffer_load_dwordx4 v36,s[28:31],s84 offen lds
buffer_load_dwordx4 v37,s[28:31],s84 offen offset:1024 lds
buffer_load_dwordx4 v38,s[28:31],s84 offen offset:2048 lds
buffer_load_dwordx4 v39,s[28:31],s84 offen offset:3072 lds
s_add_u32 s84,s84,128
s_xor_b32 s85,s85,98304
s_add_u32 m0,s88,0
s_nop 0
buffer_load_dwordx4 v40,s[36:39],s83 offen lds
s_add_u32 m0,s88,2048
s_nop 0
buffer_load_dwordx4 v42,s[36:39],s83 offen lds
s_add_u32 m0,s88,4096
s_nop 0
buffer_load_dwordx4 v44,s[36:39],s83 offen lds
s_add_u32 m0,s88,6144
s_nop 0
buffer_load_dwordx4 v46,s[36:39],s83 offen lds
s_lshl_b32 s67,s61,21
s_add_u32 s68,s67,101711872
s_add_u32 s44,s12,s68
s_addc_u32 s45,s13,0
s_and_b32 s45,s45,0xffff
s_mov_b32 s46,4096
s_mov_b32 s47,0x20000
v_mov_b32 v10,0
buffer_load_dword v9,v10,s[44:47],0 offen sc1
v_lshlrev_b32 v6,4,v23
v_lshlrev_b32 v72,12,v22
v_lshl_add_u32 v72,v21,6,v72
v_lshl_add_u32 v72,v20,4,v72
v_subrev_u32 v51,s89,v72
v_add_u32 v50,131072,v72
v_and_b32 v74,15,v23
v_lshrrev_b32 v76,1,v74
v_lshlrev_b32 v75,7,v74
s_add_u32 s66,s89,65536
v_add_u32 v75,s66,v75
v_or_b32 v77,0,v22
v_xor_b32 v77,v77,v76
v_lshl_add_u32 v48,v77,4,v75
v_or_b32 v77,4,v22
v_xor_b32 v77,v77,v76
v_lshl_add_u32 v49,v77,4,v75
s_add_u32 s86,s89,131072
s_add_u32 s87,s89,98304
s_mov_b64 exec,0
s_cmp_eq_u32 s60,0
s_cbranch_scc1 L_pd1_%=
s_cmp_eq_u32 s60,1
s_cbranch_scc1 L_pd2_%=
s_cmp_eq_u32 s60,2
s_cbranch_scc1 L_pd3_%=
s_branch L_pd4_%=
L_trt0_%=:
s_mov_b64 exec,-1
s_add_u32 m0,s88,1024
s_nop 0
buffer_load_dwordx4 v41,s[36:39],s83 offen lds
s_add_u32 m0,s88,3072
s_nop 0
buffer_load_dwordx4 v43,s[36:39],s83 offen lds
s_add_u32 m0,s88,5120
s_nop 0
buffer_load_dwordx4 v45,s[36:39],s83 offen lds
s_add_u32 m0,s88,7168
s_nop 0
buffer_load_dwordx4 v47,s[36:39],s83 offen lds
s_add_u32 s83,s83,524288
s_mov_b32 s88,s87
s_add_u32 m0,s88,0
s_nop 0
buffer_load_dwordx4 v40,s[36:39],s83 offen lds
s_add_u32 m0,s88,1024
s_nop 0
buffer_load_dwordx4 v41,s[36:39],s83 offen lds
s_add_u32 m0,s88,2048
s_nop 0
buffer_load_dwordx4 v42,s[36:39],s83 offen lds
s_add_u32 m0,s88,3072
s_nop 0
buffer_load_dwordx4 v43,s[36:39],s83 offen lds
s_add_u32 m0,s88,4096
s_nop 0
buffer_load_dwordx4 v44,s[36:39],s83 offen lds
s_add_u32 m0,s88,5120
s_nop 0
buffer_load_dwordx4 v45,s[36:39],s83 offen lds
s_add_u32 m0,s88,6144
s_nop 0
buffer_load_dwordx4 v46,s[36:39],s83 offen lds
s_add_u32 m0,s88,7168
s_nop 0
buffer_load_dwordx4 v47,s[36:39],s83 offen lds
s_add_u32 s83,s83,524288
s_add_u32 s88,s89,0
s_mov_b64 exec,0
s_cmp_eq_u32 s60,0
s_cbranch_scc1 L_pd9_%=
s_cmp_eq_u32 s60,1
s_cbranch_scc1 L_pd10_%=
s_cmp_eq_u32 s60,2
s_cbranch_scc1 L_pd11_%=
s_branch L_pd12_%=
L_t1b_%=:
s_cmp_ge_u32 s62,8
s_cbranch_scc1 L_pd19_%=
s_branch L_pd14_%=
L_t1c_%=:
s_cmp_ge_u32 s62,8
s_cbranch_scc1 L_pd20_%=
s_branch L_pd15_%=
s_nop 0
s_nop 0
s_nop 0
s_nop 0
s_nop 0
s_nop 0
L_trt1_%=:
s_mov_b64 exec,-1
s_branch L_pacx_%=
L_pace_%=:
s_cmp_ge_u32 s4,8
s_cselect_b32 s5,3,1
s_sub_u32 s5,s4,s5
s_max_i32 s5,s5,s7
L_pacl_%=:
s_sub_u32 s4,s4,1
s_cmp_eq_u32 s4,0
s_cbranch_scc1 L_pace2_%=
s_nop 2
s_branch L_pacl_%=
L_pace2_%=:
s_setpc_b64 s[92:93]
L_pacx_%=:
s_waitcnt vmcnt(17)
ds_read_b128 a[192:195],v48
ds_read_b128 a[196:199],v49
ds_read_b128 a[200:203],v48 offset:2048
ds_read_b128 a[204:207],v49 offset:2048
ds_read_b128 a[208:211],v48 offset:4096
ds_read_b128 a[212:215],v49 offset:4096
ds_read_b128 a[216:219],v48 offset:6144
ds_read_b128 a[220:223],v49 offset:6144
s_waitcnt vmcnt(13)
s_barrier
ds_read_b64_tr_b16 a[64:65],v50
ds_read_b64_tr_b16 a[66:67],v50 offset:2048
ds_read_b64_tr_b16 a[68:69],v50 offset:16384
ds_read_b64_tr_b16 a[70:71],v50 offset:18432
s_waitcnt lgkmcnt(2)
v_mfma_f32_16x16x32_bf16 a[0:3],a[64:67],a[192:195],0
s_add_u32 m0,s85,0
ds_read_b64_tr_b16 a[72:73],v50 offset:8
v_xor_b32 v48,98304,v48
s_waitcnt lgkmcnt(1)
v_mfma_f32_16x16x32_bf16 a[0:3],a[68:71],a[196:199],a[0:3]
buffer_load_dwordx4 v32,s[28:31],s84 offen lds
v_mfma_f32_16x16x32_bf16 a[16:19],a[68:71],a[204:207],0
s_mov_b32 s69,s86
ds_read_b64_tr_b16 a[74:75],v50 offset:2056
v_xor_b32 v49,98304,v49
v_mfma_f32_16x16x32_bf16 a[16:19],a[64:67],a[200:203],a[16:19]
s_mov_b32 s86,s87
s_mov_b32 s87,s88
ds_read_b64_tr_b16 a[76:77],v50 offset:16392
v_mfma_f32_16x16x32_bf16 a[32:35],a[64:67],a[208:211],0
ds_read_b64_tr_b16 a[78:79],v50 offset:18440
v_mfma_f32_16x16x32_bf16 a[32:35],a[68:71],a[212:215],a[32:35]
ds_read_b64_tr_b16 a[80:81],v50 offset:256
v_mfma_f32_16x16x32_bf16 a[48:51],a[68:71],a[220:223],0
buffer_load_dwordx4 v33,s[28:31],s84 offen offset:1024 lds
v_mfma_f32_16x16x32_bf16 a[48:51],a[64:67],a[216:219],a[48:51]
s_waitcnt vmcnt(14) lgkmcnt(1)
ds_read_b64_tr_b16 a[82:83],v50 offset:2304
v_mfma_f32_16x16x32_bf16 a[4:7],a[72:75],a[192:195],0
s_lshl_b32 s65,s61,5
ds_read_b64_tr_b16 a[84:85],v50 offset:16640
v_cmp_lt_u32 vcc,6,v23
v_mfma_f32_16x16x32_bf16 a[4:7],a[76:79],a[196:199],a[4:7]
s_lshl_b32 s66,s62,1
ds_read_b64_tr_b16 a[86:87],v50 offset:18688
v_mov_b32 v17,0
v_mfma_f32_16x16x32_bf16 a[20:23],a[76:79],a[204:207],0
s_add_u32 s65,s65,s66
s_lshl_b32 s66,s65,18
v_readfirstlane_b32 s52,v9
v_mfma_f32_16x16x32_bf16 a[20:23],a[72:75],a[200:203],a[20:23]
buffer_load_dwordx4 v34,s[28:31],s84 offen offset:2048 lds
v_mfma_f32_16x16x32_bf16 a[36:39],a[72:75],a[208:211],0
s_add_u32 s20,s6,s66
s_addc_u32 s21,s7,0
ds_read_b64_tr_b16 a[88:89],v50 offset:264
v_mfma_f32_16x16x32_bf16 a[36:39],a[76:79],a[212:215],a[36:39]
s_and_b32 s21,s21,0xffff
s_mov_b32 s22,524288
ds_read_b64_tr_b16 a[90:91],v50 offset:2312
v_mfma_f32_16x16x32_bf16 a[52:55],a[76:79],a[220:223],0
s_mov_b32 s23,0x20000
s_mov_b32 s26,524288
ds_read_b64_tr_b16 a[92:93],v50 offset:16648
v_mfma_f32_16x16x32_bf16 a[52:55],a[72:75],a[216:219],a[52:55]
s_add_u32 s24,s8,s66
s_addc_u32 s25,s9,0
ds_read_b64_tr_b16 a[94:95],v50 offset:18696
s_waitcnt lgkmcnt(6)
v_mfma_f32_16x16x32_bf16 a[8:11],a[80:83],a[192:195],0
buffer_load_dwordx4 v35,s[28:31],s84 offen offset:3072 lds
s_waitcnt lgkmcnt(4)
v_mfma_f32_16x16x32_bf16 a[8:11],a[84:87],a[196:199],a[8:11]
s_and_b32 s25,s25,0xffff
s_mov_b32 s27,0x20000
ds_read_b64_tr_b16 a[96:97],v50 offset:512
v_mfma_f32_16x16x32_bf16 a[24:27],a[84:87],a[204:207],0
s_lshl_b32 s66,s65,16
s_mov_b32 s42,131072
ds_read_b64_tr_b16 a[98:99],v50 offset:2560
v_mfma_f32_16x16x32_bf16 a[24:27],a[80:83],a[200:203],a[24:27]
s_add_u32 m0,s85,4096
s_xor_b32 s85,s85,98304
ds_read_b64_tr_b16 a[100:101],v50 offset:16896
v_mfma_f32_16x16x32_bf16 a[40:43],a[80:83],a[208:211],0
s_add_u32 s40,s12,s66
s_addc_u32 s41,s13,0
s_and_b32 s41,s41,0xffff
v_mfma_f32_16x16x32_bf16 a[40:43],a[84:87],a[212:215],a[40:43]
buffer_load_dwordx4 v36,s[28:31],s84 offen lds
v_mfma_f32_16x16x32_bf16 a[56:59],a[84:87],a[220:223],0
s_mov_b32 s43,0x20000
s_mov_b32 s48,s14
ds_read_b64_tr_b16 a[102:103],v50 offset:18944
v_mfma_f32_16x16x32_bf16 a[56:59],a[80:83],a[216:219],a[56:59]
s_and_b32 s49,s15,0xffff
s_mov_b32 s50,5242880
ds_read_b64_tr_b16 a[104:105],v50 offset:520
s_waitcnt lgkmcnt(5)
v_mfma_f32_16x16x32_bf16 a[12:15],a[88:91],a[192:195],0
s_mov_b32 s51,0x20000
s_lshl_b32 s67,s60,12
ds_read_b64_tr_b16 a[106:107],v50 offset:2568
v_mfma_f32_16x16x32_bf16 a[12:15],a[92:95],a[196:199],a[12:15]
s_add_u32 s76,s67,98304
s_mul_i32 s74,s61,5
ds_read_b64_tr_b16 a[108:109],v50 offset:16904
v_mfma_f32_16x16x32_bf16 a[28:31],a[92:95],a[204:207],0
buffer_load_dwordx4 v37,s[28:31],s84 offen offset:1024 lds
v_mfma_f32_16x16x32_bf16 a[28:31],a[88:91],a[200:203],a[28:31]
s_add_u32 s74,s74,s62
s_and_b32 s74,s74,15
ds_read_b64_tr_b16 a[110:111],v50 offset:18952
v_mfma_f32_16x16x32_bf16 a[44:47],a[88:91],a[208:211],0
s_lshl_b32 s74,s74,14
s_mov_b32 s75,0
ds_read_b64_tr_b16 a[112:113],v50 offset:768
v_mfma_f32_16x16x32_bf16 a[44:47],a[92:95],a[212:215],a[44:47]
s_mov_b32 s77,98304
s_mov_b32 s71,0x3e0293ee
ds_read_b64_tr_b16 a[114:115],v50 offset:2816
v_mfma_f32_16x16x32_bf16 a[60:63],a[92:95],a[220:223],0
s_mov_b32 s73,0x53800000
s_lshl_b32 s79,s60,14
s_add_u32 s80,s79,4096
v_mfma_f32_16x16x32_bf16 a[60:63],a[88:91],a[216:219],a[60:63]
buffer_load_dwordx4 v38,s[28:31],s84 offen offset:2048 lds
s_waitcnt lgkmcnt(5)
v_mfma_f32_16x16x32_bf16 v[64:67],a[96:99],a[192:195],0
s_add_u32 s81,s79,8192
s_add_u32 s82,s79,12288
ds_read_b64_tr_b16 a[116:117],v50 offset:17152
v_mfma_f32_16x16x32_bf16 v[64:67],a[100:103],a[196:199],v[64:67]
s_lshr_b32 s63,s62,2
s_and_b32 s64,s62,3
ds_read_b64_tr_b16 a[118:119],v50 offset:19200
v_mfma_f32_16x16x32_bf16 v[80:83],a[100:103],a[204:207],0
s_mov_b32 s72,0xff800000
s_lshl_b32 s18,s60,24
ds_read_b64_tr_b16 a[120:121],v50 offset:776
v_mfma_f32_16x16x32_bf16 v[80:83],a[96:99],a[200:203],v[80:83]
s_lshl_b32 s68,s62,6
s_add_u32 s18,s18,s68
ds_read_b64_tr_b16 a[122:123],v50 offset:2824
v_mfma_f32_16x16x32_bf16 v[96:99],a[96:99],a[208:211],0
buffer_load_dwordx4 v39,s[28:31],s84 offen offset:3072 lds
v_mfma_f32_16x16x32_bf16 v[96:99],a[100:103],a[212:215],v[96:99]
s_add_u32 s84,s84,128
ds_read_b64_tr_b16 a[124:125],v50 offset:17160
v_lshlrev_b32 v19,3,v6
v_mfma_f32_16x16x32_bf16 v[112:115],a[100:103],a[220:223],0
s_cmp_eq_u32 s60,3
s_cselect_b32 s72,0x7149f2ca,s72
ds_read_b64_tr_b16 a[126:127],v50 offset:19208
v_mfma_f32_16x16x32_bf16 v[112:115],a[96:99],a[216:219],v[112:115]
s_add_u32 m0,s88,0
v_cndmask_b32_e64 v19,v19,-1,vcc
v_lshrrev_b32 v18,1,v19
s_waitcnt lgkmcnt(6)
v_mfma_f32_16x16x32_bf16 v[68:71],a[104:107],a[192:195],0
s_lshl_b32 s90,%1,2
s_add_u32 s90,s90,s60
v_min_u32 v19,v19,v19
v_mfma_f32_16x16x32_bf16 v[68:71],a[108:111],a[196:199],v[68:71]
buffer_load_dwordx4 v40,s[36:39],s83 offen lds
v_mfma_f32_16x16x32_bf16 v[84:87],a[108:111],a[204:207],0
s_mul_i32 s90,s90,5120
s_mul_i32 s58,s61,307200
v_cndmask_b32_e64 v19,v19,v18,vcc
v_mfma_f32_16x16x32_bf16 v[84:87],a[104:107],a[200:203],v[84:87]
s_add_u32 s56,s40,52224
s_addc_u32 s57,s41,0
s_add_u32 s52,s52,1
v_mfma_f32_16x16x32_bf16 v[100:103],a[104:107],a[208:211],0
)A"
R"A(s_add_u32 m0,s88,1024
s_add_u32 s58,s16,s58
s_addc_u32 s59,s17,0
v_mfma_f32_16x16x32_bf16 v[100:103],a[108:111],a[212:215],v[100:103]
s_cmp_eq_u32 s60,1
s_cselect_b64 s[56:57],s[58:59],s[56:57]
s_xor_b32 s53,s52,0x9e3779b9
v_mfma_f32_16x16x32_bf16 v[116:119],a[108:111],a[220:223],0
buffer_load_dwordx4 v41,s[36:39],s83 offen lds
v_mfma_f32_16x16x32_bf16 v[116:119],a[104:107],a[216:219],v[116:119]
s_cmp_eq_u32 s60,2
s_cselect_b64 s[56:57],s[20:21],s[56:57]
v_lshlrev_b32 v11,8,v21
s_waitcnt vmcnt(18)
s_barrier
v_mfma_f32_16x16x32_bf16 v[72:75],a[112:115],a[192:195],0
s_add_u32 m0,s88,2048
ds_read_b64_tr_b16 a[64:65],v50 offset:1024
v_lshl_add_u32 v11,v22,6,v11
s_waitcnt lgkmcnt(1)
v_mfma_f32_16x16x32_bf16 v[72:75],a[116:119],a[196:199],v[72:75]
s_cmp_eq_u32 s60,3
s_cselect_b64 s[56:57],s[24:25],s[56:57]
ds_read_b64_tr_b16 a[66:67],v50 offset:3072
v_mfma_f32_16x16x32_bf16 v[88:91],a[116:119],a[204:207],0
buffer_load_dwordx4 v42,s[36:39],s83 offen lds
v_mfma_f32_16x16x32_bf16 v[88:91],a[112:115],a[200:203],v[88:91]
ds_read_b64_tr_b16 a[68:69],v50 offset:17408
v_add_u32 v11,s67,v11
v_mfma_f32_16x16x32_bf16 v[104:107],a[112:115],a[208:211],0
ds_read_b64_tr_b16 a[70:71],v50 offset:19456
v_lshl_add_u32 v4,v20,4,v11
v_mfma_f32_16x16x32_bf16 v[104:107],a[116:119],a[212:215],v[104:107]
s_add_u32 m0,s88,3072
ds_read_b64_tr_b16 a[72:73],v50 offset:1032
v_xor_b32 v12,2,v20
v_mfma_f32_16x16x32_bf16 v[120:123],a[116:119],a[220:223],0
ds_read_b64_tr_b16 a[74:75],v50 offset:3080
v_lshl_add_u32 v5,v12,4,v11
v_mfma_f32_16x16x32_bf16 v[120:123],a[112:115],a[216:219],v[120:123]
buffer_load_dwordx4 v43,s[36:39],s83 offen lds
v_mfma_f32_16x16x32_bf16 v[76:79],a[120:123],a[192:195],0
ds_read_b64_tr_b16 a[76:77],v50 offset:17416
v_lshlrev_b32 v11,10,v21
v_mfma_f32_16x16x32_bf16 v[76:79],a[124:127],a[196:199],v[76:79]
ds_read_b64_tr_b16 a[78:79],v50 offset:19464
v_lshl_add_u32 v11,v20,6,v11
v_mfma_f32_16x16x32_bf16 v[92:95],a[124:127],a[204:207],0
s_add_u32 m0,s88,4096
ds_read_b64_tr_b16 a[80:81],v50 offset:1280
v_and_b32 v12,2,v21
v_mfma_f32_16x16x32_bf16 v[92:95],a[120:123],a[200:203],v[92:95]
v_xor_b32 v12,v12,v22
v_lshl_add_u32 v11,v12,4,v11
v_mfma_f32_16x16x32_bf16 v[108:111],a[120:123],a[208:211],0
buffer_load_dwordx4 v44,s[36:39],s83 offen lds
v_mfma_f32_16x16x32_bf16 v[108:111],a[124:127],a[212:215],v[108:111]
ds_read_b64_tr_b16 a[82:83],v50 offset:3328
v_add_u32 v1,98304,v11
v_mfma_f32_16x16x32_bf16 v[124:127],a[124:127],a[220:223],0
s_waitcnt lgkmcnt(4)
ds_read_b64_tr_b16 a[84:85],v50 offset:17664
v_lshlrev_b32 v11,10,v22
v_mfma_f32_16x16x32_bf16 v[124:127],a[120:123],a[216:219],v[124:127]
s_add_u32 m0,s88,5120
ds_read_b64_tr_b16 a[86:87],v50 offset:19712
v_lshl_add_u32 v11,v21,6,v11
v_mfma_f32_16x16x32_bf16 v[128:131],a[64:67],a[192:195],0
ds_read_b64_tr_b16 a[88:89],v50 offset:1288
v_lshl_add_u32 v11,v20,3,v11
v_mfma_f32_16x16x32_bf16 v[128:131],a[68:71],a[196:199],v[128:131]
buffer_load_dwordx4 v45,s[36:39],s83 offen lds
v_mfma_f32_16x16x32_bf16 v[160:163],a[68:71],a[204:207],0
ds_read_b64_tr_b16 a[90:91],v50 offset:3336
v_add_u32 v11,114688,v11
v_mfma_f32_16x16x32_bf16 v[160:163],a[64:67],a[200:203],v[160:163]
ds_read_b64_tr_b16 a[92:93],v50 offset:17672
v_and_b32 v12,1,v22
v_mfma_f32_16x16x32_bf16 v[192:195],a[64:67],a[208:211],0
s_add_u32 m0,s88,6144
ds_read_b64_tr_b16 a[94:95],v50 offset:19720
v_lshl_add_u32 v2,v12,5,v11
v_mfma_f32_16x16x32_bf16 v[192:195],a[68:71],a[212:215],v[192:195]
v_xor_b32 v12,1,v12
v_lshl_add_u32 v3,v12,5,v11
v_mfma_f32_16x16x32_bf16 v[224:227],a[68:71],a[220:223],0
buffer_load_dwordx4 v46,s[36:39],s83 offen lds
v_mfma_f32_16x16x32_bf16 v[224:227],a[64:67],a[216:219],v[224:227]
ds_read_b64_tr_b16 a[96:97],v50 offset:1536
v_mov_b32 v28,0x3f803f80
v_mfma_f32_16x16x32_bf16 v[132:135],a[72:75],a[192:195],0
s_waitcnt lgkmcnt(4)
ds_read_b64_tr_b16 a[98:99],v50 offset:3584
v_mov_b32 v29,0x3f803f80
v_mfma_f32_16x16x32_bf16 v[132:135],a[76:79],a[196:199],v[132:135]
s_add_u32 m0,s88,7168
s_mov_b32 s88,s69
ds_read_b64_tr_b16 a[100:101],v50 offset:17920
v_mfma_f32_16x16x32_bf16 v[164:167],a[76:79],a[204:207],0
ds_read_b64_tr_b16 a[102:103],v50 offset:19968
v_mov_b32 v30,0x3f803f80
v_mfma_f32_16x16x32_bf16 v[164:167],a[72:75],a[200:203],v[164:167]
buffer_load_dwordx4 v47,s[36:39],s83 offen lds
v_mfma_f32_16x16x32_bf16 v[196:199],a[72:75],a[208:211],0
s_add_u32 s83,s83,524288
ds_read_b64_tr_b16 a[104:105],v50 offset:1544
v_mov_b32 v31,0x3f803f80
v_mfma_f32_16x16x32_bf16 v[196:199],a[76:79],a[212:215],v[196:199]
ds_read_b64_tr_b16 a[106:107],v50 offset:3592
v_mfma_f32_16x16x32_bf16 v[228:231],a[76:79],a[220:223],0
s_add_u32 m0,s85,0
ds_read_b64_tr_b16 a[108:109],v50 offset:17928
v_mfma_f32_16x16x32_bf16 v[228:231],a[72:75],a[216:219],v[228:231]
v_mfma_f32_16x16x32_bf16 v[136:139],a[80:83],a[192:195],0
buffer_load_dwordx4 v32,s[28:31],s84 offen lds
v_mfma_f32_16x16x32_bf16 v[136:139],a[84:87],a[196:199],v[136:139]
ds_read_b64_tr_b16 a[110:111],v50 offset:19976
v_mfma_f32_16x16x32_bf16 v[168:171],a[84:87],a[204:207],0
ds_read_b64_tr_b16 a[112:113],v50 offset:1792
v_mfma_f32_16x16x32_bf16 v[168:171],a[80:83],a[200:203],v[168:171]
ds_read_b64_tr_b16 a[114:115],v50 offset:3840
v_mfma_f32_16x16x32_bf16 v[200:203],a[80:83],a[208:211],0
ds_read_b64_tr_b16 a[116:117],v50 offset:18176
v_mfma_f32_16x16x32_bf16 v[200:203],a[84:87],a[212:215],v[200:203]
buffer_load_dwordx4 v33,s[28:31],s84 offen offset:1024 lds
v_mfma_f32_16x16x32_bf16 v[232:235],a[84:87],a[220:223],0
ds_read_b64_tr_b16 a[118:119],v50 offset:20224
v_mfma_f32_16x16x32_bf16 v[232:235],a[80:83],a[216:219],v[232:235]
s_waitcnt lgkmcnt(5)
ds_read_b64_tr_b16 a[120:121],v50 offset:1800
v_mfma_f32_16x16x32_bf16 v[140:143],a[88:91],a[192:195],0
ds_read_b64_tr_b16 a[122:123],v50 offset:3848
v_mfma_f32_16x16x32_bf16 v[140:143],a[92:95],a[196:199],v[140:143]
v_mfma_f32_16x16x32_bf16 v[172:175],a[92:95],a[204:207],0
buffer_load_dwordx4 v34,s[28:31],s84 offen offset:2048 lds
v_mfma_f32_16x16x32_bf16 v[172:175],a[88:91],a[200:203],v[172:175]
ds_read_b64_tr_b16 a[124:125],v50 offset:18184
v_mfma_f32_16x16x32_bf16 v[204:207],a[88:91],a[208:211],0
ds_read_b64_tr_b16 a[126:127],v50 offset:20232
v_add_u32 v50,s86,v51
v_mfma_f32_16x16x32_bf16 v[204:207],a[92:95],a[212:215],v[204:207]
v_mfma_f32_16x16x32_bf16 v[236:239],a[92:95],a[220:223],0
v_mfma_f32_16x16x32_bf16 v[236:239],a[88:91],a[216:219],v[236:239]
buffer_load_dwordx4 v35,s[28:31],s84 offen offset:3072 lds
s_waitcnt vmcnt(20) lgkmcnt(0)
s_barrier
v_mfma_f32_16x16x32_bf16 v[144:147],a[96:99],a[192:195],0
s_waitcnt vmcnt(12)
ds_read_b128 a[224:227],v48
v_mfma_f32_16x16x32_bf16 v[144:147],a[100:103],a[196:199],v[144:147]
s_add_u32 m0,s85,4096
s_xor_b32 s85,s85,98304
ds_read_b128 a[228:231],v49
v_mfma_f32_16x16x32_bf16 v[176:179],a[100:103],a[204:207],0
ds_read_b128 a[232:235],v48 offset:2048
v_mfma_f32_16x16x32_bf16 v[176:179],a[96:99],a[200:203],v[176:179]
buffer_load_dwordx4 v36,s[28:31],s84 offen lds
v_mfma_f32_16x16x32_bf16 v[208:211],a[96:99],a[208:211],0
ds_read_b128 a[236:239],v49 offset:2048
v_mfma_f32_16x16x32_bf16 v[208:211],a[100:103],a[212:215],v[208:211]
ds_read_b128 a[240:243],v48 offset:4096
v_mfma_f32_16x16x32_bf16 v[240:243],a[100:103],a[220:223],0
ds_read_b128 a[244:247],v49 offset:4096
v_mfma_f32_16x16x32_bf16 v[240:243],a[96:99],a[216:219],v[240:243]
ds_read_b128 a[248:251],v48 offset:6144
v_mfma_f32_16x16x32_bf16 v[148:151],a[104:107],a[192:195],0
buffer_load_dwordx4 v37,s[28:31],s84 offen offset:1024 lds
v_mfma_f32_16x16x32_bf16 v[148:151],a[108:111],a[196:199],v[148:151]
ds_read_b128 a[252:255],v49 offset:6144
v_mfma_f32_16x16x32_bf16 v[180:183],a[108:111],a[204:207],0
ds_read_b64_tr_b16 a[64:65],v50
v_mfma_f32_16x16x32_bf16 v[180:183],a[104:107],a[200:203],v[180:183]
ds_read_b64_tr_b16 a[66:67],v50 offset:2048
v_mfma_f32_16x16x32_bf16 v[212:215],a[104:107],a[208:211],0
v_mfma_f32_16x16x32_bf16 v[212:215],a[108:111],a[212:215],v[212:215]
buffer_load_dwordx4 v38,s[28:31],s84 offen offset:2048 lds
v_mfma_f32_16x16x32_bf16 v[244:247],a[108:111],a[220:223],0
ds_read_b64_tr_b16 a[68:69],v50 offset:16384
v_mfma_f32_16x16x32_bf16 v[244:247],a[104:107],a[216:219],v[244:247]
ds_read_b64_tr_b16 a[70:71],v50 offset:18432
v_mfma_f32_16x16x32_bf16 v[152:155],a[112:115],a[192:195],0
ds_read_b64_tr_b16 a[72:73],v50 offset:8
v_mfma_f32_16x16x32_bf16 v[152:155],a[116:119],a[196:199],v[152:155]
ds_read_b64_tr_b16 a[74:75],v50 offset:2056
v_mfma_f32_16x16x32_bf16 v[184:187],a[116:119],a[204:207],0
buffer_load_dwordx4 v39,s[28:31],s84 offen offset:3072 lds
v_mfma_f32_16x16x32_bf16 v[184:187],a[112:115],a[200:203],v[184:187]
s_add_u32 s84,s84,128
ds_read_b64_tr_b16 a[76:77],v50 offset:16392
v_mfma_f32_16x16x32_bf16 v[216:219],a[112:115],a[208:211],0
ds_read_b64_tr_b16 a[78:79],v50 offset:18440
v_mfma_f32_16x16x32_bf16 v[216:219],a[116:119],a[212:215],v[216:219]
ds_read_b64_tr_b16 a[80:81],v50 offset:256
v_mfma_f32_16x16x32_bf16 v[248:251],a[116:119],a[220:223],0
v_mfma_f32_16x16x32_bf16 v[248:251],a[112:115],a[216:219],v[248:251]
ds_read_b64_tr_b16 a[82:83],v50 offset:2304
v_mfma_f32_16x16x32_bf16 v[156:159],a[120:123],a[192:195],0
global_load_dword a184,v17,s[56:57] nt
ds_read_b64_tr_b16 a[84:85],v50 offset:16640
v_mfma_f32_16x16x32_bf16 v[156:159],a[124:127],a[196:199],v[156:159]
ds_read_b64_tr_b16 a[86:87],v50 offset:18688
v_mfma_f32_16x16x32_bf16 v[188:191],a[124:127],a[204:207],0
v_mfma_f32_16x16x32_bf16 v[188:191],a[120:123],a[200:203],v[188:191]
ds_read_b64_tr_b16 a[88:89],v50 offset:264
v_mfma_f32_16x16x32_bf16 v[220:223],a[120:123],a[208:211],0
ds_read_b64_tr_b16 a[90:91],v50 offset:2312
v_mfma_f32_16x16x32_bf16 v[220:223],a[124:127],a[212:215],v[220:223]
ds_read_b64_tr_b16 a[92:93],v50 offset:16648
v_mfma_f32_16x16x32_bf16 v[252:255],a[124:127],a[220:223],0
v_mfma_f32_16x16x32_bf16 v[252:255],a[120:123],a[216:219],v[252:255]
ds_read_b64_tr_b16 a[94:95],v50 offset:18696
s_mov_b32 s70,3
s_memrealtime s[8:9]
s_memtime s[4:5]
L_qp_%=:
s_waitcnt lgkmcnt(6)
v_mfma_f32_16x16x32_bf16 a[0:3],a[64:67],a[224:227],a[0:3]
s_add_u32 m0,s88,0
ds_read_b64_tr_b16 a[96:97],v50 offset:512
v_xor_b32 v48,98304,v48
s_nop 0
L_pd1_%=:
s_cbranch_execz L_pd5_%=
v_mfma_f32_16x16x32_bf16 a[0:3],a[68:71],a[228:231],a[0:3]
buffer_load_dwordx4 v40,s[36:39],s83 offen lds
v_mfma_f32_16x16x32_bf16 a[16:19],a[68:71],a[236:239],a[16:19]
s_mov_b32 s69,s86
ds_read_b64_tr_b16 a[98:99],v50 offset:2560
v_xor_b32 v49,98304,v49
v_mfma_f32_16x16x32_bf16 a[16:19],a[64:67],a[232:235],a[16:19]
s_mov_b32 s86,s87
s_mov_b32 s87,s88
ds_read_b64_tr_b16 a[100:101],v50 offset:16896
v_mfma_f32_16x16x32_bf16 a[32:35],a[64:67],a[240:243],a[32:35]
ds_read_b64_tr_b16 a[102:103],v50 offset:18944
v_mfma_f32_16x16x32_bf16 a[32:35],a[68:71],a[244:247],a[32:35]
ds_read_b64_tr_b16 a[104:105],v50 offset:520
v_mfma_f32_16x16x32_bf16 a[48:51],a[68:71],a[252:255],a[48:51]
s_add_u32 m0,s88,1024
v_mfma_f32_16x16x32_bf16 a[48:51],a[64:67],a[248:251],a[48:51]
ds_read_b64_tr_b16 a[106:107],v50 offset:2568
v_mfma_f32_16x16x32_bf16 a[4:7],a[72:75],a[224:227],a[4:7]
buffer_load_dwordx4 v41,s[36:39],s83 offen lds
v_mfma_f32_16x16x32_bf16 a[4:7],a[76:79],a[228:231],a[4:7]
ds_read_b64_tr_b16 a[108:109],v50 offset:16904
v_mfma_f32_16x16x32_bf16 a[20:23],a[76:79],a[236:239],a[20:23]
ds_read_b64_tr_b16 a[110:111],v50 offset:18952
v_mfma_f32_16x16x32_bf16 a[20:23],a[72:75],a[232:235],a[20:23]
ds_read_b64_tr_b16 a[112:113],v50 offset:768
v_mfma_f32_16x16x32_bf16 a[36:39],a[72:75],a[240:243],a[36:39]
ds_read_b64_tr_b16 a[114:115],v50 offset:2816
v_mfma_f32_16x16x32_bf16 a[36:39],a[76:79],a[244:247],a[36:39]
s_add_u32 m0,s88,2048
v_mfma_f32_16x16x32_bf16 a[52:55],a[76:79],a[252:255],a[52:55]
ds_read_b64_tr_b16 a[116:117],v50 offset:17152
v_mfma_f32_16x16x32_bf16 a[52:55],a[72:75],a[248:251],a[52:55]
buffer_load_dwordx4 v42,s[36:39],s83 offen lds
v_mfma_f32_16x16x32_bf16 a[8:11],a[80:83],a[224:227],a[8:11]
s_waitcnt lgkmcnt(5)
ds_read_b64_tr_b16 a[118:119],v50 offset:19200
v_mfma_f32_16x16x32_bf16 a[8:11],a[84:87],a[228:231],a[8:11]
ds_read_b64_tr_b16 a[120:121],v50 offset:776
)A"
R"A(v_mfma_f32_16x16x32_bf16 a[24:27],a[84:87],a[236:239],a[24:27]
ds_read_b64_tr_b16 a[122:123],v50 offset:2824
v_mfma_f32_16x16x32_bf16 a[24:27],a[80:83],a[232:235],a[24:27]
ds_read_b64_tr_b16 a[124:125],v50 offset:17160
v_mfma_f32_16x16x32_bf16 a[40:43],a[80:83],a[240:243],a[40:43]
s_add_u32 m0,s88,3072
v_mfma_f32_16x16x32_bf16 a[40:43],a[84:87],a[244:247],a[40:43]
ds_read_b64_tr_b16 a[126:127],v50 offset:19208
v_mfma_f32_16x16x32_bf16 a[56:59],a[84:87],a[252:255],a[56:59]
buffer_load_dwordx4 v43,s[36:39],s83 offen lds
v_mfma_f32_16x16x32_bf16 a[56:59],a[80:83],a[248:251],a[56:59]
ds_read_b64_tr_b16 a[64:65],v50 offset:1024
v_mfma_f32_16x16x32_bf16 a[12:15],a[88:91],a[224:227],a[12:15]
ds_read_b64_tr_b16 a[66:67],v50 offset:3072
v_mfma_f32_16x16x32_bf16 a[12:15],a[92:95],a[228:231],a[12:15]
ds_read_b64_tr_b16 a[68:69],v50 offset:17408
v_mfma_f32_16x16x32_bf16 a[28:31],a[92:95],a[236:239],a[28:31]
ds_read_b64_tr_b16 a[70:71],v50 offset:19456
v_mfma_f32_16x16x32_bf16 a[28:31],a[88:91],a[232:235],a[28:31]
s_add_u32 m0,s88,4096
v_mfma_f32_16x16x32_bf16 a[44:47],a[88:91],a[240:243],a[44:47]
ds_read_b64_tr_b16 a[72:73],v50 offset:1032
v_mfma_f32_16x16x32_bf16 a[44:47],a[92:95],a[244:247],a[44:47]
buffer_load_dwordx4 v44,s[36:39],s83 offen lds
v_mfma_f32_16x16x32_bf16 a[60:63],a[92:95],a[252:255],a[60:63]
ds_read_b64_tr_b16 a[74:75],v50 offset:3080
v_mfma_f32_16x16x32_bf16 a[60:63],a[88:91],a[248:251],a[60:63]
ds_read_b64_tr_b16 a[76:77],v50 offset:17416
v_mfma_f32_16x16x32_bf16 v[64:67],a[96:99],a[224:227],v[64:67]
ds_read_b64_tr_b16 a[78:79],v50 offset:19464
v_mfma_f32_16x16x32_bf16 v[64:67],a[100:103],a[228:231],v[64:67]
ds_read_b64_tr_b16 a[80:81],v50 offset:1280
v_mfma_f32_16x16x32_bf16 v[80:83],a[100:103],a[236:239],v[80:83]
s_add_u32 m0,s88,5120
v_mfma_f32_16x16x32_bf16 v[80:83],a[96:99],a[232:235],v[80:83]
ds_read_b64_tr_b16 a[82:83],v50 offset:3328
v_mfma_f32_16x16x32_bf16 v[96:99],a[96:99],a[240:243],v[96:99]
buffer_load_dwordx4 v45,s[36:39],s83 offen lds
v_mfma_f32_16x16x32_bf16 v[96:99],a[100:103],a[244:247],v[96:99]
ds_read_b64_tr_b16 a[84:85],v50 offset:17664
v_mfma_f32_16x16x32_bf16 v[112:115],a[100:103],a[252:255],v[112:115]
ds_read_b64_tr_b16 a[86:87],v50 offset:19712
v_mfma_f32_16x16x32_bf16 v[112:115],a[96:99],a[248:251],v[112:115]
ds_read_b64_tr_b16 a[88:89],v50 offset:1288
v_mfma_f32_16x16x32_bf16 v[68:71],a[104:107],a[224:227],v[68:71]
s_waitcnt lgkmcnt(5)
ds_read_b64_tr_b16 a[90:91],v50 offset:3336
v_mfma_f32_16x16x32_bf16 v[68:71],a[108:111],a[228:231],v[68:71]
s_add_u32 m0,s88,6144
v_mfma_f32_16x16x32_bf16 v[84:87],a[108:111],a[236:239],v[84:87]
ds_read_b64_tr_b16 a[92:93],v50 offset:17672
v_mfma_f32_16x16x32_bf16 v[84:87],a[104:107],a[232:235],v[84:87]
buffer_load_dwordx4 v46,s[36:39],s83 offen lds
v_mfma_f32_16x16x32_bf16 v[100:103],a[104:107],a[240:243],v[100:103]
ds_read_b64_tr_b16 a[94:95],v50 offset:19720
v_mfma_f32_16x16x32_bf16 v[100:103],a[108:111],a[244:247],v[100:103]
ds_read_b64_tr_b16 a[96:97],v50 offset:1536
v_mfma_f32_16x16x32_bf16 v[116:119],a[108:111],a[252:255],v[116:119]
ds_read_b64_tr_b16 a[98:99],v50 offset:3584
v_mfma_f32_16x16x32_bf16 v[116:119],a[104:107],a[248:251],v[116:119]
ds_read_b64_tr_b16 a[100:101],v50 offset:17920
v_mfma_f32_16x16x32_bf16 v[72:75],a[112:115],a[224:227],v[72:75]
s_add_u32 m0,s88,7168
s_mov_b32 s88,s69
s_mov_b32 s69,s86
v_mfma_f32_16x16x32_bf16 v[72:75],a[116:119],a[228:231],v[72:75]
ds_read_b64_tr_b16 a[102:103],v50 offset:19968
v_mfma_f32_16x16x32_bf16 v[88:91],a[116:119],a[236:239],v[88:91]
buffer_load_dwordx4 v47,s[36:39],s83 offen lds
v_mfma_f32_16x16x32_bf16 v[88:91],a[112:115],a[232:235],v[88:91]
s_add_u32 s83,s83,524288
ds_read_b64_tr_b16 a[104:105],v50 offset:1544
v_mfma_f32_16x16x32_bf16 v[104:107],a[112:115],a[240:243],v[104:107]
ds_read_b64_tr_b16 a[106:107],v50 offset:3592
v_mfma_f32_16x16x32_bf16 v[104:107],a[116:119],a[244:247],v[104:107]
ds_read_b64_tr_b16 a[108:109],v50 offset:17928
v_mfma_f32_16x16x32_bf16 v[120:123],a[116:119],a[252:255],v[120:123]
ds_read_b64_tr_b16 a[110:111],v50 offset:19976
v_mfma_f32_16x16x32_bf16 v[120:123],a[112:115],a[248:251],v[120:123]
s_add_u32 m0,s85,0
v_mfma_f32_16x16x32_bf16 v[76:79],a[120:123],a[224:227],v[76:79]
ds_read_b64_tr_b16 a[112:113],v50 offset:1792
v_mfma_f32_16x16x32_bf16 v[76:79],a[124:127],a[228:231],v[76:79]
buffer_load_dwordx4 v32,s[28:31],s84 offen lds
v_mfma_f32_16x16x32_bf16 v[92:95],a[124:127],a[236:239],v[92:95]
ds_read_b64_tr_b16 a[114:115],v50 offset:3840
v_mfma_f32_16x16x32_bf16 v[92:95],a[120:123],a[232:235],v[92:95]
ds_read_b64_tr_b16 a[116:117],v50 offset:18176
v_mfma_f32_16x16x32_bf16 v[108:111],a[120:123],a[240:243],v[108:111]
ds_read_b64_tr_b16 a[118:119],v50 offset:20224
v_mfma_f32_16x16x32_bf16 v[108:111],a[124:127],a[244:247],v[108:111]
s_cmp_eq_u32 s70,1
s_cselect_b32 s65,256,0
s_cselect_b32 s66,524288,0
v_mfma_f32_16x16x32_bf16 v[124:127],a[124:127],a[252:255],v[124:127]
ds_read_b64_tr_b16 a[124:125],v50 offset:18184
v_add_u32 v40,s65,v40
v_mfma_f32_16x16x32_bf16 v[124:127],a[120:123],a[248:251],v[124:127]
ds_read_b64_tr_b16 a[120:121],v50 offset:1800
v_add_u32 v41,s66,v41
v_mfma_f32_16x16x32_bf16 v[128:131],a[64:67],a[224:227],v[128:131]
buffer_load_dwordx4 v33,s[28:31],s84 offen offset:1024 lds
v_mfma_f32_16x16x32_bf16 v[128:131],a[68:71],a[228:231],v[128:131]
ds_read_b64_tr_b16 a[122:123],v50 offset:3848
v_add_u32 v42,s65,v42
v_mfma_f32_16x16x32_bf16 v[160:163],a[68:71],a[236:239],v[160:163]
ds_read_b64_tr_b16 a[126:127],v50 offset:20232
v_add_u32 v50,s86,v51
v_mfma_f32_16x16x32_bf16 v[160:163],a[64:67],a[232:235],v[160:163]
s_mov_b32 s86,s87
v_add_u32 v43,s66,v43
v_add_u32 v44,s65,v44
v_mfma_f32_16x16x32_bf16 v[192:195],a[64:67],a[240:243],v[192:195]
s_mov_b32 s87,s88
v_add_u32 v45,s66,v45
v_add_u32 v46,s65,v46
v_mfma_f32_16x16x32_bf16 v[192:195],a[68:71],a[244:247],v[192:195]
v_add_u32 v47,s66,v47
v_mfma_f32_16x16x32_bf16 v[224:227],a[68:71],a[252:255],v[224:227]
v_mfma_f32_16x16x32_bf16 v[224:227],a[64:67],a[248:251],v[224:227]
buffer_load_dwordx4 v34,s[28:31],s84 offen offset:2048 lds
v_mfma_f32_16x16x32_bf16 v[132:135],a[72:75],a[224:227],v[132:135]
v_mfma_f32_16x16x32_bf16 v[132:135],a[76:79],a[228:231],v[132:135]
v_mfma_f32_16x16x32_bf16 v[164:167],a[76:79],a[236:239],v[164:167]
v_mfma_f32_16x16x32_bf16 v[164:167],a[72:75],a[232:235],v[164:167]
v_mfma_f32_16x16x32_bf16 v[196:199],a[72:75],a[240:243],v[196:199]
v_mfma_f32_16x16x32_bf16 v[196:199],a[76:79],a[244:247],v[196:199]
v_mfma_f32_16x16x32_bf16 v[228:231],a[76:79],a[252:255],v[228:231]
buffer_load_dwordx4 v35,s[28:31],s84 offen offset:3072 lds
v_mfma_f32_16x16x32_bf16 v[228:231],a[72:75],a[248:251],v[228:231]
s_waitcnt lgkmcnt(0)
v_mfma_f32_16x16x32_bf16 v[136:139],a[80:83],a[224:227],v[136:139]
v_mfma_f32_16x16x32_bf16 v[136:139],a[84:87],a[228:231],v[136:139]
v_mfma_f32_16x16x32_bf16 v[168:171],a[84:87],a[236:239],v[168:171]
v_mfma_f32_16x16x32_bf16 v[168:171],a[80:83],a[232:235],v[168:171]
s_add_u32 m0,s85,4096
s_xor_b32 s85,s85,98304
v_mfma_f32_16x16x32_bf16 v[200:203],a[80:83],a[240:243],v[200:203]
v_mfma_f32_16x16x32_bf16 v[200:203],a[84:87],a[244:247],v[200:203]
buffer_load_dwordx4 v36,s[28:31],s84 offen lds
v_mfma_f32_16x16x32_bf16 v[232:235],a[84:87],a[252:255],v[232:235]
v_mfma_f32_16x16x32_bf16 v[232:235],a[80:83],a[248:251],v[232:235]
v_mfma_f32_16x16x32_bf16 v[140:143],a[88:91],a[224:227],v[140:143]
v_mfma_f32_16x16x32_bf16 v[140:143],a[92:95],a[228:231],v[140:143]
v_mfma_f32_16x16x32_bf16 v[172:175],a[92:95],a[236:239],v[172:175]
v_mfma_f32_16x16x32_bf16 v[172:175],a[88:91],a[232:235],v[172:175]
v_mfma_f32_16x16x32_bf16 v[204:207],a[88:91],a[240:243],v[204:207]
buffer_load_dwordx4 v37,s[28:31],s84 offen offset:1024 lds
v_mfma_f32_16x16x32_bf16 v[204:207],a[92:95],a[244:247],v[204:207]
v_mfma_f32_16x16x32_bf16 v[236:239],a[92:95],a[252:255],v[236:239]
v_mfma_f32_16x16x32_bf16 v[236:239],a[88:91],a[248:251],v[236:239]
s_waitcnt vmcnt(23)
s_barrier
v_mfma_f32_16x16x32_bf16 v[144:147],a[96:99],a[224:227],v[144:147]
s_waitcnt vmcnt(15)
ds_read_b128 a[192:195],v48
v_mfma_f32_16x16x32_bf16 v[144:147],a[100:103],a[228:231],v[144:147]
ds_read_b128 a[196:199],v49
v_mfma_f32_16x16x32_bf16 v[176:179],a[100:103],a[236:239],v[176:179]
buffer_load_dwordx4 v38,s[28:31],s84 offen offset:2048 lds
v_mfma_f32_16x16x32_bf16 v[176:179],a[96:99],a[232:235],v[176:179]
ds_read_b128 a[200:203],v48 offset:2048
v_mfma_f32_16x16x32_bf16 v[208:211],a[96:99],a[240:243],v[208:211]
ds_read_b128 a[204:207],v49 offset:2048
v_mfma_f32_16x16x32_bf16 v[208:211],a[100:103],a[244:247],v[208:211]
ds_read_b128 a[208:211],v48 offset:4096
v_mfma_f32_16x16x32_bf16 v[240:243],a[100:103],a[252:255],v[240:243]
ds_read_b128 a[212:215],v49 offset:4096
v_mfma_f32_16x16x32_bf16 v[240:243],a[96:99],a[248:251],v[240:243]
v_mfma_f32_16x16x32_bf16 v[148:151],a[104:107],a[224:227],v[148:151]
ds_read_b128 a[216:219],v48 offset:6144
v_xor_b32 v48,98304,v48
v_mfma_f32_16x16x32_bf16 v[148:151],a[108:111],a[228:231],v[148:151]
buffer_load_dwordx4 v39,s[28:31],s84 offen offset:3072 lds
v_mfma_f32_16x16x32_bf16 v[180:183],a[108:111],a[236:239],v[180:183]
s_add_u32 s84,s84,128
ds_read_b128 a[220:223],v49 offset:6144
v_xor_b32 v49,98304,v49
v_mfma_f32_16x16x32_bf16 v[180:183],a[104:107],a[232:235],v[180:183]
ds_read_b64_tr_b16 a[64:65],v50
v_mfma_f32_16x16x32_bf16 v[212:215],a[104:107],a[240:243],v[212:215]
ds_read_b64_tr_b16 a[66:67],v50 offset:2048
v_mfma_f32_16x16x32_bf16 v[212:215],a[108:111],a[244:247],v[212:215]
ds_read_b64_tr_b16 a[68:69],v50 offset:16384
v_mfma_f32_16x16x32_bf16 v[244:247],a[108:111],a[252:255],v[244:247]
v_mfma_f32_16x16x32_bf16 v[244:247],a[104:107],a[248:251],v[244:247]
ds_read_b64_tr_b16 a[70:71],v50 offset:18432
v_mfma_f32_16x16x32_bf16 v[152:155],a[112:115],a[224:227],v[152:155]
buffer_load_dword v9,v19,s[48:51],s90 offen
v_mfma_f32_16x16x32_bf16 v[152:155],a[116:119],a[228:231],v[152:155]
s_add_u32 s90,s90,896
ds_read_b64_tr_b16 a[72:73],v50 offset:8
v_mfma_f32_16x16x32_bf16 v[184:187],a[116:119],a[236:239],v[184:187]
ds_read_b64_tr_b16 a[74:75],v50 offset:2056
v_mfma_f32_16x16x32_bf16 v[184:187],a[112:115],a[232:235],v[184:187]
ds_read_b64_tr_b16 a[76:77],v50 offset:16392
v_mfma_f32_16x16x32_bf16 v[216:219],a[112:115],a[240:243],v[216:219]
ds_read_b64_tr_b16 a[78:79],v50 offset:18440
v_mfma_f32_16x16x32_bf16 v[216:219],a[116:119],a[244:247],v[216:219]
v_mfma_f32_16x16x32_bf16 v[248:251],a[116:119],a[252:255],v[248:251]
ds_read_b64_tr_b16 a[80:81],v50 offset:256
v_mfma_f32_16x16x32_bf16 v[248:251],a[112:115],a[248:251],v[248:251]
ds_read_b64_tr_b16 a[82:83],v50 offset:2304
v_mfma_f32_16x16x32_bf16 v[156:159],a[120:123],a[224:227],v[156:159]
ds_read_b64_tr_b16 a[84:85],v50 offset:16640
v_mfma_f32_16x16x32_bf16 v[156:159],a[124:127],a[228:231],v[156:159]
v_mfma_f32_16x16x32_bf16 v[188:191],a[124:127],a[236:239],v[188:191]
ds_read_b64_tr_b16 a[86:87],v50 offset:18688
v_mfma_f32_16x16x32_bf16 v[188:191],a[120:123],a[232:235],v[188:191]
ds_read_b64_tr_b16 a[88:89],v50 offset:264
v_mfma_f32_16x16x32_bf16 v[220:223],a[120:123],a[240:243],v[220:223]
ds_read_b64_tr_b16 a[90:91],v50 offset:2312
v_mfma_f32_16x16x32_bf16 v[220:223],a[124:127],a[244:247],v[220:223]
v_mfma_f32_16x16x32_bf16 v[252:255],a[124:127],a[252:255],v[252:255]
s_waitcnt lgkmcnt(4)
ds_read_b64_tr_b16 a[92:93],v50 offset:16648
v_mfma_f32_16x16x32_bf16 v[252:255],a[120:123],a[248:251],v[252:255]
s_add_u32 m0,s88,0
ds_read_b64_tr_b16 a[94:95],v50 offset:18696
v_mfma_f32_16x16x32_bf16 a[0:3],a[64:67],a[192:195],a[0:3]
ds_read_b64_tr_b16 a[96:97],v50 offset:512
v_mfma_f32_16x16x32_bf16 a[0:3],a[68:71],a[196:199],a[0:3]
buffer_load_dwordx4 v40,s[36:39],s83 offen lds
v_mfma_f32_16x16x32_bf16 a[16:19],a[68:71],a[204:207],a[16:19]
ds_read_b64_tr_b16 a[98:99],v50 offset:2560
v_mfma_f32_16x16x32_bf16 a[16:19],a[64:67],a[200:203],a[16:19]
ds_read_b64_tr_b16 a[100:101],v50 offset:16896
v_mfma_f32_16x16x32_bf16 a[32:35],a[64:67],a[208:211],a[32:35]
ds_read_b64_tr_b16 a[102:103],v50 offset:18944
v_mfma_f32_16x16x32_bf16 a[32:35],a[68:71],a[212:215],a[32:35]
)A"
R"A(v_mfma_f32_16x16x32_bf16 a[48:51],a[68:71],a[220:223],a[48:51]
s_add_u32 m0,s88,1024
ds_read_b64_tr_b16 a[104:105],v50 offset:520
v_mfma_f32_16x16x32_bf16 a[48:51],a[64:67],a[216:219],a[48:51]
ds_read_b64_tr_b16 a[106:107],v50 offset:2568
v_mfma_f32_16x16x32_bf16 a[4:7],a[72:75],a[192:195],a[4:7]
buffer_load_dwordx4 v41,s[36:39],s83 offen lds
v_mfma_f32_16x16x32_bf16 a[4:7],a[76:79],a[196:199],a[4:7]
ds_read_b64_tr_b16 a[108:109],v50 offset:16904
v_mfma_f32_16x16x32_bf16 a[20:23],a[76:79],a[204:207],a[20:23]
ds_read_b64_tr_b16 a[110:111],v50 offset:18952
v_mfma_f32_16x16x32_bf16 a[20:23],a[72:75],a[200:203],a[20:23]
ds_read_b64_tr_b16 a[112:113],v50 offset:768
v_mfma_f32_16x16x32_bf16 a[36:39],a[72:75],a[208:211],a[36:39]
ds_read_b64_tr_b16 a[114:115],v50 offset:2816
v_mfma_f32_16x16x32_bf16 a[36:39],a[76:79],a[212:215],a[36:39]
s_add_u32 m0,s88,2048
v_mfma_f32_16x16x32_bf16 a[52:55],a[76:79],a[220:223],a[52:55]
ds_read_b64_tr_b16 a[116:117],v50 offset:17152
v_mfma_f32_16x16x32_bf16 a[52:55],a[72:75],a[216:219],a[52:55]
buffer_load_dwordx4 v42,s[36:39],s83 offen lds
v_mfma_f32_16x16x32_bf16 a[8:11],a[80:83],a[192:195],a[8:11]
s_waitcnt lgkmcnt(5)
ds_read_b64_tr_b16 a[118:119],v50 offset:19200
v_mfma_f32_16x16x32_bf16 a[8:11],a[84:87],a[196:199],a[8:11]
ds_read_b64_tr_b16 a[120:121],v50 offset:776
v_mfma_f32_16x16x32_bf16 a[24:27],a[84:87],a[204:207],a[24:27]
ds_read_b64_tr_b16 a[122:123],v50 offset:2824
v_mfma_f32_16x16x32_bf16 a[24:27],a[80:83],a[200:203],a[24:27]
ds_read_b64_tr_b16 a[124:125],v50 offset:17160
v_mfma_f32_16x16x32_bf16 a[40:43],a[80:83],a[208:211],a[40:43]
s_add_u32 m0,s88,3072
v_mfma_f32_16x16x32_bf16 a[40:43],a[84:87],a[212:215],a[40:43]
ds_read_b64_tr_b16 a[126:127],v50 offset:19208
v_mfma_f32_16x16x32_bf16 a[56:59],a[84:87],a[220:223],a[56:59]
buffer_load_dwordx4 v43,s[36:39],s83 offen lds
v_mfma_f32_16x16x32_bf16 a[56:59],a[80:83],a[216:219],a[56:59]
ds_read_b64_tr_b16 a[64:65],v50 offset:1024
v_mfma_f32_16x16x32_bf16 a[12:15],a[88:91],a[192:195],a[12:15]
ds_read_b64_tr_b16 a[66:67],v50 offset:3072
v_mfma_f32_16x16x32_bf16 a[12:15],a[92:95],a[196:199],a[12:15]
ds_read_b64_tr_b16 a[68:69],v50 offset:17408
v_mfma_f32_16x16x32_bf16 a[28:31],a[92:95],a[204:207],a[28:31]
ds_read_b64_tr_b16 a[70:71],v50 offset:19456
v_mfma_f32_16x16x32_bf16 a[28:31],a[88:91],a[200:203],a[28:31]
s_add_u32 m0,s88,4096
v_mfma_f32_16x16x32_bf16 a[44:47],a[88:91],a[208:211],a[44:47]
ds_read_b64_tr_b16 a[72:73],v50 offset:1032
v_mfma_f32_16x16x32_bf16 a[44:47],a[92:95],a[212:215],a[44:47]
buffer_load_dwordx4 v44,s[36:39],s83 offen lds
v_mfma_f32_16x16x32_bf16 a[60:63],a[92:95],a[220:223],a[60:63]
ds_read_b64_tr_b16 a[74:75],v50 offset:3080
v_mfma_f32_16x16x32_bf16 a[60:63],a[88:91],a[216:219],a[60:63]
ds_read_b64_tr_b16 a[76:77],v50 offset:17416
v_mfma_f32_16x16x32_bf16 v[64:67],a[96:99],a[192:195],v[64:67]
ds_read_b64_tr_b16 a[78:79],v50 offset:19464
v_mfma_f32_16x16x32_bf16 v[64:67],a[100:103],a[196:199],v[64:67]
ds_read_b64_tr_b16 a[80:81],v50 offset:1280
v_mfma_f32_16x16x32_bf16 v[80:83],a[100:103],a[204:207],v[80:83]
s_add_u32 m0,s88,5120
v_mfma_f32_16x16x32_bf16 v[80:83],a[96:99],a[200:203],v[80:83]
ds_read_b64_tr_b16 a[82:83],v50 offset:3328
v_mfma_f32_16x16x32_bf16 v[96:99],a[96:99],a[208:211],v[96:99]
buffer_load_dwordx4 v45,s[36:39],s83 offen lds
v_mfma_f32_16x16x32_bf16 v[96:99],a[100:103],a[212:215],v[96:99]
ds_read_b64_tr_b16 a[84:85],v50 offset:17664
v_mfma_f32_16x16x32_bf16 v[112:115],a[100:103],a[220:223],v[112:115]
ds_read_b64_tr_b16 a[86:87],v50 offset:19712
v_mfma_f32_16x16x32_bf16 v[112:115],a[96:99],a[216:219],v[112:115]
ds_read_b64_tr_b16 a[88:89],v50 offset:1288
v_mfma_f32_16x16x32_bf16 v[68:71],a[104:107],a[192:195],v[68:71]
s_waitcnt lgkmcnt(5)
ds_read_b64_tr_b16 a[90:91],v50 offset:3336
v_mfma_f32_16x16x32_bf16 v[68:71],a[108:111],a[196:199],v[68:71]
s_add_u32 m0,s88,6144
v_mfma_f32_16x16x32_bf16 v[84:87],a[108:111],a[204:207],v[84:87]
ds_read_b64_tr_b16 a[92:93],v50 offset:17672
v_mfma_f32_16x16x32_bf16 v[84:87],a[104:107],a[200:203],v[84:87]
buffer_load_dwordx4 v46,s[36:39],s83 offen lds
v_mfma_f32_16x16x32_bf16 v[100:103],a[104:107],a[208:211],v[100:103]
ds_read_b64_tr_b16 a[94:95],v50 offset:19720
v_mfma_f32_16x16x32_bf16 v[100:103],a[108:111],a[212:215],v[100:103]
ds_read_b64_tr_b16 a[96:97],v50 offset:1536
v_mfma_f32_16x16x32_bf16 v[116:119],a[108:111],a[220:223],v[116:119]
ds_read_b64_tr_b16 a[98:99],v50 offset:3584
v_mfma_f32_16x16x32_bf16 v[116:119],a[104:107],a[216:219],v[116:119]
ds_read_b64_tr_b16 a[100:101],v50 offset:17920
v_mfma_f32_16x16x32_bf16 v[72:75],a[112:115],a[192:195],v[72:75]
s_add_u32 m0,s88,7168
s_mov_b32 s88,s69
v_mfma_f32_16x16x32_bf16 v[72:75],a[116:119],a[196:199],v[72:75]
ds_read_b64_tr_b16 a[102:103],v50 offset:19968
v_mfma_f32_16x16x32_bf16 v[88:91],a[116:119],a[204:207],v[88:91]
buffer_load_dwordx4 v47,s[36:39],s83 offen lds
v_mfma_f32_16x16x32_bf16 v[88:91],a[112:115],a[200:203],v[88:91]
s_add_u32 s83,s83,524288
ds_read_b64_tr_b16 a[104:105],v50 offset:1544
v_mfma_f32_16x16x32_bf16 v[104:107],a[112:115],a[208:211],v[104:107]
ds_read_b64_tr_b16 a[106:107],v50 offset:3592
v_mfma_f32_16x16x32_bf16 v[104:107],a[116:119],a[212:215],v[104:107]
ds_read_b64_tr_b16 a[108:109],v50 offset:17928
v_mfma_f32_16x16x32_bf16 v[120:123],a[116:119],a[220:223],v[120:123]
ds_read_b64_tr_b16 a[110:111],v50 offset:19976
v_mfma_f32_16x16x32_bf16 v[120:123],a[112:115],a[216:219],v[120:123]
s_add_u32 m0,s85,0
v_mfma_f32_16x16x32_bf16 v[76:79],a[120:123],a[192:195],v[76:79]
ds_read_b64_tr_b16 a[112:113],v50 offset:1792
v_mfma_f32_16x16x32_bf16 v[76:79],a[124:127],a[196:199],v[76:79]
buffer_load_dwordx4 v32,s[28:31],s84 offen lds
v_mfma_f32_16x16x32_bf16 v[92:95],a[124:127],a[204:207],v[92:95]
ds_read_b64_tr_b16 a[114:115],v50 offset:3840
v_mfma_f32_16x16x32_bf16 v[92:95],a[120:123],a[200:203],v[92:95]
ds_read_b64_tr_b16 a[116:117],v50 offset:18176
v_mfma_f32_16x16x32_bf16 v[108:111],a[120:123],a[208:211],v[108:111]
ds_read_b64_tr_b16 a[118:119],v50 offset:20224
v_mfma_f32_16x16x32_bf16 v[108:111],a[124:127],a[212:215],v[108:111]
v_mfma_f32_16x16x32_bf16 v[124:127],a[124:127],a[220:223],v[124:127]
ds_read_b64_tr_b16 a[124:125],v50 offset:18184
v_mfma_f32_16x16x32_bf16 v[124:127],a[120:123],a[216:219],v[124:127]
ds_read_b64_tr_b16 a[120:121],v50 offset:1800
v_mfma_f32_16x16x32_bf16 v[128:131],a[64:67],a[192:195],v[128:131]
buffer_load_dwordx4 v33,s[28:31],s84 offen offset:1024 lds
v_mfma_f32_16x16x32_bf16 v[128:131],a[68:71],a[196:199],v[128:131]
ds_read_b64_tr_b16 a[122:123],v50 offset:3848
v_mfma_f32_16x16x32_bf16 v[160:163],a[68:71],a[204:207],v[160:163]
ds_read_b64_tr_b16 a[126:127],v50 offset:20232
v_add_u32 v50,s86,v51
v_mfma_f32_16x16x32_bf16 v[160:163],a[64:67],a[200:203],v[160:163]
v_mfma_f32_16x16x32_bf16 v[192:195],a[64:67],a[208:211],v[192:195]
v_mfma_f32_16x16x32_bf16 v[192:195],a[68:71],a[212:215],v[192:195]
v_mfma_f32_16x16x32_bf16 v[224:227],a[68:71],a[220:223],v[224:227]
v_mfma_f32_16x16x32_bf16 v[224:227],a[64:67],a[216:219],v[224:227]
buffer_load_dwordx4 v34,s[28:31],s84 offen offset:2048 lds
v_mfma_f32_16x16x32_bf16 v[132:135],a[72:75],a[192:195],v[132:135]
v_mfma_f32_16x16x32_bf16 v[132:135],a[76:79],a[196:199],v[132:135]
v_mfma_f32_16x16x32_bf16 v[164:167],a[76:79],a[204:207],v[164:167]
v_mfma_f32_16x16x32_bf16 v[164:167],a[72:75],a[200:203],v[164:167]
v_mfma_f32_16x16x32_bf16 v[196:199],a[72:75],a[208:211],v[196:199]
v_mfma_f32_16x16x32_bf16 v[196:199],a[76:79],a[212:215],v[196:199]
v_mfma_f32_16x16x32_bf16 v[228:231],a[76:79],a[220:223],v[228:231]
buffer_load_dwordx4 v35,s[28:31],s84 offen offset:3072 lds
v_mfma_f32_16x16x32_bf16 v[228:231],a[72:75],a[216:219],v[228:231]
s_waitcnt lgkmcnt(0)
v_mfma_f32_16x16x32_bf16 v[136:139],a[80:83],a[192:195],v[136:139]
v_mfma_f32_16x16x32_bf16 v[136:139],a[84:87],a[196:199],v[136:139]
v_mfma_f32_16x16x32_bf16 v[168:171],a[84:87],a[204:207],v[168:171]
v_mfma_f32_16x16x32_bf16 v[168:171],a[80:83],a[200:203],v[168:171]
s_add_u32 m0,s85,4096
s_xor_b32 s85,s85,98304
v_mfma_f32_16x16x32_bf16 v[200:203],a[80:83],a[208:211],v[200:203]
v_mfma_f32_16x16x32_bf16 v[200:203],a[84:87],a[212:215],v[200:203]
buffer_load_dwordx4 v36,s[28:31],s84 offen lds
v_mfma_f32_16x16x32_bf16 v[232:235],a[84:87],a[220:223],v[232:235]
v_mfma_f32_16x16x32_bf16 v[232:235],a[80:83],a[216:219],v[232:235]
v_mfma_f32_16x16x32_bf16 v[140:143],a[88:91],a[192:195],v[140:143]
v_mfma_f32_16x16x32_bf16 v[140:143],a[92:95],a[196:199],v[140:143]
v_mfma_f32_16x16x32_bf16 v[172:175],a[92:95],a[204:207],v[172:175]
v_mfma_f32_16x16x32_bf16 v[172:175],a[88:91],a[200:203],v[172:175]
v_mfma_f32_16x16x32_bf16 v[204:207],a[88:91],a[208:211],v[204:207]
buffer_load_dwordx4 v37,s[28:31],s84 offen offset:1024 lds
v_mfma_f32_16x16x32_bf16 v[204:207],a[92:95],a[212:215],v[204:207]
v_mfma_f32_16x16x32_bf16 v[236:239],a[92:95],a[220:223],v[236:239]
v_mfma_f32_16x16x32_bf16 v[236:239],a[88:91],a[216:219],v[236:239]
s_waitcnt vmcnt(23)
s_barrier
v_mfma_f32_16x16x32_bf16 v[144:147],a[96:99],a[192:195],v[144:147]
s_waitcnt vmcnt(15)
ds_read_b128 a[224:227],v48
v_mfma_f32_16x16x32_bf16 v[144:147],a[100:103],a[196:199],v[144:147]
ds_read_b128 a[228:231],v49
v_mfma_f32_16x16x32_bf16 v[176:179],a[100:103],a[204:207],v[176:179]
buffer_load_dwordx4 v38,s[28:31],s84 offen offset:2048 lds
v_mfma_f32_16x16x32_bf16 v[176:179],a[96:99],a[200:203],v[176:179]
ds_read_b128 a[232:235],v48 offset:2048
v_mfma_f32_16x16x32_bf16 v[208:211],a[96:99],a[208:211],v[208:211]
ds_read_b128 a[236:239],v49 offset:2048
v_mfma_f32_16x16x32_bf16 v[208:211],a[100:103],a[212:215],v[208:211]
ds_read_b128 a[240:243],v48 offset:4096
v_mfma_f32_16x16x32_bf16 v[240:243],a[100:103],a[220:223],v[240:243]
ds_read_b128 a[244:247],v49 offset:4096
v_mfma_f32_16x16x32_bf16 v[240:243],a[96:99],a[216:219],v[240:243]
v_mfma_f32_16x16x32_bf16 v[148:151],a[104:107],a[192:195],v[148:151]
ds_read_b128 a[248:251],v48 offset:6144
v_mfma_f32_16x16x32_bf16 v[148:151],a[108:111],a[196:199],v[148:151]
buffer_load_dwordx4 v39,s[28:31],s84 offen offset:3072 lds
v_mfma_f32_16x16x32_bf16 v[180:183],a[108:111],a[204:207],v[180:183]
s_add_u32 s84,s84,128
ds_read_b128 a[252:255],v49 offset:6144
v_mfma_f32_16x16x32_bf16 v[180:183],a[104:107],a[200:203],v[180:183]
ds_read_b64_tr_b16 a[64:65],v50
v_mfma_f32_16x16x32_bf16 v[212:215],a[104:107],a[208:211],v[212:215]
ds_read_b64_tr_b16 a[66:67],v50 offset:2048
v_mfma_f32_16x16x32_bf16 v[212:215],a[108:111],a[212:215],v[212:215]
ds_read_b64_tr_b16 a[68:69],v50 offset:16384
v_mfma_f32_16x16x32_bf16 v[244:247],a[108:111],a[220:223],v[244:247]
v_mfma_f32_16x16x32_bf16 v[244:247],a[104:107],a[216:219],v[244:247]
ds_read_b64_tr_b16 a[70:71],v50 offset:18432
v_mfma_f32_16x16x32_bf16 v[152:155],a[112:115],a[192:195],v[152:155]
buffer_load_dword v8,v19,s[48:51],s90 offen
v_mfma_f32_16x16x32_bf16 v[152:155],a[116:119],a[196:199],v[152:155]
s_add_u32 s90,s90,896
ds_read_b64_tr_b16 a[72:73],v50 offset:8
v_mfma_f32_16x16x32_bf16 v[184:187],a[116:119],a[204:207],v[184:187]
ds_read_b64_tr_b16 a[74:75],v50 offset:2056
v_mfma_f32_16x16x32_bf16 v[184:187],a[112:115],a[200:203],v[184:187]
ds_read_b64_tr_b16 a[76:77],v50 offset:16392
v_mfma_f32_16x16x32_bf16 v[216:219],a[112:115],a[208:211],v[216:219]
ds_read_b64_tr_b16 a[78:79],v50 offset:18440
v_mfma_f32_16x16x32_bf16 v[216:219],a[116:119],a[212:215],v[216:219]
v_mfma_f32_16x16x32_bf16 v[248:251],a[116:119],a[220:223],v[248:251]
ds_read_b64_tr_b16 a[80:81],v50 offset:256
v_mfma_f32_16x16x32_bf16 v[248:251],a[112:115],a[216:219],v[248:251]
ds_read_b64_tr_b16 a[82:83],v50 offset:2304
v_mfma_f32_16x16x32_bf16 v[156:159],a[120:123],a[192:195],v[156:159]
ds_read_b64_tr_b16 a[84:85],v50 offset:16640
v_mfma_f32_16x16x32_bf16 v[156:159],a[124:127],a[196:199],v[156:159]
v_mfma_f32_16x16x32_bf16 v[188:191],a[124:127],a[204:207],v[188:191]
ds_read_b64_tr_b16 a[86:87],v50 offset:18688
v_mfma_f32_16x16x32_bf16 v[188:191],a[120:123],a[200:203],v[188:191]
ds_read_b64_tr_b16 a[88:89],v50 offset:264
v_mfma_f32_16x16x32_bf16 v[220:223],a[120:123],a[208:211],v[220:223]
)A"
R"A(ds_read_b64_tr_b16 a[90:91],v50 offset:2312
v_mfma_f32_16x16x32_bf16 v[220:223],a[124:127],a[212:215],v[220:223]
v_mfma_f32_16x16x32_bf16 v[252:255],a[124:127],a[220:223],v[252:255]
ds_read_b64_tr_b16 a[92:93],v50 offset:16648
v_mfma_f32_16x16x32_bf16 v[252:255],a[120:123],a[216:219],v[252:255]
ds_read_b64_tr_b16 a[94:95],v50 offset:18696
s_sub_u32 s70,s70,1
s_cmp_lg_u32 s70,0
s_cbranch_scc1 L_qp_%=
s_waitcnt lgkmcnt(6)
v_mfma_f32_16x16x32_bf16 a[0:3],a[64:67],a[224:227],a[0:3]
s_memrealtime s[92:93]
s_memtime s[56:57]
s_add_u32 m0,s85,0
v_mfma_f32_16x16x32_bf16 a[0:3],a[68:71],a[228:231],a[0:3]
buffer_load_dwordx4 v32,s[28:31],s84 offen lds
v_mfma_f32_16x16x32_bf16 a[16:19],a[68:71],a[236:239],a[16:19]
s_sub_u32 s83,s83,524288
ds_read_b64_tr_b16 a[96:97],v50 offset:512
v_subrev_u32 v40,256,v40
v_mfma_f32_16x16x32_bf16 a[16:19],a[64:67],a[232:235],a[16:19]
s_mov_b32 s69,s86
ds_read_b64_tr_b16 a[98:99],v50 offset:2560
v_subrev_u32 v41,256,v41
v_mfma_f32_16x16x32_bf16 a[32:35],a[64:67],a[240:243],a[32:35]
s_mov_b32 s86,s87
ds_read_b64_tr_b16 a[100:101],v50 offset:16896
v_subrev_u32 v42,256,v42
v_mfma_f32_16x16x32_bf16 a[32:35],a[68:71],a[244:247],a[32:35]
s_mov_b32 s87,s88
ds_read_b64_tr_b16 a[102:103],v50 offset:18944
v_subrev_u32 v43,256,v43
v_mfma_f32_16x16x32_bf16 a[48:51],a[68:71],a[252:255],a[48:51]
s_add_u32 s75,s75,16384
v_subrev_u32 v44,256,v44
v_subrev_u32 v45,256,v45
v_mfma_f32_16x16x32_bf16 a[48:51],a[64:67],a[248:251],a[48:51]
buffer_load_dwordx4 v33,s[28:31],s84 offen offset:1024 lds
v_mfma_f32_16x16x32_bf16 a[4:7],a[72:75],a[224:227],a[4:7]
s_sub_u32 s78,s77,32768
ds_read_b64_tr_b16 a[104:105],v50 offset:520
v_subrev_u32 v46,256,v46
v_mfma_f32_16x16x32_bf16 a[4:7],a[76:79],a[228:231],a[4:7]
L_pd2_%=:
s_cbranch_execz L_pd6_%=
s_and_b32 s78,s78,0x1ffff
ds_read_b64_tr_b16 a[106:107],v50 offset:2568
v_subrev_u32 v47,256,v47
v_mfma_f32_16x16x32_bf16 a[20:23],a[76:79],a[236:239],a[20:23]
s_sub_u32 s78,s78,s77
ds_read_b64_tr_b16 a[108:109],v50 offset:16904
v_xor_b32 v48,98304,v48
v_mfma_f32_16x16x32_bf16 a[20:23],a[72:75],a[232:235],a[20:23]
s_add_u32 s77,s77,s78
ds_read_b64_tr_b16 a[110:111],v50 offset:18952
v_xor_b32 v49,98304,v49
v_mfma_f32_16x16x32_bf16 a[36:39],a[72:75],a[240:243],a[36:39]
s_mov_b32 s6,0
v_add_u32 v52,s87,v51
v_mfma_f32_16x16x32_bf16 a[36:39],a[76:79],a[244:247],a[36:39]
buffer_load_dwordx4 v34,s[28:31],s84 offen offset:2048 lds
v_mfma_f32_16x16x32_bf16 a[52:55],a[76:79],a[252:255],a[52:55]
s_mul_i32 s66,s60,40960
ds_read_b64_tr_b16 a[112:113],v50 offset:768
v_add_u32 v11,0,v23
v_mfma_f32_16x16x32_bf16 a[52:55],a[72:75],a[248:251],a[52:55]
s_add_u32 s65,s63,1
ds_read_b64_tr_b16 a[114:115],v50 offset:2816
v_mul_u32_u24 v12,3277,v11
v_mfma_f32_16x16x32_bf16 a[8:11],a[80:83],a[224:227],a[8:11]
s_waitcnt lgkmcnt(5)
ds_read_b64_tr_b16 a[116:117],v50 offset:17152
v_lshrrev_b32 v12,16,v12
v_mfma_f32_16x16x32_bf16 a[8:11],a[84:87],a[228:231],a[8:11]
ds_read_b64_tr_b16 a[118:119],v50 offset:19200
v_mul_u32_u24 v13,20,v12
v_mfma_f32_16x16x32_bf16 a[24:27],a[84:87],a[236:239],a[24:27]
v_sub_u32 v13,v11,v13
v_bfe_u32 v14,v12,2,1
v_mfma_f32_16x16x32_bf16 a[24:27],a[80:83],a[232:235],a[24:27]
buffer_load_dwordx4 v35,s[28:31],s84 offen offset:3072 lds
v_mfma_f32_16x16x32_bf16 a[40:43],a[80:83],a[240:243],a[40:43]
ds_read_b64_tr_b16 a[120:121],v50 offset:776
v_lshlrev_b32 v14,1,v14
v_mfma_f32_16x16x32_bf16 a[40:43],a[84:87],a[244:247],a[40:43]
ds_read_b64_tr_b16 a[122:123],v50 offset:2824
v_xor_b32 v13,v13,v14
v_mfma_f32_16x16x32_bf16 a[56:59],a[84:87],a[252:255],a[56:59]
ds_read_b64_tr_b16 a[124:125],v50 offset:17160
v_lshrrev_b32 v14,2,v13
v_mfma_f32_16x16x32_bf16 a[56:59],a[80:83],a[248:251],a[56:59]
s_add_u32 m0,s85,4096
s_xor_b32 s85,s85,98304
ds_read_b64_tr_b16 a[126:127],v50 offset:19208
v_mfma_f32_16x16x32_bf16 a[12:15],a[88:91],a[224:227],a[12:15]
v_cmp_eq_u32 vcc,3,v14
v_min_u32 v15,3,v14
v_mfma_f32_16x16x32_bf16 a[12:15],a[92:95],a[228:231],a[12:15]
buffer_load_dwordx4 v36,s[28:31],s84 offen lds
v_mfma_f32_16x16x32_bf16 a[28:31],a[92:95],a[236:239],a[28:31]
ds_read_b64_tr_b16 a[64:65],v50 offset:1024
v_add_u32 v15,s65,v15
v_mfma_f32_16x16x32_bf16 a[28:31],a[88:91],a[232:235],a[28:31]
ds_read_b64_tr_b16 a[66:67],v50 offset:3072
v_and_b32 v15,3,v15
v_mfma_f32_16x16x32_bf16 a[44:47],a[88:91],a[240:243],a[44:47]
ds_read_b64_tr_b16 a[68:69],v50 offset:17408
v_cndmask_b32_e64 v15,v15,4,vcc
v_mfma_f32_16x16x32_bf16 a[44:47],a[92:95],a[244:247],a[44:47]
ds_read_b64_tr_b16 a[70:71],v50 offset:19456
v_and_b32 v13,3,v13
v_mfma_f32_16x16x32_bf16 a[60:63],a[92:95],a[252:255],a[60:63]
v_lshl_add_u32 v13,v15,2,v13
v_mul_u32_u24 v12,1280,v12
v_mfma_f32_16x16x32_bf16 a[60:63],a[88:91],a[248:251],a[60:63]
buffer_load_dwordx4 v37,s[28:31],s84 offen offset:1024 lds
v_mfma_f32_16x16x32_bf16 v[64:67],a[96:99],a[224:227],v[64:67]
ds_read_b64_tr_b16 a[72:73],v50 offset:1032
v_lshl_add_u32 v12,v13,4,v12
v_mfma_f32_16x16x32_bf16 v[64:67],a[100:103],a[228:231],v[64:67]
ds_read_b64_tr_b16 a[74:75],v50 offset:3080
v_add_u32 v12,s66,v12
v_mfma_f32_16x16x32_bf16 v[80:83],a[100:103],a[236:239],v[80:83]
ds_read_b64_tr_b16 a[76:77],v50 offset:17416
v_subrev_u32 v24,0,v12
v_mfma_f32_16x16x32_bf16 v[80:83],a[96:99],a[232:235],v[80:83]
ds_read_b64_tr_b16 a[78:79],v50 offset:19464
v_add_u32 v11,64,v23
v_mfma_f32_16x16x32_bf16 v[96:99],a[96:99],a[240:243],v[96:99]
v_mul_u32_u24 v12,3277,v11
v_lshrrev_b32 v12,16,v12
v_mfma_f32_16x16x32_bf16 v[96:99],a[100:103],a[244:247],v[96:99]
buffer_load_dwordx4 v38,s[28:31],s84 offen offset:2048 lds
v_mfma_f32_16x16x32_bf16 v[112:115],a[100:103],a[252:255],v[112:115]
ds_read_b64_tr_b16 a[80:81],v50 offset:1280
v_mul_u32_u24 v13,20,v12
v_mfma_f32_16x16x32_bf16 v[112:115],a[96:99],a[248:251],v[112:115]
s_waitcnt lgkmcnt(5)
ds_read_b64_tr_b16 a[82:83],v50 offset:3328
v_sub_u32 v13,v11,v13
v_mfma_f32_16x16x32_bf16 v[68:71],a[104:107],a[224:227],v[68:71]
ds_read_b64_tr_b16 a[84:85],v50 offset:17664
v_bfe_u32 v14,v12,2,1
v_mfma_f32_16x16x32_bf16 v[68:71],a[108:111],a[228:231],v[68:71]
ds_read_b64_tr_b16 a[86:87],v50 offset:19712
v_lshlrev_b32 v14,1,v14
v_mfma_f32_16x16x32_bf16 v[84:87],a[108:111],a[236:239],v[84:87]
v_xor_b32 v13,v13,v14
v_lshrrev_b32 v14,2,v13
v_mfma_f32_16x16x32_bf16 v[84:87],a[104:107],a[232:235],v[84:87]
buffer_load_dwordx4 v39,s[28:31],s84 offen offset:3072 lds
v_mfma_f32_16x16x32_bf16 v[100:103],a[104:107],a[240:243],v[100:103]
s_add_u32 s84,s84,128
ds_read_b64_tr_b16 a[88:89],v50 offset:1288
v_cmp_eq_u32 vcc,3,v14
v_mfma_f32_16x16x32_bf16 v[100:103],a[108:111],a[244:247],v[100:103]
ds_read_b64_tr_b16 a[90:91],v50 offset:3336
v_min_u32 v15,3,v14
v_mfma_f32_16x16x32_bf16 v[116:119],a[108:111],a[252:255],v[116:119]
ds_read_b64_tr_b16 a[92:93],v50 offset:17672
v_add_u32 v15,s65,v15
v_mfma_f32_16x16x32_bf16 v[116:119],a[104:107],a[248:251],v[116:119]
s_add_u32 m0,s88,0
ds_read_b64_tr_b16 a[94:95],v50 offset:19720
v_and_b32 v15,3,v15
v_mfma_f32_16x16x32_bf16 v[72:75],a[112:115],a[224:227],v[72:75]
v_cndmask_b32_e64 v15,v15,4,vcc
v_and_b32 v13,3,v13
v_mfma_f32_16x16x32_bf16 v[72:75],a[116:119],a[228:231],v[72:75]
buffer_load_dwordx4 v40,s[36:39],s83 offen lds
v_mfma_f32_16x16x32_bf16 v[88:91],a[116:119],a[236:239],v[88:91]
ds_read_b64_tr_b16 a[96:97],v50 offset:1536
v_lshl_add_u32 v13,v15,2,v13
v_mfma_f32_16x16x32_bf16 v[88:91],a[112:115],a[232:235],v[88:91]
ds_read_b64_tr_b16 a[98:99],v50 offset:3584
v_mul_u32_u24 v12,1280,v12
v_mfma_f32_16x16x32_bf16 v[104:107],a[112:115],a[240:243],v[104:107]
ds_read_b64_tr_b16 a[100:101],v50 offset:17920
v_lshl_add_u32 v12,v13,4,v12
v_mfma_f32_16x16x32_bf16 v[104:107],a[116:119],a[244:247],v[104:107]
s_add_u32 m0,s88,1024
ds_read_b64_tr_b16 a[102:103],v50 offset:19968
v_add_u32 v12,s66,v12
v_mfma_f32_16x16x32_bf16 v[120:123],a[116:119],a[252:255],v[120:123]
v_subrev_u32 v25,1024,v12
v_add_u32 v11,128,v23
v_mfma_f32_16x16x32_bf16 v[120:123],a[112:115],a[248:251],v[120:123]
buffer_load_dwordx4 v41,s[36:39],s83 offen lds
v_mfma_f32_16x16x32_bf16 v[76:79],a[120:123],a[224:227],v[76:79]
ds_read_b64_tr_b16 a[104:105],v50 offset:1544
v_mul_u32_u24 v12,3277,v11
v_mfma_f32_16x16x32_bf16 v[76:79],a[124:127],a[228:231],v[76:79]
ds_read_b64_tr_b16 a[106:107],v50 offset:3592
v_lshrrev_b32 v12,16,v12
v_mfma_f32_16x16x32_bf16 v[92:95],a[124:127],a[236:239],v[92:95]
ds_read_b64_tr_b16 a[108:109],v50 offset:17928
v_mul_u32_u24 v13,20,v12
v_mfma_f32_16x16x32_bf16 v[92:95],a[120:123],a[232:235],v[92:95]
s_add_u32 m0,s88,2048
ds_read_b64_tr_b16 a[110:111],v50 offset:19976
v_sub_u32 v13,v11,v13
v_mfma_f32_16x16x32_bf16 v[108:111],a[120:123],a[240:243],v[108:111]
v_bfe_u32 v14,v12,2,1
v_lshlrev_b32 v14,1,v14
v_mfma_f32_16x16x32_bf16 v[108:111],a[124:127],a[244:247],v[108:111]
buffer_load_dwordx4 v42,s[36:39],s83 offen lds
v_mfma_f32_16x16x32_bf16 v[124:127],a[124:127],a[252:255],v[124:127]
ds_read_b64_tr_b16 a[112:113],v50 offset:1792
v_xor_b32 v13,v13,v14
v_mfma_f32_16x16x32_bf16 v[124:127],a[120:123],a[248:251],v[124:127]
ds_read_b64_tr_b16 a[114:115],v50 offset:3840
v_lshrrev_b32 v14,2,v13
v_mfma_f32_16x16x32_bf16 v[128:131],a[64:67],a[224:227],v[128:131]
ds_read_b64_tr_b16 a[116:117],v50 offset:18176
v_cmp_eq_u32 vcc,3,v14
v_mfma_f32_16x16x32_bf16 v[128:131],a[68:71],a[228:231],v[128:131]
s_add_u32 m0,s88,3072
ds_read_b64_tr_b16 a[118:119],v50 offset:20224
v_min_u32 v15,3,v14
v_mfma_f32_16x16x32_bf16 v[160:163],a[68:71],a[236:239],v[160:163]
v_add_u32 v15,s65,v15
v_and_b32 v15,3,v15
v_mfma_f32_16x16x32_bf16 v[160:163],a[64:67],a[232:235],v[160:163]
buffer_load_dwordx4 v43,s[36:39],s83 offen lds
v_mfma_f32_16x16x32_bf16 v[192:195],a[64:67],a[240:243],v[192:195]
ds_read_b64_tr_b16 a[120:121],v50 offset:1800
v_cndmask_b32_e64 v15,v15,4,vcc
v_mfma_f32_16x16x32_bf16 v[192:195],a[68:71],a[244:247],v[192:195]
ds_read_b64_tr_b16 a[122:123],v50 offset:3848
v_and_b32 v13,3,v13
v_mfma_f32_16x16x32_bf16 v[224:227],a[68:71],a[252:255],v[224:227]
s_waitcnt lgkmcnt(4)
ds_read_b64_tr_b16 a[124:125],v50 offset:18184
v_lshl_add_u32 v13,v15,2,v13
v_mfma_f32_16x16x32_bf16 v[224:227],a[64:67],a[248:251],v[224:227]
s_add_u32 m0,s88,4096
ds_read_b64_tr_b16 a[126:127],v50 offset:20232
v_add_u32 v50,s86,v51
v_mfma_f32_16x16x32_bf16 v[132:135],a[72:75],a[224:227],v[132:135]
v_mul_u32_u24 v12,1280,v12
v_lshl_add_u32 v12,v13,4,v12
v_mfma_f32_16x16x32_bf16 v[132:135],a[76:79],a[228:231],v[132:135]
buffer_load_dwordx4 v44,s[36:39],s83 offen lds
v_mfma_f32_16x16x32_bf16 v[164:167],a[76:79],a[236:239],v[164:167]
v_add_u32 v12,s66,v12
v_subrev_u32 v26,2048,v12
v_mfma_f32_16x16x32_bf16 v[164:167],a[72:75],a[232:235],v[164:167]
v_add_u32 v11,192,v23
v_mul_u32_u24 v12,3277,v11
v_mfma_f32_16x16x32_bf16 v[196:199],a[72:75],a[240:243],v[196:199]
v_lshrrev_b32 v12,16,v12
v_mul_u32_u24 v13,20,v12
v_mfma_f32_16x16x32_bf16 v[196:199],a[76:79],a[244:247],v[196:199]
s_add_u32 m0,s88,5120
v_sub_u32 v13,v11,v13
v_bfe_u32 v14,v12,2,1
v_mfma_f32_16x16x32_bf16 v[228:231],a[76:79],a[252:255],v[228:231]
v_lshlrev_b32 v14,1,v14
v_xor_b32 v13,v13,v14
v_mfma_f32_16x16x32_bf16 v[228:231],a[72:75],a[248:251],v[228:231]
buffer_load_dwordx4 v45,s[36:39],s83 offen lds
v_mfma_f32_16x16x32_bf16 v[136:139],a[80:83],a[224:227],v[136:139]
v_lshrrev_b32 v14,2,v13
v_cmp_eq_u32 vcc,3,v14
v_mfma_f32_16x16x32_bf16 v[136:139],a[84:87],a[228:231],v[136:139]
v_min_u32 v15,3,v14
v_add_u32 v15,s65,v15
v_mfma_f32_16x16x32_bf16 v[168:171],a[84:87],a[236:239],v[168:171]
v_and_b32 v15,3,v15
v_cndmask_b32_e64 v15,v15,4,vcc
v_mfma_f32_16x16x32_bf16 v[168:171],a[80:83],a[232:235],v[168:171]
s_add_u32 m0,s88,6144
v_and_b32 v13,3,v13
v_lshl_add_u32 v13,v15,2,v13
v_mfma_f32_16x16x32_bf16 v[200:203],a[80:83],a[240:243],v[200:203]
v_mul_u32_u24 v12,1280,v12
v_lshl_add_u32 v12,v13,4,v12
v_mfma_f32_16x16x32_bf16 v[200:203],a[84:87],a[244:247],v[200:203]
buffer_load_dwordx4 v46,s[36:39],s83 offen lds
v_mfma_f32_16x16x32_bf16 v[232:235],a[84:87],a[252:255],v[232:235]
v_add_u32 v12,s66,v12
v_subrev_u32 v27,3072,v12
v_mfma_f32_16x16x32_bf16 v[232:235],a[80:83],a[248:251],v[232:235]
v_add_u32 v11,256,v23
v_mul_u32_u24 v12,3277,v11
)A"
R"A(v_mfma_f32_16x16x32_bf16 v[140:143],a[88:91],a[224:227],v[140:143]
v_lshrrev_b32 v12,16,v12
v_mul_u32_u24 v13,20,v12
v_mfma_f32_16x16x32_bf16 v[140:143],a[92:95],a[228:231],v[140:143]
s_add_u32 m0,s88,7168
v_sub_u32 v13,v11,v13
v_bfe_u32 v14,v12,2,1
v_mfma_f32_16x16x32_bf16 v[172:175],a[92:95],a[236:239],v[172:175]
s_mov_b32 s88,s69
v_lshlrev_b32 v14,1,v14
v_xor_b32 v13,v13,v14
v_mfma_f32_16x16x32_bf16 v[172:175],a[88:91],a[232:235],v[172:175]
buffer_load_dwordx4 v47,s[36:39],s83 offen lds
v_mfma_f32_16x16x32_bf16 v[204:207],a[88:91],a[240:243],v[204:207]
s_waitcnt lgkmcnt(0)
s_add_u32 s83,s83,524288
s_sub_u32 s92,s92,s8
v_mfma_f32_16x16x32_bf16 v[204:207],a[92:95],a[244:247],v[204:207]
s_sub_u32 s56,s56,s4
s_mov_b32 s93,782
s_mul_i32 s92,s92,12600
v_mfma_f32_16x16x32_bf16 v[236:239],a[92:95],a[252:255],v[236:239]
s_cmp_ge_u32 s63,2
s_cselect_b32 s93,823,s93
s_mul_i32 s93,s93,s56
v_mfma_f32_16x16x32_bf16 v[236:239],a[88:91],a[248:251],v[236:239]
s_add_u32 s10,s20,s74
s_addc_u32 s11,s21,0
v_lshrrev_b32 v14,2,v13
s_waitcnt vmcnt(25)
s_barrier
v_mfma_f32_16x16x32_bf16 v[144:147],a[96:99],a[224:227],v[144:147]
s_add_u32 m0,s76,0
s_waitcnt vmcnt(17)
ds_read_b128 a[192:195],v48
v_cmp_eq_u32 vcc,3,v14
v_mfma_f32_16x16x32_bf16 v[144:147],a[100:103],a[228:231],v[144:147]
s_and_b32 s69,s75,0xfffc0000
ds_read_b128 a[196:199],v49
v_min_u32 v15,3,v14
v_mfma_f32_16x16x32_bf16 v[176:179],a[100:103],a[236:239],v[176:179]
global_load_lds_dwordx4 v4,s[10:11] nt
ds_read_b128 a[200:203],v48 offset:2048
v_mfma_f32_16x16x32_bf16 v[176:179],a[96:99],a[232:235],v[176:179]
s_add_u32 s75,s75,16384
ds_read_b128 a[204:207],v49 offset:2048
v_add_u32 v15,s65,v15
v_mfma_f32_16x16x32_bf16 v[208:211],a[96:99],a[240:243],v[208:211]
s_mov_b32 s65,262144
v_and_b32 v15,3,v15
v_cndmask_b32_e64 v15,v15,4,vcc
v_mfma_f32_16x16x32_bf16 v[208:211],a[100:103],a[244:247],v[208:211]
ds_read_b128 a[208:211],v48 offset:4096
v_and_b32 v13,3,v13
v_mfma_f32_16x16x32_bf16 v[240:243],a[100:103],a[252:255],v[240:243]
ds_read_b128 a[212:215],v49 offset:4096
v_lshl_add_u32 v13,v15,2,v13
v_mfma_f32_16x16x32_bf16 v[240:243],a[96:99],a[248:251],v[240:243]
ds_read_b128 a[216:219],v48 offset:6144
v_xor_b32 v48,98304,v48
v_mfma_f32_16x16x32_bf16 v[148:151],a[104:107],a[224:227],v[148:151]
v_mul_u32_u24 v12,1280,v12
v_lshl_add_u32 v12,v13,4,v12
v_mfma_f32_16x16x32_bf16 v[148:151],a[108:111],a[228:231],v[148:151]
ds_read_b128 a[220:223],v49 offset:6144
v_xor_b32 v49,98304,v49
v_mfma_f32_16x16x32_bf16 v[180:183],a[108:111],a[236:239],v[180:183]
ds_read_b64_tr_b16 a[64:65],v50
v_add_u32 v12,s66,v12
v_mfma_f32_16x16x32_bf16 v[180:183],a[104:107],a[232:235],v[180:183]
ds_read_b64_tr_b16 a[66:67],v50 offset:2048
v_subrev_u32 v7,0,v12
v_mfma_f32_16x16x32_bf16 v[212:215],a[104:107],a[240:243],v[212:215]
v_mfma_f32_16x16x32_bf16 v[212:215],a[108:111],a[244:247],v[212:215]
ds_read_b64_tr_b16 a[68:69],v50 offset:16384
v_mfma_f32_16x16x32_bf16 v[244:247],a[108:111],a[252:255],v[244:247]
ds_read_b64_tr_b16 a[70:71],v50 offset:18432
v_mfma_f32_16x16x32_bf16 v[244:247],a[104:107],a[248:251],v[244:247]
ds_read_b64_tr_b16 a[72:73],v50 offset:8
v_mfma_f32_16x16x32_bf16 v[152:155],a[112:115],a[224:227],v[152:155]
v_mfma_f32_16x16x32_bf16 v[152:155],a[116:119],a[228:231],v[152:155]
ds_read_b64_tr_b16 a[74:75],v50 offset:2056
v_mfma_f32_16x16x32_bf16 v[184:187],a[116:119],a[236:239],v[184:187]
global_load_lds_dwordx4 v4,s[10:11] offset:1024 nt
ds_read_b64_tr_b16 a[76:77],v50 offset:16392
v_mfma_f32_16x16x32_bf16 v[184:187],a[112:115],a[232:235],v[184:187]
ds_read_b64_tr_b16 a[78:79],v50 offset:18440
v_mfma_f32_16x16x32_bf16 v[216:219],a[112:115],a[240:243],v[216:219]
v_mfma_f32_16x16x32_bf16 v[216:219],a[116:119],a[244:247],v[216:219]
ds_read_b64_tr_b16 a[80:81],v50 offset:1024
v_mfma_f32_16x16x32_bf16 v[248:251],a[116:119],a[252:255],v[248:251]
ds_read_b64_tr_b16 a[82:83],v50 offset:3072
v_mfma_f32_16x16x32_bf16 v[248:251],a[112:115],a[248:251],v[248:251]
ds_read_b64_tr_b16 a[84:85],v50 offset:17408
v_mfma_f32_16x16x32_bf16 v[156:159],a[120:123],a[224:227],v[156:159]
v_mfma_f32_16x16x32_bf16 v[156:159],a[124:127],a[228:231],v[156:159]
ds_read_b64_tr_b16 a[86:87],v50 offset:19456
v_mfma_f32_16x16x32_bf16 v[188:191],a[124:127],a[236:239],v[188:191]
ds_read_b64_tr_b16 a[88:89],v50 offset:1032
v_mfma_f32_16x16x32_bf16 v[188:191],a[120:123],a[232:235],v[188:191]
s_cmp_lt_u32 s92,s93
s_cselect_b32 s5,6,0
s_cselect_b32 s7,3,0
s_cselect_b32 s9,8,10
ds_read_b64_tr_b16 a[90:91],v50 offset:3080
v_mfma_f32_16x16x32_bf16 v[220:223],a[120:123],a[240:243],v[220:223]
s_mov_b32 s4,s5
v_mfma_f32_16x16x32_bf16 v[220:223],a[124:127],a[244:247],v[220:223]
ds_read_b64_tr_b16 a[92:93],v50 offset:17416
v_mfma_f32_16x16x32_bf16 v[252:255],a[124:127],a[252:255],v[252:255]
ds_read_b64_tr_b16 a[94:95],v50 offset:19464
v_mfma_f32_16x16x32_bf16 v[252:255],a[120:123],a[248:251],v[252:255]
s_waitcnt vmcnt(10) lgkmcnt(5)
ds_read_b128 a[224:227],v48
v_mfma_f32_16x16x32_bf16 v[128:131],a[64:67],a[192:195],v[128:131]
v_mfma_f32_16x16x32_bf16 v[128:131],a[68:71],a[196:199],v[128:131]
ds_read_b128 a[228:231],v49
v_mfma_f32_16x16x32_bf16 v[160:163],a[68:71],a[204:207],v[160:163]
global_load_lds_dwordx4 v5,s[10:11] offset:2048 nt
ds_read_b128 a[236:239],v49 offset:2048
v_mfma_f32_16x16x32_bf16 v[160:163],a[64:67],a[200:203],v[160:163]
ds_read_b128 a[232:235],v48 offset:2048
v_mfma_f32_16x16x32_bf16 v[192:195],a[64:67],a[208:211],v[192:195]
v_mfma_f32_16x16x32_bf16 v[192:195],a[68:71],a[212:215],v[192:195]
ds_read_b128 a[240:243],v48 offset:4096
v_mfma_f32_16x16x32_bf16 v[224:227],a[68:71],a[220:223],v[224:227]
ds_read_b128 a[244:247],v49 offset:4096
v_mfma_f32_16x16x32_bf16 v[224:227],a[64:67],a[216:219],v[224:227]
ds_read_b128 a[252:255],v49 offset:6144
v_mfma_f32_16x16x32_bf16 v[132:135],a[72:75],a[192:195],v[132:135]
v_mfma_f32_16x16x32_bf16 v[132:135],a[76:79],a[196:199],v[132:135]
ds_read_b128 a[248:251],v48 offset:6144
v_mfma_f32_16x16x32_bf16 v[164:167],a[76:79],a[204:207],v[164:167]
ds_read_b64_tr_b16 a[96:97],v50 offset:256
v_mfma_f32_16x16x32_bf16 v[164:167],a[72:75],a[200:203],v[164:167]
ds_read_b64_tr_b16 a[98:99],v50 offset:2304
v_mfma_f32_16x16x32_bf16 v[196:199],a[72:75],a[208:211],v[196:199]
v_mfma_f32_16x16x32_bf16 v[196:199],a[76:79],a[212:215],v[196:199]
ds_read_b64_tr_b16 a[100:101],v50 offset:16640
v_mfma_f32_16x16x32_bf16 v[228:231],a[76:79],a[220:223],v[228:231]
ds_read_b64_tr_b16 a[102:103],v50 offset:18688
v_mfma_f32_16x16x32_bf16 v[228:231],a[72:75],a[216:219],v[228:231]
s_waitcnt lgkmcnt(5)
ds_read_b64_tr_b16 a[104:105],v50 offset:1280
v_mfma_f32_16x16x32_bf16 v[128:131],a[80:83],a[224:227],v[128:131]
v_mfma_f32_16x16x32_bf16 v[128:131],a[84:87],a[228:231],v[128:131]
ds_read_b64_tr_b16 a[106:107],v50 offset:3328
v_mfma_f32_16x16x32_bf16 v[160:163],a[84:87],a[236:239],v[160:163]
global_load_lds_dwordx4 v5,s[10:11] offset:3072
ds_read_b64_tr_b16 a[108:109],v50 offset:17664
v_mfma_f32_16x16x32_bf16 v[160:163],a[80:83],a[232:235],v[160:163]
ds_read_b64_tr_b16 a[110:111],v50 offset:19712
v_mfma_f32_16x16x32_bf16 v[192:195],a[80:83],a[240:243],v[192:195]
v_mfma_f32_16x16x32_bf16 v[192:195],a[84:87],a[244:247],v[192:195]
ds_read_b64_tr_b16 a[112:113],v50 offset:264
v_mul_f32 v128,s71,v128
v_mfma_f32_16x16x32_bf16 v[224:227],a[84:87],a[252:255],v[224:227]
s_waitcnt lgkmcnt(5)
ds_read_b64_tr_b16 a[114:115],v50 offset:2312
v_mul_f32 v129,s71,v129
v_mfma_f32_16x16x32_bf16 v[224:227],a[80:83],a[248:251],v[224:227]
ds_read_b64_tr_b16 a[116:117],v50 offset:16648
v_mul_f32 v130,s71,v130
v_mfma_f32_16x16x32_bf16 v[132:135],a[88:91],a[224:227],v[132:135]
v_mul_f32 v131,s71,v131
v_cvt_pk_bf16_f32 v128,v128,v129
v_mfma_f32_16x16x32_bf16 v[132:135],a[92:95],a[228:231],v[132:135]
ds_read_b64_tr_b16 a[118:119],v50 offset:18696
v_cvt_pk_bf16_f32 v129,v130,v131
v_mfma_f32_16x16x32_bf16 v[164:167],a[92:95],a[236:239],v[164:167]
ds_read_b64_tr_b16 a[120:121],v50 offset:1288
v_mul_f32 v192,s71,v192
v_mfma_f32_16x16x32_bf16 v[164:167],a[88:91],a[232:235],v[164:167]
ds_read_b64_tr_b16 a[122:123],v50 offset:3336
v_mul_f32 v193,s71,v193
v_mfma_f32_16x16x32_bf16 v[196:199],a[88:91],a[240:243],v[196:199]
v_mul_f32 v132,s71,v132
v_mul_f32 v133,s71,v133
v_mfma_f32_16x16x32_bf16 v[196:199],a[92:95],a[244:247],v[196:199]
ds_read_b64_tr_b16 a[124:125],v50 offset:17672
v_mul_f32 v134,s71,v134
v_mfma_f32_16x16x32_bf16 v[228:231],a[92:95],a[252:255],v[228:231]
s_add_u32 s10,s24,s74
s_addc_u32 s11,s25,0
ds_read_b64_tr_b16 a[126:127],v50 offset:19720
v_mfma_f32_16x16x32_bf16 v[228:231],a[88:91],a[248:251],v[228:231]
s_add_u32 s74,s74,16384
s_and_b32 s74,s74,0x3ffff
ds_read_b64_tr_b16 a[128:129],v50 offset:512
v_mfma_f32_16x16x32_bf16 v[136:139],a[96:99],a[192:195],v[136:139]
s_add_u32 m0,s76,16384
s_or_b32 s74,s74,s69
s_sub_u32 s76,s76,32768
v_mfma_f32_16x16x32_bf16 v[136:139],a[100:103],a[196:199],v[136:139]
s_and_b32 s76,s76,0x1ffff
s_and_b32 s69,s75,0xfffc0000
ds_read_b64_tr_b16 a[130:131],v50 offset:2560
s_waitcnt lgkmcnt(6)
v_mfma_f32_16x16x32_bf16 v[136:139],a[104:107],a[224:227],v[136:139]
global_load_lds_dwordx4 v4,s[10:11]
ds_read_b64_tr_b16 a[132:133],v50 offset:16896
v_mfma_f32_16x16x32_bf16 v[136:139],a[108:111],a[228:231],v[136:139]
s_add_u32 s75,s75,16384
ds_read_b64_tr_b16 a[134:135],v50 offset:18944
v_mul_f32 v135,s71,v135
v_mfma_f32_16x16x32_bf16 v[168:171],a[108:111],a[236:239],v[168:171]
v_cvt_pk_bf16_f32 v130,v132,v133
v_cvt_pk_bf16_f32 v131,v134,v135
v_mfma_f32_16x16x32_bf16 v[168:171],a[104:107],a[232:235],v[168:171]
ds_read_b64_tr_b16 a[136:137],v50 offset:1536
v_mul_f32 v194,s71,v194
v_mfma_f32_16x16x32_bf16 v[168:171],a[100:103],a[204:207],v[168:171]
ds_read_b64_tr_b16 a[138:139],v50 offset:3584
v_mul_f32 v195,s71,v195
v_mfma_f32_16x16x32_bf16 v[168:171],a[96:99],a[200:203],v[168:171]
ds_read_b64_tr_b16 a[140:141],v50 offset:17920
v_mul_f32 v196,s71,v196
v_mfma_f32_16x16x32_bf16 v[200:203],a[96:99],a[208:211],v[200:203]
v_mul_f32 v197,s71,v197
v_mul_f32 v198,s71,v198
v_mfma_f32_16x16x32_bf16 v[200:203],a[100:103],a[212:215],v[200:203]
ds_read_b64_tr_b16 a[142:143],v50 offset:19968
v_mul_f32 v199,s71,v199
v_mfma_f32_16x16x32_bf16 v[200:203],a[104:107],a[240:243],v[200:203]
ds_read_b64_tr_b16 a[144:145],v50 offset:520
v_cvt_pk_bf16_f32 v192,v192,v193
v_mfma_f32_16x16x32_bf16 v[200:203],a[108:111],a[244:247],v[200:203]
ds_read_b64_tr_b16 a[146:147],v50 offset:2568
v_cvt_pk_bf16_f32 v193,v194,v195
v_mfma_f32_16x16x32_bf16 v[232:235],a[108:111],a[252:255],v[232:235]
v_cvt_pk_bf16_f32 v194,v196,v197
v_cvt_pk_bf16_f32 v195,v198,v199
v_mfma_f32_16x16x32_bf16 v[232:235],a[104:107],a[248:251],v[232:235]
ds_read_b64_tr_b16 a[148:149],v50 offset:16904
v_mul_f32 v136,s71,v136
v_mfma_f32_16x16x32_bf16 v[232:235],a[100:103],a[220:223],v[232:235]
ds_read_b64_tr_b16 a[150:151],v50 offset:18952
v_mul_f32 v137,s71,v137
v_mfma_f32_16x16x32_bf16 v[232:235],a[96:99],a[216:219],v[232:235]
ds_read_b64_tr_b16 a[152:153],v50 offset:1544
v_mul_f32 v138,s71,v138
v_mfma_f32_16x16x32_bf16 v[140:143],a[112:115],a[192:195],v[140:143]
v_mul_f32 v139,s71,v139
v_cvt_pk_bf16_f32 v136,v136,v137
v_mfma_f32_16x16x32_bf16 v[140:143],a[116:119],a[196:199],v[140:143]
s_waitcnt lgkmcnt(5)
ds_read_b64_tr_b16 a[154:155],v50 offset:3592
v_cvt_pk_bf16_f32 v137,v138,v139
v_mfma_f32_16x16x32_bf16 v[140:143],a[120:123],a[224:227],v[140:143]
global_load_lds_dwordx4 v5,s[10:11] offset:1024
ds_read_b64_tr_b16 a[156:157],v50 offset:17928
v_mfma_f32_16x16x32_bf16 v[140:143],a[124:127],a[228:231],v[140:143]
ds_read_b64_tr_b16 a[158:159],v50 offset:19976
v_mul_f32 v160,s71,v160
v_mfma_f32_16x16x32_bf16 v[172:175],a[124:127],a[236:239],v[172:175]
v_mul_f32 v161,s71,v161
v_mul_f32 v162,s71,v162
v_mfma_f32_16x16x32_bf16 v[172:175],a[120:123],a[232:235],v[172:175]
ds_read_b64_tr_b16 a[160:161],v50 offset:768
v_mul_f32 v163,s71,v163
v_mfma_f32_16x16x32_bf16 v[172:175],a[116:119],a[204:207],v[172:175]
ds_read_b64_tr_b16 a[162:163],v50 offset:2816
v_mul_f32 v140,s71,v140
v_mfma_f32_16x16x32_bf16 v[172:175],a[112:115],a[200:203],v[172:175]
ds_read_b64_tr_b16 a[164:165],v50 offset:17152
)A"
R"A(v_mul_f32 v141,s71,v141
v_mfma_f32_16x16x32_bf16 v[204:207],a[112:115],a[208:211],v[204:207]
v_mul_f32 v142,s71,v142
v_mul_f32 v143,s71,v143
v_mfma_f32_16x16x32_bf16 v[204:207],a[116:119],a[212:215],v[204:207]
ds_read_b64_tr_b16 a[166:167],v50 offset:19200
v_cvt_pk_bf16_f32 v138,v140,v141
v_mfma_f32_16x16x32_bf16 v[204:207],a[120:123],a[240:243],v[204:207]
ds_read_b64_tr_b16 a[168:169],v50 offset:1792
v_cvt_pk_bf16_f32 v139,v142,v143
v_mfma_f32_16x16x32_bf16 v[204:207],a[124:127],a[244:247],v[204:207]
ds_read_b64_tr_b16 a[170:171],v50 offset:3840
v_mul_f32 v164,s71,v164
v_mfma_f32_16x16x32_bf16 v[236:239],a[124:127],a[252:255],v[236:239]
v_mul_f32 v165,s71,v165
v_mul_f32 v166,s71,v166
v_mfma_f32_16x16x32_bf16 v[236:239],a[120:123],a[248:251],v[236:239]
ds_read_b64_tr_b16 a[172:173],v50 offset:18176
v_mul_f32 v167,s71,v167
v_mfma_f32_16x16x32_bf16 v[236:239],a[116:119],a[220:223],v[236:239]
ds_read_b64_tr_b16 a[174:175],v50 offset:20224
v_cvt_pk_bf16_f32 v160,v160,v161
v_mfma_f32_16x16x32_bf16 v[236:239],a[112:115],a[216:219],v[236:239]
ds_read_b64_tr_b16 a[176:177],v50 offset:776
v_cvt_pk_bf16_f32 v161,v162,v163
v_mfma_f32_16x16x32_bf16 v[144:147],a[128:131],a[192:195],v[144:147]
v_cvt_pk_bf16_f32 v162,v164,v165
v_cvt_pk_bf16_f32 v163,v166,v167
v_mfma_f32_16x16x32_bf16 v[144:147],a[132:135],a[196:199],v[144:147]
ds_read_b64_tr_b16 a[178:179],v50 offset:2824
v_mul_f32 v224,s71,v224
v_mfma_f32_16x16x32_bf16 v[144:147],a[136:139],a[224:227],v[144:147]
global_load_lds_dwordx4 v4,s[10:11] offset:2048
ds_read_b64_tr_b16 a[180:181],v50 offset:17160
v_mfma_f32_16x16x32_bf16 v[144:147],a[140:143],a[228:231],v[144:147]
ds_read_b64_tr_b16 a[182:183],v50 offset:19208
v_mul_f32 v225,s71,v225
v_mfma_f32_16x16x32_bf16 v[176:179],a[140:143],a[236:239],v[176:179]
v_mul_f32 v226,s71,v226
v_mul_f32 v227,s71,v227
v_mfma_f32_16x16x32_bf16 v[176:179],a[136:139],a[232:235],v[176:179]
ds_read_b64_tr_b16 a[184:185],v50 offset:1800
v_mul_f32 v228,s71,v228
v_mfma_f32_16x16x32_bf16 v[176:179],a[132:135],a[204:207],v[176:179]
ds_read_b64_tr_b16 a[186:187],v50 offset:3848
v_mul_f32 v144,s71,v144
v_mfma_f32_16x16x32_bf16 v[176:179],a[128:131],a[200:203],v[176:179]
ds_read_b64_tr_b16 a[188:189],v50 offset:18184
v_mul_f32 v145,s71,v145
v_mfma_f32_16x16x32_bf16 v[208:211],a[128:131],a[208:211],v[208:211]
v_mul_f32 v146,s71,v146
v_mul_f32 v147,s71,v147
v_mfma_f32_16x16x32_bf16 v[208:211],a[132:135],a[212:215],v[208:211]
ds_read_b64_tr_b16 a[190:191],v50 offset:20232
v_cvt_pk_bf16_f32 v144,v144,v145
v_mfma_f32_16x16x32_bf16 v[208:211],a[136:139],a[240:243],v[208:211]
v_cvt_pk_bf16_f32 v145,v146,v147
v_mul_f32 v229,s71,v229
v_mfma_f32_16x16x32_bf16 v[208:211],a[140:143],a[244:247],v[208:211]
v_mul_f32 v230,s71,v230
v_mul_f32 v231,s71,v231
v_mfma_f32_16x16x32_bf16 v[240:243],a[140:143],a[252:255],v[240:243]
v_cvt_pk_bf16_f32 v224,v224,v225
v_cvt_pk_bf16_f32 v225,v226,v227
v_mfma_f32_16x16x32_bf16 v[240:243],a[136:139],a[248:251],v[240:243]
v_cvt_pk_bf16_f32 v226,v228,v229
v_cvt_pk_bf16_f32 v227,v230,v231
v_mfma_f32_16x16x32_bf16 v[240:243],a[132:135],a[220:223],v[240:243]
v_mul_f32 v168,s71,v168
v_mul_f32 v169,s71,v169
v_mfma_f32_16x16x32_bf16 v[240:243],a[128:131],a[216:219],v[240:243]
v_mul_f32 v170,s71,v170
v_mul_f32 v171,s71,v171
s_waitcnt vmcnt(7)
s_barrier
s_waitcnt lgkmcnt(1)
v_mfma_f32_16x16x32_bf16 v[148:151],a[144:147],a[192:195],v[148:151]
ds_read_b64_tr_b16 a[64:65],v52
v_mul_f32 v172,s71,v172
v_mfma_f32_16x16x32_bf16 v[148:151],a[148:151],a[196:199],v[148:151]
global_load_lds_dwordx4 v5,s[10:11] offset:3072
ds_read_b64_tr_b16 a[66:67],v52 offset:2048
L_pd3_%=:
s_cbranch_execz L_pd7_%=
v_mfma_f32_16x16x32_bf16 v[148:151],a[152:155],a[224:227],v[148:151]
ds_read_b64_tr_b16 a[68:69],v52 offset:16384
v_mul_f32 v173,s71,v173
v_mfma_f32_16x16x32_bf16 v[148:151],a[156:159],a[228:231],v[148:151]
ds_read_b64_tr_b16 a[70:71],v52 offset:18432
v_mul_f32 v174,s71,v174
v_mfma_f32_16x16x32_bf16 v[180:183],a[156:159],a[236:239],v[180:183]
v_mul_f32 v175,s71,v175
v_cvt_pk_bf16_f32 v168,v168,v169
v_mfma_f32_16x16x32_bf16 v[180:183],a[152:155],a[232:235],v[180:183]
ds_read_b64_tr_b16 a[72:73],v52 offset:1024
v_cvt_pk_bf16_f32 v169,v170,v171
v_mfma_f32_16x16x32_bf16 v[180:183],a[148:151],a[204:207],v[180:183]
ds_read_b64_tr_b16 a[74:75],v52 offset:3072
v_mul_f32 v148,s71,v148
v_mfma_f32_16x16x32_bf16 v[180:183],a[144:147],a[200:203],v[180:183]
ds_read_b64_tr_b16 a[76:77],v52 offset:17408
v_mul_f32 v149,s71,v149
v_mfma_f32_16x16x32_bf16 v[212:215],a[144:147],a[208:211],v[212:215]
v_mul_f32 v150,s71,v150
v_mul_f32 v151,s71,v151
v_mfma_f32_16x16x32_bf16 v[212:215],a[148:151],a[212:215],v[212:215]
ds_read_b64_tr_b16 a[78:79],v52 offset:19456
v_cvt_pk_bf16_f32 v146,v148,v149
v_mfma_f32_16x16x32_bf16 v[212:215],a[152:155],a[240:243],v[212:215]
ds_read_b64_tr_b16 a[80:81],v52 offset:8
v_cvt_pk_bf16_f32 v147,v150,v151
v_mfma_f32_16x16x32_bf16 v[212:215],a[156:159],a[244:247],v[212:215]
ds_read_b64_tr_b16 a[82:83],v52 offset:2056
v_cvt_pk_bf16_f32 v170,v172,v173
v_mfma_f32_16x16x32_bf16 v[244:247],a[156:159],a[252:255],v[244:247]
v_cvt_pk_bf16_f32 v171,v174,v175
v_mul_f32 v176,s71,v176
v_mfma_f32_16x16x32_bf16 v[244:247],a[152:155],a[248:251],v[244:247]
ds_read_b64_tr_b16 a[84:85],v52 offset:16392
v_mul_f32 v177,s71,v177
v_mfma_f32_16x16x32_bf16 v[244:247],a[148:151],a[220:223],v[244:247]
s_add_u32 s10,s20,s74
s_addc_u32 s11,s21,0
ds_read_b64_tr_b16 a[86:87],v52 offset:18440
v_mfma_f32_16x16x32_bf16 v[244:247],a[144:147],a[216:219],v[244:247]
ds_read_b64_tr_b16 a[88:89],v52 offset:1032
v_mul_f32 v178,s71,v178
v_mfma_f32_16x16x32_bf16 v[152:155],a[160:163],a[192:195],v[152:155]
s_add_u32 m0,s76,0
v_mul_f32 v179,s71,v179
v_mul_f32 v180,s71,v180
v_mfma_f32_16x16x32_bf16 v[152:155],a[164:167],a[196:199],v[152:155]
ds_read_b64_tr_b16 a[90:91],v52 offset:3080
v_mul_f32 v181,s71,v181
v_mfma_f32_16x16x32_bf16 v[152:155],a[168:171],a[224:227],v[152:155]
global_load_lds_dwordx4 v4,s[10:11] nt
ds_read_b64_tr_b16 a[92:93],v52 offset:17416
v_mfma_f32_16x16x32_bf16 v[152:155],a[172:175],a[228:231],v[152:155]
ds_read_b64_tr_b16 a[94:95],v52 offset:19464
v_mul_f32 v182,s71,v182
v_mfma_f32_16x16x32_bf16 v[184:187],a[172:175],a[236:239],v[184:187]
v_mul_f32 v183,s71,v183
v_cvt_pk_bf16_f32 v176,v176,v177
v_mfma_f32_16x16x32_bf16 v[184:187],a[168:171],a[232:235],v[184:187]
ds_read_b64_tr_b16 a[96:97],v52 offset:256
v_cvt_pk_bf16_f32 v177,v178,v179
v_mfma_f32_16x16x32_bf16 v[184:187],a[164:167],a[204:207],v[184:187]
ds_read_b64_tr_b16 a[98:99],v52 offset:2304
v_mul_f32 v152,s71,v152
v_mfma_f32_16x16x32_bf16 v[184:187],a[160:163],a[200:203],v[184:187]
ds_read_b64_tr_b16 a[100:101],v52 offset:16640
v_mul_f32 v153,s71,v153
v_mfma_f32_16x16x32_bf16 v[216:219],a[160:163],a[208:211],v[216:219]
v_mul_f32 v154,s71,v154
v_mul_f32 v155,s71,v155
v_mfma_f32_16x16x32_bf16 v[216:219],a[164:167],a[212:215],v[216:219]
ds_read_b64_tr_b16 a[102:103],v52 offset:18688
v_cvt_pk_bf16_f32 v152,v152,v153
v_mfma_f32_16x16x32_bf16 v[216:219],a[168:171],a[240:243],v[216:219]
ds_read_b64_tr_b16 a[104:105],v52 offset:1280
v_cvt_pk_bf16_f32 v153,v154,v155
v_mfma_f32_16x16x32_bf16 v[216:219],a[172:175],a[244:247],v[216:219]
ds_read_b64_tr_b16 a[106:107],v52 offset:3328
v_cvt_pk_bf16_f32 v178,v180,v181
v_mfma_f32_16x16x32_bf16 v[248:251],a[172:175],a[252:255],v[248:251]
v_cvt_pk_bf16_f32 v179,v182,v183
v_mul_f32 v184,s71,v184
v_mfma_f32_16x16x32_bf16 v[248:251],a[168:171],a[248:251],v[248:251]
ds_read_b64_tr_b16 a[108:109],v52 offset:17664
v_mul_f32 v185,s71,v185
v_mfma_f32_16x16x32_bf16 v[248:251],a[164:167],a[220:223],v[248:251]
ds_read_b64_tr_b16 a[110:111],v52 offset:19712
v_mul_f32 v186,s71,v186
v_mfma_f32_16x16x32_bf16 v[248:251],a[160:163],a[216:219],v[248:251]
ds_read_b64_tr_b16 a[112:113],v52 offset:264
v_mul_f32 v187,s71,v187
v_mfma_f32_16x16x32_bf16 v[156:159],a[176:179],a[192:195],v[156:159]
v_cvt_pk_bf16_f32 v184,v184,v185
v_cvt_pk_bf16_f32 v185,v186,v187
v_mfma_f32_16x16x32_bf16 v[156:159],a[180:183],a[196:199],v[156:159]
global_load_lds_dwordx4 v4,s[10:11] offset:1024 nt
ds_read_b64_tr_b16 a[114:115],v52 offset:2312
v_mfma_f32_16x16x32_bf16 v[156:159],a[184:187],a[224:227],v[156:159]
s_waitcnt lgkmcnt(5)
ds_read_b64_tr_b16 a[116:117],v52 offset:16648
v_mul_f32 v200,s71,v200
v_mfma_f32_16x16x32_bf16 v[156:159],a[188:191],a[228:231],v[156:159]
ds_read_b64_tr_b16 a[118:119],v52 offset:18696
v_mul_f32 v201,s71,v201
v_mfma_f32_16x16x32_bf16 v[188:191],a[188:191],a[236:239],v[188:191]
v_mul_f32 v202,s71,v202
v_mul_f32 v203,s71,v203
v_mfma_f32_16x16x32_bf16 v[188:191],a[184:187],a[232:235],v[188:191]
ds_read_b64_tr_b16 a[120:121],v52 offset:1288
v_mul_f32 v204,s71,v204
v_mfma_f32_16x16x32_bf16 v[188:191],a[180:183],a[204:207],v[188:191]
ds_read_b64_tr_b16 a[122:123],v52 offset:3336
v_mul_f32 v156,s71,v156
v_mfma_f32_16x16x32_bf16 v[188:191],a[176:179],a[200:203],v[188:191]
ds_read_b64_tr_b16 a[124:125],v52 offset:17672
v_mul_f32 v157,s71,v157
v_mfma_f32_16x16x32_bf16 v[220:223],a[176:179],a[208:211],v[220:223]
v_mul_f32 v158,s71,v158
v_mul_f32 v159,s71,v159
v_mfma_f32_16x16x32_bf16 v[220:223],a[180:183],a[212:215],v[220:223]
ds_read_b64_tr_b16 a[126:127],v52 offset:19720
v_cvt_pk_bf16_f32 v154,v156,v157
v_mfma_f32_16x16x32_bf16 v[220:223],a[184:187],a[240:243],v[220:223]
v_cvt_pk_bf16_f32 v155,v158,v159
v_mul_f32 v188,s71,v188
v_mfma_f32_16x16x32_bf16 v[220:223],a[188:191],a[244:247],v[220:223]
v_mul_f32 v189,s71,v189
v_mul_f32 v190,s71,v190
v_mfma_f32_16x16x32_bf16 v[252:255],a[188:191],a[252:255],v[252:255]
v_mul_f32 v191,s71,v191
v_cvt_pk_bf16_f32 v186,v188,v189
v_mfma_f32_16x16x32_bf16 v[252:255],a[184:187],a[248:251],v[252:255]
v_cvt_pk_bf16_f32 v187,v190,v191
v_mul_f32 v205,s71,v205
v_mfma_f32_16x16x32_bf16 v[252:255],a[180:183],a[220:223],v[252:255]
v_mul_f32 v206,s71,v206
v_mul_f32 v207,s71,v207
v_mfma_f32_16x16x32_bf16 v[252:255],a[176:179],a[216:219],v[252:255]
v_cvt_pk_bf16_f32 v200,v200,v201
v_cvt_pk_bf16_f32 v201,v202,v203
v_mfma_f32_16x16x32_bf16 a[0:3],a[64:67],a[192:195],a[0:3]
global_load_lds_dwordx4 v5,s[10:11] offset:2048 nt
v_cvt_pk_bf16_f32 v202,v204,v205
v_mfma_f32_16x16x32_bf16 a[0:3],a[68:71],a[196:199],a[0:3]
v_cvt_pk_bf16_f32 v203,v206,v207
v_mul_f32 v208,s71,v208
v_mfma_f32_16x16x32_bf16 a[0:3],a[72:75],a[224:227],a[0:3]
v_mul_f32 v209,s71,v209
v_mul_f32 v210,s71,v210
v_mfma_f32_16x16x32_bf16 a[0:3],a[76:79],a[228:231],a[0:3]
v_mul_f32 v211,s71,v211
v_mul_f32 v212,s71,v212
v_mfma_f32_16x16x32_bf16 a[16:19],a[76:79],a[236:239],a[16:19]
v_mul_f32 v213,s71,v213
v_mul_f32 v214,s71,v214
v_mfma_f32_16x16x32_bf16 a[16:19],a[72:75],a[232:235],a[16:19]
v_mul_f32 v215,s71,v215
v_cvt_pk_bf16_f32 v208,v208,v209
v_mfma_f32_16x16x32_bf16 a[16:19],a[68:71],a[204:207],a[16:19]
v_accvgpr_read_b32 v8,a0
v_accvgpr_read_b32 v9,a1
v_mfma_f32_16x16x32_bf16 a[16:19],a[64:67],a[200:203],a[16:19]
v_accvgpr_read_b32 v10,a2
v_accvgpr_read_b32 v11,a3
v_mfma_f32_16x16x32_bf16 a[32:35],a[64:67],a[208:211],a[32:35]
v_mul_f32 v8,s71,v8
v_mul_f32 v9,s71,v9
v_mfma_f32_16x16x32_bf16 a[32:35],a[68:71],a[212:215],a[32:35]
v_mul_f32 v10,s71,v10
v_mul_f32 v11,s71,v11
v_mfma_f32_16x16x32_bf16 a[32:35],a[72:75],a[240:243],a[32:35]
v_cvt_pk_bf16_f32 v132,v8,v9
v_cvt_pk_bf16_f32 v133,v10,v11
v_mfma_f32_16x16x32_bf16 a[32:35],a[76:79],a[244:247],a[32:35]
v_accvgpr_read_b32 v16,a16
v_accvgpr_read_b32 v17,a17
v_mfma_f32_16x16x32_bf16 a[48:51],a[76:79],a[252:255],a[48:51]
ds_read_b64_tr_b16 a[76:77],v52 offset:17920
v_accvgpr_read_b32 v18,a18
v_mfma_f32_16x16x32_bf16 a[48:51],a[72:75],a[248:251],a[48:51]
ds_read_b64_tr_b16 a[72:73],v52 offset:1536
v_accvgpr_read_b32 v19,a19
v_mfma_f32_16x16x32_bf16 a[48:51],a[68:71],a[220:223],a[48:51]
ds_read_b64_tr_b16 a[68:69],v52 offset:16896
v_accvgpr_read_b32 v8,a32
v_mfma_f32_16x16x32_bf16 a[48:51],a[64:67],a[216:219],a[48:51]
global_load_lds_dwordx4 v5,s[10:11] offset:3072
ds_read_b64_tr_b16 a[64:65],v52 offset:512
v_mfma_f32_16x16x32_bf16 a[4:7],a[80:83],a[192:195],a[4:7]
v_accvgpr_read_b32 v9,a33
v_accvgpr_read_b32 v10,a34
v_mfma_f32_16x16x32_bf16 a[4:7],a[84:87],a[196:199],a[4:7]
)A"
R"A(ds_read_b64_tr_b16 a[66:67],v52 offset:2560
v_accvgpr_read_b32 v11,a35
v_mfma_f32_16x16x32_bf16 a[4:7],a[88:91],a[224:227],a[4:7]
ds_read_b64_tr_b16 a[70:71],v52 offset:18944
v_mul_f32 v8,s71,v8
v_mfma_f32_16x16x32_bf16 a[4:7],a[92:95],a[228:231],a[4:7]
ds_read_b64_tr_b16 a[74:75],v52 offset:3584
v_mul_f32 v9,s71,v9
v_mfma_f32_16x16x32_bf16 a[20:23],a[92:95],a[236:239],a[20:23]
v_mul_f32 v10,s71,v10
v_mul_f32 v11,s71,v11
v_mfma_f32_16x16x32_bf16 a[20:23],a[88:91],a[232:235],a[20:23]
ds_read_b64_tr_b16 a[78:79],v52 offset:19968
v_cvt_pk_bf16_f32 v196,v8,v9
v_mfma_f32_16x16x32_bf16 a[20:23],a[84:87],a[204:207],a[20:23]
v_accvgpr_read_b32 v12,a4
v_accvgpr_read_b32 v13,a5
v_mfma_f32_16x16x32_bf16 a[20:23],a[80:83],a[200:203],a[20:23]
v_accvgpr_read_b32 v14,a6
v_accvgpr_read_b32 v15,a7
v_mfma_f32_16x16x32_bf16 a[36:39],a[80:83],a[208:211],a[36:39]
v_mul_f32 v12,s71,v12
v_mul_f32 v13,s71,v13
v_mfma_f32_16x16x32_bf16 a[36:39],a[84:87],a[212:215],a[36:39]
v_mul_f32 v14,s71,v14
v_mul_f32 v15,s71,v15
v_mfma_f32_16x16x32_bf16 a[36:39],a[88:91],a[240:243],a[36:39]
s_add_u32 s10,s24,s74
s_addc_u32 s11,s25,0
s_add_u32 s74,s74,16384
v_mfma_f32_16x16x32_bf16 a[36:39],a[92:95],a[244:247],a[36:39]
s_and_b32 s74,s74,0x3ffff
s_or_b32 s74,s74,s69
v_cvt_pk_bf16_f32 v134,v12,v13
v_mfma_f32_16x16x32_bf16 a[52:55],a[92:95],a[252:255],a[52:55]
s_add_u32 m0,s76,16384
s_sub_u32 s76,s76,32768
ds_read_b64_tr_b16 a[92:93],v52 offset:17928
v_mfma_f32_16x16x32_bf16 a[52:55],a[88:91],a[248:251],a[52:55]
s_and_b32 s76,s76,0x1ffff
ds_read_b64_tr_b16 a[88:89],v52 offset:1544
v_cvt_pk_bf16_f32 v135,v14,v15
v_mfma_f32_16x16x32_bf16 a[52:55],a[84:87],a[220:223],a[52:55]
global_load_lds_dwordx4 v4,s[10:11]
ds_read_b64_tr_b16 a[84:85],v52 offset:16904
v_mfma_f32_16x16x32_bf16 a[52:55],a[80:83],a[216:219],a[52:55]
s_and_b32 s69,s75,0xfffc0000
ds_read_b64_tr_b16 a[80:81],v52 offset:520
v_accvgpr_read_b32 v12,a36
v_mfma_f32_16x16x32_bf16 a[8:11],a[96:99],a[192:195],a[8:11]
s_add_u32 s75,s75,16384
v_accvgpr_read_b32 v13,a37
v_accvgpr_read_b32 v14,a38
v_mfma_f32_16x16x32_bf16 a[8:11],a[100:103],a[196:199],a[8:11]
s_waitcnt lgkmcnt(4)
ds_read_b64_tr_b16 a[82:83],v52 offset:2568
v_accvgpr_read_b32 v15,a39
v_mfma_f32_16x16x32_bf16 a[8:11],a[104:107],a[224:227],a[8:11]
ds_read_b64_tr_b16 a[86:87],v52 offset:18952
v_mul_f32 v12,s71,v12
v_mfma_f32_16x16x32_bf16 a[8:11],a[108:111],a[228:231],a[8:11]
ds_read_b64_tr_b16 a[90:91],v52 offset:3592
v_mul_f32 v13,s71,v13
v_mfma_f32_16x16x32_bf16 a[24:27],a[108:111],a[236:239],a[24:27]
v_mul_f32 v14,s71,v14
v_mul_f32 v15,s71,v15
v_mfma_f32_16x16x32_bf16 a[24:27],a[104:107],a[232:235],a[24:27]
ds_read_b64_tr_b16 a[94:95],v52 offset:19976
v_cvt_pk_bf16_f32 v197,v10,v11
v_mfma_f32_16x16x32_bf16 a[24:27],a[100:103],a[204:207],a[24:27]
v_cvt_pk_bf16_f32 v198,v12,v13
v_cvt_pk_bf16_f32 v199,v14,v15
v_mfma_f32_16x16x32_bf16 a[24:27],a[96:99],a[200:203],a[24:27]
v_accvgpr_read_b32 v8,a8
v_accvgpr_read_b32 v9,a9
v_mfma_f32_16x16x32_bf16 a[40:43],a[96:99],a[208:211],a[40:43]
v_accvgpr_read_b32 v10,a10
v_accvgpr_read_b32 v11,a11
v_mfma_f32_16x16x32_bf16 a[40:43],a[100:103],a[212:215],a[40:43]
v_mul_f32 v8,s71,v8
v_mul_f32 v9,s71,v9
v_mfma_f32_16x16x32_bf16 a[40:43],a[104:107],a[240:243],a[40:43]
v_mul_f32 v10,s71,v10
v_mul_f32 v11,s71,v11
v_mfma_f32_16x16x32_bf16 a[40:43],a[108:111],a[244:247],a[40:43]
v_cvt_pk_bf16_f32 v140,v8,v9
v_cvt_pk_bf16_f32 v141,v10,v11
v_mfma_f32_16x16x32_bf16 a[56:59],a[108:111],a[252:255],a[56:59]
ds_read_b64_tr_b16 a[108:109],v52 offset:18176
v_accvgpr_read_b32 v20,a20
v_mfma_f32_16x16x32_bf16 a[56:59],a[104:107],a[248:251],a[56:59]
global_load_lds_dwordx4 v5,s[10:11] offset:1024
ds_read_b64_tr_b16 a[104:105],v52 offset:1792
v_mfma_f32_16x16x32_bf16 a[56:59],a[100:103],a[220:223],a[56:59]
ds_read_b64_tr_b16 a[100:101],v52 offset:17152
v_accvgpr_read_b32 v21,a21
v_mfma_f32_16x16x32_bf16 a[56:59],a[96:99],a[216:219],a[56:59]
ds_read_b64_tr_b16 a[96:97],v52 offset:768
v_accvgpr_read_b32 v22,a22
v_mfma_f32_16x16x32_bf16 a[12:15],a[112:115],a[192:195],a[12:15]
v_accvgpr_read_b32 v23,a23
v_mul_f32 v16,s71,v16
v_mfma_f32_16x16x32_bf16 a[12:15],a[116:119],a[196:199],a[12:15]
ds_read_b64_tr_b16 a[98:99],v52 offset:2816
v_mul_f32 v17,s71,v17
v_mfma_f32_16x16x32_bf16 a[12:15],a[120:123],a[224:227],a[12:15]
ds_read_b64_tr_b16 a[102:103],v52 offset:19200
v_mul_f32 v18,s71,v18
v_mfma_f32_16x16x32_bf16 a[12:15],a[124:127],a[228:231],a[12:15]
ds_read_b64_tr_b16 a[106:107],v52 offset:3840
v_mul_f32 v19,s71,v19
v_mfma_f32_16x16x32_bf16 a[28:31],a[124:127],a[236:239],a[28:31]
v_mul_f32 v20,s71,v20
v_mul_f32 v21,s71,v21
v_mfma_f32_16x16x32_bf16 a[28:31],a[120:123],a[232:235],a[28:31]
ds_read_b64_tr_b16 a[110:111],v52 offset:20224
v_mul_f32 v22,s71,v22
v_mfma_f32_16x16x32_bf16 a[28:31],a[116:119],a[204:207],a[28:31]
v_accvgpr_read_b32 v12,a12
v_accvgpr_read_b32 v13,a13
v_mfma_f32_16x16x32_bf16 a[28:31],a[112:115],a[200:203],a[28:31]
v_accvgpr_read_b32 v14,a14
v_accvgpr_read_b32 v15,a15
v_mfma_f32_16x16x32_bf16 a[44:47],a[112:115],a[208:211],a[44:47]
v_mul_f32 v12,s71,v12
v_mul_f32 v13,s71,v13
v_mfma_f32_16x16x32_bf16 a[44:47],a[116:119],a[212:215],a[44:47]
v_mul_f32 v14,s71,v14
v_mul_f32 v15,s71,v15
v_mfma_f32_16x16x32_bf16 a[44:47],a[120:123],a[240:243],a[44:47]
v_cvt_pk_bf16_f32 v142,v12,v13
v_cvt_pk_bf16_f32 v143,v14,v15
v_mfma_f32_16x16x32_bf16 a[44:47],a[124:127],a[244:247],a[44:47]
v_mul_f32 v23,s71,v23
v_cvt_pk_bf16_f32 v164,v16,v17
v_mfma_f32_16x16x32_bf16 a[60:63],a[124:127],a[252:255],a[60:63]
global_load_lds_dwordx4 v4,s[10:11] offset:2048
ds_read_b64_tr_b16 a[124:125],v52 offset:18184
v_mfma_f32_16x16x32_bf16 a[60:63],a[120:123],a[248:251],a[60:63]
ds_read_b64_tr_b16 a[120:121],v52 offset:1800
v_cvt_pk_bf16_f32 v165,v18,v19
v_mfma_f32_16x16x32_bf16 a[60:63],a[116:119],a[220:223],a[60:63]
ds_read_b64_tr_b16 a[116:117],v52 offset:17160
v_cvt_pk_bf16_f32 v166,v20,v21
v_mfma_f32_16x16x32_bf16 a[60:63],a[112:115],a[216:219],a[60:63]
ds_read_b64_tr_b16 a[112:113],v52 offset:776
v_cvt_pk_bf16_f32 v167,v22,v23
v_mfma_f32_16x16x32_bf16 v[64:67],a[64:67],a[192:195],v[64:67]
v_accvgpr_read_b32 v16,a48
v_accvgpr_read_b32 v17,a49
v_mfma_f32_16x16x32_bf16 v[64:67],a[68:71],a[196:199],v[64:67]
ds_read_b64_tr_b16 a[114:115],v52 offset:2824
v_accvgpr_read_b32 v18,a50
v_mfma_f32_16x16x32_bf16 v[64:67],a[72:75],a[224:227],v[64:67]
ds_read_b64_tr_b16 a[118:119],v52 offset:19208
v_accvgpr_read_b32 v19,a51
v_mfma_f32_16x16x32_bf16 v[64:67],a[76:79],a[228:231],v[64:67]
ds_read_b64_tr_b16 a[122:123],v52 offset:3848
v_accvgpr_read_b32 v20,a52
v_mfma_f32_16x16x32_bf16 v[80:83],a[76:79],a[236:239],v[80:83]
v_accvgpr_read_b32 v21,a53
v_accvgpr_read_b32 v22,a54
v_mfma_f32_16x16x32_bf16 v[80:83],a[72:75],a[232:235],v[80:83]
ds_read_b64_tr_b16 a[126:127],v52 offset:20232
v_accvgpr_read_b32 v23,a55
v_mfma_f32_16x16x32_bf16 v[80:83],a[68:71],a[204:207],v[80:83]
v_mul_f32 v64,s71,v64
v_mul_f32 v65,s71,v65
v_mfma_f32_16x16x32_bf16 v[80:83],a[64:67],a[200:203],v[80:83]
v_mul_f32 v66,s71,v66
v_mul_f32 v67,s71,v67
v_mfma_f32_16x16x32_bf16 v[96:99],a[64:67],a[208:211],v[96:99]
v_cvt_pk_bf16_f32 v148,v64,v65
v_cvt_pk_bf16_f32 v149,v66,v67
v_mfma_f32_16x16x32_bf16 v[96:99],a[68:71],a[212:215],v[96:99]
v_mul_f32 v16,s71,v16
v_mul_f32 v17,s71,v17
v_mfma_f32_16x16x32_bf16 v[96:99],a[72:75],a[240:243],v[96:99]
v_mul_f32 v18,s71,v18
v_mul_f32 v19,s71,v19
v_mfma_f32_16x16x32_bf16 v[96:99],a[76:79],a[244:247],v[96:99]
global_load_lds_dwordx4 v5,s[10:11] offset:3072
v_mul_f32 v20,s71,v20
v_mfma_f32_16x16x32_bf16 v[112:115],a[76:79],a[252:255],v[112:115]
v_mul_f32 v21,s71,v21
v_mul_f32 v22,s71,v22
v_mfma_f32_16x16x32_bf16 v[112:115],a[72:75],a[248:251],v[112:115]
v_mul_f32 v23,s71,v23
v_cvt_pk_bf16_f32 v228,v16,v17
v_mfma_f32_16x16x32_bf16 v[112:115],a[68:71],a[220:223],v[112:115]
v_cvt_pk_bf16_f32 v229,v18,v19
v_cvt_pk_bf16_f32 v230,v20,v21
v_mfma_f32_16x16x32_bf16 v[112:115],a[64:67],a[216:219],v[112:115]
v_cvt_pk_bf16_f32 v231,v22,v23
v_accvgpr_read_b32 v16,a24
s_waitcnt vmcnt(8) lgkmcnt(0)
s_barrier
v_mfma_f32_16x16x32_bf16 v[68:71],a[80:83],a[192:195],v[68:71]
ds_read_b128 a[128:131],v1
v_accvgpr_read_b32 v17,a25
v_mfma_f32_16x16x32_bf16 v[68:71],a[84:87],a[196:199],v[68:71]
ds_read_b128 a[132:135],v1 offset:256
v_accvgpr_read_b32 v18,a26
v_mfma_f32_16x16x32_bf16 v[68:71],a[88:91],a[224:227],v[68:71]
ds_read_b128 a[136:139],v1 offset:512
v_accvgpr_read_b32 v19,a27
v_mfma_f32_16x16x32_bf16 v[68:71],a[92:95],a[228:231],v[68:71]
ds_read_b128 a[140:143],v1 offset:768
v_accvgpr_read_b32 v20,a28
v_mfma_f32_16x16x32_bf16 v[84:87],a[92:95],a[236:239],v[84:87]
v_accvgpr_read_b32 v21,a29
v_accvgpr_read_b32 v22,a30
v_mfma_f32_16x16x32_bf16 v[84:87],a[88:91],a[232:235],v[84:87]
v_accvgpr_read_b32 v23,a31
v_mul_f32 v16,s71,v16
v_mfma_f32_16x16x32_bf16 v[84:87],a[84:87],a[204:207],v[84:87]
s_add_u32 s10,s20,s74
s_addc_u32 s11,s21,0
v_mul_f32 v68,s71,v68
v_mfma_f32_16x16x32_bf16 v[84:87],a[80:83],a[200:203],v[84:87]
v_mul_f32 v69,s71,v69
v_mul_f32 v70,s71,v70
v_mfma_f32_16x16x32_bf16 v[100:103],a[80:83],a[208:211],v[100:103]
s_add_u32 m0,s76,0
v_mul_f32 v71,s71,v71
v_cvt_pk_bf16_f32 v150,v68,v69
v_mfma_f32_16x16x32_bf16 v[100:103],a[84:87],a[212:215],v[100:103]
v_cvt_pk_bf16_f32 v151,v70,v71
v_mul_f32 v17,s71,v17
v_mfma_f32_16x16x32_bf16 v[100:103],a[88:91],a[240:243],v[100:103]
global_load_lds_dwordx4 v4,s[10:11] nt
v_mul_f32 v18,s71,v18
v_mfma_f32_16x16x32_bf16 v[100:103],a[92:95],a[244:247],v[100:103]
v_mul_f32 v19,s71,v19
v_mul_f32 v20,s71,v20
v_mfma_f32_16x16x32_bf16 v[116:119],a[92:95],a[252:255],v[116:119]
v_mul_f32 v21,s71,v21
v_mul_f32 v22,s71,v22
v_mfma_f32_16x16x32_bf16 v[116:119],a[88:91],a[248:251],v[116:119]
v_mul_f32 v23,s71,v23
v_cvt_pk_bf16_f32 v172,v16,v17
v_mfma_f32_16x16x32_bf16 v[116:119],a[84:87],a[220:223],v[116:119]
v_cvt_pk_bf16_f32 v173,v18,v19
v_cvt_pk_bf16_f32 v174,v20,v21
v_mfma_f32_16x16x32_bf16 v[116:119],a[80:83],a[216:219],v[116:119]
v_cvt_pk_bf16_f32 v175,v22,v23
v_mul_f32 v80,s71,v80
v_mfma_f32_16x16x32_bf16 v[72:75],a[96:99],a[192:195],v[72:75]
v_mul_f32 v81,s71,v81
v_mul_f32 v82,s71,v82
v_mfma_f32_16x16x32_bf16 v[72:75],a[100:103],a[196:199],v[72:75]
v_mul_f32 v83,s71,v83
v_mul_f32 v84,s71,v84
v_mfma_f32_16x16x32_bf16 v[72:75],a[104:107],a[224:227],v[72:75]
v_mul_f32 v85,s71,v85
v_mul_f32 v86,s71,v86
v_mfma_f32_16x16x32_bf16 v[72:75],a[108:111],a[228:231],v[72:75]
v_mul_f32 v87,s71,v87
v_cvt_pk_bf16_f32 v180,v80,v81
v_mfma_f32_16x16x32_bf16 v[88:91],a[108:111],a[236:239],v[88:91]
v_cvt_pk_bf16_f32 v181,v82,v83
v_cvt_pk_bf16_f32 v182,v84,v85
v_mfma_f32_16x16x32_bf16 v[88:91],a[104:107],a[232:235],v[88:91]
v_cvt_pk_bf16_f32 v183,v86,v87
v_accvgpr_read_b32 v8,a40
v_mfma_f32_16x16x32_bf16 v[88:91],a[100:103],a[204:207],v[88:91]
v_mul_f32 v72,s71,v72
v_mul_f32 v73,s71,v73
v_mfma_f32_16x16x32_bf16 v[88:91],a[96:99],a[200:203],v[88:91]
v_mul_f32 v74,s71,v74
v_mul_f32 v75,s71,v75
v_mfma_f32_16x16x32_bf16 v[104:107],a[96:99],a[208:211],v[104:107]
global_load_lds_dwordx4 v4,s[10:11] offset:1024 nt
v_cvt_pk_bf16_f32 v156,v72,v73
v_mfma_f32_16x16x32_bf16 v[104:107],a[100:103],a[212:215],v[104:107]
v_cvt_pk_bf16_f32 v157,v74,v75
v_accvgpr_read_b32 v9,a41
v_mfma_f32_16x16x32_bf16 v[104:107],a[104:107],a[240:243],v[104:107]
v_mul_f32 v88,s71,v88
v_mul_f32 v89,s71,v89
v_mfma_f32_16x16x32_bf16 v[104:107],a[108:111],a[244:247],v[104:107]
v_mul_f32 v90,s71,v90
v_mul_f32 v91,s71,v91
v_mfma_f32_16x16x32_bf16 v[120:123],a[108:111],a[252:255],v[120:123]
v_cvt_pk_bf16_f32 v188,v88,v89
v_cvt_pk_bf16_f32 v189,v90,v91
v_mfma_f32_16x16x32_bf16 v[120:123],a[104:107],a[248:251],v[120:123]
v_accvgpr_read_b32 v10,a42
v_accvgpr_read_b32 v11,a43
v_mfma_f32_16x16x32_bf16 v[120:123],a[100:103],a[220:223],v[120:123]
v_accvgpr_read_b32 v12,a44
v_accvgpr_read_b32 v13,a45
v_mfma_f32_16x16x32_bf16 v[120:123],a[96:99],a[216:219],v[120:123]
v_accvgpr_read_b32 v14,a46
v_accvgpr_read_b32 v15,a47
v_mfma_f32_16x16x32_bf16 v[76:79],a[112:115],a[192:195],v[76:79]
v_mul_f32 v8,s71,v8
v_mul_f32 v9,s71,v9
v_mfma_f32_16x16x32_bf16 v[76:79],a[116:119],a[196:199],v[76:79]
)A"
R"A(v_mul_f32 v10,s71,v10
v_mul_f32 v11,s71,v11
v_mfma_f32_16x16x32_bf16 v[76:79],a[120:123],a[224:227],v[76:79]
v_mul_f32 v12,s71,v12
v_mul_f32 v13,s71,v13
v_mfma_f32_16x16x32_bf16 v[76:79],a[124:127],a[228:231],v[76:79]
v_mul_f32 v14,s71,v14
v_mul_f32 v15,s71,v15
v_mfma_f32_16x16x32_bf16 v[92:95],a[124:127],a[236:239],v[92:95]
v_cvt_pk_bf16_f32 v204,v8,v9
v_cvt_pk_bf16_f32 v205,v10,v11
v_mfma_f32_16x16x32_bf16 v[92:95],a[120:123],a[232:235],v[92:95]
v_cvt_pk_bf16_f32 v206,v12,v13
v_cvt_pk_bf16_f32 v207,v14,v15
v_mfma_f32_16x16x32_bf16 v[92:95],a[116:119],a[204:207],v[92:95]
global_load_lds_dwordx4 v5,s[10:11] offset:2048 nt
v_mul_f32 v76,s71,v76
v_mfma_f32_16x16x32_bf16 v[92:95],a[112:115],a[200:203],v[92:95]
v_mul_f32 v77,s71,v77
v_mul_f32 v78,s71,v78
v_mfma_f32_16x16x32_bf16 v[108:111],a[112:115],a[208:211],v[108:111]
v_mul_f32 v79,s71,v79
v_cvt_pk_bf16_f32 v158,v76,v77
v_mfma_f32_16x16x32_bf16 v[108:111],a[116:119],a[212:215],v[108:111]
ds_read_b128 a[156:159],v1 offset:4864
v_cvt_pk_bf16_f32 v159,v78,v79
v_mfma_f32_16x16x32_bf16 v[108:111],a[120:123],a[240:243],v[108:111]
v_mul_f32 v92,s71,v92
v_mul_f32 v93,s71,v93
v_mfma_f32_16x16x32_bf16 v[108:111],a[124:127],a[244:247],v[108:111]
v_mul_f32 v94,s71,v94
v_mul_f32 v95,s71,v95
v_mfma_f32_16x16x32_bf16 v[124:127],a[124:127],a[252:255],v[124:127]
v_cvt_pk_bf16_f32 v190,v92,v93
v_cvt_pk_bf16_f32 v191,v94,v95
v_mfma_f32_16x16x32_bf16 v[124:127],a[120:123],a[248:251],v[124:127]
v_cvt_pk_bf16_f32 v209,v210,v211
v_cvt_pk_bf16_f32 v210,v212,v213
v_mfma_f32_16x16x32_bf16 v[124:127],a[116:119],a[220:223],v[124:127]
ds_read_b128 a[152:155],v1 offset:4608
v_cvt_pk_bf16_f32 v211,v214,v215
v_mfma_f32_16x16x32_bf16 v[124:127],a[112:115],a[216:219],v[124:127]
v_mul_f32 v96,s71,v96
v_mul_f32 v97,s71,v97
s_waitcnt lgkmcnt(2)
v_mfma_f32_16x16x32_bf16 v[64:67],a[128:131],v[132:135],0
v_mul_f32 v98,s71,v98
v_mul_f32 v99,s71,v99
v_mfma_f32_16x16x32_bf16 v[64:67],a[132:135],v[140:143],v[64:67]
v_mul_f32 v100,s71,v100
v_mul_f32 v101,s71,v101
v_mfma_f32_16x16x32_bf16 v[64:67],a[136:139],v[148:151],v[64:67]
v_mul_f32 v102,s71,v102
v_mul_f32 v103,s71,v103
v_mfma_f32_16x16x32_bf16 v[64:67],a[140:143],v[156:159],v[64:67]
global_load_lds_dwordx4 v5,s[10:11] offset:3072
v_cvt_pk_bf16_f32 v212,v96,v97
v_mfma_f32_16x16x32_bf16 v[80:83],a[128:131],v[164:167],0
v_cvt_pk_bf16_f32 v213,v98,v99
v_cvt_pk_bf16_f32 v214,v100,v101
v_mfma_f32_16x16x32_bf16 v[80:83],a[132:135],v[172:175],v[80:83]
v_cvt_pk_bf16_f32 v215,v102,v103
v_mul_f32 v216,s71,v216
v_mfma_f32_16x16x32_bf16 v[80:83],a[136:139],v[180:183],v[80:83]
v_max3_f32 v64,v64,v65,v66
v_max_f32 v64,v64,v67
v_mov_b32 v65,v64
v_mfma_f32_16x16x32_bf16 v[80:83],a[140:143],v[188:191],v[80:83]
v_mul_f32 v217,s71,v217
v_mul_f32 v218,s71,v218
v_mfma_f32_16x16x32_bf16 v[96:99],a[128:131],v[196:199],0
v_permlane32_swap_b32 v64,v65
v_max_f32 v64,v64,v65
v_mov_b32 v65,v64
v_mul_f32 v219,s71,v219
v_mul_f32 v220,s71,v220
v_mul_f32 v221,s71,v221
v_mul_f32 v222,s71,v222
v_mul_f32 v223,s71,v223
v_cvt_pk_bf16_f32 v216,v216,v217
v_cvt_pk_bf16_f32 v217,v218,v219
v_cvt_pk_bf16_f32 v218,v220,v221
v_cvt_pk_bf16_f32 v219,v222,v223
v_mul_f32 v104,s71,v104
v_mul_f32 v105,s71,v105
v_mul_f32 v106,s71,v106
v_mul_f32 v107,s71,v107
v_mul_f32 v108,s71,v108
v_mul_f32 v109,s71,v109
v_mul_f32 v110,s71,v110
v_mul_f32 v111,s71,v111
v_cvt_pk_bf16_f32 v220,v104,v105
v_cvt_pk_bf16_f32 v221,v106,v107
v_cvt_pk_bf16_f32 v222,v108,v109
v_cvt_pk_bf16_f32 v223,v110,v111
v_mfma_f32_16x16x32_bf16 v[96:99],a[132:135],v[204:207],v[96:99]
ds_read_b128 a[148:151],v1 offset:4352
v_max3_f32 v80,v80,v81,v82
v_mfma_f32_16x16x32_bf16 v[96:99],a[136:139],v[212:215],v[96:99]
ds_read_b128 a[144:147],v1 offset:4096
v_permlane16_swap_b32 v64,v65
v_max_f32 v64,v64,v65
v_sub_f32 v35,0,v64
v_sub_f32 v34,0,v64
v_sub_f32 v33,0,v64
v_sub_f32 v32,0,v64
v_mfma_f32_16x16x32_bf16 v[96:99],a[140:143],v[220:223],v[96:99]
ds_read_b128 a[160:163],v1 offset:8192
v_max_f32 v80,v80,v83
v_mfma_f32_16x16x32_bf16 v[64:67],a[128:131],v[132:135],v[32:35]
s_waitcnt lgkmcnt(3)
ds_read_b128 a[164:167],v1 offset:8448
v_mov_b32 v81,v80
L_pd4_%=:
s_cbranch_execz L_pd8_%=
v_mfma_f32_16x16x32_bf16 v[64:67],a[132:135],v[140:143],v[64:67]
s_add_u32 s10,s24,s74
s_addc_u32 s11,s25,0
v_mul_f32 v232,s71,v232
v_mfma_f32_16x16x32_bf16 v[64:67],a[136:139],v[148:151],v[64:67]
s_add_u32 s74,s74,16384
ds_read_b128 a[168:171],v1 offset:8704
v_max3_f32 v96,v96,v97,v98
v_mfma_f32_16x16x32_bf16 v[64:67],a[140:143],v[156:159],v[64:67]
s_add_u32 m0,s76,16384
ds_read_b128 a[172:175],v1 offset:8960
v_max_f32 v96,v96,v99
v_mfma_f32_16x16x32_bf16 v[68:71],a[156:159],v[156:159],v[32:35]
s_and_b32 s74,s74,0x3ffff
ds_read_b128 a[188:191],v1 offset:13056
v_mov_b32 v97,v96
v_mfma_f32_16x16x32_bf16 v[68:71],a[152:155],v[148:151],v[68:71]
global_load_lds_dwordx4 v4,s[10:11]
v_mul_f32 v233,s71,v233
s_waitcnt lgkmcnt(6)
v_mfma_f32_16x16x32_bf16 v[68:71],a[148:151],v[140:143],v[68:71]
s_or_b32 s74,s74,s69
ds_read_b128 a[184:187],v1 offset:12800
v_mul_f32 v234,s71,v234
s_waitcnt lgkmcnt(6)
v_mfma_f32_16x16x32_bf16 v[68:71],a[144:147],v[132:135],v[68:71]
s_sub_u32 s76,s76,32768
ds_read_b128 a[180:183],v1 offset:12544
v_mul_f32 v235,s71,v235
s_waitcnt lgkmcnt(6)
v_mfma_f32_16x16x32_bf16 v[72:75],a[160:163],v[132:135],v[32:35]
s_and_b32 s76,s76,0x1ffff
ds_read_b128 a[176:179],v1 offset:12288
v_mul_f32 v236,s71,v236
s_waitcnt lgkmcnt(6)
v_mfma_f32_16x16x32_bf16 v[72:75],a[164:167],v[140:143],v[72:75]
s_and_b32 s69,s75,0xfffc0000
v_permlane32_swap_b32 v80,v81
s_waitcnt lgkmcnt(5)
v_mfma_f32_16x16x32_bf16 v[72:75],a[168:171],v[148:151],v[72:75]
s_add_u32 s75,s75,16384
v_max_f32 v80,v80,v81
v_mov_b32 v81,v80
s_waitcnt lgkmcnt(4)
v_mfma_f32_16x16x32_bf16 v[72:75],a[172:175],v[156:159],v[72:75]
s_waitcnt lgkmcnt(3)
v_mul_f32 v237,s71,v237
v_mul_f32 v238,s71,v238
v_mfma_f32_16x16x32_bf16 v[76:79],a[188:191],v[156:159],v[32:35]
s_waitcnt lgkmcnt(2)
v_permlane16_swap_b32 v80,v81
v_mfma_f32_16x16x32_bf16 v[76:79],a[184:187],v[148:151],v[76:79]
s_waitcnt lgkmcnt(1)
v_max_f32 v80,v80,v81
v_sub_f32 v39,0,v80
v_sub_f32 v38,0,v80
v_sub_f32 v37,0,v80
v_sub_f32 v36,0,v80
v_mfma_f32_16x16x32_bf16 v[76:79],a[180:183],v[140:143],v[76:79]
s_waitcnt lgkmcnt(0)
v_mul_f32 v239,s71,v239
v_cvt_pk_bf16_f32 v232,v232,v233
v_mfma_f32_16x16x32_bf16 v[76:79],a[176:179],v[132:135],v[76:79]
v_cvt_pk_bf16_f32 v233,v234,v235
v_cvt_pk_bf16_f32 v234,v236,v237
v_mfma_f32_16x16x32_bf16 v[80:83],a[128:131],v[164:167],v[36:39]
v_cvt_pk_bf16_f32 v235,v238,v239
v_accvgpr_read_b32 v16,a56
v_mfma_f32_16x16x32_bf16 v[80:83],a[132:135],v[172:175],v[80:83]
v_accvgpr_read_b32 v17,a57
v_accvgpr_read_b32 v18,a58
v_mfma_f32_16x16x32_bf16 v[80:83],a[136:139],v[180:183],v[80:83]
v_accvgpr_read_b32 v19,a59
v_accvgpr_read_b32 v20,a60
v_mfma_f32_16x16x32_bf16 v[80:83],a[140:143],v[188:191],v[80:83]
global_load_lds_dwordx4 v5,s[10:11] offset:1024
v_accvgpr_read_b32 v21,a61
v_mfma_f32_16x16x32_bf16 v[84:87],a[156:159],v[188:191],v[36:39]
v_accvgpr_read_b32 v22,a62
v_accvgpr_read_b32 v23,a63
v_mfma_f32_16x16x32_bf16 v[84:87],a[152:155],v[180:183],v[84:87]
v_mul_f32 v16,s71,v16
v_mul_f32 v17,s71,v17
v_mfma_f32_16x16x32_bf16 v[84:87],a[148:151],v[172:175],v[84:87]
v_mul_f32 v18,s71,v18
v_mul_f32 v19,s71,v19
v_mfma_f32_16x16x32_bf16 v[84:87],a[144:147],v[164:167],v[84:87]
v_mul_f32 v20,s71,v20
v_mul_f32 v21,s71,v21
v_mfma_f32_16x16x32_bf16 v[88:91],a[160:163],v[164:167],v[36:39]
v_mul_f32 v22,s71,v22
v_mul_f32 v23,s71,v23
v_mfma_f32_16x16x32_bf16 v[88:91],a[164:167],v[172:175],v[88:91]
v_cvt_pk_bf16_f32 v236,v16,v17
v_cvt_pk_bf16_f32 v237,v18,v19
v_mfma_f32_16x16x32_bf16 v[88:91],a[168:171],v[180:183],v[88:91]
v_cvt_pk_bf16_f32 v238,v20,v21
v_cvt_pk_bf16_f32 v239,v22,v23
v_mfma_f32_16x16x32_bf16 v[88:91],a[172:175],v[188:191],v[88:91]
v_mul_f32 v240,s71,v240
v_mul_f32 v241,s71,v241
v_mfma_f32_16x16x32_bf16 v[92:95],a[188:191],v[188:191],v[36:39]
v_mul_f32 v242,s71,v242
v_mul_f32 v243,s71,v243
v_mfma_f32_16x16x32_bf16 v[92:95],a[184:187],v[180:183],v[92:95]
v_mul_f32 v244,s71,v244
v_mul_f32 v245,s71,v245
v_mfma_f32_16x16x32_bf16 v[92:95],a[180:183],v[172:175],v[92:95]
v_mul_f32 v246,s71,v246
v_mul_f32 v247,s71,v247
v_mfma_f32_16x16x32_bf16 v[92:95],a[176:179],v[164:167],v[92:95]
v_cvt_pk_bf16_f32 v240,v240,v241
v_cvt_pk_bf16_f32 v241,v242,v243
v_cvt_pk_bf16_f32 v242,v244,v245
v_cvt_pk_bf16_f32 v243,v246,v247
v_mul_f32 v112,s71,v112
v_mul_f32 v113,s71,v113
v_mul_f32 v114,s71,v114
v_mul_f32 v115,s71,v115
v_mul_f32 v116,s71,v116
v_mul_f32 v117,s71,v117
v_mul_f32 v118,s71,v118
v_mul_f32 v119,s71,v119
v_cvt_pk_bf16_f32 v244,v112,v113
v_cvt_pk_bf16_f32 v245,v114,v115
v_cvt_pk_bf16_f32 v246,v116,v117
v_cvt_pk_bf16_f32 v247,v118,v119
v_permlane32_swap_b32 v96,v97
v_max_f32 v96,v96,v97
v_mov_b32 v97,v96
v_mfma_f32_16x16x32_bf16 v[112:115],a[128:131],v[228:231],0
v_mul_f32 v248,s71,v248
v_mul_f32 v249,s71,v249
v_mul_f32 v250,s71,v250
v_mul_f32 v251,s71,v251
v_mul_f32 v252,s71,v252
v_mul_f32 v253,s71,v253
v_mul_f32 v254,s71,v254
v_mul_f32 v255,s71,v255
v_cvt_pk_bf16_f32 v248,v248,v249
v_cvt_pk_bf16_f32 v249,v250,v251
v_cvt_pk_bf16_f32 v250,v252,v253
v_cvt_pk_bf16_f32 v251,v254,v255
v_mul_f32 v120,s71,v120
v_mul_f32 v121,s71,v121
v_mul_f32 v122,s71,v122
v_mul_f32 v123,s71,v123
v_mul_f32 v124,s71,v124
v_mul_f32 v125,s71,v125
v_mul_f32 v126,s71,v126
v_mul_f32 v127,s71,v127
v_cvt_pk_bf16_f32 v252,v120,v121
v_cvt_pk_bf16_f32 v253,v122,v123
v_cvt_pk_bf16_f32 v254,v124,v125
v_cvt_pk_bf16_f32 v255,v126,v127
v_mfma_f32_16x16x32_bf16 v[112:115],a[132:135],v[236:239],v[112:115]
global_load_lds_dwordx4 v4,s[10:11] offset:2048
v_permlane16_swap_b32 v96,v97
v_max_f32 v96,v96,v97
v_sub_f32 v43,0,v96
v_sub_f32 v42,0,v96
v_sub_f32 v41,0,v96
v_sub_f32 v40,0,v96
v_mfma_f32_16x16x32_bf16 v[112:115],a[136:139],v[244:247],v[112:115]
ds_read_b64_tr_b16 a[192:193],v2
ds_read_b64_tr_b16 a[194:195],v2 offset:4096
v_mfma_f32_16x16x32_bf16 v[112:115],a[140:143],v[252:255],v[112:115]
ds_read_b64_tr_b16 a[196:197],v2 offset:8192
ds_read_b64_tr_b16 a[198:199],v2 offset:12288
v_mfma_f32_16x16x32_bf16 v[96:99],a[128:131],v[196:199],v[40:43]
ds_read_b64_tr_b16 a[200:201],v3
ds_read_b64_tr_b16 a[202:203],v3 offset:4096
v_mfma_f32_16x16x32_bf16 v[96:99],a[132:135],v[204:207],v[96:99]
ds_read_b64_tr_b16 a[204:205],v3 offset:8192
ds_read_b64_tr_b16 a[206:207],v3 offset:12288
v_mfma_f32_16x16x32_bf16 v[96:99],a[136:139],v[212:215],v[96:99]
ds_read_b64_tr_b16 a[208:209],v2 offset:256
v_max3_f32 v112,v112,v113,v114
v_mfma_f32_16x16x32_bf16 v[96:99],a[140:143],v[220:223],v[96:99]
ds_read_b64_tr_b16 a[210:211],v2 offset:4352
ds_read_b64_tr_b16 a[212:213],v2 offset:8448
v_mfma_f32_16x16x32_bf16 v[100:103],a[156:159],v[220:223],v[40:43]
ds_read_b64_tr_b16 a[214:215],v2 offset:12544
v_max_f32 v112,v112,v115
v_mfma_f32_16x16x32_bf16 v[100:103],a[152:155],v[212:215],v[100:103]
ds_read_b64_tr_b16 a[216:217],v3 offset:256
ds_read_b64_tr_b16 a[218:219],v3 offset:4352
v_mfma_f32_16x16x32_bf16 v[100:103],a[148:151],v[204:207],v[100:103]
v_exp_f32 v64,v64
v_mfma_f32_16x16x32_bf16 v[100:103],a[144:147],v[196:199],v[100:103]
v_exp_f32 v65,v65
v_mfma_f32_16x16x32_bf16 v[104:107],a[160:163],v[196:199],v[40:43]
v_exp_f32 v66,v66
v_mfma_f32_16x16x32_bf16 v[104:107],a[164:167],v[204:207],v[104:107]
v_exp_f32 v67,v67
v_mfma_f32_16x16x32_bf16 v[104:107],a[168:171],v[212:215],v[104:107]
v_exp_f32 v68,v68
v_mfma_f32_16x16x32_bf16 v[104:107],a[172:175],v[220:223],v[104:107]
global_load_lds_dwordx4 v5,s[10:11] offset:3072
ds_read_b64_tr_b16 a[220:221],v3 offset:8448
v_mfma_f32_16x16x32_bf16 v[108:111],a[188:191],v[220:223],v[40:43]
v_exp_f32 v69,v69
v_exp_f32 v70,v70
v_exp_f32 v71,v71
v_mfma_f32_16x16x32_bf16 v[108:111],a[184:187],v[212:215],v[108:111]
ds_read_b64_tr_b16 a[222:223],v3 offset:12544
v_cvt_pk_bf16_f32 v64,v64,v65
v_cvt_pk_bf16_f32 v65,v66,v67
v_cvt_pk_bf16_f32 v66,v68,v69
v_cvt_pk_bf16_f32 v67,v70,v71
v_exp_f32 v72,v72
v_exp_f32 v73,v73
v_exp_f32 v74,v74
v_exp_f32 v75,v75
v_exp_f32 v76,v76
v_exp_f32 v77,v77
v_exp_f32 v78,v78
v_exp_f32 v79,v79
v_mfma_f32_16x16x32_bf16 v[108:111],a[180:183],v[204:207],v[108:111]
)A"
R"A(ds_read_b64_tr_b16 a[224:225],v2 offset:512
ds_read_b64_tr_b16 a[226:227],v2 offset:4608
v_cvt_pk_bf16_f32 v72,v72,v73
v_cvt_pk_bf16_f32 v73,v74,v75
v_cvt_pk_bf16_f32 v74,v76,v77
v_cvt_pk_bf16_f32 v75,v78,v79
v_mfma_f32_16x16x32_bf16 v[108:111],a[176:179],v[196:199],v[108:111]
s_waitcnt lgkmcnt(4)
ds_read_b64_tr_b16 a[228:229],v2 offset:8704
ds_read_b64_tr_b16 a[230:231],v2 offset:12800
v_mfma_f32_16x16x32_bf16 a[0:3],a[192:195],v[64:67],0
ds_read_b64_tr_b16 a[232:233],v3 offset:512
ds_read_b64_tr_b16 a[234:235],v3 offset:4608
v_mfma_f32_16x16x32_bf16 a[0:3],a[196:199],v[72:75],a[0:3]
ds_read_b64_tr_b16 a[236:237],v3 offset:8704
ds_read_b64_tr_b16 a[238:239],v3 offset:12800
v_mfma_f32_16x16x32_bf16 a[4:7],a[200:203],v[64:67],0
ds_read_b64_tr_b16 a[240:241],v2 offset:768
v_mov_b32 v113,v112
v_mfma_f32_16x16x32_bf16 a[4:7],a[204:207],v[72:75],a[4:7]
ds_read_b64_tr_b16 a[242:243],v2 offset:4864
ds_read_b64_tr_b16 a[244:245],v2 offset:8960
v_mfma_f32_16x16x32_bf16 a[8:11],a[208:211],v[64:67],0
ds_read_b64_tr_b16 a[246:247],v2 offset:13056
v_add_u32 v1,s78,v1
v_mfma_f32_16x16x32_bf16 a[8:11],a[212:215],v[72:75],a[8:11]
ds_read_b64_tr_b16 a[248:249],v3 offset:768
ds_read_b64_tr_b16 a[250:251],v3 offset:4864
v_mfma_f32_16x16x32_bf16 a[12:15],a[216:219],v[64:67],0
s_waitcnt lgkmcnt(12)
ds_read_b64_tr_b16 a[252:253],v3 offset:8960
v_add_u32 v2,s78,v2
v_mfma_f32_16x16x32_bf16 a[12:15],a[220:223],v[72:75],a[12:15]
s_waitcnt lgkmcnt(6)
ds_read_b64_tr_b16 a[254:255],v3 offset:13056
v_add_u32 v3,s78,v3
v_mfma_f32_16x16x32_bf16 a[16:19],a[224:227],v[64:67],0
s_sub_u32 s78,s77,32768
v_permlane32_swap_b32 v112,v113
v_mfma_f32_16x16x32_bf16 a[16:19],a[228:231],v[72:75],a[16:19]
s_and_b32 s78,s78,0x1ffff
v_max_f32 v112,v112,v113
v_mov_b32 v113,v112
v_mfma_f32_16x16x32_bf16 a[20:23],a[232:235],v[64:67],0
s_sub_u32 s78,s78,s77
v_exp_f32 v80,v80
v_mfma_f32_16x16x32_bf16 a[20:23],a[236:239],v[72:75],a[20:23]
s_add_u32 s77,s77,s78
v_permlane16_swap_b32 v112,v113
s_waitcnt lgkmcnt(5)
v_mfma_f32_16x16x32_bf16 a[24:27],a[240:243],v[64:67],0
s_waitcnt lgkmcnt(4)
v_max_f32 v112,v112,v113
v_max_f32 v112,s72,v112
v_mfma_f32_16x16x32_bf16 a[24:27],a[244:247],v[72:75],a[24:27]
s_waitcnt lgkmcnt(2)
v_sub_f32 v47,0,v112
v_sub_f32 v46,0,v112
v_mfma_f32_16x16x32_bf16 a[28:31],a[248:251],v[64:67],0
s_waitcnt lgkmcnt(0)
v_sub_f32 v45,0,v112
v_sub_f32 v44,0,v112
v_mfma_f32_16x16x32_bf16 a[28:31],a[252:255],v[72:75],a[28:31]
v_exp_f32 v81,v81
v_mfma_f32_16x16x32_bf16 v[48:51],v[28:31],v[64:67],0
v_exp_f32 v82,v82
v_mfma_f32_16x16x32_bf16 v[48:51],v[28:31],v[72:75],v[48:51]
v_exp_f32 v83,v83
s_waitcnt vmcnt(8)
s_barrier
v_mfma_f32_16x16x32_bf16 v[112:115],a[128:131],v[228:231],v[44:47]
s_add_u32 s10,s20,s74
s_addc_u32 s11,s21,0
s_add_u32 m0,s76,0
v_mfma_f32_16x16x32_bf16 v[112:115],a[132:135],v[236:239],v[112:115]
v_exp_f32 v84,v84
v_mfma_f32_16x16x32_bf16 v[112:115],a[136:139],v[244:247],v[112:115]
global_load_lds_dwordx4 v4,s[10:11] nt
v_cvt_pk_bf16_f32 v80,v80,v81
v_mfma_f32_16x16x32_bf16 v[112:115],a[140:143],v[252:255],v[112:115]
v_exp_f32 v85,v85
v_mfma_f32_16x16x32_bf16 v[116:119],a[156:159],v[252:255],v[44:47]
v_exp_f32 v86,v86
v_mfma_f32_16x16x32_bf16 v[116:119],a[152:155],v[244:247],v[116:119]
v_exp_f32 v87,v87
v_mfma_f32_16x16x32_bf16 v[116:119],a[148:151],v[236:239],v[116:119]
v_cvt_pk_bf16_f32 v81,v82,v83
v_cvt_pk_bf16_f32 v82,v84,v85
v_mfma_f32_16x16x32_bf16 v[116:119],a[144:147],v[228:231],v[116:119]
ds_read_b128 a[128:131],v1
v_cvt_pk_bf16_f32 v83,v86,v87
v_mfma_f32_16x16x32_bf16 v[120:123],a[160:163],v[228:231],v[44:47]
v_exp_f32 v88,v88
v_mfma_f32_16x16x32_bf16 v[120:123],a[164:167],v[236:239],v[120:123]
v_exp_f32 v89,v89
v_mfma_f32_16x16x32_bf16 v[120:123],a[168:171],v[244:247],v[120:123]
v_exp_f32 v90,v90
v_mfma_f32_16x16x32_bf16 v[120:123],a[172:175],v[252:255],v[120:123]
v_exp_f32 v91,v91
v_mfma_f32_16x16x32_bf16 v[124:127],a[188:191],v[252:255],v[44:47]
v_exp_f32 v92,v92
v_mfma_f32_16x16x32_bf16 v[124:127],a[184:187],v[244:247],v[124:127]
v_exp_f32 v93,v93
v_exp_f32 v94,v94
v_exp_f32 v95,v95
v_mfma_f32_16x16x32_bf16 v[124:127],a[180:183],v[236:239],v[124:127]
v_cvt_pk_bf16_f32 v88,v88,v89
v_cvt_pk_bf16_f32 v89,v90,v91
v_cvt_pk_bf16_f32 v90,v92,v93
v_cvt_pk_bf16_f32 v91,v94,v95
v_mfma_f32_16x16x32_bf16 v[124:127],a[176:179],v[228:231],v[124:127]
v_exp_f32 v96,v96
v_mfma_f32_16x16x32_bf16 a[32:35],a[192:195],v[80:83],0
global_load_lds_dwordx4 v4,s[10:11] offset:1024 nt
ds_read_b128 a[132:135],v1 offset:256
v_mfma_f32_16x16x32_bf16 a[32:35],a[196:199],v[88:91],a[32:35]
v_exp_f32 v97,v97
v_mfma_f32_16x16x32_bf16 a[36:39],a[200:203],v[80:83],0
ds_read_b128 a[136:139],v1 offset:512
v_cvt_pk_bf16_f32 v96,v96,v97
v_mfma_f32_16x16x32_bf16 a[36:39],a[204:207],v[88:91],a[36:39]
v_exp_f32 v98,v98
v_mfma_f32_16x16x32_bf16 a[40:43],a[208:211],v[80:83],0
v_exp_f32 v99,v99
v_mfma_f32_16x16x32_bf16 a[40:43],a[212:215],v[88:91],a[40:43]
ds_read_b128 a[140:143],v1 offset:768
v_cvt_pk_bf16_f32 v97,v98,v99
v_mfma_f32_16x16x32_bf16 a[44:47],a[216:219],v[80:83],0
v_exp_f32 v100,v100
v_mfma_f32_16x16x32_bf16 a[44:47],a[220:223],v[88:91],a[44:47]
v_exp_f32 v101,v101
v_mfma_f32_16x16x32_bf16 a[48:51],a[224:227],v[80:83],0
ds_read_b128 a[156:159],v1 offset:4864
v_cvt_pk_bf16_f32 v98,v100,v101
v_mfma_f32_16x16x32_bf16 a[48:51],a[228:231],v[88:91],a[48:51]
v_exp_f32 v102,v102
v_mfma_f32_16x16x32_bf16 a[52:55],a[232:235],v[80:83],0
v_exp_f32 v103,v103
v_mfma_f32_16x16x32_bf16 a[52:55],a[236:239],v[88:91],a[52:55]
ds_read_b128 a[152:155],v1 offset:4608
v_cvt_pk_bf16_f32 v99,v102,v103
v_mfma_f32_16x16x32_bf16 a[56:59],a[240:243],v[80:83],0
ds_read_b128 a[148:151],v1 offset:4352
v_mfma_f32_16x16x32_bf16 a[56:59],a[244:247],v[88:91],a[56:59]
ds_read_b128 a[144:147],v1 offset:4096
v_mfma_f32_16x16x32_bf16 a[60:63],a[248:251],v[80:83],0
global_load_lds_dwordx4 v5,s[10:11] offset:2048 nt
ds_read_b128 a[160:163],v1 offset:8192
v_mfma_f32_16x16x32_bf16 a[60:63],a[252:255],v[88:91],a[60:63]
v_exp_f32 v104,v104
v_mfma_f32_16x16x32_bf16 v[52:55],v[28:31],v[80:83],0
ds_read_b128 a[164:167],v1 offset:8448
v_mfma_f32_16x16x32_bf16 v[52:55],v[28:31],v[88:91],v[52:55]
s_waitcnt lgkmcnt(5)
ds_read_b128 a[168:171],v1 offset:8704
v_mfma_f32_16x16x32_bf16 v[64:67],a[128:131],v[132:135],v[32:35]
ds_read_b128 a[172:175],v1 offset:8960
v_mfma_f32_16x16x32_bf16 v[64:67],a[132:135],v[140:143],v[64:67]
v_exp_f32 v105,v105
v_mfma_f32_16x16x32_bf16 v[64:67],a[136:139],v[148:151],v[64:67]
ds_read_b128 a[188:191],v1 offset:13056
v_cvt_pk_bf16_f32 v104,v104,v105
v_mfma_f32_16x16x32_bf16 v[64:67],a[140:143],v[156:159],v[64:67]
ds_read_b128 a[184:187],v1 offset:12800
v_mfma_f32_16x16x32_bf16 v[68:71],a[156:159],v[156:159],v[32:35]
s_waitcnt lgkmcnt(5)
ds_read_b128 a[180:183],v1 offset:12544
v_mfma_f32_16x16x32_bf16 v[68:71],a[152:155],v[148:151],v[68:71]
v_exp_f32 v106,v106
v_mfma_f32_16x16x32_bf16 v[68:71],a[148:151],v[140:143],v[68:71]
ds_read_b128 a[176:179],v1 offset:12288
v_add_u32 v1,s78,v1
v_mfma_f32_16x16x32_bf16 v[68:71],a[144:147],v[132:135],v[68:71]
v_exp_f32 v107,v107
v_mfma_f32_16x16x32_bf16 v[72:75],a[160:163],v[132:135],v[32:35]
s_waitcnt lgkmcnt(4)
v_exp_f32 v108,v108
v_mfma_f32_16x16x32_bf16 v[72:75],a[164:167],v[140:143],v[72:75]
v_exp_f32 v109,v109
v_mfma_f32_16x16x32_bf16 v[72:75],a[168:171],v[148:151],v[72:75]
global_load_lds_dwordx4 v5,s[10:11] offset:3072
v_cvt_pk_bf16_f32 v105,v106,v107
v_mfma_f32_16x16x32_bf16 v[72:75],a[172:175],v[156:159],v[72:75]
s_waitcnt lgkmcnt(2)
v_exp_f32 v110,v110
v_mfma_f32_16x16x32_bf16 v[76:79],a[188:191],v[156:159],v[32:35]
v_exp_f32 v111,v111
v_mfma_f32_16x16x32_bf16 v[76:79],a[184:187],v[148:151],v[76:79]
s_waitcnt lgkmcnt(1)
v_cvt_pk_bf16_f32 v106,v108,v109
v_cvt_pk_bf16_f32 v107,v110,v111
v_mfma_f32_16x16x32_bf16 v[76:79],a[180:183],v[140:143],v[76:79]
s_waitcnt lgkmcnt(0)
v_exp_f32 v112,v112
v_mfma_f32_16x16x32_bf16 v[76:79],a[176:179],v[132:135],v[76:79]
v_exp_f32 v113,v113
v_mfma_f32_16x16x32_bf16 a[64:67],a[192:195],v[96:99],0
v_exp_f32 v114,v114
v_mfma_f32_16x16x32_bf16 a[64:67],a[196:199],v[104:107],a[64:67]
v_exp_f32 v115,v115
v_mfma_f32_16x16x32_bf16 a[68:71],a[200:203],v[96:99],0
v_exp_f32 v116,v116
v_mfma_f32_16x16x32_bf16 a[68:71],a[204:207],v[104:107],a[68:71]
v_exp_f32 v117,v117
v_mfma_f32_16x16x32_bf16 a[72:75],a[208:211],v[96:99],0
s_add_u32 s10,s24,s74
s_addc_u32 s11,s25,0
s_add_u32 s74,s74,16384
s_and_b32 s74,s74,0x3ffff
s_or_b32 s74,s74,s69
s_lshl_b32 s69,s60,13
s_add_u32 s69,s69,131072
v_add_u32 v8,s69,v6
v_mfma_f32_16x16x32_bf16 a[72:75],a[212:215],v[104:107],a[72:75]
s_lshl_b32 s69,s60,13
ds_write_b128 v8,v[24:27] offset:4096
v_mfma_f32_16x16x32_bf16 a[76:79],a[216:219],v[96:99],0
s_add_u32 m0,s76,16384
ds_write_b32 v8,v7 offset:5120
v_cvt_pk_bf16_f32 v112,v112,v113
v_mfma_f32_16x16x32_bf16 a[76:79],a[220:223],v[104:107],a[76:79]
s_sub_u32 s76,s76,32768
v_bfe_u32 v24,v6,4,4
v_cvt_pk_bf16_f32 v113,v114,v115
v_mfma_f32_16x16x32_bf16 a[80:83],a[224:227],v[96:99],0
global_load_lds_dwordx4 v4,s[10:11]
v_lshrrev_b32 v7,8,v6
v_lshlrev_b32 v7,4,v7
v_lshl_add_u32 v7,v24,8,v7
v_mfma_f32_16x16x32_bf16 a[80:83],a[228:231],v[104:107],a[80:83]
s_and_b32 s76,s76,0x1ffff
v_exp_f32 v118,v118
v_mfma_f32_16x16x32_bf16 a[84:87],a[232:235],v[96:99],0
s_add_u32 s69,s69,131072
v_exp_f32 v119,v119
v_mfma_f32_16x16x32_bf16 a[84:87],a[236:239],v[104:107],a[84:87]
v_cvt_pk_bf16_f32 v114,v116,v117
v_cvt_pk_bf16_f32 v115,v118,v119
v_mfma_f32_16x16x32_bf16 a[88:91],a[240:243],v[96:99],0
v_exp_f32 v120,v120
v_mfma_f32_16x16x32_bf16 a[88:91],a[244:247],v[104:107],a[88:91]
buffer_load_dwordx4 v[8:11],v7,s[20:23],s65 offen nt
v_mfma_f32_16x16x32_bf16 a[92:95],a[248:251],v[96:99],0
v_exp_f32 v121,v121
v_mfma_f32_16x16x32_bf16 a[92:95],a[252:255],v[104:107],a[92:95]
v_exp_f32 v122,v122
v_mfma_f32_16x16x32_bf16 v[56:59],v[28:31],v[96:99],0
v_exp_f32 v123,v123
v_mfma_f32_16x16x32_bf16 v[56:59],v[28:31],v[104:107],v[56:59]
v_exp_f32 v124,v124
v_mfma_f32_16x16x32_bf16 v[80:83],a[128:131],v[164:167],v[36:39]
buffer_load_dwordx4 v[12:15],v7,s[20:23],s65 offen offset:64 nt
v_mfma_f32_16x16x32_bf16 v[80:83],a[132:135],v[172:175],v[80:83]
v_exp_f32 v125,v125
v_mfma_f32_16x16x32_bf16 v[80:83],a[136:139],v[180:183],v[80:83]
v_exp_f32 v126,v126
v_mfma_f32_16x16x32_bf16 v[80:83],a[140:143],v[188:191],v[80:83]
v_exp_f32 v127,v127
v_mfma_f32_16x16x32_bf16 v[84:87],a[156:159],v[188:191],v[36:39]
v_cvt_pk_bf16_f32 v120,v120,v121
v_cvt_pk_bf16_f32 v121,v122,v123
v_mfma_f32_16x16x32_bf16 v[84:87],a[152:155],v[180:183],v[84:87]
buffer_load_dwordx4 v[16:19],v7,s[20:23],s65 offen offset:128 nt
v_mfma_f32_16x16x32_bf16 v[84:87],a[148:151],v[172:175],v[84:87]
v_cvt_pk_bf16_f32 v122,v124,v125
v_cvt_pk_bf16_f32 v123,v126,v127
v_mfma_f32_16x16x32_bf16 v[84:87],a[144:147],v[164:167],v[84:87]
v_exp_f32 v64,v64
v_mfma_f32_16x16x32_bf16 v[88:91],a[160:163],v[164:167],v[36:39]
v_exp_f32 v65,v65
v_mfma_f32_16x16x32_bf16 v[88:91],a[164:167],v[172:175],v[88:91]
v_exp_f32 v66,v66
v_mfma_f32_16x16x32_bf16 v[88:91],a[168:171],v[180:183],v[88:91]
v_exp_f32 v67,v67
v_mfma_f32_16x16x32_bf16 v[88:91],a[172:175],v[188:191],v[88:91]
global_load_lds_dwordx4 v5,s[10:11] offset:1024
v_cvt_pk_bf16_f32 v64,v64,v65
v_mfma_f32_16x16x32_bf16 v[92:95],a[188:191],v[188:191],v[36:39]
v_exp_f32 v68,v68
v_mfma_f32_16x16x32_bf16 v[92:95],a[184:187],v[180:183],v[92:95]
v_exp_f32 v69,v69
v_mfma_f32_16x16x32_bf16 v[92:95],a[180:183],v[172:175],v[92:95]
v_exp_f32 v70,v70
v_mfma_f32_16x16x32_bf16 v[92:95],a[176:179],v[164:167],v[92:95]
v_exp_f32 v71,v71
v_mfma_f32_16x16x32_bf16 a[96:99],a[192:195],v[112:115],0
buffer_load_dwordx4 v[20:23],v7,s[20:23],s65 offen offset:192 nt
v_mfma_f32_16x16x32_bf16 a[96:99],a[196:199],v[120:123],a[96:99]
ds_read_b64_tr_b16 a[192:193],v2
ds_read_b64_tr_b16 a[194:195],v2 offset:4096
v_mfma_f32_16x16x32_bf16 a[100:103],a[200:203],v[112:115],0
ds_read_b64_tr_b16 a[196:197],v2 offset:8192
ds_read_b64_tr_b16 a[198:199],v2 offset:12288
v_mfma_f32_16x16x32_bf16 a[100:103],a[204:207],v[120:123],a[100:103]
ds_read_b64_tr_b16 a[200:201],v3
ds_read_b64_tr_b16 a[202:203],v3 offset:4096
)A"
R"A(v_mfma_f32_16x16x32_bf16 a[104:107],a[208:211],v[112:115],0
ds_read_b64_tr_b16 a[204:205],v3 offset:8192
ds_read_b64_tr_b16 a[206:207],v3 offset:12288
v_mfma_f32_16x16x32_bf16 a[104:107],a[212:215],v[120:123],a[104:107]
ds_read_b64_tr_b16 a[208:209],v2 offset:256
v_cvt_pk_bf16_f32 v65,v66,v67
v_mfma_f32_16x16x32_bf16 a[108:111],a[216:219],v[112:115],0
global_load_lds_dwordx4 v4,s[10:11] offset:2048
ds_read_b64_tr_b16 a[210:211],v2 offset:4352
v_mfma_f32_16x16x32_bf16 a[108:111],a[220:223],v[120:123],a[108:111]
ds_read_b64_tr_b16 a[212:213],v2 offset:8448
ds_read_b64_tr_b16 a[214:215],v2 offset:12544
v_mfma_f32_16x16x32_bf16 a[112:115],a[224:227],v[112:115],0
ds_read_b64_tr_b16 a[216:217],v3 offset:256
ds_read_b64_tr_b16 a[218:219],v3 offset:4352
v_mfma_f32_16x16x32_bf16 a[112:115],a[228:231],v[120:123],a[112:115]
v_cvt_pk_bf16_f32 v66,v68,v69
v_cvt_pk_bf16_f32 v67,v70,v71
v_mfma_f32_16x16x32_bf16 a[116:119],a[232:235],v[112:115],0
v_exp_f32 v72,v72
v_mfma_f32_16x16x32_bf16 a[116:119],a[236:239],v[120:123],a[116:119]
v_exp_f32 v73,v73
v_mfma_f32_16x16x32_bf16 a[120:123],a[240:243],v[112:115],0
v_exp_f32 v74,v74
v_mfma_f32_16x16x32_bf16 a[120:123],a[244:247],v[120:123],a[120:123]
v_exp_f32 v75,v75
v_mfma_f32_16x16x32_bf16 a[124:127],a[248:251],v[112:115],0
v_exp_f32 v76,v76
v_mfma_f32_16x16x32_bf16 a[124:127],a[252:255],v[120:123],a[124:127]
v_exp_f32 v77,v77
v_mfma_f32_16x16x32_bf16 v[60:63],v[28:31],v[112:115],0
v_exp_f32 v78,v78
v_mfma_f32_16x16x32_bf16 v[60:63],v[28:31],v[120:123],v[60:63]
global_load_lds_dwordx4 v5,s[10:11] offset:3072
ds_read_b64_tr_b16 a[220:221],v3 offset:8448
v_mfma_f32_16x16x32_bf16 v[96:99],a[128:131],v[196:199],v[40:43]
v_exp_f32 v79,v79
v_mfma_f32_16x16x32_bf16 v[96:99],a[132:135],v[204:207],v[96:99]
ds_read_b64_tr_b16 a[222:223],v3 offset:12544
v_cvt_pk_bf16_f32 v72,v72,v73
v_mfma_f32_16x16x32_bf16 v[96:99],a[136:139],v[212:215],v[96:99]
v_cvt_pk_bf16_f32 v73,v74,v75
v_cvt_pk_bf16_f32 v74,v76,v77
v_mfma_f32_16x16x32_bf16 v[96:99],a[140:143],v[220:223],v[96:99]
ds_read_b64_tr_b16 a[224:225],v2 offset:512
v_cvt_pk_bf16_f32 v75,v78,v79
v_mfma_f32_16x16x32_bf16 v[100:103],a[156:159],v[220:223],v[40:43]
ds_read_b64_tr_b16 a[226:227],v2 offset:4608
ds_read_b64_tr_b16 a[228:229],v2 offset:8704
v_mfma_f32_16x16x32_bf16 v[100:103],a[152:155],v[212:215],v[100:103]
ds_read_b64_tr_b16 a[230:231],v2 offset:12800
ds_read_b64_tr_b16 a[232:233],v3 offset:512
v_mfma_f32_16x16x32_bf16 v[100:103],a[148:151],v[204:207],v[100:103]
ds_read_b64_tr_b16 a[234:235],v3 offset:4608
ds_read_b64_tr_b16 a[236:237],v3 offset:8704
v_mfma_f32_16x16x32_bf16 v[100:103],a[144:147],v[196:199],v[100:103]
ds_read_b64_tr_b16 a[238:239],v3 offset:12800
ds_read_b64_tr_b16 a[240:241],v2 offset:768
v_mfma_f32_16x16x32_bf16 v[104:107],a[160:163],v[196:199],v[40:43]
ds_read_b64_tr_b16 a[242:243],v2 offset:4864
v_add_u32 v7,s69,v6
v_mfma_f32_16x16x32_bf16 v[104:107],a[164:167],v[204:207],v[104:107]
s_add_u32 s10,s20,s74
s_addc_u32 s11,s21,0
ds_read_b64_tr_b16 a[244:245],v2 offset:8960
v_mfma_f32_16x16x32_bf16 v[104:107],a[168:171],v[212:215],v[104:107]
s_and_b32 s69,s75,0xfffc0000
ds_read_b64_tr_b16 a[246:247],v2 offset:13056
ds_read_b64_tr_b16 a[248:249],v3 offset:768
v_mfma_f32_16x16x32_bf16 v[104:107],a[172:175],v[220:223],v[104:107]
s_add_u32 m0,s76,0
ds_read_b64_tr_b16 a[250:251],v3 offset:4864
ds_read_b64_tr_b16 a[252:253],v3 offset:8960
v_mfma_f32_16x16x32_bf16 v[108:111],a[188:191],v[220:223],v[40:43]
s_add_u32 s75,s75,16384
ds_read_b64_tr_b16 a[254:255],v3 offset:13056
v_add_u32 v2,s78,v2
v_mfma_f32_16x16x32_bf16 v[108:111],a[184:187],v[212:215],v[108:111]
global_load_lds_dwordx4 v4,s[10:11] nt
v_add_u32 v3,s78,v3
v_mfma_f32_16x16x32_bf16 v[108:111],a[180:183],v[204:207],v[108:111]
s_sub_u32 s78,s77,32768
v_exp_f32 v80,v80
v_mfma_f32_16x16x32_bf16 v[108:111],a[176:179],v[196:199],v[108:111]
s_and_b32 s78,s78,0x1ffff
v_exp_f32 v81,v81
s_waitcnt lgkmcnt(7)
v_mfma_f32_16x16x32_bf16 a[0:3],a[192:195],v[64:67],a[0:3]
s_sub_u32 s78,s78,s77
v_exp_f32 v82,v82
v_mfma_f32_16x16x32_bf16 a[0:3],a[196:199],v[72:75],a[0:3]
s_add_u32 s77,s77,s78
v_exp_f32 v83,v83
v_mfma_f32_16x16x32_bf16 a[4:7],a[200:203],v[64:67],a[4:7]
v_exp_f32 v84,v84
v_mfma_f32_16x16x32_bf16 a[4:7],a[204:207],v[72:75],a[4:7]
v_exp_f32 v85,v85
v_mfma_f32_16x16x32_bf16 a[8:11],a[208:211],v[64:67],a[8:11]
v_exp_f32 v86,v86
v_mfma_f32_16x16x32_bf16 a[8:11],a[212:215],v[72:75],a[8:11]
v_exp_f32 v87,v87
v_mfma_f32_16x16x32_bf16 a[12:15],a[216:219],v[64:67],a[12:15]
v_cvt_pk_bf16_f32 v80,v80,v81
v_cvt_pk_bf16_f32 v81,v82,v83
v_mfma_f32_16x16x32_bf16 a[12:15],a[220:223],v[72:75],a[12:15]
v_cvt_pk_bf16_f32 v82,v84,v85
v_cvt_pk_bf16_f32 v83,v86,v87
s_nop 0
L_pd5_%=:
s_cbranch_execz L_trt0_%=
v_mfma_f32_16x16x32_bf16 a[16:19],a[224:227],v[64:67],a[16:19]
v_exp_f32 v88,v88
v_mfma_f32_16x16x32_bf16 a[16:19],a[228:231],v[72:75],a[16:19]
v_exp_f32 v89,v89
v_mfma_f32_16x16x32_bf16 a[20:23],a[232:235],v[64:67],a[20:23]
v_exp_f32 v90,v90
v_mfma_f32_16x16x32_bf16 a[20:23],a[236:239],v[72:75],a[20:23]
s_waitcnt lgkmcnt(0)
v_exp_f32 v91,v91
v_mfma_f32_16x16x32_bf16 a[24:27],a[240:243],v[64:67],a[24:27]
v_exp_f32 v92,v92
v_mfma_f32_16x16x32_bf16 a[24:27],a[244:247],v[72:75],a[24:27]
v_exp_f32 v93,v93
v_mfma_f32_16x16x32_bf16 a[28:31],a[248:251],v[64:67],a[28:31]
v_exp_f32 v94,v94
v_mfma_f32_16x16x32_bf16 a[28:31],a[252:255],v[72:75],a[28:31]
s_getpc_b64 s[92:93]
s_cmp_lg_u32 s4,0
s_cbranch_scc1 L_pace_%=
v_mfma_f32_16x16x32_bf16 v[48:51],v[28:31],v[64:67],v[48:51]
v_exp_f32 v95,v95
v_mfma_f32_16x16x32_bf16 v[48:51],v[28:31],v[72:75],v[48:51]
s_add_u32 s10,s20,s74
s_addc_u32 s11,s21,0
v_cvt_pk_bf16_f32 v88,v88,v89
s_waitcnt vmcnt(13)
s_barrier
v_mfma_f32_16x16x32_bf16 v[112:115],a[128:131],v[228:231],v[44:47]
s_add_u32 m0,s76,0
v_cvt_pk_bf16_f32 v89,v90,v91
v_cvt_pk_bf16_f32 v90,v92,v93
v_mfma_f32_16x16x32_bf16 v[112:115],a[132:135],v[236:239],v[112:115]
ds_read_b128 a[128:131],v1
v_cvt_pk_bf16_f32 v91,v94,v95
v_mfma_f32_16x16x32_bf16 v[112:115],a[136:139],v[244:247],v[112:115]
global_load_lds_dwordx4 v4,s[10:11] offset:1024 nt
ds_read_b128 a[132:135],v1 offset:256
v_mfma_f32_16x16x32_bf16 v[112:115],a[140:143],v[252:255],v[112:115]
v_exp_f32 v96,v96
v_mfma_f32_16x16x32_bf16 v[116:119],a[156:159],v[252:255],v[44:47]
v_exp_f32 v97,v97
v_mfma_f32_16x16x32_bf16 v[116:119],a[152:155],v[244:247],v[116:119]
ds_read_b128 a[136:139],v1 offset:512
v_cvt_pk_bf16_f32 v96,v96,v97
v_mfma_f32_16x16x32_bf16 v[116:119],a[148:151],v[236:239],v[116:119]
v_exp_f32 v98,v98
v_mfma_f32_16x16x32_bf16 v[116:119],a[144:147],v[228:231],v[116:119]
v_exp_f32 v99,v99
v_mfma_f32_16x16x32_bf16 v[120:123],a[160:163],v[228:231],v[44:47]
ds_read_b128 a[140:143],v1 offset:768
v_cvt_pk_bf16_f32 v97,v98,v99
v_mfma_f32_16x16x32_bf16 v[120:123],a[164:167],v[236:239],v[120:123]
v_exp_f32 v100,v100
v_mfma_f32_16x16x32_bf16 v[120:123],a[168:171],v[244:247],v[120:123]
v_exp_f32 v101,v101
v_mfma_f32_16x16x32_bf16 v[120:123],a[172:175],v[252:255],v[120:123]
ds_read_b128 a[156:159],v1 offset:4864
v_cvt_pk_bf16_f32 v98,v100,v101
v_mfma_f32_16x16x32_bf16 v[124:127],a[188:191],v[252:255],v[44:47]
v_exp_f32 v102,v102
v_mfma_f32_16x16x32_bf16 v[124:127],a[184:187],v[244:247],v[124:127]
v_exp_f32 v103,v103
v_mfma_f32_16x16x32_bf16 v[124:127],a[180:183],v[236:239],v[124:127]
ds_read_b128 a[152:155],v1 offset:4608
v_cvt_pk_bf16_f32 v99,v102,v103
v_mfma_f32_16x16x32_bf16 v[124:127],a[176:179],v[228:231],v[124:127]
v_exp_f32 v104,v104
v_mfma_f32_16x16x32_bf16 a[32:35],a[192:195],v[80:83],a[32:35]
global_load_lds_dwordx4 v5,s[10:11] offset:2048 nt
ds_read_b128 a[148:151],v1 offset:4352
v_mfma_f32_16x16x32_bf16 a[32:35],a[196:199],v[88:91],a[32:35]
v_exp_f32 v105,v105
v_mfma_f32_16x16x32_bf16 a[36:39],a[200:203],v[80:83],a[36:39]
ds_read_b128 a[144:147],v1 offset:4096
v_cvt_pk_bf16_f32 v104,v104,v105
v_mfma_f32_16x16x32_bf16 a[36:39],a[204:207],v[88:91],a[36:39]
v_exp_f32 v106,v106
v_mfma_f32_16x16x32_bf16 a[40:43],a[208:211],v[80:83],a[40:43]
v_exp_f32 v107,v107
v_mfma_f32_16x16x32_bf16 a[40:43],a[212:215],v[88:91],a[40:43]
ds_read_b128 a[160:163],v1 offset:8192
v_cvt_pk_bf16_f32 v105,v106,v107
v_mfma_f32_16x16x32_bf16 a[44:47],a[216:219],v[80:83],a[44:47]
v_exp_f32 v108,v108
v_mfma_f32_16x16x32_bf16 a[44:47],a[220:223],v[88:91],a[44:47]
v_exp_f32 v109,v109
v_mfma_f32_16x16x32_bf16 a[48:51],a[224:227],v[80:83],a[48:51]
ds_read_b128 a[164:167],v1 offset:8448
v_cvt_pk_bf16_f32 v106,v108,v109
v_mfma_f32_16x16x32_bf16 a[48:51],a[228:231],v[88:91],a[48:51]
v_exp_f32 v110,v110
v_mfma_f32_16x16x32_bf16 a[52:55],a[232:235],v[80:83],a[52:55]
v_exp_f32 v111,v111
v_mfma_f32_16x16x32_bf16 a[52:55],a[236:239],v[88:91],a[52:55]
ds_read_b128 a[168:171],v1 offset:8704
v_cvt_pk_bf16_f32 v107,v110,v111
v_mfma_f32_16x16x32_bf16 a[56:59],a[240:243],v[80:83],a[56:59]
v_exp_f32 v112,v112
v_mfma_f32_16x16x32_bf16 a[56:59],a[244:247],v[88:91],a[56:59]
v_exp_f32 v113,v113
v_mfma_f32_16x16x32_bf16 a[60:63],a[248:251],v[80:83],a[60:63]
global_load_lds_dwordx4 v5,s[10:11] offset:3072
ds_read_b128 a[172:175],v1 offset:8960
v_mfma_f32_16x16x32_bf16 a[60:63],a[252:255],v[88:91],a[60:63]
ds_read_b128 a[188:191],v1 offset:13056
v_cvt_pk_bf16_f32 v112,v112,v113
v_mfma_f32_16x16x32_bf16 v[52:55],v[28:31],v[80:83],v[52:55]
v_exp_f32 v114,v114
v_mfma_f32_16x16x32_bf16 v[52:55],v[28:31],v[88:91],v[52:55]
s_waitcnt lgkmcnt(3)
v_exp_f32 v115,v115
v_mfma_f32_16x16x32_bf16 v[64:67],a[128:131],v[132:135],v[32:35]
ds_read_b128 a[184:187],v1 offset:12800
v_cvt_pk_bf16_f32 v113,v114,v115
v_mfma_f32_16x16x32_bf16 v[64:67],a[132:135],v[140:143],v[64:67]
v_exp_f32 v116,v116
v_mfma_f32_16x16x32_bf16 v[64:67],a[136:139],v[148:151],v[64:67]
ds_read_b128 a[180:183],v1 offset:12544
v_mfma_f32_16x16x32_bf16 v[64:67],a[140:143],v[156:159],v[64:67]
ds_read_b128 a[176:179],v1 offset:12288
v_add_u32 v1,s78,v1
v_mfma_f32_16x16x32_bf16 v[68:71],a[156:159],v[156:159],v[32:35]
v_exp_f32 v117,v117
v_mfma_f32_16x16x32_bf16 v[68:71],a[152:155],v[148:151],v[68:71]
v_exp_f32 v118,v118
v_mfma_f32_16x16x32_bf16 v[68:71],a[148:151],v[140:143],v[68:71]
s_add_u32 s10,s24,s74
s_addc_u32 s11,s25,0
v_cvt_pk_bf16_f32 v114,v116,v117
v_mfma_f32_16x16x32_bf16 v[68:71],a[144:147],v[132:135],v[68:71]
s_add_u32 s74,s74,16384
v_exp_f32 v119,v119
v_mfma_f32_16x16x32_bf16 v[72:75],a[160:163],v[132:135],v[32:35]
s_add_u32 m0,s76,16384
v_exp_f32 v120,v120
v_mfma_f32_16x16x32_bf16 v[72:75],a[164:167],v[140:143],v[72:75]
s_and_b32 s74,s74,0x3ffff
v_exp_f32 v121,v121
s_waitcnt lgkmcnt(2)
v_mfma_f32_16x16x32_bf16 v[72:75],a[168:171],v[148:151],v[72:75]
global_load_lds_dwordx4 v4,s[10:11]
v_cvt_pk_bf16_f32 v115,v118,v119
v_mfma_f32_16x16x32_bf16 v[72:75],a[172:175],v[156:159],v[72:75]
s_or_b32 s74,s74,s69
v_exp_f32 v122,v122
v_mfma_f32_16x16x32_bf16 v[76:79],a[188:191],v[156:159],v[32:35]
s_sub_u32 s76,s76,32768
v_exp_f32 v123,v123
v_mfma_f32_16x16x32_bf16 v[76:79],a[184:187],v[148:151],v[76:79]
s_and_b32 s76,s76,0x1ffff
v_exp_f32 v124,v124
s_waitcnt lgkmcnt(0)
v_mfma_f32_16x16x32_bf16 v[76:79],a[180:183],v[140:143],v[76:79]
s_and_b32 s69,s75,0xfffc0000
v_exp_f32 v125,v125
v_mfma_f32_16x16x32_bf16 v[76:79],a[176:179],v[132:135],v[76:79]
v_exp_f32 v126,v126
v_mfma_f32_16x16x32_bf16 a[64:67],a[192:195],v[96:99],a[64:67]
v_exp_f32 v127,v127
v_mfma_f32_16x16x32_bf16 a[64:67],a[196:199],v[104:107],a[64:67]
v_cvt_pk_bf16_f32 v120,v120,v121
v_cvt_pk_bf16_f32 v121,v122,v123
v_mfma_f32_16x16x32_bf16 a[68:71],a[200:203],v[96:99],a[68:71]
v_cvt_pk_bf16_f32 v122,v124,v125
v_cvt_pk_bf16_f32 v123,v126,v127
v_mfma_f32_16x16x32_bf16 a[68:71],a[204:207],v[104:107],a[68:71]
v_exp_f32 v64,v64
v_mfma_f32_16x16x32_bf16 a[72:75],a[208:211],v[96:99],a[72:75]
v_exp_f32 v65,v65
v_mfma_f32_16x16x32_bf16 a[72:75],a[212:215],v[104:107],a[72:75]
v_exp_f32 v66,v66
v_mfma_f32_16x16x32_bf16 a[76:79],a[216:219],v[96:99],a[76:79]
v_exp_f32 v67,v67
v_mfma_f32_16x16x32_bf16 a[76:79],a[220:223],v[104:107],a[76:79]
v_exp_f32 v68,v68
v_mfma_f32_16x16x32_bf16 a[80:83],a[224:227],v[96:99],a[80:83]
global_load_lds_dwordx4 v5,s[10:11] offset:1024
)A"
R"A(v_cvt_pk_bf16_f32 v64,v64,v65
v_mfma_f32_16x16x32_bf16 a[80:83],a[228:231],v[104:107],a[80:83]
v_exp_f32 v69,v69
v_mfma_f32_16x16x32_bf16 a[84:87],a[232:235],v[96:99],a[84:87]
v_exp_f32 v70,v70
v_mfma_f32_16x16x32_bf16 a[84:87],a[236:239],v[104:107],a[84:87]
v_exp_f32 v71,v71
v_mfma_f32_16x16x32_bf16 a[88:91],a[240:243],v[96:99],a[88:91]
v_cvt_pk_bf16_f32 v65,v66,v67
v_cvt_pk_bf16_f32 v66,v68,v69
v_mfma_f32_16x16x32_bf16 a[88:91],a[244:247],v[104:107],a[88:91]
v_exp_f32 v72,v72
v_mfma_f32_16x16x32_bf16 a[92:95],a[248:251],v[96:99],a[92:95]
v_exp_f32 v73,v73
v_mfma_f32_16x16x32_bf16 a[92:95],a[252:255],v[104:107],a[92:95]
v_cvt_pk_bf16_f32 v67,v70,v71
v_cvt_pk_bf16_f32 v72,v72,v73
v_mfma_f32_16x16x32_bf16 v[56:59],v[28:31],v[96:99],v[56:59]
v_exp_f32 v74,v74
v_mfma_f32_16x16x32_bf16 v[56:59],v[28:31],v[104:107],v[56:59]
v_exp_f32 v75,v75
v_mfma_f32_16x16x32_bf16 v[80:83],a[128:131],v[164:167],v[36:39]
v_exp_f32 v76,v76
v_mfma_f32_16x16x32_bf16 v[80:83],a[132:135],v[172:175],v[80:83]
v_exp_f32 v77,v77
v_mfma_f32_16x16x32_bf16 v[80:83],a[136:139],v[180:183],v[80:83]
v_exp_f32 v78,v78
v_mfma_f32_16x16x32_bf16 v[80:83],a[140:143],v[188:191],v[80:83]
v_exp_f32 v79,v79
v_mfma_f32_16x16x32_bf16 v[84:87],a[156:159],v[188:191],v[36:39]
v_cvt_pk_bf16_f32 v73,v74,v75
v_cvt_pk_bf16_f32 v74,v76,v77
v_mfma_f32_16x16x32_bf16 v[84:87],a[152:155],v[180:183],v[84:87]
global_load_lds_dwordx4 v4,s[10:11] offset:2048
v_cvt_pk_bf16_f32 v75,v78,v79
v_mfma_f32_16x16x32_bf16 v[84:87],a[148:151],v[172:175],v[84:87]
v_exp_f32 v80,v80
v_mfma_f32_16x16x32_bf16 v[84:87],a[144:147],v[164:167],v[84:87]
v_exp_f32 v81,v81
v_mfma_f32_16x16x32_bf16 v[88:91],a[160:163],v[164:167],v[36:39]
v_exp_f32 v82,v82
v_mfma_f32_16x16x32_bf16 v[88:91],a[164:167],v[172:175],v[88:91]
v_exp_f32 v83,v83
v_mfma_f32_16x16x32_bf16 v[88:91],a[168:171],v[180:183],v[88:91]
v_cvt_pk_bf16_f32 v80,v80,v81
v_cvt_pk_bf16_f32 v81,v82,v83
v_mfma_f32_16x16x32_bf16 v[88:91],a[172:175],v[188:191],v[88:91]
v_exp_f32 v84,v84
v_mfma_f32_16x16x32_bf16 v[92:95],a[188:191],v[188:191],v[36:39]
v_exp_f32 v85,v85
v_mfma_f32_16x16x32_bf16 v[92:95],a[184:187],v[180:183],v[92:95]
v_exp_f32 v86,v86
v_mfma_f32_16x16x32_bf16 v[92:95],a[180:183],v[172:175],v[92:95]
v_exp_f32 v87,v87
v_mfma_f32_16x16x32_bf16 v[92:95],a[176:179],v[164:167],v[92:95]
v_cvt_pk_bf16_f32 v82,v84,v85
v_cvt_pk_bf16_f32 v83,v86,v87
v_mfma_f32_16x16x32_bf16 a[96:99],a[192:195],v[112:115],a[96:99]
ds_read_b64_tr_b16 a[192:193],v2
ds_read_b64_tr_b16 a[194:195],v2 offset:4096
v_mfma_f32_16x16x32_bf16 a[96:99],a[196:199],v[120:123],a[96:99]
ds_read_b64_tr_b16 a[196:197],v2 offset:8192
ds_read_b64_tr_b16 a[198:199],v2 offset:12288
v_mfma_f32_16x16x32_bf16 a[100:103],a[200:203],v[112:115],a[100:103]
ds_read_b64_tr_b16 a[200:201],v3
ds_read_b64_tr_b16 a[202:203],v3 offset:4096
v_mfma_f32_16x16x32_bf16 a[100:103],a[204:207],v[120:123],a[100:103]
global_load_lds_dwordx4 v5,s[10:11] offset:3072
ds_read_b64_tr_b16 a[204:205],v3 offset:8192
v_mfma_f32_16x16x32_bf16 a[104:107],a[208:211],v[112:115],a[104:107]
ds_read_b64_tr_b16 a[206:207],v3 offset:12288
ds_read_b64_tr_b16 a[208:209],v2 offset:256
v_mfma_f32_16x16x32_bf16 a[104:107],a[212:215],v[120:123],a[104:107]
ds_read_b64_tr_b16 a[210:211],v2 offset:4352
ds_read_b64_tr_b16 a[212:213],v2 offset:8448
v_mfma_f32_16x16x32_bf16 a[108:111],a[216:219],v[112:115],a[108:111]
v_exp_f32 v88,v88
v_mfma_f32_16x16x32_bf16 a[108:111],a[220:223],v[120:123],a[108:111]
ds_read_b64_tr_b16 a[214:215],v2 offset:12544
ds_read_b64_tr_b16 a[216:217],v3 offset:256
v_mfma_f32_16x16x32_bf16 a[112:115],a[224:227],v[112:115],a[112:115]
ds_read_b64_tr_b16 a[218:219],v3 offset:4352
ds_read_b64_tr_b16 a[220:221],v3 offset:8448
v_mfma_f32_16x16x32_bf16 a[112:115],a[228:231],v[120:123],a[112:115]
ds_read_b64_tr_b16 a[222:223],v3 offset:12544
ds_read_b64_tr_b16 a[224:225],v2 offset:512
v_mfma_f32_16x16x32_bf16 a[116:119],a[232:235],v[112:115],a[116:119]
v_exp_f32 v89,v89
v_mfma_f32_16x16x32_bf16 a[116:119],a[236:239],v[120:123],a[116:119]
ds_read_b64_tr_b16 a[226:227],v2 offset:4608
ds_read_b64_tr_b16 a[228:229],v2 offset:8704
v_mfma_f32_16x16x32_bf16 a[120:123],a[240:243],v[112:115],a[120:123]
ds_read_b64_tr_b16 a[230:231],v2 offset:12800
ds_read_b64_tr_b16 a[232:233],v3 offset:512
v_mfma_f32_16x16x32_bf16 a[120:123],a[244:247],v[120:123],a[120:123]
s_add_u32 s10,s20,s74
s_addc_u32 s11,s21,0
ds_read_b64_tr_b16 a[234:235],v3 offset:4608
v_mfma_f32_16x16x32_bf16 a[124:127],a[248:251],v[112:115],a[124:127]
ds_read_b64_tr_b16 a[236:237],v3 offset:8704
ds_read_b64_tr_b16 a[238:239],v3 offset:12800
v_mfma_f32_16x16x32_bf16 a[124:127],a[252:255],v[120:123],a[124:127]
s_add_u32 m0,s76,0
ds_read_b64_tr_b16 a[240:241],v2 offset:768
ds_read_b64_tr_b16 a[242:243],v2 offset:4864
v_mfma_f32_16x16x32_bf16 v[60:63],v[28:31],v[112:115],v[60:63]
ds_read_b64_tr_b16 a[244:245],v2 offset:8960
v_cvt_pk_bf16_f32 v88,v88,v89
v_mfma_f32_16x16x32_bf16 v[60:63],v[28:31],v[120:123],v[60:63]
global_load_lds_dwordx4 v4,s[10:11] nt
ds_read_b64_tr_b16 a[246:247],v2 offset:13056
v_mfma_f32_16x16x32_bf16 v[96:99],a[128:131],v[196:199],v[40:43]
ds_read_b64_tr_b16 a[248:249],v3 offset:768
ds_read_b64_tr_b16 a[250:251],v3 offset:4864
v_mfma_f32_16x16x32_bf16 v[96:99],a[132:135],v[204:207],v[96:99]
ds_read_b64_tr_b16 a[252:253],v3 offset:8960
ds_read_b64_tr_b16 a[254:255],v3 offset:13056
v_mfma_f32_16x16x32_bf16 v[96:99],a[136:139],v[212:215],v[96:99]
v_exp_f32 v90,v90
v_mfma_f32_16x16x32_bf16 v[96:99],a[140:143],v[220:223],v[96:99]
v_exp_f32 v91,v91
v_mfma_f32_16x16x32_bf16 v[100:103],a[156:159],v[220:223],v[40:43]
v_exp_f32 v92,v92
v_mfma_f32_16x16x32_bf16 v[100:103],a[152:155],v[212:215],v[100:103]
v_exp_f32 v93,v93
v_mfma_f32_16x16x32_bf16 v[100:103],a[148:151],v[204:207],v[100:103]
v_exp_f32 v94,v94
v_mfma_f32_16x16x32_bf16 v[100:103],a[144:147],v[196:199],v[100:103]
v_exp_f32 v95,v95
v_mfma_f32_16x16x32_bf16 v[104:107],a[160:163],v[196:199],v[40:43]
v_cvt_pk_bf16_f32 v89,v90,v91
v_cvt_pk_bf16_f32 v90,v92,v93
v_mfma_f32_16x16x32_bf16 v[104:107],a[164:167],v[204:207],v[104:107]
v_cvt_pk_bf16_f32 v91,v94,v95
v_add_u32 v2,s78,v2
v_mfma_f32_16x16x32_bf16 v[104:107],a[168:171],v[212:215],v[104:107]
v_exp_f32 v96,v96
v_mfma_f32_16x16x32_bf16 v[104:107],a[172:175],v[220:223],v[104:107]
v_exp_f32 v97,v97
v_mfma_f32_16x16x32_bf16 v[108:111],a[188:191],v[220:223],v[40:43]
v_exp_f32 v98,v98
v_mfma_f32_16x16x32_bf16 v[108:111],a[184:187],v[212:215],v[108:111]
global_load_lds_dwordx4 v4,s[10:11] offset:1024 nt
v_cvt_pk_bf16_f32 v96,v96,v97
v_mfma_f32_16x16x32_bf16 v[108:111],a[180:183],v[204:207],v[108:111]
v_exp_f32 v99,v99
v_mfma_f32_16x16x32_bf16 v[108:111],a[176:179],v[196:199],v[108:111]
s_waitcnt lgkmcnt(0)
v_exp_f32 v100,v100
v_mfma_f32_16x16x32_bf16 a[0:3],a[192:195],v[64:67],a[0:3]
v_exp_f32 v101,v101
v_mfma_f32_16x16x32_bf16 a[0:3],a[196:199],v[72:75],a[0:3]
v_exp_f32 v102,v102
v_mfma_f32_16x16x32_bf16 a[4:7],a[200:203],v[64:67],a[4:7]
v_exp_f32 v103,v103
v_mfma_f32_16x16x32_bf16 a[4:7],a[204:207],v[72:75],a[4:7]
v_cvt_pk_bf16_f32 v97,v98,v99
v_cvt_pk_bf16_f32 v98,v100,v101
v_mfma_f32_16x16x32_bf16 a[8:11],a[208:211],v[64:67],a[8:11]
v_cvt_pk_bf16_f32 v99,v102,v103
v_add_u32 v3,s78,v3
v_mfma_f32_16x16x32_bf16 a[8:11],a[212:215],v[72:75],a[8:11]
s_sub_u32 s78,s77,32768
v_exp_f32 v104,v104
v_mfma_f32_16x16x32_bf16 a[12:15],a[216:219],v[64:67],a[12:15]
s_and_b32 s78,s78,0x1ffff
v_exp_f32 v105,v105
v_mfma_f32_16x16x32_bf16 a[12:15],a[220:223],v[72:75],a[12:15]
s_sub_u32 s78,s78,s77
v_exp_f32 v106,v106
v_mfma_f32_16x16x32_bf16 a[16:19],a[224:227],v[64:67],a[16:19]
s_add_u32 s77,s77,s78
v_exp_f32 v107,v107
v_mfma_f32_16x16x32_bf16 a[16:19],a[228:231],v[72:75],a[16:19]
v_exp_f32 v108,v108
v_mfma_f32_16x16x32_bf16 a[20:23],a[232:235],v[64:67],a[20:23]
s_getreg_b32 s92,hwreg(HW_REG_IB_STS,0,4)
s_min_u32 s4,s92,s6
s_mov_b32 s6,s92
v_mfma_f32_16x16x32_bf16 a[20:23],a[236:239],v[72:75],a[20:23]
s_cmp_gt_u32 s4,s9
s_cselect_b32 s4,15,s5
v_cvt_pk_bf16_f32 v104,v104,v105
v_mfma_f32_16x16x32_bf16 a[24:27],a[240:243],v[64:67],a[24:27]
v_exp_f32 v109,v109
v_mfma_f32_16x16x32_bf16 a[24:27],a[244:247],v[72:75],a[24:27]
v_exp_f32 v110,v110
v_mfma_f32_16x16x32_bf16 a[28:31],a[248:251],v[64:67],a[28:31]
v_exp_f32 v111,v111
v_mfma_f32_16x16x32_bf16 a[28:31],a[252:255],v[72:75],a[28:31]
s_getpc_b64 s[92:93]
s_cmp_lg_u32 s4,0
s_cbranch_scc1 L_pace_%=
v_mfma_f32_16x16x32_bf16 v[48:51],v[28:31],v[64:67],v[48:51]
v_cvt_pk_bf16_f32 v105,v106,v107
v_cvt_pk_bf16_f32 v106,v108,v109
v_mfma_f32_16x16x32_bf16 v[48:51],v[28:31],v[72:75],v[48:51]
s_add_u32 s10,s20,s74
s_addc_u32 s11,s21,0
v_cvt_pk_bf16_f32 v107,v110,v111
s_waitcnt vmcnt(10)
s_barrier
v_mfma_f32_16x16x32_bf16 v[24:27],v[8:11],v[128:131],0
s_add_u32 m0,s76,0
v_mfma_f32_16x16x32_bf16 v[24:27],v[12:15],v[136:139],v[24:27]
v_mfma_f32_16x16x32_bf16 v[24:27],v[16:19],v[144:147],v[24:27]
global_load_lds_dwordx4 v5,s[10:11] offset:2048 nt
v_mfma_f32_16x16x32_bf16 v[24:27],v[20:23],v[152:155],v[24:27]
v_mfma_f32_16x16x32_bf16 v[112:115],a[128:131],v[228:231],v[44:47]
ds_read_b128 a[128:131],v1
v_mfma_f32_16x16x32_bf16 v[112:115],a[132:135],v[236:239],v[112:115]
ds_read_b128 a[132:135],v1 offset:256
v_mfma_f32_16x16x32_bf16 v[112:115],a[136:139],v[244:247],v[112:115]
ds_read_b128 a[136:139],v1 offset:512
v_mfma_f32_16x16x32_bf16 v[112:115],a[140:143],v[252:255],v[112:115]
ds_read_b128 a[140:143],v1 offset:768
v_mfma_f32_16x16x32_bf16 v[116:119],a[156:159],v[252:255],v[44:47]
v_max3_f32 v24,v24,v25,v26
v_max_f32 v24,v24,v27
v_mfma_f32_16x16x32_bf16 v[116:119],a[152:155],v[244:247],v[116:119]
ds_read_b128 a[156:159],v1 offset:4864
v_mov_b32 v25,v24
v_mfma_f32_16x16x32_bf16 v[116:119],a[148:151],v[236:239],v[116:119]
v_exp_f32 v112,v112
v_mfma_f32_16x16x32_bf16 v[116:119],a[144:147],v[228:231],v[116:119]
v_permlane32_swap_b32 v24,v25
v_mfma_f32_16x16x32_bf16 v[120:123],a[160:163],v[228:231],v[44:47]
v_max_f32 v24,v24,v25
v_mov_b32 v25,v24
v_mfma_f32_16x16x32_bf16 v[120:123],a[164:167],v[236:239],v[120:123]
v_exp_f32 v113,v113
v_mfma_f32_16x16x32_bf16 v[120:123],a[168:171],v[244:247],v[120:123]
v_permlane16_swap_b32 v24,v25
v_mfma_f32_16x16x32_bf16 v[120:123],a[172:175],v[252:255],v[120:123]
v_max_f32 v24,v24,v25
v_sub_f32 v27,0,v24
v_mfma_f32_16x16x32_bf16 v[124:127],a[188:191],v[252:255],v[44:47]
global_load_lds_dwordx4 v5,s[10:11] offset:3072
v_sub_f32 v26,0,v24
v_sub_f32 v25,0,v24
v_sub_f32 v24,0,v24
v_mfma_f32_16x16x32_bf16 v[124:127],a[184:187],v[244:247],v[124:127]
ds_write_b128 v7,v[24:27]
v_mfma_f32_16x16x32_bf16 v[124:127],a[180:183],v[236:239],v[124:127]
ds_read_b128 a[152:155],v1 offset:4608
v_cvt_pk_bf16_f32 v112,v112,v113
v_mfma_f32_16x16x32_bf16 v[124:127],a[176:179],v[228:231],v[124:127]
v_exp_f32 v114,v114
v_mfma_f32_16x16x32_bf16 v[24:27],v[8:11],v[160:163],0
v_exp_f32 v115,v115
v_mfma_f32_16x16x32_bf16 v[24:27],v[12:15],v[168:171],v[24:27]
ds_read_b128 a[148:151],v1 offset:4352
v_cvt_pk_bf16_f32 v113,v114,v115
v_mfma_f32_16x16x32_bf16 v[24:27],v[16:19],v[176:179],v[24:27]
v_exp_f32 v116,v116
v_mfma_f32_16x16x32_bf16 v[24:27],v[20:23],v[184:187],v[24:27]
v_exp_f32 v117,v117
v_mfma_f32_16x16x32_bf16 a[32:35],a[192:195],v[80:83],a[32:35]
ds_read_b128 a[144:147],v1 offset:4096
v_cvt_pk_bf16_f32 v114,v116,v117
v_mfma_f32_16x16x32_bf16 a[32:35],a[196:199],v[88:91],a[32:35]
v_exp_f32 v118,v118
v_mfma_f32_16x16x32_bf16 a[36:39],a[200:203],v[80:83],a[36:39]
s_add_u32 s10,s24,s74
s_addc_u32 s11,s25,0
ds_read_b128 a[160:163],v1 offset:8192
v_mfma_f32_16x16x32_bf16 a[36:39],a[204:207],v[88:91],a[36:39]
s_add_u32 s74,s74,16384
v_max3_f32 v24,v24,v25,v26
v_max_f32 v24,v24,v27
v_mfma_f32_16x16x32_bf16 a[40:43],a[208:211],v[80:83],a[40:43]
s_add_u32 m0,s76,16384
ds_read_b128 a[164:167],v1 offset:8448
v_mov_b32 v25,v24
v_mfma_f32_16x16x32_bf16 a[40:43],a[212:215],v[88:91],a[40:43]
s_and_b32 s74,s74,0x3ffff
v_exp_f32 v119,v119
v_mfma_f32_16x16x32_bf16 a[44:47],a[216:219],v[80:83],a[44:47]
global_load_lds_dwordx4 v4,s[10:11]
ds_read_b128 a[168:171],v1 offset:8704
v_mfma_f32_16x16x32_bf16 a[44:47],a[220:223],v[88:91],a[44:47]
)A"
R"A(s_or_b32 s74,s74,s69
v_permlane32_swap_b32 v24,v25
v_mfma_f32_16x16x32_bf16 a[48:51],a[224:227],v[80:83],a[48:51]
s_sub_u32 s76,s76,32768
v_max_f32 v24,v24,v25
v_mov_b32 v25,v24
v_mfma_f32_16x16x32_bf16 a[48:51],a[228:231],v[88:91],a[48:51]
s_and_b32 s76,s76,0x1ffff
ds_read_b128 a[172:175],v1 offset:8960
v_cvt_pk_bf16_f32 v115,v118,v119
v_mfma_f32_16x16x32_bf16 a[52:55],a[232:235],v[80:83],a[52:55]
v_permlane16_swap_b32 v24,v25
v_mfma_f32_16x16x32_bf16 a[52:55],a[236:239],v[88:91],a[52:55]
v_max_f32 v24,v24,v25
v_sub_f32 v27,0,v24
v_mfma_f32_16x16x32_bf16 a[56:59],a[240:243],v[80:83],a[56:59]
v_sub_f32 v26,0,v24
v_sub_f32 v25,0,v24
v_mfma_f32_16x16x32_bf16 a[56:59],a[244:247],v[88:91],a[56:59]
ds_read_b128 a[188:191],v1 offset:13056
v_sub_f32 v24,0,v24
v_mfma_f32_16x16x32_bf16 a[60:63],a[248:251],v[80:83],a[60:63]
ds_write_b128 v7,v[24:27] offset:1024
v_mfma_f32_16x16x32_bf16 a[60:63],a[252:255],v[88:91],a[60:63]
v_exp_f32 v120,v120
v_mfma_f32_16x16x32_bf16 v[52:55],v[28:31],v[80:83],v[52:55]
v_exp_f32 v121,v121
v_mfma_f32_16x16x32_bf16 v[52:55],v[28:31],v[88:91],v[52:55]
ds_read_b128 a[184:187],v1 offset:12800
v_cvt_pk_bf16_f32 v120,v120,v121
v_mfma_f32_16x16x32_bf16 v[24:27],v[8:11],v[192:195],0
v_exp_f32 v122,v122
v_mfma_f32_16x16x32_bf16 v[24:27],v[12:15],v[200:203],v[24:27]
v_exp_f32 v123,v123
v_mfma_f32_16x16x32_bf16 v[24:27],v[16:19],v[208:211],v[24:27]
s_waitcnt lgkmcnt(2)
ds_read_b128 a[180:183],v1 offset:12544
v_cvt_pk_bf16_f32 v121,v122,v123
v_mfma_f32_16x16x32_bf16 v[24:27],v[20:23],v[216:219],v[24:27]
global_load_lds_dwordx4 v5,s[10:11] offset:1024
ds_read_b128 a[176:179],v1 offset:12288
v_mfma_f32_16x16x32_bf16 v[64:67],a[128:131],v[132:135],v[32:35]
v_exp_f32 v124,v124
v_mfma_f32_16x16x32_bf16 v[64:67],a[132:135],v[140:143],v[64:67]
v_exp_f32 v125,v125
v_mfma_f32_16x16x32_bf16 v[64:67],a[136:139],v[148:151],v[64:67]
v_exp_f32 v126,v126
v_mfma_f32_16x16x32_bf16 v[64:67],a[140:143],v[156:159],v[64:67]
v_max3_f32 v24,v24,v25,v26
v_max_f32 v24,v24,v27
v_mfma_f32_16x16x32_bf16 v[68:71],a[156:159],v[156:159],v[32:35]
v_mov_b32 v25,v24
v_cvt_pk_bf16_f32 v122,v124,v125
v_mfma_f32_16x16x32_bf16 v[68:71],a[152:155],v[148:151],v[68:71]
v_exp_f32 v127,v127
v_mfma_f32_16x16x32_bf16 v[68:71],a[148:151],v[140:143],v[68:71]
v_permlane32_swap_b32 v24,v25
v_mfma_f32_16x16x32_bf16 v[68:71],a[144:147],v[132:135],v[68:71]
v_max_f32 v24,v24,v25
v_mov_b32 v25,v24
v_mfma_f32_16x16x32_bf16 v[72:75],a[160:163],v[132:135],v[32:35]
v_cvt_pk_bf16_f32 v123,v126,v127
v_add_u32 v1,s78,v1
v_mfma_f32_16x16x32_bf16 v[72:75],a[164:167],v[140:143],v[72:75]
v_permlane16_swap_b32 v24,v25
v_mfma_f32_16x16x32_bf16 v[72:75],a[168:171],v[148:151],v[72:75]
v_max_f32 v24,v24,v25
v_sub_f32 v27,0,v24
v_mfma_f32_16x16x32_bf16 v[72:75],a[172:175],v[156:159],v[72:75]
v_sub_f32 v26,0,v24
v_sub_f32 v25,0,v24
v_mfma_f32_16x16x32_bf16 v[76:79],a[188:191],v[156:159],v[32:35]
s_waitcnt lgkmcnt(0)
v_exp_f32 v64,v64
v_mfma_f32_16x16x32_bf16 v[76:79],a[184:187],v[148:151],v[76:79]
global_load_lds_dwordx4 v4,s[10:11] offset:2048
v_sub_f32 v24,0,v24
v_mfma_f32_16x16x32_bf16 v[76:79],a[180:183],v[140:143],v[76:79]
ds_write_b128 v7,v[24:27] offset:2048
v_mfma_f32_16x16x32_bf16 v[76:79],a[176:179],v[132:135],v[76:79]
v_exp_f32 v65,v65
v_mfma_f32_16x16x32_bf16 a[64:67],a[192:195],v[96:99],a[64:67]
v_exp_f32 v66,v66
v_mfma_f32_16x16x32_bf16 a[64:67],a[196:199],v[104:107],a[64:67]
v_exp_f32 v67,v67
v_mfma_f32_16x16x32_bf16 a[68:71],a[200:203],v[96:99],a[68:71]
v_exp_f32 v68,v68
v_mfma_f32_16x16x32_bf16 a[68:71],a[204:207],v[104:107],a[68:71]
v_exp_f32 v69,v69
v_mfma_f32_16x16x32_bf16 a[72:75],a[208:211],v[96:99],a[72:75]
v_exp_f32 v70,v70
v_mfma_f32_16x16x32_bf16 a[72:75],a[212:215],v[104:107],a[72:75]
v_exp_f32 v71,v71
v_mfma_f32_16x16x32_bf16 a[76:79],a[216:219],v[96:99],a[76:79]
v_cvt_pk_bf16_f32 v64,v64,v65
v_cvt_pk_bf16_f32 v65,v66,v67
v_mfma_f32_16x16x32_bf16 a[76:79],a[220:223],v[104:107],a[76:79]
v_cvt_pk_bf16_f32 v66,v68,v69
v_cvt_pk_bf16_f32 v67,v70,v71
v_mfma_f32_16x16x32_bf16 a[80:83],a[224:227],v[96:99],a[80:83]
v_exp_f32 v72,v72
v_mfma_f32_16x16x32_bf16 a[80:83],a[228:231],v[104:107],a[80:83]
v_exp_f32 v73,v73
v_mfma_f32_16x16x32_bf16 a[84:87],a[232:235],v[96:99],a[84:87]
v_exp_f32 v74,v74
v_mfma_f32_16x16x32_bf16 a[84:87],a[236:239],v[104:107],a[84:87]
v_exp_f32 v75,v75
v_mfma_f32_16x16x32_bf16 a[88:91],a[240:243],v[96:99],a[88:91]
global_load_lds_dwordx4 v5,s[10:11] offset:3072
v_cvt_pk_bf16_f32 v72,v72,v73
v_mfma_f32_16x16x32_bf16 a[88:91],a[244:247],v[104:107],a[88:91]
v_exp_f32 v76,v76
v_mfma_f32_16x16x32_bf16 a[92:95],a[248:251],v[96:99],a[92:95]
v_exp_f32 v77,v77
v_mfma_f32_16x16x32_bf16 a[92:95],a[252:255],v[104:107],a[92:95]
v_exp_f32 v78,v78
v_mfma_f32_16x16x32_bf16 v[56:59],v[28:31],v[96:99],v[56:59]
v_exp_f32 v79,v79
v_mfma_f32_16x16x32_bf16 v[56:59],v[28:31],v[104:107],v[56:59]
v_cvt_pk_bf16_f32 v73,v74,v75
v_cvt_pk_bf16_f32 v74,v76,v77
v_mfma_f32_16x16x32_bf16 v[24:27],v[8:11],v[224:227],0
v_cvt_pk_bf16_f32 v75,v78,v79
v_mfma_f32_16x16x32_bf16 v[24:27],v[12:15],v[232:235],v[24:27]
v_mfma_f32_16x16x32_bf16 v[24:27],v[16:19],v[240:243],v[24:27]
v_mfma_f32_16x16x32_bf16 v[24:27],v[20:23],v[248:251],v[24:27]
v_mfma_f32_16x16x32_bf16 v[80:83],a[128:131],v[164:167],v[36:39]
s_add_u32 s10,s20,s74
s_addc_u32 s11,s21,0
v_mfma_f32_16x16x32_bf16 v[80:83],a[132:135],v[172:175],v[80:83]
v_mfma_f32_16x16x32_bf16 v[80:83],a[136:139],v[180:183],v[80:83]
s_add_u32 m0,s76,0
v_mfma_f32_16x16x32_bf16 v[80:83],a[140:143],v[188:191],v[80:83]
v_mfma_f32_16x16x32_bf16 v[84:87],a[156:159],v[188:191],v[36:39]
global_load_lds_dwordx4 v4,s[10:11] nt
v_max3_f32 v24,v24,v25,v26
v_mfma_f32_16x16x32_bf16 v[84:87],a[152:155],v[180:183],v[84:87]
v_max_f32 v24,v24,v27
v_mov_b32 v25,v24
v_mfma_f32_16x16x32_bf16 v[84:87],a[148:151],v[172:175],v[84:87]
v_mfma_f32_16x16x32_bf16 v[84:87],a[144:147],v[164:167],v[84:87]
v_permlane32_swap_b32 v24,v25
v_mfma_f32_16x16x32_bf16 v[88:91],a[160:163],v[164:167],v[36:39]
v_max_f32 v24,v24,v25
v_mov_b32 v25,v24
v_mfma_f32_16x16x32_bf16 v[88:91],a[164:167],v[172:175],v[88:91]
v_exp_f32 v80,v80
v_mfma_f32_16x16x32_bf16 v[88:91],a[168:171],v[180:183],v[88:91]
v_permlane16_swap_b32 v24,v25
v_mfma_f32_16x16x32_bf16 v[88:91],a[172:175],v[188:191],v[88:91]
v_max_f32 v24,v24,v25
v_max_f32 v24,s72,v24
v_mfma_f32_16x16x32_bf16 v[92:95],a[188:191],v[188:191],v[36:39]
v_sub_f32 v27,0,v24
v_sub_f32 v26,0,v24
v_mfma_f32_16x16x32_bf16 v[92:95],a[184:187],v[180:183],v[92:95]
v_sub_f32 v25,0,v24
v_sub_f32 v24,0,v24
v_mfma_f32_16x16x32_bf16 v[92:95],a[180:183],v[172:175],v[92:95]
v_exp_f32 v81,v81
L_pd6_%=:
s_cbranch_execz L_trt0_%=
v_mfma_f32_16x16x32_bf16 v[92:95],a[176:179],v[164:167],v[92:95]
v_exp_f32 v82,v82
v_mfma_f32_16x16x32_bf16 a[96:99],a[192:195],v[112:115],a[96:99]
ds_read_b64_tr_b16 a[192:193],v2
ds_read_b64_tr_b16 a[194:195],v2 offset:4096
v_mfma_f32_16x16x32_bf16 a[96:99],a[196:199],v[120:123],a[96:99]
ds_read_b64_tr_b16 a[196:197],v2 offset:8192
ds_read_b64_tr_b16 a[198:199],v2 offset:12288
v_mfma_f32_16x16x32_bf16 a[100:103],a[200:203],v[112:115],a[100:103]
ds_read_b64_tr_b16 a[200:201],v3
ds_read_b64_tr_b16 a[202:203],v3 offset:4096
v_mfma_f32_16x16x32_bf16 a[100:103],a[204:207],v[120:123],a[100:103]
global_load_lds_dwordx4 v4,s[10:11] offset:1024 nt
ds_read_b64_tr_b16 a[204:205],v3 offset:8192
v_mfma_f32_16x16x32_bf16 a[104:107],a[208:211],v[112:115],a[104:107]
ds_read_b64_tr_b16 a[206:207],v3 offset:12288
ds_read_b64_tr_b16 a[208:209],v2 offset:256
v_mfma_f32_16x16x32_bf16 a[104:107],a[212:215],v[120:123],a[104:107]
ds_read_b64_tr_b16 a[210:211],v2 offset:4352
ds_read_b64_tr_b16 a[212:213],v2 offset:8448
v_mfma_f32_16x16x32_bf16 a[108:111],a[216:219],v[112:115],a[108:111]
ds_read_b64_tr_b16 a[214:215],v2 offset:12544
v_cvt_pk_bf16_f32 v80,v80,v81
v_mfma_f32_16x16x32_bf16 a[108:111],a[220:223],v[120:123],a[108:111]
ds_read_b64_tr_b16 a[216:217],v3 offset:256
ds_read_b64_tr_b16 a[218:219],v3 offset:4352
v_mfma_f32_16x16x32_bf16 a[112:115],a[224:227],v[112:115],a[112:115]
v_exp_f32 v83,v83
v_mfma_f32_16x16x32_bf16 a[112:115],a[228:231],v[120:123],a[112:115]
ds_read_b64_tr_b16 a[220:221],v3 offset:8448
ds_read_b64_tr_b16 a[222:223],v3 offset:12544
v_mfma_f32_16x16x32_bf16 a[116:119],a[232:235],v[112:115],a[116:119]
ds_read_b64_tr_b16 a[224:225],v2 offset:512
ds_read_b64_tr_b16 a[226:227],v2 offset:4608
v_mfma_f32_16x16x32_bf16 a[116:119],a[236:239],v[120:123],a[116:119]
ds_read_b64_tr_b16 a[228:229],v2 offset:8704
ds_read_b64_tr_b16 a[230:231],v2 offset:12800
v_mfma_f32_16x16x32_bf16 a[120:123],a[240:243],v[112:115],a[120:123]
ds_read_b64_tr_b16 a[232:233],v3 offset:512
v_cvt_pk_bf16_f32 v81,v82,v83
v_mfma_f32_16x16x32_bf16 a[120:123],a[244:247],v[120:123],a[120:123]
ds_read_b64_tr_b16 a[234:235],v3 offset:4608
ds_read_b64_tr_b16 a[236:237],v3 offset:8704
v_mfma_f32_16x16x32_bf16 a[124:127],a[248:251],v[112:115],a[124:127]
v_exp_f32 v84,v84
v_mfma_f32_16x16x32_bf16 a[124:127],a[252:255],v[120:123],a[124:127]
ds_read_b64_tr_b16 a[238:239],v3 offset:12800
ds_read_b64_tr_b16 a[240:241],v2 offset:768
v_mfma_f32_16x16x32_bf16 v[60:63],v[28:31],v[112:115],v[60:63]
ds_read_b64_tr_b16 a[242:243],v2 offset:4864
ds_read_b64_tr_b16 a[244:245],v2 offset:8960
v_mfma_f32_16x16x32_bf16 v[60:63],v[28:31],v[120:123],v[60:63]
global_load_lds_dwordx4 v5,s[10:11] offset:2048 nt
ds_read_b64_tr_b16 a[246:247],v2 offset:13056
v_mfma_f32_16x16x32_bf16 v[96:99],a[128:131],v[196:199],v[40:43]
ds_read_b64_tr_b16 a[248:249],v3 offset:768
ds_read_b64_tr_b16 a[250:251],v3 offset:4864
v_mfma_f32_16x16x32_bf16 v[96:99],a[132:135],v[204:207],v[96:99]
ds_read_b64_tr_b16 a[252:253],v3 offset:8960
ds_read_b64_tr_b16 a[254:255],v3 offset:13056
v_mfma_f32_16x16x32_bf16 v[96:99],a[136:139],v[212:215],v[96:99]
v_exp_f32 v85,v85
v_mfma_f32_16x16x32_bf16 v[96:99],a[140:143],v[220:223],v[96:99]
v_exp_f32 v86,v86
v_mfma_f32_16x16x32_bf16 v[100:103],a[156:159],v[220:223],v[40:43]
v_exp_f32 v87,v87
v_mfma_f32_16x16x32_bf16 v[100:103],a[152:155],v[212:215],v[100:103]
v_exp_f32 v88,v88
v_mfma_f32_16x16x32_bf16 v[100:103],a[148:151],v[204:207],v[100:103]
v_exp_f32 v89,v89
v_mfma_f32_16x16x32_bf16 v[100:103],a[144:147],v[196:199],v[100:103]
v_exp_f32 v90,v90
v_mfma_f32_16x16x32_bf16 v[104:107],a[160:163],v[196:199],v[40:43]
v_exp_f32 v91,v91
v_mfma_f32_16x16x32_bf16 v[104:107],a[164:167],v[204:207],v[104:107]
v_exp_f32 v92,v92
v_mfma_f32_16x16x32_bf16 v[104:107],a[168:171],v[212:215],v[104:107]
v_exp_f32 v93,v93
v_mfma_f32_16x16x32_bf16 v[104:107],a[172:175],v[220:223],v[104:107]
v_exp_f32 v94,v94
v_mfma_f32_16x16x32_bf16 v[108:111],a[188:191],v[220:223],v[40:43]
v_exp_f32 v95,v95
v_mfma_f32_16x16x32_bf16 v[108:111],a[184:187],v[212:215],v[108:111]
v_exp_f32 v96,v96
v_mfma_f32_16x16x32_bf16 v[108:111],a[180:183],v[204:207],v[108:111]
global_load_lds_dwordx4 v5,s[10:11] offset:3072
v_cvt_pk_bf16_f32 v82,v84,v85
v_mfma_f32_16x16x32_bf16 v[108:111],a[176:179],v[196:199],v[108:111]
s_waitcnt lgkmcnt(0)
v_exp_f32 v97,v97
v_mfma_f32_16x16x32_bf16 a[0:3],a[192:195],v[64:67],a[0:3]
v_exp_f32 v98,v98
v_mfma_f32_16x16x32_bf16 a[0:3],a[196:199],v[72:75],a[0:3]
v_exp_f32 v99,v99
v_mfma_f32_16x16x32_bf16 a[4:7],a[200:203],v[64:67],a[4:7]
v_exp_f32 v100,v100
v_mfma_f32_16x16x32_bf16 a[4:7],a[204:207],v[72:75],a[4:7]
v_exp_f32 v101,v101
v_mfma_f32_16x16x32_bf16 a[8:11],a[208:211],v[64:67],a[8:11]
v_exp_f32 v102,v102
v_mfma_f32_16x16x32_bf16 a[8:11],a[212:215],v[72:75],a[8:11]
v_exp_f32 v103,v103
v_mfma_f32_16x16x32_bf16 a[12:15],a[216:219],v[64:67],a[12:15]
ds_write_b128 v7,v[24:27] offset:3072
v_mfma_f32_16x16x32_bf16 a[12:15],a[220:223],v[72:75],a[12:15]
v_cvt_pk_bf16_f32 v83,v86,v87
v_cvt_pk_bf16_f32 v88,v88,v89
v_mfma_f32_16x16x32_bf16 a[16:19],a[224:227],v[64:67],a[16:19]
v_cvt_pk_bf16_f32 v89,v90,v91
v_cvt_pk_bf16_f32 v90,v92,v93
v_mfma_f32_16x16x32_bf16 a[16:19],a[228:231],v[72:75],a[16:19]
v_cvt_pk_bf16_f32 v91,v94,v95
v_cvt_pk_bf16_f32 v96,v96,v97
v_mfma_f32_16x16x32_bf16 a[20:23],a[232:235],v[64:67],a[20:23]
s_getreg_b32 s92,hwreg(HW_REG_IB_STS,0,4)
)A"
R"A(s_min_u32 s4,s92,s6
s_mov_b32 s6,s92
v_mfma_f32_16x16x32_bf16 a[20:23],a[236:239],v[72:75],a[20:23]
s_cmp_gt_u32 s4,s9
s_cselect_b32 s4,15,s5
v_cvt_pk_bf16_f32 v97,v98,v99
v_mfma_f32_16x16x32_bf16 a[24:27],a[240:243],v[64:67],a[24:27]
v_cvt_pk_bf16_f32 v98,v100,v101
v_cvt_pk_bf16_f32 v99,v102,v103
v_mfma_f32_16x16x32_bf16 a[24:27],a[244:247],v[72:75],a[24:27]
v_mfma_f32_16x16x32_bf16 a[28:31],a[248:251],v[64:67],a[28:31]
v_mfma_f32_16x16x32_bf16 a[28:31],a[252:255],v[72:75],a[28:31]
s_getpc_b64 s[92:93]
s_cmp_lg_u32 s4,0
s_cbranch_scc1 L_pace_%=
v_mfma_f32_16x16x32_bf16 v[48:51],v[28:31],v[64:67],v[48:51]
v_mfma_f32_16x16x32_bf16 v[48:51],v[28:31],v[72:75],v[48:51]
s_mov_b32 s70,11
L_a0_%=:
s_waitcnt vmcnt(12)
s_barrier
v_mfma_f32_16x16x32_bf16 v[112:115],a[128:131],v[228:231],v[44:47]
s_add_u32 s10,s24,s74
s_addc_u32 s11,s25,0
s_add_u32 m0,s76,16384
v_mfma_f32_16x16x32_bf16 v[112:115],a[132:135],v[236:239],v[112:115]
s_add_u32 s75,s75,16384
s_add_u32 s74,s74,16384
ds_read_b128 a[128:131],v1
v_mfma_f32_16x16x32_bf16 v[112:115],a[136:139],v[244:247],v[112:115]
s_and_b32 s74,s74,0x3ffff
global_load_lds_dwordx4 v4,s[10:11]
v_mfma_f32_16x16x32_bf16 v[112:115],a[140:143],v[252:255],v[112:115]
s_and_b32 s69,s75,0xfffc0000
s_or_b32 s74,s74,s69
ds_read_b128 a[132:135],v1 offset:256
v_mfma_f32_16x16x32_bf16 v[116:119],a[156:159],v[252:255],v[44:47]
s_sub_u32 s76,s76,32768
s_and_b32 s76,s76,0x1ffff
v_mfma_f32_16x16x32_bf16 v[116:119],a[152:155],v[244:247],v[116:119]
ds_read_b128 a[136:139],v1 offset:512
v_mfma_f32_16x16x32_bf16 v[116:119],a[148:151],v[236:239],v[116:119]
v_mfma_f32_16x16x32_bf16 v[116:119],a[144:147],v[228:231],v[116:119]
ds_read_b128 a[140:143],v1 offset:768
v_mfma_f32_16x16x32_bf16 v[120:123],a[160:163],v[228:231],v[44:47]
v_mfma_f32_16x16x32_bf16 v[120:123],a[164:167],v[236:239],v[120:123]
ds_read_b128 a[156:159],v1 offset:4864
v_mfma_f32_16x16x32_bf16 v[120:123],a[168:171],v[244:247],v[120:123]
v_mfma_f32_16x16x32_bf16 v[120:123],a[172:175],v[252:255],v[120:123]
ds_read_b128 a[152:155],v1 offset:4608
v_mfma_f32_16x16x32_bf16 v[124:127],a[188:191],v[252:255],v[44:47]
v_exp_f32 v104,v104
v_mfma_f32_16x16x32_bf16 v[124:127],a[184:187],v[244:247],v[124:127]
ds_read_b128 a[148:151],v1 offset:4352
v_add_u32 v2,s78,v2
v_mfma_f32_16x16x32_bf16 v[124:127],a[180:183],v[236:239],v[124:127]
v_exp_f32 v105,v105
v_mfma_f32_16x16x32_bf16 v[124:127],a[176:179],v[228:231],v[124:127]
ds_read_b128 a[144:147],v1 offset:4096
v_cvt_pk_bf16_f32 v104,v104,v105
v_mfma_f32_16x16x32_bf16 a[32:35],a[192:195],v[80:83],a[32:35]
v_exp_f32 v106,v106
v_mfma_f32_16x16x32_bf16 a[32:35],a[196:199],v[88:91],a[32:35]
ds_read_b128 a[160:163],v1 offset:8192
v_add_u32 v3,s78,v3
v_mfma_f32_16x16x32_bf16 a[36:39],a[200:203],v[80:83],a[36:39]
s_sub_u32 s78,s77,32768
global_load_lds_dwordx4 v5,s[10:11] offset:1024
v_mfma_f32_16x16x32_bf16 a[36:39],a[204:207],v[88:91],a[36:39]
s_and_b32 s78,s78,0x1ffff
v_exp_f32 v107,v107
v_mfma_f32_16x16x32_bf16 a[40:43],a[208:211],v[80:83],a[40:43]
s_sub_u32 s78,s78,s77
ds_read_b128 a[164:167],v1 offset:8448
v_cvt_pk_bf16_f32 v105,v106,v107
v_mfma_f32_16x16x32_bf16 a[40:43],a[212:215],v[88:91],a[40:43]
s_add_u32 s77,s77,s78
v_exp_f32 v108,v108
v_mfma_f32_16x16x32_bf16 a[44:47],a[216:219],v[80:83],a[44:47]
v_exp_f32 v109,v109
v_mfma_f32_16x16x32_bf16 a[44:47],a[220:223],v[88:91],a[44:47]
ds_read_b128 a[168:171],v1 offset:8704
v_cvt_pk_bf16_f32 v106,v108,v109
v_mfma_f32_16x16x32_bf16 a[48:51],a[224:227],v[80:83],a[48:51]
v_exp_f32 v110,v110
v_mfma_f32_16x16x32_bf16 a[48:51],a[228:231],v[88:91],a[48:51]
v_exp_f32 v111,v111
v_mfma_f32_16x16x32_bf16 a[52:55],a[232:235],v[80:83],a[52:55]
ds_read_b128 a[172:175],v1 offset:8960
v_cvt_pk_bf16_f32 v107,v110,v111
v_mfma_f32_16x16x32_bf16 a[52:55],a[236:239],v[88:91],a[52:55]
v_exp_f32 v112,v112
v_mfma_f32_16x16x32_bf16 a[56:59],a[240:243],v[80:83],a[56:59]
v_exp_f32 v113,v113
v_mfma_f32_16x16x32_bf16 a[56:59],a[244:247],v[88:91],a[56:59]
ds_read_b128 a[188:191],v1 offset:13056
v_cvt_pk_bf16_f32 v112,v112,v113
v_mfma_f32_16x16x32_bf16 a[60:63],a[248:251],v[80:83],a[60:63]
v_exp_f32 v114,v114
v_mfma_f32_16x16x32_bf16 a[60:63],a[252:255],v[88:91],a[60:63]
v_exp_f32 v115,v115
v_mfma_f32_16x16x32_bf16 v[52:55],v[28:31],v[80:83],v[52:55]
ds_read_b128 a[184:187],v1 offset:12800
v_cvt_pk_bf16_f32 v113,v114,v115
v_mfma_f32_16x16x32_bf16 v[52:55],v[28:31],v[88:91],v[52:55]
s_waitcnt lgkmcnt(3)
v_exp_f32 v116,v116
v_mfma_f32_16x16x32_bf16 v[64:67],a[128:131],v[132:135],v[32:35]
global_load_lds_dwordx4 v4,s[10:11] offset:2048
ds_read_b128 a[180:183],v1 offset:12544
v_mfma_f32_16x16x32_bf16 v[64:67],a[132:135],v[140:143],v[64:67]
v_exp_f32 v117,v117
v_mfma_f32_16x16x32_bf16 v[64:67],a[136:139],v[148:151],v[64:67]
ds_read_b128 a[176:179],v1 offset:12288
v_cvt_pk_bf16_f32 v114,v116,v117
v_mfma_f32_16x16x32_bf16 v[64:67],a[140:143],v[156:159],v[64:67]
v_exp_f32 v118,v118
v_mfma_f32_16x16x32_bf16 v[68:71],a[156:159],v[156:159],v[32:35]
v_exp_f32 v119,v119
v_mfma_f32_16x16x32_bf16 v[68:71],a[152:155],v[148:151],v[68:71]
v_cvt_pk_bf16_f32 v115,v118,v119
v_add_u32 v1,s78,v1
v_mfma_f32_16x16x32_bf16 v[68:71],a[148:151],v[140:143],v[68:71]
v_exp_f32 v120,v120
v_mfma_f32_16x16x32_bf16 v[68:71],a[144:147],v[132:135],v[68:71]
v_exp_f32 v121,v121
v_mfma_f32_16x16x32_bf16 v[72:75],a[160:163],v[132:135],v[32:35]
v_exp_f32 v122,v122
v_mfma_f32_16x16x32_bf16 v[72:75],a[164:167],v[140:143],v[72:75]
v_exp_f32 v123,v123
v_mfma_f32_16x16x32_bf16 v[72:75],a[168:171],v[148:151],v[72:75]
s_waitcnt lgkmcnt(0)
v_exp_f32 v124,v124
v_mfma_f32_16x16x32_bf16 v[72:75],a[172:175],v[156:159],v[72:75]
v_exp_f32 v125,v125
v_mfma_f32_16x16x32_bf16 v[76:79],a[188:191],v[156:159],v[32:35]
v_exp_f32 v126,v126
v_mfma_f32_16x16x32_bf16 v[76:79],a[184:187],v[148:151],v[76:79]
v_exp_f32 v127,v127
v_mfma_f32_16x16x32_bf16 v[76:79],a[180:183],v[140:143],v[76:79]
v_cvt_pk_bf16_f32 v120,v120,v121
v_cvt_pk_bf16_f32 v121,v122,v123
v_mfma_f32_16x16x32_bf16 v[76:79],a[176:179],v[132:135],v[76:79]
v_cvt_pk_bf16_f32 v122,v124,v125
v_cvt_pk_bf16_f32 v123,v126,v127
v_mfma_f32_16x16x32_bf16 a[64:67],a[192:195],v[96:99],a[64:67]
global_load_lds_dwordx4 v5,s[10:11] offset:3072
v_mfma_f32_16x16x32_bf16 a[64:67],a[196:199],v[104:107],a[64:67]
v_exp_f32 v64,v64
v_mfma_f32_16x16x32_bf16 a[68:71],a[200:203],v[96:99],a[68:71]
v_exp_f32 v65,v65
v_mfma_f32_16x16x32_bf16 a[68:71],a[204:207],v[104:107],a[68:71]
v_exp_f32 v66,v66
v_mfma_f32_16x16x32_bf16 a[72:75],a[208:211],v[96:99],a[72:75]
v_exp_f32 v67,v67
v_mfma_f32_16x16x32_bf16 a[72:75],a[212:215],v[104:107],a[72:75]
v_exp_f32 v68,v68
v_mfma_f32_16x16x32_bf16 a[76:79],a[216:219],v[96:99],a[76:79]
v_exp_f32 v69,v69
v_mfma_f32_16x16x32_bf16 a[76:79],a[220:223],v[104:107],a[76:79]
v_exp_f32 v70,v70
v_mfma_f32_16x16x32_bf16 a[80:83],a[224:227],v[96:99],a[80:83]
v_exp_f32 v71,v71
v_mfma_f32_16x16x32_bf16 a[80:83],a[228:231],v[104:107],a[80:83]
v_cvt_pk_bf16_f32 v64,v64,v65
v_cvt_pk_bf16_f32 v65,v66,v67
v_mfma_f32_16x16x32_bf16 a[84:87],a[232:235],v[96:99],a[84:87]
v_cvt_pk_bf16_f32 v66,v68,v69
v_cvt_pk_bf16_f32 v67,v70,v71
v_mfma_f32_16x16x32_bf16 a[84:87],a[236:239],v[104:107],a[84:87]
v_exp_f32 v72,v72
v_mfma_f32_16x16x32_bf16 a[88:91],a[240:243],v[96:99],a[88:91]
s_add_u32 s10,s20,s74
s_addc_u32 s11,s21,0
v_mfma_f32_16x16x32_bf16 a[88:91],a[244:247],v[104:107],a[88:91]
v_exp_f32 v73,v73
v_mfma_f32_16x16x32_bf16 a[92:95],a[248:251],v[96:99],a[92:95]
s_add_u32 m0,s76,0
v_exp_f32 v74,v74
v_mfma_f32_16x16x32_bf16 a[92:95],a[252:255],v[104:107],a[92:95]
v_exp_f32 v75,v75
v_mfma_f32_16x16x32_bf16 v[56:59],v[28:31],v[96:99],v[56:59]
global_load_lds_dwordx4 v4,s[10:11] nt
v_cvt_pk_bf16_f32 v72,v72,v73
v_mfma_f32_16x16x32_bf16 v[56:59],v[28:31],v[104:107],v[56:59]
v_exp_f32 v76,v76
v_mfma_f32_16x16x32_bf16 v[80:83],a[128:131],v[164:167],v[36:39]
v_exp_f32 v77,v77
v_mfma_f32_16x16x32_bf16 v[80:83],a[132:135],v[172:175],v[80:83]
v_exp_f32 v78,v78
v_mfma_f32_16x16x32_bf16 v[80:83],a[136:139],v[180:183],v[80:83]
v_exp_f32 v79,v79
v_mfma_f32_16x16x32_bf16 v[80:83],a[140:143],v[188:191],v[80:83]
v_cvt_pk_bf16_f32 v73,v74,v75
v_cvt_pk_bf16_f32 v74,v76,v77
v_mfma_f32_16x16x32_bf16 v[84:87],a[156:159],v[188:191],v[36:39]
v_cvt_pk_bf16_f32 v75,v78,v79
v_mfma_f32_16x16x32_bf16 v[84:87],a[152:155],v[180:183],v[84:87]
v_mfma_f32_16x16x32_bf16 v[84:87],a[148:151],v[172:175],v[84:87]
v_mfma_f32_16x16x32_bf16 v[84:87],a[144:147],v[164:167],v[84:87]
v_mfma_f32_16x16x32_bf16 v[88:91],a[160:163],v[164:167],v[36:39]
v_exp_f32 v80,v80
v_mfma_f32_16x16x32_bf16 v[88:91],a[164:167],v[172:175],v[88:91]
v_exp_f32 v81,v81
v_mfma_f32_16x16x32_bf16 v[88:91],a[168:171],v[180:183],v[88:91]
v_exp_f32 v82,v82
v_mfma_f32_16x16x32_bf16 v[88:91],a[172:175],v[188:191],v[88:91]
v_exp_f32 v83,v83
v_mfma_f32_16x16x32_bf16 v[92:95],a[188:191],v[188:191],v[36:39]
v_exp_f32 v84,v84
v_mfma_f32_16x16x32_bf16 v[92:95],a[184:187],v[180:183],v[92:95]
v_exp_f32 v85,v85
v_mfma_f32_16x16x32_bf16 v[92:95],a[180:183],v[172:175],v[92:95]
global_load_lds_dwordx4 v4,s[10:11] offset:1024 nt
v_cvt_pk_bf16_f32 v80,v80,v81
v_mfma_f32_16x16x32_bf16 v[92:95],a[176:179],v[164:167],v[92:95]
v_exp_f32 v86,v86
v_mfma_f32_16x16x32_bf16 a[96:99],a[192:195],v[112:115],a[96:99]
ds_read_b64_tr_b16 a[192:193],v2
ds_read_b64_tr_b16 a[194:195],v2 offset:4096
v_mfma_f32_16x16x32_bf16 a[96:99],a[196:199],v[120:123],a[96:99]
ds_read_b64_tr_b16 a[196:197],v2 offset:8192
v_cvt_pk_bf16_f32 v81,v82,v83
v_mfma_f32_16x16x32_bf16 a[100:103],a[200:203],v[112:115],a[100:103]
ds_read_b64_tr_b16 a[198:199],v2 offset:12288
v_cvt_pk_bf16_f32 v82,v84,v85
v_mfma_f32_16x16x32_bf16 a[100:103],a[204:207],v[120:123],a[100:103]
v_exp_f32 v87,v87
v_mfma_f32_16x16x32_bf16 a[104:107],a[208:211],v[112:115],a[104:107]
ds_read_b64_tr_b16 a[200:201],v3
ds_read_b64_tr_b16 a[202:203],v3 offset:4096
v_mfma_f32_16x16x32_bf16 a[104:107],a[212:215],v[120:123],a[104:107]
ds_read_b64_tr_b16 a[204:205],v3 offset:8192
v_cvt_pk_bf16_f32 v83,v86,v87
v_mfma_f32_16x16x32_bf16 a[108:111],a[216:219],v[112:115],a[108:111]
v_exp_f32 v88,v88
v_mfma_f32_16x16x32_bf16 a[108:111],a[220:223],v[120:123],a[108:111]
ds_read_b64_tr_b16 a[206:207],v3 offset:12288
ds_read_b64_tr_b16 a[208:209],v2 offset:256
v_mfma_f32_16x16x32_bf16 a[112:115],a[224:227],v[112:115],a[112:115]
v_exp_f32 v89,v89
v_mfma_f32_16x16x32_bf16 a[112:115],a[228:231],v[120:123],a[112:115]
ds_read_b64_tr_b16 a[210:211],v2 offset:4352
ds_read_b64_tr_b16 a[212:213],v2 offset:8448
v_mfma_f32_16x16x32_bf16 a[116:119],a[232:235],v[112:115],a[116:119]
ds_read_b64_tr_b16 a[214:215],v2 offset:12544
v_cvt_pk_bf16_f32 v88,v88,v89
v_mfma_f32_16x16x32_bf16 a[116:119],a[236:239],v[120:123],a[116:119]
v_exp_f32 v90,v90
v_mfma_f32_16x16x32_bf16 a[120:123],a[240:243],v[112:115],a[120:123]
ds_read_b64_tr_b16 a[216:217],v3 offset:256
ds_read_b64_tr_b16 a[218:219],v3 offset:4352
v_mfma_f32_16x16x32_bf16 a[120:123],a[244:247],v[120:123],a[120:123]
v_exp_f32 v91,v91
v_mfma_f32_16x16x32_bf16 a[124:127],a[248:251],v[112:115],a[124:127]
global_load_lds_dwordx4 v5,s[10:11] offset:2048 nt
ds_read_b64_tr_b16 a[220:221],v3 offset:8448
v_mfma_f32_16x16x32_bf16 a[124:127],a[252:255],v[120:123],a[124:127]
ds_read_b64_tr_b16 a[222:223],v3 offset:12544
ds_read_b64_tr_b16 a[224:225],v2 offset:512
v_mfma_f32_16x16x32_bf16 v[60:63],v[28:31],v[112:115],v[60:63]
ds_read_b64_tr_b16 a[226:227],v2 offset:4608
v_cvt_pk_bf16_f32 v89,v90,v91
v_mfma_f32_16x16x32_bf16 v[60:63],v[28:31],v[120:123],v[60:63]
v_exp_f32 v92,v92
v_mfma_f32_16x16x32_bf16 v[96:99],a[128:131],v[196:199],v[40:43]
ds_read_b64_tr_b16 a[228:229],v2 offset:8704
ds_read_b64_tr_b16 a[230:231],v2 offset:12800
v_mfma_f32_16x16x32_bf16 v[96:99],a[132:135],v[204:207],v[96:99]
v_exp_f32 v93,v93
v_mfma_f32_16x16x32_bf16 v[96:99],a[136:139],v[212:215],v[96:99]
ds_read_b64_tr_b16 a[232:233],v3 offset:512
ds_read_b64_tr_b16 a[234:235],v3 offset:4608
v_mfma_f32_16x16x32_bf16 v[96:99],a[140:143],v[220:223],v[96:99]
ds_read_b64_tr_b16 a[236:237],v3 offset:8704
v_cvt_pk_bf16_f32 v90,v92,v93
v_mfma_f32_16x16x32_bf16 v[100:103],a[156:159],v[220:223],v[40:43]
)A"
R"A(v_exp_f32 v94,v94
v_mfma_f32_16x16x32_bf16 v[100:103],a[152:155],v[212:215],v[100:103]
ds_read_b64_tr_b16 a[238:239],v3 offset:12800
ds_read_b64_tr_b16 a[240:241],v2 offset:768
v_mfma_f32_16x16x32_bf16 v[100:103],a[148:151],v[204:207],v[100:103]
v_exp_f32 v95,v95
v_mfma_f32_16x16x32_bf16 v[100:103],a[144:147],v[196:199],v[100:103]
ds_read_b64_tr_b16 a[242:243],v2 offset:4864
ds_read_b64_tr_b16 a[244:245],v2 offset:8960
v_mfma_f32_16x16x32_bf16 v[104:107],a[160:163],v[196:199],v[40:43]
ds_read_b64_tr_b16 a[246:247],v2 offset:13056
v_cvt_pk_bf16_f32 v91,v94,v95
v_mfma_f32_16x16x32_bf16 v[104:107],a[164:167],v[204:207],v[104:107]
v_exp_f32 v96,v96
v_mfma_f32_16x16x32_bf16 v[104:107],a[168:171],v[212:215],v[104:107]
ds_read_b64_tr_b16 a[248:249],v3 offset:768
ds_read_b64_tr_b16 a[250:251],v3 offset:4864
v_mfma_f32_16x16x32_bf16 v[104:107],a[172:175],v[220:223],v[104:107]
v_exp_f32 v97,v97
v_mfma_f32_16x16x32_bf16 v[108:111],a[188:191],v[220:223],v[40:43]
global_load_lds_dwordx4 v5,s[10:11] offset:3072
ds_read_b64_tr_b16 a[252:253],v3 offset:8960
v_mfma_f32_16x16x32_bf16 v[108:111],a[184:187],v[212:215],v[108:111]
ds_read_b64_tr_b16 a[254:255],v3 offset:13056
v_cvt_pk_bf16_f32 v96,v96,v97
v_mfma_f32_16x16x32_bf16 v[108:111],a[180:183],v[204:207],v[108:111]
v_exp_f32 v98,v98
v_mfma_f32_16x16x32_bf16 v[108:111],a[176:179],v[196:199],v[108:111]
s_waitcnt lgkmcnt(5)
v_exp_f32 v99,v99
v_mfma_f32_16x16x32_bf16 a[0:3],a[192:195],v[64:67],a[0:3]
v_exp_f32 v100,v100
v_mfma_f32_16x16x32_bf16 a[0:3],a[196:199],v[72:75],a[0:3]
v_exp_f32 v101,v101
v_mfma_f32_16x16x32_bf16 a[4:7],a[200:203],v[64:67],a[4:7]
v_exp_f32 v102,v102
v_mfma_f32_16x16x32_bf16 a[4:7],a[204:207],v[72:75],a[4:7]
v_exp_f32 v103,v103
v_mfma_f32_16x16x32_bf16 a[8:11],a[208:211],v[64:67],a[8:11]
v_cvt_pk_bf16_f32 v97,v98,v99
v_cvt_pk_bf16_f32 v98,v100,v101
v_mfma_f32_16x16x32_bf16 a[8:11],a[212:215],v[72:75],a[8:11]
v_cvt_pk_bf16_f32 v99,v102,v103
v_mfma_f32_16x16x32_bf16 a[12:15],a[216:219],v[64:67],a[12:15]
v_mfma_f32_16x16x32_bf16 a[12:15],a[220:223],v[72:75],a[12:15]
v_mfma_f32_16x16x32_bf16 a[16:19],a[224:227],v[64:67],a[16:19]
v_mfma_f32_16x16x32_bf16 a[16:19],a[228:231],v[72:75],a[16:19]
v_mfma_f32_16x16x32_bf16 a[20:23],a[232:235],v[64:67],a[20:23]
s_getreg_b32 s92,hwreg(HW_REG_IB_STS,0,4)
s_min_u32 s4,s92,s6
s_mov_b32 s6,s92
v_mfma_f32_16x16x32_bf16 a[20:23],a[236:239],v[72:75],a[20:23]
s_cmp_ge_u32 s4,s9
s_cselect_b32 s4,15,s5
v_mfma_f32_16x16x32_bf16 a[24:27],a[240:243],v[64:67],a[24:27]
s_waitcnt lgkmcnt(0)
v_mfma_f32_16x16x32_bf16 a[24:27],a[244:247],v[72:75],a[24:27]
v_mfma_f32_16x16x32_bf16 a[28:31],a[248:251],v[64:67],a[28:31]
v_mfma_f32_16x16x32_bf16 a[28:31],a[252:255],v[72:75],a[28:31]
s_getpc_b64 s[92:93]
s_cmp_lg_u32 s4,0
s_cbranch_scc1 L_pace_%=
v_mfma_f32_16x16x32_bf16 v[48:51],v[28:31],v[64:67],v[48:51]
v_mfma_f32_16x16x32_bf16 v[48:51],v[28:31],v[72:75],v[48:51]
s_sub_u32 s70,s70,1
s_cbranch_scc0 L_a0_%=
L_nx0_%=:
s_waitcnt vmcnt(12)
s_barrier
v_mfma_f32_16x16x32_bf16 v[112:115],a[128:131],v[228:231],v[44:47]
s_add_u32 s10,s24,s74
s_addc_u32 s11,s25,0
s_add_u32 m0,s76,16384
v_mfma_f32_16x16x32_bf16 v[112:115],a[132:135],v[236:239],v[112:115]
s_add_u32 s75,s75,16384
s_add_u32 s74,s74,16384
ds_read_b128 v[32:35],v7
v_mfma_f32_16x16x32_bf16 v[112:115],a[136:139],v[244:247],v[112:115]
s_and_b32 s74,s74,0x3ffff
global_load_lds_dwordx4 v4,s[10:11]
v_mfma_f32_16x16x32_bf16 v[112:115],a[140:143],v[252:255],v[112:115]
s_and_b32 s69,s75,0xfffc0000
s_or_b32 s74,s74,s69
ds_read_b128 a[128:131],v1
v_mfma_f32_16x16x32_bf16 v[116:119],a[156:159],v[252:255],v[44:47]
s_sub_u32 s76,s76,32768
v_exp_f32 v104,v104
v_mfma_f32_16x16x32_bf16 v[116:119],a[152:155],v[244:247],v[116:119]
s_and_b32 s76,s76,0x1ffff
ds_read_b128 a[132:135],v1 offset:256
v_add_u32 v2,s78,v2
v_mfma_f32_16x16x32_bf16 v[116:119],a[148:151],v[236:239],v[116:119]
s_min_u32 s7,s7,1
v_exp_f32 v105,v105
v_mfma_f32_16x16x32_bf16 v[116:119],a[144:147],v[228:231],v[116:119]
s_min_u32 s4,s5,1
ds_read_b128 a[136:139],v1 offset:512
v_cvt_pk_bf16_f32 v104,v104,v105
v_mfma_f32_16x16x32_bf16 v[120:123],a[160:163],v[228:231],v[44:47]
v_exp_f32 v106,v106
v_mfma_f32_16x16x32_bf16 v[120:123],a[164:167],v[236:239],v[120:123]
ds_read_b128 a[140:143],v1 offset:768
v_add_u32 v3,s78,v3
v_mfma_f32_16x16x32_bf16 v[120:123],a[168:171],v[244:247],v[120:123]
s_sub_u32 s78,s77,32768
v_exp_f32 v107,v107
v_mfma_f32_16x16x32_bf16 v[120:123],a[172:175],v[252:255],v[120:123]
s_and_b32 s78,s78,0x1ffff
ds_read_b128 a[156:159],v1 offset:4864
v_cvt_pk_bf16_f32 v105,v106,v107
v_mfma_f32_16x16x32_bf16 v[124:127],a[188:191],v[252:255],v[44:47]
s_sub_u32 s78,s78,s77
v_exp_f32 v108,v108
v_mfma_f32_16x16x32_bf16 v[124:127],a[184:187],v[244:247],v[124:127]
s_add_u32 s77,s77,s78
ds_read_b128 a[152:155],v1 offset:4608
v_accvgpr_read_b32 v8,a0
v_mfma_f32_16x16x32_bf16 v[124:127],a[180:183],v[236:239],v[124:127]
v_exp_f32 v109,v109
v_mfma_f32_16x16x32_bf16 v[124:127],a[176:179],v[228:231],v[124:127]
ds_read_b128 a[148:151],v1 offset:4352
v_cvt_pk_bf16_f32 v106,v108,v109
v_mfma_f32_16x16x32_bf16 v[52:55],v[28:31],v[80:83],v[52:55]
global_load_lds_dwordx4 v5,s[10:11] offset:1024
v_accvgpr_read_b32 v9,a1
v_mfma_f32_16x16x32_bf16 v[52:55],v[28:31],v[88:91],v[52:55]
ds_read_b128 a[144:147],v1 offset:4096
v_accvgpr_read_b32 v10,a2
v_mfma_f32_16x16x32_bf16 a[32:35],a[192:195],v[80:83],a[32:35]
v_exp_f32 v110,v110
v_mfma_f32_16x16x32_bf16 a[32:35],a[196:199],v[88:91],a[32:35]
ds_read_b128 a[160:163],v1 offset:8192
v_accvgpr_read_b32 v11,a3
v_mfma_f32_16x16x32_bf16 a[36:39],a[200:203],v[80:83],a[36:39]
v_exp_f32 v111,v111
v_mfma_f32_16x16x32_bf16 a[36:39],a[204:207],v[88:91],a[36:39]
ds_read_b128 a[164:167],v1 offset:8448
v_cvt_pk_bf16_f32 v107,v110,v111
v_mfma_f32_16x16x32_bf16 a[40:43],a[208:211],v[80:83],a[40:43]
v_exp_f32 v112,v112
v_mfma_f32_16x16x32_bf16 a[40:43],a[212:215],v[88:91],a[40:43]
ds_read_b128 a[168:171],v1 offset:8704
v_accvgpr_read_b32 v12,a4
v_mfma_f32_16x16x32_bf16 a[44:47],a[216:219],v[80:83],a[44:47]
v_exp_f32 v113,v113
v_mfma_f32_16x16x32_bf16 a[44:47],a[220:223],v[88:91],a[44:47]
ds_read_b128 a[172:175],v1 offset:8960
v_cvt_pk_bf16_f32 v112,v112,v113
v_mfma_f32_16x16x32_bf16 a[48:51],a[224:227],v[80:83],a[48:51]
v_exp_f32 v114,v114
v_mfma_f32_16x16x32_bf16 a[48:51],a[228:231],v[88:91],a[48:51]
ds_read_b128 a[188:191],v1 offset:13056
v_accvgpr_read_b32 v13,a5
v_mfma_f32_16x16x32_bf16 a[52:55],a[232:235],v[80:83],a[52:55]
v_exp_f32 v115,v115
v_mfma_f32_16x16x32_bf16 a[52:55],a[236:239],v[88:91],a[52:55]
ds_read_b128 a[184:187],v1 offset:12800
v_cvt_pk_bf16_f32 v113,v114,v115
v_mfma_f32_16x16x32_bf16 a[56:59],a[240:243],v[80:83],a[56:59]
global_load_lds_dwordx4 v4,s[10:11] offset:2048
v_accvgpr_read_b32 v14,a6
v_mfma_f32_16x16x32_bf16 a[56:59],a[244:247],v[88:91],a[56:59]
ds_read_b128 a[180:183],v1 offset:12544
v_accvgpr_read_b32 v15,a7
v_mfma_f32_16x16x32_bf16 a[60:63],a[248:251],v[80:83],a[60:63]
v_exp_f32 v116,v116
v_mfma_f32_16x16x32_bf16 a[60:63],a[252:255],v[88:91],a[60:63]
s_waitcnt lgkmcnt(3)
ds_read_b128 a[176:179],v1 offset:12288
v_max_f32 v24,0x00800000,v48
v_mfma_f32_16x16x32_bf16 v[64:67],a[128:131],v[128:131],v[32:35]
v_exp_f32 v117,v117
v_mfma_f32_16x16x32_bf16 v[64:67],a[132:135],v[136:139],v[64:67]
ds_read_b128 v[36:39],v7 offset:1024
v_cvt_pk_bf16_f32 v114,v116,v117
v_mfma_f32_16x16x32_bf16 v[64:67],a[136:139],v[144:147],v[64:67]
v_exp_f32 v118,v118
v_mfma_f32_16x16x32_bf16 v[64:67],a[140:143],v[152:155],v[64:67]
v_exp_f32 v119,v119
v_mfma_f32_16x16x32_bf16 v[68:71],a[156:159],v[152:155],v[32:35]
ds_read_b128 v[40:43],v7 offset:2048
v_cvt_pk_bf16_f32 v115,v118,v119
v_mfma_f32_16x16x32_bf16 v[68:71],a[152:155],v[144:147],v[68:71]
v_exp_f32 v120,v120
v_mfma_f32_16x16x32_bf16 v[68:71],a[148:151],v[136:139],v[68:71]
v_exp_f32 v121,v121
v_mfma_f32_16x16x32_bf16 v[68:71],a[144:147],v[128:131],v[68:71]
v_exp_f32 v122,v122
v_mfma_f32_16x16x32_bf16 v[72:75],a[160:163],v[128:131],v[32:35]
v_exp_f32 v123,v123
v_mfma_f32_16x16x32_bf16 v[72:75],a[164:167],v[136:139],v[72:75]
v_exp_f32 v124,v124
v_mfma_f32_16x16x32_bf16 v[72:75],a[168:171],v[144:147],v[72:75]
s_waitcnt lgkmcnt(1)
v_exp_f32 v125,v125
v_mfma_f32_16x16x32_bf16 v[72:75],a[172:175],v[152:155],v[72:75]
global_load_lds_dwordx4 v5,s[10:11] offset:3072
v_cvt_pk_bf16_f32 v120,v120,v121
v_mfma_f32_16x16x32_bf16 v[76:79],a[188:191],v[152:155],v[32:35]
v_exp_f32 v126,v126
v_mfma_f32_16x16x32_bf16 v[76:79],a[184:187],v[144:147],v[76:79]
L_pd7_%=:
s_cbranch_execz L_trt0_%=
v_exp_f32 v127,v127
v_mfma_f32_16x16x32_bf16 v[76:79],a[180:183],v[136:139],v[76:79]
v_cvt_pk_bf16_f32 v121,v122,v123
v_cvt_pk_bf16_f32 v122,v124,v125
v_mfma_f32_16x16x32_bf16 v[76:79],a[176:179],v[128:131],v[76:79]
v_cvt_pk_bf16_f32 v123,v126,v127
v_accvgpr_read_b32 v16,a8
v_mfma_f32_16x16x32_bf16 v[56:59],v[28:31],v[96:99],v[56:59]
v_exp_f32 v64,v64
v_mfma_f32_16x16x32_bf16 v[56:59],v[28:31],v[104:107],v[56:59]
v_exp_f32 v65,v65
v_mfma_f32_16x16x32_bf16 a[64:67],a[192:195],v[96:99],a[64:67]
v_exp_f32 v66,v66
v_mfma_f32_16x16x32_bf16 a[64:67],a[196:199],v[104:107],a[64:67]
v_exp_f32 v67,v67
v_mfma_f32_16x16x32_bf16 a[68:71],a[200:203],v[96:99],a[68:71]
v_exp_f32 v68,v68
v_mfma_f32_16x16x32_bf16 a[68:71],a[204:207],v[104:107],a[68:71]
s_add_u32 s10,s20,s74
s_addc_u32 s11,s21,0
v_cvt_pk_bf16_f32 v64,v64,v65
v_mfma_f32_16x16x32_bf16 a[72:75],a[208:211],v[96:99],a[72:75]
v_exp_f32 v69,v69
v_mfma_f32_16x16x32_bf16 a[72:75],a[212:215],v[104:107],a[72:75]
s_add_u32 m0,s76,0
v_exp_f32 v70,v70
v_mfma_f32_16x16x32_bf16 a[76:79],a[216:219],v[96:99],a[76:79]
v_exp_f32 v71,v71
v_mfma_f32_16x16x32_bf16 a[76:79],a[220:223],v[104:107],a[76:79]
global_load_lds_dwordx4 v4,s[10:11] nt
v_cvt_pk_bf16_f32 v65,v66,v67
v_mfma_f32_16x16x32_bf16 a[80:83],a[224:227],v[96:99],a[80:83]
v_cvt_pk_bf16_f32 v66,v68,v69
v_cvt_pk_bf16_f32 v67,v70,v71
v_mfma_f32_16x16x32_bf16 a[80:83],a[228:231],v[104:107],a[80:83]
v_exp_f32 v72,v72
v_mfma_f32_16x16x32_bf16 a[84:87],a[232:235],v[96:99],a[84:87]
v_exp_f32 v73,v73
v_mfma_f32_16x16x32_bf16 a[84:87],a[236:239],v[104:107],a[84:87]
v_exp_f32 v74,v74
v_mfma_f32_16x16x32_bf16 a[88:91],a[240:243],v[96:99],a[88:91]
v_exp_f32 v75,v75
v_mfma_f32_16x16x32_bf16 a[88:91],a[244:247],v[104:107],a[88:91]
v_exp_f32 v76,v76
v_mfma_f32_16x16x32_bf16 a[92:95],a[248:251],v[96:99],a[92:95]
v_exp_f32 v77,v77
v_mfma_f32_16x16x32_bf16 a[92:95],a[252:255],v[104:107],a[92:95]
v_exp_f32 v78,v78
v_mfma_f32_16x16x32_bf16 v[80:83],a[128:131],v[160:163],v[36:39]
v_exp_f32 v79,v79
v_mfma_f32_16x16x32_bf16 v[80:83],a[132:135],v[168:171],v[80:83]
v_cvt_pk_bf16_f32 v72,v72,v73
v_cvt_pk_bf16_f32 v73,v74,v75
v_mfma_f32_16x16x32_bf16 v[80:83],a[136:139],v[176:179],v[80:83]
v_cvt_pk_bf16_f32 v74,v76,v77
v_cvt_pk_bf16_f32 v75,v78,v79
v_mfma_f32_16x16x32_bf16 v[80:83],a[140:143],v[184:187],v[80:83]
v_rcp_f32 v24,v24
v_mfma_f32_16x16x32_bf16 v[84:87],a[156:159],v[184:187],v[36:39]
v_accvgpr_read_b32 v17,a9
v_accvgpr_read_b32 v18,a10
v_mfma_f32_16x16x32_bf16 v[84:87],a[152:155],v[176:179],v[84:87]
v_accvgpr_read_b32 v19,a11
v_mul_f32 v8,v8,v24
v_mfma_f32_16x16x32_bf16 v[84:87],a[148:151],v[168:171],v[84:87]
global_load_lds_dwordx4 v4,s[10:11] offset:1024 nt
v_mul_f32 v9,v9,v24
v_mfma_f32_16x16x32_bf16 v[84:87],a[144:147],v[160:163],v[84:87]
v_mul_f32 v10,v10,v24
v_mul_f32 v11,v11,v24
v_mfma_f32_16x16x32_bf16 v[88:91],a[160:163],v[160:163],v[36:39]
v_mul_f32 v12,v12,v24
v_mul_f32 v13,v13,v24
v_mfma_f32_16x16x32_bf16 v[88:91],a[164:167],v[168:171],v[88:91]
v_mul_f32 v14,v14,v24
v_mul_f32 v15,v15,v24
v_mfma_f32_16x16x32_bf16 v[88:91],a[168:171],v[176:179],v[88:91]
v_cvt_pk_bf16_f32 v8,v8,v9
v_cvt_pk_bf16_f32 v9,v10,v11
v_mfma_f32_16x16x32_bf16 v[88:91],a[172:175],v[184:187],v[88:91]
v_cvt_pk_bf16_f32 v10,v12,v13
v_cvt_pk_bf16_f32 v11,v14,v15
v_mfma_f32_16x16x32_bf16 v[92:95],a[188:191],v[184:187],v[36:39]
ds_write_b128 v7,v[8:11] offset:6144
v_mfma_f32_16x16x32_bf16 v[92:95],a[184:187],v[176:179],v[92:95]
v_accvgpr_read_b32 v20,a12
v_accvgpr_read_b32 v21,a13
v_mfma_f32_16x16x32_bf16 v[92:95],a[180:183],v[168:171],v[92:95]
)A"
R"A(v_accvgpr_read_b32 v22,a14
v_accvgpr_read_b32 v23,a15
v_mfma_f32_16x16x32_bf16 v[92:95],a[176:179],v[160:163],v[92:95]
v_accvgpr_read_b32 v8,a16
v_accvgpr_read_b32 v9,a17
v_mfma_f32_16x16x32_bf16 v[60:63],v[28:31],v[112:115],v[60:63]
v_accvgpr_read_b32 v10,a18
v_accvgpr_read_b32 v11,a19
v_mfma_f32_16x16x32_bf16 v[60:63],v[28:31],v[120:123],v[60:63]
v_mul_f32 v16,v16,v24
v_mul_f32 v17,v17,v24
v_mfma_f32_16x16x32_bf16 a[96:99],a[192:195],v[112:115],a[96:99]
ds_read_b64_tr_b16 a[192:193],v2
ds_read_b64_tr_b16 a[194:195],v2 offset:4096
v_mfma_f32_16x16x32_bf16 a[96:99],a[196:199],v[120:123],a[96:99]
ds_read_b64_tr_b16 a[196:197],v2 offset:8192
v_mul_f32 v18,v18,v24
v_mfma_f32_16x16x32_bf16 a[100:103],a[200:203],v[112:115],a[100:103]
global_load_lds_dwordx4 v5,s[10:11] offset:2048 nt
ds_read_b64_tr_b16 a[198:199],v2 offset:12288
v_mfma_f32_16x16x32_bf16 a[100:103],a[204:207],v[120:123],a[100:103]
ds_read_b64_tr_b16 a[200:201],v3
v_mul_f32 v19,v19,v24
v_mfma_f32_16x16x32_bf16 a[104:107],a[208:211],v[112:115],a[104:107]
ds_read_b64_tr_b16 a[202:203],v3 offset:4096
ds_read_b64_tr_b16 a[204:205],v3 offset:8192
v_mfma_f32_16x16x32_bf16 a[104:107],a[212:215],v[120:123],a[104:107]
ds_read_b64_tr_b16 a[206:207],v3 offset:12288
v_mul_f32 v20,v20,v24
v_mfma_f32_16x16x32_bf16 a[108:111],a[216:219],v[112:115],a[108:111]
ds_read_b64_tr_b16 a[208:209],v2 offset:256
v_mul_f32 v21,v21,v24
v_mfma_f32_16x16x32_bf16 a[108:111],a[220:223],v[120:123],a[108:111]
ds_read_b64_tr_b16 a[210:211],v2 offset:4352
v_mul_f32 v22,v22,v24
v_mfma_f32_16x16x32_bf16 a[112:115],a[224:227],v[112:115],a[112:115]
ds_read_b64_tr_b16 a[212:213],v2 offset:8448
ds_read_b64_tr_b16 a[214:215],v2 offset:12544
v_mfma_f32_16x16x32_bf16 a[112:115],a[228:231],v[120:123],a[112:115]
ds_read_b64_tr_b16 a[216:217],v3 offset:256
v_mul_f32 v23,v23,v24
v_mfma_f32_16x16x32_bf16 a[116:119],a[232:235],v[112:115],a[116:119]
ds_read_b64_tr_b16 a[218:219],v3 offset:4352
v_cvt_pk_bf16_f32 v16,v16,v17
v_mfma_f32_16x16x32_bf16 a[116:119],a[236:239],v[120:123],a[116:119]
ds_read_b64_tr_b16 a[220:221],v3 offset:8448
v_cvt_pk_bf16_f32 v17,v18,v19
v_mfma_f32_16x16x32_bf16 a[120:123],a[240:243],v[112:115],a[120:123]
ds_read_b64_tr_b16 a[222:223],v3 offset:12544
ds_read_b64_tr_b16 a[224:225],v2 offset:512
v_mfma_f32_16x16x32_bf16 a[120:123],a[244:247],v[120:123],a[120:123]
ds_read_b64_tr_b16 a[226:227],v2 offset:4608
v_cvt_pk_bf16_f32 v18,v20,v21
v_mfma_f32_16x16x32_bf16 a[124:127],a[248:251],v[112:115],a[124:127]
ds_read_b64_tr_b16 a[228:229],v2 offset:8704
v_cvt_pk_bf16_f32 v19,v22,v23
v_mfma_f32_16x16x32_bf16 a[124:127],a[252:255],v[120:123],a[124:127]
s_waitcnt lgkmcnt(9)
ds_read_b64_tr_b16 a[230:231],v2 offset:12800
v_accvgpr_read_b32 v12,a20
v_mfma_f32_16x16x32_bf16 v[96:99],a[128:131],v[192:195],v[40:43]
ds_read_b64_tr_b16 a[232:233],v3 offset:512
ds_read_b64_tr_b16 a[234:235],v3 offset:4608
v_mfma_f32_16x16x32_bf16 v[96:99],a[132:135],v[200:203],v[96:99]
global_load_lds_dwordx4 v5,s[10:11] offset:3072
ds_read_b64_tr_b16 a[236:237],v3 offset:8704
v_mfma_f32_16x16x32_bf16 v[96:99],a[136:139],v[208:211],v[96:99]
ds_read_b64_tr_b16 a[238:239],v3 offset:12800
v_accvgpr_read_b32 v13,a21
v_mfma_f32_16x16x32_bf16 v[96:99],a[140:143],v[216:219],v[96:99]
ds_read_b64_tr_b16 a[240:241],v2 offset:768
v_accvgpr_read_b32 v14,a22
v_mfma_f32_16x16x32_bf16 v[100:103],a[156:159],v[216:219],v[40:43]
ds_read_b64_tr_b16 a[242:243],v2 offset:4864
ds_read_b64_tr_b16 a[244:245],v2 offset:8960
v_mfma_f32_16x16x32_bf16 v[100:103],a[152:155],v[208:211],v[100:103]
ds_read_b64_tr_b16 a[246:247],v2 offset:13056
v_accvgpr_read_b32 v15,a23
v_mfma_f32_16x16x32_bf16 v[100:103],a[148:151],v[200:203],v[100:103]
s_cmp_le_u32 s5,3
s_cselect_b32 s5,s4,s5
ds_read_b64_tr_b16 a[248:249],v3 offset:768
v_mfma_f32_16x16x32_bf16 v[100:103],a[144:147],v[192:195],v[100:103]
ds_read_b64_tr_b16 a[250:251],v3 offset:4864
v_accvgpr_read_b32 v20,a28
v_mfma_f32_16x16x32_bf16 v[104:107],a[160:163],v[192:195],v[40:43]
ds_read_b64_tr_b16 a[252:253],v3 offset:8960
ds_read_b64_tr_b16 a[254:255],v3 offset:13056
v_mfma_f32_16x16x32_bf16 v[104:107],a[164:167],v[200:203],v[104:107]
v_accvgpr_read_b32 v21,a29
v_accvgpr_read_b32 v22,a30
v_mfma_f32_16x16x32_bf16 v[104:107],a[168:171],v[208:211],v[104:107]
ds_write_b128 v7,v[16:19] offset:7168
v_mfma_f32_16x16x32_bf16 v[104:107],a[172:175],v[216:219],v[104:107]
s_add_u32 s10,s24,s74
s_addc_u32 s11,s25,0
v_accvgpr_read_b32 v23,a31
v_mfma_f32_16x16x32_bf16 v[108:111],a[188:191],v[216:219],v[40:43]
v_accvgpr_read_b32 v16,a24
v_accvgpr_read_b32 v17,a25
v_mfma_f32_16x16x32_bf16 v[108:111],a[184:187],v[208:211],v[108:111]
s_add_u32 m0,s76,16384
v_accvgpr_read_b32 v18,a26
v_accvgpr_read_b32 v19,a27
v_mfma_f32_16x16x32_bf16 v[108:111],a[180:183],v[200:203],v[108:111]
v_cmp_nlt_f32 s[54:55],v48,s73
v_cmp_nlt_f32 s[56:57],v52,s73
v_mfma_f32_16x16x32_bf16 v[108:111],a[176:179],v[192:195],v[108:111]
s_or_b64 s[54:55],s[54:55],s[56:57]
global_load_lds_dwordx4 v4,s[10:11]
v_mfma_f32_16x16x32_bf16 a[0:3],a[192:195],v[64:67],0
v_cmp_nlt_f32 s[56:57],v56,s73
v_mul_f32 v8,v8,v24
v_mfma_f32_16x16x32_bf16 a[0:3],a[196:199],v[72:75],a[0:3]
s_or_b64 s[54:55],s[54:55],s[56:57]
v_mul_f32 v9,v9,v24
v_mul_f32 v10,v10,v24
v_mfma_f32_16x16x32_bf16 a[4:7],a[200:203],v[64:67],0
v_mul_f32 v11,v11,v24
v_mul_f32 v12,v12,v24
v_mfma_f32_16x16x32_bf16 a[4:7],a[204:207],v[72:75],a[4:7]
v_mul_f32 v13,v13,v24
v_mul_f32 v14,v14,v24
v_mfma_f32_16x16x32_bf16 a[8:11],a[208:211],v[64:67],0
s_waitcnt lgkmcnt(0)
v_mul_f32 v15,v15,v24
v_cvt_pk_bf16_f32 v8,v8,v9
v_mfma_f32_16x16x32_bf16 a[8:11],a[212:215],v[72:75],a[8:11]
v_cvt_pk_bf16_f32 v9,v10,v11
v_cvt_pk_bf16_f32 v10,v12,v13
v_mfma_f32_16x16x32_bf16 a[12:15],a[216:219],v[64:67],0
v_cvt_pk_bf16_f32 v11,v14,v15
v_mul_f32 v16,v16,v24
v_mfma_f32_16x16x32_bf16 a[12:15],a[220:223],v[72:75],a[12:15]
v_mul_f32 v17,v17,v24
v_mul_f32 v18,v18,v24
v_mfma_f32_16x16x32_bf16 a[16:19],a[224:227],v[64:67],0
v_mul_f32 v19,v19,v24
v_mul_f32 v20,v20,v24
v_mfma_f32_16x16x32_bf16 a[16:19],a[228:231],v[72:75],a[16:19]
v_mul_f32 v21,v21,v24
v_mul_f32 v22,v22,v24
v_mfma_f32_16x16x32_bf16 a[20:23],a[232:235],v[64:67],0
s_getreg_b32 s92,hwreg(HW_REG_IB_STS,0,4)
s_min_u32 s4,s92,s6
s_mov_b32 s6,s92
v_mfma_f32_16x16x32_bf16 a[20:23],a[236:239],v[72:75],a[20:23]
s_cmp_gt_u32 s4,s9
s_cselect_b32 s4,15,s5
v_mul_f32 v23,v23,v24
v_mfma_f32_16x16x32_bf16 a[24:27],a[240:243],v[64:67],0
v_cvt_pk_bf16_f32 v16,v16,v17
v_cvt_pk_bf16_f32 v17,v18,v19
v_mfma_f32_16x16x32_bf16 a[24:27],a[244:247],v[72:75],a[24:27]
v_cvt_pk_bf16_f32 v18,v20,v21
v_cvt_pk_bf16_f32 v19,v22,v23
v_mfma_f32_16x16x32_bf16 a[28:31],a[248:251],v[64:67],0
global_load_lds_dwordx4 v5,s[10:11] offset:1024
v_cmp_nlt_f32 s[56:57],v60,s73
v_mfma_f32_16x16x32_bf16 a[28:31],a[252:255],v[72:75],a[28:31]
s_or_b64 s[54:55],s[54:55],s[56:57]
v_exp_f32 v80,v80
v_mfma_f32_16x16x32_bf16 v[48:51],v[28:31],v[64:67],0
s_getpc_b64 s[92:93]
s_cmp_lg_u32 s4,0
s_cbranch_scc1 L_pace_%=
v_exp_f32 v81,v81
v_exp_f32 v82,v82
v_exp_f32 v83,v83
v_exp_f32 v84,v84
v_exp_f32 v85,v85
v_exp_f32 v86,v86
v_exp_f32 v87,v87
v_exp_f32 v88,v88
v_exp_f32 v89,v89
v_mfma_f32_16x16x32_bf16 v[48:51],v[28:31],v[72:75],v[48:51]
ds_write_b128 v7,v[8:11]
v_add_u32 v1,s78,v1
v_max_f32 v25,0x00800000,v52
v_rcp_f32 v25,v25
v_max_f32 v26,0x00800000,v56
v_rcp_f32 v26,v26
v_cvt_pk_bf16_f32 v80,v80,v81
v_cvt_pk_bf16_f32 v81,v82,v83
v_cvt_pk_bf16_f32 v82,v84,v85
v_cvt_pk_bf16_f32 v83,v86,v87
v_cvt_pk_bf16_f32 v88,v88,v89
v_max_f32 v27,0x00800000,v60
v_rcp_f32 v27,v27
ds_write_b128 v7,v[16:19] offset:1024
ds_read_b128 v[44:47],v7 offset:3072
s_cmp_lg_u64 s[54:55],0
s_cbranch_scc1 L_slow0_%=
L_ok0_%=:
s_waitcnt vmcnt(14)
s_barrier
s_waitcnt lgkmcnt(0)
v_mfma_f32_16x16x32_bf16 v[112:115],a[128:131],v[224:227],v[44:47]
s_add_u32 s10,s24,s74
s_addc_u32 s11,s25,0
s_add_u32 m0,s76,16384
v_mfma_f32_16x16x32_bf16 v[112:115],a[132:135],v[232:235],v[112:115]
s_add_u32 s75,s75,16384
v_exp_f32 v90,v90
v_mfma_f32_16x16x32_bf16 v[112:115],a[136:139],v[240:243],v[112:115]
global_load_lds_dwordx4 v4,s[10:11] offset:2048
ds_read_b128 a[128:131],v1
v_mfma_f32_16x16x32_bf16 v[112:115],a[140:143],v[248:251],v[112:115]
s_add_u32 s74,s74,16384
v_exp_f32 v91,v91
v_mfma_f32_16x16x32_bf16 v[116:119],a[156:159],v[248:251],v[44:47]
s_and_b32 s74,s74,0x3ffff
v_exp_f32 v92,v92
v_mfma_f32_16x16x32_bf16 v[116:119],a[152:155],v[240:243],v[116:119]
s_and_b32 s69,s75,0xfffc0000
v_exp_f32 v93,v93
v_mfma_f32_16x16x32_bf16 v[116:119],a[148:151],v[232:235],v[116:119]
s_or_b32 s74,s74,s69
v_exp_f32 v94,v94
v_mfma_f32_16x16x32_bf16 v[116:119],a[144:147],v[224:227],v[116:119]
s_sub_u32 s76,s76,32768
v_exp_f32 v95,v95
v_mfma_f32_16x16x32_bf16 v[120:123],a[160:163],v[224:227],v[44:47]
s_and_b32 s76,s76,0x1ffff
v_cvt_pk_bf16_f32 v89,v90,v91
v_cvt_pk_bf16_f32 v90,v92,v93
v_mfma_f32_16x16x32_bf16 v[120:123],a[164:167],v[232:235],v[120:123]
s_add_u32 s75,s75,16384
ds_read_b128 a[132:135],v1 offset:256
v_cvt_pk_bf16_f32 v91,v94,v95
v_mfma_f32_16x16x32_bf16 v[120:123],a[168:171],v[240:243],v[120:123]
s_and_b32 s69,s75,0xfffc0000
v_exp_f32 v96,v96
v_mfma_f32_16x16x32_bf16 v[120:123],a[172:175],v[248:251],v[120:123]
s_add_u32 s75,s75,16384
ds_read_b128 a[136:139],v1 offset:512
v_accvgpr_read_b32 v16,a32
v_mfma_f32_16x16x32_bf16 v[124:127],a[188:191],v[248:251],v[44:47]
v_exp_f32 v97,v97
v_mfma_f32_16x16x32_bf16 v[124:127],a[184:187],v[240:243],v[124:127]
ds_read_b128 a[140:143],v1 offset:768
v_cvt_pk_bf16_f32 v96,v96,v97
v_mfma_f32_16x16x32_bf16 v[124:127],a[180:183],v[232:235],v[124:127]
v_exp_f32 v98,v98
v_mfma_f32_16x16x32_bf16 v[124:127],a[176:179],v[224:227],v[124:127]
ds_read_b128 a[156:159],v1 offset:4864
v_accvgpr_read_b32 v17,a33
v_mfma_f32_16x16x32_bf16 v[132:135],a[192:195],v[80:83],0
global_load_lds_dwordx4 v5,s[10:11] offset:3072
v_accvgpr_read_b32 v18,a34
v_mfma_f32_16x16x32_bf16 v[132:135],a[196:199],v[88:91],v[132:135]
ds_read_b128 a[152:155],v1 offset:4608
v_accvgpr_read_b32 v19,a35
v_mfma_f32_16x16x32_bf16 v[140:143],a[200:203],v[80:83],0
v_exp_f32 v99,v99
v_mfma_f32_16x16x32_bf16 v[140:143],a[204:207],v[88:91],v[140:143]
ds_read_b128 a[148:151],v1 offset:4352
v_cvt_pk_bf16_f32 v97,v98,v99
v_mfma_f32_16x16x32_bf16 v[148:151],a[208:211],v[80:83],0
v_exp_f32 v100,v100
v_mfma_f32_16x16x32_bf16 v[148:151],a[212:215],v[88:91],v[148:151]
ds_read_b128 a[144:147],v1 offset:4096
v_accvgpr_read_b32 v20,a36
v_mfma_f32_16x16x32_bf16 v[156:159],a[216:219],v[80:83],0
v_exp_f32 v101,v101
v_mfma_f32_16x16x32_bf16 v[156:159],a[220:223],v[88:91],v[156:159]
ds_read_b128 a[160:163],v1 offset:8192
v_cvt_pk_bf16_f32 v98,v100,v101
v_mfma_f32_16x16x32_bf16 v[164:167],a[224:227],v[80:83],0
v_exp_f32 v102,v102
v_mfma_f32_16x16x32_bf16 v[164:167],a[228:231],v[88:91],v[164:167]
ds_read_b128 a[164:167],v1 offset:8448
v_accvgpr_read_b32 v21,a37
v_mfma_f32_16x16x32_bf16 v[172:175],a[232:235],v[80:83],0
s_add_u32 s10,s20,s74
s_addc_u32 s11,s21,0
v_accvgpr_read_b32 v22,a38
v_mfma_f32_16x16x32_bf16 v[172:175],a[236:239],v[88:91],v[172:175]
ds_read_b128 a[168:171],v1 offset:8704
v_accvgpr_read_b32 v23,a39
v_mfma_f32_16x16x32_bf16 v[180:183],a[240:243],v[80:83],0
s_add_u32 m0,s76,0
v_exp_f32 v103,v103
v_mfma_f32_16x16x32_bf16 v[180:183],a[244:247],v[88:91],v[180:183]
ds_read_b128 a[172:175],v1 offset:8960
v_cvt_pk_bf16_f32 v99,v102,v103
v_mfma_f32_16x16x32_bf16 v[188:191],a[248:251],v[80:83],0
global_load_lds_dwordx4 v4,s[10:11] nt
v_mul_f32 v16,v16,v25
v_mfma_f32_16x16x32_bf16 v[188:191],a[252:255],v[88:91],v[188:191]
ds_read_b128 a[188:191],v1 offset:13056
v_mul_f32 v17,v17,v25
v_mfma_f32_16x16x32_bf16 v[52:55],v[28:31],v[80:83],0
v_exp_f32 v104,v104
v_mfma_f32_16x16x32_bf16 v[52:55],v[28:31],v[88:91],v[52:55]
s_waitcnt lgkmcnt(3)
ds_read_b128 a[184:187],v1 offset:12800
v_mul_f32 v18,v18,v25
v_mfma_f32_16x16x32_bf16 v[64:67],a[128:131],v[128:131],v[32:35]
v_exp_f32 v105,v105
v_mfma_f32_16x16x32_bf16 v[64:67],a[132:135],v[136:139],v[64:67]
ds_read_b128 a[180:183],v1 offset:12544
v_cvt_pk_bf16_f32 v104,v104,v105
v_mfma_f32_16x16x32_bf16 v[64:67],a[136:139],v[144:147],v[64:67]
)A"
R"A(v_exp_f32 v106,v106
v_mfma_f32_16x16x32_bf16 v[64:67],a[140:143],v[152:155],v[64:67]
ds_read_b128 a[176:179],v1 offset:12288
v_mul_f32 v19,v19,v25
v_mfma_f32_16x16x32_bf16 v[68:71],a[156:159],v[152:155],v[32:35]
v_exp_f32 v107,v107
v_mfma_f32_16x16x32_bf16 v[68:71],a[152:155],v[144:147],v[68:71]
v_exp_f32 v108,v108
v_mfma_f32_16x16x32_bf16 v[68:71],a[148:151],v[136:139],v[68:71]
v_exp_f32 v109,v109
v_mfma_f32_16x16x32_bf16 v[68:71],a[144:147],v[128:131],v[68:71]
v_exp_f32 v110,v110
v_mfma_f32_16x16x32_bf16 v[72:75],a[160:163],v[128:131],v[32:35]
v_exp_f32 v111,v111
v_mfma_f32_16x16x32_bf16 v[72:75],a[164:167],v[136:139],v[72:75]
s_waitcnt lgkmcnt(1)
v_cvt_pk_bf16_f32 v105,v106,v107
v_cvt_pk_bf16_f32 v106,v108,v109
v_mfma_f32_16x16x32_bf16 v[72:75],a[168:171],v[144:147],v[72:75]
global_load_lds_dwordx4 v4,s[10:11] offset:1024 nt
v_cvt_pk_bf16_f32 v107,v110,v111
v_mfma_f32_16x16x32_bf16 v[72:75],a[172:175],v[152:155],v[72:75]
v_exp_f32 v112,v112
v_mfma_f32_16x16x32_bf16 v[76:79],a[188:191],v[152:155],v[32:35]
v_exp_f32 v113,v113
v_mfma_f32_16x16x32_bf16 v[76:79],a[184:187],v[144:147],v[76:79]
v_exp_f32 v114,v114
v_mfma_f32_16x16x32_bf16 v[76:79],a[180:183],v[136:139],v[76:79]
s_waitcnt lgkmcnt(0)
v_exp_f32 v115,v115
v_mfma_f32_16x16x32_bf16 v[76:79],a[176:179],v[128:131],v[76:79]
v_exp_f32 v116,v116
v_mfma_f32_16x16x32_bf16 v[196:199],a[192:195],v[96:99],0
v_exp_f32 v117,v117
v_mfma_f32_16x16x32_bf16 v[196:199],a[196:199],v[104:107],v[196:199]
v_exp_f32 v118,v118
v_mfma_f32_16x16x32_bf16 v[204:207],a[200:203],v[96:99],0
v_exp_f32 v119,v119
v_mfma_f32_16x16x32_bf16 v[204:207],a[204:207],v[104:107],v[204:207]
v_cvt_pk_bf16_f32 v112,v112,v113
v_cvt_pk_bf16_f32 v113,v114,v115
v_mfma_f32_16x16x32_bf16 v[212:215],a[208:211],v[96:99],0
v_cvt_pk_bf16_f32 v114,v116,v117
v_cvt_pk_bf16_f32 v115,v118,v119
v_mfma_f32_16x16x32_bf16 v[212:215],a[212:215],v[104:107],v[212:215]
v_exp_f32 v120,v120
v_mfma_f32_16x16x32_bf16 v[220:223],a[216:219],v[96:99],0
v_exp_f32 v121,v121
v_mfma_f32_16x16x32_bf16 v[220:223],a[220:223],v[104:107],v[220:223]
v_exp_f32 v122,v122
v_mfma_f32_16x16x32_bf16 v[228:231],a[224:227],v[96:99],0
global_load_lds_dwordx4 v5,s[10:11] offset:2048 nt
v_cvt_pk_bf16_f32 v120,v120,v121
v_mfma_f32_16x16x32_bf16 v[228:231],a[228:231],v[104:107],v[228:231]
v_exp_f32 v123,v123
v_mfma_f32_16x16x32_bf16 v[236:239],a[232:235],v[96:99],0
v_exp_f32 v124,v124
v_mfma_f32_16x16x32_bf16 v[236:239],a[236:239],v[104:107],v[236:239]
v_exp_f32 v125,v125
v_mfma_f32_16x16x32_bf16 v[244:247],a[240:243],v[96:99],0
v_exp_f32 v126,v126
v_mfma_f32_16x16x32_bf16 v[244:247],a[244:247],v[104:107],v[244:247]
v_exp_f32 v127,v127
v_mfma_f32_16x16x32_bf16 v[252:255],a[248:251],v[96:99],0
v_cvt_pk_bf16_f32 v121,v122,v123
v_cvt_pk_bf16_f32 v122,v124,v125
v_mfma_f32_16x16x32_bf16 v[252:255],a[252:255],v[104:107],v[252:255]
v_cvt_pk_bf16_f32 v123,v126,v127
v_mul_f32 v20,v20,v25
v_mfma_f32_16x16x32_bf16 v[56:59],v[28:31],v[96:99],0
v_mul_f32 v21,v21,v25
v_mul_f32 v22,v22,v25
v_mfma_f32_16x16x32_bf16 v[56:59],v[28:31],v[104:107],v[56:59]
v_mul_f32 v23,v23,v25
v_cvt_pk_bf16_f32 v16,v16,v17
v_mfma_f32_16x16x32_bf16 v[80:83],a[128:131],v[160:163],v[36:39]
v_cvt_pk_bf16_f32 v17,v18,v19
v_cvt_pk_bf16_f32 v18,v20,v21
v_mfma_f32_16x16x32_bf16 v[80:83],a[132:135],v[168:171],v[80:83]
v_cvt_pk_bf16_f32 v19,v22,v23
v_accvgpr_read_b32 v20,a44
v_mfma_f32_16x16x32_bf16 v[80:83],a[136:139],v[176:179],v[80:83]
ds_write_b128 v7,v[16:19] offset:2048
v_mfma_f32_16x16x32_bf16 v[80:83],a[140:143],v[184:187],v[80:83]
v_accvgpr_read_b32 v21,a45
v_accvgpr_read_b32 v22,a46
v_mfma_f32_16x16x32_bf16 v[84:87],a[156:159],v[184:187],v[36:39]
v_accvgpr_read_b32 v16,a40
v_accvgpr_read_b32 v17,a41
v_mfma_f32_16x16x32_bf16 v[84:87],a[152:155],v[176:179],v[84:87]
global_load_lds_dwordx4 v5,s[10:11] offset:3072
v_accvgpr_read_b32 v18,a42
v_mfma_f32_16x16x32_bf16 v[84:87],a[148:151],v[168:171],v[84:87]
v_accvgpr_read_b32 v19,a43
v_accvgpr_read_b32 v23,a47
v_mfma_f32_16x16x32_bf16 v[84:87],a[144:147],v[160:163],v[84:87]
v_accvgpr_read_b32 v8,a48
v_accvgpr_read_b32 v9,a49
v_mfma_f32_16x16x32_bf16 v[88:91],a[160:163],v[160:163],v[36:39]
v_accvgpr_read_b32 v10,a50
v_accvgpr_read_b32 v11,a51
v_mfma_f32_16x16x32_bf16 v[88:91],a[164:167],v[168:171],v[88:91]
v_mul_f32 v16,v16,v25
v_mul_f32 v17,v17,v25
v_mfma_f32_16x16x32_bf16 v[88:91],a[168:171],v[176:179],v[88:91]
v_mul_f32 v18,v18,v25
v_mul_f32 v19,v19,v25
v_mfma_f32_16x16x32_bf16 v[88:91],a[172:175],v[184:187],v[88:91]
v_mul_f32 v20,v20,v25
v_mul_f32 v21,v21,v25
v_mfma_f32_16x16x32_bf16 v[92:95],a[188:191],v[184:187],v[36:39]
v_mul_f32 v22,v22,v25
v_mul_f32 v23,v23,v25
v_mfma_f32_16x16x32_bf16 v[92:95],a[184:187],v[176:179],v[92:95]
v_cvt_pk_bf16_f32 v16,v16,v17
v_cvt_pk_bf16_f32 v17,v18,v19
v_mfma_f32_16x16x32_bf16 v[92:95],a[180:183],v[168:171],v[92:95]
v_cvt_pk_bf16_f32 v18,v20,v21
v_cvt_pk_bf16_f32 v19,v22,v23
v_mfma_f32_16x16x32_bf16 v[92:95],a[176:179],v[160:163],v[92:95]
s_add_u32 s10,s24,s74
s_addc_u32 s11,s25,0
v_accvgpr_read_b32 v12,a52
v_mfma_f32_16x16x32_bf16 a[32:35],a[192:195],v[112:115],0
ds_write_b128 v7,v[16:19] offset:3072
v_mfma_f32_16x16x32_bf16 a[32:35],a[196:199],v[120:123],a[32:35]
s_add_u32 m0,s76,16384
v_accvgpr_read_b32 v13,a53
v_accvgpr_read_b32 v14,a54
v_mfma_f32_16x16x32_bf16 a[36:39],a[200:203],v[112:115],0
v_accvgpr_read_b32 v15,a55
v_accvgpr_read_b32 v16,a56
v_mfma_f32_16x16x32_bf16 a[36:39],a[204:207],v[120:123],a[36:39]
global_load_lds_dwordx4 v4,s[10:11]
v_accvgpr_read_b32 v17,a57
v_mfma_f32_16x16x32_bf16 a[40:43],a[208:211],v[112:115],0
v_accvgpr_read_b32 v18,a58
v_accvgpr_read_b32 v19,a59
v_mfma_f32_16x16x32_bf16 a[40:43],a[212:215],v[120:123],a[40:43]
v_accvgpr_read_b32 v20,a60
v_accvgpr_read_b32 v21,a61
v_mfma_f32_16x16x32_bf16 a[44:47],a[216:219],v[112:115],0
v_accvgpr_read_b32 v22,a62
v_accvgpr_read_b32 v23,a63
v_mfma_f32_16x16x32_bf16 a[44:47],a[220:223],v[120:123],a[44:47]
v_add_u32 v2,s78,v2
v_add_u32 v3,s78,v3
v_mfma_f32_16x16x32_bf16 a[48:51],a[224:227],v[112:115],0
s_sub_u32 s78,s77,32768
ds_read_b64_tr_b16 a[192:193],v2
ds_read_b64_tr_b16 a[194:195],v2 offset:4096
v_mfma_f32_16x16x32_bf16 a[48:51],a[228:231],v[120:123],a[48:51]
s_and_b32 s78,s78,0x1ffff
ds_read_b64_tr_b16 a[196:197],v2 offset:8192
v_mul_f32 v8,v8,v25
v_mfma_f32_16x16x32_bf16 a[52:55],a[232:235],v[112:115],0
s_sub_u32 s78,s78,s77
ds_read_b64_tr_b16 a[198:199],v2 offset:12288
v_mul_f32 v9,v9,v25
v_mfma_f32_16x16x32_bf16 a[52:55],a[236:239],v[120:123],a[52:55]
s_add_u32 s77,s77,s78
ds_read_b64_tr_b16 a[200:201],v3
v_mul_f32 v10,v10,v25
v_mfma_f32_16x16x32_bf16 a[56:59],a[240:243],v[112:115],0
ds_read_b64_tr_b16 a[202:203],v3 offset:4096
ds_read_b64_tr_b16 a[204:205],v3 offset:8192
v_mfma_f32_16x16x32_bf16 a[56:59],a[244:247],v[120:123],a[56:59]
ds_read_b64_tr_b16 a[206:207],v3 offset:12288
v_mul_f32 v11,v11,v25
v_mfma_f32_16x16x32_bf16 a[60:63],a[248:251],v[112:115],0
ds_read_b64_tr_b16 a[208:209],v2 offset:256
v_mul_f32 v12,v12,v25
v_mfma_f32_16x16x32_bf16 a[60:63],a[252:255],v[120:123],a[60:63]
ds_read_b64_tr_b16 a[210:211],v2 offset:4352
v_mul_f32 v13,v13,v25
v_mfma_f32_16x16x32_bf16 v[60:63],v[28:31],v[112:115],0
ds_read_b64_tr_b16 a[212:213],v2 offset:8448
ds_read_b64_tr_b16 a[214:215],v2 offset:12544
v_mfma_f32_16x16x32_bf16 v[60:63],v[28:31],v[120:123],v[60:63]
global_load_lds_dwordx4 v5,s[10:11] offset:1024
ds_read_b64_tr_b16 a[216:217],v3 offset:256
v_mfma_f32_16x16x32_bf16 v[96:99],a[128:131],v[192:195],v[40:43]
v_exp_f32 v64,v64
v_mfma_f32_16x16x32_bf16 v[96:99],a[132:135],v[200:203],v[96:99]
v_exp_f32 v65,v65
v_mfma_f32_16x16x32_bf16 v[96:99],a[136:139],v[208:211],v[96:99]
v_exp_f32 v66,v66
v_mfma_f32_16x16x32_bf16 v[96:99],a[140:143],v[216:219],v[96:99]
v_exp_f32 v67,v67
v_mfma_f32_16x16x32_bf16 v[100:103],a[156:159],v[216:219],v[40:43]
v_exp_f32 v68,v68
v_mfma_f32_16x16x32_bf16 v[100:103],a[152:155],v[208:211],v[100:103]
v_exp_f32 v69,v69
v_mfma_f32_16x16x32_bf16 v[100:103],a[148:151],v[200:203],v[100:103]
v_exp_f32 v70,v70
v_mfma_f32_16x16x32_bf16 v[100:103],a[144:147],v[192:195],v[100:103]
v_exp_f32 v71,v71
v_mfma_f32_16x16x32_bf16 v[104:107],a[160:163],v[192:195],v[40:43]
ds_read_b64_tr_b16 a[218:219],v3 offset:4352
v_cvt_pk_bf16_f32 v64,v64,v65
v_mfma_f32_16x16x32_bf16 v[104:107],a[164:167],v[200:203],v[104:107]
v_cvt_pk_bf16_f32 v65,v66,v67
v_cvt_pk_bf16_f32 v66,v68,v69
v_mfma_f32_16x16x32_bf16 v[104:107],a[168:171],v[208:211],v[104:107]
ds_read_b64_tr_b16 a[220:221],v3 offset:8448
v_cvt_pk_bf16_f32 v67,v70,v71
s_nop 0
L_pd8_%=:
s_cbranch_execz L_trt0_%=
v_mfma_f32_16x16x32_bf16 v[104:107],a[172:175],v[216:219],v[104:107]
v_exp_f32 v72,v72
v_mfma_f32_16x16x32_bf16 v[108:111],a[188:191],v[216:219],v[40:43]
v_exp_f32 v73,v73
v_mfma_f32_16x16x32_bf16 v[108:111],a[184:187],v[208:211],v[108:111]
global_load_lds_dwordx4 v4,s[10:11] offset:2048
v_exp_f32 v74,v74
v_exp_f32 v75,v75
v_exp_f32 v76,v76
v_exp_f32 v77,v77
v_exp_f32 v78,v78
v_exp_f32 v79,v79
v_mfma_f32_16x16x32_bf16 v[108:111],a[180:183],v[200:203],v[108:111]
ds_read_b64_tr_b16 a[222:223],v3 offset:12544
v_cvt_pk_bf16_f32 v72,v72,v73
v_cvt_pk_bf16_f32 v73,v74,v75
v_cvt_pk_bf16_f32 v74,v76,v77
v_cvt_pk_bf16_f32 v75,v78,v79
v_mfma_f32_16x16x32_bf16 v[108:111],a[176:179],v[192:195],v[108:111]
s_waitcnt lgkmcnt(3)
ds_read_b64_tr_b16 a[224:225],v2 offset:512
ds_read_b64_tr_b16 a[226:227],v2 offset:4608
v_mfma_f32_16x16x32_bf16 a[0:3],a[192:195],v[64:67],a[0:3]
ds_read_b64_tr_b16 a[228:229],v2 offset:8704
v_mul_f32 v14,v14,v25
v_mfma_f32_16x16x32_bf16 a[0:3],a[196:199],v[72:75],a[0:3]
ds_read_b64_tr_b16 a[230:231],v2 offset:12800
v_mul_f32 v15,v15,v25
v_mfma_f32_16x16x32_bf16 a[4:7],a[200:203],v[64:67],a[4:7]
ds_read_b64_tr_b16 a[232:233],v3 offset:512
v_cvt_pk_bf16_f32 v8,v8,v9
v_mfma_f32_16x16x32_bf16 a[4:7],a[204:207],v[72:75],a[4:7]
ds_read_b64_tr_b16 a[234:235],v3 offset:4608
ds_read_b64_tr_b16 a[236:237],v3 offset:8704
v_mfma_f32_16x16x32_bf16 a[8:11],a[208:211],v[64:67],a[8:11]
ds_read_b64_tr_b16 a[238:239],v3 offset:12800
v_cvt_pk_bf16_f32 v9,v10,v11
v_mfma_f32_16x16x32_bf16 a[8:11],a[212:215],v[72:75],a[8:11]
s_waitcnt lgkmcnt(9)
ds_read_b64_tr_b16 a[240:241],v2 offset:768
v_cvt_pk_bf16_f32 v10,v12,v13
v_mfma_f32_16x16x32_bf16 a[12:15],a[216:219],v[64:67],a[12:15]
s_waitcnt lgkmcnt(9)
ds_read_b64_tr_b16 a[242:243],v2 offset:4864
v_cvt_pk_bf16_f32 v11,v14,v15
v_mfma_f32_16x16x32_bf16 a[12:15],a[220:223],v[72:75],a[12:15]
s_waitcnt lgkmcnt(8)
ds_read_b64_tr_b16 a[244:245],v2 offset:8960
ds_read_b64_tr_b16 a[246:247],v2 offset:13056
v_mfma_f32_16x16x32_bf16 a[16:19],a[224:227],v[64:67],a[16:19]
s_waitcnt lgkmcnt(8)
ds_read_b64_tr_b16 a[248:249],v3 offset:768
v_mul_f32 v16,v16,v25
v_mfma_f32_16x16x32_bf16 a[16:19],a[228:231],v[72:75],a[16:19]
s_waitcnt lgkmcnt(1)
ds_read_b64_tr_b16 a[250:251],v3 offset:4864
v_mul_f32 v17,v17,v25
v_mfma_f32_16x16x32_bf16 a[20:23],a[232:235],v[64:67],a[20:23]
s_getreg_b32 s92,hwreg(HW_REG_IB_STS,0,4)
s_min_u32 s4,s92,s6
ds_read_b64_tr_b16 a[252:253],v3 offset:8960
v_mfma_f32_16x16x32_bf16 a[20:23],a[236:239],v[72:75],a[20:23]
s_mov_b32 s6,s92
ds_read_b64_tr_b16 a[254:255],v3 offset:13056
v_mul_f32 v18,v18,v25
v_mfma_f32_16x16x32_bf16 a[24:27],a[240:243],v[64:67],a[24:27]
s_cmp_gt_u32 s4,s9
s_cselect_b32 s4,15,s5
v_mul_f32 v19,v19,v25
v_mfma_f32_16x16x32_bf16 a[24:27],a[244:247],v[72:75],a[24:27]
s_waitcnt lgkmcnt(2)
v_mul_f32 v20,v20,v25
v_mul_f32 v21,v21,v25
v_mfma_f32_16x16x32_bf16 a[28:31],a[248:251],v[64:67],a[28:31]
s_waitcnt lgkmcnt(0)
v_mul_f32 v22,v22,v25
v_mul_f32 v23,v23,v25
v_mfma_f32_16x16x32_bf16 a[28:31],a[252:255],v[72:75],a[28:31]
s_getpc_b64 s[92:93]
s_cmp_lg_u32 s4,0
s_cbranch_scc1 L_pace_%=
v_mfma_f32_16x16x32_bf16 v[48:51],v[28:31],v[64:67],v[48:51]
v_cvt_pk_bf16_f32 v12,v16,v17
v_cvt_pk_bf16_f32 v13,v18,v19
v_mfma_f32_16x16x32_bf16 v[48:51],v[28:31],v[72:75],v[48:51]
s_add_u32 s10,s24,s74
s_addc_u32 s11,s25,0
v_cvt_pk_bf16_f32 v14,v20,v21
s_waitcnt vmcnt(15)
s_barrier
v_mfma_f32_16x16x32_bf16 v[112:115],a[128:131],v[224:227],v[44:47]
s_add_u32 m0,s76,16384
v_cvt_pk_bf16_f32 v15,v22,v23
)A"
R"A(v_accvgpr_read_b32 v16,a112
v_mfma_f32_16x16x32_bf16 v[112:115],a[132:135],v[232:235],v[112:115]
s_add_u32 s74,s74,16384
v_accvgpr_read_b32 v17,a113
v_accvgpr_read_b32 v18,a114
v_mfma_f32_16x16x32_bf16 v[112:115],a[136:139],v[240:243],v[112:115]
global_load_lds_dwordx4 v5,s[10:11] offset:3072
v_accvgpr_read_b32 v19,a115
v_mfma_f32_16x16x32_bf16 v[112:115],a[140:143],v[248:251],v[112:115]
s_and_b32 s74,s74,0x3ffff
v_accvgpr_read_b32 v20,a116
v_accvgpr_read_b32 v21,a117
v_mfma_f32_16x16x32_bf16 v[116:119],a[156:159],v[248:251],v[44:47]
s_or_b32 s74,s74,s69
v_accvgpr_read_b32 v22,a118
v_accvgpr_read_b32 v23,a119
v_mfma_f32_16x16x32_bf16 v[116:119],a[152:155],v[240:243],v[116:119]
s_sub_u32 s76,s76,32768
v_mul_f32 v16,v16,v27
v_mul_f32 v17,v17,v27
v_mfma_f32_16x16x32_bf16 v[116:119],a[148:151],v[232:235],v[116:119]
s_and_b32 s76,s76,0x1ffff
v_mul_f32 v18,v18,v27
v_mul_f32 v19,v19,v27
v_mfma_f32_16x16x32_bf16 v[116:119],a[144:147],v[224:227],v[116:119]
s_and_b32 s69,s75,0xfffc0000
v_mul_f32 v20,v20,v27
v_mul_f32 v21,v21,v27
v_mfma_f32_16x16x32_bf16 v[120:123],a[160:163],v[224:227],v[44:47]
s_add_u32 s75,s75,16384
v_mul_f32 v22,v22,v27
v_mul_f32 v23,v23,v27
v_mfma_f32_16x16x32_bf16 v[120:123],a[164:167],v[232:235],v[120:123]
v_cvt_pk_bf16_f32 v16,v16,v17
v_cvt_pk_bf16_f32 v17,v18,v19
v_mfma_f32_16x16x32_bf16 v[120:123],a[168:171],v[240:243],v[120:123]
v_cvt_pk_bf16_f32 v18,v20,v21
v_cvt_pk_bf16_f32 v19,v22,v23
v_mfma_f32_16x16x32_bf16 v[120:123],a[172:175],v[248:251],v[120:123]
v_accvgpr_write_b32 a112,v16
v_accvgpr_write_b32 a113,v17
v_mfma_f32_16x16x32_bf16 v[124:127],a[188:191],v[248:251],v[44:47]
v_accvgpr_write_b32 a114,v18
v_accvgpr_write_b32 a115,v19
v_accvgpr_write_b32 a116,v8
v_accvgpr_write_b32 a117,v9
v_accvgpr_write_b32 a118,v10
v_accvgpr_write_b32 a119,v11
v_exp_f32 v80,v80
v_exp_f32 v81,v81
v_exp_f32 v82,v82
v_exp_f32 v83,v83
v_exp_f32 v84,v84
v_exp_f32 v85,v85
v_exp_f32 v86,v86
v_exp_f32 v87,v87
v_mfma_f32_16x16x32_bf16 v[124:127],a[184:187],v[240:243],v[124:127]
v_cvt_pk_bf16_f32 v80,v80,v81
v_cvt_pk_bf16_f32 v81,v82,v83
v_cvt_pk_bf16_f32 v82,v84,v85
v_cvt_pk_bf16_f32 v83,v86,v87
v_exp_f32 v88,v88
v_exp_f32 v89,v89
v_exp_f32 v90,v90
v_exp_f32 v91,v91
v_exp_f32 v92,v92
v_exp_f32 v93,v93
v_exp_f32 v94,v94
v_exp_f32 v95,v95
v_mfma_f32_16x16x32_bf16 v[124:127],a[180:183],v[232:235],v[124:127]
v_cvt_pk_bf16_f32 v88,v88,v89
v_cvt_pk_bf16_f32 v89,v90,v91
v_cvt_pk_bf16_f32 v90,v92,v93
v_cvt_pk_bf16_f32 v91,v94,v95
v_mfma_f32_16x16x32_bf16 v[124:127],a[176:179],v[224:227],v[124:127]
s_add_u32 s10,s20,s74
s_addc_u32 s11,s21,0
s_add_u32 m0,s76,0
v_mfma_f32_16x16x32_bf16 v[132:135],a[192:195],v[80:83],v[132:135]
global_load_lds_dwordx4 v4,s[10:11] nt
v_add_u32 v1,s78,v1
v_mfma_f32_16x16x32_bf16 v[132:135],a[196:199],v[88:91],v[132:135]
ds_read_b128 a[128:131],v1
v_accvgpr_read_b32 v16,a120
v_mfma_f32_16x16x32_bf16 v[140:143],a[200:203],v[80:83],v[140:143]
v_exp_f32 v96,v96
v_mfma_f32_16x16x32_bf16 v[140:143],a[204:207],v[88:91],v[140:143]
ds_read_b128 a[132:135],v1 offset:256
v_accvgpr_read_b32 v17,a121
v_mfma_f32_16x16x32_bf16 v[148:151],a[208:211],v[80:83],v[148:151]
v_exp_f32 v97,v97
v_mfma_f32_16x16x32_bf16 v[148:151],a[212:215],v[88:91],v[148:151]
ds_read_b128 a[136:139],v1 offset:512
v_cvt_pk_bf16_f32 v96,v96,v97
v_mfma_f32_16x16x32_bf16 v[156:159],a[216:219],v[80:83],v[156:159]
v_exp_f32 v98,v98
v_mfma_f32_16x16x32_bf16 v[156:159],a[220:223],v[88:91],v[156:159]
ds_read_b128 a[140:143],v1 offset:768
v_accvgpr_read_b32 v18,a122
v_mfma_f32_16x16x32_bf16 v[164:167],a[224:227],v[80:83],v[164:167]
v_exp_f32 v99,v99
v_mfma_f32_16x16x32_bf16 v[164:167],a[228:231],v[88:91],v[164:167]
ds_read_b128 a[156:159],v1 offset:4864
v_cvt_pk_bf16_f32 v97,v98,v99
v_mfma_f32_16x16x32_bf16 v[172:175],a[232:235],v[80:83],v[172:175]
v_exp_f32 v100,v100
v_mfma_f32_16x16x32_bf16 v[172:175],a[236:239],v[88:91],v[172:175]
ds_read_b128 a[152:155],v1 offset:4608
v_accvgpr_read_b32 v19,a123
v_mfma_f32_16x16x32_bf16 v[180:183],a[240:243],v[80:83],v[180:183]
v_exp_f32 v101,v101
v_mfma_f32_16x16x32_bf16 v[180:183],a[244:247],v[88:91],v[180:183]
ds_read_b128 a[148:151],v1 offset:4352
v_cvt_pk_bf16_f32 v98,v100,v101
v_mfma_f32_16x16x32_bf16 v[188:191],a[248:251],v[80:83],v[188:191]
global_load_lds_dwordx4 v4,s[10:11] offset:1024 nt
v_accvgpr_read_b32 v20,a124
v_mfma_f32_16x16x32_bf16 v[188:191],a[252:255],v[88:91],v[188:191]
ds_read_b128 a[144:147],v1 offset:4096
v_accvgpr_read_b32 v21,a125
v_mfma_f32_16x16x32_bf16 v[52:55],v[28:31],v[80:83],v[52:55]
v_exp_f32 v102,v102
v_mfma_f32_16x16x32_bf16 v[52:55],v[28:31],v[88:91],v[52:55]
s_waitcnt lgkmcnt(3)
ds_read_b128 a[160:163],v1 offset:8192
v_accvgpr_read_b32 v22,a126
v_mfma_f32_16x16x32_bf16 v[64:67],a[128:131],v[128:131],v[32:35]
v_exp_f32 v103,v103
v_mfma_f32_16x16x32_bf16 v[64:67],a[132:135],v[136:139],v[64:67]
ds_read_b128 a[164:167],v1 offset:8448
v_cvt_pk_bf16_f32 v99,v102,v103
v_mfma_f32_16x16x32_bf16 v[64:67],a[136:139],v[144:147],v[64:67]
v_exp_f32 v104,v104
v_mfma_f32_16x16x32_bf16 v[64:67],a[140:143],v[152:155],v[64:67]
ds_read_b128 a[168:171],v1 offset:8704
v_accvgpr_read_b32 v23,a127
v_mfma_f32_16x16x32_bf16 v[68:71],a[156:159],v[152:155],v[32:35]
s_waitcnt lgkmcnt(4)
v_exp_f32 v105,v105
v_mfma_f32_16x16x32_bf16 v[68:71],a[152:155],v[144:147],v[68:71]
ds_read_b128 a[172:175],v1 offset:8960
v_cvt_pk_bf16_f32 v104,v104,v105
v_mfma_f32_16x16x32_bf16 v[68:71],a[148:151],v[136:139],v[68:71]
s_waitcnt lgkmcnt(4)
v_exp_f32 v106,v106
v_mfma_f32_16x16x32_bf16 v[68:71],a[144:147],v[128:131],v[68:71]
s_waitcnt lgkmcnt(3)
ds_read_b128 a[188:191],v1 offset:13056
v_mul_f32 v16,v16,v27
v_mfma_f32_16x16x32_bf16 v[72:75],a[160:163],v[128:131],v[32:35]
s_waitcnt lgkmcnt(3)
v_exp_f32 v107,v107
v_mfma_f32_16x16x32_bf16 v[72:75],a[164:167],v[136:139],v[72:75]
s_waitcnt lgkmcnt(1)
ds_read_b128 a[184:187],v1 offset:12800
v_cvt_pk_bf16_f32 v105,v106,v107
v_mfma_f32_16x16x32_bf16 v[72:75],a[168:171],v[144:147],v[72:75]
global_load_lds_dwordx4 v5,s[10:11] offset:2048 nt
ds_read_b128 a[180:183],v1 offset:12544
v_mfma_f32_16x16x32_bf16 v[72:75],a[172:175],v[152:155],v[72:75]
s_waitcnt lgkmcnt(2)
ds_read_b128 a[176:179],v1 offset:12288
v_mul_f32 v17,v17,v27
v_mfma_f32_16x16x32_bf16 v[76:79],a[188:191],v[152:155],v[32:35]
s_waitcnt lgkmcnt(2)
v_exp_f32 v108,v108
v_mfma_f32_16x16x32_bf16 v[76:79],a[184:187],v[144:147],v[76:79]
s_waitcnt lgkmcnt(1)
v_exp_f32 v109,v109
v_exp_f32 v110,v110
v_exp_f32 v111,v111
v_mfma_f32_16x16x32_bf16 v[76:79],a[180:183],v[136:139],v[76:79]
s_waitcnt lgkmcnt(0)
v_cvt_pk_bf16_f32 v106,v108,v109
v_cvt_pk_bf16_f32 v107,v110,v111
v_mfma_f32_16x16x32_bf16 v[76:79],a[176:179],v[128:131],v[76:79]
v_mul_f32 v18,v18,v27
v_mul_f32 v19,v19,v27
v_mfma_f32_16x16x32_bf16 v[196:199],a[192:195],v[96:99],v[196:199]
v_mul_f32 v20,v20,v27
v_mul_f32 v21,v21,v27
v_mfma_f32_16x16x32_bf16 v[196:199],a[196:199],v[104:107],v[196:199]
v_mul_f32 v22,v22,v27
v_mul_f32 v23,v23,v27
v_mfma_f32_16x16x32_bf16 v[204:207],a[200:203],v[96:99],v[204:207]
v_cvt_pk_bf16_f32 v16,v16,v17
v_cvt_pk_bf16_f32 v17,v18,v19
v_mfma_f32_16x16x32_bf16 v[204:207],a[204:207],v[104:107],v[204:207]
v_cvt_pk_bf16_f32 v18,v20,v21
v_cvt_pk_bf16_f32 v19,v22,v23
v_mfma_f32_16x16x32_bf16 v[212:215],a[208:211],v[96:99],v[212:215]
v_accvgpr_write_b32 a120,v16
v_accvgpr_write_b32 a121,v17
v_mfma_f32_16x16x32_bf16 v[212:215],a[212:215],v[104:107],v[212:215]
v_accvgpr_write_b32 a122,v18
v_accvgpr_write_b32 a123,v19
v_mfma_f32_16x16x32_bf16 v[220:223],a[216:219],v[96:99],v[220:223]
v_accvgpr_write_b32 a124,v12
v_accvgpr_write_b32 a125,v13
v_mfma_f32_16x16x32_bf16 v[220:223],a[220:223],v[104:107],v[220:223]
v_accvgpr_write_b32 a126,v14
v_accvgpr_write_b32 a127,v15
v_mfma_f32_16x16x32_bf16 v[228:231],a[224:227],v[96:99],v[228:231]
global_load_lds_dwordx4 v5,s[10:11] offset:3072
v_add_u32 v2,s78,v2
v_mfma_f32_16x16x32_bf16 v[228:231],a[228:231],v[104:107],v[228:231]
v_exp_f32 v112,v112
v_mfma_f32_16x16x32_bf16 v[236:239],a[232:235],v[96:99],v[236:239]
v_exp_f32 v113,v113
v_mfma_f32_16x16x32_bf16 v[236:239],a[236:239],v[104:107],v[236:239]
v_exp_f32 v114,v114
v_mfma_f32_16x16x32_bf16 v[244:247],a[240:243],v[96:99],v[244:247]
v_exp_f32 v115,v115
v_mfma_f32_16x16x32_bf16 v[244:247],a[244:247],v[104:107],v[244:247]
v_exp_f32 v116,v116
v_mfma_f32_16x16x32_bf16 v[252:255],a[248:251],v[96:99],v[252:255]
v_exp_f32 v117,v117
v_mfma_f32_16x16x32_bf16 v[252:255],a[252:255],v[104:107],v[252:255]
v_exp_f32 v118,v118
v_mfma_f32_16x16x32_bf16 v[56:59],v[28:31],v[96:99],v[56:59]
v_exp_f32 v119,v119
v_mfma_f32_16x16x32_bf16 v[56:59],v[28:31],v[104:107],v[56:59]
v_cvt_pk_bf16_f32 v112,v112,v113
v_cvt_pk_bf16_f32 v113,v114,v115
v_mfma_f32_16x16x32_bf16 v[80:83],a[128:131],v[160:163],v[36:39]
v_cvt_pk_bf16_f32 v114,v116,v117
v_cvt_pk_bf16_f32 v115,v118,v119
v_mfma_f32_16x16x32_bf16 v[80:83],a[132:135],v[168:171],v[80:83]
s_add_u32 s10,s24,s74
s_addc_u32 s11,s25,0
v_add_u32 v3,s78,v3
v_mfma_f32_16x16x32_bf16 v[80:83],a[136:139],v[176:179],v[80:83]
s_add_u32 s74,s74,16384
v_exp_f32 v120,v120
v_mfma_f32_16x16x32_bf16 v[80:83],a[140:143],v[184:187],v[80:83]
s_add_u32 m0,s76,16384
v_exp_f32 v121,v121
v_mfma_f32_16x16x32_bf16 v[84:87],a[156:159],v[184:187],v[36:39]
s_and_b32 s74,s74,0x3ffff
v_exp_f32 v122,v122
v_mfma_f32_16x16x32_bf16 v[84:87],a[152:155],v[176:179],v[84:87]
global_load_lds_dwordx4 v4,s[10:11]
v_cvt_pk_bf16_f32 v120,v120,v121
v_mfma_f32_16x16x32_bf16 v[84:87],a[148:151],v[168:171],v[84:87]
s_or_b32 s74,s74,s69
v_exp_f32 v123,v123
v_mfma_f32_16x16x32_bf16 v[84:87],a[144:147],v[160:163],v[84:87]
s_sub_u32 s76,s76,32768
v_exp_f32 v124,v124
v_mfma_f32_16x16x32_bf16 v[88:91],a[160:163],v[160:163],v[36:39]
s_and_b32 s76,s76,0x1ffff
v_exp_f32 v125,v125
v_mfma_f32_16x16x32_bf16 v[88:91],a[164:167],v[168:171],v[88:91]
s_sub_u32 s78,s77,32768
v_exp_f32 v126,v126
v_mfma_f32_16x16x32_bf16 v[88:91],a[168:171],v[176:179],v[88:91]
s_and_b32 s78,s78,0x1ffff
v_exp_f32 v127,v127
v_mfma_f32_16x16x32_bf16 v[88:91],a[172:175],v[184:187],v[88:91]
s_sub_u32 s78,s78,s77
v_cvt_pk_bf16_f32 v121,v122,v123
v_cvt_pk_bf16_f32 v122,v124,v125
v_mfma_f32_16x16x32_bf16 v[92:95],a[188:191],v[184:187],v[36:39]
s_and_b32 s69,s75,0xfffc0000
v_cvt_pk_bf16_f32 v123,v126,v127
v_accvgpr_read_b32 v12,a100
v_mfma_f32_16x16x32_bf16 v[92:95],a[184:187],v[176:179],v[92:95]
s_add_u32 s77,s77,s78
v_exp_f32 v64,v64
v_mfma_f32_16x16x32_bf16 v[92:95],a[180:183],v[168:171],v[92:95]
s_add_u32 s75,s75,16384
v_exp_f32 v65,v65
v_mfma_f32_16x16x32_bf16 v[92:95],a[176:179],v[160:163],v[92:95]
v_exp_f32 v66,v66
v_mfma_f32_16x16x32_bf16 a[32:35],a[192:195],v[112:115],a[32:35]
ds_read_b64_tr_b16 a[192:193],v2
ds_read_b64_tr_b16 a[194:195],v2 offset:4096
v_mfma_f32_16x16x32_bf16 a[32:35],a[196:199],v[120:123],a[32:35]
ds_read_b64_tr_b16 a[196:197],v2 offset:8192
v_cvt_pk_bf16_f32 v64,v64,v65
v_mfma_f32_16x16x32_bf16 a[36:39],a[200:203],v[112:115],a[36:39]
ds_read_b64_tr_b16 a[198:199],v2 offset:12288
v_accvgpr_read_b32 v13,a101
v_mfma_f32_16x16x32_bf16 a[36:39],a[204:207],v[120:123],a[36:39]
global_load_lds_dwordx4 v5,s[10:11] offset:1024
ds_read_b64_tr_b16 a[200:201],v3
v_mfma_f32_16x16x32_bf16 a[40:43],a[208:211],v[112:115],a[40:43]
ds_read_b64_tr_b16 a[202:203],v3 offset:4096
ds_read_b64_tr_b16 a[204:205],v3 offset:8192
v_mfma_f32_16x16x32_bf16 a[40:43],a[212:215],v[120:123],a[40:43]
ds_read_b64_tr_b16 a[206:207],v3 offset:12288
v_accvgpr_read_b32 v14,a102
v_mfma_f32_16x16x32_bf16 a[44:47],a[216:219],v[112:115],a[44:47]
ds_read_b64_tr_b16 a[208:209],v2 offset:256
v_accvgpr_read_b32 v15,a103
v_mfma_f32_16x16x32_bf16 a[44:47],a[220:223],v[120:123],a[44:47]
ds_read_b64_tr_b16 a[210:211],v2 offset:4352
v_accvgpr_read_b32 v8,a96
v_mfma_f32_16x16x32_bf16 a[48:51],a[224:227],v[112:115],a[48:51]
ds_read_b64_tr_b16 a[212:213],v2 offset:8448
ds_read_b64_tr_b16 a[214:215],v2 offset:12544
v_mfma_f32_16x16x32_bf16 a[48:51],a[228:231],v[120:123],a[48:51]
v_exp_f32 v67,v67
v_mfma_f32_16x16x32_bf16 a[52:55],a[232:235],v[112:115],a[52:55]
v_exp_f32 v68,v68
v_mfma_f32_16x16x32_bf16 a[52:55],a[236:239],v[120:123],a[52:55]
)A"
R"A(v_exp_f32 v69,v69
v_mfma_f32_16x16x32_bf16 a[56:59],a[240:243],v[112:115],a[56:59]
v_exp_f32 v70,v70
v_mfma_f32_16x16x32_bf16 a[56:59],a[244:247],v[120:123],a[56:59]
v_exp_f32 v71,v71
v_mfma_f32_16x16x32_bf16 a[60:63],a[248:251],v[112:115],a[60:63]
ds_read_b64_tr_b16 a[216:217],v3 offset:256
ds_read_b64_tr_b16 a[218:219],v3 offset:4352
v_mfma_f32_16x16x32_bf16 a[60:63],a[252:255],v[120:123],a[60:63]
v_cvt_pk_bf16_f32 v65,v66,v67
v_cvt_pk_bf16_f32 v66,v68,v69
v_mfma_f32_16x16x32_bf16 v[60:63],v[28:31],v[112:115],v[60:63]
ds_read_b64_tr_b16 a[220:221],v3 offset:8448
v_cvt_pk_bf16_f32 v67,v70,v71
v_mfma_f32_16x16x32_bf16 v[60:63],v[28:31],v[120:123],v[60:63]
global_load_lds_dwordx4 v4,s[10:11] offset:2048
ds_read_b64_tr_b16 a[222:223],v3 offset:12544
v_mfma_f32_16x16x32_bf16 v[96:99],a[128:131],v[192:195],v[40:43]
v_exp_f32 v72,v72
v_mfma_f32_16x16x32_bf16 v[96:99],a[132:135],v[200:203],v[96:99]
v_exp_f32 v73,v73
v_mfma_f32_16x16x32_bf16 v[96:99],a[136:139],v[208:211],v[96:99]
v_exp_f32 v74,v74
v_mfma_f32_16x16x32_bf16 v[96:99],a[140:143],v[216:219],v[96:99]
v_exp_f32 v75,v75
v_mfma_f32_16x16x32_bf16 v[100:103],a[156:159],v[216:219],v[40:43]
v_exp_f32 v76,v76
v_mfma_f32_16x16x32_bf16 v[100:103],a[152:155],v[208:211],v[100:103]
v_exp_f32 v77,v77
v_mfma_f32_16x16x32_bf16 v[100:103],a[148:151],v[200:203],v[100:103]
v_exp_f32 v78,v78
v_mfma_f32_16x16x32_bf16 v[100:103],a[144:147],v[192:195],v[100:103]
v_exp_f32 v79,v79
v_mfma_f32_16x16x32_bf16 v[104:107],a[160:163],v[192:195],v[40:43]
v_cvt_pk_bf16_f32 v72,v72,v73
v_cvt_pk_bf16_f32 v73,v74,v75
v_mfma_f32_16x16x32_bf16 v[104:107],a[164:167],v[200:203],v[104:107]
v_cvt_pk_bf16_f32 v74,v76,v77
v_cvt_pk_bf16_f32 v75,v78,v79
v_mfma_f32_16x16x32_bf16 v[104:107],a[168:171],v[208:211],v[104:107]
ds_read_b64_tr_b16 a[224:225],v2 offset:512
ds_read_b64_tr_b16 a[226:227],v2 offset:4608
v_mfma_f32_16x16x32_bf16 v[104:107],a[172:175],v[216:219],v[104:107]
v_accvgpr_read_b32 v9,a97
v_accvgpr_read_b32 v10,a98
v_mfma_f32_16x16x32_bf16 v[108:111],a[188:191],v[216:219],v[40:43]
v_accvgpr_read_b32 v11,a99
v_mul_f32 v8,v8,v27
v_mfma_f32_16x16x32_bf16 v[108:111],a[184:187],v[208:211],v[108:111]
global_load_lds_dwordx4 v5,s[10:11] offset:3072
v_mul_f32 v9,v9,v27
v_mfma_f32_16x16x32_bf16 v[108:111],a[180:183],v[200:203],v[108:111]
v_mul_f32 v10,v10,v27
v_mul_f32 v11,v11,v27
v_mfma_f32_16x16x32_bf16 v[108:111],a[176:179],v[192:195],v[108:111]
s_waitcnt lgkmcnt(2)
v_mul_f32 v12,v12,v27
v_mul_f32 v13,v13,v27
v_mul_f32 v14,v14,v27
v_mul_f32 v15,v15,v27
v_cvt_pk_bf16_f32 v8,v8,v9
v_cvt_pk_bf16_f32 v9,v10,v11
v_cvt_pk_bf16_f32 v10,v12,v13
v_cvt_pk_bf16_f32 v11,v14,v15
v_accvgpr_write_b32 a96,v8
v_accvgpr_write_b32 a97,v9
v_accvgpr_write_b32 a98,v10
v_accvgpr_write_b32 a99,v11
v_mfma_f32_16x16x32_bf16 a[0:3],a[192:195],v[64:67],a[0:3]
ds_read_b128 a[100:103],v7 offset:2048
v_add_u32 v1,s78,v1
v_mfma_f32_16x16x32_bf16 a[0:3],a[196:199],v[72:75],a[0:3]
ds_read_b64_tr_b16 a[228:229],v2 offset:8704
v_accvgpr_read_b32 v20,a108
v_mfma_f32_16x16x32_bf16 a[4:7],a[200:203],v[64:67],a[4:7]
ds_read_b64_tr_b16 a[230:231],v2 offset:12800
v_accvgpr_read_b32 v21,a109
v_mfma_f32_16x16x32_bf16 a[4:7],a[204:207],v[72:75],a[4:7]
ds_read_b64_tr_b16 a[232:233],v3 offset:512
v_accvgpr_read_b32 v22,a110
v_mfma_f32_16x16x32_bf16 a[8:11],a[208:211],v[64:67],a[8:11]
ds_read_b64_tr_b16 a[234:235],v3 offset:4608
ds_read_b64_tr_b16 a[236:237],v3 offset:8704
v_mfma_f32_16x16x32_bf16 a[8:11],a[212:215],v[72:75],a[8:11]
ds_read_b64_tr_b16 a[238:239],v3 offset:12800
v_accvgpr_read_b32 v23,a111
v_mfma_f32_16x16x32_bf16 a[12:15],a[216:219],v[64:67],a[12:15]
ds_read_b64_tr_b16 a[240:241],v2 offset:768
v_accvgpr_read_b32 v16,a104
v_mfma_f32_16x16x32_bf16 a[12:15],a[220:223],v[72:75],a[12:15]
s_waitcnt lgkmcnt(8)
ds_read_b64_tr_b16 a[242:243],v2 offset:4864
v_accvgpr_read_b32 v17,a105
v_mfma_f32_16x16x32_bf16 a[16:19],a[224:227],v[64:67],a[16:19]
s_waitcnt lgkmcnt(6)
ds_read_b64_tr_b16 a[244:245],v2 offset:8960
ds_read_b64_tr_b16 a[246:247],v2 offset:13056
v_mfma_f32_16x16x32_bf16 a[16:19],a[228:231],v[72:75],a[16:19]
s_waitcnt lgkmcnt(0)
ds_read_b64_tr_b16 a[248:249],v3 offset:768
v_accvgpr_read_b32 v18,a106
v_mfma_f32_16x16x32_bf16 a[20:23],a[232:235],v[64:67],a[20:23]
s_getreg_b32 s92,hwreg(HW_REG_IB_STS,0,4)
s_min_u32 s4,s92,s6
ds_read_b64_tr_b16 a[250:251],v3 offset:4864
v_mfma_f32_16x16x32_bf16 a[20:23],a[236:239],v[72:75],a[20:23]
s_mov_b32 s6,s92
ds_read_b64_tr_b16 a[252:253],v3 offset:8960
ds_read_b64_tr_b16 a[254:255],v3 offset:13056
v_mfma_f32_16x16x32_bf16 a[24:27],a[240:243],v[64:67],a[24:27]
s_cmp_gt_u32 s4,s9
s_cselect_b32 s4,15,s5
v_accvgpr_read_b32 v19,a107
v_mfma_f32_16x16x32_bf16 a[24:27],a[244:247],v[72:75],a[24:27]
s_waitcnt lgkmcnt(2)
v_exp_f32 v80,v80
v_mfma_f32_16x16x32_bf16 a[28:31],a[248:251],v[64:67],a[28:31]
s_waitcnt lgkmcnt(0)
v_exp_f32 v81,v81
v_mfma_f32_16x16x32_bf16 a[28:31],a[252:255],v[72:75],a[28:31]
s_getpc_b64 s[92:93]
s_cmp_lg_u32 s4,0
s_cbranch_scc1 L_pace_%=
v_mfma_f32_16x16x32_bf16 v[48:51],v[28:31],v[64:67],v[48:51]
v_exp_f32 v82,v82
v_mfma_f32_16x16x32_bf16 v[48:51],v[28:31],v[72:75],v[48:51]
s_add_u32 s10,s20,s74
s_addc_u32 s11,s21,0
v_cvt_pk_bf16_f32 v80,v80,v81
s_waitcnt vmcnt(16)
s_barrier
v_mfma_f32_16x16x32_bf16 v[112:115],a[128:131],v[224:227],v[44:47]
s_add_u32 m0,s76,0
v_exp_f32 v83,v83
v_mfma_f32_16x16x32_bf16 v[112:115],a[132:135],v[232:235],v[112:115]
v_exp_f32 v84,v84
v_mfma_f32_16x16x32_bf16 v[112:115],a[136:139],v[240:243],v[112:115]
global_load_lds_dwordx4 v4,s[10:11] nt
v_cvt_pk_bf16_f32 v81,v82,v83
v_mfma_f32_16x16x32_bf16 v[112:115],a[140:143],v[248:251],v[112:115]
v_exp_f32 v85,v85
v_mfma_f32_16x16x32_bf16 v[116:119],a[156:159],v[248:251],v[44:47]
v_exp_f32 v86,v86
v_mfma_f32_16x16x32_bf16 v[116:119],a[152:155],v[240:243],v[116:119]
v_exp_f32 v87,v87
v_mfma_f32_16x16x32_bf16 v[116:119],a[148:151],v[232:235],v[116:119]
v_cvt_pk_bf16_f32 v82,v84,v85
v_cvt_pk_bf16_f32 v83,v86,v87
v_mfma_f32_16x16x32_bf16 v[116:119],a[144:147],v[224:227],v[116:119]
v_exp_f32 v88,v88
v_mfma_f32_16x16x32_bf16 v[120:123],a[160:163],v[224:227],v[44:47]
v_exp_f32 v89,v89
v_mfma_f32_16x16x32_bf16 v[120:123],a[164:167],v[232:235],v[120:123]
v_exp_f32 v90,v90
v_mfma_f32_16x16x32_bf16 v[120:123],a[168:171],v[240:243],v[120:123]
v_exp_f32 v91,v91
v_mfma_f32_16x16x32_bf16 v[120:123],a[172:175],v[248:251],v[120:123]
v_exp_f32 v92,v92
v_mfma_f32_16x16x32_bf16 v[124:127],a[188:191],v[248:251],v[44:47]
v_exp_f32 v93,v93
v_mfma_f32_16x16x32_bf16 v[124:127],a[184:187],v[240:243],v[124:127]
v_exp_f32 v94,v94
v_exp_f32 v95,v95
v_mfma_f32_16x16x32_bf16 v[124:127],a[180:183],v[232:235],v[124:127]
v_cvt_pk_bf16_f32 v88,v88,v89
v_cvt_pk_bf16_f32 v89,v90,v91
v_cvt_pk_bf16_f32 v90,v92,v93
v_cvt_pk_bf16_f32 v91,v94,v95
v_mfma_f32_16x16x32_bf16 v[124:127],a[176:179],v[224:227],v[124:127]
ds_read_b128 a[128:131],v1
v_mul_f32 v16,v16,v27
v_mfma_f32_16x16x32_bf16 v[132:135],a[192:195],v[80:83],v[132:135]
v_mul_f32 v17,v17,v27
v_mul_f32 v18,v18,v27
v_mfma_f32_16x16x32_bf16 v[132:135],a[196:199],v[88:91],v[132:135]
ds_read_b128 a[132:135],v1 offset:256
v_mul_f32 v19,v19,v27
v_mfma_f32_16x16x32_bf16 v[140:143],a[200:203],v[80:83],v[140:143]
global_load_lds_dwordx4 v4,s[10:11] offset:1024 nt
v_mul_f32 v20,v20,v27
v_mfma_f32_16x16x32_bf16 v[140:143],a[204:207],v[88:91],v[140:143]
ds_read_b128 a[136:139],v1 offset:512
v_mul_f32 v21,v21,v27
v_mfma_f32_16x16x32_bf16 v[148:151],a[208:211],v[80:83],v[148:151]
v_mul_f32 v22,v22,v27
v_mul_f32 v23,v23,v27
v_mfma_f32_16x16x32_bf16 v[148:151],a[212:215],v[88:91],v[148:151]
ds_read_b128 a[140:143],v1 offset:768
v_cvt_pk_bf16_f32 v16,v16,v17
v_mfma_f32_16x16x32_bf16 v[156:159],a[216:219],v[80:83],v[156:159]
v_cvt_pk_bf16_f32 v17,v18,v19
v_cvt_pk_bf16_f32 v18,v20,v21
v_mfma_f32_16x16x32_bf16 v[156:159],a[220:223],v[88:91],v[156:159]
ds_read_b128 a[156:159],v1 offset:4864
v_cvt_pk_bf16_f32 v19,v22,v23
v_mfma_f32_16x16x32_bf16 v[164:167],a[224:227],v[80:83],v[164:167]
v_accvgpr_write_b32 a104,v16
v_accvgpr_write_b32 a105,v17
v_mfma_f32_16x16x32_bf16 v[164:167],a[228:231],v[88:91],v[164:167]
ds_read_b128 a[152:155],v1 offset:4608
v_accvgpr_write_b32 a106,v18
v_mfma_f32_16x16x32_bf16 v[172:175],a[232:235],v[80:83],v[172:175]
v_accvgpr_write_b32 a107,v19
v_accvgpr_read_b32 v12,a68
v_mfma_f32_16x16x32_bf16 v[172:175],a[236:239],v[88:91],v[172:175]
ds_read_b128 a[148:151],v1 offset:4352
v_accvgpr_read_b32 v13,a69
v_mfma_f32_16x16x32_bf16 v[180:183],a[240:243],v[80:83],v[180:183]
v_exp_f32 v96,v96
L_pd9_%=:
s_cbranch_execz L_pd13_%=
v_mfma_f32_16x16x32_bf16 v[180:183],a[244:247],v[88:91],v[180:183]
ds_read_b128 a[144:147],v1 offset:4096
v_accvgpr_read_b32 v14,a70
v_mfma_f32_16x16x32_bf16 v[188:191],a[248:251],v[80:83],v[188:191]
v_exp_f32 v97,v97
v_mfma_f32_16x16x32_bf16 v[188:191],a[252:255],v[88:91],v[188:191]
ds_read_b128 a[160:163],v1 offset:8192
v_cvt_pk_bf16_f32 v96,v96,v97
v_mfma_f32_16x16x32_bf16 v[52:55],v[28:31],v[80:83],v[52:55]
v_exp_f32 v98,v98
v_mfma_f32_16x16x32_bf16 v[52:55],v[28:31],v[88:91],v[52:55]
s_waitcnt lgkmcnt(3)
ds_read_b128 a[164:167],v1 offset:8448
v_accvgpr_read_b32 v15,a71
v_mfma_f32_16x16x32_bf16 v[64:67],a[128:131],v[128:131],v[32:35]
global_load_lds_dwordx4 v5,s[10:11] offset:2048 nt
v_accvgpr_read_b32 v8,a64
v_mfma_f32_16x16x32_bf16 v[64:67],a[132:135],v[136:139],v[64:67]
ds_read_b128 a[168:171],v1 offset:8704
v_accvgpr_read_b32 v9,a65
v_mfma_f32_16x16x32_bf16 v[64:67],a[136:139],v[144:147],v[64:67]
v_exp_f32 v99,v99
v_mfma_f32_16x16x32_bf16 v[64:67],a[140:143],v[152:155],v[64:67]
ds_read_b128 a[108:111],v7 offset:3072
v_cvt_pk_bf16_f32 v97,v98,v99
v_mfma_f32_16x16x32_bf16 v[68:71],a[156:159],v[152:155],v[32:35]
v_exp_f32 v100,v100
v_mfma_f32_16x16x32_bf16 v[68:71],a[152:155],v[144:147],v[68:71]
s_waitcnt lgkmcnt(3)
ds_read_b128 a[172:175],v1 offset:8960
v_accvgpr_read_b32 v10,a66
v_mfma_f32_16x16x32_bf16 v[68:71],a[148:151],v[136:139],v[68:71]
v_exp_f32 v101,v101
v_mfma_f32_16x16x32_bf16 v[68:71],a[144:147],v[128:131],v[68:71]
ds_read_b128 a[188:191],v1 offset:13056
v_cvt_pk_bf16_f32 v98,v100,v101
v_mfma_f32_16x16x32_bf16 v[72:75],a[160:163],v[128:131],v[32:35]
s_waitcnt lgkmcnt(4)
v_exp_f32 v102,v102
v_mfma_f32_16x16x32_bf16 v[72:75],a[164:167],v[136:139],v[72:75]
s_waitcnt lgkmcnt(3)
ds_read_b128 a[184:187],v1 offset:12800
v_accvgpr_read_b32 v11,a67
v_mfma_f32_16x16x32_bf16 v[72:75],a[168:171],v[144:147],v[72:75]
s_waitcnt lgkmcnt(2)
ds_read_b128 a[180:183],v1 offset:12544
v_mul_f32 v8,v8,v26
v_mfma_f32_16x16x32_bf16 v[72:75],a[172:175],v[152:155],v[72:75]
s_waitcnt lgkmcnt(2)
ds_read_b128 a[176:179],v1 offset:12288
v_mul_f32 v9,v9,v26
v_mfma_f32_16x16x32_bf16 v[76:79],a[188:191],v[152:155],v[32:35]
s_waitcnt lgkmcnt(2)
v_exp_f32 v103,v103
v_mfma_f32_16x16x32_bf16 v[76:79],a[184:187],v[144:147],v[76:79]
s_waitcnt lgkmcnt(1)
v_cvt_pk_bf16_f32 v99,v102,v103
v_exp_f32 v104,v104
v_exp_f32 v105,v105
v_exp_f32 v106,v106
v_exp_f32 v107,v107
v_exp_f32 v108,v108
v_exp_f32 v109,v109
v_exp_f32 v110,v110
v_exp_f32 v111,v111
v_mfma_f32_16x16x32_bf16 v[76:79],a[180:183],v[136:139],v[76:79]
s_waitcnt lgkmcnt(0)
v_cvt_pk_bf16_f32 v104,v104,v105
v_cvt_pk_bf16_f32 v105,v106,v107
v_cvt_pk_bf16_f32 v106,v108,v109
v_cvt_pk_bf16_f32 v107,v110,v111
v_mfma_f32_16x16x32_bf16 v[76:79],a[176:179],v[128:131],v[76:79]
v_exp_f32 v112,v112
v_mfma_f32_16x16x32_bf16 v[196:199],a[192:195],v[96:99],v[196:199]
global_load_lds_dwordx4 v5,s[10:11] offset:3072
v_mul_f32 v10,v10,v26
v_mfma_f32_16x16x32_bf16 v[196:199],a[196:199],v[104:107],v[196:199]
v_exp_f32 v113,v113
v_mfma_f32_16x16x32_bf16 v[204:207],a[200:203],v[96:99],v[204:207]
v_exp_f32 v114,v114
v_mfma_f32_16x16x32_bf16 v[204:207],a[204:207],v[104:107],v[204:207]
v_exp_f32 v115,v115
v_mfma_f32_16x16x32_bf16 v[212:215],a[208:211],v[96:99],v[212:215]
v_exp_f32 v116,v116
v_mfma_f32_16x16x32_bf16 v[212:215],a[212:215],v[104:107],v[212:215]
v_exp_f32 v117,v117
v_mfma_f32_16x16x32_bf16 v[220:223],a[216:219],v[96:99],v[220:223]
v_exp_f32 v118,v118
v_mfma_f32_16x16x32_bf16 v[220:223],a[220:223],v[104:107],v[220:223]
)A"
R"A(v_exp_f32 v119,v119
v_mfma_f32_16x16x32_bf16 v[228:231],a[224:227],v[96:99],v[228:231]
v_cvt_pk_bf16_f32 v112,v112,v113
v_cvt_pk_bf16_f32 v113,v114,v115
v_mfma_f32_16x16x32_bf16 v[228:231],a[228:231],v[104:107],v[228:231]
v_cvt_pk_bf16_f32 v114,v116,v117
v_cvt_pk_bf16_f32 v115,v118,v119
v_mfma_f32_16x16x32_bf16 v[236:239],a[232:235],v[96:99],v[236:239]
v_exp_f32 v120,v120
v_mfma_f32_16x16x32_bf16 v[236:239],a[236:239],v[104:107],v[236:239]
v_exp_f32 v121,v121
v_mfma_f32_16x16x32_bf16 v[244:247],a[240:243],v[96:99],v[244:247]
s_add_u32 s10,s24,s74
s_addc_u32 s11,s25,0
v_cvt_pk_bf16_f32 v120,v120,v121
v_mfma_f32_16x16x32_bf16 v[244:247],a[244:247],v[104:107],v[244:247]
s_add_u32 s74,s74,16384
v_exp_f32 v122,v122
v_mfma_f32_16x16x32_bf16 v[252:255],a[248:251],v[96:99],v[252:255]
s_add_u32 m0,s76,16384
v_exp_f32 v123,v123
v_mfma_f32_16x16x32_bf16 v[252:255],a[252:255],v[104:107],v[252:255]
s_and_b32 s74,s74,0x3ffff
v_exp_f32 v124,v124
v_mfma_f32_16x16x32_bf16 v[56:59],v[28:31],v[96:99],v[56:59]
global_load_lds_dwordx4 v4,s[10:11]
v_cvt_pk_bf16_f32 v121,v122,v123
v_mfma_f32_16x16x32_bf16 v[56:59],v[28:31],v[104:107],v[56:59]
s_or_b32 s74,s74,s69
v_exp_f32 v125,v125
v_mfma_f32_16x16x32_bf16 v[80:83],a[128:131],v[160:163],v[36:39]
s_sub_u32 s76,s76,32768
v_exp_f32 v126,v126
v_mfma_f32_16x16x32_bf16 v[80:83],a[132:135],v[168:171],v[80:83]
s_and_b32 s76,s76,0x1ffff
v_exp_f32 v127,v127
v_mfma_f32_16x16x32_bf16 v[80:83],a[136:139],v[176:179],v[80:83]
s_and_b32 s69,s75,0xfffc0000
v_cvt_pk_bf16_f32 v122,v124,v125
v_cvt_pk_bf16_f32 v123,v126,v127
v_mfma_f32_16x16x32_bf16 v[80:83],a[140:143],v[184:187],v[80:83]
v_mul_f32 v11,v11,v26
v_mul_f32 v12,v12,v26
v_mfma_f32_16x16x32_bf16 v[84:87],a[156:159],v[184:187],v[36:39]
v_mul_f32 v13,v13,v26
v_mul_f32 v14,v14,v26
v_mfma_f32_16x16x32_bf16 v[84:87],a[152:155],v[176:179],v[84:87]
v_mul_f32 v15,v15,v26
v_cvt_pk_bf16_f32 v8,v8,v9
v_mfma_f32_16x16x32_bf16 v[84:87],a[148:151],v[168:171],v[84:87]
v_cvt_pk_bf16_f32 v9,v10,v11
v_cvt_pk_bf16_f32 v10,v12,v13
v_mfma_f32_16x16x32_bf16 v[84:87],a[144:147],v[160:163],v[84:87]
v_cvt_pk_bf16_f32 v11,v14,v15
v_accvgpr_write_b32 a64,v8
v_mfma_f32_16x16x32_bf16 v[88:91],a[160:163],v[160:163],v[36:39]
v_accvgpr_write_b32 a65,v9
v_accvgpr_write_b32 a66,v10
v_mfma_f32_16x16x32_bf16 v[88:91],a[164:167],v[168:171],v[88:91]
ds_read_b128 a[68:71],v7 offset:6144
v_accvgpr_write_b32 a67,v11
v_mfma_f32_16x16x32_bf16 v[88:91],a[168:171],v[176:179],v[88:91]
v_add_u32 v2,s78,v2
v_add_u32 v3,s78,v3
v_mfma_f32_16x16x32_bf16 v[88:91],a[172:175],v[184:187],v[88:91]
s_sub_u32 s78,s77,32768
v_exp_f32 v64,v64
v_mfma_f32_16x16x32_bf16 v[92:95],a[188:191],v[184:187],v[36:39]
s_and_b32 s78,s78,0x1ffff
v_exp_f32 v65,v65
v_mfma_f32_16x16x32_bf16 v[92:95],a[184:187],v[176:179],v[92:95]
s_sub_u32 s78,s78,s77
v_exp_f32 v66,v66
v_mfma_f32_16x16x32_bf16 v[92:95],a[180:183],v[168:171],v[92:95]
global_load_lds_dwordx4 v5,s[10:11] offset:1024
v_cvt_pk_bf16_f32 v64,v64,v65
v_mfma_f32_16x16x32_bf16 v[92:95],a[176:179],v[160:163],v[92:95]
s_add_u32 s77,s77,s78
v_exp_f32 v67,v67
v_mfma_f32_16x16x32_bf16 a[32:35],a[192:195],v[112:115],a[32:35]
ds_read_b64_tr_b16 a[192:193],v2
ds_read_b64_tr_b16 a[194:195],v2 offset:4096
v_mfma_f32_16x16x32_bf16 a[32:35],a[196:199],v[120:123],a[32:35]
ds_read_b64_tr_b16 a[196:197],v2 offset:8192
v_cvt_pk_bf16_f32 v65,v66,v67
v_mfma_f32_16x16x32_bf16 a[36:39],a[200:203],v[112:115],a[36:39]
ds_read_b64_tr_b16 a[198:199],v2 offset:12288
v_accvgpr_read_b32 v20,a76
v_mfma_f32_16x16x32_bf16 a[36:39],a[204:207],v[120:123],a[36:39]
ds_read_b64_tr_b16 a[200:201],v3
v_accvgpr_read_b32 v21,a77
v_mfma_f32_16x16x32_bf16 a[40:43],a[208:211],v[112:115],a[40:43]
ds_read_b64_tr_b16 a[202:203],v3 offset:4096
ds_read_b64_tr_b16 a[204:205],v3 offset:8192
v_mfma_f32_16x16x32_bf16 a[40:43],a[212:215],v[120:123],a[40:43]
ds_read_b64_tr_b16 a[206:207],v3 offset:12288
v_accvgpr_read_b32 v22,a78
v_mfma_f32_16x16x32_bf16 a[44:47],a[216:219],v[112:115],a[44:47]
ds_read_b64_tr_b16 a[208:209],v2 offset:256
v_accvgpr_read_b32 v23,a79
v_mfma_f32_16x16x32_bf16 a[44:47],a[220:223],v[120:123],a[44:47]
ds_read_b64_tr_b16 a[210:211],v2 offset:4352
v_accvgpr_read_b32 v16,a72
v_mfma_f32_16x16x32_bf16 a[48:51],a[224:227],v[112:115],a[48:51]
ds_read_b64_tr_b16 a[212:213],v2 offset:8448
ds_read_b64_tr_b16 a[214:215],v2 offset:12544
v_mfma_f32_16x16x32_bf16 a[48:51],a[228:231],v[120:123],a[48:51]
v_exp_f32 v68,v68
v_mfma_f32_16x16x32_bf16 a[52:55],a[232:235],v[112:115],a[52:55]
v_exp_f32 v69,v69
v_mfma_f32_16x16x32_bf16 a[52:55],a[236:239],v[120:123],a[52:55]
v_exp_f32 v70,v70
v_mfma_f32_16x16x32_bf16 a[56:59],a[240:243],v[112:115],a[56:59]
v_exp_f32 v71,v71
v_mfma_f32_16x16x32_bf16 a[56:59],a[244:247],v[120:123],a[56:59]
ds_read_b64_tr_b16 a[216:217],v3 offset:256
ds_read_b64_tr_b16 a[218:219],v3 offset:4352
v_mfma_f32_16x16x32_bf16 a[60:63],a[248:251],v[112:115],a[60:63]
global_load_lds_dwordx4 v4,s[10:11] offset:2048
v_cvt_pk_bf16_f32 v66,v68,v69
v_mfma_f32_16x16x32_bf16 a[60:63],a[252:255],v[120:123],a[60:63]
ds_read_b64_tr_b16 a[220:221],v3 offset:8448
v_cvt_pk_bf16_f32 v67,v70,v71
v_mfma_f32_16x16x32_bf16 v[60:63],v[28:31],v[112:115],v[60:63]
v_exp_f32 v72,v72
v_mfma_f32_16x16x32_bf16 v[60:63],v[28:31],v[120:123],v[60:63]
v_exp_f32 v73,v73
v_mfma_f32_16x16x32_bf16 v[96:99],a[128:131],v[192:195],v[40:43]
v_exp_f32 v74,v74
v_mfma_f32_16x16x32_bf16 v[96:99],a[132:135],v[200:203],v[96:99]
v_exp_f32 v75,v75
v_mfma_f32_16x16x32_bf16 v[96:99],a[136:139],v[208:211],v[96:99]
v_exp_f32 v76,v76
v_mfma_f32_16x16x32_bf16 v[96:99],a[140:143],v[216:219],v[96:99]
v_exp_f32 v77,v77
v_mfma_f32_16x16x32_bf16 v[100:103],a[156:159],v[216:219],v[40:43]
v_exp_f32 v78,v78
v_mfma_f32_16x16x32_bf16 v[100:103],a[152:155],v[208:211],v[100:103]
v_exp_f32 v79,v79
v_mfma_f32_16x16x32_bf16 v[100:103],a[148:151],v[200:203],v[100:103]
ds_read_b64_tr_b16 a[222:223],v3 offset:12544
v_cvt_pk_bf16_f32 v72,v72,v73
v_mfma_f32_16x16x32_bf16 v[100:103],a[144:147],v[192:195],v[100:103]
v_cvt_pk_bf16_f32 v73,v74,v75
v_cvt_pk_bf16_f32 v74,v76,v77
v_mfma_f32_16x16x32_bf16 v[104:107],a[160:163],v[192:195],v[40:43]
ds_read_b64_tr_b16 a[224:225],v2 offset:512
v_cvt_pk_bf16_f32 v75,v78,v79
v_mfma_f32_16x16x32_bf16 v[104:107],a[164:167],v[200:203],v[104:107]
ds_read_b64_tr_b16 a[226:227],v2 offset:4608
ds_read_b64_tr_b16 a[228:229],v2 offset:8704
v_mfma_f32_16x16x32_bf16 v[104:107],a[168:171],v[208:211],v[104:107]
ds_read_b64_tr_b16 a[230:231],v2 offset:12800
v_accvgpr_read_b32 v17,a73
v_mfma_f32_16x16x32_bf16 v[104:107],a[172:175],v[216:219],v[104:107]
ds_read_b64_tr_b16 a[232:233],v3 offset:512
v_accvgpr_read_b32 v18,a74
v_mfma_f32_16x16x32_bf16 v[108:111],a[188:191],v[216:219],v[40:43]
global_load_lds_dwordx4 v5,s[10:11] offset:3072
ds_read_b64_tr_b16 a[234:235],v3 offset:4608
v_mfma_f32_16x16x32_bf16 v[108:111],a[184:187],v[208:211],v[108:111]
ds_read_b64_tr_b16 a[236:237],v3 offset:8704
ds_read_b64_tr_b16 a[238:239],v3 offset:12800
v_mfma_f32_16x16x32_bf16 v[108:111],a[180:183],v[200:203],v[108:111]
ds_read_b64_tr_b16 a[240:241],v2 offset:768
v_accvgpr_read_b32 v19,a75
v_mfma_f32_16x16x32_bf16 v[108:111],a[176:179],v[192:195],v[108:111]
s_waitcnt lgkmcnt(9)
ds_read_b64_tr_b16 a[242:243],v2 offset:4864
v_mul_f32 v16,v16,v26
v_mfma_f32_16x16x32_bf16 a[0:3],a[192:195],v[64:67],a[0:3]
ds_read_b64_tr_b16 a[244:245],v2 offset:8960
v_mul_f32 v17,v17,v26
v_mfma_f32_16x16x32_bf16 a[0:3],a[196:199],v[72:75],a[0:3]
ds_read_b64_tr_b16 a[246:247],v2 offset:13056
ds_read_b64_tr_b16 a[248:249],v3 offset:768
v_mfma_f32_16x16x32_bf16 a[4:7],a[200:203],v[64:67],a[4:7]
ds_read_b64_tr_b16 a[250:251],v3 offset:4864
v_mul_f32 v18,v18,v26
v_mfma_f32_16x16x32_bf16 a[4:7],a[204:207],v[72:75],a[4:7]
ds_read_b64_tr_b16 a[252:253],v3 offset:8960
v_mul_f32 v19,v19,v26
v_mfma_f32_16x16x32_bf16 a[8:11],a[208:211],v[64:67],a[8:11]
ds_read_b64_tr_b16 a[254:255],v3 offset:13056
v_mul_f32 v20,v20,v26
v_mfma_f32_16x16x32_bf16 a[8:11],a[212:215],v[72:75],a[8:11]
v_exp_f32 v80,v80
v_mfma_f32_16x16x32_bf16 a[12:15],a[216:219],v[64:67],a[12:15]
v_exp_f32 v81,v81
v_mfma_f32_16x16x32_bf16 a[12:15],a[220:223],v[72:75],a[12:15]
s_waitcnt lgkmcnt(6)
v_exp_f32 v82,v82
v_mfma_f32_16x16x32_bf16 a[16:19],a[224:227],v[64:67],a[16:19]
v_exp_f32 v83,v83
v_mfma_f32_16x16x32_bf16 a[16:19],a[228:231],v[72:75],a[16:19]
v_exp_f32 v84,v84
v_mfma_f32_16x16x32_bf16 a[20:23],a[232:235],v[64:67],a[20:23]
s_getreg_b32 s92,hwreg(HW_REG_IB_STS,0,4)
s_min_u32 s4,s92,s6
s_mov_b32 s6,s92
v_mfma_f32_16x16x32_bf16 a[20:23],a[236:239],v[72:75],a[20:23]
s_cmp_ge_u32 s4,s9
s_cselect_b32 s4,15,s5
v_cvt_pk_bf16_f32 v80,v80,v81
v_mfma_f32_16x16x32_bf16 a[24:27],a[240:243],v[64:67],a[24:27]
s_waitcnt lgkmcnt(0)
v_exp_f32 v85,v85
v_mfma_f32_16x16x32_bf16 a[24:27],a[244:247],v[72:75],a[24:27]
v_exp_f32 v86,v86
v_mfma_f32_16x16x32_bf16 a[28:31],a[248:251],v[64:67],a[28:31]
v_exp_f32 v87,v87
v_mfma_f32_16x16x32_bf16 a[28:31],a[252:255],v[72:75],a[28:31]
s_getpc_b64 s[92:93]
s_cmp_lg_u32 s4,0
s_cbranch_scc1 L_pace_%=
v_mfma_f32_16x16x32_bf16 v[48:51],v[28:31],v[64:67],v[48:51]
v_cvt_pk_bf16_f32 v81,v82,v83
v_cvt_pk_bf16_f32 v82,v84,v85
v_mfma_f32_16x16x32_bf16 v[48:51],v[28:31],v[72:75],v[48:51]
s_add_u32 s10,s20,s74
s_addc_u32 s11,s21,0
v_cvt_pk_bf16_f32 v83,v86,v87
s_waitcnt vmcnt(16)
s_barrier
v_mfma_f32_16x16x32_bf16 v[112:115],a[128:131],v[224:227],v[44:47]
s_add_u32 m0,s76,0
v_exp_f32 v88,v88
v_mfma_f32_16x16x32_bf16 v[112:115],a[132:135],v[232:235],v[112:115]
v_exp_f32 v89,v89
v_mfma_f32_16x16x32_bf16 v[112:115],a[136:139],v[240:243],v[112:115]
global_load_lds_dwordx4 v4,s[10:11] nt
v_cvt_pk_bf16_f32 v88,v88,v89
v_mfma_f32_16x16x32_bf16 v[112:115],a[140:143],v[248:251],v[112:115]
v_exp_f32 v90,v90
v_mfma_f32_16x16x32_bf16 v[116:119],a[156:159],v[248:251],v[44:47]
v_exp_f32 v91,v91
v_mfma_f32_16x16x32_bf16 v[116:119],a[152:155],v[240:243],v[116:119]
v_exp_f32 v92,v92
v_mfma_f32_16x16x32_bf16 v[116:119],a[148:151],v[232:235],v[116:119]
v_exp_f32 v93,v93
v_mfma_f32_16x16x32_bf16 v[116:119],a[144:147],v[224:227],v[116:119]
v_exp_f32 v94,v94
v_mfma_f32_16x16x32_bf16 v[120:123],a[160:163],v[224:227],v[44:47]
v_exp_f32 v95,v95
v_mfma_f32_16x16x32_bf16 v[120:123],a[164:167],v[232:235],v[120:123]
v_cvt_pk_bf16_f32 v89,v90,v91
v_cvt_pk_bf16_f32 v90,v92,v93
v_mfma_f32_16x16x32_bf16 v[120:123],a[168:171],v[240:243],v[120:123]
v_cvt_pk_bf16_f32 v91,v94,v95
v_mul_f32 v21,v21,v26
v_mfma_f32_16x16x32_bf16 v[120:123],a[172:175],v[248:251],v[120:123]
v_mul_f32 v22,v22,v26
v_mul_f32 v23,v23,v26
v_mfma_f32_16x16x32_bf16 v[124:127],a[188:191],v[248:251],v[44:47]
v_cvt_pk_bf16_f32 v16,v16,v17
v_cvt_pk_bf16_f32 v17,v18,v19
v_mfma_f32_16x16x32_bf16 v[124:127],a[184:187],v[240:243],v[124:127]
v_cvt_pk_bf16_f32 v18,v20,v21
v_cvt_pk_bf16_f32 v19,v22,v23
v_mfma_f32_16x16x32_bf16 v[124:127],a[180:183],v[232:235],v[124:127]
v_accvgpr_write_b32 a72,v16
v_accvgpr_write_b32 a73,v17
v_mfma_f32_16x16x32_bf16 v[124:127],a[176:179],v[224:227],v[124:127]
v_accvgpr_write_b32 a74,v18
v_accvgpr_write_b32 a75,v19
v_mfma_f32_16x16x32_bf16 v[132:135],a[192:195],v[80:83],v[132:135]
ds_read_b128 a[76:79],v7 offset:7168
v_add_u32 v1,s78,v1
v_mfma_f32_16x16x32_bf16 v[132:135],a[196:199],v[88:91],v[132:135]
v_exp_f32 v96,v96
v_mfma_f32_16x16x32_bf16 v[140:143],a[200:203],v[80:83],v[140:143]
global_load_lds_dwordx4 v4,s[10:11] offset:1024 nt
ds_read_b128 a[128:131],v1
v_mfma_f32_16x16x32_bf16 v[140:143],a[204:207],v[88:91],v[140:143]
v_exp_f32 v97,v97
v_mfma_f32_16x16x32_bf16 v[148:151],a[208:211],v[80:83],v[148:151]
ds_read_b128 a[132:135],v1 offset:256
v_cvt_pk_bf16_f32 v96,v96,v97
v_mfma_f32_16x16x32_bf16 v[148:151],a[212:215],v[88:91],v[148:151]
v_exp_f32 v98,v98
v_mfma_f32_16x16x32_bf16 v[156:159],a[216:219],v[80:83],v[156:159]
ds_read_b128 a[136:139],v1 offset:512
v_accvgpr_read_b32 v12,a84
v_mfma_f32_16x16x32_bf16 v[156:159],a[220:223],v[88:91],v[156:159]
v_exp_f32 v99,v99
v_mfma_f32_16x16x32_bf16 v[164:167],a[224:227],v[80:83],v[164:167]
ds_read_b128 a[140:143],v1 offset:768
v_cvt_pk_bf16_f32 v97,v98,v99
v_mfma_f32_16x16x32_bf16 v[164:167],a[228:231],v[88:91],v[164:167]
)A"
R"A(v_exp_f32 v100,v100
v_mfma_f32_16x16x32_bf16 v[172:175],a[232:235],v[80:83],v[172:175]
ds_read_b128 a[156:159],v1 offset:4864
v_accvgpr_read_b32 v13,a85
v_mfma_f32_16x16x32_bf16 v[172:175],a[236:239],v[88:91],v[172:175]
v_exp_f32 v101,v101
v_mfma_f32_16x16x32_bf16 v[180:183],a[240:243],v[80:83],v[180:183]
ds_read_b128 a[152:155],v1 offset:4608
v_cvt_pk_bf16_f32 v98,v100,v101
v_mfma_f32_16x16x32_bf16 v[180:183],a[244:247],v[88:91],v[180:183]
v_exp_f32 v102,v102
v_mfma_f32_16x16x32_bf16 v[188:191],a[248:251],v[80:83],v[188:191]
ds_read_b128 a[148:151],v1 offset:4352
v_accvgpr_read_b32 v14,a86
v_mfma_f32_16x16x32_bf16 v[188:191],a[252:255],v[88:91],v[188:191]
v_exp_f32 v103,v103
v_mfma_f32_16x16x32_bf16 v[52:55],v[28:31],v[80:83],v[52:55]
ds_read_b128 a[144:147],v1 offset:4096
v_cvt_pk_bf16_f32 v99,v102,v103
v_mfma_f32_16x16x32_bf16 v[52:55],v[28:31],v[88:91],v[52:55]
s_waitcnt lgkmcnt(4)
v_exp_f32 v104,v104
v_mfma_f32_16x16x32_bf16 v[64:67],a[128:131],v[128:131],v[32:35]
global_load_lds_dwordx4 v5,s[10:11] offset:2048 nt
ds_read_b128 a[160:163],v1 offset:8192
v_mfma_f32_16x16x32_bf16 v[64:67],a[132:135],v[136:139],v[64:67]
v_exp_f32 v105,v105
v_mfma_f32_16x16x32_bf16 v[64:67],a[136:139],v[144:147],v[64:67]
ds_read_b128 a[164:167],v1 offset:8448
v_cvt_pk_bf16_f32 v104,v104,v105
v_mfma_f32_16x16x32_bf16 v[64:67],a[140:143],v[152:155],v[64:67]
s_waitcnt lgkmcnt(4)
v_exp_f32 v106,v106
v_mfma_f32_16x16x32_bf16 v[68:71],a[156:159],v[152:155],v[32:35]
ds_read_b128 a[168:171],v1 offset:8704
v_accvgpr_read_b32 v15,a87
v_mfma_f32_16x16x32_bf16 v[68:71],a[152:155],v[144:147],v[68:71]
s_waitcnt lgkmcnt(4)
v_exp_f32 v107,v107
v_mfma_f32_16x16x32_bf16 v[68:71],a[148:151],v[136:139],v[68:71]
s_waitcnt lgkmcnt(3)
ds_read_b128 a[172:175],v1 offset:8960
v_cvt_pk_bf16_f32 v105,v106,v107
v_mfma_f32_16x16x32_bf16 v[68:71],a[144:147],v[128:131],v[68:71]
s_waitcnt lgkmcnt(3)
v_exp_f32 v108,v108
v_mfma_f32_16x16x32_bf16 v[72:75],a[160:163],v[128:131],v[32:35]
s_waitcnt lgkmcnt(2)
ds_read_b128 a[188:191],v1 offset:13056
v_accvgpr_read_b32 v8,a80
v_mfma_f32_16x16x32_bf16 v[72:75],a[164:167],v[136:139],v[72:75]
s_waitcnt lgkmcnt(2)
ds_read_b128 a[184:187],v1 offset:12800
v_accvgpr_read_b32 v9,a81
v_mfma_f32_16x16x32_bf16 v[72:75],a[168:171],v[144:147],v[72:75]
s_waitcnt lgkmcnt(2)
ds_read_b128 a[180:183],v1 offset:12544
v_accvgpr_read_b32 v10,a82
v_mfma_f32_16x16x32_bf16 v[72:75],a[172:175],v[152:155],v[72:75]
s_waitcnt lgkmcnt(2)
ds_read_b128 a[176:179],v1 offset:12288
v_accvgpr_read_b32 v11,a83
v_mfma_f32_16x16x32_bf16 v[76:79],a[188:191],v[152:155],v[32:35]
s_waitcnt lgkmcnt(2)
v_exp_f32 v109,v109
v_mfma_f32_16x16x32_bf16 v[76:79],a[184:187],v[144:147],v[76:79]
s_waitcnt lgkmcnt(1)
v_exp_f32 v110,v110
v_exp_f32 v111,v111
v_mfma_f32_16x16x32_bf16 v[76:79],a[180:183],v[136:139],v[76:79]
s_waitcnt lgkmcnt(0)
v_cvt_pk_bf16_f32 v106,v108,v109
v_cvt_pk_bf16_f32 v107,v110,v111
v_mfma_f32_16x16x32_bf16 v[76:79],a[176:179],v[128:131],v[76:79]
v_mul_f32 v8,v8,v26
v_mul_f32 v9,v9,v26
v_mfma_f32_16x16x32_bf16 v[196:199],a[192:195],v[96:99],v[196:199]
global_load_lds_dwordx4 v5,s[10:11] offset:3072
v_mul_f32 v10,v10,v26
v_mfma_f32_16x16x32_bf16 v[196:199],a[196:199],v[104:107],v[196:199]
v_mul_f32 v11,v11,v26
v_mul_f32 v12,v12,v26
v_mfma_f32_16x16x32_bf16 v[204:207],a[200:203],v[96:99],v[204:207]
v_mul_f32 v13,v13,v26
v_mul_f32 v14,v14,v26
v_mfma_f32_16x16x32_bf16 v[204:207],a[204:207],v[104:107],v[204:207]
v_mul_f32 v15,v15,v26
v_cvt_pk_bf16_f32 v8,v8,v9
v_mfma_f32_16x16x32_bf16 v[212:215],a[208:211],v[96:99],v[212:215]
v_cvt_pk_bf16_f32 v9,v10,v11
v_cvt_pk_bf16_f32 v10,v12,v13
v_mfma_f32_16x16x32_bf16 v[212:215],a[212:215],v[104:107],v[212:215]
v_cvt_pk_bf16_f32 v11,v14,v15
v_accvgpr_write_b32 a80,v8
v_mfma_f32_16x16x32_bf16 v[220:223],a[216:219],v[96:99],v[220:223]
v_accvgpr_write_b32 a81,v9
v_accvgpr_write_b32 a82,v10
v_mfma_f32_16x16x32_bf16 v[220:223],a[220:223],v[104:107],v[220:223]
ds_read_b128 a[84:87],v7
v_accvgpr_write_b32 a83,v11
v_mfma_f32_16x16x32_bf16 v[228:231],a[224:227],v[96:99],v[228:231]
v_exp_f32 v112,v112
v_mfma_f32_16x16x32_bf16 v[228:231],a[228:231],v[104:107],v[228:231]
v_exp_f32 v113,v113
v_mfma_f32_16x16x32_bf16 v[236:239],a[232:235],v[96:99],v[236:239]
v_exp_f32 v114,v114
v_mfma_f32_16x16x32_bf16 v[236:239],a[236:239],v[104:107],v[236:239]
v_exp_f32 v115,v115
v_mfma_f32_16x16x32_bf16 v[244:247],a[240:243],v[96:99],v[244:247]
s_add_u32 s10,s24,s74
s_addc_u32 s11,s25,0
v_cvt_pk_bf16_f32 v112,v112,v113
v_mfma_f32_16x16x32_bf16 v[244:247],a[244:247],v[104:107],v[244:247]
s_add_u32 s74,s74,16384
v_exp_f32 v116,v116
v_mfma_f32_16x16x32_bf16 v[252:255],a[248:251],v[96:99],v[252:255]
s_add_u32 m0,s76,16384
v_exp_f32 v117,v117
v_mfma_f32_16x16x32_bf16 v[252:255],a[252:255],v[104:107],v[252:255]
s_and_b32 s74,s74,0x3ffff
v_exp_f32 v118,v118
v_mfma_f32_16x16x32_bf16 v[56:59],v[28:31],v[96:99],v[56:59]
global_load_lds_dwordx4 v4,s[10:11]
v_cvt_pk_bf16_f32 v113,v114,v115
v_mfma_f32_16x16x32_bf16 v[56:59],v[28:31],v[104:107],v[56:59]
s_or_b32 s74,s74,s69
v_exp_f32 v119,v119
v_mfma_f32_16x16x32_bf16 v[80:83],a[128:131],v[160:163],v[36:39]
s_sub_u32 s76,s76,32768
v_cvt_pk_bf16_f32 v114,v116,v117
v_cvt_pk_bf16_f32 v115,v118,v119
v_mfma_f32_16x16x32_bf16 v[80:83],a[132:135],v[168:171],v[80:83]
s_and_b32 s76,s76,0x1ffff
v_exp_f32 v120,v120
v_mfma_f32_16x16x32_bf16 v[80:83],a[136:139],v[176:179],v[80:83]
v_exp_f32 v121,v121
v_mfma_f32_16x16x32_bf16 v[80:83],a[140:143],v[184:187],v[80:83]
v_exp_f32 v122,v122
v_mfma_f32_16x16x32_bf16 v[84:87],a[156:159],v[184:187],v[36:39]
v_exp_f32 v123,v123
v_mfma_f32_16x16x32_bf16 v[84:87],a[152:155],v[176:179],v[84:87]
v_exp_f32 v124,v124
v_mfma_f32_16x16x32_bf16 v[84:87],a[148:151],v[168:171],v[84:87]
v_exp_f32 v125,v125
v_mfma_f32_16x16x32_bf16 v[84:87],a[144:147],v[160:163],v[84:87]
v_exp_f32 v126,v126
v_mfma_f32_16x16x32_bf16 v[88:91],a[160:163],v[160:163],v[36:39]
v_exp_f32 v127,v127
v_mfma_f32_16x16x32_bf16 v[88:91],a[164:167],v[168:171],v[88:91]
v_cvt_pk_bf16_f32 v120,v120,v121
v_cvt_pk_bf16_f32 v121,v122,v123
v_mfma_f32_16x16x32_bf16 v[88:91],a[168:171],v[176:179],v[88:91]
v_cvt_pk_bf16_f32 v122,v124,v125
v_cvt_pk_bf16_f32 v123,v126,v127
v_mfma_f32_16x16x32_bf16 v[88:91],a[172:175],v[184:187],v[88:91]
v_add_u32 v2,s78,v2
v_add_u32 v3,s78,v3
v_mfma_f32_16x16x32_bf16 v[92:95],a[188:191],v[184:187],v[36:39]
s_sub_u32 s78,s77,32768
v_exp_f32 v64,v64
v_mfma_f32_16x16x32_bf16 v[92:95],a[184:187],v[176:179],v[92:95]
s_and_b32 s78,s78,0x1ffff
v_exp_f32 v65,v65
v_mfma_f32_16x16x32_bf16 v[92:95],a[180:183],v[168:171],v[92:95]
global_load_lds_dwordx4 v5,s[10:11] offset:1024
v_cvt_pk_bf16_f32 v64,v64,v65
v_mfma_f32_16x16x32_bf16 v[92:95],a[176:179],v[160:163],v[92:95]
s_sub_u32 s78,s78,s77
v_exp_f32 v66,v66
v_mfma_f32_16x16x32_bf16 a[32:35],a[192:195],v[112:115],a[32:35]
s_add_u32 s77,s77,s78
ds_read_b64_tr_b16 a[192:193],v2
ds_read_b64_tr_b16 a[194:195],v2 offset:4096
v_mfma_f32_16x16x32_bf16 a[32:35],a[196:199],v[120:123],a[32:35]
ds_read_b64_tr_b16 a[196:197],v2 offset:8192
v_accvgpr_read_b32 v20,a92
v_mfma_f32_16x16x32_bf16 a[36:39],a[200:203],v[112:115],a[36:39]
ds_read_b64_tr_b16 a[198:199],v2 offset:12288
v_accvgpr_read_b32 v21,a93
v_mfma_f32_16x16x32_bf16 a[36:39],a[204:207],v[120:123],a[36:39]
ds_read_b64_tr_b16 a[200:201],v3
v_accvgpr_read_b32 v22,a94
v_mfma_f32_16x16x32_bf16 a[40:43],a[208:211],v[112:115],a[40:43]
ds_read_b64_tr_b16 a[202:203],v3 offset:4096
ds_read_b64_tr_b16 a[204:205],v3 offset:8192
v_mfma_f32_16x16x32_bf16 a[40:43],a[212:215],v[120:123],a[40:43]
ds_read_b64_tr_b16 a[206:207],v3 offset:12288
v_accvgpr_read_b32 v23,a95
v_mfma_f32_16x16x32_bf16 a[44:47],a[216:219],v[112:115],a[44:47]
ds_read_b64_tr_b16 a[208:209],v2 offset:256
v_accvgpr_read_b32 v16,a88
v_mfma_f32_16x16x32_bf16 a[44:47],a[220:223],v[120:123],a[44:47]
ds_read_b64_tr_b16 a[210:211],v2 offset:4352
v_accvgpr_read_b32 v17,a89
v_mfma_f32_16x16x32_bf16 a[48:51],a[224:227],v[112:115],a[48:51]
ds_read_b64_tr_b16 a[212:213],v2 offset:8448
ds_read_b64_tr_b16 a[214:215],v2 offset:12544
v_mfma_f32_16x16x32_bf16 a[48:51],a[228:231],v[120:123],a[48:51]
v_exp_f32 v67,v67
v_mfma_f32_16x16x32_bf16 a[52:55],a[232:235],v[112:115],a[52:55]
v_exp_f32 v68,v68
v_mfma_f32_16x16x32_bf16 a[52:55],a[236:239],v[120:123],a[52:55]
v_exp_f32 v69,v69
v_mfma_f32_16x16x32_bf16 a[56:59],a[240:243],v[112:115],a[56:59]
v_exp_f32 v70,v70
v_mfma_f32_16x16x32_bf16 a[56:59],a[244:247],v[120:123],a[56:59]
v_exp_f32 v71,v71
v_mfma_f32_16x16x32_bf16 a[60:63],a[248:251],v[112:115],a[60:63]
global_load_lds_dwordx4 v4,s[10:11] offset:2048
ds_read_b64_tr_b16 a[216:217],v3 offset:256
v_mfma_f32_16x16x32_bf16 a[60:63],a[252:255],v[120:123],a[60:63]
ds_read_b64_tr_b16 a[218:219],v3 offset:4352
v_cvt_pk_bf16_f32 v65,v66,v67
v_mfma_f32_16x16x32_bf16 v[60:63],v[28:31],v[112:115],v[60:63]
v_cvt_pk_bf16_f32 v66,v68,v69
v_cvt_pk_bf16_f32 v67,v70,v71
v_mfma_f32_16x16x32_bf16 v[60:63],v[28:31],v[120:123],v[60:63]
v_exp_f32 v72,v72
v_mfma_f32_16x16x32_bf16 v[96:99],a[128:131],v[192:195],v[40:43]
v_exp_f32 v73,v73
v_mfma_f32_16x16x32_bf16 v[96:99],a[132:135],v[200:203],v[96:99]
v_exp_f32 v74,v74
v_mfma_f32_16x16x32_bf16 v[96:99],a[136:139],v[208:211],v[96:99]
v_exp_f32 v75,v75
v_mfma_f32_16x16x32_bf16 v[96:99],a[140:143],v[216:219],v[96:99]
L_pd10_%=:
s_cbranch_execz L_t1b_%=
v_exp_f32 v76,v76
v_mfma_f32_16x16x32_bf16 v[100:103],a[156:159],v[216:219],v[40:43]
v_exp_f32 v77,v77
v_mfma_f32_16x16x32_bf16 v[100:103],a[152:155],v[208:211],v[100:103]
v_exp_f32 v78,v78
v_mfma_f32_16x16x32_bf16 v[100:103],a[148:151],v[200:203],v[100:103]
v_exp_f32 v79,v79
v_mfma_f32_16x16x32_bf16 v[100:103],a[144:147],v[192:195],v[100:103]
ds_read_b64_tr_b16 a[220:221],v3 offset:8448
ds_read_b64_tr_b16 a[222:223],v3 offset:12544
v_mfma_f32_16x16x32_bf16 v[104:107],a[160:163],v[192:195],v[40:43]
v_cvt_pk_bf16_f32 v72,v72,v73
v_cvt_pk_bf16_f32 v73,v74,v75
v_mfma_f32_16x16x32_bf16 v[104:107],a[164:167],v[200:203],v[104:107]
v_cvt_pk_bf16_f32 v74,v76,v77
v_cvt_pk_bf16_f32 v75,v78,v79
v_mfma_f32_16x16x32_bf16 v[104:107],a[168:171],v[208:211],v[104:107]
ds_read_b64_tr_b16 a[224:225],v2 offset:512
ds_read_b64_tr_b16 a[226:227],v2 offset:4608
v_mfma_f32_16x16x32_bf16 v[104:107],a[172:175],v[216:219],v[104:107]
ds_read_b64_tr_b16 a[228:229],v2 offset:8704
v_accvgpr_read_b32 v18,a90
v_mfma_f32_16x16x32_bf16 v[108:111],a[188:191],v[216:219],v[40:43]
global_load_lds_dwordx4 v5,s[10:11] offset:3072
ds_read_b64_tr_b16 a[230:231],v2 offset:12800
v_mfma_f32_16x16x32_bf16 v[108:111],a[184:187],v[208:211],v[108:111]
ds_read_b64_tr_b16 a[232:233],v3 offset:512
v_accvgpr_read_b32 v19,a91
v_mfma_f32_16x16x32_bf16 v[108:111],a[180:183],v[200:203],v[108:111]
ds_read_b64_tr_b16 a[234:235],v3 offset:4608
ds_read_b64_tr_b16 a[236:237],v3 offset:8704
v_mfma_f32_16x16x32_bf16 v[108:111],a[176:179],v[192:195],v[108:111]
s_waitcnt lgkmcnt(7)
ds_read_b64_tr_b16 a[238:239],v3 offset:12800
v_mul_f32 v16,v16,v26
v_mfma_f32_16x16x32_bf16 a[0:3],a[192:195],v[64:67],a[0:3]
ds_read_b64_tr_b16 a[240:241],v2 offset:768
v_mul_f32 v17,v17,v26
v_mfma_f32_16x16x32_bf16 a[0:3],a[196:199],v[72:75],a[0:3]
ds_read_b64_tr_b16 a[242:243],v2 offset:4864
v_mul_f32 v18,v18,v26
v_mfma_f32_16x16x32_bf16 a[4:7],a[200:203],v[64:67],a[4:7]
ds_read_b64_tr_b16 a[244:245],v2 offset:8960
ds_read_b64_tr_b16 a[246:247],v2 offset:13056
v_mfma_f32_16x16x32_bf16 a[4:7],a[204:207],v[72:75],a[4:7]
ds_read_b64_tr_b16 a[248:249],v3 offset:768
v_mul_f32 v19,v19,v26
v_mfma_f32_16x16x32_bf16 a[8:11],a[208:211],v[64:67],a[8:11]
ds_read_b64_tr_b16 a[250:251],v3 offset:4864
v_mul_f32 v20,v20,v26
v_mfma_f32_16x16x32_bf16 a[8:11],a[212:215],v[72:75],a[8:11]
v_mul_f32 v21,v21,v26
v_mul_f32 v22,v22,v26
v_mul_f32 v23,v23,v26
v_cvt_pk_bf16_f32 v16,v16,v17
v_cvt_pk_bf16_f32 v17,v18,v19
v_cvt_pk_bf16_f32 v18,v20,v21
v_cvt_pk_bf16_f32 v19,v22,v23
v_accvgpr_write_b32 a88,v16
v_accvgpr_write_b32 a89,v17
v_accvgpr_write_b32 a90,v18
v_accvgpr_write_b32 a91,v19
v_mfma_f32_16x16x32_bf16 a[12:15],a[216:219],v[64:67],a[12:15]
ds_read_b128 a[92:95],v7 offset:1024
)A"
R"A(v_mov_b32 v8,v7
v_mfma_f32_16x16x32_bf16 a[12:15],a[220:223],v[72:75],a[12:15]
s_waitcnt lgkmcnt(7)
ds_read_b64_tr_b16 a[252:253],v3 offset:8960
v_add_u32 v1,s78,v1
v_mfma_f32_16x16x32_bf16 a[16:19],a[224:227],v[64:67],a[16:19]
ds_read_b64_tr_b16 a[254:255],v3 offset:13056
v_mfma_f32_16x16x32_bf16 a[16:19],a[228:231],v[72:75],a[16:19]
s_waitcnt lgkmcnt(4)
v_exp_f32 v80,v80
v_mfma_f32_16x16x32_bf16 a[20:23],a[232:235],v[64:67],a[20:23]
s_getreg_b32 s92,hwreg(HW_REG_IB_STS,0,4)
s_min_u32 s4,s92,s6
s_mov_b32 s6,s92
v_mfma_f32_16x16x32_bf16 a[20:23],a[236:239],v[72:75],a[20:23]
s_cmp_ge_u32 s4,s9
s_cselect_b32 s4,15,s5
ds_read_b128 v[24:27],v8 offset:4096
v_mfma_f32_16x16x32_bf16 a[24:27],a[240:243],v[64:67],a[24:27]
v_exp_f32 v81,v81
v_mfma_f32_16x16x32_bf16 a[24:27],a[244:247],v[72:75],a[24:27]
s_waitcnt lgkmcnt(4)
v_exp_f32 v82,v82
v_mfma_f32_16x16x32_bf16 a[28:31],a[248:251],v[64:67],a[28:31]
s_waitcnt lgkmcnt(1)
v_exp_f32 v83,v83
v_mfma_f32_16x16x32_bf16 a[28:31],a[252:255],v[72:75],a[28:31]
v_exp_f32 v84,v84
v_mfma_f32_16x16x32_bf16 v[48:51],v[28:31],v[64:67],v[48:51]
s_getpc_b64 s[92:93]
s_cmp_lg_u32 s4,0
s_cbranch_scc1 L_pace_%=
v_exp_f32 v85,v85
v_exp_f32 v86,v86
v_exp_f32 v87,v87
v_exp_f32 v88,v88
v_exp_f32 v89,v89
v_exp_f32 v90,v90
v_exp_f32 v91,v91
v_exp_f32 v92,v92
v_exp_f32 v93,v93
v_exp_f32 v94,v94
v_exp_f32 v95,v95
v_mfma_f32_16x16x32_bf16 v[48:51],v[28:31],v[72:75],v[48:51]
ds_read_b32 v7,v8 offset:5120
v_cvt_pk_bf16_f32 v80,v80,v81
v_cvt_pk_bf16_f32 v81,v82,v83
v_cvt_pk_bf16_f32 v82,v84,v85
v_cvt_pk_bf16_f32 v83,v86,v87
v_cvt_pk_bf16_f32 v88,v88,v89
v_cvt_pk_bf16_f32 v89,v90,v91
v_cvt_pk_bf16_f32 v90,v92,v93
v_cvt_pk_bf16_f32 v91,v94,v95
s_mov_b32 s70,7
L_a1_%=:
s_waitcnt vmcnt(16)
s_barrier
v_mfma_f32_16x16x32_bf16 v[112:115],a[128:131],v[224:227],v[44:47]
s_add_u32 s10,s20,s74
s_addc_u32 s11,s21,0
s_add_u32 m0,s76,0
v_mfma_f32_16x16x32_bf16 v[112:115],a[132:135],v[232:235],v[112:115]
s_add_u32 s75,s75,16384
s_and_b32 s69,s75,0xfffc0000
ds_read_b128 a[128:131],v1
v_mfma_f32_16x16x32_bf16 v[112:115],a[136:139],v[240:243],v[112:115]
global_load_lds_dwordx4 v4,s[10:11] nt
v_mfma_f32_16x16x32_bf16 v[112:115],a[140:143],v[248:251],v[112:115]
ds_read_b128 a[132:135],v1 offset:256
v_mfma_f32_16x16x32_bf16 v[116:119],a[156:159],v[248:251],v[44:47]
v_mfma_f32_16x16x32_bf16 v[116:119],a[152:155],v[240:243],v[116:119]
ds_read_b128 a[136:139],v1 offset:512
v_mfma_f32_16x16x32_bf16 v[116:119],a[148:151],v[232:235],v[116:119]
v_mfma_f32_16x16x32_bf16 v[116:119],a[144:147],v[224:227],v[116:119]
ds_read_b128 a[140:143],v1 offset:768
v_mfma_f32_16x16x32_bf16 v[120:123],a[160:163],v[224:227],v[44:47]
v_mfma_f32_16x16x32_bf16 v[120:123],a[164:167],v[232:235],v[120:123]
ds_read_b128 a[156:159],v1 offset:4864
v_mfma_f32_16x16x32_bf16 v[120:123],a[168:171],v[240:243],v[120:123]
v_mfma_f32_16x16x32_bf16 v[120:123],a[172:175],v[248:251],v[120:123]
ds_read_b128 a[152:155],v1 offset:4608
v_mfma_f32_16x16x32_bf16 v[124:127],a[188:191],v[248:251],v[44:47]
v_exp_f32 v96,v96
v_mfma_f32_16x16x32_bf16 v[124:127],a[184:187],v[240:243],v[124:127]
ds_read_b128 a[148:151],v1 offset:4352
v_add_u32 v2,s78,v2
v_mfma_f32_16x16x32_bf16 v[124:127],a[180:183],v[232:235],v[124:127]
v_exp_f32 v97,v97
v_mfma_f32_16x16x32_bf16 v[124:127],a[176:179],v[224:227],v[124:127]
ds_read_b128 a[144:147],v1 offset:4096
v_cvt_pk_bf16_f32 v96,v96,v97
v_mfma_f32_16x16x32_bf16 v[132:135],a[192:195],v[80:83],v[132:135]
v_exp_f32 v98,v98
v_mfma_f32_16x16x32_bf16 v[132:135],a[196:199],v[88:91],v[132:135]
ds_read_b128 a[160:163],v1 offset:8192
v_add_u32 v3,s78,v3
v_mfma_f32_16x16x32_bf16 v[140:143],a[200:203],v[80:83],v[140:143]
s_sub_u32 s78,s77,32768
global_load_lds_dwordx4 v4,s[10:11] offset:1024 nt
v_mfma_f32_16x16x32_bf16 v[140:143],a[204:207],v[88:91],v[140:143]
s_and_b32 s78,s78,0x1ffff
v_exp_f32 v99,v99
v_mfma_f32_16x16x32_bf16 v[148:151],a[208:211],v[80:83],v[148:151]
s_sub_u32 s78,s78,s77
ds_read_b128 a[164:167],v1 offset:8448
v_cvt_pk_bf16_f32 v97,v98,v99
v_mfma_f32_16x16x32_bf16 v[148:151],a[212:215],v[88:91],v[148:151]
s_add_u32 s77,s77,s78
v_exp_f32 v100,v100
v_mfma_f32_16x16x32_bf16 v[156:159],a[216:219],v[80:83],v[156:159]
v_exp_f32 v101,v101
v_mfma_f32_16x16x32_bf16 v[156:159],a[220:223],v[88:91],v[156:159]
ds_read_b128 a[168:171],v1 offset:8704
v_cvt_pk_bf16_f32 v98,v100,v101
v_mfma_f32_16x16x32_bf16 v[164:167],a[224:227],v[80:83],v[164:167]
v_exp_f32 v102,v102
v_mfma_f32_16x16x32_bf16 v[164:167],a[228:231],v[88:91],v[164:167]
v_exp_f32 v103,v103
v_mfma_f32_16x16x32_bf16 v[172:175],a[232:235],v[80:83],v[172:175]
ds_read_b128 a[172:175],v1 offset:8960
v_cvt_pk_bf16_f32 v99,v102,v103
v_mfma_f32_16x16x32_bf16 v[172:175],a[236:239],v[88:91],v[172:175]
v_exp_f32 v104,v104
v_mfma_f32_16x16x32_bf16 v[180:183],a[240:243],v[80:83],v[180:183]
v_exp_f32 v105,v105
v_mfma_f32_16x16x32_bf16 v[180:183],a[244:247],v[88:91],v[180:183]
ds_read_b128 a[188:191],v1 offset:13056
v_cvt_pk_bf16_f32 v104,v104,v105
v_mfma_f32_16x16x32_bf16 v[188:191],a[248:251],v[80:83],v[188:191]
v_exp_f32 v106,v106
v_mfma_f32_16x16x32_bf16 v[188:191],a[252:255],v[88:91],v[188:191]
v_exp_f32 v107,v107
v_mfma_f32_16x16x32_bf16 v[52:55],v[28:31],v[80:83],v[52:55]
ds_read_b128 a[184:187],v1 offset:12800
v_cvt_pk_bf16_f32 v105,v106,v107
v_mfma_f32_16x16x32_bf16 v[52:55],v[28:31],v[88:91],v[52:55]
s_waitcnt lgkmcnt(3)
v_exp_f32 v108,v108
v_mfma_f32_16x16x32_bf16 v[64:67],a[128:131],v[128:131],v[32:35]
global_load_lds_dwordx4 v5,s[10:11] offset:2048 nt
ds_read_b128 a[180:183],v1 offset:12544
v_mfma_f32_16x16x32_bf16 v[64:67],a[132:135],v[136:139],v[64:67]
v_exp_f32 v109,v109
v_mfma_f32_16x16x32_bf16 v[64:67],a[136:139],v[144:147],v[64:67]
ds_read_b128 a[176:179],v1 offset:12288
v_cvt_pk_bf16_f32 v106,v108,v109
v_mfma_f32_16x16x32_bf16 v[64:67],a[140:143],v[152:155],v[64:67]
v_exp_f32 v110,v110
v_mfma_f32_16x16x32_bf16 v[68:71],a[156:159],v[152:155],v[32:35]
v_exp_f32 v111,v111
v_mfma_f32_16x16x32_bf16 v[68:71],a[152:155],v[144:147],v[68:71]
v_cvt_pk_bf16_f32 v107,v110,v111
v_add_u32 v1,s78,v1
v_mfma_f32_16x16x32_bf16 v[68:71],a[148:151],v[136:139],v[68:71]
v_exp_f32 v112,v112
v_mfma_f32_16x16x32_bf16 v[68:71],a[144:147],v[128:131],v[68:71]
v_exp_f32 v113,v113
v_mfma_f32_16x16x32_bf16 v[72:75],a[160:163],v[128:131],v[32:35]
v_exp_f32 v114,v114
v_mfma_f32_16x16x32_bf16 v[72:75],a[164:167],v[136:139],v[72:75]
v_exp_f32 v115,v115
v_mfma_f32_16x16x32_bf16 v[72:75],a[168:171],v[144:147],v[72:75]
s_waitcnt lgkmcnt(0)
v_exp_f32 v116,v116
v_mfma_f32_16x16x32_bf16 v[72:75],a[172:175],v[152:155],v[72:75]
v_exp_f32 v117,v117
v_mfma_f32_16x16x32_bf16 v[76:79],a[188:191],v[152:155],v[32:35]
v_exp_f32 v118,v118
v_mfma_f32_16x16x32_bf16 v[76:79],a[184:187],v[144:147],v[76:79]
v_exp_f32 v119,v119
v_mfma_f32_16x16x32_bf16 v[76:79],a[180:183],v[136:139],v[76:79]
v_cvt_pk_bf16_f32 v112,v112,v113
v_cvt_pk_bf16_f32 v113,v114,v115
v_mfma_f32_16x16x32_bf16 v[76:79],a[176:179],v[128:131],v[76:79]
v_cvt_pk_bf16_f32 v114,v116,v117
v_cvt_pk_bf16_f32 v115,v118,v119
v_mfma_f32_16x16x32_bf16 v[196:199],a[192:195],v[96:99],v[196:199]
global_load_lds_dwordx4 v5,s[10:11] offset:3072
v_mfma_f32_16x16x32_bf16 v[196:199],a[196:199],v[104:107],v[196:199]
v_exp_f32 v120,v120
v_mfma_f32_16x16x32_bf16 v[204:207],a[200:203],v[96:99],v[204:207]
v_exp_f32 v121,v121
v_mfma_f32_16x16x32_bf16 v[204:207],a[204:207],v[104:107],v[204:207]
v_exp_f32 v122,v122
v_mfma_f32_16x16x32_bf16 v[212:215],a[208:211],v[96:99],v[212:215]
v_exp_f32 v123,v123
v_mfma_f32_16x16x32_bf16 v[212:215],a[212:215],v[104:107],v[212:215]
v_exp_f32 v124,v124
v_mfma_f32_16x16x32_bf16 v[220:223],a[216:219],v[96:99],v[220:223]
v_exp_f32 v125,v125
v_mfma_f32_16x16x32_bf16 v[220:223],a[220:223],v[104:107],v[220:223]
v_exp_f32 v126,v126
v_mfma_f32_16x16x32_bf16 v[228:231],a[224:227],v[96:99],v[228:231]
v_exp_f32 v127,v127
v_mfma_f32_16x16x32_bf16 v[228:231],a[228:231],v[104:107],v[228:231]
v_cvt_pk_bf16_f32 v120,v120,v121
v_cvt_pk_bf16_f32 v121,v122,v123
v_mfma_f32_16x16x32_bf16 v[236:239],a[232:235],v[96:99],v[236:239]
v_cvt_pk_bf16_f32 v122,v124,v125
v_cvt_pk_bf16_f32 v123,v126,v127
v_mfma_f32_16x16x32_bf16 v[236:239],a[236:239],v[104:107],v[236:239]
v_exp_f32 v64,v64
v_mfma_f32_16x16x32_bf16 v[244:247],a[240:243],v[96:99],v[244:247]
s_add_u32 s10,s24,s74
s_addc_u32 s11,s25,0
s_add_u32 s74,s74,16384
v_mfma_f32_16x16x32_bf16 v[244:247],a[244:247],v[104:107],v[244:247]
s_and_b32 s74,s74,0x3ffff
v_exp_f32 v65,v65
v_mfma_f32_16x16x32_bf16 v[252:255],a[248:251],v[96:99],v[252:255]
s_add_u32 m0,s76,16384
v_exp_f32 v66,v66
v_mfma_f32_16x16x32_bf16 v[252:255],a[252:255],v[104:107],v[252:255]
s_or_b32 s74,s74,s69
v_exp_f32 v67,v67
v_mfma_f32_16x16x32_bf16 v[56:59],v[28:31],v[96:99],v[56:59]
global_load_lds_dwordx4 v4,s[10:11]
v_cvt_pk_bf16_f32 v64,v64,v65
v_mfma_f32_16x16x32_bf16 v[56:59],v[28:31],v[104:107],v[56:59]
s_sub_u32 s76,s76,32768
v_exp_f32 v68,v68
v_mfma_f32_16x16x32_bf16 v[80:83],a[128:131],v[160:163],v[36:39]
s_and_b32 s76,s76,0x1ffff
v_exp_f32 v69,v69
v_mfma_f32_16x16x32_bf16 v[80:83],a[132:135],v[168:171],v[80:83]
v_exp_f32 v70,v70
v_mfma_f32_16x16x32_bf16 v[80:83],a[136:139],v[176:179],v[80:83]
v_exp_f32 v71,v71
v_mfma_f32_16x16x32_bf16 v[80:83],a[140:143],v[184:187],v[80:83]
v_cvt_pk_bf16_f32 v65,v66,v67
v_cvt_pk_bf16_f32 v66,v68,v69
v_mfma_f32_16x16x32_bf16 v[84:87],a[156:159],v[184:187],v[36:39]
v_exp_f32 v72,v72
v_mfma_f32_16x16x32_bf16 v[84:87],a[152:155],v[176:179],v[84:87]
v_exp_f32 v73,v73
v_mfma_f32_16x16x32_bf16 v[84:87],a[148:151],v[168:171],v[84:87]
v_cvt_pk_bf16_f32 v67,v70,v71
v_cvt_pk_bf16_f32 v72,v72,v73
v_mfma_f32_16x16x32_bf16 v[84:87],a[144:147],v[160:163],v[84:87]
v_exp_f32 v74,v74
v_mfma_f32_16x16x32_bf16 v[88:91],a[160:163],v[160:163],v[36:39]
v_exp_f32 v75,v75
v_mfma_f32_16x16x32_bf16 v[88:91],a[164:167],v[168:171],v[88:91]
v_exp_f32 v76,v76
v_mfma_f32_16x16x32_bf16 v[88:91],a[168:171],v[176:179],v[88:91]
v_exp_f32 v77,v77
v_mfma_f32_16x16x32_bf16 v[88:91],a[172:175],v[184:187],v[88:91]
v_exp_f32 v78,v78
v_mfma_f32_16x16x32_bf16 v[92:95],a[188:191],v[184:187],v[36:39]
v_exp_f32 v79,v79
v_mfma_f32_16x16x32_bf16 v[92:95],a[184:187],v[176:179],v[92:95]
v_cvt_pk_bf16_f32 v73,v74,v75
v_cvt_pk_bf16_f32 v74,v76,v77
v_mfma_f32_16x16x32_bf16 v[92:95],a[180:183],v[168:171],v[92:95]
global_load_lds_dwordx4 v5,s[10:11] offset:1024
v_cvt_pk_bf16_f32 v75,v78,v79
v_mfma_f32_16x16x32_bf16 v[92:95],a[176:179],v[160:163],v[92:95]
v_exp_f32 v80,v80
v_mfma_f32_16x16x32_bf16 a[32:35],a[192:195],v[112:115],a[32:35]
ds_read_b64_tr_b16 a[192:193],v2
ds_read_b64_tr_b16 a[194:195],v2 offset:4096
v_mfma_f32_16x16x32_bf16 a[32:35],a[196:199],v[120:123],a[32:35]
v_exp_f32 v81,v81
v_mfma_f32_16x16x32_bf16 a[36:39],a[200:203],v[112:115],a[36:39]
ds_read_b64_tr_b16 a[196:197],v2 offset:8192
ds_read_b64_tr_b16 a[198:199],v2 offset:12288
v_mfma_f32_16x16x32_bf16 a[36:39],a[204:207],v[120:123],a[36:39]
ds_read_b64_tr_b16 a[200:201],v3
v_cvt_pk_bf16_f32 v80,v80,v81
v_mfma_f32_16x16x32_bf16 a[40:43],a[208:211],v[112:115],a[40:43]
v_exp_f32 v82,v82
v_mfma_f32_16x16x32_bf16 a[40:43],a[212:215],v[120:123],a[40:43]
ds_read_b64_tr_b16 a[202:203],v3 offset:4096
ds_read_b64_tr_b16 a[204:205],v3 offset:8192
v_mfma_f32_16x16x32_bf16 a[44:47],a[216:219],v[112:115],a[44:47]
v_exp_f32 v83,v83
v_mfma_f32_16x16x32_bf16 a[44:47],a[220:223],v[120:123],a[44:47]
ds_read_b64_tr_b16 a[206:207],v3 offset:12288
ds_read_b64_tr_b16 a[208:209],v2 offset:256
v_mfma_f32_16x16x32_bf16 a[48:51],a[224:227],v[112:115],a[48:51]
ds_read_b64_tr_b16 a[210:211],v2 offset:4352
v_cvt_pk_bf16_f32 v81,v82,v83
v_mfma_f32_16x16x32_bf16 a[48:51],a[228:231],v[120:123],a[48:51]
v_exp_f32 v84,v84
v_mfma_f32_16x16x32_bf16 a[52:55],a[232:235],v[112:115],a[52:55]
ds_read_b64_tr_b16 a[212:213],v2 offset:8448
ds_read_b64_tr_b16 a[214:215],v2 offset:12544
v_mfma_f32_16x16x32_bf16 a[52:55],a[236:239],v[120:123],a[52:55]
v_exp_f32 v85,v85
v_mfma_f32_16x16x32_bf16 a[56:59],a[240:243],v[112:115],a[56:59]
ds_read_b64_tr_b16 a[216:217],v3 offset:256
ds_read_b64_tr_b16 a[218:219],v3 offset:4352
)A"
R"A(v_mfma_f32_16x16x32_bf16 a[56:59],a[244:247],v[120:123],a[56:59]
ds_read_b64_tr_b16 a[220:221],v3 offset:8448
v_cvt_pk_bf16_f32 v82,v84,v85
v_mfma_f32_16x16x32_bf16 a[60:63],a[248:251],v[112:115],a[60:63]
global_load_lds_dwordx4 v4,s[10:11] offset:2048
ds_read_b64_tr_b16 a[222:223],v3 offset:12544
v_mfma_f32_16x16x32_bf16 a[60:63],a[252:255],v[120:123],a[60:63]
v_exp_f32 v86,v86
v_mfma_f32_16x16x32_bf16 v[60:63],v[28:31],v[112:115],v[60:63]
ds_read_b64_tr_b16 a[224:225],v2 offset:512
ds_read_b64_tr_b16 a[226:227],v2 offset:4608
v_mfma_f32_16x16x32_bf16 v[60:63],v[28:31],v[120:123],v[60:63]
v_exp_f32 v87,v87
v_mfma_f32_16x16x32_bf16 v[96:99],a[128:131],v[192:195],v[40:43]
ds_read_b64_tr_b16 a[228:229],v2 offset:8704
ds_read_b64_tr_b16 a[230:231],v2 offset:12800
v_mfma_f32_16x16x32_bf16 v[96:99],a[132:135],v[200:203],v[96:99]
ds_read_b64_tr_b16 a[232:233],v3 offset:512
v_cvt_pk_bf16_f32 v83,v86,v87
v_mfma_f32_16x16x32_bf16 v[96:99],a[136:139],v[208:211],v[96:99]
v_exp_f32 v88,v88
v_mfma_f32_16x16x32_bf16 v[96:99],a[140:143],v[216:219],v[96:99]
ds_read_b64_tr_b16 a[234:235],v3 offset:4608
ds_read_b64_tr_b16 a[236:237],v3 offset:8704
v_mfma_f32_16x16x32_bf16 v[100:103],a[156:159],v[216:219],v[40:43]
v_exp_f32 v89,v89
v_mfma_f32_16x16x32_bf16 v[100:103],a[152:155],v[208:211],v[100:103]
ds_read_b64_tr_b16 a[238:239],v3 offset:12800
ds_read_b64_tr_b16 a[240:241],v2 offset:768
v_mfma_f32_16x16x32_bf16 v[100:103],a[148:151],v[200:203],v[100:103]
ds_read_b64_tr_b16 a[242:243],v2 offset:4864
v_cvt_pk_bf16_f32 v88,v88,v89
v_mfma_f32_16x16x32_bf16 v[100:103],a[144:147],v[192:195],v[100:103]
v_exp_f32 v90,v90
v_mfma_f32_16x16x32_bf16 v[104:107],a[160:163],v[192:195],v[40:43]
ds_read_b64_tr_b16 a[244:245],v2 offset:8960
ds_read_b64_tr_b16 a[246:247],v2 offset:13056
v_mfma_f32_16x16x32_bf16 v[104:107],a[164:167],v[200:203],v[104:107]
v_exp_f32 v91,v91
v_mfma_f32_16x16x32_bf16 v[104:107],a[168:171],v[208:211],v[104:107]
ds_read_b64_tr_b16 a[248:249],v3 offset:768
ds_read_b64_tr_b16 a[250:251],v3 offset:4864
v_mfma_f32_16x16x32_bf16 v[104:107],a[172:175],v[216:219],v[104:107]
ds_read_b64_tr_b16 a[252:253],v3 offset:8960
v_cvt_pk_bf16_f32 v89,v90,v91
v_mfma_f32_16x16x32_bf16 v[108:111],a[188:191],v[216:219],v[40:43]
global_load_lds_dwordx4 v5,s[10:11] offset:3072
ds_read_b64_tr_b16 a[254:255],v3 offset:13056
v_mfma_f32_16x16x32_bf16 v[108:111],a[184:187],v[208:211],v[108:111]
v_exp_f32 v92,v92
v_mfma_f32_16x16x32_bf16 v[108:111],a[180:183],v[200:203],v[108:111]
v_exp_f32 v93,v93
v_mfma_f32_16x16x32_bf16 v[108:111],a[176:179],v[192:195],v[108:111]
s_waitcnt lgkmcnt(6)
v_exp_f32 v94,v94
v_mfma_f32_16x16x32_bf16 a[0:3],a[192:195],v[64:67],a[0:3]
v_exp_f32 v95,v95
v_mfma_f32_16x16x32_bf16 a[0:3],a[196:199],v[72:75],a[0:3]
v_cvt_pk_bf16_f32 v90,v92,v93
v_cvt_pk_bf16_f32 v91,v94,v95
v_mfma_f32_16x16x32_bf16 a[4:7],a[200:203],v[64:67],a[4:7]
v_mfma_f32_16x16x32_bf16 a[4:7],a[204:207],v[72:75],a[4:7]
v_mfma_f32_16x16x32_bf16 a[8:11],a[208:211],v[64:67],a[8:11]
v_mfma_f32_16x16x32_bf16 a[8:11],a[212:215],v[72:75],a[8:11]
v_mfma_f32_16x16x32_bf16 a[12:15],a[216:219],v[64:67],a[12:15]
v_mfma_f32_16x16x32_bf16 a[12:15],a[220:223],v[72:75],a[12:15]
v_mfma_f32_16x16x32_bf16 a[16:19],a[224:227],v[64:67],a[16:19]
v_mfma_f32_16x16x32_bf16 a[16:19],a[228:231],v[72:75],a[16:19]
v_mfma_f32_16x16x32_bf16 a[20:23],a[232:235],v[64:67],a[20:23]
s_getreg_b32 s92,hwreg(HW_REG_IB_STS,0,4)
s_min_u32 s4,s92,s6
s_mov_b32 s6,s92
v_mfma_f32_16x16x32_bf16 a[20:23],a[236:239],v[72:75],a[20:23]
s_cmp_ge_u32 s4,s9
s_cselect_b32 s4,15,s5
v_mfma_f32_16x16x32_bf16 a[24:27],a[240:243],v[64:67],a[24:27]
s_waitcnt lgkmcnt(0)
v_mfma_f32_16x16x32_bf16 a[24:27],a[244:247],v[72:75],a[24:27]
v_mfma_f32_16x16x32_bf16 a[28:31],a[248:251],v[64:67],a[28:31]
v_mfma_f32_16x16x32_bf16 a[28:31],a[252:255],v[72:75],a[28:31]
s_getpc_b64 s[92:93]
s_cmp_lg_u32 s4,0
s_cbranch_scc1 L_pace_%=
v_mfma_f32_16x16x32_bf16 v[48:51],v[28:31],v[64:67],v[48:51]
v_mfma_f32_16x16x32_bf16 v[48:51],v[28:31],v[72:75],v[48:51]
s_sub_u32 s70,s70,1
s_cbranch_scc0 L_a1_%=
L_nx1_%=:
s_waitcnt vmcnt(16)
s_barrier
v_mfma_f32_16x16x32_bf16 v[112:115],a[128:131],v[224:227],v[44:47]
s_mul_i32 s86,s60,10240
s_mul_i32 s85,s63,1310720
s_mul_i32 s66,s64,320
v_mfma_f32_16x16x32_bf16 v[112:115],a[132:135],v[232:235],v[112:115]
s_add_u32 s85,s85,s66
s_add_u32 s83,s64,0
s_and_b32 s83,s83,3
v_mfma_f32_16x16x32_bf16 v[112:115],a[136:139],v[240:243],v[112:115]
s_lshl_b32 s83,s83,1
s_mul_i32 s83,s83,163840
s_add_u32 s83,s83,s85
v_mfma_f32_16x16x32_bf16 v[112:115],a[140:143],v[248:251],v[112:115]
s_cmp_eq_u32 s60,3
s_cselect_b32 s69,0x40000000,0
v_add_u32 v14,s69,v6
v_mfma_f32_16x16x32_bf16 v[116:119],a[156:159],v[248:251],v[44:47]
s_add_u32 s84,s83,20480
ds_read_b128 a[128:131],v1
v_add_u32 v2,s78,v2
v_mfma_f32_16x16x32_bf16 v[116:119],a[152:155],v[240:243],v[116:119]
s_add_u32 m0,s86,122880
v_exp_f32 v96,v96
v_mfma_f32_16x16x32_bf16 v[116:119],a[148:151],v[232:235],v[116:119]
s_getpc_b64 s[8:9]
L_pcg_%=:
ds_read_b128 a[132:135],v1 offset:256
v_add_u32 v3,s78,v3
v_mfma_f32_16x16x32_bf16 v[116:119],a[144:147],v[224:227],v[116:119]
buffer_load_dwordx4 v24,s[48:51],s83 offen lds
v_mfma_f32_16x16x32_bf16 v[120:123],a[160:163],v[224:227],v[44:47]
s_sub_u32 s78,s77,32768
s_and_b32 s78,s78,0x1ffff
ds_read_b128 a[136:139],v1 offset:512
v_mfma_f32_16x16x32_bf16 v[120:123],a[164:167],v[232:235],v[120:123]
buffer_store_dwordx4 a[112:115],v14,s[40:43],s81 offen offset:3072 sc1
v_mfma_f32_16x16x32_bf16 v[120:123],a[168:171],v[240:243],v[120:123]
s_sub_u32 s78,s78,s77
ds_read_b128 a[140:143],v1 offset:768
v_lshlrev_b32 v4,10,v6
v_mfma_f32_16x16x32_bf16 v[120:123],a[172:175],v[248:251],v[120:123]
s_add_u32 s77,s77,s78
v_exp_f32 v97,v97
v_mfma_f32_16x16x32_bf16 v[124:127],a[188:191],v[248:251],v[44:47]
s_mov_b32 s6,L_gbF_%=-L_pcg_%=
ds_read_b128 a[156:159],v1 offset:4864
v_cvt_pk_bf16_f32 v96,v96,v97
v_mfma_f32_16x16x32_bf16 v[124:127],a[184:187],v[240:243],v[124:127]
s_mov_b32 s10,L_geF_%=-L_gbF_%=
v_exp_f32 v98,v98
v_mfma_f32_16x16x32_bf16 v[124:127],a[180:183],v[232:235],v[124:127]
s_mov_b32 s11,0x20000
s_lshr_b32 s7,%1,3
ds_read_b128 a[152:155],v1 offset:4608
v_mfma_f32_16x16x32_bf16 v[124:127],a[176:179],v[224:227],v[124:127]
s_lshl_b32 s7,s7,2
v_exp_f32 v99,v99
v_mfma_f32_16x16x32_bf16 v[132:135],a[192:195],v[80:83],v[132:135]
s_add_u32 s7,s7,s60
ds_read_b128 a[148:151],v1 offset:4352
v_cvt_pk_bf16_f32 v97,v98,v99
v_mfma_f32_16x16x32_bf16 v[132:135],a[196:199],v[88:91],v[132:135]
buffer_load_dwordx4 v25,s[48:51],s83 offen offset:1024 lds
v_mfma_f32_16x16x32_bf16 v[140:143],a[200:203],v[80:83],v[140:143]
s_lshl_b32 s7,s7,7
ds_read_b128 a[144:147],v1 offset:4096
v_mov_b32 v8,s52
v_mfma_f32_16x16x32_bf16 v[140:143],a[204:207],v[88:91],v[140:143]
s_lshl_b32 s66,s61,5
v_exp_f32 v100,v100
v_mfma_f32_16x16x32_bf16 v[148:151],a[208:211],v[80:83],v[148:151]
s_lshl_b32 s67,s63,3
ds_read_b128 a[160:163],v1 offset:8192
v_mov_b32 v9,s53
v_mfma_f32_16x16x32_bf16 v[148:151],a[212:215],v[88:91],v[148:151]
s_add_u32 s66,s66,s67
v_exp_f32 v101,v101
v_mfma_f32_16x16x32_bf16 v[156:159],a[216:219],v[80:83],v[156:159]
s_lshl_b32 s66,s66,16
ds_read_b128 a[164:167],v1 offset:8448
v_cvt_pk_bf16_f32 v98,v100,v101
v_mfma_f32_16x16x32_bf16 v[156:159],a[220:223],v[88:91],v[156:159]
s_lshl_b32 s67,s60,14
v_exp_f32 v102,v102
v_mfma_f32_16x16x32_bf16 v[164:167],a[224:227],v[80:83],v[164:167]
s_add_u32 s66,s66,s67
ds_read_b128 a[168:171],v1 offset:8704
v_lshlrev_b32 v10,20,v6
v_mfma_f32_16x16x32_bf16 v[164:167],a[228:231],v[88:91],v[164:167]
s_mov_b32 s30,1523712
v_exp_f32 v103,v103
v_mfma_f32_16x16x32_bf16 v[172:175],a[232:235],v[80:83],v[172:175]
buffer_store_dwordx4 a[116:119],v6,s[40:43],s81 offen offset:1024 sc1
v_mfma_f32_16x16x32_bf16 v[172:175],a[236:239],v[88:91],v[172:175]
s_mov_b32 s31,0x20000
ds_read_b128 a[172:175],v1 offset:8960
v_cvt_pk_bf16_f32 v99,v102,v103
v_mfma_f32_16x16x32_bf16 v[180:183],a[240:243],v[80:83],v[180:183]
buffer_load_dwordx4 v26,s[48:51],s83 offen offset:2048 lds
v_mfma_f32_16x16x32_bf16 v[180:183],a[244:247],v[88:91],v[180:183]
v_exp_f32 v104,v104
v_mfma_f32_16x16x32_bf16 v[188:191],a[248:251],v[80:83],v[188:191]
v_exp_f32 v105,v105
v_mfma_f32_16x16x32_bf16 v[188:191],a[252:255],v[88:91],v[188:191]
ds_read_b128 a[188:191],v1 offset:13056
v_cvt_pk_bf16_f32 v104,v104,v105
v_mfma_f32_16x16x32_bf16 v[52:55],v[28:31],v[80:83],v[52:55]
v_exp_f32 v106,v106
v_mfma_f32_16x16x32_bf16 v[52:55],v[28:31],v[88:91],v[52:55]
s_waitcnt lgkmcnt(2)
v_exp_f32 v107,v107
v_mfma_f32_16x16x32_bf16 v[64:67],a[128:131],v[128:131],v[32:35]
ds_read_b128 a[184:187],v1 offset:12800
v_cvt_pk_bf16_f32 v105,v106,v107
v_mfma_f32_16x16x32_bf16 v[64:67],a[132:135],v[136:139],v[64:67]
v_exp_f32 v108,v108
v_mfma_f32_16x16x32_bf16 v[64:67],a[136:139],v[144:147],v[64:67]
s_add_u32 s28,s12,s66
s_addc_u32 s29,s13,0
ds_read_b128 a[180:183],v1 offset:12544
v_mfma_f32_16x16x32_bf16 v[64:67],a[140:143],v[152:155],v[64:67]
buffer_load_dwordx4 v27,s[48:51],s83 offen offset:3072 lds
v_mfma_f32_16x16x32_bf16 v[68:71],a[156:159],v[152:155],v[32:35]
s_and_b32 s29,s29,0xffff
ds_read_b128 a[176:179],v1 offset:12288
v_add_u32 v1,s78,v1
v_mfma_f32_16x16x32_bf16 v[68:71],a[152:155],v[144:147],v[68:71]
v_exp_f32 v109,v109
v_mfma_f32_16x16x32_bf16 v[68:71],a[148:151],v[136:139],v[68:71]
v_exp_f32 v110,v110
v_mfma_f32_16x16x32_bf16 v[68:71],a[144:147],v[128:131],v[68:71]
v_exp_f32 v111,v111
v_mfma_f32_16x16x32_bf16 v[72:75],a[160:163],v[128:131],v[32:35]
v_cvt_pk_bf16_f32 v106,v108,v109
v_cvt_pk_bf16_f32 v107,v110,v111
v_mfma_f32_16x16x32_bf16 v[72:75],a[164:167],v[136:139],v[72:75]
buffer_store_dwordx4 a[120:123],v14,s[40:43],s82 offen offset:3072 sc1
v_mfma_f32_16x16x32_bf16 v[72:75],a[168:171],v[144:147],v[72:75]
s_waitcnt lgkmcnt(1)
v_exp_f32 v112,v112
v_mfma_f32_16x16x32_bf16 v[72:75],a[172:175],v[152:155],v[72:75]
s_add_u32 m0,s86,126976
v_exp_f32 v113,v113
v_mfma_f32_16x16x32_bf16 v[76:79],a[188:191],v[152:155],v[32:35]
s_waitcnt lgkmcnt(0)
v_exp_f32 v114,v114
v_mfma_f32_16x16x32_bf16 v[76:79],a[184:187],v[144:147],v[76:79]
buffer_load_dwordx4 v7,s[48:51],s83 offen lds
v_mfma_f32_16x16x32_bf16 v[76:79],a[180:183],v[136:139],v[76:79]
s_add_u32 s83,s64,0
v_exp_f32 v115,v115
v_mfma_f32_16x16x32_bf16 v[76:79],a[176:179],v[128:131],v[76:79]
s_and_b32 s83,s83,3
v_exp_f32 v116,v116
v_mfma_f32_16x16x32_bf16 v[196:199],a[192:195],v[96:99],v[196:199]
s_lshl_b32 s83,s83,1
v_exp_f32 v117,v117
v_mfma_f32_16x16x32_bf16 v[196:199],a[196:199],v[104:107],v[196:199]
s_or_b32 s83,s83,1
v_exp_f32 v118,v118
v_mfma_f32_16x16x32_bf16 v[204:207],a[200:203],v[96:99],v[204:207]
s_mul_i32 s83,s83,163840
v_exp_f32 v119,v119
v_mfma_f32_16x16x32_bf16 v[204:207],a[204:207],v[104:107],v[204:207]
s_add_u32 s83,s83,s85
v_cvt_pk_bf16_f32 v112,v112,v113
v_cvt_pk_bf16_f32 v113,v114,v115
v_mfma_f32_16x16x32_bf16 v[212:215],a[208:211],v[96:99],v[212:215]
L_pd11_%=:
s_cbranch_execz L_t1c_%=
v_cvt_pk_bf16_f32 v114,v116,v117
v_cvt_pk_bf16_f32 v115,v118,v119
v_mfma_f32_16x16x32_bf16 v[212:215],a[212:215],v[104:107],v[212:215]
s_add_u32 m0,s86,128000
v_exp_f32 v120,v120
v_mfma_f32_16x16x32_bf16 v[220:223],a[216:219],v[96:99],v[220:223]
v_exp_f32 v121,v121
v_mfma_f32_16x16x32_bf16 v[220:223],a[220:223],v[104:107],v[220:223]
buffer_load_dwordx4 v24,s[48:51],s84 offen lds
v_mfma_f32_16x16x32_bf16 v[228:231],a[224:227],v[96:99],v[228:231]
v_exp_f32 v122,v122
v_mfma_f32_16x16x32_bf16 v[228:231],a[228:231],v[104:107],v[228:231]
v_exp_f32 v123,v123
v_mfma_f32_16x16x32_bf16 v[236:239],a[232:235],v[96:99],v[236:239]
buffer_store_dwordx4 a[124:127],v6,s[40:43],s82 offen offset:1024 sc1
v_mfma_f32_16x16x32_bf16 v[236:239],a[236:239],v[104:107],v[236:239]
v_exp_f32 v124,v124
v_mfma_f32_16x16x32_bf16 v[244:247],a[240:243],v[96:99],v[244:247]
v_exp_f32 v125,v125
v_mfma_f32_16x16x32_bf16 v[244:247],a[244:247],v[104:107],v[244:247]
v_exp_f32 v126,v126
v_mfma_f32_16x16x32_bf16 v[252:255],a[248:251],v[96:99],v[252:255]
v_exp_f32 v127,v127
v_mfma_f32_16x16x32_bf16 v[252:255],a[252:255],v[104:107],v[252:255]
)A"
R"A(v_cvt_pk_bf16_f32 v120,v120,v121
v_cvt_pk_bf16_f32 v121,v122,v123
v_mfma_f32_16x16x32_bf16 v[56:59],v[28:31],v[96:99],v[56:59]
v_cvt_pk_bf16_f32 v122,v124,v125
v_cvt_pk_bf16_f32 v123,v126,v127
v_mfma_f32_16x16x32_bf16 v[56:59],v[28:31],v[104:107],v[56:59]
buffer_load_dwordx4 v25,s[48:51],s84 offen offset:1024 lds
v_mfma_f32_16x16x32_bf16 v[80:83],a[128:131],v[160:163],v[36:39]
v_exp_f32 v64,v64
v_mfma_f32_16x16x32_bf16 v[80:83],a[132:135],v[168:171],v[80:83]
v_exp_f32 v65,v65
v_mfma_f32_16x16x32_bf16 v[80:83],a[136:139],v[176:179],v[80:83]
v_exp_f32 v66,v66
v_mfma_f32_16x16x32_bf16 v[80:83],a[140:143],v[184:187],v[80:83]
v_exp_f32 v67,v67
v_mfma_f32_16x16x32_bf16 v[84:87],a[156:159],v[184:187],v[36:39]
v_exp_f32 v68,v68
v_mfma_f32_16x16x32_bf16 v[84:87],a[152:155],v[176:179],v[84:87]
v_exp_f32 v69,v69
v_mfma_f32_16x16x32_bf16 v[84:87],a[148:151],v[168:171],v[84:87]
v_exp_f32 v70,v70
v_mfma_f32_16x16x32_bf16 v[84:87],a[144:147],v[160:163],v[84:87]
v_exp_f32 v71,v71
v_mfma_f32_16x16x32_bf16 v[88:91],a[160:163],v[160:163],v[36:39]
v_cvt_pk_bf16_f32 v64,v64,v65
v_cvt_pk_bf16_f32 v65,v66,v67
v_mfma_f32_16x16x32_bf16 v[88:91],a[164:167],v[168:171],v[88:91]
buffer_store_dwordx4 a[96:99],v14,s[40:43],s79 offen offset:3072 sc1
v_mfma_f32_16x16x32_bf16 v[88:91],a[168:171],v[176:179],v[88:91]
v_cvt_pk_bf16_f32 v66,v68,v69
v_cvt_pk_bf16_f32 v67,v70,v71
v_mfma_f32_16x16x32_bf16 v[88:91],a[172:175],v[184:187],v[88:91]
buffer_load_dwordx4 v26,s[48:51],s84 offen offset:2048 lds
v_mfma_f32_16x16x32_bf16 v[92:95],a[188:191],v[184:187],v[36:39]
v_exp_f32 v72,v72
v_mfma_f32_16x16x32_bf16 v[92:95],a[184:187],v[176:179],v[92:95]
v_exp_f32 v73,v73
v_mfma_f32_16x16x32_bf16 v[92:95],a[180:183],v[168:171],v[92:95]
v_exp_f32 v74,v74
v_mfma_f32_16x16x32_bf16 v[92:95],a[176:179],v[160:163],v[92:95]
v_exp_f32 v75,v75
v_mfma_f32_16x16x32_bf16 a[32:35],a[192:195],v[112:115],a[32:35]
ds_read_b64_tr_b16 a[192:193],v2
ds_read_b64_tr_b16 a[194:195],v2 offset:4096
v_mfma_f32_16x16x32_bf16 a[32:35],a[196:199],v[120:123],a[32:35]
ds_read_b64_tr_b16 a[196:197],v2 offset:8192
v_cvt_pk_bf16_f32 v72,v72,v73
v_mfma_f32_16x16x32_bf16 a[36:39],a[200:203],v[112:115],a[36:39]
ds_read_b64_tr_b16 a[198:199],v2 offset:12288
v_cvt_pk_bf16_f32 v73,v74,v75
v_mfma_f32_16x16x32_bf16 a[36:39],a[204:207],v[120:123],a[36:39]
buffer_load_dwordx4 v27,s[48:51],s84 offen offset:3072 lds
v_mfma_f32_16x16x32_bf16 a[40:43],a[208:211],v[112:115],a[40:43]
ds_read_b64_tr_b16 a[200:201],v3
ds_read_b64_tr_b16 a[202:203],v3 offset:4096
v_mfma_f32_16x16x32_bf16 a[40:43],a[212:215],v[120:123],a[40:43]
v_exp_f32 v76,v76
v_mfma_f32_16x16x32_bf16 a[44:47],a[216:219],v[112:115],a[44:47]
ds_read_b64_tr_b16 a[204:205],v3 offset:8192
ds_read_b64_tr_b16 a[206:207],v3 offset:12288
v_mfma_f32_16x16x32_bf16 a[44:47],a[220:223],v[120:123],a[44:47]
v_exp_f32 v77,v77
v_mfma_f32_16x16x32_bf16 a[48:51],a[224:227],v[112:115],a[48:51]
ds_read_b64_tr_b16 a[208:209],v2 offset:256
ds_read_b64_tr_b16 a[210:211],v2 offset:4352
v_mfma_f32_16x16x32_bf16 a[48:51],a[228:231],v[120:123],a[48:51]
ds_read_b64_tr_b16 a[212:213],v2 offset:8448
v_cvt_pk_bf16_f32 v74,v76,v77
v_mfma_f32_16x16x32_bf16 a[52:55],a[232:235],v[112:115],a[52:55]
buffer_store_dwordx4 a[100:103],v6,s[40:43],s79 offen offset:1024 sc1
v_mfma_f32_16x16x32_bf16 a[52:55],a[236:239],v[120:123],a[52:55]
s_add_u32 m0,s86,132096
ds_read_b64_tr_b16 a[214:215],v2 offset:12544
ds_read_b64_tr_b16 a[216:217],v3 offset:256
v_mfma_f32_16x16x32_bf16 a[56:59],a[240:243],v[112:115],a[56:59]
v_exp_f32 v78,v78
v_mfma_f32_16x16x32_bf16 a[56:59],a[244:247],v[120:123],a[56:59]
buffer_load_dwordx4 v7,s[48:51],s84 offen lds
v_mfma_f32_16x16x32_bf16 a[60:63],a[248:251],v[112:115],a[60:63]
s_add_u32 s84,s83,20480
ds_read_b64_tr_b16 a[218:219],v3 offset:4352
ds_read_b64_tr_b16 a[220:221],v3 offset:8448
v_mfma_f32_16x16x32_bf16 a[60:63],a[252:255],v[120:123],a[60:63]
v_exp_f32 v79,v79
v_mfma_f32_16x16x32_bf16 v[60:63],v[28:31],v[112:115],v[60:63]
ds_read_b64_tr_b16 a[222:223],v3 offset:12544
v_cvt_pk_bf16_f32 v75,v78,v79
v_mfma_f32_16x16x32_bf16 v[60:63],v[28:31],v[120:123],v[60:63]
ds_read_b64_tr_b16 a[224:225],v2 offset:512
ds_read_b64_tr_b16 a[226:227],v2 offset:4608
v_mfma_f32_16x16x32_bf16 v[96:99],a[128:131],v[192:195],v[40:43]
v_exp_f32 v80,v80
v_mfma_f32_16x16x32_bf16 v[96:99],a[132:135],v[200:203],v[96:99]
ds_read_b64_tr_b16 a[228:229],v2 offset:8704
ds_read_b64_tr_b16 a[230:231],v2 offset:12800
v_mfma_f32_16x16x32_bf16 v[96:99],a[136:139],v[208:211],v[96:99]
v_exp_f32 v81,v81
v_mfma_f32_16x16x32_bf16 v[96:99],a[140:143],v[216:219],v[96:99]
ds_read_b64_tr_b16 a[232:233],v3 offset:512
ds_read_b64_tr_b16 a[234:235],v3 offset:4608
v_mfma_f32_16x16x32_bf16 v[100:103],a[156:159],v[216:219],v[40:43]
ds_read_b64_tr_b16 a[236:237],v3 offset:8704
v_cvt_pk_bf16_f32 v80,v80,v81
v_mfma_f32_16x16x32_bf16 v[100:103],a[152:155],v[208:211],v[100:103]
v_exp_f32 v82,v82
v_mfma_f32_16x16x32_bf16 v[100:103],a[148:151],v[200:203],v[100:103]
ds_read_b64_tr_b16 a[238:239],v3 offset:12800
ds_read_b64_tr_b16 a[240:241],v2 offset:768
v_mfma_f32_16x16x32_bf16 v[100:103],a[144:147],v[192:195],v[100:103]
v_exp_f32 v83,v83
v_mfma_f32_16x16x32_bf16 v[104:107],a[160:163],v[192:195],v[40:43]
ds_read_b64_tr_b16 a[242:243],v2 offset:4864
ds_read_b64_tr_b16 a[244:245],v2 offset:8960
v_mfma_f32_16x16x32_bf16 v[104:107],a[164:167],v[200:203],v[104:107]
buffer_store_dwordx4 a[104:107],v14,s[40:43],s80 offen offset:3072 sc1
v_mfma_f32_16x16x32_bf16 v[104:107],a[168:171],v[208:211],v[104:107]
ds_read_b64_tr_b16 a[246:247],v2 offset:13056
ds_read_b64_tr_b16 a[248:249],v3 offset:768
v_mfma_f32_16x16x32_bf16 v[104:107],a[172:175],v[216:219],v[104:107]
ds_read_b64_tr_b16 a[250:251],v3 offset:4864
v_cvt_pk_bf16_f32 v81,v82,v83
v_mfma_f32_16x16x32_bf16 v[108:111],a[188:191],v[216:219],v[40:43]
ds_read_b64_tr_b16 a[252:253],v3 offset:8960
v_add_u32 v2,s78,v2
v_mfma_f32_16x16x32_bf16 v[108:111],a[184:187],v[208:211],v[108:111]
s_waitcnt lgkmcnt(6)
ds_read_b64_tr_b16 a[254:255],v3 offset:13056
v_add_u32 v3,s78,v3
v_mfma_f32_16x16x32_bf16 v[108:111],a[180:183],v[200:203],v[108:111]
s_sub_u32 s78,s77,32768
v_exp_f32 v84,v84
v_mfma_f32_16x16x32_bf16 v[108:111],a[176:179],v[192:195],v[108:111]
s_and_b32 s78,s78,0x1ffff
v_exp_f32 v85,v85
v_mfma_f32_16x16x32_bf16 a[0:3],a[192:195],v[64:67],a[0:3]
s_sub_u32 s78,s78,s77
v_exp_f32 v86,v86
v_mfma_f32_16x16x32_bf16 a[0:3],a[196:199],v[72:75],a[0:3]
s_add_u32 s77,s77,s78
v_exp_f32 v87,v87
v_mfma_f32_16x16x32_bf16 a[4:7],a[200:203],v[64:67],a[4:7]
v_cvt_pk_bf16_f32 v82,v84,v85
v_cvt_pk_bf16_f32 v83,v86,v87
v_mfma_f32_16x16x32_bf16 a[4:7],a[204:207],v[72:75],a[4:7]
v_exp_f32 v88,v88
v_mfma_f32_16x16x32_bf16 a[8:11],a[208:211],v[64:67],a[8:11]
v_exp_f32 v89,v89
v_mfma_f32_16x16x32_bf16 a[8:11],a[212:215],v[72:75],a[8:11]
v_exp_f32 v90,v90
v_mfma_f32_16x16x32_bf16 a[12:15],a[216:219],v[64:67],a[12:15]
v_exp_f32 v91,v91
v_mfma_f32_16x16x32_bf16 a[12:15],a[220:223],v[72:75],a[12:15]
v_exp_f32 v92,v92
v_mfma_f32_16x16x32_bf16 a[16:19],a[224:227],v[64:67],a[16:19]
v_exp_f32 v93,v93
v_mfma_f32_16x16x32_bf16 a[16:19],a[228:231],v[72:75],a[16:19]
v_exp_f32 v94,v94
v_mfma_f32_16x16x32_bf16 a[20:23],a[232:235],v[64:67],a[20:23]
buffer_store_dwordx4 a[108:111],v6,s[40:43],s80 offen offset:1024 sc1
v_mfma_f32_16x16x32_bf16 a[20:23],a[236:239],v[72:75],a[20:23]
s_waitcnt lgkmcnt(0)
v_exp_f32 v95,v95
v_mfma_f32_16x16x32_bf16 a[24:27],a[240:243],v[64:67],a[24:27]
v_cvt_pk_bf16_f32 v88,v88,v89
v_cvt_pk_bf16_f32 v89,v90,v91
v_mfma_f32_16x16x32_bf16 a[24:27],a[244:247],v[72:75],a[24:27]
v_cvt_pk_bf16_f32 v90,v92,v93
v_cvt_pk_bf16_f32 v91,v94,v95
v_mfma_f32_16x16x32_bf16 a[28:31],a[248:251],v[64:67],a[28:31]
v_exp_f32 v96,v96
v_mfma_f32_16x16x32_bf16 a[28:31],a[252:255],v[72:75],a[28:31]
v_exp_f32 v97,v97
v_mfma_f32_16x16x32_bf16 v[48:51],v[28:31],v[64:67],v[48:51]
v_exp_f32 v98,v98
v_mfma_f32_16x16x32_bf16 v[48:51],v[28:31],v[72:75],v[48:51]
v_exp_f32 v99,v99
s_waitcnt vmcnt(26)
s_barrier
v_mfma_f32_16x16x32_bf16 v[112:115],a[128:131],v[224:227],v[44:47]
ds_read_b128 a[128:131],v1
v_cvt_pk_bf16_f32 v96,v96,v97
v_mfma_f32_16x16x32_bf16 v[112:115],a[132:135],v[232:235],v[112:115]
v_exp_f32 v100,v100
v_mfma_f32_16x16x32_bf16 v[112:115],a[136:139],v[240:243],v[112:115]
ds_read_b128 a[132:135],v1 offset:256
v_cvt_pk_bf16_f32 v97,v98,v99
v_mfma_f32_16x16x32_bf16 v[112:115],a[140:143],v[248:251],v[112:115]
v_exp_f32 v101,v101
v_mfma_f32_16x16x32_bf16 v[116:119],a[156:159],v[248:251],v[44:47]
ds_read_b128 a[136:139],v1 offset:512
v_cvt_pk_bf16_f32 v98,v100,v101
v_mfma_f32_16x16x32_bf16 v[116:119],a[152:155],v[240:243],v[116:119]
s_add_u32 m0,s86,81920
v_exp_f32 v102,v102
v_mfma_f32_16x16x32_bf16 v[116:119],a[148:151],v[232:235],v[116:119]
v_exp_f32 v103,v103
v_mfma_f32_16x16x32_bf16 v[116:119],a[144:147],v[224:227],v[116:119]
buffer_load_dwordx4 v24,s[48:51],s83 offen lds
v_mfma_f32_16x16x32_bf16 v[120:123],a[160:163],v[224:227],v[44:47]
ds_read_b128 a[140:143],v1 offset:768
v_cvt_pk_bf16_f32 v99,v102,v103
v_mfma_f32_16x16x32_bf16 v[120:123],a[164:167],v[232:235],v[120:123]
buffer_store_dwordx4 a[64:67],v6,s[40:43],s79 offen offset:2048 sc1
v_mfma_f32_16x16x32_bf16 v[120:123],a[168:171],v[240:243],v[120:123]
v_exp_f32 v104,v104
v_mfma_f32_16x16x32_bf16 v[120:123],a[172:175],v[248:251],v[120:123]
v_exp_f32 v105,v105
v_mfma_f32_16x16x32_bf16 v[124:127],a[188:191],v[248:251],v[44:47]
ds_read_b128 a[156:159],v1 offset:4864
v_cvt_pk_bf16_f32 v104,v104,v105
v_mfma_f32_16x16x32_bf16 v[124:127],a[184:187],v[240:243],v[124:127]
v_exp_f32 v106,v106
v_mfma_f32_16x16x32_bf16 v[124:127],a[180:183],v[232:235],v[124:127]
v_exp_f32 v107,v107
v_mfma_f32_16x16x32_bf16 v[124:127],a[176:179],v[224:227],v[124:127]
ds_read_b128 a[152:155],v1 offset:4608
v_cvt_pk_bf16_f32 v105,v106,v107
v_mfma_f32_16x16x32_bf16 v[132:135],a[192:195],v[80:83],v[132:135]
v_exp_f32 v108,v108
v_mfma_f32_16x16x32_bf16 v[132:135],a[196:199],v[88:91],v[132:135]
buffer_load_dwordx4 v25,s[48:51],s83 offen offset:1024 lds
v_mfma_f32_16x16x32_bf16 v[140:143],a[200:203],v[80:83],v[140:143]
v_exp_f32 v109,v109
v_mfma_f32_16x16x32_bf16 v[140:143],a[204:207],v[88:91],v[140:143]
ds_read_b128 a[148:151],v1 offset:4352
v_cvt_pk_bf16_f32 v106,v108,v109
v_mfma_f32_16x16x32_bf16 v[148:151],a[208:211],v[80:83],v[148:151]
v_exp_f32 v110,v110
v_mfma_f32_16x16x32_bf16 v[148:151],a[212:215],v[88:91],v[148:151]
v_exp_f32 v111,v111
v_mfma_f32_16x16x32_bf16 v[156:159],a[216:219],v[80:83],v[156:159]
ds_read_b128 a[144:147],v1 offset:4096
v_cvt_pk_bf16_f32 v107,v110,v111
v_mfma_f32_16x16x32_bf16 v[156:159],a[220:223],v[88:91],v[156:159]
v_exp_f32 v112,v112
v_mfma_f32_16x16x32_bf16 v[164:167],a[224:227],v[80:83],v[164:167]
v_exp_f32 v113,v113
v_mfma_f32_16x16x32_bf16 v[164:167],a[228:231],v[88:91],v[164:167]
buffer_store_dwordx4 a[68:71],v6,s[40:43],s79 offen sc1
v_mfma_f32_16x16x32_bf16 v[172:175],a[232:235],v[80:83],v[172:175]
s_add_u32 s79,s79,65536
ds_read_b128 a[160:163],v1 offset:8192
v_cvt_pk_bf16_f32 v112,v112,v113
v_mfma_f32_16x16x32_bf16 v[172:175],a[236:239],v[88:91],v[172:175]
buffer_load_dwordx4 v26,s[48:51],s83 offen offset:2048 lds
v_mfma_f32_16x16x32_bf16 v[180:183],a[240:243],v[80:83],v[180:183]
v_exp_f32 v114,v114
v_mfma_f32_16x16x32_bf16 v[180:183],a[244:247],v[88:91],v[180:183]
v_exp_f32 v115,v115
v_mfma_f32_16x16x32_bf16 v[188:191],a[248:251],v[80:83],v[188:191]
ds_read_b128 a[164:167],v1 offset:8448
v_cvt_pk_bf16_f32 v113,v114,v115
v_mfma_f32_16x16x32_bf16 v[188:191],a[252:255],v[88:91],v[188:191]
v_exp_f32 v116,v116
v_mfma_f32_16x16x32_bf16 v[52:55],v[28:31],v[80:83],v[52:55]
ds_read_b128 a[168:171],v1 offset:8704
v_mfma_f32_16x16x32_bf16 v[52:55],v[28:31],v[88:91],v[52:55]
s_waitcnt lgkmcnt(3)
v_exp_f32 v117,v117
v_mfma_f32_16x16x32_bf16 v[64:67],a[128:131],v[128:131],v[32:35]
ds_read_b128 a[172:175],v1 offset:8960
v_cvt_pk_bf16_f32 v114,v116,v117
v_mfma_f32_16x16x32_bf16 v[64:67],a[132:135],v[136:139],v[64:67]
v_exp_f32 v118,v118
v_mfma_f32_16x16x32_bf16 v[64:67],a[136:139],v[144:147],v[64:67]
ds_read_b128 a[188:191],v1 offset:13056
)A"
R"A(v_mfma_f32_16x16x32_bf16 v[64:67],a[140:143],v[152:155],v[64:67]
buffer_load_dwordx4 v27,s[48:51],s83 offen offset:3072 lds
v_mfma_f32_16x16x32_bf16 v[68:71],a[156:159],v[152:155],v[32:35]
ds_read_b128 a[184:187],v1 offset:12800
v_mfma_f32_16x16x32_bf16 v[68:71],a[152:155],v[144:147],v[68:71]
v_exp_f32 v119,v119
v_mfma_f32_16x16x32_bf16 v[68:71],a[148:151],v[136:139],v[68:71]
ds_read_b128 a[180:183],v1 offset:12544
v_cvt_pk_bf16_f32 v115,v118,v119
v_mfma_f32_16x16x32_bf16 v[68:71],a[144:147],v[128:131],v[68:71]
s_waitcnt lgkmcnt(4)
v_exp_f32 v120,v120
v_mfma_f32_16x16x32_bf16 v[72:75],a[160:163],v[128:131],v[32:35]
buffer_store_dwordx4 a[72:75],v6,s[40:43],s80 offen offset:2048 sc1
v_mfma_f32_16x16x32_bf16 v[72:75],a[164:167],v[136:139],v[72:75]
ds_read_b128 a[176:179],v1 offset:12288
v_add_u32 v1,s78,v1
v_mfma_f32_16x16x32_bf16 v[72:75],a[168:171],v[144:147],v[72:75]
s_waitcnt lgkmcnt(3)
v_exp_f32 v121,v121
v_mfma_f32_16x16x32_bf16 v[72:75],a[172:175],v[152:155],v[72:75]
s_add_u32 m0,s86,86016
v_exp_f32 v122,v122
v_mfma_f32_16x16x32_bf16 v[76:79],a[188:191],v[152:155],v[32:35]
s_waitcnt lgkmcnt(0)
v_exp_f32 v123,v123
v_mfma_f32_16x16x32_bf16 v[76:79],a[184:187],v[144:147],v[76:79]
buffer_load_dwordx4 v7,s[48:51],s83 offen lds
v_mfma_f32_16x16x32_bf16 v[76:79],a[180:183],v[136:139],v[76:79]
s_add_u32 s83,s64,1
v_exp_f32 v124,v124
v_mfma_f32_16x16x32_bf16 v[76:79],a[176:179],v[128:131],v[76:79]
s_and_b32 s83,s83,3
v_exp_f32 v125,v125
v_mfma_f32_16x16x32_bf16 v[196:199],a[192:195],v[96:99],v[196:199]
s_lshl_b32 s83,s83,1
v_exp_f32 v126,v126
v_mfma_f32_16x16x32_bf16 v[196:199],a[196:199],v[104:107],v[196:199]
s_mul_i32 s83,s83,163840
v_exp_f32 v127,v127
v_mfma_f32_16x16x32_bf16 v[204:207],a[200:203],v[96:99],v[204:207]
s_add_u32 s83,s83,s85
v_cvt_pk_bf16_f32 v120,v120,v121
v_cvt_pk_bf16_f32 v121,v122,v123
v_mfma_f32_16x16x32_bf16 v[204:207],a[204:207],v[104:107],v[204:207]
v_cvt_pk_bf16_f32 v122,v124,v125
v_cvt_pk_bf16_f32 v123,v126,v127
v_mfma_f32_16x16x32_bf16 v[212:215],a[208:211],v[96:99],v[212:215]
v_exp_f32 v64,v64
v_mfma_f32_16x16x32_bf16 v[212:215],a[212:215],v[104:107],v[212:215]
s_add_u32 m0,s86,87040
v_exp_f32 v65,v65
v_mfma_f32_16x16x32_bf16 v[220:223],a[216:219],v[96:99],v[220:223]
v_exp_f32 v66,v66
v_mfma_f32_16x16x32_bf16 v[220:223],a[220:223],v[104:107],v[220:223]
buffer_load_dwordx4 v24,s[48:51],s84 offen lds
v_mfma_f32_16x16x32_bf16 v[228:231],a[224:227],v[96:99],v[228:231]
v_exp_f32 v67,v67
v_mfma_f32_16x16x32_bf16 v[228:231],a[228:231],v[104:107],v[228:231]
buffer_store_dwordx4 a[76:79],v6,s[40:43],s80 offen sc1
v_mfma_f32_16x16x32_bf16 v[236:239],a[232:235],v[96:99],v[236:239]
s_add_u32 s80,s80,65536
v_exp_f32 v68,v68
v_mfma_f32_16x16x32_bf16 v[236:239],a[236:239],v[104:107],v[236:239]
v_exp_f32 v69,v69
v_mfma_f32_16x16x32_bf16 v[244:247],a[240:243],v[96:99],v[244:247]
v_exp_f32 v70,v70
v_mfma_f32_16x16x32_bf16 v[244:247],a[244:247],v[104:107],v[244:247]
v_exp_f32 v71,v71
v_mfma_f32_16x16x32_bf16 v[252:255],a[248:251],v[96:99],v[252:255]
v_cvt_pk_bf16_f32 v64,v64,v65
v_cvt_pk_bf16_f32 v65,v66,v67
v_mfma_f32_16x16x32_bf16 v[252:255],a[252:255],v[104:107],v[252:255]
v_cvt_pk_bf16_f32 v66,v68,v69
v_cvt_pk_bf16_f32 v67,v70,v71
v_mfma_f32_16x16x32_bf16 v[56:59],v[28:31],v[96:99],v[56:59]
v_exp_f32 v72,v72
v_mfma_f32_16x16x32_bf16 v[56:59],v[28:31],v[104:107],v[56:59]
buffer_load_dwordx4 v25,s[48:51],s84 offen offset:1024 lds
v_mfma_f32_16x16x32_bf16 v[80:83],a[128:131],v[160:163],v[36:39]
v_exp_f32 v73,v73
v_mfma_f32_16x16x32_bf16 v[80:83],a[132:135],v[168:171],v[80:83]
v_exp_f32 v74,v74
v_mfma_f32_16x16x32_bf16 v[80:83],a[136:139],v[176:179],v[80:83]
v_exp_f32 v75,v75
v_mfma_f32_16x16x32_bf16 v[80:83],a[140:143],v[184:187],v[80:83]
v_exp_f32 v76,v76
v_mfma_f32_16x16x32_bf16 v[84:87],a[156:159],v[184:187],v[36:39]
v_exp_f32 v77,v77
v_mfma_f32_16x16x32_bf16 v[84:87],a[152:155],v[176:179],v[84:87]
v_exp_f32 v78,v78
v_mfma_f32_16x16x32_bf16 v[84:87],a[148:151],v[168:171],v[84:87]
v_exp_f32 v79,v79
v_mfma_f32_16x16x32_bf16 v[84:87],a[144:147],v[160:163],v[84:87]
v_cvt_pk_bf16_f32 v72,v72,v73
v_cvt_pk_bf16_f32 v73,v74,v75
v_mfma_f32_16x16x32_bf16 v[88:91],a[160:163],v[160:163],v[36:39]
buffer_store_dwordx4 a[80:83],v6,s[40:43],s81 offen offset:2048 sc1
v_mfma_f32_16x16x32_bf16 v[88:91],a[164:167],v[168:171],v[88:91]
v_cvt_pk_bf16_f32 v74,v76,v77
v_cvt_pk_bf16_f32 v75,v78,v79
v_mfma_f32_16x16x32_bf16 v[88:91],a[168:171],v[176:179],v[88:91]
buffer_load_dwordx4 v26,s[48:51],s84 offen offset:2048 lds
v_mfma_f32_16x16x32_bf16 v[88:91],a[172:175],v[184:187],v[88:91]
v_exp_f32 v80,v80
v_mfma_f32_16x16x32_bf16 v[92:95],a[188:191],v[184:187],v[36:39]
v_exp_f32 v81,v81
v_mfma_f32_16x16x32_bf16 v[92:95],a[184:187],v[176:179],v[92:95]
v_exp_f32 v82,v82
v_mfma_f32_16x16x32_bf16 v[92:95],a[180:183],v[168:171],v[92:95]
v_exp_f32 v83,v83
v_mfma_f32_16x16x32_bf16 v[92:95],a[176:179],v[160:163],v[92:95]
v_exp_f32 v84,v84
v_mfma_f32_16x16x32_bf16 a[32:35],a[192:195],v[112:115],a[32:35]
ds_read_b64_tr_b16 a[192:193],v2
ds_read_b64_tr_b16 a[194:195],v2 offset:4096
v_mfma_f32_16x16x32_bf16 a[32:35],a[196:199],v[120:123],a[32:35]
ds_read_b64_tr_b16 a[196:197],v2 offset:8192
v_cvt_pk_bf16_f32 v80,v80,v81
v_mfma_f32_16x16x32_bf16 a[36:39],a[200:203],v[112:115],a[36:39]
ds_read_b64_tr_b16 a[198:199],v2 offset:12288
v_cvt_pk_bf16_f32 v81,v82,v83
v_mfma_f32_16x16x32_bf16 a[36:39],a[204:207],v[120:123],a[36:39]
buffer_load_dwordx4 v27,s[48:51],s84 offen offset:3072 lds
v_mfma_f32_16x16x32_bf16 a[40:43],a[208:211],v[112:115],a[40:43]
ds_read_b64_tr_b16 a[200:201],v3
ds_read_b64_tr_b16 a[202:203],v3 offset:4096
v_mfma_f32_16x16x32_bf16 a[40:43],a[212:215],v[120:123],a[40:43]
v_exp_f32 v85,v85
v_mfma_f32_16x16x32_bf16 a[44:47],a[216:219],v[112:115],a[44:47]
ds_read_b64_tr_b16 a[204:205],v3 offset:8192
ds_read_b64_tr_b16 a[206:207],v3 offset:12288
v_mfma_f32_16x16x32_bf16 a[44:47],a[220:223],v[120:123],a[44:47]
ds_read_b64_tr_b16 a[208:209],v2 offset:256
v_cvt_pk_bf16_f32 v82,v84,v85
v_mfma_f32_16x16x32_bf16 a[48:51],a[224:227],v[112:115],a[48:51]
v_exp_f32 v86,v86
v_mfma_f32_16x16x32_bf16 a[48:51],a[228:231],v[120:123],a[48:51]
buffer_store_dwordx4 a[84:87],v6,s[40:43],s81 offen sc1
v_mfma_f32_16x16x32_bf16 a[52:55],a[232:235],v[112:115],a[52:55]
s_add_u32 s81,s81,65536
ds_read_b64_tr_b16 a[210:211],v2 offset:4352
ds_read_b64_tr_b16 a[212:213],v2 offset:8448
v_mfma_f32_16x16x32_bf16 a[52:55],a[236:239],v[120:123],a[52:55]
s_add_u32 m0,s86,91136
v_exp_f32 v87,v87
v_mfma_f32_16x16x32_bf16 a[56:59],a[240:243],v[112:115],a[56:59]
ds_read_b64_tr_b16 a[214:215],v2 offset:12544
ds_read_b64_tr_b16 a[216:217],v3 offset:256
v_mfma_f32_16x16x32_bf16 a[56:59],a[244:247],v[120:123],a[56:59]
buffer_load_dwordx4 v7,s[48:51],s84 offen lds
v_mfma_f32_16x16x32_bf16 a[60:63],a[248:251],v[112:115],a[60:63]
s_add_u32 s84,s83,20480
ds_read_b64_tr_b16 a[218:219],v3 offset:4352
ds_read_b64_tr_b16 a[220:221],v3 offset:8448
v_mfma_f32_16x16x32_bf16 a[60:63],a[252:255],v[120:123],a[60:63]
ds_read_b64_tr_b16 a[222:223],v3 offset:12544
v_cvt_pk_bf16_f32 v83,v86,v87
v_mfma_f32_16x16x32_bf16 v[60:63],v[28:31],v[112:115],v[60:63]
v_exp_f32 v88,v88
v_mfma_f32_16x16x32_bf16 v[60:63],v[28:31],v[120:123],v[60:63]
ds_read_b64_tr_b16 a[224:225],v2 offset:512
ds_read_b64_tr_b16 a[226:227],v2 offset:4608
v_mfma_f32_16x16x32_bf16 v[96:99],a[128:131],v[192:195],v[40:43]
v_exp_f32 v89,v89
v_mfma_f32_16x16x32_bf16 v[96:99],a[132:135],v[200:203],v[96:99]
ds_read_b64_tr_b16 a[228:229],v2 offset:8704
ds_read_b64_tr_b16 a[230:231],v2 offset:12800
v_mfma_f32_16x16x32_bf16 v[96:99],a[136:139],v[208:211],v[96:99]
ds_read_b64_tr_b16 a[232:233],v3 offset:512
v_cvt_pk_bf16_f32 v88,v88,v89
v_mfma_f32_16x16x32_bf16 v[96:99],a[140:143],v[216:219],v[96:99]
v_exp_f32 v90,v90
v_mfma_f32_16x16x32_bf16 v[100:103],a[156:159],v[216:219],v[40:43]
ds_read_b64_tr_b16 a[234:235],v3 offset:4608
ds_read_b64_tr_b16 a[236:237],v3 offset:8704
v_mfma_f32_16x16x32_bf16 v[100:103],a[152:155],v[208:211],v[100:103]
v_exp_f32 v91,v91
v_mfma_f32_16x16x32_bf16 v[100:103],a[148:151],v[200:203],v[100:103]
ds_read_b64_tr_b16 a[238:239],v3 offset:12800
ds_read_b64_tr_b16 a[240:241],v2 offset:768
v_mfma_f32_16x16x32_bf16 v[100:103],a[144:147],v[192:195],v[100:103]
ds_read_b64_tr_b16 a[242:243],v2 offset:4864
v_cvt_pk_bf16_f32 v89,v90,v91
v_mfma_f32_16x16x32_bf16 v[104:107],a[160:163],v[192:195],v[40:43]
buffer_store_dwordx4 a[88:91],v6,s[40:43],s82 offen offset:2048 sc1
v_mfma_f32_16x16x32_bf16 v[104:107],a[164:167],v[200:203],v[104:107]
ds_read_b64_tr_b16 a[244:245],v2 offset:8960
ds_read_b64_tr_b16 a[246:247],v2 offset:13056
v_mfma_f32_16x16x32_bf16 v[104:107],a[168:171],v[208:211],v[104:107]
ds_read_b64_tr_b16 a[248:249],v3 offset:768
v_add_u32 v2,s78,v2
v_mfma_f32_16x16x32_bf16 v[104:107],a[172:175],v[216:219],v[104:107]
v_exp_f32 v92,v92
v_mfma_f32_16x16x32_bf16 v[108:111],a[188:191],v[216:219],v[40:43]
ds_read_b64_tr_b16 a[250:251],v3 offset:4864
ds_read_b64_tr_b16 a[252:253],v3 offset:8960
v_mfma_f32_16x16x32_bf16 v[108:111],a[184:187],v[208:211],v[108:111]
s_waitcnt lgkmcnt(5)
ds_read_b64_tr_b16 a[254:255],v3 offset:13056
v_add_u32 v3,s78,v3
v_mfma_f32_16x16x32_bf16 v[108:111],a[180:183],v[200:203],v[108:111]
s_sub_u32 s78,s77,32768
v_exp_f32 v93,v93
v_mfma_f32_16x16x32_bf16 v[108:111],a[176:179],v[192:195],v[108:111]
s_and_b32 s78,s78,0x1ffff
v_exp_f32 v94,v94
v_mfma_f32_16x16x32_bf16 a[0:3],a[192:195],v[64:67],a[0:3]
s_sub_u32 s78,s78,s77
v_exp_f32 v95,v95
v_mfma_f32_16x16x32_bf16 a[0:3],a[196:199],v[72:75],a[0:3]
s_add_u32 s77,s77,s78
v_cvt_pk_bf16_f32 v90,v92,v93
v_cvt_pk_bf16_f32 v91,v94,v95
v_mfma_f32_16x16x32_bf16 a[4:7],a[200:203],v[64:67],a[4:7]
s_cmp_ge_u32 s63,2
s_cselect_b32 s6,L_gbS_%=-L_pcg_%=,s6
s_cselect_b32 s10,L_geS_%=-L_gbS_%=,s10
v_mfma_f32_16x16x32_bf16 a[4:7],a[204:207],v[72:75],a[4:7]
s_add_u32 s8,s8,s6
s_addc_u32 s9,s9,0
s_and_b32 s8,s8,0xffffff80
v_mfma_f32_16x16x32_bf16 a[8:11],a[208:211],v[64:67],a[8:11]
s_and_b32 s9,s9,0xffff
v_exp_f32 v96,v96
v_mfma_f32_16x16x32_bf16 a[8:11],a[212:215],v[72:75],a[8:11]
v_exp_f32 v97,v97
v_mfma_f32_16x16x32_bf16 a[12:15],a[216:219],v[64:67],a[12:15]
v_exp_f32 v98,v98
v_mfma_f32_16x16x32_bf16 a[12:15],a[220:223],v[72:75],a[12:15]
v_exp_f32 v99,v99
v_mfma_f32_16x16x32_bf16 a[16:19],a[224:227],v[64:67],a[16:19]
v_exp_f32 v100,v100
v_mfma_f32_16x16x32_bf16 a[16:19],a[228:231],v[72:75],a[16:19]
buffer_store_dwordx4 a[92:95],v6,s[40:43],s82 offen sc1
v_mfma_f32_16x16x32_bf16 a[20:23],a[232:235],v[64:67],a[20:23]
s_add_u32 s82,s82,65536
v_exp_f32 v101,v101
v_mfma_f32_16x16x32_bf16 a[20:23],a[236:239],v[72:75],a[20:23]
v_exp_f32 v102,v102
v_mfma_f32_16x16x32_bf16 a[24:27],a[240:243],v[64:67],a[24:27]
s_waitcnt lgkmcnt(0)
v_exp_f32 v103,v103
v_mfma_f32_16x16x32_bf16 a[24:27],a[244:247],v[72:75],a[24:27]
v_cvt_pk_bf16_f32 v96,v96,v97
v_cvt_pk_bf16_f32 v97,v98,v99
v_mfma_f32_16x16x32_bf16 a[28:31],a[248:251],v[64:67],a[28:31]
v_cvt_pk_bf16_f32 v98,v100,v101
v_cvt_pk_bf16_f32 v99,v102,v103
v_mfma_f32_16x16x32_bf16 a[28:31],a[252:255],v[72:75],a[28:31]
v_exp_f32 v104,v104
v_mfma_f32_16x16x32_bf16 v[48:51],v[28:31],v[64:67],v[48:51]
v_exp_f32 v105,v105
v_mfma_f32_16x16x32_bf16 v[48:51],v[28:31],v[72:75],v[48:51]
v_exp_f32 v106,v106
s_waitcnt vmcnt(36)
s_barrier
v_mfma_f32_16x16x32_bf16 v[112:115],a[128:131],v[224:227],v[44:47]
ds_read_b128 a[128:131],v1
v_cvt_pk_bf16_f32 v104,v104,v105
v_mfma_f32_16x16x32_bf16 v[112:115],a[132:135],v[232:235],v[112:115]
v_exp_f32 v107,v107
v_mfma_f32_16x16x32_bf16 v[112:115],a[136:139],v[240:243],v[112:115]
ds_read_b128 a[132:135],v1 offset:256
v_cvt_pk_bf16_f32 v105,v106,v107
v_mfma_f32_16x16x32_bf16 v[112:115],a[140:143],v[248:251],v[112:115]
v_exp_f32 v108,v108
v_mfma_f32_16x16x32_bf16 v[116:119],a[156:159],v[248:251],v[44:47]
v_exp_f32 v109,v109
v_mfma_f32_16x16x32_bf16 v[116:119],a[152:155],v[240:243],v[116:119]
s_add_u32 m0,s86,40960
ds_read_b128 a[136:139],v1 offset:512
v_cvt_pk_bf16_f32 v106,v108,v109
v_mfma_f32_16x16x32_bf16 v[116:119],a[148:151],v[232:235],v[116:119]
v_exp_f32 v110,v110
)A"
R"A(v_mfma_f32_16x16x32_bf16 v[116:119],a[144:147],v[224:227],v[116:119]
buffer_load_dwordx4 v24,s[48:51],s83 offen lds
v_mfma_f32_16x16x32_bf16 v[120:123],a[160:163],v[224:227],v[44:47]
v_exp_f32 v111,v111
v_mfma_f32_16x16x32_bf16 v[120:123],a[164:167],v[232:235],v[120:123]
ds_read_b128 a[140:143],v1 offset:768
v_cvt_pk_bf16_f32 v107,v110,v111
v_mfma_f32_16x16x32_bf16 v[120:123],a[168:171],v[240:243],v[120:123]
v_exp_f32 v112,v112
v_mfma_f32_16x16x32_bf16 v[120:123],a[172:175],v[248:251],v[120:123]
v_exp_f32 v113,v113
v_mfma_f32_16x16x32_bf16 v[124:127],a[188:191],v[248:251],v[44:47]
ds_read_b128 a[156:159],v1 offset:4864
v_cvt_pk_bf16_f32 v112,v112,v113
v_mfma_f32_16x16x32_bf16 v[124:127],a[184:187],v[240:243],v[124:127]
v_exp_f32 v114,v114
v_mfma_f32_16x16x32_bf16 v[124:127],a[180:183],v[232:235],v[124:127]
v_exp_f32 v115,v115
v_mfma_f32_16x16x32_bf16 v[124:127],a[176:179],v[224:227],v[124:127]
ds_read_b128 a[152:155],v1 offset:4608
v_cvt_pk_bf16_f32 v113,v114,v115
v_mfma_f32_16x16x32_bf16 v[132:135],a[192:195],v[80:83],v[132:135]
v_exp_f32 v116,v116
L_pd12_%=:
s_cbranch_execz L_trt1_%=
v_mfma_f32_16x16x32_bf16 v[132:135],a[196:199],v[88:91],v[132:135]
buffer_load_dwordx4 v25,s[48:51],s83 offen offset:1024 lds
v_mfma_f32_16x16x32_bf16 v[140:143],a[200:203],v[80:83],v[140:143]
v_exp_f32 v117,v117
v_mfma_f32_16x16x32_bf16 v[140:143],a[204:207],v[88:91],v[140:143]
ds_read_b128 a[148:151],v1 offset:4352
v_cvt_pk_bf16_f32 v114,v116,v117
v_mfma_f32_16x16x32_bf16 v[148:151],a[208:211],v[80:83],v[148:151]
v_exp_f32 v118,v118
v_mfma_f32_16x16x32_bf16 v[148:151],a[212:215],v[88:91],v[148:151]
v_exp_f32 v119,v119
v_mfma_f32_16x16x32_bf16 v[156:159],a[216:219],v[80:83],v[156:159]
ds_read_b128 a[144:147],v1 offset:4096
v_cvt_pk_bf16_f32 v115,v118,v119
v_mfma_f32_16x16x32_bf16 v[156:159],a[220:223],v[88:91],v[156:159]
v_exp_f32 v120,v120
v_mfma_f32_16x16x32_bf16 v[164:167],a[224:227],v[80:83],v[164:167]
buffer_load_dword v5,v4,s[8:11],s7 offen
v_mfma_f32_16x16x32_bf16 v[164:167],a[228:231],v[88:91],v[164:167]
v_exp_f32 v121,v121
v_mfma_f32_16x16x32_bf16 v[172:175],a[232:235],v[80:83],v[172:175]
ds_read_b128 a[160:163],v1 offset:8192
v_cvt_pk_bf16_f32 v120,v120,v121
v_mfma_f32_16x16x32_bf16 v[172:175],a[236:239],v[88:91],v[172:175]
v_exp_f32 v122,v122
v_mfma_f32_16x16x32_bf16 v[180:183],a[240:243],v[80:83],v[180:183]
v_exp_f32 v123,v123
v_mfma_f32_16x16x32_bf16 v[180:183],a[244:247],v[88:91],v[180:183]
ds_read_b128 a[164:167],v1 offset:8448
v_cvt_pk_bf16_f32 v121,v122,v123
v_mfma_f32_16x16x32_bf16 v[188:191],a[248:251],v[80:83],v[188:191]
buffer_load_dwordx4 v26,s[48:51],s83 offen offset:2048 lds
v_mfma_f32_16x16x32_bf16 v[188:191],a[252:255],v[88:91],v[188:191]
v_exp_f32 v124,v124
v_mfma_f32_16x16x32_bf16 v[52:55],v[28:31],v[80:83],v[52:55]
ds_read_b128 a[168:171],v1 offset:8704
v_mfma_f32_16x16x32_bf16 v[52:55],v[28:31],v[88:91],v[52:55]
s_waitcnt lgkmcnt(3)
v_exp_f32 v125,v125
v_mfma_f32_16x16x32_bf16 v[64:67],a[128:131],v[128:131],v[32:35]
ds_read_b128 a[172:175],v1 offset:8960
v_cvt_pk_bf16_f32 v122,v124,v125
v_mfma_f32_16x16x32_bf16 v[64:67],a[132:135],v[136:139],v[64:67]
v_exp_f32 v126,v126
v_mfma_f32_16x16x32_bf16 v[64:67],a[136:139],v[144:147],v[64:67]
ds_read_b128 a[188:191],v1 offset:13056
v_mfma_f32_16x16x32_bf16 v[64:67],a[140:143],v[152:155],v[64:67]
buffer_load_dwordx4 v27,s[48:51],s83 offen offset:3072 lds
v_mfma_f32_16x16x32_bf16 v[68:71],a[156:159],v[152:155],v[32:35]
ds_read_b128 a[184:187],v1 offset:12800
v_mfma_f32_16x16x32_bf16 v[68:71],a[152:155],v[144:147],v[68:71]
v_exp_f32 v127,v127
v_mfma_f32_16x16x32_bf16 v[68:71],a[148:151],v[136:139],v[68:71]
ds_read_b128 a[180:183],v1 offset:12544
v_cvt_pk_bf16_f32 v123,v126,v127
v_mfma_f32_16x16x32_bf16 v[68:71],a[144:147],v[128:131],v[68:71]
s_waitcnt lgkmcnt(4)
v_exp_f32 v64,v64
v_mfma_f32_16x16x32_bf16 v[72:75],a[160:163],v[128:131],v[32:35]
ds_read_b128 a[176:179],v1 offset:12288
v_add_u32 v1,s78,v1
v_mfma_f32_16x16x32_bf16 v[72:75],a[164:167],v[136:139],v[72:75]
v_exp_f32 v65,v65
v_mfma_f32_16x16x32_bf16 v[72:75],a[168:171],v[144:147],v[72:75]
s_waitcnt lgkmcnt(3)
v_exp_f32 v66,v66
v_mfma_f32_16x16x32_bf16 v[72:75],a[172:175],v[152:155],v[72:75]
s_add_u32 m0,s86,45056
v_exp_f32 v67,v67
v_mfma_f32_16x16x32_bf16 v[76:79],a[188:191],v[152:155],v[32:35]
s_waitcnt lgkmcnt(0)
v_exp_f32 v68,v68
v_mfma_f32_16x16x32_bf16 v[76:79],a[184:187],v[144:147],v[76:79]
buffer_load_dwordx4 v7,s[48:51],s83 offen lds
v_mfma_f32_16x16x32_bf16 v[76:79],a[180:183],v[136:139],v[76:79]
s_add_u32 s83,s64,2
v_exp_f32 v69,v69
v_mfma_f32_16x16x32_bf16 v[76:79],a[176:179],v[128:131],v[76:79]
s_and_b32 s83,s83,3
v_exp_f32 v70,v70
v_mfma_f32_16x16x32_bf16 v[196:199],a[192:195],v[96:99],v[196:199]
s_lshl_b32 s83,s83,1
v_exp_f32 v71,v71
v_mfma_f32_16x16x32_bf16 v[196:199],a[196:199],v[104:107],v[196:199]
s_mul_i32 s83,s83,163840
v_cvt_pk_bf16_f32 v64,v64,v65
v_cvt_pk_bf16_f32 v65,v66,v67
v_mfma_f32_16x16x32_bf16 v[204:207],a[200:203],v[96:99],v[204:207]
s_add_u32 s83,s83,s85
v_cvt_pk_bf16_f32 v66,v68,v69
v_cvt_pk_bf16_f32 v67,v70,v71
v_mfma_f32_16x16x32_bf16 v[204:207],a[204:207],v[104:107],v[204:207]
v_exp_f32 v72,v72
v_mfma_f32_16x16x32_bf16 v[212:215],a[208:211],v[96:99],v[212:215]
v_exp_f32 v73,v73
v_mfma_f32_16x16x32_bf16 v[212:215],a[212:215],v[104:107],v[212:215]
s_add_u32 m0,s86,46080
v_exp_f32 v74,v74
v_mfma_f32_16x16x32_bf16 v[220:223],a[216:219],v[96:99],v[220:223]
v_exp_f32 v75,v75
v_mfma_f32_16x16x32_bf16 v[220:223],a[220:223],v[104:107],v[220:223]
buffer_load_dwordx4 v24,s[48:51],s84 offen lds
v_mfma_f32_16x16x32_bf16 v[228:231],a[224:227],v[96:99],v[228:231]
v_exp_f32 v76,v76
v_mfma_f32_16x16x32_bf16 v[228:231],a[228:231],v[104:107],v[228:231]
v_exp_f32 v77,v77
v_mfma_f32_16x16x32_bf16 v[236:239],a[232:235],v[96:99],v[236:239]
v_exp_f32 v78,v78
v_mfma_f32_16x16x32_bf16 v[236:239],a[236:239],v[104:107],v[236:239]
v_exp_f32 v79,v79
v_mfma_f32_16x16x32_bf16 v[244:247],a[240:243],v[96:99],v[244:247]
v_cvt_pk_bf16_f32 v72,v72,v73
v_cvt_pk_bf16_f32 v73,v74,v75
v_mfma_f32_16x16x32_bf16 v[244:247],a[244:247],v[104:107],v[244:247]
v_cvt_pk_bf16_f32 v74,v76,v77
v_cvt_pk_bf16_f32 v75,v78,v79
v_mfma_f32_16x16x32_bf16 v[252:255],a[248:251],v[96:99],v[252:255]
v_lshrrev_b32 v1,6,v6
v_mul_u32_u24 v1,320,v1
v_mfma_f32_16x16x32_bf16 v[252:255],a[252:255],v[104:107],v[252:255]
v_mfma_f32_16x16x32_bf16 v[56:59],v[28:31],v[96:99],v[56:59]
v_mfma_f32_16x16x32_bf16 v[56:59],v[28:31],v[104:107],v[56:59]
buffer_load_dwordx4 v25,s[48:51],s84 offen offset:1024 lds
v_mfma_f32_16x16x32_bf16 v[80:83],a[128:131],v[160:163],v[36:39]
v_mfma_f32_16x16x32_bf16 v[80:83],a[132:135],v[168:171],v[80:83]
v_mfma_f32_16x16x32_bf16 v[80:83],a[136:139],v[176:179],v[80:83]
v_mfma_f32_16x16x32_bf16 v[80:83],a[140:143],v[184:187],v[80:83]
v_mfma_f32_16x16x32_bf16 v[84:87],a[156:159],v[184:187],v[36:39]
v_mfma_f32_16x16x32_bf16 v[84:87],a[152:155],v[176:179],v[84:87]
v_mfma_f32_16x16x32_bf16 v[84:87],a[148:151],v[168:171],v[84:87]
v_mfma_f32_16x16x32_bf16 v[84:87],a[144:147],v[160:163],v[84:87]
v_mfma_f32_16x16x32_bf16 v[88:91],a[160:163],v[160:163],v[36:39]
v_mfma_f32_16x16x32_bf16 v[88:91],a[164:167],v[168:171],v[88:91]
buffer_load_dwordx4 v26,s[48:51],s84 offen offset:2048 lds
v_mfma_f32_16x16x32_bf16 v[88:91],a[168:171],v[176:179],v[88:91]
v_exp_f32 v80,v80
v_mfma_f32_16x16x32_bf16 v[88:91],a[172:175],v[184:187],v[88:91]
v_exp_f32 v81,v81
v_mfma_f32_16x16x32_bf16 v[92:95],a[188:191],v[184:187],v[36:39]
v_exp_f32 v82,v82
v_mfma_f32_16x16x32_bf16 v[92:95],a[184:187],v[176:179],v[92:95]
v_exp_f32 v83,v83
v_mfma_f32_16x16x32_bf16 v[92:95],a[180:183],v[168:171],v[92:95]
v_exp_f32 v84,v84
v_mfma_f32_16x16x32_bf16 v[92:95],a[176:179],v[160:163],v[92:95]
v_exp_f32 v85,v85
v_mfma_f32_16x16x32_bf16 a[32:35],a[192:195],v[112:115],a[32:35]
ds_read_b64_tr_b16 a[192:193],v2
ds_read_b64_tr_b16 a[194:195],v2 offset:4096
v_mfma_f32_16x16x32_bf16 a[32:35],a[196:199],v[120:123],a[32:35]
ds_read_b64_tr_b16 a[196:197],v2 offset:8192
v_cvt_pk_bf16_f32 v80,v80,v81
v_mfma_f32_16x16x32_bf16 a[36:39],a[200:203],v[112:115],a[36:39]
ds_read_b64_tr_b16 a[198:199],v2 offset:12288
v_cvt_pk_bf16_f32 v81,v82,v83
v_mfma_f32_16x16x32_bf16 a[36:39],a[204:207],v[120:123],a[36:39]
buffer_load_dwordx4 v27,s[48:51],s84 offen offset:3072 lds
v_mfma_f32_16x16x32_bf16 a[40:43],a[208:211],v[112:115],a[40:43]
ds_read_b64_tr_b16 a[200:201],v3
ds_read_b64_tr_b16 a[202:203],v3 offset:4096
v_mfma_f32_16x16x32_bf16 a[40:43],a[212:215],v[120:123],a[40:43]
ds_read_b64_tr_b16 a[204:205],v3 offset:8192
v_cvt_pk_bf16_f32 v82,v84,v85
v_mfma_f32_16x16x32_bf16 a[44:47],a[216:219],v[112:115],a[44:47]
v_exp_f32 v86,v86
v_mfma_f32_16x16x32_bf16 a[44:47],a[220:223],v[120:123],a[44:47]
ds_read_b64_tr_b16 a[206:207],v3 offset:12288
ds_read_b64_tr_b16 a[208:209],v2 offset:256
v_mfma_f32_16x16x32_bf16 a[48:51],a[224:227],v[112:115],a[48:51]
v_exp_f32 v87,v87
v_mfma_f32_16x16x32_bf16 a[48:51],a[228:231],v[120:123],a[48:51]
ds_read_b64_tr_b16 a[210:211],v2 offset:4352
ds_read_b64_tr_b16 a[212:213],v2 offset:8448
v_mfma_f32_16x16x32_bf16 a[52:55],a[232:235],v[112:115],a[52:55]
ds_read_b64_tr_b16 a[214:215],v2 offset:12544
v_cvt_pk_bf16_f32 v83,v86,v87
v_mfma_f32_16x16x32_bf16 a[52:55],a[236:239],v[120:123],a[52:55]
s_add_u32 m0,s86,50176
v_exp_f32 v88,v88
v_mfma_f32_16x16x32_bf16 a[56:59],a[240:243],v[112:115],a[56:59]
ds_read_b64_tr_b16 a[216:217],v3 offset:256
ds_read_b64_tr_b16 a[218:219],v3 offset:4352
v_mfma_f32_16x16x32_bf16 a[56:59],a[244:247],v[120:123],a[56:59]
buffer_load_dwordx4 v7,s[48:51],s84 offen lds
v_mfma_f32_16x16x32_bf16 a[60:63],a[248:251],v[112:115],a[60:63]
s_add_u32 s84,s83,20480
ds_read_b64_tr_b16 a[220:221],v3 offset:8448
ds_read_b64_tr_b16 a[222:223],v3 offset:12544
v_mfma_f32_16x16x32_bf16 a[60:63],a[252:255],v[120:123],a[60:63]
v_exp_f32 v89,v89
v_mfma_f32_16x16x32_bf16 v[60:63],v[28:31],v[112:115],v[60:63]
ds_read_b64_tr_b16 a[224:225],v2 offset:512
ds_read_b64_tr_b16 a[226:227],v2 offset:4608
v_mfma_f32_16x16x32_bf16 v[60:63],v[28:31],v[120:123],v[60:63]
ds_read_b64_tr_b16 a[228:229],v2 offset:8704
v_cvt_pk_bf16_f32 v88,v88,v89
v_mfma_f32_16x16x32_bf16 v[96:99],a[128:131],v[192:195],v[40:43]
v_exp_f32 v90,v90
v_mfma_f32_16x16x32_bf16 v[96:99],a[132:135],v[200:203],v[96:99]
ds_read_b64_tr_b16 a[230:231],v2 offset:12800
ds_read_b64_tr_b16 a[232:233],v3 offset:512
v_mfma_f32_16x16x32_bf16 v[96:99],a[136:139],v[208:211],v[96:99]
v_exp_f32 v91,v91
v_mfma_f32_16x16x32_bf16 v[96:99],a[140:143],v[216:219],v[96:99]
ds_read_b64_tr_b16 a[234:235],v3 offset:4608
ds_read_b64_tr_b16 a[236:237],v3 offset:8704
v_mfma_f32_16x16x32_bf16 v[100:103],a[156:159],v[216:219],v[40:43]
ds_read_b64_tr_b16 a[238:239],v3 offset:12800
v_cvt_pk_bf16_f32 v89,v90,v91
v_mfma_f32_16x16x32_bf16 v[100:103],a[152:155],v[208:211],v[100:103]
v_exp_f32 v92,v92
v_mfma_f32_16x16x32_bf16 v[100:103],a[148:151],v[200:203],v[100:103]
ds_read_b64_tr_b16 a[240:241],v2 offset:768
ds_read_b64_tr_b16 a[242:243],v2 offset:4864
v_mfma_f32_16x16x32_bf16 v[100:103],a[144:147],v[192:195],v[100:103]
v_exp_f32 v93,v93
v_mfma_f32_16x16x32_bf16 v[104:107],a[160:163],v[192:195],v[40:43]
ds_read_b64_tr_b16 a[244:245],v2 offset:8960
ds_read_b64_tr_b16 a[246:247],v2 offset:13056
v_mfma_f32_16x16x32_bf16 v[104:107],a[164:167],v[200:203],v[104:107]
ds_read_b64_tr_b16 a[248:249],v3 offset:768
v_cvt_pk_bf16_f32 v90,v92,v93
v_mfma_f32_16x16x32_bf16 v[104:107],a[168:171],v[208:211],v[104:107]
ds_read_b64_tr_b16 a[250:251],v3 offset:4864
v_bfe_u32 v2,v6,4,2
v_mfma_f32_16x16x32_bf16 v[104:107],a[172:175],v[216:219],v[104:107]
ds_read_b64_tr_b16 a[252:253],v3 offset:8960
v_lshl_add_u32 v1,v2,3,v1
v_mfma_f32_16x16x32_bf16 v[108:111],a[188:191],v[216:219],v[40:43]
ds_read_b64_tr_b16 a[254:255],v3 offset:13056
v_add_u32 v1,122880,v1
v_mfma_f32_16x16x32_bf16 v[108:111],a[184:187],v[208:211],v[108:111]
v_exp_f32 v94,v94
v_mfma_f32_16x16x32_bf16 v[108:111],a[180:183],v[200:203],v[108:111]
v_exp_f32 v95,v95
v_mfma_f32_16x16x32_bf16 v[108:111],a[176:179],v[192:195],v[108:111]
s_waitcnt lgkmcnt(6)
)A"
R"A(v_cvt_pk_bf16_f32 v91,v94,v95
v_bfe_u32 v2,v6,8,1
v_mfma_f32_16x16x32_bf16 a[0:3],a[192:195],v[64:67],a[0:3]
v_exp_f32 v96,v96
v_mfma_f32_16x16x32_bf16 a[0:3],a[196:199],v[72:75],a[0:3]
v_exp_f32 v97,v97
v_mfma_f32_16x16x32_bf16 a[4:7],a[200:203],v[64:67],a[4:7]
v_exp_f32 v98,v98
v_mfma_f32_16x16x32_bf16 a[4:7],a[204:207],v[72:75],a[4:7]
v_exp_f32 v99,v99
v_mfma_f32_16x16x32_bf16 a[8:11],a[208:211],v[64:67],a[8:11]
v_exp_f32 v100,v100
v_mfma_f32_16x16x32_bf16 a[8:11],a[212:215],v[72:75],a[8:11]
v_exp_f32 v101,v101
v_mfma_f32_16x16x32_bf16 a[12:15],a[216:219],v[64:67],a[12:15]
v_exp_f32 v102,v102
v_mfma_f32_16x16x32_bf16 a[12:15],a[220:223],v[72:75],a[12:15]
v_exp_f32 v103,v103
v_mfma_f32_16x16x32_bf16 a[16:19],a[224:227],v[64:67],a[16:19]
v_cvt_pk_bf16_f32 v96,v96,v97
v_cvt_pk_bf16_f32 v97,v98,v99
v_mfma_f32_16x16x32_bf16 a[16:19],a[228:231],v[72:75],a[16:19]
v_cvt_pk_bf16_f32 v98,v100,v101
v_cvt_pk_bf16_f32 v99,v102,v103
v_mfma_f32_16x16x32_bf16 a[20:23],a[232:235],v[64:67],a[20:23]
v_exp_f32 v104,v104
v_mfma_f32_16x16x32_bf16 a[20:23],a[236:239],v[72:75],a[20:23]
v_exp_f32 v105,v105
v_mfma_f32_16x16x32_bf16 a[24:27],a[240:243],v[64:67],a[24:27]
s_waitcnt lgkmcnt(0)
v_exp_f32 v106,v106
v_mfma_f32_16x16x32_bf16 a[24:27],a[244:247],v[72:75],a[24:27]
v_exp_f32 v107,v107
v_mfma_f32_16x16x32_bf16 a[28:31],a[248:251],v[64:67],a[28:31]
v_exp_f32 v108,v108
v_mfma_f32_16x16x32_bf16 a[28:31],a[252:255],v[72:75],a[28:31]
v_exp_f32 v109,v109
v_mfma_f32_16x16x32_bf16 v[48:51],v[28:31],v[64:67],v[48:51]
v_exp_f32 v110,v110
v_mfma_f32_16x16x32_bf16 v[48:51],v[28:31],v[72:75],v[48:51]
v_exp_f32 v111,v111
s_waitcnt vmcnt(11)
s_barrier
v_mfma_f32_16x16x32_bf16 v[112:115],a[128:131],v[224:227],v[44:47]
buffer_store_dwordx2 v[8:9],v10,s[44:47],s18 offen offset:256 sc1
v_mfma_f32_16x16x32_bf16 v[112:115],a[132:135],v[232:235],v[112:115]
v_cvt_pk_bf16_f32 v104,v104,v105
v_cvt_pk_bf16_f32 v105,v106,v107
v_mfma_f32_16x16x32_bf16 v[112:115],a[136:139],v[240:243],v[112:115]
v_cvt_pk_bf16_f32 v106,v108,v109
v_cvt_pk_bf16_f32 v107,v110,v111
v_mfma_f32_16x16x32_bf16 v[112:115],a[140:143],v[248:251],v[112:115]
s_add_u32 m0,s86,0
v_cmp_nlt_f32 s[54:55],v48,s73
v_max_f32 v49,0x00800000,v48
v_mfma_f32_16x16x32_bf16 v[116:119],a[156:159],v[248:251],v[44:47]
v_rcp_f32 v49,v49
v_mfma_f32_16x16x32_bf16 v[116:119],a[152:155],v[240:243],v[116:119]
v_lshl_add_u32 v1,v2,5,v1
v_lshlrev_b32 v2,6,v2
v_mfma_f32_16x16x32_bf16 v[116:119],a[148:151],v[232:235],v[116:119]
buffer_load_dwordx4 v24,s[48:51],s83 offen lds
v_mfma_f32_16x16x32_bf16 v[116:119],a[144:147],v[224:227],v[116:119]
v_exp_f32 v112,v112
v_mfma_f32_16x16x32_bf16 v[120:123],a[160:163],v[224:227],v[44:47]
v_exp_f32 v113,v113
v_mfma_f32_16x16x32_bf16 v[120:123],a[164:167],v[232:235],v[120:123]
v_exp_f32 v114,v114
v_mfma_f32_16x16x32_bf16 v[120:123],a[168:171],v[240:243],v[120:123]
v_exp_f32 v115,v115
v_mfma_f32_16x16x32_bf16 v[120:123],a[172:175],v[248:251],v[120:123]
v_exp_f32 v116,v116
v_mfma_f32_16x16x32_bf16 v[124:127],a[188:191],v[248:251],v[44:47]
buffer_load_dwordx4 v25,s[48:51],s83 offen offset:1024 lds
v_mfma_f32_16x16x32_bf16 v[124:127],a[184:187],v[240:243],v[124:127]
v_exp_f32 v117,v117
v_mfma_f32_16x16x32_bf16 v[124:127],a[180:183],v[232:235],v[124:127]
v_exp_f32 v118,v118
v_mfma_f32_16x16x32_bf16 v[124:127],a[176:179],v[224:227],v[124:127]
v_exp_f32 v119,v119
v_mfma_f32_16x16x32_bf16 v[52:55],v[28:31],v[80:83],v[52:55]
v_cvt_pk_bf16_f32 v112,v112,v113
v_cvt_pk_bf16_f32 v113,v114,v115
v_mfma_f32_16x16x32_bf16 v[52:55],v[28:31],v[88:91],v[52:55]
v_cvt_pk_bf16_f32 v114,v116,v117
v_cvt_pk_bf16_f32 v115,v118,v119
v_mfma_f32_16x16x32_bf16 v[132:135],a[192:195],v[80:83],v[132:135]
buffer_load_dwordx4 v26,s[48:51],s83 offen offset:2048 lds
v_mfma_f32_16x16x32_bf16 v[132:135],a[196:199],v[88:91],v[132:135]
v_exp_f32 v120,v120
v_mfma_f32_16x16x32_bf16 v[140:143],a[200:203],v[80:83],v[140:143]
v_exp_f32 v121,v121
v_mfma_f32_16x16x32_bf16 v[140:143],a[204:207],v[88:91],v[140:143]
v_exp_f32 v122,v122
v_mfma_f32_16x16x32_bf16 v[148:151],a[208:211],v[80:83],v[148:151]
v_exp_f32 v123,v123
v_mfma_f32_16x16x32_bf16 v[148:151],a[212:215],v[88:91],v[148:151]
v_exp_f32 v124,v124
v_mfma_f32_16x16x32_bf16 v[156:159],a[216:219],v[80:83],v[156:159]
buffer_load_dwordx4 v27,s[48:51],s83 offen offset:3072 lds
v_mfma_f32_16x16x32_bf16 v[156:159],a[220:223],v[88:91],v[156:159]
v_exp_f32 v125,v125
v_mfma_f32_16x16x32_bf16 v[164:167],a[224:227],v[80:83],v[164:167]
v_exp_f32 v126,v126
v_mfma_f32_16x16x32_bf16 v[164:167],a[228:231],v[88:91],v[164:167]
s_add_u32 m0,s86,4096
v_exp_f32 v127,v127
v_mfma_f32_16x16x32_bf16 v[172:175],a[232:235],v[80:83],v[172:175]
v_cvt_pk_bf16_f32 v120,v120,v121
v_cvt_pk_bf16_f32 v121,v122,v123
v_mfma_f32_16x16x32_bf16 v[172:175],a[236:239],v[88:91],v[172:175]
v_cvt_pk_bf16_f32 v122,v124,v125
v_cvt_pk_bf16_f32 v123,v126,v127
v_mfma_f32_16x16x32_bf16 v[180:183],a[240:243],v[80:83],v[180:183]
buffer_load_dwordx4 v7,s[48:51],s83 offen lds
v_mfma_f32_16x16x32_bf16 v[180:183],a[244:247],v[88:91],v[180:183]
v_cmp_nlt_f32 s[56:57],v52,s73
v_sub_u32 v2,v1,v2
v_mfma_f32_16x16x32_bf16 v[188:191],a[248:251],v[80:83],v[188:191]
s_or_b64 s[54:55],s[54:55],s[56:57]
v_accvgpr_read_b32 v8,a0
v_accvgpr_read_b32 v9,a1
v_mfma_f32_16x16x32_bf16 v[188:191],a[252:255],v[88:91],v[188:191]
s_add_u32 m0,s86,5120
v_accvgpr_read_b32 v10,a2
v_accvgpr_read_b32 v11,a3
v_mfma_f32_16x16x32_bf16 v[56:59],v[28:31],v[96:99],v[56:59]
v_accvgpr_read_b32 v12,a4
v_accvgpr_read_b32 v13,a5
v_mfma_f32_16x16x32_bf16 v[56:59],v[28:31],v[104:107],v[56:59]
v_accvgpr_read_b32 v14,a6
v_accvgpr_read_b32 v15,a7
v_mfma_f32_16x16x32_bf16 v[196:199],a[192:195],v[96:99],v[196:199]
buffer_load_dwordx4 v24,s[48:51],s84 offen lds
v_mfma_f32_16x16x32_bf16 v[196:199],a[196:199],v[104:107],v[196:199]
v_mul_f32 v8,v8,v49
v_mul_f32 v9,v9,v49
v_mfma_f32_16x16x32_bf16 v[204:207],a[200:203],v[96:99],v[204:207]
v_cmp_nlt_f32 s[56:57],v56,s73
v_mul_f32 v10,v10,v49
v_mfma_f32_16x16x32_bf16 v[204:207],a[204:207],v[104:107],v[204:207]
s_or_b64 s[54:55],s[54:55],s[56:57]
v_mul_f32 v11,v11,v49
v_mul_f32 v12,v12,v49
v_mfma_f32_16x16x32_bf16 v[212:215],a[208:211],v[96:99],v[212:215]
v_mul_f32 v13,v13,v49
v_mul_f32 v14,v14,v49
v_mfma_f32_16x16x32_bf16 v[212:215],a[212:215],v[104:107],v[212:215]
v_mul_f32 v15,v15,v49
v_cvt_pk_bf16_f32 v32,v8,v9
v_mfma_f32_16x16x32_bf16 v[220:223],a[216:219],v[96:99],v[220:223]
buffer_load_dwordx4 v25,s[48:51],s84 offen offset:1024 lds
v_mfma_f32_16x16x32_bf16 v[220:223],a[220:223],v[104:107],v[220:223]
v_cvt_pk_bf16_f32 v33,v10,v11
v_cvt_pk_bf16_f32 v34,v12,v13
v_mfma_f32_16x16x32_bf16 v[228:231],a[224:227],v[96:99],v[228:231]
v_cvt_pk_bf16_f32 v35,v14,v15
v_accvgpr_read_b32 v16,a8
v_mfma_f32_16x16x32_bf16 v[228:231],a[228:231],v[104:107],v[228:231]
v_accvgpr_read_b32 v17,a9
v_accvgpr_read_b32 v18,a10
v_mfma_f32_16x16x32_bf16 v[236:239],a[232:235],v[96:99],v[236:239]
v_accvgpr_read_b32 v19,a11
v_accvgpr_read_b32 v20,a12
v_mfma_f32_16x16x32_bf16 v[236:239],a[236:239],v[104:107],v[236:239]
v_accvgpr_read_b32 v21,a13
v_accvgpr_read_b32 v22,a14
v_mfma_f32_16x16x32_bf16 v[244:247],a[240:243],v[96:99],v[244:247]
buffer_load_dwordx4 v26,s[48:51],s84 offen offset:2048 lds
v_mfma_f32_16x16x32_bf16 v[244:247],a[244:247],v[104:107],v[244:247]
v_accvgpr_read_b32 v23,a15
v_mul_f32 v16,v16,v49
v_mfma_f32_16x16x32_bf16 v[252:255],a[248:251],v[96:99],v[252:255]
v_mul_f32 v17,v17,v49
v_mul_f32 v18,v18,v49
v_mfma_f32_16x16x32_bf16 v[252:255],a[252:255],v[104:107],v[252:255]
v_mul_f32 v19,v19,v49
v_mul_f32 v20,v20,v49
v_mfma_f32_16x16x32_bf16 v[60:63],v[28:31],v[112:115],v[60:63]
v_mul_f32 v21,v21,v49
v_mul_f32 v22,v22,v49
v_mfma_f32_16x16x32_bf16 v[60:63],v[28:31],v[120:123],v[60:63]
v_mul_f32 v23,v23,v49
v_cvt_pk_bf16_f32 v36,v16,v17
v_mfma_f32_16x16x32_bf16 a[32:35],a[192:195],v[112:115],a[32:35]
buffer_load_dwordx4 v27,s[48:51],s84 offen offset:3072 lds
v_mfma_f32_16x16x32_bf16 a[32:35],a[196:199],v[120:123],a[32:35]
v_cvt_pk_bf16_f32 v37,v18,v19
v_cvt_pk_bf16_f32 v38,v20,v21
v_mfma_f32_16x16x32_bf16 a[36:39],a[200:203],v[112:115],a[36:39]
v_cmp_nlt_f32 s[56:57],v60,s73
v_cvt_pk_bf16_f32 v39,v22,v23
v_mfma_f32_16x16x32_bf16 a[36:39],a[204:207],v[120:123],a[36:39]
s_add_u32 m0,s86,9216
v_accvgpr_read_b32 v8,a16
v_accvgpr_read_b32 v9,a17
v_mfma_f32_16x16x32_bf16 a[40:43],a[208:211],v[112:115],a[40:43]
s_or_b64 s[54:55],s[54:55],s[56:57]
v_accvgpr_read_b32 v10,a18
v_accvgpr_read_b32 v11,a19
v_mfma_f32_16x16x32_bf16 a[40:43],a[212:215],v[120:123],a[40:43]
v_accvgpr_read_b32 v12,a20
v_accvgpr_read_b32 v13,a21
v_mfma_f32_16x16x32_bf16 a[44:47],a[216:219],v[112:115],a[44:47]
buffer_load_dwordx4 v7,s[48:51],s84 offen lds
v_mfma_f32_16x16x32_bf16 a[44:47],a[220:223],v[120:123],a[44:47]
v_accvgpr_read_b32 v14,a22
v_accvgpr_read_b32 v15,a23
v_mfma_f32_16x16x32_bf16 a[48:51],a[224:227],v[112:115],a[48:51]
v_mul_f32 v8,v8,v49
v_mul_f32 v9,v9,v49
v_mfma_f32_16x16x32_bf16 a[48:51],a[228:231],v[120:123],a[48:51]
v_mul_f32 v10,v10,v49
v_mul_f32 v11,v11,v49
v_mfma_f32_16x16x32_bf16 a[52:55],a[232:235],v[112:115],a[52:55]
v_mul_f32 v12,v12,v49
v_mul_f32 v13,v13,v49
v_mfma_f32_16x16x32_bf16 a[52:55],a[236:239],v[120:123],a[52:55]
v_mul_f32 v14,v14,v49
v_mul_f32 v15,v15,v49
v_mfma_f32_16x16x32_bf16 a[56:59],a[240:243],v[112:115],a[56:59]
v_cvt_pk_bf16_f32 v40,v8,v9
v_cvt_pk_bf16_f32 v41,v10,v11
v_mfma_f32_16x16x32_bf16 a[56:59],a[244:247],v[120:123],a[56:59]
v_cvt_pk_bf16_f32 v42,v12,v13
v_cvt_pk_bf16_f32 v43,v14,v15
v_mfma_f32_16x16x32_bf16 a[60:63],a[248:251],v[112:115],a[60:63]
ds_read_b64_tr_b16 v[64:65],v1
ds_read_b64_tr_b16 v[66:67],v1 offset:5120
v_mfma_f32_16x16x32_bf16 a[60:63],a[252:255],v[120:123],a[60:63]
ds_read_b64_tr_b16 v[68:69],v1 offset:10240
v_max_f32 v53,0x00800000,v52
v_rcp_f32 v53,v53
v_max_f32 v57,0x00800000,v56
v_rcp_f32 v57,v57
v_max_f32 v61,0x00800000,v60
v_rcp_f32 v61,v61
ds_read_b64_tr_b16 v[70:71],v1 offset:15360
ds_read_b64_tr_b16 v[72:73],v1 offset:20480
ds_read_b64_tr_b16 v[74:75],v1 offset:25600
ds_read_b64_tr_b16 v[76:77],v1 offset:30720
ds_read_b64_tr_b16 v[78:79],v1 offset:35840
ds_read_b64_tr_b16 v[80:81],v2 offset:32
ds_read_b64_tr_b16 v[82:83],v2 offset:5152
ds_read_b64_tr_b16 v[84:85],v2 offset:10272
ds_read_b64_tr_b16 v[86:87],v2 offset:15392
ds_read_b64_tr_b16 v[88:89],v2 offset:20512
ds_read_b64_tr_b16 v[90:91],v2 offset:25632
ds_read_b64_tr_b16 v[92:93],v2 offset:30752
ds_read_b64_tr_b16 v[94:95],v2 offset:35872
s_cmp_lg_u64 s[54:55],0
s_cbranch_scc1 L_slow1_%=
L_ok1_%=:
s_cmp_ge_u32 s63,2
s_cbranch_scc1 L_GS_%=
L_gbF_%=:
s_waitcnt lgkmcnt(14)
v_mfma_f32_16x16x32_bf16 a[192:195],v[64:67],a[68:71],0
buffer_store_dwordx4 v[32:35],v6,s[40:43],s79 offen sc1
s_waitcnt lgkmcnt(12)
v_mfma_f32_16x16x32_bf16 a[192:195],v[68:71],a[76:79],a[192:195]
s_lshl_b32 s91,s63,8
v_bfe_u32 v3,v6,4,2
v_min_u32 v3,2,v3
s_waitcnt lgkmcnt(10)
v_mfma_f32_16x16x32_bf16 a[192:195],v[72:75],a[84:87],a[192:195]
s_add_u32 s87,s64,1
v_add3_u32 v3,v3,s64,1
v_and_b32 v3,3,v3
s_waitcnt lgkmcnt(8)
v_mfma_f32_16x16x32_bf16 a[192:195],v[76:79],a[92:95],a[192:195]
s_and_b32 s87,s87,3
ds_read_b64_tr_b16 v[96:97],v1 offset:64
v_lshlrev_b32 v3,6,v3
v_mfma_f32_16x16x32_bf16 a[196:199],v[76:79],a[124:127],0
s_lshl_b32 s87,s87,1
ds_read_b64_tr_b16 v[98:99],v1 offset:5184
ds_read_b64_tr_b16 v[100:101],v1 offset:10304
v_mfma_f32_16x16x32_bf16 a[196:199],v[72:75],a[116:119],a[196:199]
s_lshl_b32 s87,s87,16
ds_read_b64_tr_b16 v[102:103],v1 offset:15424
v_lshlrev_b32 v29,10,v6
v_mfma_f32_16x16x32_bf16 a[196:199],v[68:71],a[108:111],a[196:199]
s_add_u32 s88,s87,4096
ds_read_b64_tr_b16 v[104:105],v1 offset:20544
v_accvgpr_read_b32 v8,a24
v_mfma_f32_16x16x32_bf16 a[196:199],v[64:67],a[100:103],a[196:199]
s_add_u32 s89,s87,8192
ds_read_b64_tr_b16 v[106:107],v1 offset:25664
v_accvgpr_read_b32 v9,a25
v_mfma_f32_16x16x32_bf16 a[200:203],v[64:67],a[64:67],0
s_add_u32 s90,s87,12288
ds_read_b64_tr_b16 v[108:109],v1 offset:30784
ds_read_b64_tr_b16 v[110:111],v1 offset:35904
v_mfma_f32_16x16x32_bf16 a[200:203],v[68:71],a[72:75],a[200:203]
)A"
R"A(s_add_u32 m0,s86,122880
ds_read_b64_tr_b16 v[112:113],v2 offset:96
v_accvgpr_read_b32 v10,a26
v_mfma_f32_16x16x32_bf16 a[200:203],v[72:75],a[80:83],a[200:203]
s_add_u32 s83,s64,3
ds_read_b64_tr_b16 v[114:115],v2 offset:5216
v_accvgpr_read_b32 v11,a27
v_mfma_f32_16x16x32_bf16 a[200:203],v[76:79],a[88:91],a[200:203]
s_and_b32 s83,s83,3
ds_read_b64_tr_b16 v[116:117],v2 offset:10336
v_accvgpr_read_b32 v12,a28
v_mfma_f32_16x16x32_bf16 a[204:207],v[76:79],a[120:123],0
s_lshl_b32 s83,s83,1
ds_read_b64_tr_b16 v[118:119],v2 offset:15456
ds_read_b64_tr_b16 v[120:121],v2 offset:20576
v_mfma_f32_16x16x32_bf16 a[204:207],v[72:75],a[112:115],a[204:207]
s_mul_i32 s83,s83,163840
ds_read_b64_tr_b16 v[122:123],v2 offset:25696
v_accvgpr_read_b32 v13,a29
v_mfma_f32_16x16x32_bf16 a[204:207],v[68:71],a[104:107],a[204:207]
s_add_u32 s83,s83,s85
ds_read_b64_tr_b16 v[124:125],v2 offset:30816
v_accvgpr_read_b32 v14,a30
v_mfma_f32_16x16x32_bf16 a[204:207],v[64:67],a[96:99],a[204:207]
buffer_store_dwordx4 v[36:39],v6,s[40:43],s80 offen sc1
s_waitcnt lgkmcnt(9)
v_mfma_f32_16x16x32_bf16 a[208:211],v[80:83],a[68:71],0
s_add_u32 s84,s83,20480
ds_read_b64_tr_b16 v[126:127],v2 offset:35936
ds_read_b64_tr_b16 v[64:65],v1 offset:128
v_mfma_f32_16x16x32_bf16 a[208:211],v[84:87],a[76:79],a[208:211]
s_lshl_b32 s66,s61,3
ds_read_b64_tr_b16 v[66:67],v1 offset:5248
v_accvgpr_read_b32 v15,a31
v_mfma_f32_16x16x32_bf16 a[208:211],v[88:91],a[84:87],a[208:211]
s_add_u32 s66,s66,s64
ds_read_b64_tr_b16 v[68:69],v1 offset:10368
v_mul_f32 v8,v8,v49
v_mfma_f32_16x16x32_bf16 a[208:211],v[92:95],a[92:95],a[208:211]
s_mul_i32 s66,s66,262144
ds_read_b64_tr_b16 v[70:71],v1 offset:15488
v_mul_f32 v9,v9,v49
v_mfma_f32_16x16x32_bf16 a[212:215],v[92:95],a[124:127],0
s_waitcnt vmcnt(21)
buffer_load_dwordx2 v[4:5],v3,s[44:47],s91 offen offset:256 sc1
v_mfma_f32_16x16x32_bf16 a[212:215],v[88:91],a[116:119],a[212:215]
s_add_u32 s66,s66,100663296
ds_read_b64_tr_b16 v[72:73],v1 offset:20608
ds_read_b64_tr_b16 v[74:75],v1 offset:25728
v_mfma_f32_16x16x32_bf16 a[212:215],v[84:87],a[108:111],a[212:215]
s_mov_b32 s38,262144
ds_read_b64_tr_b16 v[76:77],v1 offset:30848
v_mul_f32 v10,v10,v49
v_mfma_f32_16x16x32_bf16 a[212:215],v[80:83],a[100:103],a[212:215]
s_mov_b32 s39,0x20000
ds_read_b64_tr_b16 v[78:79],v1 offset:35968
v_mul_f32 v11,v11,v49
s_nop 0
L_pd13_%=:
s_cbranch_execz L_trt1_%=
v_mfma_f32_16x16x32_bf16 a[216:219],v[80:83],a[64:67],0
s_add_u32 s74,s63,1
v_mul_f32 v12,v12,v49
v_mul_f32 v13,v13,v49
v_mfma_f32_16x16x32_bf16 a[216:219],v[84:87],a[72:75],a[216:219]
s_and_b32 s74,s74,3
v_mul_f32 v14,v14,v49
v_mul_f32 v15,v15,v49
v_mfma_f32_16x16x32_bf16 a[216:219],v[88:91],a[80:83],a[216:219]
s_lshl_b32 s74,s74,2
v_cvt_pk_bf16_f32 v44,v8,v9
v_cvt_pk_bf16_f32 v45,v10,v11
v_mfma_f32_16x16x32_bf16 a[216:219],v[92:95],a[88:91],a[216:219]
s_add_u32 s74,s74,s60
v_cvt_pk_bf16_f32 v46,v12,v13
v_cvt_pk_bf16_f32 v47,v14,v15
v_mfma_f32_16x16x32_bf16 a[220:223],v[92:95],a[120:123],0
s_mul_i32 s74,s74,16384
ds_read_b64_tr_b16 v[92:93],v2 offset:30880
ds_read_b64_tr_b16 v[94:95],v2 offset:36000
v_mfma_f32_16x16x32_bf16 a[220:223],v[88:91],a[112:115],a[220:223]
s_add_u32 s74,s74,8192
ds_read_b64_tr_b16 v[88:89],v2 offset:20640
v_mul_f32 v132,v132,v53
v_mfma_f32_16x16x32_bf16 a[220:223],v[84:87],a[104:107],a[220:223]
buffer_store_dwordx4 v[40:43],v6,s[40:43],s81 offen sc1
v_mfma_f32_16x16x32_bf16 a[220:223],v[80:83],a[96:99],a[220:223]
s_add_u32 s75,s63,2
ds_read_b64_tr_b16 v[80:81],v2 offset:160
ds_read_b64_tr_b16 v[82:83],v2 offset:5280
v_mfma_f32_16x16x32_bf16 a[224:227],v[96:99],a[68:71],0
s_and_b32 s75,s75,3
ds_read_b64_tr_b16 v[84:85],v2 offset:10400
v_mul_f32 v133,v133,v53
v_mfma_f32_16x16x32_bf16 a[224:227],v[100:103],a[76:79],a[224:227]
s_lshl_b32 s75,s75,2
ds_read_b64_tr_b16 v[86:87],v2 offset:15520
v_mul_f32 v134,v134,v53
v_mfma_f32_16x16x32_bf16 a[224:227],v[104:107],a[84:87],a[224:227]
s_add_u32 s75,s75,s60
ds_read_b64_tr_b16 v[90:91],v2 offset:25760
v_mul_f32 v135,v135,v53
s_waitcnt lgkmcnt(8)
v_mfma_f32_16x16x32_bf16 a[224:227],v[108:111],a[92:95],a[224:227]
s_mul_i32 s75,s75,16384
v_mul_f32 v140,v140,v53
v_mul_f32 v141,v141,v53
v_mfma_f32_16x16x32_bf16 a[228:231],v[108:111],a[124:127],0
s_add_u32 s75,s75,4096
v_mul_f32 v142,v142,v53
v_mul_f32 v143,v143,v53
v_mfma_f32_16x16x32_bf16 a[228:231],v[104:107],a[116:119],a[228:231]
s_add_u32 s76,s63,3
v_cvt_pk_bf16_f32 v132,v132,v133
v_cvt_pk_bf16_f32 v133,v134,v135
v_mfma_f32_16x16x32_bf16 a[228:231],v[100:103],a[108:111],a[228:231]
s_and_b32 s76,s76,3
v_cvt_pk_bf16_f32 v134,v140,v141
v_cvt_pk_bf16_f32 v135,v142,v143
v_mfma_f32_16x16x32_bf16 a[228:231],v[96:99],a[100:103],a[228:231]
s_lshl_b32 s76,s76,2
v_mul_f32 v148,v148,v53
v_mul_f32 v149,v149,v53
v_mfma_f32_16x16x32_bf16 a[232:235],v[96:99],a[64:67],0
s_add_u32 s76,s76,s60
v_mul_f32 v150,v150,v53
v_mul_f32 v151,v151,v53
v_mfma_f32_16x16x32_bf16 a[232:235],v[100:103],a[72:75],a[232:235]
s_mul_i32 s76,s76,16384
v_mul_f32 v156,v156,v53
v_mul_f32 v157,v157,v53
v_mfma_f32_16x16x32_bf16 a[232:235],v[104:107],a[80:83],a[232:235]
s_add_u32 s76,s76,0
v_mul_f32 v158,v158,v53
v_mul_f32 v159,v159,v53
v_mfma_f32_16x16x32_bf16 a[232:235],v[108:111],a[88:91],a[232:235]
s_lshl_b32 s67,s63,10
v_cvt_pk_bf16_f32 v148,v148,v149
v_cvt_pk_bf16_f32 v149,v150,v151
v_mfma_f32_16x16x32_bf16 a[236:239],v[108:111],a[120:123],0
s_mov_b32 s26,307200
ds_read_b64_tr_b16 v[108:109],v1 offset:30912
ds_read_b64_tr_b16 v[110:111],v1 offset:36032
v_mfma_f32_16x16x32_bf16 a[236:239],v[104:107],a[112:115],a[236:239]
buffer_store_dwordx4 v[44:47],v6,s[40:43],s82 offen sc1
v_mfma_f32_16x16x32_bf16 a[236:239],v[100:103],a[104:107],a[236:239]
s_mov_b32 s27,0x20000
ds_read_b64_tr_b16 v[100:101],v1 offset:10432
ds_read_b64_tr_b16 v[102:103],v1 offset:15552
v_mfma_f32_16x16x32_bf16 a[236:239],v[96:99],a[96:99],a[236:239]
s_lshl_b32 s77,s63,2
ds_read_b64_tr_b16 v[96:97],v1 offset:192
v_cvt_pk_bf16_f32 v150,v156,v157
v_mfma_f32_16x16x32_bf16 a[240:243],v[112:115],a[68:71],0
s_add_u32 s77,s77,s60
ds_read_b64_tr_b16 v[98:99],v1 offset:5312
v_cvt_pk_bf16_f32 v151,v158,v159
v_mfma_f32_16x16x32_bf16 a[240:243],v[116:119],a[76:79],a[240:243]
s_mul_i32 s77,s77,16384
ds_read_b64_tr_b16 v[104:105],v1 offset:20672
v_mul_f32 v164,v164,v53
v_mfma_f32_16x16x32_bf16 a[240:243],v[120:123],a[84:87],a[240:243]
s_add_u32 s65,s77,0
ds_read_b64_tr_b16 v[106:107],v1 offset:25792
v_mul_f32 v165,v165,v53
v_mfma_f32_16x16x32_bf16 a[240:243],v[124:127],a[92:95],a[240:243]
s_add_u32 s68,s77,12288
v_mul_f32 v166,v166,v53
v_mul_f32 v167,v167,v53
v_mfma_f32_16x16x32_bf16 a[244:247],v[124:127],a[124:127],0
v_mul_f32 v172,v172,v53
v_mul_f32 v173,v173,v53
v_mfma_f32_16x16x32_bf16 a[244:247],v[120:123],a[116:119],a[244:247]
v_mul_f32 v174,v174,v53
v_mul_f32 v175,v175,v53
v_mfma_f32_16x16x32_bf16 a[244:247],v[116:119],a[108:111],a[244:247]
v_cvt_pk_bf16_f32 v164,v164,v165
v_cvt_pk_bf16_f32 v165,v166,v167
v_mfma_f32_16x16x32_bf16 a[244:247],v[112:115],a[100:103],a[244:247]
v_cvt_pk_bf16_f32 v166,v172,v173
v_cvt_pk_bf16_f32 v167,v174,v175
v_mfma_f32_16x16x32_bf16 a[248:251],v[112:115],a[64:67],0
v_mul_f32 v180,v180,v53
v_mul_f32 v181,v181,v53
v_mfma_f32_16x16x32_bf16 a[248:251],v[116:119],a[72:75],a[248:251]
v_mul_f32 v182,v182,v53
v_mul_f32 v183,v183,v53
v_mfma_f32_16x16x32_bf16 a[248:251],v[120:123],a[80:83],a[248:251]
v_mul_f32 v188,v188,v53
v_mul_f32 v189,v189,v53
v_mfma_f32_16x16x32_bf16 a[248:251],v[124:127],a[88:91],a[248:251]
v_mul_f32 v190,v190,v53
v_mul_f32 v191,v191,v53
v_mfma_f32_16x16x32_bf16 a[252:255],v[124:127],a[120:123],0
buffer_load_dword v30,v29,s[8:11],s7 offen
v_mfma_f32_16x16x32_bf16 a[252:255],v[120:123],a[112:115],a[252:255]
ds_read_b64_tr_b16 v[120:121],v2 offset:20704
ds_read_b64_tr_b16 v[122:123],v2 offset:25824
v_mfma_f32_16x16x32_bf16 a[252:255],v[116:119],a[104:107],a[252:255]
buffer_store_dwordx4 v[132:135],v6,s[40:43],s79 offen offset:1024 sc1
v_mfma_f32_16x16x32_bf16 a[252:255],v[112:115],a[96:99],a[252:255]
ds_read_b64_tr_b16 v[112:113],v2 offset:224
ds_read_b64_tr_b16 v[114:115],v2 offset:5344
v_mfma_f32_16x16x32_bf16 a[0:3],v[64:67],a[68:71],0
ds_read_b64_tr_b16 v[116:117],v2 offset:10464
v_cvt_pk_bf16_f32 v180,v180,v181
v_mfma_f32_16x16x32_bf16 a[0:3],v[68:71],a[76:79],a[0:3]
ds_read_b64_tr_b16 v[118:119],v2 offset:15584
v_cvt_pk_bf16_f32 v181,v182,v183
v_mfma_f32_16x16x32_bf16 a[0:3],v[72:75],a[84:87],a[0:3]
ds_read_b64_tr_b16 v[124:125],v2 offset:30944
v_cvt_pk_bf16_f32 v182,v188,v189
v_mfma_f32_16x16x32_bf16 a[0:3],v[76:79],a[92:95],a[0:3]
ds_read_b64_tr_b16 v[126:127],v2 offset:36064
v_cvt_pk_bf16_f32 v183,v190,v191
v_mfma_f32_16x16x32_bf16 a[4:7],v[76:79],a[124:127],0
v_mul_f32 v196,v196,v57
v_mul_f32 v197,v197,v57
v_mfma_f32_16x16x32_bf16 a[4:7],v[72:75],a[116:119],a[4:7]
v_mul_f32 v198,v198,v57
v_mul_f32 v199,v199,v57
v_mfma_f32_16x16x32_bf16 a[4:7],v[68:71],a[108:111],a[4:7]
v_mul_f32 v204,v204,v57
v_mul_f32 v205,v205,v57
v_mfma_f32_16x16x32_bf16 a[4:7],v[64:67],a[100:103],a[4:7]
v_mul_f32 v206,v206,v57
v_mul_f32 v207,v207,v57
v_mfma_f32_16x16x32_bf16 a[8:11],v[64:67],a[64:67],0
v_cvt_pk_bf16_f32 v196,v196,v197
v_cvt_pk_bf16_f32 v197,v198,v199
v_mfma_f32_16x16x32_bf16 a[8:11],v[68:71],a[72:75],a[8:11]
v_cvt_pk_bf16_f32 v198,v204,v205
v_cvt_pk_bf16_f32 v199,v206,v207
v_mfma_f32_16x16x32_bf16 a[8:11],v[72:75],a[80:83],a[8:11]
v_mul_f32 v212,v212,v57
v_mul_f32 v213,v213,v57
v_mfma_f32_16x16x32_bf16 a[8:11],v[76:79],a[88:91],a[8:11]
v_mul_f32 v214,v214,v57
v_mul_f32 v215,v215,v57
v_mfma_f32_16x16x32_bf16 a[12:15],v[76:79],a[120:123],0
ds_read_b64_tr_b16 v[76:77],v1 offset:30976
ds_read_b64_tr_b16 v[78:79],v1 offset:36096
v_mfma_f32_16x16x32_bf16 a[12:15],v[72:75],a[112:115],a[12:15]
buffer_store_dwordx4 v[148:151],v6,s[40:43],s80 offen offset:1024 sc1
v_mfma_f32_16x16x32_bf16 a[12:15],v[68:71],a[104:107],a[12:15]
ds_read_b64_tr_b16 v[68:69],v1 offset:10496
ds_read_b64_tr_b16 v[70:71],v1 offset:15616
v_mfma_f32_16x16x32_bf16 a[12:15],v[64:67],a[96:99],a[12:15]
s_waitcnt lgkmcnt(4)
ds_read_b64_tr_b16 v[64:65],v1 offset:256
v_mul_f32 v220,v220,v57
v_mfma_f32_16x16x32_bf16 a[16:19],v[80:83],a[68:71],0
ds_read_b64_tr_b16 v[66:67],v1 offset:5376
v_mul_f32 v221,v221,v57
v_mfma_f32_16x16x32_bf16 a[16:19],v[84:87],a[76:79],a[16:19]
ds_read_b64_tr_b16 v[72:73],v1 offset:20736
v_mul_f32 v222,v222,v57
v_mfma_f32_16x16x32_bf16 a[16:19],v[88:91],a[84:87],a[16:19]
ds_read_b64_tr_b16 v[74:75],v1 offset:25856
v_add_u32 v1,0xffff6000,v1
v_mfma_f32_16x16x32_bf16 a[16:19],v[92:95],a[92:95],a[16:19]
v_mul_f32 v223,v223,v57
v_cvt_pk_bf16_f32 v212,v212,v213
v_mfma_f32_16x16x32_bf16 a[20:23],v[92:95],a[124:127],0
v_cvt_pk_bf16_f32 v213,v214,v215
v_cvt_pk_bf16_f32 v214,v220,v221
v_mfma_f32_16x16x32_bf16 a[20:23],v[88:91],a[116:119],a[20:23]
v_cvt_pk_bf16_f32 v215,v222,v223
v_mul_f32 v228,v228,v57
v_mfma_f32_16x16x32_bf16 a[20:23],v[84:87],a[108:111],a[20:23]
v_mul_f32 v229,v229,v57
v_mul_f32 v230,v230,v57
v_mfma_f32_16x16x32_bf16 a[20:23],v[80:83],a[100:103],a[20:23]
v_mul_f32 v231,v231,v57
v_mul_f32 v236,v236,v57
v_mfma_f32_16x16x32_bf16 a[24:27],v[80:83],a[64:67],0
v_mul_f32 v237,v237,v57
v_mul_f32 v238,v238,v57
v_mfma_f32_16x16x32_bf16 a[24:27],v[84:87],a[72:75],a[24:27]
v_mul_f32 v239,v239,v57
v_cvt_pk_bf16_f32 v228,v228,v229
v_mfma_f32_16x16x32_bf16 a[24:27],v[88:91],a[80:83],a[24:27]
s_waitcnt vmcnt(5)
v_cmp_ne_u32 s[56:57],s52,v4
v_cmp_ne_u32 s[58:59],s53,v5
v_mfma_f32_16x16x32_bf16 a[24:27],v[92:95],a[88:91],a[24:27]
s_or_b64 s[56:57],s[56:57],s[58:59]
s_cbranch_scc1 L_fr1_%=
L_fo1_%=:
v_cvt_pk_bf16_f32 v229,v230,v231
v_mfma_f32_16x16x32_bf16 a[28:31],v[92:95],a[120:123],0
buffer_store_dwordx4 v[164:167],v6,s[40:43],s81 offen offset:1024 sc1
v_mfma_f32_16x16x32_bf16 a[28:31],v[88:91],a[112:115],a[28:31]
s_lshl_b32 s91,s63,8
ds_read_b64_tr_b16 v[88:89],v2 offset:20768
ds_read_b64_tr_b16 v[90:91],v2 offset:25888
v_mfma_f32_16x16x32_bf16 a[28:31],v[84:87],a[104:107],a[28:31]
buffer_load_dwordx4 a[128:131],v6,s[28:31],s87 offen sc1
v_mfma_f32_16x16x32_bf16 a[28:31],v[80:83],a[96:99],a[28:31]
ds_read_b64_tr_b16 v[80:81],v2 offset:288
)A"
R"A(ds_read_b64_tr_b16 v[82:83],v2 offset:5408
v_mfma_f32_16x16x32_bf16 v[128:131],v[96:99],a[68:71],0
ds_read_b64_tr_b16 v[84:85],v2 offset:10528
v_cvt_pk_bf16_f32 v230,v236,v237
v_mfma_f32_16x16x32_bf16 v[128:131],v[100:103],a[76:79],v[128:131]
ds_read_b64_tr_b16 v[86:87],v2 offset:15648
v_cvt_pk_bf16_f32 v231,v238,v239
v_mfma_f32_16x16x32_bf16 v[128:131],v[104:107],a[84:87],v[128:131]
ds_read_b64_tr_b16 v[92:93],v2 offset:31008
v_mul_f32 v244,v244,v57
v_mfma_f32_16x16x32_bf16 v[128:131],v[108:111],a[92:95],v[128:131]
ds_read_b64_tr_b16 v[94:95],v2 offset:36128
v_add_u32 v2,0xffff6000,v2
v_mfma_f32_16x16x32_bf16 v[136:139],v[108:111],a[124:127],0
v_mul_f32 v245,v245,v57
v_mul_f32 v246,v246,v57
v_mfma_f32_16x16x32_bf16 v[136:139],v[104:107],a[116:119],v[136:139]
v_mul_f32 v247,v247,v57
v_mul_f32 v252,v252,v57
v_mfma_f32_16x16x32_bf16 v[136:139],v[100:103],a[108:111],v[136:139]
v_mul_f32 v253,v253,v57
v_mul_f32 v254,v254,v57
v_mfma_f32_16x16x32_bf16 v[136:139],v[96:99],a[100:103],v[136:139]
v_mul_f32 v255,v255,v57
v_cvt_pk_bf16_f32 v244,v244,v245
v_mfma_f32_16x16x32_bf16 v[144:147],v[96:99],a[64:67],0
v_cvt_pk_bf16_f32 v245,v246,v247
v_cvt_pk_bf16_f32 v246,v252,v253
v_mfma_f32_16x16x32_bf16 v[144:147],v[100:103],a[72:75],v[144:147]
buffer_load_dwordx4 a[144:147],v6,s[28:31],s88 offen sc1
v_mfma_f32_16x16x32_bf16 v[144:147],v[104:107],a[80:83],v[144:147]
v_cvt_pk_bf16_f32 v247,v254,v255
v_accvgpr_read_b32 v8,a40
v_mfma_f32_16x16x32_bf16 v[144:147],v[108:111],a[88:91],v[144:147]
buffer_store_dwordx4 v[180:183],v6,s[40:43],s82 offen offset:1024 sc1
v_mfma_f32_16x16x32_bf16 v[152:155],v[108:111],a[120:123],0
ds_read_b64_tr_b16 v[108:109],v1 offset:30720
ds_read_b64_tr_b16 v[110:111],v1 offset:35840
v_mfma_f32_16x16x32_bf16 v[152:155],v[104:107],a[112:115],v[152:155]
ds_read_b64_tr_b16 v[104:105],v1 offset:20480
v_accvgpr_read_b32 v9,a41
v_mfma_f32_16x16x32_bf16 v[152:155],v[100:103],a[104:107],v[152:155]
ds_read_b64_tr_b16 v[100:101],v1 offset:10240
v_accvgpr_read_b32 v10,a42
v_mfma_f32_16x16x32_bf16 v[152:155],v[96:99],a[96:99],v[152:155]
ds_read_b64_tr_b16 v[96:97],v1
v_accvgpr_read_b32 v11,a43
v_mfma_f32_16x16x32_bf16 v[160:163],v[112:115],a[68:71],0
ds_read_b64_tr_b16 v[98:99],v1 offset:5120
ds_read_b64_tr_b16 v[102:103],v1 offset:15360
v_mfma_f32_16x16x32_bf16 v[160:163],v[116:119],a[76:79],v[160:163]
ds_read_b64_tr_b16 v[106:107],v1 offset:25600
v_accvgpr_read_b32 v12,a44
v_mfma_f32_16x16x32_bf16 v[160:163],v[120:123],a[84:87],v[160:163]
v_accvgpr_read_b32 v13,a45
v_accvgpr_read_b32 v14,a46
v_mfma_f32_16x16x32_bf16 v[160:163],v[124:127],a[92:95],v[160:163]
v_accvgpr_read_b32 v15,a47
v_mul_f32 v8,v8,v61
v_mfma_f32_16x16x32_bf16 v[168:171],v[124:127],a[124:127],0
v_mul_f32 v9,v9,v61
v_mul_f32 v10,v10,v61
v_mfma_f32_16x16x32_bf16 v[168:171],v[120:123],a[116:119],v[168:171]
v_mul_f32 v11,v11,v61
v_mul_f32 v12,v12,v61
v_mfma_f32_16x16x32_bf16 v[168:171],v[116:119],a[108:111],v[168:171]
buffer_load_dwordx4 a[160:163],v6,s[28:31],s89 offen sc1
v_mfma_f32_16x16x32_bf16 v[168:171],v[112:115],a[100:103],v[168:171]
v_mul_f32 v13,v13,v61
v_mul_f32 v14,v14,v61
v_mfma_f32_16x16x32_bf16 v[176:179],v[112:115],a[64:67],0
v_mul_f32 v15,v15,v61
v_cvt_pk_bf16_f32 v156,v8,v9
v_mfma_f32_16x16x32_bf16 v[176:179],v[116:119],a[72:75],v[176:179]
v_cvt_pk_bf16_f32 v157,v10,v11
v_cvt_pk_bf16_f32 v158,v12,v13
v_mfma_f32_16x16x32_bf16 v[176:179],v[120:123],a[80:83],v[176:179]
buffer_store_dwordx4 v[196:199],v6,s[40:43],s79 offen offset:2048 sc1
v_mfma_f32_16x16x32_bf16 v[176:179],v[124:127],a[88:91],v[176:179]
v_cvt_pk_bf16_f32 v159,v14,v15
v_accvgpr_read_b32 v8,a56
v_mfma_f32_16x16x32_bf16 v[184:187],v[124:127],a[120:123],0
v_accvgpr_read_b32 v9,a57
v_accvgpr_read_b32 v10,a58
v_mfma_f32_16x16x32_bf16 v[184:187],v[120:123],a[112:115],v[184:187]
ds_read_b64_tr_b16 v[120:121],v2 offset:20512
ds_read_b64_tr_b16 v[122:123],v2 offset:25632
v_mfma_f32_16x16x32_bf16 v[184:187],v[116:119],a[104:107],v[184:187]
ds_read_b64_tr_b16 v[116:117],v2 offset:10272
v_accvgpr_read_b32 v11,a59
v_mfma_f32_16x16x32_bf16 v[184:187],v[112:115],a[96:99],v[184:187]
s_waitcnt lgkmcnt(3)
ds_read_b64_tr_b16 v[112:113],v2 offset:32
v_accvgpr_read_b32 v12,a60
v_mfma_f32_16x16x32_bf16 v[192:195],v[64:67],a[68:71],0
ds_read_b64_tr_b16 v[114:115],v2 offset:5152
v_accvgpr_read_b32 v13,a61
v_mfma_f32_16x16x32_bf16 v[192:195],v[68:71],a[76:79],v[192:195]
ds_read_b64_tr_b16 v[118:119],v2 offset:15392
v_accvgpr_read_b32 v14,a62
v_mfma_f32_16x16x32_bf16 v[192:195],v[72:75],a[84:87],v[192:195]
v_accvgpr_read_b32 v15,a63
v_mul_f32 v8,v8,v61
v_mfma_f32_16x16x32_bf16 v[192:195],v[76:79],a[92:95],v[192:195]
buffer_load_dwordx4 a[176:179],v6,s[28:31],s90 offen sc1
v_mfma_f32_16x16x32_bf16 v[200:203],v[76:79],a[124:127],0
v_mul_f32 v9,v9,v61
v_mul_f32 v10,v10,v61
v_mfma_f32_16x16x32_bf16 v[200:203],v[72:75],a[116:119],v[200:203]
v_mul_f32 v11,v11,v61
v_mul_f32 v12,v12,v61
v_mfma_f32_16x16x32_bf16 v[200:203],v[68:71],a[108:111],v[200:203]
v_mul_f32 v13,v13,v61
v_mul_f32 v14,v14,v61
v_mfma_f32_16x16x32_bf16 v[200:203],v[64:67],a[100:103],v[200:203]
v_mul_f32 v15,v15,v61
v_cvt_pk_bf16_f32 v188,v8,v9
v_mfma_f32_16x16x32_bf16 v[208:211],v[64:67],a[64:67],0
v_cvt_pk_bf16_f32 v189,v10,v11
v_cvt_pk_bf16_f32 v190,v12,v13
v_mfma_f32_16x16x32_bf16 v[208:211],v[68:71],a[72:75],v[208:211]
buffer_store_dwordx4 v[212:215],v6,s[40:43],s80 offen offset:2048 sc1
v_mfma_f32_16x16x32_bf16 v[208:211],v[72:75],a[80:83],v[208:211]
ds_read_b64_tr_b16 v[124:125],v2 offset:30752
v_cvt_pk_bf16_f32 v191,v14,v15
v_mfma_f32_16x16x32_bf16 v[208:211],v[76:79],a[88:91],v[208:211]
ds_read_b64_tr_b16 v[126:127],v2 offset:35872
v_accvgpr_read_b32 v16,a32
v_mfma_f32_16x16x32_bf16 v[216:219],v[76:79],a[120:123],0
v_accvgpr_read_b32 v17,a33
v_accvgpr_read_b32 v18,a34
v_mfma_f32_16x16x32_bf16 v[216:219],v[72:75],a[112:115],v[216:219]
v_accvgpr_read_b32 v19,a35
v_accvgpr_read_b32 v20,a36
v_mfma_f32_16x16x32_bf16 v[216:219],v[68:71],a[104:107],v[216:219]
v_accvgpr_read_b32 v21,a37
v_accvgpr_read_b32 v22,a38
v_mfma_f32_16x16x32_bf16 v[216:219],v[64:67],a[96:99],v[216:219]
v_accvgpr_read_b32 v23,a39
v_mul_f32 v16,v16,v61
v_mfma_f32_16x16x32_bf16 v[224:227],v[80:83],a[68:71],0
buffer_load_dwordx4 a[180:183],v6,s[28:31],s90 offen offset:1024 sc1
v_mfma_f32_16x16x32_bf16 v[224:227],v[84:87],a[76:79],v[224:227]
v_mul_f32 v17,v17,v61
v_mul_f32 v18,v18,v61
v_mfma_f32_16x16x32_bf16 v[224:227],v[88:91],a[84:87],v[224:227]
v_mul_f32 v19,v19,v61
v_mul_f32 v20,v20,v61
v_mfma_f32_16x16x32_bf16 v[224:227],v[92:95],a[92:95],v[224:227]
v_mul_f32 v21,v21,v61
v_mul_f32 v22,v22,v61
v_mfma_f32_16x16x32_bf16 v[232:235],v[92:95],a[124:127],0
v_mul_f32 v23,v23,v61
v_cvt_pk_bf16_f32 v140,v16,v17
v_mfma_f32_16x16x32_bf16 v[232:235],v[88:91],a[116:119],v[232:235]
v_cvt_pk_bf16_f32 v141,v18,v19
v_cvt_pk_bf16_f32 v142,v20,v21
v_mfma_f32_16x16x32_bf16 v[232:235],v[84:87],a[108:111],v[232:235]
v_cvt_pk_bf16_f32 v143,v22,v23
v_accvgpr_read_b32 v16,a48
v_mfma_f32_16x16x32_bf16 v[232:235],v[80:83],a[100:103],v[232:235]
v_accvgpr_read_b32 v17,a49
v_accvgpr_read_b32 v18,a50
v_mfma_f32_16x16x32_bf16 v[240:243],v[80:83],a[64:67],0
buffer_store_dwordx4 v[228:231],v6,s[40:43],s81 offen offset:2048 sc1
v_mfma_f32_16x16x32_bf16 v[240:243],v[84:87],a[72:75],v[240:243]
v_accvgpr_read_b32 v19,a51
v_accvgpr_read_b32 v20,a52
v_mfma_f32_16x16x32_bf16 v[240:243],v[88:91],a[80:83],v[240:243]
v_accvgpr_read_b32 v21,a53
v_accvgpr_read_b32 v22,a54
v_mfma_f32_16x16x32_bf16 v[240:243],v[92:95],a[88:91],v[240:243]
v_accvgpr_read_b32 v23,a55
v_mul_f32 v16,v16,v61
v_mfma_f32_16x16x32_bf16 v[248:251],v[92:95],a[120:123],0
v_mul_f32 v17,v17,v61
v_mul_f32 v18,v18,v61
v_mfma_f32_16x16x32_bf16 v[248:251],v[88:91],a[112:115],v[248:251]
buffer_load_dwordx4 a[164:167],v6,s[28:31],s89 offen offset:1024 sc1
v_mfma_f32_16x16x32_bf16 v[248:251],v[84:87],a[104:107],v[248:251]
v_mul_f32 v19,v19,v61
v_mul_f32 v20,v20,v61
v_mfma_f32_16x16x32_bf16 v[248:251],v[80:83],a[96:99],v[248:251]
v_mul_f32 v21,v21,v61
v_mul_f32 v22,v22,v61
v_mfma_f32_16x16x32_bf16 a[192:195],v[96:99],v[32:35],a[192:195]
v_mul_f32 v23,v23,v61
v_cvt_pk_bf16_f32 v172,v16,v17
v_mfma_f32_16x16x32_bf16 a[192:195],v[100:103],v[36:39],a[192:195]
v_cvt_pk_bf16_f32 v173,v18,v19
v_cvt_pk_bf16_f32 v174,v20,v21
v_mfma_f32_16x16x32_bf16 a[192:195],v[104:107],v[40:43],a[192:195]
ds_read_b64_tr_b16 v[64:65],v1 offset:64
v_cvt_pk_bf16_f32 v175,v22,v23
v_mfma_f32_16x16x32_bf16 a[192:195],v[108:111],v[44:47],a[192:195]
ds_read_b64_tr_b16 v[66:67],v1 offset:5184
ds_read_b64_tr_b16 v[68:69],v1 offset:10304
v_mfma_f32_16x16x32_bf16 a[196:199],v[108:111],v[180:183],a[196:199]
s_cmp_eq_u32 s60,3
s_cselect_b32 s69,0x40000000,0
ds_read_b64_tr_b16 v[70:71],v1 offset:15424
v_mfma_f32_16x16x32_bf16 a[196:199],v[104:107],v[164:167],a[196:199]
ds_read_b64_tr_b16 v[72:73],v1 offset:20544
v_add_u32 v28,s69,v6
v_mfma_f32_16x16x32_bf16 a[196:199],v[100:103],v[148:151],a[196:199]
s_lshl_b32 s69,s63,2
ds_read_b64_tr_b16 v[74:75],v1 offset:25664
v_mov_b32 v8,s52
v_mfma_f32_16x16x32_bf16 a[196:199],v[96:99],v[132:135],a[196:199]
buffer_store_dwordx4 v[244:247],v6,s[40:43],s82 offen offset:2048 sc1
v_mfma_f32_16x16x32_bf16 a[200:203],v[96:99],v[196:199],a[200:203]
ds_read_b64_tr_b16 v[76:77],v1 offset:30784
ds_read_b64_tr_b16 v[78:79],v1 offset:35904
v_mfma_f32_16x16x32_bf16 a[200:203],v[100:103],v[212:215],a[200:203]
ds_read_b64_tr_b16 v[80:81],v2 offset:96
v_mov_b32 v9,s53
v_mfma_f32_16x16x32_bf16 a[200:203],v[104:107],v[228:231],a[200:203]
buffer_load_dwordx4 a[148:151],v6,s[28:31],s88 offen offset:1024 sc1
v_mfma_f32_16x16x32_bf16 a[200:203],v[108:111],v[244:247],a[200:203]
ds_read_b64_tr_b16 v[82:83],v2 offset:5216
ds_read_b64_tr_b16 v[84:85],v2 offset:10336
v_mfma_f32_16x16x32_bf16 a[204:207],v[108:111],v[188:191],a[204:207]
ds_read_b64_tr_b16 v[86:87],v2 offset:15456
v_lshlrev_b32 v10,20,v6
v_mfma_f32_16x16x32_bf16 a[204:207],v[104:107],v[172:175],a[204:207]
ds_read_b64_tr_b16 v[88:89],v2 offset:20576
v_bfe_u32 v3,v6,4,2
v_mfma_f32_16x16x32_bf16 a[204:207],v[100:103],v[156:159],a[204:207]
ds_read_b64_tr_b16 v[90:91],v2 offset:25696
v_min_u32 v3,2,v3
v_mfma_f32_16x16x32_bf16 a[204:207],v[96:99],v[140:143],a[204:207]
s_waitcnt lgkmcnt(8)
ds_read_b64_tr_b16 v[92:93],v2 offset:30816
ds_read_b64_tr_b16 v[94:95],v2 offset:35936
v_mfma_f32_16x16x32_bf16 a[208:211],v[112:115],v[32:35],a[208:211]
ds_read_b64_tr_b16 v[96:97],v1 offset:128
v_add3_u32 v3,v3,s64,1
v_mfma_f32_16x16x32_bf16 a[208:211],v[116:119],v[36:39],a[208:211]
ds_read_b64_tr_b16 v[98:99],v1 offset:5248
v_and_b32 v3,3,v3
v_mfma_f32_16x16x32_bf16 a[208:211],v[120:123],v[40:43],a[208:211]
ds_read_b64_tr_b16 v[100:101],v1 offset:10368
v_lshlrev_b32 v3,6,v3
v_mfma_f32_16x16x32_bf16 a[208:211],v[124:127],v[44:47],a[208:211]
ds_read_b64_tr_b16 v[102:103],v1 offset:15488
ds_read_b64_tr_b16 v[104:105],v1 offset:20608
v_mfma_f32_16x16x32_bf16 a[212:215],v[124:127],v[180:183],a[212:215]
ds_read_b64_tr_b16 v[106:107],v1 offset:25728
v_bfe_u32 v56,v6,6,2
v_mfma_f32_16x16x32_bf16 a[212:215],v[120:123],v[164:167],a[212:215]
ds_read_b64_tr_b16 v[108:109],v1 offset:30848
v_lshrrev_b32 v57,8,v6
v_mfma_f32_16x16x32_bf16 a[212:215],v[116:119],v[148:151],a[212:215]
buffer_store_dwordx4 v[140:143],v28,s[40:43],s79 offen offset:3072 sc1
v_mfma_f32_16x16x32_bf16 a[212:215],v[112:115],v[132:135],a[212:215]
ds_read_b64_tr_b16 v[110:111],v1 offset:35968
v_cmp_eq_u32 s[56:57],v56,v57
v_mfma_f32_16x16x32_bf16 a[216:219],v[112:115],v[196:199],a[216:219]
buffer_load_dwordx4 a[132:135],v6,s[28:31],s87 offen offset:1024 sc1
v_mfma_f32_16x16x32_bf16 a[216:219],v[116:119],v[212:215],a[216:219]
v_bfe_u32 v56,v6,4,1
v_lshlrev_b32 v56,4,v56
v_mfma_f32_16x16x32_bf16 a[216:219],v[120:123],v[228:231],a[216:219]
v_mov_b32 v57,0x3f80
v_lshlrev_b32 v56,v56,v57
v_mfma_f32_16x16x32_bf16 a[216:219],v[124:127],v[244:247],a[216:219]
v_cndmask_b32 v56,0,v56,s[56:57]
v_bfe_u32 v57,v6,5,1
v_mfma_f32_16x16x32_bf16 a[220:223],v[124:127],v[188:191],a[220:223]
ds_read_b64_tr_b16 v[124:125],v2 offset:30880
ds_read_b64_tr_b16 v[126:127],v2 offset:36000
)A"
R"A(v_mfma_f32_16x16x32_bf16 a[220:223],v[120:123],v[172:175],a[220:223]
ds_read_b64_tr_b16 v[120:121],v2 offset:20640
v_cmp_eq_u32 s[56:57],0,v57
v_mfma_f32_16x16x32_bf16 a[220:223],v[116:119],v[156:159],a[220:223]
s_add_u32 s36,s12,s66
s_addc_u32 s37,s13,0
ds_read_b64_tr_b16 v[116:117],v2 offset:10400
v_mfma_f32_16x16x32_bf16 a[220:223],v[112:115],v[140:143],a[220:223]
s_and_b32 s37,s37,0xffff
ds_read_b64_tr_b16 v[112:113],v2 offset:160
v_cndmask_b32 v48,0,v56,s[56:57]
v_mfma_f32_16x16x32_bf16 a[224:227],v[64:67],v[32:35],a[224:227]
s_mul_i32 s66,s61,307200
ds_read_b64_tr_b16 v[114:115],v2 offset:5280
ds_read_b64_tr_b16 v[118:119],v2 offset:15520
v_mfma_f32_16x16x32_bf16 a[224:227],v[68:71],v[36:39],a[224:227]
ds_read_b64_tr_b16 v[122:123],v2 offset:25760
v_cndmask_b32 v49,v56,0,s[56:57]
v_mfma_f32_16x16x32_bf16 a[224:227],v[72:75],v[40:43],a[224:227]
s_waitcnt lgkmcnt(8)
v_mov_b32 v58,s52
v_mov_b32 v59,s53
v_mfma_f32_16x16x32_bf16 a[224:227],v[76:79],v[44:47],a[224:227]
s_add_u32 s24,s16,s66
s_addc_u32 s25,s17,0
v_lshlrev_b32 v60,20,v6
v_mfma_f32_16x16x32_bf16 a[228:231],v[76:79],v[180:183],a[228:231]
buffer_load_dwordx4 a[136:139],v6,s[28:31],s87 offen offset:2048 sc1
v_mfma_f32_16x16x32_bf16 a[228:231],v[72:75],v[164:167],a[228:231]
s_mul_i32 s66,s60,16384
s_add_u32 s66,s66,s67
s_add_u32 s79,s66,12288
v_mfma_f32_16x16x32_bf16 a[228:231],v[68:71],v[148:151],a[228:231]
buffer_store_dwordx4 v[156:159],v28,s[40:43],s80 offen offset:3072 sc1
v_mfma_f32_16x16x32_bf16 a[228:231],v[64:67],v[132:135],a[228:231]
s_add_u32 s80,s66,77824
v_bfe_u32 v61,v6,4,1
v_add3_u32 v61,v61,s63,2
v_mfma_f32_16x16x32_bf16 a[232:235],v[64:67],v[196:199],a[232:235]
s_and_b32 s25,s25,0xffff
v_and_b32 v61,3,v61
v_lshlrev_b32 v61,8,v61
v_mfma_f32_16x16x32_bf16 a[232:235],v[68:71],v[212:215],a[232:235]
s_add_u32 s67,s77,8192
v_mov_b32 v50,0
v_mov_b32 v51,0
v_mfma_f32_16x16x32_bf16 a[232:235],v[72:75],v[228:231],a[232:235]
v_mov_b32 v52,0
v_mov_b32 v53,0
v_mfma_f32_16x16x32_bf16 a[232:235],v[76:79],v[244:247],a[232:235]
v_mov_b32 v54,v48
v_mov_b32 v55,v49
v_mfma_f32_16x16x32_bf16 a[236:239],v[76:79],v[188:191],a[236:239]
ds_read_b64_tr_b16 v[76:77],v1 offset:30912
ds_read_b64_tr_b16 v[78:79],v1 offset:36032
v_mfma_f32_16x16x32_bf16 a[236:239],v[72:75],v[172:175],a[236:239]
ds_read_b64_tr_b16 v[72:73],v1 offset:20672
v_bfe_u32 v56,v6,4,4
v_mfma_f32_16x16x32_bf16 a[236:239],v[68:71],v[156:159],a[236:239]
ds_read_b64_tr_b16 v[68:69],v1 offset:10432
v_mul_u32_u24 v56,1280,v56
v_mfma_f32_16x16x32_bf16 a[236:239],v[64:67],v[140:143],a[236:239]
ds_read_b64_tr_b16 v[64:65],v1 offset:192
v_bfe_u32 v57,v6,8,1
v_mfma_f32_16x16x32_bf16 a[240:243],v[80:83],v[32:35],a[240:243]
ds_read_b64_tr_b16 v[66:67],v1 offset:5312
ds_read_b64_tr_b16 v[70:71],v1 offset:15552
v_mfma_f32_16x16x32_bf16 a[240:243],v[84:87],v[36:39],a[240:243]
buffer_load_dwordx4 a[152:155],v6,s[28:31],s88 offen offset:2048 sc1
v_mfma_f32_16x16x32_bf16 a[240:243],v[88:91],v[40:43],a[240:243]
ds_read_b64_tr_b16 v[74:75],v1 offset:25792
v_lshl_add_u32 v56,v57,5,v56
v_mfma_f32_16x16x32_bf16 a[240:243],v[92:95],v[44:47],a[240:243]
v_lshrrev_b32 v57,9,v6
v_lshl_add_u32 v56,v57,4,v56
v_mfma_f32_16x16x32_bf16 a[244:247],v[92:95],v[180:183],a[244:247]
v_mfma_f32_16x16x32_bf16 a[244:247],v[88:91],v[164:167],a[244:247]
buffer_store_dwordx4 v[172:175],v28,s[40:43],s81 offen offset:3072 sc1
v_mfma_f32_16x16x32_bf16 a[244:247],v[84:87],v[148:151],a[244:247]
s_nop 0
L_pd14_%=:
s_cbranch_execz L_trt1_%=
s_add_u32 s81,s66,143360
v_mfma_f32_16x16x32_bf16 a[244:247],v[80:83],v[132:135],a[244:247]
v_mfma_f32_16x16x32_bf16 a[248:251],v[80:83],v[196:199],a[248:251]
v_mfma_f32_16x16x32_bf16 a[248:251],v[84:87],v[212:215],a[248:251]
v_mfma_f32_16x16x32_bf16 a[248:251],v[88:91],v[228:231],a[248:251]
v_mfma_f32_16x16x32_bf16 a[248:251],v[92:95],v[244:247],a[248:251]
v_mfma_f32_16x16x32_bf16 a[252:255],v[92:95],v[188:191],a[252:255]
ds_read_b64_tr_b16 v[92:93],v2 offset:30944
ds_read_b64_tr_b16 v[94:95],v2 offset:36064
v_mfma_f32_16x16x32_bf16 a[252:255],v[88:91],v[172:175],a[252:255]
ds_read_b64_tr_b16 v[88:89],v2 offset:20704
v_mfma_f32_16x16x32_bf16 a[252:255],v[84:87],v[156:159],a[252:255]
buffer_load_dwordx4 a[168:171],v6,s[28:31],s89 offen offset:2048 sc1
v_mfma_f32_16x16x32_bf16 a[252:255],v[80:83],v[140:143],a[252:255]
ds_read_b64_tr_b16 v[80:81],v2 offset:224
ds_read_b64_tr_b16 v[82:83],v2 offset:5344
v_mfma_f32_16x16x32_bf16 a[0:3],v[96:99],v[32:35],a[0:3]
ds_read_b64_tr_b16 v[84:85],v2 offset:10464
v_mfma_f32_16x16x32_bf16 a[0:3],v[100:103],v[36:39],a[0:3]
ds_read_b64_tr_b16 v[86:87],v2 offset:15584
v_mfma_f32_16x16x32_bf16 a[0:3],v[104:107],v[40:43],a[0:3]
ds_read_b64_tr_b16 v[90:91],v2 offset:25824
v_mfma_f32_16x16x32_bf16 a[0:3],v[108:111],v[44:47],a[0:3]
v_mfma_f32_16x16x32_bf16 a[4:7],v[108:111],v[180:183],a[4:7]
buffer_store_dwordx4 v[188:191],v28,s[40:43],s82 offen offset:3072 sc1
v_mfma_f32_16x16x32_bf16 a[4:7],v[104:107],v[164:167],a[4:7]
s_add_u32 s82,s66,208896
s_mul_i32 s66,s64,320
v_mfma_f32_16x16x32_bf16 a[4:7],v[100:103],v[148:151],a[4:7]
v_mfma_f32_16x16x32_bf16 a[4:7],v[96:99],v[132:135],a[4:7]
v_mfma_f32_16x16x32_bf16 a[8:11],v[96:99],v[196:199],a[8:11]
v_mfma_f32_16x16x32_bf16 a[8:11],v[100:103],v[212:215],a[8:11]
v_mfma_f32_16x16x32_bf16 a[8:11],v[104:107],v[228:231],a[8:11]
v_mfma_f32_16x16x32_bf16 a[8:11],v[108:111],v[244:247],a[8:11]
buffer_load_dwordx4 a[184:187],v6,s[28:31],s90 offen offset:2048 sc1
v_mfma_f32_16x16x32_bf16 a[12:15],v[108:111],v[188:191],a[12:15]
ds_read_b64_tr_b16 v[108:109],v1 offset:30976
ds_read_b64_tr_b16 v[110:111],v1 offset:36096
v_mfma_f32_16x16x32_bf16 a[12:15],v[104:107],v[172:175],a[12:15]
ds_read_b64_tr_b16 v[104:105],v1 offset:20736
v_mfma_f32_16x16x32_bf16 a[12:15],v[100:103],v[156:159],a[12:15]
ds_read_b64_tr_b16 v[100:101],v1 offset:10496
v_mfma_f32_16x16x32_bf16 a[12:15],v[96:99],v[140:143],a[12:15]
s_waitcnt lgkmcnt(4)
ds_read_b64_tr_b16 v[96:97],v1 offset:256
v_mfma_f32_16x16x32_bf16 a[16:19],v[112:115],v[32:35],a[16:19]
ds_read_b64_tr_b16 v[98:99],v1 offset:5376
ds_read_b64_tr_b16 v[102:103],v1 offset:15616
v_mfma_f32_16x16x32_bf16 a[16:19],v[116:119],v[36:39],a[16:19]
ds_read_b64_tr_b16 v[106:107],v1 offset:25856
v_add_u32 v1,0xffff6000,v1
v_mfma_f32_16x16x32_bf16 a[16:19],v[120:123],v[40:43],a[16:19]
v_mfma_f32_16x16x32_bf16 a[16:19],v[124:127],v[44:47],a[16:19]
v_mfma_f32_16x16x32_bf16 a[20:23],v[124:127],v[180:183],a[20:23]
v_mfma_f32_16x16x32_bf16 a[20:23],v[120:123],v[164:167],a[20:23]
v_mfma_f32_16x16x32_bf16 a[20:23],v[116:119],v[148:151],a[20:23]
v_mfma_f32_16x16x32_bf16 a[20:23],v[112:115],v[132:135],a[20:23]
v_mfma_f32_16x16x32_bf16 a[24:27],v[112:115],v[196:199],a[24:27]
buffer_load_dwordx4 a[188:191],v28,s[28:31],s90 offen offset:3072 sc1
v_mfma_f32_16x16x32_bf16 a[24:27],v[116:119],v[212:215],a[24:27]
v_mfma_f32_16x16x32_bf16 a[24:27],v[120:123],v[228:231],a[24:27]
v_mfma_f32_16x16x32_bf16 a[24:27],v[124:127],v[244:247],a[24:27]
v_mfma_f32_16x16x32_bf16 a[28:31],v[124:127],v[188:191],a[28:31]
ds_read_b64_tr_b16 v[124:125],v2 offset:31008
ds_read_b64_tr_b16 v[126:127],v2 offset:36128
v_mfma_f32_16x16x32_bf16 a[28:31],v[120:123],v[172:175],a[28:31]
ds_read_b64_tr_b16 v[120:121],v2 offset:20768
v_mfma_f32_16x16x32_bf16 a[28:31],v[116:119],v[156:159],a[28:31]
ds_read_b64_tr_b16 v[116:117],v2 offset:10528
v_mfma_f32_16x16x32_bf16 a[28:31],v[112:115],v[140:143],a[28:31]
ds_read_b64_tr_b16 v[112:113],v2 offset:288
v_mfma_f32_16x16x32_bf16 v[128:131],v[64:67],v[32:35],v[128:131]
ds_read_b64_tr_b16 v[114:115],v2 offset:5408
ds_read_b64_tr_b16 v[118:119],v2 offset:15648
v_mfma_f32_16x16x32_bf16 v[128:131],v[68:71],v[36:39],v[128:131]
ds_read_b64_tr_b16 v[122:123],v2 offset:25888
v_add_u32 v2,0xffff6000,v2
v_mfma_f32_16x16x32_bf16 v[128:131],v[72:75],v[40:43],v[128:131]
v_mfma_f32_16x16x32_bf16 v[128:131],v[76:79],v[44:47],v[128:131]
v_mfma_f32_16x16x32_bf16 v[136:139],v[76:79],v[180:183],v[136:139]
v_mfma_f32_16x16x32_bf16 v[136:139],v[72:75],v[164:167],v[136:139]
buffer_load_dwordx4 a[172:175],v28,s[28:31],s89 offen offset:3072 sc1
v_mfma_f32_16x16x32_bf16 v[136:139],v[68:71],v[148:151],v[136:139]
v_mfma_f32_16x16x32_bf16 v[136:139],v[64:67],v[132:135],v[136:139]
v_mfma_f32_16x16x32_bf16 v[144:147],v[64:67],v[196:199],v[144:147]
v_mfma_f32_16x16x32_bf16 v[144:147],v[68:71],v[212:215],v[144:147]
v_mfma_f32_16x16x32_bf16 v[144:147],v[72:75],v[228:231],v[144:147]
v_mfma_f32_16x16x32_bf16 v[144:147],v[76:79],v[244:247],v[144:147]
v_mfma_f32_16x16x32_bf16 v[152:155],v[76:79],v[188:191],v[152:155]
v_mfma_f32_16x16x32_bf16 v[152:155],v[72:75],v[172:175],v[152:155]
v_mfma_f32_16x16x32_bf16 v[152:155],v[68:71],v[156:159],v[152:155]
v_mfma_f32_16x16x32_bf16 v[152:155],v[64:67],v[140:143],v[152:155]
s_mov_b64 exec,0
s_cmp_eq_u32 s60,0
s_cbranch_scc1 L_pd16_%=
s_cmp_eq_u32 s60,1
s_cbranch_scc1 L_pd17_%=
s_cmp_eq_u32 s60,2
s_cbranch_scc1 L_pd18_%=
s_branch L_trtg2F_%=
L_trtg2F_%=:
s_mov_b64 exec,-1
s_waitcnt lgkmcnt(0)
s_barrier
v_mfma_f32_16x16x32_bf16 v[160:163],v[80:83],v[32:35],v[160:163]
buffer_load_dwordx4 v24,s[48:51],s83 offen lds
v_mfma_f32_16x16x32_bf16 v[160:163],v[84:87],v[36:39],v[160:163]
ds_read_b64_tr_b16 v[64:65],v1
ds_read_b64_tr_b16 v[66:67],v1 offset:5120
v_mfma_f32_16x16x32_bf16 v[160:163],v[88:91],v[40:43],v[160:163]
ds_read_b64_tr_b16 v[68:69],v1 offset:10240
v_mfma_f32_16x16x32_bf16 v[160:163],v[92:95],v[44:47],v[160:163]
ds_read_b64_tr_b16 v[70:71],v1 offset:15360
v_mfma_f32_16x16x32_bf16 v[168:171],v[92:95],v[180:183],v[168:171]
ds_read_b64_tr_b16 v[72:73],v1 offset:20480
v_mfma_f32_16x16x32_bf16 v[168:171],v[88:91],v[164:167],v[168:171]
buffer_load_dwordx4 a[156:159],v28,s[28:31],s88 offen offset:3072 sc1
v_mfma_f32_16x16x32_bf16 v[168:171],v[84:87],v[148:151],v[168:171]
ds_read_b64_tr_b16 v[74:75],v1 offset:25600
ds_read_b64_tr_b16 v[76:77],v1 offset:30720
v_mfma_f32_16x16x32_bf16 v[168:171],v[80:83],v[132:135],v[168:171]
ds_read_b64_tr_b16 v[78:79],v1 offset:35840
v_mfma_f32_16x16x32_bf16 v[176:179],v[80:83],v[196:199],v[176:179]
v_mfma_f32_16x16x32_bf16 v[176:179],v[84:87],v[212:215],v[176:179]
v_mfma_f32_16x16x32_bf16 v[176:179],v[88:91],v[228:231],v[176:179]
v_mfma_f32_16x16x32_bf16 v[176:179],v[92:95],v[244:247],v[176:179]
buffer_load_dwordx4 v25,s[48:51],s83 offen offset:1024 lds
v_mfma_f32_16x16x32_bf16 v[184:187],v[92:95],v[188:191],v[184:187]
ds_read_b64_tr_b16 v[92:93],v2 offset:30752
ds_read_b64_tr_b16 v[94:95],v2 offset:35872
v_mfma_f32_16x16x32_bf16 v[184:187],v[88:91],v[172:175],v[184:187]
ds_read_b64_tr_b16 v[88:89],v2 offset:20512
v_mfma_f32_16x16x32_bf16 v[184:187],v[84:87],v[156:159],v[184:187]
ds_read_b64_tr_b16 v[84:85],v2 offset:10272
v_mfma_f32_16x16x32_bf16 v[184:187],v[80:83],v[140:143],v[184:187]
ds_read_b64_tr_b16 v[80:81],v2 offset:32
v_mfma_f32_16x16x32_bf16 v[192:195],v[96:99],v[32:35],v[192:195]
buffer_load_dwordx4 a[140:143],v28,s[28:31],s87 offen offset:3072 sc1
v_mfma_f32_16x16x32_bf16 v[192:195],v[100:103],v[36:39],v[192:195]
s_add_u32 s87,s64,2
s_and_b32 s87,s87,3
s_lshl_b32 s87,s87,1
v_mfma_f32_16x16x32_bf16 v[192:195],v[104:107],v[40:43],v[192:195]
s_lshl_b32 s87,s87,16
s_add_u32 s88,s87,4096
s_add_u32 s89,s87,8192
v_mfma_f32_16x16x32_bf16 v[192:195],v[108:111],v[44:47],v[192:195]
s_add_u32 s90,s87,12288
ds_read_b64_tr_b16 v[82:83],v2 offset:5152
ds_read_b64_tr_b16 v[86:87],v2 offset:15392
v_mfma_f32_16x16x32_bf16 v[200:203],v[108:111],v[180:183],v[200:203]
ds_read_b64_tr_b16 v[90:91],v2 offset:25632
v_mfma_f32_16x16x32_bf16 v[200:203],v[104:107],v[164:167],v[200:203]
buffer_load_dwordx4 a[68:71],v6,s[28:31],s87 offen sc1
v_mfma_f32_16x16x32_bf16 v[200:203],v[100:103],v[148:151],v[200:203]
v_mfma_f32_16x16x32_bf16 v[200:203],v[96:99],v[132:135],v[200:203]
v_mfma_f32_16x16x32_bf16 v[208:211],v[96:99],v[196:199],v[208:211]
v_mfma_f32_16x16x32_bf16 v[208:211],v[100:103],v[212:215],v[208:211]
v_mfma_f32_16x16x32_bf16 v[208:211],v[104:107],v[228:231],v[208:211]
buffer_load_dwordx4 a[76:79],v6,s[28:31],s88 offen sc1
v_mfma_f32_16x16x32_bf16 v[208:211],v[108:111],v[244:247],v[208:211]
)A"
R"A(v_mfma_f32_16x16x32_bf16 v[216:219],v[108:111],v[188:191],v[216:219]
ds_read_b64_tr_b16 v[108:109],v1 offset:30784
ds_read_b64_tr_b16 v[110:111],v1 offset:35904
v_mfma_f32_16x16x32_bf16 v[216:219],v[104:107],v[172:175],v[216:219]
ds_read_b64_tr_b16 v[104:105],v1 offset:20544
v_mfma_f32_16x16x32_bf16 v[216:219],v[100:103],v[156:159],v[216:219]
ds_read_b64_tr_b16 v[100:101],v1 offset:10304
v_mfma_f32_16x16x32_bf16 v[216:219],v[96:99],v[140:143],v[216:219]
buffer_load_dwordx4 a[84:87],v6,s[28:31],s89 offen sc1
v_mfma_f32_16x16x32_bf16 v[224:227],v[112:115],v[32:35],v[224:227]
ds_read_b64_tr_b16 v[96:97],v1 offset:64
ds_read_b64_tr_b16 v[98:99],v1 offset:5184
v_mfma_f32_16x16x32_bf16 v[224:227],v[116:119],v[36:39],v[224:227]
ds_read_b64_tr_b16 v[102:103],v1 offset:15424
v_mfma_f32_16x16x32_bf16 v[224:227],v[120:123],v[40:43],v[224:227]
ds_read_b64_tr_b16 v[106:107],v1 offset:25664
v_mfma_f32_16x16x32_bf16 v[224:227],v[124:127],v[44:47],v[224:227]
v_mfma_f32_16x16x32_bf16 v[232:235],v[124:127],v[180:183],v[232:235]
buffer_load_dwordx4 a[92:95],v6,s[28:31],s90 offen sc1
v_mfma_f32_16x16x32_bf16 v[232:235],v[120:123],v[164:167],v[232:235]
v_mfma_f32_16x16x32_bf16 v[232:235],v[116:119],v[148:151],v[232:235]
v_mfma_f32_16x16x32_bf16 v[232:235],v[112:115],v[132:135],v[232:235]
v_mfma_f32_16x16x32_bf16 v[240:243],v[112:115],v[196:199],v[240:243]
v_mfma_f32_16x16x32_bf16 v[240:243],v[116:119],v[212:215],v[240:243]
buffer_load_dwordx4 a[124:127],v6,s[28:31],s90 offen offset:1024 sc1
v_mfma_f32_16x16x32_bf16 v[240:243],v[120:123],v[228:231],v[240:243]
v_mfma_f32_16x16x32_bf16 v[240:243],v[124:127],v[244:247],v[240:243]
v_mfma_f32_16x16x32_bf16 v[248:251],v[124:127],v[188:191],v[248:251]
ds_read_b64_tr_b16 v[124:125],v2 offset:30816
ds_read_b64_tr_b16 v[126:127],v2 offset:35936
v_mfma_f32_16x16x32_bf16 v[248:251],v[120:123],v[172:175],v[248:251]
ds_read_b64_tr_b16 v[120:121],v2 offset:20576
v_mfma_f32_16x16x32_bf16 v[248:251],v[116:119],v[156:159],v[248:251]
buffer_load_dwordx4 a[116:119],v6,s[28:31],s89 offen offset:1024 sc1
v_mfma_f32_16x16x32_bf16 v[248:251],v[112:115],v[140:143],v[248:251]
s_waitcnt vmcnt(31) lgkmcnt(3)
ds_read_b64_tr_b16 v[112:113],v2 offset:96
ds_read_b64_tr_b16 v[114:115],v2 offset:5216
v_mfma_f32_16x16x32_bf16 a[192:195],v[64:67],a[128:131],a[192:195]
ds_read_b64_tr_b16 v[116:117],v2 offset:10336
v_mfma_f32_16x16x32_bf16 a[192:195],v[68:71],a[144:147],a[192:195]
s_waitcnt vmcnt(29)
ds_read_b64_tr_b16 v[118:119],v2 offset:15456
v_mfma_f32_16x16x32_bf16 a[192:195],v[72:75],a[160:163],a[192:195]
s_waitcnt vmcnt(25)
ds_read_b64_tr_b16 v[122:123],v2 offset:25696
v_mfma_f32_16x16x32_bf16 a[192:195],v[76:79],a[176:179],a[192:195]
buffer_load_dwordx4 a[108:111],v6,s[28:31],s88 offen offset:1024 sc1
v_mfma_f32_16x16x32_bf16 a[196:199],v[76:79],a[180:183],a[196:199]
s_waitcnt vmcnt(24)
v_mfma_f32_16x16x32_bf16 a[196:199],v[72:75],a[164:167],a[196:199]
s_waitcnt vmcnt(22)
v_mfma_f32_16x16x32_bf16 a[196:199],v[68:71],a[148:151],a[196:199]
s_waitcnt vmcnt(19)
v_mfma_f32_16x16x32_bf16 a[196:199],v[64:67],a[132:135],a[196:199]
s_waitcnt vmcnt(17)
v_mfma_f32_16x16x32_bf16 a[200:203],v[64:67],a[136:139],a[200:203]
buffer_load_dwordx4 a[100:103],v6,s[28:31],s87 offen offset:1024 sc1
v_mfma_f32_16x16x32_bf16 a[200:203],v[68:71],a[152:155],a[200:203]
s_waitcnt vmcnt(16)
v_mfma_f32_16x16x32_bf16 a[200:203],v[72:75],a[168:171],a[200:203]
s_waitcnt vmcnt(14)
v_mfma_f32_16x16x32_bf16 a[200:203],v[76:79],a[184:187],a[200:203]
s_waitcnt vmcnt(13)
v_mfma_f32_16x16x32_bf16 a[204:207],v[76:79],a[188:191],a[204:207]
s_waitcnt vmcnt(10)
ds_read_b64_tr_b16 v[76:77],v1 offset:30848
ds_read_b64_tr_b16 v[78:79],v1 offset:35968
v_mfma_f32_16x16x32_bf16 a[204:207],v[72:75],a[172:175],a[204:207]
buffer_load_dwordx4 a[64:67],v6,s[28:31],s87 offen offset:2048 sc1
v_mfma_f32_16x16x32_bf16 a[204:207],v[68:71],a[156:159],a[204:207]
s_waitcnt vmcnt(9)
ds_read_b64_tr_b16 v[68:69],v1 offset:10368
ds_read_b64_tr_b16 v[70:71],v1 offset:15488
v_mfma_f32_16x16x32_bf16 a[204:207],v[64:67],a[140:143],a[204:207]
ds_read_b64_tr_b16 v[64:65],v1 offset:128
v_mfma_f32_16x16x32_bf16 a[208:211],v[80:83],a[128:131],a[208:211]
ds_read_b64_tr_b16 v[66:67],v1 offset:5248
v_mfma_f32_16x16x32_bf16 a[208:211],v[84:87],a[144:147],a[208:211]
ds_read_b64_tr_b16 v[72:73],v1 offset:20608
v_mfma_f32_16x16x32_bf16 a[208:211],v[88:91],a[160:163],a[208:211]
ds_read_b64_tr_b16 v[74:75],v1 offset:25728
v_mfma_f32_16x16x32_bf16 a[208:211],v[92:95],a[176:179],a[208:211]
buffer_load_dwordx4 v26,s[48:51],s83 offen offset:2048 lds
v_mfma_f32_16x16x32_bf16 a[212:215],v[92:95],a[180:183],a[212:215]
v_mfma_f32_16x16x32_bf16 a[212:215],v[88:91],a[164:167],a[212:215]
v_mfma_f32_16x16x32_bf16 a[212:215],v[84:87],a[148:151],a[212:215]
v_mfma_f32_16x16x32_bf16 a[212:215],v[80:83],a[132:135],a[212:215]
v_mfma_f32_16x16x32_bf16 a[216:219],v[80:83],a[136:139],a[216:219]
buffer_load_dwordx4 a[72:75],v6,s[28:31],s88 offen offset:2048 sc1
v_mfma_f32_16x16x32_bf16 a[216:219],v[84:87],a[152:155],a[216:219]
v_mfma_f32_16x16x32_bf16 a[216:219],v[88:91],a[168:171],a[216:219]
v_mfma_f32_16x16x32_bf16 a[216:219],v[92:95],a[184:187],a[216:219]
v_mfma_f32_16x16x32_bf16 a[220:223],v[92:95],a[188:191],a[220:223]
ds_read_b64_tr_b16 v[92:93],v2 offset:30880
ds_read_b64_tr_b16 v[94:95],v2 offset:36000
v_mfma_f32_16x16x32_bf16 a[220:223],v[88:91],a[172:175],a[220:223]
ds_read_b64_tr_b16 v[88:89],v2 offset:20640
v_mfma_f32_16x16x32_bf16 a[220:223],v[84:87],a[156:159],a[220:223]
buffer_load_dwordx4 v27,s[48:51],s83 offen offset:3072 lds
v_mfma_f32_16x16x32_bf16 a[220:223],v[80:83],a[140:143],a[220:223]
s_add_u32 m0,s86,126976
ds_read_b64_tr_b16 v[80:81],v2 offset:160
ds_read_b64_tr_b16 v[82:83],v2 offset:5280
s_barrier
v_mfma_f32_16x16x32_bf16 a[224:227],v[96:99],a[128:131],a[224:227]
ds_read_b64_tr_b16 v[84:85],v2 offset:10400
v_mfma_f32_16x16x32_bf16 a[224:227],v[100:103],a[144:147],a[224:227]
ds_read_b64_tr_b16 v[86:87],v2 offset:15520
v_mfma_f32_16x16x32_bf16 a[224:227],v[104:107],a[160:163],a[224:227]
buffer_store_dwordx2 v[8:9],v10,s[44:47],s18 offen offset:1280 sc1
v_mfma_f32_16x16x32_bf16 a[224:227],v[108:111],a[176:179],a[224:227]
ds_read_b64_tr_b16 v[90:91],v2 offset:25760
v_mfma_f32_16x16x32_bf16 a[228:231],v[108:111],a[180:183],a[228:231]
v_mfma_f32_16x16x32_bf16 a[228:231],v[104:107],a[164:167],a[228:231]
v_mfma_f32_16x16x32_bf16 a[228:231],v[100:103],a[148:151],a[228:231]
v_mfma_f32_16x16x32_bf16 a[228:231],v[96:99],a[132:135],a[228:231]
buffer_load_dwordx4 a[80:83],v6,s[28:31],s89 offen offset:2048 sc1
v_mfma_f32_16x16x32_bf16 a[232:235],v[96:99],a[136:139],a[232:235]
v_mfma_f32_16x16x32_bf16 a[232:235],v[100:103],a[152:155],a[232:235]
v_mfma_f32_16x16x32_bf16 a[232:235],v[104:107],a[168:171],a[232:235]
v_mfma_f32_16x16x32_bf16 a[232:235],v[108:111],a[184:187],a[232:235]
v_mfma_f32_16x16x32_bf16 a[236:239],v[108:111],a[188:191],a[236:239]
s_waitcnt lgkmcnt(0)
ds_read_b64_tr_b16 v[108:109],v1 offset:30912
ds_read_b64_tr_b16 v[110:111],v1 offset:36032
v_mfma_f32_16x16x32_bf16 a[236:239],v[104:107],a[172:175],a[236:239]
buffer_load_dwordx4 v7,s[48:51],s83 offen lds
v_mfma_f32_16x16x32_bf16 a[236:239],v[100:103],a[156:159],a[236:239]
s_add_u32 m0,s86,128000
ds_read_b64_tr_b16 v[100:101],v1 offset:10432
ds_read_b64_tr_b16 v[102:103],v1 offset:15552
v_mfma_f32_16x16x32_bf16 a[236:239],v[96:99],a[140:143],a[236:239]
s_add_u32 s83,s64,1
s_and_b32 s83,s83,3
ds_read_b64_tr_b16 v[96:97],v1 offset:192
v_mfma_f32_16x16x32_bf16 a[240:243],v[112:115],a[128:131],a[240:243]
s_lshl_b32 s83,s83,1
s_or_b32 s83,s83,1
ds_read_b64_tr_b16 v[98:99],v1 offset:5312
v_mfma_f32_16x16x32_bf16 a[240:243],v[116:119],a[144:147],a[240:243]
s_mul_i32 s83,s83,163840
s_add_u32 s83,s83,s85
ds_read_b64_tr_b16 v[104:105],v1 offset:20672
v_mfma_f32_16x16x32_bf16 a[240:243],v[120:123],a[160:163],a[240:243]
buffer_load_dwordx4 a[88:91],v6,s[28:31],s90 offen offset:2048 sc1
v_mfma_f32_16x16x32_bf16 a[240:243],v[124:127],a[176:179],a[240:243]
ds_read_b64_tr_b16 v[106:107],v1 offset:25792
v_mfma_f32_16x16x32_bf16 a[244:247],v[124:127],a[180:183],a[244:247]
v_mfma_f32_16x16x32_bf16 a[244:247],v[120:123],a[164:167],a[244:247]
v_mfma_f32_16x16x32_bf16 a[244:247],v[116:119],a[148:151],a[244:247]
v_mfma_f32_16x16x32_bf16 a[244:247],v[112:115],a[132:135],a[244:247]
v_mfma_f32_16x16x32_bf16 a[248:251],v[112:115],a[136:139],a[248:251]
buffer_load_dwordx4 v24,s[48:51],s84 offen lds
v_mfma_f32_16x16x32_bf16 a[248:251],v[116:119],a[152:155],a[248:251]
v_mfma_f32_16x16x32_bf16 a[248:251],v[120:123],a[168:171],a[248:251]
v_mfma_f32_16x16x32_bf16 a[248:251],v[124:127],a[184:187],a[248:251]
v_mfma_f32_16x16x32_bf16 a[252:255],v[124:127],a[188:191],a[252:255]
ds_read_b64_tr_b16 v[124:125],v2 offset:30944
ds_read_b64_tr_b16 v[126:127],v2 offset:36064
v_mfma_f32_16x16x32_bf16 a[252:255],v[120:123],a[172:175],a[252:255]
buffer_load_dwordx4 a[120:123],v28,s[28:31],s90 offen offset:3072 sc1
v_mfma_f32_16x16x32_bf16 a[252:255],v[116:119],a[156:159],a[252:255]
ds_read_b64_tr_b16 v[116:117],v2 offset:10464
ds_read_b64_tr_b16 v[118:119],v2 offset:15584
v_mfma_f32_16x16x32_bf16 a[252:255],v[112:115],a[140:143],a[252:255]
ds_read_b64_tr_b16 v[112:113],v2 offset:224
v_mfma_f32_16x16x32_bf16 a[0:3],v[64:67],a[128:131],a[0:3]
ds_read_b64_tr_b16 v[114:115],v2 offset:5344
v_mfma_f32_16x16x32_bf16 a[0:3],v[68:71],a[144:147],a[0:3]
ds_read_b64_tr_b16 v[120:121],v2 offset:20704
v_mfma_f32_16x16x32_bf16 a[0:3],v[72:75],a[160:163],a[0:3]
ds_read_b64_tr_b16 v[122:123],v2 offset:25824
v_mfma_f32_16x16x32_bf16 a[0:3],v[76:79],a[176:179],a[0:3]
buffer_load_dwordx4 v25,s[48:51],s84 offen offset:1024 lds
v_mfma_f32_16x16x32_bf16 a[4:7],v[76:79],a[180:183],a[4:7]
v_mfma_f32_16x16x32_bf16 a[4:7],v[72:75],a[164:167],a[4:7]
v_mfma_f32_16x16x32_bf16 a[4:7],v[68:71],a[148:151],a[4:7]
v_mfma_f32_16x16x32_bf16 a[4:7],v[64:67],a[132:135],a[4:7]
v_mfma_f32_16x16x32_bf16 a[8:11],v[64:67],a[136:139],a[8:11]
buffer_load_dwordx4 a[112:115],v28,s[28:31],s89 offen offset:3072 sc1
v_mfma_f32_16x16x32_bf16 a[8:11],v[68:71],a[152:155],a[8:11]
v_mfma_f32_16x16x32_bf16 a[8:11],v[72:75],a[168:171],a[8:11]
v_mfma_f32_16x16x32_bf16 a[8:11],v[76:79],a[184:187],a[8:11]
v_mfma_f32_16x16x32_bf16 a[12:15],v[76:79],a[188:191],a[12:15]
ds_read_b64_tr_b16 v[76:77],v1 offset:30976
ds_read_b64_tr_b16 v[78:79],v1 offset:36096
v_mfma_f32_16x16x32_bf16 a[12:15],v[72:75],a[172:175],a[12:15]
buffer_load_dwordx4 a[104:107],v28,s[28:31],s88 offen offset:3072 sc1
v_mfma_f32_16x16x32_bf16 a[12:15],v[68:71],a[156:159],a[12:15]
ds_read_b64_tr_b16 v[68:69],v1 offset:10496
ds_read_b64_tr_b16 v[70:71],v1 offset:15616
v_mfma_f32_16x16x32_bf16 a[12:15],v[64:67],a[140:143],a[12:15]
ds_read_b64_tr_b16 v[64:65],v1 offset:256
v_mfma_f32_16x16x32_bf16 a[16:19],v[80:83],a[128:131],a[16:19]
ds_read_b64_tr_b16 v[66:67],v1 offset:5376
v_mfma_f32_16x16x32_bf16 a[16:19],v[84:87],a[144:147],a[16:19]
ds_read_b64_tr_b16 v[72:73],v1 offset:20736
v_mfma_f32_16x16x32_bf16 a[16:19],v[88:91],a[160:163],a[16:19]
ds_read_b64_tr_b16 v[74:75],v1 offset:25856
v_add_u32 v1,0xffff6000,v1
v_mfma_f32_16x16x32_bf16 a[16:19],v[92:95],a[176:179],a[16:19]
buffer_load_dwordx4 v26,s[48:51],s84 offen offset:2048 lds
v_mfma_f32_16x16x32_bf16 a[20:23],v[92:95],a[180:183],a[20:23]
v_mfma_f32_16x16x32_bf16 a[20:23],v[88:91],a[164:167],a[20:23]
v_mfma_f32_16x16x32_bf16 a[20:23],v[84:87],a[148:151],a[20:23]
v_mfma_f32_16x16x32_bf16 a[20:23],v[80:83],a[132:135],a[20:23]
v_mfma_f32_16x16x32_bf16 a[24:27],v[80:83],a[136:139],a[24:27]
buffer_load_dwordx4 a[96:99],v28,s[28:31],s87 offen offset:3072 sc1
v_mfma_f32_16x16x32_bf16 a[24:27],v[84:87],a[152:155],a[24:27]
s_add_u32 s87,s64,3
s_and_b32 s87,s87,3
s_lshl_b32 s87,s87,1
v_mfma_f32_16x16x32_bf16 a[24:27],v[88:91],a[168:171],a[24:27]
s_lshl_b32 s87,s87,16
s_add_u32 s88,s87,4096
s_add_u32 s89,s87,8192
v_mfma_f32_16x16x32_bf16 a[24:27],v[92:95],a[184:187],a[24:27]
s_add_u32 s90,s87,12288
v_mfma_f32_16x16x32_bf16 a[28:31],v[92:95],a[188:191],a[28:31]
ds_read_b64_tr_b16 v[92:93],v2 offset:31008
)A"
R"A(ds_read_b64_tr_b16 v[94:95],v2 offset:36128
v_mfma_f32_16x16x32_bf16 a[28:31],v[88:91],a[172:175],a[28:31]
s_waitcnt lgkmcnt(2)
ds_read_b64_tr_b16 v[88:89],v2 offset:20768
v_mfma_f32_16x16x32_bf16 a[28:31],v[84:87],a[156:159],a[28:31]
buffer_load_dwordx4 v27,s[48:51],s84 offen offset:3072 lds
v_mfma_f32_16x16x32_bf16 a[28:31],v[80:83],a[140:143],a[28:31]
s_add_u32 m0,s86,132096
ds_read_b64_tr_b16 v[80:81],v2 offset:288
ds_read_b64_tr_b16 v[82:83],v2 offset:5408
v_mfma_f32_16x16x32_bf16 v[128:131],v[96:99],a[128:131],v[128:131]
ds_read_b64_tr_b16 v[84:85],v2 offset:10528
v_mfma_f32_16x16x32_bf16 v[128:131],v[100:103],a[144:147],v[128:131]
ds_read_b64_tr_b16 v[86:87],v2 offset:15648
v_mfma_f32_16x16x32_bf16 v[128:131],v[104:107],a[160:163],v[128:131]
ds_read_b64_tr_b16 v[90:91],v2 offset:25888
v_add_u32 v2,0xffff6000,v2
v_mfma_f32_16x16x32_bf16 v[128:131],v[108:111],a[176:179],v[128:131]
v_mfma_f32_16x16x32_bf16 v[136:139],v[108:111],a[180:183],v[136:139]
buffer_load_dwordx4 v7,s[48:51],s84 offen lds
v_mfma_f32_16x16x32_bf16 v[136:139],v[104:107],a[164:167],v[136:139]
s_add_u32 m0,s86,81920
s_add_u32 s84,s83,20480
v_mfma_f32_16x16x32_bf16 v[136:139],v[100:103],a[148:151],v[136:139]
v_mfma_f32_16x16x32_bf16 v[136:139],v[96:99],a[132:135],v[136:139]
v_mfma_f32_16x16x32_bf16 v[144:147],v[96:99],a[136:139],v[144:147]
v_mfma_f32_16x16x32_bf16 v[144:147],v[100:103],a[152:155],v[144:147]
v_mfma_f32_16x16x32_bf16 v[144:147],v[104:107],a[168:171],v[144:147]
buffer_load_dwordx4 v24,s[48:51],s83 offen lds
v_mfma_f32_16x16x32_bf16 v[144:147],v[108:111],a[184:187],v[144:147]
v_mfma_f32_16x16x32_bf16 v[152:155],v[108:111],a[188:191],v[152:155]
ds_read_b64_tr_b16 v[108:109],v1 offset:30720
ds_read_b64_tr_b16 v[110:111],v1 offset:35840
v_mfma_f32_16x16x32_bf16 v[152:155],v[104:107],a[172:175],v[152:155]
ds_read_b64_tr_b16 v[104:105],v1 offset:20480
v_mfma_f32_16x16x32_bf16 v[152:155],v[100:103],a[156:159],v[152:155]
ds_read_b64_tr_b16 v[100:101],v1 offset:10240
v_mfma_f32_16x16x32_bf16 v[152:155],v[96:99],a[140:143],v[152:155]
ds_read_b64_tr_b16 v[96:97],v1
v_mfma_f32_16x16x32_bf16 v[160:163],v[112:115],a[128:131],v[160:163]
buffer_load_dwordx4 v25,s[48:51],s83 offen offset:1024 lds
v_mfma_f32_16x16x32_bf16 v[160:163],v[116:119],a[144:147],v[160:163]
ds_read_b64_tr_b16 v[98:99],v1 offset:5120
ds_read_b64_tr_b16 v[102:103],v1 offset:15360
v_mfma_f32_16x16x32_bf16 v[160:163],v[120:123],a[160:163],v[160:163]
ds_read_b64_tr_b16 v[106:107],v1 offset:25600
v_mfma_f32_16x16x32_bf16 v[160:163],v[124:127],a[176:179],v[160:163]
v_mfma_f32_16x16x32_bf16 v[168:171],v[124:127],a[180:183],v[168:171]
v_mfma_f32_16x16x32_bf16 v[168:171],v[120:123],a[164:167],v[168:171]
v_mfma_f32_16x16x32_bf16 v[168:171],v[116:119],a[148:151],v[168:171]
buffer_load_dwordx4 v26,s[48:51],s83 offen offset:2048 lds
v_mfma_f32_16x16x32_bf16 v[168:171],v[112:115],a[132:135],v[168:171]
v_mfma_f32_16x16x32_bf16 v[176:179],v[112:115],a[136:139],v[176:179]
v_mfma_f32_16x16x32_bf16 v[176:179],v[116:119],a[152:155],v[176:179]
v_mfma_f32_16x16x32_bf16 v[176:179],v[120:123],a[168:171],v[176:179]
v_mfma_f32_16x16x32_bf16 v[176:179],v[124:127],a[184:187],v[176:179]
v_mfma_f32_16x16x32_bf16 v[184:187],v[124:127],a[188:191],v[184:187]
buffer_load_dwordx4 v27,s[48:51],s83 offen offset:3072 lds
v_mfma_f32_16x16x32_bf16 v[184:187],v[120:123],a[172:175],v[184:187]
s_add_u32 m0,s86,86016
ds_read_b64_tr_b16 v[120:121],v2 offset:20512
ds_read_b64_tr_b16 v[122:123],v2 offset:25632
v_mfma_f32_16x16x32_bf16 v[184:187],v[116:119],a[156:159],v[184:187]
ds_read_b64_tr_b16 v[116:117],v2 offset:10272
v_mfma_f32_16x16x32_bf16 v[184:187],v[112:115],a[140:143],v[184:187]
ds_read_b64_tr_b16 v[112:113],v2 offset:32
v_mfma_f32_16x16x32_bf16 v[192:195],v[64:67],a[128:131],v[192:195]
ds_read_b64_tr_b16 v[114:115],v2 offset:5152
v_mfma_f32_16x16x32_bf16 v[192:195],v[68:71],a[144:147],v[192:195]
ds_read_b64_tr_b16 v[118:119],v2 offset:15392
ds_read_b64_tr_b16 v[124:125],v2 offset:30752
v_mfma_f32_16x16x32_bf16 v[192:195],v[72:75],a[160:163],v[192:195]
buffer_load_dwordx4 v7,s[48:51],s83 offen lds
v_mfma_f32_16x16x32_bf16 v[192:195],v[76:79],a[176:179],v[192:195]
s_add_u32 m0,s86,87040
s_add_u32 s83,s64,2
ds_read_b64_tr_b16 v[126:127],v2 offset:35872
v_mfma_f32_16x16x32_bf16 v[200:203],v[76:79],a[180:183],v[200:203]
s_and_b32 s83,s83,3
s_lshl_b32 s83,s83,1
s_or_b32 s83,s83,1
v_mfma_f32_16x16x32_bf16 v[200:203],v[72:75],a[164:167],v[200:203]
s_mul_i32 s83,s83,163840
s_add_u32 s83,s83,s85
v_mfma_f32_16x16x32_bf16 v[200:203],v[68:71],a[148:151],v[200:203]
v_mfma_f32_16x16x32_bf16 v[200:203],v[64:67],a[132:135],v[200:203]
v_mfma_f32_16x16x32_bf16 v[208:211],v[64:67],a[136:139],v[208:211]
buffer_load_dwordx4 v24,s[48:51],s84 offen lds
v_mfma_f32_16x16x32_bf16 v[208:211],v[68:71],a[152:155],v[208:211]
v_mfma_f32_16x16x32_bf16 v[208:211],v[72:75],a[168:171],v[208:211]
v_mfma_f32_16x16x32_bf16 v[208:211],v[76:79],a[184:187],v[208:211]
v_mfma_f32_16x16x32_bf16 v[216:219],v[76:79],a[188:191],v[216:219]
ds_read_b64_tr_b16 v[76:77],v1 offset:30784
ds_read_b64_tr_b16 v[78:79],v1 offset:35904
v_mfma_f32_16x16x32_bf16 v[216:219],v[72:75],a[172:175],v[216:219]
ds_read_b64_tr_b16 v[72:73],v1 offset:20544
v_mfma_f32_16x16x32_bf16 v[216:219],v[68:71],a[156:159],v[216:219]
buffer_load_dwordx2 v[4:5],v3,s[44:47],s91 offen offset:1280 sc1
v_mfma_f32_16x16x32_bf16 v[216:219],v[64:67],a[140:143],v[216:219]
s_waitcnt lgkmcnt(3)
ds_read_b64_tr_b16 v[64:65],v1 offset:64
ds_read_b64_tr_b16 v[66:67],v1 offset:5184
v_mfma_f32_16x16x32_bf16 v[224:227],v[80:83],a[128:131],v[224:227]
ds_read_b64_tr_b16 v[68:69],v1 offset:10304
v_mfma_f32_16x16x32_bf16 v[224:227],v[84:87],a[144:147],v[224:227]
ds_read_b64_tr_b16 v[70:71],v1 offset:15424
v_mfma_f32_16x16x32_bf16 v[224:227],v[88:91],a[160:163],v[224:227]
ds_read_b64_tr_b16 v[74:75],v1 offset:25664
v_mfma_f32_16x16x32_bf16 v[224:227],v[92:95],a[176:179],v[224:227]
v_mfma_f32_16x16x32_bf16 v[232:235],v[92:95],a[180:183],v[232:235]
buffer_load_dwordx4 v25,s[48:51],s84 offen offset:1024 lds
v_mfma_f32_16x16x32_bf16 v[232:235],v[88:91],a[164:167],v[232:235]
v_mfma_f32_16x16x32_bf16 v[232:235],v[84:87],a[148:151],v[232:235]
v_mfma_f32_16x16x32_bf16 v[232:235],v[80:83],a[132:135],v[232:235]
v_mfma_f32_16x16x32_bf16 v[240:243],v[80:83],a[136:139],v[240:243]
v_mfma_f32_16x16x32_bf16 v[240:243],v[84:87],a[152:155],v[240:243]
v_mfma_f32_16x16x32_bf16 v[240:243],v[88:91],a[168:171],v[240:243]
buffer_load_dwordx4 v26,s[48:51],s84 offen offset:2048 lds
v_mfma_f32_16x16x32_bf16 v[240:243],v[92:95],a[184:187],v[240:243]
v_mfma_f32_16x16x32_bf16 v[248:251],v[92:95],a[188:191],v[248:251]
ds_read_b64_tr_b16 v[92:93],v2 offset:30816
ds_read_b64_tr_b16 v[94:95],v2 offset:35936
v_mfma_f32_16x16x32_bf16 v[248:251],v[88:91],a[172:175],v[248:251]
ds_read_b64_tr_b16 v[88:89],v2 offset:20576
v_mfma_f32_16x16x32_bf16 v[248:251],v[84:87],a[156:159],v[248:251]
ds_read_b64_tr_b16 v[84:85],v2 offset:10336
v_mfma_f32_16x16x32_bf16 v[248:251],v[80:83],a[140:143],v[248:251]
s_waitcnt vmcnt(25)
ds_read_b64_tr_b16 v[80:81],v2 offset:96
v_mfma_f32_16x16x32_bf16 a[192:195],v[96:99],a[68:71],a[192:195]
buffer_load_dwordx4 v27,s[48:51],s84 offen offset:3072 lds
v_mfma_f32_16x16x32_bf16 a[192:195],v[100:103],a[76:79],a[192:195]
s_add_u32 m0,s86,91136
ds_read_b64_tr_b16 v[82:83],v2 offset:5216
ds_read_b64_tr_b16 v[86:87],v2 offset:15456
v_mfma_f32_16x16x32_bf16 a[192:195],v[104:107],a[84:87],a[192:195]
ds_read_b64_tr_b16 v[90:91],v2 offset:25696
v_mfma_f32_16x16x32_bf16 a[192:195],v[108:111],a[92:95],a[192:195]
v_mfma_f32_16x16x32_bf16 a[196:199],v[108:111],a[124:127],a[196:199]
v_mfma_f32_16x16x32_bf16 a[196:199],v[104:107],a[116:119],a[196:199]
v_mfma_f32_16x16x32_bf16 a[196:199],v[100:103],a[108:111],a[196:199]
buffer_load_dwordx4 v7,s[48:51],s84 offen lds
v_mfma_f32_16x16x32_bf16 a[196:199],v[96:99],a[100:103],a[196:199]
s_add_u32 m0,s86,40960
s_add_u32 s84,s83,20480
v_mfma_f32_16x16x32_bf16 a[200:203],v[96:99],a[64:67],a[200:203]
s_waitcnt vmcnt(25)
v_mfma_f32_16x16x32_bf16 a[200:203],v[100:103],a[72:75],a[200:203]
s_waitcnt vmcnt(22)
v_mfma_f32_16x16x32_bf16 a[200:203],v[104:107],a[80:83],a[200:203]
s_waitcnt vmcnt(18)
v_mfma_f32_16x16x32_bf16 a[200:203],v[108:111],a[88:91],a[200:203]
buffer_load_dwordx4 v[32:35],v6,s[28:31],s87 offen sc1
v_mfma_f32_16x16x32_bf16 a[204:207],v[108:111],a[120:123],a[204:207]
s_waitcnt vmcnt(17)
ds_read_b64_tr_b16 v[108:109],v1 offset:30848
L_pd15_%=:
s_cbranch_execz L_trt1_%=
ds_read_b64_tr_b16 v[110:111],v1 offset:35968
v_mfma_f32_16x16x32_bf16 a[204:207],v[104:107],a[112:115],a[204:207]
s_waitcnt vmcnt(16)
ds_read_b64_tr_b16 v[104:105],v1 offset:20608
v_mfma_f32_16x16x32_bf16 a[204:207],v[100:103],a[104:107],a[204:207]
s_waitcnt vmcnt(14)
ds_read_b64_tr_b16 v[100:101],v1 offset:10368
v_mfma_f32_16x16x32_bf16 a[204:207],v[96:99],a[96:99],a[204:207]
ds_read_b64_tr_b16 v[96:97],v1 offset:128
v_mfma_f32_16x16x32_bf16 a[208:211],v[112:115],a[68:71],a[208:211]
buffer_load_dwordx4 v[132:135],v6,s[28:31],s87 offen offset:1024 sc1
v_mfma_f32_16x16x32_bf16 a[208:211],v[116:119],a[76:79],a[208:211]
ds_read_b64_tr_b16 v[98:99],v1 offset:5248
ds_read_b64_tr_b16 v[102:103],v1 offset:15488
v_mfma_f32_16x16x32_bf16 a[208:211],v[120:123],a[84:87],a[208:211]
ds_read_b64_tr_b16 v[106:107],v1 offset:25728
v_mfma_f32_16x16x32_bf16 a[208:211],v[124:127],a[92:95],a[208:211]
v_mfma_f32_16x16x32_bf16 a[212:215],v[124:127],a[124:127],a[212:215]
v_mfma_f32_16x16x32_bf16 a[212:215],v[120:123],a[116:119],a[212:215]
buffer_load_dwordx4 v[196:199],v6,s[28:31],s87 offen offset:2048 sc1
v_mfma_f32_16x16x32_bf16 a[212:215],v[116:119],a[108:111],a[212:215]
v_mfma_f32_16x16x32_bf16 a[212:215],v[112:115],a[100:103],a[212:215]
v_mfma_f32_16x16x32_bf16 a[216:219],v[112:115],a[64:67],a[216:219]
v_mfma_f32_16x16x32_bf16 a[216:219],v[116:119],a[72:75],a[216:219]
v_mfma_f32_16x16x32_bf16 a[216:219],v[120:123],a[80:83],a[216:219]
buffer_load_dwordx4 v[36:39],v6,s[28:31],s88 offen sc1
v_mfma_f32_16x16x32_bf16 a[216:219],v[124:127],a[88:91],a[216:219]
v_mfma_f32_16x16x32_bf16 a[220:223],v[124:127],a[120:123],a[220:223]
ds_read_b64_tr_b16 v[124:125],v2 offset:30880
ds_read_b64_tr_b16 v[126:127],v2 offset:36000
v_mfma_f32_16x16x32_bf16 a[220:223],v[120:123],a[112:115],a[220:223]
ds_read_b64_tr_b16 v[120:121],v2 offset:20640
v_mfma_f32_16x16x32_bf16 a[220:223],v[116:119],a[104:107],a[220:223]
s_waitcnt lgkmcnt(3)
ds_read_b64_tr_b16 v[116:117],v2 offset:10400
v_mfma_f32_16x16x32_bf16 a[220:223],v[112:115],a[96:99],a[220:223]
buffer_load_dwordx4 v[140:143],v28,s[28:31],s87 offen offset:3072 sc1
v_mfma_f32_16x16x32_bf16 a[224:227],v[64:67],a[68:71],a[224:227]
s_add_u32 s87,s64,1
s_and_b32 s87,s87,3
s_lshl_b32 s87,s87,1
v_mfma_f32_16x16x32_bf16 a[224:227],v[68:71],a[76:79],a[224:227]
s_or_b32 s87,s87,1
s_lshl_b32 s87,s87,16
ds_read_b64_tr_b16 v[112:113],v2 offset:160
v_mfma_f32_16x16x32_bf16 a[224:227],v[72:75],a[84:87],a[224:227]
ds_read_b64_tr_b16 v[114:115],v2 offset:5280
ds_read_b64_tr_b16 v[118:119],v2 offset:15520
v_mfma_f32_16x16x32_bf16 a[224:227],v[76:79],a[92:95],a[224:227]
ds_read_b64_tr_b16 v[122:123],v2 offset:25760
v_mfma_f32_16x16x32_bf16 a[228:231],v[76:79],a[124:127],a[228:231]
buffer_load_dwordx4 v[212:215],v6,s[28:31],s88 offen offset:2048 sc1
v_mfma_f32_16x16x32_bf16 a[228:231],v[72:75],a[116:119],a[228:231]
v_mfma_f32_16x16x32_bf16 a[228:231],v[68:71],a[108:111],a[228:231]
v_mfma_f32_16x16x32_bf16 a[228:231],v[64:67],a[100:103],a[228:231]
v_mfma_f32_16x16x32_bf16 a[232:235],v[64:67],a[64:67],a[232:235]
v_mfma_f32_16x16x32_bf16 a[232:235],v[68:71],a[72:75],a[232:235]
buffer_load_dwordx4 v[148:151],v6,s[28:31],s88 offen offset:1024 sc1
v_mfma_f32_16x16x32_bf16 a[232:235],v[72:75],a[80:83],a[232:235]
s_waitcnt vmcnt(11)
v_cmp_ne_u32 s[56:57],s52,v4
v_cmp_ne_u32 s[58:59],s53,v5
v_mfma_f32_16x16x32_bf16 a[232:235],v[76:79],a[88:91],a[232:235]
s_or_b64 s[56:57],s[56:57],s[58:59]
s_cbranch_scc1 L_fr2_%=
L_fo2_%=:
v_mov_b32 v3,0
v_mfma_f32_16x16x32_bf16 a[236:239],v[76:79],a[120:123],a[236:239]
)A"
R"A(s_add_u32 s91,s64,3
ds_read_b64_tr_b16 v[76:77],v1 offset:30912
ds_read_b64_tr_b16 v[78:79],v1 offset:36032
v_mfma_f32_16x16x32_bf16 a[236:239],v[72:75],a[112:115],a[236:239]
s_and_b32 s91,s91,3
s_add_u32 s91,s91,s69
ds_read_b64_tr_b16 v[72:73],v1 offset:20672
v_mfma_f32_16x16x32_bf16 a[236:239],v[68:71],a[104:107],a[236:239]
buffer_load_dwordx4 v[40:43],v6,s[28:31],s89 offen sc1
v_mfma_f32_16x16x32_bf16 a[236:239],v[64:67],a[96:99],a[236:239]
s_add_u32 s91,s91,8
ds_read_b64_tr_b16 v[64:65],v1 offset:192
ds_read_b64_tr_b16 v[66:67],v1 offset:5312
v_mfma_f32_16x16x32_bf16 a[240:243],v[80:83],a[68:71],a[240:243]
s_lshl_b32 s91,s91,6
s_and_b32 s69,s63,s60
ds_read_b64_tr_b16 v[68:69],v1 offset:10432
v_mfma_f32_16x16x32_bf16 a[240:243],v[84:87],a[76:79],a[240:243]
s_cmp_eq_u32 s69,3
s_cselect_b32 s69,0x40000000,0
ds_read_b64_tr_b16 v[70:71],v1 offset:15552
v_mfma_f32_16x16x32_bf16 a[240:243],v[88:91],a[84:87],a[240:243]
s_add_u32 s68,s68,s69
ds_read_b64_tr_b16 v[74:75],v1 offset:25792
v_mfma_f32_16x16x32_bf16 a[240:243],v[92:95],a[92:95],a[240:243]
buffer_load_dwordx4 a[128:131],v6,s[28:31],s87 offen sc1
v_mfma_f32_16x16x32_bf16 a[244:247],v[92:95],a[124:127],a[244:247]
v_mfma_f32_16x16x32_bf16 a[244:247],v[88:91],a[116:119],a[244:247]
v_mfma_f32_16x16x32_bf16 a[244:247],v[84:87],a[108:111],a[244:247]
v_mfma_f32_16x16x32_bf16 a[244:247],v[80:83],a[100:103],a[244:247]
v_mfma_f32_16x16x32_bf16 a[248:251],v[80:83],a[64:67],a[248:251]
buffer_load_dwordx4 v[156:159],v28,s[28:31],s88 offen offset:3072 sc1
v_mfma_f32_16x16x32_bf16 a[248:251],v[84:87],a[72:75],a[248:251]
s_add_u32 s88,s87,4096
v_mfma_f32_16x16x32_bf16 a[248:251],v[88:91],a[80:83],a[248:251]
v_mfma_f32_16x16x32_bf16 a[248:251],v[92:95],a[88:91],a[248:251]
v_mfma_f32_16x16x32_bf16 a[252:255],v[92:95],a[120:123],a[252:255]
ds_read_b64_tr_b16 v[92:93],v2 offset:30944
ds_read_b64_tr_b16 v[94:95],v2 offset:36064
v_mfma_f32_16x16x32_bf16 a[252:255],v[88:91],a[112:115],a[252:255]
buffer_load_dwordx4 v[228:231],v6,s[28:31],s89 offen offset:2048 sc1
v_mfma_f32_16x16x32_bf16 a[252:255],v[84:87],a[104:107],a[252:255]
ds_read_b64_tr_b16 v[84:85],v2 offset:10464
ds_read_b64_tr_b16 v[86:87],v2 offset:15584
v_mfma_f32_16x16x32_bf16 a[252:255],v[80:83],a[96:99],a[252:255]
ds_read_b64_tr_b16 v[80:81],v2 offset:224
v_mfma_f32_16x16x32_bf16 a[0:3],v[96:99],a[68:71],a[0:3]
ds_read_b64_tr_b16 v[82:83],v2 offset:5344
v_mfma_f32_16x16x32_bf16 a[0:3],v[100:103],a[76:79],a[0:3]
ds_read_b64_tr_b16 v[88:89],v2 offset:20704
v_mfma_f32_16x16x32_bf16 a[0:3],v[104:107],a[84:87],a[0:3]
buffer_load_dwordx4 v[164:167],v6,s[28:31],s89 offen offset:1024 sc1
v_mfma_f32_16x16x32_bf16 a[0:3],v[108:111],a[92:95],a[0:3]
ds_read_b64_tr_b16 v[90:91],v2 offset:25824
v_mfma_f32_16x16x32_bf16 a[4:7],v[108:111],a[124:127],a[4:7]
v_mfma_f32_16x16x32_bf16 a[4:7],v[104:107],a[116:119],a[4:7]
v_mfma_f32_16x16x32_bf16 a[4:7],v[100:103],a[108:111],a[4:7]
v_mfma_f32_16x16x32_bf16 a[4:7],v[96:99],a[100:103],a[4:7]
buffer_load_dwordx4 v[44:47],v6,s[28:31],s90 offen sc1
v_mfma_f32_16x16x32_bf16 a[8:11],v[96:99],a[64:67],a[8:11]
v_mfma_f32_16x16x32_bf16 a[8:11],v[100:103],a[72:75],a[8:11]
v_mfma_f32_16x16x32_bf16 a[8:11],v[104:107],a[80:83],a[8:11]
v_mfma_f32_16x16x32_bf16 a[8:11],v[108:111],a[88:91],a[8:11]
v_mfma_f32_16x16x32_bf16 a[12:15],v[108:111],a[120:123],a[12:15]
buffer_load_dwordx4 a[144:147],v6,s[28:31],s88 offen sc1
v_mfma_f32_16x16x32_bf16 a[12:15],v[104:107],a[112:115],a[12:15]
ds_read_b64_tr_b16 v[104:105],v1 offset:20736
ds_read_b64_tr_b16 v[106:107],v1 offset:25856
v_mfma_f32_16x16x32_bf16 a[12:15],v[100:103],a[104:107],a[12:15]
ds_read_b64_tr_b16 v[100:101],v1 offset:10496
v_mfma_f32_16x16x32_bf16 a[12:15],v[96:99],a[96:99],a[12:15]
s_waitcnt lgkmcnt(3)
ds_read_b64_tr_b16 v[96:97],v1 offset:256
v_mfma_f32_16x16x32_bf16 a[16:19],v[112:115],a[68:71],a[16:19]
ds_read_b64_tr_b16 v[98:99],v1 offset:5376
v_mfma_f32_16x16x32_bf16 a[16:19],v[116:119],a[76:79],a[16:19]
buffer_load_dwordx4 v[172:175],v28,s[28:31],s89 offen offset:3072 sc1
v_mfma_f32_16x16x32_bf16 a[16:19],v[120:123],a[84:87],a[16:19]
s_add_u32 s89,s87,8192
ds_read_b64_tr_b16 v[102:103],v1 offset:15616
ds_read_b64_tr_b16 v[108:109],v1 offset:30976
v_mfma_f32_16x16x32_bf16 a[16:19],v[124:127],a[92:95],a[16:19]
ds_read_b64_tr_b16 v[110:111],v1 offset:36096
v_add_u32 v1,0x1e000,v1
v_mfma_f32_16x16x32_bf16 a[20:23],v[124:127],a[124:127],a[20:23]
v_mfma_f32_16x16x32_bf16 a[20:23],v[120:123],a[116:119],a[20:23]
v_mfma_f32_16x16x32_bf16 a[20:23],v[116:119],a[108:111],a[20:23]
buffer_load_dwordx4 v[180:183],v6,s[28:31],s90 offen offset:1024 sc1
v_mfma_f32_16x16x32_bf16 a[20:23],v[112:115],a[100:103],a[20:23]
v_mfma_f32_16x16x32_bf16 a[24:27],v[112:115],a[64:67],a[24:27]
v_mfma_f32_16x16x32_bf16 a[24:27],v[116:119],a[72:75],a[24:27]
v_mfma_f32_16x16x32_bf16 a[24:27],v[120:123],a[80:83],a[24:27]
v_mfma_f32_16x16x32_bf16 a[24:27],v[124:127],a[88:91],a[24:27]
buffer_load_dwordx4 a[160:163],v6,s[28:31],s89 offen sc1
v_mfma_f32_16x16x32_bf16 a[28:31],v[124:127],a[120:123],a[28:31]
ds_read_b64_tr_b16 v[124:125],v2 offset:31008
ds_read_b64_tr_b16 v[126:127],v2 offset:36128
v_mfma_f32_16x16x32_bf16 a[28:31],v[120:123],a[112:115],a[28:31]
ds_read_b64_tr_b16 v[120:121],v2 offset:20768
v_mfma_f32_16x16x32_bf16 a[28:31],v[116:119],a[104:107],a[28:31]
ds_read_b64_tr_b16 v[116:117],v2 offset:10528
v_mfma_f32_16x16x32_bf16 a[28:31],v[112:115],a[96:99],a[28:31]
ds_read_b64_tr_b16 v[112:113],v2 offset:288
v_mfma_f32_16x16x32_bf16 v[128:131],v[64:67],a[68:71],v[128:131]
buffer_load_dwordx4 v[244:247],v6,s[28:31],s90 offen offset:2048 sc1
v_mfma_f32_16x16x32_bf16 v[128:131],v[68:71],a[76:79],v[128:131]
ds_read_b64_tr_b16 v[114:115],v2 offset:5408
ds_read_b64_tr_b16 v[118:119],v2 offset:15648
v_mfma_f32_16x16x32_bf16 v[128:131],v[72:75],a[84:87],v[128:131]
ds_read_b64_tr_b16 v[122:123],v2 offset:25888
v_add_u32 v2,0x1e000,v2
v_mfma_f32_16x16x32_bf16 v[128:131],v[76:79],a[92:95],v[128:131]
v_mfma_f32_16x16x32_bf16 v[136:139],v[76:79],a[124:127],v[136:139]
v_mfma_f32_16x16x32_bf16 v[136:139],v[72:75],a[116:119],v[136:139]
buffer_load_dwordx4 v[188:191],v28,s[28:31],s90 offen offset:3072 sc1
v_mfma_f32_16x16x32_bf16 v[136:139],v[68:71],a[108:111],v[136:139]
s_add_u32 s90,s87,12288
v_mfma_f32_16x16x32_bf16 v[136:139],v[64:67],a[100:103],v[136:139]
v_mfma_f32_16x16x32_bf16 v[144:147],v[64:67],a[64:67],v[144:147]
v_mfma_f32_16x16x32_bf16 v[144:147],v[68:71],a[72:75],v[144:147]
v_mfma_f32_16x16x32_bf16 v[144:147],v[72:75],a[80:83],v[144:147]
buffer_load_dwordx4 a[176:179],v6,s[28:31],s90 offen sc1
v_mfma_f32_16x16x32_bf16 v[144:147],v[76:79],a[88:91],v[144:147]
v_mfma_f32_16x16x32_bf16 v[152:155],v[76:79],a[120:123],v[152:155]
v_mfma_f32_16x16x32_bf16 v[152:155],v[72:75],a[112:115],v[152:155]
v_mfma_f32_16x16x32_bf16 v[152:155],v[68:71],a[104:107],v[152:155]
v_mfma_f32_16x16x32_bf16 v[152:155],v[64:67],a[96:99],v[152:155]
buffer_load_dwordx4 a[180:183],v6,s[28:31],s90 offen offset:1024 sc1
s_waitcnt vmcnt(21) lgkmcnt(0)
s_barrier
v_mfma_f32_16x16x32_bf16 v[160:163],v[80:83],a[68:71],v[160:163]
ds_read_b64_tr_b16 v[64:65],v1
ds_read_b64_tr_b16 v[66:67],v1 offset:5120
v_mfma_f32_16x16x32_bf16 v[160:163],v[84:87],a[76:79],v[160:163]
ds_read_b64_tr_b16 v[68:69],v1 offset:10240
v_mfma_f32_16x16x32_bf16 v[160:163],v[88:91],a[84:87],v[160:163]
ds_read_b64_tr_b16 v[70:71],v1 offset:15360
v_mfma_f32_16x16x32_bf16 v[160:163],v[92:95],a[92:95],v[160:163]
buffer_load_dwordx4 a[164:167],v6,s[28:31],s89 offen offset:1024 sc1
v_mfma_f32_16x16x32_bf16 v[168:171],v[92:95],a[124:127],v[168:171]
ds_read_b64_tr_b16 v[72:73],v1 offset:20480
ds_read_b64_tr_b16 v[74:75],v1 offset:25600
v_mfma_f32_16x16x32_bf16 v[168:171],v[88:91],a[116:119],v[168:171]
ds_read_b64_tr_b16 v[76:77],v1 offset:30720
v_mfma_f32_16x16x32_bf16 v[168:171],v[84:87],a[108:111],v[168:171]
ds_read_b64_tr_b16 v[78:79],v1 offset:35840
v_mfma_f32_16x16x32_bf16 v[168:171],v[80:83],a[100:103],v[168:171]
v_mfma_f32_16x16x32_bf16 v[176:179],v[80:83],a[64:67],v[176:179]
v_mfma_f32_16x16x32_bf16 v[176:179],v[84:87],a[72:75],v[176:179]
buffer_load_dwordx4 v24,s[48:51],s83 offen lds
v_mfma_f32_16x16x32_bf16 v[176:179],v[88:91],a[80:83],v[176:179]
v_mfma_f32_16x16x32_bf16 v[176:179],v[92:95],a[88:91],v[176:179]
v_mfma_f32_16x16x32_bf16 v[184:187],v[92:95],a[120:123],v[184:187]
ds_read_b64_tr_b16 v[92:93],v2 offset:30752
ds_read_b64_tr_b16 v[94:95],v2 offset:35872
v_mfma_f32_16x16x32_bf16 v[184:187],v[88:91],a[112:115],v[184:187]
ds_read_b64_tr_b16 v[88:89],v2 offset:20512
v_mfma_f32_16x16x32_bf16 v[184:187],v[84:87],a[104:107],v[184:187]
buffer_load_dwordx4 a[148:151],v6,s[28:31],s88 offen offset:1024 sc1
v_mfma_f32_16x16x32_bf16 v[184:187],v[80:83],a[96:99],v[184:187]
ds_read_b64_tr_b16 v[80:81],v2 offset:32
ds_read_b64_tr_b16 v[82:83],v2 offset:5152
v_mfma_f32_16x16x32_bf16 v[192:195],v[96:99],a[68:71],v[192:195]
ds_read_b64_tr_b16 v[84:85],v2 offset:10272
v_mfma_f32_16x16x32_bf16 v[192:195],v[100:103],a[76:79],v[192:195]
ds_read_b64_tr_b16 v[86:87],v2 offset:15392
v_mfma_f32_16x16x32_bf16 v[192:195],v[104:107],a[84:87],v[192:195]
ds_read_b64_tr_b16 v[90:91],v2 offset:25632
v_mfma_f32_16x16x32_bf16 v[192:195],v[108:111],a[92:95],v[192:195]
v_mfma_f32_16x16x32_bf16 v[200:203],v[108:111],a[124:127],v[200:203]
buffer_load_dwordx4 v25,s[48:51],s83 offen offset:1024 lds
v_mfma_f32_16x16x32_bf16 v[200:203],v[104:107],a[116:119],v[200:203]
v_mfma_f32_16x16x32_bf16 v[200:203],v[100:103],a[108:111],v[200:203]
v_mfma_f32_16x16x32_bf16 v[200:203],v[96:99],a[100:103],v[200:203]
v_mfma_f32_16x16x32_bf16 v[208:211],v[96:99],a[64:67],v[208:211]
v_mfma_f32_16x16x32_bf16 v[208:211],v[100:103],a[72:75],v[208:211]
buffer_load_dwordx4 a[132:135],v6,s[28:31],s87 offen offset:1024 sc1
v_mfma_f32_16x16x32_bf16 v[208:211],v[104:107],a[80:83],v[208:211]
v_mfma_f32_16x16x32_bf16 v[208:211],v[108:111],a[88:91],v[208:211]
v_mfma_f32_16x16x32_bf16 v[216:219],v[108:111],a[120:123],v[216:219]
ds_read_b64_tr_b16 v[108:109],v1 offset:30784
ds_read_b64_tr_b16 v[110:111],v1 offset:35904
v_mfma_f32_16x16x32_bf16 v[216:219],v[104:107],a[112:115],v[216:219]
ds_read_b64_tr_b16 v[104:105],v1 offset:20544
v_mfma_f32_16x16x32_bf16 v[216:219],v[100:103],a[104:107],v[216:219]
ds_read_b64_tr_b16 v[100:101],v1 offset:10304
v_mfma_f32_16x16x32_bf16 v[216:219],v[96:99],a[96:99],v[216:219]
buffer_load_dwordx4 v26,s[48:51],s83 offen offset:2048 lds
v_mfma_f32_16x16x32_bf16 v[224:227],v[112:115],a[68:71],v[224:227]
ds_read_b64_tr_b16 v[96:97],v1 offset:64
ds_read_b64_tr_b16 v[98:99],v1 offset:5184
v_mfma_f32_16x16x32_bf16 v[224:227],v[116:119],a[76:79],v[224:227]
ds_read_b64_tr_b16 v[102:103],v1 offset:15424
v_mfma_f32_16x16x32_bf16 v[224:227],v[120:123],a[84:87],v[224:227]
ds_read_b64_tr_b16 v[106:107],v1 offset:25664
v_mfma_f32_16x16x32_bf16 v[224:227],v[124:127],a[92:95],v[224:227]
v_mfma_f32_16x16x32_bf16 v[232:235],v[124:127],a[124:127],v[232:235]
v_mfma_f32_16x16x32_bf16 v[232:235],v[120:123],a[116:119],v[232:235]
buffer_load_dwordx4 a[136:139],v6,s[28:31],s87 offen offset:2048 sc1
v_mfma_f32_16x16x32_bf16 v[232:235],v[116:119],a[108:111],v[232:235]
v_mfma_f32_16x16x32_bf16 v[232:235],v[112:115],a[100:103],v[232:235]
v_mfma_f32_16x16x32_bf16 v[240:243],v[112:115],a[64:67],v[240:243]
v_mfma_f32_16x16x32_bf16 v[240:243],v[116:119],a[72:75],v[240:243]
v_mfma_f32_16x16x32_bf16 v[240:243],v[120:123],a[80:83],v[240:243]
v_mfma_f32_16x16x32_bf16 v[240:243],v[124:127],a[88:91],v[240:243]
buffer_load_dwordx4 v27,s[48:51],s83 offen offset:3072 lds
v_mfma_f32_16x16x32_bf16 v[248:251],v[124:127],a[120:123],v[248:251]
s_add_u32 m0,s86,45056
ds_read_b64_tr_b16 v[124:125],v2 offset:30816
ds_read_b64_tr_b16 v[126:127],v2 offset:35936
v_mfma_f32_16x16x32_bf16 v[248:251],v[120:123],a[112:115],v[248:251]
ds_read_b64_tr_b16 v[120:121],v2 offset:20576
v_mfma_f32_16x16x32_bf16 v[248:251],v[116:119],a[104:107],v[248:251]
ds_read_b64_tr_b16 v[116:117],v2 offset:10336
v_mfma_f32_16x16x32_bf16 v[248:251],v[112:115],a[96:99],v[248:251]
s_waitcnt vmcnt(21) lgkmcnt(4)
ds_read_b64_tr_b16 v[112:113],v2 offset:96
)A"
R"A(v_mfma_f32_16x16x32_bf16 a[192:195],v[64:67],v[32:35],a[192:195]
s_waitcnt vmcnt(13)
ds_read_b64_tr_b16 v[114:115],v2 offset:5216
ds_read_b64_tr_b16 v[118:119],v2 offset:15456
v_mfma_f32_16x16x32_bf16 a[192:195],v[68:71],v[36:39],a[192:195]
buffer_load_dwordx4 v7,s[48:51],s83 offen lds
v_mfma_f32_16x16x32_bf16 a[192:195],v[72:75],v[40:43],a[192:195]
s_add_u32 m0,s86,46080
s_add_u32 s83,s64,3
ds_read_b64_tr_b16 v[122:123],v2 offset:25696
v_mfma_f32_16x16x32_bf16 a[192:195],v[76:79],v[44:47],a[192:195]
s_and_b32 s83,s83,3
s_lshl_b32 s83,s83,1
s_or_b32 s83,s83,17
v_mfma_f32_16x16x32_bf16 a[196:199],v[76:79],v[180:183],a[196:199]
s_mul_i32 s83,s83,163840
s_add_u32 s83,s83,s85
v_mfma_f32_16x16x32_bf16 a[196:199],v[72:75],v[164:167],a[196:199]
v_mfma_f32_16x16x32_bf16 a[196:199],v[68:71],v[148:151],a[196:199]
buffer_load_dwordx4 a[152:155],v6,s[28:31],s88 offen offset:2048 sc1
v_mfma_f32_16x16x32_bf16 a[196:199],v[64:67],v[132:135],a[196:199]
v_mfma_f32_16x16x32_bf16 a[200:203],v[64:67],v[196:199],a[200:203]
v_mfma_f32_16x16x32_bf16 a[200:203],v[68:71],v[212:215],a[200:203]
v_mfma_f32_16x16x32_bf16 a[200:203],v[72:75],v[228:231],a[200:203]
s_waitcnt vmcnt(13)
v_mfma_f32_16x16x32_bf16 a[200:203],v[76:79],v[244:247],a[200:203]
s_waitcnt vmcnt(12)
v_mfma_f32_16x16x32_bf16 a[204:207],v[76:79],v[188:191],a[204:207]
buffer_load_dwordx4 v24,s[48:51],s84 offen lds
v_mfma_f32_16x16x32_bf16 a[204:207],v[72:75],v[172:175],a[204:207]
ds_read_b64_tr_b16 v[72:73],v1 offset:20608
ds_read_b64_tr_b16 v[74:75],v1 offset:25728
v_mfma_f32_16x16x32_bf16 a[204:207],v[68:71],v[156:159],a[204:207]
ds_read_b64_tr_b16 v[68:69],v1 offset:10368
v_mfma_f32_16x16x32_bf16 a[204:207],v[64:67],v[140:143],a[204:207]
ds_read_b64_tr_b16 v[64:65],v1 offset:128
v_mfma_f32_16x16x32_bf16 a[208:211],v[80:83],v[32:35],a[208:211]
ds_read_b64_tr_b16 v[66:67],v1 offset:5248
v_mfma_f32_16x16x32_bf16 a[208:211],v[84:87],v[36:39],a[208:211]
buffer_load_dwordx4 a[168:171],v6,s[28:31],s89 offen offset:2048 sc1
v_mfma_f32_16x16x32_bf16 a[208:211],v[88:91],v[40:43],a[208:211]
ds_read_b64_tr_b16 v[70:71],v1 offset:15488
ds_read_b64_tr_b16 v[76:77],v1 offset:30848
v_mfma_f32_16x16x32_bf16 a[208:211],v[92:95],v[44:47],a[208:211]
ds_read_b64_tr_b16 v[78:79],v1 offset:35968
v_mfma_f32_16x16x32_bf16 a[212:215],v[92:95],v[180:183],a[212:215]
v_mfma_f32_16x16x32_bf16 a[212:215],v[88:91],v[164:167],a[212:215]
v_mfma_f32_16x16x32_bf16 a[212:215],v[84:87],v[148:151],a[212:215]
v_mfma_f32_16x16x32_bf16 a[212:215],v[80:83],v[132:135],a[212:215]
buffer_load_dwordx4 v25,s[48:51],s84 offen offset:1024 lds
v_mfma_f32_16x16x32_bf16 a[216:219],v[80:83],v[196:199],a[216:219]
v_mfma_f32_16x16x32_bf16 a[216:219],v[84:87],v[212:215],a[216:219]
v_mfma_f32_16x16x32_bf16 a[216:219],v[88:91],v[228:231],a[216:219]
v_mfma_f32_16x16x32_bf16 a[216:219],v[92:95],v[244:247],a[216:219]
v_mfma_f32_16x16x32_bf16 a[220:223],v[92:95],v[188:191],a[220:223]
buffer_load_dwordx4 a[184:187],v6,s[28:31],s90 offen offset:2048 sc1
v_mfma_f32_16x16x32_bf16 a[220:223],v[88:91],v[172:175],a[220:223]
ds_read_b64_tr_b16 v[88:89],v2 offset:20640
ds_read_b64_tr_b16 v[90:91],v2 offset:25760
v_mfma_f32_16x16x32_bf16 a[220:223],v[84:87],v[156:159],a[220:223]
ds_read_b64_tr_b16 v[84:85],v2 offset:10400
v_mfma_f32_16x16x32_bf16 a[220:223],v[80:83],v[140:143],a[220:223]
ds_read_b64_tr_b16 v[80:81],v2 offset:160
v_mfma_f32_16x16x32_bf16 a[224:227],v[96:99],v[32:35],a[224:227]
ds_read_b64_tr_b16 v[82:83],v2 offset:5280
v_mfma_f32_16x16x32_bf16 a[224:227],v[100:103],v[36:39],a[224:227]
ds_read_b64_tr_b16 v[86:87],v2 offset:15520
ds_read_b64_tr_b16 v[92:93],v2 offset:30880
v_mfma_f32_16x16x32_bf16 a[224:227],v[104:107],v[40:43],a[224:227]
buffer_load_dwordx4 v26,s[48:51],s84 offen offset:2048 lds
v_mfma_f32_16x16x32_bf16 a[224:227],v[108:111],v[44:47],a[224:227]
ds_read_b64_tr_b16 v[94:95],v2 offset:36000
v_mfma_f32_16x16x32_bf16 a[228:231],v[108:111],v[180:183],a[228:231]
v_mfma_f32_16x16x32_bf16 a[228:231],v[104:107],v[164:167],a[228:231]
v_mfma_f32_16x16x32_bf16 a[228:231],v[100:103],v[148:151],a[228:231]
v_mfma_f32_16x16x32_bf16 a[228:231],v[96:99],v[132:135],a[228:231]
v_mfma_f32_16x16x32_bf16 a[232:235],v[96:99],v[196:199],a[232:235]
buffer_load_dwordx4 v27,s[48:51],s84 offen offset:3072 lds
v_mfma_f32_16x16x32_bf16 a[232:235],v[100:103],v[212:215],a[232:235]
s_add_u32 m0,s86,50176
v_mfma_f32_16x16x32_bf16 a[232:235],v[104:107],v[228:231],a[232:235]
v_mfma_f32_16x16x32_bf16 a[232:235],v[108:111],v[244:247],a[232:235]
v_mfma_f32_16x16x32_bf16 a[236:239],v[108:111],v[188:191],a[236:239]
ds_read_b64_tr_b16 v[108:109],v1 offset:30912
ds_read_b64_tr_b16 v[110:111],v1 offset:36032
v_mfma_f32_16x16x32_bf16 a[236:239],v[104:107],v[172:175],a[236:239]
buffer_load_dwordx4 a[188:191],v28,s[28:31],s90 offen offset:3072 sc1
v_mfma_f32_16x16x32_bf16 a[236:239],v[100:103],v[156:159],a[236:239]
ds_read_b64_tr_b16 v[100:101],v1 offset:10432
ds_read_b64_tr_b16 v[102:103],v1 offset:15552
v_mfma_f32_16x16x32_bf16 a[236:239],v[96:99],v[140:143],a[236:239]
s_waitcnt lgkmcnt(4)
ds_read_b64_tr_b16 v[96:97],v1 offset:192
v_mfma_f32_16x16x32_bf16 a[240:243],v[112:115],v[32:35],a[240:243]
ds_read_b64_tr_b16 v[98:99],v1 offset:5312
v_mfma_f32_16x16x32_bf16 a[240:243],v[116:119],v[36:39],a[240:243]
ds_read_b64_tr_b16 v[104:105],v1 offset:20672
v_mfma_f32_16x16x32_bf16 a[240:243],v[120:123],v[40:43],a[240:243]
ds_read_b64_tr_b16 v[106:107],v1 offset:25792
v_mfma_f32_16x16x32_bf16 a[240:243],v[124:127],v[44:47],a[240:243]
buffer_load_dwordx4 v7,s[48:51],s84 offen lds
v_mfma_f32_16x16x32_bf16 a[244:247],v[124:127],v[180:183],a[244:247]
s_add_u32 m0,s86,0
s_add_u32 s84,s83,20480
v_mfma_f32_16x16x32_bf16 a[244:247],v[120:123],v[164:167],a[244:247]
v_mfma_f32_16x16x32_bf16 a[244:247],v[116:119],v[148:151],a[244:247]
v_mfma_f32_16x16x32_bf16 a[244:247],v[112:115],v[132:135],a[244:247]
v_mfma_f32_16x16x32_bf16 a[248:251],v[112:115],v[196:199],a[248:251]
buffer_load_dwordx4 a[172:175],v28,s[28:31],s89 offen offset:3072 sc1
v_mfma_f32_16x16x32_bf16 a[248:251],v[116:119],v[212:215],a[248:251]
v_mfma_f32_16x16x32_bf16 a[248:251],v[120:123],v[228:231],a[248:251]
v_mfma_f32_16x16x32_bf16 a[248:251],v[124:127],v[244:247],a[248:251]
v_mfma_f32_16x16x32_bf16 a[252:255],v[124:127],v[188:191],a[252:255]
ds_read_b64_tr_b16 v[124:125],v2 offset:30944
ds_read_b64_tr_b16 v[126:127],v2 offset:36064
v_mfma_f32_16x16x32_bf16 a[252:255],v[120:123],v[172:175],a[252:255]
ds_read_b64_tr_b16 v[120:121],v2 offset:20704
v_mfma_f32_16x16x32_bf16 a[252:255],v[116:119],v[156:159],a[252:255]
buffer_load_dwordx4 v24,s[48:51],s83 offen lds
v_mfma_f32_16x16x32_bf16 a[252:255],v[112:115],v[140:143],a[252:255]
ds_read_b64_tr_b16 v[112:113],v2 offset:224
ds_read_b64_tr_b16 v[114:115],v2 offset:5344
v_mfma_f32_16x16x32_bf16 a[0:3],v[64:67],v[32:35],a[0:3]
ds_read_b64_tr_b16 v[116:117],v2 offset:10464
v_mfma_f32_16x16x32_bf16 a[0:3],v[68:71],v[36:39],a[0:3]
ds_read_b64_tr_b16 v[118:119],v2 offset:15584
v_mfma_f32_16x16x32_bf16 a[0:3],v[72:75],v[40:43],a[0:3]
ds_read_b64_tr_b16 v[122:123],v2 offset:25824
v_mfma_f32_16x16x32_bf16 a[0:3],v[76:79],v[44:47],a[0:3]
buffer_load_dwordx4 a[156:159],v28,s[28:31],s88 offen offset:3072 sc1
v_mfma_f32_16x16x32_bf16 a[4:7],v[76:79],v[180:183],a[4:7]
v_mfma_f32_16x16x32_bf16 a[4:7],v[72:75],v[164:167],a[4:7]
v_mfma_f32_16x16x32_bf16 a[4:7],v[68:71],v[148:151],a[4:7]
v_mfma_f32_16x16x32_bf16 a[4:7],v[64:67],v[132:135],a[4:7]
v_mfma_f32_16x16x32_bf16 a[8:11],v[64:67],v[196:199],a[8:11]
v_mfma_f32_16x16x32_bf16 a[8:11],v[68:71],v[212:215],a[8:11]
buffer_load_dwordx4 v25,s[48:51],s83 offen offset:1024 lds
v_mfma_f32_16x16x32_bf16 a[8:11],v[72:75],v[228:231],a[8:11]
v_mfma_f32_16x16x32_bf16 a[8:11],v[76:79],v[244:247],a[8:11]
v_mfma_f32_16x16x32_bf16 a[12:15],v[76:79],v[188:191],a[12:15]
ds_read_b64_tr_b16 v[76:77],v1 offset:30976
ds_read_b64_tr_b16 v[78:79],v1 offset:36096
v_mfma_f32_16x16x32_bf16 a[12:15],v[72:75],v[172:175],a[12:15]
ds_read_b64_tr_b16 v[72:73],v1 offset:20736
v_mfma_f32_16x16x32_bf16 a[12:15],v[68:71],v[156:159],a[12:15]
ds_read_b64_tr_b16 v[68:69],v1 offset:10496
v_mfma_f32_16x16x32_bf16 a[12:15],v[64:67],v[140:143],a[12:15]
buffer_load_dwordx4 v26,s[48:51],s83 offen offset:2048 lds
v_mfma_f32_16x16x32_bf16 a[16:19],v[80:83],v[32:35],a[16:19]
ds_read_b64_tr_b16 v[64:65],v1 offset:256
ds_read_b64_tr_b16 v[66:67],v1 offset:5376
v_mfma_f32_16x16x32_bf16 a[16:19],v[84:87],v[36:39],a[16:19]
ds_read_b64_tr_b16 v[70:71],v1 offset:15616
v_mfma_f32_16x16x32_bf16 a[16:19],v[88:91],v[40:43],a[16:19]
ds_read_b64_tr_b16 v[74:75],v1 offset:25856
v_add_u32 v1,0xffff6000,v1
v_mfma_f32_16x16x32_bf16 a[16:19],v[92:95],v[44:47],a[16:19]
v_mfma_f32_16x16x32_bf16 a[20:23],v[92:95],v[180:183],a[20:23]
buffer_load_dwordx4 a[140:143],v28,s[28:31],s87 offen offset:3072 sc1
v_mfma_f32_16x16x32_bf16 a[20:23],v[88:91],v[164:167],a[20:23]
s_add_u32 s87,s64,2
s_and_b32 s87,s87,3
s_lshl_b32 s87,s87,1
v_mfma_f32_16x16x32_bf16 a[20:23],v[84:87],v[148:151],a[20:23]
s_or_b32 s87,s87,1
s_lshl_b32 s87,s87,16
s_add_u32 s88,s87,4096
v_mfma_f32_16x16x32_bf16 a[20:23],v[80:83],v[132:135],a[20:23]
s_add_u32 s89,s87,8192
s_add_u32 s90,s87,12288
v_mfma_f32_16x16x32_bf16 a[24:27],v[80:83],v[196:199],a[24:27]
v_mfma_f32_16x16x32_bf16 a[24:27],v[84:87],v[212:215],a[24:27]
v_mfma_f32_16x16x32_bf16 a[24:27],v[88:91],v[228:231],a[24:27]
buffer_load_dwordx4 v27,s[48:51],s83 offen offset:3072 lds
v_mfma_f32_16x16x32_bf16 a[24:27],v[92:95],v[244:247],a[24:27]
s_add_u32 m0,s86,4096
v_mfma_f32_16x16x32_bf16 a[28:31],v[92:95],v[188:191],a[28:31]
ds_read_b64_tr_b16 v[92:93],v2 offset:31008
ds_read_b64_tr_b16 v[94:95],v2 offset:36128
v_mfma_f32_16x16x32_bf16 a[28:31],v[88:91],v[172:175],a[28:31]
ds_read_b64_tr_b16 v[88:89],v2 offset:20768
v_mfma_f32_16x16x32_bf16 a[28:31],v[84:87],v[156:159],a[28:31]
ds_read_b64_tr_b16 v[84:85],v2 offset:10528
v_mfma_f32_16x16x32_bf16 a[28:31],v[80:83],v[140:143],a[28:31]
s_waitcnt lgkmcnt(4)
ds_read_b64_tr_b16 v[80:81],v2 offset:288
v_mfma_f32_16x16x32_bf16 v[128:131],v[96:99],v[32:35],v[128:131]
buffer_load_dwordx4 v7,s[48:51],s83 offen lds
v_mfma_f32_16x16x32_bf16 v[128:131],v[100:103],v[36:39],v[128:131]
s_add_u32 m0,s86,5120
ds_read_b64_tr_b16 v[82:83],v2 offset:5408
ds_read_b64_tr_b16 v[86:87],v2 offset:15648
v_mfma_f32_16x16x32_bf16 v[128:131],v[104:107],v[40:43],v[128:131]
s_add_u32 s83,s64,3
ds_read_b64_tr_b16 v[90:91],v2 offset:25888
v_add_u32 v2,0xffff6000,v2
v_mfma_f32_16x16x32_bf16 v[128:131],v[108:111],v[44:47],v[128:131]
s_and_b32 s83,s83,3
s_lshl_b32 s83,s83,1
s_or_b32 s83,s83,1
v_mfma_f32_16x16x32_bf16 v[136:139],v[108:111],v[180:183],v[136:139]
s_mul_i32 s83,s83,163840
s_add_u32 s83,s83,s85
v_mfma_f32_16x16x32_bf16 v[136:139],v[104:107],v[164:167],v[136:139]
v_mfma_f32_16x16x32_bf16 v[136:139],v[100:103],v[148:151],v[136:139]
buffer_load_dwordx4 v24,s[48:51],s84 offen lds
v_mfma_f32_16x16x32_bf16 v[136:139],v[96:99],v[132:135],v[136:139]
v_mfma_f32_16x16x32_bf16 v[144:147],v[96:99],v[196:199],v[144:147]
v_mfma_f32_16x16x32_bf16 v[144:147],v[100:103],v[212:215],v[144:147]
v_mfma_f32_16x16x32_bf16 v[144:147],v[104:107],v[228:231],v[144:147]
v_mfma_f32_16x16x32_bf16 v[144:147],v[108:111],v[244:247],v[144:147]
v_mfma_f32_16x16x32_bf16 v[152:155],v[108:111],v[188:191],v[152:155]
buffer_load_dwordx4 v25,s[48:51],s84 offen offset:1024 lds
v_mfma_f32_16x16x32_bf16 v[152:155],v[104:107],v[172:175],v[152:155]
ds_read_b64_tr_b16 v[104:105],v1 offset:20480
ds_read_b64_tr_b16 v[106:107],v1 offset:25600
v_mfma_f32_16x16x32_bf16 v[152:155],v[100:103],v[156:159],v[152:155]
ds_read_b64_tr_b16 v[100:101],v1 offset:10240
v_mfma_f32_16x16x32_bf16 v[152:155],v[96:99],v[140:143],v[152:155]
ds_read_b64_tr_b16 v[96:97],v1
v_mfma_f32_16x16x32_bf16 v[160:163],v[112:115],v[32:35],v[160:163]
ds_read_b64_tr_b16 v[98:99],v1 offset:5120
v_mfma_f32_16x16x32_bf16 v[160:163],v[116:119],v[36:39],v[160:163]
ds_read_b64_tr_b16 v[102:103],v1 offset:15360
ds_read_b64_tr_b16 v[108:109],v1 offset:30720
v_mfma_f32_16x16x32_bf16 v[160:163],v[120:123],v[40:43],v[160:163]
)A"
R"A(buffer_load_dwordx4 v26,s[48:51],s84 offen offset:2048 lds
v_mfma_f32_16x16x32_bf16 v[160:163],v[124:127],v[44:47],v[160:163]
ds_read_b64_tr_b16 v[110:111],v1 offset:35840
v_mfma_f32_16x16x32_bf16 v[168:171],v[124:127],v[180:183],v[168:171]
v_mfma_f32_16x16x32_bf16 v[168:171],v[120:123],v[164:167],v[168:171]
v_mfma_f32_16x16x32_bf16 v[168:171],v[116:119],v[148:151],v[168:171]
v_mfma_f32_16x16x32_bf16 v[168:171],v[112:115],v[132:135],v[168:171]
v_mfma_f32_16x16x32_bf16 v[176:179],v[112:115],v[196:199],v[176:179]
buffer_load_dwordx4 v27,s[48:51],s84 offen offset:3072 lds
v_mfma_f32_16x16x32_bf16 v[176:179],v[116:119],v[212:215],v[176:179]
s_add_u32 m0,s86,9216
v_mfma_f32_16x16x32_bf16 v[176:179],v[120:123],v[228:231],v[176:179]
v_mfma_f32_16x16x32_bf16 v[176:179],v[124:127],v[244:247],v[176:179]
v_mfma_f32_16x16x32_bf16 v[184:187],v[124:127],v[188:191],v[184:187]
ds_read_b64_tr_b16 v[124:125],v2 offset:30752
ds_read_b64_tr_b16 v[126:127],v2 offset:35872
v_mfma_f32_16x16x32_bf16 v[184:187],v[120:123],v[172:175],v[184:187]
ds_read_b64_tr_b16 v[120:121],v2 offset:20512
v_mfma_f32_16x16x32_bf16 v[184:187],v[116:119],v[156:159],v[184:187]
buffer_load_dwordx4 v7,s[48:51],s84 offen lds
v_mfma_f32_16x16x32_bf16 v[184:187],v[112:115],v[140:143],v[184:187]
s_add_u32 m0,s86,122880
ds_read_b64_tr_b16 v[112:113],v2 offset:32
ds_read_b64_tr_b16 v[114:115],v2 offset:5152
s_nop 0
L_pd16_%=:
s_cbranch_execz L_trtg2F_%=
v_mfma_f32_16x16x32_bf16 v[192:195],v[64:67],v[32:35],v[192:195]
s_add_u32 s84,s83,20480
ds_read_b64_tr_b16 v[116:117],v2 offset:10272
v_mfma_f32_16x16x32_bf16 v[192:195],v[68:71],v[36:39],v[192:195]
ds_read_b64_tr_b16 v[118:119],v2 offset:15392
v_mfma_f32_16x16x32_bf16 v[192:195],v[72:75],v[40:43],v[192:195]
ds_read_b64_tr_b16 v[122:123],v2 offset:25632
v_mfma_f32_16x16x32_bf16 v[192:195],v[76:79],v[44:47],v[192:195]
buffer_load_dwordx4 a[68:71],v6,s[28:31],s87 offen sc1
v_mfma_f32_16x16x32_bf16 v[200:203],v[76:79],v[180:183],v[200:203]
v_mfma_f32_16x16x32_bf16 v[200:203],v[72:75],v[164:167],v[200:203]
v_mfma_f32_16x16x32_bf16 v[200:203],v[68:71],v[148:151],v[200:203]
v_mfma_f32_16x16x32_bf16 v[200:203],v[64:67],v[132:135],v[200:203]
v_mfma_f32_16x16x32_bf16 v[208:211],v[64:67],v[196:199],v[208:211]
buffer_load_dwordx4 a[100:103],v6,s[28:31],s87 offen offset:1024 sc1
v_mfma_f32_16x16x32_bf16 v[208:211],v[68:71],v[212:215],v[208:211]
v_mfma_f32_16x16x32_bf16 v[208:211],v[72:75],v[228:231],v[208:211]
v_mfma_f32_16x16x32_bf16 v[208:211],v[76:79],v[244:247],v[208:211]
v_mfma_f32_16x16x32_bf16 v[216:219],v[76:79],v[188:191],v[216:219]
ds_read_b64_tr_b16 v[76:77],v1 offset:30784
ds_read_b64_tr_b16 v[78:79],v1 offset:35904
v_mfma_f32_16x16x32_bf16 v[216:219],v[72:75],v[172:175],v[216:219]
buffer_load_dwordx2 v[4:5],v3,s[44:47],s91 offen offset:1280 sc1
v_mfma_f32_16x16x32_bf16 v[216:219],v[68:71],v[156:159],v[216:219]
ds_read_b64_tr_b16 v[68:69],v1 offset:10304
ds_read_b64_tr_b16 v[70:71],v1 offset:15424
v_mfma_f32_16x16x32_bf16 v[216:219],v[64:67],v[140:143],v[216:219]
s_waitcnt lgkmcnt(4)
ds_read_b64_tr_b16 v[64:65],v1 offset:64
v_mfma_f32_16x16x32_bf16 v[224:227],v[80:83],v[32:35],v[224:227]
ds_read_b64_tr_b16 v[66:67],v1 offset:5184
v_mfma_f32_16x16x32_bf16 v[224:227],v[84:87],v[36:39],v[224:227]
ds_read_b64_tr_b16 v[72:73],v1 offset:20544
v_mfma_f32_16x16x32_bf16 v[224:227],v[88:91],v[40:43],v[224:227]
buffer_load_dwordx4 a[64:67],v6,s[28:31],s87 offen offset:2048 sc1
v_mfma_f32_16x16x32_bf16 v[224:227],v[92:95],v[44:47],v[224:227]
ds_read_b64_tr_b16 v[74:75],v1 offset:25664
v_mfma_f32_16x16x32_bf16 v[232:235],v[92:95],v[180:183],v[232:235]
v_mfma_f32_16x16x32_bf16 v[232:235],v[88:91],v[164:167],v[232:235]
v_mfma_f32_16x16x32_bf16 v[232:235],v[84:87],v[148:151],v[232:235]
v_mfma_f32_16x16x32_bf16 v[232:235],v[80:83],v[132:135],v[232:235]
buffer_load_dwordx4 a[76:79],v6,s[28:31],s88 offen sc1
v_mfma_f32_16x16x32_bf16 v[240:243],v[80:83],v[196:199],v[240:243]
v_mfma_f32_16x16x32_bf16 v[240:243],v[84:87],v[212:215],v[240:243]
v_mfma_f32_16x16x32_bf16 v[240:243],v[88:91],v[228:231],v[240:243]
v_mfma_f32_16x16x32_bf16 v[240:243],v[92:95],v[244:247],v[240:243]
v_mfma_f32_16x16x32_bf16 v[248:251],v[92:95],v[188:191],v[248:251]
buffer_load_dwordx4 a[96:99],v28,s[28:31],s87 offen offset:3072 sc1
v_mfma_f32_16x16x32_bf16 v[248:251],v[88:91],v[172:175],v[248:251]
s_add_u32 s87,s64,3
ds_read_b64_tr_b16 v[88:89],v2 offset:20576
ds_read_b64_tr_b16 v[90:91],v2 offset:25696
v_mfma_f32_16x16x32_bf16 v[248:251],v[84:87],v[156:159],v[248:251]
s_and_b32 s87,s87,3
s_lshl_b32 s87,s87,1
ds_read_b64_tr_b16 v[84:85],v2 offset:10336
v_mfma_f32_16x16x32_bf16 v[248:251],v[80:83],v[140:143],v[248:251]
s_or_b32 s87,s87,17
s_lshl_b32 s87,s87,16
ds_read_b64_tr_b16 v[80:81],v2 offset:96
v_mfma_f32_16x16x32_bf16 a[192:195],v[96:99],a[128:131],a[192:195]
ds_read_b64_tr_b16 v[82:83],v2 offset:5216
v_mfma_f32_16x16x32_bf16 a[192:195],v[100:103],a[144:147],a[192:195]
buffer_load_dwordx4 a[72:75],v6,s[28:31],s88 offen offset:2048 sc1
v_mfma_f32_16x16x32_bf16 a[192:195],v[104:107],a[160:163],a[192:195]
s_waitcnt vmcnt(37)
ds_read_b64_tr_b16 v[86:87],v2 offset:15456
ds_read_b64_tr_b16 v[92:93],v2 offset:30816
v_mfma_f32_16x16x32_bf16 a[192:195],v[108:111],a[176:179],a[192:195]
ds_read_b64_tr_b16 v[94:95],v2 offset:35936
v_mfma_f32_16x16x32_bf16 a[196:199],v[108:111],a[180:183],a[196:199]
v_mfma_f32_16x16x32_bf16 a[196:199],v[104:107],a[164:167],a[196:199]
s_waitcnt vmcnt(33)
v_mfma_f32_16x16x32_bf16 a[196:199],v[100:103],a[148:151],a[196:199]
buffer_load_dwordx4 a[108:111],v6,s[28:31],s88 offen offset:1024 sc1
v_mfma_f32_16x16x32_bf16 a[196:199],v[96:99],a[132:135],a[196:199]
s_waitcnt vmcnt(32)
v_mfma_f32_16x16x32_bf16 a[200:203],v[96:99],a[136:139],a[200:203]
s_waitcnt vmcnt(29)
v_mfma_f32_16x16x32_bf16 a[200:203],v[100:103],a[152:155],a[200:203]
s_waitcnt vmcnt(27)
v_mfma_f32_16x16x32_bf16 a[200:203],v[104:107],a[168:171],a[200:203]
s_waitcnt vmcnt(22)
v_mfma_f32_16x16x32_bf16 a[200:203],v[108:111],a[184:187],a[200:203]
buffer_load_dwordx4 a[84:87],v6,s[28:31],s89 offen sc1
v_mfma_f32_16x16x32_bf16 a[204:207],v[108:111],a[188:191],a[204:207]
s_waitcnt vmcnt(21)
ds_read_b64_tr_b16 v[108:109],v1 offset:30848
ds_read_b64_tr_b16 v[110:111],v1 offset:35968
v_mfma_f32_16x16x32_bf16 a[204:207],v[104:107],a[172:175],a[204:207]
s_waitcnt vmcnt(19)
ds_read_b64_tr_b16 v[104:105],v1 offset:20608
v_mfma_f32_16x16x32_bf16 a[204:207],v[100:103],a[156:159],a[204:207]
s_waitcnt vmcnt(16)
ds_read_b64_tr_b16 v[100:101],v1 offset:10368
v_mfma_f32_16x16x32_bf16 a[204:207],v[96:99],a[140:143],a[204:207]
ds_read_b64_tr_b16 v[96:97],v1 offset:128
v_mfma_f32_16x16x32_bf16 a[208:211],v[112:115],a[128:131],a[208:211]
buffer_load_dwordx4 a[104:107],v28,s[28:31],s88 offen offset:3072 sc1
v_mfma_f32_16x16x32_bf16 a[208:211],v[116:119],a[144:147],a[208:211]
s_add_u32 s88,s87,4096
ds_read_b64_tr_b16 v[98:99],v1 offset:5248
ds_read_b64_tr_b16 v[102:103],v1 offset:15488
v_mfma_f32_16x16x32_bf16 a[208:211],v[120:123],a[160:163],a[208:211]
ds_read_b64_tr_b16 v[106:107],v1 offset:25728
v_mfma_f32_16x16x32_bf16 a[208:211],v[124:127],a[176:179],a[208:211]
v_mfma_f32_16x16x32_bf16 a[212:215],v[124:127],a[180:183],a[212:215]
v_mfma_f32_16x16x32_bf16 a[212:215],v[120:123],a[164:167],a[212:215]
buffer_load_dwordx4 a[80:83],v6,s[28:31],s89 offen offset:2048 sc1
v_mfma_f32_16x16x32_bf16 a[212:215],v[116:119],a[148:151],a[212:215]
v_mfma_f32_16x16x32_bf16 a[212:215],v[112:115],a[132:135],a[212:215]
v_mfma_f32_16x16x32_bf16 a[216:219],v[112:115],a[136:139],a[216:219]
v_mfma_f32_16x16x32_bf16 a[216:219],v[116:119],a[152:155],a[216:219]
v_mfma_f32_16x16x32_bf16 a[216:219],v[120:123],a[168:171],a[216:219]
buffer_load_dwordx4 a[116:119],v6,s[28:31],s89 offen offset:1024 sc1
v_mfma_f32_16x16x32_bf16 a[216:219],v[124:127],a[184:187],a[216:219]
v_mfma_f32_16x16x32_bf16 a[220:223],v[124:127],a[188:191],a[220:223]
ds_read_b64_tr_b16 v[124:125],v2 offset:30880
ds_read_b64_tr_b16 v[126:127],v2 offset:36000
v_mfma_f32_16x16x32_bf16 a[220:223],v[120:123],a[172:175],a[220:223]
ds_read_b64_tr_b16 v[120:121],v2 offset:20640
v_mfma_f32_16x16x32_bf16 a[220:223],v[116:119],a[156:159],a[220:223]
s_waitcnt lgkmcnt(3)
ds_read_b64_tr_b16 v[116:117],v2 offset:10400
v_mfma_f32_16x16x32_bf16 a[220:223],v[112:115],a[140:143],a[220:223]
buffer_load_dwordx4 a[92:95],v6,s[28:31],s90 offen sc1
v_mfma_f32_16x16x32_bf16 a[224:227],v[64:67],a[128:131],a[224:227]
ds_read_b64_tr_b16 v[112:113],v2 offset:160
ds_read_b64_tr_b16 v[114:115],v2 offset:5280
v_mfma_f32_16x16x32_bf16 a[224:227],v[68:71],a[144:147],a[224:227]
ds_read_b64_tr_b16 v[118:119],v2 offset:15520
v_mfma_f32_16x16x32_bf16 a[224:227],v[72:75],a[160:163],a[224:227]
ds_read_b64_tr_b16 v[122:123],v2 offset:25760
v_mfma_f32_16x16x32_bf16 a[224:227],v[76:79],a[176:179],a[224:227]
v_mfma_f32_16x16x32_bf16 a[228:231],v[76:79],a[180:183],a[228:231]
buffer_load_dwordx4 a[112:115],v28,s[28:31],s89 offen offset:3072 sc1
v_mfma_f32_16x16x32_bf16 a[228:231],v[72:75],a[164:167],a[228:231]
s_add_u32 s89,s87,8192
v_mfma_f32_16x16x32_bf16 a[228:231],v[68:71],a[148:151],a[228:231]
v_mfma_f32_16x16x32_bf16 a[228:231],v[64:67],a[132:135],a[228:231]
v_mfma_f32_16x16x32_bf16 a[232:235],v[64:67],a[136:139],a[232:235]
v_mfma_f32_16x16x32_bf16 a[232:235],v[68:71],a[152:155],a[232:235]
buffer_load_dwordx4 a[124:127],v6,s[28:31],s90 offen offset:1024 sc1
v_mfma_f32_16x16x32_bf16 a[232:235],v[72:75],a[168:171],a[232:235]
s_waitcnt vmcnt(12)
v_cmp_ne_u32 s[56:57],s52,v4
v_cmp_ne_u32 s[58:59],s53,v5
v_mfma_f32_16x16x32_bf16 a[232:235],v[76:79],a[184:187],a[232:235]
s_or_b64 s[56:57],s[56:57],s[58:59]
s_cbranch_scc1 L_fr3_%=
L_fo3_%=:
s_lshl_b32 s91,s64,6
v_mfma_f32_16x16x32_bf16 a[236:239],v[76:79],a[188:191],a[236:239]
ds_read_b64_tr_b16 v[76:77],v1 offset:30912
ds_read_b64_tr_b16 v[78:79],v1 offset:36032
v_mfma_f32_16x16x32_bf16 a[236:239],v[72:75],a[172:175],a[236:239]
ds_read_b64_tr_b16 v[72:73],v1 offset:20672
v_bfe_u32 v3,v6,4,2
v_mfma_f32_16x16x32_bf16 a[236:239],v[68:71],a[156:159],a[236:239]
buffer_load_dwordx4 v[32:35],v6,s[28:31],s87 offen sc1
v_mfma_f32_16x16x32_bf16 a[236:239],v[64:67],a[140:143],a[236:239]
ds_read_b64_tr_b16 v[64:65],v1 offset:192
ds_read_b64_tr_b16 v[66:67],v1 offset:5312
v_mfma_f32_16x16x32_bf16 a[240:243],v[80:83],a[128:131],a[240:243]
ds_read_b64_tr_b16 v[68:69],v1 offset:10432
v_min_u32 v3,2,v3
v_mfma_f32_16x16x32_bf16 a[240:243],v[84:87],a[144:147],a[240:243]
ds_read_b64_tr_b16 v[70:71],v1 offset:15552
v_add3_u32 v3,v3,s63,1
v_mfma_f32_16x16x32_bf16 a[240:243],v[88:91],a[160:163],a[240:243]
ds_read_b64_tr_b16 v[74:75],v1 offset:25792
v_and_b32 v3,3,v3
v_mfma_f32_16x16x32_bf16 a[240:243],v[92:95],a[176:179],a[240:243]
buffer_load_dwordx4 a[88:91],v6,s[28:31],s90 offen offset:2048 sc1
v_mfma_f32_16x16x32_bf16 a[244:247],v[92:95],a[180:183],a[244:247]
v_lshlrev_b32 v3,8,v3
v_mfma_f32_16x16x32_bf16 a[244:247],v[88:91],a[164:167],a[244:247]
v_mfma_f32_16x16x32_bf16 a[244:247],v[84:87],a[148:151],a[244:247]
v_mfma_f32_16x16x32_bf16 a[244:247],v[80:83],a[132:135],a[244:247]
v_mfma_f32_16x16x32_bf16 a[248:251],v[80:83],a[136:139],a[248:251]
buffer_load_dwordx4 v[36:39],v6,s[28:31],s88 offen sc1
v_mfma_f32_16x16x32_bf16 a[248:251],v[84:87],a[152:155],a[248:251]
v_mfma_f32_16x16x32_bf16 a[248:251],v[88:91],a[168:171],a[248:251]
v_mfma_f32_16x16x32_bf16 a[248:251],v[92:95],a[184:187],a[248:251]
v_mfma_f32_16x16x32_bf16 a[252:255],v[92:95],a[188:191],a[252:255]
ds_read_b64_tr_b16 v[92:93],v2 offset:30944
ds_read_b64_tr_b16 v[94:95],v2 offset:36064
v_mfma_f32_16x16x32_bf16 a[252:255],v[88:91],a[172:175],a[252:255]
buffer_load_dwordx4 a[120:123],v28,s[28:31],s90 offen offset:3072 sc1
v_mfma_f32_16x16x32_bf16 a[252:255],v[84:87],a[156:159],a[252:255]
s_add_u32 s90,s87,12288
ds_read_b64_tr_b16 v[84:85],v2 offset:10464
ds_read_b64_tr_b16 v[86:87],v2 offset:15584
v_mfma_f32_16x16x32_bf16 a[252:255],v[80:83],a[140:143],a[252:255]
ds_read_b64_tr_b16 v[80:81],v2 offset:224
v_mfma_f32_16x16x32_bf16 a[0:3],v[96:99],a[128:131],a[0:3]
ds_read_b64_tr_b16 v[82:83],v2 offset:5344
)A"
R"A(v_mfma_f32_16x16x32_bf16 a[0:3],v[100:103],a[144:147],a[0:3]
ds_read_b64_tr_b16 v[88:89],v2 offset:20704
v_mfma_f32_16x16x32_bf16 a[0:3],v[104:107],a[160:163],a[0:3]
buffer_load_dwordx4 v[40:43],v6,s[28:31],s89 offen sc1
v_mfma_f32_16x16x32_bf16 a[0:3],v[108:111],a[176:179],a[0:3]
ds_read_b64_tr_b16 v[90:91],v2 offset:25824
v_mfma_f32_16x16x32_bf16 a[4:7],v[108:111],a[180:183],a[4:7]
v_mfma_f32_16x16x32_bf16 a[4:7],v[104:107],a[164:167],a[4:7]
v_mfma_f32_16x16x32_bf16 a[4:7],v[100:103],a[148:151],a[4:7]
v_mfma_f32_16x16x32_bf16 a[4:7],v[96:99],a[132:135],a[4:7]
v_mfma_f32_16x16x32_bf16 a[8:11],v[96:99],a[136:139],a[8:11]
v_mfma_f32_16x16x32_bf16 a[8:11],v[100:103],a[152:155],a[8:11]
v_mfma_f32_16x16x32_bf16 a[8:11],v[104:107],a[168:171],a[8:11]
v_mfma_f32_16x16x32_bf16 a[8:11],v[108:111],a[184:187],a[8:11]
v_mfma_f32_16x16x32_bf16 a[12:15],v[108:111],a[188:191],a[12:15]
ds_read_b64_tr_b16 v[108:109],v1 offset:30976
ds_read_b64_tr_b16 v[110:111],v1 offset:36096
v_mfma_f32_16x16x32_bf16 a[12:15],v[104:107],a[172:175],a[12:15]
buffer_load_dwordx4 v[44:47],v6,s[28:31],s90 offen sc1
v_mfma_f32_16x16x32_bf16 a[12:15],v[100:103],a[156:159],a[12:15]
ds_read_b64_tr_b16 v[100:101],v1 offset:10496
ds_read_b64_tr_b16 v[102:103],v1 offset:15616
v_mfma_f32_16x16x32_bf16 a[12:15],v[96:99],a[140:143],a[12:15]
s_waitcnt lgkmcnt(4)
ds_read_b64_tr_b16 v[96:97],v1 offset:256
v_mfma_f32_16x16x32_bf16 a[16:19],v[112:115],a[128:131],a[16:19]
ds_read_b64_tr_b16 v[98:99],v1 offset:5376
v_mfma_f32_16x16x32_bf16 a[16:19],v[116:119],a[144:147],a[16:19]
ds_read_b64_tr_b16 v[104:105],v1 offset:20736
v_mfma_f32_16x16x32_bf16 a[16:19],v[120:123],a[160:163],a[16:19]
ds_read_b64_tr_b16 v[106:107],v1 offset:25856
v_add_u32 v1,0xffff6000,v1
v_mfma_f32_16x16x32_bf16 a[16:19],v[124:127],a[176:179],a[16:19]
v_mfma_f32_16x16x32_bf16 a[20:23],v[124:127],a[180:183],a[20:23]
v_mfma_f32_16x16x32_bf16 a[20:23],v[120:123],a[164:167],a[20:23]
v_mfma_f32_16x16x32_bf16 a[20:23],v[116:119],a[148:151],a[20:23]
v_mfma_f32_16x16x32_bf16 a[20:23],v[112:115],a[132:135],a[20:23]
v_mfma_f32_16x16x32_bf16 a[24:27],v[112:115],a[136:139],a[24:27]
buffer_load_dwordx4 v[180:183],v6,s[28:31],s90 offen offset:1024 sc1
v_mfma_f32_16x16x32_bf16 a[24:27],v[116:119],a[152:155],a[24:27]
v_mfma_f32_16x16x32_bf16 a[24:27],v[120:123],a[168:171],a[24:27]
v_mfma_f32_16x16x32_bf16 a[24:27],v[124:127],a[184:187],a[24:27]
v_mfma_f32_16x16x32_bf16 a[28:31],v[124:127],a[188:191],a[28:31]
ds_read_b64_tr_b16 v[124:125],v2 offset:31008
ds_read_b64_tr_b16 v[126:127],v2 offset:36128
v_mfma_f32_16x16x32_bf16 a[28:31],v[120:123],a[172:175],a[28:31]
ds_read_b64_tr_b16 v[120:121],v2 offset:20768
v_mfma_f32_16x16x32_bf16 a[28:31],v[116:119],a[156:159],a[28:31]
ds_read_b64_tr_b16 v[116:117],v2 offset:10528
v_mfma_f32_16x16x32_bf16 a[28:31],v[112:115],a[140:143],a[28:31]
ds_read_b64_tr_b16 v[112:113],v2 offset:288
v_mfma_f32_16x16x32_bf16 v[128:131],v[64:67],a[128:131],v[128:131]
ds_read_b64_tr_b16 v[114:115],v2 offset:5408
ds_read_b64_tr_b16 v[118:119],v2 offset:15648
v_mfma_f32_16x16x32_bf16 v[128:131],v[68:71],a[144:147],v[128:131]
ds_read_b64_tr_b16 v[122:123],v2 offset:25888
v_add_u32 v2,0xffff6000,v2
v_mfma_f32_16x16x32_bf16 v[128:131],v[72:75],a[160:163],v[128:131]
v_mfma_f32_16x16x32_bf16 v[128:131],v[76:79],a[176:179],v[128:131]
buffer_load_dwordx4 v[164:167],v6,s[28:31],s89 offen offset:1024 sc1
v_mfma_f32_16x16x32_bf16 v[136:139],v[76:79],a[180:183],v[136:139]
v_mfma_f32_16x16x32_bf16 v[136:139],v[72:75],a[164:167],v[136:139]
v_mfma_f32_16x16x32_bf16 v[136:139],v[68:71],a[148:151],v[136:139]
v_mfma_f32_16x16x32_bf16 v[136:139],v[64:67],a[132:135],v[136:139]
v_mfma_f32_16x16x32_bf16 v[144:147],v[64:67],a[136:139],v[144:147]
v_mfma_f32_16x16x32_bf16 v[144:147],v[68:71],a[152:155],v[144:147]
v_mfma_f32_16x16x32_bf16 v[144:147],v[72:75],a[168:171],v[144:147]
v_mfma_f32_16x16x32_bf16 v[144:147],v[76:79],a[184:187],v[144:147]
v_mfma_f32_16x16x32_bf16 v[152:155],v[76:79],a[188:191],v[152:155]
v_mfma_f32_16x16x32_bf16 v[152:155],v[72:75],a[172:175],v[152:155]
v_mfma_f32_16x16x32_bf16 v[152:155],v[68:71],a[156:159],v[152:155]
buffer_load_dwordx4 v[148:151],v6,s[28:31],s88 offen offset:1024 sc1
v_mfma_f32_16x16x32_bf16 v[152:155],v[64:67],a[140:143],v[152:155]
s_barrier
v_mfma_f32_16x16x32_bf16 v[160:163],v[80:83],a[128:131],v[160:163]
ds_read_b64_tr_b16 v[64:65],v1
ds_read_b64_tr_b16 v[66:67],v1 offset:5120
v_mfma_f32_16x16x32_bf16 v[160:163],v[84:87],a[144:147],v[160:163]
ds_read_b64_tr_b16 v[68:69],v1 offset:10240
v_mfma_f32_16x16x32_bf16 v[160:163],v[88:91],a[160:163],v[160:163]
ds_read_b64_tr_b16 v[70:71],v1 offset:15360
v_mfma_f32_16x16x32_bf16 v[160:163],v[92:95],a[176:179],v[160:163]
buffer_load_dwordx4 v24,s[48:51],s83 offen lds
v_mfma_f32_16x16x32_bf16 v[168:171],v[92:95],a[180:183],v[168:171]
ds_read_b64_tr_b16 v[72:73],v1 offset:20480
ds_read_b64_tr_b16 v[74:75],v1 offset:25600
v_mfma_f32_16x16x32_bf16 v[168:171],v[88:91],a[164:167],v[168:171]
ds_read_b64_tr_b16 v[76:77],v1 offset:30720
v_mfma_f32_16x16x32_bf16 v[168:171],v[84:87],a[148:151],v[168:171]
ds_read_b64_tr_b16 v[78:79],v1 offset:35840
v_mfma_f32_16x16x32_bf16 v[168:171],v[80:83],a[132:135],v[168:171]
v_mfma_f32_16x16x32_bf16 v[176:179],v[80:83],a[136:139],v[176:179]
buffer_load_dwordx4 v[132:135],v6,s[28:31],s87 offen offset:1024 sc1
v_mfma_f32_16x16x32_bf16 v[176:179],v[84:87],a[152:155],v[176:179]
v_mfma_f32_16x16x32_bf16 v[176:179],v[88:91],a[168:171],v[176:179]
v_mfma_f32_16x16x32_bf16 v[176:179],v[92:95],a[184:187],v[176:179]
v_mfma_f32_16x16x32_bf16 v[184:187],v[92:95],a[188:191],v[184:187]
ds_read_b64_tr_b16 v[92:93],v2 offset:30752
ds_read_b64_tr_b16 v[94:95],v2 offset:35872
v_mfma_f32_16x16x32_bf16 v[184:187],v[88:91],a[172:175],v[184:187]
ds_read_b64_tr_b16 v[88:89],v2 offset:20512
v_mfma_f32_16x16x32_bf16 v[184:187],v[84:87],a[156:159],v[184:187]
buffer_load_dwordx4 v25,s[48:51],s83 offen offset:1024 lds
v_mfma_f32_16x16x32_bf16 v[184:187],v[80:83],a[140:143],v[184:187]
s_waitcnt lgkmcnt(3)
ds_read_b64_tr_b16 v[80:81],v2 offset:32
ds_read_b64_tr_b16 v[82:83],v2 offset:5152
v_mfma_f32_16x16x32_bf16 v[192:195],v[96:99],a[128:131],v[192:195]
ds_read_b64_tr_b16 v[84:85],v2 offset:10272
v_mfma_f32_16x16x32_bf16 v[192:195],v[100:103],a[144:147],v[192:195]
ds_read_b64_tr_b16 v[86:87],v2 offset:15392
v_mfma_f32_16x16x32_bf16 v[192:195],v[104:107],a[160:163],v[192:195]
ds_read_b64_tr_b16 v[90:91],v2 offset:25632
v_mfma_f32_16x16x32_bf16 v[192:195],v[108:111],a[176:179],v[192:195]
v_mfma_f32_16x16x32_bf16 v[200:203],v[108:111],a[180:183],v[200:203]
buffer_load_dwordx4 v26,s[48:51],s83 offen offset:2048 lds
v_mfma_f32_16x16x32_bf16 v[200:203],v[104:107],a[164:167],v[200:203]
v_mfma_f32_16x16x32_bf16 v[200:203],v[100:103],a[148:151],v[200:203]
v_mfma_f32_16x16x32_bf16 v[200:203],v[96:99],a[132:135],v[200:203]
v_mfma_f32_16x16x32_bf16 v[208:211],v[96:99],a[136:139],v[208:211]
v_mfma_f32_16x16x32_bf16 v[208:211],v[100:103],a[152:155],v[208:211]
buffer_load_dwordx4 v[196:199],v6,s[28:31],s87 offen offset:2048 sc1
v_mfma_f32_16x16x32_bf16 v[208:211],v[104:107],a[168:171],v[208:211]
v_mfma_f32_16x16x32_bf16 v[208:211],v[108:111],a[184:187],v[208:211]
v_mfma_f32_16x16x32_bf16 v[216:219],v[108:111],a[188:191],v[216:219]
ds_read_b64_tr_b16 v[108:109],v1 offset:30784
ds_read_b64_tr_b16 v[110:111],v1 offset:35904
v_mfma_f32_16x16x32_bf16 v[216:219],v[104:107],a[172:175],v[216:219]
ds_read_b64_tr_b16 v[104:105],v1 offset:20544
v_mfma_f32_16x16x32_bf16 v[216:219],v[100:103],a[156:159],v[216:219]
ds_read_b64_tr_b16 v[100:101],v1 offset:10304
v_mfma_f32_16x16x32_bf16 v[216:219],v[96:99],a[140:143],v[216:219]
buffer_load_dwordx4 v27,s[48:51],s83 offen offset:3072 lds
v_mfma_f32_16x16x32_bf16 v[224:227],v[112:115],a[128:131],v[224:227]
s_add_u32 m0,s86,126976
ds_read_b64_tr_b16 v[96:97],v1 offset:64
ds_read_b64_tr_b16 v[98:99],v1 offset:5184
v_mfma_f32_16x16x32_bf16 v[224:227],v[116:119],a[144:147],v[224:227]
ds_read_b64_tr_b16 v[102:103],v1 offset:15424
v_mfma_f32_16x16x32_bf16 v[224:227],v[120:123],a[160:163],v[224:227]
ds_read_b64_tr_b16 v[106:107],v1 offset:25664
v_mfma_f32_16x16x32_bf16 v[224:227],v[124:127],a[176:179],v[224:227]
v_mfma_f32_16x16x32_bf16 v[232:235],v[124:127],a[180:183],v[232:235]
buffer_load_dwordx4 v[212:215],v6,s[28:31],s88 offen offset:2048 sc1
v_mfma_f32_16x16x32_bf16 v[232:235],v[120:123],a[164:167],v[232:235]
v_mfma_f32_16x16x32_bf16 v[232:235],v[116:119],a[148:151],v[232:235]
v_mfma_f32_16x16x32_bf16 v[232:235],v[112:115],a[132:135],v[232:235]
v_mfma_f32_16x16x32_bf16 v[240:243],v[112:115],a[136:139],v[240:243]
v_mfma_f32_16x16x32_bf16 v[240:243],v[116:119],a[152:155],v[240:243]
v_mfma_f32_16x16x32_bf16 v[240:243],v[120:123],a[168:171],v[240:243]
buffer_load_dwordx4 v7,s[48:51],s83 offen lds
v_mfma_f32_16x16x32_bf16 v[240:243],v[124:127],a[184:187],v[240:243]
s_add_u32 m0,s86,128000
s_mul_i32 s83,s63,20480
v_mfma_f32_16x16x32_bf16 v[248:251],v[124:127],a[188:191],v[248:251]
ds_read_b64_tr_b16 v[124:125],v2 offset:30816
ds_read_b64_tr_b16 v[126:127],v2 offset:35936
v_mfma_f32_16x16x32_bf16 v[248:251],v[120:123],a[172:175],v[248:251]
ds_read_b64_tr_b16 v[120:121],v2 offset:20576
v_mfma_f32_16x16x32_bf16 v[248:251],v[116:119],a[156:159],v[248:251]
ds_read_b64_tr_b16 v[116:117],v2 offset:10336
v_mfma_f32_16x16x32_bf16 v[248:251],v[112:115],a[140:143],v[248:251]
buffer_load_dwordx4 v[228:231],v6,s[28:31],s89 offen offset:2048 sc1
v_mfma_f32_16x16x32_bf16 a[192:195],v[64:67],a[68:71],a[192:195]
s_waitcnt vmcnt(17)
ds_read_b64_tr_b16 v[112:113],v2 offset:96
ds_read_b64_tr_b16 v[114:115],v2 offset:5216
v_mfma_f32_16x16x32_bf16 a[192:195],v[68:71],a[76:79],a[192:195]
ds_read_b64_tr_b16 v[118:119],v2 offset:15456
v_mfma_f32_16x16x32_bf16 a[192:195],v[72:75],a[84:87],a[192:195]
ds_read_b64_tr_b16 v[122:123],v2 offset:25696
v_mfma_f32_16x16x32_bf16 a[192:195],v[76:79],a[92:95],a[192:195]
v_mfma_f32_16x16x32_bf16 a[196:199],v[76:79],a[124:127],a[196:199]
v_mfma_f32_16x16x32_bf16 a[196:199],v[72:75],a[116:119],a[196:199]
buffer_load_dwordx4 v24,s[48:51],s84 offen lds
v_mfma_f32_16x16x32_bf16 a[196:199],v[68:71],a[108:111],a[196:199]
v_mfma_f32_16x16x32_bf16 a[196:199],v[64:67],a[100:103],a[196:199]
v_mfma_f32_16x16x32_bf16 a[200:203],v[64:67],a[64:67],a[200:203]
v_mfma_f32_16x16x32_bf16 a[200:203],v[68:71],a[72:75],a[200:203]
s_waitcnt vmcnt(16)
v_mfma_f32_16x16x32_bf16 a[200:203],v[72:75],a[80:83],a[200:203]
buffer_load_dwordx4 v[244:247],v6,s[28:31],s90 offen offset:2048 sc1
v_mfma_f32_16x16x32_bf16 a[200:203],v[76:79],a[88:91],a[200:203]
s_waitcnt vmcnt(16)
v_mfma_f32_16x16x32_bf16 a[204:207],v[76:79],a[120:123],a[204:207]
ds_read_b64_tr_b16 v[76:77],v1 offset:30848
ds_read_b64_tr_b16 v[78:79],v1 offset:35968
v_mfma_f32_16x16x32_bf16 a[204:207],v[72:75],a[112:115],a[204:207]
ds_read_b64_tr_b16 v[72:73],v1 offset:20608
v_mfma_f32_16x16x32_bf16 a[204:207],v[68:71],a[104:107],a[204:207]
ds_read_b64_tr_b16 v[68:69],v1 offset:10368
v_mfma_f32_16x16x32_bf16 a[204:207],v[64:67],a[96:99],a[204:207]
s_waitcnt lgkmcnt(4)
ds_read_b64_tr_b16 v[64:65],v1 offset:128
v_mfma_f32_16x16x32_bf16 a[208:211],v[80:83],a[68:71],a[208:211]
buffer_load_dwordx4 v25,s[48:51],s84 offen offset:1024 lds
v_mfma_f32_16x16x32_bf16 a[208:211],v[84:87],a[76:79],a[208:211]
ds_read_b64_tr_b16 v[66:67],v1 offset:5248
ds_read_b64_tr_b16 v[70:71],v1 offset:15488
v_mfma_f32_16x16x32_bf16 a[208:211],v[88:91],a[84:87],a[208:211]
ds_read_b64_tr_b16 v[74:75],v1 offset:25728
v_mfma_f32_16x16x32_bf16 a[208:211],v[92:95],a[92:95],a[208:211]
v_mfma_f32_16x16x32_bf16 a[212:215],v[92:95],a[124:127],a[212:215]
v_mfma_f32_16x16x32_bf16 a[212:215],v[88:91],a[116:119],a[212:215]
buffer_load_dwordx4 v[188:191],v28,s[28:31],s90 offen offset:3072 sc1
v_mfma_f32_16x16x32_bf16 a[212:215],v[84:87],a[108:111],a[212:215]
v_mfma_f32_16x16x32_bf16 a[212:215],v[80:83],a[100:103],a[212:215]
v_mfma_f32_16x16x32_bf16 a[216:219],v[80:83],a[64:67],a[216:219]
v_mfma_f32_16x16x32_bf16 a[216:219],v[84:87],a[72:75],a[216:219]
v_mfma_f32_16x16x32_bf16 a[216:219],v[88:91],a[80:83],a[216:219]
)A"
R"A(v_mfma_f32_16x16x32_bf16 a[216:219],v[92:95],a[88:91],a[216:219]
buffer_load_dwordx4 v26,s[48:51],s84 offen offset:2048 lds
v_mfma_f32_16x16x32_bf16 a[220:223],v[92:95],a[120:123],a[220:223]
ds_read_b64_tr_b16 v[92:93],v2 offset:30880
ds_read_b64_tr_b16 v[94:95],v2 offset:36000
v_mfma_f32_16x16x32_bf16 a[220:223],v[88:91],a[112:115],a[220:223]
ds_read_b64_tr_b16 v[88:89],v2 offset:20640
v_mfma_f32_16x16x32_bf16 a[220:223],v[84:87],a[104:107],a[220:223]
ds_read_b64_tr_b16 v[84:85],v2 offset:10400
v_mfma_f32_16x16x32_bf16 a[220:223],v[80:83],a[96:99],a[220:223]
ds_read_b64_tr_b16 v[80:81],v2 offset:160
v_mfma_f32_16x16x32_bf16 a[224:227],v[96:99],a[68:71],a[224:227]
ds_read_b64_tr_b16 v[82:83],v2 offset:5280
ds_read_b64_tr_b16 v[86:87],v2 offset:15520
v_mfma_f32_16x16x32_bf16 a[224:227],v[100:103],a[76:79],a[224:227]
buffer_load_dwordx4 v27,s[48:51],s84 offen offset:3072 lds
v_mfma_f32_16x16x32_bf16 a[224:227],v[104:107],a[84:87],a[224:227]
s_add_u32 m0,s86,132096
ds_read_b64_tr_b16 v[90:91],v2 offset:25760
v_mfma_f32_16x16x32_bf16 a[224:227],v[108:111],a[92:95],a[224:227]
v_mfma_f32_16x16x32_bf16 a[228:231],v[108:111],a[124:127],a[228:231]
v_mfma_f32_16x16x32_bf16 a[228:231],v[104:107],a[116:119],a[228:231]
v_mfma_f32_16x16x32_bf16 a[228:231],v[100:103],a[108:111],a[228:231]
buffer_load_dwordx4 v[172:175],v28,s[28:31],s89 offen offset:3072 sc1
v_mfma_f32_16x16x32_bf16 a[228:231],v[96:99],a[100:103],a[228:231]
v_mfma_f32_16x16x32_bf16 a[232:235],v[96:99],a[64:67],a[232:235]
v_mfma_f32_16x16x32_bf16 a[232:235],v[100:103],a[72:75],a[232:235]
v_mfma_f32_16x16x32_bf16 a[232:235],v[104:107],a[80:83],a[232:235]
v_mfma_f32_16x16x32_bf16 a[232:235],v[108:111],a[88:91],a[232:235]
v_mfma_f32_16x16x32_bf16 a[236:239],v[108:111],a[120:123],a[236:239]
buffer_load_dwordx4 v7,s[48:51],s84 offen lds
v_mfma_f32_16x16x32_bf16 a[236:239],v[104:107],a[112:115],a[236:239]
ds_read_b64_tr_b16 v[104:105],v1 offset:20672
ds_read_b64_tr_b16 v[106:107],v1 offset:25792
v_mfma_f32_16x16x32_bf16 a[236:239],v[100:103],a[104:107],a[236:239]
ds_read_b64_tr_b16 v[100:101],v1 offset:10432
v_mfma_f32_16x16x32_bf16 a[236:239],v[96:99],a[96:99],a[236:239]
ds_read_b64_tr_b16 v[96:97],v1 offset:192
v_mfma_f32_16x16x32_bf16 a[240:243],v[112:115],a[68:71],a[240:243]
ds_read_b64_tr_b16 v[98:99],v1 offset:5312
v_mfma_f32_16x16x32_bf16 a[240:243],v[116:119],a[76:79],a[240:243]
buffer_load_dwordx4 v[156:159],v28,s[28:31],s88 offen offset:3072 sc1
v_mfma_f32_16x16x32_bf16 a[240:243],v[120:123],a[84:87],a[240:243]
ds_read_b64_tr_b16 v[102:103],v1 offset:15552
ds_read_b64_tr_b16 v[108:109],v1 offset:30912
v_mfma_f32_16x16x32_bf16 a[240:243],v[124:127],a[92:95],a[240:243]
ds_read_b64_tr_b16 v[110:111],v1 offset:36032
v_add_u32 v1,0xffff6000,v1
v_mfma_f32_16x16x32_bf16 a[244:247],v[124:127],a[124:127],a[244:247]
v_mfma_f32_16x16x32_bf16 a[244:247],v[120:123],a[116:119],a[244:247]
v_mfma_f32_16x16x32_bf16 a[244:247],v[116:119],a[108:111],a[244:247]
buffer_load_dwordx4 v[140:143],v28,s[28:31],s87 offen offset:3072 sc1
v_mfma_f32_16x16x32_bf16 a[244:247],v[112:115],a[100:103],a[244:247]
s_add_u32 s87,s64,3
s_and_b32 s87,s87,3
s_lshl_b32 s87,s87,1
s_or_b32 s87,s87,1
s_lshl_b32 s87,s87,16
s_add_u32 s88,s87,4096
s_add_u32 s89,s87,8192
s_add_u32 s90,s87,12288
v_mfma_f32_16x16x32_bf16 a[248:251],v[112:115],a[64:67],a[248:251]
v_mfma_f32_16x16x32_bf16 a[248:251],v[116:119],a[72:75],a[248:251]
v_mfma_f32_16x16x32_bf16 a[248:251],v[120:123],a[80:83],a[248:251]
v_mfma_f32_16x16x32_bf16 a[248:251],v[124:127],a[88:91],a[248:251]
buffer_load_dwordx4 a[128:131],v6,s[28:31],s87 offen sc1
v_mfma_f32_16x16x32_bf16 a[252:255],v[124:127],a[120:123],a[252:255]
ds_read_b64_tr_b16 v[124:125],v2 offset:30944
ds_read_b64_tr_b16 v[126:127],v2 offset:36064
v_mfma_f32_16x16x32_bf16 a[252:255],v[120:123],a[112:115],a[252:255]
ds_read_b64_tr_b16 v[120:121],v2 offset:20704
v_mfma_f32_16x16x32_bf16 a[252:255],v[116:119],a[104:107],a[252:255]
ds_read_b64_tr_b16 v[116:117],v2 offset:10464
v_mfma_f32_16x16x32_bf16 a[252:255],v[112:115],a[96:99],a[252:255]
s_waitcnt lgkmcnt(4)
ds_read_b64_tr_b16 v[112:113],v2 offset:224
v_mfma_f32_16x16x32_bf16 a[0:3],v[64:67],a[68:71],a[0:3]
buffer_load_dwordx4 a[144:147],v6,s[28:31],s88 offen sc1
v_mfma_f32_16x16x32_bf16 a[0:3],v[68:71],a[76:79],a[0:3]
ds_read_b64_tr_b16 v[114:115],v2 offset:5344
ds_read_b64_tr_b16 v[118:119],v2 offset:15584
v_mfma_f32_16x16x32_bf16 a[0:3],v[72:75],a[84:87],a[0:3]
ds_read_b64_tr_b16 v[122:123],v2 offset:25824
v_add_u32 v2,0xffff6000,v2
v_mfma_f32_16x16x32_bf16 a[0:3],v[76:79],a[92:95],a[0:3]
v_mfma_f32_16x16x32_bf16 a[4:7],v[76:79],a[124:127],a[4:7]
v_mfma_f32_16x16x32_bf16 a[4:7],v[72:75],a[116:119],a[4:7]
buffer_load_dwordx4 a[160:163],v6,s[28:31],s89 offen sc1
v_mfma_f32_16x16x32_bf16 a[4:7],v[68:71],a[108:111],a[4:7]
v_mfma_f32_16x16x32_bf16 a[4:7],v[64:67],a[100:103],a[4:7]
v_mfma_f32_16x16x32_bf16 a[8:11],v[64:67],a[64:67],a[8:11]
v_mfma_f32_16x16x32_bf16 a[8:11],v[68:71],a[72:75],a[8:11]
v_mfma_f32_16x16x32_bf16 a[8:11],v[72:75],a[80:83],a[8:11]
buffer_load_dwordx4 a[176:179],v6,s[28:31],s90 offen sc1
v_mfma_f32_16x16x32_bf16 a[8:11],v[76:79],a[88:91],a[8:11]
v_mfma_f32_16x16x32_bf16 a[12:15],v[76:79],a[120:123],a[12:15]
ds_read_b64_tr_b16 v[76:77],v1 offset:30720
ds_read_b64_tr_b16 v[78:79],v1 offset:35840
v_mfma_f32_16x16x32_bf16 a[12:15],v[72:75],a[112:115],a[12:15]
ds_read_b64_tr_b16 v[72:73],v1 offset:20480
v_mfma_f32_16x16x32_bf16 a[12:15],v[68:71],a[104:107],a[12:15]
ds_read_b64_tr_b16 v[68:69],v1 offset:10240
v_mfma_f32_16x16x32_bf16 a[12:15],v[64:67],a[96:99],a[12:15]
buffer_load_dwordx4 a[180:183],v6,s[28:31],s90 offen offset:1024 sc1
v_mfma_f32_16x16x32_bf16 a[16:19],v[80:83],a[68:71],a[16:19]
ds_read_b64_tr_b16 v[64:65],v1
ds_read_b64_tr_b16 v[66:67],v1 offset:5120
v_mfma_f32_16x16x32_bf16 a[16:19],v[84:87],a[76:79],a[16:19]
ds_read_b64_tr_b16 v[70:71],v1 offset:15360
v_mfma_f32_16x16x32_bf16 a[16:19],v[88:91],a[84:87],a[16:19]
ds_read_b64_tr_b16 v[74:75],v1 offset:25600
v_add_u32 v1,0x1e000,v1
v_mfma_f32_16x16x32_bf16 a[16:19],v[92:95],a[92:95],a[16:19]
v_mfma_f32_16x16x32_bf16 a[20:23],v[92:95],a[124:127],a[20:23]
buffer_load_dwordx4 a[164:167],v6,s[28:31],s89 offen offset:1024 sc1
v_mfma_f32_16x16x32_bf16 a[20:23],v[88:91],a[116:119],a[20:23]
v_mfma_f32_16x16x32_bf16 a[20:23],v[84:87],a[108:111],a[20:23]
v_mfma_f32_16x16x32_bf16 a[20:23],v[80:83],a[100:103],a[20:23]
v_mfma_f32_16x16x32_bf16 a[24:27],v[80:83],a[64:67],a[24:27]
v_mfma_f32_16x16x32_bf16 a[24:27],v[84:87],a[72:75],a[24:27]
buffer_load_dwordx4 a[148:151],v6,s[28:31],s88 offen offset:1024 sc1
v_mfma_f32_16x16x32_bf16 a[24:27],v[88:91],a[80:83],a[24:27]
v_mfma_f32_16x16x32_bf16 a[24:27],v[92:95],a[88:91],a[24:27]
s_nop 0
L_pd17_%=:
s_cbranch_execz L_trtg2F_%=
v_mfma_f32_16x16x32_bf16 a[28:31],v[92:95],a[120:123],a[28:31]
ds_read_b64_tr_b16 v[92:93],v2 offset:30752
ds_read_b64_tr_b16 v[94:95],v2 offset:35872
v_mfma_f32_16x16x32_bf16 a[28:31],v[88:91],a[112:115],a[28:31]
ds_read_b64_tr_b16 v[88:89],v2 offset:20512
v_mfma_f32_16x16x32_bf16 a[28:31],v[84:87],a[104:107],a[28:31]
buffer_load_dwordx4 a[132:135],v6,s[28:31],s87 offen offset:1024 sc1
v_mfma_f32_16x16x32_bf16 a[28:31],v[80:83],a[96:99],a[28:31]
ds_read_b64_tr_b16 v[80:81],v2 offset:32
ds_read_b64_tr_b16 v[82:83],v2 offset:5152
v_mfma_f32_16x16x32_bf16 v[128:131],v[96:99],a[68:71],v[128:131]
ds_read_b64_tr_b16 v[84:85],v2 offset:10272
v_mfma_f32_16x16x32_bf16 v[128:131],v[100:103],a[76:79],v[128:131]
ds_read_b64_tr_b16 v[86:87],v2 offset:15392
v_mfma_f32_16x16x32_bf16 v[128:131],v[104:107],a[84:87],v[128:131]
ds_read_b64_tr_b16 v[90:91],v2 offset:25632
v_add_u32 v2,0x1e000,v2
v_mfma_f32_16x16x32_bf16 v[128:131],v[108:111],a[92:95],v[128:131]
buffer_load_dwordx4 a[136:139],v6,s[28:31],s87 offen offset:2048 sc1
v_mfma_f32_16x16x32_bf16 v[136:139],v[108:111],a[124:127],v[136:139]
v_mfma_f32_16x16x32_bf16 v[136:139],v[104:107],a[116:119],v[136:139]
v_mfma_f32_16x16x32_bf16 v[136:139],v[100:103],a[108:111],v[136:139]
v_mfma_f32_16x16x32_bf16 v[136:139],v[96:99],a[100:103],v[136:139]
v_mfma_f32_16x16x32_bf16 v[144:147],v[96:99],a[64:67],v[144:147]
buffer_load_dwordx4 a[152:155],v6,s[28:31],s88 offen offset:2048 sc1
v_mfma_f32_16x16x32_bf16 v[144:147],v[100:103],a[72:75],v[144:147]
v_mfma_f32_16x16x32_bf16 v[144:147],v[104:107],a[80:83],v[144:147]
v_mfma_f32_16x16x32_bf16 v[144:147],v[108:111],a[88:91],v[144:147]
v_mfma_f32_16x16x32_bf16 v[152:155],v[108:111],a[120:123],v[152:155]
v_mfma_f32_16x16x32_bf16 v[152:155],v[104:107],a[112:115],v[152:155]
buffer_load_dwordx4 a[168:171],v6,s[28:31],s89 offen offset:2048 sc1
v_mfma_f32_16x16x32_bf16 v[152:155],v[100:103],a[104:107],v[152:155]
v_mfma_f32_16x16x32_bf16 v[152:155],v[96:99],a[96:99],v[152:155]
s_waitcnt vmcnt(13)
s_barrier
s_waitcnt lgkmcnt(0)
v_mfma_f32_16x16x32_bf16 v[160:163],v[112:115],a[68:71],v[160:163]
ds_read_b64_tr_b16 v[96:97],v1
ds_read_b64_tr_b16 v[98:99],v1 offset:5120
v_mfma_f32_16x16x32_bf16 v[160:163],v[116:119],a[76:79],v[160:163]
buffer_load_dwordx4 a[184:187],v6,s[28:31],s90 offen offset:2048 sc1
v_mfma_f32_16x16x32_bf16 v[160:163],v[120:123],a[84:87],v[160:163]
ds_read_b64_tr_b16 v[100:101],v1 offset:10240
ds_read_b64_tr_b16 v[102:103],v1 offset:15360
v_mfma_f32_16x16x32_bf16 v[160:163],v[124:127],a[92:95],v[160:163]
ds_read_b64_tr_b16 v[104:105],v1 offset:20480
v_mfma_f32_16x16x32_bf16 v[168:171],v[124:127],a[124:127],v[168:171]
ds_read_b64_tr_b16 v[106:107],v1 offset:25600
v_mfma_f32_16x16x32_bf16 v[168:171],v[120:123],a[116:119],v[168:171]
ds_read_b64_tr_b16 v[108:109],v1 offset:30720
v_mfma_f32_16x16x32_bf16 v[168:171],v[116:119],a[108:111],v[168:171]
buffer_load_dwordx4 a[188:191],v28,s[28:31],s90 offen offset:3072 sc1
v_mfma_f32_16x16x32_bf16 v[168:171],v[112:115],a[100:103],v[168:171]
ds_read_b64_tr_b16 v[110:111],v1 offset:35840
v_add_u32 v1,0xfffe2000,v1
v_mfma_f32_16x16x32_bf16 v[176:179],v[112:115],a[64:67],v[176:179]
v_mfma_f32_16x16x32_bf16 v[176:179],v[116:119],a[72:75],v[176:179]
v_mfma_f32_16x16x32_bf16 v[176:179],v[120:123],a[80:83],v[176:179]
v_mfma_f32_16x16x32_bf16 v[176:179],v[124:127],a[88:91],v[176:179]
buffer_load_dwordx4 a[172:175],v28,s[28:31],s89 offen offset:3072 sc1
v_mfma_f32_16x16x32_bf16 v[184:187],v[124:127],a[120:123],v[184:187]
ds_read_b64_tr_b16 v[124:125],v2 offset:30752
ds_read_b64_tr_b16 v[126:127],v2 offset:35872
v_mfma_f32_16x16x32_bf16 v[184:187],v[120:123],a[112:115],v[184:187]
ds_read_b64_tr_b16 v[120:121],v2 offset:20512
v_mfma_f32_16x16x32_bf16 v[184:187],v[116:119],a[104:107],v[184:187]
ds_read_b64_tr_b16 v[116:117],v2 offset:10272
v_mfma_f32_16x16x32_bf16 v[184:187],v[112:115],a[96:99],v[184:187]
ds_read_b64_tr_b16 v[112:113],v2 offset:32
v_mfma_f32_16x16x32_bf16 a[192:195],v[64:67],v[32:35],a[192:195]
buffer_load_dwordx4 a[156:159],v28,s[28:31],s88 offen offset:3072 sc1
v_mfma_f32_16x16x32_bf16 a[192:195],v[68:71],v[36:39],a[192:195]
ds_read_b64_tr_b16 v[114:115],v2 offset:5152
ds_read_b64_tr_b16 v[118:119],v2 offset:15392
v_mfma_f32_16x16x32_bf16 a[192:195],v[72:75],v[40:43],a[192:195]
ds_read_b64_tr_b16 v[122:123],v2 offset:25632
v_add_u32 v2,0xfffe2000,v2
v_mfma_f32_16x16x32_bf16 a[192:195],v[76:79],v[44:47],a[192:195]
v_mfma_f32_16x16x32_bf16 a[196:199],v[76:79],v[180:183],a[196:199]
v_mfma_f32_16x16x32_bf16 a[196:199],v[72:75],v[164:167],a[196:199]
buffer_load_dwordx4 a[140:143],v28,s[28:31],s87 offen offset:3072 sc1
v_mfma_f32_16x16x32_bf16 a[196:199],v[68:71],v[148:151],a[196:199]
s_mul_i32 s87,s60,81920
s_add_u32 s87,s87,s66
s_add_u32 s83,s83,s87
v_mfma_f32_16x16x32_bf16 a[196:199],v[64:67],v[132:135],a[196:199]
s_lshl_b32 s66,s63,6
s_add_u32 s87,s87,s66
s_add_u32 s88,s87,20480
v_mfma_f32_16x16x32_bf16 a[200:203],v[64:67],v[196:199],a[200:203]
s_add_u32 s89,s87,40960
s_add_u32 s90,s87,61440
s_add_u32 s66,s77,4096
v_mfma_f32_16x16x32_bf16 a[200:203],v[68:71],v[212:215],a[200:203]
v_mfma_f32_16x16x32_bf16 a[200:203],v[72:75],v[228:231],a[200:203]
v_mfma_f32_16x16x32_bf16 a[200:203],v[76:79],v[244:247],a[200:203]
v_mfma_f32_16x16x32_bf16 a[204:207],v[76:79],v[188:191],a[204:207]
)A"
R"A(ds_read_b64_tr_b16 v[76:77],v1 offset:30784
ds_read_b64_tr_b16 v[78:79],v1 offset:35904
v_mfma_f32_16x16x32_bf16 a[204:207],v[72:75],v[172:175],a[204:207]
s_waitcnt vmcnt(17)
ds_read_b64_tr_b16 v[72:73],v1 offset:20544
v_mfma_f32_16x16x32_bf16 a[204:207],v[68:71],v[156:159],a[204:207]
s_waitcnt vmcnt(16)
ds_read_b64_tr_b16 v[68:69],v1 offset:10304
v_mfma_f32_16x16x32_bf16 a[204:207],v[64:67],v[140:143],a[204:207]
ds_read_b64_tr_b16 v[64:65],v1 offset:64
v_mfma_f32_16x16x32_bf16 a[208:211],v[80:83],v[32:35],a[208:211]
ds_read_b64_tr_b16 v[66:67],v1 offset:5184
ds_read_b64_tr_b16 v[70:71],v1 offset:15424
v_mfma_f32_16x16x32_bf16 a[208:211],v[84:87],v[36:39],a[208:211]
ds_read_b64_tr_b16 v[74:75],v1 offset:25664
v_add_u32 v1,0x1e000,v1
v_mfma_f32_16x16x32_bf16 a[208:211],v[88:91],v[40:43],a[208:211]
v_mfma_f32_16x16x32_bf16 a[208:211],v[92:95],v[44:47],a[208:211]
v_mfma_f32_16x16x32_bf16 a[212:215],v[92:95],v[180:183],a[212:215]
v_mfma_f32_16x16x32_bf16 a[212:215],v[88:91],v[164:167],a[212:215]
v_mfma_f32_16x16x32_bf16 a[212:215],v[84:87],v[148:151],a[212:215]
v_mfma_f32_16x16x32_bf16 a[212:215],v[80:83],v[132:135],a[212:215]
v_mfma_f32_16x16x32_bf16 a[216:219],v[80:83],v[196:199],a[216:219]
v_mfma_f32_16x16x32_bf16 a[216:219],v[84:87],v[212:215],a[216:219]
v_mfma_f32_16x16x32_bf16 a[216:219],v[88:91],v[228:231],a[216:219]
v_mfma_f32_16x16x32_bf16 a[216:219],v[92:95],v[244:247],a[216:219]
v_mfma_f32_16x16x32_bf16 a[220:223],v[92:95],v[188:191],a[220:223]
ds_read_b64_tr_b16 v[92:93],v2 offset:30816
ds_read_b64_tr_b16 v[94:95],v2 offset:35936
v_mfma_f32_16x16x32_bf16 a[220:223],v[88:91],v[172:175],a[220:223]
ds_read_b64_tr_b16 v[88:89],v2 offset:20576
v_mfma_f32_16x16x32_bf16 a[220:223],v[84:87],v[156:159],a[220:223]
ds_read_b64_tr_b16 v[84:85],v2 offset:10336
v_mfma_f32_16x16x32_bf16 a[220:223],v[80:83],v[140:143],a[220:223]
s_waitcnt vmcnt(15) lgkmcnt(4)
ds_read_b64_tr_b16 v[80:81],v2 offset:96
v_mfma_f32_16x16x32_bf16 a[192:195],v[96:99],a[128:131],a[192:195]
s_waitcnt vmcnt(14)
ds_read_b64_tr_b16 v[82:83],v2 offset:5216
ds_read_b64_tr_b16 v[86:87],v2 offset:15456
v_mfma_f32_16x16x32_bf16 a[192:195],v[100:103],a[144:147],a[192:195]
s_waitcnt vmcnt(13)
ds_read_b64_tr_b16 v[90:91],v2 offset:25696
v_add_u32 v2,0x1e000,v2
v_mfma_f32_16x16x32_bf16 a[192:195],v[104:107],a[160:163],a[192:195]
s_waitcnt vmcnt(12)
v_mfma_f32_16x16x32_bf16 a[192:195],v[108:111],a[176:179],a[192:195]
s_waitcnt vmcnt(11)
v_mfma_f32_16x16x32_bf16 a[196:199],v[108:111],a[180:183],a[196:199]
s_waitcnt vmcnt(10)
v_mfma_f32_16x16x32_bf16 a[196:199],v[104:107],a[164:167],a[196:199]
s_waitcnt vmcnt(9)
v_mfma_f32_16x16x32_bf16 a[196:199],v[100:103],a[148:151],a[196:199]
s_waitcnt vmcnt(8)
v_mfma_f32_16x16x32_bf16 a[196:199],v[96:99],a[132:135],a[196:199]
s_waitcnt vmcnt(7)
v_mfma_f32_16x16x32_bf16 a[200:203],v[96:99],a[136:139],a[200:203]
s_waitcnt vmcnt(6)
v_mfma_f32_16x16x32_bf16 a[200:203],v[100:103],a[152:155],a[200:203]
s_waitcnt vmcnt(5)
v_mfma_f32_16x16x32_bf16 a[200:203],v[104:107],a[168:171],a[200:203]
s_waitcnt vmcnt(4)
v_mfma_f32_16x16x32_bf16 a[200:203],v[108:111],a[184:187],a[200:203]
s_waitcnt vmcnt(3)
v_accvgpr_read_b32 v16,a192
v_accvgpr_read_b32 v17,a193
v_mfma_f32_16x16x32_bf16 a[204:207],v[108:111],a[188:191],a[204:207]
s_waitcnt vmcnt(2)
v_accvgpr_read_b32 v18,a194
v_accvgpr_read_b32 v19,a195
v_mfma_f32_16x16x32_bf16 a[204:207],v[104:107],a[172:175],a[204:207]
s_waitcnt vmcnt(1)
v_cvt_pk_bf16_f32 v16,v16,v17
v_cvt_pk_bf16_f32 v17,v18,v19
v_mfma_f32_16x16x32_bf16 a[204:207],v[100:103],a[156:159],a[204:207]
s_waitcnt vmcnt(0)
v_accvgpr_read_b32 v8,a196
v_accvgpr_read_b32 v9,a197
v_mfma_f32_16x16x32_bf16 a[204:207],v[96:99],a[140:143],a[204:207]
v_accvgpr_read_b32 v10,a198
v_accvgpr_read_b32 v11,a199
v_mfma_f32_16x16x32_bf16 a[208:211],v[112:115],a[128:131],a[208:211]
v_cvt_pk_bf16_f32 v8,v8,v9
v_cvt_pk_bf16_f32 v9,v10,v11
v_mfma_f32_16x16x32_bf16 a[208:211],v[116:119],a[144:147],a[208:211]
ds_read_b64_tr_b16 v[96:97],v1 offset:64
ds_read_b64_tr_b16 v[98:99],v1 offset:5184
v_mfma_f32_16x16x32_bf16 a[208:211],v[120:123],a[160:163],a[208:211]
ds_read_b64_tr_b16 v[100:101],v1 offset:10304
v_mfma_f32_16x16x32_bf16 a[208:211],v[124:127],a[176:179],a[208:211]
ds_read_b64_tr_b16 v[102:103],v1 offset:15424
v_mfma_f32_16x16x32_bf16 a[212:215],v[124:127],a[180:183],a[212:215]
ds_read_b64_tr_b16 v[104:105],v1 offset:20544
v_mfma_f32_16x16x32_bf16 a[212:215],v[120:123],a[164:167],a[212:215]
ds_read_b64_tr_b16 v[106:107],v1 offset:25664
ds_read_b64_tr_b16 v[108:109],v1 offset:30784
v_mfma_f32_16x16x32_bf16 a[212:215],v[116:119],a[148:151],a[212:215]
ds_read_b64_tr_b16 v[110:111],v1 offset:35904
v_add_u32 v1,0xfffe2000,v1
v_mfma_f32_16x16x32_bf16 a[212:215],v[112:115],a[132:135],a[212:215]
v_accvgpr_read_b32 v20,a208
v_accvgpr_read_b32 v21,a209
v_mfma_f32_16x16x32_bf16 a[216:219],v[112:115],a[136:139],a[216:219]
v_accvgpr_read_b32 v22,a210
v_accvgpr_read_b32 v23,a211
v_mfma_f32_16x16x32_bf16 a[216:219],v[116:119],a[152:155],a[216:219]
v_cvt_pk_bf16_f32 v18,v20,v21
v_cvt_pk_bf16_f32 v19,v22,v23
v_mfma_f32_16x16x32_bf16 a[216:219],v[120:123],a[168:171],a[216:219]
buffer_store_dwordx4 v[16:19],v6,s[36:39],s74 offen sc1
v_mfma_f32_16x16x32_bf16 a[216:219],v[124:127],a[184:187],a[216:219]
v_accvgpr_read_b32 v12,a212
v_accvgpr_read_b32 v13,a213
v_mfma_f32_16x16x32_bf16 a[220:223],v[124:127],a[188:191],a[220:223]
v_accvgpr_read_b32 v14,a214
v_accvgpr_read_b32 v15,a215
v_mfma_f32_16x16x32_bf16 a[220:223],v[120:123],a[172:175],a[220:223]
v_cvt_pk_bf16_f32 v10,v12,v13
v_cvt_pk_bf16_f32 v11,v14,v15
v_mfma_f32_16x16x32_bf16 a[220:223],v[116:119],a[156:159],a[220:223]
v_accvgpr_read_b32 v16,a200
v_accvgpr_read_b32 v17,a201
v_mfma_f32_16x16x32_bf16 a[220:223],v[112:115],a[140:143],a[220:223]
v_accvgpr_read_b32 v18,a202
v_accvgpr_read_b32 v19,a203
v_mfma_f32_16x16x32_bf16 a[224:227],v[64:67],v[32:35],a[224:227]
v_accvgpr_read_b32 v20,a216
v_accvgpr_read_b32 v21,a217
v_mfma_f32_16x16x32_bf16 a[224:227],v[68:71],v[36:39],a[224:227]
v_accvgpr_read_b32 v22,a218
v_accvgpr_read_b32 v23,a219
v_mfma_f32_16x16x32_bf16 a[224:227],v[72:75],v[40:43],a[224:227]
v_cvt_pk_bf16_f32 v16,v16,v17
v_cvt_pk_bf16_f32 v17,v18,v19
v_mfma_f32_16x16x32_bf16 a[224:227],v[76:79],v[44:47],a[224:227]
v_cvt_pk_bf16_f32 v18,v20,v21
v_cvt_pk_bf16_f32 v19,v22,v23
v_mfma_f32_16x16x32_bf16 a[228:231],v[76:79],v[180:183],a[228:231]
ds_read_b64_tr_b16 v[112:113],v2 offset:96
ds_read_b64_tr_b16 v[114:115],v2 offset:5216
v_mfma_f32_16x16x32_bf16 a[228:231],v[72:75],v[164:167],a[228:231]
buffer_store_dwordx4 v[8:11],v6,s[36:39],s74 offen offset:1024 sc1
v_mfma_f32_16x16x32_bf16 a[228:231],v[68:71],v[148:151],a[228:231]
ds_read_b64_tr_b16 v[116:117],v2 offset:10336
ds_read_b64_tr_b16 v[118:119],v2 offset:15456
v_mfma_f32_16x16x32_bf16 a[228:231],v[64:67],v[132:135],a[228:231]
ds_read_b64_tr_b16 v[120:121],v2 offset:20576
v_accvgpr_read_b32 v8,a204
v_mfma_f32_16x16x32_bf16 a[232:235],v[64:67],v[196:199],a[232:235]
ds_read_b64_tr_b16 v[122:123],v2 offset:25696
v_accvgpr_read_b32 v9,a205
v_mfma_f32_16x16x32_bf16 a[232:235],v[68:71],v[212:215],a[232:235]
ds_read_b64_tr_b16 v[124:125],v2 offset:30816
v_accvgpr_read_b32 v10,a206
v_mfma_f32_16x16x32_bf16 a[232:235],v[72:75],v[228:231],a[232:235]
ds_read_b64_tr_b16 v[126:127],v2 offset:35936
v_accvgpr_read_b32 v11,a207
v_mfma_f32_16x16x32_bf16 a[232:235],v[76:79],v[244:247],a[232:235]
v_accvgpr_read_b32 v12,a220
v_accvgpr_read_b32 v13,a221
v_mfma_f32_16x16x32_bf16 a[236:239],v[76:79],v[188:191],a[236:239]
v_accvgpr_read_b32 v14,a222
v_accvgpr_read_b32 v15,a223
v_mfma_f32_16x16x32_bf16 a[236:239],v[72:75],v[172:175],a[236:239]
v_cvt_pk_bf16_f32 v8,v8,v9
v_cvt_pk_bf16_f32 v9,v10,v11
v_mfma_f32_16x16x32_bf16 a[236:239],v[68:71],v[156:159],a[236:239]
v_cvt_pk_bf16_f32 v10,v12,v13
v_cvt_pk_bf16_f32 v11,v14,v15
v_mfma_f32_16x16x32_bf16 a[236:239],v[64:67],v[140:143],a[236:239]
s_waitcnt lgkmcnt(3)
ds_read_b64_tr_b16 v[64:65],v1 offset:128
ds_read_b64_tr_b16 v[66:67],v1 offset:5248
v_mfma_f32_16x16x32_bf16 a[240:243],v[80:83],v[32:35],a[240:243]
buffer_store_dwordx4 v[16:19],v6,s[36:39],s74 offen offset:2048 sc1
v_mfma_f32_16x16x32_bf16 a[240:243],v[84:87],v[36:39],a[240:243]
ds_read_b64_tr_b16 v[68:69],v1 offset:10368
ds_read_b64_tr_b16 v[70:71],v1 offset:15488
v_mfma_f32_16x16x32_bf16 a[240:243],v[88:91],v[40:43],a[240:243]
ds_read_b64_tr_b16 v[72:73],v1 offset:20608
v_add_u32 v2,0xfffe2000,v2
v_mfma_f32_16x16x32_bf16 a[240:243],v[92:95],v[44:47],a[240:243]
ds_read_b64_tr_b16 v[74:75],v1 offset:25728
v_mfma_f32_16x16x32_bf16 a[244:247],v[92:95],v[180:183],a[244:247]
ds_read_b64_tr_b16 v[76:77],v1 offset:30848
v_mfma_f32_16x16x32_bf16 a[244:247],v[88:91],v[164:167],a[244:247]
ds_read_b64_tr_b16 v[78:79],v1 offset:35968
v_add_u32 v1,0x1e000,v1
v_mfma_f32_16x16x32_bf16 a[244:247],v[84:87],v[148:151],a[244:247]
v_mfma_f32_16x16x32_bf16 a[244:247],v[80:83],v[132:135],a[244:247]
v_mfma_f32_16x16x32_bf16 a[248:251],v[80:83],v[196:199],a[248:251]
v_mfma_f32_16x16x32_bf16 a[248:251],v[84:87],v[212:215],a[248:251]
v_mfma_f32_16x16x32_bf16 a[248:251],v[88:91],v[228:231],a[248:251]
v_mfma_f32_16x16x32_bf16 a[248:251],v[92:95],v[244:247],a[248:251]
buffer_store_dwordx4 v[8:11],v28,s[36:39],s74 offen offset:3072 sc1
v_mfma_f32_16x16x32_bf16 a[252:255],v[92:95],v[188:191],a[252:255]
ds_read_b64_tr_b16 v[92:93],v2 offset:30880
ds_read_b64_tr_b16 v[94:95],v2 offset:36000
v_mfma_f32_16x16x32_bf16 a[252:255],v[88:91],v[172:175],a[252:255]
ds_read_b64_tr_b16 v[88:89],v2 offset:20640
v_mfma_f32_16x16x32_bf16 a[252:255],v[84:87],v[156:159],a[252:255]
ds_read_b64_tr_b16 v[84:85],v2 offset:10400
v_mfma_f32_16x16x32_bf16 a[252:255],v[80:83],v[140:143],a[252:255]
ds_read_b64_tr_b16 v[80:81],v2 offset:160
v_mfma_f32_16x16x32_bf16 a[224:227],v[96:99],a[128:131],a[224:227]
ds_read_b64_tr_b16 v[82:83],v2 offset:5280
ds_read_b64_tr_b16 v[86:87],v2 offset:15520
v_mfma_f32_16x16x32_bf16 a[224:227],v[100:103],a[144:147],a[224:227]
ds_read_b64_tr_b16 v[90:91],v2 offset:25760
v_add_u32 v2,0x1e000,v2
v_mfma_f32_16x16x32_bf16 a[224:227],v[104:107],a[160:163],a[224:227]
v_mfma_f32_16x16x32_bf16 a[224:227],v[108:111],a[176:179],a[224:227]
v_mfma_f32_16x16x32_bf16 a[228:231],v[108:111],a[180:183],a[228:231]
v_mfma_f32_16x16x32_bf16 a[228:231],v[104:107],a[164:167],a[228:231]
v_mfma_f32_16x16x32_bf16 a[228:231],v[100:103],a[148:151],a[228:231]
v_mfma_f32_16x16x32_bf16 a[228:231],v[96:99],a[132:135],a[228:231]
v_mfma_f32_16x16x32_bf16 a[232:235],v[96:99],a[136:139],a[232:235]
v_mfma_f32_16x16x32_bf16 a[232:235],v[100:103],a[152:155],a[232:235]
v_mfma_f32_16x16x32_bf16 a[232:235],v[104:107],a[168:171],a[232:235]
v_mfma_f32_16x16x32_bf16 a[232:235],v[108:111],a[184:187],a[232:235]
v_accvgpr_read_b32 v16,a224
v_accvgpr_read_b32 v17,a225
v_mfma_f32_16x16x32_bf16 a[236:239],v[108:111],a[188:191],a[236:239]
v_accvgpr_read_b32 v18,a226
v_accvgpr_read_b32 v19,a227
v_mfma_f32_16x16x32_bf16 a[236:239],v[104:107],a[172:175],a[236:239]
v_cvt_pk_bf16_f32 v16,v16,v17
v_cvt_pk_bf16_f32 v17,v18,v19
v_mfma_f32_16x16x32_bf16 a[236:239],v[100:103],a[156:159],a[236:239]
v_accvgpr_read_b32 v8,a228
v_accvgpr_read_b32 v9,a229
v_mfma_f32_16x16x32_bf16 a[236:239],v[96:99],a[140:143],a[236:239]
v_accvgpr_read_b32 v10,a230
v_accvgpr_read_b32 v11,a231
v_mfma_f32_16x16x32_bf16 a[240:243],v[112:115],a[128:131],a[240:243]
v_cvt_pk_bf16_f32 v8,v8,v9
v_cvt_pk_bf16_f32 v9,v10,v11
v_mfma_f32_16x16x32_bf16 a[240:243],v[116:119],a[144:147],a[240:243]
s_waitcnt lgkmcnt(0)
ds_read_b64_tr_b16 v[96:97],v1 offset:128
ds_read_b64_tr_b16 v[98:99],v1 offset:5248
v_mfma_f32_16x16x32_bf16 a[240:243],v[120:123],a[160:163],a[240:243]
ds_read_b64_tr_b16 v[100:101],v1 offset:10368
v_mfma_f32_16x16x32_bf16 a[240:243],v[124:127],a[176:179],a[240:243]
ds_read_b64_tr_b16 v[102:103],v1 offset:15488
v_mfma_f32_16x16x32_bf16 a[244:247],v[124:127],a[180:183],a[244:247]
ds_read_b64_tr_b16 v[104:105],v1 offset:20608
v_mfma_f32_16x16x32_bf16 a[244:247],v[120:123],a[164:167],a[244:247]
ds_read_b64_tr_b16 v[106:107],v1 offset:25728
)A"
R"A(ds_read_b64_tr_b16 v[108:109],v1 offset:30848
v_mfma_f32_16x16x32_bf16 a[244:247],v[116:119],a[148:151],a[244:247]
ds_read_b64_tr_b16 v[110:111],v1 offset:35968
v_add_u32 v1,0xfffe2000,v1
v_mfma_f32_16x16x32_bf16 a[244:247],v[112:115],a[132:135],a[244:247]
v_accvgpr_read_b32 v20,a240
v_accvgpr_read_b32 v21,a241
v_mfma_f32_16x16x32_bf16 a[248:251],v[112:115],a[136:139],a[248:251]
v_accvgpr_read_b32 v22,a242
v_accvgpr_read_b32 v23,a243
v_mfma_f32_16x16x32_bf16 a[248:251],v[116:119],a[152:155],a[248:251]
v_cvt_pk_bf16_f32 v18,v20,v21
v_cvt_pk_bf16_f32 v19,v22,v23
v_mfma_f32_16x16x32_bf16 a[248:251],v[120:123],a[168:171],a[248:251]
buffer_store_dwordx4 v[16:19],v6,s[36:39],s75 offen sc1
v_mfma_f32_16x16x32_bf16 a[248:251],v[124:127],a[184:187],a[248:251]
v_accvgpr_read_b32 v12,a244
v_accvgpr_read_b32 v13,a245
v_mfma_f32_16x16x32_bf16 a[252:255],v[124:127],a[188:191],a[252:255]
v_accvgpr_read_b32 v14,a246
v_accvgpr_read_b32 v15,a247
v_mfma_f32_16x16x32_bf16 a[252:255],v[120:123],a[172:175],a[252:255]
v_cvt_pk_bf16_f32 v10,v12,v13
v_cvt_pk_bf16_f32 v11,v14,v15
v_mfma_f32_16x16x32_bf16 a[252:255],v[116:119],a[156:159],a[252:255]
v_accvgpr_read_b32 v16,a232
v_accvgpr_read_b32 v17,a233
v_mfma_f32_16x16x32_bf16 a[252:255],v[112:115],a[140:143],a[252:255]
v_accvgpr_read_b32 v18,a234
v_accvgpr_read_b32 v19,a235
v_mfma_f32_16x16x32_bf16 a[0:3],v[64:67],v[32:35],a[0:3]
v_accvgpr_read_b32 v20,a248
v_accvgpr_read_b32 v21,a249
v_mfma_f32_16x16x32_bf16 a[0:3],v[68:71],v[36:39],a[0:3]
v_accvgpr_read_b32 v22,a250
v_accvgpr_read_b32 v23,a251
v_mfma_f32_16x16x32_bf16 a[0:3],v[72:75],v[40:43],a[0:3]
v_cvt_pk_bf16_f32 v16,v16,v17
v_cvt_pk_bf16_f32 v17,v18,v19
v_mfma_f32_16x16x32_bf16 a[0:3],v[76:79],v[44:47],a[0:3]
v_cvt_pk_bf16_f32 v18,v20,v21
v_cvt_pk_bf16_f32 v19,v22,v23
v_mfma_f32_16x16x32_bf16 a[4:7],v[76:79],v[180:183],a[4:7]
ds_read_b64_tr_b16 v[112:113],v2 offset:160
ds_read_b64_tr_b16 v[114:115],v2 offset:5280
v_mfma_f32_16x16x32_bf16 a[4:7],v[72:75],v[164:167],a[4:7]
buffer_store_dwordx4 v[8:11],v6,s[36:39],s75 offen offset:1024 sc1
v_mfma_f32_16x16x32_bf16 a[4:7],v[68:71],v[148:151],a[4:7]
ds_read_b64_tr_b16 v[116:117],v2 offset:10400
ds_read_b64_tr_b16 v[118:119],v2 offset:15520
v_mfma_f32_16x16x32_bf16 a[4:7],v[64:67],v[132:135],a[4:7]
ds_read_b64_tr_b16 v[120:121],v2 offset:20640
v_accvgpr_read_b32 v8,a236
v_mfma_f32_16x16x32_bf16 a[8:11],v[64:67],v[196:199],a[8:11]
ds_read_b64_tr_b16 v[122:123],v2 offset:25760
v_accvgpr_read_b32 v9,a237
v_mfma_f32_16x16x32_bf16 a[8:11],v[68:71],v[212:215],a[8:11]
ds_read_b64_tr_b16 v[124:125],v2 offset:30880
v_accvgpr_read_b32 v10,a238
v_mfma_f32_16x16x32_bf16 a[8:11],v[72:75],v[228:231],a[8:11]
ds_read_b64_tr_b16 v[126:127],v2 offset:36000
v_accvgpr_read_b32 v11,a239
v_mfma_f32_16x16x32_bf16 a[8:11],v[76:79],v[244:247],a[8:11]
v_accvgpr_read_b32 v12,a252
v_accvgpr_read_b32 v13,a253
v_mfma_f32_16x16x32_bf16 a[12:15],v[76:79],v[188:191],a[12:15]
v_accvgpr_read_b32 v14,a254
v_accvgpr_read_b32 v15,a255
v_mfma_f32_16x16x32_bf16 a[12:15],v[72:75],v[172:175],a[12:15]
v_cvt_pk_bf16_f32 v8,v8,v9
v_cvt_pk_bf16_f32 v9,v10,v11
v_mfma_f32_16x16x32_bf16 a[12:15],v[68:71],v[156:159],a[12:15]
v_cvt_pk_bf16_f32 v10,v12,v13
v_cvt_pk_bf16_f32 v11,v14,v15
v_mfma_f32_16x16x32_bf16 a[12:15],v[64:67],v[140:143],a[12:15]
ds_read_b64_tr_b16 v[64:65],v1 offset:192
ds_read_b64_tr_b16 v[66:67],v1 offset:5312
v_mfma_f32_16x16x32_bf16 a[16:19],v[80:83],v[32:35],a[16:19]
buffer_store_dwordx4 v[16:19],v6,s[36:39],s75 offen offset:2048 sc1
v_mfma_f32_16x16x32_bf16 a[16:19],v[84:87],v[36:39],a[16:19]
ds_read_b64_tr_b16 v[68:69],v1 offset:10432
ds_read_b64_tr_b16 v[70:71],v1 offset:15552
v_mfma_f32_16x16x32_bf16 a[16:19],v[88:91],v[40:43],a[16:19]
ds_read_b64_tr_b16 v[72:73],v1 offset:20672
v_add_u32 v2,0xfffe2000,v2
v_mfma_f32_16x16x32_bf16 a[16:19],v[92:95],v[44:47],a[16:19]
ds_read_b64_tr_b16 v[74:75],v1 offset:25792
v_mfma_f32_16x16x32_bf16 a[20:23],v[92:95],v[180:183],a[20:23]
ds_read_b64_tr_b16 v[76:77],v1 offset:30912
v_mfma_f32_16x16x32_bf16 a[20:23],v[88:91],v[164:167],a[20:23]
ds_read_b64_tr_b16 v[78:79],v1 offset:36032
v_add_u32 v1,0x1e000,v1
v_mfma_f32_16x16x32_bf16 a[20:23],v[84:87],v[148:151],a[20:23]
v_mfma_f32_16x16x32_bf16 a[20:23],v[80:83],v[132:135],a[20:23]
v_mfma_f32_16x16x32_bf16 a[24:27],v[80:83],v[196:199],a[24:27]
v_mfma_f32_16x16x32_bf16 a[24:27],v[84:87],v[212:215],a[24:27]
v_mfma_f32_16x16x32_bf16 a[24:27],v[88:91],v[228:231],a[24:27]
v_mfma_f32_16x16x32_bf16 a[24:27],v[92:95],v[244:247],a[24:27]
buffer_store_dwordx4 v[8:11],v28,s[36:39],s75 offen offset:3072 sc1
v_mfma_f32_16x16x32_bf16 a[28:31],v[92:95],v[188:191],a[28:31]
ds_read_b64_tr_b16 v[92:93],v2 offset:30944
ds_read_b64_tr_b16 v[94:95],v2 offset:36064
v_mfma_f32_16x16x32_bf16 a[28:31],v[88:91],v[172:175],a[28:31]
ds_read_b64_tr_b16 v[88:89],v2 offset:20704
v_mfma_f32_16x16x32_bf16 a[28:31],v[84:87],v[156:159],a[28:31]
ds_read_b64_tr_b16 v[84:85],v2 offset:10464
v_mfma_f32_16x16x32_bf16 a[28:31],v[80:83],v[140:143],a[28:31]
s_waitcnt lgkmcnt(4)
ds_read_b64_tr_b16 v[80:81],v2 offset:224
v_mfma_f32_16x16x32_bf16 a[0:3],v[96:99],a[128:131],a[0:3]
ds_read_b64_tr_b16 v[82:83],v2 offset:5344
ds_read_b64_tr_b16 v[86:87],v2 offset:15584
v_mfma_f32_16x16x32_bf16 a[0:3],v[100:103],a[144:147],a[0:3]
ds_read_b64_tr_b16 v[90:91],v2 offset:25824
v_add_u32 v2,0x1e000,v2
v_mfma_f32_16x16x32_bf16 a[0:3],v[104:107],a[160:163],a[0:3]
v_mfma_f32_16x16x32_bf16 a[0:3],v[108:111],a[176:179],a[0:3]
v_mfma_f32_16x16x32_bf16 a[4:7],v[108:111],a[180:183],a[4:7]
v_mfma_f32_16x16x32_bf16 a[4:7],v[104:107],a[164:167],a[4:7]
v_mfma_f32_16x16x32_bf16 a[4:7],v[100:103],a[148:151],a[4:7]
v_mfma_f32_16x16x32_bf16 a[4:7],v[96:99],a[132:135],a[4:7]
v_mfma_f32_16x16x32_bf16 a[8:11],v[96:99],a[136:139],a[8:11]
v_mfma_f32_16x16x32_bf16 a[8:11],v[100:103],a[152:155],a[8:11]
v_mfma_f32_16x16x32_bf16 a[8:11],v[104:107],a[168:171],a[8:11]
v_mfma_f32_16x16x32_bf16 a[8:11],v[108:111],a[184:187],a[8:11]
v_accvgpr_read_b32 v16,a0
v_accvgpr_read_b32 v17,a1
v_mfma_f32_16x16x32_bf16 a[12:15],v[108:111],a[188:191],a[12:15]
v_accvgpr_read_b32 v18,a2
v_accvgpr_read_b32 v19,a3
v_mfma_f32_16x16x32_bf16 a[12:15],v[104:107],a[172:175],a[12:15]
v_cvt_pk_bf16_f32 v16,v16,v17
v_cvt_pk_bf16_f32 v17,v18,v19
v_mfma_f32_16x16x32_bf16 a[12:15],v[100:103],a[156:159],a[12:15]
v_accvgpr_read_b32 v8,a4
v_accvgpr_read_b32 v9,a5
v_mfma_f32_16x16x32_bf16 a[12:15],v[96:99],a[140:143],a[12:15]
v_accvgpr_read_b32 v10,a6
v_accvgpr_read_b32 v11,a7
v_mfma_f32_16x16x32_bf16 a[16:19],v[112:115],a[128:131],a[16:19]
v_cvt_pk_bf16_f32 v8,v8,v9
v_cvt_pk_bf16_f32 v9,v10,v11
v_mfma_f32_16x16x32_bf16 a[16:19],v[116:119],a[144:147],a[16:19]
ds_read_b64_tr_b16 v[96:97],v1 offset:192
ds_read_b64_tr_b16 v[98:99],v1 offset:5312
v_mfma_f32_16x16x32_bf16 a[16:19],v[120:123],a[160:163],a[16:19]
ds_read_b64_tr_b16 v[100:101],v1 offset:10432
v_mfma_f32_16x16x32_bf16 a[16:19],v[124:127],a[176:179],a[16:19]
ds_read_b64_tr_b16 v[102:103],v1 offset:15552
v_mfma_f32_16x16x32_bf16 a[20:23],v[124:127],a[180:183],a[20:23]
ds_read_b64_tr_b16 v[104:105],v1 offset:20672
v_mfma_f32_16x16x32_bf16 a[20:23],v[120:123],a[164:167],a[20:23]
ds_read_b64_tr_b16 v[106:107],v1 offset:25792
ds_read_b64_tr_b16 v[108:109],v1 offset:30912
v_mfma_f32_16x16x32_bf16 a[20:23],v[116:119],a[148:151],a[20:23]
ds_read_b64_tr_b16 v[110:111],v1 offset:36032
v_add_u32 v1,0xfffec000,v1
v_mfma_f32_16x16x32_bf16 a[20:23],v[112:115],a[132:135],a[20:23]
v_accvgpr_read_b32 v20,a16
v_accvgpr_read_b32 v21,a17
v_mfma_f32_16x16x32_bf16 a[24:27],v[112:115],a[136:139],a[24:27]
v_accvgpr_read_b32 v22,a18
v_accvgpr_read_b32 v23,a19
v_mfma_f32_16x16x32_bf16 a[24:27],v[116:119],a[152:155],a[24:27]
v_cvt_pk_bf16_f32 v18,v20,v21
v_cvt_pk_bf16_f32 v19,v22,v23
v_mfma_f32_16x16x32_bf16 a[24:27],v[120:123],a[168:171],a[24:27]
buffer_store_dwordx4 v[16:19],v6,s[36:39],s76 offen sc1
v_mfma_f32_16x16x32_bf16 a[24:27],v[124:127],a[184:187],a[24:27]
v_accvgpr_read_b32 v12,a20
v_accvgpr_read_b32 v13,a21
v_mfma_f32_16x16x32_bf16 a[28:31],v[124:127],a[188:191],a[28:31]
v_accvgpr_read_b32 v14,a22
v_accvgpr_read_b32 v15,a23
v_mfma_f32_16x16x32_bf16 a[28:31],v[120:123],a[172:175],a[28:31]
v_cvt_pk_bf16_f32 v10,v12,v13
v_cvt_pk_bf16_f32 v11,v14,v15
v_mfma_f32_16x16x32_bf16 a[28:31],v[116:119],a[156:159],a[28:31]
v_accvgpr_read_b32 v16,a8
s_nop 0
L_pd18_%=:
s_cbranch_execz L_trtg2F_%=
v_accvgpr_read_b32 v17,a9
v_mfma_f32_16x16x32_bf16 a[28:31],v[112:115],a[140:143],a[28:31]
v_accvgpr_read_b32 v18,a10
v_accvgpr_read_b32 v19,a11
v_mfma_f32_16x16x32_bf16 v[128:131],v[64:67],v[32:35],v[128:131]
v_accvgpr_read_b32 v20,a24
v_accvgpr_read_b32 v21,a25
v_mfma_f32_16x16x32_bf16 v[128:131],v[68:71],v[36:39],v[128:131]
v_accvgpr_read_b32 v22,a26
v_accvgpr_read_b32 v23,a27
v_mfma_f32_16x16x32_bf16 v[128:131],v[72:75],v[40:43],v[128:131]
v_cvt_pk_bf16_f32 v16,v16,v17
v_cvt_pk_bf16_f32 v17,v18,v19
v_mfma_f32_16x16x32_bf16 v[128:131],v[76:79],v[44:47],v[128:131]
v_cvt_pk_bf16_f32 v18,v20,v21
v_cvt_pk_bf16_f32 v19,v22,v23
v_mfma_f32_16x16x32_bf16 v[136:139],v[76:79],v[180:183],v[136:139]
ds_read_b64_tr_b16 v[112:113],v2 offset:224
ds_read_b64_tr_b16 v[114:115],v2 offset:5344
v_mfma_f32_16x16x32_bf16 v[136:139],v[72:75],v[164:167],v[136:139]
buffer_store_dwordx4 v[8:11],v6,s[36:39],s76 offen offset:1024 sc1
v_mfma_f32_16x16x32_bf16 v[136:139],v[68:71],v[148:151],v[136:139]
ds_read_b64_tr_b16 v[116:117],v2 offset:10464
ds_read_b64_tr_b16 v[118:119],v2 offset:15584
v_mfma_f32_16x16x32_bf16 v[136:139],v[64:67],v[132:135],v[136:139]
ds_read_b64_tr_b16 v[120:121],v2 offset:20704
v_accvgpr_read_b32 v8,a12
v_mfma_f32_16x16x32_bf16 v[144:147],v[64:67],v[196:199],v[144:147]
ds_read_b64_tr_b16 v[122:123],v2 offset:25824
v_accvgpr_read_b32 v9,a13
v_mfma_f32_16x16x32_bf16 v[144:147],v[68:71],v[212:215],v[144:147]
ds_read_b64_tr_b16 v[124:125],v2 offset:30944
v_accvgpr_read_b32 v10,a14
v_mfma_f32_16x16x32_bf16 v[144:147],v[72:75],v[228:231],v[144:147]
ds_read_b64_tr_b16 v[126:127],v2 offset:36064
v_accvgpr_read_b32 v11,a15
v_mfma_f32_16x16x32_bf16 v[144:147],v[76:79],v[244:247],v[144:147]
v_accvgpr_read_b32 v12,a28
v_accvgpr_read_b32 v13,a29
v_mfma_f32_16x16x32_bf16 v[152:155],v[76:79],v[188:191],v[152:155]
v_accvgpr_read_b32 v14,a30
v_accvgpr_read_b32 v15,a31
v_mfma_f32_16x16x32_bf16 v[152:155],v[72:75],v[172:175],v[152:155]
v_cvt_pk_bf16_f32 v8,v8,v9
v_cvt_pk_bf16_f32 v9,v10,v11
v_mfma_f32_16x16x32_bf16 v[152:155],v[68:71],v[156:159],v[152:155]
v_cvt_pk_bf16_f32 v10,v12,v13
v_cvt_pk_bf16_f32 v11,v14,v15
v_mfma_f32_16x16x32_bf16 v[152:155],v[64:67],v[140:143],v[152:155]
s_waitcnt lgkmcnt(3)
ds_read_b64_tr_b16 v[64:65],v1 offset:256
ds_read_b64_tr_b16 v[66:67],v1 offset:5376
v_mfma_f32_16x16x32_bf16 v[160:163],v[80:83],v[32:35],v[160:163]
buffer_store_dwordx4 v[16:19],v6,s[36:39],s76 offen offset:2048 sc1
v_mfma_f32_16x16x32_bf16 v[160:163],v[84:87],v[36:39],v[160:163]
ds_read_b64_tr_b16 v[68:69],v1 offset:10496
ds_read_b64_tr_b16 v[70:71],v1 offset:15616
v_mfma_f32_16x16x32_bf16 v[160:163],v[88:91],v[40:43],v[160:163]
ds_read_b64_tr_b16 v[72:73],v1 offset:20736
v_add_u32 v2,0xfffec000,v2
v_mfma_f32_16x16x32_bf16 v[160:163],v[92:95],v[44:47],v[160:163]
ds_read_b64_tr_b16 v[74:75],v1 offset:25856
v_mov_b32 v20,s52
v_mfma_f32_16x16x32_bf16 v[168:171],v[92:95],v[180:183],v[168:171]
ds_read_b64_tr_b16 v[76:77],v1 offset:30976
v_mov_b32 v21,s53
v_mfma_f32_16x16x32_bf16 v[168:171],v[88:91],v[164:167],v[168:171]
ds_read_b64_tr_b16 v[78:79],v1 offset:36096
v_add_u32 v1,0xffff6000,v1
v_mfma_f32_16x16x32_bf16 v[168:171],v[84:87],v[148:151],v[168:171]
v_lshlrev_b32 v22,20,v6
v_mfma_f32_16x16x32_bf16 v[168:171],v[80:83],v[132:135],v[168:171]
v_mfma_f32_16x16x32_bf16 v[176:179],v[80:83],v[196:199],v[176:179]
v_mfma_f32_16x16x32_bf16 v[176:179],v[84:87],v[212:215],v[176:179]
v_mfma_f32_16x16x32_bf16 v[176:179],v[88:91],v[228:231],v[176:179]
v_mfma_f32_16x16x32_bf16 v[176:179],v[92:95],v[244:247],v[176:179]
buffer_store_dwordx4 v[8:11],v28,s[36:39],s76 offen offset:3072 sc1
v_mfma_f32_16x16x32_bf16 v[184:187],v[92:95],v[188:191],v[184:187]
)A"
R"A(ds_read_b64_tr_b16 v[92:93],v2 offset:31008
ds_read_b64_tr_b16 v[94:95],v2 offset:36128
v_mfma_f32_16x16x32_bf16 v[184:187],v[88:91],v[172:175],v[184:187]
ds_read_b64_tr_b16 v[88:89],v2 offset:20768
v_mfma_f32_16x16x32_bf16 v[184:187],v[84:87],v[156:159],v[184:187]
ds_read_b64_tr_b16 v[84:85],v2 offset:10528
v_mfma_f32_16x16x32_bf16 v[184:187],v[80:83],v[140:143],v[184:187]
ds_read_b64_tr_b16 v[80:81],v2 offset:288
v_mfma_f32_16x16x32_bf16 v[128:131],v[96:99],a[128:131],v[128:131]
ds_read_b64_tr_b16 v[82:83],v2 offset:5408
ds_read_b64_tr_b16 v[86:87],v2 offset:15648
v_mfma_f32_16x16x32_bf16 v[128:131],v[100:103],a[144:147],v[128:131]
ds_read_b64_tr_b16 v[90:91],v2 offset:25888
v_add_u32 v2,0xffff6000,v2
v_mfma_f32_16x16x32_bf16 v[128:131],v[104:107],a[160:163],v[128:131]
v_mfma_f32_16x16x32_bf16 v[128:131],v[108:111],a[176:179],v[128:131]
v_mfma_f32_16x16x32_bf16 v[136:139],v[108:111],a[180:183],v[136:139]
v_mfma_f32_16x16x32_bf16 v[136:139],v[104:107],a[164:167],v[136:139]
v_mfma_f32_16x16x32_bf16 v[136:139],v[100:103],a[148:151],v[136:139]
v_mfma_f32_16x16x32_bf16 v[136:139],v[96:99],a[132:135],v[136:139]
v_mfma_f32_16x16x32_bf16 v[144:147],v[96:99],a[136:139],v[144:147]
v_mfma_f32_16x16x32_bf16 v[144:147],v[100:103],a[152:155],v[144:147]
v_mfma_f32_16x16x32_bf16 v[144:147],v[104:107],a[168:171],v[144:147]
v_mfma_f32_16x16x32_bf16 v[144:147],v[108:111],a[184:187],v[144:147]
v_cvt_pk_bf16_f32 v128,v128,v129
v_cvt_pk_bf16_f32 v129,v130,v131
v_mfma_f32_16x16x32_bf16 v[152:155],v[108:111],a[188:191],v[152:155]
ds_read_b64_tr_b16 v[108:109],v1 offset:30976
ds_read_b64_tr_b16 v[110:111],v1 offset:36096
v_mfma_f32_16x16x32_bf16 v[152:155],v[104:107],a[172:175],v[152:155]
v_cvt_pk_bf16_f32 v136,v136,v137
v_cvt_pk_bf16_f32 v137,v138,v139
v_mfma_f32_16x16x32_bf16 v[152:155],v[100:103],a[156:159],v[152:155]
v_cvt_pk_bf16_f32 v144,v144,v145
v_cvt_pk_bf16_f32 v145,v146,v147
v_mfma_f32_16x16x32_bf16 v[152:155],v[96:99],a[140:143],v[152:155]
ds_read_b64_tr_b16 v[96:97],v1 offset:256
ds_read_b64_tr_b16 v[98:99],v1 offset:5376
v_mfma_f32_16x16x32_bf16 v[160:163],v[112:115],a[128:131],v[160:163]
ds_read_b64_tr_b16 v[100:101],v1 offset:10496
v_mfma_f32_16x16x32_bf16 v[160:163],v[116:119],a[144:147],v[160:163]
s_waitcnt lgkmcnt(5)
ds_read_b64_tr_b16 v[102:103],v1 offset:15616
v_mfma_f32_16x16x32_bf16 v[160:163],v[120:123],a[160:163],v[160:163]
ds_read_b64_tr_b16 v[104:105],v1 offset:20736
v_mfma_f32_16x16x32_bf16 v[160:163],v[124:127],a[176:179],v[160:163]
v_cvt_pk_bf16_f32 v152,v152,v153
v_cvt_pk_bf16_f32 v153,v154,v155
v_mfma_f32_16x16x32_bf16 v[168:171],v[124:127],a[180:183],v[168:171]
ds_read_b64_tr_b16 v[106:107],v1 offset:25856
v_add_u32 v1,0x1e000,v1
v_mfma_f32_16x16x32_bf16 v[168:171],v[120:123],a[164:167],v[168:171]
v_mfma_f32_16x16x32_bf16 v[168:171],v[116:119],a[148:151],v[168:171]
v_mfma_f32_16x16x32_bf16 v[168:171],v[112:115],a[132:135],v[168:171]
v_cvt_pk_bf16_f32 v130,v160,v161
v_cvt_pk_bf16_f32 v131,v162,v163
v_mfma_f32_16x16x32_bf16 v[176:179],v[112:115],a[136:139],v[176:179]
buffer_store_dwordx4 v[128:131],v6,s[36:39],s79 offen sc1
v_mfma_f32_16x16x32_bf16 v[176:179],v[116:119],a[152:155],v[176:179]
v_mfma_f32_16x16x32_bf16 v[176:179],v[120:123],a[168:171],v[176:179]
v_mfma_f32_16x16x32_bf16 v[176:179],v[124:127],a[184:187],v[176:179]
v_mfma_f32_16x16x32_bf16 v[184:187],v[124:127],a[188:191],v[184:187]
v_cvt_pk_bf16_f32 v138,v168,v169
v_cvt_pk_bf16_f32 v139,v170,v171
v_mfma_f32_16x16x32_bf16 v[184:187],v[120:123],a[172:175],v[184:187]
buffer_store_dwordx4 v[136:139],v6,s[36:39],s80 offen sc1
v_mfma_f32_16x16x32_bf16 v[184:187],v[116:119],a[156:159],v[184:187]
ds_read_b64_tr_b16 v[116:117],v2 offset:10528
ds_read_b64_tr_b16 v[118:119],v2 offset:15648
v_mfma_f32_16x16x32_bf16 v[184:187],v[112:115],a[140:143],v[184:187]
v_cvt_pk_bf16_f32 v146,v176,v177
v_cvt_pk_bf16_f32 v147,v178,v179
s_waitcnt vmcnt(6)
s_barrier
v_mfma_f32_16x16x32_bf16 v[192:195],v[64:67],a[68:71],v[192:195]
buffer_store_dwordx4 v[144:147],v6,s[36:39],s81 offen sc1
v_mfma_f32_16x16x32_bf16 v[192:195],v[68:71],a[76:79],v[192:195]
ds_read_b64_tr_b16 v[112:113],v2 offset:288
ds_read_b64_tr_b16 v[114:115],v2 offset:5408
v_mfma_f32_16x16x32_bf16 v[192:195],v[72:75],a[84:87],v[192:195]
buffer_store_dwordx2 v[58:59],v60,s[44:47],s18 offen offset:2312 sc1
v_mfma_f32_16x16x32_bf16 v[192:195],v[76:79],a[92:95],v[192:195]
v_cvt_pk_bf16_f32 v154,v184,v185
v_cvt_pk_bf16_f32 v155,v186,v187
v_mfma_f32_16x16x32_bf16 v[200:203],v[76:79],a[124:127],v[200:203]
buffer_store_dwordx4 v[152:155],v28,s[36:39],s82 offen sc1
v_mfma_f32_16x16x32_bf16 v[200:203],v[72:75],a[116:119],v[200:203]
ds_read_b64_tr_b16 v[120:121],v2 offset:20768
ds_read_b64_tr_b16 v[122:123],v2 offset:25888
v_mfma_f32_16x16x32_bf16 v[200:203],v[68:71],a[108:111],v[200:203]
ds_read_b64_tr_b16 v[124:125],v2 offset:31008
v_mfma_f32_16x16x32_bf16 v[200:203],v[64:67],a[100:103],v[200:203]
ds_read_b64_tr_b16 v[126:127],v2 offset:36128
v_add_u32 v2,0x1e000,v2
v_mfma_f32_16x16x32_bf16 v[208:211],v[64:67],a[64:67],v[208:211]
v_mfma_f32_16x16x32_bf16 v[208:211],v[68:71],a[72:75],v[208:211]
v_mfma_f32_16x16x32_bf16 v[208:211],v[72:75],a[80:83],v[208:211]
v_mfma_f32_16x16x32_bf16 v[208:211],v[76:79],a[88:91],v[208:211]
v_mfma_f32_16x16x32_bf16 v[216:219],v[76:79],a[120:123],v[216:219]
ds_read_b64_tr_b16 v[76:77],v1 offset:30976
ds_read_b64_tr_b16 v[78:79],v1 offset:36096
v_mfma_f32_16x16x32_bf16 v[216:219],v[72:75],a[112:115],v[216:219]
ds_read_b64_tr_b16 v[72:73],v1 offset:20736
v_mfma_f32_16x16x32_bf16 v[216:219],v[68:71],a[104:107],v[216:219]
ds_read_b64_tr_b16 v[68:69],v1 offset:10496
v_mfma_f32_16x16x32_bf16 v[216:219],v[64:67],a[96:99],v[216:219]
ds_read_b64_tr_b16 v[64:65],v1 offset:256
v_mfma_f32_16x16x32_bf16 v[224:227],v[80:83],a[68:71],v[224:227]
ds_read_b64_tr_b16 v[66:67],v1 offset:5376
ds_read_b64_tr_b16 v[70:71],v1 offset:15616
v_mfma_f32_16x16x32_bf16 v[224:227],v[84:87],a[76:79],v[224:227]
ds_read_b64_tr_b16 v[74:75],v1 offset:25856
v_mfma_f32_16x16x32_bf16 v[224:227],v[88:91],a[84:87],v[224:227]
v_mfma_f32_16x16x32_bf16 v[224:227],v[92:95],a[92:95],v[224:227]
v_mfma_f32_16x16x32_bf16 v[232:235],v[92:95],a[124:127],v[232:235]
v_mfma_f32_16x16x32_bf16 v[232:235],v[88:91],a[116:119],v[232:235]
v_mfma_f32_16x16x32_bf16 v[232:235],v[84:87],a[108:111],v[232:235]
v_mfma_f32_16x16x32_bf16 v[232:235],v[80:83],a[100:103],v[232:235]
v_mfma_f32_16x16x32_bf16 v[240:243],v[80:83],a[64:67],v[240:243]
v_mfma_f32_16x16x32_bf16 v[240:243],v[84:87],a[72:75],v[240:243]
v_mfma_f32_16x16x32_bf16 v[240:243],v[88:91],a[80:83],v[240:243]
v_mfma_f32_16x16x32_bf16 v[240:243],v[92:95],a[88:91],v[240:243]
v_mfma_f32_16x16x32_bf16 v[248:251],v[92:95],a[120:123],v[248:251]
ds_read_b64_tr_b16 v[92:93],v2 offset:31008
ds_read_b64_tr_b16 v[94:95],v2 offset:36128
v_mfma_f32_16x16x32_bf16 v[248:251],v[88:91],a[112:115],v[248:251]
ds_read_b64_tr_b16 v[88:89],v2 offset:20768
v_mfma_f32_16x16x32_bf16 v[248:251],v[84:87],a[104:107],v[248:251]
ds_read_b64_tr_b16 v[84:85],v2 offset:10528
v_mfma_f32_16x16x32_bf16 v[248:251],v[80:83],a[96:99],v[248:251]
s_waitcnt lgkmcnt(4)
ds_read_b64_tr_b16 v[80:81],v2 offset:288
v_mfma_f32_16x16x32_bf16 v[192:195],v[96:99],v[32:35],v[192:195]
ds_read_b64_tr_b16 v[82:83],v2 offset:5408
ds_read_b64_tr_b16 v[86:87],v2 offset:15648
v_mfma_f32_16x16x32_bf16 v[192:195],v[100:103],v[36:39],v[192:195]
ds_read_b64_tr_b16 v[90:91],v2 offset:25888
v_mfma_f32_16x16x32_bf16 v[192:195],v[104:107],v[40:43],v[192:195]
v_mfma_f32_16x16x32_bf16 v[192:195],v[108:111],v[44:47],v[192:195]
v_mfma_f32_16x16x32_bf16 v[200:203],v[108:111],v[180:183],v[200:203]
v_mfma_f32_16x16x32_bf16 v[200:203],v[104:107],v[164:167],v[200:203]
v_mfma_f32_16x16x32_bf16 v[200:203],v[100:103],v[148:151],v[200:203]
v_mfma_f32_16x16x32_bf16 v[200:203],v[96:99],v[132:135],v[200:203]
v_mfma_f32_16x16x32_bf16 v[208:211],v[96:99],v[196:199],v[208:211]
v_mfma_f32_16x16x32_bf16 v[208:211],v[100:103],v[212:215],v[208:211]
v_mfma_f32_16x16x32_bf16 v[208:211],v[104:107],v[228:231],v[208:211]
v_mfma_f32_16x16x32_bf16 v[208:211],v[108:111],v[244:247],v[208:211]
v_mfma_f32_16x16x32_bf16 v[216:219],v[108:111],v[188:191],v[216:219]
v_mfma_f32_16x16x32_bf16 v[216:219],v[104:107],v[172:175],v[216:219]
v_mfma_f32_16x16x32_bf16 v[216:219],v[100:103],v[156:159],v[216:219]
v_mfma_f32_16x16x32_bf16 v[216:219],v[96:99],v[140:143],v[216:219]
v_mfma_f32_16x16x32_bf16 v[224:227],v[112:115],v[32:35],v[224:227]
v_mfma_f32_16x16x32_bf16 v[224:227],v[116:119],v[36:39],v[224:227]
v_mfma_f32_16x16x32_bf16 v[224:227],v[120:123],v[40:43],v[224:227]
v_mfma_f32_16x16x32_bf16 v[224:227],v[124:127],v[44:47],v[224:227]
v_mfma_f32_16x16x32_bf16 v[232:235],v[124:127],v[180:183],v[232:235]
v_mfma_f32_16x16x32_bf16 v[232:235],v[120:123],v[164:167],v[232:235]
v_mfma_f32_16x16x32_bf16 v[232:235],v[116:119],v[148:151],v[232:235]
v_mfma_f32_16x16x32_bf16 v[232:235],v[112:115],v[132:135],v[232:235]
v_mfma_f32_16x16x32_bf16 v[240:243],v[112:115],v[196:199],v[240:243]
v_mfma_f32_16x16x32_bf16 v[240:243],v[116:119],v[212:215],v[240:243]
v_mfma_f32_16x16x32_bf16 v[240:243],v[120:123],v[228:231],v[240:243]
v_mfma_f32_16x16x32_bf16 v[240:243],v[124:127],v[244:247],v[240:243]
v_mfma_f32_16x16x32_bf16 v[248:251],v[124:127],v[188:191],v[248:251]
v_mfma_f32_16x16x32_bf16 v[248:251],v[120:123],v[172:175],v[248:251]
v_mfma_f32_16x16x32_bf16 v[248:251],v[116:119],v[156:159],v[248:251]
v_mfma_f32_16x16x32_bf16 v[248:251],v[112:115],v[140:143],v[248:251]
v_mfma_f32_16x16x32_bf16 v[192:195],v[64:67],a[128:131],v[192:195]
v_mfma_f32_16x16x32_bf16 v[192:195],v[68:71],a[144:147],v[192:195]
v_mfma_f32_16x16x32_bf16 v[192:195],v[72:75],a[160:163],v[192:195]
v_mfma_f32_16x16x32_bf16 v[192:195],v[76:79],a[176:179],v[192:195]
v_mfma_f32_16x16x32_bf16 v[200:203],v[76:79],a[180:183],v[200:203]
v_mfma_f32_16x16x32_bf16 v[200:203],v[72:75],a[164:167],v[200:203]
v_mfma_f32_16x16x32_bf16 v[200:203],v[68:71],a[148:151],v[200:203]
v_mfma_f32_16x16x32_bf16 v[200:203],v[64:67],a[132:135],v[200:203]
v_mfma_f32_16x16x32_bf16 v[208:211],v[64:67],a[136:139],v[208:211]
v_mfma_f32_16x16x32_bf16 v[208:211],v[68:71],a[152:155],v[208:211]
v_mfma_f32_16x16x32_bf16 v[208:211],v[72:75],a[168:171],v[208:211]
v_mfma_f32_16x16x32_bf16 v[208:211],v[76:79],a[184:187],v[208:211]
v_mfma_f32_16x16x32_bf16 v[216:219],v[76:79],a[188:191],v[216:219]
v_mfma_f32_16x16x32_bf16 v[216:219],v[72:75],a[172:175],v[216:219]
v_mfma_f32_16x16x32_bf16 v[216:219],v[68:71],a[156:159],v[216:219]
v_mfma_f32_16x16x32_bf16 v[216:219],v[64:67],a[140:143],v[216:219]
s_waitcnt lgkmcnt(0)
v_mfma_f32_16x16x32_bf16 v[224:227],v[80:83],a[128:131],v[224:227]
buffer_load_dwordx2 v[62:63],v61,s[44:47],s91 offen offset:2312 sc1
v_mfma_f32_16x16x32_bf16 v[224:227],v[84:87],a[144:147],v[224:227]
v_mfma_f32_16x16x32_bf16 v[224:227],v[88:91],a[160:163],v[224:227]
v_mfma_f32_16x16x32_bf16 v[224:227],v[92:95],a[176:179],v[224:227]
v_mfma_f32_16x16x32_bf16 v[232:235],v[92:95],a[180:183],v[232:235]
v_mfma_f32_16x16x32_bf16 v[232:235],v[88:91],a[164:167],v[232:235]
v_mfma_f32_16x16x32_bf16 v[232:235],v[84:87],a[148:151],v[232:235]
v_mfma_f32_16x16x32_bf16 v[232:235],v[80:83],a[132:135],v[232:235]
v_mfma_f32_16x16x32_bf16 v[240:243],v[80:83],a[136:139],v[240:243]
v_mfma_f32_16x16x32_bf16 v[240:243],v[84:87],a[152:155],v[240:243]
v_mfma_f32_16x16x32_bf16 v[240:243],v[88:91],a[168:171],v[240:243]
v_mfma_f32_16x16x32_bf16 v[240:243],v[92:95],a[184:187],v[240:243]
v_mfma_f32_16x16x32_bf16 v[248:251],v[92:95],a[188:191],v[248:251]
v_mfma_f32_16x16x32_bf16 v[248:251],v[88:91],a[172:175],v[248:251]
v_mfma_f32_16x16x32_bf16 v[248:251],v[84:87],a[156:159],v[248:251]
v_mfma_f32_16x16x32_bf16 v[248:251],v[80:83],a[140:143],v[248:251]
s_waitcnt vmcnt(1)
s_barrier
buffer_store_dwordx2 v[20:21],v22,s[44:47],s18 offen offset:2304 sc1
s_waitcnt vmcnt(1)
v_cmp_ne_u32 s[56:57],s52,v62
v_cmp_ne_u32 s[58:59],s53,v63
s_nop 0
s_or_b64 s[56:57],s[56:57],s[58:59]
s_cbranch_scc1 L_fr4_%=
L_fo4_%=:
buffer_load_dwordx4 v[64:67],v6,s[36:39],s66 offen sc1
buffer_load_dwordx4 v[68:71],v6,s[36:39],s67 offen sc1
)A"
R"A(buffer_load_dwordx4 v[72:75],v6,s[36:39],s66 offen offset:1024 sc1
buffer_load_dwordx4 v[76:79],v6,s[36:39],s67 offen offset:1024 sc1
buffer_load_dwordx4 v[80:83],v6,s[36:39],s66 offen offset:2048 sc1
buffer_load_dwordx4 v[84:87],v6,s[36:39],s67 offen offset:2048 sc1
buffer_load_dwordx4 v[88:91],v28,s[36:39],s66 offen offset:3072 sc1
buffer_load_dwordx4 v[92:95],v28,s[36:39],s67 offen offset:3072 sc1
buffer_load_dwordx2 v[4:5],v3,s[44:47],s91 offen offset:2304 sc1
s_waitcnt vmcnt(8)
v_mfma_f32_16x16x32_bf16 v[192:195],v[48:51],v[64:67],v[192:195]
s_waitcnt vmcnt(7)
v_mfma_f32_16x16x32_bf16 v[192:195],v[48:51],v[68:71],v[192:195]
v_mfma_f32_16x16x32_bf16 v[224:227],v[52:55],v[64:67],v[224:227]
v_mfma_f32_16x16x32_bf16 v[224:227],v[52:55],v[68:71],v[224:227]
s_waitcnt vmcnt(6)
v_mfma_f32_16x16x32_bf16 v[200:203],v[48:51],v[72:75],v[200:203]
s_waitcnt vmcnt(5)
v_mfma_f32_16x16x32_bf16 v[200:203],v[48:51],v[76:79],v[200:203]
v_mfma_f32_16x16x32_bf16 v[232:235],v[52:55],v[72:75],v[232:235]
v_mfma_f32_16x16x32_bf16 v[232:235],v[52:55],v[76:79],v[232:235]
s_waitcnt vmcnt(4)
v_mfma_f32_16x16x32_bf16 v[208:211],v[48:51],v[80:83],v[208:211]
s_waitcnt vmcnt(3)
v_mfma_f32_16x16x32_bf16 v[208:211],v[48:51],v[84:87],v[208:211]
v_mfma_f32_16x16x32_bf16 v[240:243],v[52:55],v[80:83],v[240:243]
v_mfma_f32_16x16x32_bf16 v[240:243],v[52:55],v[84:87],v[240:243]
s_waitcnt vmcnt(2)
v_mfma_f32_16x16x32_bf16 v[216:219],v[48:51],v[88:91],v[216:219]
s_waitcnt vmcnt(1)
v_mfma_f32_16x16x32_bf16 v[216:219],v[48:51],v[92:95],v[216:219]
v_mfma_f32_16x16x32_bf16 v[248:251],v[52:55],v[88:91],v[248:251]
v_mfma_f32_16x16x32_bf16 v[248:251],v[52:55],v[92:95],v[248:251]
s_waitcnt vmcnt(0)
v_cmp_ne_u32 s[56:57],s52,v4
v_cmp_ne_u32 s[58:59],s53,v5
s_nop 0
s_or_b64 s[56:57],s[56:57],s[58:59]
s_cbranch_scc1 L_fr5_%=
L_fo5_%=:
buffer_load_dwordx4 v[96:99],v6,s[36:39],s68 offen sc1
buffer_load_dwordx4 v[100:103],v6,s[36:39],s68 offen offset:1024 sc1
buffer_load_dwordx4 v[104:107],v6,s[36:39],s68 offen offset:2048 sc1
buffer_load_dwordx4 v[108:111],v6,s[36:39],s68 offen offset:3072 sc1
buffer_load_dwordx4 v[112:115],v6,s[36:39],s65 offen sc1
buffer_load_dwordx4 v[116:119],v6,s[36:39],s65 offen offset:1024 sc1
buffer_load_dwordx4 v[120:123],v6,s[36:39],s65 offen offset:2048 sc1
buffer_load_dwordx4 v[124:127],v28,s[36:39],s65 offen offset:3072 sc1
s_waitcnt vmcnt(7)
v_mfma_f32_16x16x32_bf16 v[128:131],v[48:51],v[96:99],0
s_waitcnt vmcnt(6)
v_mfma_f32_16x16x32_bf16 v[128:131],v[48:51],v[100:103],v[128:131]
s_waitcnt vmcnt(5)
v_mfma_f32_16x16x32_bf16 v[128:131],v[48:51],v[104:107],v[128:131]
s_waitcnt vmcnt(4)
v_mfma_f32_16x16x32_bf16 v[128:131],v[48:51],v[108:111],v[128:131]
v_mfma_f32_16x16x32_bf16 v[160:163],v[52:55],v[96:99],0
v_mfma_f32_16x16x32_bf16 v[160:163],v[52:55],v[100:103],v[160:163]
v_mfma_f32_16x16x32_bf16 v[160:163],v[52:55],v[104:107],v[160:163]
v_mfma_f32_16x16x32_bf16 v[160:163],v[52:55],v[108:111],v[160:163]
s_waitcnt vmcnt(3)
v_mfma_f32_16x16x32_bf16 v[192:195],v[48:51],v[112:115],v[192:195]
v_mfma_f32_16x16x32_bf16 v[224:227],v[52:55],v[112:115],v[224:227]
s_waitcnt vmcnt(2)
v_mfma_f32_16x16x32_bf16 v[200:203],v[48:51],v[116:119],v[200:203]
v_mfma_f32_16x16x32_bf16 v[232:235],v[52:55],v[116:119],v[232:235]
s_waitcnt vmcnt(1)
v_cvt_pk_bf16_f32 v128,v128,v129
v_cvt_pk_bf16_f32 v129,v130,v131
v_mfma_f32_16x16x32_bf16 v[208:211],v[48:51],v[120:123],v[208:211]
v_mfma_f32_16x16x32_bf16 v[240:243],v[52:55],v[120:123],v[240:243]
s_waitcnt vmcnt(0)
v_cvt_pk_bf16_f32 v130,v160,v161
v_cvt_pk_bf16_f32 v131,v162,v163
v_mfma_f32_16x16x32_bf16 v[216:219],v[48:51],v[124:127],v[216:219]
v_cvt_pk_bf16_f32 v192,v192,v193
v_cvt_pk_bf16_f32 v193,v194,v195
v_cvt_pk_bf16_f32 v194,v224,v225
v_cvt_pk_bf16_f32 v195,v226,v227
v_cvt_pk_bf16_f32 v200,v200,v201
v_cvt_pk_bf16_f32 v201,v202,v203
v_mfma_f32_16x16x32_bf16 v[248:251],v[52:55],v[124:127],v[248:251]
v_permlane16_swap_b32 v128,v130
v_permlane16_swap_b32 v129,v131
v_cvt_pk_bf16_f32 v202,v232,v233
v_cvt_pk_bf16_f32 v203,v234,v235
v_cvt_pk_bf16_f32 v208,v208,v209
v_cvt_pk_bf16_f32 v209,v210,v211
v_cvt_pk_bf16_f32 v210,v240,v241
v_cvt_pk_bf16_f32 v211,v242,v243
buffer_store_dwordx4 v[128:131],v56,s[24:27],s83 offen offset:256 sc1 nt
v_permlane16_swap_b32 v192,v194
v_permlane16_swap_b32 v193,v195
buffer_store_dwordx4 v[192:195],v56,s[24:27],s87 offen sc1 nt
v_permlane16_swap_b32 v200,v202
v_permlane16_swap_b32 v201,v203
v_cvt_pk_bf16_f32 v216,v216,v217
v_cvt_pk_bf16_f32 v217,v218,v219
v_cvt_pk_bf16_f32 v218,v248,v249
v_cvt_pk_bf16_f32 v219,v250,v251
buffer_store_dwordx4 v[200:203],v56,s[24:27],s88 offen sc1 nt
v_permlane16_swap_b32 v208,v210
v_permlane16_swap_b32 v209,v211
buffer_store_dwordx4 v[208:211],v56,s[24:27],s89 offen sc1 nt
v_permlane16_swap_b32 v216,v218
v_permlane16_swap_b32 v217,v219
buffer_store_dwordx4 v[216:219],v56,s[24:27],s90 offen sc1 nt
s_lshl_b32 s69,s62,20
s_add_u32 s69,s69,s18
buffer_store_dword v20,v22,s[44:47],s69 offen sc1
L_geF_%=:
s_branch L_end_%=
.p2align 12
L_pd19_%=:
s_cbranch_execz L_trt1_%=
L_GS_%=:
L_gbS_%=:
s_waitcnt lgkmcnt(14)
v_mfma_f32_16x16x32_bf16 a[192:195],v[64:67],a[68:71],0
buffer_store_dwordx4 v[32:35],v6,s[40:43],s79 offen sc1
s_waitcnt lgkmcnt(12)
v_mfma_f32_16x16x32_bf16 a[192:195],v[68:71],a[76:79],a[192:195]
s_lshl_b32 s91,s63,8
v_bfe_u32 v3,v6,4,2
v_min_u32 v3,2,v3
s_waitcnt lgkmcnt(10)
v_mfma_f32_16x16x32_bf16 a[192:195],v[72:75],a[84:87],a[192:195]
s_add_u32 s87,s64,1
v_add3_u32 v3,v3,s64,1
v_and_b32 v3,3,v3
s_waitcnt lgkmcnt(8)
v_mfma_f32_16x16x32_bf16 a[192:195],v[76:79],a[92:95],a[192:195]
s_and_b32 s87,s87,3
ds_read_b64_tr_b16 v[96:97],v1 offset:64
v_lshlrev_b32 v3,6,v3
v_mfma_f32_16x16x32_bf16 a[196:199],v[76:79],a[124:127],0
s_lshl_b32 s87,s87,1
ds_read_b64_tr_b16 v[98:99],v1 offset:5184
ds_read_b64_tr_b16 v[100:101],v1 offset:10304
v_mfma_f32_16x16x32_bf16 a[196:199],v[72:75],a[116:119],a[196:199]
s_lshl_b32 s87,s87,16
ds_read_b64_tr_b16 v[102:103],v1 offset:15424
v_lshlrev_b32 v29,10,v6
v_mfma_f32_16x16x32_bf16 a[196:199],v[68:71],a[108:111],a[196:199]
s_add_u32 s88,s87,4096
ds_read_b64_tr_b16 v[104:105],v1 offset:20544
v_accvgpr_read_b32 v16,a24
v_mfma_f32_16x16x32_bf16 a[196:199],v[64:67],a[100:103],a[196:199]
s_add_u32 s89,s87,8192
ds_read_b64_tr_b16 v[106:107],v1 offset:25664
v_accvgpr_read_b32 v17,a25
v_mfma_f32_16x16x32_bf16 a[200:203],v[64:67],a[64:67],0
s_add_u32 s90,s87,12288
ds_read_b64_tr_b16 v[108:109],v1 offset:30784
ds_read_b64_tr_b16 v[110:111],v1 offset:35904
v_mfma_f32_16x16x32_bf16 a[200:203],v[68:71],a[72:75],a[200:203]
s_add_u32 m0,s86,122880
ds_read_b64_tr_b16 v[112:113],v2 offset:96
v_accvgpr_read_b32 v18,a26
v_mfma_f32_16x16x32_bf16 a[200:203],v[72:75],a[80:83],a[200:203]
s_add_u32 s83,s64,3
ds_read_b64_tr_b16 v[114:115],v2 offset:5216
v_accvgpr_read_b32 v19,a27
v_mfma_f32_16x16x32_bf16 a[200:203],v[76:79],a[88:91],a[200:203]
s_and_b32 s83,s83,3
ds_read_b64_tr_b16 v[116:117],v2 offset:10336
v_accvgpr_read_b32 v20,a28
v_mfma_f32_16x16x32_bf16 a[204:207],v[76:79],a[120:123],0
s_lshl_b32 s83,s83,1
ds_read_b64_tr_b16 v[118:119],v2 offset:15456
ds_read_b64_tr_b16 v[120:121],v2 offset:20576
v_mfma_f32_16x16x32_bf16 a[204:207],v[72:75],a[112:115],a[204:207]
s_mul_i32 s83,s83,163840
ds_read_b64_tr_b16 v[122:123],v2 offset:25696
v_accvgpr_read_b32 v21,a29
v_mfma_f32_16x16x32_bf16 a[204:207],v[68:71],a[104:107],a[204:207]
s_add_u32 s83,s83,s85
ds_read_b64_tr_b16 v[124:125],v2 offset:30816
v_accvgpr_read_b32 v22,a30
v_mfma_f32_16x16x32_bf16 a[204:207],v[64:67],a[96:99],a[204:207]
buffer_store_dwordx4 v[36:39],v6,s[40:43],s80 offen sc1
s_waitcnt lgkmcnt(9)
v_mfma_f32_16x16x32_bf16 a[208:211],v[80:83],a[68:71],0
s_add_u32 s84,s83,20480
ds_read_b64_tr_b16 v[126:127],v2 offset:35936
ds_read_b64_tr_b16 v[64:65],v1 offset:128
v_mfma_f32_16x16x32_bf16 a[208:211],v[84:87],a[76:79],a[208:211]
s_lshl_b32 s66,s61,3
ds_read_b64_tr_b16 v[66:67],v1 offset:5248
v_accvgpr_read_b32 v23,a31
v_mfma_f32_16x16x32_bf16 a[208:211],v[88:91],a[84:87],a[208:211]
s_add_u32 s66,s66,s64
ds_read_b64_tr_b16 v[68:69],v1 offset:10368
v_mul_f32 v16,v16,v49
v_mfma_f32_16x16x32_bf16 a[208:211],v[92:95],a[92:95],a[208:211]
s_mul_i32 s66,s66,262144
ds_read_b64_tr_b16 v[70:71],v1 offset:15488
v_mul_f32 v17,v17,v49
v_mfma_f32_16x16x32_bf16 a[212:215],v[92:95],a[124:127],0
s_waitcnt vmcnt(21)
buffer_load_dwordx2 v[4:5],v3,s[44:47],s91 offen offset:256 sc1
v_mfma_f32_16x16x32_bf16 a[212:215],v[88:91],a[116:119],a[212:215]
s_add_u32 s66,s66,100663296
ds_read_b64_tr_b16 v[72:73],v1 offset:20608
ds_read_b64_tr_b16 v[74:75],v1 offset:25728
v_mfma_f32_16x16x32_bf16 a[212:215],v[84:87],a[108:111],a[212:215]
s_mov_b32 s38,262144
ds_read_b64_tr_b16 v[76:77],v1 offset:30848
v_mul_f32 v18,v18,v49
v_mfma_f32_16x16x32_bf16 a[212:215],v[80:83],a[100:103],a[212:215]
s_mov_b32 s39,0x20000
ds_read_b64_tr_b16 v[78:79],v1 offset:35968
v_mul_f32 v19,v19,v49
v_mfma_f32_16x16x32_bf16 a[216:219],v[80:83],a[64:67],0
s_add_u32 s74,s63,1
v_mul_f32 v20,v20,v49
v_mul_f32 v21,v21,v49
v_mfma_f32_16x16x32_bf16 a[216:219],v[84:87],a[72:75],a[216:219]
s_and_b32 s74,s74,3
v_mul_f32 v22,v22,v49
v_mul_f32 v23,v23,v49
v_mfma_f32_16x16x32_bf16 a[216:219],v[88:91],a[80:83],a[216:219]
s_lshl_b32 s74,s74,2
v_cvt_pk_bf16_f32 v44,v16,v17
v_cvt_pk_bf16_f32 v45,v18,v19
v_mfma_f32_16x16x32_bf16 a[216:219],v[92:95],a[88:91],a[216:219]
s_add_u32 s74,s74,s60
v_cvt_pk_bf16_f32 v46,v20,v21
v_cvt_pk_bf16_f32 v47,v22,v23
v_mfma_f32_16x16x32_bf16 a[220:223],v[92:95],a[120:123],0
s_mul_i32 s74,s74,16384
ds_read_b64_tr_b16 v[92:93],v2 offset:30880
ds_read_b64_tr_b16 v[94:95],v2 offset:36000
v_mfma_f32_16x16x32_bf16 a[220:223],v[88:91],a[112:115],a[220:223]
s_add_u32 s74,s74,8192
ds_read_b64_tr_b16 v[88:89],v2 offset:20640
v_mul_f32 v132,v132,v53
v_mfma_f32_16x16x32_bf16 a[220:223],v[84:87],a[104:107],a[220:223]
buffer_store_dwordx4 v[40:43],v6,s[40:43],s81 offen sc1
v_mfma_f32_16x16x32_bf16 a[220:223],v[80:83],a[96:99],a[220:223]
s_add_u32 s75,s63,2
ds_read_b64_tr_b16 v[80:81],v2 offset:160
ds_read_b64_tr_b16 v[82:83],v2 offset:5280
v_mfma_f32_16x16x32_bf16 a[224:227],v[96:99],a[68:71],0
s_and_b32 s75,s75,3
ds_read_b64_tr_b16 v[84:85],v2 offset:10400
v_mul_f32 v133,v133,v53
v_mfma_f32_16x16x32_bf16 a[224:227],v[100:103],a[76:79],a[224:227]
s_lshl_b32 s75,s75,2
ds_read_b64_tr_b16 v[86:87],v2 offset:15520
v_mul_f32 v134,v134,v53
v_mfma_f32_16x16x32_bf16 a[224:227],v[104:107],a[84:87],a[224:227]
s_add_u32 s75,s75,s60
ds_read_b64_tr_b16 v[90:91],v2 offset:25760
v_mul_f32 v135,v135,v53
s_waitcnt lgkmcnt(8)
v_mfma_f32_16x16x32_bf16 a[224:227],v[108:111],a[92:95],a[224:227]
s_mul_i32 s75,s75,16384
v_mul_f32 v140,v140,v53
v_mul_f32 v141,v141,v53
v_mfma_f32_16x16x32_bf16 a[228:231],v[108:111],a[124:127],0
s_add_u32 s75,s75,4096
v_mul_f32 v142,v142,v53
v_mul_f32 v143,v143,v53
v_mfma_f32_16x16x32_bf16 a[228:231],v[104:107],a[116:119],a[228:231]
s_add_u32 s76,s63,3
v_cvt_pk_bf16_f32 v132,v132,v133
v_cvt_pk_bf16_f32 v133,v134,v135
v_mfma_f32_16x16x32_bf16 a[228:231],v[100:103],a[108:111],a[228:231]
s_and_b32 s76,s76,3
v_cvt_pk_bf16_f32 v134,v140,v141
v_cvt_pk_bf16_f32 v135,v142,v143
v_mfma_f32_16x16x32_bf16 a[228:231],v[96:99],a[100:103],a[228:231]
s_lshl_b32 s76,s76,2
v_mul_f32 v148,v148,v53
v_mul_f32 v149,v149,v53
v_mfma_f32_16x16x32_bf16 a[232:235],v[96:99],a[64:67],0
s_add_u32 s76,s76,s60
v_mul_f32 v150,v150,v53
v_mul_f32 v151,v151,v53
v_mfma_f32_16x16x32_bf16 a[232:235],v[100:103],a[72:75],a[232:235]
s_mul_i32 s76,s76,16384
v_mul_f32 v156,v156,v53
v_mul_f32 v157,v157,v53
v_mfma_f32_16x16x32_bf16 a[232:235],v[104:107],a[80:83],a[232:235]
s_add_u32 s76,s76,0
v_mul_f32 v158,v158,v53
v_mul_f32 v159,v159,v53
v_mfma_f32_16x16x32_bf16 a[232:235],v[108:111],a[88:91],a[232:235]
s_lshl_b32 s67,s63,10
v_cvt_pk_bf16_f32 v148,v148,v149
v_cvt_pk_bf16_f32 v149,v150,v151
v_mfma_f32_16x16x32_bf16 a[236:239],v[108:111],a[120:123],0
s_mov_b32 s26,307200
ds_read_b64_tr_b16 v[108:109],v1 offset:30912
ds_read_b64_tr_b16 v[110:111],v1 offset:36032
v_mfma_f32_16x16x32_bf16 a[236:239],v[104:107],a[112:115],a[236:239]
buffer_store_dwordx4 v[44:47],v6,s[40:43],s82 offen sc1
)A"
R"A(v_mfma_f32_16x16x32_bf16 a[236:239],v[100:103],a[104:107],a[236:239]
s_mov_b32 s27,0x20000
ds_read_b64_tr_b16 v[100:101],v1 offset:10432
ds_read_b64_tr_b16 v[102:103],v1 offset:15552
v_mfma_f32_16x16x32_bf16 a[236:239],v[96:99],a[96:99],a[236:239]
s_lshl_b32 s77,s63,2
ds_read_b64_tr_b16 v[96:97],v1 offset:192
v_cvt_pk_bf16_f32 v150,v156,v157
v_mfma_f32_16x16x32_bf16 a[240:243],v[112:115],a[68:71],0
s_add_u32 s77,s77,s60
ds_read_b64_tr_b16 v[98:99],v1 offset:5312
v_cvt_pk_bf16_f32 v151,v158,v159
v_mfma_f32_16x16x32_bf16 a[240:243],v[116:119],a[76:79],a[240:243]
s_mul_i32 s77,s77,16384
ds_read_b64_tr_b16 v[104:105],v1 offset:20672
v_mul_f32 v164,v164,v53
v_mfma_f32_16x16x32_bf16 a[240:243],v[120:123],a[84:87],a[240:243]
s_add_u32 s65,s77,0
ds_read_b64_tr_b16 v[106:107],v1 offset:25792
v_mul_f32 v165,v165,v53
v_mfma_f32_16x16x32_bf16 a[240:243],v[124:127],a[92:95],a[240:243]
s_add_u32 s68,s77,12288
v_mul_f32 v166,v166,v53
v_mul_f32 v167,v167,v53
v_mfma_f32_16x16x32_bf16 a[244:247],v[124:127],a[124:127],0
v_mul_f32 v172,v172,v53
v_mul_f32 v173,v173,v53
v_mfma_f32_16x16x32_bf16 a[244:247],v[120:123],a[116:119],a[244:247]
v_mul_f32 v174,v174,v53
v_mul_f32 v175,v175,v53
v_mfma_f32_16x16x32_bf16 a[244:247],v[116:119],a[108:111],a[244:247]
v_cvt_pk_bf16_f32 v164,v164,v165
v_cvt_pk_bf16_f32 v165,v166,v167
v_mfma_f32_16x16x32_bf16 a[244:247],v[112:115],a[100:103],a[244:247]
v_cvt_pk_bf16_f32 v166,v172,v173
v_cvt_pk_bf16_f32 v167,v174,v175
v_mfma_f32_16x16x32_bf16 a[248:251],v[112:115],a[64:67],0
v_mul_f32 v180,v180,v53
v_mul_f32 v181,v181,v53
v_mfma_f32_16x16x32_bf16 a[248:251],v[116:119],a[72:75],a[248:251]
v_mul_f32 v182,v182,v53
v_mul_f32 v183,v183,v53
v_mfma_f32_16x16x32_bf16 a[248:251],v[120:123],a[80:83],a[248:251]
v_mul_f32 v188,v188,v53
v_mul_f32 v189,v189,v53
v_mfma_f32_16x16x32_bf16 a[248:251],v[124:127],a[88:91],a[248:251]
v_mul_f32 v190,v190,v53
v_mul_f32 v191,v191,v53
v_mfma_f32_16x16x32_bf16 a[252:255],v[124:127],a[120:123],0
buffer_load_dword v30,v29,s[8:11],s7 offen
v_mfma_f32_16x16x32_bf16 a[252:255],v[120:123],a[112:115],a[252:255]
ds_read_b64_tr_b16 v[120:121],v2 offset:20704
ds_read_b64_tr_b16 v[122:123],v2 offset:25824
v_mfma_f32_16x16x32_bf16 a[252:255],v[116:119],a[104:107],a[252:255]
buffer_store_dwordx4 v[132:135],v6,s[40:43],s79 offen offset:1024 sc1
v_mfma_f32_16x16x32_bf16 a[252:255],v[112:115],a[96:99],a[252:255]
ds_read_b64_tr_b16 v[112:113],v2 offset:224
ds_read_b64_tr_b16 v[114:115],v2 offset:5344
v_mfma_f32_16x16x32_bf16 a[0:3],v[64:67],a[68:71],0
ds_read_b64_tr_b16 v[116:117],v2 offset:10464
v_cvt_pk_bf16_f32 v180,v180,v181
v_mfma_f32_16x16x32_bf16 a[0:3],v[68:71],a[76:79],a[0:3]
ds_read_b64_tr_b16 v[118:119],v2 offset:15584
v_cvt_pk_bf16_f32 v181,v182,v183
v_mfma_f32_16x16x32_bf16 a[0:3],v[72:75],a[84:87],a[0:3]
ds_read_b64_tr_b16 v[124:125],v2 offset:30944
v_cvt_pk_bf16_f32 v182,v188,v189
v_mfma_f32_16x16x32_bf16 a[0:3],v[76:79],a[92:95],a[0:3]
ds_read_b64_tr_b16 v[126:127],v2 offset:36064
v_cvt_pk_bf16_f32 v183,v190,v191
v_mfma_f32_16x16x32_bf16 a[4:7],v[76:79],a[124:127],0
v_mul_f32 v196,v196,v57
v_mul_f32 v197,v197,v57
v_mfma_f32_16x16x32_bf16 a[4:7],v[72:75],a[116:119],a[4:7]
v_mul_f32 v198,v198,v57
v_mul_f32 v199,v199,v57
v_mfma_f32_16x16x32_bf16 a[4:7],v[68:71],a[108:111],a[4:7]
v_mul_f32 v204,v204,v57
v_mul_f32 v205,v205,v57
v_mfma_f32_16x16x32_bf16 a[4:7],v[64:67],a[100:103],a[4:7]
v_mul_f32 v206,v206,v57
v_mul_f32 v207,v207,v57
v_mfma_f32_16x16x32_bf16 a[8:11],v[64:67],a[64:67],0
v_cvt_pk_bf16_f32 v196,v196,v197
v_cvt_pk_bf16_f32 v197,v198,v199
v_mfma_f32_16x16x32_bf16 a[8:11],v[68:71],a[72:75],a[8:11]
v_cvt_pk_bf16_f32 v198,v204,v205
v_cvt_pk_bf16_f32 v199,v206,v207
v_mfma_f32_16x16x32_bf16 a[8:11],v[72:75],a[80:83],a[8:11]
v_mul_f32 v212,v212,v57
v_mul_f32 v213,v213,v57
v_mfma_f32_16x16x32_bf16 a[8:11],v[76:79],a[88:91],a[8:11]
v_mul_f32 v214,v214,v57
v_mul_f32 v215,v215,v57
v_mfma_f32_16x16x32_bf16 a[12:15],v[76:79],a[120:123],0
ds_read_b64_tr_b16 v[76:77],v1 offset:30976
ds_read_b64_tr_b16 v[78:79],v1 offset:36096
v_mfma_f32_16x16x32_bf16 a[12:15],v[72:75],a[112:115],a[12:15]
buffer_store_dwordx4 v[148:151],v6,s[40:43],s80 offen offset:1024 sc1
v_mfma_f32_16x16x32_bf16 a[12:15],v[68:71],a[104:107],a[12:15]
ds_read_b64_tr_b16 v[68:69],v1 offset:10496
ds_read_b64_tr_b16 v[70:71],v1 offset:15616
v_mfma_f32_16x16x32_bf16 a[12:15],v[64:67],a[96:99],a[12:15]
s_waitcnt lgkmcnt(4)
ds_read_b64_tr_b16 v[64:65],v1 offset:256
v_mul_f32 v220,v220,v57
v_mfma_f32_16x16x32_bf16 a[16:19],v[80:83],a[68:71],0
ds_read_b64_tr_b16 v[66:67],v1 offset:5376
v_mul_f32 v221,v221,v57
v_mfma_f32_16x16x32_bf16 a[16:19],v[84:87],a[76:79],a[16:19]
ds_read_b64_tr_b16 v[72:73],v1 offset:20736
v_mul_f32 v222,v222,v57
v_mfma_f32_16x16x32_bf16 a[16:19],v[88:91],a[84:87],a[16:19]
ds_read_b64_tr_b16 v[74:75],v1 offset:25856
v_add_u32 v1,0xffff6000,v1
v_mfma_f32_16x16x32_bf16 a[16:19],v[92:95],a[92:95],a[16:19]
v_mul_f32 v223,v223,v57
v_cvt_pk_bf16_f32 v212,v212,v213
v_mfma_f32_16x16x32_bf16 a[20:23],v[92:95],a[124:127],0
v_cvt_pk_bf16_f32 v213,v214,v215
v_cvt_pk_bf16_f32 v214,v220,v221
v_mfma_f32_16x16x32_bf16 a[20:23],v[88:91],a[116:119],a[20:23]
v_cvt_pk_bf16_f32 v215,v222,v223
v_mul_f32 v228,v228,v57
v_mfma_f32_16x16x32_bf16 a[20:23],v[84:87],a[108:111],a[20:23]
v_mul_f32 v229,v229,v57
v_mul_f32 v230,v230,v57
v_mfma_f32_16x16x32_bf16 a[20:23],v[80:83],a[100:103],a[20:23]
v_mul_f32 v231,v231,v57
v_mul_f32 v236,v236,v57
v_mfma_f32_16x16x32_bf16 a[24:27],v[80:83],a[64:67],0
v_mul_f32 v237,v237,v57
v_mul_f32 v238,v238,v57
v_mfma_f32_16x16x32_bf16 a[24:27],v[84:87],a[72:75],a[24:27]
v_mul_f32 v239,v239,v57
v_cvt_pk_bf16_f32 v228,v228,v229
v_mfma_f32_16x16x32_bf16 a[24:27],v[88:91],a[80:83],a[24:27]
s_waitcnt vmcnt(5)
v_cmp_ne_u32 s[56:57],s52,v4
v_cmp_ne_u32 s[58:59],s53,v5
v_mfma_f32_16x16x32_bf16 a[24:27],v[92:95],a[88:91],a[24:27]
s_or_b64 s[56:57],s[56:57],s[58:59]
s_cbranch_scc1 L_fr6_%=
L_fo6_%=:
v_cvt_pk_bf16_f32 v229,v230,v231
v_mfma_f32_16x16x32_bf16 a[28:31],v[92:95],a[120:123],0
buffer_store_dwordx4 v[164:167],v6,s[40:43],s81 offen offset:1024 sc1
v_mfma_f32_16x16x32_bf16 a[28:31],v[88:91],a[112:115],a[28:31]
s_lshl_b32 s91,s63,8
ds_read_b64_tr_b16 v[88:89],v2 offset:20768
ds_read_b64_tr_b16 v[90:91],v2 offset:25888
v_mfma_f32_16x16x32_bf16 a[28:31],v[84:87],a[104:107],a[28:31]
buffer_load_dwordx4 a[128:131],v6,s[28:31],s87 offen sc1
v_mfma_f32_16x16x32_bf16 a[28:31],v[80:83],a[96:99],a[28:31]
ds_read_b64_tr_b16 v[80:81],v2 offset:288
ds_read_b64_tr_b16 v[82:83],v2 offset:5408
v_mfma_f32_16x16x32_bf16 v[128:131],v[96:99],a[68:71],0
ds_read_b64_tr_b16 v[84:85],v2 offset:10528
v_cvt_pk_bf16_f32 v230,v236,v237
v_mfma_f32_16x16x32_bf16 v[128:131],v[100:103],a[76:79],v[128:131]
ds_read_b64_tr_b16 v[86:87],v2 offset:15648
v_cvt_pk_bf16_f32 v231,v238,v239
v_mfma_f32_16x16x32_bf16 v[128:131],v[104:107],a[84:87],v[128:131]
ds_read_b64_tr_b16 v[92:93],v2 offset:31008
v_mul_f32 v244,v244,v57
v_mfma_f32_16x16x32_bf16 v[128:131],v[108:111],a[92:95],v[128:131]
ds_read_b64_tr_b16 v[94:95],v2 offset:36128
v_add_u32 v2,0xffff6000,v2
v_mfma_f32_16x16x32_bf16 v[136:139],v[108:111],a[124:127],0
v_mul_f32 v245,v245,v57
v_mul_f32 v246,v246,v57
v_mfma_f32_16x16x32_bf16 v[136:139],v[104:107],a[116:119],v[136:139]
v_mul_f32 v247,v247,v57
v_mul_f32 v252,v252,v57
v_mfma_f32_16x16x32_bf16 v[136:139],v[100:103],a[108:111],v[136:139]
v_mul_f32 v253,v253,v57
v_mul_f32 v254,v254,v57
v_mfma_f32_16x16x32_bf16 v[136:139],v[96:99],a[100:103],v[136:139]
v_mul_f32 v255,v255,v57
v_cvt_pk_bf16_f32 v244,v244,v245
v_mfma_f32_16x16x32_bf16 v[144:147],v[96:99],a[64:67],0
v_cvt_pk_bf16_f32 v245,v246,v247
v_cvt_pk_bf16_f32 v246,v252,v253
v_mfma_f32_16x16x32_bf16 v[144:147],v[100:103],a[72:75],v[144:147]
buffer_load_dwordx4 a[144:147],v6,s[28:31],s88 offen sc1
v_mfma_f32_16x16x32_bf16 v[144:147],v[104:107],a[80:83],v[144:147]
v_cvt_pk_bf16_f32 v247,v254,v255
v_accvgpr_read_b32 v16,a40
v_mfma_f32_16x16x32_bf16 v[144:147],v[108:111],a[88:91],v[144:147]
buffer_store_dwordx4 v[180:183],v6,s[40:43],s82 offen offset:1024 sc1
v_mfma_f32_16x16x32_bf16 v[152:155],v[108:111],a[120:123],0
ds_read_b64_tr_b16 v[108:109],v1 offset:30720
ds_read_b64_tr_b16 v[110:111],v1 offset:35840
v_mfma_f32_16x16x32_bf16 v[152:155],v[104:107],a[112:115],v[152:155]
ds_read_b64_tr_b16 v[104:105],v1 offset:20480
v_accvgpr_read_b32 v17,a41
v_mfma_f32_16x16x32_bf16 v[152:155],v[100:103],a[104:107],v[152:155]
ds_read_b64_tr_b16 v[100:101],v1 offset:10240
v_accvgpr_read_b32 v18,a42
v_mfma_f32_16x16x32_bf16 v[152:155],v[96:99],a[96:99],v[152:155]
ds_read_b64_tr_b16 v[96:97],v1
v_accvgpr_read_b32 v19,a43
v_mfma_f32_16x16x32_bf16 v[160:163],v[112:115],a[68:71],0
ds_read_b64_tr_b16 v[98:99],v1 offset:5120
ds_read_b64_tr_b16 v[102:103],v1 offset:15360
v_mfma_f32_16x16x32_bf16 v[160:163],v[116:119],a[76:79],v[160:163]
ds_read_b64_tr_b16 v[106:107],v1 offset:25600
v_accvgpr_read_b32 v20,a44
v_mfma_f32_16x16x32_bf16 v[160:163],v[120:123],a[84:87],v[160:163]
v_accvgpr_read_b32 v21,a45
v_accvgpr_read_b32 v22,a46
v_mfma_f32_16x16x32_bf16 v[160:163],v[124:127],a[92:95],v[160:163]
v_accvgpr_read_b32 v23,a47
v_mul_f32 v16,v16,v61
v_mfma_f32_16x16x32_bf16 v[168:171],v[124:127],a[124:127],0
v_mul_f32 v17,v17,v61
v_mul_f32 v18,v18,v61
v_mfma_f32_16x16x32_bf16 v[168:171],v[120:123],a[116:119],v[168:171]
v_mul_f32 v19,v19,v61
v_mul_f32 v20,v20,v61
v_mfma_f32_16x16x32_bf16 v[168:171],v[116:119],a[108:111],v[168:171]
buffer_load_dwordx4 a[160:163],v6,s[28:31],s89 offen sc1
v_mfma_f32_16x16x32_bf16 v[168:171],v[112:115],a[100:103],v[168:171]
v_mul_f32 v21,v21,v61
v_mul_f32 v22,v22,v61
v_mfma_f32_16x16x32_bf16 v[176:179],v[112:115],a[64:67],0
v_mul_f32 v23,v23,v61
v_cvt_pk_bf16_f32 v156,v16,v17
v_mfma_f32_16x16x32_bf16 v[176:179],v[116:119],a[72:75],v[176:179]
v_cvt_pk_bf16_f32 v157,v18,v19
v_cvt_pk_bf16_f32 v158,v20,v21
v_mfma_f32_16x16x32_bf16 v[176:179],v[120:123],a[80:83],v[176:179]
buffer_store_dwordx4 v[196:199],v6,s[40:43],s79 offen offset:2048 sc1
v_mfma_f32_16x16x32_bf16 v[176:179],v[124:127],a[88:91],v[176:179]
v_cvt_pk_bf16_f32 v159,v22,v23
v_accvgpr_read_b32 v16,a56
v_mfma_f32_16x16x32_bf16 v[184:187],v[124:127],a[120:123],0
v_accvgpr_read_b32 v17,a57
v_accvgpr_read_b32 v18,a58
v_mfma_f32_16x16x32_bf16 v[184:187],v[120:123],a[112:115],v[184:187]
ds_read_b64_tr_b16 v[120:121],v2 offset:20512
ds_read_b64_tr_b16 v[122:123],v2 offset:25632
v_mfma_f32_16x16x32_bf16 v[184:187],v[116:119],a[104:107],v[184:187]
ds_read_b64_tr_b16 v[116:117],v2 offset:10272
v_accvgpr_read_b32 v19,a59
v_mfma_f32_16x16x32_bf16 v[184:187],v[112:115],a[96:99],v[184:187]
s_waitcnt lgkmcnt(3)
ds_read_b64_tr_b16 v[112:113],v2 offset:32
v_accvgpr_read_b32 v20,a60
v_mfma_f32_16x16x32_bf16 v[192:195],v[64:67],a[68:71],0
ds_read_b64_tr_b16 v[114:115],v2 offset:5152
v_accvgpr_read_b32 v21,a61
v_mfma_f32_16x16x32_bf16 v[192:195],v[68:71],a[76:79],v[192:195]
ds_read_b64_tr_b16 v[118:119],v2 offset:15392
v_accvgpr_read_b32 v22,a62
v_mfma_f32_16x16x32_bf16 v[192:195],v[72:75],a[84:87],v[192:195]
v_accvgpr_read_b32 v23,a63
v_mul_f32 v16,v16,v61
v_mfma_f32_16x16x32_bf16 v[192:195],v[76:79],a[92:95],v[192:195]
buffer_load_dwordx4 a[176:179],v6,s[28:31],s90 offen sc1
v_mfma_f32_16x16x32_bf16 v[200:203],v[76:79],a[124:127],0
v_mul_f32 v17,v17,v61
v_mul_f32 v18,v18,v61
v_mfma_f32_16x16x32_bf16 v[200:203],v[72:75],a[116:119],v[200:203]
v_mul_f32 v19,v19,v61
v_mul_f32 v20,v20,v61
v_mfma_f32_16x16x32_bf16 v[200:203],v[68:71],a[108:111],v[200:203]
v_mul_f32 v21,v21,v61
v_mul_f32 v22,v22,v61
v_mfma_f32_16x16x32_bf16 v[200:203],v[64:67],a[100:103],v[200:203]
v_mul_f32 v23,v23,v61
v_cvt_pk_bf16_f32 v188,v16,v17
v_mfma_f32_16x16x32_bf16 v[208:211],v[64:67],a[64:67],0
v_cvt_pk_bf16_f32 v189,v18,v19
v_cvt_pk_bf16_f32 v190,v20,v21
v_mfma_f32_16x16x32_bf16 v[208:211],v[68:71],a[72:75],v[208:211]
buffer_store_dwordx4 v[212:215],v6,s[40:43],s80 offen offset:2048 sc1
v_mfma_f32_16x16x32_bf16 v[208:211],v[72:75],a[80:83],v[208:211]
ds_read_b64_tr_b16 v[124:125],v2 offset:30752
v_cvt_pk_bf16_f32 v191,v22,v23
v_mfma_f32_16x16x32_bf16 v[208:211],v[76:79],a[88:91],v[208:211]
)A"
R"A(ds_read_b64_tr_b16 v[126:127],v2 offset:35872
v_accvgpr_read_b32 v8,a32
v_mfma_f32_16x16x32_bf16 v[216:219],v[76:79],a[120:123],0
v_accvgpr_read_b32 v9,a33
v_accvgpr_read_b32 v10,a34
v_mfma_f32_16x16x32_bf16 v[216:219],v[72:75],a[112:115],v[216:219]
v_accvgpr_read_b32 v11,a35
v_accvgpr_read_b32 v12,a36
v_mfma_f32_16x16x32_bf16 v[216:219],v[68:71],a[104:107],v[216:219]
v_accvgpr_read_b32 v13,a37
v_accvgpr_read_b32 v14,a38
v_mfma_f32_16x16x32_bf16 v[216:219],v[64:67],a[96:99],v[216:219]
v_accvgpr_read_b32 v15,a39
v_mul_f32 v8,v8,v61
v_mfma_f32_16x16x32_bf16 v[224:227],v[80:83],a[68:71],0
buffer_load_dwordx4 a[180:183],v6,s[28:31],s90 offen offset:1024 sc1
v_mfma_f32_16x16x32_bf16 v[224:227],v[84:87],a[76:79],v[224:227]
v_mul_f32 v9,v9,v61
v_mul_f32 v10,v10,v61
v_mfma_f32_16x16x32_bf16 v[224:227],v[88:91],a[84:87],v[224:227]
v_mul_f32 v11,v11,v61
v_mul_f32 v12,v12,v61
v_mfma_f32_16x16x32_bf16 v[224:227],v[92:95],a[92:95],v[224:227]
v_mul_f32 v13,v13,v61
v_mul_f32 v14,v14,v61
v_mfma_f32_16x16x32_bf16 v[232:235],v[92:95],a[124:127],0
v_mul_f32 v15,v15,v61
v_cvt_pk_bf16_f32 v140,v8,v9
v_mfma_f32_16x16x32_bf16 v[232:235],v[88:91],a[116:119],v[232:235]
v_cvt_pk_bf16_f32 v141,v10,v11
v_cvt_pk_bf16_f32 v142,v12,v13
v_mfma_f32_16x16x32_bf16 v[232:235],v[84:87],a[108:111],v[232:235]
v_cvt_pk_bf16_f32 v143,v14,v15
v_accvgpr_read_b32 v8,a48
v_mfma_f32_16x16x32_bf16 v[232:235],v[80:83],a[100:103],v[232:235]
v_accvgpr_read_b32 v9,a49
v_accvgpr_read_b32 v10,a50
v_mfma_f32_16x16x32_bf16 v[240:243],v[80:83],a[64:67],0
buffer_store_dwordx4 v[228:231],v6,s[40:43],s81 offen offset:2048 sc1
v_mfma_f32_16x16x32_bf16 v[240:243],v[84:87],a[72:75],v[240:243]
v_accvgpr_read_b32 v11,a51
v_accvgpr_read_b32 v12,a52
v_mfma_f32_16x16x32_bf16 v[240:243],v[88:91],a[80:83],v[240:243]
v_accvgpr_read_b32 v13,a53
v_accvgpr_read_b32 v14,a54
v_mfma_f32_16x16x32_bf16 v[240:243],v[92:95],a[88:91],v[240:243]
v_accvgpr_read_b32 v15,a55
v_mul_f32 v8,v8,v61
v_mfma_f32_16x16x32_bf16 v[248:251],v[92:95],a[120:123],0
v_mul_f32 v9,v9,v61
v_mul_f32 v10,v10,v61
v_mfma_f32_16x16x32_bf16 v[248:251],v[88:91],a[112:115],v[248:251]
buffer_load_dwordx4 a[164:167],v6,s[28:31],s89 offen offset:1024 sc1
v_mfma_f32_16x16x32_bf16 v[248:251],v[84:87],a[104:107],v[248:251]
v_mul_f32 v11,v11,v61
v_mul_f32 v12,v12,v61
v_mfma_f32_16x16x32_bf16 v[248:251],v[80:83],a[96:99],v[248:251]
v_mul_f32 v13,v13,v61
v_mul_f32 v14,v14,v61
v_mfma_f32_16x16x32_bf16 a[192:195],v[96:99],v[32:35],a[192:195]
v_mul_f32 v15,v15,v61
v_cvt_pk_bf16_f32 v172,v8,v9
v_mfma_f32_16x16x32_bf16 a[192:195],v[100:103],v[36:39],a[192:195]
v_cvt_pk_bf16_f32 v173,v10,v11
v_cvt_pk_bf16_f32 v174,v12,v13
v_mfma_f32_16x16x32_bf16 a[192:195],v[104:107],v[40:43],a[192:195]
ds_read_b64_tr_b16 v[64:65],v1 offset:64
v_cvt_pk_bf16_f32 v175,v14,v15
v_mfma_f32_16x16x32_bf16 a[192:195],v[108:111],v[44:47],a[192:195]
ds_read_b64_tr_b16 v[66:67],v1 offset:5184
ds_read_b64_tr_b16 v[68:69],v1 offset:10304
v_mfma_f32_16x16x32_bf16 a[196:199],v[108:111],v[180:183],a[196:199]
s_cmp_eq_u32 s60,3
s_cselect_b32 s69,0x40000000,0
ds_read_b64_tr_b16 v[70:71],v1 offset:15424
v_mfma_f32_16x16x32_bf16 a[196:199],v[104:107],v[164:167],a[196:199]
ds_read_b64_tr_b16 v[72:73],v1 offset:20544
v_add_u32 v28,s69,v6
v_mfma_f32_16x16x32_bf16 a[196:199],v[100:103],v[148:151],a[196:199]
s_and_b32 s69,s63,s60
ds_read_b64_tr_b16 v[74:75],v1 offset:25664
v_mov_b32 v8,s52
v_mfma_f32_16x16x32_bf16 a[196:199],v[96:99],v[132:135],a[196:199]
buffer_store_dwordx4 v[244:247],v6,s[40:43],s82 offen offset:2048 sc1
v_mfma_f32_16x16x32_bf16 a[200:203],v[96:99],v[196:199],a[200:203]
ds_read_b64_tr_b16 v[76:77],v1 offset:30784
ds_read_b64_tr_b16 v[78:79],v1 offset:35904
v_mfma_f32_16x16x32_bf16 a[200:203],v[100:103],v[212:215],a[200:203]
ds_read_b64_tr_b16 v[80:81],v2 offset:96
v_mov_b32 v9,s53
v_mfma_f32_16x16x32_bf16 a[200:203],v[104:107],v[228:231],a[200:203]
buffer_load_dwordx4 a[148:151],v6,s[28:31],s88 offen offset:1024 sc1
v_mfma_f32_16x16x32_bf16 a[200:203],v[108:111],v[244:247],a[200:203]
ds_read_b64_tr_b16 v[82:83],v2 offset:5216
ds_read_b64_tr_b16 v[84:85],v2 offset:10336
v_mfma_f32_16x16x32_bf16 a[204:207],v[108:111],v[188:191],a[204:207]
ds_read_b64_tr_b16 v[86:87],v2 offset:15456
v_lshlrev_b32 v10,20,v6
v_mfma_f32_16x16x32_bf16 a[204:207],v[104:107],v[172:175],a[204:207]
ds_read_b64_tr_b16 v[88:89],v2 offset:20576
v_bfe_u32 v3,v6,4,2
v_mfma_f32_16x16x32_bf16 a[204:207],v[100:103],v[156:159],a[204:207]
ds_read_b64_tr_b16 v[90:91],v2 offset:25696
v_min_u32 v3,2,v3
v_mfma_f32_16x16x32_bf16 a[204:207],v[96:99],v[140:143],a[204:207]
s_waitcnt lgkmcnt(8)
ds_read_b64_tr_b16 v[92:93],v2 offset:30816
ds_read_b64_tr_b16 v[94:95],v2 offset:35936
v_mfma_f32_16x16x32_bf16 a[208:211],v[112:115],v[32:35],a[208:211]
ds_read_b64_tr_b16 v[96:97],v1 offset:128
v_add3_u32 v3,v3,s64,1
v_mfma_f32_16x16x32_bf16 a[208:211],v[116:119],v[36:39],a[208:211]
ds_read_b64_tr_b16 v[98:99],v1 offset:5248
v_and_b32 v3,3,v3
v_mfma_f32_16x16x32_bf16 a[208:211],v[120:123],v[40:43],a[208:211]
ds_read_b64_tr_b16 v[100:101],v1 offset:10368
v_lshlrev_b32 v3,6,v3
v_mfma_f32_16x16x32_bf16 a[208:211],v[124:127],v[44:47],a[208:211]
ds_read_b64_tr_b16 v[102:103],v1 offset:15488
ds_read_b64_tr_b16 v[104:105],v1 offset:20608
v_mfma_f32_16x16x32_bf16 a[212:215],v[124:127],v[180:183],a[212:215]
ds_read_b64_tr_b16 v[106:107],v1 offset:25728
v_bfe_u32 v56,v6,6,2
v_mfma_f32_16x16x32_bf16 a[212:215],v[120:123],v[164:167],a[212:215]
ds_read_b64_tr_b16 v[108:109],v1 offset:30848
v_lshrrev_b32 v57,8,v6
v_mfma_f32_16x16x32_bf16 a[212:215],v[116:119],v[148:151],a[212:215]
buffer_store_dwordx4 v[140:143],v28,s[40:43],s79 offen offset:3072 sc1
v_mfma_f32_16x16x32_bf16 a[212:215],v[112:115],v[132:135],a[212:215]
ds_read_b64_tr_b16 v[110:111],v1 offset:35968
v_cmp_eq_u32 s[56:57],v56,v57
L_pd20_%=:
s_cbranch_execz L_trt1_%=
v_mfma_f32_16x16x32_bf16 a[216:219],v[112:115],v[196:199],a[216:219]
buffer_load_dwordx4 a[132:135],v6,s[28:31],s87 offen offset:1024 sc1
v_mfma_f32_16x16x32_bf16 a[216:219],v[116:119],v[212:215],a[216:219]
v_bfe_u32 v56,v6,4,1
v_lshlrev_b32 v56,4,v56
v_mfma_f32_16x16x32_bf16 a[216:219],v[120:123],v[228:231],a[216:219]
v_mov_b32 v57,0x3f80
v_lshlrev_b32 v56,v56,v57
v_mfma_f32_16x16x32_bf16 a[216:219],v[124:127],v[244:247],a[216:219]
v_cndmask_b32 v56,0,v56,s[56:57]
v_bfe_u32 v57,v6,5,1
v_mfma_f32_16x16x32_bf16 a[220:223],v[124:127],v[188:191],a[220:223]
ds_read_b64_tr_b16 v[124:125],v2 offset:30880
ds_read_b64_tr_b16 v[126:127],v2 offset:36000
v_mfma_f32_16x16x32_bf16 a[220:223],v[120:123],v[172:175],a[220:223]
ds_read_b64_tr_b16 v[120:121],v2 offset:20640
v_cmp_eq_u32 s[56:57],0,v57
v_mfma_f32_16x16x32_bf16 a[220:223],v[116:119],v[156:159],a[220:223]
s_add_u32 s36,s12,s66
s_addc_u32 s37,s13,0
ds_read_b64_tr_b16 v[116:117],v2 offset:10400
v_mfma_f32_16x16x32_bf16 a[220:223],v[112:115],v[140:143],a[220:223]
s_and_b32 s37,s37,0xffff
ds_read_b64_tr_b16 v[112:113],v2 offset:160
v_cndmask_b32 v48,0,v56,s[56:57]
v_mfma_f32_16x16x32_bf16 a[224:227],v[64:67],v[32:35],a[224:227]
s_mul_i32 s66,s61,307200
ds_read_b64_tr_b16 v[114:115],v2 offset:5280
ds_read_b64_tr_b16 v[118:119],v2 offset:15520
v_mfma_f32_16x16x32_bf16 a[224:227],v[68:71],v[36:39],a[224:227]
ds_read_b64_tr_b16 v[122:123],v2 offset:25760
v_cndmask_b32 v49,v56,0,s[56:57]
v_mfma_f32_16x16x32_bf16 a[224:227],v[72:75],v[40:43],a[224:227]
s_waitcnt lgkmcnt(8)
v_mov_b32 v58,s52
v_mov_b32 v59,s53
v_mfma_f32_16x16x32_bf16 a[224:227],v[76:79],v[44:47],a[224:227]
s_add_u32 s24,s16,s66
s_addc_u32 s25,s17,0
v_lshlrev_b32 v60,20,v6
v_mfma_f32_16x16x32_bf16 a[228:231],v[76:79],v[180:183],a[228:231]
buffer_load_dwordx4 a[136:139],v6,s[28:31],s87 offen offset:2048 sc1
v_mfma_f32_16x16x32_bf16 a[228:231],v[72:75],v[164:167],a[228:231]
s_mul_i32 s66,s60,16384
s_add_u32 s66,s66,s67
s_add_u32 s79,s66,12288
v_mfma_f32_16x16x32_bf16 a[228:231],v[68:71],v[148:151],a[228:231]
buffer_store_dwordx4 v[156:159],v28,s[40:43],s80 offen offset:3072 sc1
v_mfma_f32_16x16x32_bf16 a[228:231],v[64:67],v[132:135],a[228:231]
s_add_u32 s80,s66,77824
v_bfe_u32 v61,v6,4,1
v_add3_u32 v61,v61,s63,2
v_mfma_f32_16x16x32_bf16 a[232:235],v[64:67],v[196:199],a[232:235]
s_and_b32 s25,s25,0xffff
v_and_b32 v61,3,v61
v_lshlrev_b32 v61,8,v61
v_mfma_f32_16x16x32_bf16 a[232:235],v[68:71],v[212:215],a[232:235]
s_add_u32 s67,s77,8192
v_mov_b32 v50,0
v_mov_b32 v51,0
v_mfma_f32_16x16x32_bf16 a[232:235],v[72:75],v[228:231],a[232:235]
v_mov_b32 v52,0
v_mov_b32 v53,0
v_mfma_f32_16x16x32_bf16 a[232:235],v[76:79],v[244:247],a[232:235]
v_mov_b32 v54,v48
v_mov_b32 v55,v49
v_mfma_f32_16x16x32_bf16 a[236:239],v[76:79],v[188:191],a[236:239]
ds_read_b64_tr_b16 v[76:77],v1 offset:30912
ds_read_b64_tr_b16 v[78:79],v1 offset:36032
v_mfma_f32_16x16x32_bf16 a[236:239],v[72:75],v[172:175],a[236:239]
ds_read_b64_tr_b16 v[72:73],v1 offset:20672
v_bfe_u32 v56,v6,4,4
v_mfma_f32_16x16x32_bf16 a[236:239],v[68:71],v[156:159],a[236:239]
ds_read_b64_tr_b16 v[68:69],v1 offset:10432
v_mul_u32_u24 v56,1280,v56
v_mfma_f32_16x16x32_bf16 a[236:239],v[64:67],v[140:143],a[236:239]
ds_read_b64_tr_b16 v[64:65],v1 offset:192
v_bfe_u32 v57,v6,8,1
v_mfma_f32_16x16x32_bf16 a[240:243],v[80:83],v[32:35],a[240:243]
ds_read_b64_tr_b16 v[66:67],v1 offset:5312
ds_read_b64_tr_b16 v[70:71],v1 offset:15552
v_mfma_f32_16x16x32_bf16 a[240:243],v[84:87],v[36:39],a[240:243]
buffer_load_dwordx4 a[152:155],v6,s[28:31],s88 offen offset:2048 sc1
v_mfma_f32_16x16x32_bf16 a[240:243],v[88:91],v[40:43],a[240:243]
ds_read_b64_tr_b16 v[74:75],v1 offset:25792
v_lshl_add_u32 v56,v57,5,v56
v_mfma_f32_16x16x32_bf16 a[240:243],v[92:95],v[44:47],a[240:243]
v_lshrrev_b32 v57,9,v6
v_lshl_add_u32 v56,v57,4,v56
v_mfma_f32_16x16x32_bf16 a[244:247],v[92:95],v[180:183],a[244:247]
s_cmp_eq_u32 s69,3
s_cselect_b32 s69,0x40000000,0
s_add_u32 s68,s68,s69
v_mfma_f32_16x16x32_bf16 a[244:247],v[88:91],v[164:167],a[244:247]
buffer_store_dwordx4 v[172:175],v28,s[40:43],s81 offen offset:3072 sc1
v_mfma_f32_16x16x32_bf16 a[244:247],v[84:87],v[148:151],a[244:247]
s_add_u32 s81,s66,143360
v_mfma_f32_16x16x32_bf16 a[244:247],v[80:83],v[132:135],a[244:247]
v_mfma_f32_16x16x32_bf16 a[248:251],v[80:83],v[196:199],a[248:251]
v_mfma_f32_16x16x32_bf16 a[248:251],v[84:87],v[212:215],a[248:251]
v_mfma_f32_16x16x32_bf16 a[248:251],v[88:91],v[228:231],a[248:251]
v_mfma_f32_16x16x32_bf16 a[248:251],v[92:95],v[244:247],a[248:251]
v_mfma_f32_16x16x32_bf16 a[252:255],v[92:95],v[188:191],a[252:255]
ds_read_b64_tr_b16 v[92:93],v2 offset:30944
ds_read_b64_tr_b16 v[94:95],v2 offset:36064
v_mfma_f32_16x16x32_bf16 a[252:255],v[88:91],v[172:175],a[252:255]
ds_read_b64_tr_b16 v[88:89],v2 offset:20704
v_mfma_f32_16x16x32_bf16 a[252:255],v[84:87],v[156:159],a[252:255]
buffer_load_dwordx4 a[168:171],v6,s[28:31],s89 offen offset:2048 sc1
v_mfma_f32_16x16x32_bf16 a[252:255],v[80:83],v[140:143],a[252:255]
ds_read_b64_tr_b16 v[80:81],v2 offset:224
ds_read_b64_tr_b16 v[82:83],v2 offset:5344
v_mfma_f32_16x16x32_bf16 a[0:3],v[96:99],v[32:35],a[0:3]
ds_read_b64_tr_b16 v[84:85],v2 offset:10464
v_mfma_f32_16x16x32_bf16 a[0:3],v[100:103],v[36:39],a[0:3]
ds_read_b64_tr_b16 v[86:87],v2 offset:15584
v_mfma_f32_16x16x32_bf16 a[0:3],v[104:107],v[40:43],a[0:3]
ds_read_b64_tr_b16 v[90:91],v2 offset:25824
v_mfma_f32_16x16x32_bf16 a[0:3],v[108:111],v[44:47],a[0:3]
v_mfma_f32_16x16x32_bf16 a[4:7],v[108:111],v[180:183],a[4:7]
buffer_store_dwordx4 v[188:191],v28,s[40:43],s82 offen offset:3072 sc1
v_mfma_f32_16x16x32_bf16 a[4:7],v[104:107],v[164:167],a[4:7]
s_add_u32 s82,s66,208896
s_mul_i32 s66,s64,320
v_mfma_f32_16x16x32_bf16 a[4:7],v[100:103],v[148:151],a[4:7]
v_mfma_f32_16x16x32_bf16 a[4:7],v[96:99],v[132:135],a[4:7]
v_mfma_f32_16x16x32_bf16 a[8:11],v[96:99],v[196:199],a[8:11]
v_mfma_f32_16x16x32_bf16 a[8:11],v[100:103],v[212:215],a[8:11]
v_mfma_f32_16x16x32_bf16 a[8:11],v[104:107],v[228:231],a[8:11]
v_mfma_f32_16x16x32_bf16 a[8:11],v[108:111],v[244:247],a[8:11]
buffer_load_dwordx4 a[184:187],v6,s[28:31],s90 offen offset:2048 sc1
v_mfma_f32_16x16x32_bf16 a[12:15],v[108:111],v[188:191],a[12:15]
)A"
R"A(ds_read_b64_tr_b16 v[108:109],v1 offset:30976
ds_read_b64_tr_b16 v[110:111],v1 offset:36096
v_mfma_f32_16x16x32_bf16 a[12:15],v[104:107],v[172:175],a[12:15]
ds_read_b64_tr_b16 v[104:105],v1 offset:20736
v_mfma_f32_16x16x32_bf16 a[12:15],v[100:103],v[156:159],a[12:15]
ds_read_b64_tr_b16 v[100:101],v1 offset:10496
v_mfma_f32_16x16x32_bf16 a[12:15],v[96:99],v[140:143],a[12:15]
s_waitcnt lgkmcnt(4)
ds_read_b64_tr_b16 v[96:97],v1 offset:256
v_mfma_f32_16x16x32_bf16 a[16:19],v[112:115],v[32:35],a[16:19]
ds_read_b64_tr_b16 v[98:99],v1 offset:5376
ds_read_b64_tr_b16 v[102:103],v1 offset:15616
v_mfma_f32_16x16x32_bf16 a[16:19],v[116:119],v[36:39],a[16:19]
ds_read_b64_tr_b16 v[106:107],v1 offset:25856
v_add_u32 v1,0xffff6000,v1
v_mfma_f32_16x16x32_bf16 a[16:19],v[120:123],v[40:43],a[16:19]
v_mfma_f32_16x16x32_bf16 a[16:19],v[124:127],v[44:47],a[16:19]
v_mfma_f32_16x16x32_bf16 a[20:23],v[124:127],v[180:183],a[20:23]
v_mfma_f32_16x16x32_bf16 a[20:23],v[120:123],v[164:167],a[20:23]
v_mfma_f32_16x16x32_bf16 a[20:23],v[116:119],v[148:151],a[20:23]
v_mfma_f32_16x16x32_bf16 a[20:23],v[112:115],v[132:135],a[20:23]
v_mfma_f32_16x16x32_bf16 a[24:27],v[112:115],v[196:199],a[24:27]
buffer_load_dwordx4 a[188:191],v28,s[28:31],s90 offen offset:3072 sc1
v_mfma_f32_16x16x32_bf16 a[24:27],v[116:119],v[212:215],a[24:27]
v_mfma_f32_16x16x32_bf16 a[24:27],v[120:123],v[228:231],a[24:27]
v_mfma_f32_16x16x32_bf16 a[24:27],v[124:127],v[244:247],a[24:27]
v_mfma_f32_16x16x32_bf16 a[28:31],v[124:127],v[188:191],a[28:31]
ds_read_b64_tr_b16 v[124:125],v2 offset:31008
ds_read_b64_tr_b16 v[126:127],v2 offset:36128
v_mfma_f32_16x16x32_bf16 a[28:31],v[120:123],v[172:175],a[28:31]
ds_read_b64_tr_b16 v[120:121],v2 offset:20768
v_mfma_f32_16x16x32_bf16 a[28:31],v[116:119],v[156:159],a[28:31]
ds_read_b64_tr_b16 v[116:117],v2 offset:10528
v_mfma_f32_16x16x32_bf16 a[28:31],v[112:115],v[140:143],a[28:31]
ds_read_b64_tr_b16 v[112:113],v2 offset:288
v_mfma_f32_16x16x32_bf16 v[128:131],v[64:67],v[32:35],v[128:131]
ds_read_b64_tr_b16 v[114:115],v2 offset:5408
ds_read_b64_tr_b16 v[118:119],v2 offset:15648
v_mfma_f32_16x16x32_bf16 v[128:131],v[68:71],v[36:39],v[128:131]
ds_read_b64_tr_b16 v[122:123],v2 offset:25888
v_add_u32 v2,0xffff6000,v2
v_mfma_f32_16x16x32_bf16 v[128:131],v[72:75],v[40:43],v[128:131]
v_mfma_f32_16x16x32_bf16 v[128:131],v[76:79],v[44:47],v[128:131]
v_mfma_f32_16x16x32_bf16 v[136:139],v[76:79],v[180:183],v[136:139]
v_mfma_f32_16x16x32_bf16 v[136:139],v[72:75],v[164:167],v[136:139]
buffer_load_dwordx4 a[172:175],v28,s[28:31],s89 offen offset:3072 sc1
v_mfma_f32_16x16x32_bf16 v[136:139],v[68:71],v[148:151],v[136:139]
v_mfma_f32_16x16x32_bf16 v[136:139],v[64:67],v[132:135],v[136:139]
v_mfma_f32_16x16x32_bf16 v[144:147],v[64:67],v[196:199],v[144:147]
v_mfma_f32_16x16x32_bf16 v[144:147],v[68:71],v[212:215],v[144:147]
v_mfma_f32_16x16x32_bf16 v[144:147],v[72:75],v[228:231],v[144:147]
v_mfma_f32_16x16x32_bf16 v[144:147],v[76:79],v[244:247],v[144:147]
v_mfma_f32_16x16x32_bf16 v[152:155],v[76:79],v[188:191],v[152:155]
v_mfma_f32_16x16x32_bf16 v[152:155],v[72:75],v[172:175],v[152:155]
v_mfma_f32_16x16x32_bf16 v[152:155],v[68:71],v[156:159],v[152:155]
v_mfma_f32_16x16x32_bf16 v[152:155],v[64:67],v[140:143],v[152:155]
s_mov_b64 exec,0
s_cmp_eq_u32 s60,0
s_cbranch_scc1 L_pd21_%=
s_cmp_eq_u32 s60,1
s_cbranch_scc1 L_pd22_%=
s_cmp_eq_u32 s60,2
s_cbranch_scc1 L_pd23_%=
s_branch L_trtg2S_%=
L_trtg2S_%=:
s_mov_b64 exec,-1
s_waitcnt lgkmcnt(0)
s_barrier
v_mfma_f32_16x16x32_bf16 v[160:163],v[80:83],v[32:35],v[160:163]
buffer_load_dwordx4 v24,s[48:51],s83 offen lds
v_mfma_f32_16x16x32_bf16 v[160:163],v[84:87],v[36:39],v[160:163]
ds_read_b64_tr_b16 v[64:65],v1
ds_read_b64_tr_b16 v[66:67],v1 offset:5120
v_mfma_f32_16x16x32_bf16 v[160:163],v[88:91],v[40:43],v[160:163]
ds_read_b64_tr_b16 v[68:69],v1 offset:10240
v_mfma_f32_16x16x32_bf16 v[160:163],v[92:95],v[44:47],v[160:163]
ds_read_b64_tr_b16 v[70:71],v1 offset:15360
v_mfma_f32_16x16x32_bf16 v[168:171],v[92:95],v[180:183],v[168:171]
ds_read_b64_tr_b16 v[72:73],v1 offset:20480
v_mfma_f32_16x16x32_bf16 v[168:171],v[88:91],v[164:167],v[168:171]
buffer_load_dwordx4 a[156:159],v28,s[28:31],s88 offen offset:3072 sc1
v_mfma_f32_16x16x32_bf16 v[168:171],v[84:87],v[148:151],v[168:171]
ds_read_b64_tr_b16 v[74:75],v1 offset:25600
ds_read_b64_tr_b16 v[76:77],v1 offset:30720
v_mfma_f32_16x16x32_bf16 v[168:171],v[80:83],v[132:135],v[168:171]
ds_read_b64_tr_b16 v[78:79],v1 offset:35840
v_mfma_f32_16x16x32_bf16 v[176:179],v[80:83],v[196:199],v[176:179]
v_mfma_f32_16x16x32_bf16 v[176:179],v[84:87],v[212:215],v[176:179]
v_mfma_f32_16x16x32_bf16 v[176:179],v[88:91],v[228:231],v[176:179]
v_mfma_f32_16x16x32_bf16 v[176:179],v[92:95],v[244:247],v[176:179]
buffer_load_dwordx4 v25,s[48:51],s83 offen offset:1024 lds
v_mfma_f32_16x16x32_bf16 v[184:187],v[92:95],v[188:191],v[184:187]
ds_read_b64_tr_b16 v[92:93],v2 offset:30752
ds_read_b64_tr_b16 v[94:95],v2 offset:35872
v_mfma_f32_16x16x32_bf16 v[184:187],v[88:91],v[172:175],v[184:187]
ds_read_b64_tr_b16 v[88:89],v2 offset:20512
v_mfma_f32_16x16x32_bf16 v[184:187],v[84:87],v[156:159],v[184:187]
ds_read_b64_tr_b16 v[84:85],v2 offset:10272
v_mfma_f32_16x16x32_bf16 v[184:187],v[80:83],v[140:143],v[184:187]
ds_read_b64_tr_b16 v[80:81],v2 offset:32
v_mfma_f32_16x16x32_bf16 v[192:195],v[96:99],v[32:35],v[192:195]
buffer_load_dwordx4 a[140:143],v28,s[28:31],s87 offen offset:3072 sc1
v_mfma_f32_16x16x32_bf16 v[192:195],v[100:103],v[36:39],v[192:195]
s_add_u32 s87,s64,2
s_and_b32 s87,s87,3
s_lshl_b32 s87,s87,1
v_mfma_f32_16x16x32_bf16 v[192:195],v[104:107],v[40:43],v[192:195]
s_lshl_b32 s87,s87,16
s_add_u32 s88,s87,4096
s_add_u32 s89,s87,8192
v_mfma_f32_16x16x32_bf16 v[192:195],v[108:111],v[44:47],v[192:195]
s_add_u32 s90,s87,12288
ds_read_b64_tr_b16 v[82:83],v2 offset:5152
ds_read_b64_tr_b16 v[86:87],v2 offset:15392
v_mfma_f32_16x16x32_bf16 v[200:203],v[108:111],v[180:183],v[200:203]
ds_read_b64_tr_b16 v[90:91],v2 offset:25632
v_mfma_f32_16x16x32_bf16 v[200:203],v[104:107],v[164:167],v[200:203]
buffer_load_dwordx4 a[68:71],v6,s[28:31],s87 offen sc1
v_mfma_f32_16x16x32_bf16 v[200:203],v[100:103],v[148:151],v[200:203]
v_mfma_f32_16x16x32_bf16 v[200:203],v[96:99],v[132:135],v[200:203]
v_mfma_f32_16x16x32_bf16 v[208:211],v[96:99],v[196:199],v[208:211]
v_mfma_f32_16x16x32_bf16 v[208:211],v[100:103],v[212:215],v[208:211]
v_mfma_f32_16x16x32_bf16 v[208:211],v[104:107],v[228:231],v[208:211]
buffer_load_dwordx4 a[76:79],v6,s[28:31],s88 offen sc1
v_mfma_f32_16x16x32_bf16 v[208:211],v[108:111],v[244:247],v[208:211]
v_mfma_f32_16x16x32_bf16 v[216:219],v[108:111],v[188:191],v[216:219]
ds_read_b64_tr_b16 v[108:109],v1 offset:30784
ds_read_b64_tr_b16 v[110:111],v1 offset:35904
v_mfma_f32_16x16x32_bf16 v[216:219],v[104:107],v[172:175],v[216:219]
ds_read_b64_tr_b16 v[104:105],v1 offset:20544
v_mfma_f32_16x16x32_bf16 v[216:219],v[100:103],v[156:159],v[216:219]
ds_read_b64_tr_b16 v[100:101],v1 offset:10304
v_mfma_f32_16x16x32_bf16 v[216:219],v[96:99],v[140:143],v[216:219]
buffer_load_dwordx4 a[84:87],v6,s[28:31],s89 offen sc1
v_mfma_f32_16x16x32_bf16 v[224:227],v[112:115],v[32:35],v[224:227]
ds_read_b64_tr_b16 v[96:97],v1 offset:64
ds_read_b64_tr_b16 v[98:99],v1 offset:5184
v_mfma_f32_16x16x32_bf16 v[224:227],v[116:119],v[36:39],v[224:227]
ds_read_b64_tr_b16 v[102:103],v1 offset:15424
v_mfma_f32_16x16x32_bf16 v[224:227],v[120:123],v[40:43],v[224:227]
ds_read_b64_tr_b16 v[106:107],v1 offset:25664
v_mfma_f32_16x16x32_bf16 v[224:227],v[124:127],v[44:47],v[224:227]
v_mfma_f32_16x16x32_bf16 v[232:235],v[124:127],v[180:183],v[232:235]
buffer_load_dwordx4 a[92:95],v6,s[28:31],s90 offen sc1
v_mfma_f32_16x16x32_bf16 v[232:235],v[120:123],v[164:167],v[232:235]
v_mfma_f32_16x16x32_bf16 v[232:235],v[116:119],v[148:151],v[232:235]
v_mfma_f32_16x16x32_bf16 v[232:235],v[112:115],v[132:135],v[232:235]
v_mfma_f32_16x16x32_bf16 v[240:243],v[112:115],v[196:199],v[240:243]
v_mfma_f32_16x16x32_bf16 v[240:243],v[116:119],v[212:215],v[240:243]
buffer_load_dwordx4 a[124:127],v6,s[28:31],s90 offen offset:1024 sc1
v_mfma_f32_16x16x32_bf16 v[240:243],v[120:123],v[228:231],v[240:243]
v_mfma_f32_16x16x32_bf16 v[240:243],v[124:127],v[244:247],v[240:243]
v_mfma_f32_16x16x32_bf16 v[248:251],v[124:127],v[188:191],v[248:251]
ds_read_b64_tr_b16 v[124:125],v2 offset:30816
ds_read_b64_tr_b16 v[126:127],v2 offset:35936
v_mfma_f32_16x16x32_bf16 v[248:251],v[120:123],v[172:175],v[248:251]
ds_read_b64_tr_b16 v[120:121],v2 offset:20576
v_mfma_f32_16x16x32_bf16 v[248:251],v[116:119],v[156:159],v[248:251]
buffer_load_dwordx4 a[116:119],v6,s[28:31],s89 offen offset:1024 sc1
v_mfma_f32_16x16x32_bf16 v[248:251],v[112:115],v[140:143],v[248:251]
s_waitcnt vmcnt(31) lgkmcnt(3)
ds_read_b64_tr_b16 v[112:113],v2 offset:96
ds_read_b64_tr_b16 v[114:115],v2 offset:5216
v_mfma_f32_16x16x32_bf16 a[192:195],v[64:67],a[128:131],a[192:195]
ds_read_b64_tr_b16 v[116:117],v2 offset:10336
v_mfma_f32_16x16x32_bf16 a[192:195],v[68:71],a[144:147],a[192:195]
s_waitcnt vmcnt(29)
ds_read_b64_tr_b16 v[118:119],v2 offset:15456
v_mfma_f32_16x16x32_bf16 a[192:195],v[72:75],a[160:163],a[192:195]
s_waitcnt vmcnt(25)
ds_read_b64_tr_b16 v[122:123],v2 offset:25696
v_mfma_f32_16x16x32_bf16 a[192:195],v[76:79],a[176:179],a[192:195]
buffer_load_dwordx4 a[108:111],v6,s[28:31],s88 offen offset:1024 sc1
v_mfma_f32_16x16x32_bf16 a[196:199],v[76:79],a[180:183],a[196:199]
s_waitcnt vmcnt(24)
v_mfma_f32_16x16x32_bf16 a[196:199],v[72:75],a[164:167],a[196:199]
s_waitcnt vmcnt(22)
v_mfma_f32_16x16x32_bf16 a[196:199],v[68:71],a[148:151],a[196:199]
s_waitcnt vmcnt(19)
v_mfma_f32_16x16x32_bf16 a[196:199],v[64:67],a[132:135],a[196:199]
s_waitcnt vmcnt(17)
v_mfma_f32_16x16x32_bf16 a[200:203],v[64:67],a[136:139],a[200:203]
buffer_load_dwordx4 a[100:103],v6,s[28:31],s87 offen offset:1024 sc1
v_mfma_f32_16x16x32_bf16 a[200:203],v[68:71],a[152:155],a[200:203]
s_waitcnt vmcnt(16)
v_mfma_f32_16x16x32_bf16 a[200:203],v[72:75],a[168:171],a[200:203]
s_waitcnt vmcnt(14)
v_mfma_f32_16x16x32_bf16 a[200:203],v[76:79],a[184:187],a[200:203]
s_waitcnt vmcnt(13)
v_mfma_f32_16x16x32_bf16 a[204:207],v[76:79],a[188:191],a[204:207]
s_waitcnt vmcnt(10)
ds_read_b64_tr_b16 v[76:77],v1 offset:30848
ds_read_b64_tr_b16 v[78:79],v1 offset:35968
v_mfma_f32_16x16x32_bf16 a[204:207],v[72:75],a[172:175],a[204:207]
buffer_load_dwordx4 a[64:67],v6,s[28:31],s87 offen offset:2048 sc1
v_mfma_f32_16x16x32_bf16 a[204:207],v[68:71],a[156:159],a[204:207]
s_waitcnt vmcnt(9)
ds_read_b64_tr_b16 v[68:69],v1 offset:10368
ds_read_b64_tr_b16 v[70:71],v1 offset:15488
v_mfma_f32_16x16x32_bf16 a[204:207],v[64:67],a[140:143],a[204:207]
ds_read_b64_tr_b16 v[64:65],v1 offset:128
v_mfma_f32_16x16x32_bf16 a[208:211],v[80:83],a[128:131],a[208:211]
ds_read_b64_tr_b16 v[66:67],v1 offset:5248
v_mfma_f32_16x16x32_bf16 a[208:211],v[84:87],a[144:147],a[208:211]
ds_read_b64_tr_b16 v[72:73],v1 offset:20608
v_mfma_f32_16x16x32_bf16 a[208:211],v[88:91],a[160:163],a[208:211]
ds_read_b64_tr_b16 v[74:75],v1 offset:25728
v_mfma_f32_16x16x32_bf16 a[208:211],v[92:95],a[176:179],a[208:211]
buffer_load_dwordx4 v26,s[48:51],s83 offen offset:2048 lds
v_mfma_f32_16x16x32_bf16 a[212:215],v[92:95],a[180:183],a[212:215]
v_mfma_f32_16x16x32_bf16 a[212:215],v[88:91],a[164:167],a[212:215]
v_mfma_f32_16x16x32_bf16 a[212:215],v[84:87],a[148:151],a[212:215]
v_mfma_f32_16x16x32_bf16 a[212:215],v[80:83],a[132:135],a[212:215]
v_mfma_f32_16x16x32_bf16 a[216:219],v[80:83],a[136:139],a[216:219]
buffer_load_dwordx4 a[72:75],v6,s[28:31],s88 offen offset:2048 sc1
v_mfma_f32_16x16x32_bf16 a[216:219],v[84:87],a[152:155],a[216:219]
v_mfma_f32_16x16x32_bf16 a[216:219],v[88:91],a[168:171],a[216:219]
v_mfma_f32_16x16x32_bf16 a[216:219],v[92:95],a[184:187],a[216:219]
v_mfma_f32_16x16x32_bf16 a[220:223],v[92:95],a[188:191],a[220:223]
ds_read_b64_tr_b16 v[92:93],v2 offset:30880
ds_read_b64_tr_b16 v[94:95],v2 offset:36000
v_mfma_f32_16x16x32_bf16 a[220:223],v[88:91],a[172:175],a[220:223]
ds_read_b64_tr_b16 v[88:89],v2 offset:20640
v_mfma_f32_16x16x32_bf16 a[220:223],v[84:87],a[156:159],a[220:223]
)A"
R"A(buffer_load_dwordx4 v27,s[48:51],s83 offen offset:3072 lds
v_mfma_f32_16x16x32_bf16 a[220:223],v[80:83],a[140:143],a[220:223]
s_add_u32 m0,s86,126976
ds_read_b64_tr_b16 v[80:81],v2 offset:160
ds_read_b64_tr_b16 v[82:83],v2 offset:5280
s_barrier
v_mfma_f32_16x16x32_bf16 a[224:227],v[96:99],a[128:131],a[224:227]
ds_read_b64_tr_b16 v[84:85],v2 offset:10400
v_mfma_f32_16x16x32_bf16 a[224:227],v[100:103],a[144:147],a[224:227]
ds_read_b64_tr_b16 v[86:87],v2 offset:15520
v_mfma_f32_16x16x32_bf16 a[224:227],v[104:107],a[160:163],a[224:227]
buffer_store_dwordx2 v[8:9],v10,s[44:47],s18 offen offset:1280 sc1
v_mfma_f32_16x16x32_bf16 a[224:227],v[108:111],a[176:179],a[224:227]
ds_read_b64_tr_b16 v[90:91],v2 offset:25760
v_mfma_f32_16x16x32_bf16 a[228:231],v[108:111],a[180:183],a[228:231]
v_mfma_f32_16x16x32_bf16 a[228:231],v[104:107],a[164:167],a[228:231]
v_mfma_f32_16x16x32_bf16 a[228:231],v[100:103],a[148:151],a[228:231]
v_mfma_f32_16x16x32_bf16 a[228:231],v[96:99],a[132:135],a[228:231]
buffer_load_dwordx4 a[80:83],v6,s[28:31],s89 offen offset:2048 sc1
v_mfma_f32_16x16x32_bf16 a[232:235],v[96:99],a[136:139],a[232:235]
v_mfma_f32_16x16x32_bf16 a[232:235],v[100:103],a[152:155],a[232:235]
v_mfma_f32_16x16x32_bf16 a[232:235],v[104:107],a[168:171],a[232:235]
v_mfma_f32_16x16x32_bf16 a[232:235],v[108:111],a[184:187],a[232:235]
v_mfma_f32_16x16x32_bf16 a[236:239],v[108:111],a[188:191],a[236:239]
s_waitcnt lgkmcnt(0)
ds_read_b64_tr_b16 v[108:109],v1 offset:30912
ds_read_b64_tr_b16 v[110:111],v1 offset:36032
v_mfma_f32_16x16x32_bf16 a[236:239],v[104:107],a[172:175],a[236:239]
buffer_load_dwordx4 v7,s[48:51],s83 offen lds
v_mfma_f32_16x16x32_bf16 a[236:239],v[100:103],a[156:159],a[236:239]
s_add_u32 m0,s86,128000
ds_read_b64_tr_b16 v[100:101],v1 offset:10432
ds_read_b64_tr_b16 v[102:103],v1 offset:15552
v_mfma_f32_16x16x32_bf16 a[236:239],v[96:99],a[140:143],a[236:239]
s_add_u32 s83,s64,1
s_and_b32 s83,s83,3
ds_read_b64_tr_b16 v[96:97],v1 offset:192
v_mfma_f32_16x16x32_bf16 a[240:243],v[112:115],a[128:131],a[240:243]
s_lshl_b32 s83,s83,1
s_or_b32 s83,s83,1
ds_read_b64_tr_b16 v[98:99],v1 offset:5312
v_mfma_f32_16x16x32_bf16 a[240:243],v[116:119],a[144:147],a[240:243]
s_mul_i32 s83,s83,163840
s_add_u32 s83,s83,s85
ds_read_b64_tr_b16 v[104:105],v1 offset:20672
v_mfma_f32_16x16x32_bf16 a[240:243],v[120:123],a[160:163],a[240:243]
buffer_load_dwordx4 a[88:91],v6,s[28:31],s90 offen offset:2048 sc1
v_mfma_f32_16x16x32_bf16 a[240:243],v[124:127],a[176:179],a[240:243]
ds_read_b64_tr_b16 v[106:107],v1 offset:25792
v_mfma_f32_16x16x32_bf16 a[244:247],v[124:127],a[180:183],a[244:247]
v_mfma_f32_16x16x32_bf16 a[244:247],v[120:123],a[164:167],a[244:247]
v_mfma_f32_16x16x32_bf16 a[244:247],v[116:119],a[148:151],a[244:247]
v_mfma_f32_16x16x32_bf16 a[244:247],v[112:115],a[132:135],a[244:247]
v_mfma_f32_16x16x32_bf16 a[248:251],v[112:115],a[136:139],a[248:251]
buffer_load_dwordx4 v24,s[48:51],s84 offen lds
v_mfma_f32_16x16x32_bf16 a[248:251],v[116:119],a[152:155],a[248:251]
v_mfma_f32_16x16x32_bf16 a[248:251],v[120:123],a[168:171],a[248:251]
v_mfma_f32_16x16x32_bf16 a[248:251],v[124:127],a[184:187],a[248:251]
v_mfma_f32_16x16x32_bf16 a[252:255],v[124:127],a[188:191],a[252:255]
ds_read_b64_tr_b16 v[124:125],v2 offset:30944
ds_read_b64_tr_b16 v[126:127],v2 offset:36064
v_mfma_f32_16x16x32_bf16 a[252:255],v[120:123],a[172:175],a[252:255]
buffer_load_dwordx4 a[120:123],v28,s[28:31],s90 offen offset:3072 sc1
v_mfma_f32_16x16x32_bf16 a[252:255],v[116:119],a[156:159],a[252:255]
ds_read_b64_tr_b16 v[116:117],v2 offset:10464
ds_read_b64_tr_b16 v[118:119],v2 offset:15584
v_mfma_f32_16x16x32_bf16 a[252:255],v[112:115],a[140:143],a[252:255]
ds_read_b64_tr_b16 v[112:113],v2 offset:224
v_mfma_f32_16x16x32_bf16 a[0:3],v[64:67],a[128:131],a[0:3]
ds_read_b64_tr_b16 v[114:115],v2 offset:5344
v_mfma_f32_16x16x32_bf16 a[0:3],v[68:71],a[144:147],a[0:3]
ds_read_b64_tr_b16 v[120:121],v2 offset:20704
v_mfma_f32_16x16x32_bf16 a[0:3],v[72:75],a[160:163],a[0:3]
ds_read_b64_tr_b16 v[122:123],v2 offset:25824
v_mfma_f32_16x16x32_bf16 a[0:3],v[76:79],a[176:179],a[0:3]
buffer_load_dwordx4 v25,s[48:51],s84 offen offset:1024 lds
v_mfma_f32_16x16x32_bf16 a[4:7],v[76:79],a[180:183],a[4:7]
v_mfma_f32_16x16x32_bf16 a[4:7],v[72:75],a[164:167],a[4:7]
v_mfma_f32_16x16x32_bf16 a[4:7],v[68:71],a[148:151],a[4:7]
v_mfma_f32_16x16x32_bf16 a[4:7],v[64:67],a[132:135],a[4:7]
v_mfma_f32_16x16x32_bf16 a[8:11],v[64:67],a[136:139],a[8:11]
buffer_load_dwordx4 a[112:115],v28,s[28:31],s89 offen offset:3072 sc1
v_mfma_f32_16x16x32_bf16 a[8:11],v[68:71],a[152:155],a[8:11]
v_mfma_f32_16x16x32_bf16 a[8:11],v[72:75],a[168:171],a[8:11]
v_mfma_f32_16x16x32_bf16 a[8:11],v[76:79],a[184:187],a[8:11]
v_mfma_f32_16x16x32_bf16 a[12:15],v[76:79],a[188:191],a[12:15]
ds_read_b64_tr_b16 v[76:77],v1 offset:30976
ds_read_b64_tr_b16 v[78:79],v1 offset:36096
v_mfma_f32_16x16x32_bf16 a[12:15],v[72:75],a[172:175],a[12:15]
buffer_load_dwordx4 a[104:107],v28,s[28:31],s88 offen offset:3072 sc1
v_mfma_f32_16x16x32_bf16 a[12:15],v[68:71],a[156:159],a[12:15]
ds_read_b64_tr_b16 v[68:69],v1 offset:10496
ds_read_b64_tr_b16 v[70:71],v1 offset:15616
v_mfma_f32_16x16x32_bf16 a[12:15],v[64:67],a[140:143],a[12:15]
ds_read_b64_tr_b16 v[64:65],v1 offset:256
v_mfma_f32_16x16x32_bf16 a[16:19],v[80:83],a[128:131],a[16:19]
ds_read_b64_tr_b16 v[66:67],v1 offset:5376
v_mfma_f32_16x16x32_bf16 a[16:19],v[84:87],a[144:147],a[16:19]
ds_read_b64_tr_b16 v[72:73],v1 offset:20736
v_mfma_f32_16x16x32_bf16 a[16:19],v[88:91],a[160:163],a[16:19]
ds_read_b64_tr_b16 v[74:75],v1 offset:25856
v_add_u32 v1,0xffff6000,v1
v_mfma_f32_16x16x32_bf16 a[16:19],v[92:95],a[176:179],a[16:19]
buffer_load_dwordx4 v26,s[48:51],s84 offen offset:2048 lds
v_mfma_f32_16x16x32_bf16 a[20:23],v[92:95],a[180:183],a[20:23]
v_mfma_f32_16x16x32_bf16 a[20:23],v[88:91],a[164:167],a[20:23]
v_mfma_f32_16x16x32_bf16 a[20:23],v[84:87],a[148:151],a[20:23]
v_mfma_f32_16x16x32_bf16 a[20:23],v[80:83],a[132:135],a[20:23]
v_mfma_f32_16x16x32_bf16 a[24:27],v[80:83],a[136:139],a[24:27]
buffer_load_dwordx4 a[96:99],v28,s[28:31],s87 offen offset:3072 sc1
v_mfma_f32_16x16x32_bf16 a[24:27],v[84:87],a[152:155],a[24:27]
s_add_u32 s87,s64,3
s_and_b32 s87,s87,3
s_lshl_b32 s87,s87,1
v_mfma_f32_16x16x32_bf16 a[24:27],v[88:91],a[168:171],a[24:27]
s_lshl_b32 s87,s87,16
s_add_u32 s88,s87,4096
s_add_u32 s89,s87,8192
v_mfma_f32_16x16x32_bf16 a[24:27],v[92:95],a[184:187],a[24:27]
s_add_u32 s90,s87,12288
v_mfma_f32_16x16x32_bf16 a[28:31],v[92:95],a[188:191],a[28:31]
ds_read_b64_tr_b16 v[92:93],v2 offset:31008
ds_read_b64_tr_b16 v[94:95],v2 offset:36128
v_mfma_f32_16x16x32_bf16 a[28:31],v[88:91],a[172:175],a[28:31]
s_waitcnt lgkmcnt(2)
ds_read_b64_tr_b16 v[88:89],v2 offset:20768
v_mfma_f32_16x16x32_bf16 a[28:31],v[84:87],a[156:159],a[28:31]
buffer_load_dwordx4 v27,s[48:51],s84 offen offset:3072 lds
v_mfma_f32_16x16x32_bf16 a[28:31],v[80:83],a[140:143],a[28:31]
s_add_u32 m0,s86,132096
ds_read_b64_tr_b16 v[80:81],v2 offset:288
ds_read_b64_tr_b16 v[82:83],v2 offset:5408
v_mfma_f32_16x16x32_bf16 v[128:131],v[96:99],a[128:131],v[128:131]
ds_read_b64_tr_b16 v[84:85],v2 offset:10528
v_mfma_f32_16x16x32_bf16 v[128:131],v[100:103],a[144:147],v[128:131]
ds_read_b64_tr_b16 v[86:87],v2 offset:15648
v_mfma_f32_16x16x32_bf16 v[128:131],v[104:107],a[160:163],v[128:131]
ds_read_b64_tr_b16 v[90:91],v2 offset:25888
v_add_u32 v2,0xffff6000,v2
v_mfma_f32_16x16x32_bf16 v[128:131],v[108:111],a[176:179],v[128:131]
v_mfma_f32_16x16x32_bf16 v[136:139],v[108:111],a[180:183],v[136:139]
buffer_load_dwordx4 v7,s[48:51],s84 offen lds
v_mfma_f32_16x16x32_bf16 v[136:139],v[104:107],a[164:167],v[136:139]
s_add_u32 m0,s86,81920
s_add_u32 s84,s83,20480
v_mfma_f32_16x16x32_bf16 v[136:139],v[100:103],a[148:151],v[136:139]
v_mfma_f32_16x16x32_bf16 v[136:139],v[96:99],a[132:135],v[136:139]
v_mfma_f32_16x16x32_bf16 v[144:147],v[96:99],a[136:139],v[144:147]
v_mfma_f32_16x16x32_bf16 v[144:147],v[100:103],a[152:155],v[144:147]
v_mfma_f32_16x16x32_bf16 v[144:147],v[104:107],a[168:171],v[144:147]
buffer_load_dwordx4 v24,s[48:51],s83 offen lds
v_mfma_f32_16x16x32_bf16 v[144:147],v[108:111],a[184:187],v[144:147]
v_mfma_f32_16x16x32_bf16 v[152:155],v[108:111],a[188:191],v[152:155]
ds_read_b64_tr_b16 v[108:109],v1 offset:30720
ds_read_b64_tr_b16 v[110:111],v1 offset:35840
v_mfma_f32_16x16x32_bf16 v[152:155],v[104:107],a[172:175],v[152:155]
ds_read_b64_tr_b16 v[104:105],v1 offset:20480
v_mfma_f32_16x16x32_bf16 v[152:155],v[100:103],a[156:159],v[152:155]
ds_read_b64_tr_b16 v[100:101],v1 offset:10240
v_mfma_f32_16x16x32_bf16 v[152:155],v[96:99],a[140:143],v[152:155]
ds_read_b64_tr_b16 v[96:97],v1
v_mfma_f32_16x16x32_bf16 v[160:163],v[112:115],a[128:131],v[160:163]
buffer_load_dwordx4 v25,s[48:51],s83 offen offset:1024 lds
v_mfma_f32_16x16x32_bf16 v[160:163],v[116:119],a[144:147],v[160:163]
ds_read_b64_tr_b16 v[98:99],v1 offset:5120
ds_read_b64_tr_b16 v[102:103],v1 offset:15360
v_mfma_f32_16x16x32_bf16 v[160:163],v[120:123],a[160:163],v[160:163]
ds_read_b64_tr_b16 v[106:107],v1 offset:25600
v_mfma_f32_16x16x32_bf16 v[160:163],v[124:127],a[176:179],v[160:163]
v_mfma_f32_16x16x32_bf16 v[168:171],v[124:127],a[180:183],v[168:171]
v_mfma_f32_16x16x32_bf16 v[168:171],v[120:123],a[164:167],v[168:171]
v_mfma_f32_16x16x32_bf16 v[168:171],v[116:119],a[148:151],v[168:171]
buffer_load_dwordx4 v26,s[48:51],s83 offen offset:2048 lds
v_mfma_f32_16x16x32_bf16 v[168:171],v[112:115],a[132:135],v[168:171]
v_mfma_f32_16x16x32_bf16 v[176:179],v[112:115],a[136:139],v[176:179]
v_mfma_f32_16x16x32_bf16 v[176:179],v[116:119],a[152:155],v[176:179]
v_mfma_f32_16x16x32_bf16 v[176:179],v[120:123],a[168:171],v[176:179]
v_mfma_f32_16x16x32_bf16 v[176:179],v[124:127],a[184:187],v[176:179]
v_mfma_f32_16x16x32_bf16 v[184:187],v[124:127],a[188:191],v[184:187]
buffer_load_dwordx4 v27,s[48:51],s83 offen offset:3072 lds
v_mfma_f32_16x16x32_bf16 v[184:187],v[120:123],a[172:175],v[184:187]
s_add_u32 m0,s86,86016
ds_read_b64_tr_b16 v[120:121],v2 offset:20512
ds_read_b64_tr_b16 v[122:123],v2 offset:25632
v_mfma_f32_16x16x32_bf16 v[184:187],v[116:119],a[156:159],v[184:187]
ds_read_b64_tr_b16 v[116:117],v2 offset:10272
v_mfma_f32_16x16x32_bf16 v[184:187],v[112:115],a[140:143],v[184:187]
ds_read_b64_tr_b16 v[112:113],v2 offset:32
L_pd21_%=:
s_cbranch_execz L_trtg2S_%=
v_mfma_f32_16x16x32_bf16 v[192:195],v[64:67],a[128:131],v[192:195]
ds_read_b64_tr_b16 v[114:115],v2 offset:5152
v_mfma_f32_16x16x32_bf16 v[192:195],v[68:71],a[144:147],v[192:195]
ds_read_b64_tr_b16 v[118:119],v2 offset:15392
ds_read_b64_tr_b16 v[124:125],v2 offset:30752
v_mfma_f32_16x16x32_bf16 v[192:195],v[72:75],a[160:163],v[192:195]
buffer_load_dwordx4 v7,s[48:51],s83 offen lds
v_mfma_f32_16x16x32_bf16 v[192:195],v[76:79],a[176:179],v[192:195]
s_add_u32 m0,s86,87040
s_add_u32 s83,s64,2
ds_read_b64_tr_b16 v[126:127],v2 offset:35872
v_mfma_f32_16x16x32_bf16 v[200:203],v[76:79],a[180:183],v[200:203]
s_and_b32 s83,s83,3
s_lshl_b32 s83,s83,1
s_or_b32 s83,s83,1
v_mfma_f32_16x16x32_bf16 v[200:203],v[72:75],a[164:167],v[200:203]
s_mul_i32 s83,s83,163840
s_add_u32 s83,s83,s85
v_mfma_f32_16x16x32_bf16 v[200:203],v[68:71],a[148:151],v[200:203]
v_mfma_f32_16x16x32_bf16 v[200:203],v[64:67],a[132:135],v[200:203]
v_mfma_f32_16x16x32_bf16 v[208:211],v[64:67],a[136:139],v[208:211]
buffer_load_dwordx4 v24,s[48:51],s84 offen lds
v_mfma_f32_16x16x32_bf16 v[208:211],v[68:71],a[152:155],v[208:211]
v_mfma_f32_16x16x32_bf16 v[208:211],v[72:75],a[168:171],v[208:211]
v_mfma_f32_16x16x32_bf16 v[208:211],v[76:79],a[184:187],v[208:211]
v_mfma_f32_16x16x32_bf16 v[216:219],v[76:79],a[188:191],v[216:219]
ds_read_b64_tr_b16 v[76:77],v1 offset:30784
ds_read_b64_tr_b16 v[78:79],v1 offset:35904
v_mfma_f32_16x16x32_bf16 v[216:219],v[72:75],a[172:175],v[216:219]
ds_read_b64_tr_b16 v[72:73],v1 offset:20544
v_mfma_f32_16x16x32_bf16 v[216:219],v[68:71],a[156:159],v[216:219]
buffer_load_dwordx2 v[4:5],v3,s[44:47],s91 offen offset:1280 sc1
v_mfma_f32_16x16x32_bf16 v[216:219],v[64:67],a[140:143],v[216:219]
s_waitcnt lgkmcnt(3)
ds_read_b64_tr_b16 v[64:65],v1 offset:64
)A"
R"A(ds_read_b64_tr_b16 v[66:67],v1 offset:5184
v_mfma_f32_16x16x32_bf16 v[224:227],v[80:83],a[128:131],v[224:227]
ds_read_b64_tr_b16 v[68:69],v1 offset:10304
v_mfma_f32_16x16x32_bf16 v[224:227],v[84:87],a[144:147],v[224:227]
ds_read_b64_tr_b16 v[70:71],v1 offset:15424
v_mfma_f32_16x16x32_bf16 v[224:227],v[88:91],a[160:163],v[224:227]
ds_read_b64_tr_b16 v[74:75],v1 offset:25664
v_mfma_f32_16x16x32_bf16 v[224:227],v[92:95],a[176:179],v[224:227]
v_mfma_f32_16x16x32_bf16 v[232:235],v[92:95],a[180:183],v[232:235]
buffer_load_dwordx4 v25,s[48:51],s84 offen offset:1024 lds
v_mfma_f32_16x16x32_bf16 v[232:235],v[88:91],a[164:167],v[232:235]
v_mfma_f32_16x16x32_bf16 v[232:235],v[84:87],a[148:151],v[232:235]
v_mfma_f32_16x16x32_bf16 v[232:235],v[80:83],a[132:135],v[232:235]
v_mfma_f32_16x16x32_bf16 v[240:243],v[80:83],a[136:139],v[240:243]
v_mfma_f32_16x16x32_bf16 v[240:243],v[84:87],a[152:155],v[240:243]
v_mfma_f32_16x16x32_bf16 v[240:243],v[88:91],a[168:171],v[240:243]
buffer_load_dwordx4 v26,s[48:51],s84 offen offset:2048 lds
v_mfma_f32_16x16x32_bf16 v[240:243],v[92:95],a[184:187],v[240:243]
v_mfma_f32_16x16x32_bf16 v[248:251],v[92:95],a[188:191],v[248:251]
ds_read_b64_tr_b16 v[92:93],v2 offset:30816
ds_read_b64_tr_b16 v[94:95],v2 offset:35936
v_mfma_f32_16x16x32_bf16 v[248:251],v[88:91],a[172:175],v[248:251]
ds_read_b64_tr_b16 v[88:89],v2 offset:20576
v_mfma_f32_16x16x32_bf16 v[248:251],v[84:87],a[156:159],v[248:251]
ds_read_b64_tr_b16 v[84:85],v2 offset:10336
v_mfma_f32_16x16x32_bf16 v[248:251],v[80:83],a[140:143],v[248:251]
s_waitcnt vmcnt(25)
ds_read_b64_tr_b16 v[80:81],v2 offset:96
v_mfma_f32_16x16x32_bf16 a[192:195],v[96:99],a[68:71],a[192:195]
buffer_load_dwordx4 v27,s[48:51],s84 offen offset:3072 lds
v_mfma_f32_16x16x32_bf16 a[192:195],v[100:103],a[76:79],a[192:195]
s_add_u32 m0,s86,91136
ds_read_b64_tr_b16 v[82:83],v2 offset:5216
ds_read_b64_tr_b16 v[86:87],v2 offset:15456
v_mfma_f32_16x16x32_bf16 a[192:195],v[104:107],a[84:87],a[192:195]
ds_read_b64_tr_b16 v[90:91],v2 offset:25696
v_mfma_f32_16x16x32_bf16 a[192:195],v[108:111],a[92:95],a[192:195]
v_mfma_f32_16x16x32_bf16 a[196:199],v[108:111],a[124:127],a[196:199]
v_mfma_f32_16x16x32_bf16 a[196:199],v[104:107],a[116:119],a[196:199]
v_mfma_f32_16x16x32_bf16 a[196:199],v[100:103],a[108:111],a[196:199]
buffer_load_dwordx4 v7,s[48:51],s84 offen lds
v_mfma_f32_16x16x32_bf16 a[196:199],v[96:99],a[100:103],a[196:199]
s_add_u32 m0,s86,40960
s_add_u32 s84,s83,20480
v_mfma_f32_16x16x32_bf16 a[200:203],v[96:99],a[64:67],a[200:203]
s_waitcnt vmcnt(25)
v_mfma_f32_16x16x32_bf16 a[200:203],v[100:103],a[72:75],a[200:203]
s_waitcnt vmcnt(22)
v_mfma_f32_16x16x32_bf16 a[200:203],v[104:107],a[80:83],a[200:203]
s_waitcnt vmcnt(18)
v_mfma_f32_16x16x32_bf16 a[200:203],v[108:111],a[88:91],a[200:203]
buffer_load_dwordx4 v[32:35],v6,s[28:31],s87 offen sc1
v_mfma_f32_16x16x32_bf16 a[204:207],v[108:111],a[120:123],a[204:207]
s_waitcnt vmcnt(17)
ds_read_b64_tr_b16 v[108:109],v1 offset:30848
ds_read_b64_tr_b16 v[110:111],v1 offset:35968
v_mfma_f32_16x16x32_bf16 a[204:207],v[104:107],a[112:115],a[204:207]
s_waitcnt vmcnt(16)
ds_read_b64_tr_b16 v[104:105],v1 offset:20608
v_mfma_f32_16x16x32_bf16 a[204:207],v[100:103],a[104:107],a[204:207]
s_waitcnt vmcnt(14)
ds_read_b64_tr_b16 v[100:101],v1 offset:10368
v_mfma_f32_16x16x32_bf16 a[204:207],v[96:99],a[96:99],a[204:207]
ds_read_b64_tr_b16 v[96:97],v1 offset:128
v_mfma_f32_16x16x32_bf16 a[208:211],v[112:115],a[68:71],a[208:211]
buffer_load_dwordx4 v[132:135],v6,s[28:31],s87 offen offset:1024 sc1
v_mfma_f32_16x16x32_bf16 a[208:211],v[116:119],a[76:79],a[208:211]
ds_read_b64_tr_b16 v[98:99],v1 offset:5248
ds_read_b64_tr_b16 v[102:103],v1 offset:15488
v_mfma_f32_16x16x32_bf16 a[208:211],v[120:123],a[84:87],a[208:211]
ds_read_b64_tr_b16 v[106:107],v1 offset:25728
v_mfma_f32_16x16x32_bf16 a[208:211],v[124:127],a[92:95],a[208:211]
v_mfma_f32_16x16x32_bf16 a[212:215],v[124:127],a[124:127],a[212:215]
v_mfma_f32_16x16x32_bf16 a[212:215],v[120:123],a[116:119],a[212:215]
buffer_load_dwordx4 v[196:199],v6,s[28:31],s87 offen offset:2048 sc1
v_mfma_f32_16x16x32_bf16 a[212:215],v[116:119],a[108:111],a[212:215]
v_mfma_f32_16x16x32_bf16 a[212:215],v[112:115],a[100:103],a[212:215]
v_mfma_f32_16x16x32_bf16 a[216:219],v[112:115],a[64:67],a[216:219]
v_mfma_f32_16x16x32_bf16 a[216:219],v[116:119],a[72:75],a[216:219]
v_mfma_f32_16x16x32_bf16 a[216:219],v[120:123],a[80:83],a[216:219]
buffer_load_dwordx4 v[36:39],v6,s[28:31],s88 offen sc1
v_mfma_f32_16x16x32_bf16 a[216:219],v[124:127],a[88:91],a[216:219]
v_mfma_f32_16x16x32_bf16 a[220:223],v[124:127],a[120:123],a[220:223]
ds_read_b64_tr_b16 v[124:125],v2 offset:30880
ds_read_b64_tr_b16 v[126:127],v2 offset:36000
v_mfma_f32_16x16x32_bf16 a[220:223],v[120:123],a[112:115],a[220:223]
ds_read_b64_tr_b16 v[120:121],v2 offset:20640
v_mfma_f32_16x16x32_bf16 a[220:223],v[116:119],a[104:107],a[220:223]
s_waitcnt lgkmcnt(3)
ds_read_b64_tr_b16 v[116:117],v2 offset:10400
v_mfma_f32_16x16x32_bf16 a[220:223],v[112:115],a[96:99],a[220:223]
buffer_load_dwordx4 v[140:143],v28,s[28:31],s87 offen offset:3072 sc1
v_mfma_f32_16x16x32_bf16 a[224:227],v[64:67],a[68:71],a[224:227]
s_add_u32 s87,s64,1
s_and_b32 s87,s87,3
s_lshl_b32 s87,s87,1
v_mfma_f32_16x16x32_bf16 a[224:227],v[68:71],a[76:79],a[224:227]
s_or_b32 s87,s87,1
s_lshl_b32 s87,s87,16
ds_read_b64_tr_b16 v[112:113],v2 offset:160
v_mfma_f32_16x16x32_bf16 a[224:227],v[72:75],a[84:87],a[224:227]
ds_read_b64_tr_b16 v[114:115],v2 offset:5280
ds_read_b64_tr_b16 v[118:119],v2 offset:15520
v_mfma_f32_16x16x32_bf16 a[224:227],v[76:79],a[92:95],a[224:227]
ds_read_b64_tr_b16 v[122:123],v2 offset:25760
v_mfma_f32_16x16x32_bf16 a[228:231],v[76:79],a[124:127],a[228:231]
buffer_load_dwordx4 v[212:215],v6,s[28:31],s88 offen offset:2048 sc1
v_mfma_f32_16x16x32_bf16 a[228:231],v[72:75],a[116:119],a[228:231]
v_mfma_f32_16x16x32_bf16 a[228:231],v[68:71],a[108:111],a[228:231]
v_mfma_f32_16x16x32_bf16 a[228:231],v[64:67],a[100:103],a[228:231]
v_mfma_f32_16x16x32_bf16 a[232:235],v[64:67],a[64:67],a[232:235]
v_mfma_f32_16x16x32_bf16 a[232:235],v[68:71],a[72:75],a[232:235]
buffer_load_dwordx4 v[148:151],v6,s[28:31],s88 offen offset:1024 sc1
v_mfma_f32_16x16x32_bf16 a[232:235],v[72:75],a[80:83],a[232:235]
s_waitcnt vmcnt(11)
v_cmp_ne_u32 s[56:57],s52,v4
v_cmp_ne_u32 s[58:59],s53,v5
v_mfma_f32_16x16x32_bf16 a[232:235],v[76:79],a[88:91],a[232:235]
s_or_b64 s[56:57],s[56:57],s[58:59]
s_cbranch_scc1 L_fr7_%=
L_fo7_%=:
s_lshl_b32 s91,s64,6
v_mfma_f32_16x16x32_bf16 a[236:239],v[76:79],a[120:123],a[236:239]
ds_read_b64_tr_b16 v[76:77],v1 offset:30912
ds_read_b64_tr_b16 v[78:79],v1 offset:36032
v_mfma_f32_16x16x32_bf16 a[236:239],v[72:75],a[112:115],a[236:239]
ds_read_b64_tr_b16 v[72:73],v1 offset:20672
v_bfe_u32 v3,v6,4,2
v_mfma_f32_16x16x32_bf16 a[236:239],v[68:71],a[104:107],a[236:239]
buffer_load_dwordx4 v[40:43],v6,s[28:31],s89 offen sc1
v_mfma_f32_16x16x32_bf16 a[236:239],v[64:67],a[96:99],a[236:239]
ds_read_b64_tr_b16 v[64:65],v1 offset:192
ds_read_b64_tr_b16 v[66:67],v1 offset:5312
v_mfma_f32_16x16x32_bf16 a[240:243],v[80:83],a[68:71],a[240:243]
ds_read_b64_tr_b16 v[68:69],v1 offset:10432
v_min_u32 v3,2,v3
v_mfma_f32_16x16x32_bf16 a[240:243],v[84:87],a[76:79],a[240:243]
ds_read_b64_tr_b16 v[70:71],v1 offset:15552
v_add3_u32 v3,v3,s63,1
v_mfma_f32_16x16x32_bf16 a[240:243],v[88:91],a[84:87],a[240:243]
ds_read_b64_tr_b16 v[74:75],v1 offset:25792
v_and_b32 v3,3,v3
v_mfma_f32_16x16x32_bf16 a[240:243],v[92:95],a[92:95],a[240:243]
buffer_load_dwordx4 a[128:131],v6,s[28:31],s87 offen sc1
v_mfma_f32_16x16x32_bf16 a[244:247],v[92:95],a[124:127],a[244:247]
v_lshlrev_b32 v3,8,v3
v_mfma_f32_16x16x32_bf16 a[244:247],v[88:91],a[116:119],a[244:247]
v_mfma_f32_16x16x32_bf16 a[244:247],v[84:87],a[108:111],a[244:247]
v_mfma_f32_16x16x32_bf16 a[244:247],v[80:83],a[100:103],a[244:247]
v_mfma_f32_16x16x32_bf16 a[248:251],v[80:83],a[64:67],a[248:251]
buffer_load_dwordx4 v[156:159],v28,s[28:31],s88 offen offset:3072 sc1
v_mfma_f32_16x16x32_bf16 a[248:251],v[84:87],a[72:75],a[248:251]
s_add_u32 s88,s87,4096
v_mfma_f32_16x16x32_bf16 a[248:251],v[88:91],a[80:83],a[248:251]
v_mfma_f32_16x16x32_bf16 a[248:251],v[92:95],a[88:91],a[248:251]
v_mfma_f32_16x16x32_bf16 a[252:255],v[92:95],a[120:123],a[252:255]
ds_read_b64_tr_b16 v[92:93],v2 offset:30944
ds_read_b64_tr_b16 v[94:95],v2 offset:36064
v_mfma_f32_16x16x32_bf16 a[252:255],v[88:91],a[112:115],a[252:255]
buffer_load_dwordx4 v[228:231],v6,s[28:31],s89 offen offset:2048 sc1
v_mfma_f32_16x16x32_bf16 a[252:255],v[84:87],a[104:107],a[252:255]
ds_read_b64_tr_b16 v[84:85],v2 offset:10464
ds_read_b64_tr_b16 v[86:87],v2 offset:15584
v_mfma_f32_16x16x32_bf16 a[252:255],v[80:83],a[96:99],a[252:255]
ds_read_b64_tr_b16 v[80:81],v2 offset:224
v_mfma_f32_16x16x32_bf16 a[0:3],v[96:99],a[68:71],a[0:3]
ds_read_b64_tr_b16 v[82:83],v2 offset:5344
v_mfma_f32_16x16x32_bf16 a[0:3],v[100:103],a[76:79],a[0:3]
ds_read_b64_tr_b16 v[88:89],v2 offset:20704
v_mfma_f32_16x16x32_bf16 a[0:3],v[104:107],a[84:87],a[0:3]
buffer_load_dwordx4 v[164:167],v6,s[28:31],s89 offen offset:1024 sc1
v_mfma_f32_16x16x32_bf16 a[0:3],v[108:111],a[92:95],a[0:3]
ds_read_b64_tr_b16 v[90:91],v2 offset:25824
v_mfma_f32_16x16x32_bf16 a[4:7],v[108:111],a[124:127],a[4:7]
v_mfma_f32_16x16x32_bf16 a[4:7],v[104:107],a[116:119],a[4:7]
v_mfma_f32_16x16x32_bf16 a[4:7],v[100:103],a[108:111],a[4:7]
v_mfma_f32_16x16x32_bf16 a[4:7],v[96:99],a[100:103],a[4:7]
buffer_load_dwordx4 v[44:47],v6,s[28:31],s90 offen sc1
v_mfma_f32_16x16x32_bf16 a[8:11],v[96:99],a[64:67],a[8:11]
v_mfma_f32_16x16x32_bf16 a[8:11],v[100:103],a[72:75],a[8:11]
v_mfma_f32_16x16x32_bf16 a[8:11],v[104:107],a[80:83],a[8:11]
v_mfma_f32_16x16x32_bf16 a[8:11],v[108:111],a[88:91],a[8:11]
v_mfma_f32_16x16x32_bf16 a[12:15],v[108:111],a[120:123],a[12:15]
buffer_load_dwordx4 a[144:147],v6,s[28:31],s88 offen sc1
v_mfma_f32_16x16x32_bf16 a[12:15],v[104:107],a[112:115],a[12:15]
ds_read_b64_tr_b16 v[104:105],v1 offset:20736
ds_read_b64_tr_b16 v[106:107],v1 offset:25856
v_mfma_f32_16x16x32_bf16 a[12:15],v[100:103],a[104:107],a[12:15]
ds_read_b64_tr_b16 v[100:101],v1 offset:10496
v_mfma_f32_16x16x32_bf16 a[12:15],v[96:99],a[96:99],a[12:15]
s_waitcnt lgkmcnt(3)
ds_read_b64_tr_b16 v[96:97],v1 offset:256
v_mfma_f32_16x16x32_bf16 a[16:19],v[112:115],a[68:71],a[16:19]
ds_read_b64_tr_b16 v[98:99],v1 offset:5376
v_mfma_f32_16x16x32_bf16 a[16:19],v[116:119],a[76:79],a[16:19]
buffer_load_dwordx4 v[172:175],v28,s[28:31],s89 offen offset:3072 sc1
v_mfma_f32_16x16x32_bf16 a[16:19],v[120:123],a[84:87],a[16:19]
s_add_u32 s89,s87,8192
ds_read_b64_tr_b16 v[102:103],v1 offset:15616
ds_read_b64_tr_b16 v[108:109],v1 offset:30976
v_mfma_f32_16x16x32_bf16 a[16:19],v[124:127],a[92:95],a[16:19]
ds_read_b64_tr_b16 v[110:111],v1 offset:36096
v_add_u32 v1,0x1e000,v1
v_mfma_f32_16x16x32_bf16 a[20:23],v[124:127],a[124:127],a[20:23]
v_mfma_f32_16x16x32_bf16 a[20:23],v[120:123],a[116:119],a[20:23]
v_mfma_f32_16x16x32_bf16 a[20:23],v[116:119],a[108:111],a[20:23]
buffer_load_dwordx4 v[180:183],v6,s[28:31],s90 offen offset:1024 sc1
v_mfma_f32_16x16x32_bf16 a[20:23],v[112:115],a[100:103],a[20:23]
v_mfma_f32_16x16x32_bf16 a[24:27],v[112:115],a[64:67],a[24:27]
v_mfma_f32_16x16x32_bf16 a[24:27],v[116:119],a[72:75],a[24:27]
v_mfma_f32_16x16x32_bf16 a[24:27],v[120:123],a[80:83],a[24:27]
v_mfma_f32_16x16x32_bf16 a[24:27],v[124:127],a[88:91],a[24:27]
buffer_load_dwordx4 a[160:163],v6,s[28:31],s89 offen sc1
v_mfma_f32_16x16x32_bf16 a[28:31],v[124:127],a[120:123],a[28:31]
ds_read_b64_tr_b16 v[124:125],v2 offset:31008
ds_read_b64_tr_b16 v[126:127],v2 offset:36128
v_mfma_f32_16x16x32_bf16 a[28:31],v[120:123],a[112:115],a[28:31]
ds_read_b64_tr_b16 v[120:121],v2 offset:20768
v_mfma_f32_16x16x32_bf16 a[28:31],v[116:119],a[104:107],a[28:31]
ds_read_b64_tr_b16 v[116:117],v2 offset:10528
v_mfma_f32_16x16x32_bf16 a[28:31],v[112:115],a[96:99],a[28:31]
ds_read_b64_tr_b16 v[112:113],v2 offset:288
v_mfma_f32_16x16x32_bf16 v[128:131],v[64:67],a[68:71],v[128:131]
buffer_load_dwordx4 v[244:247],v6,s[28:31],s90 offen offset:2048 sc1
)A"
R"A(v_mfma_f32_16x16x32_bf16 v[128:131],v[68:71],a[76:79],v[128:131]
ds_read_b64_tr_b16 v[114:115],v2 offset:5408
ds_read_b64_tr_b16 v[118:119],v2 offset:15648
v_mfma_f32_16x16x32_bf16 v[128:131],v[72:75],a[84:87],v[128:131]
ds_read_b64_tr_b16 v[122:123],v2 offset:25888
v_add_u32 v2,0x1e000,v2
v_mfma_f32_16x16x32_bf16 v[128:131],v[76:79],a[92:95],v[128:131]
v_mfma_f32_16x16x32_bf16 v[136:139],v[76:79],a[124:127],v[136:139]
v_mfma_f32_16x16x32_bf16 v[136:139],v[72:75],a[116:119],v[136:139]
buffer_load_dwordx4 v[188:191],v28,s[28:31],s90 offen offset:3072 sc1
v_mfma_f32_16x16x32_bf16 v[136:139],v[68:71],a[108:111],v[136:139]
s_add_u32 s90,s87,12288
v_mfma_f32_16x16x32_bf16 v[136:139],v[64:67],a[100:103],v[136:139]
v_mfma_f32_16x16x32_bf16 v[144:147],v[64:67],a[64:67],v[144:147]
v_mfma_f32_16x16x32_bf16 v[144:147],v[68:71],a[72:75],v[144:147]
v_mfma_f32_16x16x32_bf16 v[144:147],v[72:75],a[80:83],v[144:147]
buffer_load_dwordx4 a[176:179],v6,s[28:31],s90 offen sc1
v_mfma_f32_16x16x32_bf16 v[144:147],v[76:79],a[88:91],v[144:147]
v_mfma_f32_16x16x32_bf16 v[152:155],v[76:79],a[120:123],v[152:155]
v_mfma_f32_16x16x32_bf16 v[152:155],v[72:75],a[112:115],v[152:155]
v_mfma_f32_16x16x32_bf16 v[152:155],v[68:71],a[104:107],v[152:155]
v_mfma_f32_16x16x32_bf16 v[152:155],v[64:67],a[96:99],v[152:155]
buffer_load_dwordx4 a[180:183],v6,s[28:31],s90 offen offset:1024 sc1
s_waitcnt vmcnt(21)
s_barrier
v_mfma_f32_16x16x32_bf16 v[160:163],v[80:83],a[68:71],v[160:163]
ds_read_b64_tr_b16 v[64:65],v1
ds_read_b64_tr_b16 v[66:67],v1 offset:5120
v_mfma_f32_16x16x32_bf16 v[160:163],v[84:87],a[76:79],v[160:163]
ds_read_b64_tr_b16 v[68:69],v1 offset:10240
v_mfma_f32_16x16x32_bf16 v[160:163],v[88:91],a[84:87],v[160:163]
ds_read_b64_tr_b16 v[70:71],v1 offset:15360
v_mfma_f32_16x16x32_bf16 v[160:163],v[92:95],a[92:95],v[160:163]
buffer_load_dwordx4 a[164:167],v6,s[28:31],s89 offen offset:1024 sc1
v_mfma_f32_16x16x32_bf16 v[168:171],v[92:95],a[124:127],v[168:171]
ds_read_b64_tr_b16 v[72:73],v1 offset:20480
ds_read_b64_tr_b16 v[74:75],v1 offset:25600
v_mfma_f32_16x16x32_bf16 v[168:171],v[88:91],a[116:119],v[168:171]
ds_read_b64_tr_b16 v[76:77],v1 offset:30720
v_mfma_f32_16x16x32_bf16 v[168:171],v[84:87],a[108:111],v[168:171]
ds_read_b64_tr_b16 v[78:79],v1 offset:35840
v_mfma_f32_16x16x32_bf16 v[168:171],v[80:83],a[100:103],v[168:171]
v_mfma_f32_16x16x32_bf16 v[176:179],v[80:83],a[64:67],v[176:179]
buffer_load_dwordx4 a[148:151],v6,s[28:31],s88 offen offset:1024 sc1
v_mfma_f32_16x16x32_bf16 v[176:179],v[84:87],a[72:75],v[176:179]
v_mfma_f32_16x16x32_bf16 v[176:179],v[88:91],a[80:83],v[176:179]
v_mfma_f32_16x16x32_bf16 v[176:179],v[92:95],a[88:91],v[176:179]
v_mfma_f32_16x16x32_bf16 v[184:187],v[92:95],a[120:123],v[184:187]
ds_read_b64_tr_b16 v[92:93],v2 offset:30752
ds_read_b64_tr_b16 v[94:95],v2 offset:35872
v_mfma_f32_16x16x32_bf16 v[184:187],v[88:91],a[112:115],v[184:187]
buffer_load_dwordx4 a[132:135],v6,s[28:31],s87 offen offset:1024 sc1
v_mfma_f32_16x16x32_bf16 v[184:187],v[84:87],a[104:107],v[184:187]
ds_read_b64_tr_b16 v[84:85],v2 offset:10272
ds_read_b64_tr_b16 v[86:87],v2 offset:15392
v_mfma_f32_16x16x32_bf16 v[184:187],v[80:83],a[96:99],v[184:187]
s_waitcnt lgkmcnt(4)
ds_read_b64_tr_b16 v[80:81],v2 offset:32
v_mfma_f32_16x16x32_bf16 v[192:195],v[96:99],a[68:71],v[192:195]
ds_read_b64_tr_b16 v[82:83],v2 offset:5152
v_mfma_f32_16x16x32_bf16 v[192:195],v[100:103],a[76:79],v[192:195]
ds_read_b64_tr_b16 v[88:89],v2 offset:20512
v_mfma_f32_16x16x32_bf16 v[192:195],v[104:107],a[84:87],v[192:195]
ds_read_b64_tr_b16 v[90:91],v2 offset:25632
v_mfma_f32_16x16x32_bf16 v[192:195],v[108:111],a[92:95],v[192:195]
buffer_load_dwordx4 v24,s[48:51],s83 offen lds
v_mfma_f32_16x16x32_bf16 v[200:203],v[108:111],a[124:127],v[200:203]
v_mfma_f32_16x16x32_bf16 v[200:203],v[104:107],a[116:119],v[200:203]
v_mfma_f32_16x16x32_bf16 v[200:203],v[100:103],a[108:111],v[200:203]
v_mfma_f32_16x16x32_bf16 v[200:203],v[96:99],a[100:103],v[200:203]
v_mfma_f32_16x16x32_bf16 v[208:211],v[96:99],a[64:67],v[208:211]
buffer_load_dwordx4 a[136:139],v6,s[28:31],s87 offen offset:2048 sc1
v_mfma_f32_16x16x32_bf16 v[208:211],v[100:103],a[72:75],v[208:211]
v_mfma_f32_16x16x32_bf16 v[208:211],v[104:107],a[80:83],v[208:211]
v_mfma_f32_16x16x32_bf16 v[208:211],v[108:111],a[88:91],v[208:211]
v_mfma_f32_16x16x32_bf16 v[216:219],v[108:111],a[120:123],v[216:219]
ds_read_b64_tr_b16 v[108:109],v1 offset:30784
ds_read_b64_tr_b16 v[110:111],v1 offset:35904
v_mfma_f32_16x16x32_bf16 v[216:219],v[104:107],a[112:115],v[216:219]
ds_read_b64_tr_b16 v[104:105],v1 offset:20544
v_mfma_f32_16x16x32_bf16 v[216:219],v[100:103],a[104:107],v[216:219]
buffer_load_dwordx4 v25,s[48:51],s83 offen offset:1024 lds
v_mfma_f32_16x16x32_bf16 v[216:219],v[96:99],a[96:99],v[216:219]
ds_read_b64_tr_b16 v[96:97],v1 offset:64
ds_read_b64_tr_b16 v[98:99],v1 offset:5184
v_mfma_f32_16x16x32_bf16 v[224:227],v[112:115],a[68:71],v[224:227]
ds_read_b64_tr_b16 v[100:101],v1 offset:10304
v_mfma_f32_16x16x32_bf16 v[224:227],v[116:119],a[76:79],v[224:227]
ds_read_b64_tr_b16 v[102:103],v1 offset:15424
v_mfma_f32_16x16x32_bf16 v[224:227],v[120:123],a[84:87],v[224:227]
ds_read_b64_tr_b16 v[106:107],v1 offset:25664
v_mfma_f32_16x16x32_bf16 v[224:227],v[124:127],a[92:95],v[224:227]
buffer_load_dwordx4 a[152:155],v6,s[28:31],s88 offen offset:2048 sc1
v_mfma_f32_16x16x32_bf16 v[232:235],v[124:127],a[124:127],v[232:235]
v_mfma_f32_16x16x32_bf16 v[232:235],v[120:123],a[116:119],v[232:235]
v_mfma_f32_16x16x32_bf16 v[232:235],v[116:119],a[108:111],v[232:235]
v_mfma_f32_16x16x32_bf16 v[232:235],v[112:115],a[100:103],v[232:235]
v_mfma_f32_16x16x32_bf16 v[240:243],v[112:115],a[64:67],v[240:243]
v_mfma_f32_16x16x32_bf16 v[240:243],v[116:119],a[72:75],v[240:243]
buffer_load_dwordx4 v26,s[48:51],s83 offen offset:2048 lds
v_mfma_f32_16x16x32_bf16 v[240:243],v[120:123],a[80:83],v[240:243]
v_mfma_f32_16x16x32_bf16 v[240:243],v[124:127],a[88:91],v[240:243]
v_mfma_f32_16x16x32_bf16 v[248:251],v[124:127],a[120:123],v[248:251]
ds_read_b64_tr_b16 v[124:125],v2 offset:30816
ds_read_b64_tr_b16 v[126:127],v2 offset:35936
v_mfma_f32_16x16x32_bf16 v[248:251],v[120:123],a[112:115],v[248:251]
ds_read_b64_tr_b16 v[120:121],v2 offset:20576
v_mfma_f32_16x16x32_bf16 v[248:251],v[116:119],a[104:107],v[248:251]
buffer_load_dwordx4 a[168:171],v6,s[28:31],s89 offen offset:2048 sc1
v_mfma_f32_16x16x32_bf16 v[248:251],v[112:115],a[96:99],v[248:251]
s_waitcnt vmcnt(22)
ds_read_b64_tr_b16 v[112:113],v2 offset:96
ds_read_b64_tr_b16 v[114:115],v2 offset:5216
v_mfma_f32_16x16x32_bf16 a[192:195],v[64:67],v[32:35],a[192:195]
ds_read_b64_tr_b16 v[116:117],v2 offset:10336
v_mfma_f32_16x16x32_bf16 a[192:195],v[68:71],v[36:39],a[192:195]
ds_read_b64_tr_b16 v[118:119],v2 offset:15456
v_mfma_f32_16x16x32_bf16 a[192:195],v[72:75],v[40:43],a[192:195]
s_waitcnt vmcnt(17)
ds_read_b64_tr_b16 v[122:123],v2 offset:25696
v_mfma_f32_16x16x32_bf16 a[192:195],v[76:79],v[44:47],a[192:195]
s_waitcnt vmcnt(14)
v_mfma_f32_16x16x32_bf16 a[196:199],v[76:79],v[180:183],a[196:199]
buffer_load_dwordx4 v27,s[48:51],s83 offen offset:3072 lds
v_mfma_f32_16x16x32_bf16 a[196:199],v[72:75],v[164:167],a[196:199]
s_add_u32 m0,s86,45056
v_mfma_f32_16x16x32_bf16 a[196:199],v[68:71],v[148:151],a[196:199]
v_mfma_f32_16x16x32_bf16 a[196:199],v[64:67],v[132:135],a[196:199]
v_mfma_f32_16x16x32_bf16 a[200:203],v[64:67],v[196:199],a[200:203]
v_mfma_f32_16x16x32_bf16 a[200:203],v[68:71],v[212:215],a[200:203]
buffer_load_dwordx4 a[184:187],v6,s[28:31],s90 offen offset:2048 sc1
v_mfma_f32_16x16x32_bf16 a[200:203],v[72:75],v[228:231],a[200:203]
s_waitcnt vmcnt(14)
v_mfma_f32_16x16x32_bf16 a[200:203],v[76:79],v[244:247],a[200:203]
s_waitcnt vmcnt(13)
v_mfma_f32_16x16x32_bf16 a[204:207],v[76:79],v[188:191],a[204:207]
ds_read_b64_tr_b16 v[76:77],v1 offset:30848
ds_read_b64_tr_b16 v[78:79],v1 offset:35968
v_mfma_f32_16x16x32_bf16 a[204:207],v[72:75],v[172:175],a[204:207]
ds_read_b64_tr_b16 v[72:73],v1 offset:20608
v_mfma_f32_16x16x32_bf16 a[204:207],v[68:71],v[156:159],a[204:207]
s_waitcnt lgkmcnt(3)
ds_read_b64_tr_b16 v[68:69],v1 offset:10368
v_mfma_f32_16x16x32_bf16 a[204:207],v[64:67],v[140:143],a[204:207]
buffer_load_dwordx4 v7,s[48:51],s83 offen lds
v_mfma_f32_16x16x32_bf16 a[208:211],v[80:83],v[32:35],a[208:211]
s_add_u32 m0,s86,46080
ds_read_b64_tr_b16 v[64:65],v1 offset:128
ds_read_b64_tr_b16 v[66:67],v1 offset:5248
v_mfma_f32_16x16x32_bf16 a[208:211],v[84:87],v[36:39],a[208:211]
s_mul_i32 s83,s63,20480
ds_read_b64_tr_b16 v[70:71],v1 offset:15488
v_mfma_f32_16x16x32_bf16 a[208:211],v[88:91],v[40:43],a[208:211]
ds_read_b64_tr_b16 v[74:75],v1 offset:25728
v_mfma_f32_16x16x32_bf16 a[208:211],v[92:95],v[44:47],a[208:211]
v_mfma_f32_16x16x32_bf16 a[212:215],v[92:95],v[180:183],a[212:215]
buffer_load_dwordx4 a[188:191],v28,s[28:31],s90 offen offset:3072 sc1
v_mfma_f32_16x16x32_bf16 a[212:215],v[88:91],v[164:167],a[212:215]
v_mfma_f32_16x16x32_bf16 a[212:215],v[84:87],v[148:151],a[212:215]
v_mfma_f32_16x16x32_bf16 a[212:215],v[80:83],v[132:135],a[212:215]
v_mfma_f32_16x16x32_bf16 a[216:219],v[80:83],v[196:199],a[216:219]
v_mfma_f32_16x16x32_bf16 a[216:219],v[84:87],v[212:215],a[216:219]
v_mfma_f32_16x16x32_bf16 a[216:219],v[88:91],v[228:231],a[216:219]
buffer_load_dwordx4 v24,s[48:51],s84 offen lds
v_mfma_f32_16x16x32_bf16 a[216:219],v[92:95],v[244:247],a[216:219]
v_mfma_f32_16x16x32_bf16 a[220:223],v[92:95],v[188:191],a[220:223]
ds_read_b64_tr_b16 v[92:93],v2 offset:30880
ds_read_b64_tr_b16 v[94:95],v2 offset:36000
v_mfma_f32_16x16x32_bf16 a[220:223],v[88:91],v[172:175],a[220:223]
ds_read_b64_tr_b16 v[88:89],v2 offset:20640
v_mfma_f32_16x16x32_bf16 a[220:223],v[84:87],v[156:159],a[220:223]
ds_read_b64_tr_b16 v[84:85],v2 offset:10400
v_mfma_f32_16x16x32_bf16 a[220:223],v[80:83],v[140:143],a[220:223]
ds_read_b64_tr_b16 v[80:81],v2 offset:160
v_mfma_f32_16x16x32_bf16 a[224:227],v[96:99],v[32:35],a[224:227]
buffer_load_dwordx4 v25,s[48:51],s84 offen offset:1024 lds
v_mfma_f32_16x16x32_bf16 a[224:227],v[100:103],v[36:39],a[224:227]
ds_read_b64_tr_b16 v[82:83],v2 offset:5280
ds_read_b64_tr_b16 v[86:87],v2 offset:15520
v_mfma_f32_16x16x32_bf16 a[224:227],v[104:107],v[40:43],a[224:227]
ds_read_b64_tr_b16 v[90:91],v2 offset:25760
v_mfma_f32_16x16x32_bf16 a[224:227],v[108:111],v[44:47],a[224:227]
v_mfma_f32_16x16x32_bf16 a[228:231],v[108:111],v[180:183],a[228:231]
v_mfma_f32_16x16x32_bf16 a[228:231],v[104:107],v[164:167],a[228:231]
buffer_load_dwordx4 a[172:175],v28,s[28:31],s89 offen offset:3072 sc1
v_mfma_f32_16x16x32_bf16 a[228:231],v[100:103],v[148:151],a[228:231]
v_mfma_f32_16x16x32_bf16 a[228:231],v[96:99],v[132:135],a[228:231]
v_mfma_f32_16x16x32_bf16 a[232:235],v[96:99],v[196:199],a[232:235]
v_mfma_f32_16x16x32_bf16 a[232:235],v[100:103],v[212:215],a[232:235]
v_mfma_f32_16x16x32_bf16 a[232:235],v[104:107],v[228:231],a[232:235]
v_mfma_f32_16x16x32_bf16 a[232:235],v[108:111],v[244:247],a[232:235]
buffer_load_dwordx4 v26,s[48:51],s84 offen offset:2048 lds
v_mfma_f32_16x16x32_bf16 a[236:239],v[108:111],v[188:191],a[236:239]
ds_read_b64_tr_b16 v[108:109],v1 offset:30912
ds_read_b64_tr_b16 v[110:111],v1 offset:36032
v_mfma_f32_16x16x32_bf16 a[236:239],v[104:107],v[172:175],a[236:239]
ds_read_b64_tr_b16 v[104:105],v1 offset:20672
v_mfma_f32_16x16x32_bf16 a[236:239],v[100:103],v[156:159],a[236:239]
ds_read_b64_tr_b16 v[100:101],v1 offset:10432
v_mfma_f32_16x16x32_bf16 a[236:239],v[96:99],v[140:143],a[236:239]
ds_read_b64_tr_b16 v[96:97],v1 offset:192
v_mfma_f32_16x16x32_bf16 a[240:243],v[112:115],v[32:35],a[240:243]
ds_read_b64_tr_b16 v[98:99],v1 offset:5312
ds_read_b64_tr_b16 v[102:103],v1 offset:15552
v_mfma_f32_16x16x32_bf16 a[240:243],v[116:119],v[36:39],a[240:243]
buffer_load_dwordx4 v27,s[48:51],s84 offen offset:3072 lds
v_mfma_f32_16x16x32_bf16 a[240:243],v[120:123],v[40:43],a[240:243]
s_add_u32 m0,s86,50176
ds_read_b64_tr_b16 v[106:107],v1 offset:25792
v_add_u32 v1,0xffff6000,v1
v_mfma_f32_16x16x32_bf16 a[240:243],v[124:127],v[44:47],a[240:243]
v_mfma_f32_16x16x32_bf16 a[244:247],v[124:127],v[180:183],a[244:247]
)A"
R"A(v_mfma_f32_16x16x32_bf16 a[244:247],v[120:123],v[164:167],a[244:247]
v_mfma_f32_16x16x32_bf16 a[244:247],v[116:119],v[148:151],a[244:247]
buffer_load_dwordx4 a[140:143],v28,s[28:31],s87 offen offset:3072 sc1
v_mfma_f32_16x16x32_bf16 a[244:247],v[112:115],v[132:135],a[244:247]
s_add_u32 s87,s64,2
s_and_b32 s87,s87,3
s_lshl_b32 s87,s87,1
s_or_b32 s87,s87,1
s_lshl_b32 s87,s87,16
s_add_u32 s89,s87,8192
s_add_u32 s90,s87,12288
v_mfma_f32_16x16x32_bf16 a[248:251],v[112:115],v[196:199],a[248:251]
v_mfma_f32_16x16x32_bf16 a[248:251],v[116:119],v[212:215],a[248:251]
v_mfma_f32_16x16x32_bf16 a[248:251],v[120:123],v[228:231],a[248:251]
v_mfma_f32_16x16x32_bf16 a[248:251],v[124:127],v[244:247],a[248:251]
buffer_load_dwordx4 a[68:71],v6,s[28:31],s87 offen sc1
v_mfma_f32_16x16x32_bf16 a[252:255],v[124:127],v[188:191],a[252:255]
ds_read_b64_tr_b16 v[124:125],v2 offset:30944
ds_read_b64_tr_b16 v[126:127],v2 offset:36064
v_mfma_f32_16x16x32_bf16 a[252:255],v[120:123],v[172:175],a[252:255]
ds_read_b64_tr_b16 v[120:121],v2 offset:20704
v_mfma_f32_16x16x32_bf16 a[252:255],v[116:119],v[156:159],a[252:255]
ds_read_b64_tr_b16 v[116:117],v2 offset:10464
v_mfma_f32_16x16x32_bf16 a[252:255],v[112:115],v[140:143],a[252:255]
s_waitcnt lgkmcnt(4)
ds_read_b64_tr_b16 v[112:113],v2 offset:224
v_mfma_f32_16x16x32_bf16 a[0:3],v[64:67],v[32:35],a[0:3]
buffer_load_dwordx4 a[156:159],v28,s[28:31],s88 offen offset:3072 sc1
v_mfma_f32_16x16x32_bf16 a[0:3],v[68:71],v[36:39],a[0:3]
s_add_u32 s88,s87,4096
ds_read_b64_tr_b16 v[114:115],v2 offset:5344
ds_read_b64_tr_b16 v[118:119],v2 offset:15584
v_mfma_f32_16x16x32_bf16 a[0:3],v[72:75],v[40:43],a[0:3]
ds_read_b64_tr_b16 v[122:123],v2 offset:25824
v_add_u32 v2,0xffff6000,v2
v_mfma_f32_16x16x32_bf16 a[0:3],v[76:79],v[44:47],a[0:3]
v_mfma_f32_16x16x32_bf16 a[4:7],v[76:79],v[180:183],a[4:7]
v_mfma_f32_16x16x32_bf16 a[4:7],v[72:75],v[164:167],a[4:7]
v_mfma_f32_16x16x32_bf16 a[4:7],v[68:71],v[148:151],a[4:7]
buffer_load_dwordx4 v7,s[48:51],s84 offen lds
v_mfma_f32_16x16x32_bf16 a[4:7],v[64:67],v[132:135],a[4:7]
v_mfma_f32_16x16x32_bf16 a[8:11],v[64:67],v[196:199],a[8:11]
v_mfma_f32_16x16x32_bf16 a[8:11],v[68:71],v[212:215],a[8:11]
v_mfma_f32_16x16x32_bf16 a[8:11],v[72:75],v[228:231],a[8:11]
v_mfma_f32_16x16x32_bf16 a[8:11],v[76:79],v[244:247],a[8:11]
buffer_load_dwordx4 a[76:79],v6,s[28:31],s88 offen sc1
v_mfma_f32_16x16x32_bf16 a[12:15],v[76:79],v[188:191],a[12:15]
ds_read_b64_tr_b16 v[76:77],v1 offset:30720
ds_read_b64_tr_b16 v[78:79],v1 offset:35840
v_mfma_f32_16x16x32_bf16 a[12:15],v[72:75],v[172:175],a[12:15]
ds_read_b64_tr_b16 v[72:73],v1 offset:20480
v_mfma_f32_16x16x32_bf16 a[12:15],v[68:71],v[156:159],a[12:15]
ds_read_b64_tr_b16 v[68:69],v1 offset:10240
v_mfma_f32_16x16x32_bf16 a[12:15],v[64:67],v[140:143],a[12:15]
ds_read_b64_tr_b16 v[64:65],v1
v_mfma_f32_16x16x32_bf16 a[16:19],v[80:83],v[32:35],a[16:19]
buffer_load_dwordx4 a[84:87],v6,s[28:31],s89 offen sc1
v_mfma_f32_16x16x32_bf16 a[16:19],v[84:87],v[36:39],a[16:19]
ds_read_b64_tr_b16 v[66:67],v1 offset:5120
ds_read_b64_tr_b16 v[70:71],v1 offset:15360
v_mfma_f32_16x16x32_bf16 a[16:19],v[88:91],v[40:43],a[16:19]
ds_read_b64_tr_b16 v[74:75],v1 offset:25600
s_nop 0
L_pd22_%=:
s_cbranch_execz L_trtg2S_%=
v_add_u32 v1,0xffff6000,v1
v_mfma_f32_16x16x32_bf16 a[16:19],v[92:95],v[44:47],a[16:19]
v_mfma_f32_16x16x32_bf16 a[20:23],v[92:95],v[180:183],a[20:23]
v_mfma_f32_16x16x32_bf16 a[20:23],v[88:91],v[164:167],a[20:23]
buffer_load_dwordx4 a[92:95],v6,s[28:31],s90 offen sc1
v_mfma_f32_16x16x32_bf16 a[20:23],v[84:87],v[148:151],a[20:23]
v_mfma_f32_16x16x32_bf16 a[20:23],v[80:83],v[132:135],a[20:23]
v_mfma_f32_16x16x32_bf16 a[24:27],v[80:83],v[196:199],a[24:27]
v_mfma_f32_16x16x32_bf16 a[24:27],v[84:87],v[212:215],a[24:27]
v_mfma_f32_16x16x32_bf16 a[24:27],v[88:91],v[228:231],a[24:27]
buffer_load_dwordx4 a[124:127],v6,s[28:31],s90 offen offset:1024 sc1
v_mfma_f32_16x16x32_bf16 a[24:27],v[92:95],v[244:247],a[24:27]
v_mfma_f32_16x16x32_bf16 a[28:31],v[92:95],v[188:191],a[28:31]
ds_read_b64_tr_b16 v[92:93],v2 offset:30752
ds_read_b64_tr_b16 v[94:95],v2 offset:35872
v_mfma_f32_16x16x32_bf16 a[28:31],v[88:91],v[172:175],a[28:31]
ds_read_b64_tr_b16 v[88:89],v2 offset:20512
v_mfma_f32_16x16x32_bf16 a[28:31],v[84:87],v[156:159],a[28:31]
ds_read_b64_tr_b16 v[84:85],v2 offset:10272
v_mfma_f32_16x16x32_bf16 a[28:31],v[80:83],v[140:143],a[28:31]
buffer_load_dwordx4 a[116:119],v6,s[28:31],s89 offen offset:1024 sc1
v_mfma_f32_16x16x32_bf16 v[128:131],v[96:99],v[32:35],v[128:131]
ds_read_b64_tr_b16 v[80:81],v2 offset:32
ds_read_b64_tr_b16 v[82:83],v2 offset:5152
v_mfma_f32_16x16x32_bf16 v[128:131],v[100:103],v[36:39],v[128:131]
ds_read_b64_tr_b16 v[86:87],v2 offset:15392
v_mfma_f32_16x16x32_bf16 v[128:131],v[104:107],v[40:43],v[128:131]
ds_read_b64_tr_b16 v[90:91],v2 offset:25632
v_add_u32 v2,0xffff6000,v2
v_mfma_f32_16x16x32_bf16 v[128:131],v[108:111],v[44:47],v[128:131]
v_mfma_f32_16x16x32_bf16 v[136:139],v[108:111],v[180:183],v[136:139]
buffer_load_dwordx4 a[108:111],v6,s[28:31],s88 offen offset:1024 sc1
v_mfma_f32_16x16x32_bf16 v[136:139],v[104:107],v[164:167],v[136:139]
v_mfma_f32_16x16x32_bf16 v[136:139],v[100:103],v[148:151],v[136:139]
v_mfma_f32_16x16x32_bf16 v[136:139],v[96:99],v[132:135],v[136:139]
v_mfma_f32_16x16x32_bf16 v[144:147],v[96:99],v[196:199],v[144:147]
v_mfma_f32_16x16x32_bf16 v[144:147],v[100:103],v[212:215],v[144:147]
buffer_load_dwordx4 a[100:103],v6,s[28:31],s87 offen offset:1024 sc1
v_mfma_f32_16x16x32_bf16 v[144:147],v[104:107],v[228:231],v[144:147]
v_mfma_f32_16x16x32_bf16 v[144:147],v[108:111],v[244:247],v[144:147]
v_mfma_f32_16x16x32_bf16 v[152:155],v[108:111],v[188:191],v[152:155]
v_mfma_f32_16x16x32_bf16 v[152:155],v[104:107],v[172:175],v[152:155]
v_mfma_f32_16x16x32_bf16 v[152:155],v[100:103],v[156:159],v[152:155]
buffer_load_dwordx4 a[64:67],v6,s[28:31],s87 offen offset:2048 sc1
v_mfma_f32_16x16x32_bf16 v[152:155],v[96:99],v[140:143],v[152:155]
s_waitcnt vmcnt(8)
s_barrier
s_waitcnt lgkmcnt(0)
v_mfma_f32_16x16x32_bf16 v[160:163],v[112:115],v[32:35],v[160:163]
ds_read_b64_tr_b16 v[96:97],v1
ds_read_b64_tr_b16 v[98:99],v1 offset:5120
v_mfma_f32_16x16x32_bf16 v[160:163],v[116:119],v[36:39],v[160:163]
ds_read_b64_tr_b16 v[100:101],v1 offset:10240
v_mfma_f32_16x16x32_bf16 v[160:163],v[120:123],v[40:43],v[160:163]
buffer_load_dwordx4 a[72:75],v6,s[28:31],s88 offen offset:2048 sc1
v_mfma_f32_16x16x32_bf16 v[160:163],v[124:127],v[44:47],v[160:163]
ds_read_b64_tr_b16 v[102:103],v1 offset:15360
ds_read_b64_tr_b16 v[104:105],v1 offset:20480
v_mfma_f32_16x16x32_bf16 v[168:171],v[124:127],v[180:183],v[168:171]
ds_read_b64_tr_b16 v[106:107],v1 offset:25600
v_mfma_f32_16x16x32_bf16 v[168:171],v[120:123],v[164:167],v[168:171]
ds_read_b64_tr_b16 v[108:109],v1 offset:30720
v_mfma_f32_16x16x32_bf16 v[168:171],v[116:119],v[148:151],v[168:171]
ds_read_b64_tr_b16 v[110:111],v1 offset:35840
v_add_u32 v1,0xa000,v1
v_mfma_f32_16x16x32_bf16 v[168:171],v[112:115],v[132:135],v[168:171]
buffer_load_dwordx4 a[80:83],v6,s[28:31],s89 offen offset:2048 sc1
v_mfma_f32_16x16x32_bf16 v[176:179],v[112:115],v[196:199],v[176:179]
v_mfma_f32_16x16x32_bf16 v[176:179],v[116:119],v[212:215],v[176:179]
v_mfma_f32_16x16x32_bf16 v[176:179],v[120:123],v[228:231],v[176:179]
v_mfma_f32_16x16x32_bf16 v[176:179],v[124:127],v[244:247],v[176:179]
v_mfma_f32_16x16x32_bf16 v[184:187],v[124:127],v[188:191],v[184:187]
buffer_load_dwordx4 a[88:91],v6,s[28:31],s90 offen offset:2048 sc1
v_mfma_f32_16x16x32_bf16 v[184:187],v[120:123],v[172:175],v[184:187]
ds_read_b64_tr_b16 v[120:121],v2 offset:20512
ds_read_b64_tr_b16 v[122:123],v2 offset:25632
v_mfma_f32_16x16x32_bf16 v[184:187],v[116:119],v[156:159],v[184:187]
ds_read_b64_tr_b16 v[116:117],v2 offset:10272
v_mfma_f32_16x16x32_bf16 v[184:187],v[112:115],v[140:143],v[184:187]
ds_read_b64_tr_b16 v[112:113],v2 offset:32
v_mfma_f32_16x16x32_bf16 a[192:195],v[64:67],a[128:131],a[192:195]
ds_read_b64_tr_b16 v[114:115],v2 offset:5152
v_mfma_f32_16x16x32_bf16 a[192:195],v[68:71],a[144:147],a[192:195]
buffer_load_dwordx4 a[120:123],v28,s[28:31],s90 offen offset:3072 sc1
v_mfma_f32_16x16x32_bf16 a[192:195],v[72:75],a[160:163],a[192:195]
ds_read_b64_tr_b16 v[118:119],v2 offset:15392
ds_read_b64_tr_b16 v[124:125],v2 offset:30752
v_mfma_f32_16x16x32_bf16 a[192:195],v[76:79],a[176:179],a[192:195]
ds_read_b64_tr_b16 v[126:127],v2 offset:35872
v_add_u32 v2,0xa000,v2
v_mfma_f32_16x16x32_bf16 a[196:199],v[76:79],a[180:183],a[196:199]
v_mfma_f32_16x16x32_bf16 a[196:199],v[72:75],a[164:167],a[196:199]
v_mfma_f32_16x16x32_bf16 a[196:199],v[68:71],a[148:151],a[196:199]
buffer_load_dwordx4 a[112:115],v28,s[28:31],s89 offen offset:3072 sc1
v_mfma_f32_16x16x32_bf16 a[196:199],v[64:67],a[132:135],a[196:199]
v_mfma_f32_16x16x32_bf16 a[200:203],v[64:67],a[136:139],a[200:203]
v_mfma_f32_16x16x32_bf16 a[200:203],v[68:71],a[152:155],a[200:203]
v_mfma_f32_16x16x32_bf16 a[200:203],v[72:75],a[168:171],a[200:203]
v_mfma_f32_16x16x32_bf16 a[200:203],v[76:79],a[184:187],a[200:203]
buffer_load_dwordx4 a[104:107],v28,s[28:31],s88 offen offset:3072 sc1
v_mfma_f32_16x16x32_bf16 a[204:207],v[76:79],a[188:191],a[204:207]
ds_read_b64_tr_b16 v[76:77],v1 offset:30784
ds_read_b64_tr_b16 v[78:79],v1 offset:35904
v_mfma_f32_16x16x32_bf16 a[204:207],v[72:75],a[172:175],a[204:207]
ds_read_b64_tr_b16 v[72:73],v1 offset:20544
v_mfma_f32_16x16x32_bf16 a[204:207],v[68:71],a[156:159],a[204:207]
ds_read_b64_tr_b16 v[68:69],v1 offset:10304
v_mfma_f32_16x16x32_bf16 a[204:207],v[64:67],a[140:143],a[204:207]
ds_read_b64_tr_b16 v[64:65],v1 offset:64
v_mfma_f32_16x16x32_bf16 a[208:211],v[80:83],a[128:131],a[208:211]
buffer_load_dwordx4 a[96:99],v28,s[28:31],s87 offen offset:3072 sc1
v_mfma_f32_16x16x32_bf16 a[208:211],v[84:87],a[144:147],a[208:211]
s_mul_i32 s87,s60,81920
ds_read_b64_tr_b16 v[66:67],v1 offset:5184
ds_read_b64_tr_b16 v[70:71],v1 offset:15424
v_mfma_f32_16x16x32_bf16 a[208:211],v[88:91],a[160:163],a[208:211]
s_add_u32 s87,s87,s66
ds_read_b64_tr_b16 v[74:75],v1 offset:25664
v_add_u32 v1,0xffff6000,v1
v_mfma_f32_16x16x32_bf16 a[208:211],v[92:95],a[176:179],a[208:211]
s_add_u32 s83,s83,s87
s_lshl_b32 s66,s63,6
s_add_u32 s87,s87,s66
v_mfma_f32_16x16x32_bf16 a[212:215],v[92:95],a[180:183],a[212:215]
s_add_u32 s88,s87,20480
s_add_u32 s89,s87,40960
s_add_u32 s90,s87,61440
v_mfma_f32_16x16x32_bf16 a[212:215],v[88:91],a[164:167],a[212:215]
s_add_u32 s66,s77,4096
v_mfma_f32_16x16x32_bf16 a[212:215],v[84:87],a[148:151],a[212:215]
v_mfma_f32_16x16x32_bf16 a[212:215],v[80:83],a[132:135],a[212:215]
v_mfma_f32_16x16x32_bf16 a[216:219],v[80:83],a[136:139],a[216:219]
v_mfma_f32_16x16x32_bf16 a[216:219],v[84:87],a[152:155],a[216:219]
v_mfma_f32_16x16x32_bf16 a[216:219],v[88:91],a[168:171],a[216:219]
v_mfma_f32_16x16x32_bf16 a[216:219],v[92:95],a[184:187],a[216:219]
v_mfma_f32_16x16x32_bf16 a[220:223],v[92:95],a[188:191],a[220:223]
ds_read_b64_tr_b16 v[92:93],v2 offset:30816
ds_read_b64_tr_b16 v[94:95],v2 offset:35936
v_mfma_f32_16x16x32_bf16 a[220:223],v[88:91],a[172:175],a[220:223]
ds_read_b64_tr_b16 v[88:89],v2 offset:20576
v_mfma_f32_16x16x32_bf16 a[220:223],v[84:87],a[156:159],a[220:223]
ds_read_b64_tr_b16 v[84:85],v2 offset:10336
v_mfma_f32_16x16x32_bf16 a[220:223],v[80:83],a[140:143],a[220:223]
s_waitcnt lgkmcnt(4)
ds_read_b64_tr_b16 v[80:81],v2 offset:96
v_mfma_f32_16x16x32_bf16 a[192:195],v[96:99],a[68:71],a[192:195]
s_waitcnt vmcnt(14)
ds_read_b64_tr_b16 v[82:83],v2 offset:5216
ds_read_b64_tr_b16 v[86:87],v2 offset:15456
v_mfma_f32_16x16x32_bf16 a[192:195],v[100:103],a[76:79],a[192:195]
s_waitcnt vmcnt(13)
ds_read_b64_tr_b16 v[90:91],v2 offset:25696
v_add_u32 v2,0xffff6000,v2
v_mfma_f32_16x16x32_bf16 a[192:195],v[104:107],a[84:87],a[192:195]
s_waitcnt vmcnt(12)
v_mfma_f32_16x16x32_bf16 a[192:195],v[108:111],a[92:95],a[192:195]
s_waitcnt vmcnt(11)
v_mfma_f32_16x16x32_bf16 a[196:199],v[108:111],a[124:127],a[196:199]
s_waitcnt vmcnt(10)
v_mfma_f32_16x16x32_bf16 a[196:199],v[104:107],a[116:119],a[196:199]
s_waitcnt vmcnt(9)
v_mfma_f32_16x16x32_bf16 a[196:199],v[100:103],a[108:111],a[196:199]
)A"
R"A(s_waitcnt vmcnt(8)
v_mfma_f32_16x16x32_bf16 a[196:199],v[96:99],a[100:103],a[196:199]
s_waitcnt vmcnt(7)
v_mfma_f32_16x16x32_bf16 a[200:203],v[96:99],a[64:67],a[200:203]
s_waitcnt vmcnt(6)
v_mfma_f32_16x16x32_bf16 a[200:203],v[100:103],a[72:75],a[200:203]
s_waitcnt vmcnt(5)
v_mfma_f32_16x16x32_bf16 a[200:203],v[104:107],a[80:83],a[200:203]
s_waitcnt vmcnt(4)
v_mfma_f32_16x16x32_bf16 a[200:203],v[108:111],a[88:91],a[200:203]
s_waitcnt vmcnt(3)
v_accvgpr_read_b32 v8,a192
v_accvgpr_read_b32 v9,a193
v_mfma_f32_16x16x32_bf16 a[204:207],v[108:111],a[120:123],a[204:207]
s_waitcnt vmcnt(2)
v_accvgpr_read_b32 v10,a194
v_accvgpr_read_b32 v11,a195
v_mfma_f32_16x16x32_bf16 a[204:207],v[104:107],a[112:115],a[204:207]
s_waitcnt vmcnt(1)
v_cvt_pk_bf16_f32 v8,v8,v9
v_cvt_pk_bf16_f32 v9,v10,v11
v_mfma_f32_16x16x32_bf16 a[204:207],v[100:103],a[104:107],a[204:207]
s_waitcnt vmcnt(0)
v_accvgpr_read_b32 v16,a196
v_accvgpr_read_b32 v17,a197
v_mfma_f32_16x16x32_bf16 a[204:207],v[96:99],a[96:99],a[204:207]
v_accvgpr_read_b32 v18,a198
v_accvgpr_read_b32 v19,a199
v_mfma_f32_16x16x32_bf16 a[208:211],v[112:115],a[68:71],a[208:211]
v_cvt_pk_bf16_f32 v16,v16,v17
v_cvt_pk_bf16_f32 v17,v18,v19
v_mfma_f32_16x16x32_bf16 a[208:211],v[116:119],a[76:79],a[208:211]
ds_read_b64_tr_b16 v[96:97],v1 offset:64
ds_read_b64_tr_b16 v[98:99],v1 offset:5184
v_mfma_f32_16x16x32_bf16 a[208:211],v[120:123],a[84:87],a[208:211]
ds_read_b64_tr_b16 v[100:101],v1 offset:10304
v_mfma_f32_16x16x32_bf16 a[208:211],v[124:127],a[92:95],a[208:211]
ds_read_b64_tr_b16 v[102:103],v1 offset:15424
v_mfma_f32_16x16x32_bf16 a[212:215],v[124:127],a[124:127],a[212:215]
ds_read_b64_tr_b16 v[104:105],v1 offset:20544
v_mfma_f32_16x16x32_bf16 a[212:215],v[120:123],a[116:119],a[212:215]
ds_read_b64_tr_b16 v[106:107],v1 offset:25664
ds_read_b64_tr_b16 v[108:109],v1 offset:30784
v_mfma_f32_16x16x32_bf16 a[212:215],v[116:119],a[108:111],a[212:215]
ds_read_b64_tr_b16 v[110:111],v1 offset:35904
v_add_u32 v1,0xa000,v1
v_mfma_f32_16x16x32_bf16 a[212:215],v[112:115],a[100:103],a[212:215]
v_accvgpr_read_b32 v12,a208
v_accvgpr_read_b32 v13,a209
v_mfma_f32_16x16x32_bf16 a[216:219],v[112:115],a[64:67],a[216:219]
v_accvgpr_read_b32 v14,a210
v_accvgpr_read_b32 v15,a211
v_mfma_f32_16x16x32_bf16 a[216:219],v[116:119],a[72:75],a[216:219]
v_cvt_pk_bf16_f32 v10,v12,v13
v_cvt_pk_bf16_f32 v11,v14,v15
v_mfma_f32_16x16x32_bf16 a[216:219],v[120:123],a[80:83],a[216:219]
buffer_store_dwordx4 v[8:11],v6,s[36:39],s74 offen sc1
v_mfma_f32_16x16x32_bf16 a[216:219],v[124:127],a[88:91],a[216:219]
v_accvgpr_read_b32 v20,a212
v_accvgpr_read_b32 v21,a213
v_mfma_f32_16x16x32_bf16 a[220:223],v[124:127],a[120:123],a[220:223]
v_accvgpr_read_b32 v22,a214
v_accvgpr_read_b32 v23,a215
v_mfma_f32_16x16x32_bf16 a[220:223],v[120:123],a[112:115],a[220:223]
v_cvt_pk_bf16_f32 v18,v20,v21
v_cvt_pk_bf16_f32 v19,v22,v23
v_mfma_f32_16x16x32_bf16 a[220:223],v[116:119],a[104:107],a[220:223]
v_accvgpr_read_b32 v8,a200
v_accvgpr_read_b32 v9,a201
v_mfma_f32_16x16x32_bf16 a[220:223],v[112:115],a[96:99],a[220:223]
v_accvgpr_read_b32 v10,a202
v_accvgpr_read_b32 v11,a203
v_mfma_f32_16x16x32_bf16 a[224:227],v[64:67],a[128:131],a[224:227]
v_accvgpr_read_b32 v12,a216
v_accvgpr_read_b32 v13,a217
v_mfma_f32_16x16x32_bf16 a[224:227],v[68:71],a[144:147],a[224:227]
v_accvgpr_read_b32 v14,a218
v_accvgpr_read_b32 v15,a219
v_mfma_f32_16x16x32_bf16 a[224:227],v[72:75],a[160:163],a[224:227]
v_cvt_pk_bf16_f32 v8,v8,v9
v_cvt_pk_bf16_f32 v9,v10,v11
v_mfma_f32_16x16x32_bf16 a[224:227],v[76:79],a[176:179],a[224:227]
v_cvt_pk_bf16_f32 v10,v12,v13
v_cvt_pk_bf16_f32 v11,v14,v15
v_mfma_f32_16x16x32_bf16 a[228:231],v[76:79],a[180:183],a[228:231]
ds_read_b64_tr_b16 v[112:113],v2 offset:96
ds_read_b64_tr_b16 v[114:115],v2 offset:5216
v_mfma_f32_16x16x32_bf16 a[228:231],v[72:75],a[164:167],a[228:231]
buffer_store_dwordx4 v[16:19],v6,s[36:39],s74 offen offset:1024 sc1
v_mfma_f32_16x16x32_bf16 a[228:231],v[68:71],a[148:151],a[228:231]
ds_read_b64_tr_b16 v[116:117],v2 offset:10336
ds_read_b64_tr_b16 v[118:119],v2 offset:15456
v_mfma_f32_16x16x32_bf16 a[228:231],v[64:67],a[132:135],a[228:231]
ds_read_b64_tr_b16 v[120:121],v2 offset:20576
v_accvgpr_read_b32 v16,a204
v_mfma_f32_16x16x32_bf16 a[232:235],v[64:67],a[136:139],a[232:235]
ds_read_b64_tr_b16 v[122:123],v2 offset:25696
v_accvgpr_read_b32 v17,a205
v_mfma_f32_16x16x32_bf16 a[232:235],v[68:71],a[152:155],a[232:235]
ds_read_b64_tr_b16 v[124:125],v2 offset:30816
v_accvgpr_read_b32 v18,a206
v_mfma_f32_16x16x32_bf16 a[232:235],v[72:75],a[168:171],a[232:235]
ds_read_b64_tr_b16 v[126:127],v2 offset:35936
v_accvgpr_read_b32 v19,a207
v_mfma_f32_16x16x32_bf16 a[232:235],v[76:79],a[184:187],a[232:235]
v_accvgpr_read_b32 v20,a220
v_accvgpr_read_b32 v21,a221
v_mfma_f32_16x16x32_bf16 a[236:239],v[76:79],a[188:191],a[236:239]
v_accvgpr_read_b32 v22,a222
v_accvgpr_read_b32 v23,a223
v_mfma_f32_16x16x32_bf16 a[236:239],v[72:75],a[172:175],a[236:239]
v_cvt_pk_bf16_f32 v16,v16,v17
v_cvt_pk_bf16_f32 v17,v18,v19
v_mfma_f32_16x16x32_bf16 a[236:239],v[68:71],a[156:159],a[236:239]
v_cvt_pk_bf16_f32 v18,v20,v21
v_cvt_pk_bf16_f32 v19,v22,v23
v_mfma_f32_16x16x32_bf16 a[236:239],v[64:67],a[140:143],a[236:239]
s_waitcnt lgkmcnt(3)
ds_read_b64_tr_b16 v[64:65],v1 offset:128
ds_read_b64_tr_b16 v[66:67],v1 offset:5248
v_mfma_f32_16x16x32_bf16 a[240:243],v[80:83],a[128:131],a[240:243]
buffer_store_dwordx4 v[8:11],v6,s[36:39],s74 offen offset:2048 sc1
v_mfma_f32_16x16x32_bf16 a[240:243],v[84:87],a[144:147],a[240:243]
ds_read_b64_tr_b16 v[68:69],v1 offset:10368
ds_read_b64_tr_b16 v[70:71],v1 offset:15488
v_mfma_f32_16x16x32_bf16 a[240:243],v[88:91],a[160:163],a[240:243]
ds_read_b64_tr_b16 v[72:73],v1 offset:20608
v_add_u32 v2,0xa000,v2
v_mfma_f32_16x16x32_bf16 a[240:243],v[92:95],a[176:179],a[240:243]
ds_read_b64_tr_b16 v[74:75],v1 offset:25728
v_mfma_f32_16x16x32_bf16 a[244:247],v[92:95],a[180:183],a[244:247]
ds_read_b64_tr_b16 v[76:77],v1 offset:30848
v_mfma_f32_16x16x32_bf16 a[244:247],v[88:91],a[164:167],a[244:247]
ds_read_b64_tr_b16 v[78:79],v1 offset:35968
v_add_u32 v1,0xffff6000,v1
v_mfma_f32_16x16x32_bf16 a[244:247],v[84:87],a[148:151],a[244:247]
v_mfma_f32_16x16x32_bf16 a[244:247],v[80:83],a[132:135],a[244:247]
v_mfma_f32_16x16x32_bf16 a[248:251],v[80:83],a[136:139],a[248:251]
v_mfma_f32_16x16x32_bf16 a[248:251],v[84:87],a[152:155],a[248:251]
v_mfma_f32_16x16x32_bf16 a[248:251],v[88:91],a[168:171],a[248:251]
v_mfma_f32_16x16x32_bf16 a[248:251],v[92:95],a[184:187],a[248:251]
buffer_store_dwordx4 v[16:19],v28,s[36:39],s74 offen offset:3072 sc1
v_mfma_f32_16x16x32_bf16 a[252:255],v[92:95],a[188:191],a[252:255]
ds_read_b64_tr_b16 v[92:93],v2 offset:30880
ds_read_b64_tr_b16 v[94:95],v2 offset:36000
v_mfma_f32_16x16x32_bf16 a[252:255],v[88:91],a[172:175],a[252:255]
ds_read_b64_tr_b16 v[88:89],v2 offset:20640
v_mfma_f32_16x16x32_bf16 a[252:255],v[84:87],a[156:159],a[252:255]
ds_read_b64_tr_b16 v[84:85],v2 offset:10400
v_mfma_f32_16x16x32_bf16 a[252:255],v[80:83],a[140:143],a[252:255]
ds_read_b64_tr_b16 v[80:81],v2 offset:160
v_mfma_f32_16x16x32_bf16 a[224:227],v[96:99],a[68:71],a[224:227]
ds_read_b64_tr_b16 v[82:83],v2 offset:5280
ds_read_b64_tr_b16 v[86:87],v2 offset:15520
v_mfma_f32_16x16x32_bf16 a[224:227],v[100:103],a[76:79],a[224:227]
ds_read_b64_tr_b16 v[90:91],v2 offset:25760
v_add_u32 v2,0xffff6000,v2
v_mfma_f32_16x16x32_bf16 a[224:227],v[104:107],a[84:87],a[224:227]
v_mfma_f32_16x16x32_bf16 a[224:227],v[108:111],a[92:95],a[224:227]
v_mfma_f32_16x16x32_bf16 a[228:231],v[108:111],a[124:127],a[228:231]
v_mfma_f32_16x16x32_bf16 a[228:231],v[104:107],a[116:119],a[228:231]
v_mfma_f32_16x16x32_bf16 a[228:231],v[100:103],a[108:111],a[228:231]
v_mfma_f32_16x16x32_bf16 a[228:231],v[96:99],a[100:103],a[228:231]
v_mfma_f32_16x16x32_bf16 a[232:235],v[96:99],a[64:67],a[232:235]
v_mfma_f32_16x16x32_bf16 a[232:235],v[100:103],a[72:75],a[232:235]
v_mfma_f32_16x16x32_bf16 a[232:235],v[104:107],a[80:83],a[232:235]
v_mfma_f32_16x16x32_bf16 a[232:235],v[108:111],a[88:91],a[232:235]
v_accvgpr_read_b32 v8,a224
v_accvgpr_read_b32 v9,a225
v_mfma_f32_16x16x32_bf16 a[236:239],v[108:111],a[120:123],a[236:239]
v_accvgpr_read_b32 v10,a226
v_accvgpr_read_b32 v11,a227
v_mfma_f32_16x16x32_bf16 a[236:239],v[104:107],a[112:115],a[236:239]
v_cvt_pk_bf16_f32 v8,v8,v9
v_cvt_pk_bf16_f32 v9,v10,v11
v_mfma_f32_16x16x32_bf16 a[236:239],v[100:103],a[104:107],a[236:239]
v_accvgpr_read_b32 v16,a228
v_accvgpr_read_b32 v17,a229
v_mfma_f32_16x16x32_bf16 a[236:239],v[96:99],a[96:99],a[236:239]
v_accvgpr_read_b32 v18,a230
v_accvgpr_read_b32 v19,a231
v_mfma_f32_16x16x32_bf16 a[240:243],v[112:115],a[68:71],a[240:243]
v_cvt_pk_bf16_f32 v16,v16,v17
v_cvt_pk_bf16_f32 v17,v18,v19
v_mfma_f32_16x16x32_bf16 a[240:243],v[116:119],a[76:79],a[240:243]
s_waitcnt lgkmcnt(0)
ds_read_b64_tr_b16 v[96:97],v1 offset:128
ds_read_b64_tr_b16 v[98:99],v1 offset:5248
v_mfma_f32_16x16x32_bf16 a[240:243],v[120:123],a[84:87],a[240:243]
ds_read_b64_tr_b16 v[100:101],v1 offset:10368
v_mfma_f32_16x16x32_bf16 a[240:243],v[124:127],a[92:95],a[240:243]
ds_read_b64_tr_b16 v[102:103],v1 offset:15488
v_mfma_f32_16x16x32_bf16 a[244:247],v[124:127],a[124:127],a[244:247]
ds_read_b64_tr_b16 v[104:105],v1 offset:20608
v_mfma_f32_16x16x32_bf16 a[244:247],v[120:123],a[116:119],a[244:247]
ds_read_b64_tr_b16 v[106:107],v1 offset:25728
ds_read_b64_tr_b16 v[108:109],v1 offset:30848
v_mfma_f32_16x16x32_bf16 a[244:247],v[116:119],a[108:111],a[244:247]
ds_read_b64_tr_b16 v[110:111],v1 offset:35968
v_add_u32 v1,0xa000,v1
v_mfma_f32_16x16x32_bf16 a[244:247],v[112:115],a[100:103],a[244:247]
v_accvgpr_read_b32 v12,a240
v_accvgpr_read_b32 v13,a241
v_mfma_f32_16x16x32_bf16 a[248:251],v[112:115],a[64:67],a[248:251]
v_accvgpr_read_b32 v14,a242
v_accvgpr_read_b32 v15,a243
v_mfma_f32_16x16x32_bf16 a[248:251],v[116:119],a[72:75],a[248:251]
v_cvt_pk_bf16_f32 v10,v12,v13
v_cvt_pk_bf16_f32 v11,v14,v15
v_mfma_f32_16x16x32_bf16 a[248:251],v[120:123],a[80:83],a[248:251]
buffer_store_dwordx4 v[8:11],v6,s[36:39],s75 offen sc1
v_mfma_f32_16x16x32_bf16 a[248:251],v[124:127],a[88:91],a[248:251]
v_accvgpr_read_b32 v20,a244
v_accvgpr_read_b32 v21,a245
v_mfma_f32_16x16x32_bf16 a[252:255],v[124:127],a[120:123],a[252:255]
v_accvgpr_read_b32 v22,a246
v_accvgpr_read_b32 v23,a247
v_mfma_f32_16x16x32_bf16 a[252:255],v[120:123],a[112:115],a[252:255]
v_cvt_pk_bf16_f32 v18,v20,v21
v_cvt_pk_bf16_f32 v19,v22,v23
v_mfma_f32_16x16x32_bf16 a[252:255],v[116:119],a[104:107],a[252:255]
v_accvgpr_read_b32 v8,a232
v_accvgpr_read_b32 v9,a233
v_mfma_f32_16x16x32_bf16 a[252:255],v[112:115],a[96:99],a[252:255]
v_accvgpr_read_b32 v10,a234
v_accvgpr_read_b32 v11,a235
v_mfma_f32_16x16x32_bf16 a[0:3],v[64:67],a[128:131],a[0:3]
v_accvgpr_read_b32 v12,a248
v_accvgpr_read_b32 v13,a249
v_mfma_f32_16x16x32_bf16 a[0:3],v[68:71],a[144:147],a[0:3]
v_accvgpr_read_b32 v14,a250
v_accvgpr_read_b32 v15,a251
v_mfma_f32_16x16x32_bf16 a[0:3],v[72:75],a[160:163],a[0:3]
v_cvt_pk_bf16_f32 v8,v8,v9
v_cvt_pk_bf16_f32 v9,v10,v11
v_mfma_f32_16x16x32_bf16 a[0:3],v[76:79],a[176:179],a[0:3]
v_cvt_pk_bf16_f32 v10,v12,v13
v_cvt_pk_bf16_f32 v11,v14,v15
v_mfma_f32_16x16x32_bf16 a[4:7],v[76:79],a[180:183],a[4:7]
ds_read_b64_tr_b16 v[112:113],v2 offset:160
ds_read_b64_tr_b16 v[114:115],v2 offset:5280
v_mfma_f32_16x16x32_bf16 a[4:7],v[72:75],a[164:167],a[4:7]
buffer_store_dwordx4 v[16:19],v6,s[36:39],s75 offen offset:1024 sc1
v_mfma_f32_16x16x32_bf16 a[4:7],v[68:71],a[148:151],a[4:7]
ds_read_b64_tr_b16 v[116:117],v2 offset:10400
ds_read_b64_tr_b16 v[118:119],v2 offset:15520
v_mfma_f32_16x16x32_bf16 a[4:7],v[64:67],a[132:135],a[4:7]
ds_read_b64_tr_b16 v[120:121],v2 offset:20640
v_accvgpr_read_b32 v16,a236
v_mfma_f32_16x16x32_bf16 a[8:11],v[64:67],a[136:139],a[8:11]
ds_read_b64_tr_b16 v[122:123],v2 offset:25760
v_accvgpr_read_b32 v17,a237
v_mfma_f32_16x16x32_bf16 a[8:11],v[68:71],a[152:155],a[8:11]
ds_read_b64_tr_b16 v[124:125],v2 offset:30880
v_accvgpr_read_b32 v18,a238
v_mfma_f32_16x16x32_bf16 a[8:11],v[72:75],a[168:171],a[8:11]
ds_read_b64_tr_b16 v[126:127],v2 offset:36000
v_accvgpr_read_b32 v19,a239
)A"
R"A(v_mfma_f32_16x16x32_bf16 a[8:11],v[76:79],a[184:187],a[8:11]
v_accvgpr_read_b32 v20,a252
v_accvgpr_read_b32 v21,a253
v_mfma_f32_16x16x32_bf16 a[12:15],v[76:79],a[188:191],a[12:15]
v_accvgpr_read_b32 v22,a254
v_accvgpr_read_b32 v23,a255
v_mfma_f32_16x16x32_bf16 a[12:15],v[72:75],a[172:175],a[12:15]
v_cvt_pk_bf16_f32 v16,v16,v17
v_cvt_pk_bf16_f32 v17,v18,v19
v_mfma_f32_16x16x32_bf16 a[12:15],v[68:71],a[156:159],a[12:15]
v_cvt_pk_bf16_f32 v18,v20,v21
v_cvt_pk_bf16_f32 v19,v22,v23
v_mfma_f32_16x16x32_bf16 a[12:15],v[64:67],a[140:143],a[12:15]
ds_read_b64_tr_b16 v[64:65],v1 offset:192
ds_read_b64_tr_b16 v[66:67],v1 offset:5312
v_mfma_f32_16x16x32_bf16 a[16:19],v[80:83],a[128:131],a[16:19]
buffer_store_dwordx4 v[8:11],v6,s[36:39],s75 offen offset:2048 sc1
v_mfma_f32_16x16x32_bf16 a[16:19],v[84:87],a[144:147],a[16:19]
ds_read_b64_tr_b16 v[68:69],v1 offset:10432
ds_read_b64_tr_b16 v[70:71],v1 offset:15552
v_mfma_f32_16x16x32_bf16 a[16:19],v[88:91],a[160:163],a[16:19]
ds_read_b64_tr_b16 v[72:73],v1 offset:20672
v_add_u32 v2,0xa000,v2
v_mfma_f32_16x16x32_bf16 a[16:19],v[92:95],a[176:179],a[16:19]
ds_read_b64_tr_b16 v[74:75],v1 offset:25792
v_mfma_f32_16x16x32_bf16 a[20:23],v[92:95],a[180:183],a[20:23]
ds_read_b64_tr_b16 v[76:77],v1 offset:30912
v_mfma_f32_16x16x32_bf16 a[20:23],v[88:91],a[164:167],a[20:23]
ds_read_b64_tr_b16 v[78:79],v1 offset:36032
v_add_u32 v1,0xffff6000,v1
v_mfma_f32_16x16x32_bf16 a[20:23],v[84:87],a[148:151],a[20:23]
v_mfma_f32_16x16x32_bf16 a[20:23],v[80:83],a[132:135],a[20:23]
v_mfma_f32_16x16x32_bf16 a[24:27],v[80:83],a[136:139],a[24:27]
v_mfma_f32_16x16x32_bf16 a[24:27],v[84:87],a[152:155],a[24:27]
v_mfma_f32_16x16x32_bf16 a[24:27],v[88:91],a[168:171],a[24:27]
v_mfma_f32_16x16x32_bf16 a[24:27],v[92:95],a[184:187],a[24:27]
buffer_store_dwordx4 v[16:19],v28,s[36:39],s75 offen offset:3072 sc1
v_mfma_f32_16x16x32_bf16 a[28:31],v[92:95],a[188:191],a[28:31]
ds_read_b64_tr_b16 v[92:93],v2 offset:30944
ds_read_b64_tr_b16 v[94:95],v2 offset:36064
v_mfma_f32_16x16x32_bf16 a[28:31],v[88:91],a[172:175],a[28:31]
ds_read_b64_tr_b16 v[88:89],v2 offset:20704
v_mfma_f32_16x16x32_bf16 a[28:31],v[84:87],a[156:159],a[28:31]
ds_read_b64_tr_b16 v[84:85],v2 offset:10464
v_mfma_f32_16x16x32_bf16 a[28:31],v[80:83],a[140:143],a[28:31]
s_waitcnt lgkmcnt(4)
ds_read_b64_tr_b16 v[80:81],v2 offset:224
v_mfma_f32_16x16x32_bf16 a[0:3],v[96:99],a[68:71],a[0:3]
ds_read_b64_tr_b16 v[82:83],v2 offset:5344
ds_read_b64_tr_b16 v[86:87],v2 offset:15584
v_mfma_f32_16x16x32_bf16 a[0:3],v[100:103],a[76:79],a[0:3]
ds_read_b64_tr_b16 v[90:91],v2 offset:25824
v_add_u32 v2,0xffff6000,v2
v_mfma_f32_16x16x32_bf16 a[0:3],v[104:107],a[84:87],a[0:3]
v_mfma_f32_16x16x32_bf16 a[0:3],v[108:111],a[92:95],a[0:3]
v_mfma_f32_16x16x32_bf16 a[4:7],v[108:111],a[124:127],a[4:7]
v_mfma_f32_16x16x32_bf16 a[4:7],v[104:107],a[116:119],a[4:7]
v_mfma_f32_16x16x32_bf16 a[4:7],v[100:103],a[108:111],a[4:7]
v_mfma_f32_16x16x32_bf16 a[4:7],v[96:99],a[100:103],a[4:7]
v_mfma_f32_16x16x32_bf16 a[8:11],v[96:99],a[64:67],a[8:11]
v_mfma_f32_16x16x32_bf16 a[8:11],v[100:103],a[72:75],a[8:11]
v_mfma_f32_16x16x32_bf16 a[8:11],v[104:107],a[80:83],a[8:11]
v_mfma_f32_16x16x32_bf16 a[8:11],v[108:111],a[88:91],a[8:11]
v_accvgpr_read_b32 v8,a0
v_accvgpr_read_b32 v9,a1
v_mfma_f32_16x16x32_bf16 a[12:15],v[108:111],a[120:123],a[12:15]
v_accvgpr_read_b32 v10,a2
v_accvgpr_read_b32 v11,a3
v_mfma_f32_16x16x32_bf16 a[12:15],v[104:107],a[112:115],a[12:15]
v_cvt_pk_bf16_f32 v8,v8,v9
v_cvt_pk_bf16_f32 v9,v10,v11
v_mfma_f32_16x16x32_bf16 a[12:15],v[100:103],a[104:107],a[12:15]
v_accvgpr_read_b32 v16,a4
v_accvgpr_read_b32 v17,a5
v_mfma_f32_16x16x32_bf16 a[12:15],v[96:99],a[96:99],a[12:15]
v_accvgpr_read_b32 v18,a6
v_accvgpr_read_b32 v19,a7
v_mfma_f32_16x16x32_bf16 a[16:19],v[112:115],a[68:71],a[16:19]
v_cvt_pk_bf16_f32 v16,v16,v17
v_cvt_pk_bf16_f32 v17,v18,v19
v_mfma_f32_16x16x32_bf16 a[16:19],v[116:119],a[76:79],a[16:19]
ds_read_b64_tr_b16 v[96:97],v1 offset:192
ds_read_b64_tr_b16 v[98:99],v1 offset:5312
v_mfma_f32_16x16x32_bf16 a[16:19],v[120:123],a[84:87],a[16:19]
ds_read_b64_tr_b16 v[100:101],v1 offset:10432
v_mfma_f32_16x16x32_bf16 a[16:19],v[124:127],a[92:95],a[16:19]
ds_read_b64_tr_b16 v[102:103],v1 offset:15552
v_mfma_f32_16x16x32_bf16 a[20:23],v[124:127],a[124:127],a[20:23]
ds_read_b64_tr_b16 v[104:105],v1 offset:20672
v_mfma_f32_16x16x32_bf16 a[20:23],v[120:123],a[116:119],a[20:23]
ds_read_b64_tr_b16 v[106:107],v1 offset:25792
ds_read_b64_tr_b16 v[108:109],v1 offset:30912
v_mfma_f32_16x16x32_bf16 a[20:23],v[116:119],a[108:111],a[20:23]
ds_read_b64_tr_b16 v[110:111],v1 offset:36032
v_add_u32 v1,0x14000,v1
v_mfma_f32_16x16x32_bf16 a[20:23],v[112:115],a[100:103],a[20:23]
v_accvgpr_read_b32 v12,a16
v_accvgpr_read_b32 v13,a17
v_mfma_f32_16x16x32_bf16 a[24:27],v[112:115],a[64:67],a[24:27]
v_accvgpr_read_b32 v14,a18
v_accvgpr_read_b32 v15,a19
v_mfma_f32_16x16x32_bf16 a[24:27],v[116:119],a[72:75],a[24:27]
v_cvt_pk_bf16_f32 v10,v12,v13
v_cvt_pk_bf16_f32 v11,v14,v15
s_nop 0
L_pd23_%=:
s_cbranch_execz L_trtg2S_%=
v_mfma_f32_16x16x32_bf16 a[24:27],v[120:123],a[80:83],a[24:27]
buffer_store_dwordx4 v[8:11],v6,s[36:39],s76 offen sc1
v_mfma_f32_16x16x32_bf16 a[24:27],v[124:127],a[88:91],a[24:27]
v_accvgpr_read_b32 v20,a20
v_accvgpr_read_b32 v21,a21
v_mfma_f32_16x16x32_bf16 a[28:31],v[124:127],a[120:123],a[28:31]
v_accvgpr_read_b32 v22,a22
v_accvgpr_read_b32 v23,a23
v_mfma_f32_16x16x32_bf16 a[28:31],v[120:123],a[112:115],a[28:31]
v_cvt_pk_bf16_f32 v18,v20,v21
v_cvt_pk_bf16_f32 v19,v22,v23
v_mfma_f32_16x16x32_bf16 a[28:31],v[116:119],a[104:107],a[28:31]
v_accvgpr_read_b32 v8,a8
v_accvgpr_read_b32 v9,a9
v_mfma_f32_16x16x32_bf16 a[28:31],v[112:115],a[96:99],a[28:31]
v_accvgpr_read_b32 v10,a10
v_accvgpr_read_b32 v11,a11
v_mfma_f32_16x16x32_bf16 v[128:131],v[64:67],a[128:131],v[128:131]
v_accvgpr_read_b32 v12,a24
v_accvgpr_read_b32 v13,a25
v_mfma_f32_16x16x32_bf16 v[128:131],v[68:71],a[144:147],v[128:131]
v_accvgpr_read_b32 v14,a26
v_accvgpr_read_b32 v15,a27
v_mfma_f32_16x16x32_bf16 v[128:131],v[72:75],a[160:163],v[128:131]
v_cvt_pk_bf16_f32 v8,v8,v9
v_cvt_pk_bf16_f32 v9,v10,v11
v_mfma_f32_16x16x32_bf16 v[128:131],v[76:79],a[176:179],v[128:131]
v_cvt_pk_bf16_f32 v10,v12,v13
v_cvt_pk_bf16_f32 v11,v14,v15
v_mfma_f32_16x16x32_bf16 v[136:139],v[76:79],a[180:183],v[136:139]
ds_read_b64_tr_b16 v[112:113],v2 offset:224
ds_read_b64_tr_b16 v[114:115],v2 offset:5344
v_mfma_f32_16x16x32_bf16 v[136:139],v[72:75],a[164:167],v[136:139]
buffer_store_dwordx4 v[16:19],v6,s[36:39],s76 offen offset:1024 sc1
v_mfma_f32_16x16x32_bf16 v[136:139],v[68:71],a[148:151],v[136:139]
ds_read_b64_tr_b16 v[116:117],v2 offset:10464
ds_read_b64_tr_b16 v[118:119],v2 offset:15584
v_mfma_f32_16x16x32_bf16 v[136:139],v[64:67],a[132:135],v[136:139]
ds_read_b64_tr_b16 v[120:121],v2 offset:20704
v_accvgpr_read_b32 v16,a12
v_mfma_f32_16x16x32_bf16 v[144:147],v[64:67],a[136:139],v[144:147]
ds_read_b64_tr_b16 v[122:123],v2 offset:25824
v_accvgpr_read_b32 v17,a13
v_mfma_f32_16x16x32_bf16 v[144:147],v[68:71],a[152:155],v[144:147]
ds_read_b64_tr_b16 v[124:125],v2 offset:30944
v_accvgpr_read_b32 v18,a14
v_mfma_f32_16x16x32_bf16 v[144:147],v[72:75],a[168:171],v[144:147]
ds_read_b64_tr_b16 v[126:127],v2 offset:36064
v_accvgpr_read_b32 v19,a15
v_mfma_f32_16x16x32_bf16 v[144:147],v[76:79],a[184:187],v[144:147]
v_accvgpr_read_b32 v20,a28
v_accvgpr_read_b32 v21,a29
v_mfma_f32_16x16x32_bf16 v[152:155],v[76:79],a[188:191],v[152:155]
v_accvgpr_read_b32 v22,a30
v_accvgpr_read_b32 v23,a31
v_mfma_f32_16x16x32_bf16 v[152:155],v[72:75],a[172:175],v[152:155]
v_cvt_pk_bf16_f32 v16,v16,v17
v_cvt_pk_bf16_f32 v17,v18,v19
v_mfma_f32_16x16x32_bf16 v[152:155],v[68:71],a[156:159],v[152:155]
v_cvt_pk_bf16_f32 v18,v20,v21
v_cvt_pk_bf16_f32 v19,v22,v23
v_mfma_f32_16x16x32_bf16 v[152:155],v[64:67],a[140:143],v[152:155]
s_waitcnt lgkmcnt(3)
ds_read_b64_tr_b16 v[64:65],v1 offset:256
ds_read_b64_tr_b16 v[66:67],v1 offset:5376
v_mfma_f32_16x16x32_bf16 v[160:163],v[80:83],a[128:131],v[160:163]
buffer_store_dwordx4 v[8:11],v6,s[36:39],s76 offen offset:2048 sc1
v_mfma_f32_16x16x32_bf16 v[160:163],v[84:87],a[144:147],v[160:163]
ds_read_b64_tr_b16 v[68:69],v1 offset:10496
ds_read_b64_tr_b16 v[70:71],v1 offset:15616
v_mfma_f32_16x16x32_bf16 v[160:163],v[88:91],a[160:163],v[160:163]
ds_read_b64_tr_b16 v[72:73],v1 offset:20736
v_add_u32 v2,0x14000,v2
v_mfma_f32_16x16x32_bf16 v[160:163],v[92:95],a[176:179],v[160:163]
ds_read_b64_tr_b16 v[74:75],v1 offset:25856
v_mov_b32 v20,s52
v_mfma_f32_16x16x32_bf16 v[168:171],v[92:95],a[180:183],v[168:171]
ds_read_b64_tr_b16 v[76:77],v1 offset:30976
v_mov_b32 v21,s53
v_mfma_f32_16x16x32_bf16 v[168:171],v[88:91],a[164:167],v[168:171]
ds_read_b64_tr_b16 v[78:79],v1 offset:36096
v_add_u32 v1,0xffff6000,v1
v_mfma_f32_16x16x32_bf16 v[168:171],v[84:87],a[148:151],v[168:171]
v_lshlrev_b32 v22,20,v6
v_mfma_f32_16x16x32_bf16 v[168:171],v[80:83],a[132:135],v[168:171]
v_mfma_f32_16x16x32_bf16 v[176:179],v[80:83],a[136:139],v[176:179]
v_mfma_f32_16x16x32_bf16 v[176:179],v[84:87],a[152:155],v[176:179]
v_mfma_f32_16x16x32_bf16 v[176:179],v[88:91],a[168:171],v[176:179]
v_mfma_f32_16x16x32_bf16 v[176:179],v[92:95],a[184:187],v[176:179]
buffer_store_dwordx4 v[16:19],v28,s[36:39],s76 offen offset:3072 sc1
v_mfma_f32_16x16x32_bf16 v[184:187],v[92:95],a[188:191],v[184:187]
ds_read_b64_tr_b16 v[92:93],v2 offset:31008
ds_read_b64_tr_b16 v[94:95],v2 offset:36128
v_mfma_f32_16x16x32_bf16 v[184:187],v[88:91],a[172:175],v[184:187]
ds_read_b64_tr_b16 v[88:89],v2 offset:20768
v_mfma_f32_16x16x32_bf16 v[184:187],v[84:87],a[156:159],v[184:187]
ds_read_b64_tr_b16 v[84:85],v2 offset:10528
v_mfma_f32_16x16x32_bf16 v[184:187],v[80:83],a[140:143],v[184:187]
ds_read_b64_tr_b16 v[80:81],v2 offset:288
v_mfma_f32_16x16x32_bf16 v[128:131],v[96:99],a[68:71],v[128:131]
ds_read_b64_tr_b16 v[82:83],v2 offset:5408
ds_read_b64_tr_b16 v[86:87],v2 offset:15648
v_mfma_f32_16x16x32_bf16 v[128:131],v[100:103],a[76:79],v[128:131]
ds_read_b64_tr_b16 v[90:91],v2 offset:25888
v_add_u32 v2,0xffff6000,v2
v_mfma_f32_16x16x32_bf16 v[128:131],v[104:107],a[84:87],v[128:131]
v_mfma_f32_16x16x32_bf16 v[128:131],v[108:111],a[92:95],v[128:131]
v_mfma_f32_16x16x32_bf16 v[136:139],v[108:111],a[124:127],v[136:139]
v_mfma_f32_16x16x32_bf16 v[136:139],v[104:107],a[116:119],v[136:139]
v_mfma_f32_16x16x32_bf16 v[136:139],v[100:103],a[108:111],v[136:139]
v_mfma_f32_16x16x32_bf16 v[136:139],v[96:99],a[100:103],v[136:139]
v_mfma_f32_16x16x32_bf16 v[144:147],v[96:99],a[64:67],v[144:147]
v_mfma_f32_16x16x32_bf16 v[144:147],v[100:103],a[72:75],v[144:147]
v_mfma_f32_16x16x32_bf16 v[144:147],v[104:107],a[80:83],v[144:147]
v_mfma_f32_16x16x32_bf16 v[144:147],v[108:111],a[88:91],v[144:147]
v_cvt_pk_bf16_f32 v128,v128,v129
v_cvt_pk_bf16_f32 v129,v130,v131
v_mfma_f32_16x16x32_bf16 v[152:155],v[108:111],a[120:123],v[152:155]
ds_read_b64_tr_b16 v[108:109],v1 offset:30976
ds_read_b64_tr_b16 v[110:111],v1 offset:36096
v_mfma_f32_16x16x32_bf16 v[152:155],v[104:107],a[112:115],v[152:155]
v_cvt_pk_bf16_f32 v136,v136,v137
v_cvt_pk_bf16_f32 v137,v138,v139
v_mfma_f32_16x16x32_bf16 v[152:155],v[100:103],a[104:107],v[152:155]
v_cvt_pk_bf16_f32 v144,v144,v145
v_cvt_pk_bf16_f32 v145,v146,v147
v_mfma_f32_16x16x32_bf16 v[152:155],v[96:99],a[96:99],v[152:155]
ds_read_b64_tr_b16 v[96:97],v1 offset:256
ds_read_b64_tr_b16 v[98:99],v1 offset:5376
v_mfma_f32_16x16x32_bf16 v[160:163],v[112:115],a[68:71],v[160:163]
ds_read_b64_tr_b16 v[100:101],v1 offset:10496
v_mfma_f32_16x16x32_bf16 v[160:163],v[116:119],a[76:79],v[160:163]
s_waitcnt lgkmcnt(5)
ds_read_b64_tr_b16 v[102:103],v1 offset:15616
v_mfma_f32_16x16x32_bf16 v[160:163],v[120:123],a[84:87],v[160:163]
ds_read_b64_tr_b16 v[104:105],v1 offset:20736
v_mfma_f32_16x16x32_bf16 v[160:163],v[124:127],a[92:95],v[160:163]
v_cvt_pk_bf16_f32 v152,v152,v153
v_cvt_pk_bf16_f32 v153,v154,v155
v_mfma_f32_16x16x32_bf16 v[168:171],v[124:127],a[124:127],v[168:171]
ds_read_b64_tr_b16 v[106:107],v1 offset:25856
v_add_u32 v1,0xffff6000,v1
v_mfma_f32_16x16x32_bf16 v[168:171],v[120:123],a[116:119],v[168:171]
)A"
R"A(v_mfma_f32_16x16x32_bf16 v[168:171],v[116:119],a[108:111],v[168:171]
v_mfma_f32_16x16x32_bf16 v[168:171],v[112:115],a[100:103],v[168:171]
v_cvt_pk_bf16_f32 v130,v160,v161
v_cvt_pk_bf16_f32 v131,v162,v163
v_mfma_f32_16x16x32_bf16 v[176:179],v[112:115],a[64:67],v[176:179]
buffer_store_dwordx4 v[128:131],v6,s[36:39],s79 offen sc1
v_mfma_f32_16x16x32_bf16 v[176:179],v[116:119],a[72:75],v[176:179]
v_mfma_f32_16x16x32_bf16 v[176:179],v[120:123],a[80:83],v[176:179]
v_mfma_f32_16x16x32_bf16 v[176:179],v[124:127],a[88:91],v[176:179]
v_mfma_f32_16x16x32_bf16 v[184:187],v[124:127],a[120:123],v[184:187]
v_cvt_pk_bf16_f32 v138,v168,v169
v_cvt_pk_bf16_f32 v139,v170,v171
v_mfma_f32_16x16x32_bf16 v[184:187],v[120:123],a[112:115],v[184:187]
buffer_store_dwordx4 v[136:139],v6,s[36:39],s80 offen sc1
v_mfma_f32_16x16x32_bf16 v[184:187],v[116:119],a[104:107],v[184:187]
ds_read_b64_tr_b16 v[116:117],v2 offset:10528
ds_read_b64_tr_b16 v[118:119],v2 offset:15648
v_mfma_f32_16x16x32_bf16 v[184:187],v[112:115],a[96:99],v[184:187]
v_cvt_pk_bf16_f32 v146,v176,v177
v_cvt_pk_bf16_f32 v147,v178,v179
s_waitcnt vmcnt(6)
s_barrier
v_mfma_f32_16x16x32_bf16 v[192:195],v[64:67],v[32:35],v[192:195]
buffer_store_dwordx4 v[144:147],v6,s[36:39],s81 offen sc1
v_mfma_f32_16x16x32_bf16 v[192:195],v[68:71],v[36:39],v[192:195]
ds_read_b64_tr_b16 v[112:113],v2 offset:288
ds_read_b64_tr_b16 v[114:115],v2 offset:5408
v_mfma_f32_16x16x32_bf16 v[192:195],v[72:75],v[40:43],v[192:195]
buffer_store_dwordx2 v[58:59],v60,s[44:47],s18 offen offset:2312 sc1
v_mfma_f32_16x16x32_bf16 v[192:195],v[76:79],v[44:47],v[192:195]
v_cvt_pk_bf16_f32 v154,v184,v185
v_cvt_pk_bf16_f32 v155,v186,v187
v_mfma_f32_16x16x32_bf16 v[200:203],v[76:79],v[180:183],v[200:203]
buffer_store_dwordx4 v[152:155],v28,s[36:39],s82 offen sc1
v_mfma_f32_16x16x32_bf16 v[200:203],v[72:75],v[164:167],v[200:203]
ds_read_b64_tr_b16 v[120:121],v2 offset:20768
ds_read_b64_tr_b16 v[122:123],v2 offset:25888
v_mfma_f32_16x16x32_bf16 v[200:203],v[68:71],v[148:151],v[200:203]
ds_read_b64_tr_b16 v[124:125],v2 offset:31008
v_mfma_f32_16x16x32_bf16 v[200:203],v[64:67],v[132:135],v[200:203]
ds_read_b64_tr_b16 v[126:127],v2 offset:36128
v_add_u32 v2,0xffff6000,v2
v_mfma_f32_16x16x32_bf16 v[208:211],v[64:67],v[196:199],v[208:211]
v_mfma_f32_16x16x32_bf16 v[208:211],v[68:71],v[212:215],v[208:211]
v_mfma_f32_16x16x32_bf16 v[208:211],v[72:75],v[228:231],v[208:211]
v_mfma_f32_16x16x32_bf16 v[208:211],v[76:79],v[244:247],v[208:211]
v_mfma_f32_16x16x32_bf16 v[216:219],v[76:79],v[188:191],v[216:219]
ds_read_b64_tr_b16 v[76:77],v1 offset:30976
ds_read_b64_tr_b16 v[78:79],v1 offset:36096
v_mfma_f32_16x16x32_bf16 v[216:219],v[72:75],v[172:175],v[216:219]
ds_read_b64_tr_b16 v[72:73],v1 offset:20736
v_mfma_f32_16x16x32_bf16 v[216:219],v[68:71],v[156:159],v[216:219]
ds_read_b64_tr_b16 v[68:69],v1 offset:10496
v_mfma_f32_16x16x32_bf16 v[216:219],v[64:67],v[140:143],v[216:219]
ds_read_b64_tr_b16 v[64:65],v1 offset:256
v_mfma_f32_16x16x32_bf16 v[224:227],v[80:83],v[32:35],v[224:227]
ds_read_b64_tr_b16 v[66:67],v1 offset:5376
ds_read_b64_tr_b16 v[70:71],v1 offset:15616
v_mfma_f32_16x16x32_bf16 v[224:227],v[84:87],v[36:39],v[224:227]
ds_read_b64_tr_b16 v[74:75],v1 offset:25856
v_mfma_f32_16x16x32_bf16 v[224:227],v[88:91],v[40:43],v[224:227]
v_mfma_f32_16x16x32_bf16 v[224:227],v[92:95],v[44:47],v[224:227]
v_mfma_f32_16x16x32_bf16 v[232:235],v[92:95],v[180:183],v[232:235]
v_mfma_f32_16x16x32_bf16 v[232:235],v[88:91],v[164:167],v[232:235]
v_mfma_f32_16x16x32_bf16 v[232:235],v[84:87],v[148:151],v[232:235]
v_mfma_f32_16x16x32_bf16 v[232:235],v[80:83],v[132:135],v[232:235]
v_mfma_f32_16x16x32_bf16 v[240:243],v[80:83],v[196:199],v[240:243]
v_mfma_f32_16x16x32_bf16 v[240:243],v[84:87],v[212:215],v[240:243]
v_mfma_f32_16x16x32_bf16 v[240:243],v[88:91],v[228:231],v[240:243]
v_mfma_f32_16x16x32_bf16 v[240:243],v[92:95],v[244:247],v[240:243]
v_mfma_f32_16x16x32_bf16 v[248:251],v[92:95],v[188:191],v[248:251]
ds_read_b64_tr_b16 v[92:93],v2 offset:31008
ds_read_b64_tr_b16 v[94:95],v2 offset:36128
v_mfma_f32_16x16x32_bf16 v[248:251],v[88:91],v[172:175],v[248:251]
ds_read_b64_tr_b16 v[88:89],v2 offset:20768
v_mfma_f32_16x16x32_bf16 v[248:251],v[84:87],v[156:159],v[248:251]
ds_read_b64_tr_b16 v[84:85],v2 offset:10528
v_mfma_f32_16x16x32_bf16 v[248:251],v[80:83],v[140:143],v[248:251]
s_waitcnt lgkmcnt(4)
ds_read_b64_tr_b16 v[80:81],v2 offset:288
v_mfma_f32_16x16x32_bf16 v[192:195],v[96:99],a[128:131],v[192:195]
ds_read_b64_tr_b16 v[82:83],v2 offset:5408
ds_read_b64_tr_b16 v[86:87],v2 offset:15648
v_mfma_f32_16x16x32_bf16 v[192:195],v[100:103],a[144:147],v[192:195]
ds_read_b64_tr_b16 v[90:91],v2 offset:25888
v_mfma_f32_16x16x32_bf16 v[192:195],v[104:107],a[160:163],v[192:195]
v_mfma_f32_16x16x32_bf16 v[192:195],v[108:111],a[176:179],v[192:195]
v_mfma_f32_16x16x32_bf16 v[200:203],v[108:111],a[180:183],v[200:203]
v_mfma_f32_16x16x32_bf16 v[200:203],v[104:107],a[164:167],v[200:203]
v_mfma_f32_16x16x32_bf16 v[200:203],v[100:103],a[148:151],v[200:203]
v_mfma_f32_16x16x32_bf16 v[200:203],v[96:99],a[132:135],v[200:203]
v_mfma_f32_16x16x32_bf16 v[208:211],v[96:99],a[136:139],v[208:211]
v_mfma_f32_16x16x32_bf16 v[208:211],v[100:103],a[152:155],v[208:211]
v_mfma_f32_16x16x32_bf16 v[208:211],v[104:107],a[168:171],v[208:211]
v_mfma_f32_16x16x32_bf16 v[208:211],v[108:111],a[184:187],v[208:211]
v_mfma_f32_16x16x32_bf16 v[216:219],v[108:111],a[188:191],v[216:219]
v_mfma_f32_16x16x32_bf16 v[216:219],v[104:107],a[172:175],v[216:219]
v_mfma_f32_16x16x32_bf16 v[216:219],v[100:103],a[156:159],v[216:219]
v_mfma_f32_16x16x32_bf16 v[216:219],v[96:99],a[140:143],v[216:219]
v_mfma_f32_16x16x32_bf16 v[224:227],v[112:115],a[128:131],v[224:227]
v_mfma_f32_16x16x32_bf16 v[224:227],v[116:119],a[144:147],v[224:227]
v_mfma_f32_16x16x32_bf16 v[224:227],v[120:123],a[160:163],v[224:227]
v_mfma_f32_16x16x32_bf16 v[224:227],v[124:127],a[176:179],v[224:227]
v_mfma_f32_16x16x32_bf16 v[232:235],v[124:127],a[180:183],v[232:235]
v_mfma_f32_16x16x32_bf16 v[232:235],v[120:123],a[164:167],v[232:235]
v_mfma_f32_16x16x32_bf16 v[232:235],v[116:119],a[148:151],v[232:235]
v_mfma_f32_16x16x32_bf16 v[232:235],v[112:115],a[132:135],v[232:235]
v_mfma_f32_16x16x32_bf16 v[240:243],v[112:115],a[136:139],v[240:243]
v_mfma_f32_16x16x32_bf16 v[240:243],v[116:119],a[152:155],v[240:243]
v_mfma_f32_16x16x32_bf16 v[240:243],v[120:123],a[168:171],v[240:243]
v_mfma_f32_16x16x32_bf16 v[240:243],v[124:127],a[184:187],v[240:243]
v_mfma_f32_16x16x32_bf16 v[248:251],v[124:127],a[188:191],v[248:251]
v_mfma_f32_16x16x32_bf16 v[248:251],v[120:123],a[172:175],v[248:251]
v_mfma_f32_16x16x32_bf16 v[248:251],v[116:119],a[156:159],v[248:251]
v_mfma_f32_16x16x32_bf16 v[248:251],v[112:115],a[140:143],v[248:251]
v_mfma_f32_16x16x32_bf16 v[192:195],v[64:67],a[68:71],v[192:195]
v_mfma_f32_16x16x32_bf16 v[192:195],v[68:71],a[76:79],v[192:195]
v_mfma_f32_16x16x32_bf16 v[192:195],v[72:75],a[84:87],v[192:195]
v_mfma_f32_16x16x32_bf16 v[192:195],v[76:79],a[92:95],v[192:195]
v_mfma_f32_16x16x32_bf16 v[200:203],v[76:79],a[124:127],v[200:203]
v_mfma_f32_16x16x32_bf16 v[200:203],v[72:75],a[116:119],v[200:203]
v_mfma_f32_16x16x32_bf16 v[200:203],v[68:71],a[108:111],v[200:203]
v_mfma_f32_16x16x32_bf16 v[200:203],v[64:67],a[100:103],v[200:203]
v_mfma_f32_16x16x32_bf16 v[208:211],v[64:67],a[64:67],v[208:211]
v_mfma_f32_16x16x32_bf16 v[208:211],v[68:71],a[72:75],v[208:211]
v_mfma_f32_16x16x32_bf16 v[208:211],v[72:75],a[80:83],v[208:211]
v_mfma_f32_16x16x32_bf16 v[208:211],v[76:79],a[88:91],v[208:211]
v_mfma_f32_16x16x32_bf16 v[216:219],v[76:79],a[120:123],v[216:219]
v_mfma_f32_16x16x32_bf16 v[216:219],v[72:75],a[112:115],v[216:219]
v_mfma_f32_16x16x32_bf16 v[216:219],v[68:71],a[104:107],v[216:219]
v_mfma_f32_16x16x32_bf16 v[216:219],v[64:67],a[96:99],v[216:219]
s_waitcnt lgkmcnt(0)
v_mfma_f32_16x16x32_bf16 v[224:227],v[80:83],a[68:71],v[224:227]
buffer_load_dwordx2 v[62:63],v61,s[44:47],s91 offen offset:2312 sc1
v_mfma_f32_16x16x32_bf16 v[224:227],v[84:87],a[76:79],v[224:227]
v_mfma_f32_16x16x32_bf16 v[224:227],v[88:91],a[84:87],v[224:227]
v_mfma_f32_16x16x32_bf16 v[224:227],v[92:95],a[92:95],v[224:227]
v_mfma_f32_16x16x32_bf16 v[232:235],v[92:95],a[124:127],v[232:235]
v_mfma_f32_16x16x32_bf16 v[232:235],v[88:91],a[116:119],v[232:235]
v_mfma_f32_16x16x32_bf16 v[232:235],v[84:87],a[108:111],v[232:235]
v_mfma_f32_16x16x32_bf16 v[232:235],v[80:83],a[100:103],v[232:235]
v_mfma_f32_16x16x32_bf16 v[240:243],v[80:83],a[64:67],v[240:243]
v_mfma_f32_16x16x32_bf16 v[240:243],v[84:87],a[72:75],v[240:243]
v_mfma_f32_16x16x32_bf16 v[240:243],v[88:91],a[80:83],v[240:243]
v_mfma_f32_16x16x32_bf16 v[240:243],v[92:95],a[88:91],v[240:243]
v_mfma_f32_16x16x32_bf16 v[248:251],v[92:95],a[120:123],v[248:251]
v_mfma_f32_16x16x32_bf16 v[248:251],v[88:91],a[112:115],v[248:251]
v_mfma_f32_16x16x32_bf16 v[248:251],v[84:87],a[104:107],v[248:251]
v_mfma_f32_16x16x32_bf16 v[248:251],v[80:83],a[96:99],v[248:251]
s_waitcnt vmcnt(1)
s_barrier
buffer_store_dwordx2 v[20:21],v22,s[44:47],s18 offen offset:2304 sc1
s_waitcnt vmcnt(1)
v_cmp_ne_u32 s[56:57],s52,v62
v_cmp_ne_u32 s[58:59],s53,v63
s_nop 0
s_or_b64 s[56:57],s[56:57],s[58:59]
s_cbranch_scc1 L_fr8_%=
L_fo8_%=:
buffer_load_dwordx4 v[64:67],v6,s[36:39],s66 offen sc1
buffer_load_dwordx4 v[68:71],v6,s[36:39],s67 offen sc1
buffer_load_dwordx4 v[72:75],v6,s[36:39],s66 offen offset:1024 sc1
buffer_load_dwordx4 v[76:79],v6,s[36:39],s67 offen offset:1024 sc1
buffer_load_dwordx4 v[80:83],v6,s[36:39],s66 offen offset:2048 sc1
buffer_load_dwordx4 v[84:87],v6,s[36:39],s67 offen offset:2048 sc1
buffer_load_dwordx4 v[88:91],v28,s[36:39],s66 offen offset:3072 sc1
buffer_load_dwordx4 v[92:95],v28,s[36:39],s67 offen offset:3072 sc1
buffer_load_dwordx2 v[4:5],v3,s[44:47],s91 offen offset:2304 sc1
s_waitcnt vmcnt(8)
v_mfma_f32_16x16x32_bf16 v[192:195],v[48:51],v[64:67],v[192:195]
s_waitcnt vmcnt(7)
v_mfma_f32_16x16x32_bf16 v[192:195],v[48:51],v[68:71],v[192:195]
v_mfma_f32_16x16x32_bf16 v[224:227],v[52:55],v[64:67],v[224:227]
v_mfma_f32_16x16x32_bf16 v[224:227],v[52:55],v[68:71],v[224:227]
s_waitcnt vmcnt(6)
v_mfma_f32_16x16x32_bf16 v[200:203],v[48:51],v[72:75],v[200:203]
s_waitcnt vmcnt(5)
v_mfma_f32_16x16x32_bf16 v[200:203],v[48:51],v[76:79],v[200:203]
v_mfma_f32_16x16x32_bf16 v[232:235],v[52:55],v[72:75],v[232:235]
v_mfma_f32_16x16x32_bf16 v[232:235],v[52:55],v[76:79],v[232:235]
s_waitcnt vmcnt(4)
v_mfma_f32_16x16x32_bf16 v[208:211],v[48:51],v[80:83],v[208:211]
s_waitcnt vmcnt(3)
v_mfma_f32_16x16x32_bf16 v[208:211],v[48:51],v[84:87],v[208:211]
v_mfma_f32_16x16x32_bf16 v[240:243],v[52:55],v[80:83],v[240:243]
v_mfma_f32_16x16x32_bf16 v[240:243],v[52:55],v[84:87],v[240:243]
s_waitcnt vmcnt(2)
v_mfma_f32_16x16x32_bf16 v[216:219],v[48:51],v[88:91],v[216:219]
s_waitcnt vmcnt(1)
v_mfma_f32_16x16x32_bf16 v[216:219],v[48:51],v[92:95],v[216:219]
v_mfma_f32_16x16x32_bf16 v[248:251],v[52:55],v[88:91],v[248:251]
v_mfma_f32_16x16x32_bf16 v[248:251],v[52:55],v[92:95],v[248:251]
s_waitcnt vmcnt(0)
v_cmp_ne_u32 s[56:57],s52,v4
v_cmp_ne_u32 s[58:59],s53,v5
s_nop 0
s_or_b64 s[56:57],s[56:57],s[58:59]
s_cbranch_scc1 L_fr9_%=
L_fo9_%=:
buffer_load_dwordx4 v[96:99],v6,s[36:39],s68 offen sc1
buffer_load_dwordx4 v[100:103],v6,s[36:39],s68 offen offset:1024 sc1
buffer_load_dwordx4 v[104:107],v6,s[36:39],s68 offen offset:2048 sc1
buffer_load_dwordx4 v[108:111],v6,s[36:39],s68 offen offset:3072 sc1
buffer_load_dwordx4 v[112:115],v6,s[36:39],s65 offen sc1
buffer_load_dwordx4 v[116:119],v6,s[36:39],s65 offen offset:1024 sc1
buffer_load_dwordx4 v[120:123],v6,s[36:39],s65 offen offset:2048 sc1
buffer_load_dwordx4 v[124:127],v28,s[36:39],s65 offen offset:3072 sc1
s_waitcnt vmcnt(7)
v_mfma_f32_16x16x32_bf16 v[128:131],v[48:51],v[96:99],0
s_waitcnt vmcnt(6)
v_mfma_f32_16x16x32_bf16 v[128:131],v[48:51],v[100:103],v[128:131]
s_waitcnt vmcnt(5)
v_mfma_f32_16x16x32_bf16 v[128:131],v[48:51],v[104:107],v[128:131]
s_waitcnt vmcnt(4)
v_mfma_f32_16x16x32_bf16 v[128:131],v[48:51],v[108:111],v[128:131]
v_mfma_f32_16x16x32_bf16 v[160:163],v[52:55],v[96:99],0
)A"
R"A(v_mfma_f32_16x16x32_bf16 v[160:163],v[52:55],v[100:103],v[160:163]
v_mfma_f32_16x16x32_bf16 v[160:163],v[52:55],v[104:107],v[160:163]
v_mfma_f32_16x16x32_bf16 v[160:163],v[52:55],v[108:111],v[160:163]
s_waitcnt vmcnt(3)
v_mfma_f32_16x16x32_bf16 v[192:195],v[48:51],v[112:115],v[192:195]
v_mfma_f32_16x16x32_bf16 v[224:227],v[52:55],v[112:115],v[224:227]
s_waitcnt vmcnt(2)
v_mfma_f32_16x16x32_bf16 v[200:203],v[48:51],v[116:119],v[200:203]
v_mfma_f32_16x16x32_bf16 v[232:235],v[52:55],v[116:119],v[232:235]
s_waitcnt vmcnt(1)
v_cvt_pk_bf16_f32 v128,v128,v129
v_cvt_pk_bf16_f32 v129,v130,v131
v_mfma_f32_16x16x32_bf16 v[208:211],v[48:51],v[120:123],v[208:211]
v_mfma_f32_16x16x32_bf16 v[240:243],v[52:55],v[120:123],v[240:243]
s_waitcnt vmcnt(0)
v_cvt_pk_bf16_f32 v130,v160,v161
v_cvt_pk_bf16_f32 v131,v162,v163
v_mfma_f32_16x16x32_bf16 v[216:219],v[48:51],v[124:127],v[216:219]
v_cvt_pk_bf16_f32 v192,v192,v193
v_cvt_pk_bf16_f32 v193,v194,v195
v_cvt_pk_bf16_f32 v194,v224,v225
v_cvt_pk_bf16_f32 v195,v226,v227
v_cvt_pk_bf16_f32 v200,v200,v201
v_cvt_pk_bf16_f32 v201,v202,v203
v_mfma_f32_16x16x32_bf16 v[248:251],v[52:55],v[124:127],v[248:251]
v_permlane16_swap_b32 v128,v130
v_permlane16_swap_b32 v129,v131
v_cvt_pk_bf16_f32 v202,v232,v233
v_cvt_pk_bf16_f32 v203,v234,v235
v_cvt_pk_bf16_f32 v208,v208,v209
v_cvt_pk_bf16_f32 v209,v210,v211
v_cvt_pk_bf16_f32 v210,v240,v241
v_cvt_pk_bf16_f32 v211,v242,v243
buffer_store_dwordx4 v[128:131],v56,s[24:27],s83 offen offset:256 sc1 nt
v_permlane16_swap_b32 v192,v194
v_permlane16_swap_b32 v193,v195
buffer_store_dwordx4 v[192:195],v56,s[24:27],s87 offen sc1 nt
v_permlane16_swap_b32 v200,v202
v_permlane16_swap_b32 v201,v203
v_cvt_pk_bf16_f32 v216,v216,v217
v_cvt_pk_bf16_f32 v217,v218,v219
v_cvt_pk_bf16_f32 v218,v248,v249
v_cvt_pk_bf16_f32 v219,v250,v251
buffer_store_dwordx4 v[200:203],v56,s[24:27],s88 offen sc1 nt
v_permlane16_swap_b32 v208,v210
v_permlane16_swap_b32 v209,v211
buffer_store_dwordx4 v[208:211],v56,s[24:27],s89 offen sc1 nt
v_permlane16_swap_b32 v216,v218
v_permlane16_swap_b32 v217,v219
buffer_store_dwordx4 v[216:219],v56,s[24:27],s90 offen sc1 nt
s_lshl_b32 s69,s62,20
s_add_u32 s69,s69,s18
buffer_store_dword v20,v22,s[44:47],s69 offen sc1
L_geS_%=:
s_branch L_end_%=
L_hot_%=:
L_fr1_%=:
s_mov_b32 s19,65536
L_fl1_%=:
s_sleep 6
buffer_load_dwordx2 v[4:5],v3,s[44:47],s91 offen offset:256 sc1
s_waitcnt vmcnt(0)
s_sub_u32 s19,s19,1
s_cmp_eq_u32 s19,0
s_cbranch_scc1 L_fo1_%=
v_cmp_ne_u32 s[56:57],s52,v4
v_cmp_ne_u32 s[58:59],s53,v5
s_nop 1
s_or_b64 s[56:57],s[56:57],s[58:59]
s_cbranch_scc1 L_fl1_%=
s_branch L_fo1_%=
L_fr2_%=:
s_mov_b32 s19,65536
L_fl2_%=:
s_sleep 6
buffer_load_dwordx2 v[4:5],v3,s[44:47],s91 offen offset:1280 sc1
s_waitcnt vmcnt(0)
s_sub_u32 s19,s19,1
s_cmp_eq_u32 s19,0
s_cbranch_scc1 L_fo2_%=
v_cmp_ne_u32 s[56:57],s52,v4
v_cmp_ne_u32 s[58:59],s53,v5
s_nop 1
s_or_b64 s[56:57],s[56:57],s[58:59]
s_cbranch_scc1 L_fl2_%=
s_branch L_fo2_%=
L_fr3_%=:
s_mov_b32 s19,65536
L_fl3_%=:
s_sleep 6
buffer_load_dwordx2 v[4:5],v3,s[44:47],s91 offen offset:1280 sc1
s_waitcnt vmcnt(0)
s_sub_u32 s19,s19,1
s_cmp_eq_u32 s19,0
s_cbranch_scc1 L_fo3_%=
v_cmp_ne_u32 s[56:57],s52,v4
v_cmp_ne_u32 s[58:59],s53,v5
s_nop 1
s_or_b64 s[56:57],s[56:57],s[58:59]
s_cbranch_scc1 L_fl3_%=
s_branch L_fo3_%=
L_fr4_%=:
s_mov_b32 s19,65536
L_fl4_%=:
s_sleep 6
buffer_load_dwordx2 v[62:63],v61,s[44:47],s91 offen offset:2312 sc1
s_waitcnt vmcnt(0)
s_sub_u32 s19,s19,1
s_cmp_eq_u32 s19,0
s_cbranch_scc1 L_fo4_%=
v_cmp_ne_u32 s[56:57],s52,v62
v_cmp_ne_u32 s[58:59],s53,v63
s_nop 1
s_or_b64 s[56:57],s[56:57],s[58:59]
s_cbranch_scc1 L_fl4_%=
s_branch L_fo4_%=
L_fr5_%=:
s_mov_b32 s19,65536
L_fl5_%=:
s_sleep 6
buffer_load_dwordx2 v[4:5],v3,s[44:47],s91 offen offset:2304 sc1
s_waitcnt vmcnt(0)
s_sub_u32 s19,s19,1
s_cmp_eq_u32 s19,0
s_cbranch_scc1 L_fo5_%=
v_cmp_ne_u32 s[56:57],s52,v4
v_cmp_ne_u32 s[58:59],s53,v5
s_nop 1
s_or_b64 s[56:57],s[56:57],s[58:59]
s_cbranch_scc1 L_fl5_%=
s_branch L_fo5_%=
L_fr6_%=:
s_mov_b32 s19,65536
L_fl6_%=:
s_sleep 6
buffer_load_dwordx2 v[4:5],v3,s[44:47],s91 offen offset:256 sc1
s_waitcnt vmcnt(0)
s_sub_u32 s19,s19,1
s_cmp_eq_u32 s19,0
s_cbranch_scc1 L_fo6_%=
v_cmp_ne_u32 s[56:57],s52,v4
v_cmp_ne_u32 s[58:59],s53,v5
s_nop 1
s_or_b64 s[56:57],s[56:57],s[58:59]
s_cbranch_scc1 L_fl6_%=
s_branch L_fo6_%=
L_fr7_%=:
s_mov_b32 s19,65536
L_fl7_%=:
s_sleep 6
buffer_load_dwordx2 v[4:5],v3,s[44:47],s91 offen offset:1280 sc1
s_waitcnt vmcnt(0)
s_sub_u32 s19,s19,1
s_cmp_eq_u32 s19,0
s_cbranch_scc1 L_fo7_%=
v_cmp_ne_u32 s[56:57],s52,v4
v_cmp_ne_u32 s[58:59],s53,v5
s_nop 1
s_or_b64 s[56:57],s[56:57],s[58:59]
s_cbranch_scc1 L_fl7_%=
s_branch L_fo7_%=
L_fr8_%=:
s_mov_b32 s19,65536
L_fl8_%=:
s_sleep 6
buffer_load_dwordx2 v[62:63],v61,s[44:47],s91 offen offset:2312 sc1
s_waitcnt vmcnt(0)
s_sub_u32 s19,s19,1
s_cmp_eq_u32 s19,0
s_cbranch_scc1 L_fo8_%=
v_cmp_ne_u32 s[56:57],s52,v62
v_cmp_ne_u32 s[58:59],s53,v63
s_nop 1
s_or_b64 s[56:57],s[56:57],s[58:59]
s_cbranch_scc1 L_fl8_%=
s_branch L_fo8_%=
L_fr9_%=:
s_mov_b32 s19,65536
L_fl9_%=:
s_sleep 6
buffer_load_dwordx2 v[4:5],v3,s[44:47],s91 offen offset:2304 sc1
s_waitcnt vmcnt(0)
s_sub_u32 s19,s19,1
s_cmp_eq_u32 s19,0
s_cbranch_scc1 L_fo9_%=
v_cmp_ne_u32 s[56:57],s52,v4
v_cmp_ne_u32 s[58:59],s53,v5
s_nop 1
s_or_b64 s[56:57],s[56:57],s[58:59]
s_cbranch_scc1 L_fl9_%=
s_branch L_fo9_%=
L_slow0_%=:
s_waitcnt vmcnt(0) lgkmcnt(0)
s_nop 7
v_accvgpr_write_b32 a160,v32
v_accvgpr_write_b32 a161,v33
v_accvgpr_write_b32 a162,v34
v_accvgpr_write_b32 a163,v35
v_accvgpr_write_b32 a164,v36
v_accvgpr_write_b32 a165,v37
v_accvgpr_write_b32 a166,v38
v_accvgpr_write_b32 a167,v39
v_accvgpr_write_b32 a168,v40
v_accvgpr_write_b32 a169,v41
v_accvgpr_write_b32 a170,v42
v_accvgpr_write_b32 a171,v43
v_accvgpr_write_b32 a172,v44
v_accvgpr_write_b32 a173,v45
v_accvgpr_write_b32 a174,v46
v_accvgpr_write_b32 a175,v47
s_waitcnt vmcnt(0) lgkmcnt(0)
s_nop 7
v_bfe_u32 v11,v6,4,4
v_lshrrev_b32 v12,8,v6
v_lshlrev_b32 v8,8,v11
v_lshl_add_u32 v8,v12,4,v8
v_lshlrev_b32 v9,10,v12
v_lshl_add_u32 v9,v11,1,v9
v_add_u32 v10,4096,v9
v_mov_b32 v48,0xff800000
v_mov_b32 v52,0xff800000
v_mov_b32 v56,0xff800000
v_mov_b32 v60,0xff800000
s_mov_b32 s65,0
s_mov_b32 s66,64
L_s10_%=:
buffer_load_dwordx4 a[128:131],v8,s[20:23],s65 offen
buffer_load_dwordx4 a[132:135],v8,s[20:23],s65 offen offset:64
buffer_load_dwordx4 a[136:139],v8,s[20:23],s65 offen offset:128
buffer_load_dwordx4 a[140:143],v8,s[20:23],s65 offen offset:192
s_waitcnt vmcnt(0)
v_mfma_f32_16x16x32_bf16 v[64:67],a[128:131],v[132:135],0
v_mfma_f32_16x16x32_bf16 v[64:67],a[132:135],v[140:143],v[64:67]
v_mfma_f32_16x16x32_bf16 v[64:67],a[136:139],v[148:151],v[64:67]
v_mfma_f32_16x16x32_bf16 v[64:67],a[140:143],v[156:159],v[64:67]
v_mfma_f32_16x16x32_bf16 v[80:83],a[128:131],v[164:167],0
v_mfma_f32_16x16x32_bf16 v[80:83],a[132:135],v[172:175],v[80:83]
v_mfma_f32_16x16x32_bf16 v[80:83],a[136:139],v[180:183],v[80:83]
v_mfma_f32_16x16x32_bf16 v[80:83],a[140:143],v[188:191],v[80:83]
v_mfma_f32_16x16x32_bf16 v[96:99],a[128:131],v[196:199],0
v_mfma_f32_16x16x32_bf16 v[96:99],a[132:135],v[204:207],v[96:99]
v_mfma_f32_16x16x32_bf16 v[96:99],a[136:139],v[212:215],v[96:99]
v_mfma_f32_16x16x32_bf16 v[96:99],a[140:143],v[220:223],v[96:99]
v_mfma_f32_16x16x32_bf16 v[112:115],a[128:131],v[228:231],0
v_mfma_f32_16x16x32_bf16 v[112:115],a[132:135],v[236:239],v[112:115]
v_mfma_f32_16x16x32_bf16 v[112:115],a[136:139],v[244:247],v[112:115]
v_mfma_f32_16x16x32_bf16 v[112:115],a[140:143],v[252:255],v[112:115]
s_nop 7
v_max3_f32 v48,v48,v64,v65
v_max3_f32 v48,v48,v66,v67
v_max3_f32 v52,v52,v80,v81
v_max3_f32 v52,v52,v82,v83
v_max3_f32 v56,v56,v96,v97
v_max3_f32 v56,v56,v98,v99
v_max3_f32 v60,v60,v112,v113
v_max3_f32 v60,v60,v114,v115
s_add_u32 s65,s65,4096
s_sub_u32 s66,s66,1
s_cmp_lg_u32 s66,0
s_cbranch_scc1 L_s10_%=
v_mov_b32 v11,v48
s_nop 1
v_permlane32_swap_b32 v48,v11
v_max_f32 v48,v48,v11
v_mov_b32 v11,v48
s_nop 1
v_permlane16_swap_b32 v48,v11
v_max_f32 v48,v48,v11
v_sub_f32 v32,0,v48
v_sub_f32 v33,0,v48
v_sub_f32 v34,0,v48
v_sub_f32 v35,0,v48
v_mov_b32 v11,v52
s_nop 1
v_permlane32_swap_b32 v52,v11
v_max_f32 v52,v52,v11
v_mov_b32 v11,v52
s_nop 1
v_permlane16_swap_b32 v52,v11
v_max_f32 v52,v52,v11
v_sub_f32 v36,0,v52
v_sub_f32 v37,0,v52
v_sub_f32 v38,0,v52
v_sub_f32 v39,0,v52
v_mov_b32 v11,v56
s_nop 1
v_permlane32_swap_b32 v56,v11
v_max_f32 v56,v56,v11
v_mov_b32 v11,v56
s_nop 1
v_permlane16_swap_b32 v56,v11
v_max_f32 v56,v56,v11
v_sub_f32 v40,0,v56
v_sub_f32 v41,0,v56
v_sub_f32 v42,0,v56
v_sub_f32 v43,0,v56
v_mov_b32 v11,v60
s_nop 1
v_permlane32_swap_b32 v60,v11
v_max_f32 v60,v60,v11
v_mov_b32 v11,v60
s_nop 1
v_permlane16_swap_b32 v60,v11
v_max_f32 v60,v60,v11
v_max_f32 v60,s72,v60
v_sub_f32 v44,0,v60
v_sub_f32 v45,0,v60
v_sub_f32 v46,0,v60
v_sub_f32 v47,0,v60
v_mov_b32 v48,0
v_mov_b32 v49,0
v_mov_b32 v50,0
v_mov_b32 v51,0
v_mov_b32 v52,0
v_mov_b32 v53,0
v_mov_b32 v54,0
v_mov_b32 v55,0
v_mov_b32 v56,0
v_mov_b32 v57,0
v_mov_b32 v58,0
v_mov_b32 v59,0
v_mov_b32 v60,0
v_mov_b32 v61,0
v_mov_b32 v62,0
v_mov_b32 v63,0
v_accvgpr_write_b32 a0,0
v_accvgpr_write_b32 a1,0
v_accvgpr_write_b32 a2,0
v_accvgpr_write_b32 a3,0
v_accvgpr_write_b32 a4,0
v_accvgpr_write_b32 a5,0
v_accvgpr_write_b32 a6,0
v_accvgpr_write_b32 a7,0
v_accvgpr_write_b32 a8,0
v_accvgpr_write_b32 a9,0
v_accvgpr_write_b32 a10,0
v_accvgpr_write_b32 a11,0
v_accvgpr_write_b32 a12,0
v_accvgpr_write_b32 a13,0
v_accvgpr_write_b32 a14,0
v_accvgpr_write_b32 a15,0
v_accvgpr_write_b32 a16,0
v_accvgpr_write_b32 a17,0
v_accvgpr_write_b32 a18,0
v_accvgpr_write_b32 a19,0
v_accvgpr_write_b32 a20,0
v_accvgpr_write_b32 a21,0
v_accvgpr_write_b32 a22,0
v_accvgpr_write_b32 a23,0
v_accvgpr_write_b32 a24,0
v_accvgpr_write_b32 a25,0
v_accvgpr_write_b32 a26,0
v_accvgpr_write_b32 a27,0
v_accvgpr_write_b32 a28,0
v_accvgpr_write_b32 a29,0
v_accvgpr_write_b32 a30,0
v_accvgpr_write_b32 a31,0
v_accvgpr_write_b32 a32,0
v_accvgpr_write_b32 a33,0
v_accvgpr_write_b32 a34,0
v_accvgpr_write_b32 a35,0
v_accvgpr_write_b32 a36,0
v_accvgpr_write_b32 a37,0
v_accvgpr_write_b32 a38,0
v_accvgpr_write_b32 a39,0
v_accvgpr_write_b32 a40,0
v_accvgpr_write_b32 a41,0
v_accvgpr_write_b32 a42,0
v_accvgpr_write_b32 a43,0
v_accvgpr_write_b32 a44,0
v_accvgpr_write_b32 a45,0
v_accvgpr_write_b32 a46,0
v_accvgpr_write_b32 a47,0
v_accvgpr_write_b32 a48,0
v_accvgpr_write_b32 a49,0
v_accvgpr_write_b32 a50,0
v_accvgpr_write_b32 a51,0
v_accvgpr_write_b32 a52,0
v_accvgpr_write_b32 a53,0
v_accvgpr_write_b32 a54,0
v_accvgpr_write_b32 a55,0
v_accvgpr_write_b32 a56,0
v_accvgpr_write_b32 a57,0
v_accvgpr_write_b32 a58,0
v_accvgpr_write_b32 a59,0
v_accvgpr_write_b32 a60,0
v_accvgpr_write_b32 a61,0
v_accvgpr_write_b32 a62,0
v_accvgpr_write_b32 a63,0
v_accvgpr_write_b32 a64,0
v_accvgpr_write_b32 a65,0
v_accvgpr_write_b32 a66,0
v_accvgpr_write_b32 a67,0
v_accvgpr_write_b32 a68,0
v_accvgpr_write_b32 a69,0
v_accvgpr_write_b32 a70,0
v_accvgpr_write_b32 a71,0
v_accvgpr_write_b32 a72,0
v_accvgpr_write_b32 a73,0
v_accvgpr_write_b32 a74,0
v_accvgpr_write_b32 a75,0
v_accvgpr_write_b32 a76,0
v_accvgpr_write_b32 a77,0
v_accvgpr_write_b32 a78,0
v_accvgpr_write_b32 a79,0
v_accvgpr_write_b32 a80,0
v_accvgpr_write_b32 a81,0
v_accvgpr_write_b32 a82,0
v_accvgpr_write_b32 a83,0
v_accvgpr_write_b32 a84,0
v_accvgpr_write_b32 a85,0
v_accvgpr_write_b32 a86,0
v_accvgpr_write_b32 a87,0
v_accvgpr_write_b32 a88,0
v_accvgpr_write_b32 a89,0
v_accvgpr_write_b32 a90,0
v_accvgpr_write_b32 a91,0
v_accvgpr_write_b32 a92,0
v_accvgpr_write_b32 a93,0
v_accvgpr_write_b32 a94,0
v_accvgpr_write_b32 a95,0
v_accvgpr_write_b32 a96,0
v_accvgpr_write_b32 a97,0
v_accvgpr_write_b32 a98,0
v_accvgpr_write_b32 a99,0
v_accvgpr_write_b32 a100,0
v_accvgpr_write_b32 a101,0
v_accvgpr_write_b32 a102,0
v_accvgpr_write_b32 a103,0
v_accvgpr_write_b32 a104,0
v_accvgpr_write_b32 a105,0
v_accvgpr_write_b32 a106,0
v_accvgpr_write_b32 a107,0
v_accvgpr_write_b32 a108,0
v_accvgpr_write_b32 a109,0
v_accvgpr_write_b32 a110,0
v_accvgpr_write_b32 a111,0
v_accvgpr_write_b32 a112,0
v_accvgpr_write_b32 a113,0
v_accvgpr_write_b32 a114,0
)A"
R"A(v_accvgpr_write_b32 a115,0
v_accvgpr_write_b32 a116,0
v_accvgpr_write_b32 a117,0
v_accvgpr_write_b32 a118,0
v_accvgpr_write_b32 a119,0
v_accvgpr_write_b32 a120,0
v_accvgpr_write_b32 a121,0
v_accvgpr_write_b32 a122,0
v_accvgpr_write_b32 a123,0
v_accvgpr_write_b32 a124,0
v_accvgpr_write_b32 a125,0
v_accvgpr_write_b32 a126,0
v_accvgpr_write_b32 a127,0
s_mov_b32 s65,0
s_mov_b32 s66,32
L_s20_%=:
s_add_u32 s67,s65,4096
buffer_load_dwordx4 a[128:131],v8,s[20:23],s65 offen
buffer_load_dwordx4 a[132:135],v8,s[20:23],s65 offen offset:64
buffer_load_dwordx4 a[136:139],v8,s[20:23],s65 offen offset:128
buffer_load_dwordx4 a[140:143],v8,s[20:23],s65 offen offset:192
buffer_load_dwordx4 a[144:147],v8,s[20:23],s67 offen
buffer_load_dwordx4 a[148:151],v8,s[20:23],s67 offen offset:64
buffer_load_dwordx4 a[152:155],v8,s[20:23],s67 offen offset:128
buffer_load_dwordx4 a[156:159],v8,s[20:23],s67 offen offset:192
s_waitcnt vmcnt(0)
v_mfma_f32_16x16x32_bf16 v[64:67],a[128:131],v[132:135],v[32:35]
v_mfma_f32_16x16x32_bf16 v[64:67],a[132:135],v[140:143],v[64:67]
v_mfma_f32_16x16x32_bf16 v[64:67],a[136:139],v[148:151],v[64:67]
v_mfma_f32_16x16x32_bf16 v[64:67],a[140:143],v[156:159],v[64:67]
v_mfma_f32_16x16x32_bf16 v[68:71],a[144:147],v[132:135],v[32:35]
v_mfma_f32_16x16x32_bf16 v[68:71],a[148:151],v[140:143],v[68:71]
v_mfma_f32_16x16x32_bf16 v[68:71],a[152:155],v[148:151],v[68:71]
v_mfma_f32_16x16x32_bf16 v[68:71],a[156:159],v[156:159],v[68:71]
v_mfma_f32_16x16x32_bf16 v[80:83],a[128:131],v[164:167],v[36:39]
v_mfma_f32_16x16x32_bf16 v[80:83],a[132:135],v[172:175],v[80:83]
v_mfma_f32_16x16x32_bf16 v[80:83],a[136:139],v[180:183],v[80:83]
v_mfma_f32_16x16x32_bf16 v[80:83],a[140:143],v[188:191],v[80:83]
v_mfma_f32_16x16x32_bf16 v[84:87],a[144:147],v[164:167],v[36:39]
v_mfma_f32_16x16x32_bf16 v[84:87],a[148:151],v[172:175],v[84:87]
v_mfma_f32_16x16x32_bf16 v[84:87],a[152:155],v[180:183],v[84:87]
v_mfma_f32_16x16x32_bf16 v[84:87],a[156:159],v[188:191],v[84:87]
v_mfma_f32_16x16x32_bf16 v[96:99],a[128:131],v[196:199],v[40:43]
v_mfma_f32_16x16x32_bf16 v[96:99],a[132:135],v[204:207],v[96:99]
v_mfma_f32_16x16x32_bf16 v[96:99],a[136:139],v[212:215],v[96:99]
v_mfma_f32_16x16x32_bf16 v[96:99],a[140:143],v[220:223],v[96:99]
v_mfma_f32_16x16x32_bf16 v[100:103],a[144:147],v[196:199],v[40:43]
v_mfma_f32_16x16x32_bf16 v[100:103],a[148:151],v[204:207],v[100:103]
v_mfma_f32_16x16x32_bf16 v[100:103],a[152:155],v[212:215],v[100:103]
v_mfma_f32_16x16x32_bf16 v[100:103],a[156:159],v[220:223],v[100:103]
v_mfma_f32_16x16x32_bf16 v[112:115],a[128:131],v[228:231],v[44:47]
v_mfma_f32_16x16x32_bf16 v[112:115],a[132:135],v[236:239],v[112:115]
v_mfma_f32_16x16x32_bf16 v[112:115],a[136:139],v[244:247],v[112:115]
v_mfma_f32_16x16x32_bf16 v[112:115],a[140:143],v[252:255],v[112:115]
v_mfma_f32_16x16x32_bf16 v[116:119],a[144:147],v[228:231],v[44:47]
v_mfma_f32_16x16x32_bf16 v[116:119],a[148:151],v[236:239],v[116:119]
v_mfma_f32_16x16x32_bf16 v[116:119],a[152:155],v[244:247],v[116:119]
v_mfma_f32_16x16x32_bf16 v[116:119],a[156:159],v[252:255],v[116:119]
s_nop 7
v_exp_f32 v64,v64
v_exp_f32 v65,v65
v_exp_f32 v66,v66
v_exp_f32 v67,v67
v_exp_f32 v68,v68
v_exp_f32 v69,v69
v_exp_f32 v70,v70
v_exp_f32 v71,v71
s_nop 0
v_cvt_pk_bf16_f32 v64,v64,v65
v_cvt_pk_bf16_f32 v65,v66,v67
v_cvt_pk_bf16_f32 v66,v68,v69
v_cvt_pk_bf16_f32 v67,v70,v71
v_exp_f32 v80,v80
v_exp_f32 v81,v81
v_exp_f32 v82,v82
v_exp_f32 v83,v83
v_exp_f32 v84,v84
v_exp_f32 v85,v85
v_exp_f32 v86,v86
v_exp_f32 v87,v87
s_nop 0
v_cvt_pk_bf16_f32 v80,v80,v81
v_cvt_pk_bf16_f32 v81,v82,v83
v_cvt_pk_bf16_f32 v82,v84,v85
v_cvt_pk_bf16_f32 v83,v86,v87
v_exp_f32 v96,v96
v_exp_f32 v97,v97
v_exp_f32 v98,v98
v_exp_f32 v99,v99
v_exp_f32 v100,v100
v_exp_f32 v101,v101
v_exp_f32 v102,v102
v_exp_f32 v103,v103
s_nop 0
v_cvt_pk_bf16_f32 v96,v96,v97
v_cvt_pk_bf16_f32 v97,v98,v99
v_cvt_pk_bf16_f32 v98,v100,v101
v_cvt_pk_bf16_f32 v99,v102,v103
v_exp_f32 v112,v112
v_exp_f32 v113,v113
v_exp_f32 v114,v114
v_exp_f32 v115,v115
v_exp_f32 v116,v116
v_exp_f32 v117,v117
v_exp_f32 v118,v118
v_exp_f32 v119,v119
s_nop 0
v_cvt_pk_bf16_f32 v112,v112,v113
v_cvt_pk_bf16_f32 v113,v114,v115
v_cvt_pk_bf16_f32 v114,v116,v117
v_cvt_pk_bf16_f32 v115,v118,v119
buffer_load_ushort v72,v9,s[24:27],s65 offen
buffer_load_ushort v73,v9,s[24:27],s65 offen offset:256
buffer_load_ushort v74,v9,s[24:27],s65 offen offset:512
buffer_load_ushort v75,v9,s[24:27],s65 offen offset:768
buffer_load_ushort v76,v10,s[24:27],s65 offen
buffer_load_ushort v77,v10,s[24:27],s65 offen offset:256
buffer_load_ushort v78,v10,s[24:27],s65 offen offset:512
buffer_load_ushort v79,v10,s[24:27],s65 offen offset:768
s_waitcnt vmcnt(0)
v_lshl_or_b32 v88,v73,16,v72
v_lshl_or_b32 v89,v75,16,v74
v_lshl_or_b32 v90,v77,16,v76
v_lshl_or_b32 v91,v79,16,v78
s_nop 1
v_mfma_f32_16x16x32_bf16 a[0:3],v[88:91],v[64:67],a[0:3]
v_mfma_f32_16x16x32_bf16 a[32:35],v[88:91],v[80:83],a[32:35]
v_mfma_f32_16x16x32_bf16 a[64:67],v[88:91],v[96:99],a[64:67]
v_mfma_f32_16x16x32_bf16 a[96:99],v[88:91],v[112:115],a[96:99]
buffer_load_ushort v72,v9,s[24:27],s65 offen offset:32
buffer_load_ushort v73,v9,s[24:27],s65 offen offset:288
buffer_load_ushort v74,v9,s[24:27],s65 offen offset:544
buffer_load_ushort v75,v9,s[24:27],s65 offen offset:800
buffer_load_ushort v76,v10,s[24:27],s65 offen offset:32
buffer_load_ushort v77,v10,s[24:27],s65 offen offset:288
buffer_load_ushort v78,v10,s[24:27],s65 offen offset:544
buffer_load_ushort v79,v10,s[24:27],s65 offen offset:800
s_waitcnt vmcnt(0)
v_lshl_or_b32 v88,v73,16,v72
v_lshl_or_b32 v89,v75,16,v74
v_lshl_or_b32 v90,v77,16,v76
v_lshl_or_b32 v91,v79,16,v78
s_nop 1
v_mfma_f32_16x16x32_bf16 a[4:7],v[88:91],v[64:67],a[4:7]
v_mfma_f32_16x16x32_bf16 a[36:39],v[88:91],v[80:83],a[36:39]
v_mfma_f32_16x16x32_bf16 a[68:71],v[88:91],v[96:99],a[68:71]
v_mfma_f32_16x16x32_bf16 a[100:103],v[88:91],v[112:115],a[100:103]
buffer_load_ushort v72,v9,s[24:27],s65 offen offset:64
buffer_load_ushort v73,v9,s[24:27],s65 offen offset:320
buffer_load_ushort v74,v9,s[24:27],s65 offen offset:576
buffer_load_ushort v75,v9,s[24:27],s65 offen offset:832
buffer_load_ushort v76,v10,s[24:27],s65 offen offset:64
buffer_load_ushort v77,v10,s[24:27],s65 offen offset:320
buffer_load_ushort v78,v10,s[24:27],s65 offen offset:576
buffer_load_ushort v79,v10,s[24:27],s65 offen offset:832
s_waitcnt vmcnt(0)
v_lshl_or_b32 v88,v73,16,v72
v_lshl_or_b32 v89,v75,16,v74
v_lshl_or_b32 v90,v77,16,v76
v_lshl_or_b32 v91,v79,16,v78
s_nop 1
v_mfma_f32_16x16x32_bf16 a[8:11],v[88:91],v[64:67],a[8:11]
v_mfma_f32_16x16x32_bf16 a[40:43],v[88:91],v[80:83],a[40:43]
v_mfma_f32_16x16x32_bf16 a[72:75],v[88:91],v[96:99],a[72:75]
v_mfma_f32_16x16x32_bf16 a[104:107],v[88:91],v[112:115],a[104:107]
buffer_load_ushort v72,v9,s[24:27],s65 offen offset:96
buffer_load_ushort v73,v9,s[24:27],s65 offen offset:352
buffer_load_ushort v74,v9,s[24:27],s65 offen offset:608
buffer_load_ushort v75,v9,s[24:27],s65 offen offset:864
buffer_load_ushort v76,v10,s[24:27],s65 offen offset:96
buffer_load_ushort v77,v10,s[24:27],s65 offen offset:352
buffer_load_ushort v78,v10,s[24:27],s65 offen offset:608
buffer_load_ushort v79,v10,s[24:27],s65 offen offset:864
s_waitcnt vmcnt(0)
v_lshl_or_b32 v88,v73,16,v72
v_lshl_or_b32 v89,v75,16,v74
v_lshl_or_b32 v90,v77,16,v76
v_lshl_or_b32 v91,v79,16,v78
s_nop 1
v_mfma_f32_16x16x32_bf16 a[12:15],v[88:91],v[64:67],a[12:15]
v_mfma_f32_16x16x32_bf16 a[44:47],v[88:91],v[80:83],a[44:47]
v_mfma_f32_16x16x32_bf16 a[76:79],v[88:91],v[96:99],a[76:79]
v_mfma_f32_16x16x32_bf16 a[108:111],v[88:91],v[112:115],a[108:111]
buffer_load_ushort v72,v9,s[24:27],s65 offen offset:128
buffer_load_ushort v73,v9,s[24:27],s65 offen offset:384
buffer_load_ushort v74,v9,s[24:27],s65 offen offset:640
buffer_load_ushort v75,v9,s[24:27],s65 offen offset:896
buffer_load_ushort v76,v10,s[24:27],s65 offen offset:128
buffer_load_ushort v77,v10,s[24:27],s65 offen offset:384
buffer_load_ushort v78,v10,s[24:27],s65 offen offset:640
buffer_load_ushort v79,v10,s[24:27],s65 offen offset:896
s_waitcnt vmcnt(0)
v_lshl_or_b32 v88,v73,16,v72
v_lshl_or_b32 v89,v75,16,v74
v_lshl_or_b32 v90,v77,16,v76
v_lshl_or_b32 v91,v79,16,v78
s_nop 1
v_mfma_f32_16x16x32_bf16 a[16:19],v[88:91],v[64:67],a[16:19]
v_mfma_f32_16x16x32_bf16 a[48:51],v[88:91],v[80:83],a[48:51]
v_mfma_f32_16x16x32_bf16 a[80:83],v[88:91],v[96:99],a[80:83]
v_mfma_f32_16x16x32_bf16 a[112:115],v[88:91],v[112:115],a[112:115]
buffer_load_ushort v72,v9,s[24:27],s65 offen offset:160
buffer_load_ushort v73,v9,s[24:27],s65 offen offset:416
buffer_load_ushort v74,v9,s[24:27],s65 offen offset:672
buffer_load_ushort v75,v9,s[24:27],s65 offen offset:928
buffer_load_ushort v76,v10,s[24:27],s65 offen offset:160
buffer_load_ushort v77,v10,s[24:27],s65 offen offset:416
buffer_load_ushort v78,v10,s[24:27],s65 offen offset:672
buffer_load_ushort v79,v10,s[24:27],s65 offen offset:928
s_waitcnt vmcnt(0)
v_lshl_or_b32 v88,v73,16,v72
v_lshl_or_b32 v89,v75,16,v74
v_lshl_or_b32 v90,v77,16,v76
v_lshl_or_b32 v91,v79,16,v78
s_nop 1
v_mfma_f32_16x16x32_bf16 a[20:23],v[88:91],v[64:67],a[20:23]
v_mfma_f32_16x16x32_bf16 a[52:55],v[88:91],v[80:83],a[52:55]
v_mfma_f32_16x16x32_bf16 a[84:87],v[88:91],v[96:99],a[84:87]
v_mfma_f32_16x16x32_bf16 a[116:119],v[88:91],v[112:115],a[116:119]
buffer_load_ushort v72,v9,s[24:27],s65 offen offset:192
buffer_load_ushort v73,v9,s[24:27],s65 offen offset:448
buffer_load_ushort v74,v9,s[24:27],s65 offen offset:704
buffer_load_ushort v75,v9,s[24:27],s65 offen offset:960
buffer_load_ushort v76,v10,s[24:27],s65 offen offset:192
buffer_load_ushort v77,v10,s[24:27],s65 offen offset:448
buffer_load_ushort v78,v10,s[24:27],s65 offen offset:704
buffer_load_ushort v79,v10,s[24:27],s65 offen offset:960
s_waitcnt vmcnt(0)
v_lshl_or_b32 v88,v73,16,v72
v_lshl_or_b32 v89,v75,16,v74
v_lshl_or_b32 v90,v77,16,v76
v_lshl_or_b32 v91,v79,16,v78
s_nop 1
v_mfma_f32_16x16x32_bf16 a[24:27],v[88:91],v[64:67],a[24:27]
v_mfma_f32_16x16x32_bf16 a[56:59],v[88:91],v[80:83],a[56:59]
v_mfma_f32_16x16x32_bf16 a[88:91],v[88:91],v[96:99],a[88:91]
v_mfma_f32_16x16x32_bf16 a[120:123],v[88:91],v[112:115],a[120:123]
buffer_load_ushort v72,v9,s[24:27],s65 offen offset:224
buffer_load_ushort v73,v9,s[24:27],s65 offen offset:480
buffer_load_ushort v74,v9,s[24:27],s65 offen offset:736
buffer_load_ushort v75,v9,s[24:27],s65 offen offset:992
buffer_load_ushort v76,v10,s[24:27],s65 offen offset:224
buffer_load_ushort v77,v10,s[24:27],s65 offen offset:480
buffer_load_ushort v78,v10,s[24:27],s65 offen offset:736
buffer_load_ushort v79,v10,s[24:27],s65 offen offset:992
s_waitcnt vmcnt(0)
v_lshl_or_b32 v88,v73,16,v72
v_lshl_or_b32 v89,v75,16,v74
v_lshl_or_b32 v90,v77,16,v76
v_lshl_or_b32 v91,v79,16,v78
s_nop 1
v_mfma_f32_16x16x32_bf16 a[28:31],v[88:91],v[64:67],a[28:31]
v_mfma_f32_16x16x32_bf16 a[60:63],v[88:91],v[80:83],a[60:63]
v_mfma_f32_16x16x32_bf16 a[92:95],v[88:91],v[96:99],a[92:95]
v_mfma_f32_16x16x32_bf16 a[124:127],v[88:91],v[112:115],a[124:127]
v_mfma_f32_16x16x32_bf16 v[48:51],v[28:31],v[64:67],v[48:51]
v_mfma_f32_16x16x32_bf16 v[52:55],v[28:31],v[80:83],v[52:55]
v_mfma_f32_16x16x32_bf16 v[56:59],v[28:31],v[96:99],v[56:59]
v_mfma_f32_16x16x32_bf16 v[60:63],v[28:31],v[112:115],v[60:63]
s_add_u32 s65,s65,8192
s_sub_u32 s66,s66,1
s_cmp_lg_u32 s66,0
s_cbranch_scc1 L_s20_%=
s_nop 7
v_max_f32 v24,0x00800000,v48
v_rcp_f32 v24,v24
v_max_f32 v25,0x00800000,v52
v_rcp_f32 v25,v25
v_max_f32 v26,0x00800000,v56
v_rcp_f32 v26,v26
v_max_f32 v27,0x00800000,v60
v_rcp_f32 v27,v27
v_accvgpr_read_b32 v8,a0
v_accvgpr_read_b32 v9,a1
v_accvgpr_read_b32 v10,a2
v_accvgpr_read_b32 v11,a3
v_accvgpr_read_b32 v12,a4
v_accvgpr_read_b32 v13,a5
v_accvgpr_read_b32 v14,a6
v_accvgpr_read_b32 v15,a7
v_mul_f32 v8,v8,v24
v_mul_f32 v9,v9,v24
v_mul_f32 v10,v10,v24
v_mul_f32 v11,v11,v24
v_mul_f32 v12,v12,v24
v_mul_f32 v13,v13,v24
v_mul_f32 v14,v14,v24
v_mul_f32 v15,v15,v24
v_cvt_pk_bf16_f32 v8,v8,v9
v_cvt_pk_bf16_f32 v9,v10,v11
v_cvt_pk_bf16_f32 v10,v12,v13
v_cvt_pk_bf16_f32 v11,v14,v15
ds_write_b128 v7,v[8:11] offset:6144
v_accvgpr_read_b32 v16,a8
v_accvgpr_read_b32 v17,a9
)A"
R"A(v_accvgpr_read_b32 v18,a10
v_accvgpr_read_b32 v19,a11
v_accvgpr_read_b32 v20,a12
v_accvgpr_read_b32 v21,a13
v_accvgpr_read_b32 v22,a14
v_accvgpr_read_b32 v23,a15
v_mul_f32 v16,v16,v24
v_mul_f32 v17,v17,v24
v_mul_f32 v18,v18,v24
v_mul_f32 v19,v19,v24
v_mul_f32 v20,v20,v24
v_mul_f32 v21,v21,v24
v_mul_f32 v22,v22,v24
v_mul_f32 v23,v23,v24
v_cvt_pk_bf16_f32 v16,v16,v17
v_cvt_pk_bf16_f32 v17,v18,v19
v_cvt_pk_bf16_f32 v18,v20,v21
v_cvt_pk_bf16_f32 v19,v22,v23
ds_write_b128 v7,v[16:19] offset:7168
v_accvgpr_read_b32 v8,a16
v_accvgpr_read_b32 v9,a17
v_accvgpr_read_b32 v10,a18
v_accvgpr_read_b32 v11,a19
v_accvgpr_read_b32 v12,a20
v_accvgpr_read_b32 v13,a21
v_accvgpr_read_b32 v14,a22
v_accvgpr_read_b32 v15,a23
v_mul_f32 v8,v8,v24
v_mul_f32 v9,v9,v24
v_mul_f32 v10,v10,v24
v_mul_f32 v11,v11,v24
v_mul_f32 v12,v12,v24
v_mul_f32 v13,v13,v24
v_mul_f32 v14,v14,v24
v_mul_f32 v15,v15,v24
v_cvt_pk_bf16_f32 v8,v8,v9
v_cvt_pk_bf16_f32 v9,v10,v11
v_cvt_pk_bf16_f32 v10,v12,v13
v_cvt_pk_bf16_f32 v11,v14,v15
ds_write_b128 v7,v[8:11]
v_accvgpr_read_b32 v16,a24
v_accvgpr_read_b32 v17,a25
v_accvgpr_read_b32 v18,a26
v_accvgpr_read_b32 v19,a27
v_accvgpr_read_b32 v20,a28
v_accvgpr_read_b32 v21,a29
v_accvgpr_read_b32 v22,a30
v_accvgpr_read_b32 v23,a31
v_mul_f32 v16,v16,v24
v_mul_f32 v17,v17,v24
v_mul_f32 v18,v18,v24
v_mul_f32 v19,v19,v24
v_mul_f32 v20,v20,v24
v_mul_f32 v21,v21,v24
v_mul_f32 v22,v22,v24
v_mul_f32 v23,v23,v24
v_cvt_pk_bf16_f32 v16,v16,v17
v_cvt_pk_bf16_f32 v17,v18,v19
v_cvt_pk_bf16_f32 v18,v20,v21
v_cvt_pk_bf16_f32 v19,v22,v23
ds_write_b128 v7,v[16:19] offset:1024
v_accvgpr_read_b32 v32,a160
v_accvgpr_read_b32 v33,a161
v_accvgpr_read_b32 v34,a162
v_accvgpr_read_b32 v35,a163
v_accvgpr_read_b32 v36,a164
v_accvgpr_read_b32 v37,a165
v_accvgpr_read_b32 v38,a166
v_accvgpr_read_b32 v39,a167
v_accvgpr_read_b32 v40,a168
v_accvgpr_read_b32 v41,a169
v_accvgpr_read_b32 v42,a170
v_accvgpr_read_b32 v43,a171
v_accvgpr_read_b32 v44,a172
v_accvgpr_read_b32 v45,a173
v_accvgpr_read_b32 v46,a174
v_accvgpr_read_b32 v47,a175
s_waitcnt lgkmcnt(0)
v_subrev_u32 v8,s78,v1
ds_read_b128 a[128:131],v8
ds_read_b128 a[132:135],v8 offset:256
ds_read_b128 a[136:139],v8 offset:512
ds_read_b128 a[140:143],v8 offset:768
ds_read_b128 a[144:147],v8 offset:4096
ds_read_b128 a[148:151],v8 offset:4352
ds_read_b128 a[152:155],v8 offset:4608
ds_read_b128 a[156:159],v8 offset:4864
ds_read_b128 a[160:163],v8 offset:8192
ds_read_b128 a[164:167],v8 offset:8448
ds_read_b128 a[168:171],v8 offset:8704
ds_read_b128 a[172:175],v8 offset:8960
ds_read_b128 a[176:179],v8 offset:12288
ds_read_b128 a[180:183],v8 offset:12544
ds_read_b128 a[184:187],v8 offset:12800
ds_read_b128 a[188:191],v8 offset:13056
s_waitcnt lgkmcnt(15)
v_mfma_f32_16x16x32_bf16 v[64:67],a[128:131],v[128:131],v[32:35]
s_waitcnt lgkmcnt(14)
v_mfma_f32_16x16x32_bf16 v[64:67],a[132:135],v[136:139],v[64:67]
s_waitcnt lgkmcnt(13)
v_mfma_f32_16x16x32_bf16 v[64:67],a[136:139],v[144:147],v[64:67]
s_waitcnt lgkmcnt(12)
v_mfma_f32_16x16x32_bf16 v[64:67],a[140:143],v[152:155],v[64:67]
s_waitcnt lgkmcnt(8)
v_mfma_f32_16x16x32_bf16 v[68:71],a[156:159],v[152:155],v[32:35]
v_mfma_f32_16x16x32_bf16 v[68:71],a[152:155],v[144:147],v[68:71]
v_mfma_f32_16x16x32_bf16 v[68:71],a[148:151],v[136:139],v[68:71]
v_mfma_f32_16x16x32_bf16 v[68:71],a[144:147],v[128:131],v[68:71]
s_waitcnt lgkmcnt(0)
v_mfma_f32_16x16x32_bf16 v[72:75],a[160:163],v[128:131],v[32:35]
v_mfma_f32_16x16x32_bf16 v[72:75],a[164:167],v[136:139],v[72:75]
v_mfma_f32_16x16x32_bf16 v[72:75],a[168:171],v[144:147],v[72:75]
v_mfma_f32_16x16x32_bf16 v[72:75],a[172:175],v[152:155],v[72:75]
v_mfma_f32_16x16x32_bf16 v[76:79],a[188:191],v[152:155],v[32:35]
v_mfma_f32_16x16x32_bf16 v[76:79],a[184:187],v[144:147],v[76:79]
v_mfma_f32_16x16x32_bf16 v[76:79],a[180:183],v[136:139],v[76:79]
v_mfma_f32_16x16x32_bf16 v[76:79],a[176:179],v[128:131],v[76:79]
v_mfma_f32_16x16x32_bf16 v[80:83],a[128:131],v[160:163],v[36:39]
v_mfma_f32_16x16x32_bf16 v[80:83],a[132:135],v[168:171],v[80:83]
v_mfma_f32_16x16x32_bf16 v[80:83],a[136:139],v[176:179],v[80:83]
v_mfma_f32_16x16x32_bf16 v[80:83],a[140:143],v[184:187],v[80:83]
v_mfma_f32_16x16x32_bf16 v[84:87],a[156:159],v[184:187],v[36:39]
v_mfma_f32_16x16x32_bf16 v[84:87],a[152:155],v[176:179],v[84:87]
v_mfma_f32_16x16x32_bf16 v[84:87],a[148:151],v[168:171],v[84:87]
v_mfma_f32_16x16x32_bf16 v[84:87],a[144:147],v[160:163],v[84:87]
v_mfma_f32_16x16x32_bf16 v[88:91],a[160:163],v[160:163],v[36:39]
v_mfma_f32_16x16x32_bf16 v[88:91],a[164:167],v[168:171],v[88:91]
v_mfma_f32_16x16x32_bf16 v[88:91],a[168:171],v[176:179],v[88:91]
v_mfma_f32_16x16x32_bf16 v[88:91],a[172:175],v[184:187],v[88:91]
v_mfma_f32_16x16x32_bf16 v[92:95],a[188:191],v[184:187],v[36:39]
v_mfma_f32_16x16x32_bf16 v[92:95],a[184:187],v[176:179],v[92:95]
v_mfma_f32_16x16x32_bf16 v[92:95],a[180:183],v[168:171],v[92:95]
v_mfma_f32_16x16x32_bf16 v[92:95],a[176:179],v[160:163],v[92:95]
v_mfma_f32_16x16x32_bf16 v[96:99],a[128:131],v[192:195],v[40:43]
v_mfma_f32_16x16x32_bf16 v[96:99],a[132:135],v[200:203],v[96:99]
v_mfma_f32_16x16x32_bf16 v[96:99],a[136:139],v[208:211],v[96:99]
v_mfma_f32_16x16x32_bf16 v[96:99],a[140:143],v[216:219],v[96:99]
v_mfma_f32_16x16x32_bf16 v[100:103],a[156:159],v[216:219],v[40:43]
v_mfma_f32_16x16x32_bf16 v[100:103],a[152:155],v[208:211],v[100:103]
v_mfma_f32_16x16x32_bf16 v[100:103],a[148:151],v[200:203],v[100:103]
v_mfma_f32_16x16x32_bf16 v[100:103],a[144:147],v[192:195],v[100:103]
v_mfma_f32_16x16x32_bf16 v[104:107],a[160:163],v[192:195],v[40:43]
v_mfma_f32_16x16x32_bf16 v[104:107],a[164:167],v[200:203],v[104:107]
v_mfma_f32_16x16x32_bf16 v[104:107],a[168:171],v[208:211],v[104:107]
v_mfma_f32_16x16x32_bf16 v[104:107],a[172:175],v[216:219],v[104:107]
v_mfma_f32_16x16x32_bf16 v[108:111],a[188:191],v[216:219],v[40:43]
v_mfma_f32_16x16x32_bf16 v[108:111],a[184:187],v[208:211],v[108:111]
v_mfma_f32_16x16x32_bf16 v[108:111],a[180:183],v[200:203],v[108:111]
v_mfma_f32_16x16x32_bf16 v[108:111],a[176:179],v[192:195],v[108:111]
v_exp_f32 v64,v64
v_exp_f32 v65,v65
s_nop 0
v_cvt_pk_bf16_f32 v64,v64,v65
v_exp_f32 v66,v66
v_exp_f32 v67,v67
s_nop 0
v_cvt_pk_bf16_f32 v65,v66,v67
v_exp_f32 v68,v68
v_exp_f32 v69,v69
s_nop 0
v_cvt_pk_bf16_f32 v66,v68,v69
v_exp_f32 v70,v70
v_exp_f32 v71,v71
s_nop 0
v_cvt_pk_bf16_f32 v67,v70,v71
v_exp_f32 v72,v72
v_exp_f32 v73,v73
s_nop 0
v_cvt_pk_bf16_f32 v72,v72,v73
v_exp_f32 v74,v74
v_exp_f32 v75,v75
s_nop 0
v_cvt_pk_bf16_f32 v73,v74,v75
v_exp_f32 v76,v76
v_exp_f32 v77,v77
s_nop 0
v_cvt_pk_bf16_f32 v74,v76,v77
v_exp_f32 v78,v78
v_exp_f32 v79,v79
s_nop 0
v_cvt_pk_bf16_f32 v75,v78,v79
v_exp_f32 v80,v80
v_exp_f32 v81,v81
s_nop 0
v_cvt_pk_bf16_f32 v80,v80,v81
v_exp_f32 v82,v82
v_exp_f32 v83,v83
s_nop 0
v_cvt_pk_bf16_f32 v81,v82,v83
v_exp_f32 v84,v84
v_exp_f32 v85,v85
s_nop 0
v_cvt_pk_bf16_f32 v82,v84,v85
v_exp_f32 v86,v86
v_exp_f32 v87,v87
s_nop 0
v_cvt_pk_bf16_f32 v83,v86,v87
v_exp_f32 v88,v88
v_exp_f32 v89,v89
s_nop 0
v_cvt_pk_bf16_f32 v88,v88,v89
v_mfma_f32_16x16x32_bf16 a[0:3],a[192:195],v[64:67],0
v_mfma_f32_16x16x32_bf16 a[0:3],a[196:199],v[72:75],a[0:3]
v_mfma_f32_16x16x32_bf16 a[4:7],a[200:203],v[64:67],0
v_mfma_f32_16x16x32_bf16 a[4:7],a[204:207],v[72:75],a[4:7]
v_mfma_f32_16x16x32_bf16 a[8:11],a[208:211],v[64:67],0
v_mfma_f32_16x16x32_bf16 a[8:11],a[212:215],v[72:75],a[8:11]
v_mfma_f32_16x16x32_bf16 a[12:15],a[216:219],v[64:67],0
v_mfma_f32_16x16x32_bf16 a[12:15],a[220:223],v[72:75],a[12:15]
v_mfma_f32_16x16x32_bf16 a[16:19],a[224:227],v[64:67],0
v_mfma_f32_16x16x32_bf16 a[16:19],a[228:231],v[72:75],a[16:19]
v_mfma_f32_16x16x32_bf16 a[20:23],a[232:235],v[64:67],0
v_mfma_f32_16x16x32_bf16 a[20:23],a[236:239],v[72:75],a[20:23]
v_mfma_f32_16x16x32_bf16 a[24:27],a[240:243],v[64:67],0
v_mfma_f32_16x16x32_bf16 a[24:27],a[244:247],v[72:75],a[24:27]
v_mfma_f32_16x16x32_bf16 a[28:31],a[248:251],v[64:67],0
v_mfma_f32_16x16x32_bf16 a[28:31],a[252:255],v[72:75],a[28:31]
v_mfma_f32_16x16x32_bf16 v[48:51],v[28:31],v[64:67],0
v_mfma_f32_16x16x32_bf16 v[48:51],v[28:31],v[72:75],v[48:51]
s_waitcnt vmcnt(0) lgkmcnt(0)
s_nop 7
s_branch L_ok0_%=
L_slow1_%=:
s_waitcnt vmcnt(0) lgkmcnt(0)
s_nop 7
v_bfe_u32 v11,v6,4,4
v_lshrrev_b32 v12,8,v6
v_lshlrev_b32 v8,8,v11
v_lshl_add_u32 v8,v12,4,v8
v_lshlrev_b32 v9,10,v12
v_lshl_add_u32 v9,v11,1,v9
v_add_u32 v10,4096,v9
v_mov_b32 v48,0xff800000
v_mov_b32 v52,0xff800000
v_mov_b32 v56,0xff800000
v_mov_b32 v60,0xff800000
s_mov_b32 s65,262144
s_mov_b32 s66,64
L_s11_%=:
buffer_load_dwordx4 a[128:131],v8,s[20:23],s65 offen
buffer_load_dwordx4 a[132:135],v8,s[20:23],s65 offen offset:64
buffer_load_dwordx4 a[136:139],v8,s[20:23],s65 offen offset:128
buffer_load_dwordx4 a[140:143],v8,s[20:23],s65 offen offset:192
s_waitcnt vmcnt(0)
v_mfma_f32_16x16x32_bf16 v[64:67],a[128:131],v[128:131],0
v_mfma_f32_16x16x32_bf16 v[64:67],a[132:135],v[136:139],v[64:67]
v_mfma_f32_16x16x32_bf16 v[64:67],a[136:139],v[144:147],v[64:67]
v_mfma_f32_16x16x32_bf16 v[64:67],a[140:143],v[152:155],v[64:67]
v_mfma_f32_16x16x32_bf16 v[80:83],a[128:131],v[160:163],0
v_mfma_f32_16x16x32_bf16 v[80:83],a[132:135],v[168:171],v[80:83]
v_mfma_f32_16x16x32_bf16 v[80:83],a[136:139],v[176:179],v[80:83]
v_mfma_f32_16x16x32_bf16 v[80:83],a[140:143],v[184:187],v[80:83]
v_mfma_f32_16x16x32_bf16 v[96:99],a[128:131],v[192:195],0
v_mfma_f32_16x16x32_bf16 v[96:99],a[132:135],v[200:203],v[96:99]
v_mfma_f32_16x16x32_bf16 v[96:99],a[136:139],v[208:211],v[96:99]
v_mfma_f32_16x16x32_bf16 v[96:99],a[140:143],v[216:219],v[96:99]
v_mfma_f32_16x16x32_bf16 v[112:115],a[128:131],v[224:227],0
v_mfma_f32_16x16x32_bf16 v[112:115],a[132:135],v[232:235],v[112:115]
v_mfma_f32_16x16x32_bf16 v[112:115],a[136:139],v[240:243],v[112:115]
v_mfma_f32_16x16x32_bf16 v[112:115],a[140:143],v[248:251],v[112:115]
s_nop 7
v_max3_f32 v48,v48,v64,v65
v_max3_f32 v48,v48,v66,v67
v_max3_f32 v52,v52,v80,v81
v_max3_f32 v52,v52,v82,v83
v_max3_f32 v56,v56,v96,v97
v_max3_f32 v56,v56,v98,v99
v_max3_f32 v60,v60,v112,v113
v_max3_f32 v60,v60,v114,v115
s_add_u32 s65,s65,4096
s_sub_u32 s66,s66,1
s_cmp_lg_u32 s66,0
s_cbranch_scc1 L_s11_%=
v_mov_b32 v11,v48
s_nop 1
v_permlane32_swap_b32 v48,v11
v_max_f32 v48,v48,v11
v_mov_b32 v11,v48
s_nop 1
v_permlane16_swap_b32 v48,v11
v_max_f32 v48,v48,v11
v_sub_f32 v32,0,v48
v_sub_f32 v33,0,v48
v_sub_f32 v34,0,v48
v_sub_f32 v35,0,v48
v_mov_b32 v11,v52
s_nop 1
v_permlane32_swap_b32 v52,v11
v_max_f32 v52,v52,v11
v_mov_b32 v11,v52
s_nop 1
v_permlane16_swap_b32 v52,v11
v_max_f32 v52,v52,v11
v_sub_f32 v36,0,v52
v_sub_f32 v37,0,v52
v_sub_f32 v38,0,v52
v_sub_f32 v39,0,v52
v_mov_b32 v11,v56
s_nop 1
v_permlane32_swap_b32 v56,v11
v_max_f32 v56,v56,v11
v_mov_b32 v11,v56
s_nop 1
v_permlane16_swap_b32 v56,v11
v_max_f32 v56,v56,v11
v_sub_f32 v40,0,v56
v_sub_f32 v41,0,v56
v_sub_f32 v42,0,v56
v_sub_f32 v43,0,v56
v_mov_b32 v11,v60
s_nop 1
v_permlane32_swap_b32 v60,v11
v_max_f32 v60,v60,v11
v_mov_b32 v11,v60
s_nop 1
v_permlane16_swap_b32 v60,v11
v_max_f32 v60,v60,v11
v_max_f32 v60,s72,v60
v_sub_f32 v44,0,v60
v_sub_f32 v45,0,v60
v_sub_f32 v46,0,v60
v_sub_f32 v47,0,v60
v_mov_b32 v48,0
v_mov_b32 v49,0
v_mov_b32 v50,0
v_mov_b32 v51,0
v_mov_b32 v52,0
v_mov_b32 v53,0
v_mov_b32 v54,0
v_mov_b32 v55,0
v_mov_b32 v56,0
v_mov_b32 v57,0
v_mov_b32 v58,0
v_mov_b32 v59,0
v_mov_b32 v60,0
v_mov_b32 v61,0
v_mov_b32 v62,0
v_mov_b32 v63,0
v_accvgpr_write_b32 a0,0
v_accvgpr_write_b32 a1,0
v_accvgpr_write_b32 a2,0
v_accvgpr_write_b32 a3,0
v_accvgpr_write_b32 a4,0
v_accvgpr_write_b32 a5,0
v_accvgpr_write_b32 a6,0
v_accvgpr_write_b32 a7,0
v_accvgpr_write_b32 a8,0
v_accvgpr_write_b32 a9,0
v_accvgpr_write_b32 a10,0
v_accvgpr_write_b32 a11,0
v_accvgpr_write_b32 a12,0
v_accvgpr_write_b32 a13,0
v_accvgpr_write_b32 a14,0
v_accvgpr_write_b32 a15,0
v_accvgpr_write_b32 a16,0
v_accvgpr_write_b32 a17,0
v_accvgpr_write_b32 a18,0
v_accvgpr_write_b32 a19,0
v_accvgpr_write_b32 a20,0
v_accvgpr_write_b32 a21,0
v_accvgpr_write_b32 a22,0
v_accvgpr_write_b32 a23,0
v_accvgpr_write_b32 a24,0
v_accvgpr_write_b32 a25,0
v_accvgpr_write_b32 a26,0
v_accvgpr_write_b32 a27,0
v_accvgpr_write_b32 a28,0
v_accvgpr_write_b32 a29,0
v_accvgpr_write_b32 a30,0
)A"
R"A(v_accvgpr_write_b32 a31,0
v_mov_b32 v132,0
v_mov_b32 v133,0
v_mov_b32 v134,0
v_mov_b32 v135,0
v_mov_b32 v140,0
v_mov_b32 v141,0
v_mov_b32 v142,0
v_mov_b32 v143,0
v_mov_b32 v148,0
v_mov_b32 v149,0
v_mov_b32 v150,0
v_mov_b32 v151,0
v_mov_b32 v156,0
v_mov_b32 v157,0
v_mov_b32 v158,0
v_mov_b32 v159,0
v_mov_b32 v164,0
v_mov_b32 v165,0
v_mov_b32 v166,0
v_mov_b32 v167,0
v_mov_b32 v172,0
v_mov_b32 v173,0
v_mov_b32 v174,0
v_mov_b32 v175,0
v_mov_b32 v180,0
v_mov_b32 v181,0
v_mov_b32 v182,0
v_mov_b32 v183,0
v_mov_b32 v188,0
v_mov_b32 v189,0
v_mov_b32 v190,0
v_mov_b32 v191,0
v_mov_b32 v196,0
v_mov_b32 v197,0
v_mov_b32 v198,0
v_mov_b32 v199,0
v_mov_b32 v204,0
v_mov_b32 v205,0
v_mov_b32 v206,0
v_mov_b32 v207,0
v_mov_b32 v212,0
v_mov_b32 v213,0
v_mov_b32 v214,0
v_mov_b32 v215,0
v_mov_b32 v220,0
v_mov_b32 v221,0
v_mov_b32 v222,0
v_mov_b32 v223,0
v_mov_b32 v228,0
v_mov_b32 v229,0
v_mov_b32 v230,0
v_mov_b32 v231,0
v_mov_b32 v236,0
v_mov_b32 v237,0
v_mov_b32 v238,0
v_mov_b32 v239,0
v_mov_b32 v244,0
v_mov_b32 v245,0
v_mov_b32 v246,0
v_mov_b32 v247,0
v_mov_b32 v252,0
v_mov_b32 v253,0
v_mov_b32 v254,0
v_mov_b32 v255,0
v_accvgpr_write_b32 a32,0
v_accvgpr_write_b32 a33,0
v_accvgpr_write_b32 a34,0
v_accvgpr_write_b32 a35,0
v_accvgpr_write_b32 a36,0
v_accvgpr_write_b32 a37,0
v_accvgpr_write_b32 a38,0
v_accvgpr_write_b32 a39,0
v_accvgpr_write_b32 a40,0
v_accvgpr_write_b32 a41,0
v_accvgpr_write_b32 a42,0
v_accvgpr_write_b32 a43,0
v_accvgpr_write_b32 a44,0
v_accvgpr_write_b32 a45,0
v_accvgpr_write_b32 a46,0
v_accvgpr_write_b32 a47,0
v_accvgpr_write_b32 a48,0
v_accvgpr_write_b32 a49,0
v_accvgpr_write_b32 a50,0
v_accvgpr_write_b32 a51,0
v_accvgpr_write_b32 a52,0
v_accvgpr_write_b32 a53,0
v_accvgpr_write_b32 a54,0
v_accvgpr_write_b32 a55,0
v_accvgpr_write_b32 a56,0
v_accvgpr_write_b32 a57,0
v_accvgpr_write_b32 a58,0
v_accvgpr_write_b32 a59,0
v_accvgpr_write_b32 a60,0
v_accvgpr_write_b32 a61,0
v_accvgpr_write_b32 a62,0
v_accvgpr_write_b32 a63,0
s_mov_b32 s65,262144
s_mov_b32 s66,32
L_s21_%=:
s_add_u32 s67,s65,4096
buffer_load_dwordx4 a[128:131],v8,s[20:23],s65 offen
buffer_load_dwordx4 a[132:135],v8,s[20:23],s65 offen offset:64
buffer_load_dwordx4 a[136:139],v8,s[20:23],s65 offen offset:128
buffer_load_dwordx4 a[140:143],v8,s[20:23],s65 offen offset:192
buffer_load_dwordx4 a[144:147],v8,s[20:23],s67 offen
buffer_load_dwordx4 a[148:151],v8,s[20:23],s67 offen offset:64
buffer_load_dwordx4 a[152:155],v8,s[20:23],s67 offen offset:128
buffer_load_dwordx4 a[156:159],v8,s[20:23],s67 offen offset:192
s_waitcnt vmcnt(0)
v_mfma_f32_16x16x32_bf16 v[64:67],a[128:131],v[128:131],v[32:35]
v_mfma_f32_16x16x32_bf16 v[64:67],a[132:135],v[136:139],v[64:67]
v_mfma_f32_16x16x32_bf16 v[64:67],a[136:139],v[144:147],v[64:67]
v_mfma_f32_16x16x32_bf16 v[64:67],a[140:143],v[152:155],v[64:67]
v_mfma_f32_16x16x32_bf16 v[68:71],a[144:147],v[128:131],v[32:35]
v_mfma_f32_16x16x32_bf16 v[68:71],a[148:151],v[136:139],v[68:71]
v_mfma_f32_16x16x32_bf16 v[68:71],a[152:155],v[144:147],v[68:71]
v_mfma_f32_16x16x32_bf16 v[68:71],a[156:159],v[152:155],v[68:71]
v_mfma_f32_16x16x32_bf16 v[80:83],a[128:131],v[160:163],v[36:39]
v_mfma_f32_16x16x32_bf16 v[80:83],a[132:135],v[168:171],v[80:83]
v_mfma_f32_16x16x32_bf16 v[80:83],a[136:139],v[176:179],v[80:83]
v_mfma_f32_16x16x32_bf16 v[80:83],a[140:143],v[184:187],v[80:83]
v_mfma_f32_16x16x32_bf16 v[84:87],a[144:147],v[160:163],v[36:39]
v_mfma_f32_16x16x32_bf16 v[84:87],a[148:151],v[168:171],v[84:87]
v_mfma_f32_16x16x32_bf16 v[84:87],a[152:155],v[176:179],v[84:87]
v_mfma_f32_16x16x32_bf16 v[84:87],a[156:159],v[184:187],v[84:87]
v_mfma_f32_16x16x32_bf16 v[96:99],a[128:131],v[192:195],v[40:43]
v_mfma_f32_16x16x32_bf16 v[96:99],a[132:135],v[200:203],v[96:99]
v_mfma_f32_16x16x32_bf16 v[96:99],a[136:139],v[208:211],v[96:99]
v_mfma_f32_16x16x32_bf16 v[96:99],a[140:143],v[216:219],v[96:99]
v_mfma_f32_16x16x32_bf16 v[100:103],a[144:147],v[192:195],v[40:43]
v_mfma_f32_16x16x32_bf16 v[100:103],a[148:151],v[200:203],v[100:103]
v_mfma_f32_16x16x32_bf16 v[100:103],a[152:155],v[208:211],v[100:103]
v_mfma_f32_16x16x32_bf16 v[100:103],a[156:159],v[216:219],v[100:103]
v_mfma_f32_16x16x32_bf16 v[112:115],a[128:131],v[224:227],v[44:47]
v_mfma_f32_16x16x32_bf16 v[112:115],a[132:135],v[232:235],v[112:115]
v_mfma_f32_16x16x32_bf16 v[112:115],a[136:139],v[240:243],v[112:115]
v_mfma_f32_16x16x32_bf16 v[112:115],a[140:143],v[248:251],v[112:115]
v_mfma_f32_16x16x32_bf16 v[116:119],a[144:147],v[224:227],v[44:47]
v_mfma_f32_16x16x32_bf16 v[116:119],a[148:151],v[232:235],v[116:119]
v_mfma_f32_16x16x32_bf16 v[116:119],a[152:155],v[240:243],v[116:119]
v_mfma_f32_16x16x32_bf16 v[116:119],a[156:159],v[248:251],v[116:119]
s_nop 7
v_exp_f32 v64,v64
v_exp_f32 v65,v65
v_exp_f32 v66,v66
v_exp_f32 v67,v67
v_exp_f32 v68,v68
v_exp_f32 v69,v69
v_exp_f32 v70,v70
v_exp_f32 v71,v71
s_nop 0
v_cvt_pk_bf16_f32 v64,v64,v65
v_cvt_pk_bf16_f32 v65,v66,v67
v_cvt_pk_bf16_f32 v66,v68,v69
v_cvt_pk_bf16_f32 v67,v70,v71
v_exp_f32 v80,v80
v_exp_f32 v81,v81
v_exp_f32 v82,v82
v_exp_f32 v83,v83
v_exp_f32 v84,v84
v_exp_f32 v85,v85
v_exp_f32 v86,v86
v_exp_f32 v87,v87
s_nop 0
v_cvt_pk_bf16_f32 v80,v80,v81
v_cvt_pk_bf16_f32 v81,v82,v83
v_cvt_pk_bf16_f32 v82,v84,v85
v_cvt_pk_bf16_f32 v83,v86,v87
v_exp_f32 v96,v96
v_exp_f32 v97,v97
v_exp_f32 v98,v98
v_exp_f32 v99,v99
v_exp_f32 v100,v100
v_exp_f32 v101,v101
v_exp_f32 v102,v102
v_exp_f32 v103,v103
s_nop 0
v_cvt_pk_bf16_f32 v96,v96,v97
v_cvt_pk_bf16_f32 v97,v98,v99
v_cvt_pk_bf16_f32 v98,v100,v101
v_cvt_pk_bf16_f32 v99,v102,v103
v_exp_f32 v112,v112
v_exp_f32 v113,v113
v_exp_f32 v114,v114
v_exp_f32 v115,v115
v_exp_f32 v116,v116
v_exp_f32 v117,v117
v_exp_f32 v118,v118
v_exp_f32 v119,v119
s_nop 0
v_cvt_pk_bf16_f32 v112,v112,v113
v_cvt_pk_bf16_f32 v113,v114,v115
v_cvt_pk_bf16_f32 v114,v116,v117
v_cvt_pk_bf16_f32 v115,v118,v119
buffer_load_ushort v72,v9,s[24:27],s65 offen
buffer_load_ushort v73,v9,s[24:27],s65 offen offset:256
buffer_load_ushort v74,v9,s[24:27],s65 offen offset:512
buffer_load_ushort v75,v9,s[24:27],s65 offen offset:768
buffer_load_ushort v76,v10,s[24:27],s65 offen
buffer_load_ushort v77,v10,s[24:27],s65 offen offset:256
buffer_load_ushort v78,v10,s[24:27],s65 offen offset:512
buffer_load_ushort v79,v10,s[24:27],s65 offen offset:768
s_waitcnt vmcnt(0)
v_lshl_or_b32 v88,v73,16,v72
v_lshl_or_b32 v89,v75,16,v74
v_lshl_or_b32 v90,v77,16,v76
v_lshl_or_b32 v91,v79,16,v78
s_nop 1
v_mfma_f32_16x16x32_bf16 a[0:3],v[88:91],v[64:67],a[0:3]
v_mfma_f32_16x16x32_bf16 v[132:135],v[88:91],v[80:83],v[132:135]
v_mfma_f32_16x16x32_bf16 v[196:199],v[88:91],v[96:99],v[196:199]
v_mfma_f32_16x16x32_bf16 a[32:35],v[88:91],v[112:115],a[32:35]
buffer_load_ushort v72,v9,s[24:27],s65 offen offset:32
buffer_load_ushort v73,v9,s[24:27],s65 offen offset:288
buffer_load_ushort v74,v9,s[24:27],s65 offen offset:544
buffer_load_ushort v75,v9,s[24:27],s65 offen offset:800
buffer_load_ushort v76,v10,s[24:27],s65 offen offset:32
buffer_load_ushort v77,v10,s[24:27],s65 offen offset:288
buffer_load_ushort v78,v10,s[24:27],s65 offen offset:544
buffer_load_ushort v79,v10,s[24:27],s65 offen offset:800
s_waitcnt vmcnt(0)
v_lshl_or_b32 v88,v73,16,v72
v_lshl_or_b32 v89,v75,16,v74
v_lshl_or_b32 v90,v77,16,v76
v_lshl_or_b32 v91,v79,16,v78
s_nop 1
v_mfma_f32_16x16x32_bf16 a[4:7],v[88:91],v[64:67],a[4:7]
v_mfma_f32_16x16x32_bf16 v[140:143],v[88:91],v[80:83],v[140:143]
v_mfma_f32_16x16x32_bf16 v[204:207],v[88:91],v[96:99],v[204:207]
v_mfma_f32_16x16x32_bf16 a[36:39],v[88:91],v[112:115],a[36:39]
buffer_load_ushort v72,v9,s[24:27],s65 offen offset:64
buffer_load_ushort v73,v9,s[24:27],s65 offen offset:320
buffer_load_ushort v74,v9,s[24:27],s65 offen offset:576
buffer_load_ushort v75,v9,s[24:27],s65 offen offset:832
buffer_load_ushort v76,v10,s[24:27],s65 offen offset:64
buffer_load_ushort v77,v10,s[24:27],s65 offen offset:320
buffer_load_ushort v78,v10,s[24:27],s65 offen offset:576
buffer_load_ushort v79,v10,s[24:27],s65 offen offset:832
s_waitcnt vmcnt(0)
v_lshl_or_b32 v88,v73,16,v72
v_lshl_or_b32 v89,v75,16,v74
v_lshl_or_b32 v90,v77,16,v76
v_lshl_or_b32 v91,v79,16,v78
s_nop 1
v_mfma_f32_16x16x32_bf16 a[8:11],v[88:91],v[64:67],a[8:11]
v_mfma_f32_16x16x32_bf16 v[148:151],v[88:91],v[80:83],v[148:151]
v_mfma_f32_16x16x32_bf16 v[212:215],v[88:91],v[96:99],v[212:215]
v_mfma_f32_16x16x32_bf16 a[40:43],v[88:91],v[112:115],a[40:43]
buffer_load_ushort v72,v9,s[24:27],s65 offen offset:96
buffer_load_ushort v73,v9,s[24:27],s65 offen offset:352
buffer_load_ushort v74,v9,s[24:27],s65 offen offset:608
buffer_load_ushort v75,v9,s[24:27],s65 offen offset:864
buffer_load_ushort v76,v10,s[24:27],s65 offen offset:96
buffer_load_ushort v77,v10,s[24:27],s65 offen offset:352
buffer_load_ushort v78,v10,s[24:27],s65 offen offset:608
buffer_load_ushort v79,v10,s[24:27],s65 offen offset:864
s_waitcnt vmcnt(0)
v_lshl_or_b32 v88,v73,16,v72
v_lshl_or_b32 v89,v75,16,v74
v_lshl_or_b32 v90,v77,16,v76
v_lshl_or_b32 v91,v79,16,v78
s_nop 1
v_mfma_f32_16x16x32_bf16 a[12:15],v[88:91],v[64:67],a[12:15]
v_mfma_f32_16x16x32_bf16 v[156:159],v[88:91],v[80:83],v[156:159]
v_mfma_f32_16x16x32_bf16 v[220:223],v[88:91],v[96:99],v[220:223]
v_mfma_f32_16x16x32_bf16 a[44:47],v[88:91],v[112:115],a[44:47]
buffer_load_ushort v72,v9,s[24:27],s65 offen offset:128
buffer_load_ushort v73,v9,s[24:27],s65 offen offset:384
buffer_load_ushort v74,v9,s[24:27],s65 offen offset:640
buffer_load_ushort v75,v9,s[24:27],s65 offen offset:896
buffer_load_ushort v76,v10,s[24:27],s65 offen offset:128
buffer_load_ushort v77,v10,s[24:27],s65 offen offset:384
buffer_load_ushort v78,v10,s[24:27],s65 offen offset:640
buffer_load_ushort v79,v10,s[24:27],s65 offen offset:896
s_waitcnt vmcnt(0)
v_lshl_or_b32 v88,v73,16,v72
v_lshl_or_b32 v89,v75,16,v74
v_lshl_or_b32 v90,v77,16,v76
v_lshl_or_b32 v91,v79,16,v78
s_nop 1
v_mfma_f32_16x16x32_bf16 a[16:19],v[88:91],v[64:67],a[16:19]
v_mfma_f32_16x16x32_bf16 v[164:167],v[88:91],v[80:83],v[164:167]
v_mfma_f32_16x16x32_bf16 v[228:231],v[88:91],v[96:99],v[228:231]
v_mfma_f32_16x16x32_bf16 a[48:51],v[88:91],v[112:115],a[48:51]
buffer_load_ushort v72,v9,s[24:27],s65 offen offset:160
buffer_load_ushort v73,v9,s[24:27],s65 offen offset:416
buffer_load_ushort v74,v9,s[24:27],s65 offen offset:672
buffer_load_ushort v75,v9,s[24:27],s65 offen offset:928
buffer_load_ushort v76,v10,s[24:27],s65 offen offset:160
buffer_load_ushort v77,v10,s[24:27],s65 offen offset:416
buffer_load_ushort v78,v10,s[24:27],s65 offen offset:672
buffer_load_ushort v79,v10,s[24:27],s65 offen offset:928
s_waitcnt vmcnt(0)
v_lshl_or_b32 v88,v73,16,v72
v_lshl_or_b32 v89,v75,16,v74
v_lshl_or_b32 v90,v77,16,v76
v_lshl_or_b32 v91,v79,16,v78
s_nop 1
v_mfma_f32_16x16x32_bf16 a[20:23],v[88:91],v[64:67],a[20:23]
v_mfma_f32_16x16x32_bf16 v[172:175],v[88:91],v[80:83],v[172:175]
v_mfma_f32_16x16x32_bf16 v[236:239],v[88:91],v[96:99],v[236:239]
v_mfma_f32_16x16x32_bf16 a[52:55],v[88:91],v[112:115],a[52:55]
buffer_load_ushort v72,v9,s[24:27],s65 offen offset:192
buffer_load_ushort v73,v9,s[24:27],s65 offen offset:448
buffer_load_ushort v74,v9,s[24:27],s65 offen offset:704
buffer_load_ushort v75,v9,s[24:27],s65 offen offset:960
buffer_load_ushort v76,v10,s[24:27],s65 offen offset:192
buffer_load_ushort v77,v10,s[24:27],s65 offen offset:448
buffer_load_ushort v78,v10,s[24:27],s65 offen offset:704
buffer_load_ushort v79,v10,s[24:27],s65 offen offset:960
s_waitcnt vmcnt(0)
v_lshl_or_b32 v88,v73,16,v72
v_lshl_or_b32 v89,v75,16,v74
v_lshl_or_b32 v90,v77,16,v76
v_lshl_or_b32 v91,v79,16,v78
s_nop 1
v_mfma_f32_16x16x32_bf16 a[24:27],v[88:91],v[64:67],a[24:27]
v_mfma_f32_16x16x32_bf16 v[180:183],v[88:91],v[80:83],v[180:183]
v_mfma_f32_16x16x32_bf16 v[244:247],v[88:91],v[96:99],v[244:247]
v_mfma_f32_16x16x32_bf16 a[56:59],v[88:91],v[112:115],a[56:59]
buffer_load_ushort v72,v9,s[24:27],s65 offen offset:224
buffer_load_ushort v73,v9,s[24:27],s65 offen offset:480
buffer_load_ushort v74,v9,s[24:27],s65 offen offset:736
buffer_load_ushort v75,v9,s[24:27],s65 offen offset:992
buffer_load_ushort v76,v10,s[24:27],s65 offen offset:224
buffer_load_ushort v77,v10,s[24:27],s65 offen offset:480
)A"
R"A(buffer_load_ushort v78,v10,s[24:27],s65 offen offset:736
buffer_load_ushort v79,v10,s[24:27],s65 offen offset:992
s_waitcnt vmcnt(0)
v_lshl_or_b32 v88,v73,16,v72
v_lshl_or_b32 v89,v75,16,v74
v_lshl_or_b32 v90,v77,16,v76
v_lshl_or_b32 v91,v79,16,v78
s_nop 1
v_mfma_f32_16x16x32_bf16 a[28:31],v[88:91],v[64:67],a[28:31]
v_mfma_f32_16x16x32_bf16 v[188:191],v[88:91],v[80:83],v[188:191]
v_mfma_f32_16x16x32_bf16 v[252:255],v[88:91],v[96:99],v[252:255]
v_mfma_f32_16x16x32_bf16 a[60:63],v[88:91],v[112:115],a[60:63]
v_mfma_f32_16x16x32_bf16 v[48:51],v[28:31],v[64:67],v[48:51]
v_mfma_f32_16x16x32_bf16 v[52:55],v[28:31],v[80:83],v[52:55]
v_mfma_f32_16x16x32_bf16 v[56:59],v[28:31],v[96:99],v[56:59]
v_mfma_f32_16x16x32_bf16 v[60:63],v[28:31],v[112:115],v[60:63]
s_add_u32 s65,s65,8192
s_sub_u32 s66,s66,1
s_cmp_lg_u32 s66,0
s_cbranch_scc1 L_s21_%=
s_nop 7
v_max_f32 v49,0x00800000,v48
v_rcp_f32 v49,v49
v_max_f32 v53,0x00800000,v52
v_rcp_f32 v53,v53
v_max_f32 v57,0x00800000,v56
v_rcp_f32 v57,v57
v_max_f32 v61,0x00800000,v60
v_rcp_f32 v61,v61
v_accvgpr_read_b32 v16,a0
v_accvgpr_read_b32 v17,a1
v_accvgpr_read_b32 v18,a2
v_accvgpr_read_b32 v19,a3
v_accvgpr_read_b32 v20,a4
v_accvgpr_read_b32 v21,a5
v_accvgpr_read_b32 v22,a6
v_accvgpr_read_b32 v23,a7
v_mul_f32 v16,v16,v49
v_mul_f32 v17,v17,v49
v_mul_f32 v18,v18,v49
v_mul_f32 v19,v19,v49
v_mul_f32 v20,v20,v49
v_mul_f32 v21,v21,v49
v_mul_f32 v22,v22,v49
v_mul_f32 v23,v23,v49
v_cvt_pk_bf16_f32 v32,v16,v17
v_cvt_pk_bf16_f32 v33,v18,v19
v_cvt_pk_bf16_f32 v34,v20,v21
v_cvt_pk_bf16_f32 v35,v22,v23
v_accvgpr_read_b32 v8,a8
v_accvgpr_read_b32 v9,a9
v_accvgpr_read_b32 v10,a10
v_accvgpr_read_b32 v11,a11
v_accvgpr_read_b32 v12,a12
v_accvgpr_read_b32 v13,a13
v_accvgpr_read_b32 v14,a14
v_accvgpr_read_b32 v15,a15
v_mul_f32 v8,v8,v49
v_mul_f32 v9,v9,v49
v_mul_f32 v10,v10,v49
v_mul_f32 v11,v11,v49
v_mul_f32 v12,v12,v49
v_mul_f32 v13,v13,v49
v_mul_f32 v14,v14,v49
v_mul_f32 v15,v15,v49
v_cvt_pk_bf16_f32 v36,v8,v9
v_cvt_pk_bf16_f32 v37,v10,v11
v_cvt_pk_bf16_f32 v38,v12,v13
v_cvt_pk_bf16_f32 v39,v14,v15
v_accvgpr_read_b32 v16,a16
v_accvgpr_read_b32 v17,a17
v_accvgpr_read_b32 v18,a18
v_accvgpr_read_b32 v19,a19
v_accvgpr_read_b32 v20,a20
v_accvgpr_read_b32 v21,a21
v_accvgpr_read_b32 v22,a22
v_accvgpr_read_b32 v23,a23
v_mul_f32 v16,v16,v49
v_mul_f32 v17,v17,v49
v_mul_f32 v18,v18,v49
v_mul_f32 v19,v19,v49
v_mul_f32 v20,v20,v49
v_mul_f32 v21,v21,v49
v_mul_f32 v22,v22,v49
v_mul_f32 v23,v23,v49
v_cvt_pk_bf16_f32 v40,v16,v17
v_cvt_pk_bf16_f32 v41,v18,v19
v_cvt_pk_bf16_f32 v42,v20,v21
v_cvt_pk_bf16_f32 v43,v22,v23
ds_read_b64_tr_b16 v[64:65],v1
ds_read_b64_tr_b16 v[66:67],v1 offset:5120
ds_read_b64_tr_b16 v[68:69],v1 offset:10240
ds_read_b64_tr_b16 v[70:71],v1 offset:15360
ds_read_b64_tr_b16 v[72:73],v1 offset:20480
ds_read_b64_tr_b16 v[74:75],v1 offset:25600
ds_read_b64_tr_b16 v[76:77],v1 offset:30720
ds_read_b64_tr_b16 v[78:79],v1 offset:35840
ds_read_b64_tr_b16 v[80:81],v2 offset:32
ds_read_b64_tr_b16 v[82:83],v2 offset:5152
ds_read_b64_tr_b16 v[84:85],v2 offset:10272
ds_read_b64_tr_b16 v[86:87],v2 offset:15392
ds_read_b64_tr_b16 v[88:89],v2 offset:20512
ds_read_b64_tr_b16 v[90:91],v2 offset:25632
ds_read_b64_tr_b16 v[92:93],v2 offset:30752
ds_read_b64_tr_b16 v[94:95],v2 offset:35872
s_waitcnt vmcnt(0) lgkmcnt(0)
s_nop 7
s_branch L_ok1_%=
L_end_%=:
)A"
    :: "s"(ka), "s"(blk), "s"(wid)
    : "v0", "v1", "v2", "v3", "v4", "v5", "v6", "v7", "v8", "v9", "v10", "v11", "v12", "v13", "v14", "v15", "v16", "v17", "v18", "v19", "v20", "v21", "v22", "v23", "v24", "v25", "v26", "v27", "v28", "v29", "v30", "v31", "v32", "v33", "v34", "v35", "v36", "v37", "v38", "v39", "v40", "v41", "v42", "v43", "v44", "v45", "v46", "v47", "v48", "v49", "v50", "v51", "v52", "v53", "v54", "v55", "v56", "v57", "v58", "v59", "v60", "v61", "v62", "v63", "v64", "v65", "v66", "v67", "v68", "v69", "v70", "v71", "v72", "v73", "v74", "v75", "v76", "v77", "v78", "v79", "v80", "v81", "v82", "v83", "v84", "v85", "v86", "v87", "v88", "v89", "v90", "v91", "v92", "v93", "v94", "v95", "v96", "v97", "v98", "v99", "v100", "v101", "v102", "v103", "v104", "v105", "v106", "v107", "v108", "v109", "v110", "v111", "v112", "v113", "v114", "v115", "v116", "v117", "v118", "v119", "v120", "v121", "v122", "v123", "v124", "v125", "v126", "v127", "v128", "v129", "v130", "v131", "v132", "v133", "v134", "v135", "v136", "v137", "v138", "v139", "v140", "v141", "v142", "v143", "v144", "v145", "v146", "v147", "v148", "v149", "v150", "v151", "v152", "v153", "v154", "v155", "v156", "v157", "v158", "v159", "v160", "v161", "v162", "v163", "v164", "v165", "v166", "v167", "v168", "v169", "v170", "v171", "v172", "v173", "v174", "v175", "v176", "v177", "v178", "v179", "v180", "v181", "v182", "v183", "v184", "v185", "v186", "v187", "v188", "v189", "v190", "v191", "v192", "v193", "v194", "v195", "v196", "v197", "v198", "v199", "v200", "v201", "v202", "v203", "v204", "v205", "v206", "v207", "v208", "v209", "v210", "v211", "v212", "v213", "v214", "v215", "v216", "v217", "v218", "v219", "v220", "v221", "v222", "v223", "v224", "v225", "v226", "v227", "v228", "v229", "v230", "v231", "v232", "v233", "v234", "v235", "v236", "v237", "v238", "v239", "v240", "v241", "v242", "v243", "v244", "v245", "v246", "v247", "v248", "v249", "v250", "v251", "v252", "v253", "v254", "v255", "a0", "a1", "a2", "a3", "a4", "a5", "a6", "a7", "a8", "a9", "a10", "a11", "a12", "a13", "a14", "a15", "a16", "a17", "a18", "a19", "a20", "a21", "a22", "a23", "a24", "a25", "a26", "a27", "a28", "a29", "a30", "a31", "a32", "a33", "a34", "a35", "a36", "a37", "a38", "a39", "a40", "a41", "a42", "a43", "a44", "a45", "a46", "a47", "a48", "a49", "a50", "a51", "a52", "a53", "a54", "a55", "a56", "a57", "a58", "a59", "a60", "a61", "a62", "a63", "a64", "a65", "a66", "a67", "a68", "a69", "a70", "a71", "a72", "a73", "a74", "a75", "a76", "a77", "a78", "a79", "a80", "a81", "a82", "a83", "a84", "a85", "a86", "a87", "a88", "a89", "a90", "a91", "a92", "a93", "a94", "a95", "a96", "a97", "a98", "a99", "a100", "a101", "a102", "a103", "a104", "a105", "a106", "a107", "a108", "a109", "a110", "a111", "a112", "a113", "a114", "a115", "a116", "a117", "a118", "a119", "a120", "a121", "a122", "a123", "a124", "a125", "a126", "a127", "a128", "a129", "a130", "a131", "a132", "a133", "a134", "a135", "a136", "a137", "a138", "a139", "a140", "a141", "a142", "a143", "a144", "a145", "a146", "a147", "a148", "a149", "a150", "a151", "a152", "a153", "a154", "a155", "a156", "a157", "a158", "a159", "a160", "a161", "a162", "a163", "a164", "a165", "a166", "a167", "a168", "a169", "a170", "a171", "a172", "a173", "a174", "a175", "a176", "a177", "a178", "a179", "a180", "a181", "a182", "a183", "a184", "a185", "a186", "a187", "a188", "a189", "a190", "a191", "a192", "a193", "a194", "a195", "a196", "a197", "a198", "a199", "a200", "a201", "a202", "a203", "a204", "a205", "a206", "a207", "a208", "a209", "a210", "a211", "a212", "a213", "a214", "a215", "a216", "a217", "a218", "a219", "a220", "a221", "a222", "a223", "a224", "a225", "a226", "a227", "a228", "a229", "a230", "a231", "a232", "a233", "a234", "a235", "a236", "a237", "a238", "a239", "a240", "a241", "a242", "a243", "a244", "a245", "a246", "a247", "a248", "a249", "a250", "a251", "a252", "a253", "a254", "a255", "s4", "s5", "s6", "s7", "s8", "s9", "s10", "s11", "s12", "s13", "s14", "s15", "s16", "s17", "s18", "s19", "s20", "s21", "s22", "s23", "s24", "s25", "s26", "s27", "s28", "s29", "s30", "s31", "s36", "s37", "s38", "s39", "s40", "s41", "s42", "s43", "s44", "s45", "s46", "s47", "s48", "s49", "s50", "s51", "s52", "s53", "s54", "s55", "s56", "s57", "s58", "s59", "s60", "s61", "s62", "s63", "s64", "s65", "s66", "s67", "s68", "s69", "s70", "s71", "s72", "s73", "s74", "s75", "s76", "s77", "s78", "s79", "s80", "s81", "s82", "s83", "s84", "s85", "s86", "s87", "s88", "s89", "s90", "s91", "s92", "s93", "s94", "s95", "s96", "s97", "s98", "s99", "vcc", "scc", "memory");
}

extern "C" void kernel_launch(void* const* d_in, const int* in_sizes, int n_in,
                              void* d_out, int out_size, void* d_ws, size_t ws_size,
                              hipStream_t stream) {
  (void)hipFuncSetAttribute((const void*)qattn_kernel, hipFuncAttributeMaxDynamicSharedMemorySize, LDS_ATT);
  qattn_kernel<<<256, 256, LDS_ATT, stream>>>(d_in[0], d_in[1], d_in[2], d_in[3], d_ws, d_in[4], d_out);
}
```

```cpp
#include <hip/hip_runtime.h>
#include <hip/hip_bf16.h>

constexpr int LDS_ATT = 163840;

__global__ __launch_bounds__(256, 1) __attribute__((aligned(4096))) void qattn_kernel(const void* __restrict__ x, const void* __restrict__ kc, const void* __restrict__ vc,
                                                       const void* __restrict__ wq, void* __restrict__ ws, const void* __restrict__ wo,
                                                       void* __restrict__ out) {
  const unsigned blk = blockIdx.x;
  const unsigned wid = __builtin_amdgcn_readfirstlane(threadIdx.x >> 6);
  auto ka = __builtin_amdgcn_kernarg_segment_ptr();
  asm volatile(
R"A(s_load_dwordx8 s[4:11],%0,0x0
s_load_dwordx4 s[12:15],%0,0x20
s_load_dwordx2 s[16:17],%0,0x30
v_mbcnt_lo_u32_b32 v23,-1,0
v_mbcnt_hi_u32_b32 v23,-1,v23
s_mov_b32 s60,%2
s_and_b32 s66,%1,7
s_lshr_b32 s67,%1,3
s_getpc_b64 s[20:21]
L_pc_%=:
s_and_b32 s20,s20,0xffffff80
s_and_b32 s21,s21,0xffff
s_mov_b32 s22,L_hot_%=-L_pc_%=
s_mov_b32 s23,0x20000
s_lshl_b32 s68,s67,2
s_add_u32 s68,s68,s60
s_mul_i32 s68,s68,896
v_lshlrev_b32 v67,7,v23
s_mov_b64 exec,127
buffer_load_dword v8,v67,s[20:23],s68 offen
s_mov_b64 exec,-1
s_lshr_b32 s61,s66,2
s_lshl_b32 s61,s61,3
s_lshr_b32 s68,s67,2
s_add_u32 s61,s61,s68
s_and_b32 s62,s66,1
s_lshl_b32 s62,s62,2
s_and_b32 s68,s66,2
s_add_u32 s62,s62,s68
s_lshl_b32 s62,s62,1
s_and_b32 s68,s67,3
s_add_u32 s62,s62,s68
s_lshl_b32 s83,s62,9
s_mov_b32 s84,0
s_lshl_b32 s89,s60,13
s_add_u32 s85,s89,65536
s_add_u32 s88,s89,131072
v_and_b32 v20,3,v23
v_bfe_u32 v21,v23,2,2
v_lshrrev_b32 v22,4,v23
v_lshrrev_b32 v75,3,v23
s_lshl_b32 s66,s60,6
v_add_u32 v75,s66,v75
v_mul_u32_u24 v75,1280,v75
v_and_b32 v76,7,v23
v_or_b32 v77,0,v22
v_xor_b32 v77,v77,v76
v_lshl_add_u32 v77,v77,4,v75
v_add_u32 v32,0,v77
v_add_u32 v34,18432,v77
v_add_u32 v36,40960,v77
v_add_u32 v38,59392,v77
v_or_b32 v77,4,v22
v_xor_b32 v77,v77,v76
v_lshl_add_u32 v77,v77,4,v75
v_add_u32 v33,9216,v77
v_add_u32 v35,27648,v77
v_add_u32 v37,50176,v77
v_add_u32 v39,68608,v77
s_lshl_b32 s66,s60,17
v_lshlrev_b32 v75,13,v21
v_lshl_add_u32 v75,v22,6,v75
v_lshl_add_u32 v75,v20,4,v75
v_add_u32 v75,s66,v75
v_add_u32 v40,0,v75
v_add_u32 v41,256,v75
v_add_u32 v42,32768,v75
v_add_u32 v43,33024,v75
v_add_u32 v44,65536,v75
v_add_u32 v45,65792,v75
v_add_u32 v46,98304,v75
v_add_u32 v47,98560,v75
s_mul_i32 s66,s61,307200
s_waitcnt lgkmcnt(0)
s_add_u32 s28,s4,s66
s_addc_u32 s29,s5,0
s_and_b32 s29,s29,0xffff
s_mov_b32 s30,307200
s_mov_b32 s31,0x20000
s_mov_b32 s36,s10
s_and_b32 s37,s11,0xffff
s_mov_b32 s38,5242880
s_mov_b32 s39,0x20000
s_add_u32 m0,s85,0
s_nop 0
buffer_load_dwordx4 v32,s[28:31],s84 offen lds
buffer_load_dwordx4 v33,s[28:31],s84 offen offset:1024 lds
buffer_load_dwordx4 v34,s[28:31],s84 offen offset:2048 lds
buffer_load_dwordx4 v35,s[28:31],s84 offen offset:3072 lds
s_add_u32 m0,s85,4096
s_nop 0
buffer_load_dwordx4 v36,s[28:31],s84 offen lds
buffer_load_dwordx4 v37,s[28:31],s84 offen offset:1024 lds
buffer_load_dwordx4 v38,s[28:31],s84 offen offset:2048 lds
buffer_load_dwordx4 v39,s[28:31],s84 offen offset:3072 lds
s_add_u32 s84,s84,128
s_xor_b32 s85,s85,98304
s_add_u32 m0,s88,0
s_nop 0
buffer_load_dwordx4 v40,s[36:39],s83 offen lds
s_add_u32 m0,s88,2048
s_nop 0
buffer_load_dwordx4 v42,s[36:39],s83 offen lds
s_add_u32 m0,s88,4096
s_nop 0
buffer_load_dwordx4 v44,s[36:39],s83 offen lds
s_add_u32 m0,s88,6144
s_nop 0
buffer_load_dwordx4 v46,s[36:39],s83 offen lds
s_lshl_b32 s67,s61,21
s_add_u32 s68,s67,101711872
s_add_u32 s44,s12,s68
s_addc_u32 s45,s13,0
s_and_b32 s45,s45,0xffff
s_mov_b32 s46,4096
s_mov_b32 s47,0x20000
v_mov_b32 v10,0
buffer_load_dword v9,v10,s[44:47],0 offen sc1
v_lshlrev_b32 v6,4,v23
v_lshlrev_b32 v72,12,v22
v_lshl_add_u32 v72,v21,6,v72
v_lshl_add_u32 v72,v20,4,v72
v_subrev_u32 v51,s89,v72
v_add_u32 v50,131072,v72
v_and_b32 v74,15,v23
v_lshrrev_b32 v76,1,v74
v_lshlrev_b32 v75,7,v74
s_add_u32 s66,s89,65536
v_add_u32 v75,s66,v75
v_or_b32 v77,0,v22
v_xor_b32 v77,v77,v76
v_lshl_add_u32 v48,v77,4,v75
v_or_b32 v77,4,v22
v_xor_b32 v77,v77,v76
v_lshl_add_u32 v49,v77,4,v75
s_add_u32 s86,s89,131072
s_add_u32 s87,s89,98304
s_mov_b64 exec,0
s_cmp_eq_u32 s60,0
s_cbranch_scc1 L_pd1_%=
s_cmp_eq_u32 s60,1
s_cbranch_scc1 L_pd2_%=
s_cmp_eq_u32 s60,2
s_cbranch_scc1 L_pd3_%=
s_branch L_pd4_%=
L_trt0_%=:
s_mov_b64 exec,-1
s_add_u32 m0,s88,1024
s_nop 0
buffer_load_dwordx4 v41,s[36:39],s83 offen lds
s_add_u32 m0,s88,3072
s_nop 0
buffer_load_dwordx4 v43,s[36:39],s83 offen lds
s_add_u32 m0,s88,5120
s_nop 0
buffer_load_dwordx4 v45,s[36:39],s83 offen lds
s_add_u32 m0,s88,7168
s_nop 0
buffer_load_dwordx4 v47,s[36:39],s83 offen lds
s_add_u32 s83,s83,524288
s_mov_b32 s88,s87
s_add_u32 m0,s88,0
s_nop 0
buffer_load_dwordx4 v40,s[36:39],s83 offen lds
s_add_u32 m0,s88,1024
s_nop 0
buffer_load_dwordx4 v41,s[36:39],s83 offen lds
s_add_u32 m0,s88,2048
s_nop 0
buffer_load_dwordx4 v42,s[36:39],s83 offen lds
s_add_u32 m0,s88,3072
s_nop 0
buffer_load_dwordx4 v43,s[36:39],s83 offen lds
s_add_u32 m0,s88,4096
s_nop 0
buffer_load_dwordx4 v44,s[36:39],s83 offen lds
s_add_u32 m0,s88,5120
s_nop 0
buffer_load_dwordx4 v45,s[36:39],s83 offen lds
s_add_u32 m0,s88,6144
s_nop 0
buffer_load_dwordx4 v46,s[36:39],s83 offen lds
s_add_u32 m0,s88,7168
s_nop 0
buffer_load_dwordx4 v47,s[36:39],s83 offen lds
s_add_u32 s83,s83,524288
s_add_u32 s88,s89,0
s_mov_b64 exec,0
s_cmp_eq_u32 s60,0
s_cbranch_scc1 L_pd9_%=
s_cmp_eq_u32 s60,1
s_cbranch_scc1 L_pd10_%=
s_cmp_eq_u32 s60,2
s_cbranch_scc1 L_pd11_%=
s_branch L_pd12_%=
L_t1b_%=:
s_cmp_ge_u32 s62,8
s_cbranch_scc1 L_pd19_%=
s_branch L_pd14_%=
L_t1c_%=:
s_cmp_ge_u32 s62,8
s_cbranch_scc1 L_pd20_%=
s_branch L_pd15_%=
s_nop 0
s_nop 0
s_nop 0
s_nop 0
s_nop 0
s_nop 0
L_trt1_%=:
s_mov_b64 exec,-1
s_branch L_pacx_%=
L_pace_%=:
s_cmp_ge_u32 s4,8
s_cselect_b32 s5,3,1
s_sub_u32 s5,s4,s5
s_max_i32 s5,s5,s7
L_pacl_%=:
s_sub_u32 s4,s4,1
s_cmp_eq_u32 s4,0
s_cbranch_scc1 L_pace2_%=
s_nop 2
s_branch L_pacl_%=
L_pace2_%=:
s_setpc_b64 s[92:93]
L_pacx_%=:
s_waitcnt vmcnt(17)
ds_read_b128 a[192:195],v48
ds_read_b128 a[196:199],v49
ds_read_b128 a[200:203],v48 offset:2048
ds_read_b128 a[204:207],v49 offset:2048
ds_read_b128 a[208:211],v48 offset:4096
ds_read_b128 a[212:215],v49 offset:4096
ds_read_b128 a[216:219],v48 offset:6144
ds_read_b128 a[220:223],v49 offset:6144
s_waitcnt vmcnt(13)
s_barrier
ds_read_b64_tr_b16 a[64:65],v50
ds_read_b64_tr_b16 a[66:67],v50 offset:2048
ds_read_b64_tr_b16 a[68:69],v50 offset:16384
ds_read_b64_tr_b16 a[70:71],v50 offset:18432
s_waitcnt lgkmcnt(2)
v_mfma_f32_16x16x32_bf16 a[0:3],a[64:67],a[192:195],0
s_add_u32 m0,s85,0
ds_read_b64_tr_b16 a[72:73],v50 offset:8
v_xor_b32 v48,98304,v48
s_waitcnt lgkmcnt(1)
v_mfma_f32_16x16x32_bf16 a[0:3],a[68:71],a[196:199],a[0:3]
buffer_load_dwordx4 v32,s[28:31],s84 offen lds
v_mfma_f32_16x16x32_bf16 a[16:19],a[68:71],a[204:207],0
s_mov_b32 s69,s86
ds_read_b64_tr_b16 a[74:75],v50 offset:2056
v_xor_b32 v49,98304,v49
v_mfma_f32_16x16x32_bf16 a[16:19],a[64:67],a[200:203],a[16:19]
s_mov_b32 s86,s87
s_mov_b32 s87,s88
ds_read_b64_tr_b16 a[76:77],v50 offset:16392
v_mfma_f32_16x16x32_bf16 a[32:35],a[64:67],a[208:211],0
ds_read_b64_tr_b16 a[78:79],v50 offset:18440
v_mfma_f32_16x16x32_bf16 a[32:35],a[68:71],a[212:215],a[32:35]
ds_read_b64_tr_b16 a[80:81],v50 offset:256
v_mfma_f32_16x16x32_bf16 a[48:51],a[68:71],a[220:223],0
buffer_load_dwordx4 v33,s[28:31],s84 offen offset:1024 lds
v_mfma_f32_16x16x32_bf16 a[48:51],a[64:67],a[216:219],a[48:51]
s_waitcnt vmcnt(14) lgkmcnt(1)
ds_read_b64_tr_b16 a[82:83],v50 offset:2304
v_mfma_f32_16x16x32_bf16 a[4:7],a[72:75],a[192:195],0
s_lshl_b32 s65,s61,5
ds_read_b64_tr_b16 a[84:85],v50 offset:16640
v_cmp_lt_u32 vcc,6,v23
v_mfma_f32_16x16x32_bf16 a[4:7],a[76:79],a[196:199],a[4:7]
s_lshl_b32 s66,s62,1
ds_read_b64_tr_b16 a[86:87],v50 offset:18688
v_mov_b32 v17,0
v_mfma_f32_16x16x32_bf16 a[20:23],a[76:79],a[204:207],0
s_add_u32 s65,s65,s66
s_lshl_b32 s66,s65,18
v_readfirstlane_b32 s52,v9
v_mfma_f32_16x16x32_bf16 a[20:23],a[72:75],a[200:203],a[20:23]
buffer_load_dwordx4 v34,s[28:31],s84 offen offset:2048 lds
v_mfma_f32_16x16x32_bf16 a[36:39],a[72:75],a[208:211],0
s_add_u32 s20,s6,s66
s_addc_u32 s21,s7,0
ds_read_b64_tr_b16 a[88:89],v50 offset:264
v_mfma_f32_16x16x32_bf16 a[36:39],a[76:79],a[212:215],a[36:39]
s_and_b32 s21,s21,0xffff
s_mov_b32 s22,524288
ds_read_b64_tr_b16 a[90:91],v50 offset:2312
v_mfma_f32_16x16x32_bf16 a[52:55],a[76:79],a[220:223],0
s_mov_b32 s23,0x20000
s_mov_b32 s26,524288
ds_read_b64_tr_b16 a[92:93],v50 offset:16648
v_mfma_f32_16x16x32_bf16 a[52:55],a[72:75],a[216:219],a[52:55]
s_add_u32 s24,s8,s66
s_addc_u32 s25,s9,0
ds_read_b64_tr_b16 a[94:95],v50 offset:18696
s_waitcnt lgkmcnt(6)
v_mfma_f32_16x16x32_bf16 a[8:11],a[80:83],a[192:195],0
buffer_load_dwordx4 v35,s[28:31],s84 offen offset:3072 lds
s_waitcnt lgkmcnt(4)
v_mfma_f32_16x16x32_bf16 a[8:11],a[84:87],a[196:199],a[8:11]
s_and_b32 s25,s25,0xffff
s_mov_b32 s27,0x20000
ds_read_b64_tr_b16 a[96:97],v50 offset:512
v_mfma_f32_16x16x32_bf16 a[24:27],a[84:87],a[204:207],0
s_lshl_b32 s66,s65,16
s_mov_b32 s42,131072
ds_read_b64_tr_b16 a[98:99],v50 offset:2560
v_mfma_f32_16x16x32_bf16 a[24:27],a[80:83],a[200:203],a[24:27]
s_add_u32 m0,s85,4096
s_xor_b32 s85,s85,98304
ds_read_b64_tr_b16 a[100:101],v50 offset:16896
v_mfma_f32_16x16x32_bf16 a[40:43],a[80:83],a[208:211],0
s_add_u32 s40,s12,s66
s_addc_u32 s41,s13,0
s_and_b32 s41,s41,0xffff
v_mfma_f32_16x16x32_bf16 a[40:43],a[84:87],a[212:215],a[40:43]
buffer_load_dwordx4 v36,s[28:31],s84 offen lds
v_mfma_f32_16x16x32_bf16 a[56:59],a[84:87],a[220:223],0
s_mov_b32 s43,0x20000
s_mov_b32 s48,s14
ds_read_b64_tr_b16 a[102:103],v50 offset:18944
v_mfma_f32_16x16x32_bf16 a[56:59],a[80:83],a[216:219],a[56:59]
s_and_b32 s49,s15,0xffff
s_mov_b32 s50,5242880
ds_read_b64_tr_b16 a[104:105],v50 offset:520
s_waitcnt lgkmcnt(5)
v_mfma_f32_16x16x32_bf16 a[12:15],a[88:91],a[192:195],0
s_mov_b32 s51,0x20000
s_lshl_b32 s67,s60,12
ds_read_b64_tr_b16 a[106:107],v50 offset:2568
v_mfma_f32_16x16x32_bf16 a[12:15],a[92:95],a[196:199],a[12:15]
s_add_u32 s76,s67,98304
s_mul_i32 s74,s61,5
ds_read_b64_tr_b16 a[108:109],v50 offset:16904
v_mfma_f32_16x16x32_bf16 a[28:31],a[92:95],a[204:207],0
buffer_load_dwordx4 v37,s[28:31],s84 offen offset:1024 lds
v_mfma_f32_16x16x32_bf16 a[28:31],a[88:91],a[200:203],a[28:31]
s_add_u32 s74,s74,s62
s_and_b32 s74,s74,15
ds_read_b64_tr_b16 a[110:111],v50 offset:18952
v_mfma_f32_16x16x32_bf16 a[44:47],a[88:91],a[208:211],0
s_lshl_b32 s74,s74,14
s_mov_b32 s75,0
ds_read_b64_tr_b16 a[112:113],v50 offset:768
v_mfma_f32_16x16x32_bf16 a[44:47],a[92:95],a[212:215],a[44:47]
s_mov_b32 s77,98304
s_mov_b32 s71,0x3e0293ee
ds_read_b64_tr_b16 a[114:115],v50 offset:2816
v_mfma_f32_16x16x32_bf16 a[60:63],a[92:95],a[220:223],0
s_mov_b32 s73,0x53800000
s_lshl_b32 s79,s60,14
s_add_u32 s80,s79,4096
v_mfma_f32_16x16x32_bf16 a[60:63],a[88:91],a[216:219],a[60:63]
buffer_load_dwordx4 v38,s[28:31],s84 offen offset:2048 lds
s_waitcnt lgkmcnt(5)
v_mfma_f32_16x16x32_bf16 v[64:67],a[96:99],a[192:195],0
s_add_u32 s81,s79,8192
s_add_u32 s82,s79,12288
ds_read_b64_tr_b16 a[116:117],v50 offset:17152
v_mfma_f32_16x16x32_bf16 v[64:67],a[100:103],a[196:199],v[64:67]
s_lshr_b32 s63,s62,2
s_and_b32 s64,s62,3
ds_read_b64_tr_b16 a[118:119],v50 offset:19200
v_mfma_f32_16x16x32_bf16 v[80:83],a[100:103],a[204:207],0
s_mov_b32 s72,0xff800000
s_lshl_b32 s18,s60,24
ds_read_b64_tr_b16 a[120:121],v50 offset:776
v_mfma_f32_16x16x32_bf16 v[80:83],a[96:99],a[200:203],v[80:83]
s_lshl_b32 s68,s62,6
s_add_u32 s18,s18,s68
ds_read_b64_tr_b16 a[122:123],v50 offset:2824
v_mfma_f32_16x16x32_bf16 v[96:99],a[96:99],a[208:211],0
buffer_load_dwordx4 v39,s[28:31],s84 offen offset:3072 lds
v_mfma_f32_16x16x32_bf16 v[96:99],a[100:103],a[212:215],v[96:99]
s_add_u32 s84,s84,128
ds_read_b64_tr_b16 a[124:125],v50 offset:17160
v_lshlrev_b32 v19,3,v6
v_mfma_f32_16x16x32_bf16 v[112:115],a[100:103],a[220:223],0
s_cmp_eq_u32 s60,3
s_cselect_b32 s72,0x7149f2ca,s72
ds_read_b64_tr_b16 a[126:127],v50 offset:19208
v_mfma_f32_16x16x32_bf16 v[112:115],a[96:99],a[216:219],v[112:115]
s_add_u32 m0,s88,0
v_cndmask_b32_e64 v19,v19,-1,vcc
v_lshrrev_b32 v18,1,v19
s_waitcnt lgkmcnt(6)
v_mfma_f32_16x16x32_bf16 v[68:71],a[104:107],a[192:195],0
s_lshl_b32 s90,%1,2
s_add_u32 s90,s90,s60
v_min_u32 v19,v19,v19
v_mfma_f32_16x16x32_bf16 v[68:71],a[108:111],a[196:199],v[68:71]
buffer_load_dwordx4 v40,s[36:39],s83 offen lds
v_mfma_f32_16x16x32_bf16 v[84:87],a[108:111],a[204:207],0
s_mul_i32 s90,s90,5120
s_mul_i32 s58,s61,307200
v_cndmask_b32_e64 v19,v19,v18,vcc
v_mfma_f32_16x16x32_bf16 v[84:87],a[104:107],a[200:203],v[84:87]
s_add_u32 s56,s40,52224
s_addc_u32 s57,s41,0
s_add_u32 s52,s52,1
v_mfma_f32_16x16x32_bf16 v[100:103],a[104:107],a[208:211],0
)A"
R"A(s_add_u32 m0,s88,1024
s_add_u32 s58,s16,s58
s_addc_u32 s59,s17,0
v_mfma_f32_16x16x32_bf16 v[100:103],a[108:111],a[212:215],v[100:103]
s_cmp_eq_u32 s60,1
s_cselect_b64 s[56:57],s[58:59],s[56:57]
s_xor_b32 s53,s52,0x9e3779b9
v_mfma_f32_16x16x32_bf16 v[116:119],a[108:111],a[220:223],0
buffer_load_dwordx4 v41,s[36:39],s83 offen lds
v_mfma_f32_16x16x32_bf16 v[116:119],a[104:107],a[216:219],v[116:119]
s_cmp_eq_u32 s60,2
s_cselect_b64 s[56:57],s[20:21],s[56:57]
v_lshlrev_b32 v11,8,v21
s_waitcnt vmcnt(18)
s_barrier
v_mfma_f32_16x16x32_bf16 v[72:75],a[112:115],a[192:195],0
s_add_u32 m0,s88,2048
ds_read_b64_tr_b16 a[64:65],v50 offset:1024
v_lshl_add_u32 v11,v22,6,v11
s_waitcnt lgkmcnt(1)
v_mfma_f32_16x16x32_bf16 v[72:75],a[116:119],a[196:199],v[72:75]
s_cmp_eq_u32 s60,3
s_cselect_b64 s[56:57],s[24:25],s[56:57]
ds_read_b64_tr_b16 a[66:67],v50 offset:3072
v_mfma_f32_16x16x32_bf16 v[88:91],a[116:119],a[204:207],0
buffer_load_dwordx4 v42,s[36:39],s83 offen lds
v_mfma_f32_16x16x32_bf16 v[88:91],a[112:115],a[200:203],v[88:91]
ds_read_b64_tr_b16 a[68:69],v50 offset:17408
v_add_u32 v11,s67,v11
v_mfma_f32_16x16x32_bf16 v[104:107],a[112:115],a[208:211],0
ds_read_b64_tr_b16 a[70:71],v50 offset:19456
v_lshl_add_u32 v4,v20,4,v11
v_mfma_f32_16x16x32_bf16 v[104:107],a[116:119],a[212:215],v[104:107]
s_add_u32 m0,s88,3072
ds_read_b64_tr_b16 a[72:73],v50 offset:1032
v_xor_b32 v12,2,v20
v_mfma_f32_16x16x32_bf16 v[120:123],a[116:119],a[220:223],0
ds_read_b64_tr_b16 a[74:75],v50 offset:3080
v_lshl_add_u32 v5,v12,4,v11
v_mfma_f32_16x16x32_bf16 v[120:123],a[112:115],a[216:219],v[120:123]
buffer_load_dwordx4 v43,s[36:39],s83 offen lds
v_mfma_f32_16x16x32_bf16 v[76:79],a[120:123],a[192:195],0
ds_read_b64_tr_b16 a[76:77],v50 offset:17416
v_lshlrev_b32 v11,10,v21
v_mfma_f32_16x16x32_bf16 v[76:79],a[124:127],a[196:199],v[76:79]
ds_read_b64_tr_b16 a[78:79],v50 offset:19464
v_lshl_add_u32 v11,v20,6,v11
v_mfma_f32_16x16x32_bf16 v[92:95],a[124:127],a[204:207],0
s_add_u32 m0,s88,4096
ds_read_b64_tr_b16 a[80:81],v50 offset:1280
v_and_b32 v12,2,v21
v_mfma_f32_16x16x32_bf16 v[92:95],a[120:123],a[200:203],v[92:95]
v_xor_b32 v12,v12,v22
v_lshl_add_u32 v11,v12,4,v11
v_mfma_f32_16x16x32_bf16 v[108:111],a[120:123],a[208:211],0
buffer_load_dwordx4 v44,s[36:39],s83 offen lds
v_mfma_f32_16x16x32_bf16 v[108:111],a[124:127],a[212:215],v[108:111]
ds_read_b64_tr_b16 a[82:83],v50 offset:3328
v_add_u32 v1,98304,v11
v_mfma_f32_16x16x32_bf16 v[124:127],a[124:127],a[220:223],0
s_waitcnt lgkmcnt(4)
ds_read_b64_tr_b16 a[84:85],v50 offset:17664
v_lshlrev_b32 v11,10,v22
v_mfma_f32_16x16x32_bf16 v[124:127],a[120:123],a[216:219],v[124:127]
s_add_u32 m0,s88,5120
ds_read_b64_tr_b16 a[86:87],v50 offset:19712
v_lshl_add_u32 v11,v21,6,v11
v_mfma_f32_16x16x32_bf16 v[128:131],a[64:67],a[192:195],0
ds_read_b64_tr_b16 a[88:89],v50 offset:1288
v_lshl_add_u32 v11,v20,3,v11
v_mfma_f32_16x16x32_bf16 v[128:131],a[68:71],a[196:199],v[128:131]
buffer_load_dwordx4 v45,s[36:39],s83 offen lds
v_mfma_f32_16x16x32_bf16 v[160:163],a[68:71],a[204:207],0
ds_read_b64_tr_b16 a[90:91],v50 offset:3336
v_add_u32 v11,114688,v11
v_mfma_f32_16x16x32_bf16 v[160:163],a[64:67],a[200:203],v[160:163]
ds_read_b64_tr_b16 a[92:93],v50 offset:17672
v_and_b32 v12,1,v22
v_mfma_f32_16x16x32_bf16 v[192:195],a[64:67],a[208:211],0
s_add_u32 m0,s88,6144
ds_read_b64_tr_b16 a[94:95],v50 offset:19720
v_lshl_add_u32 v2,v12,5,v11
v_mfma_f32_16x16x32_bf16 v[192:195],a[68:71],a[212:215],v[192:195]
v_xor_b32 v12,1,v12
v_lshl_add_u32 v3,v12,5,v11
v_mfma_f32_16x16x32_bf16 v[224:227],a[68:71],a[220:223],0
buffer_load_dwordx4 v46,s[36:39],s83 offen lds
v_mfma_f32_16x16x32_bf16 v[224:227],a[64:67],a[216:219],v[224:227]
ds_read_b64_tr_b16 a[96:97],v50 offset:1536
v_mov_b32 v28,0x3f803f80
v_mfma_f32_16x16x32_bf16 v[132:135],a[72:75],a[192:195],0
s_waitcnt lgkmcnt(4)
ds_read_b64_tr_b16 a[98:99],v50 offset:3584
v_mov_b32 v29,0x3f803f80
v_mfma_f32_16x16x32_bf16 v[132:135],a[76:79],a[196:199],v[132:135]
s_add_u32 m0,s88,7168
s_mov_b32 s88,s69
ds_read_b64_tr_b16 a[100:101],v50 offset:17920
v_mfma_f32_16x16x32_bf16 v[164:167],a[76:79],a[204:207],0
ds_read_b64_tr_b16 a[102:103],v50 offset:19968
v_mov_b32 v30,0x3f803f80
v_mfma_f32_16x16x32_bf16 v[164:167],a[72:75],a[200:203],v[164:167]
buffer_load_dwordx4 v47,s[36:39],s83 offen lds
v_mfma_f32_16x16x32_bf16 v[196:199],a[72:75],a[208:211],0
s_add_u32 s83,s83,524288
ds_read_b64_tr_b16 a[104:105],v50 offset:1544
v_mov_b32 v31,0x3f803f80
v_mfma_f32_16x16x32_bf16 v[196:199],a[76:79],a[212:215],v[196:199]
ds_read_b64_tr_b16 a[106:107],v50 offset:3592
v_mfma_f32_16x16x32_bf16 v[228:231],a[76:79],a[220:223],0
s_add_u32 m0,s85,0
ds_read_b64_tr_b16 a[108:109],v50 offset:17928
v_mfma_f32_16x16x32_bf16 v[228:231],a[72:75],a[216:219],v[228:231]
v_mfma_f32_16x16x32_bf16 v[136:139],a[80:83],a[192:195],0
buffer_load_dwordx4 v32,s[28:31],s84 offen lds
v_mfma_f32_16x16x32_bf16 v[136:139],a[84:87],a[196:199],v[136:139]
ds_read_b64_tr_b16 a[110:111],v50 offset:19976
v_mfma_f32_16x16x32_bf16 v[168:171],a[84:87],a[204:207],0
ds_read_b64_tr_b16 a[112:113],v50 offset:1792
v_mfma_f32_16x16x32_bf16 v[168:171],a[80:83],a[200:203],v[168:171]
ds_read_b64_tr_b16 a[114:115],v50 offset:3840
v_mfma_f32_16x16x32_bf16 v[200:203],a[80:83],a[208:211],0
ds_read_b64_tr_b16 a[116:117],v50 offset:18176
v_mfma_f32_16x16x32_bf16 v[200:203],a[84:87],a[212:215],v[200:203]
buffer_load_dwordx4 v33,s[28:31],s84 offen offset:1024 lds
v_mfma_f32_16x16x32_bf16 v[232:235],a[84:87],a[220:223],0
ds_read_b64_tr_b16 a[118:119],v50 offset:20224
v_mfma_f32_16x16x32_bf16 v[232:235],a[80:83],a[216:219],v[232:235]
s_waitcnt lgkmcnt(5)
ds_read_b64_tr_b16 a[120:121],v50 offset:1800
v_mfma_f32_16x16x32_bf16 v[140:143],a[88:91],a[192:195],0
ds_read_b64_tr_b16 a[122:123],v50 offset:3848
v_mfma_f32_16x16x32_bf16 v[140:143],a[92:95],a[196:199],v[140:143]
v_mfma_f32_16x16x32_bf16 v[172:175],a[92:95],a[204:207],0
buffer_load_dwordx4 v34,s[28:31],s84 offen offset:2048 lds
v_mfma_f32_16x16x32_bf16 v[172:175],a[88:91],a[200:203],v[172:175]
ds_read_b64_tr_b16 a[124:125],v50 offset:18184
v_mfma_f32_16x16x32_bf16 v[204:207],a[88:91],a[208:211],0
ds_read_b64_tr_b16 a[126:127],v50 offset:20232
v_add_u32 v50,s86,v51
v_mfma_f32_16x16x32_bf16 v[204:207],a[92:95],a[212:215],v[204:207]
v_mfma_f32_16x16x32_bf16 v[236:239],a[92:95],a[220:223],0
v_mfma_f32_16x16x32_bf16 v[236:239],a[88:91],a[216:219],v[236:239]
buffer_load_dwordx4 v35,s[28:31],s84 offen offset:3072 lds
s_waitcnt vmcnt(20) lgkmcnt(0)
s_barrier
v_mfma_f32_16x16x32_bf16 v[144:147],a[96:99],a[192:195],0
s_waitcnt vmcnt(12)
ds_read_b128 a[224:227],v48
v_mfma_f32_16x16x32_bf16 v[144:147],a[100:103],a[196:199],v[144:147]
s_add_u32 m0,s85,4096
s_xor_b32 s85,s85,98304
ds_read_b128 a[228:231],v49
v_mfma_f32_16x16x32_bf16 v[176:179],a[100:103],a[204:207],0
ds_read_b128 a[232:235],v48 offset:2048
v_mfma_f32_16x16x32_bf16 v[176:179],a[96:99],a[200:203],v[176:179]
buffer_load_dwordx4 v36,s[28:31],s84 offen lds
v_mfma_f32_16x16x32_bf16 v[208:211],a[96:99],a[208:211],0
ds_read_b128 a[236:239],v49 offset:2048
v_mfma_f32_16x16x32_bf16 v[208:211],a[100:103],a[212:215],v[208:211]
ds_read_b128 a[240:243],v48 offset:4096
v_mfma_f32_16x16x32_bf16 v[240:243],a[100:103],a[220:223],0
ds_read_b128 a[244:247],v49 offset:4096
v_mfma_f32_16x16x32_bf16 v[240:243],a[96:99],a[216:219],v[240:243]
ds_read_b128 a[248:251],v48 offset:6144
v_mfma_f32_16x16x32_bf16 v[148:151],a[104:107],a[192:195],0
buffer_load_dwordx4 v37,s[28:31],s84 offen offset:1024 lds
v_mfma_f32_16x16x32_bf16 v[148:151],a[108:111],a[196:199],v[148:151]
ds_read_b128 a[252:255],v49 offset:6144
v_mfma_f32_16x16x32_bf16 v[180:183],a[108:111],a[204:207],0
ds_read_b64_tr_b16 a[64:65],v50
v_mfma_f32_16x16x32_bf16 v[180:183],a[104:107],a[200:203],v[180:183]
ds_read_b64_tr_b16 a[66:67],v50 offset:2048
v_mfma_f32_16x16x32_bf16 v[212:215],a[104:107],a[208:211],0
v_mfma_f32_16x16x32_bf16 v[212:215],a[108:111],a[212:215],v[212:215]
buffer_load_dwordx4 v38,s[28:31],s84 offen offset:2048 lds
v_mfma_f32_16x16x32_bf16 v[244:247],a[108:111],a[220:223],0
ds_read_b64_tr_b16 a[68:69],v50 offset:16384
v_mfma_f32_16x16x32_bf16 v[244:247],a[104:107],a[216:219],v[244:247]
ds_read_b64_tr_b16 a[70:71],v50 offset:18432
v_mfma_f32_16x16x32_bf16 v[152:155],a[112:115],a[192:195],0
ds_read_b64_tr_b16 a[72:73],v50 offset:8
v_mfma_f32_16x16x32_bf16 v[152:155],a[116:119],a[196:199],v[152:155]
ds_read_b64_tr_b16 a[74:75],v50 offset:2056
v_mfma_f32_16x16x32_bf16 v[184:187],a[116:119],a[204:207],0
buffer_load_dwordx4 v39,s[28:31],s84 offen offset:3072 lds
v_mfma_f32_16x16x32_bf16 v[184:187],a[112:115],a[200:203],v[184:187]
s_add_u32 s84,s84,128
ds_read_b64_tr_b16 a[76:77],v50 offset:16392
v_mfma_f32_16x16x32_bf16 v[216:219],a[112:115],a[208:211],0
ds_read_b64_tr_b16 a[78:79],v50 offset:18440
v_mfma_f32_16x16x32_bf16 v[216:219],a[116:119],a[212:215],v[216:219]
ds_read_b64_tr_b16 a[80:81],v50 offset:256
v_mfma_f32_16x16x32_bf16 v[248:251],a[116:119],a[220:223],0
v_mfma_f32_16x16x32_bf16 v[248:251],a[112:115],a[216:219],v[248:251]
ds_read_b64_tr_b16 a[82:83],v50 offset:2304
v_mfma_f32_16x16x32_bf16 v[156:159],a[120:123],a[192:195],0
global_load_dword a184,v17,s[56:57] nt
ds_read_b64_tr_b16 a[84:85],v50 offset:16640
v_mfma_f32_16x16x32_bf16 v[156:159],a[124:127],a[196:199],v[156:159]
ds_read_b64_tr_b16 a[86:87],v50 offset:18688
v_mfma_f32_16x16x32_bf16 v[188:191],a[124:127],a[204:207],0
v_mfma_f32_16x16x32_bf16 v[188:191],a[120:123],a[200:203],v[188:191]
ds_read_b64_tr_b16 a[88:89],v50 offset:264
v_mfma_f32_16x16x32_bf16 v[220:223],a[120:123],a[208:211],0
ds_read_b64_tr_b16 a[90:91],v50 offset:2312
v_mfma_f32_16x16x32_bf16 v[220:223],a[124:127],a[212:215],v[220:223]
ds_read_b64_tr_b16 a[92:93],v50 offset:16648
v_mfma_f32_16x16x32_bf16 v[252:255],a[124:127],a[220:223],0
v_mfma_f32_16x16x32_bf16 v[252:255],a[120:123],a[216:219],v[252:255]
ds_read_b64_tr_b16 a[94:95],v50 offset:18696
s_mov_b32 s70,3
s_memrealtime s[8:9]
s_memtime s[4:5]
s_nop 0
L_qp_%=:
s_waitcnt lgkmcnt(6)
v_mfma_f32_16x16x32_bf16 a[0:3],a[64:67],a[224:227],a[0:3]
s_add_u32 m0,s88,0
ds_read_b64_tr_b16 a[96:97],v50 offset:512
v_xor_b32 v48,98304,v48
L_pd1_%=:
s_cbranch_execz L_pd5_%=
v_mfma_f32_16x16x32_bf16 a[0:3],a[68:71],a[228:231],a[0:3]
buffer_load_dwordx4 v40,s[36:39],s83 offen lds
v_mfma_f32_16x16x32_bf16 a[16:19],a[68:71],a[236:239],a[16:19]
s_mov_b32 s69,s86
ds_read_b64_tr_b16 a[98:99],v50 offset:2560
v_xor_b32 v49,98304,v49
v_mfma_f32_16x16x32_bf16 a[16:19],a[64:67],a[232:235],a[16:19]
s_mov_b32 s86,s87
s_mov_b32 s87,s88
ds_read_b64_tr_b16 a[100:101],v50 offset:16896
v_mfma_f32_16x16x32_bf16 a[32:35],a[64:67],a[240:243],a[32:35]
ds_read_b64_tr_b16 a[102:103],v50 offset:18944
v_mfma_f32_16x16x32_bf16 a[32:35],a[68:71],a[244:247],a[32:35]
ds_read_b64_tr_b16 a[104:105],v50 offset:520
v_mfma_f32_16x16x32_bf16 a[48:51],a[68:71],a[252:255],a[48:51]
s_add_u32 m0,s88,1024
v_mfma_f32_16x16x32_bf16 a[48:51],a[64:67],a[248:251],a[48:51]
ds_read_b64_tr_b16 a[106:107],v50 offset:2568
v_mfma_f32_16x16x32_bf16 a[4:7],a[72:75],a[224:227],a[4:7]
buffer_load_dwordx4 v41,s[36:39],s83 offen lds
v_mfma_f32_16x16x32_bf16 a[4:7],a[76:79],a[228:231],a[4:7]
ds_read_b64_tr_b16 a[108:109],v50 offset:16904
v_mfma_f32_16x16x32_bf16 a[20:23],a[76:79],a[236:239],a[20:23]
ds_read_b64_tr_b16 a[110:111],v50 offset:18952
v_mfma_f32_16x16x32_bf16 a[20:23],a[72:75],a[232:235],a[20:23]
ds_read_b64_tr_b16 a[112:113],v50 offset:768
v_mfma_f32_16x16x32_bf16 a[36:39],a[72:75],a[240:243],a[36:39]
ds_read_b64_tr_b16 a[114:115],v50 offset:2816
v_mfma_f32_16x16x32_bf16 a[36:39],a[76:79],a[244:247],a[36:39]
s_add_u32 m0,s88,2048
v_mfma_f32_16x16x32_bf16 a[52:55],a[76:79],a[252:255],a[52:55]
ds_read_b64_tr_b16 a[116:117],v50 offset:17152
v_mfma_f32_16x16x32_bf16 a[52:55],a[72:75],a[248:251],a[52:55]
buffer_load_dwordx4 v42,s[36:39],s83 offen lds
v_mfma_f32_16x16x32_bf16 a[8:11],a[80:83],a[224:227],a[8:11]
s_waitcnt lgkmcnt(5)
ds_read_b64_tr_b16 a[118:119],v50 offset:19200
v_mfma_f32_16x16x32_bf16 a[8:11],a[84:87],a[228:231],a[8:11]
ds_read_b64_tr_b16 a[120:121],v50 offset:776
)A"
R"A(v_mfma_f32_16x16x32_bf16 a[24:27],a[84:87],a[236:239],a[24:27]
ds_read_b64_tr_b16 a[122:123],v50 offset:2824
v_mfma_f32_16x16x32_bf16 a[24:27],a[80:83],a[232:235],a[24:27]
ds_read_b64_tr_b16 a[124:125],v50 offset:17160
v_mfma_f32_16x16x32_bf16 a[40:43],a[80:83],a[240:243],a[40:43]
s_add_u32 m0,s88,3072
v_mfma_f32_16x16x32_bf16 a[40:43],a[84:87],a[244:247],a[40:43]
ds_read_b64_tr_b16 a[126:127],v50 offset:19208
v_mfma_f32_16x16x32_bf16 a[56:59],a[84:87],a[252:255],a[56:59]
buffer_load_dwordx4 v43,s[36:39],s83 offen lds
v_mfma_f32_16x16x32_bf16 a[56:59],a[80:83],a[248:251],a[56:59]
ds_read_b64_tr_b16 a[64:65],v50 offset:1024
v_mfma_f32_16x16x32_bf16 a[12:15],a[88:91],a[224:227],a[12:15]
ds_read_b64_tr_b16 a[66:67],v50 offset:3072
v_mfma_f32_16x16x32_bf16 a[12:15],a[92:95],a[228:231],a[12:15]
ds_read_b64_tr_b16 a[68:69],v50 offset:17408
v_mfma_f32_16x16x32_bf16 a[28:31],a[92:95],a[236:239],a[28:31]
ds_read_b64_tr_b16 a[70:71],v50 offset:19456
v_mfma_f32_16x16x32_bf16 a[28:31],a[88:91],a[232:235],a[28:31]
s_add_u32 m0,s88,4096
v_mfma_f32_16x16x32_bf16 a[44:47],a[88:91],a[240:243],a[44:47]
ds_read_b64_tr_b16 a[72:73],v50 offset:1032
v_mfma_f32_16x16x32_bf16 a[44:47],a[92:95],a[244:247],a[44:47]
buffer_load_dwordx4 v44,s[36:39],s83 offen lds
v_mfma_f32_16x16x32_bf16 a[60:63],a[92:95],a[252:255],a[60:63]
ds_read_b64_tr_b16 a[74:75],v50 offset:3080
v_mfma_f32_16x16x32_bf16 a[60:63],a[88:91],a[248:251],a[60:63]
ds_read_b64_tr_b16 a[76:77],v50 offset:17416
v_mfma_f32_16x16x32_bf16 v[64:67],a[96:99],a[224:227],v[64:67]
ds_read_b64_tr_b16 a[78:79],v50 offset:19464
v_mfma_f32_16x16x32_bf16 v[64:67],a[100:103],a[228:231],v[64:67]
ds_read_b64_tr_b16 a[80:81],v50 offset:1280
v_mfma_f32_16x16x32_bf16 v[80:83],a[100:103],a[236:239],v[80:83]
s_add_u32 m0,s88,5120
v_mfma_f32_16x16x32_bf16 v[80:83],a[96:99],a[232:235],v[80:83]
ds_read_b64_tr_b16 a[82:83],v50 offset:3328
v_mfma_f32_16x16x32_bf16 v[96:99],a[96:99],a[240:243],v[96:99]
buffer_load_dwordx4 v45,s[36:39],s83 offen lds
v_mfma_f32_16x16x32_bf16 v[96:99],a[100:103],a[244:247],v[96:99]
ds_read_b64_tr_b16 a[84:85],v50 offset:17664
v_mfma_f32_16x16x32_bf16 v[112:115],a[100:103],a[252:255],v[112:115]
ds_read_b64_tr_b16 a[86:87],v50 offset:19712
v_mfma_f32_16x16x32_bf16 v[112:115],a[96:99],a[248:251],v[112:115]
ds_read_b64_tr_b16 a[88:89],v50 offset:1288
v_mfma_f32_16x16x32_bf16 v[68:71],a[104:107],a[224:227],v[68:71]
s_waitcnt lgkmcnt(5)
ds_read_b64_tr_b16 a[90:91],v50 offset:3336
v_mfma_f32_16x16x32_bf16 v[68:71],a[108:111],a[228:231],v[68:71]
s_add_u32 m0,s88,6144
v_mfma_f32_16x16x32_bf16 v[84:87],a[108:111],a[236:239],v[84:87]
ds_read_b64_tr_b16 a[92:93],v50 offset:17672
v_mfma_f32_16x16x32_bf16 v[84:87],a[104:107],a[232:235],v[84:87]
buffer_load_dwordx4 v46,s[36:39],s83 offen lds
v_mfma_f32_16x16x32_bf16 v[100:103],a[104:107],a[240:243],v[100:103]
ds_read_b64_tr_b16 a[94:95],v50 offset:19720
v_mfma_f32_16x16x32_bf16 v[100:103],a[108:111],a[244:247],v[100:103]
ds_read_b64_tr_b16 a[96:97],v50 offset:1536
v_mfma_f32_16x16x32_bf16 v[116:119],a[108:111],a[252:255],v[116:119]
ds_read_b64_tr_b16 a[98:99],v50 offset:3584
v_mfma_f32_16x16x32_bf16 v[116:119],a[104:107],a[248:251],v[116:119]
ds_read_b64_tr_b16 a[100:101],v50 offset:17920
v_mfma_f32_16x16x32_bf16 v[72:75],a[112:115],a[224:227],v[72:75]
s_add_u32 m0,s88,7168
s_mov_b32 s88,s69
s_mov_b32 s69,s86
v_mfma_f32_16x16x32_bf16 v[72:75],a[116:119],a[228:231],v[72:75]
ds_read_b64_tr_b16 a[102:103],v50 offset:19968
v_mfma_f32_16x16x32_bf16 v[88:91],a[116:119],a[236:239],v[88:91]
buffer_load_dwordx4 v47,s[36:39],s83 offen lds
v_mfma_f32_16x16x32_bf16 v[88:91],a[112:115],a[232:235],v[88:91]
s_add_u32 s83,s83,524288
ds_read_b64_tr_b16 a[104:105],v50 offset:1544
v_mfma_f32_16x16x32_bf16 v[104:107],a[112:115],a[240:243],v[104:107]
ds_read_b64_tr_b16 a[106:107],v50 offset:3592
v_mfma_f32_16x16x32_bf16 v[104:107],a[116:119],a[244:247],v[104:107]
ds_read_b64_tr_b16 a[108:109],v50 offset:17928
v_mfma_f32_16x16x32_bf16 v[120:123],a[116:119],a[252:255],v[120:123]
ds_read_b64_tr_b16 a[110:111],v50 offset:19976
v_mfma_f32_16x16x32_bf16 v[120:123],a[112:115],a[248:251],v[120:123]
s_add_u32 m0,s85,0
v_mfma_f32_16x16x32_bf16 v[76:79],a[120:123],a[224:227],v[76:79]
ds_read_b64_tr_b16 a[112:113],v50 offset:1792
v_mfma_f32_16x16x32_bf16 v[76:79],a[124:127],a[228:231],v[76:79]
buffer_load_dwordx4 v32,s[28:31],s84 offen lds
v_mfma_f32_16x16x32_bf16 v[92:95],a[124:127],a[236:239],v[92:95]
ds_read_b64_tr_b16 a[114:115],v50 offset:3840
v_mfma_f32_16x16x32_bf16 v[92:95],a[120:123],a[232:235],v[92:95]
ds_read_b64_tr_b16 a[116:117],v50 offset:18176
v_mfma_f32_16x16x32_bf16 v[108:111],a[120:123],a[240:243],v[108:111]
ds_read_b64_tr_b16 a[118:119],v50 offset:20224
v_mfma_f32_16x16x32_bf16 v[108:111],a[124:127],a[244:247],v[108:111]
s_cmp_eq_u32 s70,1
s_cselect_b32 s65,256,0
s_cselect_b32 s66,524288,0
v_mfma_f32_16x16x32_bf16 v[124:127],a[124:127],a[252:255],v[124:127]
ds_read_b64_tr_b16 a[124:125],v50 offset:18184
v_add_u32 v40,s65,v40
v_mfma_f32_16x16x32_bf16 v[124:127],a[120:123],a[248:251],v[124:127]
ds_read_b64_tr_b16 a[120:121],v50 offset:1800
v_add_u32 v41,s66,v41
v_mfma_f32_16x16x32_bf16 v[128:131],a[64:67],a[224:227],v[128:131]
buffer_load_dwordx4 v33,s[28:31],s84 offen offset:1024 lds
v_mfma_f32_16x16x32_bf16 v[128:131],a[68:71],a[228:231],v[128:131]
ds_read_b64_tr_b16 a[122:123],v50 offset:3848
v_add_u32 v42,s65,v42
v_mfma_f32_16x16x32_bf16 v[160:163],a[68:71],a[236:239],v[160:163]
ds_read_b64_tr_b16 a[126:127],v50 offset:20232
v_add_u32 v50,s86,v51
v_mfma_f32_16x16x32_bf16 v[160:163],a[64:67],a[232:235],v[160:163]
s_mov_b32 s86,s87
v_add_u32 v43,s66,v43
v_add_u32 v44,s65,v44
v_mfma_f32_16x16x32_bf16 v[192:195],a[64:67],a[240:243],v[192:195]
s_mov_b32 s87,s88
v_add_u32 v45,s66,v45
v_add_u32 v46,s65,v46
v_mfma_f32_16x16x32_bf16 v[192:195],a[68:71],a[244:247],v[192:195]
v_add_u32 v47,s66,v47
v_mfma_f32_16x16x32_bf16 v[224:227],a[68:71],a[252:255],v[224:227]
v_mfma_f32_16x16x32_bf16 v[224:227],a[64:67],a[248:251],v[224:227]
buffer_load_dwordx4 v34,s[28:31],s84 offen offset:2048 lds
v_mfma_f32_16x16x32_bf16 v[132:135],a[72:75],a[224:227],v[132:135]
v_mfma_f32_16x16x32_bf16 v[132:135],a[76:79],a[228:231],v[132:135]
v_mfma_f32_16x16x32_bf16 v[164:167],a[76:79],a[236:239],v[164:167]
v_mfma_f32_16x16x32_bf16 v[164:167],a[72:75],a[232:235],v[164:167]
v_mfma_f32_16x16x32_bf16 v[196:199],a[72:75],a[240:243],v[196:199]
v_mfma_f32_16x16x32_bf16 v[196:199],a[76:79],a[244:247],v[196:199]
v_mfma_f32_16x16x32_bf16 v[228:231],a[76:79],a[252:255],v[228:231]
buffer_load_dwordx4 v35,s[28:31],s84 offen offset:3072 lds
v_mfma_f32_16x16x32_bf16 v[228:231],a[72:75],a[248:251],v[228:231]
s_waitcnt lgkmcnt(0)
v_mfma_f32_16x16x32_bf16 v[136:139],a[80:83],a[224:227],v[136:139]
v_mfma_f32_16x16x32_bf16 v[136:139],a[84:87],a[228:231],v[136:139]
v_mfma_f32_16x16x32_bf16 v[168:171],a[84:87],a[236:239],v[168:171]
v_mfma_f32_16x16x32_bf16 v[168:171],a[80:83],a[232:235],v[168:171]
s_add_u32 m0,s85,4096
s_xor_b32 s85,s85,98304
v_mfma_f32_16x16x32_bf16 v[200:203],a[80:83],a[240:243],v[200:203]
v_mfma_f32_16x16x32_bf16 v[200:203],a[84:87],a[244:247],v[200:203]
buffer_load_dwordx4 v36,s[28:31],s84 offen lds
v_mfma_f32_16x16x32_bf16 v[232:235],a[84:87],a[252:255],v[232:235]
v_mfma_f32_16x16x32_bf16 v[232:235],a[80:83],a[248:251],v[232:235]
v_mfma_f32_16x16x32_bf16 v[140:143],a[88:91],a[224:227],v[140:143]
v_mfma_f32_16x16x32_bf16 v[140:143],a[92:95],a[228:231],v[140:143]
v_mfma_f32_16x16x32_bf16 v[172:175],a[92:95],a[236:239],v[172:175]
v_mfma_f32_16x16x32_bf16 v[172:175],a[88:91],a[232:235],v[172:175]
v_mfma_f32_16x16x32_bf16 v[204:207],a[88:91],a[240:243],v[204:207]
buffer_load_dwordx4 v37,s[28:31],s84 offen offset:1024 lds
v_mfma_f32_16x16x32_bf16 v[204:207],a[92:95],a[244:247],v[204:207]
v_mfma_f32_16x16x32_bf16 v[236:239],a[92:95],a[252:255],v[236:239]
v_mfma_f32_16x16x32_bf16 v[236:239],a[88:91],a[248:251],v[236:239]
s_waitcnt vmcnt(23)
s_barrier
v_mfma_f32_16x16x32_bf16 v[144:147],a[96:99],a[224:227],v[144:147]
s_waitcnt vmcnt(15)
ds_read_b128 a[192:195],v48
v_mfma_f32_16x16x32_bf16 v[144:147],a[100:103],a[228:231],v[144:147]
ds_read_b128 a[196:199],v49
v_mfma_f32_16x16x32_bf16 v[176:179],a[100:103],a[236:239],v[176:179]
buffer_load_dwordx4 v38,s[28:31],s84 offen offset:2048 lds
v_mfma_f32_16x16x32_bf16 v[176:179],a[96:99],a[232:235],v[176:179]
ds_read_b128 a[200:203],v48 offset:2048
v_mfma_f32_16x16x32_bf16 v[208:211],a[96:99],a[240:243],v[208:211]
ds_read_b128 a[204:207],v49 offset:2048
v_mfma_f32_16x16x32_bf16 v[208:211],a[100:103],a[244:247],v[208:211]
ds_read_b128 a[208:211],v48 offset:4096
v_mfma_f32_16x16x32_bf16 v[240:243],a[100:103],a[252:255],v[240:243]
ds_read_b128 a[212:215],v49 offset:4096
v_mfma_f32_16x16x32_bf16 v[240:243],a[96:99],a[248:251],v[240:243]
v_mfma_f32_16x16x32_bf16 v[148:151],a[104:107],a[224:227],v[148:151]
ds_read_b128 a[216:219],v48 offset:6144
v_xor_b32 v48,98304,v48
v_mfma_f32_16x16x32_bf16 v[148:151],a[108:111],a[228:231],v[148:151]
buffer_load_dwordx4 v39,s[28:31],s84 offen offset:3072 lds
v_mfma_f32_16x16x32_bf16 v[180:183],a[108:111],a[236:239],v[180:183]
s_add_u32 s84,s84,128
ds_read_b128 a[220:223],v49 offset:6144
v_xor_b32 v49,98304,v49
v_mfma_f32_16x16x32_bf16 v[180:183],a[104:107],a[232:235],v[180:183]
ds_read_b64_tr_b16 a[64:65],v50
v_mfma_f32_16x16x32_bf16 v[212:215],a[104:107],a[240:243],v[212:215]
ds_read_b64_tr_b16 a[66:67],v50 offset:2048
v_mfma_f32_16x16x32_bf16 v[212:215],a[108:111],a[244:247],v[212:215]
ds_read_b64_tr_b16 a[68:69],v50 offset:16384
v_mfma_f32_16x16x32_bf16 v[244:247],a[108:111],a[252:255],v[244:247]
v_mfma_f32_16x16x32_bf16 v[244:247],a[104:107],a[248:251],v[244:247]
ds_read_b64_tr_b16 a[70:71],v50 offset:18432
v_mfma_f32_16x16x32_bf16 v[152:155],a[112:115],a[224:227],v[152:155]
buffer_load_dword v9,v19,s[48:51],s90 offen
v_mfma_f32_16x16x32_bf16 v[152:155],a[116:119],a[228:231],v[152:155]
s_add_u32 s90,s90,896
ds_read_b64_tr_b16 a[72:73],v50 offset:8
v_mfma_f32_16x16x32_bf16 v[184:187],a[116:119],a[236:239],v[184:187]
ds_read_b64_tr_b16 a[74:75],v50 offset:2056
v_mfma_f32_16x16x32_bf16 v[184:187],a[112:115],a[232:235],v[184:187]
ds_read_b64_tr_b16 a[76:77],v50 offset:16392
v_mfma_f32_16x16x32_bf16 v[216:219],a[112:115],a[240:243],v[216:219]
ds_read_b64_tr_b16 a[78:79],v50 offset:18440
v_mfma_f32_16x16x32_bf16 v[216:219],a[116:119],a[244:247],v[216:219]
v_mfma_f32_16x16x32_bf16 v[248:251],a[116:119],a[252:255],v[248:251]
ds_read_b64_tr_b16 a[80:81],v50 offset:256
v_mfma_f32_16x16x32_bf16 v[248:251],a[112:115],a[248:251],v[248:251]
ds_read_b64_tr_b16 a[82:83],v50 offset:2304
v_mfma_f32_16x16x32_bf16 v[156:159],a[120:123],a[224:227],v[156:159]
ds_read_b64_tr_b16 a[84:85],v50 offset:16640
v_mfma_f32_16x16x32_bf16 v[156:159],a[124:127],a[228:231],v[156:159]
v_mfma_f32_16x16x32_bf16 v[188:191],a[124:127],a[236:239],v[188:191]
ds_read_b64_tr_b16 a[86:87],v50 offset:18688
v_mfma_f32_16x16x32_bf16 v[188:191],a[120:123],a[232:235],v[188:191]
ds_read_b64_tr_b16 a[88:89],v50 offset:264
v_mfma_f32_16x16x32_bf16 v[220:223],a[120:123],a[240:243],v[220:223]
ds_read_b64_tr_b16 a[90:91],v50 offset:2312
v_mfma_f32_16x16x32_bf16 v[220:223],a[124:127],a[244:247],v[220:223]
v_mfma_f32_16x16x32_bf16 v[252:255],a[124:127],a[252:255],v[252:255]
s_waitcnt lgkmcnt(4)
ds_read_b64_tr_b16 a[92:93],v50 offset:16648
v_mfma_f32_16x16x32_bf16 v[252:255],a[120:123],a[248:251],v[252:255]
s_add_u32 m0,s88,0
ds_read_b64_tr_b16 a[94:95],v50 offset:18696
v_mfma_f32_16x16x32_bf16 a[0:3],a[64:67],a[192:195],a[0:3]
ds_read_b64_tr_b16 a[96:97],v50 offset:512
v_mfma_f32_16x16x32_bf16 a[0:3],a[68:71],a[196:199],a[0:3]
buffer_load_dwordx4 v40,s[36:39],s83 offen lds
v_mfma_f32_16x16x32_bf16 a[16:19],a[68:71],a[204:207],a[16:19]
ds_read_b64_tr_b16 a[98:99],v50 offset:2560
v_mfma_f32_16x16x32_bf16 a[16:19],a[64:67],a[200:203],a[16:19]
ds_read_b64_tr_b16 a[100:101],v50 offset:16896
v_mfma_f32_16x16x32_bf16 a[32:35],a[64:67],a[208:211],a[32:35]
ds_read_b64_tr_b16 a[102:103],v50 offset:18944
v_mfma_f32_16x16x32_bf16 a[32:35],a[68:71],a[212:215],a[32:35]
)A"
R"A(v_mfma_f32_16x16x32_bf16 a[48:51],a[68:71],a[220:223],a[48:51]
s_add_u32 m0,s88,1024
ds_read_b64_tr_b16 a[104:105],v50 offset:520
v_mfma_f32_16x16x32_bf16 a[48:51],a[64:67],a[216:219],a[48:51]
ds_read_b64_tr_b16 a[106:107],v50 offset:2568
v_mfma_f32_16x16x32_bf16 a[4:7],a[72:75],a[192:195],a[4:7]
buffer_load_dwordx4 v41,s[36:39],s83 offen lds
v_mfma_f32_16x16x32_bf16 a[4:7],a[76:79],a[196:199],a[4:7]
ds_read_b64_tr_b16 a[108:109],v50 offset:16904
v_mfma_f32_16x16x32_bf16 a[20:23],a[76:79],a[204:207],a[20:23]
ds_read_b64_tr_b16 a[110:111],v50 offset:18952
v_mfma_f32_16x16x32_bf16 a[20:23],a[72:75],a[200:203],a[20:23]
ds_read_b64_tr_b16 a[112:113],v50 offset:768
v_mfma_f32_16x16x32_bf16 a[36:39],a[72:75],a[208:211],a[36:39]
ds_read_b64_tr_b16 a[114:115],v50 offset:2816
v_mfma_f32_16x16x32_bf16 a[36:39],a[76:79],a[212:215],a[36:39]
s_add_u32 m0,s88,2048
v_mfma_f32_16x16x32_bf16 a[52:55],a[76:79],a[220:223],a[52:55]
ds_read_b64_tr_b16 a[116:117],v50 offset:17152
v_mfma_f32_16x16x32_bf16 a[52:55],a[72:75],a[216:219],a[52:55]
buffer_load_dwordx4 v42,s[36:39],s83 offen lds
v_mfma_f32_16x16x32_bf16 a[8:11],a[80:83],a[192:195],a[8:11]
s_waitcnt lgkmcnt(5)
ds_read_b64_tr_b16 a[118:119],v50 offset:19200
v_mfma_f32_16x16x32_bf16 a[8:11],a[84:87],a[196:199],a[8:11]
ds_read_b64_tr_b16 a[120:121],v50 offset:776
v_mfma_f32_16x16x32_bf16 a[24:27],a[84:87],a[204:207],a[24:27]
ds_read_b64_tr_b16 a[122:123],v50 offset:2824
v_mfma_f32_16x16x32_bf16 a[24:27],a[80:83],a[200:203],a[24:27]
ds_read_b64_tr_b16 a[124:125],v50 offset:17160
v_mfma_f32_16x16x32_bf16 a[40:43],a[80:83],a[208:211],a[40:43]
s_add_u32 m0,s88,3072
v_mfma_f32_16x16x32_bf16 a[40:43],a[84:87],a[212:215],a[40:43]
ds_read_b64_tr_b16 a[126:127],v50 offset:19208
v_mfma_f32_16x16x32_bf16 a[56:59],a[84:87],a[220:223],a[56:59]
buffer_load_dwordx4 v43,s[36:39],s83 offen lds
v_mfma_f32_16x16x32_bf16 a[56:59],a[80:83],a[216:219],a[56:59]
ds_read_b64_tr_b16 a[64:65],v50 offset:1024
v_mfma_f32_16x16x32_bf16 a[12:15],a[88:91],a[192:195],a[12:15]
ds_read_b64_tr_b16 a[66:67],v50 offset:3072
v_mfma_f32_16x16x32_bf16 a[12:15],a[92:95],a[196:199],a[12:15]
ds_read_b64_tr_b16 a[68:69],v50 offset:17408
v_mfma_f32_16x16x32_bf16 a[28:31],a[92:95],a[204:207],a[28:31]
ds_read_b64_tr_b16 a[70:71],v50 offset:19456
v_mfma_f32_16x16x32_bf16 a[28:31],a[88:91],a[200:203],a[28:31]
s_add_u32 m0,s88,4096
v_mfma_f32_16x16x32_bf16 a[44:47],a[88:91],a[208:211],a[44:47]
ds_read_b64_tr_b16 a[72:73],v50 offset:1032
v_mfma_f32_16x16x32_bf16 a[44:47],a[92:95],a[212:215],a[44:47]
buffer_load_dwordx4 v44,s[36:39],s83 offen lds
v_mfma_f32_16x16x32_bf16 a[60:63],a[92:95],a[220:223],a[60:63]
ds_read_b64_tr_b16 a[74:75],v50 offset:3080
v_mfma_f32_16x16x32_bf16 a[60:63],a[88:91],a[216:219],a[60:63]
ds_read_b64_tr_b16 a[76:77],v50 offset:17416
v_mfma_f32_16x16x32_bf16 v[64:67],a[96:99],a[192:195],v[64:67]
ds_read_b64_tr_b16 a[78:79],v50 offset:19464
v_mfma_f32_16x16x32_bf16 v[64:67],a[100:103],a[196:199],v[64:67]
ds_read_b64_tr_b16 a[80:81],v50 offset:1280
v_mfma_f32_16x16x32_bf16 v[80:83],a[100:103],a[204:207],v[80:83]
s_add_u32 m0,s88,5120
v_mfma_f32_16x16x32_bf16 v[80:83],a[96:99],a[200:203],v[80:83]
ds_read_b64_tr_b16 a[82:83],v50 offset:3328
v_mfma_f32_16x16x32_bf16 v[96:99],a[96:99],a[208:211],v[96:99]
buffer_load_dwordx4 v45,s[36:39],s83 offen lds
v_mfma_f32_16x16x32_bf16 v[96:99],a[100:103],a[212:215],v[96:99]
ds_read_b64_tr_b16 a[84:85],v50 offset:17664
v_mfma_f32_16x16x32_bf16 v[112:115],a[100:103],a[220:223],v[112:115]
ds_read_b64_tr_b16 a[86:87],v50 offset:19712
v_mfma_f32_16x16x32_bf16 v[112:115],a[96:99],a[216:219],v[112:115]
ds_read_b64_tr_b16 a[88:89],v50 offset:1288
v_mfma_f32_16x16x32_bf16 v[68:71],a[104:107],a[192:195],v[68:71]
s_waitcnt lgkmcnt(5)
ds_read_b64_tr_b16 a[90:91],v50 offset:3336
v_mfma_f32_16x16x32_bf16 v[68:71],a[108:111],a[196:199],v[68:71]
s_add_u32 m0,s88,6144
v_mfma_f32_16x16x32_bf16 v[84:87],a[108:111],a[204:207],v[84:87]
ds_read_b64_tr_b16 a[92:93],v50 offset:17672
v_mfma_f32_16x16x32_bf16 v[84:87],a[104:107],a[200:203],v[84:87]
buffer_load_dwordx4 v46,s[36:39],s83 offen lds
v_mfma_f32_16x16x32_bf16 v[100:103],a[104:107],a[208:211],v[100:103]
ds_read_b64_tr_b16 a[94:95],v50 offset:19720
v_mfma_f32_16x16x32_bf16 v[100:103],a[108:111],a[212:215],v[100:103]
ds_read_b64_tr_b16 a[96:97],v50 offset:1536
v_mfma_f32_16x16x32_bf16 v[116:119],a[108:111],a[220:223],v[116:119]
ds_read_b64_tr_b16 a[98:99],v50 offset:3584
v_mfma_f32_16x16x32_bf16 v[116:119],a[104:107],a[216:219],v[116:119]
ds_read_b64_tr_b16 a[100:101],v50 offset:17920
v_mfma_f32_16x16x32_bf16 v[72:75],a[112:115],a[192:195],v[72:75]
s_add_u32 m0,s88,7168
s_mov_b32 s88,s69
v_mfma_f32_16x16x32_bf16 v[72:75],a[116:119],a[196:199],v[72:75]
ds_read_b64_tr_b16 a[102:103],v50 offset:19968
v_mfma_f32_16x16x32_bf16 v[88:91],a[116:119],a[204:207],v[88:91]
buffer_load_dwordx4 v47,s[36:39],s83 offen lds
v_mfma_f32_16x16x32_bf16 v[88:91],a[112:115],a[200:203],v[88:91]
s_add_u32 s83,s83,524288
ds_read_b64_tr_b16 a[104:105],v50 offset:1544
v_mfma_f32_16x16x32_bf16 v[104:107],a[112:115],a[208:211],v[104:107]
ds_read_b64_tr_b16 a[106:107],v50 offset:3592
v_mfma_f32_16x16x32_bf16 v[104:107],a[116:119],a[212:215],v[104:107]
ds_read_b64_tr_b16 a[108:109],v50 offset:17928
v_mfma_f32_16x16x32_bf16 v[120:123],a[116:119],a[220:223],v[120:123]
ds_read_b64_tr_b16 a[110:111],v50 offset:19976
v_mfma_f32_16x16x32_bf16 v[120:123],a[112:115],a[216:219],v[120:123]
s_add_u32 m0,s85,0
v_mfma_f32_16x16x32_bf16 v[76:79],a[120:123],a[192:195],v[76:79]
ds_read_b64_tr_b16 a[112:113],v50 offset:1792
v_mfma_f32_16x16x32_bf16 v[76:79],a[124:127],a[196:199],v[76:79]
buffer_load_dwordx4 v32,s[28:31],s84 offen lds
v_mfma_f32_16x16x32_bf16 v[92:95],a[124:127],a[204:207],v[92:95]
ds_read_b64_tr_b16 a[114:115],v50 offset:3840
v_mfma_f32_16x16x32_bf16 v[92:95],a[120:123],a[200:203],v[92:95]
ds_read_b64_tr_b16 a[116:117],v50 offset:18176
v_mfma_f32_16x16x32_bf16 v[108:111],a[120:123],a[208:211],v[108:111]
ds_read_b64_tr_b16 a[118:119],v50 offset:20224
v_mfma_f32_16x16x32_bf16 v[108:111],a[124:127],a[212:215],v[108:111]
v_mfma_f32_16x16x32_bf16 v[124:127],a[124:127],a[220:223],v[124:127]
ds_read_b64_tr_b16 a[124:125],v50 offset:18184
v_mfma_f32_16x16x32_bf16 v[124:127],a[120:123],a[216:219],v[124:127]
ds_read_b64_tr_b16 a[120:121],v50 offset:1800
v_mfma_f32_16x16x32_bf16 v[128:131],a[64:67],a[192:195],v[128:131]
buffer_load_dwordx4 v33,s[28:31],s84 offen offset:1024 lds
v_mfma_f32_16x16x32_bf16 v[128:131],a[68:71],a[196:199],v[128:131]
ds_read_b64_tr_b16 a[122:123],v50 offset:3848
v_mfma_f32_16x16x32_bf16 v[160:163],a[68:71],a[204:207],v[160:163]
ds_read_b64_tr_b16 a[126:127],v50 offset:20232
v_add_u32 v50,s86,v51
v_mfma_f32_16x16x32_bf16 v[160:163],a[64:67],a[200:203],v[160:163]
v_mfma_f32_16x16x32_bf16 v[192:195],a[64:67],a[208:211],v[192:195]
v_mfma_f32_16x16x32_bf16 v[192:195],a[68:71],a[212:215],v[192:195]
v_mfma_f32_16x16x32_bf16 v[224:227],a[68:71],a[220:223],v[224:227]
v_mfma_f32_16x16x32_bf16 v[224:227],a[64:67],a[216:219],v[224:227]
buffer_load_dwordx4 v34,s[28:31],s84 offen offset:2048 lds
v_mfma_f32_16x16x32_bf16 v[132:135],a[72:75],a[192:195],v[132:135]
v_mfma_f32_16x16x32_bf16 v[132:135],a[76:79],a[196:199],v[132:135]
v_mfma_f32_16x16x32_bf16 v[164:167],a[76:79],a[204:207],v[164:167]
v_mfma_f32_16x16x32_bf16 v[164:167],a[72:75],a[200:203],v[164:167]
v_mfma_f32_16x16x32_bf16 v[196:199],a[72:75],a[208:211],v[196:199]
v_mfma_f32_16x16x32_bf16 v[196:199],a[76:79],a[212:215],v[196:199]
v_mfma_f32_16x16x32_bf16 v[228:231],a[76:79],a[220:223],v[228:231]
buffer_load_dwordx4 v35,s[28:31],s84 offen offset:3072 lds
v_mfma_f32_16x16x32_bf16 v[228:231],a[72:75],a[216:219],v[228:231]
s_waitcnt lgkmcnt(0)
v_mfma_f32_16x16x32_bf16 v[136:139],a[80:83],a[192:195],v[136:139]
v_mfma_f32_16x16x32_bf16 v[136:139],a[84:87],a[196:199],v[136:139]
v_mfma_f32_16x16x32_bf16 v[168:171],a[84:87],a[204:207],v[168:171]
v_mfma_f32_16x16x32_bf16 v[168:171],a[80:83],a[200:203],v[168:171]
s_add_u32 m0,s85,4096
s_xor_b32 s85,s85,98304
v_mfma_f32_16x16x32_bf16 v[200:203],a[80:83],a[208:211],v[200:203]
v_mfma_f32_16x16x32_bf16 v[200:203],a[84:87],a[212:215],v[200:203]
buffer_load_dwordx4 v36,s[28:31],s84 offen lds
v_mfma_f32_16x16x32_bf16 v[232:235],a[84:87],a[220:223],v[232:235]
v_mfma_f32_16x16x32_bf16 v[232:235],a[80:83],a[216:219],v[232:235]
v_mfma_f32_16x16x32_bf16 v[140:143],a[88:91],a[192:195],v[140:143]
v_mfma_f32_16x16x32_bf16 v[140:143],a[92:95],a[196:199],v[140:143]
v_mfma_f32_16x16x32_bf16 v[172:175],a[92:95],a[204:207],v[172:175]
v_mfma_f32_16x16x32_bf16 v[172:175],a[88:91],a[200:203],v[172:175]
v_mfma_f32_16x16x32_bf16 v[204:207],a[88:91],a[208:211],v[204:207]
buffer_load_dwordx4 v37,s[28:31],s84 offen offset:1024 lds
v_mfma_f32_16x16x32_bf16 v[204:207],a[92:95],a[212:215],v[204:207]
v_mfma_f32_16x16x32_bf16 v[236:239],a[92:95],a[220:223],v[236:239]
v_mfma_f32_16x16x32_bf16 v[236:239],a[88:91],a[216:219],v[236:239]
s_waitcnt vmcnt(23)
s_barrier
v_mfma_f32_16x16x32_bf16 v[144:147],a[96:99],a[192:195],v[144:147]
s_waitcnt vmcnt(15)
ds_read_b128 a[224:227],v48
v_mfma_f32_16x16x32_bf16 v[144:147],a[100:103],a[196:199],v[144:147]
ds_read_b128 a[228:231],v49
v_mfma_f32_16x16x32_bf16 v[176:179],a[100:103],a[204:207],v[176:179]
buffer_load_dwordx4 v38,s[28:31],s84 offen offset:2048 lds
v_mfma_f32_16x16x32_bf16 v[176:179],a[96:99],a[200:203],v[176:179]
ds_read_b128 a[232:235],v48 offset:2048
v_mfma_f32_16x16x32_bf16 v[208:211],a[96:99],a[208:211],v[208:211]
ds_read_b128 a[236:239],v49 offset:2048
v_mfma_f32_16x16x32_bf16 v[208:211],a[100:103],a[212:215],v[208:211]
ds_read_b128 a[240:243],v48 offset:4096
v_mfma_f32_16x16x32_bf16 v[240:243],a[100:103],a[220:223],v[240:243]
ds_read_b128 a[244:247],v49 offset:4096
v_mfma_f32_16x16x32_bf16 v[240:243],a[96:99],a[216:219],v[240:243]
v_mfma_f32_16x16x32_bf16 v[148:151],a[104:107],a[192:195],v[148:151]
ds_read_b128 a[248:251],v48 offset:6144
v_mfma_f32_16x16x32_bf16 v[148:151],a[108:111],a[196:199],v[148:151]
buffer_load_dwordx4 v39,s[28:31],s84 offen offset:3072 lds
v_mfma_f32_16x16x32_bf16 v[180:183],a[108:111],a[204:207],v[180:183]
s_add_u32 s84,s84,128
ds_read_b128 a[252:255],v49 offset:6144
v_mfma_f32_16x16x32_bf16 v[180:183],a[104:107],a[200:203],v[180:183]
ds_read_b64_tr_b16 a[64:65],v50
v_mfma_f32_16x16x32_bf16 v[212:215],a[104:107],a[208:211],v[212:215]
ds_read_b64_tr_b16 a[66:67],v50 offset:2048
v_mfma_f32_16x16x32_bf16 v[212:215],a[108:111],a[212:215],v[212:215]
ds_read_b64_tr_b16 a[68:69],v50 offset:16384
v_mfma_f32_16x16x32_bf16 v[244:247],a[108:111],a[220:223],v[244:247]
v_mfma_f32_16x16x32_bf16 v[244:247],a[104:107],a[216:219],v[244:247]
ds_read_b64_tr_b16 a[70:71],v50 offset:18432
v_mfma_f32_16x16x32_bf16 v[152:155],a[112:115],a[192:195],v[152:155]
buffer_load_dword v8,v19,s[48:51],s90 offen
v_mfma_f32_16x16x32_bf16 v[152:155],a[116:119],a[196:199],v[152:155]
s_add_u32 s90,s90,896
ds_read_b64_tr_b16 a[72:73],v50 offset:8
v_mfma_f32_16x16x32_bf16 v[184:187],a[116:119],a[204:207],v[184:187]
ds_read_b64_tr_b16 a[74:75],v50 offset:2056
v_mfma_f32_16x16x32_bf16 v[184:187],a[112:115],a[200:203],v[184:187]
ds_read_b64_tr_b16 a[76:77],v50 offset:16392
v_mfma_f32_16x16x32_bf16 v[216:219],a[112:115],a[208:211],v[216:219]
ds_read_b64_tr_b16 a[78:79],v50 offset:18440
v_mfma_f32_16x16x32_bf16 v[216:219],a[116:119],a[212:215],v[216:219]
v_mfma_f32_16x16x32_bf16 v[248:251],a[116:119],a[220:223],v[248:251]
ds_read_b64_tr_b16 a[80:81],v50 offset:256
v_mfma_f32_16x16x32_bf16 v[248:251],a[112:115],a[216:219],v[248:251]
ds_read_b64_tr_b16 a[82:83],v50 offset:2304
v_mfma_f32_16x16x32_bf16 v[156:159],a[120:123],a[192:195],v[156:159]
ds_read_b64_tr_b16 a[84:85],v50 offset:16640
v_mfma_f32_16x16x32_bf16 v[156:159],a[124:127],a[196:199],v[156:159]
v_mfma_f32_16x16x32_bf16 v[188:191],a[124:127],a[204:207],v[188:191]
ds_read_b64_tr_b16 a[86:87],v50 offset:18688
v_mfma_f32_16x16x32_bf16 v[188:191],a[120:123],a[200:203],v[188:191]
ds_read_b64_tr_b16 a[88:89],v50 offset:264
v_mfma_f32_16x16x32_bf16 v[220:223],a[120:123],a[208:211],v[220:223]
)A"
R"A(ds_read_b64_tr_b16 a[90:91],v50 offset:2312
v_mfma_f32_16x16x32_bf16 v[220:223],a[124:127],a[212:215],v[220:223]
v_mfma_f32_16x16x32_bf16 v[252:255],a[124:127],a[220:223],v[252:255]
ds_read_b64_tr_b16 a[92:93],v50 offset:16648
v_mfma_f32_16x16x32_bf16 v[252:255],a[120:123],a[216:219],v[252:255]
ds_read_b64_tr_b16 a[94:95],v50 offset:18696
s_sub_u32 s70,s70,1
s_cmp_lg_u32 s70,0
s_cbranch_scc1 L_qp_%=
s_waitcnt lgkmcnt(6)
v_mfma_f32_16x16x32_bf16 a[0:3],a[64:67],a[224:227],a[0:3]
s_memrealtime s[92:93]
s_memtime s[56:57]
s_add_u32 m0,s85,0
v_mfma_f32_16x16x32_bf16 a[0:3],a[68:71],a[228:231],a[0:3]
buffer_load_dwordx4 v32,s[28:31],s84 offen lds
v_mfma_f32_16x16x32_bf16 a[16:19],a[68:71],a[236:239],a[16:19]
s_sub_u32 s83,s83,524288
ds_read_b64_tr_b16 a[96:97],v50 offset:512
v_subrev_u32 v40,256,v40
v_mfma_f32_16x16x32_bf16 a[16:19],a[64:67],a[232:235],a[16:19]
s_mov_b32 s69,s86
ds_read_b64_tr_b16 a[98:99],v50 offset:2560
v_subrev_u32 v41,256,v41
v_mfma_f32_16x16x32_bf16 a[32:35],a[64:67],a[240:243],a[32:35]
s_mov_b32 s86,s87
ds_read_b64_tr_b16 a[100:101],v50 offset:16896
v_subrev_u32 v42,256,v42
v_mfma_f32_16x16x32_bf16 a[32:35],a[68:71],a[244:247],a[32:35]
s_mov_b32 s87,s88
ds_read_b64_tr_b16 a[102:103],v50 offset:18944
v_subrev_u32 v43,256,v43
v_mfma_f32_16x16x32_bf16 a[48:51],a[68:71],a[252:255],a[48:51]
s_add_u32 s75,s75,16384
v_subrev_u32 v44,256,v44
v_subrev_u32 v45,256,v45
v_mfma_f32_16x16x32_bf16 a[48:51],a[64:67],a[248:251],a[48:51]
buffer_load_dwordx4 v33,s[28:31],s84 offen offset:1024 lds
v_mfma_f32_16x16x32_bf16 a[4:7],a[72:75],a[224:227],a[4:7]
s_sub_u32 s78,s77,32768
ds_read_b64_tr_b16 a[104:105],v50 offset:520
v_subrev_u32 v46,256,v46
v_mfma_f32_16x16x32_bf16 a[4:7],a[76:79],a[228:231],a[4:7]
L_pd2_%=:
s_cbranch_execz L_pd6_%=
s_and_b32 s78,s78,0x1ffff
ds_read_b64_tr_b16 a[106:107],v50 offset:2568
v_subrev_u32 v47,256,v47
v_mfma_f32_16x16x32_bf16 a[20:23],a[76:79],a[236:239],a[20:23]
s_sub_u32 s78,s78,s77
ds_read_b64_tr_b16 a[108:109],v50 offset:16904
v_xor_b32 v48,98304,v48
v_mfma_f32_16x16x32_bf16 a[20:23],a[72:75],a[232:235],a[20:23]
s_add_u32 s77,s77,s78
ds_read_b64_tr_b16 a[110:111],v50 offset:18952
v_xor_b32 v49,98304,v49
v_mfma_f32_16x16x32_bf16 a[36:39],a[72:75],a[240:243],a[36:39]
s_mov_b32 s6,0
v_add_u32 v52,s87,v51
v_mfma_f32_16x16x32_bf16 a[36:39],a[76:79],a[244:247],a[36:39]
buffer_load_dwordx4 v34,s[28:31],s84 offen offset:2048 lds
v_mfma_f32_16x16x32_bf16 a[52:55],a[76:79],a[252:255],a[52:55]
s_mul_i32 s66,s60,40960
ds_read_b64_tr_b16 a[112:113],v50 offset:768
v_add_u32 v11,0,v23
v_mfma_f32_16x16x32_bf16 a[52:55],a[72:75],a[248:251],a[52:55]
s_add_u32 s65,s63,1
ds_read_b64_tr_b16 a[114:115],v50 offset:2816
v_mul_u32_u24 v12,3277,v11
v_mfma_f32_16x16x32_bf16 a[8:11],a[80:83],a[224:227],a[8:11]
s_waitcnt lgkmcnt(5)
ds_read_b64_tr_b16 a[116:117],v50 offset:17152
v_lshrrev_b32 v12,16,v12
v_mfma_f32_16x16x32_bf16 a[8:11],a[84:87],a[228:231],a[8:11]
ds_read_b64_tr_b16 a[118:119],v50 offset:19200
v_mul_u32_u24 v13,20,v12
v_mfma_f32_16x16x32_bf16 a[24:27],a[84:87],a[236:239],a[24:27]
v_sub_u32 v13,v11,v13
v_bfe_u32 v14,v12,2,1
v_mfma_f32_16x16x32_bf16 a[24:27],a[80:83],a[232:235],a[24:27]
buffer_load_dwordx4 v35,s[28:31],s84 offen offset:3072 lds
v_mfma_f32_16x16x32_bf16 a[40:43],a[80:83],a[240:243],a[40:43]
ds_read_b64_tr_b16 a[120:121],v50 offset:776
v_lshlrev_b32 v14,1,v14
v_mfma_f32_16x16x32_bf16 a[40:43],a[84:87],a[244:247],a[40:43]
ds_read_b64_tr_b16 a[122:123],v50 offset:2824
v_xor_b32 v13,v13,v14
v_mfma_f32_16x16x32_bf16 a[56:59],a[84:87],a[252:255],a[56:59]
ds_read_b64_tr_b16 a[124:125],v50 offset:17160
v_lshrrev_b32 v14,2,v13
v_mfma_f32_16x16x32_bf16 a[56:59],a[80:83],a[248:251],a[56:59]
s_add_u32 m0,s85,4096
s_xor_b32 s85,s85,98304
ds_read_b64_tr_b16 a[126:127],v50 offset:19208
v_mfma_f32_16x16x32_bf16 a[12:15],a[88:91],a[224:227],a[12:15]
v_cmp_eq_u32 vcc,3,v14
v_min_u32 v15,3,v14
v_mfma_f32_16x16x32_bf16 a[12:15],a[92:95],a[228:231],a[12:15]
buffer_load_dwordx4 v36,s[28:31],s84 offen lds
v_mfma_f32_16x16x32_bf16 a[28:31],a[92:95],a[236:239],a[28:31]
ds_read_b64_tr_b16 a[64:65],v50 offset:1024
v_add_u32 v15,s65,v15
v_mfma_f32_16x16x32_bf16 a[28:31],a[88:91],a[232:235],a[28:31]
ds_read_b64_tr_b16 a[66:67],v50 offset:3072
v_and_b32 v15,3,v15
v_mfma_f32_16x16x32_bf16 a[44:47],a[88:91],a[240:243],a[44:47]
ds_read_b64_tr_b16 a[68:69],v50 offset:17408
v_cndmask_b32_e64 v15,v15,4,vcc
v_mfma_f32_16x16x32_bf16 a[44:47],a[92:95],a[244:247],a[44:47]
ds_read_b64_tr_b16 a[70:71],v50 offset:19456
v_and_b32 v13,3,v13
v_mfma_f32_16x16x32_bf16 a[60:63],a[92:95],a[252:255],a[60:63]
v_lshl_add_u32 v13,v15,2,v13
v_mul_u32_u24 v12,1280,v12
v_mfma_f32_16x16x32_bf16 a[60:63],a[88:91],a[248:251],a[60:63]
buffer_load_dwordx4 v37,s[28:31],s84 offen offset:1024 lds
v_mfma_f32_16x16x32_bf16 v[64:67],a[96:99],a[224:227],v[64:67]
ds_read_b64_tr_b16 a[72:73],v50 offset:1032
v_lshl_add_u32 v12,v13,4,v12
v_mfma_f32_16x16x32_bf16 v[64:67],a[100:103],a[228:231],v[64:67]
ds_read_b64_tr_b16 a[74:75],v50 offset:3080
v_add_u32 v12,s66,v12
v_mfma_f32_16x16x32_bf16 v[80:83],a[100:103],a[236:239],v[80:83]
ds_read_b64_tr_b16 a[76:77],v50 offset:17416
v_subrev_u32 v24,0,v12
v_mfma_f32_16x16x32_bf16 v[80:83],a[96:99],a[232:235],v[80:83]
ds_read_b64_tr_b16 a[78:79],v50 offset:19464
v_add_u32 v11,64,v23
v_mfma_f32_16x16x32_bf16 v[96:99],a[96:99],a[240:243],v[96:99]
v_mul_u32_u24 v12,3277,v11
v_lshrrev_b32 v12,16,v12
v_mfma_f32_16x16x32_bf16 v[96:99],a[100:103],a[244:247],v[96:99]
buffer_load_dwordx4 v38,s[28:31],s84 offen offset:2048 lds
v_mfma_f32_16x16x32_bf16 v[112:115],a[100:103],a[252:255],v[112:115]
ds_read_b64_tr_b16 a[80:81],v50 offset:1280
v_mul_u32_u24 v13,20,v12
v_mfma_f32_16x16x32_bf16 v[112:115],a[96:99],a[248:251],v[112:115]
s_waitcnt lgkmcnt(5)
ds_read_b64_tr_b16 a[82:83],v50 offset:3328
v_sub_u32 v13,v11,v13
v_mfma_f32_16x16x32_bf16 v[68:71],a[104:107],a[224:227],v[68:71]
ds_read_b64_tr_b16 a[84:85],v50 offset:17664
v_bfe_u32 v14,v12,2,1
v_mfma_f32_16x16x32_bf16 v[68:71],a[108:111],a[228:231],v[68:71]
ds_read_b64_tr_b16 a[86:87],v50 offset:19712
v_lshlrev_b32 v14,1,v14
v_mfma_f32_16x16x32_bf16 v[84:87],a[108:111],a[236:239],v[84:87]
v_xor_b32 v13,v13,v14
v_lshrrev_b32 v14,2,v13
v_mfma_f32_16x16x32_bf16 v[84:87],a[104:107],a[232:235],v[84:87]
buffer_load_dwordx4 v39,s[28:31],s84 offen offset:3072 lds
v_mfma_f32_16x16x32_bf16 v[100:103],a[104:107],a[240:243],v[100:103]
s_add_u32 s84,s84,128
ds_read_b64_tr_b16 a[88:89],v50 offset:1288
v_cmp_eq_u32 vcc,3,v14
v_mfma_f32_16x16x32_bf16 v[100:103],a[108:111],a[244:247],v[100:103]
ds_read_b64_tr_b16 a[90:91],v50 offset:3336
v_min_u32 v15,3,v14
v_mfma_f32_16x16x32_bf16 v[116:119],a[108:111],a[252:255],v[116:119]
ds_read_b64_tr_b16 a[92:93],v50 offset:17672
v_add_u32 v15,s65,v15
v_mfma_f32_16x16x32_bf16 v[116:119],a[104:107],a[248:251],v[116:119]
s_add_u32 m0,s88,0
ds_read_b64_tr_b16 a[94:95],v50 offset:19720
v_and_b32 v15,3,v15
v_mfma_f32_16x16x32_bf16 v[72:75],a[112:115],a[224:227],v[72:75]
v_cndmask_b32_e64 v15,v15,4,vcc
v_and_b32 v13,3,v13
v_mfma_f32_16x16x32_bf16 v[72:75],a[116:119],a[228:231],v[72:75]
buffer_load_dwordx4 v40,s[36:39],s83 offen lds
v_mfma_f32_16x16x32_bf16 v[88:91],a[116:119],a[236:239],v[88:91]
ds_read_b64_tr_b16 a[96:97],v50 offset:1536
v_lshl_add_u32 v13,v15,2,v13
v_mfma_f32_16x16x32_bf16 v[88:91],a[112:115],a[232:235],v[88:91]
ds_read_b64_tr_b16 a[98:99],v50 offset:3584
v_mul_u32_u24 v12,1280,v12
v_mfma_f32_16x16x32_bf16 v[104:107],a[112:115],a[240:243],v[104:107]
ds_read_b64_tr_b16 a[100:101],v50 offset:17920
v_lshl_add_u32 v12,v13,4,v12
v_mfma_f32_16x16x32_bf16 v[104:107],a[116:119],a[244:247],v[104:107]
s_add_u32 m0,s88,1024
ds_read_b64_tr_b16 a[102:103],v50 offset:19968
v_add_u32 v12,s66,v12
v_mfma_f32_16x16x32_bf16 v[120:123],a[116:119],a[252:255],v[120:123]
v_subrev_u32 v25,1024,v12
v_add_u32 v11,128,v23
v_mfma_f32_16x16x32_bf16 v[120:123],a[112:115],a[248:251],v[120:123]
buffer_load_dwordx4 v41,s[36:39],s83 offen lds
v_mfma_f32_16x16x32_bf16 v[76:79],a[120:123],a[224:227],v[76:79]
ds_read_b64_tr_b16 a[104:105],v50 offset:1544
v_mul_u32_u24 v12,3277,v11
v_mfma_f32_16x16x32_bf16 v[76:79],a[124:127],a[228:231],v[76:79]
ds_read_b64_tr_b16 a[106:107],v50 offset:3592
v_lshrrev_b32 v12,16,v12
v_mfma_f32_16x16x32_bf16 v[92:95],a[124:127],a[236:239],v[92:95]
ds_read_b64_tr_b16 a[108:109],v50 offset:17928
v_mul_u32_u24 v13,20,v12
v_mfma_f32_16x16x32_bf16 v[92:95],a[120:123],a[232:235],v[92:95]
s_add_u32 m0,s88,2048
ds_read_b64_tr_b16 a[110:111],v50 offset:19976
v_sub_u32 v13,v11,v13
v_mfma_f32_16x16x32_bf16 v[108:111],a[120:123],a[240:243],v[108:111]
v_bfe_u32 v14,v12,2,1
v_lshlrev_b32 v14,1,v14
v_mfma_f32_16x16x32_bf16 v[108:111],a[124:127],a[244:247],v[108:111]
buffer_load_dwordx4 v42,s[36:39],s83 offen lds
v_mfma_f32_16x16x32_bf16 v[124:127],a[124:127],a[252:255],v[124:127]
ds_read_b64_tr_b16 a[112:113],v50 offset:1792
v_xor_b32 v13,v13,v14
v_mfma_f32_16x16x32_bf16 v[124:127],a[120:123],a[248:251],v[124:127]
ds_read_b64_tr_b16 a[114:115],v50 offset:3840
v_lshrrev_b32 v14,2,v13
v_mfma_f32_16x16x32_bf16 v[128:131],a[64:67],a[224:227],v[128:131]
ds_read_b64_tr_b16 a[116:117],v50 offset:18176
v_cmp_eq_u32 vcc,3,v14
v_mfma_f32_16x16x32_bf16 v[128:131],a[68:71],a[228:231],v[128:131]
s_add_u32 m0,s88,3072
ds_read_b64_tr_b16 a[118:119],v50 offset:20224
v_min_u32 v15,3,v14
v_mfma_f32_16x16x32_bf16 v[160:163],a[68:71],a[236:239],v[160:163]
v_add_u32 v15,s65,v15
v_and_b32 v15,3,v15
v_mfma_f32_16x16x32_bf16 v[160:163],a[64:67],a[232:235],v[160:163]
buffer_load_dwordx4 v43,s[36:39],s83 offen lds
v_mfma_f32_16x16x32_bf16 v[192:195],a[64:67],a[240:243],v[192:195]
ds_read_b64_tr_b16 a[120:121],v50 offset:1800
v_cndmask_b32_e64 v15,v15,4,vcc
v_mfma_f32_16x16x32_bf16 v[192:195],a[68:71],a[244:247],v[192:195]
ds_read_b64_tr_b16 a[122:123],v50 offset:3848
v_and_b32 v13,3,v13
v_mfma_f32_16x16x32_bf16 v[224:227],a[68:71],a[252:255],v[224:227]
s_waitcnt lgkmcnt(4)
ds_read_b64_tr_b16 a[124:125],v50 offset:18184
v_lshl_add_u32 v13,v15,2,v13
v_mfma_f32_16x16x32_bf16 v[224:227],a[64:67],a[248:251],v[224:227]
s_add_u32 m0,s88,4096
ds_read_b64_tr_b16 a[126:127],v50 offset:20232
v_add_u32 v50,s86,v51
v_mfma_f32_16x16x32_bf16 v[132:135],a[72:75],a[224:227],v[132:135]
v_mul_u32_u24 v12,1280,v12
v_lshl_add_u32 v12,v13,4,v12
v_mfma_f32_16x16x32_bf16 v[132:135],a[76:79],a[228:231],v[132:135]
buffer_load_dwordx4 v44,s[36:39],s83 offen lds
v_mfma_f32_16x16x32_bf16 v[164:167],a[76:79],a[236:239],v[164:167]
v_add_u32 v12,s66,v12
v_subrev_u32 v26,2048,v12
v_mfma_f32_16x16x32_bf16 v[164:167],a[72:75],a[232:235],v[164:167]
v_add_u32 v11,192,v23
v_mul_u32_u24 v12,3277,v11
v_mfma_f32_16x16x32_bf16 v[196:199],a[72:75],a[240:243],v[196:199]
v_lshrrev_b32 v12,16,v12
v_mul_u32_u24 v13,20,v12
v_mfma_f32_16x16x32_bf16 v[196:199],a[76:79],a[244:247],v[196:199]
s_add_u32 m0,s88,5120
v_sub_u32 v13,v11,v13
v_bfe_u32 v14,v12,2,1
v_mfma_f32_16x16x32_bf16 v[228:231],a[76:79],a[252:255],v[228:231]
v_lshlrev_b32 v14,1,v14
v_xor_b32 v13,v13,v14
v_mfma_f32_16x16x32_bf16 v[228:231],a[72:75],a[248:251],v[228:231]
buffer_load_dwordx4 v45,s[36:39],s83 offen lds
v_mfma_f32_16x16x32_bf16 v[136:139],a[80:83],a[224:227],v[136:139]
v_lshrrev_b32 v14,2,v13
v_cmp_eq_u32 vcc,3,v14
v_mfma_f32_16x16x32_bf16 v[136:139],a[84:87],a[228:231],v[136:139]
v_min_u32 v15,3,v14
v_add_u32 v15,s65,v15
v_mfma_f32_16x16x32_bf16 v[168:171],a[84:87],a[236:239],v[168:171]
v_and_b32 v15,3,v15
v_cndmask_b32_e64 v15,v15,4,vcc
v_mfma_f32_16x16x32_bf16 v[168:171],a[80:83],a[232:235],v[168:171]
s_add_u32 m0,s88,6144
v_and_b32 v13,3,v13
v_lshl_add_u32 v13,v15,2,v13
v_mfma_f32_16x16x32_bf16 v[200:203],a[80:83],a[240:243],v[200:203]
v_mul_u32_u24 v12,1280,v12
v_lshl_add_u32 v12,v13,4,v12
v_mfma_f32_16x16x32_bf16 v[200:203],a[84:87],a[244:247],v[200:203]
buffer_load_dwordx4 v46,s[36:39],s83 offen lds
v_mfma_f32_16x16x32_bf16 v[232:235],a[84:87],a[252:255],v[232:235]
v_add_u32 v12,s66,v12
v_subrev_u32 v27,3072,v12
v_mfma_f32_16x16x32_bf16 v[232:235],a[80:83],a[248:251],v[232:235]
v_add_u32 v11,256,v23
v_mul_u32_u24 v12,3277,v11
)A"
R"A(v_mfma_f32_16x16x32_bf16 v[140:143],a[88:91],a[224:227],v[140:143]
v_lshrrev_b32 v12,16,v12
v_mul_u32_u24 v13,20,v12
v_mfma_f32_16x16x32_bf16 v[140:143],a[92:95],a[228:231],v[140:143]
s_add_u32 m0,s88,7168
v_sub_u32 v13,v11,v13
v_bfe_u32 v14,v12,2,1
v_mfma_f32_16x16x32_bf16 v[172:175],a[92:95],a[236:239],v[172:175]
s_mov_b32 s88,s69
v_lshlrev_b32 v14,1,v14
v_xor_b32 v13,v13,v14
v_mfma_f32_16x16x32_bf16 v[172:175],a[88:91],a[232:235],v[172:175]
buffer_load_dwordx4 v47,s[36:39],s83 offen lds
v_mfma_f32_16x16x32_bf16 v[204:207],a[88:91],a[240:243],v[204:207]
s_waitcnt lgkmcnt(0)
s_add_u32 s83,s83,524288
s_sub_u32 s92,s92,s8
v_mfma_f32_16x16x32_bf16 v[204:207],a[92:95],a[244:247],v[204:207]
s_sub_u32 s56,s56,s4
s_mov_b32 s93,782
s_mul_i32 s92,s92,12600
v_mfma_f32_16x16x32_bf16 v[236:239],a[92:95],a[252:255],v[236:239]
s_cmp_ge_u32 s63,2
s_cselect_b32 s93,823,s93
s_mul_i32 s93,s93,s56
v_mfma_f32_16x16x32_bf16 v[236:239],a[88:91],a[248:251],v[236:239]
s_add_u32 s10,s20,s74
s_addc_u32 s11,s21,0
v_lshrrev_b32 v14,2,v13
s_waitcnt vmcnt(25)
s_barrier
v_mfma_f32_16x16x32_bf16 v[144:147],a[96:99],a[224:227],v[144:147]
s_add_u32 m0,s76,0
s_waitcnt vmcnt(17)
ds_read_b128 a[192:195],v48
v_cmp_eq_u32 vcc,3,v14
v_mfma_f32_16x16x32_bf16 v[144:147],a[100:103],a[228:231],v[144:147]
s_and_b32 s69,s75,0xfffc0000
ds_read_b128 a[196:199],v49
v_min_u32 v15,3,v14
v_mfma_f32_16x16x32_bf16 v[176:179],a[100:103],a[236:239],v[176:179]
global_load_lds_dwordx4 v4,s[10:11] nt
ds_read_b128 a[200:203],v48 offset:2048
v_mfma_f32_16x16x32_bf16 v[176:179],a[96:99],a[232:235],v[176:179]
s_add_u32 s75,s75,16384
ds_read_b128 a[204:207],v49 offset:2048
v_add_u32 v15,s65,v15
v_mfma_f32_16x16x32_bf16 v[208:211],a[96:99],a[240:243],v[208:211]
s_mov_b32 s65,262144
v_and_b32 v15,3,v15
v_cndmask_b32_e64 v15,v15,4,vcc
v_mfma_f32_16x16x32_bf16 v[208:211],a[100:103],a[244:247],v[208:211]
ds_read_b128 a[208:211],v48 offset:4096
v_and_b32 v13,3,v13
v_mfma_f32_16x16x32_bf16 v[240:243],a[100:103],a[252:255],v[240:243]
ds_read_b128 a[212:215],v49 offset:4096
v_lshl_add_u32 v13,v15,2,v13
v_mfma_f32_16x16x32_bf16 v[240:243],a[96:99],a[248:251],v[240:243]
ds_read_b128 a[216:219],v48 offset:6144
v_xor_b32 v48,98304,v48
v_mfma_f32_16x16x32_bf16 v[148:151],a[104:107],a[224:227],v[148:151]
v_mul_u32_u24 v12,1280,v12
v_lshl_add_u32 v12,v13,4,v12
v_mfma_f32_16x16x32_bf16 v[148:151],a[108:111],a[228:231],v[148:151]
ds_read_b128 a[220:223],v49 offset:6144
v_xor_b32 v49,98304,v49
v_mfma_f32_16x16x32_bf16 v[180:183],a[108:111],a[236:239],v[180:183]
ds_read_b64_tr_b16 a[64:65],v50
v_add_u32 v12,s66,v12
v_mfma_f32_16x16x32_bf16 v[180:183],a[104:107],a[232:235],v[180:183]
ds_read_b64_tr_b16 a[66:67],v50 offset:2048
v_subrev_u32 v7,0,v12
v_mfma_f32_16x16x32_bf16 v[212:215],a[104:107],a[240:243],v[212:215]
v_mfma_f32_16x16x32_bf16 v[212:215],a[108:111],a[244:247],v[212:215]
ds_read_b64_tr_b16 a[68:69],v50 offset:16384
v_mfma_f32_16x16x32_bf16 v[244:247],a[108:111],a[252:255],v[244:247]
ds_read_b64_tr_b16 a[70:71],v50 offset:18432
v_mfma_f32_16x16x32_bf16 v[244:247],a[104:107],a[248:251],v[244:247]
ds_read_b64_tr_b16 a[72:73],v50 offset:8
v_mfma_f32_16x16x32_bf16 v[152:155],a[112:115],a[224:227],v[152:155]
v_mfma_f32_16x16x32_bf16 v[152:155],a[116:119],a[228:231],v[152:155]
ds_read_b64_tr_b16 a[74:75],v50 offset:2056
v_mfma_f32_16x16x32_bf16 v[184:187],a[116:119],a[236:239],v[184:187]
global_load_lds_dwordx4 v4,s[10:11] offset:1024 nt
ds_read_b64_tr_b16 a[76:77],v50 offset:16392
v_mfma_f32_16x16x32_bf16 v[184:187],a[112:115],a[232:235],v[184:187]
ds_read_b64_tr_b16 a[78:79],v50 offset:18440
v_mfma_f32_16x16x32_bf16 v[216:219],a[112:115],a[240:243],v[216:219]
v_mfma_f32_16x16x32_bf16 v[216:219],a[116:119],a[244:247],v[216:219]
ds_read_b64_tr_b16 a[80:81],v50 offset:1024
v_mfma_f32_16x16x32_bf16 v[248:251],a[116:119],a[252:255],v[248:251]
ds_read_b64_tr_b16 a[82:83],v50 offset:3072
v_mfma_f32_16x16x32_bf16 v[248:251],a[112:115],a[248:251],v[248:251]
ds_read_b64_tr_b16 a[84:85],v50 offset:17408
v_mfma_f32_16x16x32_bf16 v[156:159],a[120:123],a[224:227],v[156:159]
v_mfma_f32_16x16x32_bf16 v[156:159],a[124:127],a[228:231],v[156:159]
ds_read_b64_tr_b16 a[86:87],v50 offset:19456
v_mfma_f32_16x16x32_bf16 v[188:191],a[124:127],a[236:239],v[188:191]
ds_read_b64_tr_b16 a[88:89],v50 offset:1032
v_mfma_f32_16x16x32_bf16 v[188:191],a[120:123],a[232:235],v[188:191]
s_cmp_lt_u32 s92,s93
s_cselect_b32 s5,6,0
s_cselect_b32 s7,3,0
s_cselect_b32 s9,8,10
ds_read_b64_tr_b16 a[90:91],v50 offset:3080
v_mfma_f32_16x16x32_bf16 v[220:223],a[120:123],a[240:243],v[220:223]
s_sub_u32 s93,s93,s92
s_max_i32 s93,s93,0
s_lshr_b32 s93,s93,8
v_mfma_f32_16x16x32_bf16 v[220:223],a[124:127],a[244:247],v[220:223]
s_mul_i32 s93,s93,95
s_lshr_b32 s93,s93,16
ds_read_b64_tr_b16 a[92:93],v50 offset:17416
v_mfma_f32_16x16x32_bf16 v[252:255],a[124:127],a[252:255],v[252:255]
s_add_u32 s93,s93,1
s_max_i32 s93,s93,1
ds_read_b64_tr_b16 a[94:95],v50 offset:19464
v_mfma_f32_16x16x32_bf16 v[252:255],a[120:123],a[248:251],v[252:255]
s_min_u32 s93,s93,6
s_mov_b32 s4,s5
s_waitcnt vmcnt(10)
ds_read_b128 a[224:227],v48
s_waitcnt lgkmcnt(6)
v_mfma_f32_16x16x32_bf16 v[128:131],a[64:67],a[192:195],v[128:131]
s_cmp_lg_u32 s7,0
s_cselect_b32 s7,s93,0
v_mfma_f32_16x16x32_bf16 v[128:131],a[68:71],a[196:199],v[128:131]
ds_read_b128 a[228:231],v49
v_mfma_f32_16x16x32_bf16 v[160:163],a[68:71],a[204:207],v[160:163]
global_load_lds_dwordx4 v5,s[10:11] offset:2048 nt
ds_read_b128 a[236:239],v49 offset:2048
v_mfma_f32_16x16x32_bf16 v[160:163],a[64:67],a[200:203],v[160:163]
ds_read_b128 a[232:235],v48 offset:2048
v_mfma_f32_16x16x32_bf16 v[192:195],a[64:67],a[208:211],v[192:195]
v_mfma_f32_16x16x32_bf16 v[192:195],a[68:71],a[212:215],v[192:195]
ds_read_b128 a[240:243],v48 offset:4096
v_mfma_f32_16x16x32_bf16 v[224:227],a[68:71],a[220:223],v[224:227]
ds_read_b128 a[244:247],v49 offset:4096
v_mfma_f32_16x16x32_bf16 v[224:227],a[64:67],a[216:219],v[224:227]
ds_read_b128 a[252:255],v49 offset:6144
v_mfma_f32_16x16x32_bf16 v[132:135],a[72:75],a[192:195],v[132:135]
v_mfma_f32_16x16x32_bf16 v[132:135],a[76:79],a[196:199],v[132:135]
ds_read_b128 a[248:251],v48 offset:6144
v_mfma_f32_16x16x32_bf16 v[164:167],a[76:79],a[204:207],v[164:167]
ds_read_b64_tr_b16 a[96:97],v50 offset:256
v_mfma_f32_16x16x32_bf16 v[164:167],a[72:75],a[200:203],v[164:167]
ds_read_b64_tr_b16 a[98:99],v50 offset:2304
v_mfma_f32_16x16x32_bf16 v[196:199],a[72:75],a[208:211],v[196:199]
v_mfma_f32_16x16x32_bf16 v[196:199],a[76:79],a[212:215],v[196:199]
ds_read_b64_tr_b16 a[100:101],v50 offset:16640
v_mfma_f32_16x16x32_bf16 v[228:231],a[76:79],a[220:223],v[228:231]
ds_read_b64_tr_b16 a[102:103],v50 offset:18688
v_mfma_f32_16x16x32_bf16 v[228:231],a[72:75],a[216:219],v[228:231]
s_waitcnt lgkmcnt(5)
ds_read_b64_tr_b16 a[104:105],v50 offset:1280
v_mfma_f32_16x16x32_bf16 v[128:131],a[80:83],a[224:227],v[128:131]
v_mfma_f32_16x16x32_bf16 v[128:131],a[84:87],a[228:231],v[128:131]
ds_read_b64_tr_b16 a[106:107],v50 offset:3328
v_mfma_f32_16x16x32_bf16 v[160:163],a[84:87],a[236:239],v[160:163]
global_load_lds_dwordx4 v5,s[10:11] offset:3072
ds_read_b64_tr_b16 a[108:109],v50 offset:17664
v_mfma_f32_16x16x32_bf16 v[160:163],a[80:83],a[232:235],v[160:163]
ds_read_b64_tr_b16 a[110:111],v50 offset:19712
v_mfma_f32_16x16x32_bf16 v[192:195],a[80:83],a[240:243],v[192:195]
v_mfma_f32_16x16x32_bf16 v[192:195],a[84:87],a[244:247],v[192:195]
ds_read_b64_tr_b16 a[112:113],v50 offset:264
v_mul_f32 v128,s71,v128
v_mfma_f32_16x16x32_bf16 v[224:227],a[84:87],a[252:255],v[224:227]
s_waitcnt lgkmcnt(5)
ds_read_b64_tr_b16 a[114:115],v50 offset:2312
v_mul_f32 v129,s71,v129
v_mfma_f32_16x16x32_bf16 v[224:227],a[80:83],a[248:251],v[224:227]
ds_read_b64_tr_b16 a[116:117],v50 offset:16648
v_mul_f32 v130,s71,v130
v_mfma_f32_16x16x32_bf16 v[132:135],a[88:91],a[224:227],v[132:135]
v_mul_f32 v131,s71,v131
v_cvt_pk_bf16_f32 v128,v128,v129
v_mfma_f32_16x16x32_bf16 v[132:135],a[92:95],a[228:231],v[132:135]
ds_read_b64_tr_b16 a[118:119],v50 offset:18696
v_cvt_pk_bf16_f32 v129,v130,v131
v_mfma_f32_16x16x32_bf16 v[164:167],a[92:95],a[236:239],v[164:167]
ds_read_b64_tr_b16 a[120:121],v50 offset:1288
v_mul_f32 v192,s71,v192
v_mfma_f32_16x16x32_bf16 v[164:167],a[88:91],a[232:235],v[164:167]
ds_read_b64_tr_b16 a[122:123],v50 offset:3336
v_mul_f32 v193,s71,v193
v_mfma_f32_16x16x32_bf16 v[196:199],a[88:91],a[240:243],v[196:199]
v_mul_f32 v132,s71,v132
v_mul_f32 v133,s71,v133
v_mfma_f32_16x16x32_bf16 v[196:199],a[92:95],a[244:247],v[196:199]
ds_read_b64_tr_b16 a[124:125],v50 offset:17672
v_mul_f32 v134,s71,v134
v_mfma_f32_16x16x32_bf16 v[228:231],a[92:95],a[252:255],v[228:231]
s_add_u32 s10,s24,s74
s_addc_u32 s11,s25,0
ds_read_b64_tr_b16 a[126:127],v50 offset:19720
v_mfma_f32_16x16x32_bf16 v[228:231],a[88:91],a[248:251],v[228:231]
s_add_u32 s74,s74,16384
s_and_b32 s74,s74,0x3ffff
ds_read_b64_tr_b16 a[128:129],v50 offset:512
v_mfma_f32_16x16x32_bf16 v[136:139],a[96:99],a[192:195],v[136:139]
s_add_u32 m0,s76,16384
s_or_b32 s74,s74,s69
s_sub_u32 s76,s76,32768
v_mfma_f32_16x16x32_bf16 v[136:139],a[100:103],a[196:199],v[136:139]
s_and_b32 s76,s76,0x1ffff
s_and_b32 s69,s75,0xfffc0000
ds_read_b64_tr_b16 a[130:131],v50 offset:2560
s_waitcnt lgkmcnt(6)
v_mfma_f32_16x16x32_bf16 v[136:139],a[104:107],a[224:227],v[136:139]
global_load_lds_dwordx4 v4,s[10:11]
ds_read_b64_tr_b16 a[132:133],v50 offset:16896
v_mfma_f32_16x16x32_bf16 v[136:139],a[108:111],a[228:231],v[136:139]
s_add_u32 s75,s75,16384
ds_read_b64_tr_b16 a[134:135],v50 offset:18944
v_mul_f32 v135,s71,v135
v_mfma_f32_16x16x32_bf16 v[168:171],a[108:111],a[236:239],v[168:171]
v_cvt_pk_bf16_f32 v130,v132,v133
v_cvt_pk_bf16_f32 v131,v134,v135
v_mfma_f32_16x16x32_bf16 v[168:171],a[104:107],a[232:235],v[168:171]
ds_read_b64_tr_b16 a[136:137],v50 offset:1536
v_mul_f32 v194,s71,v194
v_mfma_f32_16x16x32_bf16 v[168:171],a[100:103],a[204:207],v[168:171]
ds_read_b64_tr_b16 a[138:139],v50 offset:3584
v_mul_f32 v195,s71,v195
v_mfma_f32_16x16x32_bf16 v[168:171],a[96:99],a[200:203],v[168:171]
ds_read_b64_tr_b16 a[140:141],v50 offset:17920
v_mul_f32 v196,s71,v196
v_mfma_f32_16x16x32_bf16 v[200:203],a[96:99],a[208:211],v[200:203]
v_mul_f32 v197,s71,v197
v_mul_f32 v198,s71,v198
v_mfma_f32_16x16x32_bf16 v[200:203],a[100:103],a[212:215],v[200:203]
ds_read_b64_tr_b16 a[142:143],v50 offset:19968
v_mul_f32 v199,s71,v199
v_mfma_f32_16x16x32_bf16 v[200:203],a[104:107],a[240:243],v[200:203]
ds_read_b64_tr_b16 a[144:145],v50 offset:520
v_cvt_pk_bf16_f32 v192,v192,v193
v_mfma_f32_16x16x32_bf16 v[200:203],a[108:111],a[244:247],v[200:203]
ds_read_b64_tr_b16 a[146:147],v50 offset:2568
v_cvt_pk_bf16_f32 v193,v194,v195
v_mfma_f32_16x16x32_bf16 v[232:235],a[108:111],a[252:255],v[232:235]
v_cvt_pk_bf16_f32 v194,v196,v197
v_cvt_pk_bf16_f32 v195,v198,v199
v_mfma_f32_16x16x32_bf16 v[232:235],a[104:107],a[248:251],v[232:235]
ds_read_b64_tr_b16 a[148:149],v50 offset:16904
v_mul_f32 v136,s71,v136
v_mfma_f32_16x16x32_bf16 v[232:235],a[100:103],a[220:223],v[232:235]
ds_read_b64_tr_b16 a[150:151],v50 offset:18952
v_mul_f32 v137,s71,v137
v_mfma_f32_16x16x32_bf16 v[232:235],a[96:99],a[216:219],v[232:235]
ds_read_b64_tr_b16 a[152:153],v50 offset:1544
v_mul_f32 v138,s71,v138
v_mfma_f32_16x16x32_bf16 v[140:143],a[112:115],a[192:195],v[140:143]
v_mul_f32 v139,s71,v139
v_cvt_pk_bf16_f32 v136,v136,v137
v_mfma_f32_16x16x32_bf16 v[140:143],a[116:119],a[196:199],v[140:143]
s_waitcnt lgkmcnt(5)
ds_read_b64_tr_b16 a[154:155],v50 offset:3592
v_cvt_pk_bf16_f32 v137,v138,v139
v_mfma_f32_16x16x32_bf16 v[140:143],a[120:123],a[224:227],v[140:143]
global_load_lds_dwordx4 v5,s[10:11] offset:1024
ds_read_b64_tr_b16 a[156:157],v50 offset:17928
v_mfma_f32_16x16x32_bf16 v[140:143],a[124:127],a[228:231],v[140:143]
ds_read_b64_tr_b16 a[158:159],v50 offset:19976
v_mul_f32 v160,s71,v160
v_mfma_f32_16x16x32_bf16 v[172:175],a[124:127],a[236:239],v[172:175]
v_mul_f32 v161,s71,v161
v_mul_f32 v162,s71,v162
v_mfma_f32_16x16x32_bf16 v[172:175],a[120:123],a[232:235],v[172:175]
ds_read_b64_tr_b16 a[160:161],v50 offset:768
v_mul_f32 v163,s71,v163
)A"
R"A(v_mfma_f32_16x16x32_bf16 v[172:175],a[116:119],a[204:207],v[172:175]
ds_read_b64_tr_b16 a[162:163],v50 offset:2816
v_mul_f32 v140,s71,v140
v_mfma_f32_16x16x32_bf16 v[172:175],a[112:115],a[200:203],v[172:175]
ds_read_b64_tr_b16 a[164:165],v50 offset:17152
v_mul_f32 v141,s71,v141
v_mfma_f32_16x16x32_bf16 v[204:207],a[112:115],a[208:211],v[204:207]
v_mul_f32 v142,s71,v142
v_mul_f32 v143,s71,v143
v_mfma_f32_16x16x32_bf16 v[204:207],a[116:119],a[212:215],v[204:207]
ds_read_b64_tr_b16 a[166:167],v50 offset:19200
v_cvt_pk_bf16_f32 v138,v140,v141
v_mfma_f32_16x16x32_bf16 v[204:207],a[120:123],a[240:243],v[204:207]
ds_read_b64_tr_b16 a[168:169],v50 offset:1792
v_cvt_pk_bf16_f32 v139,v142,v143
v_mfma_f32_16x16x32_bf16 v[204:207],a[124:127],a[244:247],v[204:207]
ds_read_b64_tr_b16 a[170:171],v50 offset:3840
v_mul_f32 v164,s71,v164
v_mfma_f32_16x16x32_bf16 v[236:239],a[124:127],a[252:255],v[236:239]
v_mul_f32 v165,s71,v165
v_mul_f32 v166,s71,v166
v_mfma_f32_16x16x32_bf16 v[236:239],a[120:123],a[248:251],v[236:239]
ds_read_b64_tr_b16 a[172:173],v50 offset:18176
v_mul_f32 v167,s71,v167
v_mfma_f32_16x16x32_bf16 v[236:239],a[116:119],a[220:223],v[236:239]
ds_read_b64_tr_b16 a[174:175],v50 offset:20224
v_cvt_pk_bf16_f32 v160,v160,v161
v_mfma_f32_16x16x32_bf16 v[236:239],a[112:115],a[216:219],v[236:239]
ds_read_b64_tr_b16 a[176:177],v50 offset:776
v_cvt_pk_bf16_f32 v161,v162,v163
v_mfma_f32_16x16x32_bf16 v[144:147],a[128:131],a[192:195],v[144:147]
v_cvt_pk_bf16_f32 v162,v164,v165
v_cvt_pk_bf16_f32 v163,v166,v167
v_mfma_f32_16x16x32_bf16 v[144:147],a[132:135],a[196:199],v[144:147]
ds_read_b64_tr_b16 a[178:179],v50 offset:2824
v_mul_f32 v224,s71,v224
v_mfma_f32_16x16x32_bf16 v[144:147],a[136:139],a[224:227],v[144:147]
global_load_lds_dwordx4 v4,s[10:11] offset:2048
ds_read_b64_tr_b16 a[180:181],v50 offset:17160
v_mfma_f32_16x16x32_bf16 v[144:147],a[140:143],a[228:231],v[144:147]
ds_read_b64_tr_b16 a[182:183],v50 offset:19208
v_mul_f32 v225,s71,v225
v_mfma_f32_16x16x32_bf16 v[176:179],a[140:143],a[236:239],v[176:179]
v_mul_f32 v226,s71,v226
v_mul_f32 v227,s71,v227
v_mfma_f32_16x16x32_bf16 v[176:179],a[136:139],a[232:235],v[176:179]
ds_read_b64_tr_b16 a[184:185],v50 offset:1800
v_mul_f32 v228,s71,v228
v_mfma_f32_16x16x32_bf16 v[176:179],a[132:135],a[204:207],v[176:179]
ds_read_b64_tr_b16 a[186:187],v50 offset:3848
v_mul_f32 v144,s71,v144
v_mfma_f32_16x16x32_bf16 v[176:179],a[128:131],a[200:203],v[176:179]
ds_read_b64_tr_b16 a[188:189],v50 offset:18184
v_mul_f32 v145,s71,v145
v_mfma_f32_16x16x32_bf16 v[208:211],a[128:131],a[208:211],v[208:211]
v_mul_f32 v146,s71,v146
v_mul_f32 v147,s71,v147
v_mfma_f32_16x16x32_bf16 v[208:211],a[132:135],a[212:215],v[208:211]
ds_read_b64_tr_b16 a[190:191],v50 offset:20232
v_cvt_pk_bf16_f32 v144,v144,v145
v_mfma_f32_16x16x32_bf16 v[208:211],a[136:139],a[240:243],v[208:211]
v_cvt_pk_bf16_f32 v145,v146,v147
v_mul_f32 v229,s71,v229
v_mfma_f32_16x16x32_bf16 v[208:211],a[140:143],a[244:247],v[208:211]
v_mul_f32 v230,s71,v230
v_mul_f32 v231,s71,v231
v_mfma_f32_16x16x32_bf16 v[240:243],a[140:143],a[252:255],v[240:243]
v_cvt_pk_bf16_f32 v224,v224,v225
v_cvt_pk_bf16_f32 v225,v226,v227
v_mfma_f32_16x16x32_bf16 v[240:243],a[136:139],a[248:251],v[240:243]
v_cvt_pk_bf16_f32 v226,v228,v229
v_cvt_pk_bf16_f32 v227,v230,v231
v_mfma_f32_16x16x32_bf16 v[240:243],a[132:135],a[220:223],v[240:243]
v_mul_f32 v168,s71,v168
v_mul_f32 v169,s71,v169
v_mfma_f32_16x16x32_bf16 v[240:243],a[128:131],a[216:219],v[240:243]
v_mul_f32 v170,s71,v170
v_mul_f32 v171,s71,v171
s_waitcnt vmcnt(7)
s_barrier
L_pd3_%=:
s_cbranch_execz L_pd7_%=
s_waitcnt lgkmcnt(1)
v_mfma_f32_16x16x32_bf16 v[148:151],a[144:147],a[192:195],v[148:151]
ds_read_b64_tr_b16 a[64:65],v52
v_mul_f32 v172,s71,v172
v_mfma_f32_16x16x32_bf16 v[148:151],a[148:151],a[196:199],v[148:151]
global_load_lds_dwordx4 v5,s[10:11] offset:3072
ds_read_b64_tr_b16 a[66:67],v52 offset:2048
v_mfma_f32_16x16x32_bf16 v[148:151],a[152:155],a[224:227],v[148:151]
ds_read_b64_tr_b16 a[68:69],v52 offset:16384
v_mul_f32 v173,s71,v173
v_mfma_f32_16x16x32_bf16 v[148:151],a[156:159],a[228:231],v[148:151]
ds_read_b64_tr_b16 a[70:71],v52 offset:18432
v_mul_f32 v174,s71,v174
v_mfma_f32_16x16x32_bf16 v[180:183],a[156:159],a[236:239],v[180:183]
v_mul_f32 v175,s71,v175
v_cvt_pk_bf16_f32 v168,v168,v169
v_mfma_f32_16x16x32_bf16 v[180:183],a[152:155],a[232:235],v[180:183]
ds_read_b64_tr_b16 a[72:73],v52 offset:1024
v_cvt_pk_bf16_f32 v169,v170,v171
v_mfma_f32_16x16x32_bf16 v[180:183],a[148:151],a[204:207],v[180:183]
ds_read_b64_tr_b16 a[74:75],v52 offset:3072
v_mul_f32 v148,s71,v148
v_mfma_f32_16x16x32_bf16 v[180:183],a[144:147],a[200:203],v[180:183]
ds_read_b64_tr_b16 a[76:77],v52 offset:17408
v_mul_f32 v149,s71,v149
v_mfma_f32_16x16x32_bf16 v[212:215],a[144:147],a[208:211],v[212:215]
v_mul_f32 v150,s71,v150
v_mul_f32 v151,s71,v151
v_mfma_f32_16x16x32_bf16 v[212:215],a[148:151],a[212:215],v[212:215]
ds_read_b64_tr_b16 a[78:79],v52 offset:19456
v_cvt_pk_bf16_f32 v146,v148,v149
v_mfma_f32_16x16x32_bf16 v[212:215],a[152:155],a[240:243],v[212:215]
ds_read_b64_tr_b16 a[80:81],v52 offset:8
v_cvt_pk_bf16_f32 v147,v150,v151
v_mfma_f32_16x16x32_bf16 v[212:215],a[156:159],a[244:247],v[212:215]
ds_read_b64_tr_b16 a[82:83],v52 offset:2056
v_cvt_pk_bf16_f32 v170,v172,v173
v_mfma_f32_16x16x32_bf16 v[244:247],a[156:159],a[252:255],v[244:247]
v_cvt_pk_bf16_f32 v171,v174,v175
v_mul_f32 v176,s71,v176
v_mfma_f32_16x16x32_bf16 v[244:247],a[152:155],a[248:251],v[244:247]
ds_read_b64_tr_b16 a[84:85],v52 offset:16392
v_mul_f32 v177,s71,v177
v_mfma_f32_16x16x32_bf16 v[244:247],a[148:151],a[220:223],v[244:247]
s_add_u32 s10,s20,s74
s_addc_u32 s11,s21,0
ds_read_b64_tr_b16 a[86:87],v52 offset:18440
v_mfma_f32_16x16x32_bf16 v[244:247],a[144:147],a[216:219],v[244:247]
ds_read_b64_tr_b16 a[88:89],v52 offset:1032
v_mul_f32 v178,s71,v178
v_mfma_f32_16x16x32_bf16 v[152:155],a[160:163],a[192:195],v[152:155]
s_add_u32 m0,s76,0
v_mul_f32 v179,s71,v179
v_mul_f32 v180,s71,v180
v_mfma_f32_16x16x32_bf16 v[152:155],a[164:167],a[196:199],v[152:155]
ds_read_b64_tr_b16 a[90:91],v52 offset:3080
v_mul_f32 v181,s71,v181
v_mfma_f32_16x16x32_bf16 v[152:155],a[168:171],a[224:227],v[152:155]
global_load_lds_dwordx4 v4,s[10:11] nt
ds_read_b64_tr_b16 a[92:93],v52 offset:17416
v_mfma_f32_16x16x32_bf16 v[152:155],a[172:175],a[228:231],v[152:155]
ds_read_b64_tr_b16 a[94:95],v52 offset:19464
v_mul_f32 v182,s71,v182
v_mfma_f32_16x16x32_bf16 v[184:187],a[172:175],a[236:239],v[184:187]
v_mul_f32 v183,s71,v183
v_cvt_pk_bf16_f32 v176,v176,v177
v_mfma_f32_16x16x32_bf16 v[184:187],a[168:171],a[232:235],v[184:187]
ds_read_b64_tr_b16 a[96:97],v52 offset:256
v_cvt_pk_bf16_f32 v177,v178,v179
v_mfma_f32_16x16x32_bf16 v[184:187],a[164:167],a[204:207],v[184:187]
ds_read_b64_tr_b16 a[98:99],v52 offset:2304
v_mul_f32 v152,s71,v152
v_mfma_f32_16x16x32_bf16 v[184:187],a[160:163],a[200:203],v[184:187]
ds_read_b64_tr_b16 a[100:101],v52 offset:16640
v_mul_f32 v153,s71,v153
v_mfma_f32_16x16x32_bf16 v[216:219],a[160:163],a[208:211],v[216:219]
v_mul_f32 v154,s71,v154
v_mul_f32 v155,s71,v155
v_mfma_f32_16x16x32_bf16 v[216:219],a[164:167],a[212:215],v[216:219]
ds_read_b64_tr_b16 a[102:103],v52 offset:18688
v_cvt_pk_bf16_f32 v152,v152,v153
v_mfma_f32_16x16x32_bf16 v[216:219],a[168:171],a[240:243],v[216:219]
ds_read_b64_tr_b16 a[104:105],v52 offset:1280
v_cvt_pk_bf16_f32 v153,v154,v155
v_mfma_f32_16x16x32_bf16 v[216:219],a[172:175],a[244:247],v[216:219]
ds_read_b64_tr_b16 a[106:107],v52 offset:3328
v_cvt_pk_bf16_f32 v178,v180,v181
v_mfma_f32_16x16x32_bf16 v[248:251],a[172:175],a[252:255],v[248:251]
v_cvt_pk_bf16_f32 v179,v182,v183
v_mul_f32 v184,s71,v184
v_mfma_f32_16x16x32_bf16 v[248:251],a[168:171],a[248:251],v[248:251]
ds_read_b64_tr_b16 a[108:109],v52 offset:17664
v_mul_f32 v185,s71,v185
v_mfma_f32_16x16x32_bf16 v[248:251],a[164:167],a[220:223],v[248:251]
ds_read_b64_tr_b16 a[110:111],v52 offset:19712
v_mul_f32 v186,s71,v186
v_mfma_f32_16x16x32_bf16 v[248:251],a[160:163],a[216:219],v[248:251]
ds_read_b64_tr_b16 a[112:113],v52 offset:264
v_mul_f32 v187,s71,v187
v_mfma_f32_16x16x32_bf16 v[156:159],a[176:179],a[192:195],v[156:159]
v_cvt_pk_bf16_f32 v184,v184,v185
v_cvt_pk_bf16_f32 v185,v186,v187
v_mfma_f32_16x16x32_bf16 v[156:159],a[180:183],a[196:199],v[156:159]
global_load_lds_dwordx4 v4,s[10:11] offset:1024 nt
ds_read_b64_tr_b16 a[114:115],v52 offset:2312
v_mfma_f32_16x16x32_bf16 v[156:159],a[184:187],a[224:227],v[156:159]
s_waitcnt lgkmcnt(5)
ds_read_b64_tr_b16 a[116:117],v52 offset:16648
v_mul_f32 v200,s71,v200
v_mfma_f32_16x16x32_bf16 v[156:159],a[188:191],a[228:231],v[156:159]
ds_read_b64_tr_b16 a[118:119],v52 offset:18696
v_mul_f32 v201,s71,v201
v_mfma_f32_16x16x32_bf16 v[188:191],a[188:191],a[236:239],v[188:191]
v_mul_f32 v202,s71,v202
v_mul_f32 v203,s71,v203
v_mfma_f32_16x16x32_bf16 v[188:191],a[184:187],a[232:235],v[188:191]
ds_read_b64_tr_b16 a[120:121],v52 offset:1288
v_mul_f32 v204,s71,v204
v_mfma_f32_16x16x32_bf16 v[188:191],a[180:183],a[204:207],v[188:191]
ds_read_b64_tr_b16 a[122:123],v52 offset:3336
v_mul_f32 v156,s71,v156
v_mfma_f32_16x16x32_bf16 v[188:191],a[176:179],a[200:203],v[188:191]
ds_read_b64_tr_b16 a[124:125],v52 offset:17672
v_mul_f32 v157,s71,v157
v_mfma_f32_16x16x32_bf16 v[220:223],a[176:179],a[208:211],v[220:223]
v_mul_f32 v158,s71,v158
v_mul_f32 v159,s71,v159
v_mfma_f32_16x16x32_bf16 v[220:223],a[180:183],a[212:215],v[220:223]
ds_read_b64_tr_b16 a[126:127],v52 offset:19720
v_cvt_pk_bf16_f32 v154,v156,v157
v_mfma_f32_16x16x32_bf16 v[220:223],a[184:187],a[240:243],v[220:223]
v_cvt_pk_bf16_f32 v155,v158,v159
v_mul_f32 v188,s71,v188
v_mfma_f32_16x16x32_bf16 v[220:223],a[188:191],a[244:247],v[220:223]
v_mul_f32 v189,s71,v189
v_mul_f32 v190,s71,v190
v_mfma_f32_16x16x32_bf16 v[252:255],a[188:191],a[252:255],v[252:255]
v_mul_f32 v191,s71,v191
v_cvt_pk_bf16_f32 v186,v188,v189
v_mfma_f32_16x16x32_bf16 v[252:255],a[184:187],a[248:251],v[252:255]
v_cvt_pk_bf16_f32 v187,v190,v191
v_mul_f32 v205,s71,v205
v_mfma_f32_16x16x32_bf16 v[252:255],a[180:183],a[220:223],v[252:255]
v_mul_f32 v206,s71,v206
v_mul_f32 v207,s71,v207
v_mfma_f32_16x16x32_bf16 v[252:255],a[176:179],a[216:219],v[252:255]
v_cvt_pk_bf16_f32 v200,v200,v201
v_cvt_pk_bf16_f32 v201,v202,v203
v_mfma_f32_16x16x32_bf16 a[0:3],a[64:67],a[192:195],a[0:3]
global_load_lds_dwordx4 v5,s[10:11] offset:2048 nt
v_cvt_pk_bf16_f32 v202,v204,v205
v_mfma_f32_16x16x32_bf16 a[0:3],a[68:71],a[196:199],a[0:3]
v_cvt_pk_bf16_f32 v203,v206,v207
v_mul_f32 v208,s71,v208
v_mfma_f32_16x16x32_bf16 a[0:3],a[72:75],a[224:227],a[0:3]
v_mul_f32 v209,s71,v209
v_mul_f32 v210,s71,v210
v_mfma_f32_16x16x32_bf16 a[0:3],a[76:79],a[228:231],a[0:3]
v_mul_f32 v211,s71,v211
v_mul_f32 v212,s71,v212
v_mfma_f32_16x16x32_bf16 a[16:19],a[76:79],a[236:239],a[16:19]
v_mul_f32 v213,s71,v213
v_mul_f32 v214,s71,v214
v_mfma_f32_16x16x32_bf16 a[16:19],a[72:75],a[232:235],a[16:19]
v_mul_f32 v215,s71,v215
v_cvt_pk_bf16_f32 v208,v208,v209
v_mfma_f32_16x16x32_bf16 a[16:19],a[68:71],a[204:207],a[16:19]
v_accvgpr_read_b32 v8,a0
v_accvgpr_read_b32 v9,a1
v_mfma_f32_16x16x32_bf16 a[16:19],a[64:67],a[200:203],a[16:19]
v_accvgpr_read_b32 v10,a2
v_accvgpr_read_b32 v11,a3
v_mfma_f32_16x16x32_bf16 a[32:35],a[64:67],a[208:211],a[32:35]
v_mul_f32 v8,s71,v8
v_mul_f32 v9,s71,v9
v_mfma_f32_16x16x32_bf16 a[32:35],a[68:71],a[212:215],a[32:35]
v_mul_f32 v10,s71,v10
v_mul_f32 v11,s71,v11
v_mfma_f32_16x16x32_bf16 a[32:35],a[72:75],a[240:243],a[32:35]
v_cvt_pk_bf16_f32 v132,v8,v9
v_cvt_pk_bf16_f32 v133,v10,v11
v_mfma_f32_16x16x32_bf16 a[32:35],a[76:79],a[244:247],a[32:35]
v_accvgpr_read_b32 v16,a16
v_accvgpr_read_b32 v17,a17
v_mfma_f32_16x16x32_bf16 a[48:51],a[76:79],a[252:255],a[48:51]
ds_read_b64_tr_b16 a[76:77],v52 offset:17920
v_accvgpr_read_b32 v18,a18
v_mfma_f32_16x16x32_bf16 a[48:51],a[72:75],a[248:251],a[48:51]
ds_read_b64_tr_b16 a[72:73],v52 offset:1536
v_accvgpr_read_b32 v19,a19
v_mfma_f32_16x16x32_bf16 a[48:51],a[68:71],a[220:223],a[48:51]
ds_read_b64_tr_b16 a[68:69],v52 offset:16896
v_accvgpr_read_b32 v8,a32
v_mfma_f32_16x16x32_bf16 a[48:51],a[64:67],a[216:219],a[48:51]
)A"
R"A(global_load_lds_dwordx4 v5,s[10:11] offset:3072
ds_read_b64_tr_b16 a[64:65],v52 offset:512
v_mfma_f32_16x16x32_bf16 a[4:7],a[80:83],a[192:195],a[4:7]
v_accvgpr_read_b32 v9,a33
v_accvgpr_read_b32 v10,a34
v_mfma_f32_16x16x32_bf16 a[4:7],a[84:87],a[196:199],a[4:7]
ds_read_b64_tr_b16 a[66:67],v52 offset:2560
v_accvgpr_read_b32 v11,a35
v_mfma_f32_16x16x32_bf16 a[4:7],a[88:91],a[224:227],a[4:7]
ds_read_b64_tr_b16 a[70:71],v52 offset:18944
v_mul_f32 v8,s71,v8
v_mfma_f32_16x16x32_bf16 a[4:7],a[92:95],a[228:231],a[4:7]
ds_read_b64_tr_b16 a[74:75],v52 offset:3584
v_mul_f32 v9,s71,v9
v_mfma_f32_16x16x32_bf16 a[20:23],a[92:95],a[236:239],a[20:23]
v_mul_f32 v10,s71,v10
v_mul_f32 v11,s71,v11
v_mfma_f32_16x16x32_bf16 a[20:23],a[88:91],a[232:235],a[20:23]
ds_read_b64_tr_b16 a[78:79],v52 offset:19968
v_cvt_pk_bf16_f32 v196,v8,v9
v_mfma_f32_16x16x32_bf16 a[20:23],a[84:87],a[204:207],a[20:23]
v_accvgpr_read_b32 v12,a4
v_accvgpr_read_b32 v13,a5
v_mfma_f32_16x16x32_bf16 a[20:23],a[80:83],a[200:203],a[20:23]
v_accvgpr_read_b32 v14,a6
v_accvgpr_read_b32 v15,a7
v_mfma_f32_16x16x32_bf16 a[36:39],a[80:83],a[208:211],a[36:39]
v_mul_f32 v12,s71,v12
v_mul_f32 v13,s71,v13
v_mfma_f32_16x16x32_bf16 a[36:39],a[84:87],a[212:215],a[36:39]
v_mul_f32 v14,s71,v14
v_mul_f32 v15,s71,v15
v_mfma_f32_16x16x32_bf16 a[36:39],a[88:91],a[240:243],a[36:39]
s_add_u32 s10,s24,s74
s_addc_u32 s11,s25,0
s_add_u32 s74,s74,16384
v_mfma_f32_16x16x32_bf16 a[36:39],a[92:95],a[244:247],a[36:39]
s_and_b32 s74,s74,0x3ffff
s_or_b32 s74,s74,s69
v_cvt_pk_bf16_f32 v134,v12,v13
v_mfma_f32_16x16x32_bf16 a[52:55],a[92:95],a[252:255],a[52:55]
s_add_u32 m0,s76,16384
s_sub_u32 s76,s76,32768
ds_read_b64_tr_b16 a[92:93],v52 offset:17928
v_mfma_f32_16x16x32_bf16 a[52:55],a[88:91],a[248:251],a[52:55]
s_and_b32 s76,s76,0x1ffff
ds_read_b64_tr_b16 a[88:89],v52 offset:1544
v_cvt_pk_bf16_f32 v135,v14,v15
v_mfma_f32_16x16x32_bf16 a[52:55],a[84:87],a[220:223],a[52:55]
global_load_lds_dwordx4 v4,s[10:11]
ds_read_b64_tr_b16 a[84:85],v52 offset:16904
v_mfma_f32_16x16x32_bf16 a[52:55],a[80:83],a[216:219],a[52:55]
s_and_b32 s69,s75,0xfffc0000
ds_read_b64_tr_b16 a[80:81],v52 offset:520
v_accvgpr_read_b32 v12,a36
v_mfma_f32_16x16x32_bf16 a[8:11],a[96:99],a[192:195],a[8:11]
s_add_u32 s75,s75,16384
v_accvgpr_read_b32 v13,a37
v_accvgpr_read_b32 v14,a38
v_mfma_f32_16x16x32_bf16 a[8:11],a[100:103],a[196:199],a[8:11]
s_waitcnt lgkmcnt(4)
ds_read_b64_tr_b16 a[82:83],v52 offset:2568
v_accvgpr_read_b32 v15,a39
v_mfma_f32_16x16x32_bf16 a[8:11],a[104:107],a[224:227],a[8:11]
ds_read_b64_tr_b16 a[86:87],v52 offset:18952
v_mul_f32 v12,s71,v12
v_mfma_f32_16x16x32_bf16 a[8:11],a[108:111],a[228:231],a[8:11]
ds_read_b64_tr_b16 a[90:91],v52 offset:3592
v_mul_f32 v13,s71,v13
v_mfma_f32_16x16x32_bf16 a[24:27],a[108:111],a[236:239],a[24:27]
v_mul_f32 v14,s71,v14
v_mul_f32 v15,s71,v15
v_mfma_f32_16x16x32_bf16 a[24:27],a[104:107],a[232:235],a[24:27]
ds_read_b64_tr_b16 a[94:95],v52 offset:19976
v_cvt_pk_bf16_f32 v197,v10,v11
v_mfma_f32_16x16x32_bf16 a[24:27],a[100:103],a[204:207],a[24:27]
v_cvt_pk_bf16_f32 v198,v12,v13
v_cvt_pk_bf16_f32 v199,v14,v15
v_mfma_f32_16x16x32_bf16 a[24:27],a[96:99],a[200:203],a[24:27]
v_accvgpr_read_b32 v8,a8
v_accvgpr_read_b32 v9,a9
v_mfma_f32_16x16x32_bf16 a[40:43],a[96:99],a[208:211],a[40:43]
v_accvgpr_read_b32 v10,a10
v_accvgpr_read_b32 v11,a11
v_mfma_f32_16x16x32_bf16 a[40:43],a[100:103],a[212:215],a[40:43]
v_mul_f32 v8,s71,v8
v_mul_f32 v9,s71,v9
v_mfma_f32_16x16x32_bf16 a[40:43],a[104:107],a[240:243],a[40:43]
v_mul_f32 v10,s71,v10
v_mul_f32 v11,s71,v11
v_mfma_f32_16x16x32_bf16 a[40:43],a[108:111],a[244:247],a[40:43]
v_cvt_pk_bf16_f32 v140,v8,v9
v_cvt_pk_bf16_f32 v141,v10,v11
v_mfma_f32_16x16x32_bf16 a[56:59],a[108:111],a[252:255],a[56:59]
ds_read_b64_tr_b16 a[108:109],v52 offset:18176
v_accvgpr_read_b32 v20,a20
v_mfma_f32_16x16x32_bf16 a[56:59],a[104:107],a[248:251],a[56:59]
global_load_lds_dwordx4 v5,s[10:11] offset:1024
ds_read_b64_tr_b16 a[104:105],v52 offset:1792
v_mfma_f32_16x16x32_bf16 a[56:59],a[100:103],a[220:223],a[56:59]
ds_read_b64_tr_b16 a[100:101],v52 offset:17152
v_accvgpr_read_b32 v21,a21
v_mfma_f32_16x16x32_bf16 a[56:59],a[96:99],a[216:219],a[56:59]
ds_read_b64_tr_b16 a[96:97],v52 offset:768
v_accvgpr_read_b32 v22,a22
v_mfma_f32_16x16x32_bf16 a[12:15],a[112:115],a[192:195],a[12:15]
v_accvgpr_read_b32 v23,a23
v_mul_f32 v16,s71,v16
v_mfma_f32_16x16x32_bf16 a[12:15],a[116:119],a[196:199],a[12:15]
ds_read_b64_tr_b16 a[98:99],v52 offset:2816
v_mul_f32 v17,s71,v17
v_mfma_f32_16x16x32_bf16 a[12:15],a[120:123],a[224:227],a[12:15]
ds_read_b64_tr_b16 a[102:103],v52 offset:19200
v_mul_f32 v18,s71,v18
v_mfma_f32_16x16x32_bf16 a[12:15],a[124:127],a[228:231],a[12:15]
ds_read_b64_tr_b16 a[106:107],v52 offset:3840
v_mul_f32 v19,s71,v19
v_mfma_f32_16x16x32_bf16 a[28:31],a[124:127],a[236:239],a[28:31]
v_mul_f32 v20,s71,v20
v_mul_f32 v21,s71,v21
v_mfma_f32_16x16x32_bf16 a[28:31],a[120:123],a[232:235],a[28:31]
ds_read_b64_tr_b16 a[110:111],v52 offset:20224
v_mul_f32 v22,s71,v22
v_mfma_f32_16x16x32_bf16 a[28:31],a[116:119],a[204:207],a[28:31]
v_accvgpr_read_b32 v12,a12
v_accvgpr_read_b32 v13,a13
v_mfma_f32_16x16x32_bf16 a[28:31],a[112:115],a[200:203],a[28:31]
v_accvgpr_read_b32 v14,a14
v_accvgpr_read_b32 v15,a15
v_mfma_f32_16x16x32_bf16 a[44:47],a[112:115],a[208:211],a[44:47]
v_mul_f32 v12,s71,v12
v_mul_f32 v13,s71,v13
v_mfma_f32_16x16x32_bf16 a[44:47],a[116:119],a[212:215],a[44:47]
v_mul_f32 v14,s71,v14
v_mul_f32 v15,s71,v15
v_mfma_f32_16x16x32_bf16 a[44:47],a[120:123],a[240:243],a[44:47]
v_cvt_pk_bf16_f32 v142,v12,v13
v_cvt_pk_bf16_f32 v143,v14,v15
v_mfma_f32_16x16x32_bf16 a[44:47],a[124:127],a[244:247],a[44:47]
v_mul_f32 v23,s71,v23
v_cvt_pk_bf16_f32 v164,v16,v17
v_mfma_f32_16x16x32_bf16 a[60:63],a[124:127],a[252:255],a[60:63]
global_load_lds_dwordx4 v4,s[10:11] offset:2048
ds_read_b64_tr_b16 a[124:125],v52 offset:18184
v_mfma_f32_16x16x32_bf16 a[60:63],a[120:123],a[248:251],a[60:63]
ds_read_b64_tr_b16 a[120:121],v52 offset:1800
v_cvt_pk_bf16_f32 v165,v18,v19
v_mfma_f32_16x16x32_bf16 a[60:63],a[116:119],a[220:223],a[60:63]
ds_read_b64_tr_b16 a[116:117],v52 offset:17160
v_cvt_pk_bf16_f32 v166,v20,v21
v_mfma_f32_16x16x32_bf16 a[60:63],a[112:115],a[216:219],a[60:63]
ds_read_b64_tr_b16 a[112:113],v52 offset:776
v_cvt_pk_bf16_f32 v167,v22,v23
v_mfma_f32_16x16x32_bf16 v[64:67],a[64:67],a[192:195],v[64:67]
v_accvgpr_read_b32 v16,a48
v_accvgpr_read_b32 v17,a49
v_mfma_f32_16x16x32_bf16 v[64:67],a[68:71],a[196:199],v[64:67]
ds_read_b64_tr_b16 a[114:115],v52 offset:2824
v_accvgpr_read_b32 v18,a50
v_mfma_f32_16x16x32_bf16 v[64:67],a[72:75],a[224:227],v[64:67]
ds_read_b64_tr_b16 a[118:119],v52 offset:19208
v_accvgpr_read_b32 v19,a51
v_mfma_f32_16x16x32_bf16 v[64:67],a[76:79],a[228:231],v[64:67]
ds_read_b64_tr_b16 a[122:123],v52 offset:3848
v_accvgpr_read_b32 v20,a52
v_mfma_f32_16x16x32_bf16 v[80:83],a[76:79],a[236:239],v[80:83]
v_accvgpr_read_b32 v21,a53
v_accvgpr_read_b32 v22,a54
v_mfma_f32_16x16x32_bf16 v[80:83],a[72:75],a[232:235],v[80:83]
ds_read_b64_tr_b16 a[126:127],v52 offset:20232
v_accvgpr_read_b32 v23,a55
v_mfma_f32_16x16x32_bf16 v[80:83],a[68:71],a[204:207],v[80:83]
v_mul_f32 v64,s71,v64
v_mul_f32 v65,s71,v65
v_mfma_f32_16x16x32_bf16 v[80:83],a[64:67],a[200:203],v[80:83]
v_mul_f32 v66,s71,v66
v_mul_f32 v67,s71,v67
v_mfma_f32_16x16x32_bf16 v[96:99],a[64:67],a[208:211],v[96:99]
v_cvt_pk_bf16_f32 v148,v64,v65
v_cvt_pk_bf16_f32 v149,v66,v67
v_mfma_f32_16x16x32_bf16 v[96:99],a[68:71],a[212:215],v[96:99]
v_mul_f32 v16,s71,v16
v_mul_f32 v17,s71,v17
v_mfma_f32_16x16x32_bf16 v[96:99],a[72:75],a[240:243],v[96:99]
v_mul_f32 v18,s71,v18
v_mul_f32 v19,s71,v19
v_mfma_f32_16x16x32_bf16 v[96:99],a[76:79],a[244:247],v[96:99]
global_load_lds_dwordx4 v5,s[10:11] offset:3072
v_mul_f32 v20,s71,v20
v_mfma_f32_16x16x32_bf16 v[112:115],a[76:79],a[252:255],v[112:115]
v_mul_f32 v21,s71,v21
v_mul_f32 v22,s71,v22
v_mfma_f32_16x16x32_bf16 v[112:115],a[72:75],a[248:251],v[112:115]
v_mul_f32 v23,s71,v23
v_cvt_pk_bf16_f32 v228,v16,v17
v_mfma_f32_16x16x32_bf16 v[112:115],a[68:71],a[220:223],v[112:115]
v_cvt_pk_bf16_f32 v229,v18,v19
v_cvt_pk_bf16_f32 v230,v20,v21
v_mfma_f32_16x16x32_bf16 v[112:115],a[64:67],a[216:219],v[112:115]
v_cvt_pk_bf16_f32 v231,v22,v23
v_accvgpr_read_b32 v16,a24
s_waitcnt vmcnt(8) lgkmcnt(0)
s_barrier
v_mfma_f32_16x16x32_bf16 v[68:71],a[80:83],a[192:195],v[68:71]
ds_read_b128 a[128:131],v1
v_accvgpr_read_b32 v17,a25
v_mfma_f32_16x16x32_bf16 v[68:71],a[84:87],a[196:199],v[68:71]
ds_read_b128 a[132:135],v1 offset:256
v_accvgpr_read_b32 v18,a26
v_mfma_f32_16x16x32_bf16 v[68:71],a[88:91],a[224:227],v[68:71]
ds_read_b128 a[136:139],v1 offset:512
v_accvgpr_read_b32 v19,a27
v_mfma_f32_16x16x32_bf16 v[68:71],a[92:95],a[228:231],v[68:71]
ds_read_b128 a[140:143],v1 offset:768
v_accvgpr_read_b32 v20,a28
v_mfma_f32_16x16x32_bf16 v[84:87],a[92:95],a[236:239],v[84:87]
v_accvgpr_read_b32 v21,a29
v_accvgpr_read_b32 v22,a30
v_mfma_f32_16x16x32_bf16 v[84:87],a[88:91],a[232:235],v[84:87]
v_accvgpr_read_b32 v23,a31
v_mul_f32 v16,s71,v16
v_mfma_f32_16x16x32_bf16 v[84:87],a[84:87],a[204:207],v[84:87]
s_add_u32 s10,s20,s74
s_addc_u32 s11,s21,0
v_mul_f32 v68,s71,v68
v_mfma_f32_16x16x32_bf16 v[84:87],a[80:83],a[200:203],v[84:87]
v_mul_f32 v69,s71,v69
v_mul_f32 v70,s71,v70
v_mfma_f32_16x16x32_bf16 v[100:103],a[80:83],a[208:211],v[100:103]
s_add_u32 m0,s76,0
v_mul_f32 v71,s71,v71
v_cvt_pk_bf16_f32 v150,v68,v69
v_mfma_f32_16x16x32_bf16 v[100:103],a[84:87],a[212:215],v[100:103]
v_cvt_pk_bf16_f32 v151,v70,v71
v_mul_f32 v17,s71,v17
v_mfma_f32_16x16x32_bf16 v[100:103],a[88:91],a[240:243],v[100:103]
global_load_lds_dwordx4 v4,s[10:11] nt
v_mul_f32 v18,s71,v18
v_mfma_f32_16x16x32_bf16 v[100:103],a[92:95],a[244:247],v[100:103]
v_mul_f32 v19,s71,v19
v_mul_f32 v20,s71,v20
v_mfma_f32_16x16x32_bf16 v[116:119],a[92:95],a[252:255],v[116:119]
v_mul_f32 v21,s71,v21
v_mul_f32 v22,s71,v22
v_mfma_f32_16x16x32_bf16 v[116:119],a[88:91],a[248:251],v[116:119]
v_mul_f32 v23,s71,v23
v_cvt_pk_bf16_f32 v172,v16,v17
v_mfma_f32_16x16x32_bf16 v[116:119],a[84:87],a[220:223],v[116:119]
v_cvt_pk_bf16_f32 v173,v18,v19
v_cvt_pk_bf16_f32 v174,v20,v21
v_mfma_f32_16x16x32_bf16 v[116:119],a[80:83],a[216:219],v[116:119]
v_cvt_pk_bf16_f32 v175,v22,v23
v_mul_f32 v80,s71,v80
v_mfma_f32_16x16x32_bf16 v[72:75],a[96:99],a[192:195],v[72:75]
v_mul_f32 v81,s71,v81
v_mul_f32 v82,s71,v82
v_mfma_f32_16x16x32_bf16 v[72:75],a[100:103],a[196:199],v[72:75]
v_mul_f32 v83,s71,v83
v_mul_f32 v84,s71,v84
v_mfma_f32_16x16x32_bf16 v[72:75],a[104:107],a[224:227],v[72:75]
v_mul_f32 v85,s71,v85
v_mul_f32 v86,s71,v86
v_mfma_f32_16x16x32_bf16 v[72:75],a[108:111],a[228:231],v[72:75]
v_mul_f32 v87,s71,v87
v_cvt_pk_bf16_f32 v180,v80,v81
v_mfma_f32_16x16x32_bf16 v[88:91],a[108:111],a[236:239],v[88:91]
v_cvt_pk_bf16_f32 v181,v82,v83
v_cvt_pk_bf16_f32 v182,v84,v85
v_mfma_f32_16x16x32_bf16 v[88:91],a[104:107],a[232:235],v[88:91]
v_cvt_pk_bf16_f32 v183,v86,v87
v_accvgpr_read_b32 v8,a40
v_mfma_f32_16x16x32_bf16 v[88:91],a[100:103],a[204:207],v[88:91]
v_mul_f32 v72,s71,v72
v_mul_f32 v73,s71,v73
v_mfma_f32_16x16x32_bf16 v[88:91],a[96:99],a[200:203],v[88:91]
v_mul_f32 v74,s71,v74
v_mul_f32 v75,s71,v75
v_mfma_f32_16x16x32_bf16 v[104:107],a[96:99],a[208:211],v[104:107]
global_load_lds_dwordx4 v4,s[10:11] offset:1024 nt
v_cvt_pk_bf16_f32 v156,v72,v73
v_mfma_f32_16x16x32_bf16 v[104:107],a[100:103],a[212:215],v[104:107]
v_cvt_pk_bf16_f32 v157,v74,v75
v_accvgpr_read_b32 v9,a41
v_mfma_f32_16x16x32_bf16 v[104:107],a[104:107],a[240:243],v[104:107]
v_mul_f32 v88,s71,v88
v_mul_f32 v89,s71,v89
v_mfma_f32_16x16x32_bf16 v[104:107],a[108:111],a[244:247],v[104:107]
v_mul_f32 v90,s71,v90
v_mul_f32 v91,s71,v91
v_mfma_f32_16x16x32_bf16 v[120:123],a[108:111],a[252:255],v[120:123]
v_cvt_pk_bf16_f32 v188,v88,v89
v_cvt_pk_bf16_f32 v189,v90,v91
v_mfma_f32_16x16x32_bf16 v[120:123],a[104:107],a[248:251],v[120:123]
v_accvgpr_read_b32 v10,a42
v_accvgpr_read_b32 v11,a43
v_mfma_f32_16x16x32_bf16 v[120:123],a[100:103],a[220:223],v[120:123]
v_accvgpr_read_b32 v12,a44
v_accvgpr_read_b32 v13,a45
v_mfma_f32_16x16x32_bf16 v[120:123],a[96:99],a[216:219],v[120:123]
)A"
R"A(v_accvgpr_read_b32 v14,a46
v_accvgpr_read_b32 v15,a47
v_mfma_f32_16x16x32_bf16 v[76:79],a[112:115],a[192:195],v[76:79]
v_mul_f32 v8,s71,v8
v_mul_f32 v9,s71,v9
v_mfma_f32_16x16x32_bf16 v[76:79],a[116:119],a[196:199],v[76:79]
v_mul_f32 v10,s71,v10
v_mul_f32 v11,s71,v11
v_mfma_f32_16x16x32_bf16 v[76:79],a[120:123],a[224:227],v[76:79]
v_mul_f32 v12,s71,v12
v_mul_f32 v13,s71,v13
v_mfma_f32_16x16x32_bf16 v[76:79],a[124:127],a[228:231],v[76:79]
v_mul_f32 v14,s71,v14
v_mul_f32 v15,s71,v15
v_mfma_f32_16x16x32_bf16 v[92:95],a[124:127],a[236:239],v[92:95]
v_cvt_pk_bf16_f32 v204,v8,v9
v_cvt_pk_bf16_f32 v205,v10,v11
v_mfma_f32_16x16x32_bf16 v[92:95],a[120:123],a[232:235],v[92:95]
v_cvt_pk_bf16_f32 v206,v12,v13
v_cvt_pk_bf16_f32 v207,v14,v15
v_mfma_f32_16x16x32_bf16 v[92:95],a[116:119],a[204:207],v[92:95]
global_load_lds_dwordx4 v5,s[10:11] offset:2048 nt
v_mul_f32 v76,s71,v76
v_mfma_f32_16x16x32_bf16 v[92:95],a[112:115],a[200:203],v[92:95]
v_mul_f32 v77,s71,v77
v_mul_f32 v78,s71,v78
v_mfma_f32_16x16x32_bf16 v[108:111],a[112:115],a[208:211],v[108:111]
v_mul_f32 v79,s71,v79
v_cvt_pk_bf16_f32 v158,v76,v77
v_mfma_f32_16x16x32_bf16 v[108:111],a[116:119],a[212:215],v[108:111]
ds_read_b128 a[156:159],v1 offset:4864
v_cvt_pk_bf16_f32 v159,v78,v79
v_mfma_f32_16x16x32_bf16 v[108:111],a[120:123],a[240:243],v[108:111]
v_mul_f32 v92,s71,v92
v_mul_f32 v93,s71,v93
v_mfma_f32_16x16x32_bf16 v[108:111],a[124:127],a[244:247],v[108:111]
v_mul_f32 v94,s71,v94
v_mul_f32 v95,s71,v95
v_mfma_f32_16x16x32_bf16 v[124:127],a[124:127],a[252:255],v[124:127]
v_cvt_pk_bf16_f32 v190,v92,v93
v_cvt_pk_bf16_f32 v191,v94,v95
v_mfma_f32_16x16x32_bf16 v[124:127],a[120:123],a[248:251],v[124:127]
v_cvt_pk_bf16_f32 v209,v210,v211
v_cvt_pk_bf16_f32 v210,v212,v213
v_mfma_f32_16x16x32_bf16 v[124:127],a[116:119],a[220:223],v[124:127]
ds_read_b128 a[152:155],v1 offset:4608
v_cvt_pk_bf16_f32 v211,v214,v215
v_mfma_f32_16x16x32_bf16 v[124:127],a[112:115],a[216:219],v[124:127]
v_mul_f32 v96,s71,v96
v_mul_f32 v97,s71,v97
s_waitcnt lgkmcnt(2)
v_mfma_f32_16x16x32_bf16 v[64:67],a[128:131],v[132:135],0
v_mul_f32 v98,s71,v98
v_mul_f32 v99,s71,v99
v_mfma_f32_16x16x32_bf16 v[64:67],a[132:135],v[140:143],v[64:67]
v_mul_f32 v100,s71,v100
v_mul_f32 v101,s71,v101
v_mfma_f32_16x16x32_bf16 v[64:67],a[136:139],v[148:151],v[64:67]
v_mul_f32 v102,s71,v102
v_mul_f32 v103,s71,v103
v_mfma_f32_16x16x32_bf16 v[64:67],a[140:143],v[156:159],v[64:67]
global_load_lds_dwordx4 v5,s[10:11] offset:3072
v_cvt_pk_bf16_f32 v212,v96,v97
v_mfma_f32_16x16x32_bf16 v[80:83],a[128:131],v[164:167],0
v_cvt_pk_bf16_f32 v213,v98,v99
v_cvt_pk_bf16_f32 v214,v100,v101
v_mfma_f32_16x16x32_bf16 v[80:83],a[132:135],v[172:175],v[80:83]
v_cvt_pk_bf16_f32 v215,v102,v103
v_mul_f32 v216,s71,v216
v_mfma_f32_16x16x32_bf16 v[80:83],a[136:139],v[180:183],v[80:83]
v_max3_f32 v64,v64,v65,v66
v_max_f32 v64,v64,v67
v_mov_b32 v65,v64
v_mfma_f32_16x16x32_bf16 v[80:83],a[140:143],v[188:191],v[80:83]
v_mul_f32 v217,s71,v217
v_mul_f32 v218,s71,v218
v_mfma_f32_16x16x32_bf16 v[96:99],a[128:131],v[196:199],0
v_permlane32_swap_b32 v64,v65
v_max_f32 v64,v64,v65
v_mov_b32 v65,v64
v_mul_f32 v219,s71,v219
v_mul_f32 v220,s71,v220
v_mul_f32 v221,s71,v221
v_mul_f32 v222,s71,v222
v_mul_f32 v223,s71,v223
v_cvt_pk_bf16_f32 v216,v216,v217
v_cvt_pk_bf16_f32 v217,v218,v219
v_cvt_pk_bf16_f32 v218,v220,v221
v_cvt_pk_bf16_f32 v219,v222,v223
v_mul_f32 v104,s71,v104
v_mul_f32 v105,s71,v105
v_mul_f32 v106,s71,v106
v_mul_f32 v107,s71,v107
v_mul_f32 v108,s71,v108
v_mul_f32 v109,s71,v109
v_mul_f32 v110,s71,v110
v_mul_f32 v111,s71,v111
v_cvt_pk_bf16_f32 v220,v104,v105
v_cvt_pk_bf16_f32 v221,v106,v107
v_cvt_pk_bf16_f32 v222,v108,v109
v_cvt_pk_bf16_f32 v223,v110,v111
v_mfma_f32_16x16x32_bf16 v[96:99],a[132:135],v[204:207],v[96:99]
ds_read_b128 a[148:151],v1 offset:4352
v_max3_f32 v80,v80,v81,v82
v_mfma_f32_16x16x32_bf16 v[96:99],a[136:139],v[212:215],v[96:99]
ds_read_b128 a[144:147],v1 offset:4096
v_permlane16_swap_b32 v64,v65
v_max_f32 v64,v64,v65
v_sub_f32 v35,0,v64
v_sub_f32 v34,0,v64
v_sub_f32 v33,0,v64
L_pd4_%=:
s_cbranch_execz L_pd8_%=
v_sub_f32 v32,0,v64
v_mfma_f32_16x16x32_bf16 v[96:99],a[140:143],v[220:223],v[96:99]
ds_read_b128 a[160:163],v1 offset:8192
v_max_f32 v80,v80,v83
v_mfma_f32_16x16x32_bf16 v[64:67],a[128:131],v[132:135],v[32:35]
s_waitcnt lgkmcnt(3)
ds_read_b128 a[164:167],v1 offset:8448
v_mov_b32 v81,v80
v_mfma_f32_16x16x32_bf16 v[64:67],a[132:135],v[140:143],v[64:67]
s_add_u32 s10,s24,s74
s_addc_u32 s11,s25,0
v_mul_f32 v232,s71,v232
v_mfma_f32_16x16x32_bf16 v[64:67],a[136:139],v[148:151],v[64:67]
s_add_u32 s74,s74,16384
ds_read_b128 a[168:171],v1 offset:8704
v_max3_f32 v96,v96,v97,v98
v_mfma_f32_16x16x32_bf16 v[64:67],a[140:143],v[156:159],v[64:67]
s_add_u32 m0,s76,16384
ds_read_b128 a[172:175],v1 offset:8960
v_max_f32 v96,v96,v99
v_mfma_f32_16x16x32_bf16 v[68:71],a[156:159],v[156:159],v[32:35]
s_and_b32 s74,s74,0x3ffff
ds_read_b128 a[188:191],v1 offset:13056
v_mov_b32 v97,v96
v_mfma_f32_16x16x32_bf16 v[68:71],a[152:155],v[148:151],v[68:71]
global_load_lds_dwordx4 v4,s[10:11]
v_mul_f32 v233,s71,v233
s_waitcnt lgkmcnt(6)
v_mfma_f32_16x16x32_bf16 v[68:71],a[148:151],v[140:143],v[68:71]
s_or_b32 s74,s74,s69
ds_read_b128 a[184:187],v1 offset:12800
v_mul_f32 v234,s71,v234
s_waitcnt lgkmcnt(6)
v_mfma_f32_16x16x32_bf16 v[68:71],a[144:147],v[132:135],v[68:71]
s_sub_u32 s76,s76,32768
ds_read_b128 a[180:183],v1 offset:12544
v_mul_f32 v235,s71,v235
s_waitcnt lgkmcnt(6)
v_mfma_f32_16x16x32_bf16 v[72:75],a[160:163],v[132:135],v[32:35]
s_and_b32 s76,s76,0x1ffff
ds_read_b128 a[176:179],v1 offset:12288
v_mul_f32 v236,s71,v236
s_waitcnt lgkmcnt(6)
v_mfma_f32_16x16x32_bf16 v[72:75],a[164:167],v[140:143],v[72:75]
s_and_b32 s69,s75,0xfffc0000
v_permlane32_swap_b32 v80,v81
s_waitcnt lgkmcnt(5)
v_mfma_f32_16x16x32_bf16 v[72:75],a[168:171],v[148:151],v[72:75]
s_add_u32 s75,s75,16384
v_max_f32 v80,v80,v81
v_mov_b32 v81,v80
s_waitcnt lgkmcnt(4)
v_mfma_f32_16x16x32_bf16 v[72:75],a[172:175],v[156:159],v[72:75]
s_waitcnt lgkmcnt(3)
v_mul_f32 v237,s71,v237
v_mul_f32 v238,s71,v238
v_mfma_f32_16x16x32_bf16 v[76:79],a[188:191],v[156:159],v[32:35]
s_waitcnt lgkmcnt(2)
v_permlane16_swap_b32 v80,v81
v_mfma_f32_16x16x32_bf16 v[76:79],a[184:187],v[148:151],v[76:79]
s_waitcnt lgkmcnt(1)
v_max_f32 v80,v80,v81
v_sub_f32 v39,0,v80
v_sub_f32 v38,0,v80
v_sub_f32 v37,0,v80
v_sub_f32 v36,0,v80
v_mfma_f32_16x16x32_bf16 v[76:79],a[180:183],v[140:143],v[76:79]
s_waitcnt lgkmcnt(0)
v_mul_f32 v239,s71,v239
v_cvt_pk_bf16_f32 v232,v232,v233
v_mfma_f32_16x16x32_bf16 v[76:79],a[176:179],v[132:135],v[76:79]
v_cvt_pk_bf16_f32 v233,v234,v235
v_cvt_pk_bf16_f32 v234,v236,v237
v_mfma_f32_16x16x32_bf16 v[80:83],a[128:131],v[164:167],v[36:39]
v_cvt_pk_bf16_f32 v235,v238,v239
v_accvgpr_read_b32 v16,a56
v_mfma_f32_16x16x32_bf16 v[80:83],a[132:135],v[172:175],v[80:83]
v_accvgpr_read_b32 v17,a57
v_accvgpr_read_b32 v18,a58
v_mfma_f32_16x16x32_bf16 v[80:83],a[136:139],v[180:183],v[80:83]
v_accvgpr_read_b32 v19,a59
v_accvgpr_read_b32 v20,a60
v_mfma_f32_16x16x32_bf16 v[80:83],a[140:143],v[188:191],v[80:83]
global_load_lds_dwordx4 v5,s[10:11] offset:1024
v_accvgpr_read_b32 v21,a61
v_mfma_f32_16x16x32_bf16 v[84:87],a[156:159],v[188:191],v[36:39]
v_accvgpr_read_b32 v22,a62
v_accvgpr_read_b32 v23,a63
v_mfma_f32_16x16x32_bf16 v[84:87],a[152:155],v[180:183],v[84:87]
v_mul_f32 v16,s71,v16
v_mul_f32 v17,s71,v17
v_mfma_f32_16x16x32_bf16 v[84:87],a[148:151],v[172:175],v[84:87]
v_mul_f32 v18,s71,v18
v_mul_f32 v19,s71,v19
v_mfma_f32_16x16x32_bf16 v[84:87],a[144:147],v[164:167],v[84:87]
v_mul_f32 v20,s71,v20
v_mul_f32 v21,s71,v21
v_mfma_f32_16x16x32_bf16 v[88:91],a[160:163],v[164:167],v[36:39]
v_mul_f32 v22,s71,v22
v_mul_f32 v23,s71,v23
v_mfma_f32_16x16x32_bf16 v[88:91],a[164:167],v[172:175],v[88:91]
v_cvt_pk_bf16_f32 v236,v16,v17
v_cvt_pk_bf16_f32 v237,v18,v19
v_mfma_f32_16x16x32_bf16 v[88:91],a[168:171],v[180:183],v[88:91]
v_cvt_pk_bf16_f32 v238,v20,v21
v_cvt_pk_bf16_f32 v239,v22,v23
v_mfma_f32_16x16x32_bf16 v[88:91],a[172:175],v[188:191],v[88:91]
v_mul_f32 v240,s71,v240
v_mul_f32 v241,s71,v241
v_mfma_f32_16x16x32_bf16 v[92:95],a[188:191],v[188:191],v[36:39]
v_mul_f32 v242,s71,v242
v_mul_f32 v243,s71,v243
v_mfma_f32_16x16x32_bf16 v[92:95],a[184:187],v[180:183],v[92:95]
v_mul_f32 v244,s71,v244
v_mul_f32 v245,s71,v245
v_mfma_f32_16x16x32_bf16 v[92:95],a[180:183],v[172:175],v[92:95]
v_mul_f32 v246,s71,v246
v_mul_f32 v247,s71,v247
v_mfma_f32_16x16x32_bf16 v[92:95],a[176:179],v[164:167],v[92:95]
v_cvt_pk_bf16_f32 v240,v240,v241
v_cvt_pk_bf16_f32 v241,v242,v243
v_cvt_pk_bf16_f32 v242,v244,v245
v_cvt_pk_bf16_f32 v243,v246,v247
v_mul_f32 v112,s71,v112
v_mul_f32 v113,s71,v113
v_mul_f32 v114,s71,v114
v_mul_f32 v115,s71,v115
v_mul_f32 v116,s71,v116
v_mul_f32 v117,s71,v117
v_mul_f32 v118,s71,v118
v_mul_f32 v119,s71,v119
v_cvt_pk_bf16_f32 v244,v112,v113
v_cvt_pk_bf16_f32 v245,v114,v115
v_cvt_pk_bf16_f32 v246,v116,v117
v_cvt_pk_bf16_f32 v247,v118,v119
v_permlane32_swap_b32 v96,v97
v_max_f32 v96,v96,v97
v_mov_b32 v97,v96
v_mfma_f32_16x16x32_bf16 v[112:115],a[128:131],v[228:231],0
v_mul_f32 v248,s71,v248
v_mul_f32 v249,s71,v249
v_mul_f32 v250,s71,v250
v_mul_f32 v251,s71,v251
v_mul_f32 v252,s71,v252
v_mul_f32 v253,s71,v253
v_mul_f32 v254,s71,v254
v_mul_f32 v255,s71,v255
v_cvt_pk_bf16_f32 v248,v248,v249
v_cvt_pk_bf16_f32 v249,v250,v251
v_cvt_pk_bf16_f32 v250,v252,v253
v_cvt_pk_bf16_f32 v251,v254,v255
v_mul_f32 v120,s71,v120
v_mul_f32 v121,s71,v121
v_mul_f32 v122,s71,v122
v_mul_f32 v123,s71,v123
v_mul_f32 v124,s71,v124
v_mul_f32 v125,s71,v125
v_mul_f32 v126,s71,v126
v_mul_f32 v127,s71,v127
v_cvt_pk_bf16_f32 v252,v120,v121
v_cvt_pk_bf16_f32 v253,v122,v123
v_cvt_pk_bf16_f32 v254,v124,v125
v_cvt_pk_bf16_f32 v255,v126,v127
v_mfma_f32_16x16x32_bf16 v[112:115],a[132:135],v[236:239],v[112:115]
global_load_lds_dwordx4 v4,s[10:11] offset:2048
v_permlane16_swap_b32 v96,v97
v_max_f32 v96,v96,v97
v_sub_f32 v43,0,v96
v_sub_f32 v42,0,v96
v_sub_f32 v41,0,v96
v_sub_f32 v40,0,v96
v_mfma_f32_16x16x32_bf16 v[112:115],a[136:139],v[244:247],v[112:115]
ds_read_b64_tr_b16 a[192:193],v2
ds_read_b64_tr_b16 a[194:195],v2 offset:4096
v_mfma_f32_16x16x32_bf16 v[112:115],a[140:143],v[252:255],v[112:115]
ds_read_b64_tr_b16 a[196:197],v2 offset:8192
ds_read_b64_tr_b16 a[198:199],v2 offset:12288
v_mfma_f32_16x16x32_bf16 v[96:99],a[128:131],v[196:199],v[40:43]
ds_read_b64_tr_b16 a[200:201],v3
ds_read_b64_tr_b16 a[202:203],v3 offset:4096
v_mfma_f32_16x16x32_bf16 v[96:99],a[132:135],v[204:207],v[96:99]
ds_read_b64_tr_b16 a[204:205],v3 offset:8192
ds_read_b64_tr_b16 a[206:207],v3 offset:12288
v_mfma_f32_16x16x32_bf16 v[96:99],a[136:139],v[212:215],v[96:99]
ds_read_b64_tr_b16 a[208:209],v2 offset:256
v_max3_f32 v112,v112,v113,v114
v_mfma_f32_16x16x32_bf16 v[96:99],a[140:143],v[220:223],v[96:99]
ds_read_b64_tr_b16 a[210:211],v2 offset:4352
ds_read_b64_tr_b16 a[212:213],v2 offset:8448
v_mfma_f32_16x16x32_bf16 v[100:103],a[156:159],v[220:223],v[40:43]
ds_read_b64_tr_b16 a[214:215],v2 offset:12544
v_max_f32 v112,v112,v115
v_mfma_f32_16x16x32_bf16 v[100:103],a[152:155],v[212:215],v[100:103]
ds_read_b64_tr_b16 a[216:217],v3 offset:256
ds_read_b64_tr_b16 a[218:219],v3 offset:4352
v_mfma_f32_16x16x32_bf16 v[100:103],a[148:151],v[204:207],v[100:103]
v_exp_f32 v64,v64
v_mfma_f32_16x16x32_bf16 v[100:103],a[144:147],v[196:199],v[100:103]
v_exp_f32 v65,v65
v_mfma_f32_16x16x32_bf16 v[104:107],a[160:163],v[196:199],v[40:43]
v_exp_f32 v66,v66
v_mfma_f32_16x16x32_bf16 v[104:107],a[164:167],v[204:207],v[104:107]
v_exp_f32 v67,v67
v_mfma_f32_16x16x32_bf16 v[104:107],a[168:171],v[212:215],v[104:107]
v_exp_f32 v68,v68
v_mfma_f32_16x16x32_bf16 v[104:107],a[172:175],v[220:223],v[104:107]
global_load_lds_dwordx4 v5,s[10:11] offset:3072
ds_read_b64_tr_b16 a[220:221],v3 offset:8448
v_mfma_f32_16x16x32_bf16 v[108:111],a[188:191],v[220:223],v[40:43]
v_exp_f32 v69,v69
v_exp_f32 v70,v70
v_exp_f32 v71,v71
v_mfma_f32_16x16x32_bf16 v[108:111],a[184:187],v[212:215],v[108:111]
ds_read_b64_tr_b16 a[222:223],v3 offset:12544
v_cvt_pk_bf16_f32 v64,v64,v65
v_cvt_pk_bf16_f32 v65,v66,v67
)A"
R"A(v_cvt_pk_bf16_f32 v66,v68,v69
v_cvt_pk_bf16_f32 v67,v70,v71
v_exp_f32 v72,v72
v_exp_f32 v73,v73
v_exp_f32 v74,v74
v_exp_f32 v75,v75
v_exp_f32 v76,v76
v_exp_f32 v77,v77
v_exp_f32 v78,v78
v_exp_f32 v79,v79
v_mfma_f32_16x16x32_bf16 v[108:111],a[180:183],v[204:207],v[108:111]
ds_read_b64_tr_b16 a[224:225],v2 offset:512
ds_read_b64_tr_b16 a[226:227],v2 offset:4608
v_cvt_pk_bf16_f32 v72,v72,v73
v_cvt_pk_bf16_f32 v73,v74,v75
v_cvt_pk_bf16_f32 v74,v76,v77
v_cvt_pk_bf16_f32 v75,v78,v79
v_mfma_f32_16x16x32_bf16 v[108:111],a[176:179],v[196:199],v[108:111]
s_waitcnt lgkmcnt(4)
ds_read_b64_tr_b16 a[228:229],v2 offset:8704
ds_read_b64_tr_b16 a[230:231],v2 offset:12800
v_mfma_f32_16x16x32_bf16 a[0:3],a[192:195],v[64:67],0
ds_read_b64_tr_b16 a[232:233],v3 offset:512
ds_read_b64_tr_b16 a[234:235],v3 offset:4608
v_mfma_f32_16x16x32_bf16 a[0:3],a[196:199],v[72:75],a[0:3]
ds_read_b64_tr_b16 a[236:237],v3 offset:8704
ds_read_b64_tr_b16 a[238:239],v3 offset:12800
v_mfma_f32_16x16x32_bf16 a[4:7],a[200:203],v[64:67],0
ds_read_b64_tr_b16 a[240:241],v2 offset:768
v_mov_b32 v113,v112
v_mfma_f32_16x16x32_bf16 a[4:7],a[204:207],v[72:75],a[4:7]
ds_read_b64_tr_b16 a[242:243],v2 offset:4864
ds_read_b64_tr_b16 a[244:245],v2 offset:8960
v_mfma_f32_16x16x32_bf16 a[8:11],a[208:211],v[64:67],0
ds_read_b64_tr_b16 a[246:247],v2 offset:13056
v_add_u32 v1,s78,v1
v_mfma_f32_16x16x32_bf16 a[8:11],a[212:215],v[72:75],a[8:11]
ds_read_b64_tr_b16 a[248:249],v3 offset:768
ds_read_b64_tr_b16 a[250:251],v3 offset:4864
v_mfma_f32_16x16x32_bf16 a[12:15],a[216:219],v[64:67],0
s_waitcnt lgkmcnt(12)
ds_read_b64_tr_b16 a[252:253],v3 offset:8960
v_add_u32 v2,s78,v2
v_mfma_f32_16x16x32_bf16 a[12:15],a[220:223],v[72:75],a[12:15]
s_waitcnt lgkmcnt(6)
ds_read_b64_tr_b16 a[254:255],v3 offset:13056
v_add_u32 v3,s78,v3
v_mfma_f32_16x16x32_bf16 a[16:19],a[224:227],v[64:67],0
s_sub_u32 s78,s77,32768
v_permlane32_swap_b32 v112,v113
v_mfma_f32_16x16x32_bf16 a[16:19],a[228:231],v[72:75],a[16:19]
s_and_b32 s78,s78,0x1ffff
v_max_f32 v112,v112,v113
v_mov_b32 v113,v112
v_mfma_f32_16x16x32_bf16 a[20:23],a[232:235],v[64:67],0
s_sub_u32 s78,s78,s77
v_exp_f32 v80,v80
v_mfma_f32_16x16x32_bf16 a[20:23],a[236:239],v[72:75],a[20:23]
s_add_u32 s77,s77,s78
v_permlane16_swap_b32 v112,v113
s_waitcnt lgkmcnt(5)
v_mfma_f32_16x16x32_bf16 a[24:27],a[240:243],v[64:67],0
s_waitcnt lgkmcnt(4)
v_max_f32 v112,v112,v113
v_max_f32 v112,s72,v112
v_mfma_f32_16x16x32_bf16 a[24:27],a[244:247],v[72:75],a[24:27]
s_waitcnt lgkmcnt(2)
v_sub_f32 v47,0,v112
v_sub_f32 v46,0,v112
v_mfma_f32_16x16x32_bf16 a[28:31],a[248:251],v[64:67],0
s_waitcnt lgkmcnt(0)
v_sub_f32 v45,0,v112
v_sub_f32 v44,0,v112
v_mfma_f32_16x16x32_bf16 a[28:31],a[252:255],v[72:75],a[28:31]
v_exp_f32 v81,v81
v_mfma_f32_16x16x32_bf16 v[48:51],v[28:31],v[64:67],0
v_exp_f32 v82,v82
v_mfma_f32_16x16x32_bf16 v[48:51],v[28:31],v[72:75],v[48:51]
v_exp_f32 v83,v83
s_waitcnt vmcnt(8)
s_barrier
v_mfma_f32_16x16x32_bf16 v[112:115],a[128:131],v[228:231],v[44:47]
s_add_u32 s10,s20,s74
s_addc_u32 s11,s21,0
s_add_u32 m0,s76,0
v_mfma_f32_16x16x32_bf16 v[112:115],a[132:135],v[236:239],v[112:115]
v_exp_f32 v84,v84
v_mfma_f32_16x16x32_bf16 v[112:115],a[136:139],v[244:247],v[112:115]
global_load_lds_dwordx4 v4,s[10:11] nt
v_cvt_pk_bf16_f32 v80,v80,v81
v_mfma_f32_16x16x32_bf16 v[112:115],a[140:143],v[252:255],v[112:115]
v_exp_f32 v85,v85
v_mfma_f32_16x16x32_bf16 v[116:119],a[156:159],v[252:255],v[44:47]
v_exp_f32 v86,v86
v_mfma_f32_16x16x32_bf16 v[116:119],a[152:155],v[244:247],v[116:119]
v_exp_f32 v87,v87
v_mfma_f32_16x16x32_bf16 v[116:119],a[148:151],v[236:239],v[116:119]
v_cvt_pk_bf16_f32 v81,v82,v83
v_cvt_pk_bf16_f32 v82,v84,v85
v_mfma_f32_16x16x32_bf16 v[116:119],a[144:147],v[228:231],v[116:119]
ds_read_b128 a[128:131],v1
v_cvt_pk_bf16_f32 v83,v86,v87
v_mfma_f32_16x16x32_bf16 v[120:123],a[160:163],v[228:231],v[44:47]
v_exp_f32 v88,v88
v_mfma_f32_16x16x32_bf16 v[120:123],a[164:167],v[236:239],v[120:123]
v_exp_f32 v89,v89
v_mfma_f32_16x16x32_bf16 v[120:123],a[168:171],v[244:247],v[120:123]
v_exp_f32 v90,v90
v_mfma_f32_16x16x32_bf16 v[120:123],a[172:175],v[252:255],v[120:123]
v_exp_f32 v91,v91
v_mfma_f32_16x16x32_bf16 v[124:127],a[188:191],v[252:255],v[44:47]
v_exp_f32 v92,v92
v_mfma_f32_16x16x32_bf16 v[124:127],a[184:187],v[244:247],v[124:127]
v_exp_f32 v93,v93
v_exp_f32 v94,v94
v_exp_f32 v95,v95
v_mfma_f32_16x16x32_bf16 v[124:127],a[180:183],v[236:239],v[124:127]
v_cvt_pk_bf16_f32 v88,v88,v89
v_cvt_pk_bf16_f32 v89,v90,v91
v_cvt_pk_bf16_f32 v90,v92,v93
v_cvt_pk_bf16_f32 v91,v94,v95
v_mfma_f32_16x16x32_bf16 v[124:127],a[176:179],v[228:231],v[124:127]
v_exp_f32 v96,v96
v_mfma_f32_16x16x32_bf16 a[32:35],a[192:195],v[80:83],0
global_load_lds_dwordx4 v4,s[10:11] offset:1024 nt
ds_read_b128 a[132:135],v1 offset:256
v_mfma_f32_16x16x32_bf16 a[32:35],a[196:199],v[88:91],a[32:35]
v_exp_f32 v97,v97
v_mfma_f32_16x16x32_bf16 a[36:39],a[200:203],v[80:83],0
ds_read_b128 a[136:139],v1 offset:512
v_cvt_pk_bf16_f32 v96,v96,v97
v_mfma_f32_16x16x32_bf16 a[36:39],a[204:207],v[88:91],a[36:39]
v_exp_f32 v98,v98
v_mfma_f32_16x16x32_bf16 a[40:43],a[208:211],v[80:83],0
v_exp_f32 v99,v99
v_mfma_f32_16x16x32_bf16 a[40:43],a[212:215],v[88:91],a[40:43]
ds_read_b128 a[140:143],v1 offset:768
v_cvt_pk_bf16_f32 v97,v98,v99
v_mfma_f32_16x16x32_bf16 a[44:47],a[216:219],v[80:83],0
v_exp_f32 v100,v100
v_mfma_f32_16x16x32_bf16 a[44:47],a[220:223],v[88:91],a[44:47]
v_exp_f32 v101,v101
v_mfma_f32_16x16x32_bf16 a[48:51],a[224:227],v[80:83],0
ds_read_b128 a[156:159],v1 offset:4864
v_cvt_pk_bf16_f32 v98,v100,v101
v_mfma_f32_16x16x32_bf16 a[48:51],a[228:231],v[88:91],a[48:51]
v_exp_f32 v102,v102
v_mfma_f32_16x16x32_bf16 a[52:55],a[232:235],v[80:83],0
v_exp_f32 v103,v103
v_mfma_f32_16x16x32_bf16 a[52:55],a[236:239],v[88:91],a[52:55]
ds_read_b128 a[152:155],v1 offset:4608
v_cvt_pk_bf16_f32 v99,v102,v103
v_mfma_f32_16x16x32_bf16 a[56:59],a[240:243],v[80:83],0
ds_read_b128 a[148:151],v1 offset:4352
v_mfma_f32_16x16x32_bf16 a[56:59],a[244:247],v[88:91],a[56:59]
ds_read_b128 a[144:147],v1 offset:4096
v_mfma_f32_16x16x32_bf16 a[60:63],a[248:251],v[80:83],0
global_load_lds_dwordx4 v5,s[10:11] offset:2048 nt
ds_read_b128 a[160:163],v1 offset:8192
v_mfma_f32_16x16x32_bf16 a[60:63],a[252:255],v[88:91],a[60:63]
v_exp_f32 v104,v104
v_mfma_f32_16x16x32_bf16 v[52:55],v[28:31],v[80:83],0
ds_read_b128 a[164:167],v1 offset:8448
v_mfma_f32_16x16x32_bf16 v[52:55],v[28:31],v[88:91],v[52:55]
s_waitcnt lgkmcnt(5)
ds_read_b128 a[168:171],v1 offset:8704
v_mfma_f32_16x16x32_bf16 v[64:67],a[128:131],v[132:135],v[32:35]
ds_read_b128 a[172:175],v1 offset:8960
v_mfma_f32_16x16x32_bf16 v[64:67],a[132:135],v[140:143],v[64:67]
v_exp_f32 v105,v105
v_mfma_f32_16x16x32_bf16 v[64:67],a[136:139],v[148:151],v[64:67]
ds_read_b128 a[188:191],v1 offset:13056
v_cvt_pk_bf16_f32 v104,v104,v105
v_mfma_f32_16x16x32_bf16 v[64:67],a[140:143],v[156:159],v[64:67]
ds_read_b128 a[184:187],v1 offset:12800
v_mfma_f32_16x16x32_bf16 v[68:71],a[156:159],v[156:159],v[32:35]
s_waitcnt lgkmcnt(5)
ds_read_b128 a[180:183],v1 offset:12544
v_mfma_f32_16x16x32_bf16 v[68:71],a[152:155],v[148:151],v[68:71]
v_exp_f32 v106,v106
v_mfma_f32_16x16x32_bf16 v[68:71],a[148:151],v[140:143],v[68:71]
ds_read_b128 a[176:179],v1 offset:12288
v_add_u32 v1,s78,v1
v_mfma_f32_16x16x32_bf16 v[68:71],a[144:147],v[132:135],v[68:71]
v_exp_f32 v107,v107
v_mfma_f32_16x16x32_bf16 v[72:75],a[160:163],v[132:135],v[32:35]
s_waitcnt lgkmcnt(4)
v_exp_f32 v108,v108
v_mfma_f32_16x16x32_bf16 v[72:75],a[164:167],v[140:143],v[72:75]
v_exp_f32 v109,v109
v_mfma_f32_16x16x32_bf16 v[72:75],a[168:171],v[148:151],v[72:75]
global_load_lds_dwordx4 v5,s[10:11] offset:3072
v_cvt_pk_bf16_f32 v105,v106,v107
v_mfma_f32_16x16x32_bf16 v[72:75],a[172:175],v[156:159],v[72:75]
s_waitcnt lgkmcnt(2)
v_exp_f32 v110,v110
v_mfma_f32_16x16x32_bf16 v[76:79],a[188:191],v[156:159],v[32:35]
v_exp_f32 v111,v111
v_mfma_f32_16x16x32_bf16 v[76:79],a[184:187],v[148:151],v[76:79]
s_waitcnt lgkmcnt(1)
v_cvt_pk_bf16_f32 v106,v108,v109
v_cvt_pk_bf16_f32 v107,v110,v111
v_mfma_f32_16x16x32_bf16 v[76:79],a[180:183],v[140:143],v[76:79]
s_waitcnt lgkmcnt(0)
v_exp_f32 v112,v112
v_mfma_f32_16x16x32_bf16 v[76:79],a[176:179],v[132:135],v[76:79]
v_exp_f32 v113,v113
v_mfma_f32_16x16x32_bf16 a[64:67],a[192:195],v[96:99],0
v_exp_f32 v114,v114
v_mfma_f32_16x16x32_bf16 a[64:67],a[196:199],v[104:107],a[64:67]
v_exp_f32 v115,v115
v_mfma_f32_16x16x32_bf16 a[68:71],a[200:203],v[96:99],0
v_exp_f32 v116,v116
v_mfma_f32_16x16x32_bf16 a[68:71],a[204:207],v[104:107],a[68:71]
v_exp_f32 v117,v117
v_mfma_f32_16x16x32_bf16 a[72:75],a[208:211],v[96:99],0
s_add_u32 s10,s24,s74
s_addc_u32 s11,s25,0
s_add_u32 s74,s74,16384
s_and_b32 s74,s74,0x3ffff
s_or_b32 s74,s74,s69
s_lshl_b32 s69,s60,13
s_add_u32 s69,s69,131072
v_add_u32 v8,s69,v6
v_mfma_f32_16x16x32_bf16 a[72:75],a[212:215],v[104:107],a[72:75]
s_lshl_b32 s69,s60,13
ds_write_b128 v8,v[24:27] offset:4096
v_mfma_f32_16x16x32_bf16 a[76:79],a[216:219],v[96:99],0
s_add_u32 m0,s76,16384
ds_write_b32 v8,v7 offset:5120
v_cvt_pk_bf16_f32 v112,v112,v113
v_mfma_f32_16x16x32_bf16 a[76:79],a[220:223],v[104:107],a[76:79]
s_sub_u32 s76,s76,32768
v_bfe_u32 v24,v6,4,4
v_cvt_pk_bf16_f32 v113,v114,v115
v_mfma_f32_16x16x32_bf16 a[80:83],a[224:227],v[96:99],0
global_load_lds_dwordx4 v4,s[10:11]
v_lshrrev_b32 v7,8,v6
v_lshlrev_b32 v7,4,v7
v_lshl_add_u32 v7,v24,8,v7
v_mfma_f32_16x16x32_bf16 a[80:83],a[228:231],v[104:107],a[80:83]
s_and_b32 s76,s76,0x1ffff
v_exp_f32 v118,v118
v_mfma_f32_16x16x32_bf16 a[84:87],a[232:235],v[96:99],0
s_add_u32 s69,s69,131072
v_exp_f32 v119,v119
v_mfma_f32_16x16x32_bf16 a[84:87],a[236:239],v[104:107],a[84:87]
v_cvt_pk_bf16_f32 v114,v116,v117
v_cvt_pk_bf16_f32 v115,v118,v119
v_mfma_f32_16x16x32_bf16 a[88:91],a[240:243],v[96:99],0
v_exp_f32 v120,v120
v_mfma_f32_16x16x32_bf16 a[88:91],a[244:247],v[104:107],a[88:91]
buffer_load_dwordx4 v[8:11],v7,s[20:23],s65 offen nt
v_mfma_f32_16x16x32_bf16 a[92:95],a[248:251],v[96:99],0
v_exp_f32 v121,v121
v_mfma_f32_16x16x32_bf16 a[92:95],a[252:255],v[104:107],a[92:95]
v_exp_f32 v122,v122
v_mfma_f32_16x16x32_bf16 v[56:59],v[28:31],v[96:99],0
v_exp_f32 v123,v123
v_mfma_f32_16x16x32_bf16 v[56:59],v[28:31],v[104:107],v[56:59]
v_exp_f32 v124,v124
v_mfma_f32_16x16x32_bf16 v[80:83],a[128:131],v[164:167],v[36:39]
buffer_load_dwordx4 v[12:15],v7,s[20:23],s65 offen offset:64 nt
v_mfma_f32_16x16x32_bf16 v[80:83],a[132:135],v[172:175],v[80:83]
v_exp_f32 v125,v125
v_mfma_f32_16x16x32_bf16 v[80:83],a[136:139],v[180:183],v[80:83]
v_exp_f32 v126,v126
v_mfma_f32_16x16x32_bf16 v[80:83],a[140:143],v[188:191],v[80:83]
v_exp_f32 v127,v127
v_mfma_f32_16x16x32_bf16 v[84:87],a[156:159],v[188:191],v[36:39]
v_cvt_pk_bf16_f32 v120,v120,v121
v_cvt_pk_bf16_f32 v121,v122,v123
v_mfma_f32_16x16x32_bf16 v[84:87],a[152:155],v[180:183],v[84:87]
buffer_load_dwordx4 v[16:19],v7,s[20:23],s65 offen offset:128 nt
v_mfma_f32_16x16x32_bf16 v[84:87],a[148:151],v[172:175],v[84:87]
v_cvt_pk_bf16_f32 v122,v124,v125
v_cvt_pk_bf16_f32 v123,v126,v127
v_mfma_f32_16x16x32_bf16 v[84:87],a[144:147],v[164:167],v[84:87]
v_exp_f32 v64,v64
v_mfma_f32_16x16x32_bf16 v[88:91],a[160:163],v[164:167],v[36:39]
v_exp_f32 v65,v65
v_mfma_f32_16x16x32_bf16 v[88:91],a[164:167],v[172:175],v[88:91]
v_exp_f32 v66,v66
v_mfma_f32_16x16x32_bf16 v[88:91],a[168:171],v[180:183],v[88:91]
v_exp_f32 v67,v67
v_mfma_f32_16x16x32_bf16 v[88:91],a[172:175],v[188:191],v[88:91]
global_load_lds_dwordx4 v5,s[10:11] offset:1024
v_cvt_pk_bf16_f32 v64,v64,v65
v_mfma_f32_16x16x32_bf16 v[92:95],a[188:191],v[188:191],v[36:39]
v_exp_f32 v68,v68
v_mfma_f32_16x16x32_bf16 v[92:95],a[184:187],v[180:183],v[92:95]
v_exp_f32 v69,v69
v_mfma_f32_16x16x32_bf16 v[92:95],a[180:183],v[172:175],v[92:95]
v_exp_f32 v70,v70
v_mfma_f32_16x16x32_bf16 v[92:95],a[176:179],v[164:167],v[92:95]
v_exp_f32 v71,v71
v_mfma_f32_16x16x32_bf16 a[96:99],a[192:195],v[112:115],0
buffer_load_dwordx4 v[20:23],v7,s[20:23],s65 offen offset:192 nt
v_mfma_f32_16x16x32_bf16 a[96:99],a[196:199],v[120:123],a[96:99]
ds_read_b64_tr_b16 a[192:193],v2
ds_read_b64_tr_b16 a[194:195],v2 offset:4096
)A"
R"A(v_mfma_f32_16x16x32_bf16 a[100:103],a[200:203],v[112:115],0
ds_read_b64_tr_b16 a[196:197],v2 offset:8192
ds_read_b64_tr_b16 a[198:199],v2 offset:12288
v_mfma_f32_16x16x32_bf16 a[100:103],a[204:207],v[120:123],a[100:103]
ds_read_b64_tr_b16 a[200:201],v3
ds_read_b64_tr_b16 a[202:203],v3 offset:4096
v_mfma_f32_16x16x32_bf16 a[104:107],a[208:211],v[112:115],0
ds_read_b64_tr_b16 a[204:205],v3 offset:8192
ds_read_b64_tr_b16 a[206:207],v3 offset:12288
v_mfma_f32_16x16x32_bf16 a[104:107],a[212:215],v[120:123],a[104:107]
ds_read_b64_tr_b16 a[208:209],v2 offset:256
v_cvt_pk_bf16_f32 v65,v66,v67
v_mfma_f32_16x16x32_bf16 a[108:111],a[216:219],v[112:115],0
global_load_lds_dwordx4 v4,s[10:11] offset:2048
ds_read_b64_tr_b16 a[210:211],v2 offset:4352
v_mfma_f32_16x16x32_bf16 a[108:111],a[220:223],v[120:123],a[108:111]
ds_read_b64_tr_b16 a[212:213],v2 offset:8448
ds_read_b64_tr_b16 a[214:215],v2 offset:12544
v_mfma_f32_16x16x32_bf16 a[112:115],a[224:227],v[112:115],0
ds_read_b64_tr_b16 a[216:217],v3 offset:256
ds_read_b64_tr_b16 a[218:219],v3 offset:4352
v_mfma_f32_16x16x32_bf16 a[112:115],a[228:231],v[120:123],a[112:115]
v_cvt_pk_bf16_f32 v66,v68,v69
v_cvt_pk_bf16_f32 v67,v70,v71
v_mfma_f32_16x16x32_bf16 a[116:119],a[232:235],v[112:115],0
v_exp_f32 v72,v72
v_mfma_f32_16x16x32_bf16 a[116:119],a[236:239],v[120:123],a[116:119]
v_exp_f32 v73,v73
v_mfma_f32_16x16x32_bf16 a[120:123],a[240:243],v[112:115],0
v_exp_f32 v74,v74
v_mfma_f32_16x16x32_bf16 a[120:123],a[244:247],v[120:123],a[120:123]
v_exp_f32 v75,v75
v_mfma_f32_16x16x32_bf16 a[124:127],a[248:251],v[112:115],0
v_exp_f32 v76,v76
v_mfma_f32_16x16x32_bf16 a[124:127],a[252:255],v[120:123],a[124:127]
v_exp_f32 v77,v77
v_mfma_f32_16x16x32_bf16 v[60:63],v[28:31],v[112:115],0
v_exp_f32 v78,v78
v_mfma_f32_16x16x32_bf16 v[60:63],v[28:31],v[120:123],v[60:63]
global_load_lds_dwordx4 v5,s[10:11] offset:3072
ds_read_b64_tr_b16 a[220:221],v3 offset:8448
v_mfma_f32_16x16x32_bf16 v[96:99],a[128:131],v[196:199],v[40:43]
v_exp_f32 v79,v79
v_mfma_f32_16x16x32_bf16 v[96:99],a[132:135],v[204:207],v[96:99]
ds_read_b64_tr_b16 a[222:223],v3 offset:12544
v_cvt_pk_bf16_f32 v72,v72,v73
v_mfma_f32_16x16x32_bf16 v[96:99],a[136:139],v[212:215],v[96:99]
v_cvt_pk_bf16_f32 v73,v74,v75
v_cvt_pk_bf16_f32 v74,v76,v77
v_mfma_f32_16x16x32_bf16 v[96:99],a[140:143],v[220:223],v[96:99]
ds_read_b64_tr_b16 a[224:225],v2 offset:512
v_cvt_pk_bf16_f32 v75,v78,v79
v_mfma_f32_16x16x32_bf16 v[100:103],a[156:159],v[220:223],v[40:43]
ds_read_b64_tr_b16 a[226:227],v2 offset:4608
ds_read_b64_tr_b16 a[228:229],v2 offset:8704
v_mfma_f32_16x16x32_bf16 v[100:103],a[152:155],v[212:215],v[100:103]
ds_read_b64_tr_b16 a[230:231],v2 offset:12800
ds_read_b64_tr_b16 a[232:233],v3 offset:512
v_mfma_f32_16x16x32_bf16 v[100:103],a[148:151],v[204:207],v[100:103]
ds_read_b64_tr_b16 a[234:235],v3 offset:4608
ds_read_b64_tr_b16 a[236:237],v3 offset:8704
v_mfma_f32_16x16x32_bf16 v[100:103],a[144:147],v[196:199],v[100:103]
ds_read_b64_tr_b16 a[238:239],v3 offset:12800
ds_read_b64_tr_b16 a[240:241],v2 offset:768
v_mfma_f32_16x16x32_bf16 v[104:107],a[160:163],v[196:199],v[40:43]
ds_read_b64_tr_b16 a[242:243],v2 offset:4864
v_add_u32 v7,s69,v6
v_mfma_f32_16x16x32_bf16 v[104:107],a[164:167],v[204:207],v[104:107]
s_add_u32 s10,s20,s74
s_addc_u32 s11,s21,0
ds_read_b64_tr_b16 a[244:245],v2 offset:8960
v_mfma_f32_16x16x32_bf16 v[104:107],a[168:171],v[212:215],v[104:107]
s_and_b32 s69,s75,0xfffc0000
ds_read_b64_tr_b16 a[246:247],v2 offset:13056
ds_read_b64_tr_b16 a[248:249],v3 offset:768
v_mfma_f32_16x16x32_bf16 v[104:107],a[172:175],v[220:223],v[104:107]
s_add_u32 m0,s76,0
ds_read_b64_tr_b16 a[250:251],v3 offset:4864
ds_read_b64_tr_b16 a[252:253],v3 offset:8960
v_mfma_f32_16x16x32_bf16 v[108:111],a[188:191],v[220:223],v[40:43]
s_add_u32 s75,s75,16384
ds_read_b64_tr_b16 a[254:255],v3 offset:13056
v_add_u32 v2,s78,v2
v_mfma_f32_16x16x32_bf16 v[108:111],a[184:187],v[212:215],v[108:111]
global_load_lds_dwordx4 v4,s[10:11] nt
v_add_u32 v3,s78,v3
v_mfma_f32_16x16x32_bf16 v[108:111],a[180:183],v[204:207],v[108:111]
s_sub_u32 s78,s77,32768
v_exp_f32 v80,v80
v_mfma_f32_16x16x32_bf16 v[108:111],a[176:179],v[196:199],v[108:111]
s_and_b32 s78,s78,0x1ffff
v_exp_f32 v81,v81
s_waitcnt lgkmcnt(7)
v_mfma_f32_16x16x32_bf16 a[0:3],a[192:195],v[64:67],a[0:3]
s_sub_u32 s78,s78,s77
v_exp_f32 v82,v82
v_mfma_f32_16x16x32_bf16 a[0:3],a[196:199],v[72:75],a[0:3]
s_add_u32 s77,s77,s78
v_exp_f32 v83,v83
v_mfma_f32_16x16x32_bf16 a[4:7],a[200:203],v[64:67],a[4:7]
v_exp_f32 v84,v84
v_mfma_f32_16x16x32_bf16 a[4:7],a[204:207],v[72:75],a[4:7]
v_exp_f32 v85,v85
v_mfma_f32_16x16x32_bf16 a[8:11],a[208:211],v[64:67],a[8:11]
v_exp_f32 v86,v86
v_mfma_f32_16x16x32_bf16 a[8:11],a[212:215],v[72:75],a[8:11]
v_exp_f32 v87,v87
s_nop 0
L_pd5_%=:
s_cbranch_execz L_trt0_%=
v_mfma_f32_16x16x32_bf16 a[12:15],a[216:219],v[64:67],a[12:15]
v_cvt_pk_bf16_f32 v80,v80,v81
v_cvt_pk_bf16_f32 v81,v82,v83
v_mfma_f32_16x16x32_bf16 a[12:15],a[220:223],v[72:75],a[12:15]
v_cvt_pk_bf16_f32 v82,v84,v85
v_cvt_pk_bf16_f32 v83,v86,v87
v_mfma_f32_16x16x32_bf16 a[16:19],a[224:227],v[64:67],a[16:19]
v_exp_f32 v88,v88
v_mfma_f32_16x16x32_bf16 a[16:19],a[228:231],v[72:75],a[16:19]
v_exp_f32 v89,v89
v_mfma_f32_16x16x32_bf16 a[20:23],a[232:235],v[64:67],a[20:23]
v_exp_f32 v90,v90
v_mfma_f32_16x16x32_bf16 a[20:23],a[236:239],v[72:75],a[20:23]
s_waitcnt lgkmcnt(0)
v_exp_f32 v91,v91
v_mfma_f32_16x16x32_bf16 a[24:27],a[240:243],v[64:67],a[24:27]
v_exp_f32 v92,v92
v_mfma_f32_16x16x32_bf16 a[24:27],a[244:247],v[72:75],a[24:27]
v_exp_f32 v93,v93
v_mfma_f32_16x16x32_bf16 a[28:31],a[248:251],v[64:67],a[28:31]
v_exp_f32 v94,v94
v_mfma_f32_16x16x32_bf16 a[28:31],a[252:255],v[72:75],a[28:31]
s_getpc_b64 s[92:93]
s_cmp_lg_u32 s4,0
s_cbranch_scc1 L_pace_%=
v_mfma_f32_16x16x32_bf16 v[48:51],v[28:31],v[64:67],v[48:51]
v_exp_f32 v95,v95
v_mfma_f32_16x16x32_bf16 v[48:51],v[28:31],v[72:75],v[48:51]
s_add_u32 s10,s20,s74
s_addc_u32 s11,s21,0
v_cvt_pk_bf16_f32 v88,v88,v89
s_waitcnt vmcnt(13)
s_barrier
v_mfma_f32_16x16x32_bf16 v[112:115],a[128:131],v[228:231],v[44:47]
s_add_u32 m0,s76,0
v_cvt_pk_bf16_f32 v89,v90,v91
v_cvt_pk_bf16_f32 v90,v92,v93
v_mfma_f32_16x16x32_bf16 v[112:115],a[132:135],v[236:239],v[112:115]
ds_read_b128 a[128:131],v1
v_cvt_pk_bf16_f32 v91,v94,v95
v_mfma_f32_16x16x32_bf16 v[112:115],a[136:139],v[244:247],v[112:115]
global_load_lds_dwordx4 v4,s[10:11] offset:1024 nt
ds_read_b128 a[132:135],v1 offset:256
v_mfma_f32_16x16x32_bf16 v[112:115],a[140:143],v[252:255],v[112:115]
v_exp_f32 v96,v96
v_mfma_f32_16x16x32_bf16 v[116:119],a[156:159],v[252:255],v[44:47]
v_exp_f32 v97,v97
v_mfma_f32_16x16x32_bf16 v[116:119],a[152:155],v[244:247],v[116:119]
ds_read_b128 a[136:139],v1 offset:512
v_cvt_pk_bf16_f32 v96,v96,v97
v_mfma_f32_16x16x32_bf16 v[116:119],a[148:151],v[236:239],v[116:119]
v_exp_f32 v98,v98
v_mfma_f32_16x16x32_bf16 v[116:119],a[144:147],v[228:231],v[116:119]
v_exp_f32 v99,v99
v_mfma_f32_16x16x32_bf16 v[120:123],a[160:163],v[228:231],v[44:47]
ds_read_b128 a[140:143],v1 offset:768
v_cvt_pk_bf16_f32 v97,v98,v99
v_mfma_f32_16x16x32_bf16 v[120:123],a[164:167],v[236:239],v[120:123]
v_exp_f32 v100,v100
v_mfma_f32_16x16x32_bf16 v[120:123],a[168:171],v[244:247],v[120:123]
v_exp_f32 v101,v101
v_mfma_f32_16x16x32_bf16 v[120:123],a[172:175],v[252:255],v[120:123]
ds_read_b128 a[156:159],v1 offset:4864
v_cvt_pk_bf16_f32 v98,v100,v101
v_mfma_f32_16x16x32_bf16 v[124:127],a[188:191],v[252:255],v[44:47]
v_exp_f32 v102,v102
v_mfma_f32_16x16x32_bf16 v[124:127],a[184:187],v[244:247],v[124:127]
v_exp_f32 v103,v103
v_mfma_f32_16x16x32_bf16 v[124:127],a[180:183],v[236:239],v[124:127]
ds_read_b128 a[152:155],v1 offset:4608
v_cvt_pk_bf16_f32 v99,v102,v103
v_mfma_f32_16x16x32_bf16 v[124:127],a[176:179],v[228:231],v[124:127]
v_exp_f32 v104,v104
v_mfma_f32_16x16x32_bf16 a[32:35],a[192:195],v[80:83],a[32:35]
global_load_lds_dwordx4 v5,s[10:11] offset:2048 nt
ds_read_b128 a[148:151],v1 offset:4352
v_mfma_f32_16x16x32_bf16 a[32:35],a[196:199],v[88:91],a[32:35]
v_exp_f32 v105,v105
v_mfma_f32_16x16x32_bf16 a[36:39],a[200:203],v[80:83],a[36:39]
ds_read_b128 a[144:147],v1 offset:4096
v_cvt_pk_bf16_f32 v104,v104,v105
v_mfma_f32_16x16x32_bf16 a[36:39],a[204:207],v[88:91],a[36:39]
v_exp_f32 v106,v106
v_mfma_f32_16x16x32_bf16 a[40:43],a[208:211],v[80:83],a[40:43]
v_exp_f32 v107,v107
v_mfma_f32_16x16x32_bf16 a[40:43],a[212:215],v[88:91],a[40:43]
ds_read_b128 a[160:163],v1 offset:8192
v_cvt_pk_bf16_f32 v105,v106,v107
v_mfma_f32_16x16x32_bf16 a[44:47],a[216:219],v[80:83],a[44:47]
v_exp_f32 v108,v108
v_mfma_f32_16x16x32_bf16 a[44:47],a[220:223],v[88:91],a[44:47]
v_exp_f32 v109,v109
v_mfma_f32_16x16x32_bf16 a[48:51],a[224:227],v[80:83],a[48:51]
ds_read_b128 a[164:167],v1 offset:8448
v_cvt_pk_bf16_f32 v106,v108,v109
v_mfma_f32_16x16x32_bf16 a[48:51],a[228:231],v[88:91],a[48:51]
v_exp_f32 v110,v110
v_mfma_f32_16x16x32_bf16 a[52:55],a[232:235],v[80:83],a[52:55]
v_exp_f32 v111,v111
v_mfma_f32_16x16x32_bf16 a[52:55],a[236:239],v[88:91],a[52:55]
ds_read_b128 a[168:171],v1 offset:8704
v_cvt_pk_bf16_f32 v107,v110,v111
v_mfma_f32_16x16x32_bf16 a[56:59],a[240:243],v[80:83],a[56:59]
v_exp_f32 v112,v112
v_mfma_f32_16x16x32_bf16 a[56:59],a[244:247],v[88:91],a[56:59]
v_exp_f32 v113,v113
v_mfma_f32_16x16x32_bf16 a[60:63],a[248:251],v[80:83],a[60:63]
global_load_lds_dwordx4 v5,s[10:11] offset:3072
ds_read_b128 a[172:175],v1 offset:8960
v_mfma_f32_16x16x32_bf16 a[60:63],a[252:255],v[88:91],a[60:63]
ds_read_b128 a[188:191],v1 offset:13056
v_cvt_pk_bf16_f32 v112,v112,v113
v_mfma_f32_16x16x32_bf16 v[52:55],v[28:31],v[80:83],v[52:55]
v_exp_f32 v114,v114
v_mfma_f32_16x16x32_bf16 v[52:55],v[28:31],v[88:91],v[52:55]
s_waitcnt lgkmcnt(3)
v_exp_f32 v115,v115
v_mfma_f32_16x16x32_bf16 v[64:67],a[128:131],v[132:135],v[32:35]
ds_read_b128 a[184:187],v1 offset:12800
v_cvt_pk_bf16_f32 v113,v114,v115
v_mfma_f32_16x16x32_bf16 v[64:67],a[132:135],v[140:143],v[64:67]
v_exp_f32 v116,v116
v_mfma_f32_16x16x32_bf16 v[64:67],a[136:139],v[148:151],v[64:67]
ds_read_b128 a[180:183],v1 offset:12544
v_mfma_f32_16x16x32_bf16 v[64:67],a[140:143],v[156:159],v[64:67]
ds_read_b128 a[176:179],v1 offset:12288
v_add_u32 v1,s78,v1
v_mfma_f32_16x16x32_bf16 v[68:71],a[156:159],v[156:159],v[32:35]
v_exp_f32 v117,v117
v_mfma_f32_16x16x32_bf16 v[68:71],a[152:155],v[148:151],v[68:71]
v_exp_f32 v118,v118
v_mfma_f32_16x16x32_bf16 v[68:71],a[148:151],v[140:143],v[68:71]
s_add_u32 s10,s24,s74
s_addc_u32 s11,s25,0
v_cvt_pk_bf16_f32 v114,v116,v117
v_mfma_f32_16x16x32_bf16 v[68:71],a[144:147],v[132:135],v[68:71]
s_add_u32 s74,s74,16384
v_exp_f32 v119,v119
v_mfma_f32_16x16x32_bf16 v[72:75],a[160:163],v[132:135],v[32:35]
s_add_u32 m0,s76,16384
v_exp_f32 v120,v120
v_mfma_f32_16x16x32_bf16 v[72:75],a[164:167],v[140:143],v[72:75]
s_and_b32 s74,s74,0x3ffff
v_exp_f32 v121,v121
s_waitcnt lgkmcnt(2)
v_mfma_f32_16x16x32_bf16 v[72:75],a[168:171],v[148:151],v[72:75]
global_load_lds_dwordx4 v4,s[10:11]
v_cvt_pk_bf16_f32 v115,v118,v119
v_mfma_f32_16x16x32_bf16 v[72:75],a[172:175],v[156:159],v[72:75]
s_or_b32 s74,s74,s69
v_exp_f32 v122,v122
v_mfma_f32_16x16x32_bf16 v[76:79],a[188:191],v[156:159],v[32:35]
s_sub_u32 s76,s76,32768
v_exp_f32 v123,v123
v_mfma_f32_16x16x32_bf16 v[76:79],a[184:187],v[148:151],v[76:79]
s_and_b32 s76,s76,0x1ffff
v_exp_f32 v124,v124
s_waitcnt lgkmcnt(0)
v_mfma_f32_16x16x32_bf16 v[76:79],a[180:183],v[140:143],v[76:79]
s_and_b32 s69,s75,0xfffc0000
v_exp_f32 v125,v125
v_mfma_f32_16x16x32_bf16 v[76:79],a[176:179],v[132:135],v[76:79]
v_exp_f32 v126,v126
v_mfma_f32_16x16x32_bf16 a[64:67],a[192:195],v[96:99],a[64:67]
v_exp_f32 v127,v127
v_mfma_f32_16x16x32_bf16 a[64:67],a[196:199],v[104:107],a[64:67]
v_cvt_pk_bf16_f32 v120,v120,v121
v_cvt_pk_bf16_f32 v121,v122,v123
v_mfma_f32_16x16x32_bf16 a[68:71],a[200:203],v[96:99],a[68:71]
v_cvt_pk_bf16_f32 v122,v124,v125
v_cvt_pk_bf16_f32 v123,v126,v127
v_mfma_f32_16x16x32_bf16 a[68:71],a[204:207],v[104:107],a[68:71]
v_exp_f32 v64,v64
v_mfma_f32_16x16x32_bf16 a[72:75],a[208:211],v[96:99],a[72:75]
v_exp_f32 v65,v65
v_mfma_f32_16x16x32_bf16 a[72:75],a[212:215],v[104:107],a[72:75]
)A"
R"A(v_exp_f32 v66,v66
v_mfma_f32_16x16x32_bf16 a[76:79],a[216:219],v[96:99],a[76:79]
v_exp_f32 v67,v67
v_mfma_f32_16x16x32_bf16 a[76:79],a[220:223],v[104:107],a[76:79]
v_exp_f32 v68,v68
v_mfma_f32_16x16x32_bf16 a[80:83],a[224:227],v[96:99],a[80:83]
global_load_lds_dwordx4 v5,s[10:11] offset:1024
v_cvt_pk_bf16_f32 v64,v64,v65
v_mfma_f32_16x16x32_bf16 a[80:83],a[228:231],v[104:107],a[80:83]
v_exp_f32 v69,v69
v_mfma_f32_16x16x32_bf16 a[84:87],a[232:235],v[96:99],a[84:87]
v_exp_f32 v70,v70
v_mfma_f32_16x16x32_bf16 a[84:87],a[236:239],v[104:107],a[84:87]
v_exp_f32 v71,v71
v_mfma_f32_16x16x32_bf16 a[88:91],a[240:243],v[96:99],a[88:91]
v_cvt_pk_bf16_f32 v65,v66,v67
v_cvt_pk_bf16_f32 v66,v68,v69
v_mfma_f32_16x16x32_bf16 a[88:91],a[244:247],v[104:107],a[88:91]
v_exp_f32 v72,v72
v_mfma_f32_16x16x32_bf16 a[92:95],a[248:251],v[96:99],a[92:95]
v_exp_f32 v73,v73
v_mfma_f32_16x16x32_bf16 a[92:95],a[252:255],v[104:107],a[92:95]
v_cvt_pk_bf16_f32 v67,v70,v71
v_cvt_pk_bf16_f32 v72,v72,v73
v_mfma_f32_16x16x32_bf16 v[56:59],v[28:31],v[96:99],v[56:59]
v_exp_f32 v74,v74
v_mfma_f32_16x16x32_bf16 v[56:59],v[28:31],v[104:107],v[56:59]
v_exp_f32 v75,v75
v_mfma_f32_16x16x32_bf16 v[80:83],a[128:131],v[164:167],v[36:39]
v_exp_f32 v76,v76
v_mfma_f32_16x16x32_bf16 v[80:83],a[132:135],v[172:175],v[80:83]
v_exp_f32 v77,v77
v_mfma_f32_16x16x32_bf16 v[80:83],a[136:139],v[180:183],v[80:83]
v_exp_f32 v78,v78
v_mfma_f32_16x16x32_bf16 v[80:83],a[140:143],v[188:191],v[80:83]
v_exp_f32 v79,v79
v_mfma_f32_16x16x32_bf16 v[84:87],a[156:159],v[188:191],v[36:39]
v_cvt_pk_bf16_f32 v73,v74,v75
v_cvt_pk_bf16_f32 v74,v76,v77
v_mfma_f32_16x16x32_bf16 v[84:87],a[152:155],v[180:183],v[84:87]
global_load_lds_dwordx4 v4,s[10:11] offset:2048
v_cvt_pk_bf16_f32 v75,v78,v79
v_mfma_f32_16x16x32_bf16 v[84:87],a[148:151],v[172:175],v[84:87]
v_exp_f32 v80,v80
v_mfma_f32_16x16x32_bf16 v[84:87],a[144:147],v[164:167],v[84:87]
v_exp_f32 v81,v81
v_mfma_f32_16x16x32_bf16 v[88:91],a[160:163],v[164:167],v[36:39]
v_exp_f32 v82,v82
v_mfma_f32_16x16x32_bf16 v[88:91],a[164:167],v[172:175],v[88:91]
v_exp_f32 v83,v83
v_mfma_f32_16x16x32_bf16 v[88:91],a[168:171],v[180:183],v[88:91]
v_cvt_pk_bf16_f32 v80,v80,v81
v_cvt_pk_bf16_f32 v81,v82,v83
v_mfma_f32_16x16x32_bf16 v[88:91],a[172:175],v[188:191],v[88:91]
v_exp_f32 v84,v84
v_mfma_f32_16x16x32_bf16 v[92:95],a[188:191],v[188:191],v[36:39]
v_exp_f32 v85,v85
v_mfma_f32_16x16x32_bf16 v[92:95],a[184:187],v[180:183],v[92:95]
v_exp_f32 v86,v86
v_mfma_f32_16x16x32_bf16 v[92:95],a[180:183],v[172:175],v[92:95]
v_exp_f32 v87,v87
v_mfma_f32_16x16x32_bf16 v[92:95],a[176:179],v[164:167],v[92:95]
v_cvt_pk_bf16_f32 v82,v84,v85
v_cvt_pk_bf16_f32 v83,v86,v87
v_mfma_f32_16x16x32_bf16 a[96:99],a[192:195],v[112:115],a[96:99]
ds_read_b64_tr_b16 a[192:193],v2
ds_read_b64_tr_b16 a[194:195],v2 offset:4096
v_mfma_f32_16x16x32_bf16 a[96:99],a[196:199],v[120:123],a[96:99]
ds_read_b64_tr_b16 a[196:197],v2 offset:8192
ds_read_b64_tr_b16 a[198:199],v2 offset:12288
v_mfma_f32_16x16x32_bf16 a[100:103],a[200:203],v[112:115],a[100:103]
ds_read_b64_tr_b16 a[200:201],v3
ds_read_b64_tr_b16 a[202:203],v3 offset:4096
v_mfma_f32_16x16x32_bf16 a[100:103],a[204:207],v[120:123],a[100:103]
global_load_lds_dwordx4 v5,s[10:11] offset:3072
ds_read_b64_tr_b16 a[204:205],v3 offset:8192
v_mfma_f32_16x16x32_bf16 a[104:107],a[208:211],v[112:115],a[104:107]
ds_read_b64_tr_b16 a[206:207],v3 offset:12288
ds_read_b64_tr_b16 a[208:209],v2 offset:256
v_mfma_f32_16x16x32_bf16 a[104:107],a[212:215],v[120:123],a[104:107]
ds_read_b64_tr_b16 a[210:211],v2 offset:4352
ds_read_b64_tr_b16 a[212:213],v2 offset:8448
v_mfma_f32_16x16x32_bf16 a[108:111],a[216:219],v[112:115],a[108:111]
v_exp_f32 v88,v88
v_mfma_f32_16x16x32_bf16 a[108:111],a[220:223],v[120:123],a[108:111]
ds_read_b64_tr_b16 a[214:215],v2 offset:12544
ds_read_b64_tr_b16 a[216:217],v3 offset:256
v_mfma_f32_16x16x32_bf16 a[112:115],a[224:227],v[112:115],a[112:115]
ds_read_b64_tr_b16 a[218:219],v3 offset:4352
ds_read_b64_tr_b16 a[220:221],v3 offset:8448
v_mfma_f32_16x16x32_bf16 a[112:115],a[228:231],v[120:123],a[112:115]
ds_read_b64_tr_b16 a[222:223],v3 offset:12544
ds_read_b64_tr_b16 a[224:225],v2 offset:512
v_mfma_f32_16x16x32_bf16 a[116:119],a[232:235],v[112:115],a[116:119]
v_exp_f32 v89,v89
v_mfma_f32_16x16x32_bf16 a[116:119],a[236:239],v[120:123],a[116:119]
ds_read_b64_tr_b16 a[226:227],v2 offset:4608
ds_read_b64_tr_b16 a[228:229],v2 offset:8704
v_mfma_f32_16x16x32_bf16 a[120:123],a[240:243],v[112:115],a[120:123]
ds_read_b64_tr_b16 a[230:231],v2 offset:12800
ds_read_b64_tr_b16 a[232:233],v3 offset:512
v_mfma_f32_16x16x32_bf16 a[120:123],a[244:247],v[120:123],a[120:123]
s_add_u32 s10,s20,s74
s_addc_u32 s11,s21,0
ds_read_b64_tr_b16 a[234:235],v3 offset:4608
v_mfma_f32_16x16x32_bf16 a[124:127],a[248:251],v[112:115],a[124:127]
ds_read_b64_tr_b16 a[236:237],v3 offset:8704
ds_read_b64_tr_b16 a[238:239],v3 offset:12800
v_mfma_f32_16x16x32_bf16 a[124:127],a[252:255],v[120:123],a[124:127]
s_add_u32 m0,s76,0
ds_read_b64_tr_b16 a[240:241],v2 offset:768
ds_read_b64_tr_b16 a[242:243],v2 offset:4864
v_mfma_f32_16x16x32_bf16 v[60:63],v[28:31],v[112:115],v[60:63]
ds_read_b64_tr_b16 a[244:245],v2 offset:8960
v_cvt_pk_bf16_f32 v88,v88,v89
v_mfma_f32_16x16x32_bf16 v[60:63],v[28:31],v[120:123],v[60:63]
global_load_lds_dwordx4 v4,s[10:11] nt
ds_read_b64_tr_b16 a[246:247],v2 offset:13056
v_mfma_f32_16x16x32_bf16 v[96:99],a[128:131],v[196:199],v[40:43]
ds_read_b64_tr_b16 a[248:249],v3 offset:768
ds_read_b64_tr_b16 a[250:251],v3 offset:4864
v_mfma_f32_16x16x32_bf16 v[96:99],a[132:135],v[204:207],v[96:99]
ds_read_b64_tr_b16 a[252:253],v3 offset:8960
ds_read_b64_tr_b16 a[254:255],v3 offset:13056
v_mfma_f32_16x16x32_bf16 v[96:99],a[136:139],v[212:215],v[96:99]
v_exp_f32 v90,v90
v_mfma_f32_16x16x32_bf16 v[96:99],a[140:143],v[220:223],v[96:99]
v_exp_f32 v91,v91
v_mfma_f32_16x16x32_bf16 v[100:103],a[156:159],v[220:223],v[40:43]
v_exp_f32 v92,v92
v_mfma_f32_16x16x32_bf16 v[100:103],a[152:155],v[212:215],v[100:103]
v_exp_f32 v93,v93
v_mfma_f32_16x16x32_bf16 v[100:103],a[148:151],v[204:207],v[100:103]
v_exp_f32 v94,v94
v_mfma_f32_16x16x32_bf16 v[100:103],a[144:147],v[196:199],v[100:103]
v_exp_f32 v95,v95
v_mfma_f32_16x16x32_bf16 v[104:107],a[160:163],v[196:199],v[40:43]
v_cvt_pk_bf16_f32 v89,v90,v91
v_cvt_pk_bf16_f32 v90,v92,v93
v_mfma_f32_16x16x32_bf16 v[104:107],a[164:167],v[204:207],v[104:107]
v_cvt_pk_bf16_f32 v91,v94,v95
v_add_u32 v2,s78,v2
v_mfma_f32_16x16x32_bf16 v[104:107],a[168:171],v[212:215],v[104:107]
v_exp_f32 v96,v96
v_mfma_f32_16x16x32_bf16 v[104:107],a[172:175],v[220:223],v[104:107]
v_exp_f32 v97,v97
v_mfma_f32_16x16x32_bf16 v[108:111],a[188:191],v[220:223],v[40:43]
v_exp_f32 v98,v98
v_mfma_f32_16x16x32_bf16 v[108:111],a[184:187],v[212:215],v[108:111]
global_load_lds_dwordx4 v4,s[10:11] offset:1024 nt
v_cvt_pk_bf16_f32 v96,v96,v97
v_mfma_f32_16x16x32_bf16 v[108:111],a[180:183],v[204:207],v[108:111]
v_exp_f32 v99,v99
v_mfma_f32_16x16x32_bf16 v[108:111],a[176:179],v[196:199],v[108:111]
s_waitcnt lgkmcnt(0)
v_exp_f32 v100,v100
v_mfma_f32_16x16x32_bf16 a[0:3],a[192:195],v[64:67],a[0:3]
v_exp_f32 v101,v101
v_mfma_f32_16x16x32_bf16 a[0:3],a[196:199],v[72:75],a[0:3]
v_exp_f32 v102,v102
v_mfma_f32_16x16x32_bf16 a[4:7],a[200:203],v[64:67],a[4:7]
v_exp_f32 v103,v103
v_mfma_f32_16x16x32_bf16 a[4:7],a[204:207],v[72:75],a[4:7]
v_cvt_pk_bf16_f32 v97,v98,v99
v_cvt_pk_bf16_f32 v98,v100,v101
v_mfma_f32_16x16x32_bf16 a[8:11],a[208:211],v[64:67],a[8:11]
v_cvt_pk_bf16_f32 v99,v102,v103
v_add_u32 v3,s78,v3
v_mfma_f32_16x16x32_bf16 a[8:11],a[212:215],v[72:75],a[8:11]
s_sub_u32 s78,s77,32768
v_exp_f32 v104,v104
v_mfma_f32_16x16x32_bf16 a[12:15],a[216:219],v[64:67],a[12:15]
s_and_b32 s78,s78,0x1ffff
v_exp_f32 v105,v105
v_mfma_f32_16x16x32_bf16 a[12:15],a[220:223],v[72:75],a[12:15]
s_sub_u32 s78,s78,s77
v_exp_f32 v106,v106
v_mfma_f32_16x16x32_bf16 a[16:19],a[224:227],v[64:67],a[16:19]
s_add_u32 s77,s77,s78
v_exp_f32 v107,v107
v_mfma_f32_16x16x32_bf16 a[16:19],a[228:231],v[72:75],a[16:19]
v_exp_f32 v108,v108
v_mfma_f32_16x16x32_bf16 a[20:23],a[232:235],v[64:67],a[20:23]
s_getreg_b32 s92,hwreg(HW_REG_IB_STS,0,4)
s_min_u32 s4,s92,s6
s_mov_b32 s6,s92
v_mfma_f32_16x16x32_bf16 a[20:23],a[236:239],v[72:75],a[20:23]
s_cmp_gt_u32 s4,s9
s_cselect_b32 s4,15,s5
v_cvt_pk_bf16_f32 v104,v104,v105
v_mfma_f32_16x16x32_bf16 a[24:27],a[240:243],v[64:67],a[24:27]
v_exp_f32 v109,v109
v_mfma_f32_16x16x32_bf16 a[24:27],a[244:247],v[72:75],a[24:27]
v_exp_f32 v110,v110
v_mfma_f32_16x16x32_bf16 a[28:31],a[248:251],v[64:67],a[28:31]
v_exp_f32 v111,v111
v_mfma_f32_16x16x32_bf16 a[28:31],a[252:255],v[72:75],a[28:31]
s_getpc_b64 s[92:93]
s_cmp_lg_u32 s4,0
s_cbranch_scc1 L_pace_%=
v_mfma_f32_16x16x32_bf16 v[48:51],v[28:31],v[64:67],v[48:51]
v_cvt_pk_bf16_f32 v105,v106,v107
v_cvt_pk_bf16_f32 v106,v108,v109
v_mfma_f32_16x16x32_bf16 v[48:51],v[28:31],v[72:75],v[48:51]
s_add_u32 s10,s20,s74
s_addc_u32 s11,s21,0
v_cvt_pk_bf16_f32 v107,v110,v111
s_waitcnt vmcnt(10)
s_barrier
v_mfma_f32_16x16x32_bf16 v[24:27],v[8:11],v[128:131],0
s_add_u32 m0,s76,0
v_mfma_f32_16x16x32_bf16 v[24:27],v[12:15],v[136:139],v[24:27]
v_mfma_f32_16x16x32_bf16 v[24:27],v[16:19],v[144:147],v[24:27]
global_load_lds_dwordx4 v5,s[10:11] offset:2048 nt
v_mfma_f32_16x16x32_bf16 v[24:27],v[20:23],v[152:155],v[24:27]
v_mfma_f32_16x16x32_bf16 v[112:115],a[128:131],v[228:231],v[44:47]
ds_read_b128 a[128:131],v1
v_mfma_f32_16x16x32_bf16 v[112:115],a[132:135],v[236:239],v[112:115]
ds_read_b128 a[132:135],v1 offset:256
v_mfma_f32_16x16x32_bf16 v[112:115],a[136:139],v[244:247],v[112:115]
ds_read_b128 a[136:139],v1 offset:512
v_mfma_f32_16x16x32_bf16 v[112:115],a[140:143],v[252:255],v[112:115]
ds_read_b128 a[140:143],v1 offset:768
v_mfma_f32_16x16x32_bf16 v[116:119],a[156:159],v[252:255],v[44:47]
v_max3_f32 v24,v24,v25,v26
v_max_f32 v24,v24,v27
v_mfma_f32_16x16x32_bf16 v[116:119],a[152:155],v[244:247],v[116:119]
ds_read_b128 a[156:159],v1 offset:4864
v_mov_b32 v25,v24
v_mfma_f32_16x16x32_bf16 v[116:119],a[148:151],v[236:239],v[116:119]
v_exp_f32 v112,v112
v_mfma_f32_16x16x32_bf16 v[116:119],a[144:147],v[228:231],v[116:119]
v_permlane32_swap_b32 v24,v25
v_mfma_f32_16x16x32_bf16 v[120:123],a[160:163],v[228:231],v[44:47]
v_max_f32 v24,v24,v25
v_mov_b32 v25,v24
v_mfma_f32_16x16x32_bf16 v[120:123],a[164:167],v[236:239],v[120:123]
v_exp_f32 v113,v113
v_mfma_f32_16x16x32_bf16 v[120:123],a[168:171],v[244:247],v[120:123]
v_permlane16_swap_b32 v24,v25
v_mfma_f32_16x16x32_bf16 v[120:123],a[172:175],v[252:255],v[120:123]
v_max_f32 v24,v24,v25
v_sub_f32 v27,0,v24
v_mfma_f32_16x16x32_bf16 v[124:127],a[188:191],v[252:255],v[44:47]
global_load_lds_dwordx4 v5,s[10:11] offset:3072
v_sub_f32 v26,0,v24
v_sub_f32 v25,0,v24
v_sub_f32 v24,0,v24
v_mfma_f32_16x16x32_bf16 v[124:127],a[184:187],v[244:247],v[124:127]
ds_write_b128 v7,v[24:27]
v_mfma_f32_16x16x32_bf16 v[124:127],a[180:183],v[236:239],v[124:127]
ds_read_b128 a[152:155],v1 offset:4608
v_cvt_pk_bf16_f32 v112,v112,v113
v_mfma_f32_16x16x32_bf16 v[124:127],a[176:179],v[228:231],v[124:127]
v_exp_f32 v114,v114
v_mfma_f32_16x16x32_bf16 v[24:27],v[8:11],v[160:163],0
v_exp_f32 v115,v115
v_mfma_f32_16x16x32_bf16 v[24:27],v[12:15],v[168:171],v[24:27]
ds_read_b128 a[148:151],v1 offset:4352
v_cvt_pk_bf16_f32 v113,v114,v115
v_mfma_f32_16x16x32_bf16 v[24:27],v[16:19],v[176:179],v[24:27]
v_exp_f32 v116,v116
v_mfma_f32_16x16x32_bf16 v[24:27],v[20:23],v[184:187],v[24:27]
v_exp_f32 v117,v117
v_mfma_f32_16x16x32_bf16 a[32:35],a[192:195],v[80:83],a[32:35]
ds_read_b128 a[144:147],v1 offset:4096
v_cvt_pk_bf16_f32 v114,v116,v117
v_mfma_f32_16x16x32_bf16 a[32:35],a[196:199],v[88:91],a[32:35]
v_exp_f32 v118,v118
v_mfma_f32_16x16x32_bf16 a[36:39],a[200:203],v[80:83],a[36:39]
s_add_u32 s10,s24,s74
s_addc_u32 s11,s25,0
ds_read_b128 a[160:163],v1 offset:8192
v_mfma_f32_16x16x32_bf16 a[36:39],a[204:207],v[88:91],a[36:39]
s_add_u32 s74,s74,16384
v_max3_f32 v24,v24,v25,v26
v_max_f32 v24,v24,v27
v_mfma_f32_16x16x32_bf16 a[40:43],a[208:211],v[80:83],a[40:43]
s_add_u32 m0,s76,16384
ds_read_b128 a[164:167],v1 offset:8448
v_mov_b32 v25,v24
)A"
R"A(v_mfma_f32_16x16x32_bf16 a[40:43],a[212:215],v[88:91],a[40:43]
s_and_b32 s74,s74,0x3ffff
v_exp_f32 v119,v119
v_mfma_f32_16x16x32_bf16 a[44:47],a[216:219],v[80:83],a[44:47]
global_load_lds_dwordx4 v4,s[10:11]
ds_read_b128 a[168:171],v1 offset:8704
v_mfma_f32_16x16x32_bf16 a[44:47],a[220:223],v[88:91],a[44:47]
s_or_b32 s74,s74,s69
v_permlane32_swap_b32 v24,v25
v_mfma_f32_16x16x32_bf16 a[48:51],a[224:227],v[80:83],a[48:51]
s_sub_u32 s76,s76,32768
v_max_f32 v24,v24,v25
v_mov_b32 v25,v24
v_mfma_f32_16x16x32_bf16 a[48:51],a[228:231],v[88:91],a[48:51]
s_and_b32 s76,s76,0x1ffff
ds_read_b128 a[172:175],v1 offset:8960
v_cvt_pk_bf16_f32 v115,v118,v119
v_mfma_f32_16x16x32_bf16 a[52:55],a[232:235],v[80:83],a[52:55]
v_permlane16_swap_b32 v24,v25
v_mfma_f32_16x16x32_bf16 a[52:55],a[236:239],v[88:91],a[52:55]
v_max_f32 v24,v24,v25
v_sub_f32 v27,0,v24
v_mfma_f32_16x16x32_bf16 a[56:59],a[240:243],v[80:83],a[56:59]
v_sub_f32 v26,0,v24
v_sub_f32 v25,0,v24
v_mfma_f32_16x16x32_bf16 a[56:59],a[244:247],v[88:91],a[56:59]
ds_read_b128 a[188:191],v1 offset:13056
v_sub_f32 v24,0,v24
v_mfma_f32_16x16x32_bf16 a[60:63],a[248:251],v[80:83],a[60:63]
ds_write_b128 v7,v[24:27] offset:1024
v_mfma_f32_16x16x32_bf16 a[60:63],a[252:255],v[88:91],a[60:63]
v_exp_f32 v120,v120
v_mfma_f32_16x16x32_bf16 v[52:55],v[28:31],v[80:83],v[52:55]
v_exp_f32 v121,v121
v_mfma_f32_16x16x32_bf16 v[52:55],v[28:31],v[88:91],v[52:55]
ds_read_b128 a[184:187],v1 offset:12800
v_cvt_pk_bf16_f32 v120,v120,v121
v_mfma_f32_16x16x32_bf16 v[24:27],v[8:11],v[192:195],0
v_exp_f32 v122,v122
v_mfma_f32_16x16x32_bf16 v[24:27],v[12:15],v[200:203],v[24:27]
v_exp_f32 v123,v123
v_mfma_f32_16x16x32_bf16 v[24:27],v[16:19],v[208:211],v[24:27]
s_waitcnt lgkmcnt(2)
ds_read_b128 a[180:183],v1 offset:12544
v_cvt_pk_bf16_f32 v121,v122,v123
v_mfma_f32_16x16x32_bf16 v[24:27],v[20:23],v[216:219],v[24:27]
global_load_lds_dwordx4 v5,s[10:11] offset:1024
ds_read_b128 a[176:179],v1 offset:12288
v_mfma_f32_16x16x32_bf16 v[64:67],a[128:131],v[132:135],v[32:35]
v_exp_f32 v124,v124
v_mfma_f32_16x16x32_bf16 v[64:67],a[132:135],v[140:143],v[64:67]
v_exp_f32 v125,v125
v_mfma_f32_16x16x32_bf16 v[64:67],a[136:139],v[148:151],v[64:67]
v_exp_f32 v126,v126
v_mfma_f32_16x16x32_bf16 v[64:67],a[140:143],v[156:159],v[64:67]
v_max3_f32 v24,v24,v25,v26
v_max_f32 v24,v24,v27
v_mfma_f32_16x16x32_bf16 v[68:71],a[156:159],v[156:159],v[32:35]
v_mov_b32 v25,v24
v_cvt_pk_bf16_f32 v122,v124,v125
v_mfma_f32_16x16x32_bf16 v[68:71],a[152:155],v[148:151],v[68:71]
v_exp_f32 v127,v127
v_mfma_f32_16x16x32_bf16 v[68:71],a[148:151],v[140:143],v[68:71]
v_permlane32_swap_b32 v24,v25
v_mfma_f32_16x16x32_bf16 v[68:71],a[144:147],v[132:135],v[68:71]
v_max_f32 v24,v24,v25
v_mov_b32 v25,v24
v_mfma_f32_16x16x32_bf16 v[72:75],a[160:163],v[132:135],v[32:35]
v_cvt_pk_bf16_f32 v123,v126,v127
v_add_u32 v1,s78,v1
v_mfma_f32_16x16x32_bf16 v[72:75],a[164:167],v[140:143],v[72:75]
v_permlane16_swap_b32 v24,v25
v_mfma_f32_16x16x32_bf16 v[72:75],a[168:171],v[148:151],v[72:75]
v_max_f32 v24,v24,v25
v_sub_f32 v27,0,v24
v_mfma_f32_16x16x32_bf16 v[72:75],a[172:175],v[156:159],v[72:75]
v_sub_f32 v26,0,v24
v_sub_f32 v25,0,v24
v_mfma_f32_16x16x32_bf16 v[76:79],a[188:191],v[156:159],v[32:35]
s_waitcnt lgkmcnt(0)
v_exp_f32 v64,v64
v_mfma_f32_16x16x32_bf16 v[76:79],a[184:187],v[148:151],v[76:79]
global_load_lds_dwordx4 v4,s[10:11] offset:2048
v_sub_f32 v24,0,v24
v_mfma_f32_16x16x32_bf16 v[76:79],a[180:183],v[140:143],v[76:79]
ds_write_b128 v7,v[24:27] offset:2048
v_mfma_f32_16x16x32_bf16 v[76:79],a[176:179],v[132:135],v[76:79]
v_exp_f32 v65,v65
v_mfma_f32_16x16x32_bf16 a[64:67],a[192:195],v[96:99],a[64:67]
v_exp_f32 v66,v66
v_mfma_f32_16x16x32_bf16 a[64:67],a[196:199],v[104:107],a[64:67]
v_exp_f32 v67,v67
v_mfma_f32_16x16x32_bf16 a[68:71],a[200:203],v[96:99],a[68:71]
v_exp_f32 v68,v68
v_mfma_f32_16x16x32_bf16 a[68:71],a[204:207],v[104:107],a[68:71]
v_exp_f32 v69,v69
v_mfma_f32_16x16x32_bf16 a[72:75],a[208:211],v[96:99],a[72:75]
v_exp_f32 v70,v70
v_mfma_f32_16x16x32_bf16 a[72:75],a[212:215],v[104:107],a[72:75]
v_exp_f32 v71,v71
v_mfma_f32_16x16x32_bf16 a[76:79],a[216:219],v[96:99],a[76:79]
v_cvt_pk_bf16_f32 v64,v64,v65
v_cvt_pk_bf16_f32 v65,v66,v67
v_mfma_f32_16x16x32_bf16 a[76:79],a[220:223],v[104:107],a[76:79]
v_cvt_pk_bf16_f32 v66,v68,v69
v_cvt_pk_bf16_f32 v67,v70,v71
v_mfma_f32_16x16x32_bf16 a[80:83],a[224:227],v[96:99],a[80:83]
v_exp_f32 v72,v72
v_mfma_f32_16x16x32_bf16 a[80:83],a[228:231],v[104:107],a[80:83]
v_exp_f32 v73,v73
v_mfma_f32_16x16x32_bf16 a[84:87],a[232:235],v[96:99],a[84:87]
v_exp_f32 v74,v74
v_mfma_f32_16x16x32_bf16 a[84:87],a[236:239],v[104:107],a[84:87]
v_exp_f32 v75,v75
v_mfma_f32_16x16x32_bf16 a[88:91],a[240:243],v[96:99],a[88:91]
global_load_lds_dwordx4 v5,s[10:11] offset:3072
v_cvt_pk_bf16_f32 v72,v72,v73
v_mfma_f32_16x16x32_bf16 a[88:91],a[244:247],v[104:107],a[88:91]
v_exp_f32 v76,v76
v_mfma_f32_16x16x32_bf16 a[92:95],a[248:251],v[96:99],a[92:95]
v_exp_f32 v77,v77
v_mfma_f32_16x16x32_bf16 a[92:95],a[252:255],v[104:107],a[92:95]
v_exp_f32 v78,v78
v_mfma_f32_16x16x32_bf16 v[56:59],v[28:31],v[96:99],v[56:59]
v_exp_f32 v79,v79
v_mfma_f32_16x16x32_bf16 v[56:59],v[28:31],v[104:107],v[56:59]
v_cvt_pk_bf16_f32 v73,v74,v75
v_cvt_pk_bf16_f32 v74,v76,v77
v_mfma_f32_16x16x32_bf16 v[24:27],v[8:11],v[224:227],0
v_cvt_pk_bf16_f32 v75,v78,v79
v_mfma_f32_16x16x32_bf16 v[24:27],v[12:15],v[232:235],v[24:27]
v_mfma_f32_16x16x32_bf16 v[24:27],v[16:19],v[240:243],v[24:27]
v_mfma_f32_16x16x32_bf16 v[24:27],v[20:23],v[248:251],v[24:27]
v_mfma_f32_16x16x32_bf16 v[80:83],a[128:131],v[164:167],v[36:39]
s_add_u32 s10,s20,s74
s_addc_u32 s11,s21,0
v_mfma_f32_16x16x32_bf16 v[80:83],a[132:135],v[172:175],v[80:83]
v_mfma_f32_16x16x32_bf16 v[80:83],a[136:139],v[180:183],v[80:83]
s_add_u32 m0,s76,0
v_mfma_f32_16x16x32_bf16 v[80:83],a[140:143],v[188:191],v[80:83]
v_mfma_f32_16x16x32_bf16 v[84:87],a[156:159],v[188:191],v[36:39]
global_load_lds_dwordx4 v4,s[10:11] nt
v_max3_f32 v24,v24,v25,v26
v_mfma_f32_16x16x32_bf16 v[84:87],a[152:155],v[180:183],v[84:87]
v_max_f32 v24,v24,v27
v_mov_b32 v25,v24
v_mfma_f32_16x16x32_bf16 v[84:87],a[148:151],v[172:175],v[84:87]
v_mfma_f32_16x16x32_bf16 v[84:87],a[144:147],v[164:167],v[84:87]
v_permlane32_swap_b32 v24,v25
v_mfma_f32_16x16x32_bf16 v[88:91],a[160:163],v[164:167],v[36:39]
v_max_f32 v24,v24,v25
v_mov_b32 v25,v24
v_mfma_f32_16x16x32_bf16 v[88:91],a[164:167],v[172:175],v[88:91]
v_exp_f32 v80,v80
v_mfma_f32_16x16x32_bf16 v[88:91],a[168:171],v[180:183],v[88:91]
v_permlane16_swap_b32 v24,v25
v_mfma_f32_16x16x32_bf16 v[88:91],a[172:175],v[188:191],v[88:91]
v_max_f32 v24,v24,v25
L_pd6_%=:
s_cbranch_execz L_trt0_%=
v_max_f32 v24,s72,v24
v_mfma_f32_16x16x32_bf16 v[92:95],a[188:191],v[188:191],v[36:39]
v_sub_f32 v27,0,v24
v_sub_f32 v26,0,v24
v_mfma_f32_16x16x32_bf16 v[92:95],a[184:187],v[180:183],v[92:95]
v_sub_f32 v25,0,v24
v_sub_f32 v24,0,v24
v_mfma_f32_16x16x32_bf16 v[92:95],a[180:183],v[172:175],v[92:95]
v_exp_f32 v81,v81
v_mfma_f32_16x16x32_bf16 v[92:95],a[176:179],v[164:167],v[92:95]
v_exp_f32 v82,v82
v_mfma_f32_16x16x32_bf16 a[96:99],a[192:195],v[112:115],a[96:99]
ds_read_b64_tr_b16 a[192:193],v2
ds_read_b64_tr_b16 a[194:195],v2 offset:4096
v_mfma_f32_16x16x32_bf16 a[96:99],a[196:199],v[120:123],a[96:99]
ds_read_b64_tr_b16 a[196:197],v2 offset:8192
ds_read_b64_tr_b16 a[198:199],v2 offset:12288
v_mfma_f32_16x16x32_bf16 a[100:103],a[200:203],v[112:115],a[100:103]
ds_read_b64_tr_b16 a[200:201],v3
ds_read_b64_tr_b16 a[202:203],v3 offset:4096
v_mfma_f32_16x16x32_bf16 a[100:103],a[204:207],v[120:123],a[100:103]
global_load_lds_dwordx4 v4,s[10:11] offset:1024 nt
ds_read_b64_tr_b16 a[204:205],v3 offset:8192
v_mfma_f32_16x16x32_bf16 a[104:107],a[208:211],v[112:115],a[104:107]
ds_read_b64_tr_b16 a[206:207],v3 offset:12288
ds_read_b64_tr_b16 a[208:209],v2 offset:256
v_mfma_f32_16x16x32_bf16 a[104:107],a[212:215],v[120:123],a[104:107]
ds_read_b64_tr_b16 a[210:211],v2 offset:4352
ds_read_b64_tr_b16 a[212:213],v2 offset:8448
v_mfma_f32_16x16x32_bf16 a[108:111],a[216:219],v[112:115],a[108:111]
ds_read_b64_tr_b16 a[214:215],v2 offset:12544
v_cvt_pk_bf16_f32 v80,v80,v81
v_mfma_f32_16x16x32_bf16 a[108:111],a[220:223],v[120:123],a[108:111]
ds_read_b64_tr_b16 a[216:217],v3 offset:256
ds_read_b64_tr_b16 a[218:219],v3 offset:4352
v_mfma_f32_16x16x32_bf16 a[112:115],a[224:227],v[112:115],a[112:115]
v_exp_f32 v83,v83
v_mfma_f32_16x16x32_bf16 a[112:115],a[228:231],v[120:123],a[112:115]
ds_read_b64_tr_b16 a[220:221],v3 offset:8448
ds_read_b64_tr_b16 a[222:223],v3 offset:12544
v_mfma_f32_16x16x32_bf16 a[116:119],a[232:235],v[112:115],a[116:119]
ds_read_b64_tr_b16 a[224:225],v2 offset:512
ds_read_b64_tr_b16 a[226:227],v2 offset:4608
v_mfma_f32_16x16x32_bf16 a[116:119],a[236:239],v[120:123],a[116:119]
ds_read_b64_tr_b16 a[228:229],v2 offset:8704
ds_read_b64_tr_b16 a[230:231],v2 offset:12800
v_mfma_f32_16x16x32_bf16 a[120:123],a[240:243],v[112:115],a[120:123]
ds_read_b64_tr_b16 a[232:233],v3 offset:512
v_cvt_pk_bf16_f32 v81,v82,v83
v_mfma_f32_16x16x32_bf16 a[120:123],a[244:247],v[120:123],a[120:123]
ds_read_b64_tr_b16 a[234:235],v3 offset:4608
ds_read_b64_tr_b16 a[236:237],v3 offset:8704
v_mfma_f32_16x16x32_bf16 a[124:127],a[248:251],v[112:115],a[124:127]
v_exp_f32 v84,v84
v_mfma_f32_16x16x32_bf16 a[124:127],a[252:255],v[120:123],a[124:127]
ds_read_b64_tr_b16 a[238:239],v3 offset:12800
ds_read_b64_tr_b16 a[240:241],v2 offset:768
v_mfma_f32_16x16x32_bf16 v[60:63],v[28:31],v[112:115],v[60:63]
ds_read_b64_tr_b16 a[242:243],v2 offset:4864
ds_read_b64_tr_b16 a[244:245],v2 offset:8960
v_mfma_f32_16x16x32_bf16 v[60:63],v[28:31],v[120:123],v[60:63]
global_load_lds_dwordx4 v5,s[10:11] offset:2048 nt
ds_read_b64_tr_b16 a[246:247],v2 offset:13056
v_mfma_f32_16x16x32_bf16 v[96:99],a[128:131],v[196:199],v[40:43]
ds_read_b64_tr_b16 a[248:249],v3 offset:768
ds_read_b64_tr_b16 a[250:251],v3 offset:4864
v_mfma_f32_16x16x32_bf16 v[96:99],a[132:135],v[204:207],v[96:99]
ds_read_b64_tr_b16 a[252:253],v3 offset:8960
ds_read_b64_tr_b16 a[254:255],v3 offset:13056
v_mfma_f32_16x16x32_bf16 v[96:99],a[136:139],v[212:215],v[96:99]
v_exp_f32 v85,v85
v_mfma_f32_16x16x32_bf16 v[96:99],a[140:143],v[220:223],v[96:99]
v_exp_f32 v86,v86
v_mfma_f32_16x16x32_bf16 v[100:103],a[156:159],v[220:223],v[40:43]
v_exp_f32 v87,v87
v_mfma_f32_16x16x32_bf16 v[100:103],a[152:155],v[212:215],v[100:103]
v_exp_f32 v88,v88
v_mfma_f32_16x16x32_bf16 v[100:103],a[148:151],v[204:207],v[100:103]
v_exp_f32 v89,v89
v_mfma_f32_16x16x32_bf16 v[100:103],a[144:147],v[196:199],v[100:103]
v_exp_f32 v90,v90
v_mfma_f32_16x16x32_bf16 v[104:107],a[160:163],v[196:199],v[40:43]
v_exp_f32 v91,v91
v_mfma_f32_16x16x32_bf16 v[104:107],a[164:167],v[204:207],v[104:107]
v_exp_f32 v92,v92
v_mfma_f32_16x16x32_bf16 v[104:107],a[168:171],v[212:215],v[104:107]
v_exp_f32 v93,v93
v_mfma_f32_16x16x32_bf16 v[104:107],a[172:175],v[220:223],v[104:107]
v_exp_f32 v94,v94
v_mfma_f32_16x16x32_bf16 v[108:111],a[188:191],v[220:223],v[40:43]
v_exp_f32 v95,v95
v_mfma_f32_16x16x32_bf16 v[108:111],a[184:187],v[212:215],v[108:111]
v_exp_f32 v96,v96
v_mfma_f32_16x16x32_bf16 v[108:111],a[180:183],v[204:207],v[108:111]
global_load_lds_dwordx4 v5,s[10:11] offset:3072
v_cvt_pk_bf16_f32 v82,v84,v85
v_mfma_f32_16x16x32_bf16 v[108:111],a[176:179],v[196:199],v[108:111]
s_waitcnt lgkmcnt(0)
v_exp_f32 v97,v97
v_mfma_f32_16x16x32_bf16 a[0:3],a[192:195],v[64:67],a[0:3]
v_exp_f32 v98,v98
v_mfma_f32_16x16x32_bf16 a[0:3],a[196:199],v[72:75],a[0:3]
v_exp_f32 v99,v99
v_mfma_f32_16x16x32_bf16 a[4:7],a[200:203],v[64:67],a[4:7]
v_exp_f32 v100,v100
v_mfma_f32_16x16x32_bf16 a[4:7],a[204:207],v[72:75],a[4:7]
v_exp_f32 v101,v101
v_mfma_f32_16x16x32_bf16 a[8:11],a[208:211],v[64:67],a[8:11]
v_exp_f32 v102,v102
v_mfma_f32_16x16x32_bf16 a[8:11],a[212:215],v[72:75],a[8:11]
v_exp_f32 v103,v103
v_mfma_f32_16x16x32_bf16 a[12:15],a[216:219],v[64:67],a[12:15]
ds_write_b128 v7,v[24:27] offset:3072
v_mfma_f32_16x16x32_bf16 a[12:15],a[220:223],v[72:75],a[12:15]
v_cvt_pk_bf16_f32 v83,v86,v87
v_cvt_pk_bf16_f32 v88,v88,v89
v_mfma_f32_16x16x32_bf16 a[16:19],a[224:227],v[64:67],a[16:19]
)A"
R"A(v_cvt_pk_bf16_f32 v89,v90,v91
v_cvt_pk_bf16_f32 v90,v92,v93
v_mfma_f32_16x16x32_bf16 a[16:19],a[228:231],v[72:75],a[16:19]
v_cvt_pk_bf16_f32 v91,v94,v95
v_cvt_pk_bf16_f32 v96,v96,v97
v_mfma_f32_16x16x32_bf16 a[20:23],a[232:235],v[64:67],a[20:23]
s_getreg_b32 s92,hwreg(HW_REG_IB_STS,0,4)
s_min_u32 s4,s92,s6
s_mov_b32 s6,s92
v_mfma_f32_16x16x32_bf16 a[20:23],a[236:239],v[72:75],a[20:23]
s_cmp_gt_u32 s4,s9
s_cselect_b32 s4,15,s5
v_cvt_pk_bf16_f32 v97,v98,v99
v_mfma_f32_16x16x32_bf16 a[24:27],a[240:243],v[64:67],a[24:27]
v_cvt_pk_bf16_f32 v98,v100,v101
v_cvt_pk_bf16_f32 v99,v102,v103
v_mfma_f32_16x16x32_bf16 a[24:27],a[244:247],v[72:75],a[24:27]
v_mfma_f32_16x16x32_bf16 a[28:31],a[248:251],v[64:67],a[28:31]
v_mfma_f32_16x16x32_bf16 a[28:31],a[252:255],v[72:75],a[28:31]
s_getpc_b64 s[92:93]
s_cmp_lg_u32 s4,0
s_cbranch_scc1 L_pace_%=
v_mfma_f32_16x16x32_bf16 v[48:51],v[28:31],v[64:67],v[48:51]
v_mfma_f32_16x16x32_bf16 v[48:51],v[28:31],v[72:75],v[48:51]
s_mov_b32 s70,11
s_nop 0
s_nop 0
s_nop 0
L_a0_%=:
s_waitcnt vmcnt(12)
s_barrier
v_mfma_f32_16x16x32_bf16 v[112:115],a[128:131],v[228:231],v[44:47]
s_add_u32 s10,s24,s74
s_addc_u32 s11,s25,0
s_add_u32 m0,s76,16384
v_mfma_f32_16x16x32_bf16 v[112:115],a[132:135],v[236:239],v[112:115]
s_add_u32 s75,s75,16384
s_add_u32 s74,s74,16384
ds_read_b128 a[128:131],v1
v_mfma_f32_16x16x32_bf16 v[112:115],a[136:139],v[244:247],v[112:115]
s_and_b32 s74,s74,0x3ffff
global_load_lds_dwordx4 v4,s[10:11]
v_mfma_f32_16x16x32_bf16 v[112:115],a[140:143],v[252:255],v[112:115]
s_and_b32 s69,s75,0xfffc0000
s_or_b32 s74,s74,s69
ds_read_b128 a[132:135],v1 offset:256
v_mfma_f32_16x16x32_bf16 v[116:119],a[156:159],v[252:255],v[44:47]
s_sub_u32 s76,s76,32768
s_and_b32 s76,s76,0x1ffff
v_mfma_f32_16x16x32_bf16 v[116:119],a[152:155],v[244:247],v[116:119]
ds_read_b128 a[136:139],v1 offset:512
v_mfma_f32_16x16x32_bf16 v[116:119],a[148:151],v[236:239],v[116:119]
v_mfma_f32_16x16x32_bf16 v[116:119],a[144:147],v[228:231],v[116:119]
ds_read_b128 a[140:143],v1 offset:768
v_mfma_f32_16x16x32_bf16 v[120:123],a[160:163],v[228:231],v[44:47]
v_mfma_f32_16x16x32_bf16 v[120:123],a[164:167],v[236:239],v[120:123]
ds_read_b128 a[156:159],v1 offset:4864
v_mfma_f32_16x16x32_bf16 v[120:123],a[168:171],v[244:247],v[120:123]
v_mfma_f32_16x16x32_bf16 v[120:123],a[172:175],v[252:255],v[120:123]
ds_read_b128 a[152:155],v1 offset:4608
v_mfma_f32_16x16x32_bf16 v[124:127],a[188:191],v[252:255],v[44:47]
v_exp_f32 v104,v104
v_mfma_f32_16x16x32_bf16 v[124:127],a[184:187],v[244:247],v[124:127]
ds_read_b128 a[148:151],v1 offset:4352
v_add_u32 v2,s78,v2
v_mfma_f32_16x16x32_bf16 v[124:127],a[180:183],v[236:239],v[124:127]
v_exp_f32 v105,v105
v_mfma_f32_16x16x32_bf16 v[124:127],a[176:179],v[228:231],v[124:127]
ds_read_b128 a[144:147],v1 offset:4096
v_cvt_pk_bf16_f32 v104,v104,v105
v_mfma_f32_16x16x32_bf16 a[32:35],a[192:195],v[80:83],a[32:35]
v_exp_f32 v106,v106
v_mfma_f32_16x16x32_bf16 a[32:35],a[196:199],v[88:91],a[32:35]
ds_read_b128 a[160:163],v1 offset:8192
v_add_u32 v3,s78,v3
v_mfma_f32_16x16x32_bf16 a[36:39],a[200:203],v[80:83],a[36:39]
s_sub_u32 s78,s77,32768
global_load_lds_dwordx4 v5,s[10:11] offset:1024
v_mfma_f32_16x16x32_bf16 a[36:39],a[204:207],v[88:91],a[36:39]
s_and_b32 s78,s78,0x1ffff
v_exp_f32 v107,v107
v_mfma_f32_16x16x32_bf16 a[40:43],a[208:211],v[80:83],a[40:43]
s_sub_u32 s78,s78,s77
ds_read_b128 a[164:167],v1 offset:8448
v_cvt_pk_bf16_f32 v105,v106,v107
v_mfma_f32_16x16x32_bf16 a[40:43],a[212:215],v[88:91],a[40:43]
s_add_u32 s77,s77,s78
v_exp_f32 v108,v108
v_mfma_f32_16x16x32_bf16 a[44:47],a[216:219],v[80:83],a[44:47]
v_exp_f32 v109,v109
v_mfma_f32_16x16x32_bf16 a[44:47],a[220:223],v[88:91],a[44:47]
ds_read_b128 a[168:171],v1 offset:8704
v_cvt_pk_bf16_f32 v106,v108,v109
v_mfma_f32_16x16x32_bf16 a[48:51],a[224:227],v[80:83],a[48:51]
v_exp_f32 v110,v110
v_mfma_f32_16x16x32_bf16 a[48:51],a[228:231],v[88:91],a[48:51]
v_exp_f32 v111,v111
v_mfma_f32_16x16x32_bf16 a[52:55],a[232:235],v[80:83],a[52:55]
ds_read_b128 a[172:175],v1 offset:8960
v_cvt_pk_bf16_f32 v107,v110,v111
v_mfma_f32_16x16x32_bf16 a[52:55],a[236:239],v[88:91],a[52:55]
v_exp_f32 v112,v112
v_mfma_f32_16x16x32_bf16 a[56:59],a[240:243],v[80:83],a[56:59]
v_exp_f32 v113,v113
v_mfma_f32_16x16x32_bf16 a[56:59],a[244:247],v[88:91],a[56:59]
ds_read_b128 a[188:191],v1 offset:13056
v_cvt_pk_bf16_f32 v112,v112,v113
v_mfma_f32_16x16x32_bf16 a[60:63],a[248:251],v[80:83],a[60:63]
v_exp_f32 v114,v114
v_mfma_f32_16x16x32_bf16 a[60:63],a[252:255],v[88:91],a[60:63]
v_exp_f32 v115,v115
v_mfma_f32_16x16x32_bf16 v[52:55],v[28:31],v[80:83],v[52:55]
ds_read_b128 a[184:187],v1 offset:12800
v_cvt_pk_bf16_f32 v113,v114,v115
v_mfma_f32_16x16x32_bf16 v[52:55],v[28:31],v[88:91],v[52:55]
s_waitcnt lgkmcnt(3)
v_exp_f32 v116,v116
v_mfma_f32_16x16x32_bf16 v[64:67],a[128:131],v[132:135],v[32:35]
global_load_lds_dwordx4 v4,s[10:11] offset:2048
ds_read_b128 a[180:183],v1 offset:12544
v_mfma_f32_16x16x32_bf16 v[64:67],a[132:135],v[140:143],v[64:67]
v_exp_f32 v117,v117
v_mfma_f32_16x16x32_bf16 v[64:67],a[136:139],v[148:151],v[64:67]
ds_read_b128 a[176:179],v1 offset:12288
v_cvt_pk_bf16_f32 v114,v116,v117
v_mfma_f32_16x16x32_bf16 v[64:67],a[140:143],v[156:159],v[64:67]
v_exp_f32 v118,v118
v_mfma_f32_16x16x32_bf16 v[68:71],a[156:159],v[156:159],v[32:35]
v_exp_f32 v119,v119
v_mfma_f32_16x16x32_bf16 v[68:71],a[152:155],v[148:151],v[68:71]
v_cvt_pk_bf16_f32 v115,v118,v119
v_add_u32 v1,s78,v1
v_mfma_f32_16x16x32_bf16 v[68:71],a[148:151],v[140:143],v[68:71]
v_exp_f32 v120,v120
v_mfma_f32_16x16x32_bf16 v[68:71],a[144:147],v[132:135],v[68:71]
v_exp_f32 v121,v121
v_mfma_f32_16x16x32_bf16 v[72:75],a[160:163],v[132:135],v[32:35]
v_exp_f32 v122,v122
v_mfma_f32_16x16x32_bf16 v[72:75],a[164:167],v[140:143],v[72:75]
v_exp_f32 v123,v123
v_mfma_f32_16x16x32_bf16 v[72:75],a[168:171],v[148:151],v[72:75]
s_waitcnt lgkmcnt(0)
v_exp_f32 v124,v124
v_mfma_f32_16x16x32_bf16 v[72:75],a[172:175],v[156:159],v[72:75]
v_exp_f32 v125,v125
v_mfma_f32_16x16x32_bf16 v[76:79],a[188:191],v[156:159],v[32:35]
v_exp_f32 v126,v126
v_mfma_f32_16x16x32_bf16 v[76:79],a[184:187],v[148:151],v[76:79]
v_exp_f32 v127,v127
v_mfma_f32_16x16x32_bf16 v[76:79],a[180:183],v[140:143],v[76:79]
v_cvt_pk_bf16_f32 v120,v120,v121
v_cvt_pk_bf16_f32 v121,v122,v123
v_mfma_f32_16x16x32_bf16 v[76:79],a[176:179],v[132:135],v[76:79]
v_cvt_pk_bf16_f32 v122,v124,v125
v_cvt_pk_bf16_f32 v123,v126,v127
v_mfma_f32_16x16x32_bf16 a[64:67],a[192:195],v[96:99],a[64:67]
global_load_lds_dwordx4 v5,s[10:11] offset:3072
v_mfma_f32_16x16x32_bf16 a[64:67],a[196:199],v[104:107],a[64:67]
v_exp_f32 v64,v64
v_mfma_f32_16x16x32_bf16 a[68:71],a[200:203],v[96:99],a[68:71]
v_exp_f32 v65,v65
v_mfma_f32_16x16x32_bf16 a[68:71],a[204:207],v[104:107],a[68:71]
v_exp_f32 v66,v66
v_mfma_f32_16x16x32_bf16 a[72:75],a[208:211],v[96:99],a[72:75]
v_exp_f32 v67,v67
v_mfma_f32_16x16x32_bf16 a[72:75],a[212:215],v[104:107],a[72:75]
v_exp_f32 v68,v68
v_mfma_f32_16x16x32_bf16 a[76:79],a[216:219],v[96:99],a[76:79]
v_exp_f32 v69,v69
v_mfma_f32_16x16x32_bf16 a[76:79],a[220:223],v[104:107],a[76:79]
v_exp_f32 v70,v70
v_mfma_f32_16x16x32_bf16 a[80:83],a[224:227],v[96:99],a[80:83]
v_exp_f32 v71,v71
v_mfma_f32_16x16x32_bf16 a[80:83],a[228:231],v[104:107],a[80:83]
v_cvt_pk_bf16_f32 v64,v64,v65
v_cvt_pk_bf16_f32 v65,v66,v67
v_mfma_f32_16x16x32_bf16 a[84:87],a[232:235],v[96:99],a[84:87]
v_cvt_pk_bf16_f32 v66,v68,v69
v_cvt_pk_bf16_f32 v67,v70,v71
v_mfma_f32_16x16x32_bf16 a[84:87],a[236:239],v[104:107],a[84:87]
v_exp_f32 v72,v72
v_mfma_f32_16x16x32_bf16 a[88:91],a[240:243],v[96:99],a[88:91]
s_add_u32 s10,s20,s74
s_addc_u32 s11,s21,0
v_mfma_f32_16x16x32_bf16 a[88:91],a[244:247],v[104:107],a[88:91]
v_exp_f32 v73,v73
v_mfma_f32_16x16x32_bf16 a[92:95],a[248:251],v[96:99],a[92:95]
s_add_u32 m0,s76,0
v_exp_f32 v74,v74
v_mfma_f32_16x16x32_bf16 a[92:95],a[252:255],v[104:107],a[92:95]
v_exp_f32 v75,v75
v_mfma_f32_16x16x32_bf16 v[56:59],v[28:31],v[96:99],v[56:59]
global_load_lds_dwordx4 v4,s[10:11] nt
v_cvt_pk_bf16_f32 v72,v72,v73
v_mfma_f32_16x16x32_bf16 v[56:59],v[28:31],v[104:107],v[56:59]
v_exp_f32 v76,v76
v_mfma_f32_16x16x32_bf16 v[80:83],a[128:131],v[164:167],v[36:39]
v_exp_f32 v77,v77
v_mfma_f32_16x16x32_bf16 v[80:83],a[132:135],v[172:175],v[80:83]
v_exp_f32 v78,v78
v_mfma_f32_16x16x32_bf16 v[80:83],a[136:139],v[180:183],v[80:83]
v_exp_f32 v79,v79
v_mfma_f32_16x16x32_bf16 v[80:83],a[140:143],v[188:191],v[80:83]
v_cvt_pk_bf16_f32 v73,v74,v75
v_cvt_pk_bf16_f32 v74,v76,v77
v_mfma_f32_16x16x32_bf16 v[84:87],a[156:159],v[188:191],v[36:39]
v_cvt_pk_bf16_f32 v75,v78,v79
v_mfma_f32_16x16x32_bf16 v[84:87],a[152:155],v[180:183],v[84:87]
v_mfma_f32_16x16x32_bf16 v[84:87],a[148:151],v[172:175],v[84:87]
v_mfma_f32_16x16x32_bf16 v[84:87],a[144:147],v[164:167],v[84:87]
v_mfma_f32_16x16x32_bf16 v[88:91],a[160:163],v[164:167],v[36:39]
v_exp_f32 v80,v80
v_mfma_f32_16x16x32_bf16 v[88:91],a[164:167],v[172:175],v[88:91]
v_exp_f32 v81,v81
v_mfma_f32_16x16x32_bf16 v[88:91],a[168:171],v[180:183],v[88:91]
v_exp_f32 v82,v82
v_mfma_f32_16x16x32_bf16 v[88:91],a[172:175],v[188:191],v[88:91]
v_exp_f32 v83,v83
v_mfma_f32_16x16x32_bf16 v[92:95],a[188:191],v[188:191],v[36:39]
v_exp_f32 v84,v84
v_mfma_f32_16x16x32_bf16 v[92:95],a[184:187],v[180:183],v[92:95]
v_exp_f32 v85,v85
v_mfma_f32_16x16x32_bf16 v[92:95],a[180:183],v[172:175],v[92:95]
global_load_lds_dwordx4 v4,s[10:11] offset:1024 nt
v_cvt_pk_bf16_f32 v80,v80,v81
v_mfma_f32_16x16x32_bf16 v[92:95],a[176:179],v[164:167],v[92:95]
v_exp_f32 v86,v86
v_mfma_f32_16x16x32_bf16 a[96:99],a[192:195],v[112:115],a[96:99]
ds_read_b64_tr_b16 a[192:193],v2
ds_read_b64_tr_b16 a[194:195],v2 offset:4096
v_mfma_f32_16x16x32_bf16 a[96:99],a[196:199],v[120:123],a[96:99]
ds_read_b64_tr_b16 a[196:197],v2 offset:8192
v_cvt_pk_bf16_f32 v81,v82,v83
v_mfma_f32_16x16x32_bf16 a[100:103],a[200:203],v[112:115],a[100:103]
ds_read_b64_tr_b16 a[198:199],v2 offset:12288
v_cvt_pk_bf16_f32 v82,v84,v85
v_mfma_f32_16x16x32_bf16 a[100:103],a[204:207],v[120:123],a[100:103]
v_exp_f32 v87,v87
v_mfma_f32_16x16x32_bf16 a[104:107],a[208:211],v[112:115],a[104:107]
ds_read_b64_tr_b16 a[200:201],v3
ds_read_b64_tr_b16 a[202:203],v3 offset:4096
v_mfma_f32_16x16x32_bf16 a[104:107],a[212:215],v[120:123],a[104:107]
ds_read_b64_tr_b16 a[204:205],v3 offset:8192
v_cvt_pk_bf16_f32 v83,v86,v87
v_mfma_f32_16x16x32_bf16 a[108:111],a[216:219],v[112:115],a[108:111]
v_exp_f32 v88,v88
v_mfma_f32_16x16x32_bf16 a[108:111],a[220:223],v[120:123],a[108:111]
ds_read_b64_tr_b16 a[206:207],v3 offset:12288
ds_read_b64_tr_b16 a[208:209],v2 offset:256
v_mfma_f32_16x16x32_bf16 a[112:115],a[224:227],v[112:115],a[112:115]
v_exp_f32 v89,v89
v_mfma_f32_16x16x32_bf16 a[112:115],a[228:231],v[120:123],a[112:115]
ds_read_b64_tr_b16 a[210:211],v2 offset:4352
ds_read_b64_tr_b16 a[212:213],v2 offset:8448
v_mfma_f32_16x16x32_bf16 a[116:119],a[232:235],v[112:115],a[116:119]
ds_read_b64_tr_b16 a[214:215],v2 offset:12544
v_cvt_pk_bf16_f32 v88,v88,v89
v_mfma_f32_16x16x32_bf16 a[116:119],a[236:239],v[120:123],a[116:119]
v_exp_f32 v90,v90
v_mfma_f32_16x16x32_bf16 a[120:123],a[240:243],v[112:115],a[120:123]
ds_read_b64_tr_b16 a[216:217],v3 offset:256
ds_read_b64_tr_b16 a[218:219],v3 offset:4352
v_mfma_f32_16x16x32_bf16 a[120:123],a[244:247],v[120:123],a[120:123]
v_exp_f32 v91,v91
v_mfma_f32_16x16x32_bf16 a[124:127],a[248:251],v[112:115],a[124:127]
global_load_lds_dwordx4 v5,s[10:11] offset:2048 nt
ds_read_b64_tr_b16 a[220:221],v3 offset:8448
v_mfma_f32_16x16x32_bf16 a[124:127],a[252:255],v[120:123],a[124:127]
ds_read_b64_tr_b16 a[222:223],v3 offset:12544
ds_read_b64_tr_b16 a[224:225],v2 offset:512
v_mfma_f32_16x16x32_bf16 v[60:63],v[28:31],v[112:115],v[60:63]
ds_read_b64_tr_b16 a[226:227],v2 offset:4608
v_cvt_pk_bf16_f32 v89,v90,v91
v_mfma_f32_16x16x32_bf16 v[60:63],v[28:31],v[120:123],v[60:63]
v_exp_f32 v92,v92
v_mfma_f32_16x16x32_bf16 v[96:99],a[128:131],v[196:199],v[40:43]
ds_read_b64_tr_b16 a[228:229],v2 offset:8704
ds_read_b64_tr_b16 a[230:231],v2 offset:12800
v_mfma_f32_16x16x32_bf16 v[96:99],a[132:135],v[204:207],v[96:99]
v_exp_f32 v93,v93
v_mfma_f32_16x16x32_bf16 v[96:99],a[136:139],v[212:215],v[96:99]
)A"
R"A(ds_read_b64_tr_b16 a[232:233],v3 offset:512
ds_read_b64_tr_b16 a[234:235],v3 offset:4608
v_mfma_f32_16x16x32_bf16 v[96:99],a[140:143],v[220:223],v[96:99]
ds_read_b64_tr_b16 a[236:237],v3 offset:8704
v_cvt_pk_bf16_f32 v90,v92,v93
v_mfma_f32_16x16x32_bf16 v[100:103],a[156:159],v[220:223],v[40:43]
v_exp_f32 v94,v94
v_mfma_f32_16x16x32_bf16 v[100:103],a[152:155],v[212:215],v[100:103]
ds_read_b64_tr_b16 a[238:239],v3 offset:12800
ds_read_b64_tr_b16 a[240:241],v2 offset:768
v_mfma_f32_16x16x32_bf16 v[100:103],a[148:151],v[204:207],v[100:103]
v_exp_f32 v95,v95
v_mfma_f32_16x16x32_bf16 v[100:103],a[144:147],v[196:199],v[100:103]
ds_read_b64_tr_b16 a[242:243],v2 offset:4864
ds_read_b64_tr_b16 a[244:245],v2 offset:8960
v_mfma_f32_16x16x32_bf16 v[104:107],a[160:163],v[196:199],v[40:43]
ds_read_b64_tr_b16 a[246:247],v2 offset:13056
v_cvt_pk_bf16_f32 v91,v94,v95
v_mfma_f32_16x16x32_bf16 v[104:107],a[164:167],v[204:207],v[104:107]
v_exp_f32 v96,v96
v_mfma_f32_16x16x32_bf16 v[104:107],a[168:171],v[212:215],v[104:107]
ds_read_b64_tr_b16 a[248:249],v3 offset:768
ds_read_b64_tr_b16 a[250:251],v3 offset:4864
v_mfma_f32_16x16x32_bf16 v[104:107],a[172:175],v[220:223],v[104:107]
v_exp_f32 v97,v97
v_mfma_f32_16x16x32_bf16 v[108:111],a[188:191],v[220:223],v[40:43]
global_load_lds_dwordx4 v5,s[10:11] offset:3072
ds_read_b64_tr_b16 a[252:253],v3 offset:8960
v_mfma_f32_16x16x32_bf16 v[108:111],a[184:187],v[212:215],v[108:111]
ds_read_b64_tr_b16 a[254:255],v3 offset:13056
v_cvt_pk_bf16_f32 v96,v96,v97
v_mfma_f32_16x16x32_bf16 v[108:111],a[180:183],v[204:207],v[108:111]
v_exp_f32 v98,v98
v_mfma_f32_16x16x32_bf16 v[108:111],a[176:179],v[196:199],v[108:111]
s_waitcnt lgkmcnt(5)
v_exp_f32 v99,v99
v_mfma_f32_16x16x32_bf16 a[0:3],a[192:195],v[64:67],a[0:3]
v_exp_f32 v100,v100
v_mfma_f32_16x16x32_bf16 a[0:3],a[196:199],v[72:75],a[0:3]
v_exp_f32 v101,v101
v_mfma_f32_16x16x32_bf16 a[4:7],a[200:203],v[64:67],a[4:7]
v_exp_f32 v102,v102
v_mfma_f32_16x16x32_bf16 a[4:7],a[204:207],v[72:75],a[4:7]
v_exp_f32 v103,v103
v_mfma_f32_16x16x32_bf16 a[8:11],a[208:211],v[64:67],a[8:11]
v_cvt_pk_bf16_f32 v97,v98,v99
v_cvt_pk_bf16_f32 v98,v100,v101
v_mfma_f32_16x16x32_bf16 a[8:11],a[212:215],v[72:75],a[8:11]
v_cvt_pk_bf16_f32 v99,v102,v103
v_mfma_f32_16x16x32_bf16 a[12:15],a[216:219],v[64:67],a[12:15]
v_mfma_f32_16x16x32_bf16 a[12:15],a[220:223],v[72:75],a[12:15]
v_mfma_f32_16x16x32_bf16 a[16:19],a[224:227],v[64:67],a[16:19]
v_mfma_f32_16x16x32_bf16 a[16:19],a[228:231],v[72:75],a[16:19]
v_mfma_f32_16x16x32_bf16 a[20:23],a[232:235],v[64:67],a[20:23]
s_getreg_b32 s92,hwreg(HW_REG_IB_STS,0,4)
s_min_u32 s4,s92,s6
s_mov_b32 s6,s92
v_mfma_f32_16x16x32_bf16 a[20:23],a[236:239],v[72:75],a[20:23]
s_cmp_ge_u32 s4,s9
s_cselect_b32 s4,15,s5
v_mfma_f32_16x16x32_bf16 a[24:27],a[240:243],v[64:67],a[24:27]
s_waitcnt lgkmcnt(0)
v_mfma_f32_16x16x32_bf16 a[24:27],a[244:247],v[72:75],a[24:27]
v_mfma_f32_16x16x32_bf16 a[28:31],a[248:251],v[64:67],a[28:31]
v_mfma_f32_16x16x32_bf16 a[28:31],a[252:255],v[72:75],a[28:31]
s_getpc_b64 s[92:93]
s_cmp_lg_u32 s4,0
s_cbranch_scc1 L_pace_%=
v_mfma_f32_16x16x32_bf16 v[48:51],v[28:31],v[64:67],v[48:51]
v_mfma_f32_16x16x32_bf16 v[48:51],v[28:31],v[72:75],v[48:51]
s_sub_u32 s70,s70,1
s_cbranch_scc0 L_a0_%=
L_nx0_%=:
s_waitcnt vmcnt(12)
s_barrier
v_mfma_f32_16x16x32_bf16 v[112:115],a[128:131],v[228:231],v[44:47]
s_add_u32 s10,s24,s74
s_addc_u32 s11,s25,0
s_add_u32 m0,s76,16384
v_mfma_f32_16x16x32_bf16 v[112:115],a[132:135],v[236:239],v[112:115]
s_add_u32 s75,s75,16384
s_add_u32 s74,s74,16384
ds_read_b128 v[32:35],v7
v_mfma_f32_16x16x32_bf16 v[112:115],a[136:139],v[244:247],v[112:115]
s_and_b32 s74,s74,0x3ffff
global_load_lds_dwordx4 v4,s[10:11]
v_mfma_f32_16x16x32_bf16 v[112:115],a[140:143],v[252:255],v[112:115]
s_and_b32 s69,s75,0xfffc0000
s_or_b32 s74,s74,s69
ds_read_b128 a[128:131],v1
v_mfma_f32_16x16x32_bf16 v[116:119],a[156:159],v[252:255],v[44:47]
s_sub_u32 s76,s76,32768
v_exp_f32 v104,v104
v_mfma_f32_16x16x32_bf16 v[116:119],a[152:155],v[244:247],v[116:119]
s_and_b32 s76,s76,0x1ffff
ds_read_b128 a[132:135],v1 offset:256
v_add_u32 v2,s78,v2
v_mfma_f32_16x16x32_bf16 v[116:119],a[148:151],v[236:239],v[116:119]
v_exp_f32 v105,v105
v_mfma_f32_16x16x32_bf16 v[116:119],a[144:147],v[228:231],v[116:119]
ds_read_b128 a[136:139],v1 offset:512
v_cvt_pk_bf16_f32 v104,v104,v105
v_mfma_f32_16x16x32_bf16 v[120:123],a[160:163],v[228:231],v[44:47]
v_exp_f32 v106,v106
v_mfma_f32_16x16x32_bf16 v[120:123],a[164:167],v[236:239],v[120:123]
ds_read_b128 a[140:143],v1 offset:768
v_add_u32 v3,s78,v3
v_mfma_f32_16x16x32_bf16 v[120:123],a[168:171],v[244:247],v[120:123]
s_sub_u32 s78,s77,32768
v_exp_f32 v107,v107
v_mfma_f32_16x16x32_bf16 v[120:123],a[172:175],v[252:255],v[120:123]
s_and_b32 s78,s78,0x1ffff
ds_read_b128 a[156:159],v1 offset:4864
v_cvt_pk_bf16_f32 v105,v106,v107
v_mfma_f32_16x16x32_bf16 v[124:127],a[188:191],v[252:255],v[44:47]
s_sub_u32 s78,s78,s77
v_exp_f32 v108,v108
v_mfma_f32_16x16x32_bf16 v[124:127],a[184:187],v[244:247],v[124:127]
s_add_u32 s77,s77,s78
ds_read_b128 a[152:155],v1 offset:4608
v_accvgpr_read_b32 v8,a0
v_mfma_f32_16x16x32_bf16 v[124:127],a[180:183],v[236:239],v[124:127]
v_exp_f32 v109,v109
v_mfma_f32_16x16x32_bf16 v[124:127],a[176:179],v[228:231],v[124:127]
ds_read_b128 a[148:151],v1 offset:4352
v_cvt_pk_bf16_f32 v106,v108,v109
v_mfma_f32_16x16x32_bf16 v[52:55],v[28:31],v[80:83],v[52:55]
global_load_lds_dwordx4 v5,s[10:11] offset:1024
v_accvgpr_read_b32 v9,a1
v_mfma_f32_16x16x32_bf16 v[52:55],v[28:31],v[88:91],v[52:55]
ds_read_b128 a[144:147],v1 offset:4096
v_accvgpr_read_b32 v10,a2
v_mfma_f32_16x16x32_bf16 a[32:35],a[192:195],v[80:83],a[32:35]
v_exp_f32 v110,v110
v_mfma_f32_16x16x32_bf16 a[32:35],a[196:199],v[88:91],a[32:35]
ds_read_b128 a[160:163],v1 offset:8192
v_accvgpr_read_b32 v11,a3
v_mfma_f32_16x16x32_bf16 a[36:39],a[200:203],v[80:83],a[36:39]
v_exp_f32 v111,v111
v_mfma_f32_16x16x32_bf16 a[36:39],a[204:207],v[88:91],a[36:39]
ds_read_b128 a[164:167],v1 offset:8448
v_cvt_pk_bf16_f32 v107,v110,v111
v_mfma_f32_16x16x32_bf16 a[40:43],a[208:211],v[80:83],a[40:43]
v_exp_f32 v112,v112
v_mfma_f32_16x16x32_bf16 a[40:43],a[212:215],v[88:91],a[40:43]
ds_read_b128 a[168:171],v1 offset:8704
v_accvgpr_read_b32 v12,a4
v_mfma_f32_16x16x32_bf16 a[44:47],a[216:219],v[80:83],a[44:47]
v_exp_f32 v113,v113
v_mfma_f32_16x16x32_bf16 a[44:47],a[220:223],v[88:91],a[44:47]
ds_read_b128 a[172:175],v1 offset:8960
v_cvt_pk_bf16_f32 v112,v112,v113
v_mfma_f32_16x16x32_bf16 a[48:51],a[224:227],v[80:83],a[48:51]
v_exp_f32 v114,v114
v_mfma_f32_16x16x32_bf16 a[48:51],a[228:231],v[88:91],a[48:51]
ds_read_b128 a[188:191],v1 offset:13056
v_accvgpr_read_b32 v13,a5
v_mfma_f32_16x16x32_bf16 a[52:55],a[232:235],v[80:83],a[52:55]
v_exp_f32 v115,v115
v_mfma_f32_16x16x32_bf16 a[52:55],a[236:239],v[88:91],a[52:55]
ds_read_b128 a[184:187],v1 offset:12800
v_cvt_pk_bf16_f32 v113,v114,v115
v_mfma_f32_16x16x32_bf16 a[56:59],a[240:243],v[80:83],a[56:59]
global_load_lds_dwordx4 v4,s[10:11] offset:2048
v_accvgpr_read_b32 v14,a6
v_mfma_f32_16x16x32_bf16 a[56:59],a[244:247],v[88:91],a[56:59]
ds_read_b128 a[180:183],v1 offset:12544
v_accvgpr_read_b32 v15,a7
v_mfma_f32_16x16x32_bf16 a[60:63],a[248:251],v[80:83],a[60:63]
v_exp_f32 v116,v116
v_mfma_f32_16x16x32_bf16 a[60:63],a[252:255],v[88:91],a[60:63]
s_waitcnt lgkmcnt(3)
ds_read_b128 a[176:179],v1 offset:12288
v_max_f32 v24,0x00800000,v48
v_mfma_f32_16x16x32_bf16 v[64:67],a[128:131],v[128:131],v[32:35]
v_exp_f32 v117,v117
v_mfma_f32_16x16x32_bf16 v[64:67],a[132:135],v[136:139],v[64:67]
ds_read_b128 v[36:39],v7 offset:1024
v_cvt_pk_bf16_f32 v114,v116,v117
v_mfma_f32_16x16x32_bf16 v[64:67],a[136:139],v[144:147],v[64:67]
v_exp_f32 v118,v118
v_mfma_f32_16x16x32_bf16 v[64:67],a[140:143],v[152:155],v[64:67]
v_exp_f32 v119,v119
v_mfma_f32_16x16x32_bf16 v[68:71],a[156:159],v[152:155],v[32:35]
ds_read_b128 v[40:43],v7 offset:2048
v_cvt_pk_bf16_f32 v115,v118,v119
v_mfma_f32_16x16x32_bf16 v[68:71],a[152:155],v[144:147],v[68:71]
v_exp_f32 v120,v120
v_mfma_f32_16x16x32_bf16 v[68:71],a[148:151],v[136:139],v[68:71]
v_exp_f32 v121,v121
v_mfma_f32_16x16x32_bf16 v[68:71],a[144:147],v[128:131],v[68:71]
v_exp_f32 v122,v122
v_mfma_f32_16x16x32_bf16 v[72:75],a[160:163],v[128:131],v[32:35]
v_exp_f32 v123,v123
v_mfma_f32_16x16x32_bf16 v[72:75],a[164:167],v[136:139],v[72:75]
v_exp_f32 v124,v124
v_mfma_f32_16x16x32_bf16 v[72:75],a[168:171],v[144:147],v[72:75]
L_pd7_%=:
s_cbranch_execz L_trt0_%=
s_waitcnt lgkmcnt(1)
v_exp_f32 v125,v125
v_mfma_f32_16x16x32_bf16 v[72:75],a[172:175],v[152:155],v[72:75]
global_load_lds_dwordx4 v5,s[10:11] offset:3072
v_cvt_pk_bf16_f32 v120,v120,v121
v_mfma_f32_16x16x32_bf16 v[76:79],a[188:191],v[152:155],v[32:35]
v_exp_f32 v126,v126
v_mfma_f32_16x16x32_bf16 v[76:79],a[184:187],v[144:147],v[76:79]
v_exp_f32 v127,v127
v_mfma_f32_16x16x32_bf16 v[76:79],a[180:183],v[136:139],v[76:79]
v_cvt_pk_bf16_f32 v121,v122,v123
v_cvt_pk_bf16_f32 v122,v124,v125
v_mfma_f32_16x16x32_bf16 v[76:79],a[176:179],v[128:131],v[76:79]
v_cvt_pk_bf16_f32 v123,v126,v127
v_accvgpr_read_b32 v16,a8
v_mfma_f32_16x16x32_bf16 v[56:59],v[28:31],v[96:99],v[56:59]
v_exp_f32 v64,v64
v_mfma_f32_16x16x32_bf16 v[56:59],v[28:31],v[104:107],v[56:59]
v_exp_f32 v65,v65
v_mfma_f32_16x16x32_bf16 a[64:67],a[192:195],v[96:99],a[64:67]
v_exp_f32 v66,v66
v_mfma_f32_16x16x32_bf16 a[64:67],a[196:199],v[104:107],a[64:67]
v_exp_f32 v67,v67
v_mfma_f32_16x16x32_bf16 a[68:71],a[200:203],v[96:99],a[68:71]
v_exp_f32 v68,v68
v_mfma_f32_16x16x32_bf16 a[68:71],a[204:207],v[104:107],a[68:71]
s_add_u32 s10,s20,s74
s_addc_u32 s11,s21,0
v_cvt_pk_bf16_f32 v64,v64,v65
v_mfma_f32_16x16x32_bf16 a[72:75],a[208:211],v[96:99],a[72:75]
v_exp_f32 v69,v69
v_mfma_f32_16x16x32_bf16 a[72:75],a[212:215],v[104:107],a[72:75]
s_add_u32 m0,s76,0
v_exp_f32 v70,v70
v_mfma_f32_16x16x32_bf16 a[76:79],a[216:219],v[96:99],a[76:79]
v_exp_f32 v71,v71
v_mfma_f32_16x16x32_bf16 a[76:79],a[220:223],v[104:107],a[76:79]
global_load_lds_dwordx4 v4,s[10:11] nt
v_cvt_pk_bf16_f32 v65,v66,v67
v_mfma_f32_16x16x32_bf16 a[80:83],a[224:227],v[96:99],a[80:83]
v_cvt_pk_bf16_f32 v66,v68,v69
v_cvt_pk_bf16_f32 v67,v70,v71
v_mfma_f32_16x16x32_bf16 a[80:83],a[228:231],v[104:107],a[80:83]
v_exp_f32 v72,v72
v_mfma_f32_16x16x32_bf16 a[84:87],a[232:235],v[96:99],a[84:87]
v_exp_f32 v73,v73
v_mfma_f32_16x16x32_bf16 a[84:87],a[236:239],v[104:107],a[84:87]
v_exp_f32 v74,v74
v_mfma_f32_16x16x32_bf16 a[88:91],a[240:243],v[96:99],a[88:91]
v_exp_f32 v75,v75
v_mfma_f32_16x16x32_bf16 a[88:91],a[244:247],v[104:107],a[88:91]
v_exp_f32 v76,v76
v_mfma_f32_16x16x32_bf16 a[92:95],a[248:251],v[96:99],a[92:95]
v_exp_f32 v77,v77
v_mfma_f32_16x16x32_bf16 a[92:95],a[252:255],v[104:107],a[92:95]
v_exp_f32 v78,v78
v_mfma_f32_16x16x32_bf16 v[80:83],a[128:131],v[160:163],v[36:39]
v_exp_f32 v79,v79
v_mfma_f32_16x16x32_bf16 v[80:83],a[132:135],v[168:171],v[80:83]
v_cvt_pk_bf16_f32 v72,v72,v73
v_cvt_pk_bf16_f32 v73,v74,v75
v_mfma_f32_16x16x32_bf16 v[80:83],a[136:139],v[176:179],v[80:83]
v_cvt_pk_bf16_f32 v74,v76,v77
v_cvt_pk_bf16_f32 v75,v78,v79
v_mfma_f32_16x16x32_bf16 v[80:83],a[140:143],v[184:187],v[80:83]
v_rcp_f32 v24,v24
v_mfma_f32_16x16x32_bf16 v[84:87],a[156:159],v[184:187],v[36:39]
v_accvgpr_read_b32 v17,a9
v_accvgpr_read_b32 v18,a10
v_mfma_f32_16x16x32_bf16 v[84:87],a[152:155],v[176:179],v[84:87]
v_accvgpr_read_b32 v19,a11
v_mul_f32 v8,v8,v24
v_mfma_f32_16x16x32_bf16 v[84:87],a[148:151],v[168:171],v[84:87]
global_load_lds_dwordx4 v4,s[10:11] offset:1024 nt
v_mul_f32 v9,v9,v24
v_mfma_f32_16x16x32_bf16 v[84:87],a[144:147],v[160:163],v[84:87]
v_mul_f32 v10,v10,v24
v_mul_f32 v11,v11,v24
v_mfma_f32_16x16x32_bf16 v[88:91],a[160:163],v[160:163],v[36:39]
v_mul_f32 v12,v12,v24
v_mul_f32 v13,v13,v24
v_mfma_f32_16x16x32_bf16 v[88:91],a[164:167],v[168:171],v[88:91]
v_mul_f32 v14,v14,v24
v_mul_f32 v15,v15,v24
v_mfma_f32_16x16x32_bf16 v[88:91],a[168:171],v[176:179],v[88:91]
v_cvt_pk_bf16_f32 v8,v8,v9
v_cvt_pk_bf16_f32 v9,v10,v11
v_mfma_f32_16x16x32_bf16 v[88:91],a[172:175],v[184:187],v[88:91]
v_cvt_pk_bf16_f32 v10,v12,v13
v_cvt_pk_bf16_f32 v11,v14,v15
)A"
R"A(v_mfma_f32_16x16x32_bf16 v[92:95],a[188:191],v[184:187],v[36:39]
ds_write_b128 v7,v[8:11] offset:6144
v_mfma_f32_16x16x32_bf16 v[92:95],a[184:187],v[176:179],v[92:95]
v_accvgpr_read_b32 v20,a12
v_accvgpr_read_b32 v21,a13
v_mfma_f32_16x16x32_bf16 v[92:95],a[180:183],v[168:171],v[92:95]
v_accvgpr_read_b32 v22,a14
v_accvgpr_read_b32 v23,a15
v_mfma_f32_16x16x32_bf16 v[92:95],a[176:179],v[160:163],v[92:95]
s_cmp_le_u32 s5,s7
s_cselect_b32 s4,2,0
v_accvgpr_read_b32 v8,a16
v_mfma_f32_16x16x32_bf16 v[60:63],v[28:31],v[112:115],v[60:63]
s_sub_u32 s5,s5,s4
v_accvgpr_read_b32 v9,a17
v_accvgpr_read_b32 v10,a18
v_mfma_f32_16x16x32_bf16 v[60:63],v[28:31],v[120:123],v[60:63]
s_max_i32 s5,s5,0
v_accvgpr_read_b32 v11,a19
v_mul_f32 v16,v16,v24
v_mfma_f32_16x16x32_bf16 a[96:99],a[192:195],v[112:115],a[96:99]
s_sub_u32 s7,s7,2
ds_read_b64_tr_b16 a[192:193],v2
ds_read_b64_tr_b16 a[194:195],v2 offset:4096
v_mfma_f32_16x16x32_bf16 a[96:99],a[196:199],v[120:123],a[96:99]
s_max_i32 s7,s7,0
ds_read_b64_tr_b16 a[196:197],v2 offset:8192
v_mul_f32 v17,v17,v24
v_mfma_f32_16x16x32_bf16 a[100:103],a[200:203],v[112:115],a[100:103]
global_load_lds_dwordx4 v5,s[10:11] offset:2048 nt
ds_read_b64_tr_b16 a[198:199],v2 offset:12288
v_mfma_f32_16x16x32_bf16 a[100:103],a[204:207],v[120:123],a[100:103]
ds_read_b64_tr_b16 a[200:201],v3
v_mul_f32 v18,v18,v24
v_mfma_f32_16x16x32_bf16 a[104:107],a[208:211],v[112:115],a[104:107]
ds_read_b64_tr_b16 a[202:203],v3 offset:4096
ds_read_b64_tr_b16 a[204:205],v3 offset:8192
v_mfma_f32_16x16x32_bf16 a[104:107],a[212:215],v[120:123],a[104:107]
ds_read_b64_tr_b16 a[206:207],v3 offset:12288
v_mul_f32 v19,v19,v24
v_mfma_f32_16x16x32_bf16 a[108:111],a[216:219],v[112:115],a[108:111]
ds_read_b64_tr_b16 a[208:209],v2 offset:256
v_mul_f32 v20,v20,v24
v_mfma_f32_16x16x32_bf16 a[108:111],a[220:223],v[120:123],a[108:111]
ds_read_b64_tr_b16 a[210:211],v2 offset:4352
v_mul_f32 v21,v21,v24
v_mfma_f32_16x16x32_bf16 a[112:115],a[224:227],v[112:115],a[112:115]
ds_read_b64_tr_b16 a[212:213],v2 offset:8448
ds_read_b64_tr_b16 a[214:215],v2 offset:12544
v_mfma_f32_16x16x32_bf16 a[112:115],a[228:231],v[120:123],a[112:115]
ds_read_b64_tr_b16 a[216:217],v3 offset:256
v_mul_f32 v22,v22,v24
v_mfma_f32_16x16x32_bf16 a[116:119],a[232:235],v[112:115],a[116:119]
ds_read_b64_tr_b16 a[218:219],v3 offset:4352
v_mul_f32 v23,v23,v24
v_mfma_f32_16x16x32_bf16 a[116:119],a[236:239],v[120:123],a[116:119]
ds_read_b64_tr_b16 a[220:221],v3 offset:8448
v_cvt_pk_bf16_f32 v16,v16,v17
v_mfma_f32_16x16x32_bf16 a[120:123],a[240:243],v[112:115],a[120:123]
ds_read_b64_tr_b16 a[222:223],v3 offset:12544
ds_read_b64_tr_b16 a[224:225],v2 offset:512
v_mfma_f32_16x16x32_bf16 a[120:123],a[244:247],v[120:123],a[120:123]
ds_read_b64_tr_b16 a[226:227],v2 offset:4608
v_cvt_pk_bf16_f32 v17,v18,v19
v_mfma_f32_16x16x32_bf16 a[124:127],a[248:251],v[112:115],a[124:127]
ds_read_b64_tr_b16 a[228:229],v2 offset:8704
v_cvt_pk_bf16_f32 v18,v20,v21
v_mfma_f32_16x16x32_bf16 a[124:127],a[252:255],v[120:123],a[124:127]
s_waitcnt lgkmcnt(9)
ds_read_b64_tr_b16 a[230:231],v2 offset:12800
v_cvt_pk_bf16_f32 v19,v22,v23
v_mfma_f32_16x16x32_bf16 v[96:99],a[128:131],v[192:195],v[40:43]
ds_read_b64_tr_b16 a[232:233],v3 offset:512
ds_read_b64_tr_b16 a[234:235],v3 offset:4608
v_mfma_f32_16x16x32_bf16 v[96:99],a[132:135],v[200:203],v[96:99]
global_load_lds_dwordx4 v5,s[10:11] offset:3072
ds_read_b64_tr_b16 a[236:237],v3 offset:8704
v_mfma_f32_16x16x32_bf16 v[96:99],a[136:139],v[208:211],v[96:99]
ds_read_b64_tr_b16 a[238:239],v3 offset:12800
v_accvgpr_read_b32 v12,a20
v_mfma_f32_16x16x32_bf16 v[96:99],a[140:143],v[216:219],v[96:99]
ds_read_b64_tr_b16 a[240:241],v2 offset:768
v_accvgpr_read_b32 v13,a21
v_mfma_f32_16x16x32_bf16 v[100:103],a[156:159],v[216:219],v[40:43]
ds_read_b64_tr_b16 a[242:243],v2 offset:4864
ds_read_b64_tr_b16 a[244:245],v2 offset:8960
v_mfma_f32_16x16x32_bf16 v[100:103],a[152:155],v[208:211],v[100:103]
ds_read_b64_tr_b16 a[246:247],v2 offset:13056
v_accvgpr_read_b32 v14,a22
v_mfma_f32_16x16x32_bf16 v[100:103],a[148:151],v[200:203],v[100:103]
ds_read_b64_tr_b16 a[248:249],v3 offset:768
v_accvgpr_read_b32 v15,a23
v_mfma_f32_16x16x32_bf16 v[100:103],a[144:147],v[192:195],v[100:103]
ds_read_b64_tr_b16 a[250:251],v3 offset:4864
v_accvgpr_read_b32 v20,a28
v_mfma_f32_16x16x32_bf16 v[104:107],a[160:163],v[192:195],v[40:43]
ds_read_b64_tr_b16 a[252:253],v3 offset:8960
ds_read_b64_tr_b16 a[254:255],v3 offset:13056
v_mfma_f32_16x16x32_bf16 v[104:107],a[164:167],v[200:203],v[104:107]
v_accvgpr_read_b32 v21,a29
v_accvgpr_read_b32 v22,a30
v_mfma_f32_16x16x32_bf16 v[104:107],a[168:171],v[208:211],v[104:107]
ds_write_b128 v7,v[16:19] offset:7168
v_mfma_f32_16x16x32_bf16 v[104:107],a[172:175],v[216:219],v[104:107]
s_add_u32 s10,s24,s74
s_addc_u32 s11,s25,0
v_accvgpr_read_b32 v23,a31
v_mfma_f32_16x16x32_bf16 v[108:111],a[188:191],v[216:219],v[40:43]
v_accvgpr_read_b32 v16,a24
v_accvgpr_read_b32 v17,a25
v_mfma_f32_16x16x32_bf16 v[108:111],a[184:187],v[208:211],v[108:111]
s_add_u32 m0,s76,16384
v_accvgpr_read_b32 v18,a26
v_accvgpr_read_b32 v19,a27
v_mfma_f32_16x16x32_bf16 v[108:111],a[180:183],v[200:203],v[108:111]
v_cmp_nlt_f32 s[54:55],v48,s73
v_cmp_nlt_f32 s[56:57],v52,s73
v_mfma_f32_16x16x32_bf16 v[108:111],a[176:179],v[192:195],v[108:111]
s_or_b64 s[54:55],s[54:55],s[56:57]
global_load_lds_dwordx4 v4,s[10:11]
v_mfma_f32_16x16x32_bf16 a[0:3],a[192:195],v[64:67],0
v_cmp_nlt_f32 s[56:57],v56,s73
v_mul_f32 v8,v8,v24
v_mfma_f32_16x16x32_bf16 a[0:3],a[196:199],v[72:75],a[0:3]
s_or_b64 s[54:55],s[54:55],s[56:57]
v_mul_f32 v9,v9,v24
v_mul_f32 v10,v10,v24
v_mfma_f32_16x16x32_bf16 a[4:7],a[200:203],v[64:67],0
v_mul_f32 v11,v11,v24
v_mul_f32 v12,v12,v24
v_mfma_f32_16x16x32_bf16 a[4:7],a[204:207],v[72:75],a[4:7]
v_mul_f32 v13,v13,v24
v_mul_f32 v14,v14,v24
v_mfma_f32_16x16x32_bf16 a[8:11],a[208:211],v[64:67],0
s_waitcnt lgkmcnt(0)
v_mul_f32 v15,v15,v24
v_cvt_pk_bf16_f32 v8,v8,v9
v_mfma_f32_16x16x32_bf16 a[8:11],a[212:215],v[72:75],a[8:11]
v_cvt_pk_bf16_f32 v9,v10,v11
v_cvt_pk_bf16_f32 v10,v12,v13
v_mfma_f32_16x16x32_bf16 a[12:15],a[216:219],v[64:67],0
v_cvt_pk_bf16_f32 v11,v14,v15
v_mul_f32 v16,v16,v24
v_mfma_f32_16x16x32_bf16 a[12:15],a[220:223],v[72:75],a[12:15]
v_mul_f32 v17,v17,v24
v_mul_f32 v18,v18,v24
v_mfma_f32_16x16x32_bf16 a[16:19],a[224:227],v[64:67],0
v_mul_f32 v19,v19,v24
v_mul_f32 v20,v20,v24
v_mfma_f32_16x16x32_bf16 a[16:19],a[228:231],v[72:75],a[16:19]
v_mul_f32 v21,v21,v24
v_mul_f32 v22,v22,v24
v_mfma_f32_16x16x32_bf16 a[20:23],a[232:235],v[64:67],0
s_getreg_b32 s92,hwreg(HW_REG_IB_STS,0,4)
s_min_u32 s4,s92,s6
s_mov_b32 s6,s92
v_mfma_f32_16x16x32_bf16 a[20:23],a[236:239],v[72:75],a[20:23]
s_cmp_gt_u32 s4,s9
s_cselect_b32 s4,15,s5
v_mul_f32 v23,v23,v24
v_mfma_f32_16x16x32_bf16 a[24:27],a[240:243],v[64:67],0
v_cvt_pk_bf16_f32 v16,v16,v17
v_cvt_pk_bf16_f32 v17,v18,v19
v_mfma_f32_16x16x32_bf16 a[24:27],a[244:247],v[72:75],a[24:27]
v_cvt_pk_bf16_f32 v18,v20,v21
v_cvt_pk_bf16_f32 v19,v22,v23
v_mfma_f32_16x16x32_bf16 a[28:31],a[248:251],v[64:67],0
global_load_lds_dwordx4 v5,s[10:11] offset:1024
v_cmp_nlt_f32 s[56:57],v60,s73
v_mfma_f32_16x16x32_bf16 a[28:31],a[252:255],v[72:75],a[28:31]
s_or_b64 s[54:55],s[54:55],s[56:57]
v_exp_f32 v80,v80
v_mfma_f32_16x16x32_bf16 v[48:51],v[28:31],v[64:67],0
s_getpc_b64 s[92:93]
s_cmp_lg_u32 s4,0
s_cbranch_scc1 L_pace_%=
v_exp_f32 v81,v81
v_exp_f32 v82,v82
v_exp_f32 v83,v83
v_exp_f32 v84,v84
v_exp_f32 v85,v85
v_exp_f32 v86,v86
v_exp_f32 v87,v87
v_exp_f32 v88,v88
v_exp_f32 v89,v89
v_mfma_f32_16x16x32_bf16 v[48:51],v[28:31],v[72:75],v[48:51]
ds_write_b128 v7,v[8:11]
v_add_u32 v1,s78,v1
v_max_f32 v25,0x00800000,v52
v_rcp_f32 v25,v25
v_max_f32 v26,0x00800000,v56
v_rcp_f32 v26,v26
v_cvt_pk_bf16_f32 v80,v80,v81
v_cvt_pk_bf16_f32 v81,v82,v83
v_cvt_pk_bf16_f32 v82,v84,v85
v_cvt_pk_bf16_f32 v83,v86,v87
v_cvt_pk_bf16_f32 v88,v88,v89
v_max_f32 v27,0x00800000,v60
v_rcp_f32 v27,v27
ds_write_b128 v7,v[16:19] offset:1024
ds_read_b128 v[44:47],v7 offset:3072
s_cmp_lg_u64 s[54:55],0
s_cbranch_scc1 L_slow0_%=
L_ok0_%=:
s_waitcnt vmcnt(14)
s_barrier
s_waitcnt lgkmcnt(0)
v_mfma_f32_16x16x32_bf16 v[112:115],a[128:131],v[224:227],v[44:47]
s_add_u32 s10,s24,s74
s_addc_u32 s11,s25,0
s_add_u32 m0,s76,16384
v_mfma_f32_16x16x32_bf16 v[112:115],a[132:135],v[232:235],v[112:115]
s_add_u32 s75,s75,16384
v_exp_f32 v90,v90
v_mfma_f32_16x16x32_bf16 v[112:115],a[136:139],v[240:243],v[112:115]
global_load_lds_dwordx4 v4,s[10:11] offset:2048
ds_read_b128 a[128:131],v1
v_mfma_f32_16x16x32_bf16 v[112:115],a[140:143],v[248:251],v[112:115]
s_add_u32 s74,s74,16384
v_exp_f32 v91,v91
v_mfma_f32_16x16x32_bf16 v[116:119],a[156:159],v[248:251],v[44:47]
s_and_b32 s74,s74,0x3ffff
v_exp_f32 v92,v92
v_mfma_f32_16x16x32_bf16 v[116:119],a[152:155],v[240:243],v[116:119]
s_and_b32 s69,s75,0xfffc0000
v_exp_f32 v93,v93
v_mfma_f32_16x16x32_bf16 v[116:119],a[148:151],v[232:235],v[116:119]
s_or_b32 s74,s74,s69
v_exp_f32 v94,v94
v_mfma_f32_16x16x32_bf16 v[116:119],a[144:147],v[224:227],v[116:119]
s_sub_u32 s76,s76,32768
v_exp_f32 v95,v95
v_mfma_f32_16x16x32_bf16 v[120:123],a[160:163],v[224:227],v[44:47]
s_and_b32 s76,s76,0x1ffff
v_cvt_pk_bf16_f32 v89,v90,v91
v_cvt_pk_bf16_f32 v90,v92,v93
v_mfma_f32_16x16x32_bf16 v[120:123],a[164:167],v[232:235],v[120:123]
s_add_u32 s75,s75,16384
ds_read_b128 a[132:135],v1 offset:256
v_cvt_pk_bf16_f32 v91,v94,v95
v_mfma_f32_16x16x32_bf16 v[120:123],a[168:171],v[240:243],v[120:123]
s_and_b32 s69,s75,0xfffc0000
v_exp_f32 v96,v96
v_mfma_f32_16x16x32_bf16 v[120:123],a[172:175],v[248:251],v[120:123]
s_add_u32 s75,s75,16384
ds_read_b128 a[136:139],v1 offset:512
v_accvgpr_read_b32 v16,a32
v_mfma_f32_16x16x32_bf16 v[124:127],a[188:191],v[248:251],v[44:47]
v_exp_f32 v97,v97
v_mfma_f32_16x16x32_bf16 v[124:127],a[184:187],v[240:243],v[124:127]
ds_read_b128 a[140:143],v1 offset:768
v_cvt_pk_bf16_f32 v96,v96,v97
v_mfma_f32_16x16x32_bf16 v[124:127],a[180:183],v[232:235],v[124:127]
v_exp_f32 v98,v98
v_mfma_f32_16x16x32_bf16 v[124:127],a[176:179],v[224:227],v[124:127]
ds_read_b128 a[156:159],v1 offset:4864
v_accvgpr_read_b32 v17,a33
v_mfma_f32_16x16x32_bf16 v[132:135],a[192:195],v[80:83],0
global_load_lds_dwordx4 v5,s[10:11] offset:3072
v_accvgpr_read_b32 v18,a34
v_mfma_f32_16x16x32_bf16 v[132:135],a[196:199],v[88:91],v[132:135]
ds_read_b128 a[152:155],v1 offset:4608
v_accvgpr_read_b32 v19,a35
v_mfma_f32_16x16x32_bf16 v[140:143],a[200:203],v[80:83],0
v_exp_f32 v99,v99
v_mfma_f32_16x16x32_bf16 v[140:143],a[204:207],v[88:91],v[140:143]
ds_read_b128 a[148:151],v1 offset:4352
v_cvt_pk_bf16_f32 v97,v98,v99
v_mfma_f32_16x16x32_bf16 v[148:151],a[208:211],v[80:83],0
v_exp_f32 v100,v100
v_mfma_f32_16x16x32_bf16 v[148:151],a[212:215],v[88:91],v[148:151]
ds_read_b128 a[144:147],v1 offset:4096
v_accvgpr_read_b32 v20,a36
v_mfma_f32_16x16x32_bf16 v[156:159],a[216:219],v[80:83],0
v_exp_f32 v101,v101
v_mfma_f32_16x16x32_bf16 v[156:159],a[220:223],v[88:91],v[156:159]
ds_read_b128 a[160:163],v1 offset:8192
v_cvt_pk_bf16_f32 v98,v100,v101
v_mfma_f32_16x16x32_bf16 v[164:167],a[224:227],v[80:83],0
v_exp_f32 v102,v102
v_mfma_f32_16x16x32_bf16 v[164:167],a[228:231],v[88:91],v[164:167]
ds_read_b128 a[164:167],v1 offset:8448
v_accvgpr_read_b32 v21,a37
v_mfma_f32_16x16x32_bf16 v[172:175],a[232:235],v[80:83],0
s_add_u32 s10,s20,s74
s_addc_u32 s11,s21,0
v_accvgpr_read_b32 v22,a38
v_mfma_f32_16x16x32_bf16 v[172:175],a[236:239],v[88:91],v[172:175]
ds_read_b128 a[168:171],v1 offset:8704
v_accvgpr_read_b32 v23,a39
v_mfma_f32_16x16x32_bf16 v[180:183],a[240:243],v[80:83],0
s_add_u32 m0,s76,0
v_exp_f32 v103,v103
v_mfma_f32_16x16x32_bf16 v[180:183],a[244:247],v[88:91],v[180:183]
ds_read_b128 a[172:175],v1 offset:8960
v_cvt_pk_bf16_f32 v99,v102,v103
v_mfma_f32_16x16x32_bf16 v[188:191],a[248:251],v[80:83],0
global_load_lds_dwordx4 v4,s[10:11] nt
v_mul_f32 v16,v16,v25
v_mfma_f32_16x16x32_bf16 v[188:191],a[252:255],v[88:91],v[188:191]
ds_read_b128 a[188:191],v1 offset:13056
v_mul_f32 v17,v17,v25
v_mfma_f32_16x16x32_bf16 v[52:55],v[28:31],v[80:83],0
v_exp_f32 v104,v104
v_mfma_f32_16x16x32_bf16 v[52:55],v[28:31],v[88:91],v[52:55]
)A"
R"A(s_waitcnt lgkmcnt(3)
ds_read_b128 a[184:187],v1 offset:12800
v_mul_f32 v18,v18,v25
v_mfma_f32_16x16x32_bf16 v[64:67],a[128:131],v[128:131],v[32:35]
v_exp_f32 v105,v105
v_mfma_f32_16x16x32_bf16 v[64:67],a[132:135],v[136:139],v[64:67]
ds_read_b128 a[180:183],v1 offset:12544
v_cvt_pk_bf16_f32 v104,v104,v105
v_mfma_f32_16x16x32_bf16 v[64:67],a[136:139],v[144:147],v[64:67]
v_exp_f32 v106,v106
v_mfma_f32_16x16x32_bf16 v[64:67],a[140:143],v[152:155],v[64:67]
ds_read_b128 a[176:179],v1 offset:12288
v_mul_f32 v19,v19,v25
v_mfma_f32_16x16x32_bf16 v[68:71],a[156:159],v[152:155],v[32:35]
v_exp_f32 v107,v107
v_mfma_f32_16x16x32_bf16 v[68:71],a[152:155],v[144:147],v[68:71]
v_exp_f32 v108,v108
v_mfma_f32_16x16x32_bf16 v[68:71],a[148:151],v[136:139],v[68:71]
v_exp_f32 v109,v109
v_mfma_f32_16x16x32_bf16 v[68:71],a[144:147],v[128:131],v[68:71]
v_exp_f32 v110,v110
v_mfma_f32_16x16x32_bf16 v[72:75],a[160:163],v[128:131],v[32:35]
v_exp_f32 v111,v111
v_mfma_f32_16x16x32_bf16 v[72:75],a[164:167],v[136:139],v[72:75]
s_waitcnt lgkmcnt(1)
v_cvt_pk_bf16_f32 v105,v106,v107
v_cvt_pk_bf16_f32 v106,v108,v109
v_mfma_f32_16x16x32_bf16 v[72:75],a[168:171],v[144:147],v[72:75]
global_load_lds_dwordx4 v4,s[10:11] offset:1024 nt
v_cvt_pk_bf16_f32 v107,v110,v111
v_mfma_f32_16x16x32_bf16 v[72:75],a[172:175],v[152:155],v[72:75]
v_exp_f32 v112,v112
v_mfma_f32_16x16x32_bf16 v[76:79],a[188:191],v[152:155],v[32:35]
v_exp_f32 v113,v113
v_mfma_f32_16x16x32_bf16 v[76:79],a[184:187],v[144:147],v[76:79]
v_exp_f32 v114,v114
v_mfma_f32_16x16x32_bf16 v[76:79],a[180:183],v[136:139],v[76:79]
s_waitcnt lgkmcnt(0)
v_exp_f32 v115,v115
v_mfma_f32_16x16x32_bf16 v[76:79],a[176:179],v[128:131],v[76:79]
v_exp_f32 v116,v116
v_mfma_f32_16x16x32_bf16 v[196:199],a[192:195],v[96:99],0
v_exp_f32 v117,v117
v_mfma_f32_16x16x32_bf16 v[196:199],a[196:199],v[104:107],v[196:199]
v_exp_f32 v118,v118
v_mfma_f32_16x16x32_bf16 v[204:207],a[200:203],v[96:99],0
v_exp_f32 v119,v119
v_mfma_f32_16x16x32_bf16 v[204:207],a[204:207],v[104:107],v[204:207]
v_cvt_pk_bf16_f32 v112,v112,v113
v_cvt_pk_bf16_f32 v113,v114,v115
v_mfma_f32_16x16x32_bf16 v[212:215],a[208:211],v[96:99],0
v_cvt_pk_bf16_f32 v114,v116,v117
v_cvt_pk_bf16_f32 v115,v118,v119
v_mfma_f32_16x16x32_bf16 v[212:215],a[212:215],v[104:107],v[212:215]
v_exp_f32 v120,v120
v_mfma_f32_16x16x32_bf16 v[220:223],a[216:219],v[96:99],0
v_exp_f32 v121,v121
v_mfma_f32_16x16x32_bf16 v[220:223],a[220:223],v[104:107],v[220:223]
v_exp_f32 v122,v122
v_mfma_f32_16x16x32_bf16 v[228:231],a[224:227],v[96:99],0
global_load_lds_dwordx4 v5,s[10:11] offset:2048 nt
v_cvt_pk_bf16_f32 v120,v120,v121
v_mfma_f32_16x16x32_bf16 v[228:231],a[228:231],v[104:107],v[228:231]
v_exp_f32 v123,v123
v_mfma_f32_16x16x32_bf16 v[236:239],a[232:235],v[96:99],0
v_exp_f32 v124,v124
v_mfma_f32_16x16x32_bf16 v[236:239],a[236:239],v[104:107],v[236:239]
v_exp_f32 v125,v125
v_mfma_f32_16x16x32_bf16 v[244:247],a[240:243],v[96:99],0
v_exp_f32 v126,v126
v_mfma_f32_16x16x32_bf16 v[244:247],a[244:247],v[104:107],v[244:247]
v_exp_f32 v127,v127
v_mfma_f32_16x16x32_bf16 v[252:255],a[248:251],v[96:99],0
v_cvt_pk_bf16_f32 v121,v122,v123
v_cvt_pk_bf16_f32 v122,v124,v125
v_mfma_f32_16x16x32_bf16 v[252:255],a[252:255],v[104:107],v[252:255]
v_cvt_pk_bf16_f32 v123,v126,v127
v_mul_f32 v20,v20,v25
v_mfma_f32_16x16x32_bf16 v[56:59],v[28:31],v[96:99],0
v_mul_f32 v21,v21,v25
v_mul_f32 v22,v22,v25
v_mfma_f32_16x16x32_bf16 v[56:59],v[28:31],v[104:107],v[56:59]
v_mul_f32 v23,v23,v25
v_cvt_pk_bf16_f32 v16,v16,v17
v_mfma_f32_16x16x32_bf16 v[80:83],a[128:131],v[160:163],v[36:39]
v_cvt_pk_bf16_f32 v17,v18,v19
v_cvt_pk_bf16_f32 v18,v20,v21
v_mfma_f32_16x16x32_bf16 v[80:83],a[132:135],v[168:171],v[80:83]
v_cvt_pk_bf16_f32 v19,v22,v23
v_accvgpr_read_b32 v20,a44
v_mfma_f32_16x16x32_bf16 v[80:83],a[136:139],v[176:179],v[80:83]
ds_write_b128 v7,v[16:19] offset:2048
v_mfma_f32_16x16x32_bf16 v[80:83],a[140:143],v[184:187],v[80:83]
v_accvgpr_read_b32 v21,a45
v_accvgpr_read_b32 v22,a46
v_mfma_f32_16x16x32_bf16 v[84:87],a[156:159],v[184:187],v[36:39]
v_accvgpr_read_b32 v16,a40
v_accvgpr_read_b32 v17,a41
v_mfma_f32_16x16x32_bf16 v[84:87],a[152:155],v[176:179],v[84:87]
global_load_lds_dwordx4 v5,s[10:11] offset:3072
v_accvgpr_read_b32 v18,a42
v_mfma_f32_16x16x32_bf16 v[84:87],a[148:151],v[168:171],v[84:87]
v_accvgpr_read_b32 v19,a43
v_accvgpr_read_b32 v23,a47
v_mfma_f32_16x16x32_bf16 v[84:87],a[144:147],v[160:163],v[84:87]
v_accvgpr_read_b32 v8,a48
v_accvgpr_read_b32 v9,a49
v_mfma_f32_16x16x32_bf16 v[88:91],a[160:163],v[160:163],v[36:39]
v_accvgpr_read_b32 v10,a50
v_accvgpr_read_b32 v11,a51
v_mfma_f32_16x16x32_bf16 v[88:91],a[164:167],v[168:171],v[88:91]
v_mul_f32 v16,v16,v25
v_mul_f32 v17,v17,v25
v_mfma_f32_16x16x32_bf16 v[88:91],a[168:171],v[176:179],v[88:91]
v_mul_f32 v18,v18,v25
v_mul_f32 v19,v19,v25
v_mfma_f32_16x16x32_bf16 v[88:91],a[172:175],v[184:187],v[88:91]
v_mul_f32 v20,v20,v25
v_mul_f32 v21,v21,v25
v_mfma_f32_16x16x32_bf16 v[92:95],a[188:191],v[184:187],v[36:39]
v_mul_f32 v22,v22,v25
v_mul_f32 v23,v23,v25
v_mfma_f32_16x16x32_bf16 v[92:95],a[184:187],v[176:179],v[92:95]
v_cvt_pk_bf16_f32 v16,v16,v17
v_cvt_pk_bf16_f32 v17,v18,v19
v_mfma_f32_16x16x32_bf16 v[92:95],a[180:183],v[168:171],v[92:95]
v_cvt_pk_bf16_f32 v18,v20,v21
v_cvt_pk_bf16_f32 v19,v22,v23
v_mfma_f32_16x16x32_bf16 v[92:95],a[176:179],v[160:163],v[92:95]
s_add_u32 s10,s24,s74
s_addc_u32 s11,s25,0
v_accvgpr_read_b32 v12,a52
v_mfma_f32_16x16x32_bf16 a[32:35],a[192:195],v[112:115],0
ds_write_b128 v7,v[16:19] offset:3072
v_mfma_f32_16x16x32_bf16 a[32:35],a[196:199],v[120:123],a[32:35]
s_add_u32 m0,s76,16384
v_accvgpr_read_b32 v13,a53
v_accvgpr_read_b32 v14,a54
v_mfma_f32_16x16x32_bf16 a[36:39],a[200:203],v[112:115],0
v_accvgpr_read_b32 v15,a55
v_accvgpr_read_b32 v16,a56
v_mfma_f32_16x16x32_bf16 a[36:39],a[204:207],v[120:123],a[36:39]
global_load_lds_dwordx4 v4,s[10:11]
v_accvgpr_read_b32 v17,a57
v_mfma_f32_16x16x32_bf16 a[40:43],a[208:211],v[112:115],0
v_accvgpr_read_b32 v18,a58
v_accvgpr_read_b32 v19,a59
v_mfma_f32_16x16x32_bf16 a[40:43],a[212:215],v[120:123],a[40:43]
v_accvgpr_read_b32 v20,a60
v_accvgpr_read_b32 v21,a61
v_mfma_f32_16x16x32_bf16 a[44:47],a[216:219],v[112:115],0
v_accvgpr_read_b32 v22,a62
v_accvgpr_read_b32 v23,a63
v_mfma_f32_16x16x32_bf16 a[44:47],a[220:223],v[120:123],a[44:47]
v_add_u32 v2,s78,v2
v_add_u32 v3,s78,v3
v_mfma_f32_16x16x32_bf16 a[48:51],a[224:227],v[112:115],0
s_sub_u32 s78,s77,32768
ds_read_b64_tr_b16 a[192:193],v2
ds_read_b64_tr_b16 a[194:195],v2 offset:4096
v_mfma_f32_16x16x32_bf16 a[48:51],a[228:231],v[120:123],a[48:51]
s_and_b32 s78,s78,0x1ffff
ds_read_b64_tr_b16 a[196:197],v2 offset:8192
v_mul_f32 v8,v8,v25
v_mfma_f32_16x16x32_bf16 a[52:55],a[232:235],v[112:115],0
s_sub_u32 s78,s78,s77
ds_read_b64_tr_b16 a[198:199],v2 offset:12288
v_mul_f32 v9,v9,v25
v_mfma_f32_16x16x32_bf16 a[52:55],a[236:239],v[120:123],a[52:55]
s_add_u32 s77,s77,s78
ds_read_b64_tr_b16 a[200:201],v3
v_mul_f32 v10,v10,v25
v_mfma_f32_16x16x32_bf16 a[56:59],a[240:243],v[112:115],0
ds_read_b64_tr_b16 a[202:203],v3 offset:4096
ds_read_b64_tr_b16 a[204:205],v3 offset:8192
v_mfma_f32_16x16x32_bf16 a[56:59],a[244:247],v[120:123],a[56:59]
ds_read_b64_tr_b16 a[206:207],v3 offset:12288
v_mul_f32 v11,v11,v25
v_mfma_f32_16x16x32_bf16 a[60:63],a[248:251],v[112:115],0
ds_read_b64_tr_b16 a[208:209],v2 offset:256
v_mul_f32 v12,v12,v25
v_mfma_f32_16x16x32_bf16 a[60:63],a[252:255],v[120:123],a[60:63]
ds_read_b64_tr_b16 a[210:211],v2 offset:4352
v_mul_f32 v13,v13,v25
v_mfma_f32_16x16x32_bf16 v[60:63],v[28:31],v[112:115],0
ds_read_b64_tr_b16 a[212:213],v2 offset:8448
ds_read_b64_tr_b16 a[214:215],v2 offset:12544
v_mfma_f32_16x16x32_bf16 v[60:63],v[28:31],v[120:123],v[60:63]
global_load_lds_dwordx4 v5,s[10:11] offset:1024
ds_read_b64_tr_b16 a[216:217],v3 offset:256
v_mfma_f32_16x16x32_bf16 v[96:99],a[128:131],v[192:195],v[40:43]
v_exp_f32 v64,v64
v_mfma_f32_16x16x32_bf16 v[96:99],a[132:135],v[200:203],v[96:99]
v_exp_f32 v65,v65
v_mfma_f32_16x16x32_bf16 v[96:99],a[136:139],v[208:211],v[96:99]
v_exp_f32 v66,v66
v_mfma_f32_16x16x32_bf16 v[96:99],a[140:143],v[216:219],v[96:99]
v_exp_f32 v67,v67
v_mfma_f32_16x16x32_bf16 v[100:103],a[156:159],v[216:219],v[40:43]
v_exp_f32 v68,v68
v_mfma_f32_16x16x32_bf16 v[100:103],a[152:155],v[208:211],v[100:103]
v_exp_f32 v69,v69
v_mfma_f32_16x16x32_bf16 v[100:103],a[148:151],v[200:203],v[100:103]
v_exp_f32 v70,v70
v_mfma_f32_16x16x32_bf16 v[100:103],a[144:147],v[192:195],v[100:103]
v_exp_f32 v71,v71
v_mfma_f32_16x16x32_bf16 v[104:107],a[160:163],v[192:195],v[40:43]
L_pd8_%=:
s_cbranch_execz L_trt0_%=
ds_read_b64_tr_b16 a[218:219],v3 offset:4352
v_cvt_pk_bf16_f32 v64,v64,v65
v_mfma_f32_16x16x32_bf16 v[104:107],a[164:167],v[200:203],v[104:107]
v_cvt_pk_bf16_f32 v65,v66,v67
v_cvt_pk_bf16_f32 v66,v68,v69
v_mfma_f32_16x16x32_bf16 v[104:107],a[168:171],v[208:211],v[104:107]
ds_read_b64_tr_b16 a[220:221],v3 offset:8448
v_cvt_pk_bf16_f32 v67,v70,v71
v_mfma_f32_16x16x32_bf16 v[104:107],a[172:175],v[216:219],v[104:107]
v_exp_f32 v72,v72
v_mfma_f32_16x16x32_bf16 v[108:111],a[188:191],v[216:219],v[40:43]
v_exp_f32 v73,v73
v_mfma_f32_16x16x32_bf16 v[108:111],a[184:187],v[208:211],v[108:111]
global_load_lds_dwordx4 v4,s[10:11] offset:2048
v_exp_f32 v74,v74
v_exp_f32 v75,v75
v_exp_f32 v76,v76
v_exp_f32 v77,v77
v_exp_f32 v78,v78
v_exp_f32 v79,v79
v_mfma_f32_16x16x32_bf16 v[108:111],a[180:183],v[200:203],v[108:111]
ds_read_b64_tr_b16 a[222:223],v3 offset:12544
v_cvt_pk_bf16_f32 v72,v72,v73
v_cvt_pk_bf16_f32 v73,v74,v75
v_cvt_pk_bf16_f32 v74,v76,v77
v_cvt_pk_bf16_f32 v75,v78,v79
v_mfma_f32_16x16x32_bf16 v[108:111],a[176:179],v[192:195],v[108:111]
s_waitcnt lgkmcnt(3)
ds_read_b64_tr_b16 a[224:225],v2 offset:512
ds_read_b64_tr_b16 a[226:227],v2 offset:4608
v_mfma_f32_16x16x32_bf16 a[0:3],a[192:195],v[64:67],a[0:3]
ds_read_b64_tr_b16 a[228:229],v2 offset:8704
v_mul_f32 v14,v14,v25
v_mfma_f32_16x16x32_bf16 a[0:3],a[196:199],v[72:75],a[0:3]
ds_read_b64_tr_b16 a[230:231],v2 offset:12800
v_mul_f32 v15,v15,v25
v_mfma_f32_16x16x32_bf16 a[4:7],a[200:203],v[64:67],a[4:7]
ds_read_b64_tr_b16 a[232:233],v3 offset:512
v_cvt_pk_bf16_f32 v8,v8,v9
v_mfma_f32_16x16x32_bf16 a[4:7],a[204:207],v[72:75],a[4:7]
ds_read_b64_tr_b16 a[234:235],v3 offset:4608
ds_read_b64_tr_b16 a[236:237],v3 offset:8704
v_mfma_f32_16x16x32_bf16 a[8:11],a[208:211],v[64:67],a[8:11]
ds_read_b64_tr_b16 a[238:239],v3 offset:12800
v_cvt_pk_bf16_f32 v9,v10,v11
v_mfma_f32_16x16x32_bf16 a[8:11],a[212:215],v[72:75],a[8:11]
s_waitcnt lgkmcnt(9)
ds_read_b64_tr_b16 a[240:241],v2 offset:768
v_cvt_pk_bf16_f32 v10,v12,v13
v_mfma_f32_16x16x32_bf16 a[12:15],a[216:219],v[64:67],a[12:15]
s_waitcnt lgkmcnt(9)
ds_read_b64_tr_b16 a[242:243],v2 offset:4864
v_cvt_pk_bf16_f32 v11,v14,v15
v_mfma_f32_16x16x32_bf16 a[12:15],a[220:223],v[72:75],a[12:15]
s_waitcnt lgkmcnt(8)
ds_read_b64_tr_b16 a[244:245],v2 offset:8960
ds_read_b64_tr_b16 a[246:247],v2 offset:13056
v_mfma_f32_16x16x32_bf16 a[16:19],a[224:227],v[64:67],a[16:19]
s_waitcnt lgkmcnt(8)
ds_read_b64_tr_b16 a[248:249],v3 offset:768
v_mul_f32 v16,v16,v25
v_mfma_f32_16x16x32_bf16 a[16:19],a[228:231],v[72:75],a[16:19]
s_waitcnt lgkmcnt(1)
ds_read_b64_tr_b16 a[250:251],v3 offset:4864
v_mul_f32 v17,v17,v25
v_mfma_f32_16x16x32_bf16 a[20:23],a[232:235],v[64:67],a[20:23]
s_getreg_b32 s92,hwreg(HW_REG_IB_STS,0,4)
s_min_u32 s4,s92,s6
ds_read_b64_tr_b16 a[252:253],v3 offset:8960
v_mfma_f32_16x16x32_bf16 a[20:23],a[236:239],v[72:75],a[20:23]
s_mov_b32 s6,s92
ds_read_b64_tr_b16 a[254:255],v3 offset:13056
v_mul_f32 v18,v18,v25
v_mfma_f32_16x16x32_bf16 a[24:27],a[240:243],v[64:67],a[24:27]
s_cmp_gt_u32 s4,s9
s_cselect_b32 s4,15,s5
v_mul_f32 v19,v19,v25
v_mfma_f32_16x16x32_bf16 a[24:27],a[244:247],v[72:75],a[24:27]
s_waitcnt lgkmcnt(2)
v_mul_f32 v20,v20,v25
v_mul_f32 v21,v21,v25
v_mfma_f32_16x16x32_bf16 a[28:31],a[248:251],v[64:67],a[28:31]
s_waitcnt lgkmcnt(0)
v_mul_f32 v22,v22,v25
v_mul_f32 v23,v23,v25
v_mfma_f32_16x16x32_bf16 a[28:31],a[252:255],v[72:75],a[28:31]
s_getpc_b64 s[92:93]
s_cmp_lg_u32 s4,0
s_cbranch_scc1 L_pace_%=
v_mfma_f32_16x16x32_bf16 v[48:51],v[28:31],v[64:67],v[48:51]
)A"
R"A(v_cvt_pk_bf16_f32 v12,v16,v17
v_cvt_pk_bf16_f32 v13,v18,v19
v_mfma_f32_16x16x32_bf16 v[48:51],v[28:31],v[72:75],v[48:51]
s_add_u32 s10,s24,s74
s_addc_u32 s11,s25,0
v_cvt_pk_bf16_f32 v14,v20,v21
s_waitcnt vmcnt(15)
s_barrier
v_mfma_f32_16x16x32_bf16 v[112:115],a[128:131],v[224:227],v[44:47]
s_add_u32 m0,s76,16384
v_cvt_pk_bf16_f32 v15,v22,v23
v_accvgpr_read_b32 v16,a112
v_mfma_f32_16x16x32_bf16 v[112:115],a[132:135],v[232:235],v[112:115]
s_add_u32 s74,s74,16384
v_accvgpr_read_b32 v17,a113
v_accvgpr_read_b32 v18,a114
v_mfma_f32_16x16x32_bf16 v[112:115],a[136:139],v[240:243],v[112:115]
global_load_lds_dwordx4 v5,s[10:11] offset:3072
v_accvgpr_read_b32 v19,a115
v_mfma_f32_16x16x32_bf16 v[112:115],a[140:143],v[248:251],v[112:115]
s_and_b32 s74,s74,0x3ffff
v_accvgpr_read_b32 v20,a116
v_accvgpr_read_b32 v21,a117
v_mfma_f32_16x16x32_bf16 v[116:119],a[156:159],v[248:251],v[44:47]
s_or_b32 s74,s74,s69
v_accvgpr_read_b32 v22,a118
v_accvgpr_read_b32 v23,a119
v_mfma_f32_16x16x32_bf16 v[116:119],a[152:155],v[240:243],v[116:119]
s_sub_u32 s76,s76,32768
v_mul_f32 v16,v16,v27
v_mul_f32 v17,v17,v27
v_mfma_f32_16x16x32_bf16 v[116:119],a[148:151],v[232:235],v[116:119]
s_and_b32 s76,s76,0x1ffff
v_mul_f32 v18,v18,v27
v_mul_f32 v19,v19,v27
v_mfma_f32_16x16x32_bf16 v[116:119],a[144:147],v[224:227],v[116:119]
s_and_b32 s69,s75,0xfffc0000
v_mul_f32 v20,v20,v27
v_mul_f32 v21,v21,v27
v_mfma_f32_16x16x32_bf16 v[120:123],a[160:163],v[224:227],v[44:47]
s_add_u32 s75,s75,16384
v_mul_f32 v22,v22,v27
v_mul_f32 v23,v23,v27
v_mfma_f32_16x16x32_bf16 v[120:123],a[164:167],v[232:235],v[120:123]
v_cvt_pk_bf16_f32 v16,v16,v17
v_cvt_pk_bf16_f32 v17,v18,v19
v_mfma_f32_16x16x32_bf16 v[120:123],a[168:171],v[240:243],v[120:123]
v_cvt_pk_bf16_f32 v18,v20,v21
v_cvt_pk_bf16_f32 v19,v22,v23
v_mfma_f32_16x16x32_bf16 v[120:123],a[172:175],v[248:251],v[120:123]
v_accvgpr_write_b32 a112,v16
v_accvgpr_write_b32 a113,v17
v_mfma_f32_16x16x32_bf16 v[124:127],a[188:191],v[248:251],v[44:47]
v_accvgpr_write_b32 a114,v18
v_accvgpr_write_b32 a115,v19
v_accvgpr_write_b32 a116,v8
v_accvgpr_write_b32 a117,v9
v_accvgpr_write_b32 a118,v10
v_accvgpr_write_b32 a119,v11
v_exp_f32 v80,v80
v_exp_f32 v81,v81
v_exp_f32 v82,v82
v_exp_f32 v83,v83
v_exp_f32 v84,v84
v_exp_f32 v85,v85
v_exp_f32 v86,v86
v_exp_f32 v87,v87
v_mfma_f32_16x16x32_bf16 v[124:127],a[184:187],v[240:243],v[124:127]
v_cvt_pk_bf16_f32 v80,v80,v81
v_cvt_pk_bf16_f32 v81,v82,v83
v_cvt_pk_bf16_f32 v82,v84,v85
v_cvt_pk_bf16_f32 v83,v86,v87
v_exp_f32 v88,v88
v_exp_f32 v89,v89
v_exp_f32 v90,v90
v_exp_f32 v91,v91
v_exp_f32 v92,v92
v_exp_f32 v93,v93
v_exp_f32 v94,v94
v_exp_f32 v95,v95
v_mfma_f32_16x16x32_bf16 v[124:127],a[180:183],v[232:235],v[124:127]
v_cvt_pk_bf16_f32 v88,v88,v89
v_cvt_pk_bf16_f32 v89,v90,v91
v_cvt_pk_bf16_f32 v90,v92,v93
v_cvt_pk_bf16_f32 v91,v94,v95
v_mfma_f32_16x16x32_bf16 v[124:127],a[176:179],v[224:227],v[124:127]
s_add_u32 s10,s20,s74
s_addc_u32 s11,s21,0
s_add_u32 m0,s76,0
v_mfma_f32_16x16x32_bf16 v[132:135],a[192:195],v[80:83],v[132:135]
global_load_lds_dwordx4 v4,s[10:11] nt
v_add_u32 v1,s78,v1
v_mfma_f32_16x16x32_bf16 v[132:135],a[196:199],v[88:91],v[132:135]
ds_read_b128 a[128:131],v1
v_accvgpr_read_b32 v16,a120
v_mfma_f32_16x16x32_bf16 v[140:143],a[200:203],v[80:83],v[140:143]
v_exp_f32 v96,v96
v_mfma_f32_16x16x32_bf16 v[140:143],a[204:207],v[88:91],v[140:143]
ds_read_b128 a[132:135],v1 offset:256
v_accvgpr_read_b32 v17,a121
v_mfma_f32_16x16x32_bf16 v[148:151],a[208:211],v[80:83],v[148:151]
v_exp_f32 v97,v97
v_mfma_f32_16x16x32_bf16 v[148:151],a[212:215],v[88:91],v[148:151]
ds_read_b128 a[136:139],v1 offset:512
v_cvt_pk_bf16_f32 v96,v96,v97
v_mfma_f32_16x16x32_bf16 v[156:159],a[216:219],v[80:83],v[156:159]
v_exp_f32 v98,v98
v_mfma_f32_16x16x32_bf16 v[156:159],a[220:223],v[88:91],v[156:159]
ds_read_b128 a[140:143],v1 offset:768
v_accvgpr_read_b32 v18,a122
v_mfma_f32_16x16x32_bf16 v[164:167],a[224:227],v[80:83],v[164:167]
v_exp_f32 v99,v99
v_mfma_f32_16x16x32_bf16 v[164:167],a[228:231],v[88:91],v[164:167]
ds_read_b128 a[156:159],v1 offset:4864
v_cvt_pk_bf16_f32 v97,v98,v99
v_mfma_f32_16x16x32_bf16 v[172:175],a[232:235],v[80:83],v[172:175]
v_exp_f32 v100,v100
v_mfma_f32_16x16x32_bf16 v[172:175],a[236:239],v[88:91],v[172:175]
ds_read_b128 a[152:155],v1 offset:4608
v_accvgpr_read_b32 v19,a123
v_mfma_f32_16x16x32_bf16 v[180:183],a[240:243],v[80:83],v[180:183]
v_exp_f32 v101,v101
v_mfma_f32_16x16x32_bf16 v[180:183],a[244:247],v[88:91],v[180:183]
ds_read_b128 a[148:151],v1 offset:4352
v_cvt_pk_bf16_f32 v98,v100,v101
v_mfma_f32_16x16x32_bf16 v[188:191],a[248:251],v[80:83],v[188:191]
global_load_lds_dwordx4 v4,s[10:11] offset:1024 nt
v_accvgpr_read_b32 v20,a124
v_mfma_f32_16x16x32_bf16 v[188:191],a[252:255],v[88:91],v[188:191]
ds_read_b128 a[144:147],v1 offset:4096
v_accvgpr_read_b32 v21,a125
v_mfma_f32_16x16x32_bf16 v[52:55],v[28:31],v[80:83],v[52:55]
v_exp_f32 v102,v102
v_mfma_f32_16x16x32_bf16 v[52:55],v[28:31],v[88:91],v[52:55]
s_waitcnt lgkmcnt(3)
ds_read_b128 a[160:163],v1 offset:8192
v_accvgpr_read_b32 v22,a126
v_mfma_f32_16x16x32_bf16 v[64:67],a[128:131],v[128:131],v[32:35]
v_exp_f32 v103,v103
v_mfma_f32_16x16x32_bf16 v[64:67],a[132:135],v[136:139],v[64:67]
ds_read_b128 a[164:167],v1 offset:8448
v_cvt_pk_bf16_f32 v99,v102,v103
v_mfma_f32_16x16x32_bf16 v[64:67],a[136:139],v[144:147],v[64:67]
v_exp_f32 v104,v104
v_mfma_f32_16x16x32_bf16 v[64:67],a[140:143],v[152:155],v[64:67]
ds_read_b128 a[168:171],v1 offset:8704
v_accvgpr_read_b32 v23,a127
v_mfma_f32_16x16x32_bf16 v[68:71],a[156:159],v[152:155],v[32:35]
s_waitcnt lgkmcnt(4)
v_exp_f32 v105,v105
v_mfma_f32_16x16x32_bf16 v[68:71],a[152:155],v[144:147],v[68:71]
ds_read_b128 a[172:175],v1 offset:8960
v_cvt_pk_bf16_f32 v104,v104,v105
v_mfma_f32_16x16x32_bf16 v[68:71],a[148:151],v[136:139],v[68:71]
s_waitcnt lgkmcnt(4)
v_exp_f32 v106,v106
v_mfma_f32_16x16x32_bf16 v[68:71],a[144:147],v[128:131],v[68:71]
s_waitcnt lgkmcnt(3)
ds_read_b128 a[188:191],v1 offset:13056
v_mul_f32 v16,v16,v27
v_mfma_f32_16x16x32_bf16 v[72:75],a[160:163],v[128:131],v[32:35]
s_waitcnt lgkmcnt(3)
v_exp_f32 v107,v107
v_mfma_f32_16x16x32_bf16 v[72:75],a[164:167],v[136:139],v[72:75]
s_waitcnt lgkmcnt(1)
ds_read_b128 a[184:187],v1 offset:12800
v_cvt_pk_bf16_f32 v105,v106,v107
v_mfma_f32_16x16x32_bf16 v[72:75],a[168:171],v[144:147],v[72:75]
global_load_lds_dwordx4 v5,s[10:11] offset:2048 nt
ds_read_b128 a[180:183],v1 offset:12544
v_mfma_f32_16x16x32_bf16 v[72:75],a[172:175],v[152:155],v[72:75]
s_waitcnt lgkmcnt(2)
ds_read_b128 a[176:179],v1 offset:12288
v_mul_f32 v17,v17,v27
v_mfma_f32_16x16x32_bf16 v[76:79],a[188:191],v[152:155],v[32:35]
s_waitcnt lgkmcnt(2)
v_exp_f32 v108,v108
v_mfma_f32_16x16x32_bf16 v[76:79],a[184:187],v[144:147],v[76:79]
s_waitcnt lgkmcnt(1)
v_exp_f32 v109,v109
v_exp_f32 v110,v110
v_exp_f32 v111,v111
v_mfma_f32_16x16x32_bf16 v[76:79],a[180:183],v[136:139],v[76:79]
s_waitcnt lgkmcnt(0)
v_cvt_pk_bf16_f32 v106,v108,v109
v_cvt_pk_bf16_f32 v107,v110,v111
v_mfma_f32_16x16x32_bf16 v[76:79],a[176:179],v[128:131],v[76:79]
v_mul_f32 v18,v18,v27
v_mul_f32 v19,v19,v27
v_mfma_f32_16x16x32_bf16 v[196:199],a[192:195],v[96:99],v[196:199]
v_mul_f32 v20,v20,v27
v_mul_f32 v21,v21,v27
v_mfma_f32_16x16x32_bf16 v[196:199],a[196:199],v[104:107],v[196:199]
v_mul_f32 v22,v22,v27
v_mul_f32 v23,v23,v27
v_mfma_f32_16x16x32_bf16 v[204:207],a[200:203],v[96:99],v[204:207]
v_cvt_pk_bf16_f32 v16,v16,v17
v_cvt_pk_bf16_f32 v17,v18,v19
v_mfma_f32_16x16x32_bf16 v[204:207],a[204:207],v[104:107],v[204:207]
v_cvt_pk_bf16_f32 v18,v20,v21
v_cvt_pk_bf16_f32 v19,v22,v23
v_mfma_f32_16x16x32_bf16 v[212:215],a[208:211],v[96:99],v[212:215]
v_accvgpr_write_b32 a120,v16
v_accvgpr_write_b32 a121,v17
v_mfma_f32_16x16x32_bf16 v[212:215],a[212:215],v[104:107],v[212:215]
v_accvgpr_write_b32 a122,v18
v_accvgpr_write_b32 a123,v19
v_mfma_f32_16x16x32_bf16 v[220:223],a[216:219],v[96:99],v[220:223]
v_accvgpr_write_b32 a124,v12
v_accvgpr_write_b32 a125,v13
v_mfma_f32_16x16x32_bf16 v[220:223],a[220:223],v[104:107],v[220:223]
v_accvgpr_write_b32 a126,v14
v_accvgpr_write_b32 a127,v15
v_mfma_f32_16x16x32_bf16 v[228:231],a[224:227],v[96:99],v[228:231]
global_load_lds_dwordx4 v5,s[10:11] offset:3072
v_add_u32 v2,s78,v2
v_mfma_f32_16x16x32_bf16 v[228:231],a[228:231],v[104:107],v[228:231]
v_exp_f32 v112,v112
v_mfma_f32_16x16x32_bf16 v[236:239],a[232:235],v[96:99],v[236:239]
v_exp_f32 v113,v113
v_mfma_f32_16x16x32_bf16 v[236:239],a[236:239],v[104:107],v[236:239]
v_exp_f32 v114,v114
v_mfma_f32_16x16x32_bf16 v[244:247],a[240:243],v[96:99],v[244:247]
v_exp_f32 v115,v115
v_mfma_f32_16x16x32_bf16 v[244:247],a[244:247],v[104:107],v[244:247]
v_exp_f32 v116,v116
v_mfma_f32_16x16x32_bf16 v[252:255],a[248:251],v[96:99],v[252:255]
v_exp_f32 v117,v117
v_mfma_f32_16x16x32_bf16 v[252:255],a[252:255],v[104:107],v[252:255]
v_exp_f32 v118,v118
v_mfma_f32_16x16x32_bf16 v[56:59],v[28:31],v[96:99],v[56:59]
v_exp_f32 v119,v119
v_mfma_f32_16x16x32_bf16 v[56:59],v[28:31],v[104:107],v[56:59]
v_cvt_pk_bf16_f32 v112,v112,v113
v_cvt_pk_bf16_f32 v113,v114,v115
v_mfma_f32_16x16x32_bf16 v[80:83],a[128:131],v[160:163],v[36:39]
v_cvt_pk_bf16_f32 v114,v116,v117
v_cvt_pk_bf16_f32 v115,v118,v119
v_mfma_f32_16x16x32_bf16 v[80:83],a[132:135],v[168:171],v[80:83]
s_add_u32 s10,s24,s74
s_addc_u32 s11,s25,0
v_add_u32 v3,s78,v3
v_mfma_f32_16x16x32_bf16 v[80:83],a[136:139],v[176:179],v[80:83]
s_add_u32 s74,s74,16384
v_exp_f32 v120,v120
v_mfma_f32_16x16x32_bf16 v[80:83],a[140:143],v[184:187],v[80:83]
s_add_u32 m0,s76,16384
v_exp_f32 v121,v121
v_mfma_f32_16x16x32_bf16 v[84:87],a[156:159],v[184:187],v[36:39]
s_and_b32 s74,s74,0x3ffff
v_exp_f32 v122,v122
v_mfma_f32_16x16x32_bf16 v[84:87],a[152:155],v[176:179],v[84:87]
global_load_lds_dwordx4 v4,s[10:11]
v_cvt_pk_bf16_f32 v120,v120,v121
v_mfma_f32_16x16x32_bf16 v[84:87],a[148:151],v[168:171],v[84:87]
s_or_b32 s74,s74,s69
v_exp_f32 v123,v123
v_mfma_f32_16x16x32_bf16 v[84:87],a[144:147],v[160:163],v[84:87]
s_sub_u32 s76,s76,32768
v_exp_f32 v124,v124
v_mfma_f32_16x16x32_bf16 v[88:91],a[160:163],v[160:163],v[36:39]
s_and_b32 s76,s76,0x1ffff
v_exp_f32 v125,v125
v_mfma_f32_16x16x32_bf16 v[88:91],a[164:167],v[168:171],v[88:91]
s_sub_u32 s78,s77,32768
v_exp_f32 v126,v126
v_mfma_f32_16x16x32_bf16 v[88:91],a[168:171],v[176:179],v[88:91]
s_and_b32 s78,s78,0x1ffff
v_exp_f32 v127,v127
v_mfma_f32_16x16x32_bf16 v[88:91],a[172:175],v[184:187],v[88:91]
s_sub_u32 s78,s78,s77
v_cvt_pk_bf16_f32 v121,v122,v123
v_cvt_pk_bf16_f32 v122,v124,v125
v_mfma_f32_16x16x32_bf16 v[92:95],a[188:191],v[184:187],v[36:39]
s_and_b32 s69,s75,0xfffc0000
v_cvt_pk_bf16_f32 v123,v126,v127
v_accvgpr_read_b32 v12,a100
v_mfma_f32_16x16x32_bf16 v[92:95],a[184:187],v[176:179],v[92:95]
s_add_u32 s77,s77,s78
v_exp_f32 v64,v64
v_mfma_f32_16x16x32_bf16 v[92:95],a[180:183],v[168:171],v[92:95]
s_add_u32 s75,s75,16384
v_exp_f32 v65,v65
v_mfma_f32_16x16x32_bf16 v[92:95],a[176:179],v[160:163],v[92:95]
v_exp_f32 v66,v66
v_mfma_f32_16x16x32_bf16 a[32:35],a[192:195],v[112:115],a[32:35]
ds_read_b64_tr_b16 a[192:193],v2
ds_read_b64_tr_b16 a[194:195],v2 offset:4096
v_mfma_f32_16x16x32_bf16 a[32:35],a[196:199],v[120:123],a[32:35]
ds_read_b64_tr_b16 a[196:197],v2 offset:8192
v_cvt_pk_bf16_f32 v64,v64,v65
v_mfma_f32_16x16x32_bf16 a[36:39],a[200:203],v[112:115],a[36:39]
ds_read_b64_tr_b16 a[198:199],v2 offset:12288
v_accvgpr_read_b32 v13,a101
v_mfma_f32_16x16x32_bf16 a[36:39],a[204:207],v[120:123],a[36:39]
global_load_lds_dwordx4 v5,s[10:11] offset:1024
ds_read_b64_tr_b16 a[200:201],v3
v_mfma_f32_16x16x32_bf16 a[40:43],a[208:211],v[112:115],a[40:43]
ds_read_b64_tr_b16 a[202:203],v3 offset:4096
ds_read_b64_tr_b16 a[204:205],v3 offset:8192
v_mfma_f32_16x16x32_bf16 a[40:43],a[212:215],v[120:123],a[40:43]
ds_read_b64_tr_b16 a[206:207],v3 offset:12288
v_accvgpr_read_b32 v14,a102
v_mfma_f32_16x16x32_bf16 a[44:47],a[216:219],v[112:115],a[44:47]
ds_read_b64_tr_b16 a[208:209],v2 offset:256
v_accvgpr_read_b32 v15,a103
v_mfma_f32_16x16x32_bf16 a[44:47],a[220:223],v[120:123],a[44:47]
ds_read_b64_tr_b16 a[210:211],v2 offset:4352
v_accvgpr_read_b32 v8,a96
v_mfma_f32_16x16x32_bf16 a[48:51],a[224:227],v[112:115],a[48:51]
)A"
R"A(ds_read_b64_tr_b16 a[212:213],v2 offset:8448
ds_read_b64_tr_b16 a[214:215],v2 offset:12544
v_mfma_f32_16x16x32_bf16 a[48:51],a[228:231],v[120:123],a[48:51]
v_exp_f32 v67,v67
v_mfma_f32_16x16x32_bf16 a[52:55],a[232:235],v[112:115],a[52:55]
v_exp_f32 v68,v68
v_mfma_f32_16x16x32_bf16 a[52:55],a[236:239],v[120:123],a[52:55]
v_exp_f32 v69,v69
v_mfma_f32_16x16x32_bf16 a[56:59],a[240:243],v[112:115],a[56:59]
v_exp_f32 v70,v70
v_mfma_f32_16x16x32_bf16 a[56:59],a[244:247],v[120:123],a[56:59]
v_exp_f32 v71,v71
v_mfma_f32_16x16x32_bf16 a[60:63],a[248:251],v[112:115],a[60:63]
ds_read_b64_tr_b16 a[216:217],v3 offset:256
ds_read_b64_tr_b16 a[218:219],v3 offset:4352
v_mfma_f32_16x16x32_bf16 a[60:63],a[252:255],v[120:123],a[60:63]
v_cvt_pk_bf16_f32 v65,v66,v67
v_cvt_pk_bf16_f32 v66,v68,v69
v_mfma_f32_16x16x32_bf16 v[60:63],v[28:31],v[112:115],v[60:63]
ds_read_b64_tr_b16 a[220:221],v3 offset:8448
v_cvt_pk_bf16_f32 v67,v70,v71
v_mfma_f32_16x16x32_bf16 v[60:63],v[28:31],v[120:123],v[60:63]
global_load_lds_dwordx4 v4,s[10:11] offset:2048
ds_read_b64_tr_b16 a[222:223],v3 offset:12544
v_mfma_f32_16x16x32_bf16 v[96:99],a[128:131],v[192:195],v[40:43]
v_exp_f32 v72,v72
v_mfma_f32_16x16x32_bf16 v[96:99],a[132:135],v[200:203],v[96:99]
v_exp_f32 v73,v73
v_mfma_f32_16x16x32_bf16 v[96:99],a[136:139],v[208:211],v[96:99]
v_exp_f32 v74,v74
v_mfma_f32_16x16x32_bf16 v[96:99],a[140:143],v[216:219],v[96:99]
v_exp_f32 v75,v75
v_mfma_f32_16x16x32_bf16 v[100:103],a[156:159],v[216:219],v[40:43]
v_exp_f32 v76,v76
v_mfma_f32_16x16x32_bf16 v[100:103],a[152:155],v[208:211],v[100:103]
v_exp_f32 v77,v77
v_mfma_f32_16x16x32_bf16 v[100:103],a[148:151],v[200:203],v[100:103]
v_exp_f32 v78,v78
v_mfma_f32_16x16x32_bf16 v[100:103],a[144:147],v[192:195],v[100:103]
v_exp_f32 v79,v79
v_mfma_f32_16x16x32_bf16 v[104:107],a[160:163],v[192:195],v[40:43]
v_cvt_pk_bf16_f32 v72,v72,v73
v_cvt_pk_bf16_f32 v73,v74,v75
v_mfma_f32_16x16x32_bf16 v[104:107],a[164:167],v[200:203],v[104:107]
v_cvt_pk_bf16_f32 v74,v76,v77
v_cvt_pk_bf16_f32 v75,v78,v79
v_mfma_f32_16x16x32_bf16 v[104:107],a[168:171],v[208:211],v[104:107]
ds_read_b64_tr_b16 a[224:225],v2 offset:512
ds_read_b64_tr_b16 a[226:227],v2 offset:4608
v_mfma_f32_16x16x32_bf16 v[104:107],a[172:175],v[216:219],v[104:107]
v_accvgpr_read_b32 v9,a97
v_accvgpr_read_b32 v10,a98
v_mfma_f32_16x16x32_bf16 v[108:111],a[188:191],v[216:219],v[40:43]
v_accvgpr_read_b32 v11,a99
v_mul_f32 v8,v8,v27
v_mfma_f32_16x16x32_bf16 v[108:111],a[184:187],v[208:211],v[108:111]
global_load_lds_dwordx4 v5,s[10:11] offset:3072
v_mul_f32 v9,v9,v27
v_mfma_f32_16x16x32_bf16 v[108:111],a[180:183],v[200:203],v[108:111]
v_mul_f32 v10,v10,v27
v_mul_f32 v11,v11,v27
v_mfma_f32_16x16x32_bf16 v[108:111],a[176:179],v[192:195],v[108:111]
s_waitcnt lgkmcnt(2)
v_mul_f32 v12,v12,v27
v_mul_f32 v13,v13,v27
v_mul_f32 v14,v14,v27
v_mul_f32 v15,v15,v27
v_cvt_pk_bf16_f32 v8,v8,v9
v_cvt_pk_bf16_f32 v9,v10,v11
v_cvt_pk_bf16_f32 v10,v12,v13
v_cvt_pk_bf16_f32 v11,v14,v15
v_accvgpr_write_b32 a96,v8
v_accvgpr_write_b32 a97,v9
v_accvgpr_write_b32 a98,v10
v_accvgpr_write_b32 a99,v11
v_mfma_f32_16x16x32_bf16 a[0:3],a[192:195],v[64:67],a[0:3]
ds_read_b128 a[100:103],v7 offset:2048
v_add_u32 v1,s78,v1
v_mfma_f32_16x16x32_bf16 a[0:3],a[196:199],v[72:75],a[0:3]
ds_read_b64_tr_b16 a[228:229],v2 offset:8704
v_accvgpr_read_b32 v20,a108
v_mfma_f32_16x16x32_bf16 a[4:7],a[200:203],v[64:67],a[4:7]
ds_read_b64_tr_b16 a[230:231],v2 offset:12800
v_accvgpr_read_b32 v21,a109
v_mfma_f32_16x16x32_bf16 a[4:7],a[204:207],v[72:75],a[4:7]
ds_read_b64_tr_b16 a[232:233],v3 offset:512
v_accvgpr_read_b32 v22,a110
v_mfma_f32_16x16x32_bf16 a[8:11],a[208:211],v[64:67],a[8:11]
ds_read_b64_tr_b16 a[234:235],v3 offset:4608
ds_read_b64_tr_b16 a[236:237],v3 offset:8704
v_mfma_f32_16x16x32_bf16 a[8:11],a[212:215],v[72:75],a[8:11]
ds_read_b64_tr_b16 a[238:239],v3 offset:12800
v_accvgpr_read_b32 v23,a111
v_mfma_f32_16x16x32_bf16 a[12:15],a[216:219],v[64:67],a[12:15]
ds_read_b64_tr_b16 a[240:241],v2 offset:768
v_accvgpr_read_b32 v16,a104
v_mfma_f32_16x16x32_bf16 a[12:15],a[220:223],v[72:75],a[12:15]
s_waitcnt lgkmcnt(8)
ds_read_b64_tr_b16 a[242:243],v2 offset:4864
v_accvgpr_read_b32 v17,a105
v_mfma_f32_16x16x32_bf16 a[16:19],a[224:227],v[64:67],a[16:19]
s_waitcnt lgkmcnt(6)
ds_read_b64_tr_b16 a[244:245],v2 offset:8960
ds_read_b64_tr_b16 a[246:247],v2 offset:13056
v_mfma_f32_16x16x32_bf16 a[16:19],a[228:231],v[72:75],a[16:19]
s_waitcnt lgkmcnt(0)
ds_read_b64_tr_b16 a[248:249],v3 offset:768
v_accvgpr_read_b32 v18,a106
v_mfma_f32_16x16x32_bf16 a[20:23],a[232:235],v[64:67],a[20:23]
s_getreg_b32 s92,hwreg(HW_REG_IB_STS,0,4)
s_min_u32 s4,s92,s6
ds_read_b64_tr_b16 a[250:251],v3 offset:4864
v_mfma_f32_16x16x32_bf16 a[20:23],a[236:239],v[72:75],a[20:23]
s_mov_b32 s6,s92
ds_read_b64_tr_b16 a[252:253],v3 offset:8960
ds_read_b64_tr_b16 a[254:255],v3 offset:13056
v_mfma_f32_16x16x32_bf16 a[24:27],a[240:243],v[64:67],a[24:27]
s_cmp_gt_u32 s4,s9
s_cselect_b32 s4,15,s5
v_accvgpr_read_b32 v19,a107
v_mfma_f32_16x16x32_bf16 a[24:27],a[244:247],v[72:75],a[24:27]
s_waitcnt lgkmcnt(2)
v_exp_f32 v80,v80
v_mfma_f32_16x16x32_bf16 a[28:31],a[248:251],v[64:67],a[28:31]
s_waitcnt lgkmcnt(0)
v_exp_f32 v81,v81
v_mfma_f32_16x16x32_bf16 a[28:31],a[252:255],v[72:75],a[28:31]
s_getpc_b64 s[92:93]
s_cmp_lg_u32 s4,0
s_cbranch_scc1 L_pace_%=
v_mfma_f32_16x16x32_bf16 v[48:51],v[28:31],v[64:67],v[48:51]
v_exp_f32 v82,v82
v_mfma_f32_16x16x32_bf16 v[48:51],v[28:31],v[72:75],v[48:51]
s_add_u32 s10,s20,s74
s_addc_u32 s11,s21,0
v_cvt_pk_bf16_f32 v80,v80,v81
s_waitcnt vmcnt(16)
s_barrier
v_mfma_f32_16x16x32_bf16 v[112:115],a[128:131],v[224:227],v[44:47]
s_add_u32 m0,s76,0
v_exp_f32 v83,v83
v_mfma_f32_16x16x32_bf16 v[112:115],a[132:135],v[232:235],v[112:115]
v_exp_f32 v84,v84
v_mfma_f32_16x16x32_bf16 v[112:115],a[136:139],v[240:243],v[112:115]
global_load_lds_dwordx4 v4,s[10:11] nt
v_cvt_pk_bf16_f32 v81,v82,v83
v_mfma_f32_16x16x32_bf16 v[112:115],a[140:143],v[248:251],v[112:115]
v_exp_f32 v85,v85
v_mfma_f32_16x16x32_bf16 v[116:119],a[156:159],v[248:251],v[44:47]
v_exp_f32 v86,v86
v_mfma_f32_16x16x32_bf16 v[116:119],a[152:155],v[240:243],v[116:119]
v_exp_f32 v87,v87
v_mfma_f32_16x16x32_bf16 v[116:119],a[148:151],v[232:235],v[116:119]
v_cvt_pk_bf16_f32 v82,v84,v85
v_cvt_pk_bf16_f32 v83,v86,v87
v_mfma_f32_16x16x32_bf16 v[116:119],a[144:147],v[224:227],v[116:119]
v_exp_f32 v88,v88
v_mfma_f32_16x16x32_bf16 v[120:123],a[160:163],v[224:227],v[44:47]
v_exp_f32 v89,v89
v_mfma_f32_16x16x32_bf16 v[120:123],a[164:167],v[232:235],v[120:123]
v_exp_f32 v90,v90
v_mfma_f32_16x16x32_bf16 v[120:123],a[168:171],v[240:243],v[120:123]
v_exp_f32 v91,v91
v_mfma_f32_16x16x32_bf16 v[120:123],a[172:175],v[248:251],v[120:123]
v_exp_f32 v92,v92
v_mfma_f32_16x16x32_bf16 v[124:127],a[188:191],v[248:251],v[44:47]
v_exp_f32 v93,v93
v_mfma_f32_16x16x32_bf16 v[124:127],a[184:187],v[240:243],v[124:127]
v_exp_f32 v94,v94
v_exp_f32 v95,v95
v_mfma_f32_16x16x32_bf16 v[124:127],a[180:183],v[232:235],v[124:127]
v_cvt_pk_bf16_f32 v88,v88,v89
v_cvt_pk_bf16_f32 v89,v90,v91
v_cvt_pk_bf16_f32 v90,v92,v93
v_cvt_pk_bf16_f32 v91,v94,v95
v_mfma_f32_16x16x32_bf16 v[124:127],a[176:179],v[224:227],v[124:127]
ds_read_b128 a[128:131],v1
v_mul_f32 v16,v16,v27
v_mfma_f32_16x16x32_bf16 v[132:135],a[192:195],v[80:83],v[132:135]
v_mul_f32 v17,v17,v27
v_mul_f32 v18,v18,v27
v_mfma_f32_16x16x32_bf16 v[132:135],a[196:199],v[88:91],v[132:135]
ds_read_b128 a[132:135],v1 offset:256
v_mul_f32 v19,v19,v27
v_mfma_f32_16x16x32_bf16 v[140:143],a[200:203],v[80:83],v[140:143]
global_load_lds_dwordx4 v4,s[10:11] offset:1024 nt
v_mul_f32 v20,v20,v27
v_mfma_f32_16x16x32_bf16 v[140:143],a[204:207],v[88:91],v[140:143]
ds_read_b128 a[136:139],v1 offset:512
v_mul_f32 v21,v21,v27
v_mfma_f32_16x16x32_bf16 v[148:151],a[208:211],v[80:83],v[148:151]
v_mul_f32 v22,v22,v27
v_mul_f32 v23,v23,v27
v_mfma_f32_16x16x32_bf16 v[148:151],a[212:215],v[88:91],v[148:151]
ds_read_b128 a[140:143],v1 offset:768
v_cvt_pk_bf16_f32 v16,v16,v17
v_mfma_f32_16x16x32_bf16 v[156:159],a[216:219],v[80:83],v[156:159]
v_cvt_pk_bf16_f32 v17,v18,v19
v_cvt_pk_bf16_f32 v18,v20,v21
v_mfma_f32_16x16x32_bf16 v[156:159],a[220:223],v[88:91],v[156:159]
ds_read_b128 a[156:159],v1 offset:4864
v_cvt_pk_bf16_f32 v19,v22,v23
v_mfma_f32_16x16x32_bf16 v[164:167],a[224:227],v[80:83],v[164:167]
v_accvgpr_write_b32 a104,v16
v_accvgpr_write_b32 a105,v17
v_mfma_f32_16x16x32_bf16 v[164:167],a[228:231],v[88:91],v[164:167]
ds_read_b128 a[152:155],v1 offset:4608
s_nop 0
L_pd9_%=:
s_cbranch_execz L_pd13_%=
v_accvgpr_write_b32 a106,v18
v_mfma_f32_16x16x32_bf16 v[172:175],a[232:235],v[80:83],v[172:175]
v_accvgpr_write_b32 a107,v19
v_accvgpr_read_b32 v12,a68
v_mfma_f32_16x16x32_bf16 v[172:175],a[236:239],v[88:91],v[172:175]
ds_read_b128 a[148:151],v1 offset:4352
v_accvgpr_read_b32 v13,a69
v_mfma_f32_16x16x32_bf16 v[180:183],a[240:243],v[80:83],v[180:183]
v_exp_f32 v96,v96
v_mfma_f32_16x16x32_bf16 v[180:183],a[244:247],v[88:91],v[180:183]
ds_read_b128 a[144:147],v1 offset:4096
v_accvgpr_read_b32 v14,a70
v_mfma_f32_16x16x32_bf16 v[188:191],a[248:251],v[80:83],v[188:191]
v_exp_f32 v97,v97
v_mfma_f32_16x16x32_bf16 v[188:191],a[252:255],v[88:91],v[188:191]
ds_read_b128 a[160:163],v1 offset:8192
v_cvt_pk_bf16_f32 v96,v96,v97
v_mfma_f32_16x16x32_bf16 v[52:55],v[28:31],v[80:83],v[52:55]
v_exp_f32 v98,v98
v_mfma_f32_16x16x32_bf16 v[52:55],v[28:31],v[88:91],v[52:55]
s_waitcnt lgkmcnt(3)
ds_read_b128 a[164:167],v1 offset:8448
v_accvgpr_read_b32 v15,a71
v_mfma_f32_16x16x32_bf16 v[64:67],a[128:131],v[128:131],v[32:35]
global_load_lds_dwordx4 v5,s[10:11] offset:2048 nt
v_accvgpr_read_b32 v8,a64
v_mfma_f32_16x16x32_bf16 v[64:67],a[132:135],v[136:139],v[64:67]
ds_read_b128 a[168:171],v1 offset:8704
v_accvgpr_read_b32 v9,a65
v_mfma_f32_16x16x32_bf16 v[64:67],a[136:139],v[144:147],v[64:67]
v_exp_f32 v99,v99
v_mfma_f32_16x16x32_bf16 v[64:67],a[140:143],v[152:155],v[64:67]
ds_read_b128 a[108:111],v7 offset:3072
v_cvt_pk_bf16_f32 v97,v98,v99
v_mfma_f32_16x16x32_bf16 v[68:71],a[156:159],v[152:155],v[32:35]
v_exp_f32 v100,v100
v_mfma_f32_16x16x32_bf16 v[68:71],a[152:155],v[144:147],v[68:71]
s_waitcnt lgkmcnt(3)
ds_read_b128 a[172:175],v1 offset:8960
v_accvgpr_read_b32 v10,a66
v_mfma_f32_16x16x32_bf16 v[68:71],a[148:151],v[136:139],v[68:71]
v_exp_f32 v101,v101
v_mfma_f32_16x16x32_bf16 v[68:71],a[144:147],v[128:131],v[68:71]
ds_read_b128 a[188:191],v1 offset:13056
v_cvt_pk_bf16_f32 v98,v100,v101
v_mfma_f32_16x16x32_bf16 v[72:75],a[160:163],v[128:131],v[32:35]
s_waitcnt lgkmcnt(4)
v_exp_f32 v102,v102
v_mfma_f32_16x16x32_bf16 v[72:75],a[164:167],v[136:139],v[72:75]
s_waitcnt lgkmcnt(3)
ds_read_b128 a[184:187],v1 offset:12800
v_accvgpr_read_b32 v11,a67
v_mfma_f32_16x16x32_bf16 v[72:75],a[168:171],v[144:147],v[72:75]
s_waitcnt lgkmcnt(2)
ds_read_b128 a[180:183],v1 offset:12544
v_mul_f32 v8,v8,v26
v_mfma_f32_16x16x32_bf16 v[72:75],a[172:175],v[152:155],v[72:75]
s_waitcnt lgkmcnt(2)
ds_read_b128 a[176:179],v1 offset:12288
v_mul_f32 v9,v9,v26
v_mfma_f32_16x16x32_bf16 v[76:79],a[188:191],v[152:155],v[32:35]
s_waitcnt lgkmcnt(2)
v_exp_f32 v103,v103
v_mfma_f32_16x16x32_bf16 v[76:79],a[184:187],v[144:147],v[76:79]
s_waitcnt lgkmcnt(1)
v_cvt_pk_bf16_f32 v99,v102,v103
v_exp_f32 v104,v104
v_exp_f32 v105,v105
v_exp_f32 v106,v106
v_exp_f32 v107,v107
v_exp_f32 v108,v108
v_exp_f32 v109,v109
v_exp_f32 v110,v110
v_exp_f32 v111,v111
v_mfma_f32_16x16x32_bf16 v[76:79],a[180:183],v[136:139],v[76:79]
s_waitcnt lgkmcnt(0)
v_cvt_pk_bf16_f32 v104,v104,v105
v_cvt_pk_bf16_f32 v105,v106,v107
v_cvt_pk_bf16_f32 v106,v108,v109
v_cvt_pk_bf16_f32 v107,v110,v111
v_mfma_f32_16x16x32_bf16 v[76:79],a[176:179],v[128:131],v[76:79]
v_exp_f32 v112,v112
v_mfma_f32_16x16x32_bf16 v[196:199],a[192:195],v[96:99],v[196:199]
global_load_lds_dwordx4 v5,s[10:11] offset:3072
v_mul_f32 v10,v10,v26
v_mfma_f32_16x16x32_bf16 v[196:199],a[196:199],v[104:107],v[196:199]
v_exp_f32 v113,v113
v_mfma_f32_16x16x32_bf16 v[204:207],a[200:203],v[96:99],v[204:207]
v_exp_f32 v114,v114
v_mfma_f32_16x16x32_bf16 v[204:207],a[204:207],v[104:107],v[204:207]
v_exp_f32 v115,v115
)A"
R"A(v_mfma_f32_16x16x32_bf16 v[212:215],a[208:211],v[96:99],v[212:215]
v_exp_f32 v116,v116
v_mfma_f32_16x16x32_bf16 v[212:215],a[212:215],v[104:107],v[212:215]
v_exp_f32 v117,v117
v_mfma_f32_16x16x32_bf16 v[220:223],a[216:219],v[96:99],v[220:223]
v_exp_f32 v118,v118
v_mfma_f32_16x16x32_bf16 v[220:223],a[220:223],v[104:107],v[220:223]
v_exp_f32 v119,v119
v_mfma_f32_16x16x32_bf16 v[228:231],a[224:227],v[96:99],v[228:231]
v_cvt_pk_bf16_f32 v112,v112,v113
v_cvt_pk_bf16_f32 v113,v114,v115
v_mfma_f32_16x16x32_bf16 v[228:231],a[228:231],v[104:107],v[228:231]
v_cvt_pk_bf16_f32 v114,v116,v117
v_cvt_pk_bf16_f32 v115,v118,v119
v_mfma_f32_16x16x32_bf16 v[236:239],a[232:235],v[96:99],v[236:239]
v_exp_f32 v120,v120
v_mfma_f32_16x16x32_bf16 v[236:239],a[236:239],v[104:107],v[236:239]
v_exp_f32 v121,v121
v_mfma_f32_16x16x32_bf16 v[244:247],a[240:243],v[96:99],v[244:247]
s_add_u32 s10,s24,s74
s_addc_u32 s11,s25,0
v_cvt_pk_bf16_f32 v120,v120,v121
v_mfma_f32_16x16x32_bf16 v[244:247],a[244:247],v[104:107],v[244:247]
s_add_u32 s74,s74,16384
v_exp_f32 v122,v122
v_mfma_f32_16x16x32_bf16 v[252:255],a[248:251],v[96:99],v[252:255]
s_add_u32 m0,s76,16384
v_exp_f32 v123,v123
v_mfma_f32_16x16x32_bf16 v[252:255],a[252:255],v[104:107],v[252:255]
s_and_b32 s74,s74,0x3ffff
v_exp_f32 v124,v124
v_mfma_f32_16x16x32_bf16 v[56:59],v[28:31],v[96:99],v[56:59]
global_load_lds_dwordx4 v4,s[10:11]
v_cvt_pk_bf16_f32 v121,v122,v123
v_mfma_f32_16x16x32_bf16 v[56:59],v[28:31],v[104:107],v[56:59]
s_or_b32 s74,s74,s69
v_exp_f32 v125,v125
v_mfma_f32_16x16x32_bf16 v[80:83],a[128:131],v[160:163],v[36:39]
s_sub_u32 s76,s76,32768
v_exp_f32 v126,v126
v_mfma_f32_16x16x32_bf16 v[80:83],a[132:135],v[168:171],v[80:83]
s_and_b32 s76,s76,0x1ffff
v_exp_f32 v127,v127
v_mfma_f32_16x16x32_bf16 v[80:83],a[136:139],v[176:179],v[80:83]
s_and_b32 s69,s75,0xfffc0000
v_cvt_pk_bf16_f32 v122,v124,v125
v_cvt_pk_bf16_f32 v123,v126,v127
v_mfma_f32_16x16x32_bf16 v[80:83],a[140:143],v[184:187],v[80:83]
v_mul_f32 v11,v11,v26
v_mul_f32 v12,v12,v26
v_mfma_f32_16x16x32_bf16 v[84:87],a[156:159],v[184:187],v[36:39]
v_mul_f32 v13,v13,v26
v_mul_f32 v14,v14,v26
v_mfma_f32_16x16x32_bf16 v[84:87],a[152:155],v[176:179],v[84:87]
v_mul_f32 v15,v15,v26
v_cvt_pk_bf16_f32 v8,v8,v9
v_mfma_f32_16x16x32_bf16 v[84:87],a[148:151],v[168:171],v[84:87]
v_cvt_pk_bf16_f32 v9,v10,v11
v_cvt_pk_bf16_f32 v10,v12,v13
v_mfma_f32_16x16x32_bf16 v[84:87],a[144:147],v[160:163],v[84:87]
v_cvt_pk_bf16_f32 v11,v14,v15
v_accvgpr_write_b32 a64,v8
v_mfma_f32_16x16x32_bf16 v[88:91],a[160:163],v[160:163],v[36:39]
v_accvgpr_write_b32 a65,v9
v_accvgpr_write_b32 a66,v10
v_mfma_f32_16x16x32_bf16 v[88:91],a[164:167],v[168:171],v[88:91]
ds_read_b128 a[68:71],v7 offset:6144
v_accvgpr_write_b32 a67,v11
v_mfma_f32_16x16x32_bf16 v[88:91],a[168:171],v[176:179],v[88:91]
v_add_u32 v2,s78,v2
v_add_u32 v3,s78,v3
v_mfma_f32_16x16x32_bf16 v[88:91],a[172:175],v[184:187],v[88:91]
s_sub_u32 s78,s77,32768
v_exp_f32 v64,v64
v_mfma_f32_16x16x32_bf16 v[92:95],a[188:191],v[184:187],v[36:39]
s_and_b32 s78,s78,0x1ffff
v_exp_f32 v65,v65
v_mfma_f32_16x16x32_bf16 v[92:95],a[184:187],v[176:179],v[92:95]
s_sub_u32 s78,s78,s77
v_exp_f32 v66,v66
v_mfma_f32_16x16x32_bf16 v[92:95],a[180:183],v[168:171],v[92:95]
global_load_lds_dwordx4 v5,s[10:11] offset:1024
v_cvt_pk_bf16_f32 v64,v64,v65
v_mfma_f32_16x16x32_bf16 v[92:95],a[176:179],v[160:163],v[92:95]
s_add_u32 s77,s77,s78
v_exp_f32 v67,v67
v_mfma_f32_16x16x32_bf16 a[32:35],a[192:195],v[112:115],a[32:35]
ds_read_b64_tr_b16 a[192:193],v2
ds_read_b64_tr_b16 a[194:195],v2 offset:4096
v_mfma_f32_16x16x32_bf16 a[32:35],a[196:199],v[120:123],a[32:35]
ds_read_b64_tr_b16 a[196:197],v2 offset:8192
v_cvt_pk_bf16_f32 v65,v66,v67
v_mfma_f32_16x16x32_bf16 a[36:39],a[200:203],v[112:115],a[36:39]
ds_read_b64_tr_b16 a[198:199],v2 offset:12288
v_accvgpr_read_b32 v20,a76
v_mfma_f32_16x16x32_bf16 a[36:39],a[204:207],v[120:123],a[36:39]
ds_read_b64_tr_b16 a[200:201],v3
v_accvgpr_read_b32 v21,a77
v_mfma_f32_16x16x32_bf16 a[40:43],a[208:211],v[112:115],a[40:43]
ds_read_b64_tr_b16 a[202:203],v3 offset:4096
ds_read_b64_tr_b16 a[204:205],v3 offset:8192
v_mfma_f32_16x16x32_bf16 a[40:43],a[212:215],v[120:123],a[40:43]
ds_read_b64_tr_b16 a[206:207],v3 offset:12288
v_accvgpr_read_b32 v22,a78
v_mfma_f32_16x16x32_bf16 a[44:47],a[216:219],v[112:115],a[44:47]
ds_read_b64_tr_b16 a[208:209],v2 offset:256
v_accvgpr_read_b32 v23,a79
v_mfma_f32_16x16x32_bf16 a[44:47],a[220:223],v[120:123],a[44:47]
ds_read_b64_tr_b16 a[210:211],v2 offset:4352
v_accvgpr_read_b32 v16,a72
v_mfma_f32_16x16x32_bf16 a[48:51],a[224:227],v[112:115],a[48:51]
ds_read_b64_tr_b16 a[212:213],v2 offset:8448
ds_read_b64_tr_b16 a[214:215],v2 offset:12544
v_mfma_f32_16x16x32_bf16 a[48:51],a[228:231],v[120:123],a[48:51]
v_exp_f32 v68,v68
v_mfma_f32_16x16x32_bf16 a[52:55],a[232:235],v[112:115],a[52:55]
v_exp_f32 v69,v69
v_mfma_f32_16x16x32_bf16 a[52:55],a[236:239],v[120:123],a[52:55]
v_exp_f32 v70,v70
v_mfma_f32_16x16x32_bf16 a[56:59],a[240:243],v[112:115],a[56:59]
v_exp_f32 v71,v71
v_mfma_f32_16x16x32_bf16 a[56:59],a[244:247],v[120:123],a[56:59]
ds_read_b64_tr_b16 a[216:217],v3 offset:256
ds_read_b64_tr_b16 a[218:219],v3 offset:4352
v_mfma_f32_16x16x32_bf16 a[60:63],a[248:251],v[112:115],a[60:63]
global_load_lds_dwordx4 v4,s[10:11] offset:2048
v_cvt_pk_bf16_f32 v66,v68,v69
v_mfma_f32_16x16x32_bf16 a[60:63],a[252:255],v[120:123],a[60:63]
ds_read_b64_tr_b16 a[220:221],v3 offset:8448
v_cvt_pk_bf16_f32 v67,v70,v71
v_mfma_f32_16x16x32_bf16 v[60:63],v[28:31],v[112:115],v[60:63]
v_exp_f32 v72,v72
v_mfma_f32_16x16x32_bf16 v[60:63],v[28:31],v[120:123],v[60:63]
v_exp_f32 v73,v73
v_mfma_f32_16x16x32_bf16 v[96:99],a[128:131],v[192:195],v[40:43]
v_exp_f32 v74,v74
v_mfma_f32_16x16x32_bf16 v[96:99],a[132:135],v[200:203],v[96:99]
v_exp_f32 v75,v75
v_mfma_f32_16x16x32_bf16 v[96:99],a[136:139],v[208:211],v[96:99]
v_exp_f32 v76,v76
v_mfma_f32_16x16x32_bf16 v[96:99],a[140:143],v[216:219],v[96:99]
v_exp_f32 v77,v77
v_mfma_f32_16x16x32_bf16 v[100:103],a[156:159],v[216:219],v[40:43]
v_exp_f32 v78,v78
v_mfma_f32_16x16x32_bf16 v[100:103],a[152:155],v[208:211],v[100:103]
v_exp_f32 v79,v79
v_mfma_f32_16x16x32_bf16 v[100:103],a[148:151],v[200:203],v[100:103]
ds_read_b64_tr_b16 a[222:223],v3 offset:12544
v_cvt_pk_bf16_f32 v72,v72,v73
v_mfma_f32_16x16x32_bf16 v[100:103],a[144:147],v[192:195],v[100:103]
v_cvt_pk_bf16_f32 v73,v74,v75
v_cvt_pk_bf16_f32 v74,v76,v77
v_mfma_f32_16x16x32_bf16 v[104:107],a[160:163],v[192:195],v[40:43]
ds_read_b64_tr_b16 a[224:225],v2 offset:512
v_cvt_pk_bf16_f32 v75,v78,v79
v_mfma_f32_16x16x32_bf16 v[104:107],a[164:167],v[200:203],v[104:107]
ds_read_b64_tr_b16 a[226:227],v2 offset:4608
ds_read_b64_tr_b16 a[228:229],v2 offset:8704
v_mfma_f32_16x16x32_bf16 v[104:107],a[168:171],v[208:211],v[104:107]
ds_read_b64_tr_b16 a[230:231],v2 offset:12800
v_accvgpr_read_b32 v17,a73
v_mfma_f32_16x16x32_bf16 v[104:107],a[172:175],v[216:219],v[104:107]
ds_read_b64_tr_b16 a[232:233],v3 offset:512
v_accvgpr_read_b32 v18,a74
v_mfma_f32_16x16x32_bf16 v[108:111],a[188:191],v[216:219],v[40:43]
global_load_lds_dwordx4 v5,s[10:11] offset:3072
ds_read_b64_tr_b16 a[234:235],v3 offset:4608
v_mfma_f32_16x16x32_bf16 v[108:111],a[184:187],v[208:211],v[108:111]
ds_read_b64_tr_b16 a[236:237],v3 offset:8704
ds_read_b64_tr_b16 a[238:239],v3 offset:12800
v_mfma_f32_16x16x32_bf16 v[108:111],a[180:183],v[200:203],v[108:111]
ds_read_b64_tr_b16 a[240:241],v2 offset:768
v_accvgpr_read_b32 v19,a75
v_mfma_f32_16x16x32_bf16 v[108:111],a[176:179],v[192:195],v[108:111]
s_waitcnt lgkmcnt(9)
ds_read_b64_tr_b16 a[242:243],v2 offset:4864
v_mul_f32 v16,v16,v26
v_mfma_f32_16x16x32_bf16 a[0:3],a[192:195],v[64:67],a[0:3]
ds_read_b64_tr_b16 a[244:245],v2 offset:8960
v_mul_f32 v17,v17,v26
v_mfma_f32_16x16x32_bf16 a[0:3],a[196:199],v[72:75],a[0:3]
ds_read_b64_tr_b16 a[246:247],v2 offset:13056
ds_read_b64_tr_b16 a[248:249],v3 offset:768
v_mfma_f32_16x16x32_bf16 a[4:7],a[200:203],v[64:67],a[4:7]
ds_read_b64_tr_b16 a[250:251],v3 offset:4864
v_mul_f32 v18,v18,v26
v_mfma_f32_16x16x32_bf16 a[4:7],a[204:207],v[72:75],a[4:7]
ds_read_b64_tr_b16 a[252:253],v3 offset:8960
v_mul_f32 v19,v19,v26
v_mfma_f32_16x16x32_bf16 a[8:11],a[208:211],v[64:67],a[8:11]
ds_read_b64_tr_b16 a[254:255],v3 offset:13056
v_mul_f32 v20,v20,v26
v_mfma_f32_16x16x32_bf16 a[8:11],a[212:215],v[72:75],a[8:11]
v_exp_f32 v80,v80
v_mfma_f32_16x16x32_bf16 a[12:15],a[216:219],v[64:67],a[12:15]
v_exp_f32 v81,v81
v_mfma_f32_16x16x32_bf16 a[12:15],a[220:223],v[72:75],a[12:15]
s_waitcnt lgkmcnt(6)
v_exp_f32 v82,v82
v_mfma_f32_16x16x32_bf16 a[16:19],a[224:227],v[64:67],a[16:19]
v_exp_f32 v83,v83
v_mfma_f32_16x16x32_bf16 a[16:19],a[228:231],v[72:75],a[16:19]
v_exp_f32 v84,v84
v_mfma_f32_16x16x32_bf16 a[20:23],a[232:235],v[64:67],a[20:23]
s_getreg_b32 s92,hwreg(HW_REG_IB_STS,0,4)
s_min_u32 s4,s92,s6
s_mov_b32 s6,s92
v_mfma_f32_16x16x32_bf16 a[20:23],a[236:239],v[72:75],a[20:23]
s_cmp_ge_u32 s4,s9
s_cselect_b32 s4,15,s5
v_cvt_pk_bf16_f32 v80,v80,v81
v_mfma_f32_16x16x32_bf16 a[24:27],a[240:243],v[64:67],a[24:27]
s_waitcnt lgkmcnt(0)
v_exp_f32 v85,v85
v_mfma_f32_16x16x32_bf16 a[24:27],a[244:247],v[72:75],a[24:27]
v_exp_f32 v86,v86
v_mfma_f32_16x16x32_bf16 a[28:31],a[248:251],v[64:67],a[28:31]
v_exp_f32 v87,v87
v_mfma_f32_16x16x32_bf16 a[28:31],a[252:255],v[72:75],a[28:31]
s_getpc_b64 s[92:93]
s_cmp_lg_u32 s4,0
s_cbranch_scc1 L_pace_%=
v_mfma_f32_16x16x32_bf16 v[48:51],v[28:31],v[64:67],v[48:51]
v_cvt_pk_bf16_f32 v81,v82,v83
v_cvt_pk_bf16_f32 v82,v84,v85
v_mfma_f32_16x16x32_bf16 v[48:51],v[28:31],v[72:75],v[48:51]
s_add_u32 s10,s20,s74
s_addc_u32 s11,s21,0
v_cvt_pk_bf16_f32 v83,v86,v87
s_waitcnt vmcnt(16)
s_barrier
v_mfma_f32_16x16x32_bf16 v[112:115],a[128:131],v[224:227],v[44:47]
s_add_u32 m0,s76,0
v_exp_f32 v88,v88
v_mfma_f32_16x16x32_bf16 v[112:115],a[132:135],v[232:235],v[112:115]
v_exp_f32 v89,v89
v_mfma_f32_16x16x32_bf16 v[112:115],a[136:139],v[240:243],v[112:115]
global_load_lds_dwordx4 v4,s[10:11] nt
v_cvt_pk_bf16_f32 v88,v88,v89
v_mfma_f32_16x16x32_bf16 v[112:115],a[140:143],v[248:251],v[112:115]
v_exp_f32 v90,v90
v_mfma_f32_16x16x32_bf16 v[116:119],a[156:159],v[248:251],v[44:47]
v_exp_f32 v91,v91
v_mfma_f32_16x16x32_bf16 v[116:119],a[152:155],v[240:243],v[116:119]
v_exp_f32 v92,v92
v_mfma_f32_16x16x32_bf16 v[116:119],a[148:151],v[232:235],v[116:119]
v_exp_f32 v93,v93
v_mfma_f32_16x16x32_bf16 v[116:119],a[144:147],v[224:227],v[116:119]
v_exp_f32 v94,v94
v_mfma_f32_16x16x32_bf16 v[120:123],a[160:163],v[224:227],v[44:47]
v_exp_f32 v95,v95
v_mfma_f32_16x16x32_bf16 v[120:123],a[164:167],v[232:235],v[120:123]
v_cvt_pk_bf16_f32 v89,v90,v91
v_cvt_pk_bf16_f32 v90,v92,v93
v_mfma_f32_16x16x32_bf16 v[120:123],a[168:171],v[240:243],v[120:123]
v_cvt_pk_bf16_f32 v91,v94,v95
v_mul_f32 v21,v21,v26
v_mfma_f32_16x16x32_bf16 v[120:123],a[172:175],v[248:251],v[120:123]
v_mul_f32 v22,v22,v26
v_mul_f32 v23,v23,v26
v_mfma_f32_16x16x32_bf16 v[124:127],a[188:191],v[248:251],v[44:47]
v_cvt_pk_bf16_f32 v16,v16,v17
v_cvt_pk_bf16_f32 v17,v18,v19
v_mfma_f32_16x16x32_bf16 v[124:127],a[184:187],v[240:243],v[124:127]
v_cvt_pk_bf16_f32 v18,v20,v21
v_cvt_pk_bf16_f32 v19,v22,v23
v_mfma_f32_16x16x32_bf16 v[124:127],a[180:183],v[232:235],v[124:127]
v_accvgpr_write_b32 a72,v16
v_accvgpr_write_b32 a73,v17
v_mfma_f32_16x16x32_bf16 v[124:127],a[176:179],v[224:227],v[124:127]
v_accvgpr_write_b32 a74,v18
v_accvgpr_write_b32 a75,v19
v_mfma_f32_16x16x32_bf16 v[132:135],a[192:195],v[80:83],v[132:135]
ds_read_b128 a[76:79],v7 offset:7168
v_add_u32 v1,s78,v1
v_mfma_f32_16x16x32_bf16 v[132:135],a[196:199],v[88:91],v[132:135]
v_exp_f32 v96,v96
v_mfma_f32_16x16x32_bf16 v[140:143],a[200:203],v[80:83],v[140:143]
global_load_lds_dwordx4 v4,s[10:11] offset:1024 nt
ds_read_b128 a[128:131],v1
v_mfma_f32_16x16x32_bf16 v[140:143],a[204:207],v[88:91],v[140:143]
v_exp_f32 v97,v97
v_mfma_f32_16x16x32_bf16 v[148:151],a[208:211],v[80:83],v[148:151]
ds_read_b128 a[132:135],v1 offset:256
v_cvt_pk_bf16_f32 v96,v96,v97
v_mfma_f32_16x16x32_bf16 v[148:151],a[212:215],v[88:91],v[148:151]
v_exp_f32 v98,v98
v_mfma_f32_16x16x32_bf16 v[156:159],a[216:219],v[80:83],v[156:159]
)A"
R"A(ds_read_b128 a[136:139],v1 offset:512
v_accvgpr_read_b32 v12,a84
v_mfma_f32_16x16x32_bf16 v[156:159],a[220:223],v[88:91],v[156:159]
v_exp_f32 v99,v99
v_mfma_f32_16x16x32_bf16 v[164:167],a[224:227],v[80:83],v[164:167]
ds_read_b128 a[140:143],v1 offset:768
v_cvt_pk_bf16_f32 v97,v98,v99
v_mfma_f32_16x16x32_bf16 v[164:167],a[228:231],v[88:91],v[164:167]
v_exp_f32 v100,v100
v_mfma_f32_16x16x32_bf16 v[172:175],a[232:235],v[80:83],v[172:175]
ds_read_b128 a[156:159],v1 offset:4864
v_accvgpr_read_b32 v13,a85
v_mfma_f32_16x16x32_bf16 v[172:175],a[236:239],v[88:91],v[172:175]
v_exp_f32 v101,v101
v_mfma_f32_16x16x32_bf16 v[180:183],a[240:243],v[80:83],v[180:183]
ds_read_b128 a[152:155],v1 offset:4608
v_cvt_pk_bf16_f32 v98,v100,v101
v_mfma_f32_16x16x32_bf16 v[180:183],a[244:247],v[88:91],v[180:183]
v_exp_f32 v102,v102
v_mfma_f32_16x16x32_bf16 v[188:191],a[248:251],v[80:83],v[188:191]
ds_read_b128 a[148:151],v1 offset:4352
v_accvgpr_read_b32 v14,a86
v_mfma_f32_16x16x32_bf16 v[188:191],a[252:255],v[88:91],v[188:191]
v_exp_f32 v103,v103
v_mfma_f32_16x16x32_bf16 v[52:55],v[28:31],v[80:83],v[52:55]
ds_read_b128 a[144:147],v1 offset:4096
v_cvt_pk_bf16_f32 v99,v102,v103
v_mfma_f32_16x16x32_bf16 v[52:55],v[28:31],v[88:91],v[52:55]
s_waitcnt lgkmcnt(4)
v_exp_f32 v104,v104
v_mfma_f32_16x16x32_bf16 v[64:67],a[128:131],v[128:131],v[32:35]
global_load_lds_dwordx4 v5,s[10:11] offset:2048 nt
ds_read_b128 a[160:163],v1 offset:8192
v_mfma_f32_16x16x32_bf16 v[64:67],a[132:135],v[136:139],v[64:67]
v_exp_f32 v105,v105
v_mfma_f32_16x16x32_bf16 v[64:67],a[136:139],v[144:147],v[64:67]
ds_read_b128 a[164:167],v1 offset:8448
v_cvt_pk_bf16_f32 v104,v104,v105
v_mfma_f32_16x16x32_bf16 v[64:67],a[140:143],v[152:155],v[64:67]
s_waitcnt lgkmcnt(4)
v_exp_f32 v106,v106
v_mfma_f32_16x16x32_bf16 v[68:71],a[156:159],v[152:155],v[32:35]
ds_read_b128 a[168:171],v1 offset:8704
v_accvgpr_read_b32 v15,a87
v_mfma_f32_16x16x32_bf16 v[68:71],a[152:155],v[144:147],v[68:71]
s_waitcnt lgkmcnt(4)
v_exp_f32 v107,v107
v_mfma_f32_16x16x32_bf16 v[68:71],a[148:151],v[136:139],v[68:71]
s_waitcnt lgkmcnt(3)
ds_read_b128 a[172:175],v1 offset:8960
v_cvt_pk_bf16_f32 v105,v106,v107
v_mfma_f32_16x16x32_bf16 v[68:71],a[144:147],v[128:131],v[68:71]
s_waitcnt lgkmcnt(3)
v_exp_f32 v108,v108
v_mfma_f32_16x16x32_bf16 v[72:75],a[160:163],v[128:131],v[32:35]
s_waitcnt lgkmcnt(2)
ds_read_b128 a[188:191],v1 offset:13056
v_accvgpr_read_b32 v8,a80
v_mfma_f32_16x16x32_bf16 v[72:75],a[164:167],v[136:139],v[72:75]
s_waitcnt lgkmcnt(2)
ds_read_b128 a[184:187],v1 offset:12800
v_accvgpr_read_b32 v9,a81
v_mfma_f32_16x16x32_bf16 v[72:75],a[168:171],v[144:147],v[72:75]
s_waitcnt lgkmcnt(2)
ds_read_b128 a[180:183],v1 offset:12544
v_accvgpr_read_b32 v10,a82
v_mfma_f32_16x16x32_bf16 v[72:75],a[172:175],v[152:155],v[72:75]
s_waitcnt lgkmcnt(2)
ds_read_b128 a[176:179],v1 offset:12288
v_accvgpr_read_b32 v11,a83
v_mfma_f32_16x16x32_bf16 v[76:79],a[188:191],v[152:155],v[32:35]
s_waitcnt lgkmcnt(2)
v_exp_f32 v109,v109
v_mfma_f32_16x16x32_bf16 v[76:79],a[184:187],v[144:147],v[76:79]
s_waitcnt lgkmcnt(1)
v_exp_f32 v110,v110
v_exp_f32 v111,v111
v_mfma_f32_16x16x32_bf16 v[76:79],a[180:183],v[136:139],v[76:79]
s_waitcnt lgkmcnt(0)
v_cvt_pk_bf16_f32 v106,v108,v109
v_cvt_pk_bf16_f32 v107,v110,v111
v_mfma_f32_16x16x32_bf16 v[76:79],a[176:179],v[128:131],v[76:79]
v_mul_f32 v8,v8,v26
v_mul_f32 v9,v9,v26
v_mfma_f32_16x16x32_bf16 v[196:199],a[192:195],v[96:99],v[196:199]
global_load_lds_dwordx4 v5,s[10:11] offset:3072
v_mul_f32 v10,v10,v26
v_mfma_f32_16x16x32_bf16 v[196:199],a[196:199],v[104:107],v[196:199]
v_mul_f32 v11,v11,v26
v_mul_f32 v12,v12,v26
v_mfma_f32_16x16x32_bf16 v[204:207],a[200:203],v[96:99],v[204:207]
v_mul_f32 v13,v13,v26
v_mul_f32 v14,v14,v26
v_mfma_f32_16x16x32_bf16 v[204:207],a[204:207],v[104:107],v[204:207]
v_mul_f32 v15,v15,v26
v_cvt_pk_bf16_f32 v8,v8,v9
v_mfma_f32_16x16x32_bf16 v[212:215],a[208:211],v[96:99],v[212:215]
v_cvt_pk_bf16_f32 v9,v10,v11
v_cvt_pk_bf16_f32 v10,v12,v13
v_mfma_f32_16x16x32_bf16 v[212:215],a[212:215],v[104:107],v[212:215]
v_cvt_pk_bf16_f32 v11,v14,v15
v_accvgpr_write_b32 a80,v8
v_mfma_f32_16x16x32_bf16 v[220:223],a[216:219],v[96:99],v[220:223]
v_accvgpr_write_b32 a81,v9
v_accvgpr_write_b32 a82,v10
v_mfma_f32_16x16x32_bf16 v[220:223],a[220:223],v[104:107],v[220:223]
ds_read_b128 a[84:87],v7
v_accvgpr_write_b32 a83,v11
v_mfma_f32_16x16x32_bf16 v[228:231],a[224:227],v[96:99],v[228:231]
v_exp_f32 v112,v112
v_mfma_f32_16x16x32_bf16 v[228:231],a[228:231],v[104:107],v[228:231]
v_exp_f32 v113,v113
v_mfma_f32_16x16x32_bf16 v[236:239],a[232:235],v[96:99],v[236:239]
v_exp_f32 v114,v114
v_mfma_f32_16x16x32_bf16 v[236:239],a[236:239],v[104:107],v[236:239]
v_exp_f32 v115,v115
v_mfma_f32_16x16x32_bf16 v[244:247],a[240:243],v[96:99],v[244:247]
s_add_u32 s10,s24,s74
s_addc_u32 s11,s25,0
v_cvt_pk_bf16_f32 v112,v112,v113
v_mfma_f32_16x16x32_bf16 v[244:247],a[244:247],v[104:107],v[244:247]
s_add_u32 s74,s74,16384
v_exp_f32 v116,v116
v_mfma_f32_16x16x32_bf16 v[252:255],a[248:251],v[96:99],v[252:255]
s_add_u32 m0,s76,16384
v_exp_f32 v117,v117
v_mfma_f32_16x16x32_bf16 v[252:255],a[252:255],v[104:107],v[252:255]
s_and_b32 s74,s74,0x3ffff
v_exp_f32 v118,v118
v_mfma_f32_16x16x32_bf16 v[56:59],v[28:31],v[96:99],v[56:59]
global_load_lds_dwordx4 v4,s[10:11]
v_cvt_pk_bf16_f32 v113,v114,v115
v_mfma_f32_16x16x32_bf16 v[56:59],v[28:31],v[104:107],v[56:59]
s_or_b32 s74,s74,s69
v_exp_f32 v119,v119
v_mfma_f32_16x16x32_bf16 v[80:83],a[128:131],v[160:163],v[36:39]
s_sub_u32 s76,s76,32768
v_cvt_pk_bf16_f32 v114,v116,v117
v_cvt_pk_bf16_f32 v115,v118,v119
v_mfma_f32_16x16x32_bf16 v[80:83],a[132:135],v[168:171],v[80:83]
s_and_b32 s76,s76,0x1ffff
v_exp_f32 v120,v120
v_mfma_f32_16x16x32_bf16 v[80:83],a[136:139],v[176:179],v[80:83]
v_exp_f32 v121,v121
v_mfma_f32_16x16x32_bf16 v[80:83],a[140:143],v[184:187],v[80:83]
v_exp_f32 v122,v122
v_mfma_f32_16x16x32_bf16 v[84:87],a[156:159],v[184:187],v[36:39]
v_exp_f32 v123,v123
v_mfma_f32_16x16x32_bf16 v[84:87],a[152:155],v[176:179],v[84:87]
v_exp_f32 v124,v124
v_mfma_f32_16x16x32_bf16 v[84:87],a[148:151],v[168:171],v[84:87]
v_exp_f32 v125,v125
v_mfma_f32_16x16x32_bf16 v[84:87],a[144:147],v[160:163],v[84:87]
v_exp_f32 v126,v126
v_mfma_f32_16x16x32_bf16 v[88:91],a[160:163],v[160:163],v[36:39]
v_exp_f32 v127,v127
v_mfma_f32_16x16x32_bf16 v[88:91],a[164:167],v[168:171],v[88:91]
v_cvt_pk_bf16_f32 v120,v120,v121
v_cvt_pk_bf16_f32 v121,v122,v123
v_mfma_f32_16x16x32_bf16 v[88:91],a[168:171],v[176:179],v[88:91]
v_cvt_pk_bf16_f32 v122,v124,v125
v_cvt_pk_bf16_f32 v123,v126,v127
v_mfma_f32_16x16x32_bf16 v[88:91],a[172:175],v[184:187],v[88:91]
v_add_u32 v2,s78,v2
v_add_u32 v3,s78,v3
v_mfma_f32_16x16x32_bf16 v[92:95],a[188:191],v[184:187],v[36:39]
s_sub_u32 s78,s77,32768
v_exp_f32 v64,v64
v_mfma_f32_16x16x32_bf16 v[92:95],a[184:187],v[176:179],v[92:95]
s_and_b32 s78,s78,0x1ffff
v_exp_f32 v65,v65
v_mfma_f32_16x16x32_bf16 v[92:95],a[180:183],v[168:171],v[92:95]
global_load_lds_dwordx4 v5,s[10:11] offset:1024
v_cvt_pk_bf16_f32 v64,v64,v65
v_mfma_f32_16x16x32_bf16 v[92:95],a[176:179],v[160:163],v[92:95]
s_sub_u32 s78,s78,s77
v_exp_f32 v66,v66
v_mfma_f32_16x16x32_bf16 a[32:35],a[192:195],v[112:115],a[32:35]
s_add_u32 s77,s77,s78
ds_read_b64_tr_b16 a[192:193],v2
ds_read_b64_tr_b16 a[194:195],v2 offset:4096
v_mfma_f32_16x16x32_bf16 a[32:35],a[196:199],v[120:123],a[32:35]
ds_read_b64_tr_b16 a[196:197],v2 offset:8192
v_accvgpr_read_b32 v20,a92
v_mfma_f32_16x16x32_bf16 a[36:39],a[200:203],v[112:115],a[36:39]
ds_read_b64_tr_b16 a[198:199],v2 offset:12288
v_accvgpr_read_b32 v21,a93
v_mfma_f32_16x16x32_bf16 a[36:39],a[204:207],v[120:123],a[36:39]
ds_read_b64_tr_b16 a[200:201],v3
v_accvgpr_read_b32 v22,a94
v_mfma_f32_16x16x32_bf16 a[40:43],a[208:211],v[112:115],a[40:43]
ds_read_b64_tr_b16 a[202:203],v3 offset:4096
ds_read_b64_tr_b16 a[204:205],v3 offset:8192
v_mfma_f32_16x16x32_bf16 a[40:43],a[212:215],v[120:123],a[40:43]
ds_read_b64_tr_b16 a[206:207],v3 offset:12288
v_accvgpr_read_b32 v23,a95
v_mfma_f32_16x16x32_bf16 a[44:47],a[216:219],v[112:115],a[44:47]
ds_read_b64_tr_b16 a[208:209],v2 offset:256
v_accvgpr_read_b32 v16,a88
v_mfma_f32_16x16x32_bf16 a[44:47],a[220:223],v[120:123],a[44:47]
ds_read_b64_tr_b16 a[210:211],v2 offset:4352
v_accvgpr_read_b32 v17,a89
v_mfma_f32_16x16x32_bf16 a[48:51],a[224:227],v[112:115],a[48:51]
ds_read_b64_tr_b16 a[212:213],v2 offset:8448
ds_read_b64_tr_b16 a[214:215],v2 offset:12544
v_mfma_f32_16x16x32_bf16 a[48:51],a[228:231],v[120:123],a[48:51]
v_exp_f32 v67,v67
v_mfma_f32_16x16x32_bf16 a[52:55],a[232:235],v[112:115],a[52:55]
v_exp_f32 v68,v68
v_mfma_f32_16x16x32_bf16 a[52:55],a[236:239],v[120:123],a[52:55]
v_exp_f32 v69,v69
v_mfma_f32_16x16x32_bf16 a[56:59],a[240:243],v[112:115],a[56:59]
v_exp_f32 v70,v70
v_mfma_f32_16x16x32_bf16 a[56:59],a[244:247],v[120:123],a[56:59]
v_exp_f32 v71,v71
v_mfma_f32_16x16x32_bf16 a[60:63],a[248:251],v[112:115],a[60:63]
global_load_lds_dwordx4 v4,s[10:11] offset:2048
ds_read_b64_tr_b16 a[216:217],v3 offset:256
v_mfma_f32_16x16x32_bf16 a[60:63],a[252:255],v[120:123],a[60:63]
ds_read_b64_tr_b16 a[218:219],v3 offset:4352
v_cvt_pk_bf16_f32 v65,v66,v67
v_mfma_f32_16x16x32_bf16 v[60:63],v[28:31],v[112:115],v[60:63]
s_nop 0
L_pd10_%=:
s_cbranch_execz L_t1b_%=
v_cvt_pk_bf16_f32 v66,v68,v69
v_cvt_pk_bf16_f32 v67,v70,v71
v_mfma_f32_16x16x32_bf16 v[60:63],v[28:31],v[120:123],v[60:63]
v_exp_f32 v72,v72
v_mfma_f32_16x16x32_bf16 v[96:99],a[128:131],v[192:195],v[40:43]
v_exp_f32 v73,v73
v_mfma_f32_16x16x32_bf16 v[96:99],a[132:135],v[200:203],v[96:99]
v_exp_f32 v74,v74
v_mfma_f32_16x16x32_bf16 v[96:99],a[136:139],v[208:211],v[96:99]
v_exp_f32 v75,v75
v_mfma_f32_16x16x32_bf16 v[96:99],a[140:143],v[216:219],v[96:99]
v_exp_f32 v76,v76
v_mfma_f32_16x16x32_bf16 v[100:103],a[156:159],v[216:219],v[40:43]
v_exp_f32 v77,v77
v_mfma_f32_16x16x32_bf16 v[100:103],a[152:155],v[208:211],v[100:103]
v_exp_f32 v78,v78
v_mfma_f32_16x16x32_bf16 v[100:103],a[148:151],v[200:203],v[100:103]
v_exp_f32 v79,v79
v_mfma_f32_16x16x32_bf16 v[100:103],a[144:147],v[192:195],v[100:103]
ds_read_b64_tr_b16 a[220:221],v3 offset:8448
ds_read_b64_tr_b16 a[222:223],v3 offset:12544
v_mfma_f32_16x16x32_bf16 v[104:107],a[160:163],v[192:195],v[40:43]
v_cvt_pk_bf16_f32 v72,v72,v73
v_cvt_pk_bf16_f32 v73,v74,v75
v_mfma_f32_16x16x32_bf16 v[104:107],a[164:167],v[200:203],v[104:107]
v_cvt_pk_bf16_f32 v74,v76,v77
v_cvt_pk_bf16_f32 v75,v78,v79
v_mfma_f32_16x16x32_bf16 v[104:107],a[168:171],v[208:211],v[104:107]
ds_read_b64_tr_b16 a[224:225],v2 offset:512
ds_read_b64_tr_b16 a[226:227],v2 offset:4608
v_mfma_f32_16x16x32_bf16 v[104:107],a[172:175],v[216:219],v[104:107]
ds_read_b64_tr_b16 a[228:229],v2 offset:8704
v_accvgpr_read_b32 v18,a90
v_mfma_f32_16x16x32_bf16 v[108:111],a[188:191],v[216:219],v[40:43]
global_load_lds_dwordx4 v5,s[10:11] offset:3072
ds_read_b64_tr_b16 a[230:231],v2 offset:12800
v_mfma_f32_16x16x32_bf16 v[108:111],a[184:187],v[208:211],v[108:111]
ds_read_b64_tr_b16 a[232:233],v3 offset:512
v_accvgpr_read_b32 v19,a91
v_mfma_f32_16x16x32_bf16 v[108:111],a[180:183],v[200:203],v[108:111]
ds_read_b64_tr_b16 a[234:235],v3 offset:4608
ds_read_b64_tr_b16 a[236:237],v3 offset:8704
v_mfma_f32_16x16x32_bf16 v[108:111],a[176:179],v[192:195],v[108:111]
s_waitcnt lgkmcnt(7)
ds_read_b64_tr_b16 a[238:239],v3 offset:12800
v_mul_f32 v16,v16,v26
v_mfma_f32_16x16x32_bf16 a[0:3],a[192:195],v[64:67],a[0:3]
ds_read_b64_tr_b16 a[240:241],v2 offset:768
v_mul_f32 v17,v17,v26
v_mfma_f32_16x16x32_bf16 a[0:3],a[196:199],v[72:75],a[0:3]
ds_read_b64_tr_b16 a[242:243],v2 offset:4864
v_mul_f32 v18,v18,v26
v_mfma_f32_16x16x32_bf16 a[4:7],a[200:203],v[64:67],a[4:7]
ds_read_b64_tr_b16 a[244:245],v2 offset:8960
ds_read_b64_tr_b16 a[246:247],v2 offset:13056
v_mfma_f32_16x16x32_bf16 a[4:7],a[204:207],v[72:75],a[4:7]
ds_read_b64_tr_b16 a[248:249],v3 offset:768
v_mul_f32 v19,v19,v26
v_mfma_f32_16x16x32_bf16 a[8:11],a[208:211],v[64:67],a[8:11]
ds_read_b64_tr_b16 a[250:251],v3 offset:4864
v_mul_f32 v20,v20,v26
v_mfma_f32_16x16x32_bf16 a[8:11],a[212:215],v[72:75],a[8:11]
v_mul_f32 v21,v21,v26
v_mul_f32 v22,v22,v26
)A"
R"A(v_mul_f32 v23,v23,v26
v_cvt_pk_bf16_f32 v16,v16,v17
v_cvt_pk_bf16_f32 v17,v18,v19
v_cvt_pk_bf16_f32 v18,v20,v21
v_cvt_pk_bf16_f32 v19,v22,v23
v_accvgpr_write_b32 a88,v16
v_accvgpr_write_b32 a89,v17
v_accvgpr_write_b32 a90,v18
v_accvgpr_write_b32 a91,v19
v_mfma_f32_16x16x32_bf16 a[12:15],a[216:219],v[64:67],a[12:15]
ds_read_b128 a[92:95],v7 offset:1024
v_mov_b32 v8,v7
v_mfma_f32_16x16x32_bf16 a[12:15],a[220:223],v[72:75],a[12:15]
s_waitcnt lgkmcnt(7)
ds_read_b64_tr_b16 a[252:253],v3 offset:8960
v_add_u32 v1,s78,v1
v_mfma_f32_16x16x32_bf16 a[16:19],a[224:227],v[64:67],a[16:19]
ds_read_b64_tr_b16 a[254:255],v3 offset:13056
v_mfma_f32_16x16x32_bf16 a[16:19],a[228:231],v[72:75],a[16:19]
s_waitcnt lgkmcnt(4)
v_exp_f32 v80,v80
v_mfma_f32_16x16x32_bf16 a[20:23],a[232:235],v[64:67],a[20:23]
s_getreg_b32 s92,hwreg(HW_REG_IB_STS,0,4)
s_min_u32 s4,s92,s6
s_mov_b32 s6,s92
v_mfma_f32_16x16x32_bf16 a[20:23],a[236:239],v[72:75],a[20:23]
s_cmp_ge_u32 s4,s9
s_cselect_b32 s4,15,s5
ds_read_b128 v[24:27],v8 offset:4096
v_mfma_f32_16x16x32_bf16 a[24:27],a[240:243],v[64:67],a[24:27]
v_exp_f32 v81,v81
v_mfma_f32_16x16x32_bf16 a[24:27],a[244:247],v[72:75],a[24:27]
s_waitcnt lgkmcnt(4)
v_exp_f32 v82,v82
v_mfma_f32_16x16x32_bf16 a[28:31],a[248:251],v[64:67],a[28:31]
s_waitcnt lgkmcnt(1)
v_exp_f32 v83,v83
v_mfma_f32_16x16x32_bf16 a[28:31],a[252:255],v[72:75],a[28:31]
v_exp_f32 v84,v84
v_mfma_f32_16x16x32_bf16 v[48:51],v[28:31],v[64:67],v[48:51]
s_getpc_b64 s[92:93]
s_cmp_lg_u32 s4,0
s_cbranch_scc1 L_pace_%=
v_exp_f32 v85,v85
v_exp_f32 v86,v86
v_exp_f32 v87,v87
v_exp_f32 v88,v88
v_exp_f32 v89,v89
v_exp_f32 v90,v90
v_exp_f32 v91,v91
v_exp_f32 v92,v92
v_exp_f32 v93,v93
v_exp_f32 v94,v94
v_exp_f32 v95,v95
v_mfma_f32_16x16x32_bf16 v[48:51],v[28:31],v[72:75],v[48:51]
ds_read_b32 v7,v8 offset:5120
v_cvt_pk_bf16_f32 v80,v80,v81
v_cvt_pk_bf16_f32 v81,v82,v83
v_cvt_pk_bf16_f32 v82,v84,v85
v_cvt_pk_bf16_f32 v83,v86,v87
v_cvt_pk_bf16_f32 v88,v88,v89
v_cvt_pk_bf16_f32 v89,v90,v91
v_cvt_pk_bf16_f32 v90,v92,v93
v_cvt_pk_bf16_f32 v91,v94,v95
s_mov_b32 s70,7
L_a1_%=:
s_waitcnt vmcnt(16)
s_barrier
v_mfma_f32_16x16x32_bf16 v[112:115],a[128:131],v[224:227],v[44:47]
s_add_u32 s10,s20,s74
s_addc_u32 s11,s21,0
s_add_u32 m0,s76,0
v_mfma_f32_16x16x32_bf16 v[112:115],a[132:135],v[232:235],v[112:115]
s_add_u32 s75,s75,16384
s_and_b32 s69,s75,0xfffc0000
ds_read_b128 a[128:131],v1
v_mfma_f32_16x16x32_bf16 v[112:115],a[136:139],v[240:243],v[112:115]
global_load_lds_dwordx4 v4,s[10:11] nt
v_mfma_f32_16x16x32_bf16 v[112:115],a[140:143],v[248:251],v[112:115]
ds_read_b128 a[132:135],v1 offset:256
v_mfma_f32_16x16x32_bf16 v[116:119],a[156:159],v[248:251],v[44:47]
v_mfma_f32_16x16x32_bf16 v[116:119],a[152:155],v[240:243],v[116:119]
ds_read_b128 a[136:139],v1 offset:512
v_mfma_f32_16x16x32_bf16 v[116:119],a[148:151],v[232:235],v[116:119]
v_mfma_f32_16x16x32_bf16 v[116:119],a[144:147],v[224:227],v[116:119]
ds_read_b128 a[140:143],v1 offset:768
v_mfma_f32_16x16x32_bf16 v[120:123],a[160:163],v[224:227],v[44:47]
v_mfma_f32_16x16x32_bf16 v[120:123],a[164:167],v[232:235],v[120:123]
ds_read_b128 a[156:159],v1 offset:4864
v_mfma_f32_16x16x32_bf16 v[120:123],a[168:171],v[240:243],v[120:123]
v_mfma_f32_16x16x32_bf16 v[120:123],a[172:175],v[248:251],v[120:123]
ds_read_b128 a[152:155],v1 offset:4608
v_mfma_f32_16x16x32_bf16 v[124:127],a[188:191],v[248:251],v[44:47]
v_exp_f32 v96,v96
v_mfma_f32_16x16x32_bf16 v[124:127],a[184:187],v[240:243],v[124:127]
ds_read_b128 a[148:151],v1 offset:4352
v_add_u32 v2,s78,v2
v_mfma_f32_16x16x32_bf16 v[124:127],a[180:183],v[232:235],v[124:127]
v_exp_f32 v97,v97
v_mfma_f32_16x16x32_bf16 v[124:127],a[176:179],v[224:227],v[124:127]
ds_read_b128 a[144:147],v1 offset:4096
v_cvt_pk_bf16_f32 v96,v96,v97
v_mfma_f32_16x16x32_bf16 v[132:135],a[192:195],v[80:83],v[132:135]
v_exp_f32 v98,v98
v_mfma_f32_16x16x32_bf16 v[132:135],a[196:199],v[88:91],v[132:135]
ds_read_b128 a[160:163],v1 offset:8192
v_add_u32 v3,s78,v3
v_mfma_f32_16x16x32_bf16 v[140:143],a[200:203],v[80:83],v[140:143]
s_sub_u32 s78,s77,32768
global_load_lds_dwordx4 v4,s[10:11] offset:1024 nt
v_mfma_f32_16x16x32_bf16 v[140:143],a[204:207],v[88:91],v[140:143]
s_and_b32 s78,s78,0x1ffff
v_exp_f32 v99,v99
v_mfma_f32_16x16x32_bf16 v[148:151],a[208:211],v[80:83],v[148:151]
s_sub_u32 s78,s78,s77
ds_read_b128 a[164:167],v1 offset:8448
v_cvt_pk_bf16_f32 v97,v98,v99
v_mfma_f32_16x16x32_bf16 v[148:151],a[212:215],v[88:91],v[148:151]
s_add_u32 s77,s77,s78
v_exp_f32 v100,v100
v_mfma_f32_16x16x32_bf16 v[156:159],a[216:219],v[80:83],v[156:159]
v_exp_f32 v101,v101
v_mfma_f32_16x16x32_bf16 v[156:159],a[220:223],v[88:91],v[156:159]
ds_read_b128 a[168:171],v1 offset:8704
v_cvt_pk_bf16_f32 v98,v100,v101
v_mfma_f32_16x16x32_bf16 v[164:167],a[224:227],v[80:83],v[164:167]
v_exp_f32 v102,v102
v_mfma_f32_16x16x32_bf16 v[164:167],a[228:231],v[88:91],v[164:167]
v_exp_f32 v103,v103
v_mfma_f32_16x16x32_bf16 v[172:175],a[232:235],v[80:83],v[172:175]
ds_read_b128 a[172:175],v1 offset:8960
v_cvt_pk_bf16_f32 v99,v102,v103
v_mfma_f32_16x16x32_bf16 v[172:175],a[236:239],v[88:91],v[172:175]
v_exp_f32 v104,v104
v_mfma_f32_16x16x32_bf16 v[180:183],a[240:243],v[80:83],v[180:183]
v_exp_f32 v105,v105
v_mfma_f32_16x16x32_bf16 v[180:183],a[244:247],v[88:91],v[180:183]
ds_read_b128 a[188:191],v1 offset:13056
v_cvt_pk_bf16_f32 v104,v104,v105
v_mfma_f32_16x16x32_bf16 v[188:191],a[248:251],v[80:83],v[188:191]
v_exp_f32 v106,v106
v_mfma_f32_16x16x32_bf16 v[188:191],a[252:255],v[88:91],v[188:191]
v_exp_f32 v107,v107
v_mfma_f32_16x16x32_bf16 v[52:55],v[28:31],v[80:83],v[52:55]
ds_read_b128 a[184:187],v1 offset:12800
v_cvt_pk_bf16_f32 v105,v106,v107
v_mfma_f32_16x16x32_bf16 v[52:55],v[28:31],v[88:91],v[52:55]
s_waitcnt lgkmcnt(3)
v_exp_f32 v108,v108
v_mfma_f32_16x16x32_bf16 v[64:67],a[128:131],v[128:131],v[32:35]
global_load_lds_dwordx4 v5,s[10:11] offset:2048 nt
ds_read_b128 a[180:183],v1 offset:12544
v_mfma_f32_16x16x32_bf16 v[64:67],a[132:135],v[136:139],v[64:67]
v_exp_f32 v109,v109
v_mfma_f32_16x16x32_bf16 v[64:67],a[136:139],v[144:147],v[64:67]
ds_read_b128 a[176:179],v1 offset:12288
v_cvt_pk_bf16_f32 v106,v108,v109
v_mfma_f32_16x16x32_bf16 v[64:67],a[140:143],v[152:155],v[64:67]
v_exp_f32 v110,v110
v_mfma_f32_16x16x32_bf16 v[68:71],a[156:159],v[152:155],v[32:35]
v_exp_f32 v111,v111
v_mfma_f32_16x16x32_bf16 v[68:71],a[152:155],v[144:147],v[68:71]
v_cvt_pk_bf16_f32 v107,v110,v111
v_add_u32 v1,s78,v1
v_mfma_f32_16x16x32_bf16 v[68:71],a[148:151],v[136:139],v[68:71]
v_exp_f32 v112,v112
v_mfma_f32_16x16x32_bf16 v[68:71],a[144:147],v[128:131],v[68:71]
v_exp_f32 v113,v113
v_mfma_f32_16x16x32_bf16 v[72:75],a[160:163],v[128:131],v[32:35]
v_exp_f32 v114,v114
v_mfma_f32_16x16x32_bf16 v[72:75],a[164:167],v[136:139],v[72:75]
v_exp_f32 v115,v115
v_mfma_f32_16x16x32_bf16 v[72:75],a[168:171],v[144:147],v[72:75]
s_waitcnt lgkmcnt(0)
v_exp_f32 v116,v116
v_mfma_f32_16x16x32_bf16 v[72:75],a[172:175],v[152:155],v[72:75]
v_exp_f32 v117,v117
v_mfma_f32_16x16x32_bf16 v[76:79],a[188:191],v[152:155],v[32:35]
v_exp_f32 v118,v118
v_mfma_f32_16x16x32_bf16 v[76:79],a[184:187],v[144:147],v[76:79]
v_exp_f32 v119,v119
v_mfma_f32_16x16x32_bf16 v[76:79],a[180:183],v[136:139],v[76:79]
v_cvt_pk_bf16_f32 v112,v112,v113
v_cvt_pk_bf16_f32 v113,v114,v115
v_mfma_f32_16x16x32_bf16 v[76:79],a[176:179],v[128:131],v[76:79]
v_cvt_pk_bf16_f32 v114,v116,v117
v_cvt_pk_bf16_f32 v115,v118,v119
v_mfma_f32_16x16x32_bf16 v[196:199],a[192:195],v[96:99],v[196:199]
global_load_lds_dwordx4 v5,s[10:11] offset:3072
v_mfma_f32_16x16x32_bf16 v[196:199],a[196:199],v[104:107],v[196:199]
v_exp_f32 v120,v120
v_mfma_f32_16x16x32_bf16 v[204:207],a[200:203],v[96:99],v[204:207]
v_exp_f32 v121,v121
v_mfma_f32_16x16x32_bf16 v[204:207],a[204:207],v[104:107],v[204:207]
v_exp_f32 v122,v122
v_mfma_f32_16x16x32_bf16 v[212:215],a[208:211],v[96:99],v[212:215]
v_exp_f32 v123,v123
v_mfma_f32_16x16x32_bf16 v[212:215],a[212:215],v[104:107],v[212:215]
v_exp_f32 v124,v124
v_mfma_f32_16x16x32_bf16 v[220:223],a[216:219],v[96:99],v[220:223]
v_exp_f32 v125,v125
v_mfma_f32_16x16x32_bf16 v[220:223],a[220:223],v[104:107],v[220:223]
v_exp_f32 v126,v126
v_mfma_f32_16x16x32_bf16 v[228:231],a[224:227],v[96:99],v[228:231]
v_exp_f32 v127,v127
v_mfma_f32_16x16x32_bf16 v[228:231],a[228:231],v[104:107],v[228:231]
v_cvt_pk_bf16_f32 v120,v120,v121
v_cvt_pk_bf16_f32 v121,v122,v123
v_mfma_f32_16x16x32_bf16 v[236:239],a[232:235],v[96:99],v[236:239]
v_cvt_pk_bf16_f32 v122,v124,v125
v_cvt_pk_bf16_f32 v123,v126,v127
v_mfma_f32_16x16x32_bf16 v[236:239],a[236:239],v[104:107],v[236:239]
v_exp_f32 v64,v64
v_mfma_f32_16x16x32_bf16 v[244:247],a[240:243],v[96:99],v[244:247]
s_add_u32 s10,s24,s74
s_addc_u32 s11,s25,0
s_add_u32 s74,s74,16384
v_mfma_f32_16x16x32_bf16 v[244:247],a[244:247],v[104:107],v[244:247]
s_and_b32 s74,s74,0x3ffff
v_exp_f32 v65,v65
v_mfma_f32_16x16x32_bf16 v[252:255],a[248:251],v[96:99],v[252:255]
s_add_u32 m0,s76,16384
v_exp_f32 v66,v66
v_mfma_f32_16x16x32_bf16 v[252:255],a[252:255],v[104:107],v[252:255]
s_or_b32 s74,s74,s69
v_exp_f32 v67,v67
v_mfma_f32_16x16x32_bf16 v[56:59],v[28:31],v[96:99],v[56:59]
global_load_lds_dwordx4 v4,s[10:11]
v_cvt_pk_bf16_f32 v64,v64,v65
v_mfma_f32_16x16x32_bf16 v[56:59],v[28:31],v[104:107],v[56:59]
s_sub_u32 s76,s76,32768
v_exp_f32 v68,v68
v_mfma_f32_16x16x32_bf16 v[80:83],a[128:131],v[160:163],v[36:39]
s_and_b32 s76,s76,0x1ffff
v_exp_f32 v69,v69
v_mfma_f32_16x16x32_bf16 v[80:83],a[132:135],v[168:171],v[80:83]
v_exp_f32 v70,v70
v_mfma_f32_16x16x32_bf16 v[80:83],a[136:139],v[176:179],v[80:83]
v_exp_f32 v71,v71
v_mfma_f32_16x16x32_bf16 v[80:83],a[140:143],v[184:187],v[80:83]
v_cvt_pk_bf16_f32 v65,v66,v67
v_cvt_pk_bf16_f32 v66,v68,v69
v_mfma_f32_16x16x32_bf16 v[84:87],a[156:159],v[184:187],v[36:39]
v_exp_f32 v72,v72
v_mfma_f32_16x16x32_bf16 v[84:87],a[152:155],v[176:179],v[84:87]
v_exp_f32 v73,v73
v_mfma_f32_16x16x32_bf16 v[84:87],a[148:151],v[168:171],v[84:87]
v_cvt_pk_bf16_f32 v67,v70,v71
v_cvt_pk_bf16_f32 v72,v72,v73
v_mfma_f32_16x16x32_bf16 v[84:87],a[144:147],v[160:163],v[84:87]
v_exp_f32 v74,v74
v_mfma_f32_16x16x32_bf16 v[88:91],a[160:163],v[160:163],v[36:39]
v_exp_f32 v75,v75
v_mfma_f32_16x16x32_bf16 v[88:91],a[164:167],v[168:171],v[88:91]
v_exp_f32 v76,v76
v_mfma_f32_16x16x32_bf16 v[88:91],a[168:171],v[176:179],v[88:91]
v_exp_f32 v77,v77
v_mfma_f32_16x16x32_bf16 v[88:91],a[172:175],v[184:187],v[88:91]
v_exp_f32 v78,v78
v_mfma_f32_16x16x32_bf16 v[92:95],a[188:191],v[184:187],v[36:39]
v_exp_f32 v79,v79
v_mfma_f32_16x16x32_bf16 v[92:95],a[184:187],v[176:179],v[92:95]
v_cvt_pk_bf16_f32 v73,v74,v75
v_cvt_pk_bf16_f32 v74,v76,v77
v_mfma_f32_16x16x32_bf16 v[92:95],a[180:183],v[168:171],v[92:95]
global_load_lds_dwordx4 v5,s[10:11] offset:1024
v_cvt_pk_bf16_f32 v75,v78,v79
v_mfma_f32_16x16x32_bf16 v[92:95],a[176:179],v[160:163],v[92:95]
v_exp_f32 v80,v80
v_mfma_f32_16x16x32_bf16 a[32:35],a[192:195],v[112:115],a[32:35]
ds_read_b64_tr_b16 a[192:193],v2
ds_read_b64_tr_b16 a[194:195],v2 offset:4096
v_mfma_f32_16x16x32_bf16 a[32:35],a[196:199],v[120:123],a[32:35]
v_exp_f32 v81,v81
v_mfma_f32_16x16x32_bf16 a[36:39],a[200:203],v[112:115],a[36:39]
ds_read_b64_tr_b16 a[196:197],v2 offset:8192
ds_read_b64_tr_b16 a[198:199],v2 offset:12288
v_mfma_f32_16x16x32_bf16 a[36:39],a[204:207],v[120:123],a[36:39]
ds_read_b64_tr_b16 a[200:201],v3
v_cvt_pk_bf16_f32 v80,v80,v81
v_mfma_f32_16x16x32_bf16 a[40:43],a[208:211],v[112:115],a[40:43]
v_exp_f32 v82,v82
v_mfma_f32_16x16x32_bf16 a[40:43],a[212:215],v[120:123],a[40:43]
ds_read_b64_tr_b16 a[202:203],v3 offset:4096
ds_read_b64_tr_b16 a[204:205],v3 offset:8192
v_mfma_f32_16x16x32_bf16 a[44:47],a[216:219],v[112:115],a[44:47]
v_exp_f32 v83,v83
v_mfma_f32_16x16x32_bf16 a[44:47],a[220:223],v[120:123],a[44:47]
ds_read_b64_tr_b16 a[206:207],v3 offset:12288
ds_read_b64_tr_b16 a[208:209],v2 offset:256
v_mfma_f32_16x16x32_bf16 a[48:51],a[224:227],v[112:115],a[48:51]
ds_read_b64_tr_b16 a[210:211],v2 offset:4352
v_cvt_pk_bf16_f32 v81,v82,v83
v_mfma_f32_16x16x32_bf16 a[48:51],a[228:231],v[120:123],a[48:51]
v_exp_f32 v84,v84
v_mfma_f32_16x16x32_bf16 a[52:55],a[232:235],v[112:115],a[52:55]
)A"
R"A(ds_read_b64_tr_b16 a[212:213],v2 offset:8448
ds_read_b64_tr_b16 a[214:215],v2 offset:12544
v_mfma_f32_16x16x32_bf16 a[52:55],a[236:239],v[120:123],a[52:55]
v_exp_f32 v85,v85
v_mfma_f32_16x16x32_bf16 a[56:59],a[240:243],v[112:115],a[56:59]
ds_read_b64_tr_b16 a[216:217],v3 offset:256
ds_read_b64_tr_b16 a[218:219],v3 offset:4352
v_mfma_f32_16x16x32_bf16 a[56:59],a[244:247],v[120:123],a[56:59]
ds_read_b64_tr_b16 a[220:221],v3 offset:8448
v_cvt_pk_bf16_f32 v82,v84,v85
v_mfma_f32_16x16x32_bf16 a[60:63],a[248:251],v[112:115],a[60:63]
global_load_lds_dwordx4 v4,s[10:11] offset:2048
ds_read_b64_tr_b16 a[222:223],v3 offset:12544
v_mfma_f32_16x16x32_bf16 a[60:63],a[252:255],v[120:123],a[60:63]
v_exp_f32 v86,v86
v_mfma_f32_16x16x32_bf16 v[60:63],v[28:31],v[112:115],v[60:63]
ds_read_b64_tr_b16 a[224:225],v2 offset:512
ds_read_b64_tr_b16 a[226:227],v2 offset:4608
v_mfma_f32_16x16x32_bf16 v[60:63],v[28:31],v[120:123],v[60:63]
v_exp_f32 v87,v87
v_mfma_f32_16x16x32_bf16 v[96:99],a[128:131],v[192:195],v[40:43]
ds_read_b64_tr_b16 a[228:229],v2 offset:8704
ds_read_b64_tr_b16 a[230:231],v2 offset:12800
v_mfma_f32_16x16x32_bf16 v[96:99],a[132:135],v[200:203],v[96:99]
ds_read_b64_tr_b16 a[232:233],v3 offset:512
v_cvt_pk_bf16_f32 v83,v86,v87
v_mfma_f32_16x16x32_bf16 v[96:99],a[136:139],v[208:211],v[96:99]
v_exp_f32 v88,v88
v_mfma_f32_16x16x32_bf16 v[96:99],a[140:143],v[216:219],v[96:99]
ds_read_b64_tr_b16 a[234:235],v3 offset:4608
ds_read_b64_tr_b16 a[236:237],v3 offset:8704
v_mfma_f32_16x16x32_bf16 v[100:103],a[156:159],v[216:219],v[40:43]
v_exp_f32 v89,v89
v_mfma_f32_16x16x32_bf16 v[100:103],a[152:155],v[208:211],v[100:103]
ds_read_b64_tr_b16 a[238:239],v3 offset:12800
ds_read_b64_tr_b16 a[240:241],v2 offset:768
v_mfma_f32_16x16x32_bf16 v[100:103],a[148:151],v[200:203],v[100:103]
ds_read_b64_tr_b16 a[242:243],v2 offset:4864
v_cvt_pk_bf16_f32 v88,v88,v89
v_mfma_f32_16x16x32_bf16 v[100:103],a[144:147],v[192:195],v[100:103]
v_exp_f32 v90,v90
v_mfma_f32_16x16x32_bf16 v[104:107],a[160:163],v[192:195],v[40:43]
ds_read_b64_tr_b16 a[244:245],v2 offset:8960
ds_read_b64_tr_b16 a[246:247],v2 offset:13056
v_mfma_f32_16x16x32_bf16 v[104:107],a[164:167],v[200:203],v[104:107]
v_exp_f32 v91,v91
v_mfma_f32_16x16x32_bf16 v[104:107],a[168:171],v[208:211],v[104:107]
ds_read_b64_tr_b16 a[248:249],v3 offset:768
ds_read_b64_tr_b16 a[250:251],v3 offset:4864
v_mfma_f32_16x16x32_bf16 v[104:107],a[172:175],v[216:219],v[104:107]
ds_read_b64_tr_b16 a[252:253],v3 offset:8960
v_cvt_pk_bf16_f32 v89,v90,v91
v_mfma_f32_16x16x32_bf16 v[108:111],a[188:191],v[216:219],v[40:43]
global_load_lds_dwordx4 v5,s[10:11] offset:3072
ds_read_b64_tr_b16 a[254:255],v3 offset:13056
v_mfma_f32_16x16x32_bf16 v[108:111],a[184:187],v[208:211],v[108:111]
v_exp_f32 v92,v92
v_mfma_f32_16x16x32_bf16 v[108:111],a[180:183],v[200:203],v[108:111]
v_exp_f32 v93,v93
v_mfma_f32_16x16x32_bf16 v[108:111],a[176:179],v[192:195],v[108:111]
s_waitcnt lgkmcnt(6)
v_exp_f32 v94,v94
v_mfma_f32_16x16x32_bf16 a[0:3],a[192:195],v[64:67],a[0:3]
v_exp_f32 v95,v95
v_mfma_f32_16x16x32_bf16 a[0:3],a[196:199],v[72:75],a[0:3]
v_cvt_pk_bf16_f32 v90,v92,v93
v_cvt_pk_bf16_f32 v91,v94,v95
v_mfma_f32_16x16x32_bf16 a[4:7],a[200:203],v[64:67],a[4:7]
v_mfma_f32_16x16x32_bf16 a[4:7],a[204:207],v[72:75],a[4:7]
v_mfma_f32_16x16x32_bf16 a[8:11],a[208:211],v[64:67],a[8:11]
v_mfma_f32_16x16x32_bf16 a[8:11],a[212:215],v[72:75],a[8:11]
v_mfma_f32_16x16x32_bf16 a[12:15],a[216:219],v[64:67],a[12:15]
v_mfma_f32_16x16x32_bf16 a[12:15],a[220:223],v[72:75],a[12:15]
v_mfma_f32_16x16x32_bf16 a[16:19],a[224:227],v[64:67],a[16:19]
v_mfma_f32_16x16x32_bf16 a[16:19],a[228:231],v[72:75],a[16:19]
v_mfma_f32_16x16x32_bf16 a[20:23],a[232:235],v[64:67],a[20:23]
s_getreg_b32 s92,hwreg(HW_REG_IB_STS,0,4)
s_min_u32 s4,s92,s6
s_mov_b32 s6,s92
v_mfma_f32_16x16x32_bf16 a[20:23],a[236:239],v[72:75],a[20:23]
s_cmp_ge_u32 s4,s9
s_cselect_b32 s4,15,s5
v_mfma_f32_16x16x32_bf16 a[24:27],a[240:243],v[64:67],a[24:27]
s_waitcnt lgkmcnt(0)
v_mfma_f32_16x16x32_bf16 a[24:27],a[244:247],v[72:75],a[24:27]
v_mfma_f32_16x16x32_bf16 a[28:31],a[248:251],v[64:67],a[28:31]
v_mfma_f32_16x16x32_bf16 a[28:31],a[252:255],v[72:75],a[28:31]
s_getpc_b64 s[92:93]
s_cmp_lg_u32 s4,0
s_cbranch_scc1 L_pace_%=
v_mfma_f32_16x16x32_bf16 v[48:51],v[28:31],v[64:67],v[48:51]
v_mfma_f32_16x16x32_bf16 v[48:51],v[28:31],v[72:75],v[48:51]
s_sub_u32 s70,s70,1
s_cbranch_scc0 L_a1_%=
L_nx1_%=:
s_waitcnt vmcnt(16)
s_barrier
v_mfma_f32_16x16x32_bf16 v[112:115],a[128:131],v[224:227],v[44:47]
s_mul_i32 s86,s60,10240
s_mul_i32 s85,s63,1310720
s_mul_i32 s66,s64,320
v_mfma_f32_16x16x32_bf16 v[112:115],a[132:135],v[232:235],v[112:115]
s_add_u32 s85,s85,s66
s_add_u32 s83,s64,0
s_and_b32 s83,s83,3
v_mfma_f32_16x16x32_bf16 v[112:115],a[136:139],v[240:243],v[112:115]
s_lshl_b32 s83,s83,1
s_mul_i32 s83,s83,163840
s_add_u32 s83,s83,s85
v_mfma_f32_16x16x32_bf16 v[112:115],a[140:143],v[248:251],v[112:115]
s_cmp_eq_u32 s60,3
s_cselect_b32 s69,0x40000000,0
v_add_u32 v14,s69,v6
v_mfma_f32_16x16x32_bf16 v[116:119],a[156:159],v[248:251],v[44:47]
s_add_u32 s84,s83,20480
ds_read_b128 a[128:131],v1
v_add_u32 v2,s78,v2
v_mfma_f32_16x16x32_bf16 v[116:119],a[152:155],v[240:243],v[116:119]
s_add_u32 m0,s86,122880
v_exp_f32 v96,v96
v_mfma_f32_16x16x32_bf16 v[116:119],a[148:151],v[232:235],v[116:119]
s_getpc_b64 s[8:9]
L_pcg_%=:
ds_read_b128 a[132:135],v1 offset:256
v_add_u32 v3,s78,v3
v_mfma_f32_16x16x32_bf16 v[116:119],a[144:147],v[224:227],v[116:119]
buffer_load_dwordx4 v24,s[48:51],s83 offen lds
v_mfma_f32_16x16x32_bf16 v[120:123],a[160:163],v[224:227],v[44:47]
s_sub_u32 s78,s77,32768
s_and_b32 s78,s78,0x1ffff
ds_read_b128 a[136:139],v1 offset:512
v_mfma_f32_16x16x32_bf16 v[120:123],a[164:167],v[232:235],v[120:123]
buffer_store_dwordx4 a[112:115],v14,s[40:43],s81 offen offset:3072 sc1
v_mfma_f32_16x16x32_bf16 v[120:123],a[168:171],v[240:243],v[120:123]
s_sub_u32 s78,s78,s77
ds_read_b128 a[140:143],v1 offset:768
v_lshlrev_b32 v4,10,v6
v_mfma_f32_16x16x32_bf16 v[120:123],a[172:175],v[248:251],v[120:123]
s_add_u32 s77,s77,s78
v_exp_f32 v97,v97
v_mfma_f32_16x16x32_bf16 v[124:127],a[188:191],v[248:251],v[44:47]
s_mov_b32 s6,L_gbF_%=-L_pcg_%=
ds_read_b128 a[156:159],v1 offset:4864
v_cvt_pk_bf16_f32 v96,v96,v97
v_mfma_f32_16x16x32_bf16 v[124:127],a[184:187],v[240:243],v[124:127]
s_mov_b32 s10,L_geF_%=-L_gbF_%=
v_exp_f32 v98,v98
v_mfma_f32_16x16x32_bf16 v[124:127],a[180:183],v[232:235],v[124:127]
s_mov_b32 s11,0x20000
s_lshr_b32 s7,%1,3
ds_read_b128 a[152:155],v1 offset:4608
v_mfma_f32_16x16x32_bf16 v[124:127],a[176:179],v[224:227],v[124:127]
s_lshl_b32 s7,s7,2
v_exp_f32 v99,v99
v_mfma_f32_16x16x32_bf16 v[132:135],a[192:195],v[80:83],v[132:135]
s_add_u32 s7,s7,s60
ds_read_b128 a[148:151],v1 offset:4352
v_cvt_pk_bf16_f32 v97,v98,v99
v_mfma_f32_16x16x32_bf16 v[132:135],a[196:199],v[88:91],v[132:135]
buffer_load_dwordx4 v25,s[48:51],s83 offen offset:1024 lds
v_mfma_f32_16x16x32_bf16 v[140:143],a[200:203],v[80:83],v[140:143]
s_lshl_b32 s7,s7,7
ds_read_b128 a[144:147],v1 offset:4096
v_mov_b32 v8,s52
v_mfma_f32_16x16x32_bf16 v[140:143],a[204:207],v[88:91],v[140:143]
s_lshl_b32 s66,s61,5
v_exp_f32 v100,v100
v_mfma_f32_16x16x32_bf16 v[148:151],a[208:211],v[80:83],v[148:151]
s_lshl_b32 s67,s63,3
ds_read_b128 a[160:163],v1 offset:8192
v_mov_b32 v9,s53
v_mfma_f32_16x16x32_bf16 v[148:151],a[212:215],v[88:91],v[148:151]
s_add_u32 s66,s66,s67
v_exp_f32 v101,v101
v_mfma_f32_16x16x32_bf16 v[156:159],a[216:219],v[80:83],v[156:159]
s_lshl_b32 s66,s66,16
ds_read_b128 a[164:167],v1 offset:8448
v_cvt_pk_bf16_f32 v98,v100,v101
v_mfma_f32_16x16x32_bf16 v[156:159],a[220:223],v[88:91],v[156:159]
s_lshl_b32 s67,s60,14
v_exp_f32 v102,v102
v_mfma_f32_16x16x32_bf16 v[164:167],a[224:227],v[80:83],v[164:167]
s_add_u32 s66,s66,s67
ds_read_b128 a[168:171],v1 offset:8704
v_lshlrev_b32 v10,20,v6
v_mfma_f32_16x16x32_bf16 v[164:167],a[228:231],v[88:91],v[164:167]
s_mov_b32 s30,1523712
v_exp_f32 v103,v103
v_mfma_f32_16x16x32_bf16 v[172:175],a[232:235],v[80:83],v[172:175]
buffer_store_dwordx4 a[116:119],v6,s[40:43],s81 offen offset:1024 sc1
v_mfma_f32_16x16x32_bf16 v[172:175],a[236:239],v[88:91],v[172:175]
s_mov_b32 s31,0x20000
ds_read_b128 a[172:175],v1 offset:8960
v_cvt_pk_bf16_f32 v99,v102,v103
v_mfma_f32_16x16x32_bf16 v[180:183],a[240:243],v[80:83],v[180:183]
buffer_load_dwordx4 v26,s[48:51],s83 offen offset:2048 lds
v_mfma_f32_16x16x32_bf16 v[180:183],a[244:247],v[88:91],v[180:183]
v_exp_f32 v104,v104
v_mfma_f32_16x16x32_bf16 v[188:191],a[248:251],v[80:83],v[188:191]
v_exp_f32 v105,v105
v_mfma_f32_16x16x32_bf16 v[188:191],a[252:255],v[88:91],v[188:191]
ds_read_b128 a[188:191],v1 offset:13056
v_cvt_pk_bf16_f32 v104,v104,v105
v_mfma_f32_16x16x32_bf16 v[52:55],v[28:31],v[80:83],v[52:55]
v_exp_f32 v106,v106
v_mfma_f32_16x16x32_bf16 v[52:55],v[28:31],v[88:91],v[52:55]
s_waitcnt lgkmcnt(2)
v_exp_f32 v107,v107
v_mfma_f32_16x16x32_bf16 v[64:67],a[128:131],v[128:131],v[32:35]
ds_read_b128 a[184:187],v1 offset:12800
v_cvt_pk_bf16_f32 v105,v106,v107
v_mfma_f32_16x16x32_bf16 v[64:67],a[132:135],v[136:139],v[64:67]
v_exp_f32 v108,v108
v_mfma_f32_16x16x32_bf16 v[64:67],a[136:139],v[144:147],v[64:67]
s_add_u32 s28,s12,s66
s_addc_u32 s29,s13,0
ds_read_b128 a[180:183],v1 offset:12544
v_mfma_f32_16x16x32_bf16 v[64:67],a[140:143],v[152:155],v[64:67]
buffer_load_dwordx4 v27,s[48:51],s83 offen offset:3072 lds
v_mfma_f32_16x16x32_bf16 v[68:71],a[156:159],v[152:155],v[32:35]
s_and_b32 s29,s29,0xffff
ds_read_b128 a[176:179],v1 offset:12288
v_add_u32 v1,s78,v1
v_mfma_f32_16x16x32_bf16 v[68:71],a[152:155],v[144:147],v[68:71]
v_exp_f32 v109,v109
v_mfma_f32_16x16x32_bf16 v[68:71],a[148:151],v[136:139],v[68:71]
v_exp_f32 v110,v110
v_mfma_f32_16x16x32_bf16 v[68:71],a[144:147],v[128:131],v[68:71]
v_exp_f32 v111,v111
v_mfma_f32_16x16x32_bf16 v[72:75],a[160:163],v[128:131],v[32:35]
v_cvt_pk_bf16_f32 v106,v108,v109
v_cvt_pk_bf16_f32 v107,v110,v111
v_mfma_f32_16x16x32_bf16 v[72:75],a[164:167],v[136:139],v[72:75]
buffer_store_dwordx4 a[120:123],v14,s[40:43],s82 offen offset:3072 sc1
v_mfma_f32_16x16x32_bf16 v[72:75],a[168:171],v[144:147],v[72:75]
s_waitcnt lgkmcnt(1)
v_exp_f32 v112,v112
v_mfma_f32_16x16x32_bf16 v[72:75],a[172:175],v[152:155],v[72:75]
s_add_u32 m0,s86,126976
v_exp_f32 v113,v113
v_mfma_f32_16x16x32_bf16 v[76:79],a[188:191],v[152:155],v[32:35]
s_waitcnt lgkmcnt(0)
v_exp_f32 v114,v114
v_mfma_f32_16x16x32_bf16 v[76:79],a[184:187],v[144:147],v[76:79]
buffer_load_dwordx4 v7,s[48:51],s83 offen lds
v_mfma_f32_16x16x32_bf16 v[76:79],a[180:183],v[136:139],v[76:79]
s_add_u32 s83,s64,0
v_exp_f32 v115,v115
v_mfma_f32_16x16x32_bf16 v[76:79],a[176:179],v[128:131],v[76:79]
s_and_b32 s83,s83,3
v_exp_f32 v116,v116
v_mfma_f32_16x16x32_bf16 v[196:199],a[192:195],v[96:99],v[196:199]
s_lshl_b32 s83,s83,1
v_exp_f32 v117,v117
L_pd11_%=:
s_cbranch_execz L_t1c_%=
v_mfma_f32_16x16x32_bf16 v[196:199],a[196:199],v[104:107],v[196:199]
s_or_b32 s83,s83,1
v_exp_f32 v118,v118
v_mfma_f32_16x16x32_bf16 v[204:207],a[200:203],v[96:99],v[204:207]
s_mul_i32 s83,s83,163840
v_exp_f32 v119,v119
v_mfma_f32_16x16x32_bf16 v[204:207],a[204:207],v[104:107],v[204:207]
s_add_u32 s83,s83,s85
v_cvt_pk_bf16_f32 v112,v112,v113
v_cvt_pk_bf16_f32 v113,v114,v115
v_mfma_f32_16x16x32_bf16 v[212:215],a[208:211],v[96:99],v[212:215]
v_cvt_pk_bf16_f32 v114,v116,v117
v_cvt_pk_bf16_f32 v115,v118,v119
v_mfma_f32_16x16x32_bf16 v[212:215],a[212:215],v[104:107],v[212:215]
s_add_u32 m0,s86,128000
v_exp_f32 v120,v120
v_mfma_f32_16x16x32_bf16 v[220:223],a[216:219],v[96:99],v[220:223]
v_exp_f32 v121,v121
v_mfma_f32_16x16x32_bf16 v[220:223],a[220:223],v[104:107],v[220:223]
buffer_load_dwordx4 v24,s[48:51],s84 offen lds
v_mfma_f32_16x16x32_bf16 v[228:231],a[224:227],v[96:99],v[228:231]
v_exp_f32 v122,v122
v_mfma_f32_16x16x32_bf16 v[228:231],a[228:231],v[104:107],v[228:231]
v_exp_f32 v123,v123
v_mfma_f32_16x16x32_bf16 v[236:239],a[232:235],v[96:99],v[236:239]
buffer_store_dwordx4 a[124:127],v6,s[40:43],s82 offen offset:1024 sc1
v_mfma_f32_16x16x32_bf16 v[236:239],a[236:239],v[104:107],v[236:239]
v_exp_f32 v124,v124
v_mfma_f32_16x16x32_bf16 v[244:247],a[240:243],v[96:99],v[244:247]
)A"
R"A(v_exp_f32 v125,v125
v_mfma_f32_16x16x32_bf16 v[244:247],a[244:247],v[104:107],v[244:247]
v_exp_f32 v126,v126
v_mfma_f32_16x16x32_bf16 v[252:255],a[248:251],v[96:99],v[252:255]
v_exp_f32 v127,v127
v_mfma_f32_16x16x32_bf16 v[252:255],a[252:255],v[104:107],v[252:255]
v_cvt_pk_bf16_f32 v120,v120,v121
v_cvt_pk_bf16_f32 v121,v122,v123
v_mfma_f32_16x16x32_bf16 v[56:59],v[28:31],v[96:99],v[56:59]
v_cvt_pk_bf16_f32 v122,v124,v125
v_cvt_pk_bf16_f32 v123,v126,v127
v_mfma_f32_16x16x32_bf16 v[56:59],v[28:31],v[104:107],v[56:59]
buffer_load_dwordx4 v25,s[48:51],s84 offen offset:1024 lds
v_mfma_f32_16x16x32_bf16 v[80:83],a[128:131],v[160:163],v[36:39]
v_exp_f32 v64,v64
v_mfma_f32_16x16x32_bf16 v[80:83],a[132:135],v[168:171],v[80:83]
v_exp_f32 v65,v65
v_mfma_f32_16x16x32_bf16 v[80:83],a[136:139],v[176:179],v[80:83]
v_exp_f32 v66,v66
v_mfma_f32_16x16x32_bf16 v[80:83],a[140:143],v[184:187],v[80:83]
v_exp_f32 v67,v67
v_mfma_f32_16x16x32_bf16 v[84:87],a[156:159],v[184:187],v[36:39]
v_exp_f32 v68,v68
v_mfma_f32_16x16x32_bf16 v[84:87],a[152:155],v[176:179],v[84:87]
v_exp_f32 v69,v69
v_mfma_f32_16x16x32_bf16 v[84:87],a[148:151],v[168:171],v[84:87]
v_exp_f32 v70,v70
v_mfma_f32_16x16x32_bf16 v[84:87],a[144:147],v[160:163],v[84:87]
v_exp_f32 v71,v71
v_mfma_f32_16x16x32_bf16 v[88:91],a[160:163],v[160:163],v[36:39]
v_cvt_pk_bf16_f32 v64,v64,v65
v_cvt_pk_bf16_f32 v65,v66,v67
v_mfma_f32_16x16x32_bf16 v[88:91],a[164:167],v[168:171],v[88:91]
buffer_store_dwordx4 a[96:99],v14,s[40:43],s79 offen offset:3072 sc1
v_mfma_f32_16x16x32_bf16 v[88:91],a[168:171],v[176:179],v[88:91]
v_cvt_pk_bf16_f32 v66,v68,v69
v_cvt_pk_bf16_f32 v67,v70,v71
v_mfma_f32_16x16x32_bf16 v[88:91],a[172:175],v[184:187],v[88:91]
buffer_load_dwordx4 v26,s[48:51],s84 offen offset:2048 lds
v_mfma_f32_16x16x32_bf16 v[92:95],a[188:191],v[184:187],v[36:39]
v_exp_f32 v72,v72
v_mfma_f32_16x16x32_bf16 v[92:95],a[184:187],v[176:179],v[92:95]
v_exp_f32 v73,v73
v_mfma_f32_16x16x32_bf16 v[92:95],a[180:183],v[168:171],v[92:95]
v_exp_f32 v74,v74
v_mfma_f32_16x16x32_bf16 v[92:95],a[176:179],v[160:163],v[92:95]
v_exp_f32 v75,v75
v_mfma_f32_16x16x32_bf16 a[32:35],a[192:195],v[112:115],a[32:35]
ds_read_b64_tr_b16 a[192:193],v2
ds_read_b64_tr_b16 a[194:195],v2 offset:4096
v_mfma_f32_16x16x32_bf16 a[32:35],a[196:199],v[120:123],a[32:35]
ds_read_b64_tr_b16 a[196:197],v2 offset:8192
v_cvt_pk_bf16_f32 v72,v72,v73
v_mfma_f32_16x16x32_bf16 a[36:39],a[200:203],v[112:115],a[36:39]
ds_read_b64_tr_b16 a[198:199],v2 offset:12288
v_cvt_pk_bf16_f32 v73,v74,v75
v_mfma_f32_16x16x32_bf16 a[36:39],a[204:207],v[120:123],a[36:39]
buffer_load_dwordx4 v27,s[48:51],s84 offen offset:3072 lds
v_mfma_f32_16x16x32_bf16 a[40:43],a[208:211],v[112:115],a[40:43]
ds_read_b64_tr_b16 a[200:201],v3
ds_read_b64_tr_b16 a[202:203],v3 offset:4096
v_mfma_f32_16x16x32_bf16 a[40:43],a[212:215],v[120:123],a[40:43]
v_exp_f32 v76,v76
v_mfma_f32_16x16x32_bf16 a[44:47],a[216:219],v[112:115],a[44:47]
ds_read_b64_tr_b16 a[204:205],v3 offset:8192
ds_read_b64_tr_b16 a[206:207],v3 offset:12288
v_mfma_f32_16x16x32_bf16 a[44:47],a[220:223],v[120:123],a[44:47]
v_exp_f32 v77,v77
v_mfma_f32_16x16x32_bf16 a[48:51],a[224:227],v[112:115],a[48:51]
ds_read_b64_tr_b16 a[208:209],v2 offset:256
ds_read_b64_tr_b16 a[210:211],v2 offset:4352
v_mfma_f32_16x16x32_bf16 a[48:51],a[228:231],v[120:123],a[48:51]
ds_read_b64_tr_b16 a[212:213],v2 offset:8448
v_cvt_pk_bf16_f32 v74,v76,v77
v_mfma_f32_16x16x32_bf16 a[52:55],a[232:235],v[112:115],a[52:55]
buffer_store_dwordx4 a[100:103],v6,s[40:43],s79 offen offset:1024 sc1
v_mfma_f32_16x16x32_bf16 a[52:55],a[236:239],v[120:123],a[52:55]
s_add_u32 m0,s86,132096
ds_read_b64_tr_b16 a[214:215],v2 offset:12544
ds_read_b64_tr_b16 a[216:217],v3 offset:256
v_mfma_f32_16x16x32_bf16 a[56:59],a[240:243],v[112:115],a[56:59]
v_exp_f32 v78,v78
v_mfma_f32_16x16x32_bf16 a[56:59],a[244:247],v[120:123],a[56:59]
buffer_load_dwordx4 v7,s[48:51],s84 offen lds
v_mfma_f32_16x16x32_bf16 a[60:63],a[248:251],v[112:115],a[60:63]
s_add_u32 s84,s83,20480
ds_read_b64_tr_b16 a[218:219],v3 offset:4352
ds_read_b64_tr_b16 a[220:221],v3 offset:8448
v_mfma_f32_16x16x32_bf16 a[60:63],a[252:255],v[120:123],a[60:63]
v_exp_f32 v79,v79
v_mfma_f32_16x16x32_bf16 v[60:63],v[28:31],v[112:115],v[60:63]
ds_read_b64_tr_b16 a[222:223],v3 offset:12544
v_cvt_pk_bf16_f32 v75,v78,v79
v_mfma_f32_16x16x32_bf16 v[60:63],v[28:31],v[120:123],v[60:63]
ds_read_b64_tr_b16 a[224:225],v2 offset:512
ds_read_b64_tr_b16 a[226:227],v2 offset:4608
v_mfma_f32_16x16x32_bf16 v[96:99],a[128:131],v[192:195],v[40:43]
v_exp_f32 v80,v80
v_mfma_f32_16x16x32_bf16 v[96:99],a[132:135],v[200:203],v[96:99]
ds_read_b64_tr_b16 a[228:229],v2 offset:8704
ds_read_b64_tr_b16 a[230:231],v2 offset:12800
v_mfma_f32_16x16x32_bf16 v[96:99],a[136:139],v[208:211],v[96:99]
v_exp_f32 v81,v81
v_mfma_f32_16x16x32_bf16 v[96:99],a[140:143],v[216:219],v[96:99]
ds_read_b64_tr_b16 a[232:233],v3 offset:512
ds_read_b64_tr_b16 a[234:235],v3 offset:4608
v_mfma_f32_16x16x32_bf16 v[100:103],a[156:159],v[216:219],v[40:43]
ds_read_b64_tr_b16 a[236:237],v3 offset:8704
v_cvt_pk_bf16_f32 v80,v80,v81
v_mfma_f32_16x16x32_bf16 v[100:103],a[152:155],v[208:211],v[100:103]
v_exp_f32 v82,v82
v_mfma_f32_16x16x32_bf16 v[100:103],a[148:151],v[200:203],v[100:103]
ds_read_b64_tr_b16 a[238:239],v3 offset:12800
ds_read_b64_tr_b16 a[240:241],v2 offset:768
v_mfma_f32_16x16x32_bf16 v[100:103],a[144:147],v[192:195],v[100:103]
v_exp_f32 v83,v83
v_mfma_f32_16x16x32_bf16 v[104:107],a[160:163],v[192:195],v[40:43]
ds_read_b64_tr_b16 a[242:243],v2 offset:4864
ds_read_b64_tr_b16 a[244:245],v2 offset:8960
v_mfma_f32_16x16x32_bf16 v[104:107],a[164:167],v[200:203],v[104:107]
buffer_store_dwordx4 a[104:107],v14,s[40:43],s80 offen offset:3072 sc1
v_mfma_f32_16x16x32_bf16 v[104:107],a[168:171],v[208:211],v[104:107]
ds_read_b64_tr_b16 a[246:247],v2 offset:13056
ds_read_b64_tr_b16 a[248:249],v3 offset:768
v_mfma_f32_16x16x32_bf16 v[104:107],a[172:175],v[216:219],v[104:107]
ds_read_b64_tr_b16 a[250:251],v3 offset:4864
v_cvt_pk_bf16_f32 v81,v82,v83
v_mfma_f32_16x16x32_bf16 v[108:111],a[188:191],v[216:219],v[40:43]
ds_read_b64_tr_b16 a[252:253],v3 offset:8960
v_add_u32 v2,s78,v2
v_mfma_f32_16x16x32_bf16 v[108:111],a[184:187],v[208:211],v[108:111]
s_waitcnt lgkmcnt(6)
ds_read_b64_tr_b16 a[254:255],v3 offset:13056
v_add_u32 v3,s78,v3
v_mfma_f32_16x16x32_bf16 v[108:111],a[180:183],v[200:203],v[108:111]
s_sub_u32 s78,s77,32768
v_exp_f32 v84,v84
v_mfma_f32_16x16x32_bf16 v[108:111],a[176:179],v[192:195],v[108:111]
s_and_b32 s78,s78,0x1ffff
v_exp_f32 v85,v85
v_mfma_f32_16x16x32_bf16 a[0:3],a[192:195],v[64:67],a[0:3]
s_sub_u32 s78,s78,s77
v_exp_f32 v86,v86
v_mfma_f32_16x16x32_bf16 a[0:3],a[196:199],v[72:75],a[0:3]
s_add_u32 s77,s77,s78
v_exp_f32 v87,v87
v_mfma_f32_16x16x32_bf16 a[4:7],a[200:203],v[64:67],a[4:7]
v_cvt_pk_bf16_f32 v82,v84,v85
v_cvt_pk_bf16_f32 v83,v86,v87
v_mfma_f32_16x16x32_bf16 a[4:7],a[204:207],v[72:75],a[4:7]
v_exp_f32 v88,v88
v_mfma_f32_16x16x32_bf16 a[8:11],a[208:211],v[64:67],a[8:11]
v_exp_f32 v89,v89
v_mfma_f32_16x16x32_bf16 a[8:11],a[212:215],v[72:75],a[8:11]
v_exp_f32 v90,v90
v_mfma_f32_16x16x32_bf16 a[12:15],a[216:219],v[64:67],a[12:15]
v_exp_f32 v91,v91
v_mfma_f32_16x16x32_bf16 a[12:15],a[220:223],v[72:75],a[12:15]
v_exp_f32 v92,v92
v_mfma_f32_16x16x32_bf16 a[16:19],a[224:227],v[64:67],a[16:19]
v_exp_f32 v93,v93
v_mfma_f32_16x16x32_bf16 a[16:19],a[228:231],v[72:75],a[16:19]
v_exp_f32 v94,v94
v_mfma_f32_16x16x32_bf16 a[20:23],a[232:235],v[64:67],a[20:23]
buffer_store_dwordx4 a[108:111],v6,s[40:43],s80 offen offset:1024 sc1
v_mfma_f32_16x16x32_bf16 a[20:23],a[236:239],v[72:75],a[20:23]
s_waitcnt lgkmcnt(0)
v_exp_f32 v95,v95
v_mfma_f32_16x16x32_bf16 a[24:27],a[240:243],v[64:67],a[24:27]
v_cvt_pk_bf16_f32 v88,v88,v89
v_cvt_pk_bf16_f32 v89,v90,v91
v_mfma_f32_16x16x32_bf16 a[24:27],a[244:247],v[72:75],a[24:27]
v_cvt_pk_bf16_f32 v90,v92,v93
v_cvt_pk_bf16_f32 v91,v94,v95
v_mfma_f32_16x16x32_bf16 a[28:31],a[248:251],v[64:67],a[28:31]
v_exp_f32 v96,v96
v_mfma_f32_16x16x32_bf16 a[28:31],a[252:255],v[72:75],a[28:31]
v_exp_f32 v97,v97
v_mfma_f32_16x16x32_bf16 v[48:51],v[28:31],v[64:67],v[48:51]
v_exp_f32 v98,v98
v_mfma_f32_16x16x32_bf16 v[48:51],v[28:31],v[72:75],v[48:51]
v_exp_f32 v99,v99
s_waitcnt vmcnt(26)
s_barrier
v_mfma_f32_16x16x32_bf16 v[112:115],a[128:131],v[224:227],v[44:47]
ds_read_b128 a[128:131],v1
v_cvt_pk_bf16_f32 v96,v96,v97
v_mfma_f32_16x16x32_bf16 v[112:115],a[132:135],v[232:235],v[112:115]
v_exp_f32 v100,v100
v_mfma_f32_16x16x32_bf16 v[112:115],a[136:139],v[240:243],v[112:115]
ds_read_b128 a[132:135],v1 offset:256
v_cvt_pk_bf16_f32 v97,v98,v99
v_mfma_f32_16x16x32_bf16 v[112:115],a[140:143],v[248:251],v[112:115]
v_exp_f32 v101,v101
v_mfma_f32_16x16x32_bf16 v[116:119],a[156:159],v[248:251],v[44:47]
ds_read_b128 a[136:139],v1 offset:512
v_cvt_pk_bf16_f32 v98,v100,v101
v_mfma_f32_16x16x32_bf16 v[116:119],a[152:155],v[240:243],v[116:119]
s_add_u32 m0,s86,81920
v_exp_f32 v102,v102
v_mfma_f32_16x16x32_bf16 v[116:119],a[148:151],v[232:235],v[116:119]
v_exp_f32 v103,v103
v_mfma_f32_16x16x32_bf16 v[116:119],a[144:147],v[224:227],v[116:119]
buffer_load_dwordx4 v24,s[48:51],s83 offen lds
v_mfma_f32_16x16x32_bf16 v[120:123],a[160:163],v[224:227],v[44:47]
ds_read_b128 a[140:143],v1 offset:768
v_cvt_pk_bf16_f32 v99,v102,v103
v_mfma_f32_16x16x32_bf16 v[120:123],a[164:167],v[232:235],v[120:123]
buffer_store_dwordx4 a[64:67],v6,s[40:43],s79 offen offset:2048 sc1
v_mfma_f32_16x16x32_bf16 v[120:123],a[168:171],v[240:243],v[120:123]
v_exp_f32 v104,v104
v_mfma_f32_16x16x32_bf16 v[120:123],a[172:175],v[248:251],v[120:123]
v_exp_f32 v105,v105
v_mfma_f32_16x16x32_bf16 v[124:127],a[188:191],v[248:251],v[44:47]
ds_read_b128 a[156:159],v1 offset:4864
v_cvt_pk_bf16_f32 v104,v104,v105
v_mfma_f32_16x16x32_bf16 v[124:127],a[184:187],v[240:243],v[124:127]
v_exp_f32 v106,v106
v_mfma_f32_16x16x32_bf16 v[124:127],a[180:183],v[232:235],v[124:127]
v_exp_f32 v107,v107
v_mfma_f32_16x16x32_bf16 v[124:127],a[176:179],v[224:227],v[124:127]
ds_read_b128 a[152:155],v1 offset:4608
v_cvt_pk_bf16_f32 v105,v106,v107
v_mfma_f32_16x16x32_bf16 v[132:135],a[192:195],v[80:83],v[132:135]
v_exp_f32 v108,v108
v_mfma_f32_16x16x32_bf16 v[132:135],a[196:199],v[88:91],v[132:135]
buffer_load_dwordx4 v25,s[48:51],s83 offen offset:1024 lds
v_mfma_f32_16x16x32_bf16 v[140:143],a[200:203],v[80:83],v[140:143]
v_exp_f32 v109,v109
v_mfma_f32_16x16x32_bf16 v[140:143],a[204:207],v[88:91],v[140:143]
ds_read_b128 a[148:151],v1 offset:4352
v_cvt_pk_bf16_f32 v106,v108,v109
v_mfma_f32_16x16x32_bf16 v[148:151],a[208:211],v[80:83],v[148:151]
v_exp_f32 v110,v110
v_mfma_f32_16x16x32_bf16 v[148:151],a[212:215],v[88:91],v[148:151]
v_exp_f32 v111,v111
v_mfma_f32_16x16x32_bf16 v[156:159],a[216:219],v[80:83],v[156:159]
ds_read_b128 a[144:147],v1 offset:4096
v_cvt_pk_bf16_f32 v107,v110,v111
v_mfma_f32_16x16x32_bf16 v[156:159],a[220:223],v[88:91],v[156:159]
v_exp_f32 v112,v112
v_mfma_f32_16x16x32_bf16 v[164:167],a[224:227],v[80:83],v[164:167]
v_exp_f32 v113,v113
v_mfma_f32_16x16x32_bf16 v[164:167],a[228:231],v[88:91],v[164:167]
buffer_store_dwordx4 a[68:71],v6,s[40:43],s79 offen sc1
v_mfma_f32_16x16x32_bf16 v[172:175],a[232:235],v[80:83],v[172:175]
s_add_u32 s79,s79,65536
ds_read_b128 a[160:163],v1 offset:8192
v_cvt_pk_bf16_f32 v112,v112,v113
v_mfma_f32_16x16x32_bf16 v[172:175],a[236:239],v[88:91],v[172:175]
buffer_load_dwordx4 v26,s[48:51],s83 offen offset:2048 lds
v_mfma_f32_16x16x32_bf16 v[180:183],a[240:243],v[80:83],v[180:183]
v_exp_f32 v114,v114
v_mfma_f32_16x16x32_bf16 v[180:183],a[244:247],v[88:91],v[180:183]
v_exp_f32 v115,v115
v_mfma_f32_16x16x32_bf16 v[188:191],a[248:251],v[80:83],v[188:191]
ds_read_b128 a[164:167],v1 offset:8448
v_cvt_pk_bf16_f32 v113,v114,v115
v_mfma_f32_16x16x32_bf16 v[188:191],a[252:255],v[88:91],v[188:191]
v_exp_f32 v116,v116
v_mfma_f32_16x16x32_bf16 v[52:55],v[28:31],v[80:83],v[52:55]
ds_read_b128 a[168:171],v1 offset:8704
v_mfma_f32_16x16x32_bf16 v[52:55],v[28:31],v[88:91],v[52:55]
s_waitcnt lgkmcnt(3)
v_exp_f32 v117,v117
v_mfma_f32_16x16x32_bf16 v[64:67],a[128:131],v[128:131],v[32:35]
)A"
R"A(ds_read_b128 a[172:175],v1 offset:8960
v_cvt_pk_bf16_f32 v114,v116,v117
v_mfma_f32_16x16x32_bf16 v[64:67],a[132:135],v[136:139],v[64:67]
v_exp_f32 v118,v118
v_mfma_f32_16x16x32_bf16 v[64:67],a[136:139],v[144:147],v[64:67]
ds_read_b128 a[188:191],v1 offset:13056
v_mfma_f32_16x16x32_bf16 v[64:67],a[140:143],v[152:155],v[64:67]
buffer_load_dwordx4 v27,s[48:51],s83 offen offset:3072 lds
v_mfma_f32_16x16x32_bf16 v[68:71],a[156:159],v[152:155],v[32:35]
ds_read_b128 a[184:187],v1 offset:12800
v_mfma_f32_16x16x32_bf16 v[68:71],a[152:155],v[144:147],v[68:71]
v_exp_f32 v119,v119
v_mfma_f32_16x16x32_bf16 v[68:71],a[148:151],v[136:139],v[68:71]
ds_read_b128 a[180:183],v1 offset:12544
v_cvt_pk_bf16_f32 v115,v118,v119
v_mfma_f32_16x16x32_bf16 v[68:71],a[144:147],v[128:131],v[68:71]
s_waitcnt lgkmcnt(4)
v_exp_f32 v120,v120
v_mfma_f32_16x16x32_bf16 v[72:75],a[160:163],v[128:131],v[32:35]
buffer_store_dwordx4 a[72:75],v6,s[40:43],s80 offen offset:2048 sc1
v_mfma_f32_16x16x32_bf16 v[72:75],a[164:167],v[136:139],v[72:75]
ds_read_b128 a[176:179],v1 offset:12288
v_add_u32 v1,s78,v1
v_mfma_f32_16x16x32_bf16 v[72:75],a[168:171],v[144:147],v[72:75]
s_waitcnt lgkmcnt(3)
v_exp_f32 v121,v121
v_mfma_f32_16x16x32_bf16 v[72:75],a[172:175],v[152:155],v[72:75]
s_add_u32 m0,s86,86016
v_exp_f32 v122,v122
v_mfma_f32_16x16x32_bf16 v[76:79],a[188:191],v[152:155],v[32:35]
s_waitcnt lgkmcnt(0)
v_exp_f32 v123,v123
v_mfma_f32_16x16x32_bf16 v[76:79],a[184:187],v[144:147],v[76:79]
buffer_load_dwordx4 v7,s[48:51],s83 offen lds
v_mfma_f32_16x16x32_bf16 v[76:79],a[180:183],v[136:139],v[76:79]
s_add_u32 s83,s64,1
v_exp_f32 v124,v124
v_mfma_f32_16x16x32_bf16 v[76:79],a[176:179],v[128:131],v[76:79]
s_and_b32 s83,s83,3
v_exp_f32 v125,v125
v_mfma_f32_16x16x32_bf16 v[196:199],a[192:195],v[96:99],v[196:199]
s_lshl_b32 s83,s83,1
v_exp_f32 v126,v126
v_mfma_f32_16x16x32_bf16 v[196:199],a[196:199],v[104:107],v[196:199]
s_mul_i32 s83,s83,163840
v_exp_f32 v127,v127
v_mfma_f32_16x16x32_bf16 v[204:207],a[200:203],v[96:99],v[204:207]
s_add_u32 s83,s83,s85
v_cvt_pk_bf16_f32 v120,v120,v121
v_cvt_pk_bf16_f32 v121,v122,v123
v_mfma_f32_16x16x32_bf16 v[204:207],a[204:207],v[104:107],v[204:207]
v_cvt_pk_bf16_f32 v122,v124,v125
v_cvt_pk_bf16_f32 v123,v126,v127
v_mfma_f32_16x16x32_bf16 v[212:215],a[208:211],v[96:99],v[212:215]
v_exp_f32 v64,v64
v_mfma_f32_16x16x32_bf16 v[212:215],a[212:215],v[104:107],v[212:215]
s_add_u32 m0,s86,87040
v_exp_f32 v65,v65
v_mfma_f32_16x16x32_bf16 v[220:223],a[216:219],v[96:99],v[220:223]
v_exp_f32 v66,v66
v_mfma_f32_16x16x32_bf16 v[220:223],a[220:223],v[104:107],v[220:223]
buffer_load_dwordx4 v24,s[48:51],s84 offen lds
v_mfma_f32_16x16x32_bf16 v[228:231],a[224:227],v[96:99],v[228:231]
v_exp_f32 v67,v67
v_mfma_f32_16x16x32_bf16 v[228:231],a[228:231],v[104:107],v[228:231]
buffer_store_dwordx4 a[76:79],v6,s[40:43],s80 offen sc1
v_mfma_f32_16x16x32_bf16 v[236:239],a[232:235],v[96:99],v[236:239]
s_add_u32 s80,s80,65536
v_exp_f32 v68,v68
v_mfma_f32_16x16x32_bf16 v[236:239],a[236:239],v[104:107],v[236:239]
v_exp_f32 v69,v69
v_mfma_f32_16x16x32_bf16 v[244:247],a[240:243],v[96:99],v[244:247]
v_exp_f32 v70,v70
v_mfma_f32_16x16x32_bf16 v[244:247],a[244:247],v[104:107],v[244:247]
v_exp_f32 v71,v71
v_mfma_f32_16x16x32_bf16 v[252:255],a[248:251],v[96:99],v[252:255]
v_cvt_pk_bf16_f32 v64,v64,v65
v_cvt_pk_bf16_f32 v65,v66,v67
v_mfma_f32_16x16x32_bf16 v[252:255],a[252:255],v[104:107],v[252:255]
v_cvt_pk_bf16_f32 v66,v68,v69
v_cvt_pk_bf16_f32 v67,v70,v71
v_mfma_f32_16x16x32_bf16 v[56:59],v[28:31],v[96:99],v[56:59]
v_exp_f32 v72,v72
v_mfma_f32_16x16x32_bf16 v[56:59],v[28:31],v[104:107],v[56:59]
buffer_load_dwordx4 v25,s[48:51],s84 offen offset:1024 lds
v_mfma_f32_16x16x32_bf16 v[80:83],a[128:131],v[160:163],v[36:39]
v_exp_f32 v73,v73
v_mfma_f32_16x16x32_bf16 v[80:83],a[132:135],v[168:171],v[80:83]
v_exp_f32 v74,v74
v_mfma_f32_16x16x32_bf16 v[80:83],a[136:139],v[176:179],v[80:83]
v_exp_f32 v75,v75
v_mfma_f32_16x16x32_bf16 v[80:83],a[140:143],v[184:187],v[80:83]
v_exp_f32 v76,v76
v_mfma_f32_16x16x32_bf16 v[84:87],a[156:159],v[184:187],v[36:39]
v_exp_f32 v77,v77
v_mfma_f32_16x16x32_bf16 v[84:87],a[152:155],v[176:179],v[84:87]
v_exp_f32 v78,v78
v_mfma_f32_16x16x32_bf16 v[84:87],a[148:151],v[168:171],v[84:87]
v_exp_f32 v79,v79
v_mfma_f32_16x16x32_bf16 v[84:87],a[144:147],v[160:163],v[84:87]
v_cvt_pk_bf16_f32 v72,v72,v73
v_cvt_pk_bf16_f32 v73,v74,v75
v_mfma_f32_16x16x32_bf16 v[88:91],a[160:163],v[160:163],v[36:39]
buffer_store_dwordx4 a[80:83],v6,s[40:43],s81 offen offset:2048 sc1
v_mfma_f32_16x16x32_bf16 v[88:91],a[164:167],v[168:171],v[88:91]
v_cvt_pk_bf16_f32 v74,v76,v77
v_cvt_pk_bf16_f32 v75,v78,v79
v_mfma_f32_16x16x32_bf16 v[88:91],a[168:171],v[176:179],v[88:91]
buffer_load_dwordx4 v26,s[48:51],s84 offen offset:2048 lds
v_mfma_f32_16x16x32_bf16 v[88:91],a[172:175],v[184:187],v[88:91]
v_exp_f32 v80,v80
v_mfma_f32_16x16x32_bf16 v[92:95],a[188:191],v[184:187],v[36:39]
v_exp_f32 v81,v81
v_mfma_f32_16x16x32_bf16 v[92:95],a[184:187],v[176:179],v[92:95]
v_exp_f32 v82,v82
v_mfma_f32_16x16x32_bf16 v[92:95],a[180:183],v[168:171],v[92:95]
v_exp_f32 v83,v83
v_mfma_f32_16x16x32_bf16 v[92:95],a[176:179],v[160:163],v[92:95]
v_exp_f32 v84,v84
v_mfma_f32_16x16x32_bf16 a[32:35],a[192:195],v[112:115],a[32:35]
ds_read_b64_tr_b16 a[192:193],v2
ds_read_b64_tr_b16 a[194:195],v2 offset:4096
v_mfma_f32_16x16x32_bf16 a[32:35],a[196:199],v[120:123],a[32:35]
ds_read_b64_tr_b16 a[196:197],v2 offset:8192
v_cvt_pk_bf16_f32 v80,v80,v81
v_mfma_f32_16x16x32_bf16 a[36:39],a[200:203],v[112:115],a[36:39]
ds_read_b64_tr_b16 a[198:199],v2 offset:12288
v_cvt_pk_bf16_f32 v81,v82,v83
v_mfma_f32_16x16x32_bf16 a[36:39],a[204:207],v[120:123],a[36:39]
buffer_load_dwordx4 v27,s[48:51],s84 offen offset:3072 lds
v_mfma_f32_16x16x32_bf16 a[40:43],a[208:211],v[112:115],a[40:43]
ds_read_b64_tr_b16 a[200:201],v3
ds_read_b64_tr_b16 a[202:203],v3 offset:4096
v_mfma_f32_16x16x32_bf16 a[40:43],a[212:215],v[120:123],a[40:43]
v_exp_f32 v85,v85
v_mfma_f32_16x16x32_bf16 a[44:47],a[216:219],v[112:115],a[44:47]
ds_read_b64_tr_b16 a[204:205],v3 offset:8192
ds_read_b64_tr_b16 a[206:207],v3 offset:12288
v_mfma_f32_16x16x32_bf16 a[44:47],a[220:223],v[120:123],a[44:47]
ds_read_b64_tr_b16 a[208:209],v2 offset:256
v_cvt_pk_bf16_f32 v82,v84,v85
v_mfma_f32_16x16x32_bf16 a[48:51],a[224:227],v[112:115],a[48:51]
v_exp_f32 v86,v86
v_mfma_f32_16x16x32_bf16 a[48:51],a[228:231],v[120:123],a[48:51]
buffer_store_dwordx4 a[84:87],v6,s[40:43],s81 offen sc1
v_mfma_f32_16x16x32_bf16 a[52:55],a[232:235],v[112:115],a[52:55]
s_add_u32 s81,s81,65536
ds_read_b64_tr_b16 a[210:211],v2 offset:4352
ds_read_b64_tr_b16 a[212:213],v2 offset:8448
v_mfma_f32_16x16x32_bf16 a[52:55],a[236:239],v[120:123],a[52:55]
s_add_u32 m0,s86,91136
v_exp_f32 v87,v87
v_mfma_f32_16x16x32_bf16 a[56:59],a[240:243],v[112:115],a[56:59]
ds_read_b64_tr_b16 a[214:215],v2 offset:12544
ds_read_b64_tr_b16 a[216:217],v3 offset:256
v_mfma_f32_16x16x32_bf16 a[56:59],a[244:247],v[120:123],a[56:59]
buffer_load_dwordx4 v7,s[48:51],s84 offen lds
v_mfma_f32_16x16x32_bf16 a[60:63],a[248:251],v[112:115],a[60:63]
s_add_u32 s84,s83,20480
ds_read_b64_tr_b16 a[218:219],v3 offset:4352
ds_read_b64_tr_b16 a[220:221],v3 offset:8448
v_mfma_f32_16x16x32_bf16 a[60:63],a[252:255],v[120:123],a[60:63]
ds_read_b64_tr_b16 a[222:223],v3 offset:12544
v_cvt_pk_bf16_f32 v83,v86,v87
v_mfma_f32_16x16x32_bf16 v[60:63],v[28:31],v[112:115],v[60:63]
v_exp_f32 v88,v88
v_mfma_f32_16x16x32_bf16 v[60:63],v[28:31],v[120:123],v[60:63]
ds_read_b64_tr_b16 a[224:225],v2 offset:512
ds_read_b64_tr_b16 a[226:227],v2 offset:4608
v_mfma_f32_16x16x32_bf16 v[96:99],a[128:131],v[192:195],v[40:43]
v_exp_f32 v89,v89
v_mfma_f32_16x16x32_bf16 v[96:99],a[132:135],v[200:203],v[96:99]
ds_read_b64_tr_b16 a[228:229],v2 offset:8704
ds_read_b64_tr_b16 a[230:231],v2 offset:12800
v_mfma_f32_16x16x32_bf16 v[96:99],a[136:139],v[208:211],v[96:99]
ds_read_b64_tr_b16 a[232:233],v3 offset:512
v_cvt_pk_bf16_f32 v88,v88,v89
v_mfma_f32_16x16x32_bf16 v[96:99],a[140:143],v[216:219],v[96:99]
v_exp_f32 v90,v90
v_mfma_f32_16x16x32_bf16 v[100:103],a[156:159],v[216:219],v[40:43]
ds_read_b64_tr_b16 a[234:235],v3 offset:4608
ds_read_b64_tr_b16 a[236:237],v3 offset:8704
v_mfma_f32_16x16x32_bf16 v[100:103],a[152:155],v[208:211],v[100:103]
v_exp_f32 v91,v91
v_mfma_f32_16x16x32_bf16 v[100:103],a[148:151],v[200:203],v[100:103]
ds_read_b64_tr_b16 a[238:239],v3 offset:12800
ds_read_b64_tr_b16 a[240:241],v2 offset:768
v_mfma_f32_16x16x32_bf16 v[100:103],a[144:147],v[192:195],v[100:103]
ds_read_b64_tr_b16 a[242:243],v2 offset:4864
v_cvt_pk_bf16_f32 v89,v90,v91
v_mfma_f32_16x16x32_bf16 v[104:107],a[160:163],v[192:195],v[40:43]
buffer_store_dwordx4 a[88:91],v6,s[40:43],s82 offen offset:2048 sc1
v_mfma_f32_16x16x32_bf16 v[104:107],a[164:167],v[200:203],v[104:107]
ds_read_b64_tr_b16 a[244:245],v2 offset:8960
ds_read_b64_tr_b16 a[246:247],v2 offset:13056
v_mfma_f32_16x16x32_bf16 v[104:107],a[168:171],v[208:211],v[104:107]
ds_read_b64_tr_b16 a[248:249],v3 offset:768
v_add_u32 v2,s78,v2
v_mfma_f32_16x16x32_bf16 v[104:107],a[172:175],v[216:219],v[104:107]
v_exp_f32 v92,v92
v_mfma_f32_16x16x32_bf16 v[108:111],a[188:191],v[216:219],v[40:43]
ds_read_b64_tr_b16 a[250:251],v3 offset:4864
ds_read_b64_tr_b16 a[252:253],v3 offset:8960
v_mfma_f32_16x16x32_bf16 v[108:111],a[184:187],v[208:211],v[108:111]
s_waitcnt lgkmcnt(5)
ds_read_b64_tr_b16 a[254:255],v3 offset:13056
v_add_u32 v3,s78,v3
v_mfma_f32_16x16x32_bf16 v[108:111],a[180:183],v[200:203],v[108:111]
s_sub_u32 s78,s77,32768
v_exp_f32 v93,v93
v_mfma_f32_16x16x32_bf16 v[108:111],a[176:179],v[192:195],v[108:111]
s_and_b32 s78,s78,0x1ffff
v_exp_f32 v94,v94
v_mfma_f32_16x16x32_bf16 a[0:3],a[192:195],v[64:67],a[0:3]
s_sub_u32 s78,s78,s77
v_exp_f32 v95,v95
v_mfma_f32_16x16x32_bf16 a[0:3],a[196:199],v[72:75],a[0:3]
s_add_u32 s77,s77,s78
v_cvt_pk_bf16_f32 v90,v92,v93
v_cvt_pk_bf16_f32 v91,v94,v95
v_mfma_f32_16x16x32_bf16 a[4:7],a[200:203],v[64:67],a[4:7]
s_cmp_ge_u32 s63,2
s_cselect_b32 s6,L_gbS_%=-L_pcg_%=,s6
s_cselect_b32 s10,L_geS_%=-L_gbS_%=,s10
v_mfma_f32_16x16x32_bf16 a[4:7],a[204:207],v[72:75],a[4:7]
s_add_u32 s8,s8,s6
s_addc_u32 s9,s9,0
s_and_b32 s8,s8,0xffffff80
v_mfma_f32_16x16x32_bf16 a[8:11],a[208:211],v[64:67],a[8:11]
s_and_b32 s9,s9,0xffff
v_exp_f32 v96,v96
v_mfma_f32_16x16x32_bf16 a[8:11],a[212:215],v[72:75],a[8:11]
v_exp_f32 v97,v97
v_mfma_f32_16x16x32_bf16 a[12:15],a[216:219],v[64:67],a[12:15]
v_exp_f32 v98,v98
v_mfma_f32_16x16x32_bf16 a[12:15],a[220:223],v[72:75],a[12:15]
v_exp_f32 v99,v99
v_mfma_f32_16x16x32_bf16 a[16:19],a[224:227],v[64:67],a[16:19]
v_exp_f32 v100,v100
v_mfma_f32_16x16x32_bf16 a[16:19],a[228:231],v[72:75],a[16:19]
buffer_store_dwordx4 a[92:95],v6,s[40:43],s82 offen sc1
v_mfma_f32_16x16x32_bf16 a[20:23],a[232:235],v[64:67],a[20:23]
s_add_u32 s82,s82,65536
v_exp_f32 v101,v101
v_mfma_f32_16x16x32_bf16 a[20:23],a[236:239],v[72:75],a[20:23]
v_exp_f32 v102,v102
v_mfma_f32_16x16x32_bf16 a[24:27],a[240:243],v[64:67],a[24:27]
s_waitcnt lgkmcnt(0)
v_exp_f32 v103,v103
v_mfma_f32_16x16x32_bf16 a[24:27],a[244:247],v[72:75],a[24:27]
v_cvt_pk_bf16_f32 v96,v96,v97
v_cvt_pk_bf16_f32 v97,v98,v99
v_mfma_f32_16x16x32_bf16 a[28:31],a[248:251],v[64:67],a[28:31]
v_cvt_pk_bf16_f32 v98,v100,v101
v_cvt_pk_bf16_f32 v99,v102,v103
v_mfma_f32_16x16x32_bf16 a[28:31],a[252:255],v[72:75],a[28:31]
v_exp_f32 v104,v104
v_mfma_f32_16x16x32_bf16 v[48:51],v[28:31],v[64:67],v[48:51]
v_exp_f32 v105,v105
v_mfma_f32_16x16x32_bf16 v[48:51],v[28:31],v[72:75],v[48:51]
v_exp_f32 v106,v106
s_waitcnt vmcnt(36)
s_barrier
v_mfma_f32_16x16x32_bf16 v[112:115],a[128:131],v[224:227],v[44:47]
ds_read_b128 a[128:131],v1
v_cvt_pk_bf16_f32 v104,v104,v105
v_mfma_f32_16x16x32_bf16 v[112:115],a[132:135],v[232:235],v[112:115]
v_exp_f32 v107,v107
v_mfma_f32_16x16x32_bf16 v[112:115],a[136:139],v[240:243],v[112:115]
ds_read_b128 a[132:135],v1 offset:256
v_cvt_pk_bf16_f32 v105,v106,v107
v_mfma_f32_16x16x32_bf16 v[112:115],a[140:143],v[248:251],v[112:115]
v_exp_f32 v108,v108
v_mfma_f32_16x16x32_bf16 v[116:119],a[156:159],v[248:251],v[44:47]
v_exp_f32 v109,v109
)A"
R"A(v_mfma_f32_16x16x32_bf16 v[116:119],a[152:155],v[240:243],v[116:119]
s_add_u32 m0,s86,40960
ds_read_b128 a[136:139],v1 offset:512
v_cvt_pk_bf16_f32 v106,v108,v109
v_mfma_f32_16x16x32_bf16 v[116:119],a[148:151],v[232:235],v[116:119]
v_exp_f32 v110,v110
v_mfma_f32_16x16x32_bf16 v[116:119],a[144:147],v[224:227],v[116:119]
buffer_load_dwordx4 v24,s[48:51],s83 offen lds
v_mfma_f32_16x16x32_bf16 v[120:123],a[160:163],v[224:227],v[44:47]
v_exp_f32 v111,v111
v_mfma_f32_16x16x32_bf16 v[120:123],a[164:167],v[232:235],v[120:123]
ds_read_b128 a[140:143],v1 offset:768
v_cvt_pk_bf16_f32 v107,v110,v111
v_mfma_f32_16x16x32_bf16 v[120:123],a[168:171],v[240:243],v[120:123]
v_exp_f32 v112,v112
v_mfma_f32_16x16x32_bf16 v[120:123],a[172:175],v[248:251],v[120:123]
v_exp_f32 v113,v113
v_mfma_f32_16x16x32_bf16 v[124:127],a[188:191],v[248:251],v[44:47]
s_nop 0
L_pd12_%=:
s_cbranch_execz L_trt1_%=
ds_read_b128 a[156:159],v1 offset:4864
v_cvt_pk_bf16_f32 v112,v112,v113
v_mfma_f32_16x16x32_bf16 v[124:127],a[184:187],v[240:243],v[124:127]
v_exp_f32 v114,v114
v_mfma_f32_16x16x32_bf16 v[124:127],a[180:183],v[232:235],v[124:127]
v_exp_f32 v115,v115
v_mfma_f32_16x16x32_bf16 v[124:127],a[176:179],v[224:227],v[124:127]
ds_read_b128 a[152:155],v1 offset:4608
v_cvt_pk_bf16_f32 v113,v114,v115
v_mfma_f32_16x16x32_bf16 v[132:135],a[192:195],v[80:83],v[132:135]
v_exp_f32 v116,v116
v_mfma_f32_16x16x32_bf16 v[132:135],a[196:199],v[88:91],v[132:135]
buffer_load_dwordx4 v25,s[48:51],s83 offen offset:1024 lds
v_mfma_f32_16x16x32_bf16 v[140:143],a[200:203],v[80:83],v[140:143]
v_exp_f32 v117,v117
v_mfma_f32_16x16x32_bf16 v[140:143],a[204:207],v[88:91],v[140:143]
ds_read_b128 a[148:151],v1 offset:4352
v_cvt_pk_bf16_f32 v114,v116,v117
v_mfma_f32_16x16x32_bf16 v[148:151],a[208:211],v[80:83],v[148:151]
v_exp_f32 v118,v118
v_mfma_f32_16x16x32_bf16 v[148:151],a[212:215],v[88:91],v[148:151]
v_exp_f32 v119,v119
v_mfma_f32_16x16x32_bf16 v[156:159],a[216:219],v[80:83],v[156:159]
ds_read_b128 a[144:147],v1 offset:4096
v_cvt_pk_bf16_f32 v115,v118,v119
v_mfma_f32_16x16x32_bf16 v[156:159],a[220:223],v[88:91],v[156:159]
v_exp_f32 v120,v120
v_mfma_f32_16x16x32_bf16 v[164:167],a[224:227],v[80:83],v[164:167]
buffer_load_dword v5,v4,s[8:11],s7 offen
v_mfma_f32_16x16x32_bf16 v[164:167],a[228:231],v[88:91],v[164:167]
v_exp_f32 v121,v121
v_mfma_f32_16x16x32_bf16 v[172:175],a[232:235],v[80:83],v[172:175]
ds_read_b128 a[160:163],v1 offset:8192
v_cvt_pk_bf16_f32 v120,v120,v121
v_mfma_f32_16x16x32_bf16 v[172:175],a[236:239],v[88:91],v[172:175]
v_exp_f32 v122,v122
v_mfma_f32_16x16x32_bf16 v[180:183],a[240:243],v[80:83],v[180:183]
v_exp_f32 v123,v123
v_mfma_f32_16x16x32_bf16 v[180:183],a[244:247],v[88:91],v[180:183]
ds_read_b128 a[164:167],v1 offset:8448
v_cvt_pk_bf16_f32 v121,v122,v123
v_mfma_f32_16x16x32_bf16 v[188:191],a[248:251],v[80:83],v[188:191]
buffer_load_dwordx4 v26,s[48:51],s83 offen offset:2048 lds
v_mfma_f32_16x16x32_bf16 v[188:191],a[252:255],v[88:91],v[188:191]
v_exp_f32 v124,v124
v_mfma_f32_16x16x32_bf16 v[52:55],v[28:31],v[80:83],v[52:55]
ds_read_b128 a[168:171],v1 offset:8704
v_mfma_f32_16x16x32_bf16 v[52:55],v[28:31],v[88:91],v[52:55]
s_waitcnt lgkmcnt(3)
v_exp_f32 v125,v125
v_mfma_f32_16x16x32_bf16 v[64:67],a[128:131],v[128:131],v[32:35]
ds_read_b128 a[172:175],v1 offset:8960
v_cvt_pk_bf16_f32 v122,v124,v125
v_mfma_f32_16x16x32_bf16 v[64:67],a[132:135],v[136:139],v[64:67]
v_exp_f32 v126,v126
v_mfma_f32_16x16x32_bf16 v[64:67],a[136:139],v[144:147],v[64:67]
ds_read_b128 a[188:191],v1 offset:13056
v_mfma_f32_16x16x32_bf16 v[64:67],a[140:143],v[152:155],v[64:67]
buffer_load_dwordx4 v27,s[48:51],s83 offen offset:3072 lds
v_mfma_f32_16x16x32_bf16 v[68:71],a[156:159],v[152:155],v[32:35]
ds_read_b128 a[184:187],v1 offset:12800
v_mfma_f32_16x16x32_bf16 v[68:71],a[152:155],v[144:147],v[68:71]
v_exp_f32 v127,v127
v_mfma_f32_16x16x32_bf16 v[68:71],a[148:151],v[136:139],v[68:71]
ds_read_b128 a[180:183],v1 offset:12544
v_cvt_pk_bf16_f32 v123,v126,v127
v_mfma_f32_16x16x32_bf16 v[68:71],a[144:147],v[128:131],v[68:71]
s_waitcnt lgkmcnt(4)
v_exp_f32 v64,v64
v_mfma_f32_16x16x32_bf16 v[72:75],a[160:163],v[128:131],v[32:35]
ds_read_b128 a[176:179],v1 offset:12288
v_add_u32 v1,s78,v1
v_mfma_f32_16x16x32_bf16 v[72:75],a[164:167],v[136:139],v[72:75]
v_exp_f32 v65,v65
v_mfma_f32_16x16x32_bf16 v[72:75],a[168:171],v[144:147],v[72:75]
s_waitcnt lgkmcnt(3)
v_exp_f32 v66,v66
v_mfma_f32_16x16x32_bf16 v[72:75],a[172:175],v[152:155],v[72:75]
s_add_u32 m0,s86,45056
v_exp_f32 v67,v67
v_mfma_f32_16x16x32_bf16 v[76:79],a[188:191],v[152:155],v[32:35]
s_waitcnt lgkmcnt(0)
v_exp_f32 v68,v68
v_mfma_f32_16x16x32_bf16 v[76:79],a[184:187],v[144:147],v[76:79]
buffer_load_dwordx4 v7,s[48:51],s83 offen lds
v_mfma_f32_16x16x32_bf16 v[76:79],a[180:183],v[136:139],v[76:79]
s_add_u32 s83,s64,2
v_exp_f32 v69,v69
v_mfma_f32_16x16x32_bf16 v[76:79],a[176:179],v[128:131],v[76:79]
s_and_b32 s83,s83,3
v_exp_f32 v70,v70
v_mfma_f32_16x16x32_bf16 v[196:199],a[192:195],v[96:99],v[196:199]
s_lshl_b32 s83,s83,1
v_exp_f32 v71,v71
v_mfma_f32_16x16x32_bf16 v[196:199],a[196:199],v[104:107],v[196:199]
s_mul_i32 s83,s83,163840
v_cvt_pk_bf16_f32 v64,v64,v65
v_cvt_pk_bf16_f32 v65,v66,v67
v_mfma_f32_16x16x32_bf16 v[204:207],a[200:203],v[96:99],v[204:207]
s_add_u32 s83,s83,s85
v_cvt_pk_bf16_f32 v66,v68,v69
v_cvt_pk_bf16_f32 v67,v70,v71
v_mfma_f32_16x16x32_bf16 v[204:207],a[204:207],v[104:107],v[204:207]
v_exp_f32 v72,v72
v_mfma_f32_16x16x32_bf16 v[212:215],a[208:211],v[96:99],v[212:215]
v_exp_f32 v73,v73
v_mfma_f32_16x16x32_bf16 v[212:215],a[212:215],v[104:107],v[212:215]
s_add_u32 m0,s86,46080
v_exp_f32 v74,v74
v_mfma_f32_16x16x32_bf16 v[220:223],a[216:219],v[96:99],v[220:223]
v_exp_f32 v75,v75
v_mfma_f32_16x16x32_bf16 v[220:223],a[220:223],v[104:107],v[220:223]
buffer_load_dwordx4 v24,s[48:51],s84 offen lds
v_mfma_f32_16x16x32_bf16 v[228:231],a[224:227],v[96:99],v[228:231]
v_exp_f32 v76,v76
v_mfma_f32_16x16x32_bf16 v[228:231],a[228:231],v[104:107],v[228:231]
v_exp_f32 v77,v77
v_mfma_f32_16x16x32_bf16 v[236:239],a[232:235],v[96:99],v[236:239]
v_exp_f32 v78,v78
v_mfma_f32_16x16x32_bf16 v[236:239],a[236:239],v[104:107],v[236:239]
v_exp_f32 v79,v79
v_mfma_f32_16x16x32_bf16 v[244:247],a[240:243],v[96:99],v[244:247]
v_cvt_pk_bf16_f32 v72,v72,v73
v_cvt_pk_bf16_f32 v73,v74,v75
v_mfma_f32_16x16x32_bf16 v[244:247],a[244:247],v[104:107],v[244:247]
v_cvt_pk_bf16_f32 v74,v76,v77
v_cvt_pk_bf16_f32 v75,v78,v79
v_mfma_f32_16x16x32_bf16 v[252:255],a[248:251],v[96:99],v[252:255]
v_lshrrev_b32 v1,6,v6
v_mul_u32_u24 v1,320,v1
v_mfma_f32_16x16x32_bf16 v[252:255],a[252:255],v[104:107],v[252:255]
v_mfma_f32_16x16x32_bf16 v[56:59],v[28:31],v[96:99],v[56:59]
v_mfma_f32_16x16x32_bf16 v[56:59],v[28:31],v[104:107],v[56:59]
buffer_load_dwordx4 v25,s[48:51],s84 offen offset:1024 lds
v_mfma_f32_16x16x32_bf16 v[80:83],a[128:131],v[160:163],v[36:39]
v_mfma_f32_16x16x32_bf16 v[80:83],a[132:135],v[168:171],v[80:83]
v_mfma_f32_16x16x32_bf16 v[80:83],a[136:139],v[176:179],v[80:83]
v_mfma_f32_16x16x32_bf16 v[80:83],a[140:143],v[184:187],v[80:83]
v_mfma_f32_16x16x32_bf16 v[84:87],a[156:159],v[184:187],v[36:39]
v_mfma_f32_16x16x32_bf16 v[84:87],a[152:155],v[176:179],v[84:87]
v_mfma_f32_16x16x32_bf16 v[84:87],a[148:151],v[168:171],v[84:87]
v_mfma_f32_16x16x32_bf16 v[84:87],a[144:147],v[160:163],v[84:87]
v_mfma_f32_16x16x32_bf16 v[88:91],a[160:163],v[160:163],v[36:39]
v_mfma_f32_16x16x32_bf16 v[88:91],a[164:167],v[168:171],v[88:91]
buffer_load_dwordx4 v26,s[48:51],s84 offen offset:2048 lds
v_mfma_f32_16x16x32_bf16 v[88:91],a[168:171],v[176:179],v[88:91]
v_exp_f32 v80,v80
v_mfma_f32_16x16x32_bf16 v[88:91],a[172:175],v[184:187],v[88:91]
v_exp_f32 v81,v81
v_mfma_f32_16x16x32_bf16 v[92:95],a[188:191],v[184:187],v[36:39]
v_exp_f32 v82,v82
v_mfma_f32_16x16x32_bf16 v[92:95],a[184:187],v[176:179],v[92:95]
v_exp_f32 v83,v83
v_mfma_f32_16x16x32_bf16 v[92:95],a[180:183],v[168:171],v[92:95]
v_exp_f32 v84,v84
v_mfma_f32_16x16x32_bf16 v[92:95],a[176:179],v[160:163],v[92:95]
v_exp_f32 v85,v85
v_mfma_f32_16x16x32_bf16 a[32:35],a[192:195],v[112:115],a[32:35]
ds_read_b64_tr_b16 a[192:193],v2
ds_read_b64_tr_b16 a[194:195],v2 offset:4096
v_mfma_f32_16x16x32_bf16 a[32:35],a[196:199],v[120:123],a[32:35]
ds_read_b64_tr_b16 a[196:197],v2 offset:8192
v_cvt_pk_bf16_f32 v80,v80,v81
v_mfma_f32_16x16x32_bf16 a[36:39],a[200:203],v[112:115],a[36:39]
ds_read_b64_tr_b16 a[198:199],v2 offset:12288
v_cvt_pk_bf16_f32 v81,v82,v83
v_mfma_f32_16x16x32_bf16 a[36:39],a[204:207],v[120:123],a[36:39]
buffer_load_dwordx4 v27,s[48:51],s84 offen offset:3072 lds
v_mfma_f32_16x16x32_bf16 a[40:43],a[208:211],v[112:115],a[40:43]
ds_read_b64_tr_b16 a[200:201],v3
ds_read_b64_tr_b16 a[202:203],v3 offset:4096
v_mfma_f32_16x16x32_bf16 a[40:43],a[212:215],v[120:123],a[40:43]
ds_read_b64_tr_b16 a[204:205],v3 offset:8192
v_cvt_pk_bf16_f32 v82,v84,v85
v_mfma_f32_16x16x32_bf16 a[44:47],a[216:219],v[112:115],a[44:47]
v_exp_f32 v86,v86
v_mfma_f32_16x16x32_bf16 a[44:47],a[220:223],v[120:123],a[44:47]
ds_read_b64_tr_b16 a[206:207],v3 offset:12288
ds_read_b64_tr_b16 a[208:209],v2 offset:256
v_mfma_f32_16x16x32_bf16 a[48:51],a[224:227],v[112:115],a[48:51]
v_exp_f32 v87,v87
v_mfma_f32_16x16x32_bf16 a[48:51],a[228:231],v[120:123],a[48:51]
ds_read_b64_tr_b16 a[210:211],v2 offset:4352
ds_read_b64_tr_b16 a[212:213],v2 offset:8448
v_mfma_f32_16x16x32_bf16 a[52:55],a[232:235],v[112:115],a[52:55]
ds_read_b64_tr_b16 a[214:215],v2 offset:12544
v_cvt_pk_bf16_f32 v83,v86,v87
v_mfma_f32_16x16x32_bf16 a[52:55],a[236:239],v[120:123],a[52:55]
s_add_u32 m0,s86,50176
v_exp_f32 v88,v88
v_mfma_f32_16x16x32_bf16 a[56:59],a[240:243],v[112:115],a[56:59]
ds_read_b64_tr_b16 a[216:217],v3 offset:256
ds_read_b64_tr_b16 a[218:219],v3 offset:4352
v_mfma_f32_16x16x32_bf16 a[56:59],a[244:247],v[120:123],a[56:59]
buffer_load_dwordx4 v7,s[48:51],s84 offen lds
v_mfma_f32_16x16x32_bf16 a[60:63],a[248:251],v[112:115],a[60:63]
s_add_u32 s84,s83,20480
ds_read_b64_tr_b16 a[220:221],v3 offset:8448
ds_read_b64_tr_b16 a[222:223],v3 offset:12544
v_mfma_f32_16x16x32_bf16 a[60:63],a[252:255],v[120:123],a[60:63]
v_exp_f32 v89,v89
v_mfma_f32_16x16x32_bf16 v[60:63],v[28:31],v[112:115],v[60:63]
ds_read_b64_tr_b16 a[224:225],v2 offset:512
ds_read_b64_tr_b16 a[226:227],v2 offset:4608
v_mfma_f32_16x16x32_bf16 v[60:63],v[28:31],v[120:123],v[60:63]
ds_read_b64_tr_b16 a[228:229],v2 offset:8704
v_cvt_pk_bf16_f32 v88,v88,v89
v_mfma_f32_16x16x32_bf16 v[96:99],a[128:131],v[192:195],v[40:43]
v_exp_f32 v90,v90
v_mfma_f32_16x16x32_bf16 v[96:99],a[132:135],v[200:203],v[96:99]
ds_read_b64_tr_b16 a[230:231],v2 offset:12800
ds_read_b64_tr_b16 a[232:233],v3 offset:512
v_mfma_f32_16x16x32_bf16 v[96:99],a[136:139],v[208:211],v[96:99]
v_exp_f32 v91,v91
v_mfma_f32_16x16x32_bf16 v[96:99],a[140:143],v[216:219],v[96:99]
ds_read_b64_tr_b16 a[234:235],v3 offset:4608
ds_read_b64_tr_b16 a[236:237],v3 offset:8704
v_mfma_f32_16x16x32_bf16 v[100:103],a[156:159],v[216:219],v[40:43]
ds_read_b64_tr_b16 a[238:239],v3 offset:12800
v_cvt_pk_bf16_f32 v89,v90,v91
v_mfma_f32_16x16x32_bf16 v[100:103],a[152:155],v[208:211],v[100:103]
v_exp_f32 v92,v92
v_mfma_f32_16x16x32_bf16 v[100:103],a[148:151],v[200:203],v[100:103]
ds_read_b64_tr_b16 a[240:241],v2 offset:768
ds_read_b64_tr_b16 a[242:243],v2 offset:4864
v_mfma_f32_16x16x32_bf16 v[100:103],a[144:147],v[192:195],v[100:103]
v_exp_f32 v93,v93
v_mfma_f32_16x16x32_bf16 v[104:107],a[160:163],v[192:195],v[40:43]
ds_read_b64_tr_b16 a[244:245],v2 offset:8960
ds_read_b64_tr_b16 a[246:247],v2 offset:13056
v_mfma_f32_16x16x32_bf16 v[104:107],a[164:167],v[200:203],v[104:107]
ds_read_b64_tr_b16 a[248:249],v3 offset:768
v_cvt_pk_bf16_f32 v90,v92,v93
v_mfma_f32_16x16x32_bf16 v[104:107],a[168:171],v[208:211],v[104:107]
ds_read_b64_tr_b16 a[250:251],v3 offset:4864
v_bfe_u32 v2,v6,4,2
v_mfma_f32_16x16x32_bf16 v[104:107],a[172:175],v[216:219],v[104:107]
ds_read_b64_tr_b16 a[252:253],v3 offset:8960
v_lshl_add_u32 v1,v2,3,v1
v_mfma_f32_16x16x32_bf16 v[108:111],a[188:191],v[216:219],v[40:43]
ds_read_b64_tr_b16 a[254:255],v3 offset:13056
v_add_u32 v1,122880,v1
v_mfma_f32_16x16x32_bf16 v[108:111],a[184:187],v[208:211],v[108:111]
)A"
R"A(v_exp_f32 v94,v94
v_mfma_f32_16x16x32_bf16 v[108:111],a[180:183],v[200:203],v[108:111]
v_exp_f32 v95,v95
v_mfma_f32_16x16x32_bf16 v[108:111],a[176:179],v[192:195],v[108:111]
s_waitcnt lgkmcnt(6)
v_cvt_pk_bf16_f32 v91,v94,v95
v_bfe_u32 v2,v6,8,1
v_mfma_f32_16x16x32_bf16 a[0:3],a[192:195],v[64:67],a[0:3]
v_exp_f32 v96,v96
v_mfma_f32_16x16x32_bf16 a[0:3],a[196:199],v[72:75],a[0:3]
v_exp_f32 v97,v97
v_mfma_f32_16x16x32_bf16 a[4:7],a[200:203],v[64:67],a[4:7]
v_exp_f32 v98,v98
v_mfma_f32_16x16x32_bf16 a[4:7],a[204:207],v[72:75],a[4:7]
v_exp_f32 v99,v99
v_mfma_f32_16x16x32_bf16 a[8:11],a[208:211],v[64:67],a[8:11]
v_exp_f32 v100,v100
v_mfma_f32_16x16x32_bf16 a[8:11],a[212:215],v[72:75],a[8:11]
v_exp_f32 v101,v101
v_mfma_f32_16x16x32_bf16 a[12:15],a[216:219],v[64:67],a[12:15]
v_exp_f32 v102,v102
v_mfma_f32_16x16x32_bf16 a[12:15],a[220:223],v[72:75],a[12:15]
v_exp_f32 v103,v103
v_mfma_f32_16x16x32_bf16 a[16:19],a[224:227],v[64:67],a[16:19]
v_cvt_pk_bf16_f32 v96,v96,v97
v_cvt_pk_bf16_f32 v97,v98,v99
v_mfma_f32_16x16x32_bf16 a[16:19],a[228:231],v[72:75],a[16:19]
v_cvt_pk_bf16_f32 v98,v100,v101
v_cvt_pk_bf16_f32 v99,v102,v103
v_mfma_f32_16x16x32_bf16 a[20:23],a[232:235],v[64:67],a[20:23]
v_exp_f32 v104,v104
v_mfma_f32_16x16x32_bf16 a[20:23],a[236:239],v[72:75],a[20:23]
v_exp_f32 v105,v105
v_mfma_f32_16x16x32_bf16 a[24:27],a[240:243],v[64:67],a[24:27]
s_waitcnt lgkmcnt(0)
v_exp_f32 v106,v106
v_mfma_f32_16x16x32_bf16 a[24:27],a[244:247],v[72:75],a[24:27]
v_exp_f32 v107,v107
v_mfma_f32_16x16x32_bf16 a[28:31],a[248:251],v[64:67],a[28:31]
v_exp_f32 v108,v108
v_mfma_f32_16x16x32_bf16 a[28:31],a[252:255],v[72:75],a[28:31]
v_exp_f32 v109,v109
v_mfma_f32_16x16x32_bf16 v[48:51],v[28:31],v[64:67],v[48:51]
v_exp_f32 v110,v110
v_mfma_f32_16x16x32_bf16 v[48:51],v[28:31],v[72:75],v[48:51]
v_exp_f32 v111,v111
s_waitcnt vmcnt(11)
s_barrier
v_mfma_f32_16x16x32_bf16 v[112:115],a[128:131],v[224:227],v[44:47]
buffer_store_dwordx2 v[8:9],v10,s[44:47],s18 offen offset:256 sc1
v_mfma_f32_16x16x32_bf16 v[112:115],a[132:135],v[232:235],v[112:115]
v_cvt_pk_bf16_f32 v104,v104,v105
v_cvt_pk_bf16_f32 v105,v106,v107
v_mfma_f32_16x16x32_bf16 v[112:115],a[136:139],v[240:243],v[112:115]
v_cvt_pk_bf16_f32 v106,v108,v109
v_cvt_pk_bf16_f32 v107,v110,v111
v_mfma_f32_16x16x32_bf16 v[112:115],a[140:143],v[248:251],v[112:115]
s_add_u32 m0,s86,0
v_cmp_nlt_f32 s[54:55],v48,s73
v_max_f32 v49,0x00800000,v48
v_mfma_f32_16x16x32_bf16 v[116:119],a[156:159],v[248:251],v[44:47]
v_rcp_f32 v49,v49
v_mfma_f32_16x16x32_bf16 v[116:119],a[152:155],v[240:243],v[116:119]
v_lshl_add_u32 v1,v2,5,v1
v_lshlrev_b32 v2,6,v2
v_mfma_f32_16x16x32_bf16 v[116:119],a[148:151],v[232:235],v[116:119]
buffer_load_dwordx4 v24,s[48:51],s83 offen lds
v_mfma_f32_16x16x32_bf16 v[116:119],a[144:147],v[224:227],v[116:119]
v_exp_f32 v112,v112
v_mfma_f32_16x16x32_bf16 v[120:123],a[160:163],v[224:227],v[44:47]
v_exp_f32 v113,v113
v_mfma_f32_16x16x32_bf16 v[120:123],a[164:167],v[232:235],v[120:123]
v_exp_f32 v114,v114
v_mfma_f32_16x16x32_bf16 v[120:123],a[168:171],v[240:243],v[120:123]
v_exp_f32 v115,v115
v_mfma_f32_16x16x32_bf16 v[120:123],a[172:175],v[248:251],v[120:123]
v_exp_f32 v116,v116
v_mfma_f32_16x16x32_bf16 v[124:127],a[188:191],v[248:251],v[44:47]
buffer_load_dwordx4 v25,s[48:51],s83 offen offset:1024 lds
v_mfma_f32_16x16x32_bf16 v[124:127],a[184:187],v[240:243],v[124:127]
v_exp_f32 v117,v117
v_mfma_f32_16x16x32_bf16 v[124:127],a[180:183],v[232:235],v[124:127]
v_exp_f32 v118,v118
v_mfma_f32_16x16x32_bf16 v[124:127],a[176:179],v[224:227],v[124:127]
v_exp_f32 v119,v119
v_mfma_f32_16x16x32_bf16 v[52:55],v[28:31],v[80:83],v[52:55]
v_cvt_pk_bf16_f32 v112,v112,v113
v_cvt_pk_bf16_f32 v113,v114,v115
v_mfma_f32_16x16x32_bf16 v[52:55],v[28:31],v[88:91],v[52:55]
v_cvt_pk_bf16_f32 v114,v116,v117
v_cvt_pk_bf16_f32 v115,v118,v119
v_mfma_f32_16x16x32_bf16 v[132:135],a[192:195],v[80:83],v[132:135]
buffer_load_dwordx4 v26,s[48:51],s83 offen offset:2048 lds
v_mfma_f32_16x16x32_bf16 v[132:135],a[196:199],v[88:91],v[132:135]
v_exp_f32 v120,v120
v_mfma_f32_16x16x32_bf16 v[140:143],a[200:203],v[80:83],v[140:143]
v_exp_f32 v121,v121
v_mfma_f32_16x16x32_bf16 v[140:143],a[204:207],v[88:91],v[140:143]
v_exp_f32 v122,v122
v_mfma_f32_16x16x32_bf16 v[148:151],a[208:211],v[80:83],v[148:151]
v_exp_f32 v123,v123
v_mfma_f32_16x16x32_bf16 v[148:151],a[212:215],v[88:91],v[148:151]
v_exp_f32 v124,v124
v_mfma_f32_16x16x32_bf16 v[156:159],a[216:219],v[80:83],v[156:159]
buffer_load_dwordx4 v27,s[48:51],s83 offen offset:3072 lds
v_mfma_f32_16x16x32_bf16 v[156:159],a[220:223],v[88:91],v[156:159]
v_exp_f32 v125,v125
v_mfma_f32_16x16x32_bf16 v[164:167],a[224:227],v[80:83],v[164:167]
v_exp_f32 v126,v126
v_mfma_f32_16x16x32_bf16 v[164:167],a[228:231],v[88:91],v[164:167]
s_add_u32 m0,s86,4096
v_exp_f32 v127,v127
v_mfma_f32_16x16x32_bf16 v[172:175],a[232:235],v[80:83],v[172:175]
v_cvt_pk_bf16_f32 v120,v120,v121
v_cvt_pk_bf16_f32 v121,v122,v123
v_mfma_f32_16x16x32_bf16 v[172:175],a[236:239],v[88:91],v[172:175]
v_cvt_pk_bf16_f32 v122,v124,v125
v_cvt_pk_bf16_f32 v123,v126,v127
v_mfma_f32_16x16x32_bf16 v[180:183],a[240:243],v[80:83],v[180:183]
buffer_load_dwordx4 v7,s[48:51],s83 offen lds
v_mfma_f32_16x16x32_bf16 v[180:183],a[244:247],v[88:91],v[180:183]
v_cmp_nlt_f32 s[56:57],v52,s73
v_sub_u32 v2,v1,v2
v_mfma_f32_16x16x32_bf16 v[188:191],a[248:251],v[80:83],v[188:191]
s_or_b64 s[54:55],s[54:55],s[56:57]
v_accvgpr_read_b32 v8,a0
v_accvgpr_read_b32 v9,a1
v_mfma_f32_16x16x32_bf16 v[188:191],a[252:255],v[88:91],v[188:191]
s_add_u32 m0,s86,5120
v_accvgpr_read_b32 v10,a2
v_accvgpr_read_b32 v11,a3
v_mfma_f32_16x16x32_bf16 v[56:59],v[28:31],v[96:99],v[56:59]
v_accvgpr_read_b32 v12,a4
v_accvgpr_read_b32 v13,a5
v_mfma_f32_16x16x32_bf16 v[56:59],v[28:31],v[104:107],v[56:59]
v_accvgpr_read_b32 v14,a6
v_accvgpr_read_b32 v15,a7
v_mfma_f32_16x16x32_bf16 v[196:199],a[192:195],v[96:99],v[196:199]
buffer_load_dwordx4 v24,s[48:51],s84 offen lds
v_mfma_f32_16x16x32_bf16 v[196:199],a[196:199],v[104:107],v[196:199]
v_mul_f32 v8,v8,v49
v_mul_f32 v9,v9,v49
v_mfma_f32_16x16x32_bf16 v[204:207],a[200:203],v[96:99],v[204:207]
v_cmp_nlt_f32 s[56:57],v56,s73
v_mul_f32 v10,v10,v49
v_mfma_f32_16x16x32_bf16 v[204:207],a[204:207],v[104:107],v[204:207]
s_or_b64 s[54:55],s[54:55],s[56:57]
v_mul_f32 v11,v11,v49
v_mul_f32 v12,v12,v49
v_mfma_f32_16x16x32_bf16 v[212:215],a[208:211],v[96:99],v[212:215]
v_mul_f32 v13,v13,v49
v_mul_f32 v14,v14,v49
v_mfma_f32_16x16x32_bf16 v[212:215],a[212:215],v[104:107],v[212:215]
v_mul_f32 v15,v15,v49
v_cvt_pk_bf16_f32 v32,v8,v9
v_mfma_f32_16x16x32_bf16 v[220:223],a[216:219],v[96:99],v[220:223]
buffer_load_dwordx4 v25,s[48:51],s84 offen offset:1024 lds
v_mfma_f32_16x16x32_bf16 v[220:223],a[220:223],v[104:107],v[220:223]
v_cvt_pk_bf16_f32 v33,v10,v11
v_cvt_pk_bf16_f32 v34,v12,v13
v_mfma_f32_16x16x32_bf16 v[228:231],a[224:227],v[96:99],v[228:231]
v_cvt_pk_bf16_f32 v35,v14,v15
v_accvgpr_read_b32 v16,a8
v_mfma_f32_16x16x32_bf16 v[228:231],a[228:231],v[104:107],v[228:231]
v_accvgpr_read_b32 v17,a9
v_accvgpr_read_b32 v18,a10
v_mfma_f32_16x16x32_bf16 v[236:239],a[232:235],v[96:99],v[236:239]
v_accvgpr_read_b32 v19,a11
v_accvgpr_read_b32 v20,a12
v_mfma_f32_16x16x32_bf16 v[236:239],a[236:239],v[104:107],v[236:239]
v_accvgpr_read_b32 v21,a13
v_accvgpr_read_b32 v22,a14
v_mfma_f32_16x16x32_bf16 v[244:247],a[240:243],v[96:99],v[244:247]
buffer_load_dwordx4 v26,s[48:51],s84 offen offset:2048 lds
v_mfma_f32_16x16x32_bf16 v[244:247],a[244:247],v[104:107],v[244:247]
v_accvgpr_read_b32 v23,a15
v_mul_f32 v16,v16,v49
v_mfma_f32_16x16x32_bf16 v[252:255],a[248:251],v[96:99],v[252:255]
v_mul_f32 v17,v17,v49
v_mul_f32 v18,v18,v49
v_mfma_f32_16x16x32_bf16 v[252:255],a[252:255],v[104:107],v[252:255]
v_mul_f32 v19,v19,v49
v_mul_f32 v20,v20,v49
v_mfma_f32_16x16x32_bf16 v[60:63],v[28:31],v[112:115],v[60:63]
v_mul_f32 v21,v21,v49
v_mul_f32 v22,v22,v49
v_mfma_f32_16x16x32_bf16 v[60:63],v[28:31],v[120:123],v[60:63]
v_mul_f32 v23,v23,v49
v_cvt_pk_bf16_f32 v36,v16,v17
v_mfma_f32_16x16x32_bf16 a[32:35],a[192:195],v[112:115],a[32:35]
buffer_load_dwordx4 v27,s[48:51],s84 offen offset:3072 lds
v_mfma_f32_16x16x32_bf16 a[32:35],a[196:199],v[120:123],a[32:35]
v_cvt_pk_bf16_f32 v37,v18,v19
v_cvt_pk_bf16_f32 v38,v20,v21
v_mfma_f32_16x16x32_bf16 a[36:39],a[200:203],v[112:115],a[36:39]
v_cmp_nlt_f32 s[56:57],v60,s73
v_cvt_pk_bf16_f32 v39,v22,v23
v_mfma_f32_16x16x32_bf16 a[36:39],a[204:207],v[120:123],a[36:39]
s_add_u32 m0,s86,9216
v_accvgpr_read_b32 v8,a16
v_accvgpr_read_b32 v9,a17
v_mfma_f32_16x16x32_bf16 a[40:43],a[208:211],v[112:115],a[40:43]
s_or_b64 s[54:55],s[54:55],s[56:57]
v_accvgpr_read_b32 v10,a18
v_accvgpr_read_b32 v11,a19
v_mfma_f32_16x16x32_bf16 a[40:43],a[212:215],v[120:123],a[40:43]
v_accvgpr_read_b32 v12,a20
v_accvgpr_read_b32 v13,a21
v_mfma_f32_16x16x32_bf16 a[44:47],a[216:219],v[112:115],a[44:47]
buffer_load_dwordx4 v7,s[48:51],s84 offen lds
v_mfma_f32_16x16x32_bf16 a[44:47],a[220:223],v[120:123],a[44:47]
v_accvgpr_read_b32 v14,a22
v_accvgpr_read_b32 v15,a23
v_mfma_f32_16x16x32_bf16 a[48:51],a[224:227],v[112:115],a[48:51]
v_mul_f32 v8,v8,v49
v_mul_f32 v9,v9,v49
v_mfma_f32_16x16x32_bf16 a[48:51],a[228:231],v[120:123],a[48:51]
v_mul_f32 v10,v10,v49
v_mul_f32 v11,v11,v49
v_mfma_f32_16x16x32_bf16 a[52:55],a[232:235],v[112:115],a[52:55]
v_mul_f32 v12,v12,v49
v_mul_f32 v13,v13,v49
v_mfma_f32_16x16x32_bf16 a[52:55],a[236:239],v[120:123],a[52:55]
v_mul_f32 v14,v14,v49
v_mul_f32 v15,v15,v49
v_mfma_f32_16x16x32_bf16 a[56:59],a[240:243],v[112:115],a[56:59]
v_cvt_pk_bf16_f32 v40,v8,v9
v_cvt_pk_bf16_f32 v41,v10,v11
v_mfma_f32_16x16x32_bf16 a[56:59],a[244:247],v[120:123],a[56:59]
v_cvt_pk_bf16_f32 v42,v12,v13
v_cvt_pk_bf16_f32 v43,v14,v15
v_mfma_f32_16x16x32_bf16 a[60:63],a[248:251],v[112:115],a[60:63]
ds_read_b64_tr_b16 v[64:65],v1
ds_read_b64_tr_b16 v[66:67],v1 offset:5120
v_mfma_f32_16x16x32_bf16 a[60:63],a[252:255],v[120:123],a[60:63]
ds_read_b64_tr_b16 v[68:69],v1 offset:10240
v_max_f32 v53,0x00800000,v52
v_rcp_f32 v53,v53
v_max_f32 v57,0x00800000,v56
v_rcp_f32 v57,v57
v_max_f32 v61,0x00800000,v60
v_rcp_f32 v61,v61
ds_read_b64_tr_b16 v[70:71],v1 offset:15360
ds_read_b64_tr_b16 v[72:73],v1 offset:20480
ds_read_b64_tr_b16 v[74:75],v1 offset:25600
ds_read_b64_tr_b16 v[76:77],v1 offset:30720
ds_read_b64_tr_b16 v[78:79],v1 offset:35840
ds_read_b64_tr_b16 v[80:81],v2 offset:32
ds_read_b64_tr_b16 v[82:83],v2 offset:5152
ds_read_b64_tr_b16 v[84:85],v2 offset:10272
ds_read_b64_tr_b16 v[86:87],v2 offset:15392
ds_read_b64_tr_b16 v[88:89],v2 offset:20512
ds_read_b64_tr_b16 v[90:91],v2 offset:25632
ds_read_b64_tr_b16 v[92:93],v2 offset:30752
ds_read_b64_tr_b16 v[94:95],v2 offset:35872
s_cmp_lg_u64 s[54:55],0
s_cbranch_scc1 L_slow1_%=
L_ok1_%=:
s_cmp_ge_u32 s63,2
s_cbranch_scc1 L_GS_%=
L_gbF_%=:
s_waitcnt lgkmcnt(14)
v_mfma_f32_16x16x32_bf16 a[192:195],v[64:67],a[68:71],0
buffer_store_dwordx4 v[32:35],v6,s[40:43],s79 offen sc1
s_waitcnt lgkmcnt(12)
v_mfma_f32_16x16x32_bf16 a[192:195],v[68:71],a[76:79],a[192:195]
s_lshl_b32 s91,s63,8
v_bfe_u32 v3,v6,4,2
v_min_u32 v3,2,v3
s_waitcnt lgkmcnt(10)
v_mfma_f32_16x16x32_bf16 a[192:195],v[72:75],a[84:87],a[192:195]
s_add_u32 s87,s64,1
v_add3_u32 v3,v3,s64,1
v_and_b32 v3,3,v3
s_waitcnt lgkmcnt(8)
v_mfma_f32_16x16x32_bf16 a[192:195],v[76:79],a[92:95],a[192:195]
s_and_b32 s87,s87,3
ds_read_b64_tr_b16 v[96:97],v1 offset:64
v_lshlrev_b32 v3,6,v3
v_mfma_f32_16x16x32_bf16 a[196:199],v[76:79],a[124:127],0
s_lshl_b32 s87,s87,1
ds_read_b64_tr_b16 v[98:99],v1 offset:5184
ds_read_b64_tr_b16 v[100:101],v1 offset:10304
v_mfma_f32_16x16x32_bf16 a[196:199],v[72:75],a[116:119],a[196:199]
s_lshl_b32 s87,s87,16
ds_read_b64_tr_b16 v[102:103],v1 offset:15424
v_lshlrev_b32 v29,10,v6
v_mfma_f32_16x16x32_bf16 a[196:199],v[68:71],a[108:111],a[196:199]
s_add_u32 s88,s87,4096
ds_read_b64_tr_b16 v[104:105],v1 offset:20544
v_accvgpr_read_b32 v8,a24
v_mfma_f32_16x16x32_bf16 a[196:199],v[64:67],a[100:103],a[196:199]
s_add_u32 s89,s87,8192
ds_read_b64_tr_b16 v[106:107],v1 offset:25664
v_accvgpr_read_b32 v9,a25
v_mfma_f32_16x16x32_bf16 a[200:203],v[64:67],a[64:67],0
)A"
R"A(s_add_u32 s90,s87,12288
ds_read_b64_tr_b16 v[108:109],v1 offset:30784
ds_read_b64_tr_b16 v[110:111],v1 offset:35904
v_mfma_f32_16x16x32_bf16 a[200:203],v[68:71],a[72:75],a[200:203]
s_add_u32 s20,s64,2
ds_read_b64_tr_b16 v[112:113],v2 offset:96
v_accvgpr_read_b32 v10,a26
v_mfma_f32_16x16x32_bf16 a[200:203],v[72:75],a[80:83],a[200:203]
s_and_b32 s20,s20,3
ds_read_b64_tr_b16 v[114:115],v2 offset:5216
v_accvgpr_read_b32 v11,a27
v_mfma_f32_16x16x32_bf16 a[200:203],v[76:79],a[88:91],a[200:203]
s_lshl_b32 s20,s20,1
ds_read_b64_tr_b16 v[116:117],v2 offset:10336
v_accvgpr_read_b32 v12,a28
v_mfma_f32_16x16x32_bf16 a[204:207],v[76:79],a[120:123],0
s_lshl_b32 s20,s20,16
ds_read_b64_tr_b16 v[118:119],v2 offset:15456
ds_read_b64_tr_b16 v[120:121],v2 offset:20576
v_mfma_f32_16x16x32_bf16 a[204:207],v[72:75],a[112:115],a[204:207]
s_add_u32 s21,s20,4096
ds_read_b64_tr_b16 v[122:123],v2 offset:25696
v_accvgpr_read_b32 v13,a29
v_mfma_f32_16x16x32_bf16 a[204:207],v[68:71],a[104:107],a[204:207]
s_add_u32 s22,s20,8192
ds_read_b64_tr_b16 v[124:125],v2 offset:30816
v_accvgpr_read_b32 v14,a30
v_mfma_f32_16x16x32_bf16 a[204:207],v[64:67],a[96:99],a[204:207]
buffer_store_dwordx4 v[36:39],v6,s[40:43],s80 offen sc1
s_waitcnt lgkmcnt(9)
v_mfma_f32_16x16x32_bf16 a[208:211],v[80:83],a[68:71],0
s_add_u32 s23,s20,12288
ds_read_b64_tr_b16 v[126:127],v2 offset:35936
ds_read_b64_tr_b16 v[64:65],v1 offset:128
v_mfma_f32_16x16x32_bf16 a[208:211],v[84:87],a[76:79],a[208:211]
s_add_u32 m0,s86,122880
ds_read_b64_tr_b16 v[66:67],v1 offset:5248
v_accvgpr_read_b32 v15,a31
v_mfma_f32_16x16x32_bf16 a[208:211],v[88:91],a[84:87],a[208:211]
s_add_u32 s83,s64,3
ds_read_b64_tr_b16 v[68:69],v1 offset:10368
v_mul_f32 v8,v8,v49
v_mfma_f32_16x16x32_bf16 a[208:211],v[92:95],a[92:95],a[208:211]
s_and_b32 s83,s83,3
ds_read_b64_tr_b16 v[70:71],v1 offset:15488
v_mul_f32 v9,v9,v49
v_mfma_f32_16x16x32_bf16 a[212:215],v[92:95],a[124:127],0
s_waitcnt vmcnt(21)
buffer_load_dwordx2 v[4:5],v3,s[44:47],s91 offen offset:256 sc1
v_mfma_f32_16x16x32_bf16 a[212:215],v[88:91],a[116:119],a[212:215]
s_lshl_b32 s83,s83,1
s_nop 0
L_pd13_%=:
s_cbranch_execz L_trt1_%=
ds_read_b64_tr_b16 v[72:73],v1 offset:20608
ds_read_b64_tr_b16 v[74:75],v1 offset:25728
v_mfma_f32_16x16x32_bf16 a[212:215],v[84:87],a[108:111],a[212:215]
s_mul_i32 s83,s83,163840
ds_read_b64_tr_b16 v[76:77],v1 offset:30848
v_mul_f32 v10,v10,v49
v_mfma_f32_16x16x32_bf16 a[212:215],v[80:83],a[100:103],a[212:215]
s_add_u32 s83,s83,s85
ds_read_b64_tr_b16 v[78:79],v1 offset:35968
v_mul_f32 v11,v11,v49
v_mfma_f32_16x16x32_bf16 a[216:219],v[80:83],a[64:67],0
s_add_u32 s84,s83,20480
v_mul_f32 v12,v12,v49
v_mul_f32 v13,v13,v49
v_mfma_f32_16x16x32_bf16 a[216:219],v[84:87],a[72:75],a[216:219]
s_lshl_b32 s66,s61,3
v_mul_f32 v14,v14,v49
v_mul_f32 v15,v15,v49
v_mfma_f32_16x16x32_bf16 a[216:219],v[88:91],a[80:83],a[216:219]
s_add_u32 s66,s66,s64
v_cvt_pk_bf16_f32 v44,v8,v9
v_cvt_pk_bf16_f32 v45,v10,v11
v_mfma_f32_16x16x32_bf16 a[216:219],v[92:95],a[88:91],a[216:219]
s_mul_i32 s66,s66,262144
v_cvt_pk_bf16_f32 v46,v12,v13
v_cvt_pk_bf16_f32 v47,v14,v15
v_mfma_f32_16x16x32_bf16 a[220:223],v[92:95],a[120:123],0
s_add_u32 s66,s66,100663296
ds_read_b64_tr_b16 v[92:93],v2 offset:30880
ds_read_b64_tr_b16 v[94:95],v2 offset:36000
v_mfma_f32_16x16x32_bf16 a[220:223],v[88:91],a[112:115],a[220:223]
s_mov_b32 s38,262144
ds_read_b64_tr_b16 v[88:89],v2 offset:20640
v_mul_f32 v132,v132,v53
v_mfma_f32_16x16x32_bf16 a[220:223],v[84:87],a[104:107],a[220:223]
buffer_store_dwordx4 v[40:43],v6,s[40:43],s81 offen sc1
v_mfma_f32_16x16x32_bf16 a[220:223],v[80:83],a[96:99],a[220:223]
s_mov_b32 s39,0x20000
ds_read_b64_tr_b16 v[80:81],v2 offset:160
ds_read_b64_tr_b16 v[82:83],v2 offset:5280
v_mfma_f32_16x16x32_bf16 a[224:227],v[96:99],a[68:71],0
s_add_u32 s74,s63,1
ds_read_b64_tr_b16 v[84:85],v2 offset:10400
v_mul_f32 v133,v133,v53
v_mfma_f32_16x16x32_bf16 a[224:227],v[100:103],a[76:79],a[224:227]
s_and_b32 s74,s74,3
ds_read_b64_tr_b16 v[86:87],v2 offset:15520
v_mul_f32 v134,v134,v53
v_mfma_f32_16x16x32_bf16 a[224:227],v[104:107],a[84:87],a[224:227]
s_lshl_b32 s74,s74,2
ds_read_b64_tr_b16 v[90:91],v2 offset:25760
v_mul_f32 v135,v135,v53
s_waitcnt lgkmcnt(8)
v_mfma_f32_16x16x32_bf16 a[224:227],v[108:111],a[92:95],a[224:227]
s_add_u32 s74,s74,s60
v_mul_f32 v140,v140,v53
v_mul_f32 v141,v141,v53
v_mfma_f32_16x16x32_bf16 a[228:231],v[108:111],a[124:127],0
s_mul_i32 s74,s74,16384
v_mul_f32 v142,v142,v53
v_mul_f32 v143,v143,v53
v_mfma_f32_16x16x32_bf16 a[228:231],v[104:107],a[116:119],a[228:231]
s_add_u32 s74,s74,8192
v_cvt_pk_bf16_f32 v132,v132,v133
v_cvt_pk_bf16_f32 v133,v134,v135
v_mfma_f32_16x16x32_bf16 a[228:231],v[100:103],a[108:111],a[228:231]
s_add_u32 s75,s63,2
v_cvt_pk_bf16_f32 v134,v140,v141
v_cvt_pk_bf16_f32 v135,v142,v143
v_mfma_f32_16x16x32_bf16 a[228:231],v[96:99],a[100:103],a[228:231]
s_and_b32 s75,s75,3
v_mul_f32 v148,v148,v53
v_mul_f32 v149,v149,v53
v_mfma_f32_16x16x32_bf16 a[232:235],v[96:99],a[64:67],0
s_lshl_b32 s75,s75,2
v_mul_f32 v150,v150,v53
v_mul_f32 v151,v151,v53
v_mfma_f32_16x16x32_bf16 a[232:235],v[100:103],a[72:75],a[232:235]
s_add_u32 s75,s75,s60
v_mul_f32 v156,v156,v53
v_mul_f32 v157,v157,v53
v_mfma_f32_16x16x32_bf16 a[232:235],v[104:107],a[80:83],a[232:235]
s_mul_i32 s75,s75,16384
v_mul_f32 v158,v158,v53
v_mul_f32 v159,v159,v53
v_mfma_f32_16x16x32_bf16 a[232:235],v[108:111],a[88:91],a[232:235]
s_add_u32 s75,s75,4096
v_cvt_pk_bf16_f32 v148,v148,v149
v_cvt_pk_bf16_f32 v149,v150,v151
v_mfma_f32_16x16x32_bf16 a[236:239],v[108:111],a[120:123],0
s_add_u32 s76,s63,3
ds_read_b64_tr_b16 v[108:109],v1 offset:30912
ds_read_b64_tr_b16 v[110:111],v1 offset:36032
v_mfma_f32_16x16x32_bf16 a[236:239],v[104:107],a[112:115],a[236:239]
buffer_store_dwordx4 v[44:47],v6,s[40:43],s82 offen sc1
v_mfma_f32_16x16x32_bf16 a[236:239],v[100:103],a[104:107],a[236:239]
s_and_b32 s76,s76,3
ds_read_b64_tr_b16 v[100:101],v1 offset:10432
ds_read_b64_tr_b16 v[102:103],v1 offset:15552
v_mfma_f32_16x16x32_bf16 a[236:239],v[96:99],a[96:99],a[236:239]
s_lshl_b32 s76,s76,2
ds_read_b64_tr_b16 v[96:97],v1 offset:192
v_cvt_pk_bf16_f32 v150,v156,v157
v_mfma_f32_16x16x32_bf16 a[240:243],v[112:115],a[68:71],0
s_add_u32 s76,s76,s60
ds_read_b64_tr_b16 v[98:99],v1 offset:5312
v_cvt_pk_bf16_f32 v151,v158,v159
v_mfma_f32_16x16x32_bf16 a[240:243],v[116:119],a[76:79],a[240:243]
s_mul_i32 s76,s76,16384
ds_read_b64_tr_b16 v[104:105],v1 offset:20672
v_mul_f32 v164,v164,v53
v_mfma_f32_16x16x32_bf16 a[240:243],v[120:123],a[84:87],a[240:243]
s_add_u32 s76,s76,0
ds_read_b64_tr_b16 v[106:107],v1 offset:25792
v_mul_f32 v165,v165,v53
v_mfma_f32_16x16x32_bf16 a[240:243],v[124:127],a[92:95],a[240:243]
s_lshl_b32 s67,s63,10
v_mul_f32 v166,v166,v53
v_mul_f32 v167,v167,v53
v_mfma_f32_16x16x32_bf16 a[244:247],v[124:127],a[124:127],0
s_mov_b32 s26,307200
v_mul_f32 v172,v172,v53
v_mul_f32 v173,v173,v53
v_mfma_f32_16x16x32_bf16 a[244:247],v[120:123],a[116:119],a[244:247]
s_mov_b32 s27,0x20000
v_mul_f32 v174,v174,v53
v_mul_f32 v175,v175,v53
v_mfma_f32_16x16x32_bf16 a[244:247],v[116:119],a[108:111],a[244:247]
s_lshl_b32 s77,s63,2
v_cvt_pk_bf16_f32 v164,v164,v165
v_cvt_pk_bf16_f32 v165,v166,v167
v_mfma_f32_16x16x32_bf16 a[244:247],v[112:115],a[100:103],a[244:247]
s_add_u32 s77,s77,s60
v_cvt_pk_bf16_f32 v166,v172,v173
v_cvt_pk_bf16_f32 v167,v174,v175
v_mfma_f32_16x16x32_bf16 a[248:251],v[112:115],a[64:67],0
s_mul_i32 s77,s77,16384
v_mul_f32 v180,v180,v53
v_mul_f32 v181,v181,v53
v_mfma_f32_16x16x32_bf16 a[248:251],v[116:119],a[72:75],a[248:251]
s_add_u32 s65,s77,0
v_mul_f32 v182,v182,v53
v_mul_f32 v183,v183,v53
v_mfma_f32_16x16x32_bf16 a[248:251],v[120:123],a[80:83],a[248:251]
s_add_u32 s68,s77,12288
v_mul_f32 v188,v188,v53
v_mul_f32 v189,v189,v53
v_mfma_f32_16x16x32_bf16 a[248:251],v[124:127],a[88:91],a[248:251]
v_mul_f32 v190,v190,v53
v_mul_f32 v191,v191,v53
v_mfma_f32_16x16x32_bf16 a[252:255],v[124:127],a[120:123],0
buffer_load_dword v30,v29,s[8:11],s7 offen
v_mfma_f32_16x16x32_bf16 a[252:255],v[120:123],a[112:115],a[252:255]
ds_read_b64_tr_b16 v[120:121],v2 offset:20704
ds_read_b64_tr_b16 v[122:123],v2 offset:25824
v_mfma_f32_16x16x32_bf16 a[252:255],v[116:119],a[104:107],a[252:255]
buffer_store_dwordx4 v[132:135],v6,s[40:43],s79 offen offset:1024 sc1
v_mfma_f32_16x16x32_bf16 a[252:255],v[112:115],a[96:99],a[252:255]
ds_read_b64_tr_b16 v[112:113],v2 offset:224
ds_read_b64_tr_b16 v[114:115],v2 offset:5344
v_mfma_f32_16x16x32_bf16 a[0:3],v[64:67],a[68:71],0
ds_read_b64_tr_b16 v[116:117],v2 offset:10464
v_cvt_pk_bf16_f32 v180,v180,v181
v_mfma_f32_16x16x32_bf16 a[0:3],v[68:71],a[76:79],a[0:3]
ds_read_b64_tr_b16 v[118:119],v2 offset:15584
v_cvt_pk_bf16_f32 v181,v182,v183
v_mfma_f32_16x16x32_bf16 a[0:3],v[72:75],a[84:87],a[0:3]
ds_read_b64_tr_b16 v[124:125],v2 offset:30944
v_cvt_pk_bf16_f32 v182,v188,v189
v_mfma_f32_16x16x32_bf16 a[0:3],v[76:79],a[92:95],a[0:3]
ds_read_b64_tr_b16 v[126:127],v2 offset:36064
v_cvt_pk_bf16_f32 v183,v190,v191
v_mfma_f32_16x16x32_bf16 a[4:7],v[76:79],a[124:127],0
v_mul_f32 v196,v196,v57
v_mul_f32 v197,v197,v57
v_mfma_f32_16x16x32_bf16 a[4:7],v[72:75],a[116:119],a[4:7]
v_mul_f32 v198,v198,v57
v_mul_f32 v199,v199,v57
v_mfma_f32_16x16x32_bf16 a[4:7],v[68:71],a[108:111],a[4:7]
v_mul_f32 v204,v204,v57
v_mul_f32 v205,v205,v57
v_mfma_f32_16x16x32_bf16 a[4:7],v[64:67],a[100:103],a[4:7]
v_mul_f32 v206,v206,v57
v_mul_f32 v207,v207,v57
v_mfma_f32_16x16x32_bf16 a[8:11],v[64:67],a[64:67],0
v_cvt_pk_bf16_f32 v196,v196,v197
v_cvt_pk_bf16_f32 v197,v198,v199
v_mfma_f32_16x16x32_bf16 a[8:11],v[68:71],a[72:75],a[8:11]
v_cvt_pk_bf16_f32 v198,v204,v205
v_cvt_pk_bf16_f32 v199,v206,v207
v_mfma_f32_16x16x32_bf16 a[8:11],v[72:75],a[80:83],a[8:11]
v_mul_f32 v212,v212,v57
v_mul_f32 v213,v213,v57
v_mfma_f32_16x16x32_bf16 a[8:11],v[76:79],a[88:91],a[8:11]
v_mul_f32 v214,v214,v57
v_mul_f32 v215,v215,v57
v_mfma_f32_16x16x32_bf16 a[12:15],v[76:79],a[120:123],0
ds_read_b64_tr_b16 v[76:77],v1 offset:30976
ds_read_b64_tr_b16 v[78:79],v1 offset:36096
v_mfma_f32_16x16x32_bf16 a[12:15],v[72:75],a[112:115],a[12:15]
buffer_store_dwordx4 v[148:151],v6,s[40:43],s80 offen offset:1024 sc1
v_mfma_f32_16x16x32_bf16 a[12:15],v[68:71],a[104:107],a[12:15]
ds_read_b64_tr_b16 v[68:69],v1 offset:10496
ds_read_b64_tr_b16 v[70:71],v1 offset:15616
v_mfma_f32_16x16x32_bf16 a[12:15],v[64:67],a[96:99],a[12:15]
s_waitcnt lgkmcnt(4)
ds_read_b64_tr_b16 v[64:65],v1 offset:256
v_mul_f32 v220,v220,v57
v_mfma_f32_16x16x32_bf16 a[16:19],v[80:83],a[68:71],0
ds_read_b64_tr_b16 v[66:67],v1 offset:5376
v_mul_f32 v221,v221,v57
v_mfma_f32_16x16x32_bf16 a[16:19],v[84:87],a[76:79],a[16:19]
ds_read_b64_tr_b16 v[72:73],v1 offset:20736
v_mul_f32 v222,v222,v57
v_mfma_f32_16x16x32_bf16 a[16:19],v[88:91],a[84:87],a[16:19]
ds_read_b64_tr_b16 v[74:75],v1 offset:25856
v_add_u32 v1,0xffff6000,v1
v_mfma_f32_16x16x32_bf16 a[16:19],v[92:95],a[92:95],a[16:19]
v_mul_f32 v223,v223,v57
v_cvt_pk_bf16_f32 v212,v212,v213
v_mfma_f32_16x16x32_bf16 a[20:23],v[92:95],a[124:127],0
v_cvt_pk_bf16_f32 v213,v214,v215
v_cvt_pk_bf16_f32 v214,v220,v221
v_mfma_f32_16x16x32_bf16 a[20:23],v[88:91],a[116:119],a[20:23]
v_cvt_pk_bf16_f32 v215,v222,v223
v_mul_f32 v228,v228,v57
v_mfma_f32_16x16x32_bf16 a[20:23],v[84:87],a[108:111],a[20:23]
v_mul_f32 v229,v229,v57
v_mul_f32 v230,v230,v57
v_mfma_f32_16x16x32_bf16 a[20:23],v[80:83],a[100:103],a[20:23]
v_mul_f32 v231,v231,v57
v_mul_f32 v236,v236,v57
v_mfma_f32_16x16x32_bf16 a[24:27],v[80:83],a[64:67],0
v_mul_f32 v237,v237,v57
v_mul_f32 v238,v238,v57
v_mfma_f32_16x16x32_bf16 a[24:27],v[84:87],a[72:75],a[24:27]
v_mul_f32 v239,v239,v57
v_cvt_pk_bf16_f32 v228,v228,v229
v_mfma_f32_16x16x32_bf16 a[24:27],v[88:91],a[80:83],a[24:27]
s_waitcnt vmcnt(5)
v_cmp_ne_u32 s[56:57],s52,v4
v_cmp_ne_u32 s[58:59],s53,v5
v_mfma_f32_16x16x32_bf16 a[24:27],v[92:95],a[88:91],a[24:27]
s_or_b64 s[56:57],s[56:57],s[58:59]
s_cbranch_scc1 L_fr1_%=
L_fo1_%=:
v_cvt_pk_bf16_f32 v229,v230,v231
v_mfma_f32_16x16x32_bf16 a[28:31],v[92:95],a[120:123],0
buffer_store_dwordx4 v[164:167],v6,s[40:43],s81 offen offset:1024 sc1
v_mfma_f32_16x16x32_bf16 a[28:31],v[88:91],a[112:115],a[28:31]
)A"
R"A(s_lshl_b32 s91,s63,8
ds_read_b64_tr_b16 v[88:89],v2 offset:20768
ds_read_b64_tr_b16 v[90:91],v2 offset:25888
v_mfma_f32_16x16x32_bf16 a[28:31],v[84:87],a[104:107],a[28:31]
buffer_load_dwordx4 a[128:131],v6,s[28:31],s87 offen sc1
v_mfma_f32_16x16x32_bf16 a[28:31],v[80:83],a[96:99],a[28:31]
ds_read_b64_tr_b16 v[80:81],v2 offset:288
ds_read_b64_tr_b16 v[82:83],v2 offset:5408
v_mfma_f32_16x16x32_bf16 v[128:131],v[96:99],a[68:71],0
ds_read_b64_tr_b16 v[84:85],v2 offset:10528
v_cvt_pk_bf16_f32 v230,v236,v237
v_mfma_f32_16x16x32_bf16 v[128:131],v[100:103],a[76:79],v[128:131]
ds_read_b64_tr_b16 v[86:87],v2 offset:15648
v_cvt_pk_bf16_f32 v231,v238,v239
v_mfma_f32_16x16x32_bf16 v[128:131],v[104:107],a[84:87],v[128:131]
ds_read_b64_tr_b16 v[92:93],v2 offset:31008
v_mul_f32 v244,v244,v57
v_mfma_f32_16x16x32_bf16 v[128:131],v[108:111],a[92:95],v[128:131]
ds_read_b64_tr_b16 v[94:95],v2 offset:36128
v_add_u32 v2,0xffff6000,v2
v_mfma_f32_16x16x32_bf16 v[136:139],v[108:111],a[124:127],0
v_mul_f32 v245,v245,v57
v_mul_f32 v246,v246,v57
v_mfma_f32_16x16x32_bf16 v[136:139],v[104:107],a[116:119],v[136:139]
v_mul_f32 v247,v247,v57
v_mul_f32 v252,v252,v57
v_mfma_f32_16x16x32_bf16 v[136:139],v[100:103],a[108:111],v[136:139]
v_mul_f32 v253,v253,v57
v_mul_f32 v254,v254,v57
v_mfma_f32_16x16x32_bf16 v[136:139],v[96:99],a[100:103],v[136:139]
v_mul_f32 v255,v255,v57
v_cvt_pk_bf16_f32 v244,v244,v245
v_mfma_f32_16x16x32_bf16 v[144:147],v[96:99],a[64:67],0
v_cvt_pk_bf16_f32 v245,v246,v247
v_cvt_pk_bf16_f32 v246,v252,v253
v_mfma_f32_16x16x32_bf16 v[144:147],v[100:103],a[72:75],v[144:147]
buffer_load_dwordx4 a[144:147],v6,s[28:31],s88 offen sc1
v_mfma_f32_16x16x32_bf16 v[144:147],v[104:107],a[80:83],v[144:147]
v_cvt_pk_bf16_f32 v247,v254,v255
v_accvgpr_read_b32 v8,a40
v_mfma_f32_16x16x32_bf16 v[144:147],v[108:111],a[88:91],v[144:147]
buffer_store_dwordx4 v[180:183],v6,s[40:43],s82 offen offset:1024 sc1
v_mfma_f32_16x16x32_bf16 v[152:155],v[108:111],a[120:123],0
ds_read_b64_tr_b16 v[108:109],v1 offset:30720
ds_read_b64_tr_b16 v[110:111],v1 offset:35840
v_mfma_f32_16x16x32_bf16 v[152:155],v[104:107],a[112:115],v[152:155]
ds_read_b64_tr_b16 v[104:105],v1 offset:20480
v_accvgpr_read_b32 v9,a41
v_mfma_f32_16x16x32_bf16 v[152:155],v[100:103],a[104:107],v[152:155]
ds_read_b64_tr_b16 v[100:101],v1 offset:10240
v_accvgpr_read_b32 v10,a42
v_mfma_f32_16x16x32_bf16 v[152:155],v[96:99],a[96:99],v[152:155]
ds_read_b64_tr_b16 v[96:97],v1
v_accvgpr_read_b32 v11,a43
v_mfma_f32_16x16x32_bf16 v[160:163],v[112:115],a[68:71],0
ds_read_b64_tr_b16 v[98:99],v1 offset:5120
ds_read_b64_tr_b16 v[102:103],v1 offset:15360
v_mfma_f32_16x16x32_bf16 v[160:163],v[116:119],a[76:79],v[160:163]
ds_read_b64_tr_b16 v[106:107],v1 offset:25600
v_accvgpr_read_b32 v12,a44
v_mfma_f32_16x16x32_bf16 v[160:163],v[120:123],a[84:87],v[160:163]
v_accvgpr_read_b32 v13,a45
v_accvgpr_read_b32 v14,a46
v_mfma_f32_16x16x32_bf16 v[160:163],v[124:127],a[92:95],v[160:163]
v_accvgpr_read_b32 v15,a47
v_mul_f32 v8,v8,v61
v_mfma_f32_16x16x32_bf16 v[168:171],v[124:127],a[124:127],0
v_mul_f32 v9,v9,v61
v_mul_f32 v10,v10,v61
v_mfma_f32_16x16x32_bf16 v[168:171],v[120:123],a[116:119],v[168:171]
v_mul_f32 v11,v11,v61
v_mul_f32 v12,v12,v61
v_mfma_f32_16x16x32_bf16 v[168:171],v[116:119],a[108:111],v[168:171]
buffer_load_dwordx4 a[160:163],v6,s[28:31],s89 offen sc1
v_mfma_f32_16x16x32_bf16 v[168:171],v[112:115],a[100:103],v[168:171]
v_mul_f32 v13,v13,v61
v_mul_f32 v14,v14,v61
v_mfma_f32_16x16x32_bf16 v[176:179],v[112:115],a[64:67],0
v_mul_f32 v15,v15,v61
v_cvt_pk_bf16_f32 v156,v8,v9
v_mfma_f32_16x16x32_bf16 v[176:179],v[116:119],a[72:75],v[176:179]
v_cvt_pk_bf16_f32 v157,v10,v11
v_cvt_pk_bf16_f32 v158,v12,v13
v_mfma_f32_16x16x32_bf16 v[176:179],v[120:123],a[80:83],v[176:179]
buffer_store_dwordx4 v[196:199],v6,s[40:43],s79 offen offset:2048 sc1
v_mfma_f32_16x16x32_bf16 v[176:179],v[124:127],a[88:91],v[176:179]
v_cvt_pk_bf16_f32 v159,v14,v15
v_accvgpr_read_b32 v8,a56
v_mfma_f32_16x16x32_bf16 v[184:187],v[124:127],a[120:123],0
v_accvgpr_read_b32 v9,a57
v_accvgpr_read_b32 v10,a58
v_mfma_f32_16x16x32_bf16 v[184:187],v[120:123],a[112:115],v[184:187]
ds_read_b64_tr_b16 v[120:121],v2 offset:20512
ds_read_b64_tr_b16 v[122:123],v2 offset:25632
v_mfma_f32_16x16x32_bf16 v[184:187],v[116:119],a[104:107],v[184:187]
ds_read_b64_tr_b16 v[116:117],v2 offset:10272
v_accvgpr_read_b32 v11,a59
v_mfma_f32_16x16x32_bf16 v[184:187],v[112:115],a[96:99],v[184:187]
s_waitcnt lgkmcnt(3)
ds_read_b64_tr_b16 v[112:113],v2 offset:32
v_accvgpr_read_b32 v12,a60
v_mfma_f32_16x16x32_bf16 v[192:195],v[64:67],a[68:71],0
ds_read_b64_tr_b16 v[114:115],v2 offset:5152
v_accvgpr_read_b32 v13,a61
v_mfma_f32_16x16x32_bf16 v[192:195],v[68:71],a[76:79],v[192:195]
ds_read_b64_tr_b16 v[118:119],v2 offset:15392
v_accvgpr_read_b32 v14,a62
v_mfma_f32_16x16x32_bf16 v[192:195],v[72:75],a[84:87],v[192:195]
v_accvgpr_read_b32 v15,a63
v_mul_f32 v8,v8,v61
v_mfma_f32_16x16x32_bf16 v[192:195],v[76:79],a[92:95],v[192:195]
buffer_load_dwordx4 a[176:179],v6,s[28:31],s90 offen sc1
v_mfma_f32_16x16x32_bf16 v[200:203],v[76:79],a[124:127],0
v_mul_f32 v9,v9,v61
v_mul_f32 v10,v10,v61
v_mfma_f32_16x16x32_bf16 v[200:203],v[72:75],a[116:119],v[200:203]
v_mul_f32 v11,v11,v61
v_mul_f32 v12,v12,v61
v_mfma_f32_16x16x32_bf16 v[200:203],v[68:71],a[108:111],v[200:203]
v_mul_f32 v13,v13,v61
v_mul_f32 v14,v14,v61
v_mfma_f32_16x16x32_bf16 v[200:203],v[64:67],a[100:103],v[200:203]
v_mul_f32 v15,v15,v61
v_cvt_pk_bf16_f32 v188,v8,v9
v_mfma_f32_16x16x32_bf16 v[208:211],v[64:67],a[64:67],0
v_cvt_pk_bf16_f32 v189,v10,v11
v_cvt_pk_bf16_f32 v190,v12,v13
v_mfma_f32_16x16x32_bf16 v[208:211],v[68:71],a[72:75],v[208:211]
buffer_store_dwordx4 v[212:215],v6,s[40:43],s80 offen offset:2048 sc1
v_mfma_f32_16x16x32_bf16 v[208:211],v[72:75],a[80:83],v[208:211]
ds_read_b64_tr_b16 v[124:125],v2 offset:30752
v_cvt_pk_bf16_f32 v191,v14,v15
v_mfma_f32_16x16x32_bf16 v[208:211],v[76:79],a[88:91],v[208:211]
ds_read_b64_tr_b16 v[126:127],v2 offset:35872
v_accvgpr_read_b32 v16,a32
v_mfma_f32_16x16x32_bf16 v[216:219],v[76:79],a[120:123],0
v_accvgpr_read_b32 v17,a33
v_accvgpr_read_b32 v18,a34
v_mfma_f32_16x16x32_bf16 v[216:219],v[72:75],a[112:115],v[216:219]
v_accvgpr_read_b32 v19,a35
v_accvgpr_read_b32 v20,a36
v_mfma_f32_16x16x32_bf16 v[216:219],v[68:71],a[104:107],v[216:219]
v_accvgpr_read_b32 v21,a37
v_accvgpr_read_b32 v22,a38
v_mfma_f32_16x16x32_bf16 v[216:219],v[64:67],a[96:99],v[216:219]
v_accvgpr_read_b32 v23,a39
v_mul_f32 v16,v16,v61
v_mfma_f32_16x16x32_bf16 v[224:227],v[80:83],a[68:71],0
buffer_load_dwordx4 a[180:183],v6,s[28:31],s90 offen offset:1024 sc1
v_mfma_f32_16x16x32_bf16 v[224:227],v[84:87],a[76:79],v[224:227]
v_mul_f32 v17,v17,v61
v_mul_f32 v18,v18,v61
v_mfma_f32_16x16x32_bf16 v[224:227],v[88:91],a[84:87],v[224:227]
v_mul_f32 v19,v19,v61
v_mul_f32 v20,v20,v61
v_mfma_f32_16x16x32_bf16 v[224:227],v[92:95],a[92:95],v[224:227]
v_mul_f32 v21,v21,v61
v_mul_f32 v22,v22,v61
v_mfma_f32_16x16x32_bf16 v[232:235],v[92:95],a[124:127],0
v_mul_f32 v23,v23,v61
v_cvt_pk_bf16_f32 v140,v16,v17
v_mfma_f32_16x16x32_bf16 v[232:235],v[88:91],a[116:119],v[232:235]
v_cvt_pk_bf16_f32 v141,v18,v19
v_cvt_pk_bf16_f32 v142,v20,v21
v_mfma_f32_16x16x32_bf16 v[232:235],v[84:87],a[108:111],v[232:235]
v_cvt_pk_bf16_f32 v143,v22,v23
v_accvgpr_read_b32 v16,a48
v_mfma_f32_16x16x32_bf16 v[232:235],v[80:83],a[100:103],v[232:235]
v_accvgpr_read_b32 v17,a49
v_accvgpr_read_b32 v18,a50
v_mfma_f32_16x16x32_bf16 v[240:243],v[80:83],a[64:67],0
buffer_store_dwordx4 v[228:231],v6,s[40:43],s81 offen offset:2048 sc1
v_mfma_f32_16x16x32_bf16 v[240:243],v[84:87],a[72:75],v[240:243]
v_accvgpr_read_b32 v19,a51
v_accvgpr_read_b32 v20,a52
v_mfma_f32_16x16x32_bf16 v[240:243],v[88:91],a[80:83],v[240:243]
v_accvgpr_read_b32 v21,a53
v_accvgpr_read_b32 v22,a54
v_mfma_f32_16x16x32_bf16 v[240:243],v[92:95],a[88:91],v[240:243]
v_accvgpr_read_b32 v23,a55
v_mul_f32 v16,v16,v61
v_mfma_f32_16x16x32_bf16 v[248:251],v[92:95],a[120:123],0
v_mul_f32 v17,v17,v61
v_mul_f32 v18,v18,v61
v_mfma_f32_16x16x32_bf16 v[248:251],v[88:91],a[112:115],v[248:251]
buffer_load_dwordx4 a[164:167],v6,s[28:31],s89 offen offset:1024 sc1
v_mfma_f32_16x16x32_bf16 v[248:251],v[84:87],a[104:107],v[248:251]
v_mul_f32 v19,v19,v61
v_mul_f32 v20,v20,v61
v_mfma_f32_16x16x32_bf16 v[248:251],v[80:83],a[96:99],v[248:251]
v_mul_f32 v21,v21,v61
v_mul_f32 v22,v22,v61
v_mfma_f32_16x16x32_bf16 a[192:195],v[96:99],v[32:35],a[192:195]
v_mul_f32 v23,v23,v61
v_cvt_pk_bf16_f32 v172,v16,v17
v_mfma_f32_16x16x32_bf16 a[192:195],v[100:103],v[36:39],a[192:195]
v_cvt_pk_bf16_f32 v173,v18,v19
v_cvt_pk_bf16_f32 v174,v20,v21
v_mfma_f32_16x16x32_bf16 a[192:195],v[104:107],v[40:43],a[192:195]
buffer_load_dwordx4 a[68:71],v6,s[28:31],s20 offen sc1
v_mfma_f32_16x16x32_bf16 a[192:195],v[108:111],v[44:47],a[192:195]
ds_read_b64_tr_b16 v[64:65],v1 offset:64
v_cvt_pk_bf16_f32 v175,v22,v23
v_mfma_f32_16x16x32_bf16 a[196:199],v[108:111],v[180:183],a[196:199]
ds_read_b64_tr_b16 v[66:67],v1 offset:5184
ds_read_b64_tr_b16 v[68:69],v1 offset:10304
v_mfma_f32_16x16x32_bf16 a[196:199],v[104:107],v[164:167],a[196:199]
s_cmp_eq_u32 s60,3
s_cselect_b32 s69,0x40000000,0
ds_read_b64_tr_b16 v[70:71],v1 offset:15424
v_mfma_f32_16x16x32_bf16 a[196:199],v[100:103],v[148:151],a[196:199]
ds_read_b64_tr_b16 v[72:73],v1 offset:20544
v_add_u32 v28,s69,v6
v_mfma_f32_16x16x32_bf16 a[196:199],v[96:99],v[132:135],a[196:199]
buffer_store_dwordx4 v[244:247],v6,s[40:43],s82 offen offset:2048 sc1
v_mfma_f32_16x16x32_bf16 a[200:203],v[96:99],v[196:199],a[200:203]
s_lshl_b32 s69,s63,2
ds_read_b64_tr_b16 v[74:75],v1 offset:25664
ds_read_b64_tr_b16 v[76:77],v1 offset:30784
v_mfma_f32_16x16x32_bf16 a[200:203],v[100:103],v[212:215],a[200:203]
ds_read_b64_tr_b16 v[78:79],v1 offset:35904
v_mov_b32 v8,s52
v_mfma_f32_16x16x32_bf16 a[200:203],v[104:107],v[228:231],a[200:203]
buffer_load_dwordx4 a[148:151],v6,s[28:31],s88 offen offset:1024 sc1
v_mfma_f32_16x16x32_bf16 a[200:203],v[108:111],v[244:247],a[200:203]
ds_read_b64_tr_b16 v[80:81],v2 offset:96
ds_read_b64_tr_b16 v[82:83],v2 offset:5216
v_mfma_f32_16x16x32_bf16 a[204:207],v[108:111],v[188:191],a[204:207]
ds_read_b64_tr_b16 v[84:85],v2 offset:10336
v_mov_b32 v9,s53
v_mfma_f32_16x16x32_bf16 a[204:207],v[104:107],v[172:175],a[204:207]
ds_read_b64_tr_b16 v[86:87],v2 offset:15456
v_lshlrev_b32 v10,20,v6
v_mfma_f32_16x16x32_bf16 a[204:207],v[100:103],v[156:159],a[204:207]
s_waitcnt lgkmcnt(7)
ds_read_b64_tr_b16 v[88:89],v2 offset:20576
v_bfe_u32 v3,v6,4,2
v_mfma_f32_16x16x32_bf16 a[204:207],v[96:99],v[140:143],a[204:207]
buffer_load_dwordx4 a[76:79],v6,s[28:31],s21 offen sc1
v_mfma_f32_16x16x32_bf16 a[208:211],v[112:115],v[32:35],a[208:211]
ds_read_b64_tr_b16 v[90:91],v2 offset:25696
ds_read_b64_tr_b16 v[92:93],v2 offset:30816
v_mfma_f32_16x16x32_bf16 a[208:211],v[116:119],v[36:39],a[208:211]
ds_read_b64_tr_b16 v[94:95],v2 offset:35936
v_min_u32 v3,2,v3
v_mfma_f32_16x16x32_bf16 a[208:211],v[120:123],v[40:43],a[208:211]
ds_read_b64_tr_b16 v[96:97],v1 offset:128
v_add3_u32 v3,v3,s64,1
v_mfma_f32_16x16x32_bf16 a[208:211],v[124:127],v[44:47],a[208:211]
ds_read_b64_tr_b16 v[98:99],v1 offset:5248
v_and_b32 v3,3,v3
v_mfma_f32_16x16x32_bf16 a[212:215],v[124:127],v[180:183],a[212:215]
ds_read_b64_tr_b16 v[100:101],v1 offset:10368
ds_read_b64_tr_b16 v[102:103],v1 offset:15488
v_mfma_f32_16x16x32_bf16 a[212:215],v[120:123],v[164:167],a[212:215]
ds_read_b64_tr_b16 v[104:105],v1 offset:20608
v_lshlrev_b32 v3,6,v3
v_mfma_f32_16x16x32_bf16 a[212:215],v[116:119],v[148:151],a[212:215]
buffer_store_dwordx4 v[140:143],v28,s[40:43],s79 offen offset:3072 sc1
v_mfma_f32_16x16x32_bf16 a[212:215],v[112:115],v[132:135],a[212:215]
ds_read_b64_tr_b16 v[106:107],v1 offset:25728
ds_read_b64_tr_b16 v[108:109],v1 offset:30848
v_mfma_f32_16x16x32_bf16 a[216:219],v[112:115],v[196:199],a[216:219]
buffer_load_dwordx4 a[132:135],v6,s[28:31],s87 offen offset:1024 sc1
v_mfma_f32_16x16x32_bf16 a[216:219],v[116:119],v[212:215],a[216:219]
ds_read_b64_tr_b16 v[110:111],v1 offset:35968
v_bfe_u32 v56,v6,6,2
v_mfma_f32_16x16x32_bf16 a[216:219],v[120:123],v[228:231],a[216:219]
)A"
R"A(v_lshrrev_b32 v57,8,v6
v_cmp_eq_u32 s[56:57],v56,v57
v_mfma_f32_16x16x32_bf16 a[216:219],v[124:127],v[244:247],a[216:219]
v_bfe_u32 v56,v6,4,1
v_lshlrev_b32 v56,4,v56
v_mfma_f32_16x16x32_bf16 a[220:223],v[124:127],v[188:191],a[220:223]
ds_read_b64_tr_b16 v[124:125],v2 offset:30880
ds_read_b64_tr_b16 v[126:127],v2 offset:36000
v_mfma_f32_16x16x32_bf16 a[220:223],v[120:123],v[172:175],a[220:223]
buffer_load_dwordx4 a[84:87],v6,s[28:31],s22 offen sc1
v_mfma_f32_16x16x32_bf16 a[220:223],v[116:119],v[156:159],a[220:223]
ds_read_b64_tr_b16 v[116:117],v2 offset:10400
ds_read_b64_tr_b16 v[118:119],v2 offset:15520
v_mfma_f32_16x16x32_bf16 a[220:223],v[112:115],v[140:143],a[220:223]
ds_read_b64_tr_b16 v[112:113],v2 offset:160
v_mov_b32 v57,0x3f80
v_mfma_f32_16x16x32_bf16 a[224:227],v[64:67],v[32:35],a[224:227]
ds_read_b64_tr_b16 v[114:115],v2 offset:5280
v_lshlrev_b32 v56,v56,v57
v_mfma_f32_16x16x32_bf16 a[224:227],v[68:71],v[36:39],a[224:227]
s_waitcnt lgkmcnt(6)
ds_read_b64_tr_b16 v[120:121],v2 offset:20640
v_cndmask_b32 v56,0,v56,s[56:57]
v_mfma_f32_16x16x32_bf16 a[224:227],v[72:75],v[40:43],a[224:227]
ds_read_b64_tr_b16 v[122:123],v2 offset:25760
v_bfe_u32 v57,v6,5,1
v_mfma_f32_16x16x32_bf16 a[224:227],v[76:79],v[44:47],a[224:227]
s_add_u32 s36,s12,s66
s_addc_u32 s37,s13,0
v_cmp_eq_u32 s[56:57],0,v57
v_mfma_f32_16x16x32_bf16 a[228:231],v[76:79],v[180:183],a[228:231]
buffer_load_dwordx4 a[136:139],v6,s[28:31],s87 offen offset:2048 sc1
v_mfma_f32_16x16x32_bf16 a[228:231],v[72:75],v[164:167],a[228:231]
s_and_b32 s37,s37,0xffff
v_cndmask_b32 v48,0,v56,s[56:57]
v_cndmask_b32 v49,v56,0,s[56:57]
v_mfma_f32_16x16x32_bf16 a[228:231],v[68:71],v[148:151],a[228:231]
buffer_store_dwordx4 v[156:159],v28,s[40:43],s80 offen offset:3072 sc1
v_mfma_f32_16x16x32_bf16 a[228:231],v[64:67],v[132:135],a[228:231]
s_mul_i32 s66,s61,307200
v_mov_b32 v58,s52
v_mov_b32 v59,s53
v_mfma_f32_16x16x32_bf16 a[232:235],v[64:67],v[196:199],a[232:235]
s_add_u32 s24,s16,s66
s_addc_u32 s25,s17,0
v_lshlrev_b32 v60,20,v6
v_mfma_f32_16x16x32_bf16 a[232:235],v[68:71],v[212:215],a[232:235]
buffer_load_dwordx4 a[92:95],v6,s[28:31],s23 offen sc1
v_mfma_f32_16x16x32_bf16 a[232:235],v[72:75],v[228:231],a[232:235]
s_mul_i32 s66,s60,16384
s_add_u32 s66,s66,s67
s_add_u32 s79,s66,12288
v_mfma_f32_16x16x32_bf16 a[232:235],v[76:79],v[244:247],a[232:235]
s_add_u32 s80,s66,77824
v_bfe_u32 v61,v6,4,1
v_add3_u32 v61,v61,s63,2
v_mfma_f32_16x16x32_bf16 a[236:239],v[76:79],v[188:191],a[236:239]
s_and_b32 s25,s25,0xffff
ds_read_b64_tr_b16 v[76:77],v1 offset:30912
ds_read_b64_tr_b16 v[78:79],v1 offset:36032
v_mfma_f32_16x16x32_bf16 a[236:239],v[72:75],v[172:175],a[236:239]
s_add_u32 s67,s77,8192
ds_read_b64_tr_b16 v[72:73],v1 offset:20672
v_and_b32 v61,3,v61
v_mfma_f32_16x16x32_bf16 a[236:239],v[68:71],v[156:159],a[236:239]
ds_read_b64_tr_b16 v[68:69],v1 offset:10432
v_lshlrev_b32 v61,8,v61
v_mfma_f32_16x16x32_bf16 a[236:239],v[64:67],v[140:143],a[236:239]
ds_read_b64_tr_b16 v[64:65],v1 offset:192
v_mov_b32 v50,0
v_mfma_f32_16x16x32_bf16 a[240:243],v[80:83],v[32:35],a[240:243]
ds_read_b64_tr_b16 v[66:67],v1 offset:5312
L_pd14_%=:
s_cbranch_execz L_trt1_%=
ds_read_b64_tr_b16 v[70:71],v1 offset:15552
v_mfma_f32_16x16x32_bf16 a[240:243],v[84:87],v[36:39],a[240:243]
buffer_load_dwordx4 a[152:155],v6,s[28:31],s88 offen offset:2048 sc1
v_mfma_f32_16x16x32_bf16 a[240:243],v[88:91],v[40:43],a[240:243]
ds_read_b64_tr_b16 v[74:75],v1 offset:25792
v_mov_b32 v51,0
v_mfma_f32_16x16x32_bf16 a[240:243],v[92:95],v[44:47],a[240:243]
v_mov_b32 v52,0
v_mov_b32 v53,0
v_mfma_f32_16x16x32_bf16 a[244:247],v[92:95],v[180:183],a[244:247]
v_mov_b32 v54,v48
v_mov_b32 v55,v49
v_mfma_f32_16x16x32_bf16 a[244:247],v[88:91],v[164:167],a[244:247]
buffer_store_dwordx4 v[172:175],v28,s[40:43],s81 offen offset:3072 sc1
v_mfma_f32_16x16x32_bf16 a[244:247],v[84:87],v[148:151],a[244:247]
s_add_u32 s81,s66,143360
v_bfe_u32 v56,v6,4,4
v_mul_u32_u24 v56,1280,v56
v_mfma_f32_16x16x32_bf16 a[244:247],v[80:83],v[132:135],a[244:247]
buffer_load_dwordx4 a[124:127],v6,s[28:31],s23 offen offset:1024 sc1
v_mfma_f32_16x16x32_bf16 a[248:251],v[80:83],v[196:199],a[248:251]
v_bfe_u32 v57,v6,8,1
v_lshl_add_u32 v56,v57,5,v56
v_mfma_f32_16x16x32_bf16 a[248:251],v[84:87],v[212:215],a[248:251]
v_lshrrev_b32 v57,9,v6
v_lshl_add_u32 v56,v57,4,v56
v_mfma_f32_16x16x32_bf16 a[248:251],v[88:91],v[228:231],a[248:251]
v_mfma_f32_16x16x32_bf16 a[248:251],v[92:95],v[244:247],a[248:251]
v_mfma_f32_16x16x32_bf16 a[252:255],v[92:95],v[188:191],a[252:255]
ds_read_b64_tr_b16 v[92:93],v2 offset:30944
ds_read_b64_tr_b16 v[94:95],v2 offset:36064
v_mfma_f32_16x16x32_bf16 a[252:255],v[88:91],v[172:175],a[252:255]
ds_read_b64_tr_b16 v[88:89],v2 offset:20704
v_mfma_f32_16x16x32_bf16 a[252:255],v[84:87],v[156:159],a[252:255]
buffer_load_dwordx4 a[168:171],v6,s[28:31],s89 offen offset:2048 sc1
v_mfma_f32_16x16x32_bf16 a[252:255],v[80:83],v[140:143],a[252:255]
ds_read_b64_tr_b16 v[80:81],v2 offset:224
ds_read_b64_tr_b16 v[82:83],v2 offset:5344
v_mfma_f32_16x16x32_bf16 a[0:3],v[96:99],v[32:35],a[0:3]
ds_read_b64_tr_b16 v[84:85],v2 offset:10464
v_mfma_f32_16x16x32_bf16 a[0:3],v[100:103],v[36:39],a[0:3]
ds_read_b64_tr_b16 v[86:87],v2 offset:15584
v_mfma_f32_16x16x32_bf16 a[0:3],v[104:107],v[40:43],a[0:3]
ds_read_b64_tr_b16 v[90:91],v2 offset:25824
v_mfma_f32_16x16x32_bf16 a[0:3],v[108:111],v[44:47],a[0:3]
buffer_load_dwordx4 a[116:119],v6,s[28:31],s22 offen offset:1024 sc1
v_mfma_f32_16x16x32_bf16 a[4:7],v[108:111],v[180:183],a[4:7]
v_mfma_f32_16x16x32_bf16 a[4:7],v[104:107],v[164:167],a[4:7]
buffer_store_dwordx4 v[188:191],v28,s[40:43],s82 offen offset:3072 sc1
v_mfma_f32_16x16x32_bf16 a[4:7],v[100:103],v[148:151],a[4:7]
s_add_u32 s82,s66,208896
s_mul_i32 s66,s64,320
v_mfma_f32_16x16x32_bf16 a[4:7],v[96:99],v[132:135],a[4:7]
v_mfma_f32_16x16x32_bf16 a[8:11],v[96:99],v[196:199],a[8:11]
v_mfma_f32_16x16x32_bf16 a[8:11],v[100:103],v[212:215],a[8:11]
v_mfma_f32_16x16x32_bf16 a[8:11],v[104:107],v[228:231],a[8:11]
v_mfma_f32_16x16x32_bf16 a[8:11],v[108:111],v[244:247],a[8:11]
buffer_load_dwordx4 a[184:187],v6,s[28:31],s90 offen offset:2048 sc1
v_mfma_f32_16x16x32_bf16 a[12:15],v[108:111],v[188:191],a[12:15]
ds_read_b64_tr_b16 v[108:109],v1 offset:30976
ds_read_b64_tr_b16 v[110:111],v1 offset:36096
v_mfma_f32_16x16x32_bf16 a[12:15],v[104:107],v[172:175],a[12:15]
ds_read_b64_tr_b16 v[104:105],v1 offset:20736
v_mfma_f32_16x16x32_bf16 a[12:15],v[100:103],v[156:159],a[12:15]
ds_read_b64_tr_b16 v[100:101],v1 offset:10496
v_mfma_f32_16x16x32_bf16 a[12:15],v[96:99],v[140:143],a[12:15]
s_waitcnt lgkmcnt(4)
ds_read_b64_tr_b16 v[96:97],v1 offset:256
v_mfma_f32_16x16x32_bf16 a[16:19],v[112:115],v[32:35],a[16:19]
buffer_load_dwordx4 a[108:111],v6,s[28:31],s21 offen offset:1024 sc1
v_mfma_f32_16x16x32_bf16 a[16:19],v[116:119],v[36:39],a[16:19]
ds_read_b64_tr_b16 v[98:99],v1 offset:5376
ds_read_b64_tr_b16 v[102:103],v1 offset:15616
v_mfma_f32_16x16x32_bf16 a[16:19],v[120:123],v[40:43],a[16:19]
ds_read_b64_tr_b16 v[106:107],v1 offset:25856
v_add_u32 v1,0xffff6000,v1
v_mfma_f32_16x16x32_bf16 a[16:19],v[124:127],v[44:47],a[16:19]
v_mfma_f32_16x16x32_bf16 a[20:23],v[124:127],v[180:183],a[20:23]
v_mfma_f32_16x16x32_bf16 a[20:23],v[120:123],v[164:167],a[20:23]
v_mfma_f32_16x16x32_bf16 a[20:23],v[116:119],v[148:151],a[20:23]
v_mfma_f32_16x16x32_bf16 a[20:23],v[112:115],v[132:135],a[20:23]
v_mfma_f32_16x16x32_bf16 a[24:27],v[112:115],v[196:199],a[24:27]
buffer_load_dwordx4 a[188:191],v28,s[28:31],s90 offen offset:3072 sc1
v_mfma_f32_16x16x32_bf16 a[24:27],v[116:119],v[212:215],a[24:27]
v_mfma_f32_16x16x32_bf16 a[24:27],v[120:123],v[228:231],a[24:27]
v_mfma_f32_16x16x32_bf16 a[24:27],v[124:127],v[244:247],a[24:27]
v_mfma_f32_16x16x32_bf16 a[28:31],v[124:127],v[188:191],a[28:31]
ds_read_b64_tr_b16 v[124:125],v2 offset:31008
ds_read_b64_tr_b16 v[126:127],v2 offset:36128
v_mfma_f32_16x16x32_bf16 a[28:31],v[120:123],v[172:175],a[28:31]
buffer_load_dwordx4 a[100:103],v6,s[28:31],s20 offen offset:1024 sc1
v_mfma_f32_16x16x32_bf16 a[28:31],v[116:119],v[156:159],a[28:31]
ds_read_b64_tr_b16 v[116:117],v2 offset:10528
ds_read_b64_tr_b16 v[118:119],v2 offset:15648
v_mfma_f32_16x16x32_bf16 a[28:31],v[112:115],v[140:143],a[28:31]
ds_read_b64_tr_b16 v[112:113],v2 offset:288
v_mfma_f32_16x16x32_bf16 v[128:131],v[64:67],v[32:35],v[128:131]
ds_read_b64_tr_b16 v[114:115],v2 offset:5408
v_mfma_f32_16x16x32_bf16 v[128:131],v[68:71],v[36:39],v[128:131]
ds_read_b64_tr_b16 v[120:121],v2 offset:20768
v_mfma_f32_16x16x32_bf16 v[128:131],v[72:75],v[40:43],v[128:131]
ds_read_b64_tr_b16 v[122:123],v2 offset:25888
v_add_u32 v2,0xffff6000,v2
v_mfma_f32_16x16x32_bf16 v[128:131],v[76:79],v[44:47],v[128:131]
v_mfma_f32_16x16x32_bf16 v[136:139],v[76:79],v[180:183],v[136:139]
v_mfma_f32_16x16x32_bf16 v[136:139],v[72:75],v[164:167],v[136:139]
buffer_load_dwordx4 a[172:175],v28,s[28:31],s89 offen offset:3072 sc1
v_mfma_f32_16x16x32_bf16 v[136:139],v[68:71],v[148:151],v[136:139]
v_mfma_f32_16x16x32_bf16 v[136:139],v[64:67],v[132:135],v[136:139]
v_mfma_f32_16x16x32_bf16 v[144:147],v[64:67],v[196:199],v[144:147]
v_mfma_f32_16x16x32_bf16 v[144:147],v[68:71],v[212:215],v[144:147]
v_mfma_f32_16x16x32_bf16 v[144:147],v[72:75],v[228:231],v[144:147]
buffer_load_dwordx4 a[64:67],v6,s[28:31],s20 offen offset:2048 sc1
v_mfma_f32_16x16x32_bf16 v[144:147],v[76:79],v[244:247],v[144:147]
v_mfma_f32_16x16x32_bf16 v[152:155],v[76:79],v[188:191],v[152:155]
v_mfma_f32_16x16x32_bf16 v[152:155],v[72:75],v[172:175],v[152:155]
v_mfma_f32_16x16x32_bf16 v[152:155],v[68:71],v[156:159],v[152:155]
v_mfma_f32_16x16x32_bf16 v[152:155],v[64:67],v[140:143],v[152:155]
s_mov_b64 exec,0
s_cmp_eq_u32 s60,0
s_cbranch_scc1 L_pd16_%=
s_cmp_eq_u32 s60,1
s_cbranch_scc1 L_pd17_%=
s_cmp_eq_u32 s60,2
s_cbranch_scc1 L_pd18_%=
s_branch L_trtg2F_%=
L_trtg2F_%=:
s_mov_b64 exec,-1
s_waitcnt lgkmcnt(0)
s_barrier
v_mfma_f32_16x16x32_bf16 v[160:163],v[80:83],v[32:35],v[160:163]
buffer_load_dwordx4 v24,s[48:51],s83 offen lds
v_mfma_f32_16x16x32_bf16 v[160:163],v[84:87],v[36:39],v[160:163]
ds_read_b64_tr_b16 v[64:65],v1
ds_read_b64_tr_b16 v[66:67],v1 offset:5120
v_mfma_f32_16x16x32_bf16 v[160:163],v[88:91],v[40:43],v[160:163]
ds_read_b64_tr_b16 v[68:69],v1 offset:10240
v_mfma_f32_16x16x32_bf16 v[160:163],v[92:95],v[44:47],v[160:163]
ds_read_b64_tr_b16 v[70:71],v1 offset:15360
v_mfma_f32_16x16x32_bf16 v[168:171],v[92:95],v[180:183],v[168:171]
ds_read_b64_tr_b16 v[72:73],v1 offset:20480
v_mfma_f32_16x16x32_bf16 v[168:171],v[88:91],v[164:167],v[168:171]
buffer_load_dwordx4 a[156:159],v28,s[28:31],s88 offen offset:3072 sc1
v_mfma_f32_16x16x32_bf16 v[168:171],v[84:87],v[148:151],v[168:171]
ds_read_b64_tr_b16 v[74:75],v1 offset:25600
ds_read_b64_tr_b16 v[76:77],v1 offset:30720
v_mfma_f32_16x16x32_bf16 v[168:171],v[80:83],v[132:135],v[168:171]
ds_read_b64_tr_b16 v[78:79],v1 offset:35840
v_mfma_f32_16x16x32_bf16 v[176:179],v[80:83],v[196:199],v[176:179]
v_mfma_f32_16x16x32_bf16 v[176:179],v[84:87],v[212:215],v[176:179]
v_mfma_f32_16x16x32_bf16 v[176:179],v[88:91],v[228:231],v[176:179]
buffer_load_dwordx4 a[72:75],v6,s[28:31],s21 offen offset:2048 sc1
v_mfma_f32_16x16x32_bf16 v[176:179],v[92:95],v[244:247],v[176:179]
v_mfma_f32_16x16x32_bf16 v[184:187],v[92:95],v[188:191],v[184:187]
ds_read_b64_tr_b16 v[92:93],v2 offset:30752
ds_read_b64_tr_b16 v[94:95],v2 offset:35872
v_mfma_f32_16x16x32_bf16 v[184:187],v[88:91],v[172:175],v[184:187]
ds_read_b64_tr_b16 v[88:89],v2 offset:20512
v_mfma_f32_16x16x32_bf16 v[184:187],v[84:87],v[156:159],v[184:187]
ds_read_b64_tr_b16 v[84:85],v2 offset:10272
v_mfma_f32_16x16x32_bf16 v[184:187],v[80:83],v[140:143],v[184:187]
buffer_load_dwordx4 a[140:143],v28,s[28:31],s87 offen offset:3072 sc1
v_mfma_f32_16x16x32_bf16 v[192:195],v[96:99],v[32:35],v[192:195]
s_add_u32 s87,s64,3
ds_read_b64_tr_b16 v[80:81],v2 offset:32
ds_read_b64_tr_b16 v[82:83],v2 offset:5152
v_mfma_f32_16x16x32_bf16 v[192:195],v[100:103],v[36:39],v[192:195]
s_and_b32 s87,s87,3
s_lshl_b32 s87,s87,1
ds_read_b64_tr_b16 v[86:87],v2 offset:15392
v_mfma_f32_16x16x32_bf16 v[192:195],v[104:107],v[40:43],v[192:195]
)A"
R"A(s_lshl_b32 s87,s87,16
s_add_u32 s88,s87,4096
ds_read_b64_tr_b16 v[90:91],v2 offset:25632
v_mfma_f32_16x16x32_bf16 v[192:195],v[108:111],v[44:47],v[192:195]
s_add_u32 s89,s87,8192
s_add_u32 s90,s87,12288
v_mfma_f32_16x16x32_bf16 v[200:203],v[108:111],v[180:183],v[200:203]
buffer_load_dwordx4 a[80:83],v6,s[28:31],s22 offen offset:2048 sc1
v_mfma_f32_16x16x32_bf16 v[200:203],v[104:107],v[164:167],v[200:203]
v_mfma_f32_16x16x32_bf16 v[200:203],v[100:103],v[148:151],v[200:203]
v_mfma_f32_16x16x32_bf16 v[200:203],v[96:99],v[132:135],v[200:203]
v_mfma_f32_16x16x32_bf16 v[208:211],v[96:99],v[196:199],v[208:211]
v_mfma_f32_16x16x32_bf16 v[208:211],v[100:103],v[212:215],v[208:211]
v_mfma_f32_16x16x32_bf16 v[208:211],v[104:107],v[228:231],v[208:211]
v_mfma_f32_16x16x32_bf16 v[208:211],v[108:111],v[244:247],v[208:211]
buffer_load_dwordx4 v25,s[48:51],s83 offen offset:1024 lds
v_mfma_f32_16x16x32_bf16 v[216:219],v[108:111],v[188:191],v[216:219]
ds_read_b64_tr_b16 v[108:109],v1 offset:30784
ds_read_b64_tr_b16 v[110:111],v1 offset:35904
v_mfma_f32_16x16x32_bf16 v[216:219],v[104:107],v[172:175],v[216:219]
ds_read_b64_tr_b16 v[104:105],v1 offset:20544
v_mfma_f32_16x16x32_bf16 v[216:219],v[100:103],v[156:159],v[216:219]
ds_read_b64_tr_b16 v[100:101],v1 offset:10304
v_mfma_f32_16x16x32_bf16 v[216:219],v[96:99],v[140:143],v[216:219]
ds_read_b64_tr_b16 v[96:97],v1 offset:64
v_mfma_f32_16x16x32_bf16 v[224:227],v[112:115],v[32:35],v[224:227]
buffer_load_dwordx4 a[88:91],v6,s[28:31],s23 offen offset:2048 sc1
v_mfma_f32_16x16x32_bf16 v[224:227],v[116:119],v[36:39],v[224:227]
ds_read_b64_tr_b16 v[98:99],v1 offset:5184
ds_read_b64_tr_b16 v[102:103],v1 offset:15424
v_mfma_f32_16x16x32_bf16 v[224:227],v[120:123],v[40:43],v[224:227]
ds_read_b64_tr_b16 v[106:107],v1 offset:25664
v_mfma_f32_16x16x32_bf16 v[224:227],v[124:127],v[44:47],v[224:227]
v_mfma_f32_16x16x32_bf16 v[232:235],v[124:127],v[180:183],v[232:235]
v_mfma_f32_16x16x32_bf16 v[232:235],v[120:123],v[164:167],v[232:235]
v_mfma_f32_16x16x32_bf16 v[232:235],v[116:119],v[148:151],v[232:235]
v_mfma_f32_16x16x32_bf16 v[232:235],v[112:115],v[132:135],v[232:235]
buffer_load_dwordx4 v26,s[48:51],s83 offen offset:2048 lds
v_mfma_f32_16x16x32_bf16 v[240:243],v[112:115],v[196:199],v[240:243]
v_mfma_f32_16x16x32_bf16 v[240:243],v[116:119],v[212:215],v[240:243]
v_mfma_f32_16x16x32_bf16 v[240:243],v[120:123],v[228:231],v[240:243]
v_mfma_f32_16x16x32_bf16 v[240:243],v[124:127],v[244:247],v[240:243]
v_mfma_f32_16x16x32_bf16 v[248:251],v[124:127],v[188:191],v[248:251]
ds_read_b64_tr_b16 v[124:125],v2 offset:30816
ds_read_b64_tr_b16 v[126:127],v2 offset:35936
v_mfma_f32_16x16x32_bf16 v[248:251],v[120:123],v[172:175],v[248:251]
buffer_load_dwordx4 a[120:123],v28,s[28:31],s23 offen offset:3072 sc1
v_mfma_f32_16x16x32_bf16 v[248:251],v[116:119],v[156:159],v[248:251]
ds_read_b64_tr_b16 v[116:117],v2 offset:10336
ds_read_b64_tr_b16 v[118:119],v2 offset:15456
v_mfma_f32_16x16x32_bf16 v[248:251],v[112:115],v[140:143],v[248:251]
s_waitcnt vmcnt(39) lgkmcnt(4)
ds_read_b64_tr_b16 v[112:113],v2 offset:96
v_mfma_f32_16x16x32_bf16 a[192:195],v[64:67],a[128:131],a[192:195]
ds_read_b64_tr_b16 v[114:115],v2 offset:5216
v_mfma_f32_16x16x32_bf16 a[192:195],v[68:71],a[144:147],a[192:195]
s_waitcnt vmcnt(37)
ds_read_b64_tr_b16 v[120:121],v2 offset:20576
v_mfma_f32_16x16x32_bf16 a[192:195],v[72:75],a[160:163],a[192:195]
s_waitcnt vmcnt(35)
ds_read_b64_tr_b16 v[122:123],v2 offset:25696
v_mfma_f32_16x16x32_bf16 a[192:195],v[76:79],a[176:179],a[192:195]
s_waitcnt vmcnt(27)
v_mfma_f32_16x16x32_bf16 a[196:199],v[76:79],a[180:183],a[196:199]
buffer_load_dwordx4 v27,s[48:51],s83 offen offset:3072 lds
v_mfma_f32_16x16x32_bf16 a[196:199],v[72:75],a[164:167],a[196:199]
s_add_u32 m0,s86,126976
v_mfma_f32_16x16x32_bf16 a[196:199],v[68:71],a[148:151],a[196:199]
s_waitcnt vmcnt(24)
v_mfma_f32_16x16x32_bf16 a[196:199],v[64:67],a[132:135],a[196:199]
v_mfma_f32_16x16x32_bf16 a[200:203],v[64:67],a[136:139],a[200:203]
s_waitcnt vmcnt(21)
v_mfma_f32_16x16x32_bf16 a[200:203],v[68:71],a[152:155],a[200:203]
s_waitcnt vmcnt(15)
v_mfma_f32_16x16x32_bf16 a[200:203],v[72:75],a[168:171],a[200:203]
buffer_load_dwordx4 a[112:115],v28,s[28:31],s22 offen offset:3072 sc1
v_mfma_f32_16x16x32_bf16 a[200:203],v[76:79],a[184:187],a[200:203]
s_waitcnt vmcnt(14)
v_mfma_f32_16x16x32_bf16 a[204:207],v[76:79],a[188:191],a[204:207]
s_waitcnt vmcnt(12)
ds_read_b64_tr_b16 v[76:77],v1 offset:30848
ds_read_b64_tr_b16 v[78:79],v1 offset:35968
v_mfma_f32_16x16x32_bf16 a[204:207],v[72:75],a[172:175],a[204:207]
s_waitcnt vmcnt(9)
ds_read_b64_tr_b16 v[72:73],v1 offset:20608
v_mfma_f32_16x16x32_bf16 a[204:207],v[68:71],a[156:159],a[204:207]
s_waitcnt vmcnt(7)
ds_read_b64_tr_b16 v[68:69],v1 offset:10368
v_mfma_f32_16x16x32_bf16 a[204:207],v[64:67],a[140:143],a[204:207]
ds_read_b64_tr_b16 v[64:65],v1 offset:128
v_mfma_f32_16x16x32_bf16 a[208:211],v[80:83],a[128:131],a[208:211]
ds_read_b64_tr_b16 v[66:67],v1 offset:5248
ds_read_b64_tr_b16 v[70:71],v1 offset:15488
v_mfma_f32_16x16x32_bf16 a[208:211],v[84:87],a[144:147],a[208:211]
buffer_load_dwordx4 v7,s[48:51],s83 offen lds
v_mfma_f32_16x16x32_bf16 a[208:211],v[88:91],a[160:163],a[208:211]
s_add_u32 m0,s86,128000
s_add_u32 s83,s64,1
ds_read_b64_tr_b16 v[74:75],v1 offset:25728
v_mfma_f32_16x16x32_bf16 a[208:211],v[92:95],a[176:179],a[208:211]
s_and_b32 s83,s83,3
s_lshl_b32 s83,s83,1
s_or_b32 s83,s83,1
v_mfma_f32_16x16x32_bf16 a[212:215],v[92:95],a[180:183],a[212:215]
s_mul_i32 s83,s83,163840
s_add_u32 s83,s83,s85
v_mfma_f32_16x16x32_bf16 a[212:215],v[88:91],a[164:167],a[212:215]
v_mfma_f32_16x16x32_bf16 a[212:215],v[84:87],a[148:151],a[212:215]
v_mfma_f32_16x16x32_bf16 a[212:215],v[80:83],a[132:135],a[212:215]
buffer_load_dwordx4 a[104:107],v28,s[28:31],s21 offen offset:3072 sc1
v_mfma_f32_16x16x32_bf16 a[216:219],v[80:83],a[136:139],a[216:219]
v_mfma_f32_16x16x32_bf16 a[216:219],v[84:87],a[152:155],a[216:219]
v_mfma_f32_16x16x32_bf16 a[216:219],v[88:91],a[168:171],a[216:219]
v_mfma_f32_16x16x32_bf16 a[216:219],v[92:95],a[184:187],a[216:219]
v_mfma_f32_16x16x32_bf16 a[220:223],v[92:95],a[188:191],a[220:223]
ds_read_b64_tr_b16 v[92:93],v2 offset:30880
ds_read_b64_tr_b16 v[94:95],v2 offset:36000
v_mfma_f32_16x16x32_bf16 a[220:223],v[88:91],a[172:175],a[220:223]
ds_read_b64_tr_b16 v[88:89],v2 offset:20640
v_mfma_f32_16x16x32_bf16 a[220:223],v[84:87],a[156:159],a[220:223]
buffer_load_dwordx4 v24,s[48:51],s84 offen lds
v_mfma_f32_16x16x32_bf16 a[220:223],v[80:83],a[140:143],a[220:223]
ds_read_b64_tr_b16 v[80:81],v2 offset:160
ds_read_b64_tr_b16 v[82:83],v2 offset:5280
s_barrier
v_mfma_f32_16x16x32_bf16 a[224:227],v[96:99],a[128:131],a[224:227]
ds_read_b64_tr_b16 v[84:85],v2 offset:10400
v_mfma_f32_16x16x32_bf16 a[224:227],v[100:103],a[144:147],a[224:227]
ds_read_b64_tr_b16 v[86:87],v2 offset:15520
v_mfma_f32_16x16x32_bf16 a[224:227],v[104:107],a[160:163],a[224:227]
buffer_store_dwordx2 v[8:9],v10,s[44:47],s18 offen offset:1280 sc1
v_mfma_f32_16x16x32_bf16 a[224:227],v[108:111],a[176:179],a[224:227]
ds_read_b64_tr_b16 v[90:91],v2 offset:25760
v_mfma_f32_16x16x32_bf16 a[228:231],v[108:111],a[180:183],a[228:231]
v_mfma_f32_16x16x32_bf16 a[228:231],v[104:107],a[164:167],a[228:231]
v_mfma_f32_16x16x32_bf16 a[228:231],v[100:103],a[148:151],a[228:231]
v_mfma_f32_16x16x32_bf16 a[228:231],v[96:99],a[132:135],a[228:231]
buffer_load_dwordx4 a[96:99],v28,s[28:31],s20 offen offset:3072 sc1
v_mfma_f32_16x16x32_bf16 a[232:235],v[96:99],a[136:139],a[232:235]
s_add_u32 s20,s64,1
s_and_b32 s20,s20,3
s_lshl_b32 s20,s20,1
v_mfma_f32_16x16x32_bf16 a[232:235],v[100:103],a[152:155],a[232:235]
s_or_b32 s20,s20,1
s_lshl_b32 s20,s20,16
s_add_u32 s21,s20,4096
v_mfma_f32_16x16x32_bf16 a[232:235],v[104:107],a[168:171],a[232:235]
s_add_u32 s22,s20,8192
s_add_u32 s23,s20,12288
v_mfma_f32_16x16x32_bf16 a[232:235],v[108:111],a[184:187],a[232:235]
v_mfma_f32_16x16x32_bf16 a[236:239],v[108:111],a[188:191],a[236:239]
ds_read_b64_tr_b16 v[108:109],v1 offset:30912
ds_read_b64_tr_b16 v[110:111],v1 offset:36032
v_mfma_f32_16x16x32_bf16 a[236:239],v[104:107],a[172:175],a[236:239]
ds_read_b64_tr_b16 v[104:105],v1 offset:20672
v_mfma_f32_16x16x32_bf16 a[236:239],v[100:103],a[156:159],a[236:239]
buffer_load_dwordx4 v25,s[48:51],s84 offen offset:1024 lds
v_mfma_f32_16x16x32_bf16 a[236:239],v[96:99],a[140:143],a[236:239]
s_waitcnt lgkmcnt(3)
ds_read_b64_tr_b16 v[96:97],v1 offset:192
ds_read_b64_tr_b16 v[98:99],v1 offset:5312
v_mfma_f32_16x16x32_bf16 a[240:243],v[112:115],a[128:131],a[240:243]
ds_read_b64_tr_b16 v[100:101],v1 offset:10432
v_mfma_f32_16x16x32_bf16 a[240:243],v[116:119],a[144:147],a[240:243]
ds_read_b64_tr_b16 v[102:103],v1 offset:15552
v_mfma_f32_16x16x32_bf16 a[240:243],v[120:123],a[160:163],a[240:243]
ds_read_b64_tr_b16 v[106:107],v1 offset:25792
v_mfma_f32_16x16x32_bf16 a[240:243],v[124:127],a[176:179],a[240:243]
v_mfma_f32_16x16x32_bf16 a[244:247],v[124:127],a[180:183],a[244:247]
v_mfma_f32_16x16x32_bf16 a[244:247],v[120:123],a[164:167],a[244:247]
buffer_load_dwordx4 v26,s[48:51],s84 offen offset:2048 lds
v_mfma_f32_16x16x32_bf16 a[244:247],v[116:119],a[148:151],a[244:247]
v_mfma_f32_16x16x32_bf16 a[244:247],v[112:115],a[132:135],a[244:247]
v_mfma_f32_16x16x32_bf16 a[248:251],v[112:115],a[136:139],a[248:251]
v_mfma_f32_16x16x32_bf16 a[248:251],v[116:119],a[152:155],a[248:251]
v_mfma_f32_16x16x32_bf16 a[248:251],v[120:123],a[168:171],a[248:251]
v_mfma_f32_16x16x32_bf16 a[248:251],v[124:127],a[184:187],a[248:251]
v_mfma_f32_16x16x32_bf16 a[252:255],v[124:127],a[188:191],a[252:255]
buffer_load_dwordx4 v27,s[48:51],s84 offen offset:3072 lds
v_mfma_f32_16x16x32_bf16 a[252:255],v[120:123],a[172:175],a[252:255]
s_add_u32 m0,s86,132096
ds_read_b64_tr_b16 v[120:121],v2 offset:20704
ds_read_b64_tr_b16 v[122:123],v2 offset:25824
v_mfma_f32_16x16x32_bf16 a[252:255],v[116:119],a[156:159],a[252:255]
ds_read_b64_tr_b16 v[116:117],v2 offset:10464
v_mfma_f32_16x16x32_bf16 a[252:255],v[112:115],a[140:143],a[252:255]
ds_read_b64_tr_b16 v[112:113],v2 offset:224
v_mfma_f32_16x16x32_bf16 a[0:3],v[64:67],a[128:131],a[0:3]
ds_read_b64_tr_b16 v[114:115],v2 offset:5344
v_mfma_f32_16x16x32_bf16 a[0:3],v[68:71],a[144:147],a[0:3]
ds_read_b64_tr_b16 v[118:119],v2 offset:15584
ds_read_b64_tr_b16 v[124:125],v2 offset:30944
v_mfma_f32_16x16x32_bf16 a[0:3],v[72:75],a[160:163],a[0:3]
ds_read_b64_tr_b16 v[126:127],v2 offset:36064
v_mfma_f32_16x16x32_bf16 a[0:3],v[76:79],a[176:179],a[0:3]
buffer_load_dwordx4 v7,s[48:51],s84 offen lds
v_mfma_f32_16x16x32_bf16 a[4:7],v[76:79],a[180:183],a[4:7]
s_add_u32 m0,s86,81920
s_add_u32 s84,s83,20480
v_mfma_f32_16x16x32_bf16 a[4:7],v[72:75],a[164:167],a[4:7]
v_mfma_f32_16x16x32_bf16 a[4:7],v[68:71],a[148:151],a[4:7]
v_mfma_f32_16x16x32_bf16 a[4:7],v[64:67],a[132:135],a[4:7]
v_mfma_f32_16x16x32_bf16 a[8:11],v[64:67],a[136:139],a[8:11]
v_mfma_f32_16x16x32_bf16 a[8:11],v[68:71],a[152:155],a[8:11]
v_mfma_f32_16x16x32_bf16 a[8:11],v[72:75],a[168:171],a[8:11]
buffer_load_dwordx4 v24,s[48:51],s83 offen lds
v_mfma_f32_16x16x32_bf16 a[8:11],v[76:79],a[184:187],a[8:11]
v_mfma_f32_16x16x32_bf16 a[12:15],v[76:79],a[188:191],a[12:15]
ds_read_b64_tr_b16 v[76:77],v1 offset:30976
ds_read_b64_tr_b16 v[78:79],v1 offset:36096
v_mfma_f32_16x16x32_bf16 a[12:15],v[72:75],a[172:175],a[12:15]
ds_read_b64_tr_b16 v[72:73],v1 offset:20736
v_mfma_f32_16x16x32_bf16 a[12:15],v[68:71],a[156:159],a[12:15]
ds_read_b64_tr_b16 v[68:69],v1 offset:10496
v_mfma_f32_16x16x32_bf16 a[12:15],v[64:67],a[140:143],a[12:15]
ds_read_b64_tr_b16 v[64:65],v1 offset:256
v_mfma_f32_16x16x32_bf16 a[16:19],v[80:83],a[128:131],a[16:19]
ds_read_b64_tr_b16 v[66:67],v1 offset:5376
ds_read_b64_tr_b16 v[70:71],v1 offset:15616
v_mfma_f32_16x16x32_bf16 a[16:19],v[84:87],a[144:147],a[16:19]
buffer_load_dwordx4 v25,s[48:51],s83 offen offset:1024 lds
v_mfma_f32_16x16x32_bf16 a[16:19],v[88:91],a[160:163],a[16:19]
ds_read_b64_tr_b16 v[74:75],v1 offset:25856
v_add_u32 v1,0xffff6000,v1
v_mfma_f32_16x16x32_bf16 a[16:19],v[92:95],a[176:179],a[16:19]
v_mfma_f32_16x16x32_bf16 a[20:23],v[92:95],a[180:183],a[20:23]
)A"
R"A(v_mfma_f32_16x16x32_bf16 a[20:23],v[88:91],a[164:167],a[20:23]
v_mfma_f32_16x16x32_bf16 a[20:23],v[84:87],a[148:151],a[20:23]
v_mfma_f32_16x16x32_bf16 a[20:23],v[80:83],a[132:135],a[20:23]
v_mfma_f32_16x16x32_bf16 a[24:27],v[80:83],a[136:139],a[24:27]
buffer_load_dwordx4 v26,s[48:51],s83 offen offset:2048 lds
v_mfma_f32_16x16x32_bf16 a[24:27],v[84:87],a[152:155],a[24:27]
v_mfma_f32_16x16x32_bf16 a[24:27],v[88:91],a[168:171],a[24:27]
v_mfma_f32_16x16x32_bf16 a[24:27],v[92:95],a[184:187],a[24:27]
v_mfma_f32_16x16x32_bf16 a[28:31],v[92:95],a[188:191],a[28:31]
ds_read_b64_tr_b16 v[92:93],v2 offset:31008
ds_read_b64_tr_b16 v[94:95],v2 offset:36128
v_mfma_f32_16x16x32_bf16 a[28:31],v[88:91],a[172:175],a[28:31]
ds_read_b64_tr_b16 v[88:89],v2 offset:20768
v_mfma_f32_16x16x32_bf16 a[28:31],v[84:87],a[156:159],a[28:31]
s_waitcnt lgkmcnt(3)
ds_read_b64_tr_b16 v[84:85],v2 offset:10528
v_mfma_f32_16x16x32_bf16 a[28:31],v[80:83],a[140:143],a[28:31]
buffer_load_dwordx4 v27,s[48:51],s83 offen offset:3072 lds
v_mfma_f32_16x16x32_bf16 v[128:131],v[96:99],a[128:131],v[128:131]
s_add_u32 m0,s86,86016
ds_read_b64_tr_b16 v[80:81],v2 offset:288
ds_read_b64_tr_b16 v[82:83],v2 offset:5408
v_mfma_f32_16x16x32_bf16 v[128:131],v[100:103],a[144:147],v[128:131]
ds_read_b64_tr_b16 v[86:87],v2 offset:15648
v_mfma_f32_16x16x32_bf16 v[128:131],v[104:107],a[160:163],v[128:131]
ds_read_b64_tr_b16 v[90:91],v2 offset:25888
v_add_u32 v2,0xffff6000,v2
v_mfma_f32_16x16x32_bf16 v[128:131],v[108:111],a[176:179],v[128:131]
v_mfma_f32_16x16x32_bf16 v[136:139],v[108:111],a[180:183],v[136:139]
v_mfma_f32_16x16x32_bf16 v[136:139],v[104:107],a[164:167],v[136:139]
v_mfma_f32_16x16x32_bf16 v[136:139],v[100:103],a[148:151],v[136:139]
buffer_load_dwordx4 v7,s[48:51],s83 offen lds
v_mfma_f32_16x16x32_bf16 v[136:139],v[96:99],a[132:135],v[136:139]
s_add_u32 m0,s86,87040
s_add_u32 s83,s64,2
s_and_b32 s83,s83,3
v_mfma_f32_16x16x32_bf16 v[144:147],v[96:99],a[136:139],v[144:147]
s_lshl_b32 s83,s83,1
s_or_b32 s83,s83,1
s_mul_i32 s83,s83,163840
v_mfma_f32_16x16x32_bf16 v[144:147],v[100:103],a[152:155],v[144:147]
s_add_u32 s83,s83,s85
v_mfma_f32_16x16x32_bf16 v[144:147],v[104:107],a[168:171],v[144:147]
v_mfma_f32_16x16x32_bf16 v[144:147],v[108:111],a[184:187],v[144:147]
v_mfma_f32_16x16x32_bf16 v[152:155],v[108:111],a[188:191],v[152:155]
ds_read_b64_tr_b16 v[108:109],v1 offset:30720
ds_read_b64_tr_b16 v[110:111],v1 offset:35840
v_mfma_f32_16x16x32_bf16 v[152:155],v[104:107],a[172:175],v[152:155]
buffer_load_dwordx4 v24,s[48:51],s84 offen lds
v_mfma_f32_16x16x32_bf16 v[152:155],v[100:103],a[156:159],v[152:155]
ds_read_b64_tr_b16 v[100:101],v1 offset:10240
ds_read_b64_tr_b16 v[102:103],v1 offset:15360
v_mfma_f32_16x16x32_bf16 v[152:155],v[96:99],a[140:143],v[152:155]
ds_read_b64_tr_b16 v[96:97],v1
s_waitcnt lgkmcnt(5)
s_barrier
v_mfma_f32_16x16x32_bf16 v[160:163],v[112:115],a[128:131],v[160:163]
ds_read_b64_tr_b16 v[98:99],v1 offset:5120
v_mfma_f32_16x16x32_bf16 v[160:163],v[116:119],a[144:147],v[160:163]
ds_read_b64_tr_b16 v[104:105],v1 offset:20480
v_mfma_f32_16x16x32_bf16 v[160:163],v[120:123],a[160:163],v[160:163]
ds_read_b64_tr_b16 v[106:107],v1 offset:25600
v_mfma_f32_16x16x32_bf16 v[160:163],v[124:127],a[176:179],v[160:163]
buffer_load_dwordx4 v25,s[48:51],s84 offen offset:1024 lds
v_mfma_f32_16x16x32_bf16 v[168:171],v[124:127],a[180:183],v[168:171]
v_mfma_f32_16x16x32_bf16 v[168:171],v[120:123],a[164:167],v[168:171]
v_mfma_f32_16x16x32_bf16 v[168:171],v[116:119],a[148:151],v[168:171]
v_mfma_f32_16x16x32_bf16 v[168:171],v[112:115],a[132:135],v[168:171]
v_mfma_f32_16x16x32_bf16 v[176:179],v[112:115],a[136:139],v[176:179]
v_mfma_f32_16x16x32_bf16 v[176:179],v[116:119],a[152:155],v[176:179]
v_mfma_f32_16x16x32_bf16 v[176:179],v[120:123],a[168:171],v[176:179]
buffer_load_dwordx4 v26,s[48:51],s84 offen offset:2048 lds
v_mfma_f32_16x16x32_bf16 v[176:179],v[124:127],a[184:187],v[176:179]
v_mfma_f32_16x16x32_bf16 v[184:187],v[124:127],a[188:191],v[184:187]
ds_read_b64_tr_b16 v[124:125],v2 offset:30752
ds_read_b64_tr_b16 v[126:127],v2 offset:35872
v_mfma_f32_16x16x32_bf16 v[184:187],v[120:123],a[172:175],v[184:187]
ds_read_b64_tr_b16 v[120:121],v2 offset:20512
v_mfma_f32_16x16x32_bf16 v[184:187],v[116:119],a[156:159],v[184:187]
ds_read_b64_tr_b16 v[116:117],v2 offset:10272
v_mfma_f32_16x16x32_bf16 v[184:187],v[112:115],a[140:143],v[184:187]
ds_read_b64_tr_b16 v[112:113],v2 offset:32
v_mfma_f32_16x16x32_bf16 v[192:195],v[64:67],a[128:131],v[192:195]
ds_read_b64_tr_b16 v[114:115],v2 offset:5152
ds_read_b64_tr_b16 v[118:119],v2 offset:15392
v_mfma_f32_16x16x32_bf16 v[192:195],v[68:71],a[144:147],v[192:195]
buffer_load_dwordx4 v27,s[48:51],s84 offen offset:3072 lds
v_mfma_f32_16x16x32_bf16 v[192:195],v[72:75],a[160:163],v[192:195]
s_add_u32 m0,s86,91136
ds_read_b64_tr_b16 v[122:123],v2 offset:25632
v_mfma_f32_16x16x32_bf16 v[192:195],v[76:79],a[176:179],v[192:195]
v_mfma_f32_16x16x32_bf16 v[200:203],v[76:79],a[180:183],v[200:203]
v_mfma_f32_16x16x32_bf16 v[200:203],v[72:75],a[164:167],v[200:203]
v_mfma_f32_16x16x32_bf16 v[200:203],v[68:71],a[148:151],v[200:203]
v_mfma_f32_16x16x32_bf16 v[200:203],v[64:67],a[132:135],v[200:203]
v_mfma_f32_16x16x32_bf16 v[208:211],v[64:67],a[136:139],v[208:211]
buffer_load_dwordx4 v7,s[48:51],s84 offen lds
v_mfma_f32_16x16x32_bf16 v[208:211],v[68:71],a[152:155],v[208:211]
s_add_u32 m0,s86,40960
s_add_u32 s84,s83,20480
v_mfma_f32_16x16x32_bf16 v[208:211],v[72:75],a[168:171],v[208:211]
v_mfma_f32_16x16x32_bf16 v[208:211],v[76:79],a[184:187],v[208:211]
v_mfma_f32_16x16x32_bf16 v[216:219],v[76:79],a[188:191],v[216:219]
ds_read_b64_tr_b16 v[76:77],v1 offset:30784
ds_read_b64_tr_b16 v[78:79],v1 offset:35904
v_mfma_f32_16x16x32_bf16 v[216:219],v[72:75],a[172:175],v[216:219]
buffer_load_dwordx2 v[4:5],v3,s[44:47],s91 offen offset:1280 sc1
v_mfma_f32_16x16x32_bf16 v[216:219],v[68:71],a[156:159],v[216:219]
ds_read_b64_tr_b16 v[68:69],v1 offset:10304
ds_read_b64_tr_b16 v[70:71],v1 offset:15424
v_mfma_f32_16x16x32_bf16 v[216:219],v[64:67],a[140:143],v[216:219]
ds_read_b64_tr_b16 v[64:65],v1 offset:64
v_mfma_f32_16x16x32_bf16 v[224:227],v[80:83],a[128:131],v[224:227]
ds_read_b64_tr_b16 v[66:67],v1 offset:5184
v_mfma_f32_16x16x32_bf16 v[224:227],v[84:87],a[144:147],v[224:227]
ds_read_b64_tr_b16 v[72:73],v1 offset:20544
v_mfma_f32_16x16x32_bf16 v[224:227],v[88:91],a[160:163],v[224:227]
buffer_load_dwordx4 v[32:35],v6,s[28:31],s87 offen sc1
v_mfma_f32_16x16x32_bf16 v[224:227],v[92:95],a[176:179],v[224:227]
ds_read_b64_tr_b16 v[74:75],v1 offset:25664
v_mfma_f32_16x16x32_bf16 v[232:235],v[92:95],a[180:183],v[232:235]
v_mfma_f32_16x16x32_bf16 v[232:235],v[88:91],a[164:167],v[232:235]
v_mfma_f32_16x16x32_bf16 v[232:235],v[84:87],a[148:151],v[232:235]
v_mfma_f32_16x16x32_bf16 v[232:235],v[80:83],a[132:135],v[232:235]
buffer_load_dwordx4 v[36:39],v6,s[28:31],s88 offen sc1
v_mfma_f32_16x16x32_bf16 v[240:243],v[80:83],a[136:139],v[240:243]
v_mfma_f32_16x16x32_bf16 v[240:243],v[84:87],a[152:155],v[240:243]
v_mfma_f32_16x16x32_bf16 v[240:243],v[88:91],a[168:171],v[240:243]
v_mfma_f32_16x16x32_bf16 v[240:243],v[92:95],a[184:187],v[240:243]
v_mfma_f32_16x16x32_bf16 v[248:251],v[92:95],a[188:191],v[248:251]
buffer_load_dwordx4 v[40:43],v6,s[28:31],s89 offen sc1
v_mfma_f32_16x16x32_bf16 v[248:251],v[88:91],a[172:175],v[248:251]
ds_read_b64_tr_b16 v[88:89],v2 offset:20576
ds_read_b64_tr_b16 v[90:91],v2 offset:25696
v_mfma_f32_16x16x32_bf16 v[248:251],v[84:87],a[156:159],v[248:251]
ds_read_b64_tr_b16 v[84:85],v2 offset:10336
v_mfma_f32_16x16x32_bf16 v[248:251],v[80:83],a[140:143],v[248:251]
s_waitcnt lgkmcnt(3)
ds_read_b64_tr_b16 v[80:81],v2 offset:96
v_mfma_f32_16x16x32_bf16 a[192:195],v[96:99],a[68:71],a[192:195]
ds_read_b64_tr_b16 v[82:83],v2 offset:5216
v_mfma_f32_16x16x32_bf16 a[192:195],v[100:103],a[76:79],a[192:195]
s_nop 0
L_pd15_%=:
s_cbranch_execz L_trt1_%=
buffer_load_dwordx4 v[44:47],v6,s[28:31],s90 offen sc1
v_mfma_f32_16x16x32_bf16 a[192:195],v[104:107],a[84:87],a[192:195]
ds_read_b64_tr_b16 v[86:87],v2 offset:15456
ds_read_b64_tr_b16 v[92:93],v2 offset:30816
v_mfma_f32_16x16x32_bf16 a[192:195],v[108:111],a[92:95],a[192:195]
ds_read_b64_tr_b16 v[94:95],v2 offset:35936
v_mfma_f32_16x16x32_bf16 a[196:199],v[108:111],a[124:127],a[196:199]
v_mfma_f32_16x16x32_bf16 a[196:199],v[104:107],a[116:119],a[196:199]
v_mfma_f32_16x16x32_bf16 a[196:199],v[100:103],a[108:111],a[196:199]
buffer_load_dwordx4 v[180:183],v6,s[28:31],s90 offen offset:1024 sc1
v_mfma_f32_16x16x32_bf16 a[196:199],v[96:99],a[100:103],a[196:199]
v_mfma_f32_16x16x32_bf16 a[200:203],v[96:99],a[64:67],a[200:203]
v_mfma_f32_16x16x32_bf16 a[200:203],v[100:103],a[72:75],a[200:203]
s_waitcnt vmcnt(31)
v_mfma_f32_16x16x32_bf16 a[200:203],v[104:107],a[80:83],a[200:203]
s_waitcnt vmcnt(27)
v_mfma_f32_16x16x32_bf16 a[200:203],v[108:111],a[88:91],a[200:203]
buffer_load_dwordx4 v[212:215],v6,s[28:31],s88 offen offset:2048 sc1
v_mfma_f32_16x16x32_bf16 a[204:207],v[108:111],a[120:123],a[204:207]
s_waitcnt vmcnt(26)
ds_read_b64_tr_b16 v[108:109],v1 offset:30848
ds_read_b64_tr_b16 v[110:111],v1 offset:35968
v_mfma_f32_16x16x32_bf16 a[204:207],v[104:107],a[112:115],a[204:207]
s_waitcnt vmcnt(24)
ds_read_b64_tr_b16 v[104:105],v1 offset:20608
v_mfma_f32_16x16x32_bf16 a[204:207],v[100:103],a[104:107],a[204:207]
s_waitcnt vmcnt(21)
ds_read_b64_tr_b16 v[100:101],v1 offset:10368
v_mfma_f32_16x16x32_bf16 a[204:207],v[96:99],a[96:99],a[204:207]
ds_read_b64_tr_b16 v[96:97],v1 offset:128
v_mfma_f32_16x16x32_bf16 a[208:211],v[112:115],a[68:71],a[208:211]
buffer_load_dwordx4 v[164:167],v6,s[28:31],s89 offen offset:1024 sc1
v_mfma_f32_16x16x32_bf16 a[208:211],v[116:119],a[76:79],a[208:211]
ds_read_b64_tr_b16 v[98:99],v1 offset:5248
ds_read_b64_tr_b16 v[102:103],v1 offset:15488
v_mfma_f32_16x16x32_bf16 a[208:211],v[120:123],a[84:87],a[208:211]
ds_read_b64_tr_b16 v[106:107],v1 offset:25728
v_mfma_f32_16x16x32_bf16 a[208:211],v[124:127],a[92:95],a[208:211]
v_mfma_f32_16x16x32_bf16 a[212:215],v[124:127],a[124:127],a[212:215]
v_mfma_f32_16x16x32_bf16 a[212:215],v[120:123],a[116:119],a[212:215]
buffer_load_dwordx4 v[228:231],v6,s[28:31],s89 offen offset:2048 sc1
v_mfma_f32_16x16x32_bf16 a[212:215],v[116:119],a[108:111],a[212:215]
v_mfma_f32_16x16x32_bf16 a[212:215],v[112:115],a[100:103],a[212:215]
v_mfma_f32_16x16x32_bf16 a[216:219],v[112:115],a[64:67],a[216:219]
v_mfma_f32_16x16x32_bf16 a[216:219],v[116:119],a[72:75],a[216:219]
v_mfma_f32_16x16x32_bf16 a[216:219],v[120:123],a[80:83],a[216:219]
buffer_load_dwordx4 v[148:151],v6,s[28:31],s88 offen offset:1024 sc1
v_mfma_f32_16x16x32_bf16 a[216:219],v[124:127],a[88:91],a[216:219]
v_mfma_f32_16x16x32_bf16 a[220:223],v[124:127],a[120:123],a[220:223]
ds_read_b64_tr_b16 v[124:125],v2 offset:30880
ds_read_b64_tr_b16 v[126:127],v2 offset:36000
v_mfma_f32_16x16x32_bf16 a[220:223],v[120:123],a[112:115],a[220:223]
ds_read_b64_tr_b16 v[120:121],v2 offset:20640
v_mfma_f32_16x16x32_bf16 a[220:223],v[116:119],a[104:107],a[220:223]
ds_read_b64_tr_b16 v[116:117],v2 offset:10400
v_mfma_f32_16x16x32_bf16 a[220:223],v[112:115],a[96:99],a[220:223]
buffer_load_dwordx4 v[132:135],v6,s[28:31],s87 offen offset:1024 sc1
v_mfma_f32_16x16x32_bf16 a[224:227],v[64:67],a[68:71],a[224:227]
ds_read_b64_tr_b16 v[112:113],v2 offset:160
ds_read_b64_tr_b16 v[114:115],v2 offset:5280
v_mfma_f32_16x16x32_bf16 a[224:227],v[68:71],a[76:79],a[224:227]
ds_read_b64_tr_b16 v[118:119],v2 offset:15520
v_mfma_f32_16x16x32_bf16 a[224:227],v[72:75],a[84:87],a[224:227]
ds_read_b64_tr_b16 v[122:123],v2 offset:25760
v_mfma_f32_16x16x32_bf16 a[224:227],v[76:79],a[92:95],a[224:227]
v_mfma_f32_16x16x32_bf16 a[228:231],v[76:79],a[124:127],a[228:231]
buffer_load_dwordx4 v[244:247],v6,s[28:31],s90 offen offset:2048 sc1
v_mfma_f32_16x16x32_bf16 a[228:231],v[72:75],a[116:119],a[228:231]
v_mfma_f32_16x16x32_bf16 a[228:231],v[68:71],a[108:111],a[228:231]
v_mfma_f32_16x16x32_bf16 a[228:231],v[64:67],a[100:103],a[228:231]
v_mfma_f32_16x16x32_bf16 a[232:235],v[64:67],a[64:67],a[232:235]
v_mfma_f32_16x16x32_bf16 a[232:235],v[68:71],a[72:75],a[232:235]
)A"
R"A(buffer_load_dwordx4 v[188:191],v28,s[28:31],s90 offen offset:3072 sc1
v_mfma_f32_16x16x32_bf16 a[232:235],v[72:75],a[80:83],a[232:235]
s_waitcnt vmcnt(12)
v_cmp_ne_u32 s[56:57],s52,v4
v_cmp_ne_u32 s[58:59],s53,v5
v_mfma_f32_16x16x32_bf16 a[232:235],v[76:79],a[88:91],a[232:235]
s_or_b64 s[56:57],s[56:57],s[58:59]
s_cbranch_scc1 L_fr2_%=
L_fo2_%=:
v_mov_b32 v3,0
v_mfma_f32_16x16x32_bf16 a[236:239],v[76:79],a[120:123],a[236:239]
s_add_u32 s91,s64,3
ds_read_b64_tr_b16 v[76:77],v1 offset:30912
ds_read_b64_tr_b16 v[78:79],v1 offset:36032
v_mfma_f32_16x16x32_bf16 a[236:239],v[72:75],a[112:115],a[236:239]
s_and_b32 s91,s91,3
s_add_u32 s91,s91,s69
ds_read_b64_tr_b16 v[72:73],v1 offset:20672
v_mfma_f32_16x16x32_bf16 a[236:239],v[68:71],a[104:107],a[236:239]
buffer_load_dwordx4 a[128:131],v6,s[28:31],s20 offen sc1
v_mfma_f32_16x16x32_bf16 a[236:239],v[64:67],a[96:99],a[236:239]
s_add_u32 s91,s91,8
ds_read_b64_tr_b16 v[64:65],v1 offset:192
ds_read_b64_tr_b16 v[66:67],v1 offset:5312
s_waitcnt lgkmcnt(5)
v_mfma_f32_16x16x32_bf16 a[240:243],v[80:83],a[68:71],a[240:243]
s_lshl_b32 s91,s91,6
s_and_b32 s69,s63,s60
ds_read_b64_tr_b16 v[68:69],v1 offset:10432
v_mfma_f32_16x16x32_bf16 a[240:243],v[84:87],a[76:79],a[240:243]
s_cmp_eq_u32 s69,3
s_cselect_b32 s69,0x40000000,0
ds_read_b64_tr_b16 v[70:71],v1 offset:15552
v_mfma_f32_16x16x32_bf16 a[240:243],v[88:91],a[84:87],a[240:243]
s_add_u32 s68,s68,s69
ds_read_b64_tr_b16 v[74:75],v1 offset:25792
v_mfma_f32_16x16x32_bf16 a[240:243],v[92:95],a[92:95],a[240:243]
buffer_load_dwordx4 v[196:199],v6,s[28:31],s87 offen offset:2048 sc1
v_mfma_f32_16x16x32_bf16 a[244:247],v[92:95],a[124:127],a[244:247]
v_mfma_f32_16x16x32_bf16 a[244:247],v[88:91],a[116:119],a[244:247]
v_mfma_f32_16x16x32_bf16 a[244:247],v[84:87],a[108:111],a[244:247]
v_mfma_f32_16x16x32_bf16 a[244:247],v[80:83],a[100:103],a[244:247]
v_mfma_f32_16x16x32_bf16 a[248:251],v[80:83],a[64:67],a[248:251]
buffer_load_dwordx4 v[172:175],v28,s[28:31],s89 offen offset:3072 sc1
v_mfma_f32_16x16x32_bf16 a[248:251],v[84:87],a[72:75],a[248:251]
v_mfma_f32_16x16x32_bf16 a[248:251],v[88:91],a[80:83],a[248:251]
v_mfma_f32_16x16x32_bf16 a[248:251],v[92:95],a[88:91],a[248:251]
v_mfma_f32_16x16x32_bf16 a[252:255],v[92:95],a[120:123],a[252:255]
ds_read_b64_tr_b16 v[92:93],v2 offset:30944
ds_read_b64_tr_b16 v[94:95],v2 offset:36064
v_mfma_f32_16x16x32_bf16 a[252:255],v[88:91],a[112:115],a[252:255]
buffer_load_dwordx4 a[144:147],v6,s[28:31],s21 offen sc1
v_mfma_f32_16x16x32_bf16 a[252:255],v[84:87],a[104:107],a[252:255]
ds_read_b64_tr_b16 v[84:85],v2 offset:10464
ds_read_b64_tr_b16 v[86:87],v2 offset:15584
v_mfma_f32_16x16x32_bf16 a[252:255],v[80:83],a[96:99],a[252:255]
ds_read_b64_tr_b16 v[80:81],v2 offset:224
v_mfma_f32_16x16x32_bf16 a[0:3],v[96:99],a[68:71],a[0:3]
ds_read_b64_tr_b16 v[82:83],v2 offset:5344
v_mfma_f32_16x16x32_bf16 a[0:3],v[100:103],a[76:79],a[0:3]
ds_read_b64_tr_b16 v[88:89],v2 offset:20704
v_mfma_f32_16x16x32_bf16 a[0:3],v[104:107],a[84:87],a[0:3]
buffer_load_dwordx4 v[156:159],v28,s[28:31],s88 offen offset:3072 sc1
v_mfma_f32_16x16x32_bf16 a[0:3],v[108:111],a[92:95],a[0:3]
ds_read_b64_tr_b16 v[90:91],v2 offset:25824
v_mfma_f32_16x16x32_bf16 a[4:7],v[108:111],a[124:127],a[4:7]
v_mfma_f32_16x16x32_bf16 a[4:7],v[104:107],a[116:119],a[4:7]
v_mfma_f32_16x16x32_bf16 a[4:7],v[100:103],a[108:111],a[4:7]
v_mfma_f32_16x16x32_bf16 a[4:7],v[96:99],a[100:103],a[4:7]
buffer_load_dwordx4 a[160:163],v6,s[28:31],s22 offen sc1
v_mfma_f32_16x16x32_bf16 a[8:11],v[96:99],a[64:67],a[8:11]
v_mfma_f32_16x16x32_bf16 a[8:11],v[100:103],a[72:75],a[8:11]
v_mfma_f32_16x16x32_bf16 a[8:11],v[104:107],a[80:83],a[8:11]
v_mfma_f32_16x16x32_bf16 a[8:11],v[108:111],a[88:91],a[8:11]
v_mfma_f32_16x16x32_bf16 a[12:15],v[108:111],a[120:123],a[12:15]
buffer_load_dwordx4 v[140:143],v28,s[28:31],s87 offen offset:3072 sc1
v_mfma_f32_16x16x32_bf16 a[12:15],v[104:107],a[112:115],a[12:15]
s_add_u32 s87,s64,2
ds_read_b64_tr_b16 v[104:105],v1 offset:20736
ds_read_b64_tr_b16 v[106:107],v1 offset:25856
v_mfma_f32_16x16x32_bf16 a[12:15],v[100:103],a[104:107],a[12:15]
s_and_b32 s87,s87,3
s_lshl_b32 s87,s87,1
ds_read_b64_tr_b16 v[100:101],v1 offset:10496
v_mfma_f32_16x16x32_bf16 a[12:15],v[96:99],a[96:99],a[12:15]
s_or_b32 s87,s87,1
s_lshl_b32 s87,s87,16
ds_read_b64_tr_b16 v[96:97],v1 offset:256
v_mfma_f32_16x16x32_bf16 a[16:19],v[112:115],a[68:71],a[16:19]
s_add_u32 s88,s87,4096
s_add_u32 s89,s87,8192
ds_read_b64_tr_b16 v[98:99],v1 offset:5376
v_mfma_f32_16x16x32_bf16 a[16:19],v[116:119],a[76:79],a[16:19]
s_add_u32 s90,s87,12288
ds_read_b64_tr_b16 v[102:103],v1 offset:15616
ds_read_b64_tr_b16 v[108:109],v1 offset:30976
v_mfma_f32_16x16x32_bf16 a[16:19],v[120:123],a[84:87],a[16:19]
ds_read_b64_tr_b16 v[110:111],v1 offset:36096
v_add_u32 v1,0x1e000,v1
v_mfma_f32_16x16x32_bf16 a[16:19],v[124:127],a[92:95],a[16:19]
buffer_load_dwordx4 v24,s[48:51],s83 offen lds
v_mfma_f32_16x16x32_bf16 a[20:23],v[124:127],a[124:127],a[20:23]
v_mfma_f32_16x16x32_bf16 a[20:23],v[120:123],a[116:119],a[20:23]
v_mfma_f32_16x16x32_bf16 a[20:23],v[116:119],a[108:111],a[20:23]
v_mfma_f32_16x16x32_bf16 a[20:23],v[112:115],a[100:103],a[20:23]
v_mfma_f32_16x16x32_bf16 a[24:27],v[112:115],a[64:67],a[24:27]
buffer_load_dwordx4 a[176:179],v6,s[28:31],s23 offen sc1
v_mfma_f32_16x16x32_bf16 a[24:27],v[116:119],a[72:75],a[24:27]
v_mfma_f32_16x16x32_bf16 a[24:27],v[120:123],a[80:83],a[24:27]
v_mfma_f32_16x16x32_bf16 a[24:27],v[124:127],a[88:91],a[24:27]
v_mfma_f32_16x16x32_bf16 a[28:31],v[124:127],a[120:123],a[28:31]
ds_read_b64_tr_b16 v[124:125],v2 offset:31008
ds_read_b64_tr_b16 v[126:127],v2 offset:36128
v_mfma_f32_16x16x32_bf16 a[28:31],v[120:123],a[112:115],a[28:31]
ds_read_b64_tr_b16 v[120:121],v2 offset:20768
v_mfma_f32_16x16x32_bf16 a[28:31],v[116:119],a[104:107],a[28:31]
s_waitcnt lgkmcnt(3)
ds_read_b64_tr_b16 v[116:117],v2 offset:10528
v_mfma_f32_16x16x32_bf16 a[28:31],v[112:115],a[96:99],a[28:31]
buffer_load_dwordx4 v25,s[48:51],s83 offen offset:1024 lds
v_mfma_f32_16x16x32_bf16 v[128:131],v[64:67],a[68:71],v[128:131]
ds_read_b64_tr_b16 v[112:113],v2 offset:288
ds_read_b64_tr_b16 v[114:115],v2 offset:5408
v_mfma_f32_16x16x32_bf16 v[128:131],v[68:71],a[76:79],v[128:131]
ds_read_b64_tr_b16 v[118:119],v2 offset:15648
v_mfma_f32_16x16x32_bf16 v[128:131],v[72:75],a[84:87],v[128:131]
ds_read_b64_tr_b16 v[122:123],v2 offset:25888
v_add_u32 v2,0x1e000,v2
v_mfma_f32_16x16x32_bf16 v[128:131],v[76:79],a[92:95],v[128:131]
v_mfma_f32_16x16x32_bf16 v[136:139],v[76:79],a[124:127],v[136:139]
buffer_load_dwordx4 a[180:183],v6,s[28:31],s23 offen offset:1024 sc1
v_mfma_f32_16x16x32_bf16 v[136:139],v[72:75],a[116:119],v[136:139]
v_mfma_f32_16x16x32_bf16 v[136:139],v[68:71],a[108:111],v[136:139]
v_mfma_f32_16x16x32_bf16 v[136:139],v[64:67],a[100:103],v[136:139]
v_mfma_f32_16x16x32_bf16 v[144:147],v[64:67],a[64:67],v[144:147]
v_mfma_f32_16x16x32_bf16 v[144:147],v[68:71],a[72:75],v[144:147]
v_mfma_f32_16x16x32_bf16 v[144:147],v[72:75],a[80:83],v[144:147]
v_mfma_f32_16x16x32_bf16 v[144:147],v[76:79],a[88:91],v[144:147]
buffer_load_dwordx4 v26,s[48:51],s83 offen offset:2048 lds
v_mfma_f32_16x16x32_bf16 v[152:155],v[76:79],a[120:123],v[152:155]
v_mfma_f32_16x16x32_bf16 v[152:155],v[72:75],a[112:115],v[152:155]
v_mfma_f32_16x16x32_bf16 v[152:155],v[68:71],a[104:107],v[152:155]
v_mfma_f32_16x16x32_bf16 v[152:155],v[64:67],a[96:99],v[152:155]
s_waitcnt lgkmcnt(0)
s_barrier
v_mfma_f32_16x16x32_bf16 v[160:163],v[80:83],a[68:71],v[160:163]
buffer_load_dwordx4 a[164:167],v6,s[28:31],s22 offen offset:1024 sc1
v_mfma_f32_16x16x32_bf16 v[160:163],v[84:87],a[76:79],v[160:163]
ds_read_b64_tr_b16 v[64:65],v1
ds_read_b64_tr_b16 v[66:67],v1 offset:5120
v_mfma_f32_16x16x32_bf16 v[160:163],v[88:91],a[84:87],v[160:163]
ds_read_b64_tr_b16 v[68:69],v1 offset:10240
v_mfma_f32_16x16x32_bf16 v[160:163],v[92:95],a[92:95],v[160:163]
ds_read_b64_tr_b16 v[70:71],v1 offset:15360
v_mfma_f32_16x16x32_bf16 v[168:171],v[92:95],a[124:127],v[168:171]
ds_read_b64_tr_b16 v[72:73],v1 offset:20480
v_mfma_f32_16x16x32_bf16 v[168:171],v[88:91],a[116:119],v[168:171]
ds_read_b64_tr_b16 v[74:75],v1 offset:25600
ds_read_b64_tr_b16 v[76:77],v1 offset:30720
v_mfma_f32_16x16x32_bf16 v[168:171],v[84:87],a[108:111],v[168:171]
ds_read_b64_tr_b16 v[78:79],v1 offset:35840
v_mfma_f32_16x16x32_bf16 v[168:171],v[80:83],a[100:103],v[168:171]
buffer_load_dwordx4 v27,s[48:51],s83 offen offset:3072 lds
v_mfma_f32_16x16x32_bf16 v[176:179],v[80:83],a[64:67],v[176:179]
s_add_u32 m0,s86,45056
v_mfma_f32_16x16x32_bf16 v[176:179],v[84:87],a[72:75],v[176:179]
v_mfma_f32_16x16x32_bf16 v[176:179],v[88:91],a[80:83],v[176:179]
v_mfma_f32_16x16x32_bf16 v[176:179],v[92:95],a[88:91],v[176:179]
v_mfma_f32_16x16x32_bf16 v[184:187],v[92:95],a[120:123],v[184:187]
buffer_load_dwordx4 a[148:151],v6,s[28:31],s21 offen offset:1024 sc1
v_mfma_f32_16x16x32_bf16 v[184:187],v[88:91],a[112:115],v[184:187]
ds_read_b64_tr_b16 v[88:89],v2 offset:20512
ds_read_b64_tr_b16 v[90:91],v2 offset:25632
v_mfma_f32_16x16x32_bf16 v[184:187],v[84:87],a[104:107],v[184:187]
ds_read_b64_tr_b16 v[84:85],v2 offset:10272
v_mfma_f32_16x16x32_bf16 v[184:187],v[80:83],a[96:99],v[184:187]
ds_read_b64_tr_b16 v[80:81],v2 offset:32
v_mfma_f32_16x16x32_bf16 v[192:195],v[96:99],a[68:71],v[192:195]
ds_read_b64_tr_b16 v[82:83],v2 offset:5152
v_mfma_f32_16x16x32_bf16 v[192:195],v[100:103],a[76:79],v[192:195]
ds_read_b64_tr_b16 v[86:87],v2 offset:15392
ds_read_b64_tr_b16 v[92:93],v2 offset:30752
v_mfma_f32_16x16x32_bf16 v[192:195],v[104:107],a[84:87],v[192:195]
ds_read_b64_tr_b16 v[94:95],v2 offset:35872
v_mfma_f32_16x16x32_bf16 v[192:195],v[108:111],a[92:95],v[192:195]
buffer_load_dwordx4 v7,s[48:51],s83 offen lds
v_mfma_f32_16x16x32_bf16 v[200:203],v[108:111],a[124:127],v[200:203]
s_add_u32 m0,s86,46080
s_add_u32 s83,s64,3
s_and_b32 s83,s83,3
v_mfma_f32_16x16x32_bf16 v[200:203],v[104:107],a[116:119],v[200:203]
s_lshl_b32 s83,s83,1
s_or_b32 s83,s83,17
s_mul_i32 s83,s83,163840
v_mfma_f32_16x16x32_bf16 v[200:203],v[100:103],a[108:111],v[200:203]
s_add_u32 s83,s83,s85
v_mfma_f32_16x16x32_bf16 v[200:203],v[96:99],a[100:103],v[200:203]
v_mfma_f32_16x16x32_bf16 v[208:211],v[96:99],a[64:67],v[208:211]
buffer_load_dwordx4 a[132:135],v6,s[28:31],s20 offen offset:1024 sc1
v_mfma_f32_16x16x32_bf16 v[208:211],v[100:103],a[72:75],v[208:211]
v_mfma_f32_16x16x32_bf16 v[208:211],v[104:107],a[80:83],v[208:211]
v_mfma_f32_16x16x32_bf16 v[208:211],v[108:111],a[88:91],v[208:211]
v_mfma_f32_16x16x32_bf16 v[216:219],v[108:111],a[120:123],v[216:219]
ds_read_b64_tr_b16 v[108:109],v1 offset:30784
ds_read_b64_tr_b16 v[110:111],v1 offset:35904
v_mfma_f32_16x16x32_bf16 v[216:219],v[104:107],a[112:115],v[216:219]
ds_read_b64_tr_b16 v[104:105],v1 offset:20544
v_mfma_f32_16x16x32_bf16 v[216:219],v[100:103],a[104:107],v[216:219]
ds_read_b64_tr_b16 v[100:101],v1 offset:10304
v_mfma_f32_16x16x32_bf16 v[216:219],v[96:99],a[96:99],v[216:219]
buffer_load_dwordx4 v24,s[48:51],s84 offen lds
v_mfma_f32_16x16x32_bf16 v[224:227],v[112:115],a[68:71],v[224:227]
ds_read_b64_tr_b16 v[96:97],v1 offset:64
ds_read_b64_tr_b16 v[98:99],v1 offset:5184
v_mfma_f32_16x16x32_bf16 v[224:227],v[116:119],a[76:79],v[224:227]
ds_read_b64_tr_b16 v[102:103],v1 offset:15424
v_mfma_f32_16x16x32_bf16 v[224:227],v[120:123],a[84:87],v[224:227]
ds_read_b64_tr_b16 v[106:107],v1 offset:25664
v_mfma_f32_16x16x32_bf16 v[224:227],v[124:127],a[92:95],v[224:227]
v_mfma_f32_16x16x32_bf16 v[232:235],v[124:127],a[124:127],v[232:235]
buffer_load_dwordx4 a[136:139],v6,s[28:31],s20 offen offset:2048 sc1
v_mfma_f32_16x16x32_bf16 v[232:235],v[120:123],a[116:119],v[232:235]
v_mfma_f32_16x16x32_bf16 v[232:235],v[116:119],a[108:111],v[232:235]
v_mfma_f32_16x16x32_bf16 v[232:235],v[112:115],a[100:103],v[232:235]
v_mfma_f32_16x16x32_bf16 v[240:243],v[112:115],a[64:67],v[240:243]
v_mfma_f32_16x16x32_bf16 v[240:243],v[116:119],a[72:75],v[240:243]
v_mfma_f32_16x16x32_bf16 v[240:243],v[120:123],a[80:83],v[240:243]
v_mfma_f32_16x16x32_bf16 v[240:243],v[124:127],a[88:91],v[240:243]
buffer_load_dwordx4 v25,s[48:51],s84 offen offset:1024 lds
)A"
R"A(v_mfma_f32_16x16x32_bf16 v[248:251],v[124:127],a[120:123],v[248:251]
ds_read_b64_tr_b16 v[124:125],v2 offset:30816
ds_read_b64_tr_b16 v[126:127],v2 offset:35936
v_mfma_f32_16x16x32_bf16 v[248:251],v[120:123],a[112:115],v[248:251]
ds_read_b64_tr_b16 v[120:121],v2 offset:20576
v_mfma_f32_16x16x32_bf16 v[248:251],v[116:119],a[104:107],v[248:251]
ds_read_b64_tr_b16 v[116:117],v2 offset:10336
v_mfma_f32_16x16x32_bf16 v[248:251],v[112:115],a[96:99],v[248:251]
s_waitcnt vmcnt(19) lgkmcnt(4)
ds_read_b64_tr_b16 v[112:113],v2 offset:96
v_mfma_f32_16x16x32_bf16 a[192:195],v[64:67],v[32:35],a[192:195]
ds_read_b64_tr_b16 v[114:115],v2 offset:5216
ds_read_b64_tr_b16 v[118:119],v2 offset:15456
v_mfma_f32_16x16x32_bf16 a[192:195],v[68:71],v[36:39],a[192:195]
buffer_load_dwordx4 a[152:155],v6,s[28:31],s21 offen offset:2048 sc1
v_mfma_f32_16x16x32_bf16 a[192:195],v[72:75],v[40:43],a[192:195]
ds_read_b64_tr_b16 v[122:123],v2 offset:25696
v_mfma_f32_16x16x32_bf16 a[192:195],v[76:79],v[44:47],a[192:195]
v_mfma_f32_16x16x32_bf16 a[196:199],v[76:79],v[180:183],a[196:199]
v_mfma_f32_16x16x32_bf16 a[196:199],v[72:75],v[164:167],a[196:199]
v_mfma_f32_16x16x32_bf16 a[196:199],v[68:71],v[148:151],a[196:199]
v_mfma_f32_16x16x32_bf16 a[196:199],v[64:67],v[132:135],a[196:199]
s_waitcnt vmcnt(18)
v_mfma_f32_16x16x32_bf16 a[200:203],v[64:67],v[196:199],a[200:203]
buffer_load_dwordx4 v26,s[48:51],s84 offen offset:2048 lds
v_mfma_f32_16x16x32_bf16 a[200:203],v[68:71],v[212:215],a[200:203]
v_mfma_f32_16x16x32_bf16 a[200:203],v[72:75],v[228:231],a[200:203]
v_mfma_f32_16x16x32_bf16 a[200:203],v[76:79],v[244:247],a[200:203]
v_mfma_f32_16x16x32_bf16 a[204:207],v[76:79],v[188:191],a[204:207]
ds_read_b64_tr_b16 v[76:77],v1 offset:30848
ds_read_b64_tr_b16 v[78:79],v1 offset:35968
v_mfma_f32_16x16x32_bf16 a[204:207],v[72:75],v[172:175],a[204:207]
s_waitcnt vmcnt(15)
ds_read_b64_tr_b16 v[72:73],v1 offset:20608
v_mfma_f32_16x16x32_bf16 a[204:207],v[68:71],v[156:159],a[204:207]
buffer_load_dwordx4 a[168:171],v6,s[28:31],s22 offen offset:2048 sc1
v_mfma_f32_16x16x32_bf16 a[204:207],v[64:67],v[140:143],a[204:207]
ds_read_b64_tr_b16 v[64:65],v1 offset:128
ds_read_b64_tr_b16 v[66:67],v1 offset:5248
v_mfma_f32_16x16x32_bf16 a[208:211],v[80:83],v[32:35],a[208:211]
ds_read_b64_tr_b16 v[68:69],v1 offset:10368
v_mfma_f32_16x16x32_bf16 a[208:211],v[84:87],v[36:39],a[208:211]
ds_read_b64_tr_b16 v[70:71],v1 offset:15488
v_mfma_f32_16x16x32_bf16 a[208:211],v[88:91],v[40:43],a[208:211]
ds_read_b64_tr_b16 v[74:75],v1 offset:25728
v_mfma_f32_16x16x32_bf16 a[208:211],v[92:95],v[44:47],a[208:211]
v_mfma_f32_16x16x32_bf16 a[212:215],v[92:95],v[180:183],a[212:215]
v_mfma_f32_16x16x32_bf16 a[212:215],v[88:91],v[164:167],a[212:215]
buffer_load_dwordx4 v27,s[48:51],s84 offen offset:3072 lds
v_mfma_f32_16x16x32_bf16 a[212:215],v[84:87],v[148:151],a[212:215]
s_add_u32 m0,s86,50176
v_mfma_f32_16x16x32_bf16 a[212:215],v[80:83],v[132:135],a[212:215]
v_mfma_f32_16x16x32_bf16 a[216:219],v[80:83],v[196:199],a[216:219]
v_mfma_f32_16x16x32_bf16 a[216:219],v[84:87],v[212:215],a[216:219]
v_mfma_f32_16x16x32_bf16 a[216:219],v[88:91],v[228:231],a[216:219]
v_mfma_f32_16x16x32_bf16 a[216:219],v[92:95],v[244:247],a[216:219]
buffer_load_dwordx4 a[184:187],v6,s[28:31],s23 offen offset:2048 sc1
v_mfma_f32_16x16x32_bf16 a[220:223],v[92:95],v[188:191],a[220:223]
ds_read_b64_tr_b16 v[92:93],v2 offset:30880
ds_read_b64_tr_b16 v[94:95],v2 offset:36000
v_mfma_f32_16x16x32_bf16 a[220:223],v[88:91],v[172:175],a[220:223]
ds_read_b64_tr_b16 v[88:89],v2 offset:20640
v_mfma_f32_16x16x32_bf16 a[220:223],v[84:87],v[156:159],a[220:223]
ds_read_b64_tr_b16 v[84:85],v2 offset:10400
v_mfma_f32_16x16x32_bf16 a[220:223],v[80:83],v[140:143],a[220:223]
ds_read_b64_tr_b16 v[80:81],v2 offset:160
v_mfma_f32_16x16x32_bf16 a[224:227],v[96:99],v[32:35],a[224:227]
ds_read_b64_tr_b16 v[82:83],v2 offset:5280
ds_read_b64_tr_b16 v[86:87],v2 offset:15520
v_mfma_f32_16x16x32_bf16 a[224:227],v[100:103],v[36:39],a[224:227]
ds_read_b64_tr_b16 v[90:91],v2 offset:25760
v_mfma_f32_16x16x32_bf16 a[224:227],v[104:107],v[40:43],a[224:227]
buffer_load_dwordx4 v7,s[48:51],s84 offen lds
v_mfma_f32_16x16x32_bf16 a[224:227],v[108:111],v[44:47],a[224:227]
s_add_u32 m0,s86,0
s_add_u32 s84,s83,20480
v_mfma_f32_16x16x32_bf16 a[228:231],v[108:111],v[180:183],a[228:231]
v_mfma_f32_16x16x32_bf16 a[228:231],v[104:107],v[164:167],a[228:231]
v_mfma_f32_16x16x32_bf16 a[228:231],v[100:103],v[148:151],a[228:231]
v_mfma_f32_16x16x32_bf16 a[228:231],v[96:99],v[132:135],a[228:231]
buffer_load_dwordx4 a[68:71],v6,s[28:31],s87 offen sc1
v_mfma_f32_16x16x32_bf16 a[232:235],v[96:99],v[196:199],a[232:235]
v_mfma_f32_16x16x32_bf16 a[232:235],v[100:103],v[212:215],a[232:235]
v_mfma_f32_16x16x32_bf16 a[232:235],v[104:107],v[228:231],a[232:235]
v_mfma_f32_16x16x32_bf16 a[232:235],v[108:111],v[244:247],a[232:235]
v_mfma_f32_16x16x32_bf16 a[236:239],v[108:111],v[188:191],a[236:239]
buffer_load_dwordx4 a[188:191],v28,s[28:31],s23 offen offset:3072 sc1
v_mfma_f32_16x16x32_bf16 a[236:239],v[104:107],v[172:175],a[236:239]
ds_read_b64_tr_b16 v[104:105],v1 offset:20672
ds_read_b64_tr_b16 v[106:107],v1 offset:25792
v_mfma_f32_16x16x32_bf16 a[236:239],v[100:103],v[156:159],a[236:239]
ds_read_b64_tr_b16 v[100:101],v1 offset:10432
v_mfma_f32_16x16x32_bf16 a[236:239],v[96:99],v[140:143],a[236:239]
s_waitcnt lgkmcnt(3)
ds_read_b64_tr_b16 v[96:97],v1 offset:192
v_mfma_f32_16x16x32_bf16 a[240:243],v[112:115],v[32:35],a[240:243]
ds_read_b64_tr_b16 v[98:99],v1 offset:5312
v_mfma_f32_16x16x32_bf16 a[240:243],v[116:119],v[36:39],a[240:243]
buffer_load_dwordx4 a[100:103],v6,s[28:31],s87 offen offset:1024 sc1
v_mfma_f32_16x16x32_bf16 a[240:243],v[120:123],v[40:43],a[240:243]
ds_read_b64_tr_b16 v[102:103],v1 offset:15552
ds_read_b64_tr_b16 v[108:109],v1 offset:30912
v_mfma_f32_16x16x32_bf16 a[240:243],v[124:127],v[44:47],a[240:243]
ds_read_b64_tr_b16 v[110:111],v1 offset:36032
v_mfma_f32_16x16x32_bf16 a[244:247],v[124:127],v[180:183],a[244:247]
v_mfma_f32_16x16x32_bf16 a[244:247],v[120:123],v[164:167],a[244:247]
v_mfma_f32_16x16x32_bf16 a[244:247],v[116:119],v[148:151],a[244:247]
buffer_load_dwordx4 a[172:175],v28,s[28:31],s22 offen offset:3072 sc1
v_mfma_f32_16x16x32_bf16 a[244:247],v[112:115],v[132:135],a[244:247]
v_mfma_f32_16x16x32_bf16 a[248:251],v[112:115],v[196:199],a[248:251]
v_mfma_f32_16x16x32_bf16 a[248:251],v[116:119],v[212:215],a[248:251]
v_mfma_f32_16x16x32_bf16 a[248:251],v[120:123],v[228:231],a[248:251]
v_mfma_f32_16x16x32_bf16 a[248:251],v[124:127],v[244:247],a[248:251]
buffer_load_dwordx4 a[76:79],v6,s[28:31],s88 offen sc1
v_mfma_f32_16x16x32_bf16 a[252:255],v[124:127],v[188:191],a[252:255]
ds_read_b64_tr_b16 v[124:125],v2 offset:30944
ds_read_b64_tr_b16 v[126:127],v2 offset:36064
v_mfma_f32_16x16x32_bf16 a[252:255],v[120:123],v[172:175],a[252:255]
ds_read_b64_tr_b16 v[120:121],v2 offset:20704
v_mfma_f32_16x16x32_bf16 a[252:255],v[116:119],v[156:159],a[252:255]
ds_read_b64_tr_b16 v[116:117],v2 offset:10464
v_mfma_f32_16x16x32_bf16 a[252:255],v[112:115],v[140:143],a[252:255]
ds_read_b64_tr_b16 v[112:113],v2 offset:224
v_mfma_f32_16x16x32_bf16 a[0:3],v[64:67],v[32:35],a[0:3]
buffer_load_dwordx4 a[64:67],v6,s[28:31],s87 offen offset:2048 sc1
v_mfma_f32_16x16x32_bf16 a[0:3],v[68:71],v[36:39],a[0:3]
ds_read_b64_tr_b16 v[114:115],v2 offset:5344
ds_read_b64_tr_b16 v[118:119],v2 offset:15584
v_mfma_f32_16x16x32_bf16 a[0:3],v[72:75],v[40:43],a[0:3]
ds_read_b64_tr_b16 v[122:123],v2 offset:25824
v_mfma_f32_16x16x32_bf16 a[0:3],v[76:79],v[44:47],a[0:3]
v_mfma_f32_16x16x32_bf16 a[4:7],v[76:79],v[180:183],a[4:7]
v_mfma_f32_16x16x32_bf16 a[4:7],v[72:75],v[164:167],a[4:7]
buffer_load_dwordx4 a[156:159],v28,s[28:31],s21 offen offset:3072 sc1
v_mfma_f32_16x16x32_bf16 a[4:7],v[68:71],v[148:151],a[4:7]
v_mfma_f32_16x16x32_bf16 a[4:7],v[64:67],v[132:135],a[4:7]
v_mfma_f32_16x16x32_bf16 a[8:11],v[64:67],v[196:199],a[8:11]
v_mfma_f32_16x16x32_bf16 a[8:11],v[68:71],v[212:215],a[8:11]
v_mfma_f32_16x16x32_bf16 a[8:11],v[72:75],v[228:231],a[8:11]
buffer_load_dwordx4 a[84:87],v6,s[28:31],s89 offen sc1
v_mfma_f32_16x16x32_bf16 a[8:11],v[76:79],v[244:247],a[8:11]
v_mfma_f32_16x16x32_bf16 a[12:15],v[76:79],v[188:191],a[12:15]
ds_read_b64_tr_b16 v[76:77],v1 offset:30976
ds_read_b64_tr_b16 v[78:79],v1 offset:36096
v_mfma_f32_16x16x32_bf16 a[12:15],v[72:75],v[172:175],a[12:15]
ds_read_b64_tr_b16 v[72:73],v1 offset:20736
v_mfma_f32_16x16x32_bf16 a[12:15],v[68:71],v[156:159],a[12:15]
ds_read_b64_tr_b16 v[68:69],v1 offset:10496
v_mfma_f32_16x16x32_bf16 a[12:15],v[64:67],v[140:143],a[12:15]
buffer_load_dwordx4 a[140:143],v28,s[28:31],s20 offen offset:3072 sc1
v_mfma_f32_16x16x32_bf16 a[16:19],v[80:83],v[32:35],a[16:19]
s_add_u32 s20,s64,3
ds_read_b64_tr_b16 v[64:65],v1 offset:256
ds_read_b64_tr_b16 v[66:67],v1 offset:5376
v_mfma_f32_16x16x32_bf16 a[16:19],v[84:87],v[36:39],a[16:19]
s_and_b32 s20,s20,3
s_lshl_b32 s20,s20,1
ds_read_b64_tr_b16 v[70:71],v1 offset:15616
v_mfma_f32_16x16x32_bf16 a[16:19],v[88:91],v[40:43],a[16:19]
s_or_b32 s20,s20,17
ds_read_b64_tr_b16 v[74:75],v1 offset:25856
v_add_u32 v1,0xffff6000,v1
v_mfma_f32_16x16x32_bf16 a[16:19],v[92:95],v[44:47],a[16:19]
s_lshl_b32 s20,s20,16
s_add_u32 s21,s20,4096
s_add_u32 s22,s20,8192
v_mfma_f32_16x16x32_bf16 a[20:23],v[92:95],v[180:183],a[20:23]
buffer_load_dwordx4 a[72:75],v6,s[28:31],s88 offen offset:2048 sc1
v_mfma_f32_16x16x32_bf16 a[20:23],v[88:91],v[164:167],a[20:23]
s_add_u32 s23,s20,12288
v_mfma_f32_16x16x32_bf16 a[20:23],v[84:87],v[148:151],a[20:23]
v_mfma_f32_16x16x32_bf16 a[20:23],v[80:83],v[132:135],a[20:23]
v_mfma_f32_16x16x32_bf16 a[24:27],v[80:83],v[196:199],a[24:27]
v_mfma_f32_16x16x32_bf16 a[24:27],v[84:87],v[212:215],a[24:27]
buffer_load_dwordx4 a[92:95],v6,s[28:31],s90 offen sc1
v_mfma_f32_16x16x32_bf16 a[24:27],v[88:91],v[228:231],a[24:27]
v_mfma_f32_16x16x32_bf16 a[24:27],v[92:95],v[244:247],a[24:27]
v_mfma_f32_16x16x32_bf16 a[28:31],v[92:95],v[188:191],a[28:31]
ds_read_b64_tr_b16 v[92:93],v2 offset:31008
ds_read_b64_tr_b16 v[94:95],v2 offset:36128
v_mfma_f32_16x16x32_bf16 a[28:31],v[88:91],v[172:175],a[28:31]
ds_read_b64_tr_b16 v[88:89],v2 offset:20768
v_mfma_f32_16x16x32_bf16 a[28:31],v[84:87],v[156:159],a[28:31]
buffer_load_dwordx4 a[80:83],v6,s[28:31],s89 offen offset:2048 sc1
v_mfma_f32_16x16x32_bf16 a[28:31],v[80:83],v[140:143],a[28:31]
s_waitcnt lgkmcnt(3)
ds_read_b64_tr_b16 v[80:81],v2 offset:288
ds_read_b64_tr_b16 v[82:83],v2 offset:5408
v_mfma_f32_16x16x32_bf16 v[128:131],v[96:99],v[32:35],v[128:131]
ds_read_b64_tr_b16 v[84:85],v2 offset:10528
v_mfma_f32_16x16x32_bf16 v[128:131],v[100:103],v[36:39],v[128:131]
ds_read_b64_tr_b16 v[86:87],v2 offset:15648
v_mfma_f32_16x16x32_bf16 v[128:131],v[104:107],v[40:43],v[128:131]
ds_read_b64_tr_b16 v[90:91],v2 offset:25888
v_add_u32 v2,0xffff6000,v2
v_mfma_f32_16x16x32_bf16 v[128:131],v[108:111],v[44:47],v[128:131]
v_mfma_f32_16x16x32_bf16 v[136:139],v[108:111],v[180:183],v[136:139]
v_mfma_f32_16x16x32_bf16 v[136:139],v[104:107],v[164:167],v[136:139]
buffer_load_dwordx4 v24,s[48:51],s83 offen lds
v_mfma_f32_16x16x32_bf16 v[136:139],v[100:103],v[148:151],v[136:139]
v_mfma_f32_16x16x32_bf16 v[136:139],v[96:99],v[132:135],v[136:139]
v_mfma_f32_16x16x32_bf16 v[144:147],v[96:99],v[196:199],v[144:147]
v_mfma_f32_16x16x32_bf16 v[144:147],v[100:103],v[212:215],v[144:147]
v_mfma_f32_16x16x32_bf16 v[144:147],v[104:107],v[228:231],v[144:147]
v_mfma_f32_16x16x32_bf16 v[144:147],v[108:111],v[244:247],v[144:147]
v_mfma_f32_16x16x32_bf16 v[152:155],v[108:111],v[188:191],v[152:155]
buffer_load_dwordx4 v25,s[48:51],s83 offen offset:1024 lds
v_mfma_f32_16x16x32_bf16 v[152:155],v[104:107],v[172:175],v[152:155]
ds_read_b64_tr_b16 v[104:105],v1 offset:20480
ds_read_b64_tr_b16 v[106:107],v1 offset:25600
v_mfma_f32_16x16x32_bf16 v[152:155],v[100:103],v[156:159],v[152:155]
ds_read_b64_tr_b16 v[100:101],v1 offset:10240
v_mfma_f32_16x16x32_bf16 v[152:155],v[96:99],v[140:143],v[152:155]
ds_read_b64_tr_b16 v[96:97],v1
s_waitcnt vmcnt(14) lgkmcnt(4)
s_barrier
v_mfma_f32_16x16x32_bf16 v[160:163],v[112:115],v[32:35],v[160:163]
)A"
R"A(ds_read_b64_tr_b16 v[98:99],v1 offset:5120
v_mfma_f32_16x16x32_bf16 v[160:163],v[116:119],v[36:39],v[160:163]
ds_read_b64_tr_b16 v[102:103],v1 offset:15360
ds_read_b64_tr_b16 v[108:109],v1 offset:30720
v_mfma_f32_16x16x32_bf16 v[160:163],v[120:123],v[40:43],v[160:163]
buffer_load_dwordx4 v26,s[48:51],s83 offen offset:2048 lds
v_mfma_f32_16x16x32_bf16 v[160:163],v[124:127],v[44:47],v[160:163]
ds_read_b64_tr_b16 v[110:111],v1 offset:35840
v_mfma_f32_16x16x32_bf16 v[168:171],v[124:127],v[180:183],v[168:171]
v_mfma_f32_16x16x32_bf16 v[168:171],v[120:123],v[164:167],v[168:171]
v_mfma_f32_16x16x32_bf16 v[168:171],v[116:119],v[148:151],v[168:171]
v_mfma_f32_16x16x32_bf16 v[168:171],v[112:115],v[132:135],v[168:171]
v_mfma_f32_16x16x32_bf16 v[176:179],v[112:115],v[196:199],v[176:179]
v_mfma_f32_16x16x32_bf16 v[176:179],v[116:119],v[212:215],v[176:179]
buffer_load_dwordx4 v27,s[48:51],s83 offen offset:3072 lds
v_mfma_f32_16x16x32_bf16 v[176:179],v[120:123],v[228:231],v[176:179]
s_add_u32 m0,s86,4096
L_pd16_%=:
s_cbranch_execz L_trtg2F_%=
v_mfma_f32_16x16x32_bf16 v[176:179],v[124:127],v[244:247],v[176:179]
v_mfma_f32_16x16x32_bf16 v[184:187],v[124:127],v[188:191],v[184:187]
ds_read_b64_tr_b16 v[124:125],v2 offset:30752
ds_read_b64_tr_b16 v[126:127],v2 offset:35872
v_mfma_f32_16x16x32_bf16 v[184:187],v[120:123],v[172:175],v[184:187]
ds_read_b64_tr_b16 v[120:121],v2 offset:20512
v_mfma_f32_16x16x32_bf16 v[184:187],v[116:119],v[156:159],v[184:187]
buffer_load_dwordx4 a[124:127],v6,s[28:31],s90 offen offset:1024 sc1
v_mfma_f32_16x16x32_bf16 v[184:187],v[112:115],v[140:143],v[184:187]
ds_read_b64_tr_b16 v[112:113],v2 offset:32
ds_read_b64_tr_b16 v[114:115],v2 offset:5152
v_mfma_f32_16x16x32_bf16 v[192:195],v[64:67],v[32:35],v[192:195]
ds_read_b64_tr_b16 v[116:117],v2 offset:10272
v_mfma_f32_16x16x32_bf16 v[192:195],v[68:71],v[36:39],v[192:195]
ds_read_b64_tr_b16 v[118:119],v2 offset:15392
v_mfma_f32_16x16x32_bf16 v[192:195],v[72:75],v[40:43],v[192:195]
ds_read_b64_tr_b16 v[122:123],v2 offset:25632
v_mfma_f32_16x16x32_bf16 v[192:195],v[76:79],v[44:47],v[192:195]
buffer_load_dwordx4 a[88:91],v6,s[28:31],s90 offen offset:2048 sc1
v_mfma_f32_16x16x32_bf16 v[200:203],v[76:79],v[180:183],v[200:203]
v_mfma_f32_16x16x32_bf16 v[200:203],v[72:75],v[164:167],v[200:203]
v_mfma_f32_16x16x32_bf16 v[200:203],v[68:71],v[148:151],v[200:203]
v_mfma_f32_16x16x32_bf16 v[200:203],v[64:67],v[132:135],v[200:203]
v_mfma_f32_16x16x32_bf16 v[208:211],v[64:67],v[196:199],v[208:211]
v_mfma_f32_16x16x32_bf16 v[208:211],v[68:71],v[212:215],v[208:211]
v_mfma_f32_16x16x32_bf16 v[208:211],v[72:75],v[228:231],v[208:211]
buffer_load_dwordx4 v7,s[48:51],s83 offen lds
v_mfma_f32_16x16x32_bf16 v[208:211],v[76:79],v[244:247],v[208:211]
s_add_u32 m0,s86,5120
s_add_u32 s83,s64,3
s_and_b32 s83,s83,3
v_mfma_f32_16x16x32_bf16 v[216:219],v[76:79],v[188:191],v[216:219]
s_lshl_b32 s83,s83,1
ds_read_b64_tr_b16 v[76:77],v1 offset:30784
ds_read_b64_tr_b16 v[78:79],v1 offset:35904
v_mfma_f32_16x16x32_bf16 v[216:219],v[72:75],v[172:175],v[216:219]
buffer_load_dwordx2 v[4:5],v3,s[44:47],s91 offen offset:1280 sc1
v_mfma_f32_16x16x32_bf16 v[216:219],v[68:71],v[156:159],v[216:219]
s_or_b32 s83,s83,1
ds_read_b64_tr_b16 v[68:69],v1 offset:10304
ds_read_b64_tr_b16 v[70:71],v1 offset:15424
v_mfma_f32_16x16x32_bf16 v[216:219],v[64:67],v[140:143],v[216:219]
s_mul_i32 s83,s83,163840
s_add_u32 s83,s83,s85
ds_read_b64_tr_b16 v[64:65],v1 offset:64
v_mfma_f32_16x16x32_bf16 v[224:227],v[80:83],v[32:35],v[224:227]
ds_read_b64_tr_b16 v[66:67],v1 offset:5184
v_mfma_f32_16x16x32_bf16 v[224:227],v[84:87],v[36:39],v[224:227]
ds_read_b64_tr_b16 v[72:73],v1 offset:20544
v_mfma_f32_16x16x32_bf16 v[224:227],v[88:91],v[40:43],v[224:227]
ds_read_b64_tr_b16 v[74:75],v1 offset:25664
v_mfma_f32_16x16x32_bf16 v[224:227],v[92:95],v[44:47],v[224:227]
v_mfma_f32_16x16x32_bf16 v[232:235],v[92:95],v[180:183],v[232:235]
buffer_load_dwordx4 v24,s[48:51],s84 offen lds
v_mfma_f32_16x16x32_bf16 v[232:235],v[88:91],v[164:167],v[232:235]
v_mfma_f32_16x16x32_bf16 v[232:235],v[84:87],v[148:151],v[232:235]
v_mfma_f32_16x16x32_bf16 v[232:235],v[80:83],v[132:135],v[232:235]
v_mfma_f32_16x16x32_bf16 v[240:243],v[80:83],v[196:199],v[240:243]
v_mfma_f32_16x16x32_bf16 v[240:243],v[84:87],v[212:215],v[240:243]
buffer_load_dwordx4 a[116:119],v6,s[28:31],s89 offen offset:1024 sc1
v_mfma_f32_16x16x32_bf16 v[240:243],v[88:91],v[228:231],v[240:243]
v_mfma_f32_16x16x32_bf16 v[240:243],v[92:95],v[244:247],v[240:243]
v_mfma_f32_16x16x32_bf16 v[248:251],v[92:95],v[188:191],v[248:251]
ds_read_b64_tr_b16 v[92:93],v2 offset:30816
ds_read_b64_tr_b16 v[94:95],v2 offset:35936
v_mfma_f32_16x16x32_bf16 v[248:251],v[88:91],v[172:175],v[248:251]
ds_read_b64_tr_b16 v[88:89],v2 offset:20576
v_mfma_f32_16x16x32_bf16 v[248:251],v[84:87],v[156:159],v[248:251]
ds_read_b64_tr_b16 v[84:85],v2 offset:10336
v_mfma_f32_16x16x32_bf16 v[248:251],v[80:83],v[140:143],v[248:251]
s_waitcnt lgkmcnt(4)
ds_read_b64_tr_b16 v[80:81],v2 offset:96
v_mfma_f32_16x16x32_bf16 a[192:195],v[96:99],a[128:131],a[192:195]
buffer_load_dwordx4 v25,s[48:51],s84 offen offset:1024 lds
v_mfma_f32_16x16x32_bf16 a[192:195],v[100:103],a[144:147],a[192:195]
ds_read_b64_tr_b16 v[82:83],v2 offset:5216
ds_read_b64_tr_b16 v[86:87],v2 offset:15456
v_mfma_f32_16x16x32_bf16 a[192:195],v[104:107],a[160:163],a[192:195]
ds_read_b64_tr_b16 v[90:91],v2 offset:25696
v_mfma_f32_16x16x32_bf16 a[192:195],v[108:111],a[176:179],a[192:195]
v_mfma_f32_16x16x32_bf16 a[196:199],v[108:111],a[180:183],a[196:199]
v_mfma_f32_16x16x32_bf16 a[196:199],v[104:107],a[164:167],a[196:199]
v_mfma_f32_16x16x32_bf16 a[196:199],v[100:103],a[148:151],a[196:199]
v_mfma_f32_16x16x32_bf16 a[196:199],v[96:99],a[132:135],a[196:199]
buffer_load_dwordx4 v26,s[48:51],s84 offen offset:2048 lds
v_mfma_f32_16x16x32_bf16 a[200:203],v[96:99],a[136:139],a[200:203]
v_mfma_f32_16x16x32_bf16 a[200:203],v[100:103],a[152:155],a[200:203]
v_mfma_f32_16x16x32_bf16 a[200:203],v[104:107],a[168:171],a[200:203]
v_mfma_f32_16x16x32_bf16 a[200:203],v[108:111],a[184:187],a[200:203]
s_waitcnt vmcnt(19)
v_mfma_f32_16x16x32_bf16 a[204:207],v[108:111],a[188:191],a[204:207]
ds_read_b64_tr_b16 v[108:109],v1 offset:30848
ds_read_b64_tr_b16 v[110:111],v1 offset:35968
v_mfma_f32_16x16x32_bf16 a[204:207],v[104:107],a[172:175],a[204:207]
s_waitcnt vmcnt(15)
ds_read_b64_tr_b16 v[104:105],v1 offset:20608
v_mfma_f32_16x16x32_bf16 a[204:207],v[100:103],a[156:159],a[204:207]
buffer_load_dwordx4 v27,s[48:51],s84 offen offset:3072 lds
v_mfma_f32_16x16x32_bf16 a[204:207],v[96:99],a[140:143],a[204:207]
s_add_u32 m0,s86,9216
ds_read_b64_tr_b16 v[96:97],v1 offset:128
ds_read_b64_tr_b16 v[98:99],v1 offset:5248
v_mfma_f32_16x16x32_bf16 a[208:211],v[112:115],a[128:131],a[208:211]
ds_read_b64_tr_b16 v[100:101],v1 offset:10368
v_mfma_f32_16x16x32_bf16 a[208:211],v[116:119],a[144:147],a[208:211]
ds_read_b64_tr_b16 v[102:103],v1 offset:15488
v_mfma_f32_16x16x32_bf16 a[208:211],v[120:123],a[160:163],a[208:211]
ds_read_b64_tr_b16 v[106:107],v1 offset:25728
v_mfma_f32_16x16x32_bf16 a[208:211],v[124:127],a[176:179],a[208:211]
v_mfma_f32_16x16x32_bf16 a[212:215],v[124:127],a[180:183],a[212:215]
v_mfma_f32_16x16x32_bf16 a[212:215],v[120:123],a[164:167],a[212:215]
buffer_load_dwordx4 v7,s[48:51],s84 offen lds
v_mfma_f32_16x16x32_bf16 a[212:215],v[116:119],a[148:151],a[212:215]
s_add_u32 m0,s86,122880
s_add_u32 s84,s83,20480
v_mfma_f32_16x16x32_bf16 a[212:215],v[112:115],a[132:135],a[212:215]
v_mfma_f32_16x16x32_bf16 a[216:219],v[112:115],a[136:139],a[216:219]
v_mfma_f32_16x16x32_bf16 a[216:219],v[116:119],a[152:155],a[216:219]
v_mfma_f32_16x16x32_bf16 a[216:219],v[120:123],a[168:171],a[216:219]
buffer_load_dwordx4 a[120:123],v28,s[28:31],s90 offen offset:3072 sc1
v_mfma_f32_16x16x32_bf16 a[216:219],v[124:127],a[184:187],a[216:219]
v_mfma_f32_16x16x32_bf16 a[220:223],v[124:127],a[188:191],a[220:223]
ds_read_b64_tr_b16 v[124:125],v2 offset:30880
ds_read_b64_tr_b16 v[126:127],v2 offset:36000
v_mfma_f32_16x16x32_bf16 a[220:223],v[120:123],a[172:175],a[220:223]
ds_read_b64_tr_b16 v[120:121],v2 offset:20640
v_mfma_f32_16x16x32_bf16 a[220:223],v[116:119],a[156:159],a[220:223]
ds_read_b64_tr_b16 v[116:117],v2 offset:10400
v_mfma_f32_16x16x32_bf16 a[220:223],v[112:115],a[140:143],a[220:223]
buffer_load_dwordx4 a[108:111],v6,s[28:31],s88 offen offset:1024 sc1
v_mfma_f32_16x16x32_bf16 a[224:227],v[64:67],a[128:131],a[224:227]
ds_read_b64_tr_b16 v[112:113],v2 offset:160
ds_read_b64_tr_b16 v[114:115],v2 offset:5280
v_mfma_f32_16x16x32_bf16 a[224:227],v[68:71],a[144:147],a[224:227]
ds_read_b64_tr_b16 v[118:119],v2 offset:15520
v_mfma_f32_16x16x32_bf16 a[224:227],v[72:75],a[160:163],a[224:227]
ds_read_b64_tr_b16 v[122:123],v2 offset:25760
v_mfma_f32_16x16x32_bf16 a[224:227],v[76:79],a[176:179],a[224:227]
v_mfma_f32_16x16x32_bf16 a[228:231],v[76:79],a[180:183],a[228:231]
buffer_load_dwordx4 a[112:115],v28,s[28:31],s89 offen offset:3072 sc1
v_mfma_f32_16x16x32_bf16 a[228:231],v[72:75],a[164:167],a[228:231]
v_mfma_f32_16x16x32_bf16 a[228:231],v[68:71],a[148:151],a[228:231]
v_mfma_f32_16x16x32_bf16 a[228:231],v[64:67],a[132:135],a[228:231]
v_mfma_f32_16x16x32_bf16 a[232:235],v[64:67],a[136:139],a[232:235]
v_mfma_f32_16x16x32_bf16 a[232:235],v[68:71],a[152:155],a[232:235]
buffer_load_dwordx4 a[104:107],v28,s[28:31],s88 offen offset:3072 sc1
v_mfma_f32_16x16x32_bf16 a[232:235],v[72:75],a[168:171],a[232:235]
s_waitcnt vmcnt(10)
v_cmp_ne_u32 s[56:57],s52,v4
v_cmp_ne_u32 s[58:59],s53,v5
v_mfma_f32_16x16x32_bf16 a[232:235],v[76:79],a[184:187],a[232:235]
s_or_b64 s[56:57],s[56:57],s[58:59]
s_cbranch_scc1 L_fr3_%=
L_fo3_%=:
s_lshl_b32 s91,s64,6
v_mfma_f32_16x16x32_bf16 a[236:239],v[76:79],a[188:191],a[236:239]
ds_read_b64_tr_b16 v[76:77],v1 offset:30912
ds_read_b64_tr_b16 v[78:79],v1 offset:36032
v_mfma_f32_16x16x32_bf16 a[236:239],v[72:75],a[172:175],a[236:239]
ds_read_b64_tr_b16 v[72:73],v1 offset:20672
v_bfe_u32 v3,v6,4,2
v_mfma_f32_16x16x32_bf16 a[236:239],v[68:71],a[156:159],a[236:239]
buffer_load_dwordx4 v[32:35],v6,s[28:31],s20 offen sc1
v_mfma_f32_16x16x32_bf16 a[236:239],v[64:67],a[140:143],a[236:239]
s_waitcnt lgkmcnt(3)
ds_read_b64_tr_b16 v[64:65],v1 offset:192
ds_read_b64_tr_b16 v[66:67],v1 offset:5312
v_mfma_f32_16x16x32_bf16 a[240:243],v[80:83],a[128:131],a[240:243]
ds_read_b64_tr_b16 v[68:69],v1 offset:10432
v_min_u32 v3,2,v3
v_mfma_f32_16x16x32_bf16 a[240:243],v[84:87],a[144:147],a[240:243]
ds_read_b64_tr_b16 v[70:71],v1 offset:15552
v_add3_u32 v3,v3,s63,1
v_mfma_f32_16x16x32_bf16 a[240:243],v[88:91],a[160:163],a[240:243]
ds_read_b64_tr_b16 v[74:75],v1 offset:25792
v_and_b32 v3,3,v3
v_mfma_f32_16x16x32_bf16 a[240:243],v[92:95],a[176:179],a[240:243]
v_lshlrev_b32 v3,8,v3
v_mfma_f32_16x16x32_bf16 a[244:247],v[92:95],a[180:183],a[244:247]
v_mfma_f32_16x16x32_bf16 a[244:247],v[88:91],a[164:167],a[244:247]
buffer_load_dwordx4 v24,s[48:51],s83 offen lds
v_mfma_f32_16x16x32_bf16 a[244:247],v[84:87],a[148:151],a[244:247]
v_mfma_f32_16x16x32_bf16 a[244:247],v[80:83],a[132:135],a[244:247]
v_mfma_f32_16x16x32_bf16 a[248:251],v[80:83],a[136:139],a[248:251]
v_mfma_f32_16x16x32_bf16 a[248:251],v[84:87],a[152:155],a[248:251]
v_mfma_f32_16x16x32_bf16 a[248:251],v[88:91],a[168:171],a[248:251]
buffer_load_dwordx4 v[36:39],v6,s[28:31],s21 offen sc1
v_mfma_f32_16x16x32_bf16 a[248:251],v[92:95],a[184:187],a[248:251]
v_mfma_f32_16x16x32_bf16 a[252:255],v[92:95],a[188:191],a[252:255]
ds_read_b64_tr_b16 v[92:93],v2 offset:30944
ds_read_b64_tr_b16 v[94:95],v2 offset:36064
v_mfma_f32_16x16x32_bf16 a[252:255],v[88:91],a[172:175],a[252:255]
ds_read_b64_tr_b16 v[88:89],v2 offset:20704
v_mfma_f32_16x16x32_bf16 a[252:255],v[84:87],a[156:159],a[252:255]
ds_read_b64_tr_b16 v[84:85],v2 offset:10464
v_mfma_f32_16x16x32_bf16 a[252:255],v[80:83],a[140:143],a[252:255]
ds_read_b64_tr_b16 v[80:81],v2 offset:224
v_mfma_f32_16x16x32_bf16 a[0:3],v[96:99],a[128:131],a[0:3]
ds_read_b64_tr_b16 v[82:83],v2 offset:5344
ds_read_b64_tr_b16 v[86:87],v2 offset:15584
v_mfma_f32_16x16x32_bf16 a[0:3],v[100:103],a[144:147],a[0:3]
)A"
R"A(buffer_load_dwordx4 v25,s[48:51],s83 offen offset:1024 lds
v_mfma_f32_16x16x32_bf16 a[0:3],v[104:107],a[160:163],a[0:3]
ds_read_b64_tr_b16 v[90:91],v2 offset:25824
v_mfma_f32_16x16x32_bf16 a[0:3],v[108:111],a[176:179],a[0:3]
v_mfma_f32_16x16x32_bf16 a[4:7],v[108:111],a[180:183],a[4:7]
v_mfma_f32_16x16x32_bf16 a[4:7],v[104:107],a[164:167],a[4:7]
v_mfma_f32_16x16x32_bf16 a[4:7],v[100:103],a[148:151],a[4:7]
buffer_load_dwordx4 v[40:43],v6,s[28:31],s22 offen sc1
v_mfma_f32_16x16x32_bf16 a[4:7],v[96:99],a[132:135],a[4:7]
v_mfma_f32_16x16x32_bf16 a[8:11],v[96:99],a[136:139],a[8:11]
v_mfma_f32_16x16x32_bf16 a[8:11],v[100:103],a[152:155],a[8:11]
v_mfma_f32_16x16x32_bf16 a[8:11],v[104:107],a[168:171],a[8:11]
v_mfma_f32_16x16x32_bf16 a[8:11],v[108:111],a[184:187],a[8:11]
buffer_load_dwordx4 a[96:99],v28,s[28:31],s87 offen offset:3072 sc1
v_mfma_f32_16x16x32_bf16 a[12:15],v[108:111],a[188:191],a[12:15]
s_add_u32 s87,s64,3
ds_read_b64_tr_b16 v[108:109],v1 offset:30976
ds_read_b64_tr_b16 v[110:111],v1 offset:36096
v_mfma_f32_16x16x32_bf16 a[12:15],v[104:107],a[172:175],a[12:15]
s_and_b32 s87,s87,3
s_lshl_b32 s87,s87,1
ds_read_b64_tr_b16 v[104:105],v1 offset:20736
v_mfma_f32_16x16x32_bf16 a[12:15],v[100:103],a[156:159],a[12:15]
s_or_b32 s87,s87,1
s_lshl_b32 s87,s87,16
ds_read_b64_tr_b16 v[100:101],v1 offset:10496
v_mfma_f32_16x16x32_bf16 a[12:15],v[96:99],a[140:143],a[12:15]
s_add_u32 s88,s87,4096
s_add_u32 s89,s87,8192
ds_read_b64_tr_b16 v[96:97],v1 offset:256
v_mfma_f32_16x16x32_bf16 a[16:19],v[112:115],a[128:131],a[16:19]
buffer_load_dwordx4 v[44:47],v6,s[28:31],s23 offen sc1
v_mfma_f32_16x16x32_bf16 a[16:19],v[116:119],a[144:147],a[16:19]
s_add_u32 s90,s87,12288
ds_read_b64_tr_b16 v[98:99],v1 offset:5376
ds_read_b64_tr_b16 v[102:103],v1 offset:15616
v_mfma_f32_16x16x32_bf16 a[16:19],v[120:123],a[160:163],a[16:19]
ds_read_b64_tr_b16 v[106:107],v1 offset:25856
v_add_u32 v1,0xffff6000,v1
v_mfma_f32_16x16x32_bf16 a[16:19],v[124:127],a[176:179],a[16:19]
v_mfma_f32_16x16x32_bf16 a[20:23],v[124:127],a[180:183],a[20:23]
v_mfma_f32_16x16x32_bf16 a[20:23],v[120:123],a[164:167],a[20:23]
v_mfma_f32_16x16x32_bf16 a[20:23],v[116:119],a[148:151],a[20:23]
v_mfma_f32_16x16x32_bf16 a[20:23],v[112:115],a[132:135],a[20:23]
buffer_load_dwordx4 v26,s[48:51],s83 offen offset:2048 lds
v_mfma_f32_16x16x32_bf16 a[24:27],v[112:115],a[136:139],a[24:27]
v_mfma_f32_16x16x32_bf16 a[24:27],v[116:119],a[152:155],a[24:27]
v_mfma_f32_16x16x32_bf16 a[24:27],v[120:123],a[168:171],a[24:27]
v_mfma_f32_16x16x32_bf16 a[24:27],v[124:127],a[184:187],a[24:27]
v_mfma_f32_16x16x32_bf16 a[28:31],v[124:127],a[188:191],a[28:31]
buffer_load_dwordx4 v[180:183],v6,s[28:31],s23 offen offset:1024 sc1
v_mfma_f32_16x16x32_bf16 a[28:31],v[120:123],a[172:175],a[28:31]
ds_read_b64_tr_b16 v[120:121],v2 offset:20768
ds_read_b64_tr_b16 v[122:123],v2 offset:25888
v_mfma_f32_16x16x32_bf16 a[28:31],v[116:119],a[156:159],a[28:31]
ds_read_b64_tr_b16 v[116:117],v2 offset:10528
v_mfma_f32_16x16x32_bf16 a[28:31],v[112:115],a[140:143],a[28:31]
s_waitcnt lgkmcnt(3)
ds_read_b64_tr_b16 v[112:113],v2 offset:288
v_mfma_f32_16x16x32_bf16 v[128:131],v[64:67],a[128:131],v[128:131]
ds_read_b64_tr_b16 v[114:115],v2 offset:5408
v_mfma_f32_16x16x32_bf16 v[128:131],v[68:71],a[144:147],v[128:131]
ds_read_b64_tr_b16 v[118:119],v2 offset:15648
ds_read_b64_tr_b16 v[124:125],v2 offset:31008
v_mfma_f32_16x16x32_bf16 v[128:131],v[72:75],a[160:163],v[128:131]
ds_read_b64_tr_b16 v[126:127],v2 offset:36128
v_add_u32 v2,0xffff6000,v2
v_mfma_f32_16x16x32_bf16 v[128:131],v[76:79],a[176:179],v[128:131]
buffer_load_dwordx4 v[164:167],v6,s[28:31],s22 offen offset:1024 sc1
v_mfma_f32_16x16x32_bf16 v[136:139],v[76:79],a[180:183],v[136:139]
v_mfma_f32_16x16x32_bf16 v[136:139],v[72:75],a[164:167],v[136:139]
v_mfma_f32_16x16x32_bf16 v[136:139],v[68:71],a[148:151],v[136:139]
v_mfma_f32_16x16x32_bf16 v[136:139],v[64:67],a[132:135],v[136:139]
v_mfma_f32_16x16x32_bf16 v[144:147],v[64:67],a[136:139],v[144:147]
v_mfma_f32_16x16x32_bf16 v[144:147],v[68:71],a[152:155],v[144:147]
v_mfma_f32_16x16x32_bf16 v[144:147],v[72:75],a[168:171],v[144:147]
buffer_load_dwordx4 v27,s[48:51],s83 offen offset:3072 lds
v_mfma_f32_16x16x32_bf16 v[144:147],v[76:79],a[184:187],v[144:147]
s_add_u32 m0,s86,126976
v_mfma_f32_16x16x32_bf16 v[152:155],v[76:79],a[188:191],v[152:155]
ds_read_b64_tr_b16 v[76:77],v1 offset:30720
ds_read_b64_tr_b16 v[78:79],v1 offset:35840
v_mfma_f32_16x16x32_bf16 v[152:155],v[72:75],a[172:175],v[152:155]
ds_read_b64_tr_b16 v[72:73],v1 offset:20480
v_mfma_f32_16x16x32_bf16 v[152:155],v[68:71],a[156:159],v[152:155]
ds_read_b64_tr_b16 v[68:69],v1 offset:10240
v_mfma_f32_16x16x32_bf16 v[152:155],v[64:67],a[140:143],v[152:155]
buffer_load_dwordx4 v[148:151],v6,s[28:31],s21 offen offset:1024 sc1
v_mfma_f32_16x16x32_bf16 v[160:163],v[80:83],a[128:131],v[160:163]
ds_read_b64_tr_b16 v[64:65],v1
ds_read_b64_tr_b16 v[66:67],v1 offset:5120
v_mfma_f32_16x16x32_bf16 v[160:163],v[84:87],a[144:147],v[160:163]
ds_read_b64_tr_b16 v[70:71],v1 offset:15360
v_mfma_f32_16x16x32_bf16 v[160:163],v[88:91],a[160:163],v[160:163]
ds_read_b64_tr_b16 v[74:75],v1 offset:25600
v_mfma_f32_16x16x32_bf16 v[160:163],v[92:95],a[176:179],v[160:163]
v_mfma_f32_16x16x32_bf16 v[168:171],v[92:95],a[180:183],v[168:171]
v_mfma_f32_16x16x32_bf16 v[168:171],v[88:91],a[164:167],v[168:171]
v_mfma_f32_16x16x32_bf16 v[168:171],v[84:87],a[148:151],v[168:171]
buffer_load_dwordx4 v7,s[48:51],s83 offen lds
v_mfma_f32_16x16x32_bf16 v[168:171],v[80:83],a[132:135],v[168:171]
s_add_u32 m0,s86,128000
s_mul_i32 s83,s63,20480
v_mfma_f32_16x16x32_bf16 v[176:179],v[80:83],a[136:139],v[176:179]
v_mfma_f32_16x16x32_bf16 v[176:179],v[84:87],a[152:155],v[176:179]
v_mfma_f32_16x16x32_bf16 v[176:179],v[88:91],a[168:171],v[176:179]
v_mfma_f32_16x16x32_bf16 v[176:179],v[92:95],a[184:187],v[176:179]
buffer_load_dwordx4 v[132:135],v6,s[28:31],s20 offen offset:1024 sc1
v_mfma_f32_16x16x32_bf16 v[184:187],v[92:95],a[188:191],v[184:187]
ds_read_b64_tr_b16 v[92:93],v2 offset:30752
ds_read_b64_tr_b16 v[94:95],v2 offset:35872
v_mfma_f32_16x16x32_bf16 v[184:187],v[88:91],a[172:175],v[184:187]
ds_read_b64_tr_b16 v[88:89],v2 offset:20512
v_mfma_f32_16x16x32_bf16 v[184:187],v[84:87],a[156:159],v[184:187]
ds_read_b64_tr_b16 v[84:85],v2 offset:10272
v_mfma_f32_16x16x32_bf16 v[184:187],v[80:83],a[140:143],v[184:187]
ds_read_b64_tr_b16 v[80:81],v2 offset:32
v_mfma_f32_16x16x32_bf16 v[192:195],v[96:99],a[128:131],v[192:195]
ds_read_b64_tr_b16 v[82:83],v2 offset:5152
ds_read_b64_tr_b16 v[86:87],v2 offset:15392
v_mfma_f32_16x16x32_bf16 v[192:195],v[100:103],a[144:147],v[192:195]
ds_read_b64_tr_b16 v[90:91],v2 offset:25632
v_mfma_f32_16x16x32_bf16 v[192:195],v[104:107],a[160:163],v[192:195]
buffer_load_dwordx4 v24,s[48:51],s84 offen lds
v_mfma_f32_16x16x32_bf16 v[192:195],v[108:111],a[176:179],v[192:195]
v_mfma_f32_16x16x32_bf16 v[200:203],v[108:111],a[180:183],v[200:203]
v_mfma_f32_16x16x32_bf16 v[200:203],v[104:107],a[164:167],v[200:203]
v_mfma_f32_16x16x32_bf16 v[200:203],v[100:103],a[148:151],v[200:203]
v_mfma_f32_16x16x32_bf16 v[200:203],v[96:99],a[132:135],v[200:203]
buffer_load_dwordx4 v[196:199],v6,s[28:31],s20 offen offset:2048 sc1
v_mfma_f32_16x16x32_bf16 v[208:211],v[96:99],a[136:139],v[208:211]
v_mfma_f32_16x16x32_bf16 v[208:211],v[100:103],a[152:155],v[208:211]
v_mfma_f32_16x16x32_bf16 v[208:211],v[104:107],a[168:171],v[208:211]
v_mfma_f32_16x16x32_bf16 v[208:211],v[108:111],a[184:187],v[208:211]
v_mfma_f32_16x16x32_bf16 v[216:219],v[108:111],a[188:191],v[216:219]
ds_read_b64_tr_b16 v[108:109],v1 offset:30784
ds_read_b64_tr_b16 v[110:111],v1 offset:35904
v_mfma_f32_16x16x32_bf16 v[216:219],v[104:107],a[172:175],v[216:219]
ds_read_b64_tr_b16 v[104:105],v1 offset:20544
v_mfma_f32_16x16x32_bf16 v[216:219],v[100:103],a[156:159],v[216:219]
buffer_load_dwordx4 v25,s[48:51],s84 offen offset:1024 lds
v_mfma_f32_16x16x32_bf16 v[216:219],v[96:99],a[140:143],v[216:219]
s_waitcnt lgkmcnt(3)
ds_read_b64_tr_b16 v[96:97],v1 offset:64
ds_read_b64_tr_b16 v[98:99],v1 offset:5184
v_mfma_f32_16x16x32_bf16 v[224:227],v[112:115],a[128:131],v[224:227]
ds_read_b64_tr_b16 v[100:101],v1 offset:10304
v_mfma_f32_16x16x32_bf16 v[224:227],v[116:119],a[144:147],v[224:227]
ds_read_b64_tr_b16 v[102:103],v1 offset:15424
v_mfma_f32_16x16x32_bf16 v[224:227],v[120:123],a[160:163],v[224:227]
ds_read_b64_tr_b16 v[106:107],v1 offset:25664
v_mfma_f32_16x16x32_bf16 v[224:227],v[124:127],a[176:179],v[224:227]
buffer_load_dwordx4 v[212:215],v6,s[28:31],s21 offen offset:2048 sc1
v_mfma_f32_16x16x32_bf16 v[232:235],v[124:127],a[180:183],v[232:235]
v_mfma_f32_16x16x32_bf16 v[232:235],v[120:123],a[164:167],v[232:235]
v_mfma_f32_16x16x32_bf16 v[232:235],v[116:119],a[148:151],v[232:235]
v_mfma_f32_16x16x32_bf16 v[232:235],v[112:115],a[132:135],v[232:235]
v_mfma_f32_16x16x32_bf16 v[240:243],v[112:115],a[136:139],v[240:243]
v_mfma_f32_16x16x32_bf16 v[240:243],v[116:119],a[152:155],v[240:243]
v_mfma_f32_16x16x32_bf16 v[240:243],v[120:123],a[168:171],v[240:243]
buffer_load_dwordx4 v[228:231],v6,s[28:31],s22 offen offset:2048 sc1
v_mfma_f32_16x16x32_bf16 v[240:243],v[124:127],a[184:187],v[240:243]
v_mfma_f32_16x16x32_bf16 v[248:251],v[124:127],a[188:191],v[248:251]
ds_read_b64_tr_b16 v[124:125],v2 offset:30816
ds_read_b64_tr_b16 v[126:127],v2 offset:35936
v_mfma_f32_16x16x32_bf16 v[248:251],v[120:123],a[172:175],v[248:251]
ds_read_b64_tr_b16 v[120:121],v2 offset:20576
v_mfma_f32_16x16x32_bf16 v[248:251],v[116:119],a[156:159],v[248:251]
ds_read_b64_tr_b16 v[116:117],v2 offset:10336
v_mfma_f32_16x16x32_bf16 v[248:251],v[112:115],a[140:143],v[248:251]
ds_read_b64_tr_b16 v[112:113],v2 offset:96
v_mfma_f32_16x16x32_bf16 a[192:195],v[64:67],a[68:71],a[192:195]
ds_read_b64_tr_b16 v[114:115],v2 offset:5216
ds_read_b64_tr_b16 v[118:119],v2 offset:15456
v_mfma_f32_16x16x32_bf16 a[192:195],v[68:71],a[76:79],a[192:195]
buffer_load_dwordx4 v26,s[48:51],s84 offen offset:2048 lds
v_mfma_f32_16x16x32_bf16 a[192:195],v[72:75],a[84:87],a[192:195]
ds_read_b64_tr_b16 v[122:123],v2 offset:25696
v_mfma_f32_16x16x32_bf16 a[192:195],v[76:79],a[92:95],a[192:195]
v_mfma_f32_16x16x32_bf16 a[196:199],v[76:79],a[124:127],a[196:199]
s_waitcnt vmcnt(28)
v_mfma_f32_16x16x32_bf16 a[196:199],v[72:75],a[116:119],a[196:199]
s_waitcnt vmcnt(19)
v_mfma_f32_16x16x32_bf16 a[196:199],v[68:71],a[108:111],a[196:199]
buffer_load_dwordx4 v[244:247],v6,s[28:31],s23 offen offset:2048 sc1
v_mfma_f32_16x16x32_bf16 a[196:199],v[64:67],a[100:103],a[196:199]
v_mfma_f32_16x16x32_bf16 a[200:203],v[64:67],a[64:67],a[200:203]
v_mfma_f32_16x16x32_bf16 a[200:203],v[68:71],a[72:75],a[200:203]
v_mfma_f32_16x16x32_bf16 a[200:203],v[72:75],a[80:83],a[200:203]
v_mfma_f32_16x16x32_bf16 a[200:203],v[76:79],a[88:91],a[200:203]
v_mfma_f32_16x16x32_bf16 a[204:207],v[76:79],a[120:123],a[204:207]
s_waitcnt vmcnt(15)
ds_read_b64_tr_b16 v[76:77],v1 offset:30848
ds_read_b64_tr_b16 v[78:79],v1 offset:35968
v_mfma_f32_16x16x32_bf16 a[204:207],v[72:75],a[112:115],a[204:207]
buffer_load_dwordx4 v27,s[48:51],s84 offen offset:3072 lds
v_mfma_f32_16x16x32_bf16 a[204:207],v[68:71],a[104:107],a[204:207]
s_add_u32 m0,s86,132096
ds_read_b64_tr_b16 v[68:69],v1 offset:10368
ds_read_b64_tr_b16 v[70:71],v1 offset:15488
v_mfma_f32_16x16x32_bf16 a[204:207],v[64:67],a[96:99],a[204:207]
ds_read_b64_tr_b16 v[64:65],v1 offset:128
v_mfma_f32_16x16x32_bf16 a[208:211],v[80:83],a[68:71],a[208:211]
ds_read_b64_tr_b16 v[66:67],v1 offset:5248
v_mfma_f32_16x16x32_bf16 a[208:211],v[84:87],a[76:79],a[208:211]
ds_read_b64_tr_b16 v[72:73],v1 offset:20608
v_mfma_f32_16x16x32_bf16 a[208:211],v[88:91],a[84:87],a[208:211]
buffer_load_dwordx4 v[188:191],v28,s[28:31],s23 offen offset:3072 sc1
v_mfma_f32_16x16x32_bf16 a[208:211],v[92:95],a[92:95],a[208:211]
ds_read_b64_tr_b16 v[74:75],v1 offset:25728
v_mfma_f32_16x16x32_bf16 a[212:215],v[92:95],a[124:127],a[212:215]
v_mfma_f32_16x16x32_bf16 a[212:215],v[88:91],a[116:119],a[212:215]
v_mfma_f32_16x16x32_bf16 a[212:215],v[84:87],a[108:111],a[212:215]
v_mfma_f32_16x16x32_bf16 a[212:215],v[80:83],a[100:103],a[212:215]
)A"
R"A(v_mfma_f32_16x16x32_bf16 a[216:219],v[80:83],a[64:67],a[216:219]
v_mfma_f32_16x16x32_bf16 a[216:219],v[84:87],a[72:75],a[216:219]
buffer_load_dwordx4 v7,s[48:51],s84 offen lds
v_mfma_f32_16x16x32_bf16 a[216:219],v[88:91],a[80:83],a[216:219]
v_mfma_f32_16x16x32_bf16 a[216:219],v[92:95],a[88:91],a[216:219]
v_mfma_f32_16x16x32_bf16 a[220:223],v[92:95],a[120:123],a[220:223]
ds_read_b64_tr_b16 v[92:93],v2 offset:30880
ds_read_b64_tr_b16 v[94:95],v2 offset:36000
v_mfma_f32_16x16x32_bf16 a[220:223],v[88:91],a[112:115],a[220:223]
ds_read_b64_tr_b16 v[88:89],v2 offset:20640
v_mfma_f32_16x16x32_bf16 a[220:223],v[84:87],a[104:107],a[220:223]
buffer_load_dwordx4 v[172:175],v28,s[28:31],s22 offen offset:3072 sc1
v_mfma_f32_16x16x32_bf16 a[220:223],v[80:83],a[96:99],a[220:223]
s_waitcnt lgkmcnt(3)
ds_read_b64_tr_b16 v[80:81],v2 offset:160
ds_read_b64_tr_b16 v[82:83],v2 offset:5280
v_mfma_f32_16x16x32_bf16 a[224:227],v[96:99],a[68:71],a[224:227]
ds_read_b64_tr_b16 v[84:85],v2 offset:10400
v_mfma_f32_16x16x32_bf16 a[224:227],v[100:103],a[76:79],a[224:227]
ds_read_b64_tr_b16 v[86:87],v2 offset:15520
v_mfma_f32_16x16x32_bf16 a[224:227],v[104:107],a[84:87],a[224:227]
ds_read_b64_tr_b16 v[90:91],v2 offset:25760
v_mfma_f32_16x16x32_bf16 a[224:227],v[108:111],a[92:95],a[224:227]
buffer_load_dwordx4 a[128:131],v6,s[28:31],s87 offen sc1
v_mfma_f32_16x16x32_bf16 a[228:231],v[108:111],a[124:127],a[228:231]
v_mfma_f32_16x16x32_bf16 a[228:231],v[104:107],a[116:119],a[228:231]
v_mfma_f32_16x16x32_bf16 a[228:231],v[100:103],a[108:111],a[228:231]
v_mfma_f32_16x16x32_bf16 a[228:231],v[96:99],a[100:103],a[228:231]
v_mfma_f32_16x16x32_bf16 a[232:235],v[96:99],a[64:67],a[232:235]
buffer_load_dwordx4 v[156:159],v28,s[28:31],s21 offen offset:3072 sc1
v_mfma_f32_16x16x32_bf16 a[232:235],v[100:103],a[72:75],a[232:235]
v_mfma_f32_16x16x32_bf16 a[232:235],v[104:107],a[80:83],a[232:235]
v_mfma_f32_16x16x32_bf16 a[232:235],v[108:111],a[88:91],a[232:235]
v_mfma_f32_16x16x32_bf16 a[236:239],v[108:111],a[120:123],a[236:239]
ds_read_b64_tr_b16 v[108:109],v1 offset:30912
ds_read_b64_tr_b16 v[110:111],v1 offset:36032
v_mfma_f32_16x16x32_bf16 a[236:239],v[104:107],a[112:115],a[236:239]
buffer_load_dwordx4 a[144:147],v6,s[28:31],s88 offen sc1
v_mfma_f32_16x16x32_bf16 a[236:239],v[100:103],a[104:107],a[236:239]
ds_read_b64_tr_b16 v[100:101],v1 offset:10432
ds_read_b64_tr_b16 v[102:103],v1 offset:15552
v_mfma_f32_16x16x32_bf16 a[236:239],v[96:99],a[96:99],a[236:239]
ds_read_b64_tr_b16 v[96:97],v1 offset:192
v_mfma_f32_16x16x32_bf16 a[240:243],v[112:115],a[68:71],a[240:243]
ds_read_b64_tr_b16 v[98:99],v1 offset:5312
v_mfma_f32_16x16x32_bf16 a[240:243],v[116:119],a[76:79],a[240:243]
ds_read_b64_tr_b16 v[104:105],v1 offset:20672
v_mfma_f32_16x16x32_bf16 a[240:243],v[120:123],a[84:87],a[240:243]
buffer_load_dwordx4 v[140:143],v28,s[28:31],s20 offen offset:3072 sc1
v_mfma_f32_16x16x32_bf16 a[240:243],v[124:127],a[92:95],a[240:243]
ds_read_b64_tr_b16 v[106:107],v1 offset:25792
v_add_u32 v1,0xffff6000,v1
v_mfma_f32_16x16x32_bf16 a[244:247],v[124:127],a[124:127],a[244:247]
v_mfma_f32_16x16x32_bf16 a[244:247],v[120:123],a[116:119],a[244:247]
v_mfma_f32_16x16x32_bf16 a[244:247],v[116:119],a[108:111],a[244:247]
v_mfma_f32_16x16x32_bf16 a[244:247],v[112:115],a[100:103],a[244:247]
buffer_load_dwordx4 a[160:163],v6,s[28:31],s89 offen sc1
v_mfma_f32_16x16x32_bf16 a[248:251],v[112:115],a[64:67],a[248:251]
v_mfma_f32_16x16x32_bf16 a[248:251],v[116:119],a[72:75],a[248:251]
v_mfma_f32_16x16x32_bf16 a[248:251],v[120:123],a[80:83],a[248:251]
v_mfma_f32_16x16x32_bf16 a[248:251],v[124:127],a[88:91],a[248:251]
v_mfma_f32_16x16x32_bf16 a[252:255],v[124:127],a[120:123],a[252:255]
buffer_load_dwordx4 a[176:179],v6,s[28:31],s90 offen sc1
v_mfma_f32_16x16x32_bf16 a[252:255],v[120:123],a[112:115],a[252:255]
ds_read_b64_tr_b16 v[120:121],v2 offset:20704
ds_read_b64_tr_b16 v[122:123],v2 offset:25824
v_mfma_f32_16x16x32_bf16 a[252:255],v[116:119],a[104:107],a[252:255]
ds_read_b64_tr_b16 v[116:117],v2 offset:10464
v_mfma_f32_16x16x32_bf16 a[252:255],v[112:115],a[96:99],a[252:255]
ds_read_b64_tr_b16 v[112:113],v2 offset:224
v_mfma_f32_16x16x32_bf16 a[0:3],v[64:67],a[68:71],a[0:3]
ds_read_b64_tr_b16 v[114:115],v2 offset:5344
v_mfma_f32_16x16x32_bf16 a[0:3],v[68:71],a[76:79],a[0:3]
buffer_load_dwordx4 a[180:183],v6,s[28:31],s90 offen offset:1024 sc1
v_mfma_f32_16x16x32_bf16 a[0:3],v[72:75],a[84:87],a[0:3]
ds_read_b64_tr_b16 v[118:119],v2 offset:15584
ds_read_b64_tr_b16 v[124:125],v2 offset:30944
v_mfma_f32_16x16x32_bf16 a[0:3],v[76:79],a[92:95],a[0:3]
ds_read_b64_tr_b16 v[126:127],v2 offset:36064
v_add_u32 v2,0xffff6000,v2
v_mfma_f32_16x16x32_bf16 a[4:7],v[76:79],a[124:127],a[4:7]
v_mfma_f32_16x16x32_bf16 a[4:7],v[72:75],a[116:119],a[4:7]
v_mfma_f32_16x16x32_bf16 a[4:7],v[68:71],a[108:111],a[4:7]
buffer_load_dwordx4 a[164:167],v6,s[28:31],s89 offen offset:1024 sc1
v_mfma_f32_16x16x32_bf16 a[4:7],v[64:67],a[100:103],a[4:7]
v_mfma_f32_16x16x32_bf16 a[8:11],v[64:67],a[64:67],a[8:11]
v_mfma_f32_16x16x32_bf16 a[8:11],v[68:71],a[72:75],a[8:11]
v_mfma_f32_16x16x32_bf16 a[8:11],v[72:75],a[80:83],a[8:11]
v_mfma_f32_16x16x32_bf16 a[8:11],v[76:79],a[88:91],a[8:11]
buffer_load_dwordx4 a[148:151],v6,s[28:31],s88 offen offset:1024 sc1
v_mfma_f32_16x16x32_bf16 a[12:15],v[76:79],a[120:123],a[12:15]
v_mfma_f32_16x16x32_bf16 a[12:15],v[72:75],a[112:115],a[12:15]
v_mfma_f32_16x16x32_bf16 a[12:15],v[68:71],a[104:107],a[12:15]
v_mfma_f32_16x16x32_bf16 a[12:15],v[64:67],a[96:99],a[12:15]
s_waitcnt vmcnt(10)
s_barrier
s_waitcnt lgkmcnt(0)
v_mfma_f32_16x16x32_bf16 a[16:19],v[80:83],a[68:71],a[16:19]
buffer_load_dwordx4 a[132:135],v6,s[28:31],s87 offen offset:1024 sc1
v_mfma_f32_16x16x32_bf16 a[16:19],v[84:87],a[76:79],a[16:19]
ds_read_b64_tr_b16 v[64:65],v1
ds_read_b64_tr_b16 v[66:67],v1 offset:5120
v_mfma_f32_16x16x32_bf16 a[16:19],v[88:91],a[84:87],a[16:19]
ds_read_b64_tr_b16 v[68:69],v1 offset:10240
v_mfma_f32_16x16x32_bf16 a[16:19],v[92:95],a[92:95],a[16:19]
ds_read_b64_tr_b16 v[70:71],v1 offset:15360
v_mfma_f32_16x16x32_bf16 a[20:23],v[92:95],a[124:127],a[20:23]
ds_read_b64_tr_b16 v[72:73],v1 offset:20480
v_mfma_f32_16x16x32_bf16 a[20:23],v[88:91],a[116:119],a[20:23]
buffer_load_dwordx4 a[136:139],v6,s[28:31],s87 offen offset:2048 sc1
v_mfma_f32_16x16x32_bf16 a[20:23],v[84:87],a[108:111],a[20:23]
ds_read_b64_tr_b16 v[74:75],v1 offset:25600
s_nop 0
L_pd17_%=:
s_cbranch_execz L_trtg2F_%=
ds_read_b64_tr_b16 v[76:77],v1 offset:30720
v_mfma_f32_16x16x32_bf16 a[20:23],v[80:83],a[100:103],a[20:23]
ds_read_b64_tr_b16 v[78:79],v1 offset:35840
v_add_u32 v1,0x1e000,v1
v_mfma_f32_16x16x32_bf16 a[24:27],v[80:83],a[64:67],a[24:27]
v_mfma_f32_16x16x32_bf16 a[24:27],v[84:87],a[72:75],a[24:27]
v_mfma_f32_16x16x32_bf16 a[24:27],v[88:91],a[80:83],a[24:27]
buffer_load_dwordx4 a[152:155],v6,s[28:31],s88 offen offset:2048 sc1
v_mfma_f32_16x16x32_bf16 a[24:27],v[92:95],a[88:91],a[24:27]
v_mfma_f32_16x16x32_bf16 a[28:31],v[92:95],a[120:123],a[28:31]
ds_read_b64_tr_b16 v[92:93],v2 offset:30752
ds_read_b64_tr_b16 v[94:95],v2 offset:35872
v_mfma_f32_16x16x32_bf16 a[28:31],v[88:91],a[112:115],a[28:31]
ds_read_b64_tr_b16 v[88:89],v2 offset:20512
v_mfma_f32_16x16x32_bf16 a[28:31],v[84:87],a[104:107],a[28:31]
ds_read_b64_tr_b16 v[84:85],v2 offset:10272
v_mfma_f32_16x16x32_bf16 a[28:31],v[80:83],a[96:99],a[28:31]
buffer_load_dwordx4 a[168:171],v6,s[28:31],s89 offen offset:2048 sc1
v_mfma_f32_16x16x32_bf16 v[128:131],v[96:99],a[68:71],v[128:131]
ds_read_b64_tr_b16 v[80:81],v2 offset:32
ds_read_b64_tr_b16 v[82:83],v2 offset:5152
v_mfma_f32_16x16x32_bf16 v[128:131],v[100:103],a[76:79],v[128:131]
ds_read_b64_tr_b16 v[86:87],v2 offset:15392
v_mfma_f32_16x16x32_bf16 v[128:131],v[104:107],a[84:87],v[128:131]
ds_read_b64_tr_b16 v[90:91],v2 offset:25632
v_add_u32 v2,0x1e000,v2
v_mfma_f32_16x16x32_bf16 v[128:131],v[108:111],a[92:95],v[128:131]
v_mfma_f32_16x16x32_bf16 v[136:139],v[108:111],a[124:127],v[136:139]
buffer_load_dwordx4 a[184:187],v6,s[28:31],s90 offen offset:2048 sc1
v_mfma_f32_16x16x32_bf16 v[136:139],v[104:107],a[116:119],v[136:139]
v_mfma_f32_16x16x32_bf16 v[136:139],v[100:103],a[108:111],v[136:139]
v_mfma_f32_16x16x32_bf16 v[136:139],v[96:99],a[100:103],v[136:139]
v_mfma_f32_16x16x32_bf16 v[144:147],v[96:99],a[64:67],v[144:147]
v_mfma_f32_16x16x32_bf16 v[144:147],v[100:103],a[72:75],v[144:147]
buffer_load_dwordx4 a[188:191],v28,s[28:31],s90 offen offset:3072 sc1
v_mfma_f32_16x16x32_bf16 v[144:147],v[104:107],a[80:83],v[144:147]
v_mfma_f32_16x16x32_bf16 v[144:147],v[108:111],a[88:91],v[144:147]
v_mfma_f32_16x16x32_bf16 v[152:155],v[108:111],a[120:123],v[152:155]
ds_read_b64_tr_b16 v[108:109],v1 offset:30720
ds_read_b64_tr_b16 v[110:111],v1 offset:35840
v_mfma_f32_16x16x32_bf16 v[152:155],v[104:107],a[112:115],v[152:155]
ds_read_b64_tr_b16 v[104:105],v1 offset:20480
v_mfma_f32_16x16x32_bf16 v[152:155],v[100:103],a[104:107],v[152:155]
buffer_load_dwordx4 a[172:175],v28,s[28:31],s89 offen offset:3072 sc1
v_mfma_f32_16x16x32_bf16 v[152:155],v[96:99],a[96:99],v[152:155]
ds_read_b64_tr_b16 v[96:97],v1
ds_read_b64_tr_b16 v[98:99],v1 offset:5120
v_mfma_f32_16x16x32_bf16 v[160:163],v[112:115],a[68:71],v[160:163]
ds_read_b64_tr_b16 v[100:101],v1 offset:10240
v_mfma_f32_16x16x32_bf16 v[160:163],v[116:119],a[76:79],v[160:163]
ds_read_b64_tr_b16 v[102:103],v1 offset:15360
v_mfma_f32_16x16x32_bf16 v[160:163],v[120:123],a[84:87],v[160:163]
ds_read_b64_tr_b16 v[106:107],v1 offset:25600
v_add_u32 v1,0xfffe2000,v1
v_mfma_f32_16x16x32_bf16 v[160:163],v[124:127],a[92:95],v[160:163]
buffer_load_dwordx4 a[156:159],v28,s[28:31],s88 offen offset:3072 sc1
v_mfma_f32_16x16x32_bf16 v[168:171],v[124:127],a[124:127],v[168:171]
v_mfma_f32_16x16x32_bf16 v[168:171],v[120:123],a[116:119],v[168:171]
v_mfma_f32_16x16x32_bf16 v[168:171],v[116:119],a[108:111],v[168:171]
v_mfma_f32_16x16x32_bf16 v[168:171],v[112:115],a[100:103],v[168:171]
v_mfma_f32_16x16x32_bf16 v[176:179],v[112:115],a[64:67],v[176:179]
buffer_load_dwordx4 a[140:143],v28,s[28:31],s87 offen offset:3072 sc1
v_mfma_f32_16x16x32_bf16 v[176:179],v[116:119],a[72:75],v[176:179]
s_mul_i32 s87,s60,81920
s_add_u32 s87,s87,s66
s_add_u32 s83,s83,s87
v_mfma_f32_16x16x32_bf16 v[176:179],v[120:123],a[80:83],v[176:179]
s_lshl_b32 s66,s63,6
s_add_u32 s87,s87,s66
s_add_u32 s88,s87,20480
v_mfma_f32_16x16x32_bf16 v[176:179],v[124:127],a[88:91],v[176:179]
s_add_u32 s89,s87,40960
s_add_u32 s90,s87,61440
s_add_u32 s66,s77,4096
v_mfma_f32_16x16x32_bf16 v[184:187],v[124:127],a[120:123],v[184:187]
ds_read_b64_tr_b16 v[124:125],v2 offset:30752
ds_read_b64_tr_b16 v[126:127],v2 offset:35872
v_mfma_f32_16x16x32_bf16 v[184:187],v[120:123],a[112:115],v[184:187]
ds_read_b64_tr_b16 v[120:121],v2 offset:20512
v_mfma_f32_16x16x32_bf16 v[184:187],v[116:119],a[104:107],v[184:187]
ds_read_b64_tr_b16 v[116:117],v2 offset:10272
v_mfma_f32_16x16x32_bf16 v[184:187],v[112:115],a[96:99],v[184:187]
s_waitcnt lgkmcnt(4)
ds_read_b64_tr_b16 v[112:113],v2 offset:32
v_mfma_f32_16x16x32_bf16 a[192:195],v[64:67],v[32:35],a[192:195]
ds_read_b64_tr_b16 v[114:115],v2 offset:5152
ds_read_b64_tr_b16 v[118:119],v2 offset:15392
v_mfma_f32_16x16x32_bf16 a[192:195],v[68:71],v[36:39],a[192:195]
ds_read_b64_tr_b16 v[122:123],v2 offset:25632
v_add_u32 v2,0xfffe2000,v2
v_mfma_f32_16x16x32_bf16 a[192:195],v[72:75],v[40:43],a[192:195]
v_mfma_f32_16x16x32_bf16 a[192:195],v[76:79],v[44:47],a[192:195]
v_mfma_f32_16x16x32_bf16 a[196:199],v[76:79],v[180:183],a[196:199]
v_mfma_f32_16x16x32_bf16 a[196:199],v[72:75],v[164:167],a[196:199]
v_mfma_f32_16x16x32_bf16 a[196:199],v[68:71],v[148:151],a[196:199]
v_mfma_f32_16x16x32_bf16 a[196:199],v[64:67],v[132:135],a[196:199]
v_mfma_f32_16x16x32_bf16 a[200:203],v[64:67],v[196:199],a[200:203]
v_mfma_f32_16x16x32_bf16 a[200:203],v[68:71],v[212:215],a[200:203]
v_mfma_f32_16x16x32_bf16 a[200:203],v[72:75],v[228:231],a[200:203]
v_mfma_f32_16x16x32_bf16 a[200:203],v[76:79],v[244:247],a[200:203]
v_mfma_f32_16x16x32_bf16 a[204:207],v[76:79],v[188:191],a[204:207]
s_waitcnt vmcnt(18)
ds_read_b64_tr_b16 v[76:77],v1 offset:30784
)A"
R"A(ds_read_b64_tr_b16 v[78:79],v1 offset:35904
v_mfma_f32_16x16x32_bf16 a[204:207],v[72:75],v[172:175],a[204:207]
s_waitcnt vmcnt(16)
ds_read_b64_tr_b16 v[72:73],v1 offset:20544
v_mfma_f32_16x16x32_bf16 a[204:207],v[68:71],v[156:159],a[204:207]
s_waitcnt vmcnt(14)
ds_read_b64_tr_b16 v[68:69],v1 offset:10304
v_mfma_f32_16x16x32_bf16 a[204:207],v[64:67],v[140:143],a[204:207]
ds_read_b64_tr_b16 v[64:65],v1 offset:64
v_mfma_f32_16x16x32_bf16 a[208:211],v[80:83],v[32:35],a[208:211]
ds_read_b64_tr_b16 v[66:67],v1 offset:5184
ds_read_b64_tr_b16 v[70:71],v1 offset:15424
v_mfma_f32_16x16x32_bf16 a[208:211],v[84:87],v[36:39],a[208:211]
ds_read_b64_tr_b16 v[74:75],v1 offset:25664
v_add_u32 v1,0x1e000,v1
v_mfma_f32_16x16x32_bf16 a[208:211],v[88:91],v[40:43],a[208:211]
v_mfma_f32_16x16x32_bf16 a[208:211],v[92:95],v[44:47],a[208:211]
v_mfma_f32_16x16x32_bf16 a[212:215],v[92:95],v[180:183],a[212:215]
v_mfma_f32_16x16x32_bf16 a[212:215],v[88:91],v[164:167],a[212:215]
v_mfma_f32_16x16x32_bf16 a[212:215],v[84:87],v[148:151],a[212:215]
v_mfma_f32_16x16x32_bf16 a[212:215],v[80:83],v[132:135],a[212:215]
v_mfma_f32_16x16x32_bf16 a[216:219],v[80:83],v[196:199],a[216:219]
v_mfma_f32_16x16x32_bf16 a[216:219],v[84:87],v[212:215],a[216:219]
v_mfma_f32_16x16x32_bf16 a[216:219],v[88:91],v[228:231],a[216:219]
v_mfma_f32_16x16x32_bf16 a[216:219],v[92:95],v[244:247],a[216:219]
v_mfma_f32_16x16x32_bf16 a[220:223],v[92:95],v[188:191],a[220:223]
ds_read_b64_tr_b16 v[92:93],v2 offset:30816
ds_read_b64_tr_b16 v[94:95],v2 offset:35936
v_mfma_f32_16x16x32_bf16 a[220:223],v[88:91],v[172:175],a[220:223]
ds_read_b64_tr_b16 v[88:89],v2 offset:20576
v_mfma_f32_16x16x32_bf16 a[220:223],v[84:87],v[156:159],a[220:223]
ds_read_b64_tr_b16 v[84:85],v2 offset:10336
v_mfma_f32_16x16x32_bf16 a[220:223],v[80:83],v[140:143],a[220:223]
ds_read_b64_tr_b16 v[80:81],v2 offset:96
v_mfma_f32_16x16x32_bf16 a[192:195],v[96:99],a[128:131],a[192:195]
ds_read_b64_tr_b16 v[82:83],v2 offset:5216
ds_read_b64_tr_b16 v[86:87],v2 offset:15456
v_mfma_f32_16x16x32_bf16 a[192:195],v[100:103],a[144:147],a[192:195]
s_waitcnt vmcnt(13)
ds_read_b64_tr_b16 v[90:91],v2 offset:25696
v_add_u32 v2,0x1e000,v2
v_mfma_f32_16x16x32_bf16 a[192:195],v[104:107],a[160:163],a[192:195]
s_waitcnt vmcnt(12)
v_mfma_f32_16x16x32_bf16 a[192:195],v[108:111],a[176:179],a[192:195]
s_waitcnt vmcnt(11)
v_mfma_f32_16x16x32_bf16 a[196:199],v[108:111],a[180:183],a[196:199]
s_waitcnt vmcnt(10)
v_mfma_f32_16x16x32_bf16 a[196:199],v[104:107],a[164:167],a[196:199]
s_waitcnt vmcnt(9)
v_mfma_f32_16x16x32_bf16 a[196:199],v[100:103],a[148:151],a[196:199]
s_waitcnt vmcnt(8)
v_mfma_f32_16x16x32_bf16 a[196:199],v[96:99],a[132:135],a[196:199]
s_waitcnt vmcnt(7)
v_mfma_f32_16x16x32_bf16 a[200:203],v[96:99],a[136:139],a[200:203]
s_waitcnt vmcnt(6)
v_mfma_f32_16x16x32_bf16 a[200:203],v[100:103],a[152:155],a[200:203]
s_waitcnt vmcnt(5)
v_mfma_f32_16x16x32_bf16 a[200:203],v[104:107],a[168:171],a[200:203]
s_waitcnt vmcnt(4)
v_mfma_f32_16x16x32_bf16 a[200:203],v[108:111],a[184:187],a[200:203]
s_waitcnt vmcnt(3)
v_accvgpr_read_b32 v16,a192
v_accvgpr_read_b32 v17,a193
v_mfma_f32_16x16x32_bf16 a[204:207],v[108:111],a[188:191],a[204:207]
s_waitcnt vmcnt(2)
v_accvgpr_read_b32 v18,a194
v_accvgpr_read_b32 v19,a195
v_mfma_f32_16x16x32_bf16 a[204:207],v[104:107],a[172:175],a[204:207]
s_waitcnt vmcnt(1)
v_cvt_pk_bf16_f32 v16,v16,v17
v_cvt_pk_bf16_f32 v17,v18,v19
v_mfma_f32_16x16x32_bf16 a[204:207],v[100:103],a[156:159],a[204:207]
s_waitcnt vmcnt(0)
v_accvgpr_read_b32 v8,a196
v_accvgpr_read_b32 v9,a197
v_mfma_f32_16x16x32_bf16 a[204:207],v[96:99],a[140:143],a[204:207]
s_waitcnt lgkmcnt(0)
v_accvgpr_read_b32 v10,a198
v_accvgpr_read_b32 v11,a199
v_mfma_f32_16x16x32_bf16 a[208:211],v[112:115],a[128:131],a[208:211]
v_cvt_pk_bf16_f32 v8,v8,v9
v_cvt_pk_bf16_f32 v9,v10,v11
v_mfma_f32_16x16x32_bf16 a[208:211],v[116:119],a[144:147],a[208:211]
ds_read_b64_tr_b16 v[96:97],v1 offset:64
ds_read_b64_tr_b16 v[98:99],v1 offset:5184
v_mfma_f32_16x16x32_bf16 a[208:211],v[120:123],a[160:163],a[208:211]
ds_read_b64_tr_b16 v[100:101],v1 offset:10304
v_mfma_f32_16x16x32_bf16 a[208:211],v[124:127],a[176:179],a[208:211]
ds_read_b64_tr_b16 v[102:103],v1 offset:15424
v_mfma_f32_16x16x32_bf16 a[212:215],v[124:127],a[180:183],a[212:215]
ds_read_b64_tr_b16 v[104:105],v1 offset:20544
v_mfma_f32_16x16x32_bf16 a[212:215],v[120:123],a[164:167],a[212:215]
ds_read_b64_tr_b16 v[106:107],v1 offset:25664
ds_read_b64_tr_b16 v[108:109],v1 offset:30784
v_mfma_f32_16x16x32_bf16 a[212:215],v[116:119],a[148:151],a[212:215]
ds_read_b64_tr_b16 v[110:111],v1 offset:35904
v_add_u32 v1,0xfffe2000,v1
v_mfma_f32_16x16x32_bf16 a[212:215],v[112:115],a[132:135],a[212:215]
v_accvgpr_read_b32 v20,a208
v_accvgpr_read_b32 v21,a209
v_mfma_f32_16x16x32_bf16 a[216:219],v[112:115],a[136:139],a[216:219]
v_accvgpr_read_b32 v22,a210
v_accvgpr_read_b32 v23,a211
v_mfma_f32_16x16x32_bf16 a[216:219],v[116:119],a[152:155],a[216:219]
v_cvt_pk_bf16_f32 v18,v20,v21
v_cvt_pk_bf16_f32 v19,v22,v23
v_mfma_f32_16x16x32_bf16 a[216:219],v[120:123],a[168:171],a[216:219]
buffer_store_dwordx4 v[16:19],v6,s[36:39],s74 offen sc1
v_mfma_f32_16x16x32_bf16 a[216:219],v[124:127],a[184:187],a[216:219]
v_accvgpr_read_b32 v12,a212
v_accvgpr_read_b32 v13,a213
v_mfma_f32_16x16x32_bf16 a[220:223],v[124:127],a[188:191],a[220:223]
v_accvgpr_read_b32 v14,a214
v_accvgpr_read_b32 v15,a215
v_mfma_f32_16x16x32_bf16 a[220:223],v[120:123],a[172:175],a[220:223]
v_cvt_pk_bf16_f32 v10,v12,v13
v_cvt_pk_bf16_f32 v11,v14,v15
v_mfma_f32_16x16x32_bf16 a[220:223],v[116:119],a[156:159],a[220:223]
v_accvgpr_read_b32 v16,a200
v_accvgpr_read_b32 v17,a201
v_mfma_f32_16x16x32_bf16 a[220:223],v[112:115],a[140:143],a[220:223]
v_accvgpr_read_b32 v18,a202
v_accvgpr_read_b32 v19,a203
v_mfma_f32_16x16x32_bf16 a[224:227],v[64:67],v[32:35],a[224:227]
v_accvgpr_read_b32 v20,a216
v_accvgpr_read_b32 v21,a217
v_mfma_f32_16x16x32_bf16 a[224:227],v[68:71],v[36:39],a[224:227]
v_accvgpr_read_b32 v22,a218
v_accvgpr_read_b32 v23,a219
v_mfma_f32_16x16x32_bf16 a[224:227],v[72:75],v[40:43],a[224:227]
v_cvt_pk_bf16_f32 v16,v16,v17
v_cvt_pk_bf16_f32 v17,v18,v19
v_mfma_f32_16x16x32_bf16 a[224:227],v[76:79],v[44:47],a[224:227]
v_cvt_pk_bf16_f32 v18,v20,v21
v_cvt_pk_bf16_f32 v19,v22,v23
v_mfma_f32_16x16x32_bf16 a[228:231],v[76:79],v[180:183],a[228:231]
ds_read_b64_tr_b16 v[112:113],v2 offset:96
ds_read_b64_tr_b16 v[114:115],v2 offset:5216
v_mfma_f32_16x16x32_bf16 a[228:231],v[72:75],v[164:167],a[228:231]
buffer_store_dwordx4 v[8:11],v6,s[36:39],s74 offen offset:1024 sc1
v_mfma_f32_16x16x32_bf16 a[228:231],v[68:71],v[148:151],a[228:231]
ds_read_b64_tr_b16 v[116:117],v2 offset:10336
ds_read_b64_tr_b16 v[118:119],v2 offset:15456
v_mfma_f32_16x16x32_bf16 a[228:231],v[64:67],v[132:135],a[228:231]
ds_read_b64_tr_b16 v[120:121],v2 offset:20576
v_accvgpr_read_b32 v8,a204
v_mfma_f32_16x16x32_bf16 a[232:235],v[64:67],v[196:199],a[232:235]
ds_read_b64_tr_b16 v[122:123],v2 offset:25696
v_accvgpr_read_b32 v9,a205
v_mfma_f32_16x16x32_bf16 a[232:235],v[68:71],v[212:215],a[232:235]
ds_read_b64_tr_b16 v[124:125],v2 offset:30816
v_accvgpr_read_b32 v10,a206
v_mfma_f32_16x16x32_bf16 a[232:235],v[72:75],v[228:231],a[232:235]
ds_read_b64_tr_b16 v[126:127],v2 offset:35936
v_accvgpr_read_b32 v11,a207
v_mfma_f32_16x16x32_bf16 a[232:235],v[76:79],v[244:247],a[232:235]
v_accvgpr_read_b32 v12,a220
v_accvgpr_read_b32 v13,a221
v_mfma_f32_16x16x32_bf16 a[236:239],v[76:79],v[188:191],a[236:239]
v_accvgpr_read_b32 v14,a222
v_accvgpr_read_b32 v15,a223
v_mfma_f32_16x16x32_bf16 a[236:239],v[72:75],v[172:175],a[236:239]
v_cvt_pk_bf16_f32 v8,v8,v9
v_cvt_pk_bf16_f32 v9,v10,v11
v_mfma_f32_16x16x32_bf16 a[236:239],v[68:71],v[156:159],a[236:239]
v_cvt_pk_bf16_f32 v10,v12,v13
v_cvt_pk_bf16_f32 v11,v14,v15
v_mfma_f32_16x16x32_bf16 a[236:239],v[64:67],v[140:143],a[236:239]
ds_read_b64_tr_b16 v[64:65],v1 offset:128
ds_read_b64_tr_b16 v[66:67],v1 offset:5248
v_mfma_f32_16x16x32_bf16 a[240:243],v[80:83],v[32:35],a[240:243]
buffer_store_dwordx4 v[16:19],v6,s[36:39],s74 offen offset:2048 sc1
v_mfma_f32_16x16x32_bf16 a[240:243],v[84:87],v[36:39],a[240:243]
ds_read_b64_tr_b16 v[68:69],v1 offset:10368
ds_read_b64_tr_b16 v[70:71],v1 offset:15488
v_mfma_f32_16x16x32_bf16 a[240:243],v[88:91],v[40:43],a[240:243]
ds_read_b64_tr_b16 v[72:73],v1 offset:20608
v_add_u32 v2,0xfffe2000,v2
v_mfma_f32_16x16x32_bf16 a[240:243],v[92:95],v[44:47],a[240:243]
ds_read_b64_tr_b16 v[74:75],v1 offset:25728
v_mfma_f32_16x16x32_bf16 a[244:247],v[92:95],v[180:183],a[244:247]
ds_read_b64_tr_b16 v[76:77],v1 offset:30848
v_mfma_f32_16x16x32_bf16 a[244:247],v[88:91],v[164:167],a[244:247]
ds_read_b64_tr_b16 v[78:79],v1 offset:35968
v_add_u32 v1,0x1e000,v1
v_mfma_f32_16x16x32_bf16 a[244:247],v[84:87],v[148:151],a[244:247]
v_mfma_f32_16x16x32_bf16 a[244:247],v[80:83],v[132:135],a[244:247]
v_mfma_f32_16x16x32_bf16 a[248:251],v[80:83],v[196:199],a[248:251]
v_mfma_f32_16x16x32_bf16 a[248:251],v[84:87],v[212:215],a[248:251]
v_mfma_f32_16x16x32_bf16 a[248:251],v[88:91],v[228:231],a[248:251]
v_mfma_f32_16x16x32_bf16 a[248:251],v[92:95],v[244:247],a[248:251]
buffer_store_dwordx4 v[8:11],v28,s[36:39],s74 offen offset:3072 sc1
v_mfma_f32_16x16x32_bf16 a[252:255],v[92:95],v[188:191],a[252:255]
ds_read_b64_tr_b16 v[92:93],v2 offset:30880
ds_read_b64_tr_b16 v[94:95],v2 offset:36000
v_mfma_f32_16x16x32_bf16 a[252:255],v[88:91],v[172:175],a[252:255]
ds_read_b64_tr_b16 v[88:89],v2 offset:20640
v_mfma_f32_16x16x32_bf16 a[252:255],v[84:87],v[156:159],a[252:255]
ds_read_b64_tr_b16 v[84:85],v2 offset:10400
v_mfma_f32_16x16x32_bf16 a[252:255],v[80:83],v[140:143],a[252:255]
s_waitcnt lgkmcnt(4)
ds_read_b64_tr_b16 v[80:81],v2 offset:160
v_mfma_f32_16x16x32_bf16 a[224:227],v[96:99],a[128:131],a[224:227]
ds_read_b64_tr_b16 v[82:83],v2 offset:5280
ds_read_b64_tr_b16 v[86:87],v2 offset:15520
v_mfma_f32_16x16x32_bf16 a[224:227],v[100:103],a[144:147],a[224:227]
ds_read_b64_tr_b16 v[90:91],v2 offset:25760
v_add_u32 v2,0x1e000,v2
v_mfma_f32_16x16x32_bf16 a[224:227],v[104:107],a[160:163],a[224:227]
v_mfma_f32_16x16x32_bf16 a[224:227],v[108:111],a[176:179],a[224:227]
v_mfma_f32_16x16x32_bf16 a[228:231],v[108:111],a[180:183],a[228:231]
v_mfma_f32_16x16x32_bf16 a[228:231],v[104:107],a[164:167],a[228:231]
v_mfma_f32_16x16x32_bf16 a[228:231],v[100:103],a[148:151],a[228:231]
v_mfma_f32_16x16x32_bf16 a[228:231],v[96:99],a[132:135],a[228:231]
v_mfma_f32_16x16x32_bf16 a[232:235],v[96:99],a[136:139],a[232:235]
v_mfma_f32_16x16x32_bf16 a[232:235],v[100:103],a[152:155],a[232:235]
v_mfma_f32_16x16x32_bf16 a[232:235],v[104:107],a[168:171],a[232:235]
v_mfma_f32_16x16x32_bf16 a[232:235],v[108:111],a[184:187],a[232:235]
v_accvgpr_read_b32 v16,a224
v_accvgpr_read_b32 v17,a225
v_mfma_f32_16x16x32_bf16 a[236:239],v[108:111],a[188:191],a[236:239]
v_accvgpr_read_b32 v18,a226
v_accvgpr_read_b32 v19,a227
v_mfma_f32_16x16x32_bf16 a[236:239],v[104:107],a[172:175],a[236:239]
v_cvt_pk_bf16_f32 v16,v16,v17
v_cvt_pk_bf16_f32 v17,v18,v19
v_mfma_f32_16x16x32_bf16 a[236:239],v[100:103],a[156:159],a[236:239]
v_accvgpr_read_b32 v8,a228
v_accvgpr_read_b32 v9,a229
v_mfma_f32_16x16x32_bf16 a[236:239],v[96:99],a[140:143],a[236:239]
v_accvgpr_read_b32 v10,a230
v_accvgpr_read_b32 v11,a231
v_mfma_f32_16x16x32_bf16 a[240:243],v[112:115],a[128:131],a[240:243]
v_cvt_pk_bf16_f32 v8,v8,v9
v_cvt_pk_bf16_f32 v9,v10,v11
v_mfma_f32_16x16x32_bf16 a[240:243],v[116:119],a[144:147],a[240:243]
ds_read_b64_tr_b16 v[96:97],v1 offset:128
ds_read_b64_tr_b16 v[98:99],v1 offset:5248
v_mfma_f32_16x16x32_bf16 a[240:243],v[120:123],a[160:163],a[240:243]
ds_read_b64_tr_b16 v[100:101],v1 offset:10368
v_mfma_f32_16x16x32_bf16 a[240:243],v[124:127],a[176:179],a[240:243]
ds_read_b64_tr_b16 v[102:103],v1 offset:15488
v_mfma_f32_16x16x32_bf16 a[244:247],v[124:127],a[180:183],a[244:247]
ds_read_b64_tr_b16 v[104:105],v1 offset:20608
v_mfma_f32_16x16x32_bf16 a[244:247],v[120:123],a[164:167],a[244:247]
ds_read_b64_tr_b16 v[106:107],v1 offset:25728
ds_read_b64_tr_b16 v[108:109],v1 offset:30848
v_mfma_f32_16x16x32_bf16 a[244:247],v[116:119],a[148:151],a[244:247]
)A"
R"A(ds_read_b64_tr_b16 v[110:111],v1 offset:35968
v_add_u32 v1,0xfffe2000,v1
v_mfma_f32_16x16x32_bf16 a[244:247],v[112:115],a[132:135],a[244:247]
v_accvgpr_read_b32 v20,a240
v_accvgpr_read_b32 v21,a241
v_mfma_f32_16x16x32_bf16 a[248:251],v[112:115],a[136:139],a[248:251]
v_accvgpr_read_b32 v22,a242
v_accvgpr_read_b32 v23,a243
v_mfma_f32_16x16x32_bf16 a[248:251],v[116:119],a[152:155],a[248:251]
v_cvt_pk_bf16_f32 v18,v20,v21
v_cvt_pk_bf16_f32 v19,v22,v23
v_mfma_f32_16x16x32_bf16 a[248:251],v[120:123],a[168:171],a[248:251]
buffer_store_dwordx4 v[16:19],v6,s[36:39],s75 offen sc1
v_mfma_f32_16x16x32_bf16 a[248:251],v[124:127],a[184:187],a[248:251]
v_accvgpr_read_b32 v12,a244
v_accvgpr_read_b32 v13,a245
v_mfma_f32_16x16x32_bf16 a[252:255],v[124:127],a[188:191],a[252:255]
v_accvgpr_read_b32 v14,a246
v_accvgpr_read_b32 v15,a247
v_mfma_f32_16x16x32_bf16 a[252:255],v[120:123],a[172:175],a[252:255]
v_cvt_pk_bf16_f32 v10,v12,v13
v_cvt_pk_bf16_f32 v11,v14,v15
v_mfma_f32_16x16x32_bf16 a[252:255],v[116:119],a[156:159],a[252:255]
v_accvgpr_read_b32 v16,a232
v_accvgpr_read_b32 v17,a233
v_mfma_f32_16x16x32_bf16 a[252:255],v[112:115],a[140:143],a[252:255]
v_accvgpr_read_b32 v18,a234
v_accvgpr_read_b32 v19,a235
v_mfma_f32_16x16x32_bf16 a[0:3],v[64:67],v[32:35],a[0:3]
v_accvgpr_read_b32 v20,a248
v_accvgpr_read_b32 v21,a249
v_mfma_f32_16x16x32_bf16 a[0:3],v[68:71],v[36:39],a[0:3]
v_accvgpr_read_b32 v22,a250
v_accvgpr_read_b32 v23,a251
v_mfma_f32_16x16x32_bf16 a[0:3],v[72:75],v[40:43],a[0:3]
v_cvt_pk_bf16_f32 v16,v16,v17
v_cvt_pk_bf16_f32 v17,v18,v19
v_mfma_f32_16x16x32_bf16 a[0:3],v[76:79],v[44:47],a[0:3]
v_cvt_pk_bf16_f32 v18,v20,v21
v_cvt_pk_bf16_f32 v19,v22,v23
v_mfma_f32_16x16x32_bf16 a[4:7],v[76:79],v[180:183],a[4:7]
ds_read_b64_tr_b16 v[112:113],v2 offset:160
ds_read_b64_tr_b16 v[114:115],v2 offset:5280
v_mfma_f32_16x16x32_bf16 a[4:7],v[72:75],v[164:167],a[4:7]
buffer_store_dwordx4 v[8:11],v6,s[36:39],s75 offen offset:1024 sc1
v_mfma_f32_16x16x32_bf16 a[4:7],v[68:71],v[148:151],a[4:7]
ds_read_b64_tr_b16 v[116:117],v2 offset:10400
ds_read_b64_tr_b16 v[118:119],v2 offset:15520
v_mfma_f32_16x16x32_bf16 a[4:7],v[64:67],v[132:135],a[4:7]
ds_read_b64_tr_b16 v[120:121],v2 offset:20640
v_accvgpr_read_b32 v8,a236
v_mfma_f32_16x16x32_bf16 a[8:11],v[64:67],v[196:199],a[8:11]
ds_read_b64_tr_b16 v[122:123],v2 offset:25760
v_accvgpr_read_b32 v9,a237
v_mfma_f32_16x16x32_bf16 a[8:11],v[68:71],v[212:215],a[8:11]
ds_read_b64_tr_b16 v[124:125],v2 offset:30880
v_accvgpr_read_b32 v10,a238
v_mfma_f32_16x16x32_bf16 a[8:11],v[72:75],v[228:231],a[8:11]
ds_read_b64_tr_b16 v[126:127],v2 offset:36000
v_accvgpr_read_b32 v11,a239
v_mfma_f32_16x16x32_bf16 a[8:11],v[76:79],v[244:247],a[8:11]
v_accvgpr_read_b32 v12,a252
v_accvgpr_read_b32 v13,a253
v_mfma_f32_16x16x32_bf16 a[12:15],v[76:79],v[188:191],a[12:15]
v_accvgpr_read_b32 v14,a254
v_accvgpr_read_b32 v15,a255
v_mfma_f32_16x16x32_bf16 a[12:15],v[72:75],v[172:175],a[12:15]
v_cvt_pk_bf16_f32 v8,v8,v9
v_cvt_pk_bf16_f32 v9,v10,v11
v_mfma_f32_16x16x32_bf16 a[12:15],v[68:71],v[156:159],a[12:15]
v_cvt_pk_bf16_f32 v10,v12,v13
v_cvt_pk_bf16_f32 v11,v14,v15
v_mfma_f32_16x16x32_bf16 a[12:15],v[64:67],v[140:143],a[12:15]
s_waitcnt lgkmcnt(3)
ds_read_b64_tr_b16 v[64:65],v1 offset:192
ds_read_b64_tr_b16 v[66:67],v1 offset:5312
v_mfma_f32_16x16x32_bf16 a[16:19],v[80:83],v[32:35],a[16:19]
buffer_store_dwordx4 v[16:19],v6,s[36:39],s75 offen offset:2048 sc1
v_mfma_f32_16x16x32_bf16 a[16:19],v[84:87],v[36:39],a[16:19]
ds_read_b64_tr_b16 v[68:69],v1 offset:10432
ds_read_b64_tr_b16 v[70:71],v1 offset:15552
v_mfma_f32_16x16x32_bf16 a[16:19],v[88:91],v[40:43],a[16:19]
ds_read_b64_tr_b16 v[72:73],v1 offset:20672
v_add_u32 v2,0xfffe2000,v2
v_mfma_f32_16x16x32_bf16 a[16:19],v[92:95],v[44:47],a[16:19]
ds_read_b64_tr_b16 v[74:75],v1 offset:25792
v_mfma_f32_16x16x32_bf16 a[20:23],v[92:95],v[180:183],a[20:23]
ds_read_b64_tr_b16 v[76:77],v1 offset:30912
v_mfma_f32_16x16x32_bf16 a[20:23],v[88:91],v[164:167],a[20:23]
ds_read_b64_tr_b16 v[78:79],v1 offset:36032
v_add_u32 v1,0x1e000,v1
v_mfma_f32_16x16x32_bf16 a[20:23],v[84:87],v[148:151],a[20:23]
v_mfma_f32_16x16x32_bf16 a[20:23],v[80:83],v[132:135],a[20:23]
v_mfma_f32_16x16x32_bf16 a[24:27],v[80:83],v[196:199],a[24:27]
v_mfma_f32_16x16x32_bf16 a[24:27],v[84:87],v[212:215],a[24:27]
v_mfma_f32_16x16x32_bf16 a[24:27],v[88:91],v[228:231],a[24:27]
v_mfma_f32_16x16x32_bf16 a[24:27],v[92:95],v[244:247],a[24:27]
buffer_store_dwordx4 v[8:11],v28,s[36:39],s75 offen offset:3072 sc1
v_mfma_f32_16x16x32_bf16 a[28:31],v[92:95],v[188:191],a[28:31]
ds_read_b64_tr_b16 v[92:93],v2 offset:30944
ds_read_b64_tr_b16 v[94:95],v2 offset:36064
v_mfma_f32_16x16x32_bf16 a[28:31],v[88:91],v[172:175],a[28:31]
ds_read_b64_tr_b16 v[88:89],v2 offset:20704
v_mfma_f32_16x16x32_bf16 a[28:31],v[84:87],v[156:159],a[28:31]
ds_read_b64_tr_b16 v[84:85],v2 offset:10464
v_mfma_f32_16x16x32_bf16 a[28:31],v[80:83],v[140:143],a[28:31]
ds_read_b64_tr_b16 v[80:81],v2 offset:224
v_mfma_f32_16x16x32_bf16 a[0:3],v[96:99],a[128:131],a[0:3]
ds_read_b64_tr_b16 v[82:83],v2 offset:5344
ds_read_b64_tr_b16 v[86:87],v2 offset:15584
v_mfma_f32_16x16x32_bf16 a[0:3],v[100:103],a[144:147],a[0:3]
ds_read_b64_tr_b16 v[90:91],v2 offset:25824
v_add_u32 v2,0x1e000,v2
v_mfma_f32_16x16x32_bf16 a[0:3],v[104:107],a[160:163],a[0:3]
v_mfma_f32_16x16x32_bf16 a[0:3],v[108:111],a[176:179],a[0:3]
v_mfma_f32_16x16x32_bf16 a[4:7],v[108:111],a[180:183],a[4:7]
v_mfma_f32_16x16x32_bf16 a[4:7],v[104:107],a[164:167],a[4:7]
v_mfma_f32_16x16x32_bf16 a[4:7],v[100:103],a[148:151],a[4:7]
v_mfma_f32_16x16x32_bf16 a[4:7],v[96:99],a[132:135],a[4:7]
v_mfma_f32_16x16x32_bf16 a[8:11],v[96:99],a[136:139],a[8:11]
v_mfma_f32_16x16x32_bf16 a[8:11],v[100:103],a[152:155],a[8:11]
v_mfma_f32_16x16x32_bf16 a[8:11],v[104:107],a[168:171],a[8:11]
v_mfma_f32_16x16x32_bf16 a[8:11],v[108:111],a[184:187],a[8:11]
v_accvgpr_read_b32 v16,a0
v_accvgpr_read_b32 v17,a1
v_mfma_f32_16x16x32_bf16 a[12:15],v[108:111],a[188:191],a[12:15]
v_accvgpr_read_b32 v18,a2
v_accvgpr_read_b32 v19,a3
v_mfma_f32_16x16x32_bf16 a[12:15],v[104:107],a[172:175],a[12:15]
v_cvt_pk_bf16_f32 v16,v16,v17
v_cvt_pk_bf16_f32 v17,v18,v19
v_mfma_f32_16x16x32_bf16 a[12:15],v[100:103],a[156:159],a[12:15]
v_accvgpr_read_b32 v8,a4
v_accvgpr_read_b32 v9,a5
v_mfma_f32_16x16x32_bf16 a[12:15],v[96:99],a[140:143],a[12:15]
v_accvgpr_read_b32 v10,a6
v_accvgpr_read_b32 v11,a7
v_mfma_f32_16x16x32_bf16 a[16:19],v[112:115],a[128:131],a[16:19]
v_cvt_pk_bf16_f32 v8,v8,v9
v_cvt_pk_bf16_f32 v9,v10,v11
v_mfma_f32_16x16x32_bf16 a[16:19],v[116:119],a[144:147],a[16:19]
s_waitcnt lgkmcnt(0)
ds_read_b64_tr_b16 v[96:97],v1 offset:192
ds_read_b64_tr_b16 v[98:99],v1 offset:5312
v_mfma_f32_16x16x32_bf16 a[16:19],v[120:123],a[160:163],a[16:19]
ds_read_b64_tr_b16 v[100:101],v1 offset:10432
v_mfma_f32_16x16x32_bf16 a[16:19],v[124:127],a[176:179],a[16:19]
ds_read_b64_tr_b16 v[102:103],v1 offset:15552
v_mfma_f32_16x16x32_bf16 a[20:23],v[124:127],a[180:183],a[20:23]
ds_read_b64_tr_b16 v[104:105],v1 offset:20672
v_mfma_f32_16x16x32_bf16 a[20:23],v[120:123],a[164:167],a[20:23]
ds_read_b64_tr_b16 v[106:107],v1 offset:25792
ds_read_b64_tr_b16 v[108:109],v1 offset:30912
v_mfma_f32_16x16x32_bf16 a[20:23],v[116:119],a[148:151],a[20:23]
ds_read_b64_tr_b16 v[110:111],v1 offset:36032
v_add_u32 v1,0xfffec000,v1
v_mfma_f32_16x16x32_bf16 a[20:23],v[112:115],a[132:135],a[20:23]
v_accvgpr_read_b32 v20,a16
v_accvgpr_read_b32 v21,a17
v_mfma_f32_16x16x32_bf16 a[24:27],v[112:115],a[136:139],a[24:27]
v_accvgpr_read_b32 v22,a18
v_accvgpr_read_b32 v23,a19
v_mfma_f32_16x16x32_bf16 a[24:27],v[116:119],a[152:155],a[24:27]
v_cvt_pk_bf16_f32 v18,v20,v21
v_cvt_pk_bf16_f32 v19,v22,v23
v_mfma_f32_16x16x32_bf16 a[24:27],v[120:123],a[168:171],a[24:27]
buffer_store_dwordx4 v[16:19],v6,s[36:39],s76 offen sc1
v_mfma_f32_16x16x32_bf16 a[24:27],v[124:127],a[184:187],a[24:27]
v_accvgpr_read_b32 v12,a20
v_accvgpr_read_b32 v13,a21
v_mfma_f32_16x16x32_bf16 a[28:31],v[124:127],a[188:191],a[28:31]
v_accvgpr_read_b32 v14,a22
v_accvgpr_read_b32 v15,a23
s_nop 0
L_pd18_%=:
s_cbranch_execz L_trtg2F_%=
v_mfma_f32_16x16x32_bf16 a[28:31],v[120:123],a[172:175],a[28:31]
v_cvt_pk_bf16_f32 v10,v12,v13
v_cvt_pk_bf16_f32 v11,v14,v15
v_mfma_f32_16x16x32_bf16 a[28:31],v[116:119],a[156:159],a[28:31]
v_accvgpr_read_b32 v16,a8
v_accvgpr_read_b32 v17,a9
v_mfma_f32_16x16x32_bf16 a[28:31],v[112:115],a[140:143],a[28:31]
v_accvgpr_read_b32 v18,a10
v_accvgpr_read_b32 v19,a11
v_mfma_f32_16x16x32_bf16 v[128:131],v[64:67],v[32:35],v[128:131]
v_accvgpr_read_b32 v20,a24
v_accvgpr_read_b32 v21,a25
v_mfma_f32_16x16x32_bf16 v[128:131],v[68:71],v[36:39],v[128:131]
v_accvgpr_read_b32 v22,a26
v_accvgpr_read_b32 v23,a27
v_mfma_f32_16x16x32_bf16 v[128:131],v[72:75],v[40:43],v[128:131]
v_cvt_pk_bf16_f32 v16,v16,v17
v_cvt_pk_bf16_f32 v17,v18,v19
v_mfma_f32_16x16x32_bf16 v[128:131],v[76:79],v[44:47],v[128:131]
v_cvt_pk_bf16_f32 v18,v20,v21
v_cvt_pk_bf16_f32 v19,v22,v23
v_mfma_f32_16x16x32_bf16 v[136:139],v[76:79],v[180:183],v[136:139]
ds_read_b64_tr_b16 v[112:113],v2 offset:224
ds_read_b64_tr_b16 v[114:115],v2 offset:5344
v_mfma_f32_16x16x32_bf16 v[136:139],v[72:75],v[164:167],v[136:139]
buffer_store_dwordx4 v[8:11],v6,s[36:39],s76 offen offset:1024 sc1
v_mfma_f32_16x16x32_bf16 v[136:139],v[68:71],v[148:151],v[136:139]
ds_read_b64_tr_b16 v[116:117],v2 offset:10464
ds_read_b64_tr_b16 v[118:119],v2 offset:15584
v_mfma_f32_16x16x32_bf16 v[136:139],v[64:67],v[132:135],v[136:139]
ds_read_b64_tr_b16 v[120:121],v2 offset:20704
v_accvgpr_read_b32 v8,a12
v_mfma_f32_16x16x32_bf16 v[144:147],v[64:67],v[196:199],v[144:147]
ds_read_b64_tr_b16 v[122:123],v2 offset:25824
v_accvgpr_read_b32 v9,a13
v_mfma_f32_16x16x32_bf16 v[144:147],v[68:71],v[212:215],v[144:147]
ds_read_b64_tr_b16 v[124:125],v2 offset:30944
v_accvgpr_read_b32 v10,a14
v_mfma_f32_16x16x32_bf16 v[144:147],v[72:75],v[228:231],v[144:147]
ds_read_b64_tr_b16 v[126:127],v2 offset:36064
v_accvgpr_read_b32 v11,a15
v_mfma_f32_16x16x32_bf16 v[144:147],v[76:79],v[244:247],v[144:147]
v_accvgpr_read_b32 v12,a28
v_accvgpr_read_b32 v13,a29
v_mfma_f32_16x16x32_bf16 v[152:155],v[76:79],v[188:191],v[152:155]
v_accvgpr_read_b32 v14,a30
v_accvgpr_read_b32 v15,a31
v_mfma_f32_16x16x32_bf16 v[152:155],v[72:75],v[172:175],v[152:155]
v_cvt_pk_bf16_f32 v8,v8,v9
v_cvt_pk_bf16_f32 v9,v10,v11
v_mfma_f32_16x16x32_bf16 v[152:155],v[68:71],v[156:159],v[152:155]
v_cvt_pk_bf16_f32 v10,v12,v13
v_cvt_pk_bf16_f32 v11,v14,v15
v_mfma_f32_16x16x32_bf16 v[152:155],v[64:67],v[140:143],v[152:155]
ds_read_b64_tr_b16 v[64:65],v1 offset:256
ds_read_b64_tr_b16 v[66:67],v1 offset:5376
v_mfma_f32_16x16x32_bf16 v[160:163],v[80:83],v[32:35],v[160:163]
buffer_store_dwordx4 v[16:19],v6,s[36:39],s76 offen offset:2048 sc1
v_mfma_f32_16x16x32_bf16 v[160:163],v[84:87],v[36:39],v[160:163]
ds_read_b64_tr_b16 v[68:69],v1 offset:10496
ds_read_b64_tr_b16 v[70:71],v1 offset:15616
v_mfma_f32_16x16x32_bf16 v[160:163],v[88:91],v[40:43],v[160:163]
ds_read_b64_tr_b16 v[72:73],v1 offset:20736
v_add_u32 v2,0xfffec000,v2
v_mfma_f32_16x16x32_bf16 v[160:163],v[92:95],v[44:47],v[160:163]
ds_read_b64_tr_b16 v[74:75],v1 offset:25856
v_mov_b32 v20,s52
v_mfma_f32_16x16x32_bf16 v[168:171],v[92:95],v[180:183],v[168:171]
ds_read_b64_tr_b16 v[76:77],v1 offset:30976
v_mov_b32 v21,s53
v_mfma_f32_16x16x32_bf16 v[168:171],v[88:91],v[164:167],v[168:171]
ds_read_b64_tr_b16 v[78:79],v1 offset:36096
v_add_u32 v1,0xffff6000,v1
v_mfma_f32_16x16x32_bf16 v[168:171],v[84:87],v[148:151],v[168:171]
v_lshlrev_b32 v22,20,v6
v_mfma_f32_16x16x32_bf16 v[168:171],v[80:83],v[132:135],v[168:171]
v_mfma_f32_16x16x32_bf16 v[176:179],v[80:83],v[196:199],v[176:179]
v_mfma_f32_16x16x32_bf16 v[176:179],v[84:87],v[212:215],v[176:179]
v_mfma_f32_16x16x32_bf16 v[176:179],v[88:91],v[228:231],v[176:179]
v_mfma_f32_16x16x32_bf16 v[176:179],v[92:95],v[244:247],v[176:179]
buffer_store_dwordx4 v[8:11],v28,s[36:39],s76 offen offset:3072 sc1
v_mfma_f32_16x16x32_bf16 v[184:187],v[92:95],v[188:191],v[184:187]
ds_read_b64_tr_b16 v[92:93],v2 offset:31008
ds_read_b64_tr_b16 v[94:95],v2 offset:36128
)A"
R"A(v_mfma_f32_16x16x32_bf16 v[184:187],v[88:91],v[172:175],v[184:187]
ds_read_b64_tr_b16 v[88:89],v2 offset:20768
v_mfma_f32_16x16x32_bf16 v[184:187],v[84:87],v[156:159],v[184:187]
ds_read_b64_tr_b16 v[84:85],v2 offset:10528
v_mfma_f32_16x16x32_bf16 v[184:187],v[80:83],v[140:143],v[184:187]
s_waitcnt lgkmcnt(4)
ds_read_b64_tr_b16 v[80:81],v2 offset:288
v_mfma_f32_16x16x32_bf16 v[128:131],v[96:99],a[128:131],v[128:131]
ds_read_b64_tr_b16 v[82:83],v2 offset:5408
ds_read_b64_tr_b16 v[86:87],v2 offset:15648
v_mfma_f32_16x16x32_bf16 v[128:131],v[100:103],a[144:147],v[128:131]
ds_read_b64_tr_b16 v[90:91],v2 offset:25888
v_add_u32 v2,0xffff6000,v2
v_mfma_f32_16x16x32_bf16 v[128:131],v[104:107],a[160:163],v[128:131]
v_mfma_f32_16x16x32_bf16 v[128:131],v[108:111],a[176:179],v[128:131]
v_mfma_f32_16x16x32_bf16 v[136:139],v[108:111],a[180:183],v[136:139]
v_mfma_f32_16x16x32_bf16 v[136:139],v[104:107],a[164:167],v[136:139]
v_mfma_f32_16x16x32_bf16 v[136:139],v[100:103],a[148:151],v[136:139]
v_mfma_f32_16x16x32_bf16 v[136:139],v[96:99],a[132:135],v[136:139]
v_mfma_f32_16x16x32_bf16 v[144:147],v[96:99],a[136:139],v[144:147]
v_mfma_f32_16x16x32_bf16 v[144:147],v[100:103],a[152:155],v[144:147]
v_mfma_f32_16x16x32_bf16 v[144:147],v[104:107],a[168:171],v[144:147]
v_mfma_f32_16x16x32_bf16 v[144:147],v[108:111],a[184:187],v[144:147]
v_cvt_pk_bf16_f32 v128,v128,v129
v_cvt_pk_bf16_f32 v129,v130,v131
v_mfma_f32_16x16x32_bf16 v[152:155],v[108:111],a[188:191],v[152:155]
ds_read_b64_tr_b16 v[108:109],v1 offset:30976
ds_read_b64_tr_b16 v[110:111],v1 offset:36096
v_mfma_f32_16x16x32_bf16 v[152:155],v[104:107],a[172:175],v[152:155]
v_cvt_pk_bf16_f32 v136,v136,v137
v_cvt_pk_bf16_f32 v137,v138,v139
v_mfma_f32_16x16x32_bf16 v[152:155],v[100:103],a[156:159],v[152:155]
v_cvt_pk_bf16_f32 v144,v144,v145
v_cvt_pk_bf16_f32 v145,v146,v147
v_mfma_f32_16x16x32_bf16 v[152:155],v[96:99],a[140:143],v[152:155]
ds_read_b64_tr_b16 v[96:97],v1 offset:256
ds_read_b64_tr_b16 v[98:99],v1 offset:5376
v_mfma_f32_16x16x32_bf16 v[160:163],v[112:115],a[128:131],v[160:163]
ds_read_b64_tr_b16 v[100:101],v1 offset:10496
v_mfma_f32_16x16x32_bf16 v[160:163],v[116:119],a[144:147],v[160:163]
ds_read_b64_tr_b16 v[102:103],v1 offset:15616
v_mfma_f32_16x16x32_bf16 v[160:163],v[120:123],a[160:163],v[160:163]
ds_read_b64_tr_b16 v[104:105],v1 offset:20736
v_mfma_f32_16x16x32_bf16 v[160:163],v[124:127],a[176:179],v[160:163]
v_cvt_pk_bf16_f32 v152,v152,v153
v_cvt_pk_bf16_f32 v153,v154,v155
v_mfma_f32_16x16x32_bf16 v[168:171],v[124:127],a[180:183],v[168:171]
ds_read_b64_tr_b16 v[106:107],v1 offset:25856
v_add_u32 v1,0x1e000,v1
v_mfma_f32_16x16x32_bf16 v[168:171],v[120:123],a[164:167],v[168:171]
v_mfma_f32_16x16x32_bf16 v[168:171],v[116:119],a[148:151],v[168:171]
v_mfma_f32_16x16x32_bf16 v[168:171],v[112:115],a[132:135],v[168:171]
v_cvt_pk_bf16_f32 v130,v160,v161
v_cvt_pk_bf16_f32 v131,v162,v163
v_mfma_f32_16x16x32_bf16 v[176:179],v[112:115],a[136:139],v[176:179]
buffer_store_dwordx4 v[128:131],v6,s[36:39],s79 offen sc1
v_mfma_f32_16x16x32_bf16 v[176:179],v[116:119],a[152:155],v[176:179]
v_mfma_f32_16x16x32_bf16 v[176:179],v[120:123],a[168:171],v[176:179]
v_mfma_f32_16x16x32_bf16 v[176:179],v[124:127],a[184:187],v[176:179]
v_mfma_f32_16x16x32_bf16 v[184:187],v[124:127],a[188:191],v[184:187]
v_cvt_pk_bf16_f32 v138,v168,v169
v_cvt_pk_bf16_f32 v139,v170,v171
v_mfma_f32_16x16x32_bf16 v[184:187],v[120:123],a[172:175],v[184:187]
buffer_store_dwordx4 v[136:139],v6,s[36:39],s80 offen sc1
v_mfma_f32_16x16x32_bf16 v[184:187],v[116:119],a[156:159],v[184:187]
ds_read_b64_tr_b16 v[116:117],v2 offset:10528
ds_read_b64_tr_b16 v[118:119],v2 offset:15648
v_mfma_f32_16x16x32_bf16 v[184:187],v[112:115],a[140:143],v[184:187]
v_cvt_pk_bf16_f32 v146,v176,v177
v_cvt_pk_bf16_f32 v147,v178,v179
s_waitcnt vmcnt(6)
s_barrier
v_mfma_f32_16x16x32_bf16 v[192:195],v[64:67],a[68:71],v[192:195]
buffer_store_dwordx4 v[144:147],v6,s[36:39],s81 offen sc1
v_mfma_f32_16x16x32_bf16 v[192:195],v[68:71],a[76:79],v[192:195]
ds_read_b64_tr_b16 v[112:113],v2 offset:288
ds_read_b64_tr_b16 v[114:115],v2 offset:5408
v_mfma_f32_16x16x32_bf16 v[192:195],v[72:75],a[84:87],v[192:195]
buffer_store_dwordx2 v[58:59],v60,s[44:47],s18 offen offset:2312 sc1
v_mfma_f32_16x16x32_bf16 v[192:195],v[76:79],a[92:95],v[192:195]
v_cvt_pk_bf16_f32 v154,v184,v185
v_cvt_pk_bf16_f32 v155,v186,v187
v_mfma_f32_16x16x32_bf16 v[200:203],v[76:79],a[124:127],v[200:203]
buffer_store_dwordx4 v[152:155],v28,s[36:39],s82 offen sc1
v_mfma_f32_16x16x32_bf16 v[200:203],v[72:75],a[116:119],v[200:203]
ds_read_b64_tr_b16 v[120:121],v2 offset:20768
ds_read_b64_tr_b16 v[122:123],v2 offset:25888
v_mfma_f32_16x16x32_bf16 v[200:203],v[68:71],a[108:111],v[200:203]
ds_read_b64_tr_b16 v[124:125],v2 offset:31008
v_mfma_f32_16x16x32_bf16 v[200:203],v[64:67],a[100:103],v[200:203]
ds_read_b64_tr_b16 v[126:127],v2 offset:36128
v_add_u32 v2,0x1e000,v2
v_mfma_f32_16x16x32_bf16 v[208:211],v[64:67],a[64:67],v[208:211]
v_mfma_f32_16x16x32_bf16 v[208:211],v[68:71],a[72:75],v[208:211]
v_mfma_f32_16x16x32_bf16 v[208:211],v[72:75],a[80:83],v[208:211]
v_mfma_f32_16x16x32_bf16 v[208:211],v[76:79],a[88:91],v[208:211]
v_mfma_f32_16x16x32_bf16 v[216:219],v[76:79],a[120:123],v[216:219]
ds_read_b64_tr_b16 v[76:77],v1 offset:30976
ds_read_b64_tr_b16 v[78:79],v1 offset:36096
v_mfma_f32_16x16x32_bf16 v[216:219],v[72:75],a[112:115],v[216:219]
ds_read_b64_tr_b16 v[72:73],v1 offset:20736
v_mfma_f32_16x16x32_bf16 v[216:219],v[68:71],a[104:107],v[216:219]
ds_read_b64_tr_b16 v[68:69],v1 offset:10496
v_mfma_f32_16x16x32_bf16 v[216:219],v[64:67],a[96:99],v[216:219]
s_waitcnt lgkmcnt(4)
ds_read_b64_tr_b16 v[64:65],v1 offset:256
v_mfma_f32_16x16x32_bf16 v[224:227],v[80:83],a[68:71],v[224:227]
ds_read_b64_tr_b16 v[66:67],v1 offset:5376
ds_read_b64_tr_b16 v[70:71],v1 offset:15616
v_mfma_f32_16x16x32_bf16 v[224:227],v[84:87],a[76:79],v[224:227]
ds_read_b64_tr_b16 v[74:75],v1 offset:25856
v_mfma_f32_16x16x32_bf16 v[224:227],v[88:91],a[84:87],v[224:227]
v_mfma_f32_16x16x32_bf16 v[224:227],v[92:95],a[92:95],v[224:227]
v_mfma_f32_16x16x32_bf16 v[232:235],v[92:95],a[124:127],v[232:235]
v_mfma_f32_16x16x32_bf16 v[232:235],v[88:91],a[116:119],v[232:235]
v_mfma_f32_16x16x32_bf16 v[232:235],v[84:87],a[108:111],v[232:235]
v_mfma_f32_16x16x32_bf16 v[232:235],v[80:83],a[100:103],v[232:235]
v_mfma_f32_16x16x32_bf16 v[240:243],v[80:83],a[64:67],v[240:243]
v_mfma_f32_16x16x32_bf16 v[240:243],v[84:87],a[72:75],v[240:243]
v_mfma_f32_16x16x32_bf16 v[240:243],v[88:91],a[80:83],v[240:243]
v_mfma_f32_16x16x32_bf16 v[240:243],v[92:95],a[88:91],v[240:243]
v_mfma_f32_16x16x32_bf16 v[248:251],v[92:95],a[120:123],v[248:251]
ds_read_b64_tr_b16 v[92:93],v2 offset:31008
ds_read_b64_tr_b16 v[94:95],v2 offset:36128
v_mfma_f32_16x16x32_bf16 v[248:251],v[88:91],a[112:115],v[248:251]
ds_read_b64_tr_b16 v[88:89],v2 offset:20768
v_mfma_f32_16x16x32_bf16 v[248:251],v[84:87],a[104:107],v[248:251]
ds_read_b64_tr_b16 v[84:85],v2 offset:10528
v_mfma_f32_16x16x32_bf16 v[248:251],v[80:83],a[96:99],v[248:251]
ds_read_b64_tr_b16 v[80:81],v2 offset:288
v_mfma_f32_16x16x32_bf16 v[192:195],v[96:99],v[32:35],v[192:195]
ds_read_b64_tr_b16 v[82:83],v2 offset:5408
ds_read_b64_tr_b16 v[86:87],v2 offset:15648
v_mfma_f32_16x16x32_bf16 v[192:195],v[100:103],v[36:39],v[192:195]
ds_read_b64_tr_b16 v[90:91],v2 offset:25888
v_mfma_f32_16x16x32_bf16 v[192:195],v[104:107],v[40:43],v[192:195]
v_mfma_f32_16x16x32_bf16 v[192:195],v[108:111],v[44:47],v[192:195]
v_mfma_f32_16x16x32_bf16 v[200:203],v[108:111],v[180:183],v[200:203]
v_mfma_f32_16x16x32_bf16 v[200:203],v[104:107],v[164:167],v[200:203]
v_mfma_f32_16x16x32_bf16 v[200:203],v[100:103],v[148:151],v[200:203]
v_mfma_f32_16x16x32_bf16 v[200:203],v[96:99],v[132:135],v[200:203]
v_mfma_f32_16x16x32_bf16 v[208:211],v[96:99],v[196:199],v[208:211]
v_mfma_f32_16x16x32_bf16 v[208:211],v[100:103],v[212:215],v[208:211]
v_mfma_f32_16x16x32_bf16 v[208:211],v[104:107],v[228:231],v[208:211]
v_mfma_f32_16x16x32_bf16 v[208:211],v[108:111],v[244:247],v[208:211]
v_mfma_f32_16x16x32_bf16 v[216:219],v[108:111],v[188:191],v[216:219]
v_mfma_f32_16x16x32_bf16 v[216:219],v[104:107],v[172:175],v[216:219]
v_mfma_f32_16x16x32_bf16 v[216:219],v[100:103],v[156:159],v[216:219]
v_mfma_f32_16x16x32_bf16 v[216:219],v[96:99],v[140:143],v[216:219]
v_mfma_f32_16x16x32_bf16 v[224:227],v[112:115],v[32:35],v[224:227]
v_mfma_f32_16x16x32_bf16 v[224:227],v[116:119],v[36:39],v[224:227]
v_mfma_f32_16x16x32_bf16 v[224:227],v[120:123],v[40:43],v[224:227]
v_mfma_f32_16x16x32_bf16 v[224:227],v[124:127],v[44:47],v[224:227]
v_mfma_f32_16x16x32_bf16 v[232:235],v[124:127],v[180:183],v[232:235]
v_mfma_f32_16x16x32_bf16 v[232:235],v[120:123],v[164:167],v[232:235]
v_mfma_f32_16x16x32_bf16 v[232:235],v[116:119],v[148:151],v[232:235]
v_mfma_f32_16x16x32_bf16 v[232:235],v[112:115],v[132:135],v[232:235]
v_mfma_f32_16x16x32_bf16 v[240:243],v[112:115],v[196:199],v[240:243]
v_mfma_f32_16x16x32_bf16 v[240:243],v[116:119],v[212:215],v[240:243]
v_mfma_f32_16x16x32_bf16 v[240:243],v[120:123],v[228:231],v[240:243]
v_mfma_f32_16x16x32_bf16 v[240:243],v[124:127],v[244:247],v[240:243]
v_mfma_f32_16x16x32_bf16 v[248:251],v[124:127],v[188:191],v[248:251]
v_mfma_f32_16x16x32_bf16 v[248:251],v[120:123],v[172:175],v[248:251]
v_mfma_f32_16x16x32_bf16 v[248:251],v[116:119],v[156:159],v[248:251]
v_mfma_f32_16x16x32_bf16 v[248:251],v[112:115],v[140:143],v[248:251]
s_waitcnt lgkmcnt(0)
v_mfma_f32_16x16x32_bf16 v[192:195],v[64:67],a[128:131],v[192:195]
v_mfma_f32_16x16x32_bf16 v[192:195],v[68:71],a[144:147],v[192:195]
v_mfma_f32_16x16x32_bf16 v[192:195],v[72:75],a[160:163],v[192:195]
v_mfma_f32_16x16x32_bf16 v[192:195],v[76:79],a[176:179],v[192:195]
v_mfma_f32_16x16x32_bf16 v[200:203],v[76:79],a[180:183],v[200:203]
v_mfma_f32_16x16x32_bf16 v[200:203],v[72:75],a[164:167],v[200:203]
v_mfma_f32_16x16x32_bf16 v[200:203],v[68:71],a[148:151],v[200:203]
v_mfma_f32_16x16x32_bf16 v[200:203],v[64:67],a[132:135],v[200:203]
v_mfma_f32_16x16x32_bf16 v[208:211],v[64:67],a[136:139],v[208:211]
v_mfma_f32_16x16x32_bf16 v[208:211],v[68:71],a[152:155],v[208:211]
v_mfma_f32_16x16x32_bf16 v[208:211],v[72:75],a[168:171],v[208:211]
v_mfma_f32_16x16x32_bf16 v[208:211],v[76:79],a[184:187],v[208:211]
v_mfma_f32_16x16x32_bf16 v[216:219],v[76:79],a[188:191],v[216:219]
v_mfma_f32_16x16x32_bf16 v[216:219],v[72:75],a[172:175],v[216:219]
v_mfma_f32_16x16x32_bf16 v[216:219],v[68:71],a[156:159],v[216:219]
v_mfma_f32_16x16x32_bf16 v[216:219],v[64:67],a[140:143],v[216:219]
v_mfma_f32_16x16x32_bf16 v[224:227],v[80:83],a[128:131],v[224:227]
buffer_load_dwordx2 v[62:63],v61,s[44:47],s91 offen offset:2312 sc1
v_mfma_f32_16x16x32_bf16 v[224:227],v[84:87],a[144:147],v[224:227]
v_mfma_f32_16x16x32_bf16 v[224:227],v[88:91],a[160:163],v[224:227]
v_mfma_f32_16x16x32_bf16 v[224:227],v[92:95],a[176:179],v[224:227]
v_mfma_f32_16x16x32_bf16 v[232:235],v[92:95],a[180:183],v[232:235]
v_mfma_f32_16x16x32_bf16 v[232:235],v[88:91],a[164:167],v[232:235]
v_mfma_f32_16x16x32_bf16 v[232:235],v[84:87],a[148:151],v[232:235]
v_mfma_f32_16x16x32_bf16 v[232:235],v[80:83],a[132:135],v[232:235]
v_mfma_f32_16x16x32_bf16 v[240:243],v[80:83],a[136:139],v[240:243]
v_mfma_f32_16x16x32_bf16 v[240:243],v[84:87],a[152:155],v[240:243]
v_mfma_f32_16x16x32_bf16 v[240:243],v[88:91],a[168:171],v[240:243]
v_mfma_f32_16x16x32_bf16 v[240:243],v[92:95],a[184:187],v[240:243]
v_mfma_f32_16x16x32_bf16 v[248:251],v[92:95],a[188:191],v[248:251]
v_mfma_f32_16x16x32_bf16 v[248:251],v[88:91],a[172:175],v[248:251]
v_mfma_f32_16x16x32_bf16 v[248:251],v[84:87],a[156:159],v[248:251]
v_mfma_f32_16x16x32_bf16 v[248:251],v[80:83],a[140:143],v[248:251]
s_waitcnt vmcnt(1)
s_barrier
buffer_store_dwordx2 v[20:21],v22,s[44:47],s18 offen offset:2304 sc1
s_waitcnt vmcnt(1)
v_cmp_ne_u32 s[56:57],s52,v62
v_cmp_ne_u32 s[58:59],s53,v63
s_nop 0
s_or_b64 s[56:57],s[56:57],s[58:59]
s_cbranch_scc1 L_fr4_%=
L_fo4_%=:
buffer_load_dwordx4 v[64:67],v6,s[36:39],s66 offen sc1
buffer_load_dwordx4 v[68:71],v6,s[36:39],s67 offen sc1
buffer_load_dwordx4 v[72:75],v6,s[36:39],s66 offen offset:1024 sc1
)A"
R"A(buffer_load_dwordx4 v[76:79],v6,s[36:39],s67 offen offset:1024 sc1
buffer_load_dwordx4 v[80:83],v6,s[36:39],s66 offen offset:2048 sc1
buffer_load_dwordx4 v[84:87],v6,s[36:39],s67 offen offset:2048 sc1
buffer_load_dwordx4 v[88:91],v28,s[36:39],s66 offen offset:3072 sc1
buffer_load_dwordx4 v[92:95],v28,s[36:39],s67 offen offset:3072 sc1
buffer_load_dwordx2 v[4:5],v3,s[44:47],s91 offen offset:2304 sc1
s_waitcnt vmcnt(8)
v_mfma_f32_16x16x32_bf16 v[192:195],v[48:51],v[64:67],v[192:195]
s_waitcnt vmcnt(7)
v_mfma_f32_16x16x32_bf16 v[192:195],v[48:51],v[68:71],v[192:195]
v_mfma_f32_16x16x32_bf16 v[224:227],v[52:55],v[64:67],v[224:227]
v_mfma_f32_16x16x32_bf16 v[224:227],v[52:55],v[68:71],v[224:227]
s_waitcnt vmcnt(6)
v_mfma_f32_16x16x32_bf16 v[200:203],v[48:51],v[72:75],v[200:203]
s_waitcnt vmcnt(5)
v_mfma_f32_16x16x32_bf16 v[200:203],v[48:51],v[76:79],v[200:203]
v_mfma_f32_16x16x32_bf16 v[232:235],v[52:55],v[72:75],v[232:235]
v_mfma_f32_16x16x32_bf16 v[232:235],v[52:55],v[76:79],v[232:235]
s_waitcnt vmcnt(4)
v_mfma_f32_16x16x32_bf16 v[208:211],v[48:51],v[80:83],v[208:211]
s_waitcnt vmcnt(3)
v_mfma_f32_16x16x32_bf16 v[208:211],v[48:51],v[84:87],v[208:211]
v_mfma_f32_16x16x32_bf16 v[240:243],v[52:55],v[80:83],v[240:243]
v_mfma_f32_16x16x32_bf16 v[240:243],v[52:55],v[84:87],v[240:243]
s_waitcnt vmcnt(2)
v_mfma_f32_16x16x32_bf16 v[216:219],v[48:51],v[88:91],v[216:219]
s_waitcnt vmcnt(1)
v_mfma_f32_16x16x32_bf16 v[216:219],v[48:51],v[92:95],v[216:219]
v_mfma_f32_16x16x32_bf16 v[248:251],v[52:55],v[88:91],v[248:251]
v_mfma_f32_16x16x32_bf16 v[248:251],v[52:55],v[92:95],v[248:251]
s_waitcnt vmcnt(0)
v_cmp_ne_u32 s[56:57],s52,v4
v_cmp_ne_u32 s[58:59],s53,v5
s_nop 0
s_or_b64 s[56:57],s[56:57],s[58:59]
s_cbranch_scc1 L_fr5_%=
L_fo5_%=:
buffer_load_dwordx4 v[96:99],v6,s[36:39],s68 offen sc1
buffer_load_dwordx4 v[100:103],v6,s[36:39],s68 offen offset:1024 sc1
buffer_load_dwordx4 v[104:107],v6,s[36:39],s68 offen offset:2048 sc1
buffer_load_dwordx4 v[108:111],v6,s[36:39],s68 offen offset:3072 sc1
buffer_load_dwordx4 v[112:115],v6,s[36:39],s65 offen sc1
buffer_load_dwordx4 v[116:119],v6,s[36:39],s65 offen offset:1024 sc1
buffer_load_dwordx4 v[120:123],v6,s[36:39],s65 offen offset:2048 sc1
buffer_load_dwordx4 v[124:127],v28,s[36:39],s65 offen offset:3072 sc1
s_waitcnt vmcnt(7)
v_mfma_f32_16x16x32_bf16 v[128:131],v[48:51],v[96:99],0
s_waitcnt vmcnt(6)
v_mfma_f32_16x16x32_bf16 v[128:131],v[48:51],v[100:103],v[128:131]
s_waitcnt vmcnt(5)
v_mfma_f32_16x16x32_bf16 v[128:131],v[48:51],v[104:107],v[128:131]
s_waitcnt vmcnt(4)
v_mfma_f32_16x16x32_bf16 v[128:131],v[48:51],v[108:111],v[128:131]
v_mfma_f32_16x16x32_bf16 v[160:163],v[52:55],v[96:99],0
v_mfma_f32_16x16x32_bf16 v[160:163],v[52:55],v[100:103],v[160:163]
v_mfma_f32_16x16x32_bf16 v[160:163],v[52:55],v[104:107],v[160:163]
v_mfma_f32_16x16x32_bf16 v[160:163],v[52:55],v[108:111],v[160:163]
s_waitcnt vmcnt(3)
v_mfma_f32_16x16x32_bf16 v[192:195],v[48:51],v[112:115],v[192:195]
v_mfma_f32_16x16x32_bf16 v[224:227],v[52:55],v[112:115],v[224:227]
s_waitcnt vmcnt(2)
v_mfma_f32_16x16x32_bf16 v[200:203],v[48:51],v[116:119],v[200:203]
v_mfma_f32_16x16x32_bf16 v[232:235],v[52:55],v[116:119],v[232:235]
s_waitcnt vmcnt(1)
v_cvt_pk_bf16_f32 v128,v128,v129
v_cvt_pk_bf16_f32 v129,v130,v131
v_mfma_f32_16x16x32_bf16 v[208:211],v[48:51],v[120:123],v[208:211]
v_mfma_f32_16x16x32_bf16 v[240:243],v[52:55],v[120:123],v[240:243]
s_waitcnt vmcnt(0)
v_cvt_pk_bf16_f32 v130,v160,v161
v_cvt_pk_bf16_f32 v131,v162,v163
v_mfma_f32_16x16x32_bf16 v[216:219],v[48:51],v[124:127],v[216:219]
v_cvt_pk_bf16_f32 v192,v192,v193
v_cvt_pk_bf16_f32 v193,v194,v195
v_cvt_pk_bf16_f32 v194,v224,v225
v_cvt_pk_bf16_f32 v195,v226,v227
v_cvt_pk_bf16_f32 v200,v200,v201
v_cvt_pk_bf16_f32 v201,v202,v203
v_mfma_f32_16x16x32_bf16 v[248:251],v[52:55],v[124:127],v[248:251]
v_permlane16_swap_b32 v128,v130
v_permlane16_swap_b32 v129,v131
v_cvt_pk_bf16_f32 v202,v232,v233
v_cvt_pk_bf16_f32 v203,v234,v235
v_cvt_pk_bf16_f32 v208,v208,v209
v_cvt_pk_bf16_f32 v209,v210,v211
v_cvt_pk_bf16_f32 v210,v240,v241
v_cvt_pk_bf16_f32 v211,v242,v243
buffer_store_dwordx4 v[128:131],v56,s[24:27],s83 offen offset:256 sc1 nt
v_permlane16_swap_b32 v192,v194
v_permlane16_swap_b32 v193,v195
buffer_store_dwordx4 v[192:195],v56,s[24:27],s87 offen sc1 nt
v_permlane16_swap_b32 v200,v202
v_permlane16_swap_b32 v201,v203
v_cvt_pk_bf16_f32 v216,v216,v217
v_cvt_pk_bf16_f32 v217,v218,v219
v_cvt_pk_bf16_f32 v218,v248,v249
v_cvt_pk_bf16_f32 v219,v250,v251
buffer_store_dwordx4 v[200:203],v56,s[24:27],s88 offen sc1 nt
v_permlane16_swap_b32 v208,v210
v_permlane16_swap_b32 v209,v211
buffer_store_dwordx4 v[208:211],v56,s[24:27],s89 offen sc1 nt
v_permlane16_swap_b32 v216,v218
v_permlane16_swap_b32 v217,v219
buffer_store_dwordx4 v[216:219],v56,s[24:27],s90 offen sc1 nt
s_lshl_b32 s69,s62,20
s_add_u32 s69,s69,s18
buffer_store_dword v20,v22,s[44:47],s69 offen sc1
L_geF_%=:
s_branch L_end_%=
.p2align 12
L_pd19_%=:
s_cbranch_execz L_trt1_%=
L_GS_%=:
L_gbS_%=:
s_waitcnt lgkmcnt(14)
v_mfma_f32_16x16x32_bf16 a[192:195],v[64:67],a[68:71],0
buffer_store_dwordx4 v[32:35],v6,s[40:43],s79 offen sc1
s_waitcnt lgkmcnt(12)
v_mfma_f32_16x16x32_bf16 a[192:195],v[68:71],a[76:79],a[192:195]
s_lshl_b32 s91,s63,8
v_bfe_u32 v3,v6,4,2
v_min_u32 v3,2,v3
s_waitcnt lgkmcnt(10)
v_mfma_f32_16x16x32_bf16 a[192:195],v[72:75],a[84:87],a[192:195]
s_add_u32 s87,s64,1
v_add3_u32 v3,v3,s64,1
v_and_b32 v3,3,v3
s_waitcnt lgkmcnt(8)
v_mfma_f32_16x16x32_bf16 a[192:195],v[76:79],a[92:95],a[192:195]
s_and_b32 s87,s87,3
ds_read_b64_tr_b16 v[96:97],v1 offset:64
v_lshlrev_b32 v3,6,v3
v_mfma_f32_16x16x32_bf16 a[196:199],v[76:79],a[124:127],0
s_lshl_b32 s87,s87,1
ds_read_b64_tr_b16 v[98:99],v1 offset:5184
ds_read_b64_tr_b16 v[100:101],v1 offset:10304
v_mfma_f32_16x16x32_bf16 a[196:199],v[72:75],a[116:119],a[196:199]
s_lshl_b32 s87,s87,16
ds_read_b64_tr_b16 v[102:103],v1 offset:15424
v_lshlrev_b32 v29,10,v6
v_mfma_f32_16x16x32_bf16 a[196:199],v[68:71],a[108:111],a[196:199]
s_add_u32 s88,s87,4096
ds_read_b64_tr_b16 v[104:105],v1 offset:20544
v_accvgpr_read_b32 v16,a24
v_mfma_f32_16x16x32_bf16 a[196:199],v[64:67],a[100:103],a[196:199]
s_add_u32 s89,s87,8192
ds_read_b64_tr_b16 v[106:107],v1 offset:25664
v_accvgpr_read_b32 v17,a25
v_mfma_f32_16x16x32_bf16 a[200:203],v[64:67],a[64:67],0
s_add_u32 s90,s87,12288
ds_read_b64_tr_b16 v[108:109],v1 offset:30784
ds_read_b64_tr_b16 v[110:111],v1 offset:35904
v_mfma_f32_16x16x32_bf16 a[200:203],v[68:71],a[72:75],a[200:203]
s_add_u32 s20,s64,2
ds_read_b64_tr_b16 v[112:113],v2 offset:96
v_accvgpr_read_b32 v18,a26
v_mfma_f32_16x16x32_bf16 a[200:203],v[72:75],a[80:83],a[200:203]
s_and_b32 s20,s20,3
ds_read_b64_tr_b16 v[114:115],v2 offset:5216
v_accvgpr_read_b32 v19,a27
v_mfma_f32_16x16x32_bf16 a[200:203],v[76:79],a[88:91],a[200:203]
s_lshl_b32 s20,s20,1
ds_read_b64_tr_b16 v[116:117],v2 offset:10336
v_accvgpr_read_b32 v20,a28
v_mfma_f32_16x16x32_bf16 a[204:207],v[76:79],a[120:123],0
s_lshl_b32 s20,s20,16
ds_read_b64_tr_b16 v[118:119],v2 offset:15456
ds_read_b64_tr_b16 v[120:121],v2 offset:20576
v_mfma_f32_16x16x32_bf16 a[204:207],v[72:75],a[112:115],a[204:207]
s_add_u32 s21,s20,4096
ds_read_b64_tr_b16 v[122:123],v2 offset:25696
v_accvgpr_read_b32 v21,a29
v_mfma_f32_16x16x32_bf16 a[204:207],v[68:71],a[104:107],a[204:207]
s_add_u32 s22,s20,8192
ds_read_b64_tr_b16 v[124:125],v2 offset:30816
v_accvgpr_read_b32 v22,a30
v_mfma_f32_16x16x32_bf16 a[204:207],v[64:67],a[96:99],a[204:207]
buffer_store_dwordx4 v[36:39],v6,s[40:43],s80 offen sc1
s_waitcnt lgkmcnt(9)
v_mfma_f32_16x16x32_bf16 a[208:211],v[80:83],a[68:71],0
s_add_u32 s23,s20,12288
ds_read_b64_tr_b16 v[126:127],v2 offset:35936
ds_read_b64_tr_b16 v[64:65],v1 offset:128
v_mfma_f32_16x16x32_bf16 a[208:211],v[84:87],a[76:79],a[208:211]
s_add_u32 m0,s86,122880
ds_read_b64_tr_b16 v[66:67],v1 offset:5248
v_accvgpr_read_b32 v23,a31
v_mfma_f32_16x16x32_bf16 a[208:211],v[88:91],a[84:87],a[208:211]
s_add_u32 s83,s64,3
ds_read_b64_tr_b16 v[68:69],v1 offset:10368
v_mul_f32 v16,v16,v49
v_mfma_f32_16x16x32_bf16 a[208:211],v[92:95],a[92:95],a[208:211]
s_and_b32 s83,s83,3
ds_read_b64_tr_b16 v[70:71],v1 offset:15488
v_mul_f32 v17,v17,v49
v_mfma_f32_16x16x32_bf16 a[212:215],v[92:95],a[124:127],0
s_waitcnt vmcnt(21)
buffer_load_dwordx2 v[4:5],v3,s[44:47],s91 offen offset:256 sc1
v_mfma_f32_16x16x32_bf16 a[212:215],v[88:91],a[116:119],a[212:215]
s_lshl_b32 s83,s83,1
ds_read_b64_tr_b16 v[72:73],v1 offset:20608
ds_read_b64_tr_b16 v[74:75],v1 offset:25728
v_mfma_f32_16x16x32_bf16 a[212:215],v[84:87],a[108:111],a[212:215]
s_mul_i32 s83,s83,163840
ds_read_b64_tr_b16 v[76:77],v1 offset:30848
v_mul_f32 v18,v18,v49
v_mfma_f32_16x16x32_bf16 a[212:215],v[80:83],a[100:103],a[212:215]
s_add_u32 s83,s83,s85
ds_read_b64_tr_b16 v[78:79],v1 offset:35968
v_mul_f32 v19,v19,v49
v_mfma_f32_16x16x32_bf16 a[216:219],v[80:83],a[64:67],0
s_add_u32 s84,s83,20480
v_mul_f32 v20,v20,v49
v_mul_f32 v21,v21,v49
v_mfma_f32_16x16x32_bf16 a[216:219],v[84:87],a[72:75],a[216:219]
s_lshl_b32 s66,s61,3
v_mul_f32 v22,v22,v49
v_mul_f32 v23,v23,v49
v_mfma_f32_16x16x32_bf16 a[216:219],v[88:91],a[80:83],a[216:219]
s_add_u32 s66,s66,s64
v_cvt_pk_bf16_f32 v44,v16,v17
v_cvt_pk_bf16_f32 v45,v18,v19
v_mfma_f32_16x16x32_bf16 a[216:219],v[92:95],a[88:91],a[216:219]
s_mul_i32 s66,s66,262144
v_cvt_pk_bf16_f32 v46,v20,v21
v_cvt_pk_bf16_f32 v47,v22,v23
v_mfma_f32_16x16x32_bf16 a[220:223],v[92:95],a[120:123],0
s_add_u32 s66,s66,100663296
ds_read_b64_tr_b16 v[92:93],v2 offset:30880
ds_read_b64_tr_b16 v[94:95],v2 offset:36000
v_mfma_f32_16x16x32_bf16 a[220:223],v[88:91],a[112:115],a[220:223]
s_mov_b32 s38,262144
ds_read_b64_tr_b16 v[88:89],v2 offset:20640
v_mul_f32 v132,v132,v53
v_mfma_f32_16x16x32_bf16 a[220:223],v[84:87],a[104:107],a[220:223]
buffer_store_dwordx4 v[40:43],v6,s[40:43],s81 offen sc1
v_mfma_f32_16x16x32_bf16 a[220:223],v[80:83],a[96:99],a[220:223]
s_mov_b32 s39,0x20000
ds_read_b64_tr_b16 v[80:81],v2 offset:160
ds_read_b64_tr_b16 v[82:83],v2 offset:5280
v_mfma_f32_16x16x32_bf16 a[224:227],v[96:99],a[68:71],0
s_add_u32 s74,s63,1
ds_read_b64_tr_b16 v[84:85],v2 offset:10400
v_mul_f32 v133,v133,v53
v_mfma_f32_16x16x32_bf16 a[224:227],v[100:103],a[76:79],a[224:227]
s_and_b32 s74,s74,3
ds_read_b64_tr_b16 v[86:87],v2 offset:15520
v_mul_f32 v134,v134,v53
v_mfma_f32_16x16x32_bf16 a[224:227],v[104:107],a[84:87],a[224:227]
s_lshl_b32 s74,s74,2
ds_read_b64_tr_b16 v[90:91],v2 offset:25760
v_mul_f32 v135,v135,v53
s_waitcnt lgkmcnt(8)
v_mfma_f32_16x16x32_bf16 a[224:227],v[108:111],a[92:95],a[224:227]
s_add_u32 s74,s74,s60
v_mul_f32 v140,v140,v53
v_mul_f32 v141,v141,v53
v_mfma_f32_16x16x32_bf16 a[228:231],v[108:111],a[124:127],0
s_mul_i32 s74,s74,16384
v_mul_f32 v142,v142,v53
v_mul_f32 v143,v143,v53
v_mfma_f32_16x16x32_bf16 a[228:231],v[104:107],a[116:119],a[228:231]
s_add_u32 s74,s74,8192
v_cvt_pk_bf16_f32 v132,v132,v133
v_cvt_pk_bf16_f32 v133,v134,v135
v_mfma_f32_16x16x32_bf16 a[228:231],v[100:103],a[108:111],a[228:231]
s_add_u32 s75,s63,2
v_cvt_pk_bf16_f32 v134,v140,v141
v_cvt_pk_bf16_f32 v135,v142,v143
v_mfma_f32_16x16x32_bf16 a[228:231],v[96:99],a[100:103],a[228:231]
s_and_b32 s75,s75,3
v_mul_f32 v148,v148,v53
v_mul_f32 v149,v149,v53
v_mfma_f32_16x16x32_bf16 a[232:235],v[96:99],a[64:67],0
s_lshl_b32 s75,s75,2
v_mul_f32 v150,v150,v53
v_mul_f32 v151,v151,v53
v_mfma_f32_16x16x32_bf16 a[232:235],v[100:103],a[72:75],a[232:235]
s_add_u32 s75,s75,s60
v_mul_f32 v156,v156,v53
v_mul_f32 v157,v157,v53
v_mfma_f32_16x16x32_bf16 a[232:235],v[104:107],a[80:83],a[232:235]
s_mul_i32 s75,s75,16384
v_mul_f32 v158,v158,v53
v_mul_f32 v159,v159,v53
v_mfma_f32_16x16x32_bf16 a[232:235],v[108:111],a[88:91],a[232:235]
s_add_u32 s75,s75,4096
v_cvt_pk_bf16_f32 v148,v148,v149
v_cvt_pk_bf16_f32 v149,v150,v151
v_mfma_f32_16x16x32_bf16 a[236:239],v[108:111],a[120:123],0
s_add_u32 s76,s63,3
ds_read_b64_tr_b16 v[108:109],v1 offset:30912
ds_read_b64_tr_b16 v[110:111],v1 offset:36032
v_mfma_f32_16x16x32_bf16 a[236:239],v[104:107],a[112:115],a[236:239]
buffer_store_dwordx4 v[44:47],v6,s[40:43],s82 offen sc1
v_mfma_f32_16x16x32_bf16 a[236:239],v[100:103],a[104:107],a[236:239]
)A"
R"A(s_and_b32 s76,s76,3
ds_read_b64_tr_b16 v[100:101],v1 offset:10432
ds_read_b64_tr_b16 v[102:103],v1 offset:15552
v_mfma_f32_16x16x32_bf16 a[236:239],v[96:99],a[96:99],a[236:239]
s_lshl_b32 s76,s76,2
ds_read_b64_tr_b16 v[96:97],v1 offset:192
v_cvt_pk_bf16_f32 v150,v156,v157
v_mfma_f32_16x16x32_bf16 a[240:243],v[112:115],a[68:71],0
s_add_u32 s76,s76,s60
ds_read_b64_tr_b16 v[98:99],v1 offset:5312
v_cvt_pk_bf16_f32 v151,v158,v159
v_mfma_f32_16x16x32_bf16 a[240:243],v[116:119],a[76:79],a[240:243]
s_mul_i32 s76,s76,16384
ds_read_b64_tr_b16 v[104:105],v1 offset:20672
v_mul_f32 v164,v164,v53
v_mfma_f32_16x16x32_bf16 a[240:243],v[120:123],a[84:87],a[240:243]
s_add_u32 s76,s76,0
ds_read_b64_tr_b16 v[106:107],v1 offset:25792
v_mul_f32 v165,v165,v53
v_mfma_f32_16x16x32_bf16 a[240:243],v[124:127],a[92:95],a[240:243]
s_lshl_b32 s67,s63,10
v_mul_f32 v166,v166,v53
v_mul_f32 v167,v167,v53
v_mfma_f32_16x16x32_bf16 a[244:247],v[124:127],a[124:127],0
s_mov_b32 s26,307200
v_mul_f32 v172,v172,v53
v_mul_f32 v173,v173,v53
v_mfma_f32_16x16x32_bf16 a[244:247],v[120:123],a[116:119],a[244:247]
s_mov_b32 s27,0x20000
v_mul_f32 v174,v174,v53
v_mul_f32 v175,v175,v53
v_mfma_f32_16x16x32_bf16 a[244:247],v[116:119],a[108:111],a[244:247]
s_lshl_b32 s77,s63,2
v_cvt_pk_bf16_f32 v164,v164,v165
v_cvt_pk_bf16_f32 v165,v166,v167
v_mfma_f32_16x16x32_bf16 a[244:247],v[112:115],a[100:103],a[244:247]
s_add_u32 s77,s77,s60
v_cvt_pk_bf16_f32 v166,v172,v173
v_cvt_pk_bf16_f32 v167,v174,v175
v_mfma_f32_16x16x32_bf16 a[248:251],v[112:115],a[64:67],0
s_mul_i32 s77,s77,16384
v_mul_f32 v180,v180,v53
v_mul_f32 v181,v181,v53
v_mfma_f32_16x16x32_bf16 a[248:251],v[116:119],a[72:75],a[248:251]
s_add_u32 s65,s77,0
v_mul_f32 v182,v182,v53
v_mul_f32 v183,v183,v53
v_mfma_f32_16x16x32_bf16 a[248:251],v[120:123],a[80:83],a[248:251]
s_add_u32 s68,s77,12288
v_mul_f32 v188,v188,v53
v_mul_f32 v189,v189,v53
v_mfma_f32_16x16x32_bf16 a[248:251],v[124:127],a[88:91],a[248:251]
v_mul_f32 v190,v190,v53
v_mul_f32 v191,v191,v53
v_mfma_f32_16x16x32_bf16 a[252:255],v[124:127],a[120:123],0
buffer_load_dword v30,v29,s[8:11],s7 offen
v_mfma_f32_16x16x32_bf16 a[252:255],v[120:123],a[112:115],a[252:255]
ds_read_b64_tr_b16 v[120:121],v2 offset:20704
ds_read_b64_tr_b16 v[122:123],v2 offset:25824
v_mfma_f32_16x16x32_bf16 a[252:255],v[116:119],a[104:107],a[252:255]
buffer_store_dwordx4 v[132:135],v6,s[40:43],s79 offen offset:1024 sc1
v_mfma_f32_16x16x32_bf16 a[252:255],v[112:115],a[96:99],a[252:255]
ds_read_b64_tr_b16 v[112:113],v2 offset:224
ds_read_b64_tr_b16 v[114:115],v2 offset:5344
v_mfma_f32_16x16x32_bf16 a[0:3],v[64:67],a[68:71],0
ds_read_b64_tr_b16 v[116:117],v2 offset:10464
v_cvt_pk_bf16_f32 v180,v180,v181
v_mfma_f32_16x16x32_bf16 a[0:3],v[68:71],a[76:79],a[0:3]
ds_read_b64_tr_b16 v[118:119],v2 offset:15584
v_cvt_pk_bf16_f32 v181,v182,v183
v_mfma_f32_16x16x32_bf16 a[0:3],v[72:75],a[84:87],a[0:3]
ds_read_b64_tr_b16 v[124:125],v2 offset:30944
v_cvt_pk_bf16_f32 v182,v188,v189
v_mfma_f32_16x16x32_bf16 a[0:3],v[76:79],a[92:95],a[0:3]
ds_read_b64_tr_b16 v[126:127],v2 offset:36064
v_cvt_pk_bf16_f32 v183,v190,v191
v_mfma_f32_16x16x32_bf16 a[4:7],v[76:79],a[124:127],0
v_mul_f32 v196,v196,v57
v_mul_f32 v197,v197,v57
v_mfma_f32_16x16x32_bf16 a[4:7],v[72:75],a[116:119],a[4:7]
v_mul_f32 v198,v198,v57
v_mul_f32 v199,v199,v57
v_mfma_f32_16x16x32_bf16 a[4:7],v[68:71],a[108:111],a[4:7]
v_mul_f32 v204,v204,v57
v_mul_f32 v205,v205,v57
v_mfma_f32_16x16x32_bf16 a[4:7],v[64:67],a[100:103],a[4:7]
v_mul_f32 v206,v206,v57
v_mul_f32 v207,v207,v57
v_mfma_f32_16x16x32_bf16 a[8:11],v[64:67],a[64:67],0
v_cvt_pk_bf16_f32 v196,v196,v197
v_cvt_pk_bf16_f32 v197,v198,v199
v_mfma_f32_16x16x32_bf16 a[8:11],v[68:71],a[72:75],a[8:11]
v_cvt_pk_bf16_f32 v198,v204,v205
v_cvt_pk_bf16_f32 v199,v206,v207
v_mfma_f32_16x16x32_bf16 a[8:11],v[72:75],a[80:83],a[8:11]
v_mul_f32 v212,v212,v57
v_mul_f32 v213,v213,v57
v_mfma_f32_16x16x32_bf16 a[8:11],v[76:79],a[88:91],a[8:11]
v_mul_f32 v214,v214,v57
v_mul_f32 v215,v215,v57
v_mfma_f32_16x16x32_bf16 a[12:15],v[76:79],a[120:123],0
ds_read_b64_tr_b16 v[76:77],v1 offset:30976
ds_read_b64_tr_b16 v[78:79],v1 offset:36096
v_mfma_f32_16x16x32_bf16 a[12:15],v[72:75],a[112:115],a[12:15]
buffer_store_dwordx4 v[148:151],v6,s[40:43],s80 offen offset:1024 sc1
v_mfma_f32_16x16x32_bf16 a[12:15],v[68:71],a[104:107],a[12:15]
ds_read_b64_tr_b16 v[68:69],v1 offset:10496
ds_read_b64_tr_b16 v[70:71],v1 offset:15616
v_mfma_f32_16x16x32_bf16 a[12:15],v[64:67],a[96:99],a[12:15]
s_waitcnt lgkmcnt(4)
ds_read_b64_tr_b16 v[64:65],v1 offset:256
v_mul_f32 v220,v220,v57
v_mfma_f32_16x16x32_bf16 a[16:19],v[80:83],a[68:71],0
ds_read_b64_tr_b16 v[66:67],v1 offset:5376
v_mul_f32 v221,v221,v57
v_mfma_f32_16x16x32_bf16 a[16:19],v[84:87],a[76:79],a[16:19]
ds_read_b64_tr_b16 v[72:73],v1 offset:20736
v_mul_f32 v222,v222,v57
v_mfma_f32_16x16x32_bf16 a[16:19],v[88:91],a[84:87],a[16:19]
ds_read_b64_tr_b16 v[74:75],v1 offset:25856
v_add_u32 v1,0xffff6000,v1
v_mfma_f32_16x16x32_bf16 a[16:19],v[92:95],a[92:95],a[16:19]
v_mul_f32 v223,v223,v57
v_cvt_pk_bf16_f32 v212,v212,v213
v_mfma_f32_16x16x32_bf16 a[20:23],v[92:95],a[124:127],0
v_cvt_pk_bf16_f32 v213,v214,v215
v_cvt_pk_bf16_f32 v214,v220,v221
v_mfma_f32_16x16x32_bf16 a[20:23],v[88:91],a[116:119],a[20:23]
v_cvt_pk_bf16_f32 v215,v222,v223
v_mul_f32 v228,v228,v57
v_mfma_f32_16x16x32_bf16 a[20:23],v[84:87],a[108:111],a[20:23]
v_mul_f32 v229,v229,v57
v_mul_f32 v230,v230,v57
v_mfma_f32_16x16x32_bf16 a[20:23],v[80:83],a[100:103],a[20:23]
v_mul_f32 v231,v231,v57
v_mul_f32 v236,v236,v57
v_mfma_f32_16x16x32_bf16 a[24:27],v[80:83],a[64:67],0
v_mul_f32 v237,v237,v57
v_mul_f32 v238,v238,v57
v_mfma_f32_16x16x32_bf16 a[24:27],v[84:87],a[72:75],a[24:27]
v_mul_f32 v239,v239,v57
v_cvt_pk_bf16_f32 v228,v228,v229
v_mfma_f32_16x16x32_bf16 a[24:27],v[88:91],a[80:83],a[24:27]
s_waitcnt vmcnt(5)
v_cmp_ne_u32 s[56:57],s52,v4
v_cmp_ne_u32 s[58:59],s53,v5
v_mfma_f32_16x16x32_bf16 a[24:27],v[92:95],a[88:91],a[24:27]
s_or_b64 s[56:57],s[56:57],s[58:59]
s_cbranch_scc1 L_fr6_%=
L_fo6_%=:
v_cvt_pk_bf16_f32 v229,v230,v231
v_mfma_f32_16x16x32_bf16 a[28:31],v[92:95],a[120:123],0
buffer_store_dwordx4 v[164:167],v6,s[40:43],s81 offen offset:1024 sc1
v_mfma_f32_16x16x32_bf16 a[28:31],v[88:91],a[112:115],a[28:31]
s_lshl_b32 s91,s63,8
ds_read_b64_tr_b16 v[88:89],v2 offset:20768
ds_read_b64_tr_b16 v[90:91],v2 offset:25888
v_mfma_f32_16x16x32_bf16 a[28:31],v[84:87],a[104:107],a[28:31]
buffer_load_dwordx4 a[128:131],v6,s[28:31],s87 offen sc1
v_mfma_f32_16x16x32_bf16 a[28:31],v[80:83],a[96:99],a[28:31]
ds_read_b64_tr_b16 v[80:81],v2 offset:288
ds_read_b64_tr_b16 v[82:83],v2 offset:5408
v_mfma_f32_16x16x32_bf16 v[128:131],v[96:99],a[68:71],0
ds_read_b64_tr_b16 v[84:85],v2 offset:10528
v_cvt_pk_bf16_f32 v230,v236,v237
v_mfma_f32_16x16x32_bf16 v[128:131],v[100:103],a[76:79],v[128:131]
ds_read_b64_tr_b16 v[86:87],v2 offset:15648
v_cvt_pk_bf16_f32 v231,v238,v239
v_mfma_f32_16x16x32_bf16 v[128:131],v[104:107],a[84:87],v[128:131]
ds_read_b64_tr_b16 v[92:93],v2 offset:31008
v_mul_f32 v244,v244,v57
v_mfma_f32_16x16x32_bf16 v[128:131],v[108:111],a[92:95],v[128:131]
ds_read_b64_tr_b16 v[94:95],v2 offset:36128
v_add_u32 v2,0xffff6000,v2
v_mfma_f32_16x16x32_bf16 v[136:139],v[108:111],a[124:127],0
v_mul_f32 v245,v245,v57
v_mul_f32 v246,v246,v57
v_mfma_f32_16x16x32_bf16 v[136:139],v[104:107],a[116:119],v[136:139]
v_mul_f32 v247,v247,v57
v_mul_f32 v252,v252,v57
v_mfma_f32_16x16x32_bf16 v[136:139],v[100:103],a[108:111],v[136:139]
v_mul_f32 v253,v253,v57
v_mul_f32 v254,v254,v57
v_mfma_f32_16x16x32_bf16 v[136:139],v[96:99],a[100:103],v[136:139]
v_mul_f32 v255,v255,v57
v_cvt_pk_bf16_f32 v244,v244,v245
v_mfma_f32_16x16x32_bf16 v[144:147],v[96:99],a[64:67],0
v_cvt_pk_bf16_f32 v245,v246,v247
v_cvt_pk_bf16_f32 v246,v252,v253
v_mfma_f32_16x16x32_bf16 v[144:147],v[100:103],a[72:75],v[144:147]
buffer_load_dwordx4 a[144:147],v6,s[28:31],s88 offen sc1
v_mfma_f32_16x16x32_bf16 v[144:147],v[104:107],a[80:83],v[144:147]
v_cvt_pk_bf16_f32 v247,v254,v255
v_accvgpr_read_b32 v16,a40
v_mfma_f32_16x16x32_bf16 v[144:147],v[108:111],a[88:91],v[144:147]
buffer_store_dwordx4 v[180:183],v6,s[40:43],s82 offen offset:1024 sc1
v_mfma_f32_16x16x32_bf16 v[152:155],v[108:111],a[120:123],0
ds_read_b64_tr_b16 v[108:109],v1 offset:30720
ds_read_b64_tr_b16 v[110:111],v1 offset:35840
v_mfma_f32_16x16x32_bf16 v[152:155],v[104:107],a[112:115],v[152:155]
ds_read_b64_tr_b16 v[104:105],v1 offset:20480
v_accvgpr_read_b32 v17,a41
v_mfma_f32_16x16x32_bf16 v[152:155],v[100:103],a[104:107],v[152:155]
ds_read_b64_tr_b16 v[100:101],v1 offset:10240
v_accvgpr_read_b32 v18,a42
v_mfma_f32_16x16x32_bf16 v[152:155],v[96:99],a[96:99],v[152:155]
ds_read_b64_tr_b16 v[96:97],v1
v_accvgpr_read_b32 v19,a43
v_mfma_f32_16x16x32_bf16 v[160:163],v[112:115],a[68:71],0
ds_read_b64_tr_b16 v[98:99],v1 offset:5120
ds_read_b64_tr_b16 v[102:103],v1 offset:15360
v_mfma_f32_16x16x32_bf16 v[160:163],v[116:119],a[76:79],v[160:163]
ds_read_b64_tr_b16 v[106:107],v1 offset:25600
v_accvgpr_read_b32 v20,a44
v_mfma_f32_16x16x32_bf16 v[160:163],v[120:123],a[84:87],v[160:163]
v_accvgpr_read_b32 v21,a45
v_accvgpr_read_b32 v22,a46
v_mfma_f32_16x16x32_bf16 v[160:163],v[124:127],a[92:95],v[160:163]
v_accvgpr_read_b32 v23,a47
v_mul_f32 v16,v16,v61
v_mfma_f32_16x16x32_bf16 v[168:171],v[124:127],a[124:127],0
v_mul_f32 v17,v17,v61
v_mul_f32 v18,v18,v61
v_mfma_f32_16x16x32_bf16 v[168:171],v[120:123],a[116:119],v[168:171]
v_mul_f32 v19,v19,v61
v_mul_f32 v20,v20,v61
v_mfma_f32_16x16x32_bf16 v[168:171],v[116:119],a[108:111],v[168:171]
buffer_load_dwordx4 a[160:163],v6,s[28:31],s89 offen sc1
v_mfma_f32_16x16x32_bf16 v[168:171],v[112:115],a[100:103],v[168:171]
v_mul_f32 v21,v21,v61
v_mul_f32 v22,v22,v61
v_mfma_f32_16x16x32_bf16 v[176:179],v[112:115],a[64:67],0
v_mul_f32 v23,v23,v61
v_cvt_pk_bf16_f32 v156,v16,v17
v_mfma_f32_16x16x32_bf16 v[176:179],v[116:119],a[72:75],v[176:179]
v_cvt_pk_bf16_f32 v157,v18,v19
v_cvt_pk_bf16_f32 v158,v20,v21
v_mfma_f32_16x16x32_bf16 v[176:179],v[120:123],a[80:83],v[176:179]
buffer_store_dwordx4 v[196:199],v6,s[40:43],s79 offen offset:2048 sc1
v_mfma_f32_16x16x32_bf16 v[176:179],v[124:127],a[88:91],v[176:179]
v_cvt_pk_bf16_f32 v159,v22,v23
v_accvgpr_read_b32 v16,a56
v_mfma_f32_16x16x32_bf16 v[184:187],v[124:127],a[120:123],0
v_accvgpr_read_b32 v17,a57
v_accvgpr_read_b32 v18,a58
v_mfma_f32_16x16x32_bf16 v[184:187],v[120:123],a[112:115],v[184:187]
ds_read_b64_tr_b16 v[120:121],v2 offset:20512
ds_read_b64_tr_b16 v[122:123],v2 offset:25632
v_mfma_f32_16x16x32_bf16 v[184:187],v[116:119],a[104:107],v[184:187]
ds_read_b64_tr_b16 v[116:117],v2 offset:10272
v_accvgpr_read_b32 v19,a59
v_mfma_f32_16x16x32_bf16 v[184:187],v[112:115],a[96:99],v[184:187]
s_waitcnt lgkmcnt(3)
ds_read_b64_tr_b16 v[112:113],v2 offset:32
v_accvgpr_read_b32 v20,a60
v_mfma_f32_16x16x32_bf16 v[192:195],v[64:67],a[68:71],0
ds_read_b64_tr_b16 v[114:115],v2 offset:5152
v_accvgpr_read_b32 v21,a61
v_mfma_f32_16x16x32_bf16 v[192:195],v[68:71],a[76:79],v[192:195]
ds_read_b64_tr_b16 v[118:119],v2 offset:15392
v_accvgpr_read_b32 v22,a62
v_mfma_f32_16x16x32_bf16 v[192:195],v[72:75],a[84:87],v[192:195]
v_accvgpr_read_b32 v23,a63
v_mul_f32 v16,v16,v61
v_mfma_f32_16x16x32_bf16 v[192:195],v[76:79],a[92:95],v[192:195]
buffer_load_dwordx4 a[176:179],v6,s[28:31],s90 offen sc1
v_mfma_f32_16x16x32_bf16 v[200:203],v[76:79],a[124:127],0
v_mul_f32 v17,v17,v61
v_mul_f32 v18,v18,v61
v_mfma_f32_16x16x32_bf16 v[200:203],v[72:75],a[116:119],v[200:203]
v_mul_f32 v19,v19,v61
v_mul_f32 v20,v20,v61
v_mfma_f32_16x16x32_bf16 v[200:203],v[68:71],a[108:111],v[200:203]
v_mul_f32 v21,v21,v61
v_mul_f32 v22,v22,v61
v_mfma_f32_16x16x32_bf16 v[200:203],v[64:67],a[100:103],v[200:203]
v_mul_f32 v23,v23,v61
v_cvt_pk_bf16_f32 v188,v16,v17
v_mfma_f32_16x16x32_bf16 v[208:211],v[64:67],a[64:67],0
v_cvt_pk_bf16_f32 v189,v18,v19
v_cvt_pk_bf16_f32 v190,v20,v21
v_mfma_f32_16x16x32_bf16 v[208:211],v[68:71],a[72:75],v[208:211]
buffer_store_dwordx4 v[212:215],v6,s[40:43],s80 offen offset:2048 sc1
v_mfma_f32_16x16x32_bf16 v[208:211],v[72:75],a[80:83],v[208:211]
ds_read_b64_tr_b16 v[124:125],v2 offset:30752
)A"
R"A(v_cvt_pk_bf16_f32 v191,v22,v23
v_mfma_f32_16x16x32_bf16 v[208:211],v[76:79],a[88:91],v[208:211]
ds_read_b64_tr_b16 v[126:127],v2 offset:35872
v_accvgpr_read_b32 v8,a32
v_mfma_f32_16x16x32_bf16 v[216:219],v[76:79],a[120:123],0
v_accvgpr_read_b32 v9,a33
v_accvgpr_read_b32 v10,a34
v_mfma_f32_16x16x32_bf16 v[216:219],v[72:75],a[112:115],v[216:219]
v_accvgpr_read_b32 v11,a35
v_accvgpr_read_b32 v12,a36
v_mfma_f32_16x16x32_bf16 v[216:219],v[68:71],a[104:107],v[216:219]
v_accvgpr_read_b32 v13,a37
v_accvgpr_read_b32 v14,a38
v_mfma_f32_16x16x32_bf16 v[216:219],v[64:67],a[96:99],v[216:219]
v_accvgpr_read_b32 v15,a39
v_mul_f32 v8,v8,v61
v_mfma_f32_16x16x32_bf16 v[224:227],v[80:83],a[68:71],0
buffer_load_dwordx4 a[180:183],v6,s[28:31],s90 offen offset:1024 sc1
v_mfma_f32_16x16x32_bf16 v[224:227],v[84:87],a[76:79],v[224:227]
v_mul_f32 v9,v9,v61
v_mul_f32 v10,v10,v61
v_mfma_f32_16x16x32_bf16 v[224:227],v[88:91],a[84:87],v[224:227]
v_mul_f32 v11,v11,v61
v_mul_f32 v12,v12,v61
v_mfma_f32_16x16x32_bf16 v[224:227],v[92:95],a[92:95],v[224:227]
v_mul_f32 v13,v13,v61
v_mul_f32 v14,v14,v61
v_mfma_f32_16x16x32_bf16 v[232:235],v[92:95],a[124:127],0
v_mul_f32 v15,v15,v61
v_cvt_pk_bf16_f32 v140,v8,v9
v_mfma_f32_16x16x32_bf16 v[232:235],v[88:91],a[116:119],v[232:235]
v_cvt_pk_bf16_f32 v141,v10,v11
v_cvt_pk_bf16_f32 v142,v12,v13
v_mfma_f32_16x16x32_bf16 v[232:235],v[84:87],a[108:111],v[232:235]
v_cvt_pk_bf16_f32 v143,v14,v15
v_accvgpr_read_b32 v8,a48
v_mfma_f32_16x16x32_bf16 v[232:235],v[80:83],a[100:103],v[232:235]
v_accvgpr_read_b32 v9,a49
v_accvgpr_read_b32 v10,a50
v_mfma_f32_16x16x32_bf16 v[240:243],v[80:83],a[64:67],0
buffer_store_dwordx4 v[228:231],v6,s[40:43],s81 offen offset:2048 sc1
v_mfma_f32_16x16x32_bf16 v[240:243],v[84:87],a[72:75],v[240:243]
v_accvgpr_read_b32 v11,a51
v_accvgpr_read_b32 v12,a52
v_mfma_f32_16x16x32_bf16 v[240:243],v[88:91],a[80:83],v[240:243]
v_accvgpr_read_b32 v13,a53
v_accvgpr_read_b32 v14,a54
v_mfma_f32_16x16x32_bf16 v[240:243],v[92:95],a[88:91],v[240:243]
v_accvgpr_read_b32 v15,a55
v_mul_f32 v8,v8,v61
v_mfma_f32_16x16x32_bf16 v[248:251],v[92:95],a[120:123],0
v_mul_f32 v9,v9,v61
v_mul_f32 v10,v10,v61
v_mfma_f32_16x16x32_bf16 v[248:251],v[88:91],a[112:115],v[248:251]
buffer_load_dwordx4 a[164:167],v6,s[28:31],s89 offen offset:1024 sc1
v_mfma_f32_16x16x32_bf16 v[248:251],v[84:87],a[104:107],v[248:251]
v_mul_f32 v11,v11,v61
v_mul_f32 v12,v12,v61
v_mfma_f32_16x16x32_bf16 v[248:251],v[80:83],a[96:99],v[248:251]
v_mul_f32 v13,v13,v61
v_mul_f32 v14,v14,v61
v_mfma_f32_16x16x32_bf16 a[192:195],v[96:99],v[32:35],a[192:195]
v_mul_f32 v15,v15,v61
v_cvt_pk_bf16_f32 v172,v8,v9
v_mfma_f32_16x16x32_bf16 a[192:195],v[100:103],v[36:39],a[192:195]
v_cvt_pk_bf16_f32 v173,v10,v11
v_cvt_pk_bf16_f32 v174,v12,v13
v_mfma_f32_16x16x32_bf16 a[192:195],v[104:107],v[40:43],a[192:195]
buffer_load_dwordx4 a[68:71],v6,s[28:31],s20 offen sc1
v_mfma_f32_16x16x32_bf16 a[192:195],v[108:111],v[44:47],a[192:195]
ds_read_b64_tr_b16 v[64:65],v1 offset:64
v_cvt_pk_bf16_f32 v175,v14,v15
v_mfma_f32_16x16x32_bf16 a[196:199],v[108:111],v[180:183],a[196:199]
ds_read_b64_tr_b16 v[66:67],v1 offset:5184
ds_read_b64_tr_b16 v[68:69],v1 offset:10304
v_mfma_f32_16x16x32_bf16 a[196:199],v[104:107],v[164:167],a[196:199]
s_cmp_eq_u32 s60,3
s_cselect_b32 s69,0x40000000,0
ds_read_b64_tr_b16 v[70:71],v1 offset:15424
v_mfma_f32_16x16x32_bf16 a[196:199],v[100:103],v[148:151],a[196:199]
ds_read_b64_tr_b16 v[72:73],v1 offset:20544
v_add_u32 v28,s69,v6
v_mfma_f32_16x16x32_bf16 a[196:199],v[96:99],v[132:135],a[196:199]
buffer_store_dwordx4 v[244:247],v6,s[40:43],s82 offen offset:2048 sc1
v_mfma_f32_16x16x32_bf16 a[200:203],v[96:99],v[196:199],a[200:203]
s_and_b32 s69,s63,s60
ds_read_b64_tr_b16 v[74:75],v1 offset:25664
ds_read_b64_tr_b16 v[76:77],v1 offset:30784
v_mfma_f32_16x16x32_bf16 a[200:203],v[100:103],v[212:215],a[200:203]
ds_read_b64_tr_b16 v[78:79],v1 offset:35904
v_mov_b32 v8,s52
v_mfma_f32_16x16x32_bf16 a[200:203],v[104:107],v[228:231],a[200:203]
buffer_load_dwordx4 a[148:151],v6,s[28:31],s88 offen offset:1024 sc1
v_mfma_f32_16x16x32_bf16 a[200:203],v[108:111],v[244:247],a[200:203]
ds_read_b64_tr_b16 v[80:81],v2 offset:96
ds_read_b64_tr_b16 v[82:83],v2 offset:5216
v_mfma_f32_16x16x32_bf16 a[204:207],v[108:111],v[188:191],a[204:207]
ds_read_b64_tr_b16 v[84:85],v2 offset:10336
v_mov_b32 v9,s53
v_mfma_f32_16x16x32_bf16 a[204:207],v[104:107],v[172:175],a[204:207]
ds_read_b64_tr_b16 v[86:87],v2 offset:15456
v_lshlrev_b32 v10,20,v6
v_mfma_f32_16x16x32_bf16 a[204:207],v[100:103],v[156:159],a[204:207]
s_waitcnt lgkmcnt(7)
ds_read_b64_tr_b16 v[88:89],v2 offset:20576
v_bfe_u32 v3,v6,4,2
v_mfma_f32_16x16x32_bf16 a[204:207],v[96:99],v[140:143],a[204:207]
buffer_load_dwordx4 a[76:79],v6,s[28:31],s21 offen sc1
v_mfma_f32_16x16x32_bf16 a[208:211],v[112:115],v[32:35],a[208:211]
ds_read_b64_tr_b16 v[90:91],v2 offset:25696
ds_read_b64_tr_b16 v[92:93],v2 offset:30816
v_mfma_f32_16x16x32_bf16 a[208:211],v[116:119],v[36:39],a[208:211]
ds_read_b64_tr_b16 v[94:95],v2 offset:35936
v_min_u32 v3,2,v3
v_mfma_f32_16x16x32_bf16 a[208:211],v[120:123],v[40:43],a[208:211]
ds_read_b64_tr_b16 v[96:97],v1 offset:128
v_add3_u32 v3,v3,s64,1
v_mfma_f32_16x16x32_bf16 a[208:211],v[124:127],v[44:47],a[208:211]
ds_read_b64_tr_b16 v[98:99],v1 offset:5248
v_and_b32 v3,3,v3
v_mfma_f32_16x16x32_bf16 a[212:215],v[124:127],v[180:183],a[212:215]
ds_read_b64_tr_b16 v[100:101],v1 offset:10368
ds_read_b64_tr_b16 v[102:103],v1 offset:15488
v_mfma_f32_16x16x32_bf16 a[212:215],v[120:123],v[164:167],a[212:215]
ds_read_b64_tr_b16 v[104:105],v1 offset:20608
v_lshlrev_b32 v3,6,v3
v_mfma_f32_16x16x32_bf16 a[212:215],v[116:119],v[148:151],a[212:215]
s_nop 0
L_pd20_%=:
s_cbranch_execz L_trt1_%=
buffer_store_dwordx4 v[140:143],v28,s[40:43],s79 offen offset:3072 sc1
v_mfma_f32_16x16x32_bf16 a[212:215],v[112:115],v[132:135],a[212:215]
ds_read_b64_tr_b16 v[106:107],v1 offset:25728
ds_read_b64_tr_b16 v[108:109],v1 offset:30848
v_mfma_f32_16x16x32_bf16 a[216:219],v[112:115],v[196:199],a[216:219]
buffer_load_dwordx4 a[132:135],v6,s[28:31],s87 offen offset:1024 sc1
v_mfma_f32_16x16x32_bf16 a[216:219],v[116:119],v[212:215],a[216:219]
ds_read_b64_tr_b16 v[110:111],v1 offset:35968
v_bfe_u32 v56,v6,6,2
v_mfma_f32_16x16x32_bf16 a[216:219],v[120:123],v[228:231],a[216:219]
v_lshrrev_b32 v57,8,v6
v_cmp_eq_u32 s[56:57],v56,v57
v_mfma_f32_16x16x32_bf16 a[216:219],v[124:127],v[244:247],a[216:219]
v_bfe_u32 v56,v6,4,1
v_lshlrev_b32 v56,4,v56
v_mfma_f32_16x16x32_bf16 a[220:223],v[124:127],v[188:191],a[220:223]
ds_read_b64_tr_b16 v[124:125],v2 offset:30880
ds_read_b64_tr_b16 v[126:127],v2 offset:36000
v_mfma_f32_16x16x32_bf16 a[220:223],v[120:123],v[172:175],a[220:223]
buffer_load_dwordx4 a[84:87],v6,s[28:31],s22 offen sc1
v_mfma_f32_16x16x32_bf16 a[220:223],v[116:119],v[156:159],a[220:223]
ds_read_b64_tr_b16 v[116:117],v2 offset:10400
ds_read_b64_tr_b16 v[118:119],v2 offset:15520
v_mfma_f32_16x16x32_bf16 a[220:223],v[112:115],v[140:143],a[220:223]
ds_read_b64_tr_b16 v[112:113],v2 offset:160
v_mov_b32 v57,0x3f80
v_mfma_f32_16x16x32_bf16 a[224:227],v[64:67],v[32:35],a[224:227]
ds_read_b64_tr_b16 v[114:115],v2 offset:5280
v_lshlrev_b32 v56,v56,v57
v_mfma_f32_16x16x32_bf16 a[224:227],v[68:71],v[36:39],a[224:227]
s_waitcnt lgkmcnt(6)
ds_read_b64_tr_b16 v[120:121],v2 offset:20640
v_cndmask_b32 v56,0,v56,s[56:57]
v_mfma_f32_16x16x32_bf16 a[224:227],v[72:75],v[40:43],a[224:227]
ds_read_b64_tr_b16 v[122:123],v2 offset:25760
v_bfe_u32 v57,v6,5,1
v_mfma_f32_16x16x32_bf16 a[224:227],v[76:79],v[44:47],a[224:227]
s_add_u32 s36,s12,s66
s_addc_u32 s37,s13,0
v_cmp_eq_u32 s[56:57],0,v57
v_mfma_f32_16x16x32_bf16 a[228:231],v[76:79],v[180:183],a[228:231]
buffer_load_dwordx4 a[136:139],v6,s[28:31],s87 offen offset:2048 sc1
v_mfma_f32_16x16x32_bf16 a[228:231],v[72:75],v[164:167],a[228:231]
s_and_b32 s37,s37,0xffff
v_cndmask_b32 v48,0,v56,s[56:57]
v_cndmask_b32 v49,v56,0,s[56:57]
v_mfma_f32_16x16x32_bf16 a[228:231],v[68:71],v[148:151],a[228:231]
buffer_store_dwordx4 v[156:159],v28,s[40:43],s80 offen offset:3072 sc1
v_mfma_f32_16x16x32_bf16 a[228:231],v[64:67],v[132:135],a[228:231]
s_mul_i32 s66,s61,307200
v_mov_b32 v58,s52
v_mov_b32 v59,s53
v_mfma_f32_16x16x32_bf16 a[232:235],v[64:67],v[196:199],a[232:235]
s_add_u32 s24,s16,s66
s_addc_u32 s25,s17,0
v_lshlrev_b32 v60,20,v6
v_mfma_f32_16x16x32_bf16 a[232:235],v[68:71],v[212:215],a[232:235]
buffer_load_dwordx4 a[92:95],v6,s[28:31],s23 offen sc1
v_mfma_f32_16x16x32_bf16 a[232:235],v[72:75],v[228:231],a[232:235]
s_mul_i32 s66,s60,16384
s_add_u32 s66,s66,s67
s_add_u32 s79,s66,12288
v_mfma_f32_16x16x32_bf16 a[232:235],v[76:79],v[244:247],a[232:235]
s_add_u32 s80,s66,77824
v_bfe_u32 v61,v6,4,1
v_add3_u32 v61,v61,s63,2
v_mfma_f32_16x16x32_bf16 a[236:239],v[76:79],v[188:191],a[236:239]
s_and_b32 s25,s25,0xffff
ds_read_b64_tr_b16 v[76:77],v1 offset:30912
ds_read_b64_tr_b16 v[78:79],v1 offset:36032
v_mfma_f32_16x16x32_bf16 a[236:239],v[72:75],v[172:175],a[236:239]
s_add_u32 s67,s77,8192
ds_read_b64_tr_b16 v[72:73],v1 offset:20672
v_and_b32 v61,3,v61
v_mfma_f32_16x16x32_bf16 a[236:239],v[68:71],v[156:159],a[236:239]
ds_read_b64_tr_b16 v[68:69],v1 offset:10432
v_lshlrev_b32 v61,8,v61
v_mfma_f32_16x16x32_bf16 a[236:239],v[64:67],v[140:143],a[236:239]
ds_read_b64_tr_b16 v[64:65],v1 offset:192
v_mov_b32 v50,0
v_mfma_f32_16x16x32_bf16 a[240:243],v[80:83],v[32:35],a[240:243]
ds_read_b64_tr_b16 v[66:67],v1 offset:5312
ds_read_b64_tr_b16 v[70:71],v1 offset:15552
v_mfma_f32_16x16x32_bf16 a[240:243],v[84:87],v[36:39],a[240:243]
buffer_load_dwordx4 a[152:155],v6,s[28:31],s88 offen offset:2048 sc1
v_mfma_f32_16x16x32_bf16 a[240:243],v[88:91],v[40:43],a[240:243]
ds_read_b64_tr_b16 v[74:75],v1 offset:25792
v_mov_b32 v51,0
v_mfma_f32_16x16x32_bf16 a[240:243],v[92:95],v[44:47],a[240:243]
v_mov_b32 v52,0
v_mov_b32 v53,0
v_mfma_f32_16x16x32_bf16 a[244:247],v[92:95],v[180:183],a[244:247]
v_mov_b32 v54,v48
v_mov_b32 v55,v49
v_mfma_f32_16x16x32_bf16 a[244:247],v[88:91],v[164:167],a[244:247]
buffer_store_dwordx4 v[172:175],v28,s[40:43],s81 offen offset:3072 sc1
v_mfma_f32_16x16x32_bf16 a[244:247],v[84:87],v[148:151],a[244:247]
s_add_u32 s81,s66,143360
v_bfe_u32 v56,v6,4,4
v_mul_u32_u24 v56,1280,v56
v_mfma_f32_16x16x32_bf16 a[244:247],v[80:83],v[132:135],a[244:247]
buffer_load_dwordx4 a[124:127],v6,s[28:31],s23 offen offset:1024 sc1
v_mfma_f32_16x16x32_bf16 a[248:251],v[80:83],v[196:199],a[248:251]
v_bfe_u32 v57,v6,8,1
v_lshl_add_u32 v56,v57,5,v56
v_mfma_f32_16x16x32_bf16 a[248:251],v[84:87],v[212:215],a[248:251]
v_lshrrev_b32 v57,9,v6
v_lshl_add_u32 v56,v57,4,v56
v_mfma_f32_16x16x32_bf16 a[248:251],v[88:91],v[228:231],a[248:251]
s_cmp_eq_u32 s69,3
s_cselect_b32 s69,0x40000000,0
s_add_u32 s68,s68,s69
v_mfma_f32_16x16x32_bf16 a[248:251],v[92:95],v[244:247],a[248:251]
v_mfma_f32_16x16x32_bf16 a[252:255],v[92:95],v[188:191],a[252:255]
ds_read_b64_tr_b16 v[92:93],v2 offset:30944
ds_read_b64_tr_b16 v[94:95],v2 offset:36064
v_mfma_f32_16x16x32_bf16 a[252:255],v[88:91],v[172:175],a[252:255]
ds_read_b64_tr_b16 v[88:89],v2 offset:20704
v_mfma_f32_16x16x32_bf16 a[252:255],v[84:87],v[156:159],a[252:255]
buffer_load_dwordx4 a[168:171],v6,s[28:31],s89 offen offset:2048 sc1
v_mfma_f32_16x16x32_bf16 a[252:255],v[80:83],v[140:143],a[252:255]
ds_read_b64_tr_b16 v[80:81],v2 offset:224
ds_read_b64_tr_b16 v[82:83],v2 offset:5344
v_mfma_f32_16x16x32_bf16 a[0:3],v[96:99],v[32:35],a[0:3]
ds_read_b64_tr_b16 v[84:85],v2 offset:10464
v_mfma_f32_16x16x32_bf16 a[0:3],v[100:103],v[36:39],a[0:3]
ds_read_b64_tr_b16 v[86:87],v2 offset:15584
v_mfma_f32_16x16x32_bf16 a[0:3],v[104:107],v[40:43],a[0:3]
ds_read_b64_tr_b16 v[90:91],v2 offset:25824
v_mfma_f32_16x16x32_bf16 a[0:3],v[108:111],v[44:47],a[0:3]
buffer_load_dwordx4 a[116:119],v6,s[28:31],s22 offen offset:1024 sc1
v_mfma_f32_16x16x32_bf16 a[4:7],v[108:111],v[180:183],a[4:7]
v_mfma_f32_16x16x32_bf16 a[4:7],v[104:107],v[164:167],a[4:7]
buffer_store_dwordx4 v[188:191],v28,s[40:43],s82 offen offset:3072 sc1
v_mfma_f32_16x16x32_bf16 a[4:7],v[100:103],v[148:151],a[4:7]
s_add_u32 s82,s66,208896
s_mul_i32 s66,s64,320
)A"
R"A(v_mfma_f32_16x16x32_bf16 a[4:7],v[96:99],v[132:135],a[4:7]
v_mfma_f32_16x16x32_bf16 a[8:11],v[96:99],v[196:199],a[8:11]
v_mfma_f32_16x16x32_bf16 a[8:11],v[100:103],v[212:215],a[8:11]
v_mfma_f32_16x16x32_bf16 a[8:11],v[104:107],v[228:231],a[8:11]
v_mfma_f32_16x16x32_bf16 a[8:11],v[108:111],v[244:247],a[8:11]
buffer_load_dwordx4 a[184:187],v6,s[28:31],s90 offen offset:2048 sc1
v_mfma_f32_16x16x32_bf16 a[12:15],v[108:111],v[188:191],a[12:15]
ds_read_b64_tr_b16 v[108:109],v1 offset:30976
ds_read_b64_tr_b16 v[110:111],v1 offset:36096
v_mfma_f32_16x16x32_bf16 a[12:15],v[104:107],v[172:175],a[12:15]
ds_read_b64_tr_b16 v[104:105],v1 offset:20736
v_mfma_f32_16x16x32_bf16 a[12:15],v[100:103],v[156:159],a[12:15]
ds_read_b64_tr_b16 v[100:101],v1 offset:10496
v_mfma_f32_16x16x32_bf16 a[12:15],v[96:99],v[140:143],a[12:15]
s_waitcnt lgkmcnt(4)
ds_read_b64_tr_b16 v[96:97],v1 offset:256
v_mfma_f32_16x16x32_bf16 a[16:19],v[112:115],v[32:35],a[16:19]
buffer_load_dwordx4 a[108:111],v6,s[28:31],s21 offen offset:1024 sc1
v_mfma_f32_16x16x32_bf16 a[16:19],v[116:119],v[36:39],a[16:19]
ds_read_b64_tr_b16 v[98:99],v1 offset:5376
ds_read_b64_tr_b16 v[102:103],v1 offset:15616
v_mfma_f32_16x16x32_bf16 a[16:19],v[120:123],v[40:43],a[16:19]
ds_read_b64_tr_b16 v[106:107],v1 offset:25856
v_add_u32 v1,0xffff6000,v1
v_mfma_f32_16x16x32_bf16 a[16:19],v[124:127],v[44:47],a[16:19]
v_mfma_f32_16x16x32_bf16 a[20:23],v[124:127],v[180:183],a[20:23]
v_mfma_f32_16x16x32_bf16 a[20:23],v[120:123],v[164:167],a[20:23]
v_mfma_f32_16x16x32_bf16 a[20:23],v[116:119],v[148:151],a[20:23]
v_mfma_f32_16x16x32_bf16 a[20:23],v[112:115],v[132:135],a[20:23]
v_mfma_f32_16x16x32_bf16 a[24:27],v[112:115],v[196:199],a[24:27]
buffer_load_dwordx4 a[188:191],v28,s[28:31],s90 offen offset:3072 sc1
v_mfma_f32_16x16x32_bf16 a[24:27],v[116:119],v[212:215],a[24:27]
v_mfma_f32_16x16x32_bf16 a[24:27],v[120:123],v[228:231],a[24:27]
v_mfma_f32_16x16x32_bf16 a[24:27],v[124:127],v[244:247],a[24:27]
v_mfma_f32_16x16x32_bf16 a[28:31],v[124:127],v[188:191],a[28:31]
ds_read_b64_tr_b16 v[124:125],v2 offset:31008
ds_read_b64_tr_b16 v[126:127],v2 offset:36128
v_mfma_f32_16x16x32_bf16 a[28:31],v[120:123],v[172:175],a[28:31]
buffer_load_dwordx4 a[100:103],v6,s[28:31],s20 offen offset:1024 sc1
v_mfma_f32_16x16x32_bf16 a[28:31],v[116:119],v[156:159],a[28:31]
ds_read_b64_tr_b16 v[116:117],v2 offset:10528
ds_read_b64_tr_b16 v[118:119],v2 offset:15648
v_mfma_f32_16x16x32_bf16 a[28:31],v[112:115],v[140:143],a[28:31]
ds_read_b64_tr_b16 v[112:113],v2 offset:288
v_mfma_f32_16x16x32_bf16 v[128:131],v[64:67],v[32:35],v[128:131]
ds_read_b64_tr_b16 v[114:115],v2 offset:5408
v_mfma_f32_16x16x32_bf16 v[128:131],v[68:71],v[36:39],v[128:131]
ds_read_b64_tr_b16 v[120:121],v2 offset:20768
v_mfma_f32_16x16x32_bf16 v[128:131],v[72:75],v[40:43],v[128:131]
ds_read_b64_tr_b16 v[122:123],v2 offset:25888
v_add_u32 v2,0xffff6000,v2
v_mfma_f32_16x16x32_bf16 v[128:131],v[76:79],v[44:47],v[128:131]
v_mfma_f32_16x16x32_bf16 v[136:139],v[76:79],v[180:183],v[136:139]
v_mfma_f32_16x16x32_bf16 v[136:139],v[72:75],v[164:167],v[136:139]
buffer_load_dwordx4 a[172:175],v28,s[28:31],s89 offen offset:3072 sc1
v_mfma_f32_16x16x32_bf16 v[136:139],v[68:71],v[148:151],v[136:139]
v_mfma_f32_16x16x32_bf16 v[136:139],v[64:67],v[132:135],v[136:139]
v_mfma_f32_16x16x32_bf16 v[144:147],v[64:67],v[196:199],v[144:147]
v_mfma_f32_16x16x32_bf16 v[144:147],v[68:71],v[212:215],v[144:147]
v_mfma_f32_16x16x32_bf16 v[144:147],v[72:75],v[228:231],v[144:147]
buffer_load_dwordx4 a[64:67],v6,s[28:31],s20 offen offset:2048 sc1
v_mfma_f32_16x16x32_bf16 v[144:147],v[76:79],v[244:247],v[144:147]
v_mfma_f32_16x16x32_bf16 v[152:155],v[76:79],v[188:191],v[152:155]
v_mfma_f32_16x16x32_bf16 v[152:155],v[72:75],v[172:175],v[152:155]
v_mfma_f32_16x16x32_bf16 v[152:155],v[68:71],v[156:159],v[152:155]
v_mfma_f32_16x16x32_bf16 v[152:155],v[64:67],v[140:143],v[152:155]
s_mov_b64 exec,0
s_cmp_eq_u32 s60,0
s_cbranch_scc1 L_pd21_%=
s_cmp_eq_u32 s60,1
s_cbranch_scc1 L_pd22_%=
s_cmp_eq_u32 s60,2
s_cbranch_scc1 L_pd23_%=
s_branch L_trtg2S_%=
L_trtg2S_%=:
s_mov_b64 exec,-1
s_waitcnt lgkmcnt(0)
s_barrier
v_mfma_f32_16x16x32_bf16 v[160:163],v[80:83],v[32:35],v[160:163]
buffer_load_dwordx4 v24,s[48:51],s83 offen lds
v_mfma_f32_16x16x32_bf16 v[160:163],v[84:87],v[36:39],v[160:163]
ds_read_b64_tr_b16 v[64:65],v1
ds_read_b64_tr_b16 v[66:67],v1 offset:5120
v_mfma_f32_16x16x32_bf16 v[160:163],v[88:91],v[40:43],v[160:163]
ds_read_b64_tr_b16 v[68:69],v1 offset:10240
v_mfma_f32_16x16x32_bf16 v[160:163],v[92:95],v[44:47],v[160:163]
ds_read_b64_tr_b16 v[70:71],v1 offset:15360
v_mfma_f32_16x16x32_bf16 v[168:171],v[92:95],v[180:183],v[168:171]
ds_read_b64_tr_b16 v[72:73],v1 offset:20480
v_mfma_f32_16x16x32_bf16 v[168:171],v[88:91],v[164:167],v[168:171]
buffer_load_dwordx4 a[156:159],v28,s[28:31],s88 offen offset:3072 sc1
v_mfma_f32_16x16x32_bf16 v[168:171],v[84:87],v[148:151],v[168:171]
ds_read_b64_tr_b16 v[74:75],v1 offset:25600
ds_read_b64_tr_b16 v[76:77],v1 offset:30720
v_mfma_f32_16x16x32_bf16 v[168:171],v[80:83],v[132:135],v[168:171]
ds_read_b64_tr_b16 v[78:79],v1 offset:35840
v_mfma_f32_16x16x32_bf16 v[176:179],v[80:83],v[196:199],v[176:179]
v_mfma_f32_16x16x32_bf16 v[176:179],v[84:87],v[212:215],v[176:179]
v_mfma_f32_16x16x32_bf16 v[176:179],v[88:91],v[228:231],v[176:179]
buffer_load_dwordx4 a[72:75],v6,s[28:31],s21 offen offset:2048 sc1
v_mfma_f32_16x16x32_bf16 v[176:179],v[92:95],v[244:247],v[176:179]
v_mfma_f32_16x16x32_bf16 v[184:187],v[92:95],v[188:191],v[184:187]
ds_read_b64_tr_b16 v[92:93],v2 offset:30752
ds_read_b64_tr_b16 v[94:95],v2 offset:35872
v_mfma_f32_16x16x32_bf16 v[184:187],v[88:91],v[172:175],v[184:187]
ds_read_b64_tr_b16 v[88:89],v2 offset:20512
v_mfma_f32_16x16x32_bf16 v[184:187],v[84:87],v[156:159],v[184:187]
ds_read_b64_tr_b16 v[84:85],v2 offset:10272
v_mfma_f32_16x16x32_bf16 v[184:187],v[80:83],v[140:143],v[184:187]
buffer_load_dwordx4 a[140:143],v28,s[28:31],s87 offen offset:3072 sc1
v_mfma_f32_16x16x32_bf16 v[192:195],v[96:99],v[32:35],v[192:195]
s_add_u32 s87,s64,3
ds_read_b64_tr_b16 v[80:81],v2 offset:32
ds_read_b64_tr_b16 v[82:83],v2 offset:5152
v_mfma_f32_16x16x32_bf16 v[192:195],v[100:103],v[36:39],v[192:195]
s_and_b32 s87,s87,3
s_lshl_b32 s87,s87,1
ds_read_b64_tr_b16 v[86:87],v2 offset:15392
v_mfma_f32_16x16x32_bf16 v[192:195],v[104:107],v[40:43],v[192:195]
s_lshl_b32 s87,s87,16
s_add_u32 s88,s87,4096
ds_read_b64_tr_b16 v[90:91],v2 offset:25632
v_mfma_f32_16x16x32_bf16 v[192:195],v[108:111],v[44:47],v[192:195]
s_add_u32 s89,s87,8192
s_add_u32 s90,s87,12288
v_mfma_f32_16x16x32_bf16 v[200:203],v[108:111],v[180:183],v[200:203]
buffer_load_dwordx4 a[80:83],v6,s[28:31],s22 offen offset:2048 sc1
v_mfma_f32_16x16x32_bf16 v[200:203],v[104:107],v[164:167],v[200:203]
v_mfma_f32_16x16x32_bf16 v[200:203],v[100:103],v[148:151],v[200:203]
v_mfma_f32_16x16x32_bf16 v[200:203],v[96:99],v[132:135],v[200:203]
v_mfma_f32_16x16x32_bf16 v[208:211],v[96:99],v[196:199],v[208:211]
v_mfma_f32_16x16x32_bf16 v[208:211],v[100:103],v[212:215],v[208:211]
v_mfma_f32_16x16x32_bf16 v[208:211],v[104:107],v[228:231],v[208:211]
v_mfma_f32_16x16x32_bf16 v[208:211],v[108:111],v[244:247],v[208:211]
buffer_load_dwordx4 v25,s[48:51],s83 offen offset:1024 lds
v_mfma_f32_16x16x32_bf16 v[216:219],v[108:111],v[188:191],v[216:219]
ds_read_b64_tr_b16 v[108:109],v1 offset:30784
ds_read_b64_tr_b16 v[110:111],v1 offset:35904
v_mfma_f32_16x16x32_bf16 v[216:219],v[104:107],v[172:175],v[216:219]
ds_read_b64_tr_b16 v[104:105],v1 offset:20544
v_mfma_f32_16x16x32_bf16 v[216:219],v[100:103],v[156:159],v[216:219]
ds_read_b64_tr_b16 v[100:101],v1 offset:10304
v_mfma_f32_16x16x32_bf16 v[216:219],v[96:99],v[140:143],v[216:219]
ds_read_b64_tr_b16 v[96:97],v1 offset:64
v_mfma_f32_16x16x32_bf16 v[224:227],v[112:115],v[32:35],v[224:227]
buffer_load_dwordx4 a[88:91],v6,s[28:31],s23 offen offset:2048 sc1
v_mfma_f32_16x16x32_bf16 v[224:227],v[116:119],v[36:39],v[224:227]
ds_read_b64_tr_b16 v[98:99],v1 offset:5184
ds_read_b64_tr_b16 v[102:103],v1 offset:15424
v_mfma_f32_16x16x32_bf16 v[224:227],v[120:123],v[40:43],v[224:227]
ds_read_b64_tr_b16 v[106:107],v1 offset:25664
v_mfma_f32_16x16x32_bf16 v[224:227],v[124:127],v[44:47],v[224:227]
v_mfma_f32_16x16x32_bf16 v[232:235],v[124:127],v[180:183],v[232:235]
v_mfma_f32_16x16x32_bf16 v[232:235],v[120:123],v[164:167],v[232:235]
v_mfma_f32_16x16x32_bf16 v[232:235],v[116:119],v[148:151],v[232:235]
v_mfma_f32_16x16x32_bf16 v[232:235],v[112:115],v[132:135],v[232:235]
buffer_load_dwordx4 v26,s[48:51],s83 offen offset:2048 lds
v_mfma_f32_16x16x32_bf16 v[240:243],v[112:115],v[196:199],v[240:243]
v_mfma_f32_16x16x32_bf16 v[240:243],v[116:119],v[212:215],v[240:243]
v_mfma_f32_16x16x32_bf16 v[240:243],v[120:123],v[228:231],v[240:243]
v_mfma_f32_16x16x32_bf16 v[240:243],v[124:127],v[244:247],v[240:243]
v_mfma_f32_16x16x32_bf16 v[248:251],v[124:127],v[188:191],v[248:251]
ds_read_b64_tr_b16 v[124:125],v2 offset:30816
ds_read_b64_tr_b16 v[126:127],v2 offset:35936
v_mfma_f32_16x16x32_bf16 v[248:251],v[120:123],v[172:175],v[248:251]
buffer_load_dwordx4 a[120:123],v28,s[28:31],s23 offen offset:3072 sc1
v_mfma_f32_16x16x32_bf16 v[248:251],v[116:119],v[156:159],v[248:251]
ds_read_b64_tr_b16 v[116:117],v2 offset:10336
ds_read_b64_tr_b16 v[118:119],v2 offset:15456
v_mfma_f32_16x16x32_bf16 v[248:251],v[112:115],v[140:143],v[248:251]
s_waitcnt vmcnt(39) lgkmcnt(4)
ds_read_b64_tr_b16 v[112:113],v2 offset:96
v_mfma_f32_16x16x32_bf16 a[192:195],v[64:67],a[128:131],a[192:195]
ds_read_b64_tr_b16 v[114:115],v2 offset:5216
v_mfma_f32_16x16x32_bf16 a[192:195],v[68:71],a[144:147],a[192:195]
s_waitcnt vmcnt(37)
ds_read_b64_tr_b16 v[120:121],v2 offset:20576
v_mfma_f32_16x16x32_bf16 a[192:195],v[72:75],a[160:163],a[192:195]
s_waitcnt vmcnt(35)
ds_read_b64_tr_b16 v[122:123],v2 offset:25696
v_mfma_f32_16x16x32_bf16 a[192:195],v[76:79],a[176:179],a[192:195]
s_waitcnt vmcnt(27)
v_mfma_f32_16x16x32_bf16 a[196:199],v[76:79],a[180:183],a[196:199]
buffer_load_dwordx4 v27,s[48:51],s83 offen offset:3072 lds
v_mfma_f32_16x16x32_bf16 a[196:199],v[72:75],a[164:167],a[196:199]
s_add_u32 m0,s86,126976
v_mfma_f32_16x16x32_bf16 a[196:199],v[68:71],a[148:151],a[196:199]
s_waitcnt vmcnt(24)
v_mfma_f32_16x16x32_bf16 a[196:199],v[64:67],a[132:135],a[196:199]
v_mfma_f32_16x16x32_bf16 a[200:203],v[64:67],a[136:139],a[200:203]
s_waitcnt vmcnt(21)
v_mfma_f32_16x16x32_bf16 a[200:203],v[68:71],a[152:155],a[200:203]
s_waitcnt vmcnt(15)
v_mfma_f32_16x16x32_bf16 a[200:203],v[72:75],a[168:171],a[200:203]
buffer_load_dwordx4 a[112:115],v28,s[28:31],s22 offen offset:3072 sc1
v_mfma_f32_16x16x32_bf16 a[200:203],v[76:79],a[184:187],a[200:203]
s_waitcnt vmcnt(14)
v_mfma_f32_16x16x32_bf16 a[204:207],v[76:79],a[188:191],a[204:207]
s_waitcnt vmcnt(12)
ds_read_b64_tr_b16 v[76:77],v1 offset:30848
ds_read_b64_tr_b16 v[78:79],v1 offset:35968
v_mfma_f32_16x16x32_bf16 a[204:207],v[72:75],a[172:175],a[204:207]
s_waitcnt vmcnt(9)
ds_read_b64_tr_b16 v[72:73],v1 offset:20608
v_mfma_f32_16x16x32_bf16 a[204:207],v[68:71],a[156:159],a[204:207]
s_waitcnt vmcnt(7)
ds_read_b64_tr_b16 v[68:69],v1 offset:10368
v_mfma_f32_16x16x32_bf16 a[204:207],v[64:67],a[140:143],a[204:207]
ds_read_b64_tr_b16 v[64:65],v1 offset:128
v_mfma_f32_16x16x32_bf16 a[208:211],v[80:83],a[128:131],a[208:211]
ds_read_b64_tr_b16 v[66:67],v1 offset:5248
ds_read_b64_tr_b16 v[70:71],v1 offset:15488
v_mfma_f32_16x16x32_bf16 a[208:211],v[84:87],a[144:147],a[208:211]
buffer_load_dwordx4 v7,s[48:51],s83 offen lds
v_mfma_f32_16x16x32_bf16 a[208:211],v[88:91],a[160:163],a[208:211]
s_add_u32 m0,s86,128000
s_add_u32 s83,s64,1
ds_read_b64_tr_b16 v[74:75],v1 offset:25728
v_mfma_f32_16x16x32_bf16 a[208:211],v[92:95],a[176:179],a[208:211]
s_and_b32 s83,s83,3
s_lshl_b32 s83,s83,1
s_or_b32 s83,s83,1
v_mfma_f32_16x16x32_bf16 a[212:215],v[92:95],a[180:183],a[212:215]
s_mul_i32 s83,s83,163840
s_add_u32 s83,s83,s85
v_mfma_f32_16x16x32_bf16 a[212:215],v[88:91],a[164:167],a[212:215]
v_mfma_f32_16x16x32_bf16 a[212:215],v[84:87],a[148:151],a[212:215]
v_mfma_f32_16x16x32_bf16 a[212:215],v[80:83],a[132:135],a[212:215]
)A"
R"A(buffer_load_dwordx4 a[104:107],v28,s[28:31],s21 offen offset:3072 sc1
v_mfma_f32_16x16x32_bf16 a[216:219],v[80:83],a[136:139],a[216:219]
v_mfma_f32_16x16x32_bf16 a[216:219],v[84:87],a[152:155],a[216:219]
v_mfma_f32_16x16x32_bf16 a[216:219],v[88:91],a[168:171],a[216:219]
v_mfma_f32_16x16x32_bf16 a[216:219],v[92:95],a[184:187],a[216:219]
v_mfma_f32_16x16x32_bf16 a[220:223],v[92:95],a[188:191],a[220:223]
ds_read_b64_tr_b16 v[92:93],v2 offset:30880
ds_read_b64_tr_b16 v[94:95],v2 offset:36000
v_mfma_f32_16x16x32_bf16 a[220:223],v[88:91],a[172:175],a[220:223]
ds_read_b64_tr_b16 v[88:89],v2 offset:20640
v_mfma_f32_16x16x32_bf16 a[220:223],v[84:87],a[156:159],a[220:223]
buffer_load_dwordx4 v24,s[48:51],s84 offen lds
v_mfma_f32_16x16x32_bf16 a[220:223],v[80:83],a[140:143],a[220:223]
ds_read_b64_tr_b16 v[80:81],v2 offset:160
ds_read_b64_tr_b16 v[82:83],v2 offset:5280
s_barrier
v_mfma_f32_16x16x32_bf16 a[224:227],v[96:99],a[128:131],a[224:227]
ds_read_b64_tr_b16 v[84:85],v2 offset:10400
v_mfma_f32_16x16x32_bf16 a[224:227],v[100:103],a[144:147],a[224:227]
ds_read_b64_tr_b16 v[86:87],v2 offset:15520
v_mfma_f32_16x16x32_bf16 a[224:227],v[104:107],a[160:163],a[224:227]
buffer_store_dwordx2 v[8:9],v10,s[44:47],s18 offen offset:1280 sc1
v_mfma_f32_16x16x32_bf16 a[224:227],v[108:111],a[176:179],a[224:227]
ds_read_b64_tr_b16 v[90:91],v2 offset:25760
v_mfma_f32_16x16x32_bf16 a[228:231],v[108:111],a[180:183],a[228:231]
v_mfma_f32_16x16x32_bf16 a[228:231],v[104:107],a[164:167],a[228:231]
v_mfma_f32_16x16x32_bf16 a[228:231],v[100:103],a[148:151],a[228:231]
v_mfma_f32_16x16x32_bf16 a[228:231],v[96:99],a[132:135],a[228:231]
buffer_load_dwordx4 a[96:99],v28,s[28:31],s20 offen offset:3072 sc1
v_mfma_f32_16x16x32_bf16 a[232:235],v[96:99],a[136:139],a[232:235]
s_add_u32 s20,s64,1
s_and_b32 s20,s20,3
s_lshl_b32 s20,s20,1
v_mfma_f32_16x16x32_bf16 a[232:235],v[100:103],a[152:155],a[232:235]
s_or_b32 s20,s20,1
s_lshl_b32 s20,s20,16
s_add_u32 s21,s20,4096
v_mfma_f32_16x16x32_bf16 a[232:235],v[104:107],a[168:171],a[232:235]
s_add_u32 s22,s20,8192
s_add_u32 s23,s20,12288
v_mfma_f32_16x16x32_bf16 a[232:235],v[108:111],a[184:187],a[232:235]
v_mfma_f32_16x16x32_bf16 a[236:239],v[108:111],a[188:191],a[236:239]
ds_read_b64_tr_b16 v[108:109],v1 offset:30912
ds_read_b64_tr_b16 v[110:111],v1 offset:36032
v_mfma_f32_16x16x32_bf16 a[236:239],v[104:107],a[172:175],a[236:239]
ds_read_b64_tr_b16 v[104:105],v1 offset:20672
v_mfma_f32_16x16x32_bf16 a[236:239],v[100:103],a[156:159],a[236:239]
buffer_load_dwordx4 v25,s[48:51],s84 offen offset:1024 lds
v_mfma_f32_16x16x32_bf16 a[236:239],v[96:99],a[140:143],a[236:239]
s_waitcnt lgkmcnt(3)
ds_read_b64_tr_b16 v[96:97],v1 offset:192
ds_read_b64_tr_b16 v[98:99],v1 offset:5312
v_mfma_f32_16x16x32_bf16 a[240:243],v[112:115],a[128:131],a[240:243]
ds_read_b64_tr_b16 v[100:101],v1 offset:10432
v_mfma_f32_16x16x32_bf16 a[240:243],v[116:119],a[144:147],a[240:243]
ds_read_b64_tr_b16 v[102:103],v1 offset:15552
v_mfma_f32_16x16x32_bf16 a[240:243],v[120:123],a[160:163],a[240:243]
ds_read_b64_tr_b16 v[106:107],v1 offset:25792
v_mfma_f32_16x16x32_bf16 a[240:243],v[124:127],a[176:179],a[240:243]
v_mfma_f32_16x16x32_bf16 a[244:247],v[124:127],a[180:183],a[244:247]
v_mfma_f32_16x16x32_bf16 a[244:247],v[120:123],a[164:167],a[244:247]
buffer_load_dwordx4 v26,s[48:51],s84 offen offset:2048 lds
v_mfma_f32_16x16x32_bf16 a[244:247],v[116:119],a[148:151],a[244:247]
v_mfma_f32_16x16x32_bf16 a[244:247],v[112:115],a[132:135],a[244:247]
v_mfma_f32_16x16x32_bf16 a[248:251],v[112:115],a[136:139],a[248:251]
v_mfma_f32_16x16x32_bf16 a[248:251],v[116:119],a[152:155],a[248:251]
v_mfma_f32_16x16x32_bf16 a[248:251],v[120:123],a[168:171],a[248:251]
v_mfma_f32_16x16x32_bf16 a[248:251],v[124:127],a[184:187],a[248:251]
v_mfma_f32_16x16x32_bf16 a[252:255],v[124:127],a[188:191],a[252:255]
buffer_load_dwordx4 v27,s[48:51],s84 offen offset:3072 lds
v_mfma_f32_16x16x32_bf16 a[252:255],v[120:123],a[172:175],a[252:255]
s_add_u32 m0,s86,132096
ds_read_b64_tr_b16 v[120:121],v2 offset:20704
ds_read_b64_tr_b16 v[122:123],v2 offset:25824
v_mfma_f32_16x16x32_bf16 a[252:255],v[116:119],a[156:159],a[252:255]
ds_read_b64_tr_b16 v[116:117],v2 offset:10464
v_mfma_f32_16x16x32_bf16 a[252:255],v[112:115],a[140:143],a[252:255]
ds_read_b64_tr_b16 v[112:113],v2 offset:224
v_mfma_f32_16x16x32_bf16 a[0:3],v[64:67],a[128:131],a[0:3]
ds_read_b64_tr_b16 v[114:115],v2 offset:5344
v_mfma_f32_16x16x32_bf16 a[0:3],v[68:71],a[144:147],a[0:3]
ds_read_b64_tr_b16 v[118:119],v2 offset:15584
ds_read_b64_tr_b16 v[124:125],v2 offset:30944
v_mfma_f32_16x16x32_bf16 a[0:3],v[72:75],a[160:163],a[0:3]
ds_read_b64_tr_b16 v[126:127],v2 offset:36064
v_mfma_f32_16x16x32_bf16 a[0:3],v[76:79],a[176:179],a[0:3]
buffer_load_dwordx4 v7,s[48:51],s84 offen lds
v_mfma_f32_16x16x32_bf16 a[4:7],v[76:79],a[180:183],a[4:7]
s_add_u32 m0,s86,81920
s_add_u32 s84,s83,20480
v_mfma_f32_16x16x32_bf16 a[4:7],v[72:75],a[164:167],a[4:7]
v_mfma_f32_16x16x32_bf16 a[4:7],v[68:71],a[148:151],a[4:7]
v_mfma_f32_16x16x32_bf16 a[4:7],v[64:67],a[132:135],a[4:7]
v_mfma_f32_16x16x32_bf16 a[8:11],v[64:67],a[136:139],a[8:11]
v_mfma_f32_16x16x32_bf16 a[8:11],v[68:71],a[152:155],a[8:11]
v_mfma_f32_16x16x32_bf16 a[8:11],v[72:75],a[168:171],a[8:11]
buffer_load_dwordx4 v24,s[48:51],s83 offen lds
v_mfma_f32_16x16x32_bf16 a[8:11],v[76:79],a[184:187],a[8:11]
v_mfma_f32_16x16x32_bf16 a[12:15],v[76:79],a[188:191],a[12:15]
ds_read_b64_tr_b16 v[76:77],v1 offset:30976
ds_read_b64_tr_b16 v[78:79],v1 offset:36096
v_mfma_f32_16x16x32_bf16 a[12:15],v[72:75],a[172:175],a[12:15]
ds_read_b64_tr_b16 v[72:73],v1 offset:20736
v_mfma_f32_16x16x32_bf16 a[12:15],v[68:71],a[156:159],a[12:15]
ds_read_b64_tr_b16 v[68:69],v1 offset:10496
v_mfma_f32_16x16x32_bf16 a[12:15],v[64:67],a[140:143],a[12:15]
ds_read_b64_tr_b16 v[64:65],v1 offset:256
v_mfma_f32_16x16x32_bf16 a[16:19],v[80:83],a[128:131],a[16:19]
ds_read_b64_tr_b16 v[66:67],v1 offset:5376
ds_read_b64_tr_b16 v[70:71],v1 offset:15616
v_mfma_f32_16x16x32_bf16 a[16:19],v[84:87],a[144:147],a[16:19]
buffer_load_dwordx4 v25,s[48:51],s83 offen offset:1024 lds
v_mfma_f32_16x16x32_bf16 a[16:19],v[88:91],a[160:163],a[16:19]
ds_read_b64_tr_b16 v[74:75],v1 offset:25856
v_add_u32 v1,0xffff6000,v1
v_mfma_f32_16x16x32_bf16 a[16:19],v[92:95],a[176:179],a[16:19]
v_mfma_f32_16x16x32_bf16 a[20:23],v[92:95],a[180:183],a[20:23]
v_mfma_f32_16x16x32_bf16 a[20:23],v[88:91],a[164:167],a[20:23]
v_mfma_f32_16x16x32_bf16 a[20:23],v[84:87],a[148:151],a[20:23]
v_mfma_f32_16x16x32_bf16 a[20:23],v[80:83],a[132:135],a[20:23]
v_mfma_f32_16x16x32_bf16 a[24:27],v[80:83],a[136:139],a[24:27]
buffer_load_dwordx4 v26,s[48:51],s83 offen offset:2048 lds
v_mfma_f32_16x16x32_bf16 a[24:27],v[84:87],a[152:155],a[24:27]
v_mfma_f32_16x16x32_bf16 a[24:27],v[88:91],a[168:171],a[24:27]
v_mfma_f32_16x16x32_bf16 a[24:27],v[92:95],a[184:187],a[24:27]
v_mfma_f32_16x16x32_bf16 a[28:31],v[92:95],a[188:191],a[28:31]
ds_read_b64_tr_b16 v[92:93],v2 offset:31008
ds_read_b64_tr_b16 v[94:95],v2 offset:36128
v_mfma_f32_16x16x32_bf16 a[28:31],v[88:91],a[172:175],a[28:31]
ds_read_b64_tr_b16 v[88:89],v2 offset:20768
v_mfma_f32_16x16x32_bf16 a[28:31],v[84:87],a[156:159],a[28:31]
s_waitcnt lgkmcnt(3)
ds_read_b64_tr_b16 v[84:85],v2 offset:10528
v_mfma_f32_16x16x32_bf16 a[28:31],v[80:83],a[140:143],a[28:31]
buffer_load_dwordx4 v27,s[48:51],s83 offen offset:3072 lds
v_mfma_f32_16x16x32_bf16 v[128:131],v[96:99],a[128:131],v[128:131]
s_add_u32 m0,s86,86016
ds_read_b64_tr_b16 v[80:81],v2 offset:288
ds_read_b64_tr_b16 v[82:83],v2 offset:5408
v_mfma_f32_16x16x32_bf16 v[128:131],v[100:103],a[144:147],v[128:131]
ds_read_b64_tr_b16 v[86:87],v2 offset:15648
v_mfma_f32_16x16x32_bf16 v[128:131],v[104:107],a[160:163],v[128:131]
ds_read_b64_tr_b16 v[90:91],v2 offset:25888
v_add_u32 v2,0xffff6000,v2
v_mfma_f32_16x16x32_bf16 v[128:131],v[108:111],a[176:179],v[128:131]
v_mfma_f32_16x16x32_bf16 v[136:139],v[108:111],a[180:183],v[136:139]
v_mfma_f32_16x16x32_bf16 v[136:139],v[104:107],a[164:167],v[136:139]
v_mfma_f32_16x16x32_bf16 v[136:139],v[100:103],a[148:151],v[136:139]
buffer_load_dwordx4 v7,s[48:51],s83 offen lds
v_mfma_f32_16x16x32_bf16 v[136:139],v[96:99],a[132:135],v[136:139]
s_add_u32 m0,s86,87040
s_add_u32 s83,s64,2
s_and_b32 s83,s83,3
v_mfma_f32_16x16x32_bf16 v[144:147],v[96:99],a[136:139],v[144:147]
s_lshl_b32 s83,s83,1
s_or_b32 s83,s83,1
s_mul_i32 s83,s83,163840
v_mfma_f32_16x16x32_bf16 v[144:147],v[100:103],a[152:155],v[144:147]
s_add_u32 s83,s83,s85
v_mfma_f32_16x16x32_bf16 v[144:147],v[104:107],a[168:171],v[144:147]
v_mfma_f32_16x16x32_bf16 v[144:147],v[108:111],a[184:187],v[144:147]
v_mfma_f32_16x16x32_bf16 v[152:155],v[108:111],a[188:191],v[152:155]
ds_read_b64_tr_b16 v[108:109],v1 offset:30720
ds_read_b64_tr_b16 v[110:111],v1 offset:35840
v_mfma_f32_16x16x32_bf16 v[152:155],v[104:107],a[172:175],v[152:155]
buffer_load_dwordx4 v24,s[48:51],s84 offen lds
v_mfma_f32_16x16x32_bf16 v[152:155],v[100:103],a[156:159],v[152:155]
ds_read_b64_tr_b16 v[100:101],v1 offset:10240
ds_read_b64_tr_b16 v[102:103],v1 offset:15360
v_mfma_f32_16x16x32_bf16 v[152:155],v[96:99],a[140:143],v[152:155]
ds_read_b64_tr_b16 v[96:97],v1
s_waitcnt lgkmcnt(5)
s_barrier
v_mfma_f32_16x16x32_bf16 v[160:163],v[112:115],a[128:131],v[160:163]
ds_read_b64_tr_b16 v[98:99],v1 offset:5120
v_mfma_f32_16x16x32_bf16 v[160:163],v[116:119],a[144:147],v[160:163]
ds_read_b64_tr_b16 v[104:105],v1 offset:20480
v_mfma_f32_16x16x32_bf16 v[160:163],v[120:123],a[160:163],v[160:163]
ds_read_b64_tr_b16 v[106:107],v1 offset:25600
v_mfma_f32_16x16x32_bf16 v[160:163],v[124:127],a[176:179],v[160:163]
buffer_load_dwordx4 v25,s[48:51],s84 offen offset:1024 lds
v_mfma_f32_16x16x32_bf16 v[168:171],v[124:127],a[180:183],v[168:171]
v_mfma_f32_16x16x32_bf16 v[168:171],v[120:123],a[164:167],v[168:171]
L_pd21_%=:
s_cbranch_execz L_trtg2S_%=
v_mfma_f32_16x16x32_bf16 v[168:171],v[116:119],a[148:151],v[168:171]
v_mfma_f32_16x16x32_bf16 v[168:171],v[112:115],a[132:135],v[168:171]
v_mfma_f32_16x16x32_bf16 v[176:179],v[112:115],a[136:139],v[176:179]
v_mfma_f32_16x16x32_bf16 v[176:179],v[116:119],a[152:155],v[176:179]
v_mfma_f32_16x16x32_bf16 v[176:179],v[120:123],a[168:171],v[176:179]
buffer_load_dwordx4 v26,s[48:51],s84 offen offset:2048 lds
v_mfma_f32_16x16x32_bf16 v[176:179],v[124:127],a[184:187],v[176:179]
v_mfma_f32_16x16x32_bf16 v[184:187],v[124:127],a[188:191],v[184:187]
ds_read_b64_tr_b16 v[124:125],v2 offset:30752
ds_read_b64_tr_b16 v[126:127],v2 offset:35872
v_mfma_f32_16x16x32_bf16 v[184:187],v[120:123],a[172:175],v[184:187]
ds_read_b64_tr_b16 v[120:121],v2 offset:20512
v_mfma_f32_16x16x32_bf16 v[184:187],v[116:119],a[156:159],v[184:187]
ds_read_b64_tr_b16 v[116:117],v2 offset:10272
v_mfma_f32_16x16x32_bf16 v[184:187],v[112:115],a[140:143],v[184:187]
ds_read_b64_tr_b16 v[112:113],v2 offset:32
v_mfma_f32_16x16x32_bf16 v[192:195],v[64:67],a[128:131],v[192:195]
ds_read_b64_tr_b16 v[114:115],v2 offset:5152
ds_read_b64_tr_b16 v[118:119],v2 offset:15392
v_mfma_f32_16x16x32_bf16 v[192:195],v[68:71],a[144:147],v[192:195]
buffer_load_dwordx4 v27,s[48:51],s84 offen offset:3072 lds
v_mfma_f32_16x16x32_bf16 v[192:195],v[72:75],a[160:163],v[192:195]
s_add_u32 m0,s86,91136
ds_read_b64_tr_b16 v[122:123],v2 offset:25632
v_mfma_f32_16x16x32_bf16 v[192:195],v[76:79],a[176:179],v[192:195]
v_mfma_f32_16x16x32_bf16 v[200:203],v[76:79],a[180:183],v[200:203]
v_mfma_f32_16x16x32_bf16 v[200:203],v[72:75],a[164:167],v[200:203]
v_mfma_f32_16x16x32_bf16 v[200:203],v[68:71],a[148:151],v[200:203]
v_mfma_f32_16x16x32_bf16 v[200:203],v[64:67],a[132:135],v[200:203]
v_mfma_f32_16x16x32_bf16 v[208:211],v[64:67],a[136:139],v[208:211]
buffer_load_dwordx4 v7,s[48:51],s84 offen lds
v_mfma_f32_16x16x32_bf16 v[208:211],v[68:71],a[152:155],v[208:211]
s_add_u32 m0,s86,40960
s_add_u32 s84,s83,20480
v_mfma_f32_16x16x32_bf16 v[208:211],v[72:75],a[168:171],v[208:211]
v_mfma_f32_16x16x32_bf16 v[208:211],v[76:79],a[184:187],v[208:211]
v_mfma_f32_16x16x32_bf16 v[216:219],v[76:79],a[188:191],v[216:219]
ds_read_b64_tr_b16 v[76:77],v1 offset:30784
ds_read_b64_tr_b16 v[78:79],v1 offset:35904
)A"
R"A(v_mfma_f32_16x16x32_bf16 v[216:219],v[72:75],a[172:175],v[216:219]
buffer_load_dwordx2 v[4:5],v3,s[44:47],s91 offen offset:1280 sc1
v_mfma_f32_16x16x32_bf16 v[216:219],v[68:71],a[156:159],v[216:219]
ds_read_b64_tr_b16 v[68:69],v1 offset:10304
ds_read_b64_tr_b16 v[70:71],v1 offset:15424
v_mfma_f32_16x16x32_bf16 v[216:219],v[64:67],a[140:143],v[216:219]
ds_read_b64_tr_b16 v[64:65],v1 offset:64
v_mfma_f32_16x16x32_bf16 v[224:227],v[80:83],a[128:131],v[224:227]
ds_read_b64_tr_b16 v[66:67],v1 offset:5184
v_mfma_f32_16x16x32_bf16 v[224:227],v[84:87],a[144:147],v[224:227]
ds_read_b64_tr_b16 v[72:73],v1 offset:20544
v_mfma_f32_16x16x32_bf16 v[224:227],v[88:91],a[160:163],v[224:227]
buffer_load_dwordx4 v[32:35],v6,s[28:31],s87 offen sc1
v_mfma_f32_16x16x32_bf16 v[224:227],v[92:95],a[176:179],v[224:227]
ds_read_b64_tr_b16 v[74:75],v1 offset:25664
v_mfma_f32_16x16x32_bf16 v[232:235],v[92:95],a[180:183],v[232:235]
v_mfma_f32_16x16x32_bf16 v[232:235],v[88:91],a[164:167],v[232:235]
v_mfma_f32_16x16x32_bf16 v[232:235],v[84:87],a[148:151],v[232:235]
v_mfma_f32_16x16x32_bf16 v[232:235],v[80:83],a[132:135],v[232:235]
buffer_load_dwordx4 v[36:39],v6,s[28:31],s88 offen sc1
v_mfma_f32_16x16x32_bf16 v[240:243],v[80:83],a[136:139],v[240:243]
v_mfma_f32_16x16x32_bf16 v[240:243],v[84:87],a[152:155],v[240:243]
v_mfma_f32_16x16x32_bf16 v[240:243],v[88:91],a[168:171],v[240:243]
v_mfma_f32_16x16x32_bf16 v[240:243],v[92:95],a[184:187],v[240:243]
v_mfma_f32_16x16x32_bf16 v[248:251],v[92:95],a[188:191],v[248:251]
buffer_load_dwordx4 v[40:43],v6,s[28:31],s89 offen sc1
v_mfma_f32_16x16x32_bf16 v[248:251],v[88:91],a[172:175],v[248:251]
ds_read_b64_tr_b16 v[88:89],v2 offset:20576
ds_read_b64_tr_b16 v[90:91],v2 offset:25696
v_mfma_f32_16x16x32_bf16 v[248:251],v[84:87],a[156:159],v[248:251]
ds_read_b64_tr_b16 v[84:85],v2 offset:10336
v_mfma_f32_16x16x32_bf16 v[248:251],v[80:83],a[140:143],v[248:251]
s_waitcnt lgkmcnt(3)
ds_read_b64_tr_b16 v[80:81],v2 offset:96
v_mfma_f32_16x16x32_bf16 a[192:195],v[96:99],a[68:71],a[192:195]
ds_read_b64_tr_b16 v[82:83],v2 offset:5216
v_mfma_f32_16x16x32_bf16 a[192:195],v[100:103],a[76:79],a[192:195]
buffer_load_dwordx4 v[196:199],v6,s[28:31],s87 offen offset:2048 sc1
v_mfma_f32_16x16x32_bf16 a[192:195],v[104:107],a[84:87],a[192:195]
ds_read_b64_tr_b16 v[86:87],v2 offset:15456
ds_read_b64_tr_b16 v[92:93],v2 offset:30816
v_mfma_f32_16x16x32_bf16 a[192:195],v[108:111],a[92:95],a[192:195]
ds_read_b64_tr_b16 v[94:95],v2 offset:35936
v_mfma_f32_16x16x32_bf16 a[196:199],v[108:111],a[124:127],a[196:199]
v_mfma_f32_16x16x32_bf16 a[196:199],v[104:107],a[116:119],a[196:199]
v_mfma_f32_16x16x32_bf16 a[196:199],v[100:103],a[108:111],a[196:199]
buffer_load_dwordx4 v[44:47],v6,s[28:31],s90 offen sc1
v_mfma_f32_16x16x32_bf16 a[196:199],v[96:99],a[100:103],a[196:199]
v_mfma_f32_16x16x32_bf16 a[200:203],v[96:99],a[64:67],a[200:203]
v_mfma_f32_16x16x32_bf16 a[200:203],v[100:103],a[72:75],a[200:203]
s_waitcnt vmcnt(31)
v_mfma_f32_16x16x32_bf16 a[200:203],v[104:107],a[80:83],a[200:203]
s_waitcnt vmcnt(27)
v_mfma_f32_16x16x32_bf16 a[200:203],v[108:111],a[88:91],a[200:203]
buffer_load_dwordx4 v[212:215],v6,s[28:31],s88 offen offset:2048 sc1
v_mfma_f32_16x16x32_bf16 a[204:207],v[108:111],a[120:123],a[204:207]
s_waitcnt vmcnt(26)
ds_read_b64_tr_b16 v[108:109],v1 offset:30848
ds_read_b64_tr_b16 v[110:111],v1 offset:35968
v_mfma_f32_16x16x32_bf16 a[204:207],v[104:107],a[112:115],a[204:207]
s_waitcnt vmcnt(24)
ds_read_b64_tr_b16 v[104:105],v1 offset:20608
v_mfma_f32_16x16x32_bf16 a[204:207],v[100:103],a[104:107],a[204:207]
s_waitcnt vmcnt(21)
ds_read_b64_tr_b16 v[100:101],v1 offset:10368
v_mfma_f32_16x16x32_bf16 a[204:207],v[96:99],a[96:99],a[204:207]
ds_read_b64_tr_b16 v[96:97],v1 offset:128
v_mfma_f32_16x16x32_bf16 a[208:211],v[112:115],a[68:71],a[208:211]
buffer_load_dwordx4 v[180:183],v6,s[28:31],s90 offen offset:1024 sc1
v_mfma_f32_16x16x32_bf16 a[208:211],v[116:119],a[76:79],a[208:211]
ds_read_b64_tr_b16 v[98:99],v1 offset:5248
ds_read_b64_tr_b16 v[102:103],v1 offset:15488
v_mfma_f32_16x16x32_bf16 a[208:211],v[120:123],a[84:87],a[208:211]
ds_read_b64_tr_b16 v[106:107],v1 offset:25728
v_mfma_f32_16x16x32_bf16 a[208:211],v[124:127],a[92:95],a[208:211]
v_mfma_f32_16x16x32_bf16 a[212:215],v[124:127],a[124:127],a[212:215]
v_mfma_f32_16x16x32_bf16 a[212:215],v[120:123],a[116:119],a[212:215]
buffer_load_dwordx4 v[228:231],v6,s[28:31],s89 offen offset:2048 sc1
v_mfma_f32_16x16x32_bf16 a[212:215],v[116:119],a[108:111],a[212:215]
v_mfma_f32_16x16x32_bf16 a[212:215],v[112:115],a[100:103],a[212:215]
v_mfma_f32_16x16x32_bf16 a[216:219],v[112:115],a[64:67],a[216:219]
v_mfma_f32_16x16x32_bf16 a[216:219],v[116:119],a[72:75],a[216:219]
v_mfma_f32_16x16x32_bf16 a[216:219],v[120:123],a[80:83],a[216:219]
buffer_load_dwordx4 v[164:167],v6,s[28:31],s89 offen offset:1024 sc1
v_mfma_f32_16x16x32_bf16 a[216:219],v[124:127],a[88:91],a[216:219]
v_mfma_f32_16x16x32_bf16 a[220:223],v[124:127],a[120:123],a[220:223]
ds_read_b64_tr_b16 v[124:125],v2 offset:30880
ds_read_b64_tr_b16 v[126:127],v2 offset:36000
v_mfma_f32_16x16x32_bf16 a[220:223],v[120:123],a[112:115],a[220:223]
ds_read_b64_tr_b16 v[120:121],v2 offset:20640
v_mfma_f32_16x16x32_bf16 a[220:223],v[116:119],a[104:107],a[220:223]
ds_read_b64_tr_b16 v[116:117],v2 offset:10400
v_mfma_f32_16x16x32_bf16 a[220:223],v[112:115],a[96:99],a[220:223]
buffer_load_dwordx4 v[148:151],v6,s[28:31],s88 offen offset:1024 sc1
v_mfma_f32_16x16x32_bf16 a[224:227],v[64:67],a[68:71],a[224:227]
ds_read_b64_tr_b16 v[112:113],v2 offset:160
ds_read_b64_tr_b16 v[114:115],v2 offset:5280
v_mfma_f32_16x16x32_bf16 a[224:227],v[68:71],a[76:79],a[224:227]
ds_read_b64_tr_b16 v[118:119],v2 offset:15520
v_mfma_f32_16x16x32_bf16 a[224:227],v[72:75],a[84:87],a[224:227]
ds_read_b64_tr_b16 v[122:123],v2 offset:25760
v_mfma_f32_16x16x32_bf16 a[224:227],v[76:79],a[92:95],a[224:227]
v_mfma_f32_16x16x32_bf16 a[228:231],v[76:79],a[124:127],a[228:231]
buffer_load_dwordx4 v[244:247],v6,s[28:31],s90 offen offset:2048 sc1
v_mfma_f32_16x16x32_bf16 a[228:231],v[72:75],a[116:119],a[228:231]
v_mfma_f32_16x16x32_bf16 a[228:231],v[68:71],a[108:111],a[228:231]
v_mfma_f32_16x16x32_bf16 a[228:231],v[64:67],a[100:103],a[228:231]
v_mfma_f32_16x16x32_bf16 a[232:235],v[64:67],a[64:67],a[232:235]
v_mfma_f32_16x16x32_bf16 a[232:235],v[68:71],a[72:75],a[232:235]
buffer_load_dwordx4 v[188:191],v28,s[28:31],s90 offen offset:3072 sc1
v_mfma_f32_16x16x32_bf16 a[232:235],v[72:75],a[80:83],a[232:235]
s_waitcnt vmcnt(12)
v_cmp_ne_u32 s[56:57],s52,v4
v_cmp_ne_u32 s[58:59],s53,v5
v_mfma_f32_16x16x32_bf16 a[232:235],v[76:79],a[88:91],a[232:235]
s_or_b64 s[56:57],s[56:57],s[58:59]
s_cbranch_scc1 L_fr7_%=
L_fo7_%=:
s_lshl_b32 s91,s64,6
v_mfma_f32_16x16x32_bf16 a[236:239],v[76:79],a[120:123],a[236:239]
ds_read_b64_tr_b16 v[76:77],v1 offset:30912
ds_read_b64_tr_b16 v[78:79],v1 offset:36032
v_mfma_f32_16x16x32_bf16 a[236:239],v[72:75],a[112:115],a[236:239]
ds_read_b64_tr_b16 v[72:73],v1 offset:20672
v_bfe_u32 v3,v6,4,2
v_mfma_f32_16x16x32_bf16 a[236:239],v[68:71],a[104:107],a[236:239]
buffer_load_dwordx4 a[128:131],v6,s[28:31],s20 offen sc1
v_mfma_f32_16x16x32_bf16 a[236:239],v[64:67],a[96:99],a[236:239]
s_waitcnt lgkmcnt(3)
ds_read_b64_tr_b16 v[64:65],v1 offset:192
ds_read_b64_tr_b16 v[66:67],v1 offset:5312
v_mfma_f32_16x16x32_bf16 a[240:243],v[80:83],a[68:71],a[240:243]
ds_read_b64_tr_b16 v[68:69],v1 offset:10432
v_min_u32 v3,2,v3
v_mfma_f32_16x16x32_bf16 a[240:243],v[84:87],a[76:79],a[240:243]
ds_read_b64_tr_b16 v[70:71],v1 offset:15552
v_add3_u32 v3,v3,s63,1
v_mfma_f32_16x16x32_bf16 a[240:243],v[88:91],a[84:87],a[240:243]
ds_read_b64_tr_b16 v[74:75],v1 offset:25792
v_and_b32 v3,3,v3
v_mfma_f32_16x16x32_bf16 a[240:243],v[92:95],a[92:95],a[240:243]
buffer_load_dwordx4 v[132:135],v6,s[28:31],s87 offen offset:1024 sc1
v_mfma_f32_16x16x32_bf16 a[244:247],v[92:95],a[124:127],a[244:247]
v_lshlrev_b32 v3,8,v3
v_mfma_f32_16x16x32_bf16 a[244:247],v[88:91],a[116:119],a[244:247]
v_mfma_f32_16x16x32_bf16 a[244:247],v[84:87],a[108:111],a[244:247]
v_mfma_f32_16x16x32_bf16 a[244:247],v[80:83],a[100:103],a[244:247]
v_mfma_f32_16x16x32_bf16 a[248:251],v[80:83],a[64:67],a[248:251]
buffer_load_dwordx4 v[172:175],v28,s[28:31],s89 offen offset:3072 sc1
v_mfma_f32_16x16x32_bf16 a[248:251],v[84:87],a[72:75],a[248:251]
v_mfma_f32_16x16x32_bf16 a[248:251],v[88:91],a[80:83],a[248:251]
v_mfma_f32_16x16x32_bf16 a[248:251],v[92:95],a[88:91],a[248:251]
v_mfma_f32_16x16x32_bf16 a[252:255],v[92:95],a[120:123],a[252:255]
ds_read_b64_tr_b16 v[92:93],v2 offset:30944
ds_read_b64_tr_b16 v[94:95],v2 offset:36064
v_mfma_f32_16x16x32_bf16 a[252:255],v[88:91],a[112:115],a[252:255]
buffer_load_dwordx4 a[144:147],v6,s[28:31],s21 offen sc1
v_mfma_f32_16x16x32_bf16 a[252:255],v[84:87],a[104:107],a[252:255]
ds_read_b64_tr_b16 v[84:85],v2 offset:10464
ds_read_b64_tr_b16 v[86:87],v2 offset:15584
v_mfma_f32_16x16x32_bf16 a[252:255],v[80:83],a[96:99],a[252:255]
ds_read_b64_tr_b16 v[80:81],v2 offset:224
v_mfma_f32_16x16x32_bf16 a[0:3],v[96:99],a[68:71],a[0:3]
ds_read_b64_tr_b16 v[82:83],v2 offset:5344
v_mfma_f32_16x16x32_bf16 a[0:3],v[100:103],a[76:79],a[0:3]
ds_read_b64_tr_b16 v[88:89],v2 offset:20704
v_mfma_f32_16x16x32_bf16 a[0:3],v[104:107],a[84:87],a[0:3]
buffer_load_dwordx4 v[156:159],v28,s[28:31],s88 offen offset:3072 sc1
v_mfma_f32_16x16x32_bf16 a[0:3],v[108:111],a[92:95],a[0:3]
ds_read_b64_tr_b16 v[90:91],v2 offset:25824
v_mfma_f32_16x16x32_bf16 a[4:7],v[108:111],a[124:127],a[4:7]
v_mfma_f32_16x16x32_bf16 a[4:7],v[104:107],a[116:119],a[4:7]
v_mfma_f32_16x16x32_bf16 a[4:7],v[100:103],a[108:111],a[4:7]
v_mfma_f32_16x16x32_bf16 a[4:7],v[96:99],a[100:103],a[4:7]
buffer_load_dwordx4 a[160:163],v6,s[28:31],s22 offen sc1
v_mfma_f32_16x16x32_bf16 a[8:11],v[96:99],a[64:67],a[8:11]
v_mfma_f32_16x16x32_bf16 a[8:11],v[100:103],a[72:75],a[8:11]
v_mfma_f32_16x16x32_bf16 a[8:11],v[104:107],a[80:83],a[8:11]
v_mfma_f32_16x16x32_bf16 a[8:11],v[108:111],a[88:91],a[8:11]
v_mfma_f32_16x16x32_bf16 a[12:15],v[108:111],a[120:123],a[12:15]
buffer_load_dwordx4 v[140:143],v28,s[28:31],s87 offen offset:3072 sc1
v_mfma_f32_16x16x32_bf16 a[12:15],v[104:107],a[112:115],a[12:15]
s_add_u32 s87,s64,2
ds_read_b64_tr_b16 v[104:105],v1 offset:20736
ds_read_b64_tr_b16 v[106:107],v1 offset:25856
v_mfma_f32_16x16x32_bf16 a[12:15],v[100:103],a[104:107],a[12:15]
s_and_b32 s87,s87,3
s_lshl_b32 s87,s87,1
ds_read_b64_tr_b16 v[100:101],v1 offset:10496
v_mfma_f32_16x16x32_bf16 a[12:15],v[96:99],a[96:99],a[12:15]
s_or_b32 s87,s87,1
s_lshl_b32 s87,s87,16
ds_read_b64_tr_b16 v[96:97],v1 offset:256
v_mfma_f32_16x16x32_bf16 a[16:19],v[112:115],a[68:71],a[16:19]
s_add_u32 s89,s87,8192
s_add_u32 s90,s87,12288
ds_read_b64_tr_b16 v[98:99],v1 offset:5376
v_mfma_f32_16x16x32_bf16 a[16:19],v[116:119],a[76:79],a[16:19]
buffer_load_dwordx4 a[176:179],v6,s[28:31],s23 offen sc1
v_mfma_f32_16x16x32_bf16 a[16:19],v[120:123],a[84:87],a[16:19]
s_add_u32 s88,s87,4096
ds_read_b64_tr_b16 v[102:103],v1 offset:15616
ds_read_b64_tr_b16 v[108:109],v1 offset:30976
v_mfma_f32_16x16x32_bf16 a[16:19],v[124:127],a[92:95],a[16:19]
ds_read_b64_tr_b16 v[110:111],v1 offset:36096
v_add_u32 v1,0x1e000,v1
v_mfma_f32_16x16x32_bf16 a[20:23],v[124:127],a[124:127],a[20:23]
v_mfma_f32_16x16x32_bf16 a[20:23],v[120:123],a[116:119],a[20:23]
v_mfma_f32_16x16x32_bf16 a[20:23],v[116:119],a[108:111],a[20:23]
v_mfma_f32_16x16x32_bf16 a[20:23],v[112:115],a[100:103],a[20:23]
v_mfma_f32_16x16x32_bf16 a[24:27],v[112:115],a[64:67],a[24:27]
buffer_load_dwordx4 v24,s[48:51],s83 offen lds
v_mfma_f32_16x16x32_bf16 a[24:27],v[116:119],a[72:75],a[24:27]
v_mfma_f32_16x16x32_bf16 a[24:27],v[120:123],a[80:83],a[24:27]
v_mfma_f32_16x16x32_bf16 a[24:27],v[124:127],a[88:91],a[24:27]
v_mfma_f32_16x16x32_bf16 a[28:31],v[124:127],a[120:123],a[28:31]
ds_read_b64_tr_b16 v[124:125],v2 offset:31008
ds_read_b64_tr_b16 v[126:127],v2 offset:36128
v_mfma_f32_16x16x32_bf16 a[28:31],v[120:123],a[112:115],a[28:31]
buffer_load_dwordx4 a[180:183],v6,s[28:31],s23 offen offset:1024 sc1
v_mfma_f32_16x16x32_bf16 a[28:31],v[116:119],a[104:107],a[28:31]
)A"
R"A(ds_read_b64_tr_b16 v[116:117],v2 offset:10528
ds_read_b64_tr_b16 v[118:119],v2 offset:15648
v_mfma_f32_16x16x32_bf16 a[28:31],v[112:115],a[96:99],a[28:31]
s_waitcnt lgkmcnt(4)
ds_read_b64_tr_b16 v[112:113],v2 offset:288
v_mfma_f32_16x16x32_bf16 v[128:131],v[64:67],a[68:71],v[128:131]
ds_read_b64_tr_b16 v[114:115],v2 offset:5408
v_mfma_f32_16x16x32_bf16 v[128:131],v[68:71],a[76:79],v[128:131]
ds_read_b64_tr_b16 v[120:121],v2 offset:20768
v_mfma_f32_16x16x32_bf16 v[128:131],v[72:75],a[84:87],v[128:131]
ds_read_b64_tr_b16 v[122:123],v2 offset:25888
v_add_u32 v2,0x1e000,v2
v_mfma_f32_16x16x32_bf16 v[128:131],v[76:79],a[92:95],v[128:131]
v_mfma_f32_16x16x32_bf16 v[136:139],v[76:79],a[124:127],v[136:139]
buffer_load_dwordx4 v25,s[48:51],s83 offen offset:1024 lds
v_mfma_f32_16x16x32_bf16 v[136:139],v[72:75],a[116:119],v[136:139]
v_mfma_f32_16x16x32_bf16 v[136:139],v[68:71],a[108:111],v[136:139]
v_mfma_f32_16x16x32_bf16 v[136:139],v[64:67],a[100:103],v[136:139]
v_mfma_f32_16x16x32_bf16 v[144:147],v[64:67],a[64:67],v[144:147]
v_mfma_f32_16x16x32_bf16 v[144:147],v[68:71],a[72:75],v[144:147]
buffer_load_dwordx4 a[164:167],v6,s[28:31],s22 offen offset:1024 sc1
v_mfma_f32_16x16x32_bf16 v[144:147],v[72:75],a[80:83],v[144:147]
v_mfma_f32_16x16x32_bf16 v[144:147],v[76:79],a[88:91],v[144:147]
v_mfma_f32_16x16x32_bf16 v[152:155],v[76:79],a[120:123],v[152:155]
v_mfma_f32_16x16x32_bf16 v[152:155],v[72:75],a[112:115],v[152:155]
v_mfma_f32_16x16x32_bf16 v[152:155],v[68:71],a[104:107],v[152:155]
v_mfma_f32_16x16x32_bf16 v[152:155],v[64:67],a[96:99],v[152:155]
s_barrier
v_mfma_f32_16x16x32_bf16 v[160:163],v[80:83],a[68:71],v[160:163]
buffer_load_dwordx4 v26,s[48:51],s83 offen offset:2048 lds
v_mfma_f32_16x16x32_bf16 v[160:163],v[84:87],a[76:79],v[160:163]
ds_read_b64_tr_b16 v[64:65],v1
ds_read_b64_tr_b16 v[66:67],v1 offset:5120
v_mfma_f32_16x16x32_bf16 v[160:163],v[88:91],a[84:87],v[160:163]
ds_read_b64_tr_b16 v[68:69],v1 offset:10240
v_mfma_f32_16x16x32_bf16 v[160:163],v[92:95],a[92:95],v[160:163]
ds_read_b64_tr_b16 v[70:71],v1 offset:15360
v_mfma_f32_16x16x32_bf16 v[168:171],v[92:95],a[124:127],v[168:171]
ds_read_b64_tr_b16 v[72:73],v1 offset:20480
v_mfma_f32_16x16x32_bf16 v[168:171],v[88:91],a[116:119],v[168:171]
buffer_load_dwordx4 a[148:151],v6,s[28:31],s21 offen offset:1024 sc1
v_mfma_f32_16x16x32_bf16 v[168:171],v[84:87],a[108:111],v[168:171]
ds_read_b64_tr_b16 v[74:75],v1 offset:25600
ds_read_b64_tr_b16 v[76:77],v1 offset:30720
v_mfma_f32_16x16x32_bf16 v[168:171],v[80:83],a[100:103],v[168:171]
ds_read_b64_tr_b16 v[78:79],v1 offset:35840
v_mfma_f32_16x16x32_bf16 v[176:179],v[80:83],a[64:67],v[176:179]
v_mfma_f32_16x16x32_bf16 v[176:179],v[84:87],a[72:75],v[176:179]
v_mfma_f32_16x16x32_bf16 v[176:179],v[88:91],a[80:83],v[176:179]
v_mfma_f32_16x16x32_bf16 v[176:179],v[92:95],a[88:91],v[176:179]
v_mfma_f32_16x16x32_bf16 v[184:187],v[92:95],a[120:123],v[184:187]
buffer_load_dwordx4 v27,s[48:51],s83 offen offset:3072 lds
v_mfma_f32_16x16x32_bf16 v[184:187],v[88:91],a[112:115],v[184:187]
s_add_u32 m0,s86,45056
ds_read_b64_tr_b16 v[88:89],v2 offset:20512
ds_read_b64_tr_b16 v[90:91],v2 offset:25632
v_mfma_f32_16x16x32_bf16 v[184:187],v[84:87],a[104:107],v[184:187]
ds_read_b64_tr_b16 v[84:85],v2 offset:10272
v_mfma_f32_16x16x32_bf16 v[184:187],v[80:83],a[96:99],v[184:187]
ds_read_b64_tr_b16 v[80:81],v2 offset:32
v_mfma_f32_16x16x32_bf16 v[192:195],v[96:99],a[68:71],v[192:195]
ds_read_b64_tr_b16 v[82:83],v2 offset:5152
v_mfma_f32_16x16x32_bf16 v[192:195],v[100:103],a[76:79],v[192:195]
buffer_load_dwordx4 a[132:135],v6,s[28:31],s20 offen offset:1024 sc1
v_mfma_f32_16x16x32_bf16 v[192:195],v[104:107],a[84:87],v[192:195]
ds_read_b64_tr_b16 v[86:87],v2 offset:15392
ds_read_b64_tr_b16 v[92:93],v2 offset:30752
v_mfma_f32_16x16x32_bf16 v[192:195],v[108:111],a[92:95],v[192:195]
ds_read_b64_tr_b16 v[94:95],v2 offset:35872
v_mfma_f32_16x16x32_bf16 v[200:203],v[108:111],a[124:127],v[200:203]
v_mfma_f32_16x16x32_bf16 v[200:203],v[104:107],a[116:119],v[200:203]
v_mfma_f32_16x16x32_bf16 v[200:203],v[100:103],a[108:111],v[200:203]
v_mfma_f32_16x16x32_bf16 v[200:203],v[96:99],a[100:103],v[200:203]
v_mfma_f32_16x16x32_bf16 v[208:211],v[96:99],a[64:67],v[208:211]
buffer_load_dwordx4 v7,s[48:51],s83 offen lds
v_mfma_f32_16x16x32_bf16 v[208:211],v[100:103],a[72:75],v[208:211]
s_add_u32 m0,s86,46080
s_mul_i32 s83,s63,20480
v_mfma_f32_16x16x32_bf16 v[208:211],v[104:107],a[80:83],v[208:211]
v_mfma_f32_16x16x32_bf16 v[208:211],v[108:111],a[88:91],v[208:211]
v_mfma_f32_16x16x32_bf16 v[216:219],v[108:111],a[120:123],v[216:219]
ds_read_b64_tr_b16 v[108:109],v1 offset:30784
ds_read_b64_tr_b16 v[110:111],v1 offset:35904
v_mfma_f32_16x16x32_bf16 v[216:219],v[104:107],a[112:115],v[216:219]
buffer_load_dwordx4 a[136:139],v6,s[28:31],s20 offen offset:2048 sc1
v_mfma_f32_16x16x32_bf16 v[216:219],v[100:103],a[104:107],v[216:219]
ds_read_b64_tr_b16 v[100:101],v1 offset:10304
ds_read_b64_tr_b16 v[102:103],v1 offset:15424
v_mfma_f32_16x16x32_bf16 v[216:219],v[96:99],a[96:99],v[216:219]
s_waitcnt lgkmcnt(4)
ds_read_b64_tr_b16 v[96:97],v1 offset:64
v_mfma_f32_16x16x32_bf16 v[224:227],v[112:115],a[68:71],v[224:227]
ds_read_b64_tr_b16 v[98:99],v1 offset:5184
v_mfma_f32_16x16x32_bf16 v[224:227],v[116:119],a[76:79],v[224:227]
ds_read_b64_tr_b16 v[104:105],v1 offset:20544
v_mfma_f32_16x16x32_bf16 v[224:227],v[120:123],a[84:87],v[224:227]
ds_read_b64_tr_b16 v[106:107],v1 offset:25664
v_mfma_f32_16x16x32_bf16 v[224:227],v[124:127],a[92:95],v[224:227]
v_mfma_f32_16x16x32_bf16 v[232:235],v[124:127],a[124:127],v[232:235]
buffer_load_dwordx4 v24,s[48:51],s84 offen lds
v_mfma_f32_16x16x32_bf16 v[232:235],v[120:123],a[116:119],v[232:235]
v_mfma_f32_16x16x32_bf16 v[232:235],v[116:119],a[108:111],v[232:235]
v_mfma_f32_16x16x32_bf16 v[232:235],v[112:115],a[100:103],v[232:235]
v_mfma_f32_16x16x32_bf16 v[240:243],v[112:115],a[64:67],v[240:243]
v_mfma_f32_16x16x32_bf16 v[240:243],v[116:119],a[72:75],v[240:243]
buffer_load_dwordx4 a[152:155],v6,s[28:31],s21 offen offset:2048 sc1
v_mfma_f32_16x16x32_bf16 v[240:243],v[120:123],a[80:83],v[240:243]
v_mfma_f32_16x16x32_bf16 v[240:243],v[124:127],a[88:91],v[240:243]
v_mfma_f32_16x16x32_bf16 v[248:251],v[124:127],a[120:123],v[248:251]
ds_read_b64_tr_b16 v[124:125],v2 offset:30816
ds_read_b64_tr_b16 v[126:127],v2 offset:35936
v_mfma_f32_16x16x32_bf16 v[248:251],v[120:123],a[112:115],v[248:251]
ds_read_b64_tr_b16 v[120:121],v2 offset:20576
v_mfma_f32_16x16x32_bf16 v[248:251],v[116:119],a[104:107],v[248:251]
ds_read_b64_tr_b16 v[116:117],v2 offset:10336
v_mfma_f32_16x16x32_bf16 v[248:251],v[112:115],a[96:99],v[248:251]
s_waitcnt vmcnt(19)
ds_read_b64_tr_b16 v[112:113],v2 offset:96
v_mfma_f32_16x16x32_bf16 a[192:195],v[64:67],v[32:35],a[192:195]
buffer_load_dwordx4 v25,s[48:51],s84 offen offset:1024 lds
v_mfma_f32_16x16x32_bf16 a[192:195],v[68:71],v[36:39],a[192:195]
ds_read_b64_tr_b16 v[114:115],v2 offset:5216
ds_read_b64_tr_b16 v[118:119],v2 offset:15456
v_mfma_f32_16x16x32_bf16 a[192:195],v[72:75],v[40:43],a[192:195]
ds_read_b64_tr_b16 v[122:123],v2 offset:25696
v_mfma_f32_16x16x32_bf16 a[192:195],v[76:79],v[44:47],a[192:195]
v_mfma_f32_16x16x32_bf16 a[196:199],v[76:79],v[180:183],a[196:199]
v_mfma_f32_16x16x32_bf16 a[196:199],v[72:75],v[164:167],a[196:199]
buffer_load_dwordx4 a[168:171],v6,s[28:31],s22 offen offset:2048 sc1
v_mfma_f32_16x16x32_bf16 a[196:199],v[68:71],v[148:151],a[196:199]
s_waitcnt vmcnt(19)
v_mfma_f32_16x16x32_bf16 a[196:199],v[64:67],v[132:135],a[196:199]
v_mfma_f32_16x16x32_bf16 a[200:203],v[64:67],v[196:199],a[200:203]
v_mfma_f32_16x16x32_bf16 a[200:203],v[68:71],v[212:215],a[200:203]
v_mfma_f32_16x16x32_bf16 a[200:203],v[72:75],v[228:231],a[200:203]
v_mfma_f32_16x16x32_bf16 a[200:203],v[76:79],v[244:247],a[200:203]
v_mfma_f32_16x16x32_bf16 a[204:207],v[76:79],v[188:191],a[204:207]
buffer_load_dwordx4 v26,s[48:51],s84 offen offset:2048 lds
v_mfma_f32_16x16x32_bf16 a[204:207],v[72:75],v[172:175],a[204:207]
s_waitcnt vmcnt(18)
ds_read_b64_tr_b16 v[72:73],v1 offset:20608
ds_read_b64_tr_b16 v[74:75],v1 offset:25728
v_mfma_f32_16x16x32_bf16 a[204:207],v[68:71],v[156:159],a[204:207]
s_waitcnt vmcnt(16)
ds_read_b64_tr_b16 v[68:69],v1 offset:10368
v_mfma_f32_16x16x32_bf16 a[204:207],v[64:67],v[140:143],a[204:207]
ds_read_b64_tr_b16 v[64:65],v1 offset:128
v_mfma_f32_16x16x32_bf16 a[208:211],v[80:83],v[32:35],a[208:211]
ds_read_b64_tr_b16 v[66:67],v1 offset:5248
v_mfma_f32_16x16x32_bf16 a[208:211],v[84:87],v[36:39],a[208:211]
buffer_load_dwordx4 a[184:187],v6,s[28:31],s23 offen offset:2048 sc1
v_mfma_f32_16x16x32_bf16 a[208:211],v[88:91],v[40:43],a[208:211]
ds_read_b64_tr_b16 v[70:71],v1 offset:15488
ds_read_b64_tr_b16 v[76:77],v1 offset:30848
v_mfma_f32_16x16x32_bf16 a[208:211],v[92:95],v[44:47],a[208:211]
ds_read_b64_tr_b16 v[78:79],v1 offset:35968
v_mfma_f32_16x16x32_bf16 a[212:215],v[92:95],v[180:183],a[212:215]
v_mfma_f32_16x16x32_bf16 a[212:215],v[88:91],v[164:167],a[212:215]
v_mfma_f32_16x16x32_bf16 a[212:215],v[84:87],v[148:151],a[212:215]
v_mfma_f32_16x16x32_bf16 a[212:215],v[80:83],v[132:135],a[212:215]
v_mfma_f32_16x16x32_bf16 a[216:219],v[80:83],v[196:199],a[216:219]
buffer_load_dwordx4 v27,s[48:51],s84 offen offset:3072 lds
v_mfma_f32_16x16x32_bf16 a[216:219],v[84:87],v[212:215],a[216:219]
s_add_u32 m0,s86,50176
v_mfma_f32_16x16x32_bf16 a[216:219],v[88:91],v[228:231],a[216:219]
v_mfma_f32_16x16x32_bf16 a[216:219],v[92:95],v[244:247],a[216:219]
v_mfma_f32_16x16x32_bf16 a[220:223],v[92:95],v[188:191],a[220:223]
ds_read_b64_tr_b16 v[92:93],v2 offset:30880
ds_read_b64_tr_b16 v[94:95],v2 offset:36000
v_mfma_f32_16x16x32_bf16 a[220:223],v[88:91],v[172:175],a[220:223]
buffer_load_dwordx4 a[188:191],v28,s[28:31],s23 offen offset:3072 sc1
v_mfma_f32_16x16x32_bf16 a[220:223],v[84:87],v[156:159],a[220:223]
ds_read_b64_tr_b16 v[84:85],v2 offset:10400
ds_read_b64_tr_b16 v[86:87],v2 offset:15520
v_mfma_f32_16x16x32_bf16 a[220:223],v[80:83],v[140:143],a[220:223]
s_waitcnt lgkmcnt(4)
ds_read_b64_tr_b16 v[80:81],v2 offset:160
v_mfma_f32_16x16x32_bf16 a[224:227],v[96:99],v[32:35],a[224:227]
ds_read_b64_tr_b16 v[82:83],v2 offset:5280
v_mfma_f32_16x16x32_bf16 a[224:227],v[100:103],v[36:39],a[224:227]
ds_read_b64_tr_b16 v[88:89],v2 offset:20640
v_mfma_f32_16x16x32_bf16 a[224:227],v[104:107],v[40:43],a[224:227]
ds_read_b64_tr_b16 v[90:91],v2 offset:25760
v_mfma_f32_16x16x32_bf16 a[224:227],v[108:111],v[44:47],a[224:227]
v_mfma_f32_16x16x32_bf16 a[228:231],v[108:111],v[180:183],a[228:231]
buffer_load_dwordx4 v7,s[48:51],s84 offen lds
v_mfma_f32_16x16x32_bf16 a[228:231],v[104:107],v[164:167],a[228:231]
v_mfma_f32_16x16x32_bf16 a[228:231],v[100:103],v[148:151],a[228:231]
v_mfma_f32_16x16x32_bf16 a[228:231],v[96:99],v[132:135],a[228:231]
v_mfma_f32_16x16x32_bf16 a[232:235],v[96:99],v[196:199],a[232:235]
v_mfma_f32_16x16x32_bf16 a[232:235],v[100:103],v[212:215],a[232:235]
buffer_load_dwordx4 a[172:175],v28,s[28:31],s22 offen offset:3072 sc1
v_mfma_f32_16x16x32_bf16 a[232:235],v[104:107],v[228:231],a[232:235]
v_mfma_f32_16x16x32_bf16 a[232:235],v[108:111],v[244:247],a[232:235]
v_mfma_f32_16x16x32_bf16 a[236:239],v[108:111],v[188:191],a[236:239]
ds_read_b64_tr_b16 v[108:109],v1 offset:30912
ds_read_b64_tr_b16 v[110:111],v1 offset:36032
v_mfma_f32_16x16x32_bf16 a[236:239],v[104:107],v[172:175],a[236:239]
ds_read_b64_tr_b16 v[104:105],v1 offset:20672
v_mfma_f32_16x16x32_bf16 a[236:239],v[100:103],v[156:159],a[236:239]
buffer_load_dwordx4 a[84:87],v6,s[28:31],s89 offen sc1
v_mfma_f32_16x16x32_bf16 a[236:239],v[96:99],v[140:143],a[236:239]
ds_read_b64_tr_b16 v[96:97],v1 offset:192
ds_read_b64_tr_b16 v[98:99],v1 offset:5312
v_mfma_f32_16x16x32_bf16 a[240:243],v[112:115],v[32:35],a[240:243]
ds_read_b64_tr_b16 v[100:101],v1 offset:10432
v_mfma_f32_16x16x32_bf16 a[240:243],v[116:119],v[36:39],a[240:243]
ds_read_b64_tr_b16 v[102:103],v1 offset:15552
v_mfma_f32_16x16x32_bf16 a[240:243],v[120:123],v[40:43],a[240:243]
ds_read_b64_tr_b16 v[106:107],v1 offset:25792
v_add_u32 v1,0xffff6000,v1
v_mfma_f32_16x16x32_bf16 a[240:243],v[124:127],v[44:47],a[240:243]
)A"
R"A(buffer_load_dwordx4 a[92:95],v6,s[28:31],s90 offen sc1
v_mfma_f32_16x16x32_bf16 a[244:247],v[124:127],v[180:183],a[244:247]
v_mfma_f32_16x16x32_bf16 a[244:247],v[120:123],v[164:167],a[244:247]
v_mfma_f32_16x16x32_bf16 a[244:247],v[116:119],v[148:151],a[244:247]
v_mfma_f32_16x16x32_bf16 a[244:247],v[112:115],v[132:135],a[244:247]
v_mfma_f32_16x16x32_bf16 a[248:251],v[112:115],v[196:199],a[248:251]
buffer_load_dwordx4 a[156:159],v28,s[28:31],s21 offen offset:3072 sc1
v_mfma_f32_16x16x32_bf16 a[248:251],v[116:119],v[212:215],a[248:251]
v_mfma_f32_16x16x32_bf16 a[248:251],v[120:123],v[228:231],a[248:251]
v_mfma_f32_16x16x32_bf16 a[248:251],v[124:127],v[244:247],a[248:251]
v_mfma_f32_16x16x32_bf16 a[252:255],v[124:127],v[188:191],a[252:255]
ds_read_b64_tr_b16 v[124:125],v2 offset:30944
ds_read_b64_tr_b16 v[126:127],v2 offset:36064
v_mfma_f32_16x16x32_bf16 a[252:255],v[120:123],v[172:175],a[252:255]
buffer_load_dwordx4 a[68:71],v6,s[28:31],s87 offen sc1
v_mfma_f32_16x16x32_bf16 a[252:255],v[116:119],v[156:159],a[252:255]
ds_read_b64_tr_b16 v[116:117],v2 offset:10464
ds_read_b64_tr_b16 v[118:119],v2 offset:15584
v_mfma_f32_16x16x32_bf16 a[252:255],v[112:115],v[140:143],a[252:255]
ds_read_b64_tr_b16 v[112:113],v2 offset:224
v_mfma_f32_16x16x32_bf16 a[0:3],v[64:67],v[32:35],a[0:3]
ds_read_b64_tr_b16 v[114:115],v2 offset:5344
v_mfma_f32_16x16x32_bf16 a[0:3],v[68:71],v[36:39],a[0:3]
ds_read_b64_tr_b16 v[120:121],v2 offset:20704
v_mfma_f32_16x16x32_bf16 a[0:3],v[72:75],v[40:43],a[0:3]
buffer_load_dwordx4 a[140:143],v28,s[28:31],s20 offen offset:3072 sc1
v_mfma_f32_16x16x32_bf16 a[0:3],v[76:79],v[44:47],a[0:3]
ds_read_b64_tr_b16 v[122:123],v2 offset:25824
v_add_u32 v2,0xffff6000,v2
v_mfma_f32_16x16x32_bf16 a[4:7],v[76:79],v[180:183],a[4:7]
v_mfma_f32_16x16x32_bf16 a[4:7],v[72:75],v[164:167],a[4:7]
v_mfma_f32_16x16x32_bf16 a[4:7],v[68:71],v[148:151],a[4:7]
v_mfma_f32_16x16x32_bf16 a[4:7],v[64:67],v[132:135],a[4:7]
buffer_load_dwordx4 a[124:127],v6,s[28:31],s90 offen offset:1024 sc1
v_mfma_f32_16x16x32_bf16 a[8:11],v[64:67],v[196:199],a[8:11]
v_mfma_f32_16x16x32_bf16 a[8:11],v[68:71],v[212:215],a[8:11]
v_mfma_f32_16x16x32_bf16 a[8:11],v[72:75],v[228:231],a[8:11]
v_mfma_f32_16x16x32_bf16 a[8:11],v[76:79],v[244:247],a[8:11]
v_mfma_f32_16x16x32_bf16 a[12:15],v[76:79],v[188:191],a[12:15]
buffer_load_dwordx4 a[116:119],v6,s[28:31],s89 offen offset:1024 sc1
v_mfma_f32_16x16x32_bf16 a[12:15],v[72:75],v[172:175],a[12:15]
ds_read_b64_tr_b16 v[72:73],v1 offset:20480
ds_read_b64_tr_b16 v[74:75],v1 offset:25600
v_mfma_f32_16x16x32_bf16 a[12:15],v[68:71],v[156:159],a[12:15]
ds_read_b64_tr_b16 v[68:69],v1 offset:10240
v_mfma_f32_16x16x32_bf16 a[12:15],v[64:67],v[140:143],a[12:15]
s_waitcnt lgkmcnt(3)
ds_read_b64_tr_b16 v[64:65],v1
v_mfma_f32_16x16x32_bf16 a[16:19],v[80:83],v[32:35],a[16:19]
ds_read_b64_tr_b16 v[66:67],v1 offset:5120
v_mfma_f32_16x16x32_bf16 a[16:19],v[84:87],v[36:39],a[16:19]
buffer_load_dwordx4 a[76:79],v6,s[28:31],s88 offen sc1
v_mfma_f32_16x16x32_bf16 a[16:19],v[88:91],v[40:43],a[16:19]
ds_read_b64_tr_b16 v[70:71],v1 offset:15360
s_nop 0
L_pd22_%=:
s_cbranch_execz L_trtg2S_%=
ds_read_b64_tr_b16 v[76:77],v1 offset:30720
v_mfma_f32_16x16x32_bf16 a[16:19],v[92:95],v[44:47],a[16:19]
ds_read_b64_tr_b16 v[78:79],v1 offset:35840
v_add_u32 v1,0xffff6000,v1
v_mfma_f32_16x16x32_bf16 a[20:23],v[92:95],v[180:183],a[20:23]
v_mfma_f32_16x16x32_bf16 a[20:23],v[88:91],v[164:167],a[20:23]
v_mfma_f32_16x16x32_bf16 a[20:23],v[84:87],v[148:151],a[20:23]
buffer_load_dwordx4 a[108:111],v6,s[28:31],s88 offen offset:1024 sc1
v_mfma_f32_16x16x32_bf16 a[20:23],v[80:83],v[132:135],a[20:23]
v_mfma_f32_16x16x32_bf16 a[24:27],v[80:83],v[196:199],a[24:27]
v_mfma_f32_16x16x32_bf16 a[24:27],v[84:87],v[212:215],a[24:27]
v_mfma_f32_16x16x32_bf16 a[24:27],v[88:91],v[228:231],a[24:27]
v_mfma_f32_16x16x32_bf16 a[24:27],v[92:95],v[244:247],a[24:27]
buffer_load_dwordx4 a[100:103],v6,s[28:31],s87 offen offset:1024 sc1
v_mfma_f32_16x16x32_bf16 a[28:31],v[92:95],v[188:191],a[28:31]
ds_read_b64_tr_b16 v[92:93],v2 offset:30752
ds_read_b64_tr_b16 v[94:95],v2 offset:35872
v_mfma_f32_16x16x32_bf16 a[28:31],v[88:91],v[172:175],a[28:31]
ds_read_b64_tr_b16 v[88:89],v2 offset:20512
v_mfma_f32_16x16x32_bf16 a[28:31],v[84:87],v[156:159],a[28:31]
ds_read_b64_tr_b16 v[84:85],v2 offset:10272
v_mfma_f32_16x16x32_bf16 a[28:31],v[80:83],v[140:143],a[28:31]
ds_read_b64_tr_b16 v[80:81],v2 offset:32
v_mfma_f32_16x16x32_bf16 v[128:131],v[96:99],v[32:35],v[128:131]
buffer_load_dwordx4 a[64:67],v6,s[28:31],s87 offen offset:2048 sc1
v_mfma_f32_16x16x32_bf16 v[128:131],v[100:103],v[36:39],v[128:131]
ds_read_b64_tr_b16 v[82:83],v2 offset:5152
ds_read_b64_tr_b16 v[86:87],v2 offset:15392
v_mfma_f32_16x16x32_bf16 v[128:131],v[104:107],v[40:43],v[128:131]
ds_read_b64_tr_b16 v[90:91],v2 offset:25632
v_add_u32 v2,0xffff6000,v2
v_mfma_f32_16x16x32_bf16 v[128:131],v[108:111],v[44:47],v[128:131]
v_mfma_f32_16x16x32_bf16 v[136:139],v[108:111],v[180:183],v[136:139]
v_mfma_f32_16x16x32_bf16 v[136:139],v[104:107],v[164:167],v[136:139]
buffer_load_dwordx4 a[72:75],v6,s[28:31],s88 offen offset:2048 sc1
v_mfma_f32_16x16x32_bf16 v[136:139],v[100:103],v[148:151],v[136:139]
v_mfma_f32_16x16x32_bf16 v[136:139],v[96:99],v[132:135],v[136:139]
v_mfma_f32_16x16x32_bf16 v[144:147],v[96:99],v[196:199],v[144:147]
v_mfma_f32_16x16x32_bf16 v[144:147],v[100:103],v[212:215],v[144:147]
v_mfma_f32_16x16x32_bf16 v[144:147],v[104:107],v[228:231],v[144:147]
buffer_load_dwordx4 a[80:83],v6,s[28:31],s89 offen offset:2048 sc1
v_mfma_f32_16x16x32_bf16 v[144:147],v[108:111],v[244:247],v[144:147]
v_mfma_f32_16x16x32_bf16 v[152:155],v[108:111],v[188:191],v[152:155]
v_mfma_f32_16x16x32_bf16 v[152:155],v[104:107],v[172:175],v[152:155]
v_mfma_f32_16x16x32_bf16 v[152:155],v[100:103],v[156:159],v[152:155]
v_mfma_f32_16x16x32_bf16 v[152:155],v[96:99],v[140:143],v[152:155]
buffer_load_dwordx4 a[88:91],v6,s[28:31],s90 offen offset:2048 sc1
s_waitcnt vmcnt(15)
s_barrier
v_mfma_f32_16x16x32_bf16 v[160:163],v[112:115],v[32:35],v[160:163]
ds_read_b64_tr_b16 v[96:97],v1
ds_read_b64_tr_b16 v[98:99],v1 offset:5120
v_mfma_f32_16x16x32_bf16 v[160:163],v[116:119],v[36:39],v[160:163]
ds_read_b64_tr_b16 v[100:101],v1 offset:10240
v_mfma_f32_16x16x32_bf16 v[160:163],v[120:123],v[40:43],v[160:163]
ds_read_b64_tr_b16 v[102:103],v1 offset:15360
v_mfma_f32_16x16x32_bf16 v[160:163],v[124:127],v[44:47],v[160:163]
buffer_load_dwordx4 a[120:123],v28,s[28:31],s90 offen offset:3072 sc1
v_mfma_f32_16x16x32_bf16 v[168:171],v[124:127],v[180:183],v[168:171]
ds_read_b64_tr_b16 v[104:105],v1 offset:20480
ds_read_b64_tr_b16 v[106:107],v1 offset:25600
v_mfma_f32_16x16x32_bf16 v[168:171],v[120:123],v[164:167],v[168:171]
ds_read_b64_tr_b16 v[108:109],v1 offset:30720
v_mfma_f32_16x16x32_bf16 v[168:171],v[116:119],v[148:151],v[168:171]
ds_read_b64_tr_b16 v[110:111],v1 offset:35840
v_add_u32 v1,0xa000,v1
v_mfma_f32_16x16x32_bf16 v[168:171],v[112:115],v[132:135],v[168:171]
v_mfma_f32_16x16x32_bf16 v[176:179],v[112:115],v[196:199],v[176:179]
buffer_load_dwordx4 a[112:115],v28,s[28:31],s89 offen offset:3072 sc1
v_mfma_f32_16x16x32_bf16 v[176:179],v[116:119],v[212:215],v[176:179]
v_mfma_f32_16x16x32_bf16 v[176:179],v[120:123],v[228:231],v[176:179]
v_mfma_f32_16x16x32_bf16 v[176:179],v[124:127],v[244:247],v[176:179]
v_mfma_f32_16x16x32_bf16 v[184:187],v[124:127],v[188:191],v[184:187]
ds_read_b64_tr_b16 v[124:125],v2 offset:30752
ds_read_b64_tr_b16 v[126:127],v2 offset:35872
v_mfma_f32_16x16x32_bf16 v[184:187],v[120:123],v[172:175],v[184:187]
buffer_load_dwordx4 a[104:107],v28,s[28:31],s88 offen offset:3072 sc1
v_mfma_f32_16x16x32_bf16 v[184:187],v[116:119],v[156:159],v[184:187]
ds_read_b64_tr_b16 v[116:117],v2 offset:10272
ds_read_b64_tr_b16 v[118:119],v2 offset:15392
v_mfma_f32_16x16x32_bf16 v[184:187],v[112:115],v[140:143],v[184:187]
s_waitcnt lgkmcnt(4)
ds_read_b64_tr_b16 v[112:113],v2 offset:32
v_mfma_f32_16x16x32_bf16 a[192:195],v[64:67],a[128:131],a[192:195]
ds_read_b64_tr_b16 v[114:115],v2 offset:5152
v_mfma_f32_16x16x32_bf16 a[192:195],v[68:71],a[144:147],a[192:195]
ds_read_b64_tr_b16 v[120:121],v2 offset:20512
v_mfma_f32_16x16x32_bf16 a[192:195],v[72:75],a[160:163],a[192:195]
ds_read_b64_tr_b16 v[122:123],v2 offset:25632
v_add_u32 v2,0xa000,v2
v_mfma_f32_16x16x32_bf16 a[192:195],v[76:79],a[176:179],a[192:195]
v_mfma_f32_16x16x32_bf16 a[196:199],v[76:79],a[180:183],a[196:199]
v_mfma_f32_16x16x32_bf16 a[196:199],v[72:75],a[164:167],a[196:199]
buffer_load_dwordx4 a[96:99],v28,s[28:31],s87 offen offset:3072 sc1
v_mfma_f32_16x16x32_bf16 a[196:199],v[68:71],a[148:151],a[196:199]
s_mul_i32 s87,s60,81920
s_add_u32 s87,s87,s66
s_add_u32 s83,s83,s87
v_mfma_f32_16x16x32_bf16 a[196:199],v[64:67],a[132:135],a[196:199]
s_lshl_b32 s66,s63,6
s_add_u32 s87,s87,s66
s_add_u32 s88,s87,20480
v_mfma_f32_16x16x32_bf16 a[200:203],v[64:67],a[136:139],a[200:203]
s_add_u32 s89,s87,40960
s_add_u32 s90,s87,61440
s_add_u32 s66,s77,4096
v_mfma_f32_16x16x32_bf16 a[200:203],v[68:71],a[152:155],a[200:203]
v_mfma_f32_16x16x32_bf16 a[200:203],v[72:75],a[168:171],a[200:203]
v_mfma_f32_16x16x32_bf16 a[200:203],v[76:79],a[184:187],a[200:203]
v_mfma_f32_16x16x32_bf16 a[204:207],v[76:79],a[188:191],a[204:207]
s_waitcnt vmcnt(18)
ds_read_b64_tr_b16 v[76:77],v1 offset:30784
ds_read_b64_tr_b16 v[78:79],v1 offset:35904
v_mfma_f32_16x16x32_bf16 a[204:207],v[72:75],a[172:175],a[204:207]
s_waitcnt vmcnt(15)
ds_read_b64_tr_b16 v[72:73],v1 offset:20544
v_mfma_f32_16x16x32_bf16 a[204:207],v[68:71],a[156:159],a[204:207]
s_waitcnt vmcnt(13)
ds_read_b64_tr_b16 v[68:69],v1 offset:10304
v_mfma_f32_16x16x32_bf16 a[204:207],v[64:67],a[140:143],a[204:207]
ds_read_b64_tr_b16 v[64:65],v1 offset:64
v_mfma_f32_16x16x32_bf16 a[208:211],v[80:83],a[128:131],a[208:211]
ds_read_b64_tr_b16 v[66:67],v1 offset:5184
ds_read_b64_tr_b16 v[70:71],v1 offset:15424
v_mfma_f32_16x16x32_bf16 a[208:211],v[84:87],a[144:147],a[208:211]
ds_read_b64_tr_b16 v[74:75],v1 offset:25664
v_add_u32 v1,0xffff6000,v1
v_mfma_f32_16x16x32_bf16 a[208:211],v[88:91],a[160:163],a[208:211]
v_mfma_f32_16x16x32_bf16 a[208:211],v[92:95],a[176:179],a[208:211]
v_mfma_f32_16x16x32_bf16 a[212:215],v[92:95],a[180:183],a[212:215]
v_mfma_f32_16x16x32_bf16 a[212:215],v[88:91],a[164:167],a[212:215]
v_mfma_f32_16x16x32_bf16 a[212:215],v[84:87],a[148:151],a[212:215]
v_mfma_f32_16x16x32_bf16 a[212:215],v[80:83],a[132:135],a[212:215]
v_mfma_f32_16x16x32_bf16 a[216:219],v[80:83],a[136:139],a[216:219]
v_mfma_f32_16x16x32_bf16 a[216:219],v[84:87],a[152:155],a[216:219]
v_mfma_f32_16x16x32_bf16 a[216:219],v[88:91],a[168:171],a[216:219]
v_mfma_f32_16x16x32_bf16 a[216:219],v[92:95],a[184:187],a[216:219]
v_mfma_f32_16x16x32_bf16 a[220:223],v[92:95],a[188:191],a[220:223]
ds_read_b64_tr_b16 v[92:93],v2 offset:30816
ds_read_b64_tr_b16 v[94:95],v2 offset:35936
v_mfma_f32_16x16x32_bf16 a[220:223],v[88:91],a[172:175],a[220:223]
ds_read_b64_tr_b16 v[88:89],v2 offset:20576
v_mfma_f32_16x16x32_bf16 a[220:223],v[84:87],a[156:159],a[220:223]
ds_read_b64_tr_b16 v[84:85],v2 offset:10336
v_mfma_f32_16x16x32_bf16 a[220:223],v[80:83],a[140:143],a[220:223]
ds_read_b64_tr_b16 v[80:81],v2 offset:96
v_mfma_f32_16x16x32_bf16 a[192:195],v[96:99],a[68:71],a[192:195]
s_waitcnt vmcnt(10)
ds_read_b64_tr_b16 v[82:83],v2 offset:5216
ds_read_b64_tr_b16 v[86:87],v2 offset:15456
v_mfma_f32_16x16x32_bf16 a[192:195],v[100:103],a[76:79],a[192:195]
ds_read_b64_tr_b16 v[90:91],v2 offset:25696
v_add_u32 v2,0xffff6000,v2
v_mfma_f32_16x16x32_bf16 a[192:195],v[104:107],a[84:87],a[192:195]
v_mfma_f32_16x16x32_bf16 a[192:195],v[108:111],a[92:95],a[192:195]
v_mfma_f32_16x16x32_bf16 a[196:199],v[108:111],a[124:127],a[196:199]
v_mfma_f32_16x16x32_bf16 a[196:199],v[104:107],a[116:119],a[196:199]
s_waitcnt vmcnt(9)
v_mfma_f32_16x16x32_bf16 a[196:199],v[100:103],a[108:111],a[196:199]
s_waitcnt vmcnt(8)
v_mfma_f32_16x16x32_bf16 a[196:199],v[96:99],a[100:103],a[196:199]
s_waitcnt vmcnt(7)
v_mfma_f32_16x16x32_bf16 a[200:203],v[96:99],a[64:67],a[200:203]
s_waitcnt vmcnt(6)
v_mfma_f32_16x16x32_bf16 a[200:203],v[100:103],a[72:75],a[200:203]
)A"
R"A(s_waitcnt vmcnt(5)
v_mfma_f32_16x16x32_bf16 a[200:203],v[104:107],a[80:83],a[200:203]
s_waitcnt vmcnt(4)
v_mfma_f32_16x16x32_bf16 a[200:203],v[108:111],a[88:91],a[200:203]
s_waitcnt vmcnt(3)
v_accvgpr_read_b32 v8,a192
v_accvgpr_read_b32 v9,a193
v_mfma_f32_16x16x32_bf16 a[204:207],v[108:111],a[120:123],a[204:207]
s_waitcnt vmcnt(2)
v_accvgpr_read_b32 v10,a194
v_accvgpr_read_b32 v11,a195
v_mfma_f32_16x16x32_bf16 a[204:207],v[104:107],a[112:115],a[204:207]
s_waitcnt vmcnt(1)
v_cvt_pk_bf16_f32 v8,v8,v9
v_cvt_pk_bf16_f32 v9,v10,v11
v_mfma_f32_16x16x32_bf16 a[204:207],v[100:103],a[104:107],a[204:207]
s_waitcnt vmcnt(0)
v_accvgpr_read_b32 v16,a196
v_accvgpr_read_b32 v17,a197
v_mfma_f32_16x16x32_bf16 a[204:207],v[96:99],a[96:99],a[204:207]
s_waitcnt lgkmcnt(0)
v_accvgpr_read_b32 v18,a198
v_accvgpr_read_b32 v19,a199
v_mfma_f32_16x16x32_bf16 a[208:211],v[112:115],a[68:71],a[208:211]
v_cvt_pk_bf16_f32 v16,v16,v17
v_cvt_pk_bf16_f32 v17,v18,v19
v_mfma_f32_16x16x32_bf16 a[208:211],v[116:119],a[76:79],a[208:211]
ds_read_b64_tr_b16 v[96:97],v1 offset:64
ds_read_b64_tr_b16 v[98:99],v1 offset:5184
v_mfma_f32_16x16x32_bf16 a[208:211],v[120:123],a[84:87],a[208:211]
ds_read_b64_tr_b16 v[100:101],v1 offset:10304
v_mfma_f32_16x16x32_bf16 a[208:211],v[124:127],a[92:95],a[208:211]
ds_read_b64_tr_b16 v[102:103],v1 offset:15424
v_mfma_f32_16x16x32_bf16 a[212:215],v[124:127],a[124:127],a[212:215]
ds_read_b64_tr_b16 v[104:105],v1 offset:20544
v_mfma_f32_16x16x32_bf16 a[212:215],v[120:123],a[116:119],a[212:215]
ds_read_b64_tr_b16 v[106:107],v1 offset:25664
ds_read_b64_tr_b16 v[108:109],v1 offset:30784
v_mfma_f32_16x16x32_bf16 a[212:215],v[116:119],a[108:111],a[212:215]
ds_read_b64_tr_b16 v[110:111],v1 offset:35904
v_add_u32 v1,0xa000,v1
v_mfma_f32_16x16x32_bf16 a[212:215],v[112:115],a[100:103],a[212:215]
v_accvgpr_read_b32 v12,a208
v_accvgpr_read_b32 v13,a209
v_mfma_f32_16x16x32_bf16 a[216:219],v[112:115],a[64:67],a[216:219]
v_accvgpr_read_b32 v14,a210
v_accvgpr_read_b32 v15,a211
v_mfma_f32_16x16x32_bf16 a[216:219],v[116:119],a[72:75],a[216:219]
v_cvt_pk_bf16_f32 v10,v12,v13
v_cvt_pk_bf16_f32 v11,v14,v15
v_mfma_f32_16x16x32_bf16 a[216:219],v[120:123],a[80:83],a[216:219]
buffer_store_dwordx4 v[8:11],v6,s[36:39],s74 offen sc1
v_mfma_f32_16x16x32_bf16 a[216:219],v[124:127],a[88:91],a[216:219]
v_accvgpr_read_b32 v20,a212
v_accvgpr_read_b32 v21,a213
v_mfma_f32_16x16x32_bf16 a[220:223],v[124:127],a[120:123],a[220:223]
v_accvgpr_read_b32 v22,a214
v_accvgpr_read_b32 v23,a215
v_mfma_f32_16x16x32_bf16 a[220:223],v[120:123],a[112:115],a[220:223]
v_cvt_pk_bf16_f32 v18,v20,v21
v_cvt_pk_bf16_f32 v19,v22,v23
v_mfma_f32_16x16x32_bf16 a[220:223],v[116:119],a[104:107],a[220:223]
v_accvgpr_read_b32 v8,a200
v_accvgpr_read_b32 v9,a201
v_mfma_f32_16x16x32_bf16 a[220:223],v[112:115],a[96:99],a[220:223]
v_accvgpr_read_b32 v10,a202
v_accvgpr_read_b32 v11,a203
v_mfma_f32_16x16x32_bf16 a[224:227],v[64:67],a[128:131],a[224:227]
v_accvgpr_read_b32 v12,a216
v_accvgpr_read_b32 v13,a217
v_mfma_f32_16x16x32_bf16 a[224:227],v[68:71],a[144:147],a[224:227]
v_accvgpr_read_b32 v14,a218
v_accvgpr_read_b32 v15,a219
v_mfma_f32_16x16x32_bf16 a[224:227],v[72:75],a[160:163],a[224:227]
v_cvt_pk_bf16_f32 v8,v8,v9
v_cvt_pk_bf16_f32 v9,v10,v11
v_mfma_f32_16x16x32_bf16 a[224:227],v[76:79],a[176:179],a[224:227]
v_cvt_pk_bf16_f32 v10,v12,v13
v_cvt_pk_bf16_f32 v11,v14,v15
v_mfma_f32_16x16x32_bf16 a[228:231],v[76:79],a[180:183],a[228:231]
ds_read_b64_tr_b16 v[112:113],v2 offset:96
ds_read_b64_tr_b16 v[114:115],v2 offset:5216
v_mfma_f32_16x16x32_bf16 a[228:231],v[72:75],a[164:167],a[228:231]
buffer_store_dwordx4 v[16:19],v6,s[36:39],s74 offen offset:1024 sc1
v_mfma_f32_16x16x32_bf16 a[228:231],v[68:71],a[148:151],a[228:231]
ds_read_b64_tr_b16 v[116:117],v2 offset:10336
ds_read_b64_tr_b16 v[118:119],v2 offset:15456
v_mfma_f32_16x16x32_bf16 a[228:231],v[64:67],a[132:135],a[228:231]
ds_read_b64_tr_b16 v[120:121],v2 offset:20576
v_accvgpr_read_b32 v16,a204
v_mfma_f32_16x16x32_bf16 a[232:235],v[64:67],a[136:139],a[232:235]
ds_read_b64_tr_b16 v[122:123],v2 offset:25696
v_accvgpr_read_b32 v17,a205
v_mfma_f32_16x16x32_bf16 a[232:235],v[68:71],a[152:155],a[232:235]
ds_read_b64_tr_b16 v[124:125],v2 offset:30816
v_accvgpr_read_b32 v18,a206
v_mfma_f32_16x16x32_bf16 a[232:235],v[72:75],a[168:171],a[232:235]
ds_read_b64_tr_b16 v[126:127],v2 offset:35936
v_accvgpr_read_b32 v19,a207
v_mfma_f32_16x16x32_bf16 a[232:235],v[76:79],a[184:187],a[232:235]
v_accvgpr_read_b32 v20,a220
v_accvgpr_read_b32 v21,a221
v_mfma_f32_16x16x32_bf16 a[236:239],v[76:79],a[188:191],a[236:239]
v_accvgpr_read_b32 v22,a222
v_accvgpr_read_b32 v23,a223
v_mfma_f32_16x16x32_bf16 a[236:239],v[72:75],a[172:175],a[236:239]
v_cvt_pk_bf16_f32 v16,v16,v17
v_cvt_pk_bf16_f32 v17,v18,v19
v_mfma_f32_16x16x32_bf16 a[236:239],v[68:71],a[156:159],a[236:239]
v_cvt_pk_bf16_f32 v18,v20,v21
v_cvt_pk_bf16_f32 v19,v22,v23
v_mfma_f32_16x16x32_bf16 a[236:239],v[64:67],a[140:143],a[236:239]
ds_read_b64_tr_b16 v[64:65],v1 offset:128
ds_read_b64_tr_b16 v[66:67],v1 offset:5248
v_mfma_f32_16x16x32_bf16 a[240:243],v[80:83],a[128:131],a[240:243]
buffer_store_dwordx4 v[8:11],v6,s[36:39],s74 offen offset:2048 sc1
v_mfma_f32_16x16x32_bf16 a[240:243],v[84:87],a[144:147],a[240:243]
ds_read_b64_tr_b16 v[68:69],v1 offset:10368
ds_read_b64_tr_b16 v[70:71],v1 offset:15488
v_mfma_f32_16x16x32_bf16 a[240:243],v[88:91],a[160:163],a[240:243]
ds_read_b64_tr_b16 v[72:73],v1 offset:20608
v_add_u32 v2,0xa000,v2
v_mfma_f32_16x16x32_bf16 a[240:243],v[92:95],a[176:179],a[240:243]
ds_read_b64_tr_b16 v[74:75],v1 offset:25728
v_mfma_f32_16x16x32_bf16 a[244:247],v[92:95],a[180:183],a[244:247]
ds_read_b64_tr_b16 v[76:77],v1 offset:30848
v_mfma_f32_16x16x32_bf16 a[244:247],v[88:91],a[164:167],a[244:247]
ds_read_b64_tr_b16 v[78:79],v1 offset:35968
v_add_u32 v1,0xffff6000,v1
v_mfma_f32_16x16x32_bf16 a[244:247],v[84:87],a[148:151],a[244:247]
v_mfma_f32_16x16x32_bf16 a[244:247],v[80:83],a[132:135],a[244:247]
v_mfma_f32_16x16x32_bf16 a[248:251],v[80:83],a[136:139],a[248:251]
v_mfma_f32_16x16x32_bf16 a[248:251],v[84:87],a[152:155],a[248:251]
v_mfma_f32_16x16x32_bf16 a[248:251],v[88:91],a[168:171],a[248:251]
v_mfma_f32_16x16x32_bf16 a[248:251],v[92:95],a[184:187],a[248:251]
buffer_store_dwordx4 v[16:19],v28,s[36:39],s74 offen offset:3072 sc1
v_mfma_f32_16x16x32_bf16 a[252:255],v[92:95],a[188:191],a[252:255]
ds_read_b64_tr_b16 v[92:93],v2 offset:30880
ds_read_b64_tr_b16 v[94:95],v2 offset:36000
v_mfma_f32_16x16x32_bf16 a[252:255],v[88:91],a[172:175],a[252:255]
ds_read_b64_tr_b16 v[88:89],v2 offset:20640
v_mfma_f32_16x16x32_bf16 a[252:255],v[84:87],a[156:159],a[252:255]
ds_read_b64_tr_b16 v[84:85],v2 offset:10400
v_mfma_f32_16x16x32_bf16 a[252:255],v[80:83],a[140:143],a[252:255]
s_waitcnt lgkmcnt(4)
ds_read_b64_tr_b16 v[80:81],v2 offset:160
v_mfma_f32_16x16x32_bf16 a[224:227],v[96:99],a[68:71],a[224:227]
ds_read_b64_tr_b16 v[82:83],v2 offset:5280
ds_read_b64_tr_b16 v[86:87],v2 offset:15520
v_mfma_f32_16x16x32_bf16 a[224:227],v[100:103],a[76:79],a[224:227]
ds_read_b64_tr_b16 v[90:91],v2 offset:25760
v_add_u32 v2,0xffff6000,v2
v_mfma_f32_16x16x32_bf16 a[224:227],v[104:107],a[84:87],a[224:227]
v_mfma_f32_16x16x32_bf16 a[224:227],v[108:111],a[92:95],a[224:227]
v_mfma_f32_16x16x32_bf16 a[228:231],v[108:111],a[124:127],a[228:231]
v_mfma_f32_16x16x32_bf16 a[228:231],v[104:107],a[116:119],a[228:231]
v_mfma_f32_16x16x32_bf16 a[228:231],v[100:103],a[108:111],a[228:231]
v_mfma_f32_16x16x32_bf16 a[228:231],v[96:99],a[100:103],a[228:231]
v_mfma_f32_16x16x32_bf16 a[232:235],v[96:99],a[64:67],a[232:235]
v_mfma_f32_16x16x32_bf16 a[232:235],v[100:103],a[72:75],a[232:235]
v_mfma_f32_16x16x32_bf16 a[232:235],v[104:107],a[80:83],a[232:235]
v_mfma_f32_16x16x32_bf16 a[232:235],v[108:111],a[88:91],a[232:235]
v_accvgpr_read_b32 v8,a224
v_accvgpr_read_b32 v9,a225
v_mfma_f32_16x16x32_bf16 a[236:239],v[108:111],a[120:123],a[236:239]
v_accvgpr_read_b32 v10,a226
v_accvgpr_read_b32 v11,a227
v_mfma_f32_16x16x32_bf16 a[236:239],v[104:107],a[112:115],a[236:239]
v_cvt_pk_bf16_f32 v8,v8,v9
v_cvt_pk_bf16_f32 v9,v10,v11
v_mfma_f32_16x16x32_bf16 a[236:239],v[100:103],a[104:107],a[236:239]
v_accvgpr_read_b32 v16,a228
v_accvgpr_read_b32 v17,a229
v_mfma_f32_16x16x32_bf16 a[236:239],v[96:99],a[96:99],a[236:239]
v_accvgpr_read_b32 v18,a230
v_accvgpr_read_b32 v19,a231
v_mfma_f32_16x16x32_bf16 a[240:243],v[112:115],a[68:71],a[240:243]
v_cvt_pk_bf16_f32 v16,v16,v17
v_cvt_pk_bf16_f32 v17,v18,v19
v_mfma_f32_16x16x32_bf16 a[240:243],v[116:119],a[76:79],a[240:243]
ds_read_b64_tr_b16 v[96:97],v1 offset:128
ds_read_b64_tr_b16 v[98:99],v1 offset:5248
v_mfma_f32_16x16x32_bf16 a[240:243],v[120:123],a[84:87],a[240:243]
ds_read_b64_tr_b16 v[100:101],v1 offset:10368
v_mfma_f32_16x16x32_bf16 a[240:243],v[124:127],a[92:95],a[240:243]
ds_read_b64_tr_b16 v[102:103],v1 offset:15488
v_mfma_f32_16x16x32_bf16 a[244:247],v[124:127],a[124:127],a[244:247]
ds_read_b64_tr_b16 v[104:105],v1 offset:20608
v_mfma_f32_16x16x32_bf16 a[244:247],v[120:123],a[116:119],a[244:247]
ds_read_b64_tr_b16 v[106:107],v1 offset:25728
ds_read_b64_tr_b16 v[108:109],v1 offset:30848
v_mfma_f32_16x16x32_bf16 a[244:247],v[116:119],a[108:111],a[244:247]
ds_read_b64_tr_b16 v[110:111],v1 offset:35968
v_add_u32 v1,0xa000,v1
v_mfma_f32_16x16x32_bf16 a[244:247],v[112:115],a[100:103],a[244:247]
v_accvgpr_read_b32 v12,a240
v_accvgpr_read_b32 v13,a241
v_mfma_f32_16x16x32_bf16 a[248:251],v[112:115],a[64:67],a[248:251]
v_accvgpr_read_b32 v14,a242
v_accvgpr_read_b32 v15,a243
v_mfma_f32_16x16x32_bf16 a[248:251],v[116:119],a[72:75],a[248:251]
v_cvt_pk_bf16_f32 v10,v12,v13
v_cvt_pk_bf16_f32 v11,v14,v15
v_mfma_f32_16x16x32_bf16 a[248:251],v[120:123],a[80:83],a[248:251]
buffer_store_dwordx4 v[8:11],v6,s[36:39],s75 offen sc1
v_mfma_f32_16x16x32_bf16 a[248:251],v[124:127],a[88:91],a[248:251]
v_accvgpr_read_b32 v20,a244
v_accvgpr_read_b32 v21,a245
v_mfma_f32_16x16x32_bf16 a[252:255],v[124:127],a[120:123],a[252:255]
v_accvgpr_read_b32 v22,a246
v_accvgpr_read_b32 v23,a247
v_mfma_f32_16x16x32_bf16 a[252:255],v[120:123],a[112:115],a[252:255]
v_cvt_pk_bf16_f32 v18,v20,v21
v_cvt_pk_bf16_f32 v19,v22,v23
v_mfma_f32_16x16x32_bf16 a[252:255],v[116:119],a[104:107],a[252:255]
v_accvgpr_read_b32 v8,a232
v_accvgpr_read_b32 v9,a233
v_mfma_f32_16x16x32_bf16 a[252:255],v[112:115],a[96:99],a[252:255]
v_accvgpr_read_b32 v10,a234
v_accvgpr_read_b32 v11,a235
v_mfma_f32_16x16x32_bf16 a[0:3],v[64:67],a[128:131],a[0:3]
v_accvgpr_read_b32 v12,a248
v_accvgpr_read_b32 v13,a249
v_mfma_f32_16x16x32_bf16 a[0:3],v[68:71],a[144:147],a[0:3]
v_accvgpr_read_b32 v14,a250
v_accvgpr_read_b32 v15,a251
v_mfma_f32_16x16x32_bf16 a[0:3],v[72:75],a[160:163],a[0:3]
v_cvt_pk_bf16_f32 v8,v8,v9
v_cvt_pk_bf16_f32 v9,v10,v11
v_mfma_f32_16x16x32_bf16 a[0:3],v[76:79],a[176:179],a[0:3]
v_cvt_pk_bf16_f32 v10,v12,v13
v_cvt_pk_bf16_f32 v11,v14,v15
v_mfma_f32_16x16x32_bf16 a[4:7],v[76:79],a[180:183],a[4:7]
ds_read_b64_tr_b16 v[112:113],v2 offset:160
ds_read_b64_tr_b16 v[114:115],v2 offset:5280
v_mfma_f32_16x16x32_bf16 a[4:7],v[72:75],a[164:167],a[4:7]
buffer_store_dwordx4 v[16:19],v6,s[36:39],s75 offen offset:1024 sc1
v_mfma_f32_16x16x32_bf16 a[4:7],v[68:71],a[148:151],a[4:7]
ds_read_b64_tr_b16 v[116:117],v2 offset:10400
ds_read_b64_tr_b16 v[118:119],v2 offset:15520
v_mfma_f32_16x16x32_bf16 a[4:7],v[64:67],a[132:135],a[4:7]
ds_read_b64_tr_b16 v[120:121],v2 offset:20640
v_accvgpr_read_b32 v16,a236
v_mfma_f32_16x16x32_bf16 a[8:11],v[64:67],a[136:139],a[8:11]
ds_read_b64_tr_b16 v[122:123],v2 offset:25760
v_accvgpr_read_b32 v17,a237
v_mfma_f32_16x16x32_bf16 a[8:11],v[68:71],a[152:155],a[8:11]
ds_read_b64_tr_b16 v[124:125],v2 offset:30880
v_accvgpr_read_b32 v18,a238
v_mfma_f32_16x16x32_bf16 a[8:11],v[72:75],a[168:171],a[8:11]
ds_read_b64_tr_b16 v[126:127],v2 offset:36000
v_accvgpr_read_b32 v19,a239
v_mfma_f32_16x16x32_bf16 a[8:11],v[76:79],a[184:187],a[8:11]
v_accvgpr_read_b32 v20,a252
v_accvgpr_read_b32 v21,a253
v_mfma_f32_16x16x32_bf16 a[12:15],v[76:79],a[188:191],a[12:15]
v_accvgpr_read_b32 v22,a254
v_accvgpr_read_b32 v23,a255
v_mfma_f32_16x16x32_bf16 a[12:15],v[72:75],a[172:175],a[12:15]
)A"
R"A(v_cvt_pk_bf16_f32 v16,v16,v17
v_cvt_pk_bf16_f32 v17,v18,v19
v_mfma_f32_16x16x32_bf16 a[12:15],v[68:71],a[156:159],a[12:15]
v_cvt_pk_bf16_f32 v18,v20,v21
v_cvt_pk_bf16_f32 v19,v22,v23
v_mfma_f32_16x16x32_bf16 a[12:15],v[64:67],a[140:143],a[12:15]
s_waitcnt lgkmcnt(3)
ds_read_b64_tr_b16 v[64:65],v1 offset:192
ds_read_b64_tr_b16 v[66:67],v1 offset:5312
v_mfma_f32_16x16x32_bf16 a[16:19],v[80:83],a[128:131],a[16:19]
buffer_store_dwordx4 v[8:11],v6,s[36:39],s75 offen offset:2048 sc1
v_mfma_f32_16x16x32_bf16 a[16:19],v[84:87],a[144:147],a[16:19]
ds_read_b64_tr_b16 v[68:69],v1 offset:10432
ds_read_b64_tr_b16 v[70:71],v1 offset:15552
v_mfma_f32_16x16x32_bf16 a[16:19],v[88:91],a[160:163],a[16:19]
ds_read_b64_tr_b16 v[72:73],v1 offset:20672
v_add_u32 v2,0xa000,v2
v_mfma_f32_16x16x32_bf16 a[16:19],v[92:95],a[176:179],a[16:19]
ds_read_b64_tr_b16 v[74:75],v1 offset:25792
v_mfma_f32_16x16x32_bf16 a[20:23],v[92:95],a[180:183],a[20:23]
ds_read_b64_tr_b16 v[76:77],v1 offset:30912
v_mfma_f32_16x16x32_bf16 a[20:23],v[88:91],a[164:167],a[20:23]
ds_read_b64_tr_b16 v[78:79],v1 offset:36032
v_add_u32 v1,0xffff6000,v1
v_mfma_f32_16x16x32_bf16 a[20:23],v[84:87],a[148:151],a[20:23]
v_mfma_f32_16x16x32_bf16 a[20:23],v[80:83],a[132:135],a[20:23]
v_mfma_f32_16x16x32_bf16 a[24:27],v[80:83],a[136:139],a[24:27]
v_mfma_f32_16x16x32_bf16 a[24:27],v[84:87],a[152:155],a[24:27]
v_mfma_f32_16x16x32_bf16 a[24:27],v[88:91],a[168:171],a[24:27]
v_mfma_f32_16x16x32_bf16 a[24:27],v[92:95],a[184:187],a[24:27]
buffer_store_dwordx4 v[16:19],v28,s[36:39],s75 offen offset:3072 sc1
v_mfma_f32_16x16x32_bf16 a[28:31],v[92:95],a[188:191],a[28:31]
ds_read_b64_tr_b16 v[92:93],v2 offset:30944
ds_read_b64_tr_b16 v[94:95],v2 offset:36064
v_mfma_f32_16x16x32_bf16 a[28:31],v[88:91],a[172:175],a[28:31]
ds_read_b64_tr_b16 v[88:89],v2 offset:20704
v_mfma_f32_16x16x32_bf16 a[28:31],v[84:87],a[156:159],a[28:31]
ds_read_b64_tr_b16 v[84:85],v2 offset:10464
v_mfma_f32_16x16x32_bf16 a[28:31],v[80:83],a[140:143],a[28:31]
ds_read_b64_tr_b16 v[80:81],v2 offset:224
v_mfma_f32_16x16x32_bf16 a[0:3],v[96:99],a[68:71],a[0:3]
ds_read_b64_tr_b16 v[82:83],v2 offset:5344
ds_read_b64_tr_b16 v[86:87],v2 offset:15584
v_mfma_f32_16x16x32_bf16 a[0:3],v[100:103],a[76:79],a[0:3]
ds_read_b64_tr_b16 v[90:91],v2 offset:25824
v_add_u32 v2,0xffff6000,v2
v_mfma_f32_16x16x32_bf16 a[0:3],v[104:107],a[84:87],a[0:3]
v_mfma_f32_16x16x32_bf16 a[0:3],v[108:111],a[92:95],a[0:3]
v_mfma_f32_16x16x32_bf16 a[4:7],v[108:111],a[124:127],a[4:7]
v_mfma_f32_16x16x32_bf16 a[4:7],v[104:107],a[116:119],a[4:7]
v_mfma_f32_16x16x32_bf16 a[4:7],v[100:103],a[108:111],a[4:7]
v_mfma_f32_16x16x32_bf16 a[4:7],v[96:99],a[100:103],a[4:7]
v_mfma_f32_16x16x32_bf16 a[8:11],v[96:99],a[64:67],a[8:11]
v_mfma_f32_16x16x32_bf16 a[8:11],v[100:103],a[72:75],a[8:11]
v_mfma_f32_16x16x32_bf16 a[8:11],v[104:107],a[80:83],a[8:11]
v_mfma_f32_16x16x32_bf16 a[8:11],v[108:111],a[88:91],a[8:11]
v_accvgpr_read_b32 v8,a0
v_accvgpr_read_b32 v9,a1
v_mfma_f32_16x16x32_bf16 a[12:15],v[108:111],a[120:123],a[12:15]
v_accvgpr_read_b32 v10,a2
v_accvgpr_read_b32 v11,a3
v_mfma_f32_16x16x32_bf16 a[12:15],v[104:107],a[112:115],a[12:15]
v_cvt_pk_bf16_f32 v8,v8,v9
v_cvt_pk_bf16_f32 v9,v10,v11
v_mfma_f32_16x16x32_bf16 a[12:15],v[100:103],a[104:107],a[12:15]
v_accvgpr_read_b32 v16,a4
v_accvgpr_read_b32 v17,a5
v_mfma_f32_16x16x32_bf16 a[12:15],v[96:99],a[96:99],a[12:15]
v_accvgpr_read_b32 v18,a6
v_accvgpr_read_b32 v19,a7
v_mfma_f32_16x16x32_bf16 a[16:19],v[112:115],a[68:71],a[16:19]
v_cvt_pk_bf16_f32 v16,v16,v17
v_cvt_pk_bf16_f32 v17,v18,v19
v_mfma_f32_16x16x32_bf16 a[16:19],v[116:119],a[76:79],a[16:19]
s_waitcnt lgkmcnt(0)
ds_read_b64_tr_b16 v[96:97],v1 offset:192
ds_read_b64_tr_b16 v[98:99],v1 offset:5312
v_mfma_f32_16x16x32_bf16 a[16:19],v[120:123],a[84:87],a[16:19]
ds_read_b64_tr_b16 v[100:101],v1 offset:10432
v_mfma_f32_16x16x32_bf16 a[16:19],v[124:127],a[92:95],a[16:19]
ds_read_b64_tr_b16 v[102:103],v1 offset:15552
v_mfma_f32_16x16x32_bf16 a[20:23],v[124:127],a[124:127],a[20:23]
ds_read_b64_tr_b16 v[104:105],v1 offset:20672
v_mfma_f32_16x16x32_bf16 a[20:23],v[120:123],a[116:119],a[20:23]
ds_read_b64_tr_b16 v[106:107],v1 offset:25792
ds_read_b64_tr_b16 v[108:109],v1 offset:30912
v_mfma_f32_16x16x32_bf16 a[20:23],v[116:119],a[108:111],a[20:23]
ds_read_b64_tr_b16 v[110:111],v1 offset:36032
v_add_u32 v1,0x14000,v1
v_mfma_f32_16x16x32_bf16 a[20:23],v[112:115],a[100:103],a[20:23]
v_accvgpr_read_b32 v12,a16
v_accvgpr_read_b32 v13,a17
v_mfma_f32_16x16x32_bf16 a[24:27],v[112:115],a[64:67],a[24:27]
v_accvgpr_read_b32 v14,a18
v_accvgpr_read_b32 v15,a19
v_mfma_f32_16x16x32_bf16 a[24:27],v[116:119],a[72:75],a[24:27]
v_cvt_pk_bf16_f32 v10,v12,v13
v_cvt_pk_bf16_f32 v11,v14,v15
v_mfma_f32_16x16x32_bf16 a[24:27],v[120:123],a[80:83],a[24:27]
buffer_store_dwordx4 v[8:11],v6,s[36:39],s76 offen sc1
L_pd23_%=:
s_cbranch_execz L_trtg2S_%=
v_mfma_f32_16x16x32_bf16 a[24:27],v[124:127],a[88:91],a[24:27]
v_accvgpr_read_b32 v20,a20
v_accvgpr_read_b32 v21,a21
v_mfma_f32_16x16x32_bf16 a[28:31],v[124:127],a[120:123],a[28:31]
v_accvgpr_read_b32 v22,a22
v_accvgpr_read_b32 v23,a23
v_mfma_f32_16x16x32_bf16 a[28:31],v[120:123],a[112:115],a[28:31]
v_cvt_pk_bf16_f32 v18,v20,v21
v_cvt_pk_bf16_f32 v19,v22,v23
v_mfma_f32_16x16x32_bf16 a[28:31],v[116:119],a[104:107],a[28:31]
v_accvgpr_read_b32 v8,a8
v_accvgpr_read_b32 v9,a9
v_mfma_f32_16x16x32_bf16 a[28:31],v[112:115],a[96:99],a[28:31]
v_accvgpr_read_b32 v10,a10
v_accvgpr_read_b32 v11,a11
v_mfma_f32_16x16x32_bf16 v[128:131],v[64:67],a[128:131],v[128:131]
v_accvgpr_read_b32 v12,a24
v_accvgpr_read_b32 v13,a25
v_mfma_f32_16x16x32_bf16 v[128:131],v[68:71],a[144:147],v[128:131]
v_accvgpr_read_b32 v14,a26
v_accvgpr_read_b32 v15,a27
v_mfma_f32_16x16x32_bf16 v[128:131],v[72:75],a[160:163],v[128:131]
v_cvt_pk_bf16_f32 v8,v8,v9
v_cvt_pk_bf16_f32 v9,v10,v11
v_mfma_f32_16x16x32_bf16 v[128:131],v[76:79],a[176:179],v[128:131]
v_cvt_pk_bf16_f32 v10,v12,v13
v_cvt_pk_bf16_f32 v11,v14,v15
v_mfma_f32_16x16x32_bf16 v[136:139],v[76:79],a[180:183],v[136:139]
ds_read_b64_tr_b16 v[112:113],v2 offset:224
ds_read_b64_tr_b16 v[114:115],v2 offset:5344
v_mfma_f32_16x16x32_bf16 v[136:139],v[72:75],a[164:167],v[136:139]
buffer_store_dwordx4 v[16:19],v6,s[36:39],s76 offen offset:1024 sc1
v_mfma_f32_16x16x32_bf16 v[136:139],v[68:71],a[148:151],v[136:139]
ds_read_b64_tr_b16 v[116:117],v2 offset:10464
ds_read_b64_tr_b16 v[118:119],v2 offset:15584
v_mfma_f32_16x16x32_bf16 v[136:139],v[64:67],a[132:135],v[136:139]
ds_read_b64_tr_b16 v[120:121],v2 offset:20704
v_accvgpr_read_b32 v16,a12
v_mfma_f32_16x16x32_bf16 v[144:147],v[64:67],a[136:139],v[144:147]
ds_read_b64_tr_b16 v[122:123],v2 offset:25824
v_accvgpr_read_b32 v17,a13
v_mfma_f32_16x16x32_bf16 v[144:147],v[68:71],a[152:155],v[144:147]
ds_read_b64_tr_b16 v[124:125],v2 offset:30944
v_accvgpr_read_b32 v18,a14
v_mfma_f32_16x16x32_bf16 v[144:147],v[72:75],a[168:171],v[144:147]
ds_read_b64_tr_b16 v[126:127],v2 offset:36064
v_accvgpr_read_b32 v19,a15
v_mfma_f32_16x16x32_bf16 v[144:147],v[76:79],a[184:187],v[144:147]
v_accvgpr_read_b32 v20,a28
v_accvgpr_read_b32 v21,a29
v_mfma_f32_16x16x32_bf16 v[152:155],v[76:79],a[188:191],v[152:155]
v_accvgpr_read_b32 v22,a30
v_accvgpr_read_b32 v23,a31
v_mfma_f32_16x16x32_bf16 v[152:155],v[72:75],a[172:175],v[152:155]
v_cvt_pk_bf16_f32 v16,v16,v17
v_cvt_pk_bf16_f32 v17,v18,v19
v_mfma_f32_16x16x32_bf16 v[152:155],v[68:71],a[156:159],v[152:155]
v_cvt_pk_bf16_f32 v18,v20,v21
v_cvt_pk_bf16_f32 v19,v22,v23
v_mfma_f32_16x16x32_bf16 v[152:155],v[64:67],a[140:143],v[152:155]
ds_read_b64_tr_b16 v[64:65],v1 offset:256
ds_read_b64_tr_b16 v[66:67],v1 offset:5376
v_mfma_f32_16x16x32_bf16 v[160:163],v[80:83],a[128:131],v[160:163]
buffer_store_dwordx4 v[8:11],v6,s[36:39],s76 offen offset:2048 sc1
v_mfma_f32_16x16x32_bf16 v[160:163],v[84:87],a[144:147],v[160:163]
ds_read_b64_tr_b16 v[68:69],v1 offset:10496
ds_read_b64_tr_b16 v[70:71],v1 offset:15616
v_mfma_f32_16x16x32_bf16 v[160:163],v[88:91],a[160:163],v[160:163]
ds_read_b64_tr_b16 v[72:73],v1 offset:20736
v_add_u32 v2,0x14000,v2
v_mfma_f32_16x16x32_bf16 v[160:163],v[92:95],a[176:179],v[160:163]
ds_read_b64_tr_b16 v[74:75],v1 offset:25856
v_mov_b32 v20,s52
v_mfma_f32_16x16x32_bf16 v[168:171],v[92:95],a[180:183],v[168:171]
ds_read_b64_tr_b16 v[76:77],v1 offset:30976
v_mov_b32 v21,s53
v_mfma_f32_16x16x32_bf16 v[168:171],v[88:91],a[164:167],v[168:171]
ds_read_b64_tr_b16 v[78:79],v1 offset:36096
v_add_u32 v1,0xffff6000,v1
v_mfma_f32_16x16x32_bf16 v[168:171],v[84:87],a[148:151],v[168:171]
v_lshlrev_b32 v22,20,v6
v_mfma_f32_16x16x32_bf16 v[168:171],v[80:83],a[132:135],v[168:171]
v_mfma_f32_16x16x32_bf16 v[176:179],v[80:83],a[136:139],v[176:179]
v_mfma_f32_16x16x32_bf16 v[176:179],v[84:87],a[152:155],v[176:179]
v_mfma_f32_16x16x32_bf16 v[176:179],v[88:91],a[168:171],v[176:179]
v_mfma_f32_16x16x32_bf16 v[176:179],v[92:95],a[184:187],v[176:179]
buffer_store_dwordx4 v[16:19],v28,s[36:39],s76 offen offset:3072 sc1
v_mfma_f32_16x16x32_bf16 v[184:187],v[92:95],a[188:191],v[184:187]
ds_read_b64_tr_b16 v[92:93],v2 offset:31008
ds_read_b64_tr_b16 v[94:95],v2 offset:36128
v_mfma_f32_16x16x32_bf16 v[184:187],v[88:91],a[172:175],v[184:187]
ds_read_b64_tr_b16 v[88:89],v2 offset:20768
v_mfma_f32_16x16x32_bf16 v[184:187],v[84:87],a[156:159],v[184:187]
ds_read_b64_tr_b16 v[84:85],v2 offset:10528
v_mfma_f32_16x16x32_bf16 v[184:187],v[80:83],a[140:143],v[184:187]
s_waitcnt lgkmcnt(4)
ds_read_b64_tr_b16 v[80:81],v2 offset:288
v_mfma_f32_16x16x32_bf16 v[128:131],v[96:99],a[68:71],v[128:131]
ds_read_b64_tr_b16 v[82:83],v2 offset:5408
ds_read_b64_tr_b16 v[86:87],v2 offset:15648
v_mfma_f32_16x16x32_bf16 v[128:131],v[100:103],a[76:79],v[128:131]
ds_read_b64_tr_b16 v[90:91],v2 offset:25888
v_add_u32 v2,0xffff6000,v2
v_mfma_f32_16x16x32_bf16 v[128:131],v[104:107],a[84:87],v[128:131]
v_mfma_f32_16x16x32_bf16 v[128:131],v[108:111],a[92:95],v[128:131]
v_mfma_f32_16x16x32_bf16 v[136:139],v[108:111],a[124:127],v[136:139]
v_mfma_f32_16x16x32_bf16 v[136:139],v[104:107],a[116:119],v[136:139]
v_mfma_f32_16x16x32_bf16 v[136:139],v[100:103],a[108:111],v[136:139]
v_mfma_f32_16x16x32_bf16 v[136:139],v[96:99],a[100:103],v[136:139]
v_mfma_f32_16x16x32_bf16 v[144:147],v[96:99],a[64:67],v[144:147]
v_mfma_f32_16x16x32_bf16 v[144:147],v[100:103],a[72:75],v[144:147]
v_mfma_f32_16x16x32_bf16 v[144:147],v[104:107],a[80:83],v[144:147]
v_mfma_f32_16x16x32_bf16 v[144:147],v[108:111],a[88:91],v[144:147]
v_cvt_pk_bf16_f32 v128,v128,v129
v_cvt_pk_bf16_f32 v129,v130,v131
v_mfma_f32_16x16x32_bf16 v[152:155],v[108:111],a[120:123],v[152:155]
ds_read_b64_tr_b16 v[108:109],v1 offset:30976
ds_read_b64_tr_b16 v[110:111],v1 offset:36096
v_mfma_f32_16x16x32_bf16 v[152:155],v[104:107],a[112:115],v[152:155]
v_cvt_pk_bf16_f32 v136,v136,v137
v_cvt_pk_bf16_f32 v137,v138,v139
v_mfma_f32_16x16x32_bf16 v[152:155],v[100:103],a[104:107],v[152:155]
v_cvt_pk_bf16_f32 v144,v144,v145
v_cvt_pk_bf16_f32 v145,v146,v147
v_mfma_f32_16x16x32_bf16 v[152:155],v[96:99],a[96:99],v[152:155]
ds_read_b64_tr_b16 v[96:97],v1 offset:256
ds_read_b64_tr_b16 v[98:99],v1 offset:5376
v_mfma_f32_16x16x32_bf16 v[160:163],v[112:115],a[68:71],v[160:163]
ds_read_b64_tr_b16 v[100:101],v1 offset:10496
v_mfma_f32_16x16x32_bf16 v[160:163],v[116:119],a[76:79],v[160:163]
ds_read_b64_tr_b16 v[102:103],v1 offset:15616
v_mfma_f32_16x16x32_bf16 v[160:163],v[120:123],a[84:87],v[160:163]
ds_read_b64_tr_b16 v[104:105],v1 offset:20736
v_mfma_f32_16x16x32_bf16 v[160:163],v[124:127],a[92:95],v[160:163]
v_cvt_pk_bf16_f32 v152,v152,v153
v_cvt_pk_bf16_f32 v153,v154,v155
v_mfma_f32_16x16x32_bf16 v[168:171],v[124:127],a[124:127],v[168:171]
ds_read_b64_tr_b16 v[106:107],v1 offset:25856
v_add_u32 v1,0xffff6000,v1
v_mfma_f32_16x16x32_bf16 v[168:171],v[120:123],a[116:119],v[168:171]
v_mfma_f32_16x16x32_bf16 v[168:171],v[116:119],a[108:111],v[168:171]
v_mfma_f32_16x16x32_bf16 v[168:171],v[112:115],a[100:103],v[168:171]
v_cvt_pk_bf16_f32 v130,v160,v161
v_cvt_pk_bf16_f32 v131,v162,v163
v_mfma_f32_16x16x32_bf16 v[176:179],v[112:115],a[64:67],v[176:179]
buffer_store_dwordx4 v[128:131],v6,s[36:39],s79 offen sc1
)A"
R"A(v_mfma_f32_16x16x32_bf16 v[176:179],v[116:119],a[72:75],v[176:179]
v_mfma_f32_16x16x32_bf16 v[176:179],v[120:123],a[80:83],v[176:179]
v_mfma_f32_16x16x32_bf16 v[176:179],v[124:127],a[88:91],v[176:179]
v_mfma_f32_16x16x32_bf16 v[184:187],v[124:127],a[120:123],v[184:187]
v_cvt_pk_bf16_f32 v138,v168,v169
v_cvt_pk_bf16_f32 v139,v170,v171
v_mfma_f32_16x16x32_bf16 v[184:187],v[120:123],a[112:115],v[184:187]
buffer_store_dwordx4 v[136:139],v6,s[36:39],s80 offen sc1
v_mfma_f32_16x16x32_bf16 v[184:187],v[116:119],a[104:107],v[184:187]
ds_read_b64_tr_b16 v[116:117],v2 offset:10528
ds_read_b64_tr_b16 v[118:119],v2 offset:15648
v_mfma_f32_16x16x32_bf16 v[184:187],v[112:115],a[96:99],v[184:187]
v_cvt_pk_bf16_f32 v146,v176,v177
v_cvt_pk_bf16_f32 v147,v178,v179
s_waitcnt vmcnt(6)
s_barrier
v_mfma_f32_16x16x32_bf16 v[192:195],v[64:67],v[32:35],v[192:195]
buffer_store_dwordx4 v[144:147],v6,s[36:39],s81 offen sc1
v_mfma_f32_16x16x32_bf16 v[192:195],v[68:71],v[36:39],v[192:195]
ds_read_b64_tr_b16 v[112:113],v2 offset:288
ds_read_b64_tr_b16 v[114:115],v2 offset:5408
v_mfma_f32_16x16x32_bf16 v[192:195],v[72:75],v[40:43],v[192:195]
buffer_store_dwordx2 v[58:59],v60,s[44:47],s18 offen offset:2312 sc1
v_mfma_f32_16x16x32_bf16 v[192:195],v[76:79],v[44:47],v[192:195]
v_cvt_pk_bf16_f32 v154,v184,v185
v_cvt_pk_bf16_f32 v155,v186,v187
v_mfma_f32_16x16x32_bf16 v[200:203],v[76:79],v[180:183],v[200:203]
buffer_store_dwordx4 v[152:155],v28,s[36:39],s82 offen sc1
v_mfma_f32_16x16x32_bf16 v[200:203],v[72:75],v[164:167],v[200:203]
ds_read_b64_tr_b16 v[120:121],v2 offset:20768
ds_read_b64_tr_b16 v[122:123],v2 offset:25888
v_mfma_f32_16x16x32_bf16 v[200:203],v[68:71],v[148:151],v[200:203]
ds_read_b64_tr_b16 v[124:125],v2 offset:31008
v_mfma_f32_16x16x32_bf16 v[200:203],v[64:67],v[132:135],v[200:203]
ds_read_b64_tr_b16 v[126:127],v2 offset:36128
v_add_u32 v2,0xffff6000,v2
v_mfma_f32_16x16x32_bf16 v[208:211],v[64:67],v[196:199],v[208:211]
v_mfma_f32_16x16x32_bf16 v[208:211],v[68:71],v[212:215],v[208:211]
v_mfma_f32_16x16x32_bf16 v[208:211],v[72:75],v[228:231],v[208:211]
v_mfma_f32_16x16x32_bf16 v[208:211],v[76:79],v[244:247],v[208:211]
v_mfma_f32_16x16x32_bf16 v[216:219],v[76:79],v[188:191],v[216:219]
ds_read_b64_tr_b16 v[76:77],v1 offset:30976
ds_read_b64_tr_b16 v[78:79],v1 offset:36096
v_mfma_f32_16x16x32_bf16 v[216:219],v[72:75],v[172:175],v[216:219]
ds_read_b64_tr_b16 v[72:73],v1 offset:20736
v_mfma_f32_16x16x32_bf16 v[216:219],v[68:71],v[156:159],v[216:219]
ds_read_b64_tr_b16 v[68:69],v1 offset:10496
v_mfma_f32_16x16x32_bf16 v[216:219],v[64:67],v[140:143],v[216:219]
s_waitcnt lgkmcnt(4)
ds_read_b64_tr_b16 v[64:65],v1 offset:256
v_mfma_f32_16x16x32_bf16 v[224:227],v[80:83],v[32:35],v[224:227]
ds_read_b64_tr_b16 v[66:67],v1 offset:5376
ds_read_b64_tr_b16 v[70:71],v1 offset:15616
v_mfma_f32_16x16x32_bf16 v[224:227],v[84:87],v[36:39],v[224:227]
ds_read_b64_tr_b16 v[74:75],v1 offset:25856
v_mfma_f32_16x16x32_bf16 v[224:227],v[88:91],v[40:43],v[224:227]
v_mfma_f32_16x16x32_bf16 v[224:227],v[92:95],v[44:47],v[224:227]
v_mfma_f32_16x16x32_bf16 v[232:235],v[92:95],v[180:183],v[232:235]
v_mfma_f32_16x16x32_bf16 v[232:235],v[88:91],v[164:167],v[232:235]
v_mfma_f32_16x16x32_bf16 v[232:235],v[84:87],v[148:151],v[232:235]
v_mfma_f32_16x16x32_bf16 v[232:235],v[80:83],v[132:135],v[232:235]
v_mfma_f32_16x16x32_bf16 v[240:243],v[80:83],v[196:199],v[240:243]
v_mfma_f32_16x16x32_bf16 v[240:243],v[84:87],v[212:215],v[240:243]
v_mfma_f32_16x16x32_bf16 v[240:243],v[88:91],v[228:231],v[240:243]
v_mfma_f32_16x16x32_bf16 v[240:243],v[92:95],v[244:247],v[240:243]
v_mfma_f32_16x16x32_bf16 v[248:251],v[92:95],v[188:191],v[248:251]
ds_read_b64_tr_b16 v[92:93],v2 offset:31008
ds_read_b64_tr_b16 v[94:95],v2 offset:36128
v_mfma_f32_16x16x32_bf16 v[248:251],v[88:91],v[172:175],v[248:251]
ds_read_b64_tr_b16 v[88:89],v2 offset:20768
v_mfma_f32_16x16x32_bf16 v[248:251],v[84:87],v[156:159],v[248:251]
ds_read_b64_tr_b16 v[84:85],v2 offset:10528
v_mfma_f32_16x16x32_bf16 v[248:251],v[80:83],v[140:143],v[248:251]
ds_read_b64_tr_b16 v[80:81],v2 offset:288
v_mfma_f32_16x16x32_bf16 v[192:195],v[96:99],a[128:131],v[192:195]
ds_read_b64_tr_b16 v[82:83],v2 offset:5408
ds_read_b64_tr_b16 v[86:87],v2 offset:15648
v_mfma_f32_16x16x32_bf16 v[192:195],v[100:103],a[144:147],v[192:195]
ds_read_b64_tr_b16 v[90:91],v2 offset:25888
v_mfma_f32_16x16x32_bf16 v[192:195],v[104:107],a[160:163],v[192:195]
v_mfma_f32_16x16x32_bf16 v[192:195],v[108:111],a[176:179],v[192:195]
v_mfma_f32_16x16x32_bf16 v[200:203],v[108:111],a[180:183],v[200:203]
v_mfma_f32_16x16x32_bf16 v[200:203],v[104:107],a[164:167],v[200:203]
v_mfma_f32_16x16x32_bf16 v[200:203],v[100:103],a[148:151],v[200:203]
v_mfma_f32_16x16x32_bf16 v[200:203],v[96:99],a[132:135],v[200:203]
v_mfma_f32_16x16x32_bf16 v[208:211],v[96:99],a[136:139],v[208:211]
v_mfma_f32_16x16x32_bf16 v[208:211],v[100:103],a[152:155],v[208:211]
v_mfma_f32_16x16x32_bf16 v[208:211],v[104:107],a[168:171],v[208:211]
v_mfma_f32_16x16x32_bf16 v[208:211],v[108:111],a[184:187],v[208:211]
v_mfma_f32_16x16x32_bf16 v[216:219],v[108:111],a[188:191],v[216:219]
v_mfma_f32_16x16x32_bf16 v[216:219],v[104:107],a[172:175],v[216:219]
v_mfma_f32_16x16x32_bf16 v[216:219],v[100:103],a[156:159],v[216:219]
v_mfma_f32_16x16x32_bf16 v[216:219],v[96:99],a[140:143],v[216:219]
v_mfma_f32_16x16x32_bf16 v[224:227],v[112:115],a[128:131],v[224:227]
v_mfma_f32_16x16x32_bf16 v[224:227],v[116:119],a[144:147],v[224:227]
v_mfma_f32_16x16x32_bf16 v[224:227],v[120:123],a[160:163],v[224:227]
v_mfma_f32_16x16x32_bf16 v[224:227],v[124:127],a[176:179],v[224:227]
v_mfma_f32_16x16x32_bf16 v[232:235],v[124:127],a[180:183],v[232:235]
v_mfma_f32_16x16x32_bf16 v[232:235],v[120:123],a[164:167],v[232:235]
v_mfma_f32_16x16x32_bf16 v[232:235],v[116:119],a[148:151],v[232:235]
v_mfma_f32_16x16x32_bf16 v[232:235],v[112:115],a[132:135],v[232:235]
v_mfma_f32_16x16x32_bf16 v[240:243],v[112:115],a[136:139],v[240:243]
v_mfma_f32_16x16x32_bf16 v[240:243],v[116:119],a[152:155],v[240:243]
v_mfma_f32_16x16x32_bf16 v[240:243],v[120:123],a[168:171],v[240:243]
v_mfma_f32_16x16x32_bf16 v[240:243],v[124:127],a[184:187],v[240:243]
v_mfma_f32_16x16x32_bf16 v[248:251],v[124:127],a[188:191],v[248:251]
v_mfma_f32_16x16x32_bf16 v[248:251],v[120:123],a[172:175],v[248:251]
v_mfma_f32_16x16x32_bf16 v[248:251],v[116:119],a[156:159],v[248:251]
v_mfma_f32_16x16x32_bf16 v[248:251],v[112:115],a[140:143],v[248:251]
s_waitcnt lgkmcnt(0)
v_mfma_f32_16x16x32_bf16 v[192:195],v[64:67],a[68:71],v[192:195]
v_mfma_f32_16x16x32_bf16 v[192:195],v[68:71],a[76:79],v[192:195]
v_mfma_f32_16x16x32_bf16 v[192:195],v[72:75],a[84:87],v[192:195]
v_mfma_f32_16x16x32_bf16 v[192:195],v[76:79],a[92:95],v[192:195]
v_mfma_f32_16x16x32_bf16 v[200:203],v[76:79],a[124:127],v[200:203]
v_mfma_f32_16x16x32_bf16 v[200:203],v[72:75],a[116:119],v[200:203]
v_mfma_f32_16x16x32_bf16 v[200:203],v[68:71],a[108:111],v[200:203]
v_mfma_f32_16x16x32_bf16 v[200:203],v[64:67],a[100:103],v[200:203]
v_mfma_f32_16x16x32_bf16 v[208:211],v[64:67],a[64:67],v[208:211]
v_mfma_f32_16x16x32_bf16 v[208:211],v[68:71],a[72:75],v[208:211]
v_mfma_f32_16x16x32_bf16 v[208:211],v[72:75],a[80:83],v[208:211]
v_mfma_f32_16x16x32_bf16 v[208:211],v[76:79],a[88:91],v[208:211]
v_mfma_f32_16x16x32_bf16 v[216:219],v[76:79],a[120:123],v[216:219]
v_mfma_f32_16x16x32_bf16 v[216:219],v[72:75],a[112:115],v[216:219]
v_mfma_f32_16x16x32_bf16 v[216:219],v[68:71],a[104:107],v[216:219]
v_mfma_f32_16x16x32_bf16 v[216:219],v[64:67],a[96:99],v[216:219]
v_mfma_f32_16x16x32_bf16 v[224:227],v[80:83],a[68:71],v[224:227]
buffer_load_dwordx2 v[62:63],v61,s[44:47],s91 offen offset:2312 sc1
v_mfma_f32_16x16x32_bf16 v[224:227],v[84:87],a[76:79],v[224:227]
v_mfma_f32_16x16x32_bf16 v[224:227],v[88:91],a[84:87],v[224:227]
v_mfma_f32_16x16x32_bf16 v[224:227],v[92:95],a[92:95],v[224:227]
v_mfma_f32_16x16x32_bf16 v[232:235],v[92:95],a[124:127],v[232:235]
v_mfma_f32_16x16x32_bf16 v[232:235],v[88:91],a[116:119],v[232:235]
v_mfma_f32_16x16x32_bf16 v[232:235],v[84:87],a[108:111],v[232:235]
v_mfma_f32_16x16x32_bf16 v[232:235],v[80:83],a[100:103],v[232:235]
v_mfma_f32_16x16x32_bf16 v[240:243],v[80:83],a[64:67],v[240:243]
v_mfma_f32_16x16x32_bf16 v[240:243],v[84:87],a[72:75],v[240:243]
v_mfma_f32_16x16x32_bf16 v[240:243],v[88:91],a[80:83],v[240:243]
v_mfma_f32_16x16x32_bf16 v[240:243],v[92:95],a[88:91],v[240:243]
v_mfma_f32_16x16x32_bf16 v[248:251],v[92:95],a[120:123],v[248:251]
v_mfma_f32_16x16x32_bf16 v[248:251],v[88:91],a[112:115],v[248:251]
v_mfma_f32_16x16x32_bf16 v[248:251],v[84:87],a[104:107],v[248:251]
v_mfma_f32_16x16x32_bf16 v[248:251],v[80:83],a[96:99],v[248:251]
s_waitcnt vmcnt(1)
s_barrier
buffer_store_dwordx2 v[20:21],v22,s[44:47],s18 offen offset:2304 sc1
s_waitcnt vmcnt(1)
v_cmp_ne_u32 s[56:57],s52,v62
v_cmp_ne_u32 s[58:59],s53,v63
s_nop 0
s_or_b64 s[56:57],s[56:57],s[58:59]
s_cbranch_scc1 L_fr8_%=
L_fo8_%=:
buffer_load_dwordx4 v[64:67],v6,s[36:39],s66 offen sc1
buffer_load_dwordx4 v[68:71],v6,s[36:39],s67 offen sc1
buffer_load_dwordx4 v[72:75],v6,s[36:39],s66 offen offset:1024 sc1
buffer_load_dwordx4 v[76:79],v6,s[36:39],s67 offen offset:1024 sc1
buffer_load_dwordx4 v[80:83],v6,s[36:39],s66 offen offset:2048 sc1
buffer_load_dwordx4 v[84:87],v6,s[36:39],s67 offen offset:2048 sc1
buffer_load_dwordx4 v[88:91],v28,s[36:39],s66 offen offset:3072 sc1
buffer_load_dwordx4 v[92:95],v28,s[36:39],s67 offen offset:3072 sc1
buffer_load_dwordx2 v[4:5],v3,s[44:47],s91 offen offset:2304 sc1
s_waitcnt vmcnt(8)
v_mfma_f32_16x16x32_bf16 v[192:195],v[48:51],v[64:67],v[192:195]
s_waitcnt vmcnt(7)
v_mfma_f32_16x16x32_bf16 v[192:195],v[48:51],v[68:71],v[192:195]
v_mfma_f32_16x16x32_bf16 v[224:227],v[52:55],v[64:67],v[224:227]
v_mfma_f32_16x16x32_bf16 v[224:227],v[52:55],v[68:71],v[224:227]
s_waitcnt vmcnt(6)
v_mfma_f32_16x16x32_bf16 v[200:203],v[48:51],v[72:75],v[200:203]
s_waitcnt vmcnt(5)
v_mfma_f32_16x16x32_bf16 v[200:203],v[48:51],v[76:79],v[200:203]
v_mfma_f32_16x16x32_bf16 v[232:235],v[52:55],v[72:75],v[232:235]
v_mfma_f32_16x16x32_bf16 v[232:235],v[52:55],v[76:79],v[232:235]
s_waitcnt vmcnt(4)
v_mfma_f32_16x16x32_bf16 v[208:211],v[48:51],v[80:83],v[208:211]
s_waitcnt vmcnt(3)
v_mfma_f32_16x16x32_bf16 v[208:211],v[48:51],v[84:87],v[208:211]
v_mfma_f32_16x16x32_bf16 v[240:243],v[52:55],v[80:83],v[240:243]
v_mfma_f32_16x16x32_bf16 v[240:243],v[52:55],v[84:87],v[240:243]
s_waitcnt vmcnt(2)
v_mfma_f32_16x16x32_bf16 v[216:219],v[48:51],v[88:91],v[216:219]
s_waitcnt vmcnt(1)
v_mfma_f32_16x16x32_bf16 v[216:219],v[48:51],v[92:95],v[216:219]
v_mfma_f32_16x16x32_bf16 v[248:251],v[52:55],v[88:91],v[248:251]
v_mfma_f32_16x16x32_bf16 v[248:251],v[52:55],v[92:95],v[248:251]
s_waitcnt vmcnt(0)
v_cmp_ne_u32 s[56:57],s52,v4
v_cmp_ne_u32 s[58:59],s53,v5
s_nop 0
s_or_b64 s[56:57],s[56:57],s[58:59]
s_cbranch_scc1 L_fr9_%=
L_fo9_%=:
buffer_load_dwordx4 v[96:99],v6,s[36:39],s68 offen sc1
buffer_load_dwordx4 v[100:103],v6,s[36:39],s68 offen offset:1024 sc1
buffer_load_dwordx4 v[104:107],v6,s[36:39],s68 offen offset:2048 sc1
buffer_load_dwordx4 v[108:111],v6,s[36:39],s68 offen offset:3072 sc1
buffer_load_dwordx4 v[112:115],v6,s[36:39],s65 offen sc1
buffer_load_dwordx4 v[116:119],v6,s[36:39],s65 offen offset:1024 sc1
buffer_load_dwordx4 v[120:123],v6,s[36:39],s65 offen offset:2048 sc1
buffer_load_dwordx4 v[124:127],v28,s[36:39],s65 offen offset:3072 sc1
s_waitcnt vmcnt(7)
v_mfma_f32_16x16x32_bf16 v[128:131],v[48:51],v[96:99],0
s_waitcnt vmcnt(6)
v_mfma_f32_16x16x32_bf16 v[128:131],v[48:51],v[100:103],v[128:131]
s_waitcnt vmcnt(5)
v_mfma_f32_16x16x32_bf16 v[128:131],v[48:51],v[104:107],v[128:131]
s_waitcnt vmcnt(4)
v_mfma_f32_16x16x32_bf16 v[128:131],v[48:51],v[108:111],v[128:131]
v_mfma_f32_16x16x32_bf16 v[160:163],v[52:55],v[96:99],0
v_mfma_f32_16x16x32_bf16 v[160:163],v[52:55],v[100:103],v[160:163]
v_mfma_f32_16x16x32_bf16 v[160:163],v[52:55],v[104:107],v[160:163]
v_mfma_f32_16x16x32_bf16 v[160:163],v[52:55],v[108:111],v[160:163]
s_waitcnt vmcnt(3)
v_mfma_f32_16x16x32_bf16 v[192:195],v[48:51],v[112:115],v[192:195]
v_mfma_f32_16x16x32_bf16 v[224:227],v[52:55],v[112:115],v[224:227]
)A"
R"A(s_waitcnt vmcnt(2)
v_mfma_f32_16x16x32_bf16 v[200:203],v[48:51],v[116:119],v[200:203]
v_mfma_f32_16x16x32_bf16 v[232:235],v[52:55],v[116:119],v[232:235]
s_waitcnt vmcnt(1)
v_cvt_pk_bf16_f32 v128,v128,v129
v_cvt_pk_bf16_f32 v129,v130,v131
v_mfma_f32_16x16x32_bf16 v[208:211],v[48:51],v[120:123],v[208:211]
v_mfma_f32_16x16x32_bf16 v[240:243],v[52:55],v[120:123],v[240:243]
s_waitcnt vmcnt(0)
v_cvt_pk_bf16_f32 v130,v160,v161
v_cvt_pk_bf16_f32 v131,v162,v163
v_mfma_f32_16x16x32_bf16 v[216:219],v[48:51],v[124:127],v[216:219]
v_cvt_pk_bf16_f32 v192,v192,v193
v_cvt_pk_bf16_f32 v193,v194,v195
v_cvt_pk_bf16_f32 v194,v224,v225
v_cvt_pk_bf16_f32 v195,v226,v227
v_cvt_pk_bf16_f32 v200,v200,v201
v_cvt_pk_bf16_f32 v201,v202,v203
v_mfma_f32_16x16x32_bf16 v[248:251],v[52:55],v[124:127],v[248:251]
v_permlane16_swap_b32 v128,v130
v_permlane16_swap_b32 v129,v131
v_cvt_pk_bf16_f32 v202,v232,v233
v_cvt_pk_bf16_f32 v203,v234,v235
v_cvt_pk_bf16_f32 v208,v208,v209
v_cvt_pk_bf16_f32 v209,v210,v211
v_cvt_pk_bf16_f32 v210,v240,v241
v_cvt_pk_bf16_f32 v211,v242,v243
buffer_store_dwordx4 v[128:131],v56,s[24:27],s83 offen offset:256 sc1 nt
v_permlane16_swap_b32 v192,v194
v_permlane16_swap_b32 v193,v195
buffer_store_dwordx4 v[192:195],v56,s[24:27],s87 offen sc1 nt
v_permlane16_swap_b32 v200,v202
v_permlane16_swap_b32 v201,v203
v_cvt_pk_bf16_f32 v216,v216,v217
v_cvt_pk_bf16_f32 v217,v218,v219
v_cvt_pk_bf16_f32 v218,v248,v249
v_cvt_pk_bf16_f32 v219,v250,v251
buffer_store_dwordx4 v[200:203],v56,s[24:27],s88 offen sc1 nt
v_permlane16_swap_b32 v208,v210
v_permlane16_swap_b32 v209,v211
buffer_store_dwordx4 v[208:211],v56,s[24:27],s89 offen sc1 nt
v_permlane16_swap_b32 v216,v218
v_permlane16_swap_b32 v217,v219
buffer_store_dwordx4 v[216:219],v56,s[24:27],s90 offen sc1 nt
s_lshl_b32 s69,s62,20
s_add_u32 s69,s69,s18
buffer_store_dword v20,v22,s[44:47],s69 offen sc1
L_geS_%=:
s_branch L_end_%=
L_hot_%=:
L_fr1_%=:
s_mov_b32 s19,65536
L_fl1_%=:
s_sleep 6
buffer_load_dwordx2 v[4:5],v3,s[44:47],s91 offen offset:256 sc1
s_waitcnt vmcnt(0)
s_sub_u32 s19,s19,1
s_cmp_eq_u32 s19,0
s_cbranch_scc1 L_fo1_%=
v_cmp_ne_u32 s[56:57],s52,v4
v_cmp_ne_u32 s[58:59],s53,v5
s_nop 1
s_or_b64 s[56:57],s[56:57],s[58:59]
s_cbranch_scc1 L_fl1_%=
s_branch L_fo1_%=
L_fr2_%=:
s_mov_b32 s19,65536
L_fl2_%=:
s_sleep 6
buffer_load_dwordx2 v[4:5],v3,s[44:47],s91 offen offset:1280 sc1
s_waitcnt vmcnt(0)
s_sub_u32 s19,s19,1
s_cmp_eq_u32 s19,0
s_cbranch_scc1 L_fo2_%=
v_cmp_ne_u32 s[56:57],s52,v4
v_cmp_ne_u32 s[58:59],s53,v5
s_nop 1
s_or_b64 s[56:57],s[56:57],s[58:59]
s_cbranch_scc1 L_fl2_%=
s_branch L_fo2_%=
L_fr3_%=:
s_mov_b32 s19,65536
L_fl3_%=:
s_sleep 6
buffer_load_dwordx2 v[4:5],v3,s[44:47],s91 offen offset:1280 sc1
s_waitcnt vmcnt(0)
s_sub_u32 s19,s19,1
s_cmp_eq_u32 s19,0
s_cbranch_scc1 L_fo3_%=
v_cmp_ne_u32 s[56:57],s52,v4
v_cmp_ne_u32 s[58:59],s53,v5
s_nop 1
s_or_b64 s[56:57],s[56:57],s[58:59]
s_cbranch_scc1 L_fl3_%=
s_branch L_fo3_%=
L_fr4_%=:
s_mov_b32 s19,65536
L_fl4_%=:
s_sleep 6
buffer_load_dwordx2 v[62:63],v61,s[44:47],s91 offen offset:2312 sc1
s_waitcnt vmcnt(0)
s_sub_u32 s19,s19,1
s_cmp_eq_u32 s19,0
s_cbranch_scc1 L_fo4_%=
v_cmp_ne_u32 s[56:57],s52,v62
v_cmp_ne_u32 s[58:59],s53,v63
s_nop 1
s_or_b64 s[56:57],s[56:57],s[58:59]
s_cbranch_scc1 L_fl4_%=
s_branch L_fo4_%=
L_fr5_%=:
s_mov_b32 s19,65536
L_fl5_%=:
s_sleep 6
buffer_load_dwordx2 v[4:5],v3,s[44:47],s91 offen offset:2304 sc1
s_waitcnt vmcnt(0)
s_sub_u32 s19,s19,1
s_cmp_eq_u32 s19,0
s_cbranch_scc1 L_fo5_%=
v_cmp_ne_u32 s[56:57],s52,v4
v_cmp_ne_u32 s[58:59],s53,v5
s_nop 1
s_or_b64 s[56:57],s[56:57],s[58:59]
s_cbranch_scc1 L_fl5_%=
s_branch L_fo5_%=
L_fr6_%=:
s_mov_b32 s19,65536
L_fl6_%=:
s_sleep 6
buffer_load_dwordx2 v[4:5],v3,s[44:47],s91 offen offset:256 sc1
s_waitcnt vmcnt(0)
s_sub_u32 s19,s19,1
s_cmp_eq_u32 s19,0
s_cbranch_scc1 L_fo6_%=
v_cmp_ne_u32 s[56:57],s52,v4
v_cmp_ne_u32 s[58:59],s53,v5
s_nop 1
s_or_b64 s[56:57],s[56:57],s[58:59]
s_cbranch_scc1 L_fl6_%=
s_branch L_fo6_%=
L_fr7_%=:
s_mov_b32 s19,65536
L_fl7_%=:
s_sleep 6
buffer_load_dwordx2 v[4:5],v3,s[44:47],s91 offen offset:1280 sc1
s_waitcnt vmcnt(0)
s_sub_u32 s19,s19,1
s_cmp_eq_u32 s19,0
s_cbranch_scc1 L_fo7_%=
v_cmp_ne_u32 s[56:57],s52,v4
v_cmp_ne_u32 s[58:59],s53,v5
s_nop 1
s_or_b64 s[56:57],s[56:57],s[58:59]
s_cbranch_scc1 L_fl7_%=
s_branch L_fo7_%=
L_fr8_%=:
s_mov_b32 s19,65536
L_fl8_%=:
s_sleep 6
buffer_load_dwordx2 v[62:63],v61,s[44:47],s91 offen offset:2312 sc1
s_waitcnt vmcnt(0)
s_sub_u32 s19,s19,1
s_cmp_eq_u32 s19,0
s_cbranch_scc1 L_fo8_%=
v_cmp_ne_u32 s[56:57],s52,v62
v_cmp_ne_u32 s[58:59],s53,v63
s_nop 1
s_or_b64 s[56:57],s[56:57],s[58:59]
s_cbranch_scc1 L_fl8_%=
s_branch L_fo8_%=
L_fr9_%=:
s_mov_b32 s19,65536
L_fl9_%=:
s_sleep 6
buffer_load_dwordx2 v[4:5],v3,s[44:47],s91 offen offset:2304 sc1
s_waitcnt vmcnt(0)
s_sub_u32 s19,s19,1
s_cmp_eq_u32 s19,0
s_cbranch_scc1 L_fo9_%=
v_cmp_ne_u32 s[56:57],s52,v4
v_cmp_ne_u32 s[58:59],s53,v5
s_nop 1
s_or_b64 s[56:57],s[56:57],s[58:59]
s_cbranch_scc1 L_fl9_%=
s_branch L_fo9_%=
L_slow0_%=:
s_waitcnt vmcnt(0) lgkmcnt(0)
s_nop 7
v_accvgpr_write_b32 a160,v32
v_accvgpr_write_b32 a161,v33
v_accvgpr_write_b32 a162,v34
v_accvgpr_write_b32 a163,v35
v_accvgpr_write_b32 a164,v36
v_accvgpr_write_b32 a165,v37
v_accvgpr_write_b32 a166,v38
v_accvgpr_write_b32 a167,v39
v_accvgpr_write_b32 a168,v40
v_accvgpr_write_b32 a169,v41
v_accvgpr_write_b32 a170,v42
v_accvgpr_write_b32 a171,v43
v_accvgpr_write_b32 a172,v44
v_accvgpr_write_b32 a173,v45
v_accvgpr_write_b32 a174,v46
v_accvgpr_write_b32 a175,v47
s_waitcnt vmcnt(0) lgkmcnt(0)
s_nop 7
v_bfe_u32 v11,v6,4,4
v_lshrrev_b32 v12,8,v6
v_lshlrev_b32 v8,8,v11
v_lshl_add_u32 v8,v12,4,v8
v_lshlrev_b32 v9,10,v12
v_lshl_add_u32 v9,v11,1,v9
v_add_u32 v10,4096,v9
v_mov_b32 v48,0xff800000
v_mov_b32 v52,0xff800000
v_mov_b32 v56,0xff800000
v_mov_b32 v60,0xff800000
s_mov_b32 s65,0
s_mov_b32 s66,64
L_s10_%=:
buffer_load_dwordx4 a[128:131],v8,s[20:23],s65 offen
buffer_load_dwordx4 a[132:135],v8,s[20:23],s65 offen offset:64
buffer_load_dwordx4 a[136:139],v8,s[20:23],s65 offen offset:128
buffer_load_dwordx4 a[140:143],v8,s[20:23],s65 offen offset:192
s_waitcnt vmcnt(0)
v_mfma_f32_16x16x32_bf16 v[64:67],a[128:131],v[132:135],0
v_mfma_f32_16x16x32_bf16 v[64:67],a[132:135],v[140:143],v[64:67]
v_mfma_f32_16x16x32_bf16 v[64:67],a[136:139],v[148:151],v[64:67]
v_mfma_f32_16x16x32_bf16 v[64:67],a[140:143],v[156:159],v[64:67]
v_mfma_f32_16x16x32_bf16 v[80:83],a[128:131],v[164:167],0
v_mfma_f32_16x16x32_bf16 v[80:83],a[132:135],v[172:175],v[80:83]
v_mfma_f32_16x16x32_bf16 v[80:83],a[136:139],v[180:183],v[80:83]
v_mfma_f32_16x16x32_bf16 v[80:83],a[140:143],v[188:191],v[80:83]
v_mfma_f32_16x16x32_bf16 v[96:99],a[128:131],v[196:199],0
v_mfma_f32_16x16x32_bf16 v[96:99],a[132:135],v[204:207],v[96:99]
v_mfma_f32_16x16x32_bf16 v[96:99],a[136:139],v[212:215],v[96:99]
v_mfma_f32_16x16x32_bf16 v[96:99],a[140:143],v[220:223],v[96:99]
v_mfma_f32_16x16x32_bf16 v[112:115],a[128:131],v[228:231],0
v_mfma_f32_16x16x32_bf16 v[112:115],a[132:135],v[236:239],v[112:115]
v_mfma_f32_16x16x32_bf16 v[112:115],a[136:139],v[244:247],v[112:115]
v_mfma_f32_16x16x32_bf16 v[112:115],a[140:143],v[252:255],v[112:115]
s_nop 7
v_max3_f32 v48,v48,v64,v65
v_max3_f32 v48,v48,v66,v67
v_max3_f32 v52,v52,v80,v81
v_max3_f32 v52,v52,v82,v83
v_max3_f32 v56,v56,v96,v97
v_max3_f32 v56,v56,v98,v99
v_max3_f32 v60,v60,v112,v113
v_max3_f32 v60,v60,v114,v115
s_add_u32 s65,s65,4096
s_sub_u32 s66,s66,1
s_cmp_lg_u32 s66,0
s_cbranch_scc1 L_s10_%=
v_mov_b32 v11,v48
s_nop 1
v_permlane32_swap_b32 v48,v11
v_max_f32 v48,v48,v11
v_mov_b32 v11,v48
s_nop 1
v_permlane16_swap_b32 v48,v11
v_max_f32 v48,v48,v11
v_sub_f32 v32,0,v48
v_sub_f32 v33,0,v48
v_sub_f32 v34,0,v48
v_sub_f32 v35,0,v48
v_mov_b32 v11,v52
s_nop 1
v_permlane32_swap_b32 v52,v11
v_max_f32 v52,v52,v11
v_mov_b32 v11,v52
s_nop 1
v_permlane16_swap_b32 v52,v11
v_max_f32 v52,v52,v11
v_sub_f32 v36,0,v52
v_sub_f32 v37,0,v52
v_sub_f32 v38,0,v52
v_sub_f32 v39,0,v52
v_mov_b32 v11,v56
s_nop 1
v_permlane32_swap_b32 v56,v11
v_max_f32 v56,v56,v11
v_mov_b32 v11,v56
s_nop 1
v_permlane16_swap_b32 v56,v11
v_max_f32 v56,v56,v11
v_sub_f32 v40,0,v56
v_sub_f32 v41,0,v56
v_sub_f32 v42,0,v56
v_sub_f32 v43,0,v56
v_mov_b32 v11,v60
s_nop 1
v_permlane32_swap_b32 v60,v11
v_max_f32 v60,v60,v11
v_mov_b32 v11,v60
s_nop 1
v_permlane16_swap_b32 v60,v11
v_max_f32 v60,v60,v11
v_max_f32 v60,s72,v60
v_sub_f32 v44,0,v60
v_sub_f32 v45,0,v60
v_sub_f32 v46,0,v60
v_sub_f32 v47,0,v60
v_mov_b32 v48,0
v_mov_b32 v49,0
v_mov_b32 v50,0
v_mov_b32 v51,0
v_mov_b32 v52,0
v_mov_b32 v53,0
v_mov_b32 v54,0
v_mov_b32 v55,0
v_mov_b32 v56,0
v_mov_b32 v57,0
v_mov_b32 v58,0
v_mov_b32 v59,0
v_mov_b32 v60,0
v_mov_b32 v61,0
v_mov_b32 v62,0
v_mov_b32 v63,0
v_accvgpr_write_b32 a0,0
v_accvgpr_write_b32 a1,0
v_accvgpr_write_b32 a2,0
v_accvgpr_write_b32 a3,0
v_accvgpr_write_b32 a4,0
v_accvgpr_write_b32 a5,0
v_accvgpr_write_b32 a6,0
v_accvgpr_write_b32 a7,0
v_accvgpr_write_b32 a8,0
v_accvgpr_write_b32 a9,0
v_accvgpr_write_b32 a10,0
v_accvgpr_write_b32 a11,0
v_accvgpr_write_b32 a12,0
v_accvgpr_write_b32 a13,0
v_accvgpr_write_b32 a14,0
v_accvgpr_write_b32 a15,0
v_accvgpr_write_b32 a16,0
v_accvgpr_write_b32 a17,0
v_accvgpr_write_b32 a18,0
v_accvgpr_write_b32 a19,0
v_accvgpr_write_b32 a20,0
v_accvgpr_write_b32 a21,0
v_accvgpr_write_b32 a22,0
v_accvgpr_write_b32 a23,0
v_accvgpr_write_b32 a24,0
v_accvgpr_write_b32 a25,0
v_accvgpr_write_b32 a26,0
v_accvgpr_write_b32 a27,0
v_accvgpr_write_b32 a28,0
v_accvgpr_write_b32 a29,0
v_accvgpr_write_b32 a30,0
v_accvgpr_write_b32 a31,0
v_accvgpr_write_b32 a32,0
v_accvgpr_write_b32 a33,0
v_accvgpr_write_b32 a34,0
v_accvgpr_write_b32 a35,0
v_accvgpr_write_b32 a36,0
v_accvgpr_write_b32 a37,0
v_accvgpr_write_b32 a38,0
v_accvgpr_write_b32 a39,0
v_accvgpr_write_b32 a40,0
v_accvgpr_write_b32 a41,0
v_accvgpr_write_b32 a42,0
v_accvgpr_write_b32 a43,0
v_accvgpr_write_b32 a44,0
v_accvgpr_write_b32 a45,0
v_accvgpr_write_b32 a46,0
v_accvgpr_write_b32 a47,0
v_accvgpr_write_b32 a48,0
v_accvgpr_write_b32 a49,0
v_accvgpr_write_b32 a50,0
v_accvgpr_write_b32 a51,0
v_accvgpr_write_b32 a52,0
v_accvgpr_write_b32 a53,0
v_accvgpr_write_b32 a54,0
v_accvgpr_write_b32 a55,0
v_accvgpr_write_b32 a56,0
v_accvgpr_write_b32 a57,0
v_accvgpr_write_b32 a58,0
v_accvgpr_write_b32 a59,0
v_accvgpr_write_b32 a60,0
v_accvgpr_write_b32 a61,0
v_accvgpr_write_b32 a62,0
v_accvgpr_write_b32 a63,0
v_accvgpr_write_b32 a64,0
v_accvgpr_write_b32 a65,0
v_accvgpr_write_b32 a66,0
v_accvgpr_write_b32 a67,0
v_accvgpr_write_b32 a68,0
v_accvgpr_write_b32 a69,0
v_accvgpr_write_b32 a70,0
v_accvgpr_write_b32 a71,0
v_accvgpr_write_b32 a72,0
v_accvgpr_write_b32 a73,0
v_accvgpr_write_b32 a74,0
v_accvgpr_write_b32 a75,0
v_accvgpr_write_b32 a76,0
v_accvgpr_write_b32 a77,0
v_accvgpr_write_b32 a78,0
v_accvgpr_write_b32 a79,0
v_accvgpr_write_b32 a80,0
v_accvgpr_write_b32 a81,0
v_accvgpr_write_b32 a82,0
v_accvgpr_write_b32 a83,0
v_accvgpr_write_b32 a84,0
v_accvgpr_write_b32 a85,0
v_accvgpr_write_b32 a86,0
v_accvgpr_write_b32 a87,0
v_accvgpr_write_b32 a88,0
v_accvgpr_write_b32 a89,0
v_accvgpr_write_b32 a90,0
v_accvgpr_write_b32 a91,0
v_accvgpr_write_b32 a92,0
v_accvgpr_write_b32 a93,0
v_accvgpr_write_b32 a94,0
v_accvgpr_write_b32 a95,0
v_accvgpr_write_b32 a96,0
v_accvgpr_write_b32 a97,0
v_accvgpr_write_b32 a98,0
v_accvgpr_write_b32 a99,0
v_accvgpr_write_b32 a100,0
v_accvgpr_write_b32 a101,0
v_accvgpr_write_b32 a102,0
v_accvgpr_write_b32 a103,0
v_accvgpr_write_b32 a104,0
v_accvgpr_write_b32 a105,0
v_accvgpr_write_b32 a106,0
v_accvgpr_write_b32 a107,0
v_accvgpr_write_b32 a108,0
v_accvgpr_write_b32 a109,0
v_accvgpr_write_b32 a110,0
v_accvgpr_write_b32 a111,0
v_accvgpr_write_b32 a112,0
v_accvgpr_write_b32 a113,0
v_accvgpr_write_b32 a114,0
v_accvgpr_write_b32 a115,0
v_accvgpr_write_b32 a116,0
v_accvgpr_write_b32 a117,0
v_accvgpr_write_b32 a118,0
v_accvgpr_write_b32 a119,0
v_accvgpr_write_b32 a120,0
v_accvgpr_write_b32 a121,0
v_accvgpr_write_b32 a122,0
v_accvgpr_write_b32 a123,0
v_accvgpr_write_b32 a124,0
v_accvgpr_write_b32 a125,0
v_accvgpr_write_b32 a126,0
v_accvgpr_write_b32 a127,0
)A"
R"A(s_mov_b32 s65,0
s_mov_b32 s66,32
L_s20_%=:
s_add_u32 s67,s65,4096
buffer_load_dwordx4 a[128:131],v8,s[20:23],s65 offen
buffer_load_dwordx4 a[132:135],v8,s[20:23],s65 offen offset:64
buffer_load_dwordx4 a[136:139],v8,s[20:23],s65 offen offset:128
buffer_load_dwordx4 a[140:143],v8,s[20:23],s65 offen offset:192
buffer_load_dwordx4 a[144:147],v8,s[20:23],s67 offen
buffer_load_dwordx4 a[148:151],v8,s[20:23],s67 offen offset:64
buffer_load_dwordx4 a[152:155],v8,s[20:23],s67 offen offset:128
buffer_load_dwordx4 a[156:159],v8,s[20:23],s67 offen offset:192
s_waitcnt vmcnt(0)
v_mfma_f32_16x16x32_bf16 v[64:67],a[128:131],v[132:135],v[32:35]
v_mfma_f32_16x16x32_bf16 v[64:67],a[132:135],v[140:143],v[64:67]
v_mfma_f32_16x16x32_bf16 v[64:67],a[136:139],v[148:151],v[64:67]
v_mfma_f32_16x16x32_bf16 v[64:67],a[140:143],v[156:159],v[64:67]
v_mfma_f32_16x16x32_bf16 v[68:71],a[144:147],v[132:135],v[32:35]
v_mfma_f32_16x16x32_bf16 v[68:71],a[148:151],v[140:143],v[68:71]
v_mfma_f32_16x16x32_bf16 v[68:71],a[152:155],v[148:151],v[68:71]
v_mfma_f32_16x16x32_bf16 v[68:71],a[156:159],v[156:159],v[68:71]
v_mfma_f32_16x16x32_bf16 v[80:83],a[128:131],v[164:167],v[36:39]
v_mfma_f32_16x16x32_bf16 v[80:83],a[132:135],v[172:175],v[80:83]
v_mfma_f32_16x16x32_bf16 v[80:83],a[136:139],v[180:183],v[80:83]
v_mfma_f32_16x16x32_bf16 v[80:83],a[140:143],v[188:191],v[80:83]
v_mfma_f32_16x16x32_bf16 v[84:87],a[144:147],v[164:167],v[36:39]
v_mfma_f32_16x16x32_bf16 v[84:87],a[148:151],v[172:175],v[84:87]
v_mfma_f32_16x16x32_bf16 v[84:87],a[152:155],v[180:183],v[84:87]
v_mfma_f32_16x16x32_bf16 v[84:87],a[156:159],v[188:191],v[84:87]
v_mfma_f32_16x16x32_bf16 v[96:99],a[128:131],v[196:199],v[40:43]
v_mfma_f32_16x16x32_bf16 v[96:99],a[132:135],v[204:207],v[96:99]
v_mfma_f32_16x16x32_bf16 v[96:99],a[136:139],v[212:215],v[96:99]
v_mfma_f32_16x16x32_bf16 v[96:99],a[140:143],v[220:223],v[96:99]
v_mfma_f32_16x16x32_bf16 v[100:103],a[144:147],v[196:199],v[40:43]
v_mfma_f32_16x16x32_bf16 v[100:103],a[148:151],v[204:207],v[100:103]
v_mfma_f32_16x16x32_bf16 v[100:103],a[152:155],v[212:215],v[100:103]
v_mfma_f32_16x16x32_bf16 v[100:103],a[156:159],v[220:223],v[100:103]
v_mfma_f32_16x16x32_bf16 v[112:115],a[128:131],v[228:231],v[44:47]
v_mfma_f32_16x16x32_bf16 v[112:115],a[132:135],v[236:239],v[112:115]
v_mfma_f32_16x16x32_bf16 v[112:115],a[136:139],v[244:247],v[112:115]
v_mfma_f32_16x16x32_bf16 v[112:115],a[140:143],v[252:255],v[112:115]
v_mfma_f32_16x16x32_bf16 v[116:119],a[144:147],v[228:231],v[44:47]
v_mfma_f32_16x16x32_bf16 v[116:119],a[148:151],v[236:239],v[116:119]
v_mfma_f32_16x16x32_bf16 v[116:119],a[152:155],v[244:247],v[116:119]
v_mfma_f32_16x16x32_bf16 v[116:119],a[156:159],v[252:255],v[116:119]
s_nop 7
v_exp_f32 v64,v64
v_exp_f32 v65,v65
v_exp_f32 v66,v66
v_exp_f32 v67,v67
v_exp_f32 v68,v68
v_exp_f32 v69,v69
v_exp_f32 v70,v70
v_exp_f32 v71,v71
s_nop 0
v_cvt_pk_bf16_f32 v64,v64,v65
v_cvt_pk_bf16_f32 v65,v66,v67
v_cvt_pk_bf16_f32 v66,v68,v69
v_cvt_pk_bf16_f32 v67,v70,v71
v_exp_f32 v80,v80
v_exp_f32 v81,v81
v_exp_f32 v82,v82
v_exp_f32 v83,v83
v_exp_f32 v84,v84
v_exp_f32 v85,v85
v_exp_f32 v86,v86
v_exp_f32 v87,v87
s_nop 0
v_cvt_pk_bf16_f32 v80,v80,v81
v_cvt_pk_bf16_f32 v81,v82,v83
v_cvt_pk_bf16_f32 v82,v84,v85
v_cvt_pk_bf16_f32 v83,v86,v87
v_exp_f32 v96,v96
v_exp_f32 v97,v97
v_exp_f32 v98,v98
v_exp_f32 v99,v99
v_exp_f32 v100,v100
v_exp_f32 v101,v101
v_exp_f32 v102,v102
v_exp_f32 v103,v103
s_nop 0
v_cvt_pk_bf16_f32 v96,v96,v97
v_cvt_pk_bf16_f32 v97,v98,v99
v_cvt_pk_bf16_f32 v98,v100,v101
v_cvt_pk_bf16_f32 v99,v102,v103
v_exp_f32 v112,v112
v_exp_f32 v113,v113
v_exp_f32 v114,v114
v_exp_f32 v115,v115
v_exp_f32 v116,v116
v_exp_f32 v117,v117
v_exp_f32 v118,v118
v_exp_f32 v119,v119
s_nop 0
v_cvt_pk_bf16_f32 v112,v112,v113
v_cvt_pk_bf16_f32 v113,v114,v115
v_cvt_pk_bf16_f32 v114,v116,v117
v_cvt_pk_bf16_f32 v115,v118,v119
buffer_load_ushort v72,v9,s[24:27],s65 offen
buffer_load_ushort v73,v9,s[24:27],s65 offen offset:256
buffer_load_ushort v74,v9,s[24:27],s65 offen offset:512
buffer_load_ushort v75,v9,s[24:27],s65 offen offset:768
buffer_load_ushort v76,v10,s[24:27],s65 offen
buffer_load_ushort v77,v10,s[24:27],s65 offen offset:256
buffer_load_ushort v78,v10,s[24:27],s65 offen offset:512
buffer_load_ushort v79,v10,s[24:27],s65 offen offset:768
s_waitcnt vmcnt(0)
v_lshl_or_b32 v88,v73,16,v72
v_lshl_or_b32 v89,v75,16,v74
v_lshl_or_b32 v90,v77,16,v76
v_lshl_or_b32 v91,v79,16,v78
s_nop 1
v_mfma_f32_16x16x32_bf16 a[0:3],v[88:91],v[64:67],a[0:3]
v_mfma_f32_16x16x32_bf16 a[32:35],v[88:91],v[80:83],a[32:35]
v_mfma_f32_16x16x32_bf16 a[64:67],v[88:91],v[96:99],a[64:67]
v_mfma_f32_16x16x32_bf16 a[96:99],v[88:91],v[112:115],a[96:99]
buffer_load_ushort v72,v9,s[24:27],s65 offen offset:32
buffer_load_ushort v73,v9,s[24:27],s65 offen offset:288
buffer_load_ushort v74,v9,s[24:27],s65 offen offset:544
buffer_load_ushort v75,v9,s[24:27],s65 offen offset:800
buffer_load_ushort v76,v10,s[24:27],s65 offen offset:32
buffer_load_ushort v77,v10,s[24:27],s65 offen offset:288
buffer_load_ushort v78,v10,s[24:27],s65 offen offset:544
buffer_load_ushort v79,v10,s[24:27],s65 offen offset:800
s_waitcnt vmcnt(0)
v_lshl_or_b32 v88,v73,16,v72
v_lshl_or_b32 v89,v75,16,v74
v_lshl_or_b32 v90,v77,16,v76
v_lshl_or_b32 v91,v79,16,v78
s_nop 1
v_mfma_f32_16x16x32_bf16 a[4:7],v[88:91],v[64:67],a[4:7]
v_mfma_f32_16x16x32_bf16 a[36:39],v[88:91],v[80:83],a[36:39]
v_mfma_f32_16x16x32_bf16 a[68:71],v[88:91],v[96:99],a[68:71]
v_mfma_f32_16x16x32_bf16 a[100:103],v[88:91],v[112:115],a[100:103]
buffer_load_ushort v72,v9,s[24:27],s65 offen offset:64
buffer_load_ushort v73,v9,s[24:27],s65 offen offset:320
buffer_load_ushort v74,v9,s[24:27],s65 offen offset:576
buffer_load_ushort v75,v9,s[24:27],s65 offen offset:832
buffer_load_ushort v76,v10,s[24:27],s65 offen offset:64
buffer_load_ushort v77,v10,s[24:27],s65 offen offset:320
buffer_load_ushort v78,v10,s[24:27],s65 offen offset:576
buffer_load_ushort v79,v10,s[24:27],s65 offen offset:832
s_waitcnt vmcnt(0)
v_lshl_or_b32 v88,v73,16,v72
v_lshl_or_b32 v89,v75,16,v74
v_lshl_or_b32 v90,v77,16,v76
v_lshl_or_b32 v91,v79,16,v78
s_nop 1
v_mfma_f32_16x16x32_bf16 a[8:11],v[88:91],v[64:67],a[8:11]
v_mfma_f32_16x16x32_bf16 a[40:43],v[88:91],v[80:83],a[40:43]
v_mfma_f32_16x16x32_bf16 a[72:75],v[88:91],v[96:99],a[72:75]
v_mfma_f32_16x16x32_bf16 a[104:107],v[88:91],v[112:115],a[104:107]
buffer_load_ushort v72,v9,s[24:27],s65 offen offset:96
buffer_load_ushort v73,v9,s[24:27],s65 offen offset:352
buffer_load_ushort v74,v9,s[24:27],s65 offen offset:608
buffer_load_ushort v75,v9,s[24:27],s65 offen offset:864
buffer_load_ushort v76,v10,s[24:27],s65 offen offset:96
buffer_load_ushort v77,v10,s[24:27],s65 offen offset:352
buffer_load_ushort v78,v10,s[24:27],s65 offen offset:608
buffer_load_ushort v79,v10,s[24:27],s65 offen offset:864
s_waitcnt vmcnt(0)
v_lshl_or_b32 v88,v73,16,v72
v_lshl_or_b32 v89,v75,16,v74
v_lshl_or_b32 v90,v77,16,v76
v_lshl_or_b32 v91,v79,16,v78
s_nop 1
v_mfma_f32_16x16x32_bf16 a[12:15],v[88:91],v[64:67],a[12:15]
v_mfma_f32_16x16x32_bf16 a[44:47],v[88:91],v[80:83],a[44:47]
v_mfma_f32_16x16x32_bf16 a[76:79],v[88:91],v[96:99],a[76:79]
v_mfma_f32_16x16x32_bf16 a[108:111],v[88:91],v[112:115],a[108:111]
buffer_load_ushort v72,v9,s[24:27],s65 offen offset:128
buffer_load_ushort v73,v9,s[24:27],s65 offen offset:384
buffer_load_ushort v74,v9,s[24:27],s65 offen offset:640
buffer_load_ushort v75,v9,s[24:27],s65 offen offset:896
buffer_load_ushort v76,v10,s[24:27],s65 offen offset:128
buffer_load_ushort v77,v10,s[24:27],s65 offen offset:384
buffer_load_ushort v78,v10,s[24:27],s65 offen offset:640
buffer_load_ushort v79,v10,s[24:27],s65 offen offset:896
s_waitcnt vmcnt(0)
v_lshl_or_b32 v88,v73,16,v72
v_lshl_or_b32 v89,v75,16,v74
v_lshl_or_b32 v90,v77,16,v76
v_lshl_or_b32 v91,v79,16,v78
s_nop 1
v_mfma_f32_16x16x32_bf16 a[16:19],v[88:91],v[64:67],a[16:19]
v_mfma_f32_16x16x32_bf16 a[48:51],v[88:91],v[80:83],a[48:51]
v_mfma_f32_16x16x32_bf16 a[80:83],v[88:91],v[96:99],a[80:83]
v_mfma_f32_16x16x32_bf16 a[112:115],v[88:91],v[112:115],a[112:115]
buffer_load_ushort v72,v9,s[24:27],s65 offen offset:160
buffer_load_ushort v73,v9,s[24:27],s65 offen offset:416
buffer_load_ushort v74,v9,s[24:27],s65 offen offset:672
buffer_load_ushort v75,v9,s[24:27],s65 offen offset:928
buffer_load_ushort v76,v10,s[24:27],s65 offen offset:160
buffer_load_ushort v77,v10,s[24:27],s65 offen offset:416
buffer_load_ushort v78,v10,s[24:27],s65 offen offset:672
buffer_load_ushort v79,v10,s[24:27],s65 offen offset:928
s_waitcnt vmcnt(0)
v_lshl_or_b32 v88,v73,16,v72
v_lshl_or_b32 v89,v75,16,v74
v_lshl_or_b32 v90,v77,16,v76
v_lshl_or_b32 v91,v79,16,v78
s_nop 1
v_mfma_f32_16x16x32_bf16 a[20:23],v[88:91],v[64:67],a[20:23]
v_mfma_f32_16x16x32_bf16 a[52:55],v[88:91],v[80:83],a[52:55]
v_mfma_f32_16x16x32_bf16 a[84:87],v[88:91],v[96:99],a[84:87]
v_mfma_f32_16x16x32_bf16 a[116:119],v[88:91],v[112:115],a[116:119]
buffer_load_ushort v72,v9,s[24:27],s65 offen offset:192
buffer_load_ushort v73,v9,s[24:27],s65 offen offset:448
buffer_load_ushort v74,v9,s[24:27],s65 offen offset:704
buffer_load_ushort v75,v9,s[24:27],s65 offen offset:960
buffer_load_ushort v76,v10,s[24:27],s65 offen offset:192
buffer_load_ushort v77,v10,s[24:27],s65 offen offset:448
buffer_load_ushort v78,v10,s[24:27],s65 offen offset:704
buffer_load_ushort v79,v10,s[24:27],s65 offen offset:960
s_waitcnt vmcnt(0)
v_lshl_or_b32 v88,v73,16,v72
v_lshl_or_b32 v89,v75,16,v74
v_lshl_or_b32 v90,v77,16,v76
v_lshl_or_b32 v91,v79,16,v78
s_nop 1
v_mfma_f32_16x16x32_bf16 a[24:27],v[88:91],v[64:67],a[24:27]
v_mfma_f32_16x16x32_bf16 a[56:59],v[88:91],v[80:83],a[56:59]
v_mfma_f32_16x16x32_bf16 a[88:91],v[88:91],v[96:99],a[88:91]
v_mfma_f32_16x16x32_bf16 a[120:123],v[88:91],v[112:115],a[120:123]
buffer_load_ushort v72,v9,s[24:27],s65 offen offset:224
buffer_load_ushort v73,v9,s[24:27],s65 offen offset:480
buffer_load_ushort v74,v9,s[24:27],s65 offen offset:736
buffer_load_ushort v75,v9,s[24:27],s65 offen offset:992
buffer_load_ushort v76,v10,s[24:27],s65 offen offset:224
buffer_load_ushort v77,v10,s[24:27],s65 offen offset:480
buffer_load_ushort v78,v10,s[24:27],s65 offen offset:736
buffer_load_ushort v79,v10,s[24:27],s65 offen offset:992
s_waitcnt vmcnt(0)
v_lshl_or_b32 v88,v73,16,v72
v_lshl_or_b32 v89,v75,16,v74
v_lshl_or_b32 v90,v77,16,v76
v_lshl_or_b32 v91,v79,16,v78
s_nop 1
v_mfma_f32_16x16x32_bf16 a[28:31],v[88:91],v[64:67],a[28:31]
v_mfma_f32_16x16x32_bf16 a[60:63],v[88:91],v[80:83],a[60:63]
v_mfma_f32_16x16x32_bf16 a[92:95],v[88:91],v[96:99],a[92:95]
v_mfma_f32_16x16x32_bf16 a[124:127],v[88:91],v[112:115],a[124:127]
v_mfma_f32_16x16x32_bf16 v[48:51],v[28:31],v[64:67],v[48:51]
v_mfma_f32_16x16x32_bf16 v[52:55],v[28:31],v[80:83],v[52:55]
v_mfma_f32_16x16x32_bf16 v[56:59],v[28:31],v[96:99],v[56:59]
v_mfma_f32_16x16x32_bf16 v[60:63],v[28:31],v[112:115],v[60:63]
s_add_u32 s65,s65,8192
s_sub_u32 s66,s66,1
s_cmp_lg_u32 s66,0
s_cbranch_scc1 L_s20_%=
s_nop 7
v_max_f32 v24,0x00800000,v48
v_rcp_f32 v24,v24
v_max_f32 v25,0x00800000,v52
v_rcp_f32 v25,v25
v_max_f32 v26,0x00800000,v56
v_rcp_f32 v26,v26
v_max_f32 v27,0x00800000,v60
v_rcp_f32 v27,v27
v_accvgpr_read_b32 v8,a0
v_accvgpr_read_b32 v9,a1
v_accvgpr_read_b32 v10,a2
v_accvgpr_read_b32 v11,a3
v_accvgpr_read_b32 v12,a4
v_accvgpr_read_b32 v13,a5
v_accvgpr_read_b32 v14,a6
v_accvgpr_read_b32 v15,a7
v_mul_f32 v8,v8,v24
v_mul_f32 v9,v9,v24
v_mul_f32 v10,v10,v24
v_mul_f32 v11,v11,v24
v_mul_f32 v12,v12,v24
v_mul_f32 v13,v13,v24
v_mul_f32 v14,v14,v24
v_mul_f32 v15,v15,v24
v_cvt_pk_bf16_f32 v8,v8,v9
v_cvt_pk_bf16_f32 v9,v10,v11
v_cvt_pk_bf16_f32 v10,v12,v13
v_cvt_pk_bf16_f32 v11,v14,v15
ds_write_b128 v7,v[8:11] offset:6144
v_accvgpr_read_b32 v16,a8
v_accvgpr_read_b32 v17,a9
v_accvgpr_read_b32 v18,a10
v_accvgpr_read_b32 v19,a11
v_accvgpr_read_b32 v20,a12
v_accvgpr_read_b32 v21,a13
v_accvgpr_read_b32 v22,a14
v_accvgpr_read_b32 v23,a15
v_mul_f32 v16,v16,v24
v_mul_f32 v17,v17,v24
v_mul_f32 v18,v18,v24
v_mul_f32 v19,v19,v24
v_mul_f32 v20,v20,v24
v_mul_f32 v21,v21,v24
v_mul_f32 v22,v22,v24
v_mul_f32 v23,v23,v24
v_cvt_pk_bf16_f32 v16,v16,v17
)A"
R"A(v_cvt_pk_bf16_f32 v17,v18,v19
v_cvt_pk_bf16_f32 v18,v20,v21
v_cvt_pk_bf16_f32 v19,v22,v23
ds_write_b128 v7,v[16:19] offset:7168
v_accvgpr_read_b32 v8,a16
v_accvgpr_read_b32 v9,a17
v_accvgpr_read_b32 v10,a18
v_accvgpr_read_b32 v11,a19
v_accvgpr_read_b32 v12,a20
v_accvgpr_read_b32 v13,a21
v_accvgpr_read_b32 v14,a22
v_accvgpr_read_b32 v15,a23
v_mul_f32 v8,v8,v24
v_mul_f32 v9,v9,v24
v_mul_f32 v10,v10,v24
v_mul_f32 v11,v11,v24
v_mul_f32 v12,v12,v24
v_mul_f32 v13,v13,v24
v_mul_f32 v14,v14,v24
v_mul_f32 v15,v15,v24
v_cvt_pk_bf16_f32 v8,v8,v9
v_cvt_pk_bf16_f32 v9,v10,v11
v_cvt_pk_bf16_f32 v10,v12,v13
v_cvt_pk_bf16_f32 v11,v14,v15
ds_write_b128 v7,v[8:11]
v_accvgpr_read_b32 v16,a24
v_accvgpr_read_b32 v17,a25
v_accvgpr_read_b32 v18,a26
v_accvgpr_read_b32 v19,a27
v_accvgpr_read_b32 v20,a28
v_accvgpr_read_b32 v21,a29
v_accvgpr_read_b32 v22,a30
v_accvgpr_read_b32 v23,a31
v_mul_f32 v16,v16,v24
v_mul_f32 v17,v17,v24
v_mul_f32 v18,v18,v24
v_mul_f32 v19,v19,v24
v_mul_f32 v20,v20,v24
v_mul_f32 v21,v21,v24
v_mul_f32 v22,v22,v24
v_mul_f32 v23,v23,v24
v_cvt_pk_bf16_f32 v16,v16,v17
v_cvt_pk_bf16_f32 v17,v18,v19
v_cvt_pk_bf16_f32 v18,v20,v21
v_cvt_pk_bf16_f32 v19,v22,v23
ds_write_b128 v7,v[16:19] offset:1024
v_accvgpr_read_b32 v32,a160
v_accvgpr_read_b32 v33,a161
v_accvgpr_read_b32 v34,a162
v_accvgpr_read_b32 v35,a163
v_accvgpr_read_b32 v36,a164
v_accvgpr_read_b32 v37,a165
v_accvgpr_read_b32 v38,a166
v_accvgpr_read_b32 v39,a167
v_accvgpr_read_b32 v40,a168
v_accvgpr_read_b32 v41,a169
v_accvgpr_read_b32 v42,a170
v_accvgpr_read_b32 v43,a171
v_accvgpr_read_b32 v44,a172
v_accvgpr_read_b32 v45,a173
v_accvgpr_read_b32 v46,a174
v_accvgpr_read_b32 v47,a175
s_waitcnt lgkmcnt(0)
v_subrev_u32 v8,s78,v1
ds_read_b128 a[128:131],v8
ds_read_b128 a[132:135],v8 offset:256
ds_read_b128 a[136:139],v8 offset:512
ds_read_b128 a[140:143],v8 offset:768
ds_read_b128 a[144:147],v8 offset:4096
ds_read_b128 a[148:151],v8 offset:4352
ds_read_b128 a[152:155],v8 offset:4608
ds_read_b128 a[156:159],v8 offset:4864
ds_read_b128 a[160:163],v8 offset:8192
ds_read_b128 a[164:167],v8 offset:8448
ds_read_b128 a[168:171],v8 offset:8704
ds_read_b128 a[172:175],v8 offset:8960
ds_read_b128 a[176:179],v8 offset:12288
ds_read_b128 a[180:183],v8 offset:12544
ds_read_b128 a[184:187],v8 offset:12800
ds_read_b128 a[188:191],v8 offset:13056
s_waitcnt lgkmcnt(15)
v_mfma_f32_16x16x32_bf16 v[64:67],a[128:131],v[128:131],v[32:35]
s_waitcnt lgkmcnt(14)
v_mfma_f32_16x16x32_bf16 v[64:67],a[132:135],v[136:139],v[64:67]
s_waitcnt lgkmcnt(13)
v_mfma_f32_16x16x32_bf16 v[64:67],a[136:139],v[144:147],v[64:67]
s_waitcnt lgkmcnt(12)
v_mfma_f32_16x16x32_bf16 v[64:67],a[140:143],v[152:155],v[64:67]
s_waitcnt lgkmcnt(8)
v_mfma_f32_16x16x32_bf16 v[68:71],a[156:159],v[152:155],v[32:35]
v_mfma_f32_16x16x32_bf16 v[68:71],a[152:155],v[144:147],v[68:71]
v_mfma_f32_16x16x32_bf16 v[68:71],a[148:151],v[136:139],v[68:71]
v_mfma_f32_16x16x32_bf16 v[68:71],a[144:147],v[128:131],v[68:71]
s_waitcnt lgkmcnt(0)
v_mfma_f32_16x16x32_bf16 v[72:75],a[160:163],v[128:131],v[32:35]
v_mfma_f32_16x16x32_bf16 v[72:75],a[164:167],v[136:139],v[72:75]
v_mfma_f32_16x16x32_bf16 v[72:75],a[168:171],v[144:147],v[72:75]
v_mfma_f32_16x16x32_bf16 v[72:75],a[172:175],v[152:155],v[72:75]
v_mfma_f32_16x16x32_bf16 v[76:79],a[188:191],v[152:155],v[32:35]
v_mfma_f32_16x16x32_bf16 v[76:79],a[184:187],v[144:147],v[76:79]
v_mfma_f32_16x16x32_bf16 v[76:79],a[180:183],v[136:139],v[76:79]
v_mfma_f32_16x16x32_bf16 v[76:79],a[176:179],v[128:131],v[76:79]
v_mfma_f32_16x16x32_bf16 v[80:83],a[128:131],v[160:163],v[36:39]
v_mfma_f32_16x16x32_bf16 v[80:83],a[132:135],v[168:171],v[80:83]
v_mfma_f32_16x16x32_bf16 v[80:83],a[136:139],v[176:179],v[80:83]
v_mfma_f32_16x16x32_bf16 v[80:83],a[140:143],v[184:187],v[80:83]
v_mfma_f32_16x16x32_bf16 v[84:87],a[156:159],v[184:187],v[36:39]
v_mfma_f32_16x16x32_bf16 v[84:87],a[152:155],v[176:179],v[84:87]
v_mfma_f32_16x16x32_bf16 v[84:87],a[148:151],v[168:171],v[84:87]
v_mfma_f32_16x16x32_bf16 v[84:87],a[144:147],v[160:163],v[84:87]
v_mfma_f32_16x16x32_bf16 v[88:91],a[160:163],v[160:163],v[36:39]
v_mfma_f32_16x16x32_bf16 v[88:91],a[164:167],v[168:171],v[88:91]
v_mfma_f32_16x16x32_bf16 v[88:91],a[168:171],v[176:179],v[88:91]
v_mfma_f32_16x16x32_bf16 v[88:91],a[172:175],v[184:187],v[88:91]
v_mfma_f32_16x16x32_bf16 v[92:95],a[188:191],v[184:187],v[36:39]
v_mfma_f32_16x16x32_bf16 v[92:95],a[184:187],v[176:179],v[92:95]
v_mfma_f32_16x16x32_bf16 v[92:95],a[180:183],v[168:171],v[92:95]
v_mfma_f32_16x16x32_bf16 v[92:95],a[176:179],v[160:163],v[92:95]
v_mfma_f32_16x16x32_bf16 v[96:99],a[128:131],v[192:195],v[40:43]
v_mfma_f32_16x16x32_bf16 v[96:99],a[132:135],v[200:203],v[96:99]
v_mfma_f32_16x16x32_bf16 v[96:99],a[136:139],v[208:211],v[96:99]
v_mfma_f32_16x16x32_bf16 v[96:99],a[140:143],v[216:219],v[96:99]
v_mfma_f32_16x16x32_bf16 v[100:103],a[156:159],v[216:219],v[40:43]
v_mfma_f32_16x16x32_bf16 v[100:103],a[152:155],v[208:211],v[100:103]
v_mfma_f32_16x16x32_bf16 v[100:103],a[148:151],v[200:203],v[100:103]
v_mfma_f32_16x16x32_bf16 v[100:103],a[144:147],v[192:195],v[100:103]
v_mfma_f32_16x16x32_bf16 v[104:107],a[160:163],v[192:195],v[40:43]
v_mfma_f32_16x16x32_bf16 v[104:107],a[164:167],v[200:203],v[104:107]
v_mfma_f32_16x16x32_bf16 v[104:107],a[168:171],v[208:211],v[104:107]
v_mfma_f32_16x16x32_bf16 v[104:107],a[172:175],v[216:219],v[104:107]
v_mfma_f32_16x16x32_bf16 v[108:111],a[188:191],v[216:219],v[40:43]
v_mfma_f32_16x16x32_bf16 v[108:111],a[184:187],v[208:211],v[108:111]
v_mfma_f32_16x16x32_bf16 v[108:111],a[180:183],v[200:203],v[108:111]
v_mfma_f32_16x16x32_bf16 v[108:111],a[176:179],v[192:195],v[108:111]
v_exp_f32 v64,v64
v_exp_f32 v65,v65
s_nop 0
v_cvt_pk_bf16_f32 v64,v64,v65
v_exp_f32 v66,v66
v_exp_f32 v67,v67
s_nop 0
v_cvt_pk_bf16_f32 v65,v66,v67
v_exp_f32 v68,v68
v_exp_f32 v69,v69
s_nop 0
v_cvt_pk_bf16_f32 v66,v68,v69
v_exp_f32 v70,v70
v_exp_f32 v71,v71
s_nop 0
v_cvt_pk_bf16_f32 v67,v70,v71
v_exp_f32 v72,v72
v_exp_f32 v73,v73
s_nop 0
v_cvt_pk_bf16_f32 v72,v72,v73
v_exp_f32 v74,v74
v_exp_f32 v75,v75
s_nop 0
v_cvt_pk_bf16_f32 v73,v74,v75
v_exp_f32 v76,v76
v_exp_f32 v77,v77
s_nop 0
v_cvt_pk_bf16_f32 v74,v76,v77
v_exp_f32 v78,v78
v_exp_f32 v79,v79
s_nop 0
v_cvt_pk_bf16_f32 v75,v78,v79
v_exp_f32 v80,v80
v_exp_f32 v81,v81
s_nop 0
v_cvt_pk_bf16_f32 v80,v80,v81
v_exp_f32 v82,v82
v_exp_f32 v83,v83
s_nop 0
v_cvt_pk_bf16_f32 v81,v82,v83
v_exp_f32 v84,v84
v_exp_f32 v85,v85
s_nop 0
v_cvt_pk_bf16_f32 v82,v84,v85
v_exp_f32 v86,v86
v_exp_f32 v87,v87
s_nop 0
v_cvt_pk_bf16_f32 v83,v86,v87
v_exp_f32 v88,v88
v_exp_f32 v89,v89
s_nop 0
v_cvt_pk_bf16_f32 v88,v88,v89
v_mfma_f32_16x16x32_bf16 a[0:3],a[192:195],v[64:67],0
v_mfma_f32_16x16x32_bf16 a[0:3],a[196:199],v[72:75],a[0:3]
v_mfma_f32_16x16x32_bf16 a[4:7],a[200:203],v[64:67],0
v_mfma_f32_16x16x32_bf16 a[4:7],a[204:207],v[72:75],a[4:7]
v_mfma_f32_16x16x32_bf16 a[8:11],a[208:211],v[64:67],0
v_mfma_f32_16x16x32_bf16 a[8:11],a[212:215],v[72:75],a[8:11]
v_mfma_f32_16x16x32_bf16 a[12:15],a[216:219],v[64:67],0
v_mfma_f32_16x16x32_bf16 a[12:15],a[220:223],v[72:75],a[12:15]
v_mfma_f32_16x16x32_bf16 a[16:19],a[224:227],v[64:67],0
v_mfma_f32_16x16x32_bf16 a[16:19],a[228:231],v[72:75],a[16:19]
v_mfma_f32_16x16x32_bf16 a[20:23],a[232:235],v[64:67],0
v_mfma_f32_16x16x32_bf16 a[20:23],a[236:239],v[72:75],a[20:23]
v_mfma_f32_16x16x32_bf16 a[24:27],a[240:243],v[64:67],0
v_mfma_f32_16x16x32_bf16 a[24:27],a[244:247],v[72:75],a[24:27]
v_mfma_f32_16x16x32_bf16 a[28:31],a[248:251],v[64:67],0
v_mfma_f32_16x16x32_bf16 a[28:31],a[252:255],v[72:75],a[28:31]
v_mfma_f32_16x16x32_bf16 v[48:51],v[28:31],v[64:67],0
v_mfma_f32_16x16x32_bf16 v[48:51],v[28:31],v[72:75],v[48:51]
s_waitcnt vmcnt(0) lgkmcnt(0)
s_nop 7
s_branch L_ok0_%=
L_slow1_%=:
s_waitcnt vmcnt(0) lgkmcnt(0)
s_nop 7
v_bfe_u32 v11,v6,4,4
v_lshrrev_b32 v12,8,v6
v_lshlrev_b32 v8,8,v11
v_lshl_add_u32 v8,v12,4,v8
v_lshlrev_b32 v9,10,v12
v_lshl_add_u32 v9,v11,1,v9
v_add_u32 v10,4096,v9
v_mov_b32 v48,0xff800000
v_mov_b32 v52,0xff800000
v_mov_b32 v56,0xff800000
v_mov_b32 v60,0xff800000
s_mov_b32 s65,262144
s_mov_b32 s66,64
L_s11_%=:
buffer_load_dwordx4 a[128:131],v8,s[20:23],s65 offen
buffer_load_dwordx4 a[132:135],v8,s[20:23],s65 offen offset:64
buffer_load_dwordx4 a[136:139],v8,s[20:23],s65 offen offset:128
buffer_load_dwordx4 a[140:143],v8,s[20:23],s65 offen offset:192
s_waitcnt vmcnt(0)
v_mfma_f32_16x16x32_bf16 v[64:67],a[128:131],v[128:131],0
v_mfma_f32_16x16x32_bf16 v[64:67],a[132:135],v[136:139],v[64:67]
v_mfma_f32_16x16x32_bf16 v[64:67],a[136:139],v[144:147],v[64:67]
v_mfma_f32_16x16x32_bf16 v[64:67],a[140:143],v[152:155],v[64:67]
v_mfma_f32_16x16x32_bf16 v[80:83],a[128:131],v[160:163],0
v_mfma_f32_16x16x32_bf16 v[80:83],a[132:135],v[168:171],v[80:83]
v_mfma_f32_16x16x32_bf16 v[80:83],a[136:139],v[176:179],v[80:83]
v_mfma_f32_16x16x32_bf16 v[80:83],a[140:143],v[184:187],v[80:83]
v_mfma_f32_16x16x32_bf16 v[96:99],a[128:131],v[192:195],0
v_mfma_f32_16x16x32_bf16 v[96:99],a[132:135],v[200:203],v[96:99]
v_mfma_f32_16x16x32_bf16 v[96:99],a[136:139],v[208:211],v[96:99]
v_mfma_f32_16x16x32_bf16 v[96:99],a[140:143],v[216:219],v[96:99]
v_mfma_f32_16x16x32_bf16 v[112:115],a[128:131],v[224:227],0
v_mfma_f32_16x16x32_bf16 v[112:115],a[132:135],v[232:235],v[112:115]
v_mfma_f32_16x16x32_bf16 v[112:115],a[136:139],v[240:243],v[112:115]
v_mfma_f32_16x16x32_bf16 v[112:115],a[140:143],v[248:251],v[112:115]
s_nop 7
v_max3_f32 v48,v48,v64,v65
v_max3_f32 v48,v48,v66,v67
v_max3_f32 v52,v52,v80,v81
v_max3_f32 v52,v52,v82,v83
v_max3_f32 v56,v56,v96,v97
v_max3_f32 v56,v56,v98,v99
v_max3_f32 v60,v60,v112,v113
v_max3_f32 v60,v60,v114,v115
s_add_u32 s65,s65,4096
s_sub_u32 s66,s66,1
s_cmp_lg_u32 s66,0
s_cbranch_scc1 L_s11_%=
v_mov_b32 v11,v48
s_nop 1
v_permlane32_swap_b32 v48,v11
v_max_f32 v48,v48,v11
v_mov_b32 v11,v48
s_nop 1
v_permlane16_swap_b32 v48,v11
v_max_f32 v48,v48,v11
v_sub_f32 v32,0,v48
v_sub_f32 v33,0,v48
v_sub_f32 v34,0,v48
v_sub_f32 v35,0,v48
v_mov_b32 v11,v52
s_nop 1
v_permlane32_swap_b32 v52,v11
v_max_f32 v52,v52,v11
v_mov_b32 v11,v52
s_nop 1
v_permlane16_swap_b32 v52,v11
v_max_f32 v52,v52,v11
v_sub_f32 v36,0,v52
v_sub_f32 v37,0,v52
v_sub_f32 v38,0,v52
v_sub_f32 v39,0,v52
v_mov_b32 v11,v56
s_nop 1
v_permlane32_swap_b32 v56,v11
v_max_f32 v56,v56,v11
v_mov_b32 v11,v56
s_nop 1
v_permlane16_swap_b32 v56,v11
v_max_f32 v56,v56,v11
v_sub_f32 v40,0,v56
v_sub_f32 v41,0,v56
v_sub_f32 v42,0,v56
v_sub_f32 v43,0,v56
v_mov_b32 v11,v60
s_nop 1
v_permlane32_swap_b32 v60,v11
v_max_f32 v60,v60,v11
v_mov_b32 v11,v60
s_nop 1
v_permlane16_swap_b32 v60,v11
v_max_f32 v60,v60,v11
v_max_f32 v60,s72,v60
v_sub_f32 v44,0,v60
v_sub_f32 v45,0,v60
v_sub_f32 v46,0,v60
v_sub_f32 v47,0,v60
v_mov_b32 v48,0
v_mov_b32 v49,0
v_mov_b32 v50,0
v_mov_b32 v51,0
v_mov_b32 v52,0
v_mov_b32 v53,0
v_mov_b32 v54,0
v_mov_b32 v55,0
v_mov_b32 v56,0
v_mov_b32 v57,0
v_mov_b32 v58,0
v_mov_b32 v59,0
v_mov_b32 v60,0
v_mov_b32 v61,0
v_mov_b32 v62,0
v_mov_b32 v63,0
v_accvgpr_write_b32 a0,0
v_accvgpr_write_b32 a1,0
v_accvgpr_write_b32 a2,0
v_accvgpr_write_b32 a3,0
v_accvgpr_write_b32 a4,0
v_accvgpr_write_b32 a5,0
v_accvgpr_write_b32 a6,0
v_accvgpr_write_b32 a7,0
v_accvgpr_write_b32 a8,0
v_accvgpr_write_b32 a9,0
v_accvgpr_write_b32 a10,0
v_accvgpr_write_b32 a11,0
v_accvgpr_write_b32 a12,0
v_accvgpr_write_b32 a13,0
v_accvgpr_write_b32 a14,0
v_accvgpr_write_b32 a15,0
v_accvgpr_write_b32 a16,0
v_accvgpr_write_b32 a17,0
v_accvgpr_write_b32 a18,0
v_accvgpr_write_b32 a19,0
v_accvgpr_write_b32 a20,0
v_accvgpr_write_b32 a21,0
v_accvgpr_write_b32 a22,0
v_accvgpr_write_b32 a23,0
v_accvgpr_write_b32 a24,0
v_accvgpr_write_b32 a25,0
v_accvgpr_write_b32 a26,0
v_accvgpr_write_b32 a27,0
v_accvgpr_write_b32 a28,0
v_accvgpr_write_b32 a29,0
v_accvgpr_write_b32 a30,0
v_accvgpr_write_b32 a31,0
v_mov_b32 v132,0
v_mov_b32 v133,0
v_mov_b32 v134,0
v_mov_b32 v135,0
v_mov_b32 v140,0
v_mov_b32 v141,0
v_mov_b32 v142,0
v_mov_b32 v143,0
v_mov_b32 v148,0
v_mov_b32 v149,0
v_mov_b32 v150,0
v_mov_b32 v151,0
v_mov_b32 v156,0
v_mov_b32 v157,0
v_mov_b32 v158,0
v_mov_b32 v159,0
v_mov_b32 v164,0
v_mov_b32 v165,0
v_mov_b32 v166,0
)A"
R"A(v_mov_b32 v167,0
v_mov_b32 v172,0
v_mov_b32 v173,0
v_mov_b32 v174,0
v_mov_b32 v175,0
v_mov_b32 v180,0
v_mov_b32 v181,0
v_mov_b32 v182,0
v_mov_b32 v183,0
v_mov_b32 v188,0
v_mov_b32 v189,0
v_mov_b32 v190,0
v_mov_b32 v191,0
v_mov_b32 v196,0
v_mov_b32 v197,0
v_mov_b32 v198,0
v_mov_b32 v199,0
v_mov_b32 v204,0
v_mov_b32 v205,0
v_mov_b32 v206,0
v_mov_b32 v207,0
v_mov_b32 v212,0
v_mov_b32 v213,0
v_mov_b32 v214,0
v_mov_b32 v215,0
v_mov_b32 v220,0
v_mov_b32 v221,0
v_mov_b32 v222,0
v_mov_b32 v223,0
v_mov_b32 v228,0
v_mov_b32 v229,0
v_mov_b32 v230,0
v_mov_b32 v231,0
v_mov_b32 v236,0
v_mov_b32 v237,0
v_mov_b32 v238,0
v_mov_b32 v239,0
v_mov_b32 v244,0
v_mov_b32 v245,0
v_mov_b32 v246,0
v_mov_b32 v247,0
v_mov_b32 v252,0
v_mov_b32 v253,0
v_mov_b32 v254,0
v_mov_b32 v255,0
v_accvgpr_write_b32 a32,0
v_accvgpr_write_b32 a33,0
v_accvgpr_write_b32 a34,0
v_accvgpr_write_b32 a35,0
v_accvgpr_write_b32 a36,0
v_accvgpr_write_b32 a37,0
v_accvgpr_write_b32 a38,0
v_accvgpr_write_b32 a39,0
v_accvgpr_write_b32 a40,0
v_accvgpr_write_b32 a41,0
v_accvgpr_write_b32 a42,0
v_accvgpr_write_b32 a43,0
v_accvgpr_write_b32 a44,0
v_accvgpr_write_b32 a45,0
v_accvgpr_write_b32 a46,0
v_accvgpr_write_b32 a47,0
v_accvgpr_write_b32 a48,0
v_accvgpr_write_b32 a49,0
v_accvgpr_write_b32 a50,0
v_accvgpr_write_b32 a51,0
v_accvgpr_write_b32 a52,0
v_accvgpr_write_b32 a53,0
v_accvgpr_write_b32 a54,0
v_accvgpr_write_b32 a55,0
v_accvgpr_write_b32 a56,0
v_accvgpr_write_b32 a57,0
v_accvgpr_write_b32 a58,0
v_accvgpr_write_b32 a59,0
v_accvgpr_write_b32 a60,0
v_accvgpr_write_b32 a61,0
v_accvgpr_write_b32 a62,0
v_accvgpr_write_b32 a63,0
s_mov_b32 s65,262144
s_mov_b32 s66,32
L_s21_%=:
s_add_u32 s67,s65,4096
buffer_load_dwordx4 a[128:131],v8,s[20:23],s65 offen
buffer_load_dwordx4 a[132:135],v8,s[20:23],s65 offen offset:64
buffer_load_dwordx4 a[136:139],v8,s[20:23],s65 offen offset:128
buffer_load_dwordx4 a[140:143],v8,s[20:23],s65 offen offset:192
buffer_load_dwordx4 a[144:147],v8,s[20:23],s67 offen
buffer_load_dwordx4 a[148:151],v8,s[20:23],s67 offen offset:64
buffer_load_dwordx4 a[152:155],v8,s[20:23],s67 offen offset:128
buffer_load_dwordx4 a[156:159],v8,s[20:23],s67 offen offset:192
s_waitcnt vmcnt(0)
v_mfma_f32_16x16x32_bf16 v[64:67],a[128:131],v[128:131],v[32:35]
v_mfma_f32_16x16x32_bf16 v[64:67],a[132:135],v[136:139],v[64:67]
v_mfma_f32_16x16x32_bf16 v[64:67],a[136:139],v[144:147],v[64:67]
v_mfma_f32_16x16x32_bf16 v[64:67],a[140:143],v[152:155],v[64:67]
v_mfma_f32_16x16x32_bf16 v[68:71],a[144:147],v[128:131],v[32:35]
v_mfma_f32_16x16x32_bf16 v[68:71],a[148:151],v[136:139],v[68:71]
v_mfma_f32_16x16x32_bf16 v[68:71],a[152:155],v[144:147],v[68:71]
v_mfma_f32_16x16x32_bf16 v[68:71],a[156:159],v[152:155],v[68:71]
v_mfma_f32_16x16x32_bf16 v[80:83],a[128:131],v[160:163],v[36:39]
v_mfma_f32_16x16x32_bf16 v[80:83],a[132:135],v[168:171],v[80:83]
v_mfma_f32_16x16x32_bf16 v[80:83],a[136:139],v[176:179],v[80:83]
v_mfma_f32_16x16x32_bf16 v[80:83],a[140:143],v[184:187],v[80:83]
v_mfma_f32_16x16x32_bf16 v[84:87],a[144:147],v[160:163],v[36:39]
v_mfma_f32_16x16x32_bf16 v[84:87],a[148:151],v[168:171],v[84:87]
v_mfma_f32_16x16x32_bf16 v[84:87],a[152:155],v[176:179],v[84:87]
v_mfma_f32_16x16x32_bf16 v[84:87],a[156:159],v[184:187],v[84:87]
v_mfma_f32_16x16x32_bf16 v[96:99],a[128:131],v[192:195],v[40:43]
v_mfma_f32_16x16x32_bf16 v[96:99],a[132:135],v[200:203],v[96:99]
v_mfma_f32_16x16x32_bf16 v[96:99],a[136:139],v[208:211],v[96:99]
v_mfma_f32_16x16x32_bf16 v[96:99],a[140:143],v[216:219],v[96:99]
v_mfma_f32_16x16x32_bf16 v[100:103],a[144:147],v[192:195],v[40:43]
v_mfma_f32_16x16x32_bf16 v[100:103],a[148:151],v[200:203],v[100:103]
v_mfma_f32_16x16x32_bf16 v[100:103],a[152:155],v[208:211],v[100:103]
v_mfma_f32_16x16x32_bf16 v[100:103],a[156:159],v[216:219],v[100:103]
v_mfma_f32_16x16x32_bf16 v[112:115],a[128:131],v[224:227],v[44:47]
v_mfma_f32_16x16x32_bf16 v[112:115],a[132:135],v[232:235],v[112:115]
v_mfma_f32_16x16x32_bf16 v[112:115],a[136:139],v[240:243],v[112:115]
v_mfma_f32_16x16x32_bf16 v[112:115],a[140:143],v[248:251],v[112:115]
v_mfma_f32_16x16x32_bf16 v[116:119],a[144:147],v[224:227],v[44:47]
v_mfma_f32_16x16x32_bf16 v[116:119],a[148:151],v[232:235],v[116:119]
v_mfma_f32_16x16x32_bf16 v[116:119],a[152:155],v[240:243],v[116:119]
v_mfma_f32_16x16x32_bf16 v[116:119],a[156:159],v[248:251],v[116:119]
s_nop 7
v_exp_f32 v64,v64
v_exp_f32 v65,v65
v_exp_f32 v66,v66
v_exp_f32 v67,v67
v_exp_f32 v68,v68
v_exp_f32 v69,v69
v_exp_f32 v70,v70
v_exp_f32 v71,v71
s_nop 0
v_cvt_pk_bf16_f32 v64,v64,v65
v_cvt_pk_bf16_f32 v65,v66,v67
v_cvt_pk_bf16_f32 v66,v68,v69
v_cvt_pk_bf16_f32 v67,v70,v71
v_exp_f32 v80,v80
v_exp_f32 v81,v81
v_exp_f32 v82,v82
v_exp_f32 v83,v83
v_exp_f32 v84,v84
v_exp_f32 v85,v85
v_exp_f32 v86,v86
v_exp_f32 v87,v87
s_nop 0
v_cvt_pk_bf16_f32 v80,v80,v81
v_cvt_pk_bf16_f32 v81,v82,v83
v_cvt_pk_bf16_f32 v82,v84,v85
v_cvt_pk_bf16_f32 v83,v86,v87
v_exp_f32 v96,v96
v_exp_f32 v97,v97
v_exp_f32 v98,v98
v_exp_f32 v99,v99
v_exp_f32 v100,v100
v_exp_f32 v101,v101
v_exp_f32 v102,v102
v_exp_f32 v103,v103
s_nop 0
v_cvt_pk_bf16_f32 v96,v96,v97
v_cvt_pk_bf16_f32 v97,v98,v99
v_cvt_pk_bf16_f32 v98,v100,v101
v_cvt_pk_bf16_f32 v99,v102,v103
v_exp_f32 v112,v112
v_exp_f32 v113,v113
v_exp_f32 v114,v114
v_exp_f32 v115,v115
v_exp_f32 v116,v116
v_exp_f32 v117,v117
v_exp_f32 v118,v118
v_exp_f32 v119,v119
s_nop 0
v_cvt_pk_bf16_f32 v112,v112,v113
v_cvt_pk_bf16_f32 v113,v114,v115
v_cvt_pk_bf16_f32 v114,v116,v117
v_cvt_pk_bf16_f32 v115,v118,v119
buffer_load_ushort v72,v9,s[24:27],s65 offen
buffer_load_ushort v73,v9,s[24:27],s65 offen offset:256
buffer_load_ushort v74,v9,s[24:27],s65 offen offset:512
buffer_load_ushort v75,v9,s[24:27],s65 offen offset:768
buffer_load_ushort v76,v10,s[24:27],s65 offen
buffer_load_ushort v77,v10,s[24:27],s65 offen offset:256
buffer_load_ushort v78,v10,s[24:27],s65 offen offset:512
buffer_load_ushort v79,v10,s[24:27],s65 offen offset:768
s_waitcnt vmcnt(0)
v_lshl_or_b32 v88,v73,16,v72
v_lshl_or_b32 v89,v75,16,v74
v_lshl_or_b32 v90,v77,16,v76
v_lshl_or_b32 v91,v79,16,v78
s_nop 1
v_mfma_f32_16x16x32_bf16 a[0:3],v[88:91],v[64:67],a[0:3]
v_mfma_f32_16x16x32_bf16 v[132:135],v[88:91],v[80:83],v[132:135]
v_mfma_f32_16x16x32_bf16 v[196:199],v[88:91],v[96:99],v[196:199]
v_mfma_f32_16x16x32_bf16 a[32:35],v[88:91],v[112:115],a[32:35]
buffer_load_ushort v72,v9,s[24:27],s65 offen offset:32
buffer_load_ushort v73,v9,s[24:27],s65 offen offset:288
buffer_load_ushort v74,v9,s[24:27],s65 offen offset:544
buffer_load_ushort v75,v9,s[24:27],s65 offen offset:800
buffer_load_ushort v76,v10,s[24:27],s65 offen offset:32
buffer_load_ushort v77,v10,s[24:27],s65 offen offset:288
buffer_load_ushort v78,v10,s[24:27],s65 offen offset:544
buffer_load_ushort v79,v10,s[24:27],s65 offen offset:800
s_waitcnt vmcnt(0)
v_lshl_or_b32 v88,v73,16,v72
v_lshl_or_b32 v89,v75,16,v74
v_lshl_or_b32 v90,v77,16,v76
v_lshl_or_b32 v91,v79,16,v78
s_nop 1
v_mfma_f32_16x16x32_bf16 a[4:7],v[88:91],v[64:67],a[4:7]
v_mfma_f32_16x16x32_bf16 v[140:143],v[88:91],v[80:83],v[140:143]
v_mfma_f32_16x16x32_bf16 v[204:207],v[88:91],v[96:99],v[204:207]
v_mfma_f32_16x16x32_bf16 a[36:39],v[88:91],v[112:115],a[36:39]
buffer_load_ushort v72,v9,s[24:27],s65 offen offset:64
buffer_load_ushort v73,v9,s[24:27],s65 offen offset:320
buffer_load_ushort v74,v9,s[24:27],s65 offen offset:576
buffer_load_ushort v75,v9,s[24:27],s65 offen offset:832
buffer_load_ushort v76,v10,s[24:27],s65 offen offset:64
buffer_load_ushort v77,v10,s[24:27],s65 offen offset:320
buffer_load_ushort v78,v10,s[24:27],s65 offen offset:576
buffer_load_ushort v79,v10,s[24:27],s65 offen offset:832
s_waitcnt vmcnt(0)
v_lshl_or_b32 v88,v73,16,v72
v_lshl_or_b32 v89,v75,16,v74
v_lshl_or_b32 v90,v77,16,v76
v_lshl_or_b32 v91,v79,16,v78
s_nop 1
v_mfma_f32_16x16x32_bf16 a[8:11],v[88:91],v[64:67],a[8:11]
v_mfma_f32_16x16x32_bf16 v[148:151],v[88:91],v[80:83],v[148:151]
v_mfma_f32_16x16x32_bf16 v[212:215],v[88:91],v[96:99],v[212:215]
v_mfma_f32_16x16x32_bf16 a[40:43],v[88:91],v[112:115],a[40:43]
buffer_load_ushort v72,v9,s[24:27],s65 offen offset:96
buffer_load_ushort v73,v9,s[24:27],s65 offen offset:352
buffer_load_ushort v74,v9,s[24:27],s65 offen offset:608
buffer_load_ushort v75,v9,s[24:27],s65 offen offset:864
buffer_load_ushort v76,v10,s[24:27],s65 offen offset:96
buffer_load_ushort v77,v10,s[24:27],s65 offen offset:352
buffer_load_ushort v78,v10,s[24:27],s65 offen offset:608
buffer_load_ushort v79,v10,s[24:27],s65 offen offset:864
s_waitcnt vmcnt(0)
v_lshl_or_b32 v88,v73,16,v72
v_lshl_or_b32 v89,v75,16,v74
v_lshl_or_b32 v90,v77,16,v76
v_lshl_or_b32 v91,v79,16,v78
s_nop 1
v_mfma_f32_16x16x32_bf16 a[12:15],v[88:91],v[64:67],a[12:15]
v_mfma_f32_16x16x32_bf16 v[156:159],v[88:91],v[80:83],v[156:159]
v_mfma_f32_16x16x32_bf16 v[220:223],v[88:91],v[96:99],v[220:223]
v_mfma_f32_16x16x32_bf16 a[44:47],v[88:91],v[112:115],a[44:47]
buffer_load_ushort v72,v9,s[24:27],s65 offen offset:128
buffer_load_ushort v73,v9,s[24:27],s65 offen offset:384
buffer_load_ushort v74,v9,s[24:27],s65 offen offset:640
buffer_load_ushort v75,v9,s[24:27],s65 offen offset:896
buffer_load_ushort v76,v10,s[24:27],s65 offen offset:128
buffer_load_ushort v77,v10,s[24:27],s65 offen offset:384
buffer_load_ushort v78,v10,s[24:27],s65 offen offset:640
buffer_load_ushort v79,v10,s[24:27],s65 offen offset:896
s_waitcnt vmcnt(0)
v_lshl_or_b32 v88,v73,16,v72
v_lshl_or_b32 v89,v75,16,v74
v_lshl_or_b32 v90,v77,16,v76
v_lshl_or_b32 v91,v79,16,v78
s_nop 1
v_mfma_f32_16x16x32_bf16 a[16:19],v[88:91],v[64:67],a[16:19]
v_mfma_f32_16x16x32_bf16 v[164:167],v[88:91],v[80:83],v[164:167]
v_mfma_f32_16x16x32_bf16 v[228:231],v[88:91],v[96:99],v[228:231]
v_mfma_f32_16x16x32_bf16 a[48:51],v[88:91],v[112:115],a[48:51]
buffer_load_ushort v72,v9,s[24:27],s65 offen offset:160
buffer_load_ushort v73,v9,s[24:27],s65 offen offset:416
buffer_load_ushort v74,v9,s[24:27],s65 offen offset:672
buffer_load_ushort v75,v9,s[24:27],s65 offen offset:928
buffer_load_ushort v76,v10,s[24:27],s65 offen offset:160
buffer_load_ushort v77,v10,s[24:27],s65 offen offset:416
buffer_load_ushort v78,v10,s[24:27],s65 offen offset:672
buffer_load_ushort v79,v10,s[24:27],s65 offen offset:928
s_waitcnt vmcnt(0)
v_lshl_or_b32 v88,v73,16,v72
v_lshl_or_b32 v89,v75,16,v74
v_lshl_or_b32 v90,v77,16,v76
v_lshl_or_b32 v91,v79,16,v78
s_nop 1
v_mfma_f32_16x16x32_bf16 a[20:23],v[88:91],v[64:67],a[20:23]
v_mfma_f32_16x16x32_bf16 v[172:175],v[88:91],v[80:83],v[172:175]
v_mfma_f32_16x16x32_bf16 v[236:239],v[88:91],v[96:99],v[236:239]
v_mfma_f32_16x16x32_bf16 a[52:55],v[88:91],v[112:115],a[52:55]
buffer_load_ushort v72,v9,s[24:27],s65 offen offset:192
buffer_load_ushort v73,v9,s[24:27],s65 offen offset:448
buffer_load_ushort v74,v9,s[24:27],s65 offen offset:704
buffer_load_ushort v75,v9,s[24:27],s65 offen offset:960
buffer_load_ushort v76,v10,s[24:27],s65 offen offset:192
buffer_load_ushort v77,v10,s[24:27],s65 offen offset:448
buffer_load_ushort v78,v10,s[24:27],s65 offen offset:704
buffer_load_ushort v79,v10,s[24:27],s65 offen offset:960
s_waitcnt vmcnt(0)
v_lshl_or_b32 v88,v73,16,v72
v_lshl_or_b32 v89,v75,16,v74
v_lshl_or_b32 v90,v77,16,v76
v_lshl_or_b32 v91,v79,16,v78
s_nop 1
v_mfma_f32_16x16x32_bf16 a[24:27],v[88:91],v[64:67],a[24:27]
v_mfma_f32_16x16x32_bf16 v[180:183],v[88:91],v[80:83],v[180:183]
v_mfma_f32_16x16x32_bf16 v[244:247],v[88:91],v[96:99],v[244:247]
v_mfma_f32_16x16x32_bf16 a[56:59],v[88:91],v[112:115],a[56:59]
buffer_load_ushort v72,v9,s[24:27],s65 offen offset:224
buffer_load_ushort v73,v9,s[24:27],s65 offen offset:480
buffer_load_ushort v74,v9,s[24:27],s65 offen offset:736
buffer_load_ushort v75,v9,s[24:27],s65 offen offset:992
buffer_load_ushort v76,v10,s[24:27],s65 offen offset:224
buffer_load_ushort v77,v10,s[24:27],s65 offen offset:480
buffer_load_ushort v78,v10,s[24:27],s65 offen offset:736
buffer_load_ushort v79,v10,s[24:27],s65 offen offset:992
s_waitcnt vmcnt(0)
v_lshl_or_b32 v88,v73,16,v72
v_lshl_or_b32 v89,v75,16,v74
v_lshl_or_b32 v90,v77,16,v76
v_lshl_or_b32 v91,v79,16,v78
s_nop 1
v_mfma_f32_16x16x32_bf16 a[28:31],v[88:91],v[64:67],a[28:31]
v_mfma_f32_16x16x32_bf16 v[188:191],v[88:91],v[80:83],v[188:191]
)A"
R"A(v_mfma_f32_16x16x32_bf16 v[252:255],v[88:91],v[96:99],v[252:255]
v_mfma_f32_16x16x32_bf16 a[60:63],v[88:91],v[112:115],a[60:63]
v_mfma_f32_16x16x32_bf16 v[48:51],v[28:31],v[64:67],v[48:51]
v_mfma_f32_16x16x32_bf16 v[52:55],v[28:31],v[80:83],v[52:55]
v_mfma_f32_16x16x32_bf16 v[56:59],v[28:31],v[96:99],v[56:59]
v_mfma_f32_16x16x32_bf16 v[60:63],v[28:31],v[112:115],v[60:63]
s_add_u32 s65,s65,8192
s_sub_u32 s66,s66,1
s_cmp_lg_u32 s66,0
s_cbranch_scc1 L_s21_%=
s_nop 7
v_max_f32 v49,0x00800000,v48
v_rcp_f32 v49,v49
v_max_f32 v53,0x00800000,v52
v_rcp_f32 v53,v53
v_max_f32 v57,0x00800000,v56
v_rcp_f32 v57,v57
v_max_f32 v61,0x00800000,v60
v_rcp_f32 v61,v61
v_accvgpr_read_b32 v16,a0
v_accvgpr_read_b32 v17,a1
v_accvgpr_read_b32 v18,a2
v_accvgpr_read_b32 v19,a3
v_accvgpr_read_b32 v20,a4
v_accvgpr_read_b32 v21,a5
v_accvgpr_read_b32 v22,a6
v_accvgpr_read_b32 v23,a7
v_mul_f32 v16,v16,v49
v_mul_f32 v17,v17,v49
v_mul_f32 v18,v18,v49
v_mul_f32 v19,v19,v49
v_mul_f32 v20,v20,v49
v_mul_f32 v21,v21,v49
v_mul_f32 v22,v22,v49
v_mul_f32 v23,v23,v49
v_cvt_pk_bf16_f32 v32,v16,v17
v_cvt_pk_bf16_f32 v33,v18,v19
v_cvt_pk_bf16_f32 v34,v20,v21
v_cvt_pk_bf16_f32 v35,v22,v23
v_accvgpr_read_b32 v8,a8
v_accvgpr_read_b32 v9,a9
v_accvgpr_read_b32 v10,a10
v_accvgpr_read_b32 v11,a11
v_accvgpr_read_b32 v12,a12
v_accvgpr_read_b32 v13,a13
v_accvgpr_read_b32 v14,a14
v_accvgpr_read_b32 v15,a15
v_mul_f32 v8,v8,v49
v_mul_f32 v9,v9,v49
v_mul_f32 v10,v10,v49
v_mul_f32 v11,v11,v49
v_mul_f32 v12,v12,v49
v_mul_f32 v13,v13,v49
v_mul_f32 v14,v14,v49
v_mul_f32 v15,v15,v49
v_cvt_pk_bf16_f32 v36,v8,v9
v_cvt_pk_bf16_f32 v37,v10,v11
v_cvt_pk_bf16_f32 v38,v12,v13
v_cvt_pk_bf16_f32 v39,v14,v15
v_accvgpr_read_b32 v16,a16
v_accvgpr_read_b32 v17,a17
v_accvgpr_read_b32 v18,a18
v_accvgpr_read_b32 v19,a19
v_accvgpr_read_b32 v20,a20
v_accvgpr_read_b32 v21,a21
v_accvgpr_read_b32 v22,a22
v_accvgpr_read_b32 v23,a23
v_mul_f32 v16,v16,v49
v_mul_f32 v17,v17,v49
v_mul_f32 v18,v18,v49
v_mul_f32 v19,v19,v49
v_mul_f32 v20,v20,v49
v_mul_f32 v21,v21,v49
v_mul_f32 v22,v22,v49
v_mul_f32 v23,v23,v49
v_cvt_pk_bf16_f32 v40,v16,v17
v_cvt_pk_bf16_f32 v41,v18,v19
v_cvt_pk_bf16_f32 v42,v20,v21
v_cvt_pk_bf16_f32 v43,v22,v23
ds_read_b64_tr_b16 v[64:65],v1
ds_read_b64_tr_b16 v[66:67],v1 offset:5120
ds_read_b64_tr_b16 v[68:69],v1 offset:10240
ds_read_b64_tr_b16 v[70:71],v1 offset:15360
ds_read_b64_tr_b16 v[72:73],v1 offset:20480
ds_read_b64_tr_b16 v[74:75],v1 offset:25600
ds_read_b64_tr_b16 v[76:77],v1 offset:30720
ds_read_b64_tr_b16 v[78:79],v1 offset:35840
ds_read_b64_tr_b16 v[80:81],v2 offset:32
ds_read_b64_tr_b16 v[82:83],v2 offset:5152
ds_read_b64_tr_b16 v[84:85],v2 offset:10272
ds_read_b64_tr_b16 v[86:87],v2 offset:15392
ds_read_b64_tr_b16 v[88:89],v2 offset:20512
ds_read_b64_tr_b16 v[90:91],v2 offset:25632
ds_read_b64_tr_b16 v[92:93],v2 offset:30752
ds_read_b64_tr_b16 v[94:95],v2 offset:35872
s_waitcnt vmcnt(0) lgkmcnt(0)
s_nop 7
s_branch L_ok1_%=
L_end_%=:
)A"
    :: "s"(ka), "s"(blk), "s"(wid)
    : "v0", "v1", "v2", "v3", "v4", "v5", "v6", "v7", "v8", "v9", "v10", "v11", "v12", "v13", "v14", "v15", "v16", "v17", "v18", "v19", "v20", "v21", "v22", "v23", "v24", "v25", "v26", "v27", "v28", "v29", "v30", "v31", "v32", "v33", "v34", "v35", "v36", "v37", "v38", "v39", "v40", "v41", "v42", "v43", "v44", "v45", "v46", "v47", "v48", "v49", "v50", "v51", "v52", "v53", "v54", "v55", "v56", "v57", "v58", "v59", "v60", "v61", "v62", "v63", "v64", "v65", "v66", "v67", "v68", "v69", "v70", "v71", "v72", "v73", "v74", "v75", "v76", "v77", "v78", "v79", "v80", "v81", "v82", "v83", "v84", "v85", "v86", "v87", "v88", "v89", "v90", "v91", "v92", "v93", "v94", "v95", "v96", "v97", "v98", "v99", "v100", "v101", "v102", "v103", "v104", "v105", "v106", "v107", "v108", "v109", "v110", "v111", "v112", "v113", "v114", "v115", "v116", "v117", "v118", "v119", "v120", "v121", "v122", "v123", "v124", "v125", "v126", "v127", "v128", "v129", "v130", "v131", "v132", "v133", "v134", "v135", "v136", "v137", "v138", "v139", "v140", "v141", "v142", "v143", "v144", "v145", "v146", "v147", "v148", "v149", "v150", "v151", "v152", "v153", "v154", "v155", "v156", "v157", "v158", "v159", "v160", "v161", "v162", "v163", "v164", "v165", "v166", "v167", "v168", "v169", "v170", "v171", "v172", "v173", "v174", "v175", "v176", "v177", "v178", "v179", "v180", "v181", "v182", "v183", "v184", "v185", "v186", "v187", "v188", "v189", "v190", "v191", "v192", "v193", "v194", "v195", "v196", "v197", "v198", "v199", "v200", "v201", "v202", "v203", "v204", "v205", "v206", "v207", "v208", "v209", "v210", "v211", "v212", "v213", "v214", "v215", "v216", "v217", "v218", "v219", "v220", "v221", "v222", "v223", "v224", "v225", "v226", "v227", "v228", "v229", "v230", "v231", "v232", "v233", "v234", "v235", "v236", "v237", "v238", "v239", "v240", "v241", "v242", "v243", "v244", "v245", "v246", "v247", "v248", "v249", "v250", "v251", "v252", "v253", "v254", "v255", "a0", "a1", "a2", "a3", "a4", "a5", "a6", "a7", "a8", "a9", "a10", "a11", "a12", "a13", "a14", "a15", "a16", "a17", "a18", "a19", "a20", "a21", "a22", "a23", "a24", "a25", "a26", "a27", "a28", "a29", "a30", "a31", "a32", "a33", "a34", "a35", "a36", "a37", "a38", "a39", "a40", "a41", "a42", "a43", "a44", "a45", "a46", "a47", "a48", "a49", "a50", "a51", "a52", "a53", "a54", "a55", "a56", "a57", "a58", "a59", "a60", "a61", "a62", "a63", "a64", "a65", "a66", "a67", "a68", "a69", "a70", "a71", "a72", "a73", "a74", "a75", "a76", "a77", "a78", "a79", "a80", "a81", "a82", "a83", "a84", "a85", "a86", "a87", "a88", "a89", "a90", "a91", "a92", "a93", "a94", "a95", "a96", "a97", "a98", "a99", "a100", "a101", "a102", "a103", "a104", "a105", "a106", "a107", "a108", "a109", "a110", "a111", "a112", "a113", "a114", "a115", "a116", "a117", "a118", "a119", "a120", "a121", "a122", "a123", "a124", "a125", "a126", "a127", "a128", "a129", "a130", "a131", "a132", "a133", "a134", "a135", "a136", "a137", "a138", "a139", "a140", "a141", "a142", "a143", "a144", "a145", "a146", "a147", "a148", "a149", "a150", "a151", "a152", "a153", "a154", "a155", "a156", "a157", "a158", "a159", "a160", "a161", "a162", "a163", "a164", "a165", "a166", "a167", "a168", "a169", "a170", "a171", "a172", "a173", "a174", "a175", "a176", "a177", "a178", "a179", "a180", "a181", "a182", "a183", "a184", "a185", "a186", "a187", "a188", "a189", "a190", "a191", "a192", "a193", "a194", "a195", "a196", "a197", "a198", "a199", "a200", "a201", "a202", "a203", "a204", "a205", "a206", "a207", "a208", "a209", "a210", "a211", "a212", "a213", "a214", "a215", "a216", "a217", "a218", "a219", "a220", "a221", "a222", "a223", "a224", "a225", "a226", "a227", "a228", "a229", "a230", "a231", "a232", "a233", "a234", "a235", "a236", "a237", "a238", "a239", "a240", "a241", "a242", "a243", "a244", "a245", "a246", "a247", "a248", "a249", "a250", "a251", "a252", "a253", "a254", "a255", "s4", "s5", "s6", "s7", "s8", "s9", "s10", "s11", "s12", "s13", "s14", "s15", "s16", "s17", "s18", "s19", "s20", "s21", "s22", "s23", "s24", "s25", "s26", "s27", "s28", "s29", "s30", "s31", "s36", "s37", "s38", "s39", "s40", "s41", "s42", "s43", "s44", "s45", "s46", "s47", "s48", "s49", "s50", "s51", "s52", "s53", "s54", "s55", "s56", "s57", "s58", "s59", "s60", "s61", "s62", "s63", "s64", "s65", "s66", "s67", "s68", "s69", "s70", "s71", "s72", "s73", "s74", "s75", "s76", "s77", "s78", "s79", "s80", "s81", "s82", "s83", "s84", "s85", "s86", "s87", "s88", "s89", "s90", "s91", "s92", "s93", "s94", "s95", "s96", "s97", "s98", "s99", "vcc", "scc", "memory");
}

extern "C" void kernel_launch(void* const* d_in, const int* in_sizes, int n_in,
                              void* d_out, int out_size, void* d_ws, size_t ws_size,
                              hipStream_t stream) {
  (void)hipFuncSetAttribute((const void*)qattn_kernel, hipFuncAttributeMaxDynamicSharedMemorySize, LDS_ATT);
  qattn_kernel<<<256, 256, LDS_ATT, stream>>>(d_in[0], d_in[1], d_in[2], d_in[3], d_ws, d_in[4], d_out);
}
```

```cpp
#include <hip/hip_runtime.h>
#include <hip/hip_bf16.h>

constexpr int LDS_ATT = 163840;

__global__ __launch_bounds__(256, 1) __attribute__((aligned(4096))) void qattn_kernel(const void* __restrict__ x, const void* __restrict__ kc, const void* __restrict__ vc,
                                                       const void* __restrict__ wq, void* __restrict__ ws, const void* __restrict__ wo,
                                                       void* __restrict__ out) {
  const unsigned blk = blockIdx.x;
  const unsigned wid = __builtin_amdgcn_readfirstlane(threadIdx.x >> 6);
  auto ka = __builtin_amdgcn_kernarg_segment_ptr();
  asm volatile(
R"A(s_load_dwordx8 s[4:11],%0,0x0
s_load_dwordx4 s[12:15],%0,0x20
s_load_dwordx2 s[16:17],%0,0x30
v_mbcnt_lo_u32_b32 v23,-1,0
v_mbcnt_hi_u32_b32 v23,-1,v23
s_mov_b32 s60,%2
s_and_b32 s66,%1,7
s_lshr_b32 s67,%1,3
s_getpc_b64 s[20:21]
L_pc_%=:
s_and_b32 s20,s20,0xffffff80
s_and_b32 s21,s21,0xffff
s_mov_b32 s22,L_hot_%=-L_pc_%=
s_mov_b32 s23,0x20000
s_lshl_b32 s68,s67,2
s_add_u32 s68,s68,s60
s_mul_i32 s68,s68,896
v_lshlrev_b32 v67,7,v23
s_mov_b64 exec,127
buffer_load_dword v8,v67,s[20:23],s68 offen
s_mov_b64 exec,-1
s_lshr_b32 s61,s66,2
s_lshl_b32 s61,s61,3
s_lshr_b32 s68,s67,2
s_add_u32 s61,s61,s68
s_and_b32 s62,s66,1
s_lshl_b32 s62,s62,2
s_and_b32 s68,s66,2
s_add_u32 s62,s62,s68
s_lshl_b32 s62,s62,1
s_and_b32 s68,s67,3
s_add_u32 s62,s62,s68
s_lshl_b32 s83,s62,9
s_mov_b32 s84,0
s_lshl_b32 s89,s60,13
s_add_u32 s85,s89,65536
s_add_u32 s88,s89,131072
v_and_b32 v20,3,v23
v_bfe_u32 v21,v23,2,2
v_lshrrev_b32 v22,4,v23
v_lshrrev_b32 v75,3,v23
s_lshl_b32 s66,s60,6
v_add_u32 v75,s66,v75
v_mul_u32_u24 v75,1280,v75
v_and_b32 v76,7,v23
v_or_b32 v77,0,v22
v_xor_b32 v77,v77,v76
v_lshl_add_u32 v77,v77,4,v75
v_add_u32 v32,0,v77
v_add_u32 v34,18432,v77
v_add_u32 v36,40960,v77
v_add_u32 v38,59392,v77
v_or_b32 v77,4,v22
v_xor_b32 v77,v77,v76
v_lshl_add_u32 v77,v77,4,v75
v_add_u32 v33,9216,v77
v_add_u32 v35,27648,v77
v_add_u32 v37,50176,v77
v_add_u32 v39,68608,v77
s_lshl_b32 s66,s60,17
v_lshlrev_b32 v75,13,v21
v_lshl_add_u32 v75,v22,6,v75
v_lshl_add_u32 v75,v20,4,v75
v_add_u32 v75,s66,v75
v_add_u32 v40,0,v75
v_add_u32 v41,256,v75
v_add_u32 v42,32768,v75
v_add_u32 v43,33024,v75
v_add_u32 v44,65536,v75
v_add_u32 v45,65792,v75
v_add_u32 v46,98304,v75
v_add_u32 v47,98560,v75
s_mul_i32 s66,s61,307200
s_waitcnt lgkmcnt(0)
s_add_u32 s28,s4,s66
s_addc_u32 s29,s5,0
s_and_b32 s29,s29,0xffff
s_mov_b32 s30,307200
s_mov_b32 s31,0x20000
s_mov_b32 s36,s10
s_and_b32 s37,s11,0xffff
s_mov_b32 s38,5242880
s_mov_b32 s39,0x20000
s_add_u32 m0,s85,0
s_nop 0
buffer_load_dwordx4 v32,s[28:31],s84 offen lds
buffer_load_dwordx4 v33,s[28:31],s84 offen offset:1024 lds
buffer_load_dwordx4 v34,s[28:31],s84 offen offset:2048 lds
buffer_load_dwordx4 v35,s[28:31],s84 offen offset:3072 lds
s_add_u32 m0,s85,4096
s_nop 0
buffer_load_dwordx4 v36,s[28:31],s84 offen lds
buffer_load_dwordx4 v37,s[28:31],s84 offen offset:1024 lds
buffer_load_dwordx4 v38,s[28:31],s84 offen offset:2048 lds
buffer_load_dwordx4 v39,s[28:31],s84 offen offset:3072 lds
s_add_u32 s84,s84,128
s_xor_b32 s85,s85,98304
s_add_u32 m0,s88,0
s_nop 0
buffer_load_dwordx4 v40,s[36:39],s83 offen lds
s_add_u32 m0,s88,2048
s_nop 0
buffer_load_dwordx4 v42,s[36:39],s83 offen lds
s_add_u32 m0,s88,4096
s_nop 0
buffer_load_dwordx4 v44,s[36:39],s83 offen lds
s_add_u32 m0,s88,6144
s_nop 0
buffer_load_dwordx4 v46,s[36:39],s83 offen lds
s_lshl_b32 s67,s61,21
s_add_u32 s68,s67,101711872
s_add_u32 s44,s12,s68
s_addc_u32 s45,s13,0
s_and_b32 s45,s45,0xffff
s_mov_b32 s46,4096
s_mov_b32 s47,0x20000
v_mov_b32 v10,0
buffer_load_dword v9,v10,s[44:47],0 offen sc1
v_lshlrev_b32 v6,4,v23
v_lshlrev_b32 v72,12,v22
v_lshl_add_u32 v72,v21,6,v72
v_lshl_add_u32 v72,v20,4,v72
v_subrev_u32 v51,s89,v72
v_add_u32 v50,131072,v72
v_and_b32 v74,15,v23
v_lshrrev_b32 v76,1,v74
v_lshlrev_b32 v75,7,v74
s_add_u32 s66,s89,65536
v_add_u32 v75,s66,v75
v_or_b32 v77,0,v22
v_xor_b32 v77,v77,v76
v_lshl_add_u32 v48,v77,4,v75
v_or_b32 v77,4,v22
v_xor_b32 v77,v77,v76
v_lshl_add_u32 v49,v77,4,v75
s_add_u32 s86,s89,131072
s_add_u32 s87,s89,98304
s_mov_b64 exec,0
s_cmp_eq_u32 s60,0
s_cbranch_scc1 L_pd1_%=
s_cmp_eq_u32 s60,1
s_cbranch_scc1 L_pd2_%=
s_cmp_eq_u32 s60,2
s_cbranch_scc1 L_pd3_%=
s_branch L_pd4_%=
L_trt0_%=:
s_mov_b64 exec,-1
s_add_u32 m0,s88,1024
s_nop 0
buffer_load_dwordx4 v41,s[36:39],s83 offen lds
s_add_u32 m0,s88,3072
s_nop 0
buffer_load_dwordx4 v43,s[36:39],s83 offen lds
s_add_u32 m0,s88,5120
s_nop 0
buffer_load_dwordx4 v45,s[36:39],s83 offen lds
s_add_u32 m0,s88,7168
s_nop 0
buffer_load_dwordx4 v47,s[36:39],s83 offen lds
s_add_u32 s83,s83,524288
s_mov_b32 s88,s87
s_add_u32 m0,s88,0
s_nop 0
buffer_load_dwordx4 v40,s[36:39],s83 offen lds
s_add_u32 m0,s88,1024
s_nop 0
buffer_load_dwordx4 v41,s[36:39],s83 offen lds
s_add_u32 m0,s88,2048
s_nop 0
buffer_load_dwordx4 v42,s[36:39],s83 offen lds
s_add_u32 m0,s88,3072
s_nop 0
buffer_load_dwordx4 v43,s[36:39],s83 offen lds
s_add_u32 m0,s88,4096
s_nop 0
buffer_load_dwordx4 v44,s[36:39],s83 offen lds
s_add_u32 m0,s88,5120
s_nop 0
buffer_load_dwordx4 v45,s[36:39],s83 offen lds
s_add_u32 m0,s88,6144
s_nop 0
buffer_load_dwordx4 v46,s[36:39],s83 offen lds
s_add_u32 m0,s88,7168
s_nop 0
buffer_load_dwordx4 v47,s[36:39],s83 offen lds
s_add_u32 s83,s83,524288
s_add_u32 s88,s89,0
s_mov_b64 exec,0
s_cmp_eq_u32 s60,0
s_cbranch_scc1 L_pd9_%=
s_cmp_eq_u32 s60,1
s_cbranch_scc1 L_pd10_%=
s_cmp_eq_u32 s60,2
s_cbranch_scc1 L_pd11_%=
s_branch L_pd12_%=
L_t1b_%=:
s_cmp_ge_u32 s62,8
s_cbranch_scc1 L_pd19_%=
s_branch L_pd14_%=
L_t1c_%=:
s_cmp_ge_u32 s62,8
s_cbranch_scc1 L_pd20_%=
s_branch L_pd15_%=
s_nop 0
s_nop 0
s_nop 0
s_nop 0
s_nop 0
s_nop 0
L_trt1_%=:
s_mov_b64 exec,-1
s_branch L_pacx_%=
L_pace_%=:
s_cmp_ge_u32 s4,8
s_cselect_b32 s5,3,1
s_sub_u32 s5,s4,s5
s_max_i32 s5,s5,s7
L_pacl_%=:
s_sub_u32 s4,s4,1
s_cmp_eq_u32 s4,0
s_cbranch_scc1 L_pace2_%=
s_nop 2
s_branch L_pacl_%=
L_pace2_%=:
s_setpc_b64 s[92:93]
L_pacx_%=:
s_waitcnt vmcnt(17)
ds_read_b128 a[192:195],v48
ds_read_b128 a[196:199],v49
ds_read_b128 a[200:203],v48 offset:2048
ds_read_b128 a[204:207],v49 offset:2048
ds_read_b128 a[208:211],v48 offset:4096
ds_read_b128 a[212:215],v49 offset:4096
ds_read_b128 a[216:219],v48 offset:6144
ds_read_b128 a[220:223],v49 offset:6144
s_waitcnt vmcnt(13)
s_barrier
ds_read_b64_tr_b16 a[64:65],v50
ds_read_b64_tr_b16 a[66:67],v50 offset:2048
ds_read_b64_tr_b16 a[68:69],v50 offset:16384
ds_read_b64_tr_b16 a[70:71],v50 offset:18432
s_waitcnt lgkmcnt(2)
v_mfma_f32_16x16x32_bf16 a[0:3],a[64:67],a[192:195],0
s_add_u32 m0,s85,0
ds_read_b64_tr_b16 a[72:73],v50 offset:8
v_xor_b32 v48,98304,v48
s_waitcnt lgkmcnt(1)
v_mfma_f32_16x16x32_bf16 a[0:3],a[68:71],a[196:199],a[0:3]
buffer_load_dwordx4 v32,s[28:31],s84 offen lds
v_mfma_f32_16x16x32_bf16 a[16:19],a[68:71],a[204:207],0
s_mov_b32 s69,s86
ds_read_b64_tr_b16 a[74:75],v50 offset:2056
v_xor_b32 v49,98304,v49
v_mfma_f32_16x16x32_bf16 a[16:19],a[64:67],a[200:203],a[16:19]
s_mov_b32 s86,s87
s_mov_b32 s87,s88
ds_read_b64_tr_b16 a[76:77],v50 offset:16392
v_mfma_f32_16x16x32_bf16 a[32:35],a[64:67],a[208:211],0
ds_read_b64_tr_b16 a[78:79],v50 offset:18440
v_mfma_f32_16x16x32_bf16 a[32:35],a[68:71],a[212:215],a[32:35]
ds_read_b64_tr_b16 a[80:81],v50 offset:256
v_mfma_f32_16x16x32_bf16 a[48:51],a[68:71],a[220:223],0
buffer_load_dwordx4 v33,s[28:31],s84 offen offset:1024 lds
v_mfma_f32_16x16x32_bf16 a[48:51],a[64:67],a[216:219],a[48:51]
s_waitcnt vmcnt(14) lgkmcnt(1)
ds_read_b64_tr_b16 a[82:83],v50 offset:2304
v_mfma_f32_16x16x32_bf16 a[4:7],a[72:75],a[192:195],0
s_lshl_b32 s65,s61,5
ds_read_b64_tr_b16 a[84:85],v50 offset:16640
v_cmp_lt_u32 vcc,6,v23
v_mfma_f32_16x16x32_bf16 a[4:7],a[76:79],a[196:199],a[4:7]
s_lshl_b32 s66,s62,1
ds_read_b64_tr_b16 a[86:87],v50 offset:18688
v_mov_b32 v17,0
v_mfma_f32_16x16x32_bf16 a[20:23],a[76:79],a[204:207],0
s_add_u32 s65,s65,s66
s_lshl_b32 s66,s65,18
v_readfirstlane_b32 s52,v9
v_mfma_f32_16x16x32_bf16 a[20:23],a[72:75],a[200:203],a[20:23]
buffer_load_dwordx4 v34,s[28:31],s84 offen offset:2048 lds
v_mfma_f32_16x16x32_bf16 a[36:39],a[72:75],a[208:211],0
s_add_u32 s20,s6,s66
s_addc_u32 s21,s7,0
ds_read_b64_tr_b16 a[88:89],v50 offset:264
v_mfma_f32_16x16x32_bf16 a[36:39],a[76:79],a[212:215],a[36:39]
s_and_b32 s21,s21,0xffff
s_mov_b32 s22,524288
ds_read_b64_tr_b16 a[90:91],v50 offset:2312
v_mfma_f32_16x16x32_bf16 a[52:55],a[76:79],a[220:223],0
s_mov_b32 s23,0x20000
s_mov_b32 s26,524288
ds_read_b64_tr_b16 a[92:93],v50 offset:16648
v_mfma_f32_16x16x32_bf16 a[52:55],a[72:75],a[216:219],a[52:55]
s_add_u32 s24,s8,s66
s_addc_u32 s25,s9,0
ds_read_b64_tr_b16 a[94:95],v50 offset:18696
s_waitcnt lgkmcnt(6)
v_mfma_f32_16x16x32_bf16 a[8:11],a[80:83],a[192:195],0
buffer_load_dwordx4 v35,s[28:31],s84 offen offset:3072 lds
s_waitcnt lgkmcnt(4)
v_mfma_f32_16x16x32_bf16 a[8:11],a[84:87],a[196:199],a[8:11]
s_and_b32 s25,s25,0xffff
s_mov_b32 s27,0x20000
ds_read_b64_tr_b16 a[96:97],v50 offset:512
v_mfma_f32_16x16x32_bf16 a[24:27],a[84:87],a[204:207],0
s_lshl_b32 s66,s65,16
s_mov_b32 s42,131072
ds_read_b64_tr_b16 a[98:99],v50 offset:2560
v_mfma_f32_16x16x32_bf16 a[24:27],a[80:83],a[200:203],a[24:27]
s_add_u32 m0,s85,4096
s_xor_b32 s85,s85,98304
ds_read_b64_tr_b16 a[100:101],v50 offset:16896
v_mfma_f32_16x16x32_bf16 a[40:43],a[80:83],a[208:211],0
s_add_u32 s40,s12,s66
s_addc_u32 s41,s13,0
s_and_b32 s41,s41,0xffff
v_mfma_f32_16x16x32_bf16 a[40:43],a[84:87],a[212:215],a[40:43]
buffer_load_dwordx4 v36,s[28:31],s84 offen lds
v_mfma_f32_16x16x32_bf16 a[56:59],a[84:87],a[220:223],0
s_mov_b32 s43,0x20000
s_mov_b32 s48,s14
ds_read_b64_tr_b16 a[102:103],v50 offset:18944
v_mfma_f32_16x16x32_bf16 a[56:59],a[80:83],a[216:219],a[56:59]
s_and_b32 s49,s15,0xffff
s_mov_b32 s50,5242880
ds_read_b64_tr_b16 a[104:105],v50 offset:520
s_waitcnt lgkmcnt(5)
v_mfma_f32_16x16x32_bf16 a[12:15],a[88:91],a[192:195],0
s_mov_b32 s51,0x20000
s_lshl_b32 s67,s60,12
ds_read_b64_tr_b16 a[106:107],v50 offset:2568
v_mfma_f32_16x16x32_bf16 a[12:15],a[92:95],a[196:199],a[12:15]
s_add_u32 s76,s67,98304
s_mul_i32 s74,s61,5
ds_read_b64_tr_b16 a[108:109],v50 offset:16904
v_mfma_f32_16x16x32_bf16 a[28:31],a[92:95],a[204:207],0
buffer_load_dwordx4 v37,s[28:31],s84 offen offset:1024 lds
v_mfma_f32_16x16x32_bf16 a[28:31],a[88:91],a[200:203],a[28:31]
s_add_u32 s74,s74,s62
s_and_b32 s74,s74,15
ds_read_b64_tr_b16 a[110:111],v50 offset:18952
v_mfma_f32_16x16x32_bf16 a[44:47],a[88:91],a[208:211],0
s_lshl_b32 s74,s74,14
s_mov_b32 s75,0
ds_read_b64_tr_b16 a[112:113],v50 offset:768
v_mfma_f32_16x16x32_bf16 a[44:47],a[92:95],a[212:215],a[44:47]
s_mov_b32 s77,98304
s_mov_b32 s71,0x3e0293ee
ds_read_b64_tr_b16 a[114:115],v50 offset:2816
v_mfma_f32_16x16x32_bf16 a[60:63],a[92:95],a[220:223],0
s_mov_b32 s73,0x53800000
s_lshl_b32 s79,s60,14
s_add_u32 s80,s79,4096
v_mfma_f32_16x16x32_bf16 a[60:63],a[88:91],a[216:219],a[60:63]
buffer_load_dwordx4 v38,s[28:31],s84 offen offset:2048 lds
s_waitcnt lgkmcnt(5)
v_mfma_f32_16x16x32_bf16 v[64:67],a[96:99],a[192:195],0
s_add_u32 s81,s79,8192
s_add_u32 s82,s79,12288
ds_read_b64_tr_b16 a[116:117],v50 offset:17152
v_mfma_f32_16x16x32_bf16 v[64:67],a[100:103],a[196:199],v[64:67]
s_lshr_b32 s63,s62,2
s_and_b32 s64,s62,3
ds_read_b64_tr_b16 a[118:119],v50 offset:19200
v_mfma_f32_16x16x32_bf16 v[80:83],a[100:103],a[204:207],0
s_mov_b32 s72,0xff800000
s_lshl_b32 s18,s60,24
ds_read_b64_tr_b16 a[120:121],v50 offset:776
v_mfma_f32_16x16x32_bf16 v[80:83],a[96:99],a[200:203],v[80:83]
s_lshl_b32 s68,s62,6
s_add_u32 s18,s18,s68
ds_read_b64_tr_b16 a[122:123],v50 offset:2824
v_mfma_f32_16x16x32_bf16 v[96:99],a[96:99],a[208:211],0
buffer_load_dwordx4 v39,s[28:31],s84 offen offset:3072 lds
v_mfma_f32_16x16x32_bf16 v[96:99],a[100:103],a[212:215],v[96:99]
s_add_u32 s84,s84,128
ds_read_b64_tr_b16 a[124:125],v50 offset:17160
v_lshlrev_b32 v19,3,v6
v_mfma_f32_16x16x32_bf16 v[112:115],a[100:103],a[220:223],0
s_cmp_eq_u32 s60,3
s_cselect_b32 s72,0x7149f2ca,s72
ds_read_b64_tr_b16 a[126:127],v50 offset:19208
v_mfma_f32_16x16x32_bf16 v[112:115],a[96:99],a[216:219],v[112:115]
s_add_u32 m0,s88,0
v_cndmask_b32_e64 v19,v19,-1,vcc
v_lshrrev_b32 v18,1,v19
s_waitcnt lgkmcnt(6)
v_mfma_f32_16x16x32_bf16 v[68:71],a[104:107],a[192:195],0
s_lshl_b32 s90,%1,2
s_add_u32 s90,s90,s60
v_min_u32 v19,v19,v19
v_mfma_f32_16x16x32_bf16 v[68:71],a[108:111],a[196:199],v[68:71]
buffer_load_dwordx4 v40,s[36:39],s83 offen lds
v_mfma_f32_16x16x32_bf16 v[84:87],a[108:111],a[204:207],0
s_mul_i32 s90,s90,5120
s_mul_i32 s58,s61,307200
v_cndmask_b32_e64 v19,v19,v18,vcc
v_mfma_f32_16x16x32_bf16 v[84:87],a[104:107],a[200:203],v[84:87]
s_add_u32 s56,s40,52224
s_addc_u32 s57,s41,0
s_add_u32 s52,s52,1
v_mfma_f32_16x16x32_bf16 v[100:103],a[104:107],a[208:211],0
)A"
R"A(s_add_u32 m0,s88,1024
s_add_u32 s58,s16,s58
s_addc_u32 s59,s17,0
v_mfma_f32_16x16x32_bf16 v[100:103],a[108:111],a[212:215],v[100:103]
s_cmp_eq_u32 s60,1
s_cselect_b64 s[56:57],s[58:59],s[56:57]
s_xor_b32 s53,s52,0x9e3779b9
v_mfma_f32_16x16x32_bf16 v[116:119],a[108:111],a[220:223],0
buffer_load_dwordx4 v41,s[36:39],s83 offen lds
v_mfma_f32_16x16x32_bf16 v[116:119],a[104:107],a[216:219],v[116:119]
s_cmp_eq_u32 s60,2
s_cselect_b64 s[56:57],s[20:21],s[56:57]
v_lshlrev_b32 v11,8,v21
s_waitcnt vmcnt(18)
s_barrier
v_mfma_f32_16x16x32_bf16 v[72:75],a[112:115],a[192:195],0
s_add_u32 m0,s88,2048
ds_read_b64_tr_b16 a[64:65],v50 offset:1024
v_lshl_add_u32 v11,v22,6,v11
s_waitcnt lgkmcnt(1)
v_mfma_f32_16x16x32_bf16 v[72:75],a[116:119],a[196:199],v[72:75]
s_cmp_eq_u32 s60,3
s_cselect_b64 s[56:57],s[24:25],s[56:57]
ds_read_b64_tr_b16 a[66:67],v50 offset:3072
v_mfma_f32_16x16x32_bf16 v[88:91],a[116:119],a[204:207],0
buffer_load_dwordx4 v42,s[36:39],s83 offen lds
v_mfma_f32_16x16x32_bf16 v[88:91],a[112:115],a[200:203],v[88:91]
ds_read_b64_tr_b16 a[68:69],v50 offset:17408
v_add_u32 v11,s67,v11
v_mfma_f32_16x16x32_bf16 v[104:107],a[112:115],a[208:211],0
ds_read_b64_tr_b16 a[70:71],v50 offset:19456
v_lshl_add_u32 v4,v20,4,v11
v_mfma_f32_16x16x32_bf16 v[104:107],a[116:119],a[212:215],v[104:107]
s_add_u32 m0,s88,3072
ds_read_b64_tr_b16 a[72:73],v50 offset:1032
v_xor_b32 v12,2,v20
v_mfma_f32_16x16x32_bf16 v[120:123],a[116:119],a[220:223],0
ds_read_b64_tr_b16 a[74:75],v50 offset:3080
v_lshl_add_u32 v5,v12,4,v11
v_mfma_f32_16x16x32_bf16 v[120:123],a[112:115],a[216:219],v[120:123]
buffer_load_dwordx4 v43,s[36:39],s83 offen lds
v_mfma_f32_16x16x32_bf16 v[76:79],a[120:123],a[192:195],0
ds_read_b64_tr_b16 a[76:77],v50 offset:17416
v_lshlrev_b32 v11,10,v21
v_mfma_f32_16x16x32_bf16 v[76:79],a[124:127],a[196:199],v[76:79]
ds_read_b64_tr_b16 a[78:79],v50 offset:19464
v_lshl_add_u32 v11,v20,6,v11
v_mfma_f32_16x16x32_bf16 v[92:95],a[124:127],a[204:207],0
s_add_u32 m0,s88,4096
ds_read_b64_tr_b16 a[80:81],v50 offset:1280
v_and_b32 v12,2,v21
v_mfma_f32_16x16x32_bf16 v[92:95],a[120:123],a[200:203],v[92:95]
v_xor_b32 v12,v12,v22
v_lshl_add_u32 v11,v12,4,v11
v_mfma_f32_16x16x32_bf16 v[108:111],a[120:123],a[208:211],0
buffer_load_dwordx4 v44,s[36:39],s83 offen lds
v_mfma_f32_16x16x32_bf16 v[108:111],a[124:127],a[212:215],v[108:111]
ds_read_b64_tr_b16 a[82:83],v50 offset:3328
v_add_u32 v1,98304,v11
v_mfma_f32_16x16x32_bf16 v[124:127],a[124:127],a[220:223],0
s_waitcnt lgkmcnt(4)
ds_read_b64_tr_b16 a[84:85],v50 offset:17664
v_lshlrev_b32 v11,10,v22
v_mfma_f32_16x16x32_bf16 v[124:127],a[120:123],a[216:219],v[124:127]
s_add_u32 m0,s88,5120
ds_read_b64_tr_b16 a[86:87],v50 offset:19712
v_lshl_add_u32 v11,v21,6,v11
v_mfma_f32_16x16x32_bf16 v[128:131],a[64:67],a[192:195],0
ds_read_b64_tr_b16 a[88:89],v50 offset:1288
v_lshl_add_u32 v11,v20,3,v11
v_mfma_f32_16x16x32_bf16 v[128:131],a[68:71],a[196:199],v[128:131]
buffer_load_dwordx4 v45,s[36:39],s83 offen lds
v_mfma_f32_16x16x32_bf16 v[160:163],a[68:71],a[204:207],0
ds_read_b64_tr_b16 a[90:91],v50 offset:3336
v_add_u32 v11,114688,v11
v_mfma_f32_16x16x32_bf16 v[160:163],a[64:67],a[200:203],v[160:163]
ds_read_b64_tr_b16 a[92:93],v50 offset:17672
v_and_b32 v12,1,v22
v_mfma_f32_16x16x32_bf16 v[192:195],a[64:67],a[208:211],0
s_add_u32 m0,s88,6144
ds_read_b64_tr_b16 a[94:95],v50 offset:19720
v_lshl_add_u32 v2,v12,5,v11
v_mfma_f32_16x16x32_bf16 v[192:195],a[68:71],a[212:215],v[192:195]
v_xor_b32 v12,1,v12
v_lshl_add_u32 v3,v12,5,v11
v_mfma_f32_16x16x32_bf16 v[224:227],a[68:71],a[220:223],0
buffer_load_dwordx4 v46,s[36:39],s83 offen lds
v_mfma_f32_16x16x32_bf16 v[224:227],a[64:67],a[216:219],v[224:227]
ds_read_b64_tr_b16 a[96:97],v50 offset:1536
v_mov_b32 v28,0x3f803f80
v_mfma_f32_16x16x32_bf16 v[132:135],a[72:75],a[192:195],0
s_waitcnt lgkmcnt(4)
ds_read_b64_tr_b16 a[98:99],v50 offset:3584
v_mov_b32 v29,0x3f803f80
v_mfma_f32_16x16x32_bf16 v[132:135],a[76:79],a[196:199],v[132:135]
s_add_u32 m0,s88,7168
s_mov_b32 s88,s69
ds_read_b64_tr_b16 a[100:101],v50 offset:17920
v_mfma_f32_16x16x32_bf16 v[164:167],a[76:79],a[204:207],0
ds_read_b64_tr_b16 a[102:103],v50 offset:19968
v_mov_b32 v30,0x3f803f80
v_mfma_f32_16x16x32_bf16 v[164:167],a[72:75],a[200:203],v[164:167]
buffer_load_dwordx4 v47,s[36:39],s83 offen lds
v_mfma_f32_16x16x32_bf16 v[196:199],a[72:75],a[208:211],0
s_add_u32 s83,s83,524288
ds_read_b64_tr_b16 a[104:105],v50 offset:1544
v_mov_b32 v31,0x3f803f80
v_mfma_f32_16x16x32_bf16 v[196:199],a[76:79],a[212:215],v[196:199]
ds_read_b64_tr_b16 a[106:107],v50 offset:3592
v_mfma_f32_16x16x32_bf16 v[228:231],a[76:79],a[220:223],0
s_add_u32 m0,s85,0
ds_read_b64_tr_b16 a[108:109],v50 offset:17928
v_mfma_f32_16x16x32_bf16 v[228:231],a[72:75],a[216:219],v[228:231]
v_mfma_f32_16x16x32_bf16 v[136:139],a[80:83],a[192:195],0
buffer_load_dwordx4 v32,s[28:31],s84 offen lds
v_mfma_f32_16x16x32_bf16 v[136:139],a[84:87],a[196:199],v[136:139]
ds_read_b64_tr_b16 a[110:111],v50 offset:19976
v_mfma_f32_16x16x32_bf16 v[168:171],a[84:87],a[204:207],0
ds_read_b64_tr_b16 a[112:113],v50 offset:1792
v_mfma_f32_16x16x32_bf16 v[168:171],a[80:83],a[200:203],v[168:171]
ds_read_b64_tr_b16 a[114:115],v50 offset:3840
v_mfma_f32_16x16x32_bf16 v[200:203],a[80:83],a[208:211],0
ds_read_b64_tr_b16 a[116:117],v50 offset:18176
v_mfma_f32_16x16x32_bf16 v[200:203],a[84:87],a[212:215],v[200:203]
buffer_load_dwordx4 v33,s[28:31],s84 offen offset:1024 lds
v_mfma_f32_16x16x32_bf16 v[232:235],a[84:87],a[220:223],0
ds_read_b64_tr_b16 a[118:119],v50 offset:20224
v_mfma_f32_16x16x32_bf16 v[232:235],a[80:83],a[216:219],v[232:235]
s_waitcnt lgkmcnt(5)
ds_read_b64_tr_b16 a[120:121],v50 offset:1800
v_mfma_f32_16x16x32_bf16 v[140:143],a[88:91],a[192:195],0
ds_read_b64_tr_b16 a[122:123],v50 offset:3848
v_mfma_f32_16x16x32_bf16 v[140:143],a[92:95],a[196:199],v[140:143]
v_mfma_f32_16x16x32_bf16 v[172:175],a[92:95],a[204:207],0
buffer_load_dwordx4 v34,s[28:31],s84 offen offset:2048 lds
v_mfma_f32_16x16x32_bf16 v[172:175],a[88:91],a[200:203],v[172:175]
ds_read_b64_tr_b16 a[124:125],v50 offset:18184
v_mfma_f32_16x16x32_bf16 v[204:207],a[88:91],a[208:211],0
ds_read_b64_tr_b16 a[126:127],v50 offset:20232
v_add_u32 v50,s86,v51
v_mfma_f32_16x16x32_bf16 v[204:207],a[92:95],a[212:215],v[204:207]
v_mfma_f32_16x16x32_bf16 v[236:239],a[92:95],a[220:223],0
v_mfma_f32_16x16x32_bf16 v[236:239],a[88:91],a[216:219],v[236:239]
buffer_load_dwordx4 v35,s[28:31],s84 offen offset:3072 lds
s_waitcnt vmcnt(20) lgkmcnt(0)
s_barrier
v_mfma_f32_16x16x32_bf16 v[144:147],a[96:99],a[192:195],0
s_waitcnt vmcnt(12)
ds_read_b128 a[224:227],v48
v_mfma_f32_16x16x32_bf16 v[144:147],a[100:103],a[196:199],v[144:147]
s_add_u32 m0,s85,4096
s_xor_b32 s85,s85,98304
ds_read_b128 a[228:231],v49
v_mfma_f32_16x16x32_bf16 v[176:179],a[100:103],a[204:207],0
ds_read_b128 a[232:235],v48 offset:2048
v_mfma_f32_16x16x32_bf16 v[176:179],a[96:99],a[200:203],v[176:179]
buffer_load_dwordx4 v36,s[28:31],s84 offen lds
v_mfma_f32_16x16x32_bf16 v[208:211],a[96:99],a[208:211],0
ds_read_b128 a[236:239],v49 offset:2048
v_mfma_f32_16x16x32_bf16 v[208:211],a[100:103],a[212:215],v[208:211]
ds_read_b128 a[240:243],v48 offset:4096
v_mfma_f32_16x16x32_bf16 v[240:243],a[100:103],a[220:223],0
ds_read_b128 a[244:247],v49 offset:4096
v_mfma_f32_16x16x32_bf16 v[240:243],a[96:99],a[216:219],v[240:243]
ds_read_b128 a[248:251],v48 offset:6144
v_mfma_f32_16x16x32_bf16 v[148:151],a[104:107],a[192:195],0
buffer_load_dwordx4 v37,s[28:31],s84 offen offset:1024 lds
v_mfma_f32_16x16x32_bf16 v[148:151],a[108:111],a[196:199],v[148:151]
ds_read_b128 a[252:255],v49 offset:6144
v_mfma_f32_16x16x32_bf16 v[180:183],a[108:111],a[204:207],0
ds_read_b64_tr_b16 a[64:65],v50
v_mfma_f32_16x16x32_bf16 v[180:183],a[104:107],a[200:203],v[180:183]
ds_read_b64_tr_b16 a[66:67],v50 offset:2048
v_mfma_f32_16x16x32_bf16 v[212:215],a[104:107],a[208:211],0
v_mfma_f32_16x16x32_bf16 v[212:215],a[108:111],a[212:215],v[212:215]
buffer_load_dwordx4 v38,s[28:31],s84 offen offset:2048 lds
v_mfma_f32_16x16x32_bf16 v[244:247],a[108:111],a[220:223],0
ds_read_b64_tr_b16 a[68:69],v50 offset:16384
v_mfma_f32_16x16x32_bf16 v[244:247],a[104:107],a[216:219],v[244:247]
ds_read_b64_tr_b16 a[70:71],v50 offset:18432
v_mfma_f32_16x16x32_bf16 v[152:155],a[112:115],a[192:195],0
ds_read_b64_tr_b16 a[72:73],v50 offset:8
v_mfma_f32_16x16x32_bf16 v[152:155],a[116:119],a[196:199],v[152:155]
ds_read_b64_tr_b16 a[74:75],v50 offset:2056
v_mfma_f32_16x16x32_bf16 v[184:187],a[116:119],a[204:207],0
buffer_load_dwordx4 v39,s[28:31],s84 offen offset:3072 lds
v_mfma_f32_16x16x32_bf16 v[184:187],a[112:115],a[200:203],v[184:187]
s_add_u32 s84,s84,128
ds_read_b64_tr_b16 a[76:77],v50 offset:16392
v_mfma_f32_16x16x32_bf16 v[216:219],a[112:115],a[208:211],0
ds_read_b64_tr_b16 a[78:79],v50 offset:18440
v_mfma_f32_16x16x32_bf16 v[216:219],a[116:119],a[212:215],v[216:219]
ds_read_b64_tr_b16 a[80:81],v50 offset:256
v_mfma_f32_16x16x32_bf16 v[248:251],a[116:119],a[220:223],0
v_mfma_f32_16x16x32_bf16 v[248:251],a[112:115],a[216:219],v[248:251]
ds_read_b64_tr_b16 a[82:83],v50 offset:2304
v_mfma_f32_16x16x32_bf16 v[156:159],a[120:123],a[192:195],0
global_load_dword a184,v17,s[56:57] nt
ds_read_b64_tr_b16 a[84:85],v50 offset:16640
v_mfma_f32_16x16x32_bf16 v[156:159],a[124:127],a[196:199],v[156:159]
ds_read_b64_tr_b16 a[86:87],v50 offset:18688
v_mfma_f32_16x16x32_bf16 v[188:191],a[124:127],a[204:207],0
v_mfma_f32_16x16x32_bf16 v[188:191],a[120:123],a[200:203],v[188:191]
ds_read_b64_tr_b16 a[88:89],v50 offset:264
v_mfma_f32_16x16x32_bf16 v[220:223],a[120:123],a[208:211],0
ds_read_b64_tr_b16 a[90:91],v50 offset:2312
v_mfma_f32_16x16x32_bf16 v[220:223],a[124:127],a[212:215],v[220:223]
ds_read_b64_tr_b16 a[92:93],v50 offset:16648
v_mfma_f32_16x16x32_bf16 v[252:255],a[124:127],a[220:223],0
v_mfma_f32_16x16x32_bf16 v[252:255],a[120:123],a[216:219],v[252:255]
ds_read_b64_tr_b16 a[94:95],v50 offset:18696
s_mov_b32 s70,3
s_memrealtime s[8:9]
s_memtime s[4:5]
s_nop 0
L_qp_%=:
s_waitcnt lgkmcnt(6)
v_mfma_f32_16x16x32_bf16 a[0:3],a[64:67],a[224:227],a[0:3]
s_add_u32 m0,s88,0
ds_read_b64_tr_b16 a[96:97],v50 offset:512
v_xor_b32 v48,98304,v48
L_pd1_%=:
s_cbranch_execz L_pd5_%=
v_mfma_f32_16x16x32_bf16 a[0:3],a[68:71],a[228:231],a[0:3]
buffer_load_dwordx4 v40,s[36:39],s83 offen lds
v_mfma_f32_16x16x32_bf16 a[16:19],a[68:71],a[236:239],a[16:19]
s_mov_b32 s69,s86
ds_read_b64_tr_b16 a[98:99],v50 offset:2560
v_xor_b32 v49,98304,v49
v_mfma_f32_16x16x32_bf16 a[16:19],a[64:67],a[232:235],a[16:19]
s_mov_b32 s86,s87
s_mov_b32 s87,s88
ds_read_b64_tr_b16 a[100:101],v50 offset:16896
v_mfma_f32_16x16x32_bf16 a[32:35],a[64:67],a[240:243],a[32:35]
ds_read_b64_tr_b16 a[102:103],v50 offset:18944
v_mfma_f32_16x16x32_bf16 a[32:35],a[68:71],a[244:247],a[32:35]
ds_read_b64_tr_b16 a[104:105],v50 offset:520
v_mfma_f32_16x16x32_bf16 a[48:51],a[68:71],a[252:255],a[48:51]
s_add_u32 m0,s88,1024
v_mfma_f32_16x16x32_bf16 a[48:51],a[64:67],a[248:251],a[48:51]
ds_read_b64_tr_b16 a[106:107],v50 offset:2568
v_mfma_f32_16x16x32_bf16 a[4:7],a[72:75],a[224:227],a[4:7]
buffer_load_dwordx4 v41,s[36:39],s83 offen lds
v_mfma_f32_16x16x32_bf16 a[4:7],a[76:79],a[228:231],a[4:7]
ds_read_b64_tr_b16 a[108:109],v50 offset:16904
v_mfma_f32_16x16x32_bf16 a[20:23],a[76:79],a[236:239],a[20:23]
ds_read_b64_tr_b16 a[110:111],v50 offset:18952
v_mfma_f32_16x16x32_bf16 a[20:23],a[72:75],a[232:235],a[20:23]
ds_read_b64_tr_b16 a[112:113],v50 offset:768
v_mfma_f32_16x16x32_bf16 a[36:39],a[72:75],a[240:243],a[36:39]
ds_read_b64_tr_b16 a[114:115],v50 offset:2816
v_mfma_f32_16x16x32_bf16 a[36:39],a[76:79],a[244:247],a[36:39]
s_add_u32 m0,s88,2048
v_mfma_f32_16x16x32_bf16 a[52:55],a[76:79],a[252:255],a[52:55]
ds_read_b64_tr_b16 a[116:117],v50 offset:17152
v_mfma_f32_16x16x32_bf16 a[52:55],a[72:75],a[248:251],a[52:55]
buffer_load_dwordx4 v42,s[36:39],s83 offen lds
v_mfma_f32_16x16x32_bf16 a[8:11],a[80:83],a[224:227],a[8:11]
s_waitcnt lgkmcnt(5)
ds_read_b64_tr_b16 a[118:119],v50 offset:19200
v_mfma_f32_16x16x32_bf16 a[8:11],a[84:87],a[228:231],a[8:11]
ds_read_b64_tr_b16 a[120:121],v50 offset:776
)A"
R"A(v_mfma_f32_16x16x32_bf16 a[24:27],a[84:87],a[236:239],a[24:27]
ds_read_b64_tr_b16 a[122:123],v50 offset:2824
v_mfma_f32_16x16x32_bf16 a[24:27],a[80:83],a[232:235],a[24:27]
ds_read_b64_tr_b16 a[124:125],v50 offset:17160
v_mfma_f32_16x16x32_bf16 a[40:43],a[80:83],a[240:243],a[40:43]
s_add_u32 m0,s88,3072
v_mfma_f32_16x16x32_bf16 a[40:43],a[84:87],a[244:247],a[40:43]
ds_read_b64_tr_b16 a[126:127],v50 offset:19208
v_mfma_f32_16x16x32_bf16 a[56:59],a[84:87],a[252:255],a[56:59]
buffer_load_dwordx4 v43,s[36:39],s83 offen lds
v_mfma_f32_16x16x32_bf16 a[56:59],a[80:83],a[248:251],a[56:59]
ds_read_b64_tr_b16 a[64:65],v50 offset:1024
v_mfma_f32_16x16x32_bf16 a[12:15],a[88:91],a[224:227],a[12:15]
ds_read_b64_tr_b16 a[66:67],v50 offset:3072
v_mfma_f32_16x16x32_bf16 a[12:15],a[92:95],a[228:231],a[12:15]
ds_read_b64_tr_b16 a[68:69],v50 offset:17408
v_mfma_f32_16x16x32_bf16 a[28:31],a[92:95],a[236:239],a[28:31]
ds_read_b64_tr_b16 a[70:71],v50 offset:19456
v_mfma_f32_16x16x32_bf16 a[28:31],a[88:91],a[232:235],a[28:31]
s_add_u32 m0,s88,4096
v_mfma_f32_16x16x32_bf16 a[44:47],a[88:91],a[240:243],a[44:47]
ds_read_b64_tr_b16 a[72:73],v50 offset:1032
v_mfma_f32_16x16x32_bf16 a[44:47],a[92:95],a[244:247],a[44:47]
buffer_load_dwordx4 v44,s[36:39],s83 offen lds
v_mfma_f32_16x16x32_bf16 a[60:63],a[92:95],a[252:255],a[60:63]
ds_read_b64_tr_b16 a[74:75],v50 offset:3080
v_mfma_f32_16x16x32_bf16 a[60:63],a[88:91],a[248:251],a[60:63]
ds_read_b64_tr_b16 a[76:77],v50 offset:17416
v_mfma_f32_16x16x32_bf16 v[64:67],a[96:99],a[224:227],v[64:67]
ds_read_b64_tr_b16 a[78:79],v50 offset:19464
v_mfma_f32_16x16x32_bf16 v[64:67],a[100:103],a[228:231],v[64:67]
ds_read_b64_tr_b16 a[80:81],v50 offset:1280
v_mfma_f32_16x16x32_bf16 v[80:83],a[100:103],a[236:239],v[80:83]
s_add_u32 m0,s88,5120
v_mfma_f32_16x16x32_bf16 v[80:83],a[96:99],a[232:235],v[80:83]
ds_read_b64_tr_b16 a[82:83],v50 offset:3328
v_mfma_f32_16x16x32_bf16 v[96:99],a[96:99],a[240:243],v[96:99]
buffer_load_dwordx4 v45,s[36:39],s83 offen lds
v_mfma_f32_16x16x32_bf16 v[96:99],a[100:103],a[244:247],v[96:99]
ds_read_b64_tr_b16 a[84:85],v50 offset:17664
v_mfma_f32_16x16x32_bf16 v[112:115],a[100:103],a[252:255],v[112:115]
ds_read_b64_tr_b16 a[86:87],v50 offset:19712
v_mfma_f32_16x16x32_bf16 v[112:115],a[96:99],a[248:251],v[112:115]
ds_read_b64_tr_b16 a[88:89],v50 offset:1288
v_mfma_f32_16x16x32_bf16 v[68:71],a[104:107],a[224:227],v[68:71]
s_waitcnt lgkmcnt(5)
ds_read_b64_tr_b16 a[90:91],v50 offset:3336
v_mfma_f32_16x16x32_bf16 v[68:71],a[108:111],a[228:231],v[68:71]
s_add_u32 m0,s88,6144
v_mfma_f32_16x16x32_bf16 v[84:87],a[108:111],a[236:239],v[84:87]
ds_read_b64_tr_b16 a[92:93],v50 offset:17672
v_mfma_f32_16x16x32_bf16 v[84:87],a[104:107],a[232:235],v[84:87]
buffer_load_dwordx4 v46,s[36:39],s83 offen lds
v_mfma_f32_16x16x32_bf16 v[100:103],a[104:107],a[240:243],v[100:103]
ds_read_b64_tr_b16 a[94:95],v50 offset:19720
v_mfma_f32_16x16x32_bf16 v[100:103],a[108:111],a[244:247],v[100:103]
ds_read_b64_tr_b16 a[96:97],v50 offset:1536
v_mfma_f32_16x16x32_bf16 v[116:119],a[108:111],a[252:255],v[116:119]
ds_read_b64_tr_b16 a[98:99],v50 offset:3584
v_mfma_f32_16x16x32_bf16 v[116:119],a[104:107],a[248:251],v[116:119]
ds_read_b64_tr_b16 a[100:101],v50 offset:17920
v_mfma_f32_16x16x32_bf16 v[72:75],a[112:115],a[224:227],v[72:75]
s_add_u32 m0,s88,7168
s_mov_b32 s88,s69
s_mov_b32 s69,s86
v_mfma_f32_16x16x32_bf16 v[72:75],a[116:119],a[228:231],v[72:75]
ds_read_b64_tr_b16 a[102:103],v50 offset:19968
v_mfma_f32_16x16x32_bf16 v[88:91],a[116:119],a[236:239],v[88:91]
buffer_load_dwordx4 v47,s[36:39],s83 offen lds
v_mfma_f32_16x16x32_bf16 v[88:91],a[112:115],a[232:235],v[88:91]
s_add_u32 s83,s83,524288
ds_read_b64_tr_b16 a[104:105],v50 offset:1544
v_mfma_f32_16x16x32_bf16 v[104:107],a[112:115],a[240:243],v[104:107]
ds_read_b64_tr_b16 a[106:107],v50 offset:3592
v_mfma_f32_16x16x32_bf16 v[104:107],a[116:119],a[244:247],v[104:107]
ds_read_b64_tr_b16 a[108:109],v50 offset:17928
v_mfma_f32_16x16x32_bf16 v[120:123],a[116:119],a[252:255],v[120:123]
ds_read_b64_tr_b16 a[110:111],v50 offset:19976
v_mfma_f32_16x16x32_bf16 v[120:123],a[112:115],a[248:251],v[120:123]
s_add_u32 m0,s85,0
v_mfma_f32_16x16x32_bf16 v[76:79],a[120:123],a[224:227],v[76:79]
ds_read_b64_tr_b16 a[112:113],v50 offset:1792
v_mfma_f32_16x16x32_bf16 v[76:79],a[124:127],a[228:231],v[76:79]
buffer_load_dwordx4 v32,s[28:31],s84 offen lds
v_mfma_f32_16x16x32_bf16 v[92:95],a[124:127],a[236:239],v[92:95]
ds_read_b64_tr_b16 a[114:115],v50 offset:3840
v_mfma_f32_16x16x32_bf16 v[92:95],a[120:123],a[232:235],v[92:95]
ds_read_b64_tr_b16 a[116:117],v50 offset:18176
v_mfma_f32_16x16x32_bf16 v[108:111],a[120:123],a[240:243],v[108:111]
ds_read_b64_tr_b16 a[118:119],v50 offset:20224
v_mfma_f32_16x16x32_bf16 v[108:111],a[124:127],a[244:247],v[108:111]
s_cmp_eq_u32 s70,1
s_cselect_b32 s65,256,0
s_cselect_b32 s66,524288,0
v_mfma_f32_16x16x32_bf16 v[124:127],a[124:127],a[252:255],v[124:127]
ds_read_b64_tr_b16 a[124:125],v50 offset:18184
v_add_u32 v40,s65,v40
v_mfma_f32_16x16x32_bf16 v[124:127],a[120:123],a[248:251],v[124:127]
ds_read_b64_tr_b16 a[120:121],v50 offset:1800
v_add_u32 v41,s66,v41
v_mfma_f32_16x16x32_bf16 v[128:131],a[64:67],a[224:227],v[128:131]
buffer_load_dwordx4 v33,s[28:31],s84 offen offset:1024 lds
v_mfma_f32_16x16x32_bf16 v[128:131],a[68:71],a[228:231],v[128:131]
ds_read_b64_tr_b16 a[122:123],v50 offset:3848
v_add_u32 v42,s65,v42
v_mfma_f32_16x16x32_bf16 v[160:163],a[68:71],a[236:239],v[160:163]
ds_read_b64_tr_b16 a[126:127],v50 offset:20232
v_add_u32 v50,s86,v51
v_mfma_f32_16x16x32_bf16 v[160:163],a[64:67],a[232:235],v[160:163]
s_mov_b32 s86,s87
v_add_u32 v43,s66,v43
v_add_u32 v44,s65,v44
v_mfma_f32_16x16x32_bf16 v[192:195],a[64:67],a[240:243],v[192:195]
s_mov_b32 s87,s88
v_add_u32 v45,s66,v45
v_add_u32 v46,s65,v46
v_mfma_f32_16x16x32_bf16 v[192:195],a[68:71],a[244:247],v[192:195]
v_add_u32 v47,s66,v47
v_mfma_f32_16x16x32_bf16 v[224:227],a[68:71],a[252:255],v[224:227]
v_mfma_f32_16x16x32_bf16 v[224:227],a[64:67],a[248:251],v[224:227]
buffer_load_dwordx4 v34,s[28:31],s84 offen offset:2048 lds
v_mfma_f32_16x16x32_bf16 v[132:135],a[72:75],a[224:227],v[132:135]
v_mfma_f32_16x16x32_bf16 v[132:135],a[76:79],a[228:231],v[132:135]
v_mfma_f32_16x16x32_bf16 v[164:167],a[76:79],a[236:239],v[164:167]
v_mfma_f32_16x16x32_bf16 v[164:167],a[72:75],a[232:235],v[164:167]
v_mfma_f32_16x16x32_bf16 v[196:199],a[72:75],a[240:243],v[196:199]
v_mfma_f32_16x16x32_bf16 v[196:199],a[76:79],a[244:247],v[196:199]
v_mfma_f32_16x16x32_bf16 v[228:231],a[76:79],a[252:255],v[228:231]
buffer_load_dwordx4 v35,s[28:31],s84 offen offset:3072 lds
v_mfma_f32_16x16x32_bf16 v[228:231],a[72:75],a[248:251],v[228:231]
s_waitcnt lgkmcnt(0)
v_mfma_f32_16x16x32_bf16 v[136:139],a[80:83],a[224:227],v[136:139]
v_mfma_f32_16x16x32_bf16 v[136:139],a[84:87],a[228:231],v[136:139]
v_mfma_f32_16x16x32_bf16 v[168:171],a[84:87],a[236:239],v[168:171]
v_mfma_f32_16x16x32_bf16 v[168:171],a[80:83],a[232:235],v[168:171]
s_add_u32 m0,s85,4096
s_xor_b32 s85,s85,98304
v_mfma_f32_16x16x32_bf16 v[200:203],a[80:83],a[240:243],v[200:203]
v_mfma_f32_16x16x32_bf16 v[200:203],a[84:87],a[244:247],v[200:203]
buffer_load_dwordx4 v36,s[28:31],s84 offen lds
v_mfma_f32_16x16x32_bf16 v[232:235],a[84:87],a[252:255],v[232:235]
v_mfma_f32_16x16x32_bf16 v[232:235],a[80:83],a[248:251],v[232:235]
v_mfma_f32_16x16x32_bf16 v[140:143],a[88:91],a[224:227],v[140:143]
v_mfma_f32_16x16x32_bf16 v[140:143],a[92:95],a[228:231],v[140:143]
v_mfma_f32_16x16x32_bf16 v[172:175],a[92:95],a[236:239],v[172:175]
v_mfma_f32_16x16x32_bf16 v[172:175],a[88:91],a[232:235],v[172:175]
v_mfma_f32_16x16x32_bf16 v[204:207],a[88:91],a[240:243],v[204:207]
buffer_load_dwordx4 v37,s[28:31],s84 offen offset:1024 lds
v_mfma_f32_16x16x32_bf16 v[204:207],a[92:95],a[244:247],v[204:207]
v_mfma_f32_16x16x32_bf16 v[236:239],a[92:95],a[252:255],v[236:239]
v_mfma_f32_16x16x32_bf16 v[236:239],a[88:91],a[248:251],v[236:239]
s_waitcnt vmcnt(23)
s_barrier
v_mfma_f32_16x16x32_bf16 v[144:147],a[96:99],a[224:227],v[144:147]
s_waitcnt vmcnt(15)
ds_read_b128 a[192:195],v48
v_mfma_f32_16x16x32_bf16 v[144:147],a[100:103],a[228:231],v[144:147]
ds_read_b128 a[196:199],v49
v_mfma_f32_16x16x32_bf16 v[176:179],a[100:103],a[236:239],v[176:179]
buffer_load_dwordx4 v38,s[28:31],s84 offen offset:2048 lds
v_mfma_f32_16x16x32_bf16 v[176:179],a[96:99],a[232:235],v[176:179]
ds_read_b128 a[200:203],v48 offset:2048
v_mfma_f32_16x16x32_bf16 v[208:211],a[96:99],a[240:243],v[208:211]
ds_read_b128 a[204:207],v49 offset:2048
v_mfma_f32_16x16x32_bf16 v[208:211],a[100:103],a[244:247],v[208:211]
ds_read_b128 a[208:211],v48 offset:4096
v_mfma_f32_16x16x32_bf16 v[240:243],a[100:103],a[252:255],v[240:243]
ds_read_b128 a[212:215],v49 offset:4096
v_mfma_f32_16x16x32_bf16 v[240:243],a[96:99],a[248:251],v[240:243]
v_mfma_f32_16x16x32_bf16 v[148:151],a[104:107],a[224:227],v[148:151]
ds_read_b128 a[216:219],v48 offset:6144
v_xor_b32 v48,98304,v48
v_mfma_f32_16x16x32_bf16 v[148:151],a[108:111],a[228:231],v[148:151]
buffer_load_dwordx4 v39,s[28:31],s84 offen offset:3072 lds
v_mfma_f32_16x16x32_bf16 v[180:183],a[108:111],a[236:239],v[180:183]
s_add_u32 s84,s84,128
ds_read_b128 a[220:223],v49 offset:6144
v_xor_b32 v49,98304,v49
v_mfma_f32_16x16x32_bf16 v[180:183],a[104:107],a[232:235],v[180:183]
ds_read_b64_tr_b16 a[64:65],v50
v_mfma_f32_16x16x32_bf16 v[212:215],a[104:107],a[240:243],v[212:215]
ds_read_b64_tr_b16 a[66:67],v50 offset:2048
v_mfma_f32_16x16x32_bf16 v[212:215],a[108:111],a[244:247],v[212:215]
ds_read_b64_tr_b16 a[68:69],v50 offset:16384
v_mfma_f32_16x16x32_bf16 v[244:247],a[108:111],a[252:255],v[244:247]
v_mfma_f32_16x16x32_bf16 v[244:247],a[104:107],a[248:251],v[244:247]
ds_read_b64_tr_b16 a[70:71],v50 offset:18432
v_mfma_f32_16x16x32_bf16 v[152:155],a[112:115],a[224:227],v[152:155]
buffer_load_dword v9,v19,s[48:51],s90 offen
v_mfma_f32_16x16x32_bf16 v[152:155],a[116:119],a[228:231],v[152:155]
s_add_u32 s90,s90,896
ds_read_b64_tr_b16 a[72:73],v50 offset:8
v_mfma_f32_16x16x32_bf16 v[184:187],a[116:119],a[236:239],v[184:187]
ds_read_b64_tr_b16 a[74:75],v50 offset:2056
v_mfma_f32_16x16x32_bf16 v[184:187],a[112:115],a[232:235],v[184:187]
ds_read_b64_tr_b16 a[76:77],v50 offset:16392
v_mfma_f32_16x16x32_bf16 v[216:219],a[112:115],a[240:243],v[216:219]
ds_read_b64_tr_b16 a[78:79],v50 offset:18440
v_mfma_f32_16x16x32_bf16 v[216:219],a[116:119],a[244:247],v[216:219]
v_mfma_f32_16x16x32_bf16 v[248:251],a[116:119],a[252:255],v[248:251]
ds_read_b64_tr_b16 a[80:81],v50 offset:256
v_mfma_f32_16x16x32_bf16 v[248:251],a[112:115],a[248:251],v[248:251]
ds_read_b64_tr_b16 a[82:83],v50 offset:2304
v_mfma_f32_16x16x32_bf16 v[156:159],a[120:123],a[224:227],v[156:159]
ds_read_b64_tr_b16 a[84:85],v50 offset:16640
v_mfma_f32_16x16x32_bf16 v[156:159],a[124:127],a[228:231],v[156:159]
v_mfma_f32_16x16x32_bf16 v[188:191],a[124:127],a[236:239],v[188:191]
ds_read_b64_tr_b16 a[86:87],v50 offset:18688
v_mfma_f32_16x16x32_bf16 v[188:191],a[120:123],a[232:235],v[188:191]
ds_read_b64_tr_b16 a[88:89],v50 offset:264
v_mfma_f32_16x16x32_bf16 v[220:223],a[120:123],a[240:243],v[220:223]
ds_read_b64_tr_b16 a[90:91],v50 offset:2312
v_mfma_f32_16x16x32_bf16 v[220:223],a[124:127],a[244:247],v[220:223]
v_mfma_f32_16x16x32_bf16 v[252:255],a[124:127],a[252:255],v[252:255]
s_waitcnt lgkmcnt(4)
ds_read_b64_tr_b16 a[92:93],v50 offset:16648
v_mfma_f32_16x16x32_bf16 v[252:255],a[120:123],a[248:251],v[252:255]
s_add_u32 m0,s88,0
ds_read_b64_tr_b16 a[94:95],v50 offset:18696
v_mfma_f32_16x16x32_bf16 a[0:3],a[64:67],a[192:195],a[0:3]
ds_read_b64_tr_b16 a[96:97],v50 offset:512
v_mfma_f32_16x16x32_bf16 a[0:3],a[68:71],a[196:199],a[0:3]
buffer_load_dwordx4 v40,s[36:39],s83 offen lds
v_mfma_f32_16x16x32_bf16 a[16:19],a[68:71],a[204:207],a[16:19]
ds_read_b64_tr_b16 a[98:99],v50 offset:2560
v_mfma_f32_16x16x32_bf16 a[16:19],a[64:67],a[200:203],a[16:19]
ds_read_b64_tr_b16 a[100:101],v50 offset:16896
v_mfma_f32_16x16x32_bf16 a[32:35],a[64:67],a[208:211],a[32:35]
ds_read_b64_tr_b16 a[102:103],v50 offset:18944
v_mfma_f32_16x16x32_bf16 a[32:35],a[68:71],a[212:215],a[32:35]
)A"
R"A(v_mfma_f32_16x16x32_bf16 a[48:51],a[68:71],a[220:223],a[48:51]
s_add_u32 m0,s88,1024
ds_read_b64_tr_b16 a[104:105],v50 offset:520
v_mfma_f32_16x16x32_bf16 a[48:51],a[64:67],a[216:219],a[48:51]
ds_read_b64_tr_b16 a[106:107],v50 offset:2568
v_mfma_f32_16x16x32_bf16 a[4:7],a[72:75],a[192:195],a[4:7]
buffer_load_dwordx4 v41,s[36:39],s83 offen lds
v_mfma_f32_16x16x32_bf16 a[4:7],a[76:79],a[196:199],a[4:7]
ds_read_b64_tr_b16 a[108:109],v50 offset:16904
v_mfma_f32_16x16x32_bf16 a[20:23],a[76:79],a[204:207],a[20:23]
ds_read_b64_tr_b16 a[110:111],v50 offset:18952
v_mfma_f32_16x16x32_bf16 a[20:23],a[72:75],a[200:203],a[20:23]
ds_read_b64_tr_b16 a[112:113],v50 offset:768
v_mfma_f32_16x16x32_bf16 a[36:39],a[72:75],a[208:211],a[36:39]
ds_read_b64_tr_b16 a[114:115],v50 offset:2816
v_mfma_f32_16x16x32_bf16 a[36:39],a[76:79],a[212:215],a[36:39]
s_add_u32 m0,s88,2048
v_mfma_f32_16x16x32_bf16 a[52:55],a[76:79],a[220:223],a[52:55]
ds_read_b64_tr_b16 a[116:117],v50 offset:17152
v_mfma_f32_16x16x32_bf16 a[52:55],a[72:75],a[216:219],a[52:55]
buffer_load_dwordx4 v42,s[36:39],s83 offen lds
v_mfma_f32_16x16x32_bf16 a[8:11],a[80:83],a[192:195],a[8:11]
s_waitcnt lgkmcnt(5)
ds_read_b64_tr_b16 a[118:119],v50 offset:19200
v_mfma_f32_16x16x32_bf16 a[8:11],a[84:87],a[196:199],a[8:11]
ds_read_b64_tr_b16 a[120:121],v50 offset:776
v_mfma_f32_16x16x32_bf16 a[24:27],a[84:87],a[204:207],a[24:27]
ds_read_b64_tr_b16 a[122:123],v50 offset:2824
v_mfma_f32_16x16x32_bf16 a[24:27],a[80:83],a[200:203],a[24:27]
ds_read_b64_tr_b16 a[124:125],v50 offset:17160
v_mfma_f32_16x16x32_bf16 a[40:43],a[80:83],a[208:211],a[40:43]
s_add_u32 m0,s88,3072
v_mfma_f32_16x16x32_bf16 a[40:43],a[84:87],a[212:215],a[40:43]
ds_read_b64_tr_b16 a[126:127],v50 offset:19208
v_mfma_f32_16x16x32_bf16 a[56:59],a[84:87],a[220:223],a[56:59]
buffer_load_dwordx4 v43,s[36:39],s83 offen lds
v_mfma_f32_16x16x32_bf16 a[56:59],a[80:83],a[216:219],a[56:59]
ds_read_b64_tr_b16 a[64:65],v50 offset:1024
v_mfma_f32_16x16x32_bf16 a[12:15],a[88:91],a[192:195],a[12:15]
ds_read_b64_tr_b16 a[66:67],v50 offset:3072
v_mfma_f32_16x16x32_bf16 a[12:15],a[92:95],a[196:199],a[12:15]
ds_read_b64_tr_b16 a[68:69],v50 offset:17408
v_mfma_f32_16x16x32_bf16 a[28:31],a[92:95],a[204:207],a[28:31]
ds_read_b64_tr_b16 a[70:71],v50 offset:19456
v_mfma_f32_16x16x32_bf16 a[28:31],a[88:91],a[200:203],a[28:31]
s_add_u32 m0,s88,4096
v_mfma_f32_16x16x32_bf16 a[44:47],a[88:91],a[208:211],a[44:47]
ds_read_b64_tr_b16 a[72:73],v50 offset:1032
v_mfma_f32_16x16x32_bf16 a[44:47],a[92:95],a[212:215],a[44:47]
buffer_load_dwordx4 v44,s[36:39],s83 offen lds
v_mfma_f32_16x16x32_bf16 a[60:63],a[92:95],a[220:223],a[60:63]
ds_read_b64_tr_b16 a[74:75],v50 offset:3080
v_mfma_f32_16x16x32_bf16 a[60:63],a[88:91],a[216:219],a[60:63]
ds_read_b64_tr_b16 a[76:77],v50 offset:17416
v_mfma_f32_16x16x32_bf16 v[64:67],a[96:99],a[192:195],v[64:67]
ds_read_b64_tr_b16 a[78:79],v50 offset:19464
v_mfma_f32_16x16x32_bf16 v[64:67],a[100:103],a[196:199],v[64:67]
ds_read_b64_tr_b16 a[80:81],v50 offset:1280
v_mfma_f32_16x16x32_bf16 v[80:83],a[100:103],a[204:207],v[80:83]
s_add_u32 m0,s88,5120
v_mfma_f32_16x16x32_bf16 v[80:83],a[96:99],a[200:203],v[80:83]
ds_read_b64_tr_b16 a[82:83],v50 offset:3328
v_mfma_f32_16x16x32_bf16 v[96:99],a[96:99],a[208:211],v[96:99]
buffer_load_dwordx4 v45,s[36:39],s83 offen lds
v_mfma_f32_16x16x32_bf16 v[96:99],a[100:103],a[212:215],v[96:99]
ds_read_b64_tr_b16 a[84:85],v50 offset:17664
v_mfma_f32_16x16x32_bf16 v[112:115],a[100:103],a[220:223],v[112:115]
ds_read_b64_tr_b16 a[86:87],v50 offset:19712
v_mfma_f32_16x16x32_bf16 v[112:115],a[96:99],a[216:219],v[112:115]
ds_read_b64_tr_b16 a[88:89],v50 offset:1288
v_mfma_f32_16x16x32_bf16 v[68:71],a[104:107],a[192:195],v[68:71]
s_waitcnt lgkmcnt(5)
ds_read_b64_tr_b16 a[90:91],v50 offset:3336
v_mfma_f32_16x16x32_bf16 v[68:71],a[108:111],a[196:199],v[68:71]
s_add_u32 m0,s88,6144
v_mfma_f32_16x16x32_bf16 v[84:87],a[108:111],a[204:207],v[84:87]
ds_read_b64_tr_b16 a[92:93],v50 offset:17672
v_mfma_f32_16x16x32_bf16 v[84:87],a[104:107],a[200:203],v[84:87]
buffer_load_dwordx4 v46,s[36:39],s83 offen lds
v_mfma_f32_16x16x32_bf16 v[100:103],a[104:107],a[208:211],v[100:103]
ds_read_b64_tr_b16 a[94:95],v50 offset:19720
v_mfma_f32_16x16x32_bf16 v[100:103],a[108:111],a[212:215],v[100:103]
ds_read_b64_tr_b16 a[96:97],v50 offset:1536
v_mfma_f32_16x16x32_bf16 v[116:119],a[108:111],a[220:223],v[116:119]
ds_read_b64_tr_b16 a[98:99],v50 offset:3584
v_mfma_f32_16x16x32_bf16 v[116:119],a[104:107],a[216:219],v[116:119]
ds_read_b64_tr_b16 a[100:101],v50 offset:17920
v_mfma_f32_16x16x32_bf16 v[72:75],a[112:115],a[192:195],v[72:75]
s_add_u32 m0,s88,7168
s_mov_b32 s88,s69
v_mfma_f32_16x16x32_bf16 v[72:75],a[116:119],a[196:199],v[72:75]
ds_read_b64_tr_b16 a[102:103],v50 offset:19968
v_mfma_f32_16x16x32_bf16 v[88:91],a[116:119],a[204:207],v[88:91]
buffer_load_dwordx4 v47,s[36:39],s83 offen lds
v_mfma_f32_16x16x32_bf16 v[88:91],a[112:115],a[200:203],v[88:91]
s_add_u32 s83,s83,524288
ds_read_b64_tr_b16 a[104:105],v50 offset:1544
v_mfma_f32_16x16x32_bf16 v[104:107],a[112:115],a[208:211],v[104:107]
ds_read_b64_tr_b16 a[106:107],v50 offset:3592
v_mfma_f32_16x16x32_bf16 v[104:107],a[116:119],a[212:215],v[104:107]
ds_read_b64_tr_b16 a[108:109],v50 offset:17928
v_mfma_f32_16x16x32_bf16 v[120:123],a[116:119],a[220:223],v[120:123]
ds_read_b64_tr_b16 a[110:111],v50 offset:19976
v_mfma_f32_16x16x32_bf16 v[120:123],a[112:115],a[216:219],v[120:123]
s_add_u32 m0,s85,0
v_mfma_f32_16x16x32_bf16 v[76:79],a[120:123],a[192:195],v[76:79]
ds_read_b64_tr_b16 a[112:113],v50 offset:1792
v_mfma_f32_16x16x32_bf16 v[76:79],a[124:127],a[196:199],v[76:79]
buffer_load_dwordx4 v32,s[28:31],s84 offen lds
v_mfma_f32_16x16x32_bf16 v[92:95],a[124:127],a[204:207],v[92:95]
ds_read_b64_tr_b16 a[114:115],v50 offset:3840
v_mfma_f32_16x16x32_bf16 v[92:95],a[120:123],a[200:203],v[92:95]
ds_read_b64_tr_b16 a[116:117],v50 offset:18176
v_mfma_f32_16x16x32_bf16 v[108:111],a[120:123],a[208:211],v[108:111]
ds_read_b64_tr_b16 a[118:119],v50 offset:20224
v_mfma_f32_16x16x32_bf16 v[108:111],a[124:127],a[212:215],v[108:111]
v_mfma_f32_16x16x32_bf16 v[124:127],a[124:127],a[220:223],v[124:127]
ds_read_b64_tr_b16 a[124:125],v50 offset:18184
v_mfma_f32_16x16x32_bf16 v[124:127],a[120:123],a[216:219],v[124:127]
ds_read_b64_tr_b16 a[120:121],v50 offset:1800
v_mfma_f32_16x16x32_bf16 v[128:131],a[64:67],a[192:195],v[128:131]
buffer_load_dwordx4 v33,s[28:31],s84 offen offset:1024 lds
v_mfma_f32_16x16x32_bf16 v[128:131],a[68:71],a[196:199],v[128:131]
ds_read_b64_tr_b16 a[122:123],v50 offset:3848
v_mfma_f32_16x16x32_bf16 v[160:163],a[68:71],a[204:207],v[160:163]
ds_read_b64_tr_b16 a[126:127],v50 offset:20232
v_add_u32 v50,s86,v51
v_mfma_f32_16x16x32_bf16 v[160:163],a[64:67],a[200:203],v[160:163]
v_mfma_f32_16x16x32_bf16 v[192:195],a[64:67],a[208:211],v[192:195]
v_mfma_f32_16x16x32_bf16 v[192:195],a[68:71],a[212:215],v[192:195]
v_mfma_f32_16x16x32_bf16 v[224:227],a[68:71],a[220:223],v[224:227]
v_mfma_f32_16x16x32_bf16 v[224:227],a[64:67],a[216:219],v[224:227]
buffer_load_dwordx4 v34,s[28:31],s84 offen offset:2048 lds
v_mfma_f32_16x16x32_bf16 v[132:135],a[72:75],a[192:195],v[132:135]
v_mfma_f32_16x16x32_bf16 v[132:135],a[76:79],a[196:199],v[132:135]
v_mfma_f32_16x16x32_bf16 v[164:167],a[76:79],a[204:207],v[164:167]
v_mfma_f32_16x16x32_bf16 v[164:167],a[72:75],a[200:203],v[164:167]
v_mfma_f32_16x16x32_bf16 v[196:199],a[72:75],a[208:211],v[196:199]
v_mfma_f32_16x16x32_bf16 v[196:199],a[76:79],a[212:215],v[196:199]
v_mfma_f32_16x16x32_bf16 v[228:231],a[76:79],a[220:223],v[228:231]
buffer_load_dwordx4 v35,s[28:31],s84 offen offset:3072 lds
v_mfma_f32_16x16x32_bf16 v[228:231],a[72:75],a[216:219],v[228:231]
s_waitcnt lgkmcnt(0)
v_mfma_f32_16x16x32_bf16 v[136:139],a[80:83],a[192:195],v[136:139]
v_mfma_f32_16x16x32_bf16 v[136:139],a[84:87],a[196:199],v[136:139]
v_mfma_f32_16x16x32_bf16 v[168:171],a[84:87],a[204:207],v[168:171]
v_mfma_f32_16x16x32_bf16 v[168:171],a[80:83],a[200:203],v[168:171]
s_add_u32 m0,s85,4096
s_xor_b32 s85,s85,98304
v_mfma_f32_16x16x32_bf16 v[200:203],a[80:83],a[208:211],v[200:203]
v_mfma_f32_16x16x32_bf16 v[200:203],a[84:87],a[212:215],v[200:203]
buffer_load_dwordx4 v36,s[28:31],s84 offen lds
v_mfma_f32_16x16x32_bf16 v[232:235],a[84:87],a[220:223],v[232:235]
v_mfma_f32_16x16x32_bf16 v[232:235],a[80:83],a[216:219],v[232:235]
v_mfma_f32_16x16x32_bf16 v[140:143],a[88:91],a[192:195],v[140:143]
v_mfma_f32_16x16x32_bf16 v[140:143],a[92:95],a[196:199],v[140:143]
v_mfma_f32_16x16x32_bf16 v[172:175],a[92:95],a[204:207],v[172:175]
v_mfma_f32_16x16x32_bf16 v[172:175],a[88:91],a[200:203],v[172:175]
v_mfma_f32_16x16x32_bf16 v[204:207],a[88:91],a[208:211],v[204:207]
buffer_load_dwordx4 v37,s[28:31],s84 offen offset:1024 lds
v_mfma_f32_16x16x32_bf16 v[204:207],a[92:95],a[212:215],v[204:207]
v_mfma_f32_16x16x32_bf16 v[236:239],a[92:95],a[220:223],v[236:239]
v_mfma_f32_16x16x32_bf16 v[236:239],a[88:91],a[216:219],v[236:239]
s_waitcnt vmcnt(23)
s_barrier
v_mfma_f32_16x16x32_bf16 v[144:147],a[96:99],a[192:195],v[144:147]
s_waitcnt vmcnt(15)
ds_read_b128 a[224:227],v48
v_mfma_f32_16x16x32_bf16 v[144:147],a[100:103],a[196:199],v[144:147]
ds_read_b128 a[228:231],v49
v_mfma_f32_16x16x32_bf16 v[176:179],a[100:103],a[204:207],v[176:179]
buffer_load_dwordx4 v38,s[28:31],s84 offen offset:2048 lds
v_mfma_f32_16x16x32_bf16 v[176:179],a[96:99],a[200:203],v[176:179]
ds_read_b128 a[232:235],v48 offset:2048
v_mfma_f32_16x16x32_bf16 v[208:211],a[96:99],a[208:211],v[208:211]
ds_read_b128 a[236:239],v49 offset:2048
v_mfma_f32_16x16x32_bf16 v[208:211],a[100:103],a[212:215],v[208:211]
ds_read_b128 a[240:243],v48 offset:4096
v_mfma_f32_16x16x32_bf16 v[240:243],a[100:103],a[220:223],v[240:243]
ds_read_b128 a[244:247],v49 offset:4096
v_mfma_f32_16x16x32_bf16 v[240:243],a[96:99],a[216:219],v[240:243]
v_mfma_f32_16x16x32_bf16 v[148:151],a[104:107],a[192:195],v[148:151]
ds_read_b128 a[248:251],v48 offset:6144
v_mfma_f32_16x16x32_bf16 v[148:151],a[108:111],a[196:199],v[148:151]
buffer_load_dwordx4 v39,s[28:31],s84 offen offset:3072 lds
v_mfma_f32_16x16x32_bf16 v[180:183],a[108:111],a[204:207],v[180:183]
s_add_u32 s84,s84,128
ds_read_b128 a[252:255],v49 offset:6144
v_mfma_f32_16x16x32_bf16 v[180:183],a[104:107],a[200:203],v[180:183]
ds_read_b64_tr_b16 a[64:65],v50
v_mfma_f32_16x16x32_bf16 v[212:215],a[104:107],a[208:211],v[212:215]
ds_read_b64_tr_b16 a[66:67],v50 offset:2048
v_mfma_f32_16x16x32_bf16 v[212:215],a[108:111],a[212:215],v[212:215]
ds_read_b64_tr_b16 a[68:69],v50 offset:16384
v_mfma_f32_16x16x32_bf16 v[244:247],a[108:111],a[220:223],v[244:247]
v_mfma_f32_16x16x32_bf16 v[244:247],a[104:107],a[216:219],v[244:247]
ds_read_b64_tr_b16 a[70:71],v50 offset:18432
v_mfma_f32_16x16x32_bf16 v[152:155],a[112:115],a[192:195],v[152:155]
buffer_load_dword v8,v19,s[48:51],s90 offen
v_mfma_f32_16x16x32_bf16 v[152:155],a[116:119],a[196:199],v[152:155]
s_add_u32 s90,s90,896
ds_read_b64_tr_b16 a[72:73],v50 offset:8
v_mfma_f32_16x16x32_bf16 v[184:187],a[116:119],a[204:207],v[184:187]
ds_read_b64_tr_b16 a[74:75],v50 offset:2056
v_mfma_f32_16x16x32_bf16 v[184:187],a[112:115],a[200:203],v[184:187]
ds_read_b64_tr_b16 a[76:77],v50 offset:16392
v_mfma_f32_16x16x32_bf16 v[216:219],a[112:115],a[208:211],v[216:219]
ds_read_b64_tr_b16 a[78:79],v50 offset:18440
v_mfma_f32_16x16x32_bf16 v[216:219],a[116:119],a[212:215],v[216:219]
v_mfma_f32_16x16x32_bf16 v[248:251],a[116:119],a[220:223],v[248:251]
ds_read_b64_tr_b16 a[80:81],v50 offset:256
v_mfma_f32_16x16x32_bf16 v[248:251],a[112:115],a[216:219],v[248:251]
ds_read_b64_tr_b16 a[82:83],v50 offset:2304
v_mfma_f32_16x16x32_bf16 v[156:159],a[120:123],a[192:195],v[156:159]
ds_read_b64_tr_b16 a[84:85],v50 offset:16640
v_mfma_f32_16x16x32_bf16 v[156:159],a[124:127],a[196:199],v[156:159]
v_mfma_f32_16x16x32_bf16 v[188:191],a[124:127],a[204:207],v[188:191]
ds_read_b64_tr_b16 a[86:87],v50 offset:18688
v_mfma_f32_16x16x32_bf16 v[188:191],a[120:123],a[200:203],v[188:191]
ds_read_b64_tr_b16 a[88:89],v50 offset:264
v_mfma_f32_16x16x32_bf16 v[220:223],a[120:123],a[208:211],v[220:223]
)A"
R"A(ds_read_b64_tr_b16 a[90:91],v50 offset:2312
v_mfma_f32_16x16x32_bf16 v[220:223],a[124:127],a[212:215],v[220:223]
v_mfma_f32_16x16x32_bf16 v[252:255],a[124:127],a[220:223],v[252:255]
ds_read_b64_tr_b16 a[92:93],v50 offset:16648
v_mfma_f32_16x16x32_bf16 v[252:255],a[120:123],a[216:219],v[252:255]
ds_read_b64_tr_b16 a[94:95],v50 offset:18696
s_sub_u32 s70,s70,1
s_cmp_lg_u32 s70,0
s_cbranch_scc1 L_qp_%=
s_waitcnt lgkmcnt(6)
v_mfma_f32_16x16x32_bf16 a[0:3],a[64:67],a[224:227],a[0:3]
s_memrealtime s[92:93]
s_memtime s[56:57]
s_add_u32 m0,s85,0
v_mfma_f32_16x16x32_bf16 a[0:3],a[68:71],a[228:231],a[0:3]
buffer_load_dwordx4 v32,s[28:31],s84 offen lds
v_mfma_f32_16x16x32_bf16 a[16:19],a[68:71],a[236:239],a[16:19]
s_sub_u32 s83,s83,524288
ds_read_b64_tr_b16 a[96:97],v50 offset:512
v_subrev_u32 v40,256,v40
v_mfma_f32_16x16x32_bf16 a[16:19],a[64:67],a[232:235],a[16:19]
s_mov_b32 s69,s86
ds_read_b64_tr_b16 a[98:99],v50 offset:2560
v_subrev_u32 v41,256,v41
v_mfma_f32_16x16x32_bf16 a[32:35],a[64:67],a[240:243],a[32:35]
s_mov_b32 s86,s87
ds_read_b64_tr_b16 a[100:101],v50 offset:16896
v_subrev_u32 v42,256,v42
v_mfma_f32_16x16x32_bf16 a[32:35],a[68:71],a[244:247],a[32:35]
s_mov_b32 s87,s88
ds_read_b64_tr_b16 a[102:103],v50 offset:18944
v_subrev_u32 v43,256,v43
v_mfma_f32_16x16x32_bf16 a[48:51],a[68:71],a[252:255],a[48:51]
s_add_u32 s75,s75,16384
v_subrev_u32 v44,256,v44
v_subrev_u32 v45,256,v45
v_mfma_f32_16x16x32_bf16 a[48:51],a[64:67],a[248:251],a[48:51]
buffer_load_dwordx4 v33,s[28:31],s84 offen offset:1024 lds
v_mfma_f32_16x16x32_bf16 a[4:7],a[72:75],a[224:227],a[4:7]
s_sub_u32 s78,s77,32768
ds_read_b64_tr_b16 a[104:105],v50 offset:520
v_subrev_u32 v46,256,v46
v_mfma_f32_16x16x32_bf16 a[4:7],a[76:79],a[228:231],a[4:7]
L_pd2_%=:
s_cbranch_execz L_pd6_%=
s_and_b32 s78,s78,0x1ffff
ds_read_b64_tr_b16 a[106:107],v50 offset:2568
v_subrev_u32 v47,256,v47
v_mfma_f32_16x16x32_bf16 a[20:23],a[76:79],a[236:239],a[20:23]
s_sub_u32 s78,s78,s77
ds_read_b64_tr_b16 a[108:109],v50 offset:16904
v_xor_b32 v48,98304,v48
v_mfma_f32_16x16x32_bf16 a[20:23],a[72:75],a[232:235],a[20:23]
s_add_u32 s77,s77,s78
ds_read_b64_tr_b16 a[110:111],v50 offset:18952
v_xor_b32 v49,98304,v49
v_mfma_f32_16x16x32_bf16 a[36:39],a[72:75],a[240:243],a[36:39]
s_mov_b32 s6,0
v_add_u32 v52,s87,v51
v_mfma_f32_16x16x32_bf16 a[36:39],a[76:79],a[244:247],a[36:39]
buffer_load_dwordx4 v34,s[28:31],s84 offen offset:2048 lds
v_mfma_f32_16x16x32_bf16 a[52:55],a[76:79],a[252:255],a[52:55]
s_mul_i32 s66,s60,40960
ds_read_b64_tr_b16 a[112:113],v50 offset:768
v_add_u32 v11,0,v23
v_mfma_f32_16x16x32_bf16 a[52:55],a[72:75],a[248:251],a[52:55]
s_add_u32 s65,s63,1
ds_read_b64_tr_b16 a[114:115],v50 offset:2816
v_mul_u32_u24 v12,3277,v11
v_mfma_f32_16x16x32_bf16 a[8:11],a[80:83],a[224:227],a[8:11]
s_waitcnt lgkmcnt(5)
ds_read_b64_tr_b16 a[116:117],v50 offset:17152
v_lshrrev_b32 v12,16,v12
v_mfma_f32_16x16x32_bf16 a[8:11],a[84:87],a[228:231],a[8:11]
ds_read_b64_tr_b16 a[118:119],v50 offset:19200
v_mul_u32_u24 v13,20,v12
v_mfma_f32_16x16x32_bf16 a[24:27],a[84:87],a[236:239],a[24:27]
v_sub_u32 v13,v11,v13
v_bfe_u32 v14,v12,2,1
v_mfma_f32_16x16x32_bf16 a[24:27],a[80:83],a[232:235],a[24:27]
buffer_load_dwordx4 v35,s[28:31],s84 offen offset:3072 lds
v_mfma_f32_16x16x32_bf16 a[40:43],a[80:83],a[240:243],a[40:43]
ds_read_b64_tr_b16 a[120:121],v50 offset:776
v_lshlrev_b32 v14,1,v14
v_mfma_f32_16x16x32_bf16 a[40:43],a[84:87],a[244:247],a[40:43]
ds_read_b64_tr_b16 a[122:123],v50 offset:2824
v_xor_b32 v13,v13,v14
v_mfma_f32_16x16x32_bf16 a[56:59],a[84:87],a[252:255],a[56:59]
ds_read_b64_tr_b16 a[124:125],v50 offset:17160
v_lshrrev_b32 v14,2,v13
v_mfma_f32_16x16x32_bf16 a[56:59],a[80:83],a[248:251],a[56:59]
s_add_u32 m0,s85,4096
s_xor_b32 s85,s85,98304
ds_read_b64_tr_b16 a[126:127],v50 offset:19208
v_mfma_f32_16x16x32_bf16 a[12:15],a[88:91],a[224:227],a[12:15]
v_cmp_eq_u32 vcc,3,v14
v_min_u32 v15,3,v14
v_mfma_f32_16x16x32_bf16 a[12:15],a[92:95],a[228:231],a[12:15]
buffer_load_dwordx4 v36,s[28:31],s84 offen lds
v_mfma_f32_16x16x32_bf16 a[28:31],a[92:95],a[236:239],a[28:31]
ds_read_b64_tr_b16 a[64:65],v50 offset:1024
v_add_u32 v15,s65,v15
v_mfma_f32_16x16x32_bf16 a[28:31],a[88:91],a[232:235],a[28:31]
ds_read_b64_tr_b16 a[66:67],v50 offset:3072
v_and_b32 v15,3,v15
v_mfma_f32_16x16x32_bf16 a[44:47],a[88:91],a[240:243],a[44:47]
ds_read_b64_tr_b16 a[68:69],v50 offset:17408
v_cndmask_b32_e64 v15,v15,4,vcc
v_mfma_f32_16x16x32_bf16 a[44:47],a[92:95],a[244:247],a[44:47]
ds_read_b64_tr_b16 a[70:71],v50 offset:19456
v_and_b32 v13,3,v13
v_mfma_f32_16x16x32_bf16 a[60:63],a[92:95],a[252:255],a[60:63]
v_lshl_add_u32 v13,v15,2,v13
v_mul_u32_u24 v12,1280,v12
v_mfma_f32_16x16x32_bf16 a[60:63],a[88:91],a[248:251],a[60:63]
buffer_load_dwordx4 v37,s[28:31],s84 offen offset:1024 lds
v_mfma_f32_16x16x32_bf16 v[64:67],a[96:99],a[224:227],v[64:67]
ds_read_b64_tr_b16 a[72:73],v50 offset:1032
v_lshl_add_u32 v12,v13,4,v12
v_mfma_f32_16x16x32_bf16 v[64:67],a[100:103],a[228:231],v[64:67]
ds_read_b64_tr_b16 a[74:75],v50 offset:3080
v_add_u32 v12,s66,v12
v_mfma_f32_16x16x32_bf16 v[80:83],a[100:103],a[236:239],v[80:83]
ds_read_b64_tr_b16 a[76:77],v50 offset:17416
v_subrev_u32 v24,0,v12
v_mfma_f32_16x16x32_bf16 v[80:83],a[96:99],a[232:235],v[80:83]
ds_read_b64_tr_b16 a[78:79],v50 offset:19464
v_add_u32 v11,64,v23
v_mfma_f32_16x16x32_bf16 v[96:99],a[96:99],a[240:243],v[96:99]
v_mul_u32_u24 v12,3277,v11
v_lshrrev_b32 v12,16,v12
v_mfma_f32_16x16x32_bf16 v[96:99],a[100:103],a[244:247],v[96:99]
buffer_load_dwordx4 v38,s[28:31],s84 offen offset:2048 lds
v_mfma_f32_16x16x32_bf16 v[112:115],a[100:103],a[252:255],v[112:115]
ds_read_b64_tr_b16 a[80:81],v50 offset:1280
v_mul_u32_u24 v13,20,v12
v_mfma_f32_16x16x32_bf16 v[112:115],a[96:99],a[248:251],v[112:115]
s_waitcnt lgkmcnt(5)
ds_read_b64_tr_b16 a[82:83],v50 offset:3328
v_sub_u32 v13,v11,v13
v_mfma_f32_16x16x32_bf16 v[68:71],a[104:107],a[224:227],v[68:71]
ds_read_b64_tr_b16 a[84:85],v50 offset:17664
v_bfe_u32 v14,v12,2,1
v_mfma_f32_16x16x32_bf16 v[68:71],a[108:111],a[228:231],v[68:71]
ds_read_b64_tr_b16 a[86:87],v50 offset:19712
v_lshlrev_b32 v14,1,v14
v_mfma_f32_16x16x32_bf16 v[84:87],a[108:111],a[236:239],v[84:87]
v_xor_b32 v13,v13,v14
v_lshrrev_b32 v14,2,v13
v_mfma_f32_16x16x32_bf16 v[84:87],a[104:107],a[232:235],v[84:87]
buffer_load_dwordx4 v39,s[28:31],s84 offen offset:3072 lds
v_mfma_f32_16x16x32_bf16 v[100:103],a[104:107],a[240:243],v[100:103]
s_add_u32 s84,s84,128
ds_read_b64_tr_b16 a[88:89],v50 offset:1288
v_cmp_eq_u32 vcc,3,v14
v_mfma_f32_16x16x32_bf16 v[100:103],a[108:111],a[244:247],v[100:103]
ds_read_b64_tr_b16 a[90:91],v50 offset:3336
v_min_u32 v15,3,v14
v_mfma_f32_16x16x32_bf16 v[116:119],a[108:111],a[252:255],v[116:119]
ds_read_b64_tr_b16 a[92:93],v50 offset:17672
v_add_u32 v15,s65,v15
v_mfma_f32_16x16x32_bf16 v[116:119],a[104:107],a[248:251],v[116:119]
s_add_u32 m0,s88,0
ds_read_b64_tr_b16 a[94:95],v50 offset:19720
v_and_b32 v15,3,v15
v_mfma_f32_16x16x32_bf16 v[72:75],a[112:115],a[224:227],v[72:75]
v_cndmask_b32_e64 v15,v15,4,vcc
v_and_b32 v13,3,v13
v_mfma_f32_16x16x32_bf16 v[72:75],a[116:119],a[228:231],v[72:75]
buffer_load_dwordx4 v40,s[36:39],s83 offen lds
v_mfma_f32_16x16x32_bf16 v[88:91],a[116:119],a[236:239],v[88:91]
ds_read_b64_tr_b16 a[96:97],v50 offset:1536
v_lshl_add_u32 v13,v15,2,v13
v_mfma_f32_16x16x32_bf16 v[88:91],a[112:115],a[232:235],v[88:91]
ds_read_b64_tr_b16 a[98:99],v50 offset:3584
v_mul_u32_u24 v12,1280,v12
v_mfma_f32_16x16x32_bf16 v[104:107],a[112:115],a[240:243],v[104:107]
ds_read_b64_tr_b16 a[100:101],v50 offset:17920
v_lshl_add_u32 v12,v13,4,v12
v_mfma_f32_16x16x32_bf16 v[104:107],a[116:119],a[244:247],v[104:107]
s_add_u32 m0,s88,1024
ds_read_b64_tr_b16 a[102:103],v50 offset:19968
v_add_u32 v12,s66,v12
v_mfma_f32_16x16x32_bf16 v[120:123],a[116:119],a[252:255],v[120:123]
v_subrev_u32 v25,1024,v12
v_add_u32 v11,128,v23
v_mfma_f32_16x16x32_bf16 v[120:123],a[112:115],a[248:251],v[120:123]
buffer_load_dwordx4 v41,s[36:39],s83 offen lds
v_mfma_f32_16x16x32_bf16 v[76:79],a[120:123],a[224:227],v[76:79]
ds_read_b64_tr_b16 a[104:105],v50 offset:1544
v_mul_u32_u24 v12,3277,v11
v_mfma_f32_16x16x32_bf16 v[76:79],a[124:127],a[228:231],v[76:79]
ds_read_b64_tr_b16 a[106:107],v50 offset:3592
v_lshrrev_b32 v12,16,v12
v_mfma_f32_16x16x32_bf16 v[92:95],a[124:127],a[236:239],v[92:95]
ds_read_b64_tr_b16 a[108:109],v50 offset:17928
v_mul_u32_u24 v13,20,v12
v_mfma_f32_16x16x32_bf16 v[92:95],a[120:123],a[232:235],v[92:95]
s_add_u32 m0,s88,2048
ds_read_b64_tr_b16 a[110:111],v50 offset:19976
v_sub_u32 v13,v11,v13
v_mfma_f32_16x16x32_bf16 v[108:111],a[120:123],a[240:243],v[108:111]
v_bfe_u32 v14,v12,2,1
v_lshlrev_b32 v14,1,v14
v_mfma_f32_16x16x32_bf16 v[108:111],a[124:127],a[244:247],v[108:111]
buffer_load_dwordx4 v42,s[36:39],s83 offen lds
v_mfma_f32_16x16x32_bf16 v[124:127],a[124:127],a[252:255],v[124:127]
ds_read_b64_tr_b16 a[112:113],v50 offset:1792
v_xor_b32 v13,v13,v14
v_mfma_f32_16x16x32_bf16 v[124:127],a[120:123],a[248:251],v[124:127]
ds_read_b64_tr_b16 a[114:115],v50 offset:3840
v_lshrrev_b32 v14,2,v13
v_mfma_f32_16x16x32_bf16 v[128:131],a[64:67],a[224:227],v[128:131]
ds_read_b64_tr_b16 a[116:117],v50 offset:18176
v_cmp_eq_u32 vcc,3,v14
v_mfma_f32_16x16x32_bf16 v[128:131],a[68:71],a[228:231],v[128:131]
s_add_u32 m0,s88,3072
ds_read_b64_tr_b16 a[118:119],v50 offset:20224
v_min_u32 v15,3,v14
v_mfma_f32_16x16x32_bf16 v[160:163],a[68:71],a[236:239],v[160:163]
v_add_u32 v15,s65,v15
v_and_b32 v15,3,v15
v_mfma_f32_16x16x32_bf16 v[160:163],a[64:67],a[232:235],v[160:163]
buffer_load_dwordx4 v43,s[36:39],s83 offen lds
v_mfma_f32_16x16x32_bf16 v[192:195],a[64:67],a[240:243],v[192:195]
ds_read_b64_tr_b16 a[120:121],v50 offset:1800
v_cndmask_b32_e64 v15,v15,4,vcc
v_mfma_f32_16x16x32_bf16 v[192:195],a[68:71],a[244:247],v[192:195]
ds_read_b64_tr_b16 a[122:123],v50 offset:3848
v_and_b32 v13,3,v13
v_mfma_f32_16x16x32_bf16 v[224:227],a[68:71],a[252:255],v[224:227]
s_waitcnt lgkmcnt(4)
ds_read_b64_tr_b16 a[124:125],v50 offset:18184
v_lshl_add_u32 v13,v15,2,v13
v_mfma_f32_16x16x32_bf16 v[224:227],a[64:67],a[248:251],v[224:227]
s_add_u32 m0,s88,4096
ds_read_b64_tr_b16 a[126:127],v50 offset:20232
v_add_u32 v50,s86,v51
v_mfma_f32_16x16x32_bf16 v[132:135],a[72:75],a[224:227],v[132:135]
v_mul_u32_u24 v12,1280,v12
v_lshl_add_u32 v12,v13,4,v12
v_mfma_f32_16x16x32_bf16 v[132:135],a[76:79],a[228:231],v[132:135]
buffer_load_dwordx4 v44,s[36:39],s83 offen lds
v_mfma_f32_16x16x32_bf16 v[164:167],a[76:79],a[236:239],v[164:167]
v_add_u32 v12,s66,v12
v_subrev_u32 v26,2048,v12
v_mfma_f32_16x16x32_bf16 v[164:167],a[72:75],a[232:235],v[164:167]
v_add_u32 v11,192,v23
v_mul_u32_u24 v12,3277,v11
v_mfma_f32_16x16x32_bf16 v[196:199],a[72:75],a[240:243],v[196:199]
v_lshrrev_b32 v12,16,v12
v_mul_u32_u24 v13,20,v12
v_mfma_f32_16x16x32_bf16 v[196:199],a[76:79],a[244:247],v[196:199]
s_add_u32 m0,s88,5120
v_sub_u32 v13,v11,v13
v_bfe_u32 v14,v12,2,1
v_mfma_f32_16x16x32_bf16 v[228:231],a[76:79],a[252:255],v[228:231]
v_lshlrev_b32 v14,1,v14
v_xor_b32 v13,v13,v14
v_mfma_f32_16x16x32_bf16 v[228:231],a[72:75],a[248:251],v[228:231]
buffer_load_dwordx4 v45,s[36:39],s83 offen lds
v_mfma_f32_16x16x32_bf16 v[136:139],a[80:83],a[224:227],v[136:139]
v_lshrrev_b32 v14,2,v13
v_cmp_eq_u32 vcc,3,v14
v_mfma_f32_16x16x32_bf16 v[136:139],a[84:87],a[228:231],v[136:139]
v_min_u32 v15,3,v14
v_add_u32 v15,s65,v15
v_mfma_f32_16x16x32_bf16 v[168:171],a[84:87],a[236:239],v[168:171]
v_and_b32 v15,3,v15
v_cndmask_b32_e64 v15,v15,4,vcc
v_mfma_f32_16x16x32_bf16 v[168:171],a[80:83],a[232:235],v[168:171]
s_add_u32 m0,s88,6144
v_and_b32 v13,3,v13
v_lshl_add_u32 v13,v15,2,v13
v_mfma_f32_16x16x32_bf16 v[200:203],a[80:83],a[240:243],v[200:203]
v_mul_u32_u24 v12,1280,v12
v_lshl_add_u32 v12,v13,4,v12
v_mfma_f32_16x16x32_bf16 v[200:203],a[84:87],a[244:247],v[200:203]
buffer_load_dwordx4 v46,s[36:39],s83 offen lds
v_mfma_f32_16x16x32_bf16 v[232:235],a[84:87],a[252:255],v[232:235]
v_add_u32 v12,s66,v12
v_subrev_u32 v27,3072,v12
v_mfma_f32_16x16x32_bf16 v[232:235],a[80:83],a[248:251],v[232:235]
v_add_u32 v11,256,v23
v_mul_u32_u24 v12,3277,v11
)A"
R"A(v_mfma_f32_16x16x32_bf16 v[140:143],a[88:91],a[224:227],v[140:143]
v_lshrrev_b32 v12,16,v12
v_mul_u32_u24 v13,20,v12
v_mfma_f32_16x16x32_bf16 v[140:143],a[92:95],a[228:231],v[140:143]
s_add_u32 m0,s88,7168
v_sub_u32 v13,v11,v13
v_bfe_u32 v14,v12,2,1
v_mfma_f32_16x16x32_bf16 v[172:175],a[92:95],a[236:239],v[172:175]
s_mov_b32 s88,s69
v_lshlrev_b32 v14,1,v14
v_xor_b32 v13,v13,v14
v_mfma_f32_16x16x32_bf16 v[172:175],a[88:91],a[232:235],v[172:175]
buffer_load_dwordx4 v47,s[36:39],s83 offen lds
v_mfma_f32_16x16x32_bf16 v[204:207],a[88:91],a[240:243],v[204:207]
s_waitcnt lgkmcnt(0)
s_add_u32 s83,s83,524288
s_sub_u32 s92,s92,s8
v_mfma_f32_16x16x32_bf16 v[204:207],a[92:95],a[244:247],v[204:207]
s_sub_u32 s56,s56,s4
s_mov_b32 s93,782
s_mul_i32 s92,s92,24
v_mfma_f32_16x16x32_bf16 v[236:239],a[92:95],a[252:255],v[236:239]
s_cmp_ge_u32 s63,2
s_cselect_b32 s93,823,s93
s_mul_i32 s93,s93,24
v_mfma_f32_16x16x32_bf16 v[236:239],a[88:91],a[248:251],v[236:239]
s_add_u32 s10,s20,s74
s_addc_u32 s11,s21,0
s_add_u32 s93,s93,s56
s_waitcnt vmcnt(25)
s_barrier
v_mfma_f32_16x16x32_bf16 v[144:147],a[96:99],a[224:227],v[144:147]
s_add_u32 m0,s76,0
s_add_u32 s92,s92,12600
s_waitcnt vmcnt(17)
ds_read_b128 a[192:195],v48
v_mfma_f32_16x16x32_bf16 v[144:147],a[100:103],a[228:231],v[144:147]
s_and_b32 s69,s75,0xfffc0000
ds_read_b128 a[196:199],v49
v_lshrrev_b32 v14,2,v13
v_mfma_f32_16x16x32_bf16 v[176:179],a[100:103],a[236:239],v[176:179]
global_load_lds_dwordx4 v4,s[10:11] nt
ds_read_b128 a[200:203],v48 offset:2048
v_mfma_f32_16x16x32_bf16 v[176:179],a[96:99],a[232:235],v[176:179]
s_add_u32 s75,s75,16384
ds_read_b128 a[204:207],v49 offset:2048
v_cmp_eq_u32 vcc,3,v14
v_mfma_f32_16x16x32_bf16 v[208:211],a[96:99],a[240:243],v[208:211]
v_min_u32 v15,3,v14
v_add_u32 v15,s65,v15
v_mfma_f32_16x16x32_bf16 v[208:211],a[100:103],a[244:247],v[208:211]
s_mov_b32 s65,262144
ds_read_b128 a[208:211],v48 offset:4096
v_and_b32 v15,3,v15
v_mfma_f32_16x16x32_bf16 v[240:243],a[100:103],a[252:255],v[240:243]
ds_read_b128 a[212:215],v49 offset:4096
v_cndmask_b32_e64 v15,v15,4,vcc
v_mfma_f32_16x16x32_bf16 v[240:243],a[96:99],a[248:251],v[240:243]
ds_read_b128 a[216:219],v48 offset:6144
v_xor_b32 v48,98304,v48
v_mfma_f32_16x16x32_bf16 v[148:151],a[104:107],a[224:227],v[148:151]
v_and_b32 v13,3,v13
v_lshl_add_u32 v13,v15,2,v13
v_mfma_f32_16x16x32_bf16 v[148:151],a[108:111],a[228:231],v[148:151]
ds_read_b128 a[220:223],v49 offset:6144
v_xor_b32 v49,98304,v49
v_mfma_f32_16x16x32_bf16 v[180:183],a[108:111],a[236:239],v[180:183]
ds_read_b64_tr_b16 a[64:65],v50
v_mul_u32_u24 v12,1280,v12
v_mfma_f32_16x16x32_bf16 v[180:183],a[104:107],a[232:235],v[180:183]
ds_read_b64_tr_b16 a[66:67],v50 offset:2048
v_lshl_add_u32 v12,v13,4,v12
v_mfma_f32_16x16x32_bf16 v[212:215],a[104:107],a[240:243],v[212:215]
v_add_u32 v12,s66,v12
v_subrev_u32 v7,0,v12
v_mfma_f32_16x16x32_bf16 v[212:215],a[108:111],a[244:247],v[212:215]
ds_read_b64_tr_b16 a[68:69],v50 offset:16384
v_mfma_f32_16x16x32_bf16 v[244:247],a[108:111],a[252:255],v[244:247]
ds_read_b64_tr_b16 a[70:71],v50 offset:18432
v_mfma_f32_16x16x32_bf16 v[244:247],a[104:107],a[248:251],v[244:247]
ds_read_b64_tr_b16 a[72:73],v50 offset:8
v_mfma_f32_16x16x32_bf16 v[152:155],a[112:115],a[224:227],v[152:155]
v_mfma_f32_16x16x32_bf16 v[152:155],a[116:119],a[228:231],v[152:155]
ds_read_b64_tr_b16 a[74:75],v50 offset:2056
v_mfma_f32_16x16x32_bf16 v[184:187],a[116:119],a[236:239],v[184:187]
global_load_lds_dwordx4 v4,s[10:11] offset:1024 nt
ds_read_b64_tr_b16 a[76:77],v50 offset:16392
v_mfma_f32_16x16x32_bf16 v[184:187],a[112:115],a[232:235],v[184:187]
ds_read_b64_tr_b16 a[78:79],v50 offset:18440
v_mfma_f32_16x16x32_bf16 v[216:219],a[112:115],a[240:243],v[216:219]
v_mfma_f32_16x16x32_bf16 v[216:219],a[116:119],a[244:247],v[216:219]
ds_read_b64_tr_b16 a[80:81],v50 offset:1024
v_mfma_f32_16x16x32_bf16 v[248:251],a[116:119],a[252:255],v[248:251]
ds_read_b64_tr_b16 a[82:83],v50 offset:3072
v_mfma_f32_16x16x32_bf16 v[248:251],a[112:115],a[248:251],v[248:251]
ds_read_b64_tr_b16 a[84:85],v50 offset:17408
v_mfma_f32_16x16x32_bf16 v[156:159],a[120:123],a[224:227],v[156:159]
v_mfma_f32_16x16x32_bf16 v[156:159],a[124:127],a[228:231],v[156:159]
ds_read_b64_tr_b16 a[86:87],v50 offset:19456
v_mfma_f32_16x16x32_bf16 v[188:191],a[124:127],a[236:239],v[188:191]
ds_read_b64_tr_b16 a[88:89],v50 offset:1032
v_mfma_f32_16x16x32_bf16 v[188:191],a[120:123],a[232:235],v[188:191]
s_cmp_lt_u32 s92,s93
s_cselect_b32 s5,6,0
s_cselect_b32 s7,3,0
s_cselect_b32 s9,8,10
ds_read_b64_tr_b16 a[90:91],v50 offset:3080
v_mfma_f32_16x16x32_bf16 v[220:223],a[120:123],a[240:243],v[220:223]
s_sub_u32 s93,s93,s92
s_max_i32 s93,s93,0
s_min_u32 s93,s93,65535
v_mfma_f32_16x16x32_bf16 v[220:223],a[124:127],a[244:247],v[220:223]
s_mul_i32 s93,s93,195
s_lshr_b32 s93,s93,16
ds_read_b64_tr_b16 a[92:93],v50 offset:17416
v_mfma_f32_16x16x32_bf16 v[252:255],a[124:127],a[252:255],v[252:255]
s_add_u32 s93,s93,1
s_max_i32 s93,s93,1
ds_read_b64_tr_b16 a[94:95],v50 offset:19464
v_mfma_f32_16x16x32_bf16 v[252:255],a[120:123],a[248:251],v[252:255]
s_min_u32 s93,s93,6
s_mov_b32 s4,s5
s_waitcnt vmcnt(10)
ds_read_b128 a[224:227],v48
s_waitcnt lgkmcnt(6)
v_mfma_f32_16x16x32_bf16 v[128:131],a[64:67],a[192:195],v[128:131]
s_cmp_lg_u32 s7,0
s_cselect_b32 s7,s93,0
v_mfma_f32_16x16x32_bf16 v[128:131],a[68:71],a[196:199],v[128:131]
ds_read_b128 a[228:231],v49
v_mfma_f32_16x16x32_bf16 v[160:163],a[68:71],a[204:207],v[160:163]
global_load_lds_dwordx4 v5,s[10:11] offset:2048 nt
ds_read_b128 a[236:239],v49 offset:2048
v_mfma_f32_16x16x32_bf16 v[160:163],a[64:67],a[200:203],v[160:163]
ds_read_b128 a[232:235],v48 offset:2048
v_mfma_f32_16x16x32_bf16 v[192:195],a[64:67],a[208:211],v[192:195]
v_mfma_f32_16x16x32_bf16 v[192:195],a[68:71],a[212:215],v[192:195]
ds_read_b128 a[240:243],v48 offset:4096
v_mfma_f32_16x16x32_bf16 v[224:227],a[68:71],a[220:223],v[224:227]
ds_read_b128 a[244:247],v49 offset:4096
v_mfma_f32_16x16x32_bf16 v[224:227],a[64:67],a[216:219],v[224:227]
ds_read_b128 a[252:255],v49 offset:6144
v_mfma_f32_16x16x32_bf16 v[132:135],a[72:75],a[192:195],v[132:135]
v_mfma_f32_16x16x32_bf16 v[132:135],a[76:79],a[196:199],v[132:135]
ds_read_b128 a[248:251],v48 offset:6144
v_mfma_f32_16x16x32_bf16 v[164:167],a[76:79],a[204:207],v[164:167]
ds_read_b64_tr_b16 a[96:97],v50 offset:256
v_mfma_f32_16x16x32_bf16 v[164:167],a[72:75],a[200:203],v[164:167]
ds_read_b64_tr_b16 a[98:99],v50 offset:2304
v_mfma_f32_16x16x32_bf16 v[196:199],a[72:75],a[208:211],v[196:199]
v_mfma_f32_16x16x32_bf16 v[196:199],a[76:79],a[212:215],v[196:199]
ds_read_b64_tr_b16 a[100:101],v50 offset:16640
v_mfma_f32_16x16x32_bf16 v[228:231],a[76:79],a[220:223],v[228:231]
ds_read_b64_tr_b16 a[102:103],v50 offset:18688
v_mfma_f32_16x16x32_bf16 v[228:231],a[72:75],a[216:219],v[228:231]
s_waitcnt lgkmcnt(5)
ds_read_b64_tr_b16 a[104:105],v50 offset:1280
v_mfma_f32_16x16x32_bf16 v[128:131],a[80:83],a[224:227],v[128:131]
v_mfma_f32_16x16x32_bf16 v[128:131],a[84:87],a[228:231],v[128:131]
ds_read_b64_tr_b16 a[106:107],v50 offset:3328
v_mfma_f32_16x16x32_bf16 v[160:163],a[84:87],a[236:239],v[160:163]
global_load_lds_dwordx4 v5,s[10:11] offset:3072
ds_read_b64_tr_b16 a[108:109],v50 offset:17664
v_mfma_f32_16x16x32_bf16 v[160:163],a[80:83],a[232:235],v[160:163]
ds_read_b64_tr_b16 a[110:111],v50 offset:19712
v_mfma_f32_16x16x32_bf16 v[192:195],a[80:83],a[240:243],v[192:195]
v_mfma_f32_16x16x32_bf16 v[192:195],a[84:87],a[244:247],v[192:195]
ds_read_b64_tr_b16 a[112:113],v50 offset:264
v_mul_f32 v128,s71,v128
v_mfma_f32_16x16x32_bf16 v[224:227],a[84:87],a[252:255],v[224:227]
s_waitcnt lgkmcnt(5)
ds_read_b64_tr_b16 a[114:115],v50 offset:2312
v_mul_f32 v129,s71,v129
v_mfma_f32_16x16x32_bf16 v[224:227],a[80:83],a[248:251],v[224:227]
ds_read_b64_tr_b16 a[116:117],v50 offset:16648
v_mul_f32 v130,s71,v130
v_mfma_f32_16x16x32_bf16 v[132:135],a[88:91],a[224:227],v[132:135]
v_mul_f32 v131,s71,v131
v_cvt_pk_bf16_f32 v128,v128,v129
v_mfma_f32_16x16x32_bf16 v[132:135],a[92:95],a[228:231],v[132:135]
ds_read_b64_tr_b16 a[118:119],v50 offset:18696
v_cvt_pk_bf16_f32 v129,v130,v131
v_mfma_f32_16x16x32_bf16 v[164:167],a[92:95],a[236:239],v[164:167]
ds_read_b64_tr_b16 a[120:121],v50 offset:1288
v_mul_f32 v192,s71,v192
v_mfma_f32_16x16x32_bf16 v[164:167],a[88:91],a[232:235],v[164:167]
ds_read_b64_tr_b16 a[122:123],v50 offset:3336
v_mul_f32 v193,s71,v193
v_mfma_f32_16x16x32_bf16 v[196:199],a[88:91],a[240:243],v[196:199]
v_mul_f32 v132,s71,v132
v_mul_f32 v133,s71,v133
v_mfma_f32_16x16x32_bf16 v[196:199],a[92:95],a[244:247],v[196:199]
ds_read_b64_tr_b16 a[124:125],v50 offset:17672
v_mul_f32 v134,s71,v134
v_mfma_f32_16x16x32_bf16 v[228:231],a[92:95],a[252:255],v[228:231]
s_add_u32 s10,s24,s74
s_addc_u32 s11,s25,0
ds_read_b64_tr_b16 a[126:127],v50 offset:19720
v_mfma_f32_16x16x32_bf16 v[228:231],a[88:91],a[248:251],v[228:231]
s_add_u32 s74,s74,16384
s_and_b32 s74,s74,0x3ffff
ds_read_b64_tr_b16 a[128:129],v50 offset:512
v_mfma_f32_16x16x32_bf16 v[136:139],a[96:99],a[192:195],v[136:139]
s_add_u32 m0,s76,16384
s_or_b32 s74,s74,s69
s_sub_u32 s76,s76,32768
v_mfma_f32_16x16x32_bf16 v[136:139],a[100:103],a[196:199],v[136:139]
s_and_b32 s76,s76,0x1ffff
s_and_b32 s69,s75,0xfffc0000
ds_read_b64_tr_b16 a[130:131],v50 offset:2560
s_waitcnt lgkmcnt(6)
v_mfma_f32_16x16x32_bf16 v[136:139],a[104:107],a[224:227],v[136:139]
global_load_lds_dwordx4 v4,s[10:11]
ds_read_b64_tr_b16 a[132:133],v50 offset:16896
v_mfma_f32_16x16x32_bf16 v[136:139],a[108:111],a[228:231],v[136:139]
s_add_u32 s75,s75,16384
ds_read_b64_tr_b16 a[134:135],v50 offset:18944
v_mul_f32 v135,s71,v135
v_mfma_f32_16x16x32_bf16 v[168:171],a[108:111],a[236:239],v[168:171]
v_cvt_pk_bf16_f32 v130,v132,v133
v_cvt_pk_bf16_f32 v131,v134,v135
v_mfma_f32_16x16x32_bf16 v[168:171],a[104:107],a[232:235],v[168:171]
ds_read_b64_tr_b16 a[136:137],v50 offset:1536
v_mul_f32 v194,s71,v194
v_mfma_f32_16x16x32_bf16 v[168:171],a[100:103],a[204:207],v[168:171]
ds_read_b64_tr_b16 a[138:139],v50 offset:3584
v_mul_f32 v195,s71,v195
v_mfma_f32_16x16x32_bf16 v[168:171],a[96:99],a[200:203],v[168:171]
ds_read_b64_tr_b16 a[140:141],v50 offset:17920
v_mul_f32 v196,s71,v196
v_mfma_f32_16x16x32_bf16 v[200:203],a[96:99],a[208:211],v[200:203]
v_mul_f32 v197,s71,v197
v_mul_f32 v198,s71,v198
v_mfma_f32_16x16x32_bf16 v[200:203],a[100:103],a[212:215],v[200:203]
ds_read_b64_tr_b16 a[142:143],v50 offset:19968
v_mul_f32 v199,s71,v199
v_mfma_f32_16x16x32_bf16 v[200:203],a[104:107],a[240:243],v[200:203]
ds_read_b64_tr_b16 a[144:145],v50 offset:520
v_cvt_pk_bf16_f32 v192,v192,v193
v_mfma_f32_16x16x32_bf16 v[200:203],a[108:111],a[244:247],v[200:203]
ds_read_b64_tr_b16 a[146:147],v50 offset:2568
v_cvt_pk_bf16_f32 v193,v194,v195
v_mfma_f32_16x16x32_bf16 v[232:235],a[108:111],a[252:255],v[232:235]
v_cvt_pk_bf16_f32 v194,v196,v197
v_cvt_pk_bf16_f32 v195,v198,v199
v_mfma_f32_16x16x32_bf16 v[232:235],a[104:107],a[248:251],v[232:235]
ds_read_b64_tr_b16 a[148:149],v50 offset:16904
v_mul_f32 v136,s71,v136
v_mfma_f32_16x16x32_bf16 v[232:235],a[100:103],a[220:223],v[232:235]
ds_read_b64_tr_b16 a[150:151],v50 offset:18952
v_mul_f32 v137,s71,v137
v_mfma_f32_16x16x32_bf16 v[232:235],a[96:99],a[216:219],v[232:235]
ds_read_b64_tr_b16 a[152:153],v50 offset:1544
v_mul_f32 v138,s71,v138
v_mfma_f32_16x16x32_bf16 v[140:143],a[112:115],a[192:195],v[140:143]
v_mul_f32 v139,s71,v139
v_cvt_pk_bf16_f32 v136,v136,v137
v_mfma_f32_16x16x32_bf16 v[140:143],a[116:119],a[196:199],v[140:143]
s_waitcnt lgkmcnt(5)
ds_read_b64_tr_b16 a[154:155],v50 offset:3592
v_cvt_pk_bf16_f32 v137,v138,v139
v_mfma_f32_16x16x32_bf16 v[140:143],a[120:123],a[224:227],v[140:143]
global_load_lds_dwordx4 v5,s[10:11] offset:1024
ds_read_b64_tr_b16 a[156:157],v50 offset:17928
v_mfma_f32_16x16x32_bf16 v[140:143],a[124:127],a[228:231],v[140:143]
ds_read_b64_tr_b16 a[158:159],v50 offset:19976
v_mul_f32 v160,s71,v160
v_mfma_f32_16x16x32_bf16 v[172:175],a[124:127],a[236:239],v[172:175]
v_mul_f32 v161,s71,v161
v_mul_f32 v162,s71,v162
v_mfma_f32_16x16x32_bf16 v[172:175],a[120:123],a[232:235],v[172:175]
ds_read_b64_tr_b16 a[160:161],v50 offset:768
)A"
R"A(v_mul_f32 v163,s71,v163
v_mfma_f32_16x16x32_bf16 v[172:175],a[116:119],a[204:207],v[172:175]
ds_read_b64_tr_b16 a[162:163],v50 offset:2816
v_mul_f32 v140,s71,v140
v_mfma_f32_16x16x32_bf16 v[172:175],a[112:115],a[200:203],v[172:175]
ds_read_b64_tr_b16 a[164:165],v50 offset:17152
v_mul_f32 v141,s71,v141
v_mfma_f32_16x16x32_bf16 v[204:207],a[112:115],a[208:211],v[204:207]
v_mul_f32 v142,s71,v142
v_mul_f32 v143,s71,v143
v_mfma_f32_16x16x32_bf16 v[204:207],a[116:119],a[212:215],v[204:207]
ds_read_b64_tr_b16 a[166:167],v50 offset:19200
v_cvt_pk_bf16_f32 v138,v140,v141
v_mfma_f32_16x16x32_bf16 v[204:207],a[120:123],a[240:243],v[204:207]
ds_read_b64_tr_b16 a[168:169],v50 offset:1792
v_cvt_pk_bf16_f32 v139,v142,v143
v_mfma_f32_16x16x32_bf16 v[204:207],a[124:127],a[244:247],v[204:207]
ds_read_b64_tr_b16 a[170:171],v50 offset:3840
v_mul_f32 v164,s71,v164
v_mfma_f32_16x16x32_bf16 v[236:239],a[124:127],a[252:255],v[236:239]
v_mul_f32 v165,s71,v165
v_mul_f32 v166,s71,v166
v_mfma_f32_16x16x32_bf16 v[236:239],a[120:123],a[248:251],v[236:239]
ds_read_b64_tr_b16 a[172:173],v50 offset:18176
v_mul_f32 v167,s71,v167
v_mfma_f32_16x16x32_bf16 v[236:239],a[116:119],a[220:223],v[236:239]
ds_read_b64_tr_b16 a[174:175],v50 offset:20224
v_cvt_pk_bf16_f32 v160,v160,v161
v_mfma_f32_16x16x32_bf16 v[236:239],a[112:115],a[216:219],v[236:239]
ds_read_b64_tr_b16 a[176:177],v50 offset:776
v_cvt_pk_bf16_f32 v161,v162,v163
v_mfma_f32_16x16x32_bf16 v[144:147],a[128:131],a[192:195],v[144:147]
v_cvt_pk_bf16_f32 v162,v164,v165
v_cvt_pk_bf16_f32 v163,v166,v167
v_mfma_f32_16x16x32_bf16 v[144:147],a[132:135],a[196:199],v[144:147]
ds_read_b64_tr_b16 a[178:179],v50 offset:2824
v_mul_f32 v224,s71,v224
v_mfma_f32_16x16x32_bf16 v[144:147],a[136:139],a[224:227],v[144:147]
global_load_lds_dwordx4 v4,s[10:11] offset:2048
ds_read_b64_tr_b16 a[180:181],v50 offset:17160
v_mfma_f32_16x16x32_bf16 v[144:147],a[140:143],a[228:231],v[144:147]
ds_read_b64_tr_b16 a[182:183],v50 offset:19208
v_mul_f32 v225,s71,v225
v_mfma_f32_16x16x32_bf16 v[176:179],a[140:143],a[236:239],v[176:179]
v_mul_f32 v226,s71,v226
v_mul_f32 v227,s71,v227
v_mfma_f32_16x16x32_bf16 v[176:179],a[136:139],a[232:235],v[176:179]
ds_read_b64_tr_b16 a[184:185],v50 offset:1800
v_mul_f32 v228,s71,v228
v_mfma_f32_16x16x32_bf16 v[176:179],a[132:135],a[204:207],v[176:179]
ds_read_b64_tr_b16 a[186:187],v50 offset:3848
v_mul_f32 v144,s71,v144
v_mfma_f32_16x16x32_bf16 v[176:179],a[128:131],a[200:203],v[176:179]
ds_read_b64_tr_b16 a[188:189],v50 offset:18184
v_mul_f32 v145,s71,v145
v_mfma_f32_16x16x32_bf16 v[208:211],a[128:131],a[208:211],v[208:211]
v_mul_f32 v146,s71,v146
v_mul_f32 v147,s71,v147
v_mfma_f32_16x16x32_bf16 v[208:211],a[132:135],a[212:215],v[208:211]
ds_read_b64_tr_b16 a[190:191],v50 offset:20232
v_cvt_pk_bf16_f32 v144,v144,v145
v_mfma_f32_16x16x32_bf16 v[208:211],a[136:139],a[240:243],v[208:211]
v_cvt_pk_bf16_f32 v145,v146,v147
v_mul_f32 v229,s71,v229
v_mfma_f32_16x16x32_bf16 v[208:211],a[140:143],a[244:247],v[208:211]
v_mul_f32 v230,s71,v230
v_mul_f32 v231,s71,v231
v_mfma_f32_16x16x32_bf16 v[240:243],a[140:143],a[252:255],v[240:243]
v_cvt_pk_bf16_f32 v224,v224,v225
v_cvt_pk_bf16_f32 v225,v226,v227
v_mfma_f32_16x16x32_bf16 v[240:243],a[136:139],a[248:251],v[240:243]
v_cvt_pk_bf16_f32 v226,v228,v229
v_cvt_pk_bf16_f32 v227,v230,v231
v_mfma_f32_16x16x32_bf16 v[240:243],a[132:135],a[220:223],v[240:243]
v_mul_f32 v168,s71,v168
v_mul_f32 v169,s71,v169
v_mfma_f32_16x16x32_bf16 v[240:243],a[128:131],a[216:219],v[240:243]
v_mul_f32 v170,s71,v170
L_pd3_%=:
s_cbranch_execz L_pd7_%=
v_mul_f32 v171,s71,v171
s_waitcnt vmcnt(7)
s_barrier
s_waitcnt lgkmcnt(1)
v_mfma_f32_16x16x32_bf16 v[148:151],a[144:147],a[192:195],v[148:151]
ds_read_b64_tr_b16 a[64:65],v52
v_mul_f32 v172,s71,v172
v_mfma_f32_16x16x32_bf16 v[148:151],a[148:151],a[196:199],v[148:151]
global_load_lds_dwordx4 v5,s[10:11] offset:3072
ds_read_b64_tr_b16 a[66:67],v52 offset:2048
v_mfma_f32_16x16x32_bf16 v[148:151],a[152:155],a[224:227],v[148:151]
ds_read_b64_tr_b16 a[68:69],v52 offset:16384
v_mul_f32 v173,s71,v173
v_mfma_f32_16x16x32_bf16 v[148:151],a[156:159],a[228:231],v[148:151]
ds_read_b64_tr_b16 a[70:71],v52 offset:18432
v_mul_f32 v174,s71,v174
v_mfma_f32_16x16x32_bf16 v[180:183],a[156:159],a[236:239],v[180:183]
v_mul_f32 v175,s71,v175
v_cvt_pk_bf16_f32 v168,v168,v169
v_mfma_f32_16x16x32_bf16 v[180:183],a[152:155],a[232:235],v[180:183]
ds_read_b64_tr_b16 a[72:73],v52 offset:1024
v_cvt_pk_bf16_f32 v169,v170,v171
v_mfma_f32_16x16x32_bf16 v[180:183],a[148:151],a[204:207],v[180:183]
ds_read_b64_tr_b16 a[74:75],v52 offset:3072
v_mul_f32 v148,s71,v148
v_mfma_f32_16x16x32_bf16 v[180:183],a[144:147],a[200:203],v[180:183]
ds_read_b64_tr_b16 a[76:77],v52 offset:17408
v_mul_f32 v149,s71,v149
v_mfma_f32_16x16x32_bf16 v[212:215],a[144:147],a[208:211],v[212:215]
v_mul_f32 v150,s71,v150
v_mul_f32 v151,s71,v151
v_mfma_f32_16x16x32_bf16 v[212:215],a[148:151],a[212:215],v[212:215]
ds_read_b64_tr_b16 a[78:79],v52 offset:19456
v_cvt_pk_bf16_f32 v146,v148,v149
v_mfma_f32_16x16x32_bf16 v[212:215],a[152:155],a[240:243],v[212:215]
ds_read_b64_tr_b16 a[80:81],v52 offset:8
v_cvt_pk_bf16_f32 v147,v150,v151
v_mfma_f32_16x16x32_bf16 v[212:215],a[156:159],a[244:247],v[212:215]
ds_read_b64_tr_b16 a[82:83],v52 offset:2056
v_cvt_pk_bf16_f32 v170,v172,v173
v_mfma_f32_16x16x32_bf16 v[244:247],a[156:159],a[252:255],v[244:247]
v_cvt_pk_bf16_f32 v171,v174,v175
v_mul_f32 v176,s71,v176
v_mfma_f32_16x16x32_bf16 v[244:247],a[152:155],a[248:251],v[244:247]
ds_read_b64_tr_b16 a[84:85],v52 offset:16392
v_mul_f32 v177,s71,v177
v_mfma_f32_16x16x32_bf16 v[244:247],a[148:151],a[220:223],v[244:247]
s_add_u32 s10,s20,s74
s_addc_u32 s11,s21,0
ds_read_b64_tr_b16 a[86:87],v52 offset:18440
v_mfma_f32_16x16x32_bf16 v[244:247],a[144:147],a[216:219],v[244:247]
ds_read_b64_tr_b16 a[88:89],v52 offset:1032
v_mul_f32 v178,s71,v178
v_mfma_f32_16x16x32_bf16 v[152:155],a[160:163],a[192:195],v[152:155]
s_add_u32 m0,s76,0
v_mul_f32 v179,s71,v179
v_mul_f32 v180,s71,v180
v_mfma_f32_16x16x32_bf16 v[152:155],a[164:167],a[196:199],v[152:155]
ds_read_b64_tr_b16 a[90:91],v52 offset:3080
v_mul_f32 v181,s71,v181
v_mfma_f32_16x16x32_bf16 v[152:155],a[168:171],a[224:227],v[152:155]
global_load_lds_dwordx4 v4,s[10:11] nt
ds_read_b64_tr_b16 a[92:93],v52 offset:17416
v_mfma_f32_16x16x32_bf16 v[152:155],a[172:175],a[228:231],v[152:155]
ds_read_b64_tr_b16 a[94:95],v52 offset:19464
v_mul_f32 v182,s71,v182
v_mfma_f32_16x16x32_bf16 v[184:187],a[172:175],a[236:239],v[184:187]
v_mul_f32 v183,s71,v183
v_cvt_pk_bf16_f32 v176,v176,v177
v_mfma_f32_16x16x32_bf16 v[184:187],a[168:171],a[232:235],v[184:187]
ds_read_b64_tr_b16 a[96:97],v52 offset:256
v_cvt_pk_bf16_f32 v177,v178,v179
v_mfma_f32_16x16x32_bf16 v[184:187],a[164:167],a[204:207],v[184:187]
ds_read_b64_tr_b16 a[98:99],v52 offset:2304
v_mul_f32 v152,s71,v152
v_mfma_f32_16x16x32_bf16 v[184:187],a[160:163],a[200:203],v[184:187]
ds_read_b64_tr_b16 a[100:101],v52 offset:16640
v_mul_f32 v153,s71,v153
v_mfma_f32_16x16x32_bf16 v[216:219],a[160:163],a[208:211],v[216:219]
v_mul_f32 v154,s71,v154
v_mul_f32 v155,s71,v155
v_mfma_f32_16x16x32_bf16 v[216:219],a[164:167],a[212:215],v[216:219]
ds_read_b64_tr_b16 a[102:103],v52 offset:18688
v_cvt_pk_bf16_f32 v152,v152,v153
v_mfma_f32_16x16x32_bf16 v[216:219],a[168:171],a[240:243],v[216:219]
ds_read_b64_tr_b16 a[104:105],v52 offset:1280
v_cvt_pk_bf16_f32 v153,v154,v155
v_mfma_f32_16x16x32_bf16 v[216:219],a[172:175],a[244:247],v[216:219]
ds_read_b64_tr_b16 a[106:107],v52 offset:3328
v_cvt_pk_bf16_f32 v178,v180,v181
v_mfma_f32_16x16x32_bf16 v[248:251],a[172:175],a[252:255],v[248:251]
v_cvt_pk_bf16_f32 v179,v182,v183
v_mul_f32 v184,s71,v184
v_mfma_f32_16x16x32_bf16 v[248:251],a[168:171],a[248:251],v[248:251]
ds_read_b64_tr_b16 a[108:109],v52 offset:17664
v_mul_f32 v185,s71,v185
v_mfma_f32_16x16x32_bf16 v[248:251],a[164:167],a[220:223],v[248:251]
ds_read_b64_tr_b16 a[110:111],v52 offset:19712
v_mul_f32 v186,s71,v186
v_mfma_f32_16x16x32_bf16 v[248:251],a[160:163],a[216:219],v[248:251]
ds_read_b64_tr_b16 a[112:113],v52 offset:264
v_mul_f32 v187,s71,v187
v_mfma_f32_16x16x32_bf16 v[156:159],a[176:179],a[192:195],v[156:159]
v_cvt_pk_bf16_f32 v184,v184,v185
v_cvt_pk_bf16_f32 v185,v186,v187
v_mfma_f32_16x16x32_bf16 v[156:159],a[180:183],a[196:199],v[156:159]
global_load_lds_dwordx4 v4,s[10:11] offset:1024 nt
ds_read_b64_tr_b16 a[114:115],v52 offset:2312
v_mfma_f32_16x16x32_bf16 v[156:159],a[184:187],a[224:227],v[156:159]
s_waitcnt lgkmcnt(5)
ds_read_b64_tr_b16 a[116:117],v52 offset:16648
v_mul_f32 v200,s71,v200
v_mfma_f32_16x16x32_bf16 v[156:159],a[188:191],a[228:231],v[156:159]
ds_read_b64_tr_b16 a[118:119],v52 offset:18696
v_mul_f32 v201,s71,v201
v_mfma_f32_16x16x32_bf16 v[188:191],a[188:191],a[236:239],v[188:191]
v_mul_f32 v202,s71,v202
v_mul_f32 v203,s71,v203
v_mfma_f32_16x16x32_bf16 v[188:191],a[184:187],a[232:235],v[188:191]
ds_read_b64_tr_b16 a[120:121],v52 offset:1288
v_mul_f32 v204,s71,v204
v_mfma_f32_16x16x32_bf16 v[188:191],a[180:183],a[204:207],v[188:191]
ds_read_b64_tr_b16 a[122:123],v52 offset:3336
v_mul_f32 v156,s71,v156
v_mfma_f32_16x16x32_bf16 v[188:191],a[176:179],a[200:203],v[188:191]
ds_read_b64_tr_b16 a[124:125],v52 offset:17672
v_mul_f32 v157,s71,v157
v_mfma_f32_16x16x32_bf16 v[220:223],a[176:179],a[208:211],v[220:223]
v_mul_f32 v158,s71,v158
v_mul_f32 v159,s71,v159
v_mfma_f32_16x16x32_bf16 v[220:223],a[180:183],a[212:215],v[220:223]
ds_read_b64_tr_b16 a[126:127],v52 offset:19720
v_cvt_pk_bf16_f32 v154,v156,v157
v_mfma_f32_16x16x32_bf16 v[220:223],a[184:187],a[240:243],v[220:223]
v_cvt_pk_bf16_f32 v155,v158,v159
v_mul_f32 v188,s71,v188
v_mfma_f32_16x16x32_bf16 v[220:223],a[188:191],a[244:247],v[220:223]
v_mul_f32 v189,s71,v189
v_mul_f32 v190,s71,v190
v_mfma_f32_16x16x32_bf16 v[252:255],a[188:191],a[252:255],v[252:255]
v_mul_f32 v191,s71,v191
v_cvt_pk_bf16_f32 v186,v188,v189
v_mfma_f32_16x16x32_bf16 v[252:255],a[184:187],a[248:251],v[252:255]
v_cvt_pk_bf16_f32 v187,v190,v191
v_mul_f32 v205,s71,v205
v_mfma_f32_16x16x32_bf16 v[252:255],a[180:183],a[220:223],v[252:255]
v_mul_f32 v206,s71,v206
v_mul_f32 v207,s71,v207
v_mfma_f32_16x16x32_bf16 v[252:255],a[176:179],a[216:219],v[252:255]
v_cvt_pk_bf16_f32 v200,v200,v201
v_cvt_pk_bf16_f32 v201,v202,v203
v_mfma_f32_16x16x32_bf16 a[0:3],a[64:67],a[192:195],a[0:3]
global_load_lds_dwordx4 v5,s[10:11] offset:2048 nt
v_cvt_pk_bf16_f32 v202,v204,v205
v_mfma_f32_16x16x32_bf16 a[0:3],a[68:71],a[196:199],a[0:3]
v_cvt_pk_bf16_f32 v203,v206,v207
v_mul_f32 v208,s71,v208
v_mfma_f32_16x16x32_bf16 a[0:3],a[72:75],a[224:227],a[0:3]
v_mul_f32 v209,s71,v209
v_mul_f32 v210,s71,v210
v_mfma_f32_16x16x32_bf16 a[0:3],a[76:79],a[228:231],a[0:3]
v_mul_f32 v211,s71,v211
v_mul_f32 v212,s71,v212
v_mfma_f32_16x16x32_bf16 a[16:19],a[76:79],a[236:239],a[16:19]
v_mul_f32 v213,s71,v213
v_mul_f32 v214,s71,v214
v_mfma_f32_16x16x32_bf16 a[16:19],a[72:75],a[232:235],a[16:19]
v_mul_f32 v215,s71,v215
v_cvt_pk_bf16_f32 v208,v208,v209
v_mfma_f32_16x16x32_bf16 a[16:19],a[68:71],a[204:207],a[16:19]
v_accvgpr_read_b32 v8,a0
v_accvgpr_read_b32 v9,a1
v_mfma_f32_16x16x32_bf16 a[16:19],a[64:67],a[200:203],a[16:19]
v_accvgpr_read_b32 v10,a2
v_accvgpr_read_b32 v11,a3
v_mfma_f32_16x16x32_bf16 a[32:35],a[64:67],a[208:211],a[32:35]
v_mul_f32 v8,s71,v8
v_mul_f32 v9,s71,v9
v_mfma_f32_16x16x32_bf16 a[32:35],a[68:71],a[212:215],a[32:35]
v_mul_f32 v10,s71,v10
v_mul_f32 v11,s71,v11
v_mfma_f32_16x16x32_bf16 a[32:35],a[72:75],a[240:243],a[32:35]
v_cvt_pk_bf16_f32 v132,v8,v9
v_cvt_pk_bf16_f32 v133,v10,v11
v_mfma_f32_16x16x32_bf16 a[32:35],a[76:79],a[244:247],a[32:35]
v_accvgpr_read_b32 v16,a16
v_accvgpr_read_b32 v17,a17
v_mfma_f32_16x16x32_bf16 a[48:51],a[76:79],a[252:255],a[48:51]
ds_read_b64_tr_b16 a[76:77],v52 offset:17920
v_accvgpr_read_b32 v18,a18
v_mfma_f32_16x16x32_bf16 a[48:51],a[72:75],a[248:251],a[48:51]
ds_read_b64_tr_b16 a[72:73],v52 offset:1536
v_accvgpr_read_b32 v19,a19
v_mfma_f32_16x16x32_bf16 a[48:51],a[68:71],a[220:223],a[48:51]
ds_read_b64_tr_b16 a[68:69],v52 offset:16896
v_accvgpr_read_b32 v8,a32
v_mfma_f32_16x16x32_bf16 a[48:51],a[64:67],a[216:219],a[48:51]
)A"
R"A(global_load_lds_dwordx4 v5,s[10:11] offset:3072
ds_read_b64_tr_b16 a[64:65],v52 offset:512
v_mfma_f32_16x16x32_bf16 a[4:7],a[80:83],a[192:195],a[4:7]
v_accvgpr_read_b32 v9,a33
v_accvgpr_read_b32 v10,a34
v_mfma_f32_16x16x32_bf16 a[4:7],a[84:87],a[196:199],a[4:7]
ds_read_b64_tr_b16 a[66:67],v52 offset:2560
v_accvgpr_read_b32 v11,a35
v_mfma_f32_16x16x32_bf16 a[4:7],a[88:91],a[224:227],a[4:7]
ds_read_b64_tr_b16 a[70:71],v52 offset:18944
v_mul_f32 v8,s71,v8
v_mfma_f32_16x16x32_bf16 a[4:7],a[92:95],a[228:231],a[4:7]
ds_read_b64_tr_b16 a[74:75],v52 offset:3584
v_mul_f32 v9,s71,v9
v_mfma_f32_16x16x32_bf16 a[20:23],a[92:95],a[236:239],a[20:23]
v_mul_f32 v10,s71,v10
v_mul_f32 v11,s71,v11
v_mfma_f32_16x16x32_bf16 a[20:23],a[88:91],a[232:235],a[20:23]
ds_read_b64_tr_b16 a[78:79],v52 offset:19968
v_cvt_pk_bf16_f32 v196,v8,v9
v_mfma_f32_16x16x32_bf16 a[20:23],a[84:87],a[204:207],a[20:23]
v_accvgpr_read_b32 v12,a4
v_accvgpr_read_b32 v13,a5
v_mfma_f32_16x16x32_bf16 a[20:23],a[80:83],a[200:203],a[20:23]
v_accvgpr_read_b32 v14,a6
v_accvgpr_read_b32 v15,a7
v_mfma_f32_16x16x32_bf16 a[36:39],a[80:83],a[208:211],a[36:39]
v_mul_f32 v12,s71,v12
v_mul_f32 v13,s71,v13
v_mfma_f32_16x16x32_bf16 a[36:39],a[84:87],a[212:215],a[36:39]
v_mul_f32 v14,s71,v14
v_mul_f32 v15,s71,v15
v_mfma_f32_16x16x32_bf16 a[36:39],a[88:91],a[240:243],a[36:39]
s_add_u32 s10,s24,s74
s_addc_u32 s11,s25,0
s_add_u32 s74,s74,16384
v_mfma_f32_16x16x32_bf16 a[36:39],a[92:95],a[244:247],a[36:39]
s_and_b32 s74,s74,0x3ffff
s_or_b32 s74,s74,s69
v_cvt_pk_bf16_f32 v134,v12,v13
v_mfma_f32_16x16x32_bf16 a[52:55],a[92:95],a[252:255],a[52:55]
s_add_u32 m0,s76,16384
s_sub_u32 s76,s76,32768
ds_read_b64_tr_b16 a[92:93],v52 offset:17928
v_mfma_f32_16x16x32_bf16 a[52:55],a[88:91],a[248:251],a[52:55]
s_and_b32 s76,s76,0x1ffff
ds_read_b64_tr_b16 a[88:89],v52 offset:1544
v_cvt_pk_bf16_f32 v135,v14,v15
v_mfma_f32_16x16x32_bf16 a[52:55],a[84:87],a[220:223],a[52:55]
global_load_lds_dwordx4 v4,s[10:11]
ds_read_b64_tr_b16 a[84:85],v52 offset:16904
v_mfma_f32_16x16x32_bf16 a[52:55],a[80:83],a[216:219],a[52:55]
s_and_b32 s69,s75,0xfffc0000
ds_read_b64_tr_b16 a[80:81],v52 offset:520
v_accvgpr_read_b32 v12,a36
v_mfma_f32_16x16x32_bf16 a[8:11],a[96:99],a[192:195],a[8:11]
s_add_u32 s75,s75,16384
v_accvgpr_read_b32 v13,a37
v_accvgpr_read_b32 v14,a38
v_mfma_f32_16x16x32_bf16 a[8:11],a[100:103],a[196:199],a[8:11]
s_waitcnt lgkmcnt(4)
ds_read_b64_tr_b16 a[82:83],v52 offset:2568
v_accvgpr_read_b32 v15,a39
v_mfma_f32_16x16x32_bf16 a[8:11],a[104:107],a[224:227],a[8:11]
ds_read_b64_tr_b16 a[86:87],v52 offset:18952
v_mul_f32 v12,s71,v12
v_mfma_f32_16x16x32_bf16 a[8:11],a[108:111],a[228:231],a[8:11]
ds_read_b64_tr_b16 a[90:91],v52 offset:3592
v_mul_f32 v13,s71,v13
v_mfma_f32_16x16x32_bf16 a[24:27],a[108:111],a[236:239],a[24:27]
v_mul_f32 v14,s71,v14
v_mul_f32 v15,s71,v15
v_mfma_f32_16x16x32_bf16 a[24:27],a[104:107],a[232:235],a[24:27]
ds_read_b64_tr_b16 a[94:95],v52 offset:19976
v_cvt_pk_bf16_f32 v197,v10,v11
v_mfma_f32_16x16x32_bf16 a[24:27],a[100:103],a[204:207],a[24:27]
v_cvt_pk_bf16_f32 v198,v12,v13
v_cvt_pk_bf16_f32 v199,v14,v15
v_mfma_f32_16x16x32_bf16 a[24:27],a[96:99],a[200:203],a[24:27]
v_accvgpr_read_b32 v8,a8
v_accvgpr_read_b32 v9,a9
v_mfma_f32_16x16x32_bf16 a[40:43],a[96:99],a[208:211],a[40:43]
v_accvgpr_read_b32 v10,a10
v_accvgpr_read_b32 v11,a11
v_mfma_f32_16x16x32_bf16 a[40:43],a[100:103],a[212:215],a[40:43]
v_mul_f32 v8,s71,v8
v_mul_f32 v9,s71,v9
v_mfma_f32_16x16x32_bf16 a[40:43],a[104:107],a[240:243],a[40:43]
v_mul_f32 v10,s71,v10
v_mul_f32 v11,s71,v11
v_mfma_f32_16x16x32_bf16 a[40:43],a[108:111],a[244:247],a[40:43]
v_cvt_pk_bf16_f32 v140,v8,v9
v_cvt_pk_bf16_f32 v141,v10,v11
v_mfma_f32_16x16x32_bf16 a[56:59],a[108:111],a[252:255],a[56:59]
ds_read_b64_tr_b16 a[108:109],v52 offset:18176
v_accvgpr_read_b32 v20,a20
v_mfma_f32_16x16x32_bf16 a[56:59],a[104:107],a[248:251],a[56:59]
global_load_lds_dwordx4 v5,s[10:11] offset:1024
ds_read_b64_tr_b16 a[104:105],v52 offset:1792
v_mfma_f32_16x16x32_bf16 a[56:59],a[100:103],a[220:223],a[56:59]
ds_read_b64_tr_b16 a[100:101],v52 offset:17152
v_accvgpr_read_b32 v21,a21
v_mfma_f32_16x16x32_bf16 a[56:59],a[96:99],a[216:219],a[56:59]
ds_read_b64_tr_b16 a[96:97],v52 offset:768
v_accvgpr_read_b32 v22,a22
v_mfma_f32_16x16x32_bf16 a[12:15],a[112:115],a[192:195],a[12:15]
v_accvgpr_read_b32 v23,a23
v_mul_f32 v16,s71,v16
v_mfma_f32_16x16x32_bf16 a[12:15],a[116:119],a[196:199],a[12:15]
ds_read_b64_tr_b16 a[98:99],v52 offset:2816
v_mul_f32 v17,s71,v17
v_mfma_f32_16x16x32_bf16 a[12:15],a[120:123],a[224:227],a[12:15]
ds_read_b64_tr_b16 a[102:103],v52 offset:19200
v_mul_f32 v18,s71,v18
v_mfma_f32_16x16x32_bf16 a[12:15],a[124:127],a[228:231],a[12:15]
ds_read_b64_tr_b16 a[106:107],v52 offset:3840
v_mul_f32 v19,s71,v19
v_mfma_f32_16x16x32_bf16 a[28:31],a[124:127],a[236:239],a[28:31]
v_mul_f32 v20,s71,v20
v_mul_f32 v21,s71,v21
v_mfma_f32_16x16x32_bf16 a[28:31],a[120:123],a[232:235],a[28:31]
ds_read_b64_tr_b16 a[110:111],v52 offset:20224
v_mul_f32 v22,s71,v22
v_mfma_f32_16x16x32_bf16 a[28:31],a[116:119],a[204:207],a[28:31]
v_accvgpr_read_b32 v12,a12
v_accvgpr_read_b32 v13,a13
v_mfma_f32_16x16x32_bf16 a[28:31],a[112:115],a[200:203],a[28:31]
v_accvgpr_read_b32 v14,a14
v_accvgpr_read_b32 v15,a15
v_mfma_f32_16x16x32_bf16 a[44:47],a[112:115],a[208:211],a[44:47]
v_mul_f32 v12,s71,v12
v_mul_f32 v13,s71,v13
v_mfma_f32_16x16x32_bf16 a[44:47],a[116:119],a[212:215],a[44:47]
v_mul_f32 v14,s71,v14
v_mul_f32 v15,s71,v15
v_mfma_f32_16x16x32_bf16 a[44:47],a[120:123],a[240:243],a[44:47]
v_cvt_pk_bf16_f32 v142,v12,v13
v_cvt_pk_bf16_f32 v143,v14,v15
v_mfma_f32_16x16x32_bf16 a[44:47],a[124:127],a[244:247],a[44:47]
v_mul_f32 v23,s71,v23
v_cvt_pk_bf16_f32 v164,v16,v17
v_mfma_f32_16x16x32_bf16 a[60:63],a[124:127],a[252:255],a[60:63]
global_load_lds_dwordx4 v4,s[10:11] offset:2048
ds_read_b64_tr_b16 a[124:125],v52 offset:18184
v_mfma_f32_16x16x32_bf16 a[60:63],a[120:123],a[248:251],a[60:63]
ds_read_b64_tr_b16 a[120:121],v52 offset:1800
v_cvt_pk_bf16_f32 v165,v18,v19
v_mfma_f32_16x16x32_bf16 a[60:63],a[116:119],a[220:223],a[60:63]
ds_read_b64_tr_b16 a[116:117],v52 offset:17160
v_cvt_pk_bf16_f32 v166,v20,v21
v_mfma_f32_16x16x32_bf16 a[60:63],a[112:115],a[216:219],a[60:63]
ds_read_b64_tr_b16 a[112:113],v52 offset:776
v_cvt_pk_bf16_f32 v167,v22,v23
v_mfma_f32_16x16x32_bf16 v[64:67],a[64:67],a[192:195],v[64:67]
v_accvgpr_read_b32 v16,a48
v_accvgpr_read_b32 v17,a49
v_mfma_f32_16x16x32_bf16 v[64:67],a[68:71],a[196:199],v[64:67]
ds_read_b64_tr_b16 a[114:115],v52 offset:2824
v_accvgpr_read_b32 v18,a50
v_mfma_f32_16x16x32_bf16 v[64:67],a[72:75],a[224:227],v[64:67]
ds_read_b64_tr_b16 a[118:119],v52 offset:19208
v_accvgpr_read_b32 v19,a51
v_mfma_f32_16x16x32_bf16 v[64:67],a[76:79],a[228:231],v[64:67]
ds_read_b64_tr_b16 a[122:123],v52 offset:3848
v_accvgpr_read_b32 v20,a52
v_mfma_f32_16x16x32_bf16 v[80:83],a[76:79],a[236:239],v[80:83]
v_accvgpr_read_b32 v21,a53
v_accvgpr_read_b32 v22,a54
v_mfma_f32_16x16x32_bf16 v[80:83],a[72:75],a[232:235],v[80:83]
ds_read_b64_tr_b16 a[126:127],v52 offset:20232
v_accvgpr_read_b32 v23,a55
v_mfma_f32_16x16x32_bf16 v[80:83],a[68:71],a[204:207],v[80:83]
v_mul_f32 v64,s71,v64
v_mul_f32 v65,s71,v65
v_mfma_f32_16x16x32_bf16 v[80:83],a[64:67],a[200:203],v[80:83]
v_mul_f32 v66,s71,v66
v_mul_f32 v67,s71,v67
v_mfma_f32_16x16x32_bf16 v[96:99],a[64:67],a[208:211],v[96:99]
v_cvt_pk_bf16_f32 v148,v64,v65
v_cvt_pk_bf16_f32 v149,v66,v67
v_mfma_f32_16x16x32_bf16 v[96:99],a[68:71],a[212:215],v[96:99]
v_mul_f32 v16,s71,v16
v_mul_f32 v17,s71,v17
v_mfma_f32_16x16x32_bf16 v[96:99],a[72:75],a[240:243],v[96:99]
v_mul_f32 v18,s71,v18
v_mul_f32 v19,s71,v19
v_mfma_f32_16x16x32_bf16 v[96:99],a[76:79],a[244:247],v[96:99]
global_load_lds_dwordx4 v5,s[10:11] offset:3072
v_mul_f32 v20,s71,v20
v_mfma_f32_16x16x32_bf16 v[112:115],a[76:79],a[252:255],v[112:115]
v_mul_f32 v21,s71,v21
v_mul_f32 v22,s71,v22
v_mfma_f32_16x16x32_bf16 v[112:115],a[72:75],a[248:251],v[112:115]
v_mul_f32 v23,s71,v23
v_cvt_pk_bf16_f32 v228,v16,v17
v_mfma_f32_16x16x32_bf16 v[112:115],a[68:71],a[220:223],v[112:115]
v_cvt_pk_bf16_f32 v229,v18,v19
v_cvt_pk_bf16_f32 v230,v20,v21
v_mfma_f32_16x16x32_bf16 v[112:115],a[64:67],a[216:219],v[112:115]
v_cvt_pk_bf16_f32 v231,v22,v23
v_accvgpr_read_b32 v16,a24
s_waitcnt vmcnt(8) lgkmcnt(0)
s_barrier
v_mfma_f32_16x16x32_bf16 v[68:71],a[80:83],a[192:195],v[68:71]
ds_read_b128 a[128:131],v1
v_accvgpr_read_b32 v17,a25
v_mfma_f32_16x16x32_bf16 v[68:71],a[84:87],a[196:199],v[68:71]
ds_read_b128 a[132:135],v1 offset:256
v_accvgpr_read_b32 v18,a26
v_mfma_f32_16x16x32_bf16 v[68:71],a[88:91],a[224:227],v[68:71]
ds_read_b128 a[136:139],v1 offset:512
v_accvgpr_read_b32 v19,a27
v_mfma_f32_16x16x32_bf16 v[68:71],a[92:95],a[228:231],v[68:71]
ds_read_b128 a[140:143],v1 offset:768
v_accvgpr_read_b32 v20,a28
v_mfma_f32_16x16x32_bf16 v[84:87],a[92:95],a[236:239],v[84:87]
v_accvgpr_read_b32 v21,a29
v_accvgpr_read_b32 v22,a30
v_mfma_f32_16x16x32_bf16 v[84:87],a[88:91],a[232:235],v[84:87]
v_accvgpr_read_b32 v23,a31
v_mul_f32 v16,s71,v16
v_mfma_f32_16x16x32_bf16 v[84:87],a[84:87],a[204:207],v[84:87]
s_add_u32 s10,s20,s74
s_addc_u32 s11,s21,0
v_mul_f32 v68,s71,v68
v_mfma_f32_16x16x32_bf16 v[84:87],a[80:83],a[200:203],v[84:87]
v_mul_f32 v69,s71,v69
v_mul_f32 v70,s71,v70
v_mfma_f32_16x16x32_bf16 v[100:103],a[80:83],a[208:211],v[100:103]
s_add_u32 m0,s76,0
v_mul_f32 v71,s71,v71
v_cvt_pk_bf16_f32 v150,v68,v69
v_mfma_f32_16x16x32_bf16 v[100:103],a[84:87],a[212:215],v[100:103]
v_cvt_pk_bf16_f32 v151,v70,v71
v_mul_f32 v17,s71,v17
v_mfma_f32_16x16x32_bf16 v[100:103],a[88:91],a[240:243],v[100:103]
global_load_lds_dwordx4 v4,s[10:11] nt
v_mul_f32 v18,s71,v18
v_mfma_f32_16x16x32_bf16 v[100:103],a[92:95],a[244:247],v[100:103]
v_mul_f32 v19,s71,v19
v_mul_f32 v20,s71,v20
v_mfma_f32_16x16x32_bf16 v[116:119],a[92:95],a[252:255],v[116:119]
v_mul_f32 v21,s71,v21
v_mul_f32 v22,s71,v22
v_mfma_f32_16x16x32_bf16 v[116:119],a[88:91],a[248:251],v[116:119]
v_mul_f32 v23,s71,v23
v_cvt_pk_bf16_f32 v172,v16,v17
v_mfma_f32_16x16x32_bf16 v[116:119],a[84:87],a[220:223],v[116:119]
v_cvt_pk_bf16_f32 v173,v18,v19
v_cvt_pk_bf16_f32 v174,v20,v21
v_mfma_f32_16x16x32_bf16 v[116:119],a[80:83],a[216:219],v[116:119]
v_cvt_pk_bf16_f32 v175,v22,v23
v_mul_f32 v80,s71,v80
v_mfma_f32_16x16x32_bf16 v[72:75],a[96:99],a[192:195],v[72:75]
v_mul_f32 v81,s71,v81
v_mul_f32 v82,s71,v82
v_mfma_f32_16x16x32_bf16 v[72:75],a[100:103],a[196:199],v[72:75]
v_mul_f32 v83,s71,v83
v_mul_f32 v84,s71,v84
v_mfma_f32_16x16x32_bf16 v[72:75],a[104:107],a[224:227],v[72:75]
v_mul_f32 v85,s71,v85
v_mul_f32 v86,s71,v86
v_mfma_f32_16x16x32_bf16 v[72:75],a[108:111],a[228:231],v[72:75]
v_mul_f32 v87,s71,v87
v_cvt_pk_bf16_f32 v180,v80,v81
v_mfma_f32_16x16x32_bf16 v[88:91],a[108:111],a[236:239],v[88:91]
v_cvt_pk_bf16_f32 v181,v82,v83
v_cvt_pk_bf16_f32 v182,v84,v85
v_mfma_f32_16x16x32_bf16 v[88:91],a[104:107],a[232:235],v[88:91]
v_cvt_pk_bf16_f32 v183,v86,v87
v_accvgpr_read_b32 v8,a40
v_mfma_f32_16x16x32_bf16 v[88:91],a[100:103],a[204:207],v[88:91]
v_mul_f32 v72,s71,v72
v_mul_f32 v73,s71,v73
v_mfma_f32_16x16x32_bf16 v[88:91],a[96:99],a[200:203],v[88:91]
v_mul_f32 v74,s71,v74
v_mul_f32 v75,s71,v75
v_mfma_f32_16x16x32_bf16 v[104:107],a[96:99],a[208:211],v[104:107]
global_load_lds_dwordx4 v4,s[10:11] offset:1024 nt
v_cvt_pk_bf16_f32 v156,v72,v73
v_mfma_f32_16x16x32_bf16 v[104:107],a[100:103],a[212:215],v[104:107]
v_cvt_pk_bf16_f32 v157,v74,v75
v_accvgpr_read_b32 v9,a41
v_mfma_f32_16x16x32_bf16 v[104:107],a[104:107],a[240:243],v[104:107]
v_mul_f32 v88,s71,v88
v_mul_f32 v89,s71,v89
v_mfma_f32_16x16x32_bf16 v[104:107],a[108:111],a[244:247],v[104:107]
v_mul_f32 v90,s71,v90
v_mul_f32 v91,s71,v91
v_mfma_f32_16x16x32_bf16 v[120:123],a[108:111],a[252:255],v[120:123]
v_cvt_pk_bf16_f32 v188,v88,v89
v_cvt_pk_bf16_f32 v189,v90,v91
v_mfma_f32_16x16x32_bf16 v[120:123],a[104:107],a[248:251],v[120:123]
v_accvgpr_read_b32 v10,a42
v_accvgpr_read_b32 v11,a43
v_mfma_f32_16x16x32_bf16 v[120:123],a[100:103],a[220:223],v[120:123]
v_accvgpr_read_b32 v12,a44
v_accvgpr_read_b32 v13,a45
v_mfma_f32_16x16x32_bf16 v[120:123],a[96:99],a[216:219],v[120:123]
)A"
R"A(v_accvgpr_read_b32 v14,a46
v_accvgpr_read_b32 v15,a47
v_mfma_f32_16x16x32_bf16 v[76:79],a[112:115],a[192:195],v[76:79]
v_mul_f32 v8,s71,v8
v_mul_f32 v9,s71,v9
v_mfma_f32_16x16x32_bf16 v[76:79],a[116:119],a[196:199],v[76:79]
v_mul_f32 v10,s71,v10
v_mul_f32 v11,s71,v11
v_mfma_f32_16x16x32_bf16 v[76:79],a[120:123],a[224:227],v[76:79]
v_mul_f32 v12,s71,v12
v_mul_f32 v13,s71,v13
v_mfma_f32_16x16x32_bf16 v[76:79],a[124:127],a[228:231],v[76:79]
v_mul_f32 v14,s71,v14
v_mul_f32 v15,s71,v15
v_mfma_f32_16x16x32_bf16 v[92:95],a[124:127],a[236:239],v[92:95]
v_cvt_pk_bf16_f32 v204,v8,v9
v_cvt_pk_bf16_f32 v205,v10,v11
v_mfma_f32_16x16x32_bf16 v[92:95],a[120:123],a[232:235],v[92:95]
v_cvt_pk_bf16_f32 v206,v12,v13
v_cvt_pk_bf16_f32 v207,v14,v15
v_mfma_f32_16x16x32_bf16 v[92:95],a[116:119],a[204:207],v[92:95]
global_load_lds_dwordx4 v5,s[10:11] offset:2048 nt
v_mul_f32 v76,s71,v76
v_mfma_f32_16x16x32_bf16 v[92:95],a[112:115],a[200:203],v[92:95]
v_mul_f32 v77,s71,v77
v_mul_f32 v78,s71,v78
v_mfma_f32_16x16x32_bf16 v[108:111],a[112:115],a[208:211],v[108:111]
v_mul_f32 v79,s71,v79
v_cvt_pk_bf16_f32 v158,v76,v77
v_mfma_f32_16x16x32_bf16 v[108:111],a[116:119],a[212:215],v[108:111]
ds_read_b128 a[156:159],v1 offset:4864
v_cvt_pk_bf16_f32 v159,v78,v79
v_mfma_f32_16x16x32_bf16 v[108:111],a[120:123],a[240:243],v[108:111]
v_mul_f32 v92,s71,v92
v_mul_f32 v93,s71,v93
v_mfma_f32_16x16x32_bf16 v[108:111],a[124:127],a[244:247],v[108:111]
v_mul_f32 v94,s71,v94
v_mul_f32 v95,s71,v95
v_mfma_f32_16x16x32_bf16 v[124:127],a[124:127],a[252:255],v[124:127]
v_cvt_pk_bf16_f32 v190,v92,v93
v_cvt_pk_bf16_f32 v191,v94,v95
v_mfma_f32_16x16x32_bf16 v[124:127],a[120:123],a[248:251],v[124:127]
v_cvt_pk_bf16_f32 v209,v210,v211
v_cvt_pk_bf16_f32 v210,v212,v213
v_mfma_f32_16x16x32_bf16 v[124:127],a[116:119],a[220:223],v[124:127]
ds_read_b128 a[152:155],v1 offset:4608
v_cvt_pk_bf16_f32 v211,v214,v215
v_mfma_f32_16x16x32_bf16 v[124:127],a[112:115],a[216:219],v[124:127]
v_mul_f32 v96,s71,v96
v_mul_f32 v97,s71,v97
s_waitcnt lgkmcnt(2)
v_mfma_f32_16x16x32_bf16 v[64:67],a[128:131],v[132:135],0
v_mul_f32 v98,s71,v98
v_mul_f32 v99,s71,v99
v_mfma_f32_16x16x32_bf16 v[64:67],a[132:135],v[140:143],v[64:67]
v_mul_f32 v100,s71,v100
v_mul_f32 v101,s71,v101
v_mfma_f32_16x16x32_bf16 v[64:67],a[136:139],v[148:151],v[64:67]
v_mul_f32 v102,s71,v102
v_mul_f32 v103,s71,v103
v_mfma_f32_16x16x32_bf16 v[64:67],a[140:143],v[156:159],v[64:67]
global_load_lds_dwordx4 v5,s[10:11] offset:3072
v_cvt_pk_bf16_f32 v212,v96,v97
v_mfma_f32_16x16x32_bf16 v[80:83],a[128:131],v[164:167],0
v_cvt_pk_bf16_f32 v213,v98,v99
v_cvt_pk_bf16_f32 v214,v100,v101
v_mfma_f32_16x16x32_bf16 v[80:83],a[132:135],v[172:175],v[80:83]
v_cvt_pk_bf16_f32 v215,v102,v103
v_mul_f32 v216,s71,v216
v_mfma_f32_16x16x32_bf16 v[80:83],a[136:139],v[180:183],v[80:83]
v_max3_f32 v64,v64,v65,v66
v_max_f32 v64,v64,v67
v_mov_b32 v65,v64
v_mfma_f32_16x16x32_bf16 v[80:83],a[140:143],v[188:191],v[80:83]
v_mul_f32 v217,s71,v217
v_mul_f32 v218,s71,v218
v_mfma_f32_16x16x32_bf16 v[96:99],a[128:131],v[196:199],0
v_permlane32_swap_b32 v64,v65
v_max_f32 v64,v64,v65
v_mov_b32 v65,v64
v_mul_f32 v219,s71,v219
v_mul_f32 v220,s71,v220
v_mul_f32 v221,s71,v221
v_mul_f32 v222,s71,v222
v_mul_f32 v223,s71,v223
v_cvt_pk_bf16_f32 v216,v216,v217
v_cvt_pk_bf16_f32 v217,v218,v219
v_cvt_pk_bf16_f32 v218,v220,v221
v_cvt_pk_bf16_f32 v219,v222,v223
v_mul_f32 v104,s71,v104
v_mul_f32 v105,s71,v105
v_mul_f32 v106,s71,v106
v_mul_f32 v107,s71,v107
v_mul_f32 v108,s71,v108
v_mul_f32 v109,s71,v109
v_mul_f32 v110,s71,v110
v_mul_f32 v111,s71,v111
v_cvt_pk_bf16_f32 v220,v104,v105
v_cvt_pk_bf16_f32 v221,v106,v107
v_cvt_pk_bf16_f32 v222,v108,v109
v_cvt_pk_bf16_f32 v223,v110,v111
v_mfma_f32_16x16x32_bf16 v[96:99],a[132:135],v[204:207],v[96:99]
ds_read_b128 a[148:151],v1 offset:4352
v_max3_f32 v80,v80,v81,v82
v_mfma_f32_16x16x32_bf16 v[96:99],a[136:139],v[212:215],v[96:99]
ds_read_b128 a[144:147],v1 offset:4096
v_permlane16_swap_b32 v64,v65
v_max_f32 v64,v64,v65
L_pd4_%=:
s_cbranch_execz L_pd8_%=
v_sub_f32 v35,0,v64
v_sub_f32 v34,0,v64
v_sub_f32 v33,0,v64
v_sub_f32 v32,0,v64
v_mfma_f32_16x16x32_bf16 v[96:99],a[140:143],v[220:223],v[96:99]
ds_read_b128 a[160:163],v1 offset:8192
v_max_f32 v80,v80,v83
v_mfma_f32_16x16x32_bf16 v[64:67],a[128:131],v[132:135],v[32:35]
s_waitcnt lgkmcnt(3)
ds_read_b128 a[164:167],v1 offset:8448
v_mov_b32 v81,v80
v_mfma_f32_16x16x32_bf16 v[64:67],a[132:135],v[140:143],v[64:67]
s_add_u32 s10,s24,s74
s_addc_u32 s11,s25,0
v_mul_f32 v232,s71,v232
v_mfma_f32_16x16x32_bf16 v[64:67],a[136:139],v[148:151],v[64:67]
s_add_u32 s74,s74,16384
ds_read_b128 a[168:171],v1 offset:8704
v_max3_f32 v96,v96,v97,v98
v_mfma_f32_16x16x32_bf16 v[64:67],a[140:143],v[156:159],v[64:67]
s_add_u32 m0,s76,16384
ds_read_b128 a[172:175],v1 offset:8960
v_max_f32 v96,v96,v99
v_mfma_f32_16x16x32_bf16 v[68:71],a[156:159],v[156:159],v[32:35]
s_and_b32 s74,s74,0x3ffff
ds_read_b128 a[188:191],v1 offset:13056
v_mov_b32 v97,v96
v_mfma_f32_16x16x32_bf16 v[68:71],a[152:155],v[148:151],v[68:71]
global_load_lds_dwordx4 v4,s[10:11]
v_mul_f32 v233,s71,v233
s_waitcnt lgkmcnt(6)
v_mfma_f32_16x16x32_bf16 v[68:71],a[148:151],v[140:143],v[68:71]
s_or_b32 s74,s74,s69
ds_read_b128 a[184:187],v1 offset:12800
v_mul_f32 v234,s71,v234
s_waitcnt lgkmcnt(6)
v_mfma_f32_16x16x32_bf16 v[68:71],a[144:147],v[132:135],v[68:71]
s_sub_u32 s76,s76,32768
ds_read_b128 a[180:183],v1 offset:12544
v_mul_f32 v235,s71,v235
s_waitcnt lgkmcnt(6)
v_mfma_f32_16x16x32_bf16 v[72:75],a[160:163],v[132:135],v[32:35]
s_and_b32 s76,s76,0x1ffff
ds_read_b128 a[176:179],v1 offset:12288
v_mul_f32 v236,s71,v236
s_waitcnt lgkmcnt(6)
v_mfma_f32_16x16x32_bf16 v[72:75],a[164:167],v[140:143],v[72:75]
s_and_b32 s69,s75,0xfffc0000
v_permlane32_swap_b32 v80,v81
s_waitcnt lgkmcnt(5)
v_mfma_f32_16x16x32_bf16 v[72:75],a[168:171],v[148:151],v[72:75]
s_add_u32 s75,s75,16384
v_max_f32 v80,v80,v81
v_mov_b32 v81,v80
s_waitcnt lgkmcnt(4)
v_mfma_f32_16x16x32_bf16 v[72:75],a[172:175],v[156:159],v[72:75]
s_waitcnt lgkmcnt(3)
v_mul_f32 v237,s71,v237
v_mul_f32 v238,s71,v238
v_mfma_f32_16x16x32_bf16 v[76:79],a[188:191],v[156:159],v[32:35]
s_waitcnt lgkmcnt(2)
v_permlane16_swap_b32 v80,v81
v_mfma_f32_16x16x32_bf16 v[76:79],a[184:187],v[148:151],v[76:79]
s_waitcnt lgkmcnt(1)
v_max_f32 v80,v80,v81
v_sub_f32 v39,0,v80
v_sub_f32 v38,0,v80
v_sub_f32 v37,0,v80
v_sub_f32 v36,0,v80
v_mfma_f32_16x16x32_bf16 v[76:79],a[180:183],v[140:143],v[76:79]
s_waitcnt lgkmcnt(0)
v_mul_f32 v239,s71,v239
v_cvt_pk_bf16_f32 v232,v232,v233
v_mfma_f32_16x16x32_bf16 v[76:79],a[176:179],v[132:135],v[76:79]
v_cvt_pk_bf16_f32 v233,v234,v235
v_cvt_pk_bf16_f32 v234,v236,v237
v_mfma_f32_16x16x32_bf16 v[80:83],a[128:131],v[164:167],v[36:39]
v_cvt_pk_bf16_f32 v235,v238,v239
v_accvgpr_read_b32 v16,a56
v_mfma_f32_16x16x32_bf16 v[80:83],a[132:135],v[172:175],v[80:83]
v_accvgpr_read_b32 v17,a57
v_accvgpr_read_b32 v18,a58
v_mfma_f32_16x16x32_bf16 v[80:83],a[136:139],v[180:183],v[80:83]
v_accvgpr_read_b32 v19,a59
v_accvgpr_read_b32 v20,a60
v_mfma_f32_16x16x32_bf16 v[80:83],a[140:143],v[188:191],v[80:83]
global_load_lds_dwordx4 v5,s[10:11] offset:1024
v_accvgpr_read_b32 v21,a61
v_mfma_f32_16x16x32_bf16 v[84:87],a[156:159],v[188:191],v[36:39]
v_accvgpr_read_b32 v22,a62
v_accvgpr_read_b32 v23,a63
v_mfma_f32_16x16x32_bf16 v[84:87],a[152:155],v[180:183],v[84:87]
v_mul_f32 v16,s71,v16
v_mul_f32 v17,s71,v17
v_mfma_f32_16x16x32_bf16 v[84:87],a[148:151],v[172:175],v[84:87]
v_mul_f32 v18,s71,v18
v_mul_f32 v19,s71,v19
v_mfma_f32_16x16x32_bf16 v[84:87],a[144:147],v[164:167],v[84:87]
v_mul_f32 v20,s71,v20
v_mul_f32 v21,s71,v21
v_mfma_f32_16x16x32_bf16 v[88:91],a[160:163],v[164:167],v[36:39]
v_mul_f32 v22,s71,v22
v_mul_f32 v23,s71,v23
v_mfma_f32_16x16x32_bf16 v[88:91],a[164:167],v[172:175],v[88:91]
v_cvt_pk_bf16_f32 v236,v16,v17
v_cvt_pk_bf16_f32 v237,v18,v19
v_mfma_f32_16x16x32_bf16 v[88:91],a[168:171],v[180:183],v[88:91]
v_cvt_pk_bf16_f32 v238,v20,v21
v_cvt_pk_bf16_f32 v239,v22,v23
v_mfma_f32_16x16x32_bf16 v[88:91],a[172:175],v[188:191],v[88:91]
v_mul_f32 v240,s71,v240
v_mul_f32 v241,s71,v241
v_mfma_f32_16x16x32_bf16 v[92:95],a[188:191],v[188:191],v[36:39]
v_mul_f32 v242,s71,v242
v_mul_f32 v243,s71,v243
v_mfma_f32_16x16x32_bf16 v[92:95],a[184:187],v[180:183],v[92:95]
v_mul_f32 v244,s71,v244
v_mul_f32 v245,s71,v245
v_mfma_f32_16x16x32_bf16 v[92:95],a[180:183],v[172:175],v[92:95]
v_mul_f32 v246,s71,v246
v_mul_f32 v247,s71,v247
v_mfma_f32_16x16x32_bf16 v[92:95],a[176:179],v[164:167],v[92:95]
v_cvt_pk_bf16_f32 v240,v240,v241
v_cvt_pk_bf16_f32 v241,v242,v243
v_cvt_pk_bf16_f32 v242,v244,v245
v_cvt_pk_bf16_f32 v243,v246,v247
v_mul_f32 v112,s71,v112
v_mul_f32 v113,s71,v113
v_mul_f32 v114,s71,v114
v_mul_f32 v115,s71,v115
v_mul_f32 v116,s71,v116
v_mul_f32 v117,s71,v117
v_mul_f32 v118,s71,v118
v_mul_f32 v119,s71,v119
v_cvt_pk_bf16_f32 v244,v112,v113
v_cvt_pk_bf16_f32 v245,v114,v115
v_cvt_pk_bf16_f32 v246,v116,v117
v_cvt_pk_bf16_f32 v247,v118,v119
v_permlane32_swap_b32 v96,v97
v_max_f32 v96,v96,v97
v_mov_b32 v97,v96
v_mfma_f32_16x16x32_bf16 v[112:115],a[128:131],v[228:231],0
v_mul_f32 v248,s71,v248
v_mul_f32 v249,s71,v249
v_mul_f32 v250,s71,v250
v_mul_f32 v251,s71,v251
v_mul_f32 v252,s71,v252
v_mul_f32 v253,s71,v253
v_mul_f32 v254,s71,v254
v_mul_f32 v255,s71,v255
v_cvt_pk_bf16_f32 v248,v248,v249
v_cvt_pk_bf16_f32 v249,v250,v251
v_cvt_pk_bf16_f32 v250,v252,v253
v_cvt_pk_bf16_f32 v251,v254,v255
v_mul_f32 v120,s71,v120
v_mul_f32 v121,s71,v121
v_mul_f32 v122,s71,v122
v_mul_f32 v123,s71,v123
v_mul_f32 v124,s71,v124
v_mul_f32 v125,s71,v125
v_mul_f32 v126,s71,v126
v_mul_f32 v127,s71,v127
v_cvt_pk_bf16_f32 v252,v120,v121
v_cvt_pk_bf16_f32 v253,v122,v123
v_cvt_pk_bf16_f32 v254,v124,v125
v_cvt_pk_bf16_f32 v255,v126,v127
v_mfma_f32_16x16x32_bf16 v[112:115],a[132:135],v[236:239],v[112:115]
global_load_lds_dwordx4 v4,s[10:11] offset:2048
v_permlane16_swap_b32 v96,v97
v_max_f32 v96,v96,v97
v_sub_f32 v43,0,v96
v_sub_f32 v42,0,v96
v_sub_f32 v41,0,v96
v_sub_f32 v40,0,v96
v_mfma_f32_16x16x32_bf16 v[112:115],a[136:139],v[244:247],v[112:115]
ds_read_b64_tr_b16 a[192:193],v2
ds_read_b64_tr_b16 a[194:195],v2 offset:4096
v_mfma_f32_16x16x32_bf16 v[112:115],a[140:143],v[252:255],v[112:115]
ds_read_b64_tr_b16 a[196:197],v2 offset:8192
ds_read_b64_tr_b16 a[198:199],v2 offset:12288
v_mfma_f32_16x16x32_bf16 v[96:99],a[128:131],v[196:199],v[40:43]
ds_read_b64_tr_b16 a[200:201],v3
ds_read_b64_tr_b16 a[202:203],v3 offset:4096
v_mfma_f32_16x16x32_bf16 v[96:99],a[132:135],v[204:207],v[96:99]
ds_read_b64_tr_b16 a[204:205],v3 offset:8192
ds_read_b64_tr_b16 a[206:207],v3 offset:12288
v_mfma_f32_16x16x32_bf16 v[96:99],a[136:139],v[212:215],v[96:99]
ds_read_b64_tr_b16 a[208:209],v2 offset:256
v_max3_f32 v112,v112,v113,v114
v_mfma_f32_16x16x32_bf16 v[96:99],a[140:143],v[220:223],v[96:99]
ds_read_b64_tr_b16 a[210:211],v2 offset:4352
ds_read_b64_tr_b16 a[212:213],v2 offset:8448
v_mfma_f32_16x16x32_bf16 v[100:103],a[156:159],v[220:223],v[40:43]
ds_read_b64_tr_b16 a[214:215],v2 offset:12544
v_max_f32 v112,v112,v115
v_mfma_f32_16x16x32_bf16 v[100:103],a[152:155],v[212:215],v[100:103]
ds_read_b64_tr_b16 a[216:217],v3 offset:256
ds_read_b64_tr_b16 a[218:219],v3 offset:4352
v_mfma_f32_16x16x32_bf16 v[100:103],a[148:151],v[204:207],v[100:103]
v_exp_f32 v64,v64
v_mfma_f32_16x16x32_bf16 v[100:103],a[144:147],v[196:199],v[100:103]
v_exp_f32 v65,v65
v_mfma_f32_16x16x32_bf16 v[104:107],a[160:163],v[196:199],v[40:43]
v_exp_f32 v66,v66
v_mfma_f32_16x16x32_bf16 v[104:107],a[164:167],v[204:207],v[104:107]
v_exp_f32 v67,v67
v_mfma_f32_16x16x32_bf16 v[104:107],a[168:171],v[212:215],v[104:107]
v_exp_f32 v68,v68
v_mfma_f32_16x16x32_bf16 v[104:107],a[172:175],v[220:223],v[104:107]
global_load_lds_dwordx4 v5,s[10:11] offset:3072
ds_read_b64_tr_b16 a[220:221],v3 offset:8448
v_mfma_f32_16x16x32_bf16 v[108:111],a[188:191],v[220:223],v[40:43]
v_exp_f32 v69,v69
v_exp_f32 v70,v70
v_exp_f32 v71,v71
v_mfma_f32_16x16x32_bf16 v[108:111],a[184:187],v[212:215],v[108:111]
ds_read_b64_tr_b16 a[222:223],v3 offset:12544
v_cvt_pk_bf16_f32 v64,v64,v65
v_cvt_pk_bf16_f32 v65,v66,v67
)A"
R"A(v_cvt_pk_bf16_f32 v66,v68,v69
v_cvt_pk_bf16_f32 v67,v70,v71
v_exp_f32 v72,v72
v_exp_f32 v73,v73
v_exp_f32 v74,v74
v_exp_f32 v75,v75
v_exp_f32 v76,v76
v_exp_f32 v77,v77
v_exp_f32 v78,v78
v_exp_f32 v79,v79
v_mfma_f32_16x16x32_bf16 v[108:111],a[180:183],v[204:207],v[108:111]
ds_read_b64_tr_b16 a[224:225],v2 offset:512
ds_read_b64_tr_b16 a[226:227],v2 offset:4608
v_cvt_pk_bf16_f32 v72,v72,v73
v_cvt_pk_bf16_f32 v73,v74,v75
v_cvt_pk_bf16_f32 v74,v76,v77
v_cvt_pk_bf16_f32 v75,v78,v79
v_mfma_f32_16x16x32_bf16 v[108:111],a[176:179],v[196:199],v[108:111]
s_waitcnt lgkmcnt(4)
ds_read_b64_tr_b16 a[228:229],v2 offset:8704
ds_read_b64_tr_b16 a[230:231],v2 offset:12800
v_mfma_f32_16x16x32_bf16 a[0:3],a[192:195],v[64:67],0
ds_read_b64_tr_b16 a[232:233],v3 offset:512
ds_read_b64_tr_b16 a[234:235],v3 offset:4608
v_mfma_f32_16x16x32_bf16 a[0:3],a[196:199],v[72:75],a[0:3]
ds_read_b64_tr_b16 a[236:237],v3 offset:8704
ds_read_b64_tr_b16 a[238:239],v3 offset:12800
v_mfma_f32_16x16x32_bf16 a[4:7],a[200:203],v[64:67],0
ds_read_b64_tr_b16 a[240:241],v2 offset:768
v_mov_b32 v113,v112
v_mfma_f32_16x16x32_bf16 a[4:7],a[204:207],v[72:75],a[4:7]
ds_read_b64_tr_b16 a[242:243],v2 offset:4864
ds_read_b64_tr_b16 a[244:245],v2 offset:8960
v_mfma_f32_16x16x32_bf16 a[8:11],a[208:211],v[64:67],0
ds_read_b64_tr_b16 a[246:247],v2 offset:13056
v_add_u32 v1,s78,v1
v_mfma_f32_16x16x32_bf16 a[8:11],a[212:215],v[72:75],a[8:11]
ds_read_b64_tr_b16 a[248:249],v3 offset:768
ds_read_b64_tr_b16 a[250:251],v3 offset:4864
v_mfma_f32_16x16x32_bf16 a[12:15],a[216:219],v[64:67],0
s_waitcnt lgkmcnt(12)
ds_read_b64_tr_b16 a[252:253],v3 offset:8960
v_add_u32 v2,s78,v2
v_mfma_f32_16x16x32_bf16 a[12:15],a[220:223],v[72:75],a[12:15]
s_waitcnt lgkmcnt(6)
ds_read_b64_tr_b16 a[254:255],v3 offset:13056
v_add_u32 v3,s78,v3
v_mfma_f32_16x16x32_bf16 a[16:19],a[224:227],v[64:67],0
s_sub_u32 s78,s77,32768
v_permlane32_swap_b32 v112,v113
v_mfma_f32_16x16x32_bf16 a[16:19],a[228:231],v[72:75],a[16:19]
s_and_b32 s78,s78,0x1ffff
v_max_f32 v112,v112,v113
v_mov_b32 v113,v112
v_mfma_f32_16x16x32_bf16 a[20:23],a[232:235],v[64:67],0
s_sub_u32 s78,s78,s77
v_exp_f32 v80,v80
v_mfma_f32_16x16x32_bf16 a[20:23],a[236:239],v[72:75],a[20:23]
s_add_u32 s77,s77,s78
v_permlane16_swap_b32 v112,v113
s_waitcnt lgkmcnt(5)
v_mfma_f32_16x16x32_bf16 a[24:27],a[240:243],v[64:67],0
s_waitcnt lgkmcnt(4)
v_max_f32 v112,v112,v113
v_max_f32 v112,s72,v112
v_mfma_f32_16x16x32_bf16 a[24:27],a[244:247],v[72:75],a[24:27]
s_waitcnt lgkmcnt(2)
v_sub_f32 v47,0,v112
v_sub_f32 v46,0,v112
v_mfma_f32_16x16x32_bf16 a[28:31],a[248:251],v[64:67],0
s_waitcnt lgkmcnt(0)
v_sub_f32 v45,0,v112
v_sub_f32 v44,0,v112
v_mfma_f32_16x16x32_bf16 a[28:31],a[252:255],v[72:75],a[28:31]
v_exp_f32 v81,v81
v_mfma_f32_16x16x32_bf16 v[48:51],v[28:31],v[64:67],0
v_exp_f32 v82,v82
v_mfma_f32_16x16x32_bf16 v[48:51],v[28:31],v[72:75],v[48:51]
v_exp_f32 v83,v83
s_waitcnt vmcnt(8)
s_barrier
v_mfma_f32_16x16x32_bf16 v[112:115],a[128:131],v[228:231],v[44:47]
s_add_u32 s10,s20,s74
s_addc_u32 s11,s21,0
s_add_u32 m0,s76,0
v_mfma_f32_16x16x32_bf16 v[112:115],a[132:135],v[236:239],v[112:115]
v_exp_f32 v84,v84
v_mfma_f32_16x16x32_bf16 v[112:115],a[136:139],v[244:247],v[112:115]
global_load_lds_dwordx4 v4,s[10:11] nt
v_cvt_pk_bf16_f32 v80,v80,v81
v_mfma_f32_16x16x32_bf16 v[112:115],a[140:143],v[252:255],v[112:115]
v_exp_f32 v85,v85
v_mfma_f32_16x16x32_bf16 v[116:119],a[156:159],v[252:255],v[44:47]
v_exp_f32 v86,v86
v_mfma_f32_16x16x32_bf16 v[116:119],a[152:155],v[244:247],v[116:119]
v_exp_f32 v87,v87
v_mfma_f32_16x16x32_bf16 v[116:119],a[148:151],v[236:239],v[116:119]
v_cvt_pk_bf16_f32 v81,v82,v83
v_cvt_pk_bf16_f32 v82,v84,v85
v_mfma_f32_16x16x32_bf16 v[116:119],a[144:147],v[228:231],v[116:119]
ds_read_b128 a[128:131],v1
v_cvt_pk_bf16_f32 v83,v86,v87
v_mfma_f32_16x16x32_bf16 v[120:123],a[160:163],v[228:231],v[44:47]
v_exp_f32 v88,v88
v_mfma_f32_16x16x32_bf16 v[120:123],a[164:167],v[236:239],v[120:123]
v_exp_f32 v89,v89
v_mfma_f32_16x16x32_bf16 v[120:123],a[168:171],v[244:247],v[120:123]
v_exp_f32 v90,v90
v_mfma_f32_16x16x32_bf16 v[120:123],a[172:175],v[252:255],v[120:123]
v_exp_f32 v91,v91
v_mfma_f32_16x16x32_bf16 v[124:127],a[188:191],v[252:255],v[44:47]
v_exp_f32 v92,v92
v_mfma_f32_16x16x32_bf16 v[124:127],a[184:187],v[244:247],v[124:127]
v_exp_f32 v93,v93
v_exp_f32 v94,v94
v_exp_f32 v95,v95
v_mfma_f32_16x16x32_bf16 v[124:127],a[180:183],v[236:239],v[124:127]
v_cvt_pk_bf16_f32 v88,v88,v89
v_cvt_pk_bf16_f32 v89,v90,v91
v_cvt_pk_bf16_f32 v90,v92,v93
v_cvt_pk_bf16_f32 v91,v94,v95
v_mfma_f32_16x16x32_bf16 v[124:127],a[176:179],v[228:231],v[124:127]
v_exp_f32 v96,v96
v_mfma_f32_16x16x32_bf16 a[32:35],a[192:195],v[80:83],0
global_load_lds_dwordx4 v4,s[10:11] offset:1024 nt
ds_read_b128 a[132:135],v1 offset:256
v_mfma_f32_16x16x32_bf16 a[32:35],a[196:199],v[88:91],a[32:35]
v_exp_f32 v97,v97
v_mfma_f32_16x16x32_bf16 a[36:39],a[200:203],v[80:83],0
ds_read_b128 a[136:139],v1 offset:512
v_cvt_pk_bf16_f32 v96,v96,v97
v_mfma_f32_16x16x32_bf16 a[36:39],a[204:207],v[88:91],a[36:39]
v_exp_f32 v98,v98
v_mfma_f32_16x16x32_bf16 a[40:43],a[208:211],v[80:83],0
v_exp_f32 v99,v99
v_mfma_f32_16x16x32_bf16 a[40:43],a[212:215],v[88:91],a[40:43]
ds_read_b128 a[140:143],v1 offset:768
v_cvt_pk_bf16_f32 v97,v98,v99
v_mfma_f32_16x16x32_bf16 a[44:47],a[216:219],v[80:83],0
v_exp_f32 v100,v100
v_mfma_f32_16x16x32_bf16 a[44:47],a[220:223],v[88:91],a[44:47]
v_exp_f32 v101,v101
v_mfma_f32_16x16x32_bf16 a[48:51],a[224:227],v[80:83],0
ds_read_b128 a[156:159],v1 offset:4864
v_cvt_pk_bf16_f32 v98,v100,v101
v_mfma_f32_16x16x32_bf16 a[48:51],a[228:231],v[88:91],a[48:51]
v_exp_f32 v102,v102
v_mfma_f32_16x16x32_bf16 a[52:55],a[232:235],v[80:83],0
v_exp_f32 v103,v103
v_mfma_f32_16x16x32_bf16 a[52:55],a[236:239],v[88:91],a[52:55]
ds_read_b128 a[152:155],v1 offset:4608
v_cvt_pk_bf16_f32 v99,v102,v103
v_mfma_f32_16x16x32_bf16 a[56:59],a[240:243],v[80:83],0
ds_read_b128 a[148:151],v1 offset:4352
v_mfma_f32_16x16x32_bf16 a[56:59],a[244:247],v[88:91],a[56:59]
ds_read_b128 a[144:147],v1 offset:4096
v_mfma_f32_16x16x32_bf16 a[60:63],a[248:251],v[80:83],0
global_load_lds_dwordx4 v5,s[10:11] offset:2048 nt
ds_read_b128 a[160:163],v1 offset:8192
v_mfma_f32_16x16x32_bf16 a[60:63],a[252:255],v[88:91],a[60:63]
v_exp_f32 v104,v104
v_mfma_f32_16x16x32_bf16 v[52:55],v[28:31],v[80:83],0
ds_read_b128 a[164:167],v1 offset:8448
v_mfma_f32_16x16x32_bf16 v[52:55],v[28:31],v[88:91],v[52:55]
s_waitcnt lgkmcnt(5)
ds_read_b128 a[168:171],v1 offset:8704
v_mfma_f32_16x16x32_bf16 v[64:67],a[128:131],v[132:135],v[32:35]
ds_read_b128 a[172:175],v1 offset:8960
v_mfma_f32_16x16x32_bf16 v[64:67],a[132:135],v[140:143],v[64:67]
v_exp_f32 v105,v105
v_mfma_f32_16x16x32_bf16 v[64:67],a[136:139],v[148:151],v[64:67]
ds_read_b128 a[188:191],v1 offset:13056
v_cvt_pk_bf16_f32 v104,v104,v105
v_mfma_f32_16x16x32_bf16 v[64:67],a[140:143],v[156:159],v[64:67]
ds_read_b128 a[184:187],v1 offset:12800
v_mfma_f32_16x16x32_bf16 v[68:71],a[156:159],v[156:159],v[32:35]
s_waitcnt lgkmcnt(5)
ds_read_b128 a[180:183],v1 offset:12544
v_mfma_f32_16x16x32_bf16 v[68:71],a[152:155],v[148:151],v[68:71]
v_exp_f32 v106,v106
v_mfma_f32_16x16x32_bf16 v[68:71],a[148:151],v[140:143],v[68:71]
ds_read_b128 a[176:179],v1 offset:12288
v_add_u32 v1,s78,v1
v_mfma_f32_16x16x32_bf16 v[68:71],a[144:147],v[132:135],v[68:71]
v_exp_f32 v107,v107
v_mfma_f32_16x16x32_bf16 v[72:75],a[160:163],v[132:135],v[32:35]
s_waitcnt lgkmcnt(4)
v_exp_f32 v108,v108
v_mfma_f32_16x16x32_bf16 v[72:75],a[164:167],v[140:143],v[72:75]
v_exp_f32 v109,v109
v_mfma_f32_16x16x32_bf16 v[72:75],a[168:171],v[148:151],v[72:75]
global_load_lds_dwordx4 v5,s[10:11] offset:3072
v_cvt_pk_bf16_f32 v105,v106,v107
v_mfma_f32_16x16x32_bf16 v[72:75],a[172:175],v[156:159],v[72:75]
s_waitcnt lgkmcnt(2)
v_exp_f32 v110,v110
v_mfma_f32_16x16x32_bf16 v[76:79],a[188:191],v[156:159],v[32:35]
v_exp_f32 v111,v111
v_mfma_f32_16x16x32_bf16 v[76:79],a[184:187],v[148:151],v[76:79]
s_waitcnt lgkmcnt(1)
v_cvt_pk_bf16_f32 v106,v108,v109
v_cvt_pk_bf16_f32 v107,v110,v111
v_mfma_f32_16x16x32_bf16 v[76:79],a[180:183],v[140:143],v[76:79]
s_waitcnt lgkmcnt(0)
v_exp_f32 v112,v112
v_mfma_f32_16x16x32_bf16 v[76:79],a[176:179],v[132:135],v[76:79]
v_exp_f32 v113,v113
v_mfma_f32_16x16x32_bf16 a[64:67],a[192:195],v[96:99],0
v_exp_f32 v114,v114
v_mfma_f32_16x16x32_bf16 a[64:67],a[196:199],v[104:107],a[64:67]
v_exp_f32 v115,v115
v_mfma_f32_16x16x32_bf16 a[68:71],a[200:203],v[96:99],0
v_exp_f32 v116,v116
v_mfma_f32_16x16x32_bf16 a[68:71],a[204:207],v[104:107],a[68:71]
v_exp_f32 v117,v117
v_mfma_f32_16x16x32_bf16 a[72:75],a[208:211],v[96:99],0
s_add_u32 s10,s24,s74
s_addc_u32 s11,s25,0
s_add_u32 s74,s74,16384
s_and_b32 s74,s74,0x3ffff
s_or_b32 s74,s74,s69
s_lshl_b32 s69,s60,13
s_add_u32 s69,s69,131072
v_add_u32 v8,s69,v6
v_mfma_f32_16x16x32_bf16 a[72:75],a[212:215],v[104:107],a[72:75]
s_lshl_b32 s69,s60,13
ds_write_b128 v8,v[24:27] offset:4096
v_mfma_f32_16x16x32_bf16 a[76:79],a[216:219],v[96:99],0
s_add_u32 m0,s76,16384
ds_write_b32 v8,v7 offset:5120
v_cvt_pk_bf16_f32 v112,v112,v113
v_mfma_f32_16x16x32_bf16 a[76:79],a[220:223],v[104:107],a[76:79]
s_sub_u32 s76,s76,32768
v_bfe_u32 v24,v6,4,4
v_cvt_pk_bf16_f32 v113,v114,v115
v_mfma_f32_16x16x32_bf16 a[80:83],a[224:227],v[96:99],0
global_load_lds_dwordx4 v4,s[10:11]
v_lshrrev_b32 v7,8,v6
v_lshlrev_b32 v7,4,v7
v_lshl_add_u32 v7,v24,8,v7
v_mfma_f32_16x16x32_bf16 a[80:83],a[228:231],v[104:107],a[80:83]
s_and_b32 s76,s76,0x1ffff
v_exp_f32 v118,v118
v_mfma_f32_16x16x32_bf16 a[84:87],a[232:235],v[96:99],0
s_add_u32 s69,s69,131072
v_exp_f32 v119,v119
v_mfma_f32_16x16x32_bf16 a[84:87],a[236:239],v[104:107],a[84:87]
v_cvt_pk_bf16_f32 v114,v116,v117
v_cvt_pk_bf16_f32 v115,v118,v119
v_mfma_f32_16x16x32_bf16 a[88:91],a[240:243],v[96:99],0
v_exp_f32 v120,v120
v_mfma_f32_16x16x32_bf16 a[88:91],a[244:247],v[104:107],a[88:91]
buffer_load_dwordx4 v[8:11],v7,s[20:23],s65 offen nt
v_mfma_f32_16x16x32_bf16 a[92:95],a[248:251],v[96:99],0
v_exp_f32 v121,v121
v_mfma_f32_16x16x32_bf16 a[92:95],a[252:255],v[104:107],a[92:95]
v_exp_f32 v122,v122
v_mfma_f32_16x16x32_bf16 v[56:59],v[28:31],v[96:99],0
v_exp_f32 v123,v123
v_mfma_f32_16x16x32_bf16 v[56:59],v[28:31],v[104:107],v[56:59]
v_exp_f32 v124,v124
v_mfma_f32_16x16x32_bf16 v[80:83],a[128:131],v[164:167],v[36:39]
buffer_load_dwordx4 v[12:15],v7,s[20:23],s65 offen offset:64 nt
v_mfma_f32_16x16x32_bf16 v[80:83],a[132:135],v[172:175],v[80:83]
v_exp_f32 v125,v125
v_mfma_f32_16x16x32_bf16 v[80:83],a[136:139],v[180:183],v[80:83]
v_exp_f32 v126,v126
v_mfma_f32_16x16x32_bf16 v[80:83],a[140:143],v[188:191],v[80:83]
v_exp_f32 v127,v127
v_mfma_f32_16x16x32_bf16 v[84:87],a[156:159],v[188:191],v[36:39]
v_cvt_pk_bf16_f32 v120,v120,v121
v_cvt_pk_bf16_f32 v121,v122,v123
v_mfma_f32_16x16x32_bf16 v[84:87],a[152:155],v[180:183],v[84:87]
buffer_load_dwordx4 v[16:19],v7,s[20:23],s65 offen offset:128 nt
v_mfma_f32_16x16x32_bf16 v[84:87],a[148:151],v[172:175],v[84:87]
v_cvt_pk_bf16_f32 v122,v124,v125
v_cvt_pk_bf16_f32 v123,v126,v127
v_mfma_f32_16x16x32_bf16 v[84:87],a[144:147],v[164:167],v[84:87]
v_exp_f32 v64,v64
v_mfma_f32_16x16x32_bf16 v[88:91],a[160:163],v[164:167],v[36:39]
v_exp_f32 v65,v65
v_mfma_f32_16x16x32_bf16 v[88:91],a[164:167],v[172:175],v[88:91]
v_exp_f32 v66,v66
v_mfma_f32_16x16x32_bf16 v[88:91],a[168:171],v[180:183],v[88:91]
v_exp_f32 v67,v67
v_mfma_f32_16x16x32_bf16 v[88:91],a[172:175],v[188:191],v[88:91]
global_load_lds_dwordx4 v5,s[10:11] offset:1024
v_cvt_pk_bf16_f32 v64,v64,v65
v_mfma_f32_16x16x32_bf16 v[92:95],a[188:191],v[188:191],v[36:39]
v_exp_f32 v68,v68
v_mfma_f32_16x16x32_bf16 v[92:95],a[184:187],v[180:183],v[92:95]
v_exp_f32 v69,v69
v_mfma_f32_16x16x32_bf16 v[92:95],a[180:183],v[172:175],v[92:95]
v_exp_f32 v70,v70
v_mfma_f32_16x16x32_bf16 v[92:95],a[176:179],v[164:167],v[92:95]
v_exp_f32 v71,v71
v_mfma_f32_16x16x32_bf16 a[96:99],a[192:195],v[112:115],0
buffer_load_dwordx4 v[20:23],v7,s[20:23],s65 offen offset:192 nt
v_mfma_f32_16x16x32_bf16 a[96:99],a[196:199],v[120:123],a[96:99]
ds_read_b64_tr_b16 a[192:193],v2
ds_read_b64_tr_b16 a[194:195],v2 offset:4096
)A"
R"A(v_mfma_f32_16x16x32_bf16 a[100:103],a[200:203],v[112:115],0
ds_read_b64_tr_b16 a[196:197],v2 offset:8192
ds_read_b64_tr_b16 a[198:199],v2 offset:12288
v_mfma_f32_16x16x32_bf16 a[100:103],a[204:207],v[120:123],a[100:103]
ds_read_b64_tr_b16 a[200:201],v3
ds_read_b64_tr_b16 a[202:203],v3 offset:4096
v_mfma_f32_16x16x32_bf16 a[104:107],a[208:211],v[112:115],0
ds_read_b64_tr_b16 a[204:205],v3 offset:8192
ds_read_b64_tr_b16 a[206:207],v3 offset:12288
v_mfma_f32_16x16x32_bf16 a[104:107],a[212:215],v[120:123],a[104:107]
ds_read_b64_tr_b16 a[208:209],v2 offset:256
v_cvt_pk_bf16_f32 v65,v66,v67
v_mfma_f32_16x16x32_bf16 a[108:111],a[216:219],v[112:115],0
global_load_lds_dwordx4 v4,s[10:11] offset:2048
ds_read_b64_tr_b16 a[210:211],v2 offset:4352
v_mfma_f32_16x16x32_bf16 a[108:111],a[220:223],v[120:123],a[108:111]
ds_read_b64_tr_b16 a[212:213],v2 offset:8448
ds_read_b64_tr_b16 a[214:215],v2 offset:12544
v_mfma_f32_16x16x32_bf16 a[112:115],a[224:227],v[112:115],0
ds_read_b64_tr_b16 a[216:217],v3 offset:256
ds_read_b64_tr_b16 a[218:219],v3 offset:4352
v_mfma_f32_16x16x32_bf16 a[112:115],a[228:231],v[120:123],a[112:115]
v_cvt_pk_bf16_f32 v66,v68,v69
v_cvt_pk_bf16_f32 v67,v70,v71
v_mfma_f32_16x16x32_bf16 a[116:119],a[232:235],v[112:115],0
v_exp_f32 v72,v72
v_mfma_f32_16x16x32_bf16 a[116:119],a[236:239],v[120:123],a[116:119]
v_exp_f32 v73,v73
v_mfma_f32_16x16x32_bf16 a[120:123],a[240:243],v[112:115],0
v_exp_f32 v74,v74
v_mfma_f32_16x16x32_bf16 a[120:123],a[244:247],v[120:123],a[120:123]
v_exp_f32 v75,v75
v_mfma_f32_16x16x32_bf16 a[124:127],a[248:251],v[112:115],0
v_exp_f32 v76,v76
v_mfma_f32_16x16x32_bf16 a[124:127],a[252:255],v[120:123],a[124:127]
v_exp_f32 v77,v77
v_mfma_f32_16x16x32_bf16 v[60:63],v[28:31],v[112:115],0
v_exp_f32 v78,v78
v_mfma_f32_16x16x32_bf16 v[60:63],v[28:31],v[120:123],v[60:63]
global_load_lds_dwordx4 v5,s[10:11] offset:3072
ds_read_b64_tr_b16 a[220:221],v3 offset:8448
v_mfma_f32_16x16x32_bf16 v[96:99],a[128:131],v[196:199],v[40:43]
v_exp_f32 v79,v79
v_mfma_f32_16x16x32_bf16 v[96:99],a[132:135],v[204:207],v[96:99]
ds_read_b64_tr_b16 a[222:223],v3 offset:12544
v_cvt_pk_bf16_f32 v72,v72,v73
v_mfma_f32_16x16x32_bf16 v[96:99],a[136:139],v[212:215],v[96:99]
v_cvt_pk_bf16_f32 v73,v74,v75
v_cvt_pk_bf16_f32 v74,v76,v77
v_mfma_f32_16x16x32_bf16 v[96:99],a[140:143],v[220:223],v[96:99]
ds_read_b64_tr_b16 a[224:225],v2 offset:512
v_cvt_pk_bf16_f32 v75,v78,v79
v_mfma_f32_16x16x32_bf16 v[100:103],a[156:159],v[220:223],v[40:43]
ds_read_b64_tr_b16 a[226:227],v2 offset:4608
ds_read_b64_tr_b16 a[228:229],v2 offset:8704
v_mfma_f32_16x16x32_bf16 v[100:103],a[152:155],v[212:215],v[100:103]
ds_read_b64_tr_b16 a[230:231],v2 offset:12800
ds_read_b64_tr_b16 a[232:233],v3 offset:512
v_mfma_f32_16x16x32_bf16 v[100:103],a[148:151],v[204:207],v[100:103]
ds_read_b64_tr_b16 a[234:235],v3 offset:4608
ds_read_b64_tr_b16 a[236:237],v3 offset:8704
v_mfma_f32_16x16x32_bf16 v[100:103],a[144:147],v[196:199],v[100:103]
ds_read_b64_tr_b16 a[238:239],v3 offset:12800
ds_read_b64_tr_b16 a[240:241],v2 offset:768
v_mfma_f32_16x16x32_bf16 v[104:107],a[160:163],v[196:199],v[40:43]
ds_read_b64_tr_b16 a[242:243],v2 offset:4864
v_add_u32 v7,s69,v6
v_mfma_f32_16x16x32_bf16 v[104:107],a[164:167],v[204:207],v[104:107]
s_add_u32 s10,s20,s74
s_addc_u32 s11,s21,0
ds_read_b64_tr_b16 a[244:245],v2 offset:8960
v_mfma_f32_16x16x32_bf16 v[104:107],a[168:171],v[212:215],v[104:107]
s_and_b32 s69,s75,0xfffc0000
ds_read_b64_tr_b16 a[246:247],v2 offset:13056
ds_read_b64_tr_b16 a[248:249],v3 offset:768
v_mfma_f32_16x16x32_bf16 v[104:107],a[172:175],v[220:223],v[104:107]
s_add_u32 m0,s76,0
ds_read_b64_tr_b16 a[250:251],v3 offset:4864
ds_read_b64_tr_b16 a[252:253],v3 offset:8960
v_mfma_f32_16x16x32_bf16 v[108:111],a[188:191],v[220:223],v[40:43]
s_add_u32 s75,s75,16384
ds_read_b64_tr_b16 a[254:255],v3 offset:13056
v_add_u32 v2,s78,v2
v_mfma_f32_16x16x32_bf16 v[108:111],a[184:187],v[212:215],v[108:111]
global_load_lds_dwordx4 v4,s[10:11] nt
v_add_u32 v3,s78,v3
v_mfma_f32_16x16x32_bf16 v[108:111],a[180:183],v[204:207],v[108:111]
s_sub_u32 s78,s77,32768
v_exp_f32 v80,v80
v_mfma_f32_16x16x32_bf16 v[108:111],a[176:179],v[196:199],v[108:111]
s_and_b32 s78,s78,0x1ffff
v_exp_f32 v81,v81
s_waitcnt lgkmcnt(7)
v_mfma_f32_16x16x32_bf16 a[0:3],a[192:195],v[64:67],a[0:3]
s_sub_u32 s78,s78,s77
v_exp_f32 v82,v82
v_mfma_f32_16x16x32_bf16 a[0:3],a[196:199],v[72:75],a[0:3]
s_add_u32 s77,s77,s78
v_exp_f32 v83,v83
v_mfma_f32_16x16x32_bf16 a[4:7],a[200:203],v[64:67],a[4:7]
v_exp_f32 v84,v84
v_mfma_f32_16x16x32_bf16 a[4:7],a[204:207],v[72:75],a[4:7]
v_exp_f32 v85,v85
v_mfma_f32_16x16x32_bf16 a[8:11],a[208:211],v[64:67],a[8:11]
v_exp_f32 v86,v86
s_nop 0
L_pd5_%=:
s_cbranch_execz L_trt0_%=
v_mfma_f32_16x16x32_bf16 a[8:11],a[212:215],v[72:75],a[8:11]
v_exp_f32 v87,v87
v_mfma_f32_16x16x32_bf16 a[12:15],a[216:219],v[64:67],a[12:15]
v_cvt_pk_bf16_f32 v80,v80,v81
v_cvt_pk_bf16_f32 v81,v82,v83
v_mfma_f32_16x16x32_bf16 a[12:15],a[220:223],v[72:75],a[12:15]
v_cvt_pk_bf16_f32 v82,v84,v85
v_cvt_pk_bf16_f32 v83,v86,v87
v_mfma_f32_16x16x32_bf16 a[16:19],a[224:227],v[64:67],a[16:19]
v_exp_f32 v88,v88
v_mfma_f32_16x16x32_bf16 a[16:19],a[228:231],v[72:75],a[16:19]
v_exp_f32 v89,v89
v_mfma_f32_16x16x32_bf16 a[20:23],a[232:235],v[64:67],a[20:23]
v_exp_f32 v90,v90
v_mfma_f32_16x16x32_bf16 a[20:23],a[236:239],v[72:75],a[20:23]
s_waitcnt lgkmcnt(0)
v_exp_f32 v91,v91
v_mfma_f32_16x16x32_bf16 a[24:27],a[240:243],v[64:67],a[24:27]
v_exp_f32 v92,v92
v_mfma_f32_16x16x32_bf16 a[24:27],a[244:247],v[72:75],a[24:27]
v_exp_f32 v93,v93
v_mfma_f32_16x16x32_bf16 a[28:31],a[248:251],v[64:67],a[28:31]
v_exp_f32 v94,v94
v_mfma_f32_16x16x32_bf16 a[28:31],a[252:255],v[72:75],a[28:31]
s_getpc_b64 s[92:93]
s_cmp_lg_u32 s4,0
s_cbranch_scc1 L_pace_%=
v_mfma_f32_16x16x32_bf16 v[48:51],v[28:31],v[64:67],v[48:51]
v_exp_f32 v95,v95
v_mfma_f32_16x16x32_bf16 v[48:51],v[28:31],v[72:75],v[48:51]
s_add_u32 s10,s20,s74
s_addc_u32 s11,s21,0
v_cvt_pk_bf16_f32 v88,v88,v89
s_waitcnt vmcnt(13)
s_barrier
v_mfma_f32_16x16x32_bf16 v[112:115],a[128:131],v[228:231],v[44:47]
s_add_u32 m0,s76,0
v_cvt_pk_bf16_f32 v89,v90,v91
v_cvt_pk_bf16_f32 v90,v92,v93
v_mfma_f32_16x16x32_bf16 v[112:115],a[132:135],v[236:239],v[112:115]
ds_read_b128 a[128:131],v1
v_cvt_pk_bf16_f32 v91,v94,v95
v_mfma_f32_16x16x32_bf16 v[112:115],a[136:139],v[244:247],v[112:115]
global_load_lds_dwordx4 v4,s[10:11] offset:1024 nt
ds_read_b128 a[132:135],v1 offset:256
v_mfma_f32_16x16x32_bf16 v[112:115],a[140:143],v[252:255],v[112:115]
v_exp_f32 v96,v96
v_mfma_f32_16x16x32_bf16 v[116:119],a[156:159],v[252:255],v[44:47]
v_exp_f32 v97,v97
v_mfma_f32_16x16x32_bf16 v[116:119],a[152:155],v[244:247],v[116:119]
ds_read_b128 a[136:139],v1 offset:512
v_cvt_pk_bf16_f32 v96,v96,v97
v_mfma_f32_16x16x32_bf16 v[116:119],a[148:151],v[236:239],v[116:119]
v_exp_f32 v98,v98
v_mfma_f32_16x16x32_bf16 v[116:119],a[144:147],v[228:231],v[116:119]
v_exp_f32 v99,v99
v_mfma_f32_16x16x32_bf16 v[120:123],a[160:163],v[228:231],v[44:47]
ds_read_b128 a[140:143],v1 offset:768
v_cvt_pk_bf16_f32 v97,v98,v99
v_mfma_f32_16x16x32_bf16 v[120:123],a[164:167],v[236:239],v[120:123]
v_exp_f32 v100,v100
v_mfma_f32_16x16x32_bf16 v[120:123],a[168:171],v[244:247],v[120:123]
v_exp_f32 v101,v101
v_mfma_f32_16x16x32_bf16 v[120:123],a[172:175],v[252:255],v[120:123]
ds_read_b128 a[156:159],v1 offset:4864
v_cvt_pk_bf16_f32 v98,v100,v101
v_mfma_f32_16x16x32_bf16 v[124:127],a[188:191],v[252:255],v[44:47]
v_exp_f32 v102,v102
v_mfma_f32_16x16x32_bf16 v[124:127],a[184:187],v[244:247],v[124:127]
v_exp_f32 v103,v103
v_mfma_f32_16x16x32_bf16 v[124:127],a[180:183],v[236:239],v[124:127]
ds_read_b128 a[152:155],v1 offset:4608
v_cvt_pk_bf16_f32 v99,v102,v103
v_mfma_f32_16x16x32_bf16 v[124:127],a[176:179],v[228:231],v[124:127]
v_exp_f32 v104,v104
v_mfma_f32_16x16x32_bf16 a[32:35],a[192:195],v[80:83],a[32:35]
global_load_lds_dwordx4 v5,s[10:11] offset:2048 nt
ds_read_b128 a[148:151],v1 offset:4352
v_mfma_f32_16x16x32_bf16 a[32:35],a[196:199],v[88:91],a[32:35]
v_exp_f32 v105,v105
v_mfma_f32_16x16x32_bf16 a[36:39],a[200:203],v[80:83],a[36:39]
ds_read_b128 a[144:147],v1 offset:4096
v_cvt_pk_bf16_f32 v104,v104,v105
v_mfma_f32_16x16x32_bf16 a[36:39],a[204:207],v[88:91],a[36:39]
v_exp_f32 v106,v106
v_mfma_f32_16x16x32_bf16 a[40:43],a[208:211],v[80:83],a[40:43]
v_exp_f32 v107,v107
v_mfma_f32_16x16x32_bf16 a[40:43],a[212:215],v[88:91],a[40:43]
ds_read_b128 a[160:163],v1 offset:8192
v_cvt_pk_bf16_f32 v105,v106,v107
v_mfma_f32_16x16x32_bf16 a[44:47],a[216:219],v[80:83],a[44:47]
v_exp_f32 v108,v108
v_mfma_f32_16x16x32_bf16 a[44:47],a[220:223],v[88:91],a[44:47]
v_exp_f32 v109,v109
v_mfma_f32_16x16x32_bf16 a[48:51],a[224:227],v[80:83],a[48:51]
ds_read_b128 a[164:167],v1 offset:8448
v_cvt_pk_bf16_f32 v106,v108,v109
v_mfma_f32_16x16x32_bf16 a[48:51],a[228:231],v[88:91],a[48:51]
v_exp_f32 v110,v110
v_mfma_f32_16x16x32_bf16 a[52:55],a[232:235],v[80:83],a[52:55]
v_exp_f32 v111,v111
v_mfma_f32_16x16x32_bf16 a[52:55],a[236:239],v[88:91],a[52:55]
ds_read_b128 a[168:171],v1 offset:8704
v_cvt_pk_bf16_f32 v107,v110,v111
v_mfma_f32_16x16x32_bf16 a[56:59],a[240:243],v[80:83],a[56:59]
v_exp_f32 v112,v112
v_mfma_f32_16x16x32_bf16 a[56:59],a[244:247],v[88:91],a[56:59]
v_exp_f32 v113,v113
v_mfma_f32_16x16x32_bf16 a[60:63],a[248:251],v[80:83],a[60:63]
global_load_lds_dwordx4 v5,s[10:11] offset:3072
ds_read_b128 a[172:175],v1 offset:8960
v_mfma_f32_16x16x32_bf16 a[60:63],a[252:255],v[88:91],a[60:63]
ds_read_b128 a[188:191],v1 offset:13056
v_cvt_pk_bf16_f32 v112,v112,v113
v_mfma_f32_16x16x32_bf16 v[52:55],v[28:31],v[80:83],v[52:55]
v_exp_f32 v114,v114
v_mfma_f32_16x16x32_bf16 v[52:55],v[28:31],v[88:91],v[52:55]
s_waitcnt lgkmcnt(3)
v_exp_f32 v115,v115
v_mfma_f32_16x16x32_bf16 v[64:67],a[128:131],v[132:135],v[32:35]
ds_read_b128 a[184:187],v1 offset:12800
v_cvt_pk_bf16_f32 v113,v114,v115
v_mfma_f32_16x16x32_bf16 v[64:67],a[132:135],v[140:143],v[64:67]
v_exp_f32 v116,v116
v_mfma_f32_16x16x32_bf16 v[64:67],a[136:139],v[148:151],v[64:67]
ds_read_b128 a[180:183],v1 offset:12544
v_mfma_f32_16x16x32_bf16 v[64:67],a[140:143],v[156:159],v[64:67]
ds_read_b128 a[176:179],v1 offset:12288
v_add_u32 v1,s78,v1
v_mfma_f32_16x16x32_bf16 v[68:71],a[156:159],v[156:159],v[32:35]
v_exp_f32 v117,v117
v_mfma_f32_16x16x32_bf16 v[68:71],a[152:155],v[148:151],v[68:71]
v_exp_f32 v118,v118
v_mfma_f32_16x16x32_bf16 v[68:71],a[148:151],v[140:143],v[68:71]
s_add_u32 s10,s24,s74
s_addc_u32 s11,s25,0
v_cvt_pk_bf16_f32 v114,v116,v117
v_mfma_f32_16x16x32_bf16 v[68:71],a[144:147],v[132:135],v[68:71]
s_add_u32 s74,s74,16384
v_exp_f32 v119,v119
v_mfma_f32_16x16x32_bf16 v[72:75],a[160:163],v[132:135],v[32:35]
s_add_u32 m0,s76,16384
v_exp_f32 v120,v120
v_mfma_f32_16x16x32_bf16 v[72:75],a[164:167],v[140:143],v[72:75]
s_and_b32 s74,s74,0x3ffff
v_exp_f32 v121,v121
s_waitcnt lgkmcnt(2)
v_mfma_f32_16x16x32_bf16 v[72:75],a[168:171],v[148:151],v[72:75]
global_load_lds_dwordx4 v4,s[10:11]
v_cvt_pk_bf16_f32 v115,v118,v119
v_mfma_f32_16x16x32_bf16 v[72:75],a[172:175],v[156:159],v[72:75]
s_or_b32 s74,s74,s69
v_exp_f32 v122,v122
v_mfma_f32_16x16x32_bf16 v[76:79],a[188:191],v[156:159],v[32:35]
s_sub_u32 s76,s76,32768
v_exp_f32 v123,v123
v_mfma_f32_16x16x32_bf16 v[76:79],a[184:187],v[148:151],v[76:79]
s_and_b32 s76,s76,0x1ffff
v_exp_f32 v124,v124
s_waitcnt lgkmcnt(0)
v_mfma_f32_16x16x32_bf16 v[76:79],a[180:183],v[140:143],v[76:79]
s_and_b32 s69,s75,0xfffc0000
v_exp_f32 v125,v125
v_mfma_f32_16x16x32_bf16 v[76:79],a[176:179],v[132:135],v[76:79]
v_exp_f32 v126,v126
v_mfma_f32_16x16x32_bf16 a[64:67],a[192:195],v[96:99],a[64:67]
v_exp_f32 v127,v127
v_mfma_f32_16x16x32_bf16 a[64:67],a[196:199],v[104:107],a[64:67]
v_cvt_pk_bf16_f32 v120,v120,v121
v_cvt_pk_bf16_f32 v121,v122,v123
v_mfma_f32_16x16x32_bf16 a[68:71],a[200:203],v[96:99],a[68:71]
v_cvt_pk_bf16_f32 v122,v124,v125
v_cvt_pk_bf16_f32 v123,v126,v127
v_mfma_f32_16x16x32_bf16 a[68:71],a[204:207],v[104:107],a[68:71]
v_exp_f32 v64,v64
v_mfma_f32_16x16x32_bf16 a[72:75],a[208:211],v[96:99],a[72:75]
v_exp_f32 v65,v65
v_mfma_f32_16x16x32_bf16 a[72:75],a[212:215],v[104:107],a[72:75]
)A"
R"A(v_exp_f32 v66,v66
v_mfma_f32_16x16x32_bf16 a[76:79],a[216:219],v[96:99],a[76:79]
v_exp_f32 v67,v67
v_mfma_f32_16x16x32_bf16 a[76:79],a[220:223],v[104:107],a[76:79]
v_exp_f32 v68,v68
v_mfma_f32_16x16x32_bf16 a[80:83],a[224:227],v[96:99],a[80:83]
global_load_lds_dwordx4 v5,s[10:11] offset:1024
v_cvt_pk_bf16_f32 v64,v64,v65
v_mfma_f32_16x16x32_bf16 a[80:83],a[228:231],v[104:107],a[80:83]
v_exp_f32 v69,v69
v_mfma_f32_16x16x32_bf16 a[84:87],a[232:235],v[96:99],a[84:87]
v_exp_f32 v70,v70
v_mfma_f32_16x16x32_bf16 a[84:87],a[236:239],v[104:107],a[84:87]
v_exp_f32 v71,v71
v_mfma_f32_16x16x32_bf16 a[88:91],a[240:243],v[96:99],a[88:91]
v_cvt_pk_bf16_f32 v65,v66,v67
v_cvt_pk_bf16_f32 v66,v68,v69
v_mfma_f32_16x16x32_bf16 a[88:91],a[244:247],v[104:107],a[88:91]
v_exp_f32 v72,v72
v_mfma_f32_16x16x32_bf16 a[92:95],a[248:251],v[96:99],a[92:95]
v_exp_f32 v73,v73
v_mfma_f32_16x16x32_bf16 a[92:95],a[252:255],v[104:107],a[92:95]
v_cvt_pk_bf16_f32 v67,v70,v71
v_cvt_pk_bf16_f32 v72,v72,v73
v_mfma_f32_16x16x32_bf16 v[56:59],v[28:31],v[96:99],v[56:59]
v_exp_f32 v74,v74
v_mfma_f32_16x16x32_bf16 v[56:59],v[28:31],v[104:107],v[56:59]
v_exp_f32 v75,v75
v_mfma_f32_16x16x32_bf16 v[80:83],a[128:131],v[164:167],v[36:39]
v_exp_f32 v76,v76
v_mfma_f32_16x16x32_bf16 v[80:83],a[132:135],v[172:175],v[80:83]
v_exp_f32 v77,v77
v_mfma_f32_16x16x32_bf16 v[80:83],a[136:139],v[180:183],v[80:83]
v_exp_f32 v78,v78
v_mfma_f32_16x16x32_bf16 v[80:83],a[140:143],v[188:191],v[80:83]
v_exp_f32 v79,v79
v_mfma_f32_16x16x32_bf16 v[84:87],a[156:159],v[188:191],v[36:39]
v_cvt_pk_bf16_f32 v73,v74,v75
v_cvt_pk_bf16_f32 v74,v76,v77
v_mfma_f32_16x16x32_bf16 v[84:87],a[152:155],v[180:183],v[84:87]
global_load_lds_dwordx4 v4,s[10:11] offset:2048
v_cvt_pk_bf16_f32 v75,v78,v79
v_mfma_f32_16x16x32_bf16 v[84:87],a[148:151],v[172:175],v[84:87]
v_exp_f32 v80,v80
v_mfma_f32_16x16x32_bf16 v[84:87],a[144:147],v[164:167],v[84:87]
v_exp_f32 v81,v81
v_mfma_f32_16x16x32_bf16 v[88:91],a[160:163],v[164:167],v[36:39]
v_exp_f32 v82,v82
v_mfma_f32_16x16x32_bf16 v[88:91],a[164:167],v[172:175],v[88:91]
v_exp_f32 v83,v83
v_mfma_f32_16x16x32_bf16 v[88:91],a[168:171],v[180:183],v[88:91]
v_cvt_pk_bf16_f32 v80,v80,v81
v_cvt_pk_bf16_f32 v81,v82,v83
v_mfma_f32_16x16x32_bf16 v[88:91],a[172:175],v[188:191],v[88:91]
v_exp_f32 v84,v84
v_mfma_f32_16x16x32_bf16 v[92:95],a[188:191],v[188:191],v[36:39]
v_exp_f32 v85,v85
v_mfma_f32_16x16x32_bf16 v[92:95],a[184:187],v[180:183],v[92:95]
v_exp_f32 v86,v86
v_mfma_f32_16x16x32_bf16 v[92:95],a[180:183],v[172:175],v[92:95]
v_exp_f32 v87,v87
v_mfma_f32_16x16x32_bf16 v[92:95],a[176:179],v[164:167],v[92:95]
v_cvt_pk_bf16_f32 v82,v84,v85
v_cvt_pk_bf16_f32 v83,v86,v87
v_mfma_f32_16x16x32_bf16 a[96:99],a[192:195],v[112:115],a[96:99]
ds_read_b64_tr_b16 a[192:193],v2
ds_read_b64_tr_b16 a[194:195],v2 offset:4096
v_mfma_f32_16x16x32_bf16 a[96:99],a[196:199],v[120:123],a[96:99]
ds_read_b64_tr_b16 a[196:197],v2 offset:8192
ds_read_b64_tr_b16 a[198:199],v2 offset:12288
v_mfma_f32_16x16x32_bf16 a[100:103],a[200:203],v[112:115],a[100:103]
ds_read_b64_tr_b16 a[200:201],v3
ds_read_b64_tr_b16 a[202:203],v3 offset:4096
v_mfma_f32_16x16x32_bf16 a[100:103],a[204:207],v[120:123],a[100:103]
global_load_lds_dwordx4 v5,s[10:11] offset:3072
ds_read_b64_tr_b16 a[204:205],v3 offset:8192
v_mfma_f32_16x16x32_bf16 a[104:107],a[208:211],v[112:115],a[104:107]
ds_read_b64_tr_b16 a[206:207],v3 offset:12288
ds_read_b64_tr_b16 a[208:209],v2 offset:256
v_mfma_f32_16x16x32_bf16 a[104:107],a[212:215],v[120:123],a[104:107]
ds_read_b64_tr_b16 a[210:211],v2 offset:4352
ds_read_b64_tr_b16 a[212:213],v2 offset:8448
v_mfma_f32_16x16x32_bf16 a[108:111],a[216:219],v[112:115],a[108:111]
v_exp_f32 v88,v88
v_mfma_f32_16x16x32_bf16 a[108:111],a[220:223],v[120:123],a[108:111]
ds_read_b64_tr_b16 a[214:215],v2 offset:12544
ds_read_b64_tr_b16 a[216:217],v3 offset:256
v_mfma_f32_16x16x32_bf16 a[112:115],a[224:227],v[112:115],a[112:115]
ds_read_b64_tr_b16 a[218:219],v3 offset:4352
ds_read_b64_tr_b16 a[220:221],v3 offset:8448
v_mfma_f32_16x16x32_bf16 a[112:115],a[228:231],v[120:123],a[112:115]
ds_read_b64_tr_b16 a[222:223],v3 offset:12544
ds_read_b64_tr_b16 a[224:225],v2 offset:512
v_mfma_f32_16x16x32_bf16 a[116:119],a[232:235],v[112:115],a[116:119]
v_exp_f32 v89,v89
v_mfma_f32_16x16x32_bf16 a[116:119],a[236:239],v[120:123],a[116:119]
ds_read_b64_tr_b16 a[226:227],v2 offset:4608
ds_read_b64_tr_b16 a[228:229],v2 offset:8704
v_mfma_f32_16x16x32_bf16 a[120:123],a[240:243],v[112:115],a[120:123]
ds_read_b64_tr_b16 a[230:231],v2 offset:12800
ds_read_b64_tr_b16 a[232:233],v3 offset:512
v_mfma_f32_16x16x32_bf16 a[120:123],a[244:247],v[120:123],a[120:123]
s_add_u32 s10,s20,s74
s_addc_u32 s11,s21,0
ds_read_b64_tr_b16 a[234:235],v3 offset:4608
v_mfma_f32_16x16x32_bf16 a[124:127],a[248:251],v[112:115],a[124:127]
ds_read_b64_tr_b16 a[236:237],v3 offset:8704
ds_read_b64_tr_b16 a[238:239],v3 offset:12800
v_mfma_f32_16x16x32_bf16 a[124:127],a[252:255],v[120:123],a[124:127]
s_add_u32 m0,s76,0
ds_read_b64_tr_b16 a[240:241],v2 offset:768
ds_read_b64_tr_b16 a[242:243],v2 offset:4864
v_mfma_f32_16x16x32_bf16 v[60:63],v[28:31],v[112:115],v[60:63]
ds_read_b64_tr_b16 a[244:245],v2 offset:8960
v_cvt_pk_bf16_f32 v88,v88,v89
v_mfma_f32_16x16x32_bf16 v[60:63],v[28:31],v[120:123],v[60:63]
global_load_lds_dwordx4 v4,s[10:11] nt
ds_read_b64_tr_b16 a[246:247],v2 offset:13056
v_mfma_f32_16x16x32_bf16 v[96:99],a[128:131],v[196:199],v[40:43]
ds_read_b64_tr_b16 a[248:249],v3 offset:768
ds_read_b64_tr_b16 a[250:251],v3 offset:4864
v_mfma_f32_16x16x32_bf16 v[96:99],a[132:135],v[204:207],v[96:99]
ds_read_b64_tr_b16 a[252:253],v3 offset:8960
ds_read_b64_tr_b16 a[254:255],v3 offset:13056
v_mfma_f32_16x16x32_bf16 v[96:99],a[136:139],v[212:215],v[96:99]
v_exp_f32 v90,v90
v_mfma_f32_16x16x32_bf16 v[96:99],a[140:143],v[220:223],v[96:99]
v_exp_f32 v91,v91
v_mfma_f32_16x16x32_bf16 v[100:103],a[156:159],v[220:223],v[40:43]
v_exp_f32 v92,v92
v_mfma_f32_16x16x32_bf16 v[100:103],a[152:155],v[212:215],v[100:103]
v_exp_f32 v93,v93
v_mfma_f32_16x16x32_bf16 v[100:103],a[148:151],v[204:207],v[100:103]
v_exp_f32 v94,v94
v_mfma_f32_16x16x32_bf16 v[100:103],a[144:147],v[196:199],v[100:103]
v_exp_f32 v95,v95
v_mfma_f32_16x16x32_bf16 v[104:107],a[160:163],v[196:199],v[40:43]
v_cvt_pk_bf16_f32 v89,v90,v91
v_cvt_pk_bf16_f32 v90,v92,v93
v_mfma_f32_16x16x32_bf16 v[104:107],a[164:167],v[204:207],v[104:107]
v_cvt_pk_bf16_f32 v91,v94,v95
v_add_u32 v2,s78,v2
v_mfma_f32_16x16x32_bf16 v[104:107],a[168:171],v[212:215],v[104:107]
v_exp_f32 v96,v96
v_mfma_f32_16x16x32_bf16 v[104:107],a[172:175],v[220:223],v[104:107]
v_exp_f32 v97,v97
v_mfma_f32_16x16x32_bf16 v[108:111],a[188:191],v[220:223],v[40:43]
v_exp_f32 v98,v98
v_mfma_f32_16x16x32_bf16 v[108:111],a[184:187],v[212:215],v[108:111]
global_load_lds_dwordx4 v4,s[10:11] offset:1024 nt
v_cvt_pk_bf16_f32 v96,v96,v97
v_mfma_f32_16x16x32_bf16 v[108:111],a[180:183],v[204:207],v[108:111]
v_exp_f32 v99,v99
v_mfma_f32_16x16x32_bf16 v[108:111],a[176:179],v[196:199],v[108:111]
s_waitcnt lgkmcnt(0)
v_exp_f32 v100,v100
v_mfma_f32_16x16x32_bf16 a[0:3],a[192:195],v[64:67],a[0:3]
v_exp_f32 v101,v101
v_mfma_f32_16x16x32_bf16 a[0:3],a[196:199],v[72:75],a[0:3]
v_exp_f32 v102,v102
v_mfma_f32_16x16x32_bf16 a[4:7],a[200:203],v[64:67],a[4:7]
v_exp_f32 v103,v103
v_mfma_f32_16x16x32_bf16 a[4:7],a[204:207],v[72:75],a[4:7]
v_cvt_pk_bf16_f32 v97,v98,v99
v_cvt_pk_bf16_f32 v98,v100,v101
v_mfma_f32_16x16x32_bf16 a[8:11],a[208:211],v[64:67],a[8:11]
v_cvt_pk_bf16_f32 v99,v102,v103
v_add_u32 v3,s78,v3
v_mfma_f32_16x16x32_bf16 a[8:11],a[212:215],v[72:75],a[8:11]
s_sub_u32 s78,s77,32768
v_exp_f32 v104,v104
v_mfma_f32_16x16x32_bf16 a[12:15],a[216:219],v[64:67],a[12:15]
s_and_b32 s78,s78,0x1ffff
v_exp_f32 v105,v105
v_mfma_f32_16x16x32_bf16 a[12:15],a[220:223],v[72:75],a[12:15]
s_sub_u32 s78,s78,s77
v_exp_f32 v106,v106
v_mfma_f32_16x16x32_bf16 a[16:19],a[224:227],v[64:67],a[16:19]
s_add_u32 s77,s77,s78
v_exp_f32 v107,v107
v_mfma_f32_16x16x32_bf16 a[16:19],a[228:231],v[72:75],a[16:19]
v_exp_f32 v108,v108
v_mfma_f32_16x16x32_bf16 a[20:23],a[232:235],v[64:67],a[20:23]
s_getreg_b32 s92,hwreg(HW_REG_IB_STS,0,4)
s_min_u32 s4,s92,s6
s_mov_b32 s6,s92
v_mfma_f32_16x16x32_bf16 a[20:23],a[236:239],v[72:75],a[20:23]
s_cmp_gt_u32 s4,s9
s_cselect_b32 s4,15,s5
v_cvt_pk_bf16_f32 v104,v104,v105
v_mfma_f32_16x16x32_bf16 a[24:27],a[240:243],v[64:67],a[24:27]
v_exp_f32 v109,v109
v_mfma_f32_16x16x32_bf16 a[24:27],a[244:247],v[72:75],a[24:27]
v_exp_f32 v110,v110
v_mfma_f32_16x16x32_bf16 a[28:31],a[248:251],v[64:67],a[28:31]
v_exp_f32 v111,v111
v_mfma_f32_16x16x32_bf16 a[28:31],a[252:255],v[72:75],a[28:31]
s_getpc_b64 s[92:93]
s_cmp_lg_u32 s4,0
s_cbranch_scc1 L_pace_%=
v_mfma_f32_16x16x32_bf16 v[48:51],v[28:31],v[64:67],v[48:51]
v_cvt_pk_bf16_f32 v105,v106,v107
v_cvt_pk_bf16_f32 v106,v108,v109
v_mfma_f32_16x16x32_bf16 v[48:51],v[28:31],v[72:75],v[48:51]
s_add_u32 s10,s20,s74
s_addc_u32 s11,s21,0
v_cvt_pk_bf16_f32 v107,v110,v111
s_waitcnt vmcnt(10)
s_barrier
v_mfma_f32_16x16x32_bf16 v[24:27],v[8:11],v[128:131],0
s_add_u32 m0,s76,0
v_mfma_f32_16x16x32_bf16 v[24:27],v[12:15],v[136:139],v[24:27]
v_mfma_f32_16x16x32_bf16 v[24:27],v[16:19],v[144:147],v[24:27]
global_load_lds_dwordx4 v5,s[10:11] offset:2048 nt
v_mfma_f32_16x16x32_bf16 v[24:27],v[20:23],v[152:155],v[24:27]
v_mfma_f32_16x16x32_bf16 v[112:115],a[128:131],v[228:231],v[44:47]
ds_read_b128 a[128:131],v1
v_mfma_f32_16x16x32_bf16 v[112:115],a[132:135],v[236:239],v[112:115]
ds_read_b128 a[132:135],v1 offset:256
v_mfma_f32_16x16x32_bf16 v[112:115],a[136:139],v[244:247],v[112:115]
ds_read_b128 a[136:139],v1 offset:512
v_mfma_f32_16x16x32_bf16 v[112:115],a[140:143],v[252:255],v[112:115]
ds_read_b128 a[140:143],v1 offset:768
v_mfma_f32_16x16x32_bf16 v[116:119],a[156:159],v[252:255],v[44:47]
v_max3_f32 v24,v24,v25,v26
v_max_f32 v24,v24,v27
v_mfma_f32_16x16x32_bf16 v[116:119],a[152:155],v[244:247],v[116:119]
ds_read_b128 a[156:159],v1 offset:4864
v_mov_b32 v25,v24
v_mfma_f32_16x16x32_bf16 v[116:119],a[148:151],v[236:239],v[116:119]
v_exp_f32 v112,v112
v_mfma_f32_16x16x32_bf16 v[116:119],a[144:147],v[228:231],v[116:119]
v_permlane32_swap_b32 v24,v25
v_mfma_f32_16x16x32_bf16 v[120:123],a[160:163],v[228:231],v[44:47]
v_max_f32 v24,v24,v25
v_mov_b32 v25,v24
v_mfma_f32_16x16x32_bf16 v[120:123],a[164:167],v[236:239],v[120:123]
v_exp_f32 v113,v113
v_mfma_f32_16x16x32_bf16 v[120:123],a[168:171],v[244:247],v[120:123]
v_permlane16_swap_b32 v24,v25
v_mfma_f32_16x16x32_bf16 v[120:123],a[172:175],v[252:255],v[120:123]
v_max_f32 v24,v24,v25
v_sub_f32 v27,0,v24
v_mfma_f32_16x16x32_bf16 v[124:127],a[188:191],v[252:255],v[44:47]
global_load_lds_dwordx4 v5,s[10:11] offset:3072
v_sub_f32 v26,0,v24
v_sub_f32 v25,0,v24
v_sub_f32 v24,0,v24
v_mfma_f32_16x16x32_bf16 v[124:127],a[184:187],v[244:247],v[124:127]
ds_write_b128 v7,v[24:27]
v_mfma_f32_16x16x32_bf16 v[124:127],a[180:183],v[236:239],v[124:127]
ds_read_b128 a[152:155],v1 offset:4608
v_cvt_pk_bf16_f32 v112,v112,v113
v_mfma_f32_16x16x32_bf16 v[124:127],a[176:179],v[228:231],v[124:127]
v_exp_f32 v114,v114
v_mfma_f32_16x16x32_bf16 v[24:27],v[8:11],v[160:163],0
v_exp_f32 v115,v115
v_mfma_f32_16x16x32_bf16 v[24:27],v[12:15],v[168:171],v[24:27]
ds_read_b128 a[148:151],v1 offset:4352
v_cvt_pk_bf16_f32 v113,v114,v115
v_mfma_f32_16x16x32_bf16 v[24:27],v[16:19],v[176:179],v[24:27]
v_exp_f32 v116,v116
v_mfma_f32_16x16x32_bf16 v[24:27],v[20:23],v[184:187],v[24:27]
v_exp_f32 v117,v117
v_mfma_f32_16x16x32_bf16 a[32:35],a[192:195],v[80:83],a[32:35]
ds_read_b128 a[144:147],v1 offset:4096
v_cvt_pk_bf16_f32 v114,v116,v117
v_mfma_f32_16x16x32_bf16 a[32:35],a[196:199],v[88:91],a[32:35]
v_exp_f32 v118,v118
v_mfma_f32_16x16x32_bf16 a[36:39],a[200:203],v[80:83],a[36:39]
s_add_u32 s10,s24,s74
s_addc_u32 s11,s25,0
ds_read_b128 a[160:163],v1 offset:8192
v_mfma_f32_16x16x32_bf16 a[36:39],a[204:207],v[88:91],a[36:39]
s_add_u32 s74,s74,16384
v_max3_f32 v24,v24,v25,v26
v_max_f32 v24,v24,v27
v_mfma_f32_16x16x32_bf16 a[40:43],a[208:211],v[80:83],a[40:43]
s_add_u32 m0,s76,16384
ds_read_b128 a[164:167],v1 offset:8448
v_mov_b32 v25,v24
)A"
R"A(v_mfma_f32_16x16x32_bf16 a[40:43],a[212:215],v[88:91],a[40:43]
s_and_b32 s74,s74,0x3ffff
v_exp_f32 v119,v119
v_mfma_f32_16x16x32_bf16 a[44:47],a[216:219],v[80:83],a[44:47]
global_load_lds_dwordx4 v4,s[10:11]
ds_read_b128 a[168:171],v1 offset:8704
v_mfma_f32_16x16x32_bf16 a[44:47],a[220:223],v[88:91],a[44:47]
s_or_b32 s74,s74,s69
v_permlane32_swap_b32 v24,v25
v_mfma_f32_16x16x32_bf16 a[48:51],a[224:227],v[80:83],a[48:51]
s_sub_u32 s76,s76,32768
v_max_f32 v24,v24,v25
v_mov_b32 v25,v24
v_mfma_f32_16x16x32_bf16 a[48:51],a[228:231],v[88:91],a[48:51]
s_and_b32 s76,s76,0x1ffff
ds_read_b128 a[172:175],v1 offset:8960
v_cvt_pk_bf16_f32 v115,v118,v119
v_mfma_f32_16x16x32_bf16 a[52:55],a[232:235],v[80:83],a[52:55]
v_permlane16_swap_b32 v24,v25
v_mfma_f32_16x16x32_bf16 a[52:55],a[236:239],v[88:91],a[52:55]
v_max_f32 v24,v24,v25
v_sub_f32 v27,0,v24
v_mfma_f32_16x16x32_bf16 a[56:59],a[240:243],v[80:83],a[56:59]
v_sub_f32 v26,0,v24
v_sub_f32 v25,0,v24
v_mfma_f32_16x16x32_bf16 a[56:59],a[244:247],v[88:91],a[56:59]
ds_read_b128 a[188:191],v1 offset:13056
v_sub_f32 v24,0,v24
v_mfma_f32_16x16x32_bf16 a[60:63],a[248:251],v[80:83],a[60:63]
ds_write_b128 v7,v[24:27] offset:1024
v_mfma_f32_16x16x32_bf16 a[60:63],a[252:255],v[88:91],a[60:63]
v_exp_f32 v120,v120
v_mfma_f32_16x16x32_bf16 v[52:55],v[28:31],v[80:83],v[52:55]
v_exp_f32 v121,v121
v_mfma_f32_16x16x32_bf16 v[52:55],v[28:31],v[88:91],v[52:55]
ds_read_b128 a[184:187],v1 offset:12800
v_cvt_pk_bf16_f32 v120,v120,v121
v_mfma_f32_16x16x32_bf16 v[24:27],v[8:11],v[192:195],0
v_exp_f32 v122,v122
v_mfma_f32_16x16x32_bf16 v[24:27],v[12:15],v[200:203],v[24:27]
v_exp_f32 v123,v123
v_mfma_f32_16x16x32_bf16 v[24:27],v[16:19],v[208:211],v[24:27]
s_waitcnt lgkmcnt(2)
ds_read_b128 a[180:183],v1 offset:12544
v_cvt_pk_bf16_f32 v121,v122,v123
v_mfma_f32_16x16x32_bf16 v[24:27],v[20:23],v[216:219],v[24:27]
global_load_lds_dwordx4 v5,s[10:11] offset:1024
ds_read_b128 a[176:179],v1 offset:12288
v_mfma_f32_16x16x32_bf16 v[64:67],a[128:131],v[132:135],v[32:35]
v_exp_f32 v124,v124
v_mfma_f32_16x16x32_bf16 v[64:67],a[132:135],v[140:143],v[64:67]
v_exp_f32 v125,v125
v_mfma_f32_16x16x32_bf16 v[64:67],a[136:139],v[148:151],v[64:67]
v_exp_f32 v126,v126
v_mfma_f32_16x16x32_bf16 v[64:67],a[140:143],v[156:159],v[64:67]
v_max3_f32 v24,v24,v25,v26
v_max_f32 v24,v24,v27
v_mfma_f32_16x16x32_bf16 v[68:71],a[156:159],v[156:159],v[32:35]
v_mov_b32 v25,v24
v_cvt_pk_bf16_f32 v122,v124,v125
v_mfma_f32_16x16x32_bf16 v[68:71],a[152:155],v[148:151],v[68:71]
v_exp_f32 v127,v127
v_mfma_f32_16x16x32_bf16 v[68:71],a[148:151],v[140:143],v[68:71]
v_permlane32_swap_b32 v24,v25
v_mfma_f32_16x16x32_bf16 v[68:71],a[144:147],v[132:135],v[68:71]
v_max_f32 v24,v24,v25
v_mov_b32 v25,v24
v_mfma_f32_16x16x32_bf16 v[72:75],a[160:163],v[132:135],v[32:35]
v_cvt_pk_bf16_f32 v123,v126,v127
v_add_u32 v1,s78,v1
v_mfma_f32_16x16x32_bf16 v[72:75],a[164:167],v[140:143],v[72:75]
v_permlane16_swap_b32 v24,v25
v_mfma_f32_16x16x32_bf16 v[72:75],a[168:171],v[148:151],v[72:75]
v_max_f32 v24,v24,v25
v_sub_f32 v27,0,v24
v_mfma_f32_16x16x32_bf16 v[72:75],a[172:175],v[156:159],v[72:75]
v_sub_f32 v26,0,v24
v_sub_f32 v25,0,v24
v_mfma_f32_16x16x32_bf16 v[76:79],a[188:191],v[156:159],v[32:35]
s_waitcnt lgkmcnt(0)
v_exp_f32 v64,v64
v_mfma_f32_16x16x32_bf16 v[76:79],a[184:187],v[148:151],v[76:79]
global_load_lds_dwordx4 v4,s[10:11] offset:2048
v_sub_f32 v24,0,v24
v_mfma_f32_16x16x32_bf16 v[76:79],a[180:183],v[140:143],v[76:79]
ds_write_b128 v7,v[24:27] offset:2048
v_mfma_f32_16x16x32_bf16 v[76:79],a[176:179],v[132:135],v[76:79]
v_exp_f32 v65,v65
v_mfma_f32_16x16x32_bf16 a[64:67],a[192:195],v[96:99],a[64:67]
v_exp_f32 v66,v66
v_mfma_f32_16x16x32_bf16 a[64:67],a[196:199],v[104:107],a[64:67]
v_exp_f32 v67,v67
v_mfma_f32_16x16x32_bf16 a[68:71],a[200:203],v[96:99],a[68:71]
v_exp_f32 v68,v68
v_mfma_f32_16x16x32_bf16 a[68:71],a[204:207],v[104:107],a[68:71]
v_exp_f32 v69,v69
v_mfma_f32_16x16x32_bf16 a[72:75],a[208:211],v[96:99],a[72:75]
v_exp_f32 v70,v70
v_mfma_f32_16x16x32_bf16 a[72:75],a[212:215],v[104:107],a[72:75]
v_exp_f32 v71,v71
v_mfma_f32_16x16x32_bf16 a[76:79],a[216:219],v[96:99],a[76:79]
v_cvt_pk_bf16_f32 v64,v64,v65
v_cvt_pk_bf16_f32 v65,v66,v67
v_mfma_f32_16x16x32_bf16 a[76:79],a[220:223],v[104:107],a[76:79]
v_cvt_pk_bf16_f32 v66,v68,v69
v_cvt_pk_bf16_f32 v67,v70,v71
v_mfma_f32_16x16x32_bf16 a[80:83],a[224:227],v[96:99],a[80:83]
v_exp_f32 v72,v72
v_mfma_f32_16x16x32_bf16 a[80:83],a[228:231],v[104:107],a[80:83]
v_exp_f32 v73,v73
v_mfma_f32_16x16x32_bf16 a[84:87],a[232:235],v[96:99],a[84:87]
v_exp_f32 v74,v74
v_mfma_f32_16x16x32_bf16 a[84:87],a[236:239],v[104:107],a[84:87]
v_exp_f32 v75,v75
v_mfma_f32_16x16x32_bf16 a[88:91],a[240:243],v[96:99],a[88:91]
global_load_lds_dwordx4 v5,s[10:11] offset:3072
v_cvt_pk_bf16_f32 v72,v72,v73
v_mfma_f32_16x16x32_bf16 a[88:91],a[244:247],v[104:107],a[88:91]
v_exp_f32 v76,v76
v_mfma_f32_16x16x32_bf16 a[92:95],a[248:251],v[96:99],a[92:95]
v_exp_f32 v77,v77
v_mfma_f32_16x16x32_bf16 a[92:95],a[252:255],v[104:107],a[92:95]
v_exp_f32 v78,v78
v_mfma_f32_16x16x32_bf16 v[56:59],v[28:31],v[96:99],v[56:59]
v_exp_f32 v79,v79
v_mfma_f32_16x16x32_bf16 v[56:59],v[28:31],v[104:107],v[56:59]
v_cvt_pk_bf16_f32 v73,v74,v75
v_cvt_pk_bf16_f32 v74,v76,v77
v_mfma_f32_16x16x32_bf16 v[24:27],v[8:11],v[224:227],0
v_cvt_pk_bf16_f32 v75,v78,v79
v_mfma_f32_16x16x32_bf16 v[24:27],v[12:15],v[232:235],v[24:27]
v_mfma_f32_16x16x32_bf16 v[24:27],v[16:19],v[240:243],v[24:27]
v_mfma_f32_16x16x32_bf16 v[24:27],v[20:23],v[248:251],v[24:27]
v_mfma_f32_16x16x32_bf16 v[80:83],a[128:131],v[164:167],v[36:39]
s_add_u32 s10,s20,s74
s_addc_u32 s11,s21,0
v_mfma_f32_16x16x32_bf16 v[80:83],a[132:135],v[172:175],v[80:83]
v_mfma_f32_16x16x32_bf16 v[80:83],a[136:139],v[180:183],v[80:83]
s_add_u32 m0,s76,0
v_mfma_f32_16x16x32_bf16 v[80:83],a[140:143],v[188:191],v[80:83]
v_mfma_f32_16x16x32_bf16 v[84:87],a[156:159],v[188:191],v[36:39]
global_load_lds_dwordx4 v4,s[10:11] nt
v_max3_f32 v24,v24,v25,v26
v_mfma_f32_16x16x32_bf16 v[84:87],a[152:155],v[180:183],v[84:87]
v_max_f32 v24,v24,v27
v_mov_b32 v25,v24
v_mfma_f32_16x16x32_bf16 v[84:87],a[148:151],v[172:175],v[84:87]
v_mfma_f32_16x16x32_bf16 v[84:87],a[144:147],v[164:167],v[84:87]
v_permlane32_swap_b32 v24,v25
v_mfma_f32_16x16x32_bf16 v[88:91],a[160:163],v[164:167],v[36:39]
v_max_f32 v24,v24,v25
v_mov_b32 v25,v24
v_mfma_f32_16x16x32_bf16 v[88:91],a[164:167],v[172:175],v[88:91]
v_exp_f32 v80,v80
v_mfma_f32_16x16x32_bf16 v[88:91],a[168:171],v[180:183],v[88:91]
v_permlane16_swap_b32 v24,v25
L_pd6_%=:
s_cbranch_execz L_trt0_%=
v_mfma_f32_16x16x32_bf16 v[88:91],a[172:175],v[188:191],v[88:91]
v_max_f32 v24,v24,v25
v_max_f32 v24,s72,v24
v_mfma_f32_16x16x32_bf16 v[92:95],a[188:191],v[188:191],v[36:39]
v_sub_f32 v27,0,v24
v_sub_f32 v26,0,v24
v_mfma_f32_16x16x32_bf16 v[92:95],a[184:187],v[180:183],v[92:95]
v_sub_f32 v25,0,v24
v_sub_f32 v24,0,v24
v_mfma_f32_16x16x32_bf16 v[92:95],a[180:183],v[172:175],v[92:95]
v_exp_f32 v81,v81
v_mfma_f32_16x16x32_bf16 v[92:95],a[176:179],v[164:167],v[92:95]
v_exp_f32 v82,v82
v_mfma_f32_16x16x32_bf16 a[96:99],a[192:195],v[112:115],a[96:99]
ds_read_b64_tr_b16 a[192:193],v2
ds_read_b64_tr_b16 a[194:195],v2 offset:4096
v_mfma_f32_16x16x32_bf16 a[96:99],a[196:199],v[120:123],a[96:99]
ds_read_b64_tr_b16 a[196:197],v2 offset:8192
ds_read_b64_tr_b16 a[198:199],v2 offset:12288
v_mfma_f32_16x16x32_bf16 a[100:103],a[200:203],v[112:115],a[100:103]
ds_read_b64_tr_b16 a[200:201],v3
ds_read_b64_tr_b16 a[202:203],v3 offset:4096
v_mfma_f32_16x16x32_bf16 a[100:103],a[204:207],v[120:123],a[100:103]
global_load_lds_dwordx4 v4,s[10:11] offset:1024 nt
ds_read_b64_tr_b16 a[204:205],v3 offset:8192
v_mfma_f32_16x16x32_bf16 a[104:107],a[208:211],v[112:115],a[104:107]
ds_read_b64_tr_b16 a[206:207],v3 offset:12288
ds_read_b64_tr_b16 a[208:209],v2 offset:256
v_mfma_f32_16x16x32_bf16 a[104:107],a[212:215],v[120:123],a[104:107]
ds_read_b64_tr_b16 a[210:211],v2 offset:4352
ds_read_b64_tr_b16 a[212:213],v2 offset:8448
v_mfma_f32_16x16x32_bf16 a[108:111],a[216:219],v[112:115],a[108:111]
ds_read_b64_tr_b16 a[214:215],v2 offset:12544
v_cvt_pk_bf16_f32 v80,v80,v81
v_mfma_f32_16x16x32_bf16 a[108:111],a[220:223],v[120:123],a[108:111]
ds_read_b64_tr_b16 a[216:217],v3 offset:256
ds_read_b64_tr_b16 a[218:219],v3 offset:4352
v_mfma_f32_16x16x32_bf16 a[112:115],a[224:227],v[112:115],a[112:115]
v_exp_f32 v83,v83
v_mfma_f32_16x16x32_bf16 a[112:115],a[228:231],v[120:123],a[112:115]
ds_read_b64_tr_b16 a[220:221],v3 offset:8448
ds_read_b64_tr_b16 a[222:223],v3 offset:12544
v_mfma_f32_16x16x32_bf16 a[116:119],a[232:235],v[112:115],a[116:119]
ds_read_b64_tr_b16 a[224:225],v2 offset:512
ds_read_b64_tr_b16 a[226:227],v2 offset:4608
v_mfma_f32_16x16x32_bf16 a[116:119],a[236:239],v[120:123],a[116:119]
ds_read_b64_tr_b16 a[228:229],v2 offset:8704
ds_read_b64_tr_b16 a[230:231],v2 offset:12800
v_mfma_f32_16x16x32_bf16 a[120:123],a[240:243],v[112:115],a[120:123]
ds_read_b64_tr_b16 a[232:233],v3 offset:512
v_cvt_pk_bf16_f32 v81,v82,v83
v_mfma_f32_16x16x32_bf16 a[120:123],a[244:247],v[120:123],a[120:123]
ds_read_b64_tr_b16 a[234:235],v3 offset:4608
ds_read_b64_tr_b16 a[236:237],v3 offset:8704
v_mfma_f32_16x16x32_bf16 a[124:127],a[248:251],v[112:115],a[124:127]
v_exp_f32 v84,v84
v_mfma_f32_16x16x32_bf16 a[124:127],a[252:255],v[120:123],a[124:127]
ds_read_b64_tr_b16 a[238:239],v3 offset:12800
ds_read_b64_tr_b16 a[240:241],v2 offset:768
v_mfma_f32_16x16x32_bf16 v[60:63],v[28:31],v[112:115],v[60:63]
ds_read_b64_tr_b16 a[242:243],v2 offset:4864
ds_read_b64_tr_b16 a[244:245],v2 offset:8960
v_mfma_f32_16x16x32_bf16 v[60:63],v[28:31],v[120:123],v[60:63]
global_load_lds_dwordx4 v5,s[10:11] offset:2048 nt
ds_read_b64_tr_b16 a[246:247],v2 offset:13056
v_mfma_f32_16x16x32_bf16 v[96:99],a[128:131],v[196:199],v[40:43]
ds_read_b64_tr_b16 a[248:249],v3 offset:768
ds_read_b64_tr_b16 a[250:251],v3 offset:4864
v_mfma_f32_16x16x32_bf16 v[96:99],a[132:135],v[204:207],v[96:99]
ds_read_b64_tr_b16 a[252:253],v3 offset:8960
ds_read_b64_tr_b16 a[254:255],v3 offset:13056
v_mfma_f32_16x16x32_bf16 v[96:99],a[136:139],v[212:215],v[96:99]
v_exp_f32 v85,v85
v_mfma_f32_16x16x32_bf16 v[96:99],a[140:143],v[220:223],v[96:99]
v_exp_f32 v86,v86
v_mfma_f32_16x16x32_bf16 v[100:103],a[156:159],v[220:223],v[40:43]
v_exp_f32 v87,v87
v_mfma_f32_16x16x32_bf16 v[100:103],a[152:155],v[212:215],v[100:103]
v_exp_f32 v88,v88
v_mfma_f32_16x16x32_bf16 v[100:103],a[148:151],v[204:207],v[100:103]
v_exp_f32 v89,v89
v_mfma_f32_16x16x32_bf16 v[100:103],a[144:147],v[196:199],v[100:103]
v_exp_f32 v90,v90
v_mfma_f32_16x16x32_bf16 v[104:107],a[160:163],v[196:199],v[40:43]
v_exp_f32 v91,v91
v_mfma_f32_16x16x32_bf16 v[104:107],a[164:167],v[204:207],v[104:107]
v_exp_f32 v92,v92
v_mfma_f32_16x16x32_bf16 v[104:107],a[168:171],v[212:215],v[104:107]
v_exp_f32 v93,v93
v_mfma_f32_16x16x32_bf16 v[104:107],a[172:175],v[220:223],v[104:107]
v_exp_f32 v94,v94
v_mfma_f32_16x16x32_bf16 v[108:111],a[188:191],v[220:223],v[40:43]
v_exp_f32 v95,v95
v_mfma_f32_16x16x32_bf16 v[108:111],a[184:187],v[212:215],v[108:111]
v_exp_f32 v96,v96
v_mfma_f32_16x16x32_bf16 v[108:111],a[180:183],v[204:207],v[108:111]
global_load_lds_dwordx4 v5,s[10:11] offset:3072
v_cvt_pk_bf16_f32 v82,v84,v85
v_mfma_f32_16x16x32_bf16 v[108:111],a[176:179],v[196:199],v[108:111]
s_waitcnt lgkmcnt(0)
v_exp_f32 v97,v97
v_mfma_f32_16x16x32_bf16 a[0:3],a[192:195],v[64:67],a[0:3]
v_exp_f32 v98,v98
v_mfma_f32_16x16x32_bf16 a[0:3],a[196:199],v[72:75],a[0:3]
v_exp_f32 v99,v99
v_mfma_f32_16x16x32_bf16 a[4:7],a[200:203],v[64:67],a[4:7]
v_exp_f32 v100,v100
v_mfma_f32_16x16x32_bf16 a[4:7],a[204:207],v[72:75],a[4:7]
v_exp_f32 v101,v101
v_mfma_f32_16x16x32_bf16 a[8:11],a[208:211],v[64:67],a[8:11]
v_exp_f32 v102,v102
v_mfma_f32_16x16x32_bf16 a[8:11],a[212:215],v[72:75],a[8:11]
v_exp_f32 v103,v103
v_mfma_f32_16x16x32_bf16 a[12:15],a[216:219],v[64:67],a[12:15]
ds_write_b128 v7,v[24:27] offset:3072
v_mfma_f32_16x16x32_bf16 a[12:15],a[220:223],v[72:75],a[12:15]
v_cvt_pk_bf16_f32 v83,v86,v87
v_cvt_pk_bf16_f32 v88,v88,v89
v_mfma_f32_16x16x32_bf16 a[16:19],a[224:227],v[64:67],a[16:19]
)A"
R"A(v_cvt_pk_bf16_f32 v89,v90,v91
v_cvt_pk_bf16_f32 v90,v92,v93
v_mfma_f32_16x16x32_bf16 a[16:19],a[228:231],v[72:75],a[16:19]
v_cvt_pk_bf16_f32 v91,v94,v95
v_cvt_pk_bf16_f32 v96,v96,v97
v_mfma_f32_16x16x32_bf16 a[20:23],a[232:235],v[64:67],a[20:23]
s_getreg_b32 s92,hwreg(HW_REG_IB_STS,0,4)
s_min_u32 s4,s92,s6
s_mov_b32 s6,s92
v_mfma_f32_16x16x32_bf16 a[20:23],a[236:239],v[72:75],a[20:23]
s_cmp_gt_u32 s4,s9
s_cselect_b32 s4,15,s5
v_cvt_pk_bf16_f32 v97,v98,v99
v_mfma_f32_16x16x32_bf16 a[24:27],a[240:243],v[64:67],a[24:27]
v_cvt_pk_bf16_f32 v98,v100,v101
v_cvt_pk_bf16_f32 v99,v102,v103
v_mfma_f32_16x16x32_bf16 a[24:27],a[244:247],v[72:75],a[24:27]
v_mfma_f32_16x16x32_bf16 a[28:31],a[248:251],v[64:67],a[28:31]
v_mfma_f32_16x16x32_bf16 a[28:31],a[252:255],v[72:75],a[28:31]
s_getpc_b64 s[92:93]
s_cmp_lg_u32 s4,0
s_cbranch_scc1 L_pace_%=
v_mfma_f32_16x16x32_bf16 v[48:51],v[28:31],v[64:67],v[48:51]
v_mfma_f32_16x16x32_bf16 v[48:51],v[28:31],v[72:75],v[48:51]
s_mov_b32 s70,11
L_a0_%=:
s_waitcnt vmcnt(12)
s_barrier
v_mfma_f32_16x16x32_bf16 v[112:115],a[128:131],v[228:231],v[44:47]
s_add_u32 s10,s24,s74
s_addc_u32 s11,s25,0
s_add_u32 m0,s76,16384
v_mfma_f32_16x16x32_bf16 v[112:115],a[132:135],v[236:239],v[112:115]
s_add_u32 s75,s75,16384
s_add_u32 s74,s74,16384
ds_read_b128 a[128:131],v1
v_mfma_f32_16x16x32_bf16 v[112:115],a[136:139],v[244:247],v[112:115]
s_and_b32 s74,s74,0x3ffff
global_load_lds_dwordx4 v4,s[10:11]
v_mfma_f32_16x16x32_bf16 v[112:115],a[140:143],v[252:255],v[112:115]
s_and_b32 s69,s75,0xfffc0000
s_or_b32 s74,s74,s69
ds_read_b128 a[132:135],v1 offset:256
v_mfma_f32_16x16x32_bf16 v[116:119],a[156:159],v[252:255],v[44:47]
s_sub_u32 s76,s76,32768
s_and_b32 s76,s76,0x1ffff
v_mfma_f32_16x16x32_bf16 v[116:119],a[152:155],v[244:247],v[116:119]
ds_read_b128 a[136:139],v1 offset:512
v_mfma_f32_16x16x32_bf16 v[116:119],a[148:151],v[236:239],v[116:119]
v_mfma_f32_16x16x32_bf16 v[116:119],a[144:147],v[228:231],v[116:119]
ds_read_b128 a[140:143],v1 offset:768
v_mfma_f32_16x16x32_bf16 v[120:123],a[160:163],v[228:231],v[44:47]
v_mfma_f32_16x16x32_bf16 v[120:123],a[164:167],v[236:239],v[120:123]
ds_read_b128 a[156:159],v1 offset:4864
v_mfma_f32_16x16x32_bf16 v[120:123],a[168:171],v[244:247],v[120:123]
v_mfma_f32_16x16x32_bf16 v[120:123],a[172:175],v[252:255],v[120:123]
ds_read_b128 a[152:155],v1 offset:4608
v_mfma_f32_16x16x32_bf16 v[124:127],a[188:191],v[252:255],v[44:47]
v_exp_f32 v104,v104
v_mfma_f32_16x16x32_bf16 v[124:127],a[184:187],v[244:247],v[124:127]
ds_read_b128 a[148:151],v1 offset:4352
v_add_u32 v2,s78,v2
v_mfma_f32_16x16x32_bf16 v[124:127],a[180:183],v[236:239],v[124:127]
v_exp_f32 v105,v105
v_mfma_f32_16x16x32_bf16 v[124:127],a[176:179],v[228:231],v[124:127]
ds_read_b128 a[144:147],v1 offset:4096
v_cvt_pk_bf16_f32 v104,v104,v105
v_mfma_f32_16x16x32_bf16 a[32:35],a[192:195],v[80:83],a[32:35]
v_exp_f32 v106,v106
v_mfma_f32_16x16x32_bf16 a[32:35],a[196:199],v[88:91],a[32:35]
ds_read_b128 a[160:163],v1 offset:8192
v_add_u32 v3,s78,v3
v_mfma_f32_16x16x32_bf16 a[36:39],a[200:203],v[80:83],a[36:39]
s_sub_u32 s78,s77,32768
global_load_lds_dwordx4 v5,s[10:11] offset:1024
v_mfma_f32_16x16x32_bf16 a[36:39],a[204:207],v[88:91],a[36:39]
s_and_b32 s78,s78,0x1ffff
v_exp_f32 v107,v107
v_mfma_f32_16x16x32_bf16 a[40:43],a[208:211],v[80:83],a[40:43]
s_sub_u32 s78,s78,s77
ds_read_b128 a[164:167],v1 offset:8448
v_cvt_pk_bf16_f32 v105,v106,v107
v_mfma_f32_16x16x32_bf16 a[40:43],a[212:215],v[88:91],a[40:43]
s_add_u32 s77,s77,s78
v_exp_f32 v108,v108
v_mfma_f32_16x16x32_bf16 a[44:47],a[216:219],v[80:83],a[44:47]
v_exp_f32 v109,v109
v_mfma_f32_16x16x32_bf16 a[44:47],a[220:223],v[88:91],a[44:47]
ds_read_b128 a[168:171],v1 offset:8704
v_cvt_pk_bf16_f32 v106,v108,v109
v_mfma_f32_16x16x32_bf16 a[48:51],a[224:227],v[80:83],a[48:51]
v_exp_f32 v110,v110
v_mfma_f32_16x16x32_bf16 a[48:51],a[228:231],v[88:91],a[48:51]
v_exp_f32 v111,v111
v_mfma_f32_16x16x32_bf16 a[52:55],a[232:235],v[80:83],a[52:55]
ds_read_b128 a[172:175],v1 offset:8960
v_cvt_pk_bf16_f32 v107,v110,v111
v_mfma_f32_16x16x32_bf16 a[52:55],a[236:239],v[88:91],a[52:55]
v_exp_f32 v112,v112
v_mfma_f32_16x16x32_bf16 a[56:59],a[240:243],v[80:83],a[56:59]
v_exp_f32 v113,v113
v_mfma_f32_16x16x32_bf16 a[56:59],a[244:247],v[88:91],a[56:59]
ds_read_b128 a[188:191],v1 offset:13056
v_cvt_pk_bf16_f32 v112,v112,v113
v_mfma_f32_16x16x32_bf16 a[60:63],a[248:251],v[80:83],a[60:63]
v_exp_f32 v114,v114
v_mfma_f32_16x16x32_bf16 a[60:63],a[252:255],v[88:91],a[60:63]
v_exp_f32 v115,v115
v_mfma_f32_16x16x32_bf16 v[52:55],v[28:31],v[80:83],v[52:55]
ds_read_b128 a[184:187],v1 offset:12800
v_cvt_pk_bf16_f32 v113,v114,v115
v_mfma_f32_16x16x32_bf16 v[52:55],v[28:31],v[88:91],v[52:55]
s_waitcnt lgkmcnt(3)
v_exp_f32 v116,v116
v_mfma_f32_16x16x32_bf16 v[64:67],a[128:131],v[132:135],v[32:35]
global_load_lds_dwordx4 v4,s[10:11] offset:2048
ds_read_b128 a[180:183],v1 offset:12544
v_mfma_f32_16x16x32_bf16 v[64:67],a[132:135],v[140:143],v[64:67]
v_exp_f32 v117,v117
v_mfma_f32_16x16x32_bf16 v[64:67],a[136:139],v[148:151],v[64:67]
ds_read_b128 a[176:179],v1 offset:12288
v_cvt_pk_bf16_f32 v114,v116,v117
v_mfma_f32_16x16x32_bf16 v[64:67],a[140:143],v[156:159],v[64:67]
v_exp_f32 v118,v118
v_mfma_f32_16x16x32_bf16 v[68:71],a[156:159],v[156:159],v[32:35]
v_exp_f32 v119,v119
v_mfma_f32_16x16x32_bf16 v[68:71],a[152:155],v[148:151],v[68:71]
v_cvt_pk_bf16_f32 v115,v118,v119
v_add_u32 v1,s78,v1
v_mfma_f32_16x16x32_bf16 v[68:71],a[148:151],v[140:143],v[68:71]
v_exp_f32 v120,v120
v_mfma_f32_16x16x32_bf16 v[68:71],a[144:147],v[132:135],v[68:71]
v_exp_f32 v121,v121
v_mfma_f32_16x16x32_bf16 v[72:75],a[160:163],v[132:135],v[32:35]
v_exp_f32 v122,v122
v_mfma_f32_16x16x32_bf16 v[72:75],a[164:167],v[140:143],v[72:75]
v_exp_f32 v123,v123
v_mfma_f32_16x16x32_bf16 v[72:75],a[168:171],v[148:151],v[72:75]
s_waitcnt lgkmcnt(0)
v_exp_f32 v124,v124
v_mfma_f32_16x16x32_bf16 v[72:75],a[172:175],v[156:159],v[72:75]
v_exp_f32 v125,v125
v_mfma_f32_16x16x32_bf16 v[76:79],a[188:191],v[156:159],v[32:35]
v_exp_f32 v126,v126
v_mfma_f32_16x16x32_bf16 v[76:79],a[184:187],v[148:151],v[76:79]
v_exp_f32 v127,v127
v_mfma_f32_16x16x32_bf16 v[76:79],a[180:183],v[140:143],v[76:79]
v_cvt_pk_bf16_f32 v120,v120,v121
v_cvt_pk_bf16_f32 v121,v122,v123
v_mfma_f32_16x16x32_bf16 v[76:79],a[176:179],v[132:135],v[76:79]
v_cvt_pk_bf16_f32 v122,v124,v125
v_cvt_pk_bf16_f32 v123,v126,v127
v_mfma_f32_16x16x32_bf16 a[64:67],a[192:195],v[96:99],a[64:67]
global_load_lds_dwordx4 v5,s[10:11] offset:3072
v_mfma_f32_16x16x32_bf16 a[64:67],a[196:199],v[104:107],a[64:67]
v_exp_f32 v64,v64
v_mfma_f32_16x16x32_bf16 a[68:71],a[200:203],v[96:99],a[68:71]
v_exp_f32 v65,v65
v_mfma_f32_16x16x32_bf16 a[68:71],a[204:207],v[104:107],a[68:71]
v_exp_f32 v66,v66
v_mfma_f32_16x16x32_bf16 a[72:75],a[208:211],v[96:99],a[72:75]
v_exp_f32 v67,v67
v_mfma_f32_16x16x32_bf16 a[72:75],a[212:215],v[104:107],a[72:75]
v_exp_f32 v68,v68
v_mfma_f32_16x16x32_bf16 a[76:79],a[216:219],v[96:99],a[76:79]
v_exp_f32 v69,v69
v_mfma_f32_16x16x32_bf16 a[76:79],a[220:223],v[104:107],a[76:79]
v_exp_f32 v70,v70
v_mfma_f32_16x16x32_bf16 a[80:83],a[224:227],v[96:99],a[80:83]
v_exp_f32 v71,v71
v_mfma_f32_16x16x32_bf16 a[80:83],a[228:231],v[104:107],a[80:83]
v_cvt_pk_bf16_f32 v64,v64,v65
v_cvt_pk_bf16_f32 v65,v66,v67
v_mfma_f32_16x16x32_bf16 a[84:87],a[232:235],v[96:99],a[84:87]
v_cvt_pk_bf16_f32 v66,v68,v69
v_cvt_pk_bf16_f32 v67,v70,v71
v_mfma_f32_16x16x32_bf16 a[84:87],a[236:239],v[104:107],a[84:87]
v_exp_f32 v72,v72
v_mfma_f32_16x16x32_bf16 a[88:91],a[240:243],v[96:99],a[88:91]
s_add_u32 s10,s20,s74
s_addc_u32 s11,s21,0
v_mfma_f32_16x16x32_bf16 a[88:91],a[244:247],v[104:107],a[88:91]
v_exp_f32 v73,v73
v_mfma_f32_16x16x32_bf16 a[92:95],a[248:251],v[96:99],a[92:95]
s_add_u32 m0,s76,0
v_exp_f32 v74,v74
v_mfma_f32_16x16x32_bf16 a[92:95],a[252:255],v[104:107],a[92:95]
v_exp_f32 v75,v75
v_mfma_f32_16x16x32_bf16 v[56:59],v[28:31],v[96:99],v[56:59]
global_load_lds_dwordx4 v4,s[10:11] nt
v_cvt_pk_bf16_f32 v72,v72,v73
v_mfma_f32_16x16x32_bf16 v[56:59],v[28:31],v[104:107],v[56:59]
v_exp_f32 v76,v76
v_mfma_f32_16x16x32_bf16 v[80:83],a[128:131],v[164:167],v[36:39]
v_exp_f32 v77,v77
v_mfma_f32_16x16x32_bf16 v[80:83],a[132:135],v[172:175],v[80:83]
v_exp_f32 v78,v78
v_mfma_f32_16x16x32_bf16 v[80:83],a[136:139],v[180:183],v[80:83]
v_exp_f32 v79,v79
v_mfma_f32_16x16x32_bf16 v[80:83],a[140:143],v[188:191],v[80:83]
v_cvt_pk_bf16_f32 v73,v74,v75
v_cvt_pk_bf16_f32 v74,v76,v77
v_mfma_f32_16x16x32_bf16 v[84:87],a[156:159],v[188:191],v[36:39]
v_cvt_pk_bf16_f32 v75,v78,v79
v_mfma_f32_16x16x32_bf16 v[84:87],a[152:155],v[180:183],v[84:87]
v_mfma_f32_16x16x32_bf16 v[84:87],a[148:151],v[172:175],v[84:87]
v_mfma_f32_16x16x32_bf16 v[84:87],a[144:147],v[164:167],v[84:87]
v_mfma_f32_16x16x32_bf16 v[88:91],a[160:163],v[164:167],v[36:39]
v_exp_f32 v80,v80
v_mfma_f32_16x16x32_bf16 v[88:91],a[164:167],v[172:175],v[88:91]
v_exp_f32 v81,v81
v_mfma_f32_16x16x32_bf16 v[88:91],a[168:171],v[180:183],v[88:91]
v_exp_f32 v82,v82
v_mfma_f32_16x16x32_bf16 v[88:91],a[172:175],v[188:191],v[88:91]
v_exp_f32 v83,v83
v_mfma_f32_16x16x32_bf16 v[92:95],a[188:191],v[188:191],v[36:39]
v_exp_f32 v84,v84
v_mfma_f32_16x16x32_bf16 v[92:95],a[184:187],v[180:183],v[92:95]
v_exp_f32 v85,v85
v_mfma_f32_16x16x32_bf16 v[92:95],a[180:183],v[172:175],v[92:95]
global_load_lds_dwordx4 v4,s[10:11] offset:1024 nt
v_cvt_pk_bf16_f32 v80,v80,v81
v_mfma_f32_16x16x32_bf16 v[92:95],a[176:179],v[164:167],v[92:95]
v_exp_f32 v86,v86
v_mfma_f32_16x16x32_bf16 a[96:99],a[192:195],v[112:115],a[96:99]
ds_read_b64_tr_b16 a[192:193],v2
ds_read_b64_tr_b16 a[194:195],v2 offset:4096
v_mfma_f32_16x16x32_bf16 a[96:99],a[196:199],v[120:123],a[96:99]
ds_read_b64_tr_b16 a[196:197],v2 offset:8192
v_cvt_pk_bf16_f32 v81,v82,v83
v_mfma_f32_16x16x32_bf16 a[100:103],a[200:203],v[112:115],a[100:103]
ds_read_b64_tr_b16 a[198:199],v2 offset:12288
v_cvt_pk_bf16_f32 v82,v84,v85
v_mfma_f32_16x16x32_bf16 a[100:103],a[204:207],v[120:123],a[100:103]
v_exp_f32 v87,v87
v_mfma_f32_16x16x32_bf16 a[104:107],a[208:211],v[112:115],a[104:107]
ds_read_b64_tr_b16 a[200:201],v3
ds_read_b64_tr_b16 a[202:203],v3 offset:4096
v_mfma_f32_16x16x32_bf16 a[104:107],a[212:215],v[120:123],a[104:107]
ds_read_b64_tr_b16 a[204:205],v3 offset:8192
v_cvt_pk_bf16_f32 v83,v86,v87
v_mfma_f32_16x16x32_bf16 a[108:111],a[216:219],v[112:115],a[108:111]
v_exp_f32 v88,v88
v_mfma_f32_16x16x32_bf16 a[108:111],a[220:223],v[120:123],a[108:111]
ds_read_b64_tr_b16 a[206:207],v3 offset:12288
ds_read_b64_tr_b16 a[208:209],v2 offset:256
v_mfma_f32_16x16x32_bf16 a[112:115],a[224:227],v[112:115],a[112:115]
v_exp_f32 v89,v89
v_mfma_f32_16x16x32_bf16 a[112:115],a[228:231],v[120:123],a[112:115]
ds_read_b64_tr_b16 a[210:211],v2 offset:4352
ds_read_b64_tr_b16 a[212:213],v2 offset:8448
v_mfma_f32_16x16x32_bf16 a[116:119],a[232:235],v[112:115],a[116:119]
ds_read_b64_tr_b16 a[214:215],v2 offset:12544
v_cvt_pk_bf16_f32 v88,v88,v89
v_mfma_f32_16x16x32_bf16 a[116:119],a[236:239],v[120:123],a[116:119]
v_exp_f32 v90,v90
v_mfma_f32_16x16x32_bf16 a[120:123],a[240:243],v[112:115],a[120:123]
ds_read_b64_tr_b16 a[216:217],v3 offset:256
ds_read_b64_tr_b16 a[218:219],v3 offset:4352
v_mfma_f32_16x16x32_bf16 a[120:123],a[244:247],v[120:123],a[120:123]
v_exp_f32 v91,v91
v_mfma_f32_16x16x32_bf16 a[124:127],a[248:251],v[112:115],a[124:127]
global_load_lds_dwordx4 v5,s[10:11] offset:2048 nt
ds_read_b64_tr_b16 a[220:221],v3 offset:8448
v_mfma_f32_16x16x32_bf16 a[124:127],a[252:255],v[120:123],a[124:127]
ds_read_b64_tr_b16 a[222:223],v3 offset:12544
ds_read_b64_tr_b16 a[224:225],v2 offset:512
v_mfma_f32_16x16x32_bf16 v[60:63],v[28:31],v[112:115],v[60:63]
ds_read_b64_tr_b16 a[226:227],v2 offset:4608
v_cvt_pk_bf16_f32 v89,v90,v91
v_mfma_f32_16x16x32_bf16 v[60:63],v[28:31],v[120:123],v[60:63]
v_exp_f32 v92,v92
v_mfma_f32_16x16x32_bf16 v[96:99],a[128:131],v[196:199],v[40:43]
ds_read_b64_tr_b16 a[228:229],v2 offset:8704
ds_read_b64_tr_b16 a[230:231],v2 offset:12800
v_mfma_f32_16x16x32_bf16 v[96:99],a[132:135],v[204:207],v[96:99]
v_exp_f32 v93,v93
v_mfma_f32_16x16x32_bf16 v[96:99],a[136:139],v[212:215],v[96:99]
)A"
R"A(ds_read_b64_tr_b16 a[232:233],v3 offset:512
ds_read_b64_tr_b16 a[234:235],v3 offset:4608
v_mfma_f32_16x16x32_bf16 v[96:99],a[140:143],v[220:223],v[96:99]
ds_read_b64_tr_b16 a[236:237],v3 offset:8704
v_cvt_pk_bf16_f32 v90,v92,v93
v_mfma_f32_16x16x32_bf16 v[100:103],a[156:159],v[220:223],v[40:43]
v_exp_f32 v94,v94
v_mfma_f32_16x16x32_bf16 v[100:103],a[152:155],v[212:215],v[100:103]
ds_read_b64_tr_b16 a[238:239],v3 offset:12800
ds_read_b64_tr_b16 a[240:241],v2 offset:768
v_mfma_f32_16x16x32_bf16 v[100:103],a[148:151],v[204:207],v[100:103]
v_exp_f32 v95,v95
v_mfma_f32_16x16x32_bf16 v[100:103],a[144:147],v[196:199],v[100:103]
ds_read_b64_tr_b16 a[242:243],v2 offset:4864
ds_read_b64_tr_b16 a[244:245],v2 offset:8960
v_mfma_f32_16x16x32_bf16 v[104:107],a[160:163],v[196:199],v[40:43]
ds_read_b64_tr_b16 a[246:247],v2 offset:13056
v_cvt_pk_bf16_f32 v91,v94,v95
v_mfma_f32_16x16x32_bf16 v[104:107],a[164:167],v[204:207],v[104:107]
v_exp_f32 v96,v96
v_mfma_f32_16x16x32_bf16 v[104:107],a[168:171],v[212:215],v[104:107]
ds_read_b64_tr_b16 a[248:249],v3 offset:768
ds_read_b64_tr_b16 a[250:251],v3 offset:4864
v_mfma_f32_16x16x32_bf16 v[104:107],a[172:175],v[220:223],v[104:107]
v_exp_f32 v97,v97
v_mfma_f32_16x16x32_bf16 v[108:111],a[188:191],v[220:223],v[40:43]
global_load_lds_dwordx4 v5,s[10:11] offset:3072
ds_read_b64_tr_b16 a[252:253],v3 offset:8960
v_mfma_f32_16x16x32_bf16 v[108:111],a[184:187],v[212:215],v[108:111]
ds_read_b64_tr_b16 a[254:255],v3 offset:13056
v_cvt_pk_bf16_f32 v96,v96,v97
v_mfma_f32_16x16x32_bf16 v[108:111],a[180:183],v[204:207],v[108:111]
v_exp_f32 v98,v98
v_mfma_f32_16x16x32_bf16 v[108:111],a[176:179],v[196:199],v[108:111]
s_waitcnt lgkmcnt(5)
v_exp_f32 v99,v99
v_mfma_f32_16x16x32_bf16 a[0:3],a[192:195],v[64:67],a[0:3]
v_exp_f32 v100,v100
v_mfma_f32_16x16x32_bf16 a[0:3],a[196:199],v[72:75],a[0:3]
v_exp_f32 v101,v101
v_mfma_f32_16x16x32_bf16 a[4:7],a[200:203],v[64:67],a[4:7]
v_exp_f32 v102,v102
v_mfma_f32_16x16x32_bf16 a[4:7],a[204:207],v[72:75],a[4:7]
v_exp_f32 v103,v103
v_mfma_f32_16x16x32_bf16 a[8:11],a[208:211],v[64:67],a[8:11]
v_cvt_pk_bf16_f32 v97,v98,v99
v_cvt_pk_bf16_f32 v98,v100,v101
v_mfma_f32_16x16x32_bf16 a[8:11],a[212:215],v[72:75],a[8:11]
v_cvt_pk_bf16_f32 v99,v102,v103
v_mfma_f32_16x16x32_bf16 a[12:15],a[216:219],v[64:67],a[12:15]
v_mfma_f32_16x16x32_bf16 a[12:15],a[220:223],v[72:75],a[12:15]
v_mfma_f32_16x16x32_bf16 a[16:19],a[224:227],v[64:67],a[16:19]
v_mfma_f32_16x16x32_bf16 a[16:19],a[228:231],v[72:75],a[16:19]
v_mfma_f32_16x16x32_bf16 a[20:23],a[232:235],v[64:67],a[20:23]
s_getreg_b32 s92,hwreg(HW_REG_IB_STS,0,4)
s_min_u32 s4,s92,s6
s_mov_b32 s6,s92
v_mfma_f32_16x16x32_bf16 a[20:23],a[236:239],v[72:75],a[20:23]
s_cmp_ge_u32 s4,s9
s_cselect_b32 s4,15,s5
v_mfma_f32_16x16x32_bf16 a[24:27],a[240:243],v[64:67],a[24:27]
s_waitcnt lgkmcnt(0)
v_mfma_f32_16x16x32_bf16 a[24:27],a[244:247],v[72:75],a[24:27]
v_mfma_f32_16x16x32_bf16 a[28:31],a[248:251],v[64:67],a[28:31]
v_mfma_f32_16x16x32_bf16 a[28:31],a[252:255],v[72:75],a[28:31]
s_getpc_b64 s[92:93]
s_cmp_lg_u32 s4,0
s_cbranch_scc1 L_pace_%=
v_mfma_f32_16x16x32_bf16 v[48:51],v[28:31],v[64:67],v[48:51]
v_mfma_f32_16x16x32_bf16 v[48:51],v[28:31],v[72:75],v[48:51]
s_sub_u32 s70,s70,1
s_cbranch_scc0 L_a0_%=
L_nx0_%=:
s_waitcnt vmcnt(12)
s_barrier
v_mfma_f32_16x16x32_bf16 v[112:115],a[128:131],v[228:231],v[44:47]
s_add_u32 s10,s24,s74
s_addc_u32 s11,s25,0
s_add_u32 m0,s76,16384
v_mfma_f32_16x16x32_bf16 v[112:115],a[132:135],v[236:239],v[112:115]
s_add_u32 s75,s75,16384
s_add_u32 s74,s74,16384
ds_read_b128 v[32:35],v7
v_mfma_f32_16x16x32_bf16 v[112:115],a[136:139],v[244:247],v[112:115]
s_and_b32 s74,s74,0x3ffff
global_load_lds_dwordx4 v4,s[10:11]
v_mfma_f32_16x16x32_bf16 v[112:115],a[140:143],v[252:255],v[112:115]
s_and_b32 s69,s75,0xfffc0000
s_or_b32 s74,s74,s69
ds_read_b128 a[128:131],v1
v_mfma_f32_16x16x32_bf16 v[116:119],a[156:159],v[252:255],v[44:47]
s_sub_u32 s76,s76,32768
v_exp_f32 v104,v104
v_mfma_f32_16x16x32_bf16 v[116:119],a[152:155],v[244:247],v[116:119]
s_and_b32 s76,s76,0x1ffff
ds_read_b128 a[132:135],v1 offset:256
v_add_u32 v2,s78,v2
v_mfma_f32_16x16x32_bf16 v[116:119],a[148:151],v[236:239],v[116:119]
v_exp_f32 v105,v105
v_mfma_f32_16x16x32_bf16 v[116:119],a[144:147],v[228:231],v[116:119]
ds_read_b128 a[136:139],v1 offset:512
v_cvt_pk_bf16_f32 v104,v104,v105
v_mfma_f32_16x16x32_bf16 v[120:123],a[160:163],v[228:231],v[44:47]
v_exp_f32 v106,v106
v_mfma_f32_16x16x32_bf16 v[120:123],a[164:167],v[236:239],v[120:123]
ds_read_b128 a[140:143],v1 offset:768
v_add_u32 v3,s78,v3
v_mfma_f32_16x16x32_bf16 v[120:123],a[168:171],v[244:247],v[120:123]
s_sub_u32 s78,s77,32768
v_exp_f32 v107,v107
v_mfma_f32_16x16x32_bf16 v[120:123],a[172:175],v[252:255],v[120:123]
s_and_b32 s78,s78,0x1ffff
ds_read_b128 a[156:159],v1 offset:4864
v_cvt_pk_bf16_f32 v105,v106,v107
v_mfma_f32_16x16x32_bf16 v[124:127],a[188:191],v[252:255],v[44:47]
s_sub_u32 s78,s78,s77
v_exp_f32 v108,v108
v_mfma_f32_16x16x32_bf16 v[124:127],a[184:187],v[244:247],v[124:127]
s_add_u32 s77,s77,s78
ds_read_b128 a[152:155],v1 offset:4608
v_accvgpr_read_b32 v8,a0
v_mfma_f32_16x16x32_bf16 v[124:127],a[180:183],v[236:239],v[124:127]
v_exp_f32 v109,v109
v_mfma_f32_16x16x32_bf16 v[124:127],a[176:179],v[228:231],v[124:127]
ds_read_b128 a[148:151],v1 offset:4352
v_cvt_pk_bf16_f32 v106,v108,v109
v_mfma_f32_16x16x32_bf16 v[52:55],v[28:31],v[80:83],v[52:55]
global_load_lds_dwordx4 v5,s[10:11] offset:1024
v_accvgpr_read_b32 v9,a1
v_mfma_f32_16x16x32_bf16 v[52:55],v[28:31],v[88:91],v[52:55]
ds_read_b128 a[144:147],v1 offset:4096
v_accvgpr_read_b32 v10,a2
v_mfma_f32_16x16x32_bf16 a[32:35],a[192:195],v[80:83],a[32:35]
v_exp_f32 v110,v110
v_mfma_f32_16x16x32_bf16 a[32:35],a[196:199],v[88:91],a[32:35]
ds_read_b128 a[160:163],v1 offset:8192
v_accvgpr_read_b32 v11,a3
v_mfma_f32_16x16x32_bf16 a[36:39],a[200:203],v[80:83],a[36:39]
v_exp_f32 v111,v111
v_mfma_f32_16x16x32_bf16 a[36:39],a[204:207],v[88:91],a[36:39]
ds_read_b128 a[164:167],v1 offset:8448
v_cvt_pk_bf16_f32 v107,v110,v111
v_mfma_f32_16x16x32_bf16 a[40:43],a[208:211],v[80:83],a[40:43]
v_exp_f32 v112,v112
v_mfma_f32_16x16x32_bf16 a[40:43],a[212:215],v[88:91],a[40:43]
ds_read_b128 a[168:171],v1 offset:8704
v_accvgpr_read_b32 v12,a4
v_mfma_f32_16x16x32_bf16 a[44:47],a[216:219],v[80:83],a[44:47]
v_exp_f32 v113,v113
v_mfma_f32_16x16x32_bf16 a[44:47],a[220:223],v[88:91],a[44:47]
ds_read_b128 a[172:175],v1 offset:8960
v_cvt_pk_bf16_f32 v112,v112,v113
v_mfma_f32_16x16x32_bf16 a[48:51],a[224:227],v[80:83],a[48:51]
v_exp_f32 v114,v114
v_mfma_f32_16x16x32_bf16 a[48:51],a[228:231],v[88:91],a[48:51]
ds_read_b128 a[188:191],v1 offset:13056
v_accvgpr_read_b32 v13,a5
v_mfma_f32_16x16x32_bf16 a[52:55],a[232:235],v[80:83],a[52:55]
v_exp_f32 v115,v115
v_mfma_f32_16x16x32_bf16 a[52:55],a[236:239],v[88:91],a[52:55]
ds_read_b128 a[184:187],v1 offset:12800
v_cvt_pk_bf16_f32 v113,v114,v115
v_mfma_f32_16x16x32_bf16 a[56:59],a[240:243],v[80:83],a[56:59]
global_load_lds_dwordx4 v4,s[10:11] offset:2048
v_accvgpr_read_b32 v14,a6
v_mfma_f32_16x16x32_bf16 a[56:59],a[244:247],v[88:91],a[56:59]
ds_read_b128 a[180:183],v1 offset:12544
v_accvgpr_read_b32 v15,a7
v_mfma_f32_16x16x32_bf16 a[60:63],a[248:251],v[80:83],a[60:63]
v_exp_f32 v116,v116
v_mfma_f32_16x16x32_bf16 a[60:63],a[252:255],v[88:91],a[60:63]
s_waitcnt lgkmcnt(3)
ds_read_b128 a[176:179],v1 offset:12288
v_max_f32 v24,0x00800000,v48
v_mfma_f32_16x16x32_bf16 v[64:67],a[128:131],v[128:131],v[32:35]
v_exp_f32 v117,v117
v_mfma_f32_16x16x32_bf16 v[64:67],a[132:135],v[136:139],v[64:67]
ds_read_b128 v[36:39],v7 offset:1024
v_cvt_pk_bf16_f32 v114,v116,v117
v_mfma_f32_16x16x32_bf16 v[64:67],a[136:139],v[144:147],v[64:67]
v_exp_f32 v118,v118
v_mfma_f32_16x16x32_bf16 v[64:67],a[140:143],v[152:155],v[64:67]
v_exp_f32 v119,v119
v_mfma_f32_16x16x32_bf16 v[68:71],a[156:159],v[152:155],v[32:35]
ds_read_b128 v[40:43],v7 offset:2048
v_cvt_pk_bf16_f32 v115,v118,v119
v_mfma_f32_16x16x32_bf16 v[68:71],a[152:155],v[144:147],v[68:71]
v_exp_f32 v120,v120
v_mfma_f32_16x16x32_bf16 v[68:71],a[148:151],v[136:139],v[68:71]
v_exp_f32 v121,v121
v_mfma_f32_16x16x32_bf16 v[68:71],a[144:147],v[128:131],v[68:71]
v_exp_f32 v122,v122
v_mfma_f32_16x16x32_bf16 v[72:75],a[160:163],v[128:131],v[32:35]
v_exp_f32 v123,v123
v_mfma_f32_16x16x32_bf16 v[72:75],a[164:167],v[136:139],v[72:75]
v_exp_f32 v124,v124
v_mfma_f32_16x16x32_bf16 v[72:75],a[168:171],v[144:147],v[72:75]
L_pd7_%=:
s_cbranch_execz L_trt0_%=
s_waitcnt lgkmcnt(1)
v_exp_f32 v125,v125
v_mfma_f32_16x16x32_bf16 v[72:75],a[172:175],v[152:155],v[72:75]
global_load_lds_dwordx4 v5,s[10:11] offset:3072
v_cvt_pk_bf16_f32 v120,v120,v121
v_mfma_f32_16x16x32_bf16 v[76:79],a[188:191],v[152:155],v[32:35]
v_exp_f32 v126,v126
v_mfma_f32_16x16x32_bf16 v[76:79],a[184:187],v[144:147],v[76:79]
v_exp_f32 v127,v127
v_mfma_f32_16x16x32_bf16 v[76:79],a[180:183],v[136:139],v[76:79]
v_cvt_pk_bf16_f32 v121,v122,v123
v_cvt_pk_bf16_f32 v122,v124,v125
v_mfma_f32_16x16x32_bf16 v[76:79],a[176:179],v[128:131],v[76:79]
v_cvt_pk_bf16_f32 v123,v126,v127
v_accvgpr_read_b32 v16,a8
v_mfma_f32_16x16x32_bf16 v[56:59],v[28:31],v[96:99],v[56:59]
v_exp_f32 v64,v64
v_mfma_f32_16x16x32_bf16 v[56:59],v[28:31],v[104:107],v[56:59]
v_exp_f32 v65,v65
v_mfma_f32_16x16x32_bf16 a[64:67],a[192:195],v[96:99],a[64:67]
v_exp_f32 v66,v66
v_mfma_f32_16x16x32_bf16 a[64:67],a[196:199],v[104:107],a[64:67]
v_exp_f32 v67,v67
v_mfma_f32_16x16x32_bf16 a[68:71],a[200:203],v[96:99],a[68:71]
v_exp_f32 v68,v68
v_mfma_f32_16x16x32_bf16 a[68:71],a[204:207],v[104:107],a[68:71]
s_add_u32 s10,s20,s74
s_addc_u32 s11,s21,0
v_cvt_pk_bf16_f32 v64,v64,v65
v_mfma_f32_16x16x32_bf16 a[72:75],a[208:211],v[96:99],a[72:75]
v_exp_f32 v69,v69
v_mfma_f32_16x16x32_bf16 a[72:75],a[212:215],v[104:107],a[72:75]
s_add_u32 m0,s76,0
v_exp_f32 v70,v70
v_mfma_f32_16x16x32_bf16 a[76:79],a[216:219],v[96:99],a[76:79]
v_exp_f32 v71,v71
v_mfma_f32_16x16x32_bf16 a[76:79],a[220:223],v[104:107],a[76:79]
global_load_lds_dwordx4 v4,s[10:11] nt
v_cvt_pk_bf16_f32 v65,v66,v67
v_mfma_f32_16x16x32_bf16 a[80:83],a[224:227],v[96:99],a[80:83]
v_cvt_pk_bf16_f32 v66,v68,v69
v_cvt_pk_bf16_f32 v67,v70,v71
v_mfma_f32_16x16x32_bf16 a[80:83],a[228:231],v[104:107],a[80:83]
v_exp_f32 v72,v72
v_mfma_f32_16x16x32_bf16 a[84:87],a[232:235],v[96:99],a[84:87]
v_exp_f32 v73,v73
v_mfma_f32_16x16x32_bf16 a[84:87],a[236:239],v[104:107],a[84:87]
v_exp_f32 v74,v74
v_mfma_f32_16x16x32_bf16 a[88:91],a[240:243],v[96:99],a[88:91]
v_exp_f32 v75,v75
v_mfma_f32_16x16x32_bf16 a[88:91],a[244:247],v[104:107],a[88:91]
v_exp_f32 v76,v76
v_mfma_f32_16x16x32_bf16 a[92:95],a[248:251],v[96:99],a[92:95]
v_exp_f32 v77,v77
v_mfma_f32_16x16x32_bf16 a[92:95],a[252:255],v[104:107],a[92:95]
v_exp_f32 v78,v78
v_mfma_f32_16x16x32_bf16 v[80:83],a[128:131],v[160:163],v[36:39]
v_exp_f32 v79,v79
v_mfma_f32_16x16x32_bf16 v[80:83],a[132:135],v[168:171],v[80:83]
v_cvt_pk_bf16_f32 v72,v72,v73
v_cvt_pk_bf16_f32 v73,v74,v75
v_mfma_f32_16x16x32_bf16 v[80:83],a[136:139],v[176:179],v[80:83]
v_cvt_pk_bf16_f32 v74,v76,v77
v_cvt_pk_bf16_f32 v75,v78,v79
v_mfma_f32_16x16x32_bf16 v[80:83],a[140:143],v[184:187],v[80:83]
v_rcp_f32 v24,v24
v_mfma_f32_16x16x32_bf16 v[84:87],a[156:159],v[184:187],v[36:39]
v_accvgpr_read_b32 v17,a9
v_accvgpr_read_b32 v18,a10
v_mfma_f32_16x16x32_bf16 v[84:87],a[152:155],v[176:179],v[84:87]
v_accvgpr_read_b32 v19,a11
v_mul_f32 v8,v8,v24
v_mfma_f32_16x16x32_bf16 v[84:87],a[148:151],v[168:171],v[84:87]
global_load_lds_dwordx4 v4,s[10:11] offset:1024 nt
v_mul_f32 v9,v9,v24
v_mfma_f32_16x16x32_bf16 v[84:87],a[144:147],v[160:163],v[84:87]
v_mul_f32 v10,v10,v24
v_mul_f32 v11,v11,v24
v_mfma_f32_16x16x32_bf16 v[88:91],a[160:163],v[160:163],v[36:39]
v_mul_f32 v12,v12,v24
v_mul_f32 v13,v13,v24
v_mfma_f32_16x16x32_bf16 v[88:91],a[164:167],v[168:171],v[88:91]
v_mul_f32 v14,v14,v24
v_mul_f32 v15,v15,v24
v_mfma_f32_16x16x32_bf16 v[88:91],a[168:171],v[176:179],v[88:91]
v_cvt_pk_bf16_f32 v8,v8,v9
v_cvt_pk_bf16_f32 v9,v10,v11
v_mfma_f32_16x16x32_bf16 v[88:91],a[172:175],v[184:187],v[88:91]
v_cvt_pk_bf16_f32 v10,v12,v13
v_cvt_pk_bf16_f32 v11,v14,v15
)A"
R"A(v_mfma_f32_16x16x32_bf16 v[92:95],a[188:191],v[184:187],v[36:39]
ds_write_b128 v7,v[8:11] offset:6144
v_mfma_f32_16x16x32_bf16 v[92:95],a[184:187],v[176:179],v[92:95]
v_accvgpr_read_b32 v20,a12
v_accvgpr_read_b32 v21,a13
v_mfma_f32_16x16x32_bf16 v[92:95],a[180:183],v[168:171],v[92:95]
v_accvgpr_read_b32 v22,a14
v_accvgpr_read_b32 v23,a15
v_mfma_f32_16x16x32_bf16 v[92:95],a[176:179],v[160:163],v[92:95]
s_cmp_le_u32 s5,s7
s_cselect_b32 s4,2,0
v_accvgpr_read_b32 v8,a16
v_mfma_f32_16x16x32_bf16 v[60:63],v[28:31],v[112:115],v[60:63]
s_sub_u32 s5,s5,s4
v_accvgpr_read_b32 v9,a17
v_accvgpr_read_b32 v10,a18
v_mfma_f32_16x16x32_bf16 v[60:63],v[28:31],v[120:123],v[60:63]
s_max_i32 s5,s5,0
v_accvgpr_read_b32 v11,a19
v_mul_f32 v16,v16,v24
v_mfma_f32_16x16x32_bf16 a[96:99],a[192:195],v[112:115],a[96:99]
s_sub_u32 s7,s7,2
ds_read_b64_tr_b16 a[192:193],v2
ds_read_b64_tr_b16 a[194:195],v2 offset:4096
v_mfma_f32_16x16x32_bf16 a[96:99],a[196:199],v[120:123],a[96:99]
s_max_i32 s7,s7,0
ds_read_b64_tr_b16 a[196:197],v2 offset:8192
v_mul_f32 v17,v17,v24
v_mfma_f32_16x16x32_bf16 a[100:103],a[200:203],v[112:115],a[100:103]
global_load_lds_dwordx4 v5,s[10:11] offset:2048 nt
ds_read_b64_tr_b16 a[198:199],v2 offset:12288
v_mfma_f32_16x16x32_bf16 a[100:103],a[204:207],v[120:123],a[100:103]
ds_read_b64_tr_b16 a[200:201],v3
v_mul_f32 v18,v18,v24
v_mfma_f32_16x16x32_bf16 a[104:107],a[208:211],v[112:115],a[104:107]
ds_read_b64_tr_b16 a[202:203],v3 offset:4096
ds_read_b64_tr_b16 a[204:205],v3 offset:8192
v_mfma_f32_16x16x32_bf16 a[104:107],a[212:215],v[120:123],a[104:107]
ds_read_b64_tr_b16 a[206:207],v3 offset:12288
v_mul_f32 v19,v19,v24
v_mfma_f32_16x16x32_bf16 a[108:111],a[216:219],v[112:115],a[108:111]
ds_read_b64_tr_b16 a[208:209],v2 offset:256
v_mul_f32 v20,v20,v24
v_mfma_f32_16x16x32_bf16 a[108:111],a[220:223],v[120:123],a[108:111]
ds_read_b64_tr_b16 a[210:211],v2 offset:4352
v_mul_f32 v21,v21,v24
v_mfma_f32_16x16x32_bf16 a[112:115],a[224:227],v[112:115],a[112:115]
ds_read_b64_tr_b16 a[212:213],v2 offset:8448
ds_read_b64_tr_b16 a[214:215],v2 offset:12544
v_mfma_f32_16x16x32_bf16 a[112:115],a[228:231],v[120:123],a[112:115]
ds_read_b64_tr_b16 a[216:217],v3 offset:256
v_mul_f32 v22,v22,v24
v_mfma_f32_16x16x32_bf16 a[116:119],a[232:235],v[112:115],a[116:119]
ds_read_b64_tr_b16 a[218:219],v3 offset:4352
v_mul_f32 v23,v23,v24
v_mfma_f32_16x16x32_bf16 a[116:119],a[236:239],v[120:123],a[116:119]
ds_read_b64_tr_b16 a[220:221],v3 offset:8448
v_cvt_pk_bf16_f32 v16,v16,v17
v_mfma_f32_16x16x32_bf16 a[120:123],a[240:243],v[112:115],a[120:123]
ds_read_b64_tr_b16 a[222:223],v3 offset:12544
ds_read_b64_tr_b16 a[224:225],v2 offset:512
v_mfma_f32_16x16x32_bf16 a[120:123],a[244:247],v[120:123],a[120:123]
ds_read_b64_tr_b16 a[226:227],v2 offset:4608
v_cvt_pk_bf16_f32 v17,v18,v19
v_mfma_f32_16x16x32_bf16 a[124:127],a[248:251],v[112:115],a[124:127]
ds_read_b64_tr_b16 a[228:229],v2 offset:8704
v_cvt_pk_bf16_f32 v18,v20,v21
v_mfma_f32_16x16x32_bf16 a[124:127],a[252:255],v[120:123],a[124:127]
s_waitcnt lgkmcnt(9)
ds_read_b64_tr_b16 a[230:231],v2 offset:12800
v_cvt_pk_bf16_f32 v19,v22,v23
v_mfma_f32_16x16x32_bf16 v[96:99],a[128:131],v[192:195],v[40:43]
ds_read_b64_tr_b16 a[232:233],v3 offset:512
ds_read_b64_tr_b16 a[234:235],v3 offset:4608
v_mfma_f32_16x16x32_bf16 v[96:99],a[132:135],v[200:203],v[96:99]
global_load_lds_dwordx4 v5,s[10:11] offset:3072
ds_read_b64_tr_b16 a[236:237],v3 offset:8704
v_mfma_f32_16x16x32_bf16 v[96:99],a[136:139],v[208:211],v[96:99]
ds_read_b64_tr_b16 a[238:239],v3 offset:12800
v_accvgpr_read_b32 v12,a20
v_mfma_f32_16x16x32_bf16 v[96:99],a[140:143],v[216:219],v[96:99]
ds_read_b64_tr_b16 a[240:241],v2 offset:768
v_accvgpr_read_b32 v13,a21
v_mfma_f32_16x16x32_bf16 v[100:103],a[156:159],v[216:219],v[40:43]
ds_read_b64_tr_b16 a[242:243],v2 offset:4864
ds_read_b64_tr_b16 a[244:245],v2 offset:8960
v_mfma_f32_16x16x32_bf16 v[100:103],a[152:155],v[208:211],v[100:103]
ds_read_b64_tr_b16 a[246:247],v2 offset:13056
v_accvgpr_read_b32 v14,a22
v_mfma_f32_16x16x32_bf16 v[100:103],a[148:151],v[200:203],v[100:103]
ds_read_b64_tr_b16 a[248:249],v3 offset:768
v_accvgpr_read_b32 v15,a23
v_mfma_f32_16x16x32_bf16 v[100:103],a[144:147],v[192:195],v[100:103]
ds_read_b64_tr_b16 a[250:251],v3 offset:4864
v_accvgpr_read_b32 v20,a28
v_mfma_f32_16x16x32_bf16 v[104:107],a[160:163],v[192:195],v[40:43]
ds_read_b64_tr_b16 a[252:253],v3 offset:8960
ds_read_b64_tr_b16 a[254:255],v3 offset:13056
v_mfma_f32_16x16x32_bf16 v[104:107],a[164:167],v[200:203],v[104:107]
v_accvgpr_read_b32 v21,a29
v_accvgpr_read_b32 v22,a30
v_mfma_f32_16x16x32_bf16 v[104:107],a[168:171],v[208:211],v[104:107]
ds_write_b128 v7,v[16:19] offset:7168
v_mfma_f32_16x16x32_bf16 v[104:107],a[172:175],v[216:219],v[104:107]
s_add_u32 s10,s24,s74
s_addc_u32 s11,s25,0
v_accvgpr_read_b32 v23,a31
v_mfma_f32_16x16x32_bf16 v[108:111],a[188:191],v[216:219],v[40:43]
v_accvgpr_read_b32 v16,a24
v_accvgpr_read_b32 v17,a25
v_mfma_f32_16x16x32_bf16 v[108:111],a[184:187],v[208:211],v[108:111]
s_add_u32 m0,s76,16384
v_accvgpr_read_b32 v18,a26
v_accvgpr_read_b32 v19,a27
v_mfma_f32_16x16x32_bf16 v[108:111],a[180:183],v[200:203],v[108:111]
v_cmp_nlt_f32 s[54:55],v48,s73
v_cmp_nlt_f32 s[56:57],v52,s73
v_mfma_f32_16x16x32_bf16 v[108:111],a[176:179],v[192:195],v[108:111]
s_or_b64 s[54:55],s[54:55],s[56:57]
global_load_lds_dwordx4 v4,s[10:11]
v_mfma_f32_16x16x32_bf16 a[0:3],a[192:195],v[64:67],0
v_cmp_nlt_f32 s[56:57],v56,s73
v_mul_f32 v8,v8,v24
v_mfma_f32_16x16x32_bf16 a[0:3],a[196:199],v[72:75],a[0:3]
s_or_b64 s[54:55],s[54:55],s[56:57]
v_mul_f32 v9,v9,v24
v_mul_f32 v10,v10,v24
v_mfma_f32_16x16x32_bf16 a[4:7],a[200:203],v[64:67],0
v_mul_f32 v11,v11,v24
v_mul_f32 v12,v12,v24
v_mfma_f32_16x16x32_bf16 a[4:7],a[204:207],v[72:75],a[4:7]
v_mul_f32 v13,v13,v24
v_mul_f32 v14,v14,v24
v_mfma_f32_16x16x32_bf16 a[8:11],a[208:211],v[64:67],0
s_waitcnt lgkmcnt(0)
v_mul_f32 v15,v15,v24
v_cvt_pk_bf16_f32 v8,v8,v9
v_mfma_f32_16x16x32_bf16 a[8:11],a[212:215],v[72:75],a[8:11]
v_cvt_pk_bf16_f32 v9,v10,v11
v_cvt_pk_bf16_f32 v10,v12,v13
v_mfma_f32_16x16x32_bf16 a[12:15],a[216:219],v[64:67],0
v_cvt_pk_bf16_f32 v11,v14,v15
v_mul_f32 v16,v16,v24
v_mfma_f32_16x16x32_bf16 a[12:15],a[220:223],v[72:75],a[12:15]
v_mul_f32 v17,v17,v24
v_mul_f32 v18,v18,v24
v_mfma_f32_16x16x32_bf16 a[16:19],a[224:227],v[64:67],0
v_mul_f32 v19,v19,v24
v_mul_f32 v20,v20,v24
v_mfma_f32_16x16x32_bf16 a[16:19],a[228:231],v[72:75],a[16:19]
v_mul_f32 v21,v21,v24
v_mul_f32 v22,v22,v24
v_mfma_f32_16x16x32_bf16 a[20:23],a[232:235],v[64:67],0
s_getreg_b32 s92,hwreg(HW_REG_IB_STS,0,4)
s_min_u32 s4,s92,s6
s_mov_b32 s6,s92
v_mfma_f32_16x16x32_bf16 a[20:23],a[236:239],v[72:75],a[20:23]
s_cmp_gt_u32 s4,s9
s_cselect_b32 s4,15,s5
v_mul_f32 v23,v23,v24
v_mfma_f32_16x16x32_bf16 a[24:27],a[240:243],v[64:67],0
v_cvt_pk_bf16_f32 v16,v16,v17
v_cvt_pk_bf16_f32 v17,v18,v19
v_mfma_f32_16x16x32_bf16 a[24:27],a[244:247],v[72:75],a[24:27]
v_cvt_pk_bf16_f32 v18,v20,v21
v_cvt_pk_bf16_f32 v19,v22,v23
v_mfma_f32_16x16x32_bf16 a[28:31],a[248:251],v[64:67],0
global_load_lds_dwordx4 v5,s[10:11] offset:1024
v_cmp_nlt_f32 s[56:57],v60,s73
v_mfma_f32_16x16x32_bf16 a[28:31],a[252:255],v[72:75],a[28:31]
s_or_b64 s[54:55],s[54:55],s[56:57]
v_exp_f32 v80,v80
v_mfma_f32_16x16x32_bf16 v[48:51],v[28:31],v[64:67],0
s_getpc_b64 s[92:93]
s_cmp_lg_u32 s4,0
s_cbranch_scc1 L_pace_%=
v_exp_f32 v81,v81
v_exp_f32 v82,v82
v_exp_f32 v83,v83
v_exp_f32 v84,v84
v_exp_f32 v85,v85
v_exp_f32 v86,v86
v_exp_f32 v87,v87
v_exp_f32 v88,v88
v_exp_f32 v89,v89
v_mfma_f32_16x16x32_bf16 v[48:51],v[28:31],v[72:75],v[48:51]
ds_write_b128 v7,v[8:11]
v_add_u32 v1,s78,v1
v_max_f32 v25,0x00800000,v52
v_rcp_f32 v25,v25
v_max_f32 v26,0x00800000,v56
v_rcp_f32 v26,v26
v_cvt_pk_bf16_f32 v80,v80,v81
v_cvt_pk_bf16_f32 v81,v82,v83
v_cvt_pk_bf16_f32 v82,v84,v85
v_cvt_pk_bf16_f32 v83,v86,v87
v_cvt_pk_bf16_f32 v88,v88,v89
v_max_f32 v27,0x00800000,v60
v_rcp_f32 v27,v27
ds_write_b128 v7,v[16:19] offset:1024
ds_read_b128 v[44:47],v7 offset:3072
s_cmp_lg_u64 s[54:55],0
s_cbranch_scc1 L_slow0_%=
L_ok0_%=:
s_waitcnt vmcnt(14)
s_barrier
s_waitcnt lgkmcnt(0)
v_mfma_f32_16x16x32_bf16 v[112:115],a[128:131],v[224:227],v[44:47]
s_add_u32 s10,s24,s74
s_addc_u32 s11,s25,0
s_add_u32 m0,s76,16384
v_mfma_f32_16x16x32_bf16 v[112:115],a[132:135],v[232:235],v[112:115]
s_add_u32 s75,s75,16384
v_exp_f32 v90,v90
v_mfma_f32_16x16x32_bf16 v[112:115],a[136:139],v[240:243],v[112:115]
global_load_lds_dwordx4 v4,s[10:11] offset:2048
ds_read_b128 a[128:131],v1
v_mfma_f32_16x16x32_bf16 v[112:115],a[140:143],v[248:251],v[112:115]
s_add_u32 s74,s74,16384
v_exp_f32 v91,v91
v_mfma_f32_16x16x32_bf16 v[116:119],a[156:159],v[248:251],v[44:47]
s_and_b32 s74,s74,0x3ffff
v_exp_f32 v92,v92
v_mfma_f32_16x16x32_bf16 v[116:119],a[152:155],v[240:243],v[116:119]
s_and_b32 s69,s75,0xfffc0000
v_exp_f32 v93,v93
v_mfma_f32_16x16x32_bf16 v[116:119],a[148:151],v[232:235],v[116:119]
s_or_b32 s74,s74,s69
v_exp_f32 v94,v94
v_mfma_f32_16x16x32_bf16 v[116:119],a[144:147],v[224:227],v[116:119]
s_sub_u32 s76,s76,32768
v_exp_f32 v95,v95
v_mfma_f32_16x16x32_bf16 v[120:123],a[160:163],v[224:227],v[44:47]
s_and_b32 s76,s76,0x1ffff
v_cvt_pk_bf16_f32 v89,v90,v91
v_cvt_pk_bf16_f32 v90,v92,v93
v_mfma_f32_16x16x32_bf16 v[120:123],a[164:167],v[232:235],v[120:123]
s_add_u32 s75,s75,16384
ds_read_b128 a[132:135],v1 offset:256
v_cvt_pk_bf16_f32 v91,v94,v95
v_mfma_f32_16x16x32_bf16 v[120:123],a[168:171],v[240:243],v[120:123]
s_and_b32 s69,s75,0xfffc0000
v_exp_f32 v96,v96
v_mfma_f32_16x16x32_bf16 v[120:123],a[172:175],v[248:251],v[120:123]
s_add_u32 s75,s75,16384
ds_read_b128 a[136:139],v1 offset:512
v_accvgpr_read_b32 v16,a32
v_mfma_f32_16x16x32_bf16 v[124:127],a[188:191],v[248:251],v[44:47]
v_exp_f32 v97,v97
v_mfma_f32_16x16x32_bf16 v[124:127],a[184:187],v[240:243],v[124:127]
ds_read_b128 a[140:143],v1 offset:768
v_cvt_pk_bf16_f32 v96,v96,v97
v_mfma_f32_16x16x32_bf16 v[124:127],a[180:183],v[232:235],v[124:127]
v_exp_f32 v98,v98
v_mfma_f32_16x16x32_bf16 v[124:127],a[176:179],v[224:227],v[124:127]
ds_read_b128 a[156:159],v1 offset:4864
v_accvgpr_read_b32 v17,a33
v_mfma_f32_16x16x32_bf16 v[132:135],a[192:195],v[80:83],0
global_load_lds_dwordx4 v5,s[10:11] offset:3072
v_accvgpr_read_b32 v18,a34
v_mfma_f32_16x16x32_bf16 v[132:135],a[196:199],v[88:91],v[132:135]
ds_read_b128 a[152:155],v1 offset:4608
v_accvgpr_read_b32 v19,a35
v_mfma_f32_16x16x32_bf16 v[140:143],a[200:203],v[80:83],0
v_exp_f32 v99,v99
v_mfma_f32_16x16x32_bf16 v[140:143],a[204:207],v[88:91],v[140:143]
ds_read_b128 a[148:151],v1 offset:4352
v_cvt_pk_bf16_f32 v97,v98,v99
v_mfma_f32_16x16x32_bf16 v[148:151],a[208:211],v[80:83],0
v_exp_f32 v100,v100
v_mfma_f32_16x16x32_bf16 v[148:151],a[212:215],v[88:91],v[148:151]
ds_read_b128 a[144:147],v1 offset:4096
v_accvgpr_read_b32 v20,a36
v_mfma_f32_16x16x32_bf16 v[156:159],a[216:219],v[80:83],0
v_exp_f32 v101,v101
v_mfma_f32_16x16x32_bf16 v[156:159],a[220:223],v[88:91],v[156:159]
ds_read_b128 a[160:163],v1 offset:8192
v_cvt_pk_bf16_f32 v98,v100,v101
v_mfma_f32_16x16x32_bf16 v[164:167],a[224:227],v[80:83],0
v_exp_f32 v102,v102
v_mfma_f32_16x16x32_bf16 v[164:167],a[228:231],v[88:91],v[164:167]
ds_read_b128 a[164:167],v1 offset:8448
v_accvgpr_read_b32 v21,a37
v_mfma_f32_16x16x32_bf16 v[172:175],a[232:235],v[80:83],0
s_add_u32 s10,s20,s74
s_addc_u32 s11,s21,0
v_accvgpr_read_b32 v22,a38
v_mfma_f32_16x16x32_bf16 v[172:175],a[236:239],v[88:91],v[172:175]
ds_read_b128 a[168:171],v1 offset:8704
v_accvgpr_read_b32 v23,a39
v_mfma_f32_16x16x32_bf16 v[180:183],a[240:243],v[80:83],0
s_add_u32 m0,s76,0
v_exp_f32 v103,v103
v_mfma_f32_16x16x32_bf16 v[180:183],a[244:247],v[88:91],v[180:183]
ds_read_b128 a[172:175],v1 offset:8960
v_cvt_pk_bf16_f32 v99,v102,v103
v_mfma_f32_16x16x32_bf16 v[188:191],a[248:251],v[80:83],0
global_load_lds_dwordx4 v4,s[10:11] nt
v_mul_f32 v16,v16,v25
v_mfma_f32_16x16x32_bf16 v[188:191],a[252:255],v[88:91],v[188:191]
ds_read_b128 a[188:191],v1 offset:13056
v_mul_f32 v17,v17,v25
v_mfma_f32_16x16x32_bf16 v[52:55],v[28:31],v[80:83],0
v_exp_f32 v104,v104
v_mfma_f32_16x16x32_bf16 v[52:55],v[28:31],v[88:91],v[52:55]
)A"
R"A(s_waitcnt lgkmcnt(3)
ds_read_b128 a[184:187],v1 offset:12800
v_mul_f32 v18,v18,v25
v_mfma_f32_16x16x32_bf16 v[64:67],a[128:131],v[128:131],v[32:35]
v_exp_f32 v105,v105
v_mfma_f32_16x16x32_bf16 v[64:67],a[132:135],v[136:139],v[64:67]
ds_read_b128 a[180:183],v1 offset:12544
v_cvt_pk_bf16_f32 v104,v104,v105
v_mfma_f32_16x16x32_bf16 v[64:67],a[136:139],v[144:147],v[64:67]
v_exp_f32 v106,v106
v_mfma_f32_16x16x32_bf16 v[64:67],a[140:143],v[152:155],v[64:67]
ds_read_b128 a[176:179],v1 offset:12288
v_mul_f32 v19,v19,v25
v_mfma_f32_16x16x32_bf16 v[68:71],a[156:159],v[152:155],v[32:35]
v_exp_f32 v107,v107
v_mfma_f32_16x16x32_bf16 v[68:71],a[152:155],v[144:147],v[68:71]
v_exp_f32 v108,v108
v_mfma_f32_16x16x32_bf16 v[68:71],a[148:151],v[136:139],v[68:71]
v_exp_f32 v109,v109
v_mfma_f32_16x16x32_bf16 v[68:71],a[144:147],v[128:131],v[68:71]
v_exp_f32 v110,v110
v_mfma_f32_16x16x32_bf16 v[72:75],a[160:163],v[128:131],v[32:35]
v_exp_f32 v111,v111
v_mfma_f32_16x16x32_bf16 v[72:75],a[164:167],v[136:139],v[72:75]
s_waitcnt lgkmcnt(1)
v_cvt_pk_bf16_f32 v105,v106,v107
v_cvt_pk_bf16_f32 v106,v108,v109
v_mfma_f32_16x16x32_bf16 v[72:75],a[168:171],v[144:147],v[72:75]
global_load_lds_dwordx4 v4,s[10:11] offset:1024 nt
v_cvt_pk_bf16_f32 v107,v110,v111
v_mfma_f32_16x16x32_bf16 v[72:75],a[172:175],v[152:155],v[72:75]
v_exp_f32 v112,v112
v_mfma_f32_16x16x32_bf16 v[76:79],a[188:191],v[152:155],v[32:35]
v_exp_f32 v113,v113
v_mfma_f32_16x16x32_bf16 v[76:79],a[184:187],v[144:147],v[76:79]
v_exp_f32 v114,v114
v_mfma_f32_16x16x32_bf16 v[76:79],a[180:183],v[136:139],v[76:79]
s_waitcnt lgkmcnt(0)
v_exp_f32 v115,v115
v_mfma_f32_16x16x32_bf16 v[76:79],a[176:179],v[128:131],v[76:79]
v_exp_f32 v116,v116
v_mfma_f32_16x16x32_bf16 v[196:199],a[192:195],v[96:99],0
v_exp_f32 v117,v117
v_mfma_f32_16x16x32_bf16 v[196:199],a[196:199],v[104:107],v[196:199]
v_exp_f32 v118,v118
v_mfma_f32_16x16x32_bf16 v[204:207],a[200:203],v[96:99],0
v_exp_f32 v119,v119
v_mfma_f32_16x16x32_bf16 v[204:207],a[204:207],v[104:107],v[204:207]
v_cvt_pk_bf16_f32 v112,v112,v113
v_cvt_pk_bf16_f32 v113,v114,v115
v_mfma_f32_16x16x32_bf16 v[212:215],a[208:211],v[96:99],0
v_cvt_pk_bf16_f32 v114,v116,v117
v_cvt_pk_bf16_f32 v115,v118,v119
v_mfma_f32_16x16x32_bf16 v[212:215],a[212:215],v[104:107],v[212:215]
v_exp_f32 v120,v120
v_mfma_f32_16x16x32_bf16 v[220:223],a[216:219],v[96:99],0
v_exp_f32 v121,v121
v_mfma_f32_16x16x32_bf16 v[220:223],a[220:223],v[104:107],v[220:223]
v_exp_f32 v122,v122
v_mfma_f32_16x16x32_bf16 v[228:231],a[224:227],v[96:99],0
global_load_lds_dwordx4 v5,s[10:11] offset:2048 nt
v_cvt_pk_bf16_f32 v120,v120,v121
v_mfma_f32_16x16x32_bf16 v[228:231],a[228:231],v[104:107],v[228:231]
v_exp_f32 v123,v123
v_mfma_f32_16x16x32_bf16 v[236:239],a[232:235],v[96:99],0
v_exp_f32 v124,v124
v_mfma_f32_16x16x32_bf16 v[236:239],a[236:239],v[104:107],v[236:239]
v_exp_f32 v125,v125
v_mfma_f32_16x16x32_bf16 v[244:247],a[240:243],v[96:99],0
v_exp_f32 v126,v126
v_mfma_f32_16x16x32_bf16 v[244:247],a[244:247],v[104:107],v[244:247]
v_exp_f32 v127,v127
v_mfma_f32_16x16x32_bf16 v[252:255],a[248:251],v[96:99],0
v_cvt_pk_bf16_f32 v121,v122,v123
v_cvt_pk_bf16_f32 v122,v124,v125
v_mfma_f32_16x16x32_bf16 v[252:255],a[252:255],v[104:107],v[252:255]
v_cvt_pk_bf16_f32 v123,v126,v127
v_mul_f32 v20,v20,v25
v_mfma_f32_16x16x32_bf16 v[56:59],v[28:31],v[96:99],0
v_mul_f32 v21,v21,v25
v_mul_f32 v22,v22,v25
v_mfma_f32_16x16x32_bf16 v[56:59],v[28:31],v[104:107],v[56:59]
v_mul_f32 v23,v23,v25
v_cvt_pk_bf16_f32 v16,v16,v17
v_mfma_f32_16x16x32_bf16 v[80:83],a[128:131],v[160:163],v[36:39]
v_cvt_pk_bf16_f32 v17,v18,v19
v_cvt_pk_bf16_f32 v18,v20,v21
v_mfma_f32_16x16x32_bf16 v[80:83],a[132:135],v[168:171],v[80:83]
v_cvt_pk_bf16_f32 v19,v22,v23
v_accvgpr_read_b32 v20,a44
v_mfma_f32_16x16x32_bf16 v[80:83],a[136:139],v[176:179],v[80:83]
ds_write_b128 v7,v[16:19] offset:2048
v_mfma_f32_16x16x32_bf16 v[80:83],a[140:143],v[184:187],v[80:83]
v_accvgpr_read_b32 v21,a45
v_accvgpr_read_b32 v22,a46
v_mfma_f32_16x16x32_bf16 v[84:87],a[156:159],v[184:187],v[36:39]
v_accvgpr_read_b32 v16,a40
v_accvgpr_read_b32 v17,a41
v_mfma_f32_16x16x32_bf16 v[84:87],a[152:155],v[176:179],v[84:87]
global_load_lds_dwordx4 v5,s[10:11] offset:3072
v_accvgpr_read_b32 v18,a42
v_mfma_f32_16x16x32_bf16 v[84:87],a[148:151],v[168:171],v[84:87]
v_accvgpr_read_b32 v19,a43
v_accvgpr_read_b32 v23,a47
v_mfma_f32_16x16x32_bf16 v[84:87],a[144:147],v[160:163],v[84:87]
v_accvgpr_read_b32 v8,a48
v_accvgpr_read_b32 v9,a49
v_mfma_f32_16x16x32_bf16 v[88:91],a[160:163],v[160:163],v[36:39]
v_accvgpr_read_b32 v10,a50
v_accvgpr_read_b32 v11,a51
v_mfma_f32_16x16x32_bf16 v[88:91],a[164:167],v[168:171],v[88:91]
v_mul_f32 v16,v16,v25
v_mul_f32 v17,v17,v25
v_mfma_f32_16x16x32_bf16 v[88:91],a[168:171],v[176:179],v[88:91]
v_mul_f32 v18,v18,v25
v_mul_f32 v19,v19,v25
v_mfma_f32_16x16x32_bf16 v[88:91],a[172:175],v[184:187],v[88:91]
v_mul_f32 v20,v20,v25
v_mul_f32 v21,v21,v25
v_mfma_f32_16x16x32_bf16 v[92:95],a[188:191],v[184:187],v[36:39]
v_mul_f32 v22,v22,v25
v_mul_f32 v23,v23,v25
v_mfma_f32_16x16x32_bf16 v[92:95],a[184:187],v[176:179],v[92:95]
v_cvt_pk_bf16_f32 v16,v16,v17
v_cvt_pk_bf16_f32 v17,v18,v19
v_mfma_f32_16x16x32_bf16 v[92:95],a[180:183],v[168:171],v[92:95]
v_cvt_pk_bf16_f32 v18,v20,v21
v_cvt_pk_bf16_f32 v19,v22,v23
v_mfma_f32_16x16x32_bf16 v[92:95],a[176:179],v[160:163],v[92:95]
s_add_u32 s10,s24,s74
s_addc_u32 s11,s25,0
v_accvgpr_read_b32 v12,a52
v_mfma_f32_16x16x32_bf16 a[32:35],a[192:195],v[112:115],0
ds_write_b128 v7,v[16:19] offset:3072
v_mfma_f32_16x16x32_bf16 a[32:35],a[196:199],v[120:123],a[32:35]
s_add_u32 m0,s76,16384
v_accvgpr_read_b32 v13,a53
v_accvgpr_read_b32 v14,a54
v_mfma_f32_16x16x32_bf16 a[36:39],a[200:203],v[112:115],0
v_accvgpr_read_b32 v15,a55
v_accvgpr_read_b32 v16,a56
v_mfma_f32_16x16x32_bf16 a[36:39],a[204:207],v[120:123],a[36:39]
global_load_lds_dwordx4 v4,s[10:11]
v_accvgpr_read_b32 v17,a57
v_mfma_f32_16x16x32_bf16 a[40:43],a[208:211],v[112:115],0
v_accvgpr_read_b32 v18,a58
v_accvgpr_read_b32 v19,a59
v_mfma_f32_16x16x32_bf16 a[40:43],a[212:215],v[120:123],a[40:43]
v_accvgpr_read_b32 v20,a60
v_accvgpr_read_b32 v21,a61
v_mfma_f32_16x16x32_bf16 a[44:47],a[216:219],v[112:115],0
v_accvgpr_read_b32 v22,a62
v_accvgpr_read_b32 v23,a63
v_mfma_f32_16x16x32_bf16 a[44:47],a[220:223],v[120:123],a[44:47]
v_add_u32 v2,s78,v2
v_add_u32 v3,s78,v3
v_mfma_f32_16x16x32_bf16 a[48:51],a[224:227],v[112:115],0
s_sub_u32 s78,s77,32768
ds_read_b64_tr_b16 a[192:193],v2
ds_read_b64_tr_b16 a[194:195],v2 offset:4096
v_mfma_f32_16x16x32_bf16 a[48:51],a[228:231],v[120:123],a[48:51]
s_and_b32 s78,s78,0x1ffff
ds_read_b64_tr_b16 a[196:197],v2 offset:8192
v_mul_f32 v8,v8,v25
v_mfma_f32_16x16x32_bf16 a[52:55],a[232:235],v[112:115],0
s_sub_u32 s78,s78,s77
ds_read_b64_tr_b16 a[198:199],v2 offset:12288
v_mul_f32 v9,v9,v25
v_mfma_f32_16x16x32_bf16 a[52:55],a[236:239],v[120:123],a[52:55]
s_add_u32 s77,s77,s78
ds_read_b64_tr_b16 a[200:201],v3
v_mul_f32 v10,v10,v25
v_mfma_f32_16x16x32_bf16 a[56:59],a[240:243],v[112:115],0
ds_read_b64_tr_b16 a[202:203],v3 offset:4096
ds_read_b64_tr_b16 a[204:205],v3 offset:8192
v_mfma_f32_16x16x32_bf16 a[56:59],a[244:247],v[120:123],a[56:59]
ds_read_b64_tr_b16 a[206:207],v3 offset:12288
v_mul_f32 v11,v11,v25
v_mfma_f32_16x16x32_bf16 a[60:63],a[248:251],v[112:115],0
ds_read_b64_tr_b16 a[208:209],v2 offset:256
v_mul_f32 v12,v12,v25
v_mfma_f32_16x16x32_bf16 a[60:63],a[252:255],v[120:123],a[60:63]
ds_read_b64_tr_b16 a[210:211],v2 offset:4352
v_mul_f32 v13,v13,v25
v_mfma_f32_16x16x32_bf16 v[60:63],v[28:31],v[112:115],0
ds_read_b64_tr_b16 a[212:213],v2 offset:8448
ds_read_b64_tr_b16 a[214:215],v2 offset:12544
v_mfma_f32_16x16x32_bf16 v[60:63],v[28:31],v[120:123],v[60:63]
global_load_lds_dwordx4 v5,s[10:11] offset:1024
ds_read_b64_tr_b16 a[216:217],v3 offset:256
v_mfma_f32_16x16x32_bf16 v[96:99],a[128:131],v[192:195],v[40:43]
v_exp_f32 v64,v64
v_mfma_f32_16x16x32_bf16 v[96:99],a[132:135],v[200:203],v[96:99]
v_exp_f32 v65,v65
v_mfma_f32_16x16x32_bf16 v[96:99],a[136:139],v[208:211],v[96:99]
v_exp_f32 v66,v66
v_mfma_f32_16x16x32_bf16 v[96:99],a[140:143],v[216:219],v[96:99]
v_exp_f32 v67,v67
v_mfma_f32_16x16x32_bf16 v[100:103],a[156:159],v[216:219],v[40:43]
v_exp_f32 v68,v68
v_mfma_f32_16x16x32_bf16 v[100:103],a[152:155],v[208:211],v[100:103]
v_exp_f32 v69,v69
v_mfma_f32_16x16x32_bf16 v[100:103],a[148:151],v[200:203],v[100:103]
v_exp_f32 v70,v70
v_mfma_f32_16x16x32_bf16 v[100:103],a[144:147],v[192:195],v[100:103]
v_exp_f32 v71,v71
v_mfma_f32_16x16x32_bf16 v[104:107],a[160:163],v[192:195],v[40:43]
L_pd8_%=:
s_cbranch_execz L_trt0_%=
ds_read_b64_tr_b16 a[218:219],v3 offset:4352
v_cvt_pk_bf16_f32 v64,v64,v65
v_mfma_f32_16x16x32_bf16 v[104:107],a[164:167],v[200:203],v[104:107]
v_cvt_pk_bf16_f32 v65,v66,v67
v_cvt_pk_bf16_f32 v66,v68,v69
v_mfma_f32_16x16x32_bf16 v[104:107],a[168:171],v[208:211],v[104:107]
ds_read_b64_tr_b16 a[220:221],v3 offset:8448
v_cvt_pk_bf16_f32 v67,v70,v71
v_mfma_f32_16x16x32_bf16 v[104:107],a[172:175],v[216:219],v[104:107]
v_exp_f32 v72,v72
v_mfma_f32_16x16x32_bf16 v[108:111],a[188:191],v[216:219],v[40:43]
v_exp_f32 v73,v73
v_mfma_f32_16x16x32_bf16 v[108:111],a[184:187],v[208:211],v[108:111]
global_load_lds_dwordx4 v4,s[10:11] offset:2048
v_exp_f32 v74,v74
v_exp_f32 v75,v75
v_exp_f32 v76,v76
v_exp_f32 v77,v77
v_exp_f32 v78,v78
v_exp_f32 v79,v79
v_mfma_f32_16x16x32_bf16 v[108:111],a[180:183],v[200:203],v[108:111]
ds_read_b64_tr_b16 a[222:223],v3 offset:12544
v_cvt_pk_bf16_f32 v72,v72,v73
v_cvt_pk_bf16_f32 v73,v74,v75
v_cvt_pk_bf16_f32 v74,v76,v77
v_cvt_pk_bf16_f32 v75,v78,v79
v_mfma_f32_16x16x32_bf16 v[108:111],a[176:179],v[192:195],v[108:111]
s_waitcnt lgkmcnt(3)
ds_read_b64_tr_b16 a[224:225],v2 offset:512
ds_read_b64_tr_b16 a[226:227],v2 offset:4608
v_mfma_f32_16x16x32_bf16 a[0:3],a[192:195],v[64:67],a[0:3]
ds_read_b64_tr_b16 a[228:229],v2 offset:8704
v_mul_f32 v14,v14,v25
v_mfma_f32_16x16x32_bf16 a[0:3],a[196:199],v[72:75],a[0:3]
ds_read_b64_tr_b16 a[230:231],v2 offset:12800
v_mul_f32 v15,v15,v25
v_mfma_f32_16x16x32_bf16 a[4:7],a[200:203],v[64:67],a[4:7]
ds_read_b64_tr_b16 a[232:233],v3 offset:512
v_cvt_pk_bf16_f32 v8,v8,v9
v_mfma_f32_16x16x32_bf16 a[4:7],a[204:207],v[72:75],a[4:7]
ds_read_b64_tr_b16 a[234:235],v3 offset:4608
ds_read_b64_tr_b16 a[236:237],v3 offset:8704
v_mfma_f32_16x16x32_bf16 a[8:11],a[208:211],v[64:67],a[8:11]
ds_read_b64_tr_b16 a[238:239],v3 offset:12800
v_cvt_pk_bf16_f32 v9,v10,v11
v_mfma_f32_16x16x32_bf16 a[8:11],a[212:215],v[72:75],a[8:11]
s_waitcnt lgkmcnt(9)
ds_read_b64_tr_b16 a[240:241],v2 offset:768
v_cvt_pk_bf16_f32 v10,v12,v13
v_mfma_f32_16x16x32_bf16 a[12:15],a[216:219],v[64:67],a[12:15]
s_waitcnt lgkmcnt(9)
ds_read_b64_tr_b16 a[242:243],v2 offset:4864
v_cvt_pk_bf16_f32 v11,v14,v15
v_mfma_f32_16x16x32_bf16 a[12:15],a[220:223],v[72:75],a[12:15]
s_waitcnt lgkmcnt(8)
ds_read_b64_tr_b16 a[244:245],v2 offset:8960
ds_read_b64_tr_b16 a[246:247],v2 offset:13056
v_mfma_f32_16x16x32_bf16 a[16:19],a[224:227],v[64:67],a[16:19]
s_waitcnt lgkmcnt(8)
ds_read_b64_tr_b16 a[248:249],v3 offset:768
v_mul_f32 v16,v16,v25
v_mfma_f32_16x16x32_bf16 a[16:19],a[228:231],v[72:75],a[16:19]
s_waitcnt lgkmcnt(1)
ds_read_b64_tr_b16 a[250:251],v3 offset:4864
v_mul_f32 v17,v17,v25
v_mfma_f32_16x16x32_bf16 a[20:23],a[232:235],v[64:67],a[20:23]
s_getreg_b32 s92,hwreg(HW_REG_IB_STS,0,4)
s_min_u32 s4,s92,s6
ds_read_b64_tr_b16 a[252:253],v3 offset:8960
v_mfma_f32_16x16x32_bf16 a[20:23],a[236:239],v[72:75],a[20:23]
s_mov_b32 s6,s92
ds_read_b64_tr_b16 a[254:255],v3 offset:13056
v_mul_f32 v18,v18,v25
v_mfma_f32_16x16x32_bf16 a[24:27],a[240:243],v[64:67],a[24:27]
s_cmp_gt_u32 s4,s9
s_cselect_b32 s4,15,s5
v_mul_f32 v19,v19,v25
v_mfma_f32_16x16x32_bf16 a[24:27],a[244:247],v[72:75],a[24:27]
s_waitcnt lgkmcnt(2)
v_mul_f32 v20,v20,v25
v_mul_f32 v21,v21,v25
v_mfma_f32_16x16x32_bf16 a[28:31],a[248:251],v[64:67],a[28:31]
s_waitcnt lgkmcnt(0)
v_mul_f32 v22,v22,v25
v_mul_f32 v23,v23,v25
v_mfma_f32_16x16x32_bf16 a[28:31],a[252:255],v[72:75],a[28:31]
s_getpc_b64 s[92:93]
s_cmp_lg_u32 s4,0
s_cbranch_scc1 L_pace_%=
v_mfma_f32_16x16x32_bf16 v[48:51],v[28:31],v[64:67],v[48:51]
)A"
R"A(v_cvt_pk_bf16_f32 v12,v16,v17
v_cvt_pk_bf16_f32 v13,v18,v19
v_mfma_f32_16x16x32_bf16 v[48:51],v[28:31],v[72:75],v[48:51]
s_add_u32 s10,s24,s74
s_addc_u32 s11,s25,0
v_cvt_pk_bf16_f32 v14,v20,v21
s_waitcnt vmcnt(15)
s_barrier
v_mfma_f32_16x16x32_bf16 v[112:115],a[128:131],v[224:227],v[44:47]
s_add_u32 m0,s76,16384
v_cvt_pk_bf16_f32 v15,v22,v23
v_accvgpr_read_b32 v16,a112
v_mfma_f32_16x16x32_bf16 v[112:115],a[132:135],v[232:235],v[112:115]
s_add_u32 s74,s74,16384
v_accvgpr_read_b32 v17,a113
v_accvgpr_read_b32 v18,a114
v_mfma_f32_16x16x32_bf16 v[112:115],a[136:139],v[240:243],v[112:115]
global_load_lds_dwordx4 v5,s[10:11] offset:3072
v_accvgpr_read_b32 v19,a115
v_mfma_f32_16x16x32_bf16 v[112:115],a[140:143],v[248:251],v[112:115]
s_and_b32 s74,s74,0x3ffff
v_accvgpr_read_b32 v20,a116
v_accvgpr_read_b32 v21,a117
v_mfma_f32_16x16x32_bf16 v[116:119],a[156:159],v[248:251],v[44:47]
s_or_b32 s74,s74,s69
v_accvgpr_read_b32 v22,a118
v_accvgpr_read_b32 v23,a119
v_mfma_f32_16x16x32_bf16 v[116:119],a[152:155],v[240:243],v[116:119]
s_sub_u32 s76,s76,32768
v_mul_f32 v16,v16,v27
v_mul_f32 v17,v17,v27
v_mfma_f32_16x16x32_bf16 v[116:119],a[148:151],v[232:235],v[116:119]
s_and_b32 s76,s76,0x1ffff
v_mul_f32 v18,v18,v27
v_mul_f32 v19,v19,v27
v_mfma_f32_16x16x32_bf16 v[116:119],a[144:147],v[224:227],v[116:119]
s_and_b32 s69,s75,0xfffc0000
v_mul_f32 v20,v20,v27
v_mul_f32 v21,v21,v27
v_mfma_f32_16x16x32_bf16 v[120:123],a[160:163],v[224:227],v[44:47]
s_add_u32 s75,s75,16384
v_mul_f32 v22,v22,v27
v_mul_f32 v23,v23,v27
v_mfma_f32_16x16x32_bf16 v[120:123],a[164:167],v[232:235],v[120:123]
v_cvt_pk_bf16_f32 v16,v16,v17
v_cvt_pk_bf16_f32 v17,v18,v19
v_mfma_f32_16x16x32_bf16 v[120:123],a[168:171],v[240:243],v[120:123]
v_cvt_pk_bf16_f32 v18,v20,v21
v_cvt_pk_bf16_f32 v19,v22,v23
v_mfma_f32_16x16x32_bf16 v[120:123],a[172:175],v[248:251],v[120:123]
v_accvgpr_write_b32 a112,v16
v_accvgpr_write_b32 a113,v17
v_mfma_f32_16x16x32_bf16 v[124:127],a[188:191],v[248:251],v[44:47]
v_accvgpr_write_b32 a114,v18
v_accvgpr_write_b32 a115,v19
v_accvgpr_write_b32 a116,v8
v_accvgpr_write_b32 a117,v9
v_accvgpr_write_b32 a118,v10
v_accvgpr_write_b32 a119,v11
v_exp_f32 v80,v80
v_exp_f32 v81,v81
v_exp_f32 v82,v82
v_exp_f32 v83,v83
v_exp_f32 v84,v84
v_exp_f32 v85,v85
v_exp_f32 v86,v86
v_exp_f32 v87,v87
v_mfma_f32_16x16x32_bf16 v[124:127],a[184:187],v[240:243],v[124:127]
v_cvt_pk_bf16_f32 v80,v80,v81
v_cvt_pk_bf16_f32 v81,v82,v83
v_cvt_pk_bf16_f32 v82,v84,v85
v_cvt_pk_bf16_f32 v83,v86,v87
v_exp_f32 v88,v88
v_exp_f32 v89,v89
v_exp_f32 v90,v90
v_exp_f32 v91,v91
v_exp_f32 v92,v92
v_exp_f32 v93,v93
v_exp_f32 v94,v94
v_exp_f32 v95,v95
v_mfma_f32_16x16x32_bf16 v[124:127],a[180:183],v[232:235],v[124:127]
v_cvt_pk_bf16_f32 v88,v88,v89
v_cvt_pk_bf16_f32 v89,v90,v91
v_cvt_pk_bf16_f32 v90,v92,v93
v_cvt_pk_bf16_f32 v91,v94,v95
v_mfma_f32_16x16x32_bf16 v[124:127],a[176:179],v[224:227],v[124:127]
s_add_u32 s10,s20,s74
s_addc_u32 s11,s21,0
s_add_u32 m0,s76,0
v_mfma_f32_16x16x32_bf16 v[132:135],a[192:195],v[80:83],v[132:135]
global_load_lds_dwordx4 v4,s[10:11] nt
v_add_u32 v1,s78,v1
v_mfma_f32_16x16x32_bf16 v[132:135],a[196:199],v[88:91],v[132:135]
ds_read_b128 a[128:131],v1
v_accvgpr_read_b32 v16,a120
v_mfma_f32_16x16x32_bf16 v[140:143],a[200:203],v[80:83],v[140:143]
v_exp_f32 v96,v96
v_mfma_f32_16x16x32_bf16 v[140:143],a[204:207],v[88:91],v[140:143]
ds_read_b128 a[132:135],v1 offset:256
v_accvgpr_read_b32 v17,a121
v_mfma_f32_16x16x32_bf16 v[148:151],a[208:211],v[80:83],v[148:151]
v_exp_f32 v97,v97
v_mfma_f32_16x16x32_bf16 v[148:151],a[212:215],v[88:91],v[148:151]
ds_read_b128 a[136:139],v1 offset:512
v_cvt_pk_bf16_f32 v96,v96,v97
v_mfma_f32_16x16x32_bf16 v[156:159],a[216:219],v[80:83],v[156:159]
v_exp_f32 v98,v98
v_mfma_f32_16x16x32_bf16 v[156:159],a[220:223],v[88:91],v[156:159]
ds_read_b128 a[140:143],v1 offset:768
v_accvgpr_read_b32 v18,a122
v_mfma_f32_16x16x32_bf16 v[164:167],a[224:227],v[80:83],v[164:167]
v_exp_f32 v99,v99
v_mfma_f32_16x16x32_bf16 v[164:167],a[228:231],v[88:91],v[164:167]
ds_read_b128 a[156:159],v1 offset:4864
v_cvt_pk_bf16_f32 v97,v98,v99
v_mfma_f32_16x16x32_bf16 v[172:175],a[232:235],v[80:83],v[172:175]
v_exp_f32 v100,v100
v_mfma_f32_16x16x32_bf16 v[172:175],a[236:239],v[88:91],v[172:175]
ds_read_b128 a[152:155],v1 offset:4608
v_accvgpr_read_b32 v19,a123
v_mfma_f32_16x16x32_bf16 v[180:183],a[240:243],v[80:83],v[180:183]
v_exp_f32 v101,v101
v_mfma_f32_16x16x32_bf16 v[180:183],a[244:247],v[88:91],v[180:183]
ds_read_b128 a[148:151],v1 offset:4352
v_cvt_pk_bf16_f32 v98,v100,v101
v_mfma_f32_16x16x32_bf16 v[188:191],a[248:251],v[80:83],v[188:191]
global_load_lds_dwordx4 v4,s[10:11] offset:1024 nt
v_accvgpr_read_b32 v20,a124
v_mfma_f32_16x16x32_bf16 v[188:191],a[252:255],v[88:91],v[188:191]
ds_read_b128 a[144:147],v1 offset:4096
v_accvgpr_read_b32 v21,a125
v_mfma_f32_16x16x32_bf16 v[52:55],v[28:31],v[80:83],v[52:55]
v_exp_f32 v102,v102
v_mfma_f32_16x16x32_bf16 v[52:55],v[28:31],v[88:91],v[52:55]
s_waitcnt lgkmcnt(3)
ds_read_b128 a[160:163],v1 offset:8192
v_accvgpr_read_b32 v22,a126
v_mfma_f32_16x16x32_bf16 v[64:67],a[128:131],v[128:131],v[32:35]
v_exp_f32 v103,v103
v_mfma_f32_16x16x32_bf16 v[64:67],a[132:135],v[136:139],v[64:67]
ds_read_b128 a[164:167],v1 offset:8448
v_cvt_pk_bf16_f32 v99,v102,v103
v_mfma_f32_16x16x32_bf16 v[64:67],a[136:139],v[144:147],v[64:67]
v_exp_f32 v104,v104
v_mfma_f32_16x16x32_bf16 v[64:67],a[140:143],v[152:155],v[64:67]
ds_read_b128 a[168:171],v1 offset:8704
v_accvgpr_read_b32 v23,a127
v_mfma_f32_16x16x32_bf16 v[68:71],a[156:159],v[152:155],v[32:35]
s_waitcnt lgkmcnt(4)
v_exp_f32 v105,v105
v_mfma_f32_16x16x32_bf16 v[68:71],a[152:155],v[144:147],v[68:71]
ds_read_b128 a[172:175],v1 offset:8960
v_cvt_pk_bf16_f32 v104,v104,v105
v_mfma_f32_16x16x32_bf16 v[68:71],a[148:151],v[136:139],v[68:71]
s_waitcnt lgkmcnt(4)
v_exp_f32 v106,v106
v_mfma_f32_16x16x32_bf16 v[68:71],a[144:147],v[128:131],v[68:71]
s_waitcnt lgkmcnt(3)
ds_read_b128 a[188:191],v1 offset:13056
v_mul_f32 v16,v16,v27
v_mfma_f32_16x16x32_bf16 v[72:75],a[160:163],v[128:131],v[32:35]
s_waitcnt lgkmcnt(3)
v_exp_f32 v107,v107
v_mfma_f32_16x16x32_bf16 v[72:75],a[164:167],v[136:139],v[72:75]
s_waitcnt lgkmcnt(1)
ds_read_b128 a[184:187],v1 offset:12800
v_cvt_pk_bf16_f32 v105,v106,v107
v_mfma_f32_16x16x32_bf16 v[72:75],a[168:171],v[144:147],v[72:75]
global_load_lds_dwordx4 v5,s[10:11] offset:2048 nt
ds_read_b128 a[180:183],v1 offset:12544
v_mfma_f32_16x16x32_bf16 v[72:75],a[172:175],v[152:155],v[72:75]
s_waitcnt lgkmcnt(2)
ds_read_b128 a[176:179],v1 offset:12288
v_mul_f32 v17,v17,v27
v_mfma_f32_16x16x32_bf16 v[76:79],a[188:191],v[152:155],v[32:35]
s_waitcnt lgkmcnt(2)
v_exp_f32 v108,v108
v_mfma_f32_16x16x32_bf16 v[76:79],a[184:187],v[144:147],v[76:79]
s_waitcnt lgkmcnt(1)
v_exp_f32 v109,v109
v_exp_f32 v110,v110
v_exp_f32 v111,v111
v_mfma_f32_16x16x32_bf16 v[76:79],a[180:183],v[136:139],v[76:79]
s_waitcnt lgkmcnt(0)
v_cvt_pk_bf16_f32 v106,v108,v109
v_cvt_pk_bf16_f32 v107,v110,v111
v_mfma_f32_16x16x32_bf16 v[76:79],a[176:179],v[128:131],v[76:79]
v_mul_f32 v18,v18,v27
v_mul_f32 v19,v19,v27
v_mfma_f32_16x16x32_bf16 v[196:199],a[192:195],v[96:99],v[196:199]
v_mul_f32 v20,v20,v27
v_mul_f32 v21,v21,v27
v_mfma_f32_16x16x32_bf16 v[196:199],a[196:199],v[104:107],v[196:199]
v_mul_f32 v22,v22,v27
v_mul_f32 v23,v23,v27
v_mfma_f32_16x16x32_bf16 v[204:207],a[200:203],v[96:99],v[204:207]
v_cvt_pk_bf16_f32 v16,v16,v17
v_cvt_pk_bf16_f32 v17,v18,v19
v_mfma_f32_16x16x32_bf16 v[204:207],a[204:207],v[104:107],v[204:207]
v_cvt_pk_bf16_f32 v18,v20,v21
v_cvt_pk_bf16_f32 v19,v22,v23
v_mfma_f32_16x16x32_bf16 v[212:215],a[208:211],v[96:99],v[212:215]
v_accvgpr_write_b32 a120,v16
v_accvgpr_write_b32 a121,v17
v_mfma_f32_16x16x32_bf16 v[212:215],a[212:215],v[104:107],v[212:215]
v_accvgpr_write_b32 a122,v18
v_accvgpr_write_b32 a123,v19
v_mfma_f32_16x16x32_bf16 v[220:223],a[216:219],v[96:99],v[220:223]
v_accvgpr_write_b32 a124,v12
v_accvgpr_write_b32 a125,v13
v_mfma_f32_16x16x32_bf16 v[220:223],a[220:223],v[104:107],v[220:223]
v_accvgpr_write_b32 a126,v14
v_accvgpr_write_b32 a127,v15
v_mfma_f32_16x16x32_bf16 v[228:231],a[224:227],v[96:99],v[228:231]
global_load_lds_dwordx4 v5,s[10:11] offset:3072
v_add_u32 v2,s78,v2
v_mfma_f32_16x16x32_bf16 v[228:231],a[228:231],v[104:107],v[228:231]
v_exp_f32 v112,v112
v_mfma_f32_16x16x32_bf16 v[236:239],a[232:235],v[96:99],v[236:239]
v_exp_f32 v113,v113
v_mfma_f32_16x16x32_bf16 v[236:239],a[236:239],v[104:107],v[236:239]
v_exp_f32 v114,v114
v_mfma_f32_16x16x32_bf16 v[244:247],a[240:243],v[96:99],v[244:247]
v_exp_f32 v115,v115
v_mfma_f32_16x16x32_bf16 v[244:247],a[244:247],v[104:107],v[244:247]
v_exp_f32 v116,v116
v_mfma_f32_16x16x32_bf16 v[252:255],a[248:251],v[96:99],v[252:255]
v_exp_f32 v117,v117
v_mfma_f32_16x16x32_bf16 v[252:255],a[252:255],v[104:107],v[252:255]
v_exp_f32 v118,v118
v_mfma_f32_16x16x32_bf16 v[56:59],v[28:31],v[96:99],v[56:59]
v_exp_f32 v119,v119
v_mfma_f32_16x16x32_bf16 v[56:59],v[28:31],v[104:107],v[56:59]
v_cvt_pk_bf16_f32 v112,v112,v113
v_cvt_pk_bf16_f32 v113,v114,v115
v_mfma_f32_16x16x32_bf16 v[80:83],a[128:131],v[160:163],v[36:39]
v_cvt_pk_bf16_f32 v114,v116,v117
v_cvt_pk_bf16_f32 v115,v118,v119
v_mfma_f32_16x16x32_bf16 v[80:83],a[132:135],v[168:171],v[80:83]
s_add_u32 s10,s24,s74
s_addc_u32 s11,s25,0
v_add_u32 v3,s78,v3
v_mfma_f32_16x16x32_bf16 v[80:83],a[136:139],v[176:179],v[80:83]
s_add_u32 s74,s74,16384
v_exp_f32 v120,v120
v_mfma_f32_16x16x32_bf16 v[80:83],a[140:143],v[184:187],v[80:83]
s_add_u32 m0,s76,16384
v_exp_f32 v121,v121
v_mfma_f32_16x16x32_bf16 v[84:87],a[156:159],v[184:187],v[36:39]
s_and_b32 s74,s74,0x3ffff
v_exp_f32 v122,v122
v_mfma_f32_16x16x32_bf16 v[84:87],a[152:155],v[176:179],v[84:87]
global_load_lds_dwordx4 v4,s[10:11]
v_cvt_pk_bf16_f32 v120,v120,v121
v_mfma_f32_16x16x32_bf16 v[84:87],a[148:151],v[168:171],v[84:87]
s_or_b32 s74,s74,s69
v_exp_f32 v123,v123
v_mfma_f32_16x16x32_bf16 v[84:87],a[144:147],v[160:163],v[84:87]
s_sub_u32 s76,s76,32768
v_exp_f32 v124,v124
v_mfma_f32_16x16x32_bf16 v[88:91],a[160:163],v[160:163],v[36:39]
s_and_b32 s76,s76,0x1ffff
v_exp_f32 v125,v125
v_mfma_f32_16x16x32_bf16 v[88:91],a[164:167],v[168:171],v[88:91]
s_sub_u32 s78,s77,32768
v_exp_f32 v126,v126
v_mfma_f32_16x16x32_bf16 v[88:91],a[168:171],v[176:179],v[88:91]
s_and_b32 s78,s78,0x1ffff
v_exp_f32 v127,v127
v_mfma_f32_16x16x32_bf16 v[88:91],a[172:175],v[184:187],v[88:91]
s_sub_u32 s78,s78,s77
v_cvt_pk_bf16_f32 v121,v122,v123
v_cvt_pk_bf16_f32 v122,v124,v125
v_mfma_f32_16x16x32_bf16 v[92:95],a[188:191],v[184:187],v[36:39]
s_and_b32 s69,s75,0xfffc0000
v_cvt_pk_bf16_f32 v123,v126,v127
v_accvgpr_read_b32 v12,a100
v_mfma_f32_16x16x32_bf16 v[92:95],a[184:187],v[176:179],v[92:95]
s_add_u32 s77,s77,s78
v_exp_f32 v64,v64
v_mfma_f32_16x16x32_bf16 v[92:95],a[180:183],v[168:171],v[92:95]
s_add_u32 s75,s75,16384
v_exp_f32 v65,v65
v_mfma_f32_16x16x32_bf16 v[92:95],a[176:179],v[160:163],v[92:95]
v_exp_f32 v66,v66
v_mfma_f32_16x16x32_bf16 a[32:35],a[192:195],v[112:115],a[32:35]
ds_read_b64_tr_b16 a[192:193],v2
ds_read_b64_tr_b16 a[194:195],v2 offset:4096
v_mfma_f32_16x16x32_bf16 a[32:35],a[196:199],v[120:123],a[32:35]
ds_read_b64_tr_b16 a[196:197],v2 offset:8192
v_cvt_pk_bf16_f32 v64,v64,v65
v_mfma_f32_16x16x32_bf16 a[36:39],a[200:203],v[112:115],a[36:39]
ds_read_b64_tr_b16 a[198:199],v2 offset:12288
v_accvgpr_read_b32 v13,a101
v_mfma_f32_16x16x32_bf16 a[36:39],a[204:207],v[120:123],a[36:39]
global_load_lds_dwordx4 v5,s[10:11] offset:1024
ds_read_b64_tr_b16 a[200:201],v3
v_mfma_f32_16x16x32_bf16 a[40:43],a[208:211],v[112:115],a[40:43]
ds_read_b64_tr_b16 a[202:203],v3 offset:4096
ds_read_b64_tr_b16 a[204:205],v3 offset:8192
v_mfma_f32_16x16x32_bf16 a[40:43],a[212:215],v[120:123],a[40:43]
ds_read_b64_tr_b16 a[206:207],v3 offset:12288
v_accvgpr_read_b32 v14,a102
v_mfma_f32_16x16x32_bf16 a[44:47],a[216:219],v[112:115],a[44:47]
ds_read_b64_tr_b16 a[208:209],v2 offset:256
v_accvgpr_read_b32 v15,a103
v_mfma_f32_16x16x32_bf16 a[44:47],a[220:223],v[120:123],a[44:47]
ds_read_b64_tr_b16 a[210:211],v2 offset:4352
v_accvgpr_read_b32 v8,a96
v_mfma_f32_16x16x32_bf16 a[48:51],a[224:227],v[112:115],a[48:51]
)A"
R"A(ds_read_b64_tr_b16 a[212:213],v2 offset:8448
ds_read_b64_tr_b16 a[214:215],v2 offset:12544
v_mfma_f32_16x16x32_bf16 a[48:51],a[228:231],v[120:123],a[48:51]
v_exp_f32 v67,v67
v_mfma_f32_16x16x32_bf16 a[52:55],a[232:235],v[112:115],a[52:55]
v_exp_f32 v68,v68
v_mfma_f32_16x16x32_bf16 a[52:55],a[236:239],v[120:123],a[52:55]
v_exp_f32 v69,v69
v_mfma_f32_16x16x32_bf16 a[56:59],a[240:243],v[112:115],a[56:59]
v_exp_f32 v70,v70
v_mfma_f32_16x16x32_bf16 a[56:59],a[244:247],v[120:123],a[56:59]
v_exp_f32 v71,v71
v_mfma_f32_16x16x32_bf16 a[60:63],a[248:251],v[112:115],a[60:63]
ds_read_b64_tr_b16 a[216:217],v3 offset:256
ds_read_b64_tr_b16 a[218:219],v3 offset:4352
v_mfma_f32_16x16x32_bf16 a[60:63],a[252:255],v[120:123],a[60:63]
v_cvt_pk_bf16_f32 v65,v66,v67
v_cvt_pk_bf16_f32 v66,v68,v69
v_mfma_f32_16x16x32_bf16 v[60:63],v[28:31],v[112:115],v[60:63]
ds_read_b64_tr_b16 a[220:221],v3 offset:8448
v_cvt_pk_bf16_f32 v67,v70,v71
v_mfma_f32_16x16x32_bf16 v[60:63],v[28:31],v[120:123],v[60:63]
global_load_lds_dwordx4 v4,s[10:11] offset:2048
ds_read_b64_tr_b16 a[222:223],v3 offset:12544
v_mfma_f32_16x16x32_bf16 v[96:99],a[128:131],v[192:195],v[40:43]
v_exp_f32 v72,v72
v_mfma_f32_16x16x32_bf16 v[96:99],a[132:135],v[200:203],v[96:99]
v_exp_f32 v73,v73
v_mfma_f32_16x16x32_bf16 v[96:99],a[136:139],v[208:211],v[96:99]
v_exp_f32 v74,v74
v_mfma_f32_16x16x32_bf16 v[96:99],a[140:143],v[216:219],v[96:99]
v_exp_f32 v75,v75
v_mfma_f32_16x16x32_bf16 v[100:103],a[156:159],v[216:219],v[40:43]
v_exp_f32 v76,v76
v_mfma_f32_16x16x32_bf16 v[100:103],a[152:155],v[208:211],v[100:103]
v_exp_f32 v77,v77
v_mfma_f32_16x16x32_bf16 v[100:103],a[148:151],v[200:203],v[100:103]
v_exp_f32 v78,v78
v_mfma_f32_16x16x32_bf16 v[100:103],a[144:147],v[192:195],v[100:103]
v_exp_f32 v79,v79
v_mfma_f32_16x16x32_bf16 v[104:107],a[160:163],v[192:195],v[40:43]
v_cvt_pk_bf16_f32 v72,v72,v73
v_cvt_pk_bf16_f32 v73,v74,v75
v_mfma_f32_16x16x32_bf16 v[104:107],a[164:167],v[200:203],v[104:107]
v_cvt_pk_bf16_f32 v74,v76,v77
v_cvt_pk_bf16_f32 v75,v78,v79
v_mfma_f32_16x16x32_bf16 v[104:107],a[168:171],v[208:211],v[104:107]
ds_read_b64_tr_b16 a[224:225],v2 offset:512
ds_read_b64_tr_b16 a[226:227],v2 offset:4608
v_mfma_f32_16x16x32_bf16 v[104:107],a[172:175],v[216:219],v[104:107]
v_accvgpr_read_b32 v9,a97
v_accvgpr_read_b32 v10,a98
v_mfma_f32_16x16x32_bf16 v[108:111],a[188:191],v[216:219],v[40:43]
v_accvgpr_read_b32 v11,a99
v_mul_f32 v8,v8,v27
v_mfma_f32_16x16x32_bf16 v[108:111],a[184:187],v[208:211],v[108:111]
global_load_lds_dwordx4 v5,s[10:11] offset:3072
v_mul_f32 v9,v9,v27
v_mfma_f32_16x16x32_bf16 v[108:111],a[180:183],v[200:203],v[108:111]
v_mul_f32 v10,v10,v27
v_mul_f32 v11,v11,v27
v_mfma_f32_16x16x32_bf16 v[108:111],a[176:179],v[192:195],v[108:111]
s_waitcnt lgkmcnt(2)
v_mul_f32 v12,v12,v27
v_mul_f32 v13,v13,v27
v_mul_f32 v14,v14,v27
v_mul_f32 v15,v15,v27
v_cvt_pk_bf16_f32 v8,v8,v9
v_cvt_pk_bf16_f32 v9,v10,v11
v_cvt_pk_bf16_f32 v10,v12,v13
v_cvt_pk_bf16_f32 v11,v14,v15
v_accvgpr_write_b32 a96,v8
v_accvgpr_write_b32 a97,v9
v_accvgpr_write_b32 a98,v10
v_accvgpr_write_b32 a99,v11
v_mfma_f32_16x16x32_bf16 a[0:3],a[192:195],v[64:67],a[0:3]
ds_read_b128 a[100:103],v7 offset:2048
v_add_u32 v1,s78,v1
v_mfma_f32_16x16x32_bf16 a[0:3],a[196:199],v[72:75],a[0:3]
ds_read_b64_tr_b16 a[228:229],v2 offset:8704
v_accvgpr_read_b32 v20,a108
v_mfma_f32_16x16x32_bf16 a[4:7],a[200:203],v[64:67],a[4:7]
ds_read_b64_tr_b16 a[230:231],v2 offset:12800
v_accvgpr_read_b32 v21,a109
v_mfma_f32_16x16x32_bf16 a[4:7],a[204:207],v[72:75],a[4:7]
ds_read_b64_tr_b16 a[232:233],v3 offset:512
v_accvgpr_read_b32 v22,a110
v_mfma_f32_16x16x32_bf16 a[8:11],a[208:211],v[64:67],a[8:11]
ds_read_b64_tr_b16 a[234:235],v3 offset:4608
ds_read_b64_tr_b16 a[236:237],v3 offset:8704
v_mfma_f32_16x16x32_bf16 a[8:11],a[212:215],v[72:75],a[8:11]
ds_read_b64_tr_b16 a[238:239],v3 offset:12800
v_accvgpr_read_b32 v23,a111
v_mfma_f32_16x16x32_bf16 a[12:15],a[216:219],v[64:67],a[12:15]
ds_read_b64_tr_b16 a[240:241],v2 offset:768
v_accvgpr_read_b32 v16,a104
v_mfma_f32_16x16x32_bf16 a[12:15],a[220:223],v[72:75],a[12:15]
s_waitcnt lgkmcnt(8)
ds_read_b64_tr_b16 a[242:243],v2 offset:4864
v_accvgpr_read_b32 v17,a105
v_mfma_f32_16x16x32_bf16 a[16:19],a[224:227],v[64:67],a[16:19]
s_waitcnt lgkmcnt(6)
ds_read_b64_tr_b16 a[244:245],v2 offset:8960
ds_read_b64_tr_b16 a[246:247],v2 offset:13056
v_mfma_f32_16x16x32_bf16 a[16:19],a[228:231],v[72:75],a[16:19]
s_waitcnt lgkmcnt(0)
ds_read_b64_tr_b16 a[248:249],v3 offset:768
v_accvgpr_read_b32 v18,a106
v_mfma_f32_16x16x32_bf16 a[20:23],a[232:235],v[64:67],a[20:23]
s_getreg_b32 s92,hwreg(HW_REG_IB_STS,0,4)
s_min_u32 s4,s92,s6
ds_read_b64_tr_b16 a[250:251],v3 offset:4864
v_mfma_f32_16x16x32_bf16 a[20:23],a[236:239],v[72:75],a[20:23]
s_mov_b32 s6,s92
ds_read_b64_tr_b16 a[252:253],v3 offset:8960
ds_read_b64_tr_b16 a[254:255],v3 offset:13056
v_mfma_f32_16x16x32_bf16 a[24:27],a[240:243],v[64:67],a[24:27]
s_cmp_gt_u32 s4,s9
s_cselect_b32 s4,15,s5
v_accvgpr_read_b32 v19,a107
v_mfma_f32_16x16x32_bf16 a[24:27],a[244:247],v[72:75],a[24:27]
s_waitcnt lgkmcnt(2)
v_exp_f32 v80,v80
v_mfma_f32_16x16x32_bf16 a[28:31],a[248:251],v[64:67],a[28:31]
s_waitcnt lgkmcnt(0)
v_exp_f32 v81,v81
v_mfma_f32_16x16x32_bf16 a[28:31],a[252:255],v[72:75],a[28:31]
s_getpc_b64 s[92:93]
s_cmp_lg_u32 s4,0
s_cbranch_scc1 L_pace_%=
v_mfma_f32_16x16x32_bf16 v[48:51],v[28:31],v[64:67],v[48:51]
v_exp_f32 v82,v82
v_mfma_f32_16x16x32_bf16 v[48:51],v[28:31],v[72:75],v[48:51]
s_add_u32 s10,s20,s74
s_addc_u32 s11,s21,0
v_cvt_pk_bf16_f32 v80,v80,v81
s_waitcnt vmcnt(16)
s_barrier
v_mfma_f32_16x16x32_bf16 v[112:115],a[128:131],v[224:227],v[44:47]
s_add_u32 m0,s76,0
v_exp_f32 v83,v83
v_mfma_f32_16x16x32_bf16 v[112:115],a[132:135],v[232:235],v[112:115]
v_exp_f32 v84,v84
v_mfma_f32_16x16x32_bf16 v[112:115],a[136:139],v[240:243],v[112:115]
global_load_lds_dwordx4 v4,s[10:11] nt
v_cvt_pk_bf16_f32 v81,v82,v83
v_mfma_f32_16x16x32_bf16 v[112:115],a[140:143],v[248:251],v[112:115]
v_exp_f32 v85,v85
v_mfma_f32_16x16x32_bf16 v[116:119],a[156:159],v[248:251],v[44:47]
v_exp_f32 v86,v86
v_mfma_f32_16x16x32_bf16 v[116:119],a[152:155],v[240:243],v[116:119]
v_exp_f32 v87,v87
v_mfma_f32_16x16x32_bf16 v[116:119],a[148:151],v[232:235],v[116:119]
v_cvt_pk_bf16_f32 v82,v84,v85
v_cvt_pk_bf16_f32 v83,v86,v87
v_mfma_f32_16x16x32_bf16 v[116:119],a[144:147],v[224:227],v[116:119]
v_exp_f32 v88,v88
v_mfma_f32_16x16x32_bf16 v[120:123],a[160:163],v[224:227],v[44:47]
v_exp_f32 v89,v89
v_mfma_f32_16x16x32_bf16 v[120:123],a[164:167],v[232:235],v[120:123]
v_exp_f32 v90,v90
v_mfma_f32_16x16x32_bf16 v[120:123],a[168:171],v[240:243],v[120:123]
v_exp_f32 v91,v91
v_mfma_f32_16x16x32_bf16 v[120:123],a[172:175],v[248:251],v[120:123]
v_exp_f32 v92,v92
v_mfma_f32_16x16x32_bf16 v[124:127],a[188:191],v[248:251],v[44:47]
v_exp_f32 v93,v93
v_mfma_f32_16x16x32_bf16 v[124:127],a[184:187],v[240:243],v[124:127]
v_exp_f32 v94,v94
v_exp_f32 v95,v95
v_mfma_f32_16x16x32_bf16 v[124:127],a[180:183],v[232:235],v[124:127]
v_cvt_pk_bf16_f32 v88,v88,v89
v_cvt_pk_bf16_f32 v89,v90,v91
v_cvt_pk_bf16_f32 v90,v92,v93
v_cvt_pk_bf16_f32 v91,v94,v95
v_mfma_f32_16x16x32_bf16 v[124:127],a[176:179],v[224:227],v[124:127]
ds_read_b128 a[128:131],v1
v_mul_f32 v16,v16,v27
v_mfma_f32_16x16x32_bf16 v[132:135],a[192:195],v[80:83],v[132:135]
v_mul_f32 v17,v17,v27
v_mul_f32 v18,v18,v27
v_mfma_f32_16x16x32_bf16 v[132:135],a[196:199],v[88:91],v[132:135]
ds_read_b128 a[132:135],v1 offset:256
v_mul_f32 v19,v19,v27
v_mfma_f32_16x16x32_bf16 v[140:143],a[200:203],v[80:83],v[140:143]
global_load_lds_dwordx4 v4,s[10:11] offset:1024 nt
v_mul_f32 v20,v20,v27
v_mfma_f32_16x16x32_bf16 v[140:143],a[204:207],v[88:91],v[140:143]
ds_read_b128 a[136:139],v1 offset:512
v_mul_f32 v21,v21,v27
v_mfma_f32_16x16x32_bf16 v[148:151],a[208:211],v[80:83],v[148:151]
v_mul_f32 v22,v22,v27
v_mul_f32 v23,v23,v27
v_mfma_f32_16x16x32_bf16 v[148:151],a[212:215],v[88:91],v[148:151]
ds_read_b128 a[140:143],v1 offset:768
v_cvt_pk_bf16_f32 v16,v16,v17
v_mfma_f32_16x16x32_bf16 v[156:159],a[216:219],v[80:83],v[156:159]
v_cvt_pk_bf16_f32 v17,v18,v19
v_cvt_pk_bf16_f32 v18,v20,v21
v_mfma_f32_16x16x32_bf16 v[156:159],a[220:223],v[88:91],v[156:159]
ds_read_b128 a[156:159],v1 offset:4864
v_cvt_pk_bf16_f32 v19,v22,v23
v_mfma_f32_16x16x32_bf16 v[164:167],a[224:227],v[80:83],v[164:167]
v_accvgpr_write_b32 a104,v16
v_accvgpr_write_b32 a105,v17
v_mfma_f32_16x16x32_bf16 v[164:167],a[228:231],v[88:91],v[164:167]
ds_read_b128 a[152:155],v1 offset:4608
s_nop 0
L_pd9_%=:
s_cbranch_execz L_pd13_%=
v_accvgpr_write_b32 a106,v18
v_mfma_f32_16x16x32_bf16 v[172:175],a[232:235],v[80:83],v[172:175]
v_accvgpr_write_b32 a107,v19
v_accvgpr_read_b32 v12,a68
v_mfma_f32_16x16x32_bf16 v[172:175],a[236:239],v[88:91],v[172:175]
ds_read_b128 a[148:151],v1 offset:4352
v_accvgpr_read_b32 v13,a69
v_mfma_f32_16x16x32_bf16 v[180:183],a[240:243],v[80:83],v[180:183]
v_exp_f32 v96,v96
v_mfma_f32_16x16x32_bf16 v[180:183],a[244:247],v[88:91],v[180:183]
ds_read_b128 a[144:147],v1 offset:4096
v_accvgpr_read_b32 v14,a70
v_mfma_f32_16x16x32_bf16 v[188:191],a[248:251],v[80:83],v[188:191]
v_exp_f32 v97,v97
v_mfma_f32_16x16x32_bf16 v[188:191],a[252:255],v[88:91],v[188:191]
ds_read_b128 a[160:163],v1 offset:8192
v_cvt_pk_bf16_f32 v96,v96,v97
v_mfma_f32_16x16x32_bf16 v[52:55],v[28:31],v[80:83],v[52:55]
v_exp_f32 v98,v98
v_mfma_f32_16x16x32_bf16 v[52:55],v[28:31],v[88:91],v[52:55]
s_waitcnt lgkmcnt(3)
ds_read_b128 a[164:167],v1 offset:8448
v_accvgpr_read_b32 v15,a71
v_mfma_f32_16x16x32_bf16 v[64:67],a[128:131],v[128:131],v[32:35]
global_load_lds_dwordx4 v5,s[10:11] offset:2048 nt
v_accvgpr_read_b32 v8,a64
v_mfma_f32_16x16x32_bf16 v[64:67],a[132:135],v[136:139],v[64:67]
ds_read_b128 a[168:171],v1 offset:8704
v_accvgpr_read_b32 v9,a65
v_mfma_f32_16x16x32_bf16 v[64:67],a[136:139],v[144:147],v[64:67]
v_exp_f32 v99,v99
v_mfma_f32_16x16x32_bf16 v[64:67],a[140:143],v[152:155],v[64:67]
ds_read_b128 a[108:111],v7 offset:3072
v_cvt_pk_bf16_f32 v97,v98,v99
v_mfma_f32_16x16x32_bf16 v[68:71],a[156:159],v[152:155],v[32:35]
v_exp_f32 v100,v100
v_mfma_f32_16x16x32_bf16 v[68:71],a[152:155],v[144:147],v[68:71]
s_waitcnt lgkmcnt(3)
ds_read_b128 a[172:175],v1 offset:8960
v_accvgpr_read_b32 v10,a66
v_mfma_f32_16x16x32_bf16 v[68:71],a[148:151],v[136:139],v[68:71]
v_exp_f32 v101,v101
v_mfma_f32_16x16x32_bf16 v[68:71],a[144:147],v[128:131],v[68:71]
ds_read_b128 a[188:191],v1 offset:13056
v_cvt_pk_bf16_f32 v98,v100,v101
v_mfma_f32_16x16x32_bf16 v[72:75],a[160:163],v[128:131],v[32:35]
s_waitcnt lgkmcnt(4)
v_exp_f32 v102,v102
v_mfma_f32_16x16x32_bf16 v[72:75],a[164:167],v[136:139],v[72:75]
s_waitcnt lgkmcnt(3)
ds_read_b128 a[184:187],v1 offset:12800
v_accvgpr_read_b32 v11,a67
v_mfma_f32_16x16x32_bf16 v[72:75],a[168:171],v[144:147],v[72:75]
s_waitcnt lgkmcnt(2)
ds_read_b128 a[180:183],v1 offset:12544
v_mul_f32 v8,v8,v26
v_mfma_f32_16x16x32_bf16 v[72:75],a[172:175],v[152:155],v[72:75]
s_waitcnt lgkmcnt(2)
ds_read_b128 a[176:179],v1 offset:12288
v_mul_f32 v9,v9,v26
v_mfma_f32_16x16x32_bf16 v[76:79],a[188:191],v[152:155],v[32:35]
s_waitcnt lgkmcnt(2)
v_exp_f32 v103,v103
v_mfma_f32_16x16x32_bf16 v[76:79],a[184:187],v[144:147],v[76:79]
s_waitcnt lgkmcnt(1)
v_cvt_pk_bf16_f32 v99,v102,v103
v_exp_f32 v104,v104
v_exp_f32 v105,v105
v_exp_f32 v106,v106
v_exp_f32 v107,v107
v_exp_f32 v108,v108
v_exp_f32 v109,v109
v_exp_f32 v110,v110
v_exp_f32 v111,v111
v_mfma_f32_16x16x32_bf16 v[76:79],a[180:183],v[136:139],v[76:79]
s_waitcnt lgkmcnt(0)
v_cvt_pk_bf16_f32 v104,v104,v105
v_cvt_pk_bf16_f32 v105,v106,v107
v_cvt_pk_bf16_f32 v106,v108,v109
v_cvt_pk_bf16_f32 v107,v110,v111
v_mfma_f32_16x16x32_bf16 v[76:79],a[176:179],v[128:131],v[76:79]
v_exp_f32 v112,v112
v_mfma_f32_16x16x32_bf16 v[196:199],a[192:195],v[96:99],v[196:199]
global_load_lds_dwordx4 v5,s[10:11] offset:3072
v_mul_f32 v10,v10,v26
v_mfma_f32_16x16x32_bf16 v[196:199],a[196:199],v[104:107],v[196:199]
v_exp_f32 v113,v113
v_mfma_f32_16x16x32_bf16 v[204:207],a[200:203],v[96:99],v[204:207]
v_exp_f32 v114,v114
v_mfma_f32_16x16x32_bf16 v[204:207],a[204:207],v[104:107],v[204:207]
v_exp_f32 v115,v115
)A"
R"A(v_mfma_f32_16x16x32_bf16 v[212:215],a[208:211],v[96:99],v[212:215]
v_exp_f32 v116,v116
v_mfma_f32_16x16x32_bf16 v[212:215],a[212:215],v[104:107],v[212:215]
v_exp_f32 v117,v117
v_mfma_f32_16x16x32_bf16 v[220:223],a[216:219],v[96:99],v[220:223]
v_exp_f32 v118,v118
v_mfma_f32_16x16x32_bf16 v[220:223],a[220:223],v[104:107],v[220:223]
v_exp_f32 v119,v119
v_mfma_f32_16x16x32_bf16 v[228:231],a[224:227],v[96:99],v[228:231]
v_cvt_pk_bf16_f32 v112,v112,v113
v_cvt_pk_bf16_f32 v113,v114,v115
v_mfma_f32_16x16x32_bf16 v[228:231],a[228:231],v[104:107],v[228:231]
v_cvt_pk_bf16_f32 v114,v116,v117
v_cvt_pk_bf16_f32 v115,v118,v119
v_mfma_f32_16x16x32_bf16 v[236:239],a[232:235],v[96:99],v[236:239]
v_exp_f32 v120,v120
v_mfma_f32_16x16x32_bf16 v[236:239],a[236:239],v[104:107],v[236:239]
v_exp_f32 v121,v121
v_mfma_f32_16x16x32_bf16 v[244:247],a[240:243],v[96:99],v[244:247]
s_add_u32 s10,s24,s74
s_addc_u32 s11,s25,0
v_cvt_pk_bf16_f32 v120,v120,v121
v_mfma_f32_16x16x32_bf16 v[244:247],a[244:247],v[104:107],v[244:247]
s_add_u32 s74,s74,16384
v_exp_f32 v122,v122
v_mfma_f32_16x16x32_bf16 v[252:255],a[248:251],v[96:99],v[252:255]
s_add_u32 m0,s76,16384
v_exp_f32 v123,v123
v_mfma_f32_16x16x32_bf16 v[252:255],a[252:255],v[104:107],v[252:255]
s_and_b32 s74,s74,0x3ffff
v_exp_f32 v124,v124
v_mfma_f32_16x16x32_bf16 v[56:59],v[28:31],v[96:99],v[56:59]
global_load_lds_dwordx4 v4,s[10:11]
v_cvt_pk_bf16_f32 v121,v122,v123
v_mfma_f32_16x16x32_bf16 v[56:59],v[28:31],v[104:107],v[56:59]
s_or_b32 s74,s74,s69
v_exp_f32 v125,v125
v_mfma_f32_16x16x32_bf16 v[80:83],a[128:131],v[160:163],v[36:39]
s_sub_u32 s76,s76,32768
v_exp_f32 v126,v126
v_mfma_f32_16x16x32_bf16 v[80:83],a[132:135],v[168:171],v[80:83]
s_and_b32 s76,s76,0x1ffff
v_exp_f32 v127,v127
v_mfma_f32_16x16x32_bf16 v[80:83],a[136:139],v[176:179],v[80:83]
s_and_b32 s69,s75,0xfffc0000
v_cvt_pk_bf16_f32 v122,v124,v125
v_cvt_pk_bf16_f32 v123,v126,v127
v_mfma_f32_16x16x32_bf16 v[80:83],a[140:143],v[184:187],v[80:83]
v_mul_f32 v11,v11,v26
v_mul_f32 v12,v12,v26
v_mfma_f32_16x16x32_bf16 v[84:87],a[156:159],v[184:187],v[36:39]
v_mul_f32 v13,v13,v26
v_mul_f32 v14,v14,v26
v_mfma_f32_16x16x32_bf16 v[84:87],a[152:155],v[176:179],v[84:87]
v_mul_f32 v15,v15,v26
v_cvt_pk_bf16_f32 v8,v8,v9
v_mfma_f32_16x16x32_bf16 v[84:87],a[148:151],v[168:171],v[84:87]
v_cvt_pk_bf16_f32 v9,v10,v11
v_cvt_pk_bf16_f32 v10,v12,v13
v_mfma_f32_16x16x32_bf16 v[84:87],a[144:147],v[160:163],v[84:87]
v_cvt_pk_bf16_f32 v11,v14,v15
v_accvgpr_write_b32 a64,v8
v_mfma_f32_16x16x32_bf16 v[88:91],a[160:163],v[160:163],v[36:39]
v_accvgpr_write_b32 a65,v9
v_accvgpr_write_b32 a66,v10
v_mfma_f32_16x16x32_bf16 v[88:91],a[164:167],v[168:171],v[88:91]
ds_read_b128 a[68:71],v7 offset:6144
v_accvgpr_write_b32 a67,v11
v_mfma_f32_16x16x32_bf16 v[88:91],a[168:171],v[176:179],v[88:91]
v_add_u32 v2,s78,v2
v_add_u32 v3,s78,v3
v_mfma_f32_16x16x32_bf16 v[88:91],a[172:175],v[184:187],v[88:91]
s_sub_u32 s78,s77,32768
v_exp_f32 v64,v64
v_mfma_f32_16x16x32_bf16 v[92:95],a[188:191],v[184:187],v[36:39]
s_and_b32 s78,s78,0x1ffff
v_exp_f32 v65,v65
v_mfma_f32_16x16x32_bf16 v[92:95],a[184:187],v[176:179],v[92:95]
s_sub_u32 s78,s78,s77
v_exp_f32 v66,v66
v_mfma_f32_16x16x32_bf16 v[92:95],a[180:183],v[168:171],v[92:95]
global_load_lds_dwordx4 v5,s[10:11] offset:1024
v_cvt_pk_bf16_f32 v64,v64,v65
v_mfma_f32_16x16x32_bf16 v[92:95],a[176:179],v[160:163],v[92:95]
s_add_u32 s77,s77,s78
v_exp_f32 v67,v67
v_mfma_f32_16x16x32_bf16 a[32:35],a[192:195],v[112:115],a[32:35]
ds_read_b64_tr_b16 a[192:193],v2
ds_read_b64_tr_b16 a[194:195],v2 offset:4096
v_mfma_f32_16x16x32_bf16 a[32:35],a[196:199],v[120:123],a[32:35]
ds_read_b64_tr_b16 a[196:197],v2 offset:8192
v_cvt_pk_bf16_f32 v65,v66,v67
v_mfma_f32_16x16x32_bf16 a[36:39],a[200:203],v[112:115],a[36:39]
ds_read_b64_tr_b16 a[198:199],v2 offset:12288
v_accvgpr_read_b32 v20,a76
v_mfma_f32_16x16x32_bf16 a[36:39],a[204:207],v[120:123],a[36:39]
ds_read_b64_tr_b16 a[200:201],v3
v_accvgpr_read_b32 v21,a77
v_mfma_f32_16x16x32_bf16 a[40:43],a[208:211],v[112:115],a[40:43]
ds_read_b64_tr_b16 a[202:203],v3 offset:4096
ds_read_b64_tr_b16 a[204:205],v3 offset:8192
v_mfma_f32_16x16x32_bf16 a[40:43],a[212:215],v[120:123],a[40:43]
ds_read_b64_tr_b16 a[206:207],v3 offset:12288
v_accvgpr_read_b32 v22,a78
v_mfma_f32_16x16x32_bf16 a[44:47],a[216:219],v[112:115],a[44:47]
ds_read_b64_tr_b16 a[208:209],v2 offset:256
v_accvgpr_read_b32 v23,a79
v_mfma_f32_16x16x32_bf16 a[44:47],a[220:223],v[120:123],a[44:47]
ds_read_b64_tr_b16 a[210:211],v2 offset:4352
v_accvgpr_read_b32 v16,a72
v_mfma_f32_16x16x32_bf16 a[48:51],a[224:227],v[112:115],a[48:51]
ds_read_b64_tr_b16 a[212:213],v2 offset:8448
ds_read_b64_tr_b16 a[214:215],v2 offset:12544
v_mfma_f32_16x16x32_bf16 a[48:51],a[228:231],v[120:123],a[48:51]
v_exp_f32 v68,v68
v_mfma_f32_16x16x32_bf16 a[52:55],a[232:235],v[112:115],a[52:55]
v_exp_f32 v69,v69
v_mfma_f32_16x16x32_bf16 a[52:55],a[236:239],v[120:123],a[52:55]
v_exp_f32 v70,v70
v_mfma_f32_16x16x32_bf16 a[56:59],a[240:243],v[112:115],a[56:59]
v_exp_f32 v71,v71
v_mfma_f32_16x16x32_bf16 a[56:59],a[244:247],v[120:123],a[56:59]
ds_read_b64_tr_b16 a[216:217],v3 offset:256
ds_read_b64_tr_b16 a[218:219],v3 offset:4352
v_mfma_f32_16x16x32_bf16 a[60:63],a[248:251],v[112:115],a[60:63]
global_load_lds_dwordx4 v4,s[10:11] offset:2048
v_cvt_pk_bf16_f32 v66,v68,v69
v_mfma_f32_16x16x32_bf16 a[60:63],a[252:255],v[120:123],a[60:63]
ds_read_b64_tr_b16 a[220:221],v3 offset:8448
v_cvt_pk_bf16_f32 v67,v70,v71
v_mfma_f32_16x16x32_bf16 v[60:63],v[28:31],v[112:115],v[60:63]
v_exp_f32 v72,v72
v_mfma_f32_16x16x32_bf16 v[60:63],v[28:31],v[120:123],v[60:63]
v_exp_f32 v73,v73
v_mfma_f32_16x16x32_bf16 v[96:99],a[128:131],v[192:195],v[40:43]
v_exp_f32 v74,v74
v_mfma_f32_16x16x32_bf16 v[96:99],a[132:135],v[200:203],v[96:99]
v_exp_f32 v75,v75
v_mfma_f32_16x16x32_bf16 v[96:99],a[136:139],v[208:211],v[96:99]
v_exp_f32 v76,v76
v_mfma_f32_16x16x32_bf16 v[96:99],a[140:143],v[216:219],v[96:99]
v_exp_f32 v77,v77
v_mfma_f32_16x16x32_bf16 v[100:103],a[156:159],v[216:219],v[40:43]
v_exp_f32 v78,v78
v_mfma_f32_16x16x32_bf16 v[100:103],a[152:155],v[208:211],v[100:103]
v_exp_f32 v79,v79
v_mfma_f32_16x16x32_bf16 v[100:103],a[148:151],v[200:203],v[100:103]
ds_read_b64_tr_b16 a[222:223],v3 offset:12544
v_cvt_pk_bf16_f32 v72,v72,v73
v_mfma_f32_16x16x32_bf16 v[100:103],a[144:147],v[192:195],v[100:103]
v_cvt_pk_bf16_f32 v73,v74,v75
v_cvt_pk_bf16_f32 v74,v76,v77
v_mfma_f32_16x16x32_bf16 v[104:107],a[160:163],v[192:195],v[40:43]
ds_read_b64_tr_b16 a[224:225],v2 offset:512
v_cvt_pk_bf16_f32 v75,v78,v79
v_mfma_f32_16x16x32_bf16 v[104:107],a[164:167],v[200:203],v[104:107]
ds_read_b64_tr_b16 a[226:227],v2 offset:4608
ds_read_b64_tr_b16 a[228:229],v2 offset:8704
v_mfma_f32_16x16x32_bf16 v[104:107],a[168:171],v[208:211],v[104:107]
ds_read_b64_tr_b16 a[230:231],v2 offset:12800
v_accvgpr_read_b32 v17,a73
v_mfma_f32_16x16x32_bf16 v[104:107],a[172:175],v[216:219],v[104:107]
ds_read_b64_tr_b16 a[232:233],v3 offset:512
v_accvgpr_read_b32 v18,a74
v_mfma_f32_16x16x32_bf16 v[108:111],a[188:191],v[216:219],v[40:43]
global_load_lds_dwordx4 v5,s[10:11] offset:3072
ds_read_b64_tr_b16 a[234:235],v3 offset:4608
v_mfma_f32_16x16x32_bf16 v[108:111],a[184:187],v[208:211],v[108:111]
ds_read_b64_tr_b16 a[236:237],v3 offset:8704
ds_read_b64_tr_b16 a[238:239],v3 offset:12800
v_mfma_f32_16x16x32_bf16 v[108:111],a[180:183],v[200:203],v[108:111]
ds_read_b64_tr_b16 a[240:241],v2 offset:768
v_accvgpr_read_b32 v19,a75
v_mfma_f32_16x16x32_bf16 v[108:111],a[176:179],v[192:195],v[108:111]
s_waitcnt lgkmcnt(9)
ds_read_b64_tr_b16 a[242:243],v2 offset:4864
v_mul_f32 v16,v16,v26
v_mfma_f32_16x16x32_bf16 a[0:3],a[192:195],v[64:67],a[0:3]
ds_read_b64_tr_b16 a[244:245],v2 offset:8960
v_mul_f32 v17,v17,v26
v_mfma_f32_16x16x32_bf16 a[0:3],a[196:199],v[72:75],a[0:3]
ds_read_b64_tr_b16 a[246:247],v2 offset:13056
ds_read_b64_tr_b16 a[248:249],v3 offset:768
v_mfma_f32_16x16x32_bf16 a[4:7],a[200:203],v[64:67],a[4:7]
ds_read_b64_tr_b16 a[250:251],v3 offset:4864
v_mul_f32 v18,v18,v26
v_mfma_f32_16x16x32_bf16 a[4:7],a[204:207],v[72:75],a[4:7]
ds_read_b64_tr_b16 a[252:253],v3 offset:8960
v_mul_f32 v19,v19,v26
v_mfma_f32_16x16x32_bf16 a[8:11],a[208:211],v[64:67],a[8:11]
ds_read_b64_tr_b16 a[254:255],v3 offset:13056
v_mul_f32 v20,v20,v26
v_mfma_f32_16x16x32_bf16 a[8:11],a[212:215],v[72:75],a[8:11]
v_exp_f32 v80,v80
v_mfma_f32_16x16x32_bf16 a[12:15],a[216:219],v[64:67],a[12:15]
v_exp_f32 v81,v81
v_mfma_f32_16x16x32_bf16 a[12:15],a[220:223],v[72:75],a[12:15]
s_waitcnt lgkmcnt(6)
v_exp_f32 v82,v82
v_mfma_f32_16x16x32_bf16 a[16:19],a[224:227],v[64:67],a[16:19]
v_exp_f32 v83,v83
v_mfma_f32_16x16x32_bf16 a[16:19],a[228:231],v[72:75],a[16:19]
v_exp_f32 v84,v84
v_mfma_f32_16x16x32_bf16 a[20:23],a[232:235],v[64:67],a[20:23]
s_getreg_b32 s92,hwreg(HW_REG_IB_STS,0,4)
s_min_u32 s4,s92,s6
s_mov_b32 s6,s92
v_mfma_f32_16x16x32_bf16 a[20:23],a[236:239],v[72:75],a[20:23]
s_cmp_ge_u32 s4,s9
s_cselect_b32 s4,15,s5
v_cvt_pk_bf16_f32 v80,v80,v81
v_mfma_f32_16x16x32_bf16 a[24:27],a[240:243],v[64:67],a[24:27]
s_waitcnt lgkmcnt(0)
v_exp_f32 v85,v85
v_mfma_f32_16x16x32_bf16 a[24:27],a[244:247],v[72:75],a[24:27]
v_exp_f32 v86,v86
v_mfma_f32_16x16x32_bf16 a[28:31],a[248:251],v[64:67],a[28:31]
v_exp_f32 v87,v87
v_mfma_f32_16x16x32_bf16 a[28:31],a[252:255],v[72:75],a[28:31]
s_getpc_b64 s[92:93]
s_cmp_lg_u32 s4,0
s_cbranch_scc1 L_pace_%=
v_mfma_f32_16x16x32_bf16 v[48:51],v[28:31],v[64:67],v[48:51]
v_cvt_pk_bf16_f32 v81,v82,v83
v_cvt_pk_bf16_f32 v82,v84,v85
v_mfma_f32_16x16x32_bf16 v[48:51],v[28:31],v[72:75],v[48:51]
s_add_u32 s10,s20,s74
s_addc_u32 s11,s21,0
v_cvt_pk_bf16_f32 v83,v86,v87
s_waitcnt vmcnt(16)
s_barrier
v_mfma_f32_16x16x32_bf16 v[112:115],a[128:131],v[224:227],v[44:47]
s_add_u32 m0,s76,0
v_exp_f32 v88,v88
v_mfma_f32_16x16x32_bf16 v[112:115],a[132:135],v[232:235],v[112:115]
v_exp_f32 v89,v89
v_mfma_f32_16x16x32_bf16 v[112:115],a[136:139],v[240:243],v[112:115]
global_load_lds_dwordx4 v4,s[10:11] nt
v_cvt_pk_bf16_f32 v88,v88,v89
v_mfma_f32_16x16x32_bf16 v[112:115],a[140:143],v[248:251],v[112:115]
v_exp_f32 v90,v90
v_mfma_f32_16x16x32_bf16 v[116:119],a[156:159],v[248:251],v[44:47]
v_exp_f32 v91,v91
v_mfma_f32_16x16x32_bf16 v[116:119],a[152:155],v[240:243],v[116:119]
v_exp_f32 v92,v92
v_mfma_f32_16x16x32_bf16 v[116:119],a[148:151],v[232:235],v[116:119]
v_exp_f32 v93,v93
v_mfma_f32_16x16x32_bf16 v[116:119],a[144:147],v[224:227],v[116:119]
v_exp_f32 v94,v94
v_mfma_f32_16x16x32_bf16 v[120:123],a[160:163],v[224:227],v[44:47]
v_exp_f32 v95,v95
v_mfma_f32_16x16x32_bf16 v[120:123],a[164:167],v[232:235],v[120:123]
v_cvt_pk_bf16_f32 v89,v90,v91
v_cvt_pk_bf16_f32 v90,v92,v93
v_mfma_f32_16x16x32_bf16 v[120:123],a[168:171],v[240:243],v[120:123]
v_cvt_pk_bf16_f32 v91,v94,v95
v_mul_f32 v21,v21,v26
v_mfma_f32_16x16x32_bf16 v[120:123],a[172:175],v[248:251],v[120:123]
v_mul_f32 v22,v22,v26
v_mul_f32 v23,v23,v26
v_mfma_f32_16x16x32_bf16 v[124:127],a[188:191],v[248:251],v[44:47]
v_cvt_pk_bf16_f32 v16,v16,v17
v_cvt_pk_bf16_f32 v17,v18,v19
v_mfma_f32_16x16x32_bf16 v[124:127],a[184:187],v[240:243],v[124:127]
v_cvt_pk_bf16_f32 v18,v20,v21
v_cvt_pk_bf16_f32 v19,v22,v23
v_mfma_f32_16x16x32_bf16 v[124:127],a[180:183],v[232:235],v[124:127]
v_accvgpr_write_b32 a72,v16
v_accvgpr_write_b32 a73,v17
v_mfma_f32_16x16x32_bf16 v[124:127],a[176:179],v[224:227],v[124:127]
v_accvgpr_write_b32 a74,v18
v_accvgpr_write_b32 a75,v19
v_mfma_f32_16x16x32_bf16 v[132:135],a[192:195],v[80:83],v[132:135]
ds_read_b128 a[76:79],v7 offset:7168
v_add_u32 v1,s78,v1
v_mfma_f32_16x16x32_bf16 v[132:135],a[196:199],v[88:91],v[132:135]
v_exp_f32 v96,v96
v_mfma_f32_16x16x32_bf16 v[140:143],a[200:203],v[80:83],v[140:143]
global_load_lds_dwordx4 v4,s[10:11] offset:1024 nt
ds_read_b128 a[128:131],v1
v_mfma_f32_16x16x32_bf16 v[140:143],a[204:207],v[88:91],v[140:143]
v_exp_f32 v97,v97
v_mfma_f32_16x16x32_bf16 v[148:151],a[208:211],v[80:83],v[148:151]
ds_read_b128 a[132:135],v1 offset:256
v_cvt_pk_bf16_f32 v96,v96,v97
v_mfma_f32_16x16x32_bf16 v[148:151],a[212:215],v[88:91],v[148:151]
v_exp_f32 v98,v98
v_mfma_f32_16x16x32_bf16 v[156:159],a[216:219],v[80:83],v[156:159]
)A"
R"A(ds_read_b128 a[136:139],v1 offset:512
v_accvgpr_read_b32 v12,a84
v_mfma_f32_16x16x32_bf16 v[156:159],a[220:223],v[88:91],v[156:159]
v_exp_f32 v99,v99
v_mfma_f32_16x16x32_bf16 v[164:167],a[224:227],v[80:83],v[164:167]
ds_read_b128 a[140:143],v1 offset:768
v_cvt_pk_bf16_f32 v97,v98,v99
v_mfma_f32_16x16x32_bf16 v[164:167],a[228:231],v[88:91],v[164:167]
v_exp_f32 v100,v100
v_mfma_f32_16x16x32_bf16 v[172:175],a[232:235],v[80:83],v[172:175]
ds_read_b128 a[156:159],v1 offset:4864
v_accvgpr_read_b32 v13,a85
v_mfma_f32_16x16x32_bf16 v[172:175],a[236:239],v[88:91],v[172:175]
v_exp_f32 v101,v101
v_mfma_f32_16x16x32_bf16 v[180:183],a[240:243],v[80:83],v[180:183]
ds_read_b128 a[152:155],v1 offset:4608
v_cvt_pk_bf16_f32 v98,v100,v101
v_mfma_f32_16x16x32_bf16 v[180:183],a[244:247],v[88:91],v[180:183]
v_exp_f32 v102,v102
v_mfma_f32_16x16x32_bf16 v[188:191],a[248:251],v[80:83],v[188:191]
ds_read_b128 a[148:151],v1 offset:4352
v_accvgpr_read_b32 v14,a86
v_mfma_f32_16x16x32_bf16 v[188:191],a[252:255],v[88:91],v[188:191]
v_exp_f32 v103,v103
v_mfma_f32_16x16x32_bf16 v[52:55],v[28:31],v[80:83],v[52:55]
ds_read_b128 a[144:147],v1 offset:4096
v_cvt_pk_bf16_f32 v99,v102,v103
v_mfma_f32_16x16x32_bf16 v[52:55],v[28:31],v[88:91],v[52:55]
s_waitcnt lgkmcnt(4)
v_exp_f32 v104,v104
v_mfma_f32_16x16x32_bf16 v[64:67],a[128:131],v[128:131],v[32:35]
global_load_lds_dwordx4 v5,s[10:11] offset:2048 nt
ds_read_b128 a[160:163],v1 offset:8192
v_mfma_f32_16x16x32_bf16 v[64:67],a[132:135],v[136:139],v[64:67]
v_exp_f32 v105,v105
v_mfma_f32_16x16x32_bf16 v[64:67],a[136:139],v[144:147],v[64:67]
ds_read_b128 a[164:167],v1 offset:8448
v_cvt_pk_bf16_f32 v104,v104,v105
v_mfma_f32_16x16x32_bf16 v[64:67],a[140:143],v[152:155],v[64:67]
s_waitcnt lgkmcnt(4)
v_exp_f32 v106,v106
v_mfma_f32_16x16x32_bf16 v[68:71],a[156:159],v[152:155],v[32:35]
ds_read_b128 a[168:171],v1 offset:8704
v_accvgpr_read_b32 v15,a87
v_mfma_f32_16x16x32_bf16 v[68:71],a[152:155],v[144:147],v[68:71]
s_waitcnt lgkmcnt(4)
v_exp_f32 v107,v107
v_mfma_f32_16x16x32_bf16 v[68:71],a[148:151],v[136:139],v[68:71]
s_waitcnt lgkmcnt(3)
ds_read_b128 a[172:175],v1 offset:8960
v_cvt_pk_bf16_f32 v105,v106,v107
v_mfma_f32_16x16x32_bf16 v[68:71],a[144:147],v[128:131],v[68:71]
s_waitcnt lgkmcnt(3)
v_exp_f32 v108,v108
v_mfma_f32_16x16x32_bf16 v[72:75],a[160:163],v[128:131],v[32:35]
s_waitcnt lgkmcnt(2)
ds_read_b128 a[188:191],v1 offset:13056
v_accvgpr_read_b32 v8,a80
v_mfma_f32_16x16x32_bf16 v[72:75],a[164:167],v[136:139],v[72:75]
s_waitcnt lgkmcnt(2)
ds_read_b128 a[184:187],v1 offset:12800
v_accvgpr_read_b32 v9,a81
v_mfma_f32_16x16x32_bf16 v[72:75],a[168:171],v[144:147],v[72:75]
s_waitcnt lgkmcnt(2)
ds_read_b128 a[180:183],v1 offset:12544
v_accvgpr_read_b32 v10,a82
v_mfma_f32_16x16x32_bf16 v[72:75],a[172:175],v[152:155],v[72:75]
s_waitcnt lgkmcnt(2)
ds_read_b128 a[176:179],v1 offset:12288
v_accvgpr_read_b32 v11,a83
v_mfma_f32_16x16x32_bf16 v[76:79],a[188:191],v[152:155],v[32:35]
s_waitcnt lgkmcnt(2)
v_exp_f32 v109,v109
v_mfma_f32_16x16x32_bf16 v[76:79],a[184:187],v[144:147],v[76:79]
s_waitcnt lgkmcnt(1)
v_exp_f32 v110,v110
v_exp_f32 v111,v111
v_mfma_f32_16x16x32_bf16 v[76:79],a[180:183],v[136:139],v[76:79]
s_waitcnt lgkmcnt(0)
v_cvt_pk_bf16_f32 v106,v108,v109
v_cvt_pk_bf16_f32 v107,v110,v111
v_mfma_f32_16x16x32_bf16 v[76:79],a[176:179],v[128:131],v[76:79]
v_mul_f32 v8,v8,v26
v_mul_f32 v9,v9,v26
v_mfma_f32_16x16x32_bf16 v[196:199],a[192:195],v[96:99],v[196:199]
global_load_lds_dwordx4 v5,s[10:11] offset:3072
v_mul_f32 v10,v10,v26
v_mfma_f32_16x16x32_bf16 v[196:199],a[196:199],v[104:107],v[196:199]
v_mul_f32 v11,v11,v26
v_mul_f32 v12,v12,v26
v_mfma_f32_16x16x32_bf16 v[204:207],a[200:203],v[96:99],v[204:207]
v_mul_f32 v13,v13,v26
v_mul_f32 v14,v14,v26
v_mfma_f32_16x16x32_bf16 v[204:207],a[204:207],v[104:107],v[204:207]
v_mul_f32 v15,v15,v26
v_cvt_pk_bf16_f32 v8,v8,v9
v_mfma_f32_16x16x32_bf16 v[212:215],a[208:211],v[96:99],v[212:215]
v_cvt_pk_bf16_f32 v9,v10,v11
v_cvt_pk_bf16_f32 v10,v12,v13
v_mfma_f32_16x16x32_bf16 v[212:215],a[212:215],v[104:107],v[212:215]
v_cvt_pk_bf16_f32 v11,v14,v15
v_accvgpr_write_b32 a80,v8
v_mfma_f32_16x16x32_bf16 v[220:223],a[216:219],v[96:99],v[220:223]
v_accvgpr_write_b32 a81,v9
v_accvgpr_write_b32 a82,v10
v_mfma_f32_16x16x32_bf16 v[220:223],a[220:223],v[104:107],v[220:223]
ds_read_b128 a[84:87],v7
v_accvgpr_write_b32 a83,v11
v_mfma_f32_16x16x32_bf16 v[228:231],a[224:227],v[96:99],v[228:231]
v_exp_f32 v112,v112
v_mfma_f32_16x16x32_bf16 v[228:231],a[228:231],v[104:107],v[228:231]
v_exp_f32 v113,v113
v_mfma_f32_16x16x32_bf16 v[236:239],a[232:235],v[96:99],v[236:239]
v_exp_f32 v114,v114
v_mfma_f32_16x16x32_bf16 v[236:239],a[236:239],v[104:107],v[236:239]
v_exp_f32 v115,v115
v_mfma_f32_16x16x32_bf16 v[244:247],a[240:243],v[96:99],v[244:247]
s_add_u32 s10,s24,s74
s_addc_u32 s11,s25,0
v_cvt_pk_bf16_f32 v112,v112,v113
v_mfma_f32_16x16x32_bf16 v[244:247],a[244:247],v[104:107],v[244:247]
s_add_u32 s74,s74,16384
v_exp_f32 v116,v116
v_mfma_f32_16x16x32_bf16 v[252:255],a[248:251],v[96:99],v[252:255]
s_add_u32 m0,s76,16384
v_exp_f32 v117,v117
v_mfma_f32_16x16x32_bf16 v[252:255],a[252:255],v[104:107],v[252:255]
s_and_b32 s74,s74,0x3ffff
v_exp_f32 v118,v118
v_mfma_f32_16x16x32_bf16 v[56:59],v[28:31],v[96:99],v[56:59]
global_load_lds_dwordx4 v4,s[10:11]
v_cvt_pk_bf16_f32 v113,v114,v115
v_mfma_f32_16x16x32_bf16 v[56:59],v[28:31],v[104:107],v[56:59]
s_or_b32 s74,s74,s69
v_exp_f32 v119,v119
v_mfma_f32_16x16x32_bf16 v[80:83],a[128:131],v[160:163],v[36:39]
s_sub_u32 s76,s76,32768
v_cvt_pk_bf16_f32 v114,v116,v117
v_cvt_pk_bf16_f32 v115,v118,v119
v_mfma_f32_16x16x32_bf16 v[80:83],a[132:135],v[168:171],v[80:83]
s_and_b32 s76,s76,0x1ffff
v_exp_f32 v120,v120
v_mfma_f32_16x16x32_bf16 v[80:83],a[136:139],v[176:179],v[80:83]
v_exp_f32 v121,v121
v_mfma_f32_16x16x32_bf16 v[80:83],a[140:143],v[184:187],v[80:83]
v_exp_f32 v122,v122
v_mfma_f32_16x16x32_bf16 v[84:87],a[156:159],v[184:187],v[36:39]
v_exp_f32 v123,v123
v_mfma_f32_16x16x32_bf16 v[84:87],a[152:155],v[176:179],v[84:87]
v_exp_f32 v124,v124
v_mfma_f32_16x16x32_bf16 v[84:87],a[148:151],v[168:171],v[84:87]
v_exp_f32 v125,v125
v_mfma_f32_16x16x32_bf16 v[84:87],a[144:147],v[160:163],v[84:87]
v_exp_f32 v126,v126
v_mfma_f32_16x16x32_bf16 v[88:91],a[160:163],v[160:163],v[36:39]
v_exp_f32 v127,v127
v_mfma_f32_16x16x32_bf16 v[88:91],a[164:167],v[168:171],v[88:91]
v_cvt_pk_bf16_f32 v120,v120,v121
v_cvt_pk_bf16_f32 v121,v122,v123
v_mfma_f32_16x16x32_bf16 v[88:91],a[168:171],v[176:179],v[88:91]
v_cvt_pk_bf16_f32 v122,v124,v125
v_cvt_pk_bf16_f32 v123,v126,v127
v_mfma_f32_16x16x32_bf16 v[88:91],a[172:175],v[184:187],v[88:91]
v_add_u32 v2,s78,v2
v_add_u32 v3,s78,v3
v_mfma_f32_16x16x32_bf16 v[92:95],a[188:191],v[184:187],v[36:39]
s_sub_u32 s78,s77,32768
v_exp_f32 v64,v64
v_mfma_f32_16x16x32_bf16 v[92:95],a[184:187],v[176:179],v[92:95]
s_and_b32 s78,s78,0x1ffff
v_exp_f32 v65,v65
v_mfma_f32_16x16x32_bf16 v[92:95],a[180:183],v[168:171],v[92:95]
global_load_lds_dwordx4 v5,s[10:11] offset:1024
v_cvt_pk_bf16_f32 v64,v64,v65
v_mfma_f32_16x16x32_bf16 v[92:95],a[176:179],v[160:163],v[92:95]
s_sub_u32 s78,s78,s77
v_exp_f32 v66,v66
v_mfma_f32_16x16x32_bf16 a[32:35],a[192:195],v[112:115],a[32:35]
s_add_u32 s77,s77,s78
ds_read_b64_tr_b16 a[192:193],v2
ds_read_b64_tr_b16 a[194:195],v2 offset:4096
v_mfma_f32_16x16x32_bf16 a[32:35],a[196:199],v[120:123],a[32:35]
ds_read_b64_tr_b16 a[196:197],v2 offset:8192
v_accvgpr_read_b32 v20,a92
v_mfma_f32_16x16x32_bf16 a[36:39],a[200:203],v[112:115],a[36:39]
ds_read_b64_tr_b16 a[198:199],v2 offset:12288
v_accvgpr_read_b32 v21,a93
v_mfma_f32_16x16x32_bf16 a[36:39],a[204:207],v[120:123],a[36:39]
ds_read_b64_tr_b16 a[200:201],v3
v_accvgpr_read_b32 v22,a94
v_mfma_f32_16x16x32_bf16 a[40:43],a[208:211],v[112:115],a[40:43]
ds_read_b64_tr_b16 a[202:203],v3 offset:4096
ds_read_b64_tr_b16 a[204:205],v3 offset:8192
v_mfma_f32_16x16x32_bf16 a[40:43],a[212:215],v[120:123],a[40:43]
ds_read_b64_tr_b16 a[206:207],v3 offset:12288
v_accvgpr_read_b32 v23,a95
v_mfma_f32_16x16x32_bf16 a[44:47],a[216:219],v[112:115],a[44:47]
ds_read_b64_tr_b16 a[208:209],v2 offset:256
v_accvgpr_read_b32 v16,a88
v_mfma_f32_16x16x32_bf16 a[44:47],a[220:223],v[120:123],a[44:47]
ds_read_b64_tr_b16 a[210:211],v2 offset:4352
v_accvgpr_read_b32 v17,a89
v_mfma_f32_16x16x32_bf16 a[48:51],a[224:227],v[112:115],a[48:51]
ds_read_b64_tr_b16 a[212:213],v2 offset:8448
ds_read_b64_tr_b16 a[214:215],v2 offset:12544
v_mfma_f32_16x16x32_bf16 a[48:51],a[228:231],v[120:123],a[48:51]
v_exp_f32 v67,v67
v_mfma_f32_16x16x32_bf16 a[52:55],a[232:235],v[112:115],a[52:55]
v_exp_f32 v68,v68
v_mfma_f32_16x16x32_bf16 a[52:55],a[236:239],v[120:123],a[52:55]
v_exp_f32 v69,v69
v_mfma_f32_16x16x32_bf16 a[56:59],a[240:243],v[112:115],a[56:59]
v_exp_f32 v70,v70
v_mfma_f32_16x16x32_bf16 a[56:59],a[244:247],v[120:123],a[56:59]
v_exp_f32 v71,v71
v_mfma_f32_16x16x32_bf16 a[60:63],a[248:251],v[112:115],a[60:63]
global_load_lds_dwordx4 v4,s[10:11] offset:2048
ds_read_b64_tr_b16 a[216:217],v3 offset:256
v_mfma_f32_16x16x32_bf16 a[60:63],a[252:255],v[120:123],a[60:63]
ds_read_b64_tr_b16 a[218:219],v3 offset:4352
v_cvt_pk_bf16_f32 v65,v66,v67
v_mfma_f32_16x16x32_bf16 v[60:63],v[28:31],v[112:115],v[60:63]
s_nop 0
L_pd10_%=:
s_cbranch_execz L_t1b_%=
v_cvt_pk_bf16_f32 v66,v68,v69
v_cvt_pk_bf16_f32 v67,v70,v71
v_mfma_f32_16x16x32_bf16 v[60:63],v[28:31],v[120:123],v[60:63]
v_exp_f32 v72,v72
v_mfma_f32_16x16x32_bf16 v[96:99],a[128:131],v[192:195],v[40:43]
v_exp_f32 v73,v73
v_mfma_f32_16x16x32_bf16 v[96:99],a[132:135],v[200:203],v[96:99]
v_exp_f32 v74,v74
v_mfma_f32_16x16x32_bf16 v[96:99],a[136:139],v[208:211],v[96:99]
v_exp_f32 v75,v75
v_mfma_f32_16x16x32_bf16 v[96:99],a[140:143],v[216:219],v[96:99]
v_exp_f32 v76,v76
v_mfma_f32_16x16x32_bf16 v[100:103],a[156:159],v[216:219],v[40:43]
v_exp_f32 v77,v77
v_mfma_f32_16x16x32_bf16 v[100:103],a[152:155],v[208:211],v[100:103]
v_exp_f32 v78,v78
v_mfma_f32_16x16x32_bf16 v[100:103],a[148:151],v[200:203],v[100:103]
v_exp_f32 v79,v79
v_mfma_f32_16x16x32_bf16 v[100:103],a[144:147],v[192:195],v[100:103]
ds_read_b64_tr_b16 a[220:221],v3 offset:8448
ds_read_b64_tr_b16 a[222:223],v3 offset:12544
v_mfma_f32_16x16x32_bf16 v[104:107],a[160:163],v[192:195],v[40:43]
v_cvt_pk_bf16_f32 v72,v72,v73
v_cvt_pk_bf16_f32 v73,v74,v75
v_mfma_f32_16x16x32_bf16 v[104:107],a[164:167],v[200:203],v[104:107]
v_cvt_pk_bf16_f32 v74,v76,v77
v_cvt_pk_bf16_f32 v75,v78,v79
v_mfma_f32_16x16x32_bf16 v[104:107],a[168:171],v[208:211],v[104:107]
ds_read_b64_tr_b16 a[224:225],v2 offset:512
ds_read_b64_tr_b16 a[226:227],v2 offset:4608
v_mfma_f32_16x16x32_bf16 v[104:107],a[172:175],v[216:219],v[104:107]
ds_read_b64_tr_b16 a[228:229],v2 offset:8704
v_accvgpr_read_b32 v18,a90
v_mfma_f32_16x16x32_bf16 v[108:111],a[188:191],v[216:219],v[40:43]
global_load_lds_dwordx4 v5,s[10:11] offset:3072
ds_read_b64_tr_b16 a[230:231],v2 offset:12800
v_mfma_f32_16x16x32_bf16 v[108:111],a[184:187],v[208:211],v[108:111]
ds_read_b64_tr_b16 a[232:233],v3 offset:512
v_accvgpr_read_b32 v19,a91
v_mfma_f32_16x16x32_bf16 v[108:111],a[180:183],v[200:203],v[108:111]
ds_read_b64_tr_b16 a[234:235],v3 offset:4608
ds_read_b64_tr_b16 a[236:237],v3 offset:8704
v_mfma_f32_16x16x32_bf16 v[108:111],a[176:179],v[192:195],v[108:111]
s_waitcnt lgkmcnt(7)
ds_read_b64_tr_b16 a[238:239],v3 offset:12800
v_mul_f32 v16,v16,v26
v_mfma_f32_16x16x32_bf16 a[0:3],a[192:195],v[64:67],a[0:3]
ds_read_b64_tr_b16 a[240:241],v2 offset:768
v_mul_f32 v17,v17,v26
v_mfma_f32_16x16x32_bf16 a[0:3],a[196:199],v[72:75],a[0:3]
ds_read_b64_tr_b16 a[242:243],v2 offset:4864
v_mul_f32 v18,v18,v26
v_mfma_f32_16x16x32_bf16 a[4:7],a[200:203],v[64:67],a[4:7]
ds_read_b64_tr_b16 a[244:245],v2 offset:8960
ds_read_b64_tr_b16 a[246:247],v2 offset:13056
v_mfma_f32_16x16x32_bf16 a[4:7],a[204:207],v[72:75],a[4:7]
ds_read_b64_tr_b16 a[248:249],v3 offset:768
v_mul_f32 v19,v19,v26
v_mfma_f32_16x16x32_bf16 a[8:11],a[208:211],v[64:67],a[8:11]
ds_read_b64_tr_b16 a[250:251],v3 offset:4864
v_mul_f32 v20,v20,v26
v_mfma_f32_16x16x32_bf16 a[8:11],a[212:215],v[72:75],a[8:11]
v_mul_f32 v21,v21,v26
v_mul_f32 v22,v22,v26
)A"
R"A(v_mul_f32 v23,v23,v26
v_cvt_pk_bf16_f32 v16,v16,v17
v_cvt_pk_bf16_f32 v17,v18,v19
v_cvt_pk_bf16_f32 v18,v20,v21
v_cvt_pk_bf16_f32 v19,v22,v23
v_accvgpr_write_b32 a88,v16
v_accvgpr_write_b32 a89,v17
v_accvgpr_write_b32 a90,v18
v_accvgpr_write_b32 a91,v19
v_mfma_f32_16x16x32_bf16 a[12:15],a[216:219],v[64:67],a[12:15]
ds_read_b128 a[92:95],v7 offset:1024
v_mov_b32 v8,v7
v_mfma_f32_16x16x32_bf16 a[12:15],a[220:223],v[72:75],a[12:15]
s_waitcnt lgkmcnt(7)
ds_read_b64_tr_b16 a[252:253],v3 offset:8960
v_add_u32 v1,s78,v1
v_mfma_f32_16x16x32_bf16 a[16:19],a[224:227],v[64:67],a[16:19]
ds_read_b64_tr_b16 a[254:255],v3 offset:13056
v_mfma_f32_16x16x32_bf16 a[16:19],a[228:231],v[72:75],a[16:19]
s_waitcnt lgkmcnt(4)
v_exp_f32 v80,v80
v_mfma_f32_16x16x32_bf16 a[20:23],a[232:235],v[64:67],a[20:23]
s_getreg_b32 s92,hwreg(HW_REG_IB_STS,0,4)
s_min_u32 s4,s92,s6
s_mov_b32 s6,s92
v_mfma_f32_16x16x32_bf16 a[20:23],a[236:239],v[72:75],a[20:23]
s_cmp_ge_u32 s4,s9
s_cselect_b32 s4,15,s5
ds_read_b128 v[24:27],v8 offset:4096
v_mfma_f32_16x16x32_bf16 a[24:27],a[240:243],v[64:67],a[24:27]
v_exp_f32 v81,v81
v_mfma_f32_16x16x32_bf16 a[24:27],a[244:247],v[72:75],a[24:27]
s_waitcnt lgkmcnt(4)
v_exp_f32 v82,v82
v_mfma_f32_16x16x32_bf16 a[28:31],a[248:251],v[64:67],a[28:31]
s_waitcnt lgkmcnt(1)
v_exp_f32 v83,v83
v_mfma_f32_16x16x32_bf16 a[28:31],a[252:255],v[72:75],a[28:31]
v_exp_f32 v84,v84
v_mfma_f32_16x16x32_bf16 v[48:51],v[28:31],v[64:67],v[48:51]
s_getpc_b64 s[92:93]
s_cmp_lg_u32 s4,0
s_cbranch_scc1 L_pace_%=
v_exp_f32 v85,v85
v_exp_f32 v86,v86
v_exp_f32 v87,v87
v_exp_f32 v88,v88
v_exp_f32 v89,v89
v_exp_f32 v90,v90
v_exp_f32 v91,v91
v_exp_f32 v92,v92
v_exp_f32 v93,v93
v_exp_f32 v94,v94
v_exp_f32 v95,v95
v_mfma_f32_16x16x32_bf16 v[48:51],v[28:31],v[72:75],v[48:51]
ds_read_b32 v7,v8 offset:5120
v_cvt_pk_bf16_f32 v80,v80,v81
v_cvt_pk_bf16_f32 v81,v82,v83
v_cvt_pk_bf16_f32 v82,v84,v85
v_cvt_pk_bf16_f32 v83,v86,v87
v_cvt_pk_bf16_f32 v88,v88,v89
v_cvt_pk_bf16_f32 v89,v90,v91
v_cvt_pk_bf16_f32 v90,v92,v93
v_cvt_pk_bf16_f32 v91,v94,v95
s_mov_b32 s70,7
L_a1_%=:
s_waitcnt vmcnt(16)
s_barrier
v_mfma_f32_16x16x32_bf16 v[112:115],a[128:131],v[224:227],v[44:47]
s_add_u32 s10,s20,s74
s_addc_u32 s11,s21,0
s_add_u32 m0,s76,0
v_mfma_f32_16x16x32_bf16 v[112:115],a[132:135],v[232:235],v[112:115]
s_add_u32 s75,s75,16384
s_and_b32 s69,s75,0xfffc0000
ds_read_b128 a[128:131],v1
v_mfma_f32_16x16x32_bf16 v[112:115],a[136:139],v[240:243],v[112:115]
global_load_lds_dwordx4 v4,s[10:11] nt
v_mfma_f32_16x16x32_bf16 v[112:115],a[140:143],v[248:251],v[112:115]
ds_read_b128 a[132:135],v1 offset:256
v_mfma_f32_16x16x32_bf16 v[116:119],a[156:159],v[248:251],v[44:47]
v_mfma_f32_16x16x32_bf16 v[116:119],a[152:155],v[240:243],v[116:119]
ds_read_b128 a[136:139],v1 offset:512
v_mfma_f32_16x16x32_bf16 v[116:119],a[148:151],v[232:235],v[116:119]
v_mfma_f32_16x16x32_bf16 v[116:119],a[144:147],v[224:227],v[116:119]
ds_read_b128 a[140:143],v1 offset:768
v_mfma_f32_16x16x32_bf16 v[120:123],a[160:163],v[224:227],v[44:47]
v_mfma_f32_16x16x32_bf16 v[120:123],a[164:167],v[232:235],v[120:123]
ds_read_b128 a[156:159],v1 offset:4864
v_mfma_f32_16x16x32_bf16 v[120:123],a[168:171],v[240:243],v[120:123]
v_mfma_f32_16x16x32_bf16 v[120:123],a[172:175],v[248:251],v[120:123]
ds_read_b128 a[152:155],v1 offset:4608
v_mfma_f32_16x16x32_bf16 v[124:127],a[188:191],v[248:251],v[44:47]
v_exp_f32 v96,v96
v_mfma_f32_16x16x32_bf16 v[124:127],a[184:187],v[240:243],v[124:127]
ds_read_b128 a[148:151],v1 offset:4352
v_add_u32 v2,s78,v2
v_mfma_f32_16x16x32_bf16 v[124:127],a[180:183],v[232:235],v[124:127]
v_exp_f32 v97,v97
v_mfma_f32_16x16x32_bf16 v[124:127],a[176:179],v[224:227],v[124:127]
ds_read_b128 a[144:147],v1 offset:4096
v_cvt_pk_bf16_f32 v96,v96,v97
v_mfma_f32_16x16x32_bf16 v[132:135],a[192:195],v[80:83],v[132:135]
v_exp_f32 v98,v98
v_mfma_f32_16x16x32_bf16 v[132:135],a[196:199],v[88:91],v[132:135]
ds_read_b128 a[160:163],v1 offset:8192
v_add_u32 v3,s78,v3
v_mfma_f32_16x16x32_bf16 v[140:143],a[200:203],v[80:83],v[140:143]
s_sub_u32 s78,s77,32768
global_load_lds_dwordx4 v4,s[10:11] offset:1024 nt
v_mfma_f32_16x16x32_bf16 v[140:143],a[204:207],v[88:91],v[140:143]
s_and_b32 s78,s78,0x1ffff
v_exp_f32 v99,v99
v_mfma_f32_16x16x32_bf16 v[148:151],a[208:211],v[80:83],v[148:151]
s_sub_u32 s78,s78,s77
ds_read_b128 a[164:167],v1 offset:8448
v_cvt_pk_bf16_f32 v97,v98,v99
v_mfma_f32_16x16x32_bf16 v[148:151],a[212:215],v[88:91],v[148:151]
s_add_u32 s77,s77,s78
v_exp_f32 v100,v100
v_mfma_f32_16x16x32_bf16 v[156:159],a[216:219],v[80:83],v[156:159]
v_exp_f32 v101,v101
v_mfma_f32_16x16x32_bf16 v[156:159],a[220:223],v[88:91],v[156:159]
ds_read_b128 a[168:171],v1 offset:8704
v_cvt_pk_bf16_f32 v98,v100,v101
v_mfma_f32_16x16x32_bf16 v[164:167],a[224:227],v[80:83],v[164:167]
v_exp_f32 v102,v102
v_mfma_f32_16x16x32_bf16 v[164:167],a[228:231],v[88:91],v[164:167]
v_exp_f32 v103,v103
v_mfma_f32_16x16x32_bf16 v[172:175],a[232:235],v[80:83],v[172:175]
ds_read_b128 a[172:175],v1 offset:8960
v_cvt_pk_bf16_f32 v99,v102,v103
v_mfma_f32_16x16x32_bf16 v[172:175],a[236:239],v[88:91],v[172:175]
v_exp_f32 v104,v104
v_mfma_f32_16x16x32_bf16 v[180:183],a[240:243],v[80:83],v[180:183]
v_exp_f32 v105,v105
v_mfma_f32_16x16x32_bf16 v[180:183],a[244:247],v[88:91],v[180:183]
ds_read_b128 a[188:191],v1 offset:13056
v_cvt_pk_bf16_f32 v104,v104,v105
v_mfma_f32_16x16x32_bf16 v[188:191],a[248:251],v[80:83],v[188:191]
v_exp_f32 v106,v106
v_mfma_f32_16x16x32_bf16 v[188:191],a[252:255],v[88:91],v[188:191]
v_exp_f32 v107,v107
v_mfma_f32_16x16x32_bf16 v[52:55],v[28:31],v[80:83],v[52:55]
ds_read_b128 a[184:187],v1 offset:12800
v_cvt_pk_bf16_f32 v105,v106,v107
v_mfma_f32_16x16x32_bf16 v[52:55],v[28:31],v[88:91],v[52:55]
s_waitcnt lgkmcnt(3)
v_exp_f32 v108,v108
v_mfma_f32_16x16x32_bf16 v[64:67],a[128:131],v[128:131],v[32:35]
global_load_lds_dwordx4 v5,s[10:11] offset:2048 nt
ds_read_b128 a[180:183],v1 offset:12544
v_mfma_f32_16x16x32_bf16 v[64:67],a[132:135],v[136:139],v[64:67]
v_exp_f32 v109,v109
v_mfma_f32_16x16x32_bf16 v[64:67],a[136:139],v[144:147],v[64:67]
ds_read_b128 a[176:179],v1 offset:12288
v_cvt_pk_bf16_f32 v106,v108,v109
v_mfma_f32_16x16x32_bf16 v[64:67],a[140:143],v[152:155],v[64:67]
v_exp_f32 v110,v110
v_mfma_f32_16x16x32_bf16 v[68:71],a[156:159],v[152:155],v[32:35]
v_exp_f32 v111,v111
v_mfma_f32_16x16x32_bf16 v[68:71],a[152:155],v[144:147],v[68:71]
v_cvt_pk_bf16_f32 v107,v110,v111
v_add_u32 v1,s78,v1
v_mfma_f32_16x16x32_bf16 v[68:71],a[148:151],v[136:139],v[68:71]
v_exp_f32 v112,v112
v_mfma_f32_16x16x32_bf16 v[68:71],a[144:147],v[128:131],v[68:71]
v_exp_f32 v113,v113
v_mfma_f32_16x16x32_bf16 v[72:75],a[160:163],v[128:131],v[32:35]
v_exp_f32 v114,v114
v_mfma_f32_16x16x32_bf16 v[72:75],a[164:167],v[136:139],v[72:75]
v_exp_f32 v115,v115
v_mfma_f32_16x16x32_bf16 v[72:75],a[168:171],v[144:147],v[72:75]
s_waitcnt lgkmcnt(0)
v_exp_f32 v116,v116
v_mfma_f32_16x16x32_bf16 v[72:75],a[172:175],v[152:155],v[72:75]
v_exp_f32 v117,v117
v_mfma_f32_16x16x32_bf16 v[76:79],a[188:191],v[152:155],v[32:35]
v_exp_f32 v118,v118
v_mfma_f32_16x16x32_bf16 v[76:79],a[184:187],v[144:147],v[76:79]
v_exp_f32 v119,v119
v_mfma_f32_16x16x32_bf16 v[76:79],a[180:183],v[136:139],v[76:79]
v_cvt_pk_bf16_f32 v112,v112,v113
v_cvt_pk_bf16_f32 v113,v114,v115
v_mfma_f32_16x16x32_bf16 v[76:79],a[176:179],v[128:131],v[76:79]
v_cvt_pk_bf16_f32 v114,v116,v117
v_cvt_pk_bf16_f32 v115,v118,v119
v_mfma_f32_16x16x32_bf16 v[196:199],a[192:195],v[96:99],v[196:199]
global_load_lds_dwordx4 v5,s[10:11] offset:3072
v_mfma_f32_16x16x32_bf16 v[196:199],a[196:199],v[104:107],v[196:199]
v_exp_f32 v120,v120
v_mfma_f32_16x16x32_bf16 v[204:207],a[200:203],v[96:99],v[204:207]
v_exp_f32 v121,v121
v_mfma_f32_16x16x32_bf16 v[204:207],a[204:207],v[104:107],v[204:207]
v_exp_f32 v122,v122
v_mfma_f32_16x16x32_bf16 v[212:215],a[208:211],v[96:99],v[212:215]
v_exp_f32 v123,v123
v_mfma_f32_16x16x32_bf16 v[212:215],a[212:215],v[104:107],v[212:215]
v_exp_f32 v124,v124
v_mfma_f32_16x16x32_bf16 v[220:223],a[216:219],v[96:99],v[220:223]
v_exp_f32 v125,v125
v_mfma_f32_16x16x32_bf16 v[220:223],a[220:223],v[104:107],v[220:223]
v_exp_f32 v126,v126
v_mfma_f32_16x16x32_bf16 v[228:231],a[224:227],v[96:99],v[228:231]
v_exp_f32 v127,v127
v_mfma_f32_16x16x32_bf16 v[228:231],a[228:231],v[104:107],v[228:231]
v_cvt_pk_bf16_f32 v120,v120,v121
v_cvt_pk_bf16_f32 v121,v122,v123
v_mfma_f32_16x16x32_bf16 v[236:239],a[232:235],v[96:99],v[236:239]
v_cvt_pk_bf16_f32 v122,v124,v125
v_cvt_pk_bf16_f32 v123,v126,v127
v_mfma_f32_16x16x32_bf16 v[236:239],a[236:239],v[104:107],v[236:239]
v_exp_f32 v64,v64
v_mfma_f32_16x16x32_bf16 v[244:247],a[240:243],v[96:99],v[244:247]
s_add_u32 s10,s24,s74
s_addc_u32 s11,s25,0
s_add_u32 s74,s74,16384
v_mfma_f32_16x16x32_bf16 v[244:247],a[244:247],v[104:107],v[244:247]
s_and_b32 s74,s74,0x3ffff
v_exp_f32 v65,v65
v_mfma_f32_16x16x32_bf16 v[252:255],a[248:251],v[96:99],v[252:255]
s_add_u32 m0,s76,16384
v_exp_f32 v66,v66
v_mfma_f32_16x16x32_bf16 v[252:255],a[252:255],v[104:107],v[252:255]
s_or_b32 s74,s74,s69
v_exp_f32 v67,v67
v_mfma_f32_16x16x32_bf16 v[56:59],v[28:31],v[96:99],v[56:59]
global_load_lds_dwordx4 v4,s[10:11]
v_cvt_pk_bf16_f32 v64,v64,v65
v_mfma_f32_16x16x32_bf16 v[56:59],v[28:31],v[104:107],v[56:59]
s_sub_u32 s76,s76,32768
v_exp_f32 v68,v68
v_mfma_f32_16x16x32_bf16 v[80:83],a[128:131],v[160:163],v[36:39]
s_and_b32 s76,s76,0x1ffff
v_exp_f32 v69,v69
v_mfma_f32_16x16x32_bf16 v[80:83],a[132:135],v[168:171],v[80:83]
v_exp_f32 v70,v70
v_mfma_f32_16x16x32_bf16 v[80:83],a[136:139],v[176:179],v[80:83]
v_exp_f32 v71,v71
v_mfma_f32_16x16x32_bf16 v[80:83],a[140:143],v[184:187],v[80:83]
v_cvt_pk_bf16_f32 v65,v66,v67
v_cvt_pk_bf16_f32 v66,v68,v69
v_mfma_f32_16x16x32_bf16 v[84:87],a[156:159],v[184:187],v[36:39]
v_exp_f32 v72,v72
v_mfma_f32_16x16x32_bf16 v[84:87],a[152:155],v[176:179],v[84:87]
v_exp_f32 v73,v73
v_mfma_f32_16x16x32_bf16 v[84:87],a[148:151],v[168:171],v[84:87]
v_cvt_pk_bf16_f32 v67,v70,v71
v_cvt_pk_bf16_f32 v72,v72,v73
v_mfma_f32_16x16x32_bf16 v[84:87],a[144:147],v[160:163],v[84:87]
v_exp_f32 v74,v74
v_mfma_f32_16x16x32_bf16 v[88:91],a[160:163],v[160:163],v[36:39]
v_exp_f32 v75,v75
v_mfma_f32_16x16x32_bf16 v[88:91],a[164:167],v[168:171],v[88:91]
v_exp_f32 v76,v76
v_mfma_f32_16x16x32_bf16 v[88:91],a[168:171],v[176:179],v[88:91]
v_exp_f32 v77,v77
v_mfma_f32_16x16x32_bf16 v[88:91],a[172:175],v[184:187],v[88:91]
v_exp_f32 v78,v78
v_mfma_f32_16x16x32_bf16 v[92:95],a[188:191],v[184:187],v[36:39]
v_exp_f32 v79,v79
v_mfma_f32_16x16x32_bf16 v[92:95],a[184:187],v[176:179],v[92:95]
v_cvt_pk_bf16_f32 v73,v74,v75
v_cvt_pk_bf16_f32 v74,v76,v77
v_mfma_f32_16x16x32_bf16 v[92:95],a[180:183],v[168:171],v[92:95]
global_load_lds_dwordx4 v5,s[10:11] offset:1024
v_cvt_pk_bf16_f32 v75,v78,v79
v_mfma_f32_16x16x32_bf16 v[92:95],a[176:179],v[160:163],v[92:95]
v_exp_f32 v80,v80
v_mfma_f32_16x16x32_bf16 a[32:35],a[192:195],v[112:115],a[32:35]
ds_read_b64_tr_b16 a[192:193],v2
ds_read_b64_tr_b16 a[194:195],v2 offset:4096
v_mfma_f32_16x16x32_bf16 a[32:35],a[196:199],v[120:123],a[32:35]
v_exp_f32 v81,v81
v_mfma_f32_16x16x32_bf16 a[36:39],a[200:203],v[112:115],a[36:39]
ds_read_b64_tr_b16 a[196:197],v2 offset:8192
ds_read_b64_tr_b16 a[198:199],v2 offset:12288
v_mfma_f32_16x16x32_bf16 a[36:39],a[204:207],v[120:123],a[36:39]
ds_read_b64_tr_b16 a[200:201],v3
v_cvt_pk_bf16_f32 v80,v80,v81
v_mfma_f32_16x16x32_bf16 a[40:43],a[208:211],v[112:115],a[40:43]
v_exp_f32 v82,v82
v_mfma_f32_16x16x32_bf16 a[40:43],a[212:215],v[120:123],a[40:43]
ds_read_b64_tr_b16 a[202:203],v3 offset:4096
ds_read_b64_tr_b16 a[204:205],v3 offset:8192
v_mfma_f32_16x16x32_bf16 a[44:47],a[216:219],v[112:115],a[44:47]
v_exp_f32 v83,v83
v_mfma_f32_16x16x32_bf16 a[44:47],a[220:223],v[120:123],a[44:47]
ds_read_b64_tr_b16 a[206:207],v3 offset:12288
ds_read_b64_tr_b16 a[208:209],v2 offset:256
v_mfma_f32_16x16x32_bf16 a[48:51],a[224:227],v[112:115],a[48:51]
ds_read_b64_tr_b16 a[210:211],v2 offset:4352
v_cvt_pk_bf16_f32 v81,v82,v83
v_mfma_f32_16x16x32_bf16 a[48:51],a[228:231],v[120:123],a[48:51]
v_exp_f32 v84,v84
v_mfma_f32_16x16x32_bf16 a[52:55],a[232:235],v[112:115],a[52:55]
)A"
R"A(ds_read_b64_tr_b16 a[212:213],v2 offset:8448
ds_read_b64_tr_b16 a[214:215],v2 offset:12544
v_mfma_f32_16x16x32_bf16 a[52:55],a[236:239],v[120:123],a[52:55]
v_exp_f32 v85,v85
v_mfma_f32_16x16x32_bf16 a[56:59],a[240:243],v[112:115],a[56:59]
ds_read_b64_tr_b16 a[216:217],v3 offset:256
ds_read_b64_tr_b16 a[218:219],v3 offset:4352
v_mfma_f32_16x16x32_bf16 a[56:59],a[244:247],v[120:123],a[56:59]
ds_read_b64_tr_b16 a[220:221],v3 offset:8448
v_cvt_pk_bf16_f32 v82,v84,v85
v_mfma_f32_16x16x32_bf16 a[60:63],a[248:251],v[112:115],a[60:63]
global_load_lds_dwordx4 v4,s[10:11] offset:2048
ds_read_b64_tr_b16 a[222:223],v3 offset:12544
v_mfma_f32_16x16x32_bf16 a[60:63],a[252:255],v[120:123],a[60:63]
v_exp_f32 v86,v86
v_mfma_f32_16x16x32_bf16 v[60:63],v[28:31],v[112:115],v[60:63]
ds_read_b64_tr_b16 a[224:225],v2 offset:512
ds_read_b64_tr_b16 a[226:227],v2 offset:4608
v_mfma_f32_16x16x32_bf16 v[60:63],v[28:31],v[120:123],v[60:63]
v_exp_f32 v87,v87
v_mfma_f32_16x16x32_bf16 v[96:99],a[128:131],v[192:195],v[40:43]
ds_read_b64_tr_b16 a[228:229],v2 offset:8704
ds_read_b64_tr_b16 a[230:231],v2 offset:12800
v_mfma_f32_16x16x32_bf16 v[96:99],a[132:135],v[200:203],v[96:99]
ds_read_b64_tr_b16 a[232:233],v3 offset:512
v_cvt_pk_bf16_f32 v83,v86,v87
v_mfma_f32_16x16x32_bf16 v[96:99],a[136:139],v[208:211],v[96:99]
v_exp_f32 v88,v88
v_mfma_f32_16x16x32_bf16 v[96:99],a[140:143],v[216:219],v[96:99]
ds_read_b64_tr_b16 a[234:235],v3 offset:4608
ds_read_b64_tr_b16 a[236:237],v3 offset:8704
v_mfma_f32_16x16x32_bf16 v[100:103],a[156:159],v[216:219],v[40:43]
v_exp_f32 v89,v89
v_mfma_f32_16x16x32_bf16 v[100:103],a[152:155],v[208:211],v[100:103]
ds_read_b64_tr_b16 a[238:239],v3 offset:12800
ds_read_b64_tr_b16 a[240:241],v2 offset:768
v_mfma_f32_16x16x32_bf16 v[100:103],a[148:151],v[200:203],v[100:103]
ds_read_b64_tr_b16 a[242:243],v2 offset:4864
v_cvt_pk_bf16_f32 v88,v88,v89
v_mfma_f32_16x16x32_bf16 v[100:103],a[144:147],v[192:195],v[100:103]
v_exp_f32 v90,v90
v_mfma_f32_16x16x32_bf16 v[104:107],a[160:163],v[192:195],v[40:43]
ds_read_b64_tr_b16 a[244:245],v2 offset:8960
ds_read_b64_tr_b16 a[246:247],v2 offset:13056
v_mfma_f32_16x16x32_bf16 v[104:107],a[164:167],v[200:203],v[104:107]
v_exp_f32 v91,v91
v_mfma_f32_16x16x32_bf16 v[104:107],a[168:171],v[208:211],v[104:107]
ds_read_b64_tr_b16 a[248:249],v3 offset:768
ds_read_b64_tr_b16 a[250:251],v3 offset:4864
v_mfma_f32_16x16x32_bf16 v[104:107],a[172:175],v[216:219],v[104:107]
ds_read_b64_tr_b16 a[252:253],v3 offset:8960
v_cvt_pk_bf16_f32 v89,v90,v91
v_mfma_f32_16x16x32_bf16 v[108:111],a[188:191],v[216:219],v[40:43]
global_load_lds_dwordx4 v5,s[10:11] offset:3072
ds_read_b64_tr_b16 a[254:255],v3 offset:13056
v_mfma_f32_16x16x32_bf16 v[108:111],a[184:187],v[208:211],v[108:111]
v_exp_f32 v92,v92
v_mfma_f32_16x16x32_bf16 v[108:111],a[180:183],v[200:203],v[108:111]
v_exp_f32 v93,v93
v_mfma_f32_16x16x32_bf16 v[108:111],a[176:179],v[192:195],v[108:111]
s_waitcnt lgkmcnt(6)
v_exp_f32 v94,v94
v_mfma_f32_16x16x32_bf16 a[0:3],a[192:195],v[64:67],a[0:3]
v_exp_f32 v95,v95
v_mfma_f32_16x16x32_bf16 a[0:3],a[196:199],v[72:75],a[0:3]
v_cvt_pk_bf16_f32 v90,v92,v93
v_cvt_pk_bf16_f32 v91,v94,v95
v_mfma_f32_16x16x32_bf16 a[4:7],a[200:203],v[64:67],a[4:7]
v_mfma_f32_16x16x32_bf16 a[4:7],a[204:207],v[72:75],a[4:7]
v_mfma_f32_16x16x32_bf16 a[8:11],a[208:211],v[64:67],a[8:11]
v_mfma_f32_16x16x32_bf16 a[8:11],a[212:215],v[72:75],a[8:11]
v_mfma_f32_16x16x32_bf16 a[12:15],a[216:219],v[64:67],a[12:15]
v_mfma_f32_16x16x32_bf16 a[12:15],a[220:223],v[72:75],a[12:15]
v_mfma_f32_16x16x32_bf16 a[16:19],a[224:227],v[64:67],a[16:19]
v_mfma_f32_16x16x32_bf16 a[16:19],a[228:231],v[72:75],a[16:19]
v_mfma_f32_16x16x32_bf16 a[20:23],a[232:235],v[64:67],a[20:23]
s_getreg_b32 s92,hwreg(HW_REG_IB_STS,0,4)
s_min_u32 s4,s92,s6
s_mov_b32 s6,s92
v_mfma_f32_16x16x32_bf16 a[20:23],a[236:239],v[72:75],a[20:23]
s_cmp_ge_u32 s4,s9
s_cselect_b32 s4,15,s5
v_mfma_f32_16x16x32_bf16 a[24:27],a[240:243],v[64:67],a[24:27]
s_waitcnt lgkmcnt(0)
v_mfma_f32_16x16x32_bf16 a[24:27],a[244:247],v[72:75],a[24:27]
v_mfma_f32_16x16x32_bf16 a[28:31],a[248:251],v[64:67],a[28:31]
v_mfma_f32_16x16x32_bf16 a[28:31],a[252:255],v[72:75],a[28:31]
s_getpc_b64 s[92:93]
s_cmp_lg_u32 s4,0
s_cbranch_scc1 L_pace_%=
v_mfma_f32_16x16x32_bf16 v[48:51],v[28:31],v[64:67],v[48:51]
v_mfma_f32_16x16x32_bf16 v[48:51],v[28:31],v[72:75],v[48:51]
s_sub_u32 s70,s70,1
s_cbranch_scc0 L_a1_%=
L_nx1_%=:
s_waitcnt vmcnt(16)
s_barrier
v_mfma_f32_16x16x32_bf16 v[112:115],a[128:131],v[224:227],v[44:47]
s_mul_i32 s86,s60,10240
s_mul_i32 s85,s63,1310720
s_mul_i32 s66,s64,320
v_mfma_f32_16x16x32_bf16 v[112:115],a[132:135],v[232:235],v[112:115]
s_add_u32 s85,s85,s66
s_add_u32 s83,s64,0
s_and_b32 s83,s83,3
v_mfma_f32_16x16x32_bf16 v[112:115],a[136:139],v[240:243],v[112:115]
s_lshl_b32 s83,s83,1
s_mul_i32 s83,s83,163840
s_add_u32 s83,s83,s85
v_mfma_f32_16x16x32_bf16 v[112:115],a[140:143],v[248:251],v[112:115]
s_cmp_eq_u32 s60,3
s_cselect_b32 s69,0x40000000,0
v_add_u32 v14,s69,v6
v_mfma_f32_16x16x32_bf16 v[116:119],a[156:159],v[248:251],v[44:47]
s_add_u32 s84,s83,20480
ds_read_b128 a[128:131],v1
v_add_u32 v2,s78,v2
v_mfma_f32_16x16x32_bf16 v[116:119],a[152:155],v[240:243],v[116:119]
s_add_u32 m0,s86,122880
v_exp_f32 v96,v96
v_mfma_f32_16x16x32_bf16 v[116:119],a[148:151],v[232:235],v[116:119]
s_getpc_b64 s[8:9]
L_pcg_%=:
ds_read_b128 a[132:135],v1 offset:256
v_add_u32 v3,s78,v3
v_mfma_f32_16x16x32_bf16 v[116:119],a[144:147],v[224:227],v[116:119]
buffer_load_dwordx4 v24,s[48:51],s83 offen lds
v_mfma_f32_16x16x32_bf16 v[120:123],a[160:163],v[224:227],v[44:47]
s_sub_u32 s78,s77,32768
s_and_b32 s78,s78,0x1ffff
ds_read_b128 a[136:139],v1 offset:512
v_mfma_f32_16x16x32_bf16 v[120:123],a[164:167],v[232:235],v[120:123]
buffer_store_dwordx4 a[112:115],v14,s[40:43],s81 offen offset:3072 sc1
v_mfma_f32_16x16x32_bf16 v[120:123],a[168:171],v[240:243],v[120:123]
s_sub_u32 s78,s78,s77
ds_read_b128 a[140:143],v1 offset:768
v_lshlrev_b32 v4,10,v6
v_mfma_f32_16x16x32_bf16 v[120:123],a[172:175],v[248:251],v[120:123]
s_add_u32 s77,s77,s78
v_exp_f32 v97,v97
v_mfma_f32_16x16x32_bf16 v[124:127],a[188:191],v[248:251],v[44:47]
s_mov_b32 s6,L_gbF_%=-L_pcg_%=
ds_read_b128 a[156:159],v1 offset:4864
v_cvt_pk_bf16_f32 v96,v96,v97
v_mfma_f32_16x16x32_bf16 v[124:127],a[184:187],v[240:243],v[124:127]
s_mov_b32 s10,L_geF_%=-L_gbF_%=
v_exp_f32 v98,v98
v_mfma_f32_16x16x32_bf16 v[124:127],a[180:183],v[232:235],v[124:127]
s_mov_b32 s11,0x20000
s_lshr_b32 s7,%1,3
ds_read_b128 a[152:155],v1 offset:4608
v_mfma_f32_16x16x32_bf16 v[124:127],a[176:179],v[224:227],v[124:127]
s_lshl_b32 s7,s7,2
v_exp_f32 v99,v99
v_mfma_f32_16x16x32_bf16 v[132:135],a[192:195],v[80:83],v[132:135]
s_add_u32 s7,s7,s60
ds_read_b128 a[148:151],v1 offset:4352
v_cvt_pk_bf16_f32 v97,v98,v99
v_mfma_f32_16x16x32_bf16 v[132:135],a[196:199],v[88:91],v[132:135]
buffer_load_dwordx4 v25,s[48:51],s83 offen offset:1024 lds
v_mfma_f32_16x16x32_bf16 v[140:143],a[200:203],v[80:83],v[140:143]
s_lshl_b32 s7,s7,7
ds_read_b128 a[144:147],v1 offset:4096
v_mov_b32 v8,s52
v_mfma_f32_16x16x32_bf16 v[140:143],a[204:207],v[88:91],v[140:143]
s_lshl_b32 s66,s61,5
v_exp_f32 v100,v100
v_mfma_f32_16x16x32_bf16 v[148:151],a[208:211],v[80:83],v[148:151]
s_lshl_b32 s67,s63,3
ds_read_b128 a[160:163],v1 offset:8192
v_mov_b32 v9,s53
v_mfma_f32_16x16x32_bf16 v[148:151],a[212:215],v[88:91],v[148:151]
s_add_u32 s66,s66,s67
v_exp_f32 v101,v101
v_mfma_f32_16x16x32_bf16 v[156:159],a[216:219],v[80:83],v[156:159]
s_lshl_b32 s66,s66,16
ds_read_b128 a[164:167],v1 offset:8448
v_cvt_pk_bf16_f32 v98,v100,v101
v_mfma_f32_16x16x32_bf16 v[156:159],a[220:223],v[88:91],v[156:159]
s_lshl_b32 s67,s60,14
v_exp_f32 v102,v102
v_mfma_f32_16x16x32_bf16 v[164:167],a[224:227],v[80:83],v[164:167]
s_add_u32 s66,s66,s67
ds_read_b128 a[168:171],v1 offset:8704
v_lshlrev_b32 v10,20,v6
v_mfma_f32_16x16x32_bf16 v[164:167],a[228:231],v[88:91],v[164:167]
s_mov_b32 s30,1523712
v_exp_f32 v103,v103
v_mfma_f32_16x16x32_bf16 v[172:175],a[232:235],v[80:83],v[172:175]
buffer_store_dwordx4 a[116:119],v6,s[40:43],s81 offen offset:1024 sc1
v_mfma_f32_16x16x32_bf16 v[172:175],a[236:239],v[88:91],v[172:175]
s_mov_b32 s31,0x20000
ds_read_b128 a[172:175],v1 offset:8960
v_cvt_pk_bf16_f32 v99,v102,v103
v_mfma_f32_16x16x32_bf16 v[180:183],a[240:243],v[80:83],v[180:183]
buffer_load_dwordx4 v26,s[48:51],s83 offen offset:2048 lds
v_mfma_f32_16x16x32_bf16 v[180:183],a[244:247],v[88:91],v[180:183]
v_exp_f32 v104,v104
v_mfma_f32_16x16x32_bf16 v[188:191],a[248:251],v[80:83],v[188:191]
v_exp_f32 v105,v105
v_mfma_f32_16x16x32_bf16 v[188:191],a[252:255],v[88:91],v[188:191]
ds_read_b128 a[188:191],v1 offset:13056
v_cvt_pk_bf16_f32 v104,v104,v105
v_mfma_f32_16x16x32_bf16 v[52:55],v[28:31],v[80:83],v[52:55]
v_exp_f32 v106,v106
v_mfma_f32_16x16x32_bf16 v[52:55],v[28:31],v[88:91],v[52:55]
s_waitcnt lgkmcnt(2)
v_exp_f32 v107,v107
v_mfma_f32_16x16x32_bf16 v[64:67],a[128:131],v[128:131],v[32:35]
ds_read_b128 a[184:187],v1 offset:12800
v_cvt_pk_bf16_f32 v105,v106,v107
v_mfma_f32_16x16x32_bf16 v[64:67],a[132:135],v[136:139],v[64:67]
v_exp_f32 v108,v108
v_mfma_f32_16x16x32_bf16 v[64:67],a[136:139],v[144:147],v[64:67]
s_add_u32 s28,s12,s66
s_addc_u32 s29,s13,0
ds_read_b128 a[180:183],v1 offset:12544
v_mfma_f32_16x16x32_bf16 v[64:67],a[140:143],v[152:155],v[64:67]
buffer_load_dwordx4 v27,s[48:51],s83 offen offset:3072 lds
v_mfma_f32_16x16x32_bf16 v[68:71],a[156:159],v[152:155],v[32:35]
s_and_b32 s29,s29,0xffff
ds_read_b128 a[176:179],v1 offset:12288
v_add_u32 v1,s78,v1
v_mfma_f32_16x16x32_bf16 v[68:71],a[152:155],v[144:147],v[68:71]
v_exp_f32 v109,v109
v_mfma_f32_16x16x32_bf16 v[68:71],a[148:151],v[136:139],v[68:71]
v_exp_f32 v110,v110
v_mfma_f32_16x16x32_bf16 v[68:71],a[144:147],v[128:131],v[68:71]
v_exp_f32 v111,v111
v_mfma_f32_16x16x32_bf16 v[72:75],a[160:163],v[128:131],v[32:35]
v_cvt_pk_bf16_f32 v106,v108,v109
v_cvt_pk_bf16_f32 v107,v110,v111
v_mfma_f32_16x16x32_bf16 v[72:75],a[164:167],v[136:139],v[72:75]
buffer_store_dwordx4 a[120:123],v14,s[40:43],s82 offen offset:3072 sc1
v_mfma_f32_16x16x32_bf16 v[72:75],a[168:171],v[144:147],v[72:75]
s_waitcnt lgkmcnt(1)
v_exp_f32 v112,v112
v_mfma_f32_16x16x32_bf16 v[72:75],a[172:175],v[152:155],v[72:75]
s_add_u32 m0,s86,126976
v_exp_f32 v113,v113
v_mfma_f32_16x16x32_bf16 v[76:79],a[188:191],v[152:155],v[32:35]
s_waitcnt lgkmcnt(0)
v_exp_f32 v114,v114
v_mfma_f32_16x16x32_bf16 v[76:79],a[184:187],v[144:147],v[76:79]
buffer_load_dwordx4 v7,s[48:51],s83 offen lds
v_mfma_f32_16x16x32_bf16 v[76:79],a[180:183],v[136:139],v[76:79]
s_add_u32 s83,s64,0
v_exp_f32 v115,v115
v_mfma_f32_16x16x32_bf16 v[76:79],a[176:179],v[128:131],v[76:79]
s_and_b32 s83,s83,3
v_exp_f32 v116,v116
v_mfma_f32_16x16x32_bf16 v[196:199],a[192:195],v[96:99],v[196:199]
s_lshl_b32 s83,s83,1
v_exp_f32 v117,v117
L_pd11_%=:
s_cbranch_execz L_t1c_%=
v_mfma_f32_16x16x32_bf16 v[196:199],a[196:199],v[104:107],v[196:199]
s_or_b32 s83,s83,1
v_exp_f32 v118,v118
v_mfma_f32_16x16x32_bf16 v[204:207],a[200:203],v[96:99],v[204:207]
s_mul_i32 s83,s83,163840
v_exp_f32 v119,v119
v_mfma_f32_16x16x32_bf16 v[204:207],a[204:207],v[104:107],v[204:207]
s_add_u32 s83,s83,s85
v_cvt_pk_bf16_f32 v112,v112,v113
v_cvt_pk_bf16_f32 v113,v114,v115
v_mfma_f32_16x16x32_bf16 v[212:215],a[208:211],v[96:99],v[212:215]
v_cvt_pk_bf16_f32 v114,v116,v117
v_cvt_pk_bf16_f32 v115,v118,v119
v_mfma_f32_16x16x32_bf16 v[212:215],a[212:215],v[104:107],v[212:215]
s_add_u32 m0,s86,128000
v_exp_f32 v120,v120
v_mfma_f32_16x16x32_bf16 v[220:223],a[216:219],v[96:99],v[220:223]
v_exp_f32 v121,v121
v_mfma_f32_16x16x32_bf16 v[220:223],a[220:223],v[104:107],v[220:223]
buffer_load_dwordx4 v24,s[48:51],s84 offen lds
v_mfma_f32_16x16x32_bf16 v[228:231],a[224:227],v[96:99],v[228:231]
v_exp_f32 v122,v122
v_mfma_f32_16x16x32_bf16 v[228:231],a[228:231],v[104:107],v[228:231]
v_exp_f32 v123,v123
v_mfma_f32_16x16x32_bf16 v[236:239],a[232:235],v[96:99],v[236:239]
buffer_store_dwordx4 a[124:127],v6,s[40:43],s82 offen offset:1024 sc1
v_mfma_f32_16x16x32_bf16 v[236:239],a[236:239],v[104:107],v[236:239]
v_exp_f32 v124,v124
v_mfma_f32_16x16x32_bf16 v[244:247],a[240:243],v[96:99],v[244:247]
)A"
R"A(v_exp_f32 v125,v125
v_mfma_f32_16x16x32_bf16 v[244:247],a[244:247],v[104:107],v[244:247]
v_exp_f32 v126,v126
v_mfma_f32_16x16x32_bf16 v[252:255],a[248:251],v[96:99],v[252:255]
v_exp_f32 v127,v127
v_mfma_f32_16x16x32_bf16 v[252:255],a[252:255],v[104:107],v[252:255]
v_cvt_pk_bf16_f32 v120,v120,v121
v_cvt_pk_bf16_f32 v121,v122,v123
v_mfma_f32_16x16x32_bf16 v[56:59],v[28:31],v[96:99],v[56:59]
v_cvt_pk_bf16_f32 v122,v124,v125
v_cvt_pk_bf16_f32 v123,v126,v127
v_mfma_f32_16x16x32_bf16 v[56:59],v[28:31],v[104:107],v[56:59]
buffer_load_dwordx4 v25,s[48:51],s84 offen offset:1024 lds
v_mfma_f32_16x16x32_bf16 v[80:83],a[128:131],v[160:163],v[36:39]
v_exp_f32 v64,v64
v_mfma_f32_16x16x32_bf16 v[80:83],a[132:135],v[168:171],v[80:83]
v_exp_f32 v65,v65
v_mfma_f32_16x16x32_bf16 v[80:83],a[136:139],v[176:179],v[80:83]
v_exp_f32 v66,v66
v_mfma_f32_16x16x32_bf16 v[80:83],a[140:143],v[184:187],v[80:83]
v_exp_f32 v67,v67
v_mfma_f32_16x16x32_bf16 v[84:87],a[156:159],v[184:187],v[36:39]
v_exp_f32 v68,v68
v_mfma_f32_16x16x32_bf16 v[84:87],a[152:155],v[176:179],v[84:87]
v_exp_f32 v69,v69
v_mfma_f32_16x16x32_bf16 v[84:87],a[148:151],v[168:171],v[84:87]
v_exp_f32 v70,v70
v_mfma_f32_16x16x32_bf16 v[84:87],a[144:147],v[160:163],v[84:87]
v_exp_f32 v71,v71
v_mfma_f32_16x16x32_bf16 v[88:91],a[160:163],v[160:163],v[36:39]
v_cvt_pk_bf16_f32 v64,v64,v65
v_cvt_pk_bf16_f32 v65,v66,v67
v_mfma_f32_16x16x32_bf16 v[88:91],a[164:167],v[168:171],v[88:91]
buffer_store_dwordx4 a[96:99],v14,s[40:43],s79 offen offset:3072 sc1
v_mfma_f32_16x16x32_bf16 v[88:91],a[168:171],v[176:179],v[88:91]
v_cvt_pk_bf16_f32 v66,v68,v69
v_cvt_pk_bf16_f32 v67,v70,v71
v_mfma_f32_16x16x32_bf16 v[88:91],a[172:175],v[184:187],v[88:91]
buffer_load_dwordx4 v26,s[48:51],s84 offen offset:2048 lds
v_mfma_f32_16x16x32_bf16 v[92:95],a[188:191],v[184:187],v[36:39]
v_exp_f32 v72,v72
v_mfma_f32_16x16x32_bf16 v[92:95],a[184:187],v[176:179],v[92:95]
v_exp_f32 v73,v73
v_mfma_f32_16x16x32_bf16 v[92:95],a[180:183],v[168:171],v[92:95]
v_exp_f32 v74,v74
v_mfma_f32_16x16x32_bf16 v[92:95],a[176:179],v[160:163],v[92:95]
v_exp_f32 v75,v75
v_mfma_f32_16x16x32_bf16 a[32:35],a[192:195],v[112:115],a[32:35]
ds_read_b64_tr_b16 a[192:193],v2
ds_read_b64_tr_b16 a[194:195],v2 offset:4096
v_mfma_f32_16x16x32_bf16 a[32:35],a[196:199],v[120:123],a[32:35]
ds_read_b64_tr_b16 a[196:197],v2 offset:8192
v_cvt_pk_bf16_f32 v72,v72,v73
v_mfma_f32_16x16x32_bf16 a[36:39],a[200:203],v[112:115],a[36:39]
ds_read_b64_tr_b16 a[198:199],v2 offset:12288
v_cvt_pk_bf16_f32 v73,v74,v75
v_mfma_f32_16x16x32_bf16 a[36:39],a[204:207],v[120:123],a[36:39]
buffer_load_dwordx4 v27,s[48:51],s84 offen offset:3072 lds
v_mfma_f32_16x16x32_bf16 a[40:43],a[208:211],v[112:115],a[40:43]
ds_read_b64_tr_b16 a[200:201],v3
ds_read_b64_tr_b16 a[202:203],v3 offset:4096
v_mfma_f32_16x16x32_bf16 a[40:43],a[212:215],v[120:123],a[40:43]
v_exp_f32 v76,v76
v_mfma_f32_16x16x32_bf16 a[44:47],a[216:219],v[112:115],a[44:47]
ds_read_b64_tr_b16 a[204:205],v3 offset:8192
ds_read_b64_tr_b16 a[206:207],v3 offset:12288
v_mfma_f32_16x16x32_bf16 a[44:47],a[220:223],v[120:123],a[44:47]
v_exp_f32 v77,v77
v_mfma_f32_16x16x32_bf16 a[48:51],a[224:227],v[112:115],a[48:51]
ds_read_b64_tr_b16 a[208:209],v2 offset:256
ds_read_b64_tr_b16 a[210:211],v2 offset:4352
v_mfma_f32_16x16x32_bf16 a[48:51],a[228:231],v[120:123],a[48:51]
ds_read_b64_tr_b16 a[212:213],v2 offset:8448
v_cvt_pk_bf16_f32 v74,v76,v77
v_mfma_f32_16x16x32_bf16 a[52:55],a[232:235],v[112:115],a[52:55]
buffer_store_dwordx4 a[100:103],v6,s[40:43],s79 offen offset:1024 sc1
v_mfma_f32_16x16x32_bf16 a[52:55],a[236:239],v[120:123],a[52:55]
s_add_u32 m0,s86,132096
ds_read_b64_tr_b16 a[214:215],v2 offset:12544
ds_read_b64_tr_b16 a[216:217],v3 offset:256
v_mfma_f32_16x16x32_bf16 a[56:59],a[240:243],v[112:115],a[56:59]
v_exp_f32 v78,v78
v_mfma_f32_16x16x32_bf16 a[56:59],a[244:247],v[120:123],a[56:59]
buffer_load_dwordx4 v7,s[48:51],s84 offen lds
v_mfma_f32_16x16x32_bf16 a[60:63],a[248:251],v[112:115],a[60:63]
s_add_u32 s84,s83,20480
ds_read_b64_tr_b16 a[218:219],v3 offset:4352
ds_read_b64_tr_b16 a[220:221],v3 offset:8448
v_mfma_f32_16x16x32_bf16 a[60:63],a[252:255],v[120:123],a[60:63]
v_exp_f32 v79,v79
v_mfma_f32_16x16x32_bf16 v[60:63],v[28:31],v[112:115],v[60:63]
ds_read_b64_tr_b16 a[222:223],v3 offset:12544
v_cvt_pk_bf16_f32 v75,v78,v79
v_mfma_f32_16x16x32_bf16 v[60:63],v[28:31],v[120:123],v[60:63]
ds_read_b64_tr_b16 a[224:225],v2 offset:512
ds_read_b64_tr_b16 a[226:227],v2 offset:4608
v_mfma_f32_16x16x32_bf16 v[96:99],a[128:131],v[192:195],v[40:43]
v_exp_f32 v80,v80
v_mfma_f32_16x16x32_bf16 v[96:99],a[132:135],v[200:203],v[96:99]
ds_read_b64_tr_b16 a[228:229],v2 offset:8704
ds_read_b64_tr_b16 a[230:231],v2 offset:12800
v_mfma_f32_16x16x32_bf16 v[96:99],a[136:139],v[208:211],v[96:99]
v_exp_f32 v81,v81
v_mfma_f32_16x16x32_bf16 v[96:99],a[140:143],v[216:219],v[96:99]
ds_read_b64_tr_b16 a[232:233],v3 offset:512
ds_read_b64_tr_b16 a[234:235],v3 offset:4608
v_mfma_f32_16x16x32_bf16 v[100:103],a[156:159],v[216:219],v[40:43]
ds_read_b64_tr_b16 a[236:237],v3 offset:8704
v_cvt_pk_bf16_f32 v80,v80,v81
v_mfma_f32_16x16x32_bf16 v[100:103],a[152:155],v[208:211],v[100:103]
v_exp_f32 v82,v82
v_mfma_f32_16x16x32_bf16 v[100:103],a[148:151],v[200:203],v[100:103]
ds_read_b64_tr_b16 a[238:239],v3 offset:12800
ds_read_b64_tr_b16 a[240:241],v2 offset:768
v_mfma_f32_16x16x32_bf16 v[100:103],a[144:147],v[192:195],v[100:103]
v_exp_f32 v83,v83
v_mfma_f32_16x16x32_bf16 v[104:107],a[160:163],v[192:195],v[40:43]
ds_read_b64_tr_b16 a[242:243],v2 offset:4864
ds_read_b64_tr_b16 a[244:245],v2 offset:8960
v_mfma_f32_16x16x32_bf16 v[104:107],a[164:167],v[200:203],v[104:107]
buffer_store_dwordx4 a[104:107],v14,s[40:43],s80 offen offset:3072 sc1
v_mfma_f32_16x16x32_bf16 v[104:107],a[168:171],v[208:211],v[104:107]
ds_read_b64_tr_b16 a[246:247],v2 offset:13056
ds_read_b64_tr_b16 a[248:249],v3 offset:768
v_mfma_f32_16x16x32_bf16 v[104:107],a[172:175],v[216:219],v[104:107]
ds_read_b64_tr_b16 a[250:251],v3 offset:4864
v_cvt_pk_bf16_f32 v81,v82,v83
v_mfma_f32_16x16x32_bf16 v[108:111],a[188:191],v[216:219],v[40:43]
ds_read_b64_tr_b16 a[252:253],v3 offset:8960
v_add_u32 v2,s78,v2
v_mfma_f32_16x16x32_bf16 v[108:111],a[184:187],v[208:211],v[108:111]
s_waitcnt lgkmcnt(6)
ds_read_b64_tr_b16 a[254:255],v3 offset:13056
v_add_u32 v3,s78,v3
v_mfma_f32_16x16x32_bf16 v[108:111],a[180:183],v[200:203],v[108:111]
s_sub_u32 s78,s77,32768
v_exp_f32 v84,v84
v_mfma_f32_16x16x32_bf16 v[108:111],a[176:179],v[192:195],v[108:111]
s_and_b32 s78,s78,0x1ffff
v_exp_f32 v85,v85
v_mfma_f32_16x16x32_bf16 a[0:3],a[192:195],v[64:67],a[0:3]
s_sub_u32 s78,s78,s77
v_exp_f32 v86,v86
v_mfma_f32_16x16x32_bf16 a[0:3],a[196:199],v[72:75],a[0:3]
s_add_u32 s77,s77,s78
v_exp_f32 v87,v87
v_mfma_f32_16x16x32_bf16 a[4:7],a[200:203],v[64:67],a[4:7]
v_cvt_pk_bf16_f32 v82,v84,v85
v_cvt_pk_bf16_f32 v83,v86,v87
v_mfma_f32_16x16x32_bf16 a[4:7],a[204:207],v[72:75],a[4:7]
v_exp_f32 v88,v88
v_mfma_f32_16x16x32_bf16 a[8:11],a[208:211],v[64:67],a[8:11]
v_exp_f32 v89,v89
v_mfma_f32_16x16x32_bf16 a[8:11],a[212:215],v[72:75],a[8:11]
v_exp_f32 v90,v90
v_mfma_f32_16x16x32_bf16 a[12:15],a[216:219],v[64:67],a[12:15]
v_exp_f32 v91,v91
v_mfma_f32_16x16x32_bf16 a[12:15],a[220:223],v[72:75],a[12:15]
v_exp_f32 v92,v92
v_mfma_f32_16x16x32_bf16 a[16:19],a[224:227],v[64:67],a[16:19]
v_exp_f32 v93,v93
v_mfma_f32_16x16x32_bf16 a[16:19],a[228:231],v[72:75],a[16:19]
v_exp_f32 v94,v94
v_mfma_f32_16x16x32_bf16 a[20:23],a[232:235],v[64:67],a[20:23]
buffer_store_dwordx4 a[108:111],v6,s[40:43],s80 offen offset:1024 sc1
v_mfma_f32_16x16x32_bf16 a[20:23],a[236:239],v[72:75],a[20:23]
s_waitcnt lgkmcnt(0)
v_exp_f32 v95,v95
v_mfma_f32_16x16x32_bf16 a[24:27],a[240:243],v[64:67],a[24:27]
v_cvt_pk_bf16_f32 v88,v88,v89
v_cvt_pk_bf16_f32 v89,v90,v91
v_mfma_f32_16x16x32_bf16 a[24:27],a[244:247],v[72:75],a[24:27]
v_cvt_pk_bf16_f32 v90,v92,v93
v_cvt_pk_bf16_f32 v91,v94,v95
v_mfma_f32_16x16x32_bf16 a[28:31],a[248:251],v[64:67],a[28:31]
v_exp_f32 v96,v96
v_mfma_f32_16x16x32_bf16 a[28:31],a[252:255],v[72:75],a[28:31]
v_exp_f32 v97,v97
v_mfma_f32_16x16x32_bf16 v[48:51],v[28:31],v[64:67],v[48:51]
v_exp_f32 v98,v98
v_mfma_f32_16x16x32_bf16 v[48:51],v[28:31],v[72:75],v[48:51]
v_exp_f32 v99,v99
s_waitcnt vmcnt(26)
s_barrier
v_mfma_f32_16x16x32_bf16 v[112:115],a[128:131],v[224:227],v[44:47]
ds_read_b128 a[128:131],v1
v_cvt_pk_bf16_f32 v96,v96,v97
v_mfma_f32_16x16x32_bf16 v[112:115],a[132:135],v[232:235],v[112:115]
v_exp_f32 v100,v100
v_mfma_f32_16x16x32_bf16 v[112:115],a[136:139],v[240:243],v[112:115]
ds_read_b128 a[132:135],v1 offset:256
v_cvt_pk_bf16_f32 v97,v98,v99
v_mfma_f32_16x16x32_bf16 v[112:115],a[140:143],v[248:251],v[112:115]
v_exp_f32 v101,v101
v_mfma_f32_16x16x32_bf16 v[116:119],a[156:159],v[248:251],v[44:47]
ds_read_b128 a[136:139],v1 offset:512
v_cvt_pk_bf16_f32 v98,v100,v101
v_mfma_f32_16x16x32_bf16 v[116:119],a[152:155],v[240:243],v[116:119]
s_add_u32 m0,s86,81920
v_exp_f32 v102,v102
v_mfma_f32_16x16x32_bf16 v[116:119],a[148:151],v[232:235],v[116:119]
v_exp_f32 v103,v103
v_mfma_f32_16x16x32_bf16 v[116:119],a[144:147],v[224:227],v[116:119]
buffer_load_dwordx4 v24,s[48:51],s83 offen lds
v_mfma_f32_16x16x32_bf16 v[120:123],a[160:163],v[224:227],v[44:47]
ds_read_b128 a[140:143],v1 offset:768
v_cvt_pk_bf16_f32 v99,v102,v103
v_mfma_f32_16x16x32_bf16 v[120:123],a[164:167],v[232:235],v[120:123]
buffer_store_dwordx4 a[64:67],v6,s[40:43],s79 offen offset:2048 sc1
v_mfma_f32_16x16x32_bf16 v[120:123],a[168:171],v[240:243],v[120:123]
v_exp_f32 v104,v104
v_mfma_f32_16x16x32_bf16 v[120:123],a[172:175],v[248:251],v[120:123]
v_exp_f32 v105,v105
v_mfma_f32_16x16x32_bf16 v[124:127],a[188:191],v[248:251],v[44:47]
ds_read_b128 a[156:159],v1 offset:4864
v_cvt_pk_bf16_f32 v104,v104,v105
v_mfma_f32_16x16x32_bf16 v[124:127],a[184:187],v[240:243],v[124:127]
v_exp_f32 v106,v106
v_mfma_f32_16x16x32_bf16 v[124:127],a[180:183],v[232:235],v[124:127]
v_exp_f32 v107,v107
v_mfma_f32_16x16x32_bf16 v[124:127],a[176:179],v[224:227],v[124:127]
ds_read_b128 a[152:155],v1 offset:4608
v_cvt_pk_bf16_f32 v105,v106,v107
v_mfma_f32_16x16x32_bf16 v[132:135],a[192:195],v[80:83],v[132:135]
v_exp_f32 v108,v108
v_mfma_f32_16x16x32_bf16 v[132:135],a[196:199],v[88:91],v[132:135]
buffer_load_dwordx4 v25,s[48:51],s83 offen offset:1024 lds
v_mfma_f32_16x16x32_bf16 v[140:143],a[200:203],v[80:83],v[140:143]
v_exp_f32 v109,v109
v_mfma_f32_16x16x32_bf16 v[140:143],a[204:207],v[88:91],v[140:143]
ds_read_b128 a[148:151],v1 offset:4352
v_cvt_pk_bf16_f32 v106,v108,v109
v_mfma_f32_16x16x32_bf16 v[148:151],a[208:211],v[80:83],v[148:151]
v_exp_f32 v110,v110
v_mfma_f32_16x16x32_bf16 v[148:151],a[212:215],v[88:91],v[148:151]
v_exp_f32 v111,v111
v_mfma_f32_16x16x32_bf16 v[156:159],a[216:219],v[80:83],v[156:159]
ds_read_b128 a[144:147],v1 offset:4096
v_cvt_pk_bf16_f32 v107,v110,v111
v_mfma_f32_16x16x32_bf16 v[156:159],a[220:223],v[88:91],v[156:159]
v_exp_f32 v112,v112
v_mfma_f32_16x16x32_bf16 v[164:167],a[224:227],v[80:83],v[164:167]
v_exp_f32 v113,v113
v_mfma_f32_16x16x32_bf16 v[164:167],a[228:231],v[88:91],v[164:167]
buffer_store_dwordx4 a[68:71],v6,s[40:43],s79 offen sc1
v_mfma_f32_16x16x32_bf16 v[172:175],a[232:235],v[80:83],v[172:175]
s_add_u32 s79,s79,65536
ds_read_b128 a[160:163],v1 offset:8192
v_cvt_pk_bf16_f32 v112,v112,v113
v_mfma_f32_16x16x32_bf16 v[172:175],a[236:239],v[88:91],v[172:175]
buffer_load_dwordx4 v26,s[48:51],s83 offen offset:2048 lds
v_mfma_f32_16x16x32_bf16 v[180:183],a[240:243],v[80:83],v[180:183]
v_exp_f32 v114,v114
v_mfma_f32_16x16x32_bf16 v[180:183],a[244:247],v[88:91],v[180:183]
v_exp_f32 v115,v115
v_mfma_f32_16x16x32_bf16 v[188:191],a[248:251],v[80:83],v[188:191]
ds_read_b128 a[164:167],v1 offset:8448
v_cvt_pk_bf16_f32 v113,v114,v115
v_mfma_f32_16x16x32_bf16 v[188:191],a[252:255],v[88:91],v[188:191]
v_exp_f32 v116,v116
v_mfma_f32_16x16x32_bf16 v[52:55],v[28:31],v[80:83],v[52:55]
ds_read_b128 a[168:171],v1 offset:8704
v_mfma_f32_16x16x32_bf16 v[52:55],v[28:31],v[88:91],v[52:55]
s_waitcnt lgkmcnt(3)
v_exp_f32 v117,v117
v_mfma_f32_16x16x32_bf16 v[64:67],a[128:131],v[128:131],v[32:35]
)A"
R"A(ds_read_b128 a[172:175],v1 offset:8960
v_cvt_pk_bf16_f32 v114,v116,v117
v_mfma_f32_16x16x32_bf16 v[64:67],a[132:135],v[136:139],v[64:67]
v_exp_f32 v118,v118
v_mfma_f32_16x16x32_bf16 v[64:67],a[136:139],v[144:147],v[64:67]
ds_read_b128 a[188:191],v1 offset:13056
v_mfma_f32_16x16x32_bf16 v[64:67],a[140:143],v[152:155],v[64:67]
buffer_load_dwordx4 v27,s[48:51],s83 offen offset:3072 lds
v_mfma_f32_16x16x32_bf16 v[68:71],a[156:159],v[152:155],v[32:35]
ds_read_b128 a[184:187],v1 offset:12800
v_mfma_f32_16x16x32_bf16 v[68:71],a[152:155],v[144:147],v[68:71]
v_exp_f32 v119,v119
v_mfma_f32_16x16x32_bf16 v[68:71],a[148:151],v[136:139],v[68:71]
ds_read_b128 a[180:183],v1 offset:12544
v_cvt_pk_bf16_f32 v115,v118,v119
v_mfma_f32_16x16x32_bf16 v[68:71],a[144:147],v[128:131],v[68:71]
s_waitcnt lgkmcnt(4)
v_exp_f32 v120,v120
v_mfma_f32_16x16x32_bf16 v[72:75],a[160:163],v[128:131],v[32:35]
buffer_store_dwordx4 a[72:75],v6,s[40:43],s80 offen offset:2048 sc1
v_mfma_f32_16x16x32_bf16 v[72:75],a[164:167],v[136:139],v[72:75]
ds_read_b128 a[176:179],v1 offset:12288
v_add_u32 v1,s78,v1
v_mfma_f32_16x16x32_bf16 v[72:75],a[168:171],v[144:147],v[72:75]
s_waitcnt lgkmcnt(3)
v_exp_f32 v121,v121
v_mfma_f32_16x16x32_bf16 v[72:75],a[172:175],v[152:155],v[72:75]
s_add_u32 m0,s86,86016
v_exp_f32 v122,v122
v_mfma_f32_16x16x32_bf16 v[76:79],a[188:191],v[152:155],v[32:35]
s_waitcnt lgkmcnt(0)
v_exp_f32 v123,v123
v_mfma_f32_16x16x32_bf16 v[76:79],a[184:187],v[144:147],v[76:79]
buffer_load_dwordx4 v7,s[48:51],s83 offen lds
v_mfma_f32_16x16x32_bf16 v[76:79],a[180:183],v[136:139],v[76:79]
s_add_u32 s83,s64,1
v_exp_f32 v124,v124
v_mfma_f32_16x16x32_bf16 v[76:79],a[176:179],v[128:131],v[76:79]
s_and_b32 s83,s83,3
v_exp_f32 v125,v125
v_mfma_f32_16x16x32_bf16 v[196:199],a[192:195],v[96:99],v[196:199]
s_lshl_b32 s83,s83,1
v_exp_f32 v126,v126
v_mfma_f32_16x16x32_bf16 v[196:199],a[196:199],v[104:107],v[196:199]
s_mul_i32 s83,s83,163840
v_exp_f32 v127,v127
v_mfma_f32_16x16x32_bf16 v[204:207],a[200:203],v[96:99],v[204:207]
s_add_u32 s83,s83,s85
v_cvt_pk_bf16_f32 v120,v120,v121
v_cvt_pk_bf16_f32 v121,v122,v123
v_mfma_f32_16x16x32_bf16 v[204:207],a[204:207],v[104:107],v[204:207]
v_cvt_pk_bf16_f32 v122,v124,v125
v_cvt_pk_bf16_f32 v123,v126,v127
v_mfma_f32_16x16x32_bf16 v[212:215],a[208:211],v[96:99],v[212:215]
v_exp_f32 v64,v64
v_mfma_f32_16x16x32_bf16 v[212:215],a[212:215],v[104:107],v[212:215]
s_add_u32 m0,s86,87040
v_exp_f32 v65,v65
v_mfma_f32_16x16x32_bf16 v[220:223],a[216:219],v[96:99],v[220:223]
v_exp_f32 v66,v66
v_mfma_f32_16x16x32_bf16 v[220:223],a[220:223],v[104:107],v[220:223]
buffer_load_dwordx4 v24,s[48:51],s84 offen lds
v_mfma_f32_16x16x32_bf16 v[228:231],a[224:227],v[96:99],v[228:231]
v_exp_f32 v67,v67
v_mfma_f32_16x16x32_bf16 v[228:231],a[228:231],v[104:107],v[228:231]
buffer_store_dwordx4 a[76:79],v6,s[40:43],s80 offen sc1
v_mfma_f32_16x16x32_bf16 v[236:239],a[232:235],v[96:99],v[236:239]
s_add_u32 s80,s80,65536
v_exp_f32 v68,v68
v_mfma_f32_16x16x32_bf16 v[236:239],a[236:239],v[104:107],v[236:239]
v_exp_f32 v69,v69
v_mfma_f32_16x16x32_bf16 v[244:247],a[240:243],v[96:99],v[244:247]
v_exp_f32 v70,v70
v_mfma_f32_16x16x32_bf16 v[244:247],a[244:247],v[104:107],v[244:247]
v_exp_f32 v71,v71
v_mfma_f32_16x16x32_bf16 v[252:255],a[248:251],v[96:99],v[252:255]
v_cvt_pk_bf16_f32 v64,v64,v65
v_cvt_pk_bf16_f32 v65,v66,v67
v_mfma_f32_16x16x32_bf16 v[252:255],a[252:255],v[104:107],v[252:255]
v_cvt_pk_bf16_f32 v66,v68,v69
v_cvt_pk_bf16_f32 v67,v70,v71
v_mfma_f32_16x16x32_bf16 v[56:59],v[28:31],v[96:99],v[56:59]
v_exp_f32 v72,v72
v_mfma_f32_16x16x32_bf16 v[56:59],v[28:31],v[104:107],v[56:59]
buffer_load_dwordx4 v25,s[48:51],s84 offen offset:1024 lds
v_mfma_f32_16x16x32_bf16 v[80:83],a[128:131],v[160:163],v[36:39]
v_exp_f32 v73,v73
v_mfma_f32_16x16x32_bf16 v[80:83],a[132:135],v[168:171],v[80:83]
v_exp_f32 v74,v74
v_mfma_f32_16x16x32_bf16 v[80:83],a[136:139],v[176:179],v[80:83]
v_exp_f32 v75,v75
v_mfma_f32_16x16x32_bf16 v[80:83],a[140:143],v[184:187],v[80:83]
v_exp_f32 v76,v76
v_mfma_f32_16x16x32_bf16 v[84:87],a[156:159],v[184:187],v[36:39]
v_exp_f32 v77,v77
v_mfma_f32_16x16x32_bf16 v[84:87],a[152:155],v[176:179],v[84:87]
v_exp_f32 v78,v78
v_mfma_f32_16x16x32_bf16 v[84:87],a[148:151],v[168:171],v[84:87]
v_exp_f32 v79,v79
v_mfma_f32_16x16x32_bf16 v[84:87],a[144:147],v[160:163],v[84:87]
v_cvt_pk_bf16_f32 v72,v72,v73
v_cvt_pk_bf16_f32 v73,v74,v75
v_mfma_f32_16x16x32_bf16 v[88:91],a[160:163],v[160:163],v[36:39]
buffer_store_dwordx4 a[80:83],v6,s[40:43],s81 offen offset:2048 sc1
v_mfma_f32_16x16x32_bf16 v[88:91],a[164:167],v[168:171],v[88:91]
v_cvt_pk_bf16_f32 v74,v76,v77
v_cvt_pk_bf16_f32 v75,v78,v79
v_mfma_f32_16x16x32_bf16 v[88:91],a[168:171],v[176:179],v[88:91]
buffer_load_dwordx4 v26,s[48:51],s84 offen offset:2048 lds
v_mfma_f32_16x16x32_bf16 v[88:91],a[172:175],v[184:187],v[88:91]
v_exp_f32 v80,v80
v_mfma_f32_16x16x32_bf16 v[92:95],a[188:191],v[184:187],v[36:39]
v_exp_f32 v81,v81
v_mfma_f32_16x16x32_bf16 v[92:95],a[184:187],v[176:179],v[92:95]
v_exp_f32 v82,v82
v_mfma_f32_16x16x32_bf16 v[92:95],a[180:183],v[168:171],v[92:95]
v_exp_f32 v83,v83
v_mfma_f32_16x16x32_bf16 v[92:95],a[176:179],v[160:163],v[92:95]
v_exp_f32 v84,v84
v_mfma_f32_16x16x32_bf16 a[32:35],a[192:195],v[112:115],a[32:35]
ds_read_b64_tr_b16 a[192:193],v2
ds_read_b64_tr_b16 a[194:195],v2 offset:4096
v_mfma_f32_16x16x32_bf16 a[32:35],a[196:199],v[120:123],a[32:35]
ds_read_b64_tr_b16 a[196:197],v2 offset:8192
v_cvt_pk_bf16_f32 v80,v80,v81
v_mfma_f32_16x16x32_bf16 a[36:39],a[200:203],v[112:115],a[36:39]
ds_read_b64_tr_b16 a[198:199],v2 offset:12288
v_cvt_pk_bf16_f32 v81,v82,v83
v_mfma_f32_16x16x32_bf16 a[36:39],a[204:207],v[120:123],a[36:39]
buffer_load_dwordx4 v27,s[48:51],s84 offen offset:3072 lds
v_mfma_f32_16x16x32_bf16 a[40:43],a[208:211],v[112:115],a[40:43]
ds_read_b64_tr_b16 a[200:201],v3
ds_read_b64_tr_b16 a[202:203],v3 offset:4096
v_mfma_f32_16x16x32_bf16 a[40:43],a[212:215],v[120:123],a[40:43]
v_exp_f32 v85,v85
v_mfma_f32_16x16x32_bf16 a[44:47],a[216:219],v[112:115],a[44:47]
ds_read_b64_tr_b16 a[204:205],v3 offset:8192
ds_read_b64_tr_b16 a[206:207],v3 offset:12288
v_mfma_f32_16x16x32_bf16 a[44:47],a[220:223],v[120:123],a[44:47]
ds_read_b64_tr_b16 a[208:209],v2 offset:256
v_cvt_pk_bf16_f32 v82,v84,v85
v_mfma_f32_16x16x32_bf16 a[48:51],a[224:227],v[112:115],a[48:51]
v_exp_f32 v86,v86
v_mfma_f32_16x16x32_bf16 a[48:51],a[228:231],v[120:123],a[48:51]
buffer_store_dwordx4 a[84:87],v6,s[40:43],s81 offen sc1
v_mfma_f32_16x16x32_bf16 a[52:55],a[232:235],v[112:115],a[52:55]
s_add_u32 s81,s81,65536
ds_read_b64_tr_b16 a[210:211],v2 offset:4352
ds_read_b64_tr_b16 a[212:213],v2 offset:8448
v_mfma_f32_16x16x32_bf16 a[52:55],a[236:239],v[120:123],a[52:55]
s_add_u32 m0,s86,91136
v_exp_f32 v87,v87
v_mfma_f32_16x16x32_bf16 a[56:59],a[240:243],v[112:115],a[56:59]
ds_read_b64_tr_b16 a[214:215],v2 offset:12544
ds_read_b64_tr_b16 a[216:217],v3 offset:256
v_mfma_f32_16x16x32_bf16 a[56:59],a[244:247],v[120:123],a[56:59]
buffer_load_dwordx4 v7,s[48:51],s84 offen lds
v_mfma_f32_16x16x32_bf16 a[60:63],a[248:251],v[112:115],a[60:63]
s_add_u32 s84,s83,20480
ds_read_b64_tr_b16 a[218:219],v3 offset:4352
ds_read_b64_tr_b16 a[220:221],v3 offset:8448
v_mfma_f32_16x16x32_bf16 a[60:63],a[252:255],v[120:123],a[60:63]
ds_read_b64_tr_b16 a[222:223],v3 offset:12544
v_cvt_pk_bf16_f32 v83,v86,v87
v_mfma_f32_16x16x32_bf16 v[60:63],v[28:31],v[112:115],v[60:63]
v_exp_f32 v88,v88
v_mfma_f32_16x16x32_bf16 v[60:63],v[28:31],v[120:123],v[60:63]
ds_read_b64_tr_b16 a[224:225],v2 offset:512
ds_read_b64_tr_b16 a[226:227],v2 offset:4608
v_mfma_f32_16x16x32_bf16 v[96:99],a[128:131],v[192:195],v[40:43]
v_exp_f32 v89,v89
v_mfma_f32_16x16x32_bf16 v[96:99],a[132:135],v[200:203],v[96:99]
ds_read_b64_tr_b16 a[228:229],v2 offset:8704
ds_read_b64_tr_b16 a[230:231],v2 offset:12800
v_mfma_f32_16x16x32_bf16 v[96:99],a[136:139],v[208:211],v[96:99]
ds_read_b64_tr_b16 a[232:233],v3 offset:512
v_cvt_pk_bf16_f32 v88,v88,v89
v_mfma_f32_16x16x32_bf16 v[96:99],a[140:143],v[216:219],v[96:99]
v_exp_f32 v90,v90
v_mfma_f32_16x16x32_bf16 v[100:103],a[156:159],v[216:219],v[40:43]
ds_read_b64_tr_b16 a[234:235],v3 offset:4608
ds_read_b64_tr_b16 a[236:237],v3 offset:8704
v_mfma_f32_16x16x32_bf16 v[100:103],a[152:155],v[208:211],v[100:103]
v_exp_f32 v91,v91
v_mfma_f32_16x16x32_bf16 v[100:103],a[148:151],v[200:203],v[100:103]
ds_read_b64_tr_b16 a[238:239],v3 offset:12800
ds_read_b64_tr_b16 a[240:241],v2 offset:768
v_mfma_f32_16x16x32_bf16 v[100:103],a[144:147],v[192:195],v[100:103]
ds_read_b64_tr_b16 a[242:243],v2 offset:4864
v_cvt_pk_bf16_f32 v89,v90,v91
v_mfma_f32_16x16x32_bf16 v[104:107],a[160:163],v[192:195],v[40:43]
buffer_store_dwordx4 a[88:91],v6,s[40:43],s82 offen offset:2048 sc1
v_mfma_f32_16x16x32_bf16 v[104:107],a[164:167],v[200:203],v[104:107]
ds_read_b64_tr_b16 a[244:245],v2 offset:8960
ds_read_b64_tr_b16 a[246:247],v2 offset:13056
v_mfma_f32_16x16x32_bf16 v[104:107],a[168:171],v[208:211],v[104:107]
ds_read_b64_tr_b16 a[248:249],v3 offset:768
v_add_u32 v2,s78,v2
v_mfma_f32_16x16x32_bf16 v[104:107],a[172:175],v[216:219],v[104:107]
v_exp_f32 v92,v92
v_mfma_f32_16x16x32_bf16 v[108:111],a[188:191],v[216:219],v[40:43]
ds_read_b64_tr_b16 a[250:251],v3 offset:4864
ds_read_b64_tr_b16 a[252:253],v3 offset:8960
v_mfma_f32_16x16x32_bf16 v[108:111],a[184:187],v[208:211],v[108:111]
s_waitcnt lgkmcnt(5)
ds_read_b64_tr_b16 a[254:255],v3 offset:13056
v_add_u32 v3,s78,v3
v_mfma_f32_16x16x32_bf16 v[108:111],a[180:183],v[200:203],v[108:111]
s_sub_u32 s78,s77,32768
v_exp_f32 v93,v93
v_mfma_f32_16x16x32_bf16 v[108:111],a[176:179],v[192:195],v[108:111]
s_and_b32 s78,s78,0x1ffff
v_exp_f32 v94,v94
v_mfma_f32_16x16x32_bf16 a[0:3],a[192:195],v[64:67],a[0:3]
s_sub_u32 s78,s78,s77
v_exp_f32 v95,v95
v_mfma_f32_16x16x32_bf16 a[0:3],a[196:199],v[72:75],a[0:3]
s_add_u32 s77,s77,s78
v_cvt_pk_bf16_f32 v90,v92,v93
v_cvt_pk_bf16_f32 v91,v94,v95
v_mfma_f32_16x16x32_bf16 a[4:7],a[200:203],v[64:67],a[4:7]
s_cmp_ge_u32 s63,2
s_cselect_b32 s6,L_gbS_%=-L_pcg_%=,s6
s_cselect_b32 s10,L_geS_%=-L_gbS_%=,s10
v_mfma_f32_16x16x32_bf16 a[4:7],a[204:207],v[72:75],a[4:7]
s_add_u32 s8,s8,s6
s_addc_u32 s9,s9,0
s_and_b32 s8,s8,0xffffff80
v_mfma_f32_16x16x32_bf16 a[8:11],a[208:211],v[64:67],a[8:11]
s_and_b32 s9,s9,0xffff
v_exp_f32 v96,v96
v_mfma_f32_16x16x32_bf16 a[8:11],a[212:215],v[72:75],a[8:11]
v_exp_f32 v97,v97
v_mfma_f32_16x16x32_bf16 a[12:15],a[216:219],v[64:67],a[12:15]
v_exp_f32 v98,v98
v_mfma_f32_16x16x32_bf16 a[12:15],a[220:223],v[72:75],a[12:15]
v_exp_f32 v99,v99
v_mfma_f32_16x16x32_bf16 a[16:19],a[224:227],v[64:67],a[16:19]
v_exp_f32 v100,v100
v_mfma_f32_16x16x32_bf16 a[16:19],a[228:231],v[72:75],a[16:19]
buffer_store_dwordx4 a[92:95],v6,s[40:43],s82 offen sc1
v_mfma_f32_16x16x32_bf16 a[20:23],a[232:235],v[64:67],a[20:23]
s_add_u32 s82,s82,65536
v_exp_f32 v101,v101
v_mfma_f32_16x16x32_bf16 a[20:23],a[236:239],v[72:75],a[20:23]
v_exp_f32 v102,v102
v_mfma_f32_16x16x32_bf16 a[24:27],a[240:243],v[64:67],a[24:27]
s_waitcnt lgkmcnt(0)
v_exp_f32 v103,v103
v_mfma_f32_16x16x32_bf16 a[24:27],a[244:247],v[72:75],a[24:27]
v_cvt_pk_bf16_f32 v96,v96,v97
v_cvt_pk_bf16_f32 v97,v98,v99
v_mfma_f32_16x16x32_bf16 a[28:31],a[248:251],v[64:67],a[28:31]
v_cvt_pk_bf16_f32 v98,v100,v101
v_cvt_pk_bf16_f32 v99,v102,v103
v_mfma_f32_16x16x32_bf16 a[28:31],a[252:255],v[72:75],a[28:31]
v_exp_f32 v104,v104
v_mfma_f32_16x16x32_bf16 v[48:51],v[28:31],v[64:67],v[48:51]
v_exp_f32 v105,v105
v_mfma_f32_16x16x32_bf16 v[48:51],v[28:31],v[72:75],v[48:51]
v_exp_f32 v106,v106
s_waitcnt vmcnt(36)
s_barrier
v_mfma_f32_16x16x32_bf16 v[112:115],a[128:131],v[224:227],v[44:47]
ds_read_b128 a[128:131],v1
v_cvt_pk_bf16_f32 v104,v104,v105
v_mfma_f32_16x16x32_bf16 v[112:115],a[132:135],v[232:235],v[112:115]
v_exp_f32 v107,v107
v_mfma_f32_16x16x32_bf16 v[112:115],a[136:139],v[240:243],v[112:115]
ds_read_b128 a[132:135],v1 offset:256
v_cvt_pk_bf16_f32 v105,v106,v107
v_mfma_f32_16x16x32_bf16 v[112:115],a[140:143],v[248:251],v[112:115]
v_exp_f32 v108,v108
v_mfma_f32_16x16x32_bf16 v[116:119],a[156:159],v[248:251],v[44:47]
v_exp_f32 v109,v109
)A"
R"A(v_mfma_f32_16x16x32_bf16 v[116:119],a[152:155],v[240:243],v[116:119]
s_add_u32 m0,s86,40960
ds_read_b128 a[136:139],v1 offset:512
v_cvt_pk_bf16_f32 v106,v108,v109
v_mfma_f32_16x16x32_bf16 v[116:119],a[148:151],v[232:235],v[116:119]
v_exp_f32 v110,v110
v_mfma_f32_16x16x32_bf16 v[116:119],a[144:147],v[224:227],v[116:119]
buffer_load_dwordx4 v24,s[48:51],s83 offen lds
v_mfma_f32_16x16x32_bf16 v[120:123],a[160:163],v[224:227],v[44:47]
v_exp_f32 v111,v111
v_mfma_f32_16x16x32_bf16 v[120:123],a[164:167],v[232:235],v[120:123]
ds_read_b128 a[140:143],v1 offset:768
v_cvt_pk_bf16_f32 v107,v110,v111
v_mfma_f32_16x16x32_bf16 v[120:123],a[168:171],v[240:243],v[120:123]
v_exp_f32 v112,v112
v_mfma_f32_16x16x32_bf16 v[120:123],a[172:175],v[248:251],v[120:123]
v_exp_f32 v113,v113
v_mfma_f32_16x16x32_bf16 v[124:127],a[188:191],v[248:251],v[44:47]
s_nop 0
L_pd12_%=:
s_cbranch_execz L_trt1_%=
ds_read_b128 a[156:159],v1 offset:4864
v_cvt_pk_bf16_f32 v112,v112,v113
v_mfma_f32_16x16x32_bf16 v[124:127],a[184:187],v[240:243],v[124:127]
v_exp_f32 v114,v114
v_mfma_f32_16x16x32_bf16 v[124:127],a[180:183],v[232:235],v[124:127]
v_exp_f32 v115,v115
v_mfma_f32_16x16x32_bf16 v[124:127],a[176:179],v[224:227],v[124:127]
ds_read_b128 a[152:155],v1 offset:4608
v_cvt_pk_bf16_f32 v113,v114,v115
v_mfma_f32_16x16x32_bf16 v[132:135],a[192:195],v[80:83],v[132:135]
v_exp_f32 v116,v116
v_mfma_f32_16x16x32_bf16 v[132:135],a[196:199],v[88:91],v[132:135]
buffer_load_dwordx4 v25,s[48:51],s83 offen offset:1024 lds
v_mfma_f32_16x16x32_bf16 v[140:143],a[200:203],v[80:83],v[140:143]
v_exp_f32 v117,v117
v_mfma_f32_16x16x32_bf16 v[140:143],a[204:207],v[88:91],v[140:143]
ds_read_b128 a[148:151],v1 offset:4352
v_cvt_pk_bf16_f32 v114,v116,v117
v_mfma_f32_16x16x32_bf16 v[148:151],a[208:211],v[80:83],v[148:151]
v_exp_f32 v118,v118
v_mfma_f32_16x16x32_bf16 v[148:151],a[212:215],v[88:91],v[148:151]
v_exp_f32 v119,v119
v_mfma_f32_16x16x32_bf16 v[156:159],a[216:219],v[80:83],v[156:159]
ds_read_b128 a[144:147],v1 offset:4096
v_cvt_pk_bf16_f32 v115,v118,v119
v_mfma_f32_16x16x32_bf16 v[156:159],a[220:223],v[88:91],v[156:159]
v_exp_f32 v120,v120
v_mfma_f32_16x16x32_bf16 v[164:167],a[224:227],v[80:83],v[164:167]
buffer_load_dword v5,v4,s[8:11],s7 offen
v_mfma_f32_16x16x32_bf16 v[164:167],a[228:231],v[88:91],v[164:167]
v_exp_f32 v121,v121
v_mfma_f32_16x16x32_bf16 v[172:175],a[232:235],v[80:83],v[172:175]
ds_read_b128 a[160:163],v1 offset:8192
v_cvt_pk_bf16_f32 v120,v120,v121
v_mfma_f32_16x16x32_bf16 v[172:175],a[236:239],v[88:91],v[172:175]
v_exp_f32 v122,v122
v_mfma_f32_16x16x32_bf16 v[180:183],a[240:243],v[80:83],v[180:183]
v_exp_f32 v123,v123
v_mfma_f32_16x16x32_bf16 v[180:183],a[244:247],v[88:91],v[180:183]
ds_read_b128 a[164:167],v1 offset:8448
v_cvt_pk_bf16_f32 v121,v122,v123
v_mfma_f32_16x16x32_bf16 v[188:191],a[248:251],v[80:83],v[188:191]
buffer_load_dwordx4 v26,s[48:51],s83 offen offset:2048 lds
v_mfma_f32_16x16x32_bf16 v[188:191],a[252:255],v[88:91],v[188:191]
v_exp_f32 v124,v124
v_mfma_f32_16x16x32_bf16 v[52:55],v[28:31],v[80:83],v[52:55]
ds_read_b128 a[168:171],v1 offset:8704
v_mfma_f32_16x16x32_bf16 v[52:55],v[28:31],v[88:91],v[52:55]
s_waitcnt lgkmcnt(3)
v_exp_f32 v125,v125
v_mfma_f32_16x16x32_bf16 v[64:67],a[128:131],v[128:131],v[32:35]
ds_read_b128 a[172:175],v1 offset:8960
v_cvt_pk_bf16_f32 v122,v124,v125
v_mfma_f32_16x16x32_bf16 v[64:67],a[132:135],v[136:139],v[64:67]
v_exp_f32 v126,v126
v_mfma_f32_16x16x32_bf16 v[64:67],a[136:139],v[144:147],v[64:67]
ds_read_b128 a[188:191],v1 offset:13056
v_mfma_f32_16x16x32_bf16 v[64:67],a[140:143],v[152:155],v[64:67]
buffer_load_dwordx4 v27,s[48:51],s83 offen offset:3072 lds
v_mfma_f32_16x16x32_bf16 v[68:71],a[156:159],v[152:155],v[32:35]
ds_read_b128 a[184:187],v1 offset:12800
v_mfma_f32_16x16x32_bf16 v[68:71],a[152:155],v[144:147],v[68:71]
v_exp_f32 v127,v127
v_mfma_f32_16x16x32_bf16 v[68:71],a[148:151],v[136:139],v[68:71]
ds_read_b128 a[180:183],v1 offset:12544
v_cvt_pk_bf16_f32 v123,v126,v127
v_mfma_f32_16x16x32_bf16 v[68:71],a[144:147],v[128:131],v[68:71]
s_waitcnt lgkmcnt(4)
v_exp_f32 v64,v64
v_mfma_f32_16x16x32_bf16 v[72:75],a[160:163],v[128:131],v[32:35]
ds_read_b128 a[176:179],v1 offset:12288
v_add_u32 v1,s78,v1
v_mfma_f32_16x16x32_bf16 v[72:75],a[164:167],v[136:139],v[72:75]
v_exp_f32 v65,v65
v_mfma_f32_16x16x32_bf16 v[72:75],a[168:171],v[144:147],v[72:75]
s_waitcnt lgkmcnt(3)
v_exp_f32 v66,v66
v_mfma_f32_16x16x32_bf16 v[72:75],a[172:175],v[152:155],v[72:75]
s_add_u32 m0,s86,45056
v_exp_f32 v67,v67
v_mfma_f32_16x16x32_bf16 v[76:79],a[188:191],v[152:155],v[32:35]
s_waitcnt lgkmcnt(0)
v_exp_f32 v68,v68
v_mfma_f32_16x16x32_bf16 v[76:79],a[184:187],v[144:147],v[76:79]
buffer_load_dwordx4 v7,s[48:51],s83 offen lds
v_mfma_f32_16x16x32_bf16 v[76:79],a[180:183],v[136:139],v[76:79]
s_add_u32 s83,s64,2
v_exp_f32 v69,v69
v_mfma_f32_16x16x32_bf16 v[76:79],a[176:179],v[128:131],v[76:79]
s_and_b32 s83,s83,3
v_exp_f32 v70,v70
v_mfma_f32_16x16x32_bf16 v[196:199],a[192:195],v[96:99],v[196:199]
s_lshl_b32 s83,s83,1
v_exp_f32 v71,v71
v_mfma_f32_16x16x32_bf16 v[196:199],a[196:199],v[104:107],v[196:199]
s_mul_i32 s83,s83,163840
v_cvt_pk_bf16_f32 v64,v64,v65
v_cvt_pk_bf16_f32 v65,v66,v67
v_mfma_f32_16x16x32_bf16 v[204:207],a[200:203],v[96:99],v[204:207]
s_add_u32 s83,s83,s85
v_cvt_pk_bf16_f32 v66,v68,v69
v_cvt_pk_bf16_f32 v67,v70,v71
v_mfma_f32_16x16x32_bf16 v[204:207],a[204:207],v[104:107],v[204:207]
v_exp_f32 v72,v72
v_mfma_f32_16x16x32_bf16 v[212:215],a[208:211],v[96:99],v[212:215]
v_exp_f32 v73,v73
v_mfma_f32_16x16x32_bf16 v[212:215],a[212:215],v[104:107],v[212:215]
s_add_u32 m0,s86,46080
v_exp_f32 v74,v74
v_mfma_f32_16x16x32_bf16 v[220:223],a[216:219],v[96:99],v[220:223]
v_exp_f32 v75,v75
v_mfma_f32_16x16x32_bf16 v[220:223],a[220:223],v[104:107],v[220:223]
buffer_load_dwordx4 v24,s[48:51],s84 offen lds
v_mfma_f32_16x16x32_bf16 v[228:231],a[224:227],v[96:99],v[228:231]
v_exp_f32 v76,v76
v_mfma_f32_16x16x32_bf16 v[228:231],a[228:231],v[104:107],v[228:231]
v_exp_f32 v77,v77
v_mfma_f32_16x16x32_bf16 v[236:239],a[232:235],v[96:99],v[236:239]
v_exp_f32 v78,v78
v_mfma_f32_16x16x32_bf16 v[236:239],a[236:239],v[104:107],v[236:239]
v_exp_f32 v79,v79
v_mfma_f32_16x16x32_bf16 v[244:247],a[240:243],v[96:99],v[244:247]
v_cvt_pk_bf16_f32 v72,v72,v73
v_cvt_pk_bf16_f32 v73,v74,v75
v_mfma_f32_16x16x32_bf16 v[244:247],a[244:247],v[104:107],v[244:247]
v_cvt_pk_bf16_f32 v74,v76,v77
v_cvt_pk_bf16_f32 v75,v78,v79
v_mfma_f32_16x16x32_bf16 v[252:255],a[248:251],v[96:99],v[252:255]
v_lshrrev_b32 v1,6,v6
v_mul_u32_u24 v1,320,v1
v_mfma_f32_16x16x32_bf16 v[252:255],a[252:255],v[104:107],v[252:255]
v_mfma_f32_16x16x32_bf16 v[56:59],v[28:31],v[96:99],v[56:59]
v_mfma_f32_16x16x32_bf16 v[56:59],v[28:31],v[104:107],v[56:59]
buffer_load_dwordx4 v25,s[48:51],s84 offen offset:1024 lds
v_mfma_f32_16x16x32_bf16 v[80:83],a[128:131],v[160:163],v[36:39]
v_mfma_f32_16x16x32_bf16 v[80:83],a[132:135],v[168:171],v[80:83]
v_mfma_f32_16x16x32_bf16 v[80:83],a[136:139],v[176:179],v[80:83]
v_mfma_f32_16x16x32_bf16 v[80:83],a[140:143],v[184:187],v[80:83]
v_mfma_f32_16x16x32_bf16 v[84:87],a[156:159],v[184:187],v[36:39]
v_mfma_f32_16x16x32_bf16 v[84:87],a[152:155],v[176:179],v[84:87]
v_mfma_f32_16x16x32_bf16 v[84:87],a[148:151],v[168:171],v[84:87]
v_mfma_f32_16x16x32_bf16 v[84:87],a[144:147],v[160:163],v[84:87]
v_mfma_f32_16x16x32_bf16 v[88:91],a[160:163],v[160:163],v[36:39]
v_mfma_f32_16x16x32_bf16 v[88:91],a[164:167],v[168:171],v[88:91]
buffer_load_dwordx4 v26,s[48:51],s84 offen offset:2048 lds
v_mfma_f32_16x16x32_bf16 v[88:91],a[168:171],v[176:179],v[88:91]
v_exp_f32 v80,v80
v_mfma_f32_16x16x32_bf16 v[88:91],a[172:175],v[184:187],v[88:91]
v_exp_f32 v81,v81
v_mfma_f32_16x16x32_bf16 v[92:95],a[188:191],v[184:187],v[36:39]
v_exp_f32 v82,v82
v_mfma_f32_16x16x32_bf16 v[92:95],a[184:187],v[176:179],v[92:95]
v_exp_f32 v83,v83
v_mfma_f32_16x16x32_bf16 v[92:95],a[180:183],v[168:171],v[92:95]
v_exp_f32 v84,v84
v_mfma_f32_16x16x32_bf16 v[92:95],a[176:179],v[160:163],v[92:95]
v_exp_f32 v85,v85
v_mfma_f32_16x16x32_bf16 a[32:35],a[192:195],v[112:115],a[32:35]
ds_read_b64_tr_b16 a[192:193],v2
ds_read_b64_tr_b16 a[194:195],v2 offset:4096
v_mfma_f32_16x16x32_bf16 a[32:35],a[196:199],v[120:123],a[32:35]
ds_read_b64_tr_b16 a[196:197],v2 offset:8192
v_cvt_pk_bf16_f32 v80,v80,v81
v_mfma_f32_16x16x32_bf16 a[36:39],a[200:203],v[112:115],a[36:39]
ds_read_b64_tr_b16 a[198:199],v2 offset:12288
v_cvt_pk_bf16_f32 v81,v82,v83
v_mfma_f32_16x16x32_bf16 a[36:39],a[204:207],v[120:123],a[36:39]
buffer_load_dwordx4 v27,s[48:51],s84 offen offset:3072 lds
v_mfma_f32_16x16x32_bf16 a[40:43],a[208:211],v[112:115],a[40:43]
ds_read_b64_tr_b16 a[200:201],v3
ds_read_b64_tr_b16 a[202:203],v3 offset:4096
v_mfma_f32_16x16x32_bf16 a[40:43],a[212:215],v[120:123],a[40:43]
ds_read_b64_tr_b16 a[204:205],v3 offset:8192
v_cvt_pk_bf16_f32 v82,v84,v85
v_mfma_f32_16x16x32_bf16 a[44:47],a[216:219],v[112:115],a[44:47]
v_exp_f32 v86,v86
v_mfma_f32_16x16x32_bf16 a[44:47],a[220:223],v[120:123],a[44:47]
ds_read_b64_tr_b16 a[206:207],v3 offset:12288
ds_read_b64_tr_b16 a[208:209],v2 offset:256
v_mfma_f32_16x16x32_bf16 a[48:51],a[224:227],v[112:115],a[48:51]
v_exp_f32 v87,v87
v_mfma_f32_16x16x32_bf16 a[48:51],a[228:231],v[120:123],a[48:51]
ds_read_b64_tr_b16 a[210:211],v2 offset:4352
ds_read_b64_tr_b16 a[212:213],v2 offset:8448
v_mfma_f32_16x16x32_bf16 a[52:55],a[232:235],v[112:115],a[52:55]
ds_read_b64_tr_b16 a[214:215],v2 offset:12544
v_cvt_pk_bf16_f32 v83,v86,v87
v_mfma_f32_16x16x32_bf16 a[52:55],a[236:239],v[120:123],a[52:55]
s_add_u32 m0,s86,50176
v_exp_f32 v88,v88
v_mfma_f32_16x16x32_bf16 a[56:59],a[240:243],v[112:115],a[56:59]
ds_read_b64_tr_b16 a[216:217],v3 offset:256
ds_read_b64_tr_b16 a[218:219],v3 offset:4352
v_mfma_f32_16x16x32_bf16 a[56:59],a[244:247],v[120:123],a[56:59]
buffer_load_dwordx4 v7,s[48:51],s84 offen lds
v_mfma_f32_16x16x32_bf16 a[60:63],a[248:251],v[112:115],a[60:63]
s_add_u32 s84,s83,20480
ds_read_b64_tr_b16 a[220:221],v3 offset:8448
ds_read_b64_tr_b16 a[222:223],v3 offset:12544
v_mfma_f32_16x16x32_bf16 a[60:63],a[252:255],v[120:123],a[60:63]
v_exp_f32 v89,v89
v_mfma_f32_16x16x32_bf16 v[60:63],v[28:31],v[112:115],v[60:63]
ds_read_b64_tr_b16 a[224:225],v2 offset:512
ds_read_b64_tr_b16 a[226:227],v2 offset:4608
v_mfma_f32_16x16x32_bf16 v[60:63],v[28:31],v[120:123],v[60:63]
ds_read_b64_tr_b16 a[228:229],v2 offset:8704
v_cvt_pk_bf16_f32 v88,v88,v89
v_mfma_f32_16x16x32_bf16 v[96:99],a[128:131],v[192:195],v[40:43]
v_exp_f32 v90,v90
v_mfma_f32_16x16x32_bf16 v[96:99],a[132:135],v[200:203],v[96:99]
ds_read_b64_tr_b16 a[230:231],v2 offset:12800
ds_read_b64_tr_b16 a[232:233],v3 offset:512
v_mfma_f32_16x16x32_bf16 v[96:99],a[136:139],v[208:211],v[96:99]
v_exp_f32 v91,v91
v_mfma_f32_16x16x32_bf16 v[96:99],a[140:143],v[216:219],v[96:99]
ds_read_b64_tr_b16 a[234:235],v3 offset:4608
ds_read_b64_tr_b16 a[236:237],v3 offset:8704
v_mfma_f32_16x16x32_bf16 v[100:103],a[156:159],v[216:219],v[40:43]
ds_read_b64_tr_b16 a[238:239],v3 offset:12800
v_cvt_pk_bf16_f32 v89,v90,v91
v_mfma_f32_16x16x32_bf16 v[100:103],a[152:155],v[208:211],v[100:103]
v_exp_f32 v92,v92
v_mfma_f32_16x16x32_bf16 v[100:103],a[148:151],v[200:203],v[100:103]
ds_read_b64_tr_b16 a[240:241],v2 offset:768
ds_read_b64_tr_b16 a[242:243],v2 offset:4864
v_mfma_f32_16x16x32_bf16 v[100:103],a[144:147],v[192:195],v[100:103]
v_exp_f32 v93,v93
v_mfma_f32_16x16x32_bf16 v[104:107],a[160:163],v[192:195],v[40:43]
ds_read_b64_tr_b16 a[244:245],v2 offset:8960
ds_read_b64_tr_b16 a[246:247],v2 offset:13056
v_mfma_f32_16x16x32_bf16 v[104:107],a[164:167],v[200:203],v[104:107]
ds_read_b64_tr_b16 a[248:249],v3 offset:768
v_cvt_pk_bf16_f32 v90,v92,v93
v_mfma_f32_16x16x32_bf16 v[104:107],a[168:171],v[208:211],v[104:107]
ds_read_b64_tr_b16 a[250:251],v3 offset:4864
v_bfe_u32 v2,v6,4,2
v_mfma_f32_16x16x32_bf16 v[104:107],a[172:175],v[216:219],v[104:107]
ds_read_b64_tr_b16 a[252:253],v3 offset:8960
v_lshl_add_u32 v1,v2,3,v1
v_mfma_f32_16x16x32_bf16 v[108:111],a[188:191],v[216:219],v[40:43]
ds_read_b64_tr_b16 a[254:255],v3 offset:13056
v_add_u32 v1,122880,v1
v_mfma_f32_16x16x32_bf16 v[108:111],a[184:187],v[208:211],v[108:111]
)A"
R"A(v_exp_f32 v94,v94
v_mfma_f32_16x16x32_bf16 v[108:111],a[180:183],v[200:203],v[108:111]
v_exp_f32 v95,v95
v_mfma_f32_16x16x32_bf16 v[108:111],a[176:179],v[192:195],v[108:111]
s_waitcnt lgkmcnt(6)
v_cvt_pk_bf16_f32 v91,v94,v95
v_bfe_u32 v2,v6,8,1
v_mfma_f32_16x16x32_bf16 a[0:3],a[192:195],v[64:67],a[0:3]
v_exp_f32 v96,v96
v_mfma_f32_16x16x32_bf16 a[0:3],a[196:199],v[72:75],a[0:3]
v_exp_f32 v97,v97
v_mfma_f32_16x16x32_bf16 a[4:7],a[200:203],v[64:67],a[4:7]
v_exp_f32 v98,v98
v_mfma_f32_16x16x32_bf16 a[4:7],a[204:207],v[72:75],a[4:7]
v_exp_f32 v99,v99
v_mfma_f32_16x16x32_bf16 a[8:11],a[208:211],v[64:67],a[8:11]
v_exp_f32 v100,v100
v_mfma_f32_16x16x32_bf16 a[8:11],a[212:215],v[72:75],a[8:11]
v_exp_f32 v101,v101
v_mfma_f32_16x16x32_bf16 a[12:15],a[216:219],v[64:67],a[12:15]
v_exp_f32 v102,v102
v_mfma_f32_16x16x32_bf16 a[12:15],a[220:223],v[72:75],a[12:15]
v_exp_f32 v103,v103
v_mfma_f32_16x16x32_bf16 a[16:19],a[224:227],v[64:67],a[16:19]
v_cvt_pk_bf16_f32 v96,v96,v97
v_cvt_pk_bf16_f32 v97,v98,v99
v_mfma_f32_16x16x32_bf16 a[16:19],a[228:231],v[72:75],a[16:19]
v_cvt_pk_bf16_f32 v98,v100,v101
v_cvt_pk_bf16_f32 v99,v102,v103
v_mfma_f32_16x16x32_bf16 a[20:23],a[232:235],v[64:67],a[20:23]
v_exp_f32 v104,v104
v_mfma_f32_16x16x32_bf16 a[20:23],a[236:239],v[72:75],a[20:23]
v_exp_f32 v105,v105
v_mfma_f32_16x16x32_bf16 a[24:27],a[240:243],v[64:67],a[24:27]
s_waitcnt lgkmcnt(0)
v_exp_f32 v106,v106
v_mfma_f32_16x16x32_bf16 a[24:27],a[244:247],v[72:75],a[24:27]
v_exp_f32 v107,v107
v_mfma_f32_16x16x32_bf16 a[28:31],a[248:251],v[64:67],a[28:31]
v_exp_f32 v108,v108
v_mfma_f32_16x16x32_bf16 a[28:31],a[252:255],v[72:75],a[28:31]
v_exp_f32 v109,v109
v_mfma_f32_16x16x32_bf16 v[48:51],v[28:31],v[64:67],v[48:51]
v_exp_f32 v110,v110
v_mfma_f32_16x16x32_bf16 v[48:51],v[28:31],v[72:75],v[48:51]
v_exp_f32 v111,v111
s_waitcnt vmcnt(11)
s_barrier
v_mfma_f32_16x16x32_bf16 v[112:115],a[128:131],v[224:227],v[44:47]
buffer_store_dwordx2 v[8:9],v10,s[44:47],s18 offen offset:256 sc1
v_mfma_f32_16x16x32_bf16 v[112:115],a[132:135],v[232:235],v[112:115]
v_cvt_pk_bf16_f32 v104,v104,v105
v_cvt_pk_bf16_f32 v105,v106,v107
v_mfma_f32_16x16x32_bf16 v[112:115],a[136:139],v[240:243],v[112:115]
v_cvt_pk_bf16_f32 v106,v108,v109
v_cvt_pk_bf16_f32 v107,v110,v111
v_mfma_f32_16x16x32_bf16 v[112:115],a[140:143],v[248:251],v[112:115]
s_add_u32 m0,s86,0
v_cmp_nlt_f32 s[54:55],v48,s73
v_max_f32 v49,0x00800000,v48
v_mfma_f32_16x16x32_bf16 v[116:119],a[156:159],v[248:251],v[44:47]
v_rcp_f32 v49,v49
v_mfma_f32_16x16x32_bf16 v[116:119],a[152:155],v[240:243],v[116:119]
v_lshl_add_u32 v1,v2,5,v1
v_lshlrev_b32 v2,6,v2
v_mfma_f32_16x16x32_bf16 v[116:119],a[148:151],v[232:235],v[116:119]
buffer_load_dwordx4 v24,s[48:51],s83 offen lds
v_mfma_f32_16x16x32_bf16 v[116:119],a[144:147],v[224:227],v[116:119]
v_exp_f32 v112,v112
v_mfma_f32_16x16x32_bf16 v[120:123],a[160:163],v[224:227],v[44:47]
v_exp_f32 v113,v113
v_mfma_f32_16x16x32_bf16 v[120:123],a[164:167],v[232:235],v[120:123]
v_exp_f32 v114,v114
v_mfma_f32_16x16x32_bf16 v[120:123],a[168:171],v[240:243],v[120:123]
v_exp_f32 v115,v115
v_mfma_f32_16x16x32_bf16 v[120:123],a[172:175],v[248:251],v[120:123]
v_exp_f32 v116,v116
v_mfma_f32_16x16x32_bf16 v[124:127],a[188:191],v[248:251],v[44:47]
buffer_load_dwordx4 v25,s[48:51],s83 offen offset:1024 lds
v_mfma_f32_16x16x32_bf16 v[124:127],a[184:187],v[240:243],v[124:127]
v_exp_f32 v117,v117
v_mfma_f32_16x16x32_bf16 v[124:127],a[180:183],v[232:235],v[124:127]
v_exp_f32 v118,v118
v_mfma_f32_16x16x32_bf16 v[124:127],a[176:179],v[224:227],v[124:127]
v_exp_f32 v119,v119
v_mfma_f32_16x16x32_bf16 v[52:55],v[28:31],v[80:83],v[52:55]
v_cvt_pk_bf16_f32 v112,v112,v113
v_cvt_pk_bf16_f32 v113,v114,v115
v_mfma_f32_16x16x32_bf16 v[52:55],v[28:31],v[88:91],v[52:55]
v_cvt_pk_bf16_f32 v114,v116,v117
v_cvt_pk_bf16_f32 v115,v118,v119
v_mfma_f32_16x16x32_bf16 v[132:135],a[192:195],v[80:83],v[132:135]
buffer_load_dwordx4 v26,s[48:51],s83 offen offset:2048 lds
v_mfma_f32_16x16x32_bf16 v[132:135],a[196:199],v[88:91],v[132:135]
v_exp_f32 v120,v120
v_mfma_f32_16x16x32_bf16 v[140:143],a[200:203],v[80:83],v[140:143]
v_exp_f32 v121,v121
v_mfma_f32_16x16x32_bf16 v[140:143],a[204:207],v[88:91],v[140:143]
v_exp_f32 v122,v122
v_mfma_f32_16x16x32_bf16 v[148:151],a[208:211],v[80:83],v[148:151]
v_exp_f32 v123,v123
v_mfma_f32_16x16x32_bf16 v[148:151],a[212:215],v[88:91],v[148:151]
v_exp_f32 v124,v124
v_mfma_f32_16x16x32_bf16 v[156:159],a[216:219],v[80:83],v[156:159]
buffer_load_dwordx4 v27,s[48:51],s83 offen offset:3072 lds
v_mfma_f32_16x16x32_bf16 v[156:159],a[220:223],v[88:91],v[156:159]
v_exp_f32 v125,v125
v_mfma_f32_16x16x32_bf16 v[164:167],a[224:227],v[80:83],v[164:167]
v_exp_f32 v126,v126
v_mfma_f32_16x16x32_bf16 v[164:167],a[228:231],v[88:91],v[164:167]
s_add_u32 m0,s86,4096
v_exp_f32 v127,v127
v_mfma_f32_16x16x32_bf16 v[172:175],a[232:235],v[80:83],v[172:175]
v_cvt_pk_bf16_f32 v120,v120,v121
v_cvt_pk_bf16_f32 v121,v122,v123
v_mfma_f32_16x16x32_bf16 v[172:175],a[236:239],v[88:91],v[172:175]
v_cvt_pk_bf16_f32 v122,v124,v125
v_cvt_pk_bf16_f32 v123,v126,v127
v_mfma_f32_16x16x32_bf16 v[180:183],a[240:243],v[80:83],v[180:183]
buffer_load_dwordx4 v7,s[48:51],s83 offen lds
v_mfma_f32_16x16x32_bf16 v[180:183],a[244:247],v[88:91],v[180:183]
v_cmp_nlt_f32 s[56:57],v52,s73
v_sub_u32 v2,v1,v2
v_mfma_f32_16x16x32_bf16 v[188:191],a[248:251],v[80:83],v[188:191]
s_or_b64 s[54:55],s[54:55],s[56:57]
v_accvgpr_read_b32 v8,a0
v_accvgpr_read_b32 v9,a1
v_mfma_f32_16x16x32_bf16 v[188:191],a[252:255],v[88:91],v[188:191]
s_add_u32 m0,s86,5120
v_accvgpr_read_b32 v10,a2
v_accvgpr_read_b32 v11,a3
v_mfma_f32_16x16x32_bf16 v[56:59],v[28:31],v[96:99],v[56:59]
v_accvgpr_read_b32 v12,a4
v_accvgpr_read_b32 v13,a5
v_mfma_f32_16x16x32_bf16 v[56:59],v[28:31],v[104:107],v[56:59]
v_accvgpr_read_b32 v14,a6
v_accvgpr_read_b32 v15,a7
v_mfma_f32_16x16x32_bf16 v[196:199],a[192:195],v[96:99],v[196:199]
buffer_load_dwordx4 v24,s[48:51],s84 offen lds
v_mfma_f32_16x16x32_bf16 v[196:199],a[196:199],v[104:107],v[196:199]
v_mul_f32 v8,v8,v49
v_mul_f32 v9,v9,v49
v_mfma_f32_16x16x32_bf16 v[204:207],a[200:203],v[96:99],v[204:207]
v_cmp_nlt_f32 s[56:57],v56,s73
v_mul_f32 v10,v10,v49
v_mfma_f32_16x16x32_bf16 v[204:207],a[204:207],v[104:107],v[204:207]
s_or_b64 s[54:55],s[54:55],s[56:57]
v_mul_f32 v11,v11,v49
v_mul_f32 v12,v12,v49
v_mfma_f32_16x16x32_bf16 v[212:215],a[208:211],v[96:99],v[212:215]
v_mul_f32 v13,v13,v49
v_mul_f32 v14,v14,v49
v_mfma_f32_16x16x32_bf16 v[212:215],a[212:215],v[104:107],v[212:215]
v_mul_f32 v15,v15,v49
v_cvt_pk_bf16_f32 v32,v8,v9
v_mfma_f32_16x16x32_bf16 v[220:223],a[216:219],v[96:99],v[220:223]
buffer_load_dwordx4 v25,s[48:51],s84 offen offset:1024 lds
v_mfma_f32_16x16x32_bf16 v[220:223],a[220:223],v[104:107],v[220:223]
v_cvt_pk_bf16_f32 v33,v10,v11
v_cvt_pk_bf16_f32 v34,v12,v13
v_mfma_f32_16x16x32_bf16 v[228:231],a[224:227],v[96:99],v[228:231]
v_cvt_pk_bf16_f32 v35,v14,v15
v_accvgpr_read_b32 v16,a8
v_mfma_f32_16x16x32_bf16 v[228:231],a[228:231],v[104:107],v[228:231]
v_accvgpr_read_b32 v17,a9
v_accvgpr_read_b32 v18,a10
v_mfma_f32_16x16x32_bf16 v[236:239],a[232:235],v[96:99],v[236:239]
v_accvgpr_read_b32 v19,a11
v_accvgpr_read_b32 v20,a12
v_mfma_f32_16x16x32_bf16 v[236:239],a[236:239],v[104:107],v[236:239]
v_accvgpr_read_b32 v21,a13
v_accvgpr_read_b32 v22,a14
v_mfma_f32_16x16x32_bf16 v[244:247],a[240:243],v[96:99],v[244:247]
buffer_load_dwordx4 v26,s[48:51],s84 offen offset:2048 lds
v_mfma_f32_16x16x32_bf16 v[244:247],a[244:247],v[104:107],v[244:247]
v_accvgpr_read_b32 v23,a15
v_mul_f32 v16,v16,v49
v_mfma_f32_16x16x32_bf16 v[252:255],a[248:251],v[96:99],v[252:255]
v_mul_f32 v17,v17,v49
v_mul_f32 v18,v18,v49
v_mfma_f32_16x16x32_bf16 v[252:255],a[252:255],v[104:107],v[252:255]
v_mul_f32 v19,v19,v49
v_mul_f32 v20,v20,v49
v_mfma_f32_16x16x32_bf16 v[60:63],v[28:31],v[112:115],v[60:63]
v_mul_f32 v21,v21,v49
v_mul_f32 v22,v22,v49
v_mfma_f32_16x16x32_bf16 v[60:63],v[28:31],v[120:123],v[60:63]
v_mul_f32 v23,v23,v49
v_cvt_pk_bf16_f32 v36,v16,v17
v_mfma_f32_16x16x32_bf16 a[32:35],a[192:195],v[112:115],a[32:35]
buffer_load_dwordx4 v27,s[48:51],s84 offen offset:3072 lds
v_mfma_f32_16x16x32_bf16 a[32:35],a[196:199],v[120:123],a[32:35]
v_cvt_pk_bf16_f32 v37,v18,v19
v_cvt_pk_bf16_f32 v38,v20,v21
v_mfma_f32_16x16x32_bf16 a[36:39],a[200:203],v[112:115],a[36:39]
v_cmp_nlt_f32 s[56:57],v60,s73
v_cvt_pk_bf16_f32 v39,v22,v23
v_mfma_f32_16x16x32_bf16 a[36:39],a[204:207],v[120:123],a[36:39]
s_add_u32 m0,s86,9216
v_accvgpr_read_b32 v8,a16
v_accvgpr_read_b32 v9,a17
v_mfma_f32_16x16x32_bf16 a[40:43],a[208:211],v[112:115],a[40:43]
s_or_b64 s[54:55],s[54:55],s[56:57]
v_accvgpr_read_b32 v10,a18
v_accvgpr_read_b32 v11,a19
v_mfma_f32_16x16x32_bf16 a[40:43],a[212:215],v[120:123],a[40:43]
v_accvgpr_read_b32 v12,a20
v_accvgpr_read_b32 v13,a21
v_mfma_f32_16x16x32_bf16 a[44:47],a[216:219],v[112:115],a[44:47]
buffer_load_dwordx4 v7,s[48:51],s84 offen lds
v_mfma_f32_16x16x32_bf16 a[44:47],a[220:223],v[120:123],a[44:47]
v_accvgpr_read_b32 v14,a22
v_accvgpr_read_b32 v15,a23
v_mfma_f32_16x16x32_bf16 a[48:51],a[224:227],v[112:115],a[48:51]
v_mul_f32 v8,v8,v49
v_mul_f32 v9,v9,v49
v_mfma_f32_16x16x32_bf16 a[48:51],a[228:231],v[120:123],a[48:51]
v_mul_f32 v10,v10,v49
v_mul_f32 v11,v11,v49
v_mfma_f32_16x16x32_bf16 a[52:55],a[232:235],v[112:115],a[52:55]
v_mul_f32 v12,v12,v49
v_mul_f32 v13,v13,v49
v_mfma_f32_16x16x32_bf16 a[52:55],a[236:239],v[120:123],a[52:55]
v_mul_f32 v14,v14,v49
v_mul_f32 v15,v15,v49
v_mfma_f32_16x16x32_bf16 a[56:59],a[240:243],v[112:115],a[56:59]
v_cvt_pk_bf16_f32 v40,v8,v9
v_cvt_pk_bf16_f32 v41,v10,v11
v_mfma_f32_16x16x32_bf16 a[56:59],a[244:247],v[120:123],a[56:59]
v_cvt_pk_bf16_f32 v42,v12,v13
v_cvt_pk_bf16_f32 v43,v14,v15
v_mfma_f32_16x16x32_bf16 a[60:63],a[248:251],v[112:115],a[60:63]
ds_read_b64_tr_b16 v[64:65],v1
ds_read_b64_tr_b16 v[66:67],v1 offset:5120
v_mfma_f32_16x16x32_bf16 a[60:63],a[252:255],v[120:123],a[60:63]
ds_read_b64_tr_b16 v[68:69],v1 offset:10240
v_max_f32 v53,0x00800000,v52
v_rcp_f32 v53,v53
v_max_f32 v57,0x00800000,v56
v_rcp_f32 v57,v57
v_max_f32 v61,0x00800000,v60
v_rcp_f32 v61,v61
ds_read_b64_tr_b16 v[70:71],v1 offset:15360
ds_read_b64_tr_b16 v[72:73],v1 offset:20480
ds_read_b64_tr_b16 v[74:75],v1 offset:25600
ds_read_b64_tr_b16 v[76:77],v1 offset:30720
ds_read_b64_tr_b16 v[78:79],v1 offset:35840
ds_read_b64_tr_b16 v[80:81],v2 offset:32
ds_read_b64_tr_b16 v[82:83],v2 offset:5152
ds_read_b64_tr_b16 v[84:85],v2 offset:10272
ds_read_b64_tr_b16 v[86:87],v2 offset:15392
ds_read_b64_tr_b16 v[88:89],v2 offset:20512
ds_read_b64_tr_b16 v[90:91],v2 offset:25632
ds_read_b64_tr_b16 v[92:93],v2 offset:30752
ds_read_b64_tr_b16 v[94:95],v2 offset:35872
s_cmp_lg_u64 s[54:55],0
s_cbranch_scc1 L_slow1_%=
L_ok1_%=:
s_cmp_ge_u32 s63,2
s_cbranch_scc1 L_GS_%=
L_gbF_%=:
s_waitcnt lgkmcnt(14)
v_mfma_f32_16x16x32_bf16 a[192:195],v[64:67],a[68:71],0
buffer_store_dwordx4 v[32:35],v6,s[40:43],s79 offen sc1
s_waitcnt lgkmcnt(12)
v_mfma_f32_16x16x32_bf16 a[192:195],v[68:71],a[76:79],a[192:195]
s_lshl_b32 s91,s63,8
v_bfe_u32 v3,v6,4,2
v_min_u32 v3,2,v3
s_waitcnt lgkmcnt(10)
v_mfma_f32_16x16x32_bf16 a[192:195],v[72:75],a[84:87],a[192:195]
s_add_u32 s87,s64,1
v_add3_u32 v3,v3,s64,1
v_and_b32 v3,3,v3
s_waitcnt lgkmcnt(8)
v_mfma_f32_16x16x32_bf16 a[192:195],v[76:79],a[92:95],a[192:195]
s_and_b32 s87,s87,3
ds_read_b64_tr_b16 v[96:97],v1 offset:64
v_lshlrev_b32 v3,6,v3
v_mfma_f32_16x16x32_bf16 a[196:199],v[76:79],a[124:127],0
s_lshl_b32 s87,s87,1
ds_read_b64_tr_b16 v[98:99],v1 offset:5184
ds_read_b64_tr_b16 v[100:101],v1 offset:10304
v_mfma_f32_16x16x32_bf16 a[196:199],v[72:75],a[116:119],a[196:199]
s_lshl_b32 s87,s87,16
ds_read_b64_tr_b16 v[102:103],v1 offset:15424
v_lshlrev_b32 v29,10,v6
v_mfma_f32_16x16x32_bf16 a[196:199],v[68:71],a[108:111],a[196:199]
s_add_u32 s88,s87,4096
ds_read_b64_tr_b16 v[104:105],v1 offset:20544
v_accvgpr_read_b32 v8,a24
v_mfma_f32_16x16x32_bf16 a[196:199],v[64:67],a[100:103],a[196:199]
s_add_u32 s89,s87,8192
ds_read_b64_tr_b16 v[106:107],v1 offset:25664
v_accvgpr_read_b32 v9,a25
v_mfma_f32_16x16x32_bf16 a[200:203],v[64:67],a[64:67],0
)A"
R"A(s_add_u32 s90,s87,12288
ds_read_b64_tr_b16 v[108:109],v1 offset:30784
ds_read_b64_tr_b16 v[110:111],v1 offset:35904
v_mfma_f32_16x16x32_bf16 a[200:203],v[68:71],a[72:75],a[200:203]
s_add_u32 s20,s64,2
ds_read_b64_tr_b16 v[112:113],v2 offset:96
v_accvgpr_read_b32 v10,a26
v_mfma_f32_16x16x32_bf16 a[200:203],v[72:75],a[80:83],a[200:203]
s_and_b32 s20,s20,3
ds_read_b64_tr_b16 v[114:115],v2 offset:5216
v_accvgpr_read_b32 v11,a27
v_mfma_f32_16x16x32_bf16 a[200:203],v[76:79],a[88:91],a[200:203]
s_lshl_b32 s20,s20,1
ds_read_b64_tr_b16 v[116:117],v2 offset:10336
v_accvgpr_read_b32 v12,a28
v_mfma_f32_16x16x32_bf16 a[204:207],v[76:79],a[120:123],0
s_lshl_b32 s20,s20,16
ds_read_b64_tr_b16 v[118:119],v2 offset:15456
ds_read_b64_tr_b16 v[120:121],v2 offset:20576
v_mfma_f32_16x16x32_bf16 a[204:207],v[72:75],a[112:115],a[204:207]
s_add_u32 s21,s20,4096
ds_read_b64_tr_b16 v[122:123],v2 offset:25696
v_accvgpr_read_b32 v13,a29
v_mfma_f32_16x16x32_bf16 a[204:207],v[68:71],a[104:107],a[204:207]
s_add_u32 s22,s20,8192
ds_read_b64_tr_b16 v[124:125],v2 offset:30816
v_accvgpr_read_b32 v14,a30
v_mfma_f32_16x16x32_bf16 a[204:207],v[64:67],a[96:99],a[204:207]
buffer_store_dwordx4 v[36:39],v6,s[40:43],s80 offen sc1
s_waitcnt lgkmcnt(9)
v_mfma_f32_16x16x32_bf16 a[208:211],v[80:83],a[68:71],0
s_add_u32 s23,s20,12288
ds_read_b64_tr_b16 v[126:127],v2 offset:35936
ds_read_b64_tr_b16 v[64:65],v1 offset:128
v_mfma_f32_16x16x32_bf16 a[208:211],v[84:87],a[76:79],a[208:211]
s_add_u32 m0,s86,122880
ds_read_b64_tr_b16 v[66:67],v1 offset:5248
v_accvgpr_read_b32 v15,a31
v_mfma_f32_16x16x32_bf16 a[208:211],v[88:91],a[84:87],a[208:211]
s_add_u32 s83,s64,3
ds_read_b64_tr_b16 v[68:69],v1 offset:10368
v_mul_f32 v8,v8,v49
v_mfma_f32_16x16x32_bf16 a[208:211],v[92:95],a[92:95],a[208:211]
s_and_b32 s83,s83,3
ds_read_b64_tr_b16 v[70:71],v1 offset:15488
v_mul_f32 v9,v9,v49
v_mfma_f32_16x16x32_bf16 a[212:215],v[92:95],a[124:127],0
s_waitcnt vmcnt(21)
buffer_load_dwordx2 v[4:5],v3,s[44:47],s91 offen offset:256 sc1
v_mfma_f32_16x16x32_bf16 a[212:215],v[88:91],a[116:119],a[212:215]
s_lshl_b32 s83,s83,1
s_nop 0
L_pd13_%=:
s_cbranch_execz L_trt1_%=
ds_read_b64_tr_b16 v[72:73],v1 offset:20608
ds_read_b64_tr_b16 v[74:75],v1 offset:25728
v_mfma_f32_16x16x32_bf16 a[212:215],v[84:87],a[108:111],a[212:215]
s_mul_i32 s83,s83,163840
ds_read_b64_tr_b16 v[76:77],v1 offset:30848
v_mul_f32 v10,v10,v49
v_mfma_f32_16x16x32_bf16 a[212:215],v[80:83],a[100:103],a[212:215]
s_add_u32 s83,s83,s85
ds_read_b64_tr_b16 v[78:79],v1 offset:35968
v_mul_f32 v11,v11,v49
v_mfma_f32_16x16x32_bf16 a[216:219],v[80:83],a[64:67],0
s_add_u32 s84,s83,20480
v_mul_f32 v12,v12,v49
v_mul_f32 v13,v13,v49
v_mfma_f32_16x16x32_bf16 a[216:219],v[84:87],a[72:75],a[216:219]
s_lshl_b32 s66,s61,3
v_mul_f32 v14,v14,v49
v_mul_f32 v15,v15,v49
v_mfma_f32_16x16x32_bf16 a[216:219],v[88:91],a[80:83],a[216:219]
s_add_u32 s66,s66,s64
v_cvt_pk_bf16_f32 v44,v8,v9
v_cvt_pk_bf16_f32 v45,v10,v11
v_mfma_f32_16x16x32_bf16 a[216:219],v[92:95],a[88:91],a[216:219]
s_mul_i32 s66,s66,262144
v_cvt_pk_bf16_f32 v46,v12,v13
v_cvt_pk_bf16_f32 v47,v14,v15
v_mfma_f32_16x16x32_bf16 a[220:223],v[92:95],a[120:123],0
s_add_u32 s66,s66,100663296
ds_read_b64_tr_b16 v[92:93],v2 offset:30880
ds_read_b64_tr_b16 v[94:95],v2 offset:36000
v_mfma_f32_16x16x32_bf16 a[220:223],v[88:91],a[112:115],a[220:223]
s_mov_b32 s38,262144
ds_read_b64_tr_b16 v[88:89],v2 offset:20640
v_mul_f32 v132,v132,v53
v_mfma_f32_16x16x32_bf16 a[220:223],v[84:87],a[104:107],a[220:223]
buffer_store_dwordx4 v[40:43],v6,s[40:43],s81 offen sc1
v_mfma_f32_16x16x32_bf16 a[220:223],v[80:83],a[96:99],a[220:223]
s_mov_b32 s39,0x20000
ds_read_b64_tr_b16 v[80:81],v2 offset:160
ds_read_b64_tr_b16 v[82:83],v2 offset:5280
v_mfma_f32_16x16x32_bf16 a[224:227],v[96:99],a[68:71],0
s_add_u32 s74,s63,1
ds_read_b64_tr_b16 v[84:85],v2 offset:10400
v_mul_f32 v133,v133,v53
v_mfma_f32_16x16x32_bf16 a[224:227],v[100:103],a[76:79],a[224:227]
s_and_b32 s74,s74,3
ds_read_b64_tr_b16 v[86:87],v2 offset:15520
v_mul_f32 v134,v134,v53
v_mfma_f32_16x16x32_bf16 a[224:227],v[104:107],a[84:87],a[224:227]
s_lshl_b32 s74,s74,2
ds_read_b64_tr_b16 v[90:91],v2 offset:25760
v_mul_f32 v135,v135,v53
s_waitcnt lgkmcnt(8)
v_mfma_f32_16x16x32_bf16 a[224:227],v[108:111],a[92:95],a[224:227]
s_add_u32 s74,s74,s60
v_mul_f32 v140,v140,v53
v_mul_f32 v141,v141,v53
v_mfma_f32_16x16x32_bf16 a[228:231],v[108:111],a[124:127],0
s_mul_i32 s74,s74,16384
v_mul_f32 v142,v142,v53
v_mul_f32 v143,v143,v53
v_mfma_f32_16x16x32_bf16 a[228:231],v[104:107],a[116:119],a[228:231]
s_add_u32 s74,s74,8192
v_cvt_pk_bf16_f32 v132,v132,v133
v_cvt_pk_bf16_f32 v133,v134,v135
v_mfma_f32_16x16x32_bf16 a[228:231],v[100:103],a[108:111],a[228:231]
s_add_u32 s75,s63,2
v_cvt_pk_bf16_f32 v134,v140,v141
v_cvt_pk_bf16_f32 v135,v142,v143
v_mfma_f32_16x16x32_bf16 a[228:231],v[96:99],a[100:103],a[228:231]
s_and_b32 s75,s75,3
v_mul_f32 v148,v148,v53
v_mul_f32 v149,v149,v53
v_mfma_f32_16x16x32_bf16 a[232:235],v[96:99],a[64:67],0
s_lshl_b32 s75,s75,2
v_mul_f32 v150,v150,v53
v_mul_f32 v151,v151,v53
v_mfma_f32_16x16x32_bf16 a[232:235],v[100:103],a[72:75],a[232:235]
s_add_u32 s75,s75,s60
v_mul_f32 v156,v156,v53
v_mul_f32 v157,v157,v53
v_mfma_f32_16x16x32_bf16 a[232:235],v[104:107],a[80:83],a[232:235]
s_mul_i32 s75,s75,16384
v_mul_f32 v158,v158,v53
v_mul_f32 v159,v159,v53
v_mfma_f32_16x16x32_bf16 a[232:235],v[108:111],a[88:91],a[232:235]
s_add_u32 s75,s75,4096
v_cvt_pk_bf16_f32 v148,v148,v149
v_cvt_pk_bf16_f32 v149,v150,v151
v_mfma_f32_16x16x32_bf16 a[236:239],v[108:111],a[120:123],0
s_add_u32 s76,s63,3
ds_read_b64_tr_b16 v[108:109],v1 offset:30912
ds_read_b64_tr_b16 v[110:111],v1 offset:36032
v_mfma_f32_16x16x32_bf16 a[236:239],v[104:107],a[112:115],a[236:239]
buffer_store_dwordx4 v[44:47],v6,s[40:43],s82 offen sc1
v_mfma_f32_16x16x32_bf16 a[236:239],v[100:103],a[104:107],a[236:239]
s_and_b32 s76,s76,3
ds_read_b64_tr_b16 v[100:101],v1 offset:10432
ds_read_b64_tr_b16 v[102:103],v1 offset:15552
v_mfma_f32_16x16x32_bf16 a[236:239],v[96:99],a[96:99],a[236:239]
s_lshl_b32 s76,s76,2
ds_read_b64_tr_b16 v[96:97],v1 offset:192
v_cvt_pk_bf16_f32 v150,v156,v157
v_mfma_f32_16x16x32_bf16 a[240:243],v[112:115],a[68:71],0
s_add_u32 s76,s76,s60
ds_read_b64_tr_b16 v[98:99],v1 offset:5312
v_cvt_pk_bf16_f32 v151,v158,v159
v_mfma_f32_16x16x32_bf16 a[240:243],v[116:119],a[76:79],a[240:243]
s_mul_i32 s76,s76,16384
ds_read_b64_tr_b16 v[104:105],v1 offset:20672
v_mul_f32 v164,v164,v53
v_mfma_f32_16x16x32_bf16 a[240:243],v[120:123],a[84:87],a[240:243]
s_add_u32 s76,s76,0
ds_read_b64_tr_b16 v[106:107],v1 offset:25792
v_mul_f32 v165,v165,v53
v_mfma_f32_16x16x32_bf16 a[240:243],v[124:127],a[92:95],a[240:243]
s_lshl_b32 s67,s63,10
v_mul_f32 v166,v166,v53
v_mul_f32 v167,v167,v53
v_mfma_f32_16x16x32_bf16 a[244:247],v[124:127],a[124:127],0
s_mov_b32 s26,307200
v_mul_f32 v172,v172,v53
v_mul_f32 v173,v173,v53
v_mfma_f32_16x16x32_bf16 a[244:247],v[120:123],a[116:119],a[244:247]
s_mov_b32 s27,0x20000
v_mul_f32 v174,v174,v53
v_mul_f32 v175,v175,v53
v_mfma_f32_16x16x32_bf16 a[244:247],v[116:119],a[108:111],a[244:247]
s_lshl_b32 s77,s63,2
v_cvt_pk_bf16_f32 v164,v164,v165
v_cvt_pk_bf16_f32 v165,v166,v167
v_mfma_f32_16x16x32_bf16 a[244:247],v[112:115],a[100:103],a[244:247]
s_add_u32 s77,s77,s60
v_cvt_pk_bf16_f32 v166,v172,v173
v_cvt_pk_bf16_f32 v167,v174,v175
v_mfma_f32_16x16x32_bf16 a[248:251],v[112:115],a[64:67],0
s_mul_i32 s77,s77,16384
v_mul_f32 v180,v180,v53
v_mul_f32 v181,v181,v53
v_mfma_f32_16x16x32_bf16 a[248:251],v[116:119],a[72:75],a[248:251]
s_add_u32 s65,s77,0
v_mul_f32 v182,v182,v53
v_mul_f32 v183,v183,v53
v_mfma_f32_16x16x32_bf16 a[248:251],v[120:123],a[80:83],a[248:251]
s_add_u32 s68,s77,12288
v_mul_f32 v188,v188,v53
v_mul_f32 v189,v189,v53
v_mfma_f32_16x16x32_bf16 a[248:251],v[124:127],a[88:91],a[248:251]
v_mul_f32 v190,v190,v53
v_mul_f32 v191,v191,v53
v_mfma_f32_16x16x32_bf16 a[252:255],v[124:127],a[120:123],0
buffer_load_dword v30,v29,s[8:11],s7 offen
v_mfma_f32_16x16x32_bf16 a[252:255],v[120:123],a[112:115],a[252:255]
ds_read_b64_tr_b16 v[120:121],v2 offset:20704
ds_read_b64_tr_b16 v[122:123],v2 offset:25824
v_mfma_f32_16x16x32_bf16 a[252:255],v[116:119],a[104:107],a[252:255]
buffer_store_dwordx4 v[132:135],v6,s[40:43],s79 offen offset:1024 sc1
v_mfma_f32_16x16x32_bf16 a[252:255],v[112:115],a[96:99],a[252:255]
ds_read_b64_tr_b16 v[112:113],v2 offset:224
ds_read_b64_tr_b16 v[114:115],v2 offset:5344
v_mfma_f32_16x16x32_bf16 a[0:3],v[64:67],a[68:71],0
ds_read_b64_tr_b16 v[116:117],v2 offset:10464
v_cvt_pk_bf16_f32 v180,v180,v181
v_mfma_f32_16x16x32_bf16 a[0:3],v[68:71],a[76:79],a[0:3]
ds_read_b64_tr_b16 v[118:119],v2 offset:15584
v_cvt_pk_bf16_f32 v181,v182,v183
v_mfma_f32_16x16x32_bf16 a[0:3],v[72:75],a[84:87],a[0:3]
ds_read_b64_tr_b16 v[124:125],v2 offset:30944
v_cvt_pk_bf16_f32 v182,v188,v189
v_mfma_f32_16x16x32_bf16 a[0:3],v[76:79],a[92:95],a[0:3]
ds_read_b64_tr_b16 v[126:127],v2 offset:36064
v_cvt_pk_bf16_f32 v183,v190,v191
v_mfma_f32_16x16x32_bf16 a[4:7],v[76:79],a[124:127],0
v_mul_f32 v196,v196,v57
v_mul_f32 v197,v197,v57
v_mfma_f32_16x16x32_bf16 a[4:7],v[72:75],a[116:119],a[4:7]
v_mul_f32 v198,v198,v57
v_mul_f32 v199,v199,v57
v_mfma_f32_16x16x32_bf16 a[4:7],v[68:71],a[108:111],a[4:7]
v_mul_f32 v204,v204,v57
v_mul_f32 v205,v205,v57
v_mfma_f32_16x16x32_bf16 a[4:7],v[64:67],a[100:103],a[4:7]
v_mul_f32 v206,v206,v57
v_mul_f32 v207,v207,v57
v_mfma_f32_16x16x32_bf16 a[8:11],v[64:67],a[64:67],0
v_cvt_pk_bf16_f32 v196,v196,v197
v_cvt_pk_bf16_f32 v197,v198,v199
v_mfma_f32_16x16x32_bf16 a[8:11],v[68:71],a[72:75],a[8:11]
v_cvt_pk_bf16_f32 v198,v204,v205
v_cvt_pk_bf16_f32 v199,v206,v207
v_mfma_f32_16x16x32_bf16 a[8:11],v[72:75],a[80:83],a[8:11]
v_mul_f32 v212,v212,v57
v_mul_f32 v213,v213,v57
v_mfma_f32_16x16x32_bf16 a[8:11],v[76:79],a[88:91],a[8:11]
v_mul_f32 v214,v214,v57
v_mul_f32 v215,v215,v57
v_mfma_f32_16x16x32_bf16 a[12:15],v[76:79],a[120:123],0
ds_read_b64_tr_b16 v[76:77],v1 offset:30976
ds_read_b64_tr_b16 v[78:79],v1 offset:36096
v_mfma_f32_16x16x32_bf16 a[12:15],v[72:75],a[112:115],a[12:15]
buffer_store_dwordx4 v[148:151],v6,s[40:43],s80 offen offset:1024 sc1
v_mfma_f32_16x16x32_bf16 a[12:15],v[68:71],a[104:107],a[12:15]
ds_read_b64_tr_b16 v[68:69],v1 offset:10496
ds_read_b64_tr_b16 v[70:71],v1 offset:15616
v_mfma_f32_16x16x32_bf16 a[12:15],v[64:67],a[96:99],a[12:15]
s_waitcnt lgkmcnt(4)
ds_read_b64_tr_b16 v[64:65],v1 offset:256
v_mul_f32 v220,v220,v57
v_mfma_f32_16x16x32_bf16 a[16:19],v[80:83],a[68:71],0
ds_read_b64_tr_b16 v[66:67],v1 offset:5376
v_mul_f32 v221,v221,v57
v_mfma_f32_16x16x32_bf16 a[16:19],v[84:87],a[76:79],a[16:19]
ds_read_b64_tr_b16 v[72:73],v1 offset:20736
v_mul_f32 v222,v222,v57
v_mfma_f32_16x16x32_bf16 a[16:19],v[88:91],a[84:87],a[16:19]
ds_read_b64_tr_b16 v[74:75],v1 offset:25856
v_add_u32 v1,0xffff6000,v1
v_mfma_f32_16x16x32_bf16 a[16:19],v[92:95],a[92:95],a[16:19]
v_mul_f32 v223,v223,v57
v_cvt_pk_bf16_f32 v212,v212,v213
v_mfma_f32_16x16x32_bf16 a[20:23],v[92:95],a[124:127],0
v_cvt_pk_bf16_f32 v213,v214,v215
v_cvt_pk_bf16_f32 v214,v220,v221
v_mfma_f32_16x16x32_bf16 a[20:23],v[88:91],a[116:119],a[20:23]
v_cvt_pk_bf16_f32 v215,v222,v223
v_mul_f32 v228,v228,v57
v_mfma_f32_16x16x32_bf16 a[20:23],v[84:87],a[108:111],a[20:23]
v_mul_f32 v229,v229,v57
v_mul_f32 v230,v230,v57
v_mfma_f32_16x16x32_bf16 a[20:23],v[80:83],a[100:103],a[20:23]
v_mul_f32 v231,v231,v57
v_mul_f32 v236,v236,v57
v_mfma_f32_16x16x32_bf16 a[24:27],v[80:83],a[64:67],0
v_mul_f32 v237,v237,v57
v_mul_f32 v238,v238,v57
v_mfma_f32_16x16x32_bf16 a[24:27],v[84:87],a[72:75],a[24:27]
v_mul_f32 v239,v239,v57
v_cvt_pk_bf16_f32 v228,v228,v229
v_mfma_f32_16x16x32_bf16 a[24:27],v[88:91],a[80:83],a[24:27]
s_waitcnt vmcnt(5)
v_cmp_ne_u32 s[56:57],s52,v4
v_cmp_ne_u32 s[58:59],s53,v5
v_mfma_f32_16x16x32_bf16 a[24:27],v[92:95],a[88:91],a[24:27]
s_or_b64 s[56:57],s[56:57],s[58:59]
s_cbranch_scc1 L_fr1_%=
L_fo1_%=:
v_cvt_pk_bf16_f32 v229,v230,v231
v_mfma_f32_16x16x32_bf16 a[28:31],v[92:95],a[120:123],0
buffer_store_dwordx4 v[164:167],v6,s[40:43],s81 offen offset:1024 sc1
v_mfma_f32_16x16x32_bf16 a[28:31],v[88:91],a[112:115],a[28:31]
)A"
R"A(s_lshl_b32 s91,s63,8
ds_read_b64_tr_b16 v[88:89],v2 offset:20768
ds_read_b64_tr_b16 v[90:91],v2 offset:25888
v_mfma_f32_16x16x32_bf16 a[28:31],v[84:87],a[104:107],a[28:31]
buffer_load_dwordx4 a[128:131],v6,s[28:31],s87 offen sc1
v_mfma_f32_16x16x32_bf16 a[28:31],v[80:83],a[96:99],a[28:31]
ds_read_b64_tr_b16 v[80:81],v2 offset:288
ds_read_b64_tr_b16 v[82:83],v2 offset:5408
v_mfma_f32_16x16x32_bf16 v[128:131],v[96:99],a[68:71],0
ds_read_b64_tr_b16 v[84:85],v2 offset:10528
v_cvt_pk_bf16_f32 v230,v236,v237
v_mfma_f32_16x16x32_bf16 v[128:131],v[100:103],a[76:79],v[128:131]
ds_read_b64_tr_b16 v[86:87],v2 offset:15648
v_cvt_pk_bf16_f32 v231,v238,v239
v_mfma_f32_16x16x32_bf16 v[128:131],v[104:107],a[84:87],v[128:131]
ds_read_b64_tr_b16 v[92:93],v2 offset:31008
v_mul_f32 v244,v244,v57
v_mfma_f32_16x16x32_bf16 v[128:131],v[108:111],a[92:95],v[128:131]
ds_read_b64_tr_b16 v[94:95],v2 offset:36128
v_add_u32 v2,0xffff6000,v2
v_mfma_f32_16x16x32_bf16 v[136:139],v[108:111],a[124:127],0
v_mul_f32 v245,v245,v57
v_mul_f32 v246,v246,v57
v_mfma_f32_16x16x32_bf16 v[136:139],v[104:107],a[116:119],v[136:139]
v_mul_f32 v247,v247,v57
v_mul_f32 v252,v252,v57
v_mfma_f32_16x16x32_bf16 v[136:139],v[100:103],a[108:111],v[136:139]
v_mul_f32 v253,v253,v57
v_mul_f32 v254,v254,v57
v_mfma_f32_16x16x32_bf16 v[136:139],v[96:99],a[100:103],v[136:139]
v_mul_f32 v255,v255,v57
v_cvt_pk_bf16_f32 v244,v244,v245
v_mfma_f32_16x16x32_bf16 v[144:147],v[96:99],a[64:67],0
v_cvt_pk_bf16_f32 v245,v246,v247
v_cvt_pk_bf16_f32 v246,v252,v253
v_mfma_f32_16x16x32_bf16 v[144:147],v[100:103],a[72:75],v[144:147]
buffer_load_dwordx4 a[144:147],v6,s[28:31],s88 offen sc1
v_mfma_f32_16x16x32_bf16 v[144:147],v[104:107],a[80:83],v[144:147]
v_cvt_pk_bf16_f32 v247,v254,v255
v_accvgpr_read_b32 v8,a40
v_mfma_f32_16x16x32_bf16 v[144:147],v[108:111],a[88:91],v[144:147]
buffer_store_dwordx4 v[180:183],v6,s[40:43],s82 offen offset:1024 sc1
v_mfma_f32_16x16x32_bf16 v[152:155],v[108:111],a[120:123],0
ds_read_b64_tr_b16 v[108:109],v1 offset:30720
ds_read_b64_tr_b16 v[110:111],v1 offset:35840
v_mfma_f32_16x16x32_bf16 v[152:155],v[104:107],a[112:115],v[152:155]
ds_read_b64_tr_b16 v[104:105],v1 offset:20480
v_accvgpr_read_b32 v9,a41
v_mfma_f32_16x16x32_bf16 v[152:155],v[100:103],a[104:107],v[152:155]
ds_read_b64_tr_b16 v[100:101],v1 offset:10240
v_accvgpr_read_b32 v10,a42
v_mfma_f32_16x16x32_bf16 v[152:155],v[96:99],a[96:99],v[152:155]
ds_read_b64_tr_b16 v[96:97],v1
v_accvgpr_read_b32 v11,a43
v_mfma_f32_16x16x32_bf16 v[160:163],v[112:115],a[68:71],0
ds_read_b64_tr_b16 v[98:99],v1 offset:5120
ds_read_b64_tr_b16 v[102:103],v1 offset:15360
v_mfma_f32_16x16x32_bf16 v[160:163],v[116:119],a[76:79],v[160:163]
ds_read_b64_tr_b16 v[106:107],v1 offset:25600
v_accvgpr_read_b32 v12,a44
v_mfma_f32_16x16x32_bf16 v[160:163],v[120:123],a[84:87],v[160:163]
v_accvgpr_read_b32 v13,a45
v_accvgpr_read_b32 v14,a46
v_mfma_f32_16x16x32_bf16 v[160:163],v[124:127],a[92:95],v[160:163]
v_accvgpr_read_b32 v15,a47
v_mul_f32 v8,v8,v61
v_mfma_f32_16x16x32_bf16 v[168:171],v[124:127],a[124:127],0
v_mul_f32 v9,v9,v61
v_mul_f32 v10,v10,v61
v_mfma_f32_16x16x32_bf16 v[168:171],v[120:123],a[116:119],v[168:171]
v_mul_f32 v11,v11,v61
v_mul_f32 v12,v12,v61
v_mfma_f32_16x16x32_bf16 v[168:171],v[116:119],a[108:111],v[168:171]
buffer_load_dwordx4 a[160:163],v6,s[28:31],s89 offen sc1
v_mfma_f32_16x16x32_bf16 v[168:171],v[112:115],a[100:103],v[168:171]
v_mul_f32 v13,v13,v61
v_mul_f32 v14,v14,v61
v_mfma_f32_16x16x32_bf16 v[176:179],v[112:115],a[64:67],0
v_mul_f32 v15,v15,v61
v_cvt_pk_bf16_f32 v156,v8,v9
v_mfma_f32_16x16x32_bf16 v[176:179],v[116:119],a[72:75],v[176:179]
v_cvt_pk_bf16_f32 v157,v10,v11
v_cvt_pk_bf16_f32 v158,v12,v13
v_mfma_f32_16x16x32_bf16 v[176:179],v[120:123],a[80:83],v[176:179]
buffer_store_dwordx4 v[196:199],v6,s[40:43],s79 offen offset:2048 sc1
v_mfma_f32_16x16x32_bf16 v[176:179],v[124:127],a[88:91],v[176:179]
v_cvt_pk_bf16_f32 v159,v14,v15
v_accvgpr_read_b32 v8,a56
v_mfma_f32_16x16x32_bf16 v[184:187],v[124:127],a[120:123],0
v_accvgpr_read_b32 v9,a57
v_accvgpr_read_b32 v10,a58
v_mfma_f32_16x16x32_bf16 v[184:187],v[120:123],a[112:115],v[184:187]
ds_read_b64_tr_b16 v[120:121],v2 offset:20512
ds_read_b64_tr_b16 v[122:123],v2 offset:25632
v_mfma_f32_16x16x32_bf16 v[184:187],v[116:119],a[104:107],v[184:187]
ds_read_b64_tr_b16 v[116:117],v2 offset:10272
v_accvgpr_read_b32 v11,a59
v_mfma_f32_16x16x32_bf16 v[184:187],v[112:115],a[96:99],v[184:187]
s_waitcnt lgkmcnt(3)
ds_read_b64_tr_b16 v[112:113],v2 offset:32
v_accvgpr_read_b32 v12,a60
v_mfma_f32_16x16x32_bf16 v[192:195],v[64:67],a[68:71],0
ds_read_b64_tr_b16 v[114:115],v2 offset:5152
v_accvgpr_read_b32 v13,a61
v_mfma_f32_16x16x32_bf16 v[192:195],v[68:71],a[76:79],v[192:195]
ds_read_b64_tr_b16 v[118:119],v2 offset:15392
v_accvgpr_read_b32 v14,a62
v_mfma_f32_16x16x32_bf16 v[192:195],v[72:75],a[84:87],v[192:195]
v_accvgpr_read_b32 v15,a63
v_mul_f32 v8,v8,v61
v_mfma_f32_16x16x32_bf16 v[192:195],v[76:79],a[92:95],v[192:195]
buffer_load_dwordx4 a[176:179],v6,s[28:31],s90 offen sc1
v_mfma_f32_16x16x32_bf16 v[200:203],v[76:79],a[124:127],0
v_mul_f32 v9,v9,v61
v_mul_f32 v10,v10,v61
v_mfma_f32_16x16x32_bf16 v[200:203],v[72:75],a[116:119],v[200:203]
v_mul_f32 v11,v11,v61
v_mul_f32 v12,v12,v61
v_mfma_f32_16x16x32_bf16 v[200:203],v[68:71],a[108:111],v[200:203]
v_mul_f32 v13,v13,v61
v_mul_f32 v14,v14,v61
v_mfma_f32_16x16x32_bf16 v[200:203],v[64:67],a[100:103],v[200:203]
v_mul_f32 v15,v15,v61
v_cvt_pk_bf16_f32 v188,v8,v9
v_mfma_f32_16x16x32_bf16 v[208:211],v[64:67],a[64:67],0
v_cvt_pk_bf16_f32 v189,v10,v11
v_cvt_pk_bf16_f32 v190,v12,v13
v_mfma_f32_16x16x32_bf16 v[208:211],v[68:71],a[72:75],v[208:211]
buffer_store_dwordx4 v[212:215],v6,s[40:43],s80 offen offset:2048 sc1
v_mfma_f32_16x16x32_bf16 v[208:211],v[72:75],a[80:83],v[208:211]
ds_read_b64_tr_b16 v[124:125],v2 offset:30752
v_cvt_pk_bf16_f32 v191,v14,v15
v_mfma_f32_16x16x32_bf16 v[208:211],v[76:79],a[88:91],v[208:211]
ds_read_b64_tr_b16 v[126:127],v2 offset:35872
v_accvgpr_read_b32 v16,a32
v_mfma_f32_16x16x32_bf16 v[216:219],v[76:79],a[120:123],0
v_accvgpr_read_b32 v17,a33
v_accvgpr_read_b32 v18,a34
v_mfma_f32_16x16x32_bf16 v[216:219],v[72:75],a[112:115],v[216:219]
v_accvgpr_read_b32 v19,a35
v_accvgpr_read_b32 v20,a36
v_mfma_f32_16x16x32_bf16 v[216:219],v[68:71],a[104:107],v[216:219]
v_accvgpr_read_b32 v21,a37
v_accvgpr_read_b32 v22,a38
v_mfma_f32_16x16x32_bf16 v[216:219],v[64:67],a[96:99],v[216:219]
v_accvgpr_read_b32 v23,a39
v_mul_f32 v16,v16,v61
v_mfma_f32_16x16x32_bf16 v[224:227],v[80:83],a[68:71],0
buffer_load_dwordx4 a[180:183],v6,s[28:31],s90 offen offset:1024 sc1
v_mfma_f32_16x16x32_bf16 v[224:227],v[84:87],a[76:79],v[224:227]
v_mul_f32 v17,v17,v61
v_mul_f32 v18,v18,v61
v_mfma_f32_16x16x32_bf16 v[224:227],v[88:91],a[84:87],v[224:227]
v_mul_f32 v19,v19,v61
v_mul_f32 v20,v20,v61
v_mfma_f32_16x16x32_bf16 v[224:227],v[92:95],a[92:95],v[224:227]
v_mul_f32 v21,v21,v61
v_mul_f32 v22,v22,v61
v_mfma_f32_16x16x32_bf16 v[232:235],v[92:95],a[124:127],0
v_mul_f32 v23,v23,v61
v_cvt_pk_bf16_f32 v140,v16,v17
v_mfma_f32_16x16x32_bf16 v[232:235],v[88:91],a[116:119],v[232:235]
v_cvt_pk_bf16_f32 v141,v18,v19
v_cvt_pk_bf16_f32 v142,v20,v21
v_mfma_f32_16x16x32_bf16 v[232:235],v[84:87],a[108:111],v[232:235]
v_cvt_pk_bf16_f32 v143,v22,v23
v_accvgpr_read_b32 v16,a48
v_mfma_f32_16x16x32_bf16 v[232:235],v[80:83],a[100:103],v[232:235]
v_accvgpr_read_b32 v17,a49
v_accvgpr_read_b32 v18,a50
v_mfma_f32_16x16x32_bf16 v[240:243],v[80:83],a[64:67],0
buffer_store_dwordx4 v[228:231],v6,s[40:43],s81 offen offset:2048 sc1
v_mfma_f32_16x16x32_bf16 v[240:243],v[84:87],a[72:75],v[240:243]
v_accvgpr_read_b32 v19,a51
v_accvgpr_read_b32 v20,a52
v_mfma_f32_16x16x32_bf16 v[240:243],v[88:91],a[80:83],v[240:243]
v_accvgpr_read_b32 v21,a53
v_accvgpr_read_b32 v22,a54
v_mfma_f32_16x16x32_bf16 v[240:243],v[92:95],a[88:91],v[240:243]
v_accvgpr_read_b32 v23,a55
v_mul_f32 v16,v16,v61
v_mfma_f32_16x16x32_bf16 v[248:251],v[92:95],a[120:123],0
v_mul_f32 v17,v17,v61
v_mul_f32 v18,v18,v61
v_mfma_f32_16x16x32_bf16 v[248:251],v[88:91],a[112:115],v[248:251]
buffer_load_dwordx4 a[164:167],v6,s[28:31],s89 offen offset:1024 sc1
v_mfma_f32_16x16x32_bf16 v[248:251],v[84:87],a[104:107],v[248:251]
v_mul_f32 v19,v19,v61
v_mul_f32 v20,v20,v61
v_mfma_f32_16x16x32_bf16 v[248:251],v[80:83],a[96:99],v[248:251]
v_mul_f32 v21,v21,v61
v_mul_f32 v22,v22,v61
v_mfma_f32_16x16x32_bf16 a[192:195],v[96:99],v[32:35],a[192:195]
v_mul_f32 v23,v23,v61
v_cvt_pk_bf16_f32 v172,v16,v17
v_mfma_f32_16x16x32_bf16 a[192:195],v[100:103],v[36:39],a[192:195]
v_cvt_pk_bf16_f32 v173,v18,v19
v_cvt_pk_bf16_f32 v174,v20,v21
v_mfma_f32_16x16x32_bf16 a[192:195],v[104:107],v[40:43],a[192:195]
buffer_load_dwordx4 a[68:71],v6,s[28:31],s20 offen sc1
v_mfma_f32_16x16x32_bf16 a[192:195],v[108:111],v[44:47],a[192:195]
ds_read_b64_tr_b16 v[64:65],v1 offset:64
v_cvt_pk_bf16_f32 v175,v22,v23
v_mfma_f32_16x16x32_bf16 a[196:199],v[108:111],v[180:183],a[196:199]
ds_read_b64_tr_b16 v[66:67],v1 offset:5184
ds_read_b64_tr_b16 v[68:69],v1 offset:10304
v_mfma_f32_16x16x32_bf16 a[196:199],v[104:107],v[164:167],a[196:199]
s_cmp_eq_u32 s60,3
s_cselect_b32 s69,0x40000000,0
ds_read_b64_tr_b16 v[70:71],v1 offset:15424
v_mfma_f32_16x16x32_bf16 a[196:199],v[100:103],v[148:151],a[196:199]
ds_read_b64_tr_b16 v[72:73],v1 offset:20544
v_add_u32 v28,s69,v6
v_mfma_f32_16x16x32_bf16 a[196:199],v[96:99],v[132:135],a[196:199]
buffer_store_dwordx4 v[244:247],v6,s[40:43],s82 offen offset:2048 sc1
v_mfma_f32_16x16x32_bf16 a[200:203],v[96:99],v[196:199],a[200:203]
s_lshl_b32 s69,s63,2
ds_read_b64_tr_b16 v[74:75],v1 offset:25664
ds_read_b64_tr_b16 v[76:77],v1 offset:30784
v_mfma_f32_16x16x32_bf16 a[200:203],v[100:103],v[212:215],a[200:203]
ds_read_b64_tr_b16 v[78:79],v1 offset:35904
v_mov_b32 v8,s52
v_mfma_f32_16x16x32_bf16 a[200:203],v[104:107],v[228:231],a[200:203]
buffer_load_dwordx4 a[148:151],v6,s[28:31],s88 offen offset:1024 sc1
v_mfma_f32_16x16x32_bf16 a[200:203],v[108:111],v[244:247],a[200:203]
ds_read_b64_tr_b16 v[80:81],v2 offset:96
ds_read_b64_tr_b16 v[82:83],v2 offset:5216
v_mfma_f32_16x16x32_bf16 a[204:207],v[108:111],v[188:191],a[204:207]
ds_read_b64_tr_b16 v[84:85],v2 offset:10336
v_mov_b32 v9,s53
v_mfma_f32_16x16x32_bf16 a[204:207],v[104:107],v[172:175],a[204:207]
ds_read_b64_tr_b16 v[86:87],v2 offset:15456
v_lshlrev_b32 v10,20,v6
v_mfma_f32_16x16x32_bf16 a[204:207],v[100:103],v[156:159],a[204:207]
s_waitcnt lgkmcnt(7)
ds_read_b64_tr_b16 v[88:89],v2 offset:20576
v_bfe_u32 v3,v6,4,2
v_mfma_f32_16x16x32_bf16 a[204:207],v[96:99],v[140:143],a[204:207]
buffer_load_dwordx4 a[76:79],v6,s[28:31],s21 offen sc1
v_mfma_f32_16x16x32_bf16 a[208:211],v[112:115],v[32:35],a[208:211]
ds_read_b64_tr_b16 v[90:91],v2 offset:25696
ds_read_b64_tr_b16 v[92:93],v2 offset:30816
v_mfma_f32_16x16x32_bf16 a[208:211],v[116:119],v[36:39],a[208:211]
ds_read_b64_tr_b16 v[94:95],v2 offset:35936
v_min_u32 v3,2,v3
v_mfma_f32_16x16x32_bf16 a[208:211],v[120:123],v[40:43],a[208:211]
ds_read_b64_tr_b16 v[96:97],v1 offset:128
v_add3_u32 v3,v3,s64,1
v_mfma_f32_16x16x32_bf16 a[208:211],v[124:127],v[44:47],a[208:211]
ds_read_b64_tr_b16 v[98:99],v1 offset:5248
v_and_b32 v3,3,v3
v_mfma_f32_16x16x32_bf16 a[212:215],v[124:127],v[180:183],a[212:215]
ds_read_b64_tr_b16 v[100:101],v1 offset:10368
ds_read_b64_tr_b16 v[102:103],v1 offset:15488
v_mfma_f32_16x16x32_bf16 a[212:215],v[120:123],v[164:167],a[212:215]
ds_read_b64_tr_b16 v[104:105],v1 offset:20608
v_lshlrev_b32 v3,6,v3
v_mfma_f32_16x16x32_bf16 a[212:215],v[116:119],v[148:151],a[212:215]
buffer_store_dwordx4 v[140:143],v28,s[40:43],s79 offen offset:3072 sc1
v_mfma_f32_16x16x32_bf16 a[212:215],v[112:115],v[132:135],a[212:215]
ds_read_b64_tr_b16 v[106:107],v1 offset:25728
ds_read_b64_tr_b16 v[108:109],v1 offset:30848
v_mfma_f32_16x16x32_bf16 a[216:219],v[112:115],v[196:199],a[216:219]
buffer_load_dwordx4 a[132:135],v6,s[28:31],s87 offen offset:1024 sc1
v_mfma_f32_16x16x32_bf16 a[216:219],v[116:119],v[212:215],a[216:219]
ds_read_b64_tr_b16 v[110:111],v1 offset:35968
v_bfe_u32 v56,v6,6,2
v_mfma_f32_16x16x32_bf16 a[216:219],v[120:123],v[228:231],a[216:219]
)A"
R"A(v_lshrrev_b32 v57,8,v6
v_cmp_eq_u32 s[56:57],v56,v57
v_mfma_f32_16x16x32_bf16 a[216:219],v[124:127],v[244:247],a[216:219]
v_bfe_u32 v56,v6,4,1
v_lshlrev_b32 v56,4,v56
v_mfma_f32_16x16x32_bf16 a[220:223],v[124:127],v[188:191],a[220:223]
ds_read_b64_tr_b16 v[124:125],v2 offset:30880
ds_read_b64_tr_b16 v[126:127],v2 offset:36000
v_mfma_f32_16x16x32_bf16 a[220:223],v[120:123],v[172:175],a[220:223]
buffer_load_dwordx4 a[84:87],v6,s[28:31],s22 offen sc1
v_mfma_f32_16x16x32_bf16 a[220:223],v[116:119],v[156:159],a[220:223]
ds_read_b64_tr_b16 v[116:117],v2 offset:10400
ds_read_b64_tr_b16 v[118:119],v2 offset:15520
v_mfma_f32_16x16x32_bf16 a[220:223],v[112:115],v[140:143],a[220:223]
ds_read_b64_tr_b16 v[112:113],v2 offset:160
v_mov_b32 v57,0x3f80
v_mfma_f32_16x16x32_bf16 a[224:227],v[64:67],v[32:35],a[224:227]
ds_read_b64_tr_b16 v[114:115],v2 offset:5280
v_lshlrev_b32 v56,v56,v57
v_mfma_f32_16x16x32_bf16 a[224:227],v[68:71],v[36:39],a[224:227]
s_waitcnt lgkmcnt(6)
ds_read_b64_tr_b16 v[120:121],v2 offset:20640
v_cndmask_b32 v56,0,v56,s[56:57]
v_mfma_f32_16x16x32_bf16 a[224:227],v[72:75],v[40:43],a[224:227]
ds_read_b64_tr_b16 v[122:123],v2 offset:25760
v_bfe_u32 v57,v6,5,1
v_mfma_f32_16x16x32_bf16 a[224:227],v[76:79],v[44:47],a[224:227]
s_add_u32 s36,s12,s66
s_addc_u32 s37,s13,0
v_cmp_eq_u32 s[56:57],0,v57
v_mfma_f32_16x16x32_bf16 a[228:231],v[76:79],v[180:183],a[228:231]
buffer_load_dwordx4 a[136:139],v6,s[28:31],s87 offen offset:2048 sc1
v_mfma_f32_16x16x32_bf16 a[228:231],v[72:75],v[164:167],a[228:231]
s_and_b32 s37,s37,0xffff
v_cndmask_b32 v48,0,v56,s[56:57]
v_cndmask_b32 v49,v56,0,s[56:57]
v_mfma_f32_16x16x32_bf16 a[228:231],v[68:71],v[148:151],a[228:231]
buffer_store_dwordx4 v[156:159],v28,s[40:43],s80 offen offset:3072 sc1
v_mfma_f32_16x16x32_bf16 a[228:231],v[64:67],v[132:135],a[228:231]
s_mul_i32 s66,s61,307200
v_mov_b32 v58,s52
v_mov_b32 v59,s53
v_mfma_f32_16x16x32_bf16 a[232:235],v[64:67],v[196:199],a[232:235]
s_add_u32 s24,s16,s66
s_addc_u32 s25,s17,0
v_lshlrev_b32 v60,20,v6
v_mfma_f32_16x16x32_bf16 a[232:235],v[68:71],v[212:215],a[232:235]
buffer_load_dwordx4 a[92:95],v6,s[28:31],s23 offen sc1
v_mfma_f32_16x16x32_bf16 a[232:235],v[72:75],v[228:231],a[232:235]
s_mul_i32 s66,s60,16384
s_add_u32 s66,s66,s67
s_add_u32 s79,s66,12288
v_mfma_f32_16x16x32_bf16 a[232:235],v[76:79],v[244:247],a[232:235]
s_add_u32 s80,s66,77824
v_bfe_u32 v61,v6,4,1
v_add3_u32 v61,v61,s63,2
v_mfma_f32_16x16x32_bf16 a[236:239],v[76:79],v[188:191],a[236:239]
s_and_b32 s25,s25,0xffff
ds_read_b64_tr_b16 v[76:77],v1 offset:30912
ds_read_b64_tr_b16 v[78:79],v1 offset:36032
v_mfma_f32_16x16x32_bf16 a[236:239],v[72:75],v[172:175],a[236:239]
s_add_u32 s67,s77,8192
ds_read_b64_tr_b16 v[72:73],v1 offset:20672
v_and_b32 v61,3,v61
v_mfma_f32_16x16x32_bf16 a[236:239],v[68:71],v[156:159],a[236:239]
ds_read_b64_tr_b16 v[68:69],v1 offset:10432
v_lshlrev_b32 v61,8,v61
v_mfma_f32_16x16x32_bf16 a[236:239],v[64:67],v[140:143],a[236:239]
ds_read_b64_tr_b16 v[64:65],v1 offset:192
v_mov_b32 v50,0
v_mfma_f32_16x16x32_bf16 a[240:243],v[80:83],v[32:35],a[240:243]
ds_read_b64_tr_b16 v[66:67],v1 offset:5312
L_pd14_%=:
s_cbranch_execz L_trt1_%=
ds_read_b64_tr_b16 v[70:71],v1 offset:15552
v_mfma_f32_16x16x32_bf16 a[240:243],v[84:87],v[36:39],a[240:243]
buffer_load_dwordx4 a[152:155],v6,s[28:31],s88 offen offset:2048 sc1
v_mfma_f32_16x16x32_bf16 a[240:243],v[88:91],v[40:43],a[240:243]
ds_read_b64_tr_b16 v[74:75],v1 offset:25792
v_mov_b32 v51,0
v_mfma_f32_16x16x32_bf16 a[240:243],v[92:95],v[44:47],a[240:243]
v_mov_b32 v52,0
v_mov_b32 v53,0
v_mfma_f32_16x16x32_bf16 a[244:247],v[92:95],v[180:183],a[244:247]
v_mov_b32 v54,v48
v_mov_b32 v55,v49
v_mfma_f32_16x16x32_bf16 a[244:247],v[88:91],v[164:167],a[244:247]
buffer_store_dwordx4 v[172:175],v28,s[40:43],s81 offen offset:3072 sc1
v_mfma_f32_16x16x32_bf16 a[244:247],v[84:87],v[148:151],a[244:247]
s_add_u32 s81,s66,143360
v_bfe_u32 v56,v6,4,4
v_mul_u32_u24 v56,1280,v56
v_mfma_f32_16x16x32_bf16 a[244:247],v[80:83],v[132:135],a[244:247]
buffer_load_dwordx4 a[124:127],v6,s[28:31],s23 offen offset:1024 sc1
v_mfma_f32_16x16x32_bf16 a[248:251],v[80:83],v[196:199],a[248:251]
v_bfe_u32 v57,v6,8,1
v_lshl_add_u32 v56,v57,5,v56
v_mfma_f32_16x16x32_bf16 a[248:251],v[84:87],v[212:215],a[248:251]
v_lshrrev_b32 v57,9,v6
v_lshl_add_u32 v56,v57,4,v56
v_mfma_f32_16x16x32_bf16 a[248:251],v[88:91],v[228:231],a[248:251]
v_mfma_f32_16x16x32_bf16 a[248:251],v[92:95],v[244:247],a[248:251]
v_mfma_f32_16x16x32_bf16 a[252:255],v[92:95],v[188:191],a[252:255]
ds_read_b64_tr_b16 v[92:93],v2 offset:30944
ds_read_b64_tr_b16 v[94:95],v2 offset:36064
v_mfma_f32_16x16x32_bf16 a[252:255],v[88:91],v[172:175],a[252:255]
ds_read_b64_tr_b16 v[88:89],v2 offset:20704
v_mfma_f32_16x16x32_bf16 a[252:255],v[84:87],v[156:159],a[252:255]
buffer_load_dwordx4 a[168:171],v6,s[28:31],s89 offen offset:2048 sc1
v_mfma_f32_16x16x32_bf16 a[252:255],v[80:83],v[140:143],a[252:255]
ds_read_b64_tr_b16 v[80:81],v2 offset:224
ds_read_b64_tr_b16 v[82:83],v2 offset:5344
v_mfma_f32_16x16x32_bf16 a[0:3],v[96:99],v[32:35],a[0:3]
ds_read_b64_tr_b16 v[84:85],v2 offset:10464
v_mfma_f32_16x16x32_bf16 a[0:3],v[100:103],v[36:39],a[0:3]
ds_read_b64_tr_b16 v[86:87],v2 offset:15584
v_mfma_f32_16x16x32_bf16 a[0:3],v[104:107],v[40:43],a[0:3]
ds_read_b64_tr_b16 v[90:91],v2 offset:25824
v_mfma_f32_16x16x32_bf16 a[0:3],v[108:111],v[44:47],a[0:3]
buffer_load_dwordx4 a[116:119],v6,s[28:31],s22 offen offset:1024 sc1
v_mfma_f32_16x16x32_bf16 a[4:7],v[108:111],v[180:183],a[4:7]
v_mfma_f32_16x16x32_bf16 a[4:7],v[104:107],v[164:167],a[4:7]
buffer_store_dwordx4 v[188:191],v28,s[40:43],s82 offen offset:3072 sc1
v_mfma_f32_16x16x32_bf16 a[4:7],v[100:103],v[148:151],a[4:7]
s_add_u32 s82,s66,208896
s_mul_i32 s66,s64,320
v_mfma_f32_16x16x32_bf16 a[4:7],v[96:99],v[132:135],a[4:7]
v_mfma_f32_16x16x32_bf16 a[8:11],v[96:99],v[196:199],a[8:11]
v_mfma_f32_16x16x32_bf16 a[8:11],v[100:103],v[212:215],a[8:11]
v_mfma_f32_16x16x32_bf16 a[8:11],v[104:107],v[228:231],a[8:11]
v_mfma_f32_16x16x32_bf16 a[8:11],v[108:111],v[244:247],a[8:11]
buffer_load_dwordx4 a[184:187],v6,s[28:31],s90 offen offset:2048 sc1
v_mfma_f32_16x16x32_bf16 a[12:15],v[108:111],v[188:191],a[12:15]
ds_read_b64_tr_b16 v[108:109],v1 offset:30976
ds_read_b64_tr_b16 v[110:111],v1 offset:36096
v_mfma_f32_16x16x32_bf16 a[12:15],v[104:107],v[172:175],a[12:15]
ds_read_b64_tr_b16 v[104:105],v1 offset:20736
v_mfma_f32_16x16x32_bf16 a[12:15],v[100:103],v[156:159],a[12:15]
ds_read_b64_tr_b16 v[100:101],v1 offset:10496
v_mfma_f32_16x16x32_bf16 a[12:15],v[96:99],v[140:143],a[12:15]
s_waitcnt lgkmcnt(4)
ds_read_b64_tr_b16 v[96:97],v1 offset:256
v_mfma_f32_16x16x32_bf16 a[16:19],v[112:115],v[32:35],a[16:19]
buffer_load_dwordx4 a[108:111],v6,s[28:31],s21 offen offset:1024 sc1
v_mfma_f32_16x16x32_bf16 a[16:19],v[116:119],v[36:39],a[16:19]
ds_read_b64_tr_b16 v[98:99],v1 offset:5376
ds_read_b64_tr_b16 v[102:103],v1 offset:15616
v_mfma_f32_16x16x32_bf16 a[16:19],v[120:123],v[40:43],a[16:19]
ds_read_b64_tr_b16 v[106:107],v1 offset:25856
v_add_u32 v1,0xffff6000,v1
v_mfma_f32_16x16x32_bf16 a[16:19],v[124:127],v[44:47],a[16:19]
v_mfma_f32_16x16x32_bf16 a[20:23],v[124:127],v[180:183],a[20:23]
v_mfma_f32_16x16x32_bf16 a[20:23],v[120:123],v[164:167],a[20:23]
v_mfma_f32_16x16x32_bf16 a[20:23],v[116:119],v[148:151],a[20:23]
v_mfma_f32_16x16x32_bf16 a[20:23],v[112:115],v[132:135],a[20:23]
v_mfma_f32_16x16x32_bf16 a[24:27],v[112:115],v[196:199],a[24:27]
buffer_load_dwordx4 a[188:191],v28,s[28:31],s90 offen offset:3072 sc1
v_mfma_f32_16x16x32_bf16 a[24:27],v[116:119],v[212:215],a[24:27]
v_mfma_f32_16x16x32_bf16 a[24:27],v[120:123],v[228:231],a[24:27]
v_mfma_f32_16x16x32_bf16 a[24:27],v[124:127],v[244:247],a[24:27]
v_mfma_f32_16x16x32_bf16 a[28:31],v[124:127],v[188:191],a[28:31]
ds_read_b64_tr_b16 v[124:125],v2 offset:31008
ds_read_b64_tr_b16 v[126:127],v2 offset:36128
v_mfma_f32_16x16x32_bf16 a[28:31],v[120:123],v[172:175],a[28:31]
buffer_load_dwordx4 a[100:103],v6,s[28:31],s20 offen offset:1024 sc1
v_mfma_f32_16x16x32_bf16 a[28:31],v[116:119],v[156:159],a[28:31]
ds_read_b64_tr_b16 v[116:117],v2 offset:10528
ds_read_b64_tr_b16 v[118:119],v2 offset:15648
v_mfma_f32_16x16x32_bf16 a[28:31],v[112:115],v[140:143],a[28:31]
ds_read_b64_tr_b16 v[112:113],v2 offset:288
v_mfma_f32_16x16x32_bf16 v[128:131],v[64:67],v[32:35],v[128:131]
ds_read_b64_tr_b16 v[114:115],v2 offset:5408
v_mfma_f32_16x16x32_bf16 v[128:131],v[68:71],v[36:39],v[128:131]
ds_read_b64_tr_b16 v[120:121],v2 offset:20768
v_mfma_f32_16x16x32_bf16 v[128:131],v[72:75],v[40:43],v[128:131]
ds_read_b64_tr_b16 v[122:123],v2 offset:25888
v_add_u32 v2,0xffff6000,v2
v_mfma_f32_16x16x32_bf16 v[128:131],v[76:79],v[44:47],v[128:131]
v_mfma_f32_16x16x32_bf16 v[136:139],v[76:79],v[180:183],v[136:139]
v_mfma_f32_16x16x32_bf16 v[136:139],v[72:75],v[164:167],v[136:139]
buffer_load_dwordx4 a[172:175],v28,s[28:31],s89 offen offset:3072 sc1
v_mfma_f32_16x16x32_bf16 v[136:139],v[68:71],v[148:151],v[136:139]
v_mfma_f32_16x16x32_bf16 v[136:139],v[64:67],v[132:135],v[136:139]
v_mfma_f32_16x16x32_bf16 v[144:147],v[64:67],v[196:199],v[144:147]
v_mfma_f32_16x16x32_bf16 v[144:147],v[68:71],v[212:215],v[144:147]
v_mfma_f32_16x16x32_bf16 v[144:147],v[72:75],v[228:231],v[144:147]
buffer_load_dwordx4 a[64:67],v6,s[28:31],s20 offen offset:2048 sc1
v_mfma_f32_16x16x32_bf16 v[144:147],v[76:79],v[244:247],v[144:147]
v_mfma_f32_16x16x32_bf16 v[152:155],v[76:79],v[188:191],v[152:155]
v_mfma_f32_16x16x32_bf16 v[152:155],v[72:75],v[172:175],v[152:155]
v_mfma_f32_16x16x32_bf16 v[152:155],v[68:71],v[156:159],v[152:155]
v_mfma_f32_16x16x32_bf16 v[152:155],v[64:67],v[140:143],v[152:155]
s_mov_b64 exec,0
s_cmp_eq_u32 s60,0
s_cbranch_scc1 L_pd16_%=
s_cmp_eq_u32 s60,1
s_cbranch_scc1 L_pd17_%=
s_cmp_eq_u32 s60,2
s_cbranch_scc1 L_pd18_%=
s_branch L_trtg2F_%=
L_trtg2F_%=:
s_mov_b64 exec,-1
s_waitcnt lgkmcnt(0)
s_barrier
v_mfma_f32_16x16x32_bf16 v[160:163],v[80:83],v[32:35],v[160:163]
buffer_load_dwordx4 v24,s[48:51],s83 offen lds
v_mfma_f32_16x16x32_bf16 v[160:163],v[84:87],v[36:39],v[160:163]
ds_read_b64_tr_b16 v[64:65],v1
ds_read_b64_tr_b16 v[66:67],v1 offset:5120
v_mfma_f32_16x16x32_bf16 v[160:163],v[88:91],v[40:43],v[160:163]
ds_read_b64_tr_b16 v[68:69],v1 offset:10240
v_mfma_f32_16x16x32_bf16 v[160:163],v[92:95],v[44:47],v[160:163]
ds_read_b64_tr_b16 v[70:71],v1 offset:15360
v_mfma_f32_16x16x32_bf16 v[168:171],v[92:95],v[180:183],v[168:171]
ds_read_b64_tr_b16 v[72:73],v1 offset:20480
v_mfma_f32_16x16x32_bf16 v[168:171],v[88:91],v[164:167],v[168:171]
buffer_load_dwordx4 a[156:159],v28,s[28:31],s88 offen offset:3072 sc1
v_mfma_f32_16x16x32_bf16 v[168:171],v[84:87],v[148:151],v[168:171]
ds_read_b64_tr_b16 v[74:75],v1 offset:25600
ds_read_b64_tr_b16 v[76:77],v1 offset:30720
v_mfma_f32_16x16x32_bf16 v[168:171],v[80:83],v[132:135],v[168:171]
ds_read_b64_tr_b16 v[78:79],v1 offset:35840
v_mfma_f32_16x16x32_bf16 v[176:179],v[80:83],v[196:199],v[176:179]
v_mfma_f32_16x16x32_bf16 v[176:179],v[84:87],v[212:215],v[176:179]
v_mfma_f32_16x16x32_bf16 v[176:179],v[88:91],v[228:231],v[176:179]
buffer_load_dwordx4 a[72:75],v6,s[28:31],s21 offen offset:2048 sc1
v_mfma_f32_16x16x32_bf16 v[176:179],v[92:95],v[244:247],v[176:179]
v_mfma_f32_16x16x32_bf16 v[184:187],v[92:95],v[188:191],v[184:187]
ds_read_b64_tr_b16 v[92:93],v2 offset:30752
ds_read_b64_tr_b16 v[94:95],v2 offset:35872
v_mfma_f32_16x16x32_bf16 v[184:187],v[88:91],v[172:175],v[184:187]
ds_read_b64_tr_b16 v[88:89],v2 offset:20512
v_mfma_f32_16x16x32_bf16 v[184:187],v[84:87],v[156:159],v[184:187]
ds_read_b64_tr_b16 v[84:85],v2 offset:10272
v_mfma_f32_16x16x32_bf16 v[184:187],v[80:83],v[140:143],v[184:187]
buffer_load_dwordx4 a[140:143],v28,s[28:31],s87 offen offset:3072 sc1
v_mfma_f32_16x16x32_bf16 v[192:195],v[96:99],v[32:35],v[192:195]
s_add_u32 s87,s64,3
ds_read_b64_tr_b16 v[80:81],v2 offset:32
ds_read_b64_tr_b16 v[82:83],v2 offset:5152
v_mfma_f32_16x16x32_bf16 v[192:195],v[100:103],v[36:39],v[192:195]
s_and_b32 s87,s87,3
s_lshl_b32 s87,s87,1
ds_read_b64_tr_b16 v[86:87],v2 offset:15392
v_mfma_f32_16x16x32_bf16 v[192:195],v[104:107],v[40:43],v[192:195]
)A"
R"A(s_lshl_b32 s87,s87,16
s_add_u32 s88,s87,4096
ds_read_b64_tr_b16 v[90:91],v2 offset:25632
v_mfma_f32_16x16x32_bf16 v[192:195],v[108:111],v[44:47],v[192:195]
s_add_u32 s89,s87,8192
s_add_u32 s90,s87,12288
v_mfma_f32_16x16x32_bf16 v[200:203],v[108:111],v[180:183],v[200:203]
buffer_load_dwordx4 a[80:83],v6,s[28:31],s22 offen offset:2048 sc1
v_mfma_f32_16x16x32_bf16 v[200:203],v[104:107],v[164:167],v[200:203]
v_mfma_f32_16x16x32_bf16 v[200:203],v[100:103],v[148:151],v[200:203]
v_mfma_f32_16x16x32_bf16 v[200:203],v[96:99],v[132:135],v[200:203]
v_mfma_f32_16x16x32_bf16 v[208:211],v[96:99],v[196:199],v[208:211]
v_mfma_f32_16x16x32_bf16 v[208:211],v[100:103],v[212:215],v[208:211]
v_mfma_f32_16x16x32_bf16 v[208:211],v[104:107],v[228:231],v[208:211]
v_mfma_f32_16x16x32_bf16 v[208:211],v[108:111],v[244:247],v[208:211]
buffer_load_dwordx4 v25,s[48:51],s83 offen offset:1024 lds
v_mfma_f32_16x16x32_bf16 v[216:219],v[108:111],v[188:191],v[216:219]
ds_read_b64_tr_b16 v[108:109],v1 offset:30784
ds_read_b64_tr_b16 v[110:111],v1 offset:35904
v_mfma_f32_16x16x32_bf16 v[216:219],v[104:107],v[172:175],v[216:219]
ds_read_b64_tr_b16 v[104:105],v1 offset:20544
v_mfma_f32_16x16x32_bf16 v[216:219],v[100:103],v[156:159],v[216:219]
ds_read_b64_tr_b16 v[100:101],v1 offset:10304
v_mfma_f32_16x16x32_bf16 v[216:219],v[96:99],v[140:143],v[216:219]
ds_read_b64_tr_b16 v[96:97],v1 offset:64
v_mfma_f32_16x16x32_bf16 v[224:227],v[112:115],v[32:35],v[224:227]
buffer_load_dwordx4 a[88:91],v6,s[28:31],s23 offen offset:2048 sc1
v_mfma_f32_16x16x32_bf16 v[224:227],v[116:119],v[36:39],v[224:227]
ds_read_b64_tr_b16 v[98:99],v1 offset:5184
ds_read_b64_tr_b16 v[102:103],v1 offset:15424
v_mfma_f32_16x16x32_bf16 v[224:227],v[120:123],v[40:43],v[224:227]
ds_read_b64_tr_b16 v[106:107],v1 offset:25664
v_mfma_f32_16x16x32_bf16 v[224:227],v[124:127],v[44:47],v[224:227]
v_mfma_f32_16x16x32_bf16 v[232:235],v[124:127],v[180:183],v[232:235]
v_mfma_f32_16x16x32_bf16 v[232:235],v[120:123],v[164:167],v[232:235]
v_mfma_f32_16x16x32_bf16 v[232:235],v[116:119],v[148:151],v[232:235]
v_mfma_f32_16x16x32_bf16 v[232:235],v[112:115],v[132:135],v[232:235]
buffer_load_dwordx4 v26,s[48:51],s83 offen offset:2048 lds
v_mfma_f32_16x16x32_bf16 v[240:243],v[112:115],v[196:199],v[240:243]
v_mfma_f32_16x16x32_bf16 v[240:243],v[116:119],v[212:215],v[240:243]
v_mfma_f32_16x16x32_bf16 v[240:243],v[120:123],v[228:231],v[240:243]
v_mfma_f32_16x16x32_bf16 v[240:243],v[124:127],v[244:247],v[240:243]
v_mfma_f32_16x16x32_bf16 v[248:251],v[124:127],v[188:191],v[248:251]
ds_read_b64_tr_b16 v[124:125],v2 offset:30816
ds_read_b64_tr_b16 v[126:127],v2 offset:35936
v_mfma_f32_16x16x32_bf16 v[248:251],v[120:123],v[172:175],v[248:251]
buffer_load_dwordx4 a[120:123],v28,s[28:31],s23 offen offset:3072 sc1
v_mfma_f32_16x16x32_bf16 v[248:251],v[116:119],v[156:159],v[248:251]
ds_read_b64_tr_b16 v[116:117],v2 offset:10336
ds_read_b64_tr_b16 v[118:119],v2 offset:15456
v_mfma_f32_16x16x32_bf16 v[248:251],v[112:115],v[140:143],v[248:251]
s_waitcnt vmcnt(39) lgkmcnt(4)
ds_read_b64_tr_b16 v[112:113],v2 offset:96
v_mfma_f32_16x16x32_bf16 a[192:195],v[64:67],a[128:131],a[192:195]
ds_read_b64_tr_b16 v[114:115],v2 offset:5216
v_mfma_f32_16x16x32_bf16 a[192:195],v[68:71],a[144:147],a[192:195]
s_waitcnt vmcnt(37)
ds_read_b64_tr_b16 v[120:121],v2 offset:20576
v_mfma_f32_16x16x32_bf16 a[192:195],v[72:75],a[160:163],a[192:195]
s_waitcnt vmcnt(35)
ds_read_b64_tr_b16 v[122:123],v2 offset:25696
v_mfma_f32_16x16x32_bf16 a[192:195],v[76:79],a[176:179],a[192:195]
s_waitcnt vmcnt(27)
v_mfma_f32_16x16x32_bf16 a[196:199],v[76:79],a[180:183],a[196:199]
buffer_load_dwordx4 v27,s[48:51],s83 offen offset:3072 lds
v_mfma_f32_16x16x32_bf16 a[196:199],v[72:75],a[164:167],a[196:199]
s_add_u32 m0,s86,126976
v_mfma_f32_16x16x32_bf16 a[196:199],v[68:71],a[148:151],a[196:199]
s_waitcnt vmcnt(24)
v_mfma_f32_16x16x32_bf16 a[196:199],v[64:67],a[132:135],a[196:199]
v_mfma_f32_16x16x32_bf16 a[200:203],v[64:67],a[136:139],a[200:203]
s_waitcnt vmcnt(21)
v_mfma_f32_16x16x32_bf16 a[200:203],v[68:71],a[152:155],a[200:203]
s_waitcnt vmcnt(15)
v_mfma_f32_16x16x32_bf16 a[200:203],v[72:75],a[168:171],a[200:203]
buffer_load_dwordx4 a[112:115],v28,s[28:31],s22 offen offset:3072 sc1
v_mfma_f32_16x16x32_bf16 a[200:203],v[76:79],a[184:187],a[200:203]
s_waitcnt vmcnt(14)
v_mfma_f32_16x16x32_bf16 a[204:207],v[76:79],a[188:191],a[204:207]
s_waitcnt vmcnt(12)
ds_read_b64_tr_b16 v[76:77],v1 offset:30848
ds_read_b64_tr_b16 v[78:79],v1 offset:35968
v_mfma_f32_16x16x32_bf16 a[204:207],v[72:75],a[172:175],a[204:207]
s_waitcnt vmcnt(9)
ds_read_b64_tr_b16 v[72:73],v1 offset:20608
v_mfma_f32_16x16x32_bf16 a[204:207],v[68:71],a[156:159],a[204:207]
s_waitcnt vmcnt(7)
ds_read_b64_tr_b16 v[68:69],v1 offset:10368
v_mfma_f32_16x16x32_bf16 a[204:207],v[64:67],a[140:143],a[204:207]
ds_read_b64_tr_b16 v[64:65],v1 offset:128
v_mfma_f32_16x16x32_bf16 a[208:211],v[80:83],a[128:131],a[208:211]
ds_read_b64_tr_b16 v[66:67],v1 offset:5248
ds_read_b64_tr_b16 v[70:71],v1 offset:15488
v_mfma_f32_16x16x32_bf16 a[208:211],v[84:87],a[144:147],a[208:211]
buffer_load_dwordx4 v7,s[48:51],s83 offen lds
v_mfma_f32_16x16x32_bf16 a[208:211],v[88:91],a[160:163],a[208:211]
s_add_u32 m0,s86,128000
s_add_u32 s83,s64,1
ds_read_b64_tr_b16 v[74:75],v1 offset:25728
v_mfma_f32_16x16x32_bf16 a[208:211],v[92:95],a[176:179],a[208:211]
s_and_b32 s83,s83,3
s_lshl_b32 s83,s83,1
s_or_b32 s83,s83,1
v_mfma_f32_16x16x32_bf16 a[212:215],v[92:95],a[180:183],a[212:215]
s_mul_i32 s83,s83,163840
s_add_u32 s83,s83,s85
v_mfma_f32_16x16x32_bf16 a[212:215],v[88:91],a[164:167],a[212:215]
v_mfma_f32_16x16x32_bf16 a[212:215],v[84:87],a[148:151],a[212:215]
v_mfma_f32_16x16x32_bf16 a[212:215],v[80:83],a[132:135],a[212:215]
buffer_load_dwordx4 a[104:107],v28,s[28:31],s21 offen offset:3072 sc1
v_mfma_f32_16x16x32_bf16 a[216:219],v[80:83],a[136:139],a[216:219]
v_mfma_f32_16x16x32_bf16 a[216:219],v[84:87],a[152:155],a[216:219]
v_mfma_f32_16x16x32_bf16 a[216:219],v[88:91],a[168:171],a[216:219]
v_mfma_f32_16x16x32_bf16 a[216:219],v[92:95],a[184:187],a[216:219]
v_mfma_f32_16x16x32_bf16 a[220:223],v[92:95],a[188:191],a[220:223]
ds_read_b64_tr_b16 v[92:93],v2 offset:30880
ds_read_b64_tr_b16 v[94:95],v2 offset:36000
v_mfma_f32_16x16x32_bf16 a[220:223],v[88:91],a[172:175],a[220:223]
ds_read_b64_tr_b16 v[88:89],v2 offset:20640
v_mfma_f32_16x16x32_bf16 a[220:223],v[84:87],a[156:159],a[220:223]
buffer_load_dwordx4 v24,s[48:51],s84 offen lds
v_mfma_f32_16x16x32_bf16 a[220:223],v[80:83],a[140:143],a[220:223]
ds_read_b64_tr_b16 v[80:81],v2 offset:160
ds_read_b64_tr_b16 v[82:83],v2 offset:5280
s_barrier
v_mfma_f32_16x16x32_bf16 a[224:227],v[96:99],a[128:131],a[224:227]
ds_read_b64_tr_b16 v[84:85],v2 offset:10400
v_mfma_f32_16x16x32_bf16 a[224:227],v[100:103],a[144:147],a[224:227]
ds_read_b64_tr_b16 v[86:87],v2 offset:15520
v_mfma_f32_16x16x32_bf16 a[224:227],v[104:107],a[160:163],a[224:227]
buffer_store_dwordx2 v[8:9],v10,s[44:47],s18 offen offset:1280 sc1
v_mfma_f32_16x16x32_bf16 a[224:227],v[108:111],a[176:179],a[224:227]
ds_read_b64_tr_b16 v[90:91],v2 offset:25760
v_mfma_f32_16x16x32_bf16 a[228:231],v[108:111],a[180:183],a[228:231]
v_mfma_f32_16x16x32_bf16 a[228:231],v[104:107],a[164:167],a[228:231]
v_mfma_f32_16x16x32_bf16 a[228:231],v[100:103],a[148:151],a[228:231]
v_mfma_f32_16x16x32_bf16 a[228:231],v[96:99],a[132:135],a[228:231]
buffer_load_dwordx4 a[96:99],v28,s[28:31],s20 offen offset:3072 sc1
v_mfma_f32_16x16x32_bf16 a[232:235],v[96:99],a[136:139],a[232:235]
s_add_u32 s20,s64,1
s_and_b32 s20,s20,3
s_lshl_b32 s20,s20,1
v_mfma_f32_16x16x32_bf16 a[232:235],v[100:103],a[152:155],a[232:235]
s_or_b32 s20,s20,1
s_lshl_b32 s20,s20,16
s_add_u32 s21,s20,4096
v_mfma_f32_16x16x32_bf16 a[232:235],v[104:107],a[168:171],a[232:235]
s_add_u32 s22,s20,8192
s_add_u32 s23,s20,12288
v_mfma_f32_16x16x32_bf16 a[232:235],v[108:111],a[184:187],a[232:235]
v_mfma_f32_16x16x32_bf16 a[236:239],v[108:111],a[188:191],a[236:239]
ds_read_b64_tr_b16 v[108:109],v1 offset:30912
ds_read_b64_tr_b16 v[110:111],v1 offset:36032
v_mfma_f32_16x16x32_bf16 a[236:239],v[104:107],a[172:175],a[236:239]
ds_read_b64_tr_b16 v[104:105],v1 offset:20672
v_mfma_f32_16x16x32_bf16 a[236:239],v[100:103],a[156:159],a[236:239]
buffer_load_dwordx4 v25,s[48:51],s84 offen offset:1024 lds
v_mfma_f32_16x16x32_bf16 a[236:239],v[96:99],a[140:143],a[236:239]
s_waitcnt lgkmcnt(3)
ds_read_b64_tr_b16 v[96:97],v1 offset:192
ds_read_b64_tr_b16 v[98:99],v1 offset:5312
v_mfma_f32_16x16x32_bf16 a[240:243],v[112:115],a[128:131],a[240:243]
ds_read_b64_tr_b16 v[100:101],v1 offset:10432
v_mfma_f32_16x16x32_bf16 a[240:243],v[116:119],a[144:147],a[240:243]
ds_read_b64_tr_b16 v[102:103],v1 offset:15552
v_mfma_f32_16x16x32_bf16 a[240:243],v[120:123],a[160:163],a[240:243]
ds_read_b64_tr_b16 v[106:107],v1 offset:25792
v_mfma_f32_16x16x32_bf16 a[240:243],v[124:127],a[176:179],a[240:243]
v_mfma_f32_16x16x32_bf16 a[244:247],v[124:127],a[180:183],a[244:247]
v_mfma_f32_16x16x32_bf16 a[244:247],v[120:123],a[164:167],a[244:247]
buffer_load_dwordx4 v26,s[48:51],s84 offen offset:2048 lds
v_mfma_f32_16x16x32_bf16 a[244:247],v[116:119],a[148:151],a[244:247]
v_mfma_f32_16x16x32_bf16 a[244:247],v[112:115],a[132:135],a[244:247]
v_mfma_f32_16x16x32_bf16 a[248:251],v[112:115],a[136:139],a[248:251]
v_mfma_f32_16x16x32_bf16 a[248:251],v[116:119],a[152:155],a[248:251]
v_mfma_f32_16x16x32_bf16 a[248:251],v[120:123],a[168:171],a[248:251]
v_mfma_f32_16x16x32_bf16 a[248:251],v[124:127],a[184:187],a[248:251]
v_mfma_f32_16x16x32_bf16 a[252:255],v[124:127],a[188:191],a[252:255]
buffer_load_dwordx4 v27,s[48:51],s84 offen offset:3072 lds
v_mfma_f32_16x16x32_bf16 a[252:255],v[120:123],a[172:175],a[252:255]
s_add_u32 m0,s86,132096
ds_read_b64_tr_b16 v[120:121],v2 offset:20704
ds_read_b64_tr_b16 v[122:123],v2 offset:25824
v_mfma_f32_16x16x32_bf16 a[252:255],v[116:119],a[156:159],a[252:255]
ds_read_b64_tr_b16 v[116:117],v2 offset:10464
v_mfma_f32_16x16x32_bf16 a[252:255],v[112:115],a[140:143],a[252:255]
ds_read_b64_tr_b16 v[112:113],v2 offset:224
v_mfma_f32_16x16x32_bf16 a[0:3],v[64:67],a[128:131],a[0:3]
ds_read_b64_tr_b16 v[114:115],v2 offset:5344
v_mfma_f32_16x16x32_bf16 a[0:3],v[68:71],a[144:147],a[0:3]
ds_read_b64_tr_b16 v[118:119],v2 offset:15584
ds_read_b64_tr_b16 v[124:125],v2 offset:30944
v_mfma_f32_16x16x32_bf16 a[0:3],v[72:75],a[160:163],a[0:3]
ds_read_b64_tr_b16 v[126:127],v2 offset:36064
v_mfma_f32_16x16x32_bf16 a[0:3],v[76:79],a[176:179],a[0:3]
buffer_load_dwordx4 v7,s[48:51],s84 offen lds
v_mfma_f32_16x16x32_bf16 a[4:7],v[76:79],a[180:183],a[4:7]
s_add_u32 m0,s86,81920
s_add_u32 s84,s83,20480
v_mfma_f32_16x16x32_bf16 a[4:7],v[72:75],a[164:167],a[4:7]
v_mfma_f32_16x16x32_bf16 a[4:7],v[68:71],a[148:151],a[4:7]
v_mfma_f32_16x16x32_bf16 a[4:7],v[64:67],a[132:135],a[4:7]
v_mfma_f32_16x16x32_bf16 a[8:11],v[64:67],a[136:139],a[8:11]
v_mfma_f32_16x16x32_bf16 a[8:11],v[68:71],a[152:155],a[8:11]
v_mfma_f32_16x16x32_bf16 a[8:11],v[72:75],a[168:171],a[8:11]
buffer_load_dwordx4 v24,s[48:51],s83 offen lds
v_mfma_f32_16x16x32_bf16 a[8:11],v[76:79],a[184:187],a[8:11]
v_mfma_f32_16x16x32_bf16 a[12:15],v[76:79],a[188:191],a[12:15]
ds_read_b64_tr_b16 v[76:77],v1 offset:30976
ds_read_b64_tr_b16 v[78:79],v1 offset:36096
v_mfma_f32_16x16x32_bf16 a[12:15],v[72:75],a[172:175],a[12:15]
ds_read_b64_tr_b16 v[72:73],v1 offset:20736
v_mfma_f32_16x16x32_bf16 a[12:15],v[68:71],a[156:159],a[12:15]
ds_read_b64_tr_b16 v[68:69],v1 offset:10496
v_mfma_f32_16x16x32_bf16 a[12:15],v[64:67],a[140:143],a[12:15]
ds_read_b64_tr_b16 v[64:65],v1 offset:256
v_mfma_f32_16x16x32_bf16 a[16:19],v[80:83],a[128:131],a[16:19]
ds_read_b64_tr_b16 v[66:67],v1 offset:5376
ds_read_b64_tr_b16 v[70:71],v1 offset:15616
v_mfma_f32_16x16x32_bf16 a[16:19],v[84:87],a[144:147],a[16:19]
buffer_load_dwordx4 v25,s[48:51],s83 offen offset:1024 lds
v_mfma_f32_16x16x32_bf16 a[16:19],v[88:91],a[160:163],a[16:19]
ds_read_b64_tr_b16 v[74:75],v1 offset:25856
v_add_u32 v1,0xffff6000,v1
v_mfma_f32_16x16x32_bf16 a[16:19],v[92:95],a[176:179],a[16:19]
v_mfma_f32_16x16x32_bf16 a[20:23],v[92:95],a[180:183],a[20:23]
)A"
R"A(v_mfma_f32_16x16x32_bf16 a[20:23],v[88:91],a[164:167],a[20:23]
v_mfma_f32_16x16x32_bf16 a[20:23],v[84:87],a[148:151],a[20:23]
v_mfma_f32_16x16x32_bf16 a[20:23],v[80:83],a[132:135],a[20:23]
v_mfma_f32_16x16x32_bf16 a[24:27],v[80:83],a[136:139],a[24:27]
buffer_load_dwordx4 v26,s[48:51],s83 offen offset:2048 lds
v_mfma_f32_16x16x32_bf16 a[24:27],v[84:87],a[152:155],a[24:27]
v_mfma_f32_16x16x32_bf16 a[24:27],v[88:91],a[168:171],a[24:27]
v_mfma_f32_16x16x32_bf16 a[24:27],v[92:95],a[184:187],a[24:27]
v_mfma_f32_16x16x32_bf16 a[28:31],v[92:95],a[188:191],a[28:31]
ds_read_b64_tr_b16 v[92:93],v2 offset:31008
ds_read_b64_tr_b16 v[94:95],v2 offset:36128
v_mfma_f32_16x16x32_bf16 a[28:31],v[88:91],a[172:175],a[28:31]
ds_read_b64_tr_b16 v[88:89],v2 offset:20768
v_mfma_f32_16x16x32_bf16 a[28:31],v[84:87],a[156:159],a[28:31]
s_waitcnt lgkmcnt(3)
ds_read_b64_tr_b16 v[84:85],v2 offset:10528
v_mfma_f32_16x16x32_bf16 a[28:31],v[80:83],a[140:143],a[28:31]
buffer_load_dwordx4 v27,s[48:51],s83 offen offset:3072 lds
v_mfma_f32_16x16x32_bf16 v[128:131],v[96:99],a[128:131],v[128:131]
s_add_u32 m0,s86,86016
ds_read_b64_tr_b16 v[80:81],v2 offset:288
ds_read_b64_tr_b16 v[82:83],v2 offset:5408
v_mfma_f32_16x16x32_bf16 v[128:131],v[100:103],a[144:147],v[128:131]
ds_read_b64_tr_b16 v[86:87],v2 offset:15648
v_mfma_f32_16x16x32_bf16 v[128:131],v[104:107],a[160:163],v[128:131]
ds_read_b64_tr_b16 v[90:91],v2 offset:25888
v_add_u32 v2,0xffff6000,v2
v_mfma_f32_16x16x32_bf16 v[128:131],v[108:111],a[176:179],v[128:131]
v_mfma_f32_16x16x32_bf16 v[136:139],v[108:111],a[180:183],v[136:139]
v_mfma_f32_16x16x32_bf16 v[136:139],v[104:107],a[164:167],v[136:139]
v_mfma_f32_16x16x32_bf16 v[136:139],v[100:103],a[148:151],v[136:139]
buffer_load_dwordx4 v7,s[48:51],s83 offen lds
v_mfma_f32_16x16x32_bf16 v[136:139],v[96:99],a[132:135],v[136:139]
s_add_u32 m0,s86,87040
s_add_u32 s83,s64,2
s_and_b32 s83,s83,3
v_mfma_f32_16x16x32_bf16 v[144:147],v[96:99],a[136:139],v[144:147]
s_lshl_b32 s83,s83,1
s_or_b32 s83,s83,1
s_mul_i32 s83,s83,163840
v_mfma_f32_16x16x32_bf16 v[144:147],v[100:103],a[152:155],v[144:147]
s_add_u32 s83,s83,s85
v_mfma_f32_16x16x32_bf16 v[144:147],v[104:107],a[168:171],v[144:147]
v_mfma_f32_16x16x32_bf16 v[144:147],v[108:111],a[184:187],v[144:147]
v_mfma_f32_16x16x32_bf16 v[152:155],v[108:111],a[188:191],v[152:155]
ds_read_b64_tr_b16 v[108:109],v1 offset:30720
ds_read_b64_tr_b16 v[110:111],v1 offset:35840
v_mfma_f32_16x16x32_bf16 v[152:155],v[104:107],a[172:175],v[152:155]
buffer_load_dwordx4 v24,s[48:51],s84 offen lds
v_mfma_f32_16x16x32_bf16 v[152:155],v[100:103],a[156:159],v[152:155]
ds_read_b64_tr_b16 v[100:101],v1 offset:10240
ds_read_b64_tr_b16 v[102:103],v1 offset:15360
v_mfma_f32_16x16x32_bf16 v[152:155],v[96:99],a[140:143],v[152:155]
ds_read_b64_tr_b16 v[96:97],v1
s_waitcnt lgkmcnt(5)
s_barrier
v_mfma_f32_16x16x32_bf16 v[160:163],v[112:115],a[128:131],v[160:163]
ds_read_b64_tr_b16 v[98:99],v1 offset:5120
v_mfma_f32_16x16x32_bf16 v[160:163],v[116:119],a[144:147],v[160:163]
ds_read_b64_tr_b16 v[104:105],v1 offset:20480
v_mfma_f32_16x16x32_bf16 v[160:163],v[120:123],a[160:163],v[160:163]
ds_read_b64_tr_b16 v[106:107],v1 offset:25600
v_mfma_f32_16x16x32_bf16 v[160:163],v[124:127],a[176:179],v[160:163]
buffer_load_dwordx4 v25,s[48:51],s84 offen offset:1024 lds
v_mfma_f32_16x16x32_bf16 v[168:171],v[124:127],a[180:183],v[168:171]
v_mfma_f32_16x16x32_bf16 v[168:171],v[120:123],a[164:167],v[168:171]
v_mfma_f32_16x16x32_bf16 v[168:171],v[116:119],a[148:151],v[168:171]
v_mfma_f32_16x16x32_bf16 v[168:171],v[112:115],a[132:135],v[168:171]
v_mfma_f32_16x16x32_bf16 v[176:179],v[112:115],a[136:139],v[176:179]
v_mfma_f32_16x16x32_bf16 v[176:179],v[116:119],a[152:155],v[176:179]
v_mfma_f32_16x16x32_bf16 v[176:179],v[120:123],a[168:171],v[176:179]
buffer_load_dwordx4 v26,s[48:51],s84 offen offset:2048 lds
v_mfma_f32_16x16x32_bf16 v[176:179],v[124:127],a[184:187],v[176:179]
v_mfma_f32_16x16x32_bf16 v[184:187],v[124:127],a[188:191],v[184:187]
ds_read_b64_tr_b16 v[124:125],v2 offset:30752
ds_read_b64_tr_b16 v[126:127],v2 offset:35872
v_mfma_f32_16x16x32_bf16 v[184:187],v[120:123],a[172:175],v[184:187]
ds_read_b64_tr_b16 v[120:121],v2 offset:20512
v_mfma_f32_16x16x32_bf16 v[184:187],v[116:119],a[156:159],v[184:187]
ds_read_b64_tr_b16 v[116:117],v2 offset:10272
v_mfma_f32_16x16x32_bf16 v[184:187],v[112:115],a[140:143],v[184:187]
ds_read_b64_tr_b16 v[112:113],v2 offset:32
v_mfma_f32_16x16x32_bf16 v[192:195],v[64:67],a[128:131],v[192:195]
ds_read_b64_tr_b16 v[114:115],v2 offset:5152
ds_read_b64_tr_b16 v[118:119],v2 offset:15392
v_mfma_f32_16x16x32_bf16 v[192:195],v[68:71],a[144:147],v[192:195]
buffer_load_dwordx4 v27,s[48:51],s84 offen offset:3072 lds
v_mfma_f32_16x16x32_bf16 v[192:195],v[72:75],a[160:163],v[192:195]
s_add_u32 m0,s86,91136
ds_read_b64_tr_b16 v[122:123],v2 offset:25632
v_mfma_f32_16x16x32_bf16 v[192:195],v[76:79],a[176:179],v[192:195]
v_mfma_f32_16x16x32_bf16 v[200:203],v[76:79],a[180:183],v[200:203]
v_mfma_f32_16x16x32_bf16 v[200:203],v[72:75],a[164:167],v[200:203]
v_mfma_f32_16x16x32_bf16 v[200:203],v[68:71],a[148:151],v[200:203]
v_mfma_f32_16x16x32_bf16 v[200:203],v[64:67],a[132:135],v[200:203]
v_mfma_f32_16x16x32_bf16 v[208:211],v[64:67],a[136:139],v[208:211]
buffer_load_dwordx4 v7,s[48:51],s84 offen lds
v_mfma_f32_16x16x32_bf16 v[208:211],v[68:71],a[152:155],v[208:211]
s_add_u32 m0,s86,40960
s_add_u32 s84,s83,20480
v_mfma_f32_16x16x32_bf16 v[208:211],v[72:75],a[168:171],v[208:211]
v_mfma_f32_16x16x32_bf16 v[208:211],v[76:79],a[184:187],v[208:211]
v_mfma_f32_16x16x32_bf16 v[216:219],v[76:79],a[188:191],v[216:219]
ds_read_b64_tr_b16 v[76:77],v1 offset:30784
ds_read_b64_tr_b16 v[78:79],v1 offset:35904
v_mfma_f32_16x16x32_bf16 v[216:219],v[72:75],a[172:175],v[216:219]
buffer_load_dwordx2 v[4:5],v3,s[44:47],s91 offen offset:1280 sc1
v_mfma_f32_16x16x32_bf16 v[216:219],v[68:71],a[156:159],v[216:219]
ds_read_b64_tr_b16 v[68:69],v1 offset:10304
ds_read_b64_tr_b16 v[70:71],v1 offset:15424
v_mfma_f32_16x16x32_bf16 v[216:219],v[64:67],a[140:143],v[216:219]
ds_read_b64_tr_b16 v[64:65],v1 offset:64
v_mfma_f32_16x16x32_bf16 v[224:227],v[80:83],a[128:131],v[224:227]
ds_read_b64_tr_b16 v[66:67],v1 offset:5184
v_mfma_f32_16x16x32_bf16 v[224:227],v[84:87],a[144:147],v[224:227]
ds_read_b64_tr_b16 v[72:73],v1 offset:20544
v_mfma_f32_16x16x32_bf16 v[224:227],v[88:91],a[160:163],v[224:227]
buffer_load_dwordx4 v[32:35],v6,s[28:31],s87 offen sc1
v_mfma_f32_16x16x32_bf16 v[224:227],v[92:95],a[176:179],v[224:227]
ds_read_b64_tr_b16 v[74:75],v1 offset:25664
v_mfma_f32_16x16x32_bf16 v[232:235],v[92:95],a[180:183],v[232:235]
v_mfma_f32_16x16x32_bf16 v[232:235],v[88:91],a[164:167],v[232:235]
v_mfma_f32_16x16x32_bf16 v[232:235],v[84:87],a[148:151],v[232:235]
v_mfma_f32_16x16x32_bf16 v[232:235],v[80:83],a[132:135],v[232:235]
buffer_load_dwordx4 v[36:39],v6,s[28:31],s88 offen sc1
v_mfma_f32_16x16x32_bf16 v[240:243],v[80:83],a[136:139],v[240:243]
v_mfma_f32_16x16x32_bf16 v[240:243],v[84:87],a[152:155],v[240:243]
v_mfma_f32_16x16x32_bf16 v[240:243],v[88:91],a[168:171],v[240:243]
v_mfma_f32_16x16x32_bf16 v[240:243],v[92:95],a[184:187],v[240:243]
v_mfma_f32_16x16x32_bf16 v[248:251],v[92:95],a[188:191],v[248:251]
buffer_load_dwordx4 v[40:43],v6,s[28:31],s89 offen sc1
v_mfma_f32_16x16x32_bf16 v[248:251],v[88:91],a[172:175],v[248:251]
ds_read_b64_tr_b16 v[88:89],v2 offset:20576
ds_read_b64_tr_b16 v[90:91],v2 offset:25696
v_mfma_f32_16x16x32_bf16 v[248:251],v[84:87],a[156:159],v[248:251]
ds_read_b64_tr_b16 v[84:85],v2 offset:10336
v_mfma_f32_16x16x32_bf16 v[248:251],v[80:83],a[140:143],v[248:251]
s_waitcnt lgkmcnt(3)
ds_read_b64_tr_b16 v[80:81],v2 offset:96
v_mfma_f32_16x16x32_bf16 a[192:195],v[96:99],a[68:71],a[192:195]
ds_read_b64_tr_b16 v[82:83],v2 offset:5216
v_mfma_f32_16x16x32_bf16 a[192:195],v[100:103],a[76:79],a[192:195]
s_nop 0
L_pd15_%=:
s_cbranch_execz L_trt1_%=
buffer_load_dwordx4 v[44:47],v6,s[28:31],s90 offen sc1
v_mfma_f32_16x16x32_bf16 a[192:195],v[104:107],a[84:87],a[192:195]
ds_read_b64_tr_b16 v[86:87],v2 offset:15456
ds_read_b64_tr_b16 v[92:93],v2 offset:30816
v_mfma_f32_16x16x32_bf16 a[192:195],v[108:111],a[92:95],a[192:195]
ds_read_b64_tr_b16 v[94:95],v2 offset:35936
v_mfma_f32_16x16x32_bf16 a[196:199],v[108:111],a[124:127],a[196:199]
v_mfma_f32_16x16x32_bf16 a[196:199],v[104:107],a[116:119],a[196:199]
v_mfma_f32_16x16x32_bf16 a[196:199],v[100:103],a[108:111],a[196:199]
buffer_load_dwordx4 v[180:183],v6,s[28:31],s90 offen offset:1024 sc1
v_mfma_f32_16x16x32_bf16 a[196:199],v[96:99],a[100:103],a[196:199]
v_mfma_f32_16x16x32_bf16 a[200:203],v[96:99],a[64:67],a[200:203]
v_mfma_f32_16x16x32_bf16 a[200:203],v[100:103],a[72:75],a[200:203]
s_waitcnt vmcnt(31)
v_mfma_f32_16x16x32_bf16 a[200:203],v[104:107],a[80:83],a[200:203]
s_waitcnt vmcnt(27)
v_mfma_f32_16x16x32_bf16 a[200:203],v[108:111],a[88:91],a[200:203]
buffer_load_dwordx4 v[212:215],v6,s[28:31],s88 offen offset:2048 sc1
v_mfma_f32_16x16x32_bf16 a[204:207],v[108:111],a[120:123],a[204:207]
s_waitcnt vmcnt(26)
ds_read_b64_tr_b16 v[108:109],v1 offset:30848
ds_read_b64_tr_b16 v[110:111],v1 offset:35968
v_mfma_f32_16x16x32_bf16 a[204:207],v[104:107],a[112:115],a[204:207]
s_waitcnt vmcnt(24)
ds_read_b64_tr_b16 v[104:105],v1 offset:20608
v_mfma_f32_16x16x32_bf16 a[204:207],v[100:103],a[104:107],a[204:207]
s_waitcnt vmcnt(21)
ds_read_b64_tr_b16 v[100:101],v1 offset:10368
v_mfma_f32_16x16x32_bf16 a[204:207],v[96:99],a[96:99],a[204:207]
ds_read_b64_tr_b16 v[96:97],v1 offset:128
v_mfma_f32_16x16x32_bf16 a[208:211],v[112:115],a[68:71],a[208:211]
buffer_load_dwordx4 v[164:167],v6,s[28:31],s89 offen offset:1024 sc1
v_mfma_f32_16x16x32_bf16 a[208:211],v[116:119],a[76:79],a[208:211]
ds_read_b64_tr_b16 v[98:99],v1 offset:5248
ds_read_b64_tr_b16 v[102:103],v1 offset:15488
v_mfma_f32_16x16x32_bf16 a[208:211],v[120:123],a[84:87],a[208:211]
ds_read_b64_tr_b16 v[106:107],v1 offset:25728
v_mfma_f32_16x16x32_bf16 a[208:211],v[124:127],a[92:95],a[208:211]
v_mfma_f32_16x16x32_bf16 a[212:215],v[124:127],a[124:127],a[212:215]
v_mfma_f32_16x16x32_bf16 a[212:215],v[120:123],a[116:119],a[212:215]
buffer_load_dwordx4 v[228:231],v6,s[28:31],s89 offen offset:2048 sc1
v_mfma_f32_16x16x32_bf16 a[212:215],v[116:119],a[108:111],a[212:215]
v_mfma_f32_16x16x32_bf16 a[212:215],v[112:115],a[100:103],a[212:215]
v_mfma_f32_16x16x32_bf16 a[216:219],v[112:115],a[64:67],a[216:219]
v_mfma_f32_16x16x32_bf16 a[216:219],v[116:119],a[72:75],a[216:219]
v_mfma_f32_16x16x32_bf16 a[216:219],v[120:123],a[80:83],a[216:219]
buffer_load_dwordx4 v[148:151],v6,s[28:31],s88 offen offset:1024 sc1
v_mfma_f32_16x16x32_bf16 a[216:219],v[124:127],a[88:91],a[216:219]
v_mfma_f32_16x16x32_bf16 a[220:223],v[124:127],a[120:123],a[220:223]
ds_read_b64_tr_b16 v[124:125],v2 offset:30880
ds_read_b64_tr_b16 v[126:127],v2 offset:36000
v_mfma_f32_16x16x32_bf16 a[220:223],v[120:123],a[112:115],a[220:223]
ds_read_b64_tr_b16 v[120:121],v2 offset:20640
v_mfma_f32_16x16x32_bf16 a[220:223],v[116:119],a[104:107],a[220:223]
ds_read_b64_tr_b16 v[116:117],v2 offset:10400
v_mfma_f32_16x16x32_bf16 a[220:223],v[112:115],a[96:99],a[220:223]
buffer_load_dwordx4 v[132:135],v6,s[28:31],s87 offen offset:1024 sc1
v_mfma_f32_16x16x32_bf16 a[224:227],v[64:67],a[68:71],a[224:227]
ds_read_b64_tr_b16 v[112:113],v2 offset:160
ds_read_b64_tr_b16 v[114:115],v2 offset:5280
v_mfma_f32_16x16x32_bf16 a[224:227],v[68:71],a[76:79],a[224:227]
ds_read_b64_tr_b16 v[118:119],v2 offset:15520
v_mfma_f32_16x16x32_bf16 a[224:227],v[72:75],a[84:87],a[224:227]
ds_read_b64_tr_b16 v[122:123],v2 offset:25760
v_mfma_f32_16x16x32_bf16 a[224:227],v[76:79],a[92:95],a[224:227]
v_mfma_f32_16x16x32_bf16 a[228:231],v[76:79],a[124:127],a[228:231]
buffer_load_dwordx4 v[244:247],v6,s[28:31],s90 offen offset:2048 sc1
v_mfma_f32_16x16x32_bf16 a[228:231],v[72:75],a[116:119],a[228:231]
v_mfma_f32_16x16x32_bf16 a[228:231],v[68:71],a[108:111],a[228:231]
v_mfma_f32_16x16x32_bf16 a[228:231],v[64:67],a[100:103],a[228:231]
v_mfma_f32_16x16x32_bf16 a[232:235],v[64:67],a[64:67],a[232:235]
v_mfma_f32_16x16x32_bf16 a[232:235],v[68:71],a[72:75],a[232:235]
)A"
R"A(buffer_load_dwordx4 v[188:191],v28,s[28:31],s90 offen offset:3072 sc1
v_mfma_f32_16x16x32_bf16 a[232:235],v[72:75],a[80:83],a[232:235]
s_waitcnt vmcnt(12)
v_cmp_ne_u32 s[56:57],s52,v4
v_cmp_ne_u32 s[58:59],s53,v5
v_mfma_f32_16x16x32_bf16 a[232:235],v[76:79],a[88:91],a[232:235]
s_or_b64 s[56:57],s[56:57],s[58:59]
s_cbranch_scc1 L_fr2_%=
L_fo2_%=:
v_mov_b32 v3,0
v_mfma_f32_16x16x32_bf16 a[236:239],v[76:79],a[120:123],a[236:239]
s_add_u32 s91,s64,3
ds_read_b64_tr_b16 v[76:77],v1 offset:30912
ds_read_b64_tr_b16 v[78:79],v1 offset:36032
v_mfma_f32_16x16x32_bf16 a[236:239],v[72:75],a[112:115],a[236:239]
s_and_b32 s91,s91,3
s_add_u32 s91,s91,s69
ds_read_b64_tr_b16 v[72:73],v1 offset:20672
v_mfma_f32_16x16x32_bf16 a[236:239],v[68:71],a[104:107],a[236:239]
buffer_load_dwordx4 a[128:131],v6,s[28:31],s20 offen sc1
v_mfma_f32_16x16x32_bf16 a[236:239],v[64:67],a[96:99],a[236:239]
s_add_u32 s91,s91,8
ds_read_b64_tr_b16 v[64:65],v1 offset:192
ds_read_b64_tr_b16 v[66:67],v1 offset:5312
s_waitcnt lgkmcnt(5)
v_mfma_f32_16x16x32_bf16 a[240:243],v[80:83],a[68:71],a[240:243]
s_lshl_b32 s91,s91,6
s_and_b32 s69,s63,s60
ds_read_b64_tr_b16 v[68:69],v1 offset:10432
v_mfma_f32_16x16x32_bf16 a[240:243],v[84:87],a[76:79],a[240:243]
s_cmp_eq_u32 s69,3
s_cselect_b32 s69,0x40000000,0
ds_read_b64_tr_b16 v[70:71],v1 offset:15552
v_mfma_f32_16x16x32_bf16 a[240:243],v[88:91],a[84:87],a[240:243]
s_add_u32 s68,s68,s69
ds_read_b64_tr_b16 v[74:75],v1 offset:25792
v_mfma_f32_16x16x32_bf16 a[240:243],v[92:95],a[92:95],a[240:243]
buffer_load_dwordx4 v[196:199],v6,s[28:31],s87 offen offset:2048 sc1
v_mfma_f32_16x16x32_bf16 a[244:247],v[92:95],a[124:127],a[244:247]
v_mfma_f32_16x16x32_bf16 a[244:247],v[88:91],a[116:119],a[244:247]
v_mfma_f32_16x16x32_bf16 a[244:247],v[84:87],a[108:111],a[244:247]
v_mfma_f32_16x16x32_bf16 a[244:247],v[80:83],a[100:103],a[244:247]
v_mfma_f32_16x16x32_bf16 a[248:251],v[80:83],a[64:67],a[248:251]
buffer_load_dwordx4 v[172:175],v28,s[28:31],s89 offen offset:3072 sc1
v_mfma_f32_16x16x32_bf16 a[248:251],v[84:87],a[72:75],a[248:251]
v_mfma_f32_16x16x32_bf16 a[248:251],v[88:91],a[80:83],a[248:251]
v_mfma_f32_16x16x32_bf16 a[248:251],v[92:95],a[88:91],a[248:251]
v_mfma_f32_16x16x32_bf16 a[252:255],v[92:95],a[120:123],a[252:255]
ds_read_b64_tr_b16 v[92:93],v2 offset:30944
ds_read_b64_tr_b16 v[94:95],v2 offset:36064
v_mfma_f32_16x16x32_bf16 a[252:255],v[88:91],a[112:115],a[252:255]
buffer_load_dwordx4 a[144:147],v6,s[28:31],s21 offen sc1
v_mfma_f32_16x16x32_bf16 a[252:255],v[84:87],a[104:107],a[252:255]
ds_read_b64_tr_b16 v[84:85],v2 offset:10464
ds_read_b64_tr_b16 v[86:87],v2 offset:15584
v_mfma_f32_16x16x32_bf16 a[252:255],v[80:83],a[96:99],a[252:255]
ds_read_b64_tr_b16 v[80:81],v2 offset:224
v_mfma_f32_16x16x32_bf16 a[0:3],v[96:99],a[68:71],a[0:3]
ds_read_b64_tr_b16 v[82:83],v2 offset:5344
v_mfma_f32_16x16x32_bf16 a[0:3],v[100:103],a[76:79],a[0:3]
ds_read_b64_tr_b16 v[88:89],v2 offset:20704
v_mfma_f32_16x16x32_bf16 a[0:3],v[104:107],a[84:87],a[0:3]
buffer_load_dwordx4 v[156:159],v28,s[28:31],s88 offen offset:3072 sc1
v_mfma_f32_16x16x32_bf16 a[0:3],v[108:111],a[92:95],a[0:3]
ds_read_b64_tr_b16 v[90:91],v2 offset:25824
v_mfma_f32_16x16x32_bf16 a[4:7],v[108:111],a[124:127],a[4:7]
v_mfma_f32_16x16x32_bf16 a[4:7],v[104:107],a[116:119],a[4:7]
v_mfma_f32_16x16x32_bf16 a[4:7],v[100:103],a[108:111],a[4:7]
v_mfma_f32_16x16x32_bf16 a[4:7],v[96:99],a[100:103],a[4:7]
buffer_load_dwordx4 a[160:163],v6,s[28:31],s22 offen sc1
v_mfma_f32_16x16x32_bf16 a[8:11],v[96:99],a[64:67],a[8:11]
v_mfma_f32_16x16x32_bf16 a[8:11],v[100:103],a[72:75],a[8:11]
v_mfma_f32_16x16x32_bf16 a[8:11],v[104:107],a[80:83],a[8:11]
v_mfma_f32_16x16x32_bf16 a[8:11],v[108:111],a[88:91],a[8:11]
v_mfma_f32_16x16x32_bf16 a[12:15],v[108:111],a[120:123],a[12:15]
buffer_load_dwordx4 v[140:143],v28,s[28:31],s87 offen offset:3072 sc1
v_mfma_f32_16x16x32_bf16 a[12:15],v[104:107],a[112:115],a[12:15]
s_add_u32 s87,s64,2
ds_read_b64_tr_b16 v[104:105],v1 offset:20736
ds_read_b64_tr_b16 v[106:107],v1 offset:25856
v_mfma_f32_16x16x32_bf16 a[12:15],v[100:103],a[104:107],a[12:15]
s_and_b32 s87,s87,3
s_lshl_b32 s87,s87,1
ds_read_b64_tr_b16 v[100:101],v1 offset:10496
v_mfma_f32_16x16x32_bf16 a[12:15],v[96:99],a[96:99],a[12:15]
s_or_b32 s87,s87,1
s_lshl_b32 s87,s87,16
ds_read_b64_tr_b16 v[96:97],v1 offset:256
v_mfma_f32_16x16x32_bf16 a[16:19],v[112:115],a[68:71],a[16:19]
s_add_u32 s88,s87,4096
s_add_u32 s89,s87,8192
ds_read_b64_tr_b16 v[98:99],v1 offset:5376
v_mfma_f32_16x16x32_bf16 a[16:19],v[116:119],a[76:79],a[16:19]
s_add_u32 s90,s87,12288
ds_read_b64_tr_b16 v[102:103],v1 offset:15616
ds_read_b64_tr_b16 v[108:109],v1 offset:30976
v_mfma_f32_16x16x32_bf16 a[16:19],v[120:123],a[84:87],a[16:19]
ds_read_b64_tr_b16 v[110:111],v1 offset:36096
v_add_u32 v1,0x1e000,v1
v_mfma_f32_16x16x32_bf16 a[16:19],v[124:127],a[92:95],a[16:19]
buffer_load_dwordx4 v24,s[48:51],s83 offen lds
v_mfma_f32_16x16x32_bf16 a[20:23],v[124:127],a[124:127],a[20:23]
v_mfma_f32_16x16x32_bf16 a[20:23],v[120:123],a[116:119],a[20:23]
v_mfma_f32_16x16x32_bf16 a[20:23],v[116:119],a[108:111],a[20:23]
v_mfma_f32_16x16x32_bf16 a[20:23],v[112:115],a[100:103],a[20:23]
v_mfma_f32_16x16x32_bf16 a[24:27],v[112:115],a[64:67],a[24:27]
buffer_load_dwordx4 a[176:179],v6,s[28:31],s23 offen sc1
v_mfma_f32_16x16x32_bf16 a[24:27],v[116:119],a[72:75],a[24:27]
v_mfma_f32_16x16x32_bf16 a[24:27],v[120:123],a[80:83],a[24:27]
v_mfma_f32_16x16x32_bf16 a[24:27],v[124:127],a[88:91],a[24:27]
v_mfma_f32_16x16x32_bf16 a[28:31],v[124:127],a[120:123],a[28:31]
ds_read_b64_tr_b16 v[124:125],v2 offset:31008
ds_read_b64_tr_b16 v[126:127],v2 offset:36128
v_mfma_f32_16x16x32_bf16 a[28:31],v[120:123],a[112:115],a[28:31]
ds_read_b64_tr_b16 v[120:121],v2 offset:20768
v_mfma_f32_16x16x32_bf16 a[28:31],v[116:119],a[104:107],a[28:31]
s_waitcnt lgkmcnt(3)
ds_read_b64_tr_b16 v[116:117],v2 offset:10528
v_mfma_f32_16x16x32_bf16 a[28:31],v[112:115],a[96:99],a[28:31]
buffer_load_dwordx4 v25,s[48:51],s83 offen offset:1024 lds
v_mfma_f32_16x16x32_bf16 v[128:131],v[64:67],a[68:71],v[128:131]
ds_read_b64_tr_b16 v[112:113],v2 offset:288
ds_read_b64_tr_b16 v[114:115],v2 offset:5408
v_mfma_f32_16x16x32_bf16 v[128:131],v[68:71],a[76:79],v[128:131]
ds_read_b64_tr_b16 v[118:119],v2 offset:15648
v_mfma_f32_16x16x32_bf16 v[128:131],v[72:75],a[84:87],v[128:131]
ds_read_b64_tr_b16 v[122:123],v2 offset:25888
v_add_u32 v2,0x1e000,v2
v_mfma_f32_16x16x32_bf16 v[128:131],v[76:79],a[92:95],v[128:131]
v_mfma_f32_16x16x32_bf16 v[136:139],v[76:79],a[124:127],v[136:139]
buffer_load_dwordx4 a[180:183],v6,s[28:31],s23 offen offset:1024 sc1
v_mfma_f32_16x16x32_bf16 v[136:139],v[72:75],a[116:119],v[136:139]
v_mfma_f32_16x16x32_bf16 v[136:139],v[68:71],a[108:111],v[136:139]
v_mfma_f32_16x16x32_bf16 v[136:139],v[64:67],a[100:103],v[136:139]
v_mfma_f32_16x16x32_bf16 v[144:147],v[64:67],a[64:67],v[144:147]
v_mfma_f32_16x16x32_bf16 v[144:147],v[68:71],a[72:75],v[144:147]
v_mfma_f32_16x16x32_bf16 v[144:147],v[72:75],a[80:83],v[144:147]
v_mfma_f32_16x16x32_bf16 v[144:147],v[76:79],a[88:91],v[144:147]
buffer_load_dwordx4 v26,s[48:51],s83 offen offset:2048 lds
v_mfma_f32_16x16x32_bf16 v[152:155],v[76:79],a[120:123],v[152:155]
v_mfma_f32_16x16x32_bf16 v[152:155],v[72:75],a[112:115],v[152:155]
v_mfma_f32_16x16x32_bf16 v[152:155],v[68:71],a[104:107],v[152:155]
v_mfma_f32_16x16x32_bf16 v[152:155],v[64:67],a[96:99],v[152:155]
s_waitcnt lgkmcnt(0)
s_barrier
v_mfma_f32_16x16x32_bf16 v[160:163],v[80:83],a[68:71],v[160:163]
buffer_load_dwordx4 a[164:167],v6,s[28:31],s22 offen offset:1024 sc1
v_mfma_f32_16x16x32_bf16 v[160:163],v[84:87],a[76:79],v[160:163]
ds_read_b64_tr_b16 v[64:65],v1
ds_read_b64_tr_b16 v[66:67],v1 offset:5120
v_mfma_f32_16x16x32_bf16 v[160:163],v[88:91],a[84:87],v[160:163]
ds_read_b64_tr_b16 v[68:69],v1 offset:10240
v_mfma_f32_16x16x32_bf16 v[160:163],v[92:95],a[92:95],v[160:163]
ds_read_b64_tr_b16 v[70:71],v1 offset:15360
v_mfma_f32_16x16x32_bf16 v[168:171],v[92:95],a[124:127],v[168:171]
ds_read_b64_tr_b16 v[72:73],v1 offset:20480
v_mfma_f32_16x16x32_bf16 v[168:171],v[88:91],a[116:119],v[168:171]
ds_read_b64_tr_b16 v[74:75],v1 offset:25600
ds_read_b64_tr_b16 v[76:77],v1 offset:30720
v_mfma_f32_16x16x32_bf16 v[168:171],v[84:87],a[108:111],v[168:171]
ds_read_b64_tr_b16 v[78:79],v1 offset:35840
v_mfma_f32_16x16x32_bf16 v[168:171],v[80:83],a[100:103],v[168:171]
buffer_load_dwordx4 v27,s[48:51],s83 offen offset:3072 lds
v_mfma_f32_16x16x32_bf16 v[176:179],v[80:83],a[64:67],v[176:179]
s_add_u32 m0,s86,45056
v_mfma_f32_16x16x32_bf16 v[176:179],v[84:87],a[72:75],v[176:179]
v_mfma_f32_16x16x32_bf16 v[176:179],v[88:91],a[80:83],v[176:179]
v_mfma_f32_16x16x32_bf16 v[176:179],v[92:95],a[88:91],v[176:179]
v_mfma_f32_16x16x32_bf16 v[184:187],v[92:95],a[120:123],v[184:187]
buffer_load_dwordx4 a[148:151],v6,s[28:31],s21 offen offset:1024 sc1
v_mfma_f32_16x16x32_bf16 v[184:187],v[88:91],a[112:115],v[184:187]
ds_read_b64_tr_b16 v[88:89],v2 offset:20512
ds_read_b64_tr_b16 v[90:91],v2 offset:25632
v_mfma_f32_16x16x32_bf16 v[184:187],v[84:87],a[104:107],v[184:187]
ds_read_b64_tr_b16 v[84:85],v2 offset:10272
v_mfma_f32_16x16x32_bf16 v[184:187],v[80:83],a[96:99],v[184:187]
ds_read_b64_tr_b16 v[80:81],v2 offset:32
v_mfma_f32_16x16x32_bf16 v[192:195],v[96:99],a[68:71],v[192:195]
ds_read_b64_tr_b16 v[82:83],v2 offset:5152
v_mfma_f32_16x16x32_bf16 v[192:195],v[100:103],a[76:79],v[192:195]
ds_read_b64_tr_b16 v[86:87],v2 offset:15392
ds_read_b64_tr_b16 v[92:93],v2 offset:30752
v_mfma_f32_16x16x32_bf16 v[192:195],v[104:107],a[84:87],v[192:195]
ds_read_b64_tr_b16 v[94:95],v2 offset:35872
v_mfma_f32_16x16x32_bf16 v[192:195],v[108:111],a[92:95],v[192:195]
buffer_load_dwordx4 v7,s[48:51],s83 offen lds
v_mfma_f32_16x16x32_bf16 v[200:203],v[108:111],a[124:127],v[200:203]
s_add_u32 m0,s86,46080
s_add_u32 s83,s64,3
s_and_b32 s83,s83,3
v_mfma_f32_16x16x32_bf16 v[200:203],v[104:107],a[116:119],v[200:203]
s_lshl_b32 s83,s83,1
s_or_b32 s83,s83,17
s_mul_i32 s83,s83,163840
v_mfma_f32_16x16x32_bf16 v[200:203],v[100:103],a[108:111],v[200:203]
s_add_u32 s83,s83,s85
v_mfma_f32_16x16x32_bf16 v[200:203],v[96:99],a[100:103],v[200:203]
v_mfma_f32_16x16x32_bf16 v[208:211],v[96:99],a[64:67],v[208:211]
buffer_load_dwordx4 a[132:135],v6,s[28:31],s20 offen offset:1024 sc1
v_mfma_f32_16x16x32_bf16 v[208:211],v[100:103],a[72:75],v[208:211]
v_mfma_f32_16x16x32_bf16 v[208:211],v[104:107],a[80:83],v[208:211]
v_mfma_f32_16x16x32_bf16 v[208:211],v[108:111],a[88:91],v[208:211]
v_mfma_f32_16x16x32_bf16 v[216:219],v[108:111],a[120:123],v[216:219]
ds_read_b64_tr_b16 v[108:109],v1 offset:30784
ds_read_b64_tr_b16 v[110:111],v1 offset:35904
v_mfma_f32_16x16x32_bf16 v[216:219],v[104:107],a[112:115],v[216:219]
ds_read_b64_tr_b16 v[104:105],v1 offset:20544
v_mfma_f32_16x16x32_bf16 v[216:219],v[100:103],a[104:107],v[216:219]
ds_read_b64_tr_b16 v[100:101],v1 offset:10304
v_mfma_f32_16x16x32_bf16 v[216:219],v[96:99],a[96:99],v[216:219]
buffer_load_dwordx4 v24,s[48:51],s84 offen lds
v_mfma_f32_16x16x32_bf16 v[224:227],v[112:115],a[68:71],v[224:227]
ds_read_b64_tr_b16 v[96:97],v1 offset:64
ds_read_b64_tr_b16 v[98:99],v1 offset:5184
v_mfma_f32_16x16x32_bf16 v[224:227],v[116:119],a[76:79],v[224:227]
ds_read_b64_tr_b16 v[102:103],v1 offset:15424
v_mfma_f32_16x16x32_bf16 v[224:227],v[120:123],a[84:87],v[224:227]
ds_read_b64_tr_b16 v[106:107],v1 offset:25664
v_mfma_f32_16x16x32_bf16 v[224:227],v[124:127],a[92:95],v[224:227]
v_mfma_f32_16x16x32_bf16 v[232:235],v[124:127],a[124:127],v[232:235]
buffer_load_dwordx4 a[136:139],v6,s[28:31],s20 offen offset:2048 sc1
v_mfma_f32_16x16x32_bf16 v[232:235],v[120:123],a[116:119],v[232:235]
v_mfma_f32_16x16x32_bf16 v[232:235],v[116:119],a[108:111],v[232:235]
v_mfma_f32_16x16x32_bf16 v[232:235],v[112:115],a[100:103],v[232:235]
v_mfma_f32_16x16x32_bf16 v[240:243],v[112:115],a[64:67],v[240:243]
v_mfma_f32_16x16x32_bf16 v[240:243],v[116:119],a[72:75],v[240:243]
v_mfma_f32_16x16x32_bf16 v[240:243],v[120:123],a[80:83],v[240:243]
v_mfma_f32_16x16x32_bf16 v[240:243],v[124:127],a[88:91],v[240:243]
buffer_load_dwordx4 v25,s[48:51],s84 offen offset:1024 lds
)A"
R"A(v_mfma_f32_16x16x32_bf16 v[248:251],v[124:127],a[120:123],v[248:251]
ds_read_b64_tr_b16 v[124:125],v2 offset:30816
ds_read_b64_tr_b16 v[126:127],v2 offset:35936
v_mfma_f32_16x16x32_bf16 v[248:251],v[120:123],a[112:115],v[248:251]
ds_read_b64_tr_b16 v[120:121],v2 offset:20576
v_mfma_f32_16x16x32_bf16 v[248:251],v[116:119],a[104:107],v[248:251]
ds_read_b64_tr_b16 v[116:117],v2 offset:10336
v_mfma_f32_16x16x32_bf16 v[248:251],v[112:115],a[96:99],v[248:251]
s_waitcnt vmcnt(19) lgkmcnt(4)
ds_read_b64_tr_b16 v[112:113],v2 offset:96
v_mfma_f32_16x16x32_bf16 a[192:195],v[64:67],v[32:35],a[192:195]
ds_read_b64_tr_b16 v[114:115],v2 offset:5216
ds_read_b64_tr_b16 v[118:119],v2 offset:15456
v_mfma_f32_16x16x32_bf16 a[192:195],v[68:71],v[36:39],a[192:195]
buffer_load_dwordx4 a[152:155],v6,s[28:31],s21 offen offset:2048 sc1
v_mfma_f32_16x16x32_bf16 a[192:195],v[72:75],v[40:43],a[192:195]
ds_read_b64_tr_b16 v[122:123],v2 offset:25696
v_mfma_f32_16x16x32_bf16 a[192:195],v[76:79],v[44:47],a[192:195]
v_mfma_f32_16x16x32_bf16 a[196:199],v[76:79],v[180:183],a[196:199]
v_mfma_f32_16x16x32_bf16 a[196:199],v[72:75],v[164:167],a[196:199]
v_mfma_f32_16x16x32_bf16 a[196:199],v[68:71],v[148:151],a[196:199]
v_mfma_f32_16x16x32_bf16 a[196:199],v[64:67],v[132:135],a[196:199]
s_waitcnt vmcnt(18)
v_mfma_f32_16x16x32_bf16 a[200:203],v[64:67],v[196:199],a[200:203]
buffer_load_dwordx4 v26,s[48:51],s84 offen offset:2048 lds
v_mfma_f32_16x16x32_bf16 a[200:203],v[68:71],v[212:215],a[200:203]
v_mfma_f32_16x16x32_bf16 a[200:203],v[72:75],v[228:231],a[200:203]
v_mfma_f32_16x16x32_bf16 a[200:203],v[76:79],v[244:247],a[200:203]
v_mfma_f32_16x16x32_bf16 a[204:207],v[76:79],v[188:191],a[204:207]
ds_read_b64_tr_b16 v[76:77],v1 offset:30848
ds_read_b64_tr_b16 v[78:79],v1 offset:35968
v_mfma_f32_16x16x32_bf16 a[204:207],v[72:75],v[172:175],a[204:207]
s_waitcnt vmcnt(15)
ds_read_b64_tr_b16 v[72:73],v1 offset:20608
v_mfma_f32_16x16x32_bf16 a[204:207],v[68:71],v[156:159],a[204:207]
buffer_load_dwordx4 a[168:171],v6,s[28:31],s22 offen offset:2048 sc1
v_mfma_f32_16x16x32_bf16 a[204:207],v[64:67],v[140:143],a[204:207]
ds_read_b64_tr_b16 v[64:65],v1 offset:128
ds_read_b64_tr_b16 v[66:67],v1 offset:5248
v_mfma_f32_16x16x32_bf16 a[208:211],v[80:83],v[32:35],a[208:211]
ds_read_b64_tr_b16 v[68:69],v1 offset:10368
v_mfma_f32_16x16x32_bf16 a[208:211],v[84:87],v[36:39],a[208:211]
ds_read_b64_tr_b16 v[70:71],v1 offset:15488
v_mfma_f32_16x16x32_bf16 a[208:211],v[88:91],v[40:43],a[208:211]
ds_read_b64_tr_b16 v[74:75],v1 offset:25728
v_mfma_f32_16x16x32_bf16 a[208:211],v[92:95],v[44:47],a[208:211]
v_mfma_f32_16x16x32_bf16 a[212:215],v[92:95],v[180:183],a[212:215]
v_mfma_f32_16x16x32_bf16 a[212:215],v[88:91],v[164:167],a[212:215]
buffer_load_dwordx4 v27,s[48:51],s84 offen offset:3072 lds
v_mfma_f32_16x16x32_bf16 a[212:215],v[84:87],v[148:151],a[212:215]
s_add_u32 m0,s86,50176
v_mfma_f32_16x16x32_bf16 a[212:215],v[80:83],v[132:135],a[212:215]
v_mfma_f32_16x16x32_bf16 a[216:219],v[80:83],v[196:199],a[216:219]
v_mfma_f32_16x16x32_bf16 a[216:219],v[84:87],v[212:215],a[216:219]
v_mfma_f32_16x16x32_bf16 a[216:219],v[88:91],v[228:231],a[216:219]
v_mfma_f32_16x16x32_bf16 a[216:219],v[92:95],v[244:247],a[216:219]
buffer_load_dwordx4 a[184:187],v6,s[28:31],s23 offen offset:2048 sc1
v_mfma_f32_16x16x32_bf16 a[220:223],v[92:95],v[188:191],a[220:223]
ds_read_b64_tr_b16 v[92:93],v2 offset:30880
ds_read_b64_tr_b16 v[94:95],v2 offset:36000
v_mfma_f32_16x16x32_bf16 a[220:223],v[88:91],v[172:175],a[220:223]
ds_read_b64_tr_b16 v[88:89],v2 offset:20640
v_mfma_f32_16x16x32_bf16 a[220:223],v[84:87],v[156:159],a[220:223]
ds_read_b64_tr_b16 v[84:85],v2 offset:10400
v_mfma_f32_16x16x32_bf16 a[220:223],v[80:83],v[140:143],a[220:223]
ds_read_b64_tr_b16 v[80:81],v2 offset:160
v_mfma_f32_16x16x32_bf16 a[224:227],v[96:99],v[32:35],a[224:227]
ds_read_b64_tr_b16 v[82:83],v2 offset:5280
ds_read_b64_tr_b16 v[86:87],v2 offset:15520
v_mfma_f32_16x16x32_bf16 a[224:227],v[100:103],v[36:39],a[224:227]
ds_read_b64_tr_b16 v[90:91],v2 offset:25760
v_mfma_f32_16x16x32_bf16 a[224:227],v[104:107],v[40:43],a[224:227]
buffer_load_dwordx4 v7,s[48:51],s84 offen lds
v_mfma_f32_16x16x32_bf16 a[224:227],v[108:111],v[44:47],a[224:227]
s_add_u32 m0,s86,0
s_add_u32 s84,s83,20480
v_mfma_f32_16x16x32_bf16 a[228:231],v[108:111],v[180:183],a[228:231]
v_mfma_f32_16x16x32_bf16 a[228:231],v[104:107],v[164:167],a[228:231]
v_mfma_f32_16x16x32_bf16 a[228:231],v[100:103],v[148:151],a[228:231]
v_mfma_f32_16x16x32_bf16 a[228:231],v[96:99],v[132:135],a[228:231]
buffer_load_dwordx4 a[68:71],v6,s[28:31],s87 offen sc1
v_mfma_f32_16x16x32_bf16 a[232:235],v[96:99],v[196:199],a[232:235]
v_mfma_f32_16x16x32_bf16 a[232:235],v[100:103],v[212:215],a[232:235]
v_mfma_f32_16x16x32_bf16 a[232:235],v[104:107],v[228:231],a[232:235]
v_mfma_f32_16x16x32_bf16 a[232:235],v[108:111],v[244:247],a[232:235]
v_mfma_f32_16x16x32_bf16 a[236:239],v[108:111],v[188:191],a[236:239]
buffer_load_dwordx4 a[188:191],v28,s[28:31],s23 offen offset:3072 sc1
v_mfma_f32_16x16x32_bf16 a[236:239],v[104:107],v[172:175],a[236:239]
ds_read_b64_tr_b16 v[104:105],v1 offset:20672
ds_read_b64_tr_b16 v[106:107],v1 offset:25792
v_mfma_f32_16x16x32_bf16 a[236:239],v[100:103],v[156:159],a[236:239]
ds_read_b64_tr_b16 v[100:101],v1 offset:10432
v_mfma_f32_16x16x32_bf16 a[236:239],v[96:99],v[140:143],a[236:239]
s_waitcnt lgkmcnt(3)
ds_read_b64_tr_b16 v[96:97],v1 offset:192
v_mfma_f32_16x16x32_bf16 a[240:243],v[112:115],v[32:35],a[240:243]
ds_read_b64_tr_b16 v[98:99],v1 offset:5312
v_mfma_f32_16x16x32_bf16 a[240:243],v[116:119],v[36:39],a[240:243]
buffer_load_dwordx4 a[100:103],v6,s[28:31],s87 offen offset:1024 sc1
v_mfma_f32_16x16x32_bf16 a[240:243],v[120:123],v[40:43],a[240:243]
ds_read_b64_tr_b16 v[102:103],v1 offset:15552
ds_read_b64_tr_b16 v[108:109],v1 offset:30912
v_mfma_f32_16x16x32_bf16 a[240:243],v[124:127],v[44:47],a[240:243]
ds_read_b64_tr_b16 v[110:111],v1 offset:36032
v_mfma_f32_16x16x32_bf16 a[244:247],v[124:127],v[180:183],a[244:247]
v_mfma_f32_16x16x32_bf16 a[244:247],v[120:123],v[164:167],a[244:247]
v_mfma_f32_16x16x32_bf16 a[244:247],v[116:119],v[148:151],a[244:247]
buffer_load_dwordx4 a[172:175],v28,s[28:31],s22 offen offset:3072 sc1
v_mfma_f32_16x16x32_bf16 a[244:247],v[112:115],v[132:135],a[244:247]
v_mfma_f32_16x16x32_bf16 a[248:251],v[112:115],v[196:199],a[248:251]
v_mfma_f32_16x16x32_bf16 a[248:251],v[116:119],v[212:215],a[248:251]
v_mfma_f32_16x16x32_bf16 a[248:251],v[120:123],v[228:231],a[248:251]
v_mfma_f32_16x16x32_bf16 a[248:251],v[124:127],v[244:247],a[248:251]
buffer_load_dwordx4 a[76:79],v6,s[28:31],s88 offen sc1
v_mfma_f32_16x16x32_bf16 a[252:255],v[124:127],v[188:191],a[252:255]
ds_read_b64_tr_b16 v[124:125],v2 offset:30944
ds_read_b64_tr_b16 v[126:127],v2 offset:36064
v_mfma_f32_16x16x32_bf16 a[252:255],v[120:123],v[172:175],a[252:255]
ds_read_b64_tr_b16 v[120:121],v2 offset:20704
v_mfma_f32_16x16x32_bf16 a[252:255],v[116:119],v[156:159],a[252:255]
ds_read_b64_tr_b16 v[116:117],v2 offset:10464
v_mfma_f32_16x16x32_bf16 a[252:255],v[112:115],v[140:143],a[252:255]
ds_read_b64_tr_b16 v[112:113],v2 offset:224
v_mfma_f32_16x16x32_bf16 a[0:3],v[64:67],v[32:35],a[0:3]
buffer_load_dwordx4 a[64:67],v6,s[28:31],s87 offen offset:2048 sc1
v_mfma_f32_16x16x32_bf16 a[0:3],v[68:71],v[36:39],a[0:3]
ds_read_b64_tr_b16 v[114:115],v2 offset:5344
ds_read_b64_tr_b16 v[118:119],v2 offset:15584
v_mfma_f32_16x16x32_bf16 a[0:3],v[72:75],v[40:43],a[0:3]
ds_read_b64_tr_b16 v[122:123],v2 offset:25824
v_mfma_f32_16x16x32_bf16 a[0:3],v[76:79],v[44:47],a[0:3]
v_mfma_f32_16x16x32_bf16 a[4:7],v[76:79],v[180:183],a[4:7]
v_mfma_f32_16x16x32_bf16 a[4:7],v[72:75],v[164:167],a[4:7]
buffer_load_dwordx4 a[156:159],v28,s[28:31],s21 offen offset:3072 sc1
v_mfma_f32_16x16x32_bf16 a[4:7],v[68:71],v[148:151],a[4:7]
v_mfma_f32_16x16x32_bf16 a[4:7],v[64:67],v[132:135],a[4:7]
v_mfma_f32_16x16x32_bf16 a[8:11],v[64:67],v[196:199],a[8:11]
v_mfma_f32_16x16x32_bf16 a[8:11],v[68:71],v[212:215],a[8:11]
v_mfma_f32_16x16x32_bf16 a[8:11],v[72:75],v[228:231],a[8:11]
buffer_load_dwordx4 a[84:87],v6,s[28:31],s89 offen sc1
v_mfma_f32_16x16x32_bf16 a[8:11],v[76:79],v[244:247],a[8:11]
v_mfma_f32_16x16x32_bf16 a[12:15],v[76:79],v[188:191],a[12:15]
ds_read_b64_tr_b16 v[76:77],v1 offset:30976
ds_read_b64_tr_b16 v[78:79],v1 offset:36096
v_mfma_f32_16x16x32_bf16 a[12:15],v[72:75],v[172:175],a[12:15]
ds_read_b64_tr_b16 v[72:73],v1 offset:20736
v_mfma_f32_16x16x32_bf16 a[12:15],v[68:71],v[156:159],a[12:15]
ds_read_b64_tr_b16 v[68:69],v1 offset:10496
v_mfma_f32_16x16x32_bf16 a[12:15],v[64:67],v[140:143],a[12:15]
buffer_load_dwordx4 a[140:143],v28,s[28:31],s20 offen offset:3072 sc1
v_mfma_f32_16x16x32_bf16 a[16:19],v[80:83],v[32:35],a[16:19]
s_add_u32 s20,s64,3
ds_read_b64_tr_b16 v[64:65],v1 offset:256
ds_read_b64_tr_b16 v[66:67],v1 offset:5376
v_mfma_f32_16x16x32_bf16 a[16:19],v[84:87],v[36:39],a[16:19]
s_and_b32 s20,s20,3
s_lshl_b32 s20,s20,1
ds_read_b64_tr_b16 v[70:71],v1 offset:15616
v_mfma_f32_16x16x32_bf16 a[16:19],v[88:91],v[40:43],a[16:19]
s_or_b32 s20,s20,17
ds_read_b64_tr_b16 v[74:75],v1 offset:25856
v_add_u32 v1,0xffff6000,v1
v_mfma_f32_16x16x32_bf16 a[16:19],v[92:95],v[44:47],a[16:19]
s_lshl_b32 s20,s20,16
s_add_u32 s21,s20,4096
s_add_u32 s22,s20,8192
v_mfma_f32_16x16x32_bf16 a[20:23],v[92:95],v[180:183],a[20:23]
buffer_load_dwordx4 a[72:75],v6,s[28:31],s88 offen offset:2048 sc1
v_mfma_f32_16x16x32_bf16 a[20:23],v[88:91],v[164:167],a[20:23]
s_add_u32 s23,s20,12288
v_mfma_f32_16x16x32_bf16 a[20:23],v[84:87],v[148:151],a[20:23]
v_mfma_f32_16x16x32_bf16 a[20:23],v[80:83],v[132:135],a[20:23]
v_mfma_f32_16x16x32_bf16 a[24:27],v[80:83],v[196:199],a[24:27]
v_mfma_f32_16x16x32_bf16 a[24:27],v[84:87],v[212:215],a[24:27]
buffer_load_dwordx4 a[92:95],v6,s[28:31],s90 offen sc1
v_mfma_f32_16x16x32_bf16 a[24:27],v[88:91],v[228:231],a[24:27]
v_mfma_f32_16x16x32_bf16 a[24:27],v[92:95],v[244:247],a[24:27]
v_mfma_f32_16x16x32_bf16 a[28:31],v[92:95],v[188:191],a[28:31]
ds_read_b64_tr_b16 v[92:93],v2 offset:31008
ds_read_b64_tr_b16 v[94:95],v2 offset:36128
v_mfma_f32_16x16x32_bf16 a[28:31],v[88:91],v[172:175],a[28:31]
ds_read_b64_tr_b16 v[88:89],v2 offset:20768
v_mfma_f32_16x16x32_bf16 a[28:31],v[84:87],v[156:159],a[28:31]
buffer_load_dwordx4 a[80:83],v6,s[28:31],s89 offen offset:2048 sc1
v_mfma_f32_16x16x32_bf16 a[28:31],v[80:83],v[140:143],a[28:31]
s_waitcnt lgkmcnt(3)
ds_read_b64_tr_b16 v[80:81],v2 offset:288
ds_read_b64_tr_b16 v[82:83],v2 offset:5408
v_mfma_f32_16x16x32_bf16 v[128:131],v[96:99],v[32:35],v[128:131]
ds_read_b64_tr_b16 v[84:85],v2 offset:10528
v_mfma_f32_16x16x32_bf16 v[128:131],v[100:103],v[36:39],v[128:131]
ds_read_b64_tr_b16 v[86:87],v2 offset:15648
v_mfma_f32_16x16x32_bf16 v[128:131],v[104:107],v[40:43],v[128:131]
ds_read_b64_tr_b16 v[90:91],v2 offset:25888
v_add_u32 v2,0xffff6000,v2
v_mfma_f32_16x16x32_bf16 v[128:131],v[108:111],v[44:47],v[128:131]
v_mfma_f32_16x16x32_bf16 v[136:139],v[108:111],v[180:183],v[136:139]
v_mfma_f32_16x16x32_bf16 v[136:139],v[104:107],v[164:167],v[136:139]
buffer_load_dwordx4 v24,s[48:51],s83 offen lds
v_mfma_f32_16x16x32_bf16 v[136:139],v[100:103],v[148:151],v[136:139]
v_mfma_f32_16x16x32_bf16 v[136:139],v[96:99],v[132:135],v[136:139]
v_mfma_f32_16x16x32_bf16 v[144:147],v[96:99],v[196:199],v[144:147]
v_mfma_f32_16x16x32_bf16 v[144:147],v[100:103],v[212:215],v[144:147]
v_mfma_f32_16x16x32_bf16 v[144:147],v[104:107],v[228:231],v[144:147]
v_mfma_f32_16x16x32_bf16 v[144:147],v[108:111],v[244:247],v[144:147]
v_mfma_f32_16x16x32_bf16 v[152:155],v[108:111],v[188:191],v[152:155]
buffer_load_dwordx4 v25,s[48:51],s83 offen offset:1024 lds
v_mfma_f32_16x16x32_bf16 v[152:155],v[104:107],v[172:175],v[152:155]
ds_read_b64_tr_b16 v[104:105],v1 offset:20480
ds_read_b64_tr_b16 v[106:107],v1 offset:25600
v_mfma_f32_16x16x32_bf16 v[152:155],v[100:103],v[156:159],v[152:155]
ds_read_b64_tr_b16 v[100:101],v1 offset:10240
v_mfma_f32_16x16x32_bf16 v[152:155],v[96:99],v[140:143],v[152:155]
ds_read_b64_tr_b16 v[96:97],v1
s_waitcnt vmcnt(14) lgkmcnt(4)
s_barrier
v_mfma_f32_16x16x32_bf16 v[160:163],v[112:115],v[32:35],v[160:163]
)A"
R"A(ds_read_b64_tr_b16 v[98:99],v1 offset:5120
v_mfma_f32_16x16x32_bf16 v[160:163],v[116:119],v[36:39],v[160:163]
ds_read_b64_tr_b16 v[102:103],v1 offset:15360
ds_read_b64_tr_b16 v[108:109],v1 offset:30720
v_mfma_f32_16x16x32_bf16 v[160:163],v[120:123],v[40:43],v[160:163]
buffer_load_dwordx4 v26,s[48:51],s83 offen offset:2048 lds
v_mfma_f32_16x16x32_bf16 v[160:163],v[124:127],v[44:47],v[160:163]
ds_read_b64_tr_b16 v[110:111],v1 offset:35840
v_mfma_f32_16x16x32_bf16 v[168:171],v[124:127],v[180:183],v[168:171]
v_mfma_f32_16x16x32_bf16 v[168:171],v[120:123],v[164:167],v[168:171]
v_mfma_f32_16x16x32_bf16 v[168:171],v[116:119],v[148:151],v[168:171]
v_mfma_f32_16x16x32_bf16 v[168:171],v[112:115],v[132:135],v[168:171]
v_mfma_f32_16x16x32_bf16 v[176:179],v[112:115],v[196:199],v[176:179]
v_mfma_f32_16x16x32_bf16 v[176:179],v[116:119],v[212:215],v[176:179]
buffer_load_dwordx4 v27,s[48:51],s83 offen offset:3072 lds
v_mfma_f32_16x16x32_bf16 v[176:179],v[120:123],v[228:231],v[176:179]
s_add_u32 m0,s86,4096
L_pd16_%=:
s_cbranch_execz L_trtg2F_%=
v_mfma_f32_16x16x32_bf16 v[176:179],v[124:127],v[244:247],v[176:179]
v_mfma_f32_16x16x32_bf16 v[184:187],v[124:127],v[188:191],v[184:187]
ds_read_b64_tr_b16 v[124:125],v2 offset:30752
ds_read_b64_tr_b16 v[126:127],v2 offset:35872
v_mfma_f32_16x16x32_bf16 v[184:187],v[120:123],v[172:175],v[184:187]
ds_read_b64_tr_b16 v[120:121],v2 offset:20512
v_mfma_f32_16x16x32_bf16 v[184:187],v[116:119],v[156:159],v[184:187]
buffer_load_dwordx4 a[124:127],v6,s[28:31],s90 offen offset:1024 sc1
v_mfma_f32_16x16x32_bf16 v[184:187],v[112:115],v[140:143],v[184:187]
ds_read_b64_tr_b16 v[112:113],v2 offset:32
ds_read_b64_tr_b16 v[114:115],v2 offset:5152
v_mfma_f32_16x16x32_bf16 v[192:195],v[64:67],v[32:35],v[192:195]
ds_read_b64_tr_b16 v[116:117],v2 offset:10272
v_mfma_f32_16x16x32_bf16 v[192:195],v[68:71],v[36:39],v[192:195]
ds_read_b64_tr_b16 v[118:119],v2 offset:15392
v_mfma_f32_16x16x32_bf16 v[192:195],v[72:75],v[40:43],v[192:195]
ds_read_b64_tr_b16 v[122:123],v2 offset:25632
v_mfma_f32_16x16x32_bf16 v[192:195],v[76:79],v[44:47],v[192:195]
buffer_load_dwordx4 a[88:91],v6,s[28:31],s90 offen offset:2048 sc1
v_mfma_f32_16x16x32_bf16 v[200:203],v[76:79],v[180:183],v[200:203]
v_mfma_f32_16x16x32_bf16 v[200:203],v[72:75],v[164:167],v[200:203]
v_mfma_f32_16x16x32_bf16 v[200:203],v[68:71],v[148:151],v[200:203]
v_mfma_f32_16x16x32_bf16 v[200:203],v[64:67],v[132:135],v[200:203]
v_mfma_f32_16x16x32_bf16 v[208:211],v[64:67],v[196:199],v[208:211]
v_mfma_f32_16x16x32_bf16 v[208:211],v[68:71],v[212:215],v[208:211]
v_mfma_f32_16x16x32_bf16 v[208:211],v[72:75],v[228:231],v[208:211]
buffer_load_dwordx4 v7,s[48:51],s83 offen lds
v_mfma_f32_16x16x32_bf16 v[208:211],v[76:79],v[244:247],v[208:211]
s_add_u32 m0,s86,5120
s_add_u32 s83,s64,3
s_and_b32 s83,s83,3
v_mfma_f32_16x16x32_bf16 v[216:219],v[76:79],v[188:191],v[216:219]
s_lshl_b32 s83,s83,1
ds_read_b64_tr_b16 v[76:77],v1 offset:30784
ds_read_b64_tr_b16 v[78:79],v1 offset:35904
v_mfma_f32_16x16x32_bf16 v[216:219],v[72:75],v[172:175],v[216:219]
buffer_load_dwordx2 v[4:5],v3,s[44:47],s91 offen offset:1280 sc1
v_mfma_f32_16x16x32_bf16 v[216:219],v[68:71],v[156:159],v[216:219]
s_or_b32 s83,s83,1
ds_read_b64_tr_b16 v[68:69],v1 offset:10304
ds_read_b64_tr_b16 v[70:71],v1 offset:15424
v_mfma_f32_16x16x32_bf16 v[216:219],v[64:67],v[140:143],v[216:219]
s_mul_i32 s83,s83,163840
s_add_u32 s83,s83,s85
ds_read_b64_tr_b16 v[64:65],v1 offset:64
v_mfma_f32_16x16x32_bf16 v[224:227],v[80:83],v[32:35],v[224:227]
ds_read_b64_tr_b16 v[66:67],v1 offset:5184
v_mfma_f32_16x16x32_bf16 v[224:227],v[84:87],v[36:39],v[224:227]
ds_read_b64_tr_b16 v[72:73],v1 offset:20544
v_mfma_f32_16x16x32_bf16 v[224:227],v[88:91],v[40:43],v[224:227]
ds_read_b64_tr_b16 v[74:75],v1 offset:25664
v_mfma_f32_16x16x32_bf16 v[224:227],v[92:95],v[44:47],v[224:227]
v_mfma_f32_16x16x32_bf16 v[232:235],v[92:95],v[180:183],v[232:235]
buffer_load_dwordx4 v24,s[48:51],s84 offen lds
v_mfma_f32_16x16x32_bf16 v[232:235],v[88:91],v[164:167],v[232:235]
v_mfma_f32_16x16x32_bf16 v[232:235],v[84:87],v[148:151],v[232:235]
v_mfma_f32_16x16x32_bf16 v[232:235],v[80:83],v[132:135],v[232:235]
v_mfma_f32_16x16x32_bf16 v[240:243],v[80:83],v[196:199],v[240:243]
v_mfma_f32_16x16x32_bf16 v[240:243],v[84:87],v[212:215],v[240:243]
buffer_load_dwordx4 a[116:119],v6,s[28:31],s89 offen offset:1024 sc1
v_mfma_f32_16x16x32_bf16 v[240:243],v[88:91],v[228:231],v[240:243]
v_mfma_f32_16x16x32_bf16 v[240:243],v[92:95],v[244:247],v[240:243]
v_mfma_f32_16x16x32_bf16 v[248:251],v[92:95],v[188:191],v[248:251]
ds_read_b64_tr_b16 v[92:93],v2 offset:30816
ds_read_b64_tr_b16 v[94:95],v2 offset:35936
v_mfma_f32_16x16x32_bf16 v[248:251],v[88:91],v[172:175],v[248:251]
ds_read_b64_tr_b16 v[88:89],v2 offset:20576
v_mfma_f32_16x16x32_bf16 v[248:251],v[84:87],v[156:159],v[248:251]
ds_read_b64_tr_b16 v[84:85],v2 offset:10336
v_mfma_f32_16x16x32_bf16 v[248:251],v[80:83],v[140:143],v[248:251]
s_waitcnt lgkmcnt(4)
ds_read_b64_tr_b16 v[80:81],v2 offset:96
v_mfma_f32_16x16x32_bf16 a[192:195],v[96:99],a[128:131],a[192:195]
buffer_load_dwordx4 v25,s[48:51],s84 offen offset:1024 lds
v_mfma_f32_16x16x32_bf16 a[192:195],v[100:103],a[144:147],a[192:195]
ds_read_b64_tr_b16 v[82:83],v2 offset:5216
ds_read_b64_tr_b16 v[86:87],v2 offset:15456
v_mfma_f32_16x16x32_bf16 a[192:195],v[104:107],a[160:163],a[192:195]
ds_read_b64_tr_b16 v[90:91],v2 offset:25696
v_mfma_f32_16x16x32_bf16 a[192:195],v[108:111],a[176:179],a[192:195]
v_mfma_f32_16x16x32_bf16 a[196:199],v[108:111],a[180:183],a[196:199]
v_mfma_f32_16x16x32_bf16 a[196:199],v[104:107],a[164:167],a[196:199]
v_mfma_f32_16x16x32_bf16 a[196:199],v[100:103],a[148:151],a[196:199]
v_mfma_f32_16x16x32_bf16 a[196:199],v[96:99],a[132:135],a[196:199]
buffer_load_dwordx4 v26,s[48:51],s84 offen offset:2048 lds
v_mfma_f32_16x16x32_bf16 a[200:203],v[96:99],a[136:139],a[200:203]
v_mfma_f32_16x16x32_bf16 a[200:203],v[100:103],a[152:155],a[200:203]
v_mfma_f32_16x16x32_bf16 a[200:203],v[104:107],a[168:171],a[200:203]
v_mfma_f32_16x16x32_bf16 a[200:203],v[108:111],a[184:187],a[200:203]
s_waitcnt vmcnt(19)
v_mfma_f32_16x16x32_bf16 a[204:207],v[108:111],a[188:191],a[204:207]
ds_read_b64_tr_b16 v[108:109],v1 offset:30848
ds_read_b64_tr_b16 v[110:111],v1 offset:35968
v_mfma_f32_16x16x32_bf16 a[204:207],v[104:107],a[172:175],a[204:207]
s_waitcnt vmcnt(15)
ds_read_b64_tr_b16 v[104:105],v1 offset:20608
v_mfma_f32_16x16x32_bf16 a[204:207],v[100:103],a[156:159],a[204:207]
buffer_load_dwordx4 v27,s[48:51],s84 offen offset:3072 lds
v_mfma_f32_16x16x32_bf16 a[204:207],v[96:99],a[140:143],a[204:207]
s_add_u32 m0,s86,9216
ds_read_b64_tr_b16 v[96:97],v1 offset:128
ds_read_b64_tr_b16 v[98:99],v1 offset:5248
v_mfma_f32_16x16x32_bf16 a[208:211],v[112:115],a[128:131],a[208:211]
ds_read_b64_tr_b16 v[100:101],v1 offset:10368
v_mfma_f32_16x16x32_bf16 a[208:211],v[116:119],a[144:147],a[208:211]
ds_read_b64_tr_b16 v[102:103],v1 offset:15488
v_mfma_f32_16x16x32_bf16 a[208:211],v[120:123],a[160:163],a[208:211]
ds_read_b64_tr_b16 v[106:107],v1 offset:25728
v_mfma_f32_16x16x32_bf16 a[208:211],v[124:127],a[176:179],a[208:211]
v_mfma_f32_16x16x32_bf16 a[212:215],v[124:127],a[180:183],a[212:215]
v_mfma_f32_16x16x32_bf16 a[212:215],v[120:123],a[164:167],a[212:215]
buffer_load_dwordx4 v7,s[48:51],s84 offen lds
v_mfma_f32_16x16x32_bf16 a[212:215],v[116:119],a[148:151],a[212:215]
s_add_u32 m0,s86,122880
s_add_u32 s84,s83,20480
v_mfma_f32_16x16x32_bf16 a[212:215],v[112:115],a[132:135],a[212:215]
v_mfma_f32_16x16x32_bf16 a[216:219],v[112:115],a[136:139],a[216:219]
v_mfma_f32_16x16x32_bf16 a[216:219],v[116:119],a[152:155],a[216:219]
v_mfma_f32_16x16x32_bf16 a[216:219],v[120:123],a[168:171],a[216:219]
buffer_load_dwordx4 a[120:123],v28,s[28:31],s90 offen offset:3072 sc1
v_mfma_f32_16x16x32_bf16 a[216:219],v[124:127],a[184:187],a[216:219]
v_mfma_f32_16x16x32_bf16 a[220:223],v[124:127],a[188:191],a[220:223]
ds_read_b64_tr_b16 v[124:125],v2 offset:30880
ds_read_b64_tr_b16 v[126:127],v2 offset:36000
v_mfma_f32_16x16x32_bf16 a[220:223],v[120:123],a[172:175],a[220:223]
ds_read_b64_tr_b16 v[120:121],v2 offset:20640
v_mfma_f32_16x16x32_bf16 a[220:223],v[116:119],a[156:159],a[220:223]
ds_read_b64_tr_b16 v[116:117],v2 offset:10400
v_mfma_f32_16x16x32_bf16 a[220:223],v[112:115],a[140:143],a[220:223]
buffer_load_dwordx4 a[108:111],v6,s[28:31],s88 offen offset:1024 sc1
v_mfma_f32_16x16x32_bf16 a[224:227],v[64:67],a[128:131],a[224:227]
ds_read_b64_tr_b16 v[112:113],v2 offset:160
ds_read_b64_tr_b16 v[114:115],v2 offset:5280
v_mfma_f32_16x16x32_bf16 a[224:227],v[68:71],a[144:147],a[224:227]
ds_read_b64_tr_b16 v[118:119],v2 offset:15520
v_mfma_f32_16x16x32_bf16 a[224:227],v[72:75],a[160:163],a[224:227]
ds_read_b64_tr_b16 v[122:123],v2 offset:25760
v_mfma_f32_16x16x32_bf16 a[224:227],v[76:79],a[176:179],a[224:227]
v_mfma_f32_16x16x32_bf16 a[228:231],v[76:79],a[180:183],a[228:231]
buffer_load_dwordx4 a[112:115],v28,s[28:31],s89 offen offset:3072 sc1
v_mfma_f32_16x16x32_bf16 a[228:231],v[72:75],a[164:167],a[228:231]
v_mfma_f32_16x16x32_bf16 a[228:231],v[68:71],a[148:151],a[228:231]
v_mfma_f32_16x16x32_bf16 a[228:231],v[64:67],a[132:135],a[228:231]
v_mfma_f32_16x16x32_bf16 a[232:235],v[64:67],a[136:139],a[232:235]
v_mfma_f32_16x16x32_bf16 a[232:235],v[68:71],a[152:155],a[232:235]
buffer_load_dwordx4 a[104:107],v28,s[28:31],s88 offen offset:3072 sc1
v_mfma_f32_16x16x32_bf16 a[232:235],v[72:75],a[168:171],a[232:235]
s_waitcnt vmcnt(10)
v_cmp_ne_u32 s[56:57],s52,v4
v_cmp_ne_u32 s[58:59],s53,v5
v_mfma_f32_16x16x32_bf16 a[232:235],v[76:79],a[184:187],a[232:235]
s_or_b64 s[56:57],s[56:57],s[58:59]
s_cbranch_scc1 L_fr3_%=
L_fo3_%=:
s_lshl_b32 s91,s64,6
v_mfma_f32_16x16x32_bf16 a[236:239],v[76:79],a[188:191],a[236:239]
ds_read_b64_tr_b16 v[76:77],v1 offset:30912
ds_read_b64_tr_b16 v[78:79],v1 offset:36032
v_mfma_f32_16x16x32_bf16 a[236:239],v[72:75],a[172:175],a[236:239]
ds_read_b64_tr_b16 v[72:73],v1 offset:20672
v_bfe_u32 v3,v6,4,2
v_mfma_f32_16x16x32_bf16 a[236:239],v[68:71],a[156:159],a[236:239]
buffer_load_dwordx4 v[32:35],v6,s[28:31],s20 offen sc1
v_mfma_f32_16x16x32_bf16 a[236:239],v[64:67],a[140:143],a[236:239]
s_waitcnt lgkmcnt(3)
ds_read_b64_tr_b16 v[64:65],v1 offset:192
ds_read_b64_tr_b16 v[66:67],v1 offset:5312
v_mfma_f32_16x16x32_bf16 a[240:243],v[80:83],a[128:131],a[240:243]
ds_read_b64_tr_b16 v[68:69],v1 offset:10432
v_min_u32 v3,2,v3
v_mfma_f32_16x16x32_bf16 a[240:243],v[84:87],a[144:147],a[240:243]
ds_read_b64_tr_b16 v[70:71],v1 offset:15552
v_add3_u32 v3,v3,s63,1
v_mfma_f32_16x16x32_bf16 a[240:243],v[88:91],a[160:163],a[240:243]
ds_read_b64_tr_b16 v[74:75],v1 offset:25792
v_and_b32 v3,3,v3
v_mfma_f32_16x16x32_bf16 a[240:243],v[92:95],a[176:179],a[240:243]
v_lshlrev_b32 v3,8,v3
v_mfma_f32_16x16x32_bf16 a[244:247],v[92:95],a[180:183],a[244:247]
v_mfma_f32_16x16x32_bf16 a[244:247],v[88:91],a[164:167],a[244:247]
buffer_load_dwordx4 v24,s[48:51],s83 offen lds
v_mfma_f32_16x16x32_bf16 a[244:247],v[84:87],a[148:151],a[244:247]
v_mfma_f32_16x16x32_bf16 a[244:247],v[80:83],a[132:135],a[244:247]
v_mfma_f32_16x16x32_bf16 a[248:251],v[80:83],a[136:139],a[248:251]
v_mfma_f32_16x16x32_bf16 a[248:251],v[84:87],a[152:155],a[248:251]
v_mfma_f32_16x16x32_bf16 a[248:251],v[88:91],a[168:171],a[248:251]
buffer_load_dwordx4 v[36:39],v6,s[28:31],s21 offen sc1
v_mfma_f32_16x16x32_bf16 a[248:251],v[92:95],a[184:187],a[248:251]
v_mfma_f32_16x16x32_bf16 a[252:255],v[92:95],a[188:191],a[252:255]
ds_read_b64_tr_b16 v[92:93],v2 offset:30944
ds_read_b64_tr_b16 v[94:95],v2 offset:36064
v_mfma_f32_16x16x32_bf16 a[252:255],v[88:91],a[172:175],a[252:255]
ds_read_b64_tr_b16 v[88:89],v2 offset:20704
v_mfma_f32_16x16x32_bf16 a[252:255],v[84:87],a[156:159],a[252:255]
ds_read_b64_tr_b16 v[84:85],v2 offset:10464
v_mfma_f32_16x16x32_bf16 a[252:255],v[80:83],a[140:143],a[252:255]
ds_read_b64_tr_b16 v[80:81],v2 offset:224
v_mfma_f32_16x16x32_bf16 a[0:3],v[96:99],a[128:131],a[0:3]
ds_read_b64_tr_b16 v[82:83],v2 offset:5344
ds_read_b64_tr_b16 v[86:87],v2 offset:15584
v_mfma_f32_16x16x32_bf16 a[0:3],v[100:103],a[144:147],a[0:3]
)A"
R"A(buffer_load_dwordx4 v25,s[48:51],s83 offen offset:1024 lds
v_mfma_f32_16x16x32_bf16 a[0:3],v[104:107],a[160:163],a[0:3]
ds_read_b64_tr_b16 v[90:91],v2 offset:25824
v_mfma_f32_16x16x32_bf16 a[0:3],v[108:111],a[176:179],a[0:3]
v_mfma_f32_16x16x32_bf16 a[4:7],v[108:111],a[180:183],a[4:7]
v_mfma_f32_16x16x32_bf16 a[4:7],v[104:107],a[164:167],a[4:7]
v_mfma_f32_16x16x32_bf16 a[4:7],v[100:103],a[148:151],a[4:7]
buffer_load_dwordx4 v[40:43],v6,s[28:31],s22 offen sc1
v_mfma_f32_16x16x32_bf16 a[4:7],v[96:99],a[132:135],a[4:7]
v_mfma_f32_16x16x32_bf16 a[8:11],v[96:99],a[136:139],a[8:11]
v_mfma_f32_16x16x32_bf16 a[8:11],v[100:103],a[152:155],a[8:11]
v_mfma_f32_16x16x32_bf16 a[8:11],v[104:107],a[168:171],a[8:11]
v_mfma_f32_16x16x32_bf16 a[8:11],v[108:111],a[184:187],a[8:11]
buffer_load_dwordx4 a[96:99],v28,s[28:31],s87 offen offset:3072 sc1
v_mfma_f32_16x16x32_bf16 a[12:15],v[108:111],a[188:191],a[12:15]
s_add_u32 s87,s64,3
ds_read_b64_tr_b16 v[108:109],v1 offset:30976
ds_read_b64_tr_b16 v[110:111],v1 offset:36096
v_mfma_f32_16x16x32_bf16 a[12:15],v[104:107],a[172:175],a[12:15]
s_and_b32 s87,s87,3
s_lshl_b32 s87,s87,1
ds_read_b64_tr_b16 v[104:105],v1 offset:20736
v_mfma_f32_16x16x32_bf16 a[12:15],v[100:103],a[156:159],a[12:15]
s_or_b32 s87,s87,1
s_lshl_b32 s87,s87,16
ds_read_b64_tr_b16 v[100:101],v1 offset:10496
v_mfma_f32_16x16x32_bf16 a[12:15],v[96:99],a[140:143],a[12:15]
s_add_u32 s88,s87,4096
s_add_u32 s89,s87,8192
ds_read_b64_tr_b16 v[96:97],v1 offset:256
v_mfma_f32_16x16x32_bf16 a[16:19],v[112:115],a[128:131],a[16:19]
buffer_load_dwordx4 v[44:47],v6,s[28:31],s23 offen sc1
v_mfma_f32_16x16x32_bf16 a[16:19],v[116:119],a[144:147],a[16:19]
s_add_u32 s90,s87,12288
ds_read_b64_tr_b16 v[98:99],v1 offset:5376
ds_read_b64_tr_b16 v[102:103],v1 offset:15616
v_mfma_f32_16x16x32_bf16 a[16:19],v[120:123],a[160:163],a[16:19]
ds_read_b64_tr_b16 v[106:107],v1 offset:25856
v_add_u32 v1,0xffff6000,v1
v_mfma_f32_16x16x32_bf16 a[16:19],v[124:127],a[176:179],a[16:19]
v_mfma_f32_16x16x32_bf16 a[20:23],v[124:127],a[180:183],a[20:23]
v_mfma_f32_16x16x32_bf16 a[20:23],v[120:123],a[164:167],a[20:23]
v_mfma_f32_16x16x32_bf16 a[20:23],v[116:119],a[148:151],a[20:23]
v_mfma_f32_16x16x32_bf16 a[20:23],v[112:115],a[132:135],a[20:23]
buffer_load_dwordx4 v26,s[48:51],s83 offen offset:2048 lds
v_mfma_f32_16x16x32_bf16 a[24:27],v[112:115],a[136:139],a[24:27]
v_mfma_f32_16x16x32_bf16 a[24:27],v[116:119],a[152:155],a[24:27]
v_mfma_f32_16x16x32_bf16 a[24:27],v[120:123],a[168:171],a[24:27]
v_mfma_f32_16x16x32_bf16 a[24:27],v[124:127],a[184:187],a[24:27]
v_mfma_f32_16x16x32_bf16 a[28:31],v[124:127],a[188:191],a[28:31]
buffer_load_dwordx4 v[180:183],v6,s[28:31],s23 offen offset:1024 sc1
v_mfma_f32_16x16x32_bf16 a[28:31],v[120:123],a[172:175],a[28:31]
ds_read_b64_tr_b16 v[120:121],v2 offset:20768
ds_read_b64_tr_b16 v[122:123],v2 offset:25888
v_mfma_f32_16x16x32_bf16 a[28:31],v[116:119],a[156:159],a[28:31]
ds_read_b64_tr_b16 v[116:117],v2 offset:10528
v_mfma_f32_16x16x32_bf16 a[28:31],v[112:115],a[140:143],a[28:31]
s_waitcnt lgkmcnt(3)
ds_read_b64_tr_b16 v[112:113],v2 offset:288
v_mfma_f32_16x16x32_bf16 v[128:131],v[64:67],a[128:131],v[128:131]
ds_read_b64_tr_b16 v[114:115],v2 offset:5408
v_mfma_f32_16x16x32_bf16 v[128:131],v[68:71],a[144:147],v[128:131]
ds_read_b64_tr_b16 v[118:119],v2 offset:15648
ds_read_b64_tr_b16 v[124:125],v2 offset:31008
v_mfma_f32_16x16x32_bf16 v[128:131],v[72:75],a[160:163],v[128:131]
ds_read_b64_tr_b16 v[126:127],v2 offset:36128
v_add_u32 v2,0xffff6000,v2
v_mfma_f32_16x16x32_bf16 v[128:131],v[76:79],a[176:179],v[128:131]
buffer_load_dwordx4 v[164:167],v6,s[28:31],s22 offen offset:1024 sc1
v_mfma_f32_16x16x32_bf16 v[136:139],v[76:79],a[180:183],v[136:139]
v_mfma_f32_16x16x32_bf16 v[136:139],v[72:75],a[164:167],v[136:139]
v_mfma_f32_16x16x32_bf16 v[136:139],v[68:71],a[148:151],v[136:139]
v_mfma_f32_16x16x32_bf16 v[136:139],v[64:67],a[132:135],v[136:139]
v_mfma_f32_16x16x32_bf16 v[144:147],v[64:67],a[136:139],v[144:147]
v_mfma_f32_16x16x32_bf16 v[144:147],v[68:71],a[152:155],v[144:147]
v_mfma_f32_16x16x32_bf16 v[144:147],v[72:75],a[168:171],v[144:147]
buffer_load_dwordx4 v27,s[48:51],s83 offen offset:3072 lds
v_mfma_f32_16x16x32_bf16 v[144:147],v[76:79],a[184:187],v[144:147]
s_add_u32 m0,s86,126976
v_mfma_f32_16x16x32_bf16 v[152:155],v[76:79],a[188:191],v[152:155]
ds_read_b64_tr_b16 v[76:77],v1 offset:30720
ds_read_b64_tr_b16 v[78:79],v1 offset:35840
v_mfma_f32_16x16x32_bf16 v[152:155],v[72:75],a[172:175],v[152:155]
ds_read_b64_tr_b16 v[72:73],v1 offset:20480
v_mfma_f32_16x16x32_bf16 v[152:155],v[68:71],a[156:159],v[152:155]
ds_read_b64_tr_b16 v[68:69],v1 offset:10240
v_mfma_f32_16x16x32_bf16 v[152:155],v[64:67],a[140:143],v[152:155]
buffer_load_dwordx4 v[148:151],v6,s[28:31],s21 offen offset:1024 sc1
v_mfma_f32_16x16x32_bf16 v[160:163],v[80:83],a[128:131],v[160:163]
ds_read_b64_tr_b16 v[64:65],v1
ds_read_b64_tr_b16 v[66:67],v1 offset:5120
v_mfma_f32_16x16x32_bf16 v[160:163],v[84:87],a[144:147],v[160:163]
ds_read_b64_tr_b16 v[70:71],v1 offset:15360
v_mfma_f32_16x16x32_bf16 v[160:163],v[88:91],a[160:163],v[160:163]
ds_read_b64_tr_b16 v[74:75],v1 offset:25600
v_mfma_f32_16x16x32_bf16 v[160:163],v[92:95],a[176:179],v[160:163]
v_mfma_f32_16x16x32_bf16 v[168:171],v[92:95],a[180:183],v[168:171]
v_mfma_f32_16x16x32_bf16 v[168:171],v[88:91],a[164:167],v[168:171]
v_mfma_f32_16x16x32_bf16 v[168:171],v[84:87],a[148:151],v[168:171]
buffer_load_dwordx4 v7,s[48:51],s83 offen lds
v_mfma_f32_16x16x32_bf16 v[168:171],v[80:83],a[132:135],v[168:171]
s_add_u32 m0,s86,128000
s_mul_i32 s83,s63,20480
v_mfma_f32_16x16x32_bf16 v[176:179],v[80:83],a[136:139],v[176:179]
v_mfma_f32_16x16x32_bf16 v[176:179],v[84:87],a[152:155],v[176:179]
v_mfma_f32_16x16x32_bf16 v[176:179],v[88:91],a[168:171],v[176:179]
v_mfma_f32_16x16x32_bf16 v[176:179],v[92:95],a[184:187],v[176:179]
buffer_load_dwordx4 v[132:135],v6,s[28:31],s20 offen offset:1024 sc1
v_mfma_f32_16x16x32_bf16 v[184:187],v[92:95],a[188:191],v[184:187]
ds_read_b64_tr_b16 v[92:93],v2 offset:30752
ds_read_b64_tr_b16 v[94:95],v2 offset:35872
v_mfma_f32_16x16x32_bf16 v[184:187],v[88:91],a[172:175],v[184:187]
ds_read_b64_tr_b16 v[88:89],v2 offset:20512
v_mfma_f32_16x16x32_bf16 v[184:187],v[84:87],a[156:159],v[184:187]
ds_read_b64_tr_b16 v[84:85],v2 offset:10272
v_mfma_f32_16x16x32_bf16 v[184:187],v[80:83],a[140:143],v[184:187]
ds_read_b64_tr_b16 v[80:81],v2 offset:32
v_mfma_f32_16x16x32_bf16 v[192:195],v[96:99],a[128:131],v[192:195]
ds_read_b64_tr_b16 v[82:83],v2 offset:5152
ds_read_b64_tr_b16 v[86:87],v2 offset:15392
v_mfma_f32_16x16x32_bf16 v[192:195],v[100:103],a[144:147],v[192:195]
ds_read_b64_tr_b16 v[90:91],v2 offset:25632
v_mfma_f32_16x16x32_bf16 v[192:195],v[104:107],a[160:163],v[192:195]
buffer_load_dwordx4 v24,s[48:51],s84 offen lds
v_mfma_f32_16x16x32_bf16 v[192:195],v[108:111],a[176:179],v[192:195]
v_mfma_f32_16x16x32_bf16 v[200:203],v[108:111],a[180:183],v[200:203]
v_mfma_f32_16x16x32_bf16 v[200:203],v[104:107],a[164:167],v[200:203]
v_mfma_f32_16x16x32_bf16 v[200:203],v[100:103],a[148:151],v[200:203]
v_mfma_f32_16x16x32_bf16 v[200:203],v[96:99],a[132:135],v[200:203]
buffer_load_dwordx4 v[196:199],v6,s[28:31],s20 offen offset:2048 sc1
v_mfma_f32_16x16x32_bf16 v[208:211],v[96:99],a[136:139],v[208:211]
v_mfma_f32_16x16x32_bf16 v[208:211],v[100:103],a[152:155],v[208:211]
v_mfma_f32_16x16x32_bf16 v[208:211],v[104:107],a[168:171],v[208:211]
v_mfma_f32_16x16x32_bf16 v[208:211],v[108:111],a[184:187],v[208:211]
v_mfma_f32_16x16x32_bf16 v[216:219],v[108:111],a[188:191],v[216:219]
ds_read_b64_tr_b16 v[108:109],v1 offset:30784
ds_read_b64_tr_b16 v[110:111],v1 offset:35904
v_mfma_f32_16x16x32_bf16 v[216:219],v[104:107],a[172:175],v[216:219]
ds_read_b64_tr_b16 v[104:105],v1 offset:20544
v_mfma_f32_16x16x32_bf16 v[216:219],v[100:103],a[156:159],v[216:219]
buffer_load_dwordx4 v25,s[48:51],s84 offen offset:1024 lds
v_mfma_f32_16x16x32_bf16 v[216:219],v[96:99],a[140:143],v[216:219]
s_waitcnt lgkmcnt(3)
ds_read_b64_tr_b16 v[96:97],v1 offset:64
ds_read_b64_tr_b16 v[98:99],v1 offset:5184
v_mfma_f32_16x16x32_bf16 v[224:227],v[112:115],a[128:131],v[224:227]
ds_read_b64_tr_b16 v[100:101],v1 offset:10304
v_mfma_f32_16x16x32_bf16 v[224:227],v[116:119],a[144:147],v[224:227]
ds_read_b64_tr_b16 v[102:103],v1 offset:15424
v_mfma_f32_16x16x32_bf16 v[224:227],v[120:123],a[160:163],v[224:227]
ds_read_b64_tr_b16 v[106:107],v1 offset:25664
v_mfma_f32_16x16x32_bf16 v[224:227],v[124:127],a[176:179],v[224:227]
buffer_load_dwordx4 v[212:215],v6,s[28:31],s21 offen offset:2048 sc1
v_mfma_f32_16x16x32_bf16 v[232:235],v[124:127],a[180:183],v[232:235]
v_mfma_f32_16x16x32_bf16 v[232:235],v[120:123],a[164:167],v[232:235]
v_mfma_f32_16x16x32_bf16 v[232:235],v[116:119],a[148:151],v[232:235]
v_mfma_f32_16x16x32_bf16 v[232:235],v[112:115],a[132:135],v[232:235]
v_mfma_f32_16x16x32_bf16 v[240:243],v[112:115],a[136:139],v[240:243]
v_mfma_f32_16x16x32_bf16 v[240:243],v[116:119],a[152:155],v[240:243]
v_mfma_f32_16x16x32_bf16 v[240:243],v[120:123],a[168:171],v[240:243]
buffer_load_dwordx4 v[228:231],v6,s[28:31],s22 offen offset:2048 sc1
v_mfma_f32_16x16x32_bf16 v[240:243],v[124:127],a[184:187],v[240:243]
v_mfma_f32_16x16x32_bf16 v[248:251],v[124:127],a[188:191],v[248:251]
ds_read_b64_tr_b16 v[124:125],v2 offset:30816
ds_read_b64_tr_b16 v[126:127],v2 offset:35936
v_mfma_f32_16x16x32_bf16 v[248:251],v[120:123],a[172:175],v[248:251]
ds_read_b64_tr_b16 v[120:121],v2 offset:20576
v_mfma_f32_16x16x32_bf16 v[248:251],v[116:119],a[156:159],v[248:251]
ds_read_b64_tr_b16 v[116:117],v2 offset:10336
v_mfma_f32_16x16x32_bf16 v[248:251],v[112:115],a[140:143],v[248:251]
ds_read_b64_tr_b16 v[112:113],v2 offset:96
v_mfma_f32_16x16x32_bf16 a[192:195],v[64:67],a[68:71],a[192:195]
ds_read_b64_tr_b16 v[114:115],v2 offset:5216
ds_read_b64_tr_b16 v[118:119],v2 offset:15456
v_mfma_f32_16x16x32_bf16 a[192:195],v[68:71],a[76:79],a[192:195]
buffer_load_dwordx4 v26,s[48:51],s84 offen offset:2048 lds
v_mfma_f32_16x16x32_bf16 a[192:195],v[72:75],a[84:87],a[192:195]
ds_read_b64_tr_b16 v[122:123],v2 offset:25696
v_mfma_f32_16x16x32_bf16 a[192:195],v[76:79],a[92:95],a[192:195]
v_mfma_f32_16x16x32_bf16 a[196:199],v[76:79],a[124:127],a[196:199]
s_waitcnt vmcnt(28)
v_mfma_f32_16x16x32_bf16 a[196:199],v[72:75],a[116:119],a[196:199]
s_waitcnt vmcnt(19)
v_mfma_f32_16x16x32_bf16 a[196:199],v[68:71],a[108:111],a[196:199]
buffer_load_dwordx4 v[244:247],v6,s[28:31],s23 offen offset:2048 sc1
v_mfma_f32_16x16x32_bf16 a[196:199],v[64:67],a[100:103],a[196:199]
v_mfma_f32_16x16x32_bf16 a[200:203],v[64:67],a[64:67],a[200:203]
v_mfma_f32_16x16x32_bf16 a[200:203],v[68:71],a[72:75],a[200:203]
v_mfma_f32_16x16x32_bf16 a[200:203],v[72:75],a[80:83],a[200:203]
v_mfma_f32_16x16x32_bf16 a[200:203],v[76:79],a[88:91],a[200:203]
v_mfma_f32_16x16x32_bf16 a[204:207],v[76:79],a[120:123],a[204:207]
s_waitcnt vmcnt(15)
ds_read_b64_tr_b16 v[76:77],v1 offset:30848
ds_read_b64_tr_b16 v[78:79],v1 offset:35968
v_mfma_f32_16x16x32_bf16 a[204:207],v[72:75],a[112:115],a[204:207]
buffer_load_dwordx4 v27,s[48:51],s84 offen offset:3072 lds
v_mfma_f32_16x16x32_bf16 a[204:207],v[68:71],a[104:107],a[204:207]
s_add_u32 m0,s86,132096
ds_read_b64_tr_b16 v[68:69],v1 offset:10368
ds_read_b64_tr_b16 v[70:71],v1 offset:15488
v_mfma_f32_16x16x32_bf16 a[204:207],v[64:67],a[96:99],a[204:207]
ds_read_b64_tr_b16 v[64:65],v1 offset:128
v_mfma_f32_16x16x32_bf16 a[208:211],v[80:83],a[68:71],a[208:211]
ds_read_b64_tr_b16 v[66:67],v1 offset:5248
v_mfma_f32_16x16x32_bf16 a[208:211],v[84:87],a[76:79],a[208:211]
ds_read_b64_tr_b16 v[72:73],v1 offset:20608
v_mfma_f32_16x16x32_bf16 a[208:211],v[88:91],a[84:87],a[208:211]
buffer_load_dwordx4 v[188:191],v28,s[28:31],s23 offen offset:3072 sc1
v_mfma_f32_16x16x32_bf16 a[208:211],v[92:95],a[92:95],a[208:211]
ds_read_b64_tr_b16 v[74:75],v1 offset:25728
v_mfma_f32_16x16x32_bf16 a[212:215],v[92:95],a[124:127],a[212:215]
v_mfma_f32_16x16x32_bf16 a[212:215],v[88:91],a[116:119],a[212:215]
v_mfma_f32_16x16x32_bf16 a[212:215],v[84:87],a[108:111],a[212:215]
v_mfma_f32_16x16x32_bf16 a[212:215],v[80:83],a[100:103],a[212:215]
)A"
R"A(v_mfma_f32_16x16x32_bf16 a[216:219],v[80:83],a[64:67],a[216:219]
v_mfma_f32_16x16x32_bf16 a[216:219],v[84:87],a[72:75],a[216:219]
buffer_load_dwordx4 v7,s[48:51],s84 offen lds
v_mfma_f32_16x16x32_bf16 a[216:219],v[88:91],a[80:83],a[216:219]
v_mfma_f32_16x16x32_bf16 a[216:219],v[92:95],a[88:91],a[216:219]
v_mfma_f32_16x16x32_bf16 a[220:223],v[92:95],a[120:123],a[220:223]
ds_read_b64_tr_b16 v[92:93],v2 offset:30880
ds_read_b64_tr_b16 v[94:95],v2 offset:36000
v_mfma_f32_16x16x32_bf16 a[220:223],v[88:91],a[112:115],a[220:223]
ds_read_b64_tr_b16 v[88:89],v2 offset:20640
v_mfma_f32_16x16x32_bf16 a[220:223],v[84:87],a[104:107],a[220:223]
buffer_load_dwordx4 v[172:175],v28,s[28:31],s22 offen offset:3072 sc1
v_mfma_f32_16x16x32_bf16 a[220:223],v[80:83],a[96:99],a[220:223]
s_waitcnt lgkmcnt(3)
ds_read_b64_tr_b16 v[80:81],v2 offset:160
ds_read_b64_tr_b16 v[82:83],v2 offset:5280
v_mfma_f32_16x16x32_bf16 a[224:227],v[96:99],a[68:71],a[224:227]
ds_read_b64_tr_b16 v[84:85],v2 offset:10400
v_mfma_f32_16x16x32_bf16 a[224:227],v[100:103],a[76:79],a[224:227]
ds_read_b64_tr_b16 v[86:87],v2 offset:15520
v_mfma_f32_16x16x32_bf16 a[224:227],v[104:107],a[84:87],a[224:227]
ds_read_b64_tr_b16 v[90:91],v2 offset:25760
v_mfma_f32_16x16x32_bf16 a[224:227],v[108:111],a[92:95],a[224:227]
buffer_load_dwordx4 a[128:131],v6,s[28:31],s87 offen sc1
v_mfma_f32_16x16x32_bf16 a[228:231],v[108:111],a[124:127],a[228:231]
v_mfma_f32_16x16x32_bf16 a[228:231],v[104:107],a[116:119],a[228:231]
v_mfma_f32_16x16x32_bf16 a[228:231],v[100:103],a[108:111],a[228:231]
v_mfma_f32_16x16x32_bf16 a[228:231],v[96:99],a[100:103],a[228:231]
v_mfma_f32_16x16x32_bf16 a[232:235],v[96:99],a[64:67],a[232:235]
buffer_load_dwordx4 v[156:159],v28,s[28:31],s21 offen offset:3072 sc1
v_mfma_f32_16x16x32_bf16 a[232:235],v[100:103],a[72:75],a[232:235]
v_mfma_f32_16x16x32_bf16 a[232:235],v[104:107],a[80:83],a[232:235]
v_mfma_f32_16x16x32_bf16 a[232:235],v[108:111],a[88:91],a[232:235]
v_mfma_f32_16x16x32_bf16 a[236:239],v[108:111],a[120:123],a[236:239]
ds_read_b64_tr_b16 v[108:109],v1 offset:30912
ds_read_b64_tr_b16 v[110:111],v1 offset:36032
v_mfma_f32_16x16x32_bf16 a[236:239],v[104:107],a[112:115],a[236:239]
buffer_load_dwordx4 a[144:147],v6,s[28:31],s88 offen sc1
v_mfma_f32_16x16x32_bf16 a[236:239],v[100:103],a[104:107],a[236:239]
ds_read_b64_tr_b16 v[100:101],v1 offset:10432
ds_read_b64_tr_b16 v[102:103],v1 offset:15552
v_mfma_f32_16x16x32_bf16 a[236:239],v[96:99],a[96:99],a[236:239]
ds_read_b64_tr_b16 v[96:97],v1 offset:192
v_mfma_f32_16x16x32_bf16 a[240:243],v[112:115],a[68:71],a[240:243]
ds_read_b64_tr_b16 v[98:99],v1 offset:5312
v_mfma_f32_16x16x32_bf16 a[240:243],v[116:119],a[76:79],a[240:243]
ds_read_b64_tr_b16 v[104:105],v1 offset:20672
v_mfma_f32_16x16x32_bf16 a[240:243],v[120:123],a[84:87],a[240:243]
buffer_load_dwordx4 v[140:143],v28,s[28:31],s20 offen offset:3072 sc1
v_mfma_f32_16x16x32_bf16 a[240:243],v[124:127],a[92:95],a[240:243]
ds_read_b64_tr_b16 v[106:107],v1 offset:25792
v_add_u32 v1,0xffff6000,v1
v_mfma_f32_16x16x32_bf16 a[244:247],v[124:127],a[124:127],a[244:247]
v_mfma_f32_16x16x32_bf16 a[244:247],v[120:123],a[116:119],a[244:247]
v_mfma_f32_16x16x32_bf16 a[244:247],v[116:119],a[108:111],a[244:247]
v_mfma_f32_16x16x32_bf16 a[244:247],v[112:115],a[100:103],a[244:247]
buffer_load_dwordx4 a[160:163],v6,s[28:31],s89 offen sc1
v_mfma_f32_16x16x32_bf16 a[248:251],v[112:115],a[64:67],a[248:251]
v_mfma_f32_16x16x32_bf16 a[248:251],v[116:119],a[72:75],a[248:251]
v_mfma_f32_16x16x32_bf16 a[248:251],v[120:123],a[80:83],a[248:251]
v_mfma_f32_16x16x32_bf16 a[248:251],v[124:127],a[88:91],a[248:251]
v_mfma_f32_16x16x32_bf16 a[252:255],v[124:127],a[120:123],a[252:255]
buffer_load_dwordx4 a[176:179],v6,s[28:31],s90 offen sc1
v_mfma_f32_16x16x32_bf16 a[252:255],v[120:123],a[112:115],a[252:255]
ds_read_b64_tr_b16 v[120:121],v2 offset:20704
ds_read_b64_tr_b16 v[122:123],v2 offset:25824
v_mfma_f32_16x16x32_bf16 a[252:255],v[116:119],a[104:107],a[252:255]
ds_read_b64_tr_b16 v[116:117],v2 offset:10464
v_mfma_f32_16x16x32_bf16 a[252:255],v[112:115],a[96:99],a[252:255]
ds_read_b64_tr_b16 v[112:113],v2 offset:224
v_mfma_f32_16x16x32_bf16 a[0:3],v[64:67],a[68:71],a[0:3]
ds_read_b64_tr_b16 v[114:115],v2 offset:5344
v_mfma_f32_16x16x32_bf16 a[0:3],v[68:71],a[76:79],a[0:3]
buffer_load_dwordx4 a[180:183],v6,s[28:31],s90 offen offset:1024 sc1
v_mfma_f32_16x16x32_bf16 a[0:3],v[72:75],a[84:87],a[0:3]
ds_read_b64_tr_b16 v[118:119],v2 offset:15584
ds_read_b64_tr_b16 v[124:125],v2 offset:30944
v_mfma_f32_16x16x32_bf16 a[0:3],v[76:79],a[92:95],a[0:3]
ds_read_b64_tr_b16 v[126:127],v2 offset:36064
v_add_u32 v2,0xffff6000,v2
v_mfma_f32_16x16x32_bf16 a[4:7],v[76:79],a[124:127],a[4:7]
v_mfma_f32_16x16x32_bf16 a[4:7],v[72:75],a[116:119],a[4:7]
v_mfma_f32_16x16x32_bf16 a[4:7],v[68:71],a[108:111],a[4:7]
buffer_load_dwordx4 a[164:167],v6,s[28:31],s89 offen offset:1024 sc1
v_mfma_f32_16x16x32_bf16 a[4:7],v[64:67],a[100:103],a[4:7]
v_mfma_f32_16x16x32_bf16 a[8:11],v[64:67],a[64:67],a[8:11]
v_mfma_f32_16x16x32_bf16 a[8:11],v[68:71],a[72:75],a[8:11]
v_mfma_f32_16x16x32_bf16 a[8:11],v[72:75],a[80:83],a[8:11]
v_mfma_f32_16x16x32_bf16 a[8:11],v[76:79],a[88:91],a[8:11]
buffer_load_dwordx4 a[148:151],v6,s[28:31],s88 offen offset:1024 sc1
v_mfma_f32_16x16x32_bf16 a[12:15],v[76:79],a[120:123],a[12:15]
v_mfma_f32_16x16x32_bf16 a[12:15],v[72:75],a[112:115],a[12:15]
v_mfma_f32_16x16x32_bf16 a[12:15],v[68:71],a[104:107],a[12:15]
v_mfma_f32_16x16x32_bf16 a[12:15],v[64:67],a[96:99],a[12:15]
s_waitcnt vmcnt(10)
s_barrier
s_waitcnt lgkmcnt(0)
v_mfma_f32_16x16x32_bf16 a[16:19],v[80:83],a[68:71],a[16:19]
buffer_load_dwordx4 a[132:135],v6,s[28:31],s87 offen offset:1024 sc1
v_mfma_f32_16x16x32_bf16 a[16:19],v[84:87],a[76:79],a[16:19]
ds_read_b64_tr_b16 v[64:65],v1
ds_read_b64_tr_b16 v[66:67],v1 offset:5120
v_mfma_f32_16x16x32_bf16 a[16:19],v[88:91],a[84:87],a[16:19]
ds_read_b64_tr_b16 v[68:69],v1 offset:10240
v_mfma_f32_16x16x32_bf16 a[16:19],v[92:95],a[92:95],a[16:19]
ds_read_b64_tr_b16 v[70:71],v1 offset:15360
v_mfma_f32_16x16x32_bf16 a[20:23],v[92:95],a[124:127],a[20:23]
ds_read_b64_tr_b16 v[72:73],v1 offset:20480
v_mfma_f32_16x16x32_bf16 a[20:23],v[88:91],a[116:119],a[20:23]
buffer_load_dwordx4 a[136:139],v6,s[28:31],s87 offen offset:2048 sc1
v_mfma_f32_16x16x32_bf16 a[20:23],v[84:87],a[108:111],a[20:23]
ds_read_b64_tr_b16 v[74:75],v1 offset:25600
s_nop 0
L_pd17_%=:
s_cbranch_execz L_trtg2F_%=
ds_read_b64_tr_b16 v[76:77],v1 offset:30720
v_mfma_f32_16x16x32_bf16 a[20:23],v[80:83],a[100:103],a[20:23]
ds_read_b64_tr_b16 v[78:79],v1 offset:35840
v_add_u32 v1,0x1e000,v1
v_mfma_f32_16x16x32_bf16 a[24:27],v[80:83],a[64:67],a[24:27]
v_mfma_f32_16x16x32_bf16 a[24:27],v[84:87],a[72:75],a[24:27]
v_mfma_f32_16x16x32_bf16 a[24:27],v[88:91],a[80:83],a[24:27]
buffer_load_dwordx4 a[152:155],v6,s[28:31],s88 offen offset:2048 sc1
v_mfma_f32_16x16x32_bf16 a[24:27],v[92:95],a[88:91],a[24:27]
v_mfma_f32_16x16x32_bf16 a[28:31],v[92:95],a[120:123],a[28:31]
ds_read_b64_tr_b16 v[92:93],v2 offset:30752
ds_read_b64_tr_b16 v[94:95],v2 offset:35872
v_mfma_f32_16x16x32_bf16 a[28:31],v[88:91],a[112:115],a[28:31]
ds_read_b64_tr_b16 v[88:89],v2 offset:20512
v_mfma_f32_16x16x32_bf16 a[28:31],v[84:87],a[104:107],a[28:31]
ds_read_b64_tr_b16 v[84:85],v2 offset:10272
v_mfma_f32_16x16x32_bf16 a[28:31],v[80:83],a[96:99],a[28:31]
buffer_load_dwordx4 a[168:171],v6,s[28:31],s89 offen offset:2048 sc1
v_mfma_f32_16x16x32_bf16 v[128:131],v[96:99],a[68:71],v[128:131]
ds_read_b64_tr_b16 v[80:81],v2 offset:32
ds_read_b64_tr_b16 v[82:83],v2 offset:5152
v_mfma_f32_16x16x32_bf16 v[128:131],v[100:103],a[76:79],v[128:131]
ds_read_b64_tr_b16 v[86:87],v2 offset:15392
v_mfma_f32_16x16x32_bf16 v[128:131],v[104:107],a[84:87],v[128:131]
ds_read_b64_tr_b16 v[90:91],v2 offset:25632
v_add_u32 v2,0x1e000,v2
v_mfma_f32_16x16x32_bf16 v[128:131],v[108:111],a[92:95],v[128:131]
v_mfma_f32_16x16x32_bf16 v[136:139],v[108:111],a[124:127],v[136:139]
buffer_load_dwordx4 a[184:187],v6,s[28:31],s90 offen offset:2048 sc1
v_mfma_f32_16x16x32_bf16 v[136:139],v[104:107],a[116:119],v[136:139]
v_mfma_f32_16x16x32_bf16 v[136:139],v[100:103],a[108:111],v[136:139]
v_mfma_f32_16x16x32_bf16 v[136:139],v[96:99],a[100:103],v[136:139]
v_mfma_f32_16x16x32_bf16 v[144:147],v[96:99],a[64:67],v[144:147]
v_mfma_f32_16x16x32_bf16 v[144:147],v[100:103],a[72:75],v[144:147]
buffer_load_dwordx4 a[188:191],v28,s[28:31],s90 offen offset:3072 sc1
v_mfma_f32_16x16x32_bf16 v[144:147],v[104:107],a[80:83],v[144:147]
v_mfma_f32_16x16x32_bf16 v[144:147],v[108:111],a[88:91],v[144:147]
v_mfma_f32_16x16x32_bf16 v[152:155],v[108:111],a[120:123],v[152:155]
ds_read_b64_tr_b16 v[108:109],v1 offset:30720
ds_read_b64_tr_b16 v[110:111],v1 offset:35840
v_mfma_f32_16x16x32_bf16 v[152:155],v[104:107],a[112:115],v[152:155]
ds_read_b64_tr_b16 v[104:105],v1 offset:20480
v_mfma_f32_16x16x32_bf16 v[152:155],v[100:103],a[104:107],v[152:155]
buffer_load_dwordx4 a[172:175],v28,s[28:31],s89 offen offset:3072 sc1
v_mfma_f32_16x16x32_bf16 v[152:155],v[96:99],a[96:99],v[152:155]
ds_read_b64_tr_b16 v[96:97],v1
ds_read_b64_tr_b16 v[98:99],v1 offset:5120
v_mfma_f32_16x16x32_bf16 v[160:163],v[112:115],a[68:71],v[160:163]
ds_read_b64_tr_b16 v[100:101],v1 offset:10240
v_mfma_f32_16x16x32_bf16 v[160:163],v[116:119],a[76:79],v[160:163]
ds_read_b64_tr_b16 v[102:103],v1 offset:15360
v_mfma_f32_16x16x32_bf16 v[160:163],v[120:123],a[84:87],v[160:163]
ds_read_b64_tr_b16 v[106:107],v1 offset:25600
v_add_u32 v1,0xfffe2000,v1
v_mfma_f32_16x16x32_bf16 v[160:163],v[124:127],a[92:95],v[160:163]
buffer_load_dwordx4 a[156:159],v28,s[28:31],s88 offen offset:3072 sc1
v_mfma_f32_16x16x32_bf16 v[168:171],v[124:127],a[124:127],v[168:171]
v_mfma_f32_16x16x32_bf16 v[168:171],v[120:123],a[116:119],v[168:171]
v_mfma_f32_16x16x32_bf16 v[168:171],v[116:119],a[108:111],v[168:171]
v_mfma_f32_16x16x32_bf16 v[168:171],v[112:115],a[100:103],v[168:171]
v_mfma_f32_16x16x32_bf16 v[176:179],v[112:115],a[64:67],v[176:179]
buffer_load_dwordx4 a[140:143],v28,s[28:31],s87 offen offset:3072 sc1
v_mfma_f32_16x16x32_bf16 v[176:179],v[116:119],a[72:75],v[176:179]
s_mul_i32 s87,s60,81920
s_add_u32 s87,s87,s66
s_add_u32 s83,s83,s87
v_mfma_f32_16x16x32_bf16 v[176:179],v[120:123],a[80:83],v[176:179]
s_lshl_b32 s66,s63,6
s_add_u32 s87,s87,s66
s_add_u32 s88,s87,20480
v_mfma_f32_16x16x32_bf16 v[176:179],v[124:127],a[88:91],v[176:179]
s_add_u32 s89,s87,40960
s_add_u32 s90,s87,61440
s_add_u32 s66,s77,4096
v_mfma_f32_16x16x32_bf16 v[184:187],v[124:127],a[120:123],v[184:187]
ds_read_b64_tr_b16 v[124:125],v2 offset:30752
ds_read_b64_tr_b16 v[126:127],v2 offset:35872
v_mfma_f32_16x16x32_bf16 v[184:187],v[120:123],a[112:115],v[184:187]
ds_read_b64_tr_b16 v[120:121],v2 offset:20512
v_mfma_f32_16x16x32_bf16 v[184:187],v[116:119],a[104:107],v[184:187]
ds_read_b64_tr_b16 v[116:117],v2 offset:10272
v_mfma_f32_16x16x32_bf16 v[184:187],v[112:115],a[96:99],v[184:187]
s_waitcnt lgkmcnt(4)
ds_read_b64_tr_b16 v[112:113],v2 offset:32
v_mfma_f32_16x16x32_bf16 a[192:195],v[64:67],v[32:35],a[192:195]
ds_read_b64_tr_b16 v[114:115],v2 offset:5152
ds_read_b64_tr_b16 v[118:119],v2 offset:15392
v_mfma_f32_16x16x32_bf16 a[192:195],v[68:71],v[36:39],a[192:195]
ds_read_b64_tr_b16 v[122:123],v2 offset:25632
v_add_u32 v2,0xfffe2000,v2
v_mfma_f32_16x16x32_bf16 a[192:195],v[72:75],v[40:43],a[192:195]
v_mfma_f32_16x16x32_bf16 a[192:195],v[76:79],v[44:47],a[192:195]
v_mfma_f32_16x16x32_bf16 a[196:199],v[76:79],v[180:183],a[196:199]
v_mfma_f32_16x16x32_bf16 a[196:199],v[72:75],v[164:167],a[196:199]
v_mfma_f32_16x16x32_bf16 a[196:199],v[68:71],v[148:151],a[196:199]
v_mfma_f32_16x16x32_bf16 a[196:199],v[64:67],v[132:135],a[196:199]
v_mfma_f32_16x16x32_bf16 a[200:203],v[64:67],v[196:199],a[200:203]
v_mfma_f32_16x16x32_bf16 a[200:203],v[68:71],v[212:215],a[200:203]
v_mfma_f32_16x16x32_bf16 a[200:203],v[72:75],v[228:231],a[200:203]
v_mfma_f32_16x16x32_bf16 a[200:203],v[76:79],v[244:247],a[200:203]
v_mfma_f32_16x16x32_bf16 a[204:207],v[76:79],v[188:191],a[204:207]
s_waitcnt vmcnt(18)
ds_read_b64_tr_b16 v[76:77],v1 offset:30784
)A"
R"A(ds_read_b64_tr_b16 v[78:79],v1 offset:35904
v_mfma_f32_16x16x32_bf16 a[204:207],v[72:75],v[172:175],a[204:207]
s_waitcnt vmcnt(16)
ds_read_b64_tr_b16 v[72:73],v1 offset:20544
v_mfma_f32_16x16x32_bf16 a[204:207],v[68:71],v[156:159],a[204:207]
s_waitcnt vmcnt(14)
ds_read_b64_tr_b16 v[68:69],v1 offset:10304
v_mfma_f32_16x16x32_bf16 a[204:207],v[64:67],v[140:143],a[204:207]
ds_read_b64_tr_b16 v[64:65],v1 offset:64
v_mfma_f32_16x16x32_bf16 a[208:211],v[80:83],v[32:35],a[208:211]
ds_read_b64_tr_b16 v[66:67],v1 offset:5184
ds_read_b64_tr_b16 v[70:71],v1 offset:15424
v_mfma_f32_16x16x32_bf16 a[208:211],v[84:87],v[36:39],a[208:211]
ds_read_b64_tr_b16 v[74:75],v1 offset:25664
v_add_u32 v1,0x1e000,v1
v_mfma_f32_16x16x32_bf16 a[208:211],v[88:91],v[40:43],a[208:211]
v_mfma_f32_16x16x32_bf16 a[208:211],v[92:95],v[44:47],a[208:211]
v_mfma_f32_16x16x32_bf16 a[212:215],v[92:95],v[180:183],a[212:215]
v_mfma_f32_16x16x32_bf16 a[212:215],v[88:91],v[164:167],a[212:215]
v_mfma_f32_16x16x32_bf16 a[212:215],v[84:87],v[148:151],a[212:215]
v_mfma_f32_16x16x32_bf16 a[212:215],v[80:83],v[132:135],a[212:215]
v_mfma_f32_16x16x32_bf16 a[216:219],v[80:83],v[196:199],a[216:219]
v_mfma_f32_16x16x32_bf16 a[216:219],v[84:87],v[212:215],a[216:219]
v_mfma_f32_16x16x32_bf16 a[216:219],v[88:91],v[228:231],a[216:219]
v_mfma_f32_16x16x32_bf16 a[216:219],v[92:95],v[244:247],a[216:219]
v_mfma_f32_16x16x32_bf16 a[220:223],v[92:95],v[188:191],a[220:223]
ds_read_b64_tr_b16 v[92:93],v2 offset:30816
ds_read_b64_tr_b16 v[94:95],v2 offset:35936
v_mfma_f32_16x16x32_bf16 a[220:223],v[88:91],v[172:175],a[220:223]
ds_read_b64_tr_b16 v[88:89],v2 offset:20576
v_mfma_f32_16x16x32_bf16 a[220:223],v[84:87],v[156:159],a[220:223]
ds_read_b64_tr_b16 v[84:85],v2 offset:10336
v_mfma_f32_16x16x32_bf16 a[220:223],v[80:83],v[140:143],a[220:223]
ds_read_b64_tr_b16 v[80:81],v2 offset:96
v_mfma_f32_16x16x32_bf16 a[192:195],v[96:99],a[128:131],a[192:195]
ds_read_b64_tr_b16 v[82:83],v2 offset:5216
ds_read_b64_tr_b16 v[86:87],v2 offset:15456
v_mfma_f32_16x16x32_bf16 a[192:195],v[100:103],a[144:147],a[192:195]
s_waitcnt vmcnt(13)
ds_read_b64_tr_b16 v[90:91],v2 offset:25696
v_add_u32 v2,0x1e000,v2
v_mfma_f32_16x16x32_bf16 a[192:195],v[104:107],a[160:163],a[192:195]
s_waitcnt vmcnt(12)
v_mfma_f32_16x16x32_bf16 a[192:195],v[108:111],a[176:179],a[192:195]
s_waitcnt vmcnt(11)
v_mfma_f32_16x16x32_bf16 a[196:199],v[108:111],a[180:183],a[196:199]
s_waitcnt vmcnt(10)
v_mfma_f32_16x16x32_bf16 a[196:199],v[104:107],a[164:167],a[196:199]
s_waitcnt vmcnt(9)
v_mfma_f32_16x16x32_bf16 a[196:199],v[100:103],a[148:151],a[196:199]
s_waitcnt vmcnt(8)
v_mfma_f32_16x16x32_bf16 a[196:199],v[96:99],a[132:135],a[196:199]
s_waitcnt vmcnt(7)
v_mfma_f32_16x16x32_bf16 a[200:203],v[96:99],a[136:139],a[200:203]
s_waitcnt vmcnt(6)
v_mfma_f32_16x16x32_bf16 a[200:203],v[100:103],a[152:155],a[200:203]
s_waitcnt vmcnt(5)
v_mfma_f32_16x16x32_bf16 a[200:203],v[104:107],a[168:171],a[200:203]
s_waitcnt vmcnt(4)
v_mfma_f32_16x16x32_bf16 a[200:203],v[108:111],a[184:187],a[200:203]
s_waitcnt vmcnt(3)
v_accvgpr_read_b32 v16,a192
v_accvgpr_read_b32 v17,a193
v_mfma_f32_16x16x32_bf16 a[204:207],v[108:111],a[188:191],a[204:207]
s_waitcnt vmcnt(2)
v_accvgpr_read_b32 v18,a194
v_accvgpr_read_b32 v19,a195
v_mfma_f32_16x16x32_bf16 a[204:207],v[104:107],a[172:175],a[204:207]
s_waitcnt vmcnt(1)
v_cvt_pk_bf16_f32 v16,v16,v17
v_cvt_pk_bf16_f32 v17,v18,v19
v_mfma_f32_16x16x32_bf16 a[204:207],v[100:103],a[156:159],a[204:207]
s_waitcnt vmcnt(0)
v_accvgpr_read_b32 v8,a196
v_accvgpr_read_b32 v9,a197
v_mfma_f32_16x16x32_bf16 a[204:207],v[96:99],a[140:143],a[204:207]
s_waitcnt lgkmcnt(0)
v_accvgpr_read_b32 v10,a198
v_accvgpr_read_b32 v11,a199
v_mfma_f32_16x16x32_bf16 a[208:211],v[112:115],a[128:131],a[208:211]
v_cvt_pk_bf16_f32 v8,v8,v9
v_cvt_pk_bf16_f32 v9,v10,v11
v_mfma_f32_16x16x32_bf16 a[208:211],v[116:119],a[144:147],a[208:211]
ds_read_b64_tr_b16 v[96:97],v1 offset:64
ds_read_b64_tr_b16 v[98:99],v1 offset:5184
v_mfma_f32_16x16x32_bf16 a[208:211],v[120:123],a[160:163],a[208:211]
ds_read_b64_tr_b16 v[100:101],v1 offset:10304
v_mfma_f32_16x16x32_bf16 a[208:211],v[124:127],a[176:179],a[208:211]
ds_read_b64_tr_b16 v[102:103],v1 offset:15424
v_mfma_f32_16x16x32_bf16 a[212:215],v[124:127],a[180:183],a[212:215]
ds_read_b64_tr_b16 v[104:105],v1 offset:20544
v_mfma_f32_16x16x32_bf16 a[212:215],v[120:123],a[164:167],a[212:215]
ds_read_b64_tr_b16 v[106:107],v1 offset:25664
ds_read_b64_tr_b16 v[108:109],v1 offset:30784
v_mfma_f32_16x16x32_bf16 a[212:215],v[116:119],a[148:151],a[212:215]
ds_read_b64_tr_b16 v[110:111],v1 offset:35904
v_add_u32 v1,0xfffe2000,v1
v_mfma_f32_16x16x32_bf16 a[212:215],v[112:115],a[132:135],a[212:215]
v_accvgpr_read_b32 v20,a208
v_accvgpr_read_b32 v21,a209
v_mfma_f32_16x16x32_bf16 a[216:219],v[112:115],a[136:139],a[216:219]
v_accvgpr_read_b32 v22,a210
v_accvgpr_read_b32 v23,a211
v_mfma_f32_16x16x32_bf16 a[216:219],v[116:119],a[152:155],a[216:219]
v_cvt_pk_bf16_f32 v18,v20,v21
v_cvt_pk_bf16_f32 v19,v22,v23
v_mfma_f32_16x16x32_bf16 a[216:219],v[120:123],a[168:171],a[216:219]
buffer_store_dwordx4 v[16:19],v6,s[36:39],s74 offen sc1
v_mfma_f32_16x16x32_bf16 a[216:219],v[124:127],a[184:187],a[216:219]
v_accvgpr_read_b32 v12,a212
v_accvgpr_read_b32 v13,a213
v_mfma_f32_16x16x32_bf16 a[220:223],v[124:127],a[188:191],a[220:223]
v_accvgpr_read_b32 v14,a214
v_accvgpr_read_b32 v15,a215
v_mfma_f32_16x16x32_bf16 a[220:223],v[120:123],a[172:175],a[220:223]
v_cvt_pk_bf16_f32 v10,v12,v13
v_cvt_pk_bf16_f32 v11,v14,v15
v_mfma_f32_16x16x32_bf16 a[220:223],v[116:119],a[156:159],a[220:223]
v_accvgpr_read_b32 v16,a200
v_accvgpr_read_b32 v17,a201
v_mfma_f32_16x16x32_bf16 a[220:223],v[112:115],a[140:143],a[220:223]
v_accvgpr_read_b32 v18,a202
v_accvgpr_read_b32 v19,a203
v_mfma_f32_16x16x32_bf16 a[224:227],v[64:67],v[32:35],a[224:227]
v_accvgpr_read_b32 v20,a216
v_accvgpr_read_b32 v21,a217
v_mfma_f32_16x16x32_bf16 a[224:227],v[68:71],v[36:39],a[224:227]
v_accvgpr_read_b32 v22,a218
v_accvgpr_read_b32 v23,a219
v_mfma_f32_16x16x32_bf16 a[224:227],v[72:75],v[40:43],a[224:227]
v_cvt_pk_bf16_f32 v16,v16,v17
v_cvt_pk_bf16_f32 v17,v18,v19
v_mfma_f32_16x16x32_bf16 a[224:227],v[76:79],v[44:47],a[224:227]
v_cvt_pk_bf16_f32 v18,v20,v21
v_cvt_pk_bf16_f32 v19,v22,v23
v_mfma_f32_16x16x32_bf16 a[228:231],v[76:79],v[180:183],a[228:231]
ds_read_b64_tr_b16 v[112:113],v2 offset:96
ds_read_b64_tr_b16 v[114:115],v2 offset:5216
v_mfma_f32_16x16x32_bf16 a[228:231],v[72:75],v[164:167],a[228:231]
buffer_store_dwordx4 v[8:11],v6,s[36:39],s74 offen offset:1024 sc1
v_mfma_f32_16x16x32_bf16 a[228:231],v[68:71],v[148:151],a[228:231]
ds_read_b64_tr_b16 v[116:117],v2 offset:10336
ds_read_b64_tr_b16 v[118:119],v2 offset:15456
v_mfma_f32_16x16x32_bf16 a[228:231],v[64:67],v[132:135],a[228:231]
ds_read_b64_tr_b16 v[120:121],v2 offset:20576
v_accvgpr_read_b32 v8,a204
v_mfma_f32_16x16x32_bf16 a[232:235],v[64:67],v[196:199],a[232:235]
ds_read_b64_tr_b16 v[122:123],v2 offset:25696
v_accvgpr_read_b32 v9,a205
v_mfma_f32_16x16x32_bf16 a[232:235],v[68:71],v[212:215],a[232:235]
ds_read_b64_tr_b16 v[124:125],v2 offset:30816
v_accvgpr_read_b32 v10,a206
v_mfma_f32_16x16x32_bf16 a[232:235],v[72:75],v[228:231],a[232:235]
ds_read_b64_tr_b16 v[126:127],v2 offset:35936
v_accvgpr_read_b32 v11,a207
v_mfma_f32_16x16x32_bf16 a[232:235],v[76:79],v[244:247],a[232:235]
v_accvgpr_read_b32 v12,a220
v_accvgpr_read_b32 v13,a221
v_mfma_f32_16x16x32_bf16 a[236:239],v[76:79],v[188:191],a[236:239]
v_accvgpr_read_b32 v14,a222
v_accvgpr_read_b32 v15,a223
v_mfma_f32_16x16x32_bf16 a[236:239],v[72:75],v[172:175],a[236:239]
v_cvt_pk_bf16_f32 v8,v8,v9
v_cvt_pk_bf16_f32 v9,v10,v11
v_mfma_f32_16x16x32_bf16 a[236:239],v[68:71],v[156:159],a[236:239]
v_cvt_pk_bf16_f32 v10,v12,v13
v_cvt_pk_bf16_f32 v11,v14,v15
v_mfma_f32_16x16x32_bf16 a[236:239],v[64:67],v[140:143],a[236:239]
ds_read_b64_tr_b16 v[64:65],v1 offset:128
ds_read_b64_tr_b16 v[66:67],v1 offset:5248
v_mfma_f32_16x16x32_bf16 a[240:243],v[80:83],v[32:35],a[240:243]
buffer_store_dwordx4 v[16:19],v6,s[36:39],s74 offen offset:2048 sc1
v_mfma_f32_16x16x32_bf16 a[240:243],v[84:87],v[36:39],a[240:243]
ds_read_b64_tr_b16 v[68:69],v1 offset:10368
ds_read_b64_tr_b16 v[70:71],v1 offset:15488
v_mfma_f32_16x16x32_bf16 a[240:243],v[88:91],v[40:43],a[240:243]
ds_read_b64_tr_b16 v[72:73],v1 offset:20608
v_add_u32 v2,0xfffe2000,v2
v_mfma_f32_16x16x32_bf16 a[240:243],v[92:95],v[44:47],a[240:243]
ds_read_b64_tr_b16 v[74:75],v1 offset:25728
v_mfma_f32_16x16x32_bf16 a[244:247],v[92:95],v[180:183],a[244:247]
ds_read_b64_tr_b16 v[76:77],v1 offset:30848
v_mfma_f32_16x16x32_bf16 a[244:247],v[88:91],v[164:167],a[244:247]
ds_read_b64_tr_b16 v[78:79],v1 offset:35968
v_add_u32 v1,0x1e000,v1
v_mfma_f32_16x16x32_bf16 a[244:247],v[84:87],v[148:151],a[244:247]
v_mfma_f32_16x16x32_bf16 a[244:247],v[80:83],v[132:135],a[244:247]
v_mfma_f32_16x16x32_bf16 a[248:251],v[80:83],v[196:199],a[248:251]
v_mfma_f32_16x16x32_bf16 a[248:251],v[84:87],v[212:215],a[248:251]
v_mfma_f32_16x16x32_bf16 a[248:251],v[88:91],v[228:231],a[248:251]
v_mfma_f32_16x16x32_bf16 a[248:251],v[92:95],v[244:247],a[248:251]
buffer_store_dwordx4 v[8:11],v28,s[36:39],s74 offen offset:3072 sc1
v_mfma_f32_16x16x32_bf16 a[252:255],v[92:95],v[188:191],a[252:255]
ds_read_b64_tr_b16 v[92:93],v2 offset:30880
ds_read_b64_tr_b16 v[94:95],v2 offset:36000
v_mfma_f32_16x16x32_bf16 a[252:255],v[88:91],v[172:175],a[252:255]
ds_read_b64_tr_b16 v[88:89],v2 offset:20640
v_mfma_f32_16x16x32_bf16 a[252:255],v[84:87],v[156:159],a[252:255]
ds_read_b64_tr_b16 v[84:85],v2 offset:10400
v_mfma_f32_16x16x32_bf16 a[252:255],v[80:83],v[140:143],a[252:255]
s_waitcnt lgkmcnt(4)
ds_read_b64_tr_b16 v[80:81],v2 offset:160
v_mfma_f32_16x16x32_bf16 a[224:227],v[96:99],a[128:131],a[224:227]
ds_read_b64_tr_b16 v[82:83],v2 offset:5280
ds_read_b64_tr_b16 v[86:87],v2 offset:15520
v_mfma_f32_16x16x32_bf16 a[224:227],v[100:103],a[144:147],a[224:227]
ds_read_b64_tr_b16 v[90:91],v2 offset:25760
v_add_u32 v2,0x1e000,v2
v_mfma_f32_16x16x32_bf16 a[224:227],v[104:107],a[160:163],a[224:227]
v_mfma_f32_16x16x32_bf16 a[224:227],v[108:111],a[176:179],a[224:227]
v_mfma_f32_16x16x32_bf16 a[228:231],v[108:111],a[180:183],a[228:231]
v_mfma_f32_16x16x32_bf16 a[228:231],v[104:107],a[164:167],a[228:231]
v_mfma_f32_16x16x32_bf16 a[228:231],v[100:103],a[148:151],a[228:231]
v_mfma_f32_16x16x32_bf16 a[228:231],v[96:99],a[132:135],a[228:231]
v_mfma_f32_16x16x32_bf16 a[232:235],v[96:99],a[136:139],a[232:235]
v_mfma_f32_16x16x32_bf16 a[232:235],v[100:103],a[152:155],a[232:235]
v_mfma_f32_16x16x32_bf16 a[232:235],v[104:107],a[168:171],a[232:235]
v_mfma_f32_16x16x32_bf16 a[232:235],v[108:111],a[184:187],a[232:235]
v_accvgpr_read_b32 v16,a224
v_accvgpr_read_b32 v17,a225
v_mfma_f32_16x16x32_bf16 a[236:239],v[108:111],a[188:191],a[236:239]
v_accvgpr_read_b32 v18,a226
v_accvgpr_read_b32 v19,a227
v_mfma_f32_16x16x32_bf16 a[236:239],v[104:107],a[172:175],a[236:239]
v_cvt_pk_bf16_f32 v16,v16,v17
v_cvt_pk_bf16_f32 v17,v18,v19
v_mfma_f32_16x16x32_bf16 a[236:239],v[100:103],a[156:159],a[236:239]
v_accvgpr_read_b32 v8,a228
v_accvgpr_read_b32 v9,a229
v_mfma_f32_16x16x32_bf16 a[236:239],v[96:99],a[140:143],a[236:239]
v_accvgpr_read_b32 v10,a230
v_accvgpr_read_b32 v11,a231
v_mfma_f32_16x16x32_bf16 a[240:243],v[112:115],a[128:131],a[240:243]
v_cvt_pk_bf16_f32 v8,v8,v9
v_cvt_pk_bf16_f32 v9,v10,v11
v_mfma_f32_16x16x32_bf16 a[240:243],v[116:119],a[144:147],a[240:243]
ds_read_b64_tr_b16 v[96:97],v1 offset:128
ds_read_b64_tr_b16 v[98:99],v1 offset:5248
v_mfma_f32_16x16x32_bf16 a[240:243],v[120:123],a[160:163],a[240:243]
ds_read_b64_tr_b16 v[100:101],v1 offset:10368
v_mfma_f32_16x16x32_bf16 a[240:243],v[124:127],a[176:179],a[240:243]
ds_read_b64_tr_b16 v[102:103],v1 offset:15488
v_mfma_f32_16x16x32_bf16 a[244:247],v[124:127],a[180:183],a[244:247]
ds_read_b64_tr_b16 v[104:105],v1 offset:20608
v_mfma_f32_16x16x32_bf16 a[244:247],v[120:123],a[164:167],a[244:247]
ds_read_b64_tr_b16 v[106:107],v1 offset:25728
ds_read_b64_tr_b16 v[108:109],v1 offset:30848
v_mfma_f32_16x16x32_bf16 a[244:247],v[116:119],a[148:151],a[244:247]
)A"
R"A(ds_read_b64_tr_b16 v[110:111],v1 offset:35968
v_add_u32 v1,0xfffe2000,v1
v_mfma_f32_16x16x32_bf16 a[244:247],v[112:115],a[132:135],a[244:247]
v_accvgpr_read_b32 v20,a240
v_accvgpr_read_b32 v21,a241
v_mfma_f32_16x16x32_bf16 a[248:251],v[112:115],a[136:139],a[248:251]
v_accvgpr_read_b32 v22,a242
v_accvgpr_read_b32 v23,a243
v_mfma_f32_16x16x32_bf16 a[248:251],v[116:119],a[152:155],a[248:251]
v_cvt_pk_bf16_f32 v18,v20,v21
v_cvt_pk_bf16_f32 v19,v22,v23
v_mfma_f32_16x16x32_bf16 a[248:251],v[120:123],a[168:171],a[248:251]
buffer_store_dwordx4 v[16:19],v6,s[36:39],s75 offen sc1
v_mfma_f32_16x16x32_bf16 a[248:251],v[124:127],a[184:187],a[248:251]
v_accvgpr_read_b32 v12,a244
v_accvgpr_read_b32 v13,a245
v_mfma_f32_16x16x32_bf16 a[252:255],v[124:127],a[188:191],a[252:255]
v_accvgpr_read_b32 v14,a246
v_accvgpr_read_b32 v15,a247
v_mfma_f32_16x16x32_bf16 a[252:255],v[120:123],a[172:175],a[252:255]
v_cvt_pk_bf16_f32 v10,v12,v13
v_cvt_pk_bf16_f32 v11,v14,v15
v_mfma_f32_16x16x32_bf16 a[252:255],v[116:119],a[156:159],a[252:255]
v_accvgpr_read_b32 v16,a232
v_accvgpr_read_b32 v17,a233
v_mfma_f32_16x16x32_bf16 a[252:255],v[112:115],a[140:143],a[252:255]
v_accvgpr_read_b32 v18,a234
v_accvgpr_read_b32 v19,a235
v_mfma_f32_16x16x32_bf16 a[0:3],v[64:67],v[32:35],a[0:3]
v_accvgpr_read_b32 v20,a248
v_accvgpr_read_b32 v21,a249
v_mfma_f32_16x16x32_bf16 a[0:3],v[68:71],v[36:39],a[0:3]
v_accvgpr_read_b32 v22,a250
v_accvgpr_read_b32 v23,a251
v_mfma_f32_16x16x32_bf16 a[0:3],v[72:75],v[40:43],a[0:3]
v_cvt_pk_bf16_f32 v16,v16,v17
v_cvt_pk_bf16_f32 v17,v18,v19
v_mfma_f32_16x16x32_bf16 a[0:3],v[76:79],v[44:47],a[0:3]
v_cvt_pk_bf16_f32 v18,v20,v21
v_cvt_pk_bf16_f32 v19,v22,v23
v_mfma_f32_16x16x32_bf16 a[4:7],v[76:79],v[180:183],a[4:7]
ds_read_b64_tr_b16 v[112:113],v2 offset:160
ds_read_b64_tr_b16 v[114:115],v2 offset:5280
v_mfma_f32_16x16x32_bf16 a[4:7],v[72:75],v[164:167],a[4:7]
buffer_store_dwordx4 v[8:11],v6,s[36:39],s75 offen offset:1024 sc1
v_mfma_f32_16x16x32_bf16 a[4:7],v[68:71],v[148:151],a[4:7]
ds_read_b64_tr_b16 v[116:117],v2 offset:10400
ds_read_b64_tr_b16 v[118:119],v2 offset:15520
v_mfma_f32_16x16x32_bf16 a[4:7],v[64:67],v[132:135],a[4:7]
ds_read_b64_tr_b16 v[120:121],v2 offset:20640
v_accvgpr_read_b32 v8,a236
v_mfma_f32_16x16x32_bf16 a[8:11],v[64:67],v[196:199],a[8:11]
ds_read_b64_tr_b16 v[122:123],v2 offset:25760
v_accvgpr_read_b32 v9,a237
v_mfma_f32_16x16x32_bf16 a[8:11],v[68:71],v[212:215],a[8:11]
ds_read_b64_tr_b16 v[124:125],v2 offset:30880
v_accvgpr_read_b32 v10,a238
v_mfma_f32_16x16x32_bf16 a[8:11],v[72:75],v[228:231],a[8:11]
ds_read_b64_tr_b16 v[126:127],v2 offset:36000
v_accvgpr_read_b32 v11,a239
v_mfma_f32_16x16x32_bf16 a[8:11],v[76:79],v[244:247],a[8:11]
v_accvgpr_read_b32 v12,a252
v_accvgpr_read_b32 v13,a253
v_mfma_f32_16x16x32_bf16 a[12:15],v[76:79],v[188:191],a[12:15]
v_accvgpr_read_b32 v14,a254
v_accvgpr_read_b32 v15,a255
v_mfma_f32_16x16x32_bf16 a[12:15],v[72:75],v[172:175],a[12:15]
v_cvt_pk_bf16_f32 v8,v8,v9
v_cvt_pk_bf16_f32 v9,v10,v11
v_mfma_f32_16x16x32_bf16 a[12:15],v[68:71],v[156:159],a[12:15]
v_cvt_pk_bf16_f32 v10,v12,v13
v_cvt_pk_bf16_f32 v11,v14,v15
v_mfma_f32_16x16x32_bf16 a[12:15],v[64:67],v[140:143],a[12:15]
s_waitcnt lgkmcnt(3)
ds_read_b64_tr_b16 v[64:65],v1 offset:192
ds_read_b64_tr_b16 v[66:67],v1 offset:5312
v_mfma_f32_16x16x32_bf16 a[16:19],v[80:83],v[32:35],a[16:19]
buffer_store_dwordx4 v[16:19],v6,s[36:39],s75 offen offset:2048 sc1
v_mfma_f32_16x16x32_bf16 a[16:19],v[84:87],v[36:39],a[16:19]
ds_read_b64_tr_b16 v[68:69],v1 offset:10432
ds_read_b64_tr_b16 v[70:71],v1 offset:15552
v_mfma_f32_16x16x32_bf16 a[16:19],v[88:91],v[40:43],a[16:19]
ds_read_b64_tr_b16 v[72:73],v1 offset:20672
v_add_u32 v2,0xfffe2000,v2
v_mfma_f32_16x16x32_bf16 a[16:19],v[92:95],v[44:47],a[16:19]
ds_read_b64_tr_b16 v[74:75],v1 offset:25792
v_mfma_f32_16x16x32_bf16 a[20:23],v[92:95],v[180:183],a[20:23]
ds_read_b64_tr_b16 v[76:77],v1 offset:30912
v_mfma_f32_16x16x32_bf16 a[20:23],v[88:91],v[164:167],a[20:23]
ds_read_b64_tr_b16 v[78:79],v1 offset:36032
v_add_u32 v1,0x1e000,v1
v_mfma_f32_16x16x32_bf16 a[20:23],v[84:87],v[148:151],a[20:23]
v_mfma_f32_16x16x32_bf16 a[20:23],v[80:83],v[132:135],a[20:23]
v_mfma_f32_16x16x32_bf16 a[24:27],v[80:83],v[196:199],a[24:27]
v_mfma_f32_16x16x32_bf16 a[24:27],v[84:87],v[212:215],a[24:27]
v_mfma_f32_16x16x32_bf16 a[24:27],v[88:91],v[228:231],a[24:27]
v_mfma_f32_16x16x32_bf16 a[24:27],v[92:95],v[244:247],a[24:27]
buffer_store_dwordx4 v[8:11],v28,s[36:39],s75 offen offset:3072 sc1
v_mfma_f32_16x16x32_bf16 a[28:31],v[92:95],v[188:191],a[28:31]
ds_read_b64_tr_b16 v[92:93],v2 offset:30944
ds_read_b64_tr_b16 v[94:95],v2 offset:36064
v_mfma_f32_16x16x32_bf16 a[28:31],v[88:91],v[172:175],a[28:31]
ds_read_b64_tr_b16 v[88:89],v2 offset:20704
v_mfma_f32_16x16x32_bf16 a[28:31],v[84:87],v[156:159],a[28:31]
ds_read_b64_tr_b16 v[84:85],v2 offset:10464
v_mfma_f32_16x16x32_bf16 a[28:31],v[80:83],v[140:143],a[28:31]
ds_read_b64_tr_b16 v[80:81],v2 offset:224
v_mfma_f32_16x16x32_bf16 a[0:3],v[96:99],a[128:131],a[0:3]
ds_read_b64_tr_b16 v[82:83],v2 offset:5344
ds_read_b64_tr_b16 v[86:87],v2 offset:15584
v_mfma_f32_16x16x32_bf16 a[0:3],v[100:103],a[144:147],a[0:3]
ds_read_b64_tr_b16 v[90:91],v2 offset:25824
v_add_u32 v2,0x1e000,v2
v_mfma_f32_16x16x32_bf16 a[0:3],v[104:107],a[160:163],a[0:3]
v_mfma_f32_16x16x32_bf16 a[0:3],v[108:111],a[176:179],a[0:3]
v_mfma_f32_16x16x32_bf16 a[4:7],v[108:111],a[180:183],a[4:7]
v_mfma_f32_16x16x32_bf16 a[4:7],v[104:107],a[164:167],a[4:7]
v_mfma_f32_16x16x32_bf16 a[4:7],v[100:103],a[148:151],a[4:7]
v_mfma_f32_16x16x32_bf16 a[4:7],v[96:99],a[132:135],a[4:7]
v_mfma_f32_16x16x32_bf16 a[8:11],v[96:99],a[136:139],a[8:11]
v_mfma_f32_16x16x32_bf16 a[8:11],v[100:103],a[152:155],a[8:11]
v_mfma_f32_16x16x32_bf16 a[8:11],v[104:107],a[168:171],a[8:11]
v_mfma_f32_16x16x32_bf16 a[8:11],v[108:111],a[184:187],a[8:11]
v_accvgpr_read_b32 v16,a0
v_accvgpr_read_b32 v17,a1
v_mfma_f32_16x16x32_bf16 a[12:15],v[108:111],a[188:191],a[12:15]
v_accvgpr_read_b32 v18,a2
v_accvgpr_read_b32 v19,a3
v_mfma_f32_16x16x32_bf16 a[12:15],v[104:107],a[172:175],a[12:15]
v_cvt_pk_bf16_f32 v16,v16,v17
v_cvt_pk_bf16_f32 v17,v18,v19
v_mfma_f32_16x16x32_bf16 a[12:15],v[100:103],a[156:159],a[12:15]
v_accvgpr_read_b32 v8,a4
v_accvgpr_read_b32 v9,a5
v_mfma_f32_16x16x32_bf16 a[12:15],v[96:99],a[140:143],a[12:15]
v_accvgpr_read_b32 v10,a6
v_accvgpr_read_b32 v11,a7
v_mfma_f32_16x16x32_bf16 a[16:19],v[112:115],a[128:131],a[16:19]
v_cvt_pk_bf16_f32 v8,v8,v9
v_cvt_pk_bf16_f32 v9,v10,v11
v_mfma_f32_16x16x32_bf16 a[16:19],v[116:119],a[144:147],a[16:19]
s_waitcnt lgkmcnt(0)
ds_read_b64_tr_b16 v[96:97],v1 offset:192
ds_read_b64_tr_b16 v[98:99],v1 offset:5312
v_mfma_f32_16x16x32_bf16 a[16:19],v[120:123],a[160:163],a[16:19]
ds_read_b64_tr_b16 v[100:101],v1 offset:10432
v_mfma_f32_16x16x32_bf16 a[16:19],v[124:127],a[176:179],a[16:19]
ds_read_b64_tr_b16 v[102:103],v1 offset:15552
v_mfma_f32_16x16x32_bf16 a[20:23],v[124:127],a[180:183],a[20:23]
ds_read_b64_tr_b16 v[104:105],v1 offset:20672
v_mfma_f32_16x16x32_bf16 a[20:23],v[120:123],a[164:167],a[20:23]
ds_read_b64_tr_b16 v[106:107],v1 offset:25792
ds_read_b64_tr_b16 v[108:109],v1 offset:30912
v_mfma_f32_16x16x32_bf16 a[20:23],v[116:119],a[148:151],a[20:23]
ds_read_b64_tr_b16 v[110:111],v1 offset:36032
v_add_u32 v1,0xfffec000,v1
v_mfma_f32_16x16x32_bf16 a[20:23],v[112:115],a[132:135],a[20:23]
v_accvgpr_read_b32 v20,a16
v_accvgpr_read_b32 v21,a17
v_mfma_f32_16x16x32_bf16 a[24:27],v[112:115],a[136:139],a[24:27]
v_accvgpr_read_b32 v22,a18
v_accvgpr_read_b32 v23,a19
v_mfma_f32_16x16x32_bf16 a[24:27],v[116:119],a[152:155],a[24:27]
v_cvt_pk_bf16_f32 v18,v20,v21
v_cvt_pk_bf16_f32 v19,v22,v23
v_mfma_f32_16x16x32_bf16 a[24:27],v[120:123],a[168:171],a[24:27]
buffer_store_dwordx4 v[16:19],v6,s[36:39],s76 offen sc1
v_mfma_f32_16x16x32_bf16 a[24:27],v[124:127],a[184:187],a[24:27]
v_accvgpr_read_b32 v12,a20
v_accvgpr_read_b32 v13,a21
v_mfma_f32_16x16x32_bf16 a[28:31],v[124:127],a[188:191],a[28:31]
v_accvgpr_read_b32 v14,a22
v_accvgpr_read_b32 v15,a23
s_nop 0
L_pd18_%=:
s_cbranch_execz L_trtg2F_%=
v_mfma_f32_16x16x32_bf16 a[28:31],v[120:123],a[172:175],a[28:31]
v_cvt_pk_bf16_f32 v10,v12,v13
v_cvt_pk_bf16_f32 v11,v14,v15
v_mfma_f32_16x16x32_bf16 a[28:31],v[116:119],a[156:159],a[28:31]
v_accvgpr_read_b32 v16,a8
v_accvgpr_read_b32 v17,a9
v_mfma_f32_16x16x32_bf16 a[28:31],v[112:115],a[140:143],a[28:31]
v_accvgpr_read_b32 v18,a10
v_accvgpr_read_b32 v19,a11
v_mfma_f32_16x16x32_bf16 v[128:131],v[64:67],v[32:35],v[128:131]
v_accvgpr_read_b32 v20,a24
v_accvgpr_read_b32 v21,a25
v_mfma_f32_16x16x32_bf16 v[128:131],v[68:71],v[36:39],v[128:131]
v_accvgpr_read_b32 v22,a26
v_accvgpr_read_b32 v23,a27
v_mfma_f32_16x16x32_bf16 v[128:131],v[72:75],v[40:43],v[128:131]
v_cvt_pk_bf16_f32 v16,v16,v17
v_cvt_pk_bf16_f32 v17,v18,v19
v_mfma_f32_16x16x32_bf16 v[128:131],v[76:79],v[44:47],v[128:131]
v_cvt_pk_bf16_f32 v18,v20,v21
v_cvt_pk_bf16_f32 v19,v22,v23
v_mfma_f32_16x16x32_bf16 v[136:139],v[76:79],v[180:183],v[136:139]
ds_read_b64_tr_b16 v[112:113],v2 offset:224
ds_read_b64_tr_b16 v[114:115],v2 offset:5344
v_mfma_f32_16x16x32_bf16 v[136:139],v[72:75],v[164:167],v[136:139]
buffer_store_dwordx4 v[8:11],v6,s[36:39],s76 offen offset:1024 sc1
v_mfma_f32_16x16x32_bf16 v[136:139],v[68:71],v[148:151],v[136:139]
ds_read_b64_tr_b16 v[116:117],v2 offset:10464
ds_read_b64_tr_b16 v[118:119],v2 offset:15584
v_mfma_f32_16x16x32_bf16 v[136:139],v[64:67],v[132:135],v[136:139]
ds_read_b64_tr_b16 v[120:121],v2 offset:20704
v_accvgpr_read_b32 v8,a12
v_mfma_f32_16x16x32_bf16 v[144:147],v[64:67],v[196:199],v[144:147]
ds_read_b64_tr_b16 v[122:123],v2 offset:25824
v_accvgpr_read_b32 v9,a13
v_mfma_f32_16x16x32_bf16 v[144:147],v[68:71],v[212:215],v[144:147]
ds_read_b64_tr_b16 v[124:125],v2 offset:30944
v_accvgpr_read_b32 v10,a14
v_mfma_f32_16x16x32_bf16 v[144:147],v[72:75],v[228:231],v[144:147]
ds_read_b64_tr_b16 v[126:127],v2 offset:36064
v_accvgpr_read_b32 v11,a15
v_mfma_f32_16x16x32_bf16 v[144:147],v[76:79],v[244:247],v[144:147]
v_accvgpr_read_b32 v12,a28
v_accvgpr_read_b32 v13,a29
v_mfma_f32_16x16x32_bf16 v[152:155],v[76:79],v[188:191],v[152:155]
v_accvgpr_read_b32 v14,a30
v_accvgpr_read_b32 v15,a31
v_mfma_f32_16x16x32_bf16 v[152:155],v[72:75],v[172:175],v[152:155]
v_cvt_pk_bf16_f32 v8,v8,v9
v_cvt_pk_bf16_f32 v9,v10,v11
v_mfma_f32_16x16x32_bf16 v[152:155],v[68:71],v[156:159],v[152:155]
v_cvt_pk_bf16_f32 v10,v12,v13
v_cvt_pk_bf16_f32 v11,v14,v15
v_mfma_f32_16x16x32_bf16 v[152:155],v[64:67],v[140:143],v[152:155]
ds_read_b64_tr_b16 v[64:65],v1 offset:256
ds_read_b64_tr_b16 v[66:67],v1 offset:5376
v_mfma_f32_16x16x32_bf16 v[160:163],v[80:83],v[32:35],v[160:163]
buffer_store_dwordx4 v[16:19],v6,s[36:39],s76 offen offset:2048 sc1
v_mfma_f32_16x16x32_bf16 v[160:163],v[84:87],v[36:39],v[160:163]
ds_read_b64_tr_b16 v[68:69],v1 offset:10496
ds_read_b64_tr_b16 v[70:71],v1 offset:15616
v_mfma_f32_16x16x32_bf16 v[160:163],v[88:91],v[40:43],v[160:163]
ds_read_b64_tr_b16 v[72:73],v1 offset:20736
v_add_u32 v2,0xfffec000,v2
v_mfma_f32_16x16x32_bf16 v[160:163],v[92:95],v[44:47],v[160:163]
ds_read_b64_tr_b16 v[74:75],v1 offset:25856
v_mov_b32 v20,s52
v_mfma_f32_16x16x32_bf16 v[168:171],v[92:95],v[180:183],v[168:171]
ds_read_b64_tr_b16 v[76:77],v1 offset:30976
v_mov_b32 v21,s53
v_mfma_f32_16x16x32_bf16 v[168:171],v[88:91],v[164:167],v[168:171]
ds_read_b64_tr_b16 v[78:79],v1 offset:36096
v_add_u32 v1,0xffff6000,v1
v_mfma_f32_16x16x32_bf16 v[168:171],v[84:87],v[148:151],v[168:171]
v_lshlrev_b32 v22,20,v6
v_mfma_f32_16x16x32_bf16 v[168:171],v[80:83],v[132:135],v[168:171]
v_mfma_f32_16x16x32_bf16 v[176:179],v[80:83],v[196:199],v[176:179]
v_mfma_f32_16x16x32_bf16 v[176:179],v[84:87],v[212:215],v[176:179]
v_mfma_f32_16x16x32_bf16 v[176:179],v[88:91],v[228:231],v[176:179]
v_mfma_f32_16x16x32_bf16 v[176:179],v[92:95],v[244:247],v[176:179]
buffer_store_dwordx4 v[8:11],v28,s[36:39],s76 offen offset:3072 sc1
v_mfma_f32_16x16x32_bf16 v[184:187],v[92:95],v[188:191],v[184:187]
ds_read_b64_tr_b16 v[92:93],v2 offset:31008
ds_read_b64_tr_b16 v[94:95],v2 offset:36128
)A"
R"A(v_mfma_f32_16x16x32_bf16 v[184:187],v[88:91],v[172:175],v[184:187]
ds_read_b64_tr_b16 v[88:89],v2 offset:20768
v_mfma_f32_16x16x32_bf16 v[184:187],v[84:87],v[156:159],v[184:187]
ds_read_b64_tr_b16 v[84:85],v2 offset:10528
v_mfma_f32_16x16x32_bf16 v[184:187],v[80:83],v[140:143],v[184:187]
s_waitcnt lgkmcnt(4)
ds_read_b64_tr_b16 v[80:81],v2 offset:288
v_mfma_f32_16x16x32_bf16 v[128:131],v[96:99],a[128:131],v[128:131]
ds_read_b64_tr_b16 v[82:83],v2 offset:5408
ds_read_b64_tr_b16 v[86:87],v2 offset:15648
v_mfma_f32_16x16x32_bf16 v[128:131],v[100:103],a[144:147],v[128:131]
ds_read_b64_tr_b16 v[90:91],v2 offset:25888
v_add_u32 v2,0xffff6000,v2
v_mfma_f32_16x16x32_bf16 v[128:131],v[104:107],a[160:163],v[128:131]
v_mfma_f32_16x16x32_bf16 v[128:131],v[108:111],a[176:179],v[128:131]
v_mfma_f32_16x16x32_bf16 v[136:139],v[108:111],a[180:183],v[136:139]
v_mfma_f32_16x16x32_bf16 v[136:139],v[104:107],a[164:167],v[136:139]
v_mfma_f32_16x16x32_bf16 v[136:139],v[100:103],a[148:151],v[136:139]
v_mfma_f32_16x16x32_bf16 v[136:139],v[96:99],a[132:135],v[136:139]
v_mfma_f32_16x16x32_bf16 v[144:147],v[96:99],a[136:139],v[144:147]
v_mfma_f32_16x16x32_bf16 v[144:147],v[100:103],a[152:155],v[144:147]
v_mfma_f32_16x16x32_bf16 v[144:147],v[104:107],a[168:171],v[144:147]
v_mfma_f32_16x16x32_bf16 v[144:147],v[108:111],a[184:187],v[144:147]
v_cvt_pk_bf16_f32 v128,v128,v129
v_cvt_pk_bf16_f32 v129,v130,v131
v_mfma_f32_16x16x32_bf16 v[152:155],v[108:111],a[188:191],v[152:155]
ds_read_b64_tr_b16 v[108:109],v1 offset:30976
ds_read_b64_tr_b16 v[110:111],v1 offset:36096
v_mfma_f32_16x16x32_bf16 v[152:155],v[104:107],a[172:175],v[152:155]
v_cvt_pk_bf16_f32 v136,v136,v137
v_cvt_pk_bf16_f32 v137,v138,v139
v_mfma_f32_16x16x32_bf16 v[152:155],v[100:103],a[156:159],v[152:155]
v_cvt_pk_bf16_f32 v144,v144,v145
v_cvt_pk_bf16_f32 v145,v146,v147
v_mfma_f32_16x16x32_bf16 v[152:155],v[96:99],a[140:143],v[152:155]
ds_read_b64_tr_b16 v[96:97],v1 offset:256
ds_read_b64_tr_b16 v[98:99],v1 offset:5376
v_mfma_f32_16x16x32_bf16 v[160:163],v[112:115],a[128:131],v[160:163]
ds_read_b64_tr_b16 v[100:101],v1 offset:10496
v_mfma_f32_16x16x32_bf16 v[160:163],v[116:119],a[144:147],v[160:163]
ds_read_b64_tr_b16 v[102:103],v1 offset:15616
v_mfma_f32_16x16x32_bf16 v[160:163],v[120:123],a[160:163],v[160:163]
ds_read_b64_tr_b16 v[104:105],v1 offset:20736
v_mfma_f32_16x16x32_bf16 v[160:163],v[124:127],a[176:179],v[160:163]
v_cvt_pk_bf16_f32 v152,v152,v153
v_cvt_pk_bf16_f32 v153,v154,v155
v_mfma_f32_16x16x32_bf16 v[168:171],v[124:127],a[180:183],v[168:171]
ds_read_b64_tr_b16 v[106:107],v1 offset:25856
v_add_u32 v1,0x1e000,v1
v_mfma_f32_16x16x32_bf16 v[168:171],v[120:123],a[164:167],v[168:171]
v_mfma_f32_16x16x32_bf16 v[168:171],v[116:119],a[148:151],v[168:171]
v_mfma_f32_16x16x32_bf16 v[168:171],v[112:115],a[132:135],v[168:171]
v_cvt_pk_bf16_f32 v130,v160,v161
v_cvt_pk_bf16_f32 v131,v162,v163
v_mfma_f32_16x16x32_bf16 v[176:179],v[112:115],a[136:139],v[176:179]
buffer_store_dwordx4 v[128:131],v6,s[36:39],s79 offen sc1
v_mfma_f32_16x16x32_bf16 v[176:179],v[116:119],a[152:155],v[176:179]
v_mfma_f32_16x16x32_bf16 v[176:179],v[120:123],a[168:171],v[176:179]
v_mfma_f32_16x16x32_bf16 v[176:179],v[124:127],a[184:187],v[176:179]
v_mfma_f32_16x16x32_bf16 v[184:187],v[124:127],a[188:191],v[184:187]
v_cvt_pk_bf16_f32 v138,v168,v169
v_cvt_pk_bf16_f32 v139,v170,v171
v_mfma_f32_16x16x32_bf16 v[184:187],v[120:123],a[172:175],v[184:187]
buffer_store_dwordx4 v[136:139],v6,s[36:39],s80 offen sc1
v_mfma_f32_16x16x32_bf16 v[184:187],v[116:119],a[156:159],v[184:187]
ds_read_b64_tr_b16 v[116:117],v2 offset:10528
ds_read_b64_tr_b16 v[118:119],v2 offset:15648
v_mfma_f32_16x16x32_bf16 v[184:187],v[112:115],a[140:143],v[184:187]
v_cvt_pk_bf16_f32 v146,v176,v177
v_cvt_pk_bf16_f32 v147,v178,v179
s_waitcnt vmcnt(6)
s_barrier
v_mfma_f32_16x16x32_bf16 v[192:195],v[64:67],a[68:71],v[192:195]
buffer_store_dwordx4 v[144:147],v6,s[36:39],s81 offen sc1
v_mfma_f32_16x16x32_bf16 v[192:195],v[68:71],a[76:79],v[192:195]
ds_read_b64_tr_b16 v[112:113],v2 offset:288
ds_read_b64_tr_b16 v[114:115],v2 offset:5408
v_mfma_f32_16x16x32_bf16 v[192:195],v[72:75],a[84:87],v[192:195]
buffer_store_dwordx2 v[58:59],v60,s[44:47],s18 offen offset:2312 sc1
v_mfma_f32_16x16x32_bf16 v[192:195],v[76:79],a[92:95],v[192:195]
v_cvt_pk_bf16_f32 v154,v184,v185
v_cvt_pk_bf16_f32 v155,v186,v187
v_mfma_f32_16x16x32_bf16 v[200:203],v[76:79],a[124:127],v[200:203]
buffer_store_dwordx4 v[152:155],v28,s[36:39],s82 offen sc1
v_mfma_f32_16x16x32_bf16 v[200:203],v[72:75],a[116:119],v[200:203]
ds_read_b64_tr_b16 v[120:121],v2 offset:20768
ds_read_b64_tr_b16 v[122:123],v2 offset:25888
v_mfma_f32_16x16x32_bf16 v[200:203],v[68:71],a[108:111],v[200:203]
ds_read_b64_tr_b16 v[124:125],v2 offset:31008
v_mfma_f32_16x16x32_bf16 v[200:203],v[64:67],a[100:103],v[200:203]
ds_read_b64_tr_b16 v[126:127],v2 offset:36128
v_add_u32 v2,0x1e000,v2
v_mfma_f32_16x16x32_bf16 v[208:211],v[64:67],a[64:67],v[208:211]
v_mfma_f32_16x16x32_bf16 v[208:211],v[68:71],a[72:75],v[208:211]
v_mfma_f32_16x16x32_bf16 v[208:211],v[72:75],a[80:83],v[208:211]
v_mfma_f32_16x16x32_bf16 v[208:211],v[76:79],a[88:91],v[208:211]
v_mfma_f32_16x16x32_bf16 v[216:219],v[76:79],a[120:123],v[216:219]
ds_read_b64_tr_b16 v[76:77],v1 offset:30976
ds_read_b64_tr_b16 v[78:79],v1 offset:36096
v_mfma_f32_16x16x32_bf16 v[216:219],v[72:75],a[112:115],v[216:219]
ds_read_b64_tr_b16 v[72:73],v1 offset:20736
v_mfma_f32_16x16x32_bf16 v[216:219],v[68:71],a[104:107],v[216:219]
ds_read_b64_tr_b16 v[68:69],v1 offset:10496
v_mfma_f32_16x16x32_bf16 v[216:219],v[64:67],a[96:99],v[216:219]
s_waitcnt lgkmcnt(4)
ds_read_b64_tr_b16 v[64:65],v1 offset:256
v_mfma_f32_16x16x32_bf16 v[224:227],v[80:83],a[68:71],v[224:227]
ds_read_b64_tr_b16 v[66:67],v1 offset:5376
ds_read_b64_tr_b16 v[70:71],v1 offset:15616
v_mfma_f32_16x16x32_bf16 v[224:227],v[84:87],a[76:79],v[224:227]
ds_read_b64_tr_b16 v[74:75],v1 offset:25856
v_mfma_f32_16x16x32_bf16 v[224:227],v[88:91],a[84:87],v[224:227]
v_mfma_f32_16x16x32_bf16 v[224:227],v[92:95],a[92:95],v[224:227]
v_mfma_f32_16x16x32_bf16 v[232:235],v[92:95],a[124:127],v[232:235]
v_mfma_f32_16x16x32_bf16 v[232:235],v[88:91],a[116:119],v[232:235]
v_mfma_f32_16x16x32_bf16 v[232:235],v[84:87],a[108:111],v[232:235]
v_mfma_f32_16x16x32_bf16 v[232:235],v[80:83],a[100:103],v[232:235]
v_mfma_f32_16x16x32_bf16 v[240:243],v[80:83],a[64:67],v[240:243]
v_mfma_f32_16x16x32_bf16 v[240:243],v[84:87],a[72:75],v[240:243]
v_mfma_f32_16x16x32_bf16 v[240:243],v[88:91],a[80:83],v[240:243]
v_mfma_f32_16x16x32_bf16 v[240:243],v[92:95],a[88:91],v[240:243]
v_mfma_f32_16x16x32_bf16 v[248:251],v[92:95],a[120:123],v[248:251]
ds_read_b64_tr_b16 v[92:93],v2 offset:31008
ds_read_b64_tr_b16 v[94:95],v2 offset:36128
v_mfma_f32_16x16x32_bf16 v[248:251],v[88:91],a[112:115],v[248:251]
ds_read_b64_tr_b16 v[88:89],v2 offset:20768
v_mfma_f32_16x16x32_bf16 v[248:251],v[84:87],a[104:107],v[248:251]
ds_read_b64_tr_b16 v[84:85],v2 offset:10528
v_mfma_f32_16x16x32_bf16 v[248:251],v[80:83],a[96:99],v[248:251]
ds_read_b64_tr_b16 v[80:81],v2 offset:288
v_mfma_f32_16x16x32_bf16 v[192:195],v[96:99],v[32:35],v[192:195]
ds_read_b64_tr_b16 v[82:83],v2 offset:5408
ds_read_b64_tr_b16 v[86:87],v2 offset:15648
v_mfma_f32_16x16x32_bf16 v[192:195],v[100:103],v[36:39],v[192:195]
ds_read_b64_tr_b16 v[90:91],v2 offset:25888
v_mfma_f32_16x16x32_bf16 v[192:195],v[104:107],v[40:43],v[192:195]
v_mfma_f32_16x16x32_bf16 v[192:195],v[108:111],v[44:47],v[192:195]
v_mfma_f32_16x16x32_bf16 v[200:203],v[108:111],v[180:183],v[200:203]
v_mfma_f32_16x16x32_bf16 v[200:203],v[104:107],v[164:167],v[200:203]
v_mfma_f32_16x16x32_bf16 v[200:203],v[100:103],v[148:151],v[200:203]
v_mfma_f32_16x16x32_bf16 v[200:203],v[96:99],v[132:135],v[200:203]
v_mfma_f32_16x16x32_bf16 v[208:211],v[96:99],v[196:199],v[208:211]
v_mfma_f32_16x16x32_bf16 v[208:211],v[100:103],v[212:215],v[208:211]
v_mfma_f32_16x16x32_bf16 v[208:211],v[104:107],v[228:231],v[208:211]
v_mfma_f32_16x16x32_bf16 v[208:211],v[108:111],v[244:247],v[208:211]
v_mfma_f32_16x16x32_bf16 v[216:219],v[108:111],v[188:191],v[216:219]
v_mfma_f32_16x16x32_bf16 v[216:219],v[104:107],v[172:175],v[216:219]
v_mfma_f32_16x16x32_bf16 v[216:219],v[100:103],v[156:159],v[216:219]
v_mfma_f32_16x16x32_bf16 v[216:219],v[96:99],v[140:143],v[216:219]
v_mfma_f32_16x16x32_bf16 v[224:227],v[112:115],v[32:35],v[224:227]
v_mfma_f32_16x16x32_bf16 v[224:227],v[116:119],v[36:39],v[224:227]
v_mfma_f32_16x16x32_bf16 v[224:227],v[120:123],v[40:43],v[224:227]
v_mfma_f32_16x16x32_bf16 v[224:227],v[124:127],v[44:47],v[224:227]
v_mfma_f32_16x16x32_bf16 v[232:235],v[124:127],v[180:183],v[232:235]
v_mfma_f32_16x16x32_bf16 v[232:235],v[120:123],v[164:167],v[232:235]
v_mfma_f32_16x16x32_bf16 v[232:235],v[116:119],v[148:151],v[232:235]
v_mfma_f32_16x16x32_bf16 v[232:235],v[112:115],v[132:135],v[232:235]
v_mfma_f32_16x16x32_bf16 v[240:243],v[112:115],v[196:199],v[240:243]
v_mfma_f32_16x16x32_bf16 v[240:243],v[116:119],v[212:215],v[240:243]
v_mfma_f32_16x16x32_bf16 v[240:243],v[120:123],v[228:231],v[240:243]
v_mfma_f32_16x16x32_bf16 v[240:243],v[124:127],v[244:247],v[240:243]
v_mfma_f32_16x16x32_bf16 v[248:251],v[124:127],v[188:191],v[248:251]
v_mfma_f32_16x16x32_bf16 v[248:251],v[120:123],v[172:175],v[248:251]
v_mfma_f32_16x16x32_bf16 v[248:251],v[116:119],v[156:159],v[248:251]
v_mfma_f32_16x16x32_bf16 v[248:251],v[112:115],v[140:143],v[248:251]
s_waitcnt lgkmcnt(0)
v_mfma_f32_16x16x32_bf16 v[192:195],v[64:67],a[128:131],v[192:195]
v_mfma_f32_16x16x32_bf16 v[192:195],v[68:71],a[144:147],v[192:195]
v_mfma_f32_16x16x32_bf16 v[192:195],v[72:75],a[160:163],v[192:195]
v_mfma_f32_16x16x32_bf16 v[192:195],v[76:79],a[176:179],v[192:195]
v_mfma_f32_16x16x32_bf16 v[200:203],v[76:79],a[180:183],v[200:203]
v_mfma_f32_16x16x32_bf16 v[200:203],v[72:75],a[164:167],v[200:203]
v_mfma_f32_16x16x32_bf16 v[200:203],v[68:71],a[148:151],v[200:203]
v_mfma_f32_16x16x32_bf16 v[200:203],v[64:67],a[132:135],v[200:203]
v_mfma_f32_16x16x32_bf16 v[208:211],v[64:67],a[136:139],v[208:211]
v_mfma_f32_16x16x32_bf16 v[208:211],v[68:71],a[152:155],v[208:211]
v_mfma_f32_16x16x32_bf16 v[208:211],v[72:75],a[168:171],v[208:211]
v_mfma_f32_16x16x32_bf16 v[208:211],v[76:79],a[184:187],v[208:211]
v_mfma_f32_16x16x32_bf16 v[216:219],v[76:79],a[188:191],v[216:219]
v_mfma_f32_16x16x32_bf16 v[216:219],v[72:75],a[172:175],v[216:219]
v_mfma_f32_16x16x32_bf16 v[216:219],v[68:71],a[156:159],v[216:219]
v_mfma_f32_16x16x32_bf16 v[216:219],v[64:67],a[140:143],v[216:219]
v_mfma_f32_16x16x32_bf16 v[224:227],v[80:83],a[128:131],v[224:227]
buffer_load_dwordx2 v[62:63],v61,s[44:47],s91 offen offset:2312 sc1
v_mfma_f32_16x16x32_bf16 v[224:227],v[84:87],a[144:147],v[224:227]
v_mfma_f32_16x16x32_bf16 v[224:227],v[88:91],a[160:163],v[224:227]
v_mfma_f32_16x16x32_bf16 v[224:227],v[92:95],a[176:179],v[224:227]
v_mfma_f32_16x16x32_bf16 v[232:235],v[92:95],a[180:183],v[232:235]
v_mfma_f32_16x16x32_bf16 v[232:235],v[88:91],a[164:167],v[232:235]
v_mfma_f32_16x16x32_bf16 v[232:235],v[84:87],a[148:151],v[232:235]
v_mfma_f32_16x16x32_bf16 v[232:235],v[80:83],a[132:135],v[232:235]
v_mfma_f32_16x16x32_bf16 v[240:243],v[80:83],a[136:139],v[240:243]
v_mfma_f32_16x16x32_bf16 v[240:243],v[84:87],a[152:155],v[240:243]
v_mfma_f32_16x16x32_bf16 v[240:243],v[88:91],a[168:171],v[240:243]
v_mfma_f32_16x16x32_bf16 v[240:243],v[92:95],a[184:187],v[240:243]
v_mfma_f32_16x16x32_bf16 v[248:251],v[92:95],a[188:191],v[248:251]
v_mfma_f32_16x16x32_bf16 v[248:251],v[88:91],a[172:175],v[248:251]
v_mfma_f32_16x16x32_bf16 v[248:251],v[84:87],a[156:159],v[248:251]
v_mfma_f32_16x16x32_bf16 v[248:251],v[80:83],a[140:143],v[248:251]
s_waitcnt vmcnt(1)
s_barrier
buffer_store_dwordx2 v[20:21],v22,s[44:47],s18 offen offset:2304 sc1
s_waitcnt vmcnt(1)
v_cmp_ne_u32 s[56:57],s52,v62
v_cmp_ne_u32 s[58:59],s53,v63
s_nop 0
s_or_b64 s[56:57],s[56:57],s[58:59]
s_cbranch_scc1 L_fr4_%=
L_fo4_%=:
buffer_load_dwordx4 v[64:67],v6,s[36:39],s66 offen sc1
buffer_load_dwordx4 v[68:71],v6,s[36:39],s67 offen sc1
buffer_load_dwordx4 v[72:75],v6,s[36:39],s66 offen offset:1024 sc1
)A"
R"A(buffer_load_dwordx4 v[76:79],v6,s[36:39],s67 offen offset:1024 sc1
buffer_load_dwordx4 v[80:83],v6,s[36:39],s66 offen offset:2048 sc1
buffer_load_dwordx4 v[84:87],v6,s[36:39],s67 offen offset:2048 sc1
buffer_load_dwordx4 v[88:91],v28,s[36:39],s66 offen offset:3072 sc1
buffer_load_dwordx4 v[92:95],v28,s[36:39],s67 offen offset:3072 sc1
buffer_load_dwordx2 v[4:5],v3,s[44:47],s91 offen offset:2304 sc1
s_waitcnt vmcnt(8)
v_mfma_f32_16x16x32_bf16 v[192:195],v[48:51],v[64:67],v[192:195]
s_waitcnt vmcnt(7)
v_mfma_f32_16x16x32_bf16 v[192:195],v[48:51],v[68:71],v[192:195]
v_mfma_f32_16x16x32_bf16 v[224:227],v[52:55],v[64:67],v[224:227]
v_mfma_f32_16x16x32_bf16 v[224:227],v[52:55],v[68:71],v[224:227]
s_waitcnt vmcnt(6)
v_mfma_f32_16x16x32_bf16 v[200:203],v[48:51],v[72:75],v[200:203]
s_waitcnt vmcnt(5)
v_mfma_f32_16x16x32_bf16 v[200:203],v[48:51],v[76:79],v[200:203]
v_mfma_f32_16x16x32_bf16 v[232:235],v[52:55],v[72:75],v[232:235]
v_mfma_f32_16x16x32_bf16 v[232:235],v[52:55],v[76:79],v[232:235]
s_waitcnt vmcnt(4)
v_mfma_f32_16x16x32_bf16 v[208:211],v[48:51],v[80:83],v[208:211]
s_waitcnt vmcnt(3)
v_mfma_f32_16x16x32_bf16 v[208:211],v[48:51],v[84:87],v[208:211]
v_mfma_f32_16x16x32_bf16 v[240:243],v[52:55],v[80:83],v[240:243]
v_mfma_f32_16x16x32_bf16 v[240:243],v[52:55],v[84:87],v[240:243]
s_waitcnt vmcnt(2)
v_mfma_f32_16x16x32_bf16 v[216:219],v[48:51],v[88:91],v[216:219]
s_waitcnt vmcnt(1)
v_mfma_f32_16x16x32_bf16 v[216:219],v[48:51],v[92:95],v[216:219]
v_mfma_f32_16x16x32_bf16 v[248:251],v[52:55],v[88:91],v[248:251]
v_mfma_f32_16x16x32_bf16 v[248:251],v[52:55],v[92:95],v[248:251]
s_waitcnt vmcnt(0)
v_cmp_ne_u32 s[56:57],s52,v4
v_cmp_ne_u32 s[58:59],s53,v5
s_nop 0
s_or_b64 s[56:57],s[56:57],s[58:59]
s_cbranch_scc1 L_fr5_%=
L_fo5_%=:
buffer_load_dwordx4 v[96:99],v6,s[36:39],s68 offen sc1
buffer_load_dwordx4 v[100:103],v6,s[36:39],s68 offen offset:1024 sc1
buffer_load_dwordx4 v[104:107],v6,s[36:39],s68 offen offset:2048 sc1
buffer_load_dwordx4 v[108:111],v6,s[36:39],s68 offen offset:3072 sc1
buffer_load_dwordx4 v[112:115],v6,s[36:39],s65 offen sc1
buffer_load_dwordx4 v[116:119],v6,s[36:39],s65 offen offset:1024 sc1
buffer_load_dwordx4 v[120:123],v6,s[36:39],s65 offen offset:2048 sc1
buffer_load_dwordx4 v[124:127],v28,s[36:39],s65 offen offset:3072 sc1
s_waitcnt vmcnt(7)
v_mfma_f32_16x16x32_bf16 v[128:131],v[48:51],v[96:99],0
s_waitcnt vmcnt(6)
v_mfma_f32_16x16x32_bf16 v[128:131],v[48:51],v[100:103],v[128:131]
s_waitcnt vmcnt(5)
v_mfma_f32_16x16x32_bf16 v[128:131],v[48:51],v[104:107],v[128:131]
s_waitcnt vmcnt(4)
v_mfma_f32_16x16x32_bf16 v[128:131],v[48:51],v[108:111],v[128:131]
v_mfma_f32_16x16x32_bf16 v[160:163],v[52:55],v[96:99],0
v_mfma_f32_16x16x32_bf16 v[160:163],v[52:55],v[100:103],v[160:163]
v_mfma_f32_16x16x32_bf16 v[160:163],v[52:55],v[104:107],v[160:163]
v_mfma_f32_16x16x32_bf16 v[160:163],v[52:55],v[108:111],v[160:163]
s_waitcnt vmcnt(3)
v_mfma_f32_16x16x32_bf16 v[192:195],v[48:51],v[112:115],v[192:195]
v_mfma_f32_16x16x32_bf16 v[224:227],v[52:55],v[112:115],v[224:227]
s_waitcnt vmcnt(2)
v_mfma_f32_16x16x32_bf16 v[200:203],v[48:51],v[116:119],v[200:203]
v_mfma_f32_16x16x32_bf16 v[232:235],v[52:55],v[116:119],v[232:235]
s_waitcnt vmcnt(1)
v_cvt_pk_bf16_f32 v128,v128,v129
v_cvt_pk_bf16_f32 v129,v130,v131
v_mfma_f32_16x16x32_bf16 v[208:211],v[48:51],v[120:123],v[208:211]
v_mfma_f32_16x16x32_bf16 v[240:243],v[52:55],v[120:123],v[240:243]
s_waitcnt vmcnt(0)
v_cvt_pk_bf16_f32 v130,v160,v161
v_cvt_pk_bf16_f32 v131,v162,v163
v_mfma_f32_16x16x32_bf16 v[216:219],v[48:51],v[124:127],v[216:219]
v_cvt_pk_bf16_f32 v192,v192,v193
v_cvt_pk_bf16_f32 v193,v194,v195
v_cvt_pk_bf16_f32 v194,v224,v225
v_cvt_pk_bf16_f32 v195,v226,v227
v_cvt_pk_bf16_f32 v200,v200,v201
v_cvt_pk_bf16_f32 v201,v202,v203
v_mfma_f32_16x16x32_bf16 v[248:251],v[52:55],v[124:127],v[248:251]
v_permlane16_swap_b32 v128,v130
v_permlane16_swap_b32 v129,v131
v_cvt_pk_bf16_f32 v202,v232,v233
v_cvt_pk_bf16_f32 v203,v234,v235
v_cvt_pk_bf16_f32 v208,v208,v209
v_cvt_pk_bf16_f32 v209,v210,v211
v_cvt_pk_bf16_f32 v210,v240,v241
v_cvt_pk_bf16_f32 v211,v242,v243
buffer_store_dwordx4 v[128:131],v56,s[24:27],s83 offen offset:256 sc1 nt
v_permlane16_swap_b32 v192,v194
v_permlane16_swap_b32 v193,v195
buffer_store_dwordx4 v[192:195],v56,s[24:27],s87 offen sc1 nt
v_permlane16_swap_b32 v200,v202
v_permlane16_swap_b32 v201,v203
v_cvt_pk_bf16_f32 v216,v216,v217
v_cvt_pk_bf16_f32 v217,v218,v219
v_cvt_pk_bf16_f32 v218,v248,v249
v_cvt_pk_bf16_f32 v219,v250,v251
buffer_store_dwordx4 v[200:203],v56,s[24:27],s88 offen sc1 nt
v_permlane16_swap_b32 v208,v210
v_permlane16_swap_b32 v209,v211
buffer_store_dwordx4 v[208:211],v56,s[24:27],s89 offen sc1 nt
v_permlane16_swap_b32 v216,v218
v_permlane16_swap_b32 v217,v219
buffer_store_dwordx4 v[216:219],v56,s[24:27],s90 offen sc1 nt
s_lshl_b32 s69,s62,20
s_add_u32 s69,s69,s18
buffer_store_dword v20,v22,s[44:47],s69 offen sc1
L_geF_%=:
s_branch L_end_%=
.p2align 12
L_pd19_%=:
s_cbranch_execz L_trt1_%=
L_GS_%=:
L_gbS_%=:
s_waitcnt lgkmcnt(14)
v_mfma_f32_16x16x32_bf16 a[192:195],v[64:67],a[68:71],0
buffer_store_dwordx4 v[32:35],v6,s[40:43],s79 offen sc1
s_waitcnt lgkmcnt(12)
v_mfma_f32_16x16x32_bf16 a[192:195],v[68:71],a[76:79],a[192:195]
s_lshl_b32 s91,s63,8
v_bfe_u32 v3,v6,4,2
v_min_u32 v3,2,v3
s_waitcnt lgkmcnt(10)
v_mfma_f32_16x16x32_bf16 a[192:195],v[72:75],a[84:87],a[192:195]
s_add_u32 s87,s64,1
v_add3_u32 v3,v3,s64,1
v_and_b32 v3,3,v3
s_waitcnt lgkmcnt(8)
v_mfma_f32_16x16x32_bf16 a[192:195],v[76:79],a[92:95],a[192:195]
s_and_b32 s87,s87,3
ds_read_b64_tr_b16 v[96:97],v1 offset:64
v_lshlrev_b32 v3,6,v3
v_mfma_f32_16x16x32_bf16 a[196:199],v[76:79],a[124:127],0
s_lshl_b32 s87,s87,1
ds_read_b64_tr_b16 v[98:99],v1 offset:5184
ds_read_b64_tr_b16 v[100:101],v1 offset:10304
v_mfma_f32_16x16x32_bf16 a[196:199],v[72:75],a[116:119],a[196:199]
s_lshl_b32 s87,s87,16
ds_read_b64_tr_b16 v[102:103],v1 offset:15424
v_lshlrev_b32 v29,10,v6
v_mfma_f32_16x16x32_bf16 a[196:199],v[68:71],a[108:111],a[196:199]
s_add_u32 s88,s87,4096
ds_read_b64_tr_b16 v[104:105],v1 offset:20544
v_accvgpr_read_b32 v16,a24
v_mfma_f32_16x16x32_bf16 a[196:199],v[64:67],a[100:103],a[196:199]
s_add_u32 s89,s87,8192
ds_read_b64_tr_b16 v[106:107],v1 offset:25664
v_accvgpr_read_b32 v17,a25
v_mfma_f32_16x16x32_bf16 a[200:203],v[64:67],a[64:67],0
s_add_u32 s90,s87,12288
ds_read_b64_tr_b16 v[108:109],v1 offset:30784
ds_read_b64_tr_b16 v[110:111],v1 offset:35904
v_mfma_f32_16x16x32_bf16 a[200:203],v[68:71],a[72:75],a[200:203]
s_add_u32 s20,s64,2
ds_read_b64_tr_b16 v[112:113],v2 offset:96
v_accvgpr_read_b32 v18,a26
v_mfma_f32_16x16x32_bf16 a[200:203],v[72:75],a[80:83],a[200:203]
s_and_b32 s20,s20,3
ds_read_b64_tr_b16 v[114:115],v2 offset:5216
v_accvgpr_read_b32 v19,a27
v_mfma_f32_16x16x32_bf16 a[200:203],v[76:79],a[88:91],a[200:203]
s_lshl_b32 s20,s20,1
ds_read_b64_tr_b16 v[116:117],v2 offset:10336
v_accvgpr_read_b32 v20,a28
v_mfma_f32_16x16x32_bf16 a[204:207],v[76:79],a[120:123],0
s_lshl_b32 s20,s20,16
ds_read_b64_tr_b16 v[118:119],v2 offset:15456
ds_read_b64_tr_b16 v[120:121],v2 offset:20576
v_mfma_f32_16x16x32_bf16 a[204:207],v[72:75],a[112:115],a[204:207]
s_add_u32 s21,s20,4096
ds_read_b64_tr_b16 v[122:123],v2 offset:25696
v_accvgpr_read_b32 v21,a29
v_mfma_f32_16x16x32_bf16 a[204:207],v[68:71],a[104:107],a[204:207]
s_add_u32 s22,s20,8192
ds_read_b64_tr_b16 v[124:125],v2 offset:30816
v_accvgpr_read_b32 v22,a30
v_mfma_f32_16x16x32_bf16 a[204:207],v[64:67],a[96:99],a[204:207]
buffer_store_dwordx4 v[36:39],v6,s[40:43],s80 offen sc1
s_waitcnt lgkmcnt(9)
v_mfma_f32_16x16x32_bf16 a[208:211],v[80:83],a[68:71],0
s_add_u32 s23,s20,12288
ds_read_b64_tr_b16 v[126:127],v2 offset:35936
ds_read_b64_tr_b16 v[64:65],v1 offset:128
v_mfma_f32_16x16x32_bf16 a[208:211],v[84:87],a[76:79],a[208:211]
s_add_u32 m0,s86,122880
ds_read_b64_tr_b16 v[66:67],v1 offset:5248
v_accvgpr_read_b32 v23,a31
v_mfma_f32_16x16x32_bf16 a[208:211],v[88:91],a[84:87],a[208:211]
s_add_u32 s83,s64,3
ds_read_b64_tr_b16 v[68:69],v1 offset:10368
v_mul_f32 v16,v16,v49
v_mfma_f32_16x16x32_bf16 a[208:211],v[92:95],a[92:95],a[208:211]
s_and_b32 s83,s83,3
ds_read_b64_tr_b16 v[70:71],v1 offset:15488
v_mul_f32 v17,v17,v49
v_mfma_f32_16x16x32_bf16 a[212:215],v[92:95],a[124:127],0
s_waitcnt vmcnt(21)
buffer_load_dwordx2 v[4:5],v3,s[44:47],s91 offen offset:256 sc1
v_mfma_f32_16x16x32_bf16 a[212:215],v[88:91],a[116:119],a[212:215]
s_lshl_b32 s83,s83,1
ds_read_b64_tr_b16 v[72:73],v1 offset:20608
ds_read_b64_tr_b16 v[74:75],v1 offset:25728
v_mfma_f32_16x16x32_bf16 a[212:215],v[84:87],a[108:111],a[212:215]
s_mul_i32 s83,s83,163840
ds_read_b64_tr_b16 v[76:77],v1 offset:30848
v_mul_f32 v18,v18,v49
v_mfma_f32_16x16x32_bf16 a[212:215],v[80:83],a[100:103],a[212:215]
s_add_u32 s83,s83,s85
ds_read_b64_tr_b16 v[78:79],v1 offset:35968
v_mul_f32 v19,v19,v49
v_mfma_f32_16x16x32_bf16 a[216:219],v[80:83],a[64:67],0
s_add_u32 s84,s83,20480
v_mul_f32 v20,v20,v49
v_mul_f32 v21,v21,v49
v_mfma_f32_16x16x32_bf16 a[216:219],v[84:87],a[72:75],a[216:219]
s_lshl_b32 s66,s61,3
v_mul_f32 v22,v22,v49
v_mul_f32 v23,v23,v49
v_mfma_f32_16x16x32_bf16 a[216:219],v[88:91],a[80:83],a[216:219]
s_add_u32 s66,s66,s64
v_cvt_pk_bf16_f32 v44,v16,v17
v_cvt_pk_bf16_f32 v45,v18,v19
v_mfma_f32_16x16x32_bf16 a[216:219],v[92:95],a[88:91],a[216:219]
s_mul_i32 s66,s66,262144
v_cvt_pk_bf16_f32 v46,v20,v21
v_cvt_pk_bf16_f32 v47,v22,v23
v_mfma_f32_16x16x32_bf16 a[220:223],v[92:95],a[120:123],0
s_add_u32 s66,s66,100663296
ds_read_b64_tr_b16 v[92:93],v2 offset:30880
ds_read_b64_tr_b16 v[94:95],v2 offset:36000
v_mfma_f32_16x16x32_bf16 a[220:223],v[88:91],a[112:115],a[220:223]
s_mov_b32 s38,262144
ds_read_b64_tr_b16 v[88:89],v2 offset:20640
v_mul_f32 v132,v132,v53
v_mfma_f32_16x16x32_bf16 a[220:223],v[84:87],a[104:107],a[220:223]
buffer_store_dwordx4 v[40:43],v6,s[40:43],s81 offen sc1
v_mfma_f32_16x16x32_bf16 a[220:223],v[80:83],a[96:99],a[220:223]
s_mov_b32 s39,0x20000
ds_read_b64_tr_b16 v[80:81],v2 offset:160
ds_read_b64_tr_b16 v[82:83],v2 offset:5280
v_mfma_f32_16x16x32_bf16 a[224:227],v[96:99],a[68:71],0
s_add_u32 s74,s63,1
ds_read_b64_tr_b16 v[84:85],v2 offset:10400
v_mul_f32 v133,v133,v53
v_mfma_f32_16x16x32_bf16 a[224:227],v[100:103],a[76:79],a[224:227]
s_and_b32 s74,s74,3
ds_read_b64_tr_b16 v[86:87],v2 offset:15520
v_mul_f32 v134,v134,v53
v_mfma_f32_16x16x32_bf16 a[224:227],v[104:107],a[84:87],a[224:227]
s_lshl_b32 s74,s74,2
ds_read_b64_tr_b16 v[90:91],v2 offset:25760
v_mul_f32 v135,v135,v53
s_waitcnt lgkmcnt(8)
v_mfma_f32_16x16x32_bf16 a[224:227],v[108:111],a[92:95],a[224:227]
s_add_u32 s74,s74,s60
v_mul_f32 v140,v140,v53
v_mul_f32 v141,v141,v53
v_mfma_f32_16x16x32_bf16 a[228:231],v[108:111],a[124:127],0
s_mul_i32 s74,s74,16384
v_mul_f32 v142,v142,v53
v_mul_f32 v143,v143,v53
v_mfma_f32_16x16x32_bf16 a[228:231],v[104:107],a[116:119],a[228:231]
s_add_u32 s74,s74,8192
v_cvt_pk_bf16_f32 v132,v132,v133
v_cvt_pk_bf16_f32 v133,v134,v135
v_mfma_f32_16x16x32_bf16 a[228:231],v[100:103],a[108:111],a[228:231]
s_add_u32 s75,s63,2
v_cvt_pk_bf16_f32 v134,v140,v141
v_cvt_pk_bf16_f32 v135,v142,v143
v_mfma_f32_16x16x32_bf16 a[228:231],v[96:99],a[100:103],a[228:231]
s_and_b32 s75,s75,3
v_mul_f32 v148,v148,v53
v_mul_f32 v149,v149,v53
v_mfma_f32_16x16x32_bf16 a[232:235],v[96:99],a[64:67],0
s_lshl_b32 s75,s75,2
v_mul_f32 v150,v150,v53
v_mul_f32 v151,v151,v53
v_mfma_f32_16x16x32_bf16 a[232:235],v[100:103],a[72:75],a[232:235]
s_add_u32 s75,s75,s60
v_mul_f32 v156,v156,v53
v_mul_f32 v157,v157,v53
v_mfma_f32_16x16x32_bf16 a[232:235],v[104:107],a[80:83],a[232:235]
s_mul_i32 s75,s75,16384
v_mul_f32 v158,v158,v53
v_mul_f32 v159,v159,v53
v_mfma_f32_16x16x32_bf16 a[232:235],v[108:111],a[88:91],a[232:235]
s_add_u32 s75,s75,4096
v_cvt_pk_bf16_f32 v148,v148,v149
v_cvt_pk_bf16_f32 v149,v150,v151
v_mfma_f32_16x16x32_bf16 a[236:239],v[108:111],a[120:123],0
s_add_u32 s76,s63,3
ds_read_b64_tr_b16 v[108:109],v1 offset:30912
ds_read_b64_tr_b16 v[110:111],v1 offset:36032
v_mfma_f32_16x16x32_bf16 a[236:239],v[104:107],a[112:115],a[236:239]
buffer_store_dwordx4 v[44:47],v6,s[40:43],s82 offen sc1
v_mfma_f32_16x16x32_bf16 a[236:239],v[100:103],a[104:107],a[236:239]
)A"
R"A(s_and_b32 s76,s76,3
ds_read_b64_tr_b16 v[100:101],v1 offset:10432
ds_read_b64_tr_b16 v[102:103],v1 offset:15552
v_mfma_f32_16x16x32_bf16 a[236:239],v[96:99],a[96:99],a[236:239]
s_lshl_b32 s76,s76,2
ds_read_b64_tr_b16 v[96:97],v1 offset:192
v_cvt_pk_bf16_f32 v150,v156,v157
v_mfma_f32_16x16x32_bf16 a[240:243],v[112:115],a[68:71],0
s_add_u32 s76,s76,s60
ds_read_b64_tr_b16 v[98:99],v1 offset:5312
v_cvt_pk_bf16_f32 v151,v158,v159
v_mfma_f32_16x16x32_bf16 a[240:243],v[116:119],a[76:79],a[240:243]
s_mul_i32 s76,s76,16384
ds_read_b64_tr_b16 v[104:105],v1 offset:20672
v_mul_f32 v164,v164,v53
v_mfma_f32_16x16x32_bf16 a[240:243],v[120:123],a[84:87],a[240:243]
s_add_u32 s76,s76,0
ds_read_b64_tr_b16 v[106:107],v1 offset:25792
v_mul_f32 v165,v165,v53
v_mfma_f32_16x16x32_bf16 a[240:243],v[124:127],a[92:95],a[240:243]
s_lshl_b32 s67,s63,10
v_mul_f32 v166,v166,v53
v_mul_f32 v167,v167,v53
v_mfma_f32_16x16x32_bf16 a[244:247],v[124:127],a[124:127],0
s_mov_b32 s26,307200
v_mul_f32 v172,v172,v53
v_mul_f32 v173,v173,v53
v_mfma_f32_16x16x32_bf16 a[244:247],v[120:123],a[116:119],a[244:247]
s_mov_b32 s27,0x20000
v_mul_f32 v174,v174,v53
v_mul_f32 v175,v175,v53
v_mfma_f32_16x16x32_bf16 a[244:247],v[116:119],a[108:111],a[244:247]
s_lshl_b32 s77,s63,2
v_cvt_pk_bf16_f32 v164,v164,v165
v_cvt_pk_bf16_f32 v165,v166,v167
v_mfma_f32_16x16x32_bf16 a[244:247],v[112:115],a[100:103],a[244:247]
s_add_u32 s77,s77,s60
v_cvt_pk_bf16_f32 v166,v172,v173
v_cvt_pk_bf16_f32 v167,v174,v175
v_mfma_f32_16x16x32_bf16 a[248:251],v[112:115],a[64:67],0
s_mul_i32 s77,s77,16384
v_mul_f32 v180,v180,v53
v_mul_f32 v181,v181,v53
v_mfma_f32_16x16x32_bf16 a[248:251],v[116:119],a[72:75],a[248:251]
s_add_u32 s65,s77,0
v_mul_f32 v182,v182,v53
v_mul_f32 v183,v183,v53
v_mfma_f32_16x16x32_bf16 a[248:251],v[120:123],a[80:83],a[248:251]
s_add_u32 s68,s77,12288
v_mul_f32 v188,v188,v53
v_mul_f32 v189,v189,v53
v_mfma_f32_16x16x32_bf16 a[248:251],v[124:127],a[88:91],a[248:251]
v_mul_f32 v190,v190,v53
v_mul_f32 v191,v191,v53
v_mfma_f32_16x16x32_bf16 a[252:255],v[124:127],a[120:123],0
buffer_load_dword v30,v29,s[8:11],s7 offen
v_mfma_f32_16x16x32_bf16 a[252:255],v[120:123],a[112:115],a[252:255]
ds_read_b64_tr_b16 v[120:121],v2 offset:20704
ds_read_b64_tr_b16 v[122:123],v2 offset:25824
v_mfma_f32_16x16x32_bf16 a[252:255],v[116:119],a[104:107],a[252:255]
buffer_store_dwordx4 v[132:135],v6,s[40:43],s79 offen offset:1024 sc1
v_mfma_f32_16x16x32_bf16 a[252:255],v[112:115],a[96:99],a[252:255]
ds_read_b64_tr_b16 v[112:113],v2 offset:224
ds_read_b64_tr_b16 v[114:115],v2 offset:5344
v_mfma_f32_16x16x32_bf16 a[0:3],v[64:67],a[68:71],0
ds_read_b64_tr_b16 v[116:117],v2 offset:10464
v_cvt_pk_bf16_f32 v180,v180,v181
v_mfma_f32_16x16x32_bf16 a[0:3],v[68:71],a[76:79],a[0:3]
ds_read_b64_tr_b16 v[118:119],v2 offset:15584
v_cvt_pk_bf16_f32 v181,v182,v183
v_mfma_f32_16x16x32_bf16 a[0:3],v[72:75],a[84:87],a[0:3]
ds_read_b64_tr_b16 v[124:125],v2 offset:30944
v_cvt_pk_bf16_f32 v182,v188,v189
v_mfma_f32_16x16x32_bf16 a[0:3],v[76:79],a[92:95],a[0:3]
ds_read_b64_tr_b16 v[126:127],v2 offset:36064
v_cvt_pk_bf16_f32 v183,v190,v191
v_mfma_f32_16x16x32_bf16 a[4:7],v[76:79],a[124:127],0
v_mul_f32 v196,v196,v57
v_mul_f32 v197,v197,v57
v_mfma_f32_16x16x32_bf16 a[4:7],v[72:75],a[116:119],a[4:7]
v_mul_f32 v198,v198,v57
v_mul_f32 v199,v199,v57
v_mfma_f32_16x16x32_bf16 a[4:7],v[68:71],a[108:111],a[4:7]
v_mul_f32 v204,v204,v57
v_mul_f32 v205,v205,v57
v_mfma_f32_16x16x32_bf16 a[4:7],v[64:67],a[100:103],a[4:7]
v_mul_f32 v206,v206,v57
v_mul_f32 v207,v207,v57
v_mfma_f32_16x16x32_bf16 a[8:11],v[64:67],a[64:67],0
v_cvt_pk_bf16_f32 v196,v196,v197
v_cvt_pk_bf16_f32 v197,v198,v199
v_mfma_f32_16x16x32_bf16 a[8:11],v[68:71],a[72:75],a[8:11]
v_cvt_pk_bf16_f32 v198,v204,v205
v_cvt_pk_bf16_f32 v199,v206,v207
v_mfma_f32_16x16x32_bf16 a[8:11],v[72:75],a[80:83],a[8:11]
v_mul_f32 v212,v212,v57
v_mul_f32 v213,v213,v57
v_mfma_f32_16x16x32_bf16 a[8:11],v[76:79],a[88:91],a[8:11]
v_mul_f32 v214,v214,v57
v_mul_f32 v215,v215,v57
v_mfma_f32_16x16x32_bf16 a[12:15],v[76:79],a[120:123],0
ds_read_b64_tr_b16 v[76:77],v1 offset:30976
ds_read_b64_tr_b16 v[78:79],v1 offset:36096
v_mfma_f32_16x16x32_bf16 a[12:15],v[72:75],a[112:115],a[12:15]
buffer_store_dwordx4 v[148:151],v6,s[40:43],s80 offen offset:1024 sc1
v_mfma_f32_16x16x32_bf16 a[12:15],v[68:71],a[104:107],a[12:15]
ds_read_b64_tr_b16 v[68:69],v1 offset:10496
ds_read_b64_tr_b16 v[70:71],v1 offset:15616
v_mfma_f32_16x16x32_bf16 a[12:15],v[64:67],a[96:99],a[12:15]
s_waitcnt lgkmcnt(4)
ds_read_b64_tr_b16 v[64:65],v1 offset:256
v_mul_f32 v220,v220,v57
v_mfma_f32_16x16x32_bf16 a[16:19],v[80:83],a[68:71],0
ds_read_b64_tr_b16 v[66:67],v1 offset:5376
v_mul_f32 v221,v221,v57
v_mfma_f32_16x16x32_bf16 a[16:19],v[84:87],a[76:79],a[16:19]
ds_read_b64_tr_b16 v[72:73],v1 offset:20736
v_mul_f32 v222,v222,v57
v_mfma_f32_16x16x32_bf16 a[16:19],v[88:91],a[84:87],a[16:19]
ds_read_b64_tr_b16 v[74:75],v1 offset:25856
v_add_u32 v1,0xffff6000,v1
v_mfma_f32_16x16x32_bf16 a[16:19],v[92:95],a[92:95],a[16:19]
v_mul_f32 v223,v223,v57
v_cvt_pk_bf16_f32 v212,v212,v213
v_mfma_f32_16x16x32_bf16 a[20:23],v[92:95],a[124:127],0
v_cvt_pk_bf16_f32 v213,v214,v215
v_cvt_pk_bf16_f32 v214,v220,v221
v_mfma_f32_16x16x32_bf16 a[20:23],v[88:91],a[116:119],a[20:23]
v_cvt_pk_bf16_f32 v215,v222,v223
v_mul_f32 v228,v228,v57
v_mfma_f32_16x16x32_bf16 a[20:23],v[84:87],a[108:111],a[20:23]
v_mul_f32 v229,v229,v57
v_mul_f32 v230,v230,v57
v_mfma_f32_16x16x32_bf16 a[20:23],v[80:83],a[100:103],a[20:23]
v_mul_f32 v231,v231,v57
v_mul_f32 v236,v236,v57
v_mfma_f32_16x16x32_bf16 a[24:27],v[80:83],a[64:67],0
v_mul_f32 v237,v237,v57
v_mul_f32 v238,v238,v57
v_mfma_f32_16x16x32_bf16 a[24:27],v[84:87],a[72:75],a[24:27]
v_mul_f32 v239,v239,v57
v_cvt_pk_bf16_f32 v228,v228,v229
v_mfma_f32_16x16x32_bf16 a[24:27],v[88:91],a[80:83],a[24:27]
s_waitcnt vmcnt(5)
v_cmp_ne_u32 s[56:57],s52,v4
v_cmp_ne_u32 s[58:59],s53,v5
v_mfma_f32_16x16x32_bf16 a[24:27],v[92:95],a[88:91],a[24:27]
s_or_b64 s[56:57],s[56:57],s[58:59]
s_cbranch_scc1 L_fr6_%=
L_fo6_%=:
v_cvt_pk_bf16_f32 v229,v230,v231
v_mfma_f32_16x16x32_bf16 a[28:31],v[92:95],a[120:123],0
buffer_store_dwordx4 v[164:167],v6,s[40:43],s81 offen offset:1024 sc1
v_mfma_f32_16x16x32_bf16 a[28:31],v[88:91],a[112:115],a[28:31]
s_lshl_b32 s91,s63,8
ds_read_b64_tr_b16 v[88:89],v2 offset:20768
ds_read_b64_tr_b16 v[90:91],v2 offset:25888
v_mfma_f32_16x16x32_bf16 a[28:31],v[84:87],a[104:107],a[28:31]
buffer_load_dwordx4 a[128:131],v6,s[28:31],s87 offen sc1
v_mfma_f32_16x16x32_bf16 a[28:31],v[80:83],a[96:99],a[28:31]
ds_read_b64_tr_b16 v[80:81],v2 offset:288
ds_read_b64_tr_b16 v[82:83],v2 offset:5408
v_mfma_f32_16x16x32_bf16 v[128:131],v[96:99],a[68:71],0
ds_read_b64_tr_b16 v[84:85],v2 offset:10528
v_cvt_pk_bf16_f32 v230,v236,v237
v_mfma_f32_16x16x32_bf16 v[128:131],v[100:103],a[76:79],v[128:131]
ds_read_b64_tr_b16 v[86:87],v2 offset:15648
v_cvt_pk_bf16_f32 v231,v238,v239
v_mfma_f32_16x16x32_bf16 v[128:131],v[104:107],a[84:87],v[128:131]
ds_read_b64_tr_b16 v[92:93],v2 offset:31008
v_mul_f32 v244,v244,v57
v_mfma_f32_16x16x32_bf16 v[128:131],v[108:111],a[92:95],v[128:131]
ds_read_b64_tr_b16 v[94:95],v2 offset:36128
v_add_u32 v2,0xffff6000,v2
v_mfma_f32_16x16x32_bf16 v[136:139],v[108:111],a[124:127],0
v_mul_f32 v245,v245,v57
v_mul_f32 v246,v246,v57
v_mfma_f32_16x16x32_bf16 v[136:139],v[104:107],a[116:119],v[136:139]
v_mul_f32 v247,v247,v57
v_mul_f32 v252,v252,v57
v_mfma_f32_16x16x32_bf16 v[136:139],v[100:103],a[108:111],v[136:139]
v_mul_f32 v253,v253,v57
v_mul_f32 v254,v254,v57
v_mfma_f32_16x16x32_bf16 v[136:139],v[96:99],a[100:103],v[136:139]
v_mul_f32 v255,v255,v57
v_cvt_pk_bf16_f32 v244,v244,v245
v_mfma_f32_16x16x32_bf16 v[144:147],v[96:99],a[64:67],0
v_cvt_pk_bf16_f32 v245,v246,v247
v_cvt_pk_bf16_f32 v246,v252,v253
v_mfma_f32_16x16x32_bf16 v[144:147],v[100:103],a[72:75],v[144:147]
buffer_load_dwordx4 a[144:147],v6,s[28:31],s88 offen sc1
v_mfma_f32_16x16x32_bf16 v[144:147],v[104:107],a[80:83],v[144:147]
v_cvt_pk_bf16_f32 v247,v254,v255
v_accvgpr_read_b32 v16,a40
v_mfma_f32_16x16x32_bf16 v[144:147],v[108:111],a[88:91],v[144:147]
buffer_store_dwordx4 v[180:183],v6,s[40:43],s82 offen offset:1024 sc1
v_mfma_f32_16x16x32_bf16 v[152:155],v[108:111],a[120:123],0
ds_read_b64_tr_b16 v[108:109],v1 offset:30720
ds_read_b64_tr_b16 v[110:111],v1 offset:35840
v_mfma_f32_16x16x32_bf16 v[152:155],v[104:107],a[112:115],v[152:155]
ds_read_b64_tr_b16 v[104:105],v1 offset:20480
v_accvgpr_read_b32 v17,a41
v_mfma_f32_16x16x32_bf16 v[152:155],v[100:103],a[104:107],v[152:155]
ds_read_b64_tr_b16 v[100:101],v1 offset:10240
v_accvgpr_read_b32 v18,a42
v_mfma_f32_16x16x32_bf16 v[152:155],v[96:99],a[96:99],v[152:155]
ds_read_b64_tr_b16 v[96:97],v1
v_accvgpr_read_b32 v19,a43
v_mfma_f32_16x16x32_bf16 v[160:163],v[112:115],a[68:71],0
ds_read_b64_tr_b16 v[98:99],v1 offset:5120
ds_read_b64_tr_b16 v[102:103],v1 offset:15360
v_mfma_f32_16x16x32_bf16 v[160:163],v[116:119],a[76:79],v[160:163]
ds_read_b64_tr_b16 v[106:107],v1 offset:25600
v_accvgpr_read_b32 v20,a44
v_mfma_f32_16x16x32_bf16 v[160:163],v[120:123],a[84:87],v[160:163]
v_accvgpr_read_b32 v21,a45
v_accvgpr_read_b32 v22,a46
v_mfma_f32_16x16x32_bf16 v[160:163],v[124:127],a[92:95],v[160:163]
v_accvgpr_read_b32 v23,a47
v_mul_f32 v16,v16,v61
v_mfma_f32_16x16x32_bf16 v[168:171],v[124:127],a[124:127],0
v_mul_f32 v17,v17,v61
v_mul_f32 v18,v18,v61
v_mfma_f32_16x16x32_bf16 v[168:171],v[120:123],a[116:119],v[168:171]
v_mul_f32 v19,v19,v61
v_mul_f32 v20,v20,v61
v_mfma_f32_16x16x32_bf16 v[168:171],v[116:119],a[108:111],v[168:171]
buffer_load_dwordx4 a[160:163],v6,s[28:31],s89 offen sc1
v_mfma_f32_16x16x32_bf16 v[168:171],v[112:115],a[100:103],v[168:171]
v_mul_f32 v21,v21,v61
v_mul_f32 v22,v22,v61
v_mfma_f32_16x16x32_bf16 v[176:179],v[112:115],a[64:67],0
v_mul_f32 v23,v23,v61
v_cvt_pk_bf16_f32 v156,v16,v17
v_mfma_f32_16x16x32_bf16 v[176:179],v[116:119],a[72:75],v[176:179]
v_cvt_pk_bf16_f32 v157,v18,v19
v_cvt_pk_bf16_f32 v158,v20,v21
v_mfma_f32_16x16x32_bf16 v[176:179],v[120:123],a[80:83],v[176:179]
buffer_store_dwordx4 v[196:199],v6,s[40:43],s79 offen offset:2048 sc1
v_mfma_f32_16x16x32_bf16 v[176:179],v[124:127],a[88:91],v[176:179]
v_cvt_pk_bf16_f32 v159,v22,v23
v_accvgpr_read_b32 v16,a56
v_mfma_f32_16x16x32_bf16 v[184:187],v[124:127],a[120:123],0
v_accvgpr_read_b32 v17,a57
v_accvgpr_read_b32 v18,a58
v_mfma_f32_16x16x32_bf16 v[184:187],v[120:123],a[112:115],v[184:187]
ds_read_b64_tr_b16 v[120:121],v2 offset:20512
ds_read_b64_tr_b16 v[122:123],v2 offset:25632
v_mfma_f32_16x16x32_bf16 v[184:187],v[116:119],a[104:107],v[184:187]
ds_read_b64_tr_b16 v[116:117],v2 offset:10272
v_accvgpr_read_b32 v19,a59
v_mfma_f32_16x16x32_bf16 v[184:187],v[112:115],a[96:99],v[184:187]
s_waitcnt lgkmcnt(3)
ds_read_b64_tr_b16 v[112:113],v2 offset:32
v_accvgpr_read_b32 v20,a60
v_mfma_f32_16x16x32_bf16 v[192:195],v[64:67],a[68:71],0
ds_read_b64_tr_b16 v[114:115],v2 offset:5152
v_accvgpr_read_b32 v21,a61
v_mfma_f32_16x16x32_bf16 v[192:195],v[68:71],a[76:79],v[192:195]
ds_read_b64_tr_b16 v[118:119],v2 offset:15392
v_accvgpr_read_b32 v22,a62
v_mfma_f32_16x16x32_bf16 v[192:195],v[72:75],a[84:87],v[192:195]
v_accvgpr_read_b32 v23,a63
v_mul_f32 v16,v16,v61
v_mfma_f32_16x16x32_bf16 v[192:195],v[76:79],a[92:95],v[192:195]
buffer_load_dwordx4 a[176:179],v6,s[28:31],s90 offen sc1
v_mfma_f32_16x16x32_bf16 v[200:203],v[76:79],a[124:127],0
v_mul_f32 v17,v17,v61
v_mul_f32 v18,v18,v61
v_mfma_f32_16x16x32_bf16 v[200:203],v[72:75],a[116:119],v[200:203]
v_mul_f32 v19,v19,v61
v_mul_f32 v20,v20,v61
v_mfma_f32_16x16x32_bf16 v[200:203],v[68:71],a[108:111],v[200:203]
v_mul_f32 v21,v21,v61
v_mul_f32 v22,v22,v61
v_mfma_f32_16x16x32_bf16 v[200:203],v[64:67],a[100:103],v[200:203]
v_mul_f32 v23,v23,v61
v_cvt_pk_bf16_f32 v188,v16,v17
v_mfma_f32_16x16x32_bf16 v[208:211],v[64:67],a[64:67],0
v_cvt_pk_bf16_f32 v189,v18,v19
v_cvt_pk_bf16_f32 v190,v20,v21
v_mfma_f32_16x16x32_bf16 v[208:211],v[68:71],a[72:75],v[208:211]
buffer_store_dwordx4 v[212:215],v6,s[40:43],s80 offen offset:2048 sc1
v_mfma_f32_16x16x32_bf16 v[208:211],v[72:75],a[80:83],v[208:211]
ds_read_b64_tr_b16 v[124:125],v2 offset:30752
)A"
R"A(v_cvt_pk_bf16_f32 v191,v22,v23
v_mfma_f32_16x16x32_bf16 v[208:211],v[76:79],a[88:91],v[208:211]
ds_read_b64_tr_b16 v[126:127],v2 offset:35872
v_accvgpr_read_b32 v8,a32
v_mfma_f32_16x16x32_bf16 v[216:219],v[76:79],a[120:123],0
v_accvgpr_read_b32 v9,a33
v_accvgpr_read_b32 v10,a34
v_mfma_f32_16x16x32_bf16 v[216:219],v[72:75],a[112:115],v[216:219]
v_accvgpr_read_b32 v11,a35
v_accvgpr_read_b32 v12,a36
v_mfma_f32_16x16x32_bf16 v[216:219],v[68:71],a[104:107],v[216:219]
v_accvgpr_read_b32 v13,a37
v_accvgpr_read_b32 v14,a38
v_mfma_f32_16x16x32_bf16 v[216:219],v[64:67],a[96:99],v[216:219]
v_accvgpr_read_b32 v15,a39
v_mul_f32 v8,v8,v61
v_mfma_f32_16x16x32_bf16 v[224:227],v[80:83],a[68:71],0
buffer_load_dwordx4 a[180:183],v6,s[28:31],s90 offen offset:1024 sc1
v_mfma_f32_16x16x32_bf16 v[224:227],v[84:87],a[76:79],v[224:227]
v_mul_f32 v9,v9,v61
v_mul_f32 v10,v10,v61
v_mfma_f32_16x16x32_bf16 v[224:227],v[88:91],a[84:87],v[224:227]
v_mul_f32 v11,v11,v61
v_mul_f32 v12,v12,v61
v_mfma_f32_16x16x32_bf16 v[224:227],v[92:95],a[92:95],v[224:227]
v_mul_f32 v13,v13,v61
v_mul_f32 v14,v14,v61
v_mfma_f32_16x16x32_bf16 v[232:235],v[92:95],a[124:127],0
v_mul_f32 v15,v15,v61
v_cvt_pk_bf16_f32 v140,v8,v9
v_mfma_f32_16x16x32_bf16 v[232:235],v[88:91],a[116:119],v[232:235]
v_cvt_pk_bf16_f32 v141,v10,v11
v_cvt_pk_bf16_f32 v142,v12,v13
v_mfma_f32_16x16x32_bf16 v[232:235],v[84:87],a[108:111],v[232:235]
v_cvt_pk_bf16_f32 v143,v14,v15
v_accvgpr_read_b32 v8,a48
v_mfma_f32_16x16x32_bf16 v[232:235],v[80:83],a[100:103],v[232:235]
v_accvgpr_read_b32 v9,a49
v_accvgpr_read_b32 v10,a50
v_mfma_f32_16x16x32_bf16 v[240:243],v[80:83],a[64:67],0
buffer_store_dwordx4 v[228:231],v6,s[40:43],s81 offen offset:2048 sc1
v_mfma_f32_16x16x32_bf16 v[240:243],v[84:87],a[72:75],v[240:243]
v_accvgpr_read_b32 v11,a51
v_accvgpr_read_b32 v12,a52
v_mfma_f32_16x16x32_bf16 v[240:243],v[88:91],a[80:83],v[240:243]
v_accvgpr_read_b32 v13,a53
v_accvgpr_read_b32 v14,a54
v_mfma_f32_16x16x32_bf16 v[240:243],v[92:95],a[88:91],v[240:243]
v_accvgpr_read_b32 v15,a55
v_mul_f32 v8,v8,v61
v_mfma_f32_16x16x32_bf16 v[248:251],v[92:95],a[120:123],0
v_mul_f32 v9,v9,v61
v_mul_f32 v10,v10,v61
v_mfma_f32_16x16x32_bf16 v[248:251],v[88:91],a[112:115],v[248:251]
buffer_load_dwordx4 a[164:167],v6,s[28:31],s89 offen offset:1024 sc1
v_mfma_f32_16x16x32_bf16 v[248:251],v[84:87],a[104:107],v[248:251]
v_mul_f32 v11,v11,v61
v_mul_f32 v12,v12,v61
v_mfma_f32_16x16x32_bf16 v[248:251],v[80:83],a[96:99],v[248:251]
v_mul_f32 v13,v13,v61
v_mul_f32 v14,v14,v61
v_mfma_f32_16x16x32_bf16 a[192:195],v[96:99],v[32:35],a[192:195]
v_mul_f32 v15,v15,v61
v_cvt_pk_bf16_f32 v172,v8,v9
v_mfma_f32_16x16x32_bf16 a[192:195],v[100:103],v[36:39],a[192:195]
v_cvt_pk_bf16_f32 v173,v10,v11
v_cvt_pk_bf16_f32 v174,v12,v13
v_mfma_f32_16x16x32_bf16 a[192:195],v[104:107],v[40:43],a[192:195]
buffer_load_dwordx4 a[68:71],v6,s[28:31],s20 offen sc1
v_mfma_f32_16x16x32_bf16 a[192:195],v[108:111],v[44:47],a[192:195]
ds_read_b64_tr_b16 v[64:65],v1 offset:64
v_cvt_pk_bf16_f32 v175,v14,v15
v_mfma_f32_16x16x32_bf16 a[196:199],v[108:111],v[180:183],a[196:199]
ds_read_b64_tr_b16 v[66:67],v1 offset:5184
ds_read_b64_tr_b16 v[68:69],v1 offset:10304
v_mfma_f32_16x16x32_bf16 a[196:199],v[104:107],v[164:167],a[196:199]
s_cmp_eq_u32 s60,3
s_cselect_b32 s69,0x40000000,0
ds_read_b64_tr_b16 v[70:71],v1 offset:15424
v_mfma_f32_16x16x32_bf16 a[196:199],v[100:103],v[148:151],a[196:199]
ds_read_b64_tr_b16 v[72:73],v1 offset:20544
v_add_u32 v28,s69,v6
v_mfma_f32_16x16x32_bf16 a[196:199],v[96:99],v[132:135],a[196:199]
buffer_store_dwordx4 v[244:247],v6,s[40:43],s82 offen offset:2048 sc1
v_mfma_f32_16x16x32_bf16 a[200:203],v[96:99],v[196:199],a[200:203]
s_and_b32 s69,s63,s60
ds_read_b64_tr_b16 v[74:75],v1 offset:25664
ds_read_b64_tr_b16 v[76:77],v1 offset:30784
v_mfma_f32_16x16x32_bf16 a[200:203],v[100:103],v[212:215],a[200:203]
ds_read_b64_tr_b16 v[78:79],v1 offset:35904
v_mov_b32 v8,s52
v_mfma_f32_16x16x32_bf16 a[200:203],v[104:107],v[228:231],a[200:203]
buffer_load_dwordx4 a[148:151],v6,s[28:31],s88 offen offset:1024 sc1
v_mfma_f32_16x16x32_bf16 a[200:203],v[108:111],v[244:247],a[200:203]
ds_read_b64_tr_b16 v[80:81],v2 offset:96
ds_read_b64_tr_b16 v[82:83],v2 offset:5216
v_mfma_f32_16x16x32_bf16 a[204:207],v[108:111],v[188:191],a[204:207]
ds_read_b64_tr_b16 v[84:85],v2 offset:10336
v_mov_b32 v9,s53
v_mfma_f32_16x16x32_bf16 a[204:207],v[104:107],v[172:175],a[204:207]
ds_read_b64_tr_b16 v[86:87],v2 offset:15456
v_lshlrev_b32 v10,20,v6
v_mfma_f32_16x16x32_bf16 a[204:207],v[100:103],v[156:159],a[204:207]
s_waitcnt lgkmcnt(7)
ds_read_b64_tr_b16 v[88:89],v2 offset:20576
v_bfe_u32 v3,v6,4,2
v_mfma_f32_16x16x32_bf16 a[204:207],v[96:99],v[140:143],a[204:207]
buffer_load_dwordx4 a[76:79],v6,s[28:31],s21 offen sc1
v_mfma_f32_16x16x32_bf16 a[208:211],v[112:115],v[32:35],a[208:211]
ds_read_b64_tr_b16 v[90:91],v2 offset:25696
ds_read_b64_tr_b16 v[92:93],v2 offset:30816
v_mfma_f32_16x16x32_bf16 a[208:211],v[116:119],v[36:39],a[208:211]
ds_read_b64_tr_b16 v[94:95],v2 offset:35936
v_min_u32 v3,2,v3
v_mfma_f32_16x16x32_bf16 a[208:211],v[120:123],v[40:43],a[208:211]
ds_read_b64_tr_b16 v[96:97],v1 offset:128
v_add3_u32 v3,v3,s64,1
v_mfma_f32_16x16x32_bf16 a[208:211],v[124:127],v[44:47],a[208:211]
ds_read_b64_tr_b16 v[98:99],v1 offset:5248
v_and_b32 v3,3,v3
v_mfma_f32_16x16x32_bf16 a[212:215],v[124:127],v[180:183],a[212:215]
ds_read_b64_tr_b16 v[100:101],v1 offset:10368
ds_read_b64_tr_b16 v[102:103],v1 offset:15488
v_mfma_f32_16x16x32_bf16 a[212:215],v[120:123],v[164:167],a[212:215]
ds_read_b64_tr_b16 v[104:105],v1 offset:20608
v_lshlrev_b32 v3,6,v3
v_mfma_f32_16x16x32_bf16 a[212:215],v[116:119],v[148:151],a[212:215]
s_nop 0
L_pd20_%=:
s_cbranch_execz L_trt1_%=
buffer_store_dwordx4 v[140:143],v28,s[40:43],s79 offen offset:3072 sc1
v_mfma_f32_16x16x32_bf16 a[212:215],v[112:115],v[132:135],a[212:215]
ds_read_b64_tr_b16 v[106:107],v1 offset:25728
ds_read_b64_tr_b16 v[108:109],v1 offset:30848
v_mfma_f32_16x16x32_bf16 a[216:219],v[112:115],v[196:199],a[216:219]
buffer_load_dwordx4 a[132:135],v6,s[28:31],s87 offen offset:1024 sc1
v_mfma_f32_16x16x32_bf16 a[216:219],v[116:119],v[212:215],a[216:219]
ds_read_b64_tr_b16 v[110:111],v1 offset:35968
v_bfe_u32 v56,v6,6,2
v_mfma_f32_16x16x32_bf16 a[216:219],v[120:123],v[228:231],a[216:219]
v_lshrrev_b32 v57,8,v6
v_cmp_eq_u32 s[56:57],v56,v57
v_mfma_f32_16x16x32_bf16 a[216:219],v[124:127],v[244:247],a[216:219]
v_bfe_u32 v56,v6,4,1
v_lshlrev_b32 v56,4,v56
v_mfma_f32_16x16x32_bf16 a[220:223],v[124:127],v[188:191],a[220:223]
ds_read_b64_tr_b16 v[124:125],v2 offset:30880
ds_read_b64_tr_b16 v[126:127],v2 offset:36000
v_mfma_f32_16x16x32_bf16 a[220:223],v[120:123],v[172:175],a[220:223]
buffer_load_dwordx4 a[84:87],v6,s[28:31],s22 offen sc1
v_mfma_f32_16x16x32_bf16 a[220:223],v[116:119],v[156:159],a[220:223]
ds_read_b64_tr_b16 v[116:117],v2 offset:10400
ds_read_b64_tr_b16 v[118:119],v2 offset:15520
v_mfma_f32_16x16x32_bf16 a[220:223],v[112:115],v[140:143],a[220:223]
ds_read_b64_tr_b16 v[112:113],v2 offset:160
v_mov_b32 v57,0x3f80
v_mfma_f32_16x16x32_bf16 a[224:227],v[64:67],v[32:35],a[224:227]
ds_read_b64_tr_b16 v[114:115],v2 offset:5280
v_lshlrev_b32 v56,v56,v57
v_mfma_f32_16x16x32_bf16 a[224:227],v[68:71],v[36:39],a[224:227]
s_waitcnt lgkmcnt(6)
ds_read_b64_tr_b16 v[120:121],v2 offset:20640
v_cndmask_b32 v56,0,v56,s[56:57]
v_mfma_f32_16x16x32_bf16 a[224:227],v[72:75],v[40:43],a[224:227]
ds_read_b64_tr_b16 v[122:123],v2 offset:25760
v_bfe_u32 v57,v6,5,1
v_mfma_f32_16x16x32_bf16 a[224:227],v[76:79],v[44:47],a[224:227]
s_add_u32 s36,s12,s66
s_addc_u32 s37,s13,0
v_cmp_eq_u32 s[56:57],0,v57
v_mfma_f32_16x16x32_bf16 a[228:231],v[76:79],v[180:183],a[228:231]
buffer_load_dwordx4 a[136:139],v6,s[28:31],s87 offen offset:2048 sc1
v_mfma_f32_16x16x32_bf16 a[228:231],v[72:75],v[164:167],a[228:231]
s_and_b32 s37,s37,0xffff
v_cndmask_b32 v48,0,v56,s[56:57]
v_cndmask_b32 v49,v56,0,s[56:57]
v_mfma_f32_16x16x32_bf16 a[228:231],v[68:71],v[148:151],a[228:231]
buffer_store_dwordx4 v[156:159],v28,s[40:43],s80 offen offset:3072 sc1
v_mfma_f32_16x16x32_bf16 a[228:231],v[64:67],v[132:135],a[228:231]
s_mul_i32 s66,s61,307200
v_mov_b32 v58,s52
v_mov_b32 v59,s53
v_mfma_f32_16x16x32_bf16 a[232:235],v[64:67],v[196:199],a[232:235]
s_add_u32 s24,s16,s66
s_addc_u32 s25,s17,0
v_lshlrev_b32 v60,20,v6
v_mfma_f32_16x16x32_bf16 a[232:235],v[68:71],v[212:215],a[232:235]
buffer_load_dwordx4 a[92:95],v6,s[28:31],s23 offen sc1
v_mfma_f32_16x16x32_bf16 a[232:235],v[72:75],v[228:231],a[232:235]
s_mul_i32 s66,s60,16384
s_add_u32 s66,s66,s67
s_add_u32 s79,s66,12288
v_mfma_f32_16x16x32_bf16 a[232:235],v[76:79],v[244:247],a[232:235]
s_add_u32 s80,s66,77824
v_bfe_u32 v61,v6,4,1
v_add3_u32 v61,v61,s63,2
v_mfma_f32_16x16x32_bf16 a[236:239],v[76:79],v[188:191],a[236:239]
s_and_b32 s25,s25,0xffff
ds_read_b64_tr_b16 v[76:77],v1 offset:30912
ds_read_b64_tr_b16 v[78:79],v1 offset:36032
v_mfma_f32_16x16x32_bf16 a[236:239],v[72:75],v[172:175],a[236:239]
s_add_u32 s67,s77,8192
ds_read_b64_tr_b16 v[72:73],v1 offset:20672
v_and_b32 v61,3,v61
v_mfma_f32_16x16x32_bf16 a[236:239],v[68:71],v[156:159],a[236:239]
ds_read_b64_tr_b16 v[68:69],v1 offset:10432
v_lshlrev_b32 v61,8,v61
v_mfma_f32_16x16x32_bf16 a[236:239],v[64:67],v[140:143],a[236:239]
ds_read_b64_tr_b16 v[64:65],v1 offset:192
v_mov_b32 v50,0
v_mfma_f32_16x16x32_bf16 a[240:243],v[80:83],v[32:35],a[240:243]
ds_read_b64_tr_b16 v[66:67],v1 offset:5312
ds_read_b64_tr_b16 v[70:71],v1 offset:15552
v_mfma_f32_16x16x32_bf16 a[240:243],v[84:87],v[36:39],a[240:243]
buffer_load_dwordx4 a[152:155],v6,s[28:31],s88 offen offset:2048 sc1
v_mfma_f32_16x16x32_bf16 a[240:243],v[88:91],v[40:43],a[240:243]
ds_read_b64_tr_b16 v[74:75],v1 offset:25792
v_mov_b32 v51,0
v_mfma_f32_16x16x32_bf16 a[240:243],v[92:95],v[44:47],a[240:243]
v_mov_b32 v52,0
v_mov_b32 v53,0
v_mfma_f32_16x16x32_bf16 a[244:247],v[92:95],v[180:183],a[244:247]
v_mov_b32 v54,v48
v_mov_b32 v55,v49
v_mfma_f32_16x16x32_bf16 a[244:247],v[88:91],v[164:167],a[244:247]
buffer_store_dwordx4 v[172:175],v28,s[40:43],s81 offen offset:3072 sc1
v_mfma_f32_16x16x32_bf16 a[244:247],v[84:87],v[148:151],a[244:247]
s_add_u32 s81,s66,143360
v_bfe_u32 v56,v6,4,4
v_mul_u32_u24 v56,1280,v56
v_mfma_f32_16x16x32_bf16 a[244:247],v[80:83],v[132:135],a[244:247]
buffer_load_dwordx4 a[124:127],v6,s[28:31],s23 offen offset:1024 sc1
v_mfma_f32_16x16x32_bf16 a[248:251],v[80:83],v[196:199],a[248:251]
v_bfe_u32 v57,v6,8,1
v_lshl_add_u32 v56,v57,5,v56
v_mfma_f32_16x16x32_bf16 a[248:251],v[84:87],v[212:215],a[248:251]
v_lshrrev_b32 v57,9,v6
v_lshl_add_u32 v56,v57,4,v56
v_mfma_f32_16x16x32_bf16 a[248:251],v[88:91],v[228:231],a[248:251]
s_cmp_eq_u32 s69,3
s_cselect_b32 s69,0x40000000,0
s_add_u32 s68,s68,s69
v_mfma_f32_16x16x32_bf16 a[248:251],v[92:95],v[244:247],a[248:251]
v_mfma_f32_16x16x32_bf16 a[252:255],v[92:95],v[188:191],a[252:255]
ds_read_b64_tr_b16 v[92:93],v2 offset:30944
ds_read_b64_tr_b16 v[94:95],v2 offset:36064
v_mfma_f32_16x16x32_bf16 a[252:255],v[88:91],v[172:175],a[252:255]
ds_read_b64_tr_b16 v[88:89],v2 offset:20704
v_mfma_f32_16x16x32_bf16 a[252:255],v[84:87],v[156:159],a[252:255]
buffer_load_dwordx4 a[168:171],v6,s[28:31],s89 offen offset:2048 sc1
v_mfma_f32_16x16x32_bf16 a[252:255],v[80:83],v[140:143],a[252:255]
ds_read_b64_tr_b16 v[80:81],v2 offset:224
ds_read_b64_tr_b16 v[82:83],v2 offset:5344
v_mfma_f32_16x16x32_bf16 a[0:3],v[96:99],v[32:35],a[0:3]
ds_read_b64_tr_b16 v[84:85],v2 offset:10464
v_mfma_f32_16x16x32_bf16 a[0:3],v[100:103],v[36:39],a[0:3]
ds_read_b64_tr_b16 v[86:87],v2 offset:15584
v_mfma_f32_16x16x32_bf16 a[0:3],v[104:107],v[40:43],a[0:3]
ds_read_b64_tr_b16 v[90:91],v2 offset:25824
v_mfma_f32_16x16x32_bf16 a[0:3],v[108:111],v[44:47],a[0:3]
buffer_load_dwordx4 a[116:119],v6,s[28:31],s22 offen offset:1024 sc1
v_mfma_f32_16x16x32_bf16 a[4:7],v[108:111],v[180:183],a[4:7]
v_mfma_f32_16x16x32_bf16 a[4:7],v[104:107],v[164:167],a[4:7]
buffer_store_dwordx4 v[188:191],v28,s[40:43],s82 offen offset:3072 sc1
v_mfma_f32_16x16x32_bf16 a[4:7],v[100:103],v[148:151],a[4:7]
s_add_u32 s82,s66,208896
s_mul_i32 s66,s64,320
)A"
R"A(v_mfma_f32_16x16x32_bf16 a[4:7],v[96:99],v[132:135],a[4:7]
v_mfma_f32_16x16x32_bf16 a[8:11],v[96:99],v[196:199],a[8:11]
v_mfma_f32_16x16x32_bf16 a[8:11],v[100:103],v[212:215],a[8:11]
v_mfma_f32_16x16x32_bf16 a[8:11],v[104:107],v[228:231],a[8:11]
v_mfma_f32_16x16x32_bf16 a[8:11],v[108:111],v[244:247],a[8:11]
buffer_load_dwordx4 a[184:187],v6,s[28:31],s90 offen offset:2048 sc1
v_mfma_f32_16x16x32_bf16 a[12:15],v[108:111],v[188:191],a[12:15]
ds_read_b64_tr_b16 v[108:109],v1 offset:30976
ds_read_b64_tr_b16 v[110:111],v1 offset:36096
v_mfma_f32_16x16x32_bf16 a[12:15],v[104:107],v[172:175],a[12:15]
ds_read_b64_tr_b16 v[104:105],v1 offset:20736
v_mfma_f32_16x16x32_bf16 a[12:15],v[100:103],v[156:159],a[12:15]
ds_read_b64_tr_b16 v[100:101],v1 offset:10496
v_mfma_f32_16x16x32_bf16 a[12:15],v[96:99],v[140:143],a[12:15]
s_waitcnt lgkmcnt(4)
ds_read_b64_tr_b16 v[96:97],v1 offset:256
v_mfma_f32_16x16x32_bf16 a[16:19],v[112:115],v[32:35],a[16:19]
buffer_load_dwordx4 a[108:111],v6,s[28:31],s21 offen offset:1024 sc1
v_mfma_f32_16x16x32_bf16 a[16:19],v[116:119],v[36:39],a[16:19]
ds_read_b64_tr_b16 v[98:99],v1 offset:5376
ds_read_b64_tr_b16 v[102:103],v1 offset:15616
v_mfma_f32_16x16x32_bf16 a[16:19],v[120:123],v[40:43],a[16:19]
ds_read_b64_tr_b16 v[106:107],v1 offset:25856
v_add_u32 v1,0xffff6000,v1
v_mfma_f32_16x16x32_bf16 a[16:19],v[124:127],v[44:47],a[16:19]
v_mfma_f32_16x16x32_bf16 a[20:23],v[124:127],v[180:183],a[20:23]
v_mfma_f32_16x16x32_bf16 a[20:23],v[120:123],v[164:167],a[20:23]
v_mfma_f32_16x16x32_bf16 a[20:23],v[116:119],v[148:151],a[20:23]
v_mfma_f32_16x16x32_bf16 a[20:23],v[112:115],v[132:135],a[20:23]
v_mfma_f32_16x16x32_bf16 a[24:27],v[112:115],v[196:199],a[24:27]
buffer_load_dwordx4 a[188:191],v28,s[28:31],s90 offen offset:3072 sc1
v_mfma_f32_16x16x32_bf16 a[24:27],v[116:119],v[212:215],a[24:27]
v_mfma_f32_16x16x32_bf16 a[24:27],v[120:123],v[228:231],a[24:27]
v_mfma_f32_16x16x32_bf16 a[24:27],v[124:127],v[244:247],a[24:27]
v_mfma_f32_16x16x32_bf16 a[28:31],v[124:127],v[188:191],a[28:31]
ds_read_b64_tr_b16 v[124:125],v2 offset:31008
ds_read_b64_tr_b16 v[126:127],v2 offset:36128
v_mfma_f32_16x16x32_bf16 a[28:31],v[120:123],v[172:175],a[28:31]
buffer_load_dwordx4 a[100:103],v6,s[28:31],s20 offen offset:1024 sc1
v_mfma_f32_16x16x32_bf16 a[28:31],v[116:119],v[156:159],a[28:31]
ds_read_b64_tr_b16 v[116:117],v2 offset:10528
ds_read_b64_tr_b16 v[118:119],v2 offset:15648
v_mfma_f32_16x16x32_bf16 a[28:31],v[112:115],v[140:143],a[28:31]
ds_read_b64_tr_b16 v[112:113],v2 offset:288
v_mfma_f32_16x16x32_bf16 v[128:131],v[64:67],v[32:35],v[128:131]
ds_read_b64_tr_b16 v[114:115],v2 offset:5408
v_mfma_f32_16x16x32_bf16 v[128:131],v[68:71],v[36:39],v[128:131]
ds_read_b64_tr_b16 v[120:121],v2 offset:20768
v_mfma_f32_16x16x32_bf16 v[128:131],v[72:75],v[40:43],v[128:131]
ds_read_b64_tr_b16 v[122:123],v2 offset:25888
v_add_u32 v2,0xffff6000,v2
v_mfma_f32_16x16x32_bf16 v[128:131],v[76:79],v[44:47],v[128:131]
v_mfma_f32_16x16x32_bf16 v[136:139],v[76:79],v[180:183],v[136:139]
v_mfma_f32_16x16x32_bf16 v[136:139],v[72:75],v[164:167],v[136:139]
buffer_load_dwordx4 a[172:175],v28,s[28:31],s89 offen offset:3072 sc1
v_mfma_f32_16x16x32_bf16 v[136:139],v[68:71],v[148:151],v[136:139]
v_mfma_f32_16x16x32_bf16 v[136:139],v[64:67],v[132:135],v[136:139]
v_mfma_f32_16x16x32_bf16 v[144:147],v[64:67],v[196:199],v[144:147]
v_mfma_f32_16x16x32_bf16 v[144:147],v[68:71],v[212:215],v[144:147]
v_mfma_f32_16x16x32_bf16 v[144:147],v[72:75],v[228:231],v[144:147]
buffer_load_dwordx4 a[64:67],v6,s[28:31],s20 offen offset:2048 sc1
v_mfma_f32_16x16x32_bf16 v[144:147],v[76:79],v[244:247],v[144:147]
v_mfma_f32_16x16x32_bf16 v[152:155],v[76:79],v[188:191],v[152:155]
v_mfma_f32_16x16x32_bf16 v[152:155],v[72:75],v[172:175],v[152:155]
v_mfma_f32_16x16x32_bf16 v[152:155],v[68:71],v[156:159],v[152:155]
v_mfma_f32_16x16x32_bf16 v[152:155],v[64:67],v[140:143],v[152:155]
s_mov_b64 exec,0
s_cmp_eq_u32 s60,0
s_cbranch_scc1 L_pd21_%=
s_cmp_eq_u32 s60,1
s_cbranch_scc1 L_pd22_%=
s_cmp_eq_u32 s60,2
s_cbranch_scc1 L_pd23_%=
s_branch L_trtg2S_%=
L_trtg2S_%=:
s_mov_b64 exec,-1
s_waitcnt lgkmcnt(0)
s_barrier
v_mfma_f32_16x16x32_bf16 v[160:163],v[80:83],v[32:35],v[160:163]
buffer_load_dwordx4 v24,s[48:51],s83 offen lds
v_mfma_f32_16x16x32_bf16 v[160:163],v[84:87],v[36:39],v[160:163]
ds_read_b64_tr_b16 v[64:65],v1
ds_read_b64_tr_b16 v[66:67],v1 offset:5120
v_mfma_f32_16x16x32_bf16 v[160:163],v[88:91],v[40:43],v[160:163]
ds_read_b64_tr_b16 v[68:69],v1 offset:10240
v_mfma_f32_16x16x32_bf16 v[160:163],v[92:95],v[44:47],v[160:163]
ds_read_b64_tr_b16 v[70:71],v1 offset:15360
v_mfma_f32_16x16x32_bf16 v[168:171],v[92:95],v[180:183],v[168:171]
ds_read_b64_tr_b16 v[72:73],v1 offset:20480
v_mfma_f32_16x16x32_bf16 v[168:171],v[88:91],v[164:167],v[168:171]
buffer_load_dwordx4 a[156:159],v28,s[28:31],s88 offen offset:3072 sc1
v_mfma_f32_16x16x32_bf16 v[168:171],v[84:87],v[148:151],v[168:171]
ds_read_b64_tr_b16 v[74:75],v1 offset:25600
ds_read_b64_tr_b16 v[76:77],v1 offset:30720
v_mfma_f32_16x16x32_bf16 v[168:171],v[80:83],v[132:135],v[168:171]
ds_read_b64_tr_b16 v[78:79],v1 offset:35840
v_mfma_f32_16x16x32_bf16 v[176:179],v[80:83],v[196:199],v[176:179]
v_mfma_f32_16x16x32_bf16 v[176:179],v[84:87],v[212:215],v[176:179]
v_mfma_f32_16x16x32_bf16 v[176:179],v[88:91],v[228:231],v[176:179]
buffer_load_dwordx4 a[72:75],v6,s[28:31],s21 offen offset:2048 sc1
v_mfma_f32_16x16x32_bf16 v[176:179],v[92:95],v[244:247],v[176:179]
v_mfma_f32_16x16x32_bf16 v[184:187],v[92:95],v[188:191],v[184:187]
ds_read_b64_tr_b16 v[92:93],v2 offset:30752
ds_read_b64_tr_b16 v[94:95],v2 offset:35872
v_mfma_f32_16x16x32_bf16 v[184:187],v[88:91],v[172:175],v[184:187]
ds_read_b64_tr_b16 v[88:89],v2 offset:20512
v_mfma_f32_16x16x32_bf16 v[184:187],v[84:87],v[156:159],v[184:187]
ds_read_b64_tr_b16 v[84:85],v2 offset:10272
v_mfma_f32_16x16x32_bf16 v[184:187],v[80:83],v[140:143],v[184:187]
buffer_load_dwordx4 a[140:143],v28,s[28:31],s87 offen offset:3072 sc1
v_mfma_f32_16x16x32_bf16 v[192:195],v[96:99],v[32:35],v[192:195]
s_add_u32 s87,s64,3
ds_read_b64_tr_b16 v[80:81],v2 offset:32
ds_read_b64_tr_b16 v[82:83],v2 offset:5152
v_mfma_f32_16x16x32_bf16 v[192:195],v[100:103],v[36:39],v[192:195]
s_and_b32 s87,s87,3
s_lshl_b32 s87,s87,1
ds_read_b64_tr_b16 v[86:87],v2 offset:15392
v_mfma_f32_16x16x32_bf16 v[192:195],v[104:107],v[40:43],v[192:195]
s_lshl_b32 s87,s87,16
s_add_u32 s88,s87,4096
ds_read_b64_tr_b16 v[90:91],v2 offset:25632
v_mfma_f32_16x16x32_bf16 v[192:195],v[108:111],v[44:47],v[192:195]
s_add_u32 s89,s87,8192
s_add_u32 s90,s87,12288
v_mfma_f32_16x16x32_bf16 v[200:203],v[108:111],v[180:183],v[200:203]
buffer_load_dwordx4 a[80:83],v6,s[28:31],s22 offen offset:2048 sc1
v_mfma_f32_16x16x32_bf16 v[200:203],v[104:107],v[164:167],v[200:203]
v_mfma_f32_16x16x32_bf16 v[200:203],v[100:103],v[148:151],v[200:203]
v_mfma_f32_16x16x32_bf16 v[200:203],v[96:99],v[132:135],v[200:203]
v_mfma_f32_16x16x32_bf16 v[208:211],v[96:99],v[196:199],v[208:211]
v_mfma_f32_16x16x32_bf16 v[208:211],v[100:103],v[212:215],v[208:211]
v_mfma_f32_16x16x32_bf16 v[208:211],v[104:107],v[228:231],v[208:211]
v_mfma_f32_16x16x32_bf16 v[208:211],v[108:111],v[244:247],v[208:211]
buffer_load_dwordx4 v25,s[48:51],s83 offen offset:1024 lds
v_mfma_f32_16x16x32_bf16 v[216:219],v[108:111],v[188:191],v[216:219]
ds_read_b64_tr_b16 v[108:109],v1 offset:30784
ds_read_b64_tr_b16 v[110:111],v1 offset:35904
v_mfma_f32_16x16x32_bf16 v[216:219],v[104:107],v[172:175],v[216:219]
ds_read_b64_tr_b16 v[104:105],v1 offset:20544
v_mfma_f32_16x16x32_bf16 v[216:219],v[100:103],v[156:159],v[216:219]
ds_read_b64_tr_b16 v[100:101],v1 offset:10304
v_mfma_f32_16x16x32_bf16 v[216:219],v[96:99],v[140:143],v[216:219]
ds_read_b64_tr_b16 v[96:97],v1 offset:64
v_mfma_f32_16x16x32_bf16 v[224:227],v[112:115],v[32:35],v[224:227]
buffer_load_dwordx4 a[88:91],v6,s[28:31],s23 offen offset:2048 sc1
v_mfma_f32_16x16x32_bf16 v[224:227],v[116:119],v[36:39],v[224:227]
ds_read_b64_tr_b16 v[98:99],v1 offset:5184
ds_read_b64_tr_b16 v[102:103],v1 offset:15424
v_mfma_f32_16x16x32_bf16 v[224:227],v[120:123],v[40:43],v[224:227]
ds_read_b64_tr_b16 v[106:107],v1 offset:25664
v_mfma_f32_16x16x32_bf16 v[224:227],v[124:127],v[44:47],v[224:227]
v_mfma_f32_16x16x32_bf16 v[232:235],v[124:127],v[180:183],v[232:235]
v_mfma_f32_16x16x32_bf16 v[232:235],v[120:123],v[164:167],v[232:235]
v_mfma_f32_16x16x32_bf16 v[232:235],v[116:119],v[148:151],v[232:235]
v_mfma_f32_16x16x32_bf16 v[232:235],v[112:115],v[132:135],v[232:235]
buffer_load_dwordx4 v26,s[48:51],s83 offen offset:2048 lds
v_mfma_f32_16x16x32_bf16 v[240:243],v[112:115],v[196:199],v[240:243]
v_mfma_f32_16x16x32_bf16 v[240:243],v[116:119],v[212:215],v[240:243]
v_mfma_f32_16x16x32_bf16 v[240:243],v[120:123],v[228:231],v[240:243]
v_mfma_f32_16x16x32_bf16 v[240:243],v[124:127],v[244:247],v[240:243]
v_mfma_f32_16x16x32_bf16 v[248:251],v[124:127],v[188:191],v[248:251]
ds_read_b64_tr_b16 v[124:125],v2 offset:30816
ds_read_b64_tr_b16 v[126:127],v2 offset:35936
v_mfma_f32_16x16x32_bf16 v[248:251],v[120:123],v[172:175],v[248:251]
buffer_load_dwordx4 a[120:123],v28,s[28:31],s23 offen offset:3072 sc1
v_mfma_f32_16x16x32_bf16 v[248:251],v[116:119],v[156:159],v[248:251]
ds_read_b64_tr_b16 v[116:117],v2 offset:10336
ds_read_b64_tr_b16 v[118:119],v2 offset:15456
v_mfma_f32_16x16x32_bf16 v[248:251],v[112:115],v[140:143],v[248:251]
s_waitcnt vmcnt(39) lgkmcnt(4)
ds_read_b64_tr_b16 v[112:113],v2 offset:96
v_mfma_f32_16x16x32_bf16 a[192:195],v[64:67],a[128:131],a[192:195]
ds_read_b64_tr_b16 v[114:115],v2 offset:5216
v_mfma_f32_16x16x32_bf16 a[192:195],v[68:71],a[144:147],a[192:195]
s_waitcnt vmcnt(37)
ds_read_b64_tr_b16 v[120:121],v2 offset:20576
v_mfma_f32_16x16x32_bf16 a[192:195],v[72:75],a[160:163],a[192:195]
s_waitcnt vmcnt(35)
ds_read_b64_tr_b16 v[122:123],v2 offset:25696
v_mfma_f32_16x16x32_bf16 a[192:195],v[76:79],a[176:179],a[192:195]
s_waitcnt vmcnt(27)
v_mfma_f32_16x16x32_bf16 a[196:199],v[76:79],a[180:183],a[196:199]
buffer_load_dwordx4 v27,s[48:51],s83 offen offset:3072 lds
v_mfma_f32_16x16x32_bf16 a[196:199],v[72:75],a[164:167],a[196:199]
s_add_u32 m0,s86,126976
v_mfma_f32_16x16x32_bf16 a[196:199],v[68:71],a[148:151],a[196:199]
s_waitcnt vmcnt(24)
v_mfma_f32_16x16x32_bf16 a[196:199],v[64:67],a[132:135],a[196:199]
v_mfma_f32_16x16x32_bf16 a[200:203],v[64:67],a[136:139],a[200:203]
s_waitcnt vmcnt(21)
v_mfma_f32_16x16x32_bf16 a[200:203],v[68:71],a[152:155],a[200:203]
s_waitcnt vmcnt(15)
v_mfma_f32_16x16x32_bf16 a[200:203],v[72:75],a[168:171],a[200:203]
buffer_load_dwordx4 a[112:115],v28,s[28:31],s22 offen offset:3072 sc1
v_mfma_f32_16x16x32_bf16 a[200:203],v[76:79],a[184:187],a[200:203]
s_waitcnt vmcnt(14)
v_mfma_f32_16x16x32_bf16 a[204:207],v[76:79],a[188:191],a[204:207]
s_waitcnt vmcnt(12)
ds_read_b64_tr_b16 v[76:77],v1 offset:30848
ds_read_b64_tr_b16 v[78:79],v1 offset:35968
v_mfma_f32_16x16x32_bf16 a[204:207],v[72:75],a[172:175],a[204:207]
s_waitcnt vmcnt(9)
ds_read_b64_tr_b16 v[72:73],v1 offset:20608
v_mfma_f32_16x16x32_bf16 a[204:207],v[68:71],a[156:159],a[204:207]
s_waitcnt vmcnt(7)
ds_read_b64_tr_b16 v[68:69],v1 offset:10368
v_mfma_f32_16x16x32_bf16 a[204:207],v[64:67],a[140:143],a[204:207]
ds_read_b64_tr_b16 v[64:65],v1 offset:128
v_mfma_f32_16x16x32_bf16 a[208:211],v[80:83],a[128:131],a[208:211]
ds_read_b64_tr_b16 v[66:67],v1 offset:5248
ds_read_b64_tr_b16 v[70:71],v1 offset:15488
v_mfma_f32_16x16x32_bf16 a[208:211],v[84:87],a[144:147],a[208:211]
buffer_load_dwordx4 v7,s[48:51],s83 offen lds
v_mfma_f32_16x16x32_bf16 a[208:211],v[88:91],a[160:163],a[208:211]
s_add_u32 m0,s86,128000
s_add_u32 s83,s64,1
ds_read_b64_tr_b16 v[74:75],v1 offset:25728
v_mfma_f32_16x16x32_bf16 a[208:211],v[92:95],a[176:179],a[208:211]
s_and_b32 s83,s83,3
s_lshl_b32 s83,s83,1
s_or_b32 s83,s83,1
v_mfma_f32_16x16x32_bf16 a[212:215],v[92:95],a[180:183],a[212:215]
s_mul_i32 s83,s83,163840
s_add_u32 s83,s83,s85
v_mfma_f32_16x16x32_bf16 a[212:215],v[88:91],a[164:167],a[212:215]
v_mfma_f32_16x16x32_bf16 a[212:215],v[84:87],a[148:151],a[212:215]
v_mfma_f32_16x16x32_bf16 a[212:215],v[80:83],a[132:135],a[212:215]
)A"
R"A(buffer_load_dwordx4 a[104:107],v28,s[28:31],s21 offen offset:3072 sc1
v_mfma_f32_16x16x32_bf16 a[216:219],v[80:83],a[136:139],a[216:219]
v_mfma_f32_16x16x32_bf16 a[216:219],v[84:87],a[152:155],a[216:219]
v_mfma_f32_16x16x32_bf16 a[216:219],v[88:91],a[168:171],a[216:219]
v_mfma_f32_16x16x32_bf16 a[216:219],v[92:95],a[184:187],a[216:219]
v_mfma_f32_16x16x32_bf16 a[220:223],v[92:95],a[188:191],a[220:223]
ds_read_b64_tr_b16 v[92:93],v2 offset:30880
ds_read_b64_tr_b16 v[94:95],v2 offset:36000
v_mfma_f32_16x16x32_bf16 a[220:223],v[88:91],a[172:175],a[220:223]
ds_read_b64_tr_b16 v[88:89],v2 offset:20640
v_mfma_f32_16x16x32_bf16 a[220:223],v[84:87],a[156:159],a[220:223]
buffer_load_dwordx4 v24,s[48:51],s84 offen lds
v_mfma_f32_16x16x32_bf16 a[220:223],v[80:83],a[140:143],a[220:223]
ds_read_b64_tr_b16 v[80:81],v2 offset:160
ds_read_b64_tr_b16 v[82:83],v2 offset:5280
s_barrier
v_mfma_f32_16x16x32_bf16 a[224:227],v[96:99],a[128:131],a[224:227]
ds_read_b64_tr_b16 v[84:85],v2 offset:10400
v_mfma_f32_16x16x32_bf16 a[224:227],v[100:103],a[144:147],a[224:227]
ds_read_b64_tr_b16 v[86:87],v2 offset:15520
v_mfma_f32_16x16x32_bf16 a[224:227],v[104:107],a[160:163],a[224:227]
buffer_store_dwordx2 v[8:9],v10,s[44:47],s18 offen offset:1280 sc1
v_mfma_f32_16x16x32_bf16 a[224:227],v[108:111],a[176:179],a[224:227]
ds_read_b64_tr_b16 v[90:91],v2 offset:25760
v_mfma_f32_16x16x32_bf16 a[228:231],v[108:111],a[180:183],a[228:231]
v_mfma_f32_16x16x32_bf16 a[228:231],v[104:107],a[164:167],a[228:231]
v_mfma_f32_16x16x32_bf16 a[228:231],v[100:103],a[148:151],a[228:231]
v_mfma_f32_16x16x32_bf16 a[228:231],v[96:99],a[132:135],a[228:231]
buffer_load_dwordx4 a[96:99],v28,s[28:31],s20 offen offset:3072 sc1
v_mfma_f32_16x16x32_bf16 a[232:235],v[96:99],a[136:139],a[232:235]
s_add_u32 s20,s64,1
s_and_b32 s20,s20,3
s_lshl_b32 s20,s20,1
v_mfma_f32_16x16x32_bf16 a[232:235],v[100:103],a[152:155],a[232:235]
s_or_b32 s20,s20,1
s_lshl_b32 s20,s20,16
s_add_u32 s21,s20,4096
v_mfma_f32_16x16x32_bf16 a[232:235],v[104:107],a[168:171],a[232:235]
s_add_u32 s22,s20,8192
s_add_u32 s23,s20,12288
v_mfma_f32_16x16x32_bf16 a[232:235],v[108:111],a[184:187],a[232:235]
v_mfma_f32_16x16x32_bf16 a[236:239],v[108:111],a[188:191],a[236:239]
ds_read_b64_tr_b16 v[108:109],v1 offset:30912
ds_read_b64_tr_b16 v[110:111],v1 offset:36032
v_mfma_f32_16x16x32_bf16 a[236:239],v[104:107],a[172:175],a[236:239]
ds_read_b64_tr_b16 v[104:105],v1 offset:20672
v_mfma_f32_16x16x32_bf16 a[236:239],v[100:103],a[156:159],a[236:239]
buffer_load_dwordx4 v25,s[48:51],s84 offen offset:1024 lds
v_mfma_f32_16x16x32_bf16 a[236:239],v[96:99],a[140:143],a[236:239]
s_waitcnt lgkmcnt(3)
ds_read_b64_tr_b16 v[96:97],v1 offset:192
ds_read_b64_tr_b16 v[98:99],v1 offset:5312
v_mfma_f32_16x16x32_bf16 a[240:243],v[112:115],a[128:131],a[240:243]
ds_read_b64_tr_b16 v[100:101],v1 offset:10432
v_mfma_f32_16x16x32_bf16 a[240:243],v[116:119],a[144:147],a[240:243]
ds_read_b64_tr_b16 v[102:103],v1 offset:15552
v_mfma_f32_16x16x32_bf16 a[240:243],v[120:123],a[160:163],a[240:243]
ds_read_b64_tr_b16 v[106:107],v1 offset:25792
v_mfma_f32_16x16x32_bf16 a[240:243],v[124:127],a[176:179],a[240:243]
v_mfma_f32_16x16x32_bf16 a[244:247],v[124:127],a[180:183],a[244:247]
v_mfma_f32_16x16x32_bf16 a[244:247],v[120:123],a[164:167],a[244:247]
buffer_load_dwordx4 v26,s[48:51],s84 offen offset:2048 lds
v_mfma_f32_16x16x32_bf16 a[244:247],v[116:119],a[148:151],a[244:247]
v_mfma_f32_16x16x32_bf16 a[244:247],v[112:115],a[132:135],a[244:247]
v_mfma_f32_16x16x32_bf16 a[248:251],v[112:115],a[136:139],a[248:251]
v_mfma_f32_16x16x32_bf16 a[248:251],v[116:119],a[152:155],a[248:251]
v_mfma_f32_16x16x32_bf16 a[248:251],v[120:123],a[168:171],a[248:251]
v_mfma_f32_16x16x32_bf16 a[248:251],v[124:127],a[184:187],a[248:251]
v_mfma_f32_16x16x32_bf16 a[252:255],v[124:127],a[188:191],a[252:255]
buffer_load_dwordx4 v27,s[48:51],s84 offen offset:3072 lds
v_mfma_f32_16x16x32_bf16 a[252:255],v[120:123],a[172:175],a[252:255]
s_add_u32 m0,s86,132096
ds_read_b64_tr_b16 v[120:121],v2 offset:20704
ds_read_b64_tr_b16 v[122:123],v2 offset:25824
v_mfma_f32_16x16x32_bf16 a[252:255],v[116:119],a[156:159],a[252:255]
ds_read_b64_tr_b16 v[116:117],v2 offset:10464
v_mfma_f32_16x16x32_bf16 a[252:255],v[112:115],a[140:143],a[252:255]
ds_read_b64_tr_b16 v[112:113],v2 offset:224
v_mfma_f32_16x16x32_bf16 a[0:3],v[64:67],a[128:131],a[0:3]
ds_read_b64_tr_b16 v[114:115],v2 offset:5344
v_mfma_f32_16x16x32_bf16 a[0:3],v[68:71],a[144:147],a[0:3]
ds_read_b64_tr_b16 v[118:119],v2 offset:15584
ds_read_b64_tr_b16 v[124:125],v2 offset:30944
v_mfma_f32_16x16x32_bf16 a[0:3],v[72:75],a[160:163],a[0:3]
ds_read_b64_tr_b16 v[126:127],v2 offset:36064
v_mfma_f32_16x16x32_bf16 a[0:3],v[76:79],a[176:179],a[0:3]
buffer_load_dwordx4 v7,s[48:51],s84 offen lds
v_mfma_f32_16x16x32_bf16 a[4:7],v[76:79],a[180:183],a[4:7]
s_add_u32 m0,s86,81920
s_add_u32 s84,s83,20480
v_mfma_f32_16x16x32_bf16 a[4:7],v[72:75],a[164:167],a[4:7]
v_mfma_f32_16x16x32_bf16 a[4:7],v[68:71],a[148:151],a[4:7]
v_mfma_f32_16x16x32_bf16 a[4:7],v[64:67],a[132:135],a[4:7]
v_mfma_f32_16x16x32_bf16 a[8:11],v[64:67],a[136:139],a[8:11]
v_mfma_f32_16x16x32_bf16 a[8:11],v[68:71],a[152:155],a[8:11]
v_mfma_f32_16x16x32_bf16 a[8:11],v[72:75],a[168:171],a[8:11]
buffer_load_dwordx4 v24,s[48:51],s83 offen lds
v_mfma_f32_16x16x32_bf16 a[8:11],v[76:79],a[184:187],a[8:11]
v_mfma_f32_16x16x32_bf16 a[12:15],v[76:79],a[188:191],a[12:15]
ds_read_b64_tr_b16 v[76:77],v1 offset:30976
ds_read_b64_tr_b16 v[78:79],v1 offset:36096
v_mfma_f32_16x16x32_bf16 a[12:15],v[72:75],a[172:175],a[12:15]
ds_read_b64_tr_b16 v[72:73],v1 offset:20736
v_mfma_f32_16x16x32_bf16 a[12:15],v[68:71],a[156:159],a[12:15]
ds_read_b64_tr_b16 v[68:69],v1 offset:10496
v_mfma_f32_16x16x32_bf16 a[12:15],v[64:67],a[140:143],a[12:15]
ds_read_b64_tr_b16 v[64:65],v1 offset:256
v_mfma_f32_16x16x32_bf16 a[16:19],v[80:83],a[128:131],a[16:19]
ds_read_b64_tr_b16 v[66:67],v1 offset:5376
ds_read_b64_tr_b16 v[70:71],v1 offset:15616
v_mfma_f32_16x16x32_bf16 a[16:19],v[84:87],a[144:147],a[16:19]
buffer_load_dwordx4 v25,s[48:51],s83 offen offset:1024 lds
v_mfma_f32_16x16x32_bf16 a[16:19],v[88:91],a[160:163],a[16:19]
ds_read_b64_tr_b16 v[74:75],v1 offset:25856
v_add_u32 v1,0xffff6000,v1
v_mfma_f32_16x16x32_bf16 a[16:19],v[92:95],a[176:179],a[16:19]
v_mfma_f32_16x16x32_bf16 a[20:23],v[92:95],a[180:183],a[20:23]
v_mfma_f32_16x16x32_bf16 a[20:23],v[88:91],a[164:167],a[20:23]
v_mfma_f32_16x16x32_bf16 a[20:23],v[84:87],a[148:151],a[20:23]
v_mfma_f32_16x16x32_bf16 a[20:23],v[80:83],a[132:135],a[20:23]
v_mfma_f32_16x16x32_bf16 a[24:27],v[80:83],a[136:139],a[24:27]
buffer_load_dwordx4 v26,s[48:51],s83 offen offset:2048 lds
v_mfma_f32_16x16x32_bf16 a[24:27],v[84:87],a[152:155],a[24:27]
v_mfma_f32_16x16x32_bf16 a[24:27],v[88:91],a[168:171],a[24:27]
v_mfma_f32_16x16x32_bf16 a[24:27],v[92:95],a[184:187],a[24:27]
v_mfma_f32_16x16x32_bf16 a[28:31],v[92:95],a[188:191],a[28:31]
ds_read_b64_tr_b16 v[92:93],v2 offset:31008
ds_read_b64_tr_b16 v[94:95],v2 offset:36128
v_mfma_f32_16x16x32_bf16 a[28:31],v[88:91],a[172:175],a[28:31]
ds_read_b64_tr_b16 v[88:89],v2 offset:20768
v_mfma_f32_16x16x32_bf16 a[28:31],v[84:87],a[156:159],a[28:31]
s_waitcnt lgkmcnt(3)
ds_read_b64_tr_b16 v[84:85],v2 offset:10528
v_mfma_f32_16x16x32_bf16 a[28:31],v[80:83],a[140:143],a[28:31]
buffer_load_dwordx4 v27,s[48:51],s83 offen offset:3072 lds
v_mfma_f32_16x16x32_bf16 v[128:131],v[96:99],a[128:131],v[128:131]
s_add_u32 m0,s86,86016
ds_read_b64_tr_b16 v[80:81],v2 offset:288
ds_read_b64_tr_b16 v[82:83],v2 offset:5408
v_mfma_f32_16x16x32_bf16 v[128:131],v[100:103],a[144:147],v[128:131]
ds_read_b64_tr_b16 v[86:87],v2 offset:15648
v_mfma_f32_16x16x32_bf16 v[128:131],v[104:107],a[160:163],v[128:131]
ds_read_b64_tr_b16 v[90:91],v2 offset:25888
v_add_u32 v2,0xffff6000,v2
v_mfma_f32_16x16x32_bf16 v[128:131],v[108:111],a[176:179],v[128:131]
v_mfma_f32_16x16x32_bf16 v[136:139],v[108:111],a[180:183],v[136:139]
v_mfma_f32_16x16x32_bf16 v[136:139],v[104:107],a[164:167],v[136:139]
v_mfma_f32_16x16x32_bf16 v[136:139],v[100:103],a[148:151],v[136:139]
buffer_load_dwordx4 v7,s[48:51],s83 offen lds
v_mfma_f32_16x16x32_bf16 v[136:139],v[96:99],a[132:135],v[136:139]
s_add_u32 m0,s86,87040
s_add_u32 s83,s64,2
s_and_b32 s83,s83,3
v_mfma_f32_16x16x32_bf16 v[144:147],v[96:99],a[136:139],v[144:147]
s_lshl_b32 s83,s83,1
s_or_b32 s83,s83,1
s_mul_i32 s83,s83,163840
v_mfma_f32_16x16x32_bf16 v[144:147],v[100:103],a[152:155],v[144:147]
s_add_u32 s83,s83,s85
v_mfma_f32_16x16x32_bf16 v[144:147],v[104:107],a[168:171],v[144:147]
v_mfma_f32_16x16x32_bf16 v[144:147],v[108:111],a[184:187],v[144:147]
v_mfma_f32_16x16x32_bf16 v[152:155],v[108:111],a[188:191],v[152:155]
ds_read_b64_tr_b16 v[108:109],v1 offset:30720
ds_read_b64_tr_b16 v[110:111],v1 offset:35840
v_mfma_f32_16x16x32_bf16 v[152:155],v[104:107],a[172:175],v[152:155]
buffer_load_dwordx4 v24,s[48:51],s84 offen lds
v_mfma_f32_16x16x32_bf16 v[152:155],v[100:103],a[156:159],v[152:155]
ds_read_b64_tr_b16 v[100:101],v1 offset:10240
ds_read_b64_tr_b16 v[102:103],v1 offset:15360
v_mfma_f32_16x16x32_bf16 v[152:155],v[96:99],a[140:143],v[152:155]
ds_read_b64_tr_b16 v[96:97],v1
s_waitcnt lgkmcnt(5)
s_barrier
v_mfma_f32_16x16x32_bf16 v[160:163],v[112:115],a[128:131],v[160:163]
ds_read_b64_tr_b16 v[98:99],v1 offset:5120
v_mfma_f32_16x16x32_bf16 v[160:163],v[116:119],a[144:147],v[160:163]
ds_read_b64_tr_b16 v[104:105],v1 offset:20480
v_mfma_f32_16x16x32_bf16 v[160:163],v[120:123],a[160:163],v[160:163]
ds_read_b64_tr_b16 v[106:107],v1 offset:25600
v_mfma_f32_16x16x32_bf16 v[160:163],v[124:127],a[176:179],v[160:163]
buffer_load_dwordx4 v25,s[48:51],s84 offen offset:1024 lds
v_mfma_f32_16x16x32_bf16 v[168:171],v[124:127],a[180:183],v[168:171]
v_mfma_f32_16x16x32_bf16 v[168:171],v[120:123],a[164:167],v[168:171]
L_pd21_%=:
s_cbranch_execz L_trtg2S_%=
v_mfma_f32_16x16x32_bf16 v[168:171],v[116:119],a[148:151],v[168:171]
v_mfma_f32_16x16x32_bf16 v[168:171],v[112:115],a[132:135],v[168:171]
v_mfma_f32_16x16x32_bf16 v[176:179],v[112:115],a[136:139],v[176:179]
v_mfma_f32_16x16x32_bf16 v[176:179],v[116:119],a[152:155],v[176:179]
v_mfma_f32_16x16x32_bf16 v[176:179],v[120:123],a[168:171],v[176:179]
buffer_load_dwordx4 v26,s[48:51],s84 offen offset:2048 lds
v_mfma_f32_16x16x32_bf16 v[176:179],v[124:127],a[184:187],v[176:179]
v_mfma_f32_16x16x32_bf16 v[184:187],v[124:127],a[188:191],v[184:187]
ds_read_b64_tr_b16 v[124:125],v2 offset:30752
ds_read_b64_tr_b16 v[126:127],v2 offset:35872
v_mfma_f32_16x16x32_bf16 v[184:187],v[120:123],a[172:175],v[184:187]
ds_read_b64_tr_b16 v[120:121],v2 offset:20512
v_mfma_f32_16x16x32_bf16 v[184:187],v[116:119],a[156:159],v[184:187]
ds_read_b64_tr_b16 v[116:117],v2 offset:10272
v_mfma_f32_16x16x32_bf16 v[184:187],v[112:115],a[140:143],v[184:187]
ds_read_b64_tr_b16 v[112:113],v2 offset:32
v_mfma_f32_16x16x32_bf16 v[192:195],v[64:67],a[128:131],v[192:195]
ds_read_b64_tr_b16 v[114:115],v2 offset:5152
ds_read_b64_tr_b16 v[118:119],v2 offset:15392
v_mfma_f32_16x16x32_bf16 v[192:195],v[68:71],a[144:147],v[192:195]
buffer_load_dwordx4 v27,s[48:51],s84 offen offset:3072 lds
v_mfma_f32_16x16x32_bf16 v[192:195],v[72:75],a[160:163],v[192:195]
s_add_u32 m0,s86,91136
ds_read_b64_tr_b16 v[122:123],v2 offset:25632
v_mfma_f32_16x16x32_bf16 v[192:195],v[76:79],a[176:179],v[192:195]
v_mfma_f32_16x16x32_bf16 v[200:203],v[76:79],a[180:183],v[200:203]
v_mfma_f32_16x16x32_bf16 v[200:203],v[72:75],a[164:167],v[200:203]
v_mfma_f32_16x16x32_bf16 v[200:203],v[68:71],a[148:151],v[200:203]
v_mfma_f32_16x16x32_bf16 v[200:203],v[64:67],a[132:135],v[200:203]
v_mfma_f32_16x16x32_bf16 v[208:211],v[64:67],a[136:139],v[208:211]
buffer_load_dwordx4 v7,s[48:51],s84 offen lds
v_mfma_f32_16x16x32_bf16 v[208:211],v[68:71],a[152:155],v[208:211]
s_add_u32 m0,s86,40960
s_add_u32 s84,s83,20480
v_mfma_f32_16x16x32_bf16 v[208:211],v[72:75],a[168:171],v[208:211]
v_mfma_f32_16x16x32_bf16 v[208:211],v[76:79],a[184:187],v[208:211]
v_mfma_f32_16x16x32_bf16 v[216:219],v[76:79],a[188:191],v[216:219]
ds_read_b64_tr_b16 v[76:77],v1 offset:30784
ds_read_b64_tr_b16 v[78:79],v1 offset:35904
)A"
R"A(v_mfma_f32_16x16x32_bf16 v[216:219],v[72:75],a[172:175],v[216:219]
buffer_load_dwordx2 v[4:5],v3,s[44:47],s91 offen offset:1280 sc1
v_mfma_f32_16x16x32_bf16 v[216:219],v[68:71],a[156:159],v[216:219]
ds_read_b64_tr_b16 v[68:69],v1 offset:10304
ds_read_b64_tr_b16 v[70:71],v1 offset:15424
v_mfma_f32_16x16x32_bf16 v[216:219],v[64:67],a[140:143],v[216:219]
ds_read_b64_tr_b16 v[64:65],v1 offset:64
v_mfma_f32_16x16x32_bf16 v[224:227],v[80:83],a[128:131],v[224:227]
ds_read_b64_tr_b16 v[66:67],v1 offset:5184
v_mfma_f32_16x16x32_bf16 v[224:227],v[84:87],a[144:147],v[224:227]
ds_read_b64_tr_b16 v[72:73],v1 offset:20544
v_mfma_f32_16x16x32_bf16 v[224:227],v[88:91],a[160:163],v[224:227]
buffer_load_dwordx4 v[32:35],v6,s[28:31],s87 offen sc1
v_mfma_f32_16x16x32_bf16 v[224:227],v[92:95],a[176:179],v[224:227]
ds_read_b64_tr_b16 v[74:75],v1 offset:25664
v_mfma_f32_16x16x32_bf16 v[232:235],v[92:95],a[180:183],v[232:235]
v_mfma_f32_16x16x32_bf16 v[232:235],v[88:91],a[164:167],v[232:235]
v_mfma_f32_16x16x32_bf16 v[232:235],v[84:87],a[148:151],v[232:235]
v_mfma_f32_16x16x32_bf16 v[232:235],v[80:83],a[132:135],v[232:235]
buffer_load_dwordx4 v[36:39],v6,s[28:31],s88 offen sc1
v_mfma_f32_16x16x32_bf16 v[240:243],v[80:83],a[136:139],v[240:243]
v_mfma_f32_16x16x32_bf16 v[240:243],v[84:87],a[152:155],v[240:243]
v_mfma_f32_16x16x32_bf16 v[240:243],v[88:91],a[168:171],v[240:243]
v_mfma_f32_16x16x32_bf16 v[240:243],v[92:95],a[184:187],v[240:243]
v_mfma_f32_16x16x32_bf16 v[248:251],v[92:95],a[188:191],v[248:251]
buffer_load_dwordx4 v[40:43],v6,s[28:31],s89 offen sc1
v_mfma_f32_16x16x32_bf16 v[248:251],v[88:91],a[172:175],v[248:251]
ds_read_b64_tr_b16 v[88:89],v2 offset:20576
ds_read_b64_tr_b16 v[90:91],v2 offset:25696
v_mfma_f32_16x16x32_bf16 v[248:251],v[84:87],a[156:159],v[248:251]
ds_read_b64_tr_b16 v[84:85],v2 offset:10336
v_mfma_f32_16x16x32_bf16 v[248:251],v[80:83],a[140:143],v[248:251]
s_waitcnt lgkmcnt(3)
ds_read_b64_tr_b16 v[80:81],v2 offset:96
v_mfma_f32_16x16x32_bf16 a[192:195],v[96:99],a[68:71],a[192:195]
ds_read_b64_tr_b16 v[82:83],v2 offset:5216
v_mfma_f32_16x16x32_bf16 a[192:195],v[100:103],a[76:79],a[192:195]
buffer_load_dwordx4 v[196:199],v6,s[28:31],s87 offen offset:2048 sc1
v_mfma_f32_16x16x32_bf16 a[192:195],v[104:107],a[84:87],a[192:195]
ds_read_b64_tr_b16 v[86:87],v2 offset:15456
ds_read_b64_tr_b16 v[92:93],v2 offset:30816
v_mfma_f32_16x16x32_bf16 a[192:195],v[108:111],a[92:95],a[192:195]
ds_read_b64_tr_b16 v[94:95],v2 offset:35936
v_mfma_f32_16x16x32_bf16 a[196:199],v[108:111],a[124:127],a[196:199]
v_mfma_f32_16x16x32_bf16 a[196:199],v[104:107],a[116:119],a[196:199]
v_mfma_f32_16x16x32_bf16 a[196:199],v[100:103],a[108:111],a[196:199]
buffer_load_dwordx4 v[44:47],v6,s[28:31],s90 offen sc1
v_mfma_f32_16x16x32_bf16 a[196:199],v[96:99],a[100:103],a[196:199]
v_mfma_f32_16x16x32_bf16 a[200:203],v[96:99],a[64:67],a[200:203]
v_mfma_f32_16x16x32_bf16 a[200:203],v[100:103],a[72:75],a[200:203]
s_waitcnt vmcnt(31)
v_mfma_f32_16x16x32_bf16 a[200:203],v[104:107],a[80:83],a[200:203]
s_waitcnt vmcnt(27)
v_mfma_f32_16x16x32_bf16 a[200:203],v[108:111],a[88:91],a[200:203]
buffer_load_dwordx4 v[212:215],v6,s[28:31],s88 offen offset:2048 sc1
v_mfma_f32_16x16x32_bf16 a[204:207],v[108:111],a[120:123],a[204:207]
s_waitcnt vmcnt(26)
ds_read_b64_tr_b16 v[108:109],v1 offset:30848
ds_read_b64_tr_b16 v[110:111],v1 offset:35968
v_mfma_f32_16x16x32_bf16 a[204:207],v[104:107],a[112:115],a[204:207]
s_waitcnt vmcnt(24)
ds_read_b64_tr_b16 v[104:105],v1 offset:20608
v_mfma_f32_16x16x32_bf16 a[204:207],v[100:103],a[104:107],a[204:207]
s_waitcnt vmcnt(21)
ds_read_b64_tr_b16 v[100:101],v1 offset:10368
v_mfma_f32_16x16x32_bf16 a[204:207],v[96:99],a[96:99],a[204:207]
ds_read_b64_tr_b16 v[96:97],v1 offset:128
v_mfma_f32_16x16x32_bf16 a[208:211],v[112:115],a[68:71],a[208:211]
buffer_load_dwordx4 v[180:183],v6,s[28:31],s90 offen offset:1024 sc1
v_mfma_f32_16x16x32_bf16 a[208:211],v[116:119],a[76:79],a[208:211]
ds_read_b64_tr_b16 v[98:99],v1 offset:5248
ds_read_b64_tr_b16 v[102:103],v1 offset:15488
v_mfma_f32_16x16x32_bf16 a[208:211],v[120:123],a[84:87],a[208:211]
ds_read_b64_tr_b16 v[106:107],v1 offset:25728
v_mfma_f32_16x16x32_bf16 a[208:211],v[124:127],a[92:95],a[208:211]
v_mfma_f32_16x16x32_bf16 a[212:215],v[124:127],a[124:127],a[212:215]
v_mfma_f32_16x16x32_bf16 a[212:215],v[120:123],a[116:119],a[212:215]
buffer_load_dwordx4 v[228:231],v6,s[28:31],s89 offen offset:2048 sc1
v_mfma_f32_16x16x32_bf16 a[212:215],v[116:119],a[108:111],a[212:215]
v_mfma_f32_16x16x32_bf16 a[212:215],v[112:115],a[100:103],a[212:215]
v_mfma_f32_16x16x32_bf16 a[216:219],v[112:115],a[64:67],a[216:219]
v_mfma_f32_16x16x32_bf16 a[216:219],v[116:119],a[72:75],a[216:219]
v_mfma_f32_16x16x32_bf16 a[216:219],v[120:123],a[80:83],a[216:219]
buffer_load_dwordx4 v[164:167],v6,s[28:31],s89 offen offset:1024 sc1
v_mfma_f32_16x16x32_bf16 a[216:219],v[124:127],a[88:91],a[216:219]
v_mfma_f32_16x16x32_bf16 a[220:223],v[124:127],a[120:123],a[220:223]
ds_read_b64_tr_b16 v[124:125],v2 offset:30880
ds_read_b64_tr_b16 v[126:127],v2 offset:36000
v_mfma_f32_16x16x32_bf16 a[220:223],v[120:123],a[112:115],a[220:223]
ds_read_b64_tr_b16 v[120:121],v2 offset:20640
v_mfma_f32_16x16x32_bf16 a[220:223],v[116:119],a[104:107],a[220:223]
ds_read_b64_tr_b16 v[116:117],v2 offset:10400
v_mfma_f32_16x16x32_bf16 a[220:223],v[112:115],a[96:99],a[220:223]
buffer_load_dwordx4 v[148:151],v6,s[28:31],s88 offen offset:1024 sc1
v_mfma_f32_16x16x32_bf16 a[224:227],v[64:67],a[68:71],a[224:227]
ds_read_b64_tr_b16 v[112:113],v2 offset:160
ds_read_b64_tr_b16 v[114:115],v2 offset:5280
v_mfma_f32_16x16x32_bf16 a[224:227],v[68:71],a[76:79],a[224:227]
ds_read_b64_tr_b16 v[118:119],v2 offset:15520
v_mfma_f32_16x16x32_bf16 a[224:227],v[72:75],a[84:87],a[224:227]
ds_read_b64_tr_b16 v[122:123],v2 offset:25760
v_mfma_f32_16x16x32_bf16 a[224:227],v[76:79],a[92:95],a[224:227]
v_mfma_f32_16x16x32_bf16 a[228:231],v[76:79],a[124:127],a[228:231]
buffer_load_dwordx4 v[244:247],v6,s[28:31],s90 offen offset:2048 sc1
v_mfma_f32_16x16x32_bf16 a[228:231],v[72:75],a[116:119],a[228:231]
v_mfma_f32_16x16x32_bf16 a[228:231],v[68:71],a[108:111],a[228:231]
v_mfma_f32_16x16x32_bf16 a[228:231],v[64:67],a[100:103],a[228:231]
v_mfma_f32_16x16x32_bf16 a[232:235],v[64:67],a[64:67],a[232:235]
v_mfma_f32_16x16x32_bf16 a[232:235],v[68:71],a[72:75],a[232:235]
buffer_load_dwordx4 v[188:191],v28,s[28:31],s90 offen offset:3072 sc1
v_mfma_f32_16x16x32_bf16 a[232:235],v[72:75],a[80:83],a[232:235]
s_waitcnt vmcnt(12)
v_cmp_ne_u32 s[56:57],s52,v4
v_cmp_ne_u32 s[58:59],s53,v5
v_mfma_f32_16x16x32_bf16 a[232:235],v[76:79],a[88:91],a[232:235]
s_or_b64 s[56:57],s[56:57],s[58:59]
s_cbranch_scc1 L_fr7_%=
L_fo7_%=:
s_lshl_b32 s91,s64,6
v_mfma_f32_16x16x32_bf16 a[236:239],v[76:79],a[120:123],a[236:239]
ds_read_b64_tr_b16 v[76:77],v1 offset:30912
ds_read_b64_tr_b16 v[78:79],v1 offset:36032
v_mfma_f32_16x16x32_bf16 a[236:239],v[72:75],a[112:115],a[236:239]
ds_read_b64_tr_b16 v[72:73],v1 offset:20672
v_bfe_u32 v3,v6,4,2
v_mfma_f32_16x16x32_bf16 a[236:239],v[68:71],a[104:107],a[236:239]
buffer_load_dwordx4 a[128:131],v6,s[28:31],s20 offen sc1
v_mfma_f32_16x16x32_bf16 a[236:239],v[64:67],a[96:99],a[236:239]
s_waitcnt lgkmcnt(3)
ds_read_b64_tr_b16 v[64:65],v1 offset:192
ds_read_b64_tr_b16 v[66:67],v1 offset:5312
v_mfma_f32_16x16x32_bf16 a[240:243],v[80:83],a[68:71],a[240:243]
ds_read_b64_tr_b16 v[68:69],v1 offset:10432
v_min_u32 v3,2,v3
v_mfma_f32_16x16x32_bf16 a[240:243],v[84:87],a[76:79],a[240:243]
ds_read_b64_tr_b16 v[70:71],v1 offset:15552
v_add3_u32 v3,v3,s63,1
v_mfma_f32_16x16x32_bf16 a[240:243],v[88:91],a[84:87],a[240:243]
ds_read_b64_tr_b16 v[74:75],v1 offset:25792
v_and_b32 v3,3,v3
v_mfma_f32_16x16x32_bf16 a[240:243],v[92:95],a[92:95],a[240:243]
buffer_load_dwordx4 v[132:135],v6,s[28:31],s87 offen offset:1024 sc1
v_mfma_f32_16x16x32_bf16 a[244:247],v[92:95],a[124:127],a[244:247]
v_lshlrev_b32 v3,8,v3
v_mfma_f32_16x16x32_bf16 a[244:247],v[88:91],a[116:119],a[244:247]
v_mfma_f32_16x16x32_bf16 a[244:247],v[84:87],a[108:111],a[244:247]
v_mfma_f32_16x16x32_bf16 a[244:247],v[80:83],a[100:103],a[244:247]
v_mfma_f32_16x16x32_bf16 a[248:251],v[80:83],a[64:67],a[248:251]
buffer_load_dwordx4 v[172:175],v28,s[28:31],s89 offen offset:3072 sc1
v_mfma_f32_16x16x32_bf16 a[248:251],v[84:87],a[72:75],a[248:251]
v_mfma_f32_16x16x32_bf16 a[248:251],v[88:91],a[80:83],a[248:251]
v_mfma_f32_16x16x32_bf16 a[248:251],v[92:95],a[88:91],a[248:251]
v_mfma_f32_16x16x32_bf16 a[252:255],v[92:95],a[120:123],a[252:255]
ds_read_b64_tr_b16 v[92:93],v2 offset:30944
ds_read_b64_tr_b16 v[94:95],v2 offset:36064
v_mfma_f32_16x16x32_bf16 a[252:255],v[88:91],a[112:115],a[252:255]
buffer_load_dwordx4 a[144:147],v6,s[28:31],s21 offen sc1
v_mfma_f32_16x16x32_bf16 a[252:255],v[84:87],a[104:107],a[252:255]
ds_read_b64_tr_b16 v[84:85],v2 offset:10464
ds_read_b64_tr_b16 v[86:87],v2 offset:15584
v_mfma_f32_16x16x32_bf16 a[252:255],v[80:83],a[96:99],a[252:255]
ds_read_b64_tr_b16 v[80:81],v2 offset:224
v_mfma_f32_16x16x32_bf16 a[0:3],v[96:99],a[68:71],a[0:3]
ds_read_b64_tr_b16 v[82:83],v2 offset:5344
v_mfma_f32_16x16x32_bf16 a[0:3],v[100:103],a[76:79],a[0:3]
ds_read_b64_tr_b16 v[88:89],v2 offset:20704
v_mfma_f32_16x16x32_bf16 a[0:3],v[104:107],a[84:87],a[0:3]
buffer_load_dwordx4 v[156:159],v28,s[28:31],s88 offen offset:3072 sc1
v_mfma_f32_16x16x32_bf16 a[0:3],v[108:111],a[92:95],a[0:3]
ds_read_b64_tr_b16 v[90:91],v2 offset:25824
v_mfma_f32_16x16x32_bf16 a[4:7],v[108:111],a[124:127],a[4:7]
v_mfma_f32_16x16x32_bf16 a[4:7],v[104:107],a[116:119],a[4:7]
v_mfma_f32_16x16x32_bf16 a[4:7],v[100:103],a[108:111],a[4:7]
v_mfma_f32_16x16x32_bf16 a[4:7],v[96:99],a[100:103],a[4:7]
buffer_load_dwordx4 a[160:163],v6,s[28:31],s22 offen sc1
v_mfma_f32_16x16x32_bf16 a[8:11],v[96:99],a[64:67],a[8:11]
v_mfma_f32_16x16x32_bf16 a[8:11],v[100:103],a[72:75],a[8:11]
v_mfma_f32_16x16x32_bf16 a[8:11],v[104:107],a[80:83],a[8:11]
v_mfma_f32_16x16x32_bf16 a[8:11],v[108:111],a[88:91],a[8:11]
v_mfma_f32_16x16x32_bf16 a[12:15],v[108:111],a[120:123],a[12:15]
buffer_load_dwordx4 v[140:143],v28,s[28:31],s87 offen offset:3072 sc1
v_mfma_f32_16x16x32_bf16 a[12:15],v[104:107],a[112:115],a[12:15]
s_add_u32 s87,s64,2
ds_read_b64_tr_b16 v[104:105],v1 offset:20736
ds_read_b64_tr_b16 v[106:107],v1 offset:25856
v_mfma_f32_16x16x32_bf16 a[12:15],v[100:103],a[104:107],a[12:15]
s_and_b32 s87,s87,3
s_lshl_b32 s87,s87,1
ds_read_b64_tr_b16 v[100:101],v1 offset:10496
v_mfma_f32_16x16x32_bf16 a[12:15],v[96:99],a[96:99],a[12:15]
s_or_b32 s87,s87,1
s_lshl_b32 s87,s87,16
ds_read_b64_tr_b16 v[96:97],v1 offset:256
v_mfma_f32_16x16x32_bf16 a[16:19],v[112:115],a[68:71],a[16:19]
s_add_u32 s89,s87,8192
s_add_u32 s90,s87,12288
ds_read_b64_tr_b16 v[98:99],v1 offset:5376
v_mfma_f32_16x16x32_bf16 a[16:19],v[116:119],a[76:79],a[16:19]
buffer_load_dwordx4 a[176:179],v6,s[28:31],s23 offen sc1
v_mfma_f32_16x16x32_bf16 a[16:19],v[120:123],a[84:87],a[16:19]
s_add_u32 s88,s87,4096
ds_read_b64_tr_b16 v[102:103],v1 offset:15616
ds_read_b64_tr_b16 v[108:109],v1 offset:30976
v_mfma_f32_16x16x32_bf16 a[16:19],v[124:127],a[92:95],a[16:19]
ds_read_b64_tr_b16 v[110:111],v1 offset:36096
v_add_u32 v1,0x1e000,v1
v_mfma_f32_16x16x32_bf16 a[20:23],v[124:127],a[124:127],a[20:23]
v_mfma_f32_16x16x32_bf16 a[20:23],v[120:123],a[116:119],a[20:23]
v_mfma_f32_16x16x32_bf16 a[20:23],v[116:119],a[108:111],a[20:23]
v_mfma_f32_16x16x32_bf16 a[20:23],v[112:115],a[100:103],a[20:23]
v_mfma_f32_16x16x32_bf16 a[24:27],v[112:115],a[64:67],a[24:27]
buffer_load_dwordx4 v24,s[48:51],s83 offen lds
v_mfma_f32_16x16x32_bf16 a[24:27],v[116:119],a[72:75],a[24:27]
v_mfma_f32_16x16x32_bf16 a[24:27],v[120:123],a[80:83],a[24:27]
v_mfma_f32_16x16x32_bf16 a[24:27],v[124:127],a[88:91],a[24:27]
v_mfma_f32_16x16x32_bf16 a[28:31],v[124:127],a[120:123],a[28:31]
ds_read_b64_tr_b16 v[124:125],v2 offset:31008
ds_read_b64_tr_b16 v[126:127],v2 offset:36128
v_mfma_f32_16x16x32_bf16 a[28:31],v[120:123],a[112:115],a[28:31]
buffer_load_dwordx4 a[180:183],v6,s[28:31],s23 offen offset:1024 sc1
v_mfma_f32_16x16x32_bf16 a[28:31],v[116:119],a[104:107],a[28:31]
)A"
R"A(ds_read_b64_tr_b16 v[116:117],v2 offset:10528
ds_read_b64_tr_b16 v[118:119],v2 offset:15648
v_mfma_f32_16x16x32_bf16 a[28:31],v[112:115],a[96:99],a[28:31]
s_waitcnt lgkmcnt(4)
ds_read_b64_tr_b16 v[112:113],v2 offset:288
v_mfma_f32_16x16x32_bf16 v[128:131],v[64:67],a[68:71],v[128:131]
ds_read_b64_tr_b16 v[114:115],v2 offset:5408
v_mfma_f32_16x16x32_bf16 v[128:131],v[68:71],a[76:79],v[128:131]
ds_read_b64_tr_b16 v[120:121],v2 offset:20768
v_mfma_f32_16x16x32_bf16 v[128:131],v[72:75],a[84:87],v[128:131]
ds_read_b64_tr_b16 v[122:123],v2 offset:25888
v_add_u32 v2,0x1e000,v2
v_mfma_f32_16x16x32_bf16 v[128:131],v[76:79],a[92:95],v[128:131]
v_mfma_f32_16x16x32_bf16 v[136:139],v[76:79],a[124:127],v[136:139]
buffer_load_dwordx4 v25,s[48:51],s83 offen offset:1024 lds
v_mfma_f32_16x16x32_bf16 v[136:139],v[72:75],a[116:119],v[136:139]
v_mfma_f32_16x16x32_bf16 v[136:139],v[68:71],a[108:111],v[136:139]
v_mfma_f32_16x16x32_bf16 v[136:139],v[64:67],a[100:103],v[136:139]
v_mfma_f32_16x16x32_bf16 v[144:147],v[64:67],a[64:67],v[144:147]
v_mfma_f32_16x16x32_bf16 v[144:147],v[68:71],a[72:75],v[144:147]
buffer_load_dwordx4 a[164:167],v6,s[28:31],s22 offen offset:1024 sc1
v_mfma_f32_16x16x32_bf16 v[144:147],v[72:75],a[80:83],v[144:147]
v_mfma_f32_16x16x32_bf16 v[144:147],v[76:79],a[88:91],v[144:147]
v_mfma_f32_16x16x32_bf16 v[152:155],v[76:79],a[120:123],v[152:155]
v_mfma_f32_16x16x32_bf16 v[152:155],v[72:75],a[112:115],v[152:155]
v_mfma_f32_16x16x32_bf16 v[152:155],v[68:71],a[104:107],v[152:155]
v_mfma_f32_16x16x32_bf16 v[152:155],v[64:67],a[96:99],v[152:155]
s_barrier
v_mfma_f32_16x16x32_bf16 v[160:163],v[80:83],a[68:71],v[160:163]
buffer_load_dwordx4 v26,s[48:51],s83 offen offset:2048 lds
v_mfma_f32_16x16x32_bf16 v[160:163],v[84:87],a[76:79],v[160:163]
ds_read_b64_tr_b16 v[64:65],v1
ds_read_b64_tr_b16 v[66:67],v1 offset:5120
v_mfma_f32_16x16x32_bf16 v[160:163],v[88:91],a[84:87],v[160:163]
ds_read_b64_tr_b16 v[68:69],v1 offset:10240
v_mfma_f32_16x16x32_bf16 v[160:163],v[92:95],a[92:95],v[160:163]
ds_read_b64_tr_b16 v[70:71],v1 offset:15360
v_mfma_f32_16x16x32_bf16 v[168:171],v[92:95],a[124:127],v[168:171]
ds_read_b64_tr_b16 v[72:73],v1 offset:20480
v_mfma_f32_16x16x32_bf16 v[168:171],v[88:91],a[116:119],v[168:171]
buffer_load_dwordx4 a[148:151],v6,s[28:31],s21 offen offset:1024 sc1
v_mfma_f32_16x16x32_bf16 v[168:171],v[84:87],a[108:111],v[168:171]
ds_read_b64_tr_b16 v[74:75],v1 offset:25600
ds_read_b64_tr_b16 v[76:77],v1 offset:30720
v_mfma_f32_16x16x32_bf16 v[168:171],v[80:83],a[100:103],v[168:171]
ds_read_b64_tr_b16 v[78:79],v1 offset:35840
v_mfma_f32_16x16x32_bf16 v[176:179],v[80:83],a[64:67],v[176:179]
v_mfma_f32_16x16x32_bf16 v[176:179],v[84:87],a[72:75],v[176:179]
v_mfma_f32_16x16x32_bf16 v[176:179],v[88:91],a[80:83],v[176:179]
v_mfma_f32_16x16x32_bf16 v[176:179],v[92:95],a[88:91],v[176:179]
v_mfma_f32_16x16x32_bf16 v[184:187],v[92:95],a[120:123],v[184:187]
buffer_load_dwordx4 v27,s[48:51],s83 offen offset:3072 lds
v_mfma_f32_16x16x32_bf16 v[184:187],v[88:91],a[112:115],v[184:187]
s_add_u32 m0,s86,45056
ds_read_b64_tr_b16 v[88:89],v2 offset:20512
ds_read_b64_tr_b16 v[90:91],v2 offset:25632
v_mfma_f32_16x16x32_bf16 v[184:187],v[84:87],a[104:107],v[184:187]
ds_read_b64_tr_b16 v[84:85],v2 offset:10272
v_mfma_f32_16x16x32_bf16 v[184:187],v[80:83],a[96:99],v[184:187]
ds_read_b64_tr_b16 v[80:81],v2 offset:32
v_mfma_f32_16x16x32_bf16 v[192:195],v[96:99],a[68:71],v[192:195]
ds_read_b64_tr_b16 v[82:83],v2 offset:5152
v_mfma_f32_16x16x32_bf16 v[192:195],v[100:103],a[76:79],v[192:195]
buffer_load_dwordx4 a[132:135],v6,s[28:31],s20 offen offset:1024 sc1
v_mfma_f32_16x16x32_bf16 v[192:195],v[104:107],a[84:87],v[192:195]
ds_read_b64_tr_b16 v[86:87],v2 offset:15392
ds_read_b64_tr_b16 v[92:93],v2 offset:30752
v_mfma_f32_16x16x32_bf16 v[192:195],v[108:111],a[92:95],v[192:195]
ds_read_b64_tr_b16 v[94:95],v2 offset:35872
v_mfma_f32_16x16x32_bf16 v[200:203],v[108:111],a[124:127],v[200:203]
v_mfma_f32_16x16x32_bf16 v[200:203],v[104:107],a[116:119],v[200:203]
v_mfma_f32_16x16x32_bf16 v[200:203],v[100:103],a[108:111],v[200:203]
v_mfma_f32_16x16x32_bf16 v[200:203],v[96:99],a[100:103],v[200:203]
v_mfma_f32_16x16x32_bf16 v[208:211],v[96:99],a[64:67],v[208:211]
buffer_load_dwordx4 v7,s[48:51],s83 offen lds
v_mfma_f32_16x16x32_bf16 v[208:211],v[100:103],a[72:75],v[208:211]
s_add_u32 m0,s86,46080
s_mul_i32 s83,s63,20480
v_mfma_f32_16x16x32_bf16 v[208:211],v[104:107],a[80:83],v[208:211]
v_mfma_f32_16x16x32_bf16 v[208:211],v[108:111],a[88:91],v[208:211]
v_mfma_f32_16x16x32_bf16 v[216:219],v[108:111],a[120:123],v[216:219]
ds_read_b64_tr_b16 v[108:109],v1 offset:30784
ds_read_b64_tr_b16 v[110:111],v1 offset:35904
v_mfma_f32_16x16x32_bf16 v[216:219],v[104:107],a[112:115],v[216:219]
buffer_load_dwordx4 a[136:139],v6,s[28:31],s20 offen offset:2048 sc1
v_mfma_f32_16x16x32_bf16 v[216:219],v[100:103],a[104:107],v[216:219]
ds_read_b64_tr_b16 v[100:101],v1 offset:10304
ds_read_b64_tr_b16 v[102:103],v1 offset:15424
v_mfma_f32_16x16x32_bf16 v[216:219],v[96:99],a[96:99],v[216:219]
s_waitcnt lgkmcnt(4)
ds_read_b64_tr_b16 v[96:97],v1 offset:64
v_mfma_f32_16x16x32_bf16 v[224:227],v[112:115],a[68:71],v[224:227]
ds_read_b64_tr_b16 v[98:99],v1 offset:5184
v_mfma_f32_16x16x32_bf16 v[224:227],v[116:119],a[76:79],v[224:227]
ds_read_b64_tr_b16 v[104:105],v1 offset:20544
v_mfma_f32_16x16x32_bf16 v[224:227],v[120:123],a[84:87],v[224:227]
ds_read_b64_tr_b16 v[106:107],v1 offset:25664
v_mfma_f32_16x16x32_bf16 v[224:227],v[124:127],a[92:95],v[224:227]
v_mfma_f32_16x16x32_bf16 v[232:235],v[124:127],a[124:127],v[232:235]
buffer_load_dwordx4 v24,s[48:51],s84 offen lds
v_mfma_f32_16x16x32_bf16 v[232:235],v[120:123],a[116:119],v[232:235]
v_mfma_f32_16x16x32_bf16 v[232:235],v[116:119],a[108:111],v[232:235]
v_mfma_f32_16x16x32_bf16 v[232:235],v[112:115],a[100:103],v[232:235]
v_mfma_f32_16x16x32_bf16 v[240:243],v[112:115],a[64:67],v[240:243]
v_mfma_f32_16x16x32_bf16 v[240:243],v[116:119],a[72:75],v[240:243]
buffer_load_dwordx4 a[152:155],v6,s[28:31],s21 offen offset:2048 sc1
v_mfma_f32_16x16x32_bf16 v[240:243],v[120:123],a[80:83],v[240:243]
v_mfma_f32_16x16x32_bf16 v[240:243],v[124:127],a[88:91],v[240:243]
v_mfma_f32_16x16x32_bf16 v[248:251],v[124:127],a[120:123],v[248:251]
ds_read_b64_tr_b16 v[124:125],v2 offset:30816
ds_read_b64_tr_b16 v[126:127],v2 offset:35936
v_mfma_f32_16x16x32_bf16 v[248:251],v[120:123],a[112:115],v[248:251]
ds_read_b64_tr_b16 v[120:121],v2 offset:20576
v_mfma_f32_16x16x32_bf16 v[248:251],v[116:119],a[104:107],v[248:251]
ds_read_b64_tr_b16 v[116:117],v2 offset:10336
v_mfma_f32_16x16x32_bf16 v[248:251],v[112:115],a[96:99],v[248:251]
s_waitcnt vmcnt(19)
ds_read_b64_tr_b16 v[112:113],v2 offset:96
v_mfma_f32_16x16x32_bf16 a[192:195],v[64:67],v[32:35],a[192:195]
buffer_load_dwordx4 v25,s[48:51],s84 offen offset:1024 lds
v_mfma_f32_16x16x32_bf16 a[192:195],v[68:71],v[36:39],a[192:195]
ds_read_b64_tr_b16 v[114:115],v2 offset:5216
ds_read_b64_tr_b16 v[118:119],v2 offset:15456
v_mfma_f32_16x16x32_bf16 a[192:195],v[72:75],v[40:43],a[192:195]
ds_read_b64_tr_b16 v[122:123],v2 offset:25696
v_mfma_f32_16x16x32_bf16 a[192:195],v[76:79],v[44:47],a[192:195]
v_mfma_f32_16x16x32_bf16 a[196:199],v[76:79],v[180:183],a[196:199]
v_mfma_f32_16x16x32_bf16 a[196:199],v[72:75],v[164:167],a[196:199]
buffer_load_dwordx4 a[168:171],v6,s[28:31],s22 offen offset:2048 sc1
v_mfma_f32_16x16x32_bf16 a[196:199],v[68:71],v[148:151],a[196:199]
s_waitcnt vmcnt(19)
v_mfma_f32_16x16x32_bf16 a[196:199],v[64:67],v[132:135],a[196:199]
v_mfma_f32_16x16x32_bf16 a[200:203],v[64:67],v[196:199],a[200:203]
v_mfma_f32_16x16x32_bf16 a[200:203],v[68:71],v[212:215],a[200:203]
v_mfma_f32_16x16x32_bf16 a[200:203],v[72:75],v[228:231],a[200:203]
v_mfma_f32_16x16x32_bf16 a[200:203],v[76:79],v[244:247],a[200:203]
v_mfma_f32_16x16x32_bf16 a[204:207],v[76:79],v[188:191],a[204:207]
buffer_load_dwordx4 v26,s[48:51],s84 offen offset:2048 lds
v_mfma_f32_16x16x32_bf16 a[204:207],v[72:75],v[172:175],a[204:207]
s_waitcnt vmcnt(18)
ds_read_b64_tr_b16 v[72:73],v1 offset:20608
ds_read_b64_tr_b16 v[74:75],v1 offset:25728
v_mfma_f32_16x16x32_bf16 a[204:207],v[68:71],v[156:159],a[204:207]
s_waitcnt vmcnt(16)
ds_read_b64_tr_b16 v[68:69],v1 offset:10368
v_mfma_f32_16x16x32_bf16 a[204:207],v[64:67],v[140:143],a[204:207]
ds_read_b64_tr_b16 v[64:65],v1 offset:128
v_mfma_f32_16x16x32_bf16 a[208:211],v[80:83],v[32:35],a[208:211]
ds_read_b64_tr_b16 v[66:67],v1 offset:5248
v_mfma_f32_16x16x32_bf16 a[208:211],v[84:87],v[36:39],a[208:211]
buffer_load_dwordx4 a[184:187],v6,s[28:31],s23 offen offset:2048 sc1
v_mfma_f32_16x16x32_bf16 a[208:211],v[88:91],v[40:43],a[208:211]
ds_read_b64_tr_b16 v[70:71],v1 offset:15488
ds_read_b64_tr_b16 v[76:77],v1 offset:30848
v_mfma_f32_16x16x32_bf16 a[208:211],v[92:95],v[44:47],a[208:211]
ds_read_b64_tr_b16 v[78:79],v1 offset:35968
v_mfma_f32_16x16x32_bf16 a[212:215],v[92:95],v[180:183],a[212:215]
v_mfma_f32_16x16x32_bf16 a[212:215],v[88:91],v[164:167],a[212:215]
v_mfma_f32_16x16x32_bf16 a[212:215],v[84:87],v[148:151],a[212:215]
v_mfma_f32_16x16x32_bf16 a[212:215],v[80:83],v[132:135],a[212:215]
v_mfma_f32_16x16x32_bf16 a[216:219],v[80:83],v[196:199],a[216:219]
buffer_load_dwordx4 v27,s[48:51],s84 offen offset:3072 lds
v_mfma_f32_16x16x32_bf16 a[216:219],v[84:87],v[212:215],a[216:219]
s_add_u32 m0,s86,50176
v_mfma_f32_16x16x32_bf16 a[216:219],v[88:91],v[228:231],a[216:219]
v_mfma_f32_16x16x32_bf16 a[216:219],v[92:95],v[244:247],a[216:219]
v_mfma_f32_16x16x32_bf16 a[220:223],v[92:95],v[188:191],a[220:223]
ds_read_b64_tr_b16 v[92:93],v2 offset:30880
ds_read_b64_tr_b16 v[94:95],v2 offset:36000
v_mfma_f32_16x16x32_bf16 a[220:223],v[88:91],v[172:175],a[220:223]
buffer_load_dwordx4 a[188:191],v28,s[28:31],s23 offen offset:3072 sc1
v_mfma_f32_16x16x32_bf16 a[220:223],v[84:87],v[156:159],a[220:223]
ds_read_b64_tr_b16 v[84:85],v2 offset:10400
ds_read_b64_tr_b16 v[86:87],v2 offset:15520
v_mfma_f32_16x16x32_bf16 a[220:223],v[80:83],v[140:143],a[220:223]
s_waitcnt lgkmcnt(4)
ds_read_b64_tr_b16 v[80:81],v2 offset:160
v_mfma_f32_16x16x32_bf16 a[224:227],v[96:99],v[32:35],a[224:227]
ds_read_b64_tr_b16 v[82:83],v2 offset:5280
v_mfma_f32_16x16x32_bf16 a[224:227],v[100:103],v[36:39],a[224:227]
ds_read_b64_tr_b16 v[88:89],v2 offset:20640
v_mfma_f32_16x16x32_bf16 a[224:227],v[104:107],v[40:43],a[224:227]
ds_read_b64_tr_b16 v[90:91],v2 offset:25760
v_mfma_f32_16x16x32_bf16 a[224:227],v[108:111],v[44:47],a[224:227]
v_mfma_f32_16x16x32_bf16 a[228:231],v[108:111],v[180:183],a[228:231]
buffer_load_dwordx4 v7,s[48:51],s84 offen lds
v_mfma_f32_16x16x32_bf16 a[228:231],v[104:107],v[164:167],a[228:231]
v_mfma_f32_16x16x32_bf16 a[228:231],v[100:103],v[148:151],a[228:231]
v_mfma_f32_16x16x32_bf16 a[228:231],v[96:99],v[132:135],a[228:231]
v_mfma_f32_16x16x32_bf16 a[232:235],v[96:99],v[196:199],a[232:235]
v_mfma_f32_16x16x32_bf16 a[232:235],v[100:103],v[212:215],a[232:235]
buffer_load_dwordx4 a[172:175],v28,s[28:31],s22 offen offset:3072 sc1
v_mfma_f32_16x16x32_bf16 a[232:235],v[104:107],v[228:231],a[232:235]
v_mfma_f32_16x16x32_bf16 a[232:235],v[108:111],v[244:247],a[232:235]
v_mfma_f32_16x16x32_bf16 a[236:239],v[108:111],v[188:191],a[236:239]
ds_read_b64_tr_b16 v[108:109],v1 offset:30912
ds_read_b64_tr_b16 v[110:111],v1 offset:36032
v_mfma_f32_16x16x32_bf16 a[236:239],v[104:107],v[172:175],a[236:239]
ds_read_b64_tr_b16 v[104:105],v1 offset:20672
v_mfma_f32_16x16x32_bf16 a[236:239],v[100:103],v[156:159],a[236:239]
buffer_load_dwordx4 a[84:87],v6,s[28:31],s89 offen sc1
v_mfma_f32_16x16x32_bf16 a[236:239],v[96:99],v[140:143],a[236:239]
ds_read_b64_tr_b16 v[96:97],v1 offset:192
ds_read_b64_tr_b16 v[98:99],v1 offset:5312
v_mfma_f32_16x16x32_bf16 a[240:243],v[112:115],v[32:35],a[240:243]
ds_read_b64_tr_b16 v[100:101],v1 offset:10432
v_mfma_f32_16x16x32_bf16 a[240:243],v[116:119],v[36:39],a[240:243]
ds_read_b64_tr_b16 v[102:103],v1 offset:15552
v_mfma_f32_16x16x32_bf16 a[240:243],v[120:123],v[40:43],a[240:243]
ds_read_b64_tr_b16 v[106:107],v1 offset:25792
v_add_u32 v1,0xffff6000,v1
v_mfma_f32_16x16x32_bf16 a[240:243],v[124:127],v[44:47],a[240:243]
)A"
R"A(buffer_load_dwordx4 a[92:95],v6,s[28:31],s90 offen sc1
v_mfma_f32_16x16x32_bf16 a[244:247],v[124:127],v[180:183],a[244:247]
v_mfma_f32_16x16x32_bf16 a[244:247],v[120:123],v[164:167],a[244:247]
v_mfma_f32_16x16x32_bf16 a[244:247],v[116:119],v[148:151],a[244:247]
v_mfma_f32_16x16x32_bf16 a[244:247],v[112:115],v[132:135],a[244:247]
v_mfma_f32_16x16x32_bf16 a[248:251],v[112:115],v[196:199],a[248:251]
buffer_load_dwordx4 a[156:159],v28,s[28:31],s21 offen offset:3072 sc1
v_mfma_f32_16x16x32_bf16 a[248:251],v[116:119],v[212:215],a[248:251]
v_mfma_f32_16x16x32_bf16 a[248:251],v[120:123],v[228:231],a[248:251]
v_mfma_f32_16x16x32_bf16 a[248:251],v[124:127],v[244:247],a[248:251]
v_mfma_f32_16x16x32_bf16 a[252:255],v[124:127],v[188:191],a[252:255]
ds_read_b64_tr_b16 v[124:125],v2 offset:30944
ds_read_b64_tr_b16 v[126:127],v2 offset:36064
v_mfma_f32_16x16x32_bf16 a[252:255],v[120:123],v[172:175],a[252:255]
buffer_load_dwordx4 a[68:71],v6,s[28:31],s87 offen sc1
v_mfma_f32_16x16x32_bf16 a[252:255],v[116:119],v[156:159],a[252:255]
ds_read_b64_tr_b16 v[116:117],v2 offset:10464
ds_read_b64_tr_b16 v[118:119],v2 offset:15584
v_mfma_f32_16x16x32_bf16 a[252:255],v[112:115],v[140:143],a[252:255]
ds_read_b64_tr_b16 v[112:113],v2 offset:224
v_mfma_f32_16x16x32_bf16 a[0:3],v[64:67],v[32:35],a[0:3]
ds_read_b64_tr_b16 v[114:115],v2 offset:5344
v_mfma_f32_16x16x32_bf16 a[0:3],v[68:71],v[36:39],a[0:3]
ds_read_b64_tr_b16 v[120:121],v2 offset:20704
v_mfma_f32_16x16x32_bf16 a[0:3],v[72:75],v[40:43],a[0:3]
buffer_load_dwordx4 a[140:143],v28,s[28:31],s20 offen offset:3072 sc1
v_mfma_f32_16x16x32_bf16 a[0:3],v[76:79],v[44:47],a[0:3]
ds_read_b64_tr_b16 v[122:123],v2 offset:25824
v_add_u32 v2,0xffff6000,v2
v_mfma_f32_16x16x32_bf16 a[4:7],v[76:79],v[180:183],a[4:7]
v_mfma_f32_16x16x32_bf16 a[4:7],v[72:75],v[164:167],a[4:7]
v_mfma_f32_16x16x32_bf16 a[4:7],v[68:71],v[148:151],a[4:7]
v_mfma_f32_16x16x32_bf16 a[4:7],v[64:67],v[132:135],a[4:7]
buffer_load_dwordx4 a[124:127],v6,s[28:31],s90 offen offset:1024 sc1
v_mfma_f32_16x16x32_bf16 a[8:11],v[64:67],v[196:199],a[8:11]
v_mfma_f32_16x16x32_bf16 a[8:11],v[68:71],v[212:215],a[8:11]
v_mfma_f32_16x16x32_bf16 a[8:11],v[72:75],v[228:231],a[8:11]
v_mfma_f32_16x16x32_bf16 a[8:11],v[76:79],v[244:247],a[8:11]
v_mfma_f32_16x16x32_bf16 a[12:15],v[76:79],v[188:191],a[12:15]
buffer_load_dwordx4 a[116:119],v6,s[28:31],s89 offen offset:1024 sc1
v_mfma_f32_16x16x32_bf16 a[12:15],v[72:75],v[172:175],a[12:15]
ds_read_b64_tr_b16 v[72:73],v1 offset:20480
ds_read_b64_tr_b16 v[74:75],v1 offset:25600
v_mfma_f32_16x16x32_bf16 a[12:15],v[68:71],v[156:159],a[12:15]
ds_read_b64_tr_b16 v[68:69],v1 offset:10240
v_mfma_f32_16x16x32_bf16 a[12:15],v[64:67],v[140:143],a[12:15]
s_waitcnt lgkmcnt(3)
ds_read_b64_tr_b16 v[64:65],v1
v_mfma_f32_16x16x32_bf16 a[16:19],v[80:83],v[32:35],a[16:19]
ds_read_b64_tr_b16 v[66:67],v1 offset:5120
v_mfma_f32_16x16x32_bf16 a[16:19],v[84:87],v[36:39],a[16:19]
buffer_load_dwordx4 a[76:79],v6,s[28:31],s88 offen sc1
v_mfma_f32_16x16x32_bf16 a[16:19],v[88:91],v[40:43],a[16:19]
ds_read_b64_tr_b16 v[70:71],v1 offset:15360
s_nop 0
L_pd22_%=:
s_cbranch_execz L_trtg2S_%=
ds_read_b64_tr_b16 v[76:77],v1 offset:30720
v_mfma_f32_16x16x32_bf16 a[16:19],v[92:95],v[44:47],a[16:19]
ds_read_b64_tr_b16 v[78:79],v1 offset:35840
v_add_u32 v1,0xffff6000,v1
v_mfma_f32_16x16x32_bf16 a[20:23],v[92:95],v[180:183],a[20:23]
v_mfma_f32_16x16x32_bf16 a[20:23],v[88:91],v[164:167],a[20:23]
v_mfma_f32_16x16x32_bf16 a[20:23],v[84:87],v[148:151],a[20:23]
buffer_load_dwordx4 a[108:111],v6,s[28:31],s88 offen offset:1024 sc1
v_mfma_f32_16x16x32_bf16 a[20:23],v[80:83],v[132:135],a[20:23]
v_mfma_f32_16x16x32_bf16 a[24:27],v[80:83],v[196:199],a[24:27]
v_mfma_f32_16x16x32_bf16 a[24:27],v[84:87],v[212:215],a[24:27]
v_mfma_f32_16x16x32_bf16 a[24:27],v[88:91],v[228:231],a[24:27]
v_mfma_f32_16x16x32_bf16 a[24:27],v[92:95],v[244:247],a[24:27]
buffer_load_dwordx4 a[100:103],v6,s[28:31],s87 offen offset:1024 sc1
v_mfma_f32_16x16x32_bf16 a[28:31],v[92:95],v[188:191],a[28:31]
ds_read_b64_tr_b16 v[92:93],v2 offset:30752
ds_read_b64_tr_b16 v[94:95],v2 offset:35872
v_mfma_f32_16x16x32_bf16 a[28:31],v[88:91],v[172:175],a[28:31]
ds_read_b64_tr_b16 v[88:89],v2 offset:20512
v_mfma_f32_16x16x32_bf16 a[28:31],v[84:87],v[156:159],a[28:31]
ds_read_b64_tr_b16 v[84:85],v2 offset:10272
v_mfma_f32_16x16x32_bf16 a[28:31],v[80:83],v[140:143],a[28:31]
ds_read_b64_tr_b16 v[80:81],v2 offset:32
v_mfma_f32_16x16x32_bf16 v[128:131],v[96:99],v[32:35],v[128:131]
buffer_load_dwordx4 a[64:67],v6,s[28:31],s87 offen offset:2048 sc1
v_mfma_f32_16x16x32_bf16 v[128:131],v[100:103],v[36:39],v[128:131]
ds_read_b64_tr_b16 v[82:83],v2 offset:5152
ds_read_b64_tr_b16 v[86:87],v2 offset:15392
v_mfma_f32_16x16x32_bf16 v[128:131],v[104:107],v[40:43],v[128:131]
ds_read_b64_tr_b16 v[90:91],v2 offset:25632
v_add_u32 v2,0xffff6000,v2
v_mfma_f32_16x16x32_bf16 v[128:131],v[108:111],v[44:47],v[128:131]
v_mfma_f32_16x16x32_bf16 v[136:139],v[108:111],v[180:183],v[136:139]
v_mfma_f32_16x16x32_bf16 v[136:139],v[104:107],v[164:167],v[136:139]
buffer_load_dwordx4 a[72:75],v6,s[28:31],s88 offen offset:2048 sc1
v_mfma_f32_16x16x32_bf16 v[136:139],v[100:103],v[148:151],v[136:139]
v_mfma_f32_16x16x32_bf16 v[136:139],v[96:99],v[132:135],v[136:139]
v_mfma_f32_16x16x32_bf16 v[144:147],v[96:99],v[196:199],v[144:147]
v_mfma_f32_16x16x32_bf16 v[144:147],v[100:103],v[212:215],v[144:147]
v_mfma_f32_16x16x32_bf16 v[144:147],v[104:107],v[228:231],v[144:147]
buffer_load_dwordx4 a[80:83],v6,s[28:31],s89 offen offset:2048 sc1
v_mfma_f32_16x16x32_bf16 v[144:147],v[108:111],v[244:247],v[144:147]
v_mfma_f32_16x16x32_bf16 v[152:155],v[108:111],v[188:191],v[152:155]
v_mfma_f32_16x16x32_bf16 v[152:155],v[104:107],v[172:175],v[152:155]
v_mfma_f32_16x16x32_bf16 v[152:155],v[100:103],v[156:159],v[152:155]
v_mfma_f32_16x16x32_bf16 v[152:155],v[96:99],v[140:143],v[152:155]
buffer_load_dwordx4 a[88:91],v6,s[28:31],s90 offen offset:2048 sc1
s_waitcnt vmcnt(15)
s_barrier
v_mfma_f32_16x16x32_bf16 v[160:163],v[112:115],v[32:35],v[160:163]
ds_read_b64_tr_b16 v[96:97],v1
ds_read_b64_tr_b16 v[98:99],v1 offset:5120
v_mfma_f32_16x16x32_bf16 v[160:163],v[116:119],v[36:39],v[160:163]
ds_read_b64_tr_b16 v[100:101],v1 offset:10240
v_mfma_f32_16x16x32_bf16 v[160:163],v[120:123],v[40:43],v[160:163]
ds_read_b64_tr_b16 v[102:103],v1 offset:15360
v_mfma_f32_16x16x32_bf16 v[160:163],v[124:127],v[44:47],v[160:163]
buffer_load_dwordx4 a[120:123],v28,s[28:31],s90 offen offset:3072 sc1
v_mfma_f32_16x16x32_bf16 v[168:171],v[124:127],v[180:183],v[168:171]
ds_read_b64_tr_b16 v[104:105],v1 offset:20480
ds_read_b64_tr_b16 v[106:107],v1 offset:25600
v_mfma_f32_16x16x32_bf16 v[168:171],v[120:123],v[164:167],v[168:171]
ds_read_b64_tr_b16 v[108:109],v1 offset:30720
v_mfma_f32_16x16x32_bf16 v[168:171],v[116:119],v[148:151],v[168:171]
ds_read_b64_tr_b16 v[110:111],v1 offset:35840
v_add_u32 v1,0xa000,v1
v_mfma_f32_16x16x32_bf16 v[168:171],v[112:115],v[132:135],v[168:171]
v_mfma_f32_16x16x32_bf16 v[176:179],v[112:115],v[196:199],v[176:179]
buffer_load_dwordx4 a[112:115],v28,s[28:31],s89 offen offset:3072 sc1
v_mfma_f32_16x16x32_bf16 v[176:179],v[116:119],v[212:215],v[176:179]
v_mfma_f32_16x16x32_bf16 v[176:179],v[120:123],v[228:231],v[176:179]
v_mfma_f32_16x16x32_bf16 v[176:179],v[124:127],v[244:247],v[176:179]
v_mfma_f32_16x16x32_bf16 v[184:187],v[124:127],v[188:191],v[184:187]
ds_read_b64_tr_b16 v[124:125],v2 offset:30752
ds_read_b64_tr_b16 v[126:127],v2 offset:35872
v_mfma_f32_16x16x32_bf16 v[184:187],v[120:123],v[172:175],v[184:187]
buffer_load_dwordx4 a[104:107],v28,s[28:31],s88 offen offset:3072 sc1
v_mfma_f32_16x16x32_bf16 v[184:187],v[116:119],v[156:159],v[184:187]
ds_read_b64_tr_b16 v[116:117],v2 offset:10272
ds_read_b64_tr_b16 v[118:119],v2 offset:15392
v_mfma_f32_16x16x32_bf16 v[184:187],v[112:115],v[140:143],v[184:187]
s_waitcnt lgkmcnt(4)
ds_read_b64_tr_b16 v[112:113],v2 offset:32
v_mfma_f32_16x16x32_bf16 a[192:195],v[64:67],a[128:131],a[192:195]
ds_read_b64_tr_b16 v[114:115],v2 offset:5152
v_mfma_f32_16x16x32_bf16 a[192:195],v[68:71],a[144:147],a[192:195]
ds_read_b64_tr_b16 v[120:121],v2 offset:20512
v_mfma_f32_16x16x32_bf16 a[192:195],v[72:75],a[160:163],a[192:195]
ds_read_b64_tr_b16 v[122:123],v2 offset:25632
v_add_u32 v2,0xa000,v2
v_mfma_f32_16x16x32_bf16 a[192:195],v[76:79],a[176:179],a[192:195]
v_mfma_f32_16x16x32_bf16 a[196:199],v[76:79],a[180:183],a[196:199]
v_mfma_f32_16x16x32_bf16 a[196:199],v[72:75],a[164:167],a[196:199]
buffer_load_dwordx4 a[96:99],v28,s[28:31],s87 offen offset:3072 sc1
v_mfma_f32_16x16x32_bf16 a[196:199],v[68:71],a[148:151],a[196:199]
s_mul_i32 s87,s60,81920
s_add_u32 s87,s87,s66
s_add_u32 s83,s83,s87
v_mfma_f32_16x16x32_bf16 a[196:199],v[64:67],a[132:135],a[196:199]
s_lshl_b32 s66,s63,6
s_add_u32 s87,s87,s66
s_add_u32 s88,s87,20480
v_mfma_f32_16x16x32_bf16 a[200:203],v[64:67],a[136:139],a[200:203]
s_add_u32 s89,s87,40960
s_add_u32 s90,s87,61440
s_add_u32 s66,s77,4096
v_mfma_f32_16x16x32_bf16 a[200:203],v[68:71],a[152:155],a[200:203]
v_mfma_f32_16x16x32_bf16 a[200:203],v[72:75],a[168:171],a[200:203]
v_mfma_f32_16x16x32_bf16 a[200:203],v[76:79],a[184:187],a[200:203]
v_mfma_f32_16x16x32_bf16 a[204:207],v[76:79],a[188:191],a[204:207]
s_waitcnt vmcnt(18)
ds_read_b64_tr_b16 v[76:77],v1 offset:30784
ds_read_b64_tr_b16 v[78:79],v1 offset:35904
v_mfma_f32_16x16x32_bf16 a[204:207],v[72:75],a[172:175],a[204:207]
s_waitcnt vmcnt(15)
ds_read_b64_tr_b16 v[72:73],v1 offset:20544
v_mfma_f32_16x16x32_bf16 a[204:207],v[68:71],a[156:159],a[204:207]
s_waitcnt vmcnt(13)
ds_read_b64_tr_b16 v[68:69],v1 offset:10304
v_mfma_f32_16x16x32_bf16 a[204:207],v[64:67],a[140:143],a[204:207]
ds_read_b64_tr_b16 v[64:65],v1 offset:64
v_mfma_f32_16x16x32_bf16 a[208:211],v[80:83],a[128:131],a[208:211]
ds_read_b64_tr_b16 v[66:67],v1 offset:5184
ds_read_b64_tr_b16 v[70:71],v1 offset:15424
v_mfma_f32_16x16x32_bf16 a[208:211],v[84:87],a[144:147],a[208:211]
ds_read_b64_tr_b16 v[74:75],v1 offset:25664
v_add_u32 v1,0xffff6000,v1
v_mfma_f32_16x16x32_bf16 a[208:211],v[88:91],a[160:163],a[208:211]
v_mfma_f32_16x16x32_bf16 a[208:211],v[92:95],a[176:179],a[208:211]
v_mfma_f32_16x16x32_bf16 a[212:215],v[92:95],a[180:183],a[212:215]
v_mfma_f32_16x16x32_bf16 a[212:215],v[88:91],a[164:167],a[212:215]
v_mfma_f32_16x16x32_bf16 a[212:215],v[84:87],a[148:151],a[212:215]
v_mfma_f32_16x16x32_bf16 a[212:215],v[80:83],a[132:135],a[212:215]
v_mfma_f32_16x16x32_bf16 a[216:219],v[80:83],a[136:139],a[216:219]
v_mfma_f32_16x16x32_bf16 a[216:219],v[84:87],a[152:155],a[216:219]
v_mfma_f32_16x16x32_bf16 a[216:219],v[88:91],a[168:171],a[216:219]
v_mfma_f32_16x16x32_bf16 a[216:219],v[92:95],a[184:187],a[216:219]
v_mfma_f32_16x16x32_bf16 a[220:223],v[92:95],a[188:191],a[220:223]
ds_read_b64_tr_b16 v[92:93],v2 offset:30816
ds_read_b64_tr_b16 v[94:95],v2 offset:35936
v_mfma_f32_16x16x32_bf16 a[220:223],v[88:91],a[172:175],a[220:223]
ds_read_b64_tr_b16 v[88:89],v2 offset:20576
v_mfma_f32_16x16x32_bf16 a[220:223],v[84:87],a[156:159],a[220:223]
ds_read_b64_tr_b16 v[84:85],v2 offset:10336
v_mfma_f32_16x16x32_bf16 a[220:223],v[80:83],a[140:143],a[220:223]
ds_read_b64_tr_b16 v[80:81],v2 offset:96
v_mfma_f32_16x16x32_bf16 a[192:195],v[96:99],a[68:71],a[192:195]
s_waitcnt vmcnt(10)
ds_read_b64_tr_b16 v[82:83],v2 offset:5216
ds_read_b64_tr_b16 v[86:87],v2 offset:15456
v_mfma_f32_16x16x32_bf16 a[192:195],v[100:103],a[76:79],a[192:195]
ds_read_b64_tr_b16 v[90:91],v2 offset:25696
v_add_u32 v2,0xffff6000,v2
v_mfma_f32_16x16x32_bf16 a[192:195],v[104:107],a[84:87],a[192:195]
v_mfma_f32_16x16x32_bf16 a[192:195],v[108:111],a[92:95],a[192:195]
v_mfma_f32_16x16x32_bf16 a[196:199],v[108:111],a[124:127],a[196:199]
v_mfma_f32_16x16x32_bf16 a[196:199],v[104:107],a[116:119],a[196:199]
s_waitcnt vmcnt(9)
v_mfma_f32_16x16x32_bf16 a[196:199],v[100:103],a[108:111],a[196:199]
s_waitcnt vmcnt(8)
v_mfma_f32_16x16x32_bf16 a[196:199],v[96:99],a[100:103],a[196:199]
s_waitcnt vmcnt(7)
v_mfma_f32_16x16x32_bf16 a[200:203],v[96:99],a[64:67],a[200:203]
s_waitcnt vmcnt(6)
v_mfma_f32_16x16x32_bf16 a[200:203],v[100:103],a[72:75],a[200:203]
)A"
R"A(s_waitcnt vmcnt(5)
v_mfma_f32_16x16x32_bf16 a[200:203],v[104:107],a[80:83],a[200:203]
s_waitcnt vmcnt(4)
v_mfma_f32_16x16x32_bf16 a[200:203],v[108:111],a[88:91],a[200:203]
s_waitcnt vmcnt(3)
v_accvgpr_read_b32 v8,a192
v_accvgpr_read_b32 v9,a193
v_mfma_f32_16x16x32_bf16 a[204:207],v[108:111],a[120:123],a[204:207]
s_waitcnt vmcnt(2)
v_accvgpr_read_b32 v10,a194
v_accvgpr_read_b32 v11,a195
v_mfma_f32_16x16x32_bf16 a[204:207],v[104:107],a[112:115],a[204:207]
s_waitcnt vmcnt(1)
v_cvt_pk_bf16_f32 v8,v8,v9
v_cvt_pk_bf16_f32 v9,v10,v11
v_mfma_f32_16x16x32_bf16 a[204:207],v[100:103],a[104:107],a[204:207]
s_waitcnt vmcnt(0)
v_accvgpr_read_b32 v16,a196
v_accvgpr_read_b32 v17,a197
v_mfma_f32_16x16x32_bf16 a[204:207],v[96:99],a[96:99],a[204:207]
s_waitcnt lgkmcnt(0)
v_accvgpr_read_b32 v18,a198
v_accvgpr_read_b32 v19,a199
v_mfma_f32_16x16x32_bf16 a[208:211],v[112:115],a[68:71],a[208:211]
v_cvt_pk_bf16_f32 v16,v16,v17
v_cvt_pk_bf16_f32 v17,v18,v19
v_mfma_f32_16x16x32_bf16 a[208:211],v[116:119],a[76:79],a[208:211]
ds_read_b64_tr_b16 v[96:97],v1 offset:64
ds_read_b64_tr_b16 v[98:99],v1 offset:5184
v_mfma_f32_16x16x32_bf16 a[208:211],v[120:123],a[84:87],a[208:211]
ds_read_b64_tr_b16 v[100:101],v1 offset:10304
v_mfma_f32_16x16x32_bf16 a[208:211],v[124:127],a[92:95],a[208:211]
ds_read_b64_tr_b16 v[102:103],v1 offset:15424
v_mfma_f32_16x16x32_bf16 a[212:215],v[124:127],a[124:127],a[212:215]
ds_read_b64_tr_b16 v[104:105],v1 offset:20544
v_mfma_f32_16x16x32_bf16 a[212:215],v[120:123],a[116:119],a[212:215]
ds_read_b64_tr_b16 v[106:107],v1 offset:25664
ds_read_b64_tr_b16 v[108:109],v1 offset:30784
v_mfma_f32_16x16x32_bf16 a[212:215],v[116:119],a[108:111],a[212:215]
ds_read_b64_tr_b16 v[110:111],v1 offset:35904
v_add_u32 v1,0xa000,v1
v_mfma_f32_16x16x32_bf16 a[212:215],v[112:115],a[100:103],a[212:215]
v_accvgpr_read_b32 v12,a208
v_accvgpr_read_b32 v13,a209
v_mfma_f32_16x16x32_bf16 a[216:219],v[112:115],a[64:67],a[216:219]
v_accvgpr_read_b32 v14,a210
v_accvgpr_read_b32 v15,a211
v_mfma_f32_16x16x32_bf16 a[216:219],v[116:119],a[72:75],a[216:219]
v_cvt_pk_bf16_f32 v10,v12,v13
v_cvt_pk_bf16_f32 v11,v14,v15
v_mfma_f32_16x16x32_bf16 a[216:219],v[120:123],a[80:83],a[216:219]
buffer_store_dwordx4 v[8:11],v6,s[36:39],s74 offen sc1
v_mfma_f32_16x16x32_bf16 a[216:219],v[124:127],a[88:91],a[216:219]
v_accvgpr_read_b32 v20,a212
v_accvgpr_read_b32 v21,a213
v_mfma_f32_16x16x32_bf16 a[220:223],v[124:127],a[120:123],a[220:223]
v_accvgpr_read_b32 v22,a214
v_accvgpr_read_b32 v23,a215
v_mfma_f32_16x16x32_bf16 a[220:223],v[120:123],a[112:115],a[220:223]
v_cvt_pk_bf16_f32 v18,v20,v21
v_cvt_pk_bf16_f32 v19,v22,v23
v_mfma_f32_16x16x32_bf16 a[220:223],v[116:119],a[104:107],a[220:223]
v_accvgpr_read_b32 v8,a200
v_accvgpr_read_b32 v9,a201
v_mfma_f32_16x16x32_bf16 a[220:223],v[112:115],a[96:99],a[220:223]
v_accvgpr_read_b32 v10,a202
v_accvgpr_read_b32 v11,a203
v_mfma_f32_16x16x32_bf16 a[224:227],v[64:67],a[128:131],a[224:227]
v_accvgpr_read_b32 v12,a216
v_accvgpr_read_b32 v13,a217
v_mfma_f32_16x16x32_bf16 a[224:227],v[68:71],a[144:147],a[224:227]
v_accvgpr_read_b32 v14,a218
v_accvgpr_read_b32 v15,a219
v_mfma_f32_16x16x32_bf16 a[224:227],v[72:75],a[160:163],a[224:227]
v_cvt_pk_bf16_f32 v8,v8,v9
v_cvt_pk_bf16_f32 v9,v10,v11
v_mfma_f32_16x16x32_bf16 a[224:227],v[76:79],a[176:179],a[224:227]
v_cvt_pk_bf16_f32 v10,v12,v13
v_cvt_pk_bf16_f32 v11,v14,v15
v_mfma_f32_16x16x32_bf16 a[228:231],v[76:79],a[180:183],a[228:231]
ds_read_b64_tr_b16 v[112:113],v2 offset:96
ds_read_b64_tr_b16 v[114:115],v2 offset:5216
v_mfma_f32_16x16x32_bf16 a[228:231],v[72:75],a[164:167],a[228:231]
buffer_store_dwordx4 v[16:19],v6,s[36:39],s74 offen offset:1024 sc1
v_mfma_f32_16x16x32_bf16 a[228:231],v[68:71],a[148:151],a[228:231]
ds_read_b64_tr_b16 v[116:117],v2 offset:10336
ds_read_b64_tr_b16 v[118:119],v2 offset:15456
v_mfma_f32_16x16x32_bf16 a[228:231],v[64:67],a[132:135],a[228:231]
ds_read_b64_tr_b16 v[120:121],v2 offset:20576
v_accvgpr_read_b32 v16,a204
v_mfma_f32_16x16x32_bf16 a[232:235],v[64:67],a[136:139],a[232:235]
ds_read_b64_tr_b16 v[122:123],v2 offset:25696
v_accvgpr_read_b32 v17,a205
v_mfma_f32_16x16x32_bf16 a[232:235],v[68:71],a[152:155],a[232:235]
ds_read_b64_tr_b16 v[124:125],v2 offset:30816
v_accvgpr_read_b32 v18,a206
v_mfma_f32_16x16x32_bf16 a[232:235],v[72:75],a[168:171],a[232:235]
ds_read_b64_tr_b16 v[126:127],v2 offset:35936
v_accvgpr_read_b32 v19,a207
v_mfma_f32_16x16x32_bf16 a[232:235],v[76:79],a[184:187],a[232:235]
v_accvgpr_read_b32 v20,a220
v_accvgpr_read_b32 v21,a221
v_mfma_f32_16x16x32_bf16 a[236:239],v[76:79],a[188:191],a[236:239]
v_accvgpr_read_b32 v22,a222
v_accvgpr_read_b32 v23,a223
v_mfma_f32_16x16x32_bf16 a[236:239],v[72:75],a[172:175],a[236:239]
v_cvt_pk_bf16_f32 v16,v16,v17
v_cvt_pk_bf16_f32 v17,v18,v19
v_mfma_f32_16x16x32_bf16 a[236:239],v[68:71],a[156:159],a[236:239]
v_cvt_pk_bf16_f32 v18,v20,v21
v_cvt_pk_bf16_f32 v19,v22,v23
v_mfma_f32_16x16x32_bf16 a[236:239],v[64:67],a[140:143],a[236:239]
ds_read_b64_tr_b16 v[64:65],v1 offset:128
ds_read_b64_tr_b16 v[66:67],v1 offset:5248
v_mfma_f32_16x16x32_bf16 a[240:243],v[80:83],a[128:131],a[240:243]
buffer_store_dwordx4 v[8:11],v6,s[36:39],s74 offen offset:2048 sc1
v_mfma_f32_16x16x32_bf16 a[240:243],v[84:87],a[144:147],a[240:243]
ds_read_b64_tr_b16 v[68:69],v1 offset:10368
ds_read_b64_tr_b16 v[70:71],v1 offset:15488
v_mfma_f32_16x16x32_bf16 a[240:243],v[88:91],a[160:163],a[240:243]
ds_read_b64_tr_b16 v[72:73],v1 offset:20608
v_add_u32 v2,0xa000,v2
v_mfma_f32_16x16x32_bf16 a[240:243],v[92:95],a[176:179],a[240:243]
ds_read_b64_tr_b16 v[74:75],v1 offset:25728
v_mfma_f32_16x16x32_bf16 a[244:247],v[92:95],a[180:183],a[244:247]
ds_read_b64_tr_b16 v[76:77],v1 offset:30848
v_mfma_f32_16x16x32_bf16 a[244:247],v[88:91],a[164:167],a[244:247]
ds_read_b64_tr_b16 v[78:79],v1 offset:35968
v_add_u32 v1,0xffff6000,v1
v_mfma_f32_16x16x32_bf16 a[244:247],v[84:87],a[148:151],a[244:247]
v_mfma_f32_16x16x32_bf16 a[244:247],v[80:83],a[132:135],a[244:247]
v_mfma_f32_16x16x32_bf16 a[248:251],v[80:83],a[136:139],a[248:251]
v_mfma_f32_16x16x32_bf16 a[248:251],v[84:87],a[152:155],a[248:251]
v_mfma_f32_16x16x32_bf16 a[248:251],v[88:91],a[168:171],a[248:251]
v_mfma_f32_16x16x32_bf16 a[248:251],v[92:95],a[184:187],a[248:251]
buffer_store_dwordx4 v[16:19],v28,s[36:39],s74 offen offset:3072 sc1
v_mfma_f32_16x16x32_bf16 a[252:255],v[92:95],a[188:191],a[252:255]
ds_read_b64_tr_b16 v[92:93],v2 offset:30880
ds_read_b64_tr_b16 v[94:95],v2 offset:36000
v_mfma_f32_16x16x32_bf16 a[252:255],v[88:91],a[172:175],a[252:255]
ds_read_b64_tr_b16 v[88:89],v2 offset:20640
v_mfma_f32_16x16x32_bf16 a[252:255],v[84:87],a[156:159],a[252:255]
ds_read_b64_tr_b16 v[84:85],v2 offset:10400
v_mfma_f32_16x16x32_bf16 a[252:255],v[80:83],a[140:143],a[252:255]
s_waitcnt lgkmcnt(4)
ds_read_b64_tr_b16 v[80:81],v2 offset:160
v_mfma_f32_16x16x32_bf16 a[224:227],v[96:99],a[68:71],a[224:227]
ds_read_b64_tr_b16 v[82:83],v2 offset:5280
ds_read_b64_tr_b16 v[86:87],v2 offset:15520
v_mfma_f32_16x16x32_bf16 a[224:227],v[100:103],a[76:79],a[224:227]
ds_read_b64_tr_b16 v[90:91],v2 offset:25760
v_add_u32 v2,0xffff6000,v2
v_mfma_f32_16x16x32_bf16 a[224:227],v[104:107],a[84:87],a[224:227]
v_mfma_f32_16x16x32_bf16 a[224:227],v[108:111],a[92:95],a[224:227]
v_mfma_f32_16x16x32_bf16 a[228:231],v[108:111],a[124:127],a[228:231]
v_mfma_f32_16x16x32_bf16 a[228:231],v[104:107],a[116:119],a[228:231]
v_mfma_f32_16x16x32_bf16 a[228:231],v[100:103],a[108:111],a[228:231]
v_mfma_f32_16x16x32_bf16 a[228:231],v[96:99],a[100:103],a[228:231]
v_mfma_f32_16x16x32_bf16 a[232:235],v[96:99],a[64:67],a[232:235]
v_mfma_f32_16x16x32_bf16 a[232:235],v[100:103],a[72:75],a[232:235]
v_mfma_f32_16x16x32_bf16 a[232:235],v[104:107],a[80:83],a[232:235]
v_mfma_f32_16x16x32_bf16 a[232:235],v[108:111],a[88:91],a[232:235]
v_accvgpr_read_b32 v8,a224
v_accvgpr_read_b32 v9,a225
v_mfma_f32_16x16x32_bf16 a[236:239],v[108:111],a[120:123],a[236:239]
v_accvgpr_read_b32 v10,a226
v_accvgpr_read_b32 v11,a227
v_mfma_f32_16x16x32_bf16 a[236:239],v[104:107],a[112:115],a[236:239]
v_cvt_pk_bf16_f32 v8,v8,v9
v_cvt_pk_bf16_f32 v9,v10,v11
v_mfma_f32_16x16x32_bf16 a[236:239],v[100:103],a[104:107],a[236:239]
v_accvgpr_read_b32 v16,a228
v_accvgpr_read_b32 v17,a229
v_mfma_f32_16x16x32_bf16 a[236:239],v[96:99],a[96:99],a[236:239]
v_accvgpr_read_b32 v18,a230
v_accvgpr_read_b32 v19,a231
v_mfma_f32_16x16x32_bf16 a[240:243],v[112:115],a[68:71],a[240:243]
v_cvt_pk_bf16_f32 v16,v16,v17
v_cvt_pk_bf16_f32 v17,v18,v19
v_mfma_f32_16x16x32_bf16 a[240:243],v[116:119],a[76:79],a[240:243]
ds_read_b64_tr_b16 v[96:97],v1 offset:128
ds_read_b64_tr_b16 v[98:99],v1 offset:5248
v_mfma_f32_16x16x32_bf16 a[240:243],v[120:123],a[84:87],a[240:243]
ds_read_b64_tr_b16 v[100:101],v1 offset:10368
v_mfma_f32_16x16x32_bf16 a[240:243],v[124:127],a[92:95],a[240:243]
ds_read_b64_tr_b16 v[102:103],v1 offset:15488
v_mfma_f32_16x16x32_bf16 a[244:247],v[124:127],a[124:127],a[244:247]
ds_read_b64_tr_b16 v[104:105],v1 offset:20608
v_mfma_f32_16x16x32_bf16 a[244:247],v[120:123],a[116:119],a[244:247]
ds_read_b64_tr_b16 v[106:107],v1 offset:25728
ds_read_b64_tr_b16 v[108:109],v1 offset:30848
v_mfma_f32_16x16x32_bf16 a[244:247],v[116:119],a[108:111],a[244:247]
ds_read_b64_tr_b16 v[110:111],v1 offset:35968
v_add_u32 v1,0xa000,v1
v_mfma_f32_16x16x32_bf16 a[244:247],v[112:115],a[100:103],a[244:247]
v_accvgpr_read_b32 v12,a240
v_accvgpr_read_b32 v13,a241
v_mfma_f32_16x16x32_bf16 a[248:251],v[112:115],a[64:67],a[248:251]
v_accvgpr_read_b32 v14,a242
v_accvgpr_read_b32 v15,a243
v_mfma_f32_16x16x32_bf16 a[248:251],v[116:119],a[72:75],a[248:251]
v_cvt_pk_bf16_f32 v10,v12,v13
v_cvt_pk_bf16_f32 v11,v14,v15
v_mfma_f32_16x16x32_bf16 a[248:251],v[120:123],a[80:83],a[248:251]
buffer_store_dwordx4 v[8:11],v6,s[36:39],s75 offen sc1
v_mfma_f32_16x16x32_bf16 a[248:251],v[124:127],a[88:91],a[248:251]
v_accvgpr_read_b32 v20,a244
v_accvgpr_read_b32 v21,a245
v_mfma_f32_16x16x32_bf16 a[252:255],v[124:127],a[120:123],a[252:255]
v_accvgpr_read_b32 v22,a246
v_accvgpr_read_b32 v23,a247
v_mfma_f32_16x16x32_bf16 a[252:255],v[120:123],a[112:115],a[252:255]
v_cvt_pk_bf16_f32 v18,v20,v21
v_cvt_pk_bf16_f32 v19,v22,v23
v_mfma_f32_16x16x32_bf16 a[252:255],v[116:119],a[104:107],a[252:255]
v_accvgpr_read_b32 v8,a232
v_accvgpr_read_b32 v9,a233
v_mfma_f32_16x16x32_bf16 a[252:255],v[112:115],a[96:99],a[252:255]
v_accvgpr_read_b32 v10,a234
v_accvgpr_read_b32 v11,a235
v_mfma_f32_16x16x32_bf16 a[0:3],v[64:67],a[128:131],a[0:3]
v_accvgpr_read_b32 v12,a248
v_accvgpr_read_b32 v13,a249
v_mfma_f32_16x16x32_bf16 a[0:3],v[68:71],a[144:147],a[0:3]
v_accvgpr_read_b32 v14,a250
v_accvgpr_read_b32 v15,a251
v_mfma_f32_16x16x32_bf16 a[0:3],v[72:75],a[160:163],a[0:3]
v_cvt_pk_bf16_f32 v8,v8,v9
v_cvt_pk_bf16_f32 v9,v10,v11
v_mfma_f32_16x16x32_bf16 a[0:3],v[76:79],a[176:179],a[0:3]
v_cvt_pk_bf16_f32 v10,v12,v13
v_cvt_pk_bf16_f32 v11,v14,v15
v_mfma_f32_16x16x32_bf16 a[4:7],v[76:79],a[180:183],a[4:7]
ds_read_b64_tr_b16 v[112:113],v2 offset:160
ds_read_b64_tr_b16 v[114:115],v2 offset:5280
v_mfma_f32_16x16x32_bf16 a[4:7],v[72:75],a[164:167],a[4:7]
buffer_store_dwordx4 v[16:19],v6,s[36:39],s75 offen offset:1024 sc1
v_mfma_f32_16x16x32_bf16 a[4:7],v[68:71],a[148:151],a[4:7]
ds_read_b64_tr_b16 v[116:117],v2 offset:10400
ds_read_b64_tr_b16 v[118:119],v2 offset:15520
v_mfma_f32_16x16x32_bf16 a[4:7],v[64:67],a[132:135],a[4:7]
ds_read_b64_tr_b16 v[120:121],v2 offset:20640
v_accvgpr_read_b32 v16,a236
v_mfma_f32_16x16x32_bf16 a[8:11],v[64:67],a[136:139],a[8:11]
ds_read_b64_tr_b16 v[122:123],v2 offset:25760
v_accvgpr_read_b32 v17,a237
v_mfma_f32_16x16x32_bf16 a[8:11],v[68:71],a[152:155],a[8:11]
ds_read_b64_tr_b16 v[124:125],v2 offset:30880
v_accvgpr_read_b32 v18,a238
v_mfma_f32_16x16x32_bf16 a[8:11],v[72:75],a[168:171],a[8:11]
ds_read_b64_tr_b16 v[126:127],v2 offset:36000
v_accvgpr_read_b32 v19,a239
v_mfma_f32_16x16x32_bf16 a[8:11],v[76:79],a[184:187],a[8:11]
v_accvgpr_read_b32 v20,a252
v_accvgpr_read_b32 v21,a253
v_mfma_f32_16x16x32_bf16 a[12:15],v[76:79],a[188:191],a[12:15]
v_accvgpr_read_b32 v22,a254
v_accvgpr_read_b32 v23,a255
v_mfma_f32_16x16x32_bf16 a[12:15],v[72:75],a[172:175],a[12:15]
)A"
R"A(v_cvt_pk_bf16_f32 v16,v16,v17
v_cvt_pk_bf16_f32 v17,v18,v19
v_mfma_f32_16x16x32_bf16 a[12:15],v[68:71],a[156:159],a[12:15]
v_cvt_pk_bf16_f32 v18,v20,v21
v_cvt_pk_bf16_f32 v19,v22,v23
v_mfma_f32_16x16x32_bf16 a[12:15],v[64:67],a[140:143],a[12:15]
s_waitcnt lgkmcnt(3)
ds_read_b64_tr_b16 v[64:65],v1 offset:192
ds_read_b64_tr_b16 v[66:67],v1 offset:5312
v_mfma_f32_16x16x32_bf16 a[16:19],v[80:83],a[128:131],a[16:19]
buffer_store_dwordx4 v[8:11],v6,s[36:39],s75 offen offset:2048 sc1
v_mfma_f32_16x16x32_bf16 a[16:19],v[84:87],a[144:147],a[16:19]
ds_read_b64_tr_b16 v[68:69],v1 offset:10432
ds_read_b64_tr_b16 v[70:71],v1 offset:15552
v_mfma_f32_16x16x32_bf16 a[16:19],v[88:91],a[160:163],a[16:19]
ds_read_b64_tr_b16 v[72:73],v1 offset:20672
v_add_u32 v2,0xa000,v2
v_mfma_f32_16x16x32_bf16 a[16:19],v[92:95],a[176:179],a[16:19]
ds_read_b64_tr_b16 v[74:75],v1 offset:25792
v_mfma_f32_16x16x32_bf16 a[20:23],v[92:95],a[180:183],a[20:23]
ds_read_b64_tr_b16 v[76:77],v1 offset:30912
v_mfma_f32_16x16x32_bf16 a[20:23],v[88:91],a[164:167],a[20:23]
ds_read_b64_tr_b16 v[78:79],v1 offset:36032
v_add_u32 v1,0xffff6000,v1
v_mfma_f32_16x16x32_bf16 a[20:23],v[84:87],a[148:151],a[20:23]
v_mfma_f32_16x16x32_bf16 a[20:23],v[80:83],a[132:135],a[20:23]
v_mfma_f32_16x16x32_bf16 a[24:27],v[80:83],a[136:139],a[24:27]
v_mfma_f32_16x16x32_bf16 a[24:27],v[84:87],a[152:155],a[24:27]
v_mfma_f32_16x16x32_bf16 a[24:27],v[88:91],a[168:171],a[24:27]
v_mfma_f32_16x16x32_bf16 a[24:27],v[92:95],a[184:187],a[24:27]
buffer_store_dwordx4 v[16:19],v28,s[36:39],s75 offen offset:3072 sc1
v_mfma_f32_16x16x32_bf16 a[28:31],v[92:95],a[188:191],a[28:31]
ds_read_b64_tr_b16 v[92:93],v2 offset:30944
ds_read_b64_tr_b16 v[94:95],v2 offset:36064
v_mfma_f32_16x16x32_bf16 a[28:31],v[88:91],a[172:175],a[28:31]
ds_read_b64_tr_b16 v[88:89],v2 offset:20704
v_mfma_f32_16x16x32_bf16 a[28:31],v[84:87],a[156:159],a[28:31]
ds_read_b64_tr_b16 v[84:85],v2 offset:10464
v_mfma_f32_16x16x32_bf16 a[28:31],v[80:83],a[140:143],a[28:31]
ds_read_b64_tr_b16 v[80:81],v2 offset:224
v_mfma_f32_16x16x32_bf16 a[0:3],v[96:99],a[68:71],a[0:3]
ds_read_b64_tr_b16 v[82:83],v2 offset:5344
ds_read_b64_tr_b16 v[86:87],v2 offset:15584
v_mfma_f32_16x16x32_bf16 a[0:3],v[100:103],a[76:79],a[0:3]
ds_read_b64_tr_b16 v[90:91],v2 offset:25824
v_add_u32 v2,0xffff6000,v2
v_mfma_f32_16x16x32_bf16 a[0:3],v[104:107],a[84:87],a[0:3]
v_mfma_f32_16x16x32_bf16 a[0:3],v[108:111],a[92:95],a[0:3]
v_mfma_f32_16x16x32_bf16 a[4:7],v[108:111],a[124:127],a[4:7]
v_mfma_f32_16x16x32_bf16 a[4:7],v[104:107],a[116:119],a[4:7]
v_mfma_f32_16x16x32_bf16 a[4:7],v[100:103],a[108:111],a[4:7]
v_mfma_f32_16x16x32_bf16 a[4:7],v[96:99],a[100:103],a[4:7]
v_mfma_f32_16x16x32_bf16 a[8:11],v[96:99],a[64:67],a[8:11]
v_mfma_f32_16x16x32_bf16 a[8:11],v[100:103],a[72:75],a[8:11]
v_mfma_f32_16x16x32_bf16 a[8:11],v[104:107],a[80:83],a[8:11]
v_mfma_f32_16x16x32_bf16 a[8:11],v[108:111],a[88:91],a[8:11]
v_accvgpr_read_b32 v8,a0
v_accvgpr_read_b32 v9,a1
v_mfma_f32_16x16x32_bf16 a[12:15],v[108:111],a[120:123],a[12:15]
v_accvgpr_read_b32 v10,a2
v_accvgpr_read_b32 v11,a3
v_mfma_f32_16x16x32_bf16 a[12:15],v[104:107],a[112:115],a[12:15]
v_cvt_pk_bf16_f32 v8,v8,v9
v_cvt_pk_bf16_f32 v9,v10,v11
v_mfma_f32_16x16x32_bf16 a[12:15],v[100:103],a[104:107],a[12:15]
v_accvgpr_read_b32 v16,a4
v_accvgpr_read_b32 v17,a5
v_mfma_f32_16x16x32_bf16 a[12:15],v[96:99],a[96:99],a[12:15]
v_accvgpr_read_b32 v18,a6
v_accvgpr_read_b32 v19,a7
v_mfma_f32_16x16x32_bf16 a[16:19],v[112:115],a[68:71],a[16:19]
v_cvt_pk_bf16_f32 v16,v16,v17
v_cvt_pk_bf16_f32 v17,v18,v19
v_mfma_f32_16x16x32_bf16 a[16:19],v[116:119],a[76:79],a[16:19]
s_waitcnt lgkmcnt(0)
ds_read_b64_tr_b16 v[96:97],v1 offset:192
ds_read_b64_tr_b16 v[98:99],v1 offset:5312
v_mfma_f32_16x16x32_bf16 a[16:19],v[120:123],a[84:87],a[16:19]
ds_read_b64_tr_b16 v[100:101],v1 offset:10432
v_mfma_f32_16x16x32_bf16 a[16:19],v[124:127],a[92:95],a[16:19]
ds_read_b64_tr_b16 v[102:103],v1 offset:15552
v_mfma_f32_16x16x32_bf16 a[20:23],v[124:127],a[124:127],a[20:23]
ds_read_b64_tr_b16 v[104:105],v1 offset:20672
v_mfma_f32_16x16x32_bf16 a[20:23],v[120:123],a[116:119],a[20:23]
ds_read_b64_tr_b16 v[106:107],v1 offset:25792
ds_read_b64_tr_b16 v[108:109],v1 offset:30912
v_mfma_f32_16x16x32_bf16 a[20:23],v[116:119],a[108:111],a[20:23]
ds_read_b64_tr_b16 v[110:111],v1 offset:36032
v_add_u32 v1,0x14000,v1
v_mfma_f32_16x16x32_bf16 a[20:23],v[112:115],a[100:103],a[20:23]
v_accvgpr_read_b32 v12,a16
v_accvgpr_read_b32 v13,a17
v_mfma_f32_16x16x32_bf16 a[24:27],v[112:115],a[64:67],a[24:27]
v_accvgpr_read_b32 v14,a18
v_accvgpr_read_b32 v15,a19
v_mfma_f32_16x16x32_bf16 a[24:27],v[116:119],a[72:75],a[24:27]
v_cvt_pk_bf16_f32 v10,v12,v13
v_cvt_pk_bf16_f32 v11,v14,v15
v_mfma_f32_16x16x32_bf16 a[24:27],v[120:123],a[80:83],a[24:27]
buffer_store_dwordx4 v[8:11],v6,s[36:39],s76 offen sc1
L_pd23_%=:
s_cbranch_execz L_trtg2S_%=
v_mfma_f32_16x16x32_bf16 a[24:27],v[124:127],a[88:91],a[24:27]
v_accvgpr_read_b32 v20,a20
v_accvgpr_read_b32 v21,a21
v_mfma_f32_16x16x32_bf16 a[28:31],v[124:127],a[120:123],a[28:31]
v_accvgpr_read_b32 v22,a22
v_accvgpr_read_b32 v23,a23
v_mfma_f32_16x16x32_bf16 a[28:31],v[120:123],a[112:115],a[28:31]
v_cvt_pk_bf16_f32 v18,v20,v21
v_cvt_pk_bf16_f32 v19,v22,v23
v_mfma_f32_16x16x32_bf16 a[28:31],v[116:119],a[104:107],a[28:31]
v_accvgpr_read_b32 v8,a8
v_accvgpr_read_b32 v9,a9
v_mfma_f32_16x16x32_bf16 a[28:31],v[112:115],a[96:99],a[28:31]
v_accvgpr_read_b32 v10,a10
v_accvgpr_read_b32 v11,a11
v_mfma_f32_16x16x32_bf16 v[128:131],v[64:67],a[128:131],v[128:131]
v_accvgpr_read_b32 v12,a24
v_accvgpr_read_b32 v13,a25
v_mfma_f32_16x16x32_bf16 v[128:131],v[68:71],a[144:147],v[128:131]
v_accvgpr_read_b32 v14,a26
v_accvgpr_read_b32 v15,a27
v_mfma_f32_16x16x32_bf16 v[128:131],v[72:75],a[160:163],v[128:131]
v_cvt_pk_bf16_f32 v8,v8,v9
v_cvt_pk_bf16_f32 v9,v10,v11
v_mfma_f32_16x16x32_bf16 v[128:131],v[76:79],a[176:179],v[128:131]
v_cvt_pk_bf16_f32 v10,v12,v13
v_cvt_pk_bf16_f32 v11,v14,v15
v_mfma_f32_16x16x32_bf16 v[136:139],v[76:79],a[180:183],v[136:139]
ds_read_b64_tr_b16 v[112:113],v2 offset:224
ds_read_b64_tr_b16 v[114:115],v2 offset:5344
v_mfma_f32_16x16x32_bf16 v[136:139],v[72:75],a[164:167],v[136:139]
buffer_store_dwordx4 v[16:19],v6,s[36:39],s76 offen offset:1024 sc1
v_mfma_f32_16x16x32_bf16 v[136:139],v[68:71],a[148:151],v[136:139]
ds_read_b64_tr_b16 v[116:117],v2 offset:10464
ds_read_b64_tr_b16 v[118:119],v2 offset:15584
v_mfma_f32_16x16x32_bf16 v[136:139],v[64:67],a[132:135],v[136:139]
ds_read_b64_tr_b16 v[120:121],v2 offset:20704
v_accvgpr_read_b32 v16,a12
v_mfma_f32_16x16x32_bf16 v[144:147],v[64:67],a[136:139],v[144:147]
ds_read_b64_tr_b16 v[122:123],v2 offset:25824
v_accvgpr_read_b32 v17,a13
v_mfma_f32_16x16x32_bf16 v[144:147],v[68:71],a[152:155],v[144:147]
ds_read_b64_tr_b16 v[124:125],v2 offset:30944
v_accvgpr_read_b32 v18,a14
v_mfma_f32_16x16x32_bf16 v[144:147],v[72:75],a[168:171],v[144:147]
ds_read_b64_tr_b16 v[126:127],v2 offset:36064
v_accvgpr_read_b32 v19,a15
v_mfma_f32_16x16x32_bf16 v[144:147],v[76:79],a[184:187],v[144:147]
v_accvgpr_read_b32 v20,a28
v_accvgpr_read_b32 v21,a29
v_mfma_f32_16x16x32_bf16 v[152:155],v[76:79],a[188:191],v[152:155]
v_accvgpr_read_b32 v22,a30
v_accvgpr_read_b32 v23,a31
v_mfma_f32_16x16x32_bf16 v[152:155],v[72:75],a[172:175],v[152:155]
v_cvt_pk_bf16_f32 v16,v16,v17
v_cvt_pk_bf16_f32 v17,v18,v19
v_mfma_f32_16x16x32_bf16 v[152:155],v[68:71],a[156:159],v[152:155]
v_cvt_pk_bf16_f32 v18,v20,v21
v_cvt_pk_bf16_f32 v19,v22,v23
v_mfma_f32_16x16x32_bf16 v[152:155],v[64:67],a[140:143],v[152:155]
ds_read_b64_tr_b16 v[64:65],v1 offset:256
ds_read_b64_tr_b16 v[66:67],v1 offset:5376
v_mfma_f32_16x16x32_bf16 v[160:163],v[80:83],a[128:131],v[160:163]
buffer_store_dwordx4 v[8:11],v6,s[36:39],s76 offen offset:2048 sc1
v_mfma_f32_16x16x32_bf16 v[160:163],v[84:87],a[144:147],v[160:163]
ds_read_b64_tr_b16 v[68:69],v1 offset:10496
ds_read_b64_tr_b16 v[70:71],v1 offset:15616
v_mfma_f32_16x16x32_bf16 v[160:163],v[88:91],a[160:163],v[160:163]
ds_read_b64_tr_b16 v[72:73],v1 offset:20736
v_add_u32 v2,0x14000,v2
v_mfma_f32_16x16x32_bf16 v[160:163],v[92:95],a[176:179],v[160:163]
ds_read_b64_tr_b16 v[74:75],v1 offset:25856
v_mov_b32 v20,s52
v_mfma_f32_16x16x32_bf16 v[168:171],v[92:95],a[180:183],v[168:171]
ds_read_b64_tr_b16 v[76:77],v1 offset:30976
v_mov_b32 v21,s53
v_mfma_f32_16x16x32_bf16 v[168:171],v[88:91],a[164:167],v[168:171]
ds_read_b64_tr_b16 v[78:79],v1 offset:36096
v_add_u32 v1,0xffff6000,v1
v_mfma_f32_16x16x32_bf16 v[168:171],v[84:87],a[148:151],v[168:171]
v_lshlrev_b32 v22,20,v6
v_mfma_f32_16x16x32_bf16 v[168:171],v[80:83],a[132:135],v[168:171]
v_mfma_f32_16x16x32_bf16 v[176:179],v[80:83],a[136:139],v[176:179]
v_mfma_f32_16x16x32_bf16 v[176:179],v[84:87],a[152:155],v[176:179]
v_mfma_f32_16x16x32_bf16 v[176:179],v[88:91],a[168:171],v[176:179]
v_mfma_f32_16x16x32_bf16 v[176:179],v[92:95],a[184:187],v[176:179]
buffer_store_dwordx4 v[16:19],v28,s[36:39],s76 offen offset:3072 sc1
v_mfma_f32_16x16x32_bf16 v[184:187],v[92:95],a[188:191],v[184:187]
ds_read_b64_tr_b16 v[92:93],v2 offset:31008
ds_read_b64_tr_b16 v[94:95],v2 offset:36128
v_mfma_f32_16x16x32_bf16 v[184:187],v[88:91],a[172:175],v[184:187]
ds_read_b64_tr_b16 v[88:89],v2 offset:20768
v_mfma_f32_16x16x32_bf16 v[184:187],v[84:87],a[156:159],v[184:187]
ds_read_b64_tr_b16 v[84:85],v2 offset:10528
v_mfma_f32_16x16x32_bf16 v[184:187],v[80:83],a[140:143],v[184:187]
s_waitcnt lgkmcnt(4)
ds_read_b64_tr_b16 v[80:81],v2 offset:288
v_mfma_f32_16x16x32_bf16 v[128:131],v[96:99],a[68:71],v[128:131]
ds_read_b64_tr_b16 v[82:83],v2 offset:5408
ds_read_b64_tr_b16 v[86:87],v2 offset:15648
v_mfma_f32_16x16x32_bf16 v[128:131],v[100:103],a[76:79],v[128:131]
ds_read_b64_tr_b16 v[90:91],v2 offset:25888
v_add_u32 v2,0xffff6000,v2
v_mfma_f32_16x16x32_bf16 v[128:131],v[104:107],a[84:87],v[128:131]
v_mfma_f32_16x16x32_bf16 v[128:131],v[108:111],a[92:95],v[128:131]
v_mfma_f32_16x16x32_bf16 v[136:139],v[108:111],a[124:127],v[136:139]
v_mfma_f32_16x16x32_bf16 v[136:139],v[104:107],a[116:119],v[136:139]
v_mfma_f32_16x16x32_bf16 v[136:139],v[100:103],a[108:111],v[136:139]
v_mfma_f32_16x16x32_bf16 v[136:139],v[96:99],a[100:103],v[136:139]
v_mfma_f32_16x16x32_bf16 v[144:147],v[96:99],a[64:67],v[144:147]
v_mfma_f32_16x16x32_bf16 v[144:147],v[100:103],a[72:75],v[144:147]
v_mfma_f32_16x16x32_bf16 v[144:147],v[104:107],a[80:83],v[144:147]
v_mfma_f32_16x16x32_bf16 v[144:147],v[108:111],a[88:91],v[144:147]
v_cvt_pk_bf16_f32 v128,v128,v129
v_cvt_pk_bf16_f32 v129,v130,v131
v_mfma_f32_16x16x32_bf16 v[152:155],v[108:111],a[120:123],v[152:155]
ds_read_b64_tr_b16 v[108:109],v1 offset:30976
ds_read_b64_tr_b16 v[110:111],v1 offset:36096
v_mfma_f32_16x16x32_bf16 v[152:155],v[104:107],a[112:115],v[152:155]
v_cvt_pk_bf16_f32 v136,v136,v137
v_cvt_pk_bf16_f32 v137,v138,v139
v_mfma_f32_16x16x32_bf16 v[152:155],v[100:103],a[104:107],v[152:155]
v_cvt_pk_bf16_f32 v144,v144,v145
v_cvt_pk_bf16_f32 v145,v146,v147
v_mfma_f32_16x16x32_bf16 v[152:155],v[96:99],a[96:99],v[152:155]
ds_read_b64_tr_b16 v[96:97],v1 offset:256
ds_read_b64_tr_b16 v[98:99],v1 offset:5376
v_mfma_f32_16x16x32_bf16 v[160:163],v[112:115],a[68:71],v[160:163]
ds_read_b64_tr_b16 v[100:101],v1 offset:10496
v_mfma_f32_16x16x32_bf16 v[160:163],v[116:119],a[76:79],v[160:163]
ds_read_b64_tr_b16 v[102:103],v1 offset:15616
v_mfma_f32_16x16x32_bf16 v[160:163],v[120:123],a[84:87],v[160:163]
ds_read_b64_tr_b16 v[104:105],v1 offset:20736
v_mfma_f32_16x16x32_bf16 v[160:163],v[124:127],a[92:95],v[160:163]
v_cvt_pk_bf16_f32 v152,v152,v153
v_cvt_pk_bf16_f32 v153,v154,v155
v_mfma_f32_16x16x32_bf16 v[168:171],v[124:127],a[124:127],v[168:171]
ds_read_b64_tr_b16 v[106:107],v1 offset:25856
v_add_u32 v1,0xffff6000,v1
v_mfma_f32_16x16x32_bf16 v[168:171],v[120:123],a[116:119],v[168:171]
v_mfma_f32_16x16x32_bf16 v[168:171],v[116:119],a[108:111],v[168:171]
v_mfma_f32_16x16x32_bf16 v[168:171],v[112:115],a[100:103],v[168:171]
v_cvt_pk_bf16_f32 v130,v160,v161
v_cvt_pk_bf16_f32 v131,v162,v163
v_mfma_f32_16x16x32_bf16 v[176:179],v[112:115],a[64:67],v[176:179]
buffer_store_dwordx4 v[128:131],v6,s[36:39],s79 offen sc1
)A"
R"A(v_mfma_f32_16x16x32_bf16 v[176:179],v[116:119],a[72:75],v[176:179]
v_mfma_f32_16x16x32_bf16 v[176:179],v[120:123],a[80:83],v[176:179]
v_mfma_f32_16x16x32_bf16 v[176:179],v[124:127],a[88:91],v[176:179]
v_mfma_f32_16x16x32_bf16 v[184:187],v[124:127],a[120:123],v[184:187]
v_cvt_pk_bf16_f32 v138,v168,v169
v_cvt_pk_bf16_f32 v139,v170,v171
v_mfma_f32_16x16x32_bf16 v[184:187],v[120:123],a[112:115],v[184:187]
buffer_store_dwordx4 v[136:139],v6,s[36:39],s80 offen sc1
v_mfma_f32_16x16x32_bf16 v[184:187],v[116:119],a[104:107],v[184:187]
ds_read_b64_tr_b16 v[116:117],v2 offset:10528
ds_read_b64_tr_b16 v[118:119],v2 offset:15648
v_mfma_f32_16x16x32_bf16 v[184:187],v[112:115],a[96:99],v[184:187]
v_cvt_pk_bf16_f32 v146,v176,v177
v_cvt_pk_bf16_f32 v147,v178,v179
s_waitcnt vmcnt(6)
s_barrier
v_mfma_f32_16x16x32_bf16 v[192:195],v[64:67],v[32:35],v[192:195]
buffer_store_dwordx4 v[144:147],v6,s[36:39],s81 offen sc1
v_mfma_f32_16x16x32_bf16 v[192:195],v[68:71],v[36:39],v[192:195]
ds_read_b64_tr_b16 v[112:113],v2 offset:288
ds_read_b64_tr_b16 v[114:115],v2 offset:5408
v_mfma_f32_16x16x32_bf16 v[192:195],v[72:75],v[40:43],v[192:195]
buffer_store_dwordx2 v[58:59],v60,s[44:47],s18 offen offset:2312 sc1
v_mfma_f32_16x16x32_bf16 v[192:195],v[76:79],v[44:47],v[192:195]
v_cvt_pk_bf16_f32 v154,v184,v185
v_cvt_pk_bf16_f32 v155,v186,v187
v_mfma_f32_16x16x32_bf16 v[200:203],v[76:79],v[180:183],v[200:203]
buffer_store_dwordx4 v[152:155],v28,s[36:39],s82 offen sc1
v_mfma_f32_16x16x32_bf16 v[200:203],v[72:75],v[164:167],v[200:203]
ds_read_b64_tr_b16 v[120:121],v2 offset:20768
ds_read_b64_tr_b16 v[122:123],v2 offset:25888
v_mfma_f32_16x16x32_bf16 v[200:203],v[68:71],v[148:151],v[200:203]
ds_read_b64_tr_b16 v[124:125],v2 offset:31008
v_mfma_f32_16x16x32_bf16 v[200:203],v[64:67],v[132:135],v[200:203]
ds_read_b64_tr_b16 v[126:127],v2 offset:36128
v_add_u32 v2,0xffff6000,v2
v_mfma_f32_16x16x32_bf16 v[208:211],v[64:67],v[196:199],v[208:211]
v_mfma_f32_16x16x32_bf16 v[208:211],v[68:71],v[212:215],v[208:211]
v_mfma_f32_16x16x32_bf16 v[208:211],v[72:75],v[228:231],v[208:211]
v_mfma_f32_16x16x32_bf16 v[208:211],v[76:79],v[244:247],v[208:211]
v_mfma_f32_16x16x32_bf16 v[216:219],v[76:79],v[188:191],v[216:219]
ds_read_b64_tr_b16 v[76:77],v1 offset:30976
ds_read_b64_tr_b16 v[78:79],v1 offset:36096
v_mfma_f32_16x16x32_bf16 v[216:219],v[72:75],v[172:175],v[216:219]
ds_read_b64_tr_b16 v[72:73],v1 offset:20736
v_mfma_f32_16x16x32_bf16 v[216:219],v[68:71],v[156:159],v[216:219]
ds_read_b64_tr_b16 v[68:69],v1 offset:10496
v_mfma_f32_16x16x32_bf16 v[216:219],v[64:67],v[140:143],v[216:219]
s_waitcnt lgkmcnt(4)
ds_read_b64_tr_b16 v[64:65],v1 offset:256
v_mfma_f32_16x16x32_bf16 v[224:227],v[80:83],v[32:35],v[224:227]
ds_read_b64_tr_b16 v[66:67],v1 offset:5376
ds_read_b64_tr_b16 v[70:71],v1 offset:15616
v_mfma_f32_16x16x32_bf16 v[224:227],v[84:87],v[36:39],v[224:227]
ds_read_b64_tr_b16 v[74:75],v1 offset:25856
v_mfma_f32_16x16x32_bf16 v[224:227],v[88:91],v[40:43],v[224:227]
v_mfma_f32_16x16x32_bf16 v[224:227],v[92:95],v[44:47],v[224:227]
v_mfma_f32_16x16x32_bf16 v[232:235],v[92:95],v[180:183],v[232:235]
v_mfma_f32_16x16x32_bf16 v[232:235],v[88:91],v[164:167],v[232:235]
v_mfma_f32_16x16x32_bf16 v[232:235],v[84:87],v[148:151],v[232:235]
v_mfma_f32_16x16x32_bf16 v[232:235],v[80:83],v[132:135],v[232:235]
v_mfma_f32_16x16x32_bf16 v[240:243],v[80:83],v[196:199],v[240:243]
v_mfma_f32_16x16x32_bf16 v[240:243],v[84:87],v[212:215],v[240:243]
v_mfma_f32_16x16x32_bf16 v[240:243],v[88:91],v[228:231],v[240:243]
v_mfma_f32_16x16x32_bf16 v[240:243],v[92:95],v[244:247],v[240:243]
v_mfma_f32_16x16x32_bf16 v[248:251],v[92:95],v[188:191],v[248:251]
ds_read_b64_tr_b16 v[92:93],v2 offset:31008
ds_read_b64_tr_b16 v[94:95],v2 offset:36128
v_mfma_f32_16x16x32_bf16 v[248:251],v[88:91],v[172:175],v[248:251]
ds_read_b64_tr_b16 v[88:89],v2 offset:20768
v_mfma_f32_16x16x32_bf16 v[248:251],v[84:87],v[156:159],v[248:251]
ds_read_b64_tr_b16 v[84:85],v2 offset:10528
v_mfma_f32_16x16x32_bf16 v[248:251],v[80:83],v[140:143],v[248:251]
ds_read_b64_tr_b16 v[80:81],v2 offset:288
v_mfma_f32_16x16x32_bf16 v[192:195],v[96:99],a[128:131],v[192:195]
ds_read_b64_tr_b16 v[82:83],v2 offset:5408
ds_read_b64_tr_b16 v[86:87],v2 offset:15648
v_mfma_f32_16x16x32_bf16 v[192:195],v[100:103],a[144:147],v[192:195]
ds_read_b64_tr_b16 v[90:91],v2 offset:25888
v_mfma_f32_16x16x32_bf16 v[192:195],v[104:107],a[160:163],v[192:195]
v_mfma_f32_16x16x32_bf16 v[192:195],v[108:111],a[176:179],v[192:195]
v_mfma_f32_16x16x32_bf16 v[200:203],v[108:111],a[180:183],v[200:203]
v_mfma_f32_16x16x32_bf16 v[200:203],v[104:107],a[164:167],v[200:203]
v_mfma_f32_16x16x32_bf16 v[200:203],v[100:103],a[148:151],v[200:203]
v_mfma_f32_16x16x32_bf16 v[200:203],v[96:99],a[132:135],v[200:203]
v_mfma_f32_16x16x32_bf16 v[208:211],v[96:99],a[136:139],v[208:211]
v_mfma_f32_16x16x32_bf16 v[208:211],v[100:103],a[152:155],v[208:211]
v_mfma_f32_16x16x32_bf16 v[208:211],v[104:107],a[168:171],v[208:211]
v_mfma_f32_16x16x32_bf16 v[208:211],v[108:111],a[184:187],v[208:211]
v_mfma_f32_16x16x32_bf16 v[216:219],v[108:111],a[188:191],v[216:219]
v_mfma_f32_16x16x32_bf16 v[216:219],v[104:107],a[172:175],v[216:219]
v_mfma_f32_16x16x32_bf16 v[216:219],v[100:103],a[156:159],v[216:219]
v_mfma_f32_16x16x32_bf16 v[216:219],v[96:99],a[140:143],v[216:219]
v_mfma_f32_16x16x32_bf16 v[224:227],v[112:115],a[128:131],v[224:227]
v_mfma_f32_16x16x32_bf16 v[224:227],v[116:119],a[144:147],v[224:227]
v_mfma_f32_16x16x32_bf16 v[224:227],v[120:123],a[160:163],v[224:227]
v_mfma_f32_16x16x32_bf16 v[224:227],v[124:127],a[176:179],v[224:227]
v_mfma_f32_16x16x32_bf16 v[232:235],v[124:127],a[180:183],v[232:235]
v_mfma_f32_16x16x32_bf16 v[232:235],v[120:123],a[164:167],v[232:235]
v_mfma_f32_16x16x32_bf16 v[232:235],v[116:119],a[148:151],v[232:235]
v_mfma_f32_16x16x32_bf16 v[232:235],v[112:115],a[132:135],v[232:235]
v_mfma_f32_16x16x32_bf16 v[240:243],v[112:115],a[136:139],v[240:243]
v_mfma_f32_16x16x32_bf16 v[240:243],v[116:119],a[152:155],v[240:243]
v_mfma_f32_16x16x32_bf16 v[240:243],v[120:123],a[168:171],v[240:243]
v_mfma_f32_16x16x32_bf16 v[240:243],v[124:127],a[184:187],v[240:243]
v_mfma_f32_16x16x32_bf16 v[248:251],v[124:127],a[188:191],v[248:251]
v_mfma_f32_16x16x32_bf16 v[248:251],v[120:123],a[172:175],v[248:251]
v_mfma_f32_16x16x32_bf16 v[248:251],v[116:119],a[156:159],v[248:251]
v_mfma_f32_16x16x32_bf16 v[248:251],v[112:115],a[140:143],v[248:251]
s_waitcnt lgkmcnt(0)
v_mfma_f32_16x16x32_bf16 v[192:195],v[64:67],a[68:71],v[192:195]
v_mfma_f32_16x16x32_bf16 v[192:195],v[68:71],a[76:79],v[192:195]
v_mfma_f32_16x16x32_bf16 v[192:195],v[72:75],a[84:87],v[192:195]
v_mfma_f32_16x16x32_bf16 v[192:195],v[76:79],a[92:95],v[192:195]
v_mfma_f32_16x16x32_bf16 v[200:203],v[76:79],a[124:127],v[200:203]
v_mfma_f32_16x16x32_bf16 v[200:203],v[72:75],a[116:119],v[200:203]
v_mfma_f32_16x16x32_bf16 v[200:203],v[68:71],a[108:111],v[200:203]
v_mfma_f32_16x16x32_bf16 v[200:203],v[64:67],a[100:103],v[200:203]
v_mfma_f32_16x16x32_bf16 v[208:211],v[64:67],a[64:67],v[208:211]
v_mfma_f32_16x16x32_bf16 v[208:211],v[68:71],a[72:75],v[208:211]
v_mfma_f32_16x16x32_bf16 v[208:211],v[72:75],a[80:83],v[208:211]
v_mfma_f32_16x16x32_bf16 v[208:211],v[76:79],a[88:91],v[208:211]
v_mfma_f32_16x16x32_bf16 v[216:219],v[76:79],a[120:123],v[216:219]
v_mfma_f32_16x16x32_bf16 v[216:219],v[72:75],a[112:115],v[216:219]
v_mfma_f32_16x16x32_bf16 v[216:219],v[68:71],a[104:107],v[216:219]
v_mfma_f32_16x16x32_bf16 v[216:219],v[64:67],a[96:99],v[216:219]
v_mfma_f32_16x16x32_bf16 v[224:227],v[80:83],a[68:71],v[224:227]
buffer_load_dwordx2 v[62:63],v61,s[44:47],s91 offen offset:2312 sc1
v_mfma_f32_16x16x32_bf16 v[224:227],v[84:87],a[76:79],v[224:227]
v_mfma_f32_16x16x32_bf16 v[224:227],v[88:91],a[84:87],v[224:227]
v_mfma_f32_16x16x32_bf16 v[224:227],v[92:95],a[92:95],v[224:227]
v_mfma_f32_16x16x32_bf16 v[232:235],v[92:95],a[124:127],v[232:235]
v_mfma_f32_16x16x32_bf16 v[232:235],v[88:91],a[116:119],v[232:235]
v_mfma_f32_16x16x32_bf16 v[232:235],v[84:87],a[108:111],v[232:235]
v_mfma_f32_16x16x32_bf16 v[232:235],v[80:83],a[100:103],v[232:235]
v_mfma_f32_16x16x32_bf16 v[240:243],v[80:83],a[64:67],v[240:243]
v_mfma_f32_16x16x32_bf16 v[240:243],v[84:87],a[72:75],v[240:243]
v_mfma_f32_16x16x32_bf16 v[240:243],v[88:91],a[80:83],v[240:243]
v_mfma_f32_16x16x32_bf16 v[240:243],v[92:95],a[88:91],v[240:243]
v_mfma_f32_16x16x32_bf16 v[248:251],v[92:95],a[120:123],v[248:251]
v_mfma_f32_16x16x32_bf16 v[248:251],v[88:91],a[112:115],v[248:251]
v_mfma_f32_16x16x32_bf16 v[248:251],v[84:87],a[104:107],v[248:251]
v_mfma_f32_16x16x32_bf16 v[248:251],v[80:83],a[96:99],v[248:251]
s_waitcnt vmcnt(1)
s_barrier
buffer_store_dwordx2 v[20:21],v22,s[44:47],s18 offen offset:2304 sc1
s_waitcnt vmcnt(1)
v_cmp_ne_u32 s[56:57],s52,v62
v_cmp_ne_u32 s[58:59],s53,v63
s_nop 0
s_or_b64 s[56:57],s[56:57],s[58:59]
s_cbranch_scc1 L_fr8_%=
L_fo8_%=:
buffer_load_dwordx4 v[64:67],v6,s[36:39],s66 offen sc1
buffer_load_dwordx4 v[68:71],v6,s[36:39],s67 offen sc1
buffer_load_dwordx4 v[72:75],v6,s[36:39],s66 offen offset:1024 sc1
buffer_load_dwordx4 v[76:79],v6,s[36:39],s67 offen offset:1024 sc1
buffer_load_dwordx4 v[80:83],v6,s[36:39],s66 offen offset:2048 sc1
buffer_load_dwordx4 v[84:87],v6,s[36:39],s67 offen offset:2048 sc1
buffer_load_dwordx4 v[88:91],v28,s[36:39],s66 offen offset:3072 sc1
buffer_load_dwordx4 v[92:95],v28,s[36:39],s67 offen offset:3072 sc1
buffer_load_dwordx2 v[4:5],v3,s[44:47],s91 offen offset:2304 sc1
s_waitcnt vmcnt(8)
v_mfma_f32_16x16x32_bf16 v[192:195],v[48:51],v[64:67],v[192:195]
s_waitcnt vmcnt(7)
v_mfma_f32_16x16x32_bf16 v[192:195],v[48:51],v[68:71],v[192:195]
v_mfma_f32_16x16x32_bf16 v[224:227],v[52:55],v[64:67],v[224:227]
v_mfma_f32_16x16x32_bf16 v[224:227],v[52:55],v[68:71],v[224:227]
s_waitcnt vmcnt(6)
v_mfma_f32_16x16x32_bf16 v[200:203],v[48:51],v[72:75],v[200:203]
s_waitcnt vmcnt(5)
v_mfma_f32_16x16x32_bf16 v[200:203],v[48:51],v[76:79],v[200:203]
v_mfma_f32_16x16x32_bf16 v[232:235],v[52:55],v[72:75],v[232:235]
v_mfma_f32_16x16x32_bf16 v[232:235],v[52:55],v[76:79],v[232:235]
s_waitcnt vmcnt(4)
v_mfma_f32_16x16x32_bf16 v[208:211],v[48:51],v[80:83],v[208:211]
s_waitcnt vmcnt(3)
v_mfma_f32_16x16x32_bf16 v[208:211],v[48:51],v[84:87],v[208:211]
v_mfma_f32_16x16x32_bf16 v[240:243],v[52:55],v[80:83],v[240:243]
v_mfma_f32_16x16x32_bf16 v[240:243],v[52:55],v[84:87],v[240:243]
s_waitcnt vmcnt(2)
v_mfma_f32_16x16x32_bf16 v[216:219],v[48:51],v[88:91],v[216:219]
s_waitcnt vmcnt(1)
v_mfma_f32_16x16x32_bf16 v[216:219],v[48:51],v[92:95],v[216:219]
v_mfma_f32_16x16x32_bf16 v[248:251],v[52:55],v[88:91],v[248:251]
v_mfma_f32_16x16x32_bf16 v[248:251],v[52:55],v[92:95],v[248:251]
s_waitcnt vmcnt(0)
v_cmp_ne_u32 s[56:57],s52,v4
v_cmp_ne_u32 s[58:59],s53,v5
s_nop 0
s_or_b64 s[56:57],s[56:57],s[58:59]
s_cbranch_scc1 L_fr9_%=
L_fo9_%=:
buffer_load_dwordx4 v[96:99],v6,s[36:39],s68 offen sc1
buffer_load_dwordx4 v[100:103],v6,s[36:39],s68 offen offset:1024 sc1
buffer_load_dwordx4 v[104:107],v6,s[36:39],s68 offen offset:2048 sc1
buffer_load_dwordx4 v[108:111],v6,s[36:39],s68 offen offset:3072 sc1
buffer_load_dwordx4 v[112:115],v6,s[36:39],s65 offen sc1
buffer_load_dwordx4 v[116:119],v6,s[36:39],s65 offen offset:1024 sc1
buffer_load_dwordx4 v[120:123],v6,s[36:39],s65 offen offset:2048 sc1
buffer_load_dwordx4 v[124:127],v28,s[36:39],s65 offen offset:3072 sc1
s_waitcnt vmcnt(7)
v_mfma_f32_16x16x32_bf16 v[128:131],v[48:51],v[96:99],0
s_waitcnt vmcnt(6)
v_mfma_f32_16x16x32_bf16 v[128:131],v[48:51],v[100:103],v[128:131]
s_waitcnt vmcnt(5)
v_mfma_f32_16x16x32_bf16 v[128:131],v[48:51],v[104:107],v[128:131]
s_waitcnt vmcnt(4)
v_mfma_f32_16x16x32_bf16 v[128:131],v[48:51],v[108:111],v[128:131]
v_mfma_f32_16x16x32_bf16 v[160:163],v[52:55],v[96:99],0
v_mfma_f32_16x16x32_bf16 v[160:163],v[52:55],v[100:103],v[160:163]
v_mfma_f32_16x16x32_bf16 v[160:163],v[52:55],v[104:107],v[160:163]
v_mfma_f32_16x16x32_bf16 v[160:163],v[52:55],v[108:111],v[160:163]
s_waitcnt vmcnt(3)
v_mfma_f32_16x16x32_bf16 v[192:195],v[48:51],v[112:115],v[192:195]
v_mfma_f32_16x16x32_bf16 v[224:227],v[52:55],v[112:115],v[224:227]
)A"
R"A(s_waitcnt vmcnt(2)
v_mfma_f32_16x16x32_bf16 v[200:203],v[48:51],v[116:119],v[200:203]
v_mfma_f32_16x16x32_bf16 v[232:235],v[52:55],v[116:119],v[232:235]
s_waitcnt vmcnt(1)
v_cvt_pk_bf16_f32 v128,v128,v129
v_cvt_pk_bf16_f32 v129,v130,v131
v_mfma_f32_16x16x32_bf16 v[208:211],v[48:51],v[120:123],v[208:211]
v_mfma_f32_16x16x32_bf16 v[240:243],v[52:55],v[120:123],v[240:243]
s_waitcnt vmcnt(0)
v_cvt_pk_bf16_f32 v130,v160,v161
v_cvt_pk_bf16_f32 v131,v162,v163
v_mfma_f32_16x16x32_bf16 v[216:219],v[48:51],v[124:127],v[216:219]
v_cvt_pk_bf16_f32 v192,v192,v193
v_cvt_pk_bf16_f32 v193,v194,v195
v_cvt_pk_bf16_f32 v194,v224,v225
v_cvt_pk_bf16_f32 v195,v226,v227
v_cvt_pk_bf16_f32 v200,v200,v201
v_cvt_pk_bf16_f32 v201,v202,v203
v_mfma_f32_16x16x32_bf16 v[248:251],v[52:55],v[124:127],v[248:251]
v_permlane16_swap_b32 v128,v130
v_permlane16_swap_b32 v129,v131
v_cvt_pk_bf16_f32 v202,v232,v233
v_cvt_pk_bf16_f32 v203,v234,v235
v_cvt_pk_bf16_f32 v208,v208,v209
v_cvt_pk_bf16_f32 v209,v210,v211
v_cvt_pk_bf16_f32 v210,v240,v241
v_cvt_pk_bf16_f32 v211,v242,v243
buffer_store_dwordx4 v[128:131],v56,s[24:27],s83 offen offset:256 sc1 nt
v_permlane16_swap_b32 v192,v194
v_permlane16_swap_b32 v193,v195
buffer_store_dwordx4 v[192:195],v56,s[24:27],s87 offen sc1 nt
v_permlane16_swap_b32 v200,v202
v_permlane16_swap_b32 v201,v203
v_cvt_pk_bf16_f32 v216,v216,v217
v_cvt_pk_bf16_f32 v217,v218,v219
v_cvt_pk_bf16_f32 v218,v248,v249
v_cvt_pk_bf16_f32 v219,v250,v251
buffer_store_dwordx4 v[200:203],v56,s[24:27],s88 offen sc1 nt
v_permlane16_swap_b32 v208,v210
v_permlane16_swap_b32 v209,v211
buffer_store_dwordx4 v[208:211],v56,s[24:27],s89 offen sc1 nt
v_permlane16_swap_b32 v216,v218
v_permlane16_swap_b32 v217,v219
buffer_store_dwordx4 v[216:219],v56,s[24:27],s90 offen sc1 nt
s_lshl_b32 s69,s62,20
s_add_u32 s69,s69,s18
buffer_store_dword v20,v22,s[44:47],s69 offen sc1
L_geS_%=:
s_branch L_end_%=
L_hot_%=:
L_fr1_%=:
s_mov_b32 s19,65536
L_fl1_%=:
s_sleep 6
buffer_load_dwordx2 v[4:5],v3,s[44:47],s91 offen offset:256 sc1
s_waitcnt vmcnt(0)
s_sub_u32 s19,s19,1
s_cmp_eq_u32 s19,0
s_cbranch_scc1 L_fo1_%=
v_cmp_ne_u32 s[56:57],s52,v4
v_cmp_ne_u32 s[58:59],s53,v5
s_nop 1
s_or_b64 s[56:57],s[56:57],s[58:59]
s_cbranch_scc1 L_fl1_%=
s_branch L_fo1_%=
L_fr2_%=:
s_mov_b32 s19,65536
L_fl2_%=:
s_sleep 6
buffer_load_dwordx2 v[4:5],v3,s[44:47],s91 offen offset:1280 sc1
s_waitcnt vmcnt(0)
s_sub_u32 s19,s19,1
s_cmp_eq_u32 s19,0
s_cbranch_scc1 L_fo2_%=
v_cmp_ne_u32 s[56:57],s52,v4
v_cmp_ne_u32 s[58:59],s53,v5
s_nop 1
s_or_b64 s[56:57],s[56:57],s[58:59]
s_cbranch_scc1 L_fl2_%=
s_branch L_fo2_%=
L_fr3_%=:
s_mov_b32 s19,65536
L_fl3_%=:
s_sleep 6
buffer_load_dwordx2 v[4:5],v3,s[44:47],s91 offen offset:1280 sc1
s_waitcnt vmcnt(0)
s_sub_u32 s19,s19,1
s_cmp_eq_u32 s19,0
s_cbranch_scc1 L_fo3_%=
v_cmp_ne_u32 s[56:57],s52,v4
v_cmp_ne_u32 s[58:59],s53,v5
s_nop 1
s_or_b64 s[56:57],s[56:57],s[58:59]
s_cbranch_scc1 L_fl3_%=
s_branch L_fo3_%=
L_fr4_%=:
s_mov_b32 s19,65536
L_fl4_%=:
s_sleep 6
buffer_load_dwordx2 v[62:63],v61,s[44:47],s91 offen offset:2312 sc1
s_waitcnt vmcnt(0)
s_sub_u32 s19,s19,1
s_cmp_eq_u32 s19,0
s_cbranch_scc1 L_fo4_%=
v_cmp_ne_u32 s[56:57],s52,v62
v_cmp_ne_u32 s[58:59],s53,v63
s_nop 1
s_or_b64 s[56:57],s[56:57],s[58:59]
s_cbranch_scc1 L_fl4_%=
s_branch L_fo4_%=
L_fr5_%=:
s_mov_b32 s19,65536
L_fl5_%=:
s_sleep 6
buffer_load_dwordx2 v[4:5],v3,s[44:47],s91 offen offset:2304 sc1
s_waitcnt vmcnt(0)
s_sub_u32 s19,s19,1
s_cmp_eq_u32 s19,0
s_cbranch_scc1 L_fo5_%=
v_cmp_ne_u32 s[56:57],s52,v4
v_cmp_ne_u32 s[58:59],s53,v5
s_nop 1
s_or_b64 s[56:57],s[56:57],s[58:59]
s_cbranch_scc1 L_fl5_%=
s_branch L_fo5_%=
L_fr6_%=:
s_mov_b32 s19,65536
L_fl6_%=:
s_sleep 6
buffer_load_dwordx2 v[4:5],v3,s[44:47],s91 offen offset:256 sc1
s_waitcnt vmcnt(0)
s_sub_u32 s19,s19,1
s_cmp_eq_u32 s19,0
s_cbranch_scc1 L_fo6_%=
v_cmp_ne_u32 s[56:57],s52,v4
v_cmp_ne_u32 s[58:59],s53,v5
s_nop 1
s_or_b64 s[56:57],s[56:57],s[58:59]
s_cbranch_scc1 L_fl6_%=
s_branch L_fo6_%=
L_fr7_%=:
s_mov_b32 s19,65536
L_fl7_%=:
s_sleep 6
buffer_load_dwordx2 v[4:5],v3,s[44:47],s91 offen offset:1280 sc1
s_waitcnt vmcnt(0)
s_sub_u32 s19,s19,1
s_cmp_eq_u32 s19,0
s_cbranch_scc1 L_fo7_%=
v_cmp_ne_u32 s[56:57],s52,v4
v_cmp_ne_u32 s[58:59],s53,v5
s_nop 1
s_or_b64 s[56:57],s[56:57],s[58:59]
s_cbranch_scc1 L_fl7_%=
s_branch L_fo7_%=
L_fr8_%=:
s_mov_b32 s19,65536
L_fl8_%=:
s_sleep 6
buffer_load_dwordx2 v[62:63],v61,s[44:47],s91 offen offset:2312 sc1
s_waitcnt vmcnt(0)
s_sub_u32 s19,s19,1
s_cmp_eq_u32 s19,0
s_cbranch_scc1 L_fo8_%=
v_cmp_ne_u32 s[56:57],s52,v62
v_cmp_ne_u32 s[58:59],s53,v63
s_nop 1
s_or_b64 s[56:57],s[56:57],s[58:59]
s_cbranch_scc1 L_fl8_%=
s_branch L_fo8_%=
L_fr9_%=:
s_mov_b32 s19,65536
L_fl9_%=:
s_sleep 6
buffer_load_dwordx2 v[4:5],v3,s[44:47],s91 offen offset:2304 sc1
s_waitcnt vmcnt(0)
s_sub_u32 s19,s19,1
s_cmp_eq_u32 s19,0
s_cbranch_scc1 L_fo9_%=
v_cmp_ne_u32 s[56:57],s52,v4
v_cmp_ne_u32 s[58:59],s53,v5
s_nop 1
s_or_b64 s[56:57],s[56:57],s[58:59]
s_cbranch_scc1 L_fl9_%=
s_branch L_fo9_%=
L_slow0_%=:
s_waitcnt vmcnt(0) lgkmcnt(0)
s_nop 7
v_accvgpr_write_b32 a160,v32
v_accvgpr_write_b32 a161,v33
v_accvgpr_write_b32 a162,v34
v_accvgpr_write_b32 a163,v35
v_accvgpr_write_b32 a164,v36
v_accvgpr_write_b32 a165,v37
v_accvgpr_write_b32 a166,v38
v_accvgpr_write_b32 a167,v39
v_accvgpr_write_b32 a168,v40
v_accvgpr_write_b32 a169,v41
v_accvgpr_write_b32 a170,v42
v_accvgpr_write_b32 a171,v43
v_accvgpr_write_b32 a172,v44
v_accvgpr_write_b32 a173,v45
v_accvgpr_write_b32 a174,v46
v_accvgpr_write_b32 a175,v47
s_waitcnt vmcnt(0) lgkmcnt(0)
s_nop 7
v_bfe_u32 v11,v6,4,4
v_lshrrev_b32 v12,8,v6
v_lshlrev_b32 v8,8,v11
v_lshl_add_u32 v8,v12,4,v8
v_lshlrev_b32 v9,10,v12
v_lshl_add_u32 v9,v11,1,v9
v_add_u32 v10,4096,v9
v_mov_b32 v48,0xff800000
v_mov_b32 v52,0xff800000
v_mov_b32 v56,0xff800000
v_mov_b32 v60,0xff800000
s_mov_b32 s65,0
s_mov_b32 s66,64
L_s10_%=:
buffer_load_dwordx4 a[128:131],v8,s[20:23],s65 offen
buffer_load_dwordx4 a[132:135],v8,s[20:23],s65 offen offset:64
buffer_load_dwordx4 a[136:139],v8,s[20:23],s65 offen offset:128
buffer_load_dwordx4 a[140:143],v8,s[20:23],s65 offen offset:192
s_waitcnt vmcnt(0)
v_mfma_f32_16x16x32_bf16 v[64:67],a[128:131],v[132:135],0
v_mfma_f32_16x16x32_bf16 v[64:67],a[132:135],v[140:143],v[64:67]
v_mfma_f32_16x16x32_bf16 v[64:67],a[136:139],v[148:151],v[64:67]
v_mfma_f32_16x16x32_bf16 v[64:67],a[140:143],v[156:159],v[64:67]
v_mfma_f32_16x16x32_bf16 v[80:83],a[128:131],v[164:167],0
v_mfma_f32_16x16x32_bf16 v[80:83],a[132:135],v[172:175],v[80:83]
v_mfma_f32_16x16x32_bf16 v[80:83],a[136:139],v[180:183],v[80:83]
v_mfma_f32_16x16x32_bf16 v[80:83],a[140:143],v[188:191],v[80:83]
v_mfma_f32_16x16x32_bf16 v[96:99],a[128:131],v[196:199],0
v_mfma_f32_16x16x32_bf16 v[96:99],a[132:135],v[204:207],v[96:99]
v_mfma_f32_16x16x32_bf16 v[96:99],a[136:139],v[212:215],v[96:99]
v_mfma_f32_16x16x32_bf16 v[96:99],a[140:143],v[220:223],v[96:99]
v_mfma_f32_16x16x32_bf16 v[112:115],a[128:131],v[228:231],0
v_mfma_f32_16x16x32_bf16 v[112:115],a[132:135],v[236:239],v[112:115]
v_mfma_f32_16x16x32_bf16 v[112:115],a[136:139],v[244:247],v[112:115]
v_mfma_f32_16x16x32_bf16 v[112:115],a[140:143],v[252:255],v[112:115]
s_nop 7
v_max3_f32 v48,v48,v64,v65
v_max3_f32 v48,v48,v66,v67
v_max3_f32 v52,v52,v80,v81
v_max3_f32 v52,v52,v82,v83
v_max3_f32 v56,v56,v96,v97
v_max3_f32 v56,v56,v98,v99
v_max3_f32 v60,v60,v112,v113
v_max3_f32 v60,v60,v114,v115
s_add_u32 s65,s65,4096
s_sub_u32 s66,s66,1
s_cmp_lg_u32 s66,0
s_cbranch_scc1 L_s10_%=
v_mov_b32 v11,v48
s_nop 1
v_permlane32_swap_b32 v48,v11
v_max_f32 v48,v48,v11
v_mov_b32 v11,v48
s_nop 1
v_permlane16_swap_b32 v48,v11
v_max_f32 v48,v48,v11
v_sub_f32 v32,0,v48
v_sub_f32 v33,0,v48
v_sub_f32 v34,0,v48
v_sub_f32 v35,0,v48
v_mov_b32 v11,v52
s_nop 1
v_permlane32_swap_b32 v52,v11
v_max_f32 v52,v52,v11
v_mov_b32 v11,v52
s_nop 1
v_permlane16_swap_b32 v52,v11
v_max_f32 v52,v52,v11
v_sub_f32 v36,0,v52
v_sub_f32 v37,0,v52
v_sub_f32 v38,0,v52
v_sub_f32 v39,0,v52
v_mov_b32 v11,v56
s_nop 1
v_permlane32_swap_b32 v56,v11
v_max_f32 v56,v56,v11
v_mov_b32 v11,v56
s_nop 1
v_permlane16_swap_b32 v56,v11
v_max_f32 v56,v56,v11
v_sub_f32 v40,0,v56
v_sub_f32 v41,0,v56
v_sub_f32 v42,0,v56
v_sub_f32 v43,0,v56
v_mov_b32 v11,v60
s_nop 1
v_permlane32_swap_b32 v60,v11
v_max_f32 v60,v60,v11
v_mov_b32 v11,v60
s_nop 1
v_permlane16_swap_b32 v60,v11
v_max_f32 v60,v60,v11
v_max_f32 v60,s72,v60
v_sub_f32 v44,0,v60
v_sub_f32 v45,0,v60
v_sub_f32 v46,0,v60
v_sub_f32 v47,0,v60
v_mov_b32 v48,0
v_mov_b32 v49,0
v_mov_b32 v50,0
v_mov_b32 v51,0
v_mov_b32 v52,0
v_mov_b32 v53,0
v_mov_b32 v54,0
v_mov_b32 v55,0
v_mov_b32 v56,0
v_mov_b32 v57,0
v_mov_b32 v58,0
v_mov_b32 v59,0
v_mov_b32 v60,0
v_mov_b32 v61,0
v_mov_b32 v62,0
v_mov_b32 v63,0
v_accvgpr_write_b32 a0,0
v_accvgpr_write_b32 a1,0
v_accvgpr_write_b32 a2,0
v_accvgpr_write_b32 a3,0
v_accvgpr_write_b32 a4,0
v_accvgpr_write_b32 a5,0
v_accvgpr_write_b32 a6,0
v_accvgpr_write_b32 a7,0
v_accvgpr_write_b32 a8,0
v_accvgpr_write_b32 a9,0
v_accvgpr_write_b32 a10,0
v_accvgpr_write_b32 a11,0
v_accvgpr_write_b32 a12,0
v_accvgpr_write_b32 a13,0
v_accvgpr_write_b32 a14,0
v_accvgpr_write_b32 a15,0
v_accvgpr_write_b32 a16,0
v_accvgpr_write_b32 a17,0
v_accvgpr_write_b32 a18,0
v_accvgpr_write_b32 a19,0
v_accvgpr_write_b32 a20,0
v_accvgpr_write_b32 a21,0
v_accvgpr_write_b32 a22,0
v_accvgpr_write_b32 a23,0
v_accvgpr_write_b32 a24,0
v_accvgpr_write_b32 a25,0
v_accvgpr_write_b32 a26,0
v_accvgpr_write_b32 a27,0
v_accvgpr_write_b32 a28,0
v_accvgpr_write_b32 a29,0
v_accvgpr_write_b32 a30,0
v_accvgpr_write_b32 a31,0
v_accvgpr_write_b32 a32,0
v_accvgpr_write_b32 a33,0
v_accvgpr_write_b32 a34,0
v_accvgpr_write_b32 a35,0
v_accvgpr_write_b32 a36,0
v_accvgpr_write_b32 a37,0
v_accvgpr_write_b32 a38,0
v_accvgpr_write_b32 a39,0
v_accvgpr_write_b32 a40,0
v_accvgpr_write_b32 a41,0
v_accvgpr_write_b32 a42,0
v_accvgpr_write_b32 a43,0
v_accvgpr_write_b32 a44,0
v_accvgpr_write_b32 a45,0
v_accvgpr_write_b32 a46,0
v_accvgpr_write_b32 a47,0
v_accvgpr_write_b32 a48,0
v_accvgpr_write_b32 a49,0
v_accvgpr_write_b32 a50,0
v_accvgpr_write_b32 a51,0
v_accvgpr_write_b32 a52,0
v_accvgpr_write_b32 a53,0
v_accvgpr_write_b32 a54,0
v_accvgpr_write_b32 a55,0
v_accvgpr_write_b32 a56,0
v_accvgpr_write_b32 a57,0
v_accvgpr_write_b32 a58,0
v_accvgpr_write_b32 a59,0
v_accvgpr_write_b32 a60,0
v_accvgpr_write_b32 a61,0
v_accvgpr_write_b32 a62,0
v_accvgpr_write_b32 a63,0
v_accvgpr_write_b32 a64,0
v_accvgpr_write_b32 a65,0
v_accvgpr_write_b32 a66,0
v_accvgpr_write_b32 a67,0
v_accvgpr_write_b32 a68,0
v_accvgpr_write_b32 a69,0
v_accvgpr_write_b32 a70,0
v_accvgpr_write_b32 a71,0
v_accvgpr_write_b32 a72,0
v_accvgpr_write_b32 a73,0
v_accvgpr_write_b32 a74,0
v_accvgpr_write_b32 a75,0
v_accvgpr_write_b32 a76,0
v_accvgpr_write_b32 a77,0
v_accvgpr_write_b32 a78,0
v_accvgpr_write_b32 a79,0
v_accvgpr_write_b32 a80,0
v_accvgpr_write_b32 a81,0
v_accvgpr_write_b32 a82,0
v_accvgpr_write_b32 a83,0
v_accvgpr_write_b32 a84,0
v_accvgpr_write_b32 a85,0
v_accvgpr_write_b32 a86,0
v_accvgpr_write_b32 a87,0
v_accvgpr_write_b32 a88,0
v_accvgpr_write_b32 a89,0
v_accvgpr_write_b32 a90,0
v_accvgpr_write_b32 a91,0
v_accvgpr_write_b32 a92,0
v_accvgpr_write_b32 a93,0
v_accvgpr_write_b32 a94,0
v_accvgpr_write_b32 a95,0
v_accvgpr_write_b32 a96,0
v_accvgpr_write_b32 a97,0
v_accvgpr_write_b32 a98,0
v_accvgpr_write_b32 a99,0
v_accvgpr_write_b32 a100,0
v_accvgpr_write_b32 a101,0
v_accvgpr_write_b32 a102,0
v_accvgpr_write_b32 a103,0
v_accvgpr_write_b32 a104,0
v_accvgpr_write_b32 a105,0
v_accvgpr_write_b32 a106,0
v_accvgpr_write_b32 a107,0
v_accvgpr_write_b32 a108,0
v_accvgpr_write_b32 a109,0
v_accvgpr_write_b32 a110,0
v_accvgpr_write_b32 a111,0
v_accvgpr_write_b32 a112,0
v_accvgpr_write_b32 a113,0
v_accvgpr_write_b32 a114,0
v_accvgpr_write_b32 a115,0
v_accvgpr_write_b32 a116,0
v_accvgpr_write_b32 a117,0
v_accvgpr_write_b32 a118,0
v_accvgpr_write_b32 a119,0
v_accvgpr_write_b32 a120,0
v_accvgpr_write_b32 a121,0
v_accvgpr_write_b32 a122,0
v_accvgpr_write_b32 a123,0
v_accvgpr_write_b32 a124,0
v_accvgpr_write_b32 a125,0
v_accvgpr_write_b32 a126,0
v_accvgpr_write_b32 a127,0
)A"
R"A(s_mov_b32 s65,0
s_mov_b32 s66,32
L_s20_%=:
s_add_u32 s67,s65,4096
buffer_load_dwordx4 a[128:131],v8,s[20:23],s65 offen
buffer_load_dwordx4 a[132:135],v8,s[20:23],s65 offen offset:64
buffer_load_dwordx4 a[136:139],v8,s[20:23],s65 offen offset:128
buffer_load_dwordx4 a[140:143],v8,s[20:23],s65 offen offset:192
buffer_load_dwordx4 a[144:147],v8,s[20:23],s67 offen
buffer_load_dwordx4 a[148:151],v8,s[20:23],s67 offen offset:64
buffer_load_dwordx4 a[152:155],v8,s[20:23],s67 offen offset:128
buffer_load_dwordx4 a[156:159],v8,s[20:23],s67 offen offset:192
s_waitcnt vmcnt(0)
v_mfma_f32_16x16x32_bf16 v[64:67],a[128:131],v[132:135],v[32:35]
v_mfma_f32_16x16x32_bf16 v[64:67],a[132:135],v[140:143],v[64:67]
v_mfma_f32_16x16x32_bf16 v[64:67],a[136:139],v[148:151],v[64:67]
v_mfma_f32_16x16x32_bf16 v[64:67],a[140:143],v[156:159],v[64:67]
v_mfma_f32_16x16x32_bf16 v[68:71],a[144:147],v[132:135],v[32:35]
v_mfma_f32_16x16x32_bf16 v[68:71],a[148:151],v[140:143],v[68:71]
v_mfma_f32_16x16x32_bf16 v[68:71],a[152:155],v[148:151],v[68:71]
v_mfma_f32_16x16x32_bf16 v[68:71],a[156:159],v[156:159],v[68:71]
v_mfma_f32_16x16x32_bf16 v[80:83],a[128:131],v[164:167],v[36:39]
v_mfma_f32_16x16x32_bf16 v[80:83],a[132:135],v[172:175],v[80:83]
v_mfma_f32_16x16x32_bf16 v[80:83],a[136:139],v[180:183],v[80:83]
v_mfma_f32_16x16x32_bf16 v[80:83],a[140:143],v[188:191],v[80:83]
v_mfma_f32_16x16x32_bf16 v[84:87],a[144:147],v[164:167],v[36:39]
v_mfma_f32_16x16x32_bf16 v[84:87],a[148:151],v[172:175],v[84:87]
v_mfma_f32_16x16x32_bf16 v[84:87],a[152:155],v[180:183],v[84:87]
v_mfma_f32_16x16x32_bf16 v[84:87],a[156:159],v[188:191],v[84:87]
v_mfma_f32_16x16x32_bf16 v[96:99],a[128:131],v[196:199],v[40:43]
v_mfma_f32_16x16x32_bf16 v[96:99],a[132:135],v[204:207],v[96:99]
v_mfma_f32_16x16x32_bf16 v[96:99],a[136:139],v[212:215],v[96:99]
v_mfma_f32_16x16x32_bf16 v[96:99],a[140:143],v[220:223],v[96:99]
v_mfma_f32_16x16x32_bf16 v[100:103],a[144:147],v[196:199],v[40:43]
v_mfma_f32_16x16x32_bf16 v[100:103],a[148:151],v[204:207],v[100:103]
v_mfma_f32_16x16x32_bf16 v[100:103],a[152:155],v[212:215],v[100:103]
v_mfma_f32_16x16x32_bf16 v[100:103],a[156:159],v[220:223],v[100:103]
v_mfma_f32_16x16x32_bf16 v[112:115],a[128:131],v[228:231],v[44:47]
v_mfma_f32_16x16x32_bf16 v[112:115],a[132:135],v[236:239],v[112:115]
v_mfma_f32_16x16x32_bf16 v[112:115],a[136:139],v[244:247],v[112:115]
v_mfma_f32_16x16x32_bf16 v[112:115],a[140:143],v[252:255],v[112:115]
v_mfma_f32_16x16x32_bf16 v[116:119],a[144:147],v[228:231],v[44:47]
v_mfma_f32_16x16x32_bf16 v[116:119],a[148:151],v[236:239],v[116:119]
v_mfma_f32_16x16x32_bf16 v[116:119],a[152:155],v[244:247],v[116:119]
v_mfma_f32_16x16x32_bf16 v[116:119],a[156:159],v[252:255],v[116:119]
s_nop 7
v_exp_f32 v64,v64
v_exp_f32 v65,v65
v_exp_f32 v66,v66
v_exp_f32 v67,v67
v_exp_f32 v68,v68
v_exp_f32 v69,v69
v_exp_f32 v70,v70
v_exp_f32 v71,v71
s_nop 0
v_cvt_pk_bf16_f32 v64,v64,v65
v_cvt_pk_bf16_f32 v65,v66,v67
v_cvt_pk_bf16_f32 v66,v68,v69
v_cvt_pk_bf16_f32 v67,v70,v71
v_exp_f32 v80,v80
v_exp_f32 v81,v81
v_exp_f32 v82,v82
v_exp_f32 v83,v83
v_exp_f32 v84,v84
v_exp_f32 v85,v85
v_exp_f32 v86,v86
v_exp_f32 v87,v87
s_nop 0
v_cvt_pk_bf16_f32 v80,v80,v81
v_cvt_pk_bf16_f32 v81,v82,v83
v_cvt_pk_bf16_f32 v82,v84,v85
v_cvt_pk_bf16_f32 v83,v86,v87
v_exp_f32 v96,v96
v_exp_f32 v97,v97
v_exp_f32 v98,v98
v_exp_f32 v99,v99
v_exp_f32 v100,v100
v_exp_f32 v101,v101
v_exp_f32 v102,v102
v_exp_f32 v103,v103
s_nop 0
v_cvt_pk_bf16_f32 v96,v96,v97
v_cvt_pk_bf16_f32 v97,v98,v99
v_cvt_pk_bf16_f32 v98,v100,v101
v_cvt_pk_bf16_f32 v99,v102,v103
v_exp_f32 v112,v112
v_exp_f32 v113,v113
v_exp_f32 v114,v114
v_exp_f32 v115,v115
v_exp_f32 v116,v116
v_exp_f32 v117,v117
v_exp_f32 v118,v118
v_exp_f32 v119,v119
s_nop 0
v_cvt_pk_bf16_f32 v112,v112,v113
v_cvt_pk_bf16_f32 v113,v114,v115
v_cvt_pk_bf16_f32 v114,v116,v117
v_cvt_pk_bf16_f32 v115,v118,v119
buffer_load_ushort v72,v9,s[24:27],s65 offen
buffer_load_ushort v73,v9,s[24:27],s65 offen offset:256
buffer_load_ushort v74,v9,s[24:27],s65 offen offset:512
buffer_load_ushort v75,v9,s[24:27],s65 offen offset:768
buffer_load_ushort v76,v10,s[24:27],s65 offen
buffer_load_ushort v77,v10,s[24:27],s65 offen offset:256
buffer_load_ushort v78,v10,s[24:27],s65 offen offset:512
buffer_load_ushort v79,v10,s[24:27],s65 offen offset:768
s_waitcnt vmcnt(0)
v_lshl_or_b32 v88,v73,16,v72
v_lshl_or_b32 v89,v75,16,v74
v_lshl_or_b32 v90,v77,16,v76
v_lshl_or_b32 v91,v79,16,v78
s_nop 1
v_mfma_f32_16x16x32_bf16 a[0:3],v[88:91],v[64:67],a[0:3]
v_mfma_f32_16x16x32_bf16 a[32:35],v[88:91],v[80:83],a[32:35]
v_mfma_f32_16x16x32_bf16 a[64:67],v[88:91],v[96:99],a[64:67]
v_mfma_f32_16x16x32_bf16 a[96:99],v[88:91],v[112:115],a[96:99]
buffer_load_ushort v72,v9,s[24:27],s65 offen offset:32
buffer_load_ushort v73,v9,s[24:27],s65 offen offset:288
buffer_load_ushort v74,v9,s[24:27],s65 offen offset:544
buffer_load_ushort v75,v9,s[24:27],s65 offen offset:800
buffer_load_ushort v76,v10,s[24:27],s65 offen offset:32
buffer_load_ushort v77,v10,s[24:27],s65 offen offset:288
buffer_load_ushort v78,v10,s[24:27],s65 offen offset:544
buffer_load_ushort v79,v10,s[24:27],s65 offen offset:800
s_waitcnt vmcnt(0)
v_lshl_or_b32 v88,v73,16,v72
v_lshl_or_b32 v89,v75,16,v74
v_lshl_or_b32 v90,v77,16,v76
v_lshl_or_b32 v91,v79,16,v78
s_nop 1
v_mfma_f32_16x16x32_bf16 a[4:7],v[88:91],v[64:67],a[4:7]
v_mfma_f32_16x16x32_bf16 a[36:39],v[88:91],v[80:83],a[36:39]
v_mfma_f32_16x16x32_bf16 a[68:71],v[88:91],v[96:99],a[68:71]
v_mfma_f32_16x16x32_bf16 a[100:103],v[88:91],v[112:115],a[100:103]
buffer_load_ushort v72,v9,s[24:27],s65 offen offset:64
buffer_load_ushort v73,v9,s[24:27],s65 offen offset:320
buffer_load_ushort v74,v9,s[24:27],s65 offen offset:576
buffer_load_ushort v75,v9,s[24:27],s65 offen offset:832
buffer_load_ushort v76,v10,s[24:27],s65 offen offset:64
buffer_load_ushort v77,v10,s[24:27],s65 offen offset:320
buffer_load_ushort v78,v10,s[24:27],s65 offen offset:576
buffer_load_ushort v79,v10,s[24:27],s65 offen offset:832
s_waitcnt vmcnt(0)
v_lshl_or_b32 v88,v73,16,v72
v_lshl_or_b32 v89,v75,16,v74
v_lshl_or_b32 v90,v77,16,v76
v_lshl_or_b32 v91,v79,16,v78
s_nop 1
v_mfma_f32_16x16x32_bf16 a[8:11],v[88:91],v[64:67],a[8:11]
v_mfma_f32_16x16x32_bf16 a[40:43],v[88:91],v[80:83],a[40:43]
v_mfma_f32_16x16x32_bf16 a[72:75],v[88:91],v[96:99],a[72:75]
v_mfma_f32_16x16x32_bf16 a[104:107],v[88:91],v[112:115],a[104:107]
buffer_load_ushort v72,v9,s[24:27],s65 offen offset:96
buffer_load_ushort v73,v9,s[24:27],s65 offen offset:352
buffer_load_ushort v74,v9,s[24:27],s65 offen offset:608
buffer_load_ushort v75,v9,s[24:27],s65 offen offset:864
buffer_load_ushort v76,v10,s[24:27],s65 offen offset:96
buffer_load_ushort v77,v10,s[24:27],s65 offen offset:352
buffer_load_ushort v78,v10,s[24:27],s65 offen offset:608
buffer_load_ushort v79,v10,s[24:27],s65 offen offset:864
s_waitcnt vmcnt(0)
v_lshl_or_b32 v88,v73,16,v72
v_lshl_or_b32 v89,v75,16,v74
v_lshl_or_b32 v90,v77,16,v76
v_lshl_or_b32 v91,v79,16,v78
s_nop 1
v_mfma_f32_16x16x32_bf16 a[12:15],v[88:91],v[64:67],a[12:15]
v_mfma_f32_16x16x32_bf16 a[44:47],v[88:91],v[80:83],a[44:47]
v_mfma_f32_16x16x32_bf16 a[76:79],v[88:91],v[96:99],a[76:79]
v_mfma_f32_16x16x32_bf16 a[108:111],v[88:91],v[112:115],a[108:111]
buffer_load_ushort v72,v9,s[24:27],s65 offen offset:128
buffer_load_ushort v73,v9,s[24:27],s65 offen offset:384
buffer_load_ushort v74,v9,s[24:27],s65 offen offset:640
buffer_load_ushort v75,v9,s[24:27],s65 offen offset:896
buffer_load_ushort v76,v10,s[24:27],s65 offen offset:128
buffer_load_ushort v77,v10,s[24:27],s65 offen offset:384
buffer_load_ushort v78,v10,s[24:27],s65 offen offset:640
buffer_load_ushort v79,v10,s[24:27],s65 offen offset:896
s_waitcnt vmcnt(0)
v_lshl_or_b32 v88,v73,16,v72
v_lshl_or_b32 v89,v75,16,v74
v_lshl_or_b32 v90,v77,16,v76
v_lshl_or_b32 v91,v79,16,v78
s_nop 1
v_mfma_f32_16x16x32_bf16 a[16:19],v[88:91],v[64:67],a[16:19]
v_mfma_f32_16x16x32_bf16 a[48:51],v[88:91],v[80:83],a[48:51]
v_mfma_f32_16x16x32_bf16 a[80:83],v[88:91],v[96:99],a[80:83]
v_mfma_f32_16x16x32_bf16 a[112:115],v[88:91],v[112:115],a[112:115]
buffer_load_ushort v72,v9,s[24:27],s65 offen offset:160
buffer_load_ushort v73,v9,s[24:27],s65 offen offset:416
buffer_load_ushort v74,v9,s[24:27],s65 offen offset:672
buffer_load_ushort v75,v9,s[24:27],s65 offen offset:928
buffer_load_ushort v76,v10,s[24:27],s65 offen offset:160
buffer_load_ushort v77,v10,s[24:27],s65 offen offset:416
buffer_load_ushort v78,v10,s[24:27],s65 offen offset:672
buffer_load_ushort v79,v10,s[24:27],s65 offen offset:928
s_waitcnt vmcnt(0)
v_lshl_or_b32 v88,v73,16,v72
v_lshl_or_b32 v89,v75,16,v74
v_lshl_or_b32 v90,v77,16,v76
v_lshl_or_b32 v91,v79,16,v78
s_nop 1
v_mfma_f32_16x16x32_bf16 a[20:23],v[88:91],v[64:67],a[20:23]
v_mfma_f32_16x16x32_bf16 a[52:55],v[88:91],v[80:83],a[52:55]
v_mfma_f32_16x16x32_bf16 a[84:87],v[88:91],v[96:99],a[84:87]
v_mfma_f32_16x16x32_bf16 a[116:119],v[88:91],v[112:115],a[116:119]
buffer_load_ushort v72,v9,s[24:27],s65 offen offset:192
buffer_load_ushort v73,v9,s[24:27],s65 offen offset:448
buffer_load_ushort v74,v9,s[24:27],s65 offen offset:704
buffer_load_ushort v75,v9,s[24:27],s65 offen offset:960
buffer_load_ushort v76,v10,s[24:27],s65 offen offset:192
buffer_load_ushort v77,v10,s[24:27],s65 offen offset:448
buffer_load_ushort v78,v10,s[24:27],s65 offen offset:704
buffer_load_ushort v79,v10,s[24:27],s65 offen offset:960
s_waitcnt vmcnt(0)
v_lshl_or_b32 v88,v73,16,v72
v_lshl_or_b32 v89,v75,16,v74
v_lshl_or_b32 v90,v77,16,v76
v_lshl_or_b32 v91,v79,16,v78
s_nop 1
v_mfma_f32_16x16x32_bf16 a[24:27],v[88:91],v[64:67],a[24:27]
v_mfma_f32_16x16x32_bf16 a[56:59],v[88:91],v[80:83],a[56:59]
v_mfma_f32_16x16x32_bf16 a[88:91],v[88:91],v[96:99],a[88:91]
v_mfma_f32_16x16x32_bf16 a[120:123],v[88:91],v[112:115],a[120:123]
buffer_load_ushort v72,v9,s[24:27],s65 offen offset:224
buffer_load_ushort v73,v9,s[24:27],s65 offen offset:480
buffer_load_ushort v74,v9,s[24:27],s65 offen offset:736
buffer_load_ushort v75,v9,s[24:27],s65 offen offset:992
buffer_load_ushort v76,v10,s[24:27],s65 offen offset:224
buffer_load_ushort v77,v10,s[24:27],s65 offen offset:480
buffer_load_ushort v78,v10,s[24:27],s65 offen offset:736
buffer_load_ushort v79,v10,s[24:27],s65 offen offset:992
s_waitcnt vmcnt(0)
v_lshl_or_b32 v88,v73,16,v72
v_lshl_or_b32 v89,v75,16,v74
v_lshl_or_b32 v90,v77,16,v76
v_lshl_or_b32 v91,v79,16,v78
s_nop 1
v_mfma_f32_16x16x32_bf16 a[28:31],v[88:91],v[64:67],a[28:31]
v_mfma_f32_16x16x32_bf16 a[60:63],v[88:91],v[80:83],a[60:63]
v_mfma_f32_16x16x32_bf16 a[92:95],v[88:91],v[96:99],a[92:95]
v_mfma_f32_16x16x32_bf16 a[124:127],v[88:91],v[112:115],a[124:127]
v_mfma_f32_16x16x32_bf16 v[48:51],v[28:31],v[64:67],v[48:51]
v_mfma_f32_16x16x32_bf16 v[52:55],v[28:31],v[80:83],v[52:55]
v_mfma_f32_16x16x32_bf16 v[56:59],v[28:31],v[96:99],v[56:59]
v_mfma_f32_16x16x32_bf16 v[60:63],v[28:31],v[112:115],v[60:63]
s_add_u32 s65,s65,8192
s_sub_u32 s66,s66,1
s_cmp_lg_u32 s66,0
s_cbranch_scc1 L_s20_%=
s_nop 7
v_max_f32 v24,0x00800000,v48
v_rcp_f32 v24,v24
v_max_f32 v25,0x00800000,v52
v_rcp_f32 v25,v25
v_max_f32 v26,0x00800000,v56
v_rcp_f32 v26,v26
v_max_f32 v27,0x00800000,v60
v_rcp_f32 v27,v27
v_accvgpr_read_b32 v8,a0
v_accvgpr_read_b32 v9,a1
v_accvgpr_read_b32 v10,a2
v_accvgpr_read_b32 v11,a3
v_accvgpr_read_b32 v12,a4
v_accvgpr_read_b32 v13,a5
v_accvgpr_read_b32 v14,a6
v_accvgpr_read_b32 v15,a7
v_mul_f32 v8,v8,v24
v_mul_f32 v9,v9,v24
v_mul_f32 v10,v10,v24
v_mul_f32 v11,v11,v24
v_mul_f32 v12,v12,v24
v_mul_f32 v13,v13,v24
v_mul_f32 v14,v14,v24
v_mul_f32 v15,v15,v24
v_cvt_pk_bf16_f32 v8,v8,v9
v_cvt_pk_bf16_f32 v9,v10,v11
v_cvt_pk_bf16_f32 v10,v12,v13
v_cvt_pk_bf16_f32 v11,v14,v15
ds_write_b128 v7,v[8:11] offset:6144
v_accvgpr_read_b32 v16,a8
v_accvgpr_read_b32 v17,a9
v_accvgpr_read_b32 v18,a10
v_accvgpr_read_b32 v19,a11
v_accvgpr_read_b32 v20,a12
v_accvgpr_read_b32 v21,a13
v_accvgpr_read_b32 v22,a14
v_accvgpr_read_b32 v23,a15
v_mul_f32 v16,v16,v24
v_mul_f32 v17,v17,v24
v_mul_f32 v18,v18,v24
v_mul_f32 v19,v19,v24
v_mul_f32 v20,v20,v24
v_mul_f32 v21,v21,v24
v_mul_f32 v22,v22,v24
v_mul_f32 v23,v23,v24
v_cvt_pk_bf16_f32 v16,v16,v17
)A"
R"A(v_cvt_pk_bf16_f32 v17,v18,v19
v_cvt_pk_bf16_f32 v18,v20,v21
v_cvt_pk_bf16_f32 v19,v22,v23
ds_write_b128 v7,v[16:19] offset:7168
v_accvgpr_read_b32 v8,a16
v_accvgpr_read_b32 v9,a17
v_accvgpr_read_b32 v10,a18
v_accvgpr_read_b32 v11,a19
v_accvgpr_read_b32 v12,a20
v_accvgpr_read_b32 v13,a21
v_accvgpr_read_b32 v14,a22
v_accvgpr_read_b32 v15,a23
v_mul_f32 v8,v8,v24
v_mul_f32 v9,v9,v24
v_mul_f32 v10,v10,v24
v_mul_f32 v11,v11,v24
v_mul_f32 v12,v12,v24
v_mul_f32 v13,v13,v24
v_mul_f32 v14,v14,v24
v_mul_f32 v15,v15,v24
v_cvt_pk_bf16_f32 v8,v8,v9
v_cvt_pk_bf16_f32 v9,v10,v11
v_cvt_pk_bf16_f32 v10,v12,v13
v_cvt_pk_bf16_f32 v11,v14,v15
ds_write_b128 v7,v[8:11]
v_accvgpr_read_b32 v16,a24
v_accvgpr_read_b32 v17,a25
v_accvgpr_read_b32 v18,a26
v_accvgpr_read_b32 v19,a27
v_accvgpr_read_b32 v20,a28
v_accvgpr_read_b32 v21,a29
v_accvgpr_read_b32 v22,a30
v_accvgpr_read_b32 v23,a31
v_mul_f32 v16,v16,v24
v_mul_f32 v17,v17,v24
v_mul_f32 v18,v18,v24
v_mul_f32 v19,v19,v24
v_mul_f32 v20,v20,v24
v_mul_f32 v21,v21,v24
v_mul_f32 v22,v22,v24
v_mul_f32 v23,v23,v24
v_cvt_pk_bf16_f32 v16,v16,v17
v_cvt_pk_bf16_f32 v17,v18,v19
v_cvt_pk_bf16_f32 v18,v20,v21
v_cvt_pk_bf16_f32 v19,v22,v23
ds_write_b128 v7,v[16:19] offset:1024
v_accvgpr_read_b32 v32,a160
v_accvgpr_read_b32 v33,a161
v_accvgpr_read_b32 v34,a162
v_accvgpr_read_b32 v35,a163
v_accvgpr_read_b32 v36,a164
v_accvgpr_read_b32 v37,a165
v_accvgpr_read_b32 v38,a166
v_accvgpr_read_b32 v39,a167
v_accvgpr_read_b32 v40,a168
v_accvgpr_read_b32 v41,a169
v_accvgpr_read_b32 v42,a170
v_accvgpr_read_b32 v43,a171
v_accvgpr_read_b32 v44,a172
v_accvgpr_read_b32 v45,a173
v_accvgpr_read_b32 v46,a174
v_accvgpr_read_b32 v47,a175
s_waitcnt lgkmcnt(0)
v_subrev_u32 v8,s78,v1
ds_read_b128 a[128:131],v8
ds_read_b128 a[132:135],v8 offset:256
ds_read_b128 a[136:139],v8 offset:512
ds_read_b128 a[140:143],v8 offset:768
ds_read_b128 a[144:147],v8 offset:4096
ds_read_b128 a[148:151],v8 offset:4352
ds_read_b128 a[152:155],v8 offset:4608
ds_read_b128 a[156:159],v8 offset:4864
ds_read_b128 a[160:163],v8 offset:8192
ds_read_b128 a[164:167],v8 offset:8448
ds_read_b128 a[168:171],v8 offset:8704
ds_read_b128 a[172:175],v8 offset:8960
ds_read_b128 a[176:179],v8 offset:12288
ds_read_b128 a[180:183],v8 offset:12544
ds_read_b128 a[184:187],v8 offset:12800
ds_read_b128 a[188:191],v8 offset:13056
s_waitcnt lgkmcnt(15)
v_mfma_f32_16x16x32_bf16 v[64:67],a[128:131],v[128:131],v[32:35]
s_waitcnt lgkmcnt(14)
v_mfma_f32_16x16x32_bf16 v[64:67],a[132:135],v[136:139],v[64:67]
s_waitcnt lgkmcnt(13)
v_mfma_f32_16x16x32_bf16 v[64:67],a[136:139],v[144:147],v[64:67]
s_waitcnt lgkmcnt(12)
v_mfma_f32_16x16x32_bf16 v[64:67],a[140:143],v[152:155],v[64:67]
s_waitcnt lgkmcnt(8)
v_mfma_f32_16x16x32_bf16 v[68:71],a[156:159],v[152:155],v[32:35]
v_mfma_f32_16x16x32_bf16 v[68:71],a[152:155],v[144:147],v[68:71]
v_mfma_f32_16x16x32_bf16 v[68:71],a[148:151],v[136:139],v[68:71]
v_mfma_f32_16x16x32_bf16 v[68:71],a[144:147],v[128:131],v[68:71]
s_waitcnt lgkmcnt(0)
v_mfma_f32_16x16x32_bf16 v[72:75],a[160:163],v[128:131],v[32:35]
v_mfma_f32_16x16x32_bf16 v[72:75],a[164:167],v[136:139],v[72:75]
v_mfma_f32_16x16x32_bf16 v[72:75],a[168:171],v[144:147],v[72:75]
v_mfma_f32_16x16x32_bf16 v[72:75],a[172:175],v[152:155],v[72:75]
v_mfma_f32_16x16x32_bf16 v[76:79],a[188:191],v[152:155],v[32:35]
v_mfma_f32_16x16x32_bf16 v[76:79],a[184:187],v[144:147],v[76:79]
v_mfma_f32_16x16x32_bf16 v[76:79],a[180:183],v[136:139],v[76:79]
v_mfma_f32_16x16x32_bf16 v[76:79],a[176:179],v[128:131],v[76:79]
v_mfma_f32_16x16x32_bf16 v[80:83],a[128:131],v[160:163],v[36:39]
v_mfma_f32_16x16x32_bf16 v[80:83],a[132:135],v[168:171],v[80:83]
v_mfma_f32_16x16x32_bf16 v[80:83],a[136:139],v[176:179],v[80:83]
v_mfma_f32_16x16x32_bf16 v[80:83],a[140:143],v[184:187],v[80:83]
v_mfma_f32_16x16x32_bf16 v[84:87],a[156:159],v[184:187],v[36:39]
v_mfma_f32_16x16x32_bf16 v[84:87],a[152:155],v[176:179],v[84:87]
v_mfma_f32_16x16x32_bf16 v[84:87],a[148:151],v[168:171],v[84:87]
v_mfma_f32_16x16x32_bf16 v[84:87],a[144:147],v[160:163],v[84:87]
v_mfma_f32_16x16x32_bf16 v[88:91],a[160:163],v[160:163],v[36:39]
v_mfma_f32_16x16x32_bf16 v[88:91],a[164:167],v[168:171],v[88:91]
v_mfma_f32_16x16x32_bf16 v[88:91],a[168:171],v[176:179],v[88:91]
v_mfma_f32_16x16x32_bf16 v[88:91],a[172:175],v[184:187],v[88:91]
v_mfma_f32_16x16x32_bf16 v[92:95],a[188:191],v[184:187],v[36:39]
v_mfma_f32_16x16x32_bf16 v[92:95],a[184:187],v[176:179],v[92:95]
v_mfma_f32_16x16x32_bf16 v[92:95],a[180:183],v[168:171],v[92:95]
v_mfma_f32_16x16x32_bf16 v[92:95],a[176:179],v[160:163],v[92:95]
v_mfma_f32_16x16x32_bf16 v[96:99],a[128:131],v[192:195],v[40:43]
v_mfma_f32_16x16x32_bf16 v[96:99],a[132:135],v[200:203],v[96:99]
v_mfma_f32_16x16x32_bf16 v[96:99],a[136:139],v[208:211],v[96:99]
v_mfma_f32_16x16x32_bf16 v[96:99],a[140:143],v[216:219],v[96:99]
v_mfma_f32_16x16x32_bf16 v[100:103],a[156:159],v[216:219],v[40:43]
v_mfma_f32_16x16x32_bf16 v[100:103],a[152:155],v[208:211],v[100:103]
v_mfma_f32_16x16x32_bf16 v[100:103],a[148:151],v[200:203],v[100:103]
v_mfma_f32_16x16x32_bf16 v[100:103],a[144:147],v[192:195],v[100:103]
v_mfma_f32_16x16x32_bf16 v[104:107],a[160:163],v[192:195],v[40:43]
v_mfma_f32_16x16x32_bf16 v[104:107],a[164:167],v[200:203],v[104:107]
v_mfma_f32_16x16x32_bf16 v[104:107],a[168:171],v[208:211],v[104:107]
v_mfma_f32_16x16x32_bf16 v[104:107],a[172:175],v[216:219],v[104:107]
v_mfma_f32_16x16x32_bf16 v[108:111],a[188:191],v[216:219],v[40:43]
v_mfma_f32_16x16x32_bf16 v[108:111],a[184:187],v[208:211],v[108:111]
v_mfma_f32_16x16x32_bf16 v[108:111],a[180:183],v[200:203],v[108:111]
v_mfma_f32_16x16x32_bf16 v[108:111],a[176:179],v[192:195],v[108:111]
v_exp_f32 v64,v64
v_exp_f32 v65,v65
s_nop 0
v_cvt_pk_bf16_f32 v64,v64,v65
v_exp_f32 v66,v66
v_exp_f32 v67,v67
s_nop 0
v_cvt_pk_bf16_f32 v65,v66,v67
v_exp_f32 v68,v68
v_exp_f32 v69,v69
s_nop 0
v_cvt_pk_bf16_f32 v66,v68,v69
v_exp_f32 v70,v70
v_exp_f32 v71,v71
s_nop 0
v_cvt_pk_bf16_f32 v67,v70,v71
v_exp_f32 v72,v72
v_exp_f32 v73,v73
s_nop 0
v_cvt_pk_bf16_f32 v72,v72,v73
v_exp_f32 v74,v74
v_exp_f32 v75,v75
s_nop 0
v_cvt_pk_bf16_f32 v73,v74,v75
v_exp_f32 v76,v76
v_exp_f32 v77,v77
s_nop 0
v_cvt_pk_bf16_f32 v74,v76,v77
v_exp_f32 v78,v78
v_exp_f32 v79,v79
s_nop 0
v_cvt_pk_bf16_f32 v75,v78,v79
v_exp_f32 v80,v80
v_exp_f32 v81,v81
s_nop 0
v_cvt_pk_bf16_f32 v80,v80,v81
v_exp_f32 v82,v82
v_exp_f32 v83,v83
s_nop 0
v_cvt_pk_bf16_f32 v81,v82,v83
v_exp_f32 v84,v84
v_exp_f32 v85,v85
s_nop 0
v_cvt_pk_bf16_f32 v82,v84,v85
v_exp_f32 v86,v86
v_exp_f32 v87,v87
s_nop 0
v_cvt_pk_bf16_f32 v83,v86,v87
v_exp_f32 v88,v88
v_exp_f32 v89,v89
s_nop 0
v_cvt_pk_bf16_f32 v88,v88,v89
v_mfma_f32_16x16x32_bf16 a[0:3],a[192:195],v[64:67],0
v_mfma_f32_16x16x32_bf16 a[0:3],a[196:199],v[72:75],a[0:3]
v_mfma_f32_16x16x32_bf16 a[4:7],a[200:203],v[64:67],0
v_mfma_f32_16x16x32_bf16 a[4:7],a[204:207],v[72:75],a[4:7]
v_mfma_f32_16x16x32_bf16 a[8:11],a[208:211],v[64:67],0
v_mfma_f32_16x16x32_bf16 a[8:11],a[212:215],v[72:75],a[8:11]
v_mfma_f32_16x16x32_bf16 a[12:15],a[216:219],v[64:67],0
v_mfma_f32_16x16x32_bf16 a[12:15],a[220:223],v[72:75],a[12:15]
v_mfma_f32_16x16x32_bf16 a[16:19],a[224:227],v[64:67],0
v_mfma_f32_16x16x32_bf16 a[16:19],a[228:231],v[72:75],a[16:19]
v_mfma_f32_16x16x32_bf16 a[20:23],a[232:235],v[64:67],0
v_mfma_f32_16x16x32_bf16 a[20:23],a[236:239],v[72:75],a[20:23]
v_mfma_f32_16x16x32_bf16 a[24:27],a[240:243],v[64:67],0
v_mfma_f32_16x16x32_bf16 a[24:27],a[244:247],v[72:75],a[24:27]
v_mfma_f32_16x16x32_bf16 a[28:31],a[248:251],v[64:67],0
v_mfma_f32_16x16x32_bf16 a[28:31],a[252:255],v[72:75],a[28:31]
v_mfma_f32_16x16x32_bf16 v[48:51],v[28:31],v[64:67],0
v_mfma_f32_16x16x32_bf16 v[48:51],v[28:31],v[72:75],v[48:51]
s_waitcnt vmcnt(0) lgkmcnt(0)
s_nop 7
s_branch L_ok0_%=
L_slow1_%=:
s_waitcnt vmcnt(0) lgkmcnt(0)
s_nop 7
v_bfe_u32 v11,v6,4,4
v_lshrrev_b32 v12,8,v6
v_lshlrev_b32 v8,8,v11
v_lshl_add_u32 v8,v12,4,v8
v_lshlrev_b32 v9,10,v12
v_lshl_add_u32 v9,v11,1,v9
v_add_u32 v10,4096,v9
v_mov_b32 v48,0xff800000
v_mov_b32 v52,0xff800000
v_mov_b32 v56,0xff800000
v_mov_b32 v60,0xff800000
s_mov_b32 s65,262144
s_mov_b32 s66,64
L_s11_%=:
buffer_load_dwordx4 a[128:131],v8,s[20:23],s65 offen
buffer_load_dwordx4 a[132:135],v8,s[20:23],s65 offen offset:64
buffer_load_dwordx4 a[136:139],v8,s[20:23],s65 offen offset:128
buffer_load_dwordx4 a[140:143],v8,s[20:23],s65 offen offset:192
s_waitcnt vmcnt(0)
v_mfma_f32_16x16x32_bf16 v[64:67],a[128:131],v[128:131],0
v_mfma_f32_16x16x32_bf16 v[64:67],a[132:135],v[136:139],v[64:67]
v_mfma_f32_16x16x32_bf16 v[64:67],a[136:139],v[144:147],v[64:67]
v_mfma_f32_16x16x32_bf16 v[64:67],a[140:143],v[152:155],v[64:67]
v_mfma_f32_16x16x32_bf16 v[80:83],a[128:131],v[160:163],0
v_mfma_f32_16x16x32_bf16 v[80:83],a[132:135],v[168:171],v[80:83]
v_mfma_f32_16x16x32_bf16 v[80:83],a[136:139],v[176:179],v[80:83]
v_mfma_f32_16x16x32_bf16 v[80:83],a[140:143],v[184:187],v[80:83]
v_mfma_f32_16x16x32_bf16 v[96:99],a[128:131],v[192:195],0
v_mfma_f32_16x16x32_bf16 v[96:99],a[132:135],v[200:203],v[96:99]
v_mfma_f32_16x16x32_bf16 v[96:99],a[136:139],v[208:211],v[96:99]
v_mfma_f32_16x16x32_bf16 v[96:99],a[140:143],v[216:219],v[96:99]
v_mfma_f32_16x16x32_bf16 v[112:115],a[128:131],v[224:227],0
v_mfma_f32_16x16x32_bf16 v[112:115],a[132:135],v[232:235],v[112:115]
v_mfma_f32_16x16x32_bf16 v[112:115],a[136:139],v[240:243],v[112:115]
v_mfma_f32_16x16x32_bf16 v[112:115],a[140:143],v[248:251],v[112:115]
s_nop 7
v_max3_f32 v48,v48,v64,v65
v_max3_f32 v48,v48,v66,v67
v_max3_f32 v52,v52,v80,v81
v_max3_f32 v52,v52,v82,v83
v_max3_f32 v56,v56,v96,v97
v_max3_f32 v56,v56,v98,v99
v_max3_f32 v60,v60,v112,v113
v_max3_f32 v60,v60,v114,v115
s_add_u32 s65,s65,4096
s_sub_u32 s66,s66,1
s_cmp_lg_u32 s66,0
s_cbranch_scc1 L_s11_%=
v_mov_b32 v11,v48
s_nop 1
v_permlane32_swap_b32 v48,v11
v_max_f32 v48,v48,v11
v_mov_b32 v11,v48
s_nop 1
v_permlane16_swap_b32 v48,v11
v_max_f32 v48,v48,v11
v_sub_f32 v32,0,v48
v_sub_f32 v33,0,v48
v_sub_f32 v34,0,v48
v_sub_f32 v35,0,v48
v_mov_b32 v11,v52
s_nop 1
v_permlane32_swap_b32 v52,v11
v_max_f32 v52,v52,v11
v_mov_b32 v11,v52
s_nop 1
v_permlane16_swap_b32 v52,v11
v_max_f32 v52,v52,v11
v_sub_f32 v36,0,v52
v_sub_f32 v37,0,v52
v_sub_f32 v38,0,v52
v_sub_f32 v39,0,v52
v_mov_b32 v11,v56
s_nop 1
v_permlane32_swap_b32 v56,v11
v_max_f32 v56,v56,v11
v_mov_b32 v11,v56
s_nop 1
v_permlane16_swap_b32 v56,v11
v_max_f32 v56,v56,v11
v_sub_f32 v40,0,v56
v_sub_f32 v41,0,v56
v_sub_f32 v42,0,v56
v_sub_f32 v43,0,v56
v_mov_b32 v11,v60
s_nop 1
v_permlane32_swap_b32 v60,v11
v_max_f32 v60,v60,v11
v_mov_b32 v11,v60
s_nop 1
v_permlane16_swap_b32 v60,v11
v_max_f32 v60,v60,v11
v_max_f32 v60,s72,v60
v_sub_f32 v44,0,v60
v_sub_f32 v45,0,v60
v_sub_f32 v46,0,v60
v_sub_f32 v47,0,v60
v_mov_b32 v48,0
v_mov_b32 v49,0
v_mov_b32 v50,0
v_mov_b32 v51,0
v_mov_b32 v52,0
v_mov_b32 v53,0
v_mov_b32 v54,0
v_mov_b32 v55,0
v_mov_b32 v56,0
v_mov_b32 v57,0
v_mov_b32 v58,0
v_mov_b32 v59,0
v_mov_b32 v60,0
v_mov_b32 v61,0
v_mov_b32 v62,0
v_mov_b32 v63,0
v_accvgpr_write_b32 a0,0
v_accvgpr_write_b32 a1,0
v_accvgpr_write_b32 a2,0
v_accvgpr_write_b32 a3,0
v_accvgpr_write_b32 a4,0
v_accvgpr_write_b32 a5,0
v_accvgpr_write_b32 a6,0
v_accvgpr_write_b32 a7,0
v_accvgpr_write_b32 a8,0
v_accvgpr_write_b32 a9,0
v_accvgpr_write_b32 a10,0
v_accvgpr_write_b32 a11,0
v_accvgpr_write_b32 a12,0
v_accvgpr_write_b32 a13,0
v_accvgpr_write_b32 a14,0
v_accvgpr_write_b32 a15,0
v_accvgpr_write_b32 a16,0
v_accvgpr_write_b32 a17,0
v_accvgpr_write_b32 a18,0
v_accvgpr_write_b32 a19,0
v_accvgpr_write_b32 a20,0
v_accvgpr_write_b32 a21,0
v_accvgpr_write_b32 a22,0
v_accvgpr_write_b32 a23,0
v_accvgpr_write_b32 a24,0
v_accvgpr_write_b32 a25,0
v_accvgpr_write_b32 a26,0
v_accvgpr_write_b32 a27,0
v_accvgpr_write_b32 a28,0
v_accvgpr_write_b32 a29,0
v_accvgpr_write_b32 a30,0
v_accvgpr_write_b32 a31,0
v_mov_b32 v132,0
v_mov_b32 v133,0
v_mov_b32 v134,0
v_mov_b32 v135,0
v_mov_b32 v140,0
v_mov_b32 v141,0
v_mov_b32 v142,0
v_mov_b32 v143,0
v_mov_b32 v148,0
v_mov_b32 v149,0
v_mov_b32 v150,0
v_mov_b32 v151,0
v_mov_b32 v156,0
v_mov_b32 v157,0
v_mov_b32 v158,0
v_mov_b32 v159,0
v_mov_b32 v164,0
v_mov_b32 v165,0
v_mov_b32 v166,0
)A"
R"A(v_mov_b32 v167,0
v_mov_b32 v172,0
v_mov_b32 v173,0
v_mov_b32 v174,0
v_mov_b32 v175,0
v_mov_b32 v180,0
v_mov_b32 v181,0
v_mov_b32 v182,0
v_mov_b32 v183,0
v_mov_b32 v188,0
v_mov_b32 v189,0
v_mov_b32 v190,0
v_mov_b32 v191,0
v_mov_b32 v196,0
v_mov_b32 v197,0
v_mov_b32 v198,0
v_mov_b32 v199,0
v_mov_b32 v204,0
v_mov_b32 v205,0
v_mov_b32 v206,0
v_mov_b32 v207,0
v_mov_b32 v212,0
v_mov_b32 v213,0
v_mov_b32 v214,0
v_mov_b32 v215,0
v_mov_b32 v220,0
v_mov_b32 v221,0
v_mov_b32 v222,0
v_mov_b32 v223,0
v_mov_b32 v228,0
v_mov_b32 v229,0
v_mov_b32 v230,0
v_mov_b32 v231,0
v_mov_b32 v236,0
v_mov_b32 v237,0
v_mov_b32 v238,0
v_mov_b32 v239,0
v_mov_b32 v244,0
v_mov_b32 v245,0
v_mov_b32 v246,0
v_mov_b32 v247,0
v_mov_b32 v252,0
v_mov_b32 v253,0
v_mov_b32 v254,0
v_mov_b32 v255,0
v_accvgpr_write_b32 a32,0
v_accvgpr_write_b32 a33,0
v_accvgpr_write_b32 a34,0
v_accvgpr_write_b32 a35,0
v_accvgpr_write_b32 a36,0
v_accvgpr_write_b32 a37,0
v_accvgpr_write_b32 a38,0
v_accvgpr_write_b32 a39,0
v_accvgpr_write_b32 a40,0
v_accvgpr_write_b32 a41,0
v_accvgpr_write_b32 a42,0
v_accvgpr_write_b32 a43,0
v_accvgpr_write_b32 a44,0
v_accvgpr_write_b32 a45,0
v_accvgpr_write_b32 a46,0
v_accvgpr_write_b32 a47,0
v_accvgpr_write_b32 a48,0
v_accvgpr_write_b32 a49,0
v_accvgpr_write_b32 a50,0
v_accvgpr_write_b32 a51,0
v_accvgpr_write_b32 a52,0
v_accvgpr_write_b32 a53,0
v_accvgpr_write_b32 a54,0
v_accvgpr_write_b32 a55,0
v_accvgpr_write_b32 a56,0
v_accvgpr_write_b32 a57,0
v_accvgpr_write_b32 a58,0
v_accvgpr_write_b32 a59,0
v_accvgpr_write_b32 a60,0
v_accvgpr_write_b32 a61,0
v_accvgpr_write_b32 a62,0
v_accvgpr_write_b32 a63,0
s_mov_b32 s65,262144
s_mov_b32 s66,32
L_s21_%=:
s_add_u32 s67,s65,4096
buffer_load_dwordx4 a[128:131],v8,s[20:23],s65 offen
buffer_load_dwordx4 a[132:135],v8,s[20:23],s65 offen offset:64
buffer_load_dwordx4 a[136:139],v8,s[20:23],s65 offen offset:128
buffer_load_dwordx4 a[140:143],v8,s[20:23],s65 offen offset:192
buffer_load_dwordx4 a[144:147],v8,s[20:23],s67 offen
buffer_load_dwordx4 a[148:151],v8,s[20:23],s67 offen offset:64
buffer_load_dwordx4 a[152:155],v8,s[20:23],s67 offen offset:128
buffer_load_dwordx4 a[156:159],v8,s[20:23],s67 offen offset:192
s_waitcnt vmcnt(0)
v_mfma_f32_16x16x32_bf16 v[64:67],a[128:131],v[128:131],v[32:35]
v_mfma_f32_16x16x32_bf16 v[64:67],a[132:135],v[136:139],v[64:67]
v_mfma_f32_16x16x32_bf16 v[64:67],a[136:139],v[144:147],v[64:67]
v_mfma_f32_16x16x32_bf16 v[64:67],a[140:143],v[152:155],v[64:67]
v_mfma_f32_16x16x32_bf16 v[68:71],a[144:147],v[128:131],v[32:35]
v_mfma_f32_16x16x32_bf16 v[68:71],a[148:151],v[136:139],v[68:71]
v_mfma_f32_16x16x32_bf16 v[68:71],a[152:155],v[144:147],v[68:71]
v_mfma_f32_16x16x32_bf16 v[68:71],a[156:159],v[152:155],v[68:71]
v_mfma_f32_16x16x32_bf16 v[80:83],a[128:131],v[160:163],v[36:39]
v_mfma_f32_16x16x32_bf16 v[80:83],a[132:135],v[168:171],v[80:83]
v_mfma_f32_16x16x32_bf16 v[80:83],a[136:139],v[176:179],v[80:83]
v_mfma_f32_16x16x32_bf16 v[80:83],a[140:143],v[184:187],v[80:83]
v_mfma_f32_16x16x32_bf16 v[84:87],a[144:147],v[160:163],v[36:39]
v_mfma_f32_16x16x32_bf16 v[84:87],a[148:151],v[168:171],v[84:87]
v_mfma_f32_16x16x32_bf16 v[84:87],a[152:155],v[176:179],v[84:87]
v_mfma_f32_16x16x32_bf16 v[84:87],a[156:159],v[184:187],v[84:87]
v_mfma_f32_16x16x32_bf16 v[96:99],a[128:131],v[192:195],v[40:43]
v_mfma_f32_16x16x32_bf16 v[96:99],a[132:135],v[200:203],v[96:99]
v_mfma_f32_16x16x32_bf16 v[96:99],a[136:139],v[208:211],v[96:99]
v_mfma_f32_16x16x32_bf16 v[96:99],a[140:143],v[216:219],v[96:99]
v_mfma_f32_16x16x32_bf16 v[100:103],a[144:147],v[192:195],v[40:43]
v_mfma_f32_16x16x32_bf16 v[100:103],a[148:151],v[200:203],v[100:103]
v_mfma_f32_16x16x32_bf16 v[100:103],a[152:155],v[208:211],v[100:103]
v_mfma_f32_16x16x32_bf16 v[100:103],a[156:159],v[216:219],v[100:103]
v_mfma_f32_16x16x32_bf16 v[112:115],a[128:131],v[224:227],v[44:47]
v_mfma_f32_16x16x32_bf16 v[112:115],a[132:135],v[232:235],v[112:115]
v_mfma_f32_16x16x32_bf16 v[112:115],a[136:139],v[240:243],v[112:115]
v_mfma_f32_16x16x32_bf16 v[112:115],a[140:143],v[248:251],v[112:115]
v_mfma_f32_16x16x32_bf16 v[116:119],a[144:147],v[224:227],v[44:47]
v_mfma_f32_16x16x32_bf16 v[116:119],a[148:151],v[232:235],v[116:119]
v_mfma_f32_16x16x32_bf16 v[116:119],a[152:155],v[240:243],v[116:119]
v_mfma_f32_16x16x32_bf16 v[116:119],a[156:159],v[248:251],v[116:119]
s_nop 7
v_exp_f32 v64,v64
v_exp_f32 v65,v65
v_exp_f32 v66,v66
v_exp_f32 v67,v67
v_exp_f32 v68,v68
v_exp_f32 v69,v69
v_exp_f32 v70,v70
v_exp_f32 v71,v71
s_nop 0
v_cvt_pk_bf16_f32 v64,v64,v65
v_cvt_pk_bf16_f32 v65,v66,v67
v_cvt_pk_bf16_f32 v66,v68,v69
v_cvt_pk_bf16_f32 v67,v70,v71
v_exp_f32 v80,v80
v_exp_f32 v81,v81
v_exp_f32 v82,v82
v_exp_f32 v83,v83
v_exp_f32 v84,v84
v_exp_f32 v85,v85
v_exp_f32 v86,v86
v_exp_f32 v87,v87
s_nop 0
v_cvt_pk_bf16_f32 v80,v80,v81
v_cvt_pk_bf16_f32 v81,v82,v83
v_cvt_pk_bf16_f32 v82,v84,v85
v_cvt_pk_bf16_f32 v83,v86,v87
v_exp_f32 v96,v96
v_exp_f32 v97,v97
v_exp_f32 v98,v98
v_exp_f32 v99,v99
v_exp_f32 v100,v100
v_exp_f32 v101,v101
v_exp_f32 v102,v102
v_exp_f32 v103,v103
s_nop 0
v_cvt_pk_bf16_f32 v96,v96,v97
v_cvt_pk_bf16_f32 v97,v98,v99
v_cvt_pk_bf16_f32 v98,v100,v101
v_cvt_pk_bf16_f32 v99,v102,v103
v_exp_f32 v112,v112
v_exp_f32 v113,v113
v_exp_f32 v114,v114
v_exp_f32 v115,v115
v_exp_f32 v116,v116
v_exp_f32 v117,v117
v_exp_f32 v118,v118
v_exp_f32 v119,v119
s_nop 0
v_cvt_pk_bf16_f32 v112,v112,v113
v_cvt_pk_bf16_f32 v113,v114,v115
v_cvt_pk_bf16_f32 v114,v116,v117
v_cvt_pk_bf16_f32 v115,v118,v119
buffer_load_ushort v72,v9,s[24:27],s65 offen
buffer_load_ushort v73,v9,s[24:27],s65 offen offset:256
buffer_load_ushort v74,v9,s[24:27],s65 offen offset:512
buffer_load_ushort v75,v9,s[24:27],s65 offen offset:768
buffer_load_ushort v76,v10,s[24:27],s65 offen
buffer_load_ushort v77,v10,s[24:27],s65 offen offset:256
buffer_load_ushort v78,v10,s[24:27],s65 offen offset:512
buffer_load_ushort v79,v10,s[24:27],s65 offen offset:768
s_waitcnt vmcnt(0)
v_lshl_or_b32 v88,v73,16,v72
v_lshl_or_b32 v89,v75,16,v74
v_lshl_or_b32 v90,v77,16,v76
v_lshl_or_b32 v91,v79,16,v78
s_nop 1
v_mfma_f32_16x16x32_bf16 a[0:3],v[88:91],v[64:67],a[0:3]
v_mfma_f32_16x16x32_bf16 v[132:135],v[88:91],v[80:83],v[132:135]
v_mfma_f32_16x16x32_bf16 v[196:199],v[88:91],v[96:99],v[196:199]
v_mfma_f32_16x16x32_bf16 a[32:35],v[88:91],v[112:115],a[32:35]
buffer_load_ushort v72,v9,s[24:27],s65 offen offset:32
buffer_load_ushort v73,v9,s[24:27],s65 offen offset:288
buffer_load_ushort v74,v9,s[24:27],s65 offen offset:544
buffer_load_ushort v75,v9,s[24:27],s65 offen offset:800
buffer_load_ushort v76,v10,s[24:27],s65 offen offset:32
buffer_load_ushort v77,v10,s[24:27],s65 offen offset:288
buffer_load_ushort v78,v10,s[24:27],s65 offen offset:544
buffer_load_ushort v79,v10,s[24:27],s65 offen offset:800
s_waitcnt vmcnt(0)
v_lshl_or_b32 v88,v73,16,v72
v_lshl_or_b32 v89,v75,16,v74
v_lshl_or_b32 v90,v77,16,v76
v_lshl_or_b32 v91,v79,16,v78
s_nop 1
v_mfma_f32_16x16x32_bf16 a[4:7],v[88:91],v[64:67],a[4:7]
v_mfma_f32_16x16x32_bf16 v[140:143],v[88:91],v[80:83],v[140:143]
v_mfma_f32_16x16x32_bf16 v[204:207],v[88:91],v[96:99],v[204:207]
v_mfma_f32_16x16x32_bf16 a[36:39],v[88:91],v[112:115],a[36:39]
buffer_load_ushort v72,v9,s[24:27],s65 offen offset:64
buffer_load_ushort v73,v9,s[24:27],s65 offen offset:320
buffer_load_ushort v74,v9,s[24:27],s65 offen offset:576
buffer_load_ushort v75,v9,s[24:27],s65 offen offset:832
buffer_load_ushort v76,v10,s[24:27],s65 offen offset:64
buffer_load_ushort v77,v10,s[24:27],s65 offen offset:320
buffer_load_ushort v78,v10,s[24:27],s65 offen offset:576
buffer_load_ushort v79,v10,s[24:27],s65 offen offset:832
s_waitcnt vmcnt(0)
v_lshl_or_b32 v88,v73,16,v72
v_lshl_or_b32 v89,v75,16,v74
v_lshl_or_b32 v90,v77,16,v76
v_lshl_or_b32 v91,v79,16,v78
s_nop 1
v_mfma_f32_16x16x32_bf16 a[8:11],v[88:91],v[64:67],a[8:11]
v_mfma_f32_16x16x32_bf16 v[148:151],v[88:91],v[80:83],v[148:151]
v_mfma_f32_16x16x32_bf16 v[212:215],v[88:91],v[96:99],v[212:215]
v_mfma_f32_16x16x32_bf16 a[40:43],v[88:91],v[112:115],a[40:43]
buffer_load_ushort v72,v9,s[24:27],s65 offen offset:96
buffer_load_ushort v73,v9,s[24:27],s65 offen offset:352
buffer_load_ushort v74,v9,s[24:27],s65 offen offset:608
buffer_load_ushort v75,v9,s[24:27],s65 offen offset:864
buffer_load_ushort v76,v10,s[24:27],s65 offen offset:96
buffer_load_ushort v77,v10,s[24:27],s65 offen offset:352
buffer_load_ushort v78,v10,s[24:27],s65 offen offset:608
buffer_load_ushort v79,v10,s[24:27],s65 offen offset:864
s_waitcnt vmcnt(0)
v_lshl_or_b32 v88,v73,16,v72
v_lshl_or_b32 v89,v75,16,v74
v_lshl_or_b32 v90,v77,16,v76
v_lshl_or_b32 v91,v79,16,v78
s_nop 1
v_mfma_f32_16x16x32_bf16 a[12:15],v[88:91],v[64:67],a[12:15]
v_mfma_f32_16x16x32_bf16 v[156:159],v[88:91],v[80:83],v[156:159]
v_mfma_f32_16x16x32_bf16 v[220:223],v[88:91],v[96:99],v[220:223]
v_mfma_f32_16x16x32_bf16 a[44:47],v[88:91],v[112:115],a[44:47]
buffer_load_ushort v72,v9,s[24:27],s65 offen offset:128
buffer_load_ushort v73,v9,s[24:27],s65 offen offset:384
buffer_load_ushort v74,v9,s[24:27],s65 offen offset:640
buffer_load_ushort v75,v9,s[24:27],s65 offen offset:896
buffer_load_ushort v76,v10,s[24:27],s65 offen offset:128
buffer_load_ushort v77,v10,s[24:27],s65 offen offset:384
buffer_load_ushort v78,v10,s[24:27],s65 offen offset:640
buffer_load_ushort v79,v10,s[24:27],s65 offen offset:896
s_waitcnt vmcnt(0)
v_lshl_or_b32 v88,v73,16,v72
v_lshl_or_b32 v89,v75,16,v74
v_lshl_or_b32 v90,v77,16,v76
v_lshl_or_b32 v91,v79,16,v78
s_nop 1
v_mfma_f32_16x16x32_bf16 a[16:19],v[88:91],v[64:67],a[16:19]
v_mfma_f32_16x16x32_bf16 v[164:167],v[88:91],v[80:83],v[164:167]
v_mfma_f32_16x16x32_bf16 v[228:231],v[88:91],v[96:99],v[228:231]
v_mfma_f32_16x16x32_bf16 a[48:51],v[88:91],v[112:115],a[48:51]
buffer_load_ushort v72,v9,s[24:27],s65 offen offset:160
buffer_load_ushort v73,v9,s[24:27],s65 offen offset:416
buffer_load_ushort v74,v9,s[24:27],s65 offen offset:672
buffer_load_ushort v75,v9,s[24:27],s65 offen offset:928
buffer_load_ushort v76,v10,s[24:27],s65 offen offset:160
buffer_load_ushort v77,v10,s[24:27],s65 offen offset:416
buffer_load_ushort v78,v10,s[24:27],s65 offen offset:672
buffer_load_ushort v79,v10,s[24:27],s65 offen offset:928
s_waitcnt vmcnt(0)
v_lshl_or_b32 v88,v73,16,v72
v_lshl_or_b32 v89,v75,16,v74
v_lshl_or_b32 v90,v77,16,v76
v_lshl_or_b32 v91,v79,16,v78
s_nop 1
v_mfma_f32_16x16x32_bf16 a[20:23],v[88:91],v[64:67],a[20:23]
v_mfma_f32_16x16x32_bf16 v[172:175],v[88:91],v[80:83],v[172:175]
v_mfma_f32_16x16x32_bf16 v[236:239],v[88:91],v[96:99],v[236:239]
v_mfma_f32_16x16x32_bf16 a[52:55],v[88:91],v[112:115],a[52:55]
buffer_load_ushort v72,v9,s[24:27],s65 offen offset:192
buffer_load_ushort v73,v9,s[24:27],s65 offen offset:448
buffer_load_ushort v74,v9,s[24:27],s65 offen offset:704
buffer_load_ushort v75,v9,s[24:27],s65 offen offset:960
buffer_load_ushort v76,v10,s[24:27],s65 offen offset:192
buffer_load_ushort v77,v10,s[24:27],s65 offen offset:448
buffer_load_ushort v78,v10,s[24:27],s65 offen offset:704
buffer_load_ushort v79,v10,s[24:27],s65 offen offset:960
s_waitcnt vmcnt(0)
v_lshl_or_b32 v88,v73,16,v72
v_lshl_or_b32 v89,v75,16,v74
v_lshl_or_b32 v90,v77,16,v76
v_lshl_or_b32 v91,v79,16,v78
s_nop 1
v_mfma_f32_16x16x32_bf16 a[24:27],v[88:91],v[64:67],a[24:27]
v_mfma_f32_16x16x32_bf16 v[180:183],v[88:91],v[80:83],v[180:183]
v_mfma_f32_16x16x32_bf16 v[244:247],v[88:91],v[96:99],v[244:247]
v_mfma_f32_16x16x32_bf16 a[56:59],v[88:91],v[112:115],a[56:59]
buffer_load_ushort v72,v9,s[24:27],s65 offen offset:224
buffer_load_ushort v73,v9,s[24:27],s65 offen offset:480
buffer_load_ushort v74,v9,s[24:27],s65 offen offset:736
buffer_load_ushort v75,v9,s[24:27],s65 offen offset:992
buffer_load_ushort v76,v10,s[24:27],s65 offen offset:224
buffer_load_ushort v77,v10,s[24:27],s65 offen offset:480
buffer_load_ushort v78,v10,s[24:27],s65 offen offset:736
buffer_load_ushort v79,v10,s[24:27],s65 offen offset:992
s_waitcnt vmcnt(0)
v_lshl_or_b32 v88,v73,16,v72
v_lshl_or_b32 v89,v75,16,v74
v_lshl_or_b32 v90,v77,16,v76
v_lshl_or_b32 v91,v79,16,v78
s_nop 1
v_mfma_f32_16x16x32_bf16 a[28:31],v[88:91],v[64:67],a[28:31]
v_mfma_f32_16x16x32_bf16 v[188:191],v[88:91],v[80:83],v[188:191]
)A"
R"A(v_mfma_f32_16x16x32_bf16 v[252:255],v[88:91],v[96:99],v[252:255]
v_mfma_f32_16x16x32_bf16 a[60:63],v[88:91],v[112:115],a[60:63]
v_mfma_f32_16x16x32_bf16 v[48:51],v[28:31],v[64:67],v[48:51]
v_mfma_f32_16x16x32_bf16 v[52:55],v[28:31],v[80:83],v[52:55]
v_mfma_f32_16x16x32_bf16 v[56:59],v[28:31],v[96:99],v[56:59]
v_mfma_f32_16x16x32_bf16 v[60:63],v[28:31],v[112:115],v[60:63]
s_add_u32 s65,s65,8192
s_sub_u32 s66,s66,1
s_cmp_lg_u32 s66,0
s_cbranch_scc1 L_s21_%=
s_nop 7
v_max_f32 v49,0x00800000,v48
v_rcp_f32 v49,v49
v_max_f32 v53,0x00800000,v52
v_rcp_f32 v53,v53
v_max_f32 v57,0x00800000,v56
v_rcp_f32 v57,v57
v_max_f32 v61,0x00800000,v60
v_rcp_f32 v61,v61
v_accvgpr_read_b32 v16,a0
v_accvgpr_read_b32 v17,a1
v_accvgpr_read_b32 v18,a2
v_accvgpr_read_b32 v19,a3
v_accvgpr_read_b32 v20,a4
v_accvgpr_read_b32 v21,a5
v_accvgpr_read_b32 v22,a6
v_accvgpr_read_b32 v23,a7
v_mul_f32 v16,v16,v49
v_mul_f32 v17,v17,v49
v_mul_f32 v18,v18,v49
v_mul_f32 v19,v19,v49
v_mul_f32 v20,v20,v49
v_mul_f32 v21,v21,v49
v_mul_f32 v22,v22,v49
v_mul_f32 v23,v23,v49
v_cvt_pk_bf16_f32 v32,v16,v17
v_cvt_pk_bf16_f32 v33,v18,v19
v_cvt_pk_bf16_f32 v34,v20,v21
v_cvt_pk_bf16_f32 v35,v22,v23
v_accvgpr_read_b32 v8,a8
v_accvgpr_read_b32 v9,a9
v_accvgpr_read_b32 v10,a10
v_accvgpr_read_b32 v11,a11
v_accvgpr_read_b32 v12,a12
v_accvgpr_read_b32 v13,a13
v_accvgpr_read_b32 v14,a14
v_accvgpr_read_b32 v15,a15
v_mul_f32 v8,v8,v49
v_mul_f32 v9,v9,v49
v_mul_f32 v10,v10,v49
v_mul_f32 v11,v11,v49
v_mul_f32 v12,v12,v49
v_mul_f32 v13,v13,v49
v_mul_f32 v14,v14,v49
v_mul_f32 v15,v15,v49
v_cvt_pk_bf16_f32 v36,v8,v9
v_cvt_pk_bf16_f32 v37,v10,v11
v_cvt_pk_bf16_f32 v38,v12,v13
v_cvt_pk_bf16_f32 v39,v14,v15
v_accvgpr_read_b32 v16,a16
v_accvgpr_read_b32 v17,a17
v_accvgpr_read_b32 v18,a18
v_accvgpr_read_b32 v19,a19
v_accvgpr_read_b32 v20,a20
v_accvgpr_read_b32 v21,a21
v_accvgpr_read_b32 v22,a22
v_accvgpr_read_b32 v23,a23
v_mul_f32 v16,v16,v49
v_mul_f32 v17,v17,v49
v_mul_f32 v18,v18,v49
v_mul_f32 v19,v19,v49
v_mul_f32 v20,v20,v49
v_mul_f32 v21,v21,v49
v_mul_f32 v22,v22,v49
v_mul_f32 v23,v23,v49
v_cvt_pk_bf16_f32 v40,v16,v17
v_cvt_pk_bf16_f32 v41,v18,v19
v_cvt_pk_bf16_f32 v42,v20,v21
v_cvt_pk_bf16_f32 v43,v22,v23
ds_read_b64_tr_b16 v[64:65],v1
ds_read_b64_tr_b16 v[66:67],v1 offset:5120
ds_read_b64_tr_b16 v[68:69],v1 offset:10240
ds_read_b64_tr_b16 v[70:71],v1 offset:15360
ds_read_b64_tr_b16 v[72:73],v1 offset:20480
ds_read_b64_tr_b16 v[74:75],v1 offset:25600
ds_read_b64_tr_b16 v[76:77],v1 offset:30720
ds_read_b64_tr_b16 v[78:79],v1 offset:35840
ds_read_b64_tr_b16 v[80:81],v2 offset:32
ds_read_b64_tr_b16 v[82:83],v2 offset:5152
ds_read_b64_tr_b16 v[84:85],v2 offset:10272
ds_read_b64_tr_b16 v[86:87],v2 offset:15392
ds_read_b64_tr_b16 v[88:89],v2 offset:20512
ds_read_b64_tr_b16 v[90:91],v2 offset:25632
ds_read_b64_tr_b16 v[92:93],v2 offset:30752
ds_read_b64_tr_b16 v[94:95],v2 offset:35872
s_waitcnt vmcnt(0) lgkmcnt(0)
s_nop 7
s_branch L_ok1_%=
L_end_%=:
)A"
    :: "s"(ka), "s"(blk), "s"(wid)
    : "v0", "v1", "v2", "v3", "v4", "v5", "v6", "v7", "v8", "v9", "v10", "v11", "v12", "v13", "v14", "v15", "v16", "v17", "v18", "v19", "v20", "v21", "v22", "v23", "v24", "v25", "v26", "v27", "v28", "v29", "v30", "v31", "v32", "v33", "v34", "v35", "v36", "v37", "v38", "v39", "v40", "v41", "v42", "v43", "v44", "v45", "v46", "v47", "v48", "v49", "v50", "v51", "v52", "v53", "v54", "v55", "v56", "v57", "v58", "v59", "v60", "v61", "v62", "v63", "v64", "v65", "v66", "v67", "v68", "v69", "v70", "v71", "v72", "v73", "v74", "v75", "v76", "v77", "v78", "v79", "v80", "v81", "v82", "v83", "v84", "v85", "v86", "v87", "v88", "v89", "v90", "v91", "v92", "v93", "v94", "v95", "v96", "v97", "v98", "v99", "v100", "v101", "v102", "v103", "v104", "v105", "v106", "v107", "v108", "v109", "v110", "v111", "v112", "v113", "v114", "v115", "v116", "v117", "v118", "v119", "v120", "v121", "v122", "v123", "v124", "v125", "v126", "v127", "v128", "v129", "v130", "v131", "v132", "v133", "v134", "v135", "v136", "v137", "v138", "v139", "v140", "v141", "v142", "v143", "v144", "v145", "v146", "v147", "v148", "v149", "v150", "v151", "v152", "v153", "v154", "v155", "v156", "v157", "v158", "v159", "v160", "v161", "v162", "v163", "v164", "v165", "v166", "v167", "v168", "v169", "v170", "v171", "v172", "v173", "v174", "v175", "v176", "v177", "v178", "v179", "v180", "v181", "v182", "v183", "v184", "v185", "v186", "v187", "v188", "v189", "v190", "v191", "v192", "v193", "v194", "v195", "v196", "v197", "v198", "v199", "v200", "v201", "v202", "v203", "v204", "v205", "v206", "v207", "v208", "v209", "v210", "v211", "v212", "v213", "v214", "v215", "v216", "v217", "v218", "v219", "v220", "v221", "v222", "v223", "v224", "v225", "v226", "v227", "v228", "v229", "v230", "v231", "v232", "v233", "v234", "v235", "v236", "v237", "v238", "v239", "v240", "v241", "v242", "v243", "v244", "v245", "v246", "v247", "v248", "v249", "v250", "v251", "v252", "v253", "v254", "v255", "a0", "a1", "a2", "a3", "a4", "a5", "a6", "a7", "a8", "a9", "a10", "a11", "a12", "a13", "a14", "a15", "a16", "a17", "a18", "a19", "a20", "a21", "a22", "a23", "a24", "a25", "a26", "a27", "a28", "a29", "a30", "a31", "a32", "a33", "a34", "a35", "a36", "a37", "a38", "a39", "a40", "a41", "a42", "a43", "a44", "a45", "a46", "a47", "a48", "a49", "a50", "a51", "a52", "a53", "a54", "a55", "a56", "a57", "a58", "a59", "a60", "a61", "a62", "a63", "a64", "a65", "a66", "a67", "a68", "a69", "a70", "a71", "a72", "a73", "a74", "a75", "a76", "a77", "a78", "a79", "a80", "a81", "a82", "a83", "a84", "a85", "a86", "a87", "a88", "a89", "a90", "a91", "a92", "a93", "a94", "a95", "a96", "a97", "a98", "a99", "a100", "a101", "a102", "a103", "a104", "a105", "a106", "a107", "a108", "a109", "a110", "a111", "a112", "a113", "a114", "a115", "a116", "a117", "a118", "a119", "a120", "a121", "a122", "a123", "a124", "a125", "a126", "a127", "a128", "a129", "a130", "a131", "a132", "a133", "a134", "a135", "a136", "a137", "a138", "a139", "a140", "a141", "a142", "a143", "a144", "a145", "a146", "a147", "a148", "a149", "a150", "a151", "a152", "a153", "a154", "a155", "a156", "a157", "a158", "a159", "a160", "a161", "a162", "a163", "a164", "a165", "a166", "a167", "a168", "a169", "a170", "a171", "a172", "a173", "a174", "a175", "a176", "a177", "a178", "a179", "a180", "a181", "a182", "a183", "a184", "a185", "a186", "a187", "a188", "a189", "a190", "a191", "a192", "a193", "a194", "a195", "a196", "a197", "a198", "a199", "a200", "a201", "a202", "a203", "a204", "a205", "a206", "a207", "a208", "a209", "a210", "a211", "a212", "a213", "a214", "a215", "a216", "a217", "a218", "a219", "a220", "a221", "a222", "a223", "a224", "a225", "a226", "a227", "a228", "a229", "a230", "a231", "a232", "a233", "a234", "a235", "a236", "a237", "a238", "a239", "a240", "a241", "a242", "a243", "a244", "a245", "a246", "a247", "a248", "a249", "a250", "a251", "a252", "a253", "a254", "a255", "s4", "s5", "s6", "s7", "s8", "s9", "s10", "s11", "s12", "s13", "s14", "s15", "s16", "s17", "s18", "s19", "s20", "s21", "s22", "s23", "s24", "s25", "s26", "s27", "s28", "s29", "s30", "s31", "s36", "s37", "s38", "s39", "s40", "s41", "s42", "s43", "s44", "s45", "s46", "s47", "s48", "s49", "s50", "s51", "s52", "s53", "s54", "s55", "s56", "s57", "s58", "s59", "s60", "s61", "s62", "s63", "s64", "s65", "s66", "s67", "s68", "s69", "s70", "s71", "s72", "s73", "s74", "s75", "s76", "s77", "s78", "s79", "s80", "s81", "s82", "s83", "s84", "s85", "s86", "s87", "s88", "s89", "s90", "s91", "s92", "s93", "s94", "s95", "s96", "s97", "s98", "s99", "vcc", "scc", "memory");
}

extern "C" void kernel_launch(void* const* d_in, const int* in_sizes, int n_in,
                              void* d_out, int out_size, void* d_ws, size_t ws_size,
                              hipStream_t stream) {
  (void)hipFuncSetAttribute((const void*)qattn_kernel, hipFuncAttributeMaxDynamicSharedMemorySize, LDS_ATT);
  qattn_kernel<<<256, 256, LDS_ATT, stream>>>(d_in[0], d_in[1], d_in[2], d_in[3], d_ws, d_in[4], d_out);
}
```

```cpp
#include <hip/hip_runtime.h>
#include <hip/hip_bf16.h>

constexpr int LDS_ATT = 163840;

__global__ __launch_bounds__(256, 1) __attribute__((aligned(4096))) void qattn_kernel(const void* __restrict__ x, const void* __restrict__ kc, const void* __restrict__ vc,
                                                       const void* __restrict__ wq, void* __restrict__ ws, const void* __restrict__ wo,
                                                       void* __restrict__ out) {
  const unsigned blk = blockIdx.x;
  const unsigned wid = __builtin_amdgcn_readfirstlane(threadIdx.x >> 6);
  auto ka = __builtin_amdgcn_kernarg_segment_ptr();
  asm volatile(
R"A(s_load_dwordx8 s[4:11],%0,0x0
s_load_dwordx4 s[12:15],%0,0x20
s_load_dwordx2 s[16:17],%0,0x30
v_mbcnt_lo_u32_b32 v23,-1,0
v_mbcnt_hi_u32_b32 v23,-1,v23
s_mov_b32 s60,%2
s_and_b32 s66,%1,7
s_lshr_b32 s67,%1,3
s_getpc_b64 s[20:21]
L_pc_%=:
s_and_b32 s20,s20,0xffffff80
s_and_b32 s21,s21,0xffff
s_mov_b32 s22,L_hot_%=-L_pc_%=
s_mov_b32 s23,0x20000
s_lshl_b32 s68,s67,2
s_add_u32 s68,s68,s60
s_mul_i32 s68,s68,896
v_lshlrev_b32 v67,7,v23
s_mov_b64 exec,127
buffer_load_dword v8,v67,s[20:23],s68 offen
s_mov_b64 exec,-1
s_lshr_b32 s61,s66,2
s_lshl_b32 s61,s61,3
s_lshr_b32 s68,s67,2
s_add_u32 s61,s61,s68
s_and_b32 s62,s66,1
s_lshl_b32 s62,s62,2
s_and_b32 s68,s66,2
s_add_u32 s62,s62,s68
s_lshl_b32 s62,s62,1
s_and_b32 s68,s67,3
s_add_u32 s62,s62,s68
s_lshl_b32 s83,s62,9
s_mov_b32 s84,0
s_lshl_b32 s89,s60,13
s_add_u32 s85,s89,65536
s_add_u32 s88,s89,131072
v_and_b32 v20,3,v23
v_bfe_u32 v21,v23,2,2
v_lshrrev_b32 v22,4,v23
v_lshrrev_b32 v75,3,v23
s_lshl_b32 s66,s60,6
v_add_u32 v75,s66,v75
v_mul_u32_u24 v75,1280,v75
v_and_b32 v76,7,v23
v_or_b32 v77,0,v22
v_xor_b32 v77,v77,v76
v_lshl_add_u32 v77,v77,4,v75
v_add_u32 v32,0,v77
v_add_u32 v34,18432,v77
v_add_u32 v36,40960,v77
v_add_u32 v38,59392,v77
v_or_b32 v77,4,v22
v_xor_b32 v77,v77,v76
v_lshl_add_u32 v77,v77,4,v75
v_add_u32 v33,9216,v77
v_add_u32 v35,27648,v77
v_add_u32 v37,50176,v77
v_add_u32 v39,68608,v77
s_lshl_b32 s66,s60,17
v_lshlrev_b32 v75,13,v21
v_lshl_add_u32 v75,v22,6,v75
v_lshl_add_u32 v75,v20,4,v75
v_add_u32 v75,s66,v75
v_add_u32 v40,0,v75
v_add_u32 v41,256,v75
v_add_u32 v42,32768,v75
v_add_u32 v43,33024,v75
v_add_u32 v44,65536,v75
v_add_u32 v45,65792,v75
v_add_u32 v46,98304,v75
v_add_u32 v47,98560,v75
s_mul_i32 s66,s61,307200
s_waitcnt lgkmcnt(0)
s_add_u32 s28,s4,s66
s_addc_u32 s29,s5,0
s_and_b32 s29,s29,0xffff
s_mov_b32 s30,307200
s_mov_b32 s31,0x20000
s_mov_b32 s36,s10
s_and_b32 s37,s11,0xffff
s_mov_b32 s38,5242880
s_mov_b32 s39,0x20000
s_add_u32 m0,s85,0
s_nop 0
buffer_load_dwordx4 v32,s[28:31],s84 offen lds
buffer_load_dwordx4 v33,s[28:31],s84 offen offset:1024 lds
buffer_load_dwordx4 v34,s[28:31],s84 offen offset:2048 lds
buffer_load_dwordx4 v35,s[28:31],s84 offen offset:3072 lds
s_add_u32 m0,s85,4096
s_nop 0
buffer_load_dwordx4 v36,s[28:31],s84 offen lds
buffer_load_dwordx4 v37,s[28:31],s84 offen offset:1024 lds
buffer_load_dwordx4 v38,s[28:31],s84 offen offset:2048 lds
buffer_load_dwordx4 v39,s[28:31],s84 offen offset:3072 lds
s_add_u32 s84,s84,128
s_xor_b32 s85,s85,98304
s_add_u32 m0,s88,0
s_nop 0
buffer_load_dwordx4 v40,s[36:39],s83 offen lds
s_add_u32 m0,s88,2048
s_nop 0
buffer_load_dwordx4 v42,s[36:39],s83 offen lds
s_add_u32 m0,s88,4096
s_nop 0
buffer_load_dwordx4 v44,s[36:39],s83 offen lds
s_add_u32 m0,s88,6144
s_nop 0
buffer_load_dwordx4 v46,s[36:39],s83 offen lds
s_lshl_b32 s67,s61,21
s_add_u32 s68,s67,101711872
s_add_u32 s44,s12,s68
s_addc_u32 s45,s13,0
s_and_b32 s45,s45,0xffff
s_mov_b32 s46,4096
s_mov_b32 s47,0x20000
v_mov_b32 v10,0
buffer_load_dword v9,v10,s[44:47],0 offen sc1
v_lshlrev_b32 v6,4,v23
v_lshlrev_b32 v72,12,v22
v_lshl_add_u32 v72,v21,6,v72
v_lshl_add_u32 v72,v20,4,v72
v_subrev_u32 v51,s89,v72
v_add_u32 v50,131072,v72
v_and_b32 v74,15,v23
v_lshrrev_b32 v76,1,v74
v_lshlrev_b32 v75,7,v74
s_add_u32 s66,s89,65536
v_add_u32 v75,s66,v75
v_or_b32 v77,0,v22
v_xor_b32 v77,v77,v76
v_lshl_add_u32 v48,v77,4,v75
v_or_b32 v77,4,v22
v_xor_b32 v77,v77,v76
v_lshl_add_u32 v49,v77,4,v75
s_add_u32 s86,s89,131072
s_add_u32 s87,s89,98304
s_mov_b64 exec,0
s_cmp_eq_u32 s60,0
s_cbranch_scc1 L_pd1_%=
s_cmp_eq_u32 s60,1
s_cbranch_scc1 L_pd2_%=
s_cmp_eq_u32 s60,2
s_cbranch_scc1 L_pd3_%=
s_branch L_pd4_%=
L_trt0_%=:
s_mov_b64 exec,-1
s_add_u32 m0,s88,1024
s_nop 0
buffer_load_dwordx4 v41,s[36:39],s83 offen lds
s_add_u32 m0,s88,3072
s_nop 0
buffer_load_dwordx4 v43,s[36:39],s83 offen lds
s_add_u32 m0,s88,5120
s_nop 0
buffer_load_dwordx4 v45,s[36:39],s83 offen lds
s_add_u32 m0,s88,7168
s_nop 0
buffer_load_dwordx4 v47,s[36:39],s83 offen lds
s_add_u32 s83,s83,524288
s_mov_b32 s88,s87
s_add_u32 m0,s88,0
s_nop 0
buffer_load_dwordx4 v40,s[36:39],s83 offen lds
s_add_u32 m0,s88,1024
s_nop 0
buffer_load_dwordx4 v41,s[36:39],s83 offen lds
s_add_u32 m0,s88,2048
s_nop 0
buffer_load_dwordx4 v42,s[36:39],s83 offen lds
s_add_u32 m0,s88,3072
s_nop 0
buffer_load_dwordx4 v43,s[36:39],s83 offen lds
s_add_u32 m0,s88,4096
s_nop 0
buffer_load_dwordx4 v44,s[36:39],s83 offen lds
s_add_u32 m0,s88,5120
s_nop 0
buffer_load_dwordx4 v45,s[36:39],s83 offen lds
s_add_u32 m0,s88,6144
s_nop 0
buffer_load_dwordx4 v46,s[36:39],s83 offen lds
s_add_u32 m0,s88,7168
s_nop 0
buffer_load_dwordx4 v47,s[36:39],s83 offen lds
s_add_u32 s83,s83,524288
s_add_u32 s88,s89,0
s_mov_b64 exec,0
s_cmp_eq_u32 s60,0
s_cbranch_scc1 L_pd9_%=
s_cmp_eq_u32 s60,1
s_cbranch_scc1 L_pd10_%=
s_cmp_eq_u32 s60,2
s_cbranch_scc1 L_pd11_%=
s_branch L_pd12_%=
L_t1b_%=:
s_cmp_ge_u32 s62,8
s_cbranch_scc1 L_pd19_%=
s_branch L_pd14_%=
L_t1c_%=:
s_cmp_ge_u32 s62,8
s_cbranch_scc1 L_pd20_%=
s_branch L_pd15_%=
s_nop 0
s_nop 0
s_nop 0
s_nop 0
s_nop 0
s_nop 0
L_trt1_%=:
s_mov_b64 exec,-1
s_branch L_pacx_%=
L_pace_%=:
s_cmp_ge_u32 s4,8
s_cselect_b32 s5,3,1
s_sub_u32 s5,s4,s5
s_max_i32 s5,s5,s7
L_pacl_%=:
s_sub_u32 s4,s4,1
s_cmp_eq_u32 s4,0
s_cbranch_scc1 L_pace2_%=
s_nop 2
s_branch L_pacl_%=
L_pace2_%=:
s_setpc_b64 s[92:93]
L_pacx_%=:
s_waitcnt vmcnt(17)
ds_read_b128 a[192:195],v48
ds_read_b128 a[196:199],v49
ds_read_b128 a[200:203],v48 offset:2048
ds_read_b128 a[204:207],v49 offset:2048
ds_read_b128 a[208:211],v48 offset:4096
ds_read_b128 a[212:215],v49 offset:4096
ds_read_b128 a[216:219],v48 offset:6144
ds_read_b128 a[220:223],v49 offset:6144
s_waitcnt vmcnt(13)
s_barrier
ds_read_b64_tr_b16 a[64:65],v50
ds_read_b64_tr_b16 a[66:67],v50 offset:2048
ds_read_b64_tr_b16 a[68:69],v50 offset:16384
ds_read_b64_tr_b16 a[70:71],v50 offset:18432
s_waitcnt lgkmcnt(2)
v_mfma_f32_16x16x32_bf16 a[0:3],a[64:67],a[192:195],0
s_add_u32 m0,s85,0
ds_read_b64_tr_b16 a[72:73],v50 offset:8
v_xor_b32 v48,98304,v48
s_waitcnt lgkmcnt(1)
v_mfma_f32_16x16x32_bf16 a[0:3],a[68:71],a[196:199],a[0:3]
buffer_load_dwordx4 v32,s[28:31],s84 offen lds
v_mfma_f32_16x16x32_bf16 a[16:19],a[68:71],a[204:207],0
s_mov_b32 s69,s86
ds_read_b64_tr_b16 a[74:75],v50 offset:2056
v_xor_b32 v49,98304,v49
v_mfma_f32_16x16x32_bf16 a[16:19],a[64:67],a[200:203],a[16:19]
s_mov_b32 s86,s87
s_mov_b32 s87,s88
ds_read_b64_tr_b16 a[76:77],v50 offset:16392
v_mfma_f32_16x16x32_bf16 a[32:35],a[64:67],a[208:211],0
ds_read_b64_tr_b16 a[78:79],v50 offset:18440
v_mfma_f32_16x16x32_bf16 a[32:35],a[68:71],a[212:215],a[32:35]
ds_read_b64_tr_b16 a[80:81],v50 offset:256
v_mfma_f32_16x16x32_bf16 a[48:51],a[68:71],a[220:223],0
buffer_load_dwordx4 v33,s[28:31],s84 offen offset:1024 lds
v_mfma_f32_16x16x32_bf16 a[48:51],a[64:67],a[216:219],a[48:51]
s_waitcnt vmcnt(14) lgkmcnt(1)
ds_read_b64_tr_b16 a[82:83],v50 offset:2304
v_mfma_f32_16x16x32_bf16 a[4:7],a[72:75],a[192:195],0
s_lshl_b32 s65,s61,5
ds_read_b64_tr_b16 a[84:85],v50 offset:16640
v_cmp_lt_u32 vcc,6,v23
v_mfma_f32_16x16x32_bf16 a[4:7],a[76:79],a[196:199],a[4:7]
s_lshl_b32 s66,s62,1
ds_read_b64_tr_b16 a[86:87],v50 offset:18688
v_mov_b32 v17,0
v_mfma_f32_16x16x32_bf16 a[20:23],a[76:79],a[204:207],0
s_add_u32 s65,s65,s66
s_lshl_b32 s66,s65,18
v_readfirstlane_b32 s52,v9
v_mfma_f32_16x16x32_bf16 a[20:23],a[72:75],a[200:203],a[20:23]
buffer_load_dwordx4 v34,s[28:31],s84 offen offset:2048 lds
v_mfma_f32_16x16x32_bf16 a[36:39],a[72:75],a[208:211],0
s_add_u32 s20,s6,s66
s_addc_u32 s21,s7,0
ds_read_b64_tr_b16 a[88:89],v50 offset:264
v_mfma_f32_16x16x32_bf16 a[36:39],a[76:79],a[212:215],a[36:39]
s_and_b32 s21,s21,0xffff
s_mov_b32 s22,524288
ds_read_b64_tr_b16 a[90:91],v50 offset:2312
v_mfma_f32_16x16x32_bf16 a[52:55],a[76:79],a[220:223],0
s_mov_b32 s23,0x20000
s_mov_b32 s26,524288
ds_read_b64_tr_b16 a[92:93],v50 offset:16648
v_mfma_f32_16x16x32_bf16 a[52:55],a[72:75],a[216:219],a[52:55]
s_add_u32 s24,s8,s66
s_addc_u32 s25,s9,0
ds_read_b64_tr_b16 a[94:95],v50 offset:18696
s_waitcnt lgkmcnt(6)
v_mfma_f32_16x16x32_bf16 a[8:11],a[80:83],a[192:195],0
buffer_load_dwordx4 v35,s[28:31],s84 offen offset:3072 lds
s_waitcnt lgkmcnt(4)
v_mfma_f32_16x16x32_bf16 a[8:11],a[84:87],a[196:199],a[8:11]
s_and_b32 s25,s25,0xffff
s_mov_b32 s27,0x20000
ds_read_b64_tr_b16 a[96:97],v50 offset:512
v_mfma_f32_16x16x32_bf16 a[24:27],a[84:87],a[204:207],0
s_lshl_b32 s66,s65,16
s_mov_b32 s42,131072
ds_read_b64_tr_b16 a[98:99],v50 offset:2560
v_mfma_f32_16x16x32_bf16 a[24:27],a[80:83],a[200:203],a[24:27]
s_add_u32 m0,s85,4096
s_xor_b32 s85,s85,98304
ds_read_b64_tr_b16 a[100:101],v50 offset:16896
v_mfma_f32_16x16x32_bf16 a[40:43],a[80:83],a[208:211],0
s_add_u32 s40,s12,s66
s_addc_u32 s41,s13,0
s_and_b32 s41,s41,0xffff
v_mfma_f32_16x16x32_bf16 a[40:43],a[84:87],a[212:215],a[40:43]
buffer_load_dwordx4 v36,s[28:31],s84 offen lds
v_mfma_f32_16x16x32_bf16 a[56:59],a[84:87],a[220:223],0
s_mov_b32 s43,0x20000
s_mov_b32 s48,s14
ds_read_b64_tr_b16 a[102:103],v50 offset:18944
v_mfma_f32_16x16x32_bf16 a[56:59],a[80:83],a[216:219],a[56:59]
s_and_b32 s49,s15,0xffff
s_mov_b32 s50,5242880
ds_read_b64_tr_b16 a[104:105],v50 offset:520
s_waitcnt lgkmcnt(5)
v_mfma_f32_16x16x32_bf16 a[12:15],a[88:91],a[192:195],0
s_mov_b32 s51,0x20000
s_lshl_b32 s67,s60,12
ds_read_b64_tr_b16 a[106:107],v50 offset:2568
v_mfma_f32_16x16x32_bf16 a[12:15],a[92:95],a[196:199],a[12:15]
s_add_u32 s76,s67,98304
s_mul_i32 s74,s61,5
ds_read_b64_tr_b16 a[108:109],v50 offset:16904
v_mfma_f32_16x16x32_bf16 a[28:31],a[92:95],a[204:207],0
buffer_load_dwordx4 v37,s[28:31],s84 offen offset:1024 lds
v_mfma_f32_16x16x32_bf16 a[28:31],a[88:91],a[200:203],a[28:31]
s_add_u32 s74,s74,s62
s_and_b32 s74,s74,15
ds_read_b64_tr_b16 a[110:111],v50 offset:18952
v_mfma_f32_16x16x32_bf16 a[44:47],a[88:91],a[208:211],0
s_lshl_b32 s74,s74,14
s_mov_b32 s75,0
ds_read_b64_tr_b16 a[112:113],v50 offset:768
v_mfma_f32_16x16x32_bf16 a[44:47],a[92:95],a[212:215],a[44:47]
s_mov_b32 s77,98304
s_mov_b32 s71,0x3e0293ee
ds_read_b64_tr_b16 a[114:115],v50 offset:2816
v_mfma_f32_16x16x32_bf16 a[60:63],a[92:95],a[220:223],0
s_mov_b32 s73,0x53800000
s_lshl_b32 s79,s60,14
s_add_u32 s80,s79,4096
v_mfma_f32_16x16x32_bf16 a[60:63],a[88:91],a[216:219],a[60:63]
buffer_load_dwordx4 v38,s[28:31],s84 offen offset:2048 lds
s_waitcnt lgkmcnt(5)
v_mfma_f32_16x16x32_bf16 v[64:67],a[96:99],a[192:195],0
s_add_u32 s81,s79,8192
s_add_u32 s82,s79,12288
ds_read_b64_tr_b16 a[116:117],v50 offset:17152
v_mfma_f32_16x16x32_bf16 v[64:67],a[100:103],a[196:199],v[64:67]
s_lshr_b32 s63,s62,2
s_and_b32 s64,s62,3
ds_read_b64_tr_b16 a[118:119],v50 offset:19200
v_mfma_f32_16x16x32_bf16 v[80:83],a[100:103],a[204:207],0
s_mov_b32 s72,0xff800000
s_lshl_b32 s18,s60,24
ds_read_b64_tr_b16 a[120:121],v50 offset:776
v_mfma_f32_16x16x32_bf16 v[80:83],a[96:99],a[200:203],v[80:83]
s_lshl_b32 s68,s62,6
s_add_u32 s18,s18,s68
ds_read_b64_tr_b16 a[122:123],v50 offset:2824
v_mfma_f32_16x16x32_bf16 v[96:99],a[96:99],a[208:211],0
buffer_load_dwordx4 v39,s[28:31],s84 offen offset:3072 lds
v_mfma_f32_16x16x32_bf16 v[96:99],a[100:103],a[212:215],v[96:99]
s_add_u32 s84,s84,128
ds_read_b64_tr_b16 a[124:125],v50 offset:17160
v_lshlrev_b32 v19,3,v6
v_mfma_f32_16x16x32_bf16 v[112:115],a[100:103],a[220:223],0
s_cmp_eq_u32 s60,3
s_cselect_b32 s72,0x7149f2ca,s72
ds_read_b64_tr_b16 a[126:127],v50 offset:19208
v_mfma_f32_16x16x32_bf16 v[112:115],a[96:99],a[216:219],v[112:115]
s_add_u32 m0,s88,0
v_cndmask_b32_e64 v19,v19,-1,vcc
v_lshrrev_b32 v18,1,v19
s_waitcnt lgkmcnt(6)
v_mfma_f32_16x16x32_bf16 v[68:71],a[104:107],a[192:195],0
s_lshl_b32 s90,%1,2
s_add_u32 s90,s90,s60
v_min_u32 v19,v19,v19
v_mfma_f32_16x16x32_bf16 v[68:71],a[108:111],a[196:199],v[68:71]
buffer_load_dwordx4 v40,s[36:39],s83 offen lds
v_mfma_f32_16x16x32_bf16 v[84:87],a[108:111],a[204:207],0
s_mul_i32 s90,s90,5120
s_mul_i32 s58,s61,307200
v_cndmask_b32_e64 v19,v19,v18,vcc
v_mfma_f32_16x16x32_bf16 v[84:87],a[104:107],a[200:203],v[84:87]
s_add_u32 s56,s40,52224
s_addc_u32 s57,s41,0
s_add_u32 s52,s52,1
v_mfma_f32_16x16x32_bf16 v[100:103],a[104:107],a[208:211],0
)A"
R"A(s_add_u32 m0,s88,1024
s_add_u32 s58,s16,s58
s_addc_u32 s59,s17,0
v_mfma_f32_16x16x32_bf16 v[100:103],a[108:111],a[212:215],v[100:103]
s_cmp_eq_u32 s60,1
s_cselect_b64 s[56:57],s[58:59],s[56:57]
s_xor_b32 s53,s52,0x9e3779b9
v_mfma_f32_16x16x32_bf16 v[116:119],a[108:111],a[220:223],0
buffer_load_dwordx4 v41,s[36:39],s83 offen lds
v_mfma_f32_16x16x32_bf16 v[116:119],a[104:107],a[216:219],v[116:119]
s_cmp_eq_u32 s60,2
s_cselect_b64 s[56:57],s[20:21],s[56:57]
v_lshlrev_b32 v11,8,v21
s_waitcnt vmcnt(18)
s_barrier
v_mfma_f32_16x16x32_bf16 v[72:75],a[112:115],a[192:195],0
s_add_u32 m0,s88,2048
ds_read_b64_tr_b16 a[64:65],v50 offset:1024
v_lshl_add_u32 v11,v22,6,v11
s_waitcnt lgkmcnt(1)
v_mfma_f32_16x16x32_bf16 v[72:75],a[116:119],a[196:199],v[72:75]
s_cmp_eq_u32 s60,3
s_cselect_b64 s[56:57],s[24:25],s[56:57]
ds_read_b64_tr_b16 a[66:67],v50 offset:3072
v_mfma_f32_16x16x32_bf16 v[88:91],a[116:119],a[204:207],0
buffer_load_dwordx4 v42,s[36:39],s83 offen lds
v_mfma_f32_16x16x32_bf16 v[88:91],a[112:115],a[200:203],v[88:91]
ds_read_b64_tr_b16 a[68:69],v50 offset:17408
v_add_u32 v11,s67,v11
v_mfma_f32_16x16x32_bf16 v[104:107],a[112:115],a[208:211],0
ds_read_b64_tr_b16 a[70:71],v50 offset:19456
v_lshl_add_u32 v4,v20,4,v11
v_mfma_f32_16x16x32_bf16 v[104:107],a[116:119],a[212:215],v[104:107]
s_add_u32 m0,s88,3072
ds_read_b64_tr_b16 a[72:73],v50 offset:1032
v_xor_b32 v12,2,v20
v_mfma_f32_16x16x32_bf16 v[120:123],a[116:119],a[220:223],0
ds_read_b64_tr_b16 a[74:75],v50 offset:3080
v_lshl_add_u32 v5,v12,4,v11
v_mfma_f32_16x16x32_bf16 v[120:123],a[112:115],a[216:219],v[120:123]
buffer_load_dwordx4 v43,s[36:39],s83 offen lds
v_mfma_f32_16x16x32_bf16 v[76:79],a[120:123],a[192:195],0
ds_read_b64_tr_b16 a[76:77],v50 offset:17416
v_lshlrev_b32 v11,10,v21
v_mfma_f32_16x16x32_bf16 v[76:79],a[124:127],a[196:199],v[76:79]
ds_read_b64_tr_b16 a[78:79],v50 offset:19464
v_lshl_add_u32 v11,v20,6,v11
v_mfma_f32_16x16x32_bf16 v[92:95],a[124:127],a[204:207],0
s_add_u32 m0,s88,4096
ds_read_b64_tr_b16 a[80:81],v50 offset:1280
v_and_b32 v12,2,v21
v_mfma_f32_16x16x32_bf16 v[92:95],a[120:123],a[200:203],v[92:95]
v_xor_b32 v12,v12,v22
v_lshl_add_u32 v11,v12,4,v11
v_mfma_f32_16x16x32_bf16 v[108:111],a[120:123],a[208:211],0
buffer_load_dwordx4 v44,s[36:39],s83 offen lds
v_mfma_f32_16x16x32_bf16 v[108:111],a[124:127],a[212:215],v[108:111]
ds_read_b64_tr_b16 a[82:83],v50 offset:3328
v_add_u32 v1,98304,v11
v_mfma_f32_16x16x32_bf16 v[124:127],a[124:127],a[220:223],0
s_waitcnt lgkmcnt(4)
ds_read_b64_tr_b16 a[84:85],v50 offset:17664
v_lshlrev_b32 v11,10,v22
v_mfma_f32_16x16x32_bf16 v[124:127],a[120:123],a[216:219],v[124:127]
s_add_u32 m0,s88,5120
ds_read_b64_tr_b16 a[86:87],v50 offset:19712
v_lshl_add_u32 v11,v21,6,v11
v_mfma_f32_16x16x32_bf16 v[128:131],a[64:67],a[192:195],0
ds_read_b64_tr_b16 a[88:89],v50 offset:1288
v_lshl_add_u32 v11,v20,3,v11
v_mfma_f32_16x16x32_bf16 v[128:131],a[68:71],a[196:199],v[128:131]
buffer_load_dwordx4 v45,s[36:39],s83 offen lds
v_mfma_f32_16x16x32_bf16 v[160:163],a[68:71],a[204:207],0
ds_read_b64_tr_b16 a[90:91],v50 offset:3336
v_add_u32 v11,114688,v11
v_mfma_f32_16x16x32_bf16 v[160:163],a[64:67],a[200:203],v[160:163]
ds_read_b64_tr_b16 a[92:93],v50 offset:17672
v_and_b32 v12,1,v22
v_mfma_f32_16x16x32_bf16 v[192:195],a[64:67],a[208:211],0
s_add_u32 m0,s88,6144
ds_read_b64_tr_b16 a[94:95],v50 offset:19720
v_lshl_add_u32 v2,v12,5,v11
v_mfma_f32_16x16x32_bf16 v[192:195],a[68:71],a[212:215],v[192:195]
v_xor_b32 v12,1,v12
v_lshl_add_u32 v3,v12,5,v11
v_mfma_f32_16x16x32_bf16 v[224:227],a[68:71],a[220:223],0
buffer_load_dwordx4 v46,s[36:39],s83 offen lds
v_mfma_f32_16x16x32_bf16 v[224:227],a[64:67],a[216:219],v[224:227]
ds_read_b64_tr_b16 a[96:97],v50 offset:1536
v_mov_b32 v28,0x3f803f80
v_mfma_f32_16x16x32_bf16 v[132:135],a[72:75],a[192:195],0
s_waitcnt lgkmcnt(4)
ds_read_b64_tr_b16 a[98:99],v50 offset:3584
v_mov_b32 v29,0x3f803f80
v_mfma_f32_16x16x32_bf16 v[132:135],a[76:79],a[196:199],v[132:135]
s_add_u32 m0,s88,7168
s_mov_b32 s88,s69
ds_read_b64_tr_b16 a[100:101],v50 offset:17920
v_mfma_f32_16x16x32_bf16 v[164:167],a[76:79],a[204:207],0
ds_read_b64_tr_b16 a[102:103],v50 offset:19968
v_mov_b32 v30,0x3f803f80
v_mfma_f32_16x16x32_bf16 v[164:167],a[72:75],a[200:203],v[164:167]
buffer_load_dwordx4 v47,s[36:39],s83 offen lds
v_mfma_f32_16x16x32_bf16 v[196:199],a[72:75],a[208:211],0
s_add_u32 s83,s83,524288
ds_read_b64_tr_b16 a[104:105],v50 offset:1544
v_mov_b32 v31,0x3f803f80
v_mfma_f32_16x16x32_bf16 v[196:199],a[76:79],a[212:215],v[196:199]
ds_read_b64_tr_b16 a[106:107],v50 offset:3592
v_mfma_f32_16x16x32_bf16 v[228:231],a[76:79],a[220:223],0
s_add_u32 m0,s85,0
ds_read_b64_tr_b16 a[108:109],v50 offset:17928
v_mfma_f32_16x16x32_bf16 v[228:231],a[72:75],a[216:219],v[228:231]
v_mfma_f32_16x16x32_bf16 v[136:139],a[80:83],a[192:195],0
buffer_load_dwordx4 v32,s[28:31],s84 offen lds
v_mfma_f32_16x16x32_bf16 v[136:139],a[84:87],a[196:199],v[136:139]
ds_read_b64_tr_b16 a[110:111],v50 offset:19976
v_mfma_f32_16x16x32_bf16 v[168:171],a[84:87],a[204:207],0
ds_read_b64_tr_b16 a[112:113],v50 offset:1792
v_mfma_f32_16x16x32_bf16 v[168:171],a[80:83],a[200:203],v[168:171]
ds_read_b64_tr_b16 a[114:115],v50 offset:3840
v_mfma_f32_16x16x32_bf16 v[200:203],a[80:83],a[208:211],0
ds_read_b64_tr_b16 a[116:117],v50 offset:18176
v_mfma_f32_16x16x32_bf16 v[200:203],a[84:87],a[212:215],v[200:203]
buffer_load_dwordx4 v33,s[28:31],s84 offen offset:1024 lds
v_mfma_f32_16x16x32_bf16 v[232:235],a[84:87],a[220:223],0
ds_read_b64_tr_b16 a[118:119],v50 offset:20224
v_mfma_f32_16x16x32_bf16 v[232:235],a[80:83],a[216:219],v[232:235]
s_waitcnt lgkmcnt(5)
ds_read_b64_tr_b16 a[120:121],v50 offset:1800
v_mfma_f32_16x16x32_bf16 v[140:143],a[88:91],a[192:195],0
ds_read_b64_tr_b16 a[122:123],v50 offset:3848
v_mfma_f32_16x16x32_bf16 v[140:143],a[92:95],a[196:199],v[140:143]
v_mfma_f32_16x16x32_bf16 v[172:175],a[92:95],a[204:207],0
buffer_load_dwordx4 v34,s[28:31],s84 offen offset:2048 lds
v_mfma_f32_16x16x32_bf16 v[172:175],a[88:91],a[200:203],v[172:175]
ds_read_b64_tr_b16 a[124:125],v50 offset:18184
v_mfma_f32_16x16x32_bf16 v[204:207],a[88:91],a[208:211],0
ds_read_b64_tr_b16 a[126:127],v50 offset:20232
v_add_u32 v50,s86,v51
v_mfma_f32_16x16x32_bf16 v[204:207],a[92:95],a[212:215],v[204:207]
v_mfma_f32_16x16x32_bf16 v[236:239],a[92:95],a[220:223],0
v_mfma_f32_16x16x32_bf16 v[236:239],a[88:91],a[216:219],v[236:239]
buffer_load_dwordx4 v35,s[28:31],s84 offen offset:3072 lds
s_waitcnt vmcnt(20) lgkmcnt(0)
s_barrier
v_mfma_f32_16x16x32_bf16 v[144:147],a[96:99],a[192:195],0
s_waitcnt vmcnt(12)
ds_read_b128 a[224:227],v48
v_mfma_f32_16x16x32_bf16 v[144:147],a[100:103],a[196:199],v[144:147]
s_add_u32 m0,s85,4096
s_xor_b32 s85,s85,98304
ds_read_b128 a[228:231],v49
v_mfma_f32_16x16x32_bf16 v[176:179],a[100:103],a[204:207],0
ds_read_b128 a[232:235],v48 offset:2048
v_mfma_f32_16x16x32_bf16 v[176:179],a[96:99],a[200:203],v[176:179]
buffer_load_dwordx4 v36,s[28:31],s84 offen lds
v_mfma_f32_16x16x32_bf16 v[208:211],a[96:99],a[208:211],0
ds_read_b128 a[236:239],v49 offset:2048
v_mfma_f32_16x16x32_bf16 v[208:211],a[100:103],a[212:215],v[208:211]
ds_read_b128 a[240:243],v48 offset:4096
v_mfma_f32_16x16x32_bf16 v[240:243],a[100:103],a[220:223],0
ds_read_b128 a[244:247],v49 offset:4096
v_mfma_f32_16x16x32_bf16 v[240:243],a[96:99],a[216:219],v[240:243]
ds_read_b128 a[248:251],v48 offset:6144
v_mfma_f32_16x16x32_bf16 v[148:151],a[104:107],a[192:195],0
buffer_load_dwordx4 v37,s[28:31],s84 offen offset:1024 lds
v_mfma_f32_16x16x32_bf16 v[148:151],a[108:111],a[196:199],v[148:151]
ds_read_b128 a[252:255],v49 offset:6144
v_mfma_f32_16x16x32_bf16 v[180:183],a[108:111],a[204:207],0
ds_read_b64_tr_b16 a[64:65],v50
v_mfma_f32_16x16x32_bf16 v[180:183],a[104:107],a[200:203],v[180:183]
ds_read_b64_tr_b16 a[66:67],v50 offset:2048
v_mfma_f32_16x16x32_bf16 v[212:215],a[104:107],a[208:211],0
v_mfma_f32_16x16x32_bf16 v[212:215],a[108:111],a[212:215],v[212:215]
buffer_load_dwordx4 v38,s[28:31],s84 offen offset:2048 lds
v_mfma_f32_16x16x32_bf16 v[244:247],a[108:111],a[220:223],0
ds_read_b64_tr_b16 a[68:69],v50 offset:16384
v_mfma_f32_16x16x32_bf16 v[244:247],a[104:107],a[216:219],v[244:247]
ds_read_b64_tr_b16 a[70:71],v50 offset:18432
v_mfma_f32_16x16x32_bf16 v[152:155],a[112:115],a[192:195],0
ds_read_b64_tr_b16 a[72:73],v50 offset:8
v_mfma_f32_16x16x32_bf16 v[152:155],a[116:119],a[196:199],v[152:155]
ds_read_b64_tr_b16 a[74:75],v50 offset:2056
v_mfma_f32_16x16x32_bf16 v[184:187],a[116:119],a[204:207],0
buffer_load_dwordx4 v39,s[28:31],s84 offen offset:3072 lds
v_mfma_f32_16x16x32_bf16 v[184:187],a[112:115],a[200:203],v[184:187]
s_add_u32 s84,s84,128
ds_read_b64_tr_b16 a[76:77],v50 offset:16392
v_mfma_f32_16x16x32_bf16 v[216:219],a[112:115],a[208:211],0
ds_read_b64_tr_b16 a[78:79],v50 offset:18440
v_mfma_f32_16x16x32_bf16 v[216:219],a[116:119],a[212:215],v[216:219]
ds_read_b64_tr_b16 a[80:81],v50 offset:256
v_mfma_f32_16x16x32_bf16 v[248:251],a[116:119],a[220:223],0
v_mfma_f32_16x16x32_bf16 v[248:251],a[112:115],a[216:219],v[248:251]
ds_read_b64_tr_b16 a[82:83],v50 offset:2304
v_mfma_f32_16x16x32_bf16 v[156:159],a[120:123],a[192:195],0
global_load_dword a184,v17,s[56:57] nt
ds_read_b64_tr_b16 a[84:85],v50 offset:16640
v_mfma_f32_16x16x32_bf16 v[156:159],a[124:127],a[196:199],v[156:159]
ds_read_b64_tr_b16 a[86:87],v50 offset:18688
v_mfma_f32_16x16x32_bf16 v[188:191],a[124:127],a[204:207],0
v_mfma_f32_16x16x32_bf16 v[188:191],a[120:123],a[200:203],v[188:191]
ds_read_b64_tr_b16 a[88:89],v50 offset:264
v_mfma_f32_16x16x32_bf16 v[220:223],a[120:123],a[208:211],0
ds_read_b64_tr_b16 a[90:91],v50 offset:2312
v_mfma_f32_16x16x32_bf16 v[220:223],a[124:127],a[212:215],v[220:223]
ds_read_b64_tr_b16 a[92:93],v50 offset:16648
v_mfma_f32_16x16x32_bf16 v[252:255],a[124:127],a[220:223],0
v_mfma_f32_16x16x32_bf16 v[252:255],a[120:123],a[216:219],v[252:255]
ds_read_b64_tr_b16 a[94:95],v50 offset:18696
s_mov_b32 s70,3
s_memrealtime s[8:9]
s_memtime s[4:5]
s_nop 0
L_qp_%=:
s_waitcnt lgkmcnt(6)
v_mfma_f32_16x16x32_bf16 a[0:3],a[64:67],a[224:227],a[0:3]
s_add_u32 m0,s88,0
ds_read_b64_tr_b16 a[96:97],v50 offset:512
v_xor_b32 v48,98304,v48
L_pd1_%=:
s_cbranch_execz L_pd5_%=
v_mfma_f32_16x16x32_bf16 a[0:3],a[68:71],a[228:231],a[0:3]
buffer_load_dwordx4 v40,s[36:39],s83 offen lds
v_mfma_f32_16x16x32_bf16 a[16:19],a[68:71],a[236:239],a[16:19]
s_mov_b32 s69,s86
ds_read_b64_tr_b16 a[98:99],v50 offset:2560
v_xor_b32 v49,98304,v49
v_mfma_f32_16x16x32_bf16 a[16:19],a[64:67],a[232:235],a[16:19]
s_mov_b32 s86,s87
s_mov_b32 s87,s88
ds_read_b64_tr_b16 a[100:101],v50 offset:16896
v_mfma_f32_16x16x32_bf16 a[32:35],a[64:67],a[240:243],a[32:35]
ds_read_b64_tr_b16 a[102:103],v50 offset:18944
v_mfma_f32_16x16x32_bf16 a[32:35],a[68:71],a[244:247],a[32:35]
ds_read_b64_tr_b16 a[104:105],v50 offset:520
v_mfma_f32_16x16x32_bf16 a[48:51],a[68:71],a[252:255],a[48:51]
s_add_u32 m0,s88,1024
v_mfma_f32_16x16x32_bf16 a[48:51],a[64:67],a[248:251],a[48:51]
ds_read_b64_tr_b16 a[106:107],v50 offset:2568
v_mfma_f32_16x16x32_bf16 a[4:7],a[72:75],a[224:227],a[4:7]
buffer_load_dwordx4 v41,s[36:39],s83 offen lds
v_mfma_f32_16x16x32_bf16 a[4:7],a[76:79],a[228:231],a[4:7]
ds_read_b64_tr_b16 a[108:109],v50 offset:16904
v_mfma_f32_16x16x32_bf16 a[20:23],a[76:79],a[236:239],a[20:23]
ds_read_b64_tr_b16 a[110:111],v50 offset:18952
v_mfma_f32_16x16x32_bf16 a[20:23],a[72:75],a[232:235],a[20:23]
ds_read_b64_tr_b16 a[112:113],v50 offset:768
v_mfma_f32_16x16x32_bf16 a[36:39],a[72:75],a[240:243],a[36:39]
ds_read_b64_tr_b16 a[114:115],v50 offset:2816
v_mfma_f32_16x16x32_bf16 a[36:39],a[76:79],a[244:247],a[36:39]
s_add_u32 m0,s88,2048
v_mfma_f32_16x16x32_bf16 a[52:55],a[76:79],a[252:255],a[52:55]
ds_read_b64_tr_b16 a[116:117],v50 offset:17152
v_mfma_f32_16x16x32_bf16 a[52:55],a[72:75],a[248:251],a[52:55]
buffer_load_dwordx4 v42,s[36:39],s83 offen lds
v_mfma_f32_16x16x32_bf16 a[8:11],a[80:83],a[224:227],a[8:11]
s_waitcnt lgkmcnt(5)
ds_read_b64_tr_b16 a[118:119],v50 offset:19200
v_mfma_f32_16x16x32_bf16 a[8:11],a[84:87],a[228:231],a[8:11]
ds_read_b64_tr_b16 a[120:121],v50 offset:776
)A"
R"A(v_mfma_f32_16x16x32_bf16 a[24:27],a[84:87],a[236:239],a[24:27]
ds_read_b64_tr_b16 a[122:123],v50 offset:2824
v_mfma_f32_16x16x32_bf16 a[24:27],a[80:83],a[232:235],a[24:27]
ds_read_b64_tr_b16 a[124:125],v50 offset:17160
v_mfma_f32_16x16x32_bf16 a[40:43],a[80:83],a[240:243],a[40:43]
s_add_u32 m0,s88,3072
v_mfma_f32_16x16x32_bf16 a[40:43],a[84:87],a[244:247],a[40:43]
ds_read_b64_tr_b16 a[126:127],v50 offset:19208
v_mfma_f32_16x16x32_bf16 a[56:59],a[84:87],a[252:255],a[56:59]
buffer_load_dwordx4 v43,s[36:39],s83 offen lds
v_mfma_f32_16x16x32_bf16 a[56:59],a[80:83],a[248:251],a[56:59]
ds_read_b64_tr_b16 a[64:65],v50 offset:1024
v_mfma_f32_16x16x32_bf16 a[12:15],a[88:91],a[224:227],a[12:15]
ds_read_b64_tr_b16 a[66:67],v50 offset:3072
v_mfma_f32_16x16x32_bf16 a[12:15],a[92:95],a[228:231],a[12:15]
ds_read_b64_tr_b16 a[68:69],v50 offset:17408
v_mfma_f32_16x16x32_bf16 a[28:31],a[92:95],a[236:239],a[28:31]
ds_read_b64_tr_b16 a[70:71],v50 offset:19456
v_mfma_f32_16x16x32_bf16 a[28:31],a[88:91],a[232:235],a[28:31]
s_add_u32 m0,s88,4096
v_mfma_f32_16x16x32_bf16 a[44:47],a[88:91],a[240:243],a[44:47]
ds_read_b64_tr_b16 a[72:73],v50 offset:1032
v_mfma_f32_16x16x32_bf16 a[44:47],a[92:95],a[244:247],a[44:47]
buffer_load_dwordx4 v44,s[36:39],s83 offen lds
v_mfma_f32_16x16x32_bf16 a[60:63],a[92:95],a[252:255],a[60:63]
ds_read_b64_tr_b16 a[74:75],v50 offset:3080
v_mfma_f32_16x16x32_bf16 a[60:63],a[88:91],a[248:251],a[60:63]
ds_read_b64_tr_b16 a[76:77],v50 offset:17416
v_mfma_f32_16x16x32_bf16 v[64:67],a[96:99],a[224:227],v[64:67]
ds_read_b64_tr_b16 a[78:79],v50 offset:19464
v_mfma_f32_16x16x32_bf16 v[64:67],a[100:103],a[228:231],v[64:67]
ds_read_b64_tr_b16 a[80:81],v50 offset:1280
v_mfma_f32_16x16x32_bf16 v[80:83],a[100:103],a[236:239],v[80:83]
s_add_u32 m0,s88,5120
v_mfma_f32_16x16x32_bf16 v[80:83],a[96:99],a[232:235],v[80:83]
ds_read_b64_tr_b16 a[82:83],v50 offset:3328
v_mfma_f32_16x16x32_bf16 v[96:99],a[96:99],a[240:243],v[96:99]
buffer_load_dwordx4 v45,s[36:39],s83 offen lds
v_mfma_f32_16x16x32_bf16 v[96:99],a[100:103],a[244:247],v[96:99]
ds_read_b64_tr_b16 a[84:85],v50 offset:17664
v_mfma_f32_16x16x32_bf16 v[112:115],a[100:103],a[252:255],v[112:115]
ds_read_b64_tr_b16 a[86:87],v50 offset:19712
v_mfma_f32_16x16x32_bf16 v[112:115],a[96:99],a[248:251],v[112:115]
ds_read_b64_tr_b16 a[88:89],v50 offset:1288
v_mfma_f32_16x16x32_bf16 v[68:71],a[104:107],a[224:227],v[68:71]
s_waitcnt lgkmcnt(5)
ds_read_b64_tr_b16 a[90:91],v50 offset:3336
v_mfma_f32_16x16x32_bf16 v[68:71],a[108:111],a[228:231],v[68:71]
s_add_u32 m0,s88,6144
v_mfma_f32_16x16x32_bf16 v[84:87],a[108:111],a[236:239],v[84:87]
ds_read_b64_tr_b16 a[92:93],v50 offset:17672
v_mfma_f32_16x16x32_bf16 v[84:87],a[104:107],a[232:235],v[84:87]
buffer_load_dwordx4 v46,s[36:39],s83 offen lds
v_mfma_f32_16x16x32_bf16 v[100:103],a[104:107],a[240:243],v[100:103]
ds_read_b64_tr_b16 a[94:95],v50 offset:19720
v_mfma_f32_16x16x32_bf16 v[100:103],a[108:111],a[244:247],v[100:103]
ds_read_b64_tr_b16 a[96:97],v50 offset:1536
v_mfma_f32_16x16x32_bf16 v[116:119],a[108:111],a[252:255],v[116:119]
ds_read_b64_tr_b16 a[98:99],v50 offset:3584
v_mfma_f32_16x16x32_bf16 v[116:119],a[104:107],a[248:251],v[116:119]
ds_read_b64_tr_b16 a[100:101],v50 offset:17920
v_mfma_f32_16x16x32_bf16 v[72:75],a[112:115],a[224:227],v[72:75]
s_add_u32 m0,s88,7168
s_mov_b32 s88,s69
s_mov_b32 s69,s86
v_mfma_f32_16x16x32_bf16 v[72:75],a[116:119],a[228:231],v[72:75]
ds_read_b64_tr_b16 a[102:103],v50 offset:19968
v_mfma_f32_16x16x32_bf16 v[88:91],a[116:119],a[236:239],v[88:91]
buffer_load_dwordx4 v47,s[36:39],s83 offen lds
v_mfma_f32_16x16x32_bf16 v[88:91],a[112:115],a[232:235],v[88:91]
s_add_u32 s83,s83,524288
ds_read_b64_tr_b16 a[104:105],v50 offset:1544
v_mfma_f32_16x16x32_bf16 v[104:107],a[112:115],a[240:243],v[104:107]
ds_read_b64_tr_b16 a[106:107],v50 offset:3592
v_mfma_f32_16x16x32_bf16 v[104:107],a[116:119],a[244:247],v[104:107]
ds_read_b64_tr_b16 a[108:109],v50 offset:17928
v_mfma_f32_16x16x32_bf16 v[120:123],a[116:119],a[252:255],v[120:123]
ds_read_b64_tr_b16 a[110:111],v50 offset:19976
v_mfma_f32_16x16x32_bf16 v[120:123],a[112:115],a[248:251],v[120:123]
s_add_u32 m0,s85,0
v_mfma_f32_16x16x32_bf16 v[76:79],a[120:123],a[224:227],v[76:79]
ds_read_b64_tr_b16 a[112:113],v50 offset:1792
v_mfma_f32_16x16x32_bf16 v[76:79],a[124:127],a[228:231],v[76:79]
buffer_load_dwordx4 v32,s[28:31],s84 offen lds
v_mfma_f32_16x16x32_bf16 v[92:95],a[124:127],a[236:239],v[92:95]
ds_read_b64_tr_b16 a[114:115],v50 offset:3840
v_mfma_f32_16x16x32_bf16 v[92:95],a[120:123],a[232:235],v[92:95]
ds_read_b64_tr_b16 a[116:117],v50 offset:18176
v_mfma_f32_16x16x32_bf16 v[108:111],a[120:123],a[240:243],v[108:111]
ds_read_b64_tr_b16 a[118:119],v50 offset:20224
v_mfma_f32_16x16x32_bf16 v[108:111],a[124:127],a[244:247],v[108:111]
s_cmp_eq_u32 s70,1
s_cselect_b32 s65,256,0
s_cselect_b32 s66,524288,0
v_mfma_f32_16x16x32_bf16 v[124:127],a[124:127],a[252:255],v[124:127]
ds_read_b64_tr_b16 a[124:125],v50 offset:18184
v_add_u32 v40,s65,v40
v_mfma_f32_16x16x32_bf16 v[124:127],a[120:123],a[248:251],v[124:127]
ds_read_b64_tr_b16 a[120:121],v50 offset:1800
v_add_u32 v41,s66,v41
v_mfma_f32_16x16x32_bf16 v[128:131],a[64:67],a[224:227],v[128:131]
buffer_load_dwordx4 v33,s[28:31],s84 offen offset:1024 lds
v_mfma_f32_16x16x32_bf16 v[128:131],a[68:71],a[228:231],v[128:131]
ds_read_b64_tr_b16 a[122:123],v50 offset:3848
v_add_u32 v42,s65,v42
v_mfma_f32_16x16x32_bf16 v[160:163],a[68:71],a[236:239],v[160:163]
ds_read_b64_tr_b16 a[126:127],v50 offset:20232
v_add_u32 v50,s86,v51
v_mfma_f32_16x16x32_bf16 v[160:163],a[64:67],a[232:235],v[160:163]
s_mov_b32 s86,s87
v_add_u32 v43,s66,v43
v_add_u32 v44,s65,v44
v_mfma_f32_16x16x32_bf16 v[192:195],a[64:67],a[240:243],v[192:195]
s_mov_b32 s87,s88
v_add_u32 v45,s66,v45
v_add_u32 v46,s65,v46
v_mfma_f32_16x16x32_bf16 v[192:195],a[68:71],a[244:247],v[192:195]
v_add_u32 v47,s66,v47
v_mfma_f32_16x16x32_bf16 v[224:227],a[68:71],a[252:255],v[224:227]
v_mfma_f32_16x16x32_bf16 v[224:227],a[64:67],a[248:251],v[224:227]
buffer_load_dwordx4 v34,s[28:31],s84 offen offset:2048 lds
v_mfma_f32_16x16x32_bf16 v[132:135],a[72:75],a[224:227],v[132:135]
v_mfma_f32_16x16x32_bf16 v[132:135],a[76:79],a[228:231],v[132:135]
v_mfma_f32_16x16x32_bf16 v[164:167],a[76:79],a[236:239],v[164:167]
v_mfma_f32_16x16x32_bf16 v[164:167],a[72:75],a[232:235],v[164:167]
v_mfma_f32_16x16x32_bf16 v[196:199],a[72:75],a[240:243],v[196:199]
v_mfma_f32_16x16x32_bf16 v[196:199],a[76:79],a[244:247],v[196:199]
v_mfma_f32_16x16x32_bf16 v[228:231],a[76:79],a[252:255],v[228:231]
buffer_load_dwordx4 v35,s[28:31],s84 offen offset:3072 lds
v_mfma_f32_16x16x32_bf16 v[228:231],a[72:75],a[248:251],v[228:231]
s_waitcnt lgkmcnt(0)
v_mfma_f32_16x16x32_bf16 v[136:139],a[80:83],a[224:227],v[136:139]
v_mfma_f32_16x16x32_bf16 v[136:139],a[84:87],a[228:231],v[136:139]
v_mfma_f32_16x16x32_bf16 v[168:171],a[84:87],a[236:239],v[168:171]
v_mfma_f32_16x16x32_bf16 v[168:171],a[80:83],a[232:235],v[168:171]
s_add_u32 m0,s85,4096
s_xor_b32 s85,s85,98304
v_mfma_f32_16x16x32_bf16 v[200:203],a[80:83],a[240:243],v[200:203]
v_mfma_f32_16x16x32_bf16 v[200:203],a[84:87],a[244:247],v[200:203]
buffer_load_dwordx4 v36,s[28:31],s84 offen lds
v_mfma_f32_16x16x32_bf16 v[232:235],a[84:87],a[252:255],v[232:235]
v_mfma_f32_16x16x32_bf16 v[232:235],a[80:83],a[248:251],v[232:235]
v_mfma_f32_16x16x32_bf16 v[140:143],a[88:91],a[224:227],v[140:143]
v_mfma_f32_16x16x32_bf16 v[140:143],a[92:95],a[228:231],v[140:143]
v_mfma_f32_16x16x32_bf16 v[172:175],a[92:95],a[236:239],v[172:175]
v_mfma_f32_16x16x32_bf16 v[172:175],a[88:91],a[232:235],v[172:175]
v_mfma_f32_16x16x32_bf16 v[204:207],a[88:91],a[240:243],v[204:207]
buffer_load_dwordx4 v37,s[28:31],s84 offen offset:1024 lds
v_mfma_f32_16x16x32_bf16 v[204:207],a[92:95],a[244:247],v[204:207]
v_mfma_f32_16x16x32_bf16 v[236:239],a[92:95],a[252:255],v[236:239]
v_mfma_f32_16x16x32_bf16 v[236:239],a[88:91],a[248:251],v[236:239]
s_waitcnt vmcnt(23)
s_barrier
v_mfma_f32_16x16x32_bf16 v[144:147],a[96:99],a[224:227],v[144:147]
s_waitcnt vmcnt(15)
ds_read_b128 a[192:195],v48
v_mfma_f32_16x16x32_bf16 v[144:147],a[100:103],a[228:231],v[144:147]
ds_read_b128 a[196:199],v49
v_mfma_f32_16x16x32_bf16 v[176:179],a[100:103],a[236:239],v[176:179]
buffer_load_dwordx4 v38,s[28:31],s84 offen offset:2048 lds
v_mfma_f32_16x16x32_bf16 v[176:179],a[96:99],a[232:235],v[176:179]
ds_read_b128 a[200:203],v48 offset:2048
v_mfma_f32_16x16x32_bf16 v[208:211],a[96:99],a[240:243],v[208:211]
ds_read_b128 a[204:207],v49 offset:2048
v_mfma_f32_16x16x32_bf16 v[208:211],a[100:103],a[244:247],v[208:211]
ds_read_b128 a[208:211],v48 offset:4096
v_mfma_f32_16x16x32_bf16 v[240:243],a[100:103],a[252:255],v[240:243]
ds_read_b128 a[212:215],v49 offset:4096
v_mfma_f32_16x16x32_bf16 v[240:243],a[96:99],a[248:251],v[240:243]
v_mfma_f32_16x16x32_bf16 v[148:151],a[104:107],a[224:227],v[148:151]
ds_read_b128 a[216:219],v48 offset:6144
v_xor_b32 v48,98304,v48
v_mfma_f32_16x16x32_bf16 v[148:151],a[108:111],a[228:231],v[148:151]
buffer_load_dwordx4 v39,s[28:31],s84 offen offset:3072 lds
v_mfma_f32_16x16x32_bf16 v[180:183],a[108:111],a[236:239],v[180:183]
s_add_u32 s84,s84,128
ds_read_b128 a[220:223],v49 offset:6144
v_xor_b32 v49,98304,v49
v_mfma_f32_16x16x32_bf16 v[180:183],a[104:107],a[232:235],v[180:183]
ds_read_b64_tr_b16 a[64:65],v50
v_mfma_f32_16x16x32_bf16 v[212:215],a[104:107],a[240:243],v[212:215]
ds_read_b64_tr_b16 a[66:67],v50 offset:2048
v_mfma_f32_16x16x32_bf16 v[212:215],a[108:111],a[244:247],v[212:215]
ds_read_b64_tr_b16 a[68:69],v50 offset:16384
v_mfma_f32_16x16x32_bf16 v[244:247],a[108:111],a[252:255],v[244:247]
v_mfma_f32_16x16x32_bf16 v[244:247],a[104:107],a[248:251],v[244:247]
ds_read_b64_tr_b16 a[70:71],v50 offset:18432
v_mfma_f32_16x16x32_bf16 v[152:155],a[112:115],a[224:227],v[152:155]
buffer_load_dword v9,v19,s[48:51],s90 offen
v_mfma_f32_16x16x32_bf16 v[152:155],a[116:119],a[228:231],v[152:155]
s_add_u32 s90,s90,896
ds_read_b64_tr_b16 a[72:73],v50 offset:8
v_mfma_f32_16x16x32_bf16 v[184:187],a[116:119],a[236:239],v[184:187]
ds_read_b64_tr_b16 a[74:75],v50 offset:2056
v_mfma_f32_16x16x32_bf16 v[184:187],a[112:115],a[232:235],v[184:187]
ds_read_b64_tr_b16 a[76:77],v50 offset:16392
v_mfma_f32_16x16x32_bf16 v[216:219],a[112:115],a[240:243],v[216:219]
ds_read_b64_tr_b16 a[78:79],v50 offset:18440
v_mfma_f32_16x16x32_bf16 v[216:219],a[116:119],a[244:247],v[216:219]
v_mfma_f32_16x16x32_bf16 v[248:251],a[116:119],a[252:255],v[248:251]
ds_read_b64_tr_b16 a[80:81],v50 offset:256
v_mfma_f32_16x16x32_bf16 v[248:251],a[112:115],a[248:251],v[248:251]
ds_read_b64_tr_b16 a[82:83],v50 offset:2304
v_mfma_f32_16x16x32_bf16 v[156:159],a[120:123],a[224:227],v[156:159]
ds_read_b64_tr_b16 a[84:85],v50 offset:16640
v_mfma_f32_16x16x32_bf16 v[156:159],a[124:127],a[228:231],v[156:159]
v_mfma_f32_16x16x32_bf16 v[188:191],a[124:127],a[236:239],v[188:191]
ds_read_b64_tr_b16 a[86:87],v50 offset:18688
v_mfma_f32_16x16x32_bf16 v[188:191],a[120:123],a[232:235],v[188:191]
ds_read_b64_tr_b16 a[88:89],v50 offset:264
v_mfma_f32_16x16x32_bf16 v[220:223],a[120:123],a[240:243],v[220:223]
ds_read_b64_tr_b16 a[90:91],v50 offset:2312
v_mfma_f32_16x16x32_bf16 v[220:223],a[124:127],a[244:247],v[220:223]
v_mfma_f32_16x16x32_bf16 v[252:255],a[124:127],a[252:255],v[252:255]
s_waitcnt lgkmcnt(4)
ds_read_b64_tr_b16 a[92:93],v50 offset:16648
v_mfma_f32_16x16x32_bf16 v[252:255],a[120:123],a[248:251],v[252:255]
s_add_u32 m0,s88,0
ds_read_b64_tr_b16 a[94:95],v50 offset:18696
v_mfma_f32_16x16x32_bf16 a[0:3],a[64:67],a[192:195],a[0:3]
ds_read_b64_tr_b16 a[96:97],v50 offset:512
v_mfma_f32_16x16x32_bf16 a[0:3],a[68:71],a[196:199],a[0:3]
buffer_load_dwordx4 v40,s[36:39],s83 offen lds
v_mfma_f32_16x16x32_bf16 a[16:19],a[68:71],a[204:207],a[16:19]
ds_read_b64_tr_b16 a[98:99],v50 offset:2560
v_mfma_f32_16x16x32_bf16 a[16:19],a[64:67],a[200:203],a[16:19]
ds_read_b64_tr_b16 a[100:101],v50 offset:16896
v_mfma_f32_16x16x32_bf16 a[32:35],a[64:67],a[208:211],a[32:35]
ds_read_b64_tr_b16 a[102:103],v50 offset:18944
v_mfma_f32_16x16x32_bf16 a[32:35],a[68:71],a[212:215],a[32:35]
)A"
R"A(v_mfma_f32_16x16x32_bf16 a[48:51],a[68:71],a[220:223],a[48:51]
s_add_u32 m0,s88,1024
ds_read_b64_tr_b16 a[104:105],v50 offset:520
v_mfma_f32_16x16x32_bf16 a[48:51],a[64:67],a[216:219],a[48:51]
ds_read_b64_tr_b16 a[106:107],v50 offset:2568
v_mfma_f32_16x16x32_bf16 a[4:7],a[72:75],a[192:195],a[4:7]
buffer_load_dwordx4 v41,s[36:39],s83 offen lds
v_mfma_f32_16x16x32_bf16 a[4:7],a[76:79],a[196:199],a[4:7]
ds_read_b64_tr_b16 a[108:109],v50 offset:16904
v_mfma_f32_16x16x32_bf16 a[20:23],a[76:79],a[204:207],a[20:23]
ds_read_b64_tr_b16 a[110:111],v50 offset:18952
v_mfma_f32_16x16x32_bf16 a[20:23],a[72:75],a[200:203],a[20:23]
ds_read_b64_tr_b16 a[112:113],v50 offset:768
v_mfma_f32_16x16x32_bf16 a[36:39],a[72:75],a[208:211],a[36:39]
ds_read_b64_tr_b16 a[114:115],v50 offset:2816
v_mfma_f32_16x16x32_bf16 a[36:39],a[76:79],a[212:215],a[36:39]
s_add_u32 m0,s88,2048
v_mfma_f32_16x16x32_bf16 a[52:55],a[76:79],a[220:223],a[52:55]
ds_read_b64_tr_b16 a[116:117],v50 offset:17152
v_mfma_f32_16x16x32_bf16 a[52:55],a[72:75],a[216:219],a[52:55]
buffer_load_dwordx4 v42,s[36:39],s83 offen lds
v_mfma_f32_16x16x32_bf16 a[8:11],a[80:83],a[192:195],a[8:11]
s_waitcnt lgkmcnt(5)
ds_read_b64_tr_b16 a[118:119],v50 offset:19200
v_mfma_f32_16x16x32_bf16 a[8:11],a[84:87],a[196:199],a[8:11]
ds_read_b64_tr_b16 a[120:121],v50 offset:776
v_mfma_f32_16x16x32_bf16 a[24:27],a[84:87],a[204:207],a[24:27]
ds_read_b64_tr_b16 a[122:123],v50 offset:2824
v_mfma_f32_16x16x32_bf16 a[24:27],a[80:83],a[200:203],a[24:27]
ds_read_b64_tr_b16 a[124:125],v50 offset:17160
v_mfma_f32_16x16x32_bf16 a[40:43],a[80:83],a[208:211],a[40:43]
s_add_u32 m0,s88,3072
v_mfma_f32_16x16x32_bf16 a[40:43],a[84:87],a[212:215],a[40:43]
ds_read_b64_tr_b16 a[126:127],v50 offset:19208
v_mfma_f32_16x16x32_bf16 a[56:59],a[84:87],a[220:223],a[56:59]
buffer_load_dwordx4 v43,s[36:39],s83 offen lds
v_mfma_f32_16x16x32_bf16 a[56:59],a[80:83],a[216:219],a[56:59]
ds_read_b64_tr_b16 a[64:65],v50 offset:1024
v_mfma_f32_16x16x32_bf16 a[12:15],a[88:91],a[192:195],a[12:15]
ds_read_b64_tr_b16 a[66:67],v50 offset:3072
v_mfma_f32_16x16x32_bf16 a[12:15],a[92:95],a[196:199],a[12:15]
ds_read_b64_tr_b16 a[68:69],v50 offset:17408
v_mfma_f32_16x16x32_bf16 a[28:31],a[92:95],a[204:207],a[28:31]
ds_read_b64_tr_b16 a[70:71],v50 offset:19456
v_mfma_f32_16x16x32_bf16 a[28:31],a[88:91],a[200:203],a[28:31]
s_add_u32 m0,s88,4096
v_mfma_f32_16x16x32_bf16 a[44:47],a[88:91],a[208:211],a[44:47]
ds_read_b64_tr_b16 a[72:73],v50 offset:1032
v_mfma_f32_16x16x32_bf16 a[44:47],a[92:95],a[212:215],a[44:47]
buffer_load_dwordx4 v44,s[36:39],s83 offen lds
v_mfma_f32_16x16x32_bf16 a[60:63],a[92:95],a[220:223],a[60:63]
ds_read_b64_tr_b16 a[74:75],v50 offset:3080
v_mfma_f32_16x16x32_bf16 a[60:63],a[88:91],a[216:219],a[60:63]
ds_read_b64_tr_b16 a[76:77],v50 offset:17416
v_mfma_f32_16x16x32_bf16 v[64:67],a[96:99],a[192:195],v[64:67]
ds_read_b64_tr_b16 a[78:79],v50 offset:19464
v_mfma_f32_16x16x32_bf16 v[64:67],a[100:103],a[196:199],v[64:67]
ds_read_b64_tr_b16 a[80:81],v50 offset:1280
v_mfma_f32_16x16x32_bf16 v[80:83],a[100:103],a[204:207],v[80:83]
s_add_u32 m0,s88,5120
v_mfma_f32_16x16x32_bf16 v[80:83],a[96:99],a[200:203],v[80:83]
ds_read_b64_tr_b16 a[82:83],v50 offset:3328
v_mfma_f32_16x16x32_bf16 v[96:99],a[96:99],a[208:211],v[96:99]
buffer_load_dwordx4 v45,s[36:39],s83 offen lds
v_mfma_f32_16x16x32_bf16 v[96:99],a[100:103],a[212:215],v[96:99]
ds_read_b64_tr_b16 a[84:85],v50 offset:17664
v_mfma_f32_16x16x32_bf16 v[112:115],a[100:103],a[220:223],v[112:115]
ds_read_b64_tr_b16 a[86:87],v50 offset:19712
v_mfma_f32_16x16x32_bf16 v[112:115],a[96:99],a[216:219],v[112:115]
ds_read_b64_tr_b16 a[88:89],v50 offset:1288
v_mfma_f32_16x16x32_bf16 v[68:71],a[104:107],a[192:195],v[68:71]
s_waitcnt lgkmcnt(5)
ds_read_b64_tr_b16 a[90:91],v50 offset:3336
v_mfma_f32_16x16x32_bf16 v[68:71],a[108:111],a[196:199],v[68:71]
s_add_u32 m0,s88,6144
v_mfma_f32_16x16x32_bf16 v[84:87],a[108:111],a[204:207],v[84:87]
ds_read_b64_tr_b16 a[92:93],v50 offset:17672
v_mfma_f32_16x16x32_bf16 v[84:87],a[104:107],a[200:203],v[84:87]
buffer_load_dwordx4 v46,s[36:39],s83 offen lds
v_mfma_f32_16x16x32_bf16 v[100:103],a[104:107],a[208:211],v[100:103]
ds_read_b64_tr_b16 a[94:95],v50 offset:19720
v_mfma_f32_16x16x32_bf16 v[100:103],a[108:111],a[212:215],v[100:103]
ds_read_b64_tr_b16 a[96:97],v50 offset:1536
v_mfma_f32_16x16x32_bf16 v[116:119],a[108:111],a[220:223],v[116:119]
ds_read_b64_tr_b16 a[98:99],v50 offset:3584
v_mfma_f32_16x16x32_bf16 v[116:119],a[104:107],a[216:219],v[116:119]
ds_read_b64_tr_b16 a[100:101],v50 offset:17920
v_mfma_f32_16x16x32_bf16 v[72:75],a[112:115],a[192:195],v[72:75]
s_add_u32 m0,s88,7168
s_mov_b32 s88,s69
v_mfma_f32_16x16x32_bf16 v[72:75],a[116:119],a[196:199],v[72:75]
ds_read_b64_tr_b16 a[102:103],v50 offset:19968
v_mfma_f32_16x16x32_bf16 v[88:91],a[116:119],a[204:207],v[88:91]
buffer_load_dwordx4 v47,s[36:39],s83 offen lds
v_mfma_f32_16x16x32_bf16 v[88:91],a[112:115],a[200:203],v[88:91]
s_add_u32 s83,s83,524288
ds_read_b64_tr_b16 a[104:105],v50 offset:1544
v_mfma_f32_16x16x32_bf16 v[104:107],a[112:115],a[208:211],v[104:107]
ds_read_b64_tr_b16 a[106:107],v50 offset:3592
v_mfma_f32_16x16x32_bf16 v[104:107],a[116:119],a[212:215],v[104:107]
ds_read_b64_tr_b16 a[108:109],v50 offset:17928
v_mfma_f32_16x16x32_bf16 v[120:123],a[116:119],a[220:223],v[120:123]
ds_read_b64_tr_b16 a[110:111],v50 offset:19976
v_mfma_f32_16x16x32_bf16 v[120:123],a[112:115],a[216:219],v[120:123]
s_add_u32 m0,s85,0
v_mfma_f32_16x16x32_bf16 v[76:79],a[120:123],a[192:195],v[76:79]
ds_read_b64_tr_b16 a[112:113],v50 offset:1792
v_mfma_f32_16x16x32_bf16 v[76:79],a[124:127],a[196:199],v[76:79]
buffer_load_dwordx4 v32,s[28:31],s84 offen lds
v_mfma_f32_16x16x32_bf16 v[92:95],a[124:127],a[204:207],v[92:95]
ds_read_b64_tr_b16 a[114:115],v50 offset:3840
v_mfma_f32_16x16x32_bf16 v[92:95],a[120:123],a[200:203],v[92:95]
ds_read_b64_tr_b16 a[116:117],v50 offset:18176
v_mfma_f32_16x16x32_bf16 v[108:111],a[120:123],a[208:211],v[108:111]
ds_read_b64_tr_b16 a[118:119],v50 offset:20224
v_mfma_f32_16x16x32_bf16 v[108:111],a[124:127],a[212:215],v[108:111]
v_mfma_f32_16x16x32_bf16 v[124:127],a[124:127],a[220:223],v[124:127]
ds_read_b64_tr_b16 a[124:125],v50 offset:18184
v_mfma_f32_16x16x32_bf16 v[124:127],a[120:123],a[216:219],v[124:127]
ds_read_b64_tr_b16 a[120:121],v50 offset:1800
v_mfma_f32_16x16x32_bf16 v[128:131],a[64:67],a[192:195],v[128:131]
buffer_load_dwordx4 v33,s[28:31],s84 offen offset:1024 lds
v_mfma_f32_16x16x32_bf16 v[128:131],a[68:71],a[196:199],v[128:131]
ds_read_b64_tr_b16 a[122:123],v50 offset:3848
v_mfma_f32_16x16x32_bf16 v[160:163],a[68:71],a[204:207],v[160:163]
ds_read_b64_tr_b16 a[126:127],v50 offset:20232
v_add_u32 v50,s86,v51
v_mfma_f32_16x16x32_bf16 v[160:163],a[64:67],a[200:203],v[160:163]
v_mfma_f32_16x16x32_bf16 v[192:195],a[64:67],a[208:211],v[192:195]
v_mfma_f32_16x16x32_bf16 v[192:195],a[68:71],a[212:215],v[192:195]
v_mfma_f32_16x16x32_bf16 v[224:227],a[68:71],a[220:223],v[224:227]
v_mfma_f32_16x16x32_bf16 v[224:227],a[64:67],a[216:219],v[224:227]
buffer_load_dwordx4 v34,s[28:31],s84 offen offset:2048 lds
v_mfma_f32_16x16x32_bf16 v[132:135],a[72:75],a[192:195],v[132:135]
v_mfma_f32_16x16x32_bf16 v[132:135],a[76:79],a[196:199],v[132:135]
v_mfma_f32_16x16x32_bf16 v[164:167],a[76:79],a[204:207],v[164:167]
v_mfma_f32_16x16x32_bf16 v[164:167],a[72:75],a[200:203],v[164:167]
v_mfma_f32_16x16x32_bf16 v[196:199],a[72:75],a[208:211],v[196:199]
v_mfma_f32_16x16x32_bf16 v[196:199],a[76:79],a[212:215],v[196:199]
v_mfma_f32_16x16x32_bf16 v[228:231],a[76:79],a[220:223],v[228:231]
buffer_load_dwordx4 v35,s[28:31],s84 offen offset:3072 lds
v_mfma_f32_16x16x32_bf16 v[228:231],a[72:75],a[216:219],v[228:231]
s_waitcnt lgkmcnt(0)
v_mfma_f32_16x16x32_bf16 v[136:139],a[80:83],a[192:195],v[136:139]
v_mfma_f32_16x16x32_bf16 v[136:139],a[84:87],a[196:199],v[136:139]
v_mfma_f32_16x16x32_bf16 v[168:171],a[84:87],a[204:207],v[168:171]
v_mfma_f32_16x16x32_bf16 v[168:171],a[80:83],a[200:203],v[168:171]
s_add_u32 m0,s85,4096
s_xor_b32 s85,s85,98304
v_mfma_f32_16x16x32_bf16 v[200:203],a[80:83],a[208:211],v[200:203]
v_mfma_f32_16x16x32_bf16 v[200:203],a[84:87],a[212:215],v[200:203]
buffer_load_dwordx4 v36,s[28:31],s84 offen lds
v_mfma_f32_16x16x32_bf16 v[232:235],a[84:87],a[220:223],v[232:235]
v_mfma_f32_16x16x32_bf16 v[232:235],a[80:83],a[216:219],v[232:235]
v_mfma_f32_16x16x32_bf16 v[140:143],a[88:91],a[192:195],v[140:143]
v_mfma_f32_16x16x32_bf16 v[140:143],a[92:95],a[196:199],v[140:143]
v_mfma_f32_16x16x32_bf16 v[172:175],a[92:95],a[204:207],v[172:175]
v_mfma_f32_16x16x32_bf16 v[172:175],a[88:91],a[200:203],v[172:175]
v_mfma_f32_16x16x32_bf16 v[204:207],a[88:91],a[208:211],v[204:207]
buffer_load_dwordx4 v37,s[28:31],s84 offen offset:1024 lds
v_mfma_f32_16x16x32_bf16 v[204:207],a[92:95],a[212:215],v[204:207]
v_mfma_f32_16x16x32_bf16 v[236:239],a[92:95],a[220:223],v[236:239]
v_mfma_f32_16x16x32_bf16 v[236:239],a[88:91],a[216:219],v[236:239]
s_waitcnt vmcnt(23)
s_barrier
v_mfma_f32_16x16x32_bf16 v[144:147],a[96:99],a[192:195],v[144:147]
s_waitcnt vmcnt(15)
ds_read_b128 a[224:227],v48
v_mfma_f32_16x16x32_bf16 v[144:147],a[100:103],a[196:199],v[144:147]
ds_read_b128 a[228:231],v49
v_mfma_f32_16x16x32_bf16 v[176:179],a[100:103],a[204:207],v[176:179]
buffer_load_dwordx4 v38,s[28:31],s84 offen offset:2048 lds
v_mfma_f32_16x16x32_bf16 v[176:179],a[96:99],a[200:203],v[176:179]
ds_read_b128 a[232:235],v48 offset:2048
v_mfma_f32_16x16x32_bf16 v[208:211],a[96:99],a[208:211],v[208:211]
ds_read_b128 a[236:239],v49 offset:2048
v_mfma_f32_16x16x32_bf16 v[208:211],a[100:103],a[212:215],v[208:211]
ds_read_b128 a[240:243],v48 offset:4096
v_mfma_f32_16x16x32_bf16 v[240:243],a[100:103],a[220:223],v[240:243]
ds_read_b128 a[244:247],v49 offset:4096
v_mfma_f32_16x16x32_bf16 v[240:243],a[96:99],a[216:219],v[240:243]
v_mfma_f32_16x16x32_bf16 v[148:151],a[104:107],a[192:195],v[148:151]
ds_read_b128 a[248:251],v48 offset:6144
v_mfma_f32_16x16x32_bf16 v[148:151],a[108:111],a[196:199],v[148:151]
buffer_load_dwordx4 v39,s[28:31],s84 offen offset:3072 lds
v_mfma_f32_16x16x32_bf16 v[180:183],a[108:111],a[204:207],v[180:183]
s_add_u32 s84,s84,128
ds_read_b128 a[252:255],v49 offset:6144
v_mfma_f32_16x16x32_bf16 v[180:183],a[104:107],a[200:203],v[180:183]
ds_read_b64_tr_b16 a[64:65],v50
v_mfma_f32_16x16x32_bf16 v[212:215],a[104:107],a[208:211],v[212:215]
ds_read_b64_tr_b16 a[66:67],v50 offset:2048
v_mfma_f32_16x16x32_bf16 v[212:215],a[108:111],a[212:215],v[212:215]
ds_read_b64_tr_b16 a[68:69],v50 offset:16384
v_mfma_f32_16x16x32_bf16 v[244:247],a[108:111],a[220:223],v[244:247]
v_mfma_f32_16x16x32_bf16 v[244:247],a[104:107],a[216:219],v[244:247]
ds_read_b64_tr_b16 a[70:71],v50 offset:18432
v_mfma_f32_16x16x32_bf16 v[152:155],a[112:115],a[192:195],v[152:155]
buffer_load_dword v8,v19,s[48:51],s90 offen
v_mfma_f32_16x16x32_bf16 v[152:155],a[116:119],a[196:199],v[152:155]
s_add_u32 s90,s90,896
ds_read_b64_tr_b16 a[72:73],v50 offset:8
v_mfma_f32_16x16x32_bf16 v[184:187],a[116:119],a[204:207],v[184:187]
ds_read_b64_tr_b16 a[74:75],v50 offset:2056
v_mfma_f32_16x16x32_bf16 v[184:187],a[112:115],a[200:203],v[184:187]
ds_read_b64_tr_b16 a[76:77],v50 offset:16392
v_mfma_f32_16x16x32_bf16 v[216:219],a[112:115],a[208:211],v[216:219]
ds_read_b64_tr_b16 a[78:79],v50 offset:18440
v_mfma_f32_16x16x32_bf16 v[216:219],a[116:119],a[212:215],v[216:219]
v_mfma_f32_16x16x32_bf16 v[248:251],a[116:119],a[220:223],v[248:251]
ds_read_b64_tr_b16 a[80:81],v50 offset:256
v_mfma_f32_16x16x32_bf16 v[248:251],a[112:115],a[216:219],v[248:251]
ds_read_b64_tr_b16 a[82:83],v50 offset:2304
v_mfma_f32_16x16x32_bf16 v[156:159],a[120:123],a[192:195],v[156:159]
ds_read_b64_tr_b16 a[84:85],v50 offset:16640
v_mfma_f32_16x16x32_bf16 v[156:159],a[124:127],a[196:199],v[156:159]
v_mfma_f32_16x16x32_bf16 v[188:191],a[124:127],a[204:207],v[188:191]
ds_read_b64_tr_b16 a[86:87],v50 offset:18688
v_mfma_f32_16x16x32_bf16 v[188:191],a[120:123],a[200:203],v[188:191]
ds_read_b64_tr_b16 a[88:89],v50 offset:264
v_mfma_f32_16x16x32_bf16 v[220:223],a[120:123],a[208:211],v[220:223]
)A"
R"A(ds_read_b64_tr_b16 a[90:91],v50 offset:2312
v_mfma_f32_16x16x32_bf16 v[220:223],a[124:127],a[212:215],v[220:223]
v_mfma_f32_16x16x32_bf16 v[252:255],a[124:127],a[220:223],v[252:255]
ds_read_b64_tr_b16 a[92:93],v50 offset:16648
v_mfma_f32_16x16x32_bf16 v[252:255],a[120:123],a[216:219],v[252:255]
ds_read_b64_tr_b16 a[94:95],v50 offset:18696
s_sub_u32 s70,s70,1
s_cmp_lg_u32 s70,0
s_cbranch_scc1 L_qp_%=
s_waitcnt lgkmcnt(6)
v_mfma_f32_16x16x32_bf16 a[0:3],a[64:67],a[224:227],a[0:3]
s_memrealtime s[92:93]
s_memtime s[56:57]
s_add_u32 m0,s85,0
v_mfma_f32_16x16x32_bf16 a[0:3],a[68:71],a[228:231],a[0:3]
buffer_load_dwordx4 v32,s[28:31],s84 offen lds
v_mfma_f32_16x16x32_bf16 a[16:19],a[68:71],a[236:239],a[16:19]
s_sub_u32 s83,s83,524288
ds_read_b64_tr_b16 a[96:97],v50 offset:512
v_subrev_u32 v40,256,v40
v_mfma_f32_16x16x32_bf16 a[16:19],a[64:67],a[232:235],a[16:19]
s_mov_b32 s69,s86
ds_read_b64_tr_b16 a[98:99],v50 offset:2560
v_subrev_u32 v41,256,v41
v_mfma_f32_16x16x32_bf16 a[32:35],a[64:67],a[240:243],a[32:35]
s_mov_b32 s86,s87
ds_read_b64_tr_b16 a[100:101],v50 offset:16896
v_subrev_u32 v42,256,v42
v_mfma_f32_16x16x32_bf16 a[32:35],a[68:71],a[244:247],a[32:35]
s_mov_b32 s87,s88
ds_read_b64_tr_b16 a[102:103],v50 offset:18944
v_subrev_u32 v43,256,v43
v_mfma_f32_16x16x32_bf16 a[48:51],a[68:71],a[252:255],a[48:51]
s_add_u32 s75,s75,16384
v_subrev_u32 v44,256,v44
v_subrev_u32 v45,256,v45
v_mfma_f32_16x16x32_bf16 a[48:51],a[64:67],a[248:251],a[48:51]
buffer_load_dwordx4 v33,s[28:31],s84 offen offset:1024 lds
v_mfma_f32_16x16x32_bf16 a[4:7],a[72:75],a[224:227],a[4:7]
s_sub_u32 s78,s77,32768
ds_read_b64_tr_b16 a[104:105],v50 offset:520
v_subrev_u32 v46,256,v46
v_mfma_f32_16x16x32_bf16 a[4:7],a[76:79],a[228:231],a[4:7]
L_pd2_%=:
s_cbranch_execz L_pd6_%=
s_and_b32 s78,s78,0x1ffff
ds_read_b64_tr_b16 a[106:107],v50 offset:2568
v_subrev_u32 v47,256,v47
v_mfma_f32_16x16x32_bf16 a[20:23],a[76:79],a[236:239],a[20:23]
s_sub_u32 s78,s78,s77
ds_read_b64_tr_b16 a[108:109],v50 offset:16904
v_xor_b32 v48,98304,v48
v_mfma_f32_16x16x32_bf16 a[20:23],a[72:75],a[232:235],a[20:23]
s_add_u32 s77,s77,s78
ds_read_b64_tr_b16 a[110:111],v50 offset:18952
v_xor_b32 v49,98304,v49
v_mfma_f32_16x16x32_bf16 a[36:39],a[72:75],a[240:243],a[36:39]
s_mov_b32 s6,0
v_add_u32 v52,s87,v51
v_mfma_f32_16x16x32_bf16 a[36:39],a[76:79],a[244:247],a[36:39]
buffer_load_dwordx4 v34,s[28:31],s84 offen offset:2048 lds
v_mfma_f32_16x16x32_bf16 a[52:55],a[76:79],a[252:255],a[52:55]
s_mul_i32 s66,s60,40960
ds_read_b64_tr_b16 a[112:113],v50 offset:768
v_add_u32 v11,0,v23
v_mfma_f32_16x16x32_bf16 a[52:55],a[72:75],a[248:251],a[52:55]
s_add_u32 s65,s63,1
ds_read_b64_tr_b16 a[114:115],v50 offset:2816
v_mul_u32_u24 v12,3277,v11
v_mfma_f32_16x16x32_bf16 a[8:11],a[80:83],a[224:227],a[8:11]
s_waitcnt lgkmcnt(5)
ds_read_b64_tr_b16 a[116:117],v50 offset:17152
v_lshrrev_b32 v12,16,v12
v_mfma_f32_16x16x32_bf16 a[8:11],a[84:87],a[228:231],a[8:11]
ds_read_b64_tr_b16 a[118:119],v50 offset:19200
v_mul_u32_u24 v13,20,v12
v_mfma_f32_16x16x32_bf16 a[24:27],a[84:87],a[236:239],a[24:27]
v_sub_u32 v13,v11,v13
v_bfe_u32 v14,v12,2,1
v_mfma_f32_16x16x32_bf16 a[24:27],a[80:83],a[232:235],a[24:27]
buffer_load_dwordx4 v35,s[28:31],s84 offen offset:3072 lds
v_mfma_f32_16x16x32_bf16 a[40:43],a[80:83],a[240:243],a[40:43]
ds_read_b64_tr_b16 a[120:121],v50 offset:776
v_lshlrev_b32 v14,1,v14
v_mfma_f32_16x16x32_bf16 a[40:43],a[84:87],a[244:247],a[40:43]
ds_read_b64_tr_b16 a[122:123],v50 offset:2824
v_xor_b32 v13,v13,v14
v_mfma_f32_16x16x32_bf16 a[56:59],a[84:87],a[252:255],a[56:59]
ds_read_b64_tr_b16 a[124:125],v50 offset:17160
v_lshrrev_b32 v14,2,v13
v_mfma_f32_16x16x32_bf16 a[56:59],a[80:83],a[248:251],a[56:59]
s_add_u32 m0,s85,4096
s_xor_b32 s85,s85,98304
ds_read_b64_tr_b16 a[126:127],v50 offset:19208
v_mfma_f32_16x16x32_bf16 a[12:15],a[88:91],a[224:227],a[12:15]
v_cmp_eq_u32 vcc,3,v14
v_min_u32 v15,3,v14
v_mfma_f32_16x16x32_bf16 a[12:15],a[92:95],a[228:231],a[12:15]
buffer_load_dwordx4 v36,s[28:31],s84 offen lds
v_mfma_f32_16x16x32_bf16 a[28:31],a[92:95],a[236:239],a[28:31]
ds_read_b64_tr_b16 a[64:65],v50 offset:1024
v_add_u32 v15,s65,v15
v_mfma_f32_16x16x32_bf16 a[28:31],a[88:91],a[232:235],a[28:31]
ds_read_b64_tr_b16 a[66:67],v50 offset:3072
v_and_b32 v15,3,v15
v_mfma_f32_16x16x32_bf16 a[44:47],a[88:91],a[240:243],a[44:47]
ds_read_b64_tr_b16 a[68:69],v50 offset:17408
v_cndmask_b32_e64 v15,v15,4,vcc
v_mfma_f32_16x16x32_bf16 a[44:47],a[92:95],a[244:247],a[44:47]
ds_read_b64_tr_b16 a[70:71],v50 offset:19456
v_and_b32 v13,3,v13
v_mfma_f32_16x16x32_bf16 a[60:63],a[92:95],a[252:255],a[60:63]
v_lshl_add_u32 v13,v15,2,v13
v_mul_u32_u24 v12,1280,v12
v_mfma_f32_16x16x32_bf16 a[60:63],a[88:91],a[248:251],a[60:63]
buffer_load_dwordx4 v37,s[28:31],s84 offen offset:1024 lds
v_mfma_f32_16x16x32_bf16 v[64:67],a[96:99],a[224:227],v[64:67]
ds_read_b64_tr_b16 a[72:73],v50 offset:1032
v_lshl_add_u32 v12,v13,4,v12
v_mfma_f32_16x16x32_bf16 v[64:67],a[100:103],a[228:231],v[64:67]
ds_read_b64_tr_b16 a[74:75],v50 offset:3080
v_add_u32 v12,s66,v12
v_mfma_f32_16x16x32_bf16 v[80:83],a[100:103],a[236:239],v[80:83]
ds_read_b64_tr_b16 a[76:77],v50 offset:17416
v_subrev_u32 v24,0,v12
v_mfma_f32_16x16x32_bf16 v[80:83],a[96:99],a[232:235],v[80:83]
ds_read_b64_tr_b16 a[78:79],v50 offset:19464
v_add_u32 v11,64,v23
v_mfma_f32_16x16x32_bf16 v[96:99],a[96:99],a[240:243],v[96:99]
v_mul_u32_u24 v12,3277,v11
v_lshrrev_b32 v12,16,v12
v_mfma_f32_16x16x32_bf16 v[96:99],a[100:103],a[244:247],v[96:99]
buffer_load_dwordx4 v38,s[28:31],s84 offen offset:2048 lds
v_mfma_f32_16x16x32_bf16 v[112:115],a[100:103],a[252:255],v[112:115]
ds_read_b64_tr_b16 a[80:81],v50 offset:1280
v_mul_u32_u24 v13,20,v12
v_mfma_f32_16x16x32_bf16 v[112:115],a[96:99],a[248:251],v[112:115]
s_waitcnt lgkmcnt(5)
ds_read_b64_tr_b16 a[82:83],v50 offset:3328
v_sub_u32 v13,v11,v13
v_mfma_f32_16x16x32_bf16 v[68:71],a[104:107],a[224:227],v[68:71]
ds_read_b64_tr_b16 a[84:85],v50 offset:17664
v_bfe_u32 v14,v12,2,1
v_mfma_f32_16x16x32_bf16 v[68:71],a[108:111],a[228:231],v[68:71]
ds_read_b64_tr_b16 a[86:87],v50 offset:19712
v_lshlrev_b32 v14,1,v14
v_mfma_f32_16x16x32_bf16 v[84:87],a[108:111],a[236:239],v[84:87]
v_xor_b32 v13,v13,v14
v_lshrrev_b32 v14,2,v13
v_mfma_f32_16x16x32_bf16 v[84:87],a[104:107],a[232:235],v[84:87]
buffer_load_dwordx4 v39,s[28:31],s84 offen offset:3072 lds
v_mfma_f32_16x16x32_bf16 v[100:103],a[104:107],a[240:243],v[100:103]
s_add_u32 s84,s84,128
ds_read_b64_tr_b16 a[88:89],v50 offset:1288
v_cmp_eq_u32 vcc,3,v14
v_mfma_f32_16x16x32_bf16 v[100:103],a[108:111],a[244:247],v[100:103]
ds_read_b64_tr_b16 a[90:91],v50 offset:3336
v_min_u32 v15,3,v14
v_mfma_f32_16x16x32_bf16 v[116:119],a[108:111],a[252:255],v[116:119]
ds_read_b64_tr_b16 a[92:93],v50 offset:17672
v_add_u32 v15,s65,v15
v_mfma_f32_16x16x32_bf16 v[116:119],a[104:107],a[248:251],v[116:119]
s_add_u32 m0,s88,0
ds_read_b64_tr_b16 a[94:95],v50 offset:19720
v_and_b32 v15,3,v15
v_mfma_f32_16x16x32_bf16 v[72:75],a[112:115],a[224:227],v[72:75]
v_cndmask_b32_e64 v15,v15,4,vcc
v_and_b32 v13,3,v13
v_mfma_f32_16x16x32_bf16 v[72:75],a[116:119],a[228:231],v[72:75]
buffer_load_dwordx4 v40,s[36:39],s83 offen lds
v_mfma_f32_16x16x32_bf16 v[88:91],a[116:119],a[236:239],v[88:91]
ds_read_b64_tr_b16 a[96:97],v50 offset:1536
v_lshl_add_u32 v13,v15,2,v13
v_mfma_f32_16x16x32_bf16 v[88:91],a[112:115],a[232:235],v[88:91]
ds_read_b64_tr_b16 a[98:99],v50 offset:3584
v_mul_u32_u24 v12,1280,v12
v_mfma_f32_16x16x32_bf16 v[104:107],a[112:115],a[240:243],v[104:107]
ds_read_b64_tr_b16 a[100:101],v50 offset:17920
v_lshl_add_u32 v12,v13,4,v12
v_mfma_f32_16x16x32_bf16 v[104:107],a[116:119],a[244:247],v[104:107]
s_add_u32 m0,s88,1024
ds_read_b64_tr_b16 a[102:103],v50 offset:19968
v_add_u32 v12,s66,v12
v_mfma_f32_16x16x32_bf16 v[120:123],a[116:119],a[252:255],v[120:123]
v_subrev_u32 v25,1024,v12
v_add_u32 v11,128,v23
v_mfma_f32_16x16x32_bf16 v[120:123],a[112:115],a[248:251],v[120:123]
buffer_load_dwordx4 v41,s[36:39],s83 offen lds
v_mfma_f32_16x16x32_bf16 v[76:79],a[120:123],a[224:227],v[76:79]
ds_read_b64_tr_b16 a[104:105],v50 offset:1544
v_mul_u32_u24 v12,3277,v11
v_mfma_f32_16x16x32_bf16 v[76:79],a[124:127],a[228:231],v[76:79]
ds_read_b64_tr_b16 a[106:107],v50 offset:3592
v_lshrrev_b32 v12,16,v12
v_mfma_f32_16x16x32_bf16 v[92:95],a[124:127],a[236:239],v[92:95]
ds_read_b64_tr_b16 a[108:109],v50 offset:17928
v_mul_u32_u24 v13,20,v12
v_mfma_f32_16x16x32_bf16 v[92:95],a[120:123],a[232:235],v[92:95]
s_add_u32 m0,s88,2048
ds_read_b64_tr_b16 a[110:111],v50 offset:19976
v_sub_u32 v13,v11,v13
v_mfma_f32_16x16x32_bf16 v[108:111],a[120:123],a[240:243],v[108:111]
v_bfe_u32 v14,v12,2,1
v_lshlrev_b32 v14,1,v14
v_mfma_f32_16x16x32_bf16 v[108:111],a[124:127],a[244:247],v[108:111]
buffer_load_dwordx4 v42,s[36:39],s83 offen lds
v_mfma_f32_16x16x32_bf16 v[124:127],a[124:127],a[252:255],v[124:127]
ds_read_b64_tr_b16 a[112:113],v50 offset:1792
v_xor_b32 v13,v13,v14
v_mfma_f32_16x16x32_bf16 v[124:127],a[120:123],a[248:251],v[124:127]
ds_read_b64_tr_b16 a[114:115],v50 offset:3840
v_lshrrev_b32 v14,2,v13
v_mfma_f32_16x16x32_bf16 v[128:131],a[64:67],a[224:227],v[128:131]
ds_read_b64_tr_b16 a[116:117],v50 offset:18176
v_cmp_eq_u32 vcc,3,v14
v_mfma_f32_16x16x32_bf16 v[128:131],a[68:71],a[228:231],v[128:131]
s_add_u32 m0,s88,3072
ds_read_b64_tr_b16 a[118:119],v50 offset:20224
v_min_u32 v15,3,v14
v_mfma_f32_16x16x32_bf16 v[160:163],a[68:71],a[236:239],v[160:163]
v_add_u32 v15,s65,v15
v_and_b32 v15,3,v15
v_mfma_f32_16x16x32_bf16 v[160:163],a[64:67],a[232:235],v[160:163]
buffer_load_dwordx4 v43,s[36:39],s83 offen lds
v_mfma_f32_16x16x32_bf16 v[192:195],a[64:67],a[240:243],v[192:195]
ds_read_b64_tr_b16 a[120:121],v50 offset:1800
v_cndmask_b32_e64 v15,v15,4,vcc
v_mfma_f32_16x16x32_bf16 v[192:195],a[68:71],a[244:247],v[192:195]
ds_read_b64_tr_b16 a[122:123],v50 offset:3848
v_and_b32 v13,3,v13
v_mfma_f32_16x16x32_bf16 v[224:227],a[68:71],a[252:255],v[224:227]
s_waitcnt lgkmcnt(4)
ds_read_b64_tr_b16 a[124:125],v50 offset:18184
v_lshl_add_u32 v13,v15,2,v13
v_mfma_f32_16x16x32_bf16 v[224:227],a[64:67],a[248:251],v[224:227]
s_add_u32 m0,s88,4096
ds_read_b64_tr_b16 a[126:127],v50 offset:20232
v_add_u32 v50,s86,v51
v_mfma_f32_16x16x32_bf16 v[132:135],a[72:75],a[224:227],v[132:135]
v_mul_u32_u24 v12,1280,v12
v_lshl_add_u32 v12,v13,4,v12
v_mfma_f32_16x16x32_bf16 v[132:135],a[76:79],a[228:231],v[132:135]
buffer_load_dwordx4 v44,s[36:39],s83 offen lds
v_mfma_f32_16x16x32_bf16 v[164:167],a[76:79],a[236:239],v[164:167]
v_add_u32 v12,s66,v12
v_subrev_u32 v26,2048,v12
v_mfma_f32_16x16x32_bf16 v[164:167],a[72:75],a[232:235],v[164:167]
v_add_u32 v11,192,v23
v_mul_u32_u24 v12,3277,v11
v_mfma_f32_16x16x32_bf16 v[196:199],a[72:75],a[240:243],v[196:199]
v_lshrrev_b32 v12,16,v12
v_mul_u32_u24 v13,20,v12
v_mfma_f32_16x16x32_bf16 v[196:199],a[76:79],a[244:247],v[196:199]
s_add_u32 m0,s88,5120
v_sub_u32 v13,v11,v13
v_bfe_u32 v14,v12,2,1
v_mfma_f32_16x16x32_bf16 v[228:231],a[76:79],a[252:255],v[228:231]
v_lshlrev_b32 v14,1,v14
v_xor_b32 v13,v13,v14
v_mfma_f32_16x16x32_bf16 v[228:231],a[72:75],a[248:251],v[228:231]
buffer_load_dwordx4 v45,s[36:39],s83 offen lds
v_mfma_f32_16x16x32_bf16 v[136:139],a[80:83],a[224:227],v[136:139]
v_lshrrev_b32 v14,2,v13
v_cmp_eq_u32 vcc,3,v14
v_mfma_f32_16x16x32_bf16 v[136:139],a[84:87],a[228:231],v[136:139]
v_min_u32 v15,3,v14
v_add_u32 v15,s65,v15
v_mfma_f32_16x16x32_bf16 v[168:171],a[84:87],a[236:239],v[168:171]
v_and_b32 v15,3,v15
v_cndmask_b32_e64 v15,v15,4,vcc
v_mfma_f32_16x16x32_bf16 v[168:171],a[80:83],a[232:235],v[168:171]
s_add_u32 m0,s88,6144
v_and_b32 v13,3,v13
v_lshl_add_u32 v13,v15,2,v13
v_mfma_f32_16x16x32_bf16 v[200:203],a[80:83],a[240:243],v[200:203]
v_mul_u32_u24 v12,1280,v12
v_lshl_add_u32 v12,v13,4,v12
v_mfma_f32_16x16x32_bf16 v[200:203],a[84:87],a[244:247],v[200:203]
buffer_load_dwordx4 v46,s[36:39],s83 offen lds
v_mfma_f32_16x16x32_bf16 v[232:235],a[84:87],a[252:255],v[232:235]
v_add_u32 v12,s66,v12
v_subrev_u32 v27,3072,v12
v_mfma_f32_16x16x32_bf16 v[232:235],a[80:83],a[248:251],v[232:235]
v_add_u32 v11,256,v23
v_mul_u32_u24 v12,3277,v11
)A"
R"A(v_mfma_f32_16x16x32_bf16 v[140:143],a[88:91],a[224:227],v[140:143]
v_lshrrev_b32 v12,16,v12
v_mul_u32_u24 v13,20,v12
v_mfma_f32_16x16x32_bf16 v[140:143],a[92:95],a[228:231],v[140:143]
s_add_u32 m0,s88,7168
v_sub_u32 v13,v11,v13
v_bfe_u32 v14,v12,2,1
v_mfma_f32_16x16x32_bf16 v[172:175],a[92:95],a[236:239],v[172:175]
s_mov_b32 s88,s69
v_lshlrev_b32 v14,1,v14
v_xor_b32 v13,v13,v14
v_mfma_f32_16x16x32_bf16 v[172:175],a[88:91],a[232:235],v[172:175]
buffer_load_dwordx4 v47,s[36:39],s83 offen lds
v_mfma_f32_16x16x32_bf16 v[204:207],a[88:91],a[240:243],v[204:207]
s_waitcnt lgkmcnt(0)
s_add_u32 s83,s83,524288
s_sub_u32 s92,s92,s8
v_mfma_f32_16x16x32_bf16 v[204:207],a[92:95],a[244:247],v[204:207]
s_sub_u32 s56,s56,s4
s_mov_b32 s93,782
s_mul_i32 s92,s92,24
v_mfma_f32_16x16x32_bf16 v[236:239],a[92:95],a[252:255],v[236:239]
s_cmp_ge_u32 s63,2
s_cselect_b32 s93,819,s93
s_mul_i32 s93,s93,24
v_mfma_f32_16x16x32_bf16 v[236:239],a[88:91],a[248:251],v[236:239]
s_add_u32 s10,s20,s74
s_addc_u32 s11,s21,0
s_add_u32 s93,s93,s56
s_waitcnt vmcnt(25)
s_barrier
v_mfma_f32_16x16x32_bf16 v[144:147],a[96:99],a[224:227],v[144:147]
s_add_u32 m0,s76,0
s_add_u32 s92,s92,12600
s_waitcnt vmcnt(17)
ds_read_b128 a[192:195],v48
v_mfma_f32_16x16x32_bf16 v[144:147],a[100:103],a[228:231],v[144:147]
s_and_b32 s69,s75,0xfffc0000
ds_read_b128 a[196:199],v49
v_lshrrev_b32 v14,2,v13
v_mfma_f32_16x16x32_bf16 v[176:179],a[100:103],a[236:239],v[176:179]
global_load_lds_dwordx4 v4,s[10:11] nt
ds_read_b128 a[200:203],v48 offset:2048
v_mfma_f32_16x16x32_bf16 v[176:179],a[96:99],a[232:235],v[176:179]
s_add_u32 s75,s75,16384
ds_read_b128 a[204:207],v49 offset:2048
v_cmp_eq_u32 vcc,3,v14
v_mfma_f32_16x16x32_bf16 v[208:211],a[96:99],a[240:243],v[208:211]
v_min_u32 v15,3,v14
v_add_u32 v15,s65,v15
v_mfma_f32_16x16x32_bf16 v[208:211],a[100:103],a[244:247],v[208:211]
s_mov_b32 s65,262144
ds_read_b128 a[208:211],v48 offset:4096
v_and_b32 v15,3,v15
v_mfma_f32_16x16x32_bf16 v[240:243],a[100:103],a[252:255],v[240:243]
ds_read_b128 a[212:215],v49 offset:4096
v_cndmask_b32_e64 v15,v15,4,vcc
v_mfma_f32_16x16x32_bf16 v[240:243],a[96:99],a[248:251],v[240:243]
ds_read_b128 a[216:219],v48 offset:6144
v_xor_b32 v48,98304,v48
v_mfma_f32_16x16x32_bf16 v[148:151],a[104:107],a[224:227],v[148:151]
v_and_b32 v13,3,v13
v_lshl_add_u32 v13,v15,2,v13
v_mfma_f32_16x16x32_bf16 v[148:151],a[108:111],a[228:231],v[148:151]
ds_read_b128 a[220:223],v49 offset:6144
v_xor_b32 v49,98304,v49
v_mfma_f32_16x16x32_bf16 v[180:183],a[108:111],a[236:239],v[180:183]
ds_read_b64_tr_b16 a[64:65],v50
v_mul_u32_u24 v12,1280,v12
v_mfma_f32_16x16x32_bf16 v[180:183],a[104:107],a[232:235],v[180:183]
ds_read_b64_tr_b16 a[66:67],v50 offset:2048
v_lshl_add_u32 v12,v13,4,v12
v_mfma_f32_16x16x32_bf16 v[212:215],a[104:107],a[240:243],v[212:215]
v_add_u32 v12,s66,v12
v_subrev_u32 v7,0,v12
v_mfma_f32_16x16x32_bf16 v[212:215],a[108:111],a[244:247],v[212:215]
ds_read_b64_tr_b16 a[68:69],v50 offset:16384
v_mfma_f32_16x16x32_bf16 v[244:247],a[108:111],a[252:255],v[244:247]
ds_read_b64_tr_b16 a[70:71],v50 offset:18432
v_mfma_f32_16x16x32_bf16 v[244:247],a[104:107],a[248:251],v[244:247]
ds_read_b64_tr_b16 a[72:73],v50 offset:8
v_mfma_f32_16x16x32_bf16 v[152:155],a[112:115],a[224:227],v[152:155]
v_mfma_f32_16x16x32_bf16 v[152:155],a[116:119],a[228:231],v[152:155]
ds_read_b64_tr_b16 a[74:75],v50 offset:2056
v_mfma_f32_16x16x32_bf16 v[184:187],a[116:119],a[236:239],v[184:187]
global_load_lds_dwordx4 v4,s[10:11] offset:1024 nt
ds_read_b64_tr_b16 a[76:77],v50 offset:16392
v_mfma_f32_16x16x32_bf16 v[184:187],a[112:115],a[232:235],v[184:187]
ds_read_b64_tr_b16 a[78:79],v50 offset:18440
v_mfma_f32_16x16x32_bf16 v[216:219],a[112:115],a[240:243],v[216:219]
v_mfma_f32_16x16x32_bf16 v[216:219],a[116:119],a[244:247],v[216:219]
ds_read_b64_tr_b16 a[80:81],v50 offset:1024
v_mfma_f32_16x16x32_bf16 v[248:251],a[116:119],a[252:255],v[248:251]
ds_read_b64_tr_b16 a[82:83],v50 offset:3072
v_mfma_f32_16x16x32_bf16 v[248:251],a[112:115],a[248:251],v[248:251]
ds_read_b64_tr_b16 a[84:85],v50 offset:17408
v_mfma_f32_16x16x32_bf16 v[156:159],a[120:123],a[224:227],v[156:159]
v_mfma_f32_16x16x32_bf16 v[156:159],a[124:127],a[228:231],v[156:159]
ds_read_b64_tr_b16 a[86:87],v50 offset:19456
v_mfma_f32_16x16x32_bf16 v[188:191],a[124:127],a[236:239],v[188:191]
ds_read_b64_tr_b16 a[88:89],v50 offset:1032
v_mfma_f32_16x16x32_bf16 v[188:191],a[120:123],a[232:235],v[188:191]
s_cmp_lt_u32 s92,s93
s_cselect_b32 s5,6,0
s_cselect_b32 s7,3,0
s_cselect_b32 s9,8,10
ds_read_b64_tr_b16 a[90:91],v50 offset:3080
v_mfma_f32_16x16x32_bf16 v[220:223],a[120:123],a[240:243],v[220:223]
s_sub_u32 s93,s93,s92
s_max_i32 s93,s93,0
s_min_u32 s93,s93,65535
v_mfma_f32_16x16x32_bf16 v[220:223],a[124:127],a[244:247],v[220:223]
s_mul_i32 s93,s93,195
s_lshr_b32 s93,s93,16
ds_read_b64_tr_b16 a[92:93],v50 offset:17416
v_mfma_f32_16x16x32_bf16 v[252:255],a[124:127],a[252:255],v[252:255]
s_add_u32 s93,s93,1
s_max_i32 s93,s93,1
ds_read_b64_tr_b16 a[94:95],v50 offset:19464
v_mfma_f32_16x16x32_bf16 v[252:255],a[120:123],a[248:251],v[252:255]
s_min_u32 s93,s93,6
s_waitcnt vmcnt(10)
ds_read_b128 a[224:227],v48
s_waitcnt lgkmcnt(6)
v_mfma_f32_16x16x32_bf16 v[128:131],a[64:67],a[192:195],v[128:131]
s_cmp_lg_u32 s7,0
s_cselect_b32 s7,s93,0
s_add_u32 s93,s93,3
v_mfma_f32_16x16x32_bf16 v[128:131],a[68:71],a[196:199],v[128:131]
s_min_u32 s93,s93,7
ds_read_b128 a[228:231],v49
v_mfma_f32_16x16x32_bf16 v[160:163],a[68:71],a[204:207],v[160:163]
global_load_lds_dwordx4 v5,s[10:11] offset:2048 nt
ds_read_b128 a[236:239],v49 offset:2048
v_mfma_f32_16x16x32_bf16 v[160:163],a[64:67],a[200:203],v[160:163]
s_cmp_lg_u32 s7,0
s_cselect_b32 s5,s93,0
ds_read_b128 a[232:235],v48 offset:2048
v_mfma_f32_16x16x32_bf16 v[192:195],a[64:67],a[208:211],v[192:195]
s_mov_b32 s4,s5
v_mfma_f32_16x16x32_bf16 v[192:195],a[68:71],a[212:215],v[192:195]
ds_read_b128 a[240:243],v48 offset:4096
v_mfma_f32_16x16x32_bf16 v[224:227],a[68:71],a[220:223],v[224:227]
ds_read_b128 a[244:247],v49 offset:4096
v_mfma_f32_16x16x32_bf16 v[224:227],a[64:67],a[216:219],v[224:227]
ds_read_b128 a[252:255],v49 offset:6144
v_mfma_f32_16x16x32_bf16 v[132:135],a[72:75],a[192:195],v[132:135]
v_mfma_f32_16x16x32_bf16 v[132:135],a[76:79],a[196:199],v[132:135]
ds_read_b128 a[248:251],v48 offset:6144
v_mfma_f32_16x16x32_bf16 v[164:167],a[76:79],a[204:207],v[164:167]
ds_read_b64_tr_b16 a[96:97],v50 offset:256
v_mfma_f32_16x16x32_bf16 v[164:167],a[72:75],a[200:203],v[164:167]
ds_read_b64_tr_b16 a[98:99],v50 offset:2304
v_mfma_f32_16x16x32_bf16 v[196:199],a[72:75],a[208:211],v[196:199]
v_mfma_f32_16x16x32_bf16 v[196:199],a[76:79],a[212:215],v[196:199]
ds_read_b64_tr_b16 a[100:101],v50 offset:16640
v_mfma_f32_16x16x32_bf16 v[228:231],a[76:79],a[220:223],v[228:231]
ds_read_b64_tr_b16 a[102:103],v50 offset:18688
v_mfma_f32_16x16x32_bf16 v[228:231],a[72:75],a[216:219],v[228:231]
s_waitcnt lgkmcnt(5)
ds_read_b64_tr_b16 a[104:105],v50 offset:1280
v_mfma_f32_16x16x32_bf16 v[128:131],a[80:83],a[224:227],v[128:131]
v_mfma_f32_16x16x32_bf16 v[128:131],a[84:87],a[228:231],v[128:131]
ds_read_b64_tr_b16 a[106:107],v50 offset:3328
v_mfma_f32_16x16x32_bf16 v[160:163],a[84:87],a[236:239],v[160:163]
global_load_lds_dwordx4 v5,s[10:11] offset:3072
ds_read_b64_tr_b16 a[108:109],v50 offset:17664
v_mfma_f32_16x16x32_bf16 v[160:163],a[80:83],a[232:235],v[160:163]
ds_read_b64_tr_b16 a[110:111],v50 offset:19712
v_mfma_f32_16x16x32_bf16 v[192:195],a[80:83],a[240:243],v[192:195]
v_mfma_f32_16x16x32_bf16 v[192:195],a[84:87],a[244:247],v[192:195]
ds_read_b64_tr_b16 a[112:113],v50 offset:264
v_mul_f32 v128,s71,v128
v_mfma_f32_16x16x32_bf16 v[224:227],a[84:87],a[252:255],v[224:227]
s_waitcnt lgkmcnt(5)
ds_read_b64_tr_b16 a[114:115],v50 offset:2312
v_mul_f32 v129,s71,v129
v_mfma_f32_16x16x32_bf16 v[224:227],a[80:83],a[248:251],v[224:227]
ds_read_b64_tr_b16 a[116:117],v50 offset:16648
v_mul_f32 v130,s71,v130
v_mfma_f32_16x16x32_bf16 v[132:135],a[88:91],a[224:227],v[132:135]
v_mul_f32 v131,s71,v131
v_cvt_pk_bf16_f32 v128,v128,v129
v_mfma_f32_16x16x32_bf16 v[132:135],a[92:95],a[228:231],v[132:135]
ds_read_b64_tr_b16 a[118:119],v50 offset:18696
v_cvt_pk_bf16_f32 v129,v130,v131
v_mfma_f32_16x16x32_bf16 v[164:167],a[92:95],a[236:239],v[164:167]
ds_read_b64_tr_b16 a[120:121],v50 offset:1288
v_mul_f32 v192,s71,v192
v_mfma_f32_16x16x32_bf16 v[164:167],a[88:91],a[232:235],v[164:167]
ds_read_b64_tr_b16 a[122:123],v50 offset:3336
v_mul_f32 v193,s71,v193
v_mfma_f32_16x16x32_bf16 v[196:199],a[88:91],a[240:243],v[196:199]
v_mul_f32 v132,s71,v132
v_mul_f32 v133,s71,v133
v_mfma_f32_16x16x32_bf16 v[196:199],a[92:95],a[244:247],v[196:199]
ds_read_b64_tr_b16 a[124:125],v50 offset:17672
v_mul_f32 v134,s71,v134
v_mfma_f32_16x16x32_bf16 v[228:231],a[92:95],a[252:255],v[228:231]
s_add_u32 s10,s24,s74
s_addc_u32 s11,s25,0
ds_read_b64_tr_b16 a[126:127],v50 offset:19720
v_mfma_f32_16x16x32_bf16 v[228:231],a[88:91],a[248:251],v[228:231]
s_add_u32 s74,s74,16384
s_and_b32 s74,s74,0x3ffff
ds_read_b64_tr_b16 a[128:129],v50 offset:512
v_mfma_f32_16x16x32_bf16 v[136:139],a[96:99],a[192:195],v[136:139]
s_add_u32 m0,s76,16384
s_or_b32 s74,s74,s69
s_sub_u32 s76,s76,32768
v_mfma_f32_16x16x32_bf16 v[136:139],a[100:103],a[196:199],v[136:139]
s_and_b32 s76,s76,0x1ffff
s_and_b32 s69,s75,0xfffc0000
ds_read_b64_tr_b16 a[130:131],v50 offset:2560
s_waitcnt lgkmcnt(6)
v_mfma_f32_16x16x32_bf16 v[136:139],a[104:107],a[224:227],v[136:139]
global_load_lds_dwordx4 v4,s[10:11]
ds_read_b64_tr_b16 a[132:133],v50 offset:16896
v_mfma_f32_16x16x32_bf16 v[136:139],a[108:111],a[228:231],v[136:139]
s_add_u32 s75,s75,16384
ds_read_b64_tr_b16 a[134:135],v50 offset:18944
v_mul_f32 v135,s71,v135
v_mfma_f32_16x16x32_bf16 v[168:171],a[108:111],a[236:239],v[168:171]
v_cvt_pk_bf16_f32 v130,v132,v133
v_cvt_pk_bf16_f32 v131,v134,v135
v_mfma_f32_16x16x32_bf16 v[168:171],a[104:107],a[232:235],v[168:171]
ds_read_b64_tr_b16 a[136:137],v50 offset:1536
v_mul_f32 v194,s71,v194
v_mfma_f32_16x16x32_bf16 v[168:171],a[100:103],a[204:207],v[168:171]
ds_read_b64_tr_b16 a[138:139],v50 offset:3584
v_mul_f32 v195,s71,v195
v_mfma_f32_16x16x32_bf16 v[168:171],a[96:99],a[200:203],v[168:171]
ds_read_b64_tr_b16 a[140:141],v50 offset:17920
v_mul_f32 v196,s71,v196
v_mfma_f32_16x16x32_bf16 v[200:203],a[96:99],a[208:211],v[200:203]
v_mul_f32 v197,s71,v197
v_mul_f32 v198,s71,v198
v_mfma_f32_16x16x32_bf16 v[200:203],a[100:103],a[212:215],v[200:203]
ds_read_b64_tr_b16 a[142:143],v50 offset:19968
v_mul_f32 v199,s71,v199
v_mfma_f32_16x16x32_bf16 v[200:203],a[104:107],a[240:243],v[200:203]
ds_read_b64_tr_b16 a[144:145],v50 offset:520
v_cvt_pk_bf16_f32 v192,v192,v193
v_mfma_f32_16x16x32_bf16 v[200:203],a[108:111],a[244:247],v[200:203]
ds_read_b64_tr_b16 a[146:147],v50 offset:2568
v_cvt_pk_bf16_f32 v193,v194,v195
v_mfma_f32_16x16x32_bf16 v[232:235],a[108:111],a[252:255],v[232:235]
v_cvt_pk_bf16_f32 v194,v196,v197
v_cvt_pk_bf16_f32 v195,v198,v199
v_mfma_f32_16x16x32_bf16 v[232:235],a[104:107],a[248:251],v[232:235]
ds_read_b64_tr_b16 a[148:149],v50 offset:16904
v_mul_f32 v136,s71,v136
v_mfma_f32_16x16x32_bf16 v[232:235],a[100:103],a[220:223],v[232:235]
ds_read_b64_tr_b16 a[150:151],v50 offset:18952
v_mul_f32 v137,s71,v137
v_mfma_f32_16x16x32_bf16 v[232:235],a[96:99],a[216:219],v[232:235]
ds_read_b64_tr_b16 a[152:153],v50 offset:1544
v_mul_f32 v138,s71,v138
v_mfma_f32_16x16x32_bf16 v[140:143],a[112:115],a[192:195],v[140:143]
v_mul_f32 v139,s71,v139
v_cvt_pk_bf16_f32 v136,v136,v137
v_mfma_f32_16x16x32_bf16 v[140:143],a[116:119],a[196:199],v[140:143]
s_waitcnt lgkmcnt(5)
ds_read_b64_tr_b16 a[154:155],v50 offset:3592
v_cvt_pk_bf16_f32 v137,v138,v139
v_mfma_f32_16x16x32_bf16 v[140:143],a[120:123],a[224:227],v[140:143]
global_load_lds_dwordx4 v5,s[10:11] offset:1024
ds_read_b64_tr_b16 a[156:157],v50 offset:17928
v_mfma_f32_16x16x32_bf16 v[140:143],a[124:127],a[228:231],v[140:143]
ds_read_b64_tr_b16 a[158:159],v50 offset:19976
v_mul_f32 v160,s71,v160
v_mfma_f32_16x16x32_bf16 v[172:175],a[124:127],a[236:239],v[172:175]
v_mul_f32 v161,s71,v161
v_mul_f32 v162,s71,v162
v_mfma_f32_16x16x32_bf16 v[172:175],a[120:123],a[232:235],v[172:175]
)A"
R"A(ds_read_b64_tr_b16 a[160:161],v50 offset:768
v_mul_f32 v163,s71,v163
v_mfma_f32_16x16x32_bf16 v[172:175],a[116:119],a[204:207],v[172:175]
ds_read_b64_tr_b16 a[162:163],v50 offset:2816
v_mul_f32 v140,s71,v140
v_mfma_f32_16x16x32_bf16 v[172:175],a[112:115],a[200:203],v[172:175]
ds_read_b64_tr_b16 a[164:165],v50 offset:17152
v_mul_f32 v141,s71,v141
v_mfma_f32_16x16x32_bf16 v[204:207],a[112:115],a[208:211],v[204:207]
v_mul_f32 v142,s71,v142
v_mul_f32 v143,s71,v143
v_mfma_f32_16x16x32_bf16 v[204:207],a[116:119],a[212:215],v[204:207]
ds_read_b64_tr_b16 a[166:167],v50 offset:19200
v_cvt_pk_bf16_f32 v138,v140,v141
v_mfma_f32_16x16x32_bf16 v[204:207],a[120:123],a[240:243],v[204:207]
ds_read_b64_tr_b16 a[168:169],v50 offset:1792
v_cvt_pk_bf16_f32 v139,v142,v143
v_mfma_f32_16x16x32_bf16 v[204:207],a[124:127],a[244:247],v[204:207]
ds_read_b64_tr_b16 a[170:171],v50 offset:3840
v_mul_f32 v164,s71,v164
v_mfma_f32_16x16x32_bf16 v[236:239],a[124:127],a[252:255],v[236:239]
v_mul_f32 v165,s71,v165
v_mul_f32 v166,s71,v166
v_mfma_f32_16x16x32_bf16 v[236:239],a[120:123],a[248:251],v[236:239]
ds_read_b64_tr_b16 a[172:173],v50 offset:18176
v_mul_f32 v167,s71,v167
v_mfma_f32_16x16x32_bf16 v[236:239],a[116:119],a[220:223],v[236:239]
ds_read_b64_tr_b16 a[174:175],v50 offset:20224
v_cvt_pk_bf16_f32 v160,v160,v161
v_mfma_f32_16x16x32_bf16 v[236:239],a[112:115],a[216:219],v[236:239]
ds_read_b64_tr_b16 a[176:177],v50 offset:776
v_cvt_pk_bf16_f32 v161,v162,v163
v_mfma_f32_16x16x32_bf16 v[144:147],a[128:131],a[192:195],v[144:147]
v_cvt_pk_bf16_f32 v162,v164,v165
v_cvt_pk_bf16_f32 v163,v166,v167
v_mfma_f32_16x16x32_bf16 v[144:147],a[132:135],a[196:199],v[144:147]
ds_read_b64_tr_b16 a[178:179],v50 offset:2824
v_mul_f32 v224,s71,v224
v_mfma_f32_16x16x32_bf16 v[144:147],a[136:139],a[224:227],v[144:147]
global_load_lds_dwordx4 v4,s[10:11] offset:2048
ds_read_b64_tr_b16 a[180:181],v50 offset:17160
v_mfma_f32_16x16x32_bf16 v[144:147],a[140:143],a[228:231],v[144:147]
ds_read_b64_tr_b16 a[182:183],v50 offset:19208
v_mul_f32 v225,s71,v225
v_mfma_f32_16x16x32_bf16 v[176:179],a[140:143],a[236:239],v[176:179]
v_mul_f32 v226,s71,v226
v_mul_f32 v227,s71,v227
v_mfma_f32_16x16x32_bf16 v[176:179],a[136:139],a[232:235],v[176:179]
ds_read_b64_tr_b16 a[184:185],v50 offset:1800
v_mul_f32 v228,s71,v228
v_mfma_f32_16x16x32_bf16 v[176:179],a[132:135],a[204:207],v[176:179]
ds_read_b64_tr_b16 a[186:187],v50 offset:3848
v_mul_f32 v144,s71,v144
v_mfma_f32_16x16x32_bf16 v[176:179],a[128:131],a[200:203],v[176:179]
ds_read_b64_tr_b16 a[188:189],v50 offset:18184
v_mul_f32 v145,s71,v145
v_mfma_f32_16x16x32_bf16 v[208:211],a[128:131],a[208:211],v[208:211]
v_mul_f32 v146,s71,v146
v_mul_f32 v147,s71,v147
v_mfma_f32_16x16x32_bf16 v[208:211],a[132:135],a[212:215],v[208:211]
ds_read_b64_tr_b16 a[190:191],v50 offset:20232
v_cvt_pk_bf16_f32 v144,v144,v145
v_mfma_f32_16x16x32_bf16 v[208:211],a[136:139],a[240:243],v[208:211]
v_cvt_pk_bf16_f32 v145,v146,v147
v_mul_f32 v229,s71,v229
v_mfma_f32_16x16x32_bf16 v[208:211],a[140:143],a[244:247],v[208:211]
v_mul_f32 v230,s71,v230
v_mul_f32 v231,s71,v231
v_mfma_f32_16x16x32_bf16 v[240:243],a[140:143],a[252:255],v[240:243]
v_cvt_pk_bf16_f32 v224,v224,v225
v_cvt_pk_bf16_f32 v225,v226,v227
v_mfma_f32_16x16x32_bf16 v[240:243],a[136:139],a[248:251],v[240:243]
v_cvt_pk_bf16_f32 v226,v228,v229
v_cvt_pk_bf16_f32 v227,v230,v231
v_mfma_f32_16x16x32_bf16 v[240:243],a[132:135],a[220:223],v[240:243]
v_mul_f32 v168,s71,v168
L_pd3_%=:
s_cbranch_execz L_pd7_%=
v_mul_f32 v169,s71,v169
v_mfma_f32_16x16x32_bf16 v[240:243],a[128:131],a[216:219],v[240:243]
v_mul_f32 v170,s71,v170
v_mul_f32 v171,s71,v171
s_waitcnt vmcnt(7)
s_barrier
s_waitcnt lgkmcnt(1)
v_mfma_f32_16x16x32_bf16 v[148:151],a[144:147],a[192:195],v[148:151]
ds_read_b64_tr_b16 a[64:65],v52
v_mul_f32 v172,s71,v172
v_mfma_f32_16x16x32_bf16 v[148:151],a[148:151],a[196:199],v[148:151]
global_load_lds_dwordx4 v5,s[10:11] offset:3072
ds_read_b64_tr_b16 a[66:67],v52 offset:2048
v_mfma_f32_16x16x32_bf16 v[148:151],a[152:155],a[224:227],v[148:151]
ds_read_b64_tr_b16 a[68:69],v52 offset:16384
v_mul_f32 v173,s71,v173
v_mfma_f32_16x16x32_bf16 v[148:151],a[156:159],a[228:231],v[148:151]
ds_read_b64_tr_b16 a[70:71],v52 offset:18432
v_mul_f32 v174,s71,v174
v_mfma_f32_16x16x32_bf16 v[180:183],a[156:159],a[236:239],v[180:183]
v_mul_f32 v175,s71,v175
v_cvt_pk_bf16_f32 v168,v168,v169
v_mfma_f32_16x16x32_bf16 v[180:183],a[152:155],a[232:235],v[180:183]
ds_read_b64_tr_b16 a[72:73],v52 offset:1024
v_cvt_pk_bf16_f32 v169,v170,v171
v_mfma_f32_16x16x32_bf16 v[180:183],a[148:151],a[204:207],v[180:183]
ds_read_b64_tr_b16 a[74:75],v52 offset:3072
v_mul_f32 v148,s71,v148
v_mfma_f32_16x16x32_bf16 v[180:183],a[144:147],a[200:203],v[180:183]
ds_read_b64_tr_b16 a[76:77],v52 offset:17408
v_mul_f32 v149,s71,v149
v_mfma_f32_16x16x32_bf16 v[212:215],a[144:147],a[208:211],v[212:215]
v_mul_f32 v150,s71,v150
v_mul_f32 v151,s71,v151
v_mfma_f32_16x16x32_bf16 v[212:215],a[148:151],a[212:215],v[212:215]
ds_read_b64_tr_b16 a[78:79],v52 offset:19456
v_cvt_pk_bf16_f32 v146,v148,v149
v_mfma_f32_16x16x32_bf16 v[212:215],a[152:155],a[240:243],v[212:215]
ds_read_b64_tr_b16 a[80:81],v52 offset:8
v_cvt_pk_bf16_f32 v147,v150,v151
v_mfma_f32_16x16x32_bf16 v[212:215],a[156:159],a[244:247],v[212:215]
ds_read_b64_tr_b16 a[82:83],v52 offset:2056
v_cvt_pk_bf16_f32 v170,v172,v173
v_mfma_f32_16x16x32_bf16 v[244:247],a[156:159],a[252:255],v[244:247]
v_cvt_pk_bf16_f32 v171,v174,v175
v_mul_f32 v176,s71,v176
v_mfma_f32_16x16x32_bf16 v[244:247],a[152:155],a[248:251],v[244:247]
ds_read_b64_tr_b16 a[84:85],v52 offset:16392
v_mul_f32 v177,s71,v177
v_mfma_f32_16x16x32_bf16 v[244:247],a[148:151],a[220:223],v[244:247]
s_add_u32 s10,s20,s74
s_addc_u32 s11,s21,0
ds_read_b64_tr_b16 a[86:87],v52 offset:18440
v_mfma_f32_16x16x32_bf16 v[244:247],a[144:147],a[216:219],v[244:247]
ds_read_b64_tr_b16 a[88:89],v52 offset:1032
v_mul_f32 v178,s71,v178
v_mfma_f32_16x16x32_bf16 v[152:155],a[160:163],a[192:195],v[152:155]
s_add_u32 m0,s76,0
v_mul_f32 v179,s71,v179
v_mul_f32 v180,s71,v180
v_mfma_f32_16x16x32_bf16 v[152:155],a[164:167],a[196:199],v[152:155]
ds_read_b64_tr_b16 a[90:91],v52 offset:3080
v_mul_f32 v181,s71,v181
v_mfma_f32_16x16x32_bf16 v[152:155],a[168:171],a[224:227],v[152:155]
global_load_lds_dwordx4 v4,s[10:11] nt
ds_read_b64_tr_b16 a[92:93],v52 offset:17416
v_mfma_f32_16x16x32_bf16 v[152:155],a[172:175],a[228:231],v[152:155]
ds_read_b64_tr_b16 a[94:95],v52 offset:19464
v_mul_f32 v182,s71,v182
v_mfma_f32_16x16x32_bf16 v[184:187],a[172:175],a[236:239],v[184:187]
v_mul_f32 v183,s71,v183
v_cvt_pk_bf16_f32 v176,v176,v177
v_mfma_f32_16x16x32_bf16 v[184:187],a[168:171],a[232:235],v[184:187]
ds_read_b64_tr_b16 a[96:97],v52 offset:256
v_cvt_pk_bf16_f32 v177,v178,v179
v_mfma_f32_16x16x32_bf16 v[184:187],a[164:167],a[204:207],v[184:187]
ds_read_b64_tr_b16 a[98:99],v52 offset:2304
v_mul_f32 v152,s71,v152
v_mfma_f32_16x16x32_bf16 v[184:187],a[160:163],a[200:203],v[184:187]
ds_read_b64_tr_b16 a[100:101],v52 offset:16640
v_mul_f32 v153,s71,v153
v_mfma_f32_16x16x32_bf16 v[216:219],a[160:163],a[208:211],v[216:219]
v_mul_f32 v154,s71,v154
v_mul_f32 v155,s71,v155
v_mfma_f32_16x16x32_bf16 v[216:219],a[164:167],a[212:215],v[216:219]
ds_read_b64_tr_b16 a[102:103],v52 offset:18688
v_cvt_pk_bf16_f32 v152,v152,v153
v_mfma_f32_16x16x32_bf16 v[216:219],a[168:171],a[240:243],v[216:219]
ds_read_b64_tr_b16 a[104:105],v52 offset:1280
v_cvt_pk_bf16_f32 v153,v154,v155
v_mfma_f32_16x16x32_bf16 v[216:219],a[172:175],a[244:247],v[216:219]
ds_read_b64_tr_b16 a[106:107],v52 offset:3328
v_cvt_pk_bf16_f32 v178,v180,v181
v_mfma_f32_16x16x32_bf16 v[248:251],a[172:175],a[252:255],v[248:251]
v_cvt_pk_bf16_f32 v179,v182,v183
v_mul_f32 v184,s71,v184
v_mfma_f32_16x16x32_bf16 v[248:251],a[168:171],a[248:251],v[248:251]
ds_read_b64_tr_b16 a[108:109],v52 offset:17664
v_mul_f32 v185,s71,v185
v_mfma_f32_16x16x32_bf16 v[248:251],a[164:167],a[220:223],v[248:251]
ds_read_b64_tr_b16 a[110:111],v52 offset:19712
v_mul_f32 v186,s71,v186
v_mfma_f32_16x16x32_bf16 v[248:251],a[160:163],a[216:219],v[248:251]
ds_read_b64_tr_b16 a[112:113],v52 offset:264
v_mul_f32 v187,s71,v187
v_mfma_f32_16x16x32_bf16 v[156:159],a[176:179],a[192:195],v[156:159]
v_cvt_pk_bf16_f32 v184,v184,v185
v_cvt_pk_bf16_f32 v185,v186,v187
v_mfma_f32_16x16x32_bf16 v[156:159],a[180:183],a[196:199],v[156:159]
global_load_lds_dwordx4 v4,s[10:11] offset:1024 nt
ds_read_b64_tr_b16 a[114:115],v52 offset:2312
v_mfma_f32_16x16x32_bf16 v[156:159],a[184:187],a[224:227],v[156:159]
s_waitcnt lgkmcnt(5)
ds_read_b64_tr_b16 a[116:117],v52 offset:16648
v_mul_f32 v200,s71,v200
v_mfma_f32_16x16x32_bf16 v[156:159],a[188:191],a[228:231],v[156:159]
ds_read_b64_tr_b16 a[118:119],v52 offset:18696
v_mul_f32 v201,s71,v201
v_mfma_f32_16x16x32_bf16 v[188:191],a[188:191],a[236:239],v[188:191]
v_mul_f32 v202,s71,v202
v_mul_f32 v203,s71,v203
v_mfma_f32_16x16x32_bf16 v[188:191],a[184:187],a[232:235],v[188:191]
ds_read_b64_tr_b16 a[120:121],v52 offset:1288
v_mul_f32 v204,s71,v204
v_mfma_f32_16x16x32_bf16 v[188:191],a[180:183],a[204:207],v[188:191]
ds_read_b64_tr_b16 a[122:123],v52 offset:3336
v_mul_f32 v156,s71,v156
v_mfma_f32_16x16x32_bf16 v[188:191],a[176:179],a[200:203],v[188:191]
ds_read_b64_tr_b16 a[124:125],v52 offset:17672
v_mul_f32 v157,s71,v157
v_mfma_f32_16x16x32_bf16 v[220:223],a[176:179],a[208:211],v[220:223]
v_mul_f32 v158,s71,v158
v_mul_f32 v159,s71,v159
v_mfma_f32_16x16x32_bf16 v[220:223],a[180:183],a[212:215],v[220:223]
ds_read_b64_tr_b16 a[126:127],v52 offset:19720
v_cvt_pk_bf16_f32 v154,v156,v157
v_mfma_f32_16x16x32_bf16 v[220:223],a[184:187],a[240:243],v[220:223]
v_cvt_pk_bf16_f32 v155,v158,v159
v_mul_f32 v188,s71,v188
v_mfma_f32_16x16x32_bf16 v[220:223],a[188:191],a[244:247],v[220:223]
v_mul_f32 v189,s71,v189
v_mul_f32 v190,s71,v190
v_mfma_f32_16x16x32_bf16 v[252:255],a[188:191],a[252:255],v[252:255]
v_mul_f32 v191,s71,v191
v_cvt_pk_bf16_f32 v186,v188,v189
v_mfma_f32_16x16x32_bf16 v[252:255],a[184:187],a[248:251],v[252:255]
v_cvt_pk_bf16_f32 v187,v190,v191
v_mul_f32 v205,s71,v205
v_mfma_f32_16x16x32_bf16 v[252:255],a[180:183],a[220:223],v[252:255]
v_mul_f32 v206,s71,v206
v_mul_f32 v207,s71,v207
v_mfma_f32_16x16x32_bf16 v[252:255],a[176:179],a[216:219],v[252:255]
v_cvt_pk_bf16_f32 v200,v200,v201
v_cvt_pk_bf16_f32 v201,v202,v203
v_mfma_f32_16x16x32_bf16 a[0:3],a[64:67],a[192:195],a[0:3]
global_load_lds_dwordx4 v5,s[10:11] offset:2048 nt
v_cvt_pk_bf16_f32 v202,v204,v205
v_mfma_f32_16x16x32_bf16 a[0:3],a[68:71],a[196:199],a[0:3]
v_cvt_pk_bf16_f32 v203,v206,v207
v_mul_f32 v208,s71,v208
v_mfma_f32_16x16x32_bf16 a[0:3],a[72:75],a[224:227],a[0:3]
v_mul_f32 v209,s71,v209
v_mul_f32 v210,s71,v210
v_mfma_f32_16x16x32_bf16 a[0:3],a[76:79],a[228:231],a[0:3]
v_mul_f32 v211,s71,v211
v_mul_f32 v212,s71,v212
v_mfma_f32_16x16x32_bf16 a[16:19],a[76:79],a[236:239],a[16:19]
v_mul_f32 v213,s71,v213
v_mul_f32 v214,s71,v214
v_mfma_f32_16x16x32_bf16 a[16:19],a[72:75],a[232:235],a[16:19]
v_mul_f32 v215,s71,v215
v_cvt_pk_bf16_f32 v208,v208,v209
v_mfma_f32_16x16x32_bf16 a[16:19],a[68:71],a[204:207],a[16:19]
v_accvgpr_read_b32 v8,a0
v_accvgpr_read_b32 v9,a1
v_mfma_f32_16x16x32_bf16 a[16:19],a[64:67],a[200:203],a[16:19]
v_accvgpr_read_b32 v10,a2
v_accvgpr_read_b32 v11,a3
v_mfma_f32_16x16x32_bf16 a[32:35],a[64:67],a[208:211],a[32:35]
v_mul_f32 v8,s71,v8
v_mul_f32 v9,s71,v9
v_mfma_f32_16x16x32_bf16 a[32:35],a[68:71],a[212:215],a[32:35]
v_mul_f32 v10,s71,v10
v_mul_f32 v11,s71,v11
v_mfma_f32_16x16x32_bf16 a[32:35],a[72:75],a[240:243],a[32:35]
v_cvt_pk_bf16_f32 v132,v8,v9
v_cvt_pk_bf16_f32 v133,v10,v11
v_mfma_f32_16x16x32_bf16 a[32:35],a[76:79],a[244:247],a[32:35]
v_accvgpr_read_b32 v16,a16
v_accvgpr_read_b32 v17,a17
v_mfma_f32_16x16x32_bf16 a[48:51],a[76:79],a[252:255],a[48:51]
ds_read_b64_tr_b16 a[76:77],v52 offset:17920
v_accvgpr_read_b32 v18,a18
v_mfma_f32_16x16x32_bf16 a[48:51],a[72:75],a[248:251],a[48:51]
ds_read_b64_tr_b16 a[72:73],v52 offset:1536
v_accvgpr_read_b32 v19,a19
v_mfma_f32_16x16x32_bf16 a[48:51],a[68:71],a[220:223],a[48:51]
ds_read_b64_tr_b16 a[68:69],v52 offset:16896
)A"
R"A(v_accvgpr_read_b32 v8,a32
v_mfma_f32_16x16x32_bf16 a[48:51],a[64:67],a[216:219],a[48:51]
global_load_lds_dwordx4 v5,s[10:11] offset:3072
ds_read_b64_tr_b16 a[64:65],v52 offset:512
v_mfma_f32_16x16x32_bf16 a[4:7],a[80:83],a[192:195],a[4:7]
v_accvgpr_read_b32 v9,a33
v_accvgpr_read_b32 v10,a34
v_mfma_f32_16x16x32_bf16 a[4:7],a[84:87],a[196:199],a[4:7]
ds_read_b64_tr_b16 a[66:67],v52 offset:2560
v_accvgpr_read_b32 v11,a35
v_mfma_f32_16x16x32_bf16 a[4:7],a[88:91],a[224:227],a[4:7]
ds_read_b64_tr_b16 a[70:71],v52 offset:18944
v_mul_f32 v8,s71,v8
v_mfma_f32_16x16x32_bf16 a[4:7],a[92:95],a[228:231],a[4:7]
ds_read_b64_tr_b16 a[74:75],v52 offset:3584
v_mul_f32 v9,s71,v9
v_mfma_f32_16x16x32_bf16 a[20:23],a[92:95],a[236:239],a[20:23]
v_mul_f32 v10,s71,v10
v_mul_f32 v11,s71,v11
v_mfma_f32_16x16x32_bf16 a[20:23],a[88:91],a[232:235],a[20:23]
ds_read_b64_tr_b16 a[78:79],v52 offset:19968
v_cvt_pk_bf16_f32 v196,v8,v9
v_mfma_f32_16x16x32_bf16 a[20:23],a[84:87],a[204:207],a[20:23]
v_accvgpr_read_b32 v12,a4
v_accvgpr_read_b32 v13,a5
v_mfma_f32_16x16x32_bf16 a[20:23],a[80:83],a[200:203],a[20:23]
v_accvgpr_read_b32 v14,a6
v_accvgpr_read_b32 v15,a7
v_mfma_f32_16x16x32_bf16 a[36:39],a[80:83],a[208:211],a[36:39]
v_mul_f32 v12,s71,v12
v_mul_f32 v13,s71,v13
v_mfma_f32_16x16x32_bf16 a[36:39],a[84:87],a[212:215],a[36:39]
v_mul_f32 v14,s71,v14
v_mul_f32 v15,s71,v15
v_mfma_f32_16x16x32_bf16 a[36:39],a[88:91],a[240:243],a[36:39]
s_add_u32 s10,s24,s74
s_addc_u32 s11,s25,0
s_add_u32 s74,s74,16384
v_mfma_f32_16x16x32_bf16 a[36:39],a[92:95],a[244:247],a[36:39]
s_and_b32 s74,s74,0x3ffff
s_or_b32 s74,s74,s69
v_cvt_pk_bf16_f32 v134,v12,v13
v_mfma_f32_16x16x32_bf16 a[52:55],a[92:95],a[252:255],a[52:55]
s_add_u32 m0,s76,16384
s_sub_u32 s76,s76,32768
ds_read_b64_tr_b16 a[92:93],v52 offset:17928
v_mfma_f32_16x16x32_bf16 a[52:55],a[88:91],a[248:251],a[52:55]
s_and_b32 s76,s76,0x1ffff
ds_read_b64_tr_b16 a[88:89],v52 offset:1544
v_cvt_pk_bf16_f32 v135,v14,v15
v_mfma_f32_16x16x32_bf16 a[52:55],a[84:87],a[220:223],a[52:55]
global_load_lds_dwordx4 v4,s[10:11]
ds_read_b64_tr_b16 a[84:85],v52 offset:16904
v_mfma_f32_16x16x32_bf16 a[52:55],a[80:83],a[216:219],a[52:55]
s_and_b32 s69,s75,0xfffc0000
ds_read_b64_tr_b16 a[80:81],v52 offset:520
v_accvgpr_read_b32 v12,a36
v_mfma_f32_16x16x32_bf16 a[8:11],a[96:99],a[192:195],a[8:11]
s_add_u32 s75,s75,16384
v_accvgpr_read_b32 v13,a37
v_accvgpr_read_b32 v14,a38
v_mfma_f32_16x16x32_bf16 a[8:11],a[100:103],a[196:199],a[8:11]
s_waitcnt lgkmcnt(4)
ds_read_b64_tr_b16 a[82:83],v52 offset:2568
v_accvgpr_read_b32 v15,a39
v_mfma_f32_16x16x32_bf16 a[8:11],a[104:107],a[224:227],a[8:11]
ds_read_b64_tr_b16 a[86:87],v52 offset:18952
v_mul_f32 v12,s71,v12
v_mfma_f32_16x16x32_bf16 a[8:11],a[108:111],a[228:231],a[8:11]
ds_read_b64_tr_b16 a[90:91],v52 offset:3592
v_mul_f32 v13,s71,v13
v_mfma_f32_16x16x32_bf16 a[24:27],a[108:111],a[236:239],a[24:27]
v_mul_f32 v14,s71,v14
v_mul_f32 v15,s71,v15
v_mfma_f32_16x16x32_bf16 a[24:27],a[104:107],a[232:235],a[24:27]
ds_read_b64_tr_b16 a[94:95],v52 offset:19976
v_cvt_pk_bf16_f32 v197,v10,v11
v_mfma_f32_16x16x32_bf16 a[24:27],a[100:103],a[204:207],a[24:27]
v_cvt_pk_bf16_f32 v198,v12,v13
v_cvt_pk_bf16_f32 v199,v14,v15
v_mfma_f32_16x16x32_bf16 a[24:27],a[96:99],a[200:203],a[24:27]
v_accvgpr_read_b32 v8,a8
v_accvgpr_read_b32 v9,a9
v_mfma_f32_16x16x32_bf16 a[40:43],a[96:99],a[208:211],a[40:43]
v_accvgpr_read_b32 v10,a10
v_accvgpr_read_b32 v11,a11
v_mfma_f32_16x16x32_bf16 a[40:43],a[100:103],a[212:215],a[40:43]
v_mul_f32 v8,s71,v8
v_mul_f32 v9,s71,v9
v_mfma_f32_16x16x32_bf16 a[40:43],a[104:107],a[240:243],a[40:43]
v_mul_f32 v10,s71,v10
v_mul_f32 v11,s71,v11
v_mfma_f32_16x16x32_bf16 a[40:43],a[108:111],a[244:247],a[40:43]
v_cvt_pk_bf16_f32 v140,v8,v9
v_cvt_pk_bf16_f32 v141,v10,v11
v_mfma_f32_16x16x32_bf16 a[56:59],a[108:111],a[252:255],a[56:59]
ds_read_b64_tr_b16 a[108:109],v52 offset:18176
v_accvgpr_read_b32 v20,a20
v_mfma_f32_16x16x32_bf16 a[56:59],a[104:107],a[248:251],a[56:59]
global_load_lds_dwordx4 v5,s[10:11] offset:1024
ds_read_b64_tr_b16 a[104:105],v52 offset:1792
v_mfma_f32_16x16x32_bf16 a[56:59],a[100:103],a[220:223],a[56:59]
ds_read_b64_tr_b16 a[100:101],v52 offset:17152
v_accvgpr_read_b32 v21,a21
v_mfma_f32_16x16x32_bf16 a[56:59],a[96:99],a[216:219],a[56:59]
ds_read_b64_tr_b16 a[96:97],v52 offset:768
v_accvgpr_read_b32 v22,a22
v_mfma_f32_16x16x32_bf16 a[12:15],a[112:115],a[192:195],a[12:15]
v_accvgpr_read_b32 v23,a23
v_mul_f32 v16,s71,v16
v_mfma_f32_16x16x32_bf16 a[12:15],a[116:119],a[196:199],a[12:15]
ds_read_b64_tr_b16 a[98:99],v52 offset:2816
v_mul_f32 v17,s71,v17
v_mfma_f32_16x16x32_bf16 a[12:15],a[120:123],a[224:227],a[12:15]
ds_read_b64_tr_b16 a[102:103],v52 offset:19200
v_mul_f32 v18,s71,v18
v_mfma_f32_16x16x32_bf16 a[12:15],a[124:127],a[228:231],a[12:15]
ds_read_b64_tr_b16 a[106:107],v52 offset:3840
v_mul_f32 v19,s71,v19
v_mfma_f32_16x16x32_bf16 a[28:31],a[124:127],a[236:239],a[28:31]
v_mul_f32 v20,s71,v20
v_mul_f32 v21,s71,v21
v_mfma_f32_16x16x32_bf16 a[28:31],a[120:123],a[232:235],a[28:31]
ds_read_b64_tr_b16 a[110:111],v52 offset:20224
v_mul_f32 v22,s71,v22
v_mfma_f32_16x16x32_bf16 a[28:31],a[116:119],a[204:207],a[28:31]
v_accvgpr_read_b32 v12,a12
v_accvgpr_read_b32 v13,a13
v_mfma_f32_16x16x32_bf16 a[28:31],a[112:115],a[200:203],a[28:31]
v_accvgpr_read_b32 v14,a14
v_accvgpr_read_b32 v15,a15
v_mfma_f32_16x16x32_bf16 a[44:47],a[112:115],a[208:211],a[44:47]
v_mul_f32 v12,s71,v12
v_mul_f32 v13,s71,v13
v_mfma_f32_16x16x32_bf16 a[44:47],a[116:119],a[212:215],a[44:47]
v_mul_f32 v14,s71,v14
v_mul_f32 v15,s71,v15
v_mfma_f32_16x16x32_bf16 a[44:47],a[120:123],a[240:243],a[44:47]
v_cvt_pk_bf16_f32 v142,v12,v13
v_cvt_pk_bf16_f32 v143,v14,v15
v_mfma_f32_16x16x32_bf16 a[44:47],a[124:127],a[244:247],a[44:47]
v_mul_f32 v23,s71,v23
v_cvt_pk_bf16_f32 v164,v16,v17
v_mfma_f32_16x16x32_bf16 a[60:63],a[124:127],a[252:255],a[60:63]
global_load_lds_dwordx4 v4,s[10:11] offset:2048
ds_read_b64_tr_b16 a[124:125],v52 offset:18184
v_mfma_f32_16x16x32_bf16 a[60:63],a[120:123],a[248:251],a[60:63]
ds_read_b64_tr_b16 a[120:121],v52 offset:1800
v_cvt_pk_bf16_f32 v165,v18,v19
v_mfma_f32_16x16x32_bf16 a[60:63],a[116:119],a[220:223],a[60:63]
ds_read_b64_tr_b16 a[116:117],v52 offset:17160
v_cvt_pk_bf16_f32 v166,v20,v21
v_mfma_f32_16x16x32_bf16 a[60:63],a[112:115],a[216:219],a[60:63]
ds_read_b64_tr_b16 a[112:113],v52 offset:776
v_cvt_pk_bf16_f32 v167,v22,v23
v_mfma_f32_16x16x32_bf16 v[64:67],a[64:67],a[192:195],v[64:67]
v_accvgpr_read_b32 v16,a48
v_accvgpr_read_b32 v17,a49
v_mfma_f32_16x16x32_bf16 v[64:67],a[68:71],a[196:199],v[64:67]
ds_read_b64_tr_b16 a[114:115],v52 offset:2824
v_accvgpr_read_b32 v18,a50
v_mfma_f32_16x16x32_bf16 v[64:67],a[72:75],a[224:227],v[64:67]
ds_read_b64_tr_b16 a[118:119],v52 offset:19208
v_accvgpr_read_b32 v19,a51
v_mfma_f32_16x16x32_bf16 v[64:67],a[76:79],a[228:231],v[64:67]
ds_read_b64_tr_b16 a[122:123],v52 offset:3848
v_accvgpr_read_b32 v20,a52
v_mfma_f32_16x16x32_bf16 v[80:83],a[76:79],a[236:239],v[80:83]
v_accvgpr_read_b32 v21,a53
v_accvgpr_read_b32 v22,a54
v_mfma_f32_16x16x32_bf16 v[80:83],a[72:75],a[232:235],v[80:83]
ds_read_b64_tr_b16 a[126:127],v52 offset:20232
v_accvgpr_read_b32 v23,a55
v_mfma_f32_16x16x32_bf16 v[80:83],a[68:71],a[204:207],v[80:83]
v_mul_f32 v64,s71,v64
v_mul_f32 v65,s71,v65
v_mfma_f32_16x16x32_bf16 v[80:83],a[64:67],a[200:203],v[80:83]
v_mul_f32 v66,s71,v66
v_mul_f32 v67,s71,v67
v_mfma_f32_16x16x32_bf16 v[96:99],a[64:67],a[208:211],v[96:99]
v_cvt_pk_bf16_f32 v148,v64,v65
v_cvt_pk_bf16_f32 v149,v66,v67
v_mfma_f32_16x16x32_bf16 v[96:99],a[68:71],a[212:215],v[96:99]
v_mul_f32 v16,s71,v16
v_mul_f32 v17,s71,v17
v_mfma_f32_16x16x32_bf16 v[96:99],a[72:75],a[240:243],v[96:99]
v_mul_f32 v18,s71,v18
v_mul_f32 v19,s71,v19
v_mfma_f32_16x16x32_bf16 v[96:99],a[76:79],a[244:247],v[96:99]
global_load_lds_dwordx4 v5,s[10:11] offset:3072
v_mul_f32 v20,s71,v20
v_mfma_f32_16x16x32_bf16 v[112:115],a[76:79],a[252:255],v[112:115]
v_mul_f32 v21,s71,v21
v_mul_f32 v22,s71,v22
v_mfma_f32_16x16x32_bf16 v[112:115],a[72:75],a[248:251],v[112:115]
v_mul_f32 v23,s71,v23
v_cvt_pk_bf16_f32 v228,v16,v17
v_mfma_f32_16x16x32_bf16 v[112:115],a[68:71],a[220:223],v[112:115]
v_cvt_pk_bf16_f32 v229,v18,v19
v_cvt_pk_bf16_f32 v230,v20,v21
v_mfma_f32_16x16x32_bf16 v[112:115],a[64:67],a[216:219],v[112:115]
v_cvt_pk_bf16_f32 v231,v22,v23
v_accvgpr_read_b32 v16,a24
s_waitcnt vmcnt(8) lgkmcnt(0)
s_barrier
v_mfma_f32_16x16x32_bf16 v[68:71],a[80:83],a[192:195],v[68:71]
ds_read_b128 a[128:131],v1
v_accvgpr_read_b32 v17,a25
v_mfma_f32_16x16x32_bf16 v[68:71],a[84:87],a[196:199],v[68:71]
ds_read_b128 a[132:135],v1 offset:256
v_accvgpr_read_b32 v18,a26
v_mfma_f32_16x16x32_bf16 v[68:71],a[88:91],a[224:227],v[68:71]
ds_read_b128 a[136:139],v1 offset:512
v_accvgpr_read_b32 v19,a27
v_mfma_f32_16x16x32_bf16 v[68:71],a[92:95],a[228:231],v[68:71]
ds_read_b128 a[140:143],v1 offset:768
v_accvgpr_read_b32 v20,a28
v_mfma_f32_16x16x32_bf16 v[84:87],a[92:95],a[236:239],v[84:87]
v_accvgpr_read_b32 v21,a29
v_accvgpr_read_b32 v22,a30
v_mfma_f32_16x16x32_bf16 v[84:87],a[88:91],a[232:235],v[84:87]
v_accvgpr_read_b32 v23,a31
v_mul_f32 v16,s71,v16
v_mfma_f32_16x16x32_bf16 v[84:87],a[84:87],a[204:207],v[84:87]
s_add_u32 s10,s20,s74
s_addc_u32 s11,s21,0
v_mul_f32 v68,s71,v68
v_mfma_f32_16x16x32_bf16 v[84:87],a[80:83],a[200:203],v[84:87]
v_mul_f32 v69,s71,v69
v_mul_f32 v70,s71,v70
v_mfma_f32_16x16x32_bf16 v[100:103],a[80:83],a[208:211],v[100:103]
s_add_u32 m0,s76,0
v_mul_f32 v71,s71,v71
v_cvt_pk_bf16_f32 v150,v68,v69
v_mfma_f32_16x16x32_bf16 v[100:103],a[84:87],a[212:215],v[100:103]
v_cvt_pk_bf16_f32 v151,v70,v71
v_mul_f32 v17,s71,v17
v_mfma_f32_16x16x32_bf16 v[100:103],a[88:91],a[240:243],v[100:103]
global_load_lds_dwordx4 v4,s[10:11] nt
v_mul_f32 v18,s71,v18
v_mfma_f32_16x16x32_bf16 v[100:103],a[92:95],a[244:247],v[100:103]
v_mul_f32 v19,s71,v19
v_mul_f32 v20,s71,v20
v_mfma_f32_16x16x32_bf16 v[116:119],a[92:95],a[252:255],v[116:119]
v_mul_f32 v21,s71,v21
v_mul_f32 v22,s71,v22
v_mfma_f32_16x16x32_bf16 v[116:119],a[88:91],a[248:251],v[116:119]
v_mul_f32 v23,s71,v23
v_cvt_pk_bf16_f32 v172,v16,v17
v_mfma_f32_16x16x32_bf16 v[116:119],a[84:87],a[220:223],v[116:119]
v_cvt_pk_bf16_f32 v173,v18,v19
v_cvt_pk_bf16_f32 v174,v20,v21
v_mfma_f32_16x16x32_bf16 v[116:119],a[80:83],a[216:219],v[116:119]
v_cvt_pk_bf16_f32 v175,v22,v23
v_mul_f32 v80,s71,v80
v_mfma_f32_16x16x32_bf16 v[72:75],a[96:99],a[192:195],v[72:75]
v_mul_f32 v81,s71,v81
v_mul_f32 v82,s71,v82
v_mfma_f32_16x16x32_bf16 v[72:75],a[100:103],a[196:199],v[72:75]
v_mul_f32 v83,s71,v83
v_mul_f32 v84,s71,v84
v_mfma_f32_16x16x32_bf16 v[72:75],a[104:107],a[224:227],v[72:75]
v_mul_f32 v85,s71,v85
v_mul_f32 v86,s71,v86
v_mfma_f32_16x16x32_bf16 v[72:75],a[108:111],a[228:231],v[72:75]
v_mul_f32 v87,s71,v87
v_cvt_pk_bf16_f32 v180,v80,v81
v_mfma_f32_16x16x32_bf16 v[88:91],a[108:111],a[236:239],v[88:91]
v_cvt_pk_bf16_f32 v181,v82,v83
v_cvt_pk_bf16_f32 v182,v84,v85
v_mfma_f32_16x16x32_bf16 v[88:91],a[104:107],a[232:235],v[88:91]
v_cvt_pk_bf16_f32 v183,v86,v87
v_accvgpr_read_b32 v8,a40
v_mfma_f32_16x16x32_bf16 v[88:91],a[100:103],a[204:207],v[88:91]
v_mul_f32 v72,s71,v72
v_mul_f32 v73,s71,v73
v_mfma_f32_16x16x32_bf16 v[88:91],a[96:99],a[200:203],v[88:91]
v_mul_f32 v74,s71,v74
v_mul_f32 v75,s71,v75
v_mfma_f32_16x16x32_bf16 v[104:107],a[96:99],a[208:211],v[104:107]
global_load_lds_dwordx4 v4,s[10:11] offset:1024 nt
v_cvt_pk_bf16_f32 v156,v72,v73
v_mfma_f32_16x16x32_bf16 v[104:107],a[100:103],a[212:215],v[104:107]
v_cvt_pk_bf16_f32 v157,v74,v75
v_accvgpr_read_b32 v9,a41
v_mfma_f32_16x16x32_bf16 v[104:107],a[104:107],a[240:243],v[104:107]
v_mul_f32 v88,s71,v88
v_mul_f32 v89,s71,v89
v_mfma_f32_16x16x32_bf16 v[104:107],a[108:111],a[244:247],v[104:107]
v_mul_f32 v90,s71,v90
v_mul_f32 v91,s71,v91
v_mfma_f32_16x16x32_bf16 v[120:123],a[108:111],a[252:255],v[120:123]
v_cvt_pk_bf16_f32 v188,v88,v89
v_cvt_pk_bf16_f32 v189,v90,v91
v_mfma_f32_16x16x32_bf16 v[120:123],a[104:107],a[248:251],v[120:123]
v_accvgpr_read_b32 v10,a42
v_accvgpr_read_b32 v11,a43
v_mfma_f32_16x16x32_bf16 v[120:123],a[100:103],a[220:223],v[120:123]
)A"
R"A(v_accvgpr_read_b32 v12,a44
v_accvgpr_read_b32 v13,a45
v_mfma_f32_16x16x32_bf16 v[120:123],a[96:99],a[216:219],v[120:123]
v_accvgpr_read_b32 v14,a46
v_accvgpr_read_b32 v15,a47
v_mfma_f32_16x16x32_bf16 v[76:79],a[112:115],a[192:195],v[76:79]
v_mul_f32 v8,s71,v8
v_mul_f32 v9,s71,v9
v_mfma_f32_16x16x32_bf16 v[76:79],a[116:119],a[196:199],v[76:79]
v_mul_f32 v10,s71,v10
v_mul_f32 v11,s71,v11
v_mfma_f32_16x16x32_bf16 v[76:79],a[120:123],a[224:227],v[76:79]
v_mul_f32 v12,s71,v12
v_mul_f32 v13,s71,v13
v_mfma_f32_16x16x32_bf16 v[76:79],a[124:127],a[228:231],v[76:79]
v_mul_f32 v14,s71,v14
v_mul_f32 v15,s71,v15
v_mfma_f32_16x16x32_bf16 v[92:95],a[124:127],a[236:239],v[92:95]
v_cvt_pk_bf16_f32 v204,v8,v9
v_cvt_pk_bf16_f32 v205,v10,v11
v_mfma_f32_16x16x32_bf16 v[92:95],a[120:123],a[232:235],v[92:95]
v_cvt_pk_bf16_f32 v206,v12,v13
v_cvt_pk_bf16_f32 v207,v14,v15
v_mfma_f32_16x16x32_bf16 v[92:95],a[116:119],a[204:207],v[92:95]
global_load_lds_dwordx4 v5,s[10:11] offset:2048 nt
v_mul_f32 v76,s71,v76
v_mfma_f32_16x16x32_bf16 v[92:95],a[112:115],a[200:203],v[92:95]
v_mul_f32 v77,s71,v77
v_mul_f32 v78,s71,v78
v_mfma_f32_16x16x32_bf16 v[108:111],a[112:115],a[208:211],v[108:111]
v_mul_f32 v79,s71,v79
v_cvt_pk_bf16_f32 v158,v76,v77
v_mfma_f32_16x16x32_bf16 v[108:111],a[116:119],a[212:215],v[108:111]
ds_read_b128 a[156:159],v1 offset:4864
v_cvt_pk_bf16_f32 v159,v78,v79
v_mfma_f32_16x16x32_bf16 v[108:111],a[120:123],a[240:243],v[108:111]
v_mul_f32 v92,s71,v92
v_mul_f32 v93,s71,v93
v_mfma_f32_16x16x32_bf16 v[108:111],a[124:127],a[244:247],v[108:111]
v_mul_f32 v94,s71,v94
v_mul_f32 v95,s71,v95
v_mfma_f32_16x16x32_bf16 v[124:127],a[124:127],a[252:255],v[124:127]
v_cvt_pk_bf16_f32 v190,v92,v93
v_cvt_pk_bf16_f32 v191,v94,v95
v_mfma_f32_16x16x32_bf16 v[124:127],a[120:123],a[248:251],v[124:127]
v_cvt_pk_bf16_f32 v209,v210,v211
v_cvt_pk_bf16_f32 v210,v212,v213
v_mfma_f32_16x16x32_bf16 v[124:127],a[116:119],a[220:223],v[124:127]
ds_read_b128 a[152:155],v1 offset:4608
v_cvt_pk_bf16_f32 v211,v214,v215
v_mfma_f32_16x16x32_bf16 v[124:127],a[112:115],a[216:219],v[124:127]
v_mul_f32 v96,s71,v96
v_mul_f32 v97,s71,v97
s_waitcnt lgkmcnt(2)
v_mfma_f32_16x16x32_bf16 v[64:67],a[128:131],v[132:135],0
v_mul_f32 v98,s71,v98
v_mul_f32 v99,s71,v99
v_mfma_f32_16x16x32_bf16 v[64:67],a[132:135],v[140:143],v[64:67]
v_mul_f32 v100,s71,v100
v_mul_f32 v101,s71,v101
v_mfma_f32_16x16x32_bf16 v[64:67],a[136:139],v[148:151],v[64:67]
v_mul_f32 v102,s71,v102
v_mul_f32 v103,s71,v103
v_mfma_f32_16x16x32_bf16 v[64:67],a[140:143],v[156:159],v[64:67]
global_load_lds_dwordx4 v5,s[10:11] offset:3072
v_cvt_pk_bf16_f32 v212,v96,v97
v_mfma_f32_16x16x32_bf16 v[80:83],a[128:131],v[164:167],0
v_cvt_pk_bf16_f32 v213,v98,v99
v_cvt_pk_bf16_f32 v214,v100,v101
v_mfma_f32_16x16x32_bf16 v[80:83],a[132:135],v[172:175],v[80:83]
v_cvt_pk_bf16_f32 v215,v102,v103
v_mul_f32 v216,s71,v216
v_mfma_f32_16x16x32_bf16 v[80:83],a[136:139],v[180:183],v[80:83]
v_max3_f32 v64,v64,v65,v66
v_max_f32 v64,v64,v67
v_mov_b32 v65,v64
v_mfma_f32_16x16x32_bf16 v[80:83],a[140:143],v[188:191],v[80:83]
v_mul_f32 v217,s71,v217
v_mul_f32 v218,s71,v218
v_mfma_f32_16x16x32_bf16 v[96:99],a[128:131],v[196:199],0
v_permlane32_swap_b32 v64,v65
v_max_f32 v64,v64,v65
v_mov_b32 v65,v64
v_mul_f32 v219,s71,v219
v_mul_f32 v220,s71,v220
v_mul_f32 v221,s71,v221
v_mul_f32 v222,s71,v222
v_mul_f32 v223,s71,v223
v_cvt_pk_bf16_f32 v216,v216,v217
v_cvt_pk_bf16_f32 v217,v218,v219
v_cvt_pk_bf16_f32 v218,v220,v221
v_cvt_pk_bf16_f32 v219,v222,v223
v_mul_f32 v104,s71,v104
v_mul_f32 v105,s71,v105
v_mul_f32 v106,s71,v106
v_mul_f32 v107,s71,v107
v_mul_f32 v108,s71,v108
v_mul_f32 v109,s71,v109
v_mul_f32 v110,s71,v110
v_mul_f32 v111,s71,v111
v_cvt_pk_bf16_f32 v220,v104,v105
v_cvt_pk_bf16_f32 v221,v106,v107
v_cvt_pk_bf16_f32 v222,v108,v109
v_cvt_pk_bf16_f32 v223,v110,v111
v_mfma_f32_16x16x32_bf16 v[96:99],a[132:135],v[204:207],v[96:99]
ds_read_b128 a[148:151],v1 offset:4352
v_max3_f32 v80,v80,v81,v82
v_mfma_f32_16x16x32_bf16 v[96:99],a[136:139],v[212:215],v[96:99]
L_pd4_%=:
s_cbranch_execz L_pd8_%=
ds_read_b128 a[144:147],v1 offset:4096
v_permlane16_swap_b32 v64,v65
v_max_f32 v64,v64,v65
v_sub_f32 v35,0,v64
v_sub_f32 v34,0,v64
v_sub_f32 v33,0,v64
v_sub_f32 v32,0,v64
v_mfma_f32_16x16x32_bf16 v[96:99],a[140:143],v[220:223],v[96:99]
ds_read_b128 a[160:163],v1 offset:8192
v_max_f32 v80,v80,v83
v_mfma_f32_16x16x32_bf16 v[64:67],a[128:131],v[132:135],v[32:35]
s_waitcnt lgkmcnt(3)
ds_read_b128 a[164:167],v1 offset:8448
v_mov_b32 v81,v80
v_mfma_f32_16x16x32_bf16 v[64:67],a[132:135],v[140:143],v[64:67]
s_add_u32 s10,s24,s74
s_addc_u32 s11,s25,0
v_mul_f32 v232,s71,v232
v_mfma_f32_16x16x32_bf16 v[64:67],a[136:139],v[148:151],v[64:67]
s_add_u32 s74,s74,16384
ds_read_b128 a[168:171],v1 offset:8704
v_max3_f32 v96,v96,v97,v98
v_mfma_f32_16x16x32_bf16 v[64:67],a[140:143],v[156:159],v[64:67]
s_add_u32 m0,s76,16384
ds_read_b128 a[172:175],v1 offset:8960
v_max_f32 v96,v96,v99
v_mfma_f32_16x16x32_bf16 v[68:71],a[156:159],v[156:159],v[32:35]
s_and_b32 s74,s74,0x3ffff
ds_read_b128 a[188:191],v1 offset:13056
v_mov_b32 v97,v96
v_mfma_f32_16x16x32_bf16 v[68:71],a[152:155],v[148:151],v[68:71]
global_load_lds_dwordx4 v4,s[10:11]
v_mul_f32 v233,s71,v233
s_waitcnt lgkmcnt(6)
v_mfma_f32_16x16x32_bf16 v[68:71],a[148:151],v[140:143],v[68:71]
s_or_b32 s74,s74,s69
ds_read_b128 a[184:187],v1 offset:12800
v_mul_f32 v234,s71,v234
s_waitcnt lgkmcnt(6)
v_mfma_f32_16x16x32_bf16 v[68:71],a[144:147],v[132:135],v[68:71]
s_sub_u32 s76,s76,32768
ds_read_b128 a[180:183],v1 offset:12544
v_mul_f32 v235,s71,v235
s_waitcnt lgkmcnt(6)
v_mfma_f32_16x16x32_bf16 v[72:75],a[160:163],v[132:135],v[32:35]
s_and_b32 s76,s76,0x1ffff
ds_read_b128 a[176:179],v1 offset:12288
v_mul_f32 v236,s71,v236
s_waitcnt lgkmcnt(6)
v_mfma_f32_16x16x32_bf16 v[72:75],a[164:167],v[140:143],v[72:75]
s_and_b32 s69,s75,0xfffc0000
v_permlane32_swap_b32 v80,v81
s_waitcnt lgkmcnt(5)
v_mfma_f32_16x16x32_bf16 v[72:75],a[168:171],v[148:151],v[72:75]
s_add_u32 s75,s75,16384
v_max_f32 v80,v80,v81
v_mov_b32 v81,v80
s_waitcnt lgkmcnt(4)
v_mfma_f32_16x16x32_bf16 v[72:75],a[172:175],v[156:159],v[72:75]
s_waitcnt lgkmcnt(3)
v_mul_f32 v237,s71,v237
v_mul_f32 v238,s71,v238
v_mfma_f32_16x16x32_bf16 v[76:79],a[188:191],v[156:159],v[32:35]
s_waitcnt lgkmcnt(2)
v_permlane16_swap_b32 v80,v81
v_mfma_f32_16x16x32_bf16 v[76:79],a[184:187],v[148:151],v[76:79]
s_waitcnt lgkmcnt(1)
v_max_f32 v80,v80,v81
v_sub_f32 v39,0,v80
v_sub_f32 v38,0,v80
v_sub_f32 v37,0,v80
v_sub_f32 v36,0,v80
v_mfma_f32_16x16x32_bf16 v[76:79],a[180:183],v[140:143],v[76:79]
s_waitcnt lgkmcnt(0)
v_mul_f32 v239,s71,v239
v_cvt_pk_bf16_f32 v232,v232,v233
v_mfma_f32_16x16x32_bf16 v[76:79],a[176:179],v[132:135],v[76:79]
v_cvt_pk_bf16_f32 v233,v234,v235
v_cvt_pk_bf16_f32 v234,v236,v237
v_mfma_f32_16x16x32_bf16 v[80:83],a[128:131],v[164:167],v[36:39]
v_cvt_pk_bf16_f32 v235,v238,v239
v_accvgpr_read_b32 v16,a56
v_mfma_f32_16x16x32_bf16 v[80:83],a[132:135],v[172:175],v[80:83]
v_accvgpr_read_b32 v17,a57
v_accvgpr_read_b32 v18,a58
v_mfma_f32_16x16x32_bf16 v[80:83],a[136:139],v[180:183],v[80:83]
v_accvgpr_read_b32 v19,a59
v_accvgpr_read_b32 v20,a60
v_mfma_f32_16x16x32_bf16 v[80:83],a[140:143],v[188:191],v[80:83]
global_load_lds_dwordx4 v5,s[10:11] offset:1024
v_accvgpr_read_b32 v21,a61
v_mfma_f32_16x16x32_bf16 v[84:87],a[156:159],v[188:191],v[36:39]
v_accvgpr_read_b32 v22,a62
v_accvgpr_read_b32 v23,a63
v_mfma_f32_16x16x32_bf16 v[84:87],a[152:155],v[180:183],v[84:87]
v_mul_f32 v16,s71,v16
v_mul_f32 v17,s71,v17
v_mfma_f32_16x16x32_bf16 v[84:87],a[148:151],v[172:175],v[84:87]
v_mul_f32 v18,s71,v18
v_mul_f32 v19,s71,v19
v_mfma_f32_16x16x32_bf16 v[84:87],a[144:147],v[164:167],v[84:87]
v_mul_f32 v20,s71,v20
v_mul_f32 v21,s71,v21
v_mfma_f32_16x16x32_bf16 v[88:91],a[160:163],v[164:167],v[36:39]
v_mul_f32 v22,s71,v22
v_mul_f32 v23,s71,v23
v_mfma_f32_16x16x32_bf16 v[88:91],a[164:167],v[172:175],v[88:91]
v_cvt_pk_bf16_f32 v236,v16,v17
v_cvt_pk_bf16_f32 v237,v18,v19
v_mfma_f32_16x16x32_bf16 v[88:91],a[168:171],v[180:183],v[88:91]
v_cvt_pk_bf16_f32 v238,v20,v21
v_cvt_pk_bf16_f32 v239,v22,v23
v_mfma_f32_16x16x32_bf16 v[88:91],a[172:175],v[188:191],v[88:91]
v_mul_f32 v240,s71,v240
v_mul_f32 v241,s71,v241
v_mfma_f32_16x16x32_bf16 v[92:95],a[188:191],v[188:191],v[36:39]
v_mul_f32 v242,s71,v242
v_mul_f32 v243,s71,v243
v_mfma_f32_16x16x32_bf16 v[92:95],a[184:187],v[180:183],v[92:95]
v_mul_f32 v244,s71,v244
v_mul_f32 v245,s71,v245
v_mfma_f32_16x16x32_bf16 v[92:95],a[180:183],v[172:175],v[92:95]
v_mul_f32 v246,s71,v246
v_mul_f32 v247,s71,v247
v_mfma_f32_16x16x32_bf16 v[92:95],a[176:179],v[164:167],v[92:95]
v_cvt_pk_bf16_f32 v240,v240,v241
v_cvt_pk_bf16_f32 v241,v242,v243
v_cvt_pk_bf16_f32 v242,v244,v245
v_cvt_pk_bf16_f32 v243,v246,v247
v_mul_f32 v112,s71,v112
v_mul_f32 v113,s71,v113
v_mul_f32 v114,s71,v114
v_mul_f32 v115,s71,v115
v_mul_f32 v116,s71,v116
v_mul_f32 v117,s71,v117
v_mul_f32 v118,s71,v118
v_mul_f32 v119,s71,v119
v_cvt_pk_bf16_f32 v244,v112,v113
v_cvt_pk_bf16_f32 v245,v114,v115
v_cvt_pk_bf16_f32 v246,v116,v117
v_cvt_pk_bf16_f32 v247,v118,v119
v_permlane32_swap_b32 v96,v97
v_max_f32 v96,v96,v97
v_mov_b32 v97,v96
v_mfma_f32_16x16x32_bf16 v[112:115],a[128:131],v[228:231],0
v_mul_f32 v248,s71,v248
v_mul_f32 v249,s71,v249
v_mul_f32 v250,s71,v250
v_mul_f32 v251,s71,v251
v_mul_f32 v252,s71,v252
v_mul_f32 v253,s71,v253
v_mul_f32 v254,s71,v254
v_mul_f32 v255,s71,v255
v_cvt_pk_bf16_f32 v248,v248,v249
v_cvt_pk_bf16_f32 v249,v250,v251
v_cvt_pk_bf16_f32 v250,v252,v253
v_cvt_pk_bf16_f32 v251,v254,v255
v_mul_f32 v120,s71,v120
v_mul_f32 v121,s71,v121
v_mul_f32 v122,s71,v122
v_mul_f32 v123,s71,v123
v_mul_f32 v124,s71,v124
v_mul_f32 v125,s71,v125
v_mul_f32 v126,s71,v126
v_mul_f32 v127,s71,v127
v_cvt_pk_bf16_f32 v252,v120,v121
v_cvt_pk_bf16_f32 v253,v122,v123
v_cvt_pk_bf16_f32 v254,v124,v125
v_cvt_pk_bf16_f32 v255,v126,v127
v_mfma_f32_16x16x32_bf16 v[112:115],a[132:135],v[236:239],v[112:115]
global_load_lds_dwordx4 v4,s[10:11] offset:2048
v_permlane16_swap_b32 v96,v97
v_max_f32 v96,v96,v97
v_sub_f32 v43,0,v96
v_sub_f32 v42,0,v96
v_sub_f32 v41,0,v96
v_sub_f32 v40,0,v96
v_mfma_f32_16x16x32_bf16 v[112:115],a[136:139],v[244:247],v[112:115]
ds_read_b64_tr_b16 a[192:193],v2
ds_read_b64_tr_b16 a[194:195],v2 offset:4096
v_mfma_f32_16x16x32_bf16 v[112:115],a[140:143],v[252:255],v[112:115]
ds_read_b64_tr_b16 a[196:197],v2 offset:8192
ds_read_b64_tr_b16 a[198:199],v2 offset:12288
v_mfma_f32_16x16x32_bf16 v[96:99],a[128:131],v[196:199],v[40:43]
ds_read_b64_tr_b16 a[200:201],v3
ds_read_b64_tr_b16 a[202:203],v3 offset:4096
v_mfma_f32_16x16x32_bf16 v[96:99],a[132:135],v[204:207],v[96:99]
ds_read_b64_tr_b16 a[204:205],v3 offset:8192
ds_read_b64_tr_b16 a[206:207],v3 offset:12288
v_mfma_f32_16x16x32_bf16 v[96:99],a[136:139],v[212:215],v[96:99]
ds_read_b64_tr_b16 a[208:209],v2 offset:256
v_max3_f32 v112,v112,v113,v114
v_mfma_f32_16x16x32_bf16 v[96:99],a[140:143],v[220:223],v[96:99]
ds_read_b64_tr_b16 a[210:211],v2 offset:4352
ds_read_b64_tr_b16 a[212:213],v2 offset:8448
v_mfma_f32_16x16x32_bf16 v[100:103],a[156:159],v[220:223],v[40:43]
ds_read_b64_tr_b16 a[214:215],v2 offset:12544
v_max_f32 v112,v112,v115
v_mfma_f32_16x16x32_bf16 v[100:103],a[152:155],v[212:215],v[100:103]
ds_read_b64_tr_b16 a[216:217],v3 offset:256
ds_read_b64_tr_b16 a[218:219],v3 offset:4352
v_mfma_f32_16x16x32_bf16 v[100:103],a[148:151],v[204:207],v[100:103]
v_exp_f32 v64,v64
v_mfma_f32_16x16x32_bf16 v[100:103],a[144:147],v[196:199],v[100:103]
v_exp_f32 v65,v65
v_mfma_f32_16x16x32_bf16 v[104:107],a[160:163],v[196:199],v[40:43]
v_exp_f32 v66,v66
v_mfma_f32_16x16x32_bf16 v[104:107],a[164:167],v[204:207],v[104:107]
v_exp_f32 v67,v67
v_mfma_f32_16x16x32_bf16 v[104:107],a[168:171],v[212:215],v[104:107]
v_exp_f32 v68,v68
v_mfma_f32_16x16x32_bf16 v[104:107],a[172:175],v[220:223],v[104:107]
global_load_lds_dwordx4 v5,s[10:11] offset:3072
ds_read_b64_tr_b16 a[220:221],v3 offset:8448
v_mfma_f32_16x16x32_bf16 v[108:111],a[188:191],v[220:223],v[40:43]
v_exp_f32 v69,v69
v_exp_f32 v70,v70
v_exp_f32 v71,v71
v_mfma_f32_16x16x32_bf16 v[108:111],a[184:187],v[212:215],v[108:111]
)A"
R"A(ds_read_b64_tr_b16 a[222:223],v3 offset:12544
v_cvt_pk_bf16_f32 v64,v64,v65
v_cvt_pk_bf16_f32 v65,v66,v67
v_cvt_pk_bf16_f32 v66,v68,v69
v_cvt_pk_bf16_f32 v67,v70,v71
v_exp_f32 v72,v72
v_exp_f32 v73,v73
v_exp_f32 v74,v74
v_exp_f32 v75,v75
v_exp_f32 v76,v76
v_exp_f32 v77,v77
v_exp_f32 v78,v78
v_exp_f32 v79,v79
v_mfma_f32_16x16x32_bf16 v[108:111],a[180:183],v[204:207],v[108:111]
ds_read_b64_tr_b16 a[224:225],v2 offset:512
ds_read_b64_tr_b16 a[226:227],v2 offset:4608
v_cvt_pk_bf16_f32 v72,v72,v73
v_cvt_pk_bf16_f32 v73,v74,v75
v_cvt_pk_bf16_f32 v74,v76,v77
v_cvt_pk_bf16_f32 v75,v78,v79
v_mfma_f32_16x16x32_bf16 v[108:111],a[176:179],v[196:199],v[108:111]
s_waitcnt lgkmcnt(4)
ds_read_b64_tr_b16 a[228:229],v2 offset:8704
ds_read_b64_tr_b16 a[230:231],v2 offset:12800
v_mfma_f32_16x16x32_bf16 a[0:3],a[192:195],v[64:67],0
ds_read_b64_tr_b16 a[232:233],v3 offset:512
ds_read_b64_tr_b16 a[234:235],v3 offset:4608
v_mfma_f32_16x16x32_bf16 a[0:3],a[196:199],v[72:75],a[0:3]
ds_read_b64_tr_b16 a[236:237],v3 offset:8704
ds_read_b64_tr_b16 a[238:239],v3 offset:12800
v_mfma_f32_16x16x32_bf16 a[4:7],a[200:203],v[64:67],0
ds_read_b64_tr_b16 a[240:241],v2 offset:768
v_mov_b32 v113,v112
v_mfma_f32_16x16x32_bf16 a[4:7],a[204:207],v[72:75],a[4:7]
ds_read_b64_tr_b16 a[242:243],v2 offset:4864
ds_read_b64_tr_b16 a[244:245],v2 offset:8960
v_mfma_f32_16x16x32_bf16 a[8:11],a[208:211],v[64:67],0
ds_read_b64_tr_b16 a[246:247],v2 offset:13056
v_add_u32 v1,s78,v1
v_mfma_f32_16x16x32_bf16 a[8:11],a[212:215],v[72:75],a[8:11]
ds_read_b64_tr_b16 a[248:249],v3 offset:768
ds_read_b64_tr_b16 a[250:251],v3 offset:4864
v_mfma_f32_16x16x32_bf16 a[12:15],a[216:219],v[64:67],0
s_waitcnt lgkmcnt(12)
ds_read_b64_tr_b16 a[252:253],v3 offset:8960
v_add_u32 v2,s78,v2
v_mfma_f32_16x16x32_bf16 a[12:15],a[220:223],v[72:75],a[12:15]
s_waitcnt lgkmcnt(6)
ds_read_b64_tr_b16 a[254:255],v3 offset:13056
v_add_u32 v3,s78,v3
v_mfma_f32_16x16x32_bf16 a[16:19],a[224:227],v[64:67],0
s_sub_u32 s78,s77,32768
v_permlane32_swap_b32 v112,v113
v_mfma_f32_16x16x32_bf16 a[16:19],a[228:231],v[72:75],a[16:19]
s_and_b32 s78,s78,0x1ffff
v_max_f32 v112,v112,v113
v_mov_b32 v113,v112
v_mfma_f32_16x16x32_bf16 a[20:23],a[232:235],v[64:67],0
s_sub_u32 s78,s78,s77
v_exp_f32 v80,v80
v_mfma_f32_16x16x32_bf16 a[20:23],a[236:239],v[72:75],a[20:23]
s_add_u32 s77,s77,s78
v_permlane16_swap_b32 v112,v113
s_waitcnt lgkmcnt(5)
v_mfma_f32_16x16x32_bf16 a[24:27],a[240:243],v[64:67],0
s_waitcnt lgkmcnt(4)
v_max_f32 v112,v112,v113
v_max_f32 v112,s72,v112
v_mfma_f32_16x16x32_bf16 a[24:27],a[244:247],v[72:75],a[24:27]
s_waitcnt lgkmcnt(2)
v_sub_f32 v47,0,v112
v_sub_f32 v46,0,v112
v_mfma_f32_16x16x32_bf16 a[28:31],a[248:251],v[64:67],0
s_waitcnt lgkmcnt(0)
v_sub_f32 v45,0,v112
v_sub_f32 v44,0,v112
v_mfma_f32_16x16x32_bf16 a[28:31],a[252:255],v[72:75],a[28:31]
v_exp_f32 v81,v81
v_mfma_f32_16x16x32_bf16 v[48:51],v[28:31],v[64:67],0
v_exp_f32 v82,v82
v_mfma_f32_16x16x32_bf16 v[48:51],v[28:31],v[72:75],v[48:51]
v_exp_f32 v83,v83
s_waitcnt vmcnt(8)
s_barrier
v_mfma_f32_16x16x32_bf16 v[112:115],a[128:131],v[228:231],v[44:47]
s_add_u32 s10,s20,s74
s_addc_u32 s11,s21,0
s_add_u32 m0,s76,0
v_mfma_f32_16x16x32_bf16 v[112:115],a[132:135],v[236:239],v[112:115]
v_exp_f32 v84,v84
v_mfma_f32_16x16x32_bf16 v[112:115],a[136:139],v[244:247],v[112:115]
global_load_lds_dwordx4 v4,s[10:11] nt
v_cvt_pk_bf16_f32 v80,v80,v81
v_mfma_f32_16x16x32_bf16 v[112:115],a[140:143],v[252:255],v[112:115]
v_exp_f32 v85,v85
v_mfma_f32_16x16x32_bf16 v[116:119],a[156:159],v[252:255],v[44:47]
v_exp_f32 v86,v86
v_mfma_f32_16x16x32_bf16 v[116:119],a[152:155],v[244:247],v[116:119]
v_exp_f32 v87,v87
v_mfma_f32_16x16x32_bf16 v[116:119],a[148:151],v[236:239],v[116:119]
v_cvt_pk_bf16_f32 v81,v82,v83
v_cvt_pk_bf16_f32 v82,v84,v85
v_mfma_f32_16x16x32_bf16 v[116:119],a[144:147],v[228:231],v[116:119]
ds_read_b128 a[128:131],v1
v_cvt_pk_bf16_f32 v83,v86,v87
v_mfma_f32_16x16x32_bf16 v[120:123],a[160:163],v[228:231],v[44:47]
v_exp_f32 v88,v88
v_mfma_f32_16x16x32_bf16 v[120:123],a[164:167],v[236:239],v[120:123]
v_exp_f32 v89,v89
v_mfma_f32_16x16x32_bf16 v[120:123],a[168:171],v[244:247],v[120:123]
v_exp_f32 v90,v90
v_mfma_f32_16x16x32_bf16 v[120:123],a[172:175],v[252:255],v[120:123]
v_exp_f32 v91,v91
v_mfma_f32_16x16x32_bf16 v[124:127],a[188:191],v[252:255],v[44:47]
v_exp_f32 v92,v92
v_mfma_f32_16x16x32_bf16 v[124:127],a[184:187],v[244:247],v[124:127]
v_exp_f32 v93,v93
v_exp_f32 v94,v94
v_exp_f32 v95,v95
v_mfma_f32_16x16x32_bf16 v[124:127],a[180:183],v[236:239],v[124:127]
v_cvt_pk_bf16_f32 v88,v88,v89
v_cvt_pk_bf16_f32 v89,v90,v91
v_cvt_pk_bf16_f32 v90,v92,v93
v_cvt_pk_bf16_f32 v91,v94,v95
v_mfma_f32_16x16x32_bf16 v[124:127],a[176:179],v[228:231],v[124:127]
v_exp_f32 v96,v96
v_mfma_f32_16x16x32_bf16 a[32:35],a[192:195],v[80:83],0
global_load_lds_dwordx4 v4,s[10:11] offset:1024 nt
ds_read_b128 a[132:135],v1 offset:256
v_mfma_f32_16x16x32_bf16 a[32:35],a[196:199],v[88:91],a[32:35]
v_exp_f32 v97,v97
v_mfma_f32_16x16x32_bf16 a[36:39],a[200:203],v[80:83],0
ds_read_b128 a[136:139],v1 offset:512
v_cvt_pk_bf16_f32 v96,v96,v97
v_mfma_f32_16x16x32_bf16 a[36:39],a[204:207],v[88:91],a[36:39]
v_exp_f32 v98,v98
v_mfma_f32_16x16x32_bf16 a[40:43],a[208:211],v[80:83],0
v_exp_f32 v99,v99
v_mfma_f32_16x16x32_bf16 a[40:43],a[212:215],v[88:91],a[40:43]
ds_read_b128 a[140:143],v1 offset:768
v_cvt_pk_bf16_f32 v97,v98,v99
v_mfma_f32_16x16x32_bf16 a[44:47],a[216:219],v[80:83],0
v_exp_f32 v100,v100
v_mfma_f32_16x16x32_bf16 a[44:47],a[220:223],v[88:91],a[44:47]
v_exp_f32 v101,v101
v_mfma_f32_16x16x32_bf16 a[48:51],a[224:227],v[80:83],0
ds_read_b128 a[156:159],v1 offset:4864
v_cvt_pk_bf16_f32 v98,v100,v101
v_mfma_f32_16x16x32_bf16 a[48:51],a[228:231],v[88:91],a[48:51]
v_exp_f32 v102,v102
v_mfma_f32_16x16x32_bf16 a[52:55],a[232:235],v[80:83],0
v_exp_f32 v103,v103
v_mfma_f32_16x16x32_bf16 a[52:55],a[236:239],v[88:91],a[52:55]
ds_read_b128 a[152:155],v1 offset:4608
v_cvt_pk_bf16_f32 v99,v102,v103
v_mfma_f32_16x16x32_bf16 a[56:59],a[240:243],v[80:83],0
ds_read_b128 a[148:151],v1 offset:4352
v_mfma_f32_16x16x32_bf16 a[56:59],a[244:247],v[88:91],a[56:59]
ds_read_b128 a[144:147],v1 offset:4096
v_mfma_f32_16x16x32_bf16 a[60:63],a[248:251],v[80:83],0
global_load_lds_dwordx4 v5,s[10:11] offset:2048 nt
ds_read_b128 a[160:163],v1 offset:8192
v_mfma_f32_16x16x32_bf16 a[60:63],a[252:255],v[88:91],a[60:63]
v_exp_f32 v104,v104
v_mfma_f32_16x16x32_bf16 v[52:55],v[28:31],v[80:83],0
ds_read_b128 a[164:167],v1 offset:8448
v_mfma_f32_16x16x32_bf16 v[52:55],v[28:31],v[88:91],v[52:55]
s_waitcnt lgkmcnt(5)
ds_read_b128 a[168:171],v1 offset:8704
v_mfma_f32_16x16x32_bf16 v[64:67],a[128:131],v[132:135],v[32:35]
ds_read_b128 a[172:175],v1 offset:8960
v_mfma_f32_16x16x32_bf16 v[64:67],a[132:135],v[140:143],v[64:67]
v_exp_f32 v105,v105
v_mfma_f32_16x16x32_bf16 v[64:67],a[136:139],v[148:151],v[64:67]
ds_read_b128 a[188:191],v1 offset:13056
v_cvt_pk_bf16_f32 v104,v104,v105
v_mfma_f32_16x16x32_bf16 v[64:67],a[140:143],v[156:159],v[64:67]
ds_read_b128 a[184:187],v1 offset:12800
v_mfma_f32_16x16x32_bf16 v[68:71],a[156:159],v[156:159],v[32:35]
s_waitcnt lgkmcnt(5)
ds_read_b128 a[180:183],v1 offset:12544
v_mfma_f32_16x16x32_bf16 v[68:71],a[152:155],v[148:151],v[68:71]
v_exp_f32 v106,v106
v_mfma_f32_16x16x32_bf16 v[68:71],a[148:151],v[140:143],v[68:71]
ds_read_b128 a[176:179],v1 offset:12288
v_add_u32 v1,s78,v1
v_mfma_f32_16x16x32_bf16 v[68:71],a[144:147],v[132:135],v[68:71]
v_exp_f32 v107,v107
v_mfma_f32_16x16x32_bf16 v[72:75],a[160:163],v[132:135],v[32:35]
s_waitcnt lgkmcnt(4)
v_exp_f32 v108,v108
v_mfma_f32_16x16x32_bf16 v[72:75],a[164:167],v[140:143],v[72:75]
v_exp_f32 v109,v109
v_mfma_f32_16x16x32_bf16 v[72:75],a[168:171],v[148:151],v[72:75]
global_load_lds_dwordx4 v5,s[10:11] offset:3072
v_cvt_pk_bf16_f32 v105,v106,v107
v_mfma_f32_16x16x32_bf16 v[72:75],a[172:175],v[156:159],v[72:75]
s_waitcnt lgkmcnt(2)
v_exp_f32 v110,v110
v_mfma_f32_16x16x32_bf16 v[76:79],a[188:191],v[156:159],v[32:35]
v_exp_f32 v111,v111
v_mfma_f32_16x16x32_bf16 v[76:79],a[184:187],v[148:151],v[76:79]
s_waitcnt lgkmcnt(1)
v_cvt_pk_bf16_f32 v106,v108,v109
v_cvt_pk_bf16_f32 v107,v110,v111
v_mfma_f32_16x16x32_bf16 v[76:79],a[180:183],v[140:143],v[76:79]
s_waitcnt lgkmcnt(0)
v_exp_f32 v112,v112
v_mfma_f32_16x16x32_bf16 v[76:79],a[176:179],v[132:135],v[76:79]
v_exp_f32 v113,v113
v_mfma_f32_16x16x32_bf16 a[64:67],a[192:195],v[96:99],0
v_exp_f32 v114,v114
v_mfma_f32_16x16x32_bf16 a[64:67],a[196:199],v[104:107],a[64:67]
v_exp_f32 v115,v115
v_mfma_f32_16x16x32_bf16 a[68:71],a[200:203],v[96:99],0
v_exp_f32 v116,v116
v_mfma_f32_16x16x32_bf16 a[68:71],a[204:207],v[104:107],a[68:71]
v_exp_f32 v117,v117
v_mfma_f32_16x16x32_bf16 a[72:75],a[208:211],v[96:99],0
s_add_u32 s10,s24,s74
s_addc_u32 s11,s25,0
s_add_u32 s74,s74,16384
s_and_b32 s74,s74,0x3ffff
s_or_b32 s74,s74,s69
s_lshl_b32 s69,s60,13
s_add_u32 s69,s69,131072
v_add_u32 v8,s69,v6
v_mfma_f32_16x16x32_bf16 a[72:75],a[212:215],v[104:107],a[72:75]
s_lshl_b32 s69,s60,13
ds_write_b128 v8,v[24:27] offset:4096
v_mfma_f32_16x16x32_bf16 a[76:79],a[216:219],v[96:99],0
s_add_u32 m0,s76,16384
ds_write_b32 v8,v7 offset:5120
v_cvt_pk_bf16_f32 v112,v112,v113
v_mfma_f32_16x16x32_bf16 a[76:79],a[220:223],v[104:107],a[76:79]
s_sub_u32 s76,s76,32768
v_bfe_u32 v24,v6,4,4
v_cvt_pk_bf16_f32 v113,v114,v115
v_mfma_f32_16x16x32_bf16 a[80:83],a[224:227],v[96:99],0
global_load_lds_dwordx4 v4,s[10:11]
v_lshrrev_b32 v7,8,v6
v_lshlrev_b32 v7,4,v7
v_lshl_add_u32 v7,v24,8,v7
v_mfma_f32_16x16x32_bf16 a[80:83],a[228:231],v[104:107],a[80:83]
s_and_b32 s76,s76,0x1ffff
v_exp_f32 v118,v118
v_mfma_f32_16x16x32_bf16 a[84:87],a[232:235],v[96:99],0
s_add_u32 s69,s69,131072
v_exp_f32 v119,v119
v_mfma_f32_16x16x32_bf16 a[84:87],a[236:239],v[104:107],a[84:87]
v_cvt_pk_bf16_f32 v114,v116,v117
v_cvt_pk_bf16_f32 v115,v118,v119
v_mfma_f32_16x16x32_bf16 a[88:91],a[240:243],v[96:99],0
v_exp_f32 v120,v120
v_mfma_f32_16x16x32_bf16 a[88:91],a[244:247],v[104:107],a[88:91]
buffer_load_dwordx4 v[8:11],v7,s[20:23],s65 offen nt
v_mfma_f32_16x16x32_bf16 a[92:95],a[248:251],v[96:99],0
v_exp_f32 v121,v121
v_mfma_f32_16x16x32_bf16 a[92:95],a[252:255],v[104:107],a[92:95]
v_exp_f32 v122,v122
v_mfma_f32_16x16x32_bf16 v[56:59],v[28:31],v[96:99],0
v_exp_f32 v123,v123
v_mfma_f32_16x16x32_bf16 v[56:59],v[28:31],v[104:107],v[56:59]
v_exp_f32 v124,v124
v_mfma_f32_16x16x32_bf16 v[80:83],a[128:131],v[164:167],v[36:39]
buffer_load_dwordx4 v[12:15],v7,s[20:23],s65 offen offset:64 nt
v_mfma_f32_16x16x32_bf16 v[80:83],a[132:135],v[172:175],v[80:83]
v_exp_f32 v125,v125
v_mfma_f32_16x16x32_bf16 v[80:83],a[136:139],v[180:183],v[80:83]
v_exp_f32 v126,v126
v_mfma_f32_16x16x32_bf16 v[80:83],a[140:143],v[188:191],v[80:83]
v_exp_f32 v127,v127
v_mfma_f32_16x16x32_bf16 v[84:87],a[156:159],v[188:191],v[36:39]
v_cvt_pk_bf16_f32 v120,v120,v121
v_cvt_pk_bf16_f32 v121,v122,v123
v_mfma_f32_16x16x32_bf16 v[84:87],a[152:155],v[180:183],v[84:87]
buffer_load_dwordx4 v[16:19],v7,s[20:23],s65 offen offset:128 nt
v_mfma_f32_16x16x32_bf16 v[84:87],a[148:151],v[172:175],v[84:87]
v_cvt_pk_bf16_f32 v122,v124,v125
v_cvt_pk_bf16_f32 v123,v126,v127
v_mfma_f32_16x16x32_bf16 v[84:87],a[144:147],v[164:167],v[84:87]
v_exp_f32 v64,v64
v_mfma_f32_16x16x32_bf16 v[88:91],a[160:163],v[164:167],v[36:39]
v_exp_f32 v65,v65
v_mfma_f32_16x16x32_bf16 v[88:91],a[164:167],v[172:175],v[88:91]
v_exp_f32 v66,v66
v_mfma_f32_16x16x32_bf16 v[88:91],a[168:171],v[180:183],v[88:91]
v_exp_f32 v67,v67
v_mfma_f32_16x16x32_bf16 v[88:91],a[172:175],v[188:191],v[88:91]
global_load_lds_dwordx4 v5,s[10:11] offset:1024
v_cvt_pk_bf16_f32 v64,v64,v65
v_mfma_f32_16x16x32_bf16 v[92:95],a[188:191],v[188:191],v[36:39]
v_exp_f32 v68,v68
v_mfma_f32_16x16x32_bf16 v[92:95],a[184:187],v[180:183],v[92:95]
v_exp_f32 v69,v69
v_mfma_f32_16x16x32_bf16 v[92:95],a[180:183],v[172:175],v[92:95]
v_exp_f32 v70,v70
v_mfma_f32_16x16x32_bf16 v[92:95],a[176:179],v[164:167],v[92:95]
v_exp_f32 v71,v71
v_mfma_f32_16x16x32_bf16 a[96:99],a[192:195],v[112:115],0
buffer_load_dwordx4 v[20:23],v7,s[20:23],s65 offen offset:192 nt
v_mfma_f32_16x16x32_bf16 a[96:99],a[196:199],v[120:123],a[96:99]
)A"
R"A(ds_read_b64_tr_b16 a[192:193],v2
ds_read_b64_tr_b16 a[194:195],v2 offset:4096
v_mfma_f32_16x16x32_bf16 a[100:103],a[200:203],v[112:115],0
ds_read_b64_tr_b16 a[196:197],v2 offset:8192
ds_read_b64_tr_b16 a[198:199],v2 offset:12288
v_mfma_f32_16x16x32_bf16 a[100:103],a[204:207],v[120:123],a[100:103]
ds_read_b64_tr_b16 a[200:201],v3
ds_read_b64_tr_b16 a[202:203],v3 offset:4096
v_mfma_f32_16x16x32_bf16 a[104:107],a[208:211],v[112:115],0
ds_read_b64_tr_b16 a[204:205],v3 offset:8192
ds_read_b64_tr_b16 a[206:207],v3 offset:12288
v_mfma_f32_16x16x32_bf16 a[104:107],a[212:215],v[120:123],a[104:107]
ds_read_b64_tr_b16 a[208:209],v2 offset:256
v_cvt_pk_bf16_f32 v65,v66,v67
v_mfma_f32_16x16x32_bf16 a[108:111],a[216:219],v[112:115],0
global_load_lds_dwordx4 v4,s[10:11] offset:2048
ds_read_b64_tr_b16 a[210:211],v2 offset:4352
v_mfma_f32_16x16x32_bf16 a[108:111],a[220:223],v[120:123],a[108:111]
ds_read_b64_tr_b16 a[212:213],v2 offset:8448
ds_read_b64_tr_b16 a[214:215],v2 offset:12544
v_mfma_f32_16x16x32_bf16 a[112:115],a[224:227],v[112:115],0
ds_read_b64_tr_b16 a[216:217],v3 offset:256
ds_read_b64_tr_b16 a[218:219],v3 offset:4352
v_mfma_f32_16x16x32_bf16 a[112:115],a[228:231],v[120:123],a[112:115]
v_cvt_pk_bf16_f32 v66,v68,v69
v_cvt_pk_bf16_f32 v67,v70,v71
v_mfma_f32_16x16x32_bf16 a[116:119],a[232:235],v[112:115],0
v_exp_f32 v72,v72
v_mfma_f32_16x16x32_bf16 a[116:119],a[236:239],v[120:123],a[116:119]
v_exp_f32 v73,v73
v_mfma_f32_16x16x32_bf16 a[120:123],a[240:243],v[112:115],0
v_exp_f32 v74,v74
v_mfma_f32_16x16x32_bf16 a[120:123],a[244:247],v[120:123],a[120:123]
v_exp_f32 v75,v75
v_mfma_f32_16x16x32_bf16 a[124:127],a[248:251],v[112:115],0
v_exp_f32 v76,v76
v_mfma_f32_16x16x32_bf16 a[124:127],a[252:255],v[120:123],a[124:127]
v_exp_f32 v77,v77
v_mfma_f32_16x16x32_bf16 v[60:63],v[28:31],v[112:115],0
v_exp_f32 v78,v78
v_mfma_f32_16x16x32_bf16 v[60:63],v[28:31],v[120:123],v[60:63]
global_load_lds_dwordx4 v5,s[10:11] offset:3072
ds_read_b64_tr_b16 a[220:221],v3 offset:8448
v_mfma_f32_16x16x32_bf16 v[96:99],a[128:131],v[196:199],v[40:43]
v_exp_f32 v79,v79
v_mfma_f32_16x16x32_bf16 v[96:99],a[132:135],v[204:207],v[96:99]
ds_read_b64_tr_b16 a[222:223],v3 offset:12544
v_cvt_pk_bf16_f32 v72,v72,v73
v_mfma_f32_16x16x32_bf16 v[96:99],a[136:139],v[212:215],v[96:99]
v_cvt_pk_bf16_f32 v73,v74,v75
v_cvt_pk_bf16_f32 v74,v76,v77
v_mfma_f32_16x16x32_bf16 v[96:99],a[140:143],v[220:223],v[96:99]
ds_read_b64_tr_b16 a[224:225],v2 offset:512
v_cvt_pk_bf16_f32 v75,v78,v79
v_mfma_f32_16x16x32_bf16 v[100:103],a[156:159],v[220:223],v[40:43]
ds_read_b64_tr_b16 a[226:227],v2 offset:4608
ds_read_b64_tr_b16 a[228:229],v2 offset:8704
v_mfma_f32_16x16x32_bf16 v[100:103],a[152:155],v[212:215],v[100:103]
ds_read_b64_tr_b16 a[230:231],v2 offset:12800
ds_read_b64_tr_b16 a[232:233],v3 offset:512
v_mfma_f32_16x16x32_bf16 v[100:103],a[148:151],v[204:207],v[100:103]
ds_read_b64_tr_b16 a[234:235],v3 offset:4608
ds_read_b64_tr_b16 a[236:237],v3 offset:8704
v_mfma_f32_16x16x32_bf16 v[100:103],a[144:147],v[196:199],v[100:103]
ds_read_b64_tr_b16 a[238:239],v3 offset:12800
ds_read_b64_tr_b16 a[240:241],v2 offset:768
v_mfma_f32_16x16x32_bf16 v[104:107],a[160:163],v[196:199],v[40:43]
ds_read_b64_tr_b16 a[242:243],v2 offset:4864
v_add_u32 v7,s69,v6
v_mfma_f32_16x16x32_bf16 v[104:107],a[164:167],v[204:207],v[104:107]
s_add_u32 s10,s20,s74
s_addc_u32 s11,s21,0
ds_read_b64_tr_b16 a[244:245],v2 offset:8960
v_mfma_f32_16x16x32_bf16 v[104:107],a[168:171],v[212:215],v[104:107]
s_and_b32 s69,s75,0xfffc0000
ds_read_b64_tr_b16 a[246:247],v2 offset:13056
ds_read_b64_tr_b16 a[248:249],v3 offset:768
v_mfma_f32_16x16x32_bf16 v[104:107],a[172:175],v[220:223],v[104:107]
s_add_u32 m0,s76,0
ds_read_b64_tr_b16 a[250:251],v3 offset:4864
ds_read_b64_tr_b16 a[252:253],v3 offset:8960
v_mfma_f32_16x16x32_bf16 v[108:111],a[188:191],v[220:223],v[40:43]
s_add_u32 s75,s75,16384
ds_read_b64_tr_b16 a[254:255],v3 offset:13056
v_add_u32 v2,s78,v2
v_mfma_f32_16x16x32_bf16 v[108:111],a[184:187],v[212:215],v[108:111]
global_load_lds_dwordx4 v4,s[10:11] nt
v_add_u32 v3,s78,v3
v_mfma_f32_16x16x32_bf16 v[108:111],a[180:183],v[204:207],v[108:111]
s_sub_u32 s78,s77,32768
v_exp_f32 v80,v80
v_mfma_f32_16x16x32_bf16 v[108:111],a[176:179],v[196:199],v[108:111]
s_and_b32 s78,s78,0x1ffff
v_exp_f32 v81,v81
s_waitcnt lgkmcnt(7)
v_mfma_f32_16x16x32_bf16 a[0:3],a[192:195],v[64:67],a[0:3]
s_sub_u32 s78,s78,s77
v_exp_f32 v82,v82
v_mfma_f32_16x16x32_bf16 a[0:3],a[196:199],v[72:75],a[0:3]
s_add_u32 s77,s77,s78
v_exp_f32 v83,v83
v_mfma_f32_16x16x32_bf16 a[4:7],a[200:203],v[64:67],a[4:7]
v_exp_f32 v84,v84
v_mfma_f32_16x16x32_bf16 a[4:7],a[204:207],v[72:75],a[4:7]
v_exp_f32 v85,v85
L_pd5_%=:
s_cbranch_execz L_trt0_%=
v_mfma_f32_16x16x32_bf16 a[8:11],a[208:211],v[64:67],a[8:11]
v_exp_f32 v86,v86
v_mfma_f32_16x16x32_bf16 a[8:11],a[212:215],v[72:75],a[8:11]
v_exp_f32 v87,v87
v_mfma_f32_16x16x32_bf16 a[12:15],a[216:219],v[64:67],a[12:15]
v_cvt_pk_bf16_f32 v80,v80,v81
v_cvt_pk_bf16_f32 v81,v82,v83
v_mfma_f32_16x16x32_bf16 a[12:15],a[220:223],v[72:75],a[12:15]
v_cvt_pk_bf16_f32 v82,v84,v85
v_cvt_pk_bf16_f32 v83,v86,v87
v_mfma_f32_16x16x32_bf16 a[16:19],a[224:227],v[64:67],a[16:19]
v_exp_f32 v88,v88
v_mfma_f32_16x16x32_bf16 a[16:19],a[228:231],v[72:75],a[16:19]
v_exp_f32 v89,v89
v_mfma_f32_16x16x32_bf16 a[20:23],a[232:235],v[64:67],a[20:23]
v_exp_f32 v90,v90
v_mfma_f32_16x16x32_bf16 a[20:23],a[236:239],v[72:75],a[20:23]
s_waitcnt lgkmcnt(0)
v_exp_f32 v91,v91
v_mfma_f32_16x16x32_bf16 a[24:27],a[240:243],v[64:67],a[24:27]
v_exp_f32 v92,v92
v_mfma_f32_16x16x32_bf16 a[24:27],a[244:247],v[72:75],a[24:27]
v_exp_f32 v93,v93
v_mfma_f32_16x16x32_bf16 a[28:31],a[248:251],v[64:67],a[28:31]
v_exp_f32 v94,v94
v_mfma_f32_16x16x32_bf16 a[28:31],a[252:255],v[72:75],a[28:31]
s_getpc_b64 s[92:93]
s_cmp_lg_u32 s4,0
s_cbranch_scc1 L_pace_%=
v_mfma_f32_16x16x32_bf16 v[48:51],v[28:31],v[64:67],v[48:51]
v_exp_f32 v95,v95
v_mfma_f32_16x16x32_bf16 v[48:51],v[28:31],v[72:75],v[48:51]
s_add_u32 s10,s20,s74
s_addc_u32 s11,s21,0
v_cvt_pk_bf16_f32 v88,v88,v89
s_waitcnt vmcnt(13)
s_barrier
v_mfma_f32_16x16x32_bf16 v[112:115],a[128:131],v[228:231],v[44:47]
s_add_u32 m0,s76,0
v_cvt_pk_bf16_f32 v89,v90,v91
v_cvt_pk_bf16_f32 v90,v92,v93
v_mfma_f32_16x16x32_bf16 v[112:115],a[132:135],v[236:239],v[112:115]
ds_read_b128 a[128:131],v1
v_cvt_pk_bf16_f32 v91,v94,v95
v_mfma_f32_16x16x32_bf16 v[112:115],a[136:139],v[244:247],v[112:115]
global_load_lds_dwordx4 v4,s[10:11] offset:1024 nt
ds_read_b128 a[132:135],v1 offset:256
v_mfma_f32_16x16x32_bf16 v[112:115],a[140:143],v[252:255],v[112:115]
v_exp_f32 v96,v96
v_mfma_f32_16x16x32_bf16 v[116:119],a[156:159],v[252:255],v[44:47]
v_exp_f32 v97,v97
v_mfma_f32_16x16x32_bf16 v[116:119],a[152:155],v[244:247],v[116:119]
ds_read_b128 a[136:139],v1 offset:512
v_cvt_pk_bf16_f32 v96,v96,v97
v_mfma_f32_16x16x32_bf16 v[116:119],a[148:151],v[236:239],v[116:119]
v_exp_f32 v98,v98
v_mfma_f32_16x16x32_bf16 v[116:119],a[144:147],v[228:231],v[116:119]
v_exp_f32 v99,v99
v_mfma_f32_16x16x32_bf16 v[120:123],a[160:163],v[228:231],v[44:47]
ds_read_b128 a[140:143],v1 offset:768
v_cvt_pk_bf16_f32 v97,v98,v99
v_mfma_f32_16x16x32_bf16 v[120:123],a[164:167],v[236:239],v[120:123]
v_exp_f32 v100,v100
v_mfma_f32_16x16x32_bf16 v[120:123],a[168:171],v[244:247],v[120:123]
v_exp_f32 v101,v101
v_mfma_f32_16x16x32_bf16 v[120:123],a[172:175],v[252:255],v[120:123]
ds_read_b128 a[156:159],v1 offset:4864
v_cvt_pk_bf16_f32 v98,v100,v101
v_mfma_f32_16x16x32_bf16 v[124:127],a[188:191],v[252:255],v[44:47]
v_exp_f32 v102,v102
v_mfma_f32_16x16x32_bf16 v[124:127],a[184:187],v[244:247],v[124:127]
v_exp_f32 v103,v103
v_mfma_f32_16x16x32_bf16 v[124:127],a[180:183],v[236:239],v[124:127]
ds_read_b128 a[152:155],v1 offset:4608
v_cvt_pk_bf16_f32 v99,v102,v103
v_mfma_f32_16x16x32_bf16 v[124:127],a[176:179],v[228:231],v[124:127]
v_exp_f32 v104,v104
v_mfma_f32_16x16x32_bf16 a[32:35],a[192:195],v[80:83],a[32:35]
global_load_lds_dwordx4 v5,s[10:11] offset:2048 nt
ds_read_b128 a[148:151],v1 offset:4352
v_mfma_f32_16x16x32_bf16 a[32:35],a[196:199],v[88:91],a[32:35]
v_exp_f32 v105,v105
v_mfma_f32_16x16x32_bf16 a[36:39],a[200:203],v[80:83],a[36:39]
ds_read_b128 a[144:147],v1 offset:4096
v_cvt_pk_bf16_f32 v104,v104,v105
v_mfma_f32_16x16x32_bf16 a[36:39],a[204:207],v[88:91],a[36:39]
v_exp_f32 v106,v106
v_mfma_f32_16x16x32_bf16 a[40:43],a[208:211],v[80:83],a[40:43]
v_exp_f32 v107,v107
v_mfma_f32_16x16x32_bf16 a[40:43],a[212:215],v[88:91],a[40:43]
ds_read_b128 a[160:163],v1 offset:8192
v_cvt_pk_bf16_f32 v105,v106,v107
v_mfma_f32_16x16x32_bf16 a[44:47],a[216:219],v[80:83],a[44:47]
v_exp_f32 v108,v108
v_mfma_f32_16x16x32_bf16 a[44:47],a[220:223],v[88:91],a[44:47]
v_exp_f32 v109,v109
v_mfma_f32_16x16x32_bf16 a[48:51],a[224:227],v[80:83],a[48:51]
ds_read_b128 a[164:167],v1 offset:8448
v_cvt_pk_bf16_f32 v106,v108,v109
v_mfma_f32_16x16x32_bf16 a[48:51],a[228:231],v[88:91],a[48:51]
v_exp_f32 v110,v110
v_mfma_f32_16x16x32_bf16 a[52:55],a[232:235],v[80:83],a[52:55]
v_exp_f32 v111,v111
v_mfma_f32_16x16x32_bf16 a[52:55],a[236:239],v[88:91],a[52:55]
ds_read_b128 a[168:171],v1 offset:8704
v_cvt_pk_bf16_f32 v107,v110,v111
v_mfma_f32_16x16x32_bf16 a[56:59],a[240:243],v[80:83],a[56:59]
v_exp_f32 v112,v112
v_mfma_f32_16x16x32_bf16 a[56:59],a[244:247],v[88:91],a[56:59]
v_exp_f32 v113,v113
v_mfma_f32_16x16x32_bf16 a[60:63],a[248:251],v[80:83],a[60:63]
global_load_lds_dwordx4 v5,s[10:11] offset:3072
ds_read_b128 a[172:175],v1 offset:8960
v_mfma_f32_16x16x32_bf16 a[60:63],a[252:255],v[88:91],a[60:63]
ds_read_b128 a[188:191],v1 offset:13056
v_cvt_pk_bf16_f32 v112,v112,v113
v_mfma_f32_16x16x32_bf16 v[52:55],v[28:31],v[80:83],v[52:55]
v_exp_f32 v114,v114
v_mfma_f32_16x16x32_bf16 v[52:55],v[28:31],v[88:91],v[52:55]
s_waitcnt lgkmcnt(3)
v_exp_f32 v115,v115
v_mfma_f32_16x16x32_bf16 v[64:67],a[128:131],v[132:135],v[32:35]
ds_read_b128 a[184:187],v1 offset:12800
v_cvt_pk_bf16_f32 v113,v114,v115
v_mfma_f32_16x16x32_bf16 v[64:67],a[132:135],v[140:143],v[64:67]
v_exp_f32 v116,v116
v_mfma_f32_16x16x32_bf16 v[64:67],a[136:139],v[148:151],v[64:67]
ds_read_b128 a[180:183],v1 offset:12544
v_mfma_f32_16x16x32_bf16 v[64:67],a[140:143],v[156:159],v[64:67]
ds_read_b128 a[176:179],v1 offset:12288
v_add_u32 v1,s78,v1
v_mfma_f32_16x16x32_bf16 v[68:71],a[156:159],v[156:159],v[32:35]
v_exp_f32 v117,v117
v_mfma_f32_16x16x32_bf16 v[68:71],a[152:155],v[148:151],v[68:71]
v_exp_f32 v118,v118
v_mfma_f32_16x16x32_bf16 v[68:71],a[148:151],v[140:143],v[68:71]
s_add_u32 s10,s24,s74
s_addc_u32 s11,s25,0
v_cvt_pk_bf16_f32 v114,v116,v117
v_mfma_f32_16x16x32_bf16 v[68:71],a[144:147],v[132:135],v[68:71]
s_add_u32 s74,s74,16384
v_exp_f32 v119,v119
v_mfma_f32_16x16x32_bf16 v[72:75],a[160:163],v[132:135],v[32:35]
s_add_u32 m0,s76,16384
v_exp_f32 v120,v120
v_mfma_f32_16x16x32_bf16 v[72:75],a[164:167],v[140:143],v[72:75]
s_and_b32 s74,s74,0x3ffff
v_exp_f32 v121,v121
s_waitcnt lgkmcnt(2)
v_mfma_f32_16x16x32_bf16 v[72:75],a[168:171],v[148:151],v[72:75]
global_load_lds_dwordx4 v4,s[10:11]
v_cvt_pk_bf16_f32 v115,v118,v119
v_mfma_f32_16x16x32_bf16 v[72:75],a[172:175],v[156:159],v[72:75]
s_or_b32 s74,s74,s69
v_exp_f32 v122,v122
v_mfma_f32_16x16x32_bf16 v[76:79],a[188:191],v[156:159],v[32:35]
s_sub_u32 s76,s76,32768
v_exp_f32 v123,v123
v_mfma_f32_16x16x32_bf16 v[76:79],a[184:187],v[148:151],v[76:79]
s_and_b32 s76,s76,0x1ffff
v_exp_f32 v124,v124
s_waitcnt lgkmcnt(0)
v_mfma_f32_16x16x32_bf16 v[76:79],a[180:183],v[140:143],v[76:79]
s_and_b32 s69,s75,0xfffc0000
v_exp_f32 v125,v125
v_mfma_f32_16x16x32_bf16 v[76:79],a[176:179],v[132:135],v[76:79]
v_exp_f32 v126,v126
v_mfma_f32_16x16x32_bf16 a[64:67],a[192:195],v[96:99],a[64:67]
v_exp_f32 v127,v127
v_mfma_f32_16x16x32_bf16 a[64:67],a[196:199],v[104:107],a[64:67]
v_cvt_pk_bf16_f32 v120,v120,v121
v_cvt_pk_bf16_f32 v121,v122,v123
v_mfma_f32_16x16x32_bf16 a[68:71],a[200:203],v[96:99],a[68:71]
v_cvt_pk_bf16_f32 v122,v124,v125
v_cvt_pk_bf16_f32 v123,v126,v127
v_mfma_f32_16x16x32_bf16 a[68:71],a[204:207],v[104:107],a[68:71]
v_exp_f32 v64,v64
v_mfma_f32_16x16x32_bf16 a[72:75],a[208:211],v[96:99],a[72:75]
)A"
R"A(v_exp_f32 v65,v65
v_mfma_f32_16x16x32_bf16 a[72:75],a[212:215],v[104:107],a[72:75]
v_exp_f32 v66,v66
v_mfma_f32_16x16x32_bf16 a[76:79],a[216:219],v[96:99],a[76:79]
v_exp_f32 v67,v67
v_mfma_f32_16x16x32_bf16 a[76:79],a[220:223],v[104:107],a[76:79]
v_exp_f32 v68,v68
v_mfma_f32_16x16x32_bf16 a[80:83],a[224:227],v[96:99],a[80:83]
global_load_lds_dwordx4 v5,s[10:11] offset:1024
v_cvt_pk_bf16_f32 v64,v64,v65
v_mfma_f32_16x16x32_bf16 a[80:83],a[228:231],v[104:107],a[80:83]
v_exp_f32 v69,v69
v_mfma_f32_16x16x32_bf16 a[84:87],a[232:235],v[96:99],a[84:87]
v_exp_f32 v70,v70
v_mfma_f32_16x16x32_bf16 a[84:87],a[236:239],v[104:107],a[84:87]
v_exp_f32 v71,v71
v_mfma_f32_16x16x32_bf16 a[88:91],a[240:243],v[96:99],a[88:91]
v_cvt_pk_bf16_f32 v65,v66,v67
v_cvt_pk_bf16_f32 v66,v68,v69
v_mfma_f32_16x16x32_bf16 a[88:91],a[244:247],v[104:107],a[88:91]
v_exp_f32 v72,v72
v_mfma_f32_16x16x32_bf16 a[92:95],a[248:251],v[96:99],a[92:95]
v_exp_f32 v73,v73
v_mfma_f32_16x16x32_bf16 a[92:95],a[252:255],v[104:107],a[92:95]
v_cvt_pk_bf16_f32 v67,v70,v71
v_cvt_pk_bf16_f32 v72,v72,v73
v_mfma_f32_16x16x32_bf16 v[56:59],v[28:31],v[96:99],v[56:59]
v_exp_f32 v74,v74
v_mfma_f32_16x16x32_bf16 v[56:59],v[28:31],v[104:107],v[56:59]
v_exp_f32 v75,v75
v_mfma_f32_16x16x32_bf16 v[80:83],a[128:131],v[164:167],v[36:39]
v_exp_f32 v76,v76
v_mfma_f32_16x16x32_bf16 v[80:83],a[132:135],v[172:175],v[80:83]
v_exp_f32 v77,v77
v_mfma_f32_16x16x32_bf16 v[80:83],a[136:139],v[180:183],v[80:83]
v_exp_f32 v78,v78
v_mfma_f32_16x16x32_bf16 v[80:83],a[140:143],v[188:191],v[80:83]
v_exp_f32 v79,v79
v_mfma_f32_16x16x32_bf16 v[84:87],a[156:159],v[188:191],v[36:39]
v_cvt_pk_bf16_f32 v73,v74,v75
v_cvt_pk_bf16_f32 v74,v76,v77
v_mfma_f32_16x16x32_bf16 v[84:87],a[152:155],v[180:183],v[84:87]
global_load_lds_dwordx4 v4,s[10:11] offset:2048
v_cvt_pk_bf16_f32 v75,v78,v79
v_mfma_f32_16x16x32_bf16 v[84:87],a[148:151],v[172:175],v[84:87]
v_exp_f32 v80,v80
v_mfma_f32_16x16x32_bf16 v[84:87],a[144:147],v[164:167],v[84:87]
v_exp_f32 v81,v81
v_mfma_f32_16x16x32_bf16 v[88:91],a[160:163],v[164:167],v[36:39]
v_exp_f32 v82,v82
v_mfma_f32_16x16x32_bf16 v[88:91],a[164:167],v[172:175],v[88:91]
v_exp_f32 v83,v83
v_mfma_f32_16x16x32_bf16 v[88:91],a[168:171],v[180:183],v[88:91]
v_cvt_pk_bf16_f32 v80,v80,v81
v_cvt_pk_bf16_f32 v81,v82,v83
v_mfma_f32_16x16x32_bf16 v[88:91],a[172:175],v[188:191],v[88:91]
v_exp_f32 v84,v84
v_mfma_f32_16x16x32_bf16 v[92:95],a[188:191],v[188:191],v[36:39]
v_exp_f32 v85,v85
v_mfma_f32_16x16x32_bf16 v[92:95],a[184:187],v[180:183],v[92:95]
v_exp_f32 v86,v86
v_mfma_f32_16x16x32_bf16 v[92:95],a[180:183],v[172:175],v[92:95]
v_exp_f32 v87,v87
v_mfma_f32_16x16x32_bf16 v[92:95],a[176:179],v[164:167],v[92:95]
v_cvt_pk_bf16_f32 v82,v84,v85
v_cvt_pk_bf16_f32 v83,v86,v87
v_mfma_f32_16x16x32_bf16 a[96:99],a[192:195],v[112:115],a[96:99]
ds_read_b64_tr_b16 a[192:193],v2
ds_read_b64_tr_b16 a[194:195],v2 offset:4096
v_mfma_f32_16x16x32_bf16 a[96:99],a[196:199],v[120:123],a[96:99]
ds_read_b64_tr_b16 a[196:197],v2 offset:8192
ds_read_b64_tr_b16 a[198:199],v2 offset:12288
v_mfma_f32_16x16x32_bf16 a[100:103],a[200:203],v[112:115],a[100:103]
ds_read_b64_tr_b16 a[200:201],v3
ds_read_b64_tr_b16 a[202:203],v3 offset:4096
v_mfma_f32_16x16x32_bf16 a[100:103],a[204:207],v[120:123],a[100:103]
global_load_lds_dwordx4 v5,s[10:11] offset:3072
ds_read_b64_tr_b16 a[204:205],v3 offset:8192
v_mfma_f32_16x16x32_bf16 a[104:107],a[208:211],v[112:115],a[104:107]
ds_read_b64_tr_b16 a[206:207],v3 offset:12288
ds_read_b64_tr_b16 a[208:209],v2 offset:256
v_mfma_f32_16x16x32_bf16 a[104:107],a[212:215],v[120:123],a[104:107]
ds_read_b64_tr_b16 a[210:211],v2 offset:4352
ds_read_b64_tr_b16 a[212:213],v2 offset:8448
v_mfma_f32_16x16x32_bf16 a[108:111],a[216:219],v[112:115],a[108:111]
v_exp_f32 v88,v88
v_mfma_f32_16x16x32_bf16 a[108:111],a[220:223],v[120:123],a[108:111]
ds_read_b64_tr_b16 a[214:215],v2 offset:12544
ds_read_b64_tr_b16 a[216:217],v3 offset:256
v_mfma_f32_16x16x32_bf16 a[112:115],a[224:227],v[112:115],a[112:115]
ds_read_b64_tr_b16 a[218:219],v3 offset:4352
ds_read_b64_tr_b16 a[220:221],v3 offset:8448
v_mfma_f32_16x16x32_bf16 a[112:115],a[228:231],v[120:123],a[112:115]
ds_read_b64_tr_b16 a[222:223],v3 offset:12544
ds_read_b64_tr_b16 a[224:225],v2 offset:512
v_mfma_f32_16x16x32_bf16 a[116:119],a[232:235],v[112:115],a[116:119]
v_exp_f32 v89,v89
v_mfma_f32_16x16x32_bf16 a[116:119],a[236:239],v[120:123],a[116:119]
ds_read_b64_tr_b16 a[226:227],v2 offset:4608
ds_read_b64_tr_b16 a[228:229],v2 offset:8704
v_mfma_f32_16x16x32_bf16 a[120:123],a[240:243],v[112:115],a[120:123]
ds_read_b64_tr_b16 a[230:231],v2 offset:12800
ds_read_b64_tr_b16 a[232:233],v3 offset:512
v_mfma_f32_16x16x32_bf16 a[120:123],a[244:247],v[120:123],a[120:123]
s_add_u32 s10,s20,s74
s_addc_u32 s11,s21,0
ds_read_b64_tr_b16 a[234:235],v3 offset:4608
v_mfma_f32_16x16x32_bf16 a[124:127],a[248:251],v[112:115],a[124:127]
ds_read_b64_tr_b16 a[236:237],v3 offset:8704
ds_read_b64_tr_b16 a[238:239],v3 offset:12800
v_mfma_f32_16x16x32_bf16 a[124:127],a[252:255],v[120:123],a[124:127]
s_add_u32 m0,s76,0
ds_read_b64_tr_b16 a[240:241],v2 offset:768
ds_read_b64_tr_b16 a[242:243],v2 offset:4864
v_mfma_f32_16x16x32_bf16 v[60:63],v[28:31],v[112:115],v[60:63]
ds_read_b64_tr_b16 a[244:245],v2 offset:8960
v_cvt_pk_bf16_f32 v88,v88,v89
v_mfma_f32_16x16x32_bf16 v[60:63],v[28:31],v[120:123],v[60:63]
global_load_lds_dwordx4 v4,s[10:11] nt
ds_read_b64_tr_b16 a[246:247],v2 offset:13056
v_mfma_f32_16x16x32_bf16 v[96:99],a[128:131],v[196:199],v[40:43]
ds_read_b64_tr_b16 a[248:249],v3 offset:768
ds_read_b64_tr_b16 a[250:251],v3 offset:4864
v_mfma_f32_16x16x32_bf16 v[96:99],a[132:135],v[204:207],v[96:99]
ds_read_b64_tr_b16 a[252:253],v3 offset:8960
ds_read_b64_tr_b16 a[254:255],v3 offset:13056
v_mfma_f32_16x16x32_bf16 v[96:99],a[136:139],v[212:215],v[96:99]
v_exp_f32 v90,v90
v_mfma_f32_16x16x32_bf16 v[96:99],a[140:143],v[220:223],v[96:99]
v_exp_f32 v91,v91
v_mfma_f32_16x16x32_bf16 v[100:103],a[156:159],v[220:223],v[40:43]
v_exp_f32 v92,v92
v_mfma_f32_16x16x32_bf16 v[100:103],a[152:155],v[212:215],v[100:103]
v_exp_f32 v93,v93
v_mfma_f32_16x16x32_bf16 v[100:103],a[148:151],v[204:207],v[100:103]
v_exp_f32 v94,v94
v_mfma_f32_16x16x32_bf16 v[100:103],a[144:147],v[196:199],v[100:103]
v_exp_f32 v95,v95
v_mfma_f32_16x16x32_bf16 v[104:107],a[160:163],v[196:199],v[40:43]
v_cvt_pk_bf16_f32 v89,v90,v91
v_cvt_pk_bf16_f32 v90,v92,v93
v_mfma_f32_16x16x32_bf16 v[104:107],a[164:167],v[204:207],v[104:107]
v_cvt_pk_bf16_f32 v91,v94,v95
v_add_u32 v2,s78,v2
v_mfma_f32_16x16x32_bf16 v[104:107],a[168:171],v[212:215],v[104:107]
v_exp_f32 v96,v96
v_mfma_f32_16x16x32_bf16 v[104:107],a[172:175],v[220:223],v[104:107]
v_exp_f32 v97,v97
v_mfma_f32_16x16x32_bf16 v[108:111],a[188:191],v[220:223],v[40:43]
v_exp_f32 v98,v98
v_mfma_f32_16x16x32_bf16 v[108:111],a[184:187],v[212:215],v[108:111]
global_load_lds_dwordx4 v4,s[10:11] offset:1024 nt
v_cvt_pk_bf16_f32 v96,v96,v97
v_mfma_f32_16x16x32_bf16 v[108:111],a[180:183],v[204:207],v[108:111]
v_exp_f32 v99,v99
v_mfma_f32_16x16x32_bf16 v[108:111],a[176:179],v[196:199],v[108:111]
s_waitcnt lgkmcnt(0)
v_exp_f32 v100,v100
v_mfma_f32_16x16x32_bf16 a[0:3],a[192:195],v[64:67],a[0:3]
v_exp_f32 v101,v101
v_mfma_f32_16x16x32_bf16 a[0:3],a[196:199],v[72:75],a[0:3]
v_exp_f32 v102,v102
v_mfma_f32_16x16x32_bf16 a[4:7],a[200:203],v[64:67],a[4:7]
v_exp_f32 v103,v103
v_mfma_f32_16x16x32_bf16 a[4:7],a[204:207],v[72:75],a[4:7]
v_cvt_pk_bf16_f32 v97,v98,v99
v_cvt_pk_bf16_f32 v98,v100,v101
v_mfma_f32_16x16x32_bf16 a[8:11],a[208:211],v[64:67],a[8:11]
v_cvt_pk_bf16_f32 v99,v102,v103
v_add_u32 v3,s78,v3
v_mfma_f32_16x16x32_bf16 a[8:11],a[212:215],v[72:75],a[8:11]
s_sub_u32 s78,s77,32768
v_exp_f32 v104,v104
v_mfma_f32_16x16x32_bf16 a[12:15],a[216:219],v[64:67],a[12:15]
s_and_b32 s78,s78,0x1ffff
v_exp_f32 v105,v105
v_mfma_f32_16x16x32_bf16 a[12:15],a[220:223],v[72:75],a[12:15]
s_sub_u32 s78,s78,s77
v_exp_f32 v106,v106
v_mfma_f32_16x16x32_bf16 a[16:19],a[224:227],v[64:67],a[16:19]
s_add_u32 s77,s77,s78
v_exp_f32 v107,v107
v_mfma_f32_16x16x32_bf16 a[16:19],a[228:231],v[72:75],a[16:19]
v_exp_f32 v108,v108
v_mfma_f32_16x16x32_bf16 a[20:23],a[232:235],v[64:67],a[20:23]
s_getreg_b32 s92,hwreg(HW_REG_IB_STS,0,4)
s_min_u32 s4,s92,s6
s_mov_b32 s6,s92
v_mfma_f32_16x16x32_bf16 a[20:23],a[236:239],v[72:75],a[20:23]
s_cmp_gt_u32 s4,s9
s_cselect_b32 s4,15,s5
v_cvt_pk_bf16_f32 v104,v104,v105
v_mfma_f32_16x16x32_bf16 a[24:27],a[240:243],v[64:67],a[24:27]
v_exp_f32 v109,v109
v_mfma_f32_16x16x32_bf16 a[24:27],a[244:247],v[72:75],a[24:27]
v_exp_f32 v110,v110
v_mfma_f32_16x16x32_bf16 a[28:31],a[248:251],v[64:67],a[28:31]
v_exp_f32 v111,v111
v_mfma_f32_16x16x32_bf16 a[28:31],a[252:255],v[72:75],a[28:31]
s_getpc_b64 s[92:93]
s_cmp_lg_u32 s4,0
s_cbranch_scc1 L_pace_%=
v_mfma_f32_16x16x32_bf16 v[48:51],v[28:31],v[64:67],v[48:51]
v_cvt_pk_bf16_f32 v105,v106,v107
v_cvt_pk_bf16_f32 v106,v108,v109
v_mfma_f32_16x16x32_bf16 v[48:51],v[28:31],v[72:75],v[48:51]
s_add_u32 s10,s20,s74
s_addc_u32 s11,s21,0
v_cvt_pk_bf16_f32 v107,v110,v111
s_waitcnt vmcnt(10)
s_barrier
v_mfma_f32_16x16x32_bf16 v[24:27],v[8:11],v[128:131],0
s_add_u32 m0,s76,0
v_mfma_f32_16x16x32_bf16 v[24:27],v[12:15],v[136:139],v[24:27]
v_mfma_f32_16x16x32_bf16 v[24:27],v[16:19],v[144:147],v[24:27]
global_load_lds_dwordx4 v5,s[10:11] offset:2048 nt
v_mfma_f32_16x16x32_bf16 v[24:27],v[20:23],v[152:155],v[24:27]
v_mfma_f32_16x16x32_bf16 v[112:115],a[128:131],v[228:231],v[44:47]
ds_read_b128 a[128:131],v1
v_mfma_f32_16x16x32_bf16 v[112:115],a[132:135],v[236:239],v[112:115]
ds_read_b128 a[132:135],v1 offset:256
v_mfma_f32_16x16x32_bf16 v[112:115],a[136:139],v[244:247],v[112:115]
ds_read_b128 a[136:139],v1 offset:512
v_mfma_f32_16x16x32_bf16 v[112:115],a[140:143],v[252:255],v[112:115]
ds_read_b128 a[140:143],v1 offset:768
v_mfma_f32_16x16x32_bf16 v[116:119],a[156:159],v[252:255],v[44:47]
v_max3_f32 v24,v24,v25,v26
v_max_f32 v24,v24,v27
v_mfma_f32_16x16x32_bf16 v[116:119],a[152:155],v[244:247],v[116:119]
ds_read_b128 a[156:159],v1 offset:4864
v_mov_b32 v25,v24
v_mfma_f32_16x16x32_bf16 v[116:119],a[148:151],v[236:239],v[116:119]
v_exp_f32 v112,v112
v_mfma_f32_16x16x32_bf16 v[116:119],a[144:147],v[228:231],v[116:119]
v_permlane32_swap_b32 v24,v25
v_mfma_f32_16x16x32_bf16 v[120:123],a[160:163],v[228:231],v[44:47]
v_max_f32 v24,v24,v25
v_mov_b32 v25,v24
v_mfma_f32_16x16x32_bf16 v[120:123],a[164:167],v[236:239],v[120:123]
v_exp_f32 v113,v113
v_mfma_f32_16x16x32_bf16 v[120:123],a[168:171],v[244:247],v[120:123]
v_permlane16_swap_b32 v24,v25
v_mfma_f32_16x16x32_bf16 v[120:123],a[172:175],v[252:255],v[120:123]
v_max_f32 v24,v24,v25
v_sub_f32 v27,0,v24
v_mfma_f32_16x16x32_bf16 v[124:127],a[188:191],v[252:255],v[44:47]
global_load_lds_dwordx4 v5,s[10:11] offset:3072
v_sub_f32 v26,0,v24
v_sub_f32 v25,0,v24
v_sub_f32 v24,0,v24
v_mfma_f32_16x16x32_bf16 v[124:127],a[184:187],v[244:247],v[124:127]
ds_write_b128 v7,v[24:27]
v_mfma_f32_16x16x32_bf16 v[124:127],a[180:183],v[236:239],v[124:127]
ds_read_b128 a[152:155],v1 offset:4608
v_cvt_pk_bf16_f32 v112,v112,v113
v_mfma_f32_16x16x32_bf16 v[124:127],a[176:179],v[228:231],v[124:127]
v_exp_f32 v114,v114
v_mfma_f32_16x16x32_bf16 v[24:27],v[8:11],v[160:163],0
v_exp_f32 v115,v115
v_mfma_f32_16x16x32_bf16 v[24:27],v[12:15],v[168:171],v[24:27]
ds_read_b128 a[148:151],v1 offset:4352
v_cvt_pk_bf16_f32 v113,v114,v115
v_mfma_f32_16x16x32_bf16 v[24:27],v[16:19],v[176:179],v[24:27]
v_exp_f32 v116,v116
v_mfma_f32_16x16x32_bf16 v[24:27],v[20:23],v[184:187],v[24:27]
v_exp_f32 v117,v117
v_mfma_f32_16x16x32_bf16 a[32:35],a[192:195],v[80:83],a[32:35]
ds_read_b128 a[144:147],v1 offset:4096
v_cvt_pk_bf16_f32 v114,v116,v117
v_mfma_f32_16x16x32_bf16 a[32:35],a[196:199],v[88:91],a[32:35]
v_exp_f32 v118,v118
v_mfma_f32_16x16x32_bf16 a[36:39],a[200:203],v[80:83],a[36:39]
s_add_u32 s10,s24,s74
s_addc_u32 s11,s25,0
ds_read_b128 a[160:163],v1 offset:8192
v_mfma_f32_16x16x32_bf16 a[36:39],a[204:207],v[88:91],a[36:39]
s_add_u32 s74,s74,16384
v_max3_f32 v24,v24,v25,v26
v_max_f32 v24,v24,v27
v_mfma_f32_16x16x32_bf16 a[40:43],a[208:211],v[80:83],a[40:43]
)A"
R"A(s_add_u32 m0,s76,16384
ds_read_b128 a[164:167],v1 offset:8448
v_mov_b32 v25,v24
v_mfma_f32_16x16x32_bf16 a[40:43],a[212:215],v[88:91],a[40:43]
s_and_b32 s74,s74,0x3ffff
v_exp_f32 v119,v119
v_mfma_f32_16x16x32_bf16 a[44:47],a[216:219],v[80:83],a[44:47]
global_load_lds_dwordx4 v4,s[10:11]
ds_read_b128 a[168:171],v1 offset:8704
v_mfma_f32_16x16x32_bf16 a[44:47],a[220:223],v[88:91],a[44:47]
s_or_b32 s74,s74,s69
v_permlane32_swap_b32 v24,v25
v_mfma_f32_16x16x32_bf16 a[48:51],a[224:227],v[80:83],a[48:51]
s_sub_u32 s76,s76,32768
v_max_f32 v24,v24,v25
v_mov_b32 v25,v24
v_mfma_f32_16x16x32_bf16 a[48:51],a[228:231],v[88:91],a[48:51]
s_and_b32 s76,s76,0x1ffff
ds_read_b128 a[172:175],v1 offset:8960
v_cvt_pk_bf16_f32 v115,v118,v119
v_mfma_f32_16x16x32_bf16 a[52:55],a[232:235],v[80:83],a[52:55]
v_permlane16_swap_b32 v24,v25
v_mfma_f32_16x16x32_bf16 a[52:55],a[236:239],v[88:91],a[52:55]
v_max_f32 v24,v24,v25
v_sub_f32 v27,0,v24
v_mfma_f32_16x16x32_bf16 a[56:59],a[240:243],v[80:83],a[56:59]
v_sub_f32 v26,0,v24
v_sub_f32 v25,0,v24
v_mfma_f32_16x16x32_bf16 a[56:59],a[244:247],v[88:91],a[56:59]
ds_read_b128 a[188:191],v1 offset:13056
v_sub_f32 v24,0,v24
v_mfma_f32_16x16x32_bf16 a[60:63],a[248:251],v[80:83],a[60:63]
ds_write_b128 v7,v[24:27] offset:1024
v_mfma_f32_16x16x32_bf16 a[60:63],a[252:255],v[88:91],a[60:63]
v_exp_f32 v120,v120
v_mfma_f32_16x16x32_bf16 v[52:55],v[28:31],v[80:83],v[52:55]
v_exp_f32 v121,v121
v_mfma_f32_16x16x32_bf16 v[52:55],v[28:31],v[88:91],v[52:55]
ds_read_b128 a[184:187],v1 offset:12800
v_cvt_pk_bf16_f32 v120,v120,v121
v_mfma_f32_16x16x32_bf16 v[24:27],v[8:11],v[192:195],0
v_exp_f32 v122,v122
v_mfma_f32_16x16x32_bf16 v[24:27],v[12:15],v[200:203],v[24:27]
v_exp_f32 v123,v123
v_mfma_f32_16x16x32_bf16 v[24:27],v[16:19],v[208:211],v[24:27]
s_waitcnt lgkmcnt(2)
ds_read_b128 a[180:183],v1 offset:12544
v_cvt_pk_bf16_f32 v121,v122,v123
v_mfma_f32_16x16x32_bf16 v[24:27],v[20:23],v[216:219],v[24:27]
global_load_lds_dwordx4 v5,s[10:11] offset:1024
ds_read_b128 a[176:179],v1 offset:12288
v_mfma_f32_16x16x32_bf16 v[64:67],a[128:131],v[132:135],v[32:35]
v_exp_f32 v124,v124
v_mfma_f32_16x16x32_bf16 v[64:67],a[132:135],v[140:143],v[64:67]
v_exp_f32 v125,v125
v_mfma_f32_16x16x32_bf16 v[64:67],a[136:139],v[148:151],v[64:67]
v_exp_f32 v126,v126
v_mfma_f32_16x16x32_bf16 v[64:67],a[140:143],v[156:159],v[64:67]
v_max3_f32 v24,v24,v25,v26
v_max_f32 v24,v24,v27
v_mfma_f32_16x16x32_bf16 v[68:71],a[156:159],v[156:159],v[32:35]
v_mov_b32 v25,v24
v_cvt_pk_bf16_f32 v122,v124,v125
v_mfma_f32_16x16x32_bf16 v[68:71],a[152:155],v[148:151],v[68:71]
v_exp_f32 v127,v127
v_mfma_f32_16x16x32_bf16 v[68:71],a[148:151],v[140:143],v[68:71]
v_permlane32_swap_b32 v24,v25
v_mfma_f32_16x16x32_bf16 v[68:71],a[144:147],v[132:135],v[68:71]
v_max_f32 v24,v24,v25
v_mov_b32 v25,v24
v_mfma_f32_16x16x32_bf16 v[72:75],a[160:163],v[132:135],v[32:35]
v_cvt_pk_bf16_f32 v123,v126,v127
v_add_u32 v1,s78,v1
v_mfma_f32_16x16x32_bf16 v[72:75],a[164:167],v[140:143],v[72:75]
v_permlane16_swap_b32 v24,v25
v_mfma_f32_16x16x32_bf16 v[72:75],a[168:171],v[148:151],v[72:75]
v_max_f32 v24,v24,v25
v_sub_f32 v27,0,v24
v_mfma_f32_16x16x32_bf16 v[72:75],a[172:175],v[156:159],v[72:75]
v_sub_f32 v26,0,v24
v_sub_f32 v25,0,v24
v_mfma_f32_16x16x32_bf16 v[76:79],a[188:191],v[156:159],v[32:35]
s_waitcnt lgkmcnt(0)
v_exp_f32 v64,v64
v_mfma_f32_16x16x32_bf16 v[76:79],a[184:187],v[148:151],v[76:79]
global_load_lds_dwordx4 v4,s[10:11] offset:2048
v_sub_f32 v24,0,v24
v_mfma_f32_16x16x32_bf16 v[76:79],a[180:183],v[140:143],v[76:79]
ds_write_b128 v7,v[24:27] offset:2048
v_mfma_f32_16x16x32_bf16 v[76:79],a[176:179],v[132:135],v[76:79]
v_exp_f32 v65,v65
v_mfma_f32_16x16x32_bf16 a[64:67],a[192:195],v[96:99],a[64:67]
v_exp_f32 v66,v66
v_mfma_f32_16x16x32_bf16 a[64:67],a[196:199],v[104:107],a[64:67]
v_exp_f32 v67,v67
v_mfma_f32_16x16x32_bf16 a[68:71],a[200:203],v[96:99],a[68:71]
v_exp_f32 v68,v68
v_mfma_f32_16x16x32_bf16 a[68:71],a[204:207],v[104:107],a[68:71]
v_exp_f32 v69,v69
v_mfma_f32_16x16x32_bf16 a[72:75],a[208:211],v[96:99],a[72:75]
v_exp_f32 v70,v70
v_mfma_f32_16x16x32_bf16 a[72:75],a[212:215],v[104:107],a[72:75]
v_exp_f32 v71,v71
v_mfma_f32_16x16x32_bf16 a[76:79],a[216:219],v[96:99],a[76:79]
v_cvt_pk_bf16_f32 v64,v64,v65
v_cvt_pk_bf16_f32 v65,v66,v67
v_mfma_f32_16x16x32_bf16 a[76:79],a[220:223],v[104:107],a[76:79]
v_cvt_pk_bf16_f32 v66,v68,v69
v_cvt_pk_bf16_f32 v67,v70,v71
v_mfma_f32_16x16x32_bf16 a[80:83],a[224:227],v[96:99],a[80:83]
v_exp_f32 v72,v72
v_mfma_f32_16x16x32_bf16 a[80:83],a[228:231],v[104:107],a[80:83]
v_exp_f32 v73,v73
v_mfma_f32_16x16x32_bf16 a[84:87],a[232:235],v[96:99],a[84:87]
v_exp_f32 v74,v74
v_mfma_f32_16x16x32_bf16 a[84:87],a[236:239],v[104:107],a[84:87]
v_exp_f32 v75,v75
v_mfma_f32_16x16x32_bf16 a[88:91],a[240:243],v[96:99],a[88:91]
global_load_lds_dwordx4 v5,s[10:11] offset:3072
v_cvt_pk_bf16_f32 v72,v72,v73
v_mfma_f32_16x16x32_bf16 a[88:91],a[244:247],v[104:107],a[88:91]
v_exp_f32 v76,v76
v_mfma_f32_16x16x32_bf16 a[92:95],a[248:251],v[96:99],a[92:95]
v_exp_f32 v77,v77
v_mfma_f32_16x16x32_bf16 a[92:95],a[252:255],v[104:107],a[92:95]
v_exp_f32 v78,v78
v_mfma_f32_16x16x32_bf16 v[56:59],v[28:31],v[96:99],v[56:59]
v_exp_f32 v79,v79
v_mfma_f32_16x16x32_bf16 v[56:59],v[28:31],v[104:107],v[56:59]
v_cvt_pk_bf16_f32 v73,v74,v75
v_cvt_pk_bf16_f32 v74,v76,v77
v_mfma_f32_16x16x32_bf16 v[24:27],v[8:11],v[224:227],0
v_cvt_pk_bf16_f32 v75,v78,v79
v_mfma_f32_16x16x32_bf16 v[24:27],v[12:15],v[232:235],v[24:27]
v_mfma_f32_16x16x32_bf16 v[24:27],v[16:19],v[240:243],v[24:27]
v_mfma_f32_16x16x32_bf16 v[24:27],v[20:23],v[248:251],v[24:27]
v_mfma_f32_16x16x32_bf16 v[80:83],a[128:131],v[164:167],v[36:39]
s_add_u32 s10,s20,s74
s_addc_u32 s11,s21,0
v_mfma_f32_16x16x32_bf16 v[80:83],a[132:135],v[172:175],v[80:83]
v_mfma_f32_16x16x32_bf16 v[80:83],a[136:139],v[180:183],v[80:83]
s_add_u32 m0,s76,0
v_mfma_f32_16x16x32_bf16 v[80:83],a[140:143],v[188:191],v[80:83]
v_mfma_f32_16x16x32_bf16 v[84:87],a[156:159],v[188:191],v[36:39]
global_load_lds_dwordx4 v4,s[10:11] nt
v_max3_f32 v24,v24,v25,v26
v_mfma_f32_16x16x32_bf16 v[84:87],a[152:155],v[180:183],v[84:87]
v_max_f32 v24,v24,v27
v_mov_b32 v25,v24
v_mfma_f32_16x16x32_bf16 v[84:87],a[148:151],v[172:175],v[84:87]
v_mfma_f32_16x16x32_bf16 v[84:87],a[144:147],v[164:167],v[84:87]
v_permlane32_swap_b32 v24,v25
v_mfma_f32_16x16x32_bf16 v[88:91],a[160:163],v[164:167],v[36:39]
v_max_f32 v24,v24,v25
v_mov_b32 v25,v24
v_mfma_f32_16x16x32_bf16 v[88:91],a[164:167],v[172:175],v[88:91]
v_exp_f32 v80,v80
L_pd6_%=:
s_cbranch_execz L_trt0_%=
v_mfma_f32_16x16x32_bf16 v[88:91],a[168:171],v[180:183],v[88:91]
v_permlane16_swap_b32 v24,v25
v_mfma_f32_16x16x32_bf16 v[88:91],a[172:175],v[188:191],v[88:91]
v_max_f32 v24,v24,v25
v_max_f32 v24,s72,v24
v_mfma_f32_16x16x32_bf16 v[92:95],a[188:191],v[188:191],v[36:39]
v_sub_f32 v27,0,v24
v_sub_f32 v26,0,v24
v_mfma_f32_16x16x32_bf16 v[92:95],a[184:187],v[180:183],v[92:95]
v_sub_f32 v25,0,v24
v_sub_f32 v24,0,v24
v_mfma_f32_16x16x32_bf16 v[92:95],a[180:183],v[172:175],v[92:95]
v_exp_f32 v81,v81
v_mfma_f32_16x16x32_bf16 v[92:95],a[176:179],v[164:167],v[92:95]
v_exp_f32 v82,v82
v_mfma_f32_16x16x32_bf16 a[96:99],a[192:195],v[112:115],a[96:99]
ds_read_b64_tr_b16 a[192:193],v2
ds_read_b64_tr_b16 a[194:195],v2 offset:4096
v_mfma_f32_16x16x32_bf16 a[96:99],a[196:199],v[120:123],a[96:99]
ds_read_b64_tr_b16 a[196:197],v2 offset:8192
ds_read_b64_tr_b16 a[198:199],v2 offset:12288
v_mfma_f32_16x16x32_bf16 a[100:103],a[200:203],v[112:115],a[100:103]
ds_read_b64_tr_b16 a[200:201],v3
ds_read_b64_tr_b16 a[202:203],v3 offset:4096
v_mfma_f32_16x16x32_bf16 a[100:103],a[204:207],v[120:123],a[100:103]
global_load_lds_dwordx4 v4,s[10:11] offset:1024 nt
ds_read_b64_tr_b16 a[204:205],v3 offset:8192
v_mfma_f32_16x16x32_bf16 a[104:107],a[208:211],v[112:115],a[104:107]
ds_read_b64_tr_b16 a[206:207],v3 offset:12288
ds_read_b64_tr_b16 a[208:209],v2 offset:256
v_mfma_f32_16x16x32_bf16 a[104:107],a[212:215],v[120:123],a[104:107]
ds_read_b64_tr_b16 a[210:211],v2 offset:4352
ds_read_b64_tr_b16 a[212:213],v2 offset:8448
v_mfma_f32_16x16x32_bf16 a[108:111],a[216:219],v[112:115],a[108:111]
ds_read_b64_tr_b16 a[214:215],v2 offset:12544
v_cvt_pk_bf16_f32 v80,v80,v81
v_mfma_f32_16x16x32_bf16 a[108:111],a[220:223],v[120:123],a[108:111]
ds_read_b64_tr_b16 a[216:217],v3 offset:256
ds_read_b64_tr_b16 a[218:219],v3 offset:4352
v_mfma_f32_16x16x32_bf16 a[112:115],a[224:227],v[112:115],a[112:115]
v_exp_f32 v83,v83
v_mfma_f32_16x16x32_bf16 a[112:115],a[228:231],v[120:123],a[112:115]
ds_read_b64_tr_b16 a[220:221],v3 offset:8448
ds_read_b64_tr_b16 a[222:223],v3 offset:12544
v_mfma_f32_16x16x32_bf16 a[116:119],a[232:235],v[112:115],a[116:119]
ds_read_b64_tr_b16 a[224:225],v2 offset:512
ds_read_b64_tr_b16 a[226:227],v2 offset:4608
v_mfma_f32_16x16x32_bf16 a[116:119],a[236:239],v[120:123],a[116:119]
ds_read_b64_tr_b16 a[228:229],v2 offset:8704
ds_read_b64_tr_b16 a[230:231],v2 offset:12800
v_mfma_f32_16x16x32_bf16 a[120:123],a[240:243],v[112:115],a[120:123]
ds_read_b64_tr_b16 a[232:233],v3 offset:512
v_cvt_pk_bf16_f32 v81,v82,v83
v_mfma_f32_16x16x32_bf16 a[120:123],a[244:247],v[120:123],a[120:123]
ds_read_b64_tr_b16 a[234:235],v3 offset:4608
ds_read_b64_tr_b16 a[236:237],v3 offset:8704
v_mfma_f32_16x16x32_bf16 a[124:127],a[248:251],v[112:115],a[124:127]
v_exp_f32 v84,v84
v_mfma_f32_16x16x32_bf16 a[124:127],a[252:255],v[120:123],a[124:127]
ds_read_b64_tr_b16 a[238:239],v3 offset:12800
ds_read_b64_tr_b16 a[240:241],v2 offset:768
v_mfma_f32_16x16x32_bf16 v[60:63],v[28:31],v[112:115],v[60:63]
ds_read_b64_tr_b16 a[242:243],v2 offset:4864
ds_read_b64_tr_b16 a[244:245],v2 offset:8960
v_mfma_f32_16x16x32_bf16 v[60:63],v[28:31],v[120:123],v[60:63]
global_load_lds_dwordx4 v5,s[10:11] offset:2048 nt
ds_read_b64_tr_b16 a[246:247],v2 offset:13056
v_mfma_f32_16x16x32_bf16 v[96:99],a[128:131],v[196:199],v[40:43]
ds_read_b64_tr_b16 a[248:249],v3 offset:768
ds_read_b64_tr_b16 a[250:251],v3 offset:4864
v_mfma_f32_16x16x32_bf16 v[96:99],a[132:135],v[204:207],v[96:99]
ds_read_b64_tr_b16 a[252:253],v3 offset:8960
ds_read_b64_tr_b16 a[254:255],v3 offset:13056
v_mfma_f32_16x16x32_bf16 v[96:99],a[136:139],v[212:215],v[96:99]
v_exp_f32 v85,v85
v_mfma_f32_16x16x32_bf16 v[96:99],a[140:143],v[220:223],v[96:99]
v_exp_f32 v86,v86
v_mfma_f32_16x16x32_bf16 v[100:103],a[156:159],v[220:223],v[40:43]
v_exp_f32 v87,v87
v_mfma_f32_16x16x32_bf16 v[100:103],a[152:155],v[212:215],v[100:103]
v_exp_f32 v88,v88
v_mfma_f32_16x16x32_bf16 v[100:103],a[148:151],v[204:207],v[100:103]
v_exp_f32 v89,v89
v_mfma_f32_16x16x32_bf16 v[100:103],a[144:147],v[196:199],v[100:103]
v_exp_f32 v90,v90
v_mfma_f32_16x16x32_bf16 v[104:107],a[160:163],v[196:199],v[40:43]
v_exp_f32 v91,v91
v_mfma_f32_16x16x32_bf16 v[104:107],a[164:167],v[204:207],v[104:107]
v_exp_f32 v92,v92
v_mfma_f32_16x16x32_bf16 v[104:107],a[168:171],v[212:215],v[104:107]
v_exp_f32 v93,v93
v_mfma_f32_16x16x32_bf16 v[104:107],a[172:175],v[220:223],v[104:107]
v_exp_f32 v94,v94
v_mfma_f32_16x16x32_bf16 v[108:111],a[188:191],v[220:223],v[40:43]
v_exp_f32 v95,v95
v_mfma_f32_16x16x32_bf16 v[108:111],a[184:187],v[212:215],v[108:111]
v_exp_f32 v96,v96
v_mfma_f32_16x16x32_bf16 v[108:111],a[180:183],v[204:207],v[108:111]
global_load_lds_dwordx4 v5,s[10:11] offset:3072
v_cvt_pk_bf16_f32 v82,v84,v85
v_mfma_f32_16x16x32_bf16 v[108:111],a[176:179],v[196:199],v[108:111]
s_waitcnt lgkmcnt(0)
v_exp_f32 v97,v97
v_mfma_f32_16x16x32_bf16 a[0:3],a[192:195],v[64:67],a[0:3]
v_exp_f32 v98,v98
v_mfma_f32_16x16x32_bf16 a[0:3],a[196:199],v[72:75],a[0:3]
v_exp_f32 v99,v99
v_mfma_f32_16x16x32_bf16 a[4:7],a[200:203],v[64:67],a[4:7]
v_exp_f32 v100,v100
v_mfma_f32_16x16x32_bf16 a[4:7],a[204:207],v[72:75],a[4:7]
v_exp_f32 v101,v101
v_mfma_f32_16x16x32_bf16 a[8:11],a[208:211],v[64:67],a[8:11]
v_exp_f32 v102,v102
v_mfma_f32_16x16x32_bf16 a[8:11],a[212:215],v[72:75],a[8:11]
v_exp_f32 v103,v103
v_mfma_f32_16x16x32_bf16 a[12:15],a[216:219],v[64:67],a[12:15]
ds_write_b128 v7,v[24:27] offset:3072
v_mfma_f32_16x16x32_bf16 a[12:15],a[220:223],v[72:75],a[12:15]
v_cvt_pk_bf16_f32 v83,v86,v87
)A"
R"A(v_cvt_pk_bf16_f32 v88,v88,v89
v_mfma_f32_16x16x32_bf16 a[16:19],a[224:227],v[64:67],a[16:19]
v_cvt_pk_bf16_f32 v89,v90,v91
v_cvt_pk_bf16_f32 v90,v92,v93
v_mfma_f32_16x16x32_bf16 a[16:19],a[228:231],v[72:75],a[16:19]
v_cvt_pk_bf16_f32 v91,v94,v95
v_cvt_pk_bf16_f32 v96,v96,v97
v_mfma_f32_16x16x32_bf16 a[20:23],a[232:235],v[64:67],a[20:23]
s_getreg_b32 s92,hwreg(HW_REG_IB_STS,0,4)
s_min_u32 s4,s92,s6
s_mov_b32 s6,s92
v_mfma_f32_16x16x32_bf16 a[20:23],a[236:239],v[72:75],a[20:23]
s_cmp_gt_u32 s4,s9
s_cselect_b32 s4,15,s5
v_cvt_pk_bf16_f32 v97,v98,v99
v_mfma_f32_16x16x32_bf16 a[24:27],a[240:243],v[64:67],a[24:27]
v_cvt_pk_bf16_f32 v98,v100,v101
v_cvt_pk_bf16_f32 v99,v102,v103
v_mfma_f32_16x16x32_bf16 a[24:27],a[244:247],v[72:75],a[24:27]
v_mfma_f32_16x16x32_bf16 a[28:31],a[248:251],v[64:67],a[28:31]
v_mfma_f32_16x16x32_bf16 a[28:31],a[252:255],v[72:75],a[28:31]
s_getpc_b64 s[92:93]
s_cmp_lg_u32 s4,0
s_cbranch_scc1 L_pace_%=
v_mfma_f32_16x16x32_bf16 v[48:51],v[28:31],v[64:67],v[48:51]
v_mfma_f32_16x16x32_bf16 v[48:51],v[28:31],v[72:75],v[48:51]
s_mov_b32 s70,11
L_a0_%=:
s_waitcnt vmcnt(12)
s_barrier
v_mfma_f32_16x16x32_bf16 v[112:115],a[128:131],v[228:231],v[44:47]
s_add_u32 s10,s24,s74
s_addc_u32 s11,s25,0
s_add_u32 m0,s76,16384
v_mfma_f32_16x16x32_bf16 v[112:115],a[132:135],v[236:239],v[112:115]
s_add_u32 s75,s75,16384
s_add_u32 s74,s74,16384
ds_read_b128 a[128:131],v1
v_mfma_f32_16x16x32_bf16 v[112:115],a[136:139],v[244:247],v[112:115]
s_and_b32 s74,s74,0x3ffff
global_load_lds_dwordx4 v4,s[10:11]
v_mfma_f32_16x16x32_bf16 v[112:115],a[140:143],v[252:255],v[112:115]
s_and_b32 s69,s75,0xfffc0000
s_or_b32 s74,s74,s69
ds_read_b128 a[132:135],v1 offset:256
v_mfma_f32_16x16x32_bf16 v[116:119],a[156:159],v[252:255],v[44:47]
s_sub_u32 s76,s76,32768
s_and_b32 s76,s76,0x1ffff
v_mfma_f32_16x16x32_bf16 v[116:119],a[152:155],v[244:247],v[116:119]
ds_read_b128 a[136:139],v1 offset:512
v_mfma_f32_16x16x32_bf16 v[116:119],a[148:151],v[236:239],v[116:119]
v_mfma_f32_16x16x32_bf16 v[116:119],a[144:147],v[228:231],v[116:119]
ds_read_b128 a[140:143],v1 offset:768
v_mfma_f32_16x16x32_bf16 v[120:123],a[160:163],v[228:231],v[44:47]
v_mfma_f32_16x16x32_bf16 v[120:123],a[164:167],v[236:239],v[120:123]
ds_read_b128 a[156:159],v1 offset:4864
v_mfma_f32_16x16x32_bf16 v[120:123],a[168:171],v[244:247],v[120:123]
v_mfma_f32_16x16x32_bf16 v[120:123],a[172:175],v[252:255],v[120:123]
ds_read_b128 a[152:155],v1 offset:4608
v_mfma_f32_16x16x32_bf16 v[124:127],a[188:191],v[252:255],v[44:47]
v_exp_f32 v104,v104
v_mfma_f32_16x16x32_bf16 v[124:127],a[184:187],v[244:247],v[124:127]
ds_read_b128 a[148:151],v1 offset:4352
v_add_u32 v2,s78,v2
v_mfma_f32_16x16x32_bf16 v[124:127],a[180:183],v[236:239],v[124:127]
v_exp_f32 v105,v105
v_mfma_f32_16x16x32_bf16 v[124:127],a[176:179],v[228:231],v[124:127]
ds_read_b128 a[144:147],v1 offset:4096
v_cvt_pk_bf16_f32 v104,v104,v105
v_mfma_f32_16x16x32_bf16 a[32:35],a[192:195],v[80:83],a[32:35]
v_exp_f32 v106,v106
v_mfma_f32_16x16x32_bf16 a[32:35],a[196:199],v[88:91],a[32:35]
ds_read_b128 a[160:163],v1 offset:8192
v_add_u32 v3,s78,v3
v_mfma_f32_16x16x32_bf16 a[36:39],a[200:203],v[80:83],a[36:39]
s_sub_u32 s78,s77,32768
global_load_lds_dwordx4 v5,s[10:11] offset:1024
v_mfma_f32_16x16x32_bf16 a[36:39],a[204:207],v[88:91],a[36:39]
s_and_b32 s78,s78,0x1ffff
v_exp_f32 v107,v107
v_mfma_f32_16x16x32_bf16 a[40:43],a[208:211],v[80:83],a[40:43]
s_sub_u32 s78,s78,s77
ds_read_b128 a[164:167],v1 offset:8448
v_cvt_pk_bf16_f32 v105,v106,v107
v_mfma_f32_16x16x32_bf16 a[40:43],a[212:215],v[88:91],a[40:43]
s_add_u32 s77,s77,s78
v_exp_f32 v108,v108
v_mfma_f32_16x16x32_bf16 a[44:47],a[216:219],v[80:83],a[44:47]
v_exp_f32 v109,v109
v_mfma_f32_16x16x32_bf16 a[44:47],a[220:223],v[88:91],a[44:47]
ds_read_b128 a[168:171],v1 offset:8704
v_cvt_pk_bf16_f32 v106,v108,v109
v_mfma_f32_16x16x32_bf16 a[48:51],a[224:227],v[80:83],a[48:51]
v_exp_f32 v110,v110
v_mfma_f32_16x16x32_bf16 a[48:51],a[228:231],v[88:91],a[48:51]
v_exp_f32 v111,v111
v_mfma_f32_16x16x32_bf16 a[52:55],a[232:235],v[80:83],a[52:55]
ds_read_b128 a[172:175],v1 offset:8960
v_cvt_pk_bf16_f32 v107,v110,v111
v_mfma_f32_16x16x32_bf16 a[52:55],a[236:239],v[88:91],a[52:55]
v_exp_f32 v112,v112
v_mfma_f32_16x16x32_bf16 a[56:59],a[240:243],v[80:83],a[56:59]
v_exp_f32 v113,v113
v_mfma_f32_16x16x32_bf16 a[56:59],a[244:247],v[88:91],a[56:59]
ds_read_b128 a[188:191],v1 offset:13056
v_cvt_pk_bf16_f32 v112,v112,v113
v_mfma_f32_16x16x32_bf16 a[60:63],a[248:251],v[80:83],a[60:63]
v_exp_f32 v114,v114
v_mfma_f32_16x16x32_bf16 a[60:63],a[252:255],v[88:91],a[60:63]
v_exp_f32 v115,v115
v_mfma_f32_16x16x32_bf16 v[52:55],v[28:31],v[80:83],v[52:55]
ds_read_b128 a[184:187],v1 offset:12800
v_cvt_pk_bf16_f32 v113,v114,v115
v_mfma_f32_16x16x32_bf16 v[52:55],v[28:31],v[88:91],v[52:55]
s_waitcnt lgkmcnt(3)
v_exp_f32 v116,v116
v_mfma_f32_16x16x32_bf16 v[64:67],a[128:131],v[132:135],v[32:35]
global_load_lds_dwordx4 v4,s[10:11] offset:2048
ds_read_b128 a[180:183],v1 offset:12544
v_mfma_f32_16x16x32_bf16 v[64:67],a[132:135],v[140:143],v[64:67]
v_exp_f32 v117,v117
v_mfma_f32_16x16x32_bf16 v[64:67],a[136:139],v[148:151],v[64:67]
ds_read_b128 a[176:179],v1 offset:12288
v_cvt_pk_bf16_f32 v114,v116,v117
v_mfma_f32_16x16x32_bf16 v[64:67],a[140:143],v[156:159],v[64:67]
v_exp_f32 v118,v118
v_mfma_f32_16x16x32_bf16 v[68:71],a[156:159],v[156:159],v[32:35]
v_exp_f32 v119,v119
v_mfma_f32_16x16x32_bf16 v[68:71],a[152:155],v[148:151],v[68:71]
v_cvt_pk_bf16_f32 v115,v118,v119
v_add_u32 v1,s78,v1
v_mfma_f32_16x16x32_bf16 v[68:71],a[148:151],v[140:143],v[68:71]
v_exp_f32 v120,v120
v_mfma_f32_16x16x32_bf16 v[68:71],a[144:147],v[132:135],v[68:71]
v_exp_f32 v121,v121
v_mfma_f32_16x16x32_bf16 v[72:75],a[160:163],v[132:135],v[32:35]
v_exp_f32 v122,v122
v_mfma_f32_16x16x32_bf16 v[72:75],a[164:167],v[140:143],v[72:75]
v_exp_f32 v123,v123
v_mfma_f32_16x16x32_bf16 v[72:75],a[168:171],v[148:151],v[72:75]
s_waitcnt lgkmcnt(0)
v_exp_f32 v124,v124
v_mfma_f32_16x16x32_bf16 v[72:75],a[172:175],v[156:159],v[72:75]
v_exp_f32 v125,v125
v_mfma_f32_16x16x32_bf16 v[76:79],a[188:191],v[156:159],v[32:35]
v_exp_f32 v126,v126
v_mfma_f32_16x16x32_bf16 v[76:79],a[184:187],v[148:151],v[76:79]
v_exp_f32 v127,v127
v_mfma_f32_16x16x32_bf16 v[76:79],a[180:183],v[140:143],v[76:79]
v_cvt_pk_bf16_f32 v120,v120,v121
v_cvt_pk_bf16_f32 v121,v122,v123
v_mfma_f32_16x16x32_bf16 v[76:79],a[176:179],v[132:135],v[76:79]
v_cvt_pk_bf16_f32 v122,v124,v125
v_cvt_pk_bf16_f32 v123,v126,v127
v_mfma_f32_16x16x32_bf16 a[64:67],a[192:195],v[96:99],a[64:67]
global_load_lds_dwordx4 v5,s[10:11] offset:3072
v_mfma_f32_16x16x32_bf16 a[64:67],a[196:199],v[104:107],a[64:67]
v_exp_f32 v64,v64
v_mfma_f32_16x16x32_bf16 a[68:71],a[200:203],v[96:99],a[68:71]
v_exp_f32 v65,v65
v_mfma_f32_16x16x32_bf16 a[68:71],a[204:207],v[104:107],a[68:71]
v_exp_f32 v66,v66
v_mfma_f32_16x16x32_bf16 a[72:75],a[208:211],v[96:99],a[72:75]
v_exp_f32 v67,v67
v_mfma_f32_16x16x32_bf16 a[72:75],a[212:215],v[104:107],a[72:75]
v_exp_f32 v68,v68
v_mfma_f32_16x16x32_bf16 a[76:79],a[216:219],v[96:99],a[76:79]
v_exp_f32 v69,v69
v_mfma_f32_16x16x32_bf16 a[76:79],a[220:223],v[104:107],a[76:79]
v_exp_f32 v70,v70
v_mfma_f32_16x16x32_bf16 a[80:83],a[224:227],v[96:99],a[80:83]
v_exp_f32 v71,v71
v_mfma_f32_16x16x32_bf16 a[80:83],a[228:231],v[104:107],a[80:83]
v_cvt_pk_bf16_f32 v64,v64,v65
v_cvt_pk_bf16_f32 v65,v66,v67
v_mfma_f32_16x16x32_bf16 a[84:87],a[232:235],v[96:99],a[84:87]
v_cvt_pk_bf16_f32 v66,v68,v69
v_cvt_pk_bf16_f32 v67,v70,v71
v_mfma_f32_16x16x32_bf16 a[84:87],a[236:239],v[104:107],a[84:87]
v_exp_f32 v72,v72
v_mfma_f32_16x16x32_bf16 a[88:91],a[240:243],v[96:99],a[88:91]
s_add_u32 s10,s20,s74
s_addc_u32 s11,s21,0
v_mfma_f32_16x16x32_bf16 a[88:91],a[244:247],v[104:107],a[88:91]
v_exp_f32 v73,v73
v_mfma_f32_16x16x32_bf16 a[92:95],a[248:251],v[96:99],a[92:95]
s_add_u32 m0,s76,0
v_exp_f32 v74,v74
v_mfma_f32_16x16x32_bf16 a[92:95],a[252:255],v[104:107],a[92:95]
v_exp_f32 v75,v75
v_mfma_f32_16x16x32_bf16 v[56:59],v[28:31],v[96:99],v[56:59]
global_load_lds_dwordx4 v4,s[10:11] nt
v_cvt_pk_bf16_f32 v72,v72,v73
v_mfma_f32_16x16x32_bf16 v[56:59],v[28:31],v[104:107],v[56:59]
v_exp_f32 v76,v76
v_mfma_f32_16x16x32_bf16 v[80:83],a[128:131],v[164:167],v[36:39]
v_exp_f32 v77,v77
v_mfma_f32_16x16x32_bf16 v[80:83],a[132:135],v[172:175],v[80:83]
v_exp_f32 v78,v78
v_mfma_f32_16x16x32_bf16 v[80:83],a[136:139],v[180:183],v[80:83]
v_exp_f32 v79,v79
v_mfma_f32_16x16x32_bf16 v[80:83],a[140:143],v[188:191],v[80:83]
v_cvt_pk_bf16_f32 v73,v74,v75
v_cvt_pk_bf16_f32 v74,v76,v77
v_mfma_f32_16x16x32_bf16 v[84:87],a[156:159],v[188:191],v[36:39]
v_cvt_pk_bf16_f32 v75,v78,v79
v_mfma_f32_16x16x32_bf16 v[84:87],a[152:155],v[180:183],v[84:87]
v_mfma_f32_16x16x32_bf16 v[84:87],a[148:151],v[172:175],v[84:87]
v_mfma_f32_16x16x32_bf16 v[84:87],a[144:147],v[164:167],v[84:87]
v_mfma_f32_16x16x32_bf16 v[88:91],a[160:163],v[164:167],v[36:39]
v_exp_f32 v80,v80
v_mfma_f32_16x16x32_bf16 v[88:91],a[164:167],v[172:175],v[88:91]
v_exp_f32 v81,v81
v_mfma_f32_16x16x32_bf16 v[88:91],a[168:171],v[180:183],v[88:91]
v_exp_f32 v82,v82
v_mfma_f32_16x16x32_bf16 v[88:91],a[172:175],v[188:191],v[88:91]
v_exp_f32 v83,v83
v_mfma_f32_16x16x32_bf16 v[92:95],a[188:191],v[188:191],v[36:39]
v_exp_f32 v84,v84
v_mfma_f32_16x16x32_bf16 v[92:95],a[184:187],v[180:183],v[92:95]
v_exp_f32 v85,v85
v_mfma_f32_16x16x32_bf16 v[92:95],a[180:183],v[172:175],v[92:95]
global_load_lds_dwordx4 v4,s[10:11] offset:1024 nt
v_cvt_pk_bf16_f32 v80,v80,v81
v_mfma_f32_16x16x32_bf16 v[92:95],a[176:179],v[164:167],v[92:95]
v_exp_f32 v86,v86
v_mfma_f32_16x16x32_bf16 a[96:99],a[192:195],v[112:115],a[96:99]
ds_read_b64_tr_b16 a[192:193],v2
ds_read_b64_tr_b16 a[194:195],v2 offset:4096
v_mfma_f32_16x16x32_bf16 a[96:99],a[196:199],v[120:123],a[96:99]
ds_read_b64_tr_b16 a[196:197],v2 offset:8192
v_cvt_pk_bf16_f32 v81,v82,v83
v_mfma_f32_16x16x32_bf16 a[100:103],a[200:203],v[112:115],a[100:103]
ds_read_b64_tr_b16 a[198:199],v2 offset:12288
v_cvt_pk_bf16_f32 v82,v84,v85
v_mfma_f32_16x16x32_bf16 a[100:103],a[204:207],v[120:123],a[100:103]
v_exp_f32 v87,v87
v_mfma_f32_16x16x32_bf16 a[104:107],a[208:211],v[112:115],a[104:107]
ds_read_b64_tr_b16 a[200:201],v3
ds_read_b64_tr_b16 a[202:203],v3 offset:4096
v_mfma_f32_16x16x32_bf16 a[104:107],a[212:215],v[120:123],a[104:107]
ds_read_b64_tr_b16 a[204:205],v3 offset:8192
v_cvt_pk_bf16_f32 v83,v86,v87
v_mfma_f32_16x16x32_bf16 a[108:111],a[216:219],v[112:115],a[108:111]
v_exp_f32 v88,v88
v_mfma_f32_16x16x32_bf16 a[108:111],a[220:223],v[120:123],a[108:111]
ds_read_b64_tr_b16 a[206:207],v3 offset:12288
ds_read_b64_tr_b16 a[208:209],v2 offset:256
v_mfma_f32_16x16x32_bf16 a[112:115],a[224:227],v[112:115],a[112:115]
v_exp_f32 v89,v89
v_mfma_f32_16x16x32_bf16 a[112:115],a[228:231],v[120:123],a[112:115]
ds_read_b64_tr_b16 a[210:211],v2 offset:4352
ds_read_b64_tr_b16 a[212:213],v2 offset:8448
v_mfma_f32_16x16x32_bf16 a[116:119],a[232:235],v[112:115],a[116:119]
ds_read_b64_tr_b16 a[214:215],v2 offset:12544
v_cvt_pk_bf16_f32 v88,v88,v89
v_mfma_f32_16x16x32_bf16 a[116:119],a[236:239],v[120:123],a[116:119]
v_exp_f32 v90,v90
v_mfma_f32_16x16x32_bf16 a[120:123],a[240:243],v[112:115],a[120:123]
ds_read_b64_tr_b16 a[216:217],v3 offset:256
ds_read_b64_tr_b16 a[218:219],v3 offset:4352
v_mfma_f32_16x16x32_bf16 a[120:123],a[244:247],v[120:123],a[120:123]
v_exp_f32 v91,v91
v_mfma_f32_16x16x32_bf16 a[124:127],a[248:251],v[112:115],a[124:127]
global_load_lds_dwordx4 v5,s[10:11] offset:2048 nt
ds_read_b64_tr_b16 a[220:221],v3 offset:8448
v_mfma_f32_16x16x32_bf16 a[124:127],a[252:255],v[120:123],a[124:127]
ds_read_b64_tr_b16 a[222:223],v3 offset:12544
ds_read_b64_tr_b16 a[224:225],v2 offset:512
v_mfma_f32_16x16x32_bf16 v[60:63],v[28:31],v[112:115],v[60:63]
ds_read_b64_tr_b16 a[226:227],v2 offset:4608
v_cvt_pk_bf16_f32 v89,v90,v91
v_mfma_f32_16x16x32_bf16 v[60:63],v[28:31],v[120:123],v[60:63]
v_exp_f32 v92,v92
v_mfma_f32_16x16x32_bf16 v[96:99],a[128:131],v[196:199],v[40:43]
ds_read_b64_tr_b16 a[228:229],v2 offset:8704
ds_read_b64_tr_b16 a[230:231],v2 offset:12800
v_mfma_f32_16x16x32_bf16 v[96:99],a[132:135],v[204:207],v[96:99]
)A"
R"A(v_exp_f32 v93,v93
v_mfma_f32_16x16x32_bf16 v[96:99],a[136:139],v[212:215],v[96:99]
ds_read_b64_tr_b16 a[232:233],v3 offset:512
ds_read_b64_tr_b16 a[234:235],v3 offset:4608
v_mfma_f32_16x16x32_bf16 v[96:99],a[140:143],v[220:223],v[96:99]
ds_read_b64_tr_b16 a[236:237],v3 offset:8704
v_cvt_pk_bf16_f32 v90,v92,v93
v_mfma_f32_16x16x32_bf16 v[100:103],a[156:159],v[220:223],v[40:43]
v_exp_f32 v94,v94
v_mfma_f32_16x16x32_bf16 v[100:103],a[152:155],v[212:215],v[100:103]
ds_read_b64_tr_b16 a[238:239],v3 offset:12800
ds_read_b64_tr_b16 a[240:241],v2 offset:768
v_mfma_f32_16x16x32_bf16 v[100:103],a[148:151],v[204:207],v[100:103]
v_exp_f32 v95,v95
v_mfma_f32_16x16x32_bf16 v[100:103],a[144:147],v[196:199],v[100:103]
ds_read_b64_tr_b16 a[242:243],v2 offset:4864
ds_read_b64_tr_b16 a[244:245],v2 offset:8960
v_mfma_f32_16x16x32_bf16 v[104:107],a[160:163],v[196:199],v[40:43]
ds_read_b64_tr_b16 a[246:247],v2 offset:13056
v_cvt_pk_bf16_f32 v91,v94,v95
v_mfma_f32_16x16x32_bf16 v[104:107],a[164:167],v[204:207],v[104:107]
v_exp_f32 v96,v96
v_mfma_f32_16x16x32_bf16 v[104:107],a[168:171],v[212:215],v[104:107]
ds_read_b64_tr_b16 a[248:249],v3 offset:768
ds_read_b64_tr_b16 a[250:251],v3 offset:4864
v_mfma_f32_16x16x32_bf16 v[104:107],a[172:175],v[220:223],v[104:107]
v_exp_f32 v97,v97
v_mfma_f32_16x16x32_bf16 v[108:111],a[188:191],v[220:223],v[40:43]
global_load_lds_dwordx4 v5,s[10:11] offset:3072
ds_read_b64_tr_b16 a[252:253],v3 offset:8960
v_mfma_f32_16x16x32_bf16 v[108:111],a[184:187],v[212:215],v[108:111]
ds_read_b64_tr_b16 a[254:255],v3 offset:13056
v_cvt_pk_bf16_f32 v96,v96,v97
v_mfma_f32_16x16x32_bf16 v[108:111],a[180:183],v[204:207],v[108:111]
v_exp_f32 v98,v98
v_mfma_f32_16x16x32_bf16 v[108:111],a[176:179],v[196:199],v[108:111]
s_waitcnt lgkmcnt(5)
v_exp_f32 v99,v99
v_mfma_f32_16x16x32_bf16 a[0:3],a[192:195],v[64:67],a[0:3]
v_exp_f32 v100,v100
v_mfma_f32_16x16x32_bf16 a[0:3],a[196:199],v[72:75],a[0:3]
v_exp_f32 v101,v101
v_mfma_f32_16x16x32_bf16 a[4:7],a[200:203],v[64:67],a[4:7]
v_exp_f32 v102,v102
v_mfma_f32_16x16x32_bf16 a[4:7],a[204:207],v[72:75],a[4:7]
v_exp_f32 v103,v103
v_mfma_f32_16x16x32_bf16 a[8:11],a[208:211],v[64:67],a[8:11]
v_cvt_pk_bf16_f32 v97,v98,v99
v_cvt_pk_bf16_f32 v98,v100,v101
v_mfma_f32_16x16x32_bf16 a[8:11],a[212:215],v[72:75],a[8:11]
v_cvt_pk_bf16_f32 v99,v102,v103
v_mfma_f32_16x16x32_bf16 a[12:15],a[216:219],v[64:67],a[12:15]
v_mfma_f32_16x16x32_bf16 a[12:15],a[220:223],v[72:75],a[12:15]
v_mfma_f32_16x16x32_bf16 a[16:19],a[224:227],v[64:67],a[16:19]
v_mfma_f32_16x16x32_bf16 a[16:19],a[228:231],v[72:75],a[16:19]
v_mfma_f32_16x16x32_bf16 a[20:23],a[232:235],v[64:67],a[20:23]
s_getreg_b32 s92,hwreg(HW_REG_IB_STS,0,4)
s_min_u32 s4,s92,s6
s_mov_b32 s6,s92
v_mfma_f32_16x16x32_bf16 a[20:23],a[236:239],v[72:75],a[20:23]
s_cmp_ge_u32 s4,s9
s_cselect_b32 s4,15,s5
v_mfma_f32_16x16x32_bf16 a[24:27],a[240:243],v[64:67],a[24:27]
s_waitcnt lgkmcnt(0)
v_mfma_f32_16x16x32_bf16 a[24:27],a[244:247],v[72:75],a[24:27]
v_mfma_f32_16x16x32_bf16 a[28:31],a[248:251],v[64:67],a[28:31]
v_mfma_f32_16x16x32_bf16 a[28:31],a[252:255],v[72:75],a[28:31]
s_getpc_b64 s[92:93]
s_cmp_lg_u32 s4,0
s_cbranch_scc1 L_pace_%=
v_mfma_f32_16x16x32_bf16 v[48:51],v[28:31],v[64:67],v[48:51]
v_mfma_f32_16x16x32_bf16 v[48:51],v[28:31],v[72:75],v[48:51]
s_sub_u32 s70,s70,1
s_cbranch_scc0 L_a0_%=
L_nx0_%=:
s_waitcnt vmcnt(12)
s_barrier
v_mfma_f32_16x16x32_bf16 v[112:115],a[128:131],v[228:231],v[44:47]
s_add_u32 s10,s24,s74
s_addc_u32 s11,s25,0
s_add_u32 m0,s76,16384
v_mfma_f32_16x16x32_bf16 v[112:115],a[132:135],v[236:239],v[112:115]
s_add_u32 s75,s75,16384
s_add_u32 s74,s74,16384
ds_read_b128 v[32:35],v7
v_mfma_f32_16x16x32_bf16 v[112:115],a[136:139],v[244:247],v[112:115]
s_and_b32 s74,s74,0x3ffff
global_load_lds_dwordx4 v4,s[10:11]
v_mfma_f32_16x16x32_bf16 v[112:115],a[140:143],v[252:255],v[112:115]
s_and_b32 s69,s75,0xfffc0000
s_or_b32 s74,s74,s69
ds_read_b128 a[128:131],v1
v_mfma_f32_16x16x32_bf16 v[116:119],a[156:159],v[252:255],v[44:47]
s_sub_u32 s76,s76,32768
v_exp_f32 v104,v104
v_mfma_f32_16x16x32_bf16 v[116:119],a[152:155],v[244:247],v[116:119]
s_and_b32 s76,s76,0x1ffff
ds_read_b128 a[132:135],v1 offset:256
v_add_u32 v2,s78,v2
v_mfma_f32_16x16x32_bf16 v[116:119],a[148:151],v[236:239],v[116:119]
v_exp_f32 v105,v105
v_mfma_f32_16x16x32_bf16 v[116:119],a[144:147],v[228:231],v[116:119]
ds_read_b128 a[136:139],v1 offset:512
v_cvt_pk_bf16_f32 v104,v104,v105
v_mfma_f32_16x16x32_bf16 v[120:123],a[160:163],v[228:231],v[44:47]
v_exp_f32 v106,v106
v_mfma_f32_16x16x32_bf16 v[120:123],a[164:167],v[236:239],v[120:123]
ds_read_b128 a[140:143],v1 offset:768
v_add_u32 v3,s78,v3
v_mfma_f32_16x16x32_bf16 v[120:123],a[168:171],v[244:247],v[120:123]
s_sub_u32 s78,s77,32768
v_exp_f32 v107,v107
v_mfma_f32_16x16x32_bf16 v[120:123],a[172:175],v[252:255],v[120:123]
s_and_b32 s78,s78,0x1ffff
ds_read_b128 a[156:159],v1 offset:4864
v_cvt_pk_bf16_f32 v105,v106,v107
v_mfma_f32_16x16x32_bf16 v[124:127],a[188:191],v[252:255],v[44:47]
s_sub_u32 s78,s78,s77
v_exp_f32 v108,v108
v_mfma_f32_16x16x32_bf16 v[124:127],a[184:187],v[244:247],v[124:127]
s_add_u32 s77,s77,s78
ds_read_b128 a[152:155],v1 offset:4608
v_accvgpr_read_b32 v8,a0
v_mfma_f32_16x16x32_bf16 v[124:127],a[180:183],v[236:239],v[124:127]
v_exp_f32 v109,v109
v_mfma_f32_16x16x32_bf16 v[124:127],a[176:179],v[228:231],v[124:127]
ds_read_b128 a[148:151],v1 offset:4352
v_cvt_pk_bf16_f32 v106,v108,v109
v_mfma_f32_16x16x32_bf16 v[52:55],v[28:31],v[80:83],v[52:55]
global_load_lds_dwordx4 v5,s[10:11] offset:1024
v_accvgpr_read_b32 v9,a1
v_mfma_f32_16x16x32_bf16 v[52:55],v[28:31],v[88:91],v[52:55]
ds_read_b128 a[144:147],v1 offset:4096
v_accvgpr_read_b32 v10,a2
v_mfma_f32_16x16x32_bf16 a[32:35],a[192:195],v[80:83],a[32:35]
v_exp_f32 v110,v110
v_mfma_f32_16x16x32_bf16 a[32:35],a[196:199],v[88:91],a[32:35]
ds_read_b128 a[160:163],v1 offset:8192
v_accvgpr_read_b32 v11,a3
v_mfma_f32_16x16x32_bf16 a[36:39],a[200:203],v[80:83],a[36:39]
v_exp_f32 v111,v111
v_mfma_f32_16x16x32_bf16 a[36:39],a[204:207],v[88:91],a[36:39]
ds_read_b128 a[164:167],v1 offset:8448
v_cvt_pk_bf16_f32 v107,v110,v111
v_mfma_f32_16x16x32_bf16 a[40:43],a[208:211],v[80:83],a[40:43]
v_exp_f32 v112,v112
v_mfma_f32_16x16x32_bf16 a[40:43],a[212:215],v[88:91],a[40:43]
ds_read_b128 a[168:171],v1 offset:8704
v_accvgpr_read_b32 v12,a4
v_mfma_f32_16x16x32_bf16 a[44:47],a[216:219],v[80:83],a[44:47]
v_exp_f32 v113,v113
v_mfma_f32_16x16x32_bf16 a[44:47],a[220:223],v[88:91],a[44:47]
ds_read_b128 a[172:175],v1 offset:8960
v_cvt_pk_bf16_f32 v112,v112,v113
v_mfma_f32_16x16x32_bf16 a[48:51],a[224:227],v[80:83],a[48:51]
v_exp_f32 v114,v114
v_mfma_f32_16x16x32_bf16 a[48:51],a[228:231],v[88:91],a[48:51]
ds_read_b128 a[188:191],v1 offset:13056
v_accvgpr_read_b32 v13,a5
v_mfma_f32_16x16x32_bf16 a[52:55],a[232:235],v[80:83],a[52:55]
v_exp_f32 v115,v115
v_mfma_f32_16x16x32_bf16 a[52:55],a[236:239],v[88:91],a[52:55]
ds_read_b128 a[184:187],v1 offset:12800
v_cvt_pk_bf16_f32 v113,v114,v115
v_mfma_f32_16x16x32_bf16 a[56:59],a[240:243],v[80:83],a[56:59]
global_load_lds_dwordx4 v4,s[10:11] offset:2048
v_accvgpr_read_b32 v14,a6
v_mfma_f32_16x16x32_bf16 a[56:59],a[244:247],v[88:91],a[56:59]
ds_read_b128 a[180:183],v1 offset:12544
v_accvgpr_read_b32 v15,a7
v_mfma_f32_16x16x32_bf16 a[60:63],a[248:251],v[80:83],a[60:63]
v_exp_f32 v116,v116
v_mfma_f32_16x16x32_bf16 a[60:63],a[252:255],v[88:91],a[60:63]
s_waitcnt lgkmcnt(3)
ds_read_b128 a[176:179],v1 offset:12288
v_max_f32 v24,0x00800000,v48
v_mfma_f32_16x16x32_bf16 v[64:67],a[128:131],v[128:131],v[32:35]
v_exp_f32 v117,v117
v_mfma_f32_16x16x32_bf16 v[64:67],a[132:135],v[136:139],v[64:67]
ds_read_b128 v[36:39],v7 offset:1024
v_cvt_pk_bf16_f32 v114,v116,v117
v_mfma_f32_16x16x32_bf16 v[64:67],a[136:139],v[144:147],v[64:67]
v_exp_f32 v118,v118
v_mfma_f32_16x16x32_bf16 v[64:67],a[140:143],v[152:155],v[64:67]
v_exp_f32 v119,v119
v_mfma_f32_16x16x32_bf16 v[68:71],a[156:159],v[152:155],v[32:35]
ds_read_b128 v[40:43],v7 offset:2048
v_cvt_pk_bf16_f32 v115,v118,v119
v_mfma_f32_16x16x32_bf16 v[68:71],a[152:155],v[144:147],v[68:71]
v_exp_f32 v120,v120
v_mfma_f32_16x16x32_bf16 v[68:71],a[148:151],v[136:139],v[68:71]
v_exp_f32 v121,v121
v_mfma_f32_16x16x32_bf16 v[68:71],a[144:147],v[128:131],v[68:71]
v_exp_f32 v122,v122
v_mfma_f32_16x16x32_bf16 v[72:75],a[160:163],v[128:131],v[32:35]
v_exp_f32 v123,v123
v_mfma_f32_16x16x32_bf16 v[72:75],a[164:167],v[136:139],v[72:75]
L_pd7_%=:
s_cbranch_execz L_trt0_%=
v_exp_f32 v124,v124
v_mfma_f32_16x16x32_bf16 v[72:75],a[168:171],v[144:147],v[72:75]
s_waitcnt lgkmcnt(1)
v_exp_f32 v125,v125
v_mfma_f32_16x16x32_bf16 v[72:75],a[172:175],v[152:155],v[72:75]
global_load_lds_dwordx4 v5,s[10:11] offset:3072
v_cvt_pk_bf16_f32 v120,v120,v121
v_mfma_f32_16x16x32_bf16 v[76:79],a[188:191],v[152:155],v[32:35]
v_exp_f32 v126,v126
v_mfma_f32_16x16x32_bf16 v[76:79],a[184:187],v[144:147],v[76:79]
v_exp_f32 v127,v127
v_mfma_f32_16x16x32_bf16 v[76:79],a[180:183],v[136:139],v[76:79]
v_cvt_pk_bf16_f32 v121,v122,v123
v_cvt_pk_bf16_f32 v122,v124,v125
v_mfma_f32_16x16x32_bf16 v[76:79],a[176:179],v[128:131],v[76:79]
v_cvt_pk_bf16_f32 v123,v126,v127
v_accvgpr_read_b32 v16,a8
v_mfma_f32_16x16x32_bf16 v[56:59],v[28:31],v[96:99],v[56:59]
v_exp_f32 v64,v64
v_mfma_f32_16x16x32_bf16 v[56:59],v[28:31],v[104:107],v[56:59]
v_exp_f32 v65,v65
v_mfma_f32_16x16x32_bf16 a[64:67],a[192:195],v[96:99],a[64:67]
v_exp_f32 v66,v66
v_mfma_f32_16x16x32_bf16 a[64:67],a[196:199],v[104:107],a[64:67]
v_exp_f32 v67,v67
v_mfma_f32_16x16x32_bf16 a[68:71],a[200:203],v[96:99],a[68:71]
v_exp_f32 v68,v68
v_mfma_f32_16x16x32_bf16 a[68:71],a[204:207],v[104:107],a[68:71]
s_add_u32 s10,s20,s74
s_addc_u32 s11,s21,0
v_cvt_pk_bf16_f32 v64,v64,v65
v_mfma_f32_16x16x32_bf16 a[72:75],a[208:211],v[96:99],a[72:75]
v_exp_f32 v69,v69
v_mfma_f32_16x16x32_bf16 a[72:75],a[212:215],v[104:107],a[72:75]
s_add_u32 m0,s76,0
v_exp_f32 v70,v70
v_mfma_f32_16x16x32_bf16 a[76:79],a[216:219],v[96:99],a[76:79]
v_exp_f32 v71,v71
v_mfma_f32_16x16x32_bf16 a[76:79],a[220:223],v[104:107],a[76:79]
global_load_lds_dwordx4 v4,s[10:11] nt
v_cvt_pk_bf16_f32 v65,v66,v67
v_mfma_f32_16x16x32_bf16 a[80:83],a[224:227],v[96:99],a[80:83]
v_cvt_pk_bf16_f32 v66,v68,v69
v_cvt_pk_bf16_f32 v67,v70,v71
v_mfma_f32_16x16x32_bf16 a[80:83],a[228:231],v[104:107],a[80:83]
v_exp_f32 v72,v72
v_mfma_f32_16x16x32_bf16 a[84:87],a[232:235],v[96:99],a[84:87]
v_exp_f32 v73,v73
v_mfma_f32_16x16x32_bf16 a[84:87],a[236:239],v[104:107],a[84:87]
v_exp_f32 v74,v74
v_mfma_f32_16x16x32_bf16 a[88:91],a[240:243],v[96:99],a[88:91]
v_exp_f32 v75,v75
v_mfma_f32_16x16x32_bf16 a[88:91],a[244:247],v[104:107],a[88:91]
v_exp_f32 v76,v76
v_mfma_f32_16x16x32_bf16 a[92:95],a[248:251],v[96:99],a[92:95]
v_exp_f32 v77,v77
v_mfma_f32_16x16x32_bf16 a[92:95],a[252:255],v[104:107],a[92:95]
v_exp_f32 v78,v78
v_mfma_f32_16x16x32_bf16 v[80:83],a[128:131],v[160:163],v[36:39]
v_exp_f32 v79,v79
v_mfma_f32_16x16x32_bf16 v[80:83],a[132:135],v[168:171],v[80:83]
v_cvt_pk_bf16_f32 v72,v72,v73
v_cvt_pk_bf16_f32 v73,v74,v75
v_mfma_f32_16x16x32_bf16 v[80:83],a[136:139],v[176:179],v[80:83]
v_cvt_pk_bf16_f32 v74,v76,v77
v_cvt_pk_bf16_f32 v75,v78,v79
v_mfma_f32_16x16x32_bf16 v[80:83],a[140:143],v[184:187],v[80:83]
v_rcp_f32 v24,v24
v_mfma_f32_16x16x32_bf16 v[84:87],a[156:159],v[184:187],v[36:39]
v_accvgpr_read_b32 v17,a9
v_accvgpr_read_b32 v18,a10
v_mfma_f32_16x16x32_bf16 v[84:87],a[152:155],v[176:179],v[84:87]
v_accvgpr_read_b32 v19,a11
v_mul_f32 v8,v8,v24
v_mfma_f32_16x16x32_bf16 v[84:87],a[148:151],v[168:171],v[84:87]
global_load_lds_dwordx4 v4,s[10:11] offset:1024 nt
v_mul_f32 v9,v9,v24
v_mfma_f32_16x16x32_bf16 v[84:87],a[144:147],v[160:163],v[84:87]
v_mul_f32 v10,v10,v24
v_mul_f32 v11,v11,v24
v_mfma_f32_16x16x32_bf16 v[88:91],a[160:163],v[160:163],v[36:39]
v_mul_f32 v12,v12,v24
v_mul_f32 v13,v13,v24
v_mfma_f32_16x16x32_bf16 v[88:91],a[164:167],v[168:171],v[88:91]
v_mul_f32 v14,v14,v24
v_mul_f32 v15,v15,v24
v_mfma_f32_16x16x32_bf16 v[88:91],a[168:171],v[176:179],v[88:91]
v_cvt_pk_bf16_f32 v8,v8,v9
v_cvt_pk_bf16_f32 v9,v10,v11
v_mfma_f32_16x16x32_bf16 v[88:91],a[172:175],v[184:187],v[88:91]
)A"
R"A(v_cvt_pk_bf16_f32 v10,v12,v13
v_cvt_pk_bf16_f32 v11,v14,v15
v_mfma_f32_16x16x32_bf16 v[92:95],a[188:191],v[184:187],v[36:39]
ds_write_b128 v7,v[8:11] offset:6144
v_mfma_f32_16x16x32_bf16 v[92:95],a[184:187],v[176:179],v[92:95]
v_accvgpr_read_b32 v20,a12
v_accvgpr_read_b32 v21,a13
v_mfma_f32_16x16x32_bf16 v[92:95],a[180:183],v[168:171],v[92:95]
v_accvgpr_read_b32 v22,a14
v_accvgpr_read_b32 v23,a15
v_mfma_f32_16x16x32_bf16 v[92:95],a[176:179],v[160:163],v[92:95]
s_cmp_le_u32 s5,s7
s_cselect_b32 s4,2,0
v_accvgpr_read_b32 v8,a16
v_mfma_f32_16x16x32_bf16 v[60:63],v[28:31],v[112:115],v[60:63]
s_sub_u32 s5,s5,s4
v_accvgpr_read_b32 v9,a17
v_accvgpr_read_b32 v10,a18
v_mfma_f32_16x16x32_bf16 v[60:63],v[28:31],v[120:123],v[60:63]
s_max_i32 s5,s5,0
v_accvgpr_read_b32 v11,a19
v_mul_f32 v16,v16,v24
v_mfma_f32_16x16x32_bf16 a[96:99],a[192:195],v[112:115],a[96:99]
s_sub_u32 s7,s7,2
ds_read_b64_tr_b16 a[192:193],v2
ds_read_b64_tr_b16 a[194:195],v2 offset:4096
v_mfma_f32_16x16x32_bf16 a[96:99],a[196:199],v[120:123],a[96:99]
s_max_i32 s7,s7,0
ds_read_b64_tr_b16 a[196:197],v2 offset:8192
v_mul_f32 v17,v17,v24
v_mfma_f32_16x16x32_bf16 a[100:103],a[200:203],v[112:115],a[100:103]
global_load_lds_dwordx4 v5,s[10:11] offset:2048 nt
ds_read_b64_tr_b16 a[198:199],v2 offset:12288
v_mfma_f32_16x16x32_bf16 a[100:103],a[204:207],v[120:123],a[100:103]
ds_read_b64_tr_b16 a[200:201],v3
v_mul_f32 v18,v18,v24
v_mfma_f32_16x16x32_bf16 a[104:107],a[208:211],v[112:115],a[104:107]
ds_read_b64_tr_b16 a[202:203],v3 offset:4096
ds_read_b64_tr_b16 a[204:205],v3 offset:8192
v_mfma_f32_16x16x32_bf16 a[104:107],a[212:215],v[120:123],a[104:107]
ds_read_b64_tr_b16 a[206:207],v3 offset:12288
v_mul_f32 v19,v19,v24
v_mfma_f32_16x16x32_bf16 a[108:111],a[216:219],v[112:115],a[108:111]
ds_read_b64_tr_b16 a[208:209],v2 offset:256
v_mul_f32 v20,v20,v24
v_mfma_f32_16x16x32_bf16 a[108:111],a[220:223],v[120:123],a[108:111]
ds_read_b64_tr_b16 a[210:211],v2 offset:4352
v_mul_f32 v21,v21,v24
v_mfma_f32_16x16x32_bf16 a[112:115],a[224:227],v[112:115],a[112:115]
ds_read_b64_tr_b16 a[212:213],v2 offset:8448
ds_read_b64_tr_b16 a[214:215],v2 offset:12544
v_mfma_f32_16x16x32_bf16 a[112:115],a[228:231],v[120:123],a[112:115]
ds_read_b64_tr_b16 a[216:217],v3 offset:256
v_mul_f32 v22,v22,v24
v_mfma_f32_16x16x32_bf16 a[116:119],a[232:235],v[112:115],a[116:119]
ds_read_b64_tr_b16 a[218:219],v3 offset:4352
v_mul_f32 v23,v23,v24
v_mfma_f32_16x16x32_bf16 a[116:119],a[236:239],v[120:123],a[116:119]
ds_read_b64_tr_b16 a[220:221],v3 offset:8448
v_cvt_pk_bf16_f32 v16,v16,v17
v_mfma_f32_16x16x32_bf16 a[120:123],a[240:243],v[112:115],a[120:123]
ds_read_b64_tr_b16 a[222:223],v3 offset:12544
ds_read_b64_tr_b16 a[224:225],v2 offset:512
v_mfma_f32_16x16x32_bf16 a[120:123],a[244:247],v[120:123],a[120:123]
ds_read_b64_tr_b16 a[226:227],v2 offset:4608
v_cvt_pk_bf16_f32 v17,v18,v19
v_mfma_f32_16x16x32_bf16 a[124:127],a[248:251],v[112:115],a[124:127]
ds_read_b64_tr_b16 a[228:229],v2 offset:8704
v_cvt_pk_bf16_f32 v18,v20,v21
v_mfma_f32_16x16x32_bf16 a[124:127],a[252:255],v[120:123],a[124:127]
s_waitcnt lgkmcnt(9)
ds_read_b64_tr_b16 a[230:231],v2 offset:12800
v_cvt_pk_bf16_f32 v19,v22,v23
v_mfma_f32_16x16x32_bf16 v[96:99],a[128:131],v[192:195],v[40:43]
ds_read_b64_tr_b16 a[232:233],v3 offset:512
ds_read_b64_tr_b16 a[234:235],v3 offset:4608
v_mfma_f32_16x16x32_bf16 v[96:99],a[132:135],v[200:203],v[96:99]
global_load_lds_dwordx4 v5,s[10:11] offset:3072
ds_read_b64_tr_b16 a[236:237],v3 offset:8704
v_mfma_f32_16x16x32_bf16 v[96:99],a[136:139],v[208:211],v[96:99]
ds_read_b64_tr_b16 a[238:239],v3 offset:12800
v_accvgpr_read_b32 v12,a20
v_mfma_f32_16x16x32_bf16 v[96:99],a[140:143],v[216:219],v[96:99]
ds_read_b64_tr_b16 a[240:241],v2 offset:768
v_accvgpr_read_b32 v13,a21
v_mfma_f32_16x16x32_bf16 v[100:103],a[156:159],v[216:219],v[40:43]
ds_read_b64_tr_b16 a[242:243],v2 offset:4864
ds_read_b64_tr_b16 a[244:245],v2 offset:8960
v_mfma_f32_16x16x32_bf16 v[100:103],a[152:155],v[208:211],v[100:103]
ds_read_b64_tr_b16 a[246:247],v2 offset:13056
v_accvgpr_read_b32 v14,a22
v_mfma_f32_16x16x32_bf16 v[100:103],a[148:151],v[200:203],v[100:103]
ds_read_b64_tr_b16 a[248:249],v3 offset:768
v_accvgpr_read_b32 v15,a23
v_mfma_f32_16x16x32_bf16 v[100:103],a[144:147],v[192:195],v[100:103]
ds_read_b64_tr_b16 a[250:251],v3 offset:4864
v_accvgpr_read_b32 v20,a28
v_mfma_f32_16x16x32_bf16 v[104:107],a[160:163],v[192:195],v[40:43]
ds_read_b64_tr_b16 a[252:253],v3 offset:8960
ds_read_b64_tr_b16 a[254:255],v3 offset:13056
v_mfma_f32_16x16x32_bf16 v[104:107],a[164:167],v[200:203],v[104:107]
v_accvgpr_read_b32 v21,a29
v_accvgpr_read_b32 v22,a30
v_mfma_f32_16x16x32_bf16 v[104:107],a[168:171],v[208:211],v[104:107]
ds_write_b128 v7,v[16:19] offset:7168
v_mfma_f32_16x16x32_bf16 v[104:107],a[172:175],v[216:219],v[104:107]
s_add_u32 s10,s24,s74
s_addc_u32 s11,s25,0
v_accvgpr_read_b32 v23,a31
v_mfma_f32_16x16x32_bf16 v[108:111],a[188:191],v[216:219],v[40:43]
v_accvgpr_read_b32 v16,a24
v_accvgpr_read_b32 v17,a25
v_mfma_f32_16x16x32_bf16 v[108:111],a[184:187],v[208:211],v[108:111]
s_add_u32 m0,s76,16384
v_accvgpr_read_b32 v18,a26
v_accvgpr_read_b32 v19,a27
v_mfma_f32_16x16x32_bf16 v[108:111],a[180:183],v[200:203],v[108:111]
v_cmp_nlt_f32 s[54:55],v48,s73
v_cmp_nlt_f32 s[56:57],v52,s73
v_mfma_f32_16x16x32_bf16 v[108:111],a[176:179],v[192:195],v[108:111]
s_or_b64 s[54:55],s[54:55],s[56:57]
global_load_lds_dwordx4 v4,s[10:11]
v_mfma_f32_16x16x32_bf16 a[0:3],a[192:195],v[64:67],0
v_cmp_nlt_f32 s[56:57],v56,s73
v_mul_f32 v8,v8,v24
v_mfma_f32_16x16x32_bf16 a[0:3],a[196:199],v[72:75],a[0:3]
s_or_b64 s[54:55],s[54:55],s[56:57]
v_mul_f32 v9,v9,v24
v_mul_f32 v10,v10,v24
v_mfma_f32_16x16x32_bf16 a[4:7],a[200:203],v[64:67],0
v_mul_f32 v11,v11,v24
v_mul_f32 v12,v12,v24
v_mfma_f32_16x16x32_bf16 a[4:7],a[204:207],v[72:75],a[4:7]
v_mul_f32 v13,v13,v24
v_mul_f32 v14,v14,v24
v_mfma_f32_16x16x32_bf16 a[8:11],a[208:211],v[64:67],0
s_waitcnt lgkmcnt(0)
v_mul_f32 v15,v15,v24
v_cvt_pk_bf16_f32 v8,v8,v9
v_mfma_f32_16x16x32_bf16 a[8:11],a[212:215],v[72:75],a[8:11]
v_cvt_pk_bf16_f32 v9,v10,v11
v_cvt_pk_bf16_f32 v10,v12,v13
v_mfma_f32_16x16x32_bf16 a[12:15],a[216:219],v[64:67],0
v_cvt_pk_bf16_f32 v11,v14,v15
v_mul_f32 v16,v16,v24
v_mfma_f32_16x16x32_bf16 a[12:15],a[220:223],v[72:75],a[12:15]
v_mul_f32 v17,v17,v24
v_mul_f32 v18,v18,v24
v_mfma_f32_16x16x32_bf16 a[16:19],a[224:227],v[64:67],0
v_mul_f32 v19,v19,v24
v_mul_f32 v20,v20,v24
v_mfma_f32_16x16x32_bf16 a[16:19],a[228:231],v[72:75],a[16:19]
v_mul_f32 v21,v21,v24
v_mul_f32 v22,v22,v24
v_mfma_f32_16x16x32_bf16 a[20:23],a[232:235],v[64:67],0
s_getreg_b32 s92,hwreg(HW_REG_IB_STS,0,4)
s_min_u32 s4,s92,s6
s_mov_b32 s6,s92
v_mfma_f32_16x16x32_bf16 a[20:23],a[236:239],v[72:75],a[20:23]
s_cmp_gt_u32 s4,s9
s_cselect_b32 s4,15,s5
v_mul_f32 v23,v23,v24
v_mfma_f32_16x16x32_bf16 a[24:27],a[240:243],v[64:67],0
v_cvt_pk_bf16_f32 v16,v16,v17
v_cvt_pk_bf16_f32 v17,v18,v19
v_mfma_f32_16x16x32_bf16 a[24:27],a[244:247],v[72:75],a[24:27]
v_cvt_pk_bf16_f32 v18,v20,v21
v_cvt_pk_bf16_f32 v19,v22,v23
v_mfma_f32_16x16x32_bf16 a[28:31],a[248:251],v[64:67],0
global_load_lds_dwordx4 v5,s[10:11] offset:1024
v_cmp_nlt_f32 s[56:57],v60,s73
v_mfma_f32_16x16x32_bf16 a[28:31],a[252:255],v[72:75],a[28:31]
s_or_b64 s[54:55],s[54:55],s[56:57]
v_exp_f32 v80,v80
v_mfma_f32_16x16x32_bf16 v[48:51],v[28:31],v[64:67],0
s_getpc_b64 s[92:93]
s_cmp_lg_u32 s4,0
s_cbranch_scc1 L_pace_%=
v_exp_f32 v81,v81
v_exp_f32 v82,v82
v_exp_f32 v83,v83
v_exp_f32 v84,v84
v_exp_f32 v85,v85
v_exp_f32 v86,v86
v_exp_f32 v87,v87
v_exp_f32 v88,v88
v_exp_f32 v89,v89
v_mfma_f32_16x16x32_bf16 v[48:51],v[28:31],v[72:75],v[48:51]
ds_write_b128 v7,v[8:11]
v_add_u32 v1,s78,v1
v_max_f32 v25,0x00800000,v52
v_rcp_f32 v25,v25
v_max_f32 v26,0x00800000,v56
v_rcp_f32 v26,v26
v_cvt_pk_bf16_f32 v80,v80,v81
v_cvt_pk_bf16_f32 v81,v82,v83
v_cvt_pk_bf16_f32 v82,v84,v85
v_cvt_pk_bf16_f32 v83,v86,v87
v_cvt_pk_bf16_f32 v88,v88,v89
v_max_f32 v27,0x00800000,v60
v_rcp_f32 v27,v27
ds_write_b128 v7,v[16:19] offset:1024
ds_read_b128 v[44:47],v7 offset:3072
s_cmp_lg_u64 s[54:55],0
s_cbranch_scc1 L_slow0_%=
L_ok0_%=:
s_waitcnt vmcnt(14)
s_barrier
s_waitcnt lgkmcnt(0)
v_mfma_f32_16x16x32_bf16 v[112:115],a[128:131],v[224:227],v[44:47]
s_add_u32 s10,s24,s74
s_addc_u32 s11,s25,0
s_add_u32 m0,s76,16384
v_mfma_f32_16x16x32_bf16 v[112:115],a[132:135],v[232:235],v[112:115]
s_add_u32 s75,s75,16384
v_exp_f32 v90,v90
v_mfma_f32_16x16x32_bf16 v[112:115],a[136:139],v[240:243],v[112:115]
global_load_lds_dwordx4 v4,s[10:11] offset:2048
ds_read_b128 a[128:131],v1
v_mfma_f32_16x16x32_bf16 v[112:115],a[140:143],v[248:251],v[112:115]
s_add_u32 s74,s74,16384
v_exp_f32 v91,v91
v_mfma_f32_16x16x32_bf16 v[116:119],a[156:159],v[248:251],v[44:47]
s_and_b32 s74,s74,0x3ffff
v_exp_f32 v92,v92
v_mfma_f32_16x16x32_bf16 v[116:119],a[152:155],v[240:243],v[116:119]
s_and_b32 s69,s75,0xfffc0000
v_exp_f32 v93,v93
v_mfma_f32_16x16x32_bf16 v[116:119],a[148:151],v[232:235],v[116:119]
s_or_b32 s74,s74,s69
v_exp_f32 v94,v94
v_mfma_f32_16x16x32_bf16 v[116:119],a[144:147],v[224:227],v[116:119]
s_sub_u32 s76,s76,32768
v_exp_f32 v95,v95
v_mfma_f32_16x16x32_bf16 v[120:123],a[160:163],v[224:227],v[44:47]
s_and_b32 s76,s76,0x1ffff
v_cvt_pk_bf16_f32 v89,v90,v91
v_cvt_pk_bf16_f32 v90,v92,v93
v_mfma_f32_16x16x32_bf16 v[120:123],a[164:167],v[232:235],v[120:123]
s_add_u32 s75,s75,16384
ds_read_b128 a[132:135],v1 offset:256
v_cvt_pk_bf16_f32 v91,v94,v95
v_mfma_f32_16x16x32_bf16 v[120:123],a[168:171],v[240:243],v[120:123]
s_and_b32 s69,s75,0xfffc0000
v_exp_f32 v96,v96
v_mfma_f32_16x16x32_bf16 v[120:123],a[172:175],v[248:251],v[120:123]
s_add_u32 s75,s75,16384
ds_read_b128 a[136:139],v1 offset:512
v_accvgpr_read_b32 v16,a32
v_mfma_f32_16x16x32_bf16 v[124:127],a[188:191],v[248:251],v[44:47]
v_exp_f32 v97,v97
v_mfma_f32_16x16x32_bf16 v[124:127],a[184:187],v[240:243],v[124:127]
ds_read_b128 a[140:143],v1 offset:768
v_cvt_pk_bf16_f32 v96,v96,v97
v_mfma_f32_16x16x32_bf16 v[124:127],a[180:183],v[232:235],v[124:127]
v_exp_f32 v98,v98
v_mfma_f32_16x16x32_bf16 v[124:127],a[176:179],v[224:227],v[124:127]
ds_read_b128 a[156:159],v1 offset:4864
v_accvgpr_read_b32 v17,a33
v_mfma_f32_16x16x32_bf16 v[132:135],a[192:195],v[80:83],0
global_load_lds_dwordx4 v5,s[10:11] offset:3072
v_accvgpr_read_b32 v18,a34
v_mfma_f32_16x16x32_bf16 v[132:135],a[196:199],v[88:91],v[132:135]
ds_read_b128 a[152:155],v1 offset:4608
v_accvgpr_read_b32 v19,a35
v_mfma_f32_16x16x32_bf16 v[140:143],a[200:203],v[80:83],0
v_exp_f32 v99,v99
v_mfma_f32_16x16x32_bf16 v[140:143],a[204:207],v[88:91],v[140:143]
ds_read_b128 a[148:151],v1 offset:4352
v_cvt_pk_bf16_f32 v97,v98,v99
v_mfma_f32_16x16x32_bf16 v[148:151],a[208:211],v[80:83],0
v_exp_f32 v100,v100
v_mfma_f32_16x16x32_bf16 v[148:151],a[212:215],v[88:91],v[148:151]
ds_read_b128 a[144:147],v1 offset:4096
v_accvgpr_read_b32 v20,a36
v_mfma_f32_16x16x32_bf16 v[156:159],a[216:219],v[80:83],0
v_exp_f32 v101,v101
v_mfma_f32_16x16x32_bf16 v[156:159],a[220:223],v[88:91],v[156:159]
ds_read_b128 a[160:163],v1 offset:8192
v_cvt_pk_bf16_f32 v98,v100,v101
v_mfma_f32_16x16x32_bf16 v[164:167],a[224:227],v[80:83],0
v_exp_f32 v102,v102
v_mfma_f32_16x16x32_bf16 v[164:167],a[228:231],v[88:91],v[164:167]
ds_read_b128 a[164:167],v1 offset:8448
v_accvgpr_read_b32 v21,a37
v_mfma_f32_16x16x32_bf16 v[172:175],a[232:235],v[80:83],0
s_add_u32 s10,s20,s74
s_addc_u32 s11,s21,0
v_accvgpr_read_b32 v22,a38
v_mfma_f32_16x16x32_bf16 v[172:175],a[236:239],v[88:91],v[172:175]
ds_read_b128 a[168:171],v1 offset:8704
v_accvgpr_read_b32 v23,a39
v_mfma_f32_16x16x32_bf16 v[180:183],a[240:243],v[80:83],0
s_add_u32 m0,s76,0
v_exp_f32 v103,v103
v_mfma_f32_16x16x32_bf16 v[180:183],a[244:247],v[88:91],v[180:183]
ds_read_b128 a[172:175],v1 offset:8960
v_cvt_pk_bf16_f32 v99,v102,v103
v_mfma_f32_16x16x32_bf16 v[188:191],a[248:251],v[80:83],0
global_load_lds_dwordx4 v4,s[10:11] nt
v_mul_f32 v16,v16,v25
v_mfma_f32_16x16x32_bf16 v[188:191],a[252:255],v[88:91],v[188:191]
ds_read_b128 a[188:191],v1 offset:13056
v_mul_f32 v17,v17,v25
v_mfma_f32_16x16x32_bf16 v[52:55],v[28:31],v[80:83],0
)A"
R"A(v_exp_f32 v104,v104
v_mfma_f32_16x16x32_bf16 v[52:55],v[28:31],v[88:91],v[52:55]
s_waitcnt lgkmcnt(3)
ds_read_b128 a[184:187],v1 offset:12800
v_mul_f32 v18,v18,v25
v_mfma_f32_16x16x32_bf16 v[64:67],a[128:131],v[128:131],v[32:35]
v_exp_f32 v105,v105
v_mfma_f32_16x16x32_bf16 v[64:67],a[132:135],v[136:139],v[64:67]
ds_read_b128 a[180:183],v1 offset:12544
v_cvt_pk_bf16_f32 v104,v104,v105
v_mfma_f32_16x16x32_bf16 v[64:67],a[136:139],v[144:147],v[64:67]
v_exp_f32 v106,v106
v_mfma_f32_16x16x32_bf16 v[64:67],a[140:143],v[152:155],v[64:67]
ds_read_b128 a[176:179],v1 offset:12288
v_mul_f32 v19,v19,v25
v_mfma_f32_16x16x32_bf16 v[68:71],a[156:159],v[152:155],v[32:35]
v_exp_f32 v107,v107
v_mfma_f32_16x16x32_bf16 v[68:71],a[152:155],v[144:147],v[68:71]
v_exp_f32 v108,v108
v_mfma_f32_16x16x32_bf16 v[68:71],a[148:151],v[136:139],v[68:71]
v_exp_f32 v109,v109
v_mfma_f32_16x16x32_bf16 v[68:71],a[144:147],v[128:131],v[68:71]
v_exp_f32 v110,v110
v_mfma_f32_16x16x32_bf16 v[72:75],a[160:163],v[128:131],v[32:35]
v_exp_f32 v111,v111
v_mfma_f32_16x16x32_bf16 v[72:75],a[164:167],v[136:139],v[72:75]
s_waitcnt lgkmcnt(1)
v_cvt_pk_bf16_f32 v105,v106,v107
v_cvt_pk_bf16_f32 v106,v108,v109
v_mfma_f32_16x16x32_bf16 v[72:75],a[168:171],v[144:147],v[72:75]
global_load_lds_dwordx4 v4,s[10:11] offset:1024 nt
v_cvt_pk_bf16_f32 v107,v110,v111
v_mfma_f32_16x16x32_bf16 v[72:75],a[172:175],v[152:155],v[72:75]
v_exp_f32 v112,v112
v_mfma_f32_16x16x32_bf16 v[76:79],a[188:191],v[152:155],v[32:35]
v_exp_f32 v113,v113
v_mfma_f32_16x16x32_bf16 v[76:79],a[184:187],v[144:147],v[76:79]
v_exp_f32 v114,v114
v_mfma_f32_16x16x32_bf16 v[76:79],a[180:183],v[136:139],v[76:79]
s_waitcnt lgkmcnt(0)
v_exp_f32 v115,v115
v_mfma_f32_16x16x32_bf16 v[76:79],a[176:179],v[128:131],v[76:79]
v_exp_f32 v116,v116
v_mfma_f32_16x16x32_bf16 v[196:199],a[192:195],v[96:99],0
v_exp_f32 v117,v117
v_mfma_f32_16x16x32_bf16 v[196:199],a[196:199],v[104:107],v[196:199]
v_exp_f32 v118,v118
v_mfma_f32_16x16x32_bf16 v[204:207],a[200:203],v[96:99],0
v_exp_f32 v119,v119
v_mfma_f32_16x16x32_bf16 v[204:207],a[204:207],v[104:107],v[204:207]
v_cvt_pk_bf16_f32 v112,v112,v113
v_cvt_pk_bf16_f32 v113,v114,v115
v_mfma_f32_16x16x32_bf16 v[212:215],a[208:211],v[96:99],0
v_cvt_pk_bf16_f32 v114,v116,v117
v_cvt_pk_bf16_f32 v115,v118,v119
v_mfma_f32_16x16x32_bf16 v[212:215],a[212:215],v[104:107],v[212:215]
v_exp_f32 v120,v120
v_mfma_f32_16x16x32_bf16 v[220:223],a[216:219],v[96:99],0
v_exp_f32 v121,v121
v_mfma_f32_16x16x32_bf16 v[220:223],a[220:223],v[104:107],v[220:223]
v_exp_f32 v122,v122
v_mfma_f32_16x16x32_bf16 v[228:231],a[224:227],v[96:99],0
global_load_lds_dwordx4 v5,s[10:11] offset:2048 nt
v_cvt_pk_bf16_f32 v120,v120,v121
v_mfma_f32_16x16x32_bf16 v[228:231],a[228:231],v[104:107],v[228:231]
v_exp_f32 v123,v123
v_mfma_f32_16x16x32_bf16 v[236:239],a[232:235],v[96:99],0
v_exp_f32 v124,v124
v_mfma_f32_16x16x32_bf16 v[236:239],a[236:239],v[104:107],v[236:239]
v_exp_f32 v125,v125
v_mfma_f32_16x16x32_bf16 v[244:247],a[240:243],v[96:99],0
v_exp_f32 v126,v126
v_mfma_f32_16x16x32_bf16 v[244:247],a[244:247],v[104:107],v[244:247]
v_exp_f32 v127,v127
v_mfma_f32_16x16x32_bf16 v[252:255],a[248:251],v[96:99],0
v_cvt_pk_bf16_f32 v121,v122,v123
v_cvt_pk_bf16_f32 v122,v124,v125
v_mfma_f32_16x16x32_bf16 v[252:255],a[252:255],v[104:107],v[252:255]
v_cvt_pk_bf16_f32 v123,v126,v127
v_mul_f32 v20,v20,v25
v_mfma_f32_16x16x32_bf16 v[56:59],v[28:31],v[96:99],0
v_mul_f32 v21,v21,v25
v_mul_f32 v22,v22,v25
v_mfma_f32_16x16x32_bf16 v[56:59],v[28:31],v[104:107],v[56:59]
v_mul_f32 v23,v23,v25
v_cvt_pk_bf16_f32 v16,v16,v17
v_mfma_f32_16x16x32_bf16 v[80:83],a[128:131],v[160:163],v[36:39]
v_cvt_pk_bf16_f32 v17,v18,v19
v_cvt_pk_bf16_f32 v18,v20,v21
v_mfma_f32_16x16x32_bf16 v[80:83],a[132:135],v[168:171],v[80:83]
v_cvt_pk_bf16_f32 v19,v22,v23
v_accvgpr_read_b32 v20,a44
v_mfma_f32_16x16x32_bf16 v[80:83],a[136:139],v[176:179],v[80:83]
ds_write_b128 v7,v[16:19] offset:2048
v_mfma_f32_16x16x32_bf16 v[80:83],a[140:143],v[184:187],v[80:83]
v_accvgpr_read_b32 v21,a45
v_accvgpr_read_b32 v22,a46
v_mfma_f32_16x16x32_bf16 v[84:87],a[156:159],v[184:187],v[36:39]
v_accvgpr_read_b32 v16,a40
v_accvgpr_read_b32 v17,a41
v_mfma_f32_16x16x32_bf16 v[84:87],a[152:155],v[176:179],v[84:87]
global_load_lds_dwordx4 v5,s[10:11] offset:3072
v_accvgpr_read_b32 v18,a42
v_mfma_f32_16x16x32_bf16 v[84:87],a[148:151],v[168:171],v[84:87]
v_accvgpr_read_b32 v19,a43
v_accvgpr_read_b32 v23,a47
v_mfma_f32_16x16x32_bf16 v[84:87],a[144:147],v[160:163],v[84:87]
v_accvgpr_read_b32 v8,a48
v_accvgpr_read_b32 v9,a49
v_mfma_f32_16x16x32_bf16 v[88:91],a[160:163],v[160:163],v[36:39]
v_accvgpr_read_b32 v10,a50
v_accvgpr_read_b32 v11,a51
v_mfma_f32_16x16x32_bf16 v[88:91],a[164:167],v[168:171],v[88:91]
v_mul_f32 v16,v16,v25
v_mul_f32 v17,v17,v25
v_mfma_f32_16x16x32_bf16 v[88:91],a[168:171],v[176:179],v[88:91]
v_mul_f32 v18,v18,v25
v_mul_f32 v19,v19,v25
v_mfma_f32_16x16x32_bf16 v[88:91],a[172:175],v[184:187],v[88:91]
v_mul_f32 v20,v20,v25
v_mul_f32 v21,v21,v25
v_mfma_f32_16x16x32_bf16 v[92:95],a[188:191],v[184:187],v[36:39]
v_mul_f32 v22,v22,v25
v_mul_f32 v23,v23,v25
v_mfma_f32_16x16x32_bf16 v[92:95],a[184:187],v[176:179],v[92:95]
v_cvt_pk_bf16_f32 v16,v16,v17
v_cvt_pk_bf16_f32 v17,v18,v19
v_mfma_f32_16x16x32_bf16 v[92:95],a[180:183],v[168:171],v[92:95]
v_cvt_pk_bf16_f32 v18,v20,v21
v_cvt_pk_bf16_f32 v19,v22,v23
v_mfma_f32_16x16x32_bf16 v[92:95],a[176:179],v[160:163],v[92:95]
s_add_u32 s10,s24,s74
s_addc_u32 s11,s25,0
v_accvgpr_read_b32 v12,a52
v_mfma_f32_16x16x32_bf16 a[32:35],a[192:195],v[112:115],0
ds_write_b128 v7,v[16:19] offset:3072
v_mfma_f32_16x16x32_bf16 a[32:35],a[196:199],v[120:123],a[32:35]
s_add_u32 m0,s76,16384
v_accvgpr_read_b32 v13,a53
v_accvgpr_read_b32 v14,a54
v_mfma_f32_16x16x32_bf16 a[36:39],a[200:203],v[112:115],0
v_accvgpr_read_b32 v15,a55
v_accvgpr_read_b32 v16,a56
v_mfma_f32_16x16x32_bf16 a[36:39],a[204:207],v[120:123],a[36:39]
global_load_lds_dwordx4 v4,s[10:11]
v_accvgpr_read_b32 v17,a57
v_mfma_f32_16x16x32_bf16 a[40:43],a[208:211],v[112:115],0
v_accvgpr_read_b32 v18,a58
v_accvgpr_read_b32 v19,a59
v_mfma_f32_16x16x32_bf16 a[40:43],a[212:215],v[120:123],a[40:43]
v_accvgpr_read_b32 v20,a60
v_accvgpr_read_b32 v21,a61
v_mfma_f32_16x16x32_bf16 a[44:47],a[216:219],v[112:115],0
v_accvgpr_read_b32 v22,a62
v_accvgpr_read_b32 v23,a63
v_mfma_f32_16x16x32_bf16 a[44:47],a[220:223],v[120:123],a[44:47]
v_add_u32 v2,s78,v2
v_add_u32 v3,s78,v3
v_mfma_f32_16x16x32_bf16 a[48:51],a[224:227],v[112:115],0
s_sub_u32 s78,s77,32768
ds_read_b64_tr_b16 a[192:193],v2
ds_read_b64_tr_b16 a[194:195],v2 offset:4096
v_mfma_f32_16x16x32_bf16 a[48:51],a[228:231],v[120:123],a[48:51]
s_and_b32 s78,s78,0x1ffff
ds_read_b64_tr_b16 a[196:197],v2 offset:8192
v_mul_f32 v8,v8,v25
v_mfma_f32_16x16x32_bf16 a[52:55],a[232:235],v[112:115],0
s_sub_u32 s78,s78,s77
ds_read_b64_tr_b16 a[198:199],v2 offset:12288
v_mul_f32 v9,v9,v25
v_mfma_f32_16x16x32_bf16 a[52:55],a[236:239],v[120:123],a[52:55]
s_add_u32 s77,s77,s78
ds_read_b64_tr_b16 a[200:201],v3
v_mul_f32 v10,v10,v25
v_mfma_f32_16x16x32_bf16 a[56:59],a[240:243],v[112:115],0
ds_read_b64_tr_b16 a[202:203],v3 offset:4096
ds_read_b64_tr_b16 a[204:205],v3 offset:8192
v_mfma_f32_16x16x32_bf16 a[56:59],a[244:247],v[120:123],a[56:59]
ds_read_b64_tr_b16 a[206:207],v3 offset:12288
v_mul_f32 v11,v11,v25
v_mfma_f32_16x16x32_bf16 a[60:63],a[248:251],v[112:115],0
ds_read_b64_tr_b16 a[208:209],v2 offset:256
v_mul_f32 v12,v12,v25
v_mfma_f32_16x16x32_bf16 a[60:63],a[252:255],v[120:123],a[60:63]
ds_read_b64_tr_b16 a[210:211],v2 offset:4352
v_mul_f32 v13,v13,v25
v_mfma_f32_16x16x32_bf16 v[60:63],v[28:31],v[112:115],0
ds_read_b64_tr_b16 a[212:213],v2 offset:8448
ds_read_b64_tr_b16 a[214:215],v2 offset:12544
v_mfma_f32_16x16x32_bf16 v[60:63],v[28:31],v[120:123],v[60:63]
global_load_lds_dwordx4 v5,s[10:11] offset:1024
ds_read_b64_tr_b16 a[216:217],v3 offset:256
v_mfma_f32_16x16x32_bf16 v[96:99],a[128:131],v[192:195],v[40:43]
v_exp_f32 v64,v64
v_mfma_f32_16x16x32_bf16 v[96:99],a[132:135],v[200:203],v[96:99]
v_exp_f32 v65,v65
v_mfma_f32_16x16x32_bf16 v[96:99],a[136:139],v[208:211],v[96:99]
v_exp_f32 v66,v66
v_mfma_f32_16x16x32_bf16 v[96:99],a[140:143],v[216:219],v[96:99]
v_exp_f32 v67,v67
v_mfma_f32_16x16x32_bf16 v[100:103],a[156:159],v[216:219],v[40:43]
v_exp_f32 v68,v68
v_mfma_f32_16x16x32_bf16 v[100:103],a[152:155],v[208:211],v[100:103]
v_exp_f32 v69,v69
v_mfma_f32_16x16x32_bf16 v[100:103],a[148:151],v[200:203],v[100:103]
v_exp_f32 v70,v70
v_mfma_f32_16x16x32_bf16 v[100:103],a[144:147],v[192:195],v[100:103]
L_pd8_%=:
s_cbranch_execz L_trt0_%=
v_exp_f32 v71,v71
v_mfma_f32_16x16x32_bf16 v[104:107],a[160:163],v[192:195],v[40:43]
ds_read_b64_tr_b16 a[218:219],v3 offset:4352
v_cvt_pk_bf16_f32 v64,v64,v65
v_mfma_f32_16x16x32_bf16 v[104:107],a[164:167],v[200:203],v[104:107]
v_cvt_pk_bf16_f32 v65,v66,v67
v_cvt_pk_bf16_f32 v66,v68,v69
v_mfma_f32_16x16x32_bf16 v[104:107],a[168:171],v[208:211],v[104:107]
ds_read_b64_tr_b16 a[220:221],v3 offset:8448
v_cvt_pk_bf16_f32 v67,v70,v71
v_mfma_f32_16x16x32_bf16 v[104:107],a[172:175],v[216:219],v[104:107]
v_exp_f32 v72,v72
v_mfma_f32_16x16x32_bf16 v[108:111],a[188:191],v[216:219],v[40:43]
v_exp_f32 v73,v73
v_mfma_f32_16x16x32_bf16 v[108:111],a[184:187],v[208:211],v[108:111]
global_load_lds_dwordx4 v4,s[10:11] offset:2048
v_exp_f32 v74,v74
v_exp_f32 v75,v75
v_exp_f32 v76,v76
v_exp_f32 v77,v77
v_exp_f32 v78,v78
v_exp_f32 v79,v79
v_mfma_f32_16x16x32_bf16 v[108:111],a[180:183],v[200:203],v[108:111]
ds_read_b64_tr_b16 a[222:223],v3 offset:12544
v_cvt_pk_bf16_f32 v72,v72,v73
v_cvt_pk_bf16_f32 v73,v74,v75
v_cvt_pk_bf16_f32 v74,v76,v77
v_cvt_pk_bf16_f32 v75,v78,v79
v_mfma_f32_16x16x32_bf16 v[108:111],a[176:179],v[192:195],v[108:111]
s_waitcnt lgkmcnt(3)
ds_read_b64_tr_b16 a[224:225],v2 offset:512
ds_read_b64_tr_b16 a[226:227],v2 offset:4608
v_mfma_f32_16x16x32_bf16 a[0:3],a[192:195],v[64:67],a[0:3]
ds_read_b64_tr_b16 a[228:229],v2 offset:8704
v_mul_f32 v14,v14,v25
v_mfma_f32_16x16x32_bf16 a[0:3],a[196:199],v[72:75],a[0:3]
ds_read_b64_tr_b16 a[230:231],v2 offset:12800
v_mul_f32 v15,v15,v25
v_mfma_f32_16x16x32_bf16 a[4:7],a[200:203],v[64:67],a[4:7]
ds_read_b64_tr_b16 a[232:233],v3 offset:512
v_cvt_pk_bf16_f32 v8,v8,v9
v_mfma_f32_16x16x32_bf16 a[4:7],a[204:207],v[72:75],a[4:7]
ds_read_b64_tr_b16 a[234:235],v3 offset:4608
ds_read_b64_tr_b16 a[236:237],v3 offset:8704
v_mfma_f32_16x16x32_bf16 a[8:11],a[208:211],v[64:67],a[8:11]
ds_read_b64_tr_b16 a[238:239],v3 offset:12800
v_cvt_pk_bf16_f32 v9,v10,v11
v_mfma_f32_16x16x32_bf16 a[8:11],a[212:215],v[72:75],a[8:11]
s_waitcnt lgkmcnt(9)
ds_read_b64_tr_b16 a[240:241],v2 offset:768
v_cvt_pk_bf16_f32 v10,v12,v13
v_mfma_f32_16x16x32_bf16 a[12:15],a[216:219],v[64:67],a[12:15]
s_waitcnt lgkmcnt(9)
ds_read_b64_tr_b16 a[242:243],v2 offset:4864
v_cvt_pk_bf16_f32 v11,v14,v15
v_mfma_f32_16x16x32_bf16 a[12:15],a[220:223],v[72:75],a[12:15]
s_waitcnt lgkmcnt(8)
ds_read_b64_tr_b16 a[244:245],v2 offset:8960
ds_read_b64_tr_b16 a[246:247],v2 offset:13056
v_mfma_f32_16x16x32_bf16 a[16:19],a[224:227],v[64:67],a[16:19]
s_waitcnt lgkmcnt(8)
ds_read_b64_tr_b16 a[248:249],v3 offset:768
v_mul_f32 v16,v16,v25
v_mfma_f32_16x16x32_bf16 a[16:19],a[228:231],v[72:75],a[16:19]
s_waitcnt lgkmcnt(1)
ds_read_b64_tr_b16 a[250:251],v3 offset:4864
v_mul_f32 v17,v17,v25
v_mfma_f32_16x16x32_bf16 a[20:23],a[232:235],v[64:67],a[20:23]
s_getreg_b32 s92,hwreg(HW_REG_IB_STS,0,4)
s_min_u32 s4,s92,s6
ds_read_b64_tr_b16 a[252:253],v3 offset:8960
v_mfma_f32_16x16x32_bf16 a[20:23],a[236:239],v[72:75],a[20:23]
s_mov_b32 s6,s92
ds_read_b64_tr_b16 a[254:255],v3 offset:13056
v_mul_f32 v18,v18,v25
v_mfma_f32_16x16x32_bf16 a[24:27],a[240:243],v[64:67],a[24:27]
s_cmp_gt_u32 s4,s9
s_cselect_b32 s4,15,s5
v_mul_f32 v19,v19,v25
v_mfma_f32_16x16x32_bf16 a[24:27],a[244:247],v[72:75],a[24:27]
s_waitcnt lgkmcnt(2)
v_mul_f32 v20,v20,v25
v_mul_f32 v21,v21,v25
v_mfma_f32_16x16x32_bf16 a[28:31],a[248:251],v[64:67],a[28:31]
s_waitcnt lgkmcnt(0)
v_mul_f32 v22,v22,v25
v_mul_f32 v23,v23,v25
v_mfma_f32_16x16x32_bf16 a[28:31],a[252:255],v[72:75],a[28:31]
s_getpc_b64 s[92:93]
)A"
R"A(s_cmp_lg_u32 s4,0
s_cbranch_scc1 L_pace_%=
v_mfma_f32_16x16x32_bf16 v[48:51],v[28:31],v[64:67],v[48:51]
v_cvt_pk_bf16_f32 v12,v16,v17
v_cvt_pk_bf16_f32 v13,v18,v19
v_mfma_f32_16x16x32_bf16 v[48:51],v[28:31],v[72:75],v[48:51]
s_add_u32 s10,s24,s74
s_addc_u32 s11,s25,0
v_cvt_pk_bf16_f32 v14,v20,v21
s_waitcnt vmcnt(15)
s_barrier
v_mfma_f32_16x16x32_bf16 v[112:115],a[128:131],v[224:227],v[44:47]
s_add_u32 m0,s76,16384
v_cvt_pk_bf16_f32 v15,v22,v23
v_accvgpr_read_b32 v16,a112
v_mfma_f32_16x16x32_bf16 v[112:115],a[132:135],v[232:235],v[112:115]
s_add_u32 s74,s74,16384
v_accvgpr_read_b32 v17,a113
v_accvgpr_read_b32 v18,a114
v_mfma_f32_16x16x32_bf16 v[112:115],a[136:139],v[240:243],v[112:115]
global_load_lds_dwordx4 v5,s[10:11] offset:3072
v_accvgpr_read_b32 v19,a115
v_mfma_f32_16x16x32_bf16 v[112:115],a[140:143],v[248:251],v[112:115]
s_and_b32 s74,s74,0x3ffff
v_accvgpr_read_b32 v20,a116
v_accvgpr_read_b32 v21,a117
v_mfma_f32_16x16x32_bf16 v[116:119],a[156:159],v[248:251],v[44:47]
s_or_b32 s74,s74,s69
v_accvgpr_read_b32 v22,a118
v_accvgpr_read_b32 v23,a119
v_mfma_f32_16x16x32_bf16 v[116:119],a[152:155],v[240:243],v[116:119]
s_sub_u32 s76,s76,32768
v_mul_f32 v16,v16,v27
v_mul_f32 v17,v17,v27
v_mfma_f32_16x16x32_bf16 v[116:119],a[148:151],v[232:235],v[116:119]
s_and_b32 s76,s76,0x1ffff
v_mul_f32 v18,v18,v27
v_mul_f32 v19,v19,v27
v_mfma_f32_16x16x32_bf16 v[116:119],a[144:147],v[224:227],v[116:119]
s_and_b32 s69,s75,0xfffc0000
v_mul_f32 v20,v20,v27
v_mul_f32 v21,v21,v27
v_mfma_f32_16x16x32_bf16 v[120:123],a[160:163],v[224:227],v[44:47]
s_add_u32 s75,s75,16384
v_mul_f32 v22,v22,v27
v_mul_f32 v23,v23,v27
v_mfma_f32_16x16x32_bf16 v[120:123],a[164:167],v[232:235],v[120:123]
v_cvt_pk_bf16_f32 v16,v16,v17
v_cvt_pk_bf16_f32 v17,v18,v19
v_mfma_f32_16x16x32_bf16 v[120:123],a[168:171],v[240:243],v[120:123]
v_cvt_pk_bf16_f32 v18,v20,v21
v_cvt_pk_bf16_f32 v19,v22,v23
v_mfma_f32_16x16x32_bf16 v[120:123],a[172:175],v[248:251],v[120:123]
v_accvgpr_write_b32 a112,v16
v_accvgpr_write_b32 a113,v17
v_mfma_f32_16x16x32_bf16 v[124:127],a[188:191],v[248:251],v[44:47]
v_accvgpr_write_b32 a114,v18
v_accvgpr_write_b32 a115,v19
v_accvgpr_write_b32 a116,v8
v_accvgpr_write_b32 a117,v9
v_accvgpr_write_b32 a118,v10
v_accvgpr_write_b32 a119,v11
v_exp_f32 v80,v80
v_exp_f32 v81,v81
v_exp_f32 v82,v82
v_exp_f32 v83,v83
v_exp_f32 v84,v84
v_exp_f32 v85,v85
v_exp_f32 v86,v86
v_exp_f32 v87,v87
v_mfma_f32_16x16x32_bf16 v[124:127],a[184:187],v[240:243],v[124:127]
v_cvt_pk_bf16_f32 v80,v80,v81
v_cvt_pk_bf16_f32 v81,v82,v83
v_cvt_pk_bf16_f32 v82,v84,v85
v_cvt_pk_bf16_f32 v83,v86,v87
v_exp_f32 v88,v88
v_exp_f32 v89,v89
v_exp_f32 v90,v90
v_exp_f32 v91,v91
v_exp_f32 v92,v92
v_exp_f32 v93,v93
v_exp_f32 v94,v94
v_exp_f32 v95,v95
v_mfma_f32_16x16x32_bf16 v[124:127],a[180:183],v[232:235],v[124:127]
v_cvt_pk_bf16_f32 v88,v88,v89
v_cvt_pk_bf16_f32 v89,v90,v91
v_cvt_pk_bf16_f32 v90,v92,v93
v_cvt_pk_bf16_f32 v91,v94,v95
v_mfma_f32_16x16x32_bf16 v[124:127],a[176:179],v[224:227],v[124:127]
s_add_u32 s10,s20,s74
s_addc_u32 s11,s21,0
s_add_u32 m0,s76,0
v_mfma_f32_16x16x32_bf16 v[132:135],a[192:195],v[80:83],v[132:135]
global_load_lds_dwordx4 v4,s[10:11] nt
v_add_u32 v1,s78,v1
v_mfma_f32_16x16x32_bf16 v[132:135],a[196:199],v[88:91],v[132:135]
ds_read_b128 a[128:131],v1
v_accvgpr_read_b32 v16,a120
v_mfma_f32_16x16x32_bf16 v[140:143],a[200:203],v[80:83],v[140:143]
v_exp_f32 v96,v96
v_mfma_f32_16x16x32_bf16 v[140:143],a[204:207],v[88:91],v[140:143]
ds_read_b128 a[132:135],v1 offset:256
v_accvgpr_read_b32 v17,a121
v_mfma_f32_16x16x32_bf16 v[148:151],a[208:211],v[80:83],v[148:151]
v_exp_f32 v97,v97
v_mfma_f32_16x16x32_bf16 v[148:151],a[212:215],v[88:91],v[148:151]
ds_read_b128 a[136:139],v1 offset:512
v_cvt_pk_bf16_f32 v96,v96,v97
v_mfma_f32_16x16x32_bf16 v[156:159],a[216:219],v[80:83],v[156:159]
v_exp_f32 v98,v98
v_mfma_f32_16x16x32_bf16 v[156:159],a[220:223],v[88:91],v[156:159]
ds_read_b128 a[140:143],v1 offset:768
v_accvgpr_read_b32 v18,a122
v_mfma_f32_16x16x32_bf16 v[164:167],a[224:227],v[80:83],v[164:167]
v_exp_f32 v99,v99
v_mfma_f32_16x16x32_bf16 v[164:167],a[228:231],v[88:91],v[164:167]
ds_read_b128 a[156:159],v1 offset:4864
v_cvt_pk_bf16_f32 v97,v98,v99
v_mfma_f32_16x16x32_bf16 v[172:175],a[232:235],v[80:83],v[172:175]
v_exp_f32 v100,v100
v_mfma_f32_16x16x32_bf16 v[172:175],a[236:239],v[88:91],v[172:175]
ds_read_b128 a[152:155],v1 offset:4608
v_accvgpr_read_b32 v19,a123
v_mfma_f32_16x16x32_bf16 v[180:183],a[240:243],v[80:83],v[180:183]
v_exp_f32 v101,v101
v_mfma_f32_16x16x32_bf16 v[180:183],a[244:247],v[88:91],v[180:183]
ds_read_b128 a[148:151],v1 offset:4352
v_cvt_pk_bf16_f32 v98,v100,v101
v_mfma_f32_16x16x32_bf16 v[188:191],a[248:251],v[80:83],v[188:191]
global_load_lds_dwordx4 v4,s[10:11] offset:1024 nt
v_accvgpr_read_b32 v20,a124
v_mfma_f32_16x16x32_bf16 v[188:191],a[252:255],v[88:91],v[188:191]
ds_read_b128 a[144:147],v1 offset:4096
v_accvgpr_read_b32 v21,a125
v_mfma_f32_16x16x32_bf16 v[52:55],v[28:31],v[80:83],v[52:55]
v_exp_f32 v102,v102
v_mfma_f32_16x16x32_bf16 v[52:55],v[28:31],v[88:91],v[52:55]
s_waitcnt lgkmcnt(3)
ds_read_b128 a[160:163],v1 offset:8192
v_accvgpr_read_b32 v22,a126
v_mfma_f32_16x16x32_bf16 v[64:67],a[128:131],v[128:131],v[32:35]
v_exp_f32 v103,v103
v_mfma_f32_16x16x32_bf16 v[64:67],a[132:135],v[136:139],v[64:67]
ds_read_b128 a[164:167],v1 offset:8448
v_cvt_pk_bf16_f32 v99,v102,v103
v_mfma_f32_16x16x32_bf16 v[64:67],a[136:139],v[144:147],v[64:67]
v_exp_f32 v104,v104
v_mfma_f32_16x16x32_bf16 v[64:67],a[140:143],v[152:155],v[64:67]
ds_read_b128 a[168:171],v1 offset:8704
v_accvgpr_read_b32 v23,a127
v_mfma_f32_16x16x32_bf16 v[68:71],a[156:159],v[152:155],v[32:35]
s_waitcnt lgkmcnt(4)
v_exp_f32 v105,v105
v_mfma_f32_16x16x32_bf16 v[68:71],a[152:155],v[144:147],v[68:71]
ds_read_b128 a[172:175],v1 offset:8960
v_cvt_pk_bf16_f32 v104,v104,v105
v_mfma_f32_16x16x32_bf16 v[68:71],a[148:151],v[136:139],v[68:71]
s_waitcnt lgkmcnt(4)
v_exp_f32 v106,v106
v_mfma_f32_16x16x32_bf16 v[68:71],a[144:147],v[128:131],v[68:71]
s_waitcnt lgkmcnt(3)
ds_read_b128 a[188:191],v1 offset:13056
v_mul_f32 v16,v16,v27
v_mfma_f32_16x16x32_bf16 v[72:75],a[160:163],v[128:131],v[32:35]
s_waitcnt lgkmcnt(3)
v_exp_f32 v107,v107
v_mfma_f32_16x16x32_bf16 v[72:75],a[164:167],v[136:139],v[72:75]
s_waitcnt lgkmcnt(1)
ds_read_b128 a[184:187],v1 offset:12800
v_cvt_pk_bf16_f32 v105,v106,v107
v_mfma_f32_16x16x32_bf16 v[72:75],a[168:171],v[144:147],v[72:75]
global_load_lds_dwordx4 v5,s[10:11] offset:2048 nt
ds_read_b128 a[180:183],v1 offset:12544
v_mfma_f32_16x16x32_bf16 v[72:75],a[172:175],v[152:155],v[72:75]
s_waitcnt lgkmcnt(2)
ds_read_b128 a[176:179],v1 offset:12288
v_mul_f32 v17,v17,v27
v_mfma_f32_16x16x32_bf16 v[76:79],a[188:191],v[152:155],v[32:35]
s_waitcnt lgkmcnt(2)
v_exp_f32 v108,v108
v_mfma_f32_16x16x32_bf16 v[76:79],a[184:187],v[144:147],v[76:79]
s_waitcnt lgkmcnt(1)
v_exp_f32 v109,v109
v_exp_f32 v110,v110
v_exp_f32 v111,v111
v_mfma_f32_16x16x32_bf16 v[76:79],a[180:183],v[136:139],v[76:79]
s_waitcnt lgkmcnt(0)
v_cvt_pk_bf16_f32 v106,v108,v109
v_cvt_pk_bf16_f32 v107,v110,v111
v_mfma_f32_16x16x32_bf16 v[76:79],a[176:179],v[128:131],v[76:79]
v_mul_f32 v18,v18,v27
v_mul_f32 v19,v19,v27
v_mfma_f32_16x16x32_bf16 v[196:199],a[192:195],v[96:99],v[196:199]
v_mul_f32 v20,v20,v27
v_mul_f32 v21,v21,v27
v_mfma_f32_16x16x32_bf16 v[196:199],a[196:199],v[104:107],v[196:199]
v_mul_f32 v22,v22,v27
v_mul_f32 v23,v23,v27
v_mfma_f32_16x16x32_bf16 v[204:207],a[200:203],v[96:99],v[204:207]
v_cvt_pk_bf16_f32 v16,v16,v17
v_cvt_pk_bf16_f32 v17,v18,v19
v_mfma_f32_16x16x32_bf16 v[204:207],a[204:207],v[104:107],v[204:207]
v_cvt_pk_bf16_f32 v18,v20,v21
v_cvt_pk_bf16_f32 v19,v22,v23
v_mfma_f32_16x16x32_bf16 v[212:215],a[208:211],v[96:99],v[212:215]
v_accvgpr_write_b32 a120,v16
v_accvgpr_write_b32 a121,v17
v_mfma_f32_16x16x32_bf16 v[212:215],a[212:215],v[104:107],v[212:215]
v_accvgpr_write_b32 a122,v18
v_accvgpr_write_b32 a123,v19
v_mfma_f32_16x16x32_bf16 v[220:223],a[216:219],v[96:99],v[220:223]
v_accvgpr_write_b32 a124,v12
v_accvgpr_write_b32 a125,v13
v_mfma_f32_16x16x32_bf16 v[220:223],a[220:223],v[104:107],v[220:223]
v_accvgpr_write_b32 a126,v14
v_accvgpr_write_b32 a127,v15
v_mfma_f32_16x16x32_bf16 v[228:231],a[224:227],v[96:99],v[228:231]
global_load_lds_dwordx4 v5,s[10:11] offset:3072
v_add_u32 v2,s78,v2
v_mfma_f32_16x16x32_bf16 v[228:231],a[228:231],v[104:107],v[228:231]
v_exp_f32 v112,v112
v_mfma_f32_16x16x32_bf16 v[236:239],a[232:235],v[96:99],v[236:239]
v_exp_f32 v113,v113
v_mfma_f32_16x16x32_bf16 v[236:239],a[236:239],v[104:107],v[236:239]
v_exp_f32 v114,v114
v_mfma_f32_16x16x32_bf16 v[244:247],a[240:243],v[96:99],v[244:247]
v_exp_f32 v115,v115
v_mfma_f32_16x16x32_bf16 v[244:247],a[244:247],v[104:107],v[244:247]
v_exp_f32 v116,v116
v_mfma_f32_16x16x32_bf16 v[252:255],a[248:251],v[96:99],v[252:255]
v_exp_f32 v117,v117
v_mfma_f32_16x16x32_bf16 v[252:255],a[252:255],v[104:107],v[252:255]
v_exp_f32 v118,v118
v_mfma_f32_16x16x32_bf16 v[56:59],v[28:31],v[96:99],v[56:59]
v_exp_f32 v119,v119
v_mfma_f32_16x16x32_bf16 v[56:59],v[28:31],v[104:107],v[56:59]
v_cvt_pk_bf16_f32 v112,v112,v113
v_cvt_pk_bf16_f32 v113,v114,v115
v_mfma_f32_16x16x32_bf16 v[80:83],a[128:131],v[160:163],v[36:39]
v_cvt_pk_bf16_f32 v114,v116,v117
v_cvt_pk_bf16_f32 v115,v118,v119
v_mfma_f32_16x16x32_bf16 v[80:83],a[132:135],v[168:171],v[80:83]
s_add_u32 s10,s24,s74
s_addc_u32 s11,s25,0
v_add_u32 v3,s78,v3
v_mfma_f32_16x16x32_bf16 v[80:83],a[136:139],v[176:179],v[80:83]
s_add_u32 s74,s74,16384
v_exp_f32 v120,v120
v_mfma_f32_16x16x32_bf16 v[80:83],a[140:143],v[184:187],v[80:83]
s_add_u32 m0,s76,16384
v_exp_f32 v121,v121
v_mfma_f32_16x16x32_bf16 v[84:87],a[156:159],v[184:187],v[36:39]
s_and_b32 s74,s74,0x3ffff
v_exp_f32 v122,v122
v_mfma_f32_16x16x32_bf16 v[84:87],a[152:155],v[176:179],v[84:87]
global_load_lds_dwordx4 v4,s[10:11]
v_cvt_pk_bf16_f32 v120,v120,v121
v_mfma_f32_16x16x32_bf16 v[84:87],a[148:151],v[168:171],v[84:87]
s_or_b32 s74,s74,s69
v_exp_f32 v123,v123
v_mfma_f32_16x16x32_bf16 v[84:87],a[144:147],v[160:163],v[84:87]
s_sub_u32 s76,s76,32768
v_exp_f32 v124,v124
v_mfma_f32_16x16x32_bf16 v[88:91],a[160:163],v[160:163],v[36:39]
s_and_b32 s76,s76,0x1ffff
v_exp_f32 v125,v125
v_mfma_f32_16x16x32_bf16 v[88:91],a[164:167],v[168:171],v[88:91]
s_sub_u32 s78,s77,32768
v_exp_f32 v126,v126
v_mfma_f32_16x16x32_bf16 v[88:91],a[168:171],v[176:179],v[88:91]
s_and_b32 s78,s78,0x1ffff
v_exp_f32 v127,v127
v_mfma_f32_16x16x32_bf16 v[88:91],a[172:175],v[184:187],v[88:91]
s_sub_u32 s78,s78,s77
v_cvt_pk_bf16_f32 v121,v122,v123
v_cvt_pk_bf16_f32 v122,v124,v125
v_mfma_f32_16x16x32_bf16 v[92:95],a[188:191],v[184:187],v[36:39]
s_and_b32 s69,s75,0xfffc0000
v_cvt_pk_bf16_f32 v123,v126,v127
v_accvgpr_read_b32 v12,a100
v_mfma_f32_16x16x32_bf16 v[92:95],a[184:187],v[176:179],v[92:95]
s_add_u32 s77,s77,s78
v_exp_f32 v64,v64
v_mfma_f32_16x16x32_bf16 v[92:95],a[180:183],v[168:171],v[92:95]
s_add_u32 s75,s75,16384
v_exp_f32 v65,v65
v_mfma_f32_16x16x32_bf16 v[92:95],a[176:179],v[160:163],v[92:95]
v_exp_f32 v66,v66
v_mfma_f32_16x16x32_bf16 a[32:35],a[192:195],v[112:115],a[32:35]
ds_read_b64_tr_b16 a[192:193],v2
ds_read_b64_tr_b16 a[194:195],v2 offset:4096
v_mfma_f32_16x16x32_bf16 a[32:35],a[196:199],v[120:123],a[32:35]
ds_read_b64_tr_b16 a[196:197],v2 offset:8192
v_cvt_pk_bf16_f32 v64,v64,v65
v_mfma_f32_16x16x32_bf16 a[36:39],a[200:203],v[112:115],a[36:39]
ds_read_b64_tr_b16 a[198:199],v2 offset:12288
v_accvgpr_read_b32 v13,a101
v_mfma_f32_16x16x32_bf16 a[36:39],a[204:207],v[120:123],a[36:39]
global_load_lds_dwordx4 v5,s[10:11] offset:1024
ds_read_b64_tr_b16 a[200:201],v3
v_mfma_f32_16x16x32_bf16 a[40:43],a[208:211],v[112:115],a[40:43]
ds_read_b64_tr_b16 a[202:203],v3 offset:4096
ds_read_b64_tr_b16 a[204:205],v3 offset:8192
v_mfma_f32_16x16x32_bf16 a[40:43],a[212:215],v[120:123],a[40:43]
ds_read_b64_tr_b16 a[206:207],v3 offset:12288
v_accvgpr_read_b32 v14,a102
v_mfma_f32_16x16x32_bf16 a[44:47],a[216:219],v[112:115],a[44:47]
ds_read_b64_tr_b16 a[208:209],v2 offset:256
v_accvgpr_read_b32 v15,a103
v_mfma_f32_16x16x32_bf16 a[44:47],a[220:223],v[120:123],a[44:47]
)A"
R"A(ds_read_b64_tr_b16 a[210:211],v2 offset:4352
v_accvgpr_read_b32 v8,a96
v_mfma_f32_16x16x32_bf16 a[48:51],a[224:227],v[112:115],a[48:51]
ds_read_b64_tr_b16 a[212:213],v2 offset:8448
ds_read_b64_tr_b16 a[214:215],v2 offset:12544
v_mfma_f32_16x16x32_bf16 a[48:51],a[228:231],v[120:123],a[48:51]
v_exp_f32 v67,v67
v_mfma_f32_16x16x32_bf16 a[52:55],a[232:235],v[112:115],a[52:55]
v_exp_f32 v68,v68
v_mfma_f32_16x16x32_bf16 a[52:55],a[236:239],v[120:123],a[52:55]
v_exp_f32 v69,v69
v_mfma_f32_16x16x32_bf16 a[56:59],a[240:243],v[112:115],a[56:59]
v_exp_f32 v70,v70
v_mfma_f32_16x16x32_bf16 a[56:59],a[244:247],v[120:123],a[56:59]
v_exp_f32 v71,v71
v_mfma_f32_16x16x32_bf16 a[60:63],a[248:251],v[112:115],a[60:63]
ds_read_b64_tr_b16 a[216:217],v3 offset:256
ds_read_b64_tr_b16 a[218:219],v3 offset:4352
v_mfma_f32_16x16x32_bf16 a[60:63],a[252:255],v[120:123],a[60:63]
v_cvt_pk_bf16_f32 v65,v66,v67
v_cvt_pk_bf16_f32 v66,v68,v69
v_mfma_f32_16x16x32_bf16 v[60:63],v[28:31],v[112:115],v[60:63]
ds_read_b64_tr_b16 a[220:221],v3 offset:8448
v_cvt_pk_bf16_f32 v67,v70,v71
v_mfma_f32_16x16x32_bf16 v[60:63],v[28:31],v[120:123],v[60:63]
global_load_lds_dwordx4 v4,s[10:11] offset:2048
ds_read_b64_tr_b16 a[222:223],v3 offset:12544
v_mfma_f32_16x16x32_bf16 v[96:99],a[128:131],v[192:195],v[40:43]
v_exp_f32 v72,v72
v_mfma_f32_16x16x32_bf16 v[96:99],a[132:135],v[200:203],v[96:99]
v_exp_f32 v73,v73
v_mfma_f32_16x16x32_bf16 v[96:99],a[136:139],v[208:211],v[96:99]
v_exp_f32 v74,v74
v_mfma_f32_16x16x32_bf16 v[96:99],a[140:143],v[216:219],v[96:99]
v_exp_f32 v75,v75
v_mfma_f32_16x16x32_bf16 v[100:103],a[156:159],v[216:219],v[40:43]
v_exp_f32 v76,v76
v_mfma_f32_16x16x32_bf16 v[100:103],a[152:155],v[208:211],v[100:103]
v_exp_f32 v77,v77
v_mfma_f32_16x16x32_bf16 v[100:103],a[148:151],v[200:203],v[100:103]
v_exp_f32 v78,v78
v_mfma_f32_16x16x32_bf16 v[100:103],a[144:147],v[192:195],v[100:103]
v_exp_f32 v79,v79
v_mfma_f32_16x16x32_bf16 v[104:107],a[160:163],v[192:195],v[40:43]
v_cvt_pk_bf16_f32 v72,v72,v73
v_cvt_pk_bf16_f32 v73,v74,v75
v_mfma_f32_16x16x32_bf16 v[104:107],a[164:167],v[200:203],v[104:107]
v_cvt_pk_bf16_f32 v74,v76,v77
v_cvt_pk_bf16_f32 v75,v78,v79
v_mfma_f32_16x16x32_bf16 v[104:107],a[168:171],v[208:211],v[104:107]
ds_read_b64_tr_b16 a[224:225],v2 offset:512
ds_read_b64_tr_b16 a[226:227],v2 offset:4608
v_mfma_f32_16x16x32_bf16 v[104:107],a[172:175],v[216:219],v[104:107]
v_accvgpr_read_b32 v9,a97
v_accvgpr_read_b32 v10,a98
v_mfma_f32_16x16x32_bf16 v[108:111],a[188:191],v[216:219],v[40:43]
v_accvgpr_read_b32 v11,a99
v_mul_f32 v8,v8,v27
v_mfma_f32_16x16x32_bf16 v[108:111],a[184:187],v[208:211],v[108:111]
global_load_lds_dwordx4 v5,s[10:11] offset:3072
v_mul_f32 v9,v9,v27
v_mfma_f32_16x16x32_bf16 v[108:111],a[180:183],v[200:203],v[108:111]
v_mul_f32 v10,v10,v27
v_mul_f32 v11,v11,v27
v_mfma_f32_16x16x32_bf16 v[108:111],a[176:179],v[192:195],v[108:111]
s_waitcnt lgkmcnt(2)
v_mul_f32 v12,v12,v27
v_mul_f32 v13,v13,v27
v_mul_f32 v14,v14,v27
v_mul_f32 v15,v15,v27
v_cvt_pk_bf16_f32 v8,v8,v9
v_cvt_pk_bf16_f32 v9,v10,v11
v_cvt_pk_bf16_f32 v10,v12,v13
v_cvt_pk_bf16_f32 v11,v14,v15
v_accvgpr_write_b32 a96,v8
v_accvgpr_write_b32 a97,v9
v_accvgpr_write_b32 a98,v10
v_accvgpr_write_b32 a99,v11
v_mfma_f32_16x16x32_bf16 a[0:3],a[192:195],v[64:67],a[0:3]
ds_read_b128 a[100:103],v7 offset:2048
v_add_u32 v1,s78,v1
v_mfma_f32_16x16x32_bf16 a[0:3],a[196:199],v[72:75],a[0:3]
ds_read_b64_tr_b16 a[228:229],v2 offset:8704
v_accvgpr_read_b32 v20,a108
v_mfma_f32_16x16x32_bf16 a[4:7],a[200:203],v[64:67],a[4:7]
ds_read_b64_tr_b16 a[230:231],v2 offset:12800
v_accvgpr_read_b32 v21,a109
v_mfma_f32_16x16x32_bf16 a[4:7],a[204:207],v[72:75],a[4:7]
ds_read_b64_tr_b16 a[232:233],v3 offset:512
v_accvgpr_read_b32 v22,a110
v_mfma_f32_16x16x32_bf16 a[8:11],a[208:211],v[64:67],a[8:11]
ds_read_b64_tr_b16 a[234:235],v3 offset:4608
ds_read_b64_tr_b16 a[236:237],v3 offset:8704
v_mfma_f32_16x16x32_bf16 a[8:11],a[212:215],v[72:75],a[8:11]
ds_read_b64_tr_b16 a[238:239],v3 offset:12800
v_accvgpr_read_b32 v23,a111
v_mfma_f32_16x16x32_bf16 a[12:15],a[216:219],v[64:67],a[12:15]
ds_read_b64_tr_b16 a[240:241],v2 offset:768
v_accvgpr_read_b32 v16,a104
v_mfma_f32_16x16x32_bf16 a[12:15],a[220:223],v[72:75],a[12:15]
s_waitcnt lgkmcnt(8)
ds_read_b64_tr_b16 a[242:243],v2 offset:4864
v_accvgpr_read_b32 v17,a105
v_mfma_f32_16x16x32_bf16 a[16:19],a[224:227],v[64:67],a[16:19]
s_waitcnt lgkmcnt(6)
ds_read_b64_tr_b16 a[244:245],v2 offset:8960
ds_read_b64_tr_b16 a[246:247],v2 offset:13056
v_mfma_f32_16x16x32_bf16 a[16:19],a[228:231],v[72:75],a[16:19]
s_waitcnt lgkmcnt(0)
ds_read_b64_tr_b16 a[248:249],v3 offset:768
v_accvgpr_read_b32 v18,a106
v_mfma_f32_16x16x32_bf16 a[20:23],a[232:235],v[64:67],a[20:23]
s_getreg_b32 s92,hwreg(HW_REG_IB_STS,0,4)
s_min_u32 s4,s92,s6
ds_read_b64_tr_b16 a[250:251],v3 offset:4864
v_mfma_f32_16x16x32_bf16 a[20:23],a[236:239],v[72:75],a[20:23]
s_mov_b32 s6,s92
ds_read_b64_tr_b16 a[252:253],v3 offset:8960
ds_read_b64_tr_b16 a[254:255],v3 offset:13056
v_mfma_f32_16x16x32_bf16 a[24:27],a[240:243],v[64:67],a[24:27]
s_cmp_gt_u32 s4,s9
s_cselect_b32 s4,15,s5
v_accvgpr_read_b32 v19,a107
v_mfma_f32_16x16x32_bf16 a[24:27],a[244:247],v[72:75],a[24:27]
s_waitcnt lgkmcnt(2)
v_exp_f32 v80,v80
v_mfma_f32_16x16x32_bf16 a[28:31],a[248:251],v[64:67],a[28:31]
s_waitcnt lgkmcnt(0)
v_exp_f32 v81,v81
v_mfma_f32_16x16x32_bf16 a[28:31],a[252:255],v[72:75],a[28:31]
s_getpc_b64 s[92:93]
s_cmp_lg_u32 s4,0
s_cbranch_scc1 L_pace_%=
v_mfma_f32_16x16x32_bf16 v[48:51],v[28:31],v[64:67],v[48:51]
v_exp_f32 v82,v82
v_mfma_f32_16x16x32_bf16 v[48:51],v[28:31],v[72:75],v[48:51]
s_add_u32 s10,s20,s74
s_addc_u32 s11,s21,0
v_cvt_pk_bf16_f32 v80,v80,v81
s_waitcnt vmcnt(16)
s_barrier
v_mfma_f32_16x16x32_bf16 v[112:115],a[128:131],v[224:227],v[44:47]
s_add_u32 m0,s76,0
v_exp_f32 v83,v83
v_mfma_f32_16x16x32_bf16 v[112:115],a[132:135],v[232:235],v[112:115]
v_exp_f32 v84,v84
v_mfma_f32_16x16x32_bf16 v[112:115],a[136:139],v[240:243],v[112:115]
global_load_lds_dwordx4 v4,s[10:11] nt
v_cvt_pk_bf16_f32 v81,v82,v83
v_mfma_f32_16x16x32_bf16 v[112:115],a[140:143],v[248:251],v[112:115]
v_exp_f32 v85,v85
v_mfma_f32_16x16x32_bf16 v[116:119],a[156:159],v[248:251],v[44:47]
v_exp_f32 v86,v86
v_mfma_f32_16x16x32_bf16 v[116:119],a[152:155],v[240:243],v[116:119]
v_exp_f32 v87,v87
v_mfma_f32_16x16x32_bf16 v[116:119],a[148:151],v[232:235],v[116:119]
v_cvt_pk_bf16_f32 v82,v84,v85
v_cvt_pk_bf16_f32 v83,v86,v87
v_mfma_f32_16x16x32_bf16 v[116:119],a[144:147],v[224:227],v[116:119]
v_exp_f32 v88,v88
v_mfma_f32_16x16x32_bf16 v[120:123],a[160:163],v[224:227],v[44:47]
v_exp_f32 v89,v89
v_mfma_f32_16x16x32_bf16 v[120:123],a[164:167],v[232:235],v[120:123]
v_exp_f32 v90,v90
v_mfma_f32_16x16x32_bf16 v[120:123],a[168:171],v[240:243],v[120:123]
v_exp_f32 v91,v91
v_mfma_f32_16x16x32_bf16 v[120:123],a[172:175],v[248:251],v[120:123]
v_exp_f32 v92,v92
v_mfma_f32_16x16x32_bf16 v[124:127],a[188:191],v[248:251],v[44:47]
v_exp_f32 v93,v93
v_mfma_f32_16x16x32_bf16 v[124:127],a[184:187],v[240:243],v[124:127]
v_exp_f32 v94,v94
v_exp_f32 v95,v95
v_mfma_f32_16x16x32_bf16 v[124:127],a[180:183],v[232:235],v[124:127]
v_cvt_pk_bf16_f32 v88,v88,v89
v_cvt_pk_bf16_f32 v89,v90,v91
v_cvt_pk_bf16_f32 v90,v92,v93
v_cvt_pk_bf16_f32 v91,v94,v95
v_mfma_f32_16x16x32_bf16 v[124:127],a[176:179],v[224:227],v[124:127]
ds_read_b128 a[128:131],v1
v_mul_f32 v16,v16,v27
v_mfma_f32_16x16x32_bf16 v[132:135],a[192:195],v[80:83],v[132:135]
v_mul_f32 v17,v17,v27
v_mul_f32 v18,v18,v27
v_mfma_f32_16x16x32_bf16 v[132:135],a[196:199],v[88:91],v[132:135]
ds_read_b128 a[132:135],v1 offset:256
v_mul_f32 v19,v19,v27
v_mfma_f32_16x16x32_bf16 v[140:143],a[200:203],v[80:83],v[140:143]
global_load_lds_dwordx4 v4,s[10:11] offset:1024 nt
v_mul_f32 v20,v20,v27
v_mfma_f32_16x16x32_bf16 v[140:143],a[204:207],v[88:91],v[140:143]
ds_read_b128 a[136:139],v1 offset:512
v_mul_f32 v21,v21,v27
v_mfma_f32_16x16x32_bf16 v[148:151],a[208:211],v[80:83],v[148:151]
v_mul_f32 v22,v22,v27
v_mul_f32 v23,v23,v27
v_mfma_f32_16x16x32_bf16 v[148:151],a[212:215],v[88:91],v[148:151]
ds_read_b128 a[140:143],v1 offset:768
v_cvt_pk_bf16_f32 v16,v16,v17
v_mfma_f32_16x16x32_bf16 v[156:159],a[216:219],v[80:83],v[156:159]
v_cvt_pk_bf16_f32 v17,v18,v19
v_cvt_pk_bf16_f32 v18,v20,v21
v_mfma_f32_16x16x32_bf16 v[156:159],a[220:223],v[88:91],v[156:159]
ds_read_b128 a[156:159],v1 offset:4864
v_cvt_pk_bf16_f32 v19,v22,v23
v_mfma_f32_16x16x32_bf16 v[164:167],a[224:227],v[80:83],v[164:167]
v_accvgpr_write_b32 a104,v16
v_accvgpr_write_b32 a105,v17
v_mfma_f32_16x16x32_bf16 v[164:167],a[228:231],v[88:91],v[164:167]
L_pd9_%=:
s_cbranch_execz L_pd13_%=
ds_read_b128 a[152:155],v1 offset:4608
v_accvgpr_write_b32 a106,v18
v_mfma_f32_16x16x32_bf16 v[172:175],a[232:235],v[80:83],v[172:175]
v_accvgpr_write_b32 a107,v19
v_accvgpr_read_b32 v12,a68
v_mfma_f32_16x16x32_bf16 v[172:175],a[236:239],v[88:91],v[172:175]
ds_read_b128 a[148:151],v1 offset:4352
v_accvgpr_read_b32 v13,a69
v_mfma_f32_16x16x32_bf16 v[180:183],a[240:243],v[80:83],v[180:183]
v_exp_f32 v96,v96
v_mfma_f32_16x16x32_bf16 v[180:183],a[244:247],v[88:91],v[180:183]
ds_read_b128 a[144:147],v1 offset:4096
v_accvgpr_read_b32 v14,a70
v_mfma_f32_16x16x32_bf16 v[188:191],a[248:251],v[80:83],v[188:191]
v_exp_f32 v97,v97
v_mfma_f32_16x16x32_bf16 v[188:191],a[252:255],v[88:91],v[188:191]
ds_read_b128 a[160:163],v1 offset:8192
v_cvt_pk_bf16_f32 v96,v96,v97
v_mfma_f32_16x16x32_bf16 v[52:55],v[28:31],v[80:83],v[52:55]
v_exp_f32 v98,v98
v_mfma_f32_16x16x32_bf16 v[52:55],v[28:31],v[88:91],v[52:55]
s_waitcnt lgkmcnt(3)
ds_read_b128 a[164:167],v1 offset:8448
v_accvgpr_read_b32 v15,a71
v_mfma_f32_16x16x32_bf16 v[64:67],a[128:131],v[128:131],v[32:35]
global_load_lds_dwordx4 v5,s[10:11] offset:2048 nt
v_accvgpr_read_b32 v8,a64
v_mfma_f32_16x16x32_bf16 v[64:67],a[132:135],v[136:139],v[64:67]
ds_read_b128 a[168:171],v1 offset:8704
v_accvgpr_read_b32 v9,a65
v_mfma_f32_16x16x32_bf16 v[64:67],a[136:139],v[144:147],v[64:67]
v_exp_f32 v99,v99
v_mfma_f32_16x16x32_bf16 v[64:67],a[140:143],v[152:155],v[64:67]
ds_read_b128 a[108:111],v7 offset:3072
v_cvt_pk_bf16_f32 v97,v98,v99
v_mfma_f32_16x16x32_bf16 v[68:71],a[156:159],v[152:155],v[32:35]
v_exp_f32 v100,v100
v_mfma_f32_16x16x32_bf16 v[68:71],a[152:155],v[144:147],v[68:71]
s_waitcnt lgkmcnt(3)
ds_read_b128 a[172:175],v1 offset:8960
v_accvgpr_read_b32 v10,a66
v_mfma_f32_16x16x32_bf16 v[68:71],a[148:151],v[136:139],v[68:71]
v_exp_f32 v101,v101
v_mfma_f32_16x16x32_bf16 v[68:71],a[144:147],v[128:131],v[68:71]
ds_read_b128 a[188:191],v1 offset:13056
v_cvt_pk_bf16_f32 v98,v100,v101
v_mfma_f32_16x16x32_bf16 v[72:75],a[160:163],v[128:131],v[32:35]
s_waitcnt lgkmcnt(4)
v_exp_f32 v102,v102
v_mfma_f32_16x16x32_bf16 v[72:75],a[164:167],v[136:139],v[72:75]
s_waitcnt lgkmcnt(3)
ds_read_b128 a[184:187],v1 offset:12800
v_accvgpr_read_b32 v11,a67
v_mfma_f32_16x16x32_bf16 v[72:75],a[168:171],v[144:147],v[72:75]
s_waitcnt lgkmcnt(2)
ds_read_b128 a[180:183],v1 offset:12544
v_mul_f32 v8,v8,v26
v_mfma_f32_16x16x32_bf16 v[72:75],a[172:175],v[152:155],v[72:75]
s_waitcnt lgkmcnt(2)
ds_read_b128 a[176:179],v1 offset:12288
v_mul_f32 v9,v9,v26
v_mfma_f32_16x16x32_bf16 v[76:79],a[188:191],v[152:155],v[32:35]
s_waitcnt lgkmcnt(2)
v_exp_f32 v103,v103
v_mfma_f32_16x16x32_bf16 v[76:79],a[184:187],v[144:147],v[76:79]
s_waitcnt lgkmcnt(1)
v_cvt_pk_bf16_f32 v99,v102,v103
v_exp_f32 v104,v104
v_exp_f32 v105,v105
v_exp_f32 v106,v106
v_exp_f32 v107,v107
v_exp_f32 v108,v108
v_exp_f32 v109,v109
v_exp_f32 v110,v110
v_exp_f32 v111,v111
v_mfma_f32_16x16x32_bf16 v[76:79],a[180:183],v[136:139],v[76:79]
s_waitcnt lgkmcnt(0)
v_cvt_pk_bf16_f32 v104,v104,v105
v_cvt_pk_bf16_f32 v105,v106,v107
v_cvt_pk_bf16_f32 v106,v108,v109
v_cvt_pk_bf16_f32 v107,v110,v111
v_mfma_f32_16x16x32_bf16 v[76:79],a[176:179],v[128:131],v[76:79]
v_exp_f32 v112,v112
v_mfma_f32_16x16x32_bf16 v[196:199],a[192:195],v[96:99],v[196:199]
global_load_lds_dwordx4 v5,s[10:11] offset:3072
v_mul_f32 v10,v10,v26
v_mfma_f32_16x16x32_bf16 v[196:199],a[196:199],v[104:107],v[196:199]
v_exp_f32 v113,v113
v_mfma_f32_16x16x32_bf16 v[204:207],a[200:203],v[96:99],v[204:207]
)A"
R"A(v_exp_f32 v114,v114
v_mfma_f32_16x16x32_bf16 v[204:207],a[204:207],v[104:107],v[204:207]
v_exp_f32 v115,v115
v_mfma_f32_16x16x32_bf16 v[212:215],a[208:211],v[96:99],v[212:215]
v_exp_f32 v116,v116
v_mfma_f32_16x16x32_bf16 v[212:215],a[212:215],v[104:107],v[212:215]
v_exp_f32 v117,v117
v_mfma_f32_16x16x32_bf16 v[220:223],a[216:219],v[96:99],v[220:223]
v_exp_f32 v118,v118
v_mfma_f32_16x16x32_bf16 v[220:223],a[220:223],v[104:107],v[220:223]
v_exp_f32 v119,v119
v_mfma_f32_16x16x32_bf16 v[228:231],a[224:227],v[96:99],v[228:231]
v_cvt_pk_bf16_f32 v112,v112,v113
v_cvt_pk_bf16_f32 v113,v114,v115
v_mfma_f32_16x16x32_bf16 v[228:231],a[228:231],v[104:107],v[228:231]
v_cvt_pk_bf16_f32 v114,v116,v117
v_cvt_pk_bf16_f32 v115,v118,v119
v_mfma_f32_16x16x32_bf16 v[236:239],a[232:235],v[96:99],v[236:239]
v_exp_f32 v120,v120
v_mfma_f32_16x16x32_bf16 v[236:239],a[236:239],v[104:107],v[236:239]
v_exp_f32 v121,v121
v_mfma_f32_16x16x32_bf16 v[244:247],a[240:243],v[96:99],v[244:247]
s_add_u32 s10,s24,s74
s_addc_u32 s11,s25,0
v_cvt_pk_bf16_f32 v120,v120,v121
v_mfma_f32_16x16x32_bf16 v[244:247],a[244:247],v[104:107],v[244:247]
s_add_u32 s74,s74,16384
v_exp_f32 v122,v122
v_mfma_f32_16x16x32_bf16 v[252:255],a[248:251],v[96:99],v[252:255]
s_add_u32 m0,s76,16384
v_exp_f32 v123,v123
v_mfma_f32_16x16x32_bf16 v[252:255],a[252:255],v[104:107],v[252:255]
s_and_b32 s74,s74,0x3ffff
v_exp_f32 v124,v124
v_mfma_f32_16x16x32_bf16 v[56:59],v[28:31],v[96:99],v[56:59]
global_load_lds_dwordx4 v4,s[10:11]
v_cvt_pk_bf16_f32 v121,v122,v123
v_mfma_f32_16x16x32_bf16 v[56:59],v[28:31],v[104:107],v[56:59]
s_or_b32 s74,s74,s69
v_exp_f32 v125,v125
v_mfma_f32_16x16x32_bf16 v[80:83],a[128:131],v[160:163],v[36:39]
s_sub_u32 s76,s76,32768
v_exp_f32 v126,v126
v_mfma_f32_16x16x32_bf16 v[80:83],a[132:135],v[168:171],v[80:83]
s_and_b32 s76,s76,0x1ffff
v_exp_f32 v127,v127
v_mfma_f32_16x16x32_bf16 v[80:83],a[136:139],v[176:179],v[80:83]
s_and_b32 s69,s75,0xfffc0000
v_cvt_pk_bf16_f32 v122,v124,v125
v_cvt_pk_bf16_f32 v123,v126,v127
v_mfma_f32_16x16x32_bf16 v[80:83],a[140:143],v[184:187],v[80:83]
v_mul_f32 v11,v11,v26
v_mul_f32 v12,v12,v26
v_mfma_f32_16x16x32_bf16 v[84:87],a[156:159],v[184:187],v[36:39]
v_mul_f32 v13,v13,v26
v_mul_f32 v14,v14,v26
v_mfma_f32_16x16x32_bf16 v[84:87],a[152:155],v[176:179],v[84:87]
v_mul_f32 v15,v15,v26
v_cvt_pk_bf16_f32 v8,v8,v9
v_mfma_f32_16x16x32_bf16 v[84:87],a[148:151],v[168:171],v[84:87]
v_cvt_pk_bf16_f32 v9,v10,v11
v_cvt_pk_bf16_f32 v10,v12,v13
v_mfma_f32_16x16x32_bf16 v[84:87],a[144:147],v[160:163],v[84:87]
v_cvt_pk_bf16_f32 v11,v14,v15
v_accvgpr_write_b32 a64,v8
v_mfma_f32_16x16x32_bf16 v[88:91],a[160:163],v[160:163],v[36:39]
v_accvgpr_write_b32 a65,v9
v_accvgpr_write_b32 a66,v10
v_mfma_f32_16x16x32_bf16 v[88:91],a[164:167],v[168:171],v[88:91]
ds_read_b128 a[68:71],v7 offset:6144
v_accvgpr_write_b32 a67,v11
v_mfma_f32_16x16x32_bf16 v[88:91],a[168:171],v[176:179],v[88:91]
v_add_u32 v2,s78,v2
v_add_u32 v3,s78,v3
v_mfma_f32_16x16x32_bf16 v[88:91],a[172:175],v[184:187],v[88:91]
s_sub_u32 s78,s77,32768
v_exp_f32 v64,v64
v_mfma_f32_16x16x32_bf16 v[92:95],a[188:191],v[184:187],v[36:39]
s_and_b32 s78,s78,0x1ffff
v_exp_f32 v65,v65
v_mfma_f32_16x16x32_bf16 v[92:95],a[184:187],v[176:179],v[92:95]
s_sub_u32 s78,s78,s77
v_exp_f32 v66,v66
v_mfma_f32_16x16x32_bf16 v[92:95],a[180:183],v[168:171],v[92:95]
global_load_lds_dwordx4 v5,s[10:11] offset:1024
v_cvt_pk_bf16_f32 v64,v64,v65
v_mfma_f32_16x16x32_bf16 v[92:95],a[176:179],v[160:163],v[92:95]
s_add_u32 s77,s77,s78
v_exp_f32 v67,v67
v_mfma_f32_16x16x32_bf16 a[32:35],a[192:195],v[112:115],a[32:35]
ds_read_b64_tr_b16 a[192:193],v2
ds_read_b64_tr_b16 a[194:195],v2 offset:4096
v_mfma_f32_16x16x32_bf16 a[32:35],a[196:199],v[120:123],a[32:35]
ds_read_b64_tr_b16 a[196:197],v2 offset:8192
v_cvt_pk_bf16_f32 v65,v66,v67
v_mfma_f32_16x16x32_bf16 a[36:39],a[200:203],v[112:115],a[36:39]
ds_read_b64_tr_b16 a[198:199],v2 offset:12288
v_accvgpr_read_b32 v20,a76
v_mfma_f32_16x16x32_bf16 a[36:39],a[204:207],v[120:123],a[36:39]
ds_read_b64_tr_b16 a[200:201],v3
v_accvgpr_read_b32 v21,a77
v_mfma_f32_16x16x32_bf16 a[40:43],a[208:211],v[112:115],a[40:43]
ds_read_b64_tr_b16 a[202:203],v3 offset:4096
ds_read_b64_tr_b16 a[204:205],v3 offset:8192
v_mfma_f32_16x16x32_bf16 a[40:43],a[212:215],v[120:123],a[40:43]
ds_read_b64_tr_b16 a[206:207],v3 offset:12288
v_accvgpr_read_b32 v22,a78
v_mfma_f32_16x16x32_bf16 a[44:47],a[216:219],v[112:115],a[44:47]
ds_read_b64_tr_b16 a[208:209],v2 offset:256
v_accvgpr_read_b32 v23,a79
v_mfma_f32_16x16x32_bf16 a[44:47],a[220:223],v[120:123],a[44:47]
ds_read_b64_tr_b16 a[210:211],v2 offset:4352
v_accvgpr_read_b32 v16,a72
v_mfma_f32_16x16x32_bf16 a[48:51],a[224:227],v[112:115],a[48:51]
ds_read_b64_tr_b16 a[212:213],v2 offset:8448
ds_read_b64_tr_b16 a[214:215],v2 offset:12544
v_mfma_f32_16x16x32_bf16 a[48:51],a[228:231],v[120:123],a[48:51]
v_exp_f32 v68,v68
v_mfma_f32_16x16x32_bf16 a[52:55],a[232:235],v[112:115],a[52:55]
v_exp_f32 v69,v69
v_mfma_f32_16x16x32_bf16 a[52:55],a[236:239],v[120:123],a[52:55]
v_exp_f32 v70,v70
v_mfma_f32_16x16x32_bf16 a[56:59],a[240:243],v[112:115],a[56:59]
v_exp_f32 v71,v71
v_mfma_f32_16x16x32_bf16 a[56:59],a[244:247],v[120:123],a[56:59]
ds_read_b64_tr_b16 a[216:217],v3 offset:256
ds_read_b64_tr_b16 a[218:219],v3 offset:4352
v_mfma_f32_16x16x32_bf16 a[60:63],a[248:251],v[112:115],a[60:63]
global_load_lds_dwordx4 v4,s[10:11] offset:2048
v_cvt_pk_bf16_f32 v66,v68,v69
v_mfma_f32_16x16x32_bf16 a[60:63],a[252:255],v[120:123],a[60:63]
ds_read_b64_tr_b16 a[220:221],v3 offset:8448
v_cvt_pk_bf16_f32 v67,v70,v71
v_mfma_f32_16x16x32_bf16 v[60:63],v[28:31],v[112:115],v[60:63]
v_exp_f32 v72,v72
v_mfma_f32_16x16x32_bf16 v[60:63],v[28:31],v[120:123],v[60:63]
v_exp_f32 v73,v73
v_mfma_f32_16x16x32_bf16 v[96:99],a[128:131],v[192:195],v[40:43]
v_exp_f32 v74,v74
v_mfma_f32_16x16x32_bf16 v[96:99],a[132:135],v[200:203],v[96:99]
v_exp_f32 v75,v75
v_mfma_f32_16x16x32_bf16 v[96:99],a[136:139],v[208:211],v[96:99]
v_exp_f32 v76,v76
v_mfma_f32_16x16x32_bf16 v[96:99],a[140:143],v[216:219],v[96:99]
v_exp_f32 v77,v77
v_mfma_f32_16x16x32_bf16 v[100:103],a[156:159],v[216:219],v[40:43]
v_exp_f32 v78,v78
v_mfma_f32_16x16x32_bf16 v[100:103],a[152:155],v[208:211],v[100:103]
v_exp_f32 v79,v79
v_mfma_f32_16x16x32_bf16 v[100:103],a[148:151],v[200:203],v[100:103]
ds_read_b64_tr_b16 a[222:223],v3 offset:12544
v_cvt_pk_bf16_f32 v72,v72,v73
v_mfma_f32_16x16x32_bf16 v[100:103],a[144:147],v[192:195],v[100:103]
v_cvt_pk_bf16_f32 v73,v74,v75
v_cvt_pk_bf16_f32 v74,v76,v77
v_mfma_f32_16x16x32_bf16 v[104:107],a[160:163],v[192:195],v[40:43]
ds_read_b64_tr_b16 a[224:225],v2 offset:512
v_cvt_pk_bf16_f32 v75,v78,v79
v_mfma_f32_16x16x32_bf16 v[104:107],a[164:167],v[200:203],v[104:107]
ds_read_b64_tr_b16 a[226:227],v2 offset:4608
ds_read_b64_tr_b16 a[228:229],v2 offset:8704
v_mfma_f32_16x16x32_bf16 v[104:107],a[168:171],v[208:211],v[104:107]
ds_read_b64_tr_b16 a[230:231],v2 offset:12800
v_accvgpr_read_b32 v17,a73
v_mfma_f32_16x16x32_bf16 v[104:107],a[172:175],v[216:219],v[104:107]
ds_read_b64_tr_b16 a[232:233],v3 offset:512
v_accvgpr_read_b32 v18,a74
v_mfma_f32_16x16x32_bf16 v[108:111],a[188:191],v[216:219],v[40:43]
global_load_lds_dwordx4 v5,s[10:11] offset:3072
ds_read_b64_tr_b16 a[234:235],v3 offset:4608
v_mfma_f32_16x16x32_bf16 v[108:111],a[184:187],v[208:211],v[108:111]
ds_read_b64_tr_b16 a[236:237],v3 offset:8704
ds_read_b64_tr_b16 a[238:239],v3 offset:12800
v_mfma_f32_16x16x32_bf16 v[108:111],a[180:183],v[200:203],v[108:111]
ds_read_b64_tr_b16 a[240:241],v2 offset:768
v_accvgpr_read_b32 v19,a75
v_mfma_f32_16x16x32_bf16 v[108:111],a[176:179],v[192:195],v[108:111]
s_waitcnt lgkmcnt(9)
ds_read_b64_tr_b16 a[242:243],v2 offset:4864
v_mul_f32 v16,v16,v26
v_mfma_f32_16x16x32_bf16 a[0:3],a[192:195],v[64:67],a[0:3]
ds_read_b64_tr_b16 a[244:245],v2 offset:8960
v_mul_f32 v17,v17,v26
v_mfma_f32_16x16x32_bf16 a[0:3],a[196:199],v[72:75],a[0:3]
ds_read_b64_tr_b16 a[246:247],v2 offset:13056
ds_read_b64_tr_b16 a[248:249],v3 offset:768
v_mfma_f32_16x16x32_bf16 a[4:7],a[200:203],v[64:67],a[4:7]
ds_read_b64_tr_b16 a[250:251],v3 offset:4864
v_mul_f32 v18,v18,v26
v_mfma_f32_16x16x32_bf16 a[4:7],a[204:207],v[72:75],a[4:7]
ds_read_b64_tr_b16 a[252:253],v3 offset:8960
v_mul_f32 v19,v19,v26
v_mfma_f32_16x16x32_bf16 a[8:11],a[208:211],v[64:67],a[8:11]
ds_read_b64_tr_b16 a[254:255],v3 offset:13056
v_mul_f32 v20,v20,v26
v_mfma_f32_16x16x32_bf16 a[8:11],a[212:215],v[72:75],a[8:11]
v_exp_f32 v80,v80
v_mfma_f32_16x16x32_bf16 a[12:15],a[216:219],v[64:67],a[12:15]
v_exp_f32 v81,v81
v_mfma_f32_16x16x32_bf16 a[12:15],a[220:223],v[72:75],a[12:15]
s_waitcnt lgkmcnt(6)
v_exp_f32 v82,v82
v_mfma_f32_16x16x32_bf16 a[16:19],a[224:227],v[64:67],a[16:19]
v_exp_f32 v83,v83
v_mfma_f32_16x16x32_bf16 a[16:19],a[228:231],v[72:75],a[16:19]
v_exp_f32 v84,v84
v_mfma_f32_16x16x32_bf16 a[20:23],a[232:235],v[64:67],a[20:23]
s_getreg_b32 s92,hwreg(HW_REG_IB_STS,0,4)
s_min_u32 s4,s92,s6
s_mov_b32 s6,s92
v_mfma_f32_16x16x32_bf16 a[20:23],a[236:239],v[72:75],a[20:23]
s_cmp_ge_u32 s4,s9
s_cselect_b32 s4,15,s5
v_cvt_pk_bf16_f32 v80,v80,v81
v_mfma_f32_16x16x32_bf16 a[24:27],a[240:243],v[64:67],a[24:27]
s_waitcnt lgkmcnt(0)
v_exp_f32 v85,v85
v_mfma_f32_16x16x32_bf16 a[24:27],a[244:247],v[72:75],a[24:27]
v_exp_f32 v86,v86
v_mfma_f32_16x16x32_bf16 a[28:31],a[248:251],v[64:67],a[28:31]
v_exp_f32 v87,v87
v_mfma_f32_16x16x32_bf16 a[28:31],a[252:255],v[72:75],a[28:31]
s_getpc_b64 s[92:93]
s_cmp_lg_u32 s4,0
s_cbranch_scc1 L_pace_%=
v_mfma_f32_16x16x32_bf16 v[48:51],v[28:31],v[64:67],v[48:51]
v_cvt_pk_bf16_f32 v81,v82,v83
v_cvt_pk_bf16_f32 v82,v84,v85
v_mfma_f32_16x16x32_bf16 v[48:51],v[28:31],v[72:75],v[48:51]
s_add_u32 s10,s20,s74
s_addc_u32 s11,s21,0
v_cvt_pk_bf16_f32 v83,v86,v87
s_waitcnt vmcnt(16)
s_barrier
v_mfma_f32_16x16x32_bf16 v[112:115],a[128:131],v[224:227],v[44:47]
s_add_u32 m0,s76,0
v_exp_f32 v88,v88
v_mfma_f32_16x16x32_bf16 v[112:115],a[132:135],v[232:235],v[112:115]
v_exp_f32 v89,v89
v_mfma_f32_16x16x32_bf16 v[112:115],a[136:139],v[240:243],v[112:115]
global_load_lds_dwordx4 v4,s[10:11] nt
v_cvt_pk_bf16_f32 v88,v88,v89
v_mfma_f32_16x16x32_bf16 v[112:115],a[140:143],v[248:251],v[112:115]
v_exp_f32 v90,v90
v_mfma_f32_16x16x32_bf16 v[116:119],a[156:159],v[248:251],v[44:47]
v_exp_f32 v91,v91
v_mfma_f32_16x16x32_bf16 v[116:119],a[152:155],v[240:243],v[116:119]
v_exp_f32 v92,v92
v_mfma_f32_16x16x32_bf16 v[116:119],a[148:151],v[232:235],v[116:119]
v_exp_f32 v93,v93
v_mfma_f32_16x16x32_bf16 v[116:119],a[144:147],v[224:227],v[116:119]
v_exp_f32 v94,v94
v_mfma_f32_16x16x32_bf16 v[120:123],a[160:163],v[224:227],v[44:47]
v_exp_f32 v95,v95
v_mfma_f32_16x16x32_bf16 v[120:123],a[164:167],v[232:235],v[120:123]
v_cvt_pk_bf16_f32 v89,v90,v91
v_cvt_pk_bf16_f32 v90,v92,v93
v_mfma_f32_16x16x32_bf16 v[120:123],a[168:171],v[240:243],v[120:123]
v_cvt_pk_bf16_f32 v91,v94,v95
v_mul_f32 v21,v21,v26
v_mfma_f32_16x16x32_bf16 v[120:123],a[172:175],v[248:251],v[120:123]
v_mul_f32 v22,v22,v26
v_mul_f32 v23,v23,v26
v_mfma_f32_16x16x32_bf16 v[124:127],a[188:191],v[248:251],v[44:47]
v_cvt_pk_bf16_f32 v16,v16,v17
v_cvt_pk_bf16_f32 v17,v18,v19
v_mfma_f32_16x16x32_bf16 v[124:127],a[184:187],v[240:243],v[124:127]
v_cvt_pk_bf16_f32 v18,v20,v21
v_cvt_pk_bf16_f32 v19,v22,v23
v_mfma_f32_16x16x32_bf16 v[124:127],a[180:183],v[232:235],v[124:127]
v_accvgpr_write_b32 a72,v16
v_accvgpr_write_b32 a73,v17
v_mfma_f32_16x16x32_bf16 v[124:127],a[176:179],v[224:227],v[124:127]
v_accvgpr_write_b32 a74,v18
v_accvgpr_write_b32 a75,v19
v_mfma_f32_16x16x32_bf16 v[132:135],a[192:195],v[80:83],v[132:135]
ds_read_b128 a[76:79],v7 offset:7168
v_add_u32 v1,s78,v1
v_mfma_f32_16x16x32_bf16 v[132:135],a[196:199],v[88:91],v[132:135]
v_exp_f32 v96,v96
v_mfma_f32_16x16x32_bf16 v[140:143],a[200:203],v[80:83],v[140:143]
global_load_lds_dwordx4 v4,s[10:11] offset:1024 nt
ds_read_b128 a[128:131],v1
v_mfma_f32_16x16x32_bf16 v[140:143],a[204:207],v[88:91],v[140:143]
v_exp_f32 v97,v97
v_mfma_f32_16x16x32_bf16 v[148:151],a[208:211],v[80:83],v[148:151]
ds_read_b128 a[132:135],v1 offset:256
v_cvt_pk_bf16_f32 v96,v96,v97
)A"
R"A(v_mfma_f32_16x16x32_bf16 v[148:151],a[212:215],v[88:91],v[148:151]
v_exp_f32 v98,v98
v_mfma_f32_16x16x32_bf16 v[156:159],a[216:219],v[80:83],v[156:159]
ds_read_b128 a[136:139],v1 offset:512
v_accvgpr_read_b32 v12,a84
v_mfma_f32_16x16x32_bf16 v[156:159],a[220:223],v[88:91],v[156:159]
v_exp_f32 v99,v99
v_mfma_f32_16x16x32_bf16 v[164:167],a[224:227],v[80:83],v[164:167]
ds_read_b128 a[140:143],v1 offset:768
v_cvt_pk_bf16_f32 v97,v98,v99
v_mfma_f32_16x16x32_bf16 v[164:167],a[228:231],v[88:91],v[164:167]
v_exp_f32 v100,v100
v_mfma_f32_16x16x32_bf16 v[172:175],a[232:235],v[80:83],v[172:175]
ds_read_b128 a[156:159],v1 offset:4864
v_accvgpr_read_b32 v13,a85
v_mfma_f32_16x16x32_bf16 v[172:175],a[236:239],v[88:91],v[172:175]
v_exp_f32 v101,v101
v_mfma_f32_16x16x32_bf16 v[180:183],a[240:243],v[80:83],v[180:183]
ds_read_b128 a[152:155],v1 offset:4608
v_cvt_pk_bf16_f32 v98,v100,v101
v_mfma_f32_16x16x32_bf16 v[180:183],a[244:247],v[88:91],v[180:183]
v_exp_f32 v102,v102
v_mfma_f32_16x16x32_bf16 v[188:191],a[248:251],v[80:83],v[188:191]
ds_read_b128 a[148:151],v1 offset:4352
v_accvgpr_read_b32 v14,a86
v_mfma_f32_16x16x32_bf16 v[188:191],a[252:255],v[88:91],v[188:191]
v_exp_f32 v103,v103
v_mfma_f32_16x16x32_bf16 v[52:55],v[28:31],v[80:83],v[52:55]
ds_read_b128 a[144:147],v1 offset:4096
v_cvt_pk_bf16_f32 v99,v102,v103
v_mfma_f32_16x16x32_bf16 v[52:55],v[28:31],v[88:91],v[52:55]
s_waitcnt lgkmcnt(4)
v_exp_f32 v104,v104
v_mfma_f32_16x16x32_bf16 v[64:67],a[128:131],v[128:131],v[32:35]
global_load_lds_dwordx4 v5,s[10:11] offset:2048 nt
ds_read_b128 a[160:163],v1 offset:8192
v_mfma_f32_16x16x32_bf16 v[64:67],a[132:135],v[136:139],v[64:67]
v_exp_f32 v105,v105
v_mfma_f32_16x16x32_bf16 v[64:67],a[136:139],v[144:147],v[64:67]
ds_read_b128 a[164:167],v1 offset:8448
v_cvt_pk_bf16_f32 v104,v104,v105
v_mfma_f32_16x16x32_bf16 v[64:67],a[140:143],v[152:155],v[64:67]
s_waitcnt lgkmcnt(4)
v_exp_f32 v106,v106
v_mfma_f32_16x16x32_bf16 v[68:71],a[156:159],v[152:155],v[32:35]
ds_read_b128 a[168:171],v1 offset:8704
v_accvgpr_read_b32 v15,a87
v_mfma_f32_16x16x32_bf16 v[68:71],a[152:155],v[144:147],v[68:71]
s_waitcnt lgkmcnt(4)
v_exp_f32 v107,v107
v_mfma_f32_16x16x32_bf16 v[68:71],a[148:151],v[136:139],v[68:71]
s_waitcnt lgkmcnt(3)
ds_read_b128 a[172:175],v1 offset:8960
v_cvt_pk_bf16_f32 v105,v106,v107
v_mfma_f32_16x16x32_bf16 v[68:71],a[144:147],v[128:131],v[68:71]
s_waitcnt lgkmcnt(3)
v_exp_f32 v108,v108
v_mfma_f32_16x16x32_bf16 v[72:75],a[160:163],v[128:131],v[32:35]
s_waitcnt lgkmcnt(2)
ds_read_b128 a[188:191],v1 offset:13056
v_accvgpr_read_b32 v8,a80
v_mfma_f32_16x16x32_bf16 v[72:75],a[164:167],v[136:139],v[72:75]
s_waitcnt lgkmcnt(2)
ds_read_b128 a[184:187],v1 offset:12800
v_accvgpr_read_b32 v9,a81
v_mfma_f32_16x16x32_bf16 v[72:75],a[168:171],v[144:147],v[72:75]
s_waitcnt lgkmcnt(2)
ds_read_b128 a[180:183],v1 offset:12544
v_accvgpr_read_b32 v10,a82
v_mfma_f32_16x16x32_bf16 v[72:75],a[172:175],v[152:155],v[72:75]
s_waitcnt lgkmcnt(2)
ds_read_b128 a[176:179],v1 offset:12288
v_accvgpr_read_b32 v11,a83
v_mfma_f32_16x16x32_bf16 v[76:79],a[188:191],v[152:155],v[32:35]
s_waitcnt lgkmcnt(2)
v_exp_f32 v109,v109
v_mfma_f32_16x16x32_bf16 v[76:79],a[184:187],v[144:147],v[76:79]
s_waitcnt lgkmcnt(1)
v_exp_f32 v110,v110
v_exp_f32 v111,v111
v_mfma_f32_16x16x32_bf16 v[76:79],a[180:183],v[136:139],v[76:79]
s_waitcnt lgkmcnt(0)
v_cvt_pk_bf16_f32 v106,v108,v109
v_cvt_pk_bf16_f32 v107,v110,v111
v_mfma_f32_16x16x32_bf16 v[76:79],a[176:179],v[128:131],v[76:79]
v_mul_f32 v8,v8,v26
v_mul_f32 v9,v9,v26
v_mfma_f32_16x16x32_bf16 v[196:199],a[192:195],v[96:99],v[196:199]
global_load_lds_dwordx4 v5,s[10:11] offset:3072
v_mul_f32 v10,v10,v26
v_mfma_f32_16x16x32_bf16 v[196:199],a[196:199],v[104:107],v[196:199]
v_mul_f32 v11,v11,v26
v_mul_f32 v12,v12,v26
v_mfma_f32_16x16x32_bf16 v[204:207],a[200:203],v[96:99],v[204:207]
v_mul_f32 v13,v13,v26
v_mul_f32 v14,v14,v26
v_mfma_f32_16x16x32_bf16 v[204:207],a[204:207],v[104:107],v[204:207]
v_mul_f32 v15,v15,v26
v_cvt_pk_bf16_f32 v8,v8,v9
v_mfma_f32_16x16x32_bf16 v[212:215],a[208:211],v[96:99],v[212:215]
v_cvt_pk_bf16_f32 v9,v10,v11
v_cvt_pk_bf16_f32 v10,v12,v13
v_mfma_f32_16x16x32_bf16 v[212:215],a[212:215],v[104:107],v[212:215]
v_cvt_pk_bf16_f32 v11,v14,v15
v_accvgpr_write_b32 a80,v8
v_mfma_f32_16x16x32_bf16 v[220:223],a[216:219],v[96:99],v[220:223]
v_accvgpr_write_b32 a81,v9
v_accvgpr_write_b32 a82,v10
v_mfma_f32_16x16x32_bf16 v[220:223],a[220:223],v[104:107],v[220:223]
ds_read_b128 a[84:87],v7
v_accvgpr_write_b32 a83,v11
v_mfma_f32_16x16x32_bf16 v[228:231],a[224:227],v[96:99],v[228:231]
v_exp_f32 v112,v112
v_mfma_f32_16x16x32_bf16 v[228:231],a[228:231],v[104:107],v[228:231]
v_exp_f32 v113,v113
v_mfma_f32_16x16x32_bf16 v[236:239],a[232:235],v[96:99],v[236:239]
v_exp_f32 v114,v114
v_mfma_f32_16x16x32_bf16 v[236:239],a[236:239],v[104:107],v[236:239]
v_exp_f32 v115,v115
v_mfma_f32_16x16x32_bf16 v[244:247],a[240:243],v[96:99],v[244:247]
s_add_u32 s10,s24,s74
s_addc_u32 s11,s25,0
v_cvt_pk_bf16_f32 v112,v112,v113
v_mfma_f32_16x16x32_bf16 v[244:247],a[244:247],v[104:107],v[244:247]
s_add_u32 s74,s74,16384
v_exp_f32 v116,v116
v_mfma_f32_16x16x32_bf16 v[252:255],a[248:251],v[96:99],v[252:255]
s_add_u32 m0,s76,16384
v_exp_f32 v117,v117
v_mfma_f32_16x16x32_bf16 v[252:255],a[252:255],v[104:107],v[252:255]
s_and_b32 s74,s74,0x3ffff
v_exp_f32 v118,v118
v_mfma_f32_16x16x32_bf16 v[56:59],v[28:31],v[96:99],v[56:59]
global_load_lds_dwordx4 v4,s[10:11]
v_cvt_pk_bf16_f32 v113,v114,v115
v_mfma_f32_16x16x32_bf16 v[56:59],v[28:31],v[104:107],v[56:59]
s_or_b32 s74,s74,s69
v_exp_f32 v119,v119
v_mfma_f32_16x16x32_bf16 v[80:83],a[128:131],v[160:163],v[36:39]
s_sub_u32 s76,s76,32768
v_cvt_pk_bf16_f32 v114,v116,v117
v_cvt_pk_bf16_f32 v115,v118,v119
v_mfma_f32_16x16x32_bf16 v[80:83],a[132:135],v[168:171],v[80:83]
s_and_b32 s76,s76,0x1ffff
v_exp_f32 v120,v120
v_mfma_f32_16x16x32_bf16 v[80:83],a[136:139],v[176:179],v[80:83]
v_exp_f32 v121,v121
v_mfma_f32_16x16x32_bf16 v[80:83],a[140:143],v[184:187],v[80:83]
v_exp_f32 v122,v122
v_mfma_f32_16x16x32_bf16 v[84:87],a[156:159],v[184:187],v[36:39]
v_exp_f32 v123,v123
v_mfma_f32_16x16x32_bf16 v[84:87],a[152:155],v[176:179],v[84:87]
v_exp_f32 v124,v124
v_mfma_f32_16x16x32_bf16 v[84:87],a[148:151],v[168:171],v[84:87]
v_exp_f32 v125,v125
v_mfma_f32_16x16x32_bf16 v[84:87],a[144:147],v[160:163],v[84:87]
v_exp_f32 v126,v126
v_mfma_f32_16x16x32_bf16 v[88:91],a[160:163],v[160:163],v[36:39]
v_exp_f32 v127,v127
v_mfma_f32_16x16x32_bf16 v[88:91],a[164:167],v[168:171],v[88:91]
v_cvt_pk_bf16_f32 v120,v120,v121
v_cvt_pk_bf16_f32 v121,v122,v123
v_mfma_f32_16x16x32_bf16 v[88:91],a[168:171],v[176:179],v[88:91]
v_cvt_pk_bf16_f32 v122,v124,v125
v_cvt_pk_bf16_f32 v123,v126,v127
v_mfma_f32_16x16x32_bf16 v[88:91],a[172:175],v[184:187],v[88:91]
v_add_u32 v2,s78,v2
v_add_u32 v3,s78,v3
v_mfma_f32_16x16x32_bf16 v[92:95],a[188:191],v[184:187],v[36:39]
s_sub_u32 s78,s77,32768
v_exp_f32 v64,v64
v_mfma_f32_16x16x32_bf16 v[92:95],a[184:187],v[176:179],v[92:95]
s_and_b32 s78,s78,0x1ffff
v_exp_f32 v65,v65
v_mfma_f32_16x16x32_bf16 v[92:95],a[180:183],v[168:171],v[92:95]
global_load_lds_dwordx4 v5,s[10:11] offset:1024
v_cvt_pk_bf16_f32 v64,v64,v65
v_mfma_f32_16x16x32_bf16 v[92:95],a[176:179],v[160:163],v[92:95]
s_sub_u32 s78,s78,s77
v_exp_f32 v66,v66
v_mfma_f32_16x16x32_bf16 a[32:35],a[192:195],v[112:115],a[32:35]
s_add_u32 s77,s77,s78
ds_read_b64_tr_b16 a[192:193],v2
ds_read_b64_tr_b16 a[194:195],v2 offset:4096
v_mfma_f32_16x16x32_bf16 a[32:35],a[196:199],v[120:123],a[32:35]
ds_read_b64_tr_b16 a[196:197],v2 offset:8192
v_accvgpr_read_b32 v20,a92
v_mfma_f32_16x16x32_bf16 a[36:39],a[200:203],v[112:115],a[36:39]
ds_read_b64_tr_b16 a[198:199],v2 offset:12288
v_accvgpr_read_b32 v21,a93
v_mfma_f32_16x16x32_bf16 a[36:39],a[204:207],v[120:123],a[36:39]
ds_read_b64_tr_b16 a[200:201],v3
v_accvgpr_read_b32 v22,a94
v_mfma_f32_16x16x32_bf16 a[40:43],a[208:211],v[112:115],a[40:43]
ds_read_b64_tr_b16 a[202:203],v3 offset:4096
ds_read_b64_tr_b16 a[204:205],v3 offset:8192
v_mfma_f32_16x16x32_bf16 a[40:43],a[212:215],v[120:123],a[40:43]
ds_read_b64_tr_b16 a[206:207],v3 offset:12288
v_accvgpr_read_b32 v23,a95
v_mfma_f32_16x16x32_bf16 a[44:47],a[216:219],v[112:115],a[44:47]
ds_read_b64_tr_b16 a[208:209],v2 offset:256
v_accvgpr_read_b32 v16,a88
v_mfma_f32_16x16x32_bf16 a[44:47],a[220:223],v[120:123],a[44:47]
ds_read_b64_tr_b16 a[210:211],v2 offset:4352
v_accvgpr_read_b32 v17,a89
v_mfma_f32_16x16x32_bf16 a[48:51],a[224:227],v[112:115],a[48:51]
ds_read_b64_tr_b16 a[212:213],v2 offset:8448
ds_read_b64_tr_b16 a[214:215],v2 offset:12544
v_mfma_f32_16x16x32_bf16 a[48:51],a[228:231],v[120:123],a[48:51]
v_exp_f32 v67,v67
v_mfma_f32_16x16x32_bf16 a[52:55],a[232:235],v[112:115],a[52:55]
v_exp_f32 v68,v68
v_mfma_f32_16x16x32_bf16 a[52:55],a[236:239],v[120:123],a[52:55]
v_exp_f32 v69,v69
v_mfma_f32_16x16x32_bf16 a[56:59],a[240:243],v[112:115],a[56:59]
v_exp_f32 v70,v70
v_mfma_f32_16x16x32_bf16 a[56:59],a[244:247],v[120:123],a[56:59]
v_exp_f32 v71,v71
v_mfma_f32_16x16x32_bf16 a[60:63],a[248:251],v[112:115],a[60:63]
global_load_lds_dwordx4 v4,s[10:11] offset:2048
ds_read_b64_tr_b16 a[216:217],v3 offset:256
v_mfma_f32_16x16x32_bf16 a[60:63],a[252:255],v[120:123],a[60:63]
ds_read_b64_tr_b16 a[218:219],v3 offset:4352
v_cvt_pk_bf16_f32 v65,v66,v67
s_nop 0
L_pd10_%=:
s_cbranch_execz L_t1b_%=
v_mfma_f32_16x16x32_bf16 v[60:63],v[28:31],v[112:115],v[60:63]
v_cvt_pk_bf16_f32 v66,v68,v69
v_cvt_pk_bf16_f32 v67,v70,v71
v_mfma_f32_16x16x32_bf16 v[60:63],v[28:31],v[120:123],v[60:63]
v_exp_f32 v72,v72
v_mfma_f32_16x16x32_bf16 v[96:99],a[128:131],v[192:195],v[40:43]
v_exp_f32 v73,v73
v_mfma_f32_16x16x32_bf16 v[96:99],a[132:135],v[200:203],v[96:99]
v_exp_f32 v74,v74
v_mfma_f32_16x16x32_bf16 v[96:99],a[136:139],v[208:211],v[96:99]
v_exp_f32 v75,v75
v_mfma_f32_16x16x32_bf16 v[96:99],a[140:143],v[216:219],v[96:99]
v_exp_f32 v76,v76
v_mfma_f32_16x16x32_bf16 v[100:103],a[156:159],v[216:219],v[40:43]
v_exp_f32 v77,v77
v_mfma_f32_16x16x32_bf16 v[100:103],a[152:155],v[208:211],v[100:103]
v_exp_f32 v78,v78
v_mfma_f32_16x16x32_bf16 v[100:103],a[148:151],v[200:203],v[100:103]
v_exp_f32 v79,v79
v_mfma_f32_16x16x32_bf16 v[100:103],a[144:147],v[192:195],v[100:103]
ds_read_b64_tr_b16 a[220:221],v3 offset:8448
ds_read_b64_tr_b16 a[222:223],v3 offset:12544
v_mfma_f32_16x16x32_bf16 v[104:107],a[160:163],v[192:195],v[40:43]
v_cvt_pk_bf16_f32 v72,v72,v73
v_cvt_pk_bf16_f32 v73,v74,v75
v_mfma_f32_16x16x32_bf16 v[104:107],a[164:167],v[200:203],v[104:107]
v_cvt_pk_bf16_f32 v74,v76,v77
v_cvt_pk_bf16_f32 v75,v78,v79
v_mfma_f32_16x16x32_bf16 v[104:107],a[168:171],v[208:211],v[104:107]
ds_read_b64_tr_b16 a[224:225],v2 offset:512
ds_read_b64_tr_b16 a[226:227],v2 offset:4608
v_mfma_f32_16x16x32_bf16 v[104:107],a[172:175],v[216:219],v[104:107]
ds_read_b64_tr_b16 a[228:229],v2 offset:8704
v_accvgpr_read_b32 v18,a90
v_mfma_f32_16x16x32_bf16 v[108:111],a[188:191],v[216:219],v[40:43]
global_load_lds_dwordx4 v5,s[10:11] offset:3072
ds_read_b64_tr_b16 a[230:231],v2 offset:12800
v_mfma_f32_16x16x32_bf16 v[108:111],a[184:187],v[208:211],v[108:111]
ds_read_b64_tr_b16 a[232:233],v3 offset:512
v_accvgpr_read_b32 v19,a91
v_mfma_f32_16x16x32_bf16 v[108:111],a[180:183],v[200:203],v[108:111]
ds_read_b64_tr_b16 a[234:235],v3 offset:4608
ds_read_b64_tr_b16 a[236:237],v3 offset:8704
v_mfma_f32_16x16x32_bf16 v[108:111],a[176:179],v[192:195],v[108:111]
s_waitcnt lgkmcnt(7)
ds_read_b64_tr_b16 a[238:239],v3 offset:12800
v_mul_f32 v16,v16,v26
v_mfma_f32_16x16x32_bf16 a[0:3],a[192:195],v[64:67],a[0:3]
ds_read_b64_tr_b16 a[240:241],v2 offset:768
v_mul_f32 v17,v17,v26
v_mfma_f32_16x16x32_bf16 a[0:3],a[196:199],v[72:75],a[0:3]
ds_read_b64_tr_b16 a[242:243],v2 offset:4864
v_mul_f32 v18,v18,v26
v_mfma_f32_16x16x32_bf16 a[4:7],a[200:203],v[64:67],a[4:7]
ds_read_b64_tr_b16 a[244:245],v2 offset:8960
ds_read_b64_tr_b16 a[246:247],v2 offset:13056
v_mfma_f32_16x16x32_bf16 a[4:7],a[204:207],v[72:75],a[4:7]
ds_read_b64_tr_b16 a[248:249],v3 offset:768
v_mul_f32 v19,v19,v26
v_mfma_f32_16x16x32_bf16 a[8:11],a[208:211],v[64:67],a[8:11]
ds_read_b64_tr_b16 a[250:251],v3 offset:4864
)A"
R"A(v_mul_f32 v20,v20,v26
v_mfma_f32_16x16x32_bf16 a[8:11],a[212:215],v[72:75],a[8:11]
v_mul_f32 v21,v21,v26
v_mul_f32 v22,v22,v26
v_mul_f32 v23,v23,v26
v_cvt_pk_bf16_f32 v16,v16,v17
v_cvt_pk_bf16_f32 v17,v18,v19
v_cvt_pk_bf16_f32 v18,v20,v21
v_cvt_pk_bf16_f32 v19,v22,v23
v_accvgpr_write_b32 a88,v16
v_accvgpr_write_b32 a89,v17
v_accvgpr_write_b32 a90,v18
v_accvgpr_write_b32 a91,v19
v_mfma_f32_16x16x32_bf16 a[12:15],a[216:219],v[64:67],a[12:15]
ds_read_b128 a[92:95],v7 offset:1024
v_mov_b32 v8,v7
v_mfma_f32_16x16x32_bf16 a[12:15],a[220:223],v[72:75],a[12:15]
s_waitcnt lgkmcnt(7)
ds_read_b64_tr_b16 a[252:253],v3 offset:8960
v_add_u32 v1,s78,v1
v_mfma_f32_16x16x32_bf16 a[16:19],a[224:227],v[64:67],a[16:19]
ds_read_b64_tr_b16 a[254:255],v3 offset:13056
v_mfma_f32_16x16x32_bf16 a[16:19],a[228:231],v[72:75],a[16:19]
s_waitcnt lgkmcnt(4)
v_exp_f32 v80,v80
v_mfma_f32_16x16x32_bf16 a[20:23],a[232:235],v[64:67],a[20:23]
s_getreg_b32 s92,hwreg(HW_REG_IB_STS,0,4)
s_min_u32 s4,s92,s6
s_mov_b32 s6,s92
v_mfma_f32_16x16x32_bf16 a[20:23],a[236:239],v[72:75],a[20:23]
s_cmp_ge_u32 s4,s9
s_cselect_b32 s4,15,s5
ds_read_b128 v[24:27],v8 offset:4096
v_mfma_f32_16x16x32_bf16 a[24:27],a[240:243],v[64:67],a[24:27]
v_exp_f32 v81,v81
v_mfma_f32_16x16x32_bf16 a[24:27],a[244:247],v[72:75],a[24:27]
s_waitcnt lgkmcnt(4)
v_exp_f32 v82,v82
v_mfma_f32_16x16x32_bf16 a[28:31],a[248:251],v[64:67],a[28:31]
s_waitcnt lgkmcnt(1)
v_exp_f32 v83,v83
v_mfma_f32_16x16x32_bf16 a[28:31],a[252:255],v[72:75],a[28:31]
v_exp_f32 v84,v84
v_mfma_f32_16x16x32_bf16 v[48:51],v[28:31],v[64:67],v[48:51]
s_getpc_b64 s[92:93]
s_cmp_lg_u32 s4,0
s_cbranch_scc1 L_pace_%=
v_exp_f32 v85,v85
v_exp_f32 v86,v86
v_exp_f32 v87,v87
v_exp_f32 v88,v88
v_exp_f32 v89,v89
v_exp_f32 v90,v90
v_exp_f32 v91,v91
v_exp_f32 v92,v92
v_exp_f32 v93,v93
v_exp_f32 v94,v94
v_exp_f32 v95,v95
v_mfma_f32_16x16x32_bf16 v[48:51],v[28:31],v[72:75],v[48:51]
ds_read_b32 v7,v8 offset:5120
v_cvt_pk_bf16_f32 v80,v80,v81
v_cvt_pk_bf16_f32 v81,v82,v83
v_cvt_pk_bf16_f32 v82,v84,v85
v_cvt_pk_bf16_f32 v83,v86,v87
v_cvt_pk_bf16_f32 v88,v88,v89
v_cvt_pk_bf16_f32 v89,v90,v91
v_cvt_pk_bf16_f32 v90,v92,v93
v_cvt_pk_bf16_f32 v91,v94,v95
s_mov_b32 s70,7
L_a1_%=:
s_waitcnt vmcnt(16)
s_barrier
v_mfma_f32_16x16x32_bf16 v[112:115],a[128:131],v[224:227],v[44:47]
s_add_u32 s10,s20,s74
s_addc_u32 s11,s21,0
s_add_u32 m0,s76,0
v_mfma_f32_16x16x32_bf16 v[112:115],a[132:135],v[232:235],v[112:115]
s_add_u32 s75,s75,16384
s_and_b32 s69,s75,0xfffc0000
ds_read_b128 a[128:131],v1
v_mfma_f32_16x16x32_bf16 v[112:115],a[136:139],v[240:243],v[112:115]
global_load_lds_dwordx4 v4,s[10:11] nt
v_mfma_f32_16x16x32_bf16 v[112:115],a[140:143],v[248:251],v[112:115]
ds_read_b128 a[132:135],v1 offset:256
v_mfma_f32_16x16x32_bf16 v[116:119],a[156:159],v[248:251],v[44:47]
v_mfma_f32_16x16x32_bf16 v[116:119],a[152:155],v[240:243],v[116:119]
ds_read_b128 a[136:139],v1 offset:512
v_mfma_f32_16x16x32_bf16 v[116:119],a[148:151],v[232:235],v[116:119]
v_mfma_f32_16x16x32_bf16 v[116:119],a[144:147],v[224:227],v[116:119]
ds_read_b128 a[140:143],v1 offset:768
v_mfma_f32_16x16x32_bf16 v[120:123],a[160:163],v[224:227],v[44:47]
v_mfma_f32_16x16x32_bf16 v[120:123],a[164:167],v[232:235],v[120:123]
ds_read_b128 a[156:159],v1 offset:4864
v_mfma_f32_16x16x32_bf16 v[120:123],a[168:171],v[240:243],v[120:123]
v_mfma_f32_16x16x32_bf16 v[120:123],a[172:175],v[248:251],v[120:123]
ds_read_b128 a[152:155],v1 offset:4608
v_mfma_f32_16x16x32_bf16 v[124:127],a[188:191],v[248:251],v[44:47]
v_exp_f32 v96,v96
v_mfma_f32_16x16x32_bf16 v[124:127],a[184:187],v[240:243],v[124:127]
ds_read_b128 a[148:151],v1 offset:4352
v_add_u32 v2,s78,v2
v_mfma_f32_16x16x32_bf16 v[124:127],a[180:183],v[232:235],v[124:127]
v_exp_f32 v97,v97
v_mfma_f32_16x16x32_bf16 v[124:127],a[176:179],v[224:227],v[124:127]
ds_read_b128 a[144:147],v1 offset:4096
v_cvt_pk_bf16_f32 v96,v96,v97
v_mfma_f32_16x16x32_bf16 v[132:135],a[192:195],v[80:83],v[132:135]
v_exp_f32 v98,v98
v_mfma_f32_16x16x32_bf16 v[132:135],a[196:199],v[88:91],v[132:135]
ds_read_b128 a[160:163],v1 offset:8192
v_add_u32 v3,s78,v3
v_mfma_f32_16x16x32_bf16 v[140:143],a[200:203],v[80:83],v[140:143]
s_sub_u32 s78,s77,32768
global_load_lds_dwordx4 v4,s[10:11] offset:1024 nt
v_mfma_f32_16x16x32_bf16 v[140:143],a[204:207],v[88:91],v[140:143]
s_and_b32 s78,s78,0x1ffff
v_exp_f32 v99,v99
v_mfma_f32_16x16x32_bf16 v[148:151],a[208:211],v[80:83],v[148:151]
s_sub_u32 s78,s78,s77
ds_read_b128 a[164:167],v1 offset:8448
v_cvt_pk_bf16_f32 v97,v98,v99
v_mfma_f32_16x16x32_bf16 v[148:151],a[212:215],v[88:91],v[148:151]
s_add_u32 s77,s77,s78
v_exp_f32 v100,v100
v_mfma_f32_16x16x32_bf16 v[156:159],a[216:219],v[80:83],v[156:159]
v_exp_f32 v101,v101
v_mfma_f32_16x16x32_bf16 v[156:159],a[220:223],v[88:91],v[156:159]
ds_read_b128 a[168:171],v1 offset:8704
v_cvt_pk_bf16_f32 v98,v100,v101
v_mfma_f32_16x16x32_bf16 v[164:167],a[224:227],v[80:83],v[164:167]
v_exp_f32 v102,v102
v_mfma_f32_16x16x32_bf16 v[164:167],a[228:231],v[88:91],v[164:167]
v_exp_f32 v103,v103
v_mfma_f32_16x16x32_bf16 v[172:175],a[232:235],v[80:83],v[172:175]
ds_read_b128 a[172:175],v1 offset:8960
v_cvt_pk_bf16_f32 v99,v102,v103
v_mfma_f32_16x16x32_bf16 v[172:175],a[236:239],v[88:91],v[172:175]
v_exp_f32 v104,v104
v_mfma_f32_16x16x32_bf16 v[180:183],a[240:243],v[80:83],v[180:183]
v_exp_f32 v105,v105
v_mfma_f32_16x16x32_bf16 v[180:183],a[244:247],v[88:91],v[180:183]
ds_read_b128 a[188:191],v1 offset:13056
v_cvt_pk_bf16_f32 v104,v104,v105
v_mfma_f32_16x16x32_bf16 v[188:191],a[248:251],v[80:83],v[188:191]
v_exp_f32 v106,v106
v_mfma_f32_16x16x32_bf16 v[188:191],a[252:255],v[88:91],v[188:191]
v_exp_f32 v107,v107
v_mfma_f32_16x16x32_bf16 v[52:55],v[28:31],v[80:83],v[52:55]
ds_read_b128 a[184:187],v1 offset:12800
v_cvt_pk_bf16_f32 v105,v106,v107
v_mfma_f32_16x16x32_bf16 v[52:55],v[28:31],v[88:91],v[52:55]
s_waitcnt lgkmcnt(3)
v_exp_f32 v108,v108
v_mfma_f32_16x16x32_bf16 v[64:67],a[128:131],v[128:131],v[32:35]
global_load_lds_dwordx4 v5,s[10:11] offset:2048 nt
ds_read_b128 a[180:183],v1 offset:12544
v_mfma_f32_16x16x32_bf16 v[64:67],a[132:135],v[136:139],v[64:67]
v_exp_f32 v109,v109
v_mfma_f32_16x16x32_bf16 v[64:67],a[136:139],v[144:147],v[64:67]
ds_read_b128 a[176:179],v1 offset:12288
v_cvt_pk_bf16_f32 v106,v108,v109
v_mfma_f32_16x16x32_bf16 v[64:67],a[140:143],v[152:155],v[64:67]
v_exp_f32 v110,v110
v_mfma_f32_16x16x32_bf16 v[68:71],a[156:159],v[152:155],v[32:35]
v_exp_f32 v111,v111
v_mfma_f32_16x16x32_bf16 v[68:71],a[152:155],v[144:147],v[68:71]
v_cvt_pk_bf16_f32 v107,v110,v111
v_add_u32 v1,s78,v1
v_mfma_f32_16x16x32_bf16 v[68:71],a[148:151],v[136:139],v[68:71]
v_exp_f32 v112,v112
v_mfma_f32_16x16x32_bf16 v[68:71],a[144:147],v[128:131],v[68:71]
v_exp_f32 v113,v113
v_mfma_f32_16x16x32_bf16 v[72:75],a[160:163],v[128:131],v[32:35]
v_exp_f32 v114,v114
v_mfma_f32_16x16x32_bf16 v[72:75],a[164:167],v[136:139],v[72:75]
v_exp_f32 v115,v115
v_mfma_f32_16x16x32_bf16 v[72:75],a[168:171],v[144:147],v[72:75]
s_waitcnt lgkmcnt(0)
v_exp_f32 v116,v116
v_mfma_f32_16x16x32_bf16 v[72:75],a[172:175],v[152:155],v[72:75]
v_exp_f32 v117,v117
v_mfma_f32_16x16x32_bf16 v[76:79],a[188:191],v[152:155],v[32:35]
v_exp_f32 v118,v118
v_mfma_f32_16x16x32_bf16 v[76:79],a[184:187],v[144:147],v[76:79]
v_exp_f32 v119,v119
v_mfma_f32_16x16x32_bf16 v[76:79],a[180:183],v[136:139],v[76:79]
v_cvt_pk_bf16_f32 v112,v112,v113
v_cvt_pk_bf16_f32 v113,v114,v115
v_mfma_f32_16x16x32_bf16 v[76:79],a[176:179],v[128:131],v[76:79]
v_cvt_pk_bf16_f32 v114,v116,v117
v_cvt_pk_bf16_f32 v115,v118,v119
v_mfma_f32_16x16x32_bf16 v[196:199],a[192:195],v[96:99],v[196:199]
global_load_lds_dwordx4 v5,s[10:11] offset:3072
v_mfma_f32_16x16x32_bf16 v[196:199],a[196:199],v[104:107],v[196:199]
v_exp_f32 v120,v120
v_mfma_f32_16x16x32_bf16 v[204:207],a[200:203],v[96:99],v[204:207]
v_exp_f32 v121,v121
v_mfma_f32_16x16x32_bf16 v[204:207],a[204:207],v[104:107],v[204:207]
v_exp_f32 v122,v122
v_mfma_f32_16x16x32_bf16 v[212:215],a[208:211],v[96:99],v[212:215]
v_exp_f32 v123,v123
v_mfma_f32_16x16x32_bf16 v[212:215],a[212:215],v[104:107],v[212:215]
v_exp_f32 v124,v124
v_mfma_f32_16x16x32_bf16 v[220:223],a[216:219],v[96:99],v[220:223]
v_exp_f32 v125,v125
v_mfma_f32_16x16x32_bf16 v[220:223],a[220:223],v[104:107],v[220:223]
v_exp_f32 v126,v126
v_mfma_f32_16x16x32_bf16 v[228:231],a[224:227],v[96:99],v[228:231]
v_exp_f32 v127,v127
v_mfma_f32_16x16x32_bf16 v[228:231],a[228:231],v[104:107],v[228:231]
v_cvt_pk_bf16_f32 v120,v120,v121
v_cvt_pk_bf16_f32 v121,v122,v123
v_mfma_f32_16x16x32_bf16 v[236:239],a[232:235],v[96:99],v[236:239]
v_cvt_pk_bf16_f32 v122,v124,v125
v_cvt_pk_bf16_f32 v123,v126,v127
v_mfma_f32_16x16x32_bf16 v[236:239],a[236:239],v[104:107],v[236:239]
v_exp_f32 v64,v64
v_mfma_f32_16x16x32_bf16 v[244:247],a[240:243],v[96:99],v[244:247]
s_add_u32 s10,s24,s74
s_addc_u32 s11,s25,0
s_add_u32 s74,s74,16384
v_mfma_f32_16x16x32_bf16 v[244:247],a[244:247],v[104:107],v[244:247]
s_and_b32 s74,s74,0x3ffff
v_exp_f32 v65,v65
v_mfma_f32_16x16x32_bf16 v[252:255],a[248:251],v[96:99],v[252:255]
s_add_u32 m0,s76,16384
v_exp_f32 v66,v66
v_mfma_f32_16x16x32_bf16 v[252:255],a[252:255],v[104:107],v[252:255]
s_or_b32 s74,s74,s69
v_exp_f32 v67,v67
v_mfma_f32_16x16x32_bf16 v[56:59],v[28:31],v[96:99],v[56:59]
global_load_lds_dwordx4 v4,s[10:11]
v_cvt_pk_bf16_f32 v64,v64,v65
v_mfma_f32_16x16x32_bf16 v[56:59],v[28:31],v[104:107],v[56:59]
s_sub_u32 s76,s76,32768
v_exp_f32 v68,v68
v_mfma_f32_16x16x32_bf16 v[80:83],a[128:131],v[160:163],v[36:39]
s_and_b32 s76,s76,0x1ffff
v_exp_f32 v69,v69
v_mfma_f32_16x16x32_bf16 v[80:83],a[132:135],v[168:171],v[80:83]
v_exp_f32 v70,v70
v_mfma_f32_16x16x32_bf16 v[80:83],a[136:139],v[176:179],v[80:83]
v_exp_f32 v71,v71
v_mfma_f32_16x16x32_bf16 v[80:83],a[140:143],v[184:187],v[80:83]
v_cvt_pk_bf16_f32 v65,v66,v67
v_cvt_pk_bf16_f32 v66,v68,v69
v_mfma_f32_16x16x32_bf16 v[84:87],a[156:159],v[184:187],v[36:39]
v_exp_f32 v72,v72
v_mfma_f32_16x16x32_bf16 v[84:87],a[152:155],v[176:179],v[84:87]
v_exp_f32 v73,v73
v_mfma_f32_16x16x32_bf16 v[84:87],a[148:151],v[168:171],v[84:87]
v_cvt_pk_bf16_f32 v67,v70,v71
v_cvt_pk_bf16_f32 v72,v72,v73
v_mfma_f32_16x16x32_bf16 v[84:87],a[144:147],v[160:163],v[84:87]
v_exp_f32 v74,v74
v_mfma_f32_16x16x32_bf16 v[88:91],a[160:163],v[160:163],v[36:39]
v_exp_f32 v75,v75
v_mfma_f32_16x16x32_bf16 v[88:91],a[164:167],v[168:171],v[88:91]
v_exp_f32 v76,v76
v_mfma_f32_16x16x32_bf16 v[88:91],a[168:171],v[176:179],v[88:91]
v_exp_f32 v77,v77
v_mfma_f32_16x16x32_bf16 v[88:91],a[172:175],v[184:187],v[88:91]
v_exp_f32 v78,v78
v_mfma_f32_16x16x32_bf16 v[92:95],a[188:191],v[184:187],v[36:39]
v_exp_f32 v79,v79
v_mfma_f32_16x16x32_bf16 v[92:95],a[184:187],v[176:179],v[92:95]
v_cvt_pk_bf16_f32 v73,v74,v75
v_cvt_pk_bf16_f32 v74,v76,v77
v_mfma_f32_16x16x32_bf16 v[92:95],a[180:183],v[168:171],v[92:95]
global_load_lds_dwordx4 v5,s[10:11] offset:1024
v_cvt_pk_bf16_f32 v75,v78,v79
v_mfma_f32_16x16x32_bf16 v[92:95],a[176:179],v[160:163],v[92:95]
v_exp_f32 v80,v80
v_mfma_f32_16x16x32_bf16 a[32:35],a[192:195],v[112:115],a[32:35]
ds_read_b64_tr_b16 a[192:193],v2
ds_read_b64_tr_b16 a[194:195],v2 offset:4096
v_mfma_f32_16x16x32_bf16 a[32:35],a[196:199],v[120:123],a[32:35]
v_exp_f32 v81,v81
v_mfma_f32_16x16x32_bf16 a[36:39],a[200:203],v[112:115],a[36:39]
ds_read_b64_tr_b16 a[196:197],v2 offset:8192
ds_read_b64_tr_b16 a[198:199],v2 offset:12288
v_mfma_f32_16x16x32_bf16 a[36:39],a[204:207],v[120:123],a[36:39]
ds_read_b64_tr_b16 a[200:201],v3
v_cvt_pk_bf16_f32 v80,v80,v81
v_mfma_f32_16x16x32_bf16 a[40:43],a[208:211],v[112:115],a[40:43]
v_exp_f32 v82,v82
v_mfma_f32_16x16x32_bf16 a[40:43],a[212:215],v[120:123],a[40:43]
ds_read_b64_tr_b16 a[202:203],v3 offset:4096
ds_read_b64_tr_b16 a[204:205],v3 offset:8192
v_mfma_f32_16x16x32_bf16 a[44:47],a[216:219],v[112:115],a[44:47]
v_exp_f32 v83,v83
v_mfma_f32_16x16x32_bf16 a[44:47],a[220:223],v[120:123],a[44:47]
ds_read_b64_tr_b16 a[206:207],v3 offset:12288
ds_read_b64_tr_b16 a[208:209],v2 offset:256
v_mfma_f32_16x16x32_bf16 a[48:51],a[224:227],v[112:115],a[48:51]
ds_read_b64_tr_b16 a[210:211],v2 offset:4352
v_cvt_pk_bf16_f32 v81,v82,v83
)A"
R"A(v_mfma_f32_16x16x32_bf16 a[48:51],a[228:231],v[120:123],a[48:51]
v_exp_f32 v84,v84
v_mfma_f32_16x16x32_bf16 a[52:55],a[232:235],v[112:115],a[52:55]
ds_read_b64_tr_b16 a[212:213],v2 offset:8448
ds_read_b64_tr_b16 a[214:215],v2 offset:12544
v_mfma_f32_16x16x32_bf16 a[52:55],a[236:239],v[120:123],a[52:55]
v_exp_f32 v85,v85
v_mfma_f32_16x16x32_bf16 a[56:59],a[240:243],v[112:115],a[56:59]
ds_read_b64_tr_b16 a[216:217],v3 offset:256
ds_read_b64_tr_b16 a[218:219],v3 offset:4352
v_mfma_f32_16x16x32_bf16 a[56:59],a[244:247],v[120:123],a[56:59]
ds_read_b64_tr_b16 a[220:221],v3 offset:8448
v_cvt_pk_bf16_f32 v82,v84,v85
v_mfma_f32_16x16x32_bf16 a[60:63],a[248:251],v[112:115],a[60:63]
global_load_lds_dwordx4 v4,s[10:11] offset:2048
ds_read_b64_tr_b16 a[222:223],v3 offset:12544
v_mfma_f32_16x16x32_bf16 a[60:63],a[252:255],v[120:123],a[60:63]
v_exp_f32 v86,v86
v_mfma_f32_16x16x32_bf16 v[60:63],v[28:31],v[112:115],v[60:63]
ds_read_b64_tr_b16 a[224:225],v2 offset:512
ds_read_b64_tr_b16 a[226:227],v2 offset:4608
v_mfma_f32_16x16x32_bf16 v[60:63],v[28:31],v[120:123],v[60:63]
v_exp_f32 v87,v87
v_mfma_f32_16x16x32_bf16 v[96:99],a[128:131],v[192:195],v[40:43]
ds_read_b64_tr_b16 a[228:229],v2 offset:8704
ds_read_b64_tr_b16 a[230:231],v2 offset:12800
v_mfma_f32_16x16x32_bf16 v[96:99],a[132:135],v[200:203],v[96:99]
ds_read_b64_tr_b16 a[232:233],v3 offset:512
v_cvt_pk_bf16_f32 v83,v86,v87
v_mfma_f32_16x16x32_bf16 v[96:99],a[136:139],v[208:211],v[96:99]
v_exp_f32 v88,v88
v_mfma_f32_16x16x32_bf16 v[96:99],a[140:143],v[216:219],v[96:99]
ds_read_b64_tr_b16 a[234:235],v3 offset:4608
ds_read_b64_tr_b16 a[236:237],v3 offset:8704
v_mfma_f32_16x16x32_bf16 v[100:103],a[156:159],v[216:219],v[40:43]
v_exp_f32 v89,v89
v_mfma_f32_16x16x32_bf16 v[100:103],a[152:155],v[208:211],v[100:103]
ds_read_b64_tr_b16 a[238:239],v3 offset:12800
ds_read_b64_tr_b16 a[240:241],v2 offset:768
v_mfma_f32_16x16x32_bf16 v[100:103],a[148:151],v[200:203],v[100:103]
ds_read_b64_tr_b16 a[242:243],v2 offset:4864
v_cvt_pk_bf16_f32 v88,v88,v89
v_mfma_f32_16x16x32_bf16 v[100:103],a[144:147],v[192:195],v[100:103]
v_exp_f32 v90,v90
v_mfma_f32_16x16x32_bf16 v[104:107],a[160:163],v[192:195],v[40:43]
ds_read_b64_tr_b16 a[244:245],v2 offset:8960
ds_read_b64_tr_b16 a[246:247],v2 offset:13056
v_mfma_f32_16x16x32_bf16 v[104:107],a[164:167],v[200:203],v[104:107]
v_exp_f32 v91,v91
v_mfma_f32_16x16x32_bf16 v[104:107],a[168:171],v[208:211],v[104:107]
ds_read_b64_tr_b16 a[248:249],v3 offset:768
ds_read_b64_tr_b16 a[250:251],v3 offset:4864
v_mfma_f32_16x16x32_bf16 v[104:107],a[172:175],v[216:219],v[104:107]
ds_read_b64_tr_b16 a[252:253],v3 offset:8960
v_cvt_pk_bf16_f32 v89,v90,v91
v_mfma_f32_16x16x32_bf16 v[108:111],a[188:191],v[216:219],v[40:43]
global_load_lds_dwordx4 v5,s[10:11] offset:3072
ds_read_b64_tr_b16 a[254:255],v3 offset:13056
v_mfma_f32_16x16x32_bf16 v[108:111],a[184:187],v[208:211],v[108:111]
v_exp_f32 v92,v92
v_mfma_f32_16x16x32_bf16 v[108:111],a[180:183],v[200:203],v[108:111]
v_exp_f32 v93,v93
v_mfma_f32_16x16x32_bf16 v[108:111],a[176:179],v[192:195],v[108:111]
s_waitcnt lgkmcnt(6)
v_exp_f32 v94,v94
v_mfma_f32_16x16x32_bf16 a[0:3],a[192:195],v[64:67],a[0:3]
v_exp_f32 v95,v95
v_mfma_f32_16x16x32_bf16 a[0:3],a[196:199],v[72:75],a[0:3]
v_cvt_pk_bf16_f32 v90,v92,v93
v_cvt_pk_bf16_f32 v91,v94,v95
v_mfma_f32_16x16x32_bf16 a[4:7],a[200:203],v[64:67],a[4:7]
v_mfma_f32_16x16x32_bf16 a[4:7],a[204:207],v[72:75],a[4:7]
v_mfma_f32_16x16x32_bf16 a[8:11],a[208:211],v[64:67],a[8:11]
v_mfma_f32_16x16x32_bf16 a[8:11],a[212:215],v[72:75],a[8:11]
v_mfma_f32_16x16x32_bf16 a[12:15],a[216:219],v[64:67],a[12:15]
v_mfma_f32_16x16x32_bf16 a[12:15],a[220:223],v[72:75],a[12:15]
v_mfma_f32_16x16x32_bf16 a[16:19],a[224:227],v[64:67],a[16:19]
v_mfma_f32_16x16x32_bf16 a[16:19],a[228:231],v[72:75],a[16:19]
v_mfma_f32_16x16x32_bf16 a[20:23],a[232:235],v[64:67],a[20:23]
s_getreg_b32 s92,hwreg(HW_REG_IB_STS,0,4)
s_min_u32 s4,s92,s6
s_mov_b32 s6,s92
v_mfma_f32_16x16x32_bf16 a[20:23],a[236:239],v[72:75],a[20:23]
s_cmp_ge_u32 s4,s9
s_cselect_b32 s4,15,s5
v_mfma_f32_16x16x32_bf16 a[24:27],a[240:243],v[64:67],a[24:27]
s_waitcnt lgkmcnt(0)
v_mfma_f32_16x16x32_bf16 a[24:27],a[244:247],v[72:75],a[24:27]
v_mfma_f32_16x16x32_bf16 a[28:31],a[248:251],v[64:67],a[28:31]
v_mfma_f32_16x16x32_bf16 a[28:31],a[252:255],v[72:75],a[28:31]
s_getpc_b64 s[92:93]
s_cmp_lg_u32 s4,0
s_cbranch_scc1 L_pace_%=
v_mfma_f32_16x16x32_bf16 v[48:51],v[28:31],v[64:67],v[48:51]
v_mfma_f32_16x16x32_bf16 v[48:51],v[28:31],v[72:75],v[48:51]
s_sub_u32 s70,s70,1
s_cbranch_scc0 L_a1_%=
L_nx1_%=:
s_waitcnt vmcnt(16)
s_barrier
v_mfma_f32_16x16x32_bf16 v[112:115],a[128:131],v[224:227],v[44:47]
s_mul_i32 s86,s60,10240
s_mul_i32 s85,s63,1310720
s_mul_i32 s66,s64,320
v_mfma_f32_16x16x32_bf16 v[112:115],a[132:135],v[232:235],v[112:115]
s_add_u32 s85,s85,s66
s_add_u32 s83,s64,0
s_and_b32 s83,s83,3
v_mfma_f32_16x16x32_bf16 v[112:115],a[136:139],v[240:243],v[112:115]
s_lshl_b32 s83,s83,1
s_mul_i32 s83,s83,163840
s_add_u32 s83,s83,s85
v_mfma_f32_16x16x32_bf16 v[112:115],a[140:143],v[248:251],v[112:115]
s_cmp_eq_u32 s60,3
s_cselect_b32 s69,0x40000000,0
v_add_u32 v14,s69,v6
v_mfma_f32_16x16x32_bf16 v[116:119],a[156:159],v[248:251],v[44:47]
s_add_u32 s84,s83,20480
ds_read_b128 a[128:131],v1
v_add_u32 v2,s78,v2
v_mfma_f32_16x16x32_bf16 v[116:119],a[152:155],v[240:243],v[116:119]
s_add_u32 m0,s86,122880
v_exp_f32 v96,v96
v_mfma_f32_16x16x32_bf16 v[116:119],a[148:151],v[232:235],v[116:119]
s_getpc_b64 s[8:9]
L_pcg_%=:
ds_read_b128 a[132:135],v1 offset:256
v_add_u32 v3,s78,v3
v_mfma_f32_16x16x32_bf16 v[116:119],a[144:147],v[224:227],v[116:119]
buffer_load_dwordx4 v24,s[48:51],s83 offen lds
v_mfma_f32_16x16x32_bf16 v[120:123],a[160:163],v[224:227],v[44:47]
s_sub_u32 s78,s77,32768
s_and_b32 s78,s78,0x1ffff
ds_read_b128 a[136:139],v1 offset:512
v_mfma_f32_16x16x32_bf16 v[120:123],a[164:167],v[232:235],v[120:123]
buffer_store_dwordx4 a[112:115],v14,s[40:43],s81 offen offset:3072 sc1
v_mfma_f32_16x16x32_bf16 v[120:123],a[168:171],v[240:243],v[120:123]
s_sub_u32 s78,s78,s77
ds_read_b128 a[140:143],v1 offset:768
v_lshlrev_b32 v4,10,v6
v_mfma_f32_16x16x32_bf16 v[120:123],a[172:175],v[248:251],v[120:123]
s_add_u32 s77,s77,s78
v_exp_f32 v97,v97
v_mfma_f32_16x16x32_bf16 v[124:127],a[188:191],v[248:251],v[44:47]
s_mov_b32 s6,L_gbF_%=-L_pcg_%=
ds_read_b128 a[156:159],v1 offset:4864
v_cvt_pk_bf16_f32 v96,v96,v97
v_mfma_f32_16x16x32_bf16 v[124:127],a[184:187],v[240:243],v[124:127]
s_mov_b32 s10,L_geF_%=-L_gbF_%=
v_exp_f32 v98,v98
v_mfma_f32_16x16x32_bf16 v[124:127],a[180:183],v[232:235],v[124:127]
s_mov_b32 s11,0x20000
s_lshr_b32 s7,%1,3
ds_read_b128 a[152:155],v1 offset:4608
v_mfma_f32_16x16x32_bf16 v[124:127],a[176:179],v[224:227],v[124:127]
s_lshl_b32 s7,s7,2
v_exp_f32 v99,v99
v_mfma_f32_16x16x32_bf16 v[132:135],a[192:195],v[80:83],v[132:135]
s_add_u32 s7,s7,s60
ds_read_b128 a[148:151],v1 offset:4352
v_cvt_pk_bf16_f32 v97,v98,v99
v_mfma_f32_16x16x32_bf16 v[132:135],a[196:199],v[88:91],v[132:135]
buffer_load_dwordx4 v25,s[48:51],s83 offen offset:1024 lds
v_mfma_f32_16x16x32_bf16 v[140:143],a[200:203],v[80:83],v[140:143]
s_lshl_b32 s7,s7,7
ds_read_b128 a[144:147],v1 offset:4096
v_mov_b32 v8,s52
v_mfma_f32_16x16x32_bf16 v[140:143],a[204:207],v[88:91],v[140:143]
s_lshl_b32 s66,s61,5
v_exp_f32 v100,v100
v_mfma_f32_16x16x32_bf16 v[148:151],a[208:211],v[80:83],v[148:151]
s_lshl_b32 s67,s63,3
ds_read_b128 a[160:163],v1 offset:8192
v_mov_b32 v9,s53
v_mfma_f32_16x16x32_bf16 v[148:151],a[212:215],v[88:91],v[148:151]
s_add_u32 s66,s66,s67
v_exp_f32 v101,v101
v_mfma_f32_16x16x32_bf16 v[156:159],a[216:219],v[80:83],v[156:159]
s_lshl_b32 s66,s66,16
ds_read_b128 a[164:167],v1 offset:8448
v_cvt_pk_bf16_f32 v98,v100,v101
v_mfma_f32_16x16x32_bf16 v[156:159],a[220:223],v[88:91],v[156:159]
s_lshl_b32 s67,s60,14
v_exp_f32 v102,v102
v_mfma_f32_16x16x32_bf16 v[164:167],a[224:227],v[80:83],v[164:167]
s_add_u32 s66,s66,s67
ds_read_b128 a[168:171],v1 offset:8704
v_lshlrev_b32 v10,20,v6
v_mfma_f32_16x16x32_bf16 v[164:167],a[228:231],v[88:91],v[164:167]
s_mov_b32 s30,1523712
v_exp_f32 v103,v103
v_mfma_f32_16x16x32_bf16 v[172:175],a[232:235],v[80:83],v[172:175]
buffer_store_dwordx4 a[116:119],v6,s[40:43],s81 offen offset:1024 sc1
v_mfma_f32_16x16x32_bf16 v[172:175],a[236:239],v[88:91],v[172:175]
s_mov_b32 s31,0x20000
ds_read_b128 a[172:175],v1 offset:8960
v_cvt_pk_bf16_f32 v99,v102,v103
v_mfma_f32_16x16x32_bf16 v[180:183],a[240:243],v[80:83],v[180:183]
buffer_load_dwordx4 v26,s[48:51],s83 offen offset:2048 lds
v_mfma_f32_16x16x32_bf16 v[180:183],a[244:247],v[88:91],v[180:183]
v_exp_f32 v104,v104
v_mfma_f32_16x16x32_bf16 v[188:191],a[248:251],v[80:83],v[188:191]
v_exp_f32 v105,v105
v_mfma_f32_16x16x32_bf16 v[188:191],a[252:255],v[88:91],v[188:191]
ds_read_b128 a[188:191],v1 offset:13056
v_cvt_pk_bf16_f32 v104,v104,v105
v_mfma_f32_16x16x32_bf16 v[52:55],v[28:31],v[80:83],v[52:55]
v_exp_f32 v106,v106
v_mfma_f32_16x16x32_bf16 v[52:55],v[28:31],v[88:91],v[52:55]
s_waitcnt lgkmcnt(2)
v_exp_f32 v107,v107
v_mfma_f32_16x16x32_bf16 v[64:67],a[128:131],v[128:131],v[32:35]
ds_read_b128 a[184:187],v1 offset:12800
v_cvt_pk_bf16_f32 v105,v106,v107
v_mfma_f32_16x16x32_bf16 v[64:67],a[132:135],v[136:139],v[64:67]
v_exp_f32 v108,v108
v_mfma_f32_16x16x32_bf16 v[64:67],a[136:139],v[144:147],v[64:67]
s_add_u32 s28,s12,s66
s_addc_u32 s29,s13,0
ds_read_b128 a[180:183],v1 offset:12544
v_mfma_f32_16x16x32_bf16 v[64:67],a[140:143],v[152:155],v[64:67]
buffer_load_dwordx4 v27,s[48:51],s83 offen offset:3072 lds
v_mfma_f32_16x16x32_bf16 v[68:71],a[156:159],v[152:155],v[32:35]
s_and_b32 s29,s29,0xffff
ds_read_b128 a[176:179],v1 offset:12288
v_add_u32 v1,s78,v1
v_mfma_f32_16x16x32_bf16 v[68:71],a[152:155],v[144:147],v[68:71]
v_exp_f32 v109,v109
v_mfma_f32_16x16x32_bf16 v[68:71],a[148:151],v[136:139],v[68:71]
v_exp_f32 v110,v110
v_mfma_f32_16x16x32_bf16 v[68:71],a[144:147],v[128:131],v[68:71]
v_exp_f32 v111,v111
v_mfma_f32_16x16x32_bf16 v[72:75],a[160:163],v[128:131],v[32:35]
v_cvt_pk_bf16_f32 v106,v108,v109
v_cvt_pk_bf16_f32 v107,v110,v111
v_mfma_f32_16x16x32_bf16 v[72:75],a[164:167],v[136:139],v[72:75]
buffer_store_dwordx4 a[120:123],v14,s[40:43],s82 offen offset:3072 sc1
v_mfma_f32_16x16x32_bf16 v[72:75],a[168:171],v[144:147],v[72:75]
s_waitcnt lgkmcnt(1)
v_exp_f32 v112,v112
v_mfma_f32_16x16x32_bf16 v[72:75],a[172:175],v[152:155],v[72:75]
s_add_u32 m0,s86,126976
v_exp_f32 v113,v113
v_mfma_f32_16x16x32_bf16 v[76:79],a[188:191],v[152:155],v[32:35]
s_waitcnt lgkmcnt(0)
v_exp_f32 v114,v114
v_mfma_f32_16x16x32_bf16 v[76:79],a[184:187],v[144:147],v[76:79]
buffer_load_dwordx4 v7,s[48:51],s83 offen lds
v_mfma_f32_16x16x32_bf16 v[76:79],a[180:183],v[136:139],v[76:79]
s_add_u32 s83,s64,0
v_exp_f32 v115,v115
v_mfma_f32_16x16x32_bf16 v[76:79],a[176:179],v[128:131],v[76:79]
s_and_b32 s83,s83,3
v_exp_f32 v116,v116
v_mfma_f32_16x16x32_bf16 v[196:199],a[192:195],v[96:99],v[196:199]
L_pd11_%=:
s_cbranch_execz L_t1c_%=
s_lshl_b32 s83,s83,1
v_exp_f32 v117,v117
v_mfma_f32_16x16x32_bf16 v[196:199],a[196:199],v[104:107],v[196:199]
s_or_b32 s83,s83,1
v_exp_f32 v118,v118
v_mfma_f32_16x16x32_bf16 v[204:207],a[200:203],v[96:99],v[204:207]
s_mul_i32 s83,s83,163840
v_exp_f32 v119,v119
v_mfma_f32_16x16x32_bf16 v[204:207],a[204:207],v[104:107],v[204:207]
s_add_u32 s83,s83,s85
v_cvt_pk_bf16_f32 v112,v112,v113
v_cvt_pk_bf16_f32 v113,v114,v115
v_mfma_f32_16x16x32_bf16 v[212:215],a[208:211],v[96:99],v[212:215]
v_cvt_pk_bf16_f32 v114,v116,v117
v_cvt_pk_bf16_f32 v115,v118,v119
v_mfma_f32_16x16x32_bf16 v[212:215],a[212:215],v[104:107],v[212:215]
s_add_u32 m0,s86,128000
v_exp_f32 v120,v120
v_mfma_f32_16x16x32_bf16 v[220:223],a[216:219],v[96:99],v[220:223]
v_exp_f32 v121,v121
v_mfma_f32_16x16x32_bf16 v[220:223],a[220:223],v[104:107],v[220:223]
buffer_load_dwordx4 v24,s[48:51],s84 offen lds
v_mfma_f32_16x16x32_bf16 v[228:231],a[224:227],v[96:99],v[228:231]
v_exp_f32 v122,v122
v_mfma_f32_16x16x32_bf16 v[228:231],a[228:231],v[104:107],v[228:231]
v_exp_f32 v123,v123
v_mfma_f32_16x16x32_bf16 v[236:239],a[232:235],v[96:99],v[236:239]
buffer_store_dwordx4 a[124:127],v6,s[40:43],s82 offen offset:1024 sc1
)A"
R"A(v_mfma_f32_16x16x32_bf16 v[236:239],a[236:239],v[104:107],v[236:239]
v_exp_f32 v124,v124
v_mfma_f32_16x16x32_bf16 v[244:247],a[240:243],v[96:99],v[244:247]
v_exp_f32 v125,v125
v_mfma_f32_16x16x32_bf16 v[244:247],a[244:247],v[104:107],v[244:247]
v_exp_f32 v126,v126
v_mfma_f32_16x16x32_bf16 v[252:255],a[248:251],v[96:99],v[252:255]
v_exp_f32 v127,v127
v_mfma_f32_16x16x32_bf16 v[252:255],a[252:255],v[104:107],v[252:255]
v_cvt_pk_bf16_f32 v120,v120,v121
v_cvt_pk_bf16_f32 v121,v122,v123
v_mfma_f32_16x16x32_bf16 v[56:59],v[28:31],v[96:99],v[56:59]
v_cvt_pk_bf16_f32 v122,v124,v125
v_cvt_pk_bf16_f32 v123,v126,v127
v_mfma_f32_16x16x32_bf16 v[56:59],v[28:31],v[104:107],v[56:59]
buffer_load_dwordx4 v25,s[48:51],s84 offen offset:1024 lds
v_mfma_f32_16x16x32_bf16 v[80:83],a[128:131],v[160:163],v[36:39]
v_exp_f32 v64,v64
v_mfma_f32_16x16x32_bf16 v[80:83],a[132:135],v[168:171],v[80:83]
v_exp_f32 v65,v65
v_mfma_f32_16x16x32_bf16 v[80:83],a[136:139],v[176:179],v[80:83]
v_exp_f32 v66,v66
v_mfma_f32_16x16x32_bf16 v[80:83],a[140:143],v[184:187],v[80:83]
v_exp_f32 v67,v67
v_mfma_f32_16x16x32_bf16 v[84:87],a[156:159],v[184:187],v[36:39]
v_exp_f32 v68,v68
v_mfma_f32_16x16x32_bf16 v[84:87],a[152:155],v[176:179],v[84:87]
v_exp_f32 v69,v69
v_mfma_f32_16x16x32_bf16 v[84:87],a[148:151],v[168:171],v[84:87]
v_exp_f32 v70,v70
v_mfma_f32_16x16x32_bf16 v[84:87],a[144:147],v[160:163],v[84:87]
v_exp_f32 v71,v71
v_mfma_f32_16x16x32_bf16 v[88:91],a[160:163],v[160:163],v[36:39]
v_cvt_pk_bf16_f32 v64,v64,v65
v_cvt_pk_bf16_f32 v65,v66,v67
v_mfma_f32_16x16x32_bf16 v[88:91],a[164:167],v[168:171],v[88:91]
buffer_store_dwordx4 a[96:99],v14,s[40:43],s79 offen offset:3072 sc1
v_mfma_f32_16x16x32_bf16 v[88:91],a[168:171],v[176:179],v[88:91]
v_cvt_pk_bf16_f32 v66,v68,v69
v_cvt_pk_bf16_f32 v67,v70,v71
v_mfma_f32_16x16x32_bf16 v[88:91],a[172:175],v[184:187],v[88:91]
buffer_load_dwordx4 v26,s[48:51],s84 offen offset:2048 lds
v_mfma_f32_16x16x32_bf16 v[92:95],a[188:191],v[184:187],v[36:39]
v_exp_f32 v72,v72
v_mfma_f32_16x16x32_bf16 v[92:95],a[184:187],v[176:179],v[92:95]
v_exp_f32 v73,v73
v_mfma_f32_16x16x32_bf16 v[92:95],a[180:183],v[168:171],v[92:95]
v_exp_f32 v74,v74
v_mfma_f32_16x16x32_bf16 v[92:95],a[176:179],v[160:163],v[92:95]
v_exp_f32 v75,v75
v_mfma_f32_16x16x32_bf16 a[32:35],a[192:195],v[112:115],a[32:35]
ds_read_b64_tr_b16 a[192:193],v2
ds_read_b64_tr_b16 a[194:195],v2 offset:4096
v_mfma_f32_16x16x32_bf16 a[32:35],a[196:199],v[120:123],a[32:35]
ds_read_b64_tr_b16 a[196:197],v2 offset:8192
v_cvt_pk_bf16_f32 v72,v72,v73
v_mfma_f32_16x16x32_bf16 a[36:39],a[200:203],v[112:115],a[36:39]
ds_read_b64_tr_b16 a[198:199],v2 offset:12288
v_cvt_pk_bf16_f32 v73,v74,v75
v_mfma_f32_16x16x32_bf16 a[36:39],a[204:207],v[120:123],a[36:39]
buffer_load_dwordx4 v27,s[48:51],s84 offen offset:3072 lds
v_mfma_f32_16x16x32_bf16 a[40:43],a[208:211],v[112:115],a[40:43]
ds_read_b64_tr_b16 a[200:201],v3
ds_read_b64_tr_b16 a[202:203],v3 offset:4096
v_mfma_f32_16x16x32_bf16 a[40:43],a[212:215],v[120:123],a[40:43]
v_exp_f32 v76,v76
v_mfma_f32_16x16x32_bf16 a[44:47],a[216:219],v[112:115],a[44:47]
ds_read_b64_tr_b16 a[204:205],v3 offset:8192
ds_read_b64_tr_b16 a[206:207],v3 offset:12288
v_mfma_f32_16x16x32_bf16 a[44:47],a[220:223],v[120:123],a[44:47]
v_exp_f32 v77,v77
v_mfma_f32_16x16x32_bf16 a[48:51],a[224:227],v[112:115],a[48:51]
ds_read_b64_tr_b16 a[208:209],v2 offset:256
ds_read_b64_tr_b16 a[210:211],v2 offset:4352
v_mfma_f32_16x16x32_bf16 a[48:51],a[228:231],v[120:123],a[48:51]
ds_read_b64_tr_b16 a[212:213],v2 offset:8448
v_cvt_pk_bf16_f32 v74,v76,v77
v_mfma_f32_16x16x32_bf16 a[52:55],a[232:235],v[112:115],a[52:55]
buffer_store_dwordx4 a[100:103],v6,s[40:43],s79 offen offset:1024 sc1
v_mfma_f32_16x16x32_bf16 a[52:55],a[236:239],v[120:123],a[52:55]
s_add_u32 m0,s86,132096
ds_read_b64_tr_b16 a[214:215],v2 offset:12544
ds_read_b64_tr_b16 a[216:217],v3 offset:256
v_mfma_f32_16x16x32_bf16 a[56:59],a[240:243],v[112:115],a[56:59]
v_exp_f32 v78,v78
v_mfma_f32_16x16x32_bf16 a[56:59],a[244:247],v[120:123],a[56:59]
buffer_load_dwordx4 v7,s[48:51],s84 offen lds
v_mfma_f32_16x16x32_bf16 a[60:63],a[248:251],v[112:115],a[60:63]
s_add_u32 s84,s83,20480
ds_read_b64_tr_b16 a[218:219],v3 offset:4352
ds_read_b64_tr_b16 a[220:221],v3 offset:8448
v_mfma_f32_16x16x32_bf16 a[60:63],a[252:255],v[120:123],a[60:63]
v_exp_f32 v79,v79
v_mfma_f32_16x16x32_bf16 v[60:63],v[28:31],v[112:115],v[60:63]
ds_read_b64_tr_b16 a[222:223],v3 offset:12544
v_cvt_pk_bf16_f32 v75,v78,v79
v_mfma_f32_16x16x32_bf16 v[60:63],v[28:31],v[120:123],v[60:63]
ds_read_b64_tr_b16 a[224:225],v2 offset:512
ds_read_b64_tr_b16 a[226:227],v2 offset:4608
v_mfma_f32_16x16x32_bf16 v[96:99],a[128:131],v[192:195],v[40:43]
v_exp_f32 v80,v80
v_mfma_f32_16x16x32_bf16 v[96:99],a[132:135],v[200:203],v[96:99]
ds_read_b64_tr_b16 a[228:229],v2 offset:8704
ds_read_b64_tr_b16 a[230:231],v2 offset:12800
v_mfma_f32_16x16x32_bf16 v[96:99],a[136:139],v[208:211],v[96:99]
v_exp_f32 v81,v81
v_mfma_f32_16x16x32_bf16 v[96:99],a[140:143],v[216:219],v[96:99]
ds_read_b64_tr_b16 a[232:233],v3 offset:512
ds_read_b64_tr_b16 a[234:235],v3 offset:4608
v_mfma_f32_16x16x32_bf16 v[100:103],a[156:159],v[216:219],v[40:43]
ds_read_b64_tr_b16 a[236:237],v3 offset:8704
v_cvt_pk_bf16_f32 v80,v80,v81
v_mfma_f32_16x16x32_bf16 v[100:103],a[152:155],v[208:211],v[100:103]
v_exp_f32 v82,v82
v_mfma_f32_16x16x32_bf16 v[100:103],a[148:151],v[200:203],v[100:103]
ds_read_b64_tr_b16 a[238:239],v3 offset:12800
ds_read_b64_tr_b16 a[240:241],v2 offset:768
v_mfma_f32_16x16x32_bf16 v[100:103],a[144:147],v[192:195],v[100:103]
v_exp_f32 v83,v83
v_mfma_f32_16x16x32_bf16 v[104:107],a[160:163],v[192:195],v[40:43]
ds_read_b64_tr_b16 a[242:243],v2 offset:4864
ds_read_b64_tr_b16 a[244:245],v2 offset:8960
v_mfma_f32_16x16x32_bf16 v[104:107],a[164:167],v[200:203],v[104:107]
buffer_store_dwordx4 a[104:107],v14,s[40:43],s80 offen offset:3072 sc1
v_mfma_f32_16x16x32_bf16 v[104:107],a[168:171],v[208:211],v[104:107]
ds_read_b64_tr_b16 a[246:247],v2 offset:13056
ds_read_b64_tr_b16 a[248:249],v3 offset:768
v_mfma_f32_16x16x32_bf16 v[104:107],a[172:175],v[216:219],v[104:107]
ds_read_b64_tr_b16 a[250:251],v3 offset:4864
v_cvt_pk_bf16_f32 v81,v82,v83
v_mfma_f32_16x16x32_bf16 v[108:111],a[188:191],v[216:219],v[40:43]
ds_read_b64_tr_b16 a[252:253],v3 offset:8960
v_add_u32 v2,s78,v2
v_mfma_f32_16x16x32_bf16 v[108:111],a[184:187],v[208:211],v[108:111]
s_waitcnt lgkmcnt(6)
ds_read_b64_tr_b16 a[254:255],v3 offset:13056
v_add_u32 v3,s78,v3
v_mfma_f32_16x16x32_bf16 v[108:111],a[180:183],v[200:203],v[108:111]
s_sub_u32 s78,s77,32768
v_exp_f32 v84,v84
v_mfma_f32_16x16x32_bf16 v[108:111],a[176:179],v[192:195],v[108:111]
s_and_b32 s78,s78,0x1ffff
v_exp_f32 v85,v85
v_mfma_f32_16x16x32_bf16 a[0:3],a[192:195],v[64:67],a[0:3]
s_sub_u32 s78,s78,s77
v_exp_f32 v86,v86
v_mfma_f32_16x16x32_bf16 a[0:3],a[196:199],v[72:75],a[0:3]
s_add_u32 s77,s77,s78
v_exp_f32 v87,v87
v_mfma_f32_16x16x32_bf16 a[4:7],a[200:203],v[64:67],a[4:7]
v_cvt_pk_bf16_f32 v82,v84,v85
v_cvt_pk_bf16_f32 v83,v86,v87
v_mfma_f32_16x16x32_bf16 a[4:7],a[204:207],v[72:75],a[4:7]
v_exp_f32 v88,v88
v_mfma_f32_16x16x32_bf16 a[8:11],a[208:211],v[64:67],a[8:11]
v_exp_f32 v89,v89
v_mfma_f32_16x16x32_bf16 a[8:11],a[212:215],v[72:75],a[8:11]
v_exp_f32 v90,v90
v_mfma_f32_16x16x32_bf16 a[12:15],a[216:219],v[64:67],a[12:15]
v_exp_f32 v91,v91
v_mfma_f32_16x16x32_bf16 a[12:15],a[220:223],v[72:75],a[12:15]
v_exp_f32 v92,v92
v_mfma_f32_16x16x32_bf16 a[16:19],a[224:227],v[64:67],a[16:19]
v_exp_f32 v93,v93
v_mfma_f32_16x16x32_bf16 a[16:19],a[228:231],v[72:75],a[16:19]
v_exp_f32 v94,v94
v_mfma_f32_16x16x32_bf16 a[20:23],a[232:235],v[64:67],a[20:23]
buffer_store_dwordx4 a[108:111],v6,s[40:43],s80 offen offset:1024 sc1
v_mfma_f32_16x16x32_bf16 a[20:23],a[236:239],v[72:75],a[20:23]
s_waitcnt lgkmcnt(0)
v_exp_f32 v95,v95
v_mfma_f32_16x16x32_bf16 a[24:27],a[240:243],v[64:67],a[24:27]
v_cvt_pk_bf16_f32 v88,v88,v89
v_cvt_pk_bf16_f32 v89,v90,v91
v_mfma_f32_16x16x32_bf16 a[24:27],a[244:247],v[72:75],a[24:27]
v_cvt_pk_bf16_f32 v90,v92,v93
v_cvt_pk_bf16_f32 v91,v94,v95
v_mfma_f32_16x16x32_bf16 a[28:31],a[248:251],v[64:67],a[28:31]
v_exp_f32 v96,v96
v_mfma_f32_16x16x32_bf16 a[28:31],a[252:255],v[72:75],a[28:31]
v_exp_f32 v97,v97
v_mfma_f32_16x16x32_bf16 v[48:51],v[28:31],v[64:67],v[48:51]
v_exp_f32 v98,v98
v_mfma_f32_16x16x32_bf16 v[48:51],v[28:31],v[72:75],v[48:51]
v_exp_f32 v99,v99
s_waitcnt vmcnt(26)
s_barrier
v_mfma_f32_16x16x32_bf16 v[112:115],a[128:131],v[224:227],v[44:47]
ds_read_b128 a[128:131],v1
v_cvt_pk_bf16_f32 v96,v96,v97
v_mfma_f32_16x16x32_bf16 v[112:115],a[132:135],v[232:235],v[112:115]
v_exp_f32 v100,v100
v_mfma_f32_16x16x32_bf16 v[112:115],a[136:139],v[240:243],v[112:115]
ds_read_b128 a[132:135],v1 offset:256
v_cvt_pk_bf16_f32 v97,v98,v99
v_mfma_f32_16x16x32_bf16 v[112:115],a[140:143],v[248:251],v[112:115]
v_exp_f32 v101,v101
v_mfma_f32_16x16x32_bf16 v[116:119],a[156:159],v[248:251],v[44:47]
ds_read_b128 a[136:139],v1 offset:512
v_cvt_pk_bf16_f32 v98,v100,v101
v_mfma_f32_16x16x32_bf16 v[116:119],a[152:155],v[240:243],v[116:119]
s_add_u32 m0,s86,81920
v_exp_f32 v102,v102
v_mfma_f32_16x16x32_bf16 v[116:119],a[148:151],v[232:235],v[116:119]
v_exp_f32 v103,v103
v_mfma_f32_16x16x32_bf16 v[116:119],a[144:147],v[224:227],v[116:119]
buffer_load_dwordx4 v24,s[48:51],s83 offen lds
v_mfma_f32_16x16x32_bf16 v[120:123],a[160:163],v[224:227],v[44:47]
ds_read_b128 a[140:143],v1 offset:768
v_cvt_pk_bf16_f32 v99,v102,v103
v_mfma_f32_16x16x32_bf16 v[120:123],a[164:167],v[232:235],v[120:123]
buffer_store_dwordx4 a[64:67],v6,s[40:43],s79 offen offset:2048 sc1
v_mfma_f32_16x16x32_bf16 v[120:123],a[168:171],v[240:243],v[120:123]
v_exp_f32 v104,v104
v_mfma_f32_16x16x32_bf16 v[120:123],a[172:175],v[248:251],v[120:123]
v_exp_f32 v105,v105
v_mfma_f32_16x16x32_bf16 v[124:127],a[188:191],v[248:251],v[44:47]
ds_read_b128 a[156:159],v1 offset:4864
v_cvt_pk_bf16_f32 v104,v104,v105
v_mfma_f32_16x16x32_bf16 v[124:127],a[184:187],v[240:243],v[124:127]
v_exp_f32 v106,v106
v_mfma_f32_16x16x32_bf16 v[124:127],a[180:183],v[232:235],v[124:127]
v_exp_f32 v107,v107
v_mfma_f32_16x16x32_bf16 v[124:127],a[176:179],v[224:227],v[124:127]
ds_read_b128 a[152:155],v1 offset:4608
v_cvt_pk_bf16_f32 v105,v106,v107
v_mfma_f32_16x16x32_bf16 v[132:135],a[192:195],v[80:83],v[132:135]
v_exp_f32 v108,v108
v_mfma_f32_16x16x32_bf16 v[132:135],a[196:199],v[88:91],v[132:135]
buffer_load_dwordx4 v25,s[48:51],s83 offen offset:1024 lds
v_mfma_f32_16x16x32_bf16 v[140:143],a[200:203],v[80:83],v[140:143]
v_exp_f32 v109,v109
v_mfma_f32_16x16x32_bf16 v[140:143],a[204:207],v[88:91],v[140:143]
ds_read_b128 a[148:151],v1 offset:4352
v_cvt_pk_bf16_f32 v106,v108,v109
v_mfma_f32_16x16x32_bf16 v[148:151],a[208:211],v[80:83],v[148:151]
v_exp_f32 v110,v110
v_mfma_f32_16x16x32_bf16 v[148:151],a[212:215],v[88:91],v[148:151]
v_exp_f32 v111,v111
v_mfma_f32_16x16x32_bf16 v[156:159],a[216:219],v[80:83],v[156:159]
ds_read_b128 a[144:147],v1 offset:4096
v_cvt_pk_bf16_f32 v107,v110,v111
v_mfma_f32_16x16x32_bf16 v[156:159],a[220:223],v[88:91],v[156:159]
v_exp_f32 v112,v112
v_mfma_f32_16x16x32_bf16 v[164:167],a[224:227],v[80:83],v[164:167]
v_exp_f32 v113,v113
v_mfma_f32_16x16x32_bf16 v[164:167],a[228:231],v[88:91],v[164:167]
buffer_store_dwordx4 a[68:71],v6,s[40:43],s79 offen sc1
v_mfma_f32_16x16x32_bf16 v[172:175],a[232:235],v[80:83],v[172:175]
s_add_u32 s79,s79,65536
ds_read_b128 a[160:163],v1 offset:8192
v_cvt_pk_bf16_f32 v112,v112,v113
v_mfma_f32_16x16x32_bf16 v[172:175],a[236:239],v[88:91],v[172:175]
buffer_load_dwordx4 v26,s[48:51],s83 offen offset:2048 lds
v_mfma_f32_16x16x32_bf16 v[180:183],a[240:243],v[80:83],v[180:183]
v_exp_f32 v114,v114
v_mfma_f32_16x16x32_bf16 v[180:183],a[244:247],v[88:91],v[180:183]
v_exp_f32 v115,v115
v_mfma_f32_16x16x32_bf16 v[188:191],a[248:251],v[80:83],v[188:191]
ds_read_b128 a[164:167],v1 offset:8448
v_cvt_pk_bf16_f32 v113,v114,v115
v_mfma_f32_16x16x32_bf16 v[188:191],a[252:255],v[88:91],v[188:191]
v_exp_f32 v116,v116
v_mfma_f32_16x16x32_bf16 v[52:55],v[28:31],v[80:83],v[52:55]
ds_read_b128 a[168:171],v1 offset:8704
)A"
R"A(v_mfma_f32_16x16x32_bf16 v[52:55],v[28:31],v[88:91],v[52:55]
s_waitcnt lgkmcnt(3)
v_exp_f32 v117,v117
v_mfma_f32_16x16x32_bf16 v[64:67],a[128:131],v[128:131],v[32:35]
ds_read_b128 a[172:175],v1 offset:8960
v_cvt_pk_bf16_f32 v114,v116,v117
v_mfma_f32_16x16x32_bf16 v[64:67],a[132:135],v[136:139],v[64:67]
v_exp_f32 v118,v118
v_mfma_f32_16x16x32_bf16 v[64:67],a[136:139],v[144:147],v[64:67]
ds_read_b128 a[188:191],v1 offset:13056
v_mfma_f32_16x16x32_bf16 v[64:67],a[140:143],v[152:155],v[64:67]
buffer_load_dwordx4 v27,s[48:51],s83 offen offset:3072 lds
v_mfma_f32_16x16x32_bf16 v[68:71],a[156:159],v[152:155],v[32:35]
ds_read_b128 a[184:187],v1 offset:12800
v_mfma_f32_16x16x32_bf16 v[68:71],a[152:155],v[144:147],v[68:71]
v_exp_f32 v119,v119
v_mfma_f32_16x16x32_bf16 v[68:71],a[148:151],v[136:139],v[68:71]
ds_read_b128 a[180:183],v1 offset:12544
v_cvt_pk_bf16_f32 v115,v118,v119
v_mfma_f32_16x16x32_bf16 v[68:71],a[144:147],v[128:131],v[68:71]
s_waitcnt lgkmcnt(4)
v_exp_f32 v120,v120
v_mfma_f32_16x16x32_bf16 v[72:75],a[160:163],v[128:131],v[32:35]
buffer_store_dwordx4 a[72:75],v6,s[40:43],s80 offen offset:2048 sc1
v_mfma_f32_16x16x32_bf16 v[72:75],a[164:167],v[136:139],v[72:75]
ds_read_b128 a[176:179],v1 offset:12288
v_add_u32 v1,s78,v1
v_mfma_f32_16x16x32_bf16 v[72:75],a[168:171],v[144:147],v[72:75]
s_waitcnt lgkmcnt(3)
v_exp_f32 v121,v121
v_mfma_f32_16x16x32_bf16 v[72:75],a[172:175],v[152:155],v[72:75]
s_add_u32 m0,s86,86016
v_exp_f32 v122,v122
v_mfma_f32_16x16x32_bf16 v[76:79],a[188:191],v[152:155],v[32:35]
s_waitcnt lgkmcnt(0)
v_exp_f32 v123,v123
v_mfma_f32_16x16x32_bf16 v[76:79],a[184:187],v[144:147],v[76:79]
buffer_load_dwordx4 v7,s[48:51],s83 offen lds
v_mfma_f32_16x16x32_bf16 v[76:79],a[180:183],v[136:139],v[76:79]
s_add_u32 s83,s64,1
v_exp_f32 v124,v124
v_mfma_f32_16x16x32_bf16 v[76:79],a[176:179],v[128:131],v[76:79]
s_and_b32 s83,s83,3
v_exp_f32 v125,v125
v_mfma_f32_16x16x32_bf16 v[196:199],a[192:195],v[96:99],v[196:199]
s_lshl_b32 s83,s83,1
v_exp_f32 v126,v126
v_mfma_f32_16x16x32_bf16 v[196:199],a[196:199],v[104:107],v[196:199]
s_mul_i32 s83,s83,163840
v_exp_f32 v127,v127
v_mfma_f32_16x16x32_bf16 v[204:207],a[200:203],v[96:99],v[204:207]
s_add_u32 s83,s83,s85
v_cvt_pk_bf16_f32 v120,v120,v121
v_cvt_pk_bf16_f32 v121,v122,v123
v_mfma_f32_16x16x32_bf16 v[204:207],a[204:207],v[104:107],v[204:207]
v_cvt_pk_bf16_f32 v122,v124,v125
v_cvt_pk_bf16_f32 v123,v126,v127
v_mfma_f32_16x16x32_bf16 v[212:215],a[208:211],v[96:99],v[212:215]
v_exp_f32 v64,v64
v_mfma_f32_16x16x32_bf16 v[212:215],a[212:215],v[104:107],v[212:215]
s_add_u32 m0,s86,87040
v_exp_f32 v65,v65
v_mfma_f32_16x16x32_bf16 v[220:223],a[216:219],v[96:99],v[220:223]
v_exp_f32 v66,v66
v_mfma_f32_16x16x32_bf16 v[220:223],a[220:223],v[104:107],v[220:223]
buffer_load_dwordx4 v24,s[48:51],s84 offen lds
v_mfma_f32_16x16x32_bf16 v[228:231],a[224:227],v[96:99],v[228:231]
v_exp_f32 v67,v67
v_mfma_f32_16x16x32_bf16 v[228:231],a[228:231],v[104:107],v[228:231]
buffer_store_dwordx4 a[76:79],v6,s[40:43],s80 offen sc1
v_mfma_f32_16x16x32_bf16 v[236:239],a[232:235],v[96:99],v[236:239]
s_add_u32 s80,s80,65536
v_exp_f32 v68,v68
v_mfma_f32_16x16x32_bf16 v[236:239],a[236:239],v[104:107],v[236:239]
v_exp_f32 v69,v69
v_mfma_f32_16x16x32_bf16 v[244:247],a[240:243],v[96:99],v[244:247]
v_exp_f32 v70,v70
v_mfma_f32_16x16x32_bf16 v[244:247],a[244:247],v[104:107],v[244:247]
v_exp_f32 v71,v71
v_mfma_f32_16x16x32_bf16 v[252:255],a[248:251],v[96:99],v[252:255]
v_cvt_pk_bf16_f32 v64,v64,v65
v_cvt_pk_bf16_f32 v65,v66,v67
v_mfma_f32_16x16x32_bf16 v[252:255],a[252:255],v[104:107],v[252:255]
v_cvt_pk_bf16_f32 v66,v68,v69
v_cvt_pk_bf16_f32 v67,v70,v71
v_mfma_f32_16x16x32_bf16 v[56:59],v[28:31],v[96:99],v[56:59]
v_exp_f32 v72,v72
v_mfma_f32_16x16x32_bf16 v[56:59],v[28:31],v[104:107],v[56:59]
buffer_load_dwordx4 v25,s[48:51],s84 offen offset:1024 lds
v_mfma_f32_16x16x32_bf16 v[80:83],a[128:131],v[160:163],v[36:39]
v_exp_f32 v73,v73
v_mfma_f32_16x16x32_bf16 v[80:83],a[132:135],v[168:171],v[80:83]
v_exp_f32 v74,v74
v_mfma_f32_16x16x32_bf16 v[80:83],a[136:139],v[176:179],v[80:83]
v_exp_f32 v75,v75
v_mfma_f32_16x16x32_bf16 v[80:83],a[140:143],v[184:187],v[80:83]
v_exp_f32 v76,v76
v_mfma_f32_16x16x32_bf16 v[84:87],a[156:159],v[184:187],v[36:39]
v_exp_f32 v77,v77
v_mfma_f32_16x16x32_bf16 v[84:87],a[152:155],v[176:179],v[84:87]
v_exp_f32 v78,v78
v_mfma_f32_16x16x32_bf16 v[84:87],a[148:151],v[168:171],v[84:87]
v_exp_f32 v79,v79
v_mfma_f32_16x16x32_bf16 v[84:87],a[144:147],v[160:163],v[84:87]
v_cvt_pk_bf16_f32 v72,v72,v73
v_cvt_pk_bf16_f32 v73,v74,v75
v_mfma_f32_16x16x32_bf16 v[88:91],a[160:163],v[160:163],v[36:39]
buffer_store_dwordx4 a[80:83],v6,s[40:43],s81 offen offset:2048 sc1
v_mfma_f32_16x16x32_bf16 v[88:91],a[164:167],v[168:171],v[88:91]
v_cvt_pk_bf16_f32 v74,v76,v77
v_cvt_pk_bf16_f32 v75,v78,v79
v_mfma_f32_16x16x32_bf16 v[88:91],a[168:171],v[176:179],v[88:91]
buffer_load_dwordx4 v26,s[48:51],s84 offen offset:2048 lds
v_mfma_f32_16x16x32_bf16 v[88:91],a[172:175],v[184:187],v[88:91]
v_exp_f32 v80,v80
v_mfma_f32_16x16x32_bf16 v[92:95],a[188:191],v[184:187],v[36:39]
v_exp_f32 v81,v81
v_mfma_f32_16x16x32_bf16 v[92:95],a[184:187],v[176:179],v[92:95]
v_exp_f32 v82,v82
v_mfma_f32_16x16x32_bf16 v[92:95],a[180:183],v[168:171],v[92:95]
v_exp_f32 v83,v83
v_mfma_f32_16x16x32_bf16 v[92:95],a[176:179],v[160:163],v[92:95]
v_exp_f32 v84,v84
v_mfma_f32_16x16x32_bf16 a[32:35],a[192:195],v[112:115],a[32:35]
ds_read_b64_tr_b16 a[192:193],v2
ds_read_b64_tr_b16 a[194:195],v2 offset:4096
v_mfma_f32_16x16x32_bf16 a[32:35],a[196:199],v[120:123],a[32:35]
ds_read_b64_tr_b16 a[196:197],v2 offset:8192
v_cvt_pk_bf16_f32 v80,v80,v81
v_mfma_f32_16x16x32_bf16 a[36:39],a[200:203],v[112:115],a[36:39]
ds_read_b64_tr_b16 a[198:199],v2 offset:12288
v_cvt_pk_bf16_f32 v81,v82,v83
v_mfma_f32_16x16x32_bf16 a[36:39],a[204:207],v[120:123],a[36:39]
buffer_load_dwordx4 v27,s[48:51],s84 offen offset:3072 lds
v_mfma_f32_16x16x32_bf16 a[40:43],a[208:211],v[112:115],a[40:43]
ds_read_b64_tr_b16 a[200:201],v3
ds_read_b64_tr_b16 a[202:203],v3 offset:4096
v_mfma_f32_16x16x32_bf16 a[40:43],a[212:215],v[120:123],a[40:43]
v_exp_f32 v85,v85
v_mfma_f32_16x16x32_bf16 a[44:47],a[216:219],v[112:115],a[44:47]
ds_read_b64_tr_b16 a[204:205],v3 offset:8192
ds_read_b64_tr_b16 a[206:207],v3 offset:12288
v_mfma_f32_16x16x32_bf16 a[44:47],a[220:223],v[120:123],a[44:47]
ds_read_b64_tr_b16 a[208:209],v2 offset:256
v_cvt_pk_bf16_f32 v82,v84,v85
v_mfma_f32_16x16x32_bf16 a[48:51],a[224:227],v[112:115],a[48:51]
v_exp_f32 v86,v86
v_mfma_f32_16x16x32_bf16 a[48:51],a[228:231],v[120:123],a[48:51]
buffer_store_dwordx4 a[84:87],v6,s[40:43],s81 offen sc1
v_mfma_f32_16x16x32_bf16 a[52:55],a[232:235],v[112:115],a[52:55]
s_add_u32 s81,s81,65536
ds_read_b64_tr_b16 a[210:211],v2 offset:4352
ds_read_b64_tr_b16 a[212:213],v2 offset:8448
v_mfma_f32_16x16x32_bf16 a[52:55],a[236:239],v[120:123],a[52:55]
s_add_u32 m0,s86,91136
v_exp_f32 v87,v87
v_mfma_f32_16x16x32_bf16 a[56:59],a[240:243],v[112:115],a[56:59]
ds_read_b64_tr_b16 a[214:215],v2 offset:12544
ds_read_b64_tr_b16 a[216:217],v3 offset:256
v_mfma_f32_16x16x32_bf16 a[56:59],a[244:247],v[120:123],a[56:59]
buffer_load_dwordx4 v7,s[48:51],s84 offen lds
v_mfma_f32_16x16x32_bf16 a[60:63],a[248:251],v[112:115],a[60:63]
s_add_u32 s84,s83,20480
ds_read_b64_tr_b16 a[218:219],v3 offset:4352
ds_read_b64_tr_b16 a[220:221],v3 offset:8448
v_mfma_f32_16x16x32_bf16 a[60:63],a[252:255],v[120:123],a[60:63]
ds_read_b64_tr_b16 a[222:223],v3 offset:12544
v_cvt_pk_bf16_f32 v83,v86,v87
v_mfma_f32_16x16x32_bf16 v[60:63],v[28:31],v[112:115],v[60:63]
v_exp_f32 v88,v88
v_mfma_f32_16x16x32_bf16 v[60:63],v[28:31],v[120:123],v[60:63]
ds_read_b64_tr_b16 a[224:225],v2 offset:512
ds_read_b64_tr_b16 a[226:227],v2 offset:4608
v_mfma_f32_16x16x32_bf16 v[96:99],a[128:131],v[192:195],v[40:43]
v_exp_f32 v89,v89
v_mfma_f32_16x16x32_bf16 v[96:99],a[132:135],v[200:203],v[96:99]
ds_read_b64_tr_b16 a[228:229],v2 offset:8704
ds_read_b64_tr_b16 a[230:231],v2 offset:12800
v_mfma_f32_16x16x32_bf16 v[96:99],a[136:139],v[208:211],v[96:99]
ds_read_b64_tr_b16 a[232:233],v3 offset:512
v_cvt_pk_bf16_f32 v88,v88,v89
v_mfma_f32_16x16x32_bf16 v[96:99],a[140:143],v[216:219],v[96:99]
v_exp_f32 v90,v90
v_mfma_f32_16x16x32_bf16 v[100:103],a[156:159],v[216:219],v[40:43]
ds_read_b64_tr_b16 a[234:235],v3 offset:4608
ds_read_b64_tr_b16 a[236:237],v3 offset:8704
v_mfma_f32_16x16x32_bf16 v[100:103],a[152:155],v[208:211],v[100:103]
v_exp_f32 v91,v91
v_mfma_f32_16x16x32_bf16 v[100:103],a[148:151],v[200:203],v[100:103]
ds_read_b64_tr_b16 a[238:239],v3 offset:12800
ds_read_b64_tr_b16 a[240:241],v2 offset:768
v_mfma_f32_16x16x32_bf16 v[100:103],a[144:147],v[192:195],v[100:103]
ds_read_b64_tr_b16 a[242:243],v2 offset:4864
v_cvt_pk_bf16_f32 v89,v90,v91
v_mfma_f32_16x16x32_bf16 v[104:107],a[160:163],v[192:195],v[40:43]
buffer_store_dwordx4 a[88:91],v6,s[40:43],s82 offen offset:2048 sc1
v_mfma_f32_16x16x32_bf16 v[104:107],a[164:167],v[200:203],v[104:107]
ds_read_b64_tr_b16 a[244:245],v2 offset:8960
ds_read_b64_tr_b16 a[246:247],v2 offset:13056
v_mfma_f32_16x16x32_bf16 v[104:107],a[168:171],v[208:211],v[104:107]
ds_read_b64_tr_b16 a[248:249],v3 offset:768
v_add_u32 v2,s78,v2
v_mfma_f32_16x16x32_bf16 v[104:107],a[172:175],v[216:219],v[104:107]
v_exp_f32 v92,v92
v_mfma_f32_16x16x32_bf16 v[108:111],a[188:191],v[216:219],v[40:43]
ds_read_b64_tr_b16 a[250:251],v3 offset:4864
ds_read_b64_tr_b16 a[252:253],v3 offset:8960
v_mfma_f32_16x16x32_bf16 v[108:111],a[184:187],v[208:211],v[108:111]
s_waitcnt lgkmcnt(5)
ds_read_b64_tr_b16 a[254:255],v3 offset:13056
v_add_u32 v3,s78,v3
v_mfma_f32_16x16x32_bf16 v[108:111],a[180:183],v[200:203],v[108:111]
s_sub_u32 s78,s77,32768
v_exp_f32 v93,v93
v_mfma_f32_16x16x32_bf16 v[108:111],a[176:179],v[192:195],v[108:111]
s_and_b32 s78,s78,0x1ffff
v_exp_f32 v94,v94
v_mfma_f32_16x16x32_bf16 a[0:3],a[192:195],v[64:67],a[0:3]
s_sub_u32 s78,s78,s77
v_exp_f32 v95,v95
v_mfma_f32_16x16x32_bf16 a[0:3],a[196:199],v[72:75],a[0:3]
s_add_u32 s77,s77,s78
v_cvt_pk_bf16_f32 v90,v92,v93
v_cvt_pk_bf16_f32 v91,v94,v95
v_mfma_f32_16x16x32_bf16 a[4:7],a[200:203],v[64:67],a[4:7]
s_cmp_ge_u32 s63,2
s_cselect_b32 s6,L_gbS_%=-L_pcg_%=,s6
s_cselect_b32 s10,L_geS_%=-L_gbS_%=,s10
v_mfma_f32_16x16x32_bf16 a[4:7],a[204:207],v[72:75],a[4:7]
s_add_u32 s8,s8,s6
s_addc_u32 s9,s9,0
s_and_b32 s8,s8,0xffffff80
v_mfma_f32_16x16x32_bf16 a[8:11],a[208:211],v[64:67],a[8:11]
s_and_b32 s9,s9,0xffff
v_exp_f32 v96,v96
v_mfma_f32_16x16x32_bf16 a[8:11],a[212:215],v[72:75],a[8:11]
v_exp_f32 v97,v97
v_mfma_f32_16x16x32_bf16 a[12:15],a[216:219],v[64:67],a[12:15]
v_exp_f32 v98,v98
v_mfma_f32_16x16x32_bf16 a[12:15],a[220:223],v[72:75],a[12:15]
v_exp_f32 v99,v99
v_mfma_f32_16x16x32_bf16 a[16:19],a[224:227],v[64:67],a[16:19]
v_exp_f32 v100,v100
v_mfma_f32_16x16x32_bf16 a[16:19],a[228:231],v[72:75],a[16:19]
buffer_store_dwordx4 a[92:95],v6,s[40:43],s82 offen sc1
v_mfma_f32_16x16x32_bf16 a[20:23],a[232:235],v[64:67],a[20:23]
s_add_u32 s82,s82,65536
v_exp_f32 v101,v101
v_mfma_f32_16x16x32_bf16 a[20:23],a[236:239],v[72:75],a[20:23]
v_exp_f32 v102,v102
v_mfma_f32_16x16x32_bf16 a[24:27],a[240:243],v[64:67],a[24:27]
s_waitcnt lgkmcnt(0)
v_exp_f32 v103,v103
v_mfma_f32_16x16x32_bf16 a[24:27],a[244:247],v[72:75],a[24:27]
v_cvt_pk_bf16_f32 v96,v96,v97
v_cvt_pk_bf16_f32 v97,v98,v99
v_mfma_f32_16x16x32_bf16 a[28:31],a[248:251],v[64:67],a[28:31]
v_cvt_pk_bf16_f32 v98,v100,v101
v_cvt_pk_bf16_f32 v99,v102,v103
v_mfma_f32_16x16x32_bf16 a[28:31],a[252:255],v[72:75],a[28:31]
v_exp_f32 v104,v104
v_mfma_f32_16x16x32_bf16 v[48:51],v[28:31],v[64:67],v[48:51]
v_exp_f32 v105,v105
v_mfma_f32_16x16x32_bf16 v[48:51],v[28:31],v[72:75],v[48:51]
v_exp_f32 v106,v106
s_waitcnt vmcnt(36)
s_barrier
v_mfma_f32_16x16x32_bf16 v[112:115],a[128:131],v[224:227],v[44:47]
ds_read_b128 a[128:131],v1
v_cvt_pk_bf16_f32 v104,v104,v105
v_mfma_f32_16x16x32_bf16 v[112:115],a[132:135],v[232:235],v[112:115]
v_exp_f32 v107,v107
v_mfma_f32_16x16x32_bf16 v[112:115],a[136:139],v[240:243],v[112:115]
ds_read_b128 a[132:135],v1 offset:256
v_cvt_pk_bf16_f32 v105,v106,v107
)A"
R"A(v_mfma_f32_16x16x32_bf16 v[112:115],a[140:143],v[248:251],v[112:115]
v_exp_f32 v108,v108
v_mfma_f32_16x16x32_bf16 v[116:119],a[156:159],v[248:251],v[44:47]
v_exp_f32 v109,v109
v_mfma_f32_16x16x32_bf16 v[116:119],a[152:155],v[240:243],v[116:119]
s_add_u32 m0,s86,40960
ds_read_b128 a[136:139],v1 offset:512
v_cvt_pk_bf16_f32 v106,v108,v109
v_mfma_f32_16x16x32_bf16 v[116:119],a[148:151],v[232:235],v[116:119]
v_exp_f32 v110,v110
v_mfma_f32_16x16x32_bf16 v[116:119],a[144:147],v[224:227],v[116:119]
buffer_load_dwordx4 v24,s[48:51],s83 offen lds
v_mfma_f32_16x16x32_bf16 v[120:123],a[160:163],v[224:227],v[44:47]
v_exp_f32 v111,v111
v_mfma_f32_16x16x32_bf16 v[120:123],a[164:167],v[232:235],v[120:123]
ds_read_b128 a[140:143],v1 offset:768
v_cvt_pk_bf16_f32 v107,v110,v111
v_mfma_f32_16x16x32_bf16 v[120:123],a[168:171],v[240:243],v[120:123]
v_exp_f32 v112,v112
v_mfma_f32_16x16x32_bf16 v[120:123],a[172:175],v[248:251],v[120:123]
v_exp_f32 v113,v113
s_nop 0
L_pd12_%=:
s_cbranch_execz L_trt1_%=
v_mfma_f32_16x16x32_bf16 v[124:127],a[188:191],v[248:251],v[44:47]
ds_read_b128 a[156:159],v1 offset:4864
v_cvt_pk_bf16_f32 v112,v112,v113
v_mfma_f32_16x16x32_bf16 v[124:127],a[184:187],v[240:243],v[124:127]
v_exp_f32 v114,v114
v_mfma_f32_16x16x32_bf16 v[124:127],a[180:183],v[232:235],v[124:127]
v_exp_f32 v115,v115
v_mfma_f32_16x16x32_bf16 v[124:127],a[176:179],v[224:227],v[124:127]
ds_read_b128 a[152:155],v1 offset:4608
v_cvt_pk_bf16_f32 v113,v114,v115
v_mfma_f32_16x16x32_bf16 v[132:135],a[192:195],v[80:83],v[132:135]
v_exp_f32 v116,v116
v_mfma_f32_16x16x32_bf16 v[132:135],a[196:199],v[88:91],v[132:135]
buffer_load_dwordx4 v25,s[48:51],s83 offen offset:1024 lds
v_mfma_f32_16x16x32_bf16 v[140:143],a[200:203],v[80:83],v[140:143]
v_exp_f32 v117,v117
v_mfma_f32_16x16x32_bf16 v[140:143],a[204:207],v[88:91],v[140:143]
ds_read_b128 a[148:151],v1 offset:4352
v_cvt_pk_bf16_f32 v114,v116,v117
v_mfma_f32_16x16x32_bf16 v[148:151],a[208:211],v[80:83],v[148:151]
v_exp_f32 v118,v118
v_mfma_f32_16x16x32_bf16 v[148:151],a[212:215],v[88:91],v[148:151]
v_exp_f32 v119,v119
v_mfma_f32_16x16x32_bf16 v[156:159],a[216:219],v[80:83],v[156:159]
ds_read_b128 a[144:147],v1 offset:4096
v_cvt_pk_bf16_f32 v115,v118,v119
v_mfma_f32_16x16x32_bf16 v[156:159],a[220:223],v[88:91],v[156:159]
v_exp_f32 v120,v120
v_mfma_f32_16x16x32_bf16 v[164:167],a[224:227],v[80:83],v[164:167]
buffer_load_dword v5,v4,s[8:11],s7 offen
v_mfma_f32_16x16x32_bf16 v[164:167],a[228:231],v[88:91],v[164:167]
v_exp_f32 v121,v121
v_mfma_f32_16x16x32_bf16 v[172:175],a[232:235],v[80:83],v[172:175]
ds_read_b128 a[160:163],v1 offset:8192
v_cvt_pk_bf16_f32 v120,v120,v121
v_mfma_f32_16x16x32_bf16 v[172:175],a[236:239],v[88:91],v[172:175]
v_exp_f32 v122,v122
v_mfma_f32_16x16x32_bf16 v[180:183],a[240:243],v[80:83],v[180:183]
v_exp_f32 v123,v123
v_mfma_f32_16x16x32_bf16 v[180:183],a[244:247],v[88:91],v[180:183]
ds_read_b128 a[164:167],v1 offset:8448
v_cvt_pk_bf16_f32 v121,v122,v123
v_mfma_f32_16x16x32_bf16 v[188:191],a[248:251],v[80:83],v[188:191]
buffer_load_dwordx4 v26,s[48:51],s83 offen offset:2048 lds
v_mfma_f32_16x16x32_bf16 v[188:191],a[252:255],v[88:91],v[188:191]
v_exp_f32 v124,v124
v_mfma_f32_16x16x32_bf16 v[52:55],v[28:31],v[80:83],v[52:55]
ds_read_b128 a[168:171],v1 offset:8704
v_mfma_f32_16x16x32_bf16 v[52:55],v[28:31],v[88:91],v[52:55]
s_waitcnt lgkmcnt(3)
v_exp_f32 v125,v125
v_mfma_f32_16x16x32_bf16 v[64:67],a[128:131],v[128:131],v[32:35]
ds_read_b128 a[172:175],v1 offset:8960
v_cvt_pk_bf16_f32 v122,v124,v125
v_mfma_f32_16x16x32_bf16 v[64:67],a[132:135],v[136:139],v[64:67]
v_exp_f32 v126,v126
v_mfma_f32_16x16x32_bf16 v[64:67],a[136:139],v[144:147],v[64:67]
ds_read_b128 a[188:191],v1 offset:13056
v_mfma_f32_16x16x32_bf16 v[64:67],a[140:143],v[152:155],v[64:67]
buffer_load_dwordx4 v27,s[48:51],s83 offen offset:3072 lds
v_mfma_f32_16x16x32_bf16 v[68:71],a[156:159],v[152:155],v[32:35]
ds_read_b128 a[184:187],v1 offset:12800
v_mfma_f32_16x16x32_bf16 v[68:71],a[152:155],v[144:147],v[68:71]
v_exp_f32 v127,v127
v_mfma_f32_16x16x32_bf16 v[68:71],a[148:151],v[136:139],v[68:71]
ds_read_b128 a[180:183],v1 offset:12544
v_cvt_pk_bf16_f32 v123,v126,v127
v_mfma_f32_16x16x32_bf16 v[68:71],a[144:147],v[128:131],v[68:71]
s_waitcnt lgkmcnt(4)
v_exp_f32 v64,v64
v_mfma_f32_16x16x32_bf16 v[72:75],a[160:163],v[128:131],v[32:35]
ds_read_b128 a[176:179],v1 offset:12288
v_add_u32 v1,s78,v1
v_mfma_f32_16x16x32_bf16 v[72:75],a[164:167],v[136:139],v[72:75]
v_exp_f32 v65,v65
v_mfma_f32_16x16x32_bf16 v[72:75],a[168:171],v[144:147],v[72:75]
s_waitcnt lgkmcnt(3)
v_exp_f32 v66,v66
v_mfma_f32_16x16x32_bf16 v[72:75],a[172:175],v[152:155],v[72:75]
s_add_u32 m0,s86,45056
v_exp_f32 v67,v67
v_mfma_f32_16x16x32_bf16 v[76:79],a[188:191],v[152:155],v[32:35]
s_waitcnt lgkmcnt(0)
v_exp_f32 v68,v68
v_mfma_f32_16x16x32_bf16 v[76:79],a[184:187],v[144:147],v[76:79]
buffer_load_dwordx4 v7,s[48:51],s83 offen lds
v_mfma_f32_16x16x32_bf16 v[76:79],a[180:183],v[136:139],v[76:79]
s_add_u32 s83,s64,2
v_exp_f32 v69,v69
v_mfma_f32_16x16x32_bf16 v[76:79],a[176:179],v[128:131],v[76:79]
s_and_b32 s83,s83,3
v_exp_f32 v70,v70
v_mfma_f32_16x16x32_bf16 v[196:199],a[192:195],v[96:99],v[196:199]
s_lshl_b32 s83,s83,1
v_exp_f32 v71,v71
v_mfma_f32_16x16x32_bf16 v[196:199],a[196:199],v[104:107],v[196:199]
s_mul_i32 s83,s83,163840
v_cvt_pk_bf16_f32 v64,v64,v65
v_cvt_pk_bf16_f32 v65,v66,v67
v_mfma_f32_16x16x32_bf16 v[204:207],a[200:203],v[96:99],v[204:207]
s_add_u32 s83,s83,s85
v_cvt_pk_bf16_f32 v66,v68,v69
v_cvt_pk_bf16_f32 v67,v70,v71
v_mfma_f32_16x16x32_bf16 v[204:207],a[204:207],v[104:107],v[204:207]
v_exp_f32 v72,v72
v_mfma_f32_16x16x32_bf16 v[212:215],a[208:211],v[96:99],v[212:215]
v_exp_f32 v73,v73
v_mfma_f32_16x16x32_bf16 v[212:215],a[212:215],v[104:107],v[212:215]
s_add_u32 m0,s86,46080
v_exp_f32 v74,v74
v_mfma_f32_16x16x32_bf16 v[220:223],a[216:219],v[96:99],v[220:223]
v_exp_f32 v75,v75
v_mfma_f32_16x16x32_bf16 v[220:223],a[220:223],v[104:107],v[220:223]
buffer_load_dwordx4 v24,s[48:51],s84 offen lds
v_mfma_f32_16x16x32_bf16 v[228:231],a[224:227],v[96:99],v[228:231]
v_exp_f32 v76,v76
v_mfma_f32_16x16x32_bf16 v[228:231],a[228:231],v[104:107],v[228:231]
v_exp_f32 v77,v77
v_mfma_f32_16x16x32_bf16 v[236:239],a[232:235],v[96:99],v[236:239]
v_exp_f32 v78,v78
v_mfma_f32_16x16x32_bf16 v[236:239],a[236:239],v[104:107],v[236:239]
v_exp_f32 v79,v79
v_mfma_f32_16x16x32_bf16 v[244:247],a[240:243],v[96:99],v[244:247]
v_cvt_pk_bf16_f32 v72,v72,v73
v_cvt_pk_bf16_f32 v73,v74,v75
v_mfma_f32_16x16x32_bf16 v[244:247],a[244:247],v[104:107],v[244:247]
v_cvt_pk_bf16_f32 v74,v76,v77
v_cvt_pk_bf16_f32 v75,v78,v79
v_mfma_f32_16x16x32_bf16 v[252:255],a[248:251],v[96:99],v[252:255]
v_lshrrev_b32 v1,6,v6
v_mul_u32_u24 v1,320,v1
v_mfma_f32_16x16x32_bf16 v[252:255],a[252:255],v[104:107],v[252:255]
v_mfma_f32_16x16x32_bf16 v[56:59],v[28:31],v[96:99],v[56:59]
v_mfma_f32_16x16x32_bf16 v[56:59],v[28:31],v[104:107],v[56:59]
buffer_load_dwordx4 v25,s[48:51],s84 offen offset:1024 lds
v_mfma_f32_16x16x32_bf16 v[80:83],a[128:131],v[160:163],v[36:39]
v_mfma_f32_16x16x32_bf16 v[80:83],a[132:135],v[168:171],v[80:83]
v_mfma_f32_16x16x32_bf16 v[80:83],a[136:139],v[176:179],v[80:83]
v_mfma_f32_16x16x32_bf16 v[80:83],a[140:143],v[184:187],v[80:83]
v_mfma_f32_16x16x32_bf16 v[84:87],a[156:159],v[184:187],v[36:39]
v_mfma_f32_16x16x32_bf16 v[84:87],a[152:155],v[176:179],v[84:87]
v_mfma_f32_16x16x32_bf16 v[84:87],a[148:151],v[168:171],v[84:87]
v_mfma_f32_16x16x32_bf16 v[84:87],a[144:147],v[160:163],v[84:87]
v_mfma_f32_16x16x32_bf16 v[88:91],a[160:163],v[160:163],v[36:39]
v_mfma_f32_16x16x32_bf16 v[88:91],a[164:167],v[168:171],v[88:91]
buffer_load_dwordx4 v26,s[48:51],s84 offen offset:2048 lds
v_mfma_f32_16x16x32_bf16 v[88:91],a[168:171],v[176:179],v[88:91]
v_exp_f32 v80,v80
v_mfma_f32_16x16x32_bf16 v[88:91],a[172:175],v[184:187],v[88:91]
v_exp_f32 v81,v81
v_mfma_f32_16x16x32_bf16 v[92:95],a[188:191],v[184:187],v[36:39]
v_exp_f32 v82,v82
v_mfma_f32_16x16x32_bf16 v[92:95],a[184:187],v[176:179],v[92:95]
v_exp_f32 v83,v83
v_mfma_f32_16x16x32_bf16 v[92:95],a[180:183],v[168:171],v[92:95]
v_exp_f32 v84,v84
v_mfma_f32_16x16x32_bf16 v[92:95],a[176:179],v[160:163],v[92:95]
v_exp_f32 v85,v85
v_mfma_f32_16x16x32_bf16 a[32:35],a[192:195],v[112:115],a[32:35]
ds_read_b64_tr_b16 a[192:193],v2
ds_read_b64_tr_b16 a[194:195],v2 offset:4096
v_mfma_f32_16x16x32_bf16 a[32:35],a[196:199],v[120:123],a[32:35]
ds_read_b64_tr_b16 a[196:197],v2 offset:8192
v_cvt_pk_bf16_f32 v80,v80,v81
v_mfma_f32_16x16x32_bf16 a[36:39],a[200:203],v[112:115],a[36:39]
ds_read_b64_tr_b16 a[198:199],v2 offset:12288
v_cvt_pk_bf16_f32 v81,v82,v83
v_mfma_f32_16x16x32_bf16 a[36:39],a[204:207],v[120:123],a[36:39]
buffer_load_dwordx4 v27,s[48:51],s84 offen offset:3072 lds
v_mfma_f32_16x16x32_bf16 a[40:43],a[208:211],v[112:115],a[40:43]
ds_read_b64_tr_b16 a[200:201],v3
ds_read_b64_tr_b16 a[202:203],v3 offset:4096
v_mfma_f32_16x16x32_bf16 a[40:43],a[212:215],v[120:123],a[40:43]
ds_read_b64_tr_b16 a[204:205],v3 offset:8192
v_cvt_pk_bf16_f32 v82,v84,v85
v_mfma_f32_16x16x32_bf16 a[44:47],a[216:219],v[112:115],a[44:47]
v_exp_f32 v86,v86
v_mfma_f32_16x16x32_bf16 a[44:47],a[220:223],v[120:123],a[44:47]
ds_read_b64_tr_b16 a[206:207],v3 offset:12288
ds_read_b64_tr_b16 a[208:209],v2 offset:256
v_mfma_f32_16x16x32_bf16 a[48:51],a[224:227],v[112:115],a[48:51]
v_exp_f32 v87,v87
v_mfma_f32_16x16x32_bf16 a[48:51],a[228:231],v[120:123],a[48:51]
ds_read_b64_tr_b16 a[210:211],v2 offset:4352
ds_read_b64_tr_b16 a[212:213],v2 offset:8448
v_mfma_f32_16x16x32_bf16 a[52:55],a[232:235],v[112:115],a[52:55]
ds_read_b64_tr_b16 a[214:215],v2 offset:12544
v_cvt_pk_bf16_f32 v83,v86,v87
v_mfma_f32_16x16x32_bf16 a[52:55],a[236:239],v[120:123],a[52:55]
s_add_u32 m0,s86,50176
v_exp_f32 v88,v88
v_mfma_f32_16x16x32_bf16 a[56:59],a[240:243],v[112:115],a[56:59]
ds_read_b64_tr_b16 a[216:217],v3 offset:256
ds_read_b64_tr_b16 a[218:219],v3 offset:4352
v_mfma_f32_16x16x32_bf16 a[56:59],a[244:247],v[120:123],a[56:59]
buffer_load_dwordx4 v7,s[48:51],s84 offen lds
v_mfma_f32_16x16x32_bf16 a[60:63],a[248:251],v[112:115],a[60:63]
s_add_u32 s84,s83,20480
ds_read_b64_tr_b16 a[220:221],v3 offset:8448
ds_read_b64_tr_b16 a[222:223],v3 offset:12544
v_mfma_f32_16x16x32_bf16 a[60:63],a[252:255],v[120:123],a[60:63]
v_exp_f32 v89,v89
v_mfma_f32_16x16x32_bf16 v[60:63],v[28:31],v[112:115],v[60:63]
ds_read_b64_tr_b16 a[224:225],v2 offset:512
ds_read_b64_tr_b16 a[226:227],v2 offset:4608
v_mfma_f32_16x16x32_bf16 v[60:63],v[28:31],v[120:123],v[60:63]
ds_read_b64_tr_b16 a[228:229],v2 offset:8704
v_cvt_pk_bf16_f32 v88,v88,v89
v_mfma_f32_16x16x32_bf16 v[96:99],a[128:131],v[192:195],v[40:43]
v_exp_f32 v90,v90
v_mfma_f32_16x16x32_bf16 v[96:99],a[132:135],v[200:203],v[96:99]
ds_read_b64_tr_b16 a[230:231],v2 offset:12800
ds_read_b64_tr_b16 a[232:233],v3 offset:512
v_mfma_f32_16x16x32_bf16 v[96:99],a[136:139],v[208:211],v[96:99]
v_exp_f32 v91,v91
v_mfma_f32_16x16x32_bf16 v[96:99],a[140:143],v[216:219],v[96:99]
ds_read_b64_tr_b16 a[234:235],v3 offset:4608
ds_read_b64_tr_b16 a[236:237],v3 offset:8704
v_mfma_f32_16x16x32_bf16 v[100:103],a[156:159],v[216:219],v[40:43]
ds_read_b64_tr_b16 a[238:239],v3 offset:12800
v_cvt_pk_bf16_f32 v89,v90,v91
v_mfma_f32_16x16x32_bf16 v[100:103],a[152:155],v[208:211],v[100:103]
v_exp_f32 v92,v92
v_mfma_f32_16x16x32_bf16 v[100:103],a[148:151],v[200:203],v[100:103]
ds_read_b64_tr_b16 a[240:241],v2 offset:768
ds_read_b64_tr_b16 a[242:243],v2 offset:4864
v_mfma_f32_16x16x32_bf16 v[100:103],a[144:147],v[192:195],v[100:103]
v_exp_f32 v93,v93
v_mfma_f32_16x16x32_bf16 v[104:107],a[160:163],v[192:195],v[40:43]
ds_read_b64_tr_b16 a[244:245],v2 offset:8960
ds_read_b64_tr_b16 a[246:247],v2 offset:13056
v_mfma_f32_16x16x32_bf16 v[104:107],a[164:167],v[200:203],v[104:107]
ds_read_b64_tr_b16 a[248:249],v3 offset:768
v_cvt_pk_bf16_f32 v90,v92,v93
v_mfma_f32_16x16x32_bf16 v[104:107],a[168:171],v[208:211],v[104:107]
ds_read_b64_tr_b16 a[250:251],v3 offset:4864
v_bfe_u32 v2,v6,4,2
v_mfma_f32_16x16x32_bf16 v[104:107],a[172:175],v[216:219],v[104:107]
ds_read_b64_tr_b16 a[252:253],v3 offset:8960
)A"
R"A(v_lshl_add_u32 v1,v2,3,v1
v_mfma_f32_16x16x32_bf16 v[108:111],a[188:191],v[216:219],v[40:43]
ds_read_b64_tr_b16 a[254:255],v3 offset:13056
v_add_u32 v1,122880,v1
v_mfma_f32_16x16x32_bf16 v[108:111],a[184:187],v[208:211],v[108:111]
v_exp_f32 v94,v94
v_mfma_f32_16x16x32_bf16 v[108:111],a[180:183],v[200:203],v[108:111]
v_exp_f32 v95,v95
v_mfma_f32_16x16x32_bf16 v[108:111],a[176:179],v[192:195],v[108:111]
s_waitcnt lgkmcnt(6)
v_cvt_pk_bf16_f32 v91,v94,v95
v_bfe_u32 v2,v6,8,1
v_mfma_f32_16x16x32_bf16 a[0:3],a[192:195],v[64:67],a[0:3]
v_exp_f32 v96,v96
v_mfma_f32_16x16x32_bf16 a[0:3],a[196:199],v[72:75],a[0:3]
v_exp_f32 v97,v97
v_mfma_f32_16x16x32_bf16 a[4:7],a[200:203],v[64:67],a[4:7]
v_exp_f32 v98,v98
v_mfma_f32_16x16x32_bf16 a[4:7],a[204:207],v[72:75],a[4:7]
v_exp_f32 v99,v99
v_mfma_f32_16x16x32_bf16 a[8:11],a[208:211],v[64:67],a[8:11]
v_exp_f32 v100,v100
v_mfma_f32_16x16x32_bf16 a[8:11],a[212:215],v[72:75],a[8:11]
v_exp_f32 v101,v101
v_mfma_f32_16x16x32_bf16 a[12:15],a[216:219],v[64:67],a[12:15]
v_exp_f32 v102,v102
v_mfma_f32_16x16x32_bf16 a[12:15],a[220:223],v[72:75],a[12:15]
v_exp_f32 v103,v103
v_mfma_f32_16x16x32_bf16 a[16:19],a[224:227],v[64:67],a[16:19]
v_cvt_pk_bf16_f32 v96,v96,v97
v_cvt_pk_bf16_f32 v97,v98,v99
v_mfma_f32_16x16x32_bf16 a[16:19],a[228:231],v[72:75],a[16:19]
v_cvt_pk_bf16_f32 v98,v100,v101
v_cvt_pk_bf16_f32 v99,v102,v103
v_mfma_f32_16x16x32_bf16 a[20:23],a[232:235],v[64:67],a[20:23]
v_exp_f32 v104,v104
v_mfma_f32_16x16x32_bf16 a[20:23],a[236:239],v[72:75],a[20:23]
v_exp_f32 v105,v105
v_mfma_f32_16x16x32_bf16 a[24:27],a[240:243],v[64:67],a[24:27]
s_waitcnt lgkmcnt(0)
v_exp_f32 v106,v106
v_mfma_f32_16x16x32_bf16 a[24:27],a[244:247],v[72:75],a[24:27]
v_exp_f32 v107,v107
v_mfma_f32_16x16x32_bf16 a[28:31],a[248:251],v[64:67],a[28:31]
v_exp_f32 v108,v108
v_mfma_f32_16x16x32_bf16 a[28:31],a[252:255],v[72:75],a[28:31]
v_exp_f32 v109,v109
v_mfma_f32_16x16x32_bf16 v[48:51],v[28:31],v[64:67],v[48:51]
v_exp_f32 v110,v110
v_mfma_f32_16x16x32_bf16 v[48:51],v[28:31],v[72:75],v[48:51]
v_exp_f32 v111,v111
s_waitcnt vmcnt(11)
s_barrier
v_mfma_f32_16x16x32_bf16 v[112:115],a[128:131],v[224:227],v[44:47]
buffer_store_dwordx2 v[8:9],v10,s[44:47],s18 offen offset:256 sc1
v_mfma_f32_16x16x32_bf16 v[112:115],a[132:135],v[232:235],v[112:115]
v_cvt_pk_bf16_f32 v104,v104,v105
v_cvt_pk_bf16_f32 v105,v106,v107
v_mfma_f32_16x16x32_bf16 v[112:115],a[136:139],v[240:243],v[112:115]
v_cvt_pk_bf16_f32 v106,v108,v109
v_cvt_pk_bf16_f32 v107,v110,v111
v_mfma_f32_16x16x32_bf16 v[112:115],a[140:143],v[248:251],v[112:115]
s_add_u32 m0,s86,0
v_cmp_nlt_f32 s[54:55],v48,s73
v_max_f32 v49,0x00800000,v48
v_mfma_f32_16x16x32_bf16 v[116:119],a[156:159],v[248:251],v[44:47]
v_rcp_f32 v49,v49
v_mfma_f32_16x16x32_bf16 v[116:119],a[152:155],v[240:243],v[116:119]
v_lshl_add_u32 v1,v2,5,v1
v_lshlrev_b32 v2,6,v2
v_mfma_f32_16x16x32_bf16 v[116:119],a[148:151],v[232:235],v[116:119]
buffer_load_dwordx4 v24,s[48:51],s83 offen lds
v_mfma_f32_16x16x32_bf16 v[116:119],a[144:147],v[224:227],v[116:119]
v_exp_f32 v112,v112
v_mfma_f32_16x16x32_bf16 v[120:123],a[160:163],v[224:227],v[44:47]
v_exp_f32 v113,v113
v_mfma_f32_16x16x32_bf16 v[120:123],a[164:167],v[232:235],v[120:123]
v_exp_f32 v114,v114
v_mfma_f32_16x16x32_bf16 v[120:123],a[168:171],v[240:243],v[120:123]
v_exp_f32 v115,v115
v_mfma_f32_16x16x32_bf16 v[120:123],a[172:175],v[248:251],v[120:123]
v_exp_f32 v116,v116
v_mfma_f32_16x16x32_bf16 v[124:127],a[188:191],v[248:251],v[44:47]
buffer_load_dwordx4 v25,s[48:51],s83 offen offset:1024 lds
v_mfma_f32_16x16x32_bf16 v[124:127],a[184:187],v[240:243],v[124:127]
v_exp_f32 v117,v117
v_mfma_f32_16x16x32_bf16 v[124:127],a[180:183],v[232:235],v[124:127]
v_exp_f32 v118,v118
v_mfma_f32_16x16x32_bf16 v[124:127],a[176:179],v[224:227],v[124:127]
v_exp_f32 v119,v119
v_mfma_f32_16x16x32_bf16 v[52:55],v[28:31],v[80:83],v[52:55]
v_cvt_pk_bf16_f32 v112,v112,v113
v_cvt_pk_bf16_f32 v113,v114,v115
v_mfma_f32_16x16x32_bf16 v[52:55],v[28:31],v[88:91],v[52:55]
v_cvt_pk_bf16_f32 v114,v116,v117
v_cvt_pk_bf16_f32 v115,v118,v119
v_mfma_f32_16x16x32_bf16 v[132:135],a[192:195],v[80:83],v[132:135]
buffer_load_dwordx4 v26,s[48:51],s83 offen offset:2048 lds
v_mfma_f32_16x16x32_bf16 v[132:135],a[196:199],v[88:91],v[132:135]
v_exp_f32 v120,v120
v_mfma_f32_16x16x32_bf16 v[140:143],a[200:203],v[80:83],v[140:143]
v_exp_f32 v121,v121
v_mfma_f32_16x16x32_bf16 v[140:143],a[204:207],v[88:91],v[140:143]
v_exp_f32 v122,v122
v_mfma_f32_16x16x32_bf16 v[148:151],a[208:211],v[80:83],v[148:151]
v_exp_f32 v123,v123
v_mfma_f32_16x16x32_bf16 v[148:151],a[212:215],v[88:91],v[148:151]
v_exp_f32 v124,v124
v_mfma_f32_16x16x32_bf16 v[156:159],a[216:219],v[80:83],v[156:159]
buffer_load_dwordx4 v27,s[48:51],s83 offen offset:3072 lds
v_mfma_f32_16x16x32_bf16 v[156:159],a[220:223],v[88:91],v[156:159]
v_exp_f32 v125,v125
v_mfma_f32_16x16x32_bf16 v[164:167],a[224:227],v[80:83],v[164:167]
v_exp_f32 v126,v126
v_mfma_f32_16x16x32_bf16 v[164:167],a[228:231],v[88:91],v[164:167]
s_add_u32 m0,s86,4096
v_exp_f32 v127,v127
v_mfma_f32_16x16x32_bf16 v[172:175],a[232:235],v[80:83],v[172:175]
v_cvt_pk_bf16_f32 v120,v120,v121
v_cvt_pk_bf16_f32 v121,v122,v123
v_mfma_f32_16x16x32_bf16 v[172:175],a[236:239],v[88:91],v[172:175]
v_cvt_pk_bf16_f32 v122,v124,v125
v_cvt_pk_bf16_f32 v123,v126,v127
v_mfma_f32_16x16x32_bf16 v[180:183],a[240:243],v[80:83],v[180:183]
buffer_load_dwordx4 v7,s[48:51],s83 offen lds
v_mfma_f32_16x16x32_bf16 v[180:183],a[244:247],v[88:91],v[180:183]
v_cmp_nlt_f32 s[56:57],v52,s73
v_sub_u32 v2,v1,v2
v_mfma_f32_16x16x32_bf16 v[188:191],a[248:251],v[80:83],v[188:191]
s_or_b64 s[54:55],s[54:55],s[56:57]
v_accvgpr_read_b32 v8,a0
v_accvgpr_read_b32 v9,a1
v_mfma_f32_16x16x32_bf16 v[188:191],a[252:255],v[88:91],v[188:191]
s_add_u32 m0,s86,5120
v_accvgpr_read_b32 v10,a2
v_accvgpr_read_b32 v11,a3
v_mfma_f32_16x16x32_bf16 v[56:59],v[28:31],v[96:99],v[56:59]
v_accvgpr_read_b32 v12,a4
v_accvgpr_read_b32 v13,a5
v_mfma_f32_16x16x32_bf16 v[56:59],v[28:31],v[104:107],v[56:59]
v_accvgpr_read_b32 v14,a6
v_accvgpr_read_b32 v15,a7
v_mfma_f32_16x16x32_bf16 v[196:199],a[192:195],v[96:99],v[196:199]
buffer_load_dwordx4 v24,s[48:51],s84 offen lds
v_mfma_f32_16x16x32_bf16 v[196:199],a[196:199],v[104:107],v[196:199]
v_mul_f32 v8,v8,v49
v_mul_f32 v9,v9,v49
v_mfma_f32_16x16x32_bf16 v[204:207],a[200:203],v[96:99],v[204:207]
v_cmp_nlt_f32 s[56:57],v56,s73
v_mul_f32 v10,v10,v49
v_mfma_f32_16x16x32_bf16 v[204:207],a[204:207],v[104:107],v[204:207]
s_or_b64 s[54:55],s[54:55],s[56:57]
v_mul_f32 v11,v11,v49
v_mul_f32 v12,v12,v49
v_mfma_f32_16x16x32_bf16 v[212:215],a[208:211],v[96:99],v[212:215]
v_mul_f32 v13,v13,v49
v_mul_f32 v14,v14,v49
v_mfma_f32_16x16x32_bf16 v[212:215],a[212:215],v[104:107],v[212:215]
v_mul_f32 v15,v15,v49
v_cvt_pk_bf16_f32 v32,v8,v9
v_mfma_f32_16x16x32_bf16 v[220:223],a[216:219],v[96:99],v[220:223]
buffer_load_dwordx4 v25,s[48:51],s84 offen offset:1024 lds
v_mfma_f32_16x16x32_bf16 v[220:223],a[220:223],v[104:107],v[220:223]
v_cvt_pk_bf16_f32 v33,v10,v11
v_cvt_pk_bf16_f32 v34,v12,v13
v_mfma_f32_16x16x32_bf16 v[228:231],a[224:227],v[96:99],v[228:231]
v_cvt_pk_bf16_f32 v35,v14,v15
v_accvgpr_read_b32 v16,a8
v_mfma_f32_16x16x32_bf16 v[228:231],a[228:231],v[104:107],v[228:231]
v_accvgpr_read_b32 v17,a9
v_accvgpr_read_b32 v18,a10
v_mfma_f32_16x16x32_bf16 v[236:239],a[232:235],v[96:99],v[236:239]
v_accvgpr_read_b32 v19,a11
v_accvgpr_read_b32 v20,a12
v_mfma_f32_16x16x32_bf16 v[236:239],a[236:239],v[104:107],v[236:239]
v_accvgpr_read_b32 v21,a13
v_accvgpr_read_b32 v22,a14
v_mfma_f32_16x16x32_bf16 v[244:247],a[240:243],v[96:99],v[244:247]
buffer_load_dwordx4 v26,s[48:51],s84 offen offset:2048 lds
v_mfma_f32_16x16x32_bf16 v[244:247],a[244:247],v[104:107],v[244:247]
v_accvgpr_read_b32 v23,a15
v_mul_f32 v16,v16,v49
v_mfma_f32_16x16x32_bf16 v[252:255],a[248:251],v[96:99],v[252:255]
v_mul_f32 v17,v17,v49
v_mul_f32 v18,v18,v49
v_mfma_f32_16x16x32_bf16 v[252:255],a[252:255],v[104:107],v[252:255]
v_mul_f32 v19,v19,v49
v_mul_f32 v20,v20,v49
v_mfma_f32_16x16x32_bf16 v[60:63],v[28:31],v[112:115],v[60:63]
v_mul_f32 v21,v21,v49
v_mul_f32 v22,v22,v49
v_mfma_f32_16x16x32_bf16 v[60:63],v[28:31],v[120:123],v[60:63]
v_mul_f32 v23,v23,v49
v_cvt_pk_bf16_f32 v36,v16,v17
v_mfma_f32_16x16x32_bf16 a[32:35],a[192:195],v[112:115],a[32:35]
buffer_load_dwordx4 v27,s[48:51],s84 offen offset:3072 lds
v_mfma_f32_16x16x32_bf16 a[32:35],a[196:199],v[120:123],a[32:35]
v_cvt_pk_bf16_f32 v37,v18,v19
v_cvt_pk_bf16_f32 v38,v20,v21
v_mfma_f32_16x16x32_bf16 a[36:39],a[200:203],v[112:115],a[36:39]
v_cmp_nlt_f32 s[56:57],v60,s73
v_cvt_pk_bf16_f32 v39,v22,v23
v_mfma_f32_16x16x32_bf16 a[36:39],a[204:207],v[120:123],a[36:39]
s_add_u32 m0,s86,9216
v_accvgpr_read_b32 v8,a16
v_accvgpr_read_b32 v9,a17
v_mfma_f32_16x16x32_bf16 a[40:43],a[208:211],v[112:115],a[40:43]
s_or_b64 s[54:55],s[54:55],s[56:57]
v_accvgpr_read_b32 v10,a18
v_accvgpr_read_b32 v11,a19
v_mfma_f32_16x16x32_bf16 a[40:43],a[212:215],v[120:123],a[40:43]
v_accvgpr_read_b32 v12,a20
v_accvgpr_read_b32 v13,a21
v_mfma_f32_16x16x32_bf16 a[44:47],a[216:219],v[112:115],a[44:47]
buffer_load_dwordx4 v7,s[48:51],s84 offen lds
v_mfma_f32_16x16x32_bf16 a[44:47],a[220:223],v[120:123],a[44:47]
v_accvgpr_read_b32 v14,a22
v_accvgpr_read_b32 v15,a23
v_mfma_f32_16x16x32_bf16 a[48:51],a[224:227],v[112:115],a[48:51]
v_mul_f32 v8,v8,v49
v_mul_f32 v9,v9,v49
v_mfma_f32_16x16x32_bf16 a[48:51],a[228:231],v[120:123],a[48:51]
v_mul_f32 v10,v10,v49
v_mul_f32 v11,v11,v49
v_mfma_f32_16x16x32_bf16 a[52:55],a[232:235],v[112:115],a[52:55]
v_mul_f32 v12,v12,v49
v_mul_f32 v13,v13,v49
v_mfma_f32_16x16x32_bf16 a[52:55],a[236:239],v[120:123],a[52:55]
v_mul_f32 v14,v14,v49
v_mul_f32 v15,v15,v49
v_mfma_f32_16x16x32_bf16 a[56:59],a[240:243],v[112:115],a[56:59]
v_cvt_pk_bf16_f32 v40,v8,v9
v_cvt_pk_bf16_f32 v41,v10,v11
v_mfma_f32_16x16x32_bf16 a[56:59],a[244:247],v[120:123],a[56:59]
v_cvt_pk_bf16_f32 v42,v12,v13
v_cvt_pk_bf16_f32 v43,v14,v15
v_mfma_f32_16x16x32_bf16 a[60:63],a[248:251],v[112:115],a[60:63]
ds_read_b64_tr_b16 v[64:65],v1
ds_read_b64_tr_b16 v[66:67],v1 offset:5120
v_mfma_f32_16x16x32_bf16 a[60:63],a[252:255],v[120:123],a[60:63]
ds_read_b64_tr_b16 v[68:69],v1 offset:10240
v_max_f32 v53,0x00800000,v52
v_rcp_f32 v53,v53
v_max_f32 v57,0x00800000,v56
v_rcp_f32 v57,v57
v_max_f32 v61,0x00800000,v60
v_rcp_f32 v61,v61
ds_read_b64_tr_b16 v[70:71],v1 offset:15360
ds_read_b64_tr_b16 v[72:73],v1 offset:20480
ds_read_b64_tr_b16 v[74:75],v1 offset:25600
ds_read_b64_tr_b16 v[76:77],v1 offset:30720
ds_read_b64_tr_b16 v[78:79],v1 offset:35840
ds_read_b64_tr_b16 v[80:81],v2 offset:32
ds_read_b64_tr_b16 v[82:83],v2 offset:5152
ds_read_b64_tr_b16 v[84:85],v2 offset:10272
ds_read_b64_tr_b16 v[86:87],v2 offset:15392
ds_read_b64_tr_b16 v[88:89],v2 offset:20512
ds_read_b64_tr_b16 v[90:91],v2 offset:25632
ds_read_b64_tr_b16 v[92:93],v2 offset:30752
ds_read_b64_tr_b16 v[94:95],v2 offset:35872
s_cmp_lg_u64 s[54:55],0
s_cbranch_scc1 L_slow1_%=
L_ok1_%=:
s_cmp_ge_u32 s63,2
s_cbranch_scc1 L_GS_%=
L_gbF_%=:
s_waitcnt lgkmcnt(14)
v_mfma_f32_16x16x32_bf16 a[192:195],v[64:67],a[68:71],0
buffer_store_dwordx4 v[32:35],v6,s[40:43],s79 offen sc1
s_waitcnt lgkmcnt(12)
v_mfma_f32_16x16x32_bf16 a[192:195],v[68:71],a[76:79],a[192:195]
s_lshl_b32 s91,s63,8
v_bfe_u32 v3,v6,4,2
v_min_u32 v3,2,v3
s_waitcnt lgkmcnt(10)
v_mfma_f32_16x16x32_bf16 a[192:195],v[72:75],a[84:87],a[192:195]
s_add_u32 s87,s64,1
v_add3_u32 v3,v3,s64,1
v_and_b32 v3,3,v3
s_waitcnt lgkmcnt(8)
v_mfma_f32_16x16x32_bf16 a[192:195],v[76:79],a[92:95],a[192:195]
s_and_b32 s87,s87,3
ds_read_b64_tr_b16 v[96:97],v1 offset:64
v_lshlrev_b32 v3,6,v3
v_mfma_f32_16x16x32_bf16 a[196:199],v[76:79],a[124:127],0
s_lshl_b32 s87,s87,1
ds_read_b64_tr_b16 v[98:99],v1 offset:5184
ds_read_b64_tr_b16 v[100:101],v1 offset:10304
v_mfma_f32_16x16x32_bf16 a[196:199],v[72:75],a[116:119],a[196:199]
s_lshl_b32 s87,s87,16
ds_read_b64_tr_b16 v[102:103],v1 offset:15424
v_lshlrev_b32 v29,10,v6
v_mfma_f32_16x16x32_bf16 a[196:199],v[68:71],a[108:111],a[196:199]
s_add_u32 s88,s87,4096
ds_read_b64_tr_b16 v[104:105],v1 offset:20544
)A"
R"A(v_accvgpr_read_b32 v8,a24
v_mfma_f32_16x16x32_bf16 a[196:199],v[64:67],a[100:103],a[196:199]
s_add_u32 s89,s87,8192
ds_read_b64_tr_b16 v[106:107],v1 offset:25664
v_accvgpr_read_b32 v9,a25
v_mfma_f32_16x16x32_bf16 a[200:203],v[64:67],a[64:67],0
s_add_u32 s90,s87,12288
ds_read_b64_tr_b16 v[108:109],v1 offset:30784
ds_read_b64_tr_b16 v[110:111],v1 offset:35904
v_mfma_f32_16x16x32_bf16 a[200:203],v[68:71],a[72:75],a[200:203]
s_add_u32 s20,s64,2
ds_read_b64_tr_b16 v[112:113],v2 offset:96
v_accvgpr_read_b32 v10,a26
v_mfma_f32_16x16x32_bf16 a[200:203],v[72:75],a[80:83],a[200:203]
s_and_b32 s20,s20,3
ds_read_b64_tr_b16 v[114:115],v2 offset:5216
v_accvgpr_read_b32 v11,a27
v_mfma_f32_16x16x32_bf16 a[200:203],v[76:79],a[88:91],a[200:203]
s_lshl_b32 s20,s20,1
ds_read_b64_tr_b16 v[116:117],v2 offset:10336
v_accvgpr_read_b32 v12,a28
v_mfma_f32_16x16x32_bf16 a[204:207],v[76:79],a[120:123],0
s_lshl_b32 s20,s20,16
ds_read_b64_tr_b16 v[118:119],v2 offset:15456
ds_read_b64_tr_b16 v[120:121],v2 offset:20576
v_mfma_f32_16x16x32_bf16 a[204:207],v[72:75],a[112:115],a[204:207]
s_add_u32 s21,s20,4096
ds_read_b64_tr_b16 v[122:123],v2 offset:25696
v_accvgpr_read_b32 v13,a29
v_mfma_f32_16x16x32_bf16 a[204:207],v[68:71],a[104:107],a[204:207]
s_add_u32 s22,s20,8192
ds_read_b64_tr_b16 v[124:125],v2 offset:30816
v_accvgpr_read_b32 v14,a30
v_mfma_f32_16x16x32_bf16 a[204:207],v[64:67],a[96:99],a[204:207]
buffer_store_dwordx4 v[36:39],v6,s[40:43],s80 offen sc1
s_waitcnt lgkmcnt(9)
v_mfma_f32_16x16x32_bf16 a[208:211],v[80:83],a[68:71],0
s_add_u32 s23,s20,12288
ds_read_b64_tr_b16 v[126:127],v2 offset:35936
ds_read_b64_tr_b16 v[64:65],v1 offset:128
v_mfma_f32_16x16x32_bf16 a[208:211],v[84:87],a[76:79],a[208:211]
s_add_u32 m0,s86,122880
ds_read_b64_tr_b16 v[66:67],v1 offset:5248
v_accvgpr_read_b32 v15,a31
v_mfma_f32_16x16x32_bf16 a[208:211],v[88:91],a[84:87],a[208:211]
s_add_u32 s83,s64,3
ds_read_b64_tr_b16 v[68:69],v1 offset:10368
v_mul_f32 v8,v8,v49
v_mfma_f32_16x16x32_bf16 a[208:211],v[92:95],a[92:95],a[208:211]
s_and_b32 s83,s83,3
ds_read_b64_tr_b16 v[70:71],v1 offset:15488
v_mul_f32 v9,v9,v49
v_mfma_f32_16x16x32_bf16 a[212:215],v[92:95],a[124:127],0
s_waitcnt vmcnt(21)
buffer_load_dwordx2 v[4:5],v3,s[44:47],s91 offen offset:256 sc1
v_mfma_f32_16x16x32_bf16 a[212:215],v[88:91],a[116:119],a[212:215]
L_pd13_%=:
s_cbranch_execz L_trt1_%=
s_lshl_b32 s83,s83,1
ds_read_b64_tr_b16 v[72:73],v1 offset:20608
ds_read_b64_tr_b16 v[74:75],v1 offset:25728
v_mfma_f32_16x16x32_bf16 a[212:215],v[84:87],a[108:111],a[212:215]
s_mul_i32 s83,s83,163840
ds_read_b64_tr_b16 v[76:77],v1 offset:30848
v_mul_f32 v10,v10,v49
v_mfma_f32_16x16x32_bf16 a[212:215],v[80:83],a[100:103],a[212:215]
s_add_u32 s83,s83,s85
ds_read_b64_tr_b16 v[78:79],v1 offset:35968
v_mul_f32 v11,v11,v49
v_mfma_f32_16x16x32_bf16 a[216:219],v[80:83],a[64:67],0
s_add_u32 s84,s83,20480
v_mul_f32 v12,v12,v49
v_mul_f32 v13,v13,v49
v_mfma_f32_16x16x32_bf16 a[216:219],v[84:87],a[72:75],a[216:219]
s_lshl_b32 s66,s61,3
v_mul_f32 v14,v14,v49
v_mul_f32 v15,v15,v49
v_mfma_f32_16x16x32_bf16 a[216:219],v[88:91],a[80:83],a[216:219]
s_add_u32 s66,s66,s64
v_cvt_pk_bf16_f32 v44,v8,v9
v_cvt_pk_bf16_f32 v45,v10,v11
v_mfma_f32_16x16x32_bf16 a[216:219],v[92:95],a[88:91],a[216:219]
s_mul_i32 s66,s66,262144
v_cvt_pk_bf16_f32 v46,v12,v13
v_cvt_pk_bf16_f32 v47,v14,v15
v_mfma_f32_16x16x32_bf16 a[220:223],v[92:95],a[120:123],0
s_add_u32 s66,s66,100663296
ds_read_b64_tr_b16 v[92:93],v2 offset:30880
ds_read_b64_tr_b16 v[94:95],v2 offset:36000
v_mfma_f32_16x16x32_bf16 a[220:223],v[88:91],a[112:115],a[220:223]
s_mov_b32 s38,262144
ds_read_b64_tr_b16 v[88:89],v2 offset:20640
v_mul_f32 v132,v132,v53
v_mfma_f32_16x16x32_bf16 a[220:223],v[84:87],a[104:107],a[220:223]
buffer_store_dwordx4 v[40:43],v6,s[40:43],s81 offen sc1
v_mfma_f32_16x16x32_bf16 a[220:223],v[80:83],a[96:99],a[220:223]
s_mov_b32 s39,0x20000
ds_read_b64_tr_b16 v[80:81],v2 offset:160
ds_read_b64_tr_b16 v[82:83],v2 offset:5280
v_mfma_f32_16x16x32_bf16 a[224:227],v[96:99],a[68:71],0
s_add_u32 s74,s63,1
ds_read_b64_tr_b16 v[84:85],v2 offset:10400
v_mul_f32 v133,v133,v53
v_mfma_f32_16x16x32_bf16 a[224:227],v[100:103],a[76:79],a[224:227]
s_and_b32 s74,s74,3
ds_read_b64_tr_b16 v[86:87],v2 offset:15520
v_mul_f32 v134,v134,v53
v_mfma_f32_16x16x32_bf16 a[224:227],v[104:107],a[84:87],a[224:227]
s_lshl_b32 s74,s74,2
ds_read_b64_tr_b16 v[90:91],v2 offset:25760
v_mul_f32 v135,v135,v53
s_waitcnt lgkmcnt(8)
v_mfma_f32_16x16x32_bf16 a[224:227],v[108:111],a[92:95],a[224:227]
s_add_u32 s74,s74,s60
v_mul_f32 v140,v140,v53
v_mul_f32 v141,v141,v53
v_mfma_f32_16x16x32_bf16 a[228:231],v[108:111],a[124:127],0
s_mul_i32 s74,s74,16384
v_mul_f32 v142,v142,v53
v_mul_f32 v143,v143,v53
v_mfma_f32_16x16x32_bf16 a[228:231],v[104:107],a[116:119],a[228:231]
s_add_u32 s74,s74,8192
v_cvt_pk_bf16_f32 v132,v132,v133
v_cvt_pk_bf16_f32 v133,v134,v135
v_mfma_f32_16x16x32_bf16 a[228:231],v[100:103],a[108:111],a[228:231]
s_add_u32 s75,s63,2
v_cvt_pk_bf16_f32 v134,v140,v141
v_cvt_pk_bf16_f32 v135,v142,v143
v_mfma_f32_16x16x32_bf16 a[228:231],v[96:99],a[100:103],a[228:231]
s_and_b32 s75,s75,3
v_mul_f32 v148,v148,v53
v_mul_f32 v149,v149,v53
v_mfma_f32_16x16x32_bf16 a[232:235],v[96:99],a[64:67],0
s_lshl_b32 s75,s75,2
v_mul_f32 v150,v150,v53
v_mul_f32 v151,v151,v53
v_mfma_f32_16x16x32_bf16 a[232:235],v[100:103],a[72:75],a[232:235]
s_add_u32 s75,s75,s60
v_mul_f32 v156,v156,v53
v_mul_f32 v157,v157,v53
v_mfma_f32_16x16x32_bf16 a[232:235],v[104:107],a[80:83],a[232:235]
s_mul_i32 s75,s75,16384
v_mul_f32 v158,v158,v53
v_mul_f32 v159,v159,v53
v_mfma_f32_16x16x32_bf16 a[232:235],v[108:111],a[88:91],a[232:235]
s_add_u32 s75,s75,4096
v_cvt_pk_bf16_f32 v148,v148,v149
v_cvt_pk_bf16_f32 v149,v150,v151
v_mfma_f32_16x16x32_bf16 a[236:239],v[108:111],a[120:123],0
s_add_u32 s76,s63,3
ds_read_b64_tr_b16 v[108:109],v1 offset:30912
ds_read_b64_tr_b16 v[110:111],v1 offset:36032
v_mfma_f32_16x16x32_bf16 a[236:239],v[104:107],a[112:115],a[236:239]
buffer_store_dwordx4 v[44:47],v6,s[40:43],s82 offen sc1
v_mfma_f32_16x16x32_bf16 a[236:239],v[100:103],a[104:107],a[236:239]
s_and_b32 s76,s76,3
ds_read_b64_tr_b16 v[100:101],v1 offset:10432
ds_read_b64_tr_b16 v[102:103],v1 offset:15552
v_mfma_f32_16x16x32_bf16 a[236:239],v[96:99],a[96:99],a[236:239]
s_lshl_b32 s76,s76,2
ds_read_b64_tr_b16 v[96:97],v1 offset:192
v_cvt_pk_bf16_f32 v150,v156,v157
v_mfma_f32_16x16x32_bf16 a[240:243],v[112:115],a[68:71],0
s_add_u32 s76,s76,s60
ds_read_b64_tr_b16 v[98:99],v1 offset:5312
v_cvt_pk_bf16_f32 v151,v158,v159
v_mfma_f32_16x16x32_bf16 a[240:243],v[116:119],a[76:79],a[240:243]
s_mul_i32 s76,s76,16384
ds_read_b64_tr_b16 v[104:105],v1 offset:20672
v_mul_f32 v164,v164,v53
v_mfma_f32_16x16x32_bf16 a[240:243],v[120:123],a[84:87],a[240:243]
s_add_u32 s76,s76,0
ds_read_b64_tr_b16 v[106:107],v1 offset:25792
v_mul_f32 v165,v165,v53
v_mfma_f32_16x16x32_bf16 a[240:243],v[124:127],a[92:95],a[240:243]
s_lshl_b32 s67,s63,10
v_mul_f32 v166,v166,v53
v_mul_f32 v167,v167,v53
v_mfma_f32_16x16x32_bf16 a[244:247],v[124:127],a[124:127],0
s_mov_b32 s26,307200
v_mul_f32 v172,v172,v53
v_mul_f32 v173,v173,v53
v_mfma_f32_16x16x32_bf16 a[244:247],v[120:123],a[116:119],a[244:247]
s_mov_b32 s27,0x20000
v_mul_f32 v174,v174,v53
v_mul_f32 v175,v175,v53
v_mfma_f32_16x16x32_bf16 a[244:247],v[116:119],a[108:111],a[244:247]
s_lshl_b32 s77,s63,2
v_cvt_pk_bf16_f32 v164,v164,v165
v_cvt_pk_bf16_f32 v165,v166,v167
v_mfma_f32_16x16x32_bf16 a[244:247],v[112:115],a[100:103],a[244:247]
s_add_u32 s77,s77,s60
v_cvt_pk_bf16_f32 v166,v172,v173
v_cvt_pk_bf16_f32 v167,v174,v175
v_mfma_f32_16x16x32_bf16 a[248:251],v[112:115],a[64:67],0
s_mul_i32 s77,s77,16384
v_mul_f32 v180,v180,v53
v_mul_f32 v181,v181,v53
v_mfma_f32_16x16x32_bf16 a[248:251],v[116:119],a[72:75],a[248:251]
s_add_u32 s65,s77,0
v_mul_f32 v182,v182,v53
v_mul_f32 v183,v183,v53
v_mfma_f32_16x16x32_bf16 a[248:251],v[120:123],a[80:83],a[248:251]
s_add_u32 s68,s77,12288
v_mul_f32 v188,v188,v53
v_mul_f32 v189,v189,v53
v_mfma_f32_16x16x32_bf16 a[248:251],v[124:127],a[88:91],a[248:251]
v_mul_f32 v190,v190,v53
v_mul_f32 v191,v191,v53
v_mfma_f32_16x16x32_bf16 a[252:255],v[124:127],a[120:123],0
buffer_load_dword v30,v29,s[8:11],s7 offen
v_mfma_f32_16x16x32_bf16 a[252:255],v[120:123],a[112:115],a[252:255]
ds_read_b64_tr_b16 v[120:121],v2 offset:20704
ds_read_b64_tr_b16 v[122:123],v2 offset:25824
v_mfma_f32_16x16x32_bf16 a[252:255],v[116:119],a[104:107],a[252:255]
buffer_store_dwordx4 v[132:135],v6,s[40:43],s79 offen offset:1024 sc1
v_mfma_f32_16x16x32_bf16 a[252:255],v[112:115],a[96:99],a[252:255]
ds_read_b64_tr_b16 v[112:113],v2 offset:224
ds_read_b64_tr_b16 v[114:115],v2 offset:5344
v_mfma_f32_16x16x32_bf16 a[0:3],v[64:67],a[68:71],0
ds_read_b64_tr_b16 v[116:117],v2 offset:10464
v_cvt_pk_bf16_f32 v180,v180,v181
v_mfma_f32_16x16x32_bf16 a[0:3],v[68:71],a[76:79],a[0:3]
ds_read_b64_tr_b16 v[118:119],v2 offset:15584
v_cvt_pk_bf16_f32 v181,v182,v183
v_mfma_f32_16x16x32_bf16 a[0:3],v[72:75],a[84:87],a[0:3]
ds_read_b64_tr_b16 v[124:125],v2 offset:30944
v_cvt_pk_bf16_f32 v182,v188,v189
v_mfma_f32_16x16x32_bf16 a[0:3],v[76:79],a[92:95],a[0:3]
ds_read_b64_tr_b16 v[126:127],v2 offset:36064
v_cvt_pk_bf16_f32 v183,v190,v191
v_mfma_f32_16x16x32_bf16 a[4:7],v[76:79],a[124:127],0
v_mul_f32 v196,v196,v57
v_mul_f32 v197,v197,v57
v_mfma_f32_16x16x32_bf16 a[4:7],v[72:75],a[116:119],a[4:7]
v_mul_f32 v198,v198,v57
v_mul_f32 v199,v199,v57
v_mfma_f32_16x16x32_bf16 a[4:7],v[68:71],a[108:111],a[4:7]
v_mul_f32 v204,v204,v57
v_mul_f32 v205,v205,v57
v_mfma_f32_16x16x32_bf16 a[4:7],v[64:67],a[100:103],a[4:7]
v_mul_f32 v206,v206,v57
v_mul_f32 v207,v207,v57
v_mfma_f32_16x16x32_bf16 a[8:11],v[64:67],a[64:67],0
v_cvt_pk_bf16_f32 v196,v196,v197
v_cvt_pk_bf16_f32 v197,v198,v199
v_mfma_f32_16x16x32_bf16 a[8:11],v[68:71],a[72:75],a[8:11]
v_cvt_pk_bf16_f32 v198,v204,v205
v_cvt_pk_bf16_f32 v199,v206,v207
v_mfma_f32_16x16x32_bf16 a[8:11],v[72:75],a[80:83],a[8:11]
v_mul_f32 v212,v212,v57
v_mul_f32 v213,v213,v57
v_mfma_f32_16x16x32_bf16 a[8:11],v[76:79],a[88:91],a[8:11]
v_mul_f32 v214,v214,v57
v_mul_f32 v215,v215,v57
v_mfma_f32_16x16x32_bf16 a[12:15],v[76:79],a[120:123],0
ds_read_b64_tr_b16 v[76:77],v1 offset:30976
ds_read_b64_tr_b16 v[78:79],v1 offset:36096
v_mfma_f32_16x16x32_bf16 a[12:15],v[72:75],a[112:115],a[12:15]
buffer_store_dwordx4 v[148:151],v6,s[40:43],s80 offen offset:1024 sc1
v_mfma_f32_16x16x32_bf16 a[12:15],v[68:71],a[104:107],a[12:15]
ds_read_b64_tr_b16 v[68:69],v1 offset:10496
ds_read_b64_tr_b16 v[70:71],v1 offset:15616
v_mfma_f32_16x16x32_bf16 a[12:15],v[64:67],a[96:99],a[12:15]
s_waitcnt lgkmcnt(4)
ds_read_b64_tr_b16 v[64:65],v1 offset:256
v_mul_f32 v220,v220,v57
v_mfma_f32_16x16x32_bf16 a[16:19],v[80:83],a[68:71],0
ds_read_b64_tr_b16 v[66:67],v1 offset:5376
v_mul_f32 v221,v221,v57
v_mfma_f32_16x16x32_bf16 a[16:19],v[84:87],a[76:79],a[16:19]
ds_read_b64_tr_b16 v[72:73],v1 offset:20736
v_mul_f32 v222,v222,v57
v_mfma_f32_16x16x32_bf16 a[16:19],v[88:91],a[84:87],a[16:19]
ds_read_b64_tr_b16 v[74:75],v1 offset:25856
v_add_u32 v1,0xffff6000,v1
v_mfma_f32_16x16x32_bf16 a[16:19],v[92:95],a[92:95],a[16:19]
v_mul_f32 v223,v223,v57
v_cvt_pk_bf16_f32 v212,v212,v213
v_mfma_f32_16x16x32_bf16 a[20:23],v[92:95],a[124:127],0
v_cvt_pk_bf16_f32 v213,v214,v215
v_cvt_pk_bf16_f32 v214,v220,v221
v_mfma_f32_16x16x32_bf16 a[20:23],v[88:91],a[116:119],a[20:23]
v_cvt_pk_bf16_f32 v215,v222,v223
v_mul_f32 v228,v228,v57
v_mfma_f32_16x16x32_bf16 a[20:23],v[84:87],a[108:111],a[20:23]
v_mul_f32 v229,v229,v57
v_mul_f32 v230,v230,v57
v_mfma_f32_16x16x32_bf16 a[20:23],v[80:83],a[100:103],a[20:23]
v_mul_f32 v231,v231,v57
v_mul_f32 v236,v236,v57
v_mfma_f32_16x16x32_bf16 a[24:27],v[80:83],a[64:67],0
v_mul_f32 v237,v237,v57
v_mul_f32 v238,v238,v57
v_mfma_f32_16x16x32_bf16 a[24:27],v[84:87],a[72:75],a[24:27]
v_mul_f32 v239,v239,v57
v_cvt_pk_bf16_f32 v228,v228,v229
v_mfma_f32_16x16x32_bf16 a[24:27],v[88:91],a[80:83],a[24:27]
s_waitcnt vmcnt(5)
v_cmp_ne_u32 s[56:57],s52,v4
v_cmp_ne_u32 s[58:59],s53,v5
v_mfma_f32_16x16x32_bf16 a[24:27],v[92:95],a[88:91],a[24:27]
s_or_b64 s[56:57],s[56:57],s[58:59]
)A"
R"A(s_cbranch_scc1 L_fr1_%=
L_fo1_%=:
v_cvt_pk_bf16_f32 v229,v230,v231
v_mfma_f32_16x16x32_bf16 a[28:31],v[92:95],a[120:123],0
buffer_store_dwordx4 v[164:167],v6,s[40:43],s81 offen offset:1024 sc1
v_mfma_f32_16x16x32_bf16 a[28:31],v[88:91],a[112:115],a[28:31]
s_lshl_b32 s91,s63,8
ds_read_b64_tr_b16 v[88:89],v2 offset:20768
ds_read_b64_tr_b16 v[90:91],v2 offset:25888
v_mfma_f32_16x16x32_bf16 a[28:31],v[84:87],a[104:107],a[28:31]
buffer_load_dwordx4 a[128:131],v6,s[28:31],s87 offen sc1
v_mfma_f32_16x16x32_bf16 a[28:31],v[80:83],a[96:99],a[28:31]
ds_read_b64_tr_b16 v[80:81],v2 offset:288
ds_read_b64_tr_b16 v[82:83],v2 offset:5408
v_mfma_f32_16x16x32_bf16 v[128:131],v[96:99],a[68:71],0
ds_read_b64_tr_b16 v[84:85],v2 offset:10528
v_cvt_pk_bf16_f32 v230,v236,v237
v_mfma_f32_16x16x32_bf16 v[128:131],v[100:103],a[76:79],v[128:131]
ds_read_b64_tr_b16 v[86:87],v2 offset:15648
v_cvt_pk_bf16_f32 v231,v238,v239
v_mfma_f32_16x16x32_bf16 v[128:131],v[104:107],a[84:87],v[128:131]
ds_read_b64_tr_b16 v[92:93],v2 offset:31008
v_mul_f32 v244,v244,v57
v_mfma_f32_16x16x32_bf16 v[128:131],v[108:111],a[92:95],v[128:131]
ds_read_b64_tr_b16 v[94:95],v2 offset:36128
v_add_u32 v2,0xffff6000,v2
v_mfma_f32_16x16x32_bf16 v[136:139],v[108:111],a[124:127],0
v_mul_f32 v245,v245,v57
v_mul_f32 v246,v246,v57
v_mfma_f32_16x16x32_bf16 v[136:139],v[104:107],a[116:119],v[136:139]
v_mul_f32 v247,v247,v57
v_mul_f32 v252,v252,v57
v_mfma_f32_16x16x32_bf16 v[136:139],v[100:103],a[108:111],v[136:139]
v_mul_f32 v253,v253,v57
v_mul_f32 v254,v254,v57
v_mfma_f32_16x16x32_bf16 v[136:139],v[96:99],a[100:103],v[136:139]
v_mul_f32 v255,v255,v57
v_cvt_pk_bf16_f32 v244,v244,v245
v_mfma_f32_16x16x32_bf16 v[144:147],v[96:99],a[64:67],0
v_cvt_pk_bf16_f32 v245,v246,v247
v_cvt_pk_bf16_f32 v246,v252,v253
v_mfma_f32_16x16x32_bf16 v[144:147],v[100:103],a[72:75],v[144:147]
buffer_load_dwordx4 a[144:147],v6,s[28:31],s88 offen sc1
v_mfma_f32_16x16x32_bf16 v[144:147],v[104:107],a[80:83],v[144:147]
v_cvt_pk_bf16_f32 v247,v254,v255
v_accvgpr_read_b32 v8,a40
v_mfma_f32_16x16x32_bf16 v[144:147],v[108:111],a[88:91],v[144:147]
buffer_store_dwordx4 v[180:183],v6,s[40:43],s82 offen offset:1024 sc1
v_mfma_f32_16x16x32_bf16 v[152:155],v[108:111],a[120:123],0
ds_read_b64_tr_b16 v[108:109],v1 offset:30720
ds_read_b64_tr_b16 v[110:111],v1 offset:35840
v_mfma_f32_16x16x32_bf16 v[152:155],v[104:107],a[112:115],v[152:155]
ds_read_b64_tr_b16 v[104:105],v1 offset:20480
v_accvgpr_read_b32 v9,a41
v_mfma_f32_16x16x32_bf16 v[152:155],v[100:103],a[104:107],v[152:155]
ds_read_b64_tr_b16 v[100:101],v1 offset:10240
v_accvgpr_read_b32 v10,a42
v_mfma_f32_16x16x32_bf16 v[152:155],v[96:99],a[96:99],v[152:155]
ds_read_b64_tr_b16 v[96:97],v1
v_accvgpr_read_b32 v11,a43
v_mfma_f32_16x16x32_bf16 v[160:163],v[112:115],a[68:71],0
ds_read_b64_tr_b16 v[98:99],v1 offset:5120
ds_read_b64_tr_b16 v[102:103],v1 offset:15360
v_mfma_f32_16x16x32_bf16 v[160:163],v[116:119],a[76:79],v[160:163]
ds_read_b64_tr_b16 v[106:107],v1 offset:25600
v_accvgpr_read_b32 v12,a44
v_mfma_f32_16x16x32_bf16 v[160:163],v[120:123],a[84:87],v[160:163]
v_accvgpr_read_b32 v13,a45
v_accvgpr_read_b32 v14,a46
v_mfma_f32_16x16x32_bf16 v[160:163],v[124:127],a[92:95],v[160:163]
v_accvgpr_read_b32 v15,a47
v_mul_f32 v8,v8,v61
v_mfma_f32_16x16x32_bf16 v[168:171],v[124:127],a[124:127],0
v_mul_f32 v9,v9,v61
v_mul_f32 v10,v10,v61
v_mfma_f32_16x16x32_bf16 v[168:171],v[120:123],a[116:119],v[168:171]
v_mul_f32 v11,v11,v61
v_mul_f32 v12,v12,v61
v_mfma_f32_16x16x32_bf16 v[168:171],v[116:119],a[108:111],v[168:171]
buffer_load_dwordx4 a[160:163],v6,s[28:31],s89 offen sc1
v_mfma_f32_16x16x32_bf16 v[168:171],v[112:115],a[100:103],v[168:171]
v_mul_f32 v13,v13,v61
v_mul_f32 v14,v14,v61
v_mfma_f32_16x16x32_bf16 v[176:179],v[112:115],a[64:67],0
v_mul_f32 v15,v15,v61
v_cvt_pk_bf16_f32 v156,v8,v9
v_mfma_f32_16x16x32_bf16 v[176:179],v[116:119],a[72:75],v[176:179]
v_cvt_pk_bf16_f32 v157,v10,v11
v_cvt_pk_bf16_f32 v158,v12,v13
v_mfma_f32_16x16x32_bf16 v[176:179],v[120:123],a[80:83],v[176:179]
buffer_store_dwordx4 v[196:199],v6,s[40:43],s79 offen offset:2048 sc1
v_mfma_f32_16x16x32_bf16 v[176:179],v[124:127],a[88:91],v[176:179]
v_cvt_pk_bf16_f32 v159,v14,v15
v_accvgpr_read_b32 v8,a56
v_mfma_f32_16x16x32_bf16 v[184:187],v[124:127],a[120:123],0
v_accvgpr_read_b32 v9,a57
v_accvgpr_read_b32 v10,a58
v_mfma_f32_16x16x32_bf16 v[184:187],v[120:123],a[112:115],v[184:187]
ds_read_b64_tr_b16 v[120:121],v2 offset:20512
ds_read_b64_tr_b16 v[122:123],v2 offset:25632
v_mfma_f32_16x16x32_bf16 v[184:187],v[116:119],a[104:107],v[184:187]
ds_read_b64_tr_b16 v[116:117],v2 offset:10272
v_accvgpr_read_b32 v11,a59
v_mfma_f32_16x16x32_bf16 v[184:187],v[112:115],a[96:99],v[184:187]
s_waitcnt lgkmcnt(3)
ds_read_b64_tr_b16 v[112:113],v2 offset:32
v_accvgpr_read_b32 v12,a60
v_mfma_f32_16x16x32_bf16 v[192:195],v[64:67],a[68:71],0
ds_read_b64_tr_b16 v[114:115],v2 offset:5152
v_accvgpr_read_b32 v13,a61
v_mfma_f32_16x16x32_bf16 v[192:195],v[68:71],a[76:79],v[192:195]
ds_read_b64_tr_b16 v[118:119],v2 offset:15392
v_accvgpr_read_b32 v14,a62
v_mfma_f32_16x16x32_bf16 v[192:195],v[72:75],a[84:87],v[192:195]
v_accvgpr_read_b32 v15,a63
v_mul_f32 v8,v8,v61
v_mfma_f32_16x16x32_bf16 v[192:195],v[76:79],a[92:95],v[192:195]
buffer_load_dwordx4 a[176:179],v6,s[28:31],s90 offen sc1
v_mfma_f32_16x16x32_bf16 v[200:203],v[76:79],a[124:127],0
v_mul_f32 v9,v9,v61
v_mul_f32 v10,v10,v61
v_mfma_f32_16x16x32_bf16 v[200:203],v[72:75],a[116:119],v[200:203]
v_mul_f32 v11,v11,v61
v_mul_f32 v12,v12,v61
v_mfma_f32_16x16x32_bf16 v[200:203],v[68:71],a[108:111],v[200:203]
v_mul_f32 v13,v13,v61
v_mul_f32 v14,v14,v61
v_mfma_f32_16x16x32_bf16 v[200:203],v[64:67],a[100:103],v[200:203]
v_mul_f32 v15,v15,v61
v_cvt_pk_bf16_f32 v188,v8,v9
v_mfma_f32_16x16x32_bf16 v[208:211],v[64:67],a[64:67],0
v_cvt_pk_bf16_f32 v189,v10,v11
v_cvt_pk_bf16_f32 v190,v12,v13
v_mfma_f32_16x16x32_bf16 v[208:211],v[68:71],a[72:75],v[208:211]
buffer_store_dwordx4 v[212:215],v6,s[40:43],s80 offen offset:2048 sc1
v_mfma_f32_16x16x32_bf16 v[208:211],v[72:75],a[80:83],v[208:211]
ds_read_b64_tr_b16 v[124:125],v2 offset:30752
v_cvt_pk_bf16_f32 v191,v14,v15
v_mfma_f32_16x16x32_bf16 v[208:211],v[76:79],a[88:91],v[208:211]
ds_read_b64_tr_b16 v[126:127],v2 offset:35872
v_accvgpr_read_b32 v16,a32
v_mfma_f32_16x16x32_bf16 v[216:219],v[76:79],a[120:123],0
v_accvgpr_read_b32 v17,a33
v_accvgpr_read_b32 v18,a34
v_mfma_f32_16x16x32_bf16 v[216:219],v[72:75],a[112:115],v[216:219]
v_accvgpr_read_b32 v19,a35
v_accvgpr_read_b32 v20,a36
v_mfma_f32_16x16x32_bf16 v[216:219],v[68:71],a[104:107],v[216:219]
v_accvgpr_read_b32 v21,a37
v_accvgpr_read_b32 v22,a38
v_mfma_f32_16x16x32_bf16 v[216:219],v[64:67],a[96:99],v[216:219]
v_accvgpr_read_b32 v23,a39
v_mul_f32 v16,v16,v61
v_mfma_f32_16x16x32_bf16 v[224:227],v[80:83],a[68:71],0
buffer_load_dwordx4 a[180:183],v6,s[28:31],s90 offen offset:1024 sc1
v_mfma_f32_16x16x32_bf16 v[224:227],v[84:87],a[76:79],v[224:227]
v_mul_f32 v17,v17,v61
v_mul_f32 v18,v18,v61
v_mfma_f32_16x16x32_bf16 v[224:227],v[88:91],a[84:87],v[224:227]
v_mul_f32 v19,v19,v61
v_mul_f32 v20,v20,v61
v_mfma_f32_16x16x32_bf16 v[224:227],v[92:95],a[92:95],v[224:227]
v_mul_f32 v21,v21,v61
v_mul_f32 v22,v22,v61
v_mfma_f32_16x16x32_bf16 v[232:235],v[92:95],a[124:127],0
v_mul_f32 v23,v23,v61
v_cvt_pk_bf16_f32 v140,v16,v17
v_mfma_f32_16x16x32_bf16 v[232:235],v[88:91],a[116:119],v[232:235]
v_cvt_pk_bf16_f32 v141,v18,v19
v_cvt_pk_bf16_f32 v142,v20,v21
v_mfma_f32_16x16x32_bf16 v[232:235],v[84:87],a[108:111],v[232:235]
v_cvt_pk_bf16_f32 v143,v22,v23
v_accvgpr_read_b32 v16,a48
v_mfma_f32_16x16x32_bf16 v[232:235],v[80:83],a[100:103],v[232:235]
v_accvgpr_read_b32 v17,a49
v_accvgpr_read_b32 v18,a50
v_mfma_f32_16x16x32_bf16 v[240:243],v[80:83],a[64:67],0
buffer_store_dwordx4 v[228:231],v6,s[40:43],s81 offen offset:2048 sc1
v_mfma_f32_16x16x32_bf16 v[240:243],v[84:87],a[72:75],v[240:243]
v_accvgpr_read_b32 v19,a51
v_accvgpr_read_b32 v20,a52
v_mfma_f32_16x16x32_bf16 v[240:243],v[88:91],a[80:83],v[240:243]
v_accvgpr_read_b32 v21,a53
v_accvgpr_read_b32 v22,a54
v_mfma_f32_16x16x32_bf16 v[240:243],v[92:95],a[88:91],v[240:243]
v_accvgpr_read_b32 v23,a55
v_mul_f32 v16,v16,v61
v_mfma_f32_16x16x32_bf16 v[248:251],v[92:95],a[120:123],0
v_mul_f32 v17,v17,v61
v_mul_f32 v18,v18,v61
v_mfma_f32_16x16x32_bf16 v[248:251],v[88:91],a[112:115],v[248:251]
buffer_load_dwordx4 a[164:167],v6,s[28:31],s89 offen offset:1024 sc1
v_mfma_f32_16x16x32_bf16 v[248:251],v[84:87],a[104:107],v[248:251]
v_mul_f32 v19,v19,v61
v_mul_f32 v20,v20,v61
v_mfma_f32_16x16x32_bf16 v[248:251],v[80:83],a[96:99],v[248:251]
v_mul_f32 v21,v21,v61
v_mul_f32 v22,v22,v61
v_mfma_f32_16x16x32_bf16 a[192:195],v[96:99],v[32:35],a[192:195]
v_mul_f32 v23,v23,v61
v_cvt_pk_bf16_f32 v172,v16,v17
v_mfma_f32_16x16x32_bf16 a[192:195],v[100:103],v[36:39],a[192:195]
v_cvt_pk_bf16_f32 v173,v18,v19
v_cvt_pk_bf16_f32 v174,v20,v21
v_mfma_f32_16x16x32_bf16 a[192:195],v[104:107],v[40:43],a[192:195]
buffer_load_dwordx4 a[68:71],v6,s[28:31],s20 offen sc1
v_mfma_f32_16x16x32_bf16 a[192:195],v[108:111],v[44:47],a[192:195]
ds_read_b64_tr_b16 v[64:65],v1 offset:64
v_cvt_pk_bf16_f32 v175,v22,v23
v_mfma_f32_16x16x32_bf16 a[196:199],v[108:111],v[180:183],a[196:199]
ds_read_b64_tr_b16 v[66:67],v1 offset:5184
ds_read_b64_tr_b16 v[68:69],v1 offset:10304
v_mfma_f32_16x16x32_bf16 a[196:199],v[104:107],v[164:167],a[196:199]
s_cmp_eq_u32 s60,3
s_cselect_b32 s69,0x40000000,0
ds_read_b64_tr_b16 v[70:71],v1 offset:15424
v_mfma_f32_16x16x32_bf16 a[196:199],v[100:103],v[148:151],a[196:199]
ds_read_b64_tr_b16 v[72:73],v1 offset:20544
v_add_u32 v28,s69,v6
v_mfma_f32_16x16x32_bf16 a[196:199],v[96:99],v[132:135],a[196:199]
buffer_store_dwordx4 v[244:247],v6,s[40:43],s82 offen offset:2048 sc1
v_mfma_f32_16x16x32_bf16 a[200:203],v[96:99],v[196:199],a[200:203]
s_lshl_b32 s69,s63,2
ds_read_b64_tr_b16 v[74:75],v1 offset:25664
ds_read_b64_tr_b16 v[76:77],v1 offset:30784
v_mfma_f32_16x16x32_bf16 a[200:203],v[100:103],v[212:215],a[200:203]
ds_read_b64_tr_b16 v[78:79],v1 offset:35904
v_mov_b32 v8,s52
v_mfma_f32_16x16x32_bf16 a[200:203],v[104:107],v[228:231],a[200:203]
buffer_load_dwordx4 a[148:151],v6,s[28:31],s88 offen offset:1024 sc1
v_mfma_f32_16x16x32_bf16 a[200:203],v[108:111],v[244:247],a[200:203]
ds_read_b64_tr_b16 v[80:81],v2 offset:96
ds_read_b64_tr_b16 v[82:83],v2 offset:5216
v_mfma_f32_16x16x32_bf16 a[204:207],v[108:111],v[188:191],a[204:207]
ds_read_b64_tr_b16 v[84:85],v2 offset:10336
v_mov_b32 v9,s53
v_mfma_f32_16x16x32_bf16 a[204:207],v[104:107],v[172:175],a[204:207]
ds_read_b64_tr_b16 v[86:87],v2 offset:15456
v_lshlrev_b32 v10,20,v6
v_mfma_f32_16x16x32_bf16 a[204:207],v[100:103],v[156:159],a[204:207]
s_waitcnt lgkmcnt(7)
ds_read_b64_tr_b16 v[88:89],v2 offset:20576
v_bfe_u32 v3,v6,4,2
v_mfma_f32_16x16x32_bf16 a[204:207],v[96:99],v[140:143],a[204:207]
buffer_load_dwordx4 a[76:79],v6,s[28:31],s21 offen sc1
v_mfma_f32_16x16x32_bf16 a[208:211],v[112:115],v[32:35],a[208:211]
ds_read_b64_tr_b16 v[90:91],v2 offset:25696
ds_read_b64_tr_b16 v[92:93],v2 offset:30816
v_mfma_f32_16x16x32_bf16 a[208:211],v[116:119],v[36:39],a[208:211]
ds_read_b64_tr_b16 v[94:95],v2 offset:35936
v_min_u32 v3,2,v3
v_mfma_f32_16x16x32_bf16 a[208:211],v[120:123],v[40:43],a[208:211]
ds_read_b64_tr_b16 v[96:97],v1 offset:128
v_add3_u32 v3,v3,s64,1
v_mfma_f32_16x16x32_bf16 a[208:211],v[124:127],v[44:47],a[208:211]
ds_read_b64_tr_b16 v[98:99],v1 offset:5248
v_and_b32 v3,3,v3
v_mfma_f32_16x16x32_bf16 a[212:215],v[124:127],v[180:183],a[212:215]
ds_read_b64_tr_b16 v[100:101],v1 offset:10368
ds_read_b64_tr_b16 v[102:103],v1 offset:15488
v_mfma_f32_16x16x32_bf16 a[212:215],v[120:123],v[164:167],a[212:215]
ds_read_b64_tr_b16 v[104:105],v1 offset:20608
v_lshlrev_b32 v3,6,v3
v_mfma_f32_16x16x32_bf16 a[212:215],v[116:119],v[148:151],a[212:215]
buffer_store_dwordx4 v[140:143],v28,s[40:43],s79 offen offset:3072 sc1
v_mfma_f32_16x16x32_bf16 a[212:215],v[112:115],v[132:135],a[212:215]
ds_read_b64_tr_b16 v[106:107],v1 offset:25728
ds_read_b64_tr_b16 v[108:109],v1 offset:30848
v_mfma_f32_16x16x32_bf16 a[216:219],v[112:115],v[196:199],a[216:219]
)A"
R"A(buffer_load_dwordx4 a[132:135],v6,s[28:31],s87 offen offset:1024 sc1
v_mfma_f32_16x16x32_bf16 a[216:219],v[116:119],v[212:215],a[216:219]
ds_read_b64_tr_b16 v[110:111],v1 offset:35968
v_bfe_u32 v56,v6,6,2
v_mfma_f32_16x16x32_bf16 a[216:219],v[120:123],v[228:231],a[216:219]
v_lshrrev_b32 v57,8,v6
v_cmp_eq_u32 s[56:57],v56,v57
v_mfma_f32_16x16x32_bf16 a[216:219],v[124:127],v[244:247],a[216:219]
v_bfe_u32 v56,v6,4,1
v_lshlrev_b32 v56,4,v56
v_mfma_f32_16x16x32_bf16 a[220:223],v[124:127],v[188:191],a[220:223]
ds_read_b64_tr_b16 v[124:125],v2 offset:30880
ds_read_b64_tr_b16 v[126:127],v2 offset:36000
v_mfma_f32_16x16x32_bf16 a[220:223],v[120:123],v[172:175],a[220:223]
buffer_load_dwordx4 a[84:87],v6,s[28:31],s22 offen sc1
v_mfma_f32_16x16x32_bf16 a[220:223],v[116:119],v[156:159],a[220:223]
ds_read_b64_tr_b16 v[116:117],v2 offset:10400
ds_read_b64_tr_b16 v[118:119],v2 offset:15520
v_mfma_f32_16x16x32_bf16 a[220:223],v[112:115],v[140:143],a[220:223]
ds_read_b64_tr_b16 v[112:113],v2 offset:160
v_mov_b32 v57,0x3f80
v_mfma_f32_16x16x32_bf16 a[224:227],v[64:67],v[32:35],a[224:227]
ds_read_b64_tr_b16 v[114:115],v2 offset:5280
v_lshlrev_b32 v56,v56,v57
v_mfma_f32_16x16x32_bf16 a[224:227],v[68:71],v[36:39],a[224:227]
s_waitcnt lgkmcnt(6)
ds_read_b64_tr_b16 v[120:121],v2 offset:20640
v_cndmask_b32 v56,0,v56,s[56:57]
v_mfma_f32_16x16x32_bf16 a[224:227],v[72:75],v[40:43],a[224:227]
ds_read_b64_tr_b16 v[122:123],v2 offset:25760
v_bfe_u32 v57,v6,5,1
v_mfma_f32_16x16x32_bf16 a[224:227],v[76:79],v[44:47],a[224:227]
s_add_u32 s36,s12,s66
s_addc_u32 s37,s13,0
v_cmp_eq_u32 s[56:57],0,v57
v_mfma_f32_16x16x32_bf16 a[228:231],v[76:79],v[180:183],a[228:231]
buffer_load_dwordx4 a[136:139],v6,s[28:31],s87 offen offset:2048 sc1
v_mfma_f32_16x16x32_bf16 a[228:231],v[72:75],v[164:167],a[228:231]
s_and_b32 s37,s37,0xffff
v_cndmask_b32 v48,0,v56,s[56:57]
v_cndmask_b32 v49,v56,0,s[56:57]
v_mfma_f32_16x16x32_bf16 a[228:231],v[68:71],v[148:151],a[228:231]
buffer_store_dwordx4 v[156:159],v28,s[40:43],s80 offen offset:3072 sc1
v_mfma_f32_16x16x32_bf16 a[228:231],v[64:67],v[132:135],a[228:231]
s_mul_i32 s66,s61,307200
v_mov_b32 v58,s52
v_mov_b32 v59,s53
v_mfma_f32_16x16x32_bf16 a[232:235],v[64:67],v[196:199],a[232:235]
s_add_u32 s24,s16,s66
s_addc_u32 s25,s17,0
v_lshlrev_b32 v60,20,v6
v_mfma_f32_16x16x32_bf16 a[232:235],v[68:71],v[212:215],a[232:235]
buffer_load_dwordx4 a[92:95],v6,s[28:31],s23 offen sc1
v_mfma_f32_16x16x32_bf16 a[232:235],v[72:75],v[228:231],a[232:235]
s_mul_i32 s66,s60,16384
s_add_u32 s66,s66,s67
s_add_u32 s79,s66,12288
v_mfma_f32_16x16x32_bf16 a[232:235],v[76:79],v[244:247],a[232:235]
s_add_u32 s80,s66,77824
v_bfe_u32 v61,v6,4,1
v_add3_u32 v61,v61,s63,2
v_mfma_f32_16x16x32_bf16 a[236:239],v[76:79],v[188:191],a[236:239]
s_and_b32 s25,s25,0xffff
ds_read_b64_tr_b16 v[76:77],v1 offset:30912
ds_read_b64_tr_b16 v[78:79],v1 offset:36032
v_mfma_f32_16x16x32_bf16 a[236:239],v[72:75],v[172:175],a[236:239]
s_add_u32 s67,s77,8192
ds_read_b64_tr_b16 v[72:73],v1 offset:20672
v_and_b32 v61,3,v61
v_mfma_f32_16x16x32_bf16 a[236:239],v[68:71],v[156:159],a[236:239]
ds_read_b64_tr_b16 v[68:69],v1 offset:10432
v_lshlrev_b32 v61,8,v61
v_mfma_f32_16x16x32_bf16 a[236:239],v[64:67],v[140:143],a[236:239]
ds_read_b64_tr_b16 v[64:65],v1 offset:192
v_mov_b32 v50,0
v_mfma_f32_16x16x32_bf16 a[240:243],v[80:83],v[32:35],a[240:243]
s_nop 0
L_pd14_%=:
s_cbranch_execz L_trt1_%=
ds_read_b64_tr_b16 v[66:67],v1 offset:5312
ds_read_b64_tr_b16 v[70:71],v1 offset:15552
v_mfma_f32_16x16x32_bf16 a[240:243],v[84:87],v[36:39],a[240:243]
buffer_load_dwordx4 a[152:155],v6,s[28:31],s88 offen offset:2048 sc1
v_mfma_f32_16x16x32_bf16 a[240:243],v[88:91],v[40:43],a[240:243]
ds_read_b64_tr_b16 v[74:75],v1 offset:25792
v_mov_b32 v51,0
v_mfma_f32_16x16x32_bf16 a[240:243],v[92:95],v[44:47],a[240:243]
v_mov_b32 v52,0
v_mov_b32 v53,0
v_mfma_f32_16x16x32_bf16 a[244:247],v[92:95],v[180:183],a[244:247]
v_mov_b32 v54,v48
v_mov_b32 v55,v49
v_mfma_f32_16x16x32_bf16 a[244:247],v[88:91],v[164:167],a[244:247]
buffer_store_dwordx4 v[172:175],v28,s[40:43],s81 offen offset:3072 sc1
v_mfma_f32_16x16x32_bf16 a[244:247],v[84:87],v[148:151],a[244:247]
s_add_u32 s81,s66,143360
v_bfe_u32 v56,v6,4,4
v_mul_u32_u24 v56,1280,v56
v_mfma_f32_16x16x32_bf16 a[244:247],v[80:83],v[132:135],a[244:247]
buffer_load_dwordx4 a[124:127],v6,s[28:31],s23 offen offset:1024 sc1
v_mfma_f32_16x16x32_bf16 a[248:251],v[80:83],v[196:199],a[248:251]
v_bfe_u32 v57,v6,8,1
v_lshl_add_u32 v56,v57,5,v56
v_mfma_f32_16x16x32_bf16 a[248:251],v[84:87],v[212:215],a[248:251]
v_lshrrev_b32 v57,9,v6
v_lshl_add_u32 v56,v57,4,v56
v_mfma_f32_16x16x32_bf16 a[248:251],v[88:91],v[228:231],a[248:251]
v_mfma_f32_16x16x32_bf16 a[248:251],v[92:95],v[244:247],a[248:251]
v_mfma_f32_16x16x32_bf16 a[252:255],v[92:95],v[188:191],a[252:255]
ds_read_b64_tr_b16 v[92:93],v2 offset:30944
ds_read_b64_tr_b16 v[94:95],v2 offset:36064
v_mfma_f32_16x16x32_bf16 a[252:255],v[88:91],v[172:175],a[252:255]
ds_read_b64_tr_b16 v[88:89],v2 offset:20704
v_mfma_f32_16x16x32_bf16 a[252:255],v[84:87],v[156:159],a[252:255]
buffer_load_dwordx4 a[168:171],v6,s[28:31],s89 offen offset:2048 sc1
v_mfma_f32_16x16x32_bf16 a[252:255],v[80:83],v[140:143],a[252:255]
ds_read_b64_tr_b16 v[80:81],v2 offset:224
ds_read_b64_tr_b16 v[82:83],v2 offset:5344
v_mfma_f32_16x16x32_bf16 a[0:3],v[96:99],v[32:35],a[0:3]
ds_read_b64_tr_b16 v[84:85],v2 offset:10464
v_mfma_f32_16x16x32_bf16 a[0:3],v[100:103],v[36:39],a[0:3]
ds_read_b64_tr_b16 v[86:87],v2 offset:15584
v_mfma_f32_16x16x32_bf16 a[0:3],v[104:107],v[40:43],a[0:3]
ds_read_b64_tr_b16 v[90:91],v2 offset:25824
v_mfma_f32_16x16x32_bf16 a[0:3],v[108:111],v[44:47],a[0:3]
buffer_load_dwordx4 a[116:119],v6,s[28:31],s22 offen offset:1024 sc1
v_mfma_f32_16x16x32_bf16 a[4:7],v[108:111],v[180:183],a[4:7]
v_mfma_f32_16x16x32_bf16 a[4:7],v[104:107],v[164:167],a[4:7]
buffer_store_dwordx4 v[188:191],v28,s[40:43],s82 offen offset:3072 sc1
v_mfma_f32_16x16x32_bf16 a[4:7],v[100:103],v[148:151],a[4:7]
s_add_u32 s82,s66,208896
s_mul_i32 s66,s64,320
v_mfma_f32_16x16x32_bf16 a[4:7],v[96:99],v[132:135],a[4:7]
v_mfma_f32_16x16x32_bf16 a[8:11],v[96:99],v[196:199],a[8:11]
v_mfma_f32_16x16x32_bf16 a[8:11],v[100:103],v[212:215],a[8:11]
v_mfma_f32_16x16x32_bf16 a[8:11],v[104:107],v[228:231],a[8:11]
v_mfma_f32_16x16x32_bf16 a[8:11],v[108:111],v[244:247],a[8:11]
buffer_load_dwordx4 a[184:187],v6,s[28:31],s90 offen offset:2048 sc1
v_mfma_f32_16x16x32_bf16 a[12:15],v[108:111],v[188:191],a[12:15]
ds_read_b64_tr_b16 v[108:109],v1 offset:30976
ds_read_b64_tr_b16 v[110:111],v1 offset:36096
v_mfma_f32_16x16x32_bf16 a[12:15],v[104:107],v[172:175],a[12:15]
ds_read_b64_tr_b16 v[104:105],v1 offset:20736
v_mfma_f32_16x16x32_bf16 a[12:15],v[100:103],v[156:159],a[12:15]
ds_read_b64_tr_b16 v[100:101],v1 offset:10496
v_mfma_f32_16x16x32_bf16 a[12:15],v[96:99],v[140:143],a[12:15]
s_waitcnt lgkmcnt(4)
ds_read_b64_tr_b16 v[96:97],v1 offset:256
v_mfma_f32_16x16x32_bf16 a[16:19],v[112:115],v[32:35],a[16:19]
buffer_load_dwordx4 a[108:111],v6,s[28:31],s21 offen offset:1024 sc1
v_mfma_f32_16x16x32_bf16 a[16:19],v[116:119],v[36:39],a[16:19]
ds_read_b64_tr_b16 v[98:99],v1 offset:5376
ds_read_b64_tr_b16 v[102:103],v1 offset:15616
v_mfma_f32_16x16x32_bf16 a[16:19],v[120:123],v[40:43],a[16:19]
ds_read_b64_tr_b16 v[106:107],v1 offset:25856
v_add_u32 v1,0xffff6000,v1
v_mfma_f32_16x16x32_bf16 a[16:19],v[124:127],v[44:47],a[16:19]
v_mfma_f32_16x16x32_bf16 a[20:23],v[124:127],v[180:183],a[20:23]
v_mfma_f32_16x16x32_bf16 a[20:23],v[120:123],v[164:167],a[20:23]
v_mfma_f32_16x16x32_bf16 a[20:23],v[116:119],v[148:151],a[20:23]
v_mfma_f32_16x16x32_bf16 a[20:23],v[112:115],v[132:135],a[20:23]
v_mfma_f32_16x16x32_bf16 a[24:27],v[112:115],v[196:199],a[24:27]
buffer_load_dwordx4 a[188:191],v28,s[28:31],s90 offen offset:3072 sc1
v_mfma_f32_16x16x32_bf16 a[24:27],v[116:119],v[212:215],a[24:27]
v_mfma_f32_16x16x32_bf16 a[24:27],v[120:123],v[228:231],a[24:27]
v_mfma_f32_16x16x32_bf16 a[24:27],v[124:127],v[244:247],a[24:27]
v_mfma_f32_16x16x32_bf16 a[28:31],v[124:127],v[188:191],a[28:31]
ds_read_b64_tr_b16 v[124:125],v2 offset:31008
ds_read_b64_tr_b16 v[126:127],v2 offset:36128
v_mfma_f32_16x16x32_bf16 a[28:31],v[120:123],v[172:175],a[28:31]
buffer_load_dwordx4 a[100:103],v6,s[28:31],s20 offen offset:1024 sc1
v_mfma_f32_16x16x32_bf16 a[28:31],v[116:119],v[156:159],a[28:31]
ds_read_b64_tr_b16 v[116:117],v2 offset:10528
ds_read_b64_tr_b16 v[118:119],v2 offset:15648
v_mfma_f32_16x16x32_bf16 a[28:31],v[112:115],v[140:143],a[28:31]
ds_read_b64_tr_b16 v[112:113],v2 offset:288
v_mfma_f32_16x16x32_bf16 v[128:131],v[64:67],v[32:35],v[128:131]
ds_read_b64_tr_b16 v[114:115],v2 offset:5408
v_mfma_f32_16x16x32_bf16 v[128:131],v[68:71],v[36:39],v[128:131]
ds_read_b64_tr_b16 v[120:121],v2 offset:20768
v_mfma_f32_16x16x32_bf16 v[128:131],v[72:75],v[40:43],v[128:131]
ds_read_b64_tr_b16 v[122:123],v2 offset:25888
v_add_u32 v2,0xffff6000,v2
v_mfma_f32_16x16x32_bf16 v[128:131],v[76:79],v[44:47],v[128:131]
v_mfma_f32_16x16x32_bf16 v[136:139],v[76:79],v[180:183],v[136:139]
v_mfma_f32_16x16x32_bf16 v[136:139],v[72:75],v[164:167],v[136:139]
buffer_load_dwordx4 a[172:175],v28,s[28:31],s89 offen offset:3072 sc1
v_mfma_f32_16x16x32_bf16 v[136:139],v[68:71],v[148:151],v[136:139]
v_mfma_f32_16x16x32_bf16 v[136:139],v[64:67],v[132:135],v[136:139]
v_mfma_f32_16x16x32_bf16 v[144:147],v[64:67],v[196:199],v[144:147]
v_mfma_f32_16x16x32_bf16 v[144:147],v[68:71],v[212:215],v[144:147]
v_mfma_f32_16x16x32_bf16 v[144:147],v[72:75],v[228:231],v[144:147]
buffer_load_dwordx4 a[64:67],v6,s[28:31],s20 offen offset:2048 sc1
v_mfma_f32_16x16x32_bf16 v[144:147],v[76:79],v[244:247],v[144:147]
v_mfma_f32_16x16x32_bf16 v[152:155],v[76:79],v[188:191],v[152:155]
v_mfma_f32_16x16x32_bf16 v[152:155],v[72:75],v[172:175],v[152:155]
v_mfma_f32_16x16x32_bf16 v[152:155],v[68:71],v[156:159],v[152:155]
v_mfma_f32_16x16x32_bf16 v[152:155],v[64:67],v[140:143],v[152:155]
s_mov_b64 exec,0
s_cmp_eq_u32 s60,0
s_cbranch_scc1 L_pd16_%=
s_cmp_eq_u32 s60,1
s_cbranch_scc1 L_pd17_%=
s_cmp_eq_u32 s60,2
s_cbranch_scc1 L_pd18_%=
s_branch L_trtg2F_%=
L_trtg2F_%=:
s_mov_b64 exec,-1
s_waitcnt lgkmcnt(0)
s_barrier
v_mfma_f32_16x16x32_bf16 v[160:163],v[80:83],v[32:35],v[160:163]
buffer_load_dwordx4 v24,s[48:51],s83 offen lds
v_mfma_f32_16x16x32_bf16 v[160:163],v[84:87],v[36:39],v[160:163]
ds_read_b64_tr_b16 v[64:65],v1
ds_read_b64_tr_b16 v[66:67],v1 offset:5120
v_mfma_f32_16x16x32_bf16 v[160:163],v[88:91],v[40:43],v[160:163]
ds_read_b64_tr_b16 v[68:69],v1 offset:10240
v_mfma_f32_16x16x32_bf16 v[160:163],v[92:95],v[44:47],v[160:163]
ds_read_b64_tr_b16 v[70:71],v1 offset:15360
v_mfma_f32_16x16x32_bf16 v[168:171],v[92:95],v[180:183],v[168:171]
ds_read_b64_tr_b16 v[72:73],v1 offset:20480
v_mfma_f32_16x16x32_bf16 v[168:171],v[88:91],v[164:167],v[168:171]
buffer_load_dwordx4 a[156:159],v28,s[28:31],s88 offen offset:3072 sc1
v_mfma_f32_16x16x32_bf16 v[168:171],v[84:87],v[148:151],v[168:171]
ds_read_b64_tr_b16 v[74:75],v1 offset:25600
ds_read_b64_tr_b16 v[76:77],v1 offset:30720
v_mfma_f32_16x16x32_bf16 v[168:171],v[80:83],v[132:135],v[168:171]
ds_read_b64_tr_b16 v[78:79],v1 offset:35840
v_mfma_f32_16x16x32_bf16 v[176:179],v[80:83],v[196:199],v[176:179]
v_mfma_f32_16x16x32_bf16 v[176:179],v[84:87],v[212:215],v[176:179]
v_mfma_f32_16x16x32_bf16 v[176:179],v[88:91],v[228:231],v[176:179]
buffer_load_dwordx4 a[72:75],v6,s[28:31],s21 offen offset:2048 sc1
v_mfma_f32_16x16x32_bf16 v[176:179],v[92:95],v[244:247],v[176:179]
v_mfma_f32_16x16x32_bf16 v[184:187],v[92:95],v[188:191],v[184:187]
ds_read_b64_tr_b16 v[92:93],v2 offset:30752
ds_read_b64_tr_b16 v[94:95],v2 offset:35872
v_mfma_f32_16x16x32_bf16 v[184:187],v[88:91],v[172:175],v[184:187]
ds_read_b64_tr_b16 v[88:89],v2 offset:20512
v_mfma_f32_16x16x32_bf16 v[184:187],v[84:87],v[156:159],v[184:187]
ds_read_b64_tr_b16 v[84:85],v2 offset:10272
v_mfma_f32_16x16x32_bf16 v[184:187],v[80:83],v[140:143],v[184:187]
buffer_load_dwordx4 a[140:143],v28,s[28:31],s87 offen offset:3072 sc1
v_mfma_f32_16x16x32_bf16 v[192:195],v[96:99],v[32:35],v[192:195]
)A"
R"A(s_add_u32 s87,s64,3
ds_read_b64_tr_b16 v[80:81],v2 offset:32
ds_read_b64_tr_b16 v[82:83],v2 offset:5152
v_mfma_f32_16x16x32_bf16 v[192:195],v[100:103],v[36:39],v[192:195]
s_and_b32 s87,s87,3
s_lshl_b32 s87,s87,1
ds_read_b64_tr_b16 v[86:87],v2 offset:15392
v_mfma_f32_16x16x32_bf16 v[192:195],v[104:107],v[40:43],v[192:195]
s_lshl_b32 s87,s87,16
s_add_u32 s88,s87,4096
ds_read_b64_tr_b16 v[90:91],v2 offset:25632
v_mfma_f32_16x16x32_bf16 v[192:195],v[108:111],v[44:47],v[192:195]
s_add_u32 s89,s87,8192
s_add_u32 s90,s87,12288
v_mfma_f32_16x16x32_bf16 v[200:203],v[108:111],v[180:183],v[200:203]
buffer_load_dwordx4 a[80:83],v6,s[28:31],s22 offen offset:2048 sc1
v_mfma_f32_16x16x32_bf16 v[200:203],v[104:107],v[164:167],v[200:203]
v_mfma_f32_16x16x32_bf16 v[200:203],v[100:103],v[148:151],v[200:203]
v_mfma_f32_16x16x32_bf16 v[200:203],v[96:99],v[132:135],v[200:203]
v_mfma_f32_16x16x32_bf16 v[208:211],v[96:99],v[196:199],v[208:211]
v_mfma_f32_16x16x32_bf16 v[208:211],v[100:103],v[212:215],v[208:211]
v_mfma_f32_16x16x32_bf16 v[208:211],v[104:107],v[228:231],v[208:211]
v_mfma_f32_16x16x32_bf16 v[208:211],v[108:111],v[244:247],v[208:211]
buffer_load_dwordx4 v25,s[48:51],s83 offen offset:1024 lds
v_mfma_f32_16x16x32_bf16 v[216:219],v[108:111],v[188:191],v[216:219]
ds_read_b64_tr_b16 v[108:109],v1 offset:30784
ds_read_b64_tr_b16 v[110:111],v1 offset:35904
v_mfma_f32_16x16x32_bf16 v[216:219],v[104:107],v[172:175],v[216:219]
ds_read_b64_tr_b16 v[104:105],v1 offset:20544
v_mfma_f32_16x16x32_bf16 v[216:219],v[100:103],v[156:159],v[216:219]
ds_read_b64_tr_b16 v[100:101],v1 offset:10304
v_mfma_f32_16x16x32_bf16 v[216:219],v[96:99],v[140:143],v[216:219]
ds_read_b64_tr_b16 v[96:97],v1 offset:64
v_mfma_f32_16x16x32_bf16 v[224:227],v[112:115],v[32:35],v[224:227]
buffer_load_dwordx4 a[88:91],v6,s[28:31],s23 offen offset:2048 sc1
v_mfma_f32_16x16x32_bf16 v[224:227],v[116:119],v[36:39],v[224:227]
ds_read_b64_tr_b16 v[98:99],v1 offset:5184
ds_read_b64_tr_b16 v[102:103],v1 offset:15424
v_mfma_f32_16x16x32_bf16 v[224:227],v[120:123],v[40:43],v[224:227]
ds_read_b64_tr_b16 v[106:107],v1 offset:25664
v_mfma_f32_16x16x32_bf16 v[224:227],v[124:127],v[44:47],v[224:227]
v_mfma_f32_16x16x32_bf16 v[232:235],v[124:127],v[180:183],v[232:235]
v_mfma_f32_16x16x32_bf16 v[232:235],v[120:123],v[164:167],v[232:235]
v_mfma_f32_16x16x32_bf16 v[232:235],v[116:119],v[148:151],v[232:235]
v_mfma_f32_16x16x32_bf16 v[232:235],v[112:115],v[132:135],v[232:235]
buffer_load_dwordx4 v26,s[48:51],s83 offen offset:2048 lds
v_mfma_f32_16x16x32_bf16 v[240:243],v[112:115],v[196:199],v[240:243]
v_mfma_f32_16x16x32_bf16 v[240:243],v[116:119],v[212:215],v[240:243]
v_mfma_f32_16x16x32_bf16 v[240:243],v[120:123],v[228:231],v[240:243]
v_mfma_f32_16x16x32_bf16 v[240:243],v[124:127],v[244:247],v[240:243]
v_mfma_f32_16x16x32_bf16 v[248:251],v[124:127],v[188:191],v[248:251]
ds_read_b64_tr_b16 v[124:125],v2 offset:30816
ds_read_b64_tr_b16 v[126:127],v2 offset:35936
v_mfma_f32_16x16x32_bf16 v[248:251],v[120:123],v[172:175],v[248:251]
buffer_load_dwordx4 a[120:123],v28,s[28:31],s23 offen offset:3072 sc1
v_mfma_f32_16x16x32_bf16 v[248:251],v[116:119],v[156:159],v[248:251]
ds_read_b64_tr_b16 v[116:117],v2 offset:10336
ds_read_b64_tr_b16 v[118:119],v2 offset:15456
v_mfma_f32_16x16x32_bf16 v[248:251],v[112:115],v[140:143],v[248:251]
s_waitcnt vmcnt(39) lgkmcnt(4)
ds_read_b64_tr_b16 v[112:113],v2 offset:96
v_mfma_f32_16x16x32_bf16 a[192:195],v[64:67],a[128:131],a[192:195]
ds_read_b64_tr_b16 v[114:115],v2 offset:5216
v_mfma_f32_16x16x32_bf16 a[192:195],v[68:71],a[144:147],a[192:195]
s_waitcnt vmcnt(37)
ds_read_b64_tr_b16 v[120:121],v2 offset:20576
v_mfma_f32_16x16x32_bf16 a[192:195],v[72:75],a[160:163],a[192:195]
s_waitcnt vmcnt(35)
ds_read_b64_tr_b16 v[122:123],v2 offset:25696
v_mfma_f32_16x16x32_bf16 a[192:195],v[76:79],a[176:179],a[192:195]
s_waitcnt vmcnt(27)
v_mfma_f32_16x16x32_bf16 a[196:199],v[76:79],a[180:183],a[196:199]
buffer_load_dwordx4 v27,s[48:51],s83 offen offset:3072 lds
v_mfma_f32_16x16x32_bf16 a[196:199],v[72:75],a[164:167],a[196:199]
s_add_u32 m0,s86,126976
v_mfma_f32_16x16x32_bf16 a[196:199],v[68:71],a[148:151],a[196:199]
s_waitcnt vmcnt(24)
v_mfma_f32_16x16x32_bf16 a[196:199],v[64:67],a[132:135],a[196:199]
v_mfma_f32_16x16x32_bf16 a[200:203],v[64:67],a[136:139],a[200:203]
s_waitcnt vmcnt(21)
v_mfma_f32_16x16x32_bf16 a[200:203],v[68:71],a[152:155],a[200:203]
s_waitcnt vmcnt(15)
v_mfma_f32_16x16x32_bf16 a[200:203],v[72:75],a[168:171],a[200:203]
buffer_load_dwordx4 a[112:115],v28,s[28:31],s22 offen offset:3072 sc1
v_mfma_f32_16x16x32_bf16 a[200:203],v[76:79],a[184:187],a[200:203]
s_waitcnt vmcnt(14)
v_mfma_f32_16x16x32_bf16 a[204:207],v[76:79],a[188:191],a[204:207]
s_waitcnt vmcnt(12)
ds_read_b64_tr_b16 v[76:77],v1 offset:30848
ds_read_b64_tr_b16 v[78:79],v1 offset:35968
v_mfma_f32_16x16x32_bf16 a[204:207],v[72:75],a[172:175],a[204:207]
s_waitcnt vmcnt(9)
ds_read_b64_tr_b16 v[72:73],v1 offset:20608
v_mfma_f32_16x16x32_bf16 a[204:207],v[68:71],a[156:159],a[204:207]
s_waitcnt vmcnt(7)
ds_read_b64_tr_b16 v[68:69],v1 offset:10368
v_mfma_f32_16x16x32_bf16 a[204:207],v[64:67],a[140:143],a[204:207]
ds_read_b64_tr_b16 v[64:65],v1 offset:128
v_mfma_f32_16x16x32_bf16 a[208:211],v[80:83],a[128:131],a[208:211]
ds_read_b64_tr_b16 v[66:67],v1 offset:5248
ds_read_b64_tr_b16 v[70:71],v1 offset:15488
v_mfma_f32_16x16x32_bf16 a[208:211],v[84:87],a[144:147],a[208:211]
buffer_load_dwordx4 v7,s[48:51],s83 offen lds
v_mfma_f32_16x16x32_bf16 a[208:211],v[88:91],a[160:163],a[208:211]
s_add_u32 m0,s86,128000
s_add_u32 s83,s64,1
ds_read_b64_tr_b16 v[74:75],v1 offset:25728
v_mfma_f32_16x16x32_bf16 a[208:211],v[92:95],a[176:179],a[208:211]
s_and_b32 s83,s83,3
s_lshl_b32 s83,s83,1
s_or_b32 s83,s83,1
v_mfma_f32_16x16x32_bf16 a[212:215],v[92:95],a[180:183],a[212:215]
s_mul_i32 s83,s83,163840
s_add_u32 s83,s83,s85
v_mfma_f32_16x16x32_bf16 a[212:215],v[88:91],a[164:167],a[212:215]
v_mfma_f32_16x16x32_bf16 a[212:215],v[84:87],a[148:151],a[212:215]
v_mfma_f32_16x16x32_bf16 a[212:215],v[80:83],a[132:135],a[212:215]
buffer_load_dwordx4 a[104:107],v28,s[28:31],s21 offen offset:3072 sc1
v_mfma_f32_16x16x32_bf16 a[216:219],v[80:83],a[136:139],a[216:219]
v_mfma_f32_16x16x32_bf16 a[216:219],v[84:87],a[152:155],a[216:219]
v_mfma_f32_16x16x32_bf16 a[216:219],v[88:91],a[168:171],a[216:219]
v_mfma_f32_16x16x32_bf16 a[216:219],v[92:95],a[184:187],a[216:219]
v_mfma_f32_16x16x32_bf16 a[220:223],v[92:95],a[188:191],a[220:223]
ds_read_b64_tr_b16 v[92:93],v2 offset:30880
ds_read_b64_tr_b16 v[94:95],v2 offset:36000
v_mfma_f32_16x16x32_bf16 a[220:223],v[88:91],a[172:175],a[220:223]
ds_read_b64_tr_b16 v[88:89],v2 offset:20640
v_mfma_f32_16x16x32_bf16 a[220:223],v[84:87],a[156:159],a[220:223]
buffer_load_dwordx4 v24,s[48:51],s84 offen lds
v_mfma_f32_16x16x32_bf16 a[220:223],v[80:83],a[140:143],a[220:223]
ds_read_b64_tr_b16 v[80:81],v2 offset:160
ds_read_b64_tr_b16 v[82:83],v2 offset:5280
s_barrier
v_mfma_f32_16x16x32_bf16 a[224:227],v[96:99],a[128:131],a[224:227]
ds_read_b64_tr_b16 v[84:85],v2 offset:10400
v_mfma_f32_16x16x32_bf16 a[224:227],v[100:103],a[144:147],a[224:227]
ds_read_b64_tr_b16 v[86:87],v2 offset:15520
v_mfma_f32_16x16x32_bf16 a[224:227],v[104:107],a[160:163],a[224:227]
buffer_store_dwordx2 v[8:9],v10,s[44:47],s18 offen offset:1280 sc1
v_mfma_f32_16x16x32_bf16 a[224:227],v[108:111],a[176:179],a[224:227]
ds_read_b64_tr_b16 v[90:91],v2 offset:25760
v_mfma_f32_16x16x32_bf16 a[228:231],v[108:111],a[180:183],a[228:231]
v_mfma_f32_16x16x32_bf16 a[228:231],v[104:107],a[164:167],a[228:231]
v_mfma_f32_16x16x32_bf16 a[228:231],v[100:103],a[148:151],a[228:231]
v_mfma_f32_16x16x32_bf16 a[228:231],v[96:99],a[132:135],a[228:231]
buffer_load_dwordx4 a[96:99],v28,s[28:31],s20 offen offset:3072 sc1
v_mfma_f32_16x16x32_bf16 a[232:235],v[96:99],a[136:139],a[232:235]
s_add_u32 s20,s64,1
s_and_b32 s20,s20,3
s_lshl_b32 s20,s20,1
v_mfma_f32_16x16x32_bf16 a[232:235],v[100:103],a[152:155],a[232:235]
s_or_b32 s20,s20,1
s_lshl_b32 s20,s20,16
s_add_u32 s21,s20,4096
v_mfma_f32_16x16x32_bf16 a[232:235],v[104:107],a[168:171],a[232:235]
s_add_u32 s22,s20,8192
s_add_u32 s23,s20,12288
v_mfma_f32_16x16x32_bf16 a[232:235],v[108:111],a[184:187],a[232:235]
v_mfma_f32_16x16x32_bf16 a[236:239],v[108:111],a[188:191],a[236:239]
ds_read_b64_tr_b16 v[108:109],v1 offset:30912
ds_read_b64_tr_b16 v[110:111],v1 offset:36032
v_mfma_f32_16x16x32_bf16 a[236:239],v[104:107],a[172:175],a[236:239]
ds_read_b64_tr_b16 v[104:105],v1 offset:20672
v_mfma_f32_16x16x32_bf16 a[236:239],v[100:103],a[156:159],a[236:239]
buffer_load_dwordx4 v25,s[48:51],s84 offen offset:1024 lds
v_mfma_f32_16x16x32_bf16 a[236:239],v[96:99],a[140:143],a[236:239]
s_waitcnt lgkmcnt(3)
ds_read_b64_tr_b16 v[96:97],v1 offset:192
ds_read_b64_tr_b16 v[98:99],v1 offset:5312
v_mfma_f32_16x16x32_bf16 a[240:243],v[112:115],a[128:131],a[240:243]
ds_read_b64_tr_b16 v[100:101],v1 offset:10432
v_mfma_f32_16x16x32_bf16 a[240:243],v[116:119],a[144:147],a[240:243]
ds_read_b64_tr_b16 v[102:103],v1 offset:15552
v_mfma_f32_16x16x32_bf16 a[240:243],v[120:123],a[160:163],a[240:243]
ds_read_b64_tr_b16 v[106:107],v1 offset:25792
v_mfma_f32_16x16x32_bf16 a[240:243],v[124:127],a[176:179],a[240:243]
v_mfma_f32_16x16x32_bf16 a[244:247],v[124:127],a[180:183],a[244:247]
v_mfma_f32_16x16x32_bf16 a[244:247],v[120:123],a[164:167],a[244:247]
buffer_load_dwordx4 v26,s[48:51],s84 offen offset:2048 lds
v_mfma_f32_16x16x32_bf16 a[244:247],v[116:119],a[148:151],a[244:247]
v_mfma_f32_16x16x32_bf16 a[244:247],v[112:115],a[132:135],a[244:247]
v_mfma_f32_16x16x32_bf16 a[248:251],v[112:115],a[136:139],a[248:251]
v_mfma_f32_16x16x32_bf16 a[248:251],v[116:119],a[152:155],a[248:251]
v_mfma_f32_16x16x32_bf16 a[248:251],v[120:123],a[168:171],a[248:251]
v_mfma_f32_16x16x32_bf16 a[248:251],v[124:127],a[184:187],a[248:251]
v_mfma_f32_16x16x32_bf16 a[252:255],v[124:127],a[188:191],a[252:255]
buffer_load_dwordx4 v27,s[48:51],s84 offen offset:3072 lds
v_mfma_f32_16x16x32_bf16 a[252:255],v[120:123],a[172:175],a[252:255]
s_add_u32 m0,s86,132096
ds_read_b64_tr_b16 v[120:121],v2 offset:20704
ds_read_b64_tr_b16 v[122:123],v2 offset:25824
v_mfma_f32_16x16x32_bf16 a[252:255],v[116:119],a[156:159],a[252:255]
ds_read_b64_tr_b16 v[116:117],v2 offset:10464
v_mfma_f32_16x16x32_bf16 a[252:255],v[112:115],a[140:143],a[252:255]
ds_read_b64_tr_b16 v[112:113],v2 offset:224
v_mfma_f32_16x16x32_bf16 a[0:3],v[64:67],a[128:131],a[0:3]
ds_read_b64_tr_b16 v[114:115],v2 offset:5344
v_mfma_f32_16x16x32_bf16 a[0:3],v[68:71],a[144:147],a[0:3]
ds_read_b64_tr_b16 v[118:119],v2 offset:15584
ds_read_b64_tr_b16 v[124:125],v2 offset:30944
v_mfma_f32_16x16x32_bf16 a[0:3],v[72:75],a[160:163],a[0:3]
ds_read_b64_tr_b16 v[126:127],v2 offset:36064
v_mfma_f32_16x16x32_bf16 a[0:3],v[76:79],a[176:179],a[0:3]
buffer_load_dwordx4 v7,s[48:51],s84 offen lds
v_mfma_f32_16x16x32_bf16 a[4:7],v[76:79],a[180:183],a[4:7]
s_add_u32 m0,s86,81920
s_add_u32 s84,s83,20480
v_mfma_f32_16x16x32_bf16 a[4:7],v[72:75],a[164:167],a[4:7]
v_mfma_f32_16x16x32_bf16 a[4:7],v[68:71],a[148:151],a[4:7]
v_mfma_f32_16x16x32_bf16 a[4:7],v[64:67],a[132:135],a[4:7]
v_mfma_f32_16x16x32_bf16 a[8:11],v[64:67],a[136:139],a[8:11]
v_mfma_f32_16x16x32_bf16 a[8:11],v[68:71],a[152:155],a[8:11]
v_mfma_f32_16x16x32_bf16 a[8:11],v[72:75],a[168:171],a[8:11]
buffer_load_dwordx4 v24,s[48:51],s83 offen lds
v_mfma_f32_16x16x32_bf16 a[8:11],v[76:79],a[184:187],a[8:11]
v_mfma_f32_16x16x32_bf16 a[12:15],v[76:79],a[188:191],a[12:15]
ds_read_b64_tr_b16 v[76:77],v1 offset:30976
ds_read_b64_tr_b16 v[78:79],v1 offset:36096
v_mfma_f32_16x16x32_bf16 a[12:15],v[72:75],a[172:175],a[12:15]
ds_read_b64_tr_b16 v[72:73],v1 offset:20736
v_mfma_f32_16x16x32_bf16 a[12:15],v[68:71],a[156:159],a[12:15]
ds_read_b64_tr_b16 v[68:69],v1 offset:10496
v_mfma_f32_16x16x32_bf16 a[12:15],v[64:67],a[140:143],a[12:15]
ds_read_b64_tr_b16 v[64:65],v1 offset:256
v_mfma_f32_16x16x32_bf16 a[16:19],v[80:83],a[128:131],a[16:19]
ds_read_b64_tr_b16 v[66:67],v1 offset:5376
ds_read_b64_tr_b16 v[70:71],v1 offset:15616
v_mfma_f32_16x16x32_bf16 a[16:19],v[84:87],a[144:147],a[16:19]
)A"
R"A(buffer_load_dwordx4 v25,s[48:51],s83 offen offset:1024 lds
v_mfma_f32_16x16x32_bf16 a[16:19],v[88:91],a[160:163],a[16:19]
ds_read_b64_tr_b16 v[74:75],v1 offset:25856
v_add_u32 v1,0xffff6000,v1
v_mfma_f32_16x16x32_bf16 a[16:19],v[92:95],a[176:179],a[16:19]
v_mfma_f32_16x16x32_bf16 a[20:23],v[92:95],a[180:183],a[20:23]
v_mfma_f32_16x16x32_bf16 a[20:23],v[88:91],a[164:167],a[20:23]
v_mfma_f32_16x16x32_bf16 a[20:23],v[84:87],a[148:151],a[20:23]
v_mfma_f32_16x16x32_bf16 a[20:23],v[80:83],a[132:135],a[20:23]
v_mfma_f32_16x16x32_bf16 a[24:27],v[80:83],a[136:139],a[24:27]
buffer_load_dwordx4 v26,s[48:51],s83 offen offset:2048 lds
v_mfma_f32_16x16x32_bf16 a[24:27],v[84:87],a[152:155],a[24:27]
v_mfma_f32_16x16x32_bf16 a[24:27],v[88:91],a[168:171],a[24:27]
v_mfma_f32_16x16x32_bf16 a[24:27],v[92:95],a[184:187],a[24:27]
v_mfma_f32_16x16x32_bf16 a[28:31],v[92:95],a[188:191],a[28:31]
ds_read_b64_tr_b16 v[92:93],v2 offset:31008
ds_read_b64_tr_b16 v[94:95],v2 offset:36128
v_mfma_f32_16x16x32_bf16 a[28:31],v[88:91],a[172:175],a[28:31]
ds_read_b64_tr_b16 v[88:89],v2 offset:20768
v_mfma_f32_16x16x32_bf16 a[28:31],v[84:87],a[156:159],a[28:31]
s_waitcnt lgkmcnt(3)
ds_read_b64_tr_b16 v[84:85],v2 offset:10528
v_mfma_f32_16x16x32_bf16 a[28:31],v[80:83],a[140:143],a[28:31]
buffer_load_dwordx4 v27,s[48:51],s83 offen offset:3072 lds
v_mfma_f32_16x16x32_bf16 v[128:131],v[96:99],a[128:131],v[128:131]
s_add_u32 m0,s86,86016
ds_read_b64_tr_b16 v[80:81],v2 offset:288
ds_read_b64_tr_b16 v[82:83],v2 offset:5408
v_mfma_f32_16x16x32_bf16 v[128:131],v[100:103],a[144:147],v[128:131]
ds_read_b64_tr_b16 v[86:87],v2 offset:15648
v_mfma_f32_16x16x32_bf16 v[128:131],v[104:107],a[160:163],v[128:131]
ds_read_b64_tr_b16 v[90:91],v2 offset:25888
v_add_u32 v2,0xffff6000,v2
v_mfma_f32_16x16x32_bf16 v[128:131],v[108:111],a[176:179],v[128:131]
v_mfma_f32_16x16x32_bf16 v[136:139],v[108:111],a[180:183],v[136:139]
v_mfma_f32_16x16x32_bf16 v[136:139],v[104:107],a[164:167],v[136:139]
v_mfma_f32_16x16x32_bf16 v[136:139],v[100:103],a[148:151],v[136:139]
buffer_load_dwordx4 v7,s[48:51],s83 offen lds
v_mfma_f32_16x16x32_bf16 v[136:139],v[96:99],a[132:135],v[136:139]
s_add_u32 m0,s86,87040
s_add_u32 s83,s64,2
s_and_b32 s83,s83,3
v_mfma_f32_16x16x32_bf16 v[144:147],v[96:99],a[136:139],v[144:147]
s_lshl_b32 s83,s83,1
s_or_b32 s83,s83,1
s_mul_i32 s83,s83,163840
v_mfma_f32_16x16x32_bf16 v[144:147],v[100:103],a[152:155],v[144:147]
s_add_u32 s83,s83,s85
v_mfma_f32_16x16x32_bf16 v[144:147],v[104:107],a[168:171],v[144:147]
v_mfma_f32_16x16x32_bf16 v[144:147],v[108:111],a[184:187],v[144:147]
v_mfma_f32_16x16x32_bf16 v[152:155],v[108:111],a[188:191],v[152:155]
ds_read_b64_tr_b16 v[108:109],v1 offset:30720
ds_read_b64_tr_b16 v[110:111],v1 offset:35840
v_mfma_f32_16x16x32_bf16 v[152:155],v[104:107],a[172:175],v[152:155]
buffer_load_dwordx4 v24,s[48:51],s84 offen lds
v_mfma_f32_16x16x32_bf16 v[152:155],v[100:103],a[156:159],v[152:155]
ds_read_b64_tr_b16 v[100:101],v1 offset:10240
ds_read_b64_tr_b16 v[102:103],v1 offset:15360
v_mfma_f32_16x16x32_bf16 v[152:155],v[96:99],a[140:143],v[152:155]
ds_read_b64_tr_b16 v[96:97],v1
s_waitcnt lgkmcnt(5)
s_barrier
v_mfma_f32_16x16x32_bf16 v[160:163],v[112:115],a[128:131],v[160:163]
ds_read_b64_tr_b16 v[98:99],v1 offset:5120
v_mfma_f32_16x16x32_bf16 v[160:163],v[116:119],a[144:147],v[160:163]
ds_read_b64_tr_b16 v[104:105],v1 offset:20480
v_mfma_f32_16x16x32_bf16 v[160:163],v[120:123],a[160:163],v[160:163]
ds_read_b64_tr_b16 v[106:107],v1 offset:25600
v_mfma_f32_16x16x32_bf16 v[160:163],v[124:127],a[176:179],v[160:163]
buffer_load_dwordx4 v25,s[48:51],s84 offen offset:1024 lds
v_mfma_f32_16x16x32_bf16 v[168:171],v[124:127],a[180:183],v[168:171]
v_mfma_f32_16x16x32_bf16 v[168:171],v[120:123],a[164:167],v[168:171]
v_mfma_f32_16x16x32_bf16 v[168:171],v[116:119],a[148:151],v[168:171]
v_mfma_f32_16x16x32_bf16 v[168:171],v[112:115],a[132:135],v[168:171]
v_mfma_f32_16x16x32_bf16 v[176:179],v[112:115],a[136:139],v[176:179]
v_mfma_f32_16x16x32_bf16 v[176:179],v[116:119],a[152:155],v[176:179]
v_mfma_f32_16x16x32_bf16 v[176:179],v[120:123],a[168:171],v[176:179]
buffer_load_dwordx4 v26,s[48:51],s84 offen offset:2048 lds
v_mfma_f32_16x16x32_bf16 v[176:179],v[124:127],a[184:187],v[176:179]
v_mfma_f32_16x16x32_bf16 v[184:187],v[124:127],a[188:191],v[184:187]
ds_read_b64_tr_b16 v[124:125],v2 offset:30752
ds_read_b64_tr_b16 v[126:127],v2 offset:35872
v_mfma_f32_16x16x32_bf16 v[184:187],v[120:123],a[172:175],v[184:187]
ds_read_b64_tr_b16 v[120:121],v2 offset:20512
v_mfma_f32_16x16x32_bf16 v[184:187],v[116:119],a[156:159],v[184:187]
ds_read_b64_tr_b16 v[116:117],v2 offset:10272
v_mfma_f32_16x16x32_bf16 v[184:187],v[112:115],a[140:143],v[184:187]
ds_read_b64_tr_b16 v[112:113],v2 offset:32
v_mfma_f32_16x16x32_bf16 v[192:195],v[64:67],a[128:131],v[192:195]
ds_read_b64_tr_b16 v[114:115],v2 offset:5152
ds_read_b64_tr_b16 v[118:119],v2 offset:15392
v_mfma_f32_16x16x32_bf16 v[192:195],v[68:71],a[144:147],v[192:195]
buffer_load_dwordx4 v27,s[48:51],s84 offen offset:3072 lds
v_mfma_f32_16x16x32_bf16 v[192:195],v[72:75],a[160:163],v[192:195]
s_add_u32 m0,s86,91136
ds_read_b64_tr_b16 v[122:123],v2 offset:25632
v_mfma_f32_16x16x32_bf16 v[192:195],v[76:79],a[176:179],v[192:195]
v_mfma_f32_16x16x32_bf16 v[200:203],v[76:79],a[180:183],v[200:203]
v_mfma_f32_16x16x32_bf16 v[200:203],v[72:75],a[164:167],v[200:203]
v_mfma_f32_16x16x32_bf16 v[200:203],v[68:71],a[148:151],v[200:203]
v_mfma_f32_16x16x32_bf16 v[200:203],v[64:67],a[132:135],v[200:203]
v_mfma_f32_16x16x32_bf16 v[208:211],v[64:67],a[136:139],v[208:211]
buffer_load_dwordx4 v7,s[48:51],s84 offen lds
v_mfma_f32_16x16x32_bf16 v[208:211],v[68:71],a[152:155],v[208:211]
s_add_u32 m0,s86,40960
s_add_u32 s84,s83,20480
v_mfma_f32_16x16x32_bf16 v[208:211],v[72:75],a[168:171],v[208:211]
v_mfma_f32_16x16x32_bf16 v[208:211],v[76:79],a[184:187],v[208:211]
v_mfma_f32_16x16x32_bf16 v[216:219],v[76:79],a[188:191],v[216:219]
ds_read_b64_tr_b16 v[76:77],v1 offset:30784
ds_read_b64_tr_b16 v[78:79],v1 offset:35904
v_mfma_f32_16x16x32_bf16 v[216:219],v[72:75],a[172:175],v[216:219]
buffer_load_dwordx2 v[4:5],v3,s[44:47],s91 offen offset:1280 sc1
v_mfma_f32_16x16x32_bf16 v[216:219],v[68:71],a[156:159],v[216:219]
ds_read_b64_tr_b16 v[68:69],v1 offset:10304
ds_read_b64_tr_b16 v[70:71],v1 offset:15424
v_mfma_f32_16x16x32_bf16 v[216:219],v[64:67],a[140:143],v[216:219]
ds_read_b64_tr_b16 v[64:65],v1 offset:64
v_mfma_f32_16x16x32_bf16 v[224:227],v[80:83],a[128:131],v[224:227]
ds_read_b64_tr_b16 v[66:67],v1 offset:5184
v_mfma_f32_16x16x32_bf16 v[224:227],v[84:87],a[144:147],v[224:227]
ds_read_b64_tr_b16 v[72:73],v1 offset:20544
v_mfma_f32_16x16x32_bf16 v[224:227],v[88:91],a[160:163],v[224:227]
buffer_load_dwordx4 v[32:35],v6,s[28:31],s87 offen sc1
v_mfma_f32_16x16x32_bf16 v[224:227],v[92:95],a[176:179],v[224:227]
ds_read_b64_tr_b16 v[74:75],v1 offset:25664
v_mfma_f32_16x16x32_bf16 v[232:235],v[92:95],a[180:183],v[232:235]
v_mfma_f32_16x16x32_bf16 v[232:235],v[88:91],a[164:167],v[232:235]
v_mfma_f32_16x16x32_bf16 v[232:235],v[84:87],a[148:151],v[232:235]
v_mfma_f32_16x16x32_bf16 v[232:235],v[80:83],a[132:135],v[232:235]
buffer_load_dwordx4 v[36:39],v6,s[28:31],s88 offen sc1
v_mfma_f32_16x16x32_bf16 v[240:243],v[80:83],a[136:139],v[240:243]
v_mfma_f32_16x16x32_bf16 v[240:243],v[84:87],a[152:155],v[240:243]
v_mfma_f32_16x16x32_bf16 v[240:243],v[88:91],a[168:171],v[240:243]
v_mfma_f32_16x16x32_bf16 v[240:243],v[92:95],a[184:187],v[240:243]
v_mfma_f32_16x16x32_bf16 v[248:251],v[92:95],a[188:191],v[248:251]
buffer_load_dwordx4 v[40:43],v6,s[28:31],s89 offen sc1
v_mfma_f32_16x16x32_bf16 v[248:251],v[88:91],a[172:175],v[248:251]
ds_read_b64_tr_b16 v[88:89],v2 offset:20576
ds_read_b64_tr_b16 v[90:91],v2 offset:25696
v_mfma_f32_16x16x32_bf16 v[248:251],v[84:87],a[156:159],v[248:251]
ds_read_b64_tr_b16 v[84:85],v2 offset:10336
v_mfma_f32_16x16x32_bf16 v[248:251],v[80:83],a[140:143],v[248:251]
s_waitcnt lgkmcnt(3)
ds_read_b64_tr_b16 v[80:81],v2 offset:96
v_mfma_f32_16x16x32_bf16 a[192:195],v[96:99],a[68:71],a[192:195]
ds_read_b64_tr_b16 v[82:83],v2 offset:5216
s_nop 0
L_pd15_%=:
s_cbranch_execz L_trt1_%=
v_mfma_f32_16x16x32_bf16 a[192:195],v[100:103],a[76:79],a[192:195]
buffer_load_dwordx4 v[44:47],v6,s[28:31],s90 offen sc1
v_mfma_f32_16x16x32_bf16 a[192:195],v[104:107],a[84:87],a[192:195]
ds_read_b64_tr_b16 v[86:87],v2 offset:15456
ds_read_b64_tr_b16 v[92:93],v2 offset:30816
v_mfma_f32_16x16x32_bf16 a[192:195],v[108:111],a[92:95],a[192:195]
ds_read_b64_tr_b16 v[94:95],v2 offset:35936
v_mfma_f32_16x16x32_bf16 a[196:199],v[108:111],a[124:127],a[196:199]
v_mfma_f32_16x16x32_bf16 a[196:199],v[104:107],a[116:119],a[196:199]
v_mfma_f32_16x16x32_bf16 a[196:199],v[100:103],a[108:111],a[196:199]
buffer_load_dwordx4 v[180:183],v6,s[28:31],s90 offen offset:1024 sc1
v_mfma_f32_16x16x32_bf16 a[196:199],v[96:99],a[100:103],a[196:199]
v_mfma_f32_16x16x32_bf16 a[200:203],v[96:99],a[64:67],a[200:203]
v_mfma_f32_16x16x32_bf16 a[200:203],v[100:103],a[72:75],a[200:203]
s_waitcnt vmcnt(31)
v_mfma_f32_16x16x32_bf16 a[200:203],v[104:107],a[80:83],a[200:203]
s_waitcnt vmcnt(27)
v_mfma_f32_16x16x32_bf16 a[200:203],v[108:111],a[88:91],a[200:203]
buffer_load_dwordx4 v[212:215],v6,s[28:31],s88 offen offset:2048 sc1
v_mfma_f32_16x16x32_bf16 a[204:207],v[108:111],a[120:123],a[204:207]
s_waitcnt vmcnt(26)
ds_read_b64_tr_b16 v[108:109],v1 offset:30848
ds_read_b64_tr_b16 v[110:111],v1 offset:35968
v_mfma_f32_16x16x32_bf16 a[204:207],v[104:107],a[112:115],a[204:207]
s_waitcnt vmcnt(24)
ds_read_b64_tr_b16 v[104:105],v1 offset:20608
v_mfma_f32_16x16x32_bf16 a[204:207],v[100:103],a[104:107],a[204:207]
s_waitcnt vmcnt(21)
ds_read_b64_tr_b16 v[100:101],v1 offset:10368
v_mfma_f32_16x16x32_bf16 a[204:207],v[96:99],a[96:99],a[204:207]
ds_read_b64_tr_b16 v[96:97],v1 offset:128
v_mfma_f32_16x16x32_bf16 a[208:211],v[112:115],a[68:71],a[208:211]
buffer_load_dwordx4 v[164:167],v6,s[28:31],s89 offen offset:1024 sc1
v_mfma_f32_16x16x32_bf16 a[208:211],v[116:119],a[76:79],a[208:211]
ds_read_b64_tr_b16 v[98:99],v1 offset:5248
ds_read_b64_tr_b16 v[102:103],v1 offset:15488
v_mfma_f32_16x16x32_bf16 a[208:211],v[120:123],a[84:87],a[208:211]
ds_read_b64_tr_b16 v[106:107],v1 offset:25728
v_mfma_f32_16x16x32_bf16 a[208:211],v[124:127],a[92:95],a[208:211]
v_mfma_f32_16x16x32_bf16 a[212:215],v[124:127],a[124:127],a[212:215]
v_mfma_f32_16x16x32_bf16 a[212:215],v[120:123],a[116:119],a[212:215]
buffer_load_dwordx4 v[228:231],v6,s[28:31],s89 offen offset:2048 sc1
v_mfma_f32_16x16x32_bf16 a[212:215],v[116:119],a[108:111],a[212:215]
v_mfma_f32_16x16x32_bf16 a[212:215],v[112:115],a[100:103],a[212:215]
v_mfma_f32_16x16x32_bf16 a[216:219],v[112:115],a[64:67],a[216:219]
v_mfma_f32_16x16x32_bf16 a[216:219],v[116:119],a[72:75],a[216:219]
v_mfma_f32_16x16x32_bf16 a[216:219],v[120:123],a[80:83],a[216:219]
buffer_load_dwordx4 v[148:151],v6,s[28:31],s88 offen offset:1024 sc1
v_mfma_f32_16x16x32_bf16 a[216:219],v[124:127],a[88:91],a[216:219]
v_mfma_f32_16x16x32_bf16 a[220:223],v[124:127],a[120:123],a[220:223]
ds_read_b64_tr_b16 v[124:125],v2 offset:30880
ds_read_b64_tr_b16 v[126:127],v2 offset:36000
v_mfma_f32_16x16x32_bf16 a[220:223],v[120:123],a[112:115],a[220:223]
ds_read_b64_tr_b16 v[120:121],v2 offset:20640
v_mfma_f32_16x16x32_bf16 a[220:223],v[116:119],a[104:107],a[220:223]
ds_read_b64_tr_b16 v[116:117],v2 offset:10400
v_mfma_f32_16x16x32_bf16 a[220:223],v[112:115],a[96:99],a[220:223]
buffer_load_dwordx4 v[132:135],v6,s[28:31],s87 offen offset:1024 sc1
v_mfma_f32_16x16x32_bf16 a[224:227],v[64:67],a[68:71],a[224:227]
ds_read_b64_tr_b16 v[112:113],v2 offset:160
ds_read_b64_tr_b16 v[114:115],v2 offset:5280
v_mfma_f32_16x16x32_bf16 a[224:227],v[68:71],a[76:79],a[224:227]
ds_read_b64_tr_b16 v[118:119],v2 offset:15520
v_mfma_f32_16x16x32_bf16 a[224:227],v[72:75],a[84:87],a[224:227]
ds_read_b64_tr_b16 v[122:123],v2 offset:25760
v_mfma_f32_16x16x32_bf16 a[224:227],v[76:79],a[92:95],a[224:227]
v_mfma_f32_16x16x32_bf16 a[228:231],v[76:79],a[124:127],a[228:231]
buffer_load_dwordx4 v[244:247],v6,s[28:31],s90 offen offset:2048 sc1
)A"
R"A(v_mfma_f32_16x16x32_bf16 a[228:231],v[72:75],a[116:119],a[228:231]
v_mfma_f32_16x16x32_bf16 a[228:231],v[68:71],a[108:111],a[228:231]
v_mfma_f32_16x16x32_bf16 a[228:231],v[64:67],a[100:103],a[228:231]
v_mfma_f32_16x16x32_bf16 a[232:235],v[64:67],a[64:67],a[232:235]
v_mfma_f32_16x16x32_bf16 a[232:235],v[68:71],a[72:75],a[232:235]
buffer_load_dwordx4 v[188:191],v28,s[28:31],s90 offen offset:3072 sc1
v_mfma_f32_16x16x32_bf16 a[232:235],v[72:75],a[80:83],a[232:235]
s_waitcnt vmcnt(12)
v_cmp_ne_u32 s[56:57],s52,v4
v_cmp_ne_u32 s[58:59],s53,v5
v_mfma_f32_16x16x32_bf16 a[232:235],v[76:79],a[88:91],a[232:235]
s_or_b64 s[56:57],s[56:57],s[58:59]
s_cbranch_scc1 L_fr2_%=
L_fo2_%=:
v_mov_b32 v3,0
v_mfma_f32_16x16x32_bf16 a[236:239],v[76:79],a[120:123],a[236:239]
s_add_u32 s91,s64,3
ds_read_b64_tr_b16 v[76:77],v1 offset:30912
ds_read_b64_tr_b16 v[78:79],v1 offset:36032
v_mfma_f32_16x16x32_bf16 a[236:239],v[72:75],a[112:115],a[236:239]
s_and_b32 s91,s91,3
s_add_u32 s91,s91,s69
ds_read_b64_tr_b16 v[72:73],v1 offset:20672
v_mfma_f32_16x16x32_bf16 a[236:239],v[68:71],a[104:107],a[236:239]
buffer_load_dwordx4 a[128:131],v6,s[28:31],s20 offen sc1
v_mfma_f32_16x16x32_bf16 a[236:239],v[64:67],a[96:99],a[236:239]
s_add_u32 s91,s91,8
ds_read_b64_tr_b16 v[64:65],v1 offset:192
ds_read_b64_tr_b16 v[66:67],v1 offset:5312
s_waitcnt lgkmcnt(5)
v_mfma_f32_16x16x32_bf16 a[240:243],v[80:83],a[68:71],a[240:243]
s_lshl_b32 s91,s91,6
s_and_b32 s69,s63,s60
ds_read_b64_tr_b16 v[68:69],v1 offset:10432
v_mfma_f32_16x16x32_bf16 a[240:243],v[84:87],a[76:79],a[240:243]
s_cmp_eq_u32 s69,3
s_cselect_b32 s69,0x40000000,0
ds_read_b64_tr_b16 v[70:71],v1 offset:15552
v_mfma_f32_16x16x32_bf16 a[240:243],v[88:91],a[84:87],a[240:243]
s_add_u32 s68,s68,s69
ds_read_b64_tr_b16 v[74:75],v1 offset:25792
v_mfma_f32_16x16x32_bf16 a[240:243],v[92:95],a[92:95],a[240:243]
buffer_load_dwordx4 v[196:199],v6,s[28:31],s87 offen offset:2048 sc1
v_mfma_f32_16x16x32_bf16 a[244:247],v[92:95],a[124:127],a[244:247]
v_mfma_f32_16x16x32_bf16 a[244:247],v[88:91],a[116:119],a[244:247]
v_mfma_f32_16x16x32_bf16 a[244:247],v[84:87],a[108:111],a[244:247]
v_mfma_f32_16x16x32_bf16 a[244:247],v[80:83],a[100:103],a[244:247]
v_mfma_f32_16x16x32_bf16 a[248:251],v[80:83],a[64:67],a[248:251]
buffer_load_dwordx4 v[172:175],v28,s[28:31],s89 offen offset:3072 sc1
v_mfma_f32_16x16x32_bf16 a[248:251],v[84:87],a[72:75],a[248:251]
v_mfma_f32_16x16x32_bf16 a[248:251],v[88:91],a[80:83],a[248:251]
v_mfma_f32_16x16x32_bf16 a[248:251],v[92:95],a[88:91],a[248:251]
v_mfma_f32_16x16x32_bf16 a[252:255],v[92:95],a[120:123],a[252:255]
ds_read_b64_tr_b16 v[92:93],v2 offset:30944
ds_read_b64_tr_b16 v[94:95],v2 offset:36064
v_mfma_f32_16x16x32_bf16 a[252:255],v[88:91],a[112:115],a[252:255]
buffer_load_dwordx4 a[144:147],v6,s[28:31],s21 offen sc1
v_mfma_f32_16x16x32_bf16 a[252:255],v[84:87],a[104:107],a[252:255]
ds_read_b64_tr_b16 v[84:85],v2 offset:10464
ds_read_b64_tr_b16 v[86:87],v2 offset:15584
v_mfma_f32_16x16x32_bf16 a[252:255],v[80:83],a[96:99],a[252:255]
ds_read_b64_tr_b16 v[80:81],v2 offset:224
v_mfma_f32_16x16x32_bf16 a[0:3],v[96:99],a[68:71],a[0:3]
ds_read_b64_tr_b16 v[82:83],v2 offset:5344
v_mfma_f32_16x16x32_bf16 a[0:3],v[100:103],a[76:79],a[0:3]
ds_read_b64_tr_b16 v[88:89],v2 offset:20704
v_mfma_f32_16x16x32_bf16 a[0:3],v[104:107],a[84:87],a[0:3]
buffer_load_dwordx4 v[156:159],v28,s[28:31],s88 offen offset:3072 sc1
v_mfma_f32_16x16x32_bf16 a[0:3],v[108:111],a[92:95],a[0:3]
ds_read_b64_tr_b16 v[90:91],v2 offset:25824
v_mfma_f32_16x16x32_bf16 a[4:7],v[108:111],a[124:127],a[4:7]
v_mfma_f32_16x16x32_bf16 a[4:7],v[104:107],a[116:119],a[4:7]
v_mfma_f32_16x16x32_bf16 a[4:7],v[100:103],a[108:111],a[4:7]
v_mfma_f32_16x16x32_bf16 a[4:7],v[96:99],a[100:103],a[4:7]
buffer_load_dwordx4 a[160:163],v6,s[28:31],s22 offen sc1
v_mfma_f32_16x16x32_bf16 a[8:11],v[96:99],a[64:67],a[8:11]
v_mfma_f32_16x16x32_bf16 a[8:11],v[100:103],a[72:75],a[8:11]
v_mfma_f32_16x16x32_bf16 a[8:11],v[104:107],a[80:83],a[8:11]
v_mfma_f32_16x16x32_bf16 a[8:11],v[108:111],a[88:91],a[8:11]
v_mfma_f32_16x16x32_bf16 a[12:15],v[108:111],a[120:123],a[12:15]
buffer_load_dwordx4 v[140:143],v28,s[28:31],s87 offen offset:3072 sc1
v_mfma_f32_16x16x32_bf16 a[12:15],v[104:107],a[112:115],a[12:15]
s_add_u32 s87,s64,2
ds_read_b64_tr_b16 v[104:105],v1 offset:20736
ds_read_b64_tr_b16 v[106:107],v1 offset:25856
v_mfma_f32_16x16x32_bf16 a[12:15],v[100:103],a[104:107],a[12:15]
s_and_b32 s87,s87,3
s_lshl_b32 s87,s87,1
ds_read_b64_tr_b16 v[100:101],v1 offset:10496
v_mfma_f32_16x16x32_bf16 a[12:15],v[96:99],a[96:99],a[12:15]
s_or_b32 s87,s87,1
s_lshl_b32 s87,s87,16
ds_read_b64_tr_b16 v[96:97],v1 offset:256
v_mfma_f32_16x16x32_bf16 a[16:19],v[112:115],a[68:71],a[16:19]
s_add_u32 s88,s87,4096
s_add_u32 s89,s87,8192
ds_read_b64_tr_b16 v[98:99],v1 offset:5376
v_mfma_f32_16x16x32_bf16 a[16:19],v[116:119],a[76:79],a[16:19]
s_add_u32 s90,s87,12288
ds_read_b64_tr_b16 v[102:103],v1 offset:15616
ds_read_b64_tr_b16 v[108:109],v1 offset:30976
v_mfma_f32_16x16x32_bf16 a[16:19],v[120:123],a[84:87],a[16:19]
ds_read_b64_tr_b16 v[110:111],v1 offset:36096
v_add_u32 v1,0x1e000,v1
v_mfma_f32_16x16x32_bf16 a[16:19],v[124:127],a[92:95],a[16:19]
buffer_load_dwordx4 v24,s[48:51],s83 offen lds
v_mfma_f32_16x16x32_bf16 a[20:23],v[124:127],a[124:127],a[20:23]
v_mfma_f32_16x16x32_bf16 a[20:23],v[120:123],a[116:119],a[20:23]
v_mfma_f32_16x16x32_bf16 a[20:23],v[116:119],a[108:111],a[20:23]
v_mfma_f32_16x16x32_bf16 a[20:23],v[112:115],a[100:103],a[20:23]
v_mfma_f32_16x16x32_bf16 a[24:27],v[112:115],a[64:67],a[24:27]
buffer_load_dwordx4 a[176:179],v6,s[28:31],s23 offen sc1
v_mfma_f32_16x16x32_bf16 a[24:27],v[116:119],a[72:75],a[24:27]
v_mfma_f32_16x16x32_bf16 a[24:27],v[120:123],a[80:83],a[24:27]
v_mfma_f32_16x16x32_bf16 a[24:27],v[124:127],a[88:91],a[24:27]
v_mfma_f32_16x16x32_bf16 a[28:31],v[124:127],a[120:123],a[28:31]
ds_read_b64_tr_b16 v[124:125],v2 offset:31008
ds_read_b64_tr_b16 v[126:127],v2 offset:36128
v_mfma_f32_16x16x32_bf16 a[28:31],v[120:123],a[112:115],a[28:31]
ds_read_b64_tr_b16 v[120:121],v2 offset:20768
v_mfma_f32_16x16x32_bf16 a[28:31],v[116:119],a[104:107],a[28:31]
s_waitcnt lgkmcnt(3)
ds_read_b64_tr_b16 v[116:117],v2 offset:10528
v_mfma_f32_16x16x32_bf16 a[28:31],v[112:115],a[96:99],a[28:31]
buffer_load_dwordx4 v25,s[48:51],s83 offen offset:1024 lds
v_mfma_f32_16x16x32_bf16 v[128:131],v[64:67],a[68:71],v[128:131]
ds_read_b64_tr_b16 v[112:113],v2 offset:288
ds_read_b64_tr_b16 v[114:115],v2 offset:5408
v_mfma_f32_16x16x32_bf16 v[128:131],v[68:71],a[76:79],v[128:131]
ds_read_b64_tr_b16 v[118:119],v2 offset:15648
v_mfma_f32_16x16x32_bf16 v[128:131],v[72:75],a[84:87],v[128:131]
ds_read_b64_tr_b16 v[122:123],v2 offset:25888
v_add_u32 v2,0x1e000,v2
v_mfma_f32_16x16x32_bf16 v[128:131],v[76:79],a[92:95],v[128:131]
v_mfma_f32_16x16x32_bf16 v[136:139],v[76:79],a[124:127],v[136:139]
buffer_load_dwordx4 a[180:183],v6,s[28:31],s23 offen offset:1024 sc1
v_mfma_f32_16x16x32_bf16 v[136:139],v[72:75],a[116:119],v[136:139]
v_mfma_f32_16x16x32_bf16 v[136:139],v[68:71],a[108:111],v[136:139]
v_mfma_f32_16x16x32_bf16 v[136:139],v[64:67],a[100:103],v[136:139]
v_mfma_f32_16x16x32_bf16 v[144:147],v[64:67],a[64:67],v[144:147]
v_mfma_f32_16x16x32_bf16 v[144:147],v[68:71],a[72:75],v[144:147]
v_mfma_f32_16x16x32_bf16 v[144:147],v[72:75],a[80:83],v[144:147]
v_mfma_f32_16x16x32_bf16 v[144:147],v[76:79],a[88:91],v[144:147]
buffer_load_dwordx4 v26,s[48:51],s83 offen offset:2048 lds
v_mfma_f32_16x16x32_bf16 v[152:155],v[76:79],a[120:123],v[152:155]
v_mfma_f32_16x16x32_bf16 v[152:155],v[72:75],a[112:115],v[152:155]
v_mfma_f32_16x16x32_bf16 v[152:155],v[68:71],a[104:107],v[152:155]
v_mfma_f32_16x16x32_bf16 v[152:155],v[64:67],a[96:99],v[152:155]
s_waitcnt lgkmcnt(0)
s_barrier
v_mfma_f32_16x16x32_bf16 v[160:163],v[80:83],a[68:71],v[160:163]
buffer_load_dwordx4 a[164:167],v6,s[28:31],s22 offen offset:1024 sc1
v_mfma_f32_16x16x32_bf16 v[160:163],v[84:87],a[76:79],v[160:163]
ds_read_b64_tr_b16 v[64:65],v1
ds_read_b64_tr_b16 v[66:67],v1 offset:5120
v_mfma_f32_16x16x32_bf16 v[160:163],v[88:91],a[84:87],v[160:163]
ds_read_b64_tr_b16 v[68:69],v1 offset:10240
v_mfma_f32_16x16x32_bf16 v[160:163],v[92:95],a[92:95],v[160:163]
ds_read_b64_tr_b16 v[70:71],v1 offset:15360
v_mfma_f32_16x16x32_bf16 v[168:171],v[92:95],a[124:127],v[168:171]
ds_read_b64_tr_b16 v[72:73],v1 offset:20480
v_mfma_f32_16x16x32_bf16 v[168:171],v[88:91],a[116:119],v[168:171]
ds_read_b64_tr_b16 v[74:75],v1 offset:25600
ds_read_b64_tr_b16 v[76:77],v1 offset:30720
v_mfma_f32_16x16x32_bf16 v[168:171],v[84:87],a[108:111],v[168:171]
ds_read_b64_tr_b16 v[78:79],v1 offset:35840
v_mfma_f32_16x16x32_bf16 v[168:171],v[80:83],a[100:103],v[168:171]
buffer_load_dwordx4 v27,s[48:51],s83 offen offset:3072 lds
v_mfma_f32_16x16x32_bf16 v[176:179],v[80:83],a[64:67],v[176:179]
s_add_u32 m0,s86,45056
v_mfma_f32_16x16x32_bf16 v[176:179],v[84:87],a[72:75],v[176:179]
v_mfma_f32_16x16x32_bf16 v[176:179],v[88:91],a[80:83],v[176:179]
v_mfma_f32_16x16x32_bf16 v[176:179],v[92:95],a[88:91],v[176:179]
v_mfma_f32_16x16x32_bf16 v[184:187],v[92:95],a[120:123],v[184:187]
buffer_load_dwordx4 a[148:151],v6,s[28:31],s21 offen offset:1024 sc1
v_mfma_f32_16x16x32_bf16 v[184:187],v[88:91],a[112:115],v[184:187]
ds_read_b64_tr_b16 v[88:89],v2 offset:20512
ds_read_b64_tr_b16 v[90:91],v2 offset:25632
v_mfma_f32_16x16x32_bf16 v[184:187],v[84:87],a[104:107],v[184:187]
ds_read_b64_tr_b16 v[84:85],v2 offset:10272
v_mfma_f32_16x16x32_bf16 v[184:187],v[80:83],a[96:99],v[184:187]
ds_read_b64_tr_b16 v[80:81],v2 offset:32
v_mfma_f32_16x16x32_bf16 v[192:195],v[96:99],a[68:71],v[192:195]
ds_read_b64_tr_b16 v[82:83],v2 offset:5152
v_mfma_f32_16x16x32_bf16 v[192:195],v[100:103],a[76:79],v[192:195]
ds_read_b64_tr_b16 v[86:87],v2 offset:15392
ds_read_b64_tr_b16 v[92:93],v2 offset:30752
v_mfma_f32_16x16x32_bf16 v[192:195],v[104:107],a[84:87],v[192:195]
ds_read_b64_tr_b16 v[94:95],v2 offset:35872
v_mfma_f32_16x16x32_bf16 v[192:195],v[108:111],a[92:95],v[192:195]
buffer_load_dwordx4 v7,s[48:51],s83 offen lds
v_mfma_f32_16x16x32_bf16 v[200:203],v[108:111],a[124:127],v[200:203]
s_add_u32 m0,s86,46080
s_add_u32 s83,s64,3
s_and_b32 s83,s83,3
v_mfma_f32_16x16x32_bf16 v[200:203],v[104:107],a[116:119],v[200:203]
s_lshl_b32 s83,s83,1
s_or_b32 s83,s83,17
s_mul_i32 s83,s83,163840
v_mfma_f32_16x16x32_bf16 v[200:203],v[100:103],a[108:111],v[200:203]
s_add_u32 s83,s83,s85
v_mfma_f32_16x16x32_bf16 v[200:203],v[96:99],a[100:103],v[200:203]
v_mfma_f32_16x16x32_bf16 v[208:211],v[96:99],a[64:67],v[208:211]
buffer_load_dwordx4 a[132:135],v6,s[28:31],s20 offen offset:1024 sc1
v_mfma_f32_16x16x32_bf16 v[208:211],v[100:103],a[72:75],v[208:211]
v_mfma_f32_16x16x32_bf16 v[208:211],v[104:107],a[80:83],v[208:211]
v_mfma_f32_16x16x32_bf16 v[208:211],v[108:111],a[88:91],v[208:211]
v_mfma_f32_16x16x32_bf16 v[216:219],v[108:111],a[120:123],v[216:219]
ds_read_b64_tr_b16 v[108:109],v1 offset:30784
ds_read_b64_tr_b16 v[110:111],v1 offset:35904
v_mfma_f32_16x16x32_bf16 v[216:219],v[104:107],a[112:115],v[216:219]
ds_read_b64_tr_b16 v[104:105],v1 offset:20544
v_mfma_f32_16x16x32_bf16 v[216:219],v[100:103],a[104:107],v[216:219]
ds_read_b64_tr_b16 v[100:101],v1 offset:10304
v_mfma_f32_16x16x32_bf16 v[216:219],v[96:99],a[96:99],v[216:219]
buffer_load_dwordx4 v24,s[48:51],s84 offen lds
v_mfma_f32_16x16x32_bf16 v[224:227],v[112:115],a[68:71],v[224:227]
ds_read_b64_tr_b16 v[96:97],v1 offset:64
ds_read_b64_tr_b16 v[98:99],v1 offset:5184
v_mfma_f32_16x16x32_bf16 v[224:227],v[116:119],a[76:79],v[224:227]
ds_read_b64_tr_b16 v[102:103],v1 offset:15424
v_mfma_f32_16x16x32_bf16 v[224:227],v[120:123],a[84:87],v[224:227]
ds_read_b64_tr_b16 v[106:107],v1 offset:25664
v_mfma_f32_16x16x32_bf16 v[224:227],v[124:127],a[92:95],v[224:227]
v_mfma_f32_16x16x32_bf16 v[232:235],v[124:127],a[124:127],v[232:235]
buffer_load_dwordx4 a[136:139],v6,s[28:31],s20 offen offset:2048 sc1
v_mfma_f32_16x16x32_bf16 v[232:235],v[120:123],a[116:119],v[232:235]
v_mfma_f32_16x16x32_bf16 v[232:235],v[116:119],a[108:111],v[232:235]
v_mfma_f32_16x16x32_bf16 v[232:235],v[112:115],a[100:103],v[232:235]
)A"
R"A(v_mfma_f32_16x16x32_bf16 v[240:243],v[112:115],a[64:67],v[240:243]
v_mfma_f32_16x16x32_bf16 v[240:243],v[116:119],a[72:75],v[240:243]
v_mfma_f32_16x16x32_bf16 v[240:243],v[120:123],a[80:83],v[240:243]
v_mfma_f32_16x16x32_bf16 v[240:243],v[124:127],a[88:91],v[240:243]
buffer_load_dwordx4 v25,s[48:51],s84 offen offset:1024 lds
v_mfma_f32_16x16x32_bf16 v[248:251],v[124:127],a[120:123],v[248:251]
ds_read_b64_tr_b16 v[124:125],v2 offset:30816
ds_read_b64_tr_b16 v[126:127],v2 offset:35936
v_mfma_f32_16x16x32_bf16 v[248:251],v[120:123],a[112:115],v[248:251]
ds_read_b64_tr_b16 v[120:121],v2 offset:20576
v_mfma_f32_16x16x32_bf16 v[248:251],v[116:119],a[104:107],v[248:251]
ds_read_b64_tr_b16 v[116:117],v2 offset:10336
v_mfma_f32_16x16x32_bf16 v[248:251],v[112:115],a[96:99],v[248:251]
s_waitcnt vmcnt(19) lgkmcnt(4)
ds_read_b64_tr_b16 v[112:113],v2 offset:96
v_mfma_f32_16x16x32_bf16 a[192:195],v[64:67],v[32:35],a[192:195]
ds_read_b64_tr_b16 v[114:115],v2 offset:5216
ds_read_b64_tr_b16 v[118:119],v2 offset:15456
v_mfma_f32_16x16x32_bf16 a[192:195],v[68:71],v[36:39],a[192:195]
buffer_load_dwordx4 a[152:155],v6,s[28:31],s21 offen offset:2048 sc1
v_mfma_f32_16x16x32_bf16 a[192:195],v[72:75],v[40:43],a[192:195]
ds_read_b64_tr_b16 v[122:123],v2 offset:25696
v_mfma_f32_16x16x32_bf16 a[192:195],v[76:79],v[44:47],a[192:195]
v_mfma_f32_16x16x32_bf16 a[196:199],v[76:79],v[180:183],a[196:199]
v_mfma_f32_16x16x32_bf16 a[196:199],v[72:75],v[164:167],a[196:199]
v_mfma_f32_16x16x32_bf16 a[196:199],v[68:71],v[148:151],a[196:199]
v_mfma_f32_16x16x32_bf16 a[196:199],v[64:67],v[132:135],a[196:199]
s_waitcnt vmcnt(18)
v_mfma_f32_16x16x32_bf16 a[200:203],v[64:67],v[196:199],a[200:203]
buffer_load_dwordx4 v26,s[48:51],s84 offen offset:2048 lds
v_mfma_f32_16x16x32_bf16 a[200:203],v[68:71],v[212:215],a[200:203]
v_mfma_f32_16x16x32_bf16 a[200:203],v[72:75],v[228:231],a[200:203]
v_mfma_f32_16x16x32_bf16 a[200:203],v[76:79],v[244:247],a[200:203]
v_mfma_f32_16x16x32_bf16 a[204:207],v[76:79],v[188:191],a[204:207]
ds_read_b64_tr_b16 v[76:77],v1 offset:30848
ds_read_b64_tr_b16 v[78:79],v1 offset:35968
v_mfma_f32_16x16x32_bf16 a[204:207],v[72:75],v[172:175],a[204:207]
s_waitcnt vmcnt(15)
ds_read_b64_tr_b16 v[72:73],v1 offset:20608
v_mfma_f32_16x16x32_bf16 a[204:207],v[68:71],v[156:159],a[204:207]
buffer_load_dwordx4 a[168:171],v6,s[28:31],s22 offen offset:2048 sc1
v_mfma_f32_16x16x32_bf16 a[204:207],v[64:67],v[140:143],a[204:207]
ds_read_b64_tr_b16 v[64:65],v1 offset:128
ds_read_b64_tr_b16 v[66:67],v1 offset:5248
v_mfma_f32_16x16x32_bf16 a[208:211],v[80:83],v[32:35],a[208:211]
ds_read_b64_tr_b16 v[68:69],v1 offset:10368
v_mfma_f32_16x16x32_bf16 a[208:211],v[84:87],v[36:39],a[208:211]
ds_read_b64_tr_b16 v[70:71],v1 offset:15488
v_mfma_f32_16x16x32_bf16 a[208:211],v[88:91],v[40:43],a[208:211]
ds_read_b64_tr_b16 v[74:75],v1 offset:25728
v_mfma_f32_16x16x32_bf16 a[208:211],v[92:95],v[44:47],a[208:211]
v_mfma_f32_16x16x32_bf16 a[212:215],v[92:95],v[180:183],a[212:215]
v_mfma_f32_16x16x32_bf16 a[212:215],v[88:91],v[164:167],a[212:215]
buffer_load_dwordx4 v27,s[48:51],s84 offen offset:3072 lds
v_mfma_f32_16x16x32_bf16 a[212:215],v[84:87],v[148:151],a[212:215]
s_add_u32 m0,s86,50176
v_mfma_f32_16x16x32_bf16 a[212:215],v[80:83],v[132:135],a[212:215]
v_mfma_f32_16x16x32_bf16 a[216:219],v[80:83],v[196:199],a[216:219]
v_mfma_f32_16x16x32_bf16 a[216:219],v[84:87],v[212:215],a[216:219]
v_mfma_f32_16x16x32_bf16 a[216:219],v[88:91],v[228:231],a[216:219]
v_mfma_f32_16x16x32_bf16 a[216:219],v[92:95],v[244:247],a[216:219]
buffer_load_dwordx4 a[184:187],v6,s[28:31],s23 offen offset:2048 sc1
v_mfma_f32_16x16x32_bf16 a[220:223],v[92:95],v[188:191],a[220:223]
ds_read_b64_tr_b16 v[92:93],v2 offset:30880
ds_read_b64_tr_b16 v[94:95],v2 offset:36000
v_mfma_f32_16x16x32_bf16 a[220:223],v[88:91],v[172:175],a[220:223]
ds_read_b64_tr_b16 v[88:89],v2 offset:20640
v_mfma_f32_16x16x32_bf16 a[220:223],v[84:87],v[156:159],a[220:223]
ds_read_b64_tr_b16 v[84:85],v2 offset:10400
v_mfma_f32_16x16x32_bf16 a[220:223],v[80:83],v[140:143],a[220:223]
ds_read_b64_tr_b16 v[80:81],v2 offset:160
v_mfma_f32_16x16x32_bf16 a[224:227],v[96:99],v[32:35],a[224:227]
ds_read_b64_tr_b16 v[82:83],v2 offset:5280
ds_read_b64_tr_b16 v[86:87],v2 offset:15520
v_mfma_f32_16x16x32_bf16 a[224:227],v[100:103],v[36:39],a[224:227]
ds_read_b64_tr_b16 v[90:91],v2 offset:25760
v_mfma_f32_16x16x32_bf16 a[224:227],v[104:107],v[40:43],a[224:227]
buffer_load_dwordx4 v7,s[48:51],s84 offen lds
v_mfma_f32_16x16x32_bf16 a[224:227],v[108:111],v[44:47],a[224:227]
s_add_u32 m0,s86,0
s_add_u32 s84,s83,20480
v_mfma_f32_16x16x32_bf16 a[228:231],v[108:111],v[180:183],a[228:231]
v_mfma_f32_16x16x32_bf16 a[228:231],v[104:107],v[164:167],a[228:231]
v_mfma_f32_16x16x32_bf16 a[228:231],v[100:103],v[148:151],a[228:231]
v_mfma_f32_16x16x32_bf16 a[228:231],v[96:99],v[132:135],a[228:231]
buffer_load_dwordx4 a[68:71],v6,s[28:31],s87 offen sc1
v_mfma_f32_16x16x32_bf16 a[232:235],v[96:99],v[196:199],a[232:235]
v_mfma_f32_16x16x32_bf16 a[232:235],v[100:103],v[212:215],a[232:235]
v_mfma_f32_16x16x32_bf16 a[232:235],v[104:107],v[228:231],a[232:235]
v_mfma_f32_16x16x32_bf16 a[232:235],v[108:111],v[244:247],a[232:235]
v_mfma_f32_16x16x32_bf16 a[236:239],v[108:111],v[188:191],a[236:239]
buffer_load_dwordx4 a[188:191],v28,s[28:31],s23 offen offset:3072 sc1
v_mfma_f32_16x16x32_bf16 a[236:239],v[104:107],v[172:175],a[236:239]
ds_read_b64_tr_b16 v[104:105],v1 offset:20672
ds_read_b64_tr_b16 v[106:107],v1 offset:25792
v_mfma_f32_16x16x32_bf16 a[236:239],v[100:103],v[156:159],a[236:239]
ds_read_b64_tr_b16 v[100:101],v1 offset:10432
v_mfma_f32_16x16x32_bf16 a[236:239],v[96:99],v[140:143],a[236:239]
s_waitcnt lgkmcnt(3)
ds_read_b64_tr_b16 v[96:97],v1 offset:192
v_mfma_f32_16x16x32_bf16 a[240:243],v[112:115],v[32:35],a[240:243]
ds_read_b64_tr_b16 v[98:99],v1 offset:5312
v_mfma_f32_16x16x32_bf16 a[240:243],v[116:119],v[36:39],a[240:243]
buffer_load_dwordx4 a[100:103],v6,s[28:31],s87 offen offset:1024 sc1
v_mfma_f32_16x16x32_bf16 a[240:243],v[120:123],v[40:43],a[240:243]
ds_read_b64_tr_b16 v[102:103],v1 offset:15552
ds_read_b64_tr_b16 v[108:109],v1 offset:30912
v_mfma_f32_16x16x32_bf16 a[240:243],v[124:127],v[44:47],a[240:243]
ds_read_b64_tr_b16 v[110:111],v1 offset:36032
v_mfma_f32_16x16x32_bf16 a[244:247],v[124:127],v[180:183],a[244:247]
v_mfma_f32_16x16x32_bf16 a[244:247],v[120:123],v[164:167],a[244:247]
v_mfma_f32_16x16x32_bf16 a[244:247],v[116:119],v[148:151],a[244:247]
buffer_load_dwordx4 a[172:175],v28,s[28:31],s22 offen offset:3072 sc1
v_mfma_f32_16x16x32_bf16 a[244:247],v[112:115],v[132:135],a[244:247]
v_mfma_f32_16x16x32_bf16 a[248:251],v[112:115],v[196:199],a[248:251]
v_mfma_f32_16x16x32_bf16 a[248:251],v[116:119],v[212:215],a[248:251]
v_mfma_f32_16x16x32_bf16 a[248:251],v[120:123],v[228:231],a[248:251]
v_mfma_f32_16x16x32_bf16 a[248:251],v[124:127],v[244:247],a[248:251]
buffer_load_dwordx4 a[76:79],v6,s[28:31],s88 offen sc1
v_mfma_f32_16x16x32_bf16 a[252:255],v[124:127],v[188:191],a[252:255]
ds_read_b64_tr_b16 v[124:125],v2 offset:30944
ds_read_b64_tr_b16 v[126:127],v2 offset:36064
v_mfma_f32_16x16x32_bf16 a[252:255],v[120:123],v[172:175],a[252:255]
ds_read_b64_tr_b16 v[120:121],v2 offset:20704
v_mfma_f32_16x16x32_bf16 a[252:255],v[116:119],v[156:159],a[252:255]
ds_read_b64_tr_b16 v[116:117],v2 offset:10464
v_mfma_f32_16x16x32_bf16 a[252:255],v[112:115],v[140:143],a[252:255]
ds_read_b64_tr_b16 v[112:113],v2 offset:224
v_mfma_f32_16x16x32_bf16 a[0:3],v[64:67],v[32:35],a[0:3]
buffer_load_dwordx4 a[64:67],v6,s[28:31],s87 offen offset:2048 sc1
v_mfma_f32_16x16x32_bf16 a[0:3],v[68:71],v[36:39],a[0:3]
ds_read_b64_tr_b16 v[114:115],v2 offset:5344
ds_read_b64_tr_b16 v[118:119],v2 offset:15584
v_mfma_f32_16x16x32_bf16 a[0:3],v[72:75],v[40:43],a[0:3]
ds_read_b64_tr_b16 v[122:123],v2 offset:25824
v_mfma_f32_16x16x32_bf16 a[0:3],v[76:79],v[44:47],a[0:3]
v_mfma_f32_16x16x32_bf16 a[4:7],v[76:79],v[180:183],a[4:7]
v_mfma_f32_16x16x32_bf16 a[4:7],v[72:75],v[164:167],a[4:7]
buffer_load_dwordx4 a[156:159],v28,s[28:31],s21 offen offset:3072 sc1
v_mfma_f32_16x16x32_bf16 a[4:7],v[68:71],v[148:151],a[4:7]
v_mfma_f32_16x16x32_bf16 a[4:7],v[64:67],v[132:135],a[4:7]
v_mfma_f32_16x16x32_bf16 a[8:11],v[64:67],v[196:199],a[8:11]
v_mfma_f32_16x16x32_bf16 a[8:11],v[68:71],v[212:215],a[8:11]
v_mfma_f32_16x16x32_bf16 a[8:11],v[72:75],v[228:231],a[8:11]
buffer_load_dwordx4 a[84:87],v6,s[28:31],s89 offen sc1
v_mfma_f32_16x16x32_bf16 a[8:11],v[76:79],v[244:247],a[8:11]
v_mfma_f32_16x16x32_bf16 a[12:15],v[76:79],v[188:191],a[12:15]
ds_read_b64_tr_b16 v[76:77],v1 offset:30976
ds_read_b64_tr_b16 v[78:79],v1 offset:36096
v_mfma_f32_16x16x32_bf16 a[12:15],v[72:75],v[172:175],a[12:15]
ds_read_b64_tr_b16 v[72:73],v1 offset:20736
v_mfma_f32_16x16x32_bf16 a[12:15],v[68:71],v[156:159],a[12:15]
ds_read_b64_tr_b16 v[68:69],v1 offset:10496
v_mfma_f32_16x16x32_bf16 a[12:15],v[64:67],v[140:143],a[12:15]
buffer_load_dwordx4 a[140:143],v28,s[28:31],s20 offen offset:3072 sc1
v_mfma_f32_16x16x32_bf16 a[16:19],v[80:83],v[32:35],a[16:19]
s_add_u32 s20,s64,3
ds_read_b64_tr_b16 v[64:65],v1 offset:256
ds_read_b64_tr_b16 v[66:67],v1 offset:5376
v_mfma_f32_16x16x32_bf16 a[16:19],v[84:87],v[36:39],a[16:19]
s_and_b32 s20,s20,3
s_lshl_b32 s20,s20,1
ds_read_b64_tr_b16 v[70:71],v1 offset:15616
v_mfma_f32_16x16x32_bf16 a[16:19],v[88:91],v[40:43],a[16:19]
s_or_b32 s20,s20,17
ds_read_b64_tr_b16 v[74:75],v1 offset:25856
v_add_u32 v1,0xffff6000,v1
v_mfma_f32_16x16x32_bf16 a[16:19],v[92:95],v[44:47],a[16:19]
s_lshl_b32 s20,s20,16
s_add_u32 s21,s20,4096
s_add_u32 s22,s20,8192
v_mfma_f32_16x16x32_bf16 a[20:23],v[92:95],v[180:183],a[20:23]
buffer_load_dwordx4 a[72:75],v6,s[28:31],s88 offen offset:2048 sc1
v_mfma_f32_16x16x32_bf16 a[20:23],v[88:91],v[164:167],a[20:23]
s_add_u32 s23,s20,12288
v_mfma_f32_16x16x32_bf16 a[20:23],v[84:87],v[148:151],a[20:23]
v_mfma_f32_16x16x32_bf16 a[20:23],v[80:83],v[132:135],a[20:23]
v_mfma_f32_16x16x32_bf16 a[24:27],v[80:83],v[196:199],a[24:27]
v_mfma_f32_16x16x32_bf16 a[24:27],v[84:87],v[212:215],a[24:27]
buffer_load_dwordx4 a[92:95],v6,s[28:31],s90 offen sc1
v_mfma_f32_16x16x32_bf16 a[24:27],v[88:91],v[228:231],a[24:27]
v_mfma_f32_16x16x32_bf16 a[24:27],v[92:95],v[244:247],a[24:27]
v_mfma_f32_16x16x32_bf16 a[28:31],v[92:95],v[188:191],a[28:31]
ds_read_b64_tr_b16 v[92:93],v2 offset:31008
ds_read_b64_tr_b16 v[94:95],v2 offset:36128
v_mfma_f32_16x16x32_bf16 a[28:31],v[88:91],v[172:175],a[28:31]
ds_read_b64_tr_b16 v[88:89],v2 offset:20768
v_mfma_f32_16x16x32_bf16 a[28:31],v[84:87],v[156:159],a[28:31]
buffer_load_dwordx4 a[80:83],v6,s[28:31],s89 offen offset:2048 sc1
v_mfma_f32_16x16x32_bf16 a[28:31],v[80:83],v[140:143],a[28:31]
s_waitcnt lgkmcnt(3)
ds_read_b64_tr_b16 v[80:81],v2 offset:288
ds_read_b64_tr_b16 v[82:83],v2 offset:5408
v_mfma_f32_16x16x32_bf16 v[128:131],v[96:99],v[32:35],v[128:131]
ds_read_b64_tr_b16 v[84:85],v2 offset:10528
v_mfma_f32_16x16x32_bf16 v[128:131],v[100:103],v[36:39],v[128:131]
ds_read_b64_tr_b16 v[86:87],v2 offset:15648
v_mfma_f32_16x16x32_bf16 v[128:131],v[104:107],v[40:43],v[128:131]
ds_read_b64_tr_b16 v[90:91],v2 offset:25888
v_add_u32 v2,0xffff6000,v2
v_mfma_f32_16x16x32_bf16 v[128:131],v[108:111],v[44:47],v[128:131]
v_mfma_f32_16x16x32_bf16 v[136:139],v[108:111],v[180:183],v[136:139]
v_mfma_f32_16x16x32_bf16 v[136:139],v[104:107],v[164:167],v[136:139]
buffer_load_dwordx4 v24,s[48:51],s83 offen lds
v_mfma_f32_16x16x32_bf16 v[136:139],v[100:103],v[148:151],v[136:139]
v_mfma_f32_16x16x32_bf16 v[136:139],v[96:99],v[132:135],v[136:139]
v_mfma_f32_16x16x32_bf16 v[144:147],v[96:99],v[196:199],v[144:147]
v_mfma_f32_16x16x32_bf16 v[144:147],v[100:103],v[212:215],v[144:147]
v_mfma_f32_16x16x32_bf16 v[144:147],v[104:107],v[228:231],v[144:147]
v_mfma_f32_16x16x32_bf16 v[144:147],v[108:111],v[244:247],v[144:147]
v_mfma_f32_16x16x32_bf16 v[152:155],v[108:111],v[188:191],v[152:155]
buffer_load_dwordx4 v25,s[48:51],s83 offen offset:1024 lds
v_mfma_f32_16x16x32_bf16 v[152:155],v[104:107],v[172:175],v[152:155]
ds_read_b64_tr_b16 v[104:105],v1 offset:20480
)A"
R"A(ds_read_b64_tr_b16 v[106:107],v1 offset:25600
v_mfma_f32_16x16x32_bf16 v[152:155],v[100:103],v[156:159],v[152:155]
ds_read_b64_tr_b16 v[100:101],v1 offset:10240
v_mfma_f32_16x16x32_bf16 v[152:155],v[96:99],v[140:143],v[152:155]
ds_read_b64_tr_b16 v[96:97],v1
s_waitcnt vmcnt(14) lgkmcnt(4)
s_barrier
v_mfma_f32_16x16x32_bf16 v[160:163],v[112:115],v[32:35],v[160:163]
ds_read_b64_tr_b16 v[98:99],v1 offset:5120
v_mfma_f32_16x16x32_bf16 v[160:163],v[116:119],v[36:39],v[160:163]
ds_read_b64_tr_b16 v[102:103],v1 offset:15360
ds_read_b64_tr_b16 v[108:109],v1 offset:30720
v_mfma_f32_16x16x32_bf16 v[160:163],v[120:123],v[40:43],v[160:163]
buffer_load_dwordx4 v26,s[48:51],s83 offen offset:2048 lds
v_mfma_f32_16x16x32_bf16 v[160:163],v[124:127],v[44:47],v[160:163]
ds_read_b64_tr_b16 v[110:111],v1 offset:35840
v_mfma_f32_16x16x32_bf16 v[168:171],v[124:127],v[180:183],v[168:171]
v_mfma_f32_16x16x32_bf16 v[168:171],v[120:123],v[164:167],v[168:171]
v_mfma_f32_16x16x32_bf16 v[168:171],v[116:119],v[148:151],v[168:171]
v_mfma_f32_16x16x32_bf16 v[168:171],v[112:115],v[132:135],v[168:171]
v_mfma_f32_16x16x32_bf16 v[176:179],v[112:115],v[196:199],v[176:179]
v_mfma_f32_16x16x32_bf16 v[176:179],v[116:119],v[212:215],v[176:179]
buffer_load_dwordx4 v27,s[48:51],s83 offen offset:3072 lds
v_mfma_f32_16x16x32_bf16 v[176:179],v[120:123],v[228:231],v[176:179]
L_pd16_%=:
s_cbranch_execz L_trtg2F_%=
s_add_u32 m0,s86,4096
v_mfma_f32_16x16x32_bf16 v[176:179],v[124:127],v[244:247],v[176:179]
v_mfma_f32_16x16x32_bf16 v[184:187],v[124:127],v[188:191],v[184:187]
ds_read_b64_tr_b16 v[124:125],v2 offset:30752
ds_read_b64_tr_b16 v[126:127],v2 offset:35872
v_mfma_f32_16x16x32_bf16 v[184:187],v[120:123],v[172:175],v[184:187]
ds_read_b64_tr_b16 v[120:121],v2 offset:20512
v_mfma_f32_16x16x32_bf16 v[184:187],v[116:119],v[156:159],v[184:187]
buffer_load_dwordx4 a[124:127],v6,s[28:31],s90 offen offset:1024 sc1
v_mfma_f32_16x16x32_bf16 v[184:187],v[112:115],v[140:143],v[184:187]
ds_read_b64_tr_b16 v[112:113],v2 offset:32
ds_read_b64_tr_b16 v[114:115],v2 offset:5152
v_mfma_f32_16x16x32_bf16 v[192:195],v[64:67],v[32:35],v[192:195]
ds_read_b64_tr_b16 v[116:117],v2 offset:10272
v_mfma_f32_16x16x32_bf16 v[192:195],v[68:71],v[36:39],v[192:195]
ds_read_b64_tr_b16 v[118:119],v2 offset:15392
v_mfma_f32_16x16x32_bf16 v[192:195],v[72:75],v[40:43],v[192:195]
ds_read_b64_tr_b16 v[122:123],v2 offset:25632
v_mfma_f32_16x16x32_bf16 v[192:195],v[76:79],v[44:47],v[192:195]
buffer_load_dwordx4 a[88:91],v6,s[28:31],s90 offen offset:2048 sc1
v_mfma_f32_16x16x32_bf16 v[200:203],v[76:79],v[180:183],v[200:203]
v_mfma_f32_16x16x32_bf16 v[200:203],v[72:75],v[164:167],v[200:203]
v_mfma_f32_16x16x32_bf16 v[200:203],v[68:71],v[148:151],v[200:203]
v_mfma_f32_16x16x32_bf16 v[200:203],v[64:67],v[132:135],v[200:203]
v_mfma_f32_16x16x32_bf16 v[208:211],v[64:67],v[196:199],v[208:211]
v_mfma_f32_16x16x32_bf16 v[208:211],v[68:71],v[212:215],v[208:211]
v_mfma_f32_16x16x32_bf16 v[208:211],v[72:75],v[228:231],v[208:211]
buffer_load_dwordx4 v7,s[48:51],s83 offen lds
v_mfma_f32_16x16x32_bf16 v[208:211],v[76:79],v[244:247],v[208:211]
s_add_u32 m0,s86,5120
s_add_u32 s83,s64,3
s_and_b32 s83,s83,3
v_mfma_f32_16x16x32_bf16 v[216:219],v[76:79],v[188:191],v[216:219]
s_lshl_b32 s83,s83,1
ds_read_b64_tr_b16 v[76:77],v1 offset:30784
ds_read_b64_tr_b16 v[78:79],v1 offset:35904
v_mfma_f32_16x16x32_bf16 v[216:219],v[72:75],v[172:175],v[216:219]
buffer_load_dwordx2 v[4:5],v3,s[44:47],s91 offen offset:1280 sc1
v_mfma_f32_16x16x32_bf16 v[216:219],v[68:71],v[156:159],v[216:219]
s_or_b32 s83,s83,1
ds_read_b64_tr_b16 v[68:69],v1 offset:10304
ds_read_b64_tr_b16 v[70:71],v1 offset:15424
v_mfma_f32_16x16x32_bf16 v[216:219],v[64:67],v[140:143],v[216:219]
s_mul_i32 s83,s83,163840
s_add_u32 s83,s83,s85
ds_read_b64_tr_b16 v[64:65],v1 offset:64
v_mfma_f32_16x16x32_bf16 v[224:227],v[80:83],v[32:35],v[224:227]
ds_read_b64_tr_b16 v[66:67],v1 offset:5184
v_mfma_f32_16x16x32_bf16 v[224:227],v[84:87],v[36:39],v[224:227]
ds_read_b64_tr_b16 v[72:73],v1 offset:20544
v_mfma_f32_16x16x32_bf16 v[224:227],v[88:91],v[40:43],v[224:227]
ds_read_b64_tr_b16 v[74:75],v1 offset:25664
v_mfma_f32_16x16x32_bf16 v[224:227],v[92:95],v[44:47],v[224:227]
v_mfma_f32_16x16x32_bf16 v[232:235],v[92:95],v[180:183],v[232:235]
buffer_load_dwordx4 v24,s[48:51],s84 offen lds
v_mfma_f32_16x16x32_bf16 v[232:235],v[88:91],v[164:167],v[232:235]
v_mfma_f32_16x16x32_bf16 v[232:235],v[84:87],v[148:151],v[232:235]
v_mfma_f32_16x16x32_bf16 v[232:235],v[80:83],v[132:135],v[232:235]
v_mfma_f32_16x16x32_bf16 v[240:243],v[80:83],v[196:199],v[240:243]
v_mfma_f32_16x16x32_bf16 v[240:243],v[84:87],v[212:215],v[240:243]
buffer_load_dwordx4 a[116:119],v6,s[28:31],s89 offen offset:1024 sc1
v_mfma_f32_16x16x32_bf16 v[240:243],v[88:91],v[228:231],v[240:243]
v_mfma_f32_16x16x32_bf16 v[240:243],v[92:95],v[244:247],v[240:243]
v_mfma_f32_16x16x32_bf16 v[248:251],v[92:95],v[188:191],v[248:251]
ds_read_b64_tr_b16 v[92:93],v2 offset:30816
ds_read_b64_tr_b16 v[94:95],v2 offset:35936
v_mfma_f32_16x16x32_bf16 v[248:251],v[88:91],v[172:175],v[248:251]
ds_read_b64_tr_b16 v[88:89],v2 offset:20576
v_mfma_f32_16x16x32_bf16 v[248:251],v[84:87],v[156:159],v[248:251]
ds_read_b64_tr_b16 v[84:85],v2 offset:10336
v_mfma_f32_16x16x32_bf16 v[248:251],v[80:83],v[140:143],v[248:251]
s_waitcnt lgkmcnt(4)
ds_read_b64_tr_b16 v[80:81],v2 offset:96
v_mfma_f32_16x16x32_bf16 a[192:195],v[96:99],a[128:131],a[192:195]
buffer_load_dwordx4 v25,s[48:51],s84 offen offset:1024 lds
v_mfma_f32_16x16x32_bf16 a[192:195],v[100:103],a[144:147],a[192:195]
ds_read_b64_tr_b16 v[82:83],v2 offset:5216
ds_read_b64_tr_b16 v[86:87],v2 offset:15456
v_mfma_f32_16x16x32_bf16 a[192:195],v[104:107],a[160:163],a[192:195]
ds_read_b64_tr_b16 v[90:91],v2 offset:25696
v_mfma_f32_16x16x32_bf16 a[192:195],v[108:111],a[176:179],a[192:195]
v_mfma_f32_16x16x32_bf16 a[196:199],v[108:111],a[180:183],a[196:199]
v_mfma_f32_16x16x32_bf16 a[196:199],v[104:107],a[164:167],a[196:199]
v_mfma_f32_16x16x32_bf16 a[196:199],v[100:103],a[148:151],a[196:199]
v_mfma_f32_16x16x32_bf16 a[196:199],v[96:99],a[132:135],a[196:199]
buffer_load_dwordx4 v26,s[48:51],s84 offen offset:2048 lds
v_mfma_f32_16x16x32_bf16 a[200:203],v[96:99],a[136:139],a[200:203]
v_mfma_f32_16x16x32_bf16 a[200:203],v[100:103],a[152:155],a[200:203]
v_mfma_f32_16x16x32_bf16 a[200:203],v[104:107],a[168:171],a[200:203]
v_mfma_f32_16x16x32_bf16 a[200:203],v[108:111],a[184:187],a[200:203]
s_waitcnt vmcnt(19)
v_mfma_f32_16x16x32_bf16 a[204:207],v[108:111],a[188:191],a[204:207]
ds_read_b64_tr_b16 v[108:109],v1 offset:30848
ds_read_b64_tr_b16 v[110:111],v1 offset:35968
v_mfma_f32_16x16x32_bf16 a[204:207],v[104:107],a[172:175],a[204:207]
s_waitcnt vmcnt(15)
ds_read_b64_tr_b16 v[104:105],v1 offset:20608
v_mfma_f32_16x16x32_bf16 a[204:207],v[100:103],a[156:159],a[204:207]
buffer_load_dwordx4 v27,s[48:51],s84 offen offset:3072 lds
v_mfma_f32_16x16x32_bf16 a[204:207],v[96:99],a[140:143],a[204:207]
s_add_u32 m0,s86,9216
ds_read_b64_tr_b16 v[96:97],v1 offset:128
ds_read_b64_tr_b16 v[98:99],v1 offset:5248
v_mfma_f32_16x16x32_bf16 a[208:211],v[112:115],a[128:131],a[208:211]
ds_read_b64_tr_b16 v[100:101],v1 offset:10368
v_mfma_f32_16x16x32_bf16 a[208:211],v[116:119],a[144:147],a[208:211]
ds_read_b64_tr_b16 v[102:103],v1 offset:15488
v_mfma_f32_16x16x32_bf16 a[208:211],v[120:123],a[160:163],a[208:211]
ds_read_b64_tr_b16 v[106:107],v1 offset:25728
v_mfma_f32_16x16x32_bf16 a[208:211],v[124:127],a[176:179],a[208:211]
v_mfma_f32_16x16x32_bf16 a[212:215],v[124:127],a[180:183],a[212:215]
v_mfma_f32_16x16x32_bf16 a[212:215],v[120:123],a[164:167],a[212:215]
buffer_load_dwordx4 v7,s[48:51],s84 offen lds
v_mfma_f32_16x16x32_bf16 a[212:215],v[116:119],a[148:151],a[212:215]
s_add_u32 m0,s86,122880
s_add_u32 s84,s83,20480
v_mfma_f32_16x16x32_bf16 a[212:215],v[112:115],a[132:135],a[212:215]
v_mfma_f32_16x16x32_bf16 a[216:219],v[112:115],a[136:139],a[216:219]
v_mfma_f32_16x16x32_bf16 a[216:219],v[116:119],a[152:155],a[216:219]
v_mfma_f32_16x16x32_bf16 a[216:219],v[120:123],a[168:171],a[216:219]
buffer_load_dwordx4 a[120:123],v28,s[28:31],s90 offen offset:3072 sc1
v_mfma_f32_16x16x32_bf16 a[216:219],v[124:127],a[184:187],a[216:219]
v_mfma_f32_16x16x32_bf16 a[220:223],v[124:127],a[188:191],a[220:223]
ds_read_b64_tr_b16 v[124:125],v2 offset:30880
ds_read_b64_tr_b16 v[126:127],v2 offset:36000
v_mfma_f32_16x16x32_bf16 a[220:223],v[120:123],a[172:175],a[220:223]
ds_read_b64_tr_b16 v[120:121],v2 offset:20640
v_mfma_f32_16x16x32_bf16 a[220:223],v[116:119],a[156:159],a[220:223]
ds_read_b64_tr_b16 v[116:117],v2 offset:10400
v_mfma_f32_16x16x32_bf16 a[220:223],v[112:115],a[140:143],a[220:223]
buffer_load_dwordx4 a[108:111],v6,s[28:31],s88 offen offset:1024 sc1
v_mfma_f32_16x16x32_bf16 a[224:227],v[64:67],a[128:131],a[224:227]
ds_read_b64_tr_b16 v[112:113],v2 offset:160
ds_read_b64_tr_b16 v[114:115],v2 offset:5280
v_mfma_f32_16x16x32_bf16 a[224:227],v[68:71],a[144:147],a[224:227]
ds_read_b64_tr_b16 v[118:119],v2 offset:15520
v_mfma_f32_16x16x32_bf16 a[224:227],v[72:75],a[160:163],a[224:227]
ds_read_b64_tr_b16 v[122:123],v2 offset:25760
v_mfma_f32_16x16x32_bf16 a[224:227],v[76:79],a[176:179],a[224:227]
v_mfma_f32_16x16x32_bf16 a[228:231],v[76:79],a[180:183],a[228:231]
buffer_load_dwordx4 a[112:115],v28,s[28:31],s89 offen offset:3072 sc1
v_mfma_f32_16x16x32_bf16 a[228:231],v[72:75],a[164:167],a[228:231]
v_mfma_f32_16x16x32_bf16 a[228:231],v[68:71],a[148:151],a[228:231]
v_mfma_f32_16x16x32_bf16 a[228:231],v[64:67],a[132:135],a[228:231]
v_mfma_f32_16x16x32_bf16 a[232:235],v[64:67],a[136:139],a[232:235]
v_mfma_f32_16x16x32_bf16 a[232:235],v[68:71],a[152:155],a[232:235]
buffer_load_dwordx4 a[104:107],v28,s[28:31],s88 offen offset:3072 sc1
v_mfma_f32_16x16x32_bf16 a[232:235],v[72:75],a[168:171],a[232:235]
s_waitcnt vmcnt(10)
v_cmp_ne_u32 s[56:57],s52,v4
v_cmp_ne_u32 s[58:59],s53,v5
v_mfma_f32_16x16x32_bf16 a[232:235],v[76:79],a[184:187],a[232:235]
s_or_b64 s[56:57],s[56:57],s[58:59]
s_cbranch_scc1 L_fr3_%=
L_fo3_%=:
s_lshl_b32 s91,s64,6
v_mfma_f32_16x16x32_bf16 a[236:239],v[76:79],a[188:191],a[236:239]
ds_read_b64_tr_b16 v[76:77],v1 offset:30912
ds_read_b64_tr_b16 v[78:79],v1 offset:36032
v_mfma_f32_16x16x32_bf16 a[236:239],v[72:75],a[172:175],a[236:239]
ds_read_b64_tr_b16 v[72:73],v1 offset:20672
v_bfe_u32 v3,v6,4,2
v_mfma_f32_16x16x32_bf16 a[236:239],v[68:71],a[156:159],a[236:239]
buffer_load_dwordx4 v[32:35],v6,s[28:31],s20 offen sc1
v_mfma_f32_16x16x32_bf16 a[236:239],v[64:67],a[140:143],a[236:239]
s_waitcnt lgkmcnt(3)
ds_read_b64_tr_b16 v[64:65],v1 offset:192
ds_read_b64_tr_b16 v[66:67],v1 offset:5312
v_mfma_f32_16x16x32_bf16 a[240:243],v[80:83],a[128:131],a[240:243]
ds_read_b64_tr_b16 v[68:69],v1 offset:10432
v_min_u32 v3,2,v3
v_mfma_f32_16x16x32_bf16 a[240:243],v[84:87],a[144:147],a[240:243]
ds_read_b64_tr_b16 v[70:71],v1 offset:15552
v_add3_u32 v3,v3,s63,1
v_mfma_f32_16x16x32_bf16 a[240:243],v[88:91],a[160:163],a[240:243]
ds_read_b64_tr_b16 v[74:75],v1 offset:25792
v_and_b32 v3,3,v3
v_mfma_f32_16x16x32_bf16 a[240:243],v[92:95],a[176:179],a[240:243]
v_lshlrev_b32 v3,8,v3
v_mfma_f32_16x16x32_bf16 a[244:247],v[92:95],a[180:183],a[244:247]
v_mfma_f32_16x16x32_bf16 a[244:247],v[88:91],a[164:167],a[244:247]
buffer_load_dwordx4 v24,s[48:51],s83 offen lds
v_mfma_f32_16x16x32_bf16 a[244:247],v[84:87],a[148:151],a[244:247]
v_mfma_f32_16x16x32_bf16 a[244:247],v[80:83],a[132:135],a[244:247]
v_mfma_f32_16x16x32_bf16 a[248:251],v[80:83],a[136:139],a[248:251]
v_mfma_f32_16x16x32_bf16 a[248:251],v[84:87],a[152:155],a[248:251]
v_mfma_f32_16x16x32_bf16 a[248:251],v[88:91],a[168:171],a[248:251]
buffer_load_dwordx4 v[36:39],v6,s[28:31],s21 offen sc1
v_mfma_f32_16x16x32_bf16 a[248:251],v[92:95],a[184:187],a[248:251]
v_mfma_f32_16x16x32_bf16 a[252:255],v[92:95],a[188:191],a[252:255]
ds_read_b64_tr_b16 v[92:93],v2 offset:30944
ds_read_b64_tr_b16 v[94:95],v2 offset:36064
v_mfma_f32_16x16x32_bf16 a[252:255],v[88:91],a[172:175],a[252:255]
ds_read_b64_tr_b16 v[88:89],v2 offset:20704
v_mfma_f32_16x16x32_bf16 a[252:255],v[84:87],a[156:159],a[252:255]
)A"
R"A(ds_read_b64_tr_b16 v[84:85],v2 offset:10464
v_mfma_f32_16x16x32_bf16 a[252:255],v[80:83],a[140:143],a[252:255]
ds_read_b64_tr_b16 v[80:81],v2 offset:224
v_mfma_f32_16x16x32_bf16 a[0:3],v[96:99],a[128:131],a[0:3]
ds_read_b64_tr_b16 v[82:83],v2 offset:5344
ds_read_b64_tr_b16 v[86:87],v2 offset:15584
v_mfma_f32_16x16x32_bf16 a[0:3],v[100:103],a[144:147],a[0:3]
buffer_load_dwordx4 v25,s[48:51],s83 offen offset:1024 lds
v_mfma_f32_16x16x32_bf16 a[0:3],v[104:107],a[160:163],a[0:3]
ds_read_b64_tr_b16 v[90:91],v2 offset:25824
v_mfma_f32_16x16x32_bf16 a[0:3],v[108:111],a[176:179],a[0:3]
v_mfma_f32_16x16x32_bf16 a[4:7],v[108:111],a[180:183],a[4:7]
v_mfma_f32_16x16x32_bf16 a[4:7],v[104:107],a[164:167],a[4:7]
v_mfma_f32_16x16x32_bf16 a[4:7],v[100:103],a[148:151],a[4:7]
buffer_load_dwordx4 v[40:43],v6,s[28:31],s22 offen sc1
v_mfma_f32_16x16x32_bf16 a[4:7],v[96:99],a[132:135],a[4:7]
v_mfma_f32_16x16x32_bf16 a[8:11],v[96:99],a[136:139],a[8:11]
v_mfma_f32_16x16x32_bf16 a[8:11],v[100:103],a[152:155],a[8:11]
v_mfma_f32_16x16x32_bf16 a[8:11],v[104:107],a[168:171],a[8:11]
v_mfma_f32_16x16x32_bf16 a[8:11],v[108:111],a[184:187],a[8:11]
buffer_load_dwordx4 a[96:99],v28,s[28:31],s87 offen offset:3072 sc1
v_mfma_f32_16x16x32_bf16 a[12:15],v[108:111],a[188:191],a[12:15]
s_add_u32 s87,s64,3
ds_read_b64_tr_b16 v[108:109],v1 offset:30976
ds_read_b64_tr_b16 v[110:111],v1 offset:36096
v_mfma_f32_16x16x32_bf16 a[12:15],v[104:107],a[172:175],a[12:15]
s_and_b32 s87,s87,3
s_lshl_b32 s87,s87,1
ds_read_b64_tr_b16 v[104:105],v1 offset:20736
v_mfma_f32_16x16x32_bf16 a[12:15],v[100:103],a[156:159],a[12:15]
s_or_b32 s87,s87,1
s_lshl_b32 s87,s87,16
ds_read_b64_tr_b16 v[100:101],v1 offset:10496
v_mfma_f32_16x16x32_bf16 a[12:15],v[96:99],a[140:143],a[12:15]
s_add_u32 s88,s87,4096
s_add_u32 s89,s87,8192
ds_read_b64_tr_b16 v[96:97],v1 offset:256
v_mfma_f32_16x16x32_bf16 a[16:19],v[112:115],a[128:131],a[16:19]
buffer_load_dwordx4 v[44:47],v6,s[28:31],s23 offen sc1
v_mfma_f32_16x16x32_bf16 a[16:19],v[116:119],a[144:147],a[16:19]
s_add_u32 s90,s87,12288
ds_read_b64_tr_b16 v[98:99],v1 offset:5376
ds_read_b64_tr_b16 v[102:103],v1 offset:15616
v_mfma_f32_16x16x32_bf16 a[16:19],v[120:123],a[160:163],a[16:19]
ds_read_b64_tr_b16 v[106:107],v1 offset:25856
v_add_u32 v1,0xffff6000,v1
v_mfma_f32_16x16x32_bf16 a[16:19],v[124:127],a[176:179],a[16:19]
v_mfma_f32_16x16x32_bf16 a[20:23],v[124:127],a[180:183],a[20:23]
v_mfma_f32_16x16x32_bf16 a[20:23],v[120:123],a[164:167],a[20:23]
v_mfma_f32_16x16x32_bf16 a[20:23],v[116:119],a[148:151],a[20:23]
v_mfma_f32_16x16x32_bf16 a[20:23],v[112:115],a[132:135],a[20:23]
buffer_load_dwordx4 v26,s[48:51],s83 offen offset:2048 lds
v_mfma_f32_16x16x32_bf16 a[24:27],v[112:115],a[136:139],a[24:27]
v_mfma_f32_16x16x32_bf16 a[24:27],v[116:119],a[152:155],a[24:27]
v_mfma_f32_16x16x32_bf16 a[24:27],v[120:123],a[168:171],a[24:27]
v_mfma_f32_16x16x32_bf16 a[24:27],v[124:127],a[184:187],a[24:27]
v_mfma_f32_16x16x32_bf16 a[28:31],v[124:127],a[188:191],a[28:31]
buffer_load_dwordx4 v[180:183],v6,s[28:31],s23 offen offset:1024 sc1
v_mfma_f32_16x16x32_bf16 a[28:31],v[120:123],a[172:175],a[28:31]
ds_read_b64_tr_b16 v[120:121],v2 offset:20768
ds_read_b64_tr_b16 v[122:123],v2 offset:25888
v_mfma_f32_16x16x32_bf16 a[28:31],v[116:119],a[156:159],a[28:31]
ds_read_b64_tr_b16 v[116:117],v2 offset:10528
v_mfma_f32_16x16x32_bf16 a[28:31],v[112:115],a[140:143],a[28:31]
s_waitcnt lgkmcnt(3)
ds_read_b64_tr_b16 v[112:113],v2 offset:288
v_mfma_f32_16x16x32_bf16 v[128:131],v[64:67],a[128:131],v[128:131]
ds_read_b64_tr_b16 v[114:115],v2 offset:5408
v_mfma_f32_16x16x32_bf16 v[128:131],v[68:71],a[144:147],v[128:131]
ds_read_b64_tr_b16 v[118:119],v2 offset:15648
ds_read_b64_tr_b16 v[124:125],v2 offset:31008
v_mfma_f32_16x16x32_bf16 v[128:131],v[72:75],a[160:163],v[128:131]
ds_read_b64_tr_b16 v[126:127],v2 offset:36128
v_add_u32 v2,0xffff6000,v2
v_mfma_f32_16x16x32_bf16 v[128:131],v[76:79],a[176:179],v[128:131]
buffer_load_dwordx4 v[164:167],v6,s[28:31],s22 offen offset:1024 sc1
v_mfma_f32_16x16x32_bf16 v[136:139],v[76:79],a[180:183],v[136:139]
v_mfma_f32_16x16x32_bf16 v[136:139],v[72:75],a[164:167],v[136:139]
v_mfma_f32_16x16x32_bf16 v[136:139],v[68:71],a[148:151],v[136:139]
v_mfma_f32_16x16x32_bf16 v[136:139],v[64:67],a[132:135],v[136:139]
v_mfma_f32_16x16x32_bf16 v[144:147],v[64:67],a[136:139],v[144:147]
v_mfma_f32_16x16x32_bf16 v[144:147],v[68:71],a[152:155],v[144:147]
v_mfma_f32_16x16x32_bf16 v[144:147],v[72:75],a[168:171],v[144:147]
buffer_load_dwordx4 v27,s[48:51],s83 offen offset:3072 lds
v_mfma_f32_16x16x32_bf16 v[144:147],v[76:79],a[184:187],v[144:147]
s_add_u32 m0,s86,126976
v_mfma_f32_16x16x32_bf16 v[152:155],v[76:79],a[188:191],v[152:155]
ds_read_b64_tr_b16 v[76:77],v1 offset:30720
ds_read_b64_tr_b16 v[78:79],v1 offset:35840
v_mfma_f32_16x16x32_bf16 v[152:155],v[72:75],a[172:175],v[152:155]
ds_read_b64_tr_b16 v[72:73],v1 offset:20480
v_mfma_f32_16x16x32_bf16 v[152:155],v[68:71],a[156:159],v[152:155]
ds_read_b64_tr_b16 v[68:69],v1 offset:10240
v_mfma_f32_16x16x32_bf16 v[152:155],v[64:67],a[140:143],v[152:155]
buffer_load_dwordx4 v[148:151],v6,s[28:31],s21 offen offset:1024 sc1
v_mfma_f32_16x16x32_bf16 v[160:163],v[80:83],a[128:131],v[160:163]
ds_read_b64_tr_b16 v[64:65],v1
ds_read_b64_tr_b16 v[66:67],v1 offset:5120
v_mfma_f32_16x16x32_bf16 v[160:163],v[84:87],a[144:147],v[160:163]
ds_read_b64_tr_b16 v[70:71],v1 offset:15360
v_mfma_f32_16x16x32_bf16 v[160:163],v[88:91],a[160:163],v[160:163]
ds_read_b64_tr_b16 v[74:75],v1 offset:25600
v_mfma_f32_16x16x32_bf16 v[160:163],v[92:95],a[176:179],v[160:163]
v_mfma_f32_16x16x32_bf16 v[168:171],v[92:95],a[180:183],v[168:171]
v_mfma_f32_16x16x32_bf16 v[168:171],v[88:91],a[164:167],v[168:171]
v_mfma_f32_16x16x32_bf16 v[168:171],v[84:87],a[148:151],v[168:171]
buffer_load_dwordx4 v7,s[48:51],s83 offen lds
v_mfma_f32_16x16x32_bf16 v[168:171],v[80:83],a[132:135],v[168:171]
s_add_u32 m0,s86,128000
s_mul_i32 s83,s63,20480
v_mfma_f32_16x16x32_bf16 v[176:179],v[80:83],a[136:139],v[176:179]
v_mfma_f32_16x16x32_bf16 v[176:179],v[84:87],a[152:155],v[176:179]
v_mfma_f32_16x16x32_bf16 v[176:179],v[88:91],a[168:171],v[176:179]
v_mfma_f32_16x16x32_bf16 v[176:179],v[92:95],a[184:187],v[176:179]
buffer_load_dwordx4 v[132:135],v6,s[28:31],s20 offen offset:1024 sc1
v_mfma_f32_16x16x32_bf16 v[184:187],v[92:95],a[188:191],v[184:187]
ds_read_b64_tr_b16 v[92:93],v2 offset:30752
ds_read_b64_tr_b16 v[94:95],v2 offset:35872
v_mfma_f32_16x16x32_bf16 v[184:187],v[88:91],a[172:175],v[184:187]
ds_read_b64_tr_b16 v[88:89],v2 offset:20512
v_mfma_f32_16x16x32_bf16 v[184:187],v[84:87],a[156:159],v[184:187]
ds_read_b64_tr_b16 v[84:85],v2 offset:10272
v_mfma_f32_16x16x32_bf16 v[184:187],v[80:83],a[140:143],v[184:187]
ds_read_b64_tr_b16 v[80:81],v2 offset:32
v_mfma_f32_16x16x32_bf16 v[192:195],v[96:99],a[128:131],v[192:195]
ds_read_b64_tr_b16 v[82:83],v2 offset:5152
ds_read_b64_tr_b16 v[86:87],v2 offset:15392
v_mfma_f32_16x16x32_bf16 v[192:195],v[100:103],a[144:147],v[192:195]
ds_read_b64_tr_b16 v[90:91],v2 offset:25632
v_mfma_f32_16x16x32_bf16 v[192:195],v[104:107],a[160:163],v[192:195]
buffer_load_dwordx4 v24,s[48:51],s84 offen lds
v_mfma_f32_16x16x32_bf16 v[192:195],v[108:111],a[176:179],v[192:195]
v_mfma_f32_16x16x32_bf16 v[200:203],v[108:111],a[180:183],v[200:203]
v_mfma_f32_16x16x32_bf16 v[200:203],v[104:107],a[164:167],v[200:203]
v_mfma_f32_16x16x32_bf16 v[200:203],v[100:103],a[148:151],v[200:203]
v_mfma_f32_16x16x32_bf16 v[200:203],v[96:99],a[132:135],v[200:203]
buffer_load_dwordx4 v[196:199],v6,s[28:31],s20 offen offset:2048 sc1
v_mfma_f32_16x16x32_bf16 v[208:211],v[96:99],a[136:139],v[208:211]
v_mfma_f32_16x16x32_bf16 v[208:211],v[100:103],a[152:155],v[208:211]
v_mfma_f32_16x16x32_bf16 v[208:211],v[104:107],a[168:171],v[208:211]
v_mfma_f32_16x16x32_bf16 v[208:211],v[108:111],a[184:187],v[208:211]
v_mfma_f32_16x16x32_bf16 v[216:219],v[108:111],a[188:191],v[216:219]
ds_read_b64_tr_b16 v[108:109],v1 offset:30784
ds_read_b64_tr_b16 v[110:111],v1 offset:35904
v_mfma_f32_16x16x32_bf16 v[216:219],v[104:107],a[172:175],v[216:219]
ds_read_b64_tr_b16 v[104:105],v1 offset:20544
v_mfma_f32_16x16x32_bf16 v[216:219],v[100:103],a[156:159],v[216:219]
buffer_load_dwordx4 v25,s[48:51],s84 offen offset:1024 lds
v_mfma_f32_16x16x32_bf16 v[216:219],v[96:99],a[140:143],v[216:219]
s_waitcnt lgkmcnt(3)
ds_read_b64_tr_b16 v[96:97],v1 offset:64
ds_read_b64_tr_b16 v[98:99],v1 offset:5184
v_mfma_f32_16x16x32_bf16 v[224:227],v[112:115],a[128:131],v[224:227]
ds_read_b64_tr_b16 v[100:101],v1 offset:10304
v_mfma_f32_16x16x32_bf16 v[224:227],v[116:119],a[144:147],v[224:227]
ds_read_b64_tr_b16 v[102:103],v1 offset:15424
v_mfma_f32_16x16x32_bf16 v[224:227],v[120:123],a[160:163],v[224:227]
ds_read_b64_tr_b16 v[106:107],v1 offset:25664
v_mfma_f32_16x16x32_bf16 v[224:227],v[124:127],a[176:179],v[224:227]
buffer_load_dwordx4 v[212:215],v6,s[28:31],s21 offen offset:2048 sc1
v_mfma_f32_16x16x32_bf16 v[232:235],v[124:127],a[180:183],v[232:235]
v_mfma_f32_16x16x32_bf16 v[232:235],v[120:123],a[164:167],v[232:235]
v_mfma_f32_16x16x32_bf16 v[232:235],v[116:119],a[148:151],v[232:235]
v_mfma_f32_16x16x32_bf16 v[232:235],v[112:115],a[132:135],v[232:235]
v_mfma_f32_16x16x32_bf16 v[240:243],v[112:115],a[136:139],v[240:243]
v_mfma_f32_16x16x32_bf16 v[240:243],v[116:119],a[152:155],v[240:243]
v_mfma_f32_16x16x32_bf16 v[240:243],v[120:123],a[168:171],v[240:243]
buffer_load_dwordx4 v[228:231],v6,s[28:31],s22 offen offset:2048 sc1
v_mfma_f32_16x16x32_bf16 v[240:243],v[124:127],a[184:187],v[240:243]
v_mfma_f32_16x16x32_bf16 v[248:251],v[124:127],a[188:191],v[248:251]
ds_read_b64_tr_b16 v[124:125],v2 offset:30816
ds_read_b64_tr_b16 v[126:127],v2 offset:35936
v_mfma_f32_16x16x32_bf16 v[248:251],v[120:123],a[172:175],v[248:251]
ds_read_b64_tr_b16 v[120:121],v2 offset:20576
v_mfma_f32_16x16x32_bf16 v[248:251],v[116:119],a[156:159],v[248:251]
ds_read_b64_tr_b16 v[116:117],v2 offset:10336
v_mfma_f32_16x16x32_bf16 v[248:251],v[112:115],a[140:143],v[248:251]
ds_read_b64_tr_b16 v[112:113],v2 offset:96
v_mfma_f32_16x16x32_bf16 a[192:195],v[64:67],a[68:71],a[192:195]
ds_read_b64_tr_b16 v[114:115],v2 offset:5216
ds_read_b64_tr_b16 v[118:119],v2 offset:15456
v_mfma_f32_16x16x32_bf16 a[192:195],v[68:71],a[76:79],a[192:195]
buffer_load_dwordx4 v26,s[48:51],s84 offen offset:2048 lds
v_mfma_f32_16x16x32_bf16 a[192:195],v[72:75],a[84:87],a[192:195]
ds_read_b64_tr_b16 v[122:123],v2 offset:25696
v_mfma_f32_16x16x32_bf16 a[192:195],v[76:79],a[92:95],a[192:195]
v_mfma_f32_16x16x32_bf16 a[196:199],v[76:79],a[124:127],a[196:199]
s_waitcnt vmcnt(28)
v_mfma_f32_16x16x32_bf16 a[196:199],v[72:75],a[116:119],a[196:199]
s_waitcnt vmcnt(19)
v_mfma_f32_16x16x32_bf16 a[196:199],v[68:71],a[108:111],a[196:199]
buffer_load_dwordx4 v[244:247],v6,s[28:31],s23 offen offset:2048 sc1
v_mfma_f32_16x16x32_bf16 a[196:199],v[64:67],a[100:103],a[196:199]
v_mfma_f32_16x16x32_bf16 a[200:203],v[64:67],a[64:67],a[200:203]
v_mfma_f32_16x16x32_bf16 a[200:203],v[68:71],a[72:75],a[200:203]
v_mfma_f32_16x16x32_bf16 a[200:203],v[72:75],a[80:83],a[200:203]
v_mfma_f32_16x16x32_bf16 a[200:203],v[76:79],a[88:91],a[200:203]
v_mfma_f32_16x16x32_bf16 a[204:207],v[76:79],a[120:123],a[204:207]
s_waitcnt vmcnt(15)
ds_read_b64_tr_b16 v[76:77],v1 offset:30848
ds_read_b64_tr_b16 v[78:79],v1 offset:35968
v_mfma_f32_16x16x32_bf16 a[204:207],v[72:75],a[112:115],a[204:207]
buffer_load_dwordx4 v27,s[48:51],s84 offen offset:3072 lds
v_mfma_f32_16x16x32_bf16 a[204:207],v[68:71],a[104:107],a[204:207]
s_add_u32 m0,s86,132096
ds_read_b64_tr_b16 v[68:69],v1 offset:10368
ds_read_b64_tr_b16 v[70:71],v1 offset:15488
v_mfma_f32_16x16x32_bf16 a[204:207],v[64:67],a[96:99],a[204:207]
ds_read_b64_tr_b16 v[64:65],v1 offset:128
v_mfma_f32_16x16x32_bf16 a[208:211],v[80:83],a[68:71],a[208:211]
ds_read_b64_tr_b16 v[66:67],v1 offset:5248
v_mfma_f32_16x16x32_bf16 a[208:211],v[84:87],a[76:79],a[208:211]
ds_read_b64_tr_b16 v[72:73],v1 offset:20608
v_mfma_f32_16x16x32_bf16 a[208:211],v[88:91],a[84:87],a[208:211]
buffer_load_dwordx4 v[188:191],v28,s[28:31],s23 offen offset:3072 sc1
)A"
R"A(v_mfma_f32_16x16x32_bf16 a[208:211],v[92:95],a[92:95],a[208:211]
ds_read_b64_tr_b16 v[74:75],v1 offset:25728
v_mfma_f32_16x16x32_bf16 a[212:215],v[92:95],a[124:127],a[212:215]
v_mfma_f32_16x16x32_bf16 a[212:215],v[88:91],a[116:119],a[212:215]
v_mfma_f32_16x16x32_bf16 a[212:215],v[84:87],a[108:111],a[212:215]
v_mfma_f32_16x16x32_bf16 a[212:215],v[80:83],a[100:103],a[212:215]
v_mfma_f32_16x16x32_bf16 a[216:219],v[80:83],a[64:67],a[216:219]
v_mfma_f32_16x16x32_bf16 a[216:219],v[84:87],a[72:75],a[216:219]
buffer_load_dwordx4 v7,s[48:51],s84 offen lds
v_mfma_f32_16x16x32_bf16 a[216:219],v[88:91],a[80:83],a[216:219]
v_mfma_f32_16x16x32_bf16 a[216:219],v[92:95],a[88:91],a[216:219]
v_mfma_f32_16x16x32_bf16 a[220:223],v[92:95],a[120:123],a[220:223]
ds_read_b64_tr_b16 v[92:93],v2 offset:30880
ds_read_b64_tr_b16 v[94:95],v2 offset:36000
v_mfma_f32_16x16x32_bf16 a[220:223],v[88:91],a[112:115],a[220:223]
ds_read_b64_tr_b16 v[88:89],v2 offset:20640
v_mfma_f32_16x16x32_bf16 a[220:223],v[84:87],a[104:107],a[220:223]
buffer_load_dwordx4 v[172:175],v28,s[28:31],s22 offen offset:3072 sc1
v_mfma_f32_16x16x32_bf16 a[220:223],v[80:83],a[96:99],a[220:223]
s_waitcnt lgkmcnt(3)
ds_read_b64_tr_b16 v[80:81],v2 offset:160
ds_read_b64_tr_b16 v[82:83],v2 offset:5280
v_mfma_f32_16x16x32_bf16 a[224:227],v[96:99],a[68:71],a[224:227]
ds_read_b64_tr_b16 v[84:85],v2 offset:10400
v_mfma_f32_16x16x32_bf16 a[224:227],v[100:103],a[76:79],a[224:227]
ds_read_b64_tr_b16 v[86:87],v2 offset:15520
v_mfma_f32_16x16x32_bf16 a[224:227],v[104:107],a[84:87],a[224:227]
ds_read_b64_tr_b16 v[90:91],v2 offset:25760
v_mfma_f32_16x16x32_bf16 a[224:227],v[108:111],a[92:95],a[224:227]
buffer_load_dwordx4 a[128:131],v6,s[28:31],s87 offen sc1
v_mfma_f32_16x16x32_bf16 a[228:231],v[108:111],a[124:127],a[228:231]
v_mfma_f32_16x16x32_bf16 a[228:231],v[104:107],a[116:119],a[228:231]
v_mfma_f32_16x16x32_bf16 a[228:231],v[100:103],a[108:111],a[228:231]
v_mfma_f32_16x16x32_bf16 a[228:231],v[96:99],a[100:103],a[228:231]
v_mfma_f32_16x16x32_bf16 a[232:235],v[96:99],a[64:67],a[232:235]
buffer_load_dwordx4 v[156:159],v28,s[28:31],s21 offen offset:3072 sc1
v_mfma_f32_16x16x32_bf16 a[232:235],v[100:103],a[72:75],a[232:235]
v_mfma_f32_16x16x32_bf16 a[232:235],v[104:107],a[80:83],a[232:235]
v_mfma_f32_16x16x32_bf16 a[232:235],v[108:111],a[88:91],a[232:235]
v_mfma_f32_16x16x32_bf16 a[236:239],v[108:111],a[120:123],a[236:239]
ds_read_b64_tr_b16 v[108:109],v1 offset:30912
ds_read_b64_tr_b16 v[110:111],v1 offset:36032
v_mfma_f32_16x16x32_bf16 a[236:239],v[104:107],a[112:115],a[236:239]
buffer_load_dwordx4 a[144:147],v6,s[28:31],s88 offen sc1
v_mfma_f32_16x16x32_bf16 a[236:239],v[100:103],a[104:107],a[236:239]
ds_read_b64_tr_b16 v[100:101],v1 offset:10432
ds_read_b64_tr_b16 v[102:103],v1 offset:15552
v_mfma_f32_16x16x32_bf16 a[236:239],v[96:99],a[96:99],a[236:239]
ds_read_b64_tr_b16 v[96:97],v1 offset:192
v_mfma_f32_16x16x32_bf16 a[240:243],v[112:115],a[68:71],a[240:243]
ds_read_b64_tr_b16 v[98:99],v1 offset:5312
v_mfma_f32_16x16x32_bf16 a[240:243],v[116:119],a[76:79],a[240:243]
ds_read_b64_tr_b16 v[104:105],v1 offset:20672
v_mfma_f32_16x16x32_bf16 a[240:243],v[120:123],a[84:87],a[240:243]
buffer_load_dwordx4 v[140:143],v28,s[28:31],s20 offen offset:3072 sc1
v_mfma_f32_16x16x32_bf16 a[240:243],v[124:127],a[92:95],a[240:243]
ds_read_b64_tr_b16 v[106:107],v1 offset:25792
v_add_u32 v1,0xffff6000,v1
v_mfma_f32_16x16x32_bf16 a[244:247],v[124:127],a[124:127],a[244:247]
v_mfma_f32_16x16x32_bf16 a[244:247],v[120:123],a[116:119],a[244:247]
v_mfma_f32_16x16x32_bf16 a[244:247],v[116:119],a[108:111],a[244:247]
v_mfma_f32_16x16x32_bf16 a[244:247],v[112:115],a[100:103],a[244:247]
buffer_load_dwordx4 a[160:163],v6,s[28:31],s89 offen sc1
v_mfma_f32_16x16x32_bf16 a[248:251],v[112:115],a[64:67],a[248:251]
v_mfma_f32_16x16x32_bf16 a[248:251],v[116:119],a[72:75],a[248:251]
v_mfma_f32_16x16x32_bf16 a[248:251],v[120:123],a[80:83],a[248:251]
v_mfma_f32_16x16x32_bf16 a[248:251],v[124:127],a[88:91],a[248:251]
v_mfma_f32_16x16x32_bf16 a[252:255],v[124:127],a[120:123],a[252:255]
buffer_load_dwordx4 a[176:179],v6,s[28:31],s90 offen sc1
v_mfma_f32_16x16x32_bf16 a[252:255],v[120:123],a[112:115],a[252:255]
ds_read_b64_tr_b16 v[120:121],v2 offset:20704
ds_read_b64_tr_b16 v[122:123],v2 offset:25824
v_mfma_f32_16x16x32_bf16 a[252:255],v[116:119],a[104:107],a[252:255]
ds_read_b64_tr_b16 v[116:117],v2 offset:10464
v_mfma_f32_16x16x32_bf16 a[252:255],v[112:115],a[96:99],a[252:255]
ds_read_b64_tr_b16 v[112:113],v2 offset:224
v_mfma_f32_16x16x32_bf16 a[0:3],v[64:67],a[68:71],a[0:3]
ds_read_b64_tr_b16 v[114:115],v2 offset:5344
v_mfma_f32_16x16x32_bf16 a[0:3],v[68:71],a[76:79],a[0:3]
buffer_load_dwordx4 a[180:183],v6,s[28:31],s90 offen offset:1024 sc1
v_mfma_f32_16x16x32_bf16 a[0:3],v[72:75],a[84:87],a[0:3]
ds_read_b64_tr_b16 v[118:119],v2 offset:15584
ds_read_b64_tr_b16 v[124:125],v2 offset:30944
v_mfma_f32_16x16x32_bf16 a[0:3],v[76:79],a[92:95],a[0:3]
ds_read_b64_tr_b16 v[126:127],v2 offset:36064
v_add_u32 v2,0xffff6000,v2
v_mfma_f32_16x16x32_bf16 a[4:7],v[76:79],a[124:127],a[4:7]
v_mfma_f32_16x16x32_bf16 a[4:7],v[72:75],a[116:119],a[4:7]
v_mfma_f32_16x16x32_bf16 a[4:7],v[68:71],a[108:111],a[4:7]
buffer_load_dwordx4 a[164:167],v6,s[28:31],s89 offen offset:1024 sc1
v_mfma_f32_16x16x32_bf16 a[4:7],v[64:67],a[100:103],a[4:7]
v_mfma_f32_16x16x32_bf16 a[8:11],v[64:67],a[64:67],a[8:11]
v_mfma_f32_16x16x32_bf16 a[8:11],v[68:71],a[72:75],a[8:11]
v_mfma_f32_16x16x32_bf16 a[8:11],v[72:75],a[80:83],a[8:11]
v_mfma_f32_16x16x32_bf16 a[8:11],v[76:79],a[88:91],a[8:11]
buffer_load_dwordx4 a[148:151],v6,s[28:31],s88 offen offset:1024 sc1
v_mfma_f32_16x16x32_bf16 a[12:15],v[76:79],a[120:123],a[12:15]
v_mfma_f32_16x16x32_bf16 a[12:15],v[72:75],a[112:115],a[12:15]
v_mfma_f32_16x16x32_bf16 a[12:15],v[68:71],a[104:107],a[12:15]
v_mfma_f32_16x16x32_bf16 a[12:15],v[64:67],a[96:99],a[12:15]
s_waitcnt vmcnt(10)
s_barrier
s_waitcnt lgkmcnt(0)
v_mfma_f32_16x16x32_bf16 a[16:19],v[80:83],a[68:71],a[16:19]
buffer_load_dwordx4 a[132:135],v6,s[28:31],s87 offen offset:1024 sc1
v_mfma_f32_16x16x32_bf16 a[16:19],v[84:87],a[76:79],a[16:19]
ds_read_b64_tr_b16 v[64:65],v1
ds_read_b64_tr_b16 v[66:67],v1 offset:5120
v_mfma_f32_16x16x32_bf16 a[16:19],v[88:91],a[84:87],a[16:19]
ds_read_b64_tr_b16 v[68:69],v1 offset:10240
v_mfma_f32_16x16x32_bf16 a[16:19],v[92:95],a[92:95],a[16:19]
ds_read_b64_tr_b16 v[70:71],v1 offset:15360
v_mfma_f32_16x16x32_bf16 a[20:23],v[92:95],a[124:127],a[20:23]
ds_read_b64_tr_b16 v[72:73],v1 offset:20480
v_mfma_f32_16x16x32_bf16 a[20:23],v[88:91],a[116:119],a[20:23]
buffer_load_dwordx4 a[136:139],v6,s[28:31],s87 offen offset:2048 sc1
v_mfma_f32_16x16x32_bf16 a[20:23],v[84:87],a[108:111],a[20:23]
s_nop 0
L_pd17_%=:
s_cbranch_execz L_trtg2F_%=
ds_read_b64_tr_b16 v[74:75],v1 offset:25600
ds_read_b64_tr_b16 v[76:77],v1 offset:30720
v_mfma_f32_16x16x32_bf16 a[20:23],v[80:83],a[100:103],a[20:23]
ds_read_b64_tr_b16 v[78:79],v1 offset:35840
v_add_u32 v1,0x1e000,v1
v_mfma_f32_16x16x32_bf16 a[24:27],v[80:83],a[64:67],a[24:27]
v_mfma_f32_16x16x32_bf16 a[24:27],v[84:87],a[72:75],a[24:27]
v_mfma_f32_16x16x32_bf16 a[24:27],v[88:91],a[80:83],a[24:27]
buffer_load_dwordx4 a[152:155],v6,s[28:31],s88 offen offset:2048 sc1
v_mfma_f32_16x16x32_bf16 a[24:27],v[92:95],a[88:91],a[24:27]
v_mfma_f32_16x16x32_bf16 a[28:31],v[92:95],a[120:123],a[28:31]
ds_read_b64_tr_b16 v[92:93],v2 offset:30752
ds_read_b64_tr_b16 v[94:95],v2 offset:35872
v_mfma_f32_16x16x32_bf16 a[28:31],v[88:91],a[112:115],a[28:31]
ds_read_b64_tr_b16 v[88:89],v2 offset:20512
v_mfma_f32_16x16x32_bf16 a[28:31],v[84:87],a[104:107],a[28:31]
ds_read_b64_tr_b16 v[84:85],v2 offset:10272
v_mfma_f32_16x16x32_bf16 a[28:31],v[80:83],a[96:99],a[28:31]
buffer_load_dwordx4 a[168:171],v6,s[28:31],s89 offen offset:2048 sc1
v_mfma_f32_16x16x32_bf16 v[128:131],v[96:99],a[68:71],v[128:131]
ds_read_b64_tr_b16 v[80:81],v2 offset:32
ds_read_b64_tr_b16 v[82:83],v2 offset:5152
v_mfma_f32_16x16x32_bf16 v[128:131],v[100:103],a[76:79],v[128:131]
ds_read_b64_tr_b16 v[86:87],v2 offset:15392
v_mfma_f32_16x16x32_bf16 v[128:131],v[104:107],a[84:87],v[128:131]
ds_read_b64_tr_b16 v[90:91],v2 offset:25632
v_add_u32 v2,0x1e000,v2
v_mfma_f32_16x16x32_bf16 v[128:131],v[108:111],a[92:95],v[128:131]
v_mfma_f32_16x16x32_bf16 v[136:139],v[108:111],a[124:127],v[136:139]
buffer_load_dwordx4 a[184:187],v6,s[28:31],s90 offen offset:2048 sc1
v_mfma_f32_16x16x32_bf16 v[136:139],v[104:107],a[116:119],v[136:139]
v_mfma_f32_16x16x32_bf16 v[136:139],v[100:103],a[108:111],v[136:139]
v_mfma_f32_16x16x32_bf16 v[136:139],v[96:99],a[100:103],v[136:139]
v_mfma_f32_16x16x32_bf16 v[144:147],v[96:99],a[64:67],v[144:147]
v_mfma_f32_16x16x32_bf16 v[144:147],v[100:103],a[72:75],v[144:147]
buffer_load_dwordx4 a[188:191],v28,s[28:31],s90 offen offset:3072 sc1
v_mfma_f32_16x16x32_bf16 v[144:147],v[104:107],a[80:83],v[144:147]
v_mfma_f32_16x16x32_bf16 v[144:147],v[108:111],a[88:91],v[144:147]
v_mfma_f32_16x16x32_bf16 v[152:155],v[108:111],a[120:123],v[152:155]
ds_read_b64_tr_b16 v[108:109],v1 offset:30720
ds_read_b64_tr_b16 v[110:111],v1 offset:35840
v_mfma_f32_16x16x32_bf16 v[152:155],v[104:107],a[112:115],v[152:155]
ds_read_b64_tr_b16 v[104:105],v1 offset:20480
v_mfma_f32_16x16x32_bf16 v[152:155],v[100:103],a[104:107],v[152:155]
buffer_load_dwordx4 a[172:175],v28,s[28:31],s89 offen offset:3072 sc1
v_mfma_f32_16x16x32_bf16 v[152:155],v[96:99],a[96:99],v[152:155]
ds_read_b64_tr_b16 v[96:97],v1
ds_read_b64_tr_b16 v[98:99],v1 offset:5120
v_mfma_f32_16x16x32_bf16 v[160:163],v[112:115],a[68:71],v[160:163]
ds_read_b64_tr_b16 v[100:101],v1 offset:10240
v_mfma_f32_16x16x32_bf16 v[160:163],v[116:119],a[76:79],v[160:163]
ds_read_b64_tr_b16 v[102:103],v1 offset:15360
v_mfma_f32_16x16x32_bf16 v[160:163],v[120:123],a[84:87],v[160:163]
ds_read_b64_tr_b16 v[106:107],v1 offset:25600
v_add_u32 v1,0xfffe2000,v1
v_mfma_f32_16x16x32_bf16 v[160:163],v[124:127],a[92:95],v[160:163]
buffer_load_dwordx4 a[156:159],v28,s[28:31],s88 offen offset:3072 sc1
v_mfma_f32_16x16x32_bf16 v[168:171],v[124:127],a[124:127],v[168:171]
v_mfma_f32_16x16x32_bf16 v[168:171],v[120:123],a[116:119],v[168:171]
v_mfma_f32_16x16x32_bf16 v[168:171],v[116:119],a[108:111],v[168:171]
v_mfma_f32_16x16x32_bf16 v[168:171],v[112:115],a[100:103],v[168:171]
v_mfma_f32_16x16x32_bf16 v[176:179],v[112:115],a[64:67],v[176:179]
buffer_load_dwordx4 a[140:143],v28,s[28:31],s87 offen offset:3072 sc1
v_mfma_f32_16x16x32_bf16 v[176:179],v[116:119],a[72:75],v[176:179]
s_mul_i32 s87,s60,81920
s_add_u32 s87,s87,s66
s_add_u32 s83,s83,s87
v_mfma_f32_16x16x32_bf16 v[176:179],v[120:123],a[80:83],v[176:179]
s_lshl_b32 s66,s63,6
s_add_u32 s87,s87,s66
s_add_u32 s88,s87,20480
v_mfma_f32_16x16x32_bf16 v[176:179],v[124:127],a[88:91],v[176:179]
s_add_u32 s89,s87,40960
s_add_u32 s90,s87,61440
s_add_u32 s66,s77,4096
v_mfma_f32_16x16x32_bf16 v[184:187],v[124:127],a[120:123],v[184:187]
ds_read_b64_tr_b16 v[124:125],v2 offset:30752
ds_read_b64_tr_b16 v[126:127],v2 offset:35872
v_mfma_f32_16x16x32_bf16 v[184:187],v[120:123],a[112:115],v[184:187]
ds_read_b64_tr_b16 v[120:121],v2 offset:20512
v_mfma_f32_16x16x32_bf16 v[184:187],v[116:119],a[104:107],v[184:187]
ds_read_b64_tr_b16 v[116:117],v2 offset:10272
v_mfma_f32_16x16x32_bf16 v[184:187],v[112:115],a[96:99],v[184:187]
s_waitcnt lgkmcnt(4)
ds_read_b64_tr_b16 v[112:113],v2 offset:32
v_mfma_f32_16x16x32_bf16 a[192:195],v[64:67],v[32:35],a[192:195]
ds_read_b64_tr_b16 v[114:115],v2 offset:5152
ds_read_b64_tr_b16 v[118:119],v2 offset:15392
v_mfma_f32_16x16x32_bf16 a[192:195],v[68:71],v[36:39],a[192:195]
ds_read_b64_tr_b16 v[122:123],v2 offset:25632
v_add_u32 v2,0xfffe2000,v2
v_mfma_f32_16x16x32_bf16 a[192:195],v[72:75],v[40:43],a[192:195]
v_mfma_f32_16x16x32_bf16 a[192:195],v[76:79],v[44:47],a[192:195]
v_mfma_f32_16x16x32_bf16 a[196:199],v[76:79],v[180:183],a[196:199]
v_mfma_f32_16x16x32_bf16 a[196:199],v[72:75],v[164:167],a[196:199]
v_mfma_f32_16x16x32_bf16 a[196:199],v[68:71],v[148:151],a[196:199]
v_mfma_f32_16x16x32_bf16 a[196:199],v[64:67],v[132:135],a[196:199]
v_mfma_f32_16x16x32_bf16 a[200:203],v[64:67],v[196:199],a[200:203]
)A"
R"A(v_mfma_f32_16x16x32_bf16 a[200:203],v[68:71],v[212:215],a[200:203]
v_mfma_f32_16x16x32_bf16 a[200:203],v[72:75],v[228:231],a[200:203]
v_mfma_f32_16x16x32_bf16 a[200:203],v[76:79],v[244:247],a[200:203]
v_mfma_f32_16x16x32_bf16 a[204:207],v[76:79],v[188:191],a[204:207]
s_waitcnt vmcnt(18)
ds_read_b64_tr_b16 v[76:77],v1 offset:30784
ds_read_b64_tr_b16 v[78:79],v1 offset:35904
v_mfma_f32_16x16x32_bf16 a[204:207],v[72:75],v[172:175],a[204:207]
s_waitcnt vmcnt(16)
ds_read_b64_tr_b16 v[72:73],v1 offset:20544
v_mfma_f32_16x16x32_bf16 a[204:207],v[68:71],v[156:159],a[204:207]
s_waitcnt vmcnt(14)
ds_read_b64_tr_b16 v[68:69],v1 offset:10304
v_mfma_f32_16x16x32_bf16 a[204:207],v[64:67],v[140:143],a[204:207]
ds_read_b64_tr_b16 v[64:65],v1 offset:64
v_mfma_f32_16x16x32_bf16 a[208:211],v[80:83],v[32:35],a[208:211]
ds_read_b64_tr_b16 v[66:67],v1 offset:5184
ds_read_b64_tr_b16 v[70:71],v1 offset:15424
v_mfma_f32_16x16x32_bf16 a[208:211],v[84:87],v[36:39],a[208:211]
ds_read_b64_tr_b16 v[74:75],v1 offset:25664
v_add_u32 v1,0x1e000,v1
v_mfma_f32_16x16x32_bf16 a[208:211],v[88:91],v[40:43],a[208:211]
v_mfma_f32_16x16x32_bf16 a[208:211],v[92:95],v[44:47],a[208:211]
v_mfma_f32_16x16x32_bf16 a[212:215],v[92:95],v[180:183],a[212:215]
v_mfma_f32_16x16x32_bf16 a[212:215],v[88:91],v[164:167],a[212:215]
v_mfma_f32_16x16x32_bf16 a[212:215],v[84:87],v[148:151],a[212:215]
v_mfma_f32_16x16x32_bf16 a[212:215],v[80:83],v[132:135],a[212:215]
v_mfma_f32_16x16x32_bf16 a[216:219],v[80:83],v[196:199],a[216:219]
v_mfma_f32_16x16x32_bf16 a[216:219],v[84:87],v[212:215],a[216:219]
v_mfma_f32_16x16x32_bf16 a[216:219],v[88:91],v[228:231],a[216:219]
v_mfma_f32_16x16x32_bf16 a[216:219],v[92:95],v[244:247],a[216:219]
v_mfma_f32_16x16x32_bf16 a[220:223],v[92:95],v[188:191],a[220:223]
ds_read_b64_tr_b16 v[92:93],v2 offset:30816
ds_read_b64_tr_b16 v[94:95],v2 offset:35936
v_mfma_f32_16x16x32_bf16 a[220:223],v[88:91],v[172:175],a[220:223]
ds_read_b64_tr_b16 v[88:89],v2 offset:20576
v_mfma_f32_16x16x32_bf16 a[220:223],v[84:87],v[156:159],a[220:223]
ds_read_b64_tr_b16 v[84:85],v2 offset:10336
v_mfma_f32_16x16x32_bf16 a[220:223],v[80:83],v[140:143],a[220:223]
ds_read_b64_tr_b16 v[80:81],v2 offset:96
v_mfma_f32_16x16x32_bf16 a[192:195],v[96:99],a[128:131],a[192:195]
ds_read_b64_tr_b16 v[82:83],v2 offset:5216
ds_read_b64_tr_b16 v[86:87],v2 offset:15456
v_mfma_f32_16x16x32_bf16 a[192:195],v[100:103],a[144:147],a[192:195]
s_waitcnt vmcnt(13)
ds_read_b64_tr_b16 v[90:91],v2 offset:25696
v_add_u32 v2,0x1e000,v2
v_mfma_f32_16x16x32_bf16 a[192:195],v[104:107],a[160:163],a[192:195]
s_waitcnt vmcnt(12)
v_mfma_f32_16x16x32_bf16 a[192:195],v[108:111],a[176:179],a[192:195]
s_waitcnt vmcnt(11)
v_mfma_f32_16x16x32_bf16 a[196:199],v[108:111],a[180:183],a[196:199]
s_waitcnt vmcnt(10)
v_mfma_f32_16x16x32_bf16 a[196:199],v[104:107],a[164:167],a[196:199]
s_waitcnt vmcnt(9)
v_mfma_f32_16x16x32_bf16 a[196:199],v[100:103],a[148:151],a[196:199]
s_waitcnt vmcnt(8)
v_mfma_f32_16x16x32_bf16 a[196:199],v[96:99],a[132:135],a[196:199]
s_waitcnt vmcnt(7)
v_mfma_f32_16x16x32_bf16 a[200:203],v[96:99],a[136:139],a[200:203]
s_waitcnt vmcnt(6)
v_mfma_f32_16x16x32_bf16 a[200:203],v[100:103],a[152:155],a[200:203]
s_waitcnt vmcnt(5)
v_mfma_f32_16x16x32_bf16 a[200:203],v[104:107],a[168:171],a[200:203]
s_waitcnt vmcnt(4)
v_mfma_f32_16x16x32_bf16 a[200:203],v[108:111],a[184:187],a[200:203]
s_waitcnt vmcnt(3)
v_accvgpr_read_b32 v16,a192
v_accvgpr_read_b32 v17,a193
v_mfma_f32_16x16x32_bf16 a[204:207],v[108:111],a[188:191],a[204:207]
s_waitcnt vmcnt(2)
v_accvgpr_read_b32 v18,a194
v_accvgpr_read_b32 v19,a195
v_mfma_f32_16x16x32_bf16 a[204:207],v[104:107],a[172:175],a[204:207]
s_waitcnt vmcnt(1)
v_cvt_pk_bf16_f32 v16,v16,v17
v_cvt_pk_bf16_f32 v17,v18,v19
v_mfma_f32_16x16x32_bf16 a[204:207],v[100:103],a[156:159],a[204:207]
s_waitcnt vmcnt(0)
v_accvgpr_read_b32 v8,a196
v_accvgpr_read_b32 v9,a197
v_mfma_f32_16x16x32_bf16 a[204:207],v[96:99],a[140:143],a[204:207]
s_waitcnt lgkmcnt(0)
v_accvgpr_read_b32 v10,a198
v_accvgpr_read_b32 v11,a199
v_mfma_f32_16x16x32_bf16 a[208:211],v[112:115],a[128:131],a[208:211]
v_cvt_pk_bf16_f32 v8,v8,v9
v_cvt_pk_bf16_f32 v9,v10,v11
v_mfma_f32_16x16x32_bf16 a[208:211],v[116:119],a[144:147],a[208:211]
ds_read_b64_tr_b16 v[96:97],v1 offset:64
ds_read_b64_tr_b16 v[98:99],v1 offset:5184
v_mfma_f32_16x16x32_bf16 a[208:211],v[120:123],a[160:163],a[208:211]
ds_read_b64_tr_b16 v[100:101],v1 offset:10304
v_mfma_f32_16x16x32_bf16 a[208:211],v[124:127],a[176:179],a[208:211]
ds_read_b64_tr_b16 v[102:103],v1 offset:15424
v_mfma_f32_16x16x32_bf16 a[212:215],v[124:127],a[180:183],a[212:215]
ds_read_b64_tr_b16 v[104:105],v1 offset:20544
v_mfma_f32_16x16x32_bf16 a[212:215],v[120:123],a[164:167],a[212:215]
ds_read_b64_tr_b16 v[106:107],v1 offset:25664
ds_read_b64_tr_b16 v[108:109],v1 offset:30784
v_mfma_f32_16x16x32_bf16 a[212:215],v[116:119],a[148:151],a[212:215]
ds_read_b64_tr_b16 v[110:111],v1 offset:35904
v_add_u32 v1,0xfffe2000,v1
v_mfma_f32_16x16x32_bf16 a[212:215],v[112:115],a[132:135],a[212:215]
v_accvgpr_read_b32 v20,a208
v_accvgpr_read_b32 v21,a209
v_mfma_f32_16x16x32_bf16 a[216:219],v[112:115],a[136:139],a[216:219]
v_accvgpr_read_b32 v22,a210
v_accvgpr_read_b32 v23,a211
v_mfma_f32_16x16x32_bf16 a[216:219],v[116:119],a[152:155],a[216:219]
v_cvt_pk_bf16_f32 v18,v20,v21
v_cvt_pk_bf16_f32 v19,v22,v23
v_mfma_f32_16x16x32_bf16 a[216:219],v[120:123],a[168:171],a[216:219]
buffer_store_dwordx4 v[16:19],v6,s[36:39],s74 offen sc1
v_mfma_f32_16x16x32_bf16 a[216:219],v[124:127],a[184:187],a[216:219]
v_accvgpr_read_b32 v12,a212
v_accvgpr_read_b32 v13,a213
v_mfma_f32_16x16x32_bf16 a[220:223],v[124:127],a[188:191],a[220:223]
v_accvgpr_read_b32 v14,a214
v_accvgpr_read_b32 v15,a215
v_mfma_f32_16x16x32_bf16 a[220:223],v[120:123],a[172:175],a[220:223]
v_cvt_pk_bf16_f32 v10,v12,v13
v_cvt_pk_bf16_f32 v11,v14,v15
v_mfma_f32_16x16x32_bf16 a[220:223],v[116:119],a[156:159],a[220:223]
v_accvgpr_read_b32 v16,a200
v_accvgpr_read_b32 v17,a201
v_mfma_f32_16x16x32_bf16 a[220:223],v[112:115],a[140:143],a[220:223]
v_accvgpr_read_b32 v18,a202
v_accvgpr_read_b32 v19,a203
v_mfma_f32_16x16x32_bf16 a[224:227],v[64:67],v[32:35],a[224:227]
v_accvgpr_read_b32 v20,a216
v_accvgpr_read_b32 v21,a217
v_mfma_f32_16x16x32_bf16 a[224:227],v[68:71],v[36:39],a[224:227]
v_accvgpr_read_b32 v22,a218
v_accvgpr_read_b32 v23,a219
v_mfma_f32_16x16x32_bf16 a[224:227],v[72:75],v[40:43],a[224:227]
v_cvt_pk_bf16_f32 v16,v16,v17
v_cvt_pk_bf16_f32 v17,v18,v19
v_mfma_f32_16x16x32_bf16 a[224:227],v[76:79],v[44:47],a[224:227]
v_cvt_pk_bf16_f32 v18,v20,v21
v_cvt_pk_bf16_f32 v19,v22,v23
v_mfma_f32_16x16x32_bf16 a[228:231],v[76:79],v[180:183],a[228:231]
ds_read_b64_tr_b16 v[112:113],v2 offset:96
ds_read_b64_tr_b16 v[114:115],v2 offset:5216
v_mfma_f32_16x16x32_bf16 a[228:231],v[72:75],v[164:167],a[228:231]
buffer_store_dwordx4 v[8:11],v6,s[36:39],s74 offen offset:1024 sc1
v_mfma_f32_16x16x32_bf16 a[228:231],v[68:71],v[148:151],a[228:231]
ds_read_b64_tr_b16 v[116:117],v2 offset:10336
ds_read_b64_tr_b16 v[118:119],v2 offset:15456
v_mfma_f32_16x16x32_bf16 a[228:231],v[64:67],v[132:135],a[228:231]
ds_read_b64_tr_b16 v[120:121],v2 offset:20576
v_accvgpr_read_b32 v8,a204
v_mfma_f32_16x16x32_bf16 a[232:235],v[64:67],v[196:199],a[232:235]
ds_read_b64_tr_b16 v[122:123],v2 offset:25696
v_accvgpr_read_b32 v9,a205
v_mfma_f32_16x16x32_bf16 a[232:235],v[68:71],v[212:215],a[232:235]
ds_read_b64_tr_b16 v[124:125],v2 offset:30816
v_accvgpr_read_b32 v10,a206
v_mfma_f32_16x16x32_bf16 a[232:235],v[72:75],v[228:231],a[232:235]
ds_read_b64_tr_b16 v[126:127],v2 offset:35936
v_accvgpr_read_b32 v11,a207
v_mfma_f32_16x16x32_bf16 a[232:235],v[76:79],v[244:247],a[232:235]
v_accvgpr_read_b32 v12,a220
v_accvgpr_read_b32 v13,a221
v_mfma_f32_16x16x32_bf16 a[236:239],v[76:79],v[188:191],a[236:239]
v_accvgpr_read_b32 v14,a222
v_accvgpr_read_b32 v15,a223
v_mfma_f32_16x16x32_bf16 a[236:239],v[72:75],v[172:175],a[236:239]
v_cvt_pk_bf16_f32 v8,v8,v9
v_cvt_pk_bf16_f32 v9,v10,v11
v_mfma_f32_16x16x32_bf16 a[236:239],v[68:71],v[156:159],a[236:239]
v_cvt_pk_bf16_f32 v10,v12,v13
v_cvt_pk_bf16_f32 v11,v14,v15
v_mfma_f32_16x16x32_bf16 a[236:239],v[64:67],v[140:143],a[236:239]
ds_read_b64_tr_b16 v[64:65],v1 offset:128
ds_read_b64_tr_b16 v[66:67],v1 offset:5248
v_mfma_f32_16x16x32_bf16 a[240:243],v[80:83],v[32:35],a[240:243]
buffer_store_dwordx4 v[16:19],v6,s[36:39],s74 offen offset:2048 sc1
v_mfma_f32_16x16x32_bf16 a[240:243],v[84:87],v[36:39],a[240:243]
ds_read_b64_tr_b16 v[68:69],v1 offset:10368
ds_read_b64_tr_b16 v[70:71],v1 offset:15488
v_mfma_f32_16x16x32_bf16 a[240:243],v[88:91],v[40:43],a[240:243]
ds_read_b64_tr_b16 v[72:73],v1 offset:20608
v_add_u32 v2,0xfffe2000,v2
v_mfma_f32_16x16x32_bf16 a[240:243],v[92:95],v[44:47],a[240:243]
ds_read_b64_tr_b16 v[74:75],v1 offset:25728
v_mfma_f32_16x16x32_bf16 a[244:247],v[92:95],v[180:183],a[244:247]
ds_read_b64_tr_b16 v[76:77],v1 offset:30848
v_mfma_f32_16x16x32_bf16 a[244:247],v[88:91],v[164:167],a[244:247]
ds_read_b64_tr_b16 v[78:79],v1 offset:35968
v_add_u32 v1,0x1e000,v1
v_mfma_f32_16x16x32_bf16 a[244:247],v[84:87],v[148:151],a[244:247]
v_mfma_f32_16x16x32_bf16 a[244:247],v[80:83],v[132:135],a[244:247]
v_mfma_f32_16x16x32_bf16 a[248:251],v[80:83],v[196:199],a[248:251]
v_mfma_f32_16x16x32_bf16 a[248:251],v[84:87],v[212:215],a[248:251]
v_mfma_f32_16x16x32_bf16 a[248:251],v[88:91],v[228:231],a[248:251]
v_mfma_f32_16x16x32_bf16 a[248:251],v[92:95],v[244:247],a[248:251]
buffer_store_dwordx4 v[8:11],v28,s[36:39],s74 offen offset:3072 sc1
v_mfma_f32_16x16x32_bf16 a[252:255],v[92:95],v[188:191],a[252:255]
ds_read_b64_tr_b16 v[92:93],v2 offset:30880
ds_read_b64_tr_b16 v[94:95],v2 offset:36000
v_mfma_f32_16x16x32_bf16 a[252:255],v[88:91],v[172:175],a[252:255]
ds_read_b64_tr_b16 v[88:89],v2 offset:20640
v_mfma_f32_16x16x32_bf16 a[252:255],v[84:87],v[156:159],a[252:255]
ds_read_b64_tr_b16 v[84:85],v2 offset:10400
v_mfma_f32_16x16x32_bf16 a[252:255],v[80:83],v[140:143],a[252:255]
s_waitcnt lgkmcnt(4)
ds_read_b64_tr_b16 v[80:81],v2 offset:160
v_mfma_f32_16x16x32_bf16 a[224:227],v[96:99],a[128:131],a[224:227]
ds_read_b64_tr_b16 v[82:83],v2 offset:5280
ds_read_b64_tr_b16 v[86:87],v2 offset:15520
v_mfma_f32_16x16x32_bf16 a[224:227],v[100:103],a[144:147],a[224:227]
ds_read_b64_tr_b16 v[90:91],v2 offset:25760
v_add_u32 v2,0x1e000,v2
v_mfma_f32_16x16x32_bf16 a[224:227],v[104:107],a[160:163],a[224:227]
v_mfma_f32_16x16x32_bf16 a[224:227],v[108:111],a[176:179],a[224:227]
v_mfma_f32_16x16x32_bf16 a[228:231],v[108:111],a[180:183],a[228:231]
v_mfma_f32_16x16x32_bf16 a[228:231],v[104:107],a[164:167],a[228:231]
v_mfma_f32_16x16x32_bf16 a[228:231],v[100:103],a[148:151],a[228:231]
v_mfma_f32_16x16x32_bf16 a[228:231],v[96:99],a[132:135],a[228:231]
v_mfma_f32_16x16x32_bf16 a[232:235],v[96:99],a[136:139],a[232:235]
v_mfma_f32_16x16x32_bf16 a[232:235],v[100:103],a[152:155],a[232:235]
v_mfma_f32_16x16x32_bf16 a[232:235],v[104:107],a[168:171],a[232:235]
v_mfma_f32_16x16x32_bf16 a[232:235],v[108:111],a[184:187],a[232:235]
v_accvgpr_read_b32 v16,a224
v_accvgpr_read_b32 v17,a225
v_mfma_f32_16x16x32_bf16 a[236:239],v[108:111],a[188:191],a[236:239]
v_accvgpr_read_b32 v18,a226
v_accvgpr_read_b32 v19,a227
v_mfma_f32_16x16x32_bf16 a[236:239],v[104:107],a[172:175],a[236:239]
v_cvt_pk_bf16_f32 v16,v16,v17
v_cvt_pk_bf16_f32 v17,v18,v19
v_mfma_f32_16x16x32_bf16 a[236:239],v[100:103],a[156:159],a[236:239]
v_accvgpr_read_b32 v8,a228
v_accvgpr_read_b32 v9,a229
v_mfma_f32_16x16x32_bf16 a[236:239],v[96:99],a[140:143],a[236:239]
v_accvgpr_read_b32 v10,a230
v_accvgpr_read_b32 v11,a231
v_mfma_f32_16x16x32_bf16 a[240:243],v[112:115],a[128:131],a[240:243]
v_cvt_pk_bf16_f32 v8,v8,v9
v_cvt_pk_bf16_f32 v9,v10,v11
v_mfma_f32_16x16x32_bf16 a[240:243],v[116:119],a[144:147],a[240:243]
ds_read_b64_tr_b16 v[96:97],v1 offset:128
ds_read_b64_tr_b16 v[98:99],v1 offset:5248
v_mfma_f32_16x16x32_bf16 a[240:243],v[120:123],a[160:163],a[240:243]
ds_read_b64_tr_b16 v[100:101],v1 offset:10368
v_mfma_f32_16x16x32_bf16 a[240:243],v[124:127],a[176:179],a[240:243]
ds_read_b64_tr_b16 v[102:103],v1 offset:15488
)A"
R"A(v_mfma_f32_16x16x32_bf16 a[244:247],v[124:127],a[180:183],a[244:247]
ds_read_b64_tr_b16 v[104:105],v1 offset:20608
v_mfma_f32_16x16x32_bf16 a[244:247],v[120:123],a[164:167],a[244:247]
ds_read_b64_tr_b16 v[106:107],v1 offset:25728
ds_read_b64_tr_b16 v[108:109],v1 offset:30848
v_mfma_f32_16x16x32_bf16 a[244:247],v[116:119],a[148:151],a[244:247]
ds_read_b64_tr_b16 v[110:111],v1 offset:35968
v_add_u32 v1,0xfffe2000,v1
v_mfma_f32_16x16x32_bf16 a[244:247],v[112:115],a[132:135],a[244:247]
v_accvgpr_read_b32 v20,a240
v_accvgpr_read_b32 v21,a241
v_mfma_f32_16x16x32_bf16 a[248:251],v[112:115],a[136:139],a[248:251]
v_accvgpr_read_b32 v22,a242
v_accvgpr_read_b32 v23,a243
v_mfma_f32_16x16x32_bf16 a[248:251],v[116:119],a[152:155],a[248:251]
v_cvt_pk_bf16_f32 v18,v20,v21
v_cvt_pk_bf16_f32 v19,v22,v23
v_mfma_f32_16x16x32_bf16 a[248:251],v[120:123],a[168:171],a[248:251]
buffer_store_dwordx4 v[16:19],v6,s[36:39],s75 offen sc1
v_mfma_f32_16x16x32_bf16 a[248:251],v[124:127],a[184:187],a[248:251]
v_accvgpr_read_b32 v12,a244
v_accvgpr_read_b32 v13,a245
v_mfma_f32_16x16x32_bf16 a[252:255],v[124:127],a[188:191],a[252:255]
v_accvgpr_read_b32 v14,a246
v_accvgpr_read_b32 v15,a247
v_mfma_f32_16x16x32_bf16 a[252:255],v[120:123],a[172:175],a[252:255]
v_cvt_pk_bf16_f32 v10,v12,v13
v_cvt_pk_bf16_f32 v11,v14,v15
v_mfma_f32_16x16x32_bf16 a[252:255],v[116:119],a[156:159],a[252:255]
v_accvgpr_read_b32 v16,a232
v_accvgpr_read_b32 v17,a233
v_mfma_f32_16x16x32_bf16 a[252:255],v[112:115],a[140:143],a[252:255]
v_accvgpr_read_b32 v18,a234
v_accvgpr_read_b32 v19,a235
v_mfma_f32_16x16x32_bf16 a[0:3],v[64:67],v[32:35],a[0:3]
v_accvgpr_read_b32 v20,a248
v_accvgpr_read_b32 v21,a249
v_mfma_f32_16x16x32_bf16 a[0:3],v[68:71],v[36:39],a[0:3]
v_accvgpr_read_b32 v22,a250
v_accvgpr_read_b32 v23,a251
v_mfma_f32_16x16x32_bf16 a[0:3],v[72:75],v[40:43],a[0:3]
v_cvt_pk_bf16_f32 v16,v16,v17
v_cvt_pk_bf16_f32 v17,v18,v19
v_mfma_f32_16x16x32_bf16 a[0:3],v[76:79],v[44:47],a[0:3]
v_cvt_pk_bf16_f32 v18,v20,v21
v_cvt_pk_bf16_f32 v19,v22,v23
v_mfma_f32_16x16x32_bf16 a[4:7],v[76:79],v[180:183],a[4:7]
ds_read_b64_tr_b16 v[112:113],v2 offset:160
ds_read_b64_tr_b16 v[114:115],v2 offset:5280
v_mfma_f32_16x16x32_bf16 a[4:7],v[72:75],v[164:167],a[4:7]
buffer_store_dwordx4 v[8:11],v6,s[36:39],s75 offen offset:1024 sc1
v_mfma_f32_16x16x32_bf16 a[4:7],v[68:71],v[148:151],a[4:7]
ds_read_b64_tr_b16 v[116:117],v2 offset:10400
ds_read_b64_tr_b16 v[118:119],v2 offset:15520
v_mfma_f32_16x16x32_bf16 a[4:7],v[64:67],v[132:135],a[4:7]
ds_read_b64_tr_b16 v[120:121],v2 offset:20640
v_accvgpr_read_b32 v8,a236
v_mfma_f32_16x16x32_bf16 a[8:11],v[64:67],v[196:199],a[8:11]
ds_read_b64_tr_b16 v[122:123],v2 offset:25760
v_accvgpr_read_b32 v9,a237
v_mfma_f32_16x16x32_bf16 a[8:11],v[68:71],v[212:215],a[8:11]
ds_read_b64_tr_b16 v[124:125],v2 offset:30880
v_accvgpr_read_b32 v10,a238
v_mfma_f32_16x16x32_bf16 a[8:11],v[72:75],v[228:231],a[8:11]
ds_read_b64_tr_b16 v[126:127],v2 offset:36000
v_accvgpr_read_b32 v11,a239
v_mfma_f32_16x16x32_bf16 a[8:11],v[76:79],v[244:247],a[8:11]
v_accvgpr_read_b32 v12,a252
v_accvgpr_read_b32 v13,a253
v_mfma_f32_16x16x32_bf16 a[12:15],v[76:79],v[188:191],a[12:15]
v_accvgpr_read_b32 v14,a254
v_accvgpr_read_b32 v15,a255
v_mfma_f32_16x16x32_bf16 a[12:15],v[72:75],v[172:175],a[12:15]
v_cvt_pk_bf16_f32 v8,v8,v9
v_cvt_pk_bf16_f32 v9,v10,v11
v_mfma_f32_16x16x32_bf16 a[12:15],v[68:71],v[156:159],a[12:15]
v_cvt_pk_bf16_f32 v10,v12,v13
v_cvt_pk_bf16_f32 v11,v14,v15
v_mfma_f32_16x16x32_bf16 a[12:15],v[64:67],v[140:143],a[12:15]
s_waitcnt lgkmcnt(3)
ds_read_b64_tr_b16 v[64:65],v1 offset:192
ds_read_b64_tr_b16 v[66:67],v1 offset:5312
v_mfma_f32_16x16x32_bf16 a[16:19],v[80:83],v[32:35],a[16:19]
buffer_store_dwordx4 v[16:19],v6,s[36:39],s75 offen offset:2048 sc1
v_mfma_f32_16x16x32_bf16 a[16:19],v[84:87],v[36:39],a[16:19]
ds_read_b64_tr_b16 v[68:69],v1 offset:10432
ds_read_b64_tr_b16 v[70:71],v1 offset:15552
v_mfma_f32_16x16x32_bf16 a[16:19],v[88:91],v[40:43],a[16:19]
ds_read_b64_tr_b16 v[72:73],v1 offset:20672
v_add_u32 v2,0xfffe2000,v2
v_mfma_f32_16x16x32_bf16 a[16:19],v[92:95],v[44:47],a[16:19]
ds_read_b64_tr_b16 v[74:75],v1 offset:25792
v_mfma_f32_16x16x32_bf16 a[20:23],v[92:95],v[180:183],a[20:23]
ds_read_b64_tr_b16 v[76:77],v1 offset:30912
v_mfma_f32_16x16x32_bf16 a[20:23],v[88:91],v[164:167],a[20:23]
ds_read_b64_tr_b16 v[78:79],v1 offset:36032
v_add_u32 v1,0x1e000,v1
v_mfma_f32_16x16x32_bf16 a[20:23],v[84:87],v[148:151],a[20:23]
v_mfma_f32_16x16x32_bf16 a[20:23],v[80:83],v[132:135],a[20:23]
v_mfma_f32_16x16x32_bf16 a[24:27],v[80:83],v[196:199],a[24:27]
v_mfma_f32_16x16x32_bf16 a[24:27],v[84:87],v[212:215],a[24:27]
v_mfma_f32_16x16x32_bf16 a[24:27],v[88:91],v[228:231],a[24:27]
v_mfma_f32_16x16x32_bf16 a[24:27],v[92:95],v[244:247],a[24:27]
buffer_store_dwordx4 v[8:11],v28,s[36:39],s75 offen offset:3072 sc1
v_mfma_f32_16x16x32_bf16 a[28:31],v[92:95],v[188:191],a[28:31]
ds_read_b64_tr_b16 v[92:93],v2 offset:30944
ds_read_b64_tr_b16 v[94:95],v2 offset:36064
v_mfma_f32_16x16x32_bf16 a[28:31],v[88:91],v[172:175],a[28:31]
ds_read_b64_tr_b16 v[88:89],v2 offset:20704
v_mfma_f32_16x16x32_bf16 a[28:31],v[84:87],v[156:159],a[28:31]
ds_read_b64_tr_b16 v[84:85],v2 offset:10464
v_mfma_f32_16x16x32_bf16 a[28:31],v[80:83],v[140:143],a[28:31]
ds_read_b64_tr_b16 v[80:81],v2 offset:224
v_mfma_f32_16x16x32_bf16 a[0:3],v[96:99],a[128:131],a[0:3]
ds_read_b64_tr_b16 v[82:83],v2 offset:5344
ds_read_b64_tr_b16 v[86:87],v2 offset:15584
v_mfma_f32_16x16x32_bf16 a[0:3],v[100:103],a[144:147],a[0:3]
ds_read_b64_tr_b16 v[90:91],v2 offset:25824
v_add_u32 v2,0x1e000,v2
v_mfma_f32_16x16x32_bf16 a[0:3],v[104:107],a[160:163],a[0:3]
v_mfma_f32_16x16x32_bf16 a[0:3],v[108:111],a[176:179],a[0:3]
v_mfma_f32_16x16x32_bf16 a[4:7],v[108:111],a[180:183],a[4:7]
v_mfma_f32_16x16x32_bf16 a[4:7],v[104:107],a[164:167],a[4:7]
v_mfma_f32_16x16x32_bf16 a[4:7],v[100:103],a[148:151],a[4:7]
v_mfma_f32_16x16x32_bf16 a[4:7],v[96:99],a[132:135],a[4:7]
v_mfma_f32_16x16x32_bf16 a[8:11],v[96:99],a[136:139],a[8:11]
v_mfma_f32_16x16x32_bf16 a[8:11],v[100:103],a[152:155],a[8:11]
v_mfma_f32_16x16x32_bf16 a[8:11],v[104:107],a[168:171],a[8:11]
v_mfma_f32_16x16x32_bf16 a[8:11],v[108:111],a[184:187],a[8:11]
v_accvgpr_read_b32 v16,a0
v_accvgpr_read_b32 v17,a1
v_mfma_f32_16x16x32_bf16 a[12:15],v[108:111],a[188:191],a[12:15]
v_accvgpr_read_b32 v18,a2
v_accvgpr_read_b32 v19,a3
v_mfma_f32_16x16x32_bf16 a[12:15],v[104:107],a[172:175],a[12:15]
v_cvt_pk_bf16_f32 v16,v16,v17
v_cvt_pk_bf16_f32 v17,v18,v19
v_mfma_f32_16x16x32_bf16 a[12:15],v[100:103],a[156:159],a[12:15]
v_accvgpr_read_b32 v8,a4
v_accvgpr_read_b32 v9,a5
v_mfma_f32_16x16x32_bf16 a[12:15],v[96:99],a[140:143],a[12:15]
v_accvgpr_read_b32 v10,a6
v_accvgpr_read_b32 v11,a7
v_mfma_f32_16x16x32_bf16 a[16:19],v[112:115],a[128:131],a[16:19]
v_cvt_pk_bf16_f32 v8,v8,v9
v_cvt_pk_bf16_f32 v9,v10,v11
v_mfma_f32_16x16x32_bf16 a[16:19],v[116:119],a[144:147],a[16:19]
s_waitcnt lgkmcnt(0)
ds_read_b64_tr_b16 v[96:97],v1 offset:192
ds_read_b64_tr_b16 v[98:99],v1 offset:5312
v_mfma_f32_16x16x32_bf16 a[16:19],v[120:123],a[160:163],a[16:19]
ds_read_b64_tr_b16 v[100:101],v1 offset:10432
v_mfma_f32_16x16x32_bf16 a[16:19],v[124:127],a[176:179],a[16:19]
ds_read_b64_tr_b16 v[102:103],v1 offset:15552
v_mfma_f32_16x16x32_bf16 a[20:23],v[124:127],a[180:183],a[20:23]
ds_read_b64_tr_b16 v[104:105],v1 offset:20672
v_mfma_f32_16x16x32_bf16 a[20:23],v[120:123],a[164:167],a[20:23]
ds_read_b64_tr_b16 v[106:107],v1 offset:25792
ds_read_b64_tr_b16 v[108:109],v1 offset:30912
v_mfma_f32_16x16x32_bf16 a[20:23],v[116:119],a[148:151],a[20:23]
ds_read_b64_tr_b16 v[110:111],v1 offset:36032
v_add_u32 v1,0xfffec000,v1
v_mfma_f32_16x16x32_bf16 a[20:23],v[112:115],a[132:135],a[20:23]
v_accvgpr_read_b32 v20,a16
v_accvgpr_read_b32 v21,a17
v_mfma_f32_16x16x32_bf16 a[24:27],v[112:115],a[136:139],a[24:27]
v_accvgpr_read_b32 v22,a18
v_accvgpr_read_b32 v23,a19
v_mfma_f32_16x16x32_bf16 a[24:27],v[116:119],a[152:155],a[24:27]
v_cvt_pk_bf16_f32 v18,v20,v21
v_cvt_pk_bf16_f32 v19,v22,v23
v_mfma_f32_16x16x32_bf16 a[24:27],v[120:123],a[168:171],a[24:27]
buffer_store_dwordx4 v[16:19],v6,s[36:39],s76 offen sc1
v_mfma_f32_16x16x32_bf16 a[24:27],v[124:127],a[184:187],a[24:27]
v_accvgpr_read_b32 v12,a20
v_accvgpr_read_b32 v13,a21
v_mfma_f32_16x16x32_bf16 a[28:31],v[124:127],a[188:191],a[28:31]
v_accvgpr_read_b32 v14,a22
s_nop 0
L_pd18_%=:
s_cbranch_execz L_trtg2F_%=
v_accvgpr_read_b32 v15,a23
v_mfma_f32_16x16x32_bf16 a[28:31],v[120:123],a[172:175],a[28:31]
v_cvt_pk_bf16_f32 v10,v12,v13
v_cvt_pk_bf16_f32 v11,v14,v15
v_mfma_f32_16x16x32_bf16 a[28:31],v[116:119],a[156:159],a[28:31]
v_accvgpr_read_b32 v16,a8
v_accvgpr_read_b32 v17,a9
v_mfma_f32_16x16x32_bf16 a[28:31],v[112:115],a[140:143],a[28:31]
v_accvgpr_read_b32 v18,a10
v_accvgpr_read_b32 v19,a11
v_mfma_f32_16x16x32_bf16 v[128:131],v[64:67],v[32:35],v[128:131]
v_accvgpr_read_b32 v20,a24
v_accvgpr_read_b32 v21,a25
v_mfma_f32_16x16x32_bf16 v[128:131],v[68:71],v[36:39],v[128:131]
v_accvgpr_read_b32 v22,a26
v_accvgpr_read_b32 v23,a27
v_mfma_f32_16x16x32_bf16 v[128:131],v[72:75],v[40:43],v[128:131]
v_cvt_pk_bf16_f32 v16,v16,v17
v_cvt_pk_bf16_f32 v17,v18,v19
v_mfma_f32_16x16x32_bf16 v[128:131],v[76:79],v[44:47],v[128:131]
v_cvt_pk_bf16_f32 v18,v20,v21
v_cvt_pk_bf16_f32 v19,v22,v23
v_mfma_f32_16x16x32_bf16 v[136:139],v[76:79],v[180:183],v[136:139]
ds_read_b64_tr_b16 v[112:113],v2 offset:224
ds_read_b64_tr_b16 v[114:115],v2 offset:5344
v_mfma_f32_16x16x32_bf16 v[136:139],v[72:75],v[164:167],v[136:139]
buffer_store_dwordx4 v[8:11],v6,s[36:39],s76 offen offset:1024 sc1
v_mfma_f32_16x16x32_bf16 v[136:139],v[68:71],v[148:151],v[136:139]
ds_read_b64_tr_b16 v[116:117],v2 offset:10464
ds_read_b64_tr_b16 v[118:119],v2 offset:15584
v_mfma_f32_16x16x32_bf16 v[136:139],v[64:67],v[132:135],v[136:139]
ds_read_b64_tr_b16 v[120:121],v2 offset:20704
v_accvgpr_read_b32 v8,a12
v_mfma_f32_16x16x32_bf16 v[144:147],v[64:67],v[196:199],v[144:147]
ds_read_b64_tr_b16 v[122:123],v2 offset:25824
v_accvgpr_read_b32 v9,a13
v_mfma_f32_16x16x32_bf16 v[144:147],v[68:71],v[212:215],v[144:147]
ds_read_b64_tr_b16 v[124:125],v2 offset:30944
v_accvgpr_read_b32 v10,a14
v_mfma_f32_16x16x32_bf16 v[144:147],v[72:75],v[228:231],v[144:147]
ds_read_b64_tr_b16 v[126:127],v2 offset:36064
v_accvgpr_read_b32 v11,a15
v_mfma_f32_16x16x32_bf16 v[144:147],v[76:79],v[244:247],v[144:147]
v_accvgpr_read_b32 v12,a28
v_accvgpr_read_b32 v13,a29
v_mfma_f32_16x16x32_bf16 v[152:155],v[76:79],v[188:191],v[152:155]
v_accvgpr_read_b32 v14,a30
v_accvgpr_read_b32 v15,a31
v_mfma_f32_16x16x32_bf16 v[152:155],v[72:75],v[172:175],v[152:155]
v_cvt_pk_bf16_f32 v8,v8,v9
v_cvt_pk_bf16_f32 v9,v10,v11
v_mfma_f32_16x16x32_bf16 v[152:155],v[68:71],v[156:159],v[152:155]
v_cvt_pk_bf16_f32 v10,v12,v13
v_cvt_pk_bf16_f32 v11,v14,v15
v_mfma_f32_16x16x32_bf16 v[152:155],v[64:67],v[140:143],v[152:155]
ds_read_b64_tr_b16 v[64:65],v1 offset:256
ds_read_b64_tr_b16 v[66:67],v1 offset:5376
v_mfma_f32_16x16x32_bf16 v[160:163],v[80:83],v[32:35],v[160:163]
buffer_store_dwordx4 v[16:19],v6,s[36:39],s76 offen offset:2048 sc1
v_mfma_f32_16x16x32_bf16 v[160:163],v[84:87],v[36:39],v[160:163]
ds_read_b64_tr_b16 v[68:69],v1 offset:10496
ds_read_b64_tr_b16 v[70:71],v1 offset:15616
v_mfma_f32_16x16x32_bf16 v[160:163],v[88:91],v[40:43],v[160:163]
ds_read_b64_tr_b16 v[72:73],v1 offset:20736
v_add_u32 v2,0xfffec000,v2
v_mfma_f32_16x16x32_bf16 v[160:163],v[92:95],v[44:47],v[160:163]
ds_read_b64_tr_b16 v[74:75],v1 offset:25856
v_mov_b32 v20,s52
v_mfma_f32_16x16x32_bf16 v[168:171],v[92:95],v[180:183],v[168:171]
ds_read_b64_tr_b16 v[76:77],v1 offset:30976
v_mov_b32 v21,s53
v_mfma_f32_16x16x32_bf16 v[168:171],v[88:91],v[164:167],v[168:171]
ds_read_b64_tr_b16 v[78:79],v1 offset:36096
v_add_u32 v1,0xffff6000,v1
v_mfma_f32_16x16x32_bf16 v[168:171],v[84:87],v[148:151],v[168:171]
v_lshlrev_b32 v22,20,v6
v_mfma_f32_16x16x32_bf16 v[168:171],v[80:83],v[132:135],v[168:171]
v_mfma_f32_16x16x32_bf16 v[176:179],v[80:83],v[196:199],v[176:179]
v_mfma_f32_16x16x32_bf16 v[176:179],v[84:87],v[212:215],v[176:179]
v_mfma_f32_16x16x32_bf16 v[176:179],v[88:91],v[228:231],v[176:179]
)A"
R"A(v_mfma_f32_16x16x32_bf16 v[176:179],v[92:95],v[244:247],v[176:179]
buffer_store_dwordx4 v[8:11],v28,s[36:39],s76 offen offset:3072 sc1
v_mfma_f32_16x16x32_bf16 v[184:187],v[92:95],v[188:191],v[184:187]
ds_read_b64_tr_b16 v[92:93],v2 offset:31008
ds_read_b64_tr_b16 v[94:95],v2 offset:36128
v_mfma_f32_16x16x32_bf16 v[184:187],v[88:91],v[172:175],v[184:187]
ds_read_b64_tr_b16 v[88:89],v2 offset:20768
v_mfma_f32_16x16x32_bf16 v[184:187],v[84:87],v[156:159],v[184:187]
ds_read_b64_tr_b16 v[84:85],v2 offset:10528
v_mfma_f32_16x16x32_bf16 v[184:187],v[80:83],v[140:143],v[184:187]
s_waitcnt lgkmcnt(4)
ds_read_b64_tr_b16 v[80:81],v2 offset:288
v_mfma_f32_16x16x32_bf16 v[128:131],v[96:99],a[128:131],v[128:131]
ds_read_b64_tr_b16 v[82:83],v2 offset:5408
ds_read_b64_tr_b16 v[86:87],v2 offset:15648
v_mfma_f32_16x16x32_bf16 v[128:131],v[100:103],a[144:147],v[128:131]
ds_read_b64_tr_b16 v[90:91],v2 offset:25888
v_add_u32 v2,0xffff6000,v2
v_mfma_f32_16x16x32_bf16 v[128:131],v[104:107],a[160:163],v[128:131]
v_mfma_f32_16x16x32_bf16 v[128:131],v[108:111],a[176:179],v[128:131]
v_mfma_f32_16x16x32_bf16 v[136:139],v[108:111],a[180:183],v[136:139]
v_mfma_f32_16x16x32_bf16 v[136:139],v[104:107],a[164:167],v[136:139]
v_mfma_f32_16x16x32_bf16 v[136:139],v[100:103],a[148:151],v[136:139]
v_mfma_f32_16x16x32_bf16 v[136:139],v[96:99],a[132:135],v[136:139]
v_mfma_f32_16x16x32_bf16 v[144:147],v[96:99],a[136:139],v[144:147]
v_mfma_f32_16x16x32_bf16 v[144:147],v[100:103],a[152:155],v[144:147]
v_mfma_f32_16x16x32_bf16 v[144:147],v[104:107],a[168:171],v[144:147]
v_mfma_f32_16x16x32_bf16 v[144:147],v[108:111],a[184:187],v[144:147]
v_cvt_pk_bf16_f32 v128,v128,v129
v_cvt_pk_bf16_f32 v129,v130,v131
v_mfma_f32_16x16x32_bf16 v[152:155],v[108:111],a[188:191],v[152:155]
ds_read_b64_tr_b16 v[108:109],v1 offset:30976
ds_read_b64_tr_b16 v[110:111],v1 offset:36096
v_mfma_f32_16x16x32_bf16 v[152:155],v[104:107],a[172:175],v[152:155]
v_cvt_pk_bf16_f32 v136,v136,v137
v_cvt_pk_bf16_f32 v137,v138,v139
v_mfma_f32_16x16x32_bf16 v[152:155],v[100:103],a[156:159],v[152:155]
v_cvt_pk_bf16_f32 v144,v144,v145
v_cvt_pk_bf16_f32 v145,v146,v147
v_mfma_f32_16x16x32_bf16 v[152:155],v[96:99],a[140:143],v[152:155]
ds_read_b64_tr_b16 v[96:97],v1 offset:256
ds_read_b64_tr_b16 v[98:99],v1 offset:5376
v_mfma_f32_16x16x32_bf16 v[160:163],v[112:115],a[128:131],v[160:163]
ds_read_b64_tr_b16 v[100:101],v1 offset:10496
v_mfma_f32_16x16x32_bf16 v[160:163],v[116:119],a[144:147],v[160:163]
ds_read_b64_tr_b16 v[102:103],v1 offset:15616
v_mfma_f32_16x16x32_bf16 v[160:163],v[120:123],a[160:163],v[160:163]
ds_read_b64_tr_b16 v[104:105],v1 offset:20736
v_mfma_f32_16x16x32_bf16 v[160:163],v[124:127],a[176:179],v[160:163]
v_cvt_pk_bf16_f32 v152,v152,v153
v_cvt_pk_bf16_f32 v153,v154,v155
v_mfma_f32_16x16x32_bf16 v[168:171],v[124:127],a[180:183],v[168:171]
ds_read_b64_tr_b16 v[106:107],v1 offset:25856
v_add_u32 v1,0x1e000,v1
v_mfma_f32_16x16x32_bf16 v[168:171],v[120:123],a[164:167],v[168:171]
v_mfma_f32_16x16x32_bf16 v[168:171],v[116:119],a[148:151],v[168:171]
v_mfma_f32_16x16x32_bf16 v[168:171],v[112:115],a[132:135],v[168:171]
v_cvt_pk_bf16_f32 v130,v160,v161
v_cvt_pk_bf16_f32 v131,v162,v163
v_mfma_f32_16x16x32_bf16 v[176:179],v[112:115],a[136:139],v[176:179]
buffer_store_dwordx4 v[128:131],v6,s[36:39],s79 offen sc1
v_mfma_f32_16x16x32_bf16 v[176:179],v[116:119],a[152:155],v[176:179]
v_mfma_f32_16x16x32_bf16 v[176:179],v[120:123],a[168:171],v[176:179]
v_mfma_f32_16x16x32_bf16 v[176:179],v[124:127],a[184:187],v[176:179]
v_mfma_f32_16x16x32_bf16 v[184:187],v[124:127],a[188:191],v[184:187]
v_cvt_pk_bf16_f32 v138,v168,v169
v_cvt_pk_bf16_f32 v139,v170,v171
v_mfma_f32_16x16x32_bf16 v[184:187],v[120:123],a[172:175],v[184:187]
buffer_store_dwordx4 v[136:139],v6,s[36:39],s80 offen sc1
v_mfma_f32_16x16x32_bf16 v[184:187],v[116:119],a[156:159],v[184:187]
ds_read_b64_tr_b16 v[116:117],v2 offset:10528
ds_read_b64_tr_b16 v[118:119],v2 offset:15648
v_mfma_f32_16x16x32_bf16 v[184:187],v[112:115],a[140:143],v[184:187]
v_cvt_pk_bf16_f32 v146,v176,v177
v_cvt_pk_bf16_f32 v147,v178,v179
s_waitcnt vmcnt(6)
s_barrier
v_mfma_f32_16x16x32_bf16 v[192:195],v[64:67],a[68:71],v[192:195]
buffer_store_dwordx4 v[144:147],v6,s[36:39],s81 offen sc1
v_mfma_f32_16x16x32_bf16 v[192:195],v[68:71],a[76:79],v[192:195]
ds_read_b64_tr_b16 v[112:113],v2 offset:288
ds_read_b64_tr_b16 v[114:115],v2 offset:5408
v_mfma_f32_16x16x32_bf16 v[192:195],v[72:75],a[84:87],v[192:195]
buffer_store_dwordx2 v[58:59],v60,s[44:47],s18 offen offset:2312 sc1
v_mfma_f32_16x16x32_bf16 v[192:195],v[76:79],a[92:95],v[192:195]
v_cvt_pk_bf16_f32 v154,v184,v185
v_cvt_pk_bf16_f32 v155,v186,v187
v_mfma_f32_16x16x32_bf16 v[200:203],v[76:79],a[124:127],v[200:203]
buffer_store_dwordx4 v[152:155],v28,s[36:39],s82 offen sc1
v_mfma_f32_16x16x32_bf16 v[200:203],v[72:75],a[116:119],v[200:203]
ds_read_b64_tr_b16 v[120:121],v2 offset:20768
ds_read_b64_tr_b16 v[122:123],v2 offset:25888
v_mfma_f32_16x16x32_bf16 v[200:203],v[68:71],a[108:111],v[200:203]
ds_read_b64_tr_b16 v[124:125],v2 offset:31008
v_mfma_f32_16x16x32_bf16 v[200:203],v[64:67],a[100:103],v[200:203]
ds_read_b64_tr_b16 v[126:127],v2 offset:36128
v_add_u32 v2,0x1e000,v2
v_mfma_f32_16x16x32_bf16 v[208:211],v[64:67],a[64:67],v[208:211]
v_mfma_f32_16x16x32_bf16 v[208:211],v[68:71],a[72:75],v[208:211]
v_mfma_f32_16x16x32_bf16 v[208:211],v[72:75],a[80:83],v[208:211]
v_mfma_f32_16x16x32_bf16 v[208:211],v[76:79],a[88:91],v[208:211]
v_mfma_f32_16x16x32_bf16 v[216:219],v[76:79],a[120:123],v[216:219]
ds_read_b64_tr_b16 v[76:77],v1 offset:30976
ds_read_b64_tr_b16 v[78:79],v1 offset:36096
v_mfma_f32_16x16x32_bf16 v[216:219],v[72:75],a[112:115],v[216:219]
ds_read_b64_tr_b16 v[72:73],v1 offset:20736
v_mfma_f32_16x16x32_bf16 v[216:219],v[68:71],a[104:107],v[216:219]
ds_read_b64_tr_b16 v[68:69],v1 offset:10496
v_mfma_f32_16x16x32_bf16 v[216:219],v[64:67],a[96:99],v[216:219]
s_waitcnt lgkmcnt(4)
ds_read_b64_tr_b16 v[64:65],v1 offset:256
v_mfma_f32_16x16x32_bf16 v[224:227],v[80:83],a[68:71],v[224:227]
ds_read_b64_tr_b16 v[66:67],v1 offset:5376
ds_read_b64_tr_b16 v[70:71],v1 offset:15616
v_mfma_f32_16x16x32_bf16 v[224:227],v[84:87],a[76:79],v[224:227]
ds_read_b64_tr_b16 v[74:75],v1 offset:25856
v_mfma_f32_16x16x32_bf16 v[224:227],v[88:91],a[84:87],v[224:227]
v_mfma_f32_16x16x32_bf16 v[224:227],v[92:95],a[92:95],v[224:227]
v_mfma_f32_16x16x32_bf16 v[232:235],v[92:95],a[124:127],v[232:235]
v_mfma_f32_16x16x32_bf16 v[232:235],v[88:91],a[116:119],v[232:235]
v_mfma_f32_16x16x32_bf16 v[232:235],v[84:87],a[108:111],v[232:235]
v_mfma_f32_16x16x32_bf16 v[232:235],v[80:83],a[100:103],v[232:235]
v_mfma_f32_16x16x32_bf16 v[240:243],v[80:83],a[64:67],v[240:243]
v_mfma_f32_16x16x32_bf16 v[240:243],v[84:87],a[72:75],v[240:243]
v_mfma_f32_16x16x32_bf16 v[240:243],v[88:91],a[80:83],v[240:243]
v_mfma_f32_16x16x32_bf16 v[240:243],v[92:95],a[88:91],v[240:243]
v_mfma_f32_16x16x32_bf16 v[248:251],v[92:95],a[120:123],v[248:251]
ds_read_b64_tr_b16 v[92:93],v2 offset:31008
ds_read_b64_tr_b16 v[94:95],v2 offset:36128
v_mfma_f32_16x16x32_bf16 v[248:251],v[88:91],a[112:115],v[248:251]
ds_read_b64_tr_b16 v[88:89],v2 offset:20768
v_mfma_f32_16x16x32_bf16 v[248:251],v[84:87],a[104:107],v[248:251]
ds_read_b64_tr_b16 v[84:85],v2 offset:10528
v_mfma_f32_16x16x32_bf16 v[248:251],v[80:83],a[96:99],v[248:251]
ds_read_b64_tr_b16 v[80:81],v2 offset:288
v_mfma_f32_16x16x32_bf16 v[192:195],v[96:99],v[32:35],v[192:195]
ds_read_b64_tr_b16 v[82:83],v2 offset:5408
ds_read_b64_tr_b16 v[86:87],v2 offset:15648
v_mfma_f32_16x16x32_bf16 v[192:195],v[100:103],v[36:39],v[192:195]
ds_read_b64_tr_b16 v[90:91],v2 offset:25888
v_mfma_f32_16x16x32_bf16 v[192:195],v[104:107],v[40:43],v[192:195]
v_mfma_f32_16x16x32_bf16 v[192:195],v[108:111],v[44:47],v[192:195]
v_mfma_f32_16x16x32_bf16 v[200:203],v[108:111],v[180:183],v[200:203]
v_mfma_f32_16x16x32_bf16 v[200:203],v[104:107],v[164:167],v[200:203]
v_mfma_f32_16x16x32_bf16 v[200:203],v[100:103],v[148:151],v[200:203]
v_mfma_f32_16x16x32_bf16 v[200:203],v[96:99],v[132:135],v[200:203]
v_mfma_f32_16x16x32_bf16 v[208:211],v[96:99],v[196:199],v[208:211]
v_mfma_f32_16x16x32_bf16 v[208:211],v[100:103],v[212:215],v[208:211]
v_mfma_f32_16x16x32_bf16 v[208:211],v[104:107],v[228:231],v[208:211]
v_mfma_f32_16x16x32_bf16 v[208:211],v[108:111],v[244:247],v[208:211]
v_mfma_f32_16x16x32_bf16 v[216:219],v[108:111],v[188:191],v[216:219]
v_mfma_f32_16x16x32_bf16 v[216:219],v[104:107],v[172:175],v[216:219]
v_mfma_f32_16x16x32_bf16 v[216:219],v[100:103],v[156:159],v[216:219]
v_mfma_f32_16x16x32_bf16 v[216:219],v[96:99],v[140:143],v[216:219]
v_mfma_f32_16x16x32_bf16 v[224:227],v[112:115],v[32:35],v[224:227]
v_mfma_f32_16x16x32_bf16 v[224:227],v[116:119],v[36:39],v[224:227]
v_mfma_f32_16x16x32_bf16 v[224:227],v[120:123],v[40:43],v[224:227]
v_mfma_f32_16x16x32_bf16 v[224:227],v[124:127],v[44:47],v[224:227]
v_mfma_f32_16x16x32_bf16 v[232:235],v[124:127],v[180:183],v[232:235]
v_mfma_f32_16x16x32_bf16 v[232:235],v[120:123],v[164:167],v[232:235]
v_mfma_f32_16x16x32_bf16 v[232:235],v[116:119],v[148:151],v[232:235]
v_mfma_f32_16x16x32_bf16 v[232:235],v[112:115],v[132:135],v[232:235]
v_mfma_f32_16x16x32_bf16 v[240:243],v[112:115],v[196:199],v[240:243]
v_mfma_f32_16x16x32_bf16 v[240:243],v[116:119],v[212:215],v[240:243]
v_mfma_f32_16x16x32_bf16 v[240:243],v[120:123],v[228:231],v[240:243]
v_mfma_f32_16x16x32_bf16 v[240:243],v[124:127],v[244:247],v[240:243]
v_mfma_f32_16x16x32_bf16 v[248:251],v[124:127],v[188:191],v[248:251]
v_mfma_f32_16x16x32_bf16 v[248:251],v[120:123],v[172:175],v[248:251]
v_mfma_f32_16x16x32_bf16 v[248:251],v[116:119],v[156:159],v[248:251]
v_mfma_f32_16x16x32_bf16 v[248:251],v[112:115],v[140:143],v[248:251]
s_waitcnt lgkmcnt(0)
v_mfma_f32_16x16x32_bf16 v[192:195],v[64:67],a[128:131],v[192:195]
v_mfma_f32_16x16x32_bf16 v[192:195],v[68:71],a[144:147],v[192:195]
v_mfma_f32_16x16x32_bf16 v[192:195],v[72:75],a[160:163],v[192:195]
v_mfma_f32_16x16x32_bf16 v[192:195],v[76:79],a[176:179],v[192:195]
v_mfma_f32_16x16x32_bf16 v[200:203],v[76:79],a[180:183],v[200:203]
v_mfma_f32_16x16x32_bf16 v[200:203],v[72:75],a[164:167],v[200:203]
v_mfma_f32_16x16x32_bf16 v[200:203],v[68:71],a[148:151],v[200:203]
v_mfma_f32_16x16x32_bf16 v[200:203],v[64:67],a[132:135],v[200:203]
v_mfma_f32_16x16x32_bf16 v[208:211],v[64:67],a[136:139],v[208:211]
v_mfma_f32_16x16x32_bf16 v[208:211],v[68:71],a[152:155],v[208:211]
v_mfma_f32_16x16x32_bf16 v[208:211],v[72:75],a[168:171],v[208:211]
v_mfma_f32_16x16x32_bf16 v[208:211],v[76:79],a[184:187],v[208:211]
v_mfma_f32_16x16x32_bf16 v[216:219],v[76:79],a[188:191],v[216:219]
v_mfma_f32_16x16x32_bf16 v[216:219],v[72:75],a[172:175],v[216:219]
v_mfma_f32_16x16x32_bf16 v[216:219],v[68:71],a[156:159],v[216:219]
v_mfma_f32_16x16x32_bf16 v[216:219],v[64:67],a[140:143],v[216:219]
v_mfma_f32_16x16x32_bf16 v[224:227],v[80:83],a[128:131],v[224:227]
buffer_load_dwordx2 v[62:63],v61,s[44:47],s91 offen offset:2312 sc1
v_mfma_f32_16x16x32_bf16 v[224:227],v[84:87],a[144:147],v[224:227]
v_mfma_f32_16x16x32_bf16 v[224:227],v[88:91],a[160:163],v[224:227]
v_mfma_f32_16x16x32_bf16 v[224:227],v[92:95],a[176:179],v[224:227]
v_mfma_f32_16x16x32_bf16 v[232:235],v[92:95],a[180:183],v[232:235]
v_mfma_f32_16x16x32_bf16 v[232:235],v[88:91],a[164:167],v[232:235]
v_mfma_f32_16x16x32_bf16 v[232:235],v[84:87],a[148:151],v[232:235]
v_mfma_f32_16x16x32_bf16 v[232:235],v[80:83],a[132:135],v[232:235]
v_mfma_f32_16x16x32_bf16 v[240:243],v[80:83],a[136:139],v[240:243]
v_mfma_f32_16x16x32_bf16 v[240:243],v[84:87],a[152:155],v[240:243]
v_mfma_f32_16x16x32_bf16 v[240:243],v[88:91],a[168:171],v[240:243]
v_mfma_f32_16x16x32_bf16 v[240:243],v[92:95],a[184:187],v[240:243]
v_mfma_f32_16x16x32_bf16 v[248:251],v[92:95],a[188:191],v[248:251]
v_mfma_f32_16x16x32_bf16 v[248:251],v[88:91],a[172:175],v[248:251]
v_mfma_f32_16x16x32_bf16 v[248:251],v[84:87],a[156:159],v[248:251]
v_mfma_f32_16x16x32_bf16 v[248:251],v[80:83],a[140:143],v[248:251]
s_waitcnt vmcnt(1)
s_barrier
buffer_store_dwordx2 v[20:21],v22,s[44:47],s18 offen offset:2304 sc1
s_waitcnt vmcnt(1)
v_cmp_ne_u32 s[56:57],s52,v62
)A"
R"A(v_cmp_ne_u32 s[58:59],s53,v63
s_nop 0
s_or_b64 s[56:57],s[56:57],s[58:59]
s_cbranch_scc1 L_fr4_%=
L_fo4_%=:
buffer_load_dwordx4 v[64:67],v6,s[36:39],s66 offen sc1
buffer_load_dwordx4 v[68:71],v6,s[36:39],s67 offen sc1
buffer_load_dwordx4 v[72:75],v6,s[36:39],s66 offen offset:1024 sc1
buffer_load_dwordx4 v[76:79],v6,s[36:39],s67 offen offset:1024 sc1
buffer_load_dwordx4 v[80:83],v6,s[36:39],s66 offen offset:2048 sc1
buffer_load_dwordx4 v[84:87],v6,s[36:39],s67 offen offset:2048 sc1
buffer_load_dwordx4 v[88:91],v28,s[36:39],s66 offen offset:3072 sc1
buffer_load_dwordx4 v[92:95],v28,s[36:39],s67 offen offset:3072 sc1
buffer_load_dwordx2 v[4:5],v3,s[44:47],s91 offen offset:2304 sc1
s_waitcnt vmcnt(8)
v_mfma_f32_16x16x32_bf16 v[192:195],v[48:51],v[64:67],v[192:195]
s_waitcnt vmcnt(7)
v_mfma_f32_16x16x32_bf16 v[192:195],v[48:51],v[68:71],v[192:195]
v_mfma_f32_16x16x32_bf16 v[224:227],v[52:55],v[64:67],v[224:227]
v_mfma_f32_16x16x32_bf16 v[224:227],v[52:55],v[68:71],v[224:227]
s_waitcnt vmcnt(6)
v_mfma_f32_16x16x32_bf16 v[200:203],v[48:51],v[72:75],v[200:203]
s_waitcnt vmcnt(5)
v_mfma_f32_16x16x32_bf16 v[200:203],v[48:51],v[76:79],v[200:203]
v_mfma_f32_16x16x32_bf16 v[232:235],v[52:55],v[72:75],v[232:235]
v_mfma_f32_16x16x32_bf16 v[232:235],v[52:55],v[76:79],v[232:235]
s_waitcnt vmcnt(4)
v_mfma_f32_16x16x32_bf16 v[208:211],v[48:51],v[80:83],v[208:211]
s_waitcnt vmcnt(3)
v_mfma_f32_16x16x32_bf16 v[208:211],v[48:51],v[84:87],v[208:211]
v_mfma_f32_16x16x32_bf16 v[240:243],v[52:55],v[80:83],v[240:243]
v_mfma_f32_16x16x32_bf16 v[240:243],v[52:55],v[84:87],v[240:243]
s_waitcnt vmcnt(2)
v_mfma_f32_16x16x32_bf16 v[216:219],v[48:51],v[88:91],v[216:219]
s_waitcnt vmcnt(1)
v_mfma_f32_16x16x32_bf16 v[216:219],v[48:51],v[92:95],v[216:219]
v_mfma_f32_16x16x32_bf16 v[248:251],v[52:55],v[88:91],v[248:251]
v_mfma_f32_16x16x32_bf16 v[248:251],v[52:55],v[92:95],v[248:251]
s_waitcnt vmcnt(0)
v_cmp_ne_u32 s[56:57],s52,v4
v_cmp_ne_u32 s[58:59],s53,v5
s_nop 0
s_or_b64 s[56:57],s[56:57],s[58:59]
s_cbranch_scc1 L_fr5_%=
L_fo5_%=:
buffer_load_dwordx4 v[96:99],v6,s[36:39],s68 offen sc1
buffer_load_dwordx4 v[100:103],v6,s[36:39],s68 offen offset:1024 sc1
buffer_load_dwordx4 v[104:107],v6,s[36:39],s68 offen offset:2048 sc1
buffer_load_dwordx4 v[108:111],v6,s[36:39],s68 offen offset:3072 sc1
buffer_load_dwordx4 v[112:115],v6,s[36:39],s65 offen sc1
buffer_load_dwordx4 v[116:119],v6,s[36:39],s65 offen offset:1024 sc1
buffer_load_dwordx4 v[120:123],v6,s[36:39],s65 offen offset:2048 sc1
buffer_load_dwordx4 v[124:127],v28,s[36:39],s65 offen offset:3072 sc1
s_waitcnt vmcnt(7)
v_mfma_f32_16x16x32_bf16 v[128:131],v[48:51],v[96:99],0
s_waitcnt vmcnt(6)
v_mfma_f32_16x16x32_bf16 v[128:131],v[48:51],v[100:103],v[128:131]
s_waitcnt vmcnt(5)
v_mfma_f32_16x16x32_bf16 v[128:131],v[48:51],v[104:107],v[128:131]
s_waitcnt vmcnt(4)
v_mfma_f32_16x16x32_bf16 v[128:131],v[48:51],v[108:111],v[128:131]
v_mfma_f32_16x16x32_bf16 v[160:163],v[52:55],v[96:99],0
v_mfma_f32_16x16x32_bf16 v[160:163],v[52:55],v[100:103],v[160:163]
v_mfma_f32_16x16x32_bf16 v[160:163],v[52:55],v[104:107],v[160:163]
v_mfma_f32_16x16x32_bf16 v[160:163],v[52:55],v[108:111],v[160:163]
s_waitcnt vmcnt(3)
v_mfma_f32_16x16x32_bf16 v[192:195],v[48:51],v[112:115],v[192:195]
v_mfma_f32_16x16x32_bf16 v[224:227],v[52:55],v[112:115],v[224:227]
s_waitcnt vmcnt(2)
v_mfma_f32_16x16x32_bf16 v[200:203],v[48:51],v[116:119],v[200:203]
v_mfma_f32_16x16x32_bf16 v[232:235],v[52:55],v[116:119],v[232:235]
s_waitcnt vmcnt(1)
v_cvt_pk_bf16_f32 v128,v128,v129
v_cvt_pk_bf16_f32 v129,v130,v131
v_mfma_f32_16x16x32_bf16 v[208:211],v[48:51],v[120:123],v[208:211]
v_mfma_f32_16x16x32_bf16 v[240:243],v[52:55],v[120:123],v[240:243]
s_waitcnt vmcnt(0)
v_cvt_pk_bf16_f32 v130,v160,v161
v_cvt_pk_bf16_f32 v131,v162,v163
v_mfma_f32_16x16x32_bf16 v[216:219],v[48:51],v[124:127],v[216:219]
v_cvt_pk_bf16_f32 v192,v192,v193
v_cvt_pk_bf16_f32 v193,v194,v195
v_cvt_pk_bf16_f32 v194,v224,v225
v_cvt_pk_bf16_f32 v195,v226,v227
v_cvt_pk_bf16_f32 v200,v200,v201
v_cvt_pk_bf16_f32 v201,v202,v203
v_mfma_f32_16x16x32_bf16 v[248:251],v[52:55],v[124:127],v[248:251]
v_permlane16_swap_b32 v128,v130
v_permlane16_swap_b32 v129,v131
v_cvt_pk_bf16_f32 v202,v232,v233
v_cvt_pk_bf16_f32 v203,v234,v235
v_cvt_pk_bf16_f32 v208,v208,v209
v_cvt_pk_bf16_f32 v209,v210,v211
v_cvt_pk_bf16_f32 v210,v240,v241
v_cvt_pk_bf16_f32 v211,v242,v243
buffer_store_dwordx4 v[128:131],v56,s[24:27],s83 offen offset:256 sc1 nt
v_permlane16_swap_b32 v192,v194
v_permlane16_swap_b32 v193,v195
buffer_store_dwordx4 v[192:195],v56,s[24:27],s87 offen sc1 nt
v_permlane16_swap_b32 v200,v202
v_permlane16_swap_b32 v201,v203
v_cvt_pk_bf16_f32 v216,v216,v217
v_cvt_pk_bf16_f32 v217,v218,v219
v_cvt_pk_bf16_f32 v218,v248,v249
v_cvt_pk_bf16_f32 v219,v250,v251
buffer_store_dwordx4 v[200:203],v56,s[24:27],s88 offen sc1 nt
v_permlane16_swap_b32 v208,v210
v_permlane16_swap_b32 v209,v211
buffer_store_dwordx4 v[208:211],v56,s[24:27],s89 offen sc1 nt
v_permlane16_swap_b32 v216,v218
v_permlane16_swap_b32 v217,v219
buffer_store_dwordx4 v[216:219],v56,s[24:27],s90 offen sc1 nt
s_lshl_b32 s69,s62,20
s_add_u32 s69,s69,s18
buffer_store_dword v20,v22,s[44:47],s69 offen sc1
L_geF_%=:
s_branch L_end_%=
.p2align 12
L_pd19_%=:
s_cbranch_execz L_trt1_%=
L_GS_%=:
L_gbS_%=:
s_waitcnt lgkmcnt(14)
v_mfma_f32_16x16x32_bf16 a[192:195],v[64:67],a[68:71],0
buffer_store_dwordx4 v[32:35],v6,s[40:43],s79 offen sc1
s_waitcnt lgkmcnt(12)
v_mfma_f32_16x16x32_bf16 a[192:195],v[68:71],a[76:79],a[192:195]
s_lshl_b32 s91,s63,8
v_bfe_u32 v3,v6,4,2
v_min_u32 v3,2,v3
s_waitcnt lgkmcnt(10)
v_mfma_f32_16x16x32_bf16 a[192:195],v[72:75],a[84:87],a[192:195]
s_add_u32 s87,s64,1
v_add3_u32 v3,v3,s64,1
v_and_b32 v3,3,v3
s_waitcnt lgkmcnt(8)
v_mfma_f32_16x16x32_bf16 a[192:195],v[76:79],a[92:95],a[192:195]
s_and_b32 s87,s87,3
ds_read_b64_tr_b16 v[96:97],v1 offset:64
v_lshlrev_b32 v3,6,v3
v_mfma_f32_16x16x32_bf16 a[196:199],v[76:79],a[124:127],0
s_lshl_b32 s87,s87,1
ds_read_b64_tr_b16 v[98:99],v1 offset:5184
ds_read_b64_tr_b16 v[100:101],v1 offset:10304
v_mfma_f32_16x16x32_bf16 a[196:199],v[72:75],a[116:119],a[196:199]
s_lshl_b32 s87,s87,16
ds_read_b64_tr_b16 v[102:103],v1 offset:15424
v_lshlrev_b32 v29,10,v6
v_mfma_f32_16x16x32_bf16 a[196:199],v[68:71],a[108:111],a[196:199]
s_add_u32 s88,s87,4096
ds_read_b64_tr_b16 v[104:105],v1 offset:20544
v_accvgpr_read_b32 v16,a24
v_mfma_f32_16x16x32_bf16 a[196:199],v[64:67],a[100:103],a[196:199]
s_add_u32 s89,s87,8192
ds_read_b64_tr_b16 v[106:107],v1 offset:25664
v_accvgpr_read_b32 v17,a25
v_mfma_f32_16x16x32_bf16 a[200:203],v[64:67],a[64:67],0
s_add_u32 s90,s87,12288
ds_read_b64_tr_b16 v[108:109],v1 offset:30784
ds_read_b64_tr_b16 v[110:111],v1 offset:35904
v_mfma_f32_16x16x32_bf16 a[200:203],v[68:71],a[72:75],a[200:203]
s_add_u32 s20,s64,2
ds_read_b64_tr_b16 v[112:113],v2 offset:96
v_accvgpr_read_b32 v18,a26
v_mfma_f32_16x16x32_bf16 a[200:203],v[72:75],a[80:83],a[200:203]
s_and_b32 s20,s20,3
ds_read_b64_tr_b16 v[114:115],v2 offset:5216
v_accvgpr_read_b32 v19,a27
v_mfma_f32_16x16x32_bf16 a[200:203],v[76:79],a[88:91],a[200:203]
s_lshl_b32 s20,s20,1
ds_read_b64_tr_b16 v[116:117],v2 offset:10336
v_accvgpr_read_b32 v20,a28
v_mfma_f32_16x16x32_bf16 a[204:207],v[76:79],a[120:123],0
s_lshl_b32 s20,s20,16
ds_read_b64_tr_b16 v[118:119],v2 offset:15456
ds_read_b64_tr_b16 v[120:121],v2 offset:20576
v_mfma_f32_16x16x32_bf16 a[204:207],v[72:75],a[112:115],a[204:207]
s_add_u32 s21,s20,4096
ds_read_b64_tr_b16 v[122:123],v2 offset:25696
v_accvgpr_read_b32 v21,a29
v_mfma_f32_16x16x32_bf16 a[204:207],v[68:71],a[104:107],a[204:207]
s_add_u32 s22,s20,8192
ds_read_b64_tr_b16 v[124:125],v2 offset:30816
v_accvgpr_read_b32 v22,a30
v_mfma_f32_16x16x32_bf16 a[204:207],v[64:67],a[96:99],a[204:207]
buffer_store_dwordx4 v[36:39],v6,s[40:43],s80 offen sc1
s_waitcnt lgkmcnt(9)
v_mfma_f32_16x16x32_bf16 a[208:211],v[80:83],a[68:71],0
s_add_u32 s23,s20,12288
ds_read_b64_tr_b16 v[126:127],v2 offset:35936
ds_read_b64_tr_b16 v[64:65],v1 offset:128
v_mfma_f32_16x16x32_bf16 a[208:211],v[84:87],a[76:79],a[208:211]
s_add_u32 m0,s86,122880
ds_read_b64_tr_b16 v[66:67],v1 offset:5248
v_accvgpr_read_b32 v23,a31
v_mfma_f32_16x16x32_bf16 a[208:211],v[88:91],a[84:87],a[208:211]
s_add_u32 s83,s64,3
ds_read_b64_tr_b16 v[68:69],v1 offset:10368
v_mul_f32 v16,v16,v49
v_mfma_f32_16x16x32_bf16 a[208:211],v[92:95],a[92:95],a[208:211]
s_and_b32 s83,s83,3
ds_read_b64_tr_b16 v[70:71],v1 offset:15488
v_mul_f32 v17,v17,v49
v_mfma_f32_16x16x32_bf16 a[212:215],v[92:95],a[124:127],0
s_waitcnt vmcnt(21)
buffer_load_dwordx2 v[4:5],v3,s[44:47],s91 offen offset:256 sc1
v_mfma_f32_16x16x32_bf16 a[212:215],v[88:91],a[116:119],a[212:215]
s_lshl_b32 s83,s83,1
ds_read_b64_tr_b16 v[72:73],v1 offset:20608
ds_read_b64_tr_b16 v[74:75],v1 offset:25728
v_mfma_f32_16x16x32_bf16 a[212:215],v[84:87],a[108:111],a[212:215]
s_mul_i32 s83,s83,163840
ds_read_b64_tr_b16 v[76:77],v1 offset:30848
v_mul_f32 v18,v18,v49
v_mfma_f32_16x16x32_bf16 a[212:215],v[80:83],a[100:103],a[212:215]
s_add_u32 s83,s83,s85
ds_read_b64_tr_b16 v[78:79],v1 offset:35968
v_mul_f32 v19,v19,v49
v_mfma_f32_16x16x32_bf16 a[216:219],v[80:83],a[64:67],0
s_add_u32 s84,s83,20480
v_mul_f32 v20,v20,v49
v_mul_f32 v21,v21,v49
v_mfma_f32_16x16x32_bf16 a[216:219],v[84:87],a[72:75],a[216:219]
s_lshl_b32 s66,s61,3
v_mul_f32 v22,v22,v49
v_mul_f32 v23,v23,v49
v_mfma_f32_16x16x32_bf16 a[216:219],v[88:91],a[80:83],a[216:219]
s_add_u32 s66,s66,s64
v_cvt_pk_bf16_f32 v44,v16,v17
v_cvt_pk_bf16_f32 v45,v18,v19
v_mfma_f32_16x16x32_bf16 a[216:219],v[92:95],a[88:91],a[216:219]
s_mul_i32 s66,s66,262144
v_cvt_pk_bf16_f32 v46,v20,v21
v_cvt_pk_bf16_f32 v47,v22,v23
v_mfma_f32_16x16x32_bf16 a[220:223],v[92:95],a[120:123],0
s_add_u32 s66,s66,100663296
ds_read_b64_tr_b16 v[92:93],v2 offset:30880
ds_read_b64_tr_b16 v[94:95],v2 offset:36000
v_mfma_f32_16x16x32_bf16 a[220:223],v[88:91],a[112:115],a[220:223]
s_mov_b32 s38,262144
ds_read_b64_tr_b16 v[88:89],v2 offset:20640
v_mul_f32 v132,v132,v53
v_mfma_f32_16x16x32_bf16 a[220:223],v[84:87],a[104:107],a[220:223]
buffer_store_dwordx4 v[40:43],v6,s[40:43],s81 offen sc1
v_mfma_f32_16x16x32_bf16 a[220:223],v[80:83],a[96:99],a[220:223]
s_mov_b32 s39,0x20000
ds_read_b64_tr_b16 v[80:81],v2 offset:160
ds_read_b64_tr_b16 v[82:83],v2 offset:5280
v_mfma_f32_16x16x32_bf16 a[224:227],v[96:99],a[68:71],0
s_add_u32 s74,s63,1
ds_read_b64_tr_b16 v[84:85],v2 offset:10400
v_mul_f32 v133,v133,v53
v_mfma_f32_16x16x32_bf16 a[224:227],v[100:103],a[76:79],a[224:227]
s_and_b32 s74,s74,3
ds_read_b64_tr_b16 v[86:87],v2 offset:15520
v_mul_f32 v134,v134,v53
v_mfma_f32_16x16x32_bf16 a[224:227],v[104:107],a[84:87],a[224:227]
s_lshl_b32 s74,s74,2
ds_read_b64_tr_b16 v[90:91],v2 offset:25760
v_mul_f32 v135,v135,v53
s_waitcnt lgkmcnt(8)
v_mfma_f32_16x16x32_bf16 a[224:227],v[108:111],a[92:95],a[224:227]
s_add_u32 s74,s74,s60
v_mul_f32 v140,v140,v53
v_mul_f32 v141,v141,v53
v_mfma_f32_16x16x32_bf16 a[228:231],v[108:111],a[124:127],0
s_mul_i32 s74,s74,16384
v_mul_f32 v142,v142,v53
v_mul_f32 v143,v143,v53
v_mfma_f32_16x16x32_bf16 a[228:231],v[104:107],a[116:119],a[228:231]
s_add_u32 s74,s74,8192
v_cvt_pk_bf16_f32 v132,v132,v133
v_cvt_pk_bf16_f32 v133,v134,v135
v_mfma_f32_16x16x32_bf16 a[228:231],v[100:103],a[108:111],a[228:231]
s_add_u32 s75,s63,2
v_cvt_pk_bf16_f32 v134,v140,v141
v_cvt_pk_bf16_f32 v135,v142,v143
v_mfma_f32_16x16x32_bf16 a[228:231],v[96:99],a[100:103],a[228:231]
s_and_b32 s75,s75,3
v_mul_f32 v148,v148,v53
v_mul_f32 v149,v149,v53
v_mfma_f32_16x16x32_bf16 a[232:235],v[96:99],a[64:67],0
s_lshl_b32 s75,s75,2
v_mul_f32 v150,v150,v53
v_mul_f32 v151,v151,v53
v_mfma_f32_16x16x32_bf16 a[232:235],v[100:103],a[72:75],a[232:235]
s_add_u32 s75,s75,s60
v_mul_f32 v156,v156,v53
v_mul_f32 v157,v157,v53
v_mfma_f32_16x16x32_bf16 a[232:235],v[104:107],a[80:83],a[232:235]
s_mul_i32 s75,s75,16384
v_mul_f32 v158,v158,v53
v_mul_f32 v159,v159,v53
v_mfma_f32_16x16x32_bf16 a[232:235],v[108:111],a[88:91],a[232:235]
s_add_u32 s75,s75,4096
v_cvt_pk_bf16_f32 v148,v148,v149
v_cvt_pk_bf16_f32 v149,v150,v151
v_mfma_f32_16x16x32_bf16 a[236:239],v[108:111],a[120:123],0
)A"
R"A(s_add_u32 s76,s63,3
ds_read_b64_tr_b16 v[108:109],v1 offset:30912
ds_read_b64_tr_b16 v[110:111],v1 offset:36032
v_mfma_f32_16x16x32_bf16 a[236:239],v[104:107],a[112:115],a[236:239]
buffer_store_dwordx4 v[44:47],v6,s[40:43],s82 offen sc1
v_mfma_f32_16x16x32_bf16 a[236:239],v[100:103],a[104:107],a[236:239]
s_and_b32 s76,s76,3
ds_read_b64_tr_b16 v[100:101],v1 offset:10432
ds_read_b64_tr_b16 v[102:103],v1 offset:15552
v_mfma_f32_16x16x32_bf16 a[236:239],v[96:99],a[96:99],a[236:239]
s_lshl_b32 s76,s76,2
ds_read_b64_tr_b16 v[96:97],v1 offset:192
v_cvt_pk_bf16_f32 v150,v156,v157
v_mfma_f32_16x16x32_bf16 a[240:243],v[112:115],a[68:71],0
s_add_u32 s76,s76,s60
ds_read_b64_tr_b16 v[98:99],v1 offset:5312
v_cvt_pk_bf16_f32 v151,v158,v159
v_mfma_f32_16x16x32_bf16 a[240:243],v[116:119],a[76:79],a[240:243]
s_mul_i32 s76,s76,16384
ds_read_b64_tr_b16 v[104:105],v1 offset:20672
v_mul_f32 v164,v164,v53
v_mfma_f32_16x16x32_bf16 a[240:243],v[120:123],a[84:87],a[240:243]
s_add_u32 s76,s76,0
ds_read_b64_tr_b16 v[106:107],v1 offset:25792
v_mul_f32 v165,v165,v53
v_mfma_f32_16x16x32_bf16 a[240:243],v[124:127],a[92:95],a[240:243]
s_lshl_b32 s67,s63,10
v_mul_f32 v166,v166,v53
v_mul_f32 v167,v167,v53
v_mfma_f32_16x16x32_bf16 a[244:247],v[124:127],a[124:127],0
s_mov_b32 s26,307200
v_mul_f32 v172,v172,v53
v_mul_f32 v173,v173,v53
v_mfma_f32_16x16x32_bf16 a[244:247],v[120:123],a[116:119],a[244:247]
s_mov_b32 s27,0x20000
v_mul_f32 v174,v174,v53
v_mul_f32 v175,v175,v53
v_mfma_f32_16x16x32_bf16 a[244:247],v[116:119],a[108:111],a[244:247]
s_lshl_b32 s77,s63,2
v_cvt_pk_bf16_f32 v164,v164,v165
v_cvt_pk_bf16_f32 v165,v166,v167
v_mfma_f32_16x16x32_bf16 a[244:247],v[112:115],a[100:103],a[244:247]
s_add_u32 s77,s77,s60
v_cvt_pk_bf16_f32 v166,v172,v173
v_cvt_pk_bf16_f32 v167,v174,v175
v_mfma_f32_16x16x32_bf16 a[248:251],v[112:115],a[64:67],0
s_mul_i32 s77,s77,16384
v_mul_f32 v180,v180,v53
v_mul_f32 v181,v181,v53
v_mfma_f32_16x16x32_bf16 a[248:251],v[116:119],a[72:75],a[248:251]
s_add_u32 s65,s77,0
v_mul_f32 v182,v182,v53
v_mul_f32 v183,v183,v53
v_mfma_f32_16x16x32_bf16 a[248:251],v[120:123],a[80:83],a[248:251]
s_add_u32 s68,s77,12288
v_mul_f32 v188,v188,v53
v_mul_f32 v189,v189,v53
v_mfma_f32_16x16x32_bf16 a[248:251],v[124:127],a[88:91],a[248:251]
v_mul_f32 v190,v190,v53
v_mul_f32 v191,v191,v53
v_mfma_f32_16x16x32_bf16 a[252:255],v[124:127],a[120:123],0
buffer_load_dword v30,v29,s[8:11],s7 offen
v_mfma_f32_16x16x32_bf16 a[252:255],v[120:123],a[112:115],a[252:255]
ds_read_b64_tr_b16 v[120:121],v2 offset:20704
ds_read_b64_tr_b16 v[122:123],v2 offset:25824
v_mfma_f32_16x16x32_bf16 a[252:255],v[116:119],a[104:107],a[252:255]
buffer_store_dwordx4 v[132:135],v6,s[40:43],s79 offen offset:1024 sc1
v_mfma_f32_16x16x32_bf16 a[252:255],v[112:115],a[96:99],a[252:255]
ds_read_b64_tr_b16 v[112:113],v2 offset:224
ds_read_b64_tr_b16 v[114:115],v2 offset:5344
v_mfma_f32_16x16x32_bf16 a[0:3],v[64:67],a[68:71],0
ds_read_b64_tr_b16 v[116:117],v2 offset:10464
v_cvt_pk_bf16_f32 v180,v180,v181
v_mfma_f32_16x16x32_bf16 a[0:3],v[68:71],a[76:79],a[0:3]
ds_read_b64_tr_b16 v[118:119],v2 offset:15584
v_cvt_pk_bf16_f32 v181,v182,v183
v_mfma_f32_16x16x32_bf16 a[0:3],v[72:75],a[84:87],a[0:3]
ds_read_b64_tr_b16 v[124:125],v2 offset:30944
v_cvt_pk_bf16_f32 v182,v188,v189
v_mfma_f32_16x16x32_bf16 a[0:3],v[76:79],a[92:95],a[0:3]
ds_read_b64_tr_b16 v[126:127],v2 offset:36064
v_cvt_pk_bf16_f32 v183,v190,v191
v_mfma_f32_16x16x32_bf16 a[4:7],v[76:79],a[124:127],0
v_mul_f32 v196,v196,v57
v_mul_f32 v197,v197,v57
v_mfma_f32_16x16x32_bf16 a[4:7],v[72:75],a[116:119],a[4:7]
v_mul_f32 v198,v198,v57
v_mul_f32 v199,v199,v57
v_mfma_f32_16x16x32_bf16 a[4:7],v[68:71],a[108:111],a[4:7]
v_mul_f32 v204,v204,v57
v_mul_f32 v205,v205,v57
v_mfma_f32_16x16x32_bf16 a[4:7],v[64:67],a[100:103],a[4:7]
v_mul_f32 v206,v206,v57
v_mul_f32 v207,v207,v57
v_mfma_f32_16x16x32_bf16 a[8:11],v[64:67],a[64:67],0
v_cvt_pk_bf16_f32 v196,v196,v197
v_cvt_pk_bf16_f32 v197,v198,v199
v_mfma_f32_16x16x32_bf16 a[8:11],v[68:71],a[72:75],a[8:11]
v_cvt_pk_bf16_f32 v198,v204,v205
v_cvt_pk_bf16_f32 v199,v206,v207
v_mfma_f32_16x16x32_bf16 a[8:11],v[72:75],a[80:83],a[8:11]
v_mul_f32 v212,v212,v57
v_mul_f32 v213,v213,v57
v_mfma_f32_16x16x32_bf16 a[8:11],v[76:79],a[88:91],a[8:11]
v_mul_f32 v214,v214,v57
v_mul_f32 v215,v215,v57
v_mfma_f32_16x16x32_bf16 a[12:15],v[76:79],a[120:123],0
ds_read_b64_tr_b16 v[76:77],v1 offset:30976
ds_read_b64_tr_b16 v[78:79],v1 offset:36096
v_mfma_f32_16x16x32_bf16 a[12:15],v[72:75],a[112:115],a[12:15]
buffer_store_dwordx4 v[148:151],v6,s[40:43],s80 offen offset:1024 sc1
v_mfma_f32_16x16x32_bf16 a[12:15],v[68:71],a[104:107],a[12:15]
ds_read_b64_tr_b16 v[68:69],v1 offset:10496
ds_read_b64_tr_b16 v[70:71],v1 offset:15616
v_mfma_f32_16x16x32_bf16 a[12:15],v[64:67],a[96:99],a[12:15]
s_waitcnt lgkmcnt(4)
ds_read_b64_tr_b16 v[64:65],v1 offset:256
v_mul_f32 v220,v220,v57
v_mfma_f32_16x16x32_bf16 a[16:19],v[80:83],a[68:71],0
ds_read_b64_tr_b16 v[66:67],v1 offset:5376
v_mul_f32 v221,v221,v57
v_mfma_f32_16x16x32_bf16 a[16:19],v[84:87],a[76:79],a[16:19]
ds_read_b64_tr_b16 v[72:73],v1 offset:20736
v_mul_f32 v222,v222,v57
v_mfma_f32_16x16x32_bf16 a[16:19],v[88:91],a[84:87],a[16:19]
ds_read_b64_tr_b16 v[74:75],v1 offset:25856
v_add_u32 v1,0xffff6000,v1
v_mfma_f32_16x16x32_bf16 a[16:19],v[92:95],a[92:95],a[16:19]
v_mul_f32 v223,v223,v57
v_cvt_pk_bf16_f32 v212,v212,v213
v_mfma_f32_16x16x32_bf16 a[20:23],v[92:95],a[124:127],0
v_cvt_pk_bf16_f32 v213,v214,v215
v_cvt_pk_bf16_f32 v214,v220,v221
v_mfma_f32_16x16x32_bf16 a[20:23],v[88:91],a[116:119],a[20:23]
v_cvt_pk_bf16_f32 v215,v222,v223
v_mul_f32 v228,v228,v57
v_mfma_f32_16x16x32_bf16 a[20:23],v[84:87],a[108:111],a[20:23]
v_mul_f32 v229,v229,v57
v_mul_f32 v230,v230,v57
v_mfma_f32_16x16x32_bf16 a[20:23],v[80:83],a[100:103],a[20:23]
v_mul_f32 v231,v231,v57
v_mul_f32 v236,v236,v57
v_mfma_f32_16x16x32_bf16 a[24:27],v[80:83],a[64:67],0
v_mul_f32 v237,v237,v57
v_mul_f32 v238,v238,v57
v_mfma_f32_16x16x32_bf16 a[24:27],v[84:87],a[72:75],a[24:27]
v_mul_f32 v239,v239,v57
v_cvt_pk_bf16_f32 v228,v228,v229
v_mfma_f32_16x16x32_bf16 a[24:27],v[88:91],a[80:83],a[24:27]
s_waitcnt vmcnt(5)
v_cmp_ne_u32 s[56:57],s52,v4
v_cmp_ne_u32 s[58:59],s53,v5
v_mfma_f32_16x16x32_bf16 a[24:27],v[92:95],a[88:91],a[24:27]
s_or_b64 s[56:57],s[56:57],s[58:59]
s_cbranch_scc1 L_fr6_%=
L_fo6_%=:
v_cvt_pk_bf16_f32 v229,v230,v231
v_mfma_f32_16x16x32_bf16 a[28:31],v[92:95],a[120:123],0
buffer_store_dwordx4 v[164:167],v6,s[40:43],s81 offen offset:1024 sc1
v_mfma_f32_16x16x32_bf16 a[28:31],v[88:91],a[112:115],a[28:31]
s_lshl_b32 s91,s63,8
ds_read_b64_tr_b16 v[88:89],v2 offset:20768
ds_read_b64_tr_b16 v[90:91],v2 offset:25888
v_mfma_f32_16x16x32_bf16 a[28:31],v[84:87],a[104:107],a[28:31]
buffer_load_dwordx4 a[128:131],v6,s[28:31],s87 offen sc1
v_mfma_f32_16x16x32_bf16 a[28:31],v[80:83],a[96:99],a[28:31]
ds_read_b64_tr_b16 v[80:81],v2 offset:288
ds_read_b64_tr_b16 v[82:83],v2 offset:5408
v_mfma_f32_16x16x32_bf16 v[128:131],v[96:99],a[68:71],0
ds_read_b64_tr_b16 v[84:85],v2 offset:10528
v_cvt_pk_bf16_f32 v230,v236,v237
v_mfma_f32_16x16x32_bf16 v[128:131],v[100:103],a[76:79],v[128:131]
ds_read_b64_tr_b16 v[86:87],v2 offset:15648
v_cvt_pk_bf16_f32 v231,v238,v239
v_mfma_f32_16x16x32_bf16 v[128:131],v[104:107],a[84:87],v[128:131]
ds_read_b64_tr_b16 v[92:93],v2 offset:31008
v_mul_f32 v244,v244,v57
v_mfma_f32_16x16x32_bf16 v[128:131],v[108:111],a[92:95],v[128:131]
ds_read_b64_tr_b16 v[94:95],v2 offset:36128
v_add_u32 v2,0xffff6000,v2
v_mfma_f32_16x16x32_bf16 v[136:139],v[108:111],a[124:127],0
v_mul_f32 v245,v245,v57
v_mul_f32 v246,v246,v57
v_mfma_f32_16x16x32_bf16 v[136:139],v[104:107],a[116:119],v[136:139]
v_mul_f32 v247,v247,v57
v_mul_f32 v252,v252,v57
v_mfma_f32_16x16x32_bf16 v[136:139],v[100:103],a[108:111],v[136:139]
v_mul_f32 v253,v253,v57
v_mul_f32 v254,v254,v57
v_mfma_f32_16x16x32_bf16 v[136:139],v[96:99],a[100:103],v[136:139]
v_mul_f32 v255,v255,v57
v_cvt_pk_bf16_f32 v244,v244,v245
v_mfma_f32_16x16x32_bf16 v[144:147],v[96:99],a[64:67],0
v_cvt_pk_bf16_f32 v245,v246,v247
v_cvt_pk_bf16_f32 v246,v252,v253
v_mfma_f32_16x16x32_bf16 v[144:147],v[100:103],a[72:75],v[144:147]
buffer_load_dwordx4 a[144:147],v6,s[28:31],s88 offen sc1
v_mfma_f32_16x16x32_bf16 v[144:147],v[104:107],a[80:83],v[144:147]
v_cvt_pk_bf16_f32 v247,v254,v255
v_accvgpr_read_b32 v16,a40
v_mfma_f32_16x16x32_bf16 v[144:147],v[108:111],a[88:91],v[144:147]
buffer_store_dwordx4 v[180:183],v6,s[40:43],s82 offen offset:1024 sc1
v_mfma_f32_16x16x32_bf16 v[152:155],v[108:111],a[120:123],0
ds_read_b64_tr_b16 v[108:109],v1 offset:30720
ds_read_b64_tr_b16 v[110:111],v1 offset:35840
v_mfma_f32_16x16x32_bf16 v[152:155],v[104:107],a[112:115],v[152:155]
ds_read_b64_tr_b16 v[104:105],v1 offset:20480
v_accvgpr_read_b32 v17,a41
v_mfma_f32_16x16x32_bf16 v[152:155],v[100:103],a[104:107],v[152:155]
ds_read_b64_tr_b16 v[100:101],v1 offset:10240
v_accvgpr_read_b32 v18,a42
v_mfma_f32_16x16x32_bf16 v[152:155],v[96:99],a[96:99],v[152:155]
ds_read_b64_tr_b16 v[96:97],v1
v_accvgpr_read_b32 v19,a43
v_mfma_f32_16x16x32_bf16 v[160:163],v[112:115],a[68:71],0
ds_read_b64_tr_b16 v[98:99],v1 offset:5120
ds_read_b64_tr_b16 v[102:103],v1 offset:15360
v_mfma_f32_16x16x32_bf16 v[160:163],v[116:119],a[76:79],v[160:163]
ds_read_b64_tr_b16 v[106:107],v1 offset:25600
v_accvgpr_read_b32 v20,a44
v_mfma_f32_16x16x32_bf16 v[160:163],v[120:123],a[84:87],v[160:163]
v_accvgpr_read_b32 v21,a45
v_accvgpr_read_b32 v22,a46
v_mfma_f32_16x16x32_bf16 v[160:163],v[124:127],a[92:95],v[160:163]
v_accvgpr_read_b32 v23,a47
v_mul_f32 v16,v16,v61
v_mfma_f32_16x16x32_bf16 v[168:171],v[124:127],a[124:127],0
v_mul_f32 v17,v17,v61
v_mul_f32 v18,v18,v61
v_mfma_f32_16x16x32_bf16 v[168:171],v[120:123],a[116:119],v[168:171]
v_mul_f32 v19,v19,v61
v_mul_f32 v20,v20,v61
v_mfma_f32_16x16x32_bf16 v[168:171],v[116:119],a[108:111],v[168:171]
buffer_load_dwordx4 a[160:163],v6,s[28:31],s89 offen sc1
v_mfma_f32_16x16x32_bf16 v[168:171],v[112:115],a[100:103],v[168:171]
v_mul_f32 v21,v21,v61
v_mul_f32 v22,v22,v61
v_mfma_f32_16x16x32_bf16 v[176:179],v[112:115],a[64:67],0
v_mul_f32 v23,v23,v61
v_cvt_pk_bf16_f32 v156,v16,v17
v_mfma_f32_16x16x32_bf16 v[176:179],v[116:119],a[72:75],v[176:179]
v_cvt_pk_bf16_f32 v157,v18,v19
v_cvt_pk_bf16_f32 v158,v20,v21
v_mfma_f32_16x16x32_bf16 v[176:179],v[120:123],a[80:83],v[176:179]
buffer_store_dwordx4 v[196:199],v6,s[40:43],s79 offen offset:2048 sc1
v_mfma_f32_16x16x32_bf16 v[176:179],v[124:127],a[88:91],v[176:179]
v_cvt_pk_bf16_f32 v159,v22,v23
v_accvgpr_read_b32 v16,a56
v_mfma_f32_16x16x32_bf16 v[184:187],v[124:127],a[120:123],0
v_accvgpr_read_b32 v17,a57
v_accvgpr_read_b32 v18,a58
v_mfma_f32_16x16x32_bf16 v[184:187],v[120:123],a[112:115],v[184:187]
ds_read_b64_tr_b16 v[120:121],v2 offset:20512
ds_read_b64_tr_b16 v[122:123],v2 offset:25632
v_mfma_f32_16x16x32_bf16 v[184:187],v[116:119],a[104:107],v[184:187]
ds_read_b64_tr_b16 v[116:117],v2 offset:10272
v_accvgpr_read_b32 v19,a59
v_mfma_f32_16x16x32_bf16 v[184:187],v[112:115],a[96:99],v[184:187]
s_waitcnt lgkmcnt(3)
ds_read_b64_tr_b16 v[112:113],v2 offset:32
v_accvgpr_read_b32 v20,a60
v_mfma_f32_16x16x32_bf16 v[192:195],v[64:67],a[68:71],0
ds_read_b64_tr_b16 v[114:115],v2 offset:5152
v_accvgpr_read_b32 v21,a61
v_mfma_f32_16x16x32_bf16 v[192:195],v[68:71],a[76:79],v[192:195]
ds_read_b64_tr_b16 v[118:119],v2 offset:15392
v_accvgpr_read_b32 v22,a62
v_mfma_f32_16x16x32_bf16 v[192:195],v[72:75],a[84:87],v[192:195]
v_accvgpr_read_b32 v23,a63
v_mul_f32 v16,v16,v61
v_mfma_f32_16x16x32_bf16 v[192:195],v[76:79],a[92:95],v[192:195]
buffer_load_dwordx4 a[176:179],v6,s[28:31],s90 offen sc1
v_mfma_f32_16x16x32_bf16 v[200:203],v[76:79],a[124:127],0
v_mul_f32 v17,v17,v61
v_mul_f32 v18,v18,v61
v_mfma_f32_16x16x32_bf16 v[200:203],v[72:75],a[116:119],v[200:203]
v_mul_f32 v19,v19,v61
v_mul_f32 v20,v20,v61
v_mfma_f32_16x16x32_bf16 v[200:203],v[68:71],a[108:111],v[200:203]
v_mul_f32 v21,v21,v61
v_mul_f32 v22,v22,v61
v_mfma_f32_16x16x32_bf16 v[200:203],v[64:67],a[100:103],v[200:203]
v_mul_f32 v23,v23,v61
v_cvt_pk_bf16_f32 v188,v16,v17
v_mfma_f32_16x16x32_bf16 v[208:211],v[64:67],a[64:67],0
)A"
R"A(v_cvt_pk_bf16_f32 v189,v18,v19
v_cvt_pk_bf16_f32 v190,v20,v21
v_mfma_f32_16x16x32_bf16 v[208:211],v[68:71],a[72:75],v[208:211]
buffer_store_dwordx4 v[212:215],v6,s[40:43],s80 offen offset:2048 sc1
v_mfma_f32_16x16x32_bf16 v[208:211],v[72:75],a[80:83],v[208:211]
ds_read_b64_tr_b16 v[124:125],v2 offset:30752
v_cvt_pk_bf16_f32 v191,v22,v23
v_mfma_f32_16x16x32_bf16 v[208:211],v[76:79],a[88:91],v[208:211]
ds_read_b64_tr_b16 v[126:127],v2 offset:35872
v_accvgpr_read_b32 v8,a32
v_mfma_f32_16x16x32_bf16 v[216:219],v[76:79],a[120:123],0
v_accvgpr_read_b32 v9,a33
v_accvgpr_read_b32 v10,a34
v_mfma_f32_16x16x32_bf16 v[216:219],v[72:75],a[112:115],v[216:219]
v_accvgpr_read_b32 v11,a35
v_accvgpr_read_b32 v12,a36
v_mfma_f32_16x16x32_bf16 v[216:219],v[68:71],a[104:107],v[216:219]
v_accvgpr_read_b32 v13,a37
v_accvgpr_read_b32 v14,a38
v_mfma_f32_16x16x32_bf16 v[216:219],v[64:67],a[96:99],v[216:219]
v_accvgpr_read_b32 v15,a39
v_mul_f32 v8,v8,v61
v_mfma_f32_16x16x32_bf16 v[224:227],v[80:83],a[68:71],0
buffer_load_dwordx4 a[180:183],v6,s[28:31],s90 offen offset:1024 sc1
v_mfma_f32_16x16x32_bf16 v[224:227],v[84:87],a[76:79],v[224:227]
v_mul_f32 v9,v9,v61
v_mul_f32 v10,v10,v61
v_mfma_f32_16x16x32_bf16 v[224:227],v[88:91],a[84:87],v[224:227]
v_mul_f32 v11,v11,v61
v_mul_f32 v12,v12,v61
v_mfma_f32_16x16x32_bf16 v[224:227],v[92:95],a[92:95],v[224:227]
v_mul_f32 v13,v13,v61
v_mul_f32 v14,v14,v61
v_mfma_f32_16x16x32_bf16 v[232:235],v[92:95],a[124:127],0
v_mul_f32 v15,v15,v61
v_cvt_pk_bf16_f32 v140,v8,v9
v_mfma_f32_16x16x32_bf16 v[232:235],v[88:91],a[116:119],v[232:235]
v_cvt_pk_bf16_f32 v141,v10,v11
v_cvt_pk_bf16_f32 v142,v12,v13
v_mfma_f32_16x16x32_bf16 v[232:235],v[84:87],a[108:111],v[232:235]
v_cvt_pk_bf16_f32 v143,v14,v15
v_accvgpr_read_b32 v8,a48
v_mfma_f32_16x16x32_bf16 v[232:235],v[80:83],a[100:103],v[232:235]
v_accvgpr_read_b32 v9,a49
v_accvgpr_read_b32 v10,a50
v_mfma_f32_16x16x32_bf16 v[240:243],v[80:83],a[64:67],0
buffer_store_dwordx4 v[228:231],v6,s[40:43],s81 offen offset:2048 sc1
v_mfma_f32_16x16x32_bf16 v[240:243],v[84:87],a[72:75],v[240:243]
v_accvgpr_read_b32 v11,a51
v_accvgpr_read_b32 v12,a52
v_mfma_f32_16x16x32_bf16 v[240:243],v[88:91],a[80:83],v[240:243]
v_accvgpr_read_b32 v13,a53
v_accvgpr_read_b32 v14,a54
v_mfma_f32_16x16x32_bf16 v[240:243],v[92:95],a[88:91],v[240:243]
v_accvgpr_read_b32 v15,a55
v_mul_f32 v8,v8,v61
v_mfma_f32_16x16x32_bf16 v[248:251],v[92:95],a[120:123],0
v_mul_f32 v9,v9,v61
v_mul_f32 v10,v10,v61
v_mfma_f32_16x16x32_bf16 v[248:251],v[88:91],a[112:115],v[248:251]
buffer_load_dwordx4 a[164:167],v6,s[28:31],s89 offen offset:1024 sc1
v_mfma_f32_16x16x32_bf16 v[248:251],v[84:87],a[104:107],v[248:251]
v_mul_f32 v11,v11,v61
v_mul_f32 v12,v12,v61
v_mfma_f32_16x16x32_bf16 v[248:251],v[80:83],a[96:99],v[248:251]
v_mul_f32 v13,v13,v61
v_mul_f32 v14,v14,v61
v_mfma_f32_16x16x32_bf16 a[192:195],v[96:99],v[32:35],a[192:195]
v_mul_f32 v15,v15,v61
v_cvt_pk_bf16_f32 v172,v8,v9
v_mfma_f32_16x16x32_bf16 a[192:195],v[100:103],v[36:39],a[192:195]
v_cvt_pk_bf16_f32 v173,v10,v11
v_cvt_pk_bf16_f32 v174,v12,v13
v_mfma_f32_16x16x32_bf16 a[192:195],v[104:107],v[40:43],a[192:195]
buffer_load_dwordx4 a[68:71],v6,s[28:31],s20 offen sc1
v_mfma_f32_16x16x32_bf16 a[192:195],v[108:111],v[44:47],a[192:195]
ds_read_b64_tr_b16 v[64:65],v1 offset:64
v_cvt_pk_bf16_f32 v175,v14,v15
v_mfma_f32_16x16x32_bf16 a[196:199],v[108:111],v[180:183],a[196:199]
ds_read_b64_tr_b16 v[66:67],v1 offset:5184
ds_read_b64_tr_b16 v[68:69],v1 offset:10304
v_mfma_f32_16x16x32_bf16 a[196:199],v[104:107],v[164:167],a[196:199]
s_cmp_eq_u32 s60,3
s_cselect_b32 s69,0x40000000,0
ds_read_b64_tr_b16 v[70:71],v1 offset:15424
v_mfma_f32_16x16x32_bf16 a[196:199],v[100:103],v[148:151],a[196:199]
ds_read_b64_tr_b16 v[72:73],v1 offset:20544
v_add_u32 v28,s69,v6
v_mfma_f32_16x16x32_bf16 a[196:199],v[96:99],v[132:135],a[196:199]
buffer_store_dwordx4 v[244:247],v6,s[40:43],s82 offen offset:2048 sc1
v_mfma_f32_16x16x32_bf16 a[200:203],v[96:99],v[196:199],a[200:203]
s_and_b32 s69,s63,s60
ds_read_b64_tr_b16 v[74:75],v1 offset:25664
ds_read_b64_tr_b16 v[76:77],v1 offset:30784
v_mfma_f32_16x16x32_bf16 a[200:203],v[100:103],v[212:215],a[200:203]
ds_read_b64_tr_b16 v[78:79],v1 offset:35904
v_mov_b32 v8,s52
v_mfma_f32_16x16x32_bf16 a[200:203],v[104:107],v[228:231],a[200:203]
buffer_load_dwordx4 a[148:151],v6,s[28:31],s88 offen offset:1024 sc1
v_mfma_f32_16x16x32_bf16 a[200:203],v[108:111],v[244:247],a[200:203]
ds_read_b64_tr_b16 v[80:81],v2 offset:96
ds_read_b64_tr_b16 v[82:83],v2 offset:5216
v_mfma_f32_16x16x32_bf16 a[204:207],v[108:111],v[188:191],a[204:207]
ds_read_b64_tr_b16 v[84:85],v2 offset:10336
v_mov_b32 v9,s53
v_mfma_f32_16x16x32_bf16 a[204:207],v[104:107],v[172:175],a[204:207]
ds_read_b64_tr_b16 v[86:87],v2 offset:15456
v_lshlrev_b32 v10,20,v6
v_mfma_f32_16x16x32_bf16 a[204:207],v[100:103],v[156:159],a[204:207]
s_waitcnt lgkmcnt(7)
ds_read_b64_tr_b16 v[88:89],v2 offset:20576
v_bfe_u32 v3,v6,4,2
v_mfma_f32_16x16x32_bf16 a[204:207],v[96:99],v[140:143],a[204:207]
buffer_load_dwordx4 a[76:79],v6,s[28:31],s21 offen sc1
v_mfma_f32_16x16x32_bf16 a[208:211],v[112:115],v[32:35],a[208:211]
ds_read_b64_tr_b16 v[90:91],v2 offset:25696
ds_read_b64_tr_b16 v[92:93],v2 offset:30816
v_mfma_f32_16x16x32_bf16 a[208:211],v[116:119],v[36:39],a[208:211]
ds_read_b64_tr_b16 v[94:95],v2 offset:35936
v_min_u32 v3,2,v3
v_mfma_f32_16x16x32_bf16 a[208:211],v[120:123],v[40:43],a[208:211]
ds_read_b64_tr_b16 v[96:97],v1 offset:128
v_add3_u32 v3,v3,s64,1
v_mfma_f32_16x16x32_bf16 a[208:211],v[124:127],v[44:47],a[208:211]
ds_read_b64_tr_b16 v[98:99],v1 offset:5248
v_and_b32 v3,3,v3
v_mfma_f32_16x16x32_bf16 a[212:215],v[124:127],v[180:183],a[212:215]
ds_read_b64_tr_b16 v[100:101],v1 offset:10368
ds_read_b64_tr_b16 v[102:103],v1 offset:15488
v_mfma_f32_16x16x32_bf16 a[212:215],v[120:123],v[164:167],a[212:215]
ds_read_b64_tr_b16 v[104:105],v1 offset:20608
v_lshlrev_b32 v3,6,v3
v_mfma_f32_16x16x32_bf16 a[212:215],v[116:119],v[148:151],a[212:215]
s_nop 0
L_pd20_%=:
s_cbranch_execz L_trt1_%=
buffer_store_dwordx4 v[140:143],v28,s[40:43],s79 offen offset:3072 sc1
v_mfma_f32_16x16x32_bf16 a[212:215],v[112:115],v[132:135],a[212:215]
ds_read_b64_tr_b16 v[106:107],v1 offset:25728
ds_read_b64_tr_b16 v[108:109],v1 offset:30848
v_mfma_f32_16x16x32_bf16 a[216:219],v[112:115],v[196:199],a[216:219]
buffer_load_dwordx4 a[132:135],v6,s[28:31],s87 offen offset:1024 sc1
v_mfma_f32_16x16x32_bf16 a[216:219],v[116:119],v[212:215],a[216:219]
ds_read_b64_tr_b16 v[110:111],v1 offset:35968
v_bfe_u32 v56,v6,6,2
v_mfma_f32_16x16x32_bf16 a[216:219],v[120:123],v[228:231],a[216:219]
v_lshrrev_b32 v57,8,v6
v_cmp_eq_u32 s[56:57],v56,v57
v_mfma_f32_16x16x32_bf16 a[216:219],v[124:127],v[244:247],a[216:219]
v_bfe_u32 v56,v6,4,1
v_lshlrev_b32 v56,4,v56
v_mfma_f32_16x16x32_bf16 a[220:223],v[124:127],v[188:191],a[220:223]
ds_read_b64_tr_b16 v[124:125],v2 offset:30880
ds_read_b64_tr_b16 v[126:127],v2 offset:36000
v_mfma_f32_16x16x32_bf16 a[220:223],v[120:123],v[172:175],a[220:223]
buffer_load_dwordx4 a[84:87],v6,s[28:31],s22 offen sc1
v_mfma_f32_16x16x32_bf16 a[220:223],v[116:119],v[156:159],a[220:223]
ds_read_b64_tr_b16 v[116:117],v2 offset:10400
ds_read_b64_tr_b16 v[118:119],v2 offset:15520
v_mfma_f32_16x16x32_bf16 a[220:223],v[112:115],v[140:143],a[220:223]
ds_read_b64_tr_b16 v[112:113],v2 offset:160
v_mov_b32 v57,0x3f80
v_mfma_f32_16x16x32_bf16 a[224:227],v[64:67],v[32:35],a[224:227]
ds_read_b64_tr_b16 v[114:115],v2 offset:5280
v_lshlrev_b32 v56,v56,v57
v_mfma_f32_16x16x32_bf16 a[224:227],v[68:71],v[36:39],a[224:227]
s_waitcnt lgkmcnt(6)
ds_read_b64_tr_b16 v[120:121],v2 offset:20640
v_cndmask_b32 v56,0,v56,s[56:57]
v_mfma_f32_16x16x32_bf16 a[224:227],v[72:75],v[40:43],a[224:227]
ds_read_b64_tr_b16 v[122:123],v2 offset:25760
v_bfe_u32 v57,v6,5,1
v_mfma_f32_16x16x32_bf16 a[224:227],v[76:79],v[44:47],a[224:227]
s_add_u32 s36,s12,s66
s_addc_u32 s37,s13,0
v_cmp_eq_u32 s[56:57],0,v57
v_mfma_f32_16x16x32_bf16 a[228:231],v[76:79],v[180:183],a[228:231]
buffer_load_dwordx4 a[136:139],v6,s[28:31],s87 offen offset:2048 sc1
v_mfma_f32_16x16x32_bf16 a[228:231],v[72:75],v[164:167],a[228:231]
s_and_b32 s37,s37,0xffff
v_cndmask_b32 v48,0,v56,s[56:57]
v_cndmask_b32 v49,v56,0,s[56:57]
v_mfma_f32_16x16x32_bf16 a[228:231],v[68:71],v[148:151],a[228:231]
buffer_store_dwordx4 v[156:159],v28,s[40:43],s80 offen offset:3072 sc1
v_mfma_f32_16x16x32_bf16 a[228:231],v[64:67],v[132:135],a[228:231]
s_mul_i32 s66,s61,307200
v_mov_b32 v58,s52
v_mov_b32 v59,s53
v_mfma_f32_16x16x32_bf16 a[232:235],v[64:67],v[196:199],a[232:235]
s_add_u32 s24,s16,s66
s_addc_u32 s25,s17,0
v_lshlrev_b32 v60,20,v6
v_mfma_f32_16x16x32_bf16 a[232:235],v[68:71],v[212:215],a[232:235]
buffer_load_dwordx4 a[92:95],v6,s[28:31],s23 offen sc1
v_mfma_f32_16x16x32_bf16 a[232:235],v[72:75],v[228:231],a[232:235]
s_mul_i32 s66,s60,16384
s_add_u32 s66,s66,s67
s_add_u32 s79,s66,12288
v_mfma_f32_16x16x32_bf16 a[232:235],v[76:79],v[244:247],a[232:235]
s_add_u32 s80,s66,77824
v_bfe_u32 v61,v6,4,1
v_add3_u32 v61,v61,s63,2
v_mfma_f32_16x16x32_bf16 a[236:239],v[76:79],v[188:191],a[236:239]
s_and_b32 s25,s25,0xffff
ds_read_b64_tr_b16 v[76:77],v1 offset:30912
ds_read_b64_tr_b16 v[78:79],v1 offset:36032
v_mfma_f32_16x16x32_bf16 a[236:239],v[72:75],v[172:175],a[236:239]
s_add_u32 s67,s77,8192
ds_read_b64_tr_b16 v[72:73],v1 offset:20672
v_and_b32 v61,3,v61
v_mfma_f32_16x16x32_bf16 a[236:239],v[68:71],v[156:159],a[236:239]
ds_read_b64_tr_b16 v[68:69],v1 offset:10432
v_lshlrev_b32 v61,8,v61
v_mfma_f32_16x16x32_bf16 a[236:239],v[64:67],v[140:143],a[236:239]
ds_read_b64_tr_b16 v[64:65],v1 offset:192
v_mov_b32 v50,0
v_mfma_f32_16x16x32_bf16 a[240:243],v[80:83],v[32:35],a[240:243]
ds_read_b64_tr_b16 v[66:67],v1 offset:5312
ds_read_b64_tr_b16 v[70:71],v1 offset:15552
v_mfma_f32_16x16x32_bf16 a[240:243],v[84:87],v[36:39],a[240:243]
buffer_load_dwordx4 a[152:155],v6,s[28:31],s88 offen offset:2048 sc1
v_mfma_f32_16x16x32_bf16 a[240:243],v[88:91],v[40:43],a[240:243]
ds_read_b64_tr_b16 v[74:75],v1 offset:25792
v_mov_b32 v51,0
v_mfma_f32_16x16x32_bf16 a[240:243],v[92:95],v[44:47],a[240:243]
v_mov_b32 v52,0
v_mov_b32 v53,0
v_mfma_f32_16x16x32_bf16 a[244:247],v[92:95],v[180:183],a[244:247]
v_mov_b32 v54,v48
v_mov_b32 v55,v49
v_mfma_f32_16x16x32_bf16 a[244:247],v[88:91],v[164:167],a[244:247]
buffer_store_dwordx4 v[172:175],v28,s[40:43],s81 offen offset:3072 sc1
v_mfma_f32_16x16x32_bf16 a[244:247],v[84:87],v[148:151],a[244:247]
s_add_u32 s81,s66,143360
v_bfe_u32 v56,v6,4,4
v_mul_u32_u24 v56,1280,v56
v_mfma_f32_16x16x32_bf16 a[244:247],v[80:83],v[132:135],a[244:247]
buffer_load_dwordx4 a[124:127],v6,s[28:31],s23 offen offset:1024 sc1
v_mfma_f32_16x16x32_bf16 a[248:251],v[80:83],v[196:199],a[248:251]
v_bfe_u32 v57,v6,8,1
v_lshl_add_u32 v56,v57,5,v56
v_mfma_f32_16x16x32_bf16 a[248:251],v[84:87],v[212:215],a[248:251]
v_lshrrev_b32 v57,9,v6
v_lshl_add_u32 v56,v57,4,v56
v_mfma_f32_16x16x32_bf16 a[248:251],v[88:91],v[228:231],a[248:251]
s_cmp_eq_u32 s69,3
s_cselect_b32 s69,0x40000000,0
s_add_u32 s68,s68,s69
v_mfma_f32_16x16x32_bf16 a[248:251],v[92:95],v[244:247],a[248:251]
v_mfma_f32_16x16x32_bf16 a[252:255],v[92:95],v[188:191],a[252:255]
ds_read_b64_tr_b16 v[92:93],v2 offset:30944
ds_read_b64_tr_b16 v[94:95],v2 offset:36064
v_mfma_f32_16x16x32_bf16 a[252:255],v[88:91],v[172:175],a[252:255]
ds_read_b64_tr_b16 v[88:89],v2 offset:20704
v_mfma_f32_16x16x32_bf16 a[252:255],v[84:87],v[156:159],a[252:255]
buffer_load_dwordx4 a[168:171],v6,s[28:31],s89 offen offset:2048 sc1
v_mfma_f32_16x16x32_bf16 a[252:255],v[80:83],v[140:143],a[252:255]
ds_read_b64_tr_b16 v[80:81],v2 offset:224
ds_read_b64_tr_b16 v[82:83],v2 offset:5344
v_mfma_f32_16x16x32_bf16 a[0:3],v[96:99],v[32:35],a[0:3]
ds_read_b64_tr_b16 v[84:85],v2 offset:10464
v_mfma_f32_16x16x32_bf16 a[0:3],v[100:103],v[36:39],a[0:3]
ds_read_b64_tr_b16 v[86:87],v2 offset:15584
v_mfma_f32_16x16x32_bf16 a[0:3],v[104:107],v[40:43],a[0:3]
ds_read_b64_tr_b16 v[90:91],v2 offset:25824
v_mfma_f32_16x16x32_bf16 a[0:3],v[108:111],v[44:47],a[0:3]
buffer_load_dwordx4 a[116:119],v6,s[28:31],s22 offen offset:1024 sc1
)A"
R"A(v_mfma_f32_16x16x32_bf16 a[4:7],v[108:111],v[180:183],a[4:7]
v_mfma_f32_16x16x32_bf16 a[4:7],v[104:107],v[164:167],a[4:7]
buffer_store_dwordx4 v[188:191],v28,s[40:43],s82 offen offset:3072 sc1
v_mfma_f32_16x16x32_bf16 a[4:7],v[100:103],v[148:151],a[4:7]
s_add_u32 s82,s66,208896
s_mul_i32 s66,s64,320
v_mfma_f32_16x16x32_bf16 a[4:7],v[96:99],v[132:135],a[4:7]
v_mfma_f32_16x16x32_bf16 a[8:11],v[96:99],v[196:199],a[8:11]
v_mfma_f32_16x16x32_bf16 a[8:11],v[100:103],v[212:215],a[8:11]
v_mfma_f32_16x16x32_bf16 a[8:11],v[104:107],v[228:231],a[8:11]
v_mfma_f32_16x16x32_bf16 a[8:11],v[108:111],v[244:247],a[8:11]
buffer_load_dwordx4 a[184:187],v6,s[28:31],s90 offen offset:2048 sc1
v_mfma_f32_16x16x32_bf16 a[12:15],v[108:111],v[188:191],a[12:15]
ds_read_b64_tr_b16 v[108:109],v1 offset:30976
ds_read_b64_tr_b16 v[110:111],v1 offset:36096
v_mfma_f32_16x16x32_bf16 a[12:15],v[104:107],v[172:175],a[12:15]
ds_read_b64_tr_b16 v[104:105],v1 offset:20736
v_mfma_f32_16x16x32_bf16 a[12:15],v[100:103],v[156:159],a[12:15]
ds_read_b64_tr_b16 v[100:101],v1 offset:10496
v_mfma_f32_16x16x32_bf16 a[12:15],v[96:99],v[140:143],a[12:15]
s_waitcnt lgkmcnt(4)
ds_read_b64_tr_b16 v[96:97],v1 offset:256
v_mfma_f32_16x16x32_bf16 a[16:19],v[112:115],v[32:35],a[16:19]
buffer_load_dwordx4 a[108:111],v6,s[28:31],s21 offen offset:1024 sc1
v_mfma_f32_16x16x32_bf16 a[16:19],v[116:119],v[36:39],a[16:19]
ds_read_b64_tr_b16 v[98:99],v1 offset:5376
ds_read_b64_tr_b16 v[102:103],v1 offset:15616
v_mfma_f32_16x16x32_bf16 a[16:19],v[120:123],v[40:43],a[16:19]
ds_read_b64_tr_b16 v[106:107],v1 offset:25856
v_add_u32 v1,0xffff6000,v1
v_mfma_f32_16x16x32_bf16 a[16:19],v[124:127],v[44:47],a[16:19]
v_mfma_f32_16x16x32_bf16 a[20:23],v[124:127],v[180:183],a[20:23]
v_mfma_f32_16x16x32_bf16 a[20:23],v[120:123],v[164:167],a[20:23]
v_mfma_f32_16x16x32_bf16 a[20:23],v[116:119],v[148:151],a[20:23]
v_mfma_f32_16x16x32_bf16 a[20:23],v[112:115],v[132:135],a[20:23]
v_mfma_f32_16x16x32_bf16 a[24:27],v[112:115],v[196:199],a[24:27]
buffer_load_dwordx4 a[188:191],v28,s[28:31],s90 offen offset:3072 sc1
v_mfma_f32_16x16x32_bf16 a[24:27],v[116:119],v[212:215],a[24:27]
v_mfma_f32_16x16x32_bf16 a[24:27],v[120:123],v[228:231],a[24:27]
v_mfma_f32_16x16x32_bf16 a[24:27],v[124:127],v[244:247],a[24:27]
v_mfma_f32_16x16x32_bf16 a[28:31],v[124:127],v[188:191],a[28:31]
ds_read_b64_tr_b16 v[124:125],v2 offset:31008
ds_read_b64_tr_b16 v[126:127],v2 offset:36128
v_mfma_f32_16x16x32_bf16 a[28:31],v[120:123],v[172:175],a[28:31]
buffer_load_dwordx4 a[100:103],v6,s[28:31],s20 offen offset:1024 sc1
v_mfma_f32_16x16x32_bf16 a[28:31],v[116:119],v[156:159],a[28:31]
ds_read_b64_tr_b16 v[116:117],v2 offset:10528
ds_read_b64_tr_b16 v[118:119],v2 offset:15648
v_mfma_f32_16x16x32_bf16 a[28:31],v[112:115],v[140:143],a[28:31]
ds_read_b64_tr_b16 v[112:113],v2 offset:288
v_mfma_f32_16x16x32_bf16 v[128:131],v[64:67],v[32:35],v[128:131]
ds_read_b64_tr_b16 v[114:115],v2 offset:5408
v_mfma_f32_16x16x32_bf16 v[128:131],v[68:71],v[36:39],v[128:131]
ds_read_b64_tr_b16 v[120:121],v2 offset:20768
v_mfma_f32_16x16x32_bf16 v[128:131],v[72:75],v[40:43],v[128:131]
ds_read_b64_tr_b16 v[122:123],v2 offset:25888
v_add_u32 v2,0xffff6000,v2
v_mfma_f32_16x16x32_bf16 v[128:131],v[76:79],v[44:47],v[128:131]
v_mfma_f32_16x16x32_bf16 v[136:139],v[76:79],v[180:183],v[136:139]
v_mfma_f32_16x16x32_bf16 v[136:139],v[72:75],v[164:167],v[136:139]
buffer_load_dwordx4 a[172:175],v28,s[28:31],s89 offen offset:3072 sc1
v_mfma_f32_16x16x32_bf16 v[136:139],v[68:71],v[148:151],v[136:139]
v_mfma_f32_16x16x32_bf16 v[136:139],v[64:67],v[132:135],v[136:139]
v_mfma_f32_16x16x32_bf16 v[144:147],v[64:67],v[196:199],v[144:147]
v_mfma_f32_16x16x32_bf16 v[144:147],v[68:71],v[212:215],v[144:147]
v_mfma_f32_16x16x32_bf16 v[144:147],v[72:75],v[228:231],v[144:147]
buffer_load_dwordx4 a[64:67],v6,s[28:31],s20 offen offset:2048 sc1
v_mfma_f32_16x16x32_bf16 v[144:147],v[76:79],v[244:247],v[144:147]
v_mfma_f32_16x16x32_bf16 v[152:155],v[76:79],v[188:191],v[152:155]
v_mfma_f32_16x16x32_bf16 v[152:155],v[72:75],v[172:175],v[152:155]
v_mfma_f32_16x16x32_bf16 v[152:155],v[68:71],v[156:159],v[152:155]
v_mfma_f32_16x16x32_bf16 v[152:155],v[64:67],v[140:143],v[152:155]
s_mov_b64 exec,0
s_cmp_eq_u32 s60,0
s_cbranch_scc1 L_pd21_%=
s_cmp_eq_u32 s60,1
s_cbranch_scc1 L_pd22_%=
s_cmp_eq_u32 s60,2
s_cbranch_scc1 L_pd23_%=
s_branch L_trtg2S_%=
L_trtg2S_%=:
s_mov_b64 exec,-1
s_waitcnt lgkmcnt(0)
s_barrier
v_mfma_f32_16x16x32_bf16 v[160:163],v[80:83],v[32:35],v[160:163]
buffer_load_dwordx4 v24,s[48:51],s83 offen lds
v_mfma_f32_16x16x32_bf16 v[160:163],v[84:87],v[36:39],v[160:163]
ds_read_b64_tr_b16 v[64:65],v1
ds_read_b64_tr_b16 v[66:67],v1 offset:5120
v_mfma_f32_16x16x32_bf16 v[160:163],v[88:91],v[40:43],v[160:163]
ds_read_b64_tr_b16 v[68:69],v1 offset:10240
v_mfma_f32_16x16x32_bf16 v[160:163],v[92:95],v[44:47],v[160:163]
ds_read_b64_tr_b16 v[70:71],v1 offset:15360
v_mfma_f32_16x16x32_bf16 v[168:171],v[92:95],v[180:183],v[168:171]
ds_read_b64_tr_b16 v[72:73],v1 offset:20480
v_mfma_f32_16x16x32_bf16 v[168:171],v[88:91],v[164:167],v[168:171]
buffer_load_dwordx4 a[156:159],v28,s[28:31],s88 offen offset:3072 sc1
v_mfma_f32_16x16x32_bf16 v[168:171],v[84:87],v[148:151],v[168:171]
ds_read_b64_tr_b16 v[74:75],v1 offset:25600
ds_read_b64_tr_b16 v[76:77],v1 offset:30720
v_mfma_f32_16x16x32_bf16 v[168:171],v[80:83],v[132:135],v[168:171]
ds_read_b64_tr_b16 v[78:79],v1 offset:35840
v_mfma_f32_16x16x32_bf16 v[176:179],v[80:83],v[196:199],v[176:179]
v_mfma_f32_16x16x32_bf16 v[176:179],v[84:87],v[212:215],v[176:179]
v_mfma_f32_16x16x32_bf16 v[176:179],v[88:91],v[228:231],v[176:179]
buffer_load_dwordx4 a[72:75],v6,s[28:31],s21 offen offset:2048 sc1
v_mfma_f32_16x16x32_bf16 v[176:179],v[92:95],v[244:247],v[176:179]
v_mfma_f32_16x16x32_bf16 v[184:187],v[92:95],v[188:191],v[184:187]
ds_read_b64_tr_b16 v[92:93],v2 offset:30752
ds_read_b64_tr_b16 v[94:95],v2 offset:35872
v_mfma_f32_16x16x32_bf16 v[184:187],v[88:91],v[172:175],v[184:187]
ds_read_b64_tr_b16 v[88:89],v2 offset:20512
v_mfma_f32_16x16x32_bf16 v[184:187],v[84:87],v[156:159],v[184:187]
ds_read_b64_tr_b16 v[84:85],v2 offset:10272
v_mfma_f32_16x16x32_bf16 v[184:187],v[80:83],v[140:143],v[184:187]
buffer_load_dwordx4 a[140:143],v28,s[28:31],s87 offen offset:3072 sc1
v_mfma_f32_16x16x32_bf16 v[192:195],v[96:99],v[32:35],v[192:195]
s_add_u32 s87,s64,3
ds_read_b64_tr_b16 v[80:81],v2 offset:32
ds_read_b64_tr_b16 v[82:83],v2 offset:5152
v_mfma_f32_16x16x32_bf16 v[192:195],v[100:103],v[36:39],v[192:195]
s_and_b32 s87,s87,3
s_lshl_b32 s87,s87,1
ds_read_b64_tr_b16 v[86:87],v2 offset:15392
v_mfma_f32_16x16x32_bf16 v[192:195],v[104:107],v[40:43],v[192:195]
s_lshl_b32 s87,s87,16
s_add_u32 s88,s87,4096
ds_read_b64_tr_b16 v[90:91],v2 offset:25632
v_mfma_f32_16x16x32_bf16 v[192:195],v[108:111],v[44:47],v[192:195]
s_add_u32 s89,s87,8192
s_add_u32 s90,s87,12288
v_mfma_f32_16x16x32_bf16 v[200:203],v[108:111],v[180:183],v[200:203]
buffer_load_dwordx4 a[80:83],v6,s[28:31],s22 offen offset:2048 sc1
v_mfma_f32_16x16x32_bf16 v[200:203],v[104:107],v[164:167],v[200:203]
v_mfma_f32_16x16x32_bf16 v[200:203],v[100:103],v[148:151],v[200:203]
v_mfma_f32_16x16x32_bf16 v[200:203],v[96:99],v[132:135],v[200:203]
v_mfma_f32_16x16x32_bf16 v[208:211],v[96:99],v[196:199],v[208:211]
v_mfma_f32_16x16x32_bf16 v[208:211],v[100:103],v[212:215],v[208:211]
v_mfma_f32_16x16x32_bf16 v[208:211],v[104:107],v[228:231],v[208:211]
v_mfma_f32_16x16x32_bf16 v[208:211],v[108:111],v[244:247],v[208:211]
buffer_load_dwordx4 v25,s[48:51],s83 offen offset:1024 lds
v_mfma_f32_16x16x32_bf16 v[216:219],v[108:111],v[188:191],v[216:219]
ds_read_b64_tr_b16 v[108:109],v1 offset:30784
ds_read_b64_tr_b16 v[110:111],v1 offset:35904
v_mfma_f32_16x16x32_bf16 v[216:219],v[104:107],v[172:175],v[216:219]
ds_read_b64_tr_b16 v[104:105],v1 offset:20544
v_mfma_f32_16x16x32_bf16 v[216:219],v[100:103],v[156:159],v[216:219]
ds_read_b64_tr_b16 v[100:101],v1 offset:10304
v_mfma_f32_16x16x32_bf16 v[216:219],v[96:99],v[140:143],v[216:219]
ds_read_b64_tr_b16 v[96:97],v1 offset:64
v_mfma_f32_16x16x32_bf16 v[224:227],v[112:115],v[32:35],v[224:227]
buffer_load_dwordx4 a[88:91],v6,s[28:31],s23 offen offset:2048 sc1
v_mfma_f32_16x16x32_bf16 v[224:227],v[116:119],v[36:39],v[224:227]
ds_read_b64_tr_b16 v[98:99],v1 offset:5184
ds_read_b64_tr_b16 v[102:103],v1 offset:15424
v_mfma_f32_16x16x32_bf16 v[224:227],v[120:123],v[40:43],v[224:227]
ds_read_b64_tr_b16 v[106:107],v1 offset:25664
v_mfma_f32_16x16x32_bf16 v[224:227],v[124:127],v[44:47],v[224:227]
v_mfma_f32_16x16x32_bf16 v[232:235],v[124:127],v[180:183],v[232:235]
v_mfma_f32_16x16x32_bf16 v[232:235],v[120:123],v[164:167],v[232:235]
v_mfma_f32_16x16x32_bf16 v[232:235],v[116:119],v[148:151],v[232:235]
v_mfma_f32_16x16x32_bf16 v[232:235],v[112:115],v[132:135],v[232:235]
buffer_load_dwordx4 v26,s[48:51],s83 offen offset:2048 lds
v_mfma_f32_16x16x32_bf16 v[240:243],v[112:115],v[196:199],v[240:243]
v_mfma_f32_16x16x32_bf16 v[240:243],v[116:119],v[212:215],v[240:243]
v_mfma_f32_16x16x32_bf16 v[240:243],v[120:123],v[228:231],v[240:243]
v_mfma_f32_16x16x32_bf16 v[240:243],v[124:127],v[244:247],v[240:243]
v_mfma_f32_16x16x32_bf16 v[248:251],v[124:127],v[188:191],v[248:251]
ds_read_b64_tr_b16 v[124:125],v2 offset:30816
ds_read_b64_tr_b16 v[126:127],v2 offset:35936
v_mfma_f32_16x16x32_bf16 v[248:251],v[120:123],v[172:175],v[248:251]
buffer_load_dwordx4 a[120:123],v28,s[28:31],s23 offen offset:3072 sc1
v_mfma_f32_16x16x32_bf16 v[248:251],v[116:119],v[156:159],v[248:251]
ds_read_b64_tr_b16 v[116:117],v2 offset:10336
ds_read_b64_tr_b16 v[118:119],v2 offset:15456
v_mfma_f32_16x16x32_bf16 v[248:251],v[112:115],v[140:143],v[248:251]
s_waitcnt vmcnt(39) lgkmcnt(4)
ds_read_b64_tr_b16 v[112:113],v2 offset:96
v_mfma_f32_16x16x32_bf16 a[192:195],v[64:67],a[128:131],a[192:195]
ds_read_b64_tr_b16 v[114:115],v2 offset:5216
v_mfma_f32_16x16x32_bf16 a[192:195],v[68:71],a[144:147],a[192:195]
s_waitcnt vmcnt(37)
ds_read_b64_tr_b16 v[120:121],v2 offset:20576
v_mfma_f32_16x16x32_bf16 a[192:195],v[72:75],a[160:163],a[192:195]
s_waitcnt vmcnt(35)
ds_read_b64_tr_b16 v[122:123],v2 offset:25696
v_mfma_f32_16x16x32_bf16 a[192:195],v[76:79],a[176:179],a[192:195]
s_waitcnt vmcnt(27)
v_mfma_f32_16x16x32_bf16 a[196:199],v[76:79],a[180:183],a[196:199]
buffer_load_dwordx4 v27,s[48:51],s83 offen offset:3072 lds
v_mfma_f32_16x16x32_bf16 a[196:199],v[72:75],a[164:167],a[196:199]
s_add_u32 m0,s86,126976
v_mfma_f32_16x16x32_bf16 a[196:199],v[68:71],a[148:151],a[196:199]
s_waitcnt vmcnt(24)
v_mfma_f32_16x16x32_bf16 a[196:199],v[64:67],a[132:135],a[196:199]
v_mfma_f32_16x16x32_bf16 a[200:203],v[64:67],a[136:139],a[200:203]
s_waitcnt vmcnt(21)
v_mfma_f32_16x16x32_bf16 a[200:203],v[68:71],a[152:155],a[200:203]
s_waitcnt vmcnt(15)
v_mfma_f32_16x16x32_bf16 a[200:203],v[72:75],a[168:171],a[200:203]
buffer_load_dwordx4 a[112:115],v28,s[28:31],s22 offen offset:3072 sc1
v_mfma_f32_16x16x32_bf16 a[200:203],v[76:79],a[184:187],a[200:203]
s_waitcnt vmcnt(14)
v_mfma_f32_16x16x32_bf16 a[204:207],v[76:79],a[188:191],a[204:207]
s_waitcnt vmcnt(12)
ds_read_b64_tr_b16 v[76:77],v1 offset:30848
ds_read_b64_tr_b16 v[78:79],v1 offset:35968
v_mfma_f32_16x16x32_bf16 a[204:207],v[72:75],a[172:175],a[204:207]
s_waitcnt vmcnt(9)
ds_read_b64_tr_b16 v[72:73],v1 offset:20608
v_mfma_f32_16x16x32_bf16 a[204:207],v[68:71],a[156:159],a[204:207]
s_waitcnt vmcnt(7)
ds_read_b64_tr_b16 v[68:69],v1 offset:10368
v_mfma_f32_16x16x32_bf16 a[204:207],v[64:67],a[140:143],a[204:207]
ds_read_b64_tr_b16 v[64:65],v1 offset:128
v_mfma_f32_16x16x32_bf16 a[208:211],v[80:83],a[128:131],a[208:211]
ds_read_b64_tr_b16 v[66:67],v1 offset:5248
ds_read_b64_tr_b16 v[70:71],v1 offset:15488
v_mfma_f32_16x16x32_bf16 a[208:211],v[84:87],a[144:147],a[208:211]
buffer_load_dwordx4 v7,s[48:51],s83 offen lds
v_mfma_f32_16x16x32_bf16 a[208:211],v[88:91],a[160:163],a[208:211]
s_add_u32 m0,s86,128000
s_add_u32 s83,s64,1
ds_read_b64_tr_b16 v[74:75],v1 offset:25728
v_mfma_f32_16x16x32_bf16 a[208:211],v[92:95],a[176:179],a[208:211]
s_and_b32 s83,s83,3
s_lshl_b32 s83,s83,1
s_or_b32 s83,s83,1
)A"
R"A(v_mfma_f32_16x16x32_bf16 a[212:215],v[92:95],a[180:183],a[212:215]
s_mul_i32 s83,s83,163840
s_add_u32 s83,s83,s85
v_mfma_f32_16x16x32_bf16 a[212:215],v[88:91],a[164:167],a[212:215]
v_mfma_f32_16x16x32_bf16 a[212:215],v[84:87],a[148:151],a[212:215]
v_mfma_f32_16x16x32_bf16 a[212:215],v[80:83],a[132:135],a[212:215]
buffer_load_dwordx4 a[104:107],v28,s[28:31],s21 offen offset:3072 sc1
v_mfma_f32_16x16x32_bf16 a[216:219],v[80:83],a[136:139],a[216:219]
v_mfma_f32_16x16x32_bf16 a[216:219],v[84:87],a[152:155],a[216:219]
v_mfma_f32_16x16x32_bf16 a[216:219],v[88:91],a[168:171],a[216:219]
v_mfma_f32_16x16x32_bf16 a[216:219],v[92:95],a[184:187],a[216:219]
v_mfma_f32_16x16x32_bf16 a[220:223],v[92:95],a[188:191],a[220:223]
ds_read_b64_tr_b16 v[92:93],v2 offset:30880
ds_read_b64_tr_b16 v[94:95],v2 offset:36000
v_mfma_f32_16x16x32_bf16 a[220:223],v[88:91],a[172:175],a[220:223]
ds_read_b64_tr_b16 v[88:89],v2 offset:20640
v_mfma_f32_16x16x32_bf16 a[220:223],v[84:87],a[156:159],a[220:223]
buffer_load_dwordx4 v24,s[48:51],s84 offen lds
v_mfma_f32_16x16x32_bf16 a[220:223],v[80:83],a[140:143],a[220:223]
ds_read_b64_tr_b16 v[80:81],v2 offset:160
ds_read_b64_tr_b16 v[82:83],v2 offset:5280
s_barrier
v_mfma_f32_16x16x32_bf16 a[224:227],v[96:99],a[128:131],a[224:227]
ds_read_b64_tr_b16 v[84:85],v2 offset:10400
v_mfma_f32_16x16x32_bf16 a[224:227],v[100:103],a[144:147],a[224:227]
ds_read_b64_tr_b16 v[86:87],v2 offset:15520
v_mfma_f32_16x16x32_bf16 a[224:227],v[104:107],a[160:163],a[224:227]
buffer_store_dwordx2 v[8:9],v10,s[44:47],s18 offen offset:1280 sc1
v_mfma_f32_16x16x32_bf16 a[224:227],v[108:111],a[176:179],a[224:227]
ds_read_b64_tr_b16 v[90:91],v2 offset:25760
v_mfma_f32_16x16x32_bf16 a[228:231],v[108:111],a[180:183],a[228:231]
v_mfma_f32_16x16x32_bf16 a[228:231],v[104:107],a[164:167],a[228:231]
v_mfma_f32_16x16x32_bf16 a[228:231],v[100:103],a[148:151],a[228:231]
v_mfma_f32_16x16x32_bf16 a[228:231],v[96:99],a[132:135],a[228:231]
buffer_load_dwordx4 a[96:99],v28,s[28:31],s20 offen offset:3072 sc1
v_mfma_f32_16x16x32_bf16 a[232:235],v[96:99],a[136:139],a[232:235]
s_add_u32 s20,s64,1
s_and_b32 s20,s20,3
s_lshl_b32 s20,s20,1
v_mfma_f32_16x16x32_bf16 a[232:235],v[100:103],a[152:155],a[232:235]
s_or_b32 s20,s20,1
s_lshl_b32 s20,s20,16
s_add_u32 s21,s20,4096
v_mfma_f32_16x16x32_bf16 a[232:235],v[104:107],a[168:171],a[232:235]
s_add_u32 s22,s20,8192
s_add_u32 s23,s20,12288
v_mfma_f32_16x16x32_bf16 a[232:235],v[108:111],a[184:187],a[232:235]
v_mfma_f32_16x16x32_bf16 a[236:239],v[108:111],a[188:191],a[236:239]
ds_read_b64_tr_b16 v[108:109],v1 offset:30912
ds_read_b64_tr_b16 v[110:111],v1 offset:36032
v_mfma_f32_16x16x32_bf16 a[236:239],v[104:107],a[172:175],a[236:239]
ds_read_b64_tr_b16 v[104:105],v1 offset:20672
v_mfma_f32_16x16x32_bf16 a[236:239],v[100:103],a[156:159],a[236:239]
buffer_load_dwordx4 v25,s[48:51],s84 offen offset:1024 lds
v_mfma_f32_16x16x32_bf16 a[236:239],v[96:99],a[140:143],a[236:239]
s_waitcnt lgkmcnt(3)
ds_read_b64_tr_b16 v[96:97],v1 offset:192
ds_read_b64_tr_b16 v[98:99],v1 offset:5312
v_mfma_f32_16x16x32_bf16 a[240:243],v[112:115],a[128:131],a[240:243]
ds_read_b64_tr_b16 v[100:101],v1 offset:10432
v_mfma_f32_16x16x32_bf16 a[240:243],v[116:119],a[144:147],a[240:243]
ds_read_b64_tr_b16 v[102:103],v1 offset:15552
v_mfma_f32_16x16x32_bf16 a[240:243],v[120:123],a[160:163],a[240:243]
ds_read_b64_tr_b16 v[106:107],v1 offset:25792
v_mfma_f32_16x16x32_bf16 a[240:243],v[124:127],a[176:179],a[240:243]
v_mfma_f32_16x16x32_bf16 a[244:247],v[124:127],a[180:183],a[244:247]
v_mfma_f32_16x16x32_bf16 a[244:247],v[120:123],a[164:167],a[244:247]
buffer_load_dwordx4 v26,s[48:51],s84 offen offset:2048 lds
v_mfma_f32_16x16x32_bf16 a[244:247],v[116:119],a[148:151],a[244:247]
v_mfma_f32_16x16x32_bf16 a[244:247],v[112:115],a[132:135],a[244:247]
v_mfma_f32_16x16x32_bf16 a[248:251],v[112:115],a[136:139],a[248:251]
v_mfma_f32_16x16x32_bf16 a[248:251],v[116:119],a[152:155],a[248:251]
v_mfma_f32_16x16x32_bf16 a[248:251],v[120:123],a[168:171],a[248:251]
v_mfma_f32_16x16x32_bf16 a[248:251],v[124:127],a[184:187],a[248:251]
v_mfma_f32_16x16x32_bf16 a[252:255],v[124:127],a[188:191],a[252:255]
buffer_load_dwordx4 v27,s[48:51],s84 offen offset:3072 lds
v_mfma_f32_16x16x32_bf16 a[252:255],v[120:123],a[172:175],a[252:255]
s_add_u32 m0,s86,132096
ds_read_b64_tr_b16 v[120:121],v2 offset:20704
ds_read_b64_tr_b16 v[122:123],v2 offset:25824
v_mfma_f32_16x16x32_bf16 a[252:255],v[116:119],a[156:159],a[252:255]
ds_read_b64_tr_b16 v[116:117],v2 offset:10464
v_mfma_f32_16x16x32_bf16 a[252:255],v[112:115],a[140:143],a[252:255]
ds_read_b64_tr_b16 v[112:113],v2 offset:224
v_mfma_f32_16x16x32_bf16 a[0:3],v[64:67],a[128:131],a[0:3]
ds_read_b64_tr_b16 v[114:115],v2 offset:5344
v_mfma_f32_16x16x32_bf16 a[0:3],v[68:71],a[144:147],a[0:3]
ds_read_b64_tr_b16 v[118:119],v2 offset:15584
ds_read_b64_tr_b16 v[124:125],v2 offset:30944
v_mfma_f32_16x16x32_bf16 a[0:3],v[72:75],a[160:163],a[0:3]
ds_read_b64_tr_b16 v[126:127],v2 offset:36064
v_mfma_f32_16x16x32_bf16 a[0:3],v[76:79],a[176:179],a[0:3]
buffer_load_dwordx4 v7,s[48:51],s84 offen lds
v_mfma_f32_16x16x32_bf16 a[4:7],v[76:79],a[180:183],a[4:7]
s_add_u32 m0,s86,81920
s_add_u32 s84,s83,20480
v_mfma_f32_16x16x32_bf16 a[4:7],v[72:75],a[164:167],a[4:7]
v_mfma_f32_16x16x32_bf16 a[4:7],v[68:71],a[148:151],a[4:7]
v_mfma_f32_16x16x32_bf16 a[4:7],v[64:67],a[132:135],a[4:7]
v_mfma_f32_16x16x32_bf16 a[8:11],v[64:67],a[136:139],a[8:11]
v_mfma_f32_16x16x32_bf16 a[8:11],v[68:71],a[152:155],a[8:11]
v_mfma_f32_16x16x32_bf16 a[8:11],v[72:75],a[168:171],a[8:11]
buffer_load_dwordx4 v24,s[48:51],s83 offen lds
v_mfma_f32_16x16x32_bf16 a[8:11],v[76:79],a[184:187],a[8:11]
v_mfma_f32_16x16x32_bf16 a[12:15],v[76:79],a[188:191],a[12:15]
ds_read_b64_tr_b16 v[76:77],v1 offset:30976
ds_read_b64_tr_b16 v[78:79],v1 offset:36096
v_mfma_f32_16x16x32_bf16 a[12:15],v[72:75],a[172:175],a[12:15]
ds_read_b64_tr_b16 v[72:73],v1 offset:20736
v_mfma_f32_16x16x32_bf16 a[12:15],v[68:71],a[156:159],a[12:15]
ds_read_b64_tr_b16 v[68:69],v1 offset:10496
v_mfma_f32_16x16x32_bf16 a[12:15],v[64:67],a[140:143],a[12:15]
ds_read_b64_tr_b16 v[64:65],v1 offset:256
v_mfma_f32_16x16x32_bf16 a[16:19],v[80:83],a[128:131],a[16:19]
ds_read_b64_tr_b16 v[66:67],v1 offset:5376
ds_read_b64_tr_b16 v[70:71],v1 offset:15616
v_mfma_f32_16x16x32_bf16 a[16:19],v[84:87],a[144:147],a[16:19]
buffer_load_dwordx4 v25,s[48:51],s83 offen offset:1024 lds
v_mfma_f32_16x16x32_bf16 a[16:19],v[88:91],a[160:163],a[16:19]
ds_read_b64_tr_b16 v[74:75],v1 offset:25856
v_add_u32 v1,0xffff6000,v1
v_mfma_f32_16x16x32_bf16 a[16:19],v[92:95],a[176:179],a[16:19]
v_mfma_f32_16x16x32_bf16 a[20:23],v[92:95],a[180:183],a[20:23]
v_mfma_f32_16x16x32_bf16 a[20:23],v[88:91],a[164:167],a[20:23]
v_mfma_f32_16x16x32_bf16 a[20:23],v[84:87],a[148:151],a[20:23]
v_mfma_f32_16x16x32_bf16 a[20:23],v[80:83],a[132:135],a[20:23]
v_mfma_f32_16x16x32_bf16 a[24:27],v[80:83],a[136:139],a[24:27]
buffer_load_dwordx4 v26,s[48:51],s83 offen offset:2048 lds
v_mfma_f32_16x16x32_bf16 a[24:27],v[84:87],a[152:155],a[24:27]
v_mfma_f32_16x16x32_bf16 a[24:27],v[88:91],a[168:171],a[24:27]
v_mfma_f32_16x16x32_bf16 a[24:27],v[92:95],a[184:187],a[24:27]
v_mfma_f32_16x16x32_bf16 a[28:31],v[92:95],a[188:191],a[28:31]
ds_read_b64_tr_b16 v[92:93],v2 offset:31008
ds_read_b64_tr_b16 v[94:95],v2 offset:36128
v_mfma_f32_16x16x32_bf16 a[28:31],v[88:91],a[172:175],a[28:31]
ds_read_b64_tr_b16 v[88:89],v2 offset:20768
v_mfma_f32_16x16x32_bf16 a[28:31],v[84:87],a[156:159],a[28:31]
s_waitcnt lgkmcnt(3)
ds_read_b64_tr_b16 v[84:85],v2 offset:10528
v_mfma_f32_16x16x32_bf16 a[28:31],v[80:83],a[140:143],a[28:31]
buffer_load_dwordx4 v27,s[48:51],s83 offen offset:3072 lds
v_mfma_f32_16x16x32_bf16 v[128:131],v[96:99],a[128:131],v[128:131]
s_add_u32 m0,s86,86016
ds_read_b64_tr_b16 v[80:81],v2 offset:288
ds_read_b64_tr_b16 v[82:83],v2 offset:5408
v_mfma_f32_16x16x32_bf16 v[128:131],v[100:103],a[144:147],v[128:131]
ds_read_b64_tr_b16 v[86:87],v2 offset:15648
v_mfma_f32_16x16x32_bf16 v[128:131],v[104:107],a[160:163],v[128:131]
ds_read_b64_tr_b16 v[90:91],v2 offset:25888
v_add_u32 v2,0xffff6000,v2
v_mfma_f32_16x16x32_bf16 v[128:131],v[108:111],a[176:179],v[128:131]
v_mfma_f32_16x16x32_bf16 v[136:139],v[108:111],a[180:183],v[136:139]
v_mfma_f32_16x16x32_bf16 v[136:139],v[104:107],a[164:167],v[136:139]
v_mfma_f32_16x16x32_bf16 v[136:139],v[100:103],a[148:151],v[136:139]
buffer_load_dwordx4 v7,s[48:51],s83 offen lds
v_mfma_f32_16x16x32_bf16 v[136:139],v[96:99],a[132:135],v[136:139]
s_add_u32 m0,s86,87040
s_add_u32 s83,s64,2
s_and_b32 s83,s83,3
v_mfma_f32_16x16x32_bf16 v[144:147],v[96:99],a[136:139],v[144:147]
s_lshl_b32 s83,s83,1
s_or_b32 s83,s83,1
s_mul_i32 s83,s83,163840
v_mfma_f32_16x16x32_bf16 v[144:147],v[100:103],a[152:155],v[144:147]
s_add_u32 s83,s83,s85
v_mfma_f32_16x16x32_bf16 v[144:147],v[104:107],a[168:171],v[144:147]
v_mfma_f32_16x16x32_bf16 v[144:147],v[108:111],a[184:187],v[144:147]
v_mfma_f32_16x16x32_bf16 v[152:155],v[108:111],a[188:191],v[152:155]
ds_read_b64_tr_b16 v[108:109],v1 offset:30720
ds_read_b64_tr_b16 v[110:111],v1 offset:35840
v_mfma_f32_16x16x32_bf16 v[152:155],v[104:107],a[172:175],v[152:155]
buffer_load_dwordx4 v24,s[48:51],s84 offen lds
v_mfma_f32_16x16x32_bf16 v[152:155],v[100:103],a[156:159],v[152:155]
ds_read_b64_tr_b16 v[100:101],v1 offset:10240
ds_read_b64_tr_b16 v[102:103],v1 offset:15360
v_mfma_f32_16x16x32_bf16 v[152:155],v[96:99],a[140:143],v[152:155]
ds_read_b64_tr_b16 v[96:97],v1
s_waitcnt lgkmcnt(5)
s_barrier
v_mfma_f32_16x16x32_bf16 v[160:163],v[112:115],a[128:131],v[160:163]
ds_read_b64_tr_b16 v[98:99],v1 offset:5120
v_mfma_f32_16x16x32_bf16 v[160:163],v[116:119],a[144:147],v[160:163]
ds_read_b64_tr_b16 v[104:105],v1 offset:20480
v_mfma_f32_16x16x32_bf16 v[160:163],v[120:123],a[160:163],v[160:163]
ds_read_b64_tr_b16 v[106:107],v1 offset:25600
v_mfma_f32_16x16x32_bf16 v[160:163],v[124:127],a[176:179],v[160:163]
buffer_load_dwordx4 v25,s[48:51],s84 offen offset:1024 lds
v_mfma_f32_16x16x32_bf16 v[168:171],v[124:127],a[180:183],v[168:171]
v_mfma_f32_16x16x32_bf16 v[168:171],v[120:123],a[164:167],v[168:171]
L_pd21_%=:
s_cbranch_execz L_trtg2S_%=
v_mfma_f32_16x16x32_bf16 v[168:171],v[116:119],a[148:151],v[168:171]
v_mfma_f32_16x16x32_bf16 v[168:171],v[112:115],a[132:135],v[168:171]
v_mfma_f32_16x16x32_bf16 v[176:179],v[112:115],a[136:139],v[176:179]
v_mfma_f32_16x16x32_bf16 v[176:179],v[116:119],a[152:155],v[176:179]
v_mfma_f32_16x16x32_bf16 v[176:179],v[120:123],a[168:171],v[176:179]
buffer_load_dwordx4 v26,s[48:51],s84 offen offset:2048 lds
v_mfma_f32_16x16x32_bf16 v[176:179],v[124:127],a[184:187],v[176:179]
v_mfma_f32_16x16x32_bf16 v[184:187],v[124:127],a[188:191],v[184:187]
ds_read_b64_tr_b16 v[124:125],v2 offset:30752
ds_read_b64_tr_b16 v[126:127],v2 offset:35872
v_mfma_f32_16x16x32_bf16 v[184:187],v[120:123],a[172:175],v[184:187]
ds_read_b64_tr_b16 v[120:121],v2 offset:20512
v_mfma_f32_16x16x32_bf16 v[184:187],v[116:119],a[156:159],v[184:187]
ds_read_b64_tr_b16 v[116:117],v2 offset:10272
v_mfma_f32_16x16x32_bf16 v[184:187],v[112:115],a[140:143],v[184:187]
ds_read_b64_tr_b16 v[112:113],v2 offset:32
v_mfma_f32_16x16x32_bf16 v[192:195],v[64:67],a[128:131],v[192:195]
ds_read_b64_tr_b16 v[114:115],v2 offset:5152
ds_read_b64_tr_b16 v[118:119],v2 offset:15392
v_mfma_f32_16x16x32_bf16 v[192:195],v[68:71],a[144:147],v[192:195]
buffer_load_dwordx4 v27,s[48:51],s84 offen offset:3072 lds
v_mfma_f32_16x16x32_bf16 v[192:195],v[72:75],a[160:163],v[192:195]
s_add_u32 m0,s86,91136
ds_read_b64_tr_b16 v[122:123],v2 offset:25632
v_mfma_f32_16x16x32_bf16 v[192:195],v[76:79],a[176:179],v[192:195]
v_mfma_f32_16x16x32_bf16 v[200:203],v[76:79],a[180:183],v[200:203]
v_mfma_f32_16x16x32_bf16 v[200:203],v[72:75],a[164:167],v[200:203]
v_mfma_f32_16x16x32_bf16 v[200:203],v[68:71],a[148:151],v[200:203]
v_mfma_f32_16x16x32_bf16 v[200:203],v[64:67],a[132:135],v[200:203]
v_mfma_f32_16x16x32_bf16 v[208:211],v[64:67],a[136:139],v[208:211]
buffer_load_dwordx4 v7,s[48:51],s84 offen lds
v_mfma_f32_16x16x32_bf16 v[208:211],v[68:71],a[152:155],v[208:211]
)A"
R"A(s_add_u32 m0,s86,40960
s_add_u32 s84,s83,20480
v_mfma_f32_16x16x32_bf16 v[208:211],v[72:75],a[168:171],v[208:211]
v_mfma_f32_16x16x32_bf16 v[208:211],v[76:79],a[184:187],v[208:211]
v_mfma_f32_16x16x32_bf16 v[216:219],v[76:79],a[188:191],v[216:219]
ds_read_b64_tr_b16 v[76:77],v1 offset:30784
ds_read_b64_tr_b16 v[78:79],v1 offset:35904
v_mfma_f32_16x16x32_bf16 v[216:219],v[72:75],a[172:175],v[216:219]
buffer_load_dwordx2 v[4:5],v3,s[44:47],s91 offen offset:1280 sc1
v_mfma_f32_16x16x32_bf16 v[216:219],v[68:71],a[156:159],v[216:219]
ds_read_b64_tr_b16 v[68:69],v1 offset:10304
ds_read_b64_tr_b16 v[70:71],v1 offset:15424
v_mfma_f32_16x16x32_bf16 v[216:219],v[64:67],a[140:143],v[216:219]
ds_read_b64_tr_b16 v[64:65],v1 offset:64
v_mfma_f32_16x16x32_bf16 v[224:227],v[80:83],a[128:131],v[224:227]
ds_read_b64_tr_b16 v[66:67],v1 offset:5184
v_mfma_f32_16x16x32_bf16 v[224:227],v[84:87],a[144:147],v[224:227]
ds_read_b64_tr_b16 v[72:73],v1 offset:20544
v_mfma_f32_16x16x32_bf16 v[224:227],v[88:91],a[160:163],v[224:227]
buffer_load_dwordx4 v[32:35],v6,s[28:31],s87 offen sc1
v_mfma_f32_16x16x32_bf16 v[224:227],v[92:95],a[176:179],v[224:227]
ds_read_b64_tr_b16 v[74:75],v1 offset:25664
v_mfma_f32_16x16x32_bf16 v[232:235],v[92:95],a[180:183],v[232:235]
v_mfma_f32_16x16x32_bf16 v[232:235],v[88:91],a[164:167],v[232:235]
v_mfma_f32_16x16x32_bf16 v[232:235],v[84:87],a[148:151],v[232:235]
v_mfma_f32_16x16x32_bf16 v[232:235],v[80:83],a[132:135],v[232:235]
buffer_load_dwordx4 v[36:39],v6,s[28:31],s88 offen sc1
v_mfma_f32_16x16x32_bf16 v[240:243],v[80:83],a[136:139],v[240:243]
v_mfma_f32_16x16x32_bf16 v[240:243],v[84:87],a[152:155],v[240:243]
v_mfma_f32_16x16x32_bf16 v[240:243],v[88:91],a[168:171],v[240:243]
v_mfma_f32_16x16x32_bf16 v[240:243],v[92:95],a[184:187],v[240:243]
v_mfma_f32_16x16x32_bf16 v[248:251],v[92:95],a[188:191],v[248:251]
buffer_load_dwordx4 v[40:43],v6,s[28:31],s89 offen sc1
v_mfma_f32_16x16x32_bf16 v[248:251],v[88:91],a[172:175],v[248:251]
ds_read_b64_tr_b16 v[88:89],v2 offset:20576
ds_read_b64_tr_b16 v[90:91],v2 offset:25696
v_mfma_f32_16x16x32_bf16 v[248:251],v[84:87],a[156:159],v[248:251]
ds_read_b64_tr_b16 v[84:85],v2 offset:10336
v_mfma_f32_16x16x32_bf16 v[248:251],v[80:83],a[140:143],v[248:251]
s_waitcnt lgkmcnt(3)
ds_read_b64_tr_b16 v[80:81],v2 offset:96
v_mfma_f32_16x16x32_bf16 a[192:195],v[96:99],a[68:71],a[192:195]
ds_read_b64_tr_b16 v[82:83],v2 offset:5216
v_mfma_f32_16x16x32_bf16 a[192:195],v[100:103],a[76:79],a[192:195]
buffer_load_dwordx4 v[196:199],v6,s[28:31],s87 offen offset:2048 sc1
v_mfma_f32_16x16x32_bf16 a[192:195],v[104:107],a[84:87],a[192:195]
ds_read_b64_tr_b16 v[86:87],v2 offset:15456
ds_read_b64_tr_b16 v[92:93],v2 offset:30816
v_mfma_f32_16x16x32_bf16 a[192:195],v[108:111],a[92:95],a[192:195]
ds_read_b64_tr_b16 v[94:95],v2 offset:35936
v_mfma_f32_16x16x32_bf16 a[196:199],v[108:111],a[124:127],a[196:199]
v_mfma_f32_16x16x32_bf16 a[196:199],v[104:107],a[116:119],a[196:199]
v_mfma_f32_16x16x32_bf16 a[196:199],v[100:103],a[108:111],a[196:199]
buffer_load_dwordx4 v[44:47],v6,s[28:31],s90 offen sc1
v_mfma_f32_16x16x32_bf16 a[196:199],v[96:99],a[100:103],a[196:199]
v_mfma_f32_16x16x32_bf16 a[200:203],v[96:99],a[64:67],a[200:203]
v_mfma_f32_16x16x32_bf16 a[200:203],v[100:103],a[72:75],a[200:203]
s_waitcnt vmcnt(31)
v_mfma_f32_16x16x32_bf16 a[200:203],v[104:107],a[80:83],a[200:203]
s_waitcnt vmcnt(27)
v_mfma_f32_16x16x32_bf16 a[200:203],v[108:111],a[88:91],a[200:203]
buffer_load_dwordx4 v[212:215],v6,s[28:31],s88 offen offset:2048 sc1
v_mfma_f32_16x16x32_bf16 a[204:207],v[108:111],a[120:123],a[204:207]
s_waitcnt vmcnt(26)
ds_read_b64_tr_b16 v[108:109],v1 offset:30848
ds_read_b64_tr_b16 v[110:111],v1 offset:35968
v_mfma_f32_16x16x32_bf16 a[204:207],v[104:107],a[112:115],a[204:207]
s_waitcnt vmcnt(24)
ds_read_b64_tr_b16 v[104:105],v1 offset:20608
v_mfma_f32_16x16x32_bf16 a[204:207],v[100:103],a[104:107],a[204:207]
s_waitcnt vmcnt(21)
ds_read_b64_tr_b16 v[100:101],v1 offset:10368
v_mfma_f32_16x16x32_bf16 a[204:207],v[96:99],a[96:99],a[204:207]
ds_read_b64_tr_b16 v[96:97],v1 offset:128
v_mfma_f32_16x16x32_bf16 a[208:211],v[112:115],a[68:71],a[208:211]
buffer_load_dwordx4 v[180:183],v6,s[28:31],s90 offen offset:1024 sc1
v_mfma_f32_16x16x32_bf16 a[208:211],v[116:119],a[76:79],a[208:211]
ds_read_b64_tr_b16 v[98:99],v1 offset:5248
ds_read_b64_tr_b16 v[102:103],v1 offset:15488
v_mfma_f32_16x16x32_bf16 a[208:211],v[120:123],a[84:87],a[208:211]
ds_read_b64_tr_b16 v[106:107],v1 offset:25728
v_mfma_f32_16x16x32_bf16 a[208:211],v[124:127],a[92:95],a[208:211]
v_mfma_f32_16x16x32_bf16 a[212:215],v[124:127],a[124:127],a[212:215]
v_mfma_f32_16x16x32_bf16 a[212:215],v[120:123],a[116:119],a[212:215]
buffer_load_dwordx4 v[228:231],v6,s[28:31],s89 offen offset:2048 sc1
v_mfma_f32_16x16x32_bf16 a[212:215],v[116:119],a[108:111],a[212:215]
v_mfma_f32_16x16x32_bf16 a[212:215],v[112:115],a[100:103],a[212:215]
v_mfma_f32_16x16x32_bf16 a[216:219],v[112:115],a[64:67],a[216:219]
v_mfma_f32_16x16x32_bf16 a[216:219],v[116:119],a[72:75],a[216:219]
v_mfma_f32_16x16x32_bf16 a[216:219],v[120:123],a[80:83],a[216:219]
buffer_load_dwordx4 v[164:167],v6,s[28:31],s89 offen offset:1024 sc1
v_mfma_f32_16x16x32_bf16 a[216:219],v[124:127],a[88:91],a[216:219]
v_mfma_f32_16x16x32_bf16 a[220:223],v[124:127],a[120:123],a[220:223]
ds_read_b64_tr_b16 v[124:125],v2 offset:30880
ds_read_b64_tr_b16 v[126:127],v2 offset:36000
v_mfma_f32_16x16x32_bf16 a[220:223],v[120:123],a[112:115],a[220:223]
ds_read_b64_tr_b16 v[120:121],v2 offset:20640
v_mfma_f32_16x16x32_bf16 a[220:223],v[116:119],a[104:107],a[220:223]
ds_read_b64_tr_b16 v[116:117],v2 offset:10400
v_mfma_f32_16x16x32_bf16 a[220:223],v[112:115],a[96:99],a[220:223]
buffer_load_dwordx4 v[148:151],v6,s[28:31],s88 offen offset:1024 sc1
v_mfma_f32_16x16x32_bf16 a[224:227],v[64:67],a[68:71],a[224:227]
ds_read_b64_tr_b16 v[112:113],v2 offset:160
ds_read_b64_tr_b16 v[114:115],v2 offset:5280
v_mfma_f32_16x16x32_bf16 a[224:227],v[68:71],a[76:79],a[224:227]
ds_read_b64_tr_b16 v[118:119],v2 offset:15520
v_mfma_f32_16x16x32_bf16 a[224:227],v[72:75],a[84:87],a[224:227]
ds_read_b64_tr_b16 v[122:123],v2 offset:25760
v_mfma_f32_16x16x32_bf16 a[224:227],v[76:79],a[92:95],a[224:227]
v_mfma_f32_16x16x32_bf16 a[228:231],v[76:79],a[124:127],a[228:231]
buffer_load_dwordx4 v[244:247],v6,s[28:31],s90 offen offset:2048 sc1
v_mfma_f32_16x16x32_bf16 a[228:231],v[72:75],a[116:119],a[228:231]
v_mfma_f32_16x16x32_bf16 a[228:231],v[68:71],a[108:111],a[228:231]
v_mfma_f32_16x16x32_bf16 a[228:231],v[64:67],a[100:103],a[228:231]
v_mfma_f32_16x16x32_bf16 a[232:235],v[64:67],a[64:67],a[232:235]
v_mfma_f32_16x16x32_bf16 a[232:235],v[68:71],a[72:75],a[232:235]
buffer_load_dwordx4 v[188:191],v28,s[28:31],s90 offen offset:3072 sc1
v_mfma_f32_16x16x32_bf16 a[232:235],v[72:75],a[80:83],a[232:235]
s_waitcnt vmcnt(12)
v_cmp_ne_u32 s[56:57],s52,v4
v_cmp_ne_u32 s[58:59],s53,v5
v_mfma_f32_16x16x32_bf16 a[232:235],v[76:79],a[88:91],a[232:235]
s_or_b64 s[56:57],s[56:57],s[58:59]
s_cbranch_scc1 L_fr7_%=
L_fo7_%=:
s_lshl_b32 s91,s64,6
v_mfma_f32_16x16x32_bf16 a[236:239],v[76:79],a[120:123],a[236:239]
ds_read_b64_tr_b16 v[76:77],v1 offset:30912
ds_read_b64_tr_b16 v[78:79],v1 offset:36032
v_mfma_f32_16x16x32_bf16 a[236:239],v[72:75],a[112:115],a[236:239]
ds_read_b64_tr_b16 v[72:73],v1 offset:20672
v_bfe_u32 v3,v6,4,2
v_mfma_f32_16x16x32_bf16 a[236:239],v[68:71],a[104:107],a[236:239]
buffer_load_dwordx4 a[128:131],v6,s[28:31],s20 offen sc1
v_mfma_f32_16x16x32_bf16 a[236:239],v[64:67],a[96:99],a[236:239]
s_waitcnt lgkmcnt(3)
ds_read_b64_tr_b16 v[64:65],v1 offset:192
ds_read_b64_tr_b16 v[66:67],v1 offset:5312
v_mfma_f32_16x16x32_bf16 a[240:243],v[80:83],a[68:71],a[240:243]
ds_read_b64_tr_b16 v[68:69],v1 offset:10432
v_min_u32 v3,2,v3
v_mfma_f32_16x16x32_bf16 a[240:243],v[84:87],a[76:79],a[240:243]
ds_read_b64_tr_b16 v[70:71],v1 offset:15552
v_add3_u32 v3,v3,s63,1
v_mfma_f32_16x16x32_bf16 a[240:243],v[88:91],a[84:87],a[240:243]
ds_read_b64_tr_b16 v[74:75],v1 offset:25792
v_and_b32 v3,3,v3
v_mfma_f32_16x16x32_bf16 a[240:243],v[92:95],a[92:95],a[240:243]
buffer_load_dwordx4 v[132:135],v6,s[28:31],s87 offen offset:1024 sc1
v_mfma_f32_16x16x32_bf16 a[244:247],v[92:95],a[124:127],a[244:247]
v_lshlrev_b32 v3,8,v3
v_mfma_f32_16x16x32_bf16 a[244:247],v[88:91],a[116:119],a[244:247]
v_mfma_f32_16x16x32_bf16 a[244:247],v[84:87],a[108:111],a[244:247]
v_mfma_f32_16x16x32_bf16 a[244:247],v[80:83],a[100:103],a[244:247]
v_mfma_f32_16x16x32_bf16 a[248:251],v[80:83],a[64:67],a[248:251]
buffer_load_dwordx4 v[172:175],v28,s[28:31],s89 offen offset:3072 sc1
v_mfma_f32_16x16x32_bf16 a[248:251],v[84:87],a[72:75],a[248:251]
v_mfma_f32_16x16x32_bf16 a[248:251],v[88:91],a[80:83],a[248:251]
v_mfma_f32_16x16x32_bf16 a[248:251],v[92:95],a[88:91],a[248:251]
v_mfma_f32_16x16x32_bf16 a[252:255],v[92:95],a[120:123],a[252:255]
ds_read_b64_tr_b16 v[92:93],v2 offset:30944
ds_read_b64_tr_b16 v[94:95],v2 offset:36064
v_mfma_f32_16x16x32_bf16 a[252:255],v[88:91],a[112:115],a[252:255]
buffer_load_dwordx4 a[144:147],v6,s[28:31],s21 offen sc1
v_mfma_f32_16x16x32_bf16 a[252:255],v[84:87],a[104:107],a[252:255]
ds_read_b64_tr_b16 v[84:85],v2 offset:10464
ds_read_b64_tr_b16 v[86:87],v2 offset:15584
v_mfma_f32_16x16x32_bf16 a[252:255],v[80:83],a[96:99],a[252:255]
ds_read_b64_tr_b16 v[80:81],v2 offset:224
v_mfma_f32_16x16x32_bf16 a[0:3],v[96:99],a[68:71],a[0:3]
ds_read_b64_tr_b16 v[82:83],v2 offset:5344
v_mfma_f32_16x16x32_bf16 a[0:3],v[100:103],a[76:79],a[0:3]
ds_read_b64_tr_b16 v[88:89],v2 offset:20704
v_mfma_f32_16x16x32_bf16 a[0:3],v[104:107],a[84:87],a[0:3]
buffer_load_dwordx4 v[156:159],v28,s[28:31],s88 offen offset:3072 sc1
v_mfma_f32_16x16x32_bf16 a[0:3],v[108:111],a[92:95],a[0:3]
ds_read_b64_tr_b16 v[90:91],v2 offset:25824
v_mfma_f32_16x16x32_bf16 a[4:7],v[108:111],a[124:127],a[4:7]
v_mfma_f32_16x16x32_bf16 a[4:7],v[104:107],a[116:119],a[4:7]
v_mfma_f32_16x16x32_bf16 a[4:7],v[100:103],a[108:111],a[4:7]
v_mfma_f32_16x16x32_bf16 a[4:7],v[96:99],a[100:103],a[4:7]
buffer_load_dwordx4 a[160:163],v6,s[28:31],s22 offen sc1
v_mfma_f32_16x16x32_bf16 a[8:11],v[96:99],a[64:67],a[8:11]
v_mfma_f32_16x16x32_bf16 a[8:11],v[100:103],a[72:75],a[8:11]
v_mfma_f32_16x16x32_bf16 a[8:11],v[104:107],a[80:83],a[8:11]
v_mfma_f32_16x16x32_bf16 a[8:11],v[108:111],a[88:91],a[8:11]
v_mfma_f32_16x16x32_bf16 a[12:15],v[108:111],a[120:123],a[12:15]
buffer_load_dwordx4 v[140:143],v28,s[28:31],s87 offen offset:3072 sc1
v_mfma_f32_16x16x32_bf16 a[12:15],v[104:107],a[112:115],a[12:15]
s_add_u32 s87,s64,2
ds_read_b64_tr_b16 v[104:105],v1 offset:20736
ds_read_b64_tr_b16 v[106:107],v1 offset:25856
v_mfma_f32_16x16x32_bf16 a[12:15],v[100:103],a[104:107],a[12:15]
s_and_b32 s87,s87,3
s_lshl_b32 s87,s87,1
ds_read_b64_tr_b16 v[100:101],v1 offset:10496
v_mfma_f32_16x16x32_bf16 a[12:15],v[96:99],a[96:99],a[12:15]
s_or_b32 s87,s87,1
s_lshl_b32 s87,s87,16
ds_read_b64_tr_b16 v[96:97],v1 offset:256
v_mfma_f32_16x16x32_bf16 a[16:19],v[112:115],a[68:71],a[16:19]
s_add_u32 s89,s87,8192
s_add_u32 s90,s87,12288
ds_read_b64_tr_b16 v[98:99],v1 offset:5376
v_mfma_f32_16x16x32_bf16 a[16:19],v[116:119],a[76:79],a[16:19]
buffer_load_dwordx4 a[176:179],v6,s[28:31],s23 offen sc1
v_mfma_f32_16x16x32_bf16 a[16:19],v[120:123],a[84:87],a[16:19]
s_add_u32 s88,s87,4096
ds_read_b64_tr_b16 v[102:103],v1 offset:15616
ds_read_b64_tr_b16 v[108:109],v1 offset:30976
v_mfma_f32_16x16x32_bf16 a[16:19],v[124:127],a[92:95],a[16:19]
ds_read_b64_tr_b16 v[110:111],v1 offset:36096
v_add_u32 v1,0x1e000,v1
v_mfma_f32_16x16x32_bf16 a[20:23],v[124:127],a[124:127],a[20:23]
v_mfma_f32_16x16x32_bf16 a[20:23],v[120:123],a[116:119],a[20:23]
v_mfma_f32_16x16x32_bf16 a[20:23],v[116:119],a[108:111],a[20:23]
v_mfma_f32_16x16x32_bf16 a[20:23],v[112:115],a[100:103],a[20:23]
v_mfma_f32_16x16x32_bf16 a[24:27],v[112:115],a[64:67],a[24:27]
buffer_load_dwordx4 v24,s[48:51],s83 offen lds
v_mfma_f32_16x16x32_bf16 a[24:27],v[116:119],a[72:75],a[24:27]
v_mfma_f32_16x16x32_bf16 a[24:27],v[120:123],a[80:83],a[24:27]
v_mfma_f32_16x16x32_bf16 a[24:27],v[124:127],a[88:91],a[24:27]
)A"
R"A(v_mfma_f32_16x16x32_bf16 a[28:31],v[124:127],a[120:123],a[28:31]
ds_read_b64_tr_b16 v[124:125],v2 offset:31008
ds_read_b64_tr_b16 v[126:127],v2 offset:36128
v_mfma_f32_16x16x32_bf16 a[28:31],v[120:123],a[112:115],a[28:31]
buffer_load_dwordx4 a[180:183],v6,s[28:31],s23 offen offset:1024 sc1
v_mfma_f32_16x16x32_bf16 a[28:31],v[116:119],a[104:107],a[28:31]
ds_read_b64_tr_b16 v[116:117],v2 offset:10528
ds_read_b64_tr_b16 v[118:119],v2 offset:15648
v_mfma_f32_16x16x32_bf16 a[28:31],v[112:115],a[96:99],a[28:31]
s_waitcnt lgkmcnt(4)
ds_read_b64_tr_b16 v[112:113],v2 offset:288
v_mfma_f32_16x16x32_bf16 v[128:131],v[64:67],a[68:71],v[128:131]
ds_read_b64_tr_b16 v[114:115],v2 offset:5408
v_mfma_f32_16x16x32_bf16 v[128:131],v[68:71],a[76:79],v[128:131]
ds_read_b64_tr_b16 v[120:121],v2 offset:20768
v_mfma_f32_16x16x32_bf16 v[128:131],v[72:75],a[84:87],v[128:131]
ds_read_b64_tr_b16 v[122:123],v2 offset:25888
v_add_u32 v2,0x1e000,v2
v_mfma_f32_16x16x32_bf16 v[128:131],v[76:79],a[92:95],v[128:131]
v_mfma_f32_16x16x32_bf16 v[136:139],v[76:79],a[124:127],v[136:139]
buffer_load_dwordx4 v25,s[48:51],s83 offen offset:1024 lds
v_mfma_f32_16x16x32_bf16 v[136:139],v[72:75],a[116:119],v[136:139]
v_mfma_f32_16x16x32_bf16 v[136:139],v[68:71],a[108:111],v[136:139]
v_mfma_f32_16x16x32_bf16 v[136:139],v[64:67],a[100:103],v[136:139]
v_mfma_f32_16x16x32_bf16 v[144:147],v[64:67],a[64:67],v[144:147]
v_mfma_f32_16x16x32_bf16 v[144:147],v[68:71],a[72:75],v[144:147]
buffer_load_dwordx4 a[164:167],v6,s[28:31],s22 offen offset:1024 sc1
v_mfma_f32_16x16x32_bf16 v[144:147],v[72:75],a[80:83],v[144:147]
v_mfma_f32_16x16x32_bf16 v[144:147],v[76:79],a[88:91],v[144:147]
v_mfma_f32_16x16x32_bf16 v[152:155],v[76:79],a[120:123],v[152:155]
v_mfma_f32_16x16x32_bf16 v[152:155],v[72:75],a[112:115],v[152:155]
v_mfma_f32_16x16x32_bf16 v[152:155],v[68:71],a[104:107],v[152:155]
v_mfma_f32_16x16x32_bf16 v[152:155],v[64:67],a[96:99],v[152:155]
s_barrier
v_mfma_f32_16x16x32_bf16 v[160:163],v[80:83],a[68:71],v[160:163]
buffer_load_dwordx4 v26,s[48:51],s83 offen offset:2048 lds
v_mfma_f32_16x16x32_bf16 v[160:163],v[84:87],a[76:79],v[160:163]
ds_read_b64_tr_b16 v[64:65],v1
ds_read_b64_tr_b16 v[66:67],v1 offset:5120
v_mfma_f32_16x16x32_bf16 v[160:163],v[88:91],a[84:87],v[160:163]
ds_read_b64_tr_b16 v[68:69],v1 offset:10240
v_mfma_f32_16x16x32_bf16 v[160:163],v[92:95],a[92:95],v[160:163]
ds_read_b64_tr_b16 v[70:71],v1 offset:15360
v_mfma_f32_16x16x32_bf16 v[168:171],v[92:95],a[124:127],v[168:171]
ds_read_b64_tr_b16 v[72:73],v1 offset:20480
v_mfma_f32_16x16x32_bf16 v[168:171],v[88:91],a[116:119],v[168:171]
buffer_load_dwordx4 a[148:151],v6,s[28:31],s21 offen offset:1024 sc1
v_mfma_f32_16x16x32_bf16 v[168:171],v[84:87],a[108:111],v[168:171]
ds_read_b64_tr_b16 v[74:75],v1 offset:25600
ds_read_b64_tr_b16 v[76:77],v1 offset:30720
v_mfma_f32_16x16x32_bf16 v[168:171],v[80:83],a[100:103],v[168:171]
ds_read_b64_tr_b16 v[78:79],v1 offset:35840
v_mfma_f32_16x16x32_bf16 v[176:179],v[80:83],a[64:67],v[176:179]
v_mfma_f32_16x16x32_bf16 v[176:179],v[84:87],a[72:75],v[176:179]
v_mfma_f32_16x16x32_bf16 v[176:179],v[88:91],a[80:83],v[176:179]
v_mfma_f32_16x16x32_bf16 v[176:179],v[92:95],a[88:91],v[176:179]
v_mfma_f32_16x16x32_bf16 v[184:187],v[92:95],a[120:123],v[184:187]
buffer_load_dwordx4 v27,s[48:51],s83 offen offset:3072 lds
v_mfma_f32_16x16x32_bf16 v[184:187],v[88:91],a[112:115],v[184:187]
s_add_u32 m0,s86,45056
ds_read_b64_tr_b16 v[88:89],v2 offset:20512
ds_read_b64_tr_b16 v[90:91],v2 offset:25632
v_mfma_f32_16x16x32_bf16 v[184:187],v[84:87],a[104:107],v[184:187]
ds_read_b64_tr_b16 v[84:85],v2 offset:10272
v_mfma_f32_16x16x32_bf16 v[184:187],v[80:83],a[96:99],v[184:187]
ds_read_b64_tr_b16 v[80:81],v2 offset:32
v_mfma_f32_16x16x32_bf16 v[192:195],v[96:99],a[68:71],v[192:195]
ds_read_b64_tr_b16 v[82:83],v2 offset:5152
v_mfma_f32_16x16x32_bf16 v[192:195],v[100:103],a[76:79],v[192:195]
buffer_load_dwordx4 a[132:135],v6,s[28:31],s20 offen offset:1024 sc1
v_mfma_f32_16x16x32_bf16 v[192:195],v[104:107],a[84:87],v[192:195]
ds_read_b64_tr_b16 v[86:87],v2 offset:15392
ds_read_b64_tr_b16 v[92:93],v2 offset:30752
v_mfma_f32_16x16x32_bf16 v[192:195],v[108:111],a[92:95],v[192:195]
ds_read_b64_tr_b16 v[94:95],v2 offset:35872
v_mfma_f32_16x16x32_bf16 v[200:203],v[108:111],a[124:127],v[200:203]
v_mfma_f32_16x16x32_bf16 v[200:203],v[104:107],a[116:119],v[200:203]
v_mfma_f32_16x16x32_bf16 v[200:203],v[100:103],a[108:111],v[200:203]
v_mfma_f32_16x16x32_bf16 v[200:203],v[96:99],a[100:103],v[200:203]
v_mfma_f32_16x16x32_bf16 v[208:211],v[96:99],a[64:67],v[208:211]
buffer_load_dwordx4 v7,s[48:51],s83 offen lds
v_mfma_f32_16x16x32_bf16 v[208:211],v[100:103],a[72:75],v[208:211]
s_add_u32 m0,s86,46080
s_mul_i32 s83,s63,20480
v_mfma_f32_16x16x32_bf16 v[208:211],v[104:107],a[80:83],v[208:211]
v_mfma_f32_16x16x32_bf16 v[208:211],v[108:111],a[88:91],v[208:211]
v_mfma_f32_16x16x32_bf16 v[216:219],v[108:111],a[120:123],v[216:219]
ds_read_b64_tr_b16 v[108:109],v1 offset:30784
ds_read_b64_tr_b16 v[110:111],v1 offset:35904
v_mfma_f32_16x16x32_bf16 v[216:219],v[104:107],a[112:115],v[216:219]
buffer_load_dwordx4 a[136:139],v6,s[28:31],s20 offen offset:2048 sc1
v_mfma_f32_16x16x32_bf16 v[216:219],v[100:103],a[104:107],v[216:219]
ds_read_b64_tr_b16 v[100:101],v1 offset:10304
ds_read_b64_tr_b16 v[102:103],v1 offset:15424
v_mfma_f32_16x16x32_bf16 v[216:219],v[96:99],a[96:99],v[216:219]
s_waitcnt lgkmcnt(4)
ds_read_b64_tr_b16 v[96:97],v1 offset:64
v_mfma_f32_16x16x32_bf16 v[224:227],v[112:115],a[68:71],v[224:227]
ds_read_b64_tr_b16 v[98:99],v1 offset:5184
v_mfma_f32_16x16x32_bf16 v[224:227],v[116:119],a[76:79],v[224:227]
ds_read_b64_tr_b16 v[104:105],v1 offset:20544
v_mfma_f32_16x16x32_bf16 v[224:227],v[120:123],a[84:87],v[224:227]
ds_read_b64_tr_b16 v[106:107],v1 offset:25664
v_mfma_f32_16x16x32_bf16 v[224:227],v[124:127],a[92:95],v[224:227]
v_mfma_f32_16x16x32_bf16 v[232:235],v[124:127],a[124:127],v[232:235]
buffer_load_dwordx4 v24,s[48:51],s84 offen lds
v_mfma_f32_16x16x32_bf16 v[232:235],v[120:123],a[116:119],v[232:235]
v_mfma_f32_16x16x32_bf16 v[232:235],v[116:119],a[108:111],v[232:235]
v_mfma_f32_16x16x32_bf16 v[232:235],v[112:115],a[100:103],v[232:235]
v_mfma_f32_16x16x32_bf16 v[240:243],v[112:115],a[64:67],v[240:243]
v_mfma_f32_16x16x32_bf16 v[240:243],v[116:119],a[72:75],v[240:243]
buffer_load_dwordx4 a[152:155],v6,s[28:31],s21 offen offset:2048 sc1
v_mfma_f32_16x16x32_bf16 v[240:243],v[120:123],a[80:83],v[240:243]
v_mfma_f32_16x16x32_bf16 v[240:243],v[124:127],a[88:91],v[240:243]
v_mfma_f32_16x16x32_bf16 v[248:251],v[124:127],a[120:123],v[248:251]
ds_read_b64_tr_b16 v[124:125],v2 offset:30816
ds_read_b64_tr_b16 v[126:127],v2 offset:35936
v_mfma_f32_16x16x32_bf16 v[248:251],v[120:123],a[112:115],v[248:251]
ds_read_b64_tr_b16 v[120:121],v2 offset:20576
v_mfma_f32_16x16x32_bf16 v[248:251],v[116:119],a[104:107],v[248:251]
ds_read_b64_tr_b16 v[116:117],v2 offset:10336
v_mfma_f32_16x16x32_bf16 v[248:251],v[112:115],a[96:99],v[248:251]
s_waitcnt vmcnt(19)
ds_read_b64_tr_b16 v[112:113],v2 offset:96
v_mfma_f32_16x16x32_bf16 a[192:195],v[64:67],v[32:35],a[192:195]
buffer_load_dwordx4 v25,s[48:51],s84 offen offset:1024 lds
v_mfma_f32_16x16x32_bf16 a[192:195],v[68:71],v[36:39],a[192:195]
ds_read_b64_tr_b16 v[114:115],v2 offset:5216
ds_read_b64_tr_b16 v[118:119],v2 offset:15456
v_mfma_f32_16x16x32_bf16 a[192:195],v[72:75],v[40:43],a[192:195]
ds_read_b64_tr_b16 v[122:123],v2 offset:25696
v_mfma_f32_16x16x32_bf16 a[192:195],v[76:79],v[44:47],a[192:195]
v_mfma_f32_16x16x32_bf16 a[196:199],v[76:79],v[180:183],a[196:199]
v_mfma_f32_16x16x32_bf16 a[196:199],v[72:75],v[164:167],a[196:199]
buffer_load_dwordx4 a[168:171],v6,s[28:31],s22 offen offset:2048 sc1
v_mfma_f32_16x16x32_bf16 a[196:199],v[68:71],v[148:151],a[196:199]
s_waitcnt vmcnt(19)
v_mfma_f32_16x16x32_bf16 a[196:199],v[64:67],v[132:135],a[196:199]
v_mfma_f32_16x16x32_bf16 a[200:203],v[64:67],v[196:199],a[200:203]
v_mfma_f32_16x16x32_bf16 a[200:203],v[68:71],v[212:215],a[200:203]
v_mfma_f32_16x16x32_bf16 a[200:203],v[72:75],v[228:231],a[200:203]
v_mfma_f32_16x16x32_bf16 a[200:203],v[76:79],v[244:247],a[200:203]
v_mfma_f32_16x16x32_bf16 a[204:207],v[76:79],v[188:191],a[204:207]
buffer_load_dwordx4 v26,s[48:51],s84 offen offset:2048 lds
v_mfma_f32_16x16x32_bf16 a[204:207],v[72:75],v[172:175],a[204:207]
s_waitcnt vmcnt(18)
ds_read_b64_tr_b16 v[72:73],v1 offset:20608
ds_read_b64_tr_b16 v[74:75],v1 offset:25728
v_mfma_f32_16x16x32_bf16 a[204:207],v[68:71],v[156:159],a[204:207]
s_waitcnt vmcnt(16)
ds_read_b64_tr_b16 v[68:69],v1 offset:10368
v_mfma_f32_16x16x32_bf16 a[204:207],v[64:67],v[140:143],a[204:207]
ds_read_b64_tr_b16 v[64:65],v1 offset:128
v_mfma_f32_16x16x32_bf16 a[208:211],v[80:83],v[32:35],a[208:211]
ds_read_b64_tr_b16 v[66:67],v1 offset:5248
v_mfma_f32_16x16x32_bf16 a[208:211],v[84:87],v[36:39],a[208:211]
buffer_load_dwordx4 a[184:187],v6,s[28:31],s23 offen offset:2048 sc1
v_mfma_f32_16x16x32_bf16 a[208:211],v[88:91],v[40:43],a[208:211]
ds_read_b64_tr_b16 v[70:71],v1 offset:15488
ds_read_b64_tr_b16 v[76:77],v1 offset:30848
v_mfma_f32_16x16x32_bf16 a[208:211],v[92:95],v[44:47],a[208:211]
ds_read_b64_tr_b16 v[78:79],v1 offset:35968
v_mfma_f32_16x16x32_bf16 a[212:215],v[92:95],v[180:183],a[212:215]
v_mfma_f32_16x16x32_bf16 a[212:215],v[88:91],v[164:167],a[212:215]
v_mfma_f32_16x16x32_bf16 a[212:215],v[84:87],v[148:151],a[212:215]
v_mfma_f32_16x16x32_bf16 a[212:215],v[80:83],v[132:135],a[212:215]
v_mfma_f32_16x16x32_bf16 a[216:219],v[80:83],v[196:199],a[216:219]
buffer_load_dwordx4 v27,s[48:51],s84 offen offset:3072 lds
v_mfma_f32_16x16x32_bf16 a[216:219],v[84:87],v[212:215],a[216:219]
s_add_u32 m0,s86,50176
v_mfma_f32_16x16x32_bf16 a[216:219],v[88:91],v[228:231],a[216:219]
v_mfma_f32_16x16x32_bf16 a[216:219],v[92:95],v[244:247],a[216:219]
v_mfma_f32_16x16x32_bf16 a[220:223],v[92:95],v[188:191],a[220:223]
ds_read_b64_tr_b16 v[92:93],v2 offset:30880
ds_read_b64_tr_b16 v[94:95],v2 offset:36000
v_mfma_f32_16x16x32_bf16 a[220:223],v[88:91],v[172:175],a[220:223]
buffer_load_dwordx4 a[188:191],v28,s[28:31],s23 offen offset:3072 sc1
v_mfma_f32_16x16x32_bf16 a[220:223],v[84:87],v[156:159],a[220:223]
ds_read_b64_tr_b16 v[84:85],v2 offset:10400
ds_read_b64_tr_b16 v[86:87],v2 offset:15520
v_mfma_f32_16x16x32_bf16 a[220:223],v[80:83],v[140:143],a[220:223]
s_waitcnt lgkmcnt(4)
ds_read_b64_tr_b16 v[80:81],v2 offset:160
v_mfma_f32_16x16x32_bf16 a[224:227],v[96:99],v[32:35],a[224:227]
ds_read_b64_tr_b16 v[82:83],v2 offset:5280
v_mfma_f32_16x16x32_bf16 a[224:227],v[100:103],v[36:39],a[224:227]
ds_read_b64_tr_b16 v[88:89],v2 offset:20640
v_mfma_f32_16x16x32_bf16 a[224:227],v[104:107],v[40:43],a[224:227]
ds_read_b64_tr_b16 v[90:91],v2 offset:25760
v_mfma_f32_16x16x32_bf16 a[224:227],v[108:111],v[44:47],a[224:227]
v_mfma_f32_16x16x32_bf16 a[228:231],v[108:111],v[180:183],a[228:231]
buffer_load_dwordx4 v7,s[48:51],s84 offen lds
v_mfma_f32_16x16x32_bf16 a[228:231],v[104:107],v[164:167],a[228:231]
v_mfma_f32_16x16x32_bf16 a[228:231],v[100:103],v[148:151],a[228:231]
v_mfma_f32_16x16x32_bf16 a[228:231],v[96:99],v[132:135],a[228:231]
v_mfma_f32_16x16x32_bf16 a[232:235],v[96:99],v[196:199],a[232:235]
v_mfma_f32_16x16x32_bf16 a[232:235],v[100:103],v[212:215],a[232:235]
buffer_load_dwordx4 a[172:175],v28,s[28:31],s22 offen offset:3072 sc1
v_mfma_f32_16x16x32_bf16 a[232:235],v[104:107],v[228:231],a[232:235]
v_mfma_f32_16x16x32_bf16 a[232:235],v[108:111],v[244:247],a[232:235]
v_mfma_f32_16x16x32_bf16 a[236:239],v[108:111],v[188:191],a[236:239]
ds_read_b64_tr_b16 v[108:109],v1 offset:30912
ds_read_b64_tr_b16 v[110:111],v1 offset:36032
v_mfma_f32_16x16x32_bf16 a[236:239],v[104:107],v[172:175],a[236:239]
ds_read_b64_tr_b16 v[104:105],v1 offset:20672
v_mfma_f32_16x16x32_bf16 a[236:239],v[100:103],v[156:159],a[236:239]
buffer_load_dwordx4 a[84:87],v6,s[28:31],s89 offen sc1
v_mfma_f32_16x16x32_bf16 a[236:239],v[96:99],v[140:143],a[236:239]
ds_read_b64_tr_b16 v[96:97],v1 offset:192
ds_read_b64_tr_b16 v[98:99],v1 offset:5312
v_mfma_f32_16x16x32_bf16 a[240:243],v[112:115],v[32:35],a[240:243]
)A"
R"A(ds_read_b64_tr_b16 v[100:101],v1 offset:10432
v_mfma_f32_16x16x32_bf16 a[240:243],v[116:119],v[36:39],a[240:243]
ds_read_b64_tr_b16 v[102:103],v1 offset:15552
v_mfma_f32_16x16x32_bf16 a[240:243],v[120:123],v[40:43],a[240:243]
ds_read_b64_tr_b16 v[106:107],v1 offset:25792
v_add_u32 v1,0xffff6000,v1
v_mfma_f32_16x16x32_bf16 a[240:243],v[124:127],v[44:47],a[240:243]
buffer_load_dwordx4 a[92:95],v6,s[28:31],s90 offen sc1
v_mfma_f32_16x16x32_bf16 a[244:247],v[124:127],v[180:183],a[244:247]
v_mfma_f32_16x16x32_bf16 a[244:247],v[120:123],v[164:167],a[244:247]
v_mfma_f32_16x16x32_bf16 a[244:247],v[116:119],v[148:151],a[244:247]
v_mfma_f32_16x16x32_bf16 a[244:247],v[112:115],v[132:135],a[244:247]
v_mfma_f32_16x16x32_bf16 a[248:251],v[112:115],v[196:199],a[248:251]
buffer_load_dwordx4 a[156:159],v28,s[28:31],s21 offen offset:3072 sc1
v_mfma_f32_16x16x32_bf16 a[248:251],v[116:119],v[212:215],a[248:251]
v_mfma_f32_16x16x32_bf16 a[248:251],v[120:123],v[228:231],a[248:251]
v_mfma_f32_16x16x32_bf16 a[248:251],v[124:127],v[244:247],a[248:251]
v_mfma_f32_16x16x32_bf16 a[252:255],v[124:127],v[188:191],a[252:255]
ds_read_b64_tr_b16 v[124:125],v2 offset:30944
ds_read_b64_tr_b16 v[126:127],v2 offset:36064
v_mfma_f32_16x16x32_bf16 a[252:255],v[120:123],v[172:175],a[252:255]
buffer_load_dwordx4 a[68:71],v6,s[28:31],s87 offen sc1
v_mfma_f32_16x16x32_bf16 a[252:255],v[116:119],v[156:159],a[252:255]
ds_read_b64_tr_b16 v[116:117],v2 offset:10464
ds_read_b64_tr_b16 v[118:119],v2 offset:15584
v_mfma_f32_16x16x32_bf16 a[252:255],v[112:115],v[140:143],a[252:255]
ds_read_b64_tr_b16 v[112:113],v2 offset:224
v_mfma_f32_16x16x32_bf16 a[0:3],v[64:67],v[32:35],a[0:3]
ds_read_b64_tr_b16 v[114:115],v2 offset:5344
v_mfma_f32_16x16x32_bf16 a[0:3],v[68:71],v[36:39],a[0:3]
ds_read_b64_tr_b16 v[120:121],v2 offset:20704
v_mfma_f32_16x16x32_bf16 a[0:3],v[72:75],v[40:43],a[0:3]
buffer_load_dwordx4 a[140:143],v28,s[28:31],s20 offen offset:3072 sc1
v_mfma_f32_16x16x32_bf16 a[0:3],v[76:79],v[44:47],a[0:3]
ds_read_b64_tr_b16 v[122:123],v2 offset:25824
v_add_u32 v2,0xffff6000,v2
v_mfma_f32_16x16x32_bf16 a[4:7],v[76:79],v[180:183],a[4:7]
v_mfma_f32_16x16x32_bf16 a[4:7],v[72:75],v[164:167],a[4:7]
v_mfma_f32_16x16x32_bf16 a[4:7],v[68:71],v[148:151],a[4:7]
v_mfma_f32_16x16x32_bf16 a[4:7],v[64:67],v[132:135],a[4:7]
buffer_load_dwordx4 a[124:127],v6,s[28:31],s90 offen offset:1024 sc1
v_mfma_f32_16x16x32_bf16 a[8:11],v[64:67],v[196:199],a[8:11]
v_mfma_f32_16x16x32_bf16 a[8:11],v[68:71],v[212:215],a[8:11]
v_mfma_f32_16x16x32_bf16 a[8:11],v[72:75],v[228:231],a[8:11]
v_mfma_f32_16x16x32_bf16 a[8:11],v[76:79],v[244:247],a[8:11]
v_mfma_f32_16x16x32_bf16 a[12:15],v[76:79],v[188:191],a[12:15]
buffer_load_dwordx4 a[116:119],v6,s[28:31],s89 offen offset:1024 sc1
v_mfma_f32_16x16x32_bf16 a[12:15],v[72:75],v[172:175],a[12:15]
ds_read_b64_tr_b16 v[72:73],v1 offset:20480
ds_read_b64_tr_b16 v[74:75],v1 offset:25600
v_mfma_f32_16x16x32_bf16 a[12:15],v[68:71],v[156:159],a[12:15]
ds_read_b64_tr_b16 v[68:69],v1 offset:10240
v_mfma_f32_16x16x32_bf16 a[12:15],v[64:67],v[140:143],a[12:15]
s_waitcnt lgkmcnt(3)
ds_read_b64_tr_b16 v[64:65],v1
v_mfma_f32_16x16x32_bf16 a[16:19],v[80:83],v[32:35],a[16:19]
ds_read_b64_tr_b16 v[66:67],v1 offset:5120
v_mfma_f32_16x16x32_bf16 a[16:19],v[84:87],v[36:39],a[16:19]
buffer_load_dwordx4 a[76:79],v6,s[28:31],s88 offen sc1
v_mfma_f32_16x16x32_bf16 a[16:19],v[88:91],v[40:43],a[16:19]
ds_read_b64_tr_b16 v[70:71],v1 offset:15360
s_nop 0
L_pd22_%=:
s_cbranch_execz L_trtg2S_%=
ds_read_b64_tr_b16 v[76:77],v1 offset:30720
v_mfma_f32_16x16x32_bf16 a[16:19],v[92:95],v[44:47],a[16:19]
ds_read_b64_tr_b16 v[78:79],v1 offset:35840
v_add_u32 v1,0xffff6000,v1
v_mfma_f32_16x16x32_bf16 a[20:23],v[92:95],v[180:183],a[20:23]
v_mfma_f32_16x16x32_bf16 a[20:23],v[88:91],v[164:167],a[20:23]
v_mfma_f32_16x16x32_bf16 a[20:23],v[84:87],v[148:151],a[20:23]
buffer_load_dwordx4 a[108:111],v6,s[28:31],s88 offen offset:1024 sc1
v_mfma_f32_16x16x32_bf16 a[20:23],v[80:83],v[132:135],a[20:23]
v_mfma_f32_16x16x32_bf16 a[24:27],v[80:83],v[196:199],a[24:27]
v_mfma_f32_16x16x32_bf16 a[24:27],v[84:87],v[212:215],a[24:27]
v_mfma_f32_16x16x32_bf16 a[24:27],v[88:91],v[228:231],a[24:27]
v_mfma_f32_16x16x32_bf16 a[24:27],v[92:95],v[244:247],a[24:27]
buffer_load_dwordx4 a[100:103],v6,s[28:31],s87 offen offset:1024 sc1
v_mfma_f32_16x16x32_bf16 a[28:31],v[92:95],v[188:191],a[28:31]
ds_read_b64_tr_b16 v[92:93],v2 offset:30752
ds_read_b64_tr_b16 v[94:95],v2 offset:35872
v_mfma_f32_16x16x32_bf16 a[28:31],v[88:91],v[172:175],a[28:31]
ds_read_b64_tr_b16 v[88:89],v2 offset:20512
v_mfma_f32_16x16x32_bf16 a[28:31],v[84:87],v[156:159],a[28:31]
ds_read_b64_tr_b16 v[84:85],v2 offset:10272
v_mfma_f32_16x16x32_bf16 a[28:31],v[80:83],v[140:143],a[28:31]
ds_read_b64_tr_b16 v[80:81],v2 offset:32
v_mfma_f32_16x16x32_bf16 v[128:131],v[96:99],v[32:35],v[128:131]
buffer_load_dwordx4 a[64:67],v6,s[28:31],s87 offen offset:2048 sc1
v_mfma_f32_16x16x32_bf16 v[128:131],v[100:103],v[36:39],v[128:131]
ds_read_b64_tr_b16 v[82:83],v2 offset:5152
ds_read_b64_tr_b16 v[86:87],v2 offset:15392
v_mfma_f32_16x16x32_bf16 v[128:131],v[104:107],v[40:43],v[128:131]
ds_read_b64_tr_b16 v[90:91],v2 offset:25632
v_add_u32 v2,0xffff6000,v2
v_mfma_f32_16x16x32_bf16 v[128:131],v[108:111],v[44:47],v[128:131]
v_mfma_f32_16x16x32_bf16 v[136:139],v[108:111],v[180:183],v[136:139]
v_mfma_f32_16x16x32_bf16 v[136:139],v[104:107],v[164:167],v[136:139]
buffer_load_dwordx4 a[72:75],v6,s[28:31],s88 offen offset:2048 sc1
v_mfma_f32_16x16x32_bf16 v[136:139],v[100:103],v[148:151],v[136:139]
v_mfma_f32_16x16x32_bf16 v[136:139],v[96:99],v[132:135],v[136:139]
v_mfma_f32_16x16x32_bf16 v[144:147],v[96:99],v[196:199],v[144:147]
v_mfma_f32_16x16x32_bf16 v[144:147],v[100:103],v[212:215],v[144:147]
v_mfma_f32_16x16x32_bf16 v[144:147],v[104:107],v[228:231],v[144:147]
buffer_load_dwordx4 a[80:83],v6,s[28:31],s89 offen offset:2048 sc1
v_mfma_f32_16x16x32_bf16 v[144:147],v[108:111],v[244:247],v[144:147]
v_mfma_f32_16x16x32_bf16 v[152:155],v[108:111],v[188:191],v[152:155]
v_mfma_f32_16x16x32_bf16 v[152:155],v[104:107],v[172:175],v[152:155]
v_mfma_f32_16x16x32_bf16 v[152:155],v[100:103],v[156:159],v[152:155]
v_mfma_f32_16x16x32_bf16 v[152:155],v[96:99],v[140:143],v[152:155]
buffer_load_dwordx4 a[88:91],v6,s[28:31],s90 offen offset:2048 sc1
s_waitcnt vmcnt(15)
s_barrier
v_mfma_f32_16x16x32_bf16 v[160:163],v[112:115],v[32:35],v[160:163]
ds_read_b64_tr_b16 v[96:97],v1
ds_read_b64_tr_b16 v[98:99],v1 offset:5120
v_mfma_f32_16x16x32_bf16 v[160:163],v[116:119],v[36:39],v[160:163]
ds_read_b64_tr_b16 v[100:101],v1 offset:10240
v_mfma_f32_16x16x32_bf16 v[160:163],v[120:123],v[40:43],v[160:163]
ds_read_b64_tr_b16 v[102:103],v1 offset:15360
v_mfma_f32_16x16x32_bf16 v[160:163],v[124:127],v[44:47],v[160:163]
buffer_load_dwordx4 a[120:123],v28,s[28:31],s90 offen offset:3072 sc1
v_mfma_f32_16x16x32_bf16 v[168:171],v[124:127],v[180:183],v[168:171]
ds_read_b64_tr_b16 v[104:105],v1 offset:20480
ds_read_b64_tr_b16 v[106:107],v1 offset:25600
v_mfma_f32_16x16x32_bf16 v[168:171],v[120:123],v[164:167],v[168:171]
ds_read_b64_tr_b16 v[108:109],v1 offset:30720
v_mfma_f32_16x16x32_bf16 v[168:171],v[116:119],v[148:151],v[168:171]
ds_read_b64_tr_b16 v[110:111],v1 offset:35840
v_add_u32 v1,0xa000,v1
v_mfma_f32_16x16x32_bf16 v[168:171],v[112:115],v[132:135],v[168:171]
v_mfma_f32_16x16x32_bf16 v[176:179],v[112:115],v[196:199],v[176:179]
buffer_load_dwordx4 a[112:115],v28,s[28:31],s89 offen offset:3072 sc1
v_mfma_f32_16x16x32_bf16 v[176:179],v[116:119],v[212:215],v[176:179]
v_mfma_f32_16x16x32_bf16 v[176:179],v[120:123],v[228:231],v[176:179]
v_mfma_f32_16x16x32_bf16 v[176:179],v[124:127],v[244:247],v[176:179]
v_mfma_f32_16x16x32_bf16 v[184:187],v[124:127],v[188:191],v[184:187]
ds_read_b64_tr_b16 v[124:125],v2 offset:30752
ds_read_b64_tr_b16 v[126:127],v2 offset:35872
v_mfma_f32_16x16x32_bf16 v[184:187],v[120:123],v[172:175],v[184:187]
buffer_load_dwordx4 a[104:107],v28,s[28:31],s88 offen offset:3072 sc1
v_mfma_f32_16x16x32_bf16 v[184:187],v[116:119],v[156:159],v[184:187]
ds_read_b64_tr_b16 v[116:117],v2 offset:10272
ds_read_b64_tr_b16 v[118:119],v2 offset:15392
v_mfma_f32_16x16x32_bf16 v[184:187],v[112:115],v[140:143],v[184:187]
s_waitcnt lgkmcnt(4)
ds_read_b64_tr_b16 v[112:113],v2 offset:32
v_mfma_f32_16x16x32_bf16 a[192:195],v[64:67],a[128:131],a[192:195]
ds_read_b64_tr_b16 v[114:115],v2 offset:5152
v_mfma_f32_16x16x32_bf16 a[192:195],v[68:71],a[144:147],a[192:195]
ds_read_b64_tr_b16 v[120:121],v2 offset:20512
v_mfma_f32_16x16x32_bf16 a[192:195],v[72:75],a[160:163],a[192:195]
ds_read_b64_tr_b16 v[122:123],v2 offset:25632
v_add_u32 v2,0xa000,v2
v_mfma_f32_16x16x32_bf16 a[192:195],v[76:79],a[176:179],a[192:195]
v_mfma_f32_16x16x32_bf16 a[196:199],v[76:79],a[180:183],a[196:199]
v_mfma_f32_16x16x32_bf16 a[196:199],v[72:75],a[164:167],a[196:199]
buffer_load_dwordx4 a[96:99],v28,s[28:31],s87 offen offset:3072 sc1
v_mfma_f32_16x16x32_bf16 a[196:199],v[68:71],a[148:151],a[196:199]
s_mul_i32 s87,s60,81920
s_add_u32 s87,s87,s66
s_add_u32 s83,s83,s87
v_mfma_f32_16x16x32_bf16 a[196:199],v[64:67],a[132:135],a[196:199]
s_lshl_b32 s66,s63,6
s_add_u32 s87,s87,s66
s_add_u32 s88,s87,20480
v_mfma_f32_16x16x32_bf16 a[200:203],v[64:67],a[136:139],a[200:203]
s_add_u32 s89,s87,40960
s_add_u32 s90,s87,61440
s_add_u32 s66,s77,4096
v_mfma_f32_16x16x32_bf16 a[200:203],v[68:71],a[152:155],a[200:203]
v_mfma_f32_16x16x32_bf16 a[200:203],v[72:75],a[168:171],a[200:203]
v_mfma_f32_16x16x32_bf16 a[200:203],v[76:79],a[184:187],a[200:203]
v_mfma_f32_16x16x32_bf16 a[204:207],v[76:79],a[188:191],a[204:207]
s_waitcnt vmcnt(18)
ds_read_b64_tr_b16 v[76:77],v1 offset:30784
ds_read_b64_tr_b16 v[78:79],v1 offset:35904
v_mfma_f32_16x16x32_bf16 a[204:207],v[72:75],a[172:175],a[204:207]
s_waitcnt vmcnt(15)
ds_read_b64_tr_b16 v[72:73],v1 offset:20544
v_mfma_f32_16x16x32_bf16 a[204:207],v[68:71],a[156:159],a[204:207]
s_waitcnt vmcnt(13)
ds_read_b64_tr_b16 v[68:69],v1 offset:10304
v_mfma_f32_16x16x32_bf16 a[204:207],v[64:67],a[140:143],a[204:207]
ds_read_b64_tr_b16 v[64:65],v1 offset:64
v_mfma_f32_16x16x32_bf16 a[208:211],v[80:83],a[128:131],a[208:211]
ds_read_b64_tr_b16 v[66:67],v1 offset:5184
ds_read_b64_tr_b16 v[70:71],v1 offset:15424
v_mfma_f32_16x16x32_bf16 a[208:211],v[84:87],a[144:147],a[208:211]
ds_read_b64_tr_b16 v[74:75],v1 offset:25664
v_add_u32 v1,0xffff6000,v1
v_mfma_f32_16x16x32_bf16 a[208:211],v[88:91],a[160:163],a[208:211]
v_mfma_f32_16x16x32_bf16 a[208:211],v[92:95],a[176:179],a[208:211]
v_mfma_f32_16x16x32_bf16 a[212:215],v[92:95],a[180:183],a[212:215]
v_mfma_f32_16x16x32_bf16 a[212:215],v[88:91],a[164:167],a[212:215]
v_mfma_f32_16x16x32_bf16 a[212:215],v[84:87],a[148:151],a[212:215]
v_mfma_f32_16x16x32_bf16 a[212:215],v[80:83],a[132:135],a[212:215]
v_mfma_f32_16x16x32_bf16 a[216:219],v[80:83],a[136:139],a[216:219]
v_mfma_f32_16x16x32_bf16 a[216:219],v[84:87],a[152:155],a[216:219]
v_mfma_f32_16x16x32_bf16 a[216:219],v[88:91],a[168:171],a[216:219]
v_mfma_f32_16x16x32_bf16 a[216:219],v[92:95],a[184:187],a[216:219]
v_mfma_f32_16x16x32_bf16 a[220:223],v[92:95],a[188:191],a[220:223]
ds_read_b64_tr_b16 v[92:93],v2 offset:30816
ds_read_b64_tr_b16 v[94:95],v2 offset:35936
v_mfma_f32_16x16x32_bf16 a[220:223],v[88:91],a[172:175],a[220:223]
ds_read_b64_tr_b16 v[88:89],v2 offset:20576
v_mfma_f32_16x16x32_bf16 a[220:223],v[84:87],a[156:159],a[220:223]
ds_read_b64_tr_b16 v[84:85],v2 offset:10336
v_mfma_f32_16x16x32_bf16 a[220:223],v[80:83],a[140:143],a[220:223]
ds_read_b64_tr_b16 v[80:81],v2 offset:96
v_mfma_f32_16x16x32_bf16 a[192:195],v[96:99],a[68:71],a[192:195]
s_waitcnt vmcnt(10)
ds_read_b64_tr_b16 v[82:83],v2 offset:5216
ds_read_b64_tr_b16 v[86:87],v2 offset:15456
v_mfma_f32_16x16x32_bf16 a[192:195],v[100:103],a[76:79],a[192:195]
ds_read_b64_tr_b16 v[90:91],v2 offset:25696
v_add_u32 v2,0xffff6000,v2
v_mfma_f32_16x16x32_bf16 a[192:195],v[104:107],a[84:87],a[192:195]
v_mfma_f32_16x16x32_bf16 a[192:195],v[108:111],a[92:95],a[192:195]
v_mfma_f32_16x16x32_bf16 a[196:199],v[108:111],a[124:127],a[196:199]
v_mfma_f32_16x16x32_bf16 a[196:199],v[104:107],a[116:119],a[196:199]
)A"
R"A(s_waitcnt vmcnt(9)
v_mfma_f32_16x16x32_bf16 a[196:199],v[100:103],a[108:111],a[196:199]
s_waitcnt vmcnt(8)
v_mfma_f32_16x16x32_bf16 a[196:199],v[96:99],a[100:103],a[196:199]
s_waitcnt vmcnt(7)
v_mfma_f32_16x16x32_bf16 a[200:203],v[96:99],a[64:67],a[200:203]
s_waitcnt vmcnt(6)
v_mfma_f32_16x16x32_bf16 a[200:203],v[100:103],a[72:75],a[200:203]
s_waitcnt vmcnt(5)
v_mfma_f32_16x16x32_bf16 a[200:203],v[104:107],a[80:83],a[200:203]
s_waitcnt vmcnt(4)
v_mfma_f32_16x16x32_bf16 a[200:203],v[108:111],a[88:91],a[200:203]
s_waitcnt vmcnt(3)
v_accvgpr_read_b32 v8,a192
v_accvgpr_read_b32 v9,a193
v_mfma_f32_16x16x32_bf16 a[204:207],v[108:111],a[120:123],a[204:207]
s_waitcnt vmcnt(2)
v_accvgpr_read_b32 v10,a194
v_accvgpr_read_b32 v11,a195
v_mfma_f32_16x16x32_bf16 a[204:207],v[104:107],a[112:115],a[204:207]
s_waitcnt vmcnt(1)
v_cvt_pk_bf16_f32 v8,v8,v9
v_cvt_pk_bf16_f32 v9,v10,v11
v_mfma_f32_16x16x32_bf16 a[204:207],v[100:103],a[104:107],a[204:207]
s_waitcnt vmcnt(0)
v_accvgpr_read_b32 v16,a196
v_accvgpr_read_b32 v17,a197
v_mfma_f32_16x16x32_bf16 a[204:207],v[96:99],a[96:99],a[204:207]
s_waitcnt lgkmcnt(0)
v_accvgpr_read_b32 v18,a198
v_accvgpr_read_b32 v19,a199
v_mfma_f32_16x16x32_bf16 a[208:211],v[112:115],a[68:71],a[208:211]
v_cvt_pk_bf16_f32 v16,v16,v17
v_cvt_pk_bf16_f32 v17,v18,v19
v_mfma_f32_16x16x32_bf16 a[208:211],v[116:119],a[76:79],a[208:211]
ds_read_b64_tr_b16 v[96:97],v1 offset:64
ds_read_b64_tr_b16 v[98:99],v1 offset:5184
v_mfma_f32_16x16x32_bf16 a[208:211],v[120:123],a[84:87],a[208:211]
ds_read_b64_tr_b16 v[100:101],v1 offset:10304
v_mfma_f32_16x16x32_bf16 a[208:211],v[124:127],a[92:95],a[208:211]
ds_read_b64_tr_b16 v[102:103],v1 offset:15424
v_mfma_f32_16x16x32_bf16 a[212:215],v[124:127],a[124:127],a[212:215]
ds_read_b64_tr_b16 v[104:105],v1 offset:20544
v_mfma_f32_16x16x32_bf16 a[212:215],v[120:123],a[116:119],a[212:215]
ds_read_b64_tr_b16 v[106:107],v1 offset:25664
ds_read_b64_tr_b16 v[108:109],v1 offset:30784
v_mfma_f32_16x16x32_bf16 a[212:215],v[116:119],a[108:111],a[212:215]
ds_read_b64_tr_b16 v[110:111],v1 offset:35904
v_add_u32 v1,0xa000,v1
v_mfma_f32_16x16x32_bf16 a[212:215],v[112:115],a[100:103],a[212:215]
v_accvgpr_read_b32 v12,a208
v_accvgpr_read_b32 v13,a209
v_mfma_f32_16x16x32_bf16 a[216:219],v[112:115],a[64:67],a[216:219]
v_accvgpr_read_b32 v14,a210
v_accvgpr_read_b32 v15,a211
v_mfma_f32_16x16x32_bf16 a[216:219],v[116:119],a[72:75],a[216:219]
v_cvt_pk_bf16_f32 v10,v12,v13
v_cvt_pk_bf16_f32 v11,v14,v15
v_mfma_f32_16x16x32_bf16 a[216:219],v[120:123],a[80:83],a[216:219]
buffer_store_dwordx4 v[8:11],v6,s[36:39],s74 offen sc1
v_mfma_f32_16x16x32_bf16 a[216:219],v[124:127],a[88:91],a[216:219]
v_accvgpr_read_b32 v20,a212
v_accvgpr_read_b32 v21,a213
v_mfma_f32_16x16x32_bf16 a[220:223],v[124:127],a[120:123],a[220:223]
v_accvgpr_read_b32 v22,a214
v_accvgpr_read_b32 v23,a215
v_mfma_f32_16x16x32_bf16 a[220:223],v[120:123],a[112:115],a[220:223]
v_cvt_pk_bf16_f32 v18,v20,v21
v_cvt_pk_bf16_f32 v19,v22,v23
v_mfma_f32_16x16x32_bf16 a[220:223],v[116:119],a[104:107],a[220:223]
v_accvgpr_read_b32 v8,a200
v_accvgpr_read_b32 v9,a201
v_mfma_f32_16x16x32_bf16 a[220:223],v[112:115],a[96:99],a[220:223]
v_accvgpr_read_b32 v10,a202
v_accvgpr_read_b32 v11,a203
v_mfma_f32_16x16x32_bf16 a[224:227],v[64:67],a[128:131],a[224:227]
v_accvgpr_read_b32 v12,a216
v_accvgpr_read_b32 v13,a217
v_mfma_f32_16x16x32_bf16 a[224:227],v[68:71],a[144:147],a[224:227]
v_accvgpr_read_b32 v14,a218
v_accvgpr_read_b32 v15,a219
v_mfma_f32_16x16x32_bf16 a[224:227],v[72:75],a[160:163],a[224:227]
v_cvt_pk_bf16_f32 v8,v8,v9
v_cvt_pk_bf16_f32 v9,v10,v11
v_mfma_f32_16x16x32_bf16 a[224:227],v[76:79],a[176:179],a[224:227]
v_cvt_pk_bf16_f32 v10,v12,v13
v_cvt_pk_bf16_f32 v11,v14,v15
v_mfma_f32_16x16x32_bf16 a[228:231],v[76:79],a[180:183],a[228:231]
ds_read_b64_tr_b16 v[112:113],v2 offset:96
ds_read_b64_tr_b16 v[114:115],v2 offset:5216
v_mfma_f32_16x16x32_bf16 a[228:231],v[72:75],a[164:167],a[228:231]
buffer_store_dwordx4 v[16:19],v6,s[36:39],s74 offen offset:1024 sc1
v_mfma_f32_16x16x32_bf16 a[228:231],v[68:71],a[148:151],a[228:231]
ds_read_b64_tr_b16 v[116:117],v2 offset:10336
ds_read_b64_tr_b16 v[118:119],v2 offset:15456
v_mfma_f32_16x16x32_bf16 a[228:231],v[64:67],a[132:135],a[228:231]
ds_read_b64_tr_b16 v[120:121],v2 offset:20576
v_accvgpr_read_b32 v16,a204
v_mfma_f32_16x16x32_bf16 a[232:235],v[64:67],a[136:139],a[232:235]
ds_read_b64_tr_b16 v[122:123],v2 offset:25696
v_accvgpr_read_b32 v17,a205
v_mfma_f32_16x16x32_bf16 a[232:235],v[68:71],a[152:155],a[232:235]
ds_read_b64_tr_b16 v[124:125],v2 offset:30816
v_accvgpr_read_b32 v18,a206
v_mfma_f32_16x16x32_bf16 a[232:235],v[72:75],a[168:171],a[232:235]
ds_read_b64_tr_b16 v[126:127],v2 offset:35936
v_accvgpr_read_b32 v19,a207
v_mfma_f32_16x16x32_bf16 a[232:235],v[76:79],a[184:187],a[232:235]
v_accvgpr_read_b32 v20,a220
v_accvgpr_read_b32 v21,a221
v_mfma_f32_16x16x32_bf16 a[236:239],v[76:79],a[188:191],a[236:239]
v_accvgpr_read_b32 v22,a222
v_accvgpr_read_b32 v23,a223
v_mfma_f32_16x16x32_bf16 a[236:239],v[72:75],a[172:175],a[236:239]
v_cvt_pk_bf16_f32 v16,v16,v17
v_cvt_pk_bf16_f32 v17,v18,v19
v_mfma_f32_16x16x32_bf16 a[236:239],v[68:71],a[156:159],a[236:239]
v_cvt_pk_bf16_f32 v18,v20,v21
v_cvt_pk_bf16_f32 v19,v22,v23
v_mfma_f32_16x16x32_bf16 a[236:239],v[64:67],a[140:143],a[236:239]
ds_read_b64_tr_b16 v[64:65],v1 offset:128
ds_read_b64_tr_b16 v[66:67],v1 offset:5248
v_mfma_f32_16x16x32_bf16 a[240:243],v[80:83],a[128:131],a[240:243]
buffer_store_dwordx4 v[8:11],v6,s[36:39],s74 offen offset:2048 sc1
v_mfma_f32_16x16x32_bf16 a[240:243],v[84:87],a[144:147],a[240:243]
ds_read_b64_tr_b16 v[68:69],v1 offset:10368
ds_read_b64_tr_b16 v[70:71],v1 offset:15488
v_mfma_f32_16x16x32_bf16 a[240:243],v[88:91],a[160:163],a[240:243]
ds_read_b64_tr_b16 v[72:73],v1 offset:20608
v_add_u32 v2,0xa000,v2
v_mfma_f32_16x16x32_bf16 a[240:243],v[92:95],a[176:179],a[240:243]
ds_read_b64_tr_b16 v[74:75],v1 offset:25728
v_mfma_f32_16x16x32_bf16 a[244:247],v[92:95],a[180:183],a[244:247]
ds_read_b64_tr_b16 v[76:77],v1 offset:30848
v_mfma_f32_16x16x32_bf16 a[244:247],v[88:91],a[164:167],a[244:247]
ds_read_b64_tr_b16 v[78:79],v1 offset:35968
v_add_u32 v1,0xffff6000,v1
v_mfma_f32_16x16x32_bf16 a[244:247],v[84:87],a[148:151],a[244:247]
v_mfma_f32_16x16x32_bf16 a[244:247],v[80:83],a[132:135],a[244:247]
v_mfma_f32_16x16x32_bf16 a[248:251],v[80:83],a[136:139],a[248:251]
v_mfma_f32_16x16x32_bf16 a[248:251],v[84:87],a[152:155],a[248:251]
v_mfma_f32_16x16x32_bf16 a[248:251],v[88:91],a[168:171],a[248:251]
v_mfma_f32_16x16x32_bf16 a[248:251],v[92:95],a[184:187],a[248:251]
buffer_store_dwordx4 v[16:19],v28,s[36:39],s74 offen offset:3072 sc1
v_mfma_f32_16x16x32_bf16 a[252:255],v[92:95],a[188:191],a[252:255]
ds_read_b64_tr_b16 v[92:93],v2 offset:30880
ds_read_b64_tr_b16 v[94:95],v2 offset:36000
v_mfma_f32_16x16x32_bf16 a[252:255],v[88:91],a[172:175],a[252:255]
ds_read_b64_tr_b16 v[88:89],v2 offset:20640
v_mfma_f32_16x16x32_bf16 a[252:255],v[84:87],a[156:159],a[252:255]
ds_read_b64_tr_b16 v[84:85],v2 offset:10400
v_mfma_f32_16x16x32_bf16 a[252:255],v[80:83],a[140:143],a[252:255]
s_waitcnt lgkmcnt(4)
ds_read_b64_tr_b16 v[80:81],v2 offset:160
v_mfma_f32_16x16x32_bf16 a[224:227],v[96:99],a[68:71],a[224:227]
ds_read_b64_tr_b16 v[82:83],v2 offset:5280
ds_read_b64_tr_b16 v[86:87],v2 offset:15520
v_mfma_f32_16x16x32_bf16 a[224:227],v[100:103],a[76:79],a[224:227]
ds_read_b64_tr_b16 v[90:91],v2 offset:25760
v_add_u32 v2,0xffff6000,v2
v_mfma_f32_16x16x32_bf16 a[224:227],v[104:107],a[84:87],a[224:227]
v_mfma_f32_16x16x32_bf16 a[224:227],v[108:111],a[92:95],a[224:227]
v_mfma_f32_16x16x32_bf16 a[228:231],v[108:111],a[124:127],a[228:231]
v_mfma_f32_16x16x32_bf16 a[228:231],v[104:107],a[116:119],a[228:231]
v_mfma_f32_16x16x32_bf16 a[228:231],v[100:103],a[108:111],a[228:231]
v_mfma_f32_16x16x32_bf16 a[228:231],v[96:99],a[100:103],a[228:231]
v_mfma_f32_16x16x32_bf16 a[232:235],v[96:99],a[64:67],a[232:235]
v_mfma_f32_16x16x32_bf16 a[232:235],v[100:103],a[72:75],a[232:235]
v_mfma_f32_16x16x32_bf16 a[232:235],v[104:107],a[80:83],a[232:235]
v_mfma_f32_16x16x32_bf16 a[232:235],v[108:111],a[88:91],a[232:235]
v_accvgpr_read_b32 v8,a224
v_accvgpr_read_b32 v9,a225
v_mfma_f32_16x16x32_bf16 a[236:239],v[108:111],a[120:123],a[236:239]
v_accvgpr_read_b32 v10,a226
v_accvgpr_read_b32 v11,a227
v_mfma_f32_16x16x32_bf16 a[236:239],v[104:107],a[112:115],a[236:239]
v_cvt_pk_bf16_f32 v8,v8,v9
v_cvt_pk_bf16_f32 v9,v10,v11
v_mfma_f32_16x16x32_bf16 a[236:239],v[100:103],a[104:107],a[236:239]
v_accvgpr_read_b32 v16,a228
v_accvgpr_read_b32 v17,a229
v_mfma_f32_16x16x32_bf16 a[236:239],v[96:99],a[96:99],a[236:239]
v_accvgpr_read_b32 v18,a230
v_accvgpr_read_b32 v19,a231
v_mfma_f32_16x16x32_bf16 a[240:243],v[112:115],a[68:71],a[240:243]
v_cvt_pk_bf16_f32 v16,v16,v17
v_cvt_pk_bf16_f32 v17,v18,v19
v_mfma_f32_16x16x32_bf16 a[240:243],v[116:119],a[76:79],a[240:243]
ds_read_b64_tr_b16 v[96:97],v1 offset:128
ds_read_b64_tr_b16 v[98:99],v1 offset:5248
v_mfma_f32_16x16x32_bf16 a[240:243],v[120:123],a[84:87],a[240:243]
ds_read_b64_tr_b16 v[100:101],v1 offset:10368
v_mfma_f32_16x16x32_bf16 a[240:243],v[124:127],a[92:95],a[240:243]
ds_read_b64_tr_b16 v[102:103],v1 offset:15488
v_mfma_f32_16x16x32_bf16 a[244:247],v[124:127],a[124:127],a[244:247]
ds_read_b64_tr_b16 v[104:105],v1 offset:20608
v_mfma_f32_16x16x32_bf16 a[244:247],v[120:123],a[116:119],a[244:247]
ds_read_b64_tr_b16 v[106:107],v1 offset:25728
ds_read_b64_tr_b16 v[108:109],v1 offset:30848
v_mfma_f32_16x16x32_bf16 a[244:247],v[116:119],a[108:111],a[244:247]
ds_read_b64_tr_b16 v[110:111],v1 offset:35968
v_add_u32 v1,0xa000,v1
v_mfma_f32_16x16x32_bf16 a[244:247],v[112:115],a[100:103],a[244:247]
v_accvgpr_read_b32 v12,a240
v_accvgpr_read_b32 v13,a241
v_mfma_f32_16x16x32_bf16 a[248:251],v[112:115],a[64:67],a[248:251]
v_accvgpr_read_b32 v14,a242
v_accvgpr_read_b32 v15,a243
v_mfma_f32_16x16x32_bf16 a[248:251],v[116:119],a[72:75],a[248:251]
v_cvt_pk_bf16_f32 v10,v12,v13
v_cvt_pk_bf16_f32 v11,v14,v15
v_mfma_f32_16x16x32_bf16 a[248:251],v[120:123],a[80:83],a[248:251]
buffer_store_dwordx4 v[8:11],v6,s[36:39],s75 offen sc1
v_mfma_f32_16x16x32_bf16 a[248:251],v[124:127],a[88:91],a[248:251]
v_accvgpr_read_b32 v20,a244
v_accvgpr_read_b32 v21,a245
v_mfma_f32_16x16x32_bf16 a[252:255],v[124:127],a[120:123],a[252:255]
v_accvgpr_read_b32 v22,a246
v_accvgpr_read_b32 v23,a247
v_mfma_f32_16x16x32_bf16 a[252:255],v[120:123],a[112:115],a[252:255]
v_cvt_pk_bf16_f32 v18,v20,v21
v_cvt_pk_bf16_f32 v19,v22,v23
v_mfma_f32_16x16x32_bf16 a[252:255],v[116:119],a[104:107],a[252:255]
v_accvgpr_read_b32 v8,a232
v_accvgpr_read_b32 v9,a233
v_mfma_f32_16x16x32_bf16 a[252:255],v[112:115],a[96:99],a[252:255]
v_accvgpr_read_b32 v10,a234
v_accvgpr_read_b32 v11,a235
v_mfma_f32_16x16x32_bf16 a[0:3],v[64:67],a[128:131],a[0:3]
v_accvgpr_read_b32 v12,a248
v_accvgpr_read_b32 v13,a249
v_mfma_f32_16x16x32_bf16 a[0:3],v[68:71],a[144:147],a[0:3]
v_accvgpr_read_b32 v14,a250
v_accvgpr_read_b32 v15,a251
v_mfma_f32_16x16x32_bf16 a[0:3],v[72:75],a[160:163],a[0:3]
v_cvt_pk_bf16_f32 v8,v8,v9
v_cvt_pk_bf16_f32 v9,v10,v11
v_mfma_f32_16x16x32_bf16 a[0:3],v[76:79],a[176:179],a[0:3]
v_cvt_pk_bf16_f32 v10,v12,v13
v_cvt_pk_bf16_f32 v11,v14,v15
v_mfma_f32_16x16x32_bf16 a[4:7],v[76:79],a[180:183],a[4:7]
ds_read_b64_tr_b16 v[112:113],v2 offset:160
ds_read_b64_tr_b16 v[114:115],v2 offset:5280
v_mfma_f32_16x16x32_bf16 a[4:7],v[72:75],a[164:167],a[4:7]
buffer_store_dwordx4 v[16:19],v6,s[36:39],s75 offen offset:1024 sc1
v_mfma_f32_16x16x32_bf16 a[4:7],v[68:71],a[148:151],a[4:7]
ds_read_b64_tr_b16 v[116:117],v2 offset:10400
ds_read_b64_tr_b16 v[118:119],v2 offset:15520
v_mfma_f32_16x16x32_bf16 a[4:7],v[64:67],a[132:135],a[4:7]
ds_read_b64_tr_b16 v[120:121],v2 offset:20640
v_accvgpr_read_b32 v16,a236
v_mfma_f32_16x16x32_bf16 a[8:11],v[64:67],a[136:139],a[8:11]
ds_read_b64_tr_b16 v[122:123],v2 offset:25760
v_accvgpr_read_b32 v17,a237
v_mfma_f32_16x16x32_bf16 a[8:11],v[68:71],a[152:155],a[8:11]
ds_read_b64_tr_b16 v[124:125],v2 offset:30880
v_accvgpr_read_b32 v18,a238
v_mfma_f32_16x16x32_bf16 a[8:11],v[72:75],a[168:171],a[8:11]
)A"
R"A(ds_read_b64_tr_b16 v[126:127],v2 offset:36000
v_accvgpr_read_b32 v19,a239
v_mfma_f32_16x16x32_bf16 a[8:11],v[76:79],a[184:187],a[8:11]
v_accvgpr_read_b32 v20,a252
v_accvgpr_read_b32 v21,a253
v_mfma_f32_16x16x32_bf16 a[12:15],v[76:79],a[188:191],a[12:15]
v_accvgpr_read_b32 v22,a254
v_accvgpr_read_b32 v23,a255
v_mfma_f32_16x16x32_bf16 a[12:15],v[72:75],a[172:175],a[12:15]
v_cvt_pk_bf16_f32 v16,v16,v17
v_cvt_pk_bf16_f32 v17,v18,v19
v_mfma_f32_16x16x32_bf16 a[12:15],v[68:71],a[156:159],a[12:15]
v_cvt_pk_bf16_f32 v18,v20,v21
v_cvt_pk_bf16_f32 v19,v22,v23
v_mfma_f32_16x16x32_bf16 a[12:15],v[64:67],a[140:143],a[12:15]
s_waitcnt lgkmcnt(3)
ds_read_b64_tr_b16 v[64:65],v1 offset:192
ds_read_b64_tr_b16 v[66:67],v1 offset:5312
v_mfma_f32_16x16x32_bf16 a[16:19],v[80:83],a[128:131],a[16:19]
buffer_store_dwordx4 v[8:11],v6,s[36:39],s75 offen offset:2048 sc1
v_mfma_f32_16x16x32_bf16 a[16:19],v[84:87],a[144:147],a[16:19]
ds_read_b64_tr_b16 v[68:69],v1 offset:10432
ds_read_b64_tr_b16 v[70:71],v1 offset:15552
v_mfma_f32_16x16x32_bf16 a[16:19],v[88:91],a[160:163],a[16:19]
ds_read_b64_tr_b16 v[72:73],v1 offset:20672
v_add_u32 v2,0xa000,v2
v_mfma_f32_16x16x32_bf16 a[16:19],v[92:95],a[176:179],a[16:19]
ds_read_b64_tr_b16 v[74:75],v1 offset:25792
v_mfma_f32_16x16x32_bf16 a[20:23],v[92:95],a[180:183],a[20:23]
ds_read_b64_tr_b16 v[76:77],v1 offset:30912
v_mfma_f32_16x16x32_bf16 a[20:23],v[88:91],a[164:167],a[20:23]
ds_read_b64_tr_b16 v[78:79],v1 offset:36032
v_add_u32 v1,0xffff6000,v1
v_mfma_f32_16x16x32_bf16 a[20:23],v[84:87],a[148:151],a[20:23]
v_mfma_f32_16x16x32_bf16 a[20:23],v[80:83],a[132:135],a[20:23]
v_mfma_f32_16x16x32_bf16 a[24:27],v[80:83],a[136:139],a[24:27]
v_mfma_f32_16x16x32_bf16 a[24:27],v[84:87],a[152:155],a[24:27]
v_mfma_f32_16x16x32_bf16 a[24:27],v[88:91],a[168:171],a[24:27]
v_mfma_f32_16x16x32_bf16 a[24:27],v[92:95],a[184:187],a[24:27]
buffer_store_dwordx4 v[16:19],v28,s[36:39],s75 offen offset:3072 sc1
v_mfma_f32_16x16x32_bf16 a[28:31],v[92:95],a[188:191],a[28:31]
ds_read_b64_tr_b16 v[92:93],v2 offset:30944
ds_read_b64_tr_b16 v[94:95],v2 offset:36064
v_mfma_f32_16x16x32_bf16 a[28:31],v[88:91],a[172:175],a[28:31]
ds_read_b64_tr_b16 v[88:89],v2 offset:20704
v_mfma_f32_16x16x32_bf16 a[28:31],v[84:87],a[156:159],a[28:31]
ds_read_b64_tr_b16 v[84:85],v2 offset:10464
v_mfma_f32_16x16x32_bf16 a[28:31],v[80:83],a[140:143],a[28:31]
ds_read_b64_tr_b16 v[80:81],v2 offset:224
v_mfma_f32_16x16x32_bf16 a[0:3],v[96:99],a[68:71],a[0:3]
ds_read_b64_tr_b16 v[82:83],v2 offset:5344
ds_read_b64_tr_b16 v[86:87],v2 offset:15584
v_mfma_f32_16x16x32_bf16 a[0:3],v[100:103],a[76:79],a[0:3]
ds_read_b64_tr_b16 v[90:91],v2 offset:25824
v_add_u32 v2,0xffff6000,v2
v_mfma_f32_16x16x32_bf16 a[0:3],v[104:107],a[84:87],a[0:3]
v_mfma_f32_16x16x32_bf16 a[0:3],v[108:111],a[92:95],a[0:3]
v_mfma_f32_16x16x32_bf16 a[4:7],v[108:111],a[124:127],a[4:7]
v_mfma_f32_16x16x32_bf16 a[4:7],v[104:107],a[116:119],a[4:7]
v_mfma_f32_16x16x32_bf16 a[4:7],v[100:103],a[108:111],a[4:7]
v_mfma_f32_16x16x32_bf16 a[4:7],v[96:99],a[100:103],a[4:7]
v_mfma_f32_16x16x32_bf16 a[8:11],v[96:99],a[64:67],a[8:11]
v_mfma_f32_16x16x32_bf16 a[8:11],v[100:103],a[72:75],a[8:11]
v_mfma_f32_16x16x32_bf16 a[8:11],v[104:107],a[80:83],a[8:11]
v_mfma_f32_16x16x32_bf16 a[8:11],v[108:111],a[88:91],a[8:11]
v_accvgpr_read_b32 v8,a0
v_accvgpr_read_b32 v9,a1
v_mfma_f32_16x16x32_bf16 a[12:15],v[108:111],a[120:123],a[12:15]
v_accvgpr_read_b32 v10,a2
v_accvgpr_read_b32 v11,a3
v_mfma_f32_16x16x32_bf16 a[12:15],v[104:107],a[112:115],a[12:15]
v_cvt_pk_bf16_f32 v8,v8,v9
v_cvt_pk_bf16_f32 v9,v10,v11
v_mfma_f32_16x16x32_bf16 a[12:15],v[100:103],a[104:107],a[12:15]
v_accvgpr_read_b32 v16,a4
v_accvgpr_read_b32 v17,a5
v_mfma_f32_16x16x32_bf16 a[12:15],v[96:99],a[96:99],a[12:15]
v_accvgpr_read_b32 v18,a6
v_accvgpr_read_b32 v19,a7
v_mfma_f32_16x16x32_bf16 a[16:19],v[112:115],a[68:71],a[16:19]
v_cvt_pk_bf16_f32 v16,v16,v17
v_cvt_pk_bf16_f32 v17,v18,v19
v_mfma_f32_16x16x32_bf16 a[16:19],v[116:119],a[76:79],a[16:19]
s_waitcnt lgkmcnt(0)
ds_read_b64_tr_b16 v[96:97],v1 offset:192
ds_read_b64_tr_b16 v[98:99],v1 offset:5312
v_mfma_f32_16x16x32_bf16 a[16:19],v[120:123],a[84:87],a[16:19]
ds_read_b64_tr_b16 v[100:101],v1 offset:10432
v_mfma_f32_16x16x32_bf16 a[16:19],v[124:127],a[92:95],a[16:19]
ds_read_b64_tr_b16 v[102:103],v1 offset:15552
v_mfma_f32_16x16x32_bf16 a[20:23],v[124:127],a[124:127],a[20:23]
ds_read_b64_tr_b16 v[104:105],v1 offset:20672
v_mfma_f32_16x16x32_bf16 a[20:23],v[120:123],a[116:119],a[20:23]
ds_read_b64_tr_b16 v[106:107],v1 offset:25792
ds_read_b64_tr_b16 v[108:109],v1 offset:30912
v_mfma_f32_16x16x32_bf16 a[20:23],v[116:119],a[108:111],a[20:23]
ds_read_b64_tr_b16 v[110:111],v1 offset:36032
v_add_u32 v1,0x14000,v1
v_mfma_f32_16x16x32_bf16 a[20:23],v[112:115],a[100:103],a[20:23]
v_accvgpr_read_b32 v12,a16
v_accvgpr_read_b32 v13,a17
v_mfma_f32_16x16x32_bf16 a[24:27],v[112:115],a[64:67],a[24:27]
v_accvgpr_read_b32 v14,a18
v_accvgpr_read_b32 v15,a19
v_mfma_f32_16x16x32_bf16 a[24:27],v[116:119],a[72:75],a[24:27]
v_cvt_pk_bf16_f32 v10,v12,v13
v_cvt_pk_bf16_f32 v11,v14,v15
v_mfma_f32_16x16x32_bf16 a[24:27],v[120:123],a[80:83],a[24:27]
buffer_store_dwordx4 v[8:11],v6,s[36:39],s76 offen sc1
L_pd23_%=:
s_cbranch_execz L_trtg2S_%=
v_mfma_f32_16x16x32_bf16 a[24:27],v[124:127],a[88:91],a[24:27]
v_accvgpr_read_b32 v20,a20
v_accvgpr_read_b32 v21,a21
v_mfma_f32_16x16x32_bf16 a[28:31],v[124:127],a[120:123],a[28:31]
v_accvgpr_read_b32 v22,a22
v_accvgpr_read_b32 v23,a23
v_mfma_f32_16x16x32_bf16 a[28:31],v[120:123],a[112:115],a[28:31]
v_cvt_pk_bf16_f32 v18,v20,v21
v_cvt_pk_bf16_f32 v19,v22,v23
v_mfma_f32_16x16x32_bf16 a[28:31],v[116:119],a[104:107],a[28:31]
v_accvgpr_read_b32 v8,a8
v_accvgpr_read_b32 v9,a9
v_mfma_f32_16x16x32_bf16 a[28:31],v[112:115],a[96:99],a[28:31]
v_accvgpr_read_b32 v10,a10
v_accvgpr_read_b32 v11,a11
v_mfma_f32_16x16x32_bf16 v[128:131],v[64:67],a[128:131],v[128:131]
v_accvgpr_read_b32 v12,a24
v_accvgpr_read_b32 v13,a25
v_mfma_f32_16x16x32_bf16 v[128:131],v[68:71],a[144:147],v[128:131]
v_accvgpr_read_b32 v14,a26
v_accvgpr_read_b32 v15,a27
v_mfma_f32_16x16x32_bf16 v[128:131],v[72:75],a[160:163],v[128:131]
v_cvt_pk_bf16_f32 v8,v8,v9
v_cvt_pk_bf16_f32 v9,v10,v11
v_mfma_f32_16x16x32_bf16 v[128:131],v[76:79],a[176:179],v[128:131]
v_cvt_pk_bf16_f32 v10,v12,v13
v_cvt_pk_bf16_f32 v11,v14,v15
v_mfma_f32_16x16x32_bf16 v[136:139],v[76:79],a[180:183],v[136:139]
ds_read_b64_tr_b16 v[112:113],v2 offset:224
ds_read_b64_tr_b16 v[114:115],v2 offset:5344
v_mfma_f32_16x16x32_bf16 v[136:139],v[72:75],a[164:167],v[136:139]
buffer_store_dwordx4 v[16:19],v6,s[36:39],s76 offen offset:1024 sc1
v_mfma_f32_16x16x32_bf16 v[136:139],v[68:71],a[148:151],v[136:139]
ds_read_b64_tr_b16 v[116:117],v2 offset:10464
ds_read_b64_tr_b16 v[118:119],v2 offset:15584
v_mfma_f32_16x16x32_bf16 v[136:139],v[64:67],a[132:135],v[136:139]
ds_read_b64_tr_b16 v[120:121],v2 offset:20704
v_accvgpr_read_b32 v16,a12
v_mfma_f32_16x16x32_bf16 v[144:147],v[64:67],a[136:139],v[144:147]
ds_read_b64_tr_b16 v[122:123],v2 offset:25824
v_accvgpr_read_b32 v17,a13
v_mfma_f32_16x16x32_bf16 v[144:147],v[68:71],a[152:155],v[144:147]
ds_read_b64_tr_b16 v[124:125],v2 offset:30944
v_accvgpr_read_b32 v18,a14
v_mfma_f32_16x16x32_bf16 v[144:147],v[72:75],a[168:171],v[144:147]
ds_read_b64_tr_b16 v[126:127],v2 offset:36064
v_accvgpr_read_b32 v19,a15
v_mfma_f32_16x16x32_bf16 v[144:147],v[76:79],a[184:187],v[144:147]
v_accvgpr_read_b32 v20,a28
v_accvgpr_read_b32 v21,a29
v_mfma_f32_16x16x32_bf16 v[152:155],v[76:79],a[188:191],v[152:155]
v_accvgpr_read_b32 v22,a30
v_accvgpr_read_b32 v23,a31
v_mfma_f32_16x16x32_bf16 v[152:155],v[72:75],a[172:175],v[152:155]
v_cvt_pk_bf16_f32 v16,v16,v17
v_cvt_pk_bf16_f32 v17,v18,v19
v_mfma_f32_16x16x32_bf16 v[152:155],v[68:71],a[156:159],v[152:155]
v_cvt_pk_bf16_f32 v18,v20,v21
v_cvt_pk_bf16_f32 v19,v22,v23
v_mfma_f32_16x16x32_bf16 v[152:155],v[64:67],a[140:143],v[152:155]
ds_read_b64_tr_b16 v[64:65],v1 offset:256
ds_read_b64_tr_b16 v[66:67],v1 offset:5376
v_mfma_f32_16x16x32_bf16 v[160:163],v[80:83],a[128:131],v[160:163]
buffer_store_dwordx4 v[8:11],v6,s[36:39],s76 offen offset:2048 sc1
v_mfma_f32_16x16x32_bf16 v[160:163],v[84:87],a[144:147],v[160:163]
ds_read_b64_tr_b16 v[68:69],v1 offset:10496
ds_read_b64_tr_b16 v[70:71],v1 offset:15616
v_mfma_f32_16x16x32_bf16 v[160:163],v[88:91],a[160:163],v[160:163]
ds_read_b64_tr_b16 v[72:73],v1 offset:20736
v_add_u32 v2,0x14000,v2
v_mfma_f32_16x16x32_bf16 v[160:163],v[92:95],a[176:179],v[160:163]
ds_read_b64_tr_b16 v[74:75],v1 offset:25856
v_mov_b32 v20,s52
v_mfma_f32_16x16x32_bf16 v[168:171],v[92:95],a[180:183],v[168:171]
ds_read_b64_tr_b16 v[76:77],v1 offset:30976
v_mov_b32 v21,s53
v_mfma_f32_16x16x32_bf16 v[168:171],v[88:91],a[164:167],v[168:171]
ds_read_b64_tr_b16 v[78:79],v1 offset:36096
v_add_u32 v1,0xffff6000,v1
v_mfma_f32_16x16x32_bf16 v[168:171],v[84:87],a[148:151],v[168:171]
v_lshlrev_b32 v22,20,v6
v_mfma_f32_16x16x32_bf16 v[168:171],v[80:83],a[132:135],v[168:171]
v_mfma_f32_16x16x32_bf16 v[176:179],v[80:83],a[136:139],v[176:179]
v_mfma_f32_16x16x32_bf16 v[176:179],v[84:87],a[152:155],v[176:179]
v_mfma_f32_16x16x32_bf16 v[176:179],v[88:91],a[168:171],v[176:179]
v_mfma_f32_16x16x32_bf16 v[176:179],v[92:95],a[184:187],v[176:179]
buffer_store_dwordx4 v[16:19],v28,s[36:39],s76 offen offset:3072 sc1
v_mfma_f32_16x16x32_bf16 v[184:187],v[92:95],a[188:191],v[184:187]
ds_read_b64_tr_b16 v[92:93],v2 offset:31008
ds_read_b64_tr_b16 v[94:95],v2 offset:36128
v_mfma_f32_16x16x32_bf16 v[184:187],v[88:91],a[172:175],v[184:187]
ds_read_b64_tr_b16 v[88:89],v2 offset:20768
v_mfma_f32_16x16x32_bf16 v[184:187],v[84:87],a[156:159],v[184:187]
ds_read_b64_tr_b16 v[84:85],v2 offset:10528
v_mfma_f32_16x16x32_bf16 v[184:187],v[80:83],a[140:143],v[184:187]
s_waitcnt lgkmcnt(4)
ds_read_b64_tr_b16 v[80:81],v2 offset:288
v_mfma_f32_16x16x32_bf16 v[128:131],v[96:99],a[68:71],v[128:131]
ds_read_b64_tr_b16 v[82:83],v2 offset:5408
ds_read_b64_tr_b16 v[86:87],v2 offset:15648
v_mfma_f32_16x16x32_bf16 v[128:131],v[100:103],a[76:79],v[128:131]
ds_read_b64_tr_b16 v[90:91],v2 offset:25888
v_add_u32 v2,0xffff6000,v2
v_mfma_f32_16x16x32_bf16 v[128:131],v[104:107],a[84:87],v[128:131]
v_mfma_f32_16x16x32_bf16 v[128:131],v[108:111],a[92:95],v[128:131]
v_mfma_f32_16x16x32_bf16 v[136:139],v[108:111],a[124:127],v[136:139]
v_mfma_f32_16x16x32_bf16 v[136:139],v[104:107],a[116:119],v[136:139]
v_mfma_f32_16x16x32_bf16 v[136:139],v[100:103],a[108:111],v[136:139]
v_mfma_f32_16x16x32_bf16 v[136:139],v[96:99],a[100:103],v[136:139]
v_mfma_f32_16x16x32_bf16 v[144:147],v[96:99],a[64:67],v[144:147]
v_mfma_f32_16x16x32_bf16 v[144:147],v[100:103],a[72:75],v[144:147]
v_mfma_f32_16x16x32_bf16 v[144:147],v[104:107],a[80:83],v[144:147]
v_mfma_f32_16x16x32_bf16 v[144:147],v[108:111],a[88:91],v[144:147]
v_cvt_pk_bf16_f32 v128,v128,v129
v_cvt_pk_bf16_f32 v129,v130,v131
v_mfma_f32_16x16x32_bf16 v[152:155],v[108:111],a[120:123],v[152:155]
ds_read_b64_tr_b16 v[108:109],v1 offset:30976
ds_read_b64_tr_b16 v[110:111],v1 offset:36096
v_mfma_f32_16x16x32_bf16 v[152:155],v[104:107],a[112:115],v[152:155]
v_cvt_pk_bf16_f32 v136,v136,v137
v_cvt_pk_bf16_f32 v137,v138,v139
v_mfma_f32_16x16x32_bf16 v[152:155],v[100:103],a[104:107],v[152:155]
v_cvt_pk_bf16_f32 v144,v144,v145
v_cvt_pk_bf16_f32 v145,v146,v147
v_mfma_f32_16x16x32_bf16 v[152:155],v[96:99],a[96:99],v[152:155]
ds_read_b64_tr_b16 v[96:97],v1 offset:256
ds_read_b64_tr_b16 v[98:99],v1 offset:5376
v_mfma_f32_16x16x32_bf16 v[160:163],v[112:115],a[68:71],v[160:163]
ds_read_b64_tr_b16 v[100:101],v1 offset:10496
v_mfma_f32_16x16x32_bf16 v[160:163],v[116:119],a[76:79],v[160:163]
ds_read_b64_tr_b16 v[102:103],v1 offset:15616
v_mfma_f32_16x16x32_bf16 v[160:163],v[120:123],a[84:87],v[160:163]
ds_read_b64_tr_b16 v[104:105],v1 offset:20736
v_mfma_f32_16x16x32_bf16 v[160:163],v[124:127],a[92:95],v[160:163]
v_cvt_pk_bf16_f32 v152,v152,v153
v_cvt_pk_bf16_f32 v153,v154,v155
v_mfma_f32_16x16x32_bf16 v[168:171],v[124:127],a[124:127],v[168:171]
ds_read_b64_tr_b16 v[106:107],v1 offset:25856
v_add_u32 v1,0xffff6000,v1
)A"
R"A(v_mfma_f32_16x16x32_bf16 v[168:171],v[120:123],a[116:119],v[168:171]
v_mfma_f32_16x16x32_bf16 v[168:171],v[116:119],a[108:111],v[168:171]
v_mfma_f32_16x16x32_bf16 v[168:171],v[112:115],a[100:103],v[168:171]
v_cvt_pk_bf16_f32 v130,v160,v161
v_cvt_pk_bf16_f32 v131,v162,v163
v_mfma_f32_16x16x32_bf16 v[176:179],v[112:115],a[64:67],v[176:179]
buffer_store_dwordx4 v[128:131],v6,s[36:39],s79 offen sc1
v_mfma_f32_16x16x32_bf16 v[176:179],v[116:119],a[72:75],v[176:179]
v_mfma_f32_16x16x32_bf16 v[176:179],v[120:123],a[80:83],v[176:179]
v_mfma_f32_16x16x32_bf16 v[176:179],v[124:127],a[88:91],v[176:179]
v_mfma_f32_16x16x32_bf16 v[184:187],v[124:127],a[120:123],v[184:187]
v_cvt_pk_bf16_f32 v138,v168,v169
v_cvt_pk_bf16_f32 v139,v170,v171
v_mfma_f32_16x16x32_bf16 v[184:187],v[120:123],a[112:115],v[184:187]
buffer_store_dwordx4 v[136:139],v6,s[36:39],s80 offen sc1
v_mfma_f32_16x16x32_bf16 v[184:187],v[116:119],a[104:107],v[184:187]
ds_read_b64_tr_b16 v[116:117],v2 offset:10528
ds_read_b64_tr_b16 v[118:119],v2 offset:15648
v_mfma_f32_16x16x32_bf16 v[184:187],v[112:115],a[96:99],v[184:187]
v_cvt_pk_bf16_f32 v146,v176,v177
v_cvt_pk_bf16_f32 v147,v178,v179
s_waitcnt vmcnt(6)
s_barrier
v_mfma_f32_16x16x32_bf16 v[192:195],v[64:67],v[32:35],v[192:195]
buffer_store_dwordx4 v[144:147],v6,s[36:39],s81 offen sc1
v_mfma_f32_16x16x32_bf16 v[192:195],v[68:71],v[36:39],v[192:195]
ds_read_b64_tr_b16 v[112:113],v2 offset:288
ds_read_b64_tr_b16 v[114:115],v2 offset:5408
v_mfma_f32_16x16x32_bf16 v[192:195],v[72:75],v[40:43],v[192:195]
buffer_store_dwordx2 v[58:59],v60,s[44:47],s18 offen offset:2312 sc1
v_mfma_f32_16x16x32_bf16 v[192:195],v[76:79],v[44:47],v[192:195]
v_cvt_pk_bf16_f32 v154,v184,v185
v_cvt_pk_bf16_f32 v155,v186,v187
v_mfma_f32_16x16x32_bf16 v[200:203],v[76:79],v[180:183],v[200:203]
buffer_store_dwordx4 v[152:155],v28,s[36:39],s82 offen sc1
v_mfma_f32_16x16x32_bf16 v[200:203],v[72:75],v[164:167],v[200:203]
ds_read_b64_tr_b16 v[120:121],v2 offset:20768
ds_read_b64_tr_b16 v[122:123],v2 offset:25888
v_mfma_f32_16x16x32_bf16 v[200:203],v[68:71],v[148:151],v[200:203]
ds_read_b64_tr_b16 v[124:125],v2 offset:31008
v_mfma_f32_16x16x32_bf16 v[200:203],v[64:67],v[132:135],v[200:203]
ds_read_b64_tr_b16 v[126:127],v2 offset:36128
v_add_u32 v2,0xffff6000,v2
v_mfma_f32_16x16x32_bf16 v[208:211],v[64:67],v[196:199],v[208:211]
v_mfma_f32_16x16x32_bf16 v[208:211],v[68:71],v[212:215],v[208:211]
v_mfma_f32_16x16x32_bf16 v[208:211],v[72:75],v[228:231],v[208:211]
v_mfma_f32_16x16x32_bf16 v[208:211],v[76:79],v[244:247],v[208:211]
v_mfma_f32_16x16x32_bf16 v[216:219],v[76:79],v[188:191],v[216:219]
ds_read_b64_tr_b16 v[76:77],v1 offset:30976
ds_read_b64_tr_b16 v[78:79],v1 offset:36096
v_mfma_f32_16x16x32_bf16 v[216:219],v[72:75],v[172:175],v[216:219]
ds_read_b64_tr_b16 v[72:73],v1 offset:20736
v_mfma_f32_16x16x32_bf16 v[216:219],v[68:71],v[156:159],v[216:219]
ds_read_b64_tr_b16 v[68:69],v1 offset:10496
v_mfma_f32_16x16x32_bf16 v[216:219],v[64:67],v[140:143],v[216:219]
s_waitcnt lgkmcnt(4)
ds_read_b64_tr_b16 v[64:65],v1 offset:256
v_mfma_f32_16x16x32_bf16 v[224:227],v[80:83],v[32:35],v[224:227]
ds_read_b64_tr_b16 v[66:67],v1 offset:5376
ds_read_b64_tr_b16 v[70:71],v1 offset:15616
v_mfma_f32_16x16x32_bf16 v[224:227],v[84:87],v[36:39],v[224:227]
ds_read_b64_tr_b16 v[74:75],v1 offset:25856
v_mfma_f32_16x16x32_bf16 v[224:227],v[88:91],v[40:43],v[224:227]
v_mfma_f32_16x16x32_bf16 v[224:227],v[92:95],v[44:47],v[224:227]
v_mfma_f32_16x16x32_bf16 v[232:235],v[92:95],v[180:183],v[232:235]
v_mfma_f32_16x16x32_bf16 v[232:235],v[88:91],v[164:167],v[232:235]
v_mfma_f32_16x16x32_bf16 v[232:235],v[84:87],v[148:151],v[232:235]
v_mfma_f32_16x16x32_bf16 v[232:235],v[80:83],v[132:135],v[232:235]
v_mfma_f32_16x16x32_bf16 v[240:243],v[80:83],v[196:199],v[240:243]
v_mfma_f32_16x16x32_bf16 v[240:243],v[84:87],v[212:215],v[240:243]
v_mfma_f32_16x16x32_bf16 v[240:243],v[88:91],v[228:231],v[240:243]
v_mfma_f32_16x16x32_bf16 v[240:243],v[92:95],v[244:247],v[240:243]
v_mfma_f32_16x16x32_bf16 v[248:251],v[92:95],v[188:191],v[248:251]
ds_read_b64_tr_b16 v[92:93],v2 offset:31008
ds_read_b64_tr_b16 v[94:95],v2 offset:36128
v_mfma_f32_16x16x32_bf16 v[248:251],v[88:91],v[172:175],v[248:251]
ds_read_b64_tr_b16 v[88:89],v2 offset:20768
v_mfma_f32_16x16x32_bf16 v[248:251],v[84:87],v[156:159],v[248:251]
ds_read_b64_tr_b16 v[84:85],v2 offset:10528
v_mfma_f32_16x16x32_bf16 v[248:251],v[80:83],v[140:143],v[248:251]
ds_read_b64_tr_b16 v[80:81],v2 offset:288
v_mfma_f32_16x16x32_bf16 v[192:195],v[96:99],a[128:131],v[192:195]
ds_read_b64_tr_b16 v[82:83],v2 offset:5408
ds_read_b64_tr_b16 v[86:87],v2 offset:15648
v_mfma_f32_16x16x32_bf16 v[192:195],v[100:103],a[144:147],v[192:195]
ds_read_b64_tr_b16 v[90:91],v2 offset:25888
v_mfma_f32_16x16x32_bf16 v[192:195],v[104:107],a[160:163],v[192:195]
v_mfma_f32_16x16x32_bf16 v[192:195],v[108:111],a[176:179],v[192:195]
v_mfma_f32_16x16x32_bf16 v[200:203],v[108:111],a[180:183],v[200:203]
v_mfma_f32_16x16x32_bf16 v[200:203],v[104:107],a[164:167],v[200:203]
v_mfma_f32_16x16x32_bf16 v[200:203],v[100:103],a[148:151],v[200:203]
v_mfma_f32_16x16x32_bf16 v[200:203],v[96:99],a[132:135],v[200:203]
v_mfma_f32_16x16x32_bf16 v[208:211],v[96:99],a[136:139],v[208:211]
v_mfma_f32_16x16x32_bf16 v[208:211],v[100:103],a[152:155],v[208:211]
v_mfma_f32_16x16x32_bf16 v[208:211],v[104:107],a[168:171],v[208:211]
v_mfma_f32_16x16x32_bf16 v[208:211],v[108:111],a[184:187],v[208:211]
v_mfma_f32_16x16x32_bf16 v[216:219],v[108:111],a[188:191],v[216:219]
v_mfma_f32_16x16x32_bf16 v[216:219],v[104:107],a[172:175],v[216:219]
v_mfma_f32_16x16x32_bf16 v[216:219],v[100:103],a[156:159],v[216:219]
v_mfma_f32_16x16x32_bf16 v[216:219],v[96:99],a[140:143],v[216:219]
v_mfma_f32_16x16x32_bf16 v[224:227],v[112:115],a[128:131],v[224:227]
v_mfma_f32_16x16x32_bf16 v[224:227],v[116:119],a[144:147],v[224:227]
v_mfma_f32_16x16x32_bf16 v[224:227],v[120:123],a[160:163],v[224:227]
v_mfma_f32_16x16x32_bf16 v[224:227],v[124:127],a[176:179],v[224:227]
v_mfma_f32_16x16x32_bf16 v[232:235],v[124:127],a[180:183],v[232:235]
v_mfma_f32_16x16x32_bf16 v[232:235],v[120:123],a[164:167],v[232:235]
v_mfma_f32_16x16x32_bf16 v[232:235],v[116:119],a[148:151],v[232:235]
v_mfma_f32_16x16x32_bf16 v[232:235],v[112:115],a[132:135],v[232:235]
v_mfma_f32_16x16x32_bf16 v[240:243],v[112:115],a[136:139],v[240:243]
v_mfma_f32_16x16x32_bf16 v[240:243],v[116:119],a[152:155],v[240:243]
v_mfma_f32_16x16x32_bf16 v[240:243],v[120:123],a[168:171],v[240:243]
v_mfma_f32_16x16x32_bf16 v[240:243],v[124:127],a[184:187],v[240:243]
v_mfma_f32_16x16x32_bf16 v[248:251],v[124:127],a[188:191],v[248:251]
v_mfma_f32_16x16x32_bf16 v[248:251],v[120:123],a[172:175],v[248:251]
v_mfma_f32_16x16x32_bf16 v[248:251],v[116:119],a[156:159],v[248:251]
v_mfma_f32_16x16x32_bf16 v[248:251],v[112:115],a[140:143],v[248:251]
s_waitcnt lgkmcnt(0)
v_mfma_f32_16x16x32_bf16 v[192:195],v[64:67],a[68:71],v[192:195]
v_mfma_f32_16x16x32_bf16 v[192:195],v[68:71],a[76:79],v[192:195]
v_mfma_f32_16x16x32_bf16 v[192:195],v[72:75],a[84:87],v[192:195]
v_mfma_f32_16x16x32_bf16 v[192:195],v[76:79],a[92:95],v[192:195]
v_mfma_f32_16x16x32_bf16 v[200:203],v[76:79],a[124:127],v[200:203]
v_mfma_f32_16x16x32_bf16 v[200:203],v[72:75],a[116:119],v[200:203]
v_mfma_f32_16x16x32_bf16 v[200:203],v[68:71],a[108:111],v[200:203]
v_mfma_f32_16x16x32_bf16 v[200:203],v[64:67],a[100:103],v[200:203]
v_mfma_f32_16x16x32_bf16 v[208:211],v[64:67],a[64:67],v[208:211]
v_mfma_f32_16x16x32_bf16 v[208:211],v[68:71],a[72:75],v[208:211]
v_mfma_f32_16x16x32_bf16 v[208:211],v[72:75],a[80:83],v[208:211]
v_mfma_f32_16x16x32_bf16 v[208:211],v[76:79],a[88:91],v[208:211]
v_mfma_f32_16x16x32_bf16 v[216:219],v[76:79],a[120:123],v[216:219]
v_mfma_f32_16x16x32_bf16 v[216:219],v[72:75],a[112:115],v[216:219]
v_mfma_f32_16x16x32_bf16 v[216:219],v[68:71],a[104:107],v[216:219]
v_mfma_f32_16x16x32_bf16 v[216:219],v[64:67],a[96:99],v[216:219]
v_mfma_f32_16x16x32_bf16 v[224:227],v[80:83],a[68:71],v[224:227]
buffer_load_dwordx2 v[62:63],v61,s[44:47],s91 offen offset:2312 sc1
v_mfma_f32_16x16x32_bf16 v[224:227],v[84:87],a[76:79],v[224:227]
v_mfma_f32_16x16x32_bf16 v[224:227],v[88:91],a[84:87],v[224:227]
v_mfma_f32_16x16x32_bf16 v[224:227],v[92:95],a[92:95],v[224:227]
v_mfma_f32_16x16x32_bf16 v[232:235],v[92:95],a[124:127],v[232:235]
v_mfma_f32_16x16x32_bf16 v[232:235],v[88:91],a[116:119],v[232:235]
v_mfma_f32_16x16x32_bf16 v[232:235],v[84:87],a[108:111],v[232:235]
v_mfma_f32_16x16x32_bf16 v[232:235],v[80:83],a[100:103],v[232:235]
v_mfma_f32_16x16x32_bf16 v[240:243],v[80:83],a[64:67],v[240:243]
v_mfma_f32_16x16x32_bf16 v[240:243],v[84:87],a[72:75],v[240:243]
v_mfma_f32_16x16x32_bf16 v[240:243],v[88:91],a[80:83],v[240:243]
v_mfma_f32_16x16x32_bf16 v[240:243],v[92:95],a[88:91],v[240:243]
v_mfma_f32_16x16x32_bf16 v[248:251],v[92:95],a[120:123],v[248:251]
v_mfma_f32_16x16x32_bf16 v[248:251],v[88:91],a[112:115],v[248:251]
v_mfma_f32_16x16x32_bf16 v[248:251],v[84:87],a[104:107],v[248:251]
v_mfma_f32_16x16x32_bf16 v[248:251],v[80:83],a[96:99],v[248:251]
s_waitcnt vmcnt(1)
s_barrier
buffer_store_dwordx2 v[20:21],v22,s[44:47],s18 offen offset:2304 sc1
s_waitcnt vmcnt(1)
v_cmp_ne_u32 s[56:57],s52,v62
v_cmp_ne_u32 s[58:59],s53,v63
s_nop 0
s_or_b64 s[56:57],s[56:57],s[58:59]
s_cbranch_scc1 L_fr8_%=
L_fo8_%=:
buffer_load_dwordx4 v[64:67],v6,s[36:39],s66 offen sc1
buffer_load_dwordx4 v[68:71],v6,s[36:39],s67 offen sc1
buffer_load_dwordx4 v[72:75],v6,s[36:39],s66 offen offset:1024 sc1
buffer_load_dwordx4 v[76:79],v6,s[36:39],s67 offen offset:1024 sc1
buffer_load_dwordx4 v[80:83],v6,s[36:39],s66 offen offset:2048 sc1
buffer_load_dwordx4 v[84:87],v6,s[36:39],s67 offen offset:2048 sc1
buffer_load_dwordx4 v[88:91],v28,s[36:39],s66 offen offset:3072 sc1
buffer_load_dwordx4 v[92:95],v28,s[36:39],s67 offen offset:3072 sc1
buffer_load_dwordx2 v[4:5],v3,s[44:47],s91 offen offset:2304 sc1
s_waitcnt vmcnt(8)
v_mfma_f32_16x16x32_bf16 v[192:195],v[48:51],v[64:67],v[192:195]
s_waitcnt vmcnt(7)
v_mfma_f32_16x16x32_bf16 v[192:195],v[48:51],v[68:71],v[192:195]
v_mfma_f32_16x16x32_bf16 v[224:227],v[52:55],v[64:67],v[224:227]
v_mfma_f32_16x16x32_bf16 v[224:227],v[52:55],v[68:71],v[224:227]
s_waitcnt vmcnt(6)
v_mfma_f32_16x16x32_bf16 v[200:203],v[48:51],v[72:75],v[200:203]
s_waitcnt vmcnt(5)
v_mfma_f32_16x16x32_bf16 v[200:203],v[48:51],v[76:79],v[200:203]
v_mfma_f32_16x16x32_bf16 v[232:235],v[52:55],v[72:75],v[232:235]
v_mfma_f32_16x16x32_bf16 v[232:235],v[52:55],v[76:79],v[232:235]
s_waitcnt vmcnt(4)
v_mfma_f32_16x16x32_bf16 v[208:211],v[48:51],v[80:83],v[208:211]
s_waitcnt vmcnt(3)
v_mfma_f32_16x16x32_bf16 v[208:211],v[48:51],v[84:87],v[208:211]
v_mfma_f32_16x16x32_bf16 v[240:243],v[52:55],v[80:83],v[240:243]
v_mfma_f32_16x16x32_bf16 v[240:243],v[52:55],v[84:87],v[240:243]
s_waitcnt vmcnt(2)
v_mfma_f32_16x16x32_bf16 v[216:219],v[48:51],v[88:91],v[216:219]
s_waitcnt vmcnt(1)
v_mfma_f32_16x16x32_bf16 v[216:219],v[48:51],v[92:95],v[216:219]
v_mfma_f32_16x16x32_bf16 v[248:251],v[52:55],v[88:91],v[248:251]
v_mfma_f32_16x16x32_bf16 v[248:251],v[52:55],v[92:95],v[248:251]
s_waitcnt vmcnt(0)
v_cmp_ne_u32 s[56:57],s52,v4
v_cmp_ne_u32 s[58:59],s53,v5
s_nop 0
s_or_b64 s[56:57],s[56:57],s[58:59]
s_cbranch_scc1 L_fr9_%=
L_fo9_%=:
buffer_load_dwordx4 v[96:99],v6,s[36:39],s68 offen sc1
buffer_load_dwordx4 v[100:103],v6,s[36:39],s68 offen offset:1024 sc1
buffer_load_dwordx4 v[104:107],v6,s[36:39],s68 offen offset:2048 sc1
buffer_load_dwordx4 v[108:111],v6,s[36:39],s68 offen offset:3072 sc1
buffer_load_dwordx4 v[112:115],v6,s[36:39],s65 offen sc1
buffer_load_dwordx4 v[116:119],v6,s[36:39],s65 offen offset:1024 sc1
buffer_load_dwordx4 v[120:123],v6,s[36:39],s65 offen offset:2048 sc1
buffer_load_dwordx4 v[124:127],v28,s[36:39],s65 offen offset:3072 sc1
s_waitcnt vmcnt(7)
v_mfma_f32_16x16x32_bf16 v[128:131],v[48:51],v[96:99],0
s_waitcnt vmcnt(6)
v_mfma_f32_16x16x32_bf16 v[128:131],v[48:51],v[100:103],v[128:131]
s_waitcnt vmcnt(5)
v_mfma_f32_16x16x32_bf16 v[128:131],v[48:51],v[104:107],v[128:131]
s_waitcnt vmcnt(4)
v_mfma_f32_16x16x32_bf16 v[128:131],v[48:51],v[108:111],v[128:131]
)A"
R"A(v_mfma_f32_16x16x32_bf16 v[160:163],v[52:55],v[96:99],0
v_mfma_f32_16x16x32_bf16 v[160:163],v[52:55],v[100:103],v[160:163]
v_mfma_f32_16x16x32_bf16 v[160:163],v[52:55],v[104:107],v[160:163]
v_mfma_f32_16x16x32_bf16 v[160:163],v[52:55],v[108:111],v[160:163]
s_waitcnt vmcnt(3)
v_mfma_f32_16x16x32_bf16 v[192:195],v[48:51],v[112:115],v[192:195]
v_mfma_f32_16x16x32_bf16 v[224:227],v[52:55],v[112:115],v[224:227]
s_waitcnt vmcnt(2)
v_mfma_f32_16x16x32_bf16 v[200:203],v[48:51],v[116:119],v[200:203]
v_mfma_f32_16x16x32_bf16 v[232:235],v[52:55],v[116:119],v[232:235]
s_waitcnt vmcnt(1)
v_cvt_pk_bf16_f32 v128,v128,v129
v_cvt_pk_bf16_f32 v129,v130,v131
v_mfma_f32_16x16x32_bf16 v[208:211],v[48:51],v[120:123],v[208:211]
v_mfma_f32_16x16x32_bf16 v[240:243],v[52:55],v[120:123],v[240:243]
s_waitcnt vmcnt(0)
v_cvt_pk_bf16_f32 v130,v160,v161
v_cvt_pk_bf16_f32 v131,v162,v163
v_mfma_f32_16x16x32_bf16 v[216:219],v[48:51],v[124:127],v[216:219]
v_cvt_pk_bf16_f32 v192,v192,v193
v_cvt_pk_bf16_f32 v193,v194,v195
v_cvt_pk_bf16_f32 v194,v224,v225
v_cvt_pk_bf16_f32 v195,v226,v227
v_cvt_pk_bf16_f32 v200,v200,v201
v_cvt_pk_bf16_f32 v201,v202,v203
v_mfma_f32_16x16x32_bf16 v[248:251],v[52:55],v[124:127],v[248:251]
v_permlane16_swap_b32 v128,v130
v_permlane16_swap_b32 v129,v131
v_cvt_pk_bf16_f32 v202,v232,v233
v_cvt_pk_bf16_f32 v203,v234,v235
v_cvt_pk_bf16_f32 v208,v208,v209
v_cvt_pk_bf16_f32 v209,v210,v211
v_cvt_pk_bf16_f32 v210,v240,v241
v_cvt_pk_bf16_f32 v211,v242,v243
buffer_store_dwordx4 v[128:131],v56,s[24:27],s83 offen offset:256 sc1 nt
v_permlane16_swap_b32 v192,v194
v_permlane16_swap_b32 v193,v195
buffer_store_dwordx4 v[192:195],v56,s[24:27],s87 offen sc1 nt
v_permlane16_swap_b32 v200,v202
v_permlane16_swap_b32 v201,v203
v_cvt_pk_bf16_f32 v216,v216,v217
v_cvt_pk_bf16_f32 v217,v218,v219
v_cvt_pk_bf16_f32 v218,v248,v249
v_cvt_pk_bf16_f32 v219,v250,v251
buffer_store_dwordx4 v[200:203],v56,s[24:27],s88 offen sc1 nt
v_permlane16_swap_b32 v208,v210
v_permlane16_swap_b32 v209,v211
buffer_store_dwordx4 v[208:211],v56,s[24:27],s89 offen sc1 nt
v_permlane16_swap_b32 v216,v218
v_permlane16_swap_b32 v217,v219
buffer_store_dwordx4 v[216:219],v56,s[24:27],s90 offen sc1 nt
s_lshl_b32 s69,s62,20
s_add_u32 s69,s69,s18
buffer_store_dword v20,v22,s[44:47],s69 offen sc1
L_geS_%=:
s_branch L_end_%=
L_hot_%=:
L_fr1_%=:
s_mov_b32 s19,65536
L_fl1_%=:
s_sleep 6
buffer_load_dwordx2 v[4:5],v3,s[44:47],s91 offen offset:256 sc1
s_waitcnt vmcnt(0)
s_sub_u32 s19,s19,1
s_cmp_eq_u32 s19,0
s_cbranch_scc1 L_fo1_%=
v_cmp_ne_u32 s[56:57],s52,v4
v_cmp_ne_u32 s[58:59],s53,v5
s_nop 1
s_or_b64 s[56:57],s[56:57],s[58:59]
s_cbranch_scc1 L_fl1_%=
s_branch L_fo1_%=
L_fr2_%=:
s_mov_b32 s19,65536
L_fl2_%=:
s_sleep 6
buffer_load_dwordx2 v[4:5],v3,s[44:47],s91 offen offset:1280 sc1
s_waitcnt vmcnt(0)
s_sub_u32 s19,s19,1
s_cmp_eq_u32 s19,0
s_cbranch_scc1 L_fo2_%=
v_cmp_ne_u32 s[56:57],s52,v4
v_cmp_ne_u32 s[58:59],s53,v5
s_nop 1
s_or_b64 s[56:57],s[56:57],s[58:59]
s_cbranch_scc1 L_fl2_%=
s_branch L_fo2_%=
L_fr3_%=:
s_mov_b32 s19,65536
L_fl3_%=:
s_sleep 6
buffer_load_dwordx2 v[4:5],v3,s[44:47],s91 offen offset:1280 sc1
s_waitcnt vmcnt(0)
s_sub_u32 s19,s19,1
s_cmp_eq_u32 s19,0
s_cbranch_scc1 L_fo3_%=
v_cmp_ne_u32 s[56:57],s52,v4
v_cmp_ne_u32 s[58:59],s53,v5
s_nop 1
s_or_b64 s[56:57],s[56:57],s[58:59]
s_cbranch_scc1 L_fl3_%=
s_branch L_fo3_%=
L_fr4_%=:
s_mov_b32 s19,65536
L_fl4_%=:
s_sleep 6
buffer_load_dwordx2 v[62:63],v61,s[44:47],s91 offen offset:2312 sc1
s_waitcnt vmcnt(0)
s_sub_u32 s19,s19,1
s_cmp_eq_u32 s19,0
s_cbranch_scc1 L_fo4_%=
v_cmp_ne_u32 s[56:57],s52,v62
v_cmp_ne_u32 s[58:59],s53,v63
s_nop 1
s_or_b64 s[56:57],s[56:57],s[58:59]
s_cbranch_scc1 L_fl4_%=
s_branch L_fo4_%=
L_fr5_%=:
s_mov_b32 s19,65536
L_fl5_%=:
s_sleep 6
buffer_load_dwordx2 v[4:5],v3,s[44:47],s91 offen offset:2304 sc1
s_waitcnt vmcnt(0)
s_sub_u32 s19,s19,1
s_cmp_eq_u32 s19,0
s_cbranch_scc1 L_fo5_%=
v_cmp_ne_u32 s[56:57],s52,v4
v_cmp_ne_u32 s[58:59],s53,v5
s_nop 1
s_or_b64 s[56:57],s[56:57],s[58:59]
s_cbranch_scc1 L_fl5_%=
s_branch L_fo5_%=
L_fr6_%=:
s_mov_b32 s19,65536
L_fl6_%=:
s_sleep 6
buffer_load_dwordx2 v[4:5],v3,s[44:47],s91 offen offset:256 sc1
s_waitcnt vmcnt(0)
s_sub_u32 s19,s19,1
s_cmp_eq_u32 s19,0
s_cbranch_scc1 L_fo6_%=
v_cmp_ne_u32 s[56:57],s52,v4
v_cmp_ne_u32 s[58:59],s53,v5
s_nop 1
s_or_b64 s[56:57],s[56:57],s[58:59]
s_cbranch_scc1 L_fl6_%=
s_branch L_fo6_%=
L_fr7_%=:
s_mov_b32 s19,65536
L_fl7_%=:
s_sleep 6
buffer_load_dwordx2 v[4:5],v3,s[44:47],s91 offen offset:1280 sc1
s_waitcnt vmcnt(0)
s_sub_u32 s19,s19,1
s_cmp_eq_u32 s19,0
s_cbranch_scc1 L_fo7_%=
v_cmp_ne_u32 s[56:57],s52,v4
v_cmp_ne_u32 s[58:59],s53,v5
s_nop 1
s_or_b64 s[56:57],s[56:57],s[58:59]
s_cbranch_scc1 L_fl7_%=
s_branch L_fo7_%=
L_fr8_%=:
s_mov_b32 s19,65536
L_fl8_%=:
s_sleep 6
buffer_load_dwordx2 v[62:63],v61,s[44:47],s91 offen offset:2312 sc1
s_waitcnt vmcnt(0)
s_sub_u32 s19,s19,1
s_cmp_eq_u32 s19,0
s_cbranch_scc1 L_fo8_%=
v_cmp_ne_u32 s[56:57],s52,v62
v_cmp_ne_u32 s[58:59],s53,v63
s_nop 1
s_or_b64 s[56:57],s[56:57],s[58:59]
s_cbranch_scc1 L_fl8_%=
s_branch L_fo8_%=
L_fr9_%=:
s_mov_b32 s19,65536
L_fl9_%=:
s_sleep 6
buffer_load_dwordx2 v[4:5],v3,s[44:47],s91 offen offset:2304 sc1
s_waitcnt vmcnt(0)
s_sub_u32 s19,s19,1
s_cmp_eq_u32 s19,0
s_cbranch_scc1 L_fo9_%=
v_cmp_ne_u32 s[56:57],s52,v4
v_cmp_ne_u32 s[58:59],s53,v5
s_nop 1
s_or_b64 s[56:57],s[56:57],s[58:59]
s_cbranch_scc1 L_fl9_%=
s_branch L_fo9_%=
L_slow0_%=:
s_waitcnt vmcnt(0) lgkmcnt(0)
s_nop 7
v_accvgpr_write_b32 a160,v32
v_accvgpr_write_b32 a161,v33
v_accvgpr_write_b32 a162,v34
v_accvgpr_write_b32 a163,v35
v_accvgpr_write_b32 a164,v36
v_accvgpr_write_b32 a165,v37
v_accvgpr_write_b32 a166,v38
v_accvgpr_write_b32 a167,v39
v_accvgpr_write_b32 a168,v40
v_accvgpr_write_b32 a169,v41
v_accvgpr_write_b32 a170,v42
v_accvgpr_write_b32 a171,v43
v_accvgpr_write_b32 a172,v44
v_accvgpr_write_b32 a173,v45
v_accvgpr_write_b32 a174,v46
v_accvgpr_write_b32 a175,v47
s_waitcnt vmcnt(0) lgkmcnt(0)
s_nop 7
v_bfe_u32 v11,v6,4,4
v_lshrrev_b32 v12,8,v6
v_lshlrev_b32 v8,8,v11
v_lshl_add_u32 v8,v12,4,v8
v_lshlrev_b32 v9,10,v12
v_lshl_add_u32 v9,v11,1,v9
v_add_u32 v10,4096,v9
v_mov_b32 v48,0xff800000
v_mov_b32 v52,0xff800000
v_mov_b32 v56,0xff800000
v_mov_b32 v60,0xff800000
s_mov_b32 s65,0
s_mov_b32 s66,64
L_s10_%=:
buffer_load_dwordx4 a[128:131],v8,s[20:23],s65 offen
buffer_load_dwordx4 a[132:135],v8,s[20:23],s65 offen offset:64
buffer_load_dwordx4 a[136:139],v8,s[20:23],s65 offen offset:128
buffer_load_dwordx4 a[140:143],v8,s[20:23],s65 offen offset:192
s_waitcnt vmcnt(0)
v_mfma_f32_16x16x32_bf16 v[64:67],a[128:131],v[132:135],0
v_mfma_f32_16x16x32_bf16 v[64:67],a[132:135],v[140:143],v[64:67]
v_mfma_f32_16x16x32_bf16 v[64:67],a[136:139],v[148:151],v[64:67]
v_mfma_f32_16x16x32_bf16 v[64:67],a[140:143],v[156:159],v[64:67]
v_mfma_f32_16x16x32_bf16 v[80:83],a[128:131],v[164:167],0
v_mfma_f32_16x16x32_bf16 v[80:83],a[132:135],v[172:175],v[80:83]
v_mfma_f32_16x16x32_bf16 v[80:83],a[136:139],v[180:183],v[80:83]
v_mfma_f32_16x16x32_bf16 v[80:83],a[140:143],v[188:191],v[80:83]
v_mfma_f32_16x16x32_bf16 v[96:99],a[128:131],v[196:199],0
v_mfma_f32_16x16x32_bf16 v[96:99],a[132:135],v[204:207],v[96:99]
v_mfma_f32_16x16x32_bf16 v[96:99],a[136:139],v[212:215],v[96:99]
v_mfma_f32_16x16x32_bf16 v[96:99],a[140:143],v[220:223],v[96:99]
v_mfma_f32_16x16x32_bf16 v[112:115],a[128:131],v[228:231],0
v_mfma_f32_16x16x32_bf16 v[112:115],a[132:135],v[236:239],v[112:115]
v_mfma_f32_16x16x32_bf16 v[112:115],a[136:139],v[244:247],v[112:115]
v_mfma_f32_16x16x32_bf16 v[112:115],a[140:143],v[252:255],v[112:115]
s_nop 7
v_max3_f32 v48,v48,v64,v65
v_max3_f32 v48,v48,v66,v67
v_max3_f32 v52,v52,v80,v81
v_max3_f32 v52,v52,v82,v83
v_max3_f32 v56,v56,v96,v97
v_max3_f32 v56,v56,v98,v99
v_max3_f32 v60,v60,v112,v113
v_max3_f32 v60,v60,v114,v115
s_add_u32 s65,s65,4096
s_sub_u32 s66,s66,1
s_cmp_lg_u32 s66,0
s_cbranch_scc1 L_s10_%=
v_mov_b32 v11,v48
s_nop 1
v_permlane32_swap_b32 v48,v11
v_max_f32 v48,v48,v11
v_mov_b32 v11,v48
s_nop 1
v_permlane16_swap_b32 v48,v11
v_max_f32 v48,v48,v11
v_sub_f32 v32,0,v48
v_sub_f32 v33,0,v48
v_sub_f32 v34,0,v48
v_sub_f32 v35,0,v48
v_mov_b32 v11,v52
s_nop 1
v_permlane32_swap_b32 v52,v11
v_max_f32 v52,v52,v11
v_mov_b32 v11,v52
s_nop 1
v_permlane16_swap_b32 v52,v11
v_max_f32 v52,v52,v11
v_sub_f32 v36,0,v52
v_sub_f32 v37,0,v52
v_sub_f32 v38,0,v52
v_sub_f32 v39,0,v52
v_mov_b32 v11,v56
s_nop 1
v_permlane32_swap_b32 v56,v11
v_max_f32 v56,v56,v11
v_mov_b32 v11,v56
s_nop 1
v_permlane16_swap_b32 v56,v11
v_max_f32 v56,v56,v11
v_sub_f32 v40,0,v56
v_sub_f32 v41,0,v56
v_sub_f32 v42,0,v56
v_sub_f32 v43,0,v56
v_mov_b32 v11,v60
s_nop 1
v_permlane32_swap_b32 v60,v11
v_max_f32 v60,v60,v11
v_mov_b32 v11,v60
s_nop 1
v_permlane16_swap_b32 v60,v11
v_max_f32 v60,v60,v11
v_max_f32 v60,s72,v60
v_sub_f32 v44,0,v60
v_sub_f32 v45,0,v60
v_sub_f32 v46,0,v60
v_sub_f32 v47,0,v60
v_mov_b32 v48,0
v_mov_b32 v49,0
v_mov_b32 v50,0
v_mov_b32 v51,0
v_mov_b32 v52,0
v_mov_b32 v53,0
v_mov_b32 v54,0
v_mov_b32 v55,0
v_mov_b32 v56,0
v_mov_b32 v57,0
v_mov_b32 v58,0
v_mov_b32 v59,0
v_mov_b32 v60,0
v_mov_b32 v61,0
v_mov_b32 v62,0
v_mov_b32 v63,0
v_accvgpr_write_b32 a0,0
v_accvgpr_write_b32 a1,0
v_accvgpr_write_b32 a2,0
v_accvgpr_write_b32 a3,0
v_accvgpr_write_b32 a4,0
v_accvgpr_write_b32 a5,0
v_accvgpr_write_b32 a6,0
v_accvgpr_write_b32 a7,0
v_accvgpr_write_b32 a8,0
v_accvgpr_write_b32 a9,0
v_accvgpr_write_b32 a10,0
v_accvgpr_write_b32 a11,0
v_accvgpr_write_b32 a12,0
v_accvgpr_write_b32 a13,0
v_accvgpr_write_b32 a14,0
v_accvgpr_write_b32 a15,0
v_accvgpr_write_b32 a16,0
v_accvgpr_write_b32 a17,0
v_accvgpr_write_b32 a18,0
v_accvgpr_write_b32 a19,0
v_accvgpr_write_b32 a20,0
v_accvgpr_write_b32 a21,0
v_accvgpr_write_b32 a22,0
v_accvgpr_write_b32 a23,0
v_accvgpr_write_b32 a24,0
v_accvgpr_write_b32 a25,0
v_accvgpr_write_b32 a26,0
v_accvgpr_write_b32 a27,0
v_accvgpr_write_b32 a28,0
v_accvgpr_write_b32 a29,0
v_accvgpr_write_b32 a30,0
v_accvgpr_write_b32 a31,0
v_accvgpr_write_b32 a32,0
v_accvgpr_write_b32 a33,0
v_accvgpr_write_b32 a34,0
v_accvgpr_write_b32 a35,0
v_accvgpr_write_b32 a36,0
v_accvgpr_write_b32 a37,0
v_accvgpr_write_b32 a38,0
v_accvgpr_write_b32 a39,0
v_accvgpr_write_b32 a40,0
v_accvgpr_write_b32 a41,0
v_accvgpr_write_b32 a42,0
v_accvgpr_write_b32 a43,0
v_accvgpr_write_b32 a44,0
v_accvgpr_write_b32 a45,0
v_accvgpr_write_b32 a46,0
v_accvgpr_write_b32 a47,0
v_accvgpr_write_b32 a48,0
v_accvgpr_write_b32 a49,0
v_accvgpr_write_b32 a50,0
v_accvgpr_write_b32 a51,0
v_accvgpr_write_b32 a52,0
v_accvgpr_write_b32 a53,0
v_accvgpr_write_b32 a54,0
v_accvgpr_write_b32 a55,0
v_accvgpr_write_b32 a56,0
v_accvgpr_write_b32 a57,0
v_accvgpr_write_b32 a58,0
v_accvgpr_write_b32 a59,0
v_accvgpr_write_b32 a60,0
v_accvgpr_write_b32 a61,0
v_accvgpr_write_b32 a62,0
v_accvgpr_write_b32 a63,0
v_accvgpr_write_b32 a64,0
v_accvgpr_write_b32 a65,0
v_accvgpr_write_b32 a66,0
v_accvgpr_write_b32 a67,0
v_accvgpr_write_b32 a68,0
v_accvgpr_write_b32 a69,0
v_accvgpr_write_b32 a70,0
v_accvgpr_write_b32 a71,0
v_accvgpr_write_b32 a72,0
v_accvgpr_write_b32 a73,0
v_accvgpr_write_b32 a74,0
v_accvgpr_write_b32 a75,0
v_accvgpr_write_b32 a76,0
v_accvgpr_write_b32 a77,0
v_accvgpr_write_b32 a78,0
v_accvgpr_write_b32 a79,0
v_accvgpr_write_b32 a80,0
v_accvgpr_write_b32 a81,0
v_accvgpr_write_b32 a82,0
v_accvgpr_write_b32 a83,0
v_accvgpr_write_b32 a84,0
v_accvgpr_write_b32 a85,0
v_accvgpr_write_b32 a86,0
v_accvgpr_write_b32 a87,0
v_accvgpr_write_b32 a88,0
v_accvgpr_write_b32 a89,0
v_accvgpr_write_b32 a90,0
v_accvgpr_write_b32 a91,0
v_accvgpr_write_b32 a92,0
v_accvgpr_write_b32 a93,0
v_accvgpr_write_b32 a94,0
v_accvgpr_write_b32 a95,0
v_accvgpr_write_b32 a96,0
v_accvgpr_write_b32 a97,0
v_accvgpr_write_b32 a98,0
v_accvgpr_write_b32 a99,0
v_accvgpr_write_b32 a100,0
v_accvgpr_write_b32 a101,0
v_accvgpr_write_b32 a102,0
v_accvgpr_write_b32 a103,0
v_accvgpr_write_b32 a104,0
v_accvgpr_write_b32 a105,0
v_accvgpr_write_b32 a106,0
v_accvgpr_write_b32 a107,0
v_accvgpr_write_b32 a108,0
v_accvgpr_write_b32 a109,0
v_accvgpr_write_b32 a110,0
v_accvgpr_write_b32 a111,0
v_accvgpr_write_b32 a112,0
)A"
R"A(v_accvgpr_write_b32 a113,0
v_accvgpr_write_b32 a114,0
v_accvgpr_write_b32 a115,0
v_accvgpr_write_b32 a116,0
v_accvgpr_write_b32 a117,0
v_accvgpr_write_b32 a118,0
v_accvgpr_write_b32 a119,0
v_accvgpr_write_b32 a120,0
v_accvgpr_write_b32 a121,0
v_accvgpr_write_b32 a122,0
v_accvgpr_write_b32 a123,0
v_accvgpr_write_b32 a124,0
v_accvgpr_write_b32 a125,0
v_accvgpr_write_b32 a126,0
v_accvgpr_write_b32 a127,0
s_mov_b32 s65,0
s_mov_b32 s66,32
L_s20_%=:
s_add_u32 s67,s65,4096
buffer_load_dwordx4 a[128:131],v8,s[20:23],s65 offen
buffer_load_dwordx4 a[132:135],v8,s[20:23],s65 offen offset:64
buffer_load_dwordx4 a[136:139],v8,s[20:23],s65 offen offset:128
buffer_load_dwordx4 a[140:143],v8,s[20:23],s65 offen offset:192
buffer_load_dwordx4 a[144:147],v8,s[20:23],s67 offen
buffer_load_dwordx4 a[148:151],v8,s[20:23],s67 offen offset:64
buffer_load_dwordx4 a[152:155],v8,s[20:23],s67 offen offset:128
buffer_load_dwordx4 a[156:159],v8,s[20:23],s67 offen offset:192
s_waitcnt vmcnt(0)
v_mfma_f32_16x16x32_bf16 v[64:67],a[128:131],v[132:135],v[32:35]
v_mfma_f32_16x16x32_bf16 v[64:67],a[132:135],v[140:143],v[64:67]
v_mfma_f32_16x16x32_bf16 v[64:67],a[136:139],v[148:151],v[64:67]
v_mfma_f32_16x16x32_bf16 v[64:67],a[140:143],v[156:159],v[64:67]
v_mfma_f32_16x16x32_bf16 v[68:71],a[144:147],v[132:135],v[32:35]
v_mfma_f32_16x16x32_bf16 v[68:71],a[148:151],v[140:143],v[68:71]
v_mfma_f32_16x16x32_bf16 v[68:71],a[152:155],v[148:151],v[68:71]
v_mfma_f32_16x16x32_bf16 v[68:71],a[156:159],v[156:159],v[68:71]
v_mfma_f32_16x16x32_bf16 v[80:83],a[128:131],v[164:167],v[36:39]
v_mfma_f32_16x16x32_bf16 v[80:83],a[132:135],v[172:175],v[80:83]
v_mfma_f32_16x16x32_bf16 v[80:83],a[136:139],v[180:183],v[80:83]
v_mfma_f32_16x16x32_bf16 v[80:83],a[140:143],v[188:191],v[80:83]
v_mfma_f32_16x16x32_bf16 v[84:87],a[144:147],v[164:167],v[36:39]
v_mfma_f32_16x16x32_bf16 v[84:87],a[148:151],v[172:175],v[84:87]
v_mfma_f32_16x16x32_bf16 v[84:87],a[152:155],v[180:183],v[84:87]
v_mfma_f32_16x16x32_bf16 v[84:87],a[156:159],v[188:191],v[84:87]
v_mfma_f32_16x16x32_bf16 v[96:99],a[128:131],v[196:199],v[40:43]
v_mfma_f32_16x16x32_bf16 v[96:99],a[132:135],v[204:207],v[96:99]
v_mfma_f32_16x16x32_bf16 v[96:99],a[136:139],v[212:215],v[96:99]
v_mfma_f32_16x16x32_bf16 v[96:99],a[140:143],v[220:223],v[96:99]
v_mfma_f32_16x16x32_bf16 v[100:103],a[144:147],v[196:199],v[40:43]
v_mfma_f32_16x16x32_bf16 v[100:103],a[148:151],v[204:207],v[100:103]
v_mfma_f32_16x16x32_bf16 v[100:103],a[152:155],v[212:215],v[100:103]
v_mfma_f32_16x16x32_bf16 v[100:103],a[156:159],v[220:223],v[100:103]
v_mfma_f32_16x16x32_bf16 v[112:115],a[128:131],v[228:231],v[44:47]
v_mfma_f32_16x16x32_bf16 v[112:115],a[132:135],v[236:239],v[112:115]
v_mfma_f32_16x16x32_bf16 v[112:115],a[136:139],v[244:247],v[112:115]
v_mfma_f32_16x16x32_bf16 v[112:115],a[140:143],v[252:255],v[112:115]
v_mfma_f32_16x16x32_bf16 v[116:119],a[144:147],v[228:231],v[44:47]
v_mfma_f32_16x16x32_bf16 v[116:119],a[148:151],v[236:239],v[116:119]
v_mfma_f32_16x16x32_bf16 v[116:119],a[152:155],v[244:247],v[116:119]
v_mfma_f32_16x16x32_bf16 v[116:119],a[156:159],v[252:255],v[116:119]
s_nop 7
v_exp_f32 v64,v64
v_exp_f32 v65,v65
v_exp_f32 v66,v66
v_exp_f32 v67,v67
v_exp_f32 v68,v68
v_exp_f32 v69,v69
v_exp_f32 v70,v70
v_exp_f32 v71,v71
s_nop 0
v_cvt_pk_bf16_f32 v64,v64,v65
v_cvt_pk_bf16_f32 v65,v66,v67
v_cvt_pk_bf16_f32 v66,v68,v69
v_cvt_pk_bf16_f32 v67,v70,v71
v_exp_f32 v80,v80
v_exp_f32 v81,v81
v_exp_f32 v82,v82
v_exp_f32 v83,v83
v_exp_f32 v84,v84
v_exp_f32 v85,v85
v_exp_f32 v86,v86
v_exp_f32 v87,v87
s_nop 0
v_cvt_pk_bf16_f32 v80,v80,v81
v_cvt_pk_bf16_f32 v81,v82,v83
v_cvt_pk_bf16_f32 v82,v84,v85
v_cvt_pk_bf16_f32 v83,v86,v87
v_exp_f32 v96,v96
v_exp_f32 v97,v97
v_exp_f32 v98,v98
v_exp_f32 v99,v99
v_exp_f32 v100,v100
v_exp_f32 v101,v101
v_exp_f32 v102,v102
v_exp_f32 v103,v103
s_nop 0
v_cvt_pk_bf16_f32 v96,v96,v97
v_cvt_pk_bf16_f32 v97,v98,v99
v_cvt_pk_bf16_f32 v98,v100,v101
v_cvt_pk_bf16_f32 v99,v102,v103
v_exp_f32 v112,v112
v_exp_f32 v113,v113
v_exp_f32 v114,v114
v_exp_f32 v115,v115
v_exp_f32 v116,v116
v_exp_f32 v117,v117
v_exp_f32 v118,v118
v_exp_f32 v119,v119
s_nop 0
v_cvt_pk_bf16_f32 v112,v112,v113
v_cvt_pk_bf16_f32 v113,v114,v115
v_cvt_pk_bf16_f32 v114,v116,v117
v_cvt_pk_bf16_f32 v115,v118,v119
buffer_load_ushort v72,v9,s[24:27],s65 offen
buffer_load_ushort v73,v9,s[24:27],s65 offen offset:256
buffer_load_ushort v74,v9,s[24:27],s65 offen offset:512
buffer_load_ushort v75,v9,s[24:27],s65 offen offset:768
buffer_load_ushort v76,v10,s[24:27],s65 offen
buffer_load_ushort v77,v10,s[24:27],s65 offen offset:256
buffer_load_ushort v78,v10,s[24:27],s65 offen offset:512
buffer_load_ushort v79,v10,s[24:27],s65 offen offset:768
s_waitcnt vmcnt(0)
v_lshl_or_b32 v88,v73,16,v72
v_lshl_or_b32 v89,v75,16,v74
v_lshl_or_b32 v90,v77,16,v76
v_lshl_or_b32 v91,v79,16,v78
s_nop 1
v_mfma_f32_16x16x32_bf16 a[0:3],v[88:91],v[64:67],a[0:3]
v_mfma_f32_16x16x32_bf16 a[32:35],v[88:91],v[80:83],a[32:35]
v_mfma_f32_16x16x32_bf16 a[64:67],v[88:91],v[96:99],a[64:67]
v_mfma_f32_16x16x32_bf16 a[96:99],v[88:91],v[112:115],a[96:99]
buffer_load_ushort v72,v9,s[24:27],s65 offen offset:32
buffer_load_ushort v73,v9,s[24:27],s65 offen offset:288
buffer_load_ushort v74,v9,s[24:27],s65 offen offset:544
buffer_load_ushort v75,v9,s[24:27],s65 offen offset:800
buffer_load_ushort v76,v10,s[24:27],s65 offen offset:32
buffer_load_ushort v77,v10,s[24:27],s65 offen offset:288
buffer_load_ushort v78,v10,s[24:27],s65 offen offset:544
buffer_load_ushort v79,v10,s[24:27],s65 offen offset:800
s_waitcnt vmcnt(0)
v_lshl_or_b32 v88,v73,16,v72
v_lshl_or_b32 v89,v75,16,v74
v_lshl_or_b32 v90,v77,16,v76
v_lshl_or_b32 v91,v79,16,v78
s_nop 1
v_mfma_f32_16x16x32_bf16 a[4:7],v[88:91],v[64:67],a[4:7]
v_mfma_f32_16x16x32_bf16 a[36:39],v[88:91],v[80:83],a[36:39]
v_mfma_f32_16x16x32_bf16 a[68:71],v[88:91],v[96:99],a[68:71]
v_mfma_f32_16x16x32_bf16 a[100:103],v[88:91],v[112:115],a[100:103]
buffer_load_ushort v72,v9,s[24:27],s65 offen offset:64
buffer_load_ushort v73,v9,s[24:27],s65 offen offset:320
buffer_load_ushort v74,v9,s[24:27],s65 offen offset:576
buffer_load_ushort v75,v9,s[24:27],s65 offen offset:832
buffer_load_ushort v76,v10,s[24:27],s65 offen offset:64
buffer_load_ushort v77,v10,s[24:27],s65 offen offset:320
buffer_load_ushort v78,v10,s[24:27],s65 offen offset:576
buffer_load_ushort v79,v10,s[24:27],s65 offen offset:832
s_waitcnt vmcnt(0)
v_lshl_or_b32 v88,v73,16,v72
v_lshl_or_b32 v89,v75,16,v74
v_lshl_or_b32 v90,v77,16,v76
v_lshl_or_b32 v91,v79,16,v78
s_nop 1
v_mfma_f32_16x16x32_bf16 a[8:11],v[88:91],v[64:67],a[8:11]
v_mfma_f32_16x16x32_bf16 a[40:43],v[88:91],v[80:83],a[40:43]
v_mfma_f32_16x16x32_bf16 a[72:75],v[88:91],v[96:99],a[72:75]
v_mfma_f32_16x16x32_bf16 a[104:107],v[88:91],v[112:115],a[104:107]
buffer_load_ushort v72,v9,s[24:27],s65 offen offset:96
buffer_load_ushort v73,v9,s[24:27],s65 offen offset:352
buffer_load_ushort v74,v9,s[24:27],s65 offen offset:608
buffer_load_ushort v75,v9,s[24:27],s65 offen offset:864
buffer_load_ushort v76,v10,s[24:27],s65 offen offset:96
buffer_load_ushort v77,v10,s[24:27],s65 offen offset:352
buffer_load_ushort v78,v10,s[24:27],s65 offen offset:608
buffer_load_ushort v79,v10,s[24:27],s65 offen offset:864
s_waitcnt vmcnt(0)
v_lshl_or_b32 v88,v73,16,v72
v_lshl_or_b32 v89,v75,16,v74
v_lshl_or_b32 v90,v77,16,v76
v_lshl_or_b32 v91,v79,16,v78
s_nop 1
v_mfma_f32_16x16x32_bf16 a[12:15],v[88:91],v[64:67],a[12:15]
v_mfma_f32_16x16x32_bf16 a[44:47],v[88:91],v[80:83],a[44:47]
v_mfma_f32_16x16x32_bf16 a[76:79],v[88:91],v[96:99],a[76:79]
v_mfma_f32_16x16x32_bf16 a[108:111],v[88:91],v[112:115],a[108:111]
buffer_load_ushort v72,v9,s[24:27],s65 offen offset:128
buffer_load_ushort v73,v9,s[24:27],s65 offen offset:384
buffer_load_ushort v74,v9,s[24:27],s65 offen offset:640
buffer_load_ushort v75,v9,s[24:27],s65 offen offset:896
buffer_load_ushort v76,v10,s[24:27],s65 offen offset:128
buffer_load_ushort v77,v10,s[24:27],s65 offen offset:384
buffer_load_ushort v78,v10,s[24:27],s65 offen offset:640
buffer_load_ushort v79,v10,s[24:27],s65 offen offset:896
s_waitcnt vmcnt(0)
v_lshl_or_b32 v88,v73,16,v72
v_lshl_or_b32 v89,v75,16,v74
v_lshl_or_b32 v90,v77,16,v76
v_lshl_or_b32 v91,v79,16,v78
s_nop 1
v_mfma_f32_16x16x32_bf16 a[16:19],v[88:91],v[64:67],a[16:19]
v_mfma_f32_16x16x32_bf16 a[48:51],v[88:91],v[80:83],a[48:51]
v_mfma_f32_16x16x32_bf16 a[80:83],v[88:91],v[96:99],a[80:83]
v_mfma_f32_16x16x32_bf16 a[112:115],v[88:91],v[112:115],a[112:115]
buffer_load_ushort v72,v9,s[24:27],s65 offen offset:160
buffer_load_ushort v73,v9,s[24:27],s65 offen offset:416
buffer_load_ushort v74,v9,s[24:27],s65 offen offset:672
buffer_load_ushort v75,v9,s[24:27],s65 offen offset:928
buffer_load_ushort v76,v10,s[24:27],s65 offen offset:160
buffer_load_ushort v77,v10,s[24:27],s65 offen offset:416
buffer_load_ushort v78,v10,s[24:27],s65 offen offset:672
buffer_load_ushort v79,v10,s[24:27],s65 offen offset:928
s_waitcnt vmcnt(0)
v_lshl_or_b32 v88,v73,16,v72
v_lshl_or_b32 v89,v75,16,v74
v_lshl_or_b32 v90,v77,16,v76
v_lshl_or_b32 v91,v79,16,v78
s_nop 1
v_mfma_f32_16x16x32_bf16 a[20:23],v[88:91],v[64:67],a[20:23]
v_mfma_f32_16x16x32_bf16 a[52:55],v[88:91],v[80:83],a[52:55]
v_mfma_f32_16x16x32_bf16 a[84:87],v[88:91],v[96:99],a[84:87]
v_mfma_f32_16x16x32_bf16 a[116:119],v[88:91],v[112:115],a[116:119]
buffer_load_ushort v72,v9,s[24:27],s65 offen offset:192
buffer_load_ushort v73,v9,s[24:27],s65 offen offset:448
buffer_load_ushort v74,v9,s[24:27],s65 offen offset:704
buffer_load_ushort v75,v9,s[24:27],s65 offen offset:960
buffer_load_ushort v76,v10,s[24:27],s65 offen offset:192
buffer_load_ushort v77,v10,s[24:27],s65 offen offset:448
buffer_load_ushort v78,v10,s[24:27],s65 offen offset:704
buffer_load_ushort v79,v10,s[24:27],s65 offen offset:960
s_waitcnt vmcnt(0)
v_lshl_or_b32 v88,v73,16,v72
v_lshl_or_b32 v89,v75,16,v74
v_lshl_or_b32 v90,v77,16,v76
v_lshl_or_b32 v91,v79,16,v78
s_nop 1
v_mfma_f32_16x16x32_bf16 a[24:27],v[88:91],v[64:67],a[24:27]
v_mfma_f32_16x16x32_bf16 a[56:59],v[88:91],v[80:83],a[56:59]
v_mfma_f32_16x16x32_bf16 a[88:91],v[88:91],v[96:99],a[88:91]
v_mfma_f32_16x16x32_bf16 a[120:123],v[88:91],v[112:115],a[120:123]
buffer_load_ushort v72,v9,s[24:27],s65 offen offset:224
buffer_load_ushort v73,v9,s[24:27],s65 offen offset:480
buffer_load_ushort v74,v9,s[24:27],s65 offen offset:736
buffer_load_ushort v75,v9,s[24:27],s65 offen offset:992
buffer_load_ushort v76,v10,s[24:27],s65 offen offset:224
buffer_load_ushort v77,v10,s[24:27],s65 offen offset:480
buffer_load_ushort v78,v10,s[24:27],s65 offen offset:736
buffer_load_ushort v79,v10,s[24:27],s65 offen offset:992
s_waitcnt vmcnt(0)
v_lshl_or_b32 v88,v73,16,v72
v_lshl_or_b32 v89,v75,16,v74
v_lshl_or_b32 v90,v77,16,v76
v_lshl_or_b32 v91,v79,16,v78
s_nop 1
v_mfma_f32_16x16x32_bf16 a[28:31],v[88:91],v[64:67],a[28:31]
v_mfma_f32_16x16x32_bf16 a[60:63],v[88:91],v[80:83],a[60:63]
v_mfma_f32_16x16x32_bf16 a[92:95],v[88:91],v[96:99],a[92:95]
v_mfma_f32_16x16x32_bf16 a[124:127],v[88:91],v[112:115],a[124:127]
v_mfma_f32_16x16x32_bf16 v[48:51],v[28:31],v[64:67],v[48:51]
v_mfma_f32_16x16x32_bf16 v[52:55],v[28:31],v[80:83],v[52:55]
v_mfma_f32_16x16x32_bf16 v[56:59],v[28:31],v[96:99],v[56:59]
v_mfma_f32_16x16x32_bf16 v[60:63],v[28:31],v[112:115],v[60:63]
s_add_u32 s65,s65,8192
s_sub_u32 s66,s66,1
s_cmp_lg_u32 s66,0
s_cbranch_scc1 L_s20_%=
s_nop 7
v_max_f32 v24,0x00800000,v48
v_rcp_f32 v24,v24
v_max_f32 v25,0x00800000,v52
v_rcp_f32 v25,v25
v_max_f32 v26,0x00800000,v56
v_rcp_f32 v26,v26
v_max_f32 v27,0x00800000,v60
v_rcp_f32 v27,v27
v_accvgpr_read_b32 v8,a0
v_accvgpr_read_b32 v9,a1
v_accvgpr_read_b32 v10,a2
v_accvgpr_read_b32 v11,a3
v_accvgpr_read_b32 v12,a4
v_accvgpr_read_b32 v13,a5
v_accvgpr_read_b32 v14,a6
v_accvgpr_read_b32 v15,a7
v_mul_f32 v8,v8,v24
v_mul_f32 v9,v9,v24
v_mul_f32 v10,v10,v24
v_mul_f32 v11,v11,v24
v_mul_f32 v12,v12,v24
v_mul_f32 v13,v13,v24
v_mul_f32 v14,v14,v24
v_mul_f32 v15,v15,v24
v_cvt_pk_bf16_f32 v8,v8,v9
v_cvt_pk_bf16_f32 v9,v10,v11
v_cvt_pk_bf16_f32 v10,v12,v13
v_cvt_pk_bf16_f32 v11,v14,v15
ds_write_b128 v7,v[8:11] offset:6144
)A"
R"A(v_accvgpr_read_b32 v16,a8
v_accvgpr_read_b32 v17,a9
v_accvgpr_read_b32 v18,a10
v_accvgpr_read_b32 v19,a11
v_accvgpr_read_b32 v20,a12
v_accvgpr_read_b32 v21,a13
v_accvgpr_read_b32 v22,a14
v_accvgpr_read_b32 v23,a15
v_mul_f32 v16,v16,v24
v_mul_f32 v17,v17,v24
v_mul_f32 v18,v18,v24
v_mul_f32 v19,v19,v24
v_mul_f32 v20,v20,v24
v_mul_f32 v21,v21,v24
v_mul_f32 v22,v22,v24
v_mul_f32 v23,v23,v24
v_cvt_pk_bf16_f32 v16,v16,v17
v_cvt_pk_bf16_f32 v17,v18,v19
v_cvt_pk_bf16_f32 v18,v20,v21
v_cvt_pk_bf16_f32 v19,v22,v23
ds_write_b128 v7,v[16:19] offset:7168
v_accvgpr_read_b32 v8,a16
v_accvgpr_read_b32 v9,a17
v_accvgpr_read_b32 v10,a18
v_accvgpr_read_b32 v11,a19
v_accvgpr_read_b32 v12,a20
v_accvgpr_read_b32 v13,a21
v_accvgpr_read_b32 v14,a22
v_accvgpr_read_b32 v15,a23
v_mul_f32 v8,v8,v24
v_mul_f32 v9,v9,v24
v_mul_f32 v10,v10,v24
v_mul_f32 v11,v11,v24
v_mul_f32 v12,v12,v24
v_mul_f32 v13,v13,v24
v_mul_f32 v14,v14,v24
v_mul_f32 v15,v15,v24
v_cvt_pk_bf16_f32 v8,v8,v9
v_cvt_pk_bf16_f32 v9,v10,v11
v_cvt_pk_bf16_f32 v10,v12,v13
v_cvt_pk_bf16_f32 v11,v14,v15
ds_write_b128 v7,v[8:11]
v_accvgpr_read_b32 v16,a24
v_accvgpr_read_b32 v17,a25
v_accvgpr_read_b32 v18,a26
v_accvgpr_read_b32 v19,a27
v_accvgpr_read_b32 v20,a28
v_accvgpr_read_b32 v21,a29
v_accvgpr_read_b32 v22,a30
v_accvgpr_read_b32 v23,a31
v_mul_f32 v16,v16,v24
v_mul_f32 v17,v17,v24
v_mul_f32 v18,v18,v24
v_mul_f32 v19,v19,v24
v_mul_f32 v20,v20,v24
v_mul_f32 v21,v21,v24
v_mul_f32 v22,v22,v24
v_mul_f32 v23,v23,v24
v_cvt_pk_bf16_f32 v16,v16,v17
v_cvt_pk_bf16_f32 v17,v18,v19
v_cvt_pk_bf16_f32 v18,v20,v21
v_cvt_pk_bf16_f32 v19,v22,v23
ds_write_b128 v7,v[16:19] offset:1024
v_accvgpr_read_b32 v32,a160
v_accvgpr_read_b32 v33,a161
v_accvgpr_read_b32 v34,a162
v_accvgpr_read_b32 v35,a163
v_accvgpr_read_b32 v36,a164
v_accvgpr_read_b32 v37,a165
v_accvgpr_read_b32 v38,a166
v_accvgpr_read_b32 v39,a167
v_accvgpr_read_b32 v40,a168
v_accvgpr_read_b32 v41,a169
v_accvgpr_read_b32 v42,a170
v_accvgpr_read_b32 v43,a171
v_accvgpr_read_b32 v44,a172
v_accvgpr_read_b32 v45,a173
v_accvgpr_read_b32 v46,a174
v_accvgpr_read_b32 v47,a175
s_waitcnt lgkmcnt(0)
v_subrev_u32 v8,s78,v1
ds_read_b128 a[128:131],v8
ds_read_b128 a[132:135],v8 offset:256
ds_read_b128 a[136:139],v8 offset:512
ds_read_b128 a[140:143],v8 offset:768
ds_read_b128 a[144:147],v8 offset:4096
ds_read_b128 a[148:151],v8 offset:4352
ds_read_b128 a[152:155],v8 offset:4608
ds_read_b128 a[156:159],v8 offset:4864
ds_read_b128 a[160:163],v8 offset:8192
ds_read_b128 a[164:167],v8 offset:8448
ds_read_b128 a[168:171],v8 offset:8704
ds_read_b128 a[172:175],v8 offset:8960
ds_read_b128 a[176:179],v8 offset:12288
ds_read_b128 a[180:183],v8 offset:12544
ds_read_b128 a[184:187],v8 offset:12800
ds_read_b128 a[188:191],v8 offset:13056
s_waitcnt lgkmcnt(15)
v_mfma_f32_16x16x32_bf16 v[64:67],a[128:131],v[128:131],v[32:35]
s_waitcnt lgkmcnt(14)
v_mfma_f32_16x16x32_bf16 v[64:67],a[132:135],v[136:139],v[64:67]
s_waitcnt lgkmcnt(13)
v_mfma_f32_16x16x32_bf16 v[64:67],a[136:139],v[144:147],v[64:67]
s_waitcnt lgkmcnt(12)
v_mfma_f32_16x16x32_bf16 v[64:67],a[140:143],v[152:155],v[64:67]
s_waitcnt lgkmcnt(8)
v_mfma_f32_16x16x32_bf16 v[68:71],a[156:159],v[152:155],v[32:35]
v_mfma_f32_16x16x32_bf16 v[68:71],a[152:155],v[144:147],v[68:71]
v_mfma_f32_16x16x32_bf16 v[68:71],a[148:151],v[136:139],v[68:71]
v_mfma_f32_16x16x32_bf16 v[68:71],a[144:147],v[128:131],v[68:71]
s_waitcnt lgkmcnt(0)
v_mfma_f32_16x16x32_bf16 v[72:75],a[160:163],v[128:131],v[32:35]
v_mfma_f32_16x16x32_bf16 v[72:75],a[164:167],v[136:139],v[72:75]
v_mfma_f32_16x16x32_bf16 v[72:75],a[168:171],v[144:147],v[72:75]
v_mfma_f32_16x16x32_bf16 v[72:75],a[172:175],v[152:155],v[72:75]
v_mfma_f32_16x16x32_bf16 v[76:79],a[188:191],v[152:155],v[32:35]
v_mfma_f32_16x16x32_bf16 v[76:79],a[184:187],v[144:147],v[76:79]
v_mfma_f32_16x16x32_bf16 v[76:79],a[180:183],v[136:139],v[76:79]
v_mfma_f32_16x16x32_bf16 v[76:79],a[176:179],v[128:131],v[76:79]
v_mfma_f32_16x16x32_bf16 v[80:83],a[128:131],v[160:163],v[36:39]
v_mfma_f32_16x16x32_bf16 v[80:83],a[132:135],v[168:171],v[80:83]
v_mfma_f32_16x16x32_bf16 v[80:83],a[136:139],v[176:179],v[80:83]
v_mfma_f32_16x16x32_bf16 v[80:83],a[140:143],v[184:187],v[80:83]
v_mfma_f32_16x16x32_bf16 v[84:87],a[156:159],v[184:187],v[36:39]
v_mfma_f32_16x16x32_bf16 v[84:87],a[152:155],v[176:179],v[84:87]
v_mfma_f32_16x16x32_bf16 v[84:87],a[148:151],v[168:171],v[84:87]
v_mfma_f32_16x16x32_bf16 v[84:87],a[144:147],v[160:163],v[84:87]
v_mfma_f32_16x16x32_bf16 v[88:91],a[160:163],v[160:163],v[36:39]
v_mfma_f32_16x16x32_bf16 v[88:91],a[164:167],v[168:171],v[88:91]
v_mfma_f32_16x16x32_bf16 v[88:91],a[168:171],v[176:179],v[88:91]
v_mfma_f32_16x16x32_bf16 v[88:91],a[172:175],v[184:187],v[88:91]
v_mfma_f32_16x16x32_bf16 v[92:95],a[188:191],v[184:187],v[36:39]
v_mfma_f32_16x16x32_bf16 v[92:95],a[184:187],v[176:179],v[92:95]
v_mfma_f32_16x16x32_bf16 v[92:95],a[180:183],v[168:171],v[92:95]
v_mfma_f32_16x16x32_bf16 v[92:95],a[176:179],v[160:163],v[92:95]
v_mfma_f32_16x16x32_bf16 v[96:99],a[128:131],v[192:195],v[40:43]
v_mfma_f32_16x16x32_bf16 v[96:99],a[132:135],v[200:203],v[96:99]
v_mfma_f32_16x16x32_bf16 v[96:99],a[136:139],v[208:211],v[96:99]
v_mfma_f32_16x16x32_bf16 v[96:99],a[140:143],v[216:219],v[96:99]
v_mfma_f32_16x16x32_bf16 v[100:103],a[156:159],v[216:219],v[40:43]
v_mfma_f32_16x16x32_bf16 v[100:103],a[152:155],v[208:211],v[100:103]
v_mfma_f32_16x16x32_bf16 v[100:103],a[148:151],v[200:203],v[100:103]
v_mfma_f32_16x16x32_bf16 v[100:103],a[144:147],v[192:195],v[100:103]
v_mfma_f32_16x16x32_bf16 v[104:107],a[160:163],v[192:195],v[40:43]
v_mfma_f32_16x16x32_bf16 v[104:107],a[164:167],v[200:203],v[104:107]
v_mfma_f32_16x16x32_bf16 v[104:107],a[168:171],v[208:211],v[104:107]
v_mfma_f32_16x16x32_bf16 v[104:107],a[172:175],v[216:219],v[104:107]
v_mfma_f32_16x16x32_bf16 v[108:111],a[188:191],v[216:219],v[40:43]
v_mfma_f32_16x16x32_bf16 v[108:111],a[184:187],v[208:211],v[108:111]
v_mfma_f32_16x16x32_bf16 v[108:111],a[180:183],v[200:203],v[108:111]
v_mfma_f32_16x16x32_bf16 v[108:111],a[176:179],v[192:195],v[108:111]
v_exp_f32 v64,v64
v_exp_f32 v65,v65
s_nop 0
v_cvt_pk_bf16_f32 v64,v64,v65
v_exp_f32 v66,v66
v_exp_f32 v67,v67
s_nop 0
v_cvt_pk_bf16_f32 v65,v66,v67
v_exp_f32 v68,v68
v_exp_f32 v69,v69
s_nop 0
v_cvt_pk_bf16_f32 v66,v68,v69
v_exp_f32 v70,v70
v_exp_f32 v71,v71
s_nop 0
v_cvt_pk_bf16_f32 v67,v70,v71
v_exp_f32 v72,v72
v_exp_f32 v73,v73
s_nop 0
v_cvt_pk_bf16_f32 v72,v72,v73
v_exp_f32 v74,v74
v_exp_f32 v75,v75
s_nop 0
v_cvt_pk_bf16_f32 v73,v74,v75
v_exp_f32 v76,v76
v_exp_f32 v77,v77
s_nop 0
v_cvt_pk_bf16_f32 v74,v76,v77
v_exp_f32 v78,v78
v_exp_f32 v79,v79
s_nop 0
v_cvt_pk_bf16_f32 v75,v78,v79
v_exp_f32 v80,v80
v_exp_f32 v81,v81
s_nop 0
v_cvt_pk_bf16_f32 v80,v80,v81
v_exp_f32 v82,v82
v_exp_f32 v83,v83
s_nop 0
v_cvt_pk_bf16_f32 v81,v82,v83
v_exp_f32 v84,v84
v_exp_f32 v85,v85
s_nop 0
v_cvt_pk_bf16_f32 v82,v84,v85
v_exp_f32 v86,v86
v_exp_f32 v87,v87
s_nop 0
v_cvt_pk_bf16_f32 v83,v86,v87
v_exp_f32 v88,v88
v_exp_f32 v89,v89
s_nop 0
v_cvt_pk_bf16_f32 v88,v88,v89
v_mfma_f32_16x16x32_bf16 a[0:3],a[192:195],v[64:67],0
v_mfma_f32_16x16x32_bf16 a[0:3],a[196:199],v[72:75],a[0:3]
v_mfma_f32_16x16x32_bf16 a[4:7],a[200:203],v[64:67],0
v_mfma_f32_16x16x32_bf16 a[4:7],a[204:207],v[72:75],a[4:7]
v_mfma_f32_16x16x32_bf16 a[8:11],a[208:211],v[64:67],0
v_mfma_f32_16x16x32_bf16 a[8:11],a[212:215],v[72:75],a[8:11]
v_mfma_f32_16x16x32_bf16 a[12:15],a[216:219],v[64:67],0
v_mfma_f32_16x16x32_bf16 a[12:15],a[220:223],v[72:75],a[12:15]
v_mfma_f32_16x16x32_bf16 a[16:19],a[224:227],v[64:67],0
v_mfma_f32_16x16x32_bf16 a[16:19],a[228:231],v[72:75],a[16:19]
v_mfma_f32_16x16x32_bf16 a[20:23],a[232:235],v[64:67],0
v_mfma_f32_16x16x32_bf16 a[20:23],a[236:239],v[72:75],a[20:23]
v_mfma_f32_16x16x32_bf16 a[24:27],a[240:243],v[64:67],0
v_mfma_f32_16x16x32_bf16 a[24:27],a[244:247],v[72:75],a[24:27]
v_mfma_f32_16x16x32_bf16 a[28:31],a[248:251],v[64:67],0
v_mfma_f32_16x16x32_bf16 a[28:31],a[252:255],v[72:75],a[28:31]
v_mfma_f32_16x16x32_bf16 v[48:51],v[28:31],v[64:67],0
v_mfma_f32_16x16x32_bf16 v[48:51],v[28:31],v[72:75],v[48:51]
s_waitcnt vmcnt(0) lgkmcnt(0)
s_nop 7
s_branch L_ok0_%=
L_slow1_%=:
s_waitcnt vmcnt(0) lgkmcnt(0)
s_nop 7
v_bfe_u32 v11,v6,4,4
v_lshrrev_b32 v12,8,v6
v_lshlrev_b32 v8,8,v11
v_lshl_add_u32 v8,v12,4,v8
v_lshlrev_b32 v9,10,v12
v_lshl_add_u32 v9,v11,1,v9
v_add_u32 v10,4096,v9
v_mov_b32 v48,0xff800000
v_mov_b32 v52,0xff800000
v_mov_b32 v56,0xff800000
v_mov_b32 v60,0xff800000
s_mov_b32 s65,262144
s_mov_b32 s66,64
L_s11_%=:
buffer_load_dwordx4 a[128:131],v8,s[20:23],s65 offen
buffer_load_dwordx4 a[132:135],v8,s[20:23],s65 offen offset:64
buffer_load_dwordx4 a[136:139],v8,s[20:23],s65 offen offset:128
buffer_load_dwordx4 a[140:143],v8,s[20:23],s65 offen offset:192
s_waitcnt vmcnt(0)
v_mfma_f32_16x16x32_bf16 v[64:67],a[128:131],v[128:131],0
v_mfma_f32_16x16x32_bf16 v[64:67],a[132:135],v[136:139],v[64:67]
v_mfma_f32_16x16x32_bf16 v[64:67],a[136:139],v[144:147],v[64:67]
v_mfma_f32_16x16x32_bf16 v[64:67],a[140:143],v[152:155],v[64:67]
v_mfma_f32_16x16x32_bf16 v[80:83],a[128:131],v[160:163],0
v_mfma_f32_16x16x32_bf16 v[80:83],a[132:135],v[168:171],v[80:83]
v_mfma_f32_16x16x32_bf16 v[80:83],a[136:139],v[176:179],v[80:83]
v_mfma_f32_16x16x32_bf16 v[80:83],a[140:143],v[184:187],v[80:83]
v_mfma_f32_16x16x32_bf16 v[96:99],a[128:131],v[192:195],0
v_mfma_f32_16x16x32_bf16 v[96:99],a[132:135],v[200:203],v[96:99]
v_mfma_f32_16x16x32_bf16 v[96:99],a[136:139],v[208:211],v[96:99]
v_mfma_f32_16x16x32_bf16 v[96:99],a[140:143],v[216:219],v[96:99]
v_mfma_f32_16x16x32_bf16 v[112:115],a[128:131],v[224:227],0
v_mfma_f32_16x16x32_bf16 v[112:115],a[132:135],v[232:235],v[112:115]
v_mfma_f32_16x16x32_bf16 v[112:115],a[136:139],v[240:243],v[112:115]
v_mfma_f32_16x16x32_bf16 v[112:115],a[140:143],v[248:251],v[112:115]
s_nop 7
v_max3_f32 v48,v48,v64,v65
v_max3_f32 v48,v48,v66,v67
v_max3_f32 v52,v52,v80,v81
v_max3_f32 v52,v52,v82,v83
v_max3_f32 v56,v56,v96,v97
v_max3_f32 v56,v56,v98,v99
v_max3_f32 v60,v60,v112,v113
v_max3_f32 v60,v60,v114,v115
s_add_u32 s65,s65,4096
s_sub_u32 s66,s66,1
s_cmp_lg_u32 s66,0
s_cbranch_scc1 L_s11_%=
v_mov_b32 v11,v48
s_nop 1
v_permlane32_swap_b32 v48,v11
v_max_f32 v48,v48,v11
v_mov_b32 v11,v48
s_nop 1
v_permlane16_swap_b32 v48,v11
v_max_f32 v48,v48,v11
v_sub_f32 v32,0,v48
v_sub_f32 v33,0,v48
v_sub_f32 v34,0,v48
v_sub_f32 v35,0,v48
v_mov_b32 v11,v52
s_nop 1
v_permlane32_swap_b32 v52,v11
v_max_f32 v52,v52,v11
v_mov_b32 v11,v52
s_nop 1
v_permlane16_swap_b32 v52,v11
v_max_f32 v52,v52,v11
v_sub_f32 v36,0,v52
v_sub_f32 v37,0,v52
v_sub_f32 v38,0,v52
v_sub_f32 v39,0,v52
v_mov_b32 v11,v56
s_nop 1
v_permlane32_swap_b32 v56,v11
v_max_f32 v56,v56,v11
v_mov_b32 v11,v56
s_nop 1
v_permlane16_swap_b32 v56,v11
v_max_f32 v56,v56,v11
v_sub_f32 v40,0,v56
v_sub_f32 v41,0,v56
v_sub_f32 v42,0,v56
v_sub_f32 v43,0,v56
v_mov_b32 v11,v60
s_nop 1
v_permlane32_swap_b32 v60,v11
v_max_f32 v60,v60,v11
v_mov_b32 v11,v60
s_nop 1
v_permlane16_swap_b32 v60,v11
v_max_f32 v60,v60,v11
v_max_f32 v60,s72,v60
v_sub_f32 v44,0,v60
v_sub_f32 v45,0,v60
v_sub_f32 v46,0,v60
v_sub_f32 v47,0,v60
v_mov_b32 v48,0
v_mov_b32 v49,0
v_mov_b32 v50,0
v_mov_b32 v51,0
v_mov_b32 v52,0
v_mov_b32 v53,0
v_mov_b32 v54,0
v_mov_b32 v55,0
v_mov_b32 v56,0
v_mov_b32 v57,0
v_mov_b32 v58,0
v_mov_b32 v59,0
v_mov_b32 v60,0
v_mov_b32 v61,0
v_mov_b32 v62,0
v_mov_b32 v63,0
v_accvgpr_write_b32 a0,0
v_accvgpr_write_b32 a1,0
v_accvgpr_write_b32 a2,0
v_accvgpr_write_b32 a3,0
v_accvgpr_write_b32 a4,0
v_accvgpr_write_b32 a5,0
v_accvgpr_write_b32 a6,0
v_accvgpr_write_b32 a7,0
v_accvgpr_write_b32 a8,0
v_accvgpr_write_b32 a9,0
v_accvgpr_write_b32 a10,0
v_accvgpr_write_b32 a11,0
v_accvgpr_write_b32 a12,0
v_accvgpr_write_b32 a13,0
v_accvgpr_write_b32 a14,0
v_accvgpr_write_b32 a15,0
v_accvgpr_write_b32 a16,0
v_accvgpr_write_b32 a17,0
v_accvgpr_write_b32 a18,0
v_accvgpr_write_b32 a19,0
v_accvgpr_write_b32 a20,0
v_accvgpr_write_b32 a21,0
v_accvgpr_write_b32 a22,0
v_accvgpr_write_b32 a23,0
v_accvgpr_write_b32 a24,0
v_accvgpr_write_b32 a25,0
v_accvgpr_write_b32 a26,0
v_accvgpr_write_b32 a27,0
v_accvgpr_write_b32 a28,0
)A"
R"A(v_accvgpr_write_b32 a29,0
v_accvgpr_write_b32 a30,0
v_accvgpr_write_b32 a31,0
v_mov_b32 v132,0
v_mov_b32 v133,0
v_mov_b32 v134,0
v_mov_b32 v135,0
v_mov_b32 v140,0
v_mov_b32 v141,0
v_mov_b32 v142,0
v_mov_b32 v143,0
v_mov_b32 v148,0
v_mov_b32 v149,0
v_mov_b32 v150,0
v_mov_b32 v151,0
v_mov_b32 v156,0
v_mov_b32 v157,0
v_mov_b32 v158,0
v_mov_b32 v159,0
v_mov_b32 v164,0
v_mov_b32 v165,0
v_mov_b32 v166,0
v_mov_b32 v167,0
v_mov_b32 v172,0
v_mov_b32 v173,0
v_mov_b32 v174,0
v_mov_b32 v175,0
v_mov_b32 v180,0
v_mov_b32 v181,0
v_mov_b32 v182,0
v_mov_b32 v183,0
v_mov_b32 v188,0
v_mov_b32 v189,0
v_mov_b32 v190,0
v_mov_b32 v191,0
v_mov_b32 v196,0
v_mov_b32 v197,0
v_mov_b32 v198,0
v_mov_b32 v199,0
v_mov_b32 v204,0
v_mov_b32 v205,0
v_mov_b32 v206,0
v_mov_b32 v207,0
v_mov_b32 v212,0
v_mov_b32 v213,0
v_mov_b32 v214,0
v_mov_b32 v215,0
v_mov_b32 v220,0
v_mov_b32 v221,0
v_mov_b32 v222,0
v_mov_b32 v223,0
v_mov_b32 v228,0
v_mov_b32 v229,0
v_mov_b32 v230,0
v_mov_b32 v231,0
v_mov_b32 v236,0
v_mov_b32 v237,0
v_mov_b32 v238,0
v_mov_b32 v239,0
v_mov_b32 v244,0
v_mov_b32 v245,0
v_mov_b32 v246,0
v_mov_b32 v247,0
v_mov_b32 v252,0
v_mov_b32 v253,0
v_mov_b32 v254,0
v_mov_b32 v255,0
v_accvgpr_write_b32 a32,0
v_accvgpr_write_b32 a33,0
v_accvgpr_write_b32 a34,0
v_accvgpr_write_b32 a35,0
v_accvgpr_write_b32 a36,0
v_accvgpr_write_b32 a37,0
v_accvgpr_write_b32 a38,0
v_accvgpr_write_b32 a39,0
v_accvgpr_write_b32 a40,0
v_accvgpr_write_b32 a41,0
v_accvgpr_write_b32 a42,0
v_accvgpr_write_b32 a43,0
v_accvgpr_write_b32 a44,0
v_accvgpr_write_b32 a45,0
v_accvgpr_write_b32 a46,0
v_accvgpr_write_b32 a47,0
v_accvgpr_write_b32 a48,0
v_accvgpr_write_b32 a49,0
v_accvgpr_write_b32 a50,0
v_accvgpr_write_b32 a51,0
v_accvgpr_write_b32 a52,0
v_accvgpr_write_b32 a53,0
v_accvgpr_write_b32 a54,0
v_accvgpr_write_b32 a55,0
v_accvgpr_write_b32 a56,0
v_accvgpr_write_b32 a57,0
v_accvgpr_write_b32 a58,0
v_accvgpr_write_b32 a59,0
v_accvgpr_write_b32 a60,0
v_accvgpr_write_b32 a61,0
v_accvgpr_write_b32 a62,0
v_accvgpr_write_b32 a63,0
s_mov_b32 s65,262144
s_mov_b32 s66,32
L_s21_%=:
s_add_u32 s67,s65,4096
buffer_load_dwordx4 a[128:131],v8,s[20:23],s65 offen
buffer_load_dwordx4 a[132:135],v8,s[20:23],s65 offen offset:64
buffer_load_dwordx4 a[136:139],v8,s[20:23],s65 offen offset:128
buffer_load_dwordx4 a[140:143],v8,s[20:23],s65 offen offset:192
buffer_load_dwordx4 a[144:147],v8,s[20:23],s67 offen
buffer_load_dwordx4 a[148:151],v8,s[20:23],s67 offen offset:64
buffer_load_dwordx4 a[152:155],v8,s[20:23],s67 offen offset:128
buffer_load_dwordx4 a[156:159],v8,s[20:23],s67 offen offset:192
s_waitcnt vmcnt(0)
v_mfma_f32_16x16x32_bf16 v[64:67],a[128:131],v[128:131],v[32:35]
v_mfma_f32_16x16x32_bf16 v[64:67],a[132:135],v[136:139],v[64:67]
v_mfma_f32_16x16x32_bf16 v[64:67],a[136:139],v[144:147],v[64:67]
v_mfma_f32_16x16x32_bf16 v[64:67],a[140:143],v[152:155],v[64:67]
v_mfma_f32_16x16x32_bf16 v[68:71],a[144:147],v[128:131],v[32:35]
v_mfma_f32_16x16x32_bf16 v[68:71],a[148:151],v[136:139],v[68:71]
v_mfma_f32_16x16x32_bf16 v[68:71],a[152:155],v[144:147],v[68:71]
v_mfma_f32_16x16x32_bf16 v[68:71],a[156:159],v[152:155],v[68:71]
v_mfma_f32_16x16x32_bf16 v[80:83],a[128:131],v[160:163],v[36:39]
v_mfma_f32_16x16x32_bf16 v[80:83],a[132:135],v[168:171],v[80:83]
v_mfma_f32_16x16x32_bf16 v[80:83],a[136:139],v[176:179],v[80:83]
v_mfma_f32_16x16x32_bf16 v[80:83],a[140:143],v[184:187],v[80:83]
v_mfma_f32_16x16x32_bf16 v[84:87],a[144:147],v[160:163],v[36:39]
v_mfma_f32_16x16x32_bf16 v[84:87],a[148:151],v[168:171],v[84:87]
v_mfma_f32_16x16x32_bf16 v[84:87],a[152:155],v[176:179],v[84:87]
v_mfma_f32_16x16x32_bf16 v[84:87],a[156:159],v[184:187],v[84:87]
v_mfma_f32_16x16x32_bf16 v[96:99],a[128:131],v[192:195],v[40:43]
v_mfma_f32_16x16x32_bf16 v[96:99],a[132:135],v[200:203],v[96:99]
v_mfma_f32_16x16x32_bf16 v[96:99],a[136:139],v[208:211],v[96:99]
v_mfma_f32_16x16x32_bf16 v[96:99],a[140:143],v[216:219],v[96:99]
v_mfma_f32_16x16x32_bf16 v[100:103],a[144:147],v[192:195],v[40:43]
v_mfma_f32_16x16x32_bf16 v[100:103],a[148:151],v[200:203],v[100:103]
v_mfma_f32_16x16x32_bf16 v[100:103],a[152:155],v[208:211],v[100:103]
v_mfma_f32_16x16x32_bf16 v[100:103],a[156:159],v[216:219],v[100:103]
v_mfma_f32_16x16x32_bf16 v[112:115],a[128:131],v[224:227],v[44:47]
v_mfma_f32_16x16x32_bf16 v[112:115],a[132:135],v[232:235],v[112:115]
v_mfma_f32_16x16x32_bf16 v[112:115],a[136:139],v[240:243],v[112:115]
v_mfma_f32_16x16x32_bf16 v[112:115],a[140:143],v[248:251],v[112:115]
v_mfma_f32_16x16x32_bf16 v[116:119],a[144:147],v[224:227],v[44:47]
v_mfma_f32_16x16x32_bf16 v[116:119],a[148:151],v[232:235],v[116:119]
v_mfma_f32_16x16x32_bf16 v[116:119],a[152:155],v[240:243],v[116:119]
v_mfma_f32_16x16x32_bf16 v[116:119],a[156:159],v[248:251],v[116:119]
s_nop 7
v_exp_f32 v64,v64
v_exp_f32 v65,v65
v_exp_f32 v66,v66
v_exp_f32 v67,v67
v_exp_f32 v68,v68
v_exp_f32 v69,v69
v_exp_f32 v70,v70
v_exp_f32 v71,v71
s_nop 0
v_cvt_pk_bf16_f32 v64,v64,v65
v_cvt_pk_bf16_f32 v65,v66,v67
v_cvt_pk_bf16_f32 v66,v68,v69
v_cvt_pk_bf16_f32 v67,v70,v71
v_exp_f32 v80,v80
v_exp_f32 v81,v81
v_exp_f32 v82,v82
v_exp_f32 v83,v83
v_exp_f32 v84,v84
v_exp_f32 v85,v85
v_exp_f32 v86,v86
v_exp_f32 v87,v87
s_nop 0
v_cvt_pk_bf16_f32 v80,v80,v81
v_cvt_pk_bf16_f32 v81,v82,v83
v_cvt_pk_bf16_f32 v82,v84,v85
v_cvt_pk_bf16_f32 v83,v86,v87
v_exp_f32 v96,v96
v_exp_f32 v97,v97
v_exp_f32 v98,v98
v_exp_f32 v99,v99
v_exp_f32 v100,v100
v_exp_f32 v101,v101
v_exp_f32 v102,v102
v_exp_f32 v103,v103
s_nop 0
v_cvt_pk_bf16_f32 v96,v96,v97
v_cvt_pk_bf16_f32 v97,v98,v99
v_cvt_pk_bf16_f32 v98,v100,v101
v_cvt_pk_bf16_f32 v99,v102,v103
v_exp_f32 v112,v112
v_exp_f32 v113,v113
v_exp_f32 v114,v114
v_exp_f32 v115,v115
v_exp_f32 v116,v116
v_exp_f32 v117,v117
v_exp_f32 v118,v118
v_exp_f32 v119,v119
s_nop 0
v_cvt_pk_bf16_f32 v112,v112,v113
v_cvt_pk_bf16_f32 v113,v114,v115
v_cvt_pk_bf16_f32 v114,v116,v117
v_cvt_pk_bf16_f32 v115,v118,v119
buffer_load_ushort v72,v9,s[24:27],s65 offen
buffer_load_ushort v73,v9,s[24:27],s65 offen offset:256
buffer_load_ushort v74,v9,s[24:27],s65 offen offset:512
buffer_load_ushort v75,v9,s[24:27],s65 offen offset:768
buffer_load_ushort v76,v10,s[24:27],s65 offen
buffer_load_ushort v77,v10,s[24:27],s65 offen offset:256
buffer_load_ushort v78,v10,s[24:27],s65 offen offset:512
buffer_load_ushort v79,v10,s[24:27],s65 offen offset:768
s_waitcnt vmcnt(0)
v_lshl_or_b32 v88,v73,16,v72
v_lshl_or_b32 v89,v75,16,v74
v_lshl_or_b32 v90,v77,16,v76
v_lshl_or_b32 v91,v79,16,v78
s_nop 1
v_mfma_f32_16x16x32_bf16 a[0:3],v[88:91],v[64:67],a[0:3]
v_mfma_f32_16x16x32_bf16 v[132:135],v[88:91],v[80:83],v[132:135]
v_mfma_f32_16x16x32_bf16 v[196:199],v[88:91],v[96:99],v[196:199]
v_mfma_f32_16x16x32_bf16 a[32:35],v[88:91],v[112:115],a[32:35]
buffer_load_ushort v72,v9,s[24:27],s65 offen offset:32
buffer_load_ushort v73,v9,s[24:27],s65 offen offset:288
buffer_load_ushort v74,v9,s[24:27],s65 offen offset:544
buffer_load_ushort v75,v9,s[24:27],s65 offen offset:800
buffer_load_ushort v76,v10,s[24:27],s65 offen offset:32
buffer_load_ushort v77,v10,s[24:27],s65 offen offset:288
buffer_load_ushort v78,v10,s[24:27],s65 offen offset:544
buffer_load_ushort v79,v10,s[24:27],s65 offen offset:800
s_waitcnt vmcnt(0)
v_lshl_or_b32 v88,v73,16,v72
v_lshl_or_b32 v89,v75,16,v74
v_lshl_or_b32 v90,v77,16,v76
v_lshl_or_b32 v91,v79,16,v78
s_nop 1
v_mfma_f32_16x16x32_bf16 a[4:7],v[88:91],v[64:67],a[4:7]
v_mfma_f32_16x16x32_bf16 v[140:143],v[88:91],v[80:83],v[140:143]
v_mfma_f32_16x16x32_bf16 v[204:207],v[88:91],v[96:99],v[204:207]
v_mfma_f32_16x16x32_bf16 a[36:39],v[88:91],v[112:115],a[36:39]
buffer_load_ushort v72,v9,s[24:27],s65 offen offset:64
buffer_load_ushort v73,v9,s[24:27],s65 offen offset:320
buffer_load_ushort v74,v9,s[24:27],s65 offen offset:576
buffer_load_ushort v75,v9,s[24:27],s65 offen offset:832
buffer_load_ushort v76,v10,s[24:27],s65 offen offset:64
buffer_load_ushort v77,v10,s[24:27],s65 offen offset:320
buffer_load_ushort v78,v10,s[24:27],s65 offen offset:576
buffer_load_ushort v79,v10,s[24:27],s65 offen offset:832
s_waitcnt vmcnt(0)
v_lshl_or_b32 v88,v73,16,v72
v_lshl_or_b32 v89,v75,16,v74
v_lshl_or_b32 v90,v77,16,v76
v_lshl_or_b32 v91,v79,16,v78
s_nop 1
v_mfma_f32_16x16x32_bf16 a[8:11],v[88:91],v[64:67],a[8:11]
v_mfma_f32_16x16x32_bf16 v[148:151],v[88:91],v[80:83],v[148:151]
v_mfma_f32_16x16x32_bf16 v[212:215],v[88:91],v[96:99],v[212:215]
v_mfma_f32_16x16x32_bf16 a[40:43],v[88:91],v[112:115],a[40:43]
buffer_load_ushort v72,v9,s[24:27],s65 offen offset:96
buffer_load_ushort v73,v9,s[24:27],s65 offen offset:352
buffer_load_ushort v74,v9,s[24:27],s65 offen offset:608
buffer_load_ushort v75,v9,s[24:27],s65 offen offset:864
buffer_load_ushort v76,v10,s[24:27],s65 offen offset:96
buffer_load_ushort v77,v10,s[24:27],s65 offen offset:352
buffer_load_ushort v78,v10,s[24:27],s65 offen offset:608
buffer_load_ushort v79,v10,s[24:27],s65 offen offset:864
s_waitcnt vmcnt(0)
v_lshl_or_b32 v88,v73,16,v72
v_lshl_or_b32 v89,v75,16,v74
v_lshl_or_b32 v90,v77,16,v76
v_lshl_or_b32 v91,v79,16,v78
s_nop 1
v_mfma_f32_16x16x32_bf16 a[12:15],v[88:91],v[64:67],a[12:15]
v_mfma_f32_16x16x32_bf16 v[156:159],v[88:91],v[80:83],v[156:159]
v_mfma_f32_16x16x32_bf16 v[220:223],v[88:91],v[96:99],v[220:223]
v_mfma_f32_16x16x32_bf16 a[44:47],v[88:91],v[112:115],a[44:47]
buffer_load_ushort v72,v9,s[24:27],s65 offen offset:128
buffer_load_ushort v73,v9,s[24:27],s65 offen offset:384
buffer_load_ushort v74,v9,s[24:27],s65 offen offset:640
buffer_load_ushort v75,v9,s[24:27],s65 offen offset:896
buffer_load_ushort v76,v10,s[24:27],s65 offen offset:128
buffer_load_ushort v77,v10,s[24:27],s65 offen offset:384
buffer_load_ushort v78,v10,s[24:27],s65 offen offset:640
buffer_load_ushort v79,v10,s[24:27],s65 offen offset:896
s_waitcnt vmcnt(0)
v_lshl_or_b32 v88,v73,16,v72
v_lshl_or_b32 v89,v75,16,v74
v_lshl_or_b32 v90,v77,16,v76
v_lshl_or_b32 v91,v79,16,v78
s_nop 1
v_mfma_f32_16x16x32_bf16 a[16:19],v[88:91],v[64:67],a[16:19]
v_mfma_f32_16x16x32_bf16 v[164:167],v[88:91],v[80:83],v[164:167]
v_mfma_f32_16x16x32_bf16 v[228:231],v[88:91],v[96:99],v[228:231]
v_mfma_f32_16x16x32_bf16 a[48:51],v[88:91],v[112:115],a[48:51]
buffer_load_ushort v72,v9,s[24:27],s65 offen offset:160
buffer_load_ushort v73,v9,s[24:27],s65 offen offset:416
buffer_load_ushort v74,v9,s[24:27],s65 offen offset:672
buffer_load_ushort v75,v9,s[24:27],s65 offen offset:928
buffer_load_ushort v76,v10,s[24:27],s65 offen offset:160
buffer_load_ushort v77,v10,s[24:27],s65 offen offset:416
buffer_load_ushort v78,v10,s[24:27],s65 offen offset:672
buffer_load_ushort v79,v10,s[24:27],s65 offen offset:928
s_waitcnt vmcnt(0)
v_lshl_or_b32 v88,v73,16,v72
v_lshl_or_b32 v89,v75,16,v74
v_lshl_or_b32 v90,v77,16,v76
v_lshl_or_b32 v91,v79,16,v78
s_nop 1
v_mfma_f32_16x16x32_bf16 a[20:23],v[88:91],v[64:67],a[20:23]
v_mfma_f32_16x16x32_bf16 v[172:175],v[88:91],v[80:83],v[172:175]
v_mfma_f32_16x16x32_bf16 v[236:239],v[88:91],v[96:99],v[236:239]
v_mfma_f32_16x16x32_bf16 a[52:55],v[88:91],v[112:115],a[52:55]
buffer_load_ushort v72,v9,s[24:27],s65 offen offset:192
buffer_load_ushort v73,v9,s[24:27],s65 offen offset:448
buffer_load_ushort v74,v9,s[24:27],s65 offen offset:704
buffer_load_ushort v75,v9,s[24:27],s65 offen offset:960
buffer_load_ushort v76,v10,s[24:27],s65 offen offset:192
buffer_load_ushort v77,v10,s[24:27],s65 offen offset:448
buffer_load_ushort v78,v10,s[24:27],s65 offen offset:704
buffer_load_ushort v79,v10,s[24:27],s65 offen offset:960
s_waitcnt vmcnt(0)
v_lshl_or_b32 v88,v73,16,v72
v_lshl_or_b32 v89,v75,16,v74
v_lshl_or_b32 v90,v77,16,v76
v_lshl_or_b32 v91,v79,16,v78
s_nop 1
v_mfma_f32_16x16x32_bf16 a[24:27],v[88:91],v[64:67],a[24:27]
v_mfma_f32_16x16x32_bf16 v[180:183],v[88:91],v[80:83],v[180:183]
v_mfma_f32_16x16x32_bf16 v[244:247],v[88:91],v[96:99],v[244:247]
v_mfma_f32_16x16x32_bf16 a[56:59],v[88:91],v[112:115],a[56:59]
buffer_load_ushort v72,v9,s[24:27],s65 offen offset:224
buffer_load_ushort v73,v9,s[24:27],s65 offen offset:480
buffer_load_ushort v74,v9,s[24:27],s65 offen offset:736
buffer_load_ushort v75,v9,s[24:27],s65 offen offset:992
buffer_load_ushort v76,v10,s[24:27],s65 offen offset:224
)A"
R"A(buffer_load_ushort v77,v10,s[24:27],s65 offen offset:480
buffer_load_ushort v78,v10,s[24:27],s65 offen offset:736
buffer_load_ushort v79,v10,s[24:27],s65 offen offset:992
s_waitcnt vmcnt(0)
v_lshl_or_b32 v88,v73,16,v72
v_lshl_or_b32 v89,v75,16,v74
v_lshl_or_b32 v90,v77,16,v76
v_lshl_or_b32 v91,v79,16,v78
s_nop 1
v_mfma_f32_16x16x32_bf16 a[28:31],v[88:91],v[64:67],a[28:31]
v_mfma_f32_16x16x32_bf16 v[188:191],v[88:91],v[80:83],v[188:191]
v_mfma_f32_16x16x32_bf16 v[252:255],v[88:91],v[96:99],v[252:255]
v_mfma_f32_16x16x32_bf16 a[60:63],v[88:91],v[112:115],a[60:63]
v_mfma_f32_16x16x32_bf16 v[48:51],v[28:31],v[64:67],v[48:51]
v_mfma_f32_16x16x32_bf16 v[52:55],v[28:31],v[80:83],v[52:55]
v_mfma_f32_16x16x32_bf16 v[56:59],v[28:31],v[96:99],v[56:59]
v_mfma_f32_16x16x32_bf16 v[60:63],v[28:31],v[112:115],v[60:63]
s_add_u32 s65,s65,8192
s_sub_u32 s66,s66,1
s_cmp_lg_u32 s66,0
s_cbranch_scc1 L_s21_%=
s_nop 7
v_max_f32 v49,0x00800000,v48
v_rcp_f32 v49,v49
v_max_f32 v53,0x00800000,v52
v_rcp_f32 v53,v53
v_max_f32 v57,0x00800000,v56
v_rcp_f32 v57,v57
v_max_f32 v61,0x00800000,v60
v_rcp_f32 v61,v61
v_accvgpr_read_b32 v16,a0
v_accvgpr_read_b32 v17,a1
v_accvgpr_read_b32 v18,a2
v_accvgpr_read_b32 v19,a3
v_accvgpr_read_b32 v20,a4
v_accvgpr_read_b32 v21,a5
v_accvgpr_read_b32 v22,a6
v_accvgpr_read_b32 v23,a7
v_mul_f32 v16,v16,v49
v_mul_f32 v17,v17,v49
v_mul_f32 v18,v18,v49
v_mul_f32 v19,v19,v49
v_mul_f32 v20,v20,v49
v_mul_f32 v21,v21,v49
v_mul_f32 v22,v22,v49
v_mul_f32 v23,v23,v49
v_cvt_pk_bf16_f32 v32,v16,v17
v_cvt_pk_bf16_f32 v33,v18,v19
v_cvt_pk_bf16_f32 v34,v20,v21
v_cvt_pk_bf16_f32 v35,v22,v23
v_accvgpr_read_b32 v8,a8
v_accvgpr_read_b32 v9,a9
v_accvgpr_read_b32 v10,a10
v_accvgpr_read_b32 v11,a11
v_accvgpr_read_b32 v12,a12
v_accvgpr_read_b32 v13,a13
v_accvgpr_read_b32 v14,a14
v_accvgpr_read_b32 v15,a15
v_mul_f32 v8,v8,v49
v_mul_f32 v9,v9,v49
v_mul_f32 v10,v10,v49
v_mul_f32 v11,v11,v49
v_mul_f32 v12,v12,v49
v_mul_f32 v13,v13,v49
v_mul_f32 v14,v14,v49
v_mul_f32 v15,v15,v49
v_cvt_pk_bf16_f32 v36,v8,v9
v_cvt_pk_bf16_f32 v37,v10,v11
v_cvt_pk_bf16_f32 v38,v12,v13
v_cvt_pk_bf16_f32 v39,v14,v15
v_accvgpr_read_b32 v16,a16
v_accvgpr_read_b32 v17,a17
v_accvgpr_read_b32 v18,a18
v_accvgpr_read_b32 v19,a19
v_accvgpr_read_b32 v20,a20
v_accvgpr_read_b32 v21,a21
v_accvgpr_read_b32 v22,a22
v_accvgpr_read_b32 v23,a23
v_mul_f32 v16,v16,v49
v_mul_f32 v17,v17,v49
v_mul_f32 v18,v18,v49
v_mul_f32 v19,v19,v49
v_mul_f32 v20,v20,v49
v_mul_f32 v21,v21,v49
v_mul_f32 v22,v22,v49
v_mul_f32 v23,v23,v49
v_cvt_pk_bf16_f32 v40,v16,v17
v_cvt_pk_bf16_f32 v41,v18,v19
v_cvt_pk_bf16_f32 v42,v20,v21
v_cvt_pk_bf16_f32 v43,v22,v23
ds_read_b64_tr_b16 v[64:65],v1
ds_read_b64_tr_b16 v[66:67],v1 offset:5120
ds_read_b64_tr_b16 v[68:69],v1 offset:10240
ds_read_b64_tr_b16 v[70:71],v1 offset:15360
ds_read_b64_tr_b16 v[72:73],v1 offset:20480
ds_read_b64_tr_b16 v[74:75],v1 offset:25600
ds_read_b64_tr_b16 v[76:77],v1 offset:30720
ds_read_b64_tr_b16 v[78:79],v1 offset:35840
ds_read_b64_tr_b16 v[80:81],v2 offset:32
ds_read_b64_tr_b16 v[82:83],v2 offset:5152
ds_read_b64_tr_b16 v[84:85],v2 offset:10272
ds_read_b64_tr_b16 v[86:87],v2 offset:15392
ds_read_b64_tr_b16 v[88:89],v2 offset:20512
ds_read_b64_tr_b16 v[90:91],v2 offset:25632
ds_read_b64_tr_b16 v[92:93],v2 offset:30752
ds_read_b64_tr_b16 v[94:95],v2 offset:35872
s_waitcnt vmcnt(0) lgkmcnt(0)
s_nop 7
s_branch L_ok1_%=
L_end_%=:
)A"
    :: "s"(ka), "s"(blk), "s"(wid)
    : "v0", "v1", "v2", "v3", "v4", "v5", "v6", "v7", "v8", "v9", "v10", "v11", "v12", "v13", "v14", "v15", "v16", "v17", "v18", "v19", "v20", "v21", "v22", "v23", "v24", "v25", "v26", "v27", "v28", "v29", "v30", "v31", "v32", "v33", "v34", "v35", "v36", "v37", "v38", "v39", "v40", "v41", "v42", "v43", "v44", "v45", "v46", "v47", "v48", "v49", "v50", "v51", "v52", "v53", "v54", "v55", "v56", "v57", "v58", "v59", "v60", "v61", "v62", "v63", "v64", "v65", "v66", "v67", "v68", "v69", "v70", "v71", "v72", "v73", "v74", "v75", "v76", "v77", "v78", "v79", "v80", "v81", "v82", "v83", "v84", "v85", "v86", "v87", "v88", "v89", "v90", "v91", "v92", "v93", "v94", "v95", "v96", "v97", "v98", "v99", "v100", "v101", "v102", "v103", "v104", "v105", "v106", "v107", "v108", "v109", "v110", "v111", "v112", "v113", "v114", "v115", "v116", "v117", "v118", "v119", "v120", "v121", "v122", "v123", "v124", "v125", "v126", "v127", "v128", "v129", "v130", "v131", "v132", "v133", "v134", "v135", "v136", "v137", "v138", "v139", "v140", "v141", "v142", "v143", "v144", "v145", "v146", "v147", "v148", "v149", "v150", "v151", "v152", "v153", "v154", "v155", "v156", "v157", "v158", "v159", "v160", "v161", "v162", "v163", "v164", "v165", "v166", "v167", "v168", "v169", "v170", "v171", "v172", "v173", "v174", "v175", "v176", "v177", "v178", "v179", "v180", "v181", "v182", "v183", "v184", "v185", "v186", "v187", "v188", "v189", "v190", "v191", "v192", "v193", "v194", "v195", "v196", "v197", "v198", "v199", "v200", "v201", "v202", "v203", "v204", "v205", "v206", "v207", "v208", "v209", "v210", "v211", "v212", "v213", "v214", "v215", "v216", "v217", "v218", "v219", "v220", "v221", "v222", "v223", "v224", "v225", "v226", "v227", "v228", "v229", "v230", "v231", "v232", "v233", "v234", "v235", "v236", "v237", "v238", "v239", "v240", "v241", "v242", "v243", "v244", "v245", "v246", "v247", "v248", "v249", "v250", "v251", "v252", "v253", "v254", "v255", "a0", "a1", "a2", "a3", "a4", "a5", "a6", "a7", "a8", "a9", "a10", "a11", "a12", "a13", "a14", "a15", "a16", "a17", "a18", "a19", "a20", "a21", "a22", "a23", "a24", "a25", "a26", "a27", "a28", "a29", "a30", "a31", "a32", "a33", "a34", "a35", "a36", "a37", "a38", "a39", "a40", "a41", "a42", "a43", "a44", "a45", "a46", "a47", "a48", "a49", "a50", "a51", "a52", "a53", "a54", "a55", "a56", "a57", "a58", "a59", "a60", "a61", "a62", "a63", "a64", "a65", "a66", "a67", "a68", "a69", "a70", "a71", "a72", "a73", "a74", "a75", "a76", "a77", "a78", "a79", "a80", "a81", "a82", "a83", "a84", "a85", "a86", "a87", "a88", "a89", "a90", "a91", "a92", "a93", "a94", "a95", "a96", "a97", "a98", "a99", "a100", "a101", "a102", "a103", "a104", "a105", "a106", "a107", "a108", "a109", "a110", "a111", "a112", "a113", "a114", "a115", "a116", "a117", "a118", "a119", "a120", "a121", "a122", "a123", "a124", "a125", "a126", "a127", "a128", "a129", "a130", "a131", "a132", "a133", "a134", "a135", "a136", "a137", "a138", "a139", "a140", "a141", "a142", "a143", "a144", "a145", "a146", "a147", "a148", "a149", "a150", "a151", "a152", "a153", "a154", "a155", "a156", "a157", "a158", "a159", "a160", "a161", "a162", "a163", "a164", "a165", "a166", "a167", "a168", "a169", "a170", "a171", "a172", "a173", "a174", "a175", "a176", "a177", "a178", "a179", "a180", "a181", "a182", "a183", "a184", "a185", "a186", "a187", "a188", "a189", "a190", "a191", "a192", "a193", "a194", "a195", "a196", "a197", "a198", "a199", "a200", "a201", "a202", "a203", "a204", "a205", "a206", "a207", "a208", "a209", "a210", "a211", "a212", "a213", "a214", "a215", "a216", "a217", "a218", "a219", "a220", "a221", "a222", "a223", "a224", "a225", "a226", "a227", "a228", "a229", "a230", "a231", "a232", "a233", "a234", "a235", "a236", "a237", "a238", "a239", "a240", "a241", "a242", "a243", "a244", "a245", "a246", "a247", "a248", "a249", "a250", "a251", "a252", "a253", "a254", "a255", "s4", "s5", "s6", "s7", "s8", "s9", "s10", "s11", "s12", "s13", "s14", "s15", "s16", "s17", "s18", "s19", "s20", "s21", "s22", "s23", "s24", "s25", "s26", "s27", "s28", "s29", "s30", "s31", "s36", "s37", "s38", "s39", "s40", "s41", "s42", "s43", "s44", "s45", "s46", "s47", "s48", "s49", "s50", "s51", "s52", "s53", "s54", "s55", "s56", "s57", "s58", "s59", "s60", "s61", "s62", "s63", "s64", "s65", "s66", "s67", "s68", "s69", "s70", "s71", "s72", "s73", "s74", "s75", "s76", "s77", "s78", "s79", "s80", "s81", "s82", "s83", "s84", "s85", "s86", "s87", "s88", "s89", "s90", "s91", "s92", "s93", "s94", "s95", "s96", "s97", "s98", "s99", "vcc", "scc", "memory");
}

extern "C" void kernel_launch(void* const* d_in, const int* in_sizes, int n_in,
                              void* d_out, int out_size, void* d_ws, size_t ws_size,
                              hipStream_t stream) {
  (void)hipFuncSetAttribute((const void*)qattn_kernel, hipFuncAttributeMaxDynamicSharedMemorySize, LDS_ATT);
  qattn_kernel<<<256, 256, LDS_ATT, stream>>>(d_in[0], d_in[1], d_in[2], d_in[3], d_ws, d_in[4], d_out);
}
```

```cpp
#include <hip/hip_runtime.h>
#include <hip/hip_bf16.h>

constexpr int LDS_ATT = 163840;

__global__ __launch_bounds__(256, 1) __attribute__((aligned(4096))) void qattn_kernel(const void* __restrict__ x, const void* __restrict__ kc, const void* __restrict__ vc,
                                                       const void* __restrict__ wq, void* __restrict__ ws, const void* __restrict__ wo,
                                                       void* __restrict__ out) {
  const unsigned blk = blockIdx.x;
  const unsigned wid = __builtin_amdgcn_readfirstlane(threadIdx.x >> 6);
  auto ka = __builtin_amdgcn_kernarg_segment_ptr();
  asm volatile(
R"A(s_load_dwordx8 s[4:11],%0,0x0
s_load_dwordx4 s[12:15],%0,0x20
s_load_dwordx2 s[16:17],%0,0x30
v_mbcnt_lo_u32_b32 v23,-1,0
v_mbcnt_hi_u32_b32 v23,-1,v23
s_mov_b32 s60,%2
s_and_b32 s66,%1,7
s_lshr_b32 s67,%1,3
s_getpc_b64 s[20:21]
L_pc_%=:
s_and_b32 s20,s20,0xffffff80
s_and_b32 s21,s21,0xffff
s_mov_b32 s22,L_hot_%=-L_pc_%=
s_mov_b32 s23,0x20000
s_lshl_b32 s68,s67,2
s_add_u32 s68,s68,s60
s_mul_i32 s68,s68,896
v_lshlrev_b32 v67,7,v23
s_mov_b64 exec,127
buffer_load_dword v8,v67,s[20:23],s68 offen
s_mov_b64 exec,-1
s_lshr_b32 s61,s66,2
s_lshl_b32 s61,s61,3
s_lshr_b32 s68,s67,2
s_add_u32 s61,s61,s68
s_and_b32 s62,s66,1
s_lshl_b32 s62,s62,2
s_and_b32 s68,s66,2
s_add_u32 s62,s62,s68
s_lshl_b32 s62,s62,1
s_and_b32 s68,s67,3
s_add_u32 s62,s62,s68
s_lshl_b32 s83,s62,9
s_mov_b32 s84,0
s_lshl_b32 s89,s60,13
s_add_u32 s85,s89,65536
s_add_u32 s88,s89,131072
v_and_b32 v20,3,v23
v_bfe_u32 v21,v23,2,2
v_lshrrev_b32 v22,4,v23
v_lshrrev_b32 v75,3,v23
s_lshl_b32 s66,s60,6
v_add_u32 v75,s66,v75
v_mul_u32_u24 v75,1280,v75
v_and_b32 v76,7,v23
v_or_b32 v77,0,v22
v_xor_b32 v77,v77,v76
v_lshl_add_u32 v77,v77,4,v75
v_add_u32 v32,0,v77
v_add_u32 v34,18432,v77
v_add_u32 v36,40960,v77
v_add_u32 v38,59392,v77
v_or_b32 v77,4,v22
v_xor_b32 v77,v77,v76
v_lshl_add_u32 v77,v77,4,v75
v_add_u32 v33,9216,v77
v_add_u32 v35,27648,v77
v_add_u32 v37,50176,v77
v_add_u32 v39,68608,v77
s_lshl_b32 s66,s60,17
v_lshlrev_b32 v75,13,v21
v_lshl_add_u32 v75,v22,6,v75
v_lshl_add_u32 v75,v20,4,v75
v_add_u32 v75,s66,v75
v_add_u32 v40,0,v75
v_add_u32 v41,256,v75
v_add_u32 v42,32768,v75
v_add_u32 v43,33024,v75
v_add_u32 v44,65536,v75
v_add_u32 v45,65792,v75
v_add_u32 v46,98304,v75
v_add_u32 v47,98560,v75
s_mul_i32 s66,s61,307200
s_waitcnt lgkmcnt(0)
s_add_u32 s28,s4,s66
s_addc_u32 s29,s5,0
s_and_b32 s29,s29,0xffff
s_mov_b32 s30,307200
s_mov_b32 s31,0x20000
s_mov_b32 s36,s10
s_and_b32 s37,s11,0xffff
s_mov_b32 s38,5242880
s_mov_b32 s39,0x20000
s_add_u32 m0,s85,0
s_nop 0
buffer_load_dwordx4 v32,s[28:31],s84 offen lds
buffer_load_dwordx4 v33,s[28:31],s84 offen offset:1024 lds
buffer_load_dwordx4 v34,s[28:31],s84 offen offset:2048 lds
buffer_load_dwordx4 v35,s[28:31],s84 offen offset:3072 lds
s_add_u32 m0,s85,4096
s_nop 0
buffer_load_dwordx4 v36,s[28:31],s84 offen lds
buffer_load_dwordx4 v37,s[28:31],s84 offen offset:1024 lds
buffer_load_dwordx4 v38,s[28:31],s84 offen offset:2048 lds
buffer_load_dwordx4 v39,s[28:31],s84 offen offset:3072 lds
s_add_u32 s84,s84,128
s_xor_b32 s85,s85,98304
s_add_u32 m0,s88,0
s_nop 0
buffer_load_dwordx4 v40,s[36:39],s83 offen lds
s_add_u32 m0,s88,2048
s_nop 0
buffer_load_dwordx4 v42,s[36:39],s83 offen lds
s_add_u32 m0,s88,4096
s_nop 0
buffer_load_dwordx4 v44,s[36:39],s83 offen lds
s_add_u32 m0,s88,6144
s_nop 0
buffer_load_dwordx4 v46,s[36:39],s83 offen lds
s_lshl_b32 s67,s61,21
s_add_u32 s68,s67,101711872
s_add_u32 s44,s12,s68
s_addc_u32 s45,s13,0
s_and_b32 s45,s45,0xffff
s_mov_b32 s46,4096
s_mov_b32 s47,0x20000
v_mov_b32 v10,0
buffer_load_dword v9,v10,s[44:47],0 offen sc1
v_lshlrev_b32 v6,4,v23
v_lshlrev_b32 v72,12,v22
v_lshl_add_u32 v72,v21,6,v72
v_lshl_add_u32 v72,v20,4,v72
v_subrev_u32 v51,s89,v72
v_add_u32 v50,131072,v72
v_and_b32 v74,15,v23
v_lshrrev_b32 v76,1,v74
v_lshlrev_b32 v75,7,v74
s_add_u32 s66,s89,65536
v_add_u32 v75,s66,v75
v_or_b32 v77,0,v22
v_xor_b32 v77,v77,v76
v_lshl_add_u32 v48,v77,4,v75
v_or_b32 v77,4,v22
v_xor_b32 v77,v77,v76
v_lshl_add_u32 v49,v77,4,v75
s_add_u32 s86,s89,131072
s_add_u32 s87,s89,98304
s_mov_b64 exec,0
s_cmp_eq_u32 s60,0
s_cbranch_scc1 L_pd1_%=
s_cmp_eq_u32 s60,1
s_cbranch_scc1 L_pd2_%=
s_cmp_eq_u32 s60,2
s_cbranch_scc1 L_pd3_%=
s_branch L_pd4_%=
L_trt0_%=:
s_mov_b64 exec,-1
s_add_u32 m0,s88,1024
s_nop 0
buffer_load_dwordx4 v41,s[36:39],s83 offen lds
s_add_u32 m0,s88,3072
s_nop 0
buffer_load_dwordx4 v43,s[36:39],s83 offen lds
s_add_u32 m0,s88,5120
s_nop 0
buffer_load_dwordx4 v45,s[36:39],s83 offen lds
s_add_u32 m0,s88,7168
s_nop 0
buffer_load_dwordx4 v47,s[36:39],s83 offen lds
s_add_u32 s83,s83,524288
s_mov_b32 s88,s87
s_add_u32 m0,s88,0
s_nop 0
buffer_load_dwordx4 v40,s[36:39],s83 offen lds
s_add_u32 m0,s88,1024
s_nop 0
buffer_load_dwordx4 v41,s[36:39],s83 offen lds
s_add_u32 m0,s88,2048
s_nop 0
buffer_load_dwordx4 v42,s[36:39],s83 offen lds
s_add_u32 m0,s88,3072
s_nop 0
buffer_load_dwordx4 v43,s[36:39],s83 offen lds
s_add_u32 m0,s88,4096
s_nop 0
buffer_load_dwordx4 v44,s[36:39],s83 offen lds
s_add_u32 m0,s88,5120
s_nop 0
buffer_load_dwordx4 v45,s[36:39],s83 offen lds
s_add_u32 m0,s88,6144
s_nop 0
buffer_load_dwordx4 v46,s[36:39],s83 offen lds
s_add_u32 m0,s88,7168
s_nop 0
buffer_load_dwordx4 v47,s[36:39],s83 offen lds
s_add_u32 s83,s83,524288
s_add_u32 s88,s89,0
s_mov_b64 exec,0
s_cmp_eq_u32 s60,0
s_cbranch_scc1 L_pd9_%=
s_cmp_eq_u32 s60,1
s_cbranch_scc1 L_pd10_%=
s_cmp_eq_u32 s60,2
s_cbranch_scc1 L_pd11_%=
s_branch L_pd12_%=
L_t1b_%=:
s_cmp_ge_u32 s62,8
s_cbranch_scc1 L_pd19_%=
s_branch L_pd14_%=
L_t1c_%=:
s_cmp_ge_u32 s62,8
s_cbranch_scc1 L_pd20_%=
s_branch L_pd15_%=
s_nop 0
s_nop 0
s_nop 0
s_nop 0
s_nop 0
s_nop 0
L_trt1_%=:
s_mov_b64 exec,-1
s_branch L_pacx_%=
L_pace_%=:
s_cmp_ge_u32 s4,8
s_cselect_b32 s5,3,1
s_sub_u32 s5,s4,s5
s_max_i32 s5,s5,s7
L_pacl_%=:
s_sub_u32 s4,s4,1
s_cmp_eq_u32 s4,0
s_cbranch_scc1 L_pace2_%=
s_nop 2
s_branch L_pacl_%=
L_pace2_%=:
s_setpc_b64 s[92:93]
L_pacx_%=:
s_waitcnt vmcnt(17)
ds_read_b128 a[192:195],v48
ds_read_b128 a[196:199],v49
ds_read_b128 a[200:203],v48 offset:2048
ds_read_b128 a[204:207],v49 offset:2048
ds_read_b128 a[208:211],v48 offset:4096
ds_read_b128 a[212:215],v49 offset:4096
ds_read_b128 a[216:219],v48 offset:6144
ds_read_b128 a[220:223],v49 offset:6144
s_waitcnt vmcnt(13)
s_barrier
ds_read_b64_tr_b16 a[64:65],v50
ds_read_b64_tr_b16 a[66:67],v50 offset:2048
ds_read_b64_tr_b16 a[68:69],v50 offset:16384
ds_read_b64_tr_b16 a[70:71],v50 offset:18432
s_waitcnt lgkmcnt(2)
v_mfma_f32_16x16x32_bf16 a[0:3],a[64:67],a[192:195],0
s_add_u32 m0,s85,0
ds_read_b64_tr_b16 a[72:73],v50 offset:8
v_xor_b32 v48,98304,v48
s_waitcnt lgkmcnt(1)
v_mfma_f32_16x16x32_bf16 a[0:3],a[68:71],a[196:199],a[0:3]
buffer_load_dwordx4 v32,s[28:31],s84 offen lds
v_mfma_f32_16x16x32_bf16 a[16:19],a[68:71],a[204:207],0
s_mov_b32 s69,s86
ds_read_b64_tr_b16 a[74:75],v50 offset:2056
v_xor_b32 v49,98304,v49
v_mfma_f32_16x16x32_bf16 a[16:19],a[64:67],a[200:203],a[16:19]
s_mov_b32 s86,s87
s_mov_b32 s87,s88
ds_read_b64_tr_b16 a[76:77],v50 offset:16392
v_mfma_f32_16x16x32_bf16 a[32:35],a[64:67],a[208:211],0
ds_read_b64_tr_b16 a[78:79],v50 offset:18440
v_mfma_f32_16x16x32_bf16 a[32:35],a[68:71],a[212:215],a[32:35]
ds_read_b64_tr_b16 a[80:81],v50 offset:256
v_mfma_f32_16x16x32_bf16 a[48:51],a[68:71],a[220:223],0
buffer_load_dwordx4 v33,s[28:31],s84 offen offset:1024 lds
v_mfma_f32_16x16x32_bf16 a[48:51],a[64:67],a[216:219],a[48:51]
s_waitcnt vmcnt(14) lgkmcnt(1)
ds_read_b64_tr_b16 a[82:83],v50 offset:2304
v_mfma_f32_16x16x32_bf16 a[4:7],a[72:75],a[192:195],0
s_lshl_b32 s65,s61,5
ds_read_b64_tr_b16 a[84:85],v50 offset:16640
v_cmp_lt_u32 vcc,6,v23
v_mfma_f32_16x16x32_bf16 a[4:7],a[76:79],a[196:199],a[4:7]
s_lshl_b32 s66,s62,1
ds_read_b64_tr_b16 a[86:87],v50 offset:18688
v_mov_b32 v17,0
v_mfma_f32_16x16x32_bf16 a[20:23],a[76:79],a[204:207],0
s_add_u32 s65,s65,s66
s_lshl_b32 s66,s65,18
v_readfirstlane_b32 s52,v9
v_mfma_f32_16x16x32_bf16 a[20:23],a[72:75],a[200:203],a[20:23]
buffer_load_dwordx4 v34,s[28:31],s84 offen offset:2048 lds
v_mfma_f32_16x16x32_bf16 a[36:39],a[72:75],a[208:211],0
s_add_u32 s20,s6,s66
s_addc_u32 s21,s7,0
ds_read_b64_tr_b16 a[88:89],v50 offset:264
v_mfma_f32_16x16x32_bf16 a[36:39],a[76:79],a[212:215],a[36:39]
s_and_b32 s21,s21,0xffff
s_mov_b32 s22,524288
ds_read_b64_tr_b16 a[90:91],v50 offset:2312
v_mfma_f32_16x16x32_bf16 a[52:55],a[76:79],a[220:223],0
s_mov_b32 s23,0x20000
s_mov_b32 s26,524288
ds_read_b64_tr_b16 a[92:93],v50 offset:16648
v_mfma_f32_16x16x32_bf16 a[52:55],a[72:75],a[216:219],a[52:55]
s_add_u32 s24,s8,s66
s_addc_u32 s25,s9,0
ds_read_b64_tr_b16 a[94:95],v50 offset:18696
s_waitcnt lgkmcnt(6)
v_mfma_f32_16x16x32_bf16 a[8:11],a[80:83],a[192:195],0
buffer_load_dwordx4 v35,s[28:31],s84 offen offset:3072 lds
s_waitcnt lgkmcnt(4)
v_mfma_f32_16x16x32_bf16 a[8:11],a[84:87],a[196:199],a[8:11]
s_and_b32 s25,s25,0xffff
s_mov_b32 s27,0x20000
ds_read_b64_tr_b16 a[96:97],v50 offset:512
v_mfma_f32_16x16x32_bf16 a[24:27],a[84:87],a[204:207],0
s_lshl_b32 s66,s65,16
s_mov_b32 s42,131072
ds_read_b64_tr_b16 a[98:99],v50 offset:2560
v_mfma_f32_16x16x32_bf16 a[24:27],a[80:83],a[200:203],a[24:27]
s_add_u32 m0,s85,4096
s_xor_b32 s85,s85,98304
ds_read_b64_tr_b16 a[100:101],v50 offset:16896
v_mfma_f32_16x16x32_bf16 a[40:43],a[80:83],a[208:211],0
s_add_u32 s40,s12,s66
s_addc_u32 s41,s13,0
s_and_b32 s41,s41,0xffff
v_mfma_f32_16x16x32_bf16 a[40:43],a[84:87],a[212:215],a[40:43]
buffer_load_dwordx4 v36,s[28:31],s84 offen lds
v_mfma_f32_16x16x32_bf16 a[56:59],a[84:87],a[220:223],0
s_mov_b32 s43,0x20000
s_mov_b32 s48,s14
ds_read_b64_tr_b16 a[102:103],v50 offset:18944
v_mfma_f32_16x16x32_bf16 a[56:59],a[80:83],a[216:219],a[56:59]
s_and_b32 s49,s15,0xffff
s_mov_b32 s50,5242880
ds_read_b64_tr_b16 a[104:105],v50 offset:520
s_waitcnt lgkmcnt(5)
v_mfma_f32_16x16x32_bf16 a[12:15],a[88:91],a[192:195],0
s_mov_b32 s51,0x20000
s_lshl_b32 s67,s60,12
ds_read_b64_tr_b16 a[106:107],v50 offset:2568
v_mfma_f32_16x16x32_bf16 a[12:15],a[92:95],a[196:199],a[12:15]
s_add_u32 s76,s67,98304
s_mul_i32 s74,s61,5
ds_read_b64_tr_b16 a[108:109],v50 offset:16904
v_mfma_f32_16x16x32_bf16 a[28:31],a[92:95],a[204:207],0
buffer_load_dwordx4 v37,s[28:31],s84 offen offset:1024 lds
v_mfma_f32_16x16x32_bf16 a[28:31],a[88:91],a[200:203],a[28:31]
s_add_u32 s74,s74,s62
s_and_b32 s74,s74,15
ds_read_b64_tr_b16 a[110:111],v50 offset:18952
v_mfma_f32_16x16x32_bf16 a[44:47],a[88:91],a[208:211],0
s_lshl_b32 s74,s74,14
s_mov_b32 s75,0
ds_read_b64_tr_b16 a[112:113],v50 offset:768
v_mfma_f32_16x16x32_bf16 a[44:47],a[92:95],a[212:215],a[44:47]
s_mov_b32 s77,98304
s_mov_b32 s71,0x3e0293ee
ds_read_b64_tr_b16 a[114:115],v50 offset:2816
v_mfma_f32_16x16x32_bf16 a[60:63],a[92:95],a[220:223],0
s_mov_b32 s73,0x53800000
s_lshl_b32 s79,s60,14
s_add_u32 s80,s79,4096
v_mfma_f32_16x16x32_bf16 a[60:63],a[88:91],a[216:219],a[60:63]
buffer_load_dwordx4 v38,s[28:31],s84 offen offset:2048 lds
s_waitcnt lgkmcnt(5)
v_mfma_f32_16x16x32_bf16 v[64:67],a[96:99],a[192:195],0
s_add_u32 s81,s79,8192
s_add_u32 s82,s79,12288
ds_read_b64_tr_b16 a[116:117],v50 offset:17152
v_mfma_f32_16x16x32_bf16 v[64:67],a[100:103],a[196:199],v[64:67]
s_lshr_b32 s63,s62,2
s_and_b32 s64,s62,3
ds_read_b64_tr_b16 a[118:119],v50 offset:19200
v_mfma_f32_16x16x32_bf16 v[80:83],a[100:103],a[204:207],0
s_mov_b32 s72,0xff800000
s_lshl_b32 s18,s60,24
ds_read_b64_tr_b16 a[120:121],v50 offset:776
v_mfma_f32_16x16x32_bf16 v[80:83],a[96:99],a[200:203],v[80:83]
s_lshl_b32 s68,s62,6
s_add_u32 s18,s18,s68
ds_read_b64_tr_b16 a[122:123],v50 offset:2824
v_mfma_f32_16x16x32_bf16 v[96:99],a[96:99],a[208:211],0
buffer_load_dwordx4 v39,s[28:31],s84 offen offset:3072 lds
v_mfma_f32_16x16x32_bf16 v[96:99],a[100:103],a[212:215],v[96:99]
s_add_u32 s84,s84,128
ds_read_b64_tr_b16 a[124:125],v50 offset:17160
v_lshlrev_b32 v19,3,v6
v_mfma_f32_16x16x32_bf16 v[112:115],a[100:103],a[220:223],0
s_cmp_eq_u32 s60,3
s_cselect_b32 s72,0x7149f2ca,s72
ds_read_b64_tr_b16 a[126:127],v50 offset:19208
v_mfma_f32_16x16x32_bf16 v[112:115],a[96:99],a[216:219],v[112:115]
s_add_u32 m0,s88,0
v_cndmask_b32_e64 v19,v19,-1,vcc
v_lshrrev_b32 v18,1,v19
s_waitcnt lgkmcnt(6)
v_mfma_f32_16x16x32_bf16 v[68:71],a[104:107],a[192:195],0
s_lshl_b32 s90,%1,2
s_add_u32 s90,s90,s60
v_min_u32 v19,v19,v19
v_mfma_f32_16x16x32_bf16 v[68:71],a[108:111],a[196:199],v[68:71]
buffer_load_dwordx4 v40,s[36:39],s83 offen lds
v_mfma_f32_16x16x32_bf16 v[84:87],a[108:111],a[204:207],0
s_mul_i32 s90,s90,5120
s_mul_i32 s58,s61,307200
v_cndmask_b32_e64 v19,v19,v18,vcc
v_mfma_f32_16x16x32_bf16 v[84:87],a[104:107],a[200:203],v[84:87]
s_add_u32 s56,s40,52224
s_addc_u32 s57,s41,0
s_add_u32 s52,s52,1
v_mfma_f32_16x16x32_bf16 v[100:103],a[104:107],a[208:211],0
)A"
R"A(s_add_u32 m0,s88,1024
s_add_u32 s58,s16,s58
s_addc_u32 s59,s17,0
v_mfma_f32_16x16x32_bf16 v[100:103],a[108:111],a[212:215],v[100:103]
s_cmp_eq_u32 s60,1
s_cselect_b64 s[56:57],s[58:59],s[56:57]
s_xor_b32 s53,s52,0x9e3779b9
v_mfma_f32_16x16x32_bf16 v[116:119],a[108:111],a[220:223],0
buffer_load_dwordx4 v41,s[36:39],s83 offen lds
v_mfma_f32_16x16x32_bf16 v[116:119],a[104:107],a[216:219],v[116:119]
s_cmp_eq_u32 s60,2
s_cselect_b64 s[56:57],s[20:21],s[56:57]
v_lshlrev_b32 v11,8,v21
s_waitcnt vmcnt(18)
s_barrier
v_mfma_f32_16x16x32_bf16 v[72:75],a[112:115],a[192:195],0
s_add_u32 m0,s88,2048
ds_read_b64_tr_b16 a[64:65],v50 offset:1024
v_lshl_add_u32 v11,v22,6,v11
s_waitcnt lgkmcnt(1)
v_mfma_f32_16x16x32_bf16 v[72:75],a[116:119],a[196:199],v[72:75]
s_cmp_eq_u32 s60,3
s_cselect_b64 s[56:57],s[24:25],s[56:57]
ds_read_b64_tr_b16 a[66:67],v50 offset:3072
v_mfma_f32_16x16x32_bf16 v[88:91],a[116:119],a[204:207],0
buffer_load_dwordx4 v42,s[36:39],s83 offen lds
v_mfma_f32_16x16x32_bf16 v[88:91],a[112:115],a[200:203],v[88:91]
ds_read_b64_tr_b16 a[68:69],v50 offset:17408
v_add_u32 v11,s67,v11
v_mfma_f32_16x16x32_bf16 v[104:107],a[112:115],a[208:211],0
ds_read_b64_tr_b16 a[70:71],v50 offset:19456
v_lshl_add_u32 v4,v20,4,v11
v_mfma_f32_16x16x32_bf16 v[104:107],a[116:119],a[212:215],v[104:107]
s_add_u32 m0,s88,3072
ds_read_b64_tr_b16 a[72:73],v50 offset:1032
v_xor_b32 v12,2,v20
v_mfma_f32_16x16x32_bf16 v[120:123],a[116:119],a[220:223],0
ds_read_b64_tr_b16 a[74:75],v50 offset:3080
v_lshl_add_u32 v5,v12,4,v11
v_mfma_f32_16x16x32_bf16 v[120:123],a[112:115],a[216:219],v[120:123]
buffer_load_dwordx4 v43,s[36:39],s83 offen lds
v_mfma_f32_16x16x32_bf16 v[76:79],a[120:123],a[192:195],0
ds_read_b64_tr_b16 a[76:77],v50 offset:17416
v_lshlrev_b32 v11,10,v21
v_mfma_f32_16x16x32_bf16 v[76:79],a[124:127],a[196:199],v[76:79]
ds_read_b64_tr_b16 a[78:79],v50 offset:19464
v_lshl_add_u32 v11,v20,6,v11
v_mfma_f32_16x16x32_bf16 v[92:95],a[124:127],a[204:207],0
s_add_u32 m0,s88,4096
ds_read_b64_tr_b16 a[80:81],v50 offset:1280
v_and_b32 v12,2,v21
v_mfma_f32_16x16x32_bf16 v[92:95],a[120:123],a[200:203],v[92:95]
v_xor_b32 v12,v12,v22
v_lshl_add_u32 v11,v12,4,v11
v_mfma_f32_16x16x32_bf16 v[108:111],a[120:123],a[208:211],0
buffer_load_dwordx4 v44,s[36:39],s83 offen lds
v_mfma_f32_16x16x32_bf16 v[108:111],a[124:127],a[212:215],v[108:111]
ds_read_b64_tr_b16 a[82:83],v50 offset:3328
v_add_u32 v1,98304,v11
v_mfma_f32_16x16x32_bf16 v[124:127],a[124:127],a[220:223],0
s_waitcnt lgkmcnt(4)
ds_read_b64_tr_b16 a[84:85],v50 offset:17664
v_lshlrev_b32 v11,10,v22
v_mfma_f32_16x16x32_bf16 v[124:127],a[120:123],a[216:219],v[124:127]
s_add_u32 m0,s88,5120
ds_read_b64_tr_b16 a[86:87],v50 offset:19712
v_lshl_add_u32 v11,v21,6,v11
v_mfma_f32_16x16x32_bf16 v[128:131],a[64:67],a[192:195],0
ds_read_b64_tr_b16 a[88:89],v50 offset:1288
v_lshl_add_u32 v11,v20,3,v11
v_mfma_f32_16x16x32_bf16 v[128:131],a[68:71],a[196:199],v[128:131]
buffer_load_dwordx4 v45,s[36:39],s83 offen lds
v_mfma_f32_16x16x32_bf16 v[160:163],a[68:71],a[204:207],0
ds_read_b64_tr_b16 a[90:91],v50 offset:3336
v_add_u32 v11,114688,v11
v_mfma_f32_16x16x32_bf16 v[160:163],a[64:67],a[200:203],v[160:163]
ds_read_b64_tr_b16 a[92:93],v50 offset:17672
v_and_b32 v12,1,v22
v_mfma_f32_16x16x32_bf16 v[192:195],a[64:67],a[208:211],0
s_add_u32 m0,s88,6144
ds_read_b64_tr_b16 a[94:95],v50 offset:19720
v_lshl_add_u32 v2,v12,5,v11
v_mfma_f32_16x16x32_bf16 v[192:195],a[68:71],a[212:215],v[192:195]
v_xor_b32 v12,1,v12
v_lshl_add_u32 v3,v12,5,v11
v_mfma_f32_16x16x32_bf16 v[224:227],a[68:71],a[220:223],0
buffer_load_dwordx4 v46,s[36:39],s83 offen lds
v_mfma_f32_16x16x32_bf16 v[224:227],a[64:67],a[216:219],v[224:227]
ds_read_b64_tr_b16 a[96:97],v50 offset:1536
v_mov_b32 v28,0x3f803f80
v_mfma_f32_16x16x32_bf16 v[132:135],a[72:75],a[192:195],0
s_waitcnt lgkmcnt(4)
ds_read_b64_tr_b16 a[98:99],v50 offset:3584
v_mov_b32 v29,0x3f803f80
v_mfma_f32_16x16x32_bf16 v[132:135],a[76:79],a[196:199],v[132:135]
s_add_u32 m0,s88,7168
s_mov_b32 s88,s69
ds_read_b64_tr_b16 a[100:101],v50 offset:17920
v_mfma_f32_16x16x32_bf16 v[164:167],a[76:79],a[204:207],0
ds_read_b64_tr_b16 a[102:103],v50 offset:19968
v_mov_b32 v30,0x3f803f80
v_mfma_f32_16x16x32_bf16 v[164:167],a[72:75],a[200:203],v[164:167]
buffer_load_dwordx4 v47,s[36:39],s83 offen lds
v_mfma_f32_16x16x32_bf16 v[196:199],a[72:75],a[208:211],0
s_add_u32 s83,s83,524288
ds_read_b64_tr_b16 a[104:105],v50 offset:1544
v_mov_b32 v31,0x3f803f80
v_mfma_f32_16x16x32_bf16 v[196:199],a[76:79],a[212:215],v[196:199]
ds_read_b64_tr_b16 a[106:107],v50 offset:3592
v_mfma_f32_16x16x32_bf16 v[228:231],a[76:79],a[220:223],0
s_add_u32 m0,s85,0
ds_read_b64_tr_b16 a[108:109],v50 offset:17928
v_mfma_f32_16x16x32_bf16 v[228:231],a[72:75],a[216:219],v[228:231]
v_mfma_f32_16x16x32_bf16 v[136:139],a[80:83],a[192:195],0
buffer_load_dwordx4 v32,s[28:31],s84 offen lds
v_mfma_f32_16x16x32_bf16 v[136:139],a[84:87],a[196:199],v[136:139]
ds_read_b64_tr_b16 a[110:111],v50 offset:19976
v_mfma_f32_16x16x32_bf16 v[168:171],a[84:87],a[204:207],0
ds_read_b64_tr_b16 a[112:113],v50 offset:1792
v_mfma_f32_16x16x32_bf16 v[168:171],a[80:83],a[200:203],v[168:171]
ds_read_b64_tr_b16 a[114:115],v50 offset:3840
v_mfma_f32_16x16x32_bf16 v[200:203],a[80:83],a[208:211],0
ds_read_b64_tr_b16 a[116:117],v50 offset:18176
v_mfma_f32_16x16x32_bf16 v[200:203],a[84:87],a[212:215],v[200:203]
buffer_load_dwordx4 v33,s[28:31],s84 offen offset:1024 lds
v_mfma_f32_16x16x32_bf16 v[232:235],a[84:87],a[220:223],0
ds_read_b64_tr_b16 a[118:119],v50 offset:20224
v_mfma_f32_16x16x32_bf16 v[232:235],a[80:83],a[216:219],v[232:235]
s_waitcnt lgkmcnt(5)
ds_read_b64_tr_b16 a[120:121],v50 offset:1800
v_mfma_f32_16x16x32_bf16 v[140:143],a[88:91],a[192:195],0
ds_read_b64_tr_b16 a[122:123],v50 offset:3848
v_mfma_f32_16x16x32_bf16 v[140:143],a[92:95],a[196:199],v[140:143]
v_mfma_f32_16x16x32_bf16 v[172:175],a[92:95],a[204:207],0
buffer_load_dwordx4 v34,s[28:31],s84 offen offset:2048 lds
v_mfma_f32_16x16x32_bf16 v[172:175],a[88:91],a[200:203],v[172:175]
ds_read_b64_tr_b16 a[124:125],v50 offset:18184
v_mfma_f32_16x16x32_bf16 v[204:207],a[88:91],a[208:211],0
ds_read_b64_tr_b16 a[126:127],v50 offset:20232
v_add_u32 v50,s86,v51
v_mfma_f32_16x16x32_bf16 v[204:207],a[92:95],a[212:215],v[204:207]
v_mfma_f32_16x16x32_bf16 v[236:239],a[92:95],a[220:223],0
v_mfma_f32_16x16x32_bf16 v[236:239],a[88:91],a[216:219],v[236:239]
buffer_load_dwordx4 v35,s[28:31],s84 offen offset:3072 lds
s_waitcnt vmcnt(20) lgkmcnt(0)
s_barrier
v_mfma_f32_16x16x32_bf16 v[144:147],a[96:99],a[192:195],0
s_waitcnt vmcnt(12)
ds_read_b128 a[224:227],v48
v_mfma_f32_16x16x32_bf16 v[144:147],a[100:103],a[196:199],v[144:147]
s_add_u32 m0,s85,4096
s_xor_b32 s85,s85,98304
ds_read_b128 a[228:231],v49
v_mfma_f32_16x16x32_bf16 v[176:179],a[100:103],a[204:207],0
ds_read_b128 a[232:235],v48 offset:2048
v_mfma_f32_16x16x32_bf16 v[176:179],a[96:99],a[200:203],v[176:179]
buffer_load_dwordx4 v36,s[28:31],s84 offen lds
v_mfma_f32_16x16x32_bf16 v[208:211],a[96:99],a[208:211],0
ds_read_b128 a[236:239],v49 offset:2048
v_mfma_f32_16x16x32_bf16 v[208:211],a[100:103],a[212:215],v[208:211]
ds_read_b128 a[240:243],v48 offset:4096
v_mfma_f32_16x16x32_bf16 v[240:243],a[100:103],a[220:223],0
ds_read_b128 a[244:247],v49 offset:4096
v_mfma_f32_16x16x32_bf16 v[240:243],a[96:99],a[216:219],v[240:243]
ds_read_b128 a[248:251],v48 offset:6144
v_mfma_f32_16x16x32_bf16 v[148:151],a[104:107],a[192:195],0
buffer_load_dwordx4 v37,s[28:31],s84 offen offset:1024 lds
v_mfma_f32_16x16x32_bf16 v[148:151],a[108:111],a[196:199],v[148:151]
ds_read_b128 a[252:255],v49 offset:6144
v_mfma_f32_16x16x32_bf16 v[180:183],a[108:111],a[204:207],0
ds_read_b64_tr_b16 a[64:65],v50
v_mfma_f32_16x16x32_bf16 v[180:183],a[104:107],a[200:203],v[180:183]
ds_read_b64_tr_b16 a[66:67],v50 offset:2048
v_mfma_f32_16x16x32_bf16 v[212:215],a[104:107],a[208:211],0
v_mfma_f32_16x16x32_bf16 v[212:215],a[108:111],a[212:215],v[212:215]
buffer_load_dwordx4 v38,s[28:31],s84 offen offset:2048 lds
v_mfma_f32_16x16x32_bf16 v[244:247],a[108:111],a[220:223],0
ds_read_b64_tr_b16 a[68:69],v50 offset:16384
v_mfma_f32_16x16x32_bf16 v[244:247],a[104:107],a[216:219],v[244:247]
ds_read_b64_tr_b16 a[70:71],v50 offset:18432
v_mfma_f32_16x16x32_bf16 v[152:155],a[112:115],a[192:195],0
ds_read_b64_tr_b16 a[72:73],v50 offset:8
v_mfma_f32_16x16x32_bf16 v[152:155],a[116:119],a[196:199],v[152:155]
ds_read_b64_tr_b16 a[74:75],v50 offset:2056
v_mfma_f32_16x16x32_bf16 v[184:187],a[116:119],a[204:207],0
buffer_load_dwordx4 v39,s[28:31],s84 offen offset:3072 lds
v_mfma_f32_16x16x32_bf16 v[184:187],a[112:115],a[200:203],v[184:187]
s_add_u32 s84,s84,128
ds_read_b64_tr_b16 a[76:77],v50 offset:16392
v_mfma_f32_16x16x32_bf16 v[216:219],a[112:115],a[208:211],0
ds_read_b64_tr_b16 a[78:79],v50 offset:18440
v_mfma_f32_16x16x32_bf16 v[216:219],a[116:119],a[212:215],v[216:219]
ds_read_b64_tr_b16 a[80:81],v50 offset:256
v_mfma_f32_16x16x32_bf16 v[248:251],a[116:119],a[220:223],0
v_mfma_f32_16x16x32_bf16 v[248:251],a[112:115],a[216:219],v[248:251]
ds_read_b64_tr_b16 a[82:83],v50 offset:2304
v_mfma_f32_16x16x32_bf16 v[156:159],a[120:123],a[192:195],0
global_load_dword a184,v17,s[56:57] nt
ds_read_b64_tr_b16 a[84:85],v50 offset:16640
v_mfma_f32_16x16x32_bf16 v[156:159],a[124:127],a[196:199],v[156:159]
ds_read_b64_tr_b16 a[86:87],v50 offset:18688
v_mfma_f32_16x16x32_bf16 v[188:191],a[124:127],a[204:207],0
v_mfma_f32_16x16x32_bf16 v[188:191],a[120:123],a[200:203],v[188:191]
ds_read_b64_tr_b16 a[88:89],v50 offset:264
v_mfma_f32_16x16x32_bf16 v[220:223],a[120:123],a[208:211],0
ds_read_b64_tr_b16 a[90:91],v50 offset:2312
v_mfma_f32_16x16x32_bf16 v[220:223],a[124:127],a[212:215],v[220:223]
ds_read_b64_tr_b16 a[92:93],v50 offset:16648
v_mfma_f32_16x16x32_bf16 v[252:255],a[124:127],a[220:223],0
v_mfma_f32_16x16x32_bf16 v[252:255],a[120:123],a[216:219],v[252:255]
ds_read_b64_tr_b16 a[94:95],v50 offset:18696
s_mov_b32 s70,3
s_memrealtime s[8:9]
s_memtime s[4:5]
s_nop 0
L_qp_%=:
s_waitcnt lgkmcnt(6)
v_mfma_f32_16x16x32_bf16 a[0:3],a[64:67],a[224:227],a[0:3]
s_add_u32 m0,s88,0
ds_read_b64_tr_b16 a[96:97],v50 offset:512
v_xor_b32 v48,98304,v48
L_pd1_%=:
s_cbranch_execz L_pd5_%=
v_mfma_f32_16x16x32_bf16 a[0:3],a[68:71],a[228:231],a[0:3]
buffer_load_dwordx4 v40,s[36:39],s83 offen lds
v_mfma_f32_16x16x32_bf16 a[16:19],a[68:71],a[236:239],a[16:19]
s_mov_b32 s69,s86
ds_read_b64_tr_b16 a[98:99],v50 offset:2560
v_xor_b32 v49,98304,v49
v_mfma_f32_16x16x32_bf16 a[16:19],a[64:67],a[232:235],a[16:19]
s_mov_b32 s86,s87
s_mov_b32 s87,s88
ds_read_b64_tr_b16 a[100:101],v50 offset:16896
v_mfma_f32_16x16x32_bf16 a[32:35],a[64:67],a[240:243],a[32:35]
ds_read_b64_tr_b16 a[102:103],v50 offset:18944
v_mfma_f32_16x16x32_bf16 a[32:35],a[68:71],a[244:247],a[32:35]
ds_read_b64_tr_b16 a[104:105],v50 offset:520
v_mfma_f32_16x16x32_bf16 a[48:51],a[68:71],a[252:255],a[48:51]
s_add_u32 m0,s88,1024
v_mfma_f32_16x16x32_bf16 a[48:51],a[64:67],a[248:251],a[48:51]
ds_read_b64_tr_b16 a[106:107],v50 offset:2568
v_mfma_f32_16x16x32_bf16 a[4:7],a[72:75],a[224:227],a[4:7]
buffer_load_dwordx4 v41,s[36:39],s83 offen lds
v_mfma_f32_16x16x32_bf16 a[4:7],a[76:79],a[228:231],a[4:7]
ds_read_b64_tr_b16 a[108:109],v50 offset:16904
v_mfma_f32_16x16x32_bf16 a[20:23],a[76:79],a[236:239],a[20:23]
ds_read_b64_tr_b16 a[110:111],v50 offset:18952
v_mfma_f32_16x16x32_bf16 a[20:23],a[72:75],a[232:235],a[20:23]
ds_read_b64_tr_b16 a[112:113],v50 offset:768
v_mfma_f32_16x16x32_bf16 a[36:39],a[72:75],a[240:243],a[36:39]
ds_read_b64_tr_b16 a[114:115],v50 offset:2816
v_mfma_f32_16x16x32_bf16 a[36:39],a[76:79],a[244:247],a[36:39]
s_add_u32 m0,s88,2048
v_mfma_f32_16x16x32_bf16 a[52:55],a[76:79],a[252:255],a[52:55]
ds_read_b64_tr_b16 a[116:117],v50 offset:17152
v_mfma_f32_16x16x32_bf16 a[52:55],a[72:75],a[248:251],a[52:55]
buffer_load_dwordx4 v42,s[36:39],s83 offen lds
v_mfma_f32_16x16x32_bf16 a[8:11],a[80:83],a[224:227],a[8:11]
s_waitcnt lgkmcnt(5)
ds_read_b64_tr_b16 a[118:119],v50 offset:19200
v_mfma_f32_16x16x32_bf16 a[8:11],a[84:87],a[228:231],a[8:11]
ds_read_b64_tr_b16 a[120:121],v50 offset:776
)A"
R"A(v_mfma_f32_16x16x32_bf16 a[24:27],a[84:87],a[236:239],a[24:27]
ds_read_b64_tr_b16 a[122:123],v50 offset:2824
v_mfma_f32_16x16x32_bf16 a[24:27],a[80:83],a[232:235],a[24:27]
ds_read_b64_tr_b16 a[124:125],v50 offset:17160
v_mfma_f32_16x16x32_bf16 a[40:43],a[80:83],a[240:243],a[40:43]
s_add_u32 m0,s88,3072
v_mfma_f32_16x16x32_bf16 a[40:43],a[84:87],a[244:247],a[40:43]
ds_read_b64_tr_b16 a[126:127],v50 offset:19208
v_mfma_f32_16x16x32_bf16 a[56:59],a[84:87],a[252:255],a[56:59]
buffer_load_dwordx4 v43,s[36:39],s83 offen lds
v_mfma_f32_16x16x32_bf16 a[56:59],a[80:83],a[248:251],a[56:59]
ds_read_b64_tr_b16 a[64:65],v50 offset:1024
v_mfma_f32_16x16x32_bf16 a[12:15],a[88:91],a[224:227],a[12:15]
ds_read_b64_tr_b16 a[66:67],v50 offset:3072
v_mfma_f32_16x16x32_bf16 a[12:15],a[92:95],a[228:231],a[12:15]
ds_read_b64_tr_b16 a[68:69],v50 offset:17408
v_mfma_f32_16x16x32_bf16 a[28:31],a[92:95],a[236:239],a[28:31]
ds_read_b64_tr_b16 a[70:71],v50 offset:19456
v_mfma_f32_16x16x32_bf16 a[28:31],a[88:91],a[232:235],a[28:31]
s_add_u32 m0,s88,4096
v_mfma_f32_16x16x32_bf16 a[44:47],a[88:91],a[240:243],a[44:47]
ds_read_b64_tr_b16 a[72:73],v50 offset:1032
v_mfma_f32_16x16x32_bf16 a[44:47],a[92:95],a[244:247],a[44:47]
buffer_load_dwordx4 v44,s[36:39],s83 offen lds
v_mfma_f32_16x16x32_bf16 a[60:63],a[92:95],a[252:255],a[60:63]
ds_read_b64_tr_b16 a[74:75],v50 offset:3080
v_mfma_f32_16x16x32_bf16 a[60:63],a[88:91],a[248:251],a[60:63]
ds_read_b64_tr_b16 a[76:77],v50 offset:17416
v_mfma_f32_16x16x32_bf16 v[64:67],a[96:99],a[224:227],v[64:67]
ds_read_b64_tr_b16 a[78:79],v50 offset:19464
v_mfma_f32_16x16x32_bf16 v[64:67],a[100:103],a[228:231],v[64:67]
ds_read_b64_tr_b16 a[80:81],v50 offset:1280
v_mfma_f32_16x16x32_bf16 v[80:83],a[100:103],a[236:239],v[80:83]
s_add_u32 m0,s88,5120
v_mfma_f32_16x16x32_bf16 v[80:83],a[96:99],a[232:235],v[80:83]
ds_read_b64_tr_b16 a[82:83],v50 offset:3328
v_mfma_f32_16x16x32_bf16 v[96:99],a[96:99],a[240:243],v[96:99]
buffer_load_dwordx4 v45,s[36:39],s83 offen lds
v_mfma_f32_16x16x32_bf16 v[96:99],a[100:103],a[244:247],v[96:99]
ds_read_b64_tr_b16 a[84:85],v50 offset:17664
v_mfma_f32_16x16x32_bf16 v[112:115],a[100:103],a[252:255],v[112:115]
ds_read_b64_tr_b16 a[86:87],v50 offset:19712
v_mfma_f32_16x16x32_bf16 v[112:115],a[96:99],a[248:251],v[112:115]
ds_read_b64_tr_b16 a[88:89],v50 offset:1288
v_mfma_f32_16x16x32_bf16 v[68:71],a[104:107],a[224:227],v[68:71]
s_waitcnt lgkmcnt(5)
ds_read_b64_tr_b16 a[90:91],v50 offset:3336
v_mfma_f32_16x16x32_bf16 v[68:71],a[108:111],a[228:231],v[68:71]
s_add_u32 m0,s88,6144
v_mfma_f32_16x16x32_bf16 v[84:87],a[108:111],a[236:239],v[84:87]
ds_read_b64_tr_b16 a[92:93],v50 offset:17672
v_mfma_f32_16x16x32_bf16 v[84:87],a[104:107],a[232:235],v[84:87]
buffer_load_dwordx4 v46,s[36:39],s83 offen lds
v_mfma_f32_16x16x32_bf16 v[100:103],a[104:107],a[240:243],v[100:103]
ds_read_b64_tr_b16 a[94:95],v50 offset:19720
v_mfma_f32_16x16x32_bf16 v[100:103],a[108:111],a[244:247],v[100:103]
ds_read_b64_tr_b16 a[96:97],v50 offset:1536
v_mfma_f32_16x16x32_bf16 v[116:119],a[108:111],a[252:255],v[116:119]
ds_read_b64_tr_b16 a[98:99],v50 offset:3584
v_mfma_f32_16x16x32_bf16 v[116:119],a[104:107],a[248:251],v[116:119]
ds_read_b64_tr_b16 a[100:101],v50 offset:17920
v_mfma_f32_16x16x32_bf16 v[72:75],a[112:115],a[224:227],v[72:75]
s_add_u32 m0,s88,7168
s_mov_b32 s88,s69
s_mov_b32 s69,s86
v_mfma_f32_16x16x32_bf16 v[72:75],a[116:119],a[228:231],v[72:75]
ds_read_b64_tr_b16 a[102:103],v50 offset:19968
v_mfma_f32_16x16x32_bf16 v[88:91],a[116:119],a[236:239],v[88:91]
buffer_load_dwordx4 v47,s[36:39],s83 offen lds
v_mfma_f32_16x16x32_bf16 v[88:91],a[112:115],a[232:235],v[88:91]
s_add_u32 s83,s83,524288
ds_read_b64_tr_b16 a[104:105],v50 offset:1544
v_mfma_f32_16x16x32_bf16 v[104:107],a[112:115],a[240:243],v[104:107]
ds_read_b64_tr_b16 a[106:107],v50 offset:3592
v_mfma_f32_16x16x32_bf16 v[104:107],a[116:119],a[244:247],v[104:107]
ds_read_b64_tr_b16 a[108:109],v50 offset:17928
v_mfma_f32_16x16x32_bf16 v[120:123],a[116:119],a[252:255],v[120:123]
ds_read_b64_tr_b16 a[110:111],v50 offset:19976
v_mfma_f32_16x16x32_bf16 v[120:123],a[112:115],a[248:251],v[120:123]
s_add_u32 m0,s85,0
v_mfma_f32_16x16x32_bf16 v[76:79],a[120:123],a[224:227],v[76:79]
ds_read_b64_tr_b16 a[112:113],v50 offset:1792
v_mfma_f32_16x16x32_bf16 v[76:79],a[124:127],a[228:231],v[76:79]
buffer_load_dwordx4 v32,s[28:31],s84 offen lds
v_mfma_f32_16x16x32_bf16 v[92:95],a[124:127],a[236:239],v[92:95]
ds_read_b64_tr_b16 a[114:115],v50 offset:3840
v_mfma_f32_16x16x32_bf16 v[92:95],a[120:123],a[232:235],v[92:95]
ds_read_b64_tr_b16 a[116:117],v50 offset:18176
v_mfma_f32_16x16x32_bf16 v[108:111],a[120:123],a[240:243],v[108:111]
ds_read_b64_tr_b16 a[118:119],v50 offset:20224
v_mfma_f32_16x16x32_bf16 v[108:111],a[124:127],a[244:247],v[108:111]
s_cmp_eq_u32 s70,1
s_cselect_b32 s65,256,0
s_cselect_b32 s66,524288,0
v_mfma_f32_16x16x32_bf16 v[124:127],a[124:127],a[252:255],v[124:127]
ds_read_b64_tr_b16 a[124:125],v50 offset:18184
v_add_u32 v40,s65,v40
v_mfma_f32_16x16x32_bf16 v[124:127],a[120:123],a[248:251],v[124:127]
ds_read_b64_tr_b16 a[120:121],v50 offset:1800
v_add_u32 v41,s66,v41
v_mfma_f32_16x16x32_bf16 v[128:131],a[64:67],a[224:227],v[128:131]
buffer_load_dwordx4 v33,s[28:31],s84 offen offset:1024 lds
v_mfma_f32_16x16x32_bf16 v[128:131],a[68:71],a[228:231],v[128:131]
ds_read_b64_tr_b16 a[122:123],v50 offset:3848
v_add_u32 v42,s65,v42
v_mfma_f32_16x16x32_bf16 v[160:163],a[68:71],a[236:239],v[160:163]
ds_read_b64_tr_b16 a[126:127],v50 offset:20232
v_add_u32 v50,s86,v51
v_mfma_f32_16x16x32_bf16 v[160:163],a[64:67],a[232:235],v[160:163]
s_mov_b32 s86,s87
v_add_u32 v43,s66,v43
v_add_u32 v44,s65,v44
v_mfma_f32_16x16x32_bf16 v[192:195],a[64:67],a[240:243],v[192:195]
s_mov_b32 s87,s88
v_add_u32 v45,s66,v45
v_add_u32 v46,s65,v46
v_mfma_f32_16x16x32_bf16 v[192:195],a[68:71],a[244:247],v[192:195]
v_add_u32 v47,s66,v47
v_mfma_f32_16x16x32_bf16 v[224:227],a[68:71],a[252:255],v[224:227]
v_mfma_f32_16x16x32_bf16 v[224:227],a[64:67],a[248:251],v[224:227]
buffer_load_dwordx4 v34,s[28:31],s84 offen offset:2048 lds
v_mfma_f32_16x16x32_bf16 v[132:135],a[72:75],a[224:227],v[132:135]
v_mfma_f32_16x16x32_bf16 v[132:135],a[76:79],a[228:231],v[132:135]
v_mfma_f32_16x16x32_bf16 v[164:167],a[76:79],a[236:239],v[164:167]
v_mfma_f32_16x16x32_bf16 v[164:167],a[72:75],a[232:235],v[164:167]
v_mfma_f32_16x16x32_bf16 v[196:199],a[72:75],a[240:243],v[196:199]
v_mfma_f32_16x16x32_bf16 v[196:199],a[76:79],a[244:247],v[196:199]
v_mfma_f32_16x16x32_bf16 v[228:231],a[76:79],a[252:255],v[228:231]
buffer_load_dwordx4 v35,s[28:31],s84 offen offset:3072 lds
v_mfma_f32_16x16x32_bf16 v[228:231],a[72:75],a[248:251],v[228:231]
s_waitcnt lgkmcnt(0)
v_mfma_f32_16x16x32_bf16 v[136:139],a[80:83],a[224:227],v[136:139]
v_mfma_f32_16x16x32_bf16 v[136:139],a[84:87],a[228:231],v[136:139]
v_mfma_f32_16x16x32_bf16 v[168:171],a[84:87],a[236:239],v[168:171]
v_mfma_f32_16x16x32_bf16 v[168:171],a[80:83],a[232:235],v[168:171]
s_add_u32 m0,s85,4096
s_xor_b32 s85,s85,98304
v_mfma_f32_16x16x32_bf16 v[200:203],a[80:83],a[240:243],v[200:203]
v_mfma_f32_16x16x32_bf16 v[200:203],a[84:87],a[244:247],v[200:203]
buffer_load_dwordx4 v36,s[28:31],s84 offen lds
v_mfma_f32_16x16x32_bf16 v[232:235],a[84:87],a[252:255],v[232:235]
v_mfma_f32_16x16x32_bf16 v[232:235],a[80:83],a[248:251],v[232:235]
v_mfma_f32_16x16x32_bf16 v[140:143],a[88:91],a[224:227],v[140:143]
v_mfma_f32_16x16x32_bf16 v[140:143],a[92:95],a[228:231],v[140:143]
v_mfma_f32_16x16x32_bf16 v[172:175],a[92:95],a[236:239],v[172:175]
v_mfma_f32_16x16x32_bf16 v[172:175],a[88:91],a[232:235],v[172:175]
v_mfma_f32_16x16x32_bf16 v[204:207],a[88:91],a[240:243],v[204:207]
buffer_load_dwordx4 v37,s[28:31],s84 offen offset:1024 lds
v_mfma_f32_16x16x32_bf16 v[204:207],a[92:95],a[244:247],v[204:207]
v_mfma_f32_16x16x32_bf16 v[236:239],a[92:95],a[252:255],v[236:239]
v_mfma_f32_16x16x32_bf16 v[236:239],a[88:91],a[248:251],v[236:239]
s_waitcnt vmcnt(23)
s_barrier
v_mfma_f32_16x16x32_bf16 v[144:147],a[96:99],a[224:227],v[144:147]
s_waitcnt vmcnt(15)
ds_read_b128 a[192:195],v48
v_mfma_f32_16x16x32_bf16 v[144:147],a[100:103],a[228:231],v[144:147]
ds_read_b128 a[196:199],v49
v_mfma_f32_16x16x32_bf16 v[176:179],a[100:103],a[236:239],v[176:179]
buffer_load_dwordx4 v38,s[28:31],s84 offen offset:2048 lds
v_mfma_f32_16x16x32_bf16 v[176:179],a[96:99],a[232:235],v[176:179]
ds_read_b128 a[200:203],v48 offset:2048
v_mfma_f32_16x16x32_bf16 v[208:211],a[96:99],a[240:243],v[208:211]
ds_read_b128 a[204:207],v49 offset:2048
v_mfma_f32_16x16x32_bf16 v[208:211],a[100:103],a[244:247],v[208:211]
ds_read_b128 a[208:211],v48 offset:4096
v_mfma_f32_16x16x32_bf16 v[240:243],a[100:103],a[252:255],v[240:243]
ds_read_b128 a[212:215],v49 offset:4096
v_mfma_f32_16x16x32_bf16 v[240:243],a[96:99],a[248:251],v[240:243]
v_mfma_f32_16x16x32_bf16 v[148:151],a[104:107],a[224:227],v[148:151]
ds_read_b128 a[216:219],v48 offset:6144
v_xor_b32 v48,98304,v48
v_mfma_f32_16x16x32_bf16 v[148:151],a[108:111],a[228:231],v[148:151]
buffer_load_dwordx4 v39,s[28:31],s84 offen offset:3072 lds
v_mfma_f32_16x16x32_bf16 v[180:183],a[108:111],a[236:239],v[180:183]
s_add_u32 s84,s84,128
ds_read_b128 a[220:223],v49 offset:6144
v_xor_b32 v49,98304,v49
v_mfma_f32_16x16x32_bf16 v[180:183],a[104:107],a[232:235],v[180:183]
ds_read_b64_tr_b16 a[64:65],v50
v_mfma_f32_16x16x32_bf16 v[212:215],a[104:107],a[240:243],v[212:215]
ds_read_b64_tr_b16 a[66:67],v50 offset:2048
v_mfma_f32_16x16x32_bf16 v[212:215],a[108:111],a[244:247],v[212:215]
ds_read_b64_tr_b16 a[68:69],v50 offset:16384
v_mfma_f32_16x16x32_bf16 v[244:247],a[108:111],a[252:255],v[244:247]
v_mfma_f32_16x16x32_bf16 v[244:247],a[104:107],a[248:251],v[244:247]
ds_read_b64_tr_b16 a[70:71],v50 offset:18432
v_mfma_f32_16x16x32_bf16 v[152:155],a[112:115],a[224:227],v[152:155]
buffer_load_dword v9,v19,s[48:51],s90 offen
v_mfma_f32_16x16x32_bf16 v[152:155],a[116:119],a[228:231],v[152:155]
s_add_u32 s90,s90,896
ds_read_b64_tr_b16 a[72:73],v50 offset:8
v_mfma_f32_16x16x32_bf16 v[184:187],a[116:119],a[236:239],v[184:187]
ds_read_b64_tr_b16 a[74:75],v50 offset:2056
v_mfma_f32_16x16x32_bf16 v[184:187],a[112:115],a[232:235],v[184:187]
ds_read_b64_tr_b16 a[76:77],v50 offset:16392
v_mfma_f32_16x16x32_bf16 v[216:219],a[112:115],a[240:243],v[216:219]
ds_read_b64_tr_b16 a[78:79],v50 offset:18440
v_mfma_f32_16x16x32_bf16 v[216:219],a[116:119],a[244:247],v[216:219]
v_mfma_f32_16x16x32_bf16 v[248:251],a[116:119],a[252:255],v[248:251]
ds_read_b64_tr_b16 a[80:81],v50 offset:256
v_mfma_f32_16x16x32_bf16 v[248:251],a[112:115],a[248:251],v[248:251]
ds_read_b64_tr_b16 a[82:83],v50 offset:2304
v_mfma_f32_16x16x32_bf16 v[156:159],a[120:123],a[224:227],v[156:159]
ds_read_b64_tr_b16 a[84:85],v50 offset:16640
v_mfma_f32_16x16x32_bf16 v[156:159],a[124:127],a[228:231],v[156:159]
v_mfma_f32_16x16x32_bf16 v[188:191],a[124:127],a[236:239],v[188:191]
ds_read_b64_tr_b16 a[86:87],v50 offset:18688
v_mfma_f32_16x16x32_bf16 v[188:191],a[120:123],a[232:235],v[188:191]
ds_read_b64_tr_b16 a[88:89],v50 offset:264
v_mfma_f32_16x16x32_bf16 v[220:223],a[120:123],a[240:243],v[220:223]
ds_read_b64_tr_b16 a[90:91],v50 offset:2312
v_mfma_f32_16x16x32_bf16 v[220:223],a[124:127],a[244:247],v[220:223]
v_mfma_f32_16x16x32_bf16 v[252:255],a[124:127],a[252:255],v[252:255]
s_waitcnt lgkmcnt(4)
ds_read_b64_tr_b16 a[92:93],v50 offset:16648
v_mfma_f32_16x16x32_bf16 v[252:255],a[120:123],a[248:251],v[252:255]
s_add_u32 m0,s88,0
ds_read_b64_tr_b16 a[94:95],v50 offset:18696
v_mfma_f32_16x16x32_bf16 a[0:3],a[64:67],a[192:195],a[0:3]
ds_read_b64_tr_b16 a[96:97],v50 offset:512
v_mfma_f32_16x16x32_bf16 a[0:3],a[68:71],a[196:199],a[0:3]
buffer_load_dwordx4 v40,s[36:39],s83 offen lds
v_mfma_f32_16x16x32_bf16 a[16:19],a[68:71],a[204:207],a[16:19]
ds_read_b64_tr_b16 a[98:99],v50 offset:2560
v_mfma_f32_16x16x32_bf16 a[16:19],a[64:67],a[200:203],a[16:19]
ds_read_b64_tr_b16 a[100:101],v50 offset:16896
v_mfma_f32_16x16x32_bf16 a[32:35],a[64:67],a[208:211],a[32:35]
ds_read_b64_tr_b16 a[102:103],v50 offset:18944
v_mfma_f32_16x16x32_bf16 a[32:35],a[68:71],a[212:215],a[32:35]
)A"
R"A(v_mfma_f32_16x16x32_bf16 a[48:51],a[68:71],a[220:223],a[48:51]
s_add_u32 m0,s88,1024
ds_read_b64_tr_b16 a[104:105],v50 offset:520
v_mfma_f32_16x16x32_bf16 a[48:51],a[64:67],a[216:219],a[48:51]
ds_read_b64_tr_b16 a[106:107],v50 offset:2568
v_mfma_f32_16x16x32_bf16 a[4:7],a[72:75],a[192:195],a[4:7]
buffer_load_dwordx4 v41,s[36:39],s83 offen lds
v_mfma_f32_16x16x32_bf16 a[4:7],a[76:79],a[196:199],a[4:7]
ds_read_b64_tr_b16 a[108:109],v50 offset:16904
v_mfma_f32_16x16x32_bf16 a[20:23],a[76:79],a[204:207],a[20:23]
ds_read_b64_tr_b16 a[110:111],v50 offset:18952
v_mfma_f32_16x16x32_bf16 a[20:23],a[72:75],a[200:203],a[20:23]
ds_read_b64_tr_b16 a[112:113],v50 offset:768
v_mfma_f32_16x16x32_bf16 a[36:39],a[72:75],a[208:211],a[36:39]
ds_read_b64_tr_b16 a[114:115],v50 offset:2816
v_mfma_f32_16x16x32_bf16 a[36:39],a[76:79],a[212:215],a[36:39]
s_add_u32 m0,s88,2048
v_mfma_f32_16x16x32_bf16 a[52:55],a[76:79],a[220:223],a[52:55]
ds_read_b64_tr_b16 a[116:117],v50 offset:17152
v_mfma_f32_16x16x32_bf16 a[52:55],a[72:75],a[216:219],a[52:55]
buffer_load_dwordx4 v42,s[36:39],s83 offen lds
v_mfma_f32_16x16x32_bf16 a[8:11],a[80:83],a[192:195],a[8:11]
s_waitcnt lgkmcnt(5)
ds_read_b64_tr_b16 a[118:119],v50 offset:19200
v_mfma_f32_16x16x32_bf16 a[8:11],a[84:87],a[196:199],a[8:11]
ds_read_b64_tr_b16 a[120:121],v50 offset:776
v_mfma_f32_16x16x32_bf16 a[24:27],a[84:87],a[204:207],a[24:27]
ds_read_b64_tr_b16 a[122:123],v50 offset:2824
v_mfma_f32_16x16x32_bf16 a[24:27],a[80:83],a[200:203],a[24:27]
ds_read_b64_tr_b16 a[124:125],v50 offset:17160
v_mfma_f32_16x16x32_bf16 a[40:43],a[80:83],a[208:211],a[40:43]
s_add_u32 m0,s88,3072
v_mfma_f32_16x16x32_bf16 a[40:43],a[84:87],a[212:215],a[40:43]
ds_read_b64_tr_b16 a[126:127],v50 offset:19208
v_mfma_f32_16x16x32_bf16 a[56:59],a[84:87],a[220:223],a[56:59]
buffer_load_dwordx4 v43,s[36:39],s83 offen lds
v_mfma_f32_16x16x32_bf16 a[56:59],a[80:83],a[216:219],a[56:59]
ds_read_b64_tr_b16 a[64:65],v50 offset:1024
v_mfma_f32_16x16x32_bf16 a[12:15],a[88:91],a[192:195],a[12:15]
ds_read_b64_tr_b16 a[66:67],v50 offset:3072
v_mfma_f32_16x16x32_bf16 a[12:15],a[92:95],a[196:199],a[12:15]
ds_read_b64_tr_b16 a[68:69],v50 offset:17408
v_mfma_f32_16x16x32_bf16 a[28:31],a[92:95],a[204:207],a[28:31]
ds_read_b64_tr_b16 a[70:71],v50 offset:19456
v_mfma_f32_16x16x32_bf16 a[28:31],a[88:91],a[200:203],a[28:31]
s_add_u32 m0,s88,4096
v_mfma_f32_16x16x32_bf16 a[44:47],a[88:91],a[208:211],a[44:47]
ds_read_b64_tr_b16 a[72:73],v50 offset:1032
v_mfma_f32_16x16x32_bf16 a[44:47],a[92:95],a[212:215],a[44:47]
buffer_load_dwordx4 v44,s[36:39],s83 offen lds
v_mfma_f32_16x16x32_bf16 a[60:63],a[92:95],a[220:223],a[60:63]
ds_read_b64_tr_b16 a[74:75],v50 offset:3080
v_mfma_f32_16x16x32_bf16 a[60:63],a[88:91],a[216:219],a[60:63]
ds_read_b64_tr_b16 a[76:77],v50 offset:17416
v_mfma_f32_16x16x32_bf16 v[64:67],a[96:99],a[192:195],v[64:67]
ds_read_b64_tr_b16 a[78:79],v50 offset:19464
v_mfma_f32_16x16x32_bf16 v[64:67],a[100:103],a[196:199],v[64:67]
ds_read_b64_tr_b16 a[80:81],v50 offset:1280
v_mfma_f32_16x16x32_bf16 v[80:83],a[100:103],a[204:207],v[80:83]
s_add_u32 m0,s88,5120
v_mfma_f32_16x16x32_bf16 v[80:83],a[96:99],a[200:203],v[80:83]
ds_read_b64_tr_b16 a[82:83],v50 offset:3328
v_mfma_f32_16x16x32_bf16 v[96:99],a[96:99],a[208:211],v[96:99]
buffer_load_dwordx4 v45,s[36:39],s83 offen lds
v_mfma_f32_16x16x32_bf16 v[96:99],a[100:103],a[212:215],v[96:99]
ds_read_b64_tr_b16 a[84:85],v50 offset:17664
v_mfma_f32_16x16x32_bf16 v[112:115],a[100:103],a[220:223],v[112:115]
ds_read_b64_tr_b16 a[86:87],v50 offset:19712
v_mfma_f32_16x16x32_bf16 v[112:115],a[96:99],a[216:219],v[112:115]
ds_read_b64_tr_b16 a[88:89],v50 offset:1288
v_mfma_f32_16x16x32_bf16 v[68:71],a[104:107],a[192:195],v[68:71]
s_waitcnt lgkmcnt(5)
ds_read_b64_tr_b16 a[90:91],v50 offset:3336
v_mfma_f32_16x16x32_bf16 v[68:71],a[108:111],a[196:199],v[68:71]
s_add_u32 m0,s88,6144
v_mfma_f32_16x16x32_bf16 v[84:87],a[108:111],a[204:207],v[84:87]
ds_read_b64_tr_b16 a[92:93],v50 offset:17672
v_mfma_f32_16x16x32_bf16 v[84:87],a[104:107],a[200:203],v[84:87]
buffer_load_dwordx4 v46,s[36:39],s83 offen lds
v_mfma_f32_16x16x32_bf16 v[100:103],a[104:107],a[208:211],v[100:103]
ds_read_b64_tr_b16 a[94:95],v50 offset:19720
v_mfma_f32_16x16x32_bf16 v[100:103],a[108:111],a[212:215],v[100:103]
ds_read_b64_tr_b16 a[96:97],v50 offset:1536
v_mfma_f32_16x16x32_bf16 v[116:119],a[108:111],a[220:223],v[116:119]
ds_read_b64_tr_b16 a[98:99],v50 offset:3584
v_mfma_f32_16x16x32_bf16 v[116:119],a[104:107],a[216:219],v[116:119]
ds_read_b64_tr_b16 a[100:101],v50 offset:17920
v_mfma_f32_16x16x32_bf16 v[72:75],a[112:115],a[192:195],v[72:75]
s_add_u32 m0,s88,7168
s_mov_b32 s88,s69
v_mfma_f32_16x16x32_bf16 v[72:75],a[116:119],a[196:199],v[72:75]
ds_read_b64_tr_b16 a[102:103],v50 offset:19968
v_mfma_f32_16x16x32_bf16 v[88:91],a[116:119],a[204:207],v[88:91]
buffer_load_dwordx4 v47,s[36:39],s83 offen lds
v_mfma_f32_16x16x32_bf16 v[88:91],a[112:115],a[200:203],v[88:91]
s_add_u32 s83,s83,524288
ds_read_b64_tr_b16 a[104:105],v50 offset:1544
v_mfma_f32_16x16x32_bf16 v[104:107],a[112:115],a[208:211],v[104:107]
ds_read_b64_tr_b16 a[106:107],v50 offset:3592
v_mfma_f32_16x16x32_bf16 v[104:107],a[116:119],a[212:215],v[104:107]
ds_read_b64_tr_b16 a[108:109],v50 offset:17928
v_mfma_f32_16x16x32_bf16 v[120:123],a[116:119],a[220:223],v[120:123]
ds_read_b64_tr_b16 a[110:111],v50 offset:19976
v_mfma_f32_16x16x32_bf16 v[120:123],a[112:115],a[216:219],v[120:123]
s_add_u32 m0,s85,0
v_mfma_f32_16x16x32_bf16 v[76:79],a[120:123],a[192:195],v[76:79]
ds_read_b64_tr_b16 a[112:113],v50 offset:1792
v_mfma_f32_16x16x32_bf16 v[76:79],a[124:127],a[196:199],v[76:79]
buffer_load_dwordx4 v32,s[28:31],s84 offen lds
v_mfma_f32_16x16x32_bf16 v[92:95],a[124:127],a[204:207],v[92:95]
ds_read_b64_tr_b16 a[114:115],v50 offset:3840
v_mfma_f32_16x16x32_bf16 v[92:95],a[120:123],a[200:203],v[92:95]
ds_read_b64_tr_b16 a[116:117],v50 offset:18176
v_mfma_f32_16x16x32_bf16 v[108:111],a[120:123],a[208:211],v[108:111]
ds_read_b64_tr_b16 a[118:119],v50 offset:20224
v_mfma_f32_16x16x32_bf16 v[108:111],a[124:127],a[212:215],v[108:111]
v_mfma_f32_16x16x32_bf16 v[124:127],a[124:127],a[220:223],v[124:127]
ds_read_b64_tr_b16 a[124:125],v50 offset:18184
v_mfma_f32_16x16x32_bf16 v[124:127],a[120:123],a[216:219],v[124:127]
ds_read_b64_tr_b16 a[120:121],v50 offset:1800
v_mfma_f32_16x16x32_bf16 v[128:131],a[64:67],a[192:195],v[128:131]
buffer_load_dwordx4 v33,s[28:31],s84 offen offset:1024 lds
v_mfma_f32_16x16x32_bf16 v[128:131],a[68:71],a[196:199],v[128:131]
ds_read_b64_tr_b16 a[122:123],v50 offset:3848
v_mfma_f32_16x16x32_bf16 v[160:163],a[68:71],a[204:207],v[160:163]
ds_read_b64_tr_b16 a[126:127],v50 offset:20232
v_add_u32 v50,s86,v51
v_mfma_f32_16x16x32_bf16 v[160:163],a[64:67],a[200:203],v[160:163]
v_mfma_f32_16x16x32_bf16 v[192:195],a[64:67],a[208:211],v[192:195]
v_mfma_f32_16x16x32_bf16 v[192:195],a[68:71],a[212:215],v[192:195]
v_mfma_f32_16x16x32_bf16 v[224:227],a[68:71],a[220:223],v[224:227]
v_mfma_f32_16x16x32_bf16 v[224:227],a[64:67],a[216:219],v[224:227]
buffer_load_dwordx4 v34,s[28:31],s84 offen offset:2048 lds
v_mfma_f32_16x16x32_bf16 v[132:135],a[72:75],a[192:195],v[132:135]
v_mfma_f32_16x16x32_bf16 v[132:135],a[76:79],a[196:199],v[132:135]
v_mfma_f32_16x16x32_bf16 v[164:167],a[76:79],a[204:207],v[164:167]
v_mfma_f32_16x16x32_bf16 v[164:167],a[72:75],a[200:203],v[164:167]
v_mfma_f32_16x16x32_bf16 v[196:199],a[72:75],a[208:211],v[196:199]
v_mfma_f32_16x16x32_bf16 v[196:199],a[76:79],a[212:215],v[196:199]
v_mfma_f32_16x16x32_bf16 v[228:231],a[76:79],a[220:223],v[228:231]
buffer_load_dwordx4 v35,s[28:31],s84 offen offset:3072 lds
v_mfma_f32_16x16x32_bf16 v[228:231],a[72:75],a[216:219],v[228:231]
s_waitcnt lgkmcnt(0)
v_mfma_f32_16x16x32_bf16 v[136:139],a[80:83],a[192:195],v[136:139]
v_mfma_f32_16x16x32_bf16 v[136:139],a[84:87],a[196:199],v[136:139]
v_mfma_f32_16x16x32_bf16 v[168:171],a[84:87],a[204:207],v[168:171]
v_mfma_f32_16x16x32_bf16 v[168:171],a[80:83],a[200:203],v[168:171]
s_add_u32 m0,s85,4096
s_xor_b32 s85,s85,98304
v_mfma_f32_16x16x32_bf16 v[200:203],a[80:83],a[208:211],v[200:203]
v_mfma_f32_16x16x32_bf16 v[200:203],a[84:87],a[212:215],v[200:203]
buffer_load_dwordx4 v36,s[28:31],s84 offen lds
v_mfma_f32_16x16x32_bf16 v[232:235],a[84:87],a[220:223],v[232:235]
v_mfma_f32_16x16x32_bf16 v[232:235],a[80:83],a[216:219],v[232:235]
v_mfma_f32_16x16x32_bf16 v[140:143],a[88:91],a[192:195],v[140:143]
v_mfma_f32_16x16x32_bf16 v[140:143],a[92:95],a[196:199],v[140:143]
v_mfma_f32_16x16x32_bf16 v[172:175],a[92:95],a[204:207],v[172:175]
v_mfma_f32_16x16x32_bf16 v[172:175],a[88:91],a[200:203],v[172:175]
v_mfma_f32_16x16x32_bf16 v[204:207],a[88:91],a[208:211],v[204:207]
buffer_load_dwordx4 v37,s[28:31],s84 offen offset:1024 lds
v_mfma_f32_16x16x32_bf16 v[204:207],a[92:95],a[212:215],v[204:207]
v_mfma_f32_16x16x32_bf16 v[236:239],a[92:95],a[220:223],v[236:239]
v_mfma_f32_16x16x32_bf16 v[236:239],a[88:91],a[216:219],v[236:239]
s_waitcnt vmcnt(23)
s_barrier
v_mfma_f32_16x16x32_bf16 v[144:147],a[96:99],a[192:195],v[144:147]
s_waitcnt vmcnt(15)
ds_read_b128 a[224:227],v48
v_mfma_f32_16x16x32_bf16 v[144:147],a[100:103],a[196:199],v[144:147]
ds_read_b128 a[228:231],v49
v_mfma_f32_16x16x32_bf16 v[176:179],a[100:103],a[204:207],v[176:179]
buffer_load_dwordx4 v38,s[28:31],s84 offen offset:2048 lds
v_mfma_f32_16x16x32_bf16 v[176:179],a[96:99],a[200:203],v[176:179]
ds_read_b128 a[232:235],v48 offset:2048
v_mfma_f32_16x16x32_bf16 v[208:211],a[96:99],a[208:211],v[208:211]
ds_read_b128 a[236:239],v49 offset:2048
v_mfma_f32_16x16x32_bf16 v[208:211],a[100:103],a[212:215],v[208:211]
ds_read_b128 a[240:243],v48 offset:4096
v_mfma_f32_16x16x32_bf16 v[240:243],a[100:103],a[220:223],v[240:243]
ds_read_b128 a[244:247],v49 offset:4096
v_mfma_f32_16x16x32_bf16 v[240:243],a[96:99],a[216:219],v[240:243]
v_mfma_f32_16x16x32_bf16 v[148:151],a[104:107],a[192:195],v[148:151]
ds_read_b128 a[248:251],v48 offset:6144
v_mfma_f32_16x16x32_bf16 v[148:151],a[108:111],a[196:199],v[148:151]
buffer_load_dwordx4 v39,s[28:31],s84 offen offset:3072 lds
v_mfma_f32_16x16x32_bf16 v[180:183],a[108:111],a[204:207],v[180:183]
s_add_u32 s84,s84,128
ds_read_b128 a[252:255],v49 offset:6144
v_mfma_f32_16x16x32_bf16 v[180:183],a[104:107],a[200:203],v[180:183]
ds_read_b64_tr_b16 a[64:65],v50
v_mfma_f32_16x16x32_bf16 v[212:215],a[104:107],a[208:211],v[212:215]
ds_read_b64_tr_b16 a[66:67],v50 offset:2048
v_mfma_f32_16x16x32_bf16 v[212:215],a[108:111],a[212:215],v[212:215]
ds_read_b64_tr_b16 a[68:69],v50 offset:16384
v_mfma_f32_16x16x32_bf16 v[244:247],a[108:111],a[220:223],v[244:247]
v_mfma_f32_16x16x32_bf16 v[244:247],a[104:107],a[216:219],v[244:247]
ds_read_b64_tr_b16 a[70:71],v50 offset:18432
v_mfma_f32_16x16x32_bf16 v[152:155],a[112:115],a[192:195],v[152:155]
buffer_load_dword v8,v19,s[48:51],s90 offen
v_mfma_f32_16x16x32_bf16 v[152:155],a[116:119],a[196:199],v[152:155]
s_add_u32 s90,s90,896
ds_read_b64_tr_b16 a[72:73],v50 offset:8
v_mfma_f32_16x16x32_bf16 v[184:187],a[116:119],a[204:207],v[184:187]
ds_read_b64_tr_b16 a[74:75],v50 offset:2056
v_mfma_f32_16x16x32_bf16 v[184:187],a[112:115],a[200:203],v[184:187]
ds_read_b64_tr_b16 a[76:77],v50 offset:16392
v_mfma_f32_16x16x32_bf16 v[216:219],a[112:115],a[208:211],v[216:219]
ds_read_b64_tr_b16 a[78:79],v50 offset:18440
v_mfma_f32_16x16x32_bf16 v[216:219],a[116:119],a[212:215],v[216:219]
v_mfma_f32_16x16x32_bf16 v[248:251],a[116:119],a[220:223],v[248:251]
ds_read_b64_tr_b16 a[80:81],v50 offset:256
v_mfma_f32_16x16x32_bf16 v[248:251],a[112:115],a[216:219],v[248:251]
ds_read_b64_tr_b16 a[82:83],v50 offset:2304
v_mfma_f32_16x16x32_bf16 v[156:159],a[120:123],a[192:195],v[156:159]
ds_read_b64_tr_b16 a[84:85],v50 offset:16640
v_mfma_f32_16x16x32_bf16 v[156:159],a[124:127],a[196:199],v[156:159]
v_mfma_f32_16x16x32_bf16 v[188:191],a[124:127],a[204:207],v[188:191]
ds_read_b64_tr_b16 a[86:87],v50 offset:18688
v_mfma_f32_16x16x32_bf16 v[188:191],a[120:123],a[200:203],v[188:191]
ds_read_b64_tr_b16 a[88:89],v50 offset:264
v_mfma_f32_16x16x32_bf16 v[220:223],a[120:123],a[208:211],v[220:223]
)A"
R"A(ds_read_b64_tr_b16 a[90:91],v50 offset:2312
v_mfma_f32_16x16x32_bf16 v[220:223],a[124:127],a[212:215],v[220:223]
v_mfma_f32_16x16x32_bf16 v[252:255],a[124:127],a[220:223],v[252:255]
ds_read_b64_tr_b16 a[92:93],v50 offset:16648
v_mfma_f32_16x16x32_bf16 v[252:255],a[120:123],a[216:219],v[252:255]
ds_read_b64_tr_b16 a[94:95],v50 offset:18696
s_sub_u32 s70,s70,1
s_cmp_lg_u32 s70,0
s_cbranch_scc1 L_qp_%=
s_waitcnt lgkmcnt(6)
v_mfma_f32_16x16x32_bf16 a[0:3],a[64:67],a[224:227],a[0:3]
s_memrealtime s[92:93]
s_memtime s[56:57]
s_add_u32 m0,s85,0
v_mfma_f32_16x16x32_bf16 a[0:3],a[68:71],a[228:231],a[0:3]
buffer_load_dwordx4 v32,s[28:31],s84 offen lds
v_mfma_f32_16x16x32_bf16 a[16:19],a[68:71],a[236:239],a[16:19]
s_sub_u32 s83,s83,524288
ds_read_b64_tr_b16 a[96:97],v50 offset:512
v_subrev_u32 v40,256,v40
v_mfma_f32_16x16x32_bf16 a[16:19],a[64:67],a[232:235],a[16:19]
s_mov_b32 s69,s86
ds_read_b64_tr_b16 a[98:99],v50 offset:2560
v_subrev_u32 v41,256,v41
v_mfma_f32_16x16x32_bf16 a[32:35],a[64:67],a[240:243],a[32:35]
s_mov_b32 s86,s87
ds_read_b64_tr_b16 a[100:101],v50 offset:16896
v_subrev_u32 v42,256,v42
v_mfma_f32_16x16x32_bf16 a[32:35],a[68:71],a[244:247],a[32:35]
s_mov_b32 s87,s88
ds_read_b64_tr_b16 a[102:103],v50 offset:18944
v_subrev_u32 v43,256,v43
v_mfma_f32_16x16x32_bf16 a[48:51],a[68:71],a[252:255],a[48:51]
s_add_u32 s75,s75,16384
v_subrev_u32 v44,256,v44
v_subrev_u32 v45,256,v45
v_mfma_f32_16x16x32_bf16 a[48:51],a[64:67],a[248:251],a[48:51]
buffer_load_dwordx4 v33,s[28:31],s84 offen offset:1024 lds
v_mfma_f32_16x16x32_bf16 a[4:7],a[72:75],a[224:227],a[4:7]
s_sub_u32 s78,s77,32768
ds_read_b64_tr_b16 a[104:105],v50 offset:520
v_subrev_u32 v46,256,v46
v_mfma_f32_16x16x32_bf16 a[4:7],a[76:79],a[228:231],a[4:7]
L_pd2_%=:
s_cbranch_execz L_pd6_%=
s_and_b32 s78,s78,0x1ffff
ds_read_b64_tr_b16 a[106:107],v50 offset:2568
v_subrev_u32 v47,256,v47
v_mfma_f32_16x16x32_bf16 a[20:23],a[76:79],a[236:239],a[20:23]
s_sub_u32 s78,s78,s77
ds_read_b64_tr_b16 a[108:109],v50 offset:16904
v_xor_b32 v48,98304,v48
v_mfma_f32_16x16x32_bf16 a[20:23],a[72:75],a[232:235],a[20:23]
s_add_u32 s77,s77,s78
ds_read_b64_tr_b16 a[110:111],v50 offset:18952
v_xor_b32 v49,98304,v49
v_mfma_f32_16x16x32_bf16 a[36:39],a[72:75],a[240:243],a[36:39]
s_mov_b32 s6,0
v_add_u32 v52,s87,v51
v_mfma_f32_16x16x32_bf16 a[36:39],a[76:79],a[244:247],a[36:39]
buffer_load_dwordx4 v34,s[28:31],s84 offen offset:2048 lds
v_mfma_f32_16x16x32_bf16 a[52:55],a[76:79],a[252:255],a[52:55]
s_mul_i32 s66,s60,40960
ds_read_b64_tr_b16 a[112:113],v50 offset:768
v_add_u32 v11,0,v23
v_mfma_f32_16x16x32_bf16 a[52:55],a[72:75],a[248:251],a[52:55]
s_add_u32 s65,s63,1
ds_read_b64_tr_b16 a[114:115],v50 offset:2816
v_mul_u32_u24 v12,3277,v11
v_mfma_f32_16x16x32_bf16 a[8:11],a[80:83],a[224:227],a[8:11]
s_waitcnt lgkmcnt(5)
ds_read_b64_tr_b16 a[116:117],v50 offset:17152
v_lshrrev_b32 v12,16,v12
v_mfma_f32_16x16x32_bf16 a[8:11],a[84:87],a[228:231],a[8:11]
ds_read_b64_tr_b16 a[118:119],v50 offset:19200
v_mul_u32_u24 v13,20,v12
v_mfma_f32_16x16x32_bf16 a[24:27],a[84:87],a[236:239],a[24:27]
v_sub_u32 v13,v11,v13
v_bfe_u32 v14,v12,2,1
v_mfma_f32_16x16x32_bf16 a[24:27],a[80:83],a[232:235],a[24:27]
buffer_load_dwordx4 v35,s[28:31],s84 offen offset:3072 lds
v_mfma_f32_16x16x32_bf16 a[40:43],a[80:83],a[240:243],a[40:43]
ds_read_b64_tr_b16 a[120:121],v50 offset:776
v_lshlrev_b32 v14,1,v14
v_mfma_f32_16x16x32_bf16 a[40:43],a[84:87],a[244:247],a[40:43]
ds_read_b64_tr_b16 a[122:123],v50 offset:2824
v_xor_b32 v13,v13,v14
v_mfma_f32_16x16x32_bf16 a[56:59],a[84:87],a[252:255],a[56:59]
ds_read_b64_tr_b16 a[124:125],v50 offset:17160
v_lshrrev_b32 v14,2,v13
v_mfma_f32_16x16x32_bf16 a[56:59],a[80:83],a[248:251],a[56:59]
s_add_u32 m0,s85,4096
s_xor_b32 s85,s85,98304
ds_read_b64_tr_b16 a[126:127],v50 offset:19208
v_mfma_f32_16x16x32_bf16 a[12:15],a[88:91],a[224:227],a[12:15]
v_cmp_eq_u32 vcc,3,v14
v_min_u32 v15,3,v14
v_mfma_f32_16x16x32_bf16 a[12:15],a[92:95],a[228:231],a[12:15]
buffer_load_dwordx4 v36,s[28:31],s84 offen lds
v_mfma_f32_16x16x32_bf16 a[28:31],a[92:95],a[236:239],a[28:31]
ds_read_b64_tr_b16 a[64:65],v50 offset:1024
v_add_u32 v15,s65,v15
v_mfma_f32_16x16x32_bf16 a[28:31],a[88:91],a[232:235],a[28:31]
ds_read_b64_tr_b16 a[66:67],v50 offset:3072
v_and_b32 v15,3,v15
v_mfma_f32_16x16x32_bf16 a[44:47],a[88:91],a[240:243],a[44:47]
ds_read_b64_tr_b16 a[68:69],v50 offset:17408
v_cndmask_b32_e64 v15,v15,4,vcc
v_mfma_f32_16x16x32_bf16 a[44:47],a[92:95],a[244:247],a[44:47]
ds_read_b64_tr_b16 a[70:71],v50 offset:19456
v_and_b32 v13,3,v13
v_mfma_f32_16x16x32_bf16 a[60:63],a[92:95],a[252:255],a[60:63]
v_lshl_add_u32 v13,v15,2,v13
v_mul_u32_u24 v12,1280,v12
v_mfma_f32_16x16x32_bf16 a[60:63],a[88:91],a[248:251],a[60:63]
buffer_load_dwordx4 v37,s[28:31],s84 offen offset:1024 lds
v_mfma_f32_16x16x32_bf16 v[64:67],a[96:99],a[224:227],v[64:67]
ds_read_b64_tr_b16 a[72:73],v50 offset:1032
v_lshl_add_u32 v12,v13,4,v12
v_mfma_f32_16x16x32_bf16 v[64:67],a[100:103],a[228:231],v[64:67]
ds_read_b64_tr_b16 a[74:75],v50 offset:3080
v_add_u32 v12,s66,v12
v_mfma_f32_16x16x32_bf16 v[80:83],a[100:103],a[236:239],v[80:83]
ds_read_b64_tr_b16 a[76:77],v50 offset:17416
v_subrev_u32 v24,0,v12
v_mfma_f32_16x16x32_bf16 v[80:83],a[96:99],a[232:235],v[80:83]
ds_read_b64_tr_b16 a[78:79],v50 offset:19464
v_add_u32 v11,64,v23
v_mfma_f32_16x16x32_bf16 v[96:99],a[96:99],a[240:243],v[96:99]
v_mul_u32_u24 v12,3277,v11
v_lshrrev_b32 v12,16,v12
v_mfma_f32_16x16x32_bf16 v[96:99],a[100:103],a[244:247],v[96:99]
buffer_load_dwordx4 v38,s[28:31],s84 offen offset:2048 lds
v_mfma_f32_16x16x32_bf16 v[112:115],a[100:103],a[252:255],v[112:115]
ds_read_b64_tr_b16 a[80:81],v50 offset:1280
v_mul_u32_u24 v13,20,v12
v_mfma_f32_16x16x32_bf16 v[112:115],a[96:99],a[248:251],v[112:115]
s_waitcnt lgkmcnt(5)
ds_read_b64_tr_b16 a[82:83],v50 offset:3328
v_sub_u32 v13,v11,v13
v_mfma_f32_16x16x32_bf16 v[68:71],a[104:107],a[224:227],v[68:71]
ds_read_b64_tr_b16 a[84:85],v50 offset:17664
v_bfe_u32 v14,v12,2,1
v_mfma_f32_16x16x32_bf16 v[68:71],a[108:111],a[228:231],v[68:71]
ds_read_b64_tr_b16 a[86:87],v50 offset:19712
v_lshlrev_b32 v14,1,v14
v_mfma_f32_16x16x32_bf16 v[84:87],a[108:111],a[236:239],v[84:87]
v_xor_b32 v13,v13,v14
v_lshrrev_b32 v14,2,v13
v_mfma_f32_16x16x32_bf16 v[84:87],a[104:107],a[232:235],v[84:87]
buffer_load_dwordx4 v39,s[28:31],s84 offen offset:3072 lds
v_mfma_f32_16x16x32_bf16 v[100:103],a[104:107],a[240:243],v[100:103]
s_add_u32 s84,s84,128
ds_read_b64_tr_b16 a[88:89],v50 offset:1288
v_cmp_eq_u32 vcc,3,v14
v_mfma_f32_16x16x32_bf16 v[100:103],a[108:111],a[244:247],v[100:103]
ds_read_b64_tr_b16 a[90:91],v50 offset:3336
v_min_u32 v15,3,v14
v_mfma_f32_16x16x32_bf16 v[116:119],a[108:111],a[252:255],v[116:119]
ds_read_b64_tr_b16 a[92:93],v50 offset:17672
v_add_u32 v15,s65,v15
v_mfma_f32_16x16x32_bf16 v[116:119],a[104:107],a[248:251],v[116:119]
s_add_u32 m0,s88,0
ds_read_b64_tr_b16 a[94:95],v50 offset:19720
v_and_b32 v15,3,v15
v_mfma_f32_16x16x32_bf16 v[72:75],a[112:115],a[224:227],v[72:75]
v_cndmask_b32_e64 v15,v15,4,vcc
v_and_b32 v13,3,v13
v_mfma_f32_16x16x32_bf16 v[72:75],a[116:119],a[228:231],v[72:75]
buffer_load_dwordx4 v40,s[36:39],s83 offen lds
v_mfma_f32_16x16x32_bf16 v[88:91],a[116:119],a[236:239],v[88:91]
ds_read_b64_tr_b16 a[96:97],v50 offset:1536
v_lshl_add_u32 v13,v15,2,v13
v_mfma_f32_16x16x32_bf16 v[88:91],a[112:115],a[232:235],v[88:91]
ds_read_b64_tr_b16 a[98:99],v50 offset:3584
v_mul_u32_u24 v12,1280,v12
v_mfma_f32_16x16x32_bf16 v[104:107],a[112:115],a[240:243],v[104:107]
ds_read_b64_tr_b16 a[100:101],v50 offset:17920
v_lshl_add_u32 v12,v13,4,v12
v_mfma_f32_16x16x32_bf16 v[104:107],a[116:119],a[244:247],v[104:107]
s_add_u32 m0,s88,1024
ds_read_b64_tr_b16 a[102:103],v50 offset:19968
v_add_u32 v12,s66,v12
v_mfma_f32_16x16x32_bf16 v[120:123],a[116:119],a[252:255],v[120:123]
v_subrev_u32 v25,1024,v12
v_add_u32 v11,128,v23
v_mfma_f32_16x16x32_bf16 v[120:123],a[112:115],a[248:251],v[120:123]
buffer_load_dwordx4 v41,s[36:39],s83 offen lds
v_mfma_f32_16x16x32_bf16 v[76:79],a[120:123],a[224:227],v[76:79]
ds_read_b64_tr_b16 a[104:105],v50 offset:1544
v_mul_u32_u24 v12,3277,v11
v_mfma_f32_16x16x32_bf16 v[76:79],a[124:127],a[228:231],v[76:79]
ds_read_b64_tr_b16 a[106:107],v50 offset:3592
v_lshrrev_b32 v12,16,v12
v_mfma_f32_16x16x32_bf16 v[92:95],a[124:127],a[236:239],v[92:95]
ds_read_b64_tr_b16 a[108:109],v50 offset:17928
v_mul_u32_u24 v13,20,v12
v_mfma_f32_16x16x32_bf16 v[92:95],a[120:123],a[232:235],v[92:95]
s_add_u32 m0,s88,2048
ds_read_b64_tr_b16 a[110:111],v50 offset:19976
v_sub_u32 v13,v11,v13
v_mfma_f32_16x16x32_bf16 v[108:111],a[120:123],a[240:243],v[108:111]
v_bfe_u32 v14,v12,2,1
v_lshlrev_b32 v14,1,v14
v_mfma_f32_16x16x32_bf16 v[108:111],a[124:127],a[244:247],v[108:111]
buffer_load_dwordx4 v42,s[36:39],s83 offen lds
v_mfma_f32_16x16x32_bf16 v[124:127],a[124:127],a[252:255],v[124:127]
ds_read_b64_tr_b16 a[112:113],v50 offset:1792
v_xor_b32 v13,v13,v14
v_mfma_f32_16x16x32_bf16 v[124:127],a[120:123],a[248:251],v[124:127]
ds_read_b64_tr_b16 a[114:115],v50 offset:3840
v_lshrrev_b32 v14,2,v13
v_mfma_f32_16x16x32_bf16 v[128:131],a[64:67],a[224:227],v[128:131]
ds_read_b64_tr_b16 a[116:117],v50 offset:18176
v_cmp_eq_u32 vcc,3,v14
v_mfma_f32_16x16x32_bf16 v[128:131],a[68:71],a[228:231],v[128:131]
s_add_u32 m0,s88,3072
ds_read_b64_tr_b16 a[118:119],v50 offset:20224
v_min_u32 v15,3,v14
v_mfma_f32_16x16x32_bf16 v[160:163],a[68:71],a[236:239],v[160:163]
v_add_u32 v15,s65,v15
v_and_b32 v15,3,v15
v_mfma_f32_16x16x32_bf16 v[160:163],a[64:67],a[232:235],v[160:163]
buffer_load_dwordx4 v43,s[36:39],s83 offen lds
v_mfma_f32_16x16x32_bf16 v[192:195],a[64:67],a[240:243],v[192:195]
ds_read_b64_tr_b16 a[120:121],v50 offset:1800
v_cndmask_b32_e64 v15,v15,4,vcc
v_mfma_f32_16x16x32_bf16 v[192:195],a[68:71],a[244:247],v[192:195]
ds_read_b64_tr_b16 a[122:123],v50 offset:3848
v_and_b32 v13,3,v13
v_mfma_f32_16x16x32_bf16 v[224:227],a[68:71],a[252:255],v[224:227]
s_waitcnt lgkmcnt(4)
ds_read_b64_tr_b16 a[124:125],v50 offset:18184
v_lshl_add_u32 v13,v15,2,v13
v_mfma_f32_16x16x32_bf16 v[224:227],a[64:67],a[248:251],v[224:227]
s_add_u32 m0,s88,4096
ds_read_b64_tr_b16 a[126:127],v50 offset:20232
v_add_u32 v50,s86,v51
v_mfma_f32_16x16x32_bf16 v[132:135],a[72:75],a[224:227],v[132:135]
v_mul_u32_u24 v12,1280,v12
v_lshl_add_u32 v12,v13,4,v12
v_mfma_f32_16x16x32_bf16 v[132:135],a[76:79],a[228:231],v[132:135]
buffer_load_dwordx4 v44,s[36:39],s83 offen lds
v_mfma_f32_16x16x32_bf16 v[164:167],a[76:79],a[236:239],v[164:167]
v_add_u32 v12,s66,v12
v_subrev_u32 v26,2048,v12
v_mfma_f32_16x16x32_bf16 v[164:167],a[72:75],a[232:235],v[164:167]
v_add_u32 v11,192,v23
v_mul_u32_u24 v12,3277,v11
v_mfma_f32_16x16x32_bf16 v[196:199],a[72:75],a[240:243],v[196:199]
v_lshrrev_b32 v12,16,v12
v_mul_u32_u24 v13,20,v12
v_mfma_f32_16x16x32_bf16 v[196:199],a[76:79],a[244:247],v[196:199]
s_add_u32 m0,s88,5120
v_sub_u32 v13,v11,v13
v_bfe_u32 v14,v12,2,1
v_mfma_f32_16x16x32_bf16 v[228:231],a[76:79],a[252:255],v[228:231]
v_lshlrev_b32 v14,1,v14
v_xor_b32 v13,v13,v14
v_mfma_f32_16x16x32_bf16 v[228:231],a[72:75],a[248:251],v[228:231]
buffer_load_dwordx4 v45,s[36:39],s83 offen lds
v_mfma_f32_16x16x32_bf16 v[136:139],a[80:83],a[224:227],v[136:139]
v_lshrrev_b32 v14,2,v13
v_cmp_eq_u32 vcc,3,v14
v_mfma_f32_16x16x32_bf16 v[136:139],a[84:87],a[228:231],v[136:139]
v_min_u32 v15,3,v14
v_add_u32 v15,s65,v15
v_mfma_f32_16x16x32_bf16 v[168:171],a[84:87],a[236:239],v[168:171]
v_and_b32 v15,3,v15
v_cndmask_b32_e64 v15,v15,4,vcc
v_mfma_f32_16x16x32_bf16 v[168:171],a[80:83],a[232:235],v[168:171]
s_add_u32 m0,s88,6144
v_and_b32 v13,3,v13
v_lshl_add_u32 v13,v15,2,v13
v_mfma_f32_16x16x32_bf16 v[200:203],a[80:83],a[240:243],v[200:203]
v_mul_u32_u24 v12,1280,v12
v_lshl_add_u32 v12,v13,4,v12
v_mfma_f32_16x16x32_bf16 v[200:203],a[84:87],a[244:247],v[200:203]
buffer_load_dwordx4 v46,s[36:39],s83 offen lds
v_mfma_f32_16x16x32_bf16 v[232:235],a[84:87],a[252:255],v[232:235]
v_add_u32 v12,s66,v12
v_subrev_u32 v27,3072,v12
v_mfma_f32_16x16x32_bf16 v[232:235],a[80:83],a[248:251],v[232:235]
v_add_u32 v11,256,v23
v_mul_u32_u24 v12,3277,v11
)A"
R"A(v_mfma_f32_16x16x32_bf16 v[140:143],a[88:91],a[224:227],v[140:143]
v_lshrrev_b32 v12,16,v12
v_mul_u32_u24 v13,20,v12
v_mfma_f32_16x16x32_bf16 v[140:143],a[92:95],a[228:231],v[140:143]
s_add_u32 m0,s88,7168
v_sub_u32 v13,v11,v13
v_bfe_u32 v14,v12,2,1
v_mfma_f32_16x16x32_bf16 v[172:175],a[92:95],a[236:239],v[172:175]
s_mov_b32 s88,s69
v_lshlrev_b32 v14,1,v14
v_xor_b32 v13,v13,v14
v_mfma_f32_16x16x32_bf16 v[172:175],a[88:91],a[232:235],v[172:175]
buffer_load_dwordx4 v47,s[36:39],s83 offen lds
v_mfma_f32_16x16x32_bf16 v[204:207],a[88:91],a[240:243],v[204:207]
s_waitcnt lgkmcnt(0)
s_add_u32 s83,s83,524288
s_sub_u32 s92,s92,s8
v_mfma_f32_16x16x32_bf16 v[204:207],a[92:95],a[244:247],v[204:207]
s_sub_u32 s56,s56,s4
s_mov_b32 s93,782
s_mul_i32 s92,s92,24
v_mfma_f32_16x16x32_bf16 v[236:239],a[92:95],a[252:255],v[236:239]
s_cmp_ge_u32 s63,2
s_cselect_b32 s93,819,s93
s_mul_i32 s93,s93,24
v_mfma_f32_16x16x32_bf16 v[236:239],a[88:91],a[248:251],v[236:239]
s_add_u32 s10,s20,s74
s_addc_u32 s11,s21,0
s_add_u32 s93,s93,s56
s_waitcnt vmcnt(25)
s_barrier
v_mfma_f32_16x16x32_bf16 v[144:147],a[96:99],a[224:227],v[144:147]
s_add_u32 m0,s76,0
s_add_u32 s92,s92,12600
s_waitcnt vmcnt(17)
ds_read_b128 a[192:195],v48
v_mfma_f32_16x16x32_bf16 v[144:147],a[100:103],a[228:231],v[144:147]
s_and_b32 s69,s75,0xfffc0000
ds_read_b128 a[196:199],v49
v_lshrrev_b32 v14,2,v13
v_mfma_f32_16x16x32_bf16 v[176:179],a[100:103],a[236:239],v[176:179]
global_load_lds_dwordx4 v4,s[10:11] nt
ds_read_b128 a[200:203],v48 offset:2048
v_mfma_f32_16x16x32_bf16 v[176:179],a[96:99],a[232:235],v[176:179]
s_add_u32 s75,s75,16384
ds_read_b128 a[204:207],v49 offset:2048
v_cmp_eq_u32 vcc,3,v14
v_mfma_f32_16x16x32_bf16 v[208:211],a[96:99],a[240:243],v[208:211]
v_min_u32 v15,3,v14
v_add_u32 v15,s65,v15
v_mfma_f32_16x16x32_bf16 v[208:211],a[100:103],a[244:247],v[208:211]
s_mov_b32 s65,262144
ds_read_b128 a[208:211],v48 offset:4096
v_and_b32 v15,3,v15
v_mfma_f32_16x16x32_bf16 v[240:243],a[100:103],a[252:255],v[240:243]
ds_read_b128 a[212:215],v49 offset:4096
v_cndmask_b32_e64 v15,v15,4,vcc
v_mfma_f32_16x16x32_bf16 v[240:243],a[96:99],a[248:251],v[240:243]
ds_read_b128 a[216:219],v48 offset:6144
v_xor_b32 v48,98304,v48
v_mfma_f32_16x16x32_bf16 v[148:151],a[104:107],a[224:227],v[148:151]
v_and_b32 v13,3,v13
v_lshl_add_u32 v13,v15,2,v13
v_mfma_f32_16x16x32_bf16 v[148:151],a[108:111],a[228:231],v[148:151]
ds_read_b128 a[220:223],v49 offset:6144
v_xor_b32 v49,98304,v49
v_mfma_f32_16x16x32_bf16 v[180:183],a[108:111],a[236:239],v[180:183]
ds_read_b64_tr_b16 a[64:65],v50
v_mul_u32_u24 v12,1280,v12
v_mfma_f32_16x16x32_bf16 v[180:183],a[104:107],a[232:235],v[180:183]
ds_read_b64_tr_b16 a[66:67],v50 offset:2048
v_lshl_add_u32 v12,v13,4,v12
v_mfma_f32_16x16x32_bf16 v[212:215],a[104:107],a[240:243],v[212:215]
v_add_u32 v12,s66,v12
v_subrev_u32 v7,0,v12
v_mfma_f32_16x16x32_bf16 v[212:215],a[108:111],a[244:247],v[212:215]
ds_read_b64_tr_b16 a[68:69],v50 offset:16384
v_mfma_f32_16x16x32_bf16 v[244:247],a[108:111],a[252:255],v[244:247]
ds_read_b64_tr_b16 a[70:71],v50 offset:18432
v_mfma_f32_16x16x32_bf16 v[244:247],a[104:107],a[248:251],v[244:247]
ds_read_b64_tr_b16 a[72:73],v50 offset:8
v_mfma_f32_16x16x32_bf16 v[152:155],a[112:115],a[224:227],v[152:155]
v_mfma_f32_16x16x32_bf16 v[152:155],a[116:119],a[228:231],v[152:155]
ds_read_b64_tr_b16 a[74:75],v50 offset:2056
v_mfma_f32_16x16x32_bf16 v[184:187],a[116:119],a[236:239],v[184:187]
global_load_lds_dwordx4 v4,s[10:11] offset:1024 nt
ds_read_b64_tr_b16 a[76:77],v50 offset:16392
v_mfma_f32_16x16x32_bf16 v[184:187],a[112:115],a[232:235],v[184:187]
ds_read_b64_tr_b16 a[78:79],v50 offset:18440
v_mfma_f32_16x16x32_bf16 v[216:219],a[112:115],a[240:243],v[216:219]
v_mfma_f32_16x16x32_bf16 v[216:219],a[116:119],a[244:247],v[216:219]
ds_read_b64_tr_b16 a[80:81],v50 offset:1024
v_mfma_f32_16x16x32_bf16 v[248:251],a[116:119],a[252:255],v[248:251]
ds_read_b64_tr_b16 a[82:83],v50 offset:3072
v_mfma_f32_16x16x32_bf16 v[248:251],a[112:115],a[248:251],v[248:251]
ds_read_b64_tr_b16 a[84:85],v50 offset:17408
v_mfma_f32_16x16x32_bf16 v[156:159],a[120:123],a[224:227],v[156:159]
v_mfma_f32_16x16x32_bf16 v[156:159],a[124:127],a[228:231],v[156:159]
ds_read_b64_tr_b16 a[86:87],v50 offset:19456
v_mfma_f32_16x16x32_bf16 v[188:191],a[124:127],a[236:239],v[188:191]
ds_read_b64_tr_b16 a[88:89],v50 offset:1032
v_mfma_f32_16x16x32_bf16 v[188:191],a[120:123],a[232:235],v[188:191]
s_cmp_lt_u32 s92,s93
s_cselect_b32 s5,6,0
s_cselect_b32 s7,3,0
s_cselect_b32 s9,8,10
ds_read_b64_tr_b16 a[90:91],v50 offset:3080
v_mfma_f32_16x16x32_bf16 v[220:223],a[120:123],a[240:243],v[220:223]
s_sub_u32 s93,s93,s92
s_max_i32 s93,s93,0
s_min_u32 s93,s93,65535
v_mfma_f32_16x16x32_bf16 v[220:223],a[124:127],a[244:247],v[220:223]
s_mul_i32 s93,s93,195
s_lshr_b32 s93,s93,16
ds_read_b64_tr_b16 a[92:93],v50 offset:17416
v_mfma_f32_16x16x32_bf16 v[252:255],a[124:127],a[252:255],v[252:255]
s_add_u32 s93,s93,1
s_max_i32 s93,s93,1
ds_read_b64_tr_b16 a[94:95],v50 offset:19464
v_mfma_f32_16x16x32_bf16 v[252:255],a[120:123],a[248:251],v[252:255]
s_min_u32 s93,s93,6
s_waitcnt vmcnt(10)
ds_read_b128 a[224:227],v48
s_waitcnt lgkmcnt(6)
v_mfma_f32_16x16x32_bf16 v[128:131],a[64:67],a[192:195],v[128:131]
s_cmp_lg_u32 s7,0
s_cselect_b32 s7,s93,0
s_add_u32 s93,s93,3
v_mfma_f32_16x16x32_bf16 v[128:131],a[68:71],a[196:199],v[128:131]
s_min_u32 s93,s93,7
ds_read_b128 a[228:231],v49
v_mfma_f32_16x16x32_bf16 v[160:163],a[68:71],a[204:207],v[160:163]
global_load_lds_dwordx4 v5,s[10:11] offset:2048 nt
ds_read_b128 a[236:239],v49 offset:2048
v_mfma_f32_16x16x32_bf16 v[160:163],a[64:67],a[200:203],v[160:163]
s_cmp_lg_u32 s7,0
s_cselect_b32 s5,s93,0
ds_read_b128 a[232:235],v48 offset:2048
v_mfma_f32_16x16x32_bf16 v[192:195],a[64:67],a[208:211],v[192:195]
s_mov_b32 s4,s5
v_mfma_f32_16x16x32_bf16 v[192:195],a[68:71],a[212:215],v[192:195]
ds_read_b128 a[240:243],v48 offset:4096
v_mfma_f32_16x16x32_bf16 v[224:227],a[68:71],a[220:223],v[224:227]
ds_read_b128 a[244:247],v49 offset:4096
v_mfma_f32_16x16x32_bf16 v[224:227],a[64:67],a[216:219],v[224:227]
ds_read_b128 a[252:255],v49 offset:6144
v_mfma_f32_16x16x32_bf16 v[132:135],a[72:75],a[192:195],v[132:135]
v_mfma_f32_16x16x32_bf16 v[132:135],a[76:79],a[196:199],v[132:135]
ds_read_b128 a[248:251],v48 offset:6144
v_mfma_f32_16x16x32_bf16 v[164:167],a[76:79],a[204:207],v[164:167]
ds_read_b64_tr_b16 a[96:97],v50 offset:256
v_mfma_f32_16x16x32_bf16 v[164:167],a[72:75],a[200:203],v[164:167]
ds_read_b64_tr_b16 a[98:99],v50 offset:2304
v_mfma_f32_16x16x32_bf16 v[196:199],a[72:75],a[208:211],v[196:199]
v_mfma_f32_16x16x32_bf16 v[196:199],a[76:79],a[212:215],v[196:199]
ds_read_b64_tr_b16 a[100:101],v50 offset:16640
v_mfma_f32_16x16x32_bf16 v[228:231],a[76:79],a[220:223],v[228:231]
ds_read_b64_tr_b16 a[102:103],v50 offset:18688
v_mfma_f32_16x16x32_bf16 v[228:231],a[72:75],a[216:219],v[228:231]
s_waitcnt lgkmcnt(5)
ds_read_b64_tr_b16 a[104:105],v50 offset:1280
v_mfma_f32_16x16x32_bf16 v[128:131],a[80:83],a[224:227],v[128:131]
v_mfma_f32_16x16x32_bf16 v[128:131],a[84:87],a[228:231],v[128:131]
ds_read_b64_tr_b16 a[106:107],v50 offset:3328
v_mfma_f32_16x16x32_bf16 v[160:163],a[84:87],a[236:239],v[160:163]
global_load_lds_dwordx4 v5,s[10:11] offset:3072
ds_read_b64_tr_b16 a[108:109],v50 offset:17664
v_mfma_f32_16x16x32_bf16 v[160:163],a[80:83],a[232:235],v[160:163]
ds_read_b64_tr_b16 a[110:111],v50 offset:19712
v_mfma_f32_16x16x32_bf16 v[192:195],a[80:83],a[240:243],v[192:195]
v_mfma_f32_16x16x32_bf16 v[192:195],a[84:87],a[244:247],v[192:195]
ds_read_b64_tr_b16 a[112:113],v50 offset:264
v_mul_f32 v128,s71,v128
v_mfma_f32_16x16x32_bf16 v[224:227],a[84:87],a[252:255],v[224:227]
s_waitcnt lgkmcnt(5)
ds_read_b64_tr_b16 a[114:115],v50 offset:2312
v_mul_f32 v129,s71,v129
v_mfma_f32_16x16x32_bf16 v[224:227],a[80:83],a[248:251],v[224:227]
ds_read_b64_tr_b16 a[116:117],v50 offset:16648
v_mul_f32 v130,s71,v130
v_mfma_f32_16x16x32_bf16 v[132:135],a[88:91],a[224:227],v[132:135]
v_mul_f32 v131,s71,v131
v_cvt_pk_bf16_f32 v128,v128,v129
v_mfma_f32_16x16x32_bf16 v[132:135],a[92:95],a[228:231],v[132:135]
ds_read_b64_tr_b16 a[118:119],v50 offset:18696
v_cvt_pk_bf16_f32 v129,v130,v131
v_mfma_f32_16x16x32_bf16 v[164:167],a[92:95],a[236:239],v[164:167]
ds_read_b64_tr_b16 a[120:121],v50 offset:1288
v_mul_f32 v192,s71,v192
v_mfma_f32_16x16x32_bf16 v[164:167],a[88:91],a[232:235],v[164:167]
ds_read_b64_tr_b16 a[122:123],v50 offset:3336
v_mul_f32 v193,s71,v193
v_mfma_f32_16x16x32_bf16 v[196:199],a[88:91],a[240:243],v[196:199]
v_mul_f32 v132,s71,v132
v_mul_f32 v133,s71,v133
v_mfma_f32_16x16x32_bf16 v[196:199],a[92:95],a[244:247],v[196:199]
ds_read_b64_tr_b16 a[124:125],v50 offset:17672
v_mul_f32 v134,s71,v134
v_mfma_f32_16x16x32_bf16 v[228:231],a[92:95],a[252:255],v[228:231]
s_add_u32 s10,s24,s74
s_addc_u32 s11,s25,0
ds_read_b64_tr_b16 a[126:127],v50 offset:19720
v_mfma_f32_16x16x32_bf16 v[228:231],a[88:91],a[248:251],v[228:231]
s_add_u32 s74,s74,16384
s_and_b32 s74,s74,0x3ffff
ds_read_b64_tr_b16 a[128:129],v50 offset:512
v_mfma_f32_16x16x32_bf16 v[136:139],a[96:99],a[192:195],v[136:139]
s_add_u32 m0,s76,16384
s_or_b32 s74,s74,s69
s_sub_u32 s76,s76,32768
v_mfma_f32_16x16x32_bf16 v[136:139],a[100:103],a[196:199],v[136:139]
s_and_b32 s76,s76,0x1ffff
s_and_b32 s69,s75,0xfffc0000
ds_read_b64_tr_b16 a[130:131],v50 offset:2560
s_waitcnt lgkmcnt(6)
v_mfma_f32_16x16x32_bf16 v[136:139],a[104:107],a[224:227],v[136:139]
global_load_lds_dwordx4 v4,s[10:11]
ds_read_b64_tr_b16 a[132:133],v50 offset:16896
v_mfma_f32_16x16x32_bf16 v[136:139],a[108:111],a[228:231],v[136:139]
s_add_u32 s75,s75,16384
ds_read_b64_tr_b16 a[134:135],v50 offset:18944
v_mul_f32 v135,s71,v135
v_mfma_f32_16x16x32_bf16 v[168:171],a[108:111],a[236:239],v[168:171]
v_cvt_pk_bf16_f32 v130,v132,v133
v_cvt_pk_bf16_f32 v131,v134,v135
v_mfma_f32_16x16x32_bf16 v[168:171],a[104:107],a[232:235],v[168:171]
ds_read_b64_tr_b16 a[136:137],v50 offset:1536
v_mul_f32 v194,s71,v194
v_mfma_f32_16x16x32_bf16 v[168:171],a[100:103],a[204:207],v[168:171]
ds_read_b64_tr_b16 a[138:139],v50 offset:3584
v_mul_f32 v195,s71,v195
v_mfma_f32_16x16x32_bf16 v[168:171],a[96:99],a[200:203],v[168:171]
ds_read_b64_tr_b16 a[140:141],v50 offset:17920
v_mul_f32 v196,s71,v196
v_mfma_f32_16x16x32_bf16 v[200:203],a[96:99],a[208:211],v[200:203]
v_mul_f32 v197,s71,v197
v_mul_f32 v198,s71,v198
v_mfma_f32_16x16x32_bf16 v[200:203],a[100:103],a[212:215],v[200:203]
ds_read_b64_tr_b16 a[142:143],v50 offset:19968
v_mul_f32 v199,s71,v199
v_mfma_f32_16x16x32_bf16 v[200:203],a[104:107],a[240:243],v[200:203]
ds_read_b64_tr_b16 a[144:145],v50 offset:520
v_cvt_pk_bf16_f32 v192,v192,v193
v_mfma_f32_16x16x32_bf16 v[200:203],a[108:111],a[244:247],v[200:203]
ds_read_b64_tr_b16 a[146:147],v50 offset:2568
v_cvt_pk_bf16_f32 v193,v194,v195
v_mfma_f32_16x16x32_bf16 v[232:235],a[108:111],a[252:255],v[232:235]
v_cvt_pk_bf16_f32 v194,v196,v197
v_cvt_pk_bf16_f32 v195,v198,v199
v_mfma_f32_16x16x32_bf16 v[232:235],a[104:107],a[248:251],v[232:235]
ds_read_b64_tr_b16 a[148:149],v50 offset:16904
v_mul_f32 v136,s71,v136
v_mfma_f32_16x16x32_bf16 v[232:235],a[100:103],a[220:223],v[232:235]
ds_read_b64_tr_b16 a[150:151],v50 offset:18952
v_mul_f32 v137,s71,v137
v_mfma_f32_16x16x32_bf16 v[232:235],a[96:99],a[216:219],v[232:235]
ds_read_b64_tr_b16 a[152:153],v50 offset:1544
v_mul_f32 v138,s71,v138
v_mfma_f32_16x16x32_bf16 v[140:143],a[112:115],a[192:195],v[140:143]
v_mul_f32 v139,s71,v139
v_cvt_pk_bf16_f32 v136,v136,v137
v_mfma_f32_16x16x32_bf16 v[140:143],a[116:119],a[196:199],v[140:143]
s_waitcnt lgkmcnt(5)
ds_read_b64_tr_b16 a[154:155],v50 offset:3592
v_cvt_pk_bf16_f32 v137,v138,v139
v_mfma_f32_16x16x32_bf16 v[140:143],a[120:123],a[224:227],v[140:143]
global_load_lds_dwordx4 v5,s[10:11] offset:1024
ds_read_b64_tr_b16 a[156:157],v50 offset:17928
v_mfma_f32_16x16x32_bf16 v[140:143],a[124:127],a[228:231],v[140:143]
ds_read_b64_tr_b16 a[158:159],v50 offset:19976
v_mul_f32 v160,s71,v160
v_mfma_f32_16x16x32_bf16 v[172:175],a[124:127],a[236:239],v[172:175]
v_mul_f32 v161,s71,v161
v_mul_f32 v162,s71,v162
v_mfma_f32_16x16x32_bf16 v[172:175],a[120:123],a[232:235],v[172:175]
)A"
R"A(ds_read_b64_tr_b16 a[160:161],v50 offset:768
v_mul_f32 v163,s71,v163
v_mfma_f32_16x16x32_bf16 v[172:175],a[116:119],a[204:207],v[172:175]
ds_read_b64_tr_b16 a[162:163],v50 offset:2816
v_mul_f32 v140,s71,v140
v_mfma_f32_16x16x32_bf16 v[172:175],a[112:115],a[200:203],v[172:175]
ds_read_b64_tr_b16 a[164:165],v50 offset:17152
v_mul_f32 v141,s71,v141
v_mfma_f32_16x16x32_bf16 v[204:207],a[112:115],a[208:211],v[204:207]
v_mul_f32 v142,s71,v142
v_mul_f32 v143,s71,v143
v_mfma_f32_16x16x32_bf16 v[204:207],a[116:119],a[212:215],v[204:207]
ds_read_b64_tr_b16 a[166:167],v50 offset:19200
v_cvt_pk_bf16_f32 v138,v140,v141
v_mfma_f32_16x16x32_bf16 v[204:207],a[120:123],a[240:243],v[204:207]
ds_read_b64_tr_b16 a[168:169],v50 offset:1792
v_cvt_pk_bf16_f32 v139,v142,v143
v_mfma_f32_16x16x32_bf16 v[204:207],a[124:127],a[244:247],v[204:207]
ds_read_b64_tr_b16 a[170:171],v50 offset:3840
v_mul_f32 v164,s71,v164
v_mfma_f32_16x16x32_bf16 v[236:239],a[124:127],a[252:255],v[236:239]
v_mul_f32 v165,s71,v165
v_mul_f32 v166,s71,v166
v_mfma_f32_16x16x32_bf16 v[236:239],a[120:123],a[248:251],v[236:239]
ds_read_b64_tr_b16 a[172:173],v50 offset:18176
v_mul_f32 v167,s71,v167
v_mfma_f32_16x16x32_bf16 v[236:239],a[116:119],a[220:223],v[236:239]
ds_read_b64_tr_b16 a[174:175],v50 offset:20224
v_cvt_pk_bf16_f32 v160,v160,v161
v_mfma_f32_16x16x32_bf16 v[236:239],a[112:115],a[216:219],v[236:239]
ds_read_b64_tr_b16 a[176:177],v50 offset:776
v_cvt_pk_bf16_f32 v161,v162,v163
v_mfma_f32_16x16x32_bf16 v[144:147],a[128:131],a[192:195],v[144:147]
v_cvt_pk_bf16_f32 v162,v164,v165
v_cvt_pk_bf16_f32 v163,v166,v167
v_mfma_f32_16x16x32_bf16 v[144:147],a[132:135],a[196:199],v[144:147]
ds_read_b64_tr_b16 a[178:179],v50 offset:2824
v_mul_f32 v224,s71,v224
v_mfma_f32_16x16x32_bf16 v[144:147],a[136:139],a[224:227],v[144:147]
global_load_lds_dwordx4 v4,s[10:11] offset:2048
ds_read_b64_tr_b16 a[180:181],v50 offset:17160
v_mfma_f32_16x16x32_bf16 v[144:147],a[140:143],a[228:231],v[144:147]
ds_read_b64_tr_b16 a[182:183],v50 offset:19208
v_mul_f32 v225,s71,v225
v_mfma_f32_16x16x32_bf16 v[176:179],a[140:143],a[236:239],v[176:179]
v_mul_f32 v226,s71,v226
v_mul_f32 v227,s71,v227
v_mfma_f32_16x16x32_bf16 v[176:179],a[136:139],a[232:235],v[176:179]
ds_read_b64_tr_b16 a[184:185],v50 offset:1800
v_mul_f32 v228,s71,v228
v_mfma_f32_16x16x32_bf16 v[176:179],a[132:135],a[204:207],v[176:179]
ds_read_b64_tr_b16 a[186:187],v50 offset:3848
v_mul_f32 v144,s71,v144
v_mfma_f32_16x16x32_bf16 v[176:179],a[128:131],a[200:203],v[176:179]
ds_read_b64_tr_b16 a[188:189],v50 offset:18184
v_mul_f32 v145,s71,v145
v_mfma_f32_16x16x32_bf16 v[208:211],a[128:131],a[208:211],v[208:211]
v_mul_f32 v146,s71,v146
v_mul_f32 v147,s71,v147
v_mfma_f32_16x16x32_bf16 v[208:211],a[132:135],a[212:215],v[208:211]
ds_read_b64_tr_b16 a[190:191],v50 offset:20232
v_cvt_pk_bf16_f32 v144,v144,v145
v_mfma_f32_16x16x32_bf16 v[208:211],a[136:139],a[240:243],v[208:211]
v_cvt_pk_bf16_f32 v145,v146,v147
v_mul_f32 v229,s71,v229
v_mfma_f32_16x16x32_bf16 v[208:211],a[140:143],a[244:247],v[208:211]
v_mul_f32 v230,s71,v230
v_mul_f32 v231,s71,v231
v_mfma_f32_16x16x32_bf16 v[240:243],a[140:143],a[252:255],v[240:243]
v_cvt_pk_bf16_f32 v224,v224,v225
v_cvt_pk_bf16_f32 v225,v226,v227
v_mfma_f32_16x16x32_bf16 v[240:243],a[136:139],a[248:251],v[240:243]
v_cvt_pk_bf16_f32 v226,v228,v229
v_cvt_pk_bf16_f32 v227,v230,v231
v_mfma_f32_16x16x32_bf16 v[240:243],a[132:135],a[220:223],v[240:243]
v_mul_f32 v168,s71,v168
L_pd3_%=:
s_cbranch_execz L_pd7_%=
v_mul_f32 v169,s71,v169
v_mfma_f32_16x16x32_bf16 v[240:243],a[128:131],a[216:219],v[240:243]
v_mul_f32 v170,s71,v170
v_mul_f32 v171,s71,v171
s_waitcnt vmcnt(7)
s_barrier
s_waitcnt lgkmcnt(1)
v_mfma_f32_16x16x32_bf16 v[148:151],a[144:147],a[192:195],v[148:151]
ds_read_b64_tr_b16 a[64:65],v52
v_mul_f32 v172,s71,v172
v_mfma_f32_16x16x32_bf16 v[148:151],a[148:151],a[196:199],v[148:151]
global_load_lds_dwordx4 v5,s[10:11] offset:3072
ds_read_b64_tr_b16 a[66:67],v52 offset:2048
v_mfma_f32_16x16x32_bf16 v[148:151],a[152:155],a[224:227],v[148:151]
ds_read_b64_tr_b16 a[68:69],v52 offset:16384
v_mul_f32 v173,s71,v173
v_mfma_f32_16x16x32_bf16 v[148:151],a[156:159],a[228:231],v[148:151]
ds_read_b64_tr_b16 a[70:71],v52 offset:18432
v_mul_f32 v174,s71,v174
v_mfma_f32_16x16x32_bf16 v[180:183],a[156:159],a[236:239],v[180:183]
v_mul_f32 v175,s71,v175
v_cvt_pk_bf16_f32 v168,v168,v169
v_mfma_f32_16x16x32_bf16 v[180:183],a[152:155],a[232:235],v[180:183]
ds_read_b64_tr_b16 a[72:73],v52 offset:1024
v_cvt_pk_bf16_f32 v169,v170,v171
v_mfma_f32_16x16x32_bf16 v[180:183],a[148:151],a[204:207],v[180:183]
ds_read_b64_tr_b16 a[74:75],v52 offset:3072
v_mul_f32 v148,s71,v148
v_mfma_f32_16x16x32_bf16 v[180:183],a[144:147],a[200:203],v[180:183]
ds_read_b64_tr_b16 a[76:77],v52 offset:17408
v_mul_f32 v149,s71,v149
v_mfma_f32_16x16x32_bf16 v[212:215],a[144:147],a[208:211],v[212:215]
v_mul_f32 v150,s71,v150
v_mul_f32 v151,s71,v151
v_mfma_f32_16x16x32_bf16 v[212:215],a[148:151],a[212:215],v[212:215]
ds_read_b64_tr_b16 a[78:79],v52 offset:19456
v_cvt_pk_bf16_f32 v146,v148,v149
v_mfma_f32_16x16x32_bf16 v[212:215],a[152:155],a[240:243],v[212:215]
ds_read_b64_tr_b16 a[80:81],v52 offset:8
v_cvt_pk_bf16_f32 v147,v150,v151
v_mfma_f32_16x16x32_bf16 v[212:215],a[156:159],a[244:247],v[212:215]
ds_read_b64_tr_b16 a[82:83],v52 offset:2056
v_cvt_pk_bf16_f32 v170,v172,v173
v_mfma_f32_16x16x32_bf16 v[244:247],a[156:159],a[252:255],v[244:247]
v_cvt_pk_bf16_f32 v171,v174,v175
v_mul_f32 v176,s71,v176
v_mfma_f32_16x16x32_bf16 v[244:247],a[152:155],a[248:251],v[244:247]
ds_read_b64_tr_b16 a[84:85],v52 offset:16392
v_mul_f32 v177,s71,v177
v_mfma_f32_16x16x32_bf16 v[244:247],a[148:151],a[220:223],v[244:247]
s_add_u32 s10,s20,s74
s_addc_u32 s11,s21,0
ds_read_b64_tr_b16 a[86:87],v52 offset:18440
v_mfma_f32_16x16x32_bf16 v[244:247],a[144:147],a[216:219],v[244:247]
ds_read_b64_tr_b16 a[88:89],v52 offset:1032
v_mul_f32 v178,s71,v178
v_mfma_f32_16x16x32_bf16 v[152:155],a[160:163],a[192:195],v[152:155]
s_add_u32 m0,s76,0
v_mul_f32 v179,s71,v179
v_mul_f32 v180,s71,v180
v_mfma_f32_16x16x32_bf16 v[152:155],a[164:167],a[196:199],v[152:155]
ds_read_b64_tr_b16 a[90:91],v52 offset:3080
v_mul_f32 v181,s71,v181
v_mfma_f32_16x16x32_bf16 v[152:155],a[168:171],a[224:227],v[152:155]
global_load_lds_dwordx4 v4,s[10:11] nt
ds_read_b64_tr_b16 a[92:93],v52 offset:17416
v_mfma_f32_16x16x32_bf16 v[152:155],a[172:175],a[228:231],v[152:155]
ds_read_b64_tr_b16 a[94:95],v52 offset:19464
v_mul_f32 v182,s71,v182
v_mfma_f32_16x16x32_bf16 v[184:187],a[172:175],a[236:239],v[184:187]
v_mul_f32 v183,s71,v183
v_cvt_pk_bf16_f32 v176,v176,v177
v_mfma_f32_16x16x32_bf16 v[184:187],a[168:171],a[232:235],v[184:187]
ds_read_b64_tr_b16 a[96:97],v52 offset:256
v_cvt_pk_bf16_f32 v177,v178,v179
v_mfma_f32_16x16x32_bf16 v[184:187],a[164:167],a[204:207],v[184:187]
ds_read_b64_tr_b16 a[98:99],v52 offset:2304
v_mul_f32 v152,s71,v152
v_mfma_f32_16x16x32_bf16 v[184:187],a[160:163],a[200:203],v[184:187]
ds_read_b64_tr_b16 a[100:101],v52 offset:16640
v_mul_f32 v153,s71,v153
v_mfma_f32_16x16x32_bf16 v[216:219],a[160:163],a[208:211],v[216:219]
v_mul_f32 v154,s71,v154
v_mul_f32 v155,s71,v155
v_mfma_f32_16x16x32_bf16 v[216:219],a[164:167],a[212:215],v[216:219]
ds_read_b64_tr_b16 a[102:103],v52 offset:18688
v_cvt_pk_bf16_f32 v152,v152,v153
v_mfma_f32_16x16x32_bf16 v[216:219],a[168:171],a[240:243],v[216:219]
ds_read_b64_tr_b16 a[104:105],v52 offset:1280
v_cvt_pk_bf16_f32 v153,v154,v155
v_mfma_f32_16x16x32_bf16 v[216:219],a[172:175],a[244:247],v[216:219]
ds_read_b64_tr_b16 a[106:107],v52 offset:3328
v_cvt_pk_bf16_f32 v178,v180,v181
v_mfma_f32_16x16x32_bf16 v[248:251],a[172:175],a[252:255],v[248:251]
v_cvt_pk_bf16_f32 v179,v182,v183
v_mul_f32 v184,s71,v184
v_mfma_f32_16x16x32_bf16 v[248:251],a[168:171],a[248:251],v[248:251]
ds_read_b64_tr_b16 a[108:109],v52 offset:17664
v_mul_f32 v185,s71,v185
v_mfma_f32_16x16x32_bf16 v[248:251],a[164:167],a[220:223],v[248:251]
ds_read_b64_tr_b16 a[110:111],v52 offset:19712
v_mul_f32 v186,s71,v186
v_mfma_f32_16x16x32_bf16 v[248:251],a[160:163],a[216:219],v[248:251]
ds_read_b64_tr_b16 a[112:113],v52 offset:264
v_mul_f32 v187,s71,v187
v_mfma_f32_16x16x32_bf16 v[156:159],a[176:179],a[192:195],v[156:159]
v_cvt_pk_bf16_f32 v184,v184,v185
v_cvt_pk_bf16_f32 v185,v186,v187
v_mfma_f32_16x16x32_bf16 v[156:159],a[180:183],a[196:199],v[156:159]
global_load_lds_dwordx4 v4,s[10:11] offset:1024 nt
ds_read_b64_tr_b16 a[114:115],v52 offset:2312
v_mfma_f32_16x16x32_bf16 v[156:159],a[184:187],a[224:227],v[156:159]
s_waitcnt lgkmcnt(5)
ds_read_b64_tr_b16 a[116:117],v52 offset:16648
v_mul_f32 v200,s71,v200
v_mfma_f32_16x16x32_bf16 v[156:159],a[188:191],a[228:231],v[156:159]
ds_read_b64_tr_b16 a[118:119],v52 offset:18696
v_mul_f32 v201,s71,v201
v_mfma_f32_16x16x32_bf16 v[188:191],a[188:191],a[236:239],v[188:191]
v_mul_f32 v202,s71,v202
v_mul_f32 v203,s71,v203
v_mfma_f32_16x16x32_bf16 v[188:191],a[184:187],a[232:235],v[188:191]
ds_read_b64_tr_b16 a[120:121],v52 offset:1288
v_mul_f32 v204,s71,v204
v_mfma_f32_16x16x32_bf16 v[188:191],a[180:183],a[204:207],v[188:191]
ds_read_b64_tr_b16 a[122:123],v52 offset:3336
v_mul_f32 v156,s71,v156
v_mfma_f32_16x16x32_bf16 v[188:191],a[176:179],a[200:203],v[188:191]
ds_read_b64_tr_b16 a[124:125],v52 offset:17672
v_mul_f32 v157,s71,v157
v_mfma_f32_16x16x32_bf16 v[220:223],a[176:179],a[208:211],v[220:223]
v_mul_f32 v158,s71,v158
v_mul_f32 v159,s71,v159
v_mfma_f32_16x16x32_bf16 v[220:223],a[180:183],a[212:215],v[220:223]
ds_read_b64_tr_b16 a[126:127],v52 offset:19720
v_cvt_pk_bf16_f32 v154,v156,v157
v_mfma_f32_16x16x32_bf16 v[220:223],a[184:187],a[240:243],v[220:223]
v_cvt_pk_bf16_f32 v155,v158,v159
v_mul_f32 v188,s71,v188
v_mfma_f32_16x16x32_bf16 v[220:223],a[188:191],a[244:247],v[220:223]
v_mul_f32 v189,s71,v189
v_mul_f32 v190,s71,v190
v_mfma_f32_16x16x32_bf16 v[252:255],a[188:191],a[252:255],v[252:255]
v_mul_f32 v191,s71,v191
v_cvt_pk_bf16_f32 v186,v188,v189
v_mfma_f32_16x16x32_bf16 v[252:255],a[184:187],a[248:251],v[252:255]
v_cvt_pk_bf16_f32 v187,v190,v191
v_mul_f32 v205,s71,v205
v_mfma_f32_16x16x32_bf16 v[252:255],a[180:183],a[220:223],v[252:255]
v_mul_f32 v206,s71,v206
v_mul_f32 v207,s71,v207
v_mfma_f32_16x16x32_bf16 v[252:255],a[176:179],a[216:219],v[252:255]
v_cvt_pk_bf16_f32 v200,v200,v201
v_cvt_pk_bf16_f32 v201,v202,v203
v_mfma_f32_16x16x32_bf16 a[0:3],a[64:67],a[192:195],a[0:3]
global_load_lds_dwordx4 v5,s[10:11] offset:2048 nt
v_cvt_pk_bf16_f32 v202,v204,v205
v_mfma_f32_16x16x32_bf16 a[0:3],a[68:71],a[196:199],a[0:3]
v_cvt_pk_bf16_f32 v203,v206,v207
v_mul_f32 v208,s71,v208
v_mfma_f32_16x16x32_bf16 a[0:3],a[72:75],a[224:227],a[0:3]
v_mul_f32 v209,s71,v209
v_mul_f32 v210,s71,v210
v_mfma_f32_16x16x32_bf16 a[0:3],a[76:79],a[228:231],a[0:3]
v_mul_f32 v211,s71,v211
v_mul_f32 v212,s71,v212
v_mfma_f32_16x16x32_bf16 a[16:19],a[76:79],a[236:239],a[16:19]
v_mul_f32 v213,s71,v213
v_mul_f32 v214,s71,v214
v_mfma_f32_16x16x32_bf16 a[16:19],a[72:75],a[232:235],a[16:19]
v_mul_f32 v215,s71,v215
v_cvt_pk_bf16_f32 v208,v208,v209
v_mfma_f32_16x16x32_bf16 a[16:19],a[68:71],a[204:207],a[16:19]
v_accvgpr_read_b32 v8,a0
v_accvgpr_read_b32 v9,a1
v_mfma_f32_16x16x32_bf16 a[16:19],a[64:67],a[200:203],a[16:19]
v_accvgpr_read_b32 v10,a2
v_accvgpr_read_b32 v11,a3
v_mfma_f32_16x16x32_bf16 a[32:35],a[64:67],a[208:211],a[32:35]
v_mul_f32 v8,s71,v8
v_mul_f32 v9,s71,v9
v_mfma_f32_16x16x32_bf16 a[32:35],a[68:71],a[212:215],a[32:35]
v_mul_f32 v10,s71,v10
v_mul_f32 v11,s71,v11
v_mfma_f32_16x16x32_bf16 a[32:35],a[72:75],a[240:243],a[32:35]
v_cvt_pk_bf16_f32 v132,v8,v9
v_cvt_pk_bf16_f32 v133,v10,v11
v_mfma_f32_16x16x32_bf16 a[32:35],a[76:79],a[244:247],a[32:35]
v_accvgpr_read_b32 v16,a16
v_accvgpr_read_b32 v17,a17
v_mfma_f32_16x16x32_bf16 a[48:51],a[76:79],a[252:255],a[48:51]
ds_read_b64_tr_b16 a[76:77],v52 offset:17920
v_accvgpr_read_b32 v18,a18
v_mfma_f32_16x16x32_bf16 a[48:51],a[72:75],a[248:251],a[48:51]
ds_read_b64_tr_b16 a[72:73],v52 offset:1536
v_accvgpr_read_b32 v19,a19
v_mfma_f32_16x16x32_bf16 a[48:51],a[68:71],a[220:223],a[48:51]
ds_read_b64_tr_b16 a[68:69],v52 offset:16896
)A"
R"A(v_accvgpr_read_b32 v8,a32
v_mfma_f32_16x16x32_bf16 a[48:51],a[64:67],a[216:219],a[48:51]
global_load_lds_dwordx4 v5,s[10:11] offset:3072
ds_read_b64_tr_b16 a[64:65],v52 offset:512
v_mfma_f32_16x16x32_bf16 a[4:7],a[80:83],a[192:195],a[4:7]
v_accvgpr_read_b32 v9,a33
v_accvgpr_read_b32 v10,a34
v_mfma_f32_16x16x32_bf16 a[4:7],a[84:87],a[196:199],a[4:7]
ds_read_b64_tr_b16 a[66:67],v52 offset:2560
v_accvgpr_read_b32 v11,a35
v_mfma_f32_16x16x32_bf16 a[4:7],a[88:91],a[224:227],a[4:7]
ds_read_b64_tr_b16 a[70:71],v52 offset:18944
v_mul_f32 v8,s71,v8
v_mfma_f32_16x16x32_bf16 a[4:7],a[92:95],a[228:231],a[4:7]
ds_read_b64_tr_b16 a[74:75],v52 offset:3584
v_mul_f32 v9,s71,v9
v_mfma_f32_16x16x32_bf16 a[20:23],a[92:95],a[236:239],a[20:23]
v_mul_f32 v10,s71,v10
v_mul_f32 v11,s71,v11
v_mfma_f32_16x16x32_bf16 a[20:23],a[88:91],a[232:235],a[20:23]
ds_read_b64_tr_b16 a[78:79],v52 offset:19968
v_cvt_pk_bf16_f32 v196,v8,v9
v_mfma_f32_16x16x32_bf16 a[20:23],a[84:87],a[204:207],a[20:23]
v_accvgpr_read_b32 v12,a4
v_accvgpr_read_b32 v13,a5
v_mfma_f32_16x16x32_bf16 a[20:23],a[80:83],a[200:203],a[20:23]
v_accvgpr_read_b32 v14,a6
v_accvgpr_read_b32 v15,a7
v_mfma_f32_16x16x32_bf16 a[36:39],a[80:83],a[208:211],a[36:39]
v_mul_f32 v12,s71,v12
v_mul_f32 v13,s71,v13
v_mfma_f32_16x16x32_bf16 a[36:39],a[84:87],a[212:215],a[36:39]
v_mul_f32 v14,s71,v14
v_mul_f32 v15,s71,v15
v_mfma_f32_16x16x32_bf16 a[36:39],a[88:91],a[240:243],a[36:39]
s_add_u32 s10,s24,s74
s_addc_u32 s11,s25,0
s_add_u32 s74,s74,16384
v_mfma_f32_16x16x32_bf16 a[36:39],a[92:95],a[244:247],a[36:39]
s_and_b32 s74,s74,0x3ffff
s_or_b32 s74,s74,s69
v_cvt_pk_bf16_f32 v134,v12,v13
v_mfma_f32_16x16x32_bf16 a[52:55],a[92:95],a[252:255],a[52:55]
s_add_u32 m0,s76,16384
s_sub_u32 s76,s76,32768
ds_read_b64_tr_b16 a[92:93],v52 offset:17928
v_mfma_f32_16x16x32_bf16 a[52:55],a[88:91],a[248:251],a[52:55]
s_and_b32 s76,s76,0x1ffff
ds_read_b64_tr_b16 a[88:89],v52 offset:1544
v_cvt_pk_bf16_f32 v135,v14,v15
v_mfma_f32_16x16x32_bf16 a[52:55],a[84:87],a[220:223],a[52:55]
global_load_lds_dwordx4 v4,s[10:11]
ds_read_b64_tr_b16 a[84:85],v52 offset:16904
v_mfma_f32_16x16x32_bf16 a[52:55],a[80:83],a[216:219],a[52:55]
s_and_b32 s69,s75,0xfffc0000
ds_read_b64_tr_b16 a[80:81],v52 offset:520
v_accvgpr_read_b32 v12,a36
v_mfma_f32_16x16x32_bf16 a[8:11],a[96:99],a[192:195],a[8:11]
s_add_u32 s75,s75,16384
v_accvgpr_read_b32 v13,a37
v_accvgpr_read_b32 v14,a38
v_mfma_f32_16x16x32_bf16 a[8:11],a[100:103],a[196:199],a[8:11]
s_waitcnt lgkmcnt(4)
ds_read_b64_tr_b16 a[82:83],v52 offset:2568
v_accvgpr_read_b32 v15,a39
v_mfma_f32_16x16x32_bf16 a[8:11],a[104:107],a[224:227],a[8:11]
ds_read_b64_tr_b16 a[86:87],v52 offset:18952
v_mul_f32 v12,s71,v12
v_mfma_f32_16x16x32_bf16 a[8:11],a[108:111],a[228:231],a[8:11]
ds_read_b64_tr_b16 a[90:91],v52 offset:3592
v_mul_f32 v13,s71,v13
v_mfma_f32_16x16x32_bf16 a[24:27],a[108:111],a[236:239],a[24:27]
v_mul_f32 v14,s71,v14
v_mul_f32 v15,s71,v15
v_mfma_f32_16x16x32_bf16 a[24:27],a[104:107],a[232:235],a[24:27]
ds_read_b64_tr_b16 a[94:95],v52 offset:19976
v_cvt_pk_bf16_f32 v197,v10,v11
v_mfma_f32_16x16x32_bf16 a[24:27],a[100:103],a[204:207],a[24:27]
v_cvt_pk_bf16_f32 v198,v12,v13
v_cvt_pk_bf16_f32 v199,v14,v15
v_mfma_f32_16x16x32_bf16 a[24:27],a[96:99],a[200:203],a[24:27]
v_accvgpr_read_b32 v8,a8
v_accvgpr_read_b32 v9,a9
v_mfma_f32_16x16x32_bf16 a[40:43],a[96:99],a[208:211],a[40:43]
v_accvgpr_read_b32 v10,a10
v_accvgpr_read_b32 v11,a11
v_mfma_f32_16x16x32_bf16 a[40:43],a[100:103],a[212:215],a[40:43]
v_mul_f32 v8,s71,v8
v_mul_f32 v9,s71,v9
v_mfma_f32_16x16x32_bf16 a[40:43],a[104:107],a[240:243],a[40:43]
v_mul_f32 v10,s71,v10
v_mul_f32 v11,s71,v11
v_mfma_f32_16x16x32_bf16 a[40:43],a[108:111],a[244:247],a[40:43]
v_cvt_pk_bf16_f32 v140,v8,v9
v_cvt_pk_bf16_f32 v141,v10,v11
v_mfma_f32_16x16x32_bf16 a[56:59],a[108:111],a[252:255],a[56:59]
ds_read_b64_tr_b16 a[108:109],v52 offset:18176
v_accvgpr_read_b32 v20,a20
v_mfma_f32_16x16x32_bf16 a[56:59],a[104:107],a[248:251],a[56:59]
global_load_lds_dwordx4 v5,s[10:11] offset:1024
ds_read_b64_tr_b16 a[104:105],v52 offset:1792
v_mfma_f32_16x16x32_bf16 a[56:59],a[100:103],a[220:223],a[56:59]
ds_read_b64_tr_b16 a[100:101],v52 offset:17152
v_accvgpr_read_b32 v21,a21
v_mfma_f32_16x16x32_bf16 a[56:59],a[96:99],a[216:219],a[56:59]
ds_read_b64_tr_b16 a[96:97],v52 offset:768
v_accvgpr_read_b32 v22,a22
v_mfma_f32_16x16x32_bf16 a[12:15],a[112:115],a[192:195],a[12:15]
v_accvgpr_read_b32 v23,a23
v_mul_f32 v16,s71,v16
v_mfma_f32_16x16x32_bf16 a[12:15],a[116:119],a[196:199],a[12:15]
ds_read_b64_tr_b16 a[98:99],v52 offset:2816
v_mul_f32 v17,s71,v17
v_mfma_f32_16x16x32_bf16 a[12:15],a[120:123],a[224:227],a[12:15]
ds_read_b64_tr_b16 a[102:103],v52 offset:19200
v_mul_f32 v18,s71,v18
v_mfma_f32_16x16x32_bf16 a[12:15],a[124:127],a[228:231],a[12:15]
ds_read_b64_tr_b16 a[106:107],v52 offset:3840
v_mul_f32 v19,s71,v19
v_mfma_f32_16x16x32_bf16 a[28:31],a[124:127],a[236:239],a[28:31]
v_mul_f32 v20,s71,v20
v_mul_f32 v21,s71,v21
v_mfma_f32_16x16x32_bf16 a[28:31],a[120:123],a[232:235],a[28:31]
ds_read_b64_tr_b16 a[110:111],v52 offset:20224
v_mul_f32 v22,s71,v22
v_mfma_f32_16x16x32_bf16 a[28:31],a[116:119],a[204:207],a[28:31]
v_accvgpr_read_b32 v12,a12
v_accvgpr_read_b32 v13,a13
v_mfma_f32_16x16x32_bf16 a[28:31],a[112:115],a[200:203],a[28:31]
v_accvgpr_read_b32 v14,a14
v_accvgpr_read_b32 v15,a15
v_mfma_f32_16x16x32_bf16 a[44:47],a[112:115],a[208:211],a[44:47]
v_mul_f32 v12,s71,v12
v_mul_f32 v13,s71,v13
v_mfma_f32_16x16x32_bf16 a[44:47],a[116:119],a[212:215],a[44:47]
v_mul_f32 v14,s71,v14
v_mul_f32 v15,s71,v15
v_mfma_f32_16x16x32_bf16 a[44:47],a[120:123],a[240:243],a[44:47]
v_cvt_pk_bf16_f32 v142,v12,v13
v_cvt_pk_bf16_f32 v143,v14,v15
v_mfma_f32_16x16x32_bf16 a[44:47],a[124:127],a[244:247],a[44:47]
v_mul_f32 v23,s71,v23
v_cvt_pk_bf16_f32 v164,v16,v17
v_mfma_f32_16x16x32_bf16 a[60:63],a[124:127],a[252:255],a[60:63]
global_load_lds_dwordx4 v4,s[10:11] offset:2048
ds_read_b64_tr_b16 a[124:125],v52 offset:18184
v_mfma_f32_16x16x32_bf16 a[60:63],a[120:123],a[248:251],a[60:63]
ds_read_b64_tr_b16 a[120:121],v52 offset:1800
v_cvt_pk_bf16_f32 v165,v18,v19
v_mfma_f32_16x16x32_bf16 a[60:63],a[116:119],a[220:223],a[60:63]
ds_read_b64_tr_b16 a[116:117],v52 offset:17160
v_cvt_pk_bf16_f32 v166,v20,v21
v_mfma_f32_16x16x32_bf16 a[60:63],a[112:115],a[216:219],a[60:63]
ds_read_b64_tr_b16 a[112:113],v52 offset:776
v_cvt_pk_bf16_f32 v167,v22,v23
v_mfma_f32_16x16x32_bf16 v[64:67],a[64:67],a[192:195],v[64:67]
v_accvgpr_read_b32 v16,a48
v_accvgpr_read_b32 v17,a49
v_mfma_f32_16x16x32_bf16 v[64:67],a[68:71],a[196:199],v[64:67]
ds_read_b64_tr_b16 a[114:115],v52 offset:2824
v_accvgpr_read_b32 v18,a50
v_mfma_f32_16x16x32_bf16 v[64:67],a[72:75],a[224:227],v[64:67]
ds_read_b64_tr_b16 a[118:119],v52 offset:19208
v_accvgpr_read_b32 v19,a51
v_mfma_f32_16x16x32_bf16 v[64:67],a[76:79],a[228:231],v[64:67]
ds_read_b64_tr_b16 a[122:123],v52 offset:3848
v_accvgpr_read_b32 v20,a52
v_mfma_f32_16x16x32_bf16 v[80:83],a[76:79],a[236:239],v[80:83]
v_accvgpr_read_b32 v21,a53
v_accvgpr_read_b32 v22,a54
v_mfma_f32_16x16x32_bf16 v[80:83],a[72:75],a[232:235],v[80:83]
ds_read_b64_tr_b16 a[126:127],v52 offset:20232
v_accvgpr_read_b32 v23,a55
v_mfma_f32_16x16x32_bf16 v[80:83],a[68:71],a[204:207],v[80:83]
v_mul_f32 v64,s71,v64
v_mul_f32 v65,s71,v65
v_mfma_f32_16x16x32_bf16 v[80:83],a[64:67],a[200:203],v[80:83]
v_mul_f32 v66,s71,v66
v_mul_f32 v67,s71,v67
v_mfma_f32_16x16x32_bf16 v[96:99],a[64:67],a[208:211],v[96:99]
v_cvt_pk_bf16_f32 v148,v64,v65
v_cvt_pk_bf16_f32 v149,v66,v67
v_mfma_f32_16x16x32_bf16 v[96:99],a[68:71],a[212:215],v[96:99]
v_mul_f32 v16,s71,v16
v_mul_f32 v17,s71,v17
v_mfma_f32_16x16x32_bf16 v[96:99],a[72:75],a[240:243],v[96:99]
v_mul_f32 v18,s71,v18
v_mul_f32 v19,s71,v19
v_mfma_f32_16x16x32_bf16 v[96:99],a[76:79],a[244:247],v[96:99]
global_load_lds_dwordx4 v5,s[10:11] offset:3072
v_mul_f32 v20,s71,v20
v_mfma_f32_16x16x32_bf16 v[112:115],a[76:79],a[252:255],v[112:115]
v_mul_f32 v21,s71,v21
v_mul_f32 v22,s71,v22
v_mfma_f32_16x16x32_bf16 v[112:115],a[72:75],a[248:251],v[112:115]
v_mul_f32 v23,s71,v23
v_cvt_pk_bf16_f32 v228,v16,v17
v_mfma_f32_16x16x32_bf16 v[112:115],a[68:71],a[220:223],v[112:115]
v_cvt_pk_bf16_f32 v229,v18,v19
v_cvt_pk_bf16_f32 v230,v20,v21
v_mfma_f32_16x16x32_bf16 v[112:115],a[64:67],a[216:219],v[112:115]
v_cvt_pk_bf16_f32 v231,v22,v23
v_accvgpr_read_b32 v16,a24
s_waitcnt vmcnt(8) lgkmcnt(0)
s_barrier
v_mfma_f32_16x16x32_bf16 v[68:71],a[80:83],a[192:195],v[68:71]
ds_read_b128 a[128:131],v1
v_accvgpr_read_b32 v17,a25
v_mfma_f32_16x16x32_bf16 v[68:71],a[84:87],a[196:199],v[68:71]
ds_read_b128 a[132:135],v1 offset:256
v_accvgpr_read_b32 v18,a26
v_mfma_f32_16x16x32_bf16 v[68:71],a[88:91],a[224:227],v[68:71]
ds_read_b128 a[136:139],v1 offset:512
v_accvgpr_read_b32 v19,a27
v_mfma_f32_16x16x32_bf16 v[68:71],a[92:95],a[228:231],v[68:71]
ds_read_b128 a[140:143],v1 offset:768
v_accvgpr_read_b32 v20,a28
v_mfma_f32_16x16x32_bf16 v[84:87],a[92:95],a[236:239],v[84:87]
v_accvgpr_read_b32 v21,a29
v_accvgpr_read_b32 v22,a30
v_mfma_f32_16x16x32_bf16 v[84:87],a[88:91],a[232:235],v[84:87]
v_accvgpr_read_b32 v23,a31
v_mul_f32 v16,s71,v16
v_mfma_f32_16x16x32_bf16 v[84:87],a[84:87],a[204:207],v[84:87]
s_add_u32 s10,s20,s74
s_addc_u32 s11,s21,0
v_mul_f32 v68,s71,v68
v_mfma_f32_16x16x32_bf16 v[84:87],a[80:83],a[200:203],v[84:87]
v_mul_f32 v69,s71,v69
v_mul_f32 v70,s71,v70
v_mfma_f32_16x16x32_bf16 v[100:103],a[80:83],a[208:211],v[100:103]
s_add_u32 m0,s76,0
v_mul_f32 v71,s71,v71
v_cvt_pk_bf16_f32 v150,v68,v69
v_mfma_f32_16x16x32_bf16 v[100:103],a[84:87],a[212:215],v[100:103]
v_cvt_pk_bf16_f32 v151,v70,v71
v_mul_f32 v17,s71,v17
v_mfma_f32_16x16x32_bf16 v[100:103],a[88:91],a[240:243],v[100:103]
global_load_lds_dwordx4 v4,s[10:11] nt
v_mul_f32 v18,s71,v18
v_mfma_f32_16x16x32_bf16 v[100:103],a[92:95],a[244:247],v[100:103]
v_mul_f32 v19,s71,v19
v_mul_f32 v20,s71,v20
v_mfma_f32_16x16x32_bf16 v[116:119],a[92:95],a[252:255],v[116:119]
v_mul_f32 v21,s71,v21
v_mul_f32 v22,s71,v22
v_mfma_f32_16x16x32_bf16 v[116:119],a[88:91],a[248:251],v[116:119]
v_mul_f32 v23,s71,v23
v_cvt_pk_bf16_f32 v172,v16,v17
v_mfma_f32_16x16x32_bf16 v[116:119],a[84:87],a[220:223],v[116:119]
v_cvt_pk_bf16_f32 v173,v18,v19
v_cvt_pk_bf16_f32 v174,v20,v21
v_mfma_f32_16x16x32_bf16 v[116:119],a[80:83],a[216:219],v[116:119]
v_cvt_pk_bf16_f32 v175,v22,v23
v_mul_f32 v80,s71,v80
v_mfma_f32_16x16x32_bf16 v[72:75],a[96:99],a[192:195],v[72:75]
v_mul_f32 v81,s71,v81
v_mul_f32 v82,s71,v82
v_mfma_f32_16x16x32_bf16 v[72:75],a[100:103],a[196:199],v[72:75]
v_mul_f32 v83,s71,v83
v_mul_f32 v84,s71,v84
v_mfma_f32_16x16x32_bf16 v[72:75],a[104:107],a[224:227],v[72:75]
v_mul_f32 v85,s71,v85
v_mul_f32 v86,s71,v86
v_mfma_f32_16x16x32_bf16 v[72:75],a[108:111],a[228:231],v[72:75]
v_mul_f32 v87,s71,v87
v_cvt_pk_bf16_f32 v180,v80,v81
v_mfma_f32_16x16x32_bf16 v[88:91],a[108:111],a[236:239],v[88:91]
v_cvt_pk_bf16_f32 v181,v82,v83
v_cvt_pk_bf16_f32 v182,v84,v85
v_mfma_f32_16x16x32_bf16 v[88:91],a[104:107],a[232:235],v[88:91]
v_cvt_pk_bf16_f32 v183,v86,v87
v_accvgpr_read_b32 v8,a40
v_mfma_f32_16x16x32_bf16 v[88:91],a[100:103],a[204:207],v[88:91]
v_mul_f32 v72,s71,v72
v_mul_f32 v73,s71,v73
v_mfma_f32_16x16x32_bf16 v[88:91],a[96:99],a[200:203],v[88:91]
v_mul_f32 v74,s71,v74
v_mul_f32 v75,s71,v75
v_mfma_f32_16x16x32_bf16 v[104:107],a[96:99],a[208:211],v[104:107]
global_load_lds_dwordx4 v4,s[10:11] offset:1024 nt
v_cvt_pk_bf16_f32 v156,v72,v73
v_mfma_f32_16x16x32_bf16 v[104:107],a[100:103],a[212:215],v[104:107]
v_cvt_pk_bf16_f32 v157,v74,v75
v_accvgpr_read_b32 v9,a41
v_mfma_f32_16x16x32_bf16 v[104:107],a[104:107],a[240:243],v[104:107]
v_mul_f32 v88,s71,v88
v_mul_f32 v89,s71,v89
v_mfma_f32_16x16x32_bf16 v[104:107],a[108:111],a[244:247],v[104:107]
v_mul_f32 v90,s71,v90
v_mul_f32 v91,s71,v91
v_mfma_f32_16x16x32_bf16 v[120:123],a[108:111],a[252:255],v[120:123]
v_cvt_pk_bf16_f32 v188,v88,v89
v_cvt_pk_bf16_f32 v189,v90,v91
v_mfma_f32_16x16x32_bf16 v[120:123],a[104:107],a[248:251],v[120:123]
v_accvgpr_read_b32 v10,a42
v_accvgpr_read_b32 v11,a43
v_mfma_f32_16x16x32_bf16 v[120:123],a[100:103],a[220:223],v[120:123]
)A"
R"A(v_accvgpr_read_b32 v12,a44
v_accvgpr_read_b32 v13,a45
v_mfma_f32_16x16x32_bf16 v[120:123],a[96:99],a[216:219],v[120:123]
v_accvgpr_read_b32 v14,a46
v_accvgpr_read_b32 v15,a47
v_mfma_f32_16x16x32_bf16 v[76:79],a[112:115],a[192:195],v[76:79]
v_mul_f32 v8,s71,v8
v_mul_f32 v9,s71,v9
v_mfma_f32_16x16x32_bf16 v[76:79],a[116:119],a[196:199],v[76:79]
v_mul_f32 v10,s71,v10
v_mul_f32 v11,s71,v11
v_mfma_f32_16x16x32_bf16 v[76:79],a[120:123],a[224:227],v[76:79]
v_mul_f32 v12,s71,v12
v_mul_f32 v13,s71,v13
v_mfma_f32_16x16x32_bf16 v[76:79],a[124:127],a[228:231],v[76:79]
v_mul_f32 v14,s71,v14
v_mul_f32 v15,s71,v15
v_mfma_f32_16x16x32_bf16 v[92:95],a[124:127],a[236:239],v[92:95]
v_cvt_pk_bf16_f32 v204,v8,v9
v_cvt_pk_bf16_f32 v205,v10,v11
v_mfma_f32_16x16x32_bf16 v[92:95],a[120:123],a[232:235],v[92:95]
v_cvt_pk_bf16_f32 v206,v12,v13
v_cvt_pk_bf16_f32 v207,v14,v15
v_mfma_f32_16x16x32_bf16 v[92:95],a[116:119],a[204:207],v[92:95]
global_load_lds_dwordx4 v5,s[10:11] offset:2048 nt
v_mul_f32 v76,s71,v76
v_mfma_f32_16x16x32_bf16 v[92:95],a[112:115],a[200:203],v[92:95]
v_mul_f32 v77,s71,v77
v_mul_f32 v78,s71,v78
v_mfma_f32_16x16x32_bf16 v[108:111],a[112:115],a[208:211],v[108:111]
v_mul_f32 v79,s71,v79
v_cvt_pk_bf16_f32 v158,v76,v77
v_mfma_f32_16x16x32_bf16 v[108:111],a[116:119],a[212:215],v[108:111]
ds_read_b128 a[156:159],v1 offset:4864
v_cvt_pk_bf16_f32 v159,v78,v79
v_mfma_f32_16x16x32_bf16 v[108:111],a[120:123],a[240:243],v[108:111]
v_mul_f32 v92,s71,v92
v_mul_f32 v93,s71,v93
v_mfma_f32_16x16x32_bf16 v[108:111],a[124:127],a[244:247],v[108:111]
v_mul_f32 v94,s71,v94
v_mul_f32 v95,s71,v95
v_mfma_f32_16x16x32_bf16 v[124:127],a[124:127],a[252:255],v[124:127]
v_cvt_pk_bf16_f32 v190,v92,v93
v_cvt_pk_bf16_f32 v191,v94,v95
v_mfma_f32_16x16x32_bf16 v[124:127],a[120:123],a[248:251],v[124:127]
v_cvt_pk_bf16_f32 v209,v210,v211
v_cvt_pk_bf16_f32 v210,v212,v213
v_mfma_f32_16x16x32_bf16 v[124:127],a[116:119],a[220:223],v[124:127]
ds_read_b128 a[152:155],v1 offset:4608
v_cvt_pk_bf16_f32 v211,v214,v215
v_mfma_f32_16x16x32_bf16 v[124:127],a[112:115],a[216:219],v[124:127]
v_mul_f32 v96,s71,v96
v_mul_f32 v97,s71,v97
s_waitcnt lgkmcnt(2)
v_mfma_f32_16x16x32_bf16 v[64:67],a[128:131],v[132:135],0
v_mul_f32 v98,s71,v98
v_mul_f32 v99,s71,v99
v_mfma_f32_16x16x32_bf16 v[64:67],a[132:135],v[140:143],v[64:67]
v_mul_f32 v100,s71,v100
v_mul_f32 v101,s71,v101
v_mfma_f32_16x16x32_bf16 v[64:67],a[136:139],v[148:151],v[64:67]
v_mul_f32 v102,s71,v102
v_mul_f32 v103,s71,v103
v_mfma_f32_16x16x32_bf16 v[64:67],a[140:143],v[156:159],v[64:67]
global_load_lds_dwordx4 v5,s[10:11] offset:3072
v_cvt_pk_bf16_f32 v212,v96,v97
v_mfma_f32_16x16x32_bf16 v[80:83],a[128:131],v[164:167],0
v_cvt_pk_bf16_f32 v213,v98,v99
v_cvt_pk_bf16_f32 v214,v100,v101
v_mfma_f32_16x16x32_bf16 v[80:83],a[132:135],v[172:175],v[80:83]
v_cvt_pk_bf16_f32 v215,v102,v103
v_mul_f32 v216,s71,v216
v_mfma_f32_16x16x32_bf16 v[80:83],a[136:139],v[180:183],v[80:83]
v_max3_f32 v64,v64,v65,v66
v_max_f32 v64,v64,v67
v_mov_b32 v65,v64
v_mfma_f32_16x16x32_bf16 v[80:83],a[140:143],v[188:191],v[80:83]
v_mul_f32 v217,s71,v217
v_mul_f32 v218,s71,v218
v_mfma_f32_16x16x32_bf16 v[96:99],a[128:131],v[196:199],0
v_permlane32_swap_b32 v64,v65
v_max_f32 v64,v64,v65
v_mov_b32 v65,v64
v_mul_f32 v219,s71,v219
v_mul_f32 v220,s71,v220
v_mul_f32 v221,s71,v221
v_mul_f32 v222,s71,v222
v_mul_f32 v223,s71,v223
v_cvt_pk_bf16_f32 v216,v216,v217
v_cvt_pk_bf16_f32 v217,v218,v219
v_cvt_pk_bf16_f32 v218,v220,v221
v_cvt_pk_bf16_f32 v219,v222,v223
v_mul_f32 v104,s71,v104
v_mul_f32 v105,s71,v105
v_mul_f32 v106,s71,v106
v_mul_f32 v107,s71,v107
v_mul_f32 v108,s71,v108
v_mul_f32 v109,s71,v109
v_mul_f32 v110,s71,v110
v_mul_f32 v111,s71,v111
v_cvt_pk_bf16_f32 v220,v104,v105
v_cvt_pk_bf16_f32 v221,v106,v107
v_cvt_pk_bf16_f32 v222,v108,v109
v_cvt_pk_bf16_f32 v223,v110,v111
v_mfma_f32_16x16x32_bf16 v[96:99],a[132:135],v[204:207],v[96:99]
ds_read_b128 a[148:151],v1 offset:4352
v_max3_f32 v80,v80,v81,v82
v_mfma_f32_16x16x32_bf16 v[96:99],a[136:139],v[212:215],v[96:99]
L_pd4_%=:
s_cbranch_execz L_pd8_%=
ds_read_b128 a[144:147],v1 offset:4096
v_permlane16_swap_b32 v64,v65
v_max_f32 v64,v64,v65
v_sub_f32 v35,0,v64
v_sub_f32 v34,0,v64
v_sub_f32 v33,0,v64
v_sub_f32 v32,0,v64
v_mfma_f32_16x16x32_bf16 v[96:99],a[140:143],v[220:223],v[96:99]
ds_read_b128 a[160:163],v1 offset:8192
v_max_f32 v80,v80,v83
v_mfma_f32_16x16x32_bf16 v[64:67],a[128:131],v[132:135],v[32:35]
s_waitcnt lgkmcnt(3)
ds_read_b128 a[164:167],v1 offset:8448
v_mov_b32 v81,v80
v_mfma_f32_16x16x32_bf16 v[64:67],a[132:135],v[140:143],v[64:67]
s_add_u32 s10,s24,s74
s_addc_u32 s11,s25,0
v_mul_f32 v232,s71,v232
v_mfma_f32_16x16x32_bf16 v[64:67],a[136:139],v[148:151],v[64:67]
s_add_u32 s74,s74,16384
ds_read_b128 a[168:171],v1 offset:8704
v_max3_f32 v96,v96,v97,v98
v_mfma_f32_16x16x32_bf16 v[64:67],a[140:143],v[156:159],v[64:67]
s_add_u32 m0,s76,16384
ds_read_b128 a[172:175],v1 offset:8960
v_max_f32 v96,v96,v99
v_mfma_f32_16x16x32_bf16 v[68:71],a[156:159],v[156:159],v[32:35]
s_and_b32 s74,s74,0x3ffff
ds_read_b128 a[188:191],v1 offset:13056
v_mov_b32 v97,v96
v_mfma_f32_16x16x32_bf16 v[68:71],a[152:155],v[148:151],v[68:71]
global_load_lds_dwordx4 v4,s[10:11]
v_mul_f32 v233,s71,v233
s_waitcnt lgkmcnt(6)
v_mfma_f32_16x16x32_bf16 v[68:71],a[148:151],v[140:143],v[68:71]
s_or_b32 s74,s74,s69
ds_read_b128 a[184:187],v1 offset:12800
v_mul_f32 v234,s71,v234
s_waitcnt lgkmcnt(6)
v_mfma_f32_16x16x32_bf16 v[68:71],a[144:147],v[132:135],v[68:71]
s_sub_u32 s76,s76,32768
ds_read_b128 a[180:183],v1 offset:12544
v_mul_f32 v235,s71,v235
s_waitcnt lgkmcnt(6)
v_mfma_f32_16x16x32_bf16 v[72:75],a[160:163],v[132:135],v[32:35]
s_and_b32 s76,s76,0x1ffff
ds_read_b128 a[176:179],v1 offset:12288
v_mul_f32 v236,s71,v236
s_waitcnt lgkmcnt(6)
v_mfma_f32_16x16x32_bf16 v[72:75],a[164:167],v[140:143],v[72:75]
s_and_b32 s69,s75,0xfffc0000
v_permlane32_swap_b32 v80,v81
s_waitcnt lgkmcnt(5)
v_mfma_f32_16x16x32_bf16 v[72:75],a[168:171],v[148:151],v[72:75]
s_add_u32 s75,s75,16384
v_max_f32 v80,v80,v81
v_mov_b32 v81,v80
s_waitcnt lgkmcnt(4)
v_mfma_f32_16x16x32_bf16 v[72:75],a[172:175],v[156:159],v[72:75]
s_waitcnt lgkmcnt(3)
v_mul_f32 v237,s71,v237
v_mul_f32 v238,s71,v238
v_mfma_f32_16x16x32_bf16 v[76:79],a[188:191],v[156:159],v[32:35]
s_waitcnt lgkmcnt(2)
v_permlane16_swap_b32 v80,v81
v_mfma_f32_16x16x32_bf16 v[76:79],a[184:187],v[148:151],v[76:79]
s_waitcnt lgkmcnt(1)
v_max_f32 v80,v80,v81
v_sub_f32 v39,0,v80
v_sub_f32 v38,0,v80
v_sub_f32 v37,0,v80
v_sub_f32 v36,0,v80
v_mfma_f32_16x16x32_bf16 v[76:79],a[180:183],v[140:143],v[76:79]
s_waitcnt lgkmcnt(0)
v_mul_f32 v239,s71,v239
v_cvt_pk_bf16_f32 v232,v232,v233
v_mfma_f32_16x16x32_bf16 v[76:79],a[176:179],v[132:135],v[76:79]
v_cvt_pk_bf16_f32 v233,v234,v235
v_cvt_pk_bf16_f32 v234,v236,v237
v_mfma_f32_16x16x32_bf16 v[80:83],a[128:131],v[164:167],v[36:39]
v_cvt_pk_bf16_f32 v235,v238,v239
v_accvgpr_read_b32 v16,a56
v_mfma_f32_16x16x32_bf16 v[80:83],a[132:135],v[172:175],v[80:83]
v_accvgpr_read_b32 v17,a57
v_accvgpr_read_b32 v18,a58
v_mfma_f32_16x16x32_bf16 v[80:83],a[136:139],v[180:183],v[80:83]
v_accvgpr_read_b32 v19,a59
v_accvgpr_read_b32 v20,a60
v_mfma_f32_16x16x32_bf16 v[80:83],a[140:143],v[188:191],v[80:83]
global_load_lds_dwordx4 v5,s[10:11] offset:1024
v_accvgpr_read_b32 v21,a61
v_mfma_f32_16x16x32_bf16 v[84:87],a[156:159],v[188:191],v[36:39]
v_accvgpr_read_b32 v22,a62
v_accvgpr_read_b32 v23,a63
v_mfma_f32_16x16x32_bf16 v[84:87],a[152:155],v[180:183],v[84:87]
v_mul_f32 v16,s71,v16
v_mul_f32 v17,s71,v17
v_mfma_f32_16x16x32_bf16 v[84:87],a[148:151],v[172:175],v[84:87]
v_mul_f32 v18,s71,v18
v_mul_f32 v19,s71,v19
v_mfma_f32_16x16x32_bf16 v[84:87],a[144:147],v[164:167],v[84:87]
v_mul_f32 v20,s71,v20
v_mul_f32 v21,s71,v21
v_mfma_f32_16x16x32_bf16 v[88:91],a[160:163],v[164:167],v[36:39]
v_mul_f32 v22,s71,v22
v_mul_f32 v23,s71,v23
v_mfma_f32_16x16x32_bf16 v[88:91],a[164:167],v[172:175],v[88:91]
v_cvt_pk_bf16_f32 v236,v16,v17
v_cvt_pk_bf16_f32 v237,v18,v19
v_mfma_f32_16x16x32_bf16 v[88:91],a[168:171],v[180:183],v[88:91]
v_cvt_pk_bf16_f32 v238,v20,v21
v_cvt_pk_bf16_f32 v239,v22,v23
v_mfma_f32_16x16x32_bf16 v[88:91],a[172:175],v[188:191],v[88:91]
v_mul_f32 v240,s71,v240
v_mul_f32 v241,s71,v241
v_mfma_f32_16x16x32_bf16 v[92:95],a[188:191],v[188:191],v[36:39]
v_mul_f32 v242,s71,v242
v_mul_f32 v243,s71,v243
v_mfma_f32_16x16x32_bf16 v[92:95],a[184:187],v[180:183],v[92:95]
v_mul_f32 v244,s71,v244
v_mul_f32 v245,s71,v245
v_mfma_f32_16x16x32_bf16 v[92:95],a[180:183],v[172:175],v[92:95]
v_mul_f32 v246,s71,v246
v_mul_f32 v247,s71,v247
v_mfma_f32_16x16x32_bf16 v[92:95],a[176:179],v[164:167],v[92:95]
v_cvt_pk_bf16_f32 v240,v240,v241
v_cvt_pk_bf16_f32 v241,v242,v243
v_cvt_pk_bf16_f32 v242,v244,v245
v_cvt_pk_bf16_f32 v243,v246,v247
v_mul_f32 v112,s71,v112
v_mul_f32 v113,s71,v113
v_mul_f32 v114,s71,v114
v_mul_f32 v115,s71,v115
v_mul_f32 v116,s71,v116
v_mul_f32 v117,s71,v117
v_mul_f32 v118,s71,v118
v_mul_f32 v119,s71,v119
v_cvt_pk_bf16_f32 v244,v112,v113
v_cvt_pk_bf16_f32 v245,v114,v115
v_cvt_pk_bf16_f32 v246,v116,v117
v_cvt_pk_bf16_f32 v247,v118,v119
v_permlane32_swap_b32 v96,v97
v_max_f32 v96,v96,v97
v_mov_b32 v97,v96
v_mfma_f32_16x16x32_bf16 v[112:115],a[128:131],v[228:231],0
v_mul_f32 v248,s71,v248
v_mul_f32 v249,s71,v249
v_mul_f32 v250,s71,v250
v_mul_f32 v251,s71,v251
v_mul_f32 v252,s71,v252
v_mul_f32 v253,s71,v253
v_mul_f32 v254,s71,v254
v_mul_f32 v255,s71,v255
v_cvt_pk_bf16_f32 v248,v248,v249
v_cvt_pk_bf16_f32 v249,v250,v251
v_cvt_pk_bf16_f32 v250,v252,v253
v_cvt_pk_bf16_f32 v251,v254,v255
v_mul_f32 v120,s71,v120
v_mul_f32 v121,s71,v121
v_mul_f32 v122,s71,v122
v_mul_f32 v123,s71,v123
v_mul_f32 v124,s71,v124
v_mul_f32 v125,s71,v125
v_mul_f32 v126,s71,v126
v_mul_f32 v127,s71,v127
v_cvt_pk_bf16_f32 v252,v120,v121
v_cvt_pk_bf16_f32 v253,v122,v123
v_cvt_pk_bf16_f32 v254,v124,v125
v_cvt_pk_bf16_f32 v255,v126,v127
v_mfma_f32_16x16x32_bf16 v[112:115],a[132:135],v[236:239],v[112:115]
global_load_lds_dwordx4 v4,s[10:11] offset:2048
v_permlane16_swap_b32 v96,v97
v_max_f32 v96,v96,v97
v_sub_f32 v43,0,v96
v_sub_f32 v42,0,v96
v_sub_f32 v41,0,v96
v_sub_f32 v40,0,v96
v_mfma_f32_16x16x32_bf16 v[112:115],a[136:139],v[244:247],v[112:115]
ds_read_b64_tr_b16 a[192:193],v2
ds_read_b64_tr_b16 a[194:195],v2 offset:4096
v_mfma_f32_16x16x32_bf16 v[112:115],a[140:143],v[252:255],v[112:115]
ds_read_b64_tr_b16 a[196:197],v2 offset:8192
ds_read_b64_tr_b16 a[198:199],v2 offset:12288
v_mfma_f32_16x16x32_bf16 v[96:99],a[128:131],v[196:199],v[40:43]
ds_read_b64_tr_b16 a[200:201],v3
ds_read_b64_tr_b16 a[202:203],v3 offset:4096
v_mfma_f32_16x16x32_bf16 v[96:99],a[132:135],v[204:207],v[96:99]
ds_read_b64_tr_b16 a[204:205],v3 offset:8192
ds_read_b64_tr_b16 a[206:207],v3 offset:12288
v_mfma_f32_16x16x32_bf16 v[96:99],a[136:139],v[212:215],v[96:99]
ds_read_b64_tr_b16 a[208:209],v2 offset:256
v_max3_f32 v112,v112,v113,v114
v_mfma_f32_16x16x32_bf16 v[96:99],a[140:143],v[220:223],v[96:99]
ds_read_b64_tr_b16 a[210:211],v2 offset:4352
ds_read_b64_tr_b16 a[212:213],v2 offset:8448
v_mfma_f32_16x16x32_bf16 v[100:103],a[156:159],v[220:223],v[40:43]
ds_read_b64_tr_b16 a[214:215],v2 offset:12544
v_max_f32 v112,v112,v115
v_mfma_f32_16x16x32_bf16 v[100:103],a[152:155],v[212:215],v[100:103]
ds_read_b64_tr_b16 a[216:217],v3 offset:256
ds_read_b64_tr_b16 a[218:219],v3 offset:4352
v_mfma_f32_16x16x32_bf16 v[100:103],a[148:151],v[204:207],v[100:103]
v_exp_f32 v64,v64
v_mfma_f32_16x16x32_bf16 v[100:103],a[144:147],v[196:199],v[100:103]
v_exp_f32 v65,v65
v_mfma_f32_16x16x32_bf16 v[104:107],a[160:163],v[196:199],v[40:43]
v_exp_f32 v66,v66
v_mfma_f32_16x16x32_bf16 v[104:107],a[164:167],v[204:207],v[104:107]
v_exp_f32 v67,v67
v_mfma_f32_16x16x32_bf16 v[104:107],a[168:171],v[212:215],v[104:107]
v_exp_f32 v68,v68
v_mfma_f32_16x16x32_bf16 v[104:107],a[172:175],v[220:223],v[104:107]
global_load_lds_dwordx4 v5,s[10:11] offset:3072
ds_read_b64_tr_b16 a[220:221],v3 offset:8448
v_mfma_f32_16x16x32_bf16 v[108:111],a[188:191],v[220:223],v[40:43]
v_exp_f32 v69,v69
v_exp_f32 v70,v70
v_exp_f32 v71,v71
v_mfma_f32_16x16x32_bf16 v[108:111],a[184:187],v[212:215],v[108:111]
)A"
R"A(ds_read_b64_tr_b16 a[222:223],v3 offset:12544
v_cvt_pk_bf16_f32 v64,v64,v65
v_cvt_pk_bf16_f32 v65,v66,v67
v_cvt_pk_bf16_f32 v66,v68,v69
v_cvt_pk_bf16_f32 v67,v70,v71
v_exp_f32 v72,v72
v_exp_f32 v73,v73
v_exp_f32 v74,v74
v_exp_f32 v75,v75
v_exp_f32 v76,v76
v_exp_f32 v77,v77
v_exp_f32 v78,v78
v_exp_f32 v79,v79
v_mfma_f32_16x16x32_bf16 v[108:111],a[180:183],v[204:207],v[108:111]
ds_read_b64_tr_b16 a[224:225],v2 offset:512
ds_read_b64_tr_b16 a[226:227],v2 offset:4608
v_cvt_pk_bf16_f32 v72,v72,v73
v_cvt_pk_bf16_f32 v73,v74,v75
v_cvt_pk_bf16_f32 v74,v76,v77
v_cvt_pk_bf16_f32 v75,v78,v79
v_mfma_f32_16x16x32_bf16 v[108:111],a[176:179],v[196:199],v[108:111]
s_waitcnt lgkmcnt(4)
ds_read_b64_tr_b16 a[228:229],v2 offset:8704
ds_read_b64_tr_b16 a[230:231],v2 offset:12800
v_mfma_f32_16x16x32_bf16 a[0:3],a[192:195],v[64:67],0
ds_read_b64_tr_b16 a[232:233],v3 offset:512
ds_read_b64_tr_b16 a[234:235],v3 offset:4608
v_mfma_f32_16x16x32_bf16 a[0:3],a[196:199],v[72:75],a[0:3]
ds_read_b64_tr_b16 a[236:237],v3 offset:8704
ds_read_b64_tr_b16 a[238:239],v3 offset:12800
v_mfma_f32_16x16x32_bf16 a[4:7],a[200:203],v[64:67],0
ds_read_b64_tr_b16 a[240:241],v2 offset:768
v_mov_b32 v113,v112
v_mfma_f32_16x16x32_bf16 a[4:7],a[204:207],v[72:75],a[4:7]
ds_read_b64_tr_b16 a[242:243],v2 offset:4864
ds_read_b64_tr_b16 a[244:245],v2 offset:8960
v_mfma_f32_16x16x32_bf16 a[8:11],a[208:211],v[64:67],0
ds_read_b64_tr_b16 a[246:247],v2 offset:13056
v_add_u32 v1,s78,v1
v_mfma_f32_16x16x32_bf16 a[8:11],a[212:215],v[72:75],a[8:11]
ds_read_b64_tr_b16 a[248:249],v3 offset:768
ds_read_b64_tr_b16 a[250:251],v3 offset:4864
v_mfma_f32_16x16x32_bf16 a[12:15],a[216:219],v[64:67],0
s_waitcnt lgkmcnt(12)
ds_read_b64_tr_b16 a[252:253],v3 offset:8960
v_add_u32 v2,s78,v2
v_mfma_f32_16x16x32_bf16 a[12:15],a[220:223],v[72:75],a[12:15]
s_waitcnt lgkmcnt(6)
ds_read_b64_tr_b16 a[254:255],v3 offset:13056
v_add_u32 v3,s78,v3
v_mfma_f32_16x16x32_bf16 a[16:19],a[224:227],v[64:67],0
s_sub_u32 s78,s77,32768
v_permlane32_swap_b32 v112,v113
v_mfma_f32_16x16x32_bf16 a[16:19],a[228:231],v[72:75],a[16:19]
s_and_b32 s78,s78,0x1ffff
v_max_f32 v112,v112,v113
v_mov_b32 v113,v112
v_mfma_f32_16x16x32_bf16 a[20:23],a[232:235],v[64:67],0
s_sub_u32 s78,s78,s77
v_exp_f32 v80,v80
v_mfma_f32_16x16x32_bf16 a[20:23],a[236:239],v[72:75],a[20:23]
s_add_u32 s77,s77,s78
v_permlane16_swap_b32 v112,v113
s_waitcnt lgkmcnt(5)
v_mfma_f32_16x16x32_bf16 a[24:27],a[240:243],v[64:67],0
s_waitcnt lgkmcnt(4)
v_max_f32 v112,v112,v113
v_max_f32 v112,s72,v112
v_mfma_f32_16x16x32_bf16 a[24:27],a[244:247],v[72:75],a[24:27]
s_waitcnt lgkmcnt(2)
v_sub_f32 v47,0,v112
v_sub_f32 v46,0,v112
v_mfma_f32_16x16x32_bf16 a[28:31],a[248:251],v[64:67],0
s_waitcnt lgkmcnt(0)
v_sub_f32 v45,0,v112
v_sub_f32 v44,0,v112
v_mfma_f32_16x16x32_bf16 a[28:31],a[252:255],v[72:75],a[28:31]
v_exp_f32 v81,v81
v_mfma_f32_16x16x32_bf16 v[48:51],v[28:31],v[64:67],0
v_exp_f32 v82,v82
v_mfma_f32_16x16x32_bf16 v[48:51],v[28:31],v[72:75],v[48:51]
v_exp_f32 v83,v83
s_waitcnt vmcnt(8)
s_barrier
v_mfma_f32_16x16x32_bf16 v[112:115],a[128:131],v[228:231],v[44:47]
s_add_u32 s10,s20,s74
s_addc_u32 s11,s21,0
s_add_u32 m0,s76,0
v_mfma_f32_16x16x32_bf16 v[112:115],a[132:135],v[236:239],v[112:115]
v_exp_f32 v84,v84
v_mfma_f32_16x16x32_bf16 v[112:115],a[136:139],v[244:247],v[112:115]
global_load_lds_dwordx4 v4,s[10:11] nt
v_cvt_pk_bf16_f32 v80,v80,v81
v_mfma_f32_16x16x32_bf16 v[112:115],a[140:143],v[252:255],v[112:115]
v_exp_f32 v85,v85
v_mfma_f32_16x16x32_bf16 v[116:119],a[156:159],v[252:255],v[44:47]
v_exp_f32 v86,v86
v_mfma_f32_16x16x32_bf16 v[116:119],a[152:155],v[244:247],v[116:119]
v_exp_f32 v87,v87
v_mfma_f32_16x16x32_bf16 v[116:119],a[148:151],v[236:239],v[116:119]
v_cvt_pk_bf16_f32 v81,v82,v83
v_cvt_pk_bf16_f32 v82,v84,v85
v_mfma_f32_16x16x32_bf16 v[116:119],a[144:147],v[228:231],v[116:119]
ds_read_b128 a[128:131],v1
v_cvt_pk_bf16_f32 v83,v86,v87
v_mfma_f32_16x16x32_bf16 v[120:123],a[160:163],v[228:231],v[44:47]
v_exp_f32 v88,v88
v_mfma_f32_16x16x32_bf16 v[120:123],a[164:167],v[236:239],v[120:123]
v_exp_f32 v89,v89
v_mfma_f32_16x16x32_bf16 v[120:123],a[168:171],v[244:247],v[120:123]
v_exp_f32 v90,v90
v_mfma_f32_16x16x32_bf16 v[120:123],a[172:175],v[252:255],v[120:123]
v_exp_f32 v91,v91
v_mfma_f32_16x16x32_bf16 v[124:127],a[188:191],v[252:255],v[44:47]
v_exp_f32 v92,v92
v_mfma_f32_16x16x32_bf16 v[124:127],a[184:187],v[244:247],v[124:127]
v_exp_f32 v93,v93
v_exp_f32 v94,v94
v_exp_f32 v95,v95
v_mfma_f32_16x16x32_bf16 v[124:127],a[180:183],v[236:239],v[124:127]
v_cvt_pk_bf16_f32 v88,v88,v89
v_cvt_pk_bf16_f32 v89,v90,v91
v_cvt_pk_bf16_f32 v90,v92,v93
v_cvt_pk_bf16_f32 v91,v94,v95
v_mfma_f32_16x16x32_bf16 v[124:127],a[176:179],v[228:231],v[124:127]
v_exp_f32 v96,v96
v_mfma_f32_16x16x32_bf16 a[32:35],a[192:195],v[80:83],0
global_load_lds_dwordx4 v4,s[10:11] offset:1024 nt
ds_read_b128 a[132:135],v1 offset:256
v_mfma_f32_16x16x32_bf16 a[32:35],a[196:199],v[88:91],a[32:35]
v_exp_f32 v97,v97
v_mfma_f32_16x16x32_bf16 a[36:39],a[200:203],v[80:83],0
ds_read_b128 a[136:139],v1 offset:512
v_cvt_pk_bf16_f32 v96,v96,v97
v_mfma_f32_16x16x32_bf16 a[36:39],a[204:207],v[88:91],a[36:39]
v_exp_f32 v98,v98
v_mfma_f32_16x16x32_bf16 a[40:43],a[208:211],v[80:83],0
v_exp_f32 v99,v99
v_mfma_f32_16x16x32_bf16 a[40:43],a[212:215],v[88:91],a[40:43]
ds_read_b128 a[140:143],v1 offset:768
v_cvt_pk_bf16_f32 v97,v98,v99
v_mfma_f32_16x16x32_bf16 a[44:47],a[216:219],v[80:83],0
v_exp_f32 v100,v100
v_mfma_f32_16x16x32_bf16 a[44:47],a[220:223],v[88:91],a[44:47]
v_exp_f32 v101,v101
v_mfma_f32_16x16x32_bf16 a[48:51],a[224:227],v[80:83],0
ds_read_b128 a[156:159],v1 offset:4864
v_cvt_pk_bf16_f32 v98,v100,v101
v_mfma_f32_16x16x32_bf16 a[48:51],a[228:231],v[88:91],a[48:51]
v_exp_f32 v102,v102
v_mfma_f32_16x16x32_bf16 a[52:55],a[232:235],v[80:83],0
v_exp_f32 v103,v103
v_mfma_f32_16x16x32_bf16 a[52:55],a[236:239],v[88:91],a[52:55]
ds_read_b128 a[152:155],v1 offset:4608
v_cvt_pk_bf16_f32 v99,v102,v103
v_mfma_f32_16x16x32_bf16 a[56:59],a[240:243],v[80:83],0
v_exp_f32 v104,v104
v_mfma_f32_16x16x32_bf16 a[56:59],a[244:247],v[88:91],a[56:59]
ds_read_b128 a[148:151],v1 offset:4352
v_mfma_f32_16x16x32_bf16 a[60:63],a[248:251],v[80:83],0
global_load_lds_dwordx4 v5,s[10:11] offset:2048 nt
v_mfma_f32_16x16x32_bf16 a[60:63],a[252:255],v[88:91],a[60:63]
ds_read_b128 a[144:147],v1 offset:4096
v_mfma_f32_16x16x32_bf16 v[52:55],v[28:31],v[80:83],0
v_exp_f32 v105,v105
v_mfma_f32_16x16x32_bf16 v[52:55],v[28:31],v[88:91],v[52:55]
s_waitcnt lgkmcnt(3)
ds_read_b128 a[160:163],v1 offset:8192
v_cvt_pk_bf16_f32 v104,v104,v105
v_mfma_f32_16x16x32_bf16 v[64:67],a[128:131],v[132:135],v[32:35]
v_exp_f32 v106,v106
v_mfma_f32_16x16x32_bf16 v[64:67],a[132:135],v[140:143],v[64:67]
ds_read_b128 a[164:167],v1 offset:8448
v_mfma_f32_16x16x32_bf16 v[64:67],a[136:139],v[148:151],v[64:67]
v_exp_f32 v107,v107
v_mfma_f32_16x16x32_bf16 v[64:67],a[140:143],v[156:159],v[64:67]
ds_read_b128 a[168:171],v1 offset:8704
v_cvt_pk_bf16_f32 v105,v106,v107
v_mfma_f32_16x16x32_bf16 v[68:71],a[156:159],v[156:159],v[32:35]
s_waitcnt lgkmcnt(4)
v_exp_f32 v108,v108
v_mfma_f32_16x16x32_bf16 v[68:71],a[152:155],v[148:151],v[68:71]
ds_read_b128 a[172:175],v1 offset:8960
v_mfma_f32_16x16x32_bf16 v[68:71],a[148:151],v[140:143],v[68:71]
s_waitcnt lgkmcnt(4)
v_exp_f32 v109,v109
v_mfma_f32_16x16x32_bf16 v[68:71],a[144:147],v[132:135],v[68:71]
s_waitcnt lgkmcnt(3)
ds_read_b128 a[188:191],v1 offset:13056
v_cvt_pk_bf16_f32 v106,v108,v109
v_mfma_f32_16x16x32_bf16 v[72:75],a[160:163],v[132:135],v[32:35]
s_waitcnt lgkmcnt(3)
v_exp_f32 v110,v110
v_mfma_f32_16x16x32_bf16 v[72:75],a[164:167],v[140:143],v[72:75]
s_waitcnt lgkmcnt(1)
ds_read_b128 a[184:187],v1 offset:12800
v_mfma_f32_16x16x32_bf16 v[72:75],a[168:171],v[148:151],v[72:75]
global_load_lds_dwordx4 v5,s[10:11] offset:3072
ds_read_b128 a[180:183],v1 offset:12544
v_mfma_f32_16x16x32_bf16 v[72:75],a[172:175],v[156:159],v[72:75]
s_waitcnt lgkmcnt(2)
ds_read_b128 a[176:179],v1 offset:12288
v_add_u32 v1,s78,v1
v_mfma_f32_16x16x32_bf16 v[76:79],a[188:191],v[156:159],v[32:35]
s_waitcnt lgkmcnt(2)
v_exp_f32 v111,v111
v_mfma_f32_16x16x32_bf16 v[76:79],a[184:187],v[148:151],v[76:79]
s_waitcnt lgkmcnt(1)
v_cvt_pk_bf16_f32 v107,v110,v111
v_mfma_f32_16x16x32_bf16 v[76:79],a[180:183],v[140:143],v[76:79]
s_waitcnt lgkmcnt(0)
v_exp_f32 v112,v112
v_mfma_f32_16x16x32_bf16 v[76:79],a[176:179],v[132:135],v[76:79]
v_exp_f32 v113,v113
v_mfma_f32_16x16x32_bf16 a[64:67],a[192:195],v[96:99],0
v_exp_f32 v114,v114
v_mfma_f32_16x16x32_bf16 a[64:67],a[196:199],v[104:107],a[64:67]
v_exp_f32 v115,v115
v_mfma_f32_16x16x32_bf16 a[68:71],a[200:203],v[96:99],0
v_exp_f32 v116,v116
v_mfma_f32_16x16x32_bf16 a[68:71],a[204:207],v[104:107],a[68:71]
v_exp_f32 v117,v117
v_mfma_f32_16x16x32_bf16 a[72:75],a[208:211],v[96:99],0
s_add_u32 s10,s24,s74
s_addc_u32 s11,s25,0
s_add_u32 s74,s74,16384
s_and_b32 s74,s74,0x3ffff
s_or_b32 s74,s74,s69
s_lshl_b32 s69,s60,13
s_add_u32 s69,s69,131072
v_add_u32 v8,s69,v6
v_mfma_f32_16x16x32_bf16 a[72:75],a[212:215],v[104:107],a[72:75]
s_lshl_b32 s69,s60,13
ds_write_b128 v8,v[24:27] offset:4096
v_mfma_f32_16x16x32_bf16 a[76:79],a[216:219],v[96:99],0
s_add_u32 m0,s76,16384
ds_write_b32 v8,v7 offset:5120
v_cvt_pk_bf16_f32 v112,v112,v113
v_mfma_f32_16x16x32_bf16 a[76:79],a[220:223],v[104:107],a[76:79]
s_sub_u32 s76,s76,32768
v_bfe_u32 v24,v6,4,4
v_cvt_pk_bf16_f32 v113,v114,v115
v_mfma_f32_16x16x32_bf16 a[80:83],a[224:227],v[96:99],0
global_load_lds_dwordx4 v4,s[10:11]
v_lshrrev_b32 v7,8,v6
v_lshlrev_b32 v7,4,v7
v_lshl_add_u32 v7,v24,8,v7
v_mfma_f32_16x16x32_bf16 a[80:83],a[228:231],v[104:107],a[80:83]
s_and_b32 s76,s76,0x1ffff
v_exp_f32 v118,v118
v_mfma_f32_16x16x32_bf16 a[84:87],a[232:235],v[96:99],0
s_add_u32 s69,s69,131072
v_exp_f32 v119,v119
v_mfma_f32_16x16x32_bf16 a[84:87],a[236:239],v[104:107],a[84:87]
v_cvt_pk_bf16_f32 v114,v116,v117
v_cvt_pk_bf16_f32 v115,v118,v119
v_mfma_f32_16x16x32_bf16 a[88:91],a[240:243],v[96:99],0
v_exp_f32 v120,v120
v_mfma_f32_16x16x32_bf16 a[88:91],a[244:247],v[104:107],a[88:91]
buffer_load_dwordx4 v[8:11],v7,s[20:23],s65 offen nt
v_mfma_f32_16x16x32_bf16 a[92:95],a[248:251],v[96:99],0
v_exp_f32 v121,v121
v_mfma_f32_16x16x32_bf16 a[92:95],a[252:255],v[104:107],a[92:95]
v_exp_f32 v122,v122
v_mfma_f32_16x16x32_bf16 v[56:59],v[28:31],v[96:99],0
v_exp_f32 v123,v123
v_mfma_f32_16x16x32_bf16 v[56:59],v[28:31],v[104:107],v[56:59]
v_exp_f32 v124,v124
v_mfma_f32_16x16x32_bf16 v[80:83],a[128:131],v[164:167],v[36:39]
buffer_load_dwordx4 v[12:15],v7,s[20:23],s65 offen offset:64 nt
v_mfma_f32_16x16x32_bf16 v[80:83],a[132:135],v[172:175],v[80:83]
v_exp_f32 v125,v125
v_mfma_f32_16x16x32_bf16 v[80:83],a[136:139],v[180:183],v[80:83]
v_exp_f32 v126,v126
v_mfma_f32_16x16x32_bf16 v[80:83],a[140:143],v[188:191],v[80:83]
v_exp_f32 v127,v127
v_mfma_f32_16x16x32_bf16 v[84:87],a[156:159],v[188:191],v[36:39]
v_cvt_pk_bf16_f32 v120,v120,v121
v_cvt_pk_bf16_f32 v121,v122,v123
v_mfma_f32_16x16x32_bf16 v[84:87],a[152:155],v[180:183],v[84:87]
buffer_load_dwordx4 v[16:19],v7,s[20:23],s65 offen offset:128 nt
v_mfma_f32_16x16x32_bf16 v[84:87],a[148:151],v[172:175],v[84:87]
v_cvt_pk_bf16_f32 v122,v124,v125
v_cvt_pk_bf16_f32 v123,v126,v127
v_mfma_f32_16x16x32_bf16 v[84:87],a[144:147],v[164:167],v[84:87]
v_exp_f32 v64,v64
v_mfma_f32_16x16x32_bf16 v[88:91],a[160:163],v[164:167],v[36:39]
v_exp_f32 v65,v65
v_mfma_f32_16x16x32_bf16 v[88:91],a[164:167],v[172:175],v[88:91]
v_exp_f32 v66,v66
v_mfma_f32_16x16x32_bf16 v[88:91],a[168:171],v[180:183],v[88:91]
v_exp_f32 v67,v67
v_mfma_f32_16x16x32_bf16 v[88:91],a[172:175],v[188:191],v[88:91]
global_load_lds_dwordx4 v5,s[10:11] offset:1024
v_cvt_pk_bf16_f32 v64,v64,v65
v_mfma_f32_16x16x32_bf16 v[92:95],a[188:191],v[188:191],v[36:39]
v_exp_f32 v68,v68
v_mfma_f32_16x16x32_bf16 v[92:95],a[184:187],v[180:183],v[92:95]
v_exp_f32 v69,v69
v_mfma_f32_16x16x32_bf16 v[92:95],a[180:183],v[172:175],v[92:95]
v_exp_f32 v70,v70
v_mfma_f32_16x16x32_bf16 v[92:95],a[176:179],v[164:167],v[92:95]
v_exp_f32 v71,v71
v_mfma_f32_16x16x32_bf16 a[96:99],a[192:195],v[112:115],0
buffer_load_dwordx4 v[20:23],v7,s[20:23],s65 offen offset:192 nt
)A"
R"A(v_mfma_f32_16x16x32_bf16 a[96:99],a[196:199],v[120:123],a[96:99]
ds_read_b64_tr_b16 a[192:193],v2
ds_read_b64_tr_b16 a[194:195],v2 offset:4096
v_mfma_f32_16x16x32_bf16 a[100:103],a[200:203],v[112:115],0
ds_read_b64_tr_b16 a[196:197],v2 offset:8192
v_cvt_pk_bf16_f32 v65,v66,v67
v_mfma_f32_16x16x32_bf16 a[100:103],a[204:207],v[120:123],a[100:103]
ds_read_b64_tr_b16 a[198:199],v2 offset:12288
v_cvt_pk_bf16_f32 v66,v68,v69
v_mfma_f32_16x16x32_bf16 a[104:107],a[208:211],v[112:115],0
ds_read_b64_tr_b16 a[200:201],v3
v_cvt_pk_bf16_f32 v67,v70,v71
v_mfma_f32_16x16x32_bf16 a[104:107],a[212:215],v[120:123],a[104:107]
ds_read_b64_tr_b16 a[202:203],v3 offset:4096
ds_read_b64_tr_b16 a[204:205],v3 offset:8192
v_mfma_f32_16x16x32_bf16 a[108:111],a[216:219],v[112:115],0
global_load_lds_dwordx4 v4,s[10:11] offset:2048
ds_read_b64_tr_b16 a[206:207],v3 offset:12288
v_mfma_f32_16x16x32_bf16 a[108:111],a[220:223],v[120:123],a[108:111]
ds_read_b64_tr_b16 a[208:209],v2 offset:256
v_add_u32 v7,s69,v6
v_mfma_f32_16x16x32_bf16 a[112:115],a[224:227],v[112:115],0
s_and_b32 s69,s75,0xfffc0000
v_exp_f32 v72,v72
v_mfma_f32_16x16x32_bf16 a[112:115],a[228:231],v[120:123],a[112:115]
s_add_u32 s75,s75,16384
ds_read_b64_tr_b16 a[210:211],v2 offset:4352
ds_read_b64_tr_b16 a[212:213],v2 offset:8448
v_mfma_f32_16x16x32_bf16 a[116:119],a[232:235],v[112:115],0
v_exp_f32 v73,v73
v_mfma_f32_16x16x32_bf16 a[116:119],a[236:239],v[120:123],a[116:119]
ds_read_b64_tr_b16 a[214:215],v2 offset:12544
ds_read_b64_tr_b16 a[216:217],v3 offset:256
v_mfma_f32_16x16x32_bf16 a[120:123],a[240:243],v[112:115],0
ds_read_b64_tr_b16 a[218:219],v3 offset:4352
v_cvt_pk_bf16_f32 v72,v72,v73
v_mfma_f32_16x16x32_bf16 a[120:123],a[244:247],v[120:123],a[120:123]
v_exp_f32 v74,v74
v_mfma_f32_16x16x32_bf16 a[124:127],a[248:251],v[112:115],0
v_exp_f32 v75,v75
v_mfma_f32_16x16x32_bf16 a[124:127],a[252:255],v[120:123],a[124:127]
v_exp_f32 v76,v76
v_mfma_f32_16x16x32_bf16 v[60:63],v[28:31],v[112:115],0
v_exp_f32 v77,v77
v_mfma_f32_16x16x32_bf16 v[60:63],v[28:31],v[120:123],v[60:63]
global_load_lds_dwordx4 v5,s[10:11] offset:3072
ds_read_b64_tr_b16 a[220:221],v3 offset:8448
v_mfma_f32_16x16x32_bf16 v[96:99],a[128:131],v[196:199],v[40:43]
v_exp_f32 v78,v78
v_mfma_f32_16x16x32_bf16 v[96:99],a[132:135],v[204:207],v[96:99]
v_exp_f32 v79,v79
v_mfma_f32_16x16x32_bf16 v[96:99],a[136:139],v[212:215],v[96:99]
ds_read_b64_tr_b16 a[222:223],v3 offset:12544
v_cvt_pk_bf16_f32 v73,v74,v75
v_mfma_f32_16x16x32_bf16 v[96:99],a[140:143],v[220:223],v[96:99]
v_cvt_pk_bf16_f32 v74,v76,v77
v_cvt_pk_bf16_f32 v75,v78,v79
v_mfma_f32_16x16x32_bf16 v[100:103],a[156:159],v[220:223],v[40:43]
ds_read_b64_tr_b16 a[224:225],v2 offset:512
ds_read_b64_tr_b16 a[226:227],v2 offset:4608
v_mfma_f32_16x16x32_bf16 v[100:103],a[152:155],v[212:215],v[100:103]
v_exp_f32 v80,v80
v_mfma_f32_16x16x32_bf16 v[100:103],a[148:151],v[204:207],v[100:103]
ds_read_b64_tr_b16 a[228:229],v2 offset:8704
ds_read_b64_tr_b16 a[230:231],v2 offset:12800
v_mfma_f32_16x16x32_bf16 v[100:103],a[144:147],v[196:199],v[100:103]
v_exp_f32 v81,v81
v_mfma_f32_16x16x32_bf16 v[104:107],a[160:163],v[196:199],v[40:43]
ds_read_b64_tr_b16 a[232:233],v3 offset:512
ds_read_b64_tr_b16 a[234:235],v3 offset:4608
v_mfma_f32_16x16x32_bf16 v[104:107],a[164:167],v[204:207],v[104:107]
s_add_u32 s10,s20,s74
s_addc_u32 s11,s21,0
ds_read_b64_tr_b16 a[236:237],v3 offset:8704
v_mfma_f32_16x16x32_bf16 v[104:107],a[168:171],v[212:215],v[104:107]
ds_read_b64_tr_b16 a[238:239],v3 offset:12800
v_cvt_pk_bf16_f32 v80,v80,v81
v_mfma_f32_16x16x32_bf16 v[104:107],a[172:175],v[220:223],v[104:107]
s_add_u32 m0,s76,0
v_exp_f32 v82,v82
v_mfma_f32_16x16x32_bf16 v[108:111],a[188:191],v[220:223],v[40:43]
ds_read_b64_tr_b16 a[240:241],v2 offset:768
ds_read_b64_tr_b16 a[242:243],v2 offset:4864
v_mfma_f32_16x16x32_bf16 v[108:111],a[184:187],v[212:215],v[108:111]
global_load_lds_dwordx4 v4,s[10:11] nt
ds_read_b64_tr_b16 a[244:245],v2 offset:8960
v_mfma_f32_16x16x32_bf16 v[108:111],a[180:183],v[204:207],v[108:111]
ds_read_b64_tr_b16 a[246:247],v2 offset:13056
v_add_u32 v2,s78,v2
v_mfma_f32_16x16x32_bf16 v[108:111],a[176:179],v[196:199],v[108:111]
s_waitcnt lgkmcnt(8)
v_exp_f32 v83,v83
v_mfma_f32_16x16x32_bf16 a[0:3],a[192:195],v[64:67],a[0:3]
ds_read_b64_tr_b16 a[248:249],v3 offset:768
ds_read_b64_tr_b16 a[250:251],v3 offset:4864
v_mfma_f32_16x16x32_bf16 a[0:3],a[196:199],v[72:75],a[0:3]
ds_read_b64_tr_b16 a[252:253],v3 offset:8960
v_cvt_pk_bf16_f32 v81,v82,v83
v_mfma_f32_16x16x32_bf16 a[4:7],a[200:203],v[64:67],a[4:7]
ds_read_b64_tr_b16 a[254:255],v3 offset:13056
v_add_u32 v3,s78,v3
v_mfma_f32_16x16x32_bf16 a[4:7],a[204:207],v[72:75],a[4:7]
L_pd5_%=:
s_cbranch_execz L_trt0_%=
s_sub_u32 s78,s77,32768
v_exp_f32 v84,v84
v_mfma_f32_16x16x32_bf16 a[8:11],a[208:211],v[64:67],a[8:11]
s_and_b32 s78,s78,0x1ffff
v_exp_f32 v85,v85
v_mfma_f32_16x16x32_bf16 a[8:11],a[212:215],v[72:75],a[8:11]
s_sub_u32 s78,s78,s77
v_exp_f32 v86,v86
v_mfma_f32_16x16x32_bf16 a[12:15],a[216:219],v[64:67],a[12:15]
s_add_u32 s77,s77,s78
v_exp_f32 v87,v87
v_mfma_f32_16x16x32_bf16 a[12:15],a[220:223],v[72:75],a[12:15]
v_cvt_pk_bf16_f32 v82,v84,v85
v_cvt_pk_bf16_f32 v83,v86,v87
v_mfma_f32_16x16x32_bf16 a[16:19],a[224:227],v[64:67],a[16:19]
v_exp_f32 v88,v88
v_mfma_f32_16x16x32_bf16 a[16:19],a[228:231],v[72:75],a[16:19]
s_waitcnt lgkmcnt(1)
v_exp_f32 v89,v89
v_mfma_f32_16x16x32_bf16 a[20:23],a[232:235],v[64:67],a[20:23]
v_exp_f32 v90,v90
v_mfma_f32_16x16x32_bf16 a[20:23],a[236:239],v[72:75],a[20:23]
v_exp_f32 v91,v91
v_mfma_f32_16x16x32_bf16 a[24:27],a[240:243],v[64:67],a[24:27]
v_exp_f32 v92,v92
v_mfma_f32_16x16x32_bf16 a[24:27],a[244:247],v[72:75],a[24:27]
v_exp_f32 v93,v93
v_mfma_f32_16x16x32_bf16 a[28:31],a[248:251],v[64:67],a[28:31]
s_waitcnt lgkmcnt(0)
v_exp_f32 v94,v94
v_mfma_f32_16x16x32_bf16 a[28:31],a[252:255],v[72:75],a[28:31]
s_getpc_b64 s[92:93]
s_cmp_lg_u32 s4,0
s_cbranch_scc1 L_pace_%=
v_mfma_f32_16x16x32_bf16 v[48:51],v[28:31],v[64:67],v[48:51]
v_exp_f32 v95,v95
v_mfma_f32_16x16x32_bf16 v[48:51],v[28:31],v[72:75],v[48:51]
s_add_u32 s10,s20,s74
s_addc_u32 s11,s21,0
v_cvt_pk_bf16_f32 v88,v88,v89
s_waitcnt vmcnt(13)
s_barrier
v_mfma_f32_16x16x32_bf16 v[112:115],a[128:131],v[228:231],v[44:47]
s_add_u32 m0,s76,0
v_cvt_pk_bf16_f32 v89,v90,v91
v_cvt_pk_bf16_f32 v90,v92,v93
v_mfma_f32_16x16x32_bf16 v[112:115],a[132:135],v[236:239],v[112:115]
ds_read_b128 a[128:131],v1
v_cvt_pk_bf16_f32 v91,v94,v95
v_mfma_f32_16x16x32_bf16 v[112:115],a[136:139],v[244:247],v[112:115]
global_load_lds_dwordx4 v4,s[10:11] offset:1024 nt
v_mfma_f32_16x16x32_bf16 v[112:115],a[140:143],v[252:255],v[112:115]
v_exp_f32 v96,v96
v_mfma_f32_16x16x32_bf16 v[116:119],a[156:159],v[252:255],v[44:47]
v_exp_f32 v97,v97
v_mfma_f32_16x16x32_bf16 v[116:119],a[152:155],v[244:247],v[116:119]
ds_read_b128 a[132:135],v1 offset:256
v_cvt_pk_bf16_f32 v96,v96,v97
v_mfma_f32_16x16x32_bf16 v[116:119],a[148:151],v[236:239],v[116:119]
v_exp_f32 v98,v98
v_mfma_f32_16x16x32_bf16 v[116:119],a[144:147],v[228:231],v[116:119]
v_exp_f32 v99,v99
v_mfma_f32_16x16x32_bf16 v[120:123],a[160:163],v[228:231],v[44:47]
ds_read_b128 a[136:139],v1 offset:512
v_cvt_pk_bf16_f32 v97,v98,v99
v_mfma_f32_16x16x32_bf16 v[120:123],a[164:167],v[236:239],v[120:123]
v_exp_f32 v100,v100
v_mfma_f32_16x16x32_bf16 v[120:123],a[168:171],v[244:247],v[120:123]
v_exp_f32 v101,v101
v_mfma_f32_16x16x32_bf16 v[120:123],a[172:175],v[252:255],v[120:123]
ds_read_b128 a[140:143],v1 offset:768
v_cvt_pk_bf16_f32 v98,v100,v101
v_mfma_f32_16x16x32_bf16 v[124:127],a[188:191],v[252:255],v[44:47]
v_exp_f32 v102,v102
v_mfma_f32_16x16x32_bf16 v[124:127],a[184:187],v[244:247],v[124:127]
v_exp_f32 v103,v103
v_mfma_f32_16x16x32_bf16 v[124:127],a[180:183],v[236:239],v[124:127]
ds_read_b128 a[156:159],v1 offset:4864
v_cvt_pk_bf16_f32 v99,v102,v103
v_mfma_f32_16x16x32_bf16 v[124:127],a[176:179],v[228:231],v[124:127]
v_exp_f32 v104,v104
v_mfma_f32_16x16x32_bf16 a[32:35],a[192:195],v[80:83],a[32:35]
global_load_lds_dwordx4 v5,s[10:11] offset:2048 nt
ds_read_b128 a[152:155],v1 offset:4608
v_mfma_f32_16x16x32_bf16 a[32:35],a[196:199],v[88:91],a[32:35]
v_exp_f32 v105,v105
v_mfma_f32_16x16x32_bf16 a[36:39],a[200:203],v[80:83],a[36:39]
ds_read_b128 a[148:151],v1 offset:4352
v_cvt_pk_bf16_f32 v104,v104,v105
v_mfma_f32_16x16x32_bf16 a[36:39],a[204:207],v[88:91],a[36:39]
v_exp_f32 v106,v106
v_mfma_f32_16x16x32_bf16 a[40:43],a[208:211],v[80:83],a[40:43]
v_exp_f32 v107,v107
v_mfma_f32_16x16x32_bf16 a[40:43],a[212:215],v[88:91],a[40:43]
ds_read_b128 a[144:147],v1 offset:4096
v_cvt_pk_bf16_f32 v105,v106,v107
v_mfma_f32_16x16x32_bf16 a[44:47],a[216:219],v[80:83],a[44:47]
v_exp_f32 v108,v108
v_mfma_f32_16x16x32_bf16 a[44:47],a[220:223],v[88:91],a[44:47]
v_exp_f32 v109,v109
v_mfma_f32_16x16x32_bf16 a[48:51],a[224:227],v[80:83],a[48:51]
ds_read_b128 a[160:163],v1 offset:8192
v_cvt_pk_bf16_f32 v106,v108,v109
v_mfma_f32_16x16x32_bf16 a[48:51],a[228:231],v[88:91],a[48:51]
v_exp_f32 v110,v110
v_mfma_f32_16x16x32_bf16 a[52:55],a[232:235],v[80:83],a[52:55]
v_exp_f32 v111,v111
v_mfma_f32_16x16x32_bf16 a[52:55],a[236:239],v[88:91],a[52:55]
ds_read_b128 a[164:167],v1 offset:8448
v_cvt_pk_bf16_f32 v107,v110,v111
v_mfma_f32_16x16x32_bf16 a[56:59],a[240:243],v[80:83],a[56:59]
v_exp_f32 v112,v112
v_mfma_f32_16x16x32_bf16 a[56:59],a[244:247],v[88:91],a[56:59]
v_exp_f32 v113,v113
v_mfma_f32_16x16x32_bf16 a[60:63],a[248:251],v[80:83],a[60:63]
global_load_lds_dwordx4 v5,s[10:11] offset:3072
ds_read_b128 a[168:171],v1 offset:8704
v_mfma_f32_16x16x32_bf16 a[60:63],a[252:255],v[88:91],a[60:63]
v_exp_f32 v114,v114
v_mfma_f32_16x16x32_bf16 v[52:55],v[28:31],v[80:83],v[52:55]
ds_read_b128 a[172:175],v1 offset:8960
v_cvt_pk_bf16_f32 v112,v112,v113
v_mfma_f32_16x16x32_bf16 v[52:55],v[28:31],v[88:91],v[52:55]
s_waitcnt lgkmcnt(3)
v_exp_f32 v115,v115
v_mfma_f32_16x16x32_bf16 v[64:67],a[128:131],v[132:135],v[32:35]
ds_read_b128 a[188:191],v1 offset:13056
v_cvt_pk_bf16_f32 v113,v114,v115
v_mfma_f32_16x16x32_bf16 v[64:67],a[132:135],v[140:143],v[64:67]
v_exp_f32 v116,v116
v_mfma_f32_16x16x32_bf16 v[64:67],a[136:139],v[148:151],v[64:67]
ds_read_b128 a[184:187],v1 offset:12800
v_mfma_f32_16x16x32_bf16 v[64:67],a[140:143],v[156:159],v[64:67]
v_exp_f32 v117,v117
v_mfma_f32_16x16x32_bf16 v[68:71],a[156:159],v[156:159],v[32:35]
ds_read_b128 a[180:183],v1 offset:12544
v_cvt_pk_bf16_f32 v114,v116,v117
v_mfma_f32_16x16x32_bf16 v[68:71],a[152:155],v[148:151],v[68:71]
s_waitcnt lgkmcnt(2)
v_exp_f32 v118,v118
v_mfma_f32_16x16x32_bf16 v[68:71],a[148:151],v[140:143],v[68:71]
s_add_u32 s10,s24,s74
s_addc_u32 s11,s25,0
ds_read_b128 a[176:179],v1 offset:12288
v_mfma_f32_16x16x32_bf16 v[68:71],a[144:147],v[132:135],v[68:71]
s_add_u32 s74,s74,16384
v_exp_f32 v119,v119
v_mfma_f32_16x16x32_bf16 v[72:75],a[160:163],v[132:135],v[32:35]
s_add_u32 m0,s76,16384
s_and_b32 s74,s74,0x3ffff
v_cvt_pk_bf16_f32 v115,v118,v119
v_mfma_f32_16x16x32_bf16 v[72:75],a[164:167],v[140:143],v[72:75]
s_or_b32 s74,s74,s69
v_exp_f32 v120,v120
v_mfma_f32_16x16x32_bf16 v[72:75],a[168:171],v[148:151],v[72:75]
s_sub_u32 s76,s76,32768
global_load_lds_dwordx4 v4,s[10:11]
v_mfma_f32_16x16x32_bf16 v[72:75],a[172:175],v[156:159],v[72:75]
s_and_b32 s76,s76,0x1ffff
v_exp_f32 v121,v121
v_mfma_f32_16x16x32_bf16 v[76:79],a[188:191],v[156:159],v[32:35]
s_and_b32 s69,s75,0xfffc0000
v_exp_f32 v122,v122
s_waitcnt lgkmcnt(1)
v_mfma_f32_16x16x32_bf16 v[76:79],a[184:187],v[148:151],v[76:79]
v_exp_f32 v123,v123
v_mfma_f32_16x16x32_bf16 v[76:79],a[180:183],v[140:143],v[76:79]
s_waitcnt lgkmcnt(0)
v_exp_f32 v124,v124
v_mfma_f32_16x16x32_bf16 v[76:79],a[176:179],v[132:135],v[76:79]
v_exp_f32 v125,v125
v_mfma_f32_16x16x32_bf16 a[64:67],a[192:195],v[96:99],a[64:67]
v_exp_f32 v126,v126
v_mfma_f32_16x16x32_bf16 a[64:67],a[196:199],v[104:107],a[64:67]
v_exp_f32 v127,v127
v_mfma_f32_16x16x32_bf16 a[68:71],a[200:203],v[96:99],a[68:71]
v_cvt_pk_bf16_f32 v120,v120,v121
v_cvt_pk_bf16_f32 v121,v122,v123
v_mfma_f32_16x16x32_bf16 a[68:71],a[204:207],v[104:107],a[68:71]
v_cvt_pk_bf16_f32 v122,v124,v125
v_cvt_pk_bf16_f32 v123,v126,v127
)A"
R"A(v_mfma_f32_16x16x32_bf16 a[72:75],a[208:211],v[96:99],a[72:75]
v_exp_f32 v64,v64
v_mfma_f32_16x16x32_bf16 a[72:75],a[212:215],v[104:107],a[72:75]
v_exp_f32 v65,v65
v_mfma_f32_16x16x32_bf16 a[76:79],a[216:219],v[96:99],a[76:79]
v_exp_f32 v66,v66
v_mfma_f32_16x16x32_bf16 a[76:79],a[220:223],v[104:107],a[76:79]
v_exp_f32 v67,v67
v_mfma_f32_16x16x32_bf16 a[80:83],a[224:227],v[96:99],a[80:83]
global_load_lds_dwordx4 v5,s[10:11] offset:1024
v_cvt_pk_bf16_f32 v64,v64,v65
v_mfma_f32_16x16x32_bf16 a[80:83],a[228:231],v[104:107],a[80:83]
v_exp_f32 v68,v68
v_mfma_f32_16x16x32_bf16 a[84:87],a[232:235],v[96:99],a[84:87]
v_exp_f32 v69,v69
v_mfma_f32_16x16x32_bf16 a[84:87],a[236:239],v[104:107],a[84:87]
v_exp_f32 v70,v70
v_mfma_f32_16x16x32_bf16 a[88:91],a[240:243],v[96:99],a[88:91]
v_exp_f32 v71,v71
v_mfma_f32_16x16x32_bf16 a[88:91],a[244:247],v[104:107],a[88:91]
v_cvt_pk_bf16_f32 v65,v66,v67
v_cvt_pk_bf16_f32 v66,v68,v69
v_mfma_f32_16x16x32_bf16 a[92:95],a[248:251],v[96:99],a[92:95]
v_cvt_pk_bf16_f32 v67,v70,v71
v_add_u32 v1,s78,v1
v_mfma_f32_16x16x32_bf16 a[92:95],a[252:255],v[104:107],a[92:95]
v_exp_f32 v72,v72
v_mfma_f32_16x16x32_bf16 v[56:59],v[28:31],v[96:99],v[56:59]
v_exp_f32 v73,v73
v_mfma_f32_16x16x32_bf16 v[56:59],v[28:31],v[104:107],v[56:59]
v_exp_f32 v74,v74
v_mfma_f32_16x16x32_bf16 v[80:83],a[128:131],v[164:167],v[36:39]
v_exp_f32 v75,v75
v_mfma_f32_16x16x32_bf16 v[80:83],a[132:135],v[172:175],v[80:83]
v_exp_f32 v76,v76
v_mfma_f32_16x16x32_bf16 v[80:83],a[136:139],v[180:183],v[80:83]
v_exp_f32 v77,v77
v_mfma_f32_16x16x32_bf16 v[80:83],a[140:143],v[188:191],v[80:83]
v_exp_f32 v78,v78
v_mfma_f32_16x16x32_bf16 v[84:87],a[156:159],v[188:191],v[36:39]
v_exp_f32 v79,v79
v_mfma_f32_16x16x32_bf16 v[84:87],a[152:155],v[180:183],v[84:87]
global_load_lds_dwordx4 v4,s[10:11] offset:2048
v_cvt_pk_bf16_f32 v72,v72,v73
v_mfma_f32_16x16x32_bf16 v[84:87],a[148:151],v[172:175],v[84:87]
v_cvt_pk_bf16_f32 v73,v74,v75
v_cvt_pk_bf16_f32 v74,v76,v77
v_mfma_f32_16x16x32_bf16 v[84:87],a[144:147],v[164:167],v[84:87]
v_exp_f32 v80,v80
v_mfma_f32_16x16x32_bf16 v[88:91],a[160:163],v[164:167],v[36:39]
v_exp_f32 v81,v81
v_mfma_f32_16x16x32_bf16 v[88:91],a[164:167],v[172:175],v[88:91]
v_cvt_pk_bf16_f32 v75,v78,v79
v_cvt_pk_bf16_f32 v80,v80,v81
v_mfma_f32_16x16x32_bf16 v[88:91],a[168:171],v[180:183],v[88:91]
v_exp_f32 v82,v82
v_mfma_f32_16x16x32_bf16 v[88:91],a[172:175],v[188:191],v[88:91]
v_exp_f32 v83,v83
v_mfma_f32_16x16x32_bf16 v[92:95],a[188:191],v[188:191],v[36:39]
v_exp_f32 v84,v84
v_mfma_f32_16x16x32_bf16 v[92:95],a[184:187],v[180:183],v[92:95]
v_exp_f32 v85,v85
v_mfma_f32_16x16x32_bf16 v[92:95],a[180:183],v[172:175],v[92:95]
v_exp_f32 v86,v86
v_mfma_f32_16x16x32_bf16 v[92:95],a[176:179],v[164:167],v[92:95]
v_exp_f32 v87,v87
v_mfma_f32_16x16x32_bf16 a[96:99],a[192:195],v[112:115],a[96:99]
ds_read_b64_tr_b16 a[192:193],v2
ds_read_b64_tr_b16 a[194:195],v2 offset:4096
v_mfma_f32_16x16x32_bf16 a[96:99],a[196:199],v[120:123],a[96:99]
ds_read_b64_tr_b16 a[196:197],v2 offset:8192
v_cvt_pk_bf16_f32 v81,v82,v83
v_mfma_f32_16x16x32_bf16 a[100:103],a[200:203],v[112:115],a[100:103]
ds_read_b64_tr_b16 a[198:199],v2 offset:12288
v_cvt_pk_bf16_f32 v82,v84,v85
v_mfma_f32_16x16x32_bf16 a[100:103],a[204:207],v[120:123],a[100:103]
global_load_lds_dwordx4 v5,s[10:11] offset:3072
ds_read_b64_tr_b16 a[200:201],v3
v_mfma_f32_16x16x32_bf16 a[104:107],a[208:211],v[112:115],a[104:107]
ds_read_b64_tr_b16 a[202:203],v3 offset:4096
ds_read_b64_tr_b16 a[204:205],v3 offset:8192
v_mfma_f32_16x16x32_bf16 a[104:107],a[212:215],v[120:123],a[104:107]
ds_read_b64_tr_b16 a[206:207],v3 offset:12288
v_cvt_pk_bf16_f32 v83,v86,v87
v_mfma_f32_16x16x32_bf16 a[108:111],a[216:219],v[112:115],a[108:111]
v_exp_f32 v88,v88
v_mfma_f32_16x16x32_bf16 a[108:111],a[220:223],v[120:123],a[108:111]
ds_read_b64_tr_b16 a[208:209],v2 offset:256
ds_read_b64_tr_b16 a[210:211],v2 offset:4352
v_mfma_f32_16x16x32_bf16 a[112:115],a[224:227],v[112:115],a[112:115]
v_exp_f32 v89,v89
v_mfma_f32_16x16x32_bf16 a[112:115],a[228:231],v[120:123],a[112:115]
ds_read_b64_tr_b16 a[212:213],v2 offset:8448
ds_read_b64_tr_b16 a[214:215],v2 offset:12544
v_mfma_f32_16x16x32_bf16 a[116:119],a[232:235],v[112:115],a[116:119]
ds_read_b64_tr_b16 a[216:217],v3 offset:256
v_cvt_pk_bf16_f32 v88,v88,v89
v_mfma_f32_16x16x32_bf16 a[116:119],a[236:239],v[120:123],a[116:119]
v_exp_f32 v90,v90
v_mfma_f32_16x16x32_bf16 a[120:123],a[240:243],v[112:115],a[120:123]
ds_read_b64_tr_b16 a[218:219],v3 offset:4352
ds_read_b64_tr_b16 a[220:221],v3 offset:8448
v_mfma_f32_16x16x32_bf16 a[120:123],a[244:247],v[120:123],a[120:123]
s_add_u32 s10,s20,s74
s_addc_u32 s11,s21,0
ds_read_b64_tr_b16 a[222:223],v3 offset:12544
v_mfma_f32_16x16x32_bf16 a[124:127],a[248:251],v[112:115],a[124:127]
v_exp_f32 v91,v91
v_mfma_f32_16x16x32_bf16 a[124:127],a[252:255],v[120:123],a[124:127]
s_add_u32 m0,s76,0
ds_read_b64_tr_b16 a[224:225],v2 offset:512
ds_read_b64_tr_b16 a[226:227],v2 offset:4608
v_mfma_f32_16x16x32_bf16 v[60:63],v[28:31],v[112:115],v[60:63]
ds_read_b64_tr_b16 a[228:229],v2 offset:8704
v_cvt_pk_bf16_f32 v89,v90,v91
v_mfma_f32_16x16x32_bf16 v[60:63],v[28:31],v[120:123],v[60:63]
global_load_lds_dwordx4 v4,s[10:11] nt
ds_read_b64_tr_b16 a[230:231],v2 offset:12800
v_mfma_f32_16x16x32_bf16 v[96:99],a[128:131],v[196:199],v[40:43]
v_exp_f32 v92,v92
v_mfma_f32_16x16x32_bf16 v[96:99],a[132:135],v[204:207],v[96:99]
ds_read_b64_tr_b16 a[232:233],v3 offset:512
ds_read_b64_tr_b16 a[234:235],v3 offset:4608
v_mfma_f32_16x16x32_bf16 v[96:99],a[136:139],v[212:215],v[96:99]
v_exp_f32 v93,v93
v_mfma_f32_16x16x32_bf16 v[96:99],a[140:143],v[220:223],v[96:99]
ds_read_b64_tr_b16 a[236:237],v3 offset:8704
ds_read_b64_tr_b16 a[238:239],v3 offset:12800
v_mfma_f32_16x16x32_bf16 v[100:103],a[156:159],v[220:223],v[40:43]
ds_read_b64_tr_b16 a[240:241],v2 offset:768
v_cvt_pk_bf16_f32 v90,v92,v93
v_mfma_f32_16x16x32_bf16 v[100:103],a[152:155],v[212:215],v[100:103]
v_exp_f32 v94,v94
v_mfma_f32_16x16x32_bf16 v[100:103],a[148:151],v[204:207],v[100:103]
ds_read_b64_tr_b16 a[242:243],v2 offset:4864
ds_read_b64_tr_b16 a[244:245],v2 offset:8960
v_mfma_f32_16x16x32_bf16 v[100:103],a[144:147],v[196:199],v[100:103]
ds_read_b64_tr_b16 a[246:247],v2 offset:13056
v_add_u32 v2,s78,v2
v_mfma_f32_16x16x32_bf16 v[104:107],a[160:163],v[196:199],v[40:43]
v_exp_f32 v95,v95
v_mfma_f32_16x16x32_bf16 v[104:107],a[164:167],v[204:207],v[104:107]
ds_read_b64_tr_b16 a[248:249],v3 offset:768
ds_read_b64_tr_b16 a[250:251],v3 offset:4864
v_mfma_f32_16x16x32_bf16 v[104:107],a[168:171],v[212:215],v[104:107]
ds_read_b64_tr_b16 a[252:253],v3 offset:8960
v_cvt_pk_bf16_f32 v91,v94,v95
v_mfma_f32_16x16x32_bf16 v[104:107],a[172:175],v[220:223],v[104:107]
ds_read_b64_tr_b16 a[254:255],v3 offset:13056
v_add_u32 v3,s78,v3
v_mfma_f32_16x16x32_bf16 v[108:111],a[188:191],v[220:223],v[40:43]
s_sub_u32 s78,s77,32768
v_exp_f32 v96,v96
v_mfma_f32_16x16x32_bf16 v[108:111],a[184:187],v[212:215],v[108:111]
s_and_b32 s78,s78,0x1ffff
global_load_lds_dwordx4 v4,s[10:11] offset:1024 nt
v_mfma_f32_16x16x32_bf16 v[108:111],a[180:183],v[204:207],v[108:111]
s_sub_u32 s78,s78,s77
v_exp_f32 v97,v97
v_mfma_f32_16x16x32_bf16 v[108:111],a[176:179],v[196:199],v[108:111]
s_add_u32 s77,s77,s78
v_exp_f32 v98,v98
s_waitcnt lgkmcnt(4)
v_mfma_f32_16x16x32_bf16 a[0:3],a[192:195],v[64:67],a[0:3]
v_exp_f32 v99,v99
v_mfma_f32_16x16x32_bf16 a[0:3],a[196:199],v[72:75],a[0:3]
v_exp_f32 v100,v100
v_mfma_f32_16x16x32_bf16 a[4:7],a[200:203],v[64:67],a[4:7]
v_exp_f32 v101,v101
v_mfma_f32_16x16x32_bf16 a[4:7],a[204:207],v[72:75],a[4:7]
v_exp_f32 v102,v102
v_mfma_f32_16x16x32_bf16 a[8:11],a[208:211],v[64:67],a[8:11]
v_exp_f32 v103,v103
v_mfma_f32_16x16x32_bf16 a[8:11],a[212:215],v[72:75],a[8:11]
v_cvt_pk_bf16_f32 v96,v96,v97
v_cvt_pk_bf16_f32 v97,v98,v99
v_mfma_f32_16x16x32_bf16 a[12:15],a[216:219],v[64:67],a[12:15]
v_cvt_pk_bf16_f32 v98,v100,v101
v_cvt_pk_bf16_f32 v99,v102,v103
v_mfma_f32_16x16x32_bf16 a[12:15],a[220:223],v[72:75],a[12:15]
v_exp_f32 v104,v104
v_mfma_f32_16x16x32_bf16 a[16:19],a[224:227],v[64:67],a[16:19]
v_exp_f32 v105,v105
v_mfma_f32_16x16x32_bf16 a[16:19],a[228:231],v[72:75],a[16:19]
v_exp_f32 v106,v106
v_mfma_f32_16x16x32_bf16 a[20:23],a[232:235],v[64:67],a[20:23]
s_getreg_b32 s92,hwreg(HW_REG_IB_STS,0,4)
s_min_u32 s4,s92,s6
s_mov_b32 s6,s92
v_mfma_f32_16x16x32_bf16 a[20:23],a[236:239],v[72:75],a[20:23]
s_cmp_gt_u32 s4,s9
s_cselect_b32 s4,15,s5
v_cvt_pk_bf16_f32 v104,v104,v105
v_mfma_f32_16x16x32_bf16 a[24:27],a[240:243],v[64:67],a[24:27]
v_exp_f32 v107,v107
v_mfma_f32_16x16x32_bf16 a[24:27],a[244:247],v[72:75],a[24:27]
s_waitcnt lgkmcnt(0)
v_exp_f32 v108,v108
v_mfma_f32_16x16x32_bf16 a[28:31],a[248:251],v[64:67],a[28:31]
v_exp_f32 v109,v109
v_mfma_f32_16x16x32_bf16 a[28:31],a[252:255],v[72:75],a[28:31]
s_getpc_b64 s[92:93]
s_cmp_lg_u32 s4,0
s_cbranch_scc1 L_pace_%=
v_mfma_f32_16x16x32_bf16 v[48:51],v[28:31],v[64:67],v[48:51]
v_exp_f32 v110,v110
v_mfma_f32_16x16x32_bf16 v[48:51],v[28:31],v[72:75],v[48:51]
s_add_u32 s10,s20,s74
s_addc_u32 s11,s21,0
v_cvt_pk_bf16_f32 v105,v106,v107
s_waitcnt vmcnt(10)
s_barrier
v_mfma_f32_16x16x32_bf16 v[24:27],v[8:11],v[128:131],0
s_add_u32 m0,s76,0
v_exp_f32 v111,v111
v_mfma_f32_16x16x32_bf16 v[24:27],v[12:15],v[136:139],v[24:27]
v_cvt_pk_bf16_f32 v106,v108,v109
v_cvt_pk_bf16_f32 v107,v110,v111
v_mfma_f32_16x16x32_bf16 v[24:27],v[16:19],v[144:147],v[24:27]
global_load_lds_dwordx4 v5,s[10:11] offset:2048 nt
v_mfma_f32_16x16x32_bf16 v[24:27],v[20:23],v[152:155],v[24:27]
v_mfma_f32_16x16x32_bf16 v[112:115],a[128:131],v[228:231],v[44:47]
ds_read_b128 a[128:131],v1
v_mfma_f32_16x16x32_bf16 v[112:115],a[132:135],v[236:239],v[112:115]
v_mfma_f32_16x16x32_bf16 v[112:115],a[136:139],v[244:247],v[112:115]
ds_read_b128 a[132:135],v1 offset:256
v_mfma_f32_16x16x32_bf16 v[112:115],a[140:143],v[252:255],v[112:115]
v_mfma_f32_16x16x32_bf16 v[116:119],a[156:159],v[252:255],v[44:47]
ds_read_b128 a[136:139],v1 offset:512
v_mfma_f32_16x16x32_bf16 v[116:119],a[152:155],v[244:247],v[116:119]
v_max3_f32 v24,v24,v25,v26
v_max_f32 v24,v24,v27
v_mfma_f32_16x16x32_bf16 v[116:119],a[148:151],v[236:239],v[116:119]
ds_read_b128 a[140:143],v1 offset:768
v_mov_b32 v25,v24
v_mfma_f32_16x16x32_bf16 v[116:119],a[144:147],v[228:231],v[116:119]
v_exp_f32 v112,v112
v_mfma_f32_16x16x32_bf16 v[120:123],a[160:163],v[228:231],v[44:47]
v_permlane32_swap_b32 v24,v25
v_mfma_f32_16x16x32_bf16 v[120:123],a[164:167],v[236:239],v[120:123]
v_max_f32 v24,v24,v25
v_mov_b32 v25,v24
v_mfma_f32_16x16x32_bf16 v[120:123],a[168:171],v[244:247],v[120:123]
v_exp_f32 v113,v113
v_mfma_f32_16x16x32_bf16 v[120:123],a[172:175],v[252:255],v[120:123]
v_permlane16_swap_b32 v24,v25
v_mfma_f32_16x16x32_bf16 v[124:127],a[188:191],v[252:255],v[44:47]
global_load_lds_dwordx4 v5,s[10:11] offset:3072
v_max_f32 v24,v24,v25
v_sub_f32 v27,0,v24
v_sub_f32 v26,0,v24
v_sub_f32 v25,0,v24
v_sub_f32 v24,0,v24
v_mfma_f32_16x16x32_bf16 v[124:127],a[184:187],v[244:247],v[124:127]
ds_write_b128 v7,v[24:27]
v_mfma_f32_16x16x32_bf16 v[124:127],a[180:183],v[236:239],v[124:127]
v_exp_f32 v114,v114
v_mfma_f32_16x16x32_bf16 v[124:127],a[176:179],v[228:231],v[124:127]
ds_read_b128 a[156:159],v1 offset:4864
v_cvt_pk_bf16_f32 v112,v112,v113
v_mfma_f32_16x16x32_bf16 v[24:27],v[8:11],v[160:163],0
v_exp_f32 v115,v115
v_mfma_f32_16x16x32_bf16 v[24:27],v[12:15],v[168:171],v[24:27]
ds_read_b128 a[152:155],v1 offset:4608
v_cvt_pk_bf16_f32 v113,v114,v115
v_mfma_f32_16x16x32_bf16 v[24:27],v[16:19],v[176:179],v[24:27]
v_exp_f32 v116,v116
v_mfma_f32_16x16x32_bf16 v[24:27],v[20:23],v[184:187],v[24:27]
v_exp_f32 v117,v117
v_mfma_f32_16x16x32_bf16 a[32:35],a[192:195],v[80:83],a[32:35]
ds_read_b128 a[148:151],v1 offset:4352
v_cvt_pk_bf16_f32 v114,v116,v117
v_mfma_f32_16x16x32_bf16 a[32:35],a[196:199],v[88:91],a[32:35]
v_exp_f32 v118,v118
v_mfma_f32_16x16x32_bf16 a[36:39],a[200:203],v[80:83],a[36:39]
s_add_u32 s10,s24,s74
s_addc_u32 s11,s25,0
ds_read_b128 a[144:147],v1 offset:4096
v_mfma_f32_16x16x32_bf16 a[36:39],a[204:207],v[88:91],a[36:39]
s_add_u32 s74,s74,16384
v_max3_f32 v24,v24,v25,v26
)A"
R"A(v_max_f32 v24,v24,v27
v_mfma_f32_16x16x32_bf16 a[40:43],a[208:211],v[80:83],a[40:43]
s_add_u32 m0,s76,16384
ds_read_b128 a[160:163],v1 offset:8192
v_mov_b32 v25,v24
v_mfma_f32_16x16x32_bf16 a[40:43],a[212:215],v[88:91],a[40:43]
s_and_b32 s74,s74,0x3ffff
s_or_b32 s74,s74,s69
s_sub_u32 s76,s76,32768
v_mfma_f32_16x16x32_bf16 a[44:47],a[216:219],v[80:83],a[44:47]
global_load_lds_dwordx4 v4,s[10:11]
ds_read_b128 a[164:167],v1 offset:8448
v_mfma_f32_16x16x32_bf16 a[44:47],a[220:223],v[88:91],a[44:47]
s_and_b32 s76,s76,0x1ffff
v_permlane32_swap_b32 v24,v25
v_mfma_f32_16x16x32_bf16 a[48:51],a[224:227],v[80:83],a[48:51]
v_max_f32 v24,v24,v25
v_mov_b32 v25,v24
v_mfma_f32_16x16x32_bf16 a[48:51],a[228:231],v[88:91],a[48:51]
v_exp_f32 v119,v119
v_mfma_f32_16x16x32_bf16 a[52:55],a[232:235],v[80:83],a[52:55]
v_permlane16_swap_b32 v24,v25
v_mfma_f32_16x16x32_bf16 a[52:55],a[236:239],v[88:91],a[52:55]
v_max_f32 v24,v24,v25
v_sub_f32 v27,0,v24
v_mfma_f32_16x16x32_bf16 a[56:59],a[240:243],v[80:83],a[56:59]
v_sub_f32 v26,0,v24
v_sub_f32 v25,0,v24
v_mfma_f32_16x16x32_bf16 a[56:59],a[244:247],v[88:91],a[56:59]
ds_read_b128 a[168:171],v1 offset:8704
v_sub_f32 v24,0,v24
v_mfma_f32_16x16x32_bf16 a[60:63],a[248:251],v[80:83],a[60:63]
v_exp_f32 v120,v120
v_mfma_f32_16x16x32_bf16 a[60:63],a[252:255],v[88:91],a[60:63]
ds_write_b128 v7,v[24:27] offset:1024
v_mfma_f32_16x16x32_bf16 v[52:55],v[28:31],v[80:83],v[52:55]
v_exp_f32 v121,v121
v_mfma_f32_16x16x32_bf16 v[52:55],v[28:31],v[88:91],v[52:55]
ds_read_b128 a[172:175],v1 offset:8960
v_cvt_pk_bf16_f32 v115,v118,v119
v_mfma_f32_16x16x32_bf16 v[24:27],v[8:11],v[192:195],0
v_exp_f32 v122,v122
v_mfma_f32_16x16x32_bf16 v[24:27],v[12:15],v[200:203],v[24:27]
ds_read_b128 a[188:191],v1 offset:13056
v_cvt_pk_bf16_f32 v120,v120,v121
v_mfma_f32_16x16x32_bf16 v[24:27],v[16:19],v[208:211],v[24:27]
s_waitcnt lgkmcnt(3)
v_exp_f32 v123,v123
v_mfma_f32_16x16x32_bf16 v[24:27],v[20:23],v[216:219],v[24:27]
global_load_lds_dwordx4 v5,s[10:11] offset:1024
ds_read_b128 a[184:187],v1 offset:12800
v_mfma_f32_16x16x32_bf16 v[64:67],a[128:131],v[132:135],v[32:35]
v_exp_f32 v124,v124
v_mfma_f32_16x16x32_bf16 v[64:67],a[132:135],v[140:143],v[64:67]
ds_read_b128 a[180:183],v1 offset:12544
v_cvt_pk_bf16_f32 v121,v122,v123
v_mfma_f32_16x16x32_bf16 v[64:67],a[136:139],v[148:151],v[64:67]
v_max3_f32 v24,v24,v25,v26
v_max_f32 v24,v24,v27
v_mfma_f32_16x16x32_bf16 v[64:67],a[140:143],v[156:159],v[64:67]
ds_read_b128 a[176:179],v1 offset:12288
v_mov_b32 v25,v24
v_mfma_f32_16x16x32_bf16 v[68:71],a[156:159],v[156:159],v[32:35]
v_exp_f32 v125,v125
v_mfma_f32_16x16x32_bf16 v[68:71],a[152:155],v[148:151],v[68:71]
v_permlane32_swap_b32 v24,v25
v_mfma_f32_16x16x32_bf16 v[68:71],a[148:151],v[140:143],v[68:71]
v_max_f32 v24,v24,v25
v_mov_b32 v25,v24
v_mfma_f32_16x16x32_bf16 v[68:71],a[144:147],v[132:135],v[68:71]
v_exp_f32 v126,v126
v_mfma_f32_16x16x32_bf16 v[72:75],a[160:163],v[132:135],v[32:35]
v_permlane16_swap_b32 v24,v25
v_mfma_f32_16x16x32_bf16 v[72:75],a[164:167],v[140:143],v[72:75]
v_max_f32 v24,v24,v25
v_sub_f32 v27,0,v24
v_mfma_f32_16x16x32_bf16 v[72:75],a[168:171],v[148:151],v[72:75]
s_waitcnt lgkmcnt(1)
v_sub_f32 v26,0,v24
v_sub_f32 v25,0,v24
v_mfma_f32_16x16x32_bf16 v[72:75],a[172:175],v[156:159],v[72:75]
v_sub_f32 v24,0,v24
v_cvt_pk_bf16_f32 v122,v124,v125
v_mfma_f32_16x16x32_bf16 v[76:79],a[188:191],v[156:159],v[32:35]
ds_write_b128 v7,v[24:27] offset:2048
v_mfma_f32_16x16x32_bf16 v[76:79],a[184:187],v[148:151],v[76:79]
global_load_lds_dwordx4 v4,s[10:11] offset:2048
v_add_u32 v1,s78,v1
v_mfma_f32_16x16x32_bf16 v[76:79],a[180:183],v[140:143],v[76:79]
s_waitcnt lgkmcnt(1)
v_exp_f32 v127,v127
v_mfma_f32_16x16x32_bf16 v[76:79],a[176:179],v[132:135],v[76:79]
v_exp_f32 v64,v64
v_mfma_f32_16x16x32_bf16 a[64:67],a[192:195],v[96:99],a[64:67]
v_exp_f32 v65,v65
v_mfma_f32_16x16x32_bf16 a[64:67],a[196:199],v[104:107],a[64:67]
v_cvt_pk_bf16_f32 v123,v126,v127
v_cvt_pk_bf16_f32 v64,v64,v65
v_mfma_f32_16x16x32_bf16 a[68:71],a[200:203],v[96:99],a[68:71]
v_exp_f32 v66,v66
v_mfma_f32_16x16x32_bf16 a[68:71],a[204:207],v[104:107],a[68:71]
v_exp_f32 v67,v67
v_mfma_f32_16x16x32_bf16 a[72:75],a[208:211],v[96:99],a[72:75]
v_exp_f32 v68,v68
v_mfma_f32_16x16x32_bf16 a[72:75],a[212:215],v[104:107],a[72:75]
v_exp_f32 v69,v69
v_mfma_f32_16x16x32_bf16 a[76:79],a[216:219],v[96:99],a[76:79]
v_exp_f32 v70,v70
v_mfma_f32_16x16x32_bf16 a[76:79],a[220:223],v[104:107],a[76:79]
v_exp_f32 v71,v71
v_mfma_f32_16x16x32_bf16 a[80:83],a[224:227],v[96:99],a[80:83]
v_cvt_pk_bf16_f32 v65,v66,v67
v_cvt_pk_bf16_f32 v66,v68,v69
v_mfma_f32_16x16x32_bf16 a[80:83],a[228:231],v[104:107],a[80:83]
v_exp_f32 v72,v72
v_mfma_f32_16x16x32_bf16 a[84:87],a[232:235],v[96:99],a[84:87]
v_exp_f32 v73,v73
v_mfma_f32_16x16x32_bf16 a[84:87],a[236:239],v[104:107],a[84:87]
v_cvt_pk_bf16_f32 v67,v70,v71
v_cvt_pk_bf16_f32 v72,v72,v73
v_mfma_f32_16x16x32_bf16 a[88:91],a[240:243],v[96:99],a[88:91]
global_load_lds_dwordx4 v5,s[10:11] offset:3072
v_mfma_f32_16x16x32_bf16 a[88:91],a[244:247],v[104:107],a[88:91]
v_exp_f32 v74,v74
v_mfma_f32_16x16x32_bf16 a[92:95],a[248:251],v[96:99],a[92:95]
v_exp_f32 v75,v75
v_mfma_f32_16x16x32_bf16 a[92:95],a[252:255],v[104:107],a[92:95]
v_exp_f32 v76,v76
v_mfma_f32_16x16x32_bf16 v[56:59],v[28:31],v[96:99],v[56:59]
v_exp_f32 v77,v77
v_mfma_f32_16x16x32_bf16 v[56:59],v[28:31],v[104:107],v[56:59]
v_exp_f32 v78,v78
v_mfma_f32_16x16x32_bf16 v[24:27],v[8:11],v[224:227],0
v_exp_f32 v79,v79
v_mfma_f32_16x16x32_bf16 v[24:27],v[12:15],v[232:235],v[24:27]
v_cvt_pk_bf16_f32 v73,v74,v75
v_cvt_pk_bf16_f32 v74,v76,v77
v_mfma_f32_16x16x32_bf16 v[24:27],v[16:19],v[240:243],v[24:27]
v_cvt_pk_bf16_f32 v75,v78,v79
v_mfma_f32_16x16x32_bf16 v[24:27],v[20:23],v[248:251],v[24:27]
v_mfma_f32_16x16x32_bf16 v[80:83],a[128:131],v[164:167],v[36:39]
s_add_u32 s10,s20,s74
s_addc_u32 s11,s21,0
v_mfma_f32_16x16x32_bf16 v[80:83],a[132:135],v[172:175],v[80:83]
v_mfma_f32_16x16x32_bf16 v[80:83],a[136:139],v[180:183],v[80:83]
s_add_u32 m0,s76,0
v_mfma_f32_16x16x32_bf16 v[80:83],a[140:143],v[188:191],v[80:83]
v_mfma_f32_16x16x32_bf16 v[84:87],a[156:159],v[188:191],v[36:39]
global_load_lds_dwordx4 v4,s[10:11] nt
v_max3_f32 v24,v24,v25,v26
v_mfma_f32_16x16x32_bf16 v[84:87],a[152:155],v[180:183],v[84:87]
v_max_f32 v24,v24,v27
v_mov_b32 v25,v24
v_mfma_f32_16x16x32_bf16 v[84:87],a[148:151],v[172:175],v[84:87]
v_mfma_f32_16x16x32_bf16 v[84:87],a[144:147],v[164:167],v[84:87]
L_pd6_%=:
s_cbranch_execz L_trt0_%=
v_permlane32_swap_b32 v24,v25
v_mfma_f32_16x16x32_bf16 v[88:91],a[160:163],v[164:167],v[36:39]
v_max_f32 v24,v24,v25
v_mov_b32 v25,v24
v_mfma_f32_16x16x32_bf16 v[88:91],a[164:167],v[172:175],v[88:91]
v_exp_f32 v80,v80
v_mfma_f32_16x16x32_bf16 v[88:91],a[168:171],v[180:183],v[88:91]
v_permlane16_swap_b32 v24,v25
v_mfma_f32_16x16x32_bf16 v[88:91],a[172:175],v[188:191],v[88:91]
v_max_f32 v24,v24,v25
v_max_f32 v24,s72,v24
v_mfma_f32_16x16x32_bf16 v[92:95],a[188:191],v[188:191],v[36:39]
v_sub_f32 v27,0,v24
v_sub_f32 v26,0,v24
v_mfma_f32_16x16x32_bf16 v[92:95],a[184:187],v[180:183],v[92:95]
v_sub_f32 v25,0,v24
v_sub_f32 v24,0,v24
v_mfma_f32_16x16x32_bf16 v[92:95],a[180:183],v[172:175],v[92:95]
v_exp_f32 v81,v81
v_mfma_f32_16x16x32_bf16 v[92:95],a[176:179],v[164:167],v[92:95]
v_exp_f32 v82,v82
v_mfma_f32_16x16x32_bf16 a[96:99],a[192:195],v[112:115],a[96:99]
ds_read_b64_tr_b16 a[192:193],v2
ds_read_b64_tr_b16 a[194:195],v2 offset:4096
v_mfma_f32_16x16x32_bf16 a[96:99],a[196:199],v[120:123],a[96:99]
ds_read_b64_tr_b16 a[196:197],v2 offset:8192
v_cvt_pk_bf16_f32 v80,v80,v81
v_mfma_f32_16x16x32_bf16 a[100:103],a[200:203],v[112:115],a[100:103]
v_exp_f32 v83,v83
v_mfma_f32_16x16x32_bf16 a[100:103],a[204:207],v[120:123],a[100:103]
global_load_lds_dwordx4 v4,s[10:11] offset:1024 nt
ds_read_b64_tr_b16 a[198:199],v2 offset:12288
v_mfma_f32_16x16x32_bf16 a[104:107],a[208:211],v[112:115],a[104:107]
ds_read_b64_tr_b16 a[200:201],v3
ds_read_b64_tr_b16 a[202:203],v3 offset:4096
v_mfma_f32_16x16x32_bf16 a[104:107],a[212:215],v[120:123],a[104:107]
ds_read_b64_tr_b16 a[204:205],v3 offset:8192
v_cvt_pk_bf16_f32 v81,v82,v83
v_mfma_f32_16x16x32_bf16 a[108:111],a[216:219],v[112:115],a[108:111]
v_exp_f32 v84,v84
v_mfma_f32_16x16x32_bf16 a[108:111],a[220:223],v[120:123],a[108:111]
ds_read_b64_tr_b16 a[206:207],v3 offset:12288
ds_read_b64_tr_b16 a[208:209],v2 offset:256
v_mfma_f32_16x16x32_bf16 a[112:115],a[224:227],v[112:115],a[112:115]
v_exp_f32 v85,v85
v_mfma_f32_16x16x32_bf16 a[112:115],a[228:231],v[120:123],a[112:115]
ds_read_b64_tr_b16 a[210:211],v2 offset:4352
ds_read_b64_tr_b16 a[212:213],v2 offset:8448
v_mfma_f32_16x16x32_bf16 a[116:119],a[232:235],v[112:115],a[116:119]
ds_read_b64_tr_b16 a[214:215],v2 offset:12544
v_cvt_pk_bf16_f32 v82,v84,v85
v_mfma_f32_16x16x32_bf16 a[116:119],a[236:239],v[120:123],a[116:119]
v_exp_f32 v86,v86
v_mfma_f32_16x16x32_bf16 a[120:123],a[240:243],v[112:115],a[120:123]
ds_read_b64_tr_b16 a[216:217],v3 offset:256
ds_read_b64_tr_b16 a[218:219],v3 offset:4352
v_mfma_f32_16x16x32_bf16 a[120:123],a[244:247],v[120:123],a[120:123]
v_exp_f32 v87,v87
v_mfma_f32_16x16x32_bf16 a[124:127],a[248:251],v[112:115],a[124:127]
ds_read_b64_tr_b16 a[220:221],v3 offset:8448
ds_read_b64_tr_b16 a[222:223],v3 offset:12544
v_mfma_f32_16x16x32_bf16 a[124:127],a[252:255],v[120:123],a[124:127]
ds_read_b64_tr_b16 a[224:225],v2 offset:512
v_cvt_pk_bf16_f32 v83,v86,v87
v_mfma_f32_16x16x32_bf16 v[60:63],v[28:31],v[112:115],v[60:63]
v_exp_f32 v88,v88
v_mfma_f32_16x16x32_bf16 v[60:63],v[28:31],v[120:123],v[60:63]
global_load_lds_dwordx4 v5,s[10:11] offset:2048 nt
ds_read_b64_tr_b16 a[226:227],v2 offset:4608
v_mfma_f32_16x16x32_bf16 v[96:99],a[128:131],v[196:199],v[40:43]
ds_read_b64_tr_b16 a[228:229],v2 offset:8704
ds_read_b64_tr_b16 a[230:231],v2 offset:12800
v_mfma_f32_16x16x32_bf16 v[96:99],a[132:135],v[204:207],v[96:99]
v_exp_f32 v89,v89
v_mfma_f32_16x16x32_bf16 v[96:99],a[136:139],v[212:215],v[96:99]
ds_read_b64_tr_b16 a[232:233],v3 offset:512
ds_read_b64_tr_b16 a[234:235],v3 offset:4608
v_mfma_f32_16x16x32_bf16 v[96:99],a[140:143],v[220:223],v[96:99]
ds_read_b64_tr_b16 a[236:237],v3 offset:8704
v_cvt_pk_bf16_f32 v88,v88,v89
v_mfma_f32_16x16x32_bf16 v[100:103],a[156:159],v[220:223],v[40:43]
v_exp_f32 v90,v90
v_mfma_f32_16x16x32_bf16 v[100:103],a[152:155],v[212:215],v[100:103]
ds_read_b64_tr_b16 a[238:239],v3 offset:12800
ds_read_b64_tr_b16 a[240:241],v2 offset:768
v_mfma_f32_16x16x32_bf16 v[100:103],a[148:151],v[204:207],v[100:103]
v_exp_f32 v91,v91
v_mfma_f32_16x16x32_bf16 v[100:103],a[144:147],v[196:199],v[100:103]
ds_read_b64_tr_b16 a[242:243],v2 offset:4864
ds_read_b64_tr_b16 a[244:245],v2 offset:8960
v_mfma_f32_16x16x32_bf16 v[104:107],a[160:163],v[196:199],v[40:43]
ds_read_b64_tr_b16 a[246:247],v2 offset:13056
v_cvt_pk_bf16_f32 v89,v90,v91
v_mfma_f32_16x16x32_bf16 v[104:107],a[164:167],v[204:207],v[104:107]
v_exp_f32 v92,v92
v_mfma_f32_16x16x32_bf16 v[104:107],a[168:171],v[212:215],v[104:107]
ds_read_b64_tr_b16 a[248:249],v3 offset:768
ds_read_b64_tr_b16 a[250:251],v3 offset:4864
v_mfma_f32_16x16x32_bf16 v[104:107],a[172:175],v[220:223],v[104:107]
v_exp_f32 v93,v93
v_mfma_f32_16x16x32_bf16 v[108:111],a[188:191],v[220:223],v[40:43]
ds_read_b64_tr_b16 a[252:253],v3 offset:8960
ds_read_b64_tr_b16 a[254:255],v3 offset:13056
v_mfma_f32_16x16x32_bf16 v[108:111],a[184:187],v[212:215],v[108:111]
v_exp_f32 v94,v94
v_mfma_f32_16x16x32_bf16 v[108:111],a[180:183],v[204:207],v[108:111]
global_load_lds_dwordx4 v5,s[10:11] offset:3072
v_cvt_pk_bf16_f32 v90,v92,v93
v_mfma_f32_16x16x32_bf16 v[108:111],a[176:179],v[196:199],v[108:111]
s_waitcnt lgkmcnt(5)
v_exp_f32 v95,v95
v_mfma_f32_16x16x32_bf16 a[0:3],a[192:195],v[64:67],a[0:3]
v_exp_f32 v96,v96
v_mfma_f32_16x16x32_bf16 a[0:3],a[196:199],v[72:75],a[0:3]
v_exp_f32 v97,v97
v_mfma_f32_16x16x32_bf16 a[4:7],a[200:203],v[64:67],a[4:7]
v_exp_f32 v98,v98
v_mfma_f32_16x16x32_bf16 a[4:7],a[204:207],v[72:75],a[4:7]
v_exp_f32 v99,v99
v_mfma_f32_16x16x32_bf16 a[8:11],a[208:211],v[64:67],a[8:11]
v_exp_f32 v100,v100
v_mfma_f32_16x16x32_bf16 a[8:11],a[212:215],v[72:75],a[8:11]
)A"
R"A(v_exp_f32 v101,v101
v_mfma_f32_16x16x32_bf16 a[12:15],a[216:219],v[64:67],a[12:15]
v_exp_f32 v102,v102
v_mfma_f32_16x16x32_bf16 a[12:15],a[220:223],v[72:75],a[12:15]
v_exp_f32 v103,v103
v_mfma_f32_16x16x32_bf16 a[16:19],a[224:227],v[64:67],a[16:19]
ds_write_b128 v7,v[24:27] offset:3072
v_mfma_f32_16x16x32_bf16 a[16:19],a[228:231],v[72:75],a[16:19]
v_cvt_pk_bf16_f32 v91,v94,v95
v_cvt_pk_bf16_f32 v96,v96,v97
v_mfma_f32_16x16x32_bf16 a[20:23],a[232:235],v[64:67],a[20:23]
s_getreg_b32 s92,hwreg(HW_REG_IB_STS,0,4)
s_min_u32 s4,s92,s6
s_mov_b32 s6,s92
v_mfma_f32_16x16x32_bf16 a[20:23],a[236:239],v[72:75],a[20:23]
s_cmp_gt_u32 s4,s9
s_cselect_b32 s4,15,s5
v_cvt_pk_bf16_f32 v97,v98,v99
v_mfma_f32_16x16x32_bf16 a[24:27],a[240:243],v[64:67],a[24:27]
s_waitcnt lgkmcnt(1)
v_cvt_pk_bf16_f32 v98,v100,v101
v_cvt_pk_bf16_f32 v99,v102,v103
v_mfma_f32_16x16x32_bf16 a[24:27],a[244:247],v[72:75],a[24:27]
v_mfma_f32_16x16x32_bf16 a[28:31],a[248:251],v[64:67],a[28:31]
v_mfma_f32_16x16x32_bf16 a[28:31],a[252:255],v[72:75],a[28:31]
s_getpc_b64 s[92:93]
s_cmp_lg_u32 s4,0
s_cbranch_scc1 L_pace_%=
v_mfma_f32_16x16x32_bf16 v[48:51],v[28:31],v[64:67],v[48:51]
v_mfma_f32_16x16x32_bf16 v[48:51],v[28:31],v[72:75],v[48:51]
s_mov_b32 s70,11
s_nop 0
s_nop 0
s_nop 0
s_nop 0
L_a0_%=:
s_waitcnt vmcnt(12)
s_barrier
v_mfma_f32_16x16x32_bf16 v[112:115],a[128:131],v[228:231],v[44:47]
s_add_u32 s10,s24,s74
s_addc_u32 s11,s25,0
s_add_u32 m0,s76,16384
v_mfma_f32_16x16x32_bf16 v[112:115],a[132:135],v[236:239],v[112:115]
s_add_u32 s75,s75,16384
s_add_u32 s74,s74,16384
ds_read_b128 a[128:131],v1
v_mfma_f32_16x16x32_bf16 v[112:115],a[136:139],v[244:247],v[112:115]
s_and_b32 s74,s74,0x3ffff
global_load_lds_dwordx4 v4,s[10:11]
v_mfma_f32_16x16x32_bf16 v[112:115],a[140:143],v[252:255],v[112:115]
s_and_b32 s69,s75,0xfffc0000
s_or_b32 s74,s74,s69
ds_read_b128 a[132:135],v1 offset:256
v_mfma_f32_16x16x32_bf16 v[116:119],a[156:159],v[252:255],v[44:47]
s_sub_u32 s76,s76,32768
s_and_b32 s76,s76,0x1ffff
v_mfma_f32_16x16x32_bf16 v[116:119],a[152:155],v[244:247],v[116:119]
ds_read_b128 a[136:139],v1 offset:512
v_mfma_f32_16x16x32_bf16 v[116:119],a[148:151],v[236:239],v[116:119]
v_mfma_f32_16x16x32_bf16 v[116:119],a[144:147],v[228:231],v[116:119]
ds_read_b128 a[140:143],v1 offset:768
v_mfma_f32_16x16x32_bf16 v[120:123],a[160:163],v[228:231],v[44:47]
v_mfma_f32_16x16x32_bf16 v[120:123],a[164:167],v[236:239],v[120:123]
ds_read_b128 a[156:159],v1 offset:4864
v_mfma_f32_16x16x32_bf16 v[120:123],a[168:171],v[244:247],v[120:123]
v_mfma_f32_16x16x32_bf16 v[120:123],a[172:175],v[252:255],v[120:123]
ds_read_b128 a[152:155],v1 offset:4608
v_mfma_f32_16x16x32_bf16 v[124:127],a[188:191],v[252:255],v[44:47]
v_exp_f32 v104,v104
v_mfma_f32_16x16x32_bf16 v[124:127],a[184:187],v[244:247],v[124:127]
ds_read_b128 a[148:151],v1 offset:4352
v_add_u32 v2,s78,v2
v_mfma_f32_16x16x32_bf16 v[124:127],a[180:183],v[236:239],v[124:127]
v_exp_f32 v105,v105
v_mfma_f32_16x16x32_bf16 v[124:127],a[176:179],v[228:231],v[124:127]
ds_read_b128 a[144:147],v1 offset:4096
v_cvt_pk_bf16_f32 v104,v104,v105
v_mfma_f32_16x16x32_bf16 a[32:35],a[192:195],v[80:83],a[32:35]
v_exp_f32 v106,v106
v_mfma_f32_16x16x32_bf16 a[32:35],a[196:199],v[88:91],a[32:35]
ds_read_b128 a[160:163],v1 offset:8192
v_add_u32 v3,s78,v3
v_mfma_f32_16x16x32_bf16 a[36:39],a[200:203],v[80:83],a[36:39]
s_sub_u32 s78,s77,32768
global_load_lds_dwordx4 v5,s[10:11] offset:1024
v_mfma_f32_16x16x32_bf16 a[36:39],a[204:207],v[88:91],a[36:39]
s_and_b32 s78,s78,0x1ffff
v_exp_f32 v107,v107
v_mfma_f32_16x16x32_bf16 a[40:43],a[208:211],v[80:83],a[40:43]
s_sub_u32 s78,s78,s77
ds_read_b128 a[164:167],v1 offset:8448
v_cvt_pk_bf16_f32 v105,v106,v107
v_mfma_f32_16x16x32_bf16 a[40:43],a[212:215],v[88:91],a[40:43]
s_add_u32 s77,s77,s78
v_exp_f32 v108,v108
v_mfma_f32_16x16x32_bf16 a[44:47],a[216:219],v[80:83],a[44:47]
v_exp_f32 v109,v109
v_mfma_f32_16x16x32_bf16 a[44:47],a[220:223],v[88:91],a[44:47]
ds_read_b128 a[168:171],v1 offset:8704
v_cvt_pk_bf16_f32 v106,v108,v109
v_mfma_f32_16x16x32_bf16 a[48:51],a[224:227],v[80:83],a[48:51]
v_exp_f32 v110,v110
v_mfma_f32_16x16x32_bf16 a[48:51],a[228:231],v[88:91],a[48:51]
v_exp_f32 v111,v111
v_mfma_f32_16x16x32_bf16 a[52:55],a[232:235],v[80:83],a[52:55]
ds_read_b128 a[172:175],v1 offset:8960
v_cvt_pk_bf16_f32 v107,v110,v111
v_mfma_f32_16x16x32_bf16 a[52:55],a[236:239],v[88:91],a[52:55]
v_exp_f32 v112,v112
v_mfma_f32_16x16x32_bf16 a[56:59],a[240:243],v[80:83],a[56:59]
v_exp_f32 v113,v113
v_mfma_f32_16x16x32_bf16 a[56:59],a[244:247],v[88:91],a[56:59]
ds_read_b128 a[188:191],v1 offset:13056
v_cvt_pk_bf16_f32 v112,v112,v113
v_mfma_f32_16x16x32_bf16 a[60:63],a[248:251],v[80:83],a[60:63]
v_exp_f32 v114,v114
v_mfma_f32_16x16x32_bf16 a[60:63],a[252:255],v[88:91],a[60:63]
v_exp_f32 v115,v115
v_mfma_f32_16x16x32_bf16 v[52:55],v[28:31],v[80:83],v[52:55]
ds_read_b128 a[184:187],v1 offset:12800
v_cvt_pk_bf16_f32 v113,v114,v115
v_mfma_f32_16x16x32_bf16 v[52:55],v[28:31],v[88:91],v[52:55]
s_waitcnt lgkmcnt(3)
v_exp_f32 v116,v116
v_mfma_f32_16x16x32_bf16 v[64:67],a[128:131],v[132:135],v[32:35]
global_load_lds_dwordx4 v4,s[10:11] offset:2048
ds_read_b128 a[180:183],v1 offset:12544
v_mfma_f32_16x16x32_bf16 v[64:67],a[132:135],v[140:143],v[64:67]
v_exp_f32 v117,v117
v_mfma_f32_16x16x32_bf16 v[64:67],a[136:139],v[148:151],v[64:67]
ds_read_b128 a[176:179],v1 offset:12288
v_cvt_pk_bf16_f32 v114,v116,v117
v_mfma_f32_16x16x32_bf16 v[64:67],a[140:143],v[156:159],v[64:67]
v_exp_f32 v118,v118
v_mfma_f32_16x16x32_bf16 v[68:71],a[156:159],v[156:159],v[32:35]
v_exp_f32 v119,v119
v_mfma_f32_16x16x32_bf16 v[68:71],a[152:155],v[148:151],v[68:71]
v_cvt_pk_bf16_f32 v115,v118,v119
v_add_u32 v1,s78,v1
v_mfma_f32_16x16x32_bf16 v[68:71],a[148:151],v[140:143],v[68:71]
v_exp_f32 v120,v120
v_mfma_f32_16x16x32_bf16 v[68:71],a[144:147],v[132:135],v[68:71]
v_exp_f32 v121,v121
v_mfma_f32_16x16x32_bf16 v[72:75],a[160:163],v[132:135],v[32:35]
v_exp_f32 v122,v122
v_mfma_f32_16x16x32_bf16 v[72:75],a[164:167],v[140:143],v[72:75]
v_exp_f32 v123,v123
v_mfma_f32_16x16x32_bf16 v[72:75],a[168:171],v[148:151],v[72:75]
s_waitcnt lgkmcnt(0)
v_exp_f32 v124,v124
v_mfma_f32_16x16x32_bf16 v[72:75],a[172:175],v[156:159],v[72:75]
v_exp_f32 v125,v125
v_mfma_f32_16x16x32_bf16 v[76:79],a[188:191],v[156:159],v[32:35]
v_exp_f32 v126,v126
v_mfma_f32_16x16x32_bf16 v[76:79],a[184:187],v[148:151],v[76:79]
v_exp_f32 v127,v127
v_mfma_f32_16x16x32_bf16 v[76:79],a[180:183],v[140:143],v[76:79]
v_cvt_pk_bf16_f32 v120,v120,v121
v_cvt_pk_bf16_f32 v121,v122,v123
v_mfma_f32_16x16x32_bf16 v[76:79],a[176:179],v[132:135],v[76:79]
v_cvt_pk_bf16_f32 v122,v124,v125
v_cvt_pk_bf16_f32 v123,v126,v127
v_mfma_f32_16x16x32_bf16 a[64:67],a[192:195],v[96:99],a[64:67]
global_load_lds_dwordx4 v5,s[10:11] offset:3072
v_mfma_f32_16x16x32_bf16 a[64:67],a[196:199],v[104:107],a[64:67]
v_exp_f32 v64,v64
v_mfma_f32_16x16x32_bf16 a[68:71],a[200:203],v[96:99],a[68:71]
v_exp_f32 v65,v65
v_mfma_f32_16x16x32_bf16 a[68:71],a[204:207],v[104:107],a[68:71]
v_exp_f32 v66,v66
v_mfma_f32_16x16x32_bf16 a[72:75],a[208:211],v[96:99],a[72:75]
v_exp_f32 v67,v67
v_mfma_f32_16x16x32_bf16 a[72:75],a[212:215],v[104:107],a[72:75]
v_exp_f32 v68,v68
v_mfma_f32_16x16x32_bf16 a[76:79],a[216:219],v[96:99],a[76:79]
v_exp_f32 v69,v69
v_mfma_f32_16x16x32_bf16 a[76:79],a[220:223],v[104:107],a[76:79]
v_exp_f32 v70,v70
v_mfma_f32_16x16x32_bf16 a[80:83],a[224:227],v[96:99],a[80:83]
v_exp_f32 v71,v71
v_mfma_f32_16x16x32_bf16 a[80:83],a[228:231],v[104:107],a[80:83]
v_cvt_pk_bf16_f32 v64,v64,v65
v_cvt_pk_bf16_f32 v65,v66,v67
v_mfma_f32_16x16x32_bf16 a[84:87],a[232:235],v[96:99],a[84:87]
v_cvt_pk_bf16_f32 v66,v68,v69
v_cvt_pk_bf16_f32 v67,v70,v71
v_mfma_f32_16x16x32_bf16 a[84:87],a[236:239],v[104:107],a[84:87]
v_exp_f32 v72,v72
v_mfma_f32_16x16x32_bf16 a[88:91],a[240:243],v[96:99],a[88:91]
s_add_u32 s10,s20,s74
s_addc_u32 s11,s21,0
v_mfma_f32_16x16x32_bf16 a[88:91],a[244:247],v[104:107],a[88:91]
v_exp_f32 v73,v73
v_mfma_f32_16x16x32_bf16 a[92:95],a[248:251],v[96:99],a[92:95]
s_add_u32 m0,s76,0
v_exp_f32 v74,v74
v_mfma_f32_16x16x32_bf16 a[92:95],a[252:255],v[104:107],a[92:95]
v_exp_f32 v75,v75
v_mfma_f32_16x16x32_bf16 v[56:59],v[28:31],v[96:99],v[56:59]
global_load_lds_dwordx4 v4,s[10:11] nt
v_cvt_pk_bf16_f32 v72,v72,v73
v_mfma_f32_16x16x32_bf16 v[56:59],v[28:31],v[104:107],v[56:59]
v_exp_f32 v76,v76
v_mfma_f32_16x16x32_bf16 v[80:83],a[128:131],v[164:167],v[36:39]
v_exp_f32 v77,v77
v_mfma_f32_16x16x32_bf16 v[80:83],a[132:135],v[172:175],v[80:83]
v_exp_f32 v78,v78
v_mfma_f32_16x16x32_bf16 v[80:83],a[136:139],v[180:183],v[80:83]
v_exp_f32 v79,v79
v_mfma_f32_16x16x32_bf16 v[80:83],a[140:143],v[188:191],v[80:83]
v_cvt_pk_bf16_f32 v73,v74,v75
v_cvt_pk_bf16_f32 v74,v76,v77
v_mfma_f32_16x16x32_bf16 v[84:87],a[156:159],v[188:191],v[36:39]
v_cvt_pk_bf16_f32 v75,v78,v79
v_mfma_f32_16x16x32_bf16 v[84:87],a[152:155],v[180:183],v[84:87]
v_mfma_f32_16x16x32_bf16 v[84:87],a[148:151],v[172:175],v[84:87]
v_mfma_f32_16x16x32_bf16 v[84:87],a[144:147],v[164:167],v[84:87]
v_mfma_f32_16x16x32_bf16 v[88:91],a[160:163],v[164:167],v[36:39]
v_exp_f32 v80,v80
v_mfma_f32_16x16x32_bf16 v[88:91],a[164:167],v[172:175],v[88:91]
v_exp_f32 v81,v81
v_mfma_f32_16x16x32_bf16 v[88:91],a[168:171],v[180:183],v[88:91]
v_exp_f32 v82,v82
v_mfma_f32_16x16x32_bf16 v[88:91],a[172:175],v[188:191],v[88:91]
v_exp_f32 v83,v83
v_mfma_f32_16x16x32_bf16 v[92:95],a[188:191],v[188:191],v[36:39]
v_exp_f32 v84,v84
v_mfma_f32_16x16x32_bf16 v[92:95],a[184:187],v[180:183],v[92:95]
v_exp_f32 v85,v85
v_mfma_f32_16x16x32_bf16 v[92:95],a[180:183],v[172:175],v[92:95]
global_load_lds_dwordx4 v4,s[10:11] offset:1024 nt
v_cvt_pk_bf16_f32 v80,v80,v81
v_mfma_f32_16x16x32_bf16 v[92:95],a[176:179],v[164:167],v[92:95]
v_exp_f32 v86,v86
v_mfma_f32_16x16x32_bf16 a[96:99],a[192:195],v[112:115],a[96:99]
ds_read_b64_tr_b16 a[192:193],v2
ds_read_b64_tr_b16 a[194:195],v2 offset:4096
v_mfma_f32_16x16x32_bf16 a[96:99],a[196:199],v[120:123],a[96:99]
ds_read_b64_tr_b16 a[196:197],v2 offset:8192
v_cvt_pk_bf16_f32 v81,v82,v83
v_mfma_f32_16x16x32_bf16 a[100:103],a[200:203],v[112:115],a[100:103]
ds_read_b64_tr_b16 a[198:199],v2 offset:12288
v_cvt_pk_bf16_f32 v82,v84,v85
v_mfma_f32_16x16x32_bf16 a[100:103],a[204:207],v[120:123],a[100:103]
v_exp_f32 v87,v87
v_mfma_f32_16x16x32_bf16 a[104:107],a[208:211],v[112:115],a[104:107]
ds_read_b64_tr_b16 a[200:201],v3
ds_read_b64_tr_b16 a[202:203],v3 offset:4096
v_mfma_f32_16x16x32_bf16 a[104:107],a[212:215],v[120:123],a[104:107]
ds_read_b64_tr_b16 a[204:205],v3 offset:8192
v_cvt_pk_bf16_f32 v83,v86,v87
v_mfma_f32_16x16x32_bf16 a[108:111],a[216:219],v[112:115],a[108:111]
v_exp_f32 v88,v88
v_mfma_f32_16x16x32_bf16 a[108:111],a[220:223],v[120:123],a[108:111]
ds_read_b64_tr_b16 a[206:207],v3 offset:12288
ds_read_b64_tr_b16 a[208:209],v2 offset:256
v_mfma_f32_16x16x32_bf16 a[112:115],a[224:227],v[112:115],a[112:115]
v_exp_f32 v89,v89
v_mfma_f32_16x16x32_bf16 a[112:115],a[228:231],v[120:123],a[112:115]
ds_read_b64_tr_b16 a[210:211],v2 offset:4352
ds_read_b64_tr_b16 a[212:213],v2 offset:8448
v_mfma_f32_16x16x32_bf16 a[116:119],a[232:235],v[112:115],a[116:119]
ds_read_b64_tr_b16 a[214:215],v2 offset:12544
v_cvt_pk_bf16_f32 v88,v88,v89
v_mfma_f32_16x16x32_bf16 a[116:119],a[236:239],v[120:123],a[116:119]
v_exp_f32 v90,v90
v_mfma_f32_16x16x32_bf16 a[120:123],a[240:243],v[112:115],a[120:123]
ds_read_b64_tr_b16 a[216:217],v3 offset:256
ds_read_b64_tr_b16 a[218:219],v3 offset:4352
v_mfma_f32_16x16x32_bf16 a[120:123],a[244:247],v[120:123],a[120:123]
v_exp_f32 v91,v91
v_mfma_f32_16x16x32_bf16 a[124:127],a[248:251],v[112:115],a[124:127]
global_load_lds_dwordx4 v5,s[10:11] offset:2048 nt
ds_read_b64_tr_b16 a[220:221],v3 offset:8448
v_mfma_f32_16x16x32_bf16 a[124:127],a[252:255],v[120:123],a[124:127]
ds_read_b64_tr_b16 a[222:223],v3 offset:12544
ds_read_b64_tr_b16 a[224:225],v2 offset:512
v_mfma_f32_16x16x32_bf16 v[60:63],v[28:31],v[112:115],v[60:63]
ds_read_b64_tr_b16 a[226:227],v2 offset:4608
v_cvt_pk_bf16_f32 v89,v90,v91
v_mfma_f32_16x16x32_bf16 v[60:63],v[28:31],v[120:123],v[60:63]
v_exp_f32 v92,v92
)A"
R"A(v_mfma_f32_16x16x32_bf16 v[96:99],a[128:131],v[196:199],v[40:43]
ds_read_b64_tr_b16 a[228:229],v2 offset:8704
ds_read_b64_tr_b16 a[230:231],v2 offset:12800
v_mfma_f32_16x16x32_bf16 v[96:99],a[132:135],v[204:207],v[96:99]
v_exp_f32 v93,v93
v_mfma_f32_16x16x32_bf16 v[96:99],a[136:139],v[212:215],v[96:99]
ds_read_b64_tr_b16 a[232:233],v3 offset:512
ds_read_b64_tr_b16 a[234:235],v3 offset:4608
v_mfma_f32_16x16x32_bf16 v[96:99],a[140:143],v[220:223],v[96:99]
ds_read_b64_tr_b16 a[236:237],v3 offset:8704
v_cvt_pk_bf16_f32 v90,v92,v93
v_mfma_f32_16x16x32_bf16 v[100:103],a[156:159],v[220:223],v[40:43]
v_exp_f32 v94,v94
v_mfma_f32_16x16x32_bf16 v[100:103],a[152:155],v[212:215],v[100:103]
ds_read_b64_tr_b16 a[238:239],v3 offset:12800
ds_read_b64_tr_b16 a[240:241],v2 offset:768
v_mfma_f32_16x16x32_bf16 v[100:103],a[148:151],v[204:207],v[100:103]
v_exp_f32 v95,v95
v_mfma_f32_16x16x32_bf16 v[100:103],a[144:147],v[196:199],v[100:103]
ds_read_b64_tr_b16 a[242:243],v2 offset:4864
ds_read_b64_tr_b16 a[244:245],v2 offset:8960
v_mfma_f32_16x16x32_bf16 v[104:107],a[160:163],v[196:199],v[40:43]
ds_read_b64_tr_b16 a[246:247],v2 offset:13056
v_cvt_pk_bf16_f32 v91,v94,v95
v_mfma_f32_16x16x32_bf16 v[104:107],a[164:167],v[204:207],v[104:107]
v_exp_f32 v96,v96
v_mfma_f32_16x16x32_bf16 v[104:107],a[168:171],v[212:215],v[104:107]
ds_read_b64_tr_b16 a[248:249],v3 offset:768
ds_read_b64_tr_b16 a[250:251],v3 offset:4864
v_mfma_f32_16x16x32_bf16 v[104:107],a[172:175],v[220:223],v[104:107]
v_exp_f32 v97,v97
v_mfma_f32_16x16x32_bf16 v[108:111],a[188:191],v[220:223],v[40:43]
global_load_lds_dwordx4 v5,s[10:11] offset:3072
ds_read_b64_tr_b16 a[252:253],v3 offset:8960
v_mfma_f32_16x16x32_bf16 v[108:111],a[184:187],v[212:215],v[108:111]
ds_read_b64_tr_b16 a[254:255],v3 offset:13056
v_cvt_pk_bf16_f32 v96,v96,v97
v_mfma_f32_16x16x32_bf16 v[108:111],a[180:183],v[204:207],v[108:111]
v_exp_f32 v98,v98
v_mfma_f32_16x16x32_bf16 v[108:111],a[176:179],v[196:199],v[108:111]
s_waitcnt lgkmcnt(5)
v_exp_f32 v99,v99
v_mfma_f32_16x16x32_bf16 a[0:3],a[192:195],v[64:67],a[0:3]
v_exp_f32 v100,v100
v_mfma_f32_16x16x32_bf16 a[0:3],a[196:199],v[72:75],a[0:3]
v_exp_f32 v101,v101
v_mfma_f32_16x16x32_bf16 a[4:7],a[200:203],v[64:67],a[4:7]
v_exp_f32 v102,v102
v_mfma_f32_16x16x32_bf16 a[4:7],a[204:207],v[72:75],a[4:7]
v_exp_f32 v103,v103
v_mfma_f32_16x16x32_bf16 a[8:11],a[208:211],v[64:67],a[8:11]
v_cvt_pk_bf16_f32 v97,v98,v99
v_cvt_pk_bf16_f32 v98,v100,v101
v_mfma_f32_16x16x32_bf16 a[8:11],a[212:215],v[72:75],a[8:11]
v_cvt_pk_bf16_f32 v99,v102,v103
v_mfma_f32_16x16x32_bf16 a[12:15],a[216:219],v[64:67],a[12:15]
v_mfma_f32_16x16x32_bf16 a[12:15],a[220:223],v[72:75],a[12:15]
v_mfma_f32_16x16x32_bf16 a[16:19],a[224:227],v[64:67],a[16:19]
v_mfma_f32_16x16x32_bf16 a[16:19],a[228:231],v[72:75],a[16:19]
v_mfma_f32_16x16x32_bf16 a[20:23],a[232:235],v[64:67],a[20:23]
s_getreg_b32 s92,hwreg(HW_REG_IB_STS,0,4)
s_min_u32 s4,s92,s6
s_mov_b32 s6,s92
v_mfma_f32_16x16x32_bf16 a[20:23],a[236:239],v[72:75],a[20:23]
s_cmp_ge_u32 s4,s9
s_cselect_b32 s4,15,s5
v_mfma_f32_16x16x32_bf16 a[24:27],a[240:243],v[64:67],a[24:27]
s_waitcnt lgkmcnt(0)
v_mfma_f32_16x16x32_bf16 a[24:27],a[244:247],v[72:75],a[24:27]
v_mfma_f32_16x16x32_bf16 a[28:31],a[248:251],v[64:67],a[28:31]
v_mfma_f32_16x16x32_bf16 a[28:31],a[252:255],v[72:75],a[28:31]
s_getpc_b64 s[92:93]
s_cmp_lg_u32 s4,0
s_cbranch_scc1 L_pace_%=
v_mfma_f32_16x16x32_bf16 v[48:51],v[28:31],v[64:67],v[48:51]
v_mfma_f32_16x16x32_bf16 v[48:51],v[28:31],v[72:75],v[48:51]
s_sub_u32 s70,s70,1
s_cbranch_scc0 L_a0_%=
L_nx0_%=:
s_waitcnt vmcnt(12)
s_barrier
v_mfma_f32_16x16x32_bf16 v[112:115],a[128:131],v[228:231],v[44:47]
s_add_u32 s10,s24,s74
s_addc_u32 s11,s25,0
s_add_u32 m0,s76,16384
v_mfma_f32_16x16x32_bf16 v[112:115],a[132:135],v[236:239],v[112:115]
s_add_u32 s75,s75,16384
s_add_u32 s74,s74,16384
ds_read_b128 v[32:35],v7
v_mfma_f32_16x16x32_bf16 v[112:115],a[136:139],v[244:247],v[112:115]
s_and_b32 s74,s74,0x3ffff
global_load_lds_dwordx4 v4,s[10:11]
v_mfma_f32_16x16x32_bf16 v[112:115],a[140:143],v[252:255],v[112:115]
s_and_b32 s69,s75,0xfffc0000
s_or_b32 s74,s74,s69
ds_read_b128 a[128:131],v1
v_mfma_f32_16x16x32_bf16 v[116:119],a[156:159],v[252:255],v[44:47]
s_sub_u32 s76,s76,32768
v_exp_f32 v104,v104
v_mfma_f32_16x16x32_bf16 v[116:119],a[152:155],v[244:247],v[116:119]
s_and_b32 s76,s76,0x1ffff
ds_read_b128 a[132:135],v1 offset:256
v_add_u32 v2,s78,v2
v_mfma_f32_16x16x32_bf16 v[116:119],a[148:151],v[236:239],v[116:119]
v_exp_f32 v105,v105
v_mfma_f32_16x16x32_bf16 v[116:119],a[144:147],v[228:231],v[116:119]
ds_read_b128 a[136:139],v1 offset:512
v_cvt_pk_bf16_f32 v104,v104,v105
v_mfma_f32_16x16x32_bf16 v[120:123],a[160:163],v[228:231],v[44:47]
v_exp_f32 v106,v106
v_mfma_f32_16x16x32_bf16 v[120:123],a[164:167],v[236:239],v[120:123]
ds_read_b128 a[140:143],v1 offset:768
v_add_u32 v3,s78,v3
v_mfma_f32_16x16x32_bf16 v[120:123],a[168:171],v[244:247],v[120:123]
s_sub_u32 s78,s77,32768
v_exp_f32 v107,v107
v_mfma_f32_16x16x32_bf16 v[120:123],a[172:175],v[252:255],v[120:123]
s_and_b32 s78,s78,0x1ffff
ds_read_b128 a[156:159],v1 offset:4864
v_cvt_pk_bf16_f32 v105,v106,v107
v_mfma_f32_16x16x32_bf16 v[124:127],a[188:191],v[252:255],v[44:47]
s_sub_u32 s78,s78,s77
v_exp_f32 v108,v108
v_mfma_f32_16x16x32_bf16 v[124:127],a[184:187],v[244:247],v[124:127]
s_add_u32 s77,s77,s78
ds_read_b128 a[152:155],v1 offset:4608
v_accvgpr_read_b32 v8,a0
v_mfma_f32_16x16x32_bf16 v[124:127],a[180:183],v[236:239],v[124:127]
v_exp_f32 v109,v109
v_mfma_f32_16x16x32_bf16 v[124:127],a[176:179],v[228:231],v[124:127]
ds_read_b128 a[148:151],v1 offset:4352
v_cvt_pk_bf16_f32 v106,v108,v109
v_mfma_f32_16x16x32_bf16 v[52:55],v[28:31],v[80:83],v[52:55]
global_load_lds_dwordx4 v5,s[10:11] offset:1024
v_accvgpr_read_b32 v9,a1
v_mfma_f32_16x16x32_bf16 v[52:55],v[28:31],v[88:91],v[52:55]
ds_read_b128 a[144:147],v1 offset:4096
v_accvgpr_read_b32 v10,a2
v_mfma_f32_16x16x32_bf16 a[32:35],a[192:195],v[80:83],a[32:35]
v_exp_f32 v110,v110
v_mfma_f32_16x16x32_bf16 a[32:35],a[196:199],v[88:91],a[32:35]
ds_read_b128 a[160:163],v1 offset:8192
v_accvgpr_read_b32 v11,a3
v_mfma_f32_16x16x32_bf16 a[36:39],a[200:203],v[80:83],a[36:39]
v_exp_f32 v111,v111
v_mfma_f32_16x16x32_bf16 a[36:39],a[204:207],v[88:91],a[36:39]
ds_read_b128 a[164:167],v1 offset:8448
v_cvt_pk_bf16_f32 v107,v110,v111
v_mfma_f32_16x16x32_bf16 a[40:43],a[208:211],v[80:83],a[40:43]
v_exp_f32 v112,v112
v_mfma_f32_16x16x32_bf16 a[40:43],a[212:215],v[88:91],a[40:43]
ds_read_b128 a[168:171],v1 offset:8704
v_accvgpr_read_b32 v12,a4
v_mfma_f32_16x16x32_bf16 a[44:47],a[216:219],v[80:83],a[44:47]
v_exp_f32 v113,v113
v_mfma_f32_16x16x32_bf16 a[44:47],a[220:223],v[88:91],a[44:47]
ds_read_b128 a[172:175],v1 offset:8960
v_cvt_pk_bf16_f32 v112,v112,v113
v_mfma_f32_16x16x32_bf16 a[48:51],a[224:227],v[80:83],a[48:51]
v_exp_f32 v114,v114
v_mfma_f32_16x16x32_bf16 a[48:51],a[228:231],v[88:91],a[48:51]
ds_read_b128 a[188:191],v1 offset:13056
v_accvgpr_read_b32 v13,a5
v_mfma_f32_16x16x32_bf16 a[52:55],a[232:235],v[80:83],a[52:55]
v_exp_f32 v115,v115
v_mfma_f32_16x16x32_bf16 a[52:55],a[236:239],v[88:91],a[52:55]
ds_read_b128 a[184:187],v1 offset:12800
v_cvt_pk_bf16_f32 v113,v114,v115
v_mfma_f32_16x16x32_bf16 a[56:59],a[240:243],v[80:83],a[56:59]
global_load_lds_dwordx4 v4,s[10:11] offset:2048
v_accvgpr_read_b32 v14,a6
v_mfma_f32_16x16x32_bf16 a[56:59],a[244:247],v[88:91],a[56:59]
ds_read_b128 a[180:183],v1 offset:12544
v_accvgpr_read_b32 v15,a7
v_mfma_f32_16x16x32_bf16 a[60:63],a[248:251],v[80:83],a[60:63]
v_exp_f32 v116,v116
v_mfma_f32_16x16x32_bf16 a[60:63],a[252:255],v[88:91],a[60:63]
s_waitcnt lgkmcnt(3)
ds_read_b128 a[176:179],v1 offset:12288
v_max_f32 v24,0x00800000,v48
v_mfma_f32_16x16x32_bf16 v[64:67],a[128:131],v[128:131],v[32:35]
v_exp_f32 v117,v117
v_mfma_f32_16x16x32_bf16 v[64:67],a[132:135],v[136:139],v[64:67]
ds_read_b128 v[36:39],v7 offset:1024
v_cvt_pk_bf16_f32 v114,v116,v117
v_mfma_f32_16x16x32_bf16 v[64:67],a[136:139],v[144:147],v[64:67]
v_exp_f32 v118,v118
v_mfma_f32_16x16x32_bf16 v[64:67],a[140:143],v[152:155],v[64:67]
v_exp_f32 v119,v119
v_mfma_f32_16x16x32_bf16 v[68:71],a[156:159],v[152:155],v[32:35]
ds_read_b128 v[40:43],v7 offset:2048
v_cvt_pk_bf16_f32 v115,v118,v119
s_nop 0
L_pd7_%=:
s_cbranch_execz L_trt0_%=
v_mfma_f32_16x16x32_bf16 v[68:71],a[152:155],v[144:147],v[68:71]
v_exp_f32 v120,v120
v_mfma_f32_16x16x32_bf16 v[68:71],a[148:151],v[136:139],v[68:71]
v_exp_f32 v121,v121
v_mfma_f32_16x16x32_bf16 v[68:71],a[144:147],v[128:131],v[68:71]
v_exp_f32 v122,v122
v_mfma_f32_16x16x32_bf16 v[72:75],a[160:163],v[128:131],v[32:35]
v_exp_f32 v123,v123
v_mfma_f32_16x16x32_bf16 v[72:75],a[164:167],v[136:139],v[72:75]
v_exp_f32 v124,v124
v_mfma_f32_16x16x32_bf16 v[72:75],a[168:171],v[144:147],v[72:75]
s_waitcnt lgkmcnt(1)
v_exp_f32 v125,v125
v_mfma_f32_16x16x32_bf16 v[72:75],a[172:175],v[152:155],v[72:75]
global_load_lds_dwordx4 v5,s[10:11] offset:3072
v_cvt_pk_bf16_f32 v120,v120,v121
v_mfma_f32_16x16x32_bf16 v[76:79],a[188:191],v[152:155],v[32:35]
v_exp_f32 v126,v126
v_mfma_f32_16x16x32_bf16 v[76:79],a[184:187],v[144:147],v[76:79]
v_exp_f32 v127,v127
v_mfma_f32_16x16x32_bf16 v[76:79],a[180:183],v[136:139],v[76:79]
v_cvt_pk_bf16_f32 v121,v122,v123
v_cvt_pk_bf16_f32 v122,v124,v125
v_mfma_f32_16x16x32_bf16 v[76:79],a[176:179],v[128:131],v[76:79]
v_cvt_pk_bf16_f32 v123,v126,v127
v_accvgpr_read_b32 v16,a8
v_mfma_f32_16x16x32_bf16 v[56:59],v[28:31],v[96:99],v[56:59]
v_exp_f32 v64,v64
v_mfma_f32_16x16x32_bf16 v[56:59],v[28:31],v[104:107],v[56:59]
v_exp_f32 v65,v65
v_mfma_f32_16x16x32_bf16 a[64:67],a[192:195],v[96:99],a[64:67]
v_exp_f32 v66,v66
v_mfma_f32_16x16x32_bf16 a[64:67],a[196:199],v[104:107],a[64:67]
v_exp_f32 v67,v67
v_mfma_f32_16x16x32_bf16 a[68:71],a[200:203],v[96:99],a[68:71]
v_exp_f32 v68,v68
v_mfma_f32_16x16x32_bf16 a[68:71],a[204:207],v[104:107],a[68:71]
s_add_u32 s10,s20,s74
s_addc_u32 s11,s21,0
v_cvt_pk_bf16_f32 v64,v64,v65
v_mfma_f32_16x16x32_bf16 a[72:75],a[208:211],v[96:99],a[72:75]
v_exp_f32 v69,v69
v_mfma_f32_16x16x32_bf16 a[72:75],a[212:215],v[104:107],a[72:75]
s_add_u32 m0,s76,0
v_exp_f32 v70,v70
v_mfma_f32_16x16x32_bf16 a[76:79],a[216:219],v[96:99],a[76:79]
v_exp_f32 v71,v71
v_mfma_f32_16x16x32_bf16 a[76:79],a[220:223],v[104:107],a[76:79]
global_load_lds_dwordx4 v4,s[10:11] nt
v_cvt_pk_bf16_f32 v65,v66,v67
v_mfma_f32_16x16x32_bf16 a[80:83],a[224:227],v[96:99],a[80:83]
v_cvt_pk_bf16_f32 v66,v68,v69
v_cvt_pk_bf16_f32 v67,v70,v71
v_mfma_f32_16x16x32_bf16 a[80:83],a[228:231],v[104:107],a[80:83]
v_exp_f32 v72,v72
v_mfma_f32_16x16x32_bf16 a[84:87],a[232:235],v[96:99],a[84:87]
v_exp_f32 v73,v73
v_mfma_f32_16x16x32_bf16 a[84:87],a[236:239],v[104:107],a[84:87]
v_exp_f32 v74,v74
v_mfma_f32_16x16x32_bf16 a[88:91],a[240:243],v[96:99],a[88:91]
v_exp_f32 v75,v75
v_mfma_f32_16x16x32_bf16 a[88:91],a[244:247],v[104:107],a[88:91]
v_exp_f32 v76,v76
v_mfma_f32_16x16x32_bf16 a[92:95],a[248:251],v[96:99],a[92:95]
v_exp_f32 v77,v77
v_mfma_f32_16x16x32_bf16 a[92:95],a[252:255],v[104:107],a[92:95]
v_exp_f32 v78,v78
v_mfma_f32_16x16x32_bf16 v[80:83],a[128:131],v[160:163],v[36:39]
v_exp_f32 v79,v79
v_mfma_f32_16x16x32_bf16 v[80:83],a[132:135],v[168:171],v[80:83]
v_cvt_pk_bf16_f32 v72,v72,v73
v_cvt_pk_bf16_f32 v73,v74,v75
v_mfma_f32_16x16x32_bf16 v[80:83],a[136:139],v[176:179],v[80:83]
v_cvt_pk_bf16_f32 v74,v76,v77
v_cvt_pk_bf16_f32 v75,v78,v79
v_mfma_f32_16x16x32_bf16 v[80:83],a[140:143],v[184:187],v[80:83]
v_rcp_f32 v24,v24
v_mfma_f32_16x16x32_bf16 v[84:87],a[156:159],v[184:187],v[36:39]
v_accvgpr_read_b32 v17,a9
v_accvgpr_read_b32 v18,a10
v_mfma_f32_16x16x32_bf16 v[84:87],a[152:155],v[176:179],v[84:87]
v_accvgpr_read_b32 v19,a11
v_mul_f32 v8,v8,v24
v_mfma_f32_16x16x32_bf16 v[84:87],a[148:151],v[168:171],v[84:87]
global_load_lds_dwordx4 v4,s[10:11] offset:1024 nt
v_mul_f32 v9,v9,v24
v_mfma_f32_16x16x32_bf16 v[84:87],a[144:147],v[160:163],v[84:87]
v_mul_f32 v10,v10,v24
v_mul_f32 v11,v11,v24
v_mfma_f32_16x16x32_bf16 v[88:91],a[160:163],v[160:163],v[36:39]
v_mul_f32 v12,v12,v24
v_mul_f32 v13,v13,v24
v_mfma_f32_16x16x32_bf16 v[88:91],a[164:167],v[168:171],v[88:91]
)A"
R"A(v_mul_f32 v14,v14,v24
v_mul_f32 v15,v15,v24
v_mfma_f32_16x16x32_bf16 v[88:91],a[168:171],v[176:179],v[88:91]
v_cvt_pk_bf16_f32 v8,v8,v9
v_cvt_pk_bf16_f32 v9,v10,v11
v_mfma_f32_16x16x32_bf16 v[88:91],a[172:175],v[184:187],v[88:91]
v_cvt_pk_bf16_f32 v10,v12,v13
v_cvt_pk_bf16_f32 v11,v14,v15
v_mfma_f32_16x16x32_bf16 v[92:95],a[188:191],v[184:187],v[36:39]
ds_write_b128 v7,v[8:11] offset:6144
v_mfma_f32_16x16x32_bf16 v[92:95],a[184:187],v[176:179],v[92:95]
v_accvgpr_read_b32 v20,a12
v_accvgpr_read_b32 v21,a13
v_mfma_f32_16x16x32_bf16 v[92:95],a[180:183],v[168:171],v[92:95]
v_accvgpr_read_b32 v22,a14
v_accvgpr_read_b32 v23,a15
v_mfma_f32_16x16x32_bf16 v[92:95],a[176:179],v[160:163],v[92:95]
s_cmp_le_u32 s5,s7
s_cselect_b32 s4,2,0
v_accvgpr_read_b32 v8,a16
v_mfma_f32_16x16x32_bf16 v[60:63],v[28:31],v[112:115],v[60:63]
s_sub_u32 s5,s5,s4
v_accvgpr_read_b32 v9,a17
v_accvgpr_read_b32 v10,a18
v_mfma_f32_16x16x32_bf16 v[60:63],v[28:31],v[120:123],v[60:63]
s_max_i32 s5,s5,0
v_accvgpr_read_b32 v11,a19
v_mul_f32 v16,v16,v24
v_mfma_f32_16x16x32_bf16 a[96:99],a[192:195],v[112:115],a[96:99]
s_sub_u32 s7,s7,2
ds_read_b64_tr_b16 a[192:193],v2
ds_read_b64_tr_b16 a[194:195],v2 offset:4096
v_mfma_f32_16x16x32_bf16 a[96:99],a[196:199],v[120:123],a[96:99]
s_max_i32 s7,s7,0
ds_read_b64_tr_b16 a[196:197],v2 offset:8192
v_mul_f32 v17,v17,v24
v_mfma_f32_16x16x32_bf16 a[100:103],a[200:203],v[112:115],a[100:103]
global_load_lds_dwordx4 v5,s[10:11] offset:2048 nt
ds_read_b64_tr_b16 a[198:199],v2 offset:12288
v_mfma_f32_16x16x32_bf16 a[100:103],a[204:207],v[120:123],a[100:103]
ds_read_b64_tr_b16 a[200:201],v3
v_mul_f32 v18,v18,v24
v_mfma_f32_16x16x32_bf16 a[104:107],a[208:211],v[112:115],a[104:107]
ds_read_b64_tr_b16 a[202:203],v3 offset:4096
ds_read_b64_tr_b16 a[204:205],v3 offset:8192
v_mfma_f32_16x16x32_bf16 a[104:107],a[212:215],v[120:123],a[104:107]
ds_read_b64_tr_b16 a[206:207],v3 offset:12288
v_mul_f32 v19,v19,v24
v_mfma_f32_16x16x32_bf16 a[108:111],a[216:219],v[112:115],a[108:111]
ds_read_b64_tr_b16 a[208:209],v2 offset:256
v_mul_f32 v20,v20,v24
v_mfma_f32_16x16x32_bf16 a[108:111],a[220:223],v[120:123],a[108:111]
ds_read_b64_tr_b16 a[210:211],v2 offset:4352
v_mul_f32 v21,v21,v24
v_mfma_f32_16x16x32_bf16 a[112:115],a[224:227],v[112:115],a[112:115]
ds_read_b64_tr_b16 a[212:213],v2 offset:8448
ds_read_b64_tr_b16 a[214:215],v2 offset:12544
v_mfma_f32_16x16x32_bf16 a[112:115],a[228:231],v[120:123],a[112:115]
ds_read_b64_tr_b16 a[216:217],v3 offset:256
v_mul_f32 v22,v22,v24
v_mfma_f32_16x16x32_bf16 a[116:119],a[232:235],v[112:115],a[116:119]
ds_read_b64_tr_b16 a[218:219],v3 offset:4352
v_mul_f32 v23,v23,v24
v_mfma_f32_16x16x32_bf16 a[116:119],a[236:239],v[120:123],a[116:119]
ds_read_b64_tr_b16 a[220:221],v3 offset:8448
v_cvt_pk_bf16_f32 v16,v16,v17
v_mfma_f32_16x16x32_bf16 a[120:123],a[240:243],v[112:115],a[120:123]
ds_read_b64_tr_b16 a[222:223],v3 offset:12544
ds_read_b64_tr_b16 a[224:225],v2 offset:512
v_mfma_f32_16x16x32_bf16 a[120:123],a[244:247],v[120:123],a[120:123]
ds_read_b64_tr_b16 a[226:227],v2 offset:4608
v_cvt_pk_bf16_f32 v17,v18,v19
v_mfma_f32_16x16x32_bf16 a[124:127],a[248:251],v[112:115],a[124:127]
ds_read_b64_tr_b16 a[228:229],v2 offset:8704
v_cvt_pk_bf16_f32 v18,v20,v21
v_mfma_f32_16x16x32_bf16 a[124:127],a[252:255],v[120:123],a[124:127]
s_waitcnt lgkmcnt(9)
ds_read_b64_tr_b16 a[230:231],v2 offset:12800
v_cvt_pk_bf16_f32 v19,v22,v23
v_mfma_f32_16x16x32_bf16 v[96:99],a[128:131],v[192:195],v[40:43]
ds_read_b64_tr_b16 a[232:233],v3 offset:512
ds_read_b64_tr_b16 a[234:235],v3 offset:4608
v_mfma_f32_16x16x32_bf16 v[96:99],a[132:135],v[200:203],v[96:99]
global_load_lds_dwordx4 v5,s[10:11] offset:3072
ds_read_b64_tr_b16 a[236:237],v3 offset:8704
v_mfma_f32_16x16x32_bf16 v[96:99],a[136:139],v[208:211],v[96:99]
ds_read_b64_tr_b16 a[238:239],v3 offset:12800
v_accvgpr_read_b32 v12,a20
v_mfma_f32_16x16x32_bf16 v[96:99],a[140:143],v[216:219],v[96:99]
ds_read_b64_tr_b16 a[240:241],v2 offset:768
v_accvgpr_read_b32 v13,a21
v_mfma_f32_16x16x32_bf16 v[100:103],a[156:159],v[216:219],v[40:43]
ds_read_b64_tr_b16 a[242:243],v2 offset:4864
ds_read_b64_tr_b16 a[244:245],v2 offset:8960
v_mfma_f32_16x16x32_bf16 v[100:103],a[152:155],v[208:211],v[100:103]
ds_read_b64_tr_b16 a[246:247],v2 offset:13056
v_accvgpr_read_b32 v14,a22
v_mfma_f32_16x16x32_bf16 v[100:103],a[148:151],v[200:203],v[100:103]
ds_read_b64_tr_b16 a[248:249],v3 offset:768
v_accvgpr_read_b32 v15,a23
v_mfma_f32_16x16x32_bf16 v[100:103],a[144:147],v[192:195],v[100:103]
ds_read_b64_tr_b16 a[250:251],v3 offset:4864
v_accvgpr_read_b32 v20,a28
v_mfma_f32_16x16x32_bf16 v[104:107],a[160:163],v[192:195],v[40:43]
ds_read_b64_tr_b16 a[252:253],v3 offset:8960
ds_read_b64_tr_b16 a[254:255],v3 offset:13056
v_mfma_f32_16x16x32_bf16 v[104:107],a[164:167],v[200:203],v[104:107]
v_accvgpr_read_b32 v21,a29
v_accvgpr_read_b32 v22,a30
v_mfma_f32_16x16x32_bf16 v[104:107],a[168:171],v[208:211],v[104:107]
ds_write_b128 v7,v[16:19] offset:7168
v_mfma_f32_16x16x32_bf16 v[104:107],a[172:175],v[216:219],v[104:107]
s_add_u32 s10,s24,s74
s_addc_u32 s11,s25,0
v_accvgpr_read_b32 v23,a31
v_mfma_f32_16x16x32_bf16 v[108:111],a[188:191],v[216:219],v[40:43]
v_accvgpr_read_b32 v16,a24
v_accvgpr_read_b32 v17,a25
v_mfma_f32_16x16x32_bf16 v[108:111],a[184:187],v[208:211],v[108:111]
s_add_u32 m0,s76,16384
v_accvgpr_read_b32 v18,a26
v_accvgpr_read_b32 v19,a27
v_mfma_f32_16x16x32_bf16 v[108:111],a[180:183],v[200:203],v[108:111]
v_cmp_nlt_f32 s[54:55],v48,s73
v_cmp_nlt_f32 s[56:57],v52,s73
v_mfma_f32_16x16x32_bf16 v[108:111],a[176:179],v[192:195],v[108:111]
s_or_b64 s[54:55],s[54:55],s[56:57]
global_load_lds_dwordx4 v4,s[10:11]
v_mfma_f32_16x16x32_bf16 a[0:3],a[192:195],v[64:67],0
v_cmp_nlt_f32 s[56:57],v56,s73
v_mul_f32 v8,v8,v24
v_mfma_f32_16x16x32_bf16 a[0:3],a[196:199],v[72:75],a[0:3]
s_or_b64 s[54:55],s[54:55],s[56:57]
v_mul_f32 v9,v9,v24
v_mul_f32 v10,v10,v24
v_mfma_f32_16x16x32_bf16 a[4:7],a[200:203],v[64:67],0
v_mul_f32 v11,v11,v24
v_mul_f32 v12,v12,v24
v_mfma_f32_16x16x32_bf16 a[4:7],a[204:207],v[72:75],a[4:7]
v_mul_f32 v13,v13,v24
v_mul_f32 v14,v14,v24
v_mfma_f32_16x16x32_bf16 a[8:11],a[208:211],v[64:67],0
s_waitcnt lgkmcnt(0)
v_mul_f32 v15,v15,v24
v_cvt_pk_bf16_f32 v8,v8,v9
v_mfma_f32_16x16x32_bf16 a[8:11],a[212:215],v[72:75],a[8:11]
v_cvt_pk_bf16_f32 v9,v10,v11
v_cvt_pk_bf16_f32 v10,v12,v13
v_mfma_f32_16x16x32_bf16 a[12:15],a[216:219],v[64:67],0
v_cvt_pk_bf16_f32 v11,v14,v15
v_mul_f32 v16,v16,v24
v_mfma_f32_16x16x32_bf16 a[12:15],a[220:223],v[72:75],a[12:15]
v_mul_f32 v17,v17,v24
v_mul_f32 v18,v18,v24
v_mfma_f32_16x16x32_bf16 a[16:19],a[224:227],v[64:67],0
v_mul_f32 v19,v19,v24
v_mul_f32 v20,v20,v24
v_mfma_f32_16x16x32_bf16 a[16:19],a[228:231],v[72:75],a[16:19]
v_mul_f32 v21,v21,v24
v_mul_f32 v22,v22,v24
v_mfma_f32_16x16x32_bf16 a[20:23],a[232:235],v[64:67],0
s_getreg_b32 s92,hwreg(HW_REG_IB_STS,0,4)
s_min_u32 s4,s92,s6
s_mov_b32 s6,s92
v_mfma_f32_16x16x32_bf16 a[20:23],a[236:239],v[72:75],a[20:23]
s_cmp_gt_u32 s4,s9
s_cselect_b32 s4,15,s5
v_mul_f32 v23,v23,v24
v_mfma_f32_16x16x32_bf16 a[24:27],a[240:243],v[64:67],0
v_cvt_pk_bf16_f32 v16,v16,v17
v_cvt_pk_bf16_f32 v17,v18,v19
v_mfma_f32_16x16x32_bf16 a[24:27],a[244:247],v[72:75],a[24:27]
v_cvt_pk_bf16_f32 v18,v20,v21
v_cvt_pk_bf16_f32 v19,v22,v23
v_mfma_f32_16x16x32_bf16 a[28:31],a[248:251],v[64:67],0
global_load_lds_dwordx4 v5,s[10:11] offset:1024
v_cmp_nlt_f32 s[56:57],v60,s73
v_mfma_f32_16x16x32_bf16 a[28:31],a[252:255],v[72:75],a[28:31]
s_or_b64 s[54:55],s[54:55],s[56:57]
v_exp_f32 v80,v80
v_mfma_f32_16x16x32_bf16 v[48:51],v[28:31],v[64:67],0
s_getpc_b64 s[92:93]
s_cmp_lg_u32 s4,0
s_cbranch_scc1 L_pace_%=
v_exp_f32 v81,v81
v_exp_f32 v82,v82
v_exp_f32 v83,v83
v_exp_f32 v84,v84
v_exp_f32 v85,v85
v_exp_f32 v86,v86
v_exp_f32 v87,v87
v_exp_f32 v88,v88
v_exp_f32 v89,v89
v_mfma_f32_16x16x32_bf16 v[48:51],v[28:31],v[72:75],v[48:51]
ds_write_b128 v7,v[8:11]
v_add_u32 v1,s78,v1
v_max_f32 v25,0x00800000,v52
v_rcp_f32 v25,v25
v_max_f32 v26,0x00800000,v56
v_rcp_f32 v26,v26
v_cvt_pk_bf16_f32 v80,v80,v81
v_cvt_pk_bf16_f32 v81,v82,v83
v_cvt_pk_bf16_f32 v82,v84,v85
v_cvt_pk_bf16_f32 v83,v86,v87
v_cvt_pk_bf16_f32 v88,v88,v89
v_max_f32 v27,0x00800000,v60
v_rcp_f32 v27,v27
ds_write_b128 v7,v[16:19] offset:1024
ds_read_b128 v[44:47],v7 offset:3072
s_cmp_lg_u64 s[54:55],0
s_cbranch_scc1 L_slow0_%=
L_ok0_%=:
s_waitcnt vmcnt(14)
s_barrier
s_waitcnt lgkmcnt(0)
v_mfma_f32_16x16x32_bf16 v[112:115],a[128:131],v[224:227],v[44:47]
s_add_u32 s10,s24,s74
s_addc_u32 s11,s25,0
s_add_u32 m0,s76,16384
v_mfma_f32_16x16x32_bf16 v[112:115],a[132:135],v[232:235],v[112:115]
s_add_u32 s75,s75,16384
v_exp_f32 v90,v90
v_mfma_f32_16x16x32_bf16 v[112:115],a[136:139],v[240:243],v[112:115]
global_load_lds_dwordx4 v4,s[10:11] offset:2048
ds_read_b128 a[128:131],v1
v_mfma_f32_16x16x32_bf16 v[112:115],a[140:143],v[248:251],v[112:115]
s_add_u32 s74,s74,16384
v_exp_f32 v91,v91
v_mfma_f32_16x16x32_bf16 v[116:119],a[156:159],v[248:251],v[44:47]
s_and_b32 s74,s74,0x3ffff
v_exp_f32 v92,v92
v_mfma_f32_16x16x32_bf16 v[116:119],a[152:155],v[240:243],v[116:119]
s_and_b32 s69,s75,0xfffc0000
v_exp_f32 v93,v93
v_mfma_f32_16x16x32_bf16 v[116:119],a[148:151],v[232:235],v[116:119]
s_or_b32 s74,s74,s69
v_exp_f32 v94,v94
v_mfma_f32_16x16x32_bf16 v[116:119],a[144:147],v[224:227],v[116:119]
s_sub_u32 s76,s76,32768
v_exp_f32 v95,v95
v_mfma_f32_16x16x32_bf16 v[120:123],a[160:163],v[224:227],v[44:47]
s_and_b32 s76,s76,0x1ffff
v_cvt_pk_bf16_f32 v89,v90,v91
v_cvt_pk_bf16_f32 v90,v92,v93
v_mfma_f32_16x16x32_bf16 v[120:123],a[164:167],v[232:235],v[120:123]
s_add_u32 s75,s75,16384
ds_read_b128 a[132:135],v1 offset:256
v_cvt_pk_bf16_f32 v91,v94,v95
v_mfma_f32_16x16x32_bf16 v[120:123],a[168:171],v[240:243],v[120:123]
s_and_b32 s69,s75,0xfffc0000
v_exp_f32 v96,v96
v_mfma_f32_16x16x32_bf16 v[120:123],a[172:175],v[248:251],v[120:123]
s_add_u32 s75,s75,16384
ds_read_b128 a[136:139],v1 offset:512
v_accvgpr_read_b32 v16,a32
v_mfma_f32_16x16x32_bf16 v[124:127],a[188:191],v[248:251],v[44:47]
v_exp_f32 v97,v97
v_mfma_f32_16x16x32_bf16 v[124:127],a[184:187],v[240:243],v[124:127]
ds_read_b128 a[140:143],v1 offset:768
v_cvt_pk_bf16_f32 v96,v96,v97
v_mfma_f32_16x16x32_bf16 v[124:127],a[180:183],v[232:235],v[124:127]
v_exp_f32 v98,v98
v_mfma_f32_16x16x32_bf16 v[124:127],a[176:179],v[224:227],v[124:127]
ds_read_b128 a[156:159],v1 offset:4864
v_accvgpr_read_b32 v17,a33
v_mfma_f32_16x16x32_bf16 v[132:135],a[192:195],v[80:83],0
global_load_lds_dwordx4 v5,s[10:11] offset:3072
v_accvgpr_read_b32 v18,a34
v_mfma_f32_16x16x32_bf16 v[132:135],a[196:199],v[88:91],v[132:135]
ds_read_b128 a[152:155],v1 offset:4608
v_accvgpr_read_b32 v19,a35
v_mfma_f32_16x16x32_bf16 v[140:143],a[200:203],v[80:83],0
v_exp_f32 v99,v99
v_mfma_f32_16x16x32_bf16 v[140:143],a[204:207],v[88:91],v[140:143]
ds_read_b128 a[148:151],v1 offset:4352
v_cvt_pk_bf16_f32 v97,v98,v99
v_mfma_f32_16x16x32_bf16 v[148:151],a[208:211],v[80:83],0
v_exp_f32 v100,v100
v_mfma_f32_16x16x32_bf16 v[148:151],a[212:215],v[88:91],v[148:151]
ds_read_b128 a[144:147],v1 offset:4096
v_accvgpr_read_b32 v20,a36
v_mfma_f32_16x16x32_bf16 v[156:159],a[216:219],v[80:83],0
v_exp_f32 v101,v101
v_mfma_f32_16x16x32_bf16 v[156:159],a[220:223],v[88:91],v[156:159]
ds_read_b128 a[160:163],v1 offset:8192
v_cvt_pk_bf16_f32 v98,v100,v101
v_mfma_f32_16x16x32_bf16 v[164:167],a[224:227],v[80:83],0
v_exp_f32 v102,v102
v_mfma_f32_16x16x32_bf16 v[164:167],a[228:231],v[88:91],v[164:167]
ds_read_b128 a[164:167],v1 offset:8448
v_accvgpr_read_b32 v21,a37
v_mfma_f32_16x16x32_bf16 v[172:175],a[232:235],v[80:83],0
s_add_u32 s10,s20,s74
s_addc_u32 s11,s21,0
v_accvgpr_read_b32 v22,a38
v_mfma_f32_16x16x32_bf16 v[172:175],a[236:239],v[88:91],v[172:175]
ds_read_b128 a[168:171],v1 offset:8704
v_accvgpr_read_b32 v23,a39
v_mfma_f32_16x16x32_bf16 v[180:183],a[240:243],v[80:83],0
s_add_u32 m0,s76,0
v_exp_f32 v103,v103
v_mfma_f32_16x16x32_bf16 v[180:183],a[244:247],v[88:91],v[180:183]
ds_read_b128 a[172:175],v1 offset:8960
v_cvt_pk_bf16_f32 v99,v102,v103
v_mfma_f32_16x16x32_bf16 v[188:191],a[248:251],v[80:83],0
global_load_lds_dwordx4 v4,s[10:11] nt
)A"
R"A(v_mul_f32 v16,v16,v25
v_mfma_f32_16x16x32_bf16 v[188:191],a[252:255],v[88:91],v[188:191]
ds_read_b128 a[188:191],v1 offset:13056
v_mul_f32 v17,v17,v25
v_mfma_f32_16x16x32_bf16 v[52:55],v[28:31],v[80:83],0
v_exp_f32 v104,v104
v_mfma_f32_16x16x32_bf16 v[52:55],v[28:31],v[88:91],v[52:55]
s_waitcnt lgkmcnt(3)
ds_read_b128 a[184:187],v1 offset:12800
v_mul_f32 v18,v18,v25
v_mfma_f32_16x16x32_bf16 v[64:67],a[128:131],v[128:131],v[32:35]
v_exp_f32 v105,v105
v_mfma_f32_16x16x32_bf16 v[64:67],a[132:135],v[136:139],v[64:67]
ds_read_b128 a[180:183],v1 offset:12544
v_cvt_pk_bf16_f32 v104,v104,v105
v_mfma_f32_16x16x32_bf16 v[64:67],a[136:139],v[144:147],v[64:67]
v_exp_f32 v106,v106
v_mfma_f32_16x16x32_bf16 v[64:67],a[140:143],v[152:155],v[64:67]
ds_read_b128 a[176:179],v1 offset:12288
v_mul_f32 v19,v19,v25
v_mfma_f32_16x16x32_bf16 v[68:71],a[156:159],v[152:155],v[32:35]
v_exp_f32 v107,v107
v_mfma_f32_16x16x32_bf16 v[68:71],a[152:155],v[144:147],v[68:71]
v_exp_f32 v108,v108
v_mfma_f32_16x16x32_bf16 v[68:71],a[148:151],v[136:139],v[68:71]
v_exp_f32 v109,v109
v_mfma_f32_16x16x32_bf16 v[68:71],a[144:147],v[128:131],v[68:71]
v_exp_f32 v110,v110
v_mfma_f32_16x16x32_bf16 v[72:75],a[160:163],v[128:131],v[32:35]
v_exp_f32 v111,v111
v_mfma_f32_16x16x32_bf16 v[72:75],a[164:167],v[136:139],v[72:75]
s_waitcnt lgkmcnt(1)
v_cvt_pk_bf16_f32 v105,v106,v107
v_cvt_pk_bf16_f32 v106,v108,v109
v_mfma_f32_16x16x32_bf16 v[72:75],a[168:171],v[144:147],v[72:75]
global_load_lds_dwordx4 v4,s[10:11] offset:1024 nt
v_cvt_pk_bf16_f32 v107,v110,v111
v_mfma_f32_16x16x32_bf16 v[72:75],a[172:175],v[152:155],v[72:75]
v_exp_f32 v112,v112
v_mfma_f32_16x16x32_bf16 v[76:79],a[188:191],v[152:155],v[32:35]
v_exp_f32 v113,v113
v_mfma_f32_16x16x32_bf16 v[76:79],a[184:187],v[144:147],v[76:79]
v_exp_f32 v114,v114
v_mfma_f32_16x16x32_bf16 v[76:79],a[180:183],v[136:139],v[76:79]
s_waitcnt lgkmcnt(0)
v_exp_f32 v115,v115
v_mfma_f32_16x16x32_bf16 v[76:79],a[176:179],v[128:131],v[76:79]
v_exp_f32 v116,v116
v_mfma_f32_16x16x32_bf16 v[196:199],a[192:195],v[96:99],0
v_exp_f32 v117,v117
v_mfma_f32_16x16x32_bf16 v[196:199],a[196:199],v[104:107],v[196:199]
v_exp_f32 v118,v118
v_mfma_f32_16x16x32_bf16 v[204:207],a[200:203],v[96:99],0
v_exp_f32 v119,v119
v_mfma_f32_16x16x32_bf16 v[204:207],a[204:207],v[104:107],v[204:207]
v_cvt_pk_bf16_f32 v112,v112,v113
v_cvt_pk_bf16_f32 v113,v114,v115
v_mfma_f32_16x16x32_bf16 v[212:215],a[208:211],v[96:99],0
v_cvt_pk_bf16_f32 v114,v116,v117
v_cvt_pk_bf16_f32 v115,v118,v119
v_mfma_f32_16x16x32_bf16 v[212:215],a[212:215],v[104:107],v[212:215]
v_exp_f32 v120,v120
v_mfma_f32_16x16x32_bf16 v[220:223],a[216:219],v[96:99],0
v_exp_f32 v121,v121
v_mfma_f32_16x16x32_bf16 v[220:223],a[220:223],v[104:107],v[220:223]
v_exp_f32 v122,v122
v_mfma_f32_16x16x32_bf16 v[228:231],a[224:227],v[96:99],0
global_load_lds_dwordx4 v5,s[10:11] offset:2048 nt
v_cvt_pk_bf16_f32 v120,v120,v121
v_mfma_f32_16x16x32_bf16 v[228:231],a[228:231],v[104:107],v[228:231]
v_exp_f32 v123,v123
v_mfma_f32_16x16x32_bf16 v[236:239],a[232:235],v[96:99],0
v_exp_f32 v124,v124
v_mfma_f32_16x16x32_bf16 v[236:239],a[236:239],v[104:107],v[236:239]
v_exp_f32 v125,v125
v_mfma_f32_16x16x32_bf16 v[244:247],a[240:243],v[96:99],0
v_exp_f32 v126,v126
v_mfma_f32_16x16x32_bf16 v[244:247],a[244:247],v[104:107],v[244:247]
v_exp_f32 v127,v127
v_mfma_f32_16x16x32_bf16 v[252:255],a[248:251],v[96:99],0
v_cvt_pk_bf16_f32 v121,v122,v123
v_cvt_pk_bf16_f32 v122,v124,v125
v_mfma_f32_16x16x32_bf16 v[252:255],a[252:255],v[104:107],v[252:255]
v_cvt_pk_bf16_f32 v123,v126,v127
v_mul_f32 v20,v20,v25
v_mfma_f32_16x16x32_bf16 v[56:59],v[28:31],v[96:99],0
v_mul_f32 v21,v21,v25
v_mul_f32 v22,v22,v25
v_mfma_f32_16x16x32_bf16 v[56:59],v[28:31],v[104:107],v[56:59]
v_mul_f32 v23,v23,v25
v_cvt_pk_bf16_f32 v16,v16,v17
v_mfma_f32_16x16x32_bf16 v[80:83],a[128:131],v[160:163],v[36:39]
v_cvt_pk_bf16_f32 v17,v18,v19
v_cvt_pk_bf16_f32 v18,v20,v21
v_mfma_f32_16x16x32_bf16 v[80:83],a[132:135],v[168:171],v[80:83]
v_cvt_pk_bf16_f32 v19,v22,v23
v_accvgpr_read_b32 v20,a44
v_mfma_f32_16x16x32_bf16 v[80:83],a[136:139],v[176:179],v[80:83]
ds_write_b128 v7,v[16:19] offset:2048
v_mfma_f32_16x16x32_bf16 v[80:83],a[140:143],v[184:187],v[80:83]
v_accvgpr_read_b32 v21,a45
v_accvgpr_read_b32 v22,a46
v_mfma_f32_16x16x32_bf16 v[84:87],a[156:159],v[184:187],v[36:39]
v_accvgpr_read_b32 v16,a40
v_accvgpr_read_b32 v17,a41
v_mfma_f32_16x16x32_bf16 v[84:87],a[152:155],v[176:179],v[84:87]
global_load_lds_dwordx4 v5,s[10:11] offset:3072
v_accvgpr_read_b32 v18,a42
v_mfma_f32_16x16x32_bf16 v[84:87],a[148:151],v[168:171],v[84:87]
v_accvgpr_read_b32 v19,a43
v_accvgpr_read_b32 v23,a47
v_mfma_f32_16x16x32_bf16 v[84:87],a[144:147],v[160:163],v[84:87]
v_accvgpr_read_b32 v8,a48
v_accvgpr_read_b32 v9,a49
v_mfma_f32_16x16x32_bf16 v[88:91],a[160:163],v[160:163],v[36:39]
v_accvgpr_read_b32 v10,a50
v_accvgpr_read_b32 v11,a51
v_mfma_f32_16x16x32_bf16 v[88:91],a[164:167],v[168:171],v[88:91]
v_mul_f32 v16,v16,v25
v_mul_f32 v17,v17,v25
v_mfma_f32_16x16x32_bf16 v[88:91],a[168:171],v[176:179],v[88:91]
v_mul_f32 v18,v18,v25
v_mul_f32 v19,v19,v25
v_mfma_f32_16x16x32_bf16 v[88:91],a[172:175],v[184:187],v[88:91]
v_mul_f32 v20,v20,v25
v_mul_f32 v21,v21,v25
v_mfma_f32_16x16x32_bf16 v[92:95],a[188:191],v[184:187],v[36:39]
v_mul_f32 v22,v22,v25
v_mul_f32 v23,v23,v25
v_mfma_f32_16x16x32_bf16 v[92:95],a[184:187],v[176:179],v[92:95]
v_cvt_pk_bf16_f32 v16,v16,v17
v_cvt_pk_bf16_f32 v17,v18,v19
v_mfma_f32_16x16x32_bf16 v[92:95],a[180:183],v[168:171],v[92:95]
v_cvt_pk_bf16_f32 v18,v20,v21
v_cvt_pk_bf16_f32 v19,v22,v23
v_mfma_f32_16x16x32_bf16 v[92:95],a[176:179],v[160:163],v[92:95]
s_add_u32 s10,s24,s74
s_addc_u32 s11,s25,0
v_accvgpr_read_b32 v12,a52
v_mfma_f32_16x16x32_bf16 a[32:35],a[192:195],v[112:115],0
ds_write_b128 v7,v[16:19] offset:3072
v_mfma_f32_16x16x32_bf16 a[32:35],a[196:199],v[120:123],a[32:35]
s_add_u32 m0,s76,16384
v_accvgpr_read_b32 v13,a53
v_accvgpr_read_b32 v14,a54
v_mfma_f32_16x16x32_bf16 a[36:39],a[200:203],v[112:115],0
v_accvgpr_read_b32 v15,a55
v_accvgpr_read_b32 v16,a56
v_mfma_f32_16x16x32_bf16 a[36:39],a[204:207],v[120:123],a[36:39]
global_load_lds_dwordx4 v4,s[10:11]
v_accvgpr_read_b32 v17,a57
v_mfma_f32_16x16x32_bf16 a[40:43],a[208:211],v[112:115],0
v_accvgpr_read_b32 v18,a58
v_accvgpr_read_b32 v19,a59
v_mfma_f32_16x16x32_bf16 a[40:43],a[212:215],v[120:123],a[40:43]
v_accvgpr_read_b32 v20,a60
v_accvgpr_read_b32 v21,a61
v_mfma_f32_16x16x32_bf16 a[44:47],a[216:219],v[112:115],0
v_accvgpr_read_b32 v22,a62
v_accvgpr_read_b32 v23,a63
v_mfma_f32_16x16x32_bf16 a[44:47],a[220:223],v[120:123],a[44:47]
v_add_u32 v2,s78,v2
v_add_u32 v3,s78,v3
v_mfma_f32_16x16x32_bf16 a[48:51],a[224:227],v[112:115],0
s_sub_u32 s78,s77,32768
ds_read_b64_tr_b16 a[192:193],v2
ds_read_b64_tr_b16 a[194:195],v2 offset:4096
v_mfma_f32_16x16x32_bf16 a[48:51],a[228:231],v[120:123],a[48:51]
s_and_b32 s78,s78,0x1ffff
ds_read_b64_tr_b16 a[196:197],v2 offset:8192
v_mul_f32 v8,v8,v25
v_mfma_f32_16x16x32_bf16 a[52:55],a[232:235],v[112:115],0
s_sub_u32 s78,s78,s77
ds_read_b64_tr_b16 a[198:199],v2 offset:12288
v_mul_f32 v9,v9,v25
v_mfma_f32_16x16x32_bf16 a[52:55],a[236:239],v[120:123],a[52:55]
s_add_u32 s77,s77,s78
ds_read_b64_tr_b16 a[200:201],v3
v_mul_f32 v10,v10,v25
v_mfma_f32_16x16x32_bf16 a[56:59],a[240:243],v[112:115],0
ds_read_b64_tr_b16 a[202:203],v3 offset:4096
ds_read_b64_tr_b16 a[204:205],v3 offset:8192
v_mfma_f32_16x16x32_bf16 a[56:59],a[244:247],v[120:123],a[56:59]
ds_read_b64_tr_b16 a[206:207],v3 offset:12288
v_mul_f32 v11,v11,v25
v_mfma_f32_16x16x32_bf16 a[60:63],a[248:251],v[112:115],0
ds_read_b64_tr_b16 a[208:209],v2 offset:256
v_mul_f32 v12,v12,v25
v_mfma_f32_16x16x32_bf16 a[60:63],a[252:255],v[120:123],a[60:63]
ds_read_b64_tr_b16 a[210:211],v2 offset:4352
v_mul_f32 v13,v13,v25
v_mfma_f32_16x16x32_bf16 v[60:63],v[28:31],v[112:115],0
ds_read_b64_tr_b16 a[212:213],v2 offset:8448
ds_read_b64_tr_b16 a[214:215],v2 offset:12544
v_mfma_f32_16x16x32_bf16 v[60:63],v[28:31],v[120:123],v[60:63]
global_load_lds_dwordx4 v5,s[10:11] offset:1024
ds_read_b64_tr_b16 a[216:217],v3 offset:256
v_mfma_f32_16x16x32_bf16 v[96:99],a[128:131],v[192:195],v[40:43]
v_exp_f32 v64,v64
v_mfma_f32_16x16x32_bf16 v[96:99],a[132:135],v[200:203],v[96:99]
v_exp_f32 v65,v65
v_mfma_f32_16x16x32_bf16 v[96:99],a[136:139],v[208:211],v[96:99]
v_exp_f32 v66,v66
L_pd8_%=:
s_cbranch_execz L_trt0_%=
v_mfma_f32_16x16x32_bf16 v[96:99],a[140:143],v[216:219],v[96:99]
v_exp_f32 v67,v67
v_mfma_f32_16x16x32_bf16 v[100:103],a[156:159],v[216:219],v[40:43]
v_exp_f32 v68,v68
v_mfma_f32_16x16x32_bf16 v[100:103],a[152:155],v[208:211],v[100:103]
v_exp_f32 v69,v69
v_mfma_f32_16x16x32_bf16 v[100:103],a[148:151],v[200:203],v[100:103]
v_exp_f32 v70,v70
v_mfma_f32_16x16x32_bf16 v[100:103],a[144:147],v[192:195],v[100:103]
v_exp_f32 v71,v71
v_mfma_f32_16x16x32_bf16 v[104:107],a[160:163],v[192:195],v[40:43]
ds_read_b64_tr_b16 a[218:219],v3 offset:4352
v_cvt_pk_bf16_f32 v64,v64,v65
v_mfma_f32_16x16x32_bf16 v[104:107],a[164:167],v[200:203],v[104:107]
v_cvt_pk_bf16_f32 v65,v66,v67
v_cvt_pk_bf16_f32 v66,v68,v69
v_mfma_f32_16x16x32_bf16 v[104:107],a[168:171],v[208:211],v[104:107]
ds_read_b64_tr_b16 a[220:221],v3 offset:8448
v_cvt_pk_bf16_f32 v67,v70,v71
v_mfma_f32_16x16x32_bf16 v[104:107],a[172:175],v[216:219],v[104:107]
v_exp_f32 v72,v72
v_mfma_f32_16x16x32_bf16 v[108:111],a[188:191],v[216:219],v[40:43]
v_exp_f32 v73,v73
v_mfma_f32_16x16x32_bf16 v[108:111],a[184:187],v[208:211],v[108:111]
global_load_lds_dwordx4 v4,s[10:11] offset:2048
v_exp_f32 v74,v74
v_exp_f32 v75,v75
v_exp_f32 v76,v76
v_exp_f32 v77,v77
v_exp_f32 v78,v78
v_exp_f32 v79,v79
v_mfma_f32_16x16x32_bf16 v[108:111],a[180:183],v[200:203],v[108:111]
ds_read_b64_tr_b16 a[222:223],v3 offset:12544
v_cvt_pk_bf16_f32 v72,v72,v73
v_cvt_pk_bf16_f32 v73,v74,v75
v_cvt_pk_bf16_f32 v74,v76,v77
v_cvt_pk_bf16_f32 v75,v78,v79
v_mfma_f32_16x16x32_bf16 v[108:111],a[176:179],v[192:195],v[108:111]
s_waitcnt lgkmcnt(3)
ds_read_b64_tr_b16 a[224:225],v2 offset:512
ds_read_b64_tr_b16 a[226:227],v2 offset:4608
v_mfma_f32_16x16x32_bf16 a[0:3],a[192:195],v[64:67],a[0:3]
ds_read_b64_tr_b16 a[228:229],v2 offset:8704
v_mul_f32 v14,v14,v25
v_mfma_f32_16x16x32_bf16 a[0:3],a[196:199],v[72:75],a[0:3]
ds_read_b64_tr_b16 a[230:231],v2 offset:12800
v_mul_f32 v15,v15,v25
v_mfma_f32_16x16x32_bf16 a[4:7],a[200:203],v[64:67],a[4:7]
ds_read_b64_tr_b16 a[232:233],v3 offset:512
v_cvt_pk_bf16_f32 v8,v8,v9
v_mfma_f32_16x16x32_bf16 a[4:7],a[204:207],v[72:75],a[4:7]
ds_read_b64_tr_b16 a[234:235],v3 offset:4608
ds_read_b64_tr_b16 a[236:237],v3 offset:8704
v_mfma_f32_16x16x32_bf16 a[8:11],a[208:211],v[64:67],a[8:11]
ds_read_b64_tr_b16 a[238:239],v3 offset:12800
v_cvt_pk_bf16_f32 v9,v10,v11
v_mfma_f32_16x16x32_bf16 a[8:11],a[212:215],v[72:75],a[8:11]
s_waitcnt lgkmcnt(9)
ds_read_b64_tr_b16 a[240:241],v2 offset:768
v_cvt_pk_bf16_f32 v10,v12,v13
v_mfma_f32_16x16x32_bf16 a[12:15],a[216:219],v[64:67],a[12:15]
s_waitcnt lgkmcnt(9)
ds_read_b64_tr_b16 a[242:243],v2 offset:4864
v_cvt_pk_bf16_f32 v11,v14,v15
v_mfma_f32_16x16x32_bf16 a[12:15],a[220:223],v[72:75],a[12:15]
s_waitcnt lgkmcnt(8)
ds_read_b64_tr_b16 a[244:245],v2 offset:8960
ds_read_b64_tr_b16 a[246:247],v2 offset:13056
v_mfma_f32_16x16x32_bf16 a[16:19],a[224:227],v[64:67],a[16:19]
s_waitcnt lgkmcnt(8)
ds_read_b64_tr_b16 a[248:249],v3 offset:768
v_mul_f32 v16,v16,v25
v_mfma_f32_16x16x32_bf16 a[16:19],a[228:231],v[72:75],a[16:19]
s_waitcnt lgkmcnt(1)
ds_read_b64_tr_b16 a[250:251],v3 offset:4864
v_mul_f32 v17,v17,v25
v_mfma_f32_16x16x32_bf16 a[20:23],a[232:235],v[64:67],a[20:23]
s_getreg_b32 s92,hwreg(HW_REG_IB_STS,0,4)
s_min_u32 s4,s92,s6
ds_read_b64_tr_b16 a[252:253],v3 offset:8960
v_mfma_f32_16x16x32_bf16 a[20:23],a[236:239],v[72:75],a[20:23]
s_mov_b32 s6,s92
ds_read_b64_tr_b16 a[254:255],v3 offset:13056
v_mul_f32 v18,v18,v25
v_mfma_f32_16x16x32_bf16 a[24:27],a[240:243],v[64:67],a[24:27]
s_cmp_gt_u32 s4,s9
s_cselect_b32 s4,15,s5
v_mul_f32 v19,v19,v25
v_mfma_f32_16x16x32_bf16 a[24:27],a[244:247],v[72:75],a[24:27]
s_waitcnt lgkmcnt(2)
v_mul_f32 v20,v20,v25
v_mul_f32 v21,v21,v25
)A"
R"A(v_mfma_f32_16x16x32_bf16 a[28:31],a[248:251],v[64:67],a[28:31]
s_waitcnt lgkmcnt(0)
v_mul_f32 v22,v22,v25
v_mul_f32 v23,v23,v25
v_mfma_f32_16x16x32_bf16 a[28:31],a[252:255],v[72:75],a[28:31]
s_getpc_b64 s[92:93]
s_cmp_lg_u32 s4,0
s_cbranch_scc1 L_pace_%=
v_mfma_f32_16x16x32_bf16 v[48:51],v[28:31],v[64:67],v[48:51]
v_cvt_pk_bf16_f32 v12,v16,v17
v_cvt_pk_bf16_f32 v13,v18,v19
v_mfma_f32_16x16x32_bf16 v[48:51],v[28:31],v[72:75],v[48:51]
s_add_u32 s10,s24,s74
s_addc_u32 s11,s25,0
v_cvt_pk_bf16_f32 v14,v20,v21
s_waitcnt vmcnt(15)
s_barrier
v_mfma_f32_16x16x32_bf16 v[112:115],a[128:131],v[224:227],v[44:47]
s_add_u32 m0,s76,16384
v_cvt_pk_bf16_f32 v15,v22,v23
v_accvgpr_read_b32 v16,a112
v_mfma_f32_16x16x32_bf16 v[112:115],a[132:135],v[232:235],v[112:115]
s_add_u32 s74,s74,16384
v_accvgpr_read_b32 v17,a113
v_accvgpr_read_b32 v18,a114
v_mfma_f32_16x16x32_bf16 v[112:115],a[136:139],v[240:243],v[112:115]
global_load_lds_dwordx4 v5,s[10:11] offset:3072
v_accvgpr_read_b32 v19,a115
v_mfma_f32_16x16x32_bf16 v[112:115],a[140:143],v[248:251],v[112:115]
s_and_b32 s74,s74,0x3ffff
v_accvgpr_read_b32 v20,a116
v_accvgpr_read_b32 v21,a117
v_mfma_f32_16x16x32_bf16 v[116:119],a[156:159],v[248:251],v[44:47]
s_or_b32 s74,s74,s69
v_accvgpr_read_b32 v22,a118
v_accvgpr_read_b32 v23,a119
v_mfma_f32_16x16x32_bf16 v[116:119],a[152:155],v[240:243],v[116:119]
s_sub_u32 s76,s76,32768
v_mul_f32 v16,v16,v27
v_mul_f32 v17,v17,v27
v_mfma_f32_16x16x32_bf16 v[116:119],a[148:151],v[232:235],v[116:119]
s_and_b32 s76,s76,0x1ffff
v_mul_f32 v18,v18,v27
v_mul_f32 v19,v19,v27
v_mfma_f32_16x16x32_bf16 v[116:119],a[144:147],v[224:227],v[116:119]
s_and_b32 s69,s75,0xfffc0000
v_mul_f32 v20,v20,v27
v_mul_f32 v21,v21,v27
v_mfma_f32_16x16x32_bf16 v[120:123],a[160:163],v[224:227],v[44:47]
s_add_u32 s75,s75,16384
v_mul_f32 v22,v22,v27
v_mul_f32 v23,v23,v27
v_mfma_f32_16x16x32_bf16 v[120:123],a[164:167],v[232:235],v[120:123]
v_cvt_pk_bf16_f32 v16,v16,v17
v_cvt_pk_bf16_f32 v17,v18,v19
v_mfma_f32_16x16x32_bf16 v[120:123],a[168:171],v[240:243],v[120:123]
v_cvt_pk_bf16_f32 v18,v20,v21
v_cvt_pk_bf16_f32 v19,v22,v23
v_mfma_f32_16x16x32_bf16 v[120:123],a[172:175],v[248:251],v[120:123]
v_accvgpr_write_b32 a112,v16
v_accvgpr_write_b32 a113,v17
v_mfma_f32_16x16x32_bf16 v[124:127],a[188:191],v[248:251],v[44:47]
v_accvgpr_write_b32 a114,v18
v_accvgpr_write_b32 a115,v19
v_accvgpr_write_b32 a116,v8
v_accvgpr_write_b32 a117,v9
v_accvgpr_write_b32 a118,v10
v_accvgpr_write_b32 a119,v11
v_exp_f32 v80,v80
v_exp_f32 v81,v81
v_exp_f32 v82,v82
v_exp_f32 v83,v83
v_exp_f32 v84,v84
v_exp_f32 v85,v85
v_exp_f32 v86,v86
v_exp_f32 v87,v87
v_mfma_f32_16x16x32_bf16 v[124:127],a[184:187],v[240:243],v[124:127]
v_cvt_pk_bf16_f32 v80,v80,v81
v_cvt_pk_bf16_f32 v81,v82,v83
v_cvt_pk_bf16_f32 v82,v84,v85
v_cvt_pk_bf16_f32 v83,v86,v87
v_exp_f32 v88,v88
v_exp_f32 v89,v89
v_exp_f32 v90,v90
v_exp_f32 v91,v91
v_exp_f32 v92,v92
v_exp_f32 v93,v93
v_exp_f32 v94,v94
v_exp_f32 v95,v95
v_mfma_f32_16x16x32_bf16 v[124:127],a[180:183],v[232:235],v[124:127]
v_cvt_pk_bf16_f32 v88,v88,v89
v_cvt_pk_bf16_f32 v89,v90,v91
v_cvt_pk_bf16_f32 v90,v92,v93
v_cvt_pk_bf16_f32 v91,v94,v95
v_mfma_f32_16x16x32_bf16 v[124:127],a[176:179],v[224:227],v[124:127]
s_add_u32 s10,s20,s74
s_addc_u32 s11,s21,0
s_add_u32 m0,s76,0
v_mfma_f32_16x16x32_bf16 v[132:135],a[192:195],v[80:83],v[132:135]
global_load_lds_dwordx4 v4,s[10:11] nt
v_add_u32 v1,s78,v1
v_mfma_f32_16x16x32_bf16 v[132:135],a[196:199],v[88:91],v[132:135]
ds_read_b128 a[128:131],v1
v_accvgpr_read_b32 v16,a120
v_mfma_f32_16x16x32_bf16 v[140:143],a[200:203],v[80:83],v[140:143]
v_exp_f32 v96,v96
v_mfma_f32_16x16x32_bf16 v[140:143],a[204:207],v[88:91],v[140:143]
ds_read_b128 a[132:135],v1 offset:256
v_accvgpr_read_b32 v17,a121
v_mfma_f32_16x16x32_bf16 v[148:151],a[208:211],v[80:83],v[148:151]
v_exp_f32 v97,v97
v_mfma_f32_16x16x32_bf16 v[148:151],a[212:215],v[88:91],v[148:151]
ds_read_b128 a[136:139],v1 offset:512
v_cvt_pk_bf16_f32 v96,v96,v97
v_mfma_f32_16x16x32_bf16 v[156:159],a[216:219],v[80:83],v[156:159]
v_exp_f32 v98,v98
v_mfma_f32_16x16x32_bf16 v[156:159],a[220:223],v[88:91],v[156:159]
ds_read_b128 a[140:143],v1 offset:768
v_accvgpr_read_b32 v18,a122
v_mfma_f32_16x16x32_bf16 v[164:167],a[224:227],v[80:83],v[164:167]
v_exp_f32 v99,v99
v_mfma_f32_16x16x32_bf16 v[164:167],a[228:231],v[88:91],v[164:167]
ds_read_b128 a[156:159],v1 offset:4864
v_cvt_pk_bf16_f32 v97,v98,v99
v_mfma_f32_16x16x32_bf16 v[172:175],a[232:235],v[80:83],v[172:175]
v_exp_f32 v100,v100
v_mfma_f32_16x16x32_bf16 v[172:175],a[236:239],v[88:91],v[172:175]
ds_read_b128 a[152:155],v1 offset:4608
v_accvgpr_read_b32 v19,a123
v_mfma_f32_16x16x32_bf16 v[180:183],a[240:243],v[80:83],v[180:183]
v_exp_f32 v101,v101
v_mfma_f32_16x16x32_bf16 v[180:183],a[244:247],v[88:91],v[180:183]
ds_read_b128 a[148:151],v1 offset:4352
v_cvt_pk_bf16_f32 v98,v100,v101
v_mfma_f32_16x16x32_bf16 v[188:191],a[248:251],v[80:83],v[188:191]
global_load_lds_dwordx4 v4,s[10:11] offset:1024 nt
v_accvgpr_read_b32 v20,a124
v_mfma_f32_16x16x32_bf16 v[188:191],a[252:255],v[88:91],v[188:191]
ds_read_b128 a[144:147],v1 offset:4096
v_accvgpr_read_b32 v21,a125
v_mfma_f32_16x16x32_bf16 v[52:55],v[28:31],v[80:83],v[52:55]
v_exp_f32 v102,v102
v_mfma_f32_16x16x32_bf16 v[52:55],v[28:31],v[88:91],v[52:55]
s_waitcnt lgkmcnt(3)
ds_read_b128 a[160:163],v1 offset:8192
v_accvgpr_read_b32 v22,a126
v_mfma_f32_16x16x32_bf16 v[64:67],a[128:131],v[128:131],v[32:35]
v_exp_f32 v103,v103
v_mfma_f32_16x16x32_bf16 v[64:67],a[132:135],v[136:139],v[64:67]
ds_read_b128 a[164:167],v1 offset:8448
v_cvt_pk_bf16_f32 v99,v102,v103
v_mfma_f32_16x16x32_bf16 v[64:67],a[136:139],v[144:147],v[64:67]
v_exp_f32 v104,v104
v_mfma_f32_16x16x32_bf16 v[64:67],a[140:143],v[152:155],v[64:67]
ds_read_b128 a[168:171],v1 offset:8704
v_accvgpr_read_b32 v23,a127
v_mfma_f32_16x16x32_bf16 v[68:71],a[156:159],v[152:155],v[32:35]
s_waitcnt lgkmcnt(4)
v_exp_f32 v105,v105
v_mfma_f32_16x16x32_bf16 v[68:71],a[152:155],v[144:147],v[68:71]
ds_read_b128 a[172:175],v1 offset:8960
v_cvt_pk_bf16_f32 v104,v104,v105
v_mfma_f32_16x16x32_bf16 v[68:71],a[148:151],v[136:139],v[68:71]
s_waitcnt lgkmcnt(4)
v_exp_f32 v106,v106
v_mfma_f32_16x16x32_bf16 v[68:71],a[144:147],v[128:131],v[68:71]
s_waitcnt lgkmcnt(3)
ds_read_b128 a[188:191],v1 offset:13056
v_mul_f32 v16,v16,v27
v_mfma_f32_16x16x32_bf16 v[72:75],a[160:163],v[128:131],v[32:35]
s_waitcnt lgkmcnt(3)
v_exp_f32 v107,v107
v_mfma_f32_16x16x32_bf16 v[72:75],a[164:167],v[136:139],v[72:75]
s_waitcnt lgkmcnt(1)
ds_read_b128 a[184:187],v1 offset:12800
v_cvt_pk_bf16_f32 v105,v106,v107
v_mfma_f32_16x16x32_bf16 v[72:75],a[168:171],v[144:147],v[72:75]
global_load_lds_dwordx4 v5,s[10:11] offset:2048 nt
ds_read_b128 a[180:183],v1 offset:12544
v_mfma_f32_16x16x32_bf16 v[72:75],a[172:175],v[152:155],v[72:75]
s_waitcnt lgkmcnt(2)
ds_read_b128 a[176:179],v1 offset:12288
v_mul_f32 v17,v17,v27
v_mfma_f32_16x16x32_bf16 v[76:79],a[188:191],v[152:155],v[32:35]
s_waitcnt lgkmcnt(2)
v_exp_f32 v108,v108
v_mfma_f32_16x16x32_bf16 v[76:79],a[184:187],v[144:147],v[76:79]
s_waitcnt lgkmcnt(1)
v_exp_f32 v109,v109
v_exp_f32 v110,v110
v_exp_f32 v111,v111
v_mfma_f32_16x16x32_bf16 v[76:79],a[180:183],v[136:139],v[76:79]
s_waitcnt lgkmcnt(0)
v_cvt_pk_bf16_f32 v106,v108,v109
v_cvt_pk_bf16_f32 v107,v110,v111
v_mfma_f32_16x16x32_bf16 v[76:79],a[176:179],v[128:131],v[76:79]
v_mul_f32 v18,v18,v27
v_mul_f32 v19,v19,v27
v_mfma_f32_16x16x32_bf16 v[196:199],a[192:195],v[96:99],v[196:199]
v_mul_f32 v20,v20,v27
v_mul_f32 v21,v21,v27
v_mfma_f32_16x16x32_bf16 v[196:199],a[196:199],v[104:107],v[196:199]
v_mul_f32 v22,v22,v27
v_mul_f32 v23,v23,v27
v_mfma_f32_16x16x32_bf16 v[204:207],a[200:203],v[96:99],v[204:207]
v_cvt_pk_bf16_f32 v16,v16,v17
v_cvt_pk_bf16_f32 v17,v18,v19
v_mfma_f32_16x16x32_bf16 v[204:207],a[204:207],v[104:107],v[204:207]
v_cvt_pk_bf16_f32 v18,v20,v21
v_cvt_pk_bf16_f32 v19,v22,v23
v_mfma_f32_16x16x32_bf16 v[212:215],a[208:211],v[96:99],v[212:215]
v_accvgpr_write_b32 a120,v16
v_accvgpr_write_b32 a121,v17
v_mfma_f32_16x16x32_bf16 v[212:215],a[212:215],v[104:107],v[212:215]
v_accvgpr_write_b32 a122,v18
v_accvgpr_write_b32 a123,v19
v_mfma_f32_16x16x32_bf16 v[220:223],a[216:219],v[96:99],v[220:223]
v_accvgpr_write_b32 a124,v12
v_accvgpr_write_b32 a125,v13
v_mfma_f32_16x16x32_bf16 v[220:223],a[220:223],v[104:107],v[220:223]
v_accvgpr_write_b32 a126,v14
v_accvgpr_write_b32 a127,v15
v_mfma_f32_16x16x32_bf16 v[228:231],a[224:227],v[96:99],v[228:231]
global_load_lds_dwordx4 v5,s[10:11] offset:3072
v_add_u32 v2,s78,v2
v_mfma_f32_16x16x32_bf16 v[228:231],a[228:231],v[104:107],v[228:231]
v_exp_f32 v112,v112
v_mfma_f32_16x16x32_bf16 v[236:239],a[232:235],v[96:99],v[236:239]
v_exp_f32 v113,v113
v_mfma_f32_16x16x32_bf16 v[236:239],a[236:239],v[104:107],v[236:239]
v_exp_f32 v114,v114
v_mfma_f32_16x16x32_bf16 v[244:247],a[240:243],v[96:99],v[244:247]
v_exp_f32 v115,v115
v_mfma_f32_16x16x32_bf16 v[244:247],a[244:247],v[104:107],v[244:247]
v_exp_f32 v116,v116
v_mfma_f32_16x16x32_bf16 v[252:255],a[248:251],v[96:99],v[252:255]
v_exp_f32 v117,v117
v_mfma_f32_16x16x32_bf16 v[252:255],a[252:255],v[104:107],v[252:255]
v_exp_f32 v118,v118
v_mfma_f32_16x16x32_bf16 v[56:59],v[28:31],v[96:99],v[56:59]
v_exp_f32 v119,v119
v_mfma_f32_16x16x32_bf16 v[56:59],v[28:31],v[104:107],v[56:59]
v_cvt_pk_bf16_f32 v112,v112,v113
v_cvt_pk_bf16_f32 v113,v114,v115
v_mfma_f32_16x16x32_bf16 v[80:83],a[128:131],v[160:163],v[36:39]
v_cvt_pk_bf16_f32 v114,v116,v117
v_cvt_pk_bf16_f32 v115,v118,v119
v_mfma_f32_16x16x32_bf16 v[80:83],a[132:135],v[168:171],v[80:83]
s_add_u32 s10,s24,s74
s_addc_u32 s11,s25,0
v_add_u32 v3,s78,v3
v_mfma_f32_16x16x32_bf16 v[80:83],a[136:139],v[176:179],v[80:83]
s_add_u32 s74,s74,16384
v_exp_f32 v120,v120
v_mfma_f32_16x16x32_bf16 v[80:83],a[140:143],v[184:187],v[80:83]
s_add_u32 m0,s76,16384
v_exp_f32 v121,v121
v_mfma_f32_16x16x32_bf16 v[84:87],a[156:159],v[184:187],v[36:39]
s_and_b32 s74,s74,0x3ffff
v_exp_f32 v122,v122
v_mfma_f32_16x16x32_bf16 v[84:87],a[152:155],v[176:179],v[84:87]
global_load_lds_dwordx4 v4,s[10:11]
v_cvt_pk_bf16_f32 v120,v120,v121
v_mfma_f32_16x16x32_bf16 v[84:87],a[148:151],v[168:171],v[84:87]
s_or_b32 s74,s74,s69
v_exp_f32 v123,v123
v_mfma_f32_16x16x32_bf16 v[84:87],a[144:147],v[160:163],v[84:87]
s_sub_u32 s76,s76,32768
v_exp_f32 v124,v124
v_mfma_f32_16x16x32_bf16 v[88:91],a[160:163],v[160:163],v[36:39]
s_and_b32 s76,s76,0x1ffff
v_exp_f32 v125,v125
v_mfma_f32_16x16x32_bf16 v[88:91],a[164:167],v[168:171],v[88:91]
s_sub_u32 s78,s77,32768
v_exp_f32 v126,v126
v_mfma_f32_16x16x32_bf16 v[88:91],a[168:171],v[176:179],v[88:91]
s_and_b32 s78,s78,0x1ffff
v_exp_f32 v127,v127
v_mfma_f32_16x16x32_bf16 v[88:91],a[172:175],v[184:187],v[88:91]
s_sub_u32 s78,s78,s77
v_cvt_pk_bf16_f32 v121,v122,v123
v_cvt_pk_bf16_f32 v122,v124,v125
v_mfma_f32_16x16x32_bf16 v[92:95],a[188:191],v[184:187],v[36:39]
s_and_b32 s69,s75,0xfffc0000
v_cvt_pk_bf16_f32 v123,v126,v127
v_accvgpr_read_b32 v12,a100
v_mfma_f32_16x16x32_bf16 v[92:95],a[184:187],v[176:179],v[92:95]
s_add_u32 s77,s77,s78
v_exp_f32 v64,v64
v_mfma_f32_16x16x32_bf16 v[92:95],a[180:183],v[168:171],v[92:95]
s_add_u32 s75,s75,16384
v_exp_f32 v65,v65
v_mfma_f32_16x16x32_bf16 v[92:95],a[176:179],v[160:163],v[92:95]
v_exp_f32 v66,v66
v_mfma_f32_16x16x32_bf16 a[32:35],a[192:195],v[112:115],a[32:35]
ds_read_b64_tr_b16 a[192:193],v2
ds_read_b64_tr_b16 a[194:195],v2 offset:4096
v_mfma_f32_16x16x32_bf16 a[32:35],a[196:199],v[120:123],a[32:35]
ds_read_b64_tr_b16 a[196:197],v2 offset:8192
v_cvt_pk_bf16_f32 v64,v64,v65
v_mfma_f32_16x16x32_bf16 a[36:39],a[200:203],v[112:115],a[36:39]
ds_read_b64_tr_b16 a[198:199],v2 offset:12288
v_accvgpr_read_b32 v13,a101
v_mfma_f32_16x16x32_bf16 a[36:39],a[204:207],v[120:123],a[36:39]
global_load_lds_dwordx4 v5,s[10:11] offset:1024
ds_read_b64_tr_b16 a[200:201],v3
v_mfma_f32_16x16x32_bf16 a[40:43],a[208:211],v[112:115],a[40:43]
ds_read_b64_tr_b16 a[202:203],v3 offset:4096
ds_read_b64_tr_b16 a[204:205],v3 offset:8192
v_mfma_f32_16x16x32_bf16 a[40:43],a[212:215],v[120:123],a[40:43]
ds_read_b64_tr_b16 a[206:207],v3 offset:12288
v_accvgpr_read_b32 v14,a102
)A"
R"A(v_mfma_f32_16x16x32_bf16 a[44:47],a[216:219],v[112:115],a[44:47]
ds_read_b64_tr_b16 a[208:209],v2 offset:256
v_accvgpr_read_b32 v15,a103
v_mfma_f32_16x16x32_bf16 a[44:47],a[220:223],v[120:123],a[44:47]
ds_read_b64_tr_b16 a[210:211],v2 offset:4352
v_accvgpr_read_b32 v8,a96
v_mfma_f32_16x16x32_bf16 a[48:51],a[224:227],v[112:115],a[48:51]
ds_read_b64_tr_b16 a[212:213],v2 offset:8448
ds_read_b64_tr_b16 a[214:215],v2 offset:12544
v_mfma_f32_16x16x32_bf16 a[48:51],a[228:231],v[120:123],a[48:51]
v_exp_f32 v67,v67
v_mfma_f32_16x16x32_bf16 a[52:55],a[232:235],v[112:115],a[52:55]
v_exp_f32 v68,v68
v_mfma_f32_16x16x32_bf16 a[52:55],a[236:239],v[120:123],a[52:55]
v_exp_f32 v69,v69
v_mfma_f32_16x16x32_bf16 a[56:59],a[240:243],v[112:115],a[56:59]
v_exp_f32 v70,v70
v_mfma_f32_16x16x32_bf16 a[56:59],a[244:247],v[120:123],a[56:59]
v_exp_f32 v71,v71
v_mfma_f32_16x16x32_bf16 a[60:63],a[248:251],v[112:115],a[60:63]
ds_read_b64_tr_b16 a[216:217],v3 offset:256
ds_read_b64_tr_b16 a[218:219],v3 offset:4352
v_mfma_f32_16x16x32_bf16 a[60:63],a[252:255],v[120:123],a[60:63]
v_cvt_pk_bf16_f32 v65,v66,v67
v_cvt_pk_bf16_f32 v66,v68,v69
v_mfma_f32_16x16x32_bf16 v[60:63],v[28:31],v[112:115],v[60:63]
ds_read_b64_tr_b16 a[220:221],v3 offset:8448
v_cvt_pk_bf16_f32 v67,v70,v71
v_mfma_f32_16x16x32_bf16 v[60:63],v[28:31],v[120:123],v[60:63]
global_load_lds_dwordx4 v4,s[10:11] offset:2048
ds_read_b64_tr_b16 a[222:223],v3 offset:12544
v_mfma_f32_16x16x32_bf16 v[96:99],a[128:131],v[192:195],v[40:43]
v_exp_f32 v72,v72
v_mfma_f32_16x16x32_bf16 v[96:99],a[132:135],v[200:203],v[96:99]
v_exp_f32 v73,v73
v_mfma_f32_16x16x32_bf16 v[96:99],a[136:139],v[208:211],v[96:99]
v_exp_f32 v74,v74
v_mfma_f32_16x16x32_bf16 v[96:99],a[140:143],v[216:219],v[96:99]
v_exp_f32 v75,v75
v_mfma_f32_16x16x32_bf16 v[100:103],a[156:159],v[216:219],v[40:43]
v_exp_f32 v76,v76
v_mfma_f32_16x16x32_bf16 v[100:103],a[152:155],v[208:211],v[100:103]
v_exp_f32 v77,v77
v_mfma_f32_16x16x32_bf16 v[100:103],a[148:151],v[200:203],v[100:103]
v_exp_f32 v78,v78
v_mfma_f32_16x16x32_bf16 v[100:103],a[144:147],v[192:195],v[100:103]
v_exp_f32 v79,v79
v_mfma_f32_16x16x32_bf16 v[104:107],a[160:163],v[192:195],v[40:43]
v_cvt_pk_bf16_f32 v72,v72,v73
v_cvt_pk_bf16_f32 v73,v74,v75
v_mfma_f32_16x16x32_bf16 v[104:107],a[164:167],v[200:203],v[104:107]
v_cvt_pk_bf16_f32 v74,v76,v77
v_cvt_pk_bf16_f32 v75,v78,v79
v_mfma_f32_16x16x32_bf16 v[104:107],a[168:171],v[208:211],v[104:107]
ds_read_b64_tr_b16 a[224:225],v2 offset:512
ds_read_b64_tr_b16 a[226:227],v2 offset:4608
v_mfma_f32_16x16x32_bf16 v[104:107],a[172:175],v[216:219],v[104:107]
v_accvgpr_read_b32 v9,a97
v_accvgpr_read_b32 v10,a98
v_mfma_f32_16x16x32_bf16 v[108:111],a[188:191],v[216:219],v[40:43]
v_accvgpr_read_b32 v11,a99
v_mul_f32 v8,v8,v27
v_mfma_f32_16x16x32_bf16 v[108:111],a[184:187],v[208:211],v[108:111]
global_load_lds_dwordx4 v5,s[10:11] offset:3072
v_mul_f32 v9,v9,v27
v_mfma_f32_16x16x32_bf16 v[108:111],a[180:183],v[200:203],v[108:111]
v_mul_f32 v10,v10,v27
v_mul_f32 v11,v11,v27
v_mfma_f32_16x16x32_bf16 v[108:111],a[176:179],v[192:195],v[108:111]
s_waitcnt lgkmcnt(2)
v_mul_f32 v12,v12,v27
v_mul_f32 v13,v13,v27
v_mul_f32 v14,v14,v27
v_mul_f32 v15,v15,v27
v_cvt_pk_bf16_f32 v8,v8,v9
v_cvt_pk_bf16_f32 v9,v10,v11
v_cvt_pk_bf16_f32 v10,v12,v13
v_cvt_pk_bf16_f32 v11,v14,v15
v_accvgpr_write_b32 a96,v8
v_accvgpr_write_b32 a97,v9
v_accvgpr_write_b32 a98,v10
v_accvgpr_write_b32 a99,v11
v_mfma_f32_16x16x32_bf16 a[0:3],a[192:195],v[64:67],a[0:3]
ds_read_b128 a[100:103],v7 offset:2048
v_add_u32 v1,s78,v1
v_mfma_f32_16x16x32_bf16 a[0:3],a[196:199],v[72:75],a[0:3]
ds_read_b64_tr_b16 a[228:229],v2 offset:8704
v_accvgpr_read_b32 v20,a108
v_mfma_f32_16x16x32_bf16 a[4:7],a[200:203],v[64:67],a[4:7]
ds_read_b64_tr_b16 a[230:231],v2 offset:12800
v_accvgpr_read_b32 v21,a109
v_mfma_f32_16x16x32_bf16 a[4:7],a[204:207],v[72:75],a[4:7]
ds_read_b64_tr_b16 a[232:233],v3 offset:512
v_accvgpr_read_b32 v22,a110
v_mfma_f32_16x16x32_bf16 a[8:11],a[208:211],v[64:67],a[8:11]
ds_read_b64_tr_b16 a[234:235],v3 offset:4608
ds_read_b64_tr_b16 a[236:237],v3 offset:8704
v_mfma_f32_16x16x32_bf16 a[8:11],a[212:215],v[72:75],a[8:11]
ds_read_b64_tr_b16 a[238:239],v3 offset:12800
v_accvgpr_read_b32 v23,a111
v_mfma_f32_16x16x32_bf16 a[12:15],a[216:219],v[64:67],a[12:15]
ds_read_b64_tr_b16 a[240:241],v2 offset:768
v_accvgpr_read_b32 v16,a104
v_mfma_f32_16x16x32_bf16 a[12:15],a[220:223],v[72:75],a[12:15]
s_waitcnt lgkmcnt(8)
ds_read_b64_tr_b16 a[242:243],v2 offset:4864
v_accvgpr_read_b32 v17,a105
v_mfma_f32_16x16x32_bf16 a[16:19],a[224:227],v[64:67],a[16:19]
s_waitcnt lgkmcnt(6)
ds_read_b64_tr_b16 a[244:245],v2 offset:8960
ds_read_b64_tr_b16 a[246:247],v2 offset:13056
v_mfma_f32_16x16x32_bf16 a[16:19],a[228:231],v[72:75],a[16:19]
s_waitcnt lgkmcnt(0)
ds_read_b64_tr_b16 a[248:249],v3 offset:768
v_accvgpr_read_b32 v18,a106
v_mfma_f32_16x16x32_bf16 a[20:23],a[232:235],v[64:67],a[20:23]
s_getreg_b32 s92,hwreg(HW_REG_IB_STS,0,4)
s_min_u32 s4,s92,s6
ds_read_b64_tr_b16 a[250:251],v3 offset:4864
v_mfma_f32_16x16x32_bf16 a[20:23],a[236:239],v[72:75],a[20:23]
s_mov_b32 s6,s92
ds_read_b64_tr_b16 a[252:253],v3 offset:8960
ds_read_b64_tr_b16 a[254:255],v3 offset:13056
v_mfma_f32_16x16x32_bf16 a[24:27],a[240:243],v[64:67],a[24:27]
s_cmp_gt_u32 s4,s9
s_cselect_b32 s4,15,s5
v_accvgpr_read_b32 v19,a107
v_mfma_f32_16x16x32_bf16 a[24:27],a[244:247],v[72:75],a[24:27]
s_waitcnt lgkmcnt(2)
v_exp_f32 v80,v80
v_mfma_f32_16x16x32_bf16 a[28:31],a[248:251],v[64:67],a[28:31]
s_waitcnt lgkmcnt(0)
v_exp_f32 v81,v81
v_mfma_f32_16x16x32_bf16 a[28:31],a[252:255],v[72:75],a[28:31]
s_getpc_b64 s[92:93]
s_cmp_lg_u32 s4,0
s_cbranch_scc1 L_pace_%=
v_mfma_f32_16x16x32_bf16 v[48:51],v[28:31],v[64:67],v[48:51]
v_exp_f32 v82,v82
v_mfma_f32_16x16x32_bf16 v[48:51],v[28:31],v[72:75],v[48:51]
s_add_u32 s10,s20,s74
s_addc_u32 s11,s21,0
v_cvt_pk_bf16_f32 v80,v80,v81
s_waitcnt vmcnt(16)
s_barrier
v_mfma_f32_16x16x32_bf16 v[112:115],a[128:131],v[224:227],v[44:47]
s_add_u32 m0,s76,0
v_exp_f32 v83,v83
v_mfma_f32_16x16x32_bf16 v[112:115],a[132:135],v[232:235],v[112:115]
v_exp_f32 v84,v84
v_mfma_f32_16x16x32_bf16 v[112:115],a[136:139],v[240:243],v[112:115]
global_load_lds_dwordx4 v4,s[10:11] nt
v_cvt_pk_bf16_f32 v81,v82,v83
v_mfma_f32_16x16x32_bf16 v[112:115],a[140:143],v[248:251],v[112:115]
v_exp_f32 v85,v85
v_mfma_f32_16x16x32_bf16 v[116:119],a[156:159],v[248:251],v[44:47]
v_exp_f32 v86,v86
v_mfma_f32_16x16x32_bf16 v[116:119],a[152:155],v[240:243],v[116:119]
v_exp_f32 v87,v87
v_mfma_f32_16x16x32_bf16 v[116:119],a[148:151],v[232:235],v[116:119]
v_cvt_pk_bf16_f32 v82,v84,v85
v_cvt_pk_bf16_f32 v83,v86,v87
v_mfma_f32_16x16x32_bf16 v[116:119],a[144:147],v[224:227],v[116:119]
v_exp_f32 v88,v88
v_mfma_f32_16x16x32_bf16 v[120:123],a[160:163],v[224:227],v[44:47]
v_exp_f32 v89,v89
v_mfma_f32_16x16x32_bf16 v[120:123],a[164:167],v[232:235],v[120:123]
v_exp_f32 v90,v90
v_mfma_f32_16x16x32_bf16 v[120:123],a[168:171],v[240:243],v[120:123]
v_exp_f32 v91,v91
v_mfma_f32_16x16x32_bf16 v[120:123],a[172:175],v[248:251],v[120:123]
v_exp_f32 v92,v92
v_mfma_f32_16x16x32_bf16 v[124:127],a[188:191],v[248:251],v[44:47]
v_exp_f32 v93,v93
v_mfma_f32_16x16x32_bf16 v[124:127],a[184:187],v[240:243],v[124:127]
v_exp_f32 v94,v94
v_exp_f32 v95,v95
v_mfma_f32_16x16x32_bf16 v[124:127],a[180:183],v[232:235],v[124:127]
v_cvt_pk_bf16_f32 v88,v88,v89
v_cvt_pk_bf16_f32 v89,v90,v91
v_cvt_pk_bf16_f32 v90,v92,v93
v_cvt_pk_bf16_f32 v91,v94,v95
v_mfma_f32_16x16x32_bf16 v[124:127],a[176:179],v[224:227],v[124:127]
ds_read_b128 a[128:131],v1
v_mul_f32 v16,v16,v27
v_mfma_f32_16x16x32_bf16 v[132:135],a[192:195],v[80:83],v[132:135]
v_mul_f32 v17,v17,v27
v_mul_f32 v18,v18,v27
v_mfma_f32_16x16x32_bf16 v[132:135],a[196:199],v[88:91],v[132:135]
ds_read_b128 a[132:135],v1 offset:256
v_mul_f32 v19,v19,v27
v_mfma_f32_16x16x32_bf16 v[140:143],a[200:203],v[80:83],v[140:143]
global_load_lds_dwordx4 v4,s[10:11] offset:1024 nt
v_mul_f32 v20,v20,v27
v_mfma_f32_16x16x32_bf16 v[140:143],a[204:207],v[88:91],v[140:143]
ds_read_b128 a[136:139],v1 offset:512
v_mul_f32 v21,v21,v27
v_mfma_f32_16x16x32_bf16 v[148:151],a[208:211],v[80:83],v[148:151]
v_mul_f32 v22,v22,v27
v_mul_f32 v23,v23,v27
v_mfma_f32_16x16x32_bf16 v[148:151],a[212:215],v[88:91],v[148:151]
ds_read_b128 a[140:143],v1 offset:768
v_cvt_pk_bf16_f32 v16,v16,v17
v_mfma_f32_16x16x32_bf16 v[156:159],a[216:219],v[80:83],v[156:159]
v_cvt_pk_bf16_f32 v17,v18,v19
v_cvt_pk_bf16_f32 v18,v20,v21
L_pd9_%=:
s_cbranch_execz L_pd13_%=
v_mfma_f32_16x16x32_bf16 v[156:159],a[220:223],v[88:91],v[156:159]
ds_read_b128 a[156:159],v1 offset:4864
v_cvt_pk_bf16_f32 v19,v22,v23
v_mfma_f32_16x16x32_bf16 v[164:167],a[224:227],v[80:83],v[164:167]
v_accvgpr_write_b32 a104,v16
v_accvgpr_write_b32 a105,v17
v_mfma_f32_16x16x32_bf16 v[164:167],a[228:231],v[88:91],v[164:167]
ds_read_b128 a[152:155],v1 offset:4608
v_accvgpr_write_b32 a106,v18
v_mfma_f32_16x16x32_bf16 v[172:175],a[232:235],v[80:83],v[172:175]
v_accvgpr_write_b32 a107,v19
v_accvgpr_read_b32 v12,a68
v_mfma_f32_16x16x32_bf16 v[172:175],a[236:239],v[88:91],v[172:175]
ds_read_b128 a[148:151],v1 offset:4352
v_accvgpr_read_b32 v13,a69
v_mfma_f32_16x16x32_bf16 v[180:183],a[240:243],v[80:83],v[180:183]
v_exp_f32 v96,v96
v_mfma_f32_16x16x32_bf16 v[180:183],a[244:247],v[88:91],v[180:183]
ds_read_b128 a[144:147],v1 offset:4096
v_accvgpr_read_b32 v14,a70
v_mfma_f32_16x16x32_bf16 v[188:191],a[248:251],v[80:83],v[188:191]
v_exp_f32 v97,v97
v_mfma_f32_16x16x32_bf16 v[188:191],a[252:255],v[88:91],v[188:191]
ds_read_b128 a[160:163],v1 offset:8192
v_cvt_pk_bf16_f32 v96,v96,v97
v_mfma_f32_16x16x32_bf16 v[52:55],v[28:31],v[80:83],v[52:55]
v_exp_f32 v98,v98
v_mfma_f32_16x16x32_bf16 v[52:55],v[28:31],v[88:91],v[52:55]
s_waitcnt lgkmcnt(3)
ds_read_b128 a[164:167],v1 offset:8448
v_accvgpr_read_b32 v15,a71
v_mfma_f32_16x16x32_bf16 v[64:67],a[128:131],v[128:131],v[32:35]
global_load_lds_dwordx4 v5,s[10:11] offset:2048 nt
v_accvgpr_read_b32 v8,a64
v_mfma_f32_16x16x32_bf16 v[64:67],a[132:135],v[136:139],v[64:67]
ds_read_b128 a[168:171],v1 offset:8704
v_accvgpr_read_b32 v9,a65
v_mfma_f32_16x16x32_bf16 v[64:67],a[136:139],v[144:147],v[64:67]
v_exp_f32 v99,v99
v_mfma_f32_16x16x32_bf16 v[64:67],a[140:143],v[152:155],v[64:67]
ds_read_b128 a[108:111],v7 offset:3072
v_cvt_pk_bf16_f32 v97,v98,v99
v_mfma_f32_16x16x32_bf16 v[68:71],a[156:159],v[152:155],v[32:35]
v_exp_f32 v100,v100
v_mfma_f32_16x16x32_bf16 v[68:71],a[152:155],v[144:147],v[68:71]
s_waitcnt lgkmcnt(3)
ds_read_b128 a[172:175],v1 offset:8960
v_accvgpr_read_b32 v10,a66
v_mfma_f32_16x16x32_bf16 v[68:71],a[148:151],v[136:139],v[68:71]
v_exp_f32 v101,v101
v_mfma_f32_16x16x32_bf16 v[68:71],a[144:147],v[128:131],v[68:71]
ds_read_b128 a[188:191],v1 offset:13056
v_cvt_pk_bf16_f32 v98,v100,v101
v_mfma_f32_16x16x32_bf16 v[72:75],a[160:163],v[128:131],v[32:35]
s_waitcnt lgkmcnt(4)
v_exp_f32 v102,v102
v_mfma_f32_16x16x32_bf16 v[72:75],a[164:167],v[136:139],v[72:75]
s_waitcnt lgkmcnt(3)
ds_read_b128 a[184:187],v1 offset:12800
v_accvgpr_read_b32 v11,a67
v_mfma_f32_16x16x32_bf16 v[72:75],a[168:171],v[144:147],v[72:75]
s_waitcnt lgkmcnt(2)
ds_read_b128 a[180:183],v1 offset:12544
v_mul_f32 v8,v8,v26
v_mfma_f32_16x16x32_bf16 v[72:75],a[172:175],v[152:155],v[72:75]
s_waitcnt lgkmcnt(2)
ds_read_b128 a[176:179],v1 offset:12288
v_mul_f32 v9,v9,v26
v_mfma_f32_16x16x32_bf16 v[76:79],a[188:191],v[152:155],v[32:35]
s_waitcnt lgkmcnt(2)
v_exp_f32 v103,v103
v_mfma_f32_16x16x32_bf16 v[76:79],a[184:187],v[144:147],v[76:79]
s_waitcnt lgkmcnt(1)
v_cvt_pk_bf16_f32 v99,v102,v103
v_exp_f32 v104,v104
v_exp_f32 v105,v105
v_exp_f32 v106,v106
v_exp_f32 v107,v107
v_exp_f32 v108,v108
v_exp_f32 v109,v109
v_exp_f32 v110,v110
v_exp_f32 v111,v111
v_mfma_f32_16x16x32_bf16 v[76:79],a[180:183],v[136:139],v[76:79]
s_waitcnt lgkmcnt(0)
v_cvt_pk_bf16_f32 v104,v104,v105
v_cvt_pk_bf16_f32 v105,v106,v107
v_cvt_pk_bf16_f32 v106,v108,v109
v_cvt_pk_bf16_f32 v107,v110,v111
v_mfma_f32_16x16x32_bf16 v[76:79],a[176:179],v[128:131],v[76:79]
v_exp_f32 v112,v112
v_mfma_f32_16x16x32_bf16 v[196:199],a[192:195],v[96:99],v[196:199]
)A"
R"A(global_load_lds_dwordx4 v5,s[10:11] offset:3072
v_mul_f32 v10,v10,v26
v_mfma_f32_16x16x32_bf16 v[196:199],a[196:199],v[104:107],v[196:199]
v_exp_f32 v113,v113
v_mfma_f32_16x16x32_bf16 v[204:207],a[200:203],v[96:99],v[204:207]
v_exp_f32 v114,v114
v_mfma_f32_16x16x32_bf16 v[204:207],a[204:207],v[104:107],v[204:207]
v_exp_f32 v115,v115
v_mfma_f32_16x16x32_bf16 v[212:215],a[208:211],v[96:99],v[212:215]
v_exp_f32 v116,v116
v_mfma_f32_16x16x32_bf16 v[212:215],a[212:215],v[104:107],v[212:215]
v_exp_f32 v117,v117
v_mfma_f32_16x16x32_bf16 v[220:223],a[216:219],v[96:99],v[220:223]
v_exp_f32 v118,v118
v_mfma_f32_16x16x32_bf16 v[220:223],a[220:223],v[104:107],v[220:223]
v_exp_f32 v119,v119
v_mfma_f32_16x16x32_bf16 v[228:231],a[224:227],v[96:99],v[228:231]
v_cvt_pk_bf16_f32 v112,v112,v113
v_cvt_pk_bf16_f32 v113,v114,v115
v_mfma_f32_16x16x32_bf16 v[228:231],a[228:231],v[104:107],v[228:231]
v_cvt_pk_bf16_f32 v114,v116,v117
v_cvt_pk_bf16_f32 v115,v118,v119
v_mfma_f32_16x16x32_bf16 v[236:239],a[232:235],v[96:99],v[236:239]
v_exp_f32 v120,v120
v_mfma_f32_16x16x32_bf16 v[236:239],a[236:239],v[104:107],v[236:239]
v_exp_f32 v121,v121
v_mfma_f32_16x16x32_bf16 v[244:247],a[240:243],v[96:99],v[244:247]
s_add_u32 s10,s24,s74
s_addc_u32 s11,s25,0
v_cvt_pk_bf16_f32 v120,v120,v121
v_mfma_f32_16x16x32_bf16 v[244:247],a[244:247],v[104:107],v[244:247]
s_add_u32 s74,s74,16384
v_exp_f32 v122,v122
v_mfma_f32_16x16x32_bf16 v[252:255],a[248:251],v[96:99],v[252:255]
s_add_u32 m0,s76,16384
v_exp_f32 v123,v123
v_mfma_f32_16x16x32_bf16 v[252:255],a[252:255],v[104:107],v[252:255]
s_and_b32 s74,s74,0x3ffff
v_exp_f32 v124,v124
v_mfma_f32_16x16x32_bf16 v[56:59],v[28:31],v[96:99],v[56:59]
global_load_lds_dwordx4 v4,s[10:11]
v_cvt_pk_bf16_f32 v121,v122,v123
v_mfma_f32_16x16x32_bf16 v[56:59],v[28:31],v[104:107],v[56:59]
s_or_b32 s74,s74,s69
v_exp_f32 v125,v125
v_mfma_f32_16x16x32_bf16 v[80:83],a[128:131],v[160:163],v[36:39]
s_sub_u32 s76,s76,32768
v_exp_f32 v126,v126
v_mfma_f32_16x16x32_bf16 v[80:83],a[132:135],v[168:171],v[80:83]
s_and_b32 s76,s76,0x1ffff
v_exp_f32 v127,v127
v_mfma_f32_16x16x32_bf16 v[80:83],a[136:139],v[176:179],v[80:83]
s_and_b32 s69,s75,0xfffc0000
v_cvt_pk_bf16_f32 v122,v124,v125
v_cvt_pk_bf16_f32 v123,v126,v127
v_mfma_f32_16x16x32_bf16 v[80:83],a[140:143],v[184:187],v[80:83]
v_mul_f32 v11,v11,v26
v_mul_f32 v12,v12,v26
v_mfma_f32_16x16x32_bf16 v[84:87],a[156:159],v[184:187],v[36:39]
v_mul_f32 v13,v13,v26
v_mul_f32 v14,v14,v26
v_mfma_f32_16x16x32_bf16 v[84:87],a[152:155],v[176:179],v[84:87]
v_mul_f32 v15,v15,v26
v_cvt_pk_bf16_f32 v8,v8,v9
v_mfma_f32_16x16x32_bf16 v[84:87],a[148:151],v[168:171],v[84:87]
v_cvt_pk_bf16_f32 v9,v10,v11
v_cvt_pk_bf16_f32 v10,v12,v13
v_mfma_f32_16x16x32_bf16 v[84:87],a[144:147],v[160:163],v[84:87]
v_cvt_pk_bf16_f32 v11,v14,v15
v_accvgpr_write_b32 a64,v8
v_mfma_f32_16x16x32_bf16 v[88:91],a[160:163],v[160:163],v[36:39]
v_accvgpr_write_b32 a65,v9
v_accvgpr_write_b32 a66,v10
v_mfma_f32_16x16x32_bf16 v[88:91],a[164:167],v[168:171],v[88:91]
ds_read_b128 a[68:71],v7 offset:6144
v_accvgpr_write_b32 a67,v11
v_mfma_f32_16x16x32_bf16 v[88:91],a[168:171],v[176:179],v[88:91]
v_add_u32 v2,s78,v2
v_add_u32 v3,s78,v3
v_mfma_f32_16x16x32_bf16 v[88:91],a[172:175],v[184:187],v[88:91]
s_sub_u32 s78,s77,32768
v_exp_f32 v64,v64
v_mfma_f32_16x16x32_bf16 v[92:95],a[188:191],v[184:187],v[36:39]
s_and_b32 s78,s78,0x1ffff
v_exp_f32 v65,v65
v_mfma_f32_16x16x32_bf16 v[92:95],a[184:187],v[176:179],v[92:95]
s_sub_u32 s78,s78,s77
v_exp_f32 v66,v66
v_mfma_f32_16x16x32_bf16 v[92:95],a[180:183],v[168:171],v[92:95]
global_load_lds_dwordx4 v5,s[10:11] offset:1024
v_cvt_pk_bf16_f32 v64,v64,v65
v_mfma_f32_16x16x32_bf16 v[92:95],a[176:179],v[160:163],v[92:95]
s_add_u32 s77,s77,s78
v_exp_f32 v67,v67
v_mfma_f32_16x16x32_bf16 a[32:35],a[192:195],v[112:115],a[32:35]
ds_read_b64_tr_b16 a[192:193],v2
ds_read_b64_tr_b16 a[194:195],v2 offset:4096
v_mfma_f32_16x16x32_bf16 a[32:35],a[196:199],v[120:123],a[32:35]
ds_read_b64_tr_b16 a[196:197],v2 offset:8192
v_cvt_pk_bf16_f32 v65,v66,v67
v_mfma_f32_16x16x32_bf16 a[36:39],a[200:203],v[112:115],a[36:39]
ds_read_b64_tr_b16 a[198:199],v2 offset:12288
v_accvgpr_read_b32 v20,a76
v_mfma_f32_16x16x32_bf16 a[36:39],a[204:207],v[120:123],a[36:39]
ds_read_b64_tr_b16 a[200:201],v3
v_accvgpr_read_b32 v21,a77
v_mfma_f32_16x16x32_bf16 a[40:43],a[208:211],v[112:115],a[40:43]
ds_read_b64_tr_b16 a[202:203],v3 offset:4096
ds_read_b64_tr_b16 a[204:205],v3 offset:8192
v_mfma_f32_16x16x32_bf16 a[40:43],a[212:215],v[120:123],a[40:43]
ds_read_b64_tr_b16 a[206:207],v3 offset:12288
v_accvgpr_read_b32 v22,a78
v_mfma_f32_16x16x32_bf16 a[44:47],a[216:219],v[112:115],a[44:47]
ds_read_b64_tr_b16 a[208:209],v2 offset:256
v_accvgpr_read_b32 v23,a79
v_mfma_f32_16x16x32_bf16 a[44:47],a[220:223],v[120:123],a[44:47]
ds_read_b64_tr_b16 a[210:211],v2 offset:4352
v_accvgpr_read_b32 v16,a72
v_mfma_f32_16x16x32_bf16 a[48:51],a[224:227],v[112:115],a[48:51]
ds_read_b64_tr_b16 a[212:213],v2 offset:8448
ds_read_b64_tr_b16 a[214:215],v2 offset:12544
v_mfma_f32_16x16x32_bf16 a[48:51],a[228:231],v[120:123],a[48:51]
v_exp_f32 v68,v68
v_mfma_f32_16x16x32_bf16 a[52:55],a[232:235],v[112:115],a[52:55]
v_exp_f32 v69,v69
v_mfma_f32_16x16x32_bf16 a[52:55],a[236:239],v[120:123],a[52:55]
v_exp_f32 v70,v70
v_mfma_f32_16x16x32_bf16 a[56:59],a[240:243],v[112:115],a[56:59]
v_exp_f32 v71,v71
v_mfma_f32_16x16x32_bf16 a[56:59],a[244:247],v[120:123],a[56:59]
ds_read_b64_tr_b16 a[216:217],v3 offset:256
ds_read_b64_tr_b16 a[218:219],v3 offset:4352
v_mfma_f32_16x16x32_bf16 a[60:63],a[248:251],v[112:115],a[60:63]
global_load_lds_dwordx4 v4,s[10:11] offset:2048
v_cvt_pk_bf16_f32 v66,v68,v69
v_mfma_f32_16x16x32_bf16 a[60:63],a[252:255],v[120:123],a[60:63]
ds_read_b64_tr_b16 a[220:221],v3 offset:8448
v_cvt_pk_bf16_f32 v67,v70,v71
v_mfma_f32_16x16x32_bf16 v[60:63],v[28:31],v[112:115],v[60:63]
v_exp_f32 v72,v72
v_mfma_f32_16x16x32_bf16 v[60:63],v[28:31],v[120:123],v[60:63]
v_exp_f32 v73,v73
v_mfma_f32_16x16x32_bf16 v[96:99],a[128:131],v[192:195],v[40:43]
v_exp_f32 v74,v74
v_mfma_f32_16x16x32_bf16 v[96:99],a[132:135],v[200:203],v[96:99]
v_exp_f32 v75,v75
v_mfma_f32_16x16x32_bf16 v[96:99],a[136:139],v[208:211],v[96:99]
v_exp_f32 v76,v76
v_mfma_f32_16x16x32_bf16 v[96:99],a[140:143],v[216:219],v[96:99]
v_exp_f32 v77,v77
v_mfma_f32_16x16x32_bf16 v[100:103],a[156:159],v[216:219],v[40:43]
v_exp_f32 v78,v78
v_mfma_f32_16x16x32_bf16 v[100:103],a[152:155],v[208:211],v[100:103]
v_exp_f32 v79,v79
v_mfma_f32_16x16x32_bf16 v[100:103],a[148:151],v[200:203],v[100:103]
ds_read_b64_tr_b16 a[222:223],v3 offset:12544
v_cvt_pk_bf16_f32 v72,v72,v73
v_mfma_f32_16x16x32_bf16 v[100:103],a[144:147],v[192:195],v[100:103]
v_cvt_pk_bf16_f32 v73,v74,v75
v_cvt_pk_bf16_f32 v74,v76,v77
v_mfma_f32_16x16x32_bf16 v[104:107],a[160:163],v[192:195],v[40:43]
ds_read_b64_tr_b16 a[224:225],v2 offset:512
v_cvt_pk_bf16_f32 v75,v78,v79
v_mfma_f32_16x16x32_bf16 v[104:107],a[164:167],v[200:203],v[104:107]
ds_read_b64_tr_b16 a[226:227],v2 offset:4608
ds_read_b64_tr_b16 a[228:229],v2 offset:8704
v_mfma_f32_16x16x32_bf16 v[104:107],a[168:171],v[208:211],v[104:107]
ds_read_b64_tr_b16 a[230:231],v2 offset:12800
v_accvgpr_read_b32 v17,a73
v_mfma_f32_16x16x32_bf16 v[104:107],a[172:175],v[216:219],v[104:107]
ds_read_b64_tr_b16 a[232:233],v3 offset:512
v_accvgpr_read_b32 v18,a74
v_mfma_f32_16x16x32_bf16 v[108:111],a[188:191],v[216:219],v[40:43]
global_load_lds_dwordx4 v5,s[10:11] offset:3072
ds_read_b64_tr_b16 a[234:235],v3 offset:4608
v_mfma_f32_16x16x32_bf16 v[108:111],a[184:187],v[208:211],v[108:111]
ds_read_b64_tr_b16 a[236:237],v3 offset:8704
ds_read_b64_tr_b16 a[238:239],v3 offset:12800
v_mfma_f32_16x16x32_bf16 v[108:111],a[180:183],v[200:203],v[108:111]
ds_read_b64_tr_b16 a[240:241],v2 offset:768
v_accvgpr_read_b32 v19,a75
v_mfma_f32_16x16x32_bf16 v[108:111],a[176:179],v[192:195],v[108:111]
s_waitcnt lgkmcnt(9)
ds_read_b64_tr_b16 a[242:243],v2 offset:4864
v_mul_f32 v16,v16,v26
v_mfma_f32_16x16x32_bf16 a[0:3],a[192:195],v[64:67],a[0:3]
ds_read_b64_tr_b16 a[244:245],v2 offset:8960
v_mul_f32 v17,v17,v26
v_mfma_f32_16x16x32_bf16 a[0:3],a[196:199],v[72:75],a[0:3]
ds_read_b64_tr_b16 a[246:247],v2 offset:13056
ds_read_b64_tr_b16 a[248:249],v3 offset:768
v_mfma_f32_16x16x32_bf16 a[4:7],a[200:203],v[64:67],a[4:7]
ds_read_b64_tr_b16 a[250:251],v3 offset:4864
v_mul_f32 v18,v18,v26
v_mfma_f32_16x16x32_bf16 a[4:7],a[204:207],v[72:75],a[4:7]
ds_read_b64_tr_b16 a[252:253],v3 offset:8960
v_mul_f32 v19,v19,v26
v_mfma_f32_16x16x32_bf16 a[8:11],a[208:211],v[64:67],a[8:11]
ds_read_b64_tr_b16 a[254:255],v3 offset:13056
v_mul_f32 v20,v20,v26
v_mfma_f32_16x16x32_bf16 a[8:11],a[212:215],v[72:75],a[8:11]
v_exp_f32 v80,v80
v_mfma_f32_16x16x32_bf16 a[12:15],a[216:219],v[64:67],a[12:15]
v_exp_f32 v81,v81
v_mfma_f32_16x16x32_bf16 a[12:15],a[220:223],v[72:75],a[12:15]
s_waitcnt lgkmcnt(6)
v_exp_f32 v82,v82
v_mfma_f32_16x16x32_bf16 a[16:19],a[224:227],v[64:67],a[16:19]
v_exp_f32 v83,v83
v_mfma_f32_16x16x32_bf16 a[16:19],a[228:231],v[72:75],a[16:19]
v_exp_f32 v84,v84
v_mfma_f32_16x16x32_bf16 a[20:23],a[232:235],v[64:67],a[20:23]
s_getreg_b32 s92,hwreg(HW_REG_IB_STS,0,4)
s_min_u32 s4,s92,s6
s_mov_b32 s6,s92
v_mfma_f32_16x16x32_bf16 a[20:23],a[236:239],v[72:75],a[20:23]
s_cmp_ge_u32 s4,s9
s_cselect_b32 s4,15,s5
v_cvt_pk_bf16_f32 v80,v80,v81
v_mfma_f32_16x16x32_bf16 a[24:27],a[240:243],v[64:67],a[24:27]
s_waitcnt lgkmcnt(0)
v_exp_f32 v85,v85
v_mfma_f32_16x16x32_bf16 a[24:27],a[244:247],v[72:75],a[24:27]
v_exp_f32 v86,v86
v_mfma_f32_16x16x32_bf16 a[28:31],a[248:251],v[64:67],a[28:31]
v_exp_f32 v87,v87
v_mfma_f32_16x16x32_bf16 a[28:31],a[252:255],v[72:75],a[28:31]
s_getpc_b64 s[92:93]
s_cmp_lg_u32 s4,0
s_cbranch_scc1 L_pace_%=
v_mfma_f32_16x16x32_bf16 v[48:51],v[28:31],v[64:67],v[48:51]
v_cvt_pk_bf16_f32 v81,v82,v83
v_cvt_pk_bf16_f32 v82,v84,v85
v_mfma_f32_16x16x32_bf16 v[48:51],v[28:31],v[72:75],v[48:51]
s_add_u32 s10,s20,s74
s_addc_u32 s11,s21,0
v_cvt_pk_bf16_f32 v83,v86,v87
s_waitcnt vmcnt(16)
s_barrier
v_mfma_f32_16x16x32_bf16 v[112:115],a[128:131],v[224:227],v[44:47]
s_add_u32 m0,s76,0
v_exp_f32 v88,v88
v_mfma_f32_16x16x32_bf16 v[112:115],a[132:135],v[232:235],v[112:115]
v_exp_f32 v89,v89
v_mfma_f32_16x16x32_bf16 v[112:115],a[136:139],v[240:243],v[112:115]
global_load_lds_dwordx4 v4,s[10:11] nt
v_cvt_pk_bf16_f32 v88,v88,v89
v_mfma_f32_16x16x32_bf16 v[112:115],a[140:143],v[248:251],v[112:115]
v_exp_f32 v90,v90
v_mfma_f32_16x16x32_bf16 v[116:119],a[156:159],v[248:251],v[44:47]
v_exp_f32 v91,v91
v_mfma_f32_16x16x32_bf16 v[116:119],a[152:155],v[240:243],v[116:119]
v_exp_f32 v92,v92
v_mfma_f32_16x16x32_bf16 v[116:119],a[148:151],v[232:235],v[116:119]
v_exp_f32 v93,v93
v_mfma_f32_16x16x32_bf16 v[116:119],a[144:147],v[224:227],v[116:119]
v_exp_f32 v94,v94
v_mfma_f32_16x16x32_bf16 v[120:123],a[160:163],v[224:227],v[44:47]
v_exp_f32 v95,v95
v_mfma_f32_16x16x32_bf16 v[120:123],a[164:167],v[232:235],v[120:123]
v_cvt_pk_bf16_f32 v89,v90,v91
v_cvt_pk_bf16_f32 v90,v92,v93
v_mfma_f32_16x16x32_bf16 v[120:123],a[168:171],v[240:243],v[120:123]
v_cvt_pk_bf16_f32 v91,v94,v95
v_mul_f32 v21,v21,v26
v_mfma_f32_16x16x32_bf16 v[120:123],a[172:175],v[248:251],v[120:123]
v_mul_f32 v22,v22,v26
v_mul_f32 v23,v23,v26
v_mfma_f32_16x16x32_bf16 v[124:127],a[188:191],v[248:251],v[44:47]
v_cvt_pk_bf16_f32 v16,v16,v17
v_cvt_pk_bf16_f32 v17,v18,v19
v_mfma_f32_16x16x32_bf16 v[124:127],a[184:187],v[240:243],v[124:127]
v_cvt_pk_bf16_f32 v18,v20,v21
v_cvt_pk_bf16_f32 v19,v22,v23
v_mfma_f32_16x16x32_bf16 v[124:127],a[180:183],v[232:235],v[124:127]
v_accvgpr_write_b32 a72,v16
v_accvgpr_write_b32 a73,v17
v_mfma_f32_16x16x32_bf16 v[124:127],a[176:179],v[224:227],v[124:127]
v_accvgpr_write_b32 a74,v18
v_accvgpr_write_b32 a75,v19
v_mfma_f32_16x16x32_bf16 v[132:135],a[192:195],v[80:83],v[132:135]
ds_read_b128 a[76:79],v7 offset:7168
v_add_u32 v1,s78,v1
v_mfma_f32_16x16x32_bf16 v[132:135],a[196:199],v[88:91],v[132:135]
v_exp_f32 v96,v96
v_mfma_f32_16x16x32_bf16 v[140:143],a[200:203],v[80:83],v[140:143]
global_load_lds_dwordx4 v4,s[10:11] offset:1024 nt
ds_read_b128 a[128:131],v1
)A"
R"A(v_mfma_f32_16x16x32_bf16 v[140:143],a[204:207],v[88:91],v[140:143]
v_exp_f32 v97,v97
v_mfma_f32_16x16x32_bf16 v[148:151],a[208:211],v[80:83],v[148:151]
ds_read_b128 a[132:135],v1 offset:256
v_cvt_pk_bf16_f32 v96,v96,v97
v_mfma_f32_16x16x32_bf16 v[148:151],a[212:215],v[88:91],v[148:151]
v_exp_f32 v98,v98
v_mfma_f32_16x16x32_bf16 v[156:159],a[216:219],v[80:83],v[156:159]
ds_read_b128 a[136:139],v1 offset:512
v_accvgpr_read_b32 v12,a84
v_mfma_f32_16x16x32_bf16 v[156:159],a[220:223],v[88:91],v[156:159]
v_exp_f32 v99,v99
v_mfma_f32_16x16x32_bf16 v[164:167],a[224:227],v[80:83],v[164:167]
ds_read_b128 a[140:143],v1 offset:768
v_cvt_pk_bf16_f32 v97,v98,v99
v_mfma_f32_16x16x32_bf16 v[164:167],a[228:231],v[88:91],v[164:167]
v_exp_f32 v100,v100
v_mfma_f32_16x16x32_bf16 v[172:175],a[232:235],v[80:83],v[172:175]
ds_read_b128 a[156:159],v1 offset:4864
v_accvgpr_read_b32 v13,a85
v_mfma_f32_16x16x32_bf16 v[172:175],a[236:239],v[88:91],v[172:175]
v_exp_f32 v101,v101
v_mfma_f32_16x16x32_bf16 v[180:183],a[240:243],v[80:83],v[180:183]
ds_read_b128 a[152:155],v1 offset:4608
v_cvt_pk_bf16_f32 v98,v100,v101
v_mfma_f32_16x16x32_bf16 v[180:183],a[244:247],v[88:91],v[180:183]
v_exp_f32 v102,v102
v_mfma_f32_16x16x32_bf16 v[188:191],a[248:251],v[80:83],v[188:191]
ds_read_b128 a[148:151],v1 offset:4352
v_accvgpr_read_b32 v14,a86
v_mfma_f32_16x16x32_bf16 v[188:191],a[252:255],v[88:91],v[188:191]
v_exp_f32 v103,v103
v_mfma_f32_16x16x32_bf16 v[52:55],v[28:31],v[80:83],v[52:55]
ds_read_b128 a[144:147],v1 offset:4096
v_cvt_pk_bf16_f32 v99,v102,v103
v_mfma_f32_16x16x32_bf16 v[52:55],v[28:31],v[88:91],v[52:55]
s_waitcnt lgkmcnt(4)
v_exp_f32 v104,v104
v_mfma_f32_16x16x32_bf16 v[64:67],a[128:131],v[128:131],v[32:35]
global_load_lds_dwordx4 v5,s[10:11] offset:2048 nt
ds_read_b128 a[160:163],v1 offset:8192
v_mfma_f32_16x16x32_bf16 v[64:67],a[132:135],v[136:139],v[64:67]
v_exp_f32 v105,v105
v_mfma_f32_16x16x32_bf16 v[64:67],a[136:139],v[144:147],v[64:67]
ds_read_b128 a[164:167],v1 offset:8448
v_cvt_pk_bf16_f32 v104,v104,v105
v_mfma_f32_16x16x32_bf16 v[64:67],a[140:143],v[152:155],v[64:67]
s_waitcnt lgkmcnt(4)
v_exp_f32 v106,v106
v_mfma_f32_16x16x32_bf16 v[68:71],a[156:159],v[152:155],v[32:35]
ds_read_b128 a[168:171],v1 offset:8704
v_accvgpr_read_b32 v15,a87
v_mfma_f32_16x16x32_bf16 v[68:71],a[152:155],v[144:147],v[68:71]
s_waitcnt lgkmcnt(4)
v_exp_f32 v107,v107
v_mfma_f32_16x16x32_bf16 v[68:71],a[148:151],v[136:139],v[68:71]
s_waitcnt lgkmcnt(3)
ds_read_b128 a[172:175],v1 offset:8960
v_cvt_pk_bf16_f32 v105,v106,v107
v_mfma_f32_16x16x32_bf16 v[68:71],a[144:147],v[128:131],v[68:71]
s_waitcnt lgkmcnt(3)
v_exp_f32 v108,v108
v_mfma_f32_16x16x32_bf16 v[72:75],a[160:163],v[128:131],v[32:35]
s_waitcnt lgkmcnt(2)
ds_read_b128 a[188:191],v1 offset:13056
v_accvgpr_read_b32 v8,a80
v_mfma_f32_16x16x32_bf16 v[72:75],a[164:167],v[136:139],v[72:75]
s_waitcnt lgkmcnt(2)
ds_read_b128 a[184:187],v1 offset:12800
v_accvgpr_read_b32 v9,a81
v_mfma_f32_16x16x32_bf16 v[72:75],a[168:171],v[144:147],v[72:75]
s_waitcnt lgkmcnt(2)
ds_read_b128 a[180:183],v1 offset:12544
v_accvgpr_read_b32 v10,a82
v_mfma_f32_16x16x32_bf16 v[72:75],a[172:175],v[152:155],v[72:75]
s_waitcnt lgkmcnt(2)
ds_read_b128 a[176:179],v1 offset:12288
v_accvgpr_read_b32 v11,a83
v_mfma_f32_16x16x32_bf16 v[76:79],a[188:191],v[152:155],v[32:35]
s_waitcnt lgkmcnt(2)
v_exp_f32 v109,v109
v_mfma_f32_16x16x32_bf16 v[76:79],a[184:187],v[144:147],v[76:79]
s_waitcnt lgkmcnt(1)
v_exp_f32 v110,v110
v_exp_f32 v111,v111
v_mfma_f32_16x16x32_bf16 v[76:79],a[180:183],v[136:139],v[76:79]
s_waitcnt lgkmcnt(0)
v_cvt_pk_bf16_f32 v106,v108,v109
v_cvt_pk_bf16_f32 v107,v110,v111
v_mfma_f32_16x16x32_bf16 v[76:79],a[176:179],v[128:131],v[76:79]
v_mul_f32 v8,v8,v26
v_mul_f32 v9,v9,v26
v_mfma_f32_16x16x32_bf16 v[196:199],a[192:195],v[96:99],v[196:199]
global_load_lds_dwordx4 v5,s[10:11] offset:3072
v_mul_f32 v10,v10,v26
v_mfma_f32_16x16x32_bf16 v[196:199],a[196:199],v[104:107],v[196:199]
v_mul_f32 v11,v11,v26
v_mul_f32 v12,v12,v26
v_mfma_f32_16x16x32_bf16 v[204:207],a[200:203],v[96:99],v[204:207]
v_mul_f32 v13,v13,v26
v_mul_f32 v14,v14,v26
v_mfma_f32_16x16x32_bf16 v[204:207],a[204:207],v[104:107],v[204:207]
v_mul_f32 v15,v15,v26
v_cvt_pk_bf16_f32 v8,v8,v9
v_mfma_f32_16x16x32_bf16 v[212:215],a[208:211],v[96:99],v[212:215]
v_cvt_pk_bf16_f32 v9,v10,v11
v_cvt_pk_bf16_f32 v10,v12,v13
v_mfma_f32_16x16x32_bf16 v[212:215],a[212:215],v[104:107],v[212:215]
v_cvt_pk_bf16_f32 v11,v14,v15
v_accvgpr_write_b32 a80,v8
v_mfma_f32_16x16x32_bf16 v[220:223],a[216:219],v[96:99],v[220:223]
v_accvgpr_write_b32 a81,v9
v_accvgpr_write_b32 a82,v10
v_mfma_f32_16x16x32_bf16 v[220:223],a[220:223],v[104:107],v[220:223]
ds_read_b128 a[84:87],v7
v_accvgpr_write_b32 a83,v11
v_mfma_f32_16x16x32_bf16 v[228:231],a[224:227],v[96:99],v[228:231]
v_exp_f32 v112,v112
v_mfma_f32_16x16x32_bf16 v[228:231],a[228:231],v[104:107],v[228:231]
v_exp_f32 v113,v113
v_mfma_f32_16x16x32_bf16 v[236:239],a[232:235],v[96:99],v[236:239]
v_exp_f32 v114,v114
v_mfma_f32_16x16x32_bf16 v[236:239],a[236:239],v[104:107],v[236:239]
v_exp_f32 v115,v115
v_mfma_f32_16x16x32_bf16 v[244:247],a[240:243],v[96:99],v[244:247]
s_add_u32 s10,s24,s74
s_addc_u32 s11,s25,0
v_cvt_pk_bf16_f32 v112,v112,v113
v_mfma_f32_16x16x32_bf16 v[244:247],a[244:247],v[104:107],v[244:247]
s_add_u32 s74,s74,16384
v_exp_f32 v116,v116
v_mfma_f32_16x16x32_bf16 v[252:255],a[248:251],v[96:99],v[252:255]
s_add_u32 m0,s76,16384
v_exp_f32 v117,v117
v_mfma_f32_16x16x32_bf16 v[252:255],a[252:255],v[104:107],v[252:255]
s_and_b32 s74,s74,0x3ffff
v_exp_f32 v118,v118
v_mfma_f32_16x16x32_bf16 v[56:59],v[28:31],v[96:99],v[56:59]
global_load_lds_dwordx4 v4,s[10:11]
v_cvt_pk_bf16_f32 v113,v114,v115
v_mfma_f32_16x16x32_bf16 v[56:59],v[28:31],v[104:107],v[56:59]
s_or_b32 s74,s74,s69
v_exp_f32 v119,v119
v_mfma_f32_16x16x32_bf16 v[80:83],a[128:131],v[160:163],v[36:39]
s_sub_u32 s76,s76,32768
v_cvt_pk_bf16_f32 v114,v116,v117
v_cvt_pk_bf16_f32 v115,v118,v119
v_mfma_f32_16x16x32_bf16 v[80:83],a[132:135],v[168:171],v[80:83]
s_and_b32 s76,s76,0x1ffff
v_exp_f32 v120,v120
v_mfma_f32_16x16x32_bf16 v[80:83],a[136:139],v[176:179],v[80:83]
v_exp_f32 v121,v121
v_mfma_f32_16x16x32_bf16 v[80:83],a[140:143],v[184:187],v[80:83]
v_exp_f32 v122,v122
v_mfma_f32_16x16x32_bf16 v[84:87],a[156:159],v[184:187],v[36:39]
v_exp_f32 v123,v123
v_mfma_f32_16x16x32_bf16 v[84:87],a[152:155],v[176:179],v[84:87]
v_exp_f32 v124,v124
v_mfma_f32_16x16x32_bf16 v[84:87],a[148:151],v[168:171],v[84:87]
v_exp_f32 v125,v125
v_mfma_f32_16x16x32_bf16 v[84:87],a[144:147],v[160:163],v[84:87]
v_exp_f32 v126,v126
v_mfma_f32_16x16x32_bf16 v[88:91],a[160:163],v[160:163],v[36:39]
v_exp_f32 v127,v127
v_mfma_f32_16x16x32_bf16 v[88:91],a[164:167],v[168:171],v[88:91]
v_cvt_pk_bf16_f32 v120,v120,v121
v_cvt_pk_bf16_f32 v121,v122,v123
v_mfma_f32_16x16x32_bf16 v[88:91],a[168:171],v[176:179],v[88:91]
v_cvt_pk_bf16_f32 v122,v124,v125
v_cvt_pk_bf16_f32 v123,v126,v127
v_mfma_f32_16x16x32_bf16 v[88:91],a[172:175],v[184:187],v[88:91]
v_add_u32 v2,s78,v2
v_add_u32 v3,s78,v3
v_mfma_f32_16x16x32_bf16 v[92:95],a[188:191],v[184:187],v[36:39]
s_sub_u32 s78,s77,32768
v_exp_f32 v64,v64
v_mfma_f32_16x16x32_bf16 v[92:95],a[184:187],v[176:179],v[92:95]
s_and_b32 s78,s78,0x1ffff
v_exp_f32 v65,v65
v_mfma_f32_16x16x32_bf16 v[92:95],a[180:183],v[168:171],v[92:95]
global_load_lds_dwordx4 v5,s[10:11] offset:1024
v_cvt_pk_bf16_f32 v64,v64,v65
v_mfma_f32_16x16x32_bf16 v[92:95],a[176:179],v[160:163],v[92:95]
s_sub_u32 s78,s78,s77
v_exp_f32 v66,v66
v_mfma_f32_16x16x32_bf16 a[32:35],a[192:195],v[112:115],a[32:35]
s_add_u32 s77,s77,s78
ds_read_b64_tr_b16 a[192:193],v2
ds_read_b64_tr_b16 a[194:195],v2 offset:4096
v_mfma_f32_16x16x32_bf16 a[32:35],a[196:199],v[120:123],a[32:35]
ds_read_b64_tr_b16 a[196:197],v2 offset:8192
v_accvgpr_read_b32 v20,a92
v_mfma_f32_16x16x32_bf16 a[36:39],a[200:203],v[112:115],a[36:39]
ds_read_b64_tr_b16 a[198:199],v2 offset:12288
v_accvgpr_read_b32 v21,a93
v_mfma_f32_16x16x32_bf16 a[36:39],a[204:207],v[120:123],a[36:39]
ds_read_b64_tr_b16 a[200:201],v3
v_accvgpr_read_b32 v22,a94
v_mfma_f32_16x16x32_bf16 a[40:43],a[208:211],v[112:115],a[40:43]
ds_read_b64_tr_b16 a[202:203],v3 offset:4096
ds_read_b64_tr_b16 a[204:205],v3 offset:8192
v_mfma_f32_16x16x32_bf16 a[40:43],a[212:215],v[120:123],a[40:43]
ds_read_b64_tr_b16 a[206:207],v3 offset:12288
v_accvgpr_read_b32 v23,a95
v_mfma_f32_16x16x32_bf16 a[44:47],a[216:219],v[112:115],a[44:47]
ds_read_b64_tr_b16 a[208:209],v2 offset:256
v_accvgpr_read_b32 v16,a88
v_mfma_f32_16x16x32_bf16 a[44:47],a[220:223],v[120:123],a[44:47]
ds_read_b64_tr_b16 a[210:211],v2 offset:4352
v_accvgpr_read_b32 v17,a89
v_mfma_f32_16x16x32_bf16 a[48:51],a[224:227],v[112:115],a[48:51]
ds_read_b64_tr_b16 a[212:213],v2 offset:8448
ds_read_b64_tr_b16 a[214:215],v2 offset:12544
v_mfma_f32_16x16x32_bf16 a[48:51],a[228:231],v[120:123],a[48:51]
v_exp_f32 v67,v67
v_mfma_f32_16x16x32_bf16 a[52:55],a[232:235],v[112:115],a[52:55]
v_exp_f32 v68,v68
v_mfma_f32_16x16x32_bf16 a[52:55],a[236:239],v[120:123],a[52:55]
v_exp_f32 v69,v69
v_mfma_f32_16x16x32_bf16 a[56:59],a[240:243],v[112:115],a[56:59]
v_exp_f32 v70,v70
v_mfma_f32_16x16x32_bf16 a[56:59],a[244:247],v[120:123],a[56:59]
L_pd10_%=:
s_cbranch_execz L_t1b_%=
v_exp_f32 v71,v71
v_mfma_f32_16x16x32_bf16 a[60:63],a[248:251],v[112:115],a[60:63]
global_load_lds_dwordx4 v4,s[10:11] offset:2048
ds_read_b64_tr_b16 a[216:217],v3 offset:256
v_mfma_f32_16x16x32_bf16 a[60:63],a[252:255],v[120:123],a[60:63]
ds_read_b64_tr_b16 a[218:219],v3 offset:4352
v_cvt_pk_bf16_f32 v65,v66,v67
v_mfma_f32_16x16x32_bf16 v[60:63],v[28:31],v[112:115],v[60:63]
v_cvt_pk_bf16_f32 v66,v68,v69
v_cvt_pk_bf16_f32 v67,v70,v71
v_mfma_f32_16x16x32_bf16 v[60:63],v[28:31],v[120:123],v[60:63]
v_exp_f32 v72,v72
v_mfma_f32_16x16x32_bf16 v[96:99],a[128:131],v[192:195],v[40:43]
v_exp_f32 v73,v73
v_mfma_f32_16x16x32_bf16 v[96:99],a[132:135],v[200:203],v[96:99]
v_exp_f32 v74,v74
v_mfma_f32_16x16x32_bf16 v[96:99],a[136:139],v[208:211],v[96:99]
v_exp_f32 v75,v75
v_mfma_f32_16x16x32_bf16 v[96:99],a[140:143],v[216:219],v[96:99]
v_exp_f32 v76,v76
v_mfma_f32_16x16x32_bf16 v[100:103],a[156:159],v[216:219],v[40:43]
v_exp_f32 v77,v77
v_mfma_f32_16x16x32_bf16 v[100:103],a[152:155],v[208:211],v[100:103]
v_exp_f32 v78,v78
v_mfma_f32_16x16x32_bf16 v[100:103],a[148:151],v[200:203],v[100:103]
v_exp_f32 v79,v79
v_mfma_f32_16x16x32_bf16 v[100:103],a[144:147],v[192:195],v[100:103]
ds_read_b64_tr_b16 a[220:221],v3 offset:8448
ds_read_b64_tr_b16 a[222:223],v3 offset:12544
v_mfma_f32_16x16x32_bf16 v[104:107],a[160:163],v[192:195],v[40:43]
v_cvt_pk_bf16_f32 v72,v72,v73
v_cvt_pk_bf16_f32 v73,v74,v75
v_mfma_f32_16x16x32_bf16 v[104:107],a[164:167],v[200:203],v[104:107]
v_cvt_pk_bf16_f32 v74,v76,v77
v_cvt_pk_bf16_f32 v75,v78,v79
v_mfma_f32_16x16x32_bf16 v[104:107],a[168:171],v[208:211],v[104:107]
ds_read_b64_tr_b16 a[224:225],v2 offset:512
ds_read_b64_tr_b16 a[226:227],v2 offset:4608
v_mfma_f32_16x16x32_bf16 v[104:107],a[172:175],v[216:219],v[104:107]
ds_read_b64_tr_b16 a[228:229],v2 offset:8704
v_accvgpr_read_b32 v18,a90
v_mfma_f32_16x16x32_bf16 v[108:111],a[188:191],v[216:219],v[40:43]
global_load_lds_dwordx4 v5,s[10:11] offset:3072
ds_read_b64_tr_b16 a[230:231],v2 offset:12800
v_mfma_f32_16x16x32_bf16 v[108:111],a[184:187],v[208:211],v[108:111]
ds_read_b64_tr_b16 a[232:233],v3 offset:512
v_accvgpr_read_b32 v19,a91
v_mfma_f32_16x16x32_bf16 v[108:111],a[180:183],v[200:203],v[108:111]
ds_read_b64_tr_b16 a[234:235],v3 offset:4608
ds_read_b64_tr_b16 a[236:237],v3 offset:8704
v_mfma_f32_16x16x32_bf16 v[108:111],a[176:179],v[192:195],v[108:111]
s_waitcnt lgkmcnt(7)
ds_read_b64_tr_b16 a[238:239],v3 offset:12800
v_mul_f32 v16,v16,v26
v_mfma_f32_16x16x32_bf16 a[0:3],a[192:195],v[64:67],a[0:3]
ds_read_b64_tr_b16 a[240:241],v2 offset:768
v_mul_f32 v17,v17,v26
v_mfma_f32_16x16x32_bf16 a[0:3],a[196:199],v[72:75],a[0:3]
ds_read_b64_tr_b16 a[242:243],v2 offset:4864
v_mul_f32 v18,v18,v26
v_mfma_f32_16x16x32_bf16 a[4:7],a[200:203],v[64:67],a[4:7]
ds_read_b64_tr_b16 a[244:245],v2 offset:8960
ds_read_b64_tr_b16 a[246:247],v2 offset:13056
)A"
R"A(v_mfma_f32_16x16x32_bf16 a[4:7],a[204:207],v[72:75],a[4:7]
ds_read_b64_tr_b16 a[248:249],v3 offset:768
v_mul_f32 v19,v19,v26
v_mfma_f32_16x16x32_bf16 a[8:11],a[208:211],v[64:67],a[8:11]
ds_read_b64_tr_b16 a[250:251],v3 offset:4864
v_mul_f32 v20,v20,v26
v_mfma_f32_16x16x32_bf16 a[8:11],a[212:215],v[72:75],a[8:11]
v_mul_f32 v21,v21,v26
v_mul_f32 v22,v22,v26
v_mul_f32 v23,v23,v26
v_cvt_pk_bf16_f32 v16,v16,v17
v_cvt_pk_bf16_f32 v17,v18,v19
v_cvt_pk_bf16_f32 v18,v20,v21
v_cvt_pk_bf16_f32 v19,v22,v23
v_accvgpr_write_b32 a88,v16
v_accvgpr_write_b32 a89,v17
v_accvgpr_write_b32 a90,v18
v_accvgpr_write_b32 a91,v19
v_mfma_f32_16x16x32_bf16 a[12:15],a[216:219],v[64:67],a[12:15]
ds_read_b128 a[92:95],v7 offset:1024
v_mov_b32 v8,v7
v_mfma_f32_16x16x32_bf16 a[12:15],a[220:223],v[72:75],a[12:15]
s_waitcnt lgkmcnt(7)
ds_read_b64_tr_b16 a[252:253],v3 offset:8960
v_add_u32 v1,s78,v1
v_mfma_f32_16x16x32_bf16 a[16:19],a[224:227],v[64:67],a[16:19]
ds_read_b64_tr_b16 a[254:255],v3 offset:13056
v_mfma_f32_16x16x32_bf16 a[16:19],a[228:231],v[72:75],a[16:19]
s_waitcnt lgkmcnt(4)
v_exp_f32 v80,v80
v_mfma_f32_16x16x32_bf16 a[20:23],a[232:235],v[64:67],a[20:23]
s_getreg_b32 s92,hwreg(HW_REG_IB_STS,0,4)
s_min_u32 s4,s92,s6
s_mov_b32 s6,s92
v_mfma_f32_16x16x32_bf16 a[20:23],a[236:239],v[72:75],a[20:23]
s_cmp_ge_u32 s4,s9
s_cselect_b32 s4,15,s5
ds_read_b128 v[24:27],v8 offset:4096
v_mfma_f32_16x16x32_bf16 a[24:27],a[240:243],v[64:67],a[24:27]
v_exp_f32 v81,v81
v_mfma_f32_16x16x32_bf16 a[24:27],a[244:247],v[72:75],a[24:27]
s_waitcnt lgkmcnt(4)
v_exp_f32 v82,v82
v_mfma_f32_16x16x32_bf16 a[28:31],a[248:251],v[64:67],a[28:31]
s_waitcnt lgkmcnt(1)
v_exp_f32 v83,v83
v_mfma_f32_16x16x32_bf16 a[28:31],a[252:255],v[72:75],a[28:31]
v_exp_f32 v84,v84
v_mfma_f32_16x16x32_bf16 v[48:51],v[28:31],v[64:67],v[48:51]
s_getpc_b64 s[92:93]
s_cmp_lg_u32 s4,0
s_cbranch_scc1 L_pace_%=
v_exp_f32 v85,v85
v_exp_f32 v86,v86
v_exp_f32 v87,v87
v_exp_f32 v88,v88
v_exp_f32 v89,v89
v_exp_f32 v90,v90
v_exp_f32 v91,v91
v_exp_f32 v92,v92
v_exp_f32 v93,v93
v_exp_f32 v94,v94
v_exp_f32 v95,v95
v_mfma_f32_16x16x32_bf16 v[48:51],v[28:31],v[72:75],v[48:51]
ds_read_b32 v7,v8 offset:5120
v_cvt_pk_bf16_f32 v80,v80,v81
v_cvt_pk_bf16_f32 v81,v82,v83
v_cvt_pk_bf16_f32 v82,v84,v85
v_cvt_pk_bf16_f32 v83,v86,v87
v_cvt_pk_bf16_f32 v88,v88,v89
v_cvt_pk_bf16_f32 v89,v90,v91
v_cvt_pk_bf16_f32 v90,v92,v93
v_cvt_pk_bf16_f32 v91,v94,v95
s_mov_b32 s70,7
L_a1_%=:
s_waitcnt vmcnt(16)
s_barrier
v_mfma_f32_16x16x32_bf16 v[112:115],a[128:131],v[224:227],v[44:47]
s_add_u32 s10,s20,s74
s_addc_u32 s11,s21,0
s_add_u32 m0,s76,0
v_mfma_f32_16x16x32_bf16 v[112:115],a[132:135],v[232:235],v[112:115]
s_add_u32 s75,s75,16384
s_and_b32 s69,s75,0xfffc0000
ds_read_b128 a[128:131],v1
v_mfma_f32_16x16x32_bf16 v[112:115],a[136:139],v[240:243],v[112:115]
global_load_lds_dwordx4 v4,s[10:11] nt
v_mfma_f32_16x16x32_bf16 v[112:115],a[140:143],v[248:251],v[112:115]
ds_read_b128 a[132:135],v1 offset:256
v_mfma_f32_16x16x32_bf16 v[116:119],a[156:159],v[248:251],v[44:47]
v_mfma_f32_16x16x32_bf16 v[116:119],a[152:155],v[240:243],v[116:119]
ds_read_b128 a[136:139],v1 offset:512
v_mfma_f32_16x16x32_bf16 v[116:119],a[148:151],v[232:235],v[116:119]
v_mfma_f32_16x16x32_bf16 v[116:119],a[144:147],v[224:227],v[116:119]
ds_read_b128 a[140:143],v1 offset:768
v_mfma_f32_16x16x32_bf16 v[120:123],a[160:163],v[224:227],v[44:47]
v_mfma_f32_16x16x32_bf16 v[120:123],a[164:167],v[232:235],v[120:123]
ds_read_b128 a[156:159],v1 offset:4864
v_mfma_f32_16x16x32_bf16 v[120:123],a[168:171],v[240:243],v[120:123]
v_mfma_f32_16x16x32_bf16 v[120:123],a[172:175],v[248:251],v[120:123]
ds_read_b128 a[152:155],v1 offset:4608
v_mfma_f32_16x16x32_bf16 v[124:127],a[188:191],v[248:251],v[44:47]
v_exp_f32 v96,v96
v_mfma_f32_16x16x32_bf16 v[124:127],a[184:187],v[240:243],v[124:127]
ds_read_b128 a[148:151],v1 offset:4352
v_add_u32 v2,s78,v2
v_mfma_f32_16x16x32_bf16 v[124:127],a[180:183],v[232:235],v[124:127]
v_exp_f32 v97,v97
v_mfma_f32_16x16x32_bf16 v[124:127],a[176:179],v[224:227],v[124:127]
ds_read_b128 a[144:147],v1 offset:4096
v_cvt_pk_bf16_f32 v96,v96,v97
v_mfma_f32_16x16x32_bf16 v[132:135],a[192:195],v[80:83],v[132:135]
v_exp_f32 v98,v98
v_mfma_f32_16x16x32_bf16 v[132:135],a[196:199],v[88:91],v[132:135]
ds_read_b128 a[160:163],v1 offset:8192
v_add_u32 v3,s78,v3
v_mfma_f32_16x16x32_bf16 v[140:143],a[200:203],v[80:83],v[140:143]
s_sub_u32 s78,s77,32768
global_load_lds_dwordx4 v4,s[10:11] offset:1024 nt
v_mfma_f32_16x16x32_bf16 v[140:143],a[204:207],v[88:91],v[140:143]
s_and_b32 s78,s78,0x1ffff
v_exp_f32 v99,v99
v_mfma_f32_16x16x32_bf16 v[148:151],a[208:211],v[80:83],v[148:151]
s_sub_u32 s78,s78,s77
ds_read_b128 a[164:167],v1 offset:8448
v_cvt_pk_bf16_f32 v97,v98,v99
v_mfma_f32_16x16x32_bf16 v[148:151],a[212:215],v[88:91],v[148:151]
s_add_u32 s77,s77,s78
v_exp_f32 v100,v100
v_mfma_f32_16x16x32_bf16 v[156:159],a[216:219],v[80:83],v[156:159]
v_exp_f32 v101,v101
v_mfma_f32_16x16x32_bf16 v[156:159],a[220:223],v[88:91],v[156:159]
ds_read_b128 a[168:171],v1 offset:8704
v_cvt_pk_bf16_f32 v98,v100,v101
v_mfma_f32_16x16x32_bf16 v[164:167],a[224:227],v[80:83],v[164:167]
v_exp_f32 v102,v102
v_mfma_f32_16x16x32_bf16 v[164:167],a[228:231],v[88:91],v[164:167]
v_exp_f32 v103,v103
v_mfma_f32_16x16x32_bf16 v[172:175],a[232:235],v[80:83],v[172:175]
ds_read_b128 a[172:175],v1 offset:8960
v_cvt_pk_bf16_f32 v99,v102,v103
v_mfma_f32_16x16x32_bf16 v[172:175],a[236:239],v[88:91],v[172:175]
v_exp_f32 v104,v104
v_mfma_f32_16x16x32_bf16 v[180:183],a[240:243],v[80:83],v[180:183]
v_exp_f32 v105,v105
v_mfma_f32_16x16x32_bf16 v[180:183],a[244:247],v[88:91],v[180:183]
ds_read_b128 a[188:191],v1 offset:13056
v_cvt_pk_bf16_f32 v104,v104,v105
v_mfma_f32_16x16x32_bf16 v[188:191],a[248:251],v[80:83],v[188:191]
v_exp_f32 v106,v106
v_mfma_f32_16x16x32_bf16 v[188:191],a[252:255],v[88:91],v[188:191]
v_exp_f32 v107,v107
v_mfma_f32_16x16x32_bf16 v[52:55],v[28:31],v[80:83],v[52:55]
ds_read_b128 a[184:187],v1 offset:12800
v_cvt_pk_bf16_f32 v105,v106,v107
v_mfma_f32_16x16x32_bf16 v[52:55],v[28:31],v[88:91],v[52:55]
s_waitcnt lgkmcnt(3)
v_exp_f32 v108,v108
v_mfma_f32_16x16x32_bf16 v[64:67],a[128:131],v[128:131],v[32:35]
global_load_lds_dwordx4 v5,s[10:11] offset:2048 nt
ds_read_b128 a[180:183],v1 offset:12544
v_mfma_f32_16x16x32_bf16 v[64:67],a[132:135],v[136:139],v[64:67]
v_exp_f32 v109,v109
v_mfma_f32_16x16x32_bf16 v[64:67],a[136:139],v[144:147],v[64:67]
ds_read_b128 a[176:179],v1 offset:12288
v_cvt_pk_bf16_f32 v106,v108,v109
v_mfma_f32_16x16x32_bf16 v[64:67],a[140:143],v[152:155],v[64:67]
v_exp_f32 v110,v110
v_mfma_f32_16x16x32_bf16 v[68:71],a[156:159],v[152:155],v[32:35]
v_exp_f32 v111,v111
v_mfma_f32_16x16x32_bf16 v[68:71],a[152:155],v[144:147],v[68:71]
v_cvt_pk_bf16_f32 v107,v110,v111
v_add_u32 v1,s78,v1
v_mfma_f32_16x16x32_bf16 v[68:71],a[148:151],v[136:139],v[68:71]
v_exp_f32 v112,v112
v_mfma_f32_16x16x32_bf16 v[68:71],a[144:147],v[128:131],v[68:71]
v_exp_f32 v113,v113
v_mfma_f32_16x16x32_bf16 v[72:75],a[160:163],v[128:131],v[32:35]
v_exp_f32 v114,v114
v_mfma_f32_16x16x32_bf16 v[72:75],a[164:167],v[136:139],v[72:75]
v_exp_f32 v115,v115
v_mfma_f32_16x16x32_bf16 v[72:75],a[168:171],v[144:147],v[72:75]
s_waitcnt lgkmcnt(0)
v_exp_f32 v116,v116
v_mfma_f32_16x16x32_bf16 v[72:75],a[172:175],v[152:155],v[72:75]
v_exp_f32 v117,v117
v_mfma_f32_16x16x32_bf16 v[76:79],a[188:191],v[152:155],v[32:35]
v_exp_f32 v118,v118
v_mfma_f32_16x16x32_bf16 v[76:79],a[184:187],v[144:147],v[76:79]
v_exp_f32 v119,v119
v_mfma_f32_16x16x32_bf16 v[76:79],a[180:183],v[136:139],v[76:79]
v_cvt_pk_bf16_f32 v112,v112,v113
v_cvt_pk_bf16_f32 v113,v114,v115
v_mfma_f32_16x16x32_bf16 v[76:79],a[176:179],v[128:131],v[76:79]
v_cvt_pk_bf16_f32 v114,v116,v117
v_cvt_pk_bf16_f32 v115,v118,v119
v_mfma_f32_16x16x32_bf16 v[196:199],a[192:195],v[96:99],v[196:199]
global_load_lds_dwordx4 v5,s[10:11] offset:3072
v_mfma_f32_16x16x32_bf16 v[196:199],a[196:199],v[104:107],v[196:199]
v_exp_f32 v120,v120
v_mfma_f32_16x16x32_bf16 v[204:207],a[200:203],v[96:99],v[204:207]
v_exp_f32 v121,v121
v_mfma_f32_16x16x32_bf16 v[204:207],a[204:207],v[104:107],v[204:207]
v_exp_f32 v122,v122
v_mfma_f32_16x16x32_bf16 v[212:215],a[208:211],v[96:99],v[212:215]
v_exp_f32 v123,v123
v_mfma_f32_16x16x32_bf16 v[212:215],a[212:215],v[104:107],v[212:215]
v_exp_f32 v124,v124
v_mfma_f32_16x16x32_bf16 v[220:223],a[216:219],v[96:99],v[220:223]
v_exp_f32 v125,v125
v_mfma_f32_16x16x32_bf16 v[220:223],a[220:223],v[104:107],v[220:223]
v_exp_f32 v126,v126
v_mfma_f32_16x16x32_bf16 v[228:231],a[224:227],v[96:99],v[228:231]
v_exp_f32 v127,v127
v_mfma_f32_16x16x32_bf16 v[228:231],a[228:231],v[104:107],v[228:231]
v_cvt_pk_bf16_f32 v120,v120,v121
v_cvt_pk_bf16_f32 v121,v122,v123
v_mfma_f32_16x16x32_bf16 v[236:239],a[232:235],v[96:99],v[236:239]
v_cvt_pk_bf16_f32 v122,v124,v125
v_cvt_pk_bf16_f32 v123,v126,v127
v_mfma_f32_16x16x32_bf16 v[236:239],a[236:239],v[104:107],v[236:239]
v_exp_f32 v64,v64
v_mfma_f32_16x16x32_bf16 v[244:247],a[240:243],v[96:99],v[244:247]
s_add_u32 s10,s24,s74
s_addc_u32 s11,s25,0
s_add_u32 s74,s74,16384
v_mfma_f32_16x16x32_bf16 v[244:247],a[244:247],v[104:107],v[244:247]
s_and_b32 s74,s74,0x3ffff
v_exp_f32 v65,v65
v_mfma_f32_16x16x32_bf16 v[252:255],a[248:251],v[96:99],v[252:255]
s_add_u32 m0,s76,16384
v_exp_f32 v66,v66
v_mfma_f32_16x16x32_bf16 v[252:255],a[252:255],v[104:107],v[252:255]
s_or_b32 s74,s74,s69
v_exp_f32 v67,v67
v_mfma_f32_16x16x32_bf16 v[56:59],v[28:31],v[96:99],v[56:59]
global_load_lds_dwordx4 v4,s[10:11]
v_cvt_pk_bf16_f32 v64,v64,v65
v_mfma_f32_16x16x32_bf16 v[56:59],v[28:31],v[104:107],v[56:59]
s_sub_u32 s76,s76,32768
v_exp_f32 v68,v68
v_mfma_f32_16x16x32_bf16 v[80:83],a[128:131],v[160:163],v[36:39]
s_and_b32 s76,s76,0x1ffff
v_exp_f32 v69,v69
v_mfma_f32_16x16x32_bf16 v[80:83],a[132:135],v[168:171],v[80:83]
v_exp_f32 v70,v70
v_mfma_f32_16x16x32_bf16 v[80:83],a[136:139],v[176:179],v[80:83]
v_exp_f32 v71,v71
v_mfma_f32_16x16x32_bf16 v[80:83],a[140:143],v[184:187],v[80:83]
v_cvt_pk_bf16_f32 v65,v66,v67
v_cvt_pk_bf16_f32 v66,v68,v69
v_mfma_f32_16x16x32_bf16 v[84:87],a[156:159],v[184:187],v[36:39]
v_exp_f32 v72,v72
v_mfma_f32_16x16x32_bf16 v[84:87],a[152:155],v[176:179],v[84:87]
v_exp_f32 v73,v73
v_mfma_f32_16x16x32_bf16 v[84:87],a[148:151],v[168:171],v[84:87]
v_cvt_pk_bf16_f32 v67,v70,v71
v_cvt_pk_bf16_f32 v72,v72,v73
v_mfma_f32_16x16x32_bf16 v[84:87],a[144:147],v[160:163],v[84:87]
v_exp_f32 v74,v74
v_mfma_f32_16x16x32_bf16 v[88:91],a[160:163],v[160:163],v[36:39]
v_exp_f32 v75,v75
v_mfma_f32_16x16x32_bf16 v[88:91],a[164:167],v[168:171],v[88:91]
v_exp_f32 v76,v76
v_mfma_f32_16x16x32_bf16 v[88:91],a[168:171],v[176:179],v[88:91]
v_exp_f32 v77,v77
v_mfma_f32_16x16x32_bf16 v[88:91],a[172:175],v[184:187],v[88:91]
v_exp_f32 v78,v78
v_mfma_f32_16x16x32_bf16 v[92:95],a[188:191],v[184:187],v[36:39]
v_exp_f32 v79,v79
v_mfma_f32_16x16x32_bf16 v[92:95],a[184:187],v[176:179],v[92:95]
v_cvt_pk_bf16_f32 v73,v74,v75
v_cvt_pk_bf16_f32 v74,v76,v77
v_mfma_f32_16x16x32_bf16 v[92:95],a[180:183],v[168:171],v[92:95]
global_load_lds_dwordx4 v5,s[10:11] offset:1024
v_cvt_pk_bf16_f32 v75,v78,v79
v_mfma_f32_16x16x32_bf16 v[92:95],a[176:179],v[160:163],v[92:95]
v_exp_f32 v80,v80
v_mfma_f32_16x16x32_bf16 a[32:35],a[192:195],v[112:115],a[32:35]
ds_read_b64_tr_b16 a[192:193],v2
ds_read_b64_tr_b16 a[194:195],v2 offset:4096
v_mfma_f32_16x16x32_bf16 a[32:35],a[196:199],v[120:123],a[32:35]
v_exp_f32 v81,v81
v_mfma_f32_16x16x32_bf16 a[36:39],a[200:203],v[112:115],a[36:39]
ds_read_b64_tr_b16 a[196:197],v2 offset:8192
ds_read_b64_tr_b16 a[198:199],v2 offset:12288
v_mfma_f32_16x16x32_bf16 a[36:39],a[204:207],v[120:123],a[36:39]
ds_read_b64_tr_b16 a[200:201],v3
v_cvt_pk_bf16_f32 v80,v80,v81
v_mfma_f32_16x16x32_bf16 a[40:43],a[208:211],v[112:115],a[40:43]
v_exp_f32 v82,v82
v_mfma_f32_16x16x32_bf16 a[40:43],a[212:215],v[120:123],a[40:43]
ds_read_b64_tr_b16 a[202:203],v3 offset:4096
ds_read_b64_tr_b16 a[204:205],v3 offset:8192
v_mfma_f32_16x16x32_bf16 a[44:47],a[216:219],v[112:115],a[44:47]
v_exp_f32 v83,v83
v_mfma_f32_16x16x32_bf16 a[44:47],a[220:223],v[120:123],a[44:47]
)A"
R"A(ds_read_b64_tr_b16 a[206:207],v3 offset:12288
ds_read_b64_tr_b16 a[208:209],v2 offset:256
v_mfma_f32_16x16x32_bf16 a[48:51],a[224:227],v[112:115],a[48:51]
ds_read_b64_tr_b16 a[210:211],v2 offset:4352
v_cvt_pk_bf16_f32 v81,v82,v83
v_mfma_f32_16x16x32_bf16 a[48:51],a[228:231],v[120:123],a[48:51]
v_exp_f32 v84,v84
v_mfma_f32_16x16x32_bf16 a[52:55],a[232:235],v[112:115],a[52:55]
ds_read_b64_tr_b16 a[212:213],v2 offset:8448
ds_read_b64_tr_b16 a[214:215],v2 offset:12544
v_mfma_f32_16x16x32_bf16 a[52:55],a[236:239],v[120:123],a[52:55]
v_exp_f32 v85,v85
v_mfma_f32_16x16x32_bf16 a[56:59],a[240:243],v[112:115],a[56:59]
ds_read_b64_tr_b16 a[216:217],v3 offset:256
ds_read_b64_tr_b16 a[218:219],v3 offset:4352
v_mfma_f32_16x16x32_bf16 a[56:59],a[244:247],v[120:123],a[56:59]
ds_read_b64_tr_b16 a[220:221],v3 offset:8448
v_cvt_pk_bf16_f32 v82,v84,v85
v_mfma_f32_16x16x32_bf16 a[60:63],a[248:251],v[112:115],a[60:63]
global_load_lds_dwordx4 v4,s[10:11] offset:2048
ds_read_b64_tr_b16 a[222:223],v3 offset:12544
v_mfma_f32_16x16x32_bf16 a[60:63],a[252:255],v[120:123],a[60:63]
v_exp_f32 v86,v86
v_mfma_f32_16x16x32_bf16 v[60:63],v[28:31],v[112:115],v[60:63]
ds_read_b64_tr_b16 a[224:225],v2 offset:512
ds_read_b64_tr_b16 a[226:227],v2 offset:4608
v_mfma_f32_16x16x32_bf16 v[60:63],v[28:31],v[120:123],v[60:63]
v_exp_f32 v87,v87
v_mfma_f32_16x16x32_bf16 v[96:99],a[128:131],v[192:195],v[40:43]
ds_read_b64_tr_b16 a[228:229],v2 offset:8704
ds_read_b64_tr_b16 a[230:231],v2 offset:12800
v_mfma_f32_16x16x32_bf16 v[96:99],a[132:135],v[200:203],v[96:99]
ds_read_b64_tr_b16 a[232:233],v3 offset:512
v_cvt_pk_bf16_f32 v83,v86,v87
v_mfma_f32_16x16x32_bf16 v[96:99],a[136:139],v[208:211],v[96:99]
v_exp_f32 v88,v88
v_mfma_f32_16x16x32_bf16 v[96:99],a[140:143],v[216:219],v[96:99]
ds_read_b64_tr_b16 a[234:235],v3 offset:4608
ds_read_b64_tr_b16 a[236:237],v3 offset:8704
v_mfma_f32_16x16x32_bf16 v[100:103],a[156:159],v[216:219],v[40:43]
v_exp_f32 v89,v89
v_mfma_f32_16x16x32_bf16 v[100:103],a[152:155],v[208:211],v[100:103]
ds_read_b64_tr_b16 a[238:239],v3 offset:12800
ds_read_b64_tr_b16 a[240:241],v2 offset:768
v_mfma_f32_16x16x32_bf16 v[100:103],a[148:151],v[200:203],v[100:103]
ds_read_b64_tr_b16 a[242:243],v2 offset:4864
v_cvt_pk_bf16_f32 v88,v88,v89
v_mfma_f32_16x16x32_bf16 v[100:103],a[144:147],v[192:195],v[100:103]
v_exp_f32 v90,v90
v_mfma_f32_16x16x32_bf16 v[104:107],a[160:163],v[192:195],v[40:43]
ds_read_b64_tr_b16 a[244:245],v2 offset:8960
ds_read_b64_tr_b16 a[246:247],v2 offset:13056
v_mfma_f32_16x16x32_bf16 v[104:107],a[164:167],v[200:203],v[104:107]
v_exp_f32 v91,v91
v_mfma_f32_16x16x32_bf16 v[104:107],a[168:171],v[208:211],v[104:107]
ds_read_b64_tr_b16 a[248:249],v3 offset:768
ds_read_b64_tr_b16 a[250:251],v3 offset:4864
v_mfma_f32_16x16x32_bf16 v[104:107],a[172:175],v[216:219],v[104:107]
ds_read_b64_tr_b16 a[252:253],v3 offset:8960
v_cvt_pk_bf16_f32 v89,v90,v91
v_mfma_f32_16x16x32_bf16 v[108:111],a[188:191],v[216:219],v[40:43]
global_load_lds_dwordx4 v5,s[10:11] offset:3072
ds_read_b64_tr_b16 a[254:255],v3 offset:13056
v_mfma_f32_16x16x32_bf16 v[108:111],a[184:187],v[208:211],v[108:111]
v_exp_f32 v92,v92
v_mfma_f32_16x16x32_bf16 v[108:111],a[180:183],v[200:203],v[108:111]
v_exp_f32 v93,v93
v_mfma_f32_16x16x32_bf16 v[108:111],a[176:179],v[192:195],v[108:111]
s_waitcnt lgkmcnt(6)
v_exp_f32 v94,v94
v_mfma_f32_16x16x32_bf16 a[0:3],a[192:195],v[64:67],a[0:3]
v_exp_f32 v95,v95
v_mfma_f32_16x16x32_bf16 a[0:3],a[196:199],v[72:75],a[0:3]
v_cvt_pk_bf16_f32 v90,v92,v93
v_cvt_pk_bf16_f32 v91,v94,v95
v_mfma_f32_16x16x32_bf16 a[4:7],a[200:203],v[64:67],a[4:7]
v_mfma_f32_16x16x32_bf16 a[4:7],a[204:207],v[72:75],a[4:7]
v_mfma_f32_16x16x32_bf16 a[8:11],a[208:211],v[64:67],a[8:11]
v_mfma_f32_16x16x32_bf16 a[8:11],a[212:215],v[72:75],a[8:11]
v_mfma_f32_16x16x32_bf16 a[12:15],a[216:219],v[64:67],a[12:15]
v_mfma_f32_16x16x32_bf16 a[12:15],a[220:223],v[72:75],a[12:15]
v_mfma_f32_16x16x32_bf16 a[16:19],a[224:227],v[64:67],a[16:19]
v_mfma_f32_16x16x32_bf16 a[16:19],a[228:231],v[72:75],a[16:19]
v_mfma_f32_16x16x32_bf16 a[20:23],a[232:235],v[64:67],a[20:23]
s_getreg_b32 s92,hwreg(HW_REG_IB_STS,0,4)
s_min_u32 s4,s92,s6
s_mov_b32 s6,s92
v_mfma_f32_16x16x32_bf16 a[20:23],a[236:239],v[72:75],a[20:23]
s_cmp_ge_u32 s4,s9
s_cselect_b32 s4,15,s5
v_mfma_f32_16x16x32_bf16 a[24:27],a[240:243],v[64:67],a[24:27]
s_waitcnt lgkmcnt(0)
v_mfma_f32_16x16x32_bf16 a[24:27],a[244:247],v[72:75],a[24:27]
v_mfma_f32_16x16x32_bf16 a[28:31],a[248:251],v[64:67],a[28:31]
v_mfma_f32_16x16x32_bf16 a[28:31],a[252:255],v[72:75],a[28:31]
s_getpc_b64 s[92:93]
s_cmp_lg_u32 s4,0
s_cbranch_scc1 L_pace_%=
v_mfma_f32_16x16x32_bf16 v[48:51],v[28:31],v[64:67],v[48:51]
v_mfma_f32_16x16x32_bf16 v[48:51],v[28:31],v[72:75],v[48:51]
s_sub_u32 s70,s70,1
s_cbranch_scc0 L_a1_%=
L_nx1_%=:
s_waitcnt vmcnt(16)
s_barrier
v_mfma_f32_16x16x32_bf16 v[112:115],a[128:131],v[224:227],v[44:47]
s_mul_i32 s86,s60,10240
s_mul_i32 s85,s63,1310720
s_mul_i32 s66,s64,320
v_mfma_f32_16x16x32_bf16 v[112:115],a[132:135],v[232:235],v[112:115]
s_add_u32 s85,s85,s66
s_add_u32 s83,s64,0
s_and_b32 s83,s83,3
v_mfma_f32_16x16x32_bf16 v[112:115],a[136:139],v[240:243],v[112:115]
s_lshl_b32 s83,s83,1
s_mul_i32 s83,s83,163840
s_add_u32 s83,s83,s85
v_mfma_f32_16x16x32_bf16 v[112:115],a[140:143],v[248:251],v[112:115]
s_cmp_eq_u32 s60,3
s_cselect_b32 s69,0x40000000,0
v_add_u32 v14,s69,v6
v_mfma_f32_16x16x32_bf16 v[116:119],a[156:159],v[248:251],v[44:47]
s_add_u32 s84,s83,20480
ds_read_b128 a[128:131],v1
v_add_u32 v2,s78,v2
v_mfma_f32_16x16x32_bf16 v[116:119],a[152:155],v[240:243],v[116:119]
s_add_u32 m0,s86,122880
v_exp_f32 v96,v96
v_mfma_f32_16x16x32_bf16 v[116:119],a[148:151],v[232:235],v[116:119]
s_getpc_b64 s[8:9]
L_pcg_%=:
ds_read_b128 a[132:135],v1 offset:256
v_add_u32 v3,s78,v3
v_mfma_f32_16x16x32_bf16 v[116:119],a[144:147],v[224:227],v[116:119]
buffer_load_dwordx4 v24,s[48:51],s83 offen lds
v_mfma_f32_16x16x32_bf16 v[120:123],a[160:163],v[224:227],v[44:47]
s_sub_u32 s78,s77,32768
s_and_b32 s78,s78,0x1ffff
ds_read_b128 a[136:139],v1 offset:512
v_mfma_f32_16x16x32_bf16 v[120:123],a[164:167],v[232:235],v[120:123]
buffer_store_dwordx4 a[112:115],v14,s[40:43],s81 offen offset:3072 sc1
v_mfma_f32_16x16x32_bf16 v[120:123],a[168:171],v[240:243],v[120:123]
s_sub_u32 s78,s78,s77
ds_read_b128 a[140:143],v1 offset:768
v_lshlrev_b32 v4,10,v6
v_mfma_f32_16x16x32_bf16 v[120:123],a[172:175],v[248:251],v[120:123]
s_add_u32 s77,s77,s78
v_exp_f32 v97,v97
v_mfma_f32_16x16x32_bf16 v[124:127],a[188:191],v[248:251],v[44:47]
s_mov_b32 s6,L_gbF_%=-L_pcg_%=
ds_read_b128 a[156:159],v1 offset:4864
v_cvt_pk_bf16_f32 v96,v96,v97
v_mfma_f32_16x16x32_bf16 v[124:127],a[184:187],v[240:243],v[124:127]
s_mov_b32 s10,L_geF_%=-L_gbF_%=
v_exp_f32 v98,v98
v_mfma_f32_16x16x32_bf16 v[124:127],a[180:183],v[232:235],v[124:127]
s_mov_b32 s11,0x20000
s_lshr_b32 s7,%1,3
ds_read_b128 a[152:155],v1 offset:4608
v_mfma_f32_16x16x32_bf16 v[124:127],a[176:179],v[224:227],v[124:127]
s_lshl_b32 s7,s7,2
v_exp_f32 v99,v99
v_mfma_f32_16x16x32_bf16 v[132:135],a[192:195],v[80:83],v[132:135]
s_add_u32 s7,s7,s60
ds_read_b128 a[148:151],v1 offset:4352
v_cvt_pk_bf16_f32 v97,v98,v99
v_mfma_f32_16x16x32_bf16 v[132:135],a[196:199],v[88:91],v[132:135]
buffer_load_dwordx4 v25,s[48:51],s83 offen offset:1024 lds
v_mfma_f32_16x16x32_bf16 v[140:143],a[200:203],v[80:83],v[140:143]
s_lshl_b32 s7,s7,7
ds_read_b128 a[144:147],v1 offset:4096
v_mov_b32 v8,s52
v_mfma_f32_16x16x32_bf16 v[140:143],a[204:207],v[88:91],v[140:143]
s_lshl_b32 s66,s61,5
v_exp_f32 v100,v100
v_mfma_f32_16x16x32_bf16 v[148:151],a[208:211],v[80:83],v[148:151]
s_lshl_b32 s67,s63,3
ds_read_b128 a[160:163],v1 offset:8192
v_mov_b32 v9,s53
v_mfma_f32_16x16x32_bf16 v[148:151],a[212:215],v[88:91],v[148:151]
s_add_u32 s66,s66,s67
v_exp_f32 v101,v101
v_mfma_f32_16x16x32_bf16 v[156:159],a[216:219],v[80:83],v[156:159]
s_lshl_b32 s66,s66,16
ds_read_b128 a[164:167],v1 offset:8448
v_cvt_pk_bf16_f32 v98,v100,v101
v_mfma_f32_16x16x32_bf16 v[156:159],a[220:223],v[88:91],v[156:159]
s_lshl_b32 s67,s60,14
v_exp_f32 v102,v102
v_mfma_f32_16x16x32_bf16 v[164:167],a[224:227],v[80:83],v[164:167]
s_add_u32 s66,s66,s67
ds_read_b128 a[168:171],v1 offset:8704
v_lshlrev_b32 v10,20,v6
v_mfma_f32_16x16x32_bf16 v[164:167],a[228:231],v[88:91],v[164:167]
s_mov_b32 s30,1523712
v_exp_f32 v103,v103
v_mfma_f32_16x16x32_bf16 v[172:175],a[232:235],v[80:83],v[172:175]
buffer_store_dwordx4 a[116:119],v6,s[40:43],s81 offen offset:1024 sc1
v_mfma_f32_16x16x32_bf16 v[172:175],a[236:239],v[88:91],v[172:175]
s_mov_b32 s31,0x20000
ds_read_b128 a[172:175],v1 offset:8960
v_cvt_pk_bf16_f32 v99,v102,v103
v_mfma_f32_16x16x32_bf16 v[180:183],a[240:243],v[80:83],v[180:183]
buffer_load_dwordx4 v26,s[48:51],s83 offen offset:2048 lds
v_mfma_f32_16x16x32_bf16 v[180:183],a[244:247],v[88:91],v[180:183]
v_exp_f32 v104,v104
v_mfma_f32_16x16x32_bf16 v[188:191],a[248:251],v[80:83],v[188:191]
v_exp_f32 v105,v105
v_mfma_f32_16x16x32_bf16 v[188:191],a[252:255],v[88:91],v[188:191]
ds_read_b128 a[188:191],v1 offset:13056
v_cvt_pk_bf16_f32 v104,v104,v105
v_mfma_f32_16x16x32_bf16 v[52:55],v[28:31],v[80:83],v[52:55]
v_exp_f32 v106,v106
v_mfma_f32_16x16x32_bf16 v[52:55],v[28:31],v[88:91],v[52:55]
s_waitcnt lgkmcnt(2)
v_exp_f32 v107,v107
v_mfma_f32_16x16x32_bf16 v[64:67],a[128:131],v[128:131],v[32:35]
ds_read_b128 a[184:187],v1 offset:12800
v_cvt_pk_bf16_f32 v105,v106,v107
v_mfma_f32_16x16x32_bf16 v[64:67],a[132:135],v[136:139],v[64:67]
v_exp_f32 v108,v108
v_mfma_f32_16x16x32_bf16 v[64:67],a[136:139],v[144:147],v[64:67]
s_add_u32 s28,s12,s66
s_addc_u32 s29,s13,0
ds_read_b128 a[180:183],v1 offset:12544
v_mfma_f32_16x16x32_bf16 v[64:67],a[140:143],v[152:155],v[64:67]
buffer_load_dwordx4 v27,s[48:51],s83 offen offset:3072 lds
v_mfma_f32_16x16x32_bf16 v[68:71],a[156:159],v[152:155],v[32:35]
s_and_b32 s29,s29,0xffff
ds_read_b128 a[176:179],v1 offset:12288
v_add_u32 v1,s78,v1
v_mfma_f32_16x16x32_bf16 v[68:71],a[152:155],v[144:147],v[68:71]
v_exp_f32 v109,v109
v_mfma_f32_16x16x32_bf16 v[68:71],a[148:151],v[136:139],v[68:71]
v_exp_f32 v110,v110
v_mfma_f32_16x16x32_bf16 v[68:71],a[144:147],v[128:131],v[68:71]
v_exp_f32 v111,v111
v_mfma_f32_16x16x32_bf16 v[72:75],a[160:163],v[128:131],v[32:35]
v_cvt_pk_bf16_f32 v106,v108,v109
v_cvt_pk_bf16_f32 v107,v110,v111
v_mfma_f32_16x16x32_bf16 v[72:75],a[164:167],v[136:139],v[72:75]
buffer_store_dwordx4 a[120:123],v14,s[40:43],s82 offen offset:3072 sc1
v_mfma_f32_16x16x32_bf16 v[72:75],a[168:171],v[144:147],v[72:75]
s_waitcnt lgkmcnt(1)
v_exp_f32 v112,v112
v_mfma_f32_16x16x32_bf16 v[72:75],a[172:175],v[152:155],v[72:75]
s_add_u32 m0,s86,126976
v_exp_f32 v113,v113
v_mfma_f32_16x16x32_bf16 v[76:79],a[188:191],v[152:155],v[32:35]
s_waitcnt lgkmcnt(0)
v_exp_f32 v114,v114
s_nop 0
L_pd11_%=:
s_cbranch_execz L_t1c_%=
v_mfma_f32_16x16x32_bf16 v[76:79],a[184:187],v[144:147],v[76:79]
buffer_load_dwordx4 v7,s[48:51],s83 offen lds
v_mfma_f32_16x16x32_bf16 v[76:79],a[180:183],v[136:139],v[76:79]
s_add_u32 s83,s64,0
v_exp_f32 v115,v115
v_mfma_f32_16x16x32_bf16 v[76:79],a[176:179],v[128:131],v[76:79]
s_and_b32 s83,s83,3
v_exp_f32 v116,v116
v_mfma_f32_16x16x32_bf16 v[196:199],a[192:195],v[96:99],v[196:199]
s_lshl_b32 s83,s83,1
v_exp_f32 v117,v117
v_mfma_f32_16x16x32_bf16 v[196:199],a[196:199],v[104:107],v[196:199]
s_or_b32 s83,s83,1
v_exp_f32 v118,v118
v_mfma_f32_16x16x32_bf16 v[204:207],a[200:203],v[96:99],v[204:207]
s_mul_i32 s83,s83,163840
v_exp_f32 v119,v119
v_mfma_f32_16x16x32_bf16 v[204:207],a[204:207],v[104:107],v[204:207]
s_add_u32 s83,s83,s85
v_cvt_pk_bf16_f32 v112,v112,v113
v_cvt_pk_bf16_f32 v113,v114,v115
v_mfma_f32_16x16x32_bf16 v[212:215],a[208:211],v[96:99],v[212:215]
v_cvt_pk_bf16_f32 v114,v116,v117
v_cvt_pk_bf16_f32 v115,v118,v119
v_mfma_f32_16x16x32_bf16 v[212:215],a[212:215],v[104:107],v[212:215]
s_add_u32 m0,s86,128000
v_exp_f32 v120,v120
v_mfma_f32_16x16x32_bf16 v[220:223],a[216:219],v[96:99],v[220:223]
v_exp_f32 v121,v121
v_mfma_f32_16x16x32_bf16 v[220:223],a[220:223],v[104:107],v[220:223]
buffer_load_dwordx4 v24,s[48:51],s84 offen lds
v_mfma_f32_16x16x32_bf16 v[228:231],a[224:227],v[96:99],v[228:231]
)A"
R"A(v_exp_f32 v122,v122
v_mfma_f32_16x16x32_bf16 v[228:231],a[228:231],v[104:107],v[228:231]
v_exp_f32 v123,v123
v_mfma_f32_16x16x32_bf16 v[236:239],a[232:235],v[96:99],v[236:239]
buffer_store_dwordx4 a[124:127],v6,s[40:43],s82 offen offset:1024 sc1
v_mfma_f32_16x16x32_bf16 v[236:239],a[236:239],v[104:107],v[236:239]
v_exp_f32 v124,v124
v_mfma_f32_16x16x32_bf16 v[244:247],a[240:243],v[96:99],v[244:247]
v_exp_f32 v125,v125
v_mfma_f32_16x16x32_bf16 v[244:247],a[244:247],v[104:107],v[244:247]
v_exp_f32 v126,v126
v_mfma_f32_16x16x32_bf16 v[252:255],a[248:251],v[96:99],v[252:255]
v_exp_f32 v127,v127
v_mfma_f32_16x16x32_bf16 v[252:255],a[252:255],v[104:107],v[252:255]
v_cvt_pk_bf16_f32 v120,v120,v121
v_cvt_pk_bf16_f32 v121,v122,v123
v_mfma_f32_16x16x32_bf16 v[56:59],v[28:31],v[96:99],v[56:59]
v_cvt_pk_bf16_f32 v122,v124,v125
v_cvt_pk_bf16_f32 v123,v126,v127
v_mfma_f32_16x16x32_bf16 v[56:59],v[28:31],v[104:107],v[56:59]
buffer_load_dwordx4 v25,s[48:51],s84 offen offset:1024 lds
v_mfma_f32_16x16x32_bf16 v[80:83],a[128:131],v[160:163],v[36:39]
v_exp_f32 v64,v64
v_mfma_f32_16x16x32_bf16 v[80:83],a[132:135],v[168:171],v[80:83]
v_exp_f32 v65,v65
v_mfma_f32_16x16x32_bf16 v[80:83],a[136:139],v[176:179],v[80:83]
v_exp_f32 v66,v66
v_mfma_f32_16x16x32_bf16 v[80:83],a[140:143],v[184:187],v[80:83]
v_exp_f32 v67,v67
v_mfma_f32_16x16x32_bf16 v[84:87],a[156:159],v[184:187],v[36:39]
v_exp_f32 v68,v68
v_mfma_f32_16x16x32_bf16 v[84:87],a[152:155],v[176:179],v[84:87]
v_exp_f32 v69,v69
v_mfma_f32_16x16x32_bf16 v[84:87],a[148:151],v[168:171],v[84:87]
v_exp_f32 v70,v70
v_mfma_f32_16x16x32_bf16 v[84:87],a[144:147],v[160:163],v[84:87]
v_exp_f32 v71,v71
v_mfma_f32_16x16x32_bf16 v[88:91],a[160:163],v[160:163],v[36:39]
v_cvt_pk_bf16_f32 v64,v64,v65
v_cvt_pk_bf16_f32 v65,v66,v67
v_mfma_f32_16x16x32_bf16 v[88:91],a[164:167],v[168:171],v[88:91]
buffer_store_dwordx4 a[96:99],v14,s[40:43],s79 offen offset:3072 sc1
v_mfma_f32_16x16x32_bf16 v[88:91],a[168:171],v[176:179],v[88:91]
v_cvt_pk_bf16_f32 v66,v68,v69
v_cvt_pk_bf16_f32 v67,v70,v71
v_mfma_f32_16x16x32_bf16 v[88:91],a[172:175],v[184:187],v[88:91]
buffer_load_dwordx4 v26,s[48:51],s84 offen offset:2048 lds
v_mfma_f32_16x16x32_bf16 v[92:95],a[188:191],v[184:187],v[36:39]
v_exp_f32 v72,v72
v_mfma_f32_16x16x32_bf16 v[92:95],a[184:187],v[176:179],v[92:95]
v_exp_f32 v73,v73
v_mfma_f32_16x16x32_bf16 v[92:95],a[180:183],v[168:171],v[92:95]
v_exp_f32 v74,v74
v_mfma_f32_16x16x32_bf16 v[92:95],a[176:179],v[160:163],v[92:95]
v_exp_f32 v75,v75
v_mfma_f32_16x16x32_bf16 a[32:35],a[192:195],v[112:115],a[32:35]
ds_read_b64_tr_b16 a[192:193],v2
ds_read_b64_tr_b16 a[194:195],v2 offset:4096
v_mfma_f32_16x16x32_bf16 a[32:35],a[196:199],v[120:123],a[32:35]
ds_read_b64_tr_b16 a[196:197],v2 offset:8192
v_cvt_pk_bf16_f32 v72,v72,v73
v_mfma_f32_16x16x32_bf16 a[36:39],a[200:203],v[112:115],a[36:39]
ds_read_b64_tr_b16 a[198:199],v2 offset:12288
v_cvt_pk_bf16_f32 v73,v74,v75
v_mfma_f32_16x16x32_bf16 a[36:39],a[204:207],v[120:123],a[36:39]
buffer_load_dwordx4 v27,s[48:51],s84 offen offset:3072 lds
v_mfma_f32_16x16x32_bf16 a[40:43],a[208:211],v[112:115],a[40:43]
ds_read_b64_tr_b16 a[200:201],v3
ds_read_b64_tr_b16 a[202:203],v3 offset:4096
v_mfma_f32_16x16x32_bf16 a[40:43],a[212:215],v[120:123],a[40:43]
v_exp_f32 v76,v76
v_mfma_f32_16x16x32_bf16 a[44:47],a[216:219],v[112:115],a[44:47]
ds_read_b64_tr_b16 a[204:205],v3 offset:8192
ds_read_b64_tr_b16 a[206:207],v3 offset:12288
v_mfma_f32_16x16x32_bf16 a[44:47],a[220:223],v[120:123],a[44:47]
v_exp_f32 v77,v77
v_mfma_f32_16x16x32_bf16 a[48:51],a[224:227],v[112:115],a[48:51]
ds_read_b64_tr_b16 a[208:209],v2 offset:256
ds_read_b64_tr_b16 a[210:211],v2 offset:4352
v_mfma_f32_16x16x32_bf16 a[48:51],a[228:231],v[120:123],a[48:51]
ds_read_b64_tr_b16 a[212:213],v2 offset:8448
v_cvt_pk_bf16_f32 v74,v76,v77
v_mfma_f32_16x16x32_bf16 a[52:55],a[232:235],v[112:115],a[52:55]
buffer_store_dwordx4 a[100:103],v6,s[40:43],s79 offen offset:1024 sc1
v_mfma_f32_16x16x32_bf16 a[52:55],a[236:239],v[120:123],a[52:55]
s_add_u32 m0,s86,132096
ds_read_b64_tr_b16 a[214:215],v2 offset:12544
ds_read_b64_tr_b16 a[216:217],v3 offset:256
v_mfma_f32_16x16x32_bf16 a[56:59],a[240:243],v[112:115],a[56:59]
v_exp_f32 v78,v78
v_mfma_f32_16x16x32_bf16 a[56:59],a[244:247],v[120:123],a[56:59]
buffer_load_dwordx4 v7,s[48:51],s84 offen lds
v_mfma_f32_16x16x32_bf16 a[60:63],a[248:251],v[112:115],a[60:63]
s_add_u32 s84,s83,20480
ds_read_b64_tr_b16 a[218:219],v3 offset:4352
ds_read_b64_tr_b16 a[220:221],v3 offset:8448
v_mfma_f32_16x16x32_bf16 a[60:63],a[252:255],v[120:123],a[60:63]
v_exp_f32 v79,v79
v_mfma_f32_16x16x32_bf16 v[60:63],v[28:31],v[112:115],v[60:63]
ds_read_b64_tr_b16 a[222:223],v3 offset:12544
v_cvt_pk_bf16_f32 v75,v78,v79
v_mfma_f32_16x16x32_bf16 v[60:63],v[28:31],v[120:123],v[60:63]
ds_read_b64_tr_b16 a[224:225],v2 offset:512
ds_read_b64_tr_b16 a[226:227],v2 offset:4608
v_mfma_f32_16x16x32_bf16 v[96:99],a[128:131],v[192:195],v[40:43]
v_exp_f32 v80,v80
v_mfma_f32_16x16x32_bf16 v[96:99],a[132:135],v[200:203],v[96:99]
ds_read_b64_tr_b16 a[228:229],v2 offset:8704
ds_read_b64_tr_b16 a[230:231],v2 offset:12800
v_mfma_f32_16x16x32_bf16 v[96:99],a[136:139],v[208:211],v[96:99]
v_exp_f32 v81,v81
v_mfma_f32_16x16x32_bf16 v[96:99],a[140:143],v[216:219],v[96:99]
ds_read_b64_tr_b16 a[232:233],v3 offset:512
ds_read_b64_tr_b16 a[234:235],v3 offset:4608
v_mfma_f32_16x16x32_bf16 v[100:103],a[156:159],v[216:219],v[40:43]
ds_read_b64_tr_b16 a[236:237],v3 offset:8704
v_cvt_pk_bf16_f32 v80,v80,v81
v_mfma_f32_16x16x32_bf16 v[100:103],a[152:155],v[208:211],v[100:103]
v_exp_f32 v82,v82
v_mfma_f32_16x16x32_bf16 v[100:103],a[148:151],v[200:203],v[100:103]
ds_read_b64_tr_b16 a[238:239],v3 offset:12800
ds_read_b64_tr_b16 a[240:241],v2 offset:768
v_mfma_f32_16x16x32_bf16 v[100:103],a[144:147],v[192:195],v[100:103]
v_exp_f32 v83,v83
v_mfma_f32_16x16x32_bf16 v[104:107],a[160:163],v[192:195],v[40:43]
ds_read_b64_tr_b16 a[242:243],v2 offset:4864
ds_read_b64_tr_b16 a[244:245],v2 offset:8960
v_mfma_f32_16x16x32_bf16 v[104:107],a[164:167],v[200:203],v[104:107]
buffer_store_dwordx4 a[104:107],v14,s[40:43],s80 offen offset:3072 sc1
v_mfma_f32_16x16x32_bf16 v[104:107],a[168:171],v[208:211],v[104:107]
ds_read_b64_tr_b16 a[246:247],v2 offset:13056
ds_read_b64_tr_b16 a[248:249],v3 offset:768
v_mfma_f32_16x16x32_bf16 v[104:107],a[172:175],v[216:219],v[104:107]
ds_read_b64_tr_b16 a[250:251],v3 offset:4864
v_cvt_pk_bf16_f32 v81,v82,v83
v_mfma_f32_16x16x32_bf16 v[108:111],a[188:191],v[216:219],v[40:43]
ds_read_b64_tr_b16 a[252:253],v3 offset:8960
v_add_u32 v2,s78,v2
v_mfma_f32_16x16x32_bf16 v[108:111],a[184:187],v[208:211],v[108:111]
s_waitcnt lgkmcnt(6)
ds_read_b64_tr_b16 a[254:255],v3 offset:13056
v_add_u32 v3,s78,v3
v_mfma_f32_16x16x32_bf16 v[108:111],a[180:183],v[200:203],v[108:111]
s_sub_u32 s78,s77,32768
v_exp_f32 v84,v84
v_mfma_f32_16x16x32_bf16 v[108:111],a[176:179],v[192:195],v[108:111]
s_and_b32 s78,s78,0x1ffff
v_exp_f32 v85,v85
v_mfma_f32_16x16x32_bf16 a[0:3],a[192:195],v[64:67],a[0:3]
s_sub_u32 s78,s78,s77
v_exp_f32 v86,v86
v_mfma_f32_16x16x32_bf16 a[0:3],a[196:199],v[72:75],a[0:3]
s_add_u32 s77,s77,s78
v_exp_f32 v87,v87
v_mfma_f32_16x16x32_bf16 a[4:7],a[200:203],v[64:67],a[4:7]
v_cvt_pk_bf16_f32 v82,v84,v85
v_cvt_pk_bf16_f32 v83,v86,v87
v_mfma_f32_16x16x32_bf16 a[4:7],a[204:207],v[72:75],a[4:7]
v_exp_f32 v88,v88
v_mfma_f32_16x16x32_bf16 a[8:11],a[208:211],v[64:67],a[8:11]
v_exp_f32 v89,v89
v_mfma_f32_16x16x32_bf16 a[8:11],a[212:215],v[72:75],a[8:11]
v_exp_f32 v90,v90
v_mfma_f32_16x16x32_bf16 a[12:15],a[216:219],v[64:67],a[12:15]
v_exp_f32 v91,v91
v_mfma_f32_16x16x32_bf16 a[12:15],a[220:223],v[72:75],a[12:15]
v_exp_f32 v92,v92
v_mfma_f32_16x16x32_bf16 a[16:19],a[224:227],v[64:67],a[16:19]
v_exp_f32 v93,v93
v_mfma_f32_16x16x32_bf16 a[16:19],a[228:231],v[72:75],a[16:19]
v_exp_f32 v94,v94
v_mfma_f32_16x16x32_bf16 a[20:23],a[232:235],v[64:67],a[20:23]
buffer_store_dwordx4 a[108:111],v6,s[40:43],s80 offen offset:1024 sc1
v_mfma_f32_16x16x32_bf16 a[20:23],a[236:239],v[72:75],a[20:23]
s_waitcnt lgkmcnt(0)
v_exp_f32 v95,v95
v_mfma_f32_16x16x32_bf16 a[24:27],a[240:243],v[64:67],a[24:27]
v_cvt_pk_bf16_f32 v88,v88,v89
v_cvt_pk_bf16_f32 v89,v90,v91
v_mfma_f32_16x16x32_bf16 a[24:27],a[244:247],v[72:75],a[24:27]
v_cvt_pk_bf16_f32 v90,v92,v93
v_cvt_pk_bf16_f32 v91,v94,v95
v_mfma_f32_16x16x32_bf16 a[28:31],a[248:251],v[64:67],a[28:31]
v_exp_f32 v96,v96
v_mfma_f32_16x16x32_bf16 a[28:31],a[252:255],v[72:75],a[28:31]
v_exp_f32 v97,v97
v_mfma_f32_16x16x32_bf16 v[48:51],v[28:31],v[64:67],v[48:51]
v_exp_f32 v98,v98
v_mfma_f32_16x16x32_bf16 v[48:51],v[28:31],v[72:75],v[48:51]
v_exp_f32 v99,v99
s_waitcnt vmcnt(26)
s_barrier
v_mfma_f32_16x16x32_bf16 v[112:115],a[128:131],v[224:227],v[44:47]
ds_read_b128 a[128:131],v1
v_cvt_pk_bf16_f32 v96,v96,v97
v_mfma_f32_16x16x32_bf16 v[112:115],a[132:135],v[232:235],v[112:115]
v_exp_f32 v100,v100
v_mfma_f32_16x16x32_bf16 v[112:115],a[136:139],v[240:243],v[112:115]
ds_read_b128 a[132:135],v1 offset:256
v_cvt_pk_bf16_f32 v97,v98,v99
v_mfma_f32_16x16x32_bf16 v[112:115],a[140:143],v[248:251],v[112:115]
v_exp_f32 v101,v101
v_mfma_f32_16x16x32_bf16 v[116:119],a[156:159],v[248:251],v[44:47]
ds_read_b128 a[136:139],v1 offset:512
v_cvt_pk_bf16_f32 v98,v100,v101
v_mfma_f32_16x16x32_bf16 v[116:119],a[152:155],v[240:243],v[116:119]
s_add_u32 m0,s86,81920
v_exp_f32 v102,v102
v_mfma_f32_16x16x32_bf16 v[116:119],a[148:151],v[232:235],v[116:119]
v_exp_f32 v103,v103
v_mfma_f32_16x16x32_bf16 v[116:119],a[144:147],v[224:227],v[116:119]
buffer_load_dwordx4 v24,s[48:51],s83 offen lds
v_mfma_f32_16x16x32_bf16 v[120:123],a[160:163],v[224:227],v[44:47]
ds_read_b128 a[140:143],v1 offset:768
v_cvt_pk_bf16_f32 v99,v102,v103
v_mfma_f32_16x16x32_bf16 v[120:123],a[164:167],v[232:235],v[120:123]
buffer_store_dwordx4 a[64:67],v6,s[40:43],s79 offen offset:2048 sc1
v_mfma_f32_16x16x32_bf16 v[120:123],a[168:171],v[240:243],v[120:123]
v_exp_f32 v104,v104
v_mfma_f32_16x16x32_bf16 v[120:123],a[172:175],v[248:251],v[120:123]
v_exp_f32 v105,v105
v_mfma_f32_16x16x32_bf16 v[124:127],a[188:191],v[248:251],v[44:47]
ds_read_b128 a[156:159],v1 offset:4864
v_cvt_pk_bf16_f32 v104,v104,v105
v_mfma_f32_16x16x32_bf16 v[124:127],a[184:187],v[240:243],v[124:127]
v_exp_f32 v106,v106
v_mfma_f32_16x16x32_bf16 v[124:127],a[180:183],v[232:235],v[124:127]
v_exp_f32 v107,v107
v_mfma_f32_16x16x32_bf16 v[124:127],a[176:179],v[224:227],v[124:127]
ds_read_b128 a[152:155],v1 offset:4608
v_cvt_pk_bf16_f32 v105,v106,v107
v_mfma_f32_16x16x32_bf16 v[132:135],a[192:195],v[80:83],v[132:135]
v_exp_f32 v108,v108
v_mfma_f32_16x16x32_bf16 v[132:135],a[196:199],v[88:91],v[132:135]
buffer_load_dwordx4 v25,s[48:51],s83 offen offset:1024 lds
v_mfma_f32_16x16x32_bf16 v[140:143],a[200:203],v[80:83],v[140:143]
v_exp_f32 v109,v109
v_mfma_f32_16x16x32_bf16 v[140:143],a[204:207],v[88:91],v[140:143]
ds_read_b128 a[148:151],v1 offset:4352
v_cvt_pk_bf16_f32 v106,v108,v109
v_mfma_f32_16x16x32_bf16 v[148:151],a[208:211],v[80:83],v[148:151]
v_exp_f32 v110,v110
v_mfma_f32_16x16x32_bf16 v[148:151],a[212:215],v[88:91],v[148:151]
v_exp_f32 v111,v111
v_mfma_f32_16x16x32_bf16 v[156:159],a[216:219],v[80:83],v[156:159]
ds_read_b128 a[144:147],v1 offset:4096
v_cvt_pk_bf16_f32 v107,v110,v111
v_mfma_f32_16x16x32_bf16 v[156:159],a[220:223],v[88:91],v[156:159]
v_exp_f32 v112,v112
v_mfma_f32_16x16x32_bf16 v[164:167],a[224:227],v[80:83],v[164:167]
v_exp_f32 v113,v113
v_mfma_f32_16x16x32_bf16 v[164:167],a[228:231],v[88:91],v[164:167]
buffer_store_dwordx4 a[68:71],v6,s[40:43],s79 offen sc1
v_mfma_f32_16x16x32_bf16 v[172:175],a[232:235],v[80:83],v[172:175]
s_add_u32 s79,s79,65536
ds_read_b128 a[160:163],v1 offset:8192
v_cvt_pk_bf16_f32 v112,v112,v113
v_mfma_f32_16x16x32_bf16 v[172:175],a[236:239],v[88:91],v[172:175]
buffer_load_dwordx4 v26,s[48:51],s83 offen offset:2048 lds
v_mfma_f32_16x16x32_bf16 v[180:183],a[240:243],v[80:83],v[180:183]
v_exp_f32 v114,v114
v_mfma_f32_16x16x32_bf16 v[180:183],a[244:247],v[88:91],v[180:183]
v_exp_f32 v115,v115
v_mfma_f32_16x16x32_bf16 v[188:191],a[248:251],v[80:83],v[188:191]
)A"
R"A(ds_read_b128 a[164:167],v1 offset:8448
v_cvt_pk_bf16_f32 v113,v114,v115
v_mfma_f32_16x16x32_bf16 v[188:191],a[252:255],v[88:91],v[188:191]
v_exp_f32 v116,v116
v_mfma_f32_16x16x32_bf16 v[52:55],v[28:31],v[80:83],v[52:55]
ds_read_b128 a[168:171],v1 offset:8704
v_mfma_f32_16x16x32_bf16 v[52:55],v[28:31],v[88:91],v[52:55]
s_waitcnt lgkmcnt(3)
v_exp_f32 v117,v117
v_mfma_f32_16x16x32_bf16 v[64:67],a[128:131],v[128:131],v[32:35]
ds_read_b128 a[172:175],v1 offset:8960
v_cvt_pk_bf16_f32 v114,v116,v117
v_mfma_f32_16x16x32_bf16 v[64:67],a[132:135],v[136:139],v[64:67]
v_exp_f32 v118,v118
v_mfma_f32_16x16x32_bf16 v[64:67],a[136:139],v[144:147],v[64:67]
ds_read_b128 a[188:191],v1 offset:13056
v_mfma_f32_16x16x32_bf16 v[64:67],a[140:143],v[152:155],v[64:67]
buffer_load_dwordx4 v27,s[48:51],s83 offen offset:3072 lds
v_mfma_f32_16x16x32_bf16 v[68:71],a[156:159],v[152:155],v[32:35]
ds_read_b128 a[184:187],v1 offset:12800
v_mfma_f32_16x16x32_bf16 v[68:71],a[152:155],v[144:147],v[68:71]
v_exp_f32 v119,v119
v_mfma_f32_16x16x32_bf16 v[68:71],a[148:151],v[136:139],v[68:71]
ds_read_b128 a[180:183],v1 offset:12544
v_cvt_pk_bf16_f32 v115,v118,v119
v_mfma_f32_16x16x32_bf16 v[68:71],a[144:147],v[128:131],v[68:71]
s_waitcnt lgkmcnt(4)
v_exp_f32 v120,v120
v_mfma_f32_16x16x32_bf16 v[72:75],a[160:163],v[128:131],v[32:35]
buffer_store_dwordx4 a[72:75],v6,s[40:43],s80 offen offset:2048 sc1
v_mfma_f32_16x16x32_bf16 v[72:75],a[164:167],v[136:139],v[72:75]
ds_read_b128 a[176:179],v1 offset:12288
v_add_u32 v1,s78,v1
v_mfma_f32_16x16x32_bf16 v[72:75],a[168:171],v[144:147],v[72:75]
s_waitcnt lgkmcnt(3)
v_exp_f32 v121,v121
v_mfma_f32_16x16x32_bf16 v[72:75],a[172:175],v[152:155],v[72:75]
s_add_u32 m0,s86,86016
v_exp_f32 v122,v122
v_mfma_f32_16x16x32_bf16 v[76:79],a[188:191],v[152:155],v[32:35]
s_waitcnt lgkmcnt(0)
v_exp_f32 v123,v123
v_mfma_f32_16x16x32_bf16 v[76:79],a[184:187],v[144:147],v[76:79]
buffer_load_dwordx4 v7,s[48:51],s83 offen lds
v_mfma_f32_16x16x32_bf16 v[76:79],a[180:183],v[136:139],v[76:79]
s_add_u32 s83,s64,1
v_exp_f32 v124,v124
v_mfma_f32_16x16x32_bf16 v[76:79],a[176:179],v[128:131],v[76:79]
s_and_b32 s83,s83,3
v_exp_f32 v125,v125
v_mfma_f32_16x16x32_bf16 v[196:199],a[192:195],v[96:99],v[196:199]
s_lshl_b32 s83,s83,1
v_exp_f32 v126,v126
v_mfma_f32_16x16x32_bf16 v[196:199],a[196:199],v[104:107],v[196:199]
s_mul_i32 s83,s83,163840
v_exp_f32 v127,v127
v_mfma_f32_16x16x32_bf16 v[204:207],a[200:203],v[96:99],v[204:207]
s_add_u32 s83,s83,s85
v_cvt_pk_bf16_f32 v120,v120,v121
v_cvt_pk_bf16_f32 v121,v122,v123
v_mfma_f32_16x16x32_bf16 v[204:207],a[204:207],v[104:107],v[204:207]
v_cvt_pk_bf16_f32 v122,v124,v125
v_cvt_pk_bf16_f32 v123,v126,v127
v_mfma_f32_16x16x32_bf16 v[212:215],a[208:211],v[96:99],v[212:215]
v_exp_f32 v64,v64
v_mfma_f32_16x16x32_bf16 v[212:215],a[212:215],v[104:107],v[212:215]
s_add_u32 m0,s86,87040
v_exp_f32 v65,v65
v_mfma_f32_16x16x32_bf16 v[220:223],a[216:219],v[96:99],v[220:223]
v_exp_f32 v66,v66
v_mfma_f32_16x16x32_bf16 v[220:223],a[220:223],v[104:107],v[220:223]
buffer_load_dwordx4 v24,s[48:51],s84 offen lds
v_mfma_f32_16x16x32_bf16 v[228:231],a[224:227],v[96:99],v[228:231]
v_exp_f32 v67,v67
v_mfma_f32_16x16x32_bf16 v[228:231],a[228:231],v[104:107],v[228:231]
buffer_store_dwordx4 a[76:79],v6,s[40:43],s80 offen sc1
v_mfma_f32_16x16x32_bf16 v[236:239],a[232:235],v[96:99],v[236:239]
s_add_u32 s80,s80,65536
v_exp_f32 v68,v68
v_mfma_f32_16x16x32_bf16 v[236:239],a[236:239],v[104:107],v[236:239]
v_exp_f32 v69,v69
v_mfma_f32_16x16x32_bf16 v[244:247],a[240:243],v[96:99],v[244:247]
v_exp_f32 v70,v70
v_mfma_f32_16x16x32_bf16 v[244:247],a[244:247],v[104:107],v[244:247]
v_exp_f32 v71,v71
v_mfma_f32_16x16x32_bf16 v[252:255],a[248:251],v[96:99],v[252:255]
v_cvt_pk_bf16_f32 v64,v64,v65
v_cvt_pk_bf16_f32 v65,v66,v67
v_mfma_f32_16x16x32_bf16 v[252:255],a[252:255],v[104:107],v[252:255]
v_cvt_pk_bf16_f32 v66,v68,v69
v_cvt_pk_bf16_f32 v67,v70,v71
v_mfma_f32_16x16x32_bf16 v[56:59],v[28:31],v[96:99],v[56:59]
v_exp_f32 v72,v72
v_mfma_f32_16x16x32_bf16 v[56:59],v[28:31],v[104:107],v[56:59]
buffer_load_dwordx4 v25,s[48:51],s84 offen offset:1024 lds
v_mfma_f32_16x16x32_bf16 v[80:83],a[128:131],v[160:163],v[36:39]
v_exp_f32 v73,v73
v_mfma_f32_16x16x32_bf16 v[80:83],a[132:135],v[168:171],v[80:83]
v_exp_f32 v74,v74
v_mfma_f32_16x16x32_bf16 v[80:83],a[136:139],v[176:179],v[80:83]
v_exp_f32 v75,v75
v_mfma_f32_16x16x32_bf16 v[80:83],a[140:143],v[184:187],v[80:83]
v_exp_f32 v76,v76
v_mfma_f32_16x16x32_bf16 v[84:87],a[156:159],v[184:187],v[36:39]
v_exp_f32 v77,v77
v_mfma_f32_16x16x32_bf16 v[84:87],a[152:155],v[176:179],v[84:87]
v_exp_f32 v78,v78
v_mfma_f32_16x16x32_bf16 v[84:87],a[148:151],v[168:171],v[84:87]
v_exp_f32 v79,v79
v_mfma_f32_16x16x32_bf16 v[84:87],a[144:147],v[160:163],v[84:87]
v_cvt_pk_bf16_f32 v72,v72,v73
v_cvt_pk_bf16_f32 v73,v74,v75
v_mfma_f32_16x16x32_bf16 v[88:91],a[160:163],v[160:163],v[36:39]
buffer_store_dwordx4 a[80:83],v6,s[40:43],s81 offen offset:2048 sc1
v_mfma_f32_16x16x32_bf16 v[88:91],a[164:167],v[168:171],v[88:91]
v_cvt_pk_bf16_f32 v74,v76,v77
v_cvt_pk_bf16_f32 v75,v78,v79
v_mfma_f32_16x16x32_bf16 v[88:91],a[168:171],v[176:179],v[88:91]
buffer_load_dwordx4 v26,s[48:51],s84 offen offset:2048 lds
v_mfma_f32_16x16x32_bf16 v[88:91],a[172:175],v[184:187],v[88:91]
v_exp_f32 v80,v80
v_mfma_f32_16x16x32_bf16 v[92:95],a[188:191],v[184:187],v[36:39]
v_exp_f32 v81,v81
v_mfma_f32_16x16x32_bf16 v[92:95],a[184:187],v[176:179],v[92:95]
v_exp_f32 v82,v82
v_mfma_f32_16x16x32_bf16 v[92:95],a[180:183],v[168:171],v[92:95]
v_exp_f32 v83,v83
v_mfma_f32_16x16x32_bf16 v[92:95],a[176:179],v[160:163],v[92:95]
v_exp_f32 v84,v84
v_mfma_f32_16x16x32_bf16 a[32:35],a[192:195],v[112:115],a[32:35]
ds_read_b64_tr_b16 a[192:193],v2
ds_read_b64_tr_b16 a[194:195],v2 offset:4096
v_mfma_f32_16x16x32_bf16 a[32:35],a[196:199],v[120:123],a[32:35]
ds_read_b64_tr_b16 a[196:197],v2 offset:8192
v_cvt_pk_bf16_f32 v80,v80,v81
v_mfma_f32_16x16x32_bf16 a[36:39],a[200:203],v[112:115],a[36:39]
ds_read_b64_tr_b16 a[198:199],v2 offset:12288
v_cvt_pk_bf16_f32 v81,v82,v83
v_mfma_f32_16x16x32_bf16 a[36:39],a[204:207],v[120:123],a[36:39]
buffer_load_dwordx4 v27,s[48:51],s84 offen offset:3072 lds
v_mfma_f32_16x16x32_bf16 a[40:43],a[208:211],v[112:115],a[40:43]
ds_read_b64_tr_b16 a[200:201],v3
ds_read_b64_tr_b16 a[202:203],v3 offset:4096
v_mfma_f32_16x16x32_bf16 a[40:43],a[212:215],v[120:123],a[40:43]
v_exp_f32 v85,v85
v_mfma_f32_16x16x32_bf16 a[44:47],a[216:219],v[112:115],a[44:47]
ds_read_b64_tr_b16 a[204:205],v3 offset:8192
ds_read_b64_tr_b16 a[206:207],v3 offset:12288
v_mfma_f32_16x16x32_bf16 a[44:47],a[220:223],v[120:123],a[44:47]
ds_read_b64_tr_b16 a[208:209],v2 offset:256
v_cvt_pk_bf16_f32 v82,v84,v85
v_mfma_f32_16x16x32_bf16 a[48:51],a[224:227],v[112:115],a[48:51]
v_exp_f32 v86,v86
v_mfma_f32_16x16x32_bf16 a[48:51],a[228:231],v[120:123],a[48:51]
buffer_store_dwordx4 a[84:87],v6,s[40:43],s81 offen sc1
v_mfma_f32_16x16x32_bf16 a[52:55],a[232:235],v[112:115],a[52:55]
s_add_u32 s81,s81,65536
ds_read_b64_tr_b16 a[210:211],v2 offset:4352
ds_read_b64_tr_b16 a[212:213],v2 offset:8448
v_mfma_f32_16x16x32_bf16 a[52:55],a[236:239],v[120:123],a[52:55]
s_add_u32 m0,s86,91136
v_exp_f32 v87,v87
v_mfma_f32_16x16x32_bf16 a[56:59],a[240:243],v[112:115],a[56:59]
ds_read_b64_tr_b16 a[214:215],v2 offset:12544
ds_read_b64_tr_b16 a[216:217],v3 offset:256
v_mfma_f32_16x16x32_bf16 a[56:59],a[244:247],v[120:123],a[56:59]
buffer_load_dwordx4 v7,s[48:51],s84 offen lds
v_mfma_f32_16x16x32_bf16 a[60:63],a[248:251],v[112:115],a[60:63]
s_add_u32 s84,s83,20480
ds_read_b64_tr_b16 a[218:219],v3 offset:4352
ds_read_b64_tr_b16 a[220:221],v3 offset:8448
v_mfma_f32_16x16x32_bf16 a[60:63],a[252:255],v[120:123],a[60:63]
ds_read_b64_tr_b16 a[222:223],v3 offset:12544
v_cvt_pk_bf16_f32 v83,v86,v87
v_mfma_f32_16x16x32_bf16 v[60:63],v[28:31],v[112:115],v[60:63]
v_exp_f32 v88,v88
v_mfma_f32_16x16x32_bf16 v[60:63],v[28:31],v[120:123],v[60:63]
ds_read_b64_tr_b16 a[224:225],v2 offset:512
ds_read_b64_tr_b16 a[226:227],v2 offset:4608
v_mfma_f32_16x16x32_bf16 v[96:99],a[128:131],v[192:195],v[40:43]
v_exp_f32 v89,v89
v_mfma_f32_16x16x32_bf16 v[96:99],a[132:135],v[200:203],v[96:99]
ds_read_b64_tr_b16 a[228:229],v2 offset:8704
ds_read_b64_tr_b16 a[230:231],v2 offset:12800
v_mfma_f32_16x16x32_bf16 v[96:99],a[136:139],v[208:211],v[96:99]
ds_read_b64_tr_b16 a[232:233],v3 offset:512
v_cvt_pk_bf16_f32 v88,v88,v89
v_mfma_f32_16x16x32_bf16 v[96:99],a[140:143],v[216:219],v[96:99]
v_exp_f32 v90,v90
v_mfma_f32_16x16x32_bf16 v[100:103],a[156:159],v[216:219],v[40:43]
ds_read_b64_tr_b16 a[234:235],v3 offset:4608
ds_read_b64_tr_b16 a[236:237],v3 offset:8704
v_mfma_f32_16x16x32_bf16 v[100:103],a[152:155],v[208:211],v[100:103]
v_exp_f32 v91,v91
v_mfma_f32_16x16x32_bf16 v[100:103],a[148:151],v[200:203],v[100:103]
ds_read_b64_tr_b16 a[238:239],v3 offset:12800
ds_read_b64_tr_b16 a[240:241],v2 offset:768
v_mfma_f32_16x16x32_bf16 v[100:103],a[144:147],v[192:195],v[100:103]
ds_read_b64_tr_b16 a[242:243],v2 offset:4864
v_cvt_pk_bf16_f32 v89,v90,v91
v_mfma_f32_16x16x32_bf16 v[104:107],a[160:163],v[192:195],v[40:43]
buffer_store_dwordx4 a[88:91],v6,s[40:43],s82 offen offset:2048 sc1
v_mfma_f32_16x16x32_bf16 v[104:107],a[164:167],v[200:203],v[104:107]
ds_read_b64_tr_b16 a[244:245],v2 offset:8960
ds_read_b64_tr_b16 a[246:247],v2 offset:13056
v_mfma_f32_16x16x32_bf16 v[104:107],a[168:171],v[208:211],v[104:107]
ds_read_b64_tr_b16 a[248:249],v3 offset:768
v_add_u32 v2,s78,v2
v_mfma_f32_16x16x32_bf16 v[104:107],a[172:175],v[216:219],v[104:107]
v_exp_f32 v92,v92
v_mfma_f32_16x16x32_bf16 v[108:111],a[188:191],v[216:219],v[40:43]
ds_read_b64_tr_b16 a[250:251],v3 offset:4864
ds_read_b64_tr_b16 a[252:253],v3 offset:8960
v_mfma_f32_16x16x32_bf16 v[108:111],a[184:187],v[208:211],v[108:111]
s_waitcnt lgkmcnt(5)
ds_read_b64_tr_b16 a[254:255],v3 offset:13056
v_add_u32 v3,s78,v3
v_mfma_f32_16x16x32_bf16 v[108:111],a[180:183],v[200:203],v[108:111]
s_sub_u32 s78,s77,32768
v_exp_f32 v93,v93
v_mfma_f32_16x16x32_bf16 v[108:111],a[176:179],v[192:195],v[108:111]
s_and_b32 s78,s78,0x1ffff
v_exp_f32 v94,v94
v_mfma_f32_16x16x32_bf16 a[0:3],a[192:195],v[64:67],a[0:3]
s_sub_u32 s78,s78,s77
v_exp_f32 v95,v95
v_mfma_f32_16x16x32_bf16 a[0:3],a[196:199],v[72:75],a[0:3]
s_add_u32 s77,s77,s78
v_cvt_pk_bf16_f32 v90,v92,v93
v_cvt_pk_bf16_f32 v91,v94,v95
v_mfma_f32_16x16x32_bf16 a[4:7],a[200:203],v[64:67],a[4:7]
s_cmp_ge_u32 s63,2
s_cselect_b32 s6,L_gbS_%=-L_pcg_%=,s6
s_cselect_b32 s10,L_geS_%=-L_gbS_%=,s10
v_mfma_f32_16x16x32_bf16 a[4:7],a[204:207],v[72:75],a[4:7]
s_add_u32 s8,s8,s6
s_addc_u32 s9,s9,0
s_and_b32 s8,s8,0xffffff80
v_mfma_f32_16x16x32_bf16 a[8:11],a[208:211],v[64:67],a[8:11]
s_and_b32 s9,s9,0xffff
v_exp_f32 v96,v96
v_mfma_f32_16x16x32_bf16 a[8:11],a[212:215],v[72:75],a[8:11]
v_exp_f32 v97,v97
v_mfma_f32_16x16x32_bf16 a[12:15],a[216:219],v[64:67],a[12:15]
v_exp_f32 v98,v98
v_mfma_f32_16x16x32_bf16 a[12:15],a[220:223],v[72:75],a[12:15]
v_exp_f32 v99,v99
v_mfma_f32_16x16x32_bf16 a[16:19],a[224:227],v[64:67],a[16:19]
v_exp_f32 v100,v100
v_mfma_f32_16x16x32_bf16 a[16:19],a[228:231],v[72:75],a[16:19]
buffer_store_dwordx4 a[92:95],v6,s[40:43],s82 offen sc1
v_mfma_f32_16x16x32_bf16 a[20:23],a[232:235],v[64:67],a[20:23]
s_add_u32 s82,s82,65536
v_exp_f32 v101,v101
v_mfma_f32_16x16x32_bf16 a[20:23],a[236:239],v[72:75],a[20:23]
v_exp_f32 v102,v102
v_mfma_f32_16x16x32_bf16 a[24:27],a[240:243],v[64:67],a[24:27]
s_waitcnt lgkmcnt(0)
v_exp_f32 v103,v103
v_mfma_f32_16x16x32_bf16 a[24:27],a[244:247],v[72:75],a[24:27]
v_cvt_pk_bf16_f32 v96,v96,v97
v_cvt_pk_bf16_f32 v97,v98,v99
v_mfma_f32_16x16x32_bf16 a[28:31],a[248:251],v[64:67],a[28:31]
v_cvt_pk_bf16_f32 v98,v100,v101
v_cvt_pk_bf16_f32 v99,v102,v103
v_mfma_f32_16x16x32_bf16 a[28:31],a[252:255],v[72:75],a[28:31]
v_exp_f32 v104,v104
v_mfma_f32_16x16x32_bf16 v[48:51],v[28:31],v[64:67],v[48:51]
v_exp_f32 v105,v105
v_mfma_f32_16x16x32_bf16 v[48:51],v[28:31],v[72:75],v[48:51]
v_exp_f32 v106,v106
s_waitcnt vmcnt(36)
s_barrier
v_mfma_f32_16x16x32_bf16 v[112:115],a[128:131],v[224:227],v[44:47]
ds_read_b128 a[128:131],v1
)A"
R"A(v_cvt_pk_bf16_f32 v104,v104,v105
v_mfma_f32_16x16x32_bf16 v[112:115],a[132:135],v[232:235],v[112:115]
v_exp_f32 v107,v107
v_mfma_f32_16x16x32_bf16 v[112:115],a[136:139],v[240:243],v[112:115]
ds_read_b128 a[132:135],v1 offset:256
v_cvt_pk_bf16_f32 v105,v106,v107
v_mfma_f32_16x16x32_bf16 v[112:115],a[140:143],v[248:251],v[112:115]
v_exp_f32 v108,v108
v_mfma_f32_16x16x32_bf16 v[116:119],a[156:159],v[248:251],v[44:47]
v_exp_f32 v109,v109
v_mfma_f32_16x16x32_bf16 v[116:119],a[152:155],v[240:243],v[116:119]
s_add_u32 m0,s86,40960
ds_read_b128 a[136:139],v1 offset:512
v_cvt_pk_bf16_f32 v106,v108,v109
v_mfma_f32_16x16x32_bf16 v[116:119],a[148:151],v[232:235],v[116:119]
v_exp_f32 v110,v110
v_mfma_f32_16x16x32_bf16 v[116:119],a[144:147],v[224:227],v[116:119]
buffer_load_dwordx4 v24,s[48:51],s83 offen lds
v_mfma_f32_16x16x32_bf16 v[120:123],a[160:163],v[224:227],v[44:47]
L_pd12_%=:
s_cbranch_execz L_trt1_%=
v_exp_f32 v111,v111
v_mfma_f32_16x16x32_bf16 v[120:123],a[164:167],v[232:235],v[120:123]
ds_read_b128 a[140:143],v1 offset:768
v_cvt_pk_bf16_f32 v107,v110,v111
v_mfma_f32_16x16x32_bf16 v[120:123],a[168:171],v[240:243],v[120:123]
v_exp_f32 v112,v112
v_mfma_f32_16x16x32_bf16 v[120:123],a[172:175],v[248:251],v[120:123]
v_exp_f32 v113,v113
v_mfma_f32_16x16x32_bf16 v[124:127],a[188:191],v[248:251],v[44:47]
ds_read_b128 a[156:159],v1 offset:4864
v_cvt_pk_bf16_f32 v112,v112,v113
v_mfma_f32_16x16x32_bf16 v[124:127],a[184:187],v[240:243],v[124:127]
v_exp_f32 v114,v114
v_mfma_f32_16x16x32_bf16 v[124:127],a[180:183],v[232:235],v[124:127]
v_exp_f32 v115,v115
v_mfma_f32_16x16x32_bf16 v[124:127],a[176:179],v[224:227],v[124:127]
ds_read_b128 a[152:155],v1 offset:4608
v_cvt_pk_bf16_f32 v113,v114,v115
v_mfma_f32_16x16x32_bf16 v[132:135],a[192:195],v[80:83],v[132:135]
v_exp_f32 v116,v116
v_mfma_f32_16x16x32_bf16 v[132:135],a[196:199],v[88:91],v[132:135]
buffer_load_dwordx4 v25,s[48:51],s83 offen offset:1024 lds
v_mfma_f32_16x16x32_bf16 v[140:143],a[200:203],v[80:83],v[140:143]
v_exp_f32 v117,v117
v_mfma_f32_16x16x32_bf16 v[140:143],a[204:207],v[88:91],v[140:143]
ds_read_b128 a[148:151],v1 offset:4352
v_cvt_pk_bf16_f32 v114,v116,v117
v_mfma_f32_16x16x32_bf16 v[148:151],a[208:211],v[80:83],v[148:151]
v_exp_f32 v118,v118
v_mfma_f32_16x16x32_bf16 v[148:151],a[212:215],v[88:91],v[148:151]
v_exp_f32 v119,v119
v_mfma_f32_16x16x32_bf16 v[156:159],a[216:219],v[80:83],v[156:159]
ds_read_b128 a[144:147],v1 offset:4096
v_cvt_pk_bf16_f32 v115,v118,v119
v_mfma_f32_16x16x32_bf16 v[156:159],a[220:223],v[88:91],v[156:159]
v_exp_f32 v120,v120
v_mfma_f32_16x16x32_bf16 v[164:167],a[224:227],v[80:83],v[164:167]
buffer_load_dword v5,v4,s[8:11],s7 offen
v_mfma_f32_16x16x32_bf16 v[164:167],a[228:231],v[88:91],v[164:167]
v_exp_f32 v121,v121
v_mfma_f32_16x16x32_bf16 v[172:175],a[232:235],v[80:83],v[172:175]
ds_read_b128 a[160:163],v1 offset:8192
v_cvt_pk_bf16_f32 v120,v120,v121
v_mfma_f32_16x16x32_bf16 v[172:175],a[236:239],v[88:91],v[172:175]
v_exp_f32 v122,v122
v_mfma_f32_16x16x32_bf16 v[180:183],a[240:243],v[80:83],v[180:183]
v_exp_f32 v123,v123
v_mfma_f32_16x16x32_bf16 v[180:183],a[244:247],v[88:91],v[180:183]
ds_read_b128 a[164:167],v1 offset:8448
v_cvt_pk_bf16_f32 v121,v122,v123
v_mfma_f32_16x16x32_bf16 v[188:191],a[248:251],v[80:83],v[188:191]
buffer_load_dwordx4 v26,s[48:51],s83 offen offset:2048 lds
v_mfma_f32_16x16x32_bf16 v[188:191],a[252:255],v[88:91],v[188:191]
v_exp_f32 v124,v124
v_mfma_f32_16x16x32_bf16 v[52:55],v[28:31],v[80:83],v[52:55]
ds_read_b128 a[168:171],v1 offset:8704
v_mfma_f32_16x16x32_bf16 v[52:55],v[28:31],v[88:91],v[52:55]
s_waitcnt lgkmcnt(3)
v_exp_f32 v125,v125
v_mfma_f32_16x16x32_bf16 v[64:67],a[128:131],v[128:131],v[32:35]
ds_read_b128 a[172:175],v1 offset:8960
v_cvt_pk_bf16_f32 v122,v124,v125
v_mfma_f32_16x16x32_bf16 v[64:67],a[132:135],v[136:139],v[64:67]
v_exp_f32 v126,v126
v_mfma_f32_16x16x32_bf16 v[64:67],a[136:139],v[144:147],v[64:67]
ds_read_b128 a[188:191],v1 offset:13056
v_mfma_f32_16x16x32_bf16 v[64:67],a[140:143],v[152:155],v[64:67]
buffer_load_dwordx4 v27,s[48:51],s83 offen offset:3072 lds
v_mfma_f32_16x16x32_bf16 v[68:71],a[156:159],v[152:155],v[32:35]
ds_read_b128 a[184:187],v1 offset:12800
v_mfma_f32_16x16x32_bf16 v[68:71],a[152:155],v[144:147],v[68:71]
v_exp_f32 v127,v127
v_mfma_f32_16x16x32_bf16 v[68:71],a[148:151],v[136:139],v[68:71]
ds_read_b128 a[180:183],v1 offset:12544
v_cvt_pk_bf16_f32 v123,v126,v127
v_mfma_f32_16x16x32_bf16 v[68:71],a[144:147],v[128:131],v[68:71]
s_waitcnt lgkmcnt(4)
v_exp_f32 v64,v64
v_mfma_f32_16x16x32_bf16 v[72:75],a[160:163],v[128:131],v[32:35]
ds_read_b128 a[176:179],v1 offset:12288
v_add_u32 v1,s78,v1
v_mfma_f32_16x16x32_bf16 v[72:75],a[164:167],v[136:139],v[72:75]
v_exp_f32 v65,v65
v_mfma_f32_16x16x32_bf16 v[72:75],a[168:171],v[144:147],v[72:75]
s_waitcnt lgkmcnt(3)
v_exp_f32 v66,v66
v_mfma_f32_16x16x32_bf16 v[72:75],a[172:175],v[152:155],v[72:75]
s_add_u32 m0,s86,45056
v_exp_f32 v67,v67
v_mfma_f32_16x16x32_bf16 v[76:79],a[188:191],v[152:155],v[32:35]
s_waitcnt lgkmcnt(0)
v_exp_f32 v68,v68
v_mfma_f32_16x16x32_bf16 v[76:79],a[184:187],v[144:147],v[76:79]
buffer_load_dwordx4 v7,s[48:51],s83 offen lds
v_mfma_f32_16x16x32_bf16 v[76:79],a[180:183],v[136:139],v[76:79]
s_add_u32 s83,s64,2
v_exp_f32 v69,v69
v_mfma_f32_16x16x32_bf16 v[76:79],a[176:179],v[128:131],v[76:79]
s_and_b32 s83,s83,3
v_exp_f32 v70,v70
v_mfma_f32_16x16x32_bf16 v[196:199],a[192:195],v[96:99],v[196:199]
s_lshl_b32 s83,s83,1
v_exp_f32 v71,v71
v_mfma_f32_16x16x32_bf16 v[196:199],a[196:199],v[104:107],v[196:199]
s_mul_i32 s83,s83,163840
v_cvt_pk_bf16_f32 v64,v64,v65
v_cvt_pk_bf16_f32 v65,v66,v67
v_mfma_f32_16x16x32_bf16 v[204:207],a[200:203],v[96:99],v[204:207]
s_add_u32 s83,s83,s85
v_cvt_pk_bf16_f32 v66,v68,v69
v_cvt_pk_bf16_f32 v67,v70,v71
v_mfma_f32_16x16x32_bf16 v[204:207],a[204:207],v[104:107],v[204:207]
v_exp_f32 v72,v72
v_mfma_f32_16x16x32_bf16 v[212:215],a[208:211],v[96:99],v[212:215]
v_exp_f32 v73,v73
v_mfma_f32_16x16x32_bf16 v[212:215],a[212:215],v[104:107],v[212:215]
s_add_u32 m0,s86,46080
v_exp_f32 v74,v74
v_mfma_f32_16x16x32_bf16 v[220:223],a[216:219],v[96:99],v[220:223]
v_exp_f32 v75,v75
v_mfma_f32_16x16x32_bf16 v[220:223],a[220:223],v[104:107],v[220:223]
buffer_load_dwordx4 v24,s[48:51],s84 offen lds
v_mfma_f32_16x16x32_bf16 v[228:231],a[224:227],v[96:99],v[228:231]
v_exp_f32 v76,v76
v_mfma_f32_16x16x32_bf16 v[228:231],a[228:231],v[104:107],v[228:231]
v_exp_f32 v77,v77
v_mfma_f32_16x16x32_bf16 v[236:239],a[232:235],v[96:99],v[236:239]
v_exp_f32 v78,v78
v_mfma_f32_16x16x32_bf16 v[236:239],a[236:239],v[104:107],v[236:239]
v_exp_f32 v79,v79
v_mfma_f32_16x16x32_bf16 v[244:247],a[240:243],v[96:99],v[244:247]
v_cvt_pk_bf16_f32 v72,v72,v73
v_cvt_pk_bf16_f32 v73,v74,v75
v_mfma_f32_16x16x32_bf16 v[244:247],a[244:247],v[104:107],v[244:247]
v_cvt_pk_bf16_f32 v74,v76,v77
v_cvt_pk_bf16_f32 v75,v78,v79
v_mfma_f32_16x16x32_bf16 v[252:255],a[248:251],v[96:99],v[252:255]
v_lshrrev_b32 v1,6,v6
v_mul_u32_u24 v1,320,v1
v_mfma_f32_16x16x32_bf16 v[252:255],a[252:255],v[104:107],v[252:255]
v_mfma_f32_16x16x32_bf16 v[56:59],v[28:31],v[96:99],v[56:59]
v_mfma_f32_16x16x32_bf16 v[56:59],v[28:31],v[104:107],v[56:59]
buffer_load_dwordx4 v25,s[48:51],s84 offen offset:1024 lds
v_mfma_f32_16x16x32_bf16 v[80:83],a[128:131],v[160:163],v[36:39]
v_mfma_f32_16x16x32_bf16 v[80:83],a[132:135],v[168:171],v[80:83]
v_mfma_f32_16x16x32_bf16 v[80:83],a[136:139],v[176:179],v[80:83]
v_mfma_f32_16x16x32_bf16 v[80:83],a[140:143],v[184:187],v[80:83]
v_mfma_f32_16x16x32_bf16 v[84:87],a[156:159],v[184:187],v[36:39]
v_mfma_f32_16x16x32_bf16 v[84:87],a[152:155],v[176:179],v[84:87]
v_mfma_f32_16x16x32_bf16 v[84:87],a[148:151],v[168:171],v[84:87]
v_mfma_f32_16x16x32_bf16 v[84:87],a[144:147],v[160:163],v[84:87]
v_mfma_f32_16x16x32_bf16 v[88:91],a[160:163],v[160:163],v[36:39]
v_mfma_f32_16x16x32_bf16 v[88:91],a[164:167],v[168:171],v[88:91]
buffer_load_dwordx4 v26,s[48:51],s84 offen offset:2048 lds
v_mfma_f32_16x16x32_bf16 v[88:91],a[168:171],v[176:179],v[88:91]
v_exp_f32 v80,v80
v_mfma_f32_16x16x32_bf16 v[88:91],a[172:175],v[184:187],v[88:91]
v_exp_f32 v81,v81
v_mfma_f32_16x16x32_bf16 v[92:95],a[188:191],v[184:187],v[36:39]
v_exp_f32 v82,v82
v_mfma_f32_16x16x32_bf16 v[92:95],a[184:187],v[176:179],v[92:95]
v_exp_f32 v83,v83
v_mfma_f32_16x16x32_bf16 v[92:95],a[180:183],v[168:171],v[92:95]
v_exp_f32 v84,v84
v_mfma_f32_16x16x32_bf16 v[92:95],a[176:179],v[160:163],v[92:95]
v_exp_f32 v85,v85
v_mfma_f32_16x16x32_bf16 a[32:35],a[192:195],v[112:115],a[32:35]
ds_read_b64_tr_b16 a[192:193],v2
ds_read_b64_tr_b16 a[194:195],v2 offset:4096
v_mfma_f32_16x16x32_bf16 a[32:35],a[196:199],v[120:123],a[32:35]
ds_read_b64_tr_b16 a[196:197],v2 offset:8192
v_cvt_pk_bf16_f32 v80,v80,v81
v_mfma_f32_16x16x32_bf16 a[36:39],a[200:203],v[112:115],a[36:39]
ds_read_b64_tr_b16 a[198:199],v2 offset:12288
v_cvt_pk_bf16_f32 v81,v82,v83
v_mfma_f32_16x16x32_bf16 a[36:39],a[204:207],v[120:123],a[36:39]
buffer_load_dwordx4 v27,s[48:51],s84 offen offset:3072 lds
v_mfma_f32_16x16x32_bf16 a[40:43],a[208:211],v[112:115],a[40:43]
ds_read_b64_tr_b16 a[200:201],v3
ds_read_b64_tr_b16 a[202:203],v3 offset:4096
v_mfma_f32_16x16x32_bf16 a[40:43],a[212:215],v[120:123],a[40:43]
ds_read_b64_tr_b16 a[204:205],v3 offset:8192
v_cvt_pk_bf16_f32 v82,v84,v85
v_mfma_f32_16x16x32_bf16 a[44:47],a[216:219],v[112:115],a[44:47]
v_exp_f32 v86,v86
v_mfma_f32_16x16x32_bf16 a[44:47],a[220:223],v[120:123],a[44:47]
ds_read_b64_tr_b16 a[206:207],v3 offset:12288
ds_read_b64_tr_b16 a[208:209],v2 offset:256
v_mfma_f32_16x16x32_bf16 a[48:51],a[224:227],v[112:115],a[48:51]
v_exp_f32 v87,v87
v_mfma_f32_16x16x32_bf16 a[48:51],a[228:231],v[120:123],a[48:51]
ds_read_b64_tr_b16 a[210:211],v2 offset:4352
ds_read_b64_tr_b16 a[212:213],v2 offset:8448
v_mfma_f32_16x16x32_bf16 a[52:55],a[232:235],v[112:115],a[52:55]
ds_read_b64_tr_b16 a[214:215],v2 offset:12544
v_cvt_pk_bf16_f32 v83,v86,v87
v_mfma_f32_16x16x32_bf16 a[52:55],a[236:239],v[120:123],a[52:55]
s_add_u32 m0,s86,50176
v_exp_f32 v88,v88
v_mfma_f32_16x16x32_bf16 a[56:59],a[240:243],v[112:115],a[56:59]
ds_read_b64_tr_b16 a[216:217],v3 offset:256
ds_read_b64_tr_b16 a[218:219],v3 offset:4352
v_mfma_f32_16x16x32_bf16 a[56:59],a[244:247],v[120:123],a[56:59]
buffer_load_dwordx4 v7,s[48:51],s84 offen lds
v_mfma_f32_16x16x32_bf16 a[60:63],a[248:251],v[112:115],a[60:63]
s_add_u32 s84,s83,20480
ds_read_b64_tr_b16 a[220:221],v3 offset:8448
ds_read_b64_tr_b16 a[222:223],v3 offset:12544
v_mfma_f32_16x16x32_bf16 a[60:63],a[252:255],v[120:123],a[60:63]
v_exp_f32 v89,v89
v_mfma_f32_16x16x32_bf16 v[60:63],v[28:31],v[112:115],v[60:63]
ds_read_b64_tr_b16 a[224:225],v2 offset:512
ds_read_b64_tr_b16 a[226:227],v2 offset:4608
v_mfma_f32_16x16x32_bf16 v[60:63],v[28:31],v[120:123],v[60:63]
ds_read_b64_tr_b16 a[228:229],v2 offset:8704
v_cvt_pk_bf16_f32 v88,v88,v89
v_mfma_f32_16x16x32_bf16 v[96:99],a[128:131],v[192:195],v[40:43]
v_exp_f32 v90,v90
v_mfma_f32_16x16x32_bf16 v[96:99],a[132:135],v[200:203],v[96:99]
ds_read_b64_tr_b16 a[230:231],v2 offset:12800
ds_read_b64_tr_b16 a[232:233],v3 offset:512
v_mfma_f32_16x16x32_bf16 v[96:99],a[136:139],v[208:211],v[96:99]
v_exp_f32 v91,v91
v_mfma_f32_16x16x32_bf16 v[96:99],a[140:143],v[216:219],v[96:99]
ds_read_b64_tr_b16 a[234:235],v3 offset:4608
ds_read_b64_tr_b16 a[236:237],v3 offset:8704
v_mfma_f32_16x16x32_bf16 v[100:103],a[156:159],v[216:219],v[40:43]
ds_read_b64_tr_b16 a[238:239],v3 offset:12800
v_cvt_pk_bf16_f32 v89,v90,v91
v_mfma_f32_16x16x32_bf16 v[100:103],a[152:155],v[208:211],v[100:103]
v_exp_f32 v92,v92
v_mfma_f32_16x16x32_bf16 v[100:103],a[148:151],v[200:203],v[100:103]
ds_read_b64_tr_b16 a[240:241],v2 offset:768
ds_read_b64_tr_b16 a[242:243],v2 offset:4864
v_mfma_f32_16x16x32_bf16 v[100:103],a[144:147],v[192:195],v[100:103]
v_exp_f32 v93,v93
v_mfma_f32_16x16x32_bf16 v[104:107],a[160:163],v[192:195],v[40:43]
ds_read_b64_tr_b16 a[244:245],v2 offset:8960
ds_read_b64_tr_b16 a[246:247],v2 offset:13056
v_mfma_f32_16x16x32_bf16 v[104:107],a[164:167],v[200:203],v[104:107]
ds_read_b64_tr_b16 a[248:249],v3 offset:768
v_cvt_pk_bf16_f32 v90,v92,v93
)A"
R"A(v_mfma_f32_16x16x32_bf16 v[104:107],a[168:171],v[208:211],v[104:107]
ds_read_b64_tr_b16 a[250:251],v3 offset:4864
v_bfe_u32 v2,v6,4,2
v_mfma_f32_16x16x32_bf16 v[104:107],a[172:175],v[216:219],v[104:107]
ds_read_b64_tr_b16 a[252:253],v3 offset:8960
v_lshl_add_u32 v1,v2,3,v1
v_mfma_f32_16x16x32_bf16 v[108:111],a[188:191],v[216:219],v[40:43]
ds_read_b64_tr_b16 a[254:255],v3 offset:13056
v_add_u32 v1,122880,v1
v_mfma_f32_16x16x32_bf16 v[108:111],a[184:187],v[208:211],v[108:111]
v_exp_f32 v94,v94
v_mfma_f32_16x16x32_bf16 v[108:111],a[180:183],v[200:203],v[108:111]
v_exp_f32 v95,v95
v_mfma_f32_16x16x32_bf16 v[108:111],a[176:179],v[192:195],v[108:111]
s_waitcnt lgkmcnt(6)
v_cvt_pk_bf16_f32 v91,v94,v95
v_bfe_u32 v2,v6,8,1
v_mfma_f32_16x16x32_bf16 a[0:3],a[192:195],v[64:67],a[0:3]
v_exp_f32 v96,v96
v_mfma_f32_16x16x32_bf16 a[0:3],a[196:199],v[72:75],a[0:3]
v_exp_f32 v97,v97
v_mfma_f32_16x16x32_bf16 a[4:7],a[200:203],v[64:67],a[4:7]
v_exp_f32 v98,v98
v_mfma_f32_16x16x32_bf16 a[4:7],a[204:207],v[72:75],a[4:7]
v_exp_f32 v99,v99
v_mfma_f32_16x16x32_bf16 a[8:11],a[208:211],v[64:67],a[8:11]
v_exp_f32 v100,v100
v_mfma_f32_16x16x32_bf16 a[8:11],a[212:215],v[72:75],a[8:11]
v_exp_f32 v101,v101
v_mfma_f32_16x16x32_bf16 a[12:15],a[216:219],v[64:67],a[12:15]
v_exp_f32 v102,v102
v_mfma_f32_16x16x32_bf16 a[12:15],a[220:223],v[72:75],a[12:15]
v_exp_f32 v103,v103
v_mfma_f32_16x16x32_bf16 a[16:19],a[224:227],v[64:67],a[16:19]
v_cvt_pk_bf16_f32 v96,v96,v97
v_cvt_pk_bf16_f32 v97,v98,v99
v_mfma_f32_16x16x32_bf16 a[16:19],a[228:231],v[72:75],a[16:19]
v_cvt_pk_bf16_f32 v98,v100,v101
v_cvt_pk_bf16_f32 v99,v102,v103
v_mfma_f32_16x16x32_bf16 a[20:23],a[232:235],v[64:67],a[20:23]
v_exp_f32 v104,v104
v_mfma_f32_16x16x32_bf16 a[20:23],a[236:239],v[72:75],a[20:23]
v_exp_f32 v105,v105
v_mfma_f32_16x16x32_bf16 a[24:27],a[240:243],v[64:67],a[24:27]
s_waitcnt lgkmcnt(0)
v_exp_f32 v106,v106
v_mfma_f32_16x16x32_bf16 a[24:27],a[244:247],v[72:75],a[24:27]
v_exp_f32 v107,v107
v_mfma_f32_16x16x32_bf16 a[28:31],a[248:251],v[64:67],a[28:31]
v_exp_f32 v108,v108
v_mfma_f32_16x16x32_bf16 a[28:31],a[252:255],v[72:75],a[28:31]
v_exp_f32 v109,v109
v_mfma_f32_16x16x32_bf16 v[48:51],v[28:31],v[64:67],v[48:51]
v_exp_f32 v110,v110
v_mfma_f32_16x16x32_bf16 v[48:51],v[28:31],v[72:75],v[48:51]
v_exp_f32 v111,v111
s_waitcnt vmcnt(11)
s_barrier
v_mfma_f32_16x16x32_bf16 v[112:115],a[128:131],v[224:227],v[44:47]
buffer_store_dwordx2 v[8:9],v10,s[44:47],s18 offen offset:256 sc1
v_mfma_f32_16x16x32_bf16 v[112:115],a[132:135],v[232:235],v[112:115]
v_cvt_pk_bf16_f32 v104,v104,v105
v_cvt_pk_bf16_f32 v105,v106,v107
v_mfma_f32_16x16x32_bf16 v[112:115],a[136:139],v[240:243],v[112:115]
v_cvt_pk_bf16_f32 v106,v108,v109
v_cvt_pk_bf16_f32 v107,v110,v111
v_mfma_f32_16x16x32_bf16 v[112:115],a[140:143],v[248:251],v[112:115]
s_add_u32 m0,s86,0
v_cmp_nlt_f32 s[54:55],v48,s73
v_max_f32 v49,0x00800000,v48
v_mfma_f32_16x16x32_bf16 v[116:119],a[156:159],v[248:251],v[44:47]
v_rcp_f32 v49,v49
v_mfma_f32_16x16x32_bf16 v[116:119],a[152:155],v[240:243],v[116:119]
v_lshl_add_u32 v1,v2,5,v1
v_lshlrev_b32 v2,6,v2
v_mfma_f32_16x16x32_bf16 v[116:119],a[148:151],v[232:235],v[116:119]
buffer_load_dwordx4 v24,s[48:51],s83 offen lds
v_mfma_f32_16x16x32_bf16 v[116:119],a[144:147],v[224:227],v[116:119]
v_exp_f32 v112,v112
v_mfma_f32_16x16x32_bf16 v[120:123],a[160:163],v[224:227],v[44:47]
v_exp_f32 v113,v113
v_mfma_f32_16x16x32_bf16 v[120:123],a[164:167],v[232:235],v[120:123]
v_exp_f32 v114,v114
v_mfma_f32_16x16x32_bf16 v[120:123],a[168:171],v[240:243],v[120:123]
v_exp_f32 v115,v115
v_mfma_f32_16x16x32_bf16 v[120:123],a[172:175],v[248:251],v[120:123]
v_exp_f32 v116,v116
v_mfma_f32_16x16x32_bf16 v[124:127],a[188:191],v[248:251],v[44:47]
buffer_load_dwordx4 v25,s[48:51],s83 offen offset:1024 lds
v_mfma_f32_16x16x32_bf16 v[124:127],a[184:187],v[240:243],v[124:127]
v_exp_f32 v117,v117
v_mfma_f32_16x16x32_bf16 v[124:127],a[180:183],v[232:235],v[124:127]
v_exp_f32 v118,v118
v_mfma_f32_16x16x32_bf16 v[124:127],a[176:179],v[224:227],v[124:127]
v_exp_f32 v119,v119
v_mfma_f32_16x16x32_bf16 v[52:55],v[28:31],v[80:83],v[52:55]
v_cvt_pk_bf16_f32 v112,v112,v113
v_cvt_pk_bf16_f32 v113,v114,v115
v_mfma_f32_16x16x32_bf16 v[52:55],v[28:31],v[88:91],v[52:55]
v_cvt_pk_bf16_f32 v114,v116,v117
v_cvt_pk_bf16_f32 v115,v118,v119
v_mfma_f32_16x16x32_bf16 v[132:135],a[192:195],v[80:83],v[132:135]
buffer_load_dwordx4 v26,s[48:51],s83 offen offset:2048 lds
v_mfma_f32_16x16x32_bf16 v[132:135],a[196:199],v[88:91],v[132:135]
v_exp_f32 v120,v120
v_mfma_f32_16x16x32_bf16 v[140:143],a[200:203],v[80:83],v[140:143]
v_exp_f32 v121,v121
v_mfma_f32_16x16x32_bf16 v[140:143],a[204:207],v[88:91],v[140:143]
v_exp_f32 v122,v122
v_mfma_f32_16x16x32_bf16 v[148:151],a[208:211],v[80:83],v[148:151]
v_exp_f32 v123,v123
v_mfma_f32_16x16x32_bf16 v[148:151],a[212:215],v[88:91],v[148:151]
v_exp_f32 v124,v124
v_mfma_f32_16x16x32_bf16 v[156:159],a[216:219],v[80:83],v[156:159]
buffer_load_dwordx4 v27,s[48:51],s83 offen offset:3072 lds
v_mfma_f32_16x16x32_bf16 v[156:159],a[220:223],v[88:91],v[156:159]
v_exp_f32 v125,v125
v_mfma_f32_16x16x32_bf16 v[164:167],a[224:227],v[80:83],v[164:167]
v_exp_f32 v126,v126
v_mfma_f32_16x16x32_bf16 v[164:167],a[228:231],v[88:91],v[164:167]
s_add_u32 m0,s86,4096
v_exp_f32 v127,v127
v_mfma_f32_16x16x32_bf16 v[172:175],a[232:235],v[80:83],v[172:175]
v_cvt_pk_bf16_f32 v120,v120,v121
v_cvt_pk_bf16_f32 v121,v122,v123
v_mfma_f32_16x16x32_bf16 v[172:175],a[236:239],v[88:91],v[172:175]
v_cvt_pk_bf16_f32 v122,v124,v125
v_cvt_pk_bf16_f32 v123,v126,v127
v_mfma_f32_16x16x32_bf16 v[180:183],a[240:243],v[80:83],v[180:183]
buffer_load_dwordx4 v7,s[48:51],s83 offen lds
v_mfma_f32_16x16x32_bf16 v[180:183],a[244:247],v[88:91],v[180:183]
v_cmp_nlt_f32 s[56:57],v52,s73
v_sub_u32 v2,v1,v2
v_mfma_f32_16x16x32_bf16 v[188:191],a[248:251],v[80:83],v[188:191]
s_or_b64 s[54:55],s[54:55],s[56:57]
v_accvgpr_read_b32 v8,a0
v_accvgpr_read_b32 v9,a1
v_mfma_f32_16x16x32_bf16 v[188:191],a[252:255],v[88:91],v[188:191]
s_add_u32 m0,s86,5120
v_accvgpr_read_b32 v10,a2
v_accvgpr_read_b32 v11,a3
v_mfma_f32_16x16x32_bf16 v[56:59],v[28:31],v[96:99],v[56:59]
v_accvgpr_read_b32 v12,a4
v_accvgpr_read_b32 v13,a5
v_mfma_f32_16x16x32_bf16 v[56:59],v[28:31],v[104:107],v[56:59]
v_accvgpr_read_b32 v14,a6
v_accvgpr_read_b32 v15,a7
v_mfma_f32_16x16x32_bf16 v[196:199],a[192:195],v[96:99],v[196:199]
buffer_load_dwordx4 v24,s[48:51],s84 offen lds
v_mfma_f32_16x16x32_bf16 v[196:199],a[196:199],v[104:107],v[196:199]
v_mul_f32 v8,v8,v49
v_mul_f32 v9,v9,v49
v_mfma_f32_16x16x32_bf16 v[204:207],a[200:203],v[96:99],v[204:207]
v_cmp_nlt_f32 s[56:57],v56,s73
v_mul_f32 v10,v10,v49
v_mfma_f32_16x16x32_bf16 v[204:207],a[204:207],v[104:107],v[204:207]
s_or_b64 s[54:55],s[54:55],s[56:57]
v_mul_f32 v11,v11,v49
v_mul_f32 v12,v12,v49
v_mfma_f32_16x16x32_bf16 v[212:215],a[208:211],v[96:99],v[212:215]
v_mul_f32 v13,v13,v49
v_mul_f32 v14,v14,v49
v_mfma_f32_16x16x32_bf16 v[212:215],a[212:215],v[104:107],v[212:215]
v_mul_f32 v15,v15,v49
v_cvt_pk_bf16_f32 v32,v8,v9
v_mfma_f32_16x16x32_bf16 v[220:223],a[216:219],v[96:99],v[220:223]
buffer_load_dwordx4 v25,s[48:51],s84 offen offset:1024 lds
v_mfma_f32_16x16x32_bf16 v[220:223],a[220:223],v[104:107],v[220:223]
v_cvt_pk_bf16_f32 v33,v10,v11
v_cvt_pk_bf16_f32 v34,v12,v13
v_mfma_f32_16x16x32_bf16 v[228:231],a[224:227],v[96:99],v[228:231]
v_cvt_pk_bf16_f32 v35,v14,v15
v_accvgpr_read_b32 v16,a8
v_mfma_f32_16x16x32_bf16 v[228:231],a[228:231],v[104:107],v[228:231]
v_accvgpr_read_b32 v17,a9
v_accvgpr_read_b32 v18,a10
v_mfma_f32_16x16x32_bf16 v[236:239],a[232:235],v[96:99],v[236:239]
v_accvgpr_read_b32 v19,a11
v_accvgpr_read_b32 v20,a12
v_mfma_f32_16x16x32_bf16 v[236:239],a[236:239],v[104:107],v[236:239]
v_accvgpr_read_b32 v21,a13
v_accvgpr_read_b32 v22,a14
v_mfma_f32_16x16x32_bf16 v[244:247],a[240:243],v[96:99],v[244:247]
buffer_load_dwordx4 v26,s[48:51],s84 offen offset:2048 lds
v_mfma_f32_16x16x32_bf16 v[244:247],a[244:247],v[104:107],v[244:247]
v_accvgpr_read_b32 v23,a15
v_mul_f32 v16,v16,v49
v_mfma_f32_16x16x32_bf16 v[252:255],a[248:251],v[96:99],v[252:255]
v_mul_f32 v17,v17,v49
v_mul_f32 v18,v18,v49
v_mfma_f32_16x16x32_bf16 v[252:255],a[252:255],v[104:107],v[252:255]
v_mul_f32 v19,v19,v49
v_mul_f32 v20,v20,v49
v_mfma_f32_16x16x32_bf16 v[60:63],v[28:31],v[112:115],v[60:63]
v_mul_f32 v21,v21,v49
v_mul_f32 v22,v22,v49
v_mfma_f32_16x16x32_bf16 v[60:63],v[28:31],v[120:123],v[60:63]
v_mul_f32 v23,v23,v49
v_cvt_pk_bf16_f32 v36,v16,v17
v_mfma_f32_16x16x32_bf16 a[32:35],a[192:195],v[112:115],a[32:35]
buffer_load_dwordx4 v27,s[48:51],s84 offen offset:3072 lds
v_mfma_f32_16x16x32_bf16 a[32:35],a[196:199],v[120:123],a[32:35]
v_cvt_pk_bf16_f32 v37,v18,v19
v_cvt_pk_bf16_f32 v38,v20,v21
v_mfma_f32_16x16x32_bf16 a[36:39],a[200:203],v[112:115],a[36:39]
v_cmp_nlt_f32 s[56:57],v60,s73
v_cvt_pk_bf16_f32 v39,v22,v23
v_mfma_f32_16x16x32_bf16 a[36:39],a[204:207],v[120:123],a[36:39]
s_add_u32 m0,s86,9216
v_accvgpr_read_b32 v8,a16
v_accvgpr_read_b32 v9,a17
v_mfma_f32_16x16x32_bf16 a[40:43],a[208:211],v[112:115],a[40:43]
s_or_b64 s[54:55],s[54:55],s[56:57]
v_accvgpr_read_b32 v10,a18
v_accvgpr_read_b32 v11,a19
v_mfma_f32_16x16x32_bf16 a[40:43],a[212:215],v[120:123],a[40:43]
v_accvgpr_read_b32 v12,a20
v_accvgpr_read_b32 v13,a21
v_mfma_f32_16x16x32_bf16 a[44:47],a[216:219],v[112:115],a[44:47]
buffer_load_dwordx4 v7,s[48:51],s84 offen lds
v_mfma_f32_16x16x32_bf16 a[44:47],a[220:223],v[120:123],a[44:47]
v_accvgpr_read_b32 v14,a22
v_accvgpr_read_b32 v15,a23
v_mfma_f32_16x16x32_bf16 a[48:51],a[224:227],v[112:115],a[48:51]
v_mul_f32 v8,v8,v49
v_mul_f32 v9,v9,v49
v_mfma_f32_16x16x32_bf16 a[48:51],a[228:231],v[120:123],a[48:51]
v_mul_f32 v10,v10,v49
v_mul_f32 v11,v11,v49
v_mfma_f32_16x16x32_bf16 a[52:55],a[232:235],v[112:115],a[52:55]
v_mul_f32 v12,v12,v49
v_mul_f32 v13,v13,v49
v_mfma_f32_16x16x32_bf16 a[52:55],a[236:239],v[120:123],a[52:55]
v_mul_f32 v14,v14,v49
v_mul_f32 v15,v15,v49
v_mfma_f32_16x16x32_bf16 a[56:59],a[240:243],v[112:115],a[56:59]
v_cvt_pk_bf16_f32 v40,v8,v9
v_cvt_pk_bf16_f32 v41,v10,v11
v_mfma_f32_16x16x32_bf16 a[56:59],a[244:247],v[120:123],a[56:59]
v_cvt_pk_bf16_f32 v42,v12,v13
v_cvt_pk_bf16_f32 v43,v14,v15
v_mfma_f32_16x16x32_bf16 a[60:63],a[248:251],v[112:115],a[60:63]
ds_read_b64_tr_b16 v[64:65],v1
ds_read_b64_tr_b16 v[66:67],v1 offset:5120
v_mfma_f32_16x16x32_bf16 a[60:63],a[252:255],v[120:123],a[60:63]
ds_read_b64_tr_b16 v[68:69],v1 offset:10240
v_max_f32 v53,0x00800000,v52
v_rcp_f32 v53,v53
v_max_f32 v57,0x00800000,v56
v_rcp_f32 v57,v57
v_max_f32 v61,0x00800000,v60
v_rcp_f32 v61,v61
ds_read_b64_tr_b16 v[70:71],v1 offset:15360
ds_read_b64_tr_b16 v[72:73],v1 offset:20480
ds_read_b64_tr_b16 v[74:75],v1 offset:25600
ds_read_b64_tr_b16 v[76:77],v1 offset:30720
ds_read_b64_tr_b16 v[78:79],v1 offset:35840
ds_read_b64_tr_b16 v[80:81],v2 offset:32
ds_read_b64_tr_b16 v[82:83],v2 offset:5152
ds_read_b64_tr_b16 v[84:85],v2 offset:10272
ds_read_b64_tr_b16 v[86:87],v2 offset:15392
ds_read_b64_tr_b16 v[88:89],v2 offset:20512
ds_read_b64_tr_b16 v[90:91],v2 offset:25632
ds_read_b64_tr_b16 v[92:93],v2 offset:30752
ds_read_b64_tr_b16 v[94:95],v2 offset:35872
s_cmp_lg_u64 s[54:55],0
s_cbranch_scc1 L_slow1_%=
L_ok1_%=:
s_cmp_ge_u32 s63,2
s_cbranch_scc1 L_GS_%=
L_gbF_%=:
s_waitcnt lgkmcnt(14)
v_mfma_f32_16x16x32_bf16 a[192:195],v[64:67],a[68:71],0
buffer_store_dwordx4 v[32:35],v6,s[40:43],s79 offen sc1
s_waitcnt lgkmcnt(12)
v_mfma_f32_16x16x32_bf16 a[192:195],v[68:71],a[76:79],a[192:195]
s_lshl_b32 s91,s63,8
v_bfe_u32 v3,v6,4,2
v_min_u32 v3,2,v3
s_waitcnt lgkmcnt(10)
v_mfma_f32_16x16x32_bf16 a[192:195],v[72:75],a[84:87],a[192:195]
s_add_u32 s87,s64,1
v_add3_u32 v3,v3,s64,1
v_and_b32 v3,3,v3
s_waitcnt lgkmcnt(8)
v_mfma_f32_16x16x32_bf16 a[192:195],v[76:79],a[92:95],a[192:195]
s_and_b32 s87,s87,3
ds_read_b64_tr_b16 v[96:97],v1 offset:64
v_lshlrev_b32 v3,6,v3
v_mfma_f32_16x16x32_bf16 a[196:199],v[76:79],a[124:127],0
s_lshl_b32 s87,s87,1
ds_read_b64_tr_b16 v[98:99],v1 offset:5184
ds_read_b64_tr_b16 v[100:101],v1 offset:10304
v_mfma_f32_16x16x32_bf16 a[196:199],v[72:75],a[116:119],a[196:199]
)A"
R"A(s_lshl_b32 s87,s87,16
ds_read_b64_tr_b16 v[102:103],v1 offset:15424
v_lshlrev_b32 v29,10,v6
v_mfma_f32_16x16x32_bf16 a[196:199],v[68:71],a[108:111],a[196:199]
s_add_u32 s88,s87,4096
ds_read_b64_tr_b16 v[104:105],v1 offset:20544
v_accvgpr_read_b32 v8,a24
v_mfma_f32_16x16x32_bf16 a[196:199],v[64:67],a[100:103],a[196:199]
s_add_u32 s89,s87,8192
ds_read_b64_tr_b16 v[106:107],v1 offset:25664
v_accvgpr_read_b32 v9,a25
v_mfma_f32_16x16x32_bf16 a[200:203],v[64:67],a[64:67],0
s_add_u32 s90,s87,12288
ds_read_b64_tr_b16 v[108:109],v1 offset:30784
ds_read_b64_tr_b16 v[110:111],v1 offset:35904
v_mfma_f32_16x16x32_bf16 a[200:203],v[68:71],a[72:75],a[200:203]
s_add_u32 s20,s64,2
ds_read_b64_tr_b16 v[112:113],v2 offset:96
v_accvgpr_read_b32 v10,a26
v_mfma_f32_16x16x32_bf16 a[200:203],v[72:75],a[80:83],a[200:203]
s_and_b32 s20,s20,3
ds_read_b64_tr_b16 v[114:115],v2 offset:5216
v_accvgpr_read_b32 v11,a27
v_mfma_f32_16x16x32_bf16 a[200:203],v[76:79],a[88:91],a[200:203]
s_lshl_b32 s20,s20,1
ds_read_b64_tr_b16 v[116:117],v2 offset:10336
v_accvgpr_read_b32 v12,a28
v_mfma_f32_16x16x32_bf16 a[204:207],v[76:79],a[120:123],0
s_lshl_b32 s20,s20,16
ds_read_b64_tr_b16 v[118:119],v2 offset:15456
ds_read_b64_tr_b16 v[120:121],v2 offset:20576
v_mfma_f32_16x16x32_bf16 a[204:207],v[72:75],a[112:115],a[204:207]
s_add_u32 s21,s20,4096
ds_read_b64_tr_b16 v[122:123],v2 offset:25696
v_accvgpr_read_b32 v13,a29
v_mfma_f32_16x16x32_bf16 a[204:207],v[68:71],a[104:107],a[204:207]
s_add_u32 s22,s20,8192
ds_read_b64_tr_b16 v[124:125],v2 offset:30816
v_accvgpr_read_b32 v14,a30
v_mfma_f32_16x16x32_bf16 a[204:207],v[64:67],a[96:99],a[204:207]
buffer_store_dwordx4 v[36:39],v6,s[40:43],s80 offen sc1
s_waitcnt lgkmcnt(9)
v_mfma_f32_16x16x32_bf16 a[208:211],v[80:83],a[68:71],0
s_add_u32 s23,s20,12288
ds_read_b64_tr_b16 v[126:127],v2 offset:35936
ds_read_b64_tr_b16 v[64:65],v1 offset:128
v_mfma_f32_16x16x32_bf16 a[208:211],v[84:87],a[76:79],a[208:211]
s_add_u32 m0,s86,122880
ds_read_b64_tr_b16 v[66:67],v1 offset:5248
v_accvgpr_read_b32 v15,a31
v_mfma_f32_16x16x32_bf16 a[208:211],v[88:91],a[84:87],a[208:211]
s_add_u32 s83,s64,3
ds_read_b64_tr_b16 v[68:69],v1 offset:10368
v_mul_f32 v8,v8,v49
L_pd13_%=:
s_cbranch_execz L_trt1_%=
v_mfma_f32_16x16x32_bf16 a[208:211],v[92:95],a[92:95],a[208:211]
s_and_b32 s83,s83,3
ds_read_b64_tr_b16 v[70:71],v1 offset:15488
v_mul_f32 v9,v9,v49
v_mfma_f32_16x16x32_bf16 a[212:215],v[92:95],a[124:127],0
s_waitcnt vmcnt(21)
buffer_load_dwordx2 v[4:5],v3,s[44:47],s91 offen offset:256 sc1
v_mfma_f32_16x16x32_bf16 a[212:215],v[88:91],a[116:119],a[212:215]
s_lshl_b32 s83,s83,1
ds_read_b64_tr_b16 v[72:73],v1 offset:20608
ds_read_b64_tr_b16 v[74:75],v1 offset:25728
v_mfma_f32_16x16x32_bf16 a[212:215],v[84:87],a[108:111],a[212:215]
s_mul_i32 s83,s83,163840
ds_read_b64_tr_b16 v[76:77],v1 offset:30848
v_mul_f32 v10,v10,v49
v_mfma_f32_16x16x32_bf16 a[212:215],v[80:83],a[100:103],a[212:215]
s_add_u32 s83,s83,s85
ds_read_b64_tr_b16 v[78:79],v1 offset:35968
v_mul_f32 v11,v11,v49
v_mfma_f32_16x16x32_bf16 a[216:219],v[80:83],a[64:67],0
s_add_u32 s84,s83,20480
v_mul_f32 v12,v12,v49
v_mul_f32 v13,v13,v49
v_mfma_f32_16x16x32_bf16 a[216:219],v[84:87],a[72:75],a[216:219]
s_lshl_b32 s66,s61,3
v_mul_f32 v14,v14,v49
v_mul_f32 v15,v15,v49
v_mfma_f32_16x16x32_bf16 a[216:219],v[88:91],a[80:83],a[216:219]
s_add_u32 s66,s66,s64
v_cvt_pk_bf16_f32 v44,v8,v9
v_cvt_pk_bf16_f32 v45,v10,v11
v_mfma_f32_16x16x32_bf16 a[216:219],v[92:95],a[88:91],a[216:219]
s_mul_i32 s66,s66,262144
v_cvt_pk_bf16_f32 v46,v12,v13
v_cvt_pk_bf16_f32 v47,v14,v15
v_mfma_f32_16x16x32_bf16 a[220:223],v[92:95],a[120:123],0
s_add_u32 s66,s66,100663296
ds_read_b64_tr_b16 v[92:93],v2 offset:30880
ds_read_b64_tr_b16 v[94:95],v2 offset:36000
v_mfma_f32_16x16x32_bf16 a[220:223],v[88:91],a[112:115],a[220:223]
s_mov_b32 s38,262144
ds_read_b64_tr_b16 v[88:89],v2 offset:20640
v_mul_f32 v132,v132,v53
v_mfma_f32_16x16x32_bf16 a[220:223],v[84:87],a[104:107],a[220:223]
buffer_store_dwordx4 v[40:43],v6,s[40:43],s81 offen sc1
v_mfma_f32_16x16x32_bf16 a[220:223],v[80:83],a[96:99],a[220:223]
s_mov_b32 s39,0x20000
ds_read_b64_tr_b16 v[80:81],v2 offset:160
ds_read_b64_tr_b16 v[82:83],v2 offset:5280
v_mfma_f32_16x16x32_bf16 a[224:227],v[96:99],a[68:71],0
s_add_u32 s74,s63,1
ds_read_b64_tr_b16 v[84:85],v2 offset:10400
v_mul_f32 v133,v133,v53
v_mfma_f32_16x16x32_bf16 a[224:227],v[100:103],a[76:79],a[224:227]
s_and_b32 s74,s74,3
ds_read_b64_tr_b16 v[86:87],v2 offset:15520
v_mul_f32 v134,v134,v53
v_mfma_f32_16x16x32_bf16 a[224:227],v[104:107],a[84:87],a[224:227]
s_lshl_b32 s74,s74,2
ds_read_b64_tr_b16 v[90:91],v2 offset:25760
v_mul_f32 v135,v135,v53
s_waitcnt lgkmcnt(8)
v_mfma_f32_16x16x32_bf16 a[224:227],v[108:111],a[92:95],a[224:227]
s_add_u32 s74,s74,s60
v_mul_f32 v140,v140,v53
v_mul_f32 v141,v141,v53
v_mfma_f32_16x16x32_bf16 a[228:231],v[108:111],a[124:127],0
s_mul_i32 s74,s74,16384
v_mul_f32 v142,v142,v53
v_mul_f32 v143,v143,v53
v_mfma_f32_16x16x32_bf16 a[228:231],v[104:107],a[116:119],a[228:231]
s_add_u32 s74,s74,8192
v_cvt_pk_bf16_f32 v132,v132,v133
v_cvt_pk_bf16_f32 v133,v134,v135
v_mfma_f32_16x16x32_bf16 a[228:231],v[100:103],a[108:111],a[228:231]
s_add_u32 s75,s63,2
v_cvt_pk_bf16_f32 v134,v140,v141
v_cvt_pk_bf16_f32 v135,v142,v143
v_mfma_f32_16x16x32_bf16 a[228:231],v[96:99],a[100:103],a[228:231]
s_and_b32 s75,s75,3
v_mul_f32 v148,v148,v53
v_mul_f32 v149,v149,v53
v_mfma_f32_16x16x32_bf16 a[232:235],v[96:99],a[64:67],0
s_lshl_b32 s75,s75,2
v_mul_f32 v150,v150,v53
v_mul_f32 v151,v151,v53
v_mfma_f32_16x16x32_bf16 a[232:235],v[100:103],a[72:75],a[232:235]
s_add_u32 s75,s75,s60
v_mul_f32 v156,v156,v53
v_mul_f32 v157,v157,v53
v_mfma_f32_16x16x32_bf16 a[232:235],v[104:107],a[80:83],a[232:235]
s_mul_i32 s75,s75,16384
v_mul_f32 v158,v158,v53
v_mul_f32 v159,v159,v53
v_mfma_f32_16x16x32_bf16 a[232:235],v[108:111],a[88:91],a[232:235]
s_add_u32 s75,s75,4096
v_cvt_pk_bf16_f32 v148,v148,v149
v_cvt_pk_bf16_f32 v149,v150,v151
v_mfma_f32_16x16x32_bf16 a[236:239],v[108:111],a[120:123],0
s_add_u32 s76,s63,3
ds_read_b64_tr_b16 v[108:109],v1 offset:30912
ds_read_b64_tr_b16 v[110:111],v1 offset:36032
v_mfma_f32_16x16x32_bf16 a[236:239],v[104:107],a[112:115],a[236:239]
buffer_store_dwordx4 v[44:47],v6,s[40:43],s82 offen sc1
v_mfma_f32_16x16x32_bf16 a[236:239],v[100:103],a[104:107],a[236:239]
s_and_b32 s76,s76,3
ds_read_b64_tr_b16 v[100:101],v1 offset:10432
ds_read_b64_tr_b16 v[102:103],v1 offset:15552
v_mfma_f32_16x16x32_bf16 a[236:239],v[96:99],a[96:99],a[236:239]
s_lshl_b32 s76,s76,2
ds_read_b64_tr_b16 v[96:97],v1 offset:192
v_cvt_pk_bf16_f32 v150,v156,v157
v_mfma_f32_16x16x32_bf16 a[240:243],v[112:115],a[68:71],0
s_add_u32 s76,s76,s60
ds_read_b64_tr_b16 v[98:99],v1 offset:5312
v_cvt_pk_bf16_f32 v151,v158,v159
v_mfma_f32_16x16x32_bf16 a[240:243],v[116:119],a[76:79],a[240:243]
s_mul_i32 s76,s76,16384
ds_read_b64_tr_b16 v[104:105],v1 offset:20672
v_mul_f32 v164,v164,v53
v_mfma_f32_16x16x32_bf16 a[240:243],v[120:123],a[84:87],a[240:243]
s_add_u32 s76,s76,0
ds_read_b64_tr_b16 v[106:107],v1 offset:25792
v_mul_f32 v165,v165,v53
v_mfma_f32_16x16x32_bf16 a[240:243],v[124:127],a[92:95],a[240:243]
s_lshl_b32 s67,s63,10
v_mul_f32 v166,v166,v53
v_mul_f32 v167,v167,v53
v_mfma_f32_16x16x32_bf16 a[244:247],v[124:127],a[124:127],0
s_mov_b32 s26,307200
v_mul_f32 v172,v172,v53
v_mul_f32 v173,v173,v53
v_mfma_f32_16x16x32_bf16 a[244:247],v[120:123],a[116:119],a[244:247]
s_mov_b32 s27,0x20000
v_mul_f32 v174,v174,v53
v_mul_f32 v175,v175,v53
v_mfma_f32_16x16x32_bf16 a[244:247],v[116:119],a[108:111],a[244:247]
s_lshl_b32 s77,s63,2
v_cvt_pk_bf16_f32 v164,v164,v165
v_cvt_pk_bf16_f32 v165,v166,v167
v_mfma_f32_16x16x32_bf16 a[244:247],v[112:115],a[100:103],a[244:247]
s_add_u32 s77,s77,s60
v_cvt_pk_bf16_f32 v166,v172,v173
v_cvt_pk_bf16_f32 v167,v174,v175
v_mfma_f32_16x16x32_bf16 a[248:251],v[112:115],a[64:67],0
s_mul_i32 s77,s77,16384
v_mul_f32 v180,v180,v53
v_mul_f32 v181,v181,v53
v_mfma_f32_16x16x32_bf16 a[248:251],v[116:119],a[72:75],a[248:251]
s_add_u32 s65,s77,0
v_mul_f32 v182,v182,v53
v_mul_f32 v183,v183,v53
v_mfma_f32_16x16x32_bf16 a[248:251],v[120:123],a[80:83],a[248:251]
s_add_u32 s68,s77,12288
v_mul_f32 v188,v188,v53
v_mul_f32 v189,v189,v53
v_mfma_f32_16x16x32_bf16 a[248:251],v[124:127],a[88:91],a[248:251]
v_mul_f32 v190,v190,v53
v_mul_f32 v191,v191,v53
v_mfma_f32_16x16x32_bf16 a[252:255],v[124:127],a[120:123],0
buffer_load_dword v30,v29,s[8:11],s7 offen
v_mfma_f32_16x16x32_bf16 a[252:255],v[120:123],a[112:115],a[252:255]
ds_read_b64_tr_b16 v[120:121],v2 offset:20704
ds_read_b64_tr_b16 v[122:123],v2 offset:25824
v_mfma_f32_16x16x32_bf16 a[252:255],v[116:119],a[104:107],a[252:255]
buffer_store_dwordx4 v[132:135],v6,s[40:43],s79 offen offset:1024 sc1
v_mfma_f32_16x16x32_bf16 a[252:255],v[112:115],a[96:99],a[252:255]
ds_read_b64_tr_b16 v[112:113],v2 offset:224
ds_read_b64_tr_b16 v[114:115],v2 offset:5344
v_mfma_f32_16x16x32_bf16 a[0:3],v[64:67],a[68:71],0
ds_read_b64_tr_b16 v[116:117],v2 offset:10464
v_cvt_pk_bf16_f32 v180,v180,v181
v_mfma_f32_16x16x32_bf16 a[0:3],v[68:71],a[76:79],a[0:3]
ds_read_b64_tr_b16 v[118:119],v2 offset:15584
v_cvt_pk_bf16_f32 v181,v182,v183
v_mfma_f32_16x16x32_bf16 a[0:3],v[72:75],a[84:87],a[0:3]
ds_read_b64_tr_b16 v[124:125],v2 offset:30944
v_cvt_pk_bf16_f32 v182,v188,v189
v_mfma_f32_16x16x32_bf16 a[0:3],v[76:79],a[92:95],a[0:3]
ds_read_b64_tr_b16 v[126:127],v2 offset:36064
v_cvt_pk_bf16_f32 v183,v190,v191
v_mfma_f32_16x16x32_bf16 a[4:7],v[76:79],a[124:127],0
v_mul_f32 v196,v196,v57
v_mul_f32 v197,v197,v57
v_mfma_f32_16x16x32_bf16 a[4:7],v[72:75],a[116:119],a[4:7]
v_mul_f32 v198,v198,v57
v_mul_f32 v199,v199,v57
v_mfma_f32_16x16x32_bf16 a[4:7],v[68:71],a[108:111],a[4:7]
v_mul_f32 v204,v204,v57
v_mul_f32 v205,v205,v57
v_mfma_f32_16x16x32_bf16 a[4:7],v[64:67],a[100:103],a[4:7]
v_mul_f32 v206,v206,v57
v_mul_f32 v207,v207,v57
v_mfma_f32_16x16x32_bf16 a[8:11],v[64:67],a[64:67],0
v_cvt_pk_bf16_f32 v196,v196,v197
v_cvt_pk_bf16_f32 v197,v198,v199
v_mfma_f32_16x16x32_bf16 a[8:11],v[68:71],a[72:75],a[8:11]
v_cvt_pk_bf16_f32 v198,v204,v205
v_cvt_pk_bf16_f32 v199,v206,v207
v_mfma_f32_16x16x32_bf16 a[8:11],v[72:75],a[80:83],a[8:11]
v_mul_f32 v212,v212,v57
v_mul_f32 v213,v213,v57
v_mfma_f32_16x16x32_bf16 a[8:11],v[76:79],a[88:91],a[8:11]
v_mul_f32 v214,v214,v57
v_mul_f32 v215,v215,v57
v_mfma_f32_16x16x32_bf16 a[12:15],v[76:79],a[120:123],0
ds_read_b64_tr_b16 v[76:77],v1 offset:30976
ds_read_b64_tr_b16 v[78:79],v1 offset:36096
v_mfma_f32_16x16x32_bf16 a[12:15],v[72:75],a[112:115],a[12:15]
buffer_store_dwordx4 v[148:151],v6,s[40:43],s80 offen offset:1024 sc1
v_mfma_f32_16x16x32_bf16 a[12:15],v[68:71],a[104:107],a[12:15]
ds_read_b64_tr_b16 v[68:69],v1 offset:10496
ds_read_b64_tr_b16 v[70:71],v1 offset:15616
v_mfma_f32_16x16x32_bf16 a[12:15],v[64:67],a[96:99],a[12:15]
s_waitcnt lgkmcnt(4)
ds_read_b64_tr_b16 v[64:65],v1 offset:256
v_mul_f32 v220,v220,v57
v_mfma_f32_16x16x32_bf16 a[16:19],v[80:83],a[68:71],0
ds_read_b64_tr_b16 v[66:67],v1 offset:5376
v_mul_f32 v221,v221,v57
v_mfma_f32_16x16x32_bf16 a[16:19],v[84:87],a[76:79],a[16:19]
ds_read_b64_tr_b16 v[72:73],v1 offset:20736
v_mul_f32 v222,v222,v57
v_mfma_f32_16x16x32_bf16 a[16:19],v[88:91],a[84:87],a[16:19]
ds_read_b64_tr_b16 v[74:75],v1 offset:25856
v_add_u32 v1,0xffff6000,v1
v_mfma_f32_16x16x32_bf16 a[16:19],v[92:95],a[92:95],a[16:19]
v_mul_f32 v223,v223,v57
v_cvt_pk_bf16_f32 v212,v212,v213
v_mfma_f32_16x16x32_bf16 a[20:23],v[92:95],a[124:127],0
v_cvt_pk_bf16_f32 v213,v214,v215
v_cvt_pk_bf16_f32 v214,v220,v221
v_mfma_f32_16x16x32_bf16 a[20:23],v[88:91],a[116:119],a[20:23]
v_cvt_pk_bf16_f32 v215,v222,v223
v_mul_f32 v228,v228,v57
v_mfma_f32_16x16x32_bf16 a[20:23],v[84:87],a[108:111],a[20:23]
v_mul_f32 v229,v229,v57
v_mul_f32 v230,v230,v57
v_mfma_f32_16x16x32_bf16 a[20:23],v[80:83],a[100:103],a[20:23]
v_mul_f32 v231,v231,v57
v_mul_f32 v236,v236,v57
v_mfma_f32_16x16x32_bf16 a[24:27],v[80:83],a[64:67],0
v_mul_f32 v237,v237,v57
v_mul_f32 v238,v238,v57
v_mfma_f32_16x16x32_bf16 a[24:27],v[84:87],a[72:75],a[24:27]
v_mul_f32 v239,v239,v57
v_cvt_pk_bf16_f32 v228,v228,v229
)A"
R"A(v_mfma_f32_16x16x32_bf16 a[24:27],v[88:91],a[80:83],a[24:27]
s_waitcnt vmcnt(5)
v_cmp_ne_u32 s[56:57],s52,v4
v_cmp_ne_u32 s[58:59],s53,v5
v_mfma_f32_16x16x32_bf16 a[24:27],v[92:95],a[88:91],a[24:27]
s_or_b64 s[56:57],s[56:57],s[58:59]
s_cbranch_scc1 L_fr1_%=
L_fo1_%=:
v_cvt_pk_bf16_f32 v229,v230,v231
v_mfma_f32_16x16x32_bf16 a[28:31],v[92:95],a[120:123],0
buffer_store_dwordx4 v[164:167],v6,s[40:43],s81 offen offset:1024 sc1
v_mfma_f32_16x16x32_bf16 a[28:31],v[88:91],a[112:115],a[28:31]
s_lshl_b32 s91,s63,8
ds_read_b64_tr_b16 v[88:89],v2 offset:20768
ds_read_b64_tr_b16 v[90:91],v2 offset:25888
v_mfma_f32_16x16x32_bf16 a[28:31],v[84:87],a[104:107],a[28:31]
buffer_load_dwordx4 a[128:131],v6,s[28:31],s87 offen sc1
v_mfma_f32_16x16x32_bf16 a[28:31],v[80:83],a[96:99],a[28:31]
ds_read_b64_tr_b16 v[80:81],v2 offset:288
ds_read_b64_tr_b16 v[82:83],v2 offset:5408
v_mfma_f32_16x16x32_bf16 v[128:131],v[96:99],a[68:71],0
ds_read_b64_tr_b16 v[84:85],v2 offset:10528
v_cvt_pk_bf16_f32 v230,v236,v237
v_mfma_f32_16x16x32_bf16 v[128:131],v[100:103],a[76:79],v[128:131]
ds_read_b64_tr_b16 v[86:87],v2 offset:15648
v_cvt_pk_bf16_f32 v231,v238,v239
v_mfma_f32_16x16x32_bf16 v[128:131],v[104:107],a[84:87],v[128:131]
ds_read_b64_tr_b16 v[92:93],v2 offset:31008
v_mul_f32 v244,v244,v57
v_mfma_f32_16x16x32_bf16 v[128:131],v[108:111],a[92:95],v[128:131]
ds_read_b64_tr_b16 v[94:95],v2 offset:36128
v_add_u32 v2,0xffff6000,v2
v_mfma_f32_16x16x32_bf16 v[136:139],v[108:111],a[124:127],0
v_mul_f32 v245,v245,v57
v_mul_f32 v246,v246,v57
v_mfma_f32_16x16x32_bf16 v[136:139],v[104:107],a[116:119],v[136:139]
v_mul_f32 v247,v247,v57
v_mul_f32 v252,v252,v57
v_mfma_f32_16x16x32_bf16 v[136:139],v[100:103],a[108:111],v[136:139]
v_mul_f32 v253,v253,v57
v_mul_f32 v254,v254,v57
v_mfma_f32_16x16x32_bf16 v[136:139],v[96:99],a[100:103],v[136:139]
v_mul_f32 v255,v255,v57
v_cvt_pk_bf16_f32 v244,v244,v245
v_mfma_f32_16x16x32_bf16 v[144:147],v[96:99],a[64:67],0
v_cvt_pk_bf16_f32 v245,v246,v247
v_cvt_pk_bf16_f32 v246,v252,v253
v_mfma_f32_16x16x32_bf16 v[144:147],v[100:103],a[72:75],v[144:147]
buffer_load_dwordx4 a[144:147],v6,s[28:31],s88 offen sc1
v_mfma_f32_16x16x32_bf16 v[144:147],v[104:107],a[80:83],v[144:147]
v_cvt_pk_bf16_f32 v247,v254,v255
v_accvgpr_read_b32 v8,a40
v_mfma_f32_16x16x32_bf16 v[144:147],v[108:111],a[88:91],v[144:147]
buffer_store_dwordx4 v[180:183],v6,s[40:43],s82 offen offset:1024 sc1
v_mfma_f32_16x16x32_bf16 v[152:155],v[108:111],a[120:123],0
ds_read_b64_tr_b16 v[108:109],v1 offset:30720
ds_read_b64_tr_b16 v[110:111],v1 offset:35840
v_mfma_f32_16x16x32_bf16 v[152:155],v[104:107],a[112:115],v[152:155]
ds_read_b64_tr_b16 v[104:105],v1 offset:20480
v_accvgpr_read_b32 v9,a41
v_mfma_f32_16x16x32_bf16 v[152:155],v[100:103],a[104:107],v[152:155]
ds_read_b64_tr_b16 v[100:101],v1 offset:10240
v_accvgpr_read_b32 v10,a42
v_mfma_f32_16x16x32_bf16 v[152:155],v[96:99],a[96:99],v[152:155]
ds_read_b64_tr_b16 v[96:97],v1
v_accvgpr_read_b32 v11,a43
v_mfma_f32_16x16x32_bf16 v[160:163],v[112:115],a[68:71],0
ds_read_b64_tr_b16 v[98:99],v1 offset:5120
ds_read_b64_tr_b16 v[102:103],v1 offset:15360
v_mfma_f32_16x16x32_bf16 v[160:163],v[116:119],a[76:79],v[160:163]
ds_read_b64_tr_b16 v[106:107],v1 offset:25600
v_accvgpr_read_b32 v12,a44
v_mfma_f32_16x16x32_bf16 v[160:163],v[120:123],a[84:87],v[160:163]
v_accvgpr_read_b32 v13,a45
v_accvgpr_read_b32 v14,a46
v_mfma_f32_16x16x32_bf16 v[160:163],v[124:127],a[92:95],v[160:163]
v_accvgpr_read_b32 v15,a47
v_mul_f32 v8,v8,v61
v_mfma_f32_16x16x32_bf16 v[168:171],v[124:127],a[124:127],0
v_mul_f32 v9,v9,v61
v_mul_f32 v10,v10,v61
v_mfma_f32_16x16x32_bf16 v[168:171],v[120:123],a[116:119],v[168:171]
v_mul_f32 v11,v11,v61
v_mul_f32 v12,v12,v61
v_mfma_f32_16x16x32_bf16 v[168:171],v[116:119],a[108:111],v[168:171]
buffer_load_dwordx4 a[160:163],v6,s[28:31],s89 offen sc1
v_mfma_f32_16x16x32_bf16 v[168:171],v[112:115],a[100:103],v[168:171]
v_mul_f32 v13,v13,v61
v_mul_f32 v14,v14,v61
v_mfma_f32_16x16x32_bf16 v[176:179],v[112:115],a[64:67],0
v_mul_f32 v15,v15,v61
v_cvt_pk_bf16_f32 v156,v8,v9
v_mfma_f32_16x16x32_bf16 v[176:179],v[116:119],a[72:75],v[176:179]
v_cvt_pk_bf16_f32 v157,v10,v11
v_cvt_pk_bf16_f32 v158,v12,v13
v_mfma_f32_16x16x32_bf16 v[176:179],v[120:123],a[80:83],v[176:179]
buffer_store_dwordx4 v[196:199],v6,s[40:43],s79 offen offset:2048 sc1
v_mfma_f32_16x16x32_bf16 v[176:179],v[124:127],a[88:91],v[176:179]
v_cvt_pk_bf16_f32 v159,v14,v15
v_accvgpr_read_b32 v8,a56
v_mfma_f32_16x16x32_bf16 v[184:187],v[124:127],a[120:123],0
v_accvgpr_read_b32 v9,a57
v_accvgpr_read_b32 v10,a58
v_mfma_f32_16x16x32_bf16 v[184:187],v[120:123],a[112:115],v[184:187]
ds_read_b64_tr_b16 v[120:121],v2 offset:20512
ds_read_b64_tr_b16 v[122:123],v2 offset:25632
v_mfma_f32_16x16x32_bf16 v[184:187],v[116:119],a[104:107],v[184:187]
ds_read_b64_tr_b16 v[116:117],v2 offset:10272
v_accvgpr_read_b32 v11,a59
v_mfma_f32_16x16x32_bf16 v[184:187],v[112:115],a[96:99],v[184:187]
s_waitcnt lgkmcnt(3)
ds_read_b64_tr_b16 v[112:113],v2 offset:32
v_accvgpr_read_b32 v12,a60
v_mfma_f32_16x16x32_bf16 v[192:195],v[64:67],a[68:71],0
ds_read_b64_tr_b16 v[114:115],v2 offset:5152
v_accvgpr_read_b32 v13,a61
v_mfma_f32_16x16x32_bf16 v[192:195],v[68:71],a[76:79],v[192:195]
ds_read_b64_tr_b16 v[118:119],v2 offset:15392
v_accvgpr_read_b32 v14,a62
v_mfma_f32_16x16x32_bf16 v[192:195],v[72:75],a[84:87],v[192:195]
v_accvgpr_read_b32 v15,a63
v_mul_f32 v8,v8,v61
v_mfma_f32_16x16x32_bf16 v[192:195],v[76:79],a[92:95],v[192:195]
buffer_load_dwordx4 a[176:179],v6,s[28:31],s90 offen sc1
v_mfma_f32_16x16x32_bf16 v[200:203],v[76:79],a[124:127],0
v_mul_f32 v9,v9,v61
v_mul_f32 v10,v10,v61
v_mfma_f32_16x16x32_bf16 v[200:203],v[72:75],a[116:119],v[200:203]
v_mul_f32 v11,v11,v61
v_mul_f32 v12,v12,v61
v_mfma_f32_16x16x32_bf16 v[200:203],v[68:71],a[108:111],v[200:203]
v_mul_f32 v13,v13,v61
v_mul_f32 v14,v14,v61
v_mfma_f32_16x16x32_bf16 v[200:203],v[64:67],a[100:103],v[200:203]
v_mul_f32 v15,v15,v61
v_cvt_pk_bf16_f32 v188,v8,v9
v_mfma_f32_16x16x32_bf16 v[208:211],v[64:67],a[64:67],0
v_cvt_pk_bf16_f32 v189,v10,v11
v_cvt_pk_bf16_f32 v190,v12,v13
v_mfma_f32_16x16x32_bf16 v[208:211],v[68:71],a[72:75],v[208:211]
buffer_store_dwordx4 v[212:215],v6,s[40:43],s80 offen offset:2048 sc1
v_mfma_f32_16x16x32_bf16 v[208:211],v[72:75],a[80:83],v[208:211]
ds_read_b64_tr_b16 v[124:125],v2 offset:30752
v_cvt_pk_bf16_f32 v191,v14,v15
v_mfma_f32_16x16x32_bf16 v[208:211],v[76:79],a[88:91],v[208:211]
ds_read_b64_tr_b16 v[126:127],v2 offset:35872
v_accvgpr_read_b32 v16,a32
v_mfma_f32_16x16x32_bf16 v[216:219],v[76:79],a[120:123],0
v_accvgpr_read_b32 v17,a33
v_accvgpr_read_b32 v18,a34
v_mfma_f32_16x16x32_bf16 v[216:219],v[72:75],a[112:115],v[216:219]
v_accvgpr_read_b32 v19,a35
v_accvgpr_read_b32 v20,a36
v_mfma_f32_16x16x32_bf16 v[216:219],v[68:71],a[104:107],v[216:219]
v_accvgpr_read_b32 v21,a37
v_accvgpr_read_b32 v22,a38
v_mfma_f32_16x16x32_bf16 v[216:219],v[64:67],a[96:99],v[216:219]
v_accvgpr_read_b32 v23,a39
v_mul_f32 v16,v16,v61
v_mfma_f32_16x16x32_bf16 v[224:227],v[80:83],a[68:71],0
buffer_load_dwordx4 a[180:183],v6,s[28:31],s90 offen offset:1024 sc1
v_mfma_f32_16x16x32_bf16 v[224:227],v[84:87],a[76:79],v[224:227]
v_mul_f32 v17,v17,v61
v_mul_f32 v18,v18,v61
v_mfma_f32_16x16x32_bf16 v[224:227],v[88:91],a[84:87],v[224:227]
v_mul_f32 v19,v19,v61
v_mul_f32 v20,v20,v61
v_mfma_f32_16x16x32_bf16 v[224:227],v[92:95],a[92:95],v[224:227]
v_mul_f32 v21,v21,v61
v_mul_f32 v22,v22,v61
v_mfma_f32_16x16x32_bf16 v[232:235],v[92:95],a[124:127],0
v_mul_f32 v23,v23,v61
v_cvt_pk_bf16_f32 v140,v16,v17
v_mfma_f32_16x16x32_bf16 v[232:235],v[88:91],a[116:119],v[232:235]
v_cvt_pk_bf16_f32 v141,v18,v19
v_cvt_pk_bf16_f32 v142,v20,v21
v_mfma_f32_16x16x32_bf16 v[232:235],v[84:87],a[108:111],v[232:235]
v_cvt_pk_bf16_f32 v143,v22,v23
v_accvgpr_read_b32 v16,a48
v_mfma_f32_16x16x32_bf16 v[232:235],v[80:83],a[100:103],v[232:235]
v_accvgpr_read_b32 v17,a49
v_accvgpr_read_b32 v18,a50
v_mfma_f32_16x16x32_bf16 v[240:243],v[80:83],a[64:67],0
buffer_store_dwordx4 v[228:231],v6,s[40:43],s81 offen offset:2048 sc1
v_mfma_f32_16x16x32_bf16 v[240:243],v[84:87],a[72:75],v[240:243]
v_accvgpr_read_b32 v19,a51
v_accvgpr_read_b32 v20,a52
v_mfma_f32_16x16x32_bf16 v[240:243],v[88:91],a[80:83],v[240:243]
v_accvgpr_read_b32 v21,a53
v_accvgpr_read_b32 v22,a54
v_mfma_f32_16x16x32_bf16 v[240:243],v[92:95],a[88:91],v[240:243]
v_accvgpr_read_b32 v23,a55
v_mul_f32 v16,v16,v61
v_mfma_f32_16x16x32_bf16 v[248:251],v[92:95],a[120:123],0
v_mul_f32 v17,v17,v61
v_mul_f32 v18,v18,v61
v_mfma_f32_16x16x32_bf16 v[248:251],v[88:91],a[112:115],v[248:251]
buffer_load_dwordx4 a[164:167],v6,s[28:31],s89 offen offset:1024 sc1
v_mfma_f32_16x16x32_bf16 v[248:251],v[84:87],a[104:107],v[248:251]
v_mul_f32 v19,v19,v61
v_mul_f32 v20,v20,v61
v_mfma_f32_16x16x32_bf16 v[248:251],v[80:83],a[96:99],v[248:251]
v_mul_f32 v21,v21,v61
v_mul_f32 v22,v22,v61
v_mfma_f32_16x16x32_bf16 a[192:195],v[96:99],v[32:35],a[192:195]
v_mul_f32 v23,v23,v61
v_cvt_pk_bf16_f32 v172,v16,v17
v_mfma_f32_16x16x32_bf16 a[192:195],v[100:103],v[36:39],a[192:195]
v_cvt_pk_bf16_f32 v173,v18,v19
v_cvt_pk_bf16_f32 v174,v20,v21
v_mfma_f32_16x16x32_bf16 a[192:195],v[104:107],v[40:43],a[192:195]
buffer_load_dwordx4 a[68:71],v6,s[28:31],s20 offen sc1
v_mfma_f32_16x16x32_bf16 a[192:195],v[108:111],v[44:47],a[192:195]
ds_read_b64_tr_b16 v[64:65],v1 offset:64
v_cvt_pk_bf16_f32 v175,v22,v23
v_mfma_f32_16x16x32_bf16 a[196:199],v[108:111],v[180:183],a[196:199]
ds_read_b64_tr_b16 v[66:67],v1 offset:5184
ds_read_b64_tr_b16 v[68:69],v1 offset:10304
v_mfma_f32_16x16x32_bf16 a[196:199],v[104:107],v[164:167],a[196:199]
s_cmp_eq_u32 s60,3
s_cselect_b32 s69,0x40000000,0
ds_read_b64_tr_b16 v[70:71],v1 offset:15424
v_mfma_f32_16x16x32_bf16 a[196:199],v[100:103],v[148:151],a[196:199]
ds_read_b64_tr_b16 v[72:73],v1 offset:20544
v_add_u32 v28,s69,v6
v_mfma_f32_16x16x32_bf16 a[196:199],v[96:99],v[132:135],a[196:199]
buffer_store_dwordx4 v[244:247],v6,s[40:43],s82 offen offset:2048 sc1
v_mfma_f32_16x16x32_bf16 a[200:203],v[96:99],v[196:199],a[200:203]
s_lshl_b32 s69,s63,2
ds_read_b64_tr_b16 v[74:75],v1 offset:25664
ds_read_b64_tr_b16 v[76:77],v1 offset:30784
v_mfma_f32_16x16x32_bf16 a[200:203],v[100:103],v[212:215],a[200:203]
ds_read_b64_tr_b16 v[78:79],v1 offset:35904
v_mov_b32 v8,s52
v_mfma_f32_16x16x32_bf16 a[200:203],v[104:107],v[228:231],a[200:203]
buffer_load_dwordx4 a[148:151],v6,s[28:31],s88 offen offset:1024 sc1
v_mfma_f32_16x16x32_bf16 a[200:203],v[108:111],v[244:247],a[200:203]
ds_read_b64_tr_b16 v[80:81],v2 offset:96
ds_read_b64_tr_b16 v[82:83],v2 offset:5216
v_mfma_f32_16x16x32_bf16 a[204:207],v[108:111],v[188:191],a[204:207]
ds_read_b64_tr_b16 v[84:85],v2 offset:10336
v_mov_b32 v9,s53
v_mfma_f32_16x16x32_bf16 a[204:207],v[104:107],v[172:175],a[204:207]
ds_read_b64_tr_b16 v[86:87],v2 offset:15456
v_lshlrev_b32 v10,20,v6
v_mfma_f32_16x16x32_bf16 a[204:207],v[100:103],v[156:159],a[204:207]
s_waitcnt lgkmcnt(7)
ds_read_b64_tr_b16 v[88:89],v2 offset:20576
v_bfe_u32 v3,v6,4,2
v_mfma_f32_16x16x32_bf16 a[204:207],v[96:99],v[140:143],a[204:207]
buffer_load_dwordx4 a[76:79],v6,s[28:31],s21 offen sc1
v_mfma_f32_16x16x32_bf16 a[208:211],v[112:115],v[32:35],a[208:211]
ds_read_b64_tr_b16 v[90:91],v2 offset:25696
ds_read_b64_tr_b16 v[92:93],v2 offset:30816
v_mfma_f32_16x16x32_bf16 a[208:211],v[116:119],v[36:39],a[208:211]
ds_read_b64_tr_b16 v[94:95],v2 offset:35936
v_min_u32 v3,2,v3
v_mfma_f32_16x16x32_bf16 a[208:211],v[120:123],v[40:43],a[208:211]
ds_read_b64_tr_b16 v[96:97],v1 offset:128
v_add3_u32 v3,v3,s64,1
v_mfma_f32_16x16x32_bf16 a[208:211],v[124:127],v[44:47],a[208:211]
ds_read_b64_tr_b16 v[98:99],v1 offset:5248
v_and_b32 v3,3,v3
v_mfma_f32_16x16x32_bf16 a[212:215],v[124:127],v[180:183],a[212:215]
ds_read_b64_tr_b16 v[100:101],v1 offset:10368
ds_read_b64_tr_b16 v[102:103],v1 offset:15488
v_mfma_f32_16x16x32_bf16 a[212:215],v[120:123],v[164:167],a[212:215]
ds_read_b64_tr_b16 v[104:105],v1 offset:20608
v_lshlrev_b32 v3,6,v3
v_mfma_f32_16x16x32_bf16 a[212:215],v[116:119],v[148:151],a[212:215]
buffer_store_dwordx4 v[140:143],v28,s[40:43],s79 offen offset:3072 sc1
)A"
R"A(v_mfma_f32_16x16x32_bf16 a[212:215],v[112:115],v[132:135],a[212:215]
ds_read_b64_tr_b16 v[106:107],v1 offset:25728
ds_read_b64_tr_b16 v[108:109],v1 offset:30848
v_mfma_f32_16x16x32_bf16 a[216:219],v[112:115],v[196:199],a[216:219]
buffer_load_dwordx4 a[132:135],v6,s[28:31],s87 offen offset:1024 sc1
v_mfma_f32_16x16x32_bf16 a[216:219],v[116:119],v[212:215],a[216:219]
ds_read_b64_tr_b16 v[110:111],v1 offset:35968
v_bfe_u32 v56,v6,6,2
v_mfma_f32_16x16x32_bf16 a[216:219],v[120:123],v[228:231],a[216:219]
v_lshrrev_b32 v57,8,v6
v_cmp_eq_u32 s[56:57],v56,v57
v_mfma_f32_16x16x32_bf16 a[216:219],v[124:127],v[244:247],a[216:219]
v_bfe_u32 v56,v6,4,1
v_lshlrev_b32 v56,4,v56
v_mfma_f32_16x16x32_bf16 a[220:223],v[124:127],v[188:191],a[220:223]
ds_read_b64_tr_b16 v[124:125],v2 offset:30880
ds_read_b64_tr_b16 v[126:127],v2 offset:36000
v_mfma_f32_16x16x32_bf16 a[220:223],v[120:123],v[172:175],a[220:223]
buffer_load_dwordx4 a[84:87],v6,s[28:31],s22 offen sc1
v_mfma_f32_16x16x32_bf16 a[220:223],v[116:119],v[156:159],a[220:223]
ds_read_b64_tr_b16 v[116:117],v2 offset:10400
ds_read_b64_tr_b16 v[118:119],v2 offset:15520
v_mfma_f32_16x16x32_bf16 a[220:223],v[112:115],v[140:143],a[220:223]
ds_read_b64_tr_b16 v[112:113],v2 offset:160
v_mov_b32 v57,0x3f80
v_mfma_f32_16x16x32_bf16 a[224:227],v[64:67],v[32:35],a[224:227]
ds_read_b64_tr_b16 v[114:115],v2 offset:5280
v_lshlrev_b32 v56,v56,v57
v_mfma_f32_16x16x32_bf16 a[224:227],v[68:71],v[36:39],a[224:227]
s_waitcnt lgkmcnt(6)
ds_read_b64_tr_b16 v[120:121],v2 offset:20640
v_cndmask_b32 v56,0,v56,s[56:57]
v_mfma_f32_16x16x32_bf16 a[224:227],v[72:75],v[40:43],a[224:227]
ds_read_b64_tr_b16 v[122:123],v2 offset:25760
v_bfe_u32 v57,v6,5,1
v_mfma_f32_16x16x32_bf16 a[224:227],v[76:79],v[44:47],a[224:227]
s_add_u32 s36,s12,s66
s_addc_u32 s37,s13,0
v_cmp_eq_u32 s[56:57],0,v57
v_mfma_f32_16x16x32_bf16 a[228:231],v[76:79],v[180:183],a[228:231]
buffer_load_dwordx4 a[136:139],v6,s[28:31],s87 offen offset:2048 sc1
v_mfma_f32_16x16x32_bf16 a[228:231],v[72:75],v[164:167],a[228:231]
s_and_b32 s37,s37,0xffff
v_cndmask_b32 v48,0,v56,s[56:57]
v_cndmask_b32 v49,v56,0,s[56:57]
v_mfma_f32_16x16x32_bf16 a[228:231],v[68:71],v[148:151],a[228:231]
buffer_store_dwordx4 v[156:159],v28,s[40:43],s80 offen offset:3072 sc1
v_mfma_f32_16x16x32_bf16 a[228:231],v[64:67],v[132:135],a[228:231]
s_mul_i32 s66,s61,307200
v_mov_b32 v58,s52
v_mov_b32 v59,s53
v_mfma_f32_16x16x32_bf16 a[232:235],v[64:67],v[196:199],a[232:235]
s_add_u32 s24,s16,s66
s_addc_u32 s25,s17,0
v_lshlrev_b32 v60,20,v6
v_mfma_f32_16x16x32_bf16 a[232:235],v[68:71],v[212:215],a[232:235]
buffer_load_dwordx4 a[92:95],v6,s[28:31],s23 offen sc1
v_mfma_f32_16x16x32_bf16 a[232:235],v[72:75],v[228:231],a[232:235]
s_mul_i32 s66,s60,16384
s_add_u32 s66,s66,s67
s_add_u32 s79,s66,12288
v_mfma_f32_16x16x32_bf16 a[232:235],v[76:79],v[244:247],a[232:235]
s_add_u32 s80,s66,77824
v_bfe_u32 v61,v6,4,1
v_add3_u32 v61,v61,s63,2
v_mfma_f32_16x16x32_bf16 a[236:239],v[76:79],v[188:191],a[236:239]
s_and_b32 s25,s25,0xffff
ds_read_b64_tr_b16 v[76:77],v1 offset:30912
ds_read_b64_tr_b16 v[78:79],v1 offset:36032
v_mfma_f32_16x16x32_bf16 a[236:239],v[72:75],v[172:175],a[236:239]
s_add_u32 s67,s77,8192
ds_read_b64_tr_b16 v[72:73],v1 offset:20672
v_and_b32 v61,3,v61
L_pd14_%=:
s_cbranch_execz L_trt1_%=
v_mfma_f32_16x16x32_bf16 a[236:239],v[68:71],v[156:159],a[236:239]
ds_read_b64_tr_b16 v[68:69],v1 offset:10432
v_lshlrev_b32 v61,8,v61
v_mfma_f32_16x16x32_bf16 a[236:239],v[64:67],v[140:143],a[236:239]
ds_read_b64_tr_b16 v[64:65],v1 offset:192
v_mov_b32 v50,0
v_mfma_f32_16x16x32_bf16 a[240:243],v[80:83],v[32:35],a[240:243]
ds_read_b64_tr_b16 v[66:67],v1 offset:5312
ds_read_b64_tr_b16 v[70:71],v1 offset:15552
v_mfma_f32_16x16x32_bf16 a[240:243],v[84:87],v[36:39],a[240:243]
buffer_load_dwordx4 a[152:155],v6,s[28:31],s88 offen offset:2048 sc1
v_mfma_f32_16x16x32_bf16 a[240:243],v[88:91],v[40:43],a[240:243]
ds_read_b64_tr_b16 v[74:75],v1 offset:25792
v_mov_b32 v51,0
v_mfma_f32_16x16x32_bf16 a[240:243],v[92:95],v[44:47],a[240:243]
v_mov_b32 v52,0
v_mov_b32 v53,0
v_mfma_f32_16x16x32_bf16 a[244:247],v[92:95],v[180:183],a[244:247]
v_mov_b32 v54,v48
v_mov_b32 v55,v49
v_mfma_f32_16x16x32_bf16 a[244:247],v[88:91],v[164:167],a[244:247]
buffer_store_dwordx4 v[172:175],v28,s[40:43],s81 offen offset:3072 sc1
v_mfma_f32_16x16x32_bf16 a[244:247],v[84:87],v[148:151],a[244:247]
s_add_u32 s81,s66,143360
v_bfe_u32 v56,v6,4,4
v_mul_u32_u24 v56,1280,v56
v_mfma_f32_16x16x32_bf16 a[244:247],v[80:83],v[132:135],a[244:247]
buffer_load_dwordx4 a[124:127],v6,s[28:31],s23 offen offset:1024 sc1
v_mfma_f32_16x16x32_bf16 a[248:251],v[80:83],v[196:199],a[248:251]
v_bfe_u32 v57,v6,8,1
v_lshl_add_u32 v56,v57,5,v56
v_mfma_f32_16x16x32_bf16 a[248:251],v[84:87],v[212:215],a[248:251]
v_lshrrev_b32 v57,9,v6
v_lshl_add_u32 v56,v57,4,v56
v_mfma_f32_16x16x32_bf16 a[248:251],v[88:91],v[228:231],a[248:251]
v_mfma_f32_16x16x32_bf16 a[248:251],v[92:95],v[244:247],a[248:251]
v_mfma_f32_16x16x32_bf16 a[252:255],v[92:95],v[188:191],a[252:255]
ds_read_b64_tr_b16 v[92:93],v2 offset:30944
ds_read_b64_tr_b16 v[94:95],v2 offset:36064
v_mfma_f32_16x16x32_bf16 a[252:255],v[88:91],v[172:175],a[252:255]
ds_read_b64_tr_b16 v[88:89],v2 offset:20704
v_mfma_f32_16x16x32_bf16 a[252:255],v[84:87],v[156:159],a[252:255]
buffer_load_dwordx4 a[168:171],v6,s[28:31],s89 offen offset:2048 sc1
v_mfma_f32_16x16x32_bf16 a[252:255],v[80:83],v[140:143],a[252:255]
ds_read_b64_tr_b16 v[80:81],v2 offset:224
ds_read_b64_tr_b16 v[82:83],v2 offset:5344
v_mfma_f32_16x16x32_bf16 a[0:3],v[96:99],v[32:35],a[0:3]
ds_read_b64_tr_b16 v[84:85],v2 offset:10464
v_mfma_f32_16x16x32_bf16 a[0:3],v[100:103],v[36:39],a[0:3]
ds_read_b64_tr_b16 v[86:87],v2 offset:15584
v_mfma_f32_16x16x32_bf16 a[0:3],v[104:107],v[40:43],a[0:3]
ds_read_b64_tr_b16 v[90:91],v2 offset:25824
v_mfma_f32_16x16x32_bf16 a[0:3],v[108:111],v[44:47],a[0:3]
buffer_load_dwordx4 a[116:119],v6,s[28:31],s22 offen offset:1024 sc1
v_mfma_f32_16x16x32_bf16 a[4:7],v[108:111],v[180:183],a[4:7]
v_mfma_f32_16x16x32_bf16 a[4:7],v[104:107],v[164:167],a[4:7]
buffer_store_dwordx4 v[188:191],v28,s[40:43],s82 offen offset:3072 sc1
v_mfma_f32_16x16x32_bf16 a[4:7],v[100:103],v[148:151],a[4:7]
s_add_u32 s82,s66,208896
s_mul_i32 s66,s64,320
v_mfma_f32_16x16x32_bf16 a[4:7],v[96:99],v[132:135],a[4:7]
v_mfma_f32_16x16x32_bf16 a[8:11],v[96:99],v[196:199],a[8:11]
v_mfma_f32_16x16x32_bf16 a[8:11],v[100:103],v[212:215],a[8:11]
v_mfma_f32_16x16x32_bf16 a[8:11],v[104:107],v[228:231],a[8:11]
v_mfma_f32_16x16x32_bf16 a[8:11],v[108:111],v[244:247],a[8:11]
buffer_load_dwordx4 a[184:187],v6,s[28:31],s90 offen offset:2048 sc1
v_mfma_f32_16x16x32_bf16 a[12:15],v[108:111],v[188:191],a[12:15]
ds_read_b64_tr_b16 v[108:109],v1 offset:30976
ds_read_b64_tr_b16 v[110:111],v1 offset:36096
v_mfma_f32_16x16x32_bf16 a[12:15],v[104:107],v[172:175],a[12:15]
ds_read_b64_tr_b16 v[104:105],v1 offset:20736
v_mfma_f32_16x16x32_bf16 a[12:15],v[100:103],v[156:159],a[12:15]
ds_read_b64_tr_b16 v[100:101],v1 offset:10496
v_mfma_f32_16x16x32_bf16 a[12:15],v[96:99],v[140:143],a[12:15]
s_waitcnt lgkmcnt(4)
ds_read_b64_tr_b16 v[96:97],v1 offset:256
v_mfma_f32_16x16x32_bf16 a[16:19],v[112:115],v[32:35],a[16:19]
buffer_load_dwordx4 a[108:111],v6,s[28:31],s21 offen offset:1024 sc1
v_mfma_f32_16x16x32_bf16 a[16:19],v[116:119],v[36:39],a[16:19]
ds_read_b64_tr_b16 v[98:99],v1 offset:5376
ds_read_b64_tr_b16 v[102:103],v1 offset:15616
v_mfma_f32_16x16x32_bf16 a[16:19],v[120:123],v[40:43],a[16:19]
ds_read_b64_tr_b16 v[106:107],v1 offset:25856
v_add_u32 v1,0xffff6000,v1
v_mfma_f32_16x16x32_bf16 a[16:19],v[124:127],v[44:47],a[16:19]
v_mfma_f32_16x16x32_bf16 a[20:23],v[124:127],v[180:183],a[20:23]
v_mfma_f32_16x16x32_bf16 a[20:23],v[120:123],v[164:167],a[20:23]
v_mfma_f32_16x16x32_bf16 a[20:23],v[116:119],v[148:151],a[20:23]
v_mfma_f32_16x16x32_bf16 a[20:23],v[112:115],v[132:135],a[20:23]
v_mfma_f32_16x16x32_bf16 a[24:27],v[112:115],v[196:199],a[24:27]
buffer_load_dwordx4 a[188:191],v28,s[28:31],s90 offen offset:3072 sc1
v_mfma_f32_16x16x32_bf16 a[24:27],v[116:119],v[212:215],a[24:27]
v_mfma_f32_16x16x32_bf16 a[24:27],v[120:123],v[228:231],a[24:27]
v_mfma_f32_16x16x32_bf16 a[24:27],v[124:127],v[244:247],a[24:27]
v_mfma_f32_16x16x32_bf16 a[28:31],v[124:127],v[188:191],a[28:31]
ds_read_b64_tr_b16 v[124:125],v2 offset:31008
ds_read_b64_tr_b16 v[126:127],v2 offset:36128
v_mfma_f32_16x16x32_bf16 a[28:31],v[120:123],v[172:175],a[28:31]
buffer_load_dwordx4 a[100:103],v6,s[28:31],s20 offen offset:1024 sc1
v_mfma_f32_16x16x32_bf16 a[28:31],v[116:119],v[156:159],a[28:31]
ds_read_b64_tr_b16 v[116:117],v2 offset:10528
ds_read_b64_tr_b16 v[118:119],v2 offset:15648
v_mfma_f32_16x16x32_bf16 a[28:31],v[112:115],v[140:143],a[28:31]
ds_read_b64_tr_b16 v[112:113],v2 offset:288
v_mfma_f32_16x16x32_bf16 v[128:131],v[64:67],v[32:35],v[128:131]
ds_read_b64_tr_b16 v[114:115],v2 offset:5408
v_mfma_f32_16x16x32_bf16 v[128:131],v[68:71],v[36:39],v[128:131]
ds_read_b64_tr_b16 v[120:121],v2 offset:20768
v_mfma_f32_16x16x32_bf16 v[128:131],v[72:75],v[40:43],v[128:131]
ds_read_b64_tr_b16 v[122:123],v2 offset:25888
v_add_u32 v2,0xffff6000,v2
v_mfma_f32_16x16x32_bf16 v[128:131],v[76:79],v[44:47],v[128:131]
v_mfma_f32_16x16x32_bf16 v[136:139],v[76:79],v[180:183],v[136:139]
v_mfma_f32_16x16x32_bf16 v[136:139],v[72:75],v[164:167],v[136:139]
buffer_load_dwordx4 a[172:175],v28,s[28:31],s89 offen offset:3072 sc1
v_mfma_f32_16x16x32_bf16 v[136:139],v[68:71],v[148:151],v[136:139]
v_mfma_f32_16x16x32_bf16 v[136:139],v[64:67],v[132:135],v[136:139]
v_mfma_f32_16x16x32_bf16 v[144:147],v[64:67],v[196:199],v[144:147]
v_mfma_f32_16x16x32_bf16 v[144:147],v[68:71],v[212:215],v[144:147]
v_mfma_f32_16x16x32_bf16 v[144:147],v[72:75],v[228:231],v[144:147]
buffer_load_dwordx4 a[64:67],v6,s[28:31],s20 offen offset:2048 sc1
v_mfma_f32_16x16x32_bf16 v[144:147],v[76:79],v[244:247],v[144:147]
v_mfma_f32_16x16x32_bf16 v[152:155],v[76:79],v[188:191],v[152:155]
v_mfma_f32_16x16x32_bf16 v[152:155],v[72:75],v[172:175],v[152:155]
v_mfma_f32_16x16x32_bf16 v[152:155],v[68:71],v[156:159],v[152:155]
v_mfma_f32_16x16x32_bf16 v[152:155],v[64:67],v[140:143],v[152:155]
s_mov_b64 exec,0
s_cmp_eq_u32 s60,0
s_cbranch_scc1 L_pd16_%=
s_cmp_eq_u32 s60,1
s_cbranch_scc1 L_pd17_%=
s_cmp_eq_u32 s60,2
s_cbranch_scc1 L_pd18_%=
s_branch L_trtg2F_%=
L_trtg2F_%=:
s_mov_b64 exec,-1
s_waitcnt lgkmcnt(0)
s_barrier
v_mfma_f32_16x16x32_bf16 v[160:163],v[80:83],v[32:35],v[160:163]
buffer_load_dwordx4 v24,s[48:51],s83 offen lds
v_mfma_f32_16x16x32_bf16 v[160:163],v[84:87],v[36:39],v[160:163]
ds_read_b64_tr_b16 v[64:65],v1
ds_read_b64_tr_b16 v[66:67],v1 offset:5120
v_mfma_f32_16x16x32_bf16 v[160:163],v[88:91],v[40:43],v[160:163]
ds_read_b64_tr_b16 v[68:69],v1 offset:10240
v_mfma_f32_16x16x32_bf16 v[160:163],v[92:95],v[44:47],v[160:163]
ds_read_b64_tr_b16 v[70:71],v1 offset:15360
v_mfma_f32_16x16x32_bf16 v[168:171],v[92:95],v[180:183],v[168:171]
ds_read_b64_tr_b16 v[72:73],v1 offset:20480
v_mfma_f32_16x16x32_bf16 v[168:171],v[88:91],v[164:167],v[168:171]
buffer_load_dwordx4 a[156:159],v28,s[28:31],s88 offen offset:3072 sc1
v_mfma_f32_16x16x32_bf16 v[168:171],v[84:87],v[148:151],v[168:171]
ds_read_b64_tr_b16 v[74:75],v1 offset:25600
ds_read_b64_tr_b16 v[76:77],v1 offset:30720
v_mfma_f32_16x16x32_bf16 v[168:171],v[80:83],v[132:135],v[168:171]
ds_read_b64_tr_b16 v[78:79],v1 offset:35840
v_mfma_f32_16x16x32_bf16 v[176:179],v[80:83],v[196:199],v[176:179]
v_mfma_f32_16x16x32_bf16 v[176:179],v[84:87],v[212:215],v[176:179]
v_mfma_f32_16x16x32_bf16 v[176:179],v[88:91],v[228:231],v[176:179]
buffer_load_dwordx4 a[72:75],v6,s[28:31],s21 offen offset:2048 sc1
v_mfma_f32_16x16x32_bf16 v[176:179],v[92:95],v[244:247],v[176:179]
v_mfma_f32_16x16x32_bf16 v[184:187],v[92:95],v[188:191],v[184:187]
ds_read_b64_tr_b16 v[92:93],v2 offset:30752
ds_read_b64_tr_b16 v[94:95],v2 offset:35872
v_mfma_f32_16x16x32_bf16 v[184:187],v[88:91],v[172:175],v[184:187]
ds_read_b64_tr_b16 v[88:89],v2 offset:20512
v_mfma_f32_16x16x32_bf16 v[184:187],v[84:87],v[156:159],v[184:187]
ds_read_b64_tr_b16 v[84:85],v2 offset:10272
)A"
R"A(v_mfma_f32_16x16x32_bf16 v[184:187],v[80:83],v[140:143],v[184:187]
buffer_load_dwordx4 a[140:143],v28,s[28:31],s87 offen offset:3072 sc1
v_mfma_f32_16x16x32_bf16 v[192:195],v[96:99],v[32:35],v[192:195]
s_add_u32 s87,s64,3
ds_read_b64_tr_b16 v[80:81],v2 offset:32
ds_read_b64_tr_b16 v[82:83],v2 offset:5152
v_mfma_f32_16x16x32_bf16 v[192:195],v[100:103],v[36:39],v[192:195]
s_and_b32 s87,s87,3
s_lshl_b32 s87,s87,1
ds_read_b64_tr_b16 v[86:87],v2 offset:15392
v_mfma_f32_16x16x32_bf16 v[192:195],v[104:107],v[40:43],v[192:195]
s_lshl_b32 s87,s87,16
s_add_u32 s88,s87,4096
ds_read_b64_tr_b16 v[90:91],v2 offset:25632
v_mfma_f32_16x16x32_bf16 v[192:195],v[108:111],v[44:47],v[192:195]
s_add_u32 s89,s87,8192
s_add_u32 s90,s87,12288
v_mfma_f32_16x16x32_bf16 v[200:203],v[108:111],v[180:183],v[200:203]
buffer_load_dwordx4 a[80:83],v6,s[28:31],s22 offen offset:2048 sc1
v_mfma_f32_16x16x32_bf16 v[200:203],v[104:107],v[164:167],v[200:203]
v_mfma_f32_16x16x32_bf16 v[200:203],v[100:103],v[148:151],v[200:203]
v_mfma_f32_16x16x32_bf16 v[200:203],v[96:99],v[132:135],v[200:203]
v_mfma_f32_16x16x32_bf16 v[208:211],v[96:99],v[196:199],v[208:211]
v_mfma_f32_16x16x32_bf16 v[208:211],v[100:103],v[212:215],v[208:211]
v_mfma_f32_16x16x32_bf16 v[208:211],v[104:107],v[228:231],v[208:211]
v_mfma_f32_16x16x32_bf16 v[208:211],v[108:111],v[244:247],v[208:211]
buffer_load_dwordx4 v25,s[48:51],s83 offen offset:1024 lds
v_mfma_f32_16x16x32_bf16 v[216:219],v[108:111],v[188:191],v[216:219]
ds_read_b64_tr_b16 v[108:109],v1 offset:30784
ds_read_b64_tr_b16 v[110:111],v1 offset:35904
v_mfma_f32_16x16x32_bf16 v[216:219],v[104:107],v[172:175],v[216:219]
ds_read_b64_tr_b16 v[104:105],v1 offset:20544
v_mfma_f32_16x16x32_bf16 v[216:219],v[100:103],v[156:159],v[216:219]
ds_read_b64_tr_b16 v[100:101],v1 offset:10304
v_mfma_f32_16x16x32_bf16 v[216:219],v[96:99],v[140:143],v[216:219]
ds_read_b64_tr_b16 v[96:97],v1 offset:64
v_mfma_f32_16x16x32_bf16 v[224:227],v[112:115],v[32:35],v[224:227]
buffer_load_dwordx4 a[88:91],v6,s[28:31],s23 offen offset:2048 sc1
v_mfma_f32_16x16x32_bf16 v[224:227],v[116:119],v[36:39],v[224:227]
ds_read_b64_tr_b16 v[98:99],v1 offset:5184
ds_read_b64_tr_b16 v[102:103],v1 offset:15424
v_mfma_f32_16x16x32_bf16 v[224:227],v[120:123],v[40:43],v[224:227]
ds_read_b64_tr_b16 v[106:107],v1 offset:25664
v_mfma_f32_16x16x32_bf16 v[224:227],v[124:127],v[44:47],v[224:227]
v_mfma_f32_16x16x32_bf16 v[232:235],v[124:127],v[180:183],v[232:235]
v_mfma_f32_16x16x32_bf16 v[232:235],v[120:123],v[164:167],v[232:235]
v_mfma_f32_16x16x32_bf16 v[232:235],v[116:119],v[148:151],v[232:235]
v_mfma_f32_16x16x32_bf16 v[232:235],v[112:115],v[132:135],v[232:235]
buffer_load_dwordx4 v26,s[48:51],s83 offen offset:2048 lds
v_mfma_f32_16x16x32_bf16 v[240:243],v[112:115],v[196:199],v[240:243]
v_mfma_f32_16x16x32_bf16 v[240:243],v[116:119],v[212:215],v[240:243]
v_mfma_f32_16x16x32_bf16 v[240:243],v[120:123],v[228:231],v[240:243]
v_mfma_f32_16x16x32_bf16 v[240:243],v[124:127],v[244:247],v[240:243]
v_mfma_f32_16x16x32_bf16 v[248:251],v[124:127],v[188:191],v[248:251]
ds_read_b64_tr_b16 v[124:125],v2 offset:30816
ds_read_b64_tr_b16 v[126:127],v2 offset:35936
v_mfma_f32_16x16x32_bf16 v[248:251],v[120:123],v[172:175],v[248:251]
buffer_load_dwordx4 a[120:123],v28,s[28:31],s23 offen offset:3072 sc1
v_mfma_f32_16x16x32_bf16 v[248:251],v[116:119],v[156:159],v[248:251]
ds_read_b64_tr_b16 v[116:117],v2 offset:10336
ds_read_b64_tr_b16 v[118:119],v2 offset:15456
v_mfma_f32_16x16x32_bf16 v[248:251],v[112:115],v[140:143],v[248:251]
s_waitcnt vmcnt(39) lgkmcnt(4)
ds_read_b64_tr_b16 v[112:113],v2 offset:96
v_mfma_f32_16x16x32_bf16 a[192:195],v[64:67],a[128:131],a[192:195]
ds_read_b64_tr_b16 v[114:115],v2 offset:5216
v_mfma_f32_16x16x32_bf16 a[192:195],v[68:71],a[144:147],a[192:195]
s_waitcnt vmcnt(37)
ds_read_b64_tr_b16 v[120:121],v2 offset:20576
v_mfma_f32_16x16x32_bf16 a[192:195],v[72:75],a[160:163],a[192:195]
s_waitcnt vmcnt(35)
ds_read_b64_tr_b16 v[122:123],v2 offset:25696
v_mfma_f32_16x16x32_bf16 a[192:195],v[76:79],a[176:179],a[192:195]
s_waitcnt vmcnt(27)
v_mfma_f32_16x16x32_bf16 a[196:199],v[76:79],a[180:183],a[196:199]
buffer_load_dwordx4 v27,s[48:51],s83 offen offset:3072 lds
v_mfma_f32_16x16x32_bf16 a[196:199],v[72:75],a[164:167],a[196:199]
s_add_u32 m0,s86,126976
v_mfma_f32_16x16x32_bf16 a[196:199],v[68:71],a[148:151],a[196:199]
s_waitcnt vmcnt(24)
v_mfma_f32_16x16x32_bf16 a[196:199],v[64:67],a[132:135],a[196:199]
v_mfma_f32_16x16x32_bf16 a[200:203],v[64:67],a[136:139],a[200:203]
s_waitcnt vmcnt(21)
v_mfma_f32_16x16x32_bf16 a[200:203],v[68:71],a[152:155],a[200:203]
s_waitcnt vmcnt(15)
v_mfma_f32_16x16x32_bf16 a[200:203],v[72:75],a[168:171],a[200:203]
buffer_load_dwordx4 a[112:115],v28,s[28:31],s22 offen offset:3072 sc1
v_mfma_f32_16x16x32_bf16 a[200:203],v[76:79],a[184:187],a[200:203]
s_waitcnt vmcnt(14)
v_mfma_f32_16x16x32_bf16 a[204:207],v[76:79],a[188:191],a[204:207]
s_waitcnt vmcnt(12)
ds_read_b64_tr_b16 v[76:77],v1 offset:30848
ds_read_b64_tr_b16 v[78:79],v1 offset:35968
v_mfma_f32_16x16x32_bf16 a[204:207],v[72:75],a[172:175],a[204:207]
s_waitcnt vmcnt(9)
ds_read_b64_tr_b16 v[72:73],v1 offset:20608
v_mfma_f32_16x16x32_bf16 a[204:207],v[68:71],a[156:159],a[204:207]
s_waitcnt vmcnt(7)
ds_read_b64_tr_b16 v[68:69],v1 offset:10368
v_mfma_f32_16x16x32_bf16 a[204:207],v[64:67],a[140:143],a[204:207]
ds_read_b64_tr_b16 v[64:65],v1 offset:128
v_mfma_f32_16x16x32_bf16 a[208:211],v[80:83],a[128:131],a[208:211]
ds_read_b64_tr_b16 v[66:67],v1 offset:5248
ds_read_b64_tr_b16 v[70:71],v1 offset:15488
v_mfma_f32_16x16x32_bf16 a[208:211],v[84:87],a[144:147],a[208:211]
buffer_load_dwordx4 v7,s[48:51],s83 offen lds
v_mfma_f32_16x16x32_bf16 a[208:211],v[88:91],a[160:163],a[208:211]
s_add_u32 m0,s86,128000
s_add_u32 s83,s64,1
ds_read_b64_tr_b16 v[74:75],v1 offset:25728
v_mfma_f32_16x16x32_bf16 a[208:211],v[92:95],a[176:179],a[208:211]
s_and_b32 s83,s83,3
s_lshl_b32 s83,s83,1
s_or_b32 s83,s83,1
v_mfma_f32_16x16x32_bf16 a[212:215],v[92:95],a[180:183],a[212:215]
s_mul_i32 s83,s83,163840
s_add_u32 s83,s83,s85
v_mfma_f32_16x16x32_bf16 a[212:215],v[88:91],a[164:167],a[212:215]
v_mfma_f32_16x16x32_bf16 a[212:215],v[84:87],a[148:151],a[212:215]
v_mfma_f32_16x16x32_bf16 a[212:215],v[80:83],a[132:135],a[212:215]
buffer_load_dwordx4 a[104:107],v28,s[28:31],s21 offen offset:3072 sc1
v_mfma_f32_16x16x32_bf16 a[216:219],v[80:83],a[136:139],a[216:219]
v_mfma_f32_16x16x32_bf16 a[216:219],v[84:87],a[152:155],a[216:219]
v_mfma_f32_16x16x32_bf16 a[216:219],v[88:91],a[168:171],a[216:219]
v_mfma_f32_16x16x32_bf16 a[216:219],v[92:95],a[184:187],a[216:219]
v_mfma_f32_16x16x32_bf16 a[220:223],v[92:95],a[188:191],a[220:223]
ds_read_b64_tr_b16 v[92:93],v2 offset:30880
ds_read_b64_tr_b16 v[94:95],v2 offset:36000
v_mfma_f32_16x16x32_bf16 a[220:223],v[88:91],a[172:175],a[220:223]
ds_read_b64_tr_b16 v[88:89],v2 offset:20640
v_mfma_f32_16x16x32_bf16 a[220:223],v[84:87],a[156:159],a[220:223]
buffer_load_dwordx4 v24,s[48:51],s84 offen lds
v_mfma_f32_16x16x32_bf16 a[220:223],v[80:83],a[140:143],a[220:223]
ds_read_b64_tr_b16 v[80:81],v2 offset:160
ds_read_b64_tr_b16 v[82:83],v2 offset:5280
s_barrier
v_mfma_f32_16x16x32_bf16 a[224:227],v[96:99],a[128:131],a[224:227]
ds_read_b64_tr_b16 v[84:85],v2 offset:10400
v_mfma_f32_16x16x32_bf16 a[224:227],v[100:103],a[144:147],a[224:227]
ds_read_b64_tr_b16 v[86:87],v2 offset:15520
v_mfma_f32_16x16x32_bf16 a[224:227],v[104:107],a[160:163],a[224:227]
buffer_store_dwordx2 v[8:9],v10,s[44:47],s18 offen offset:1280 sc1
v_mfma_f32_16x16x32_bf16 a[224:227],v[108:111],a[176:179],a[224:227]
ds_read_b64_tr_b16 v[90:91],v2 offset:25760
v_mfma_f32_16x16x32_bf16 a[228:231],v[108:111],a[180:183],a[228:231]
v_mfma_f32_16x16x32_bf16 a[228:231],v[104:107],a[164:167],a[228:231]
v_mfma_f32_16x16x32_bf16 a[228:231],v[100:103],a[148:151],a[228:231]
v_mfma_f32_16x16x32_bf16 a[228:231],v[96:99],a[132:135],a[228:231]
buffer_load_dwordx4 a[96:99],v28,s[28:31],s20 offen offset:3072 sc1
v_mfma_f32_16x16x32_bf16 a[232:235],v[96:99],a[136:139],a[232:235]
s_add_u32 s20,s64,1
s_and_b32 s20,s20,3
s_lshl_b32 s20,s20,1
v_mfma_f32_16x16x32_bf16 a[232:235],v[100:103],a[152:155],a[232:235]
s_or_b32 s20,s20,1
s_lshl_b32 s20,s20,16
s_add_u32 s21,s20,4096
v_mfma_f32_16x16x32_bf16 a[232:235],v[104:107],a[168:171],a[232:235]
s_add_u32 s22,s20,8192
s_add_u32 s23,s20,12288
v_mfma_f32_16x16x32_bf16 a[232:235],v[108:111],a[184:187],a[232:235]
v_mfma_f32_16x16x32_bf16 a[236:239],v[108:111],a[188:191],a[236:239]
ds_read_b64_tr_b16 v[108:109],v1 offset:30912
ds_read_b64_tr_b16 v[110:111],v1 offset:36032
v_mfma_f32_16x16x32_bf16 a[236:239],v[104:107],a[172:175],a[236:239]
ds_read_b64_tr_b16 v[104:105],v1 offset:20672
v_mfma_f32_16x16x32_bf16 a[236:239],v[100:103],a[156:159],a[236:239]
buffer_load_dwordx4 v25,s[48:51],s84 offen offset:1024 lds
v_mfma_f32_16x16x32_bf16 a[236:239],v[96:99],a[140:143],a[236:239]
s_waitcnt lgkmcnt(3)
ds_read_b64_tr_b16 v[96:97],v1 offset:192
ds_read_b64_tr_b16 v[98:99],v1 offset:5312
v_mfma_f32_16x16x32_bf16 a[240:243],v[112:115],a[128:131],a[240:243]
ds_read_b64_tr_b16 v[100:101],v1 offset:10432
v_mfma_f32_16x16x32_bf16 a[240:243],v[116:119],a[144:147],a[240:243]
ds_read_b64_tr_b16 v[102:103],v1 offset:15552
v_mfma_f32_16x16x32_bf16 a[240:243],v[120:123],a[160:163],a[240:243]
ds_read_b64_tr_b16 v[106:107],v1 offset:25792
v_mfma_f32_16x16x32_bf16 a[240:243],v[124:127],a[176:179],a[240:243]
v_mfma_f32_16x16x32_bf16 a[244:247],v[124:127],a[180:183],a[244:247]
v_mfma_f32_16x16x32_bf16 a[244:247],v[120:123],a[164:167],a[244:247]
buffer_load_dwordx4 v26,s[48:51],s84 offen offset:2048 lds
v_mfma_f32_16x16x32_bf16 a[244:247],v[116:119],a[148:151],a[244:247]
v_mfma_f32_16x16x32_bf16 a[244:247],v[112:115],a[132:135],a[244:247]
v_mfma_f32_16x16x32_bf16 a[248:251],v[112:115],a[136:139],a[248:251]
v_mfma_f32_16x16x32_bf16 a[248:251],v[116:119],a[152:155],a[248:251]
v_mfma_f32_16x16x32_bf16 a[248:251],v[120:123],a[168:171],a[248:251]
v_mfma_f32_16x16x32_bf16 a[248:251],v[124:127],a[184:187],a[248:251]
v_mfma_f32_16x16x32_bf16 a[252:255],v[124:127],a[188:191],a[252:255]
buffer_load_dwordx4 v27,s[48:51],s84 offen offset:3072 lds
v_mfma_f32_16x16x32_bf16 a[252:255],v[120:123],a[172:175],a[252:255]
s_add_u32 m0,s86,132096
ds_read_b64_tr_b16 v[120:121],v2 offset:20704
ds_read_b64_tr_b16 v[122:123],v2 offset:25824
v_mfma_f32_16x16x32_bf16 a[252:255],v[116:119],a[156:159],a[252:255]
ds_read_b64_tr_b16 v[116:117],v2 offset:10464
v_mfma_f32_16x16x32_bf16 a[252:255],v[112:115],a[140:143],a[252:255]
ds_read_b64_tr_b16 v[112:113],v2 offset:224
v_mfma_f32_16x16x32_bf16 a[0:3],v[64:67],a[128:131],a[0:3]
ds_read_b64_tr_b16 v[114:115],v2 offset:5344
v_mfma_f32_16x16x32_bf16 a[0:3],v[68:71],a[144:147],a[0:3]
ds_read_b64_tr_b16 v[118:119],v2 offset:15584
ds_read_b64_tr_b16 v[124:125],v2 offset:30944
v_mfma_f32_16x16x32_bf16 a[0:3],v[72:75],a[160:163],a[0:3]
ds_read_b64_tr_b16 v[126:127],v2 offset:36064
v_mfma_f32_16x16x32_bf16 a[0:3],v[76:79],a[176:179],a[0:3]
buffer_load_dwordx4 v7,s[48:51],s84 offen lds
v_mfma_f32_16x16x32_bf16 a[4:7],v[76:79],a[180:183],a[4:7]
s_add_u32 m0,s86,81920
s_add_u32 s84,s83,20480
v_mfma_f32_16x16x32_bf16 a[4:7],v[72:75],a[164:167],a[4:7]
v_mfma_f32_16x16x32_bf16 a[4:7],v[68:71],a[148:151],a[4:7]
v_mfma_f32_16x16x32_bf16 a[4:7],v[64:67],a[132:135],a[4:7]
v_mfma_f32_16x16x32_bf16 a[8:11],v[64:67],a[136:139],a[8:11]
v_mfma_f32_16x16x32_bf16 a[8:11],v[68:71],a[152:155],a[8:11]
v_mfma_f32_16x16x32_bf16 a[8:11],v[72:75],a[168:171],a[8:11]
buffer_load_dwordx4 v24,s[48:51],s83 offen lds
v_mfma_f32_16x16x32_bf16 a[8:11],v[76:79],a[184:187],a[8:11]
v_mfma_f32_16x16x32_bf16 a[12:15],v[76:79],a[188:191],a[12:15]
ds_read_b64_tr_b16 v[76:77],v1 offset:30976
ds_read_b64_tr_b16 v[78:79],v1 offset:36096
v_mfma_f32_16x16x32_bf16 a[12:15],v[72:75],a[172:175],a[12:15]
ds_read_b64_tr_b16 v[72:73],v1 offset:20736
v_mfma_f32_16x16x32_bf16 a[12:15],v[68:71],a[156:159],a[12:15]
ds_read_b64_tr_b16 v[68:69],v1 offset:10496
v_mfma_f32_16x16x32_bf16 a[12:15],v[64:67],a[140:143],a[12:15]
ds_read_b64_tr_b16 v[64:65],v1 offset:256
)A"
R"A(v_mfma_f32_16x16x32_bf16 a[16:19],v[80:83],a[128:131],a[16:19]
ds_read_b64_tr_b16 v[66:67],v1 offset:5376
ds_read_b64_tr_b16 v[70:71],v1 offset:15616
v_mfma_f32_16x16x32_bf16 a[16:19],v[84:87],a[144:147],a[16:19]
buffer_load_dwordx4 v25,s[48:51],s83 offen offset:1024 lds
v_mfma_f32_16x16x32_bf16 a[16:19],v[88:91],a[160:163],a[16:19]
ds_read_b64_tr_b16 v[74:75],v1 offset:25856
v_add_u32 v1,0xffff6000,v1
v_mfma_f32_16x16x32_bf16 a[16:19],v[92:95],a[176:179],a[16:19]
v_mfma_f32_16x16x32_bf16 a[20:23],v[92:95],a[180:183],a[20:23]
v_mfma_f32_16x16x32_bf16 a[20:23],v[88:91],a[164:167],a[20:23]
v_mfma_f32_16x16x32_bf16 a[20:23],v[84:87],a[148:151],a[20:23]
v_mfma_f32_16x16x32_bf16 a[20:23],v[80:83],a[132:135],a[20:23]
v_mfma_f32_16x16x32_bf16 a[24:27],v[80:83],a[136:139],a[24:27]
buffer_load_dwordx4 v26,s[48:51],s83 offen offset:2048 lds
v_mfma_f32_16x16x32_bf16 a[24:27],v[84:87],a[152:155],a[24:27]
v_mfma_f32_16x16x32_bf16 a[24:27],v[88:91],a[168:171],a[24:27]
v_mfma_f32_16x16x32_bf16 a[24:27],v[92:95],a[184:187],a[24:27]
v_mfma_f32_16x16x32_bf16 a[28:31],v[92:95],a[188:191],a[28:31]
ds_read_b64_tr_b16 v[92:93],v2 offset:31008
ds_read_b64_tr_b16 v[94:95],v2 offset:36128
v_mfma_f32_16x16x32_bf16 a[28:31],v[88:91],a[172:175],a[28:31]
ds_read_b64_tr_b16 v[88:89],v2 offset:20768
v_mfma_f32_16x16x32_bf16 a[28:31],v[84:87],a[156:159],a[28:31]
s_waitcnt lgkmcnt(3)
ds_read_b64_tr_b16 v[84:85],v2 offset:10528
v_mfma_f32_16x16x32_bf16 a[28:31],v[80:83],a[140:143],a[28:31]
buffer_load_dwordx4 v27,s[48:51],s83 offen offset:3072 lds
v_mfma_f32_16x16x32_bf16 v[128:131],v[96:99],a[128:131],v[128:131]
s_add_u32 m0,s86,86016
ds_read_b64_tr_b16 v[80:81],v2 offset:288
ds_read_b64_tr_b16 v[82:83],v2 offset:5408
v_mfma_f32_16x16x32_bf16 v[128:131],v[100:103],a[144:147],v[128:131]
ds_read_b64_tr_b16 v[86:87],v2 offset:15648
v_mfma_f32_16x16x32_bf16 v[128:131],v[104:107],a[160:163],v[128:131]
ds_read_b64_tr_b16 v[90:91],v2 offset:25888
v_add_u32 v2,0xffff6000,v2
v_mfma_f32_16x16x32_bf16 v[128:131],v[108:111],a[176:179],v[128:131]
v_mfma_f32_16x16x32_bf16 v[136:139],v[108:111],a[180:183],v[136:139]
v_mfma_f32_16x16x32_bf16 v[136:139],v[104:107],a[164:167],v[136:139]
v_mfma_f32_16x16x32_bf16 v[136:139],v[100:103],a[148:151],v[136:139]
buffer_load_dwordx4 v7,s[48:51],s83 offen lds
v_mfma_f32_16x16x32_bf16 v[136:139],v[96:99],a[132:135],v[136:139]
s_add_u32 m0,s86,87040
s_add_u32 s83,s64,2
s_and_b32 s83,s83,3
v_mfma_f32_16x16x32_bf16 v[144:147],v[96:99],a[136:139],v[144:147]
s_lshl_b32 s83,s83,1
s_or_b32 s83,s83,1
s_mul_i32 s83,s83,163840
v_mfma_f32_16x16x32_bf16 v[144:147],v[100:103],a[152:155],v[144:147]
s_add_u32 s83,s83,s85
v_mfma_f32_16x16x32_bf16 v[144:147],v[104:107],a[168:171],v[144:147]
v_mfma_f32_16x16x32_bf16 v[144:147],v[108:111],a[184:187],v[144:147]
v_mfma_f32_16x16x32_bf16 v[152:155],v[108:111],a[188:191],v[152:155]
ds_read_b64_tr_b16 v[108:109],v1 offset:30720
ds_read_b64_tr_b16 v[110:111],v1 offset:35840
v_mfma_f32_16x16x32_bf16 v[152:155],v[104:107],a[172:175],v[152:155]
buffer_load_dwordx4 v24,s[48:51],s84 offen lds
v_mfma_f32_16x16x32_bf16 v[152:155],v[100:103],a[156:159],v[152:155]
ds_read_b64_tr_b16 v[100:101],v1 offset:10240
ds_read_b64_tr_b16 v[102:103],v1 offset:15360
v_mfma_f32_16x16x32_bf16 v[152:155],v[96:99],a[140:143],v[152:155]
ds_read_b64_tr_b16 v[96:97],v1
s_waitcnt lgkmcnt(5)
s_barrier
v_mfma_f32_16x16x32_bf16 v[160:163],v[112:115],a[128:131],v[160:163]
ds_read_b64_tr_b16 v[98:99],v1 offset:5120
v_mfma_f32_16x16x32_bf16 v[160:163],v[116:119],a[144:147],v[160:163]
ds_read_b64_tr_b16 v[104:105],v1 offset:20480
v_mfma_f32_16x16x32_bf16 v[160:163],v[120:123],a[160:163],v[160:163]
ds_read_b64_tr_b16 v[106:107],v1 offset:25600
v_mfma_f32_16x16x32_bf16 v[160:163],v[124:127],a[176:179],v[160:163]
buffer_load_dwordx4 v25,s[48:51],s84 offen offset:1024 lds
v_mfma_f32_16x16x32_bf16 v[168:171],v[124:127],a[180:183],v[168:171]
v_mfma_f32_16x16x32_bf16 v[168:171],v[120:123],a[164:167],v[168:171]
v_mfma_f32_16x16x32_bf16 v[168:171],v[116:119],a[148:151],v[168:171]
v_mfma_f32_16x16x32_bf16 v[168:171],v[112:115],a[132:135],v[168:171]
v_mfma_f32_16x16x32_bf16 v[176:179],v[112:115],a[136:139],v[176:179]
v_mfma_f32_16x16x32_bf16 v[176:179],v[116:119],a[152:155],v[176:179]
v_mfma_f32_16x16x32_bf16 v[176:179],v[120:123],a[168:171],v[176:179]
buffer_load_dwordx4 v26,s[48:51],s84 offen offset:2048 lds
v_mfma_f32_16x16x32_bf16 v[176:179],v[124:127],a[184:187],v[176:179]
v_mfma_f32_16x16x32_bf16 v[184:187],v[124:127],a[188:191],v[184:187]
ds_read_b64_tr_b16 v[124:125],v2 offset:30752
ds_read_b64_tr_b16 v[126:127],v2 offset:35872
v_mfma_f32_16x16x32_bf16 v[184:187],v[120:123],a[172:175],v[184:187]
ds_read_b64_tr_b16 v[120:121],v2 offset:20512
v_mfma_f32_16x16x32_bf16 v[184:187],v[116:119],a[156:159],v[184:187]
ds_read_b64_tr_b16 v[116:117],v2 offset:10272
v_mfma_f32_16x16x32_bf16 v[184:187],v[112:115],a[140:143],v[184:187]
ds_read_b64_tr_b16 v[112:113],v2 offset:32
v_mfma_f32_16x16x32_bf16 v[192:195],v[64:67],a[128:131],v[192:195]
ds_read_b64_tr_b16 v[114:115],v2 offset:5152
ds_read_b64_tr_b16 v[118:119],v2 offset:15392
v_mfma_f32_16x16x32_bf16 v[192:195],v[68:71],a[144:147],v[192:195]
buffer_load_dwordx4 v27,s[48:51],s84 offen offset:3072 lds
v_mfma_f32_16x16x32_bf16 v[192:195],v[72:75],a[160:163],v[192:195]
s_add_u32 m0,s86,91136
ds_read_b64_tr_b16 v[122:123],v2 offset:25632
v_mfma_f32_16x16x32_bf16 v[192:195],v[76:79],a[176:179],v[192:195]
v_mfma_f32_16x16x32_bf16 v[200:203],v[76:79],a[180:183],v[200:203]
v_mfma_f32_16x16x32_bf16 v[200:203],v[72:75],a[164:167],v[200:203]
v_mfma_f32_16x16x32_bf16 v[200:203],v[68:71],a[148:151],v[200:203]
v_mfma_f32_16x16x32_bf16 v[200:203],v[64:67],a[132:135],v[200:203]
v_mfma_f32_16x16x32_bf16 v[208:211],v[64:67],a[136:139],v[208:211]
buffer_load_dwordx4 v7,s[48:51],s84 offen lds
v_mfma_f32_16x16x32_bf16 v[208:211],v[68:71],a[152:155],v[208:211]
s_add_u32 m0,s86,40960
s_add_u32 s84,s83,20480
v_mfma_f32_16x16x32_bf16 v[208:211],v[72:75],a[168:171],v[208:211]
v_mfma_f32_16x16x32_bf16 v[208:211],v[76:79],a[184:187],v[208:211]
v_mfma_f32_16x16x32_bf16 v[216:219],v[76:79],a[188:191],v[216:219]
ds_read_b64_tr_b16 v[76:77],v1 offset:30784
ds_read_b64_tr_b16 v[78:79],v1 offset:35904
v_mfma_f32_16x16x32_bf16 v[216:219],v[72:75],a[172:175],v[216:219]
buffer_load_dwordx2 v[4:5],v3,s[44:47],s91 offen offset:1280 sc1
v_mfma_f32_16x16x32_bf16 v[216:219],v[68:71],a[156:159],v[216:219]
ds_read_b64_tr_b16 v[68:69],v1 offset:10304
ds_read_b64_tr_b16 v[70:71],v1 offset:15424
v_mfma_f32_16x16x32_bf16 v[216:219],v[64:67],a[140:143],v[216:219]
ds_read_b64_tr_b16 v[64:65],v1 offset:64
v_mfma_f32_16x16x32_bf16 v[224:227],v[80:83],a[128:131],v[224:227]
ds_read_b64_tr_b16 v[66:67],v1 offset:5184
v_mfma_f32_16x16x32_bf16 v[224:227],v[84:87],a[144:147],v[224:227]
ds_read_b64_tr_b16 v[72:73],v1 offset:20544
v_mfma_f32_16x16x32_bf16 v[224:227],v[88:91],a[160:163],v[224:227]
buffer_load_dwordx4 v[32:35],v6,s[28:31],s87 offen sc1
v_mfma_f32_16x16x32_bf16 v[224:227],v[92:95],a[176:179],v[224:227]
ds_read_b64_tr_b16 v[74:75],v1 offset:25664
v_mfma_f32_16x16x32_bf16 v[232:235],v[92:95],a[180:183],v[232:235]
v_mfma_f32_16x16x32_bf16 v[232:235],v[88:91],a[164:167],v[232:235]
v_mfma_f32_16x16x32_bf16 v[232:235],v[84:87],a[148:151],v[232:235]
v_mfma_f32_16x16x32_bf16 v[232:235],v[80:83],a[132:135],v[232:235]
buffer_load_dwordx4 v[36:39],v6,s[28:31],s88 offen sc1
v_mfma_f32_16x16x32_bf16 v[240:243],v[80:83],a[136:139],v[240:243]
v_mfma_f32_16x16x32_bf16 v[240:243],v[84:87],a[152:155],v[240:243]
v_mfma_f32_16x16x32_bf16 v[240:243],v[88:91],a[168:171],v[240:243]
v_mfma_f32_16x16x32_bf16 v[240:243],v[92:95],a[184:187],v[240:243]
v_mfma_f32_16x16x32_bf16 v[248:251],v[92:95],a[188:191],v[248:251]
buffer_load_dwordx4 v[40:43],v6,s[28:31],s89 offen sc1
v_mfma_f32_16x16x32_bf16 v[248:251],v[88:91],a[172:175],v[248:251]
ds_read_b64_tr_b16 v[88:89],v2 offset:20576
ds_read_b64_tr_b16 v[90:91],v2 offset:25696
v_mfma_f32_16x16x32_bf16 v[248:251],v[84:87],a[156:159],v[248:251]
L_pd15_%=:
s_cbranch_execz L_trt1_%=
ds_read_b64_tr_b16 v[84:85],v2 offset:10336
v_mfma_f32_16x16x32_bf16 v[248:251],v[80:83],a[140:143],v[248:251]
s_waitcnt lgkmcnt(3)
ds_read_b64_tr_b16 v[80:81],v2 offset:96
v_mfma_f32_16x16x32_bf16 a[192:195],v[96:99],a[68:71],a[192:195]
ds_read_b64_tr_b16 v[82:83],v2 offset:5216
v_mfma_f32_16x16x32_bf16 a[192:195],v[100:103],a[76:79],a[192:195]
buffer_load_dwordx4 v[44:47],v6,s[28:31],s90 offen sc1
v_mfma_f32_16x16x32_bf16 a[192:195],v[104:107],a[84:87],a[192:195]
ds_read_b64_tr_b16 v[86:87],v2 offset:15456
ds_read_b64_tr_b16 v[92:93],v2 offset:30816
v_mfma_f32_16x16x32_bf16 a[192:195],v[108:111],a[92:95],a[192:195]
ds_read_b64_tr_b16 v[94:95],v2 offset:35936
v_mfma_f32_16x16x32_bf16 a[196:199],v[108:111],a[124:127],a[196:199]
v_mfma_f32_16x16x32_bf16 a[196:199],v[104:107],a[116:119],a[196:199]
v_mfma_f32_16x16x32_bf16 a[196:199],v[100:103],a[108:111],a[196:199]
buffer_load_dwordx4 v[180:183],v6,s[28:31],s90 offen offset:1024 sc1
v_mfma_f32_16x16x32_bf16 a[196:199],v[96:99],a[100:103],a[196:199]
v_mfma_f32_16x16x32_bf16 a[200:203],v[96:99],a[64:67],a[200:203]
v_mfma_f32_16x16x32_bf16 a[200:203],v[100:103],a[72:75],a[200:203]
s_waitcnt vmcnt(31)
v_mfma_f32_16x16x32_bf16 a[200:203],v[104:107],a[80:83],a[200:203]
s_waitcnt vmcnt(27)
v_mfma_f32_16x16x32_bf16 a[200:203],v[108:111],a[88:91],a[200:203]
buffer_load_dwordx4 v[212:215],v6,s[28:31],s88 offen offset:2048 sc1
v_mfma_f32_16x16x32_bf16 a[204:207],v[108:111],a[120:123],a[204:207]
s_waitcnt vmcnt(26)
ds_read_b64_tr_b16 v[108:109],v1 offset:30848
ds_read_b64_tr_b16 v[110:111],v1 offset:35968
v_mfma_f32_16x16x32_bf16 a[204:207],v[104:107],a[112:115],a[204:207]
s_waitcnt vmcnt(24)
ds_read_b64_tr_b16 v[104:105],v1 offset:20608
v_mfma_f32_16x16x32_bf16 a[204:207],v[100:103],a[104:107],a[204:207]
s_waitcnt vmcnt(21)
ds_read_b64_tr_b16 v[100:101],v1 offset:10368
v_mfma_f32_16x16x32_bf16 a[204:207],v[96:99],a[96:99],a[204:207]
ds_read_b64_tr_b16 v[96:97],v1 offset:128
v_mfma_f32_16x16x32_bf16 a[208:211],v[112:115],a[68:71],a[208:211]
buffer_load_dwordx4 v[164:167],v6,s[28:31],s89 offen offset:1024 sc1
v_mfma_f32_16x16x32_bf16 a[208:211],v[116:119],a[76:79],a[208:211]
ds_read_b64_tr_b16 v[98:99],v1 offset:5248
ds_read_b64_tr_b16 v[102:103],v1 offset:15488
v_mfma_f32_16x16x32_bf16 a[208:211],v[120:123],a[84:87],a[208:211]
ds_read_b64_tr_b16 v[106:107],v1 offset:25728
v_mfma_f32_16x16x32_bf16 a[208:211],v[124:127],a[92:95],a[208:211]
v_mfma_f32_16x16x32_bf16 a[212:215],v[124:127],a[124:127],a[212:215]
v_mfma_f32_16x16x32_bf16 a[212:215],v[120:123],a[116:119],a[212:215]
buffer_load_dwordx4 v[228:231],v6,s[28:31],s89 offen offset:2048 sc1
v_mfma_f32_16x16x32_bf16 a[212:215],v[116:119],a[108:111],a[212:215]
v_mfma_f32_16x16x32_bf16 a[212:215],v[112:115],a[100:103],a[212:215]
v_mfma_f32_16x16x32_bf16 a[216:219],v[112:115],a[64:67],a[216:219]
v_mfma_f32_16x16x32_bf16 a[216:219],v[116:119],a[72:75],a[216:219]
v_mfma_f32_16x16x32_bf16 a[216:219],v[120:123],a[80:83],a[216:219]
buffer_load_dwordx4 v[148:151],v6,s[28:31],s88 offen offset:1024 sc1
v_mfma_f32_16x16x32_bf16 a[216:219],v[124:127],a[88:91],a[216:219]
v_mfma_f32_16x16x32_bf16 a[220:223],v[124:127],a[120:123],a[220:223]
ds_read_b64_tr_b16 v[124:125],v2 offset:30880
ds_read_b64_tr_b16 v[126:127],v2 offset:36000
v_mfma_f32_16x16x32_bf16 a[220:223],v[120:123],a[112:115],a[220:223]
ds_read_b64_tr_b16 v[120:121],v2 offset:20640
v_mfma_f32_16x16x32_bf16 a[220:223],v[116:119],a[104:107],a[220:223]
ds_read_b64_tr_b16 v[116:117],v2 offset:10400
v_mfma_f32_16x16x32_bf16 a[220:223],v[112:115],a[96:99],a[220:223]
buffer_load_dwordx4 v[132:135],v6,s[28:31],s87 offen offset:1024 sc1
v_mfma_f32_16x16x32_bf16 a[224:227],v[64:67],a[68:71],a[224:227]
ds_read_b64_tr_b16 v[112:113],v2 offset:160
ds_read_b64_tr_b16 v[114:115],v2 offset:5280
v_mfma_f32_16x16x32_bf16 a[224:227],v[68:71],a[76:79],a[224:227]
ds_read_b64_tr_b16 v[118:119],v2 offset:15520
v_mfma_f32_16x16x32_bf16 a[224:227],v[72:75],a[84:87],a[224:227]
ds_read_b64_tr_b16 v[122:123],v2 offset:25760
)A"
R"A(v_mfma_f32_16x16x32_bf16 a[224:227],v[76:79],a[92:95],a[224:227]
v_mfma_f32_16x16x32_bf16 a[228:231],v[76:79],a[124:127],a[228:231]
buffer_load_dwordx4 v[244:247],v6,s[28:31],s90 offen offset:2048 sc1
v_mfma_f32_16x16x32_bf16 a[228:231],v[72:75],a[116:119],a[228:231]
v_mfma_f32_16x16x32_bf16 a[228:231],v[68:71],a[108:111],a[228:231]
v_mfma_f32_16x16x32_bf16 a[228:231],v[64:67],a[100:103],a[228:231]
v_mfma_f32_16x16x32_bf16 a[232:235],v[64:67],a[64:67],a[232:235]
v_mfma_f32_16x16x32_bf16 a[232:235],v[68:71],a[72:75],a[232:235]
buffer_load_dwordx4 v[188:191],v28,s[28:31],s90 offen offset:3072 sc1
v_mfma_f32_16x16x32_bf16 a[232:235],v[72:75],a[80:83],a[232:235]
s_waitcnt vmcnt(12)
v_cmp_ne_u32 s[56:57],s52,v4
v_cmp_ne_u32 s[58:59],s53,v5
v_mfma_f32_16x16x32_bf16 a[232:235],v[76:79],a[88:91],a[232:235]
s_or_b64 s[56:57],s[56:57],s[58:59]
s_cbranch_scc1 L_fr2_%=
L_fo2_%=:
v_mov_b32 v3,0
v_mfma_f32_16x16x32_bf16 a[236:239],v[76:79],a[120:123],a[236:239]
s_add_u32 s91,s64,3
ds_read_b64_tr_b16 v[76:77],v1 offset:30912
ds_read_b64_tr_b16 v[78:79],v1 offset:36032
v_mfma_f32_16x16x32_bf16 a[236:239],v[72:75],a[112:115],a[236:239]
s_and_b32 s91,s91,3
s_add_u32 s91,s91,s69
ds_read_b64_tr_b16 v[72:73],v1 offset:20672
v_mfma_f32_16x16x32_bf16 a[236:239],v[68:71],a[104:107],a[236:239]
buffer_load_dwordx4 a[128:131],v6,s[28:31],s20 offen sc1
v_mfma_f32_16x16x32_bf16 a[236:239],v[64:67],a[96:99],a[236:239]
s_add_u32 s91,s91,8
ds_read_b64_tr_b16 v[64:65],v1 offset:192
ds_read_b64_tr_b16 v[66:67],v1 offset:5312
s_waitcnt lgkmcnt(5)
v_mfma_f32_16x16x32_bf16 a[240:243],v[80:83],a[68:71],a[240:243]
s_lshl_b32 s91,s91,6
s_and_b32 s69,s63,s60
ds_read_b64_tr_b16 v[68:69],v1 offset:10432
v_mfma_f32_16x16x32_bf16 a[240:243],v[84:87],a[76:79],a[240:243]
s_cmp_eq_u32 s69,3
s_cselect_b32 s69,0x40000000,0
ds_read_b64_tr_b16 v[70:71],v1 offset:15552
v_mfma_f32_16x16x32_bf16 a[240:243],v[88:91],a[84:87],a[240:243]
s_add_u32 s68,s68,s69
ds_read_b64_tr_b16 v[74:75],v1 offset:25792
v_mfma_f32_16x16x32_bf16 a[240:243],v[92:95],a[92:95],a[240:243]
buffer_load_dwordx4 v[196:199],v6,s[28:31],s87 offen offset:2048 sc1
v_mfma_f32_16x16x32_bf16 a[244:247],v[92:95],a[124:127],a[244:247]
v_mfma_f32_16x16x32_bf16 a[244:247],v[88:91],a[116:119],a[244:247]
v_mfma_f32_16x16x32_bf16 a[244:247],v[84:87],a[108:111],a[244:247]
v_mfma_f32_16x16x32_bf16 a[244:247],v[80:83],a[100:103],a[244:247]
v_mfma_f32_16x16x32_bf16 a[248:251],v[80:83],a[64:67],a[248:251]
buffer_load_dwordx4 v[172:175],v28,s[28:31],s89 offen offset:3072 sc1
v_mfma_f32_16x16x32_bf16 a[248:251],v[84:87],a[72:75],a[248:251]
v_mfma_f32_16x16x32_bf16 a[248:251],v[88:91],a[80:83],a[248:251]
v_mfma_f32_16x16x32_bf16 a[248:251],v[92:95],a[88:91],a[248:251]
v_mfma_f32_16x16x32_bf16 a[252:255],v[92:95],a[120:123],a[252:255]
ds_read_b64_tr_b16 v[92:93],v2 offset:30944
ds_read_b64_tr_b16 v[94:95],v2 offset:36064
v_mfma_f32_16x16x32_bf16 a[252:255],v[88:91],a[112:115],a[252:255]
buffer_load_dwordx4 a[144:147],v6,s[28:31],s21 offen sc1
v_mfma_f32_16x16x32_bf16 a[252:255],v[84:87],a[104:107],a[252:255]
ds_read_b64_tr_b16 v[84:85],v2 offset:10464
ds_read_b64_tr_b16 v[86:87],v2 offset:15584
v_mfma_f32_16x16x32_bf16 a[252:255],v[80:83],a[96:99],a[252:255]
ds_read_b64_tr_b16 v[80:81],v2 offset:224
v_mfma_f32_16x16x32_bf16 a[0:3],v[96:99],a[68:71],a[0:3]
ds_read_b64_tr_b16 v[82:83],v2 offset:5344
v_mfma_f32_16x16x32_bf16 a[0:3],v[100:103],a[76:79],a[0:3]
ds_read_b64_tr_b16 v[88:89],v2 offset:20704
v_mfma_f32_16x16x32_bf16 a[0:3],v[104:107],a[84:87],a[0:3]
buffer_load_dwordx4 v[156:159],v28,s[28:31],s88 offen offset:3072 sc1
v_mfma_f32_16x16x32_bf16 a[0:3],v[108:111],a[92:95],a[0:3]
ds_read_b64_tr_b16 v[90:91],v2 offset:25824
v_mfma_f32_16x16x32_bf16 a[4:7],v[108:111],a[124:127],a[4:7]
v_mfma_f32_16x16x32_bf16 a[4:7],v[104:107],a[116:119],a[4:7]
v_mfma_f32_16x16x32_bf16 a[4:7],v[100:103],a[108:111],a[4:7]
v_mfma_f32_16x16x32_bf16 a[4:7],v[96:99],a[100:103],a[4:7]
buffer_load_dwordx4 a[160:163],v6,s[28:31],s22 offen sc1
v_mfma_f32_16x16x32_bf16 a[8:11],v[96:99],a[64:67],a[8:11]
v_mfma_f32_16x16x32_bf16 a[8:11],v[100:103],a[72:75],a[8:11]
v_mfma_f32_16x16x32_bf16 a[8:11],v[104:107],a[80:83],a[8:11]
v_mfma_f32_16x16x32_bf16 a[8:11],v[108:111],a[88:91],a[8:11]
v_mfma_f32_16x16x32_bf16 a[12:15],v[108:111],a[120:123],a[12:15]
buffer_load_dwordx4 v[140:143],v28,s[28:31],s87 offen offset:3072 sc1
v_mfma_f32_16x16x32_bf16 a[12:15],v[104:107],a[112:115],a[12:15]
s_add_u32 s87,s64,2
ds_read_b64_tr_b16 v[104:105],v1 offset:20736
ds_read_b64_tr_b16 v[106:107],v1 offset:25856
v_mfma_f32_16x16x32_bf16 a[12:15],v[100:103],a[104:107],a[12:15]
s_and_b32 s87,s87,3
s_lshl_b32 s87,s87,1
ds_read_b64_tr_b16 v[100:101],v1 offset:10496
v_mfma_f32_16x16x32_bf16 a[12:15],v[96:99],a[96:99],a[12:15]
s_or_b32 s87,s87,1
s_lshl_b32 s87,s87,16
ds_read_b64_tr_b16 v[96:97],v1 offset:256
v_mfma_f32_16x16x32_bf16 a[16:19],v[112:115],a[68:71],a[16:19]
s_add_u32 s88,s87,4096
s_add_u32 s89,s87,8192
ds_read_b64_tr_b16 v[98:99],v1 offset:5376
v_mfma_f32_16x16x32_bf16 a[16:19],v[116:119],a[76:79],a[16:19]
s_add_u32 s90,s87,12288
ds_read_b64_tr_b16 v[102:103],v1 offset:15616
ds_read_b64_tr_b16 v[108:109],v1 offset:30976
v_mfma_f32_16x16x32_bf16 a[16:19],v[120:123],a[84:87],a[16:19]
ds_read_b64_tr_b16 v[110:111],v1 offset:36096
v_add_u32 v1,0x1e000,v1
v_mfma_f32_16x16x32_bf16 a[16:19],v[124:127],a[92:95],a[16:19]
buffer_load_dwordx4 v24,s[48:51],s83 offen lds
v_mfma_f32_16x16x32_bf16 a[20:23],v[124:127],a[124:127],a[20:23]
v_mfma_f32_16x16x32_bf16 a[20:23],v[120:123],a[116:119],a[20:23]
v_mfma_f32_16x16x32_bf16 a[20:23],v[116:119],a[108:111],a[20:23]
v_mfma_f32_16x16x32_bf16 a[20:23],v[112:115],a[100:103],a[20:23]
v_mfma_f32_16x16x32_bf16 a[24:27],v[112:115],a[64:67],a[24:27]
buffer_load_dwordx4 a[176:179],v6,s[28:31],s23 offen sc1
v_mfma_f32_16x16x32_bf16 a[24:27],v[116:119],a[72:75],a[24:27]
v_mfma_f32_16x16x32_bf16 a[24:27],v[120:123],a[80:83],a[24:27]
v_mfma_f32_16x16x32_bf16 a[24:27],v[124:127],a[88:91],a[24:27]
v_mfma_f32_16x16x32_bf16 a[28:31],v[124:127],a[120:123],a[28:31]
ds_read_b64_tr_b16 v[124:125],v2 offset:31008
ds_read_b64_tr_b16 v[126:127],v2 offset:36128
v_mfma_f32_16x16x32_bf16 a[28:31],v[120:123],a[112:115],a[28:31]
ds_read_b64_tr_b16 v[120:121],v2 offset:20768
v_mfma_f32_16x16x32_bf16 a[28:31],v[116:119],a[104:107],a[28:31]
s_waitcnt lgkmcnt(3)
ds_read_b64_tr_b16 v[116:117],v2 offset:10528
v_mfma_f32_16x16x32_bf16 a[28:31],v[112:115],a[96:99],a[28:31]
buffer_load_dwordx4 v25,s[48:51],s83 offen offset:1024 lds
v_mfma_f32_16x16x32_bf16 v[128:131],v[64:67],a[68:71],v[128:131]
ds_read_b64_tr_b16 v[112:113],v2 offset:288
ds_read_b64_tr_b16 v[114:115],v2 offset:5408
v_mfma_f32_16x16x32_bf16 v[128:131],v[68:71],a[76:79],v[128:131]
ds_read_b64_tr_b16 v[118:119],v2 offset:15648
v_mfma_f32_16x16x32_bf16 v[128:131],v[72:75],a[84:87],v[128:131]
ds_read_b64_tr_b16 v[122:123],v2 offset:25888
v_add_u32 v2,0x1e000,v2
v_mfma_f32_16x16x32_bf16 v[128:131],v[76:79],a[92:95],v[128:131]
v_mfma_f32_16x16x32_bf16 v[136:139],v[76:79],a[124:127],v[136:139]
buffer_load_dwordx4 a[180:183],v6,s[28:31],s23 offen offset:1024 sc1
v_mfma_f32_16x16x32_bf16 v[136:139],v[72:75],a[116:119],v[136:139]
v_mfma_f32_16x16x32_bf16 v[136:139],v[68:71],a[108:111],v[136:139]
v_mfma_f32_16x16x32_bf16 v[136:139],v[64:67],a[100:103],v[136:139]
v_mfma_f32_16x16x32_bf16 v[144:147],v[64:67],a[64:67],v[144:147]
v_mfma_f32_16x16x32_bf16 v[144:147],v[68:71],a[72:75],v[144:147]
v_mfma_f32_16x16x32_bf16 v[144:147],v[72:75],a[80:83],v[144:147]
v_mfma_f32_16x16x32_bf16 v[144:147],v[76:79],a[88:91],v[144:147]
buffer_load_dwordx4 v26,s[48:51],s83 offen offset:2048 lds
v_mfma_f32_16x16x32_bf16 v[152:155],v[76:79],a[120:123],v[152:155]
v_mfma_f32_16x16x32_bf16 v[152:155],v[72:75],a[112:115],v[152:155]
v_mfma_f32_16x16x32_bf16 v[152:155],v[68:71],a[104:107],v[152:155]
v_mfma_f32_16x16x32_bf16 v[152:155],v[64:67],a[96:99],v[152:155]
s_waitcnt lgkmcnt(0)
s_barrier
v_mfma_f32_16x16x32_bf16 v[160:163],v[80:83],a[68:71],v[160:163]
buffer_load_dwordx4 a[164:167],v6,s[28:31],s22 offen offset:1024 sc1
v_mfma_f32_16x16x32_bf16 v[160:163],v[84:87],a[76:79],v[160:163]
ds_read_b64_tr_b16 v[64:65],v1
ds_read_b64_tr_b16 v[66:67],v1 offset:5120
v_mfma_f32_16x16x32_bf16 v[160:163],v[88:91],a[84:87],v[160:163]
ds_read_b64_tr_b16 v[68:69],v1 offset:10240
v_mfma_f32_16x16x32_bf16 v[160:163],v[92:95],a[92:95],v[160:163]
ds_read_b64_tr_b16 v[70:71],v1 offset:15360
v_mfma_f32_16x16x32_bf16 v[168:171],v[92:95],a[124:127],v[168:171]
ds_read_b64_tr_b16 v[72:73],v1 offset:20480
v_mfma_f32_16x16x32_bf16 v[168:171],v[88:91],a[116:119],v[168:171]
ds_read_b64_tr_b16 v[74:75],v1 offset:25600
ds_read_b64_tr_b16 v[76:77],v1 offset:30720
v_mfma_f32_16x16x32_bf16 v[168:171],v[84:87],a[108:111],v[168:171]
ds_read_b64_tr_b16 v[78:79],v1 offset:35840
v_mfma_f32_16x16x32_bf16 v[168:171],v[80:83],a[100:103],v[168:171]
buffer_load_dwordx4 v27,s[48:51],s83 offen offset:3072 lds
v_mfma_f32_16x16x32_bf16 v[176:179],v[80:83],a[64:67],v[176:179]
s_add_u32 m0,s86,45056
v_mfma_f32_16x16x32_bf16 v[176:179],v[84:87],a[72:75],v[176:179]
v_mfma_f32_16x16x32_bf16 v[176:179],v[88:91],a[80:83],v[176:179]
v_mfma_f32_16x16x32_bf16 v[176:179],v[92:95],a[88:91],v[176:179]
v_mfma_f32_16x16x32_bf16 v[184:187],v[92:95],a[120:123],v[184:187]
buffer_load_dwordx4 a[148:151],v6,s[28:31],s21 offen offset:1024 sc1
v_mfma_f32_16x16x32_bf16 v[184:187],v[88:91],a[112:115],v[184:187]
ds_read_b64_tr_b16 v[88:89],v2 offset:20512
ds_read_b64_tr_b16 v[90:91],v2 offset:25632
v_mfma_f32_16x16x32_bf16 v[184:187],v[84:87],a[104:107],v[184:187]
ds_read_b64_tr_b16 v[84:85],v2 offset:10272
v_mfma_f32_16x16x32_bf16 v[184:187],v[80:83],a[96:99],v[184:187]
ds_read_b64_tr_b16 v[80:81],v2 offset:32
v_mfma_f32_16x16x32_bf16 v[192:195],v[96:99],a[68:71],v[192:195]
ds_read_b64_tr_b16 v[82:83],v2 offset:5152
v_mfma_f32_16x16x32_bf16 v[192:195],v[100:103],a[76:79],v[192:195]
ds_read_b64_tr_b16 v[86:87],v2 offset:15392
ds_read_b64_tr_b16 v[92:93],v2 offset:30752
v_mfma_f32_16x16x32_bf16 v[192:195],v[104:107],a[84:87],v[192:195]
ds_read_b64_tr_b16 v[94:95],v2 offset:35872
v_mfma_f32_16x16x32_bf16 v[192:195],v[108:111],a[92:95],v[192:195]
buffer_load_dwordx4 v7,s[48:51],s83 offen lds
v_mfma_f32_16x16x32_bf16 v[200:203],v[108:111],a[124:127],v[200:203]
s_add_u32 m0,s86,46080
s_add_u32 s83,s64,3
s_and_b32 s83,s83,3
v_mfma_f32_16x16x32_bf16 v[200:203],v[104:107],a[116:119],v[200:203]
s_lshl_b32 s83,s83,1
s_or_b32 s83,s83,17
s_mul_i32 s83,s83,163840
v_mfma_f32_16x16x32_bf16 v[200:203],v[100:103],a[108:111],v[200:203]
s_add_u32 s83,s83,s85
v_mfma_f32_16x16x32_bf16 v[200:203],v[96:99],a[100:103],v[200:203]
v_mfma_f32_16x16x32_bf16 v[208:211],v[96:99],a[64:67],v[208:211]
buffer_load_dwordx4 a[132:135],v6,s[28:31],s20 offen offset:1024 sc1
v_mfma_f32_16x16x32_bf16 v[208:211],v[100:103],a[72:75],v[208:211]
v_mfma_f32_16x16x32_bf16 v[208:211],v[104:107],a[80:83],v[208:211]
v_mfma_f32_16x16x32_bf16 v[208:211],v[108:111],a[88:91],v[208:211]
v_mfma_f32_16x16x32_bf16 v[216:219],v[108:111],a[120:123],v[216:219]
ds_read_b64_tr_b16 v[108:109],v1 offset:30784
ds_read_b64_tr_b16 v[110:111],v1 offset:35904
v_mfma_f32_16x16x32_bf16 v[216:219],v[104:107],a[112:115],v[216:219]
ds_read_b64_tr_b16 v[104:105],v1 offset:20544
v_mfma_f32_16x16x32_bf16 v[216:219],v[100:103],a[104:107],v[216:219]
ds_read_b64_tr_b16 v[100:101],v1 offset:10304
v_mfma_f32_16x16x32_bf16 v[216:219],v[96:99],a[96:99],v[216:219]
buffer_load_dwordx4 v24,s[48:51],s84 offen lds
v_mfma_f32_16x16x32_bf16 v[224:227],v[112:115],a[68:71],v[224:227]
ds_read_b64_tr_b16 v[96:97],v1 offset:64
ds_read_b64_tr_b16 v[98:99],v1 offset:5184
v_mfma_f32_16x16x32_bf16 v[224:227],v[116:119],a[76:79],v[224:227]
ds_read_b64_tr_b16 v[102:103],v1 offset:15424
v_mfma_f32_16x16x32_bf16 v[224:227],v[120:123],a[84:87],v[224:227]
ds_read_b64_tr_b16 v[106:107],v1 offset:25664
v_mfma_f32_16x16x32_bf16 v[224:227],v[124:127],a[92:95],v[224:227]
v_mfma_f32_16x16x32_bf16 v[232:235],v[124:127],a[124:127],v[232:235]
buffer_load_dwordx4 a[136:139],v6,s[28:31],s20 offen offset:2048 sc1
)A"
R"A(v_mfma_f32_16x16x32_bf16 v[232:235],v[120:123],a[116:119],v[232:235]
v_mfma_f32_16x16x32_bf16 v[232:235],v[116:119],a[108:111],v[232:235]
v_mfma_f32_16x16x32_bf16 v[232:235],v[112:115],a[100:103],v[232:235]
v_mfma_f32_16x16x32_bf16 v[240:243],v[112:115],a[64:67],v[240:243]
v_mfma_f32_16x16x32_bf16 v[240:243],v[116:119],a[72:75],v[240:243]
v_mfma_f32_16x16x32_bf16 v[240:243],v[120:123],a[80:83],v[240:243]
v_mfma_f32_16x16x32_bf16 v[240:243],v[124:127],a[88:91],v[240:243]
buffer_load_dwordx4 v25,s[48:51],s84 offen offset:1024 lds
v_mfma_f32_16x16x32_bf16 v[248:251],v[124:127],a[120:123],v[248:251]
ds_read_b64_tr_b16 v[124:125],v2 offset:30816
ds_read_b64_tr_b16 v[126:127],v2 offset:35936
v_mfma_f32_16x16x32_bf16 v[248:251],v[120:123],a[112:115],v[248:251]
ds_read_b64_tr_b16 v[120:121],v2 offset:20576
v_mfma_f32_16x16x32_bf16 v[248:251],v[116:119],a[104:107],v[248:251]
ds_read_b64_tr_b16 v[116:117],v2 offset:10336
v_mfma_f32_16x16x32_bf16 v[248:251],v[112:115],a[96:99],v[248:251]
s_waitcnt vmcnt(19) lgkmcnt(4)
ds_read_b64_tr_b16 v[112:113],v2 offset:96
v_mfma_f32_16x16x32_bf16 a[192:195],v[64:67],v[32:35],a[192:195]
ds_read_b64_tr_b16 v[114:115],v2 offset:5216
ds_read_b64_tr_b16 v[118:119],v2 offset:15456
v_mfma_f32_16x16x32_bf16 a[192:195],v[68:71],v[36:39],a[192:195]
buffer_load_dwordx4 a[152:155],v6,s[28:31],s21 offen offset:2048 sc1
v_mfma_f32_16x16x32_bf16 a[192:195],v[72:75],v[40:43],a[192:195]
ds_read_b64_tr_b16 v[122:123],v2 offset:25696
v_mfma_f32_16x16x32_bf16 a[192:195],v[76:79],v[44:47],a[192:195]
v_mfma_f32_16x16x32_bf16 a[196:199],v[76:79],v[180:183],a[196:199]
v_mfma_f32_16x16x32_bf16 a[196:199],v[72:75],v[164:167],a[196:199]
v_mfma_f32_16x16x32_bf16 a[196:199],v[68:71],v[148:151],a[196:199]
v_mfma_f32_16x16x32_bf16 a[196:199],v[64:67],v[132:135],a[196:199]
s_waitcnt vmcnt(18)
v_mfma_f32_16x16x32_bf16 a[200:203],v[64:67],v[196:199],a[200:203]
buffer_load_dwordx4 v26,s[48:51],s84 offen offset:2048 lds
v_mfma_f32_16x16x32_bf16 a[200:203],v[68:71],v[212:215],a[200:203]
v_mfma_f32_16x16x32_bf16 a[200:203],v[72:75],v[228:231],a[200:203]
v_mfma_f32_16x16x32_bf16 a[200:203],v[76:79],v[244:247],a[200:203]
v_mfma_f32_16x16x32_bf16 a[204:207],v[76:79],v[188:191],a[204:207]
ds_read_b64_tr_b16 v[76:77],v1 offset:30848
ds_read_b64_tr_b16 v[78:79],v1 offset:35968
v_mfma_f32_16x16x32_bf16 a[204:207],v[72:75],v[172:175],a[204:207]
s_waitcnt vmcnt(15)
ds_read_b64_tr_b16 v[72:73],v1 offset:20608
v_mfma_f32_16x16x32_bf16 a[204:207],v[68:71],v[156:159],a[204:207]
buffer_load_dwordx4 a[168:171],v6,s[28:31],s22 offen offset:2048 sc1
v_mfma_f32_16x16x32_bf16 a[204:207],v[64:67],v[140:143],a[204:207]
ds_read_b64_tr_b16 v[64:65],v1 offset:128
ds_read_b64_tr_b16 v[66:67],v1 offset:5248
v_mfma_f32_16x16x32_bf16 a[208:211],v[80:83],v[32:35],a[208:211]
ds_read_b64_tr_b16 v[68:69],v1 offset:10368
v_mfma_f32_16x16x32_bf16 a[208:211],v[84:87],v[36:39],a[208:211]
ds_read_b64_tr_b16 v[70:71],v1 offset:15488
v_mfma_f32_16x16x32_bf16 a[208:211],v[88:91],v[40:43],a[208:211]
ds_read_b64_tr_b16 v[74:75],v1 offset:25728
v_mfma_f32_16x16x32_bf16 a[208:211],v[92:95],v[44:47],a[208:211]
v_mfma_f32_16x16x32_bf16 a[212:215],v[92:95],v[180:183],a[212:215]
v_mfma_f32_16x16x32_bf16 a[212:215],v[88:91],v[164:167],a[212:215]
buffer_load_dwordx4 v27,s[48:51],s84 offen offset:3072 lds
v_mfma_f32_16x16x32_bf16 a[212:215],v[84:87],v[148:151],a[212:215]
s_add_u32 m0,s86,50176
v_mfma_f32_16x16x32_bf16 a[212:215],v[80:83],v[132:135],a[212:215]
v_mfma_f32_16x16x32_bf16 a[216:219],v[80:83],v[196:199],a[216:219]
v_mfma_f32_16x16x32_bf16 a[216:219],v[84:87],v[212:215],a[216:219]
v_mfma_f32_16x16x32_bf16 a[216:219],v[88:91],v[228:231],a[216:219]
v_mfma_f32_16x16x32_bf16 a[216:219],v[92:95],v[244:247],a[216:219]
buffer_load_dwordx4 a[184:187],v6,s[28:31],s23 offen offset:2048 sc1
v_mfma_f32_16x16x32_bf16 a[220:223],v[92:95],v[188:191],a[220:223]
ds_read_b64_tr_b16 v[92:93],v2 offset:30880
ds_read_b64_tr_b16 v[94:95],v2 offset:36000
v_mfma_f32_16x16x32_bf16 a[220:223],v[88:91],v[172:175],a[220:223]
ds_read_b64_tr_b16 v[88:89],v2 offset:20640
v_mfma_f32_16x16x32_bf16 a[220:223],v[84:87],v[156:159],a[220:223]
ds_read_b64_tr_b16 v[84:85],v2 offset:10400
v_mfma_f32_16x16x32_bf16 a[220:223],v[80:83],v[140:143],a[220:223]
ds_read_b64_tr_b16 v[80:81],v2 offset:160
v_mfma_f32_16x16x32_bf16 a[224:227],v[96:99],v[32:35],a[224:227]
ds_read_b64_tr_b16 v[82:83],v2 offset:5280
ds_read_b64_tr_b16 v[86:87],v2 offset:15520
v_mfma_f32_16x16x32_bf16 a[224:227],v[100:103],v[36:39],a[224:227]
ds_read_b64_tr_b16 v[90:91],v2 offset:25760
v_mfma_f32_16x16x32_bf16 a[224:227],v[104:107],v[40:43],a[224:227]
buffer_load_dwordx4 v7,s[48:51],s84 offen lds
v_mfma_f32_16x16x32_bf16 a[224:227],v[108:111],v[44:47],a[224:227]
s_add_u32 m0,s86,0
s_add_u32 s84,s83,20480
v_mfma_f32_16x16x32_bf16 a[228:231],v[108:111],v[180:183],a[228:231]
v_mfma_f32_16x16x32_bf16 a[228:231],v[104:107],v[164:167],a[228:231]
v_mfma_f32_16x16x32_bf16 a[228:231],v[100:103],v[148:151],a[228:231]
v_mfma_f32_16x16x32_bf16 a[228:231],v[96:99],v[132:135],a[228:231]
buffer_load_dwordx4 a[68:71],v6,s[28:31],s87 offen sc1
v_mfma_f32_16x16x32_bf16 a[232:235],v[96:99],v[196:199],a[232:235]
v_mfma_f32_16x16x32_bf16 a[232:235],v[100:103],v[212:215],a[232:235]
v_mfma_f32_16x16x32_bf16 a[232:235],v[104:107],v[228:231],a[232:235]
v_mfma_f32_16x16x32_bf16 a[232:235],v[108:111],v[244:247],a[232:235]
v_mfma_f32_16x16x32_bf16 a[236:239],v[108:111],v[188:191],a[236:239]
buffer_load_dwordx4 a[188:191],v28,s[28:31],s23 offen offset:3072 sc1
v_mfma_f32_16x16x32_bf16 a[236:239],v[104:107],v[172:175],a[236:239]
ds_read_b64_tr_b16 v[104:105],v1 offset:20672
ds_read_b64_tr_b16 v[106:107],v1 offset:25792
v_mfma_f32_16x16x32_bf16 a[236:239],v[100:103],v[156:159],a[236:239]
ds_read_b64_tr_b16 v[100:101],v1 offset:10432
v_mfma_f32_16x16x32_bf16 a[236:239],v[96:99],v[140:143],a[236:239]
s_waitcnt lgkmcnt(3)
ds_read_b64_tr_b16 v[96:97],v1 offset:192
v_mfma_f32_16x16x32_bf16 a[240:243],v[112:115],v[32:35],a[240:243]
ds_read_b64_tr_b16 v[98:99],v1 offset:5312
v_mfma_f32_16x16x32_bf16 a[240:243],v[116:119],v[36:39],a[240:243]
buffer_load_dwordx4 a[100:103],v6,s[28:31],s87 offen offset:1024 sc1
v_mfma_f32_16x16x32_bf16 a[240:243],v[120:123],v[40:43],a[240:243]
ds_read_b64_tr_b16 v[102:103],v1 offset:15552
ds_read_b64_tr_b16 v[108:109],v1 offset:30912
v_mfma_f32_16x16x32_bf16 a[240:243],v[124:127],v[44:47],a[240:243]
ds_read_b64_tr_b16 v[110:111],v1 offset:36032
v_mfma_f32_16x16x32_bf16 a[244:247],v[124:127],v[180:183],a[244:247]
v_mfma_f32_16x16x32_bf16 a[244:247],v[120:123],v[164:167],a[244:247]
v_mfma_f32_16x16x32_bf16 a[244:247],v[116:119],v[148:151],a[244:247]
buffer_load_dwordx4 a[172:175],v28,s[28:31],s22 offen offset:3072 sc1
v_mfma_f32_16x16x32_bf16 a[244:247],v[112:115],v[132:135],a[244:247]
v_mfma_f32_16x16x32_bf16 a[248:251],v[112:115],v[196:199],a[248:251]
v_mfma_f32_16x16x32_bf16 a[248:251],v[116:119],v[212:215],a[248:251]
v_mfma_f32_16x16x32_bf16 a[248:251],v[120:123],v[228:231],a[248:251]
v_mfma_f32_16x16x32_bf16 a[248:251],v[124:127],v[244:247],a[248:251]
buffer_load_dwordx4 a[76:79],v6,s[28:31],s88 offen sc1
v_mfma_f32_16x16x32_bf16 a[252:255],v[124:127],v[188:191],a[252:255]
ds_read_b64_tr_b16 v[124:125],v2 offset:30944
ds_read_b64_tr_b16 v[126:127],v2 offset:36064
v_mfma_f32_16x16x32_bf16 a[252:255],v[120:123],v[172:175],a[252:255]
ds_read_b64_tr_b16 v[120:121],v2 offset:20704
v_mfma_f32_16x16x32_bf16 a[252:255],v[116:119],v[156:159],a[252:255]
ds_read_b64_tr_b16 v[116:117],v2 offset:10464
v_mfma_f32_16x16x32_bf16 a[252:255],v[112:115],v[140:143],a[252:255]
ds_read_b64_tr_b16 v[112:113],v2 offset:224
v_mfma_f32_16x16x32_bf16 a[0:3],v[64:67],v[32:35],a[0:3]
buffer_load_dwordx4 a[64:67],v6,s[28:31],s87 offen offset:2048 sc1
v_mfma_f32_16x16x32_bf16 a[0:3],v[68:71],v[36:39],a[0:3]
ds_read_b64_tr_b16 v[114:115],v2 offset:5344
ds_read_b64_tr_b16 v[118:119],v2 offset:15584
v_mfma_f32_16x16x32_bf16 a[0:3],v[72:75],v[40:43],a[0:3]
ds_read_b64_tr_b16 v[122:123],v2 offset:25824
v_mfma_f32_16x16x32_bf16 a[0:3],v[76:79],v[44:47],a[0:3]
v_mfma_f32_16x16x32_bf16 a[4:7],v[76:79],v[180:183],a[4:7]
v_mfma_f32_16x16x32_bf16 a[4:7],v[72:75],v[164:167],a[4:7]
buffer_load_dwordx4 a[156:159],v28,s[28:31],s21 offen offset:3072 sc1
v_mfma_f32_16x16x32_bf16 a[4:7],v[68:71],v[148:151],a[4:7]
v_mfma_f32_16x16x32_bf16 a[4:7],v[64:67],v[132:135],a[4:7]
v_mfma_f32_16x16x32_bf16 a[8:11],v[64:67],v[196:199],a[8:11]
v_mfma_f32_16x16x32_bf16 a[8:11],v[68:71],v[212:215],a[8:11]
v_mfma_f32_16x16x32_bf16 a[8:11],v[72:75],v[228:231],a[8:11]
buffer_load_dwordx4 a[84:87],v6,s[28:31],s89 offen sc1
v_mfma_f32_16x16x32_bf16 a[8:11],v[76:79],v[244:247],a[8:11]
v_mfma_f32_16x16x32_bf16 a[12:15],v[76:79],v[188:191],a[12:15]
ds_read_b64_tr_b16 v[76:77],v1 offset:30976
ds_read_b64_tr_b16 v[78:79],v1 offset:36096
v_mfma_f32_16x16x32_bf16 a[12:15],v[72:75],v[172:175],a[12:15]
ds_read_b64_tr_b16 v[72:73],v1 offset:20736
v_mfma_f32_16x16x32_bf16 a[12:15],v[68:71],v[156:159],a[12:15]
ds_read_b64_tr_b16 v[68:69],v1 offset:10496
v_mfma_f32_16x16x32_bf16 a[12:15],v[64:67],v[140:143],a[12:15]
buffer_load_dwordx4 a[140:143],v28,s[28:31],s20 offen offset:3072 sc1
v_mfma_f32_16x16x32_bf16 a[16:19],v[80:83],v[32:35],a[16:19]
s_add_u32 s20,s64,3
ds_read_b64_tr_b16 v[64:65],v1 offset:256
ds_read_b64_tr_b16 v[66:67],v1 offset:5376
v_mfma_f32_16x16x32_bf16 a[16:19],v[84:87],v[36:39],a[16:19]
s_and_b32 s20,s20,3
s_lshl_b32 s20,s20,1
ds_read_b64_tr_b16 v[70:71],v1 offset:15616
v_mfma_f32_16x16x32_bf16 a[16:19],v[88:91],v[40:43],a[16:19]
s_or_b32 s20,s20,17
ds_read_b64_tr_b16 v[74:75],v1 offset:25856
v_add_u32 v1,0xffff6000,v1
v_mfma_f32_16x16x32_bf16 a[16:19],v[92:95],v[44:47],a[16:19]
s_lshl_b32 s20,s20,16
s_add_u32 s21,s20,4096
s_add_u32 s22,s20,8192
v_mfma_f32_16x16x32_bf16 a[20:23],v[92:95],v[180:183],a[20:23]
buffer_load_dwordx4 a[72:75],v6,s[28:31],s88 offen offset:2048 sc1
v_mfma_f32_16x16x32_bf16 a[20:23],v[88:91],v[164:167],a[20:23]
s_add_u32 s23,s20,12288
v_mfma_f32_16x16x32_bf16 a[20:23],v[84:87],v[148:151],a[20:23]
v_mfma_f32_16x16x32_bf16 a[20:23],v[80:83],v[132:135],a[20:23]
v_mfma_f32_16x16x32_bf16 a[24:27],v[80:83],v[196:199],a[24:27]
v_mfma_f32_16x16x32_bf16 a[24:27],v[84:87],v[212:215],a[24:27]
buffer_load_dwordx4 a[92:95],v6,s[28:31],s90 offen sc1
v_mfma_f32_16x16x32_bf16 a[24:27],v[88:91],v[228:231],a[24:27]
v_mfma_f32_16x16x32_bf16 a[24:27],v[92:95],v[244:247],a[24:27]
v_mfma_f32_16x16x32_bf16 a[28:31],v[92:95],v[188:191],a[28:31]
ds_read_b64_tr_b16 v[92:93],v2 offset:31008
ds_read_b64_tr_b16 v[94:95],v2 offset:36128
v_mfma_f32_16x16x32_bf16 a[28:31],v[88:91],v[172:175],a[28:31]
ds_read_b64_tr_b16 v[88:89],v2 offset:20768
v_mfma_f32_16x16x32_bf16 a[28:31],v[84:87],v[156:159],a[28:31]
buffer_load_dwordx4 a[80:83],v6,s[28:31],s89 offen offset:2048 sc1
v_mfma_f32_16x16x32_bf16 a[28:31],v[80:83],v[140:143],a[28:31]
s_waitcnt lgkmcnt(3)
ds_read_b64_tr_b16 v[80:81],v2 offset:288
ds_read_b64_tr_b16 v[82:83],v2 offset:5408
v_mfma_f32_16x16x32_bf16 v[128:131],v[96:99],v[32:35],v[128:131]
ds_read_b64_tr_b16 v[84:85],v2 offset:10528
v_mfma_f32_16x16x32_bf16 v[128:131],v[100:103],v[36:39],v[128:131]
ds_read_b64_tr_b16 v[86:87],v2 offset:15648
v_mfma_f32_16x16x32_bf16 v[128:131],v[104:107],v[40:43],v[128:131]
ds_read_b64_tr_b16 v[90:91],v2 offset:25888
v_add_u32 v2,0xffff6000,v2
v_mfma_f32_16x16x32_bf16 v[128:131],v[108:111],v[44:47],v[128:131]
v_mfma_f32_16x16x32_bf16 v[136:139],v[108:111],v[180:183],v[136:139]
v_mfma_f32_16x16x32_bf16 v[136:139],v[104:107],v[164:167],v[136:139]
buffer_load_dwordx4 v24,s[48:51],s83 offen lds
v_mfma_f32_16x16x32_bf16 v[136:139],v[100:103],v[148:151],v[136:139]
v_mfma_f32_16x16x32_bf16 v[136:139],v[96:99],v[132:135],v[136:139]
v_mfma_f32_16x16x32_bf16 v[144:147],v[96:99],v[196:199],v[144:147]
v_mfma_f32_16x16x32_bf16 v[144:147],v[100:103],v[212:215],v[144:147]
v_mfma_f32_16x16x32_bf16 v[144:147],v[104:107],v[228:231],v[144:147]
v_mfma_f32_16x16x32_bf16 v[144:147],v[108:111],v[244:247],v[144:147]
v_mfma_f32_16x16x32_bf16 v[152:155],v[108:111],v[188:191],v[152:155]
)A"
R"A(buffer_load_dwordx4 v25,s[48:51],s83 offen offset:1024 lds
v_mfma_f32_16x16x32_bf16 v[152:155],v[104:107],v[172:175],v[152:155]
ds_read_b64_tr_b16 v[104:105],v1 offset:20480
ds_read_b64_tr_b16 v[106:107],v1 offset:25600
v_mfma_f32_16x16x32_bf16 v[152:155],v[100:103],v[156:159],v[152:155]
ds_read_b64_tr_b16 v[100:101],v1 offset:10240
v_mfma_f32_16x16x32_bf16 v[152:155],v[96:99],v[140:143],v[152:155]
ds_read_b64_tr_b16 v[96:97],v1
s_waitcnt vmcnt(14) lgkmcnt(4)
s_barrier
v_mfma_f32_16x16x32_bf16 v[160:163],v[112:115],v[32:35],v[160:163]
ds_read_b64_tr_b16 v[98:99],v1 offset:5120
v_mfma_f32_16x16x32_bf16 v[160:163],v[116:119],v[36:39],v[160:163]
ds_read_b64_tr_b16 v[102:103],v1 offset:15360
ds_read_b64_tr_b16 v[108:109],v1 offset:30720
v_mfma_f32_16x16x32_bf16 v[160:163],v[120:123],v[40:43],v[160:163]
buffer_load_dwordx4 v26,s[48:51],s83 offen offset:2048 lds
v_mfma_f32_16x16x32_bf16 v[160:163],v[124:127],v[44:47],v[160:163]
ds_read_b64_tr_b16 v[110:111],v1 offset:35840
v_mfma_f32_16x16x32_bf16 v[168:171],v[124:127],v[180:183],v[168:171]
v_mfma_f32_16x16x32_bf16 v[168:171],v[120:123],v[164:167],v[168:171]
s_nop 0
L_pd16_%=:
s_cbranch_execz L_trtg2F_%=
v_mfma_f32_16x16x32_bf16 v[168:171],v[116:119],v[148:151],v[168:171]
v_mfma_f32_16x16x32_bf16 v[168:171],v[112:115],v[132:135],v[168:171]
v_mfma_f32_16x16x32_bf16 v[176:179],v[112:115],v[196:199],v[176:179]
v_mfma_f32_16x16x32_bf16 v[176:179],v[116:119],v[212:215],v[176:179]
buffer_load_dwordx4 v27,s[48:51],s83 offen offset:3072 lds
v_mfma_f32_16x16x32_bf16 v[176:179],v[120:123],v[228:231],v[176:179]
s_add_u32 m0,s86,4096
v_mfma_f32_16x16x32_bf16 v[176:179],v[124:127],v[244:247],v[176:179]
v_mfma_f32_16x16x32_bf16 v[184:187],v[124:127],v[188:191],v[184:187]
ds_read_b64_tr_b16 v[124:125],v2 offset:30752
ds_read_b64_tr_b16 v[126:127],v2 offset:35872
v_mfma_f32_16x16x32_bf16 v[184:187],v[120:123],v[172:175],v[184:187]
ds_read_b64_tr_b16 v[120:121],v2 offset:20512
v_mfma_f32_16x16x32_bf16 v[184:187],v[116:119],v[156:159],v[184:187]
buffer_load_dwordx4 a[124:127],v6,s[28:31],s90 offen offset:1024 sc1
v_mfma_f32_16x16x32_bf16 v[184:187],v[112:115],v[140:143],v[184:187]
ds_read_b64_tr_b16 v[112:113],v2 offset:32
ds_read_b64_tr_b16 v[114:115],v2 offset:5152
v_mfma_f32_16x16x32_bf16 v[192:195],v[64:67],v[32:35],v[192:195]
ds_read_b64_tr_b16 v[116:117],v2 offset:10272
v_mfma_f32_16x16x32_bf16 v[192:195],v[68:71],v[36:39],v[192:195]
ds_read_b64_tr_b16 v[118:119],v2 offset:15392
v_mfma_f32_16x16x32_bf16 v[192:195],v[72:75],v[40:43],v[192:195]
ds_read_b64_tr_b16 v[122:123],v2 offset:25632
v_mfma_f32_16x16x32_bf16 v[192:195],v[76:79],v[44:47],v[192:195]
buffer_load_dwordx4 a[88:91],v6,s[28:31],s90 offen offset:2048 sc1
v_mfma_f32_16x16x32_bf16 v[200:203],v[76:79],v[180:183],v[200:203]
v_mfma_f32_16x16x32_bf16 v[200:203],v[72:75],v[164:167],v[200:203]
v_mfma_f32_16x16x32_bf16 v[200:203],v[68:71],v[148:151],v[200:203]
v_mfma_f32_16x16x32_bf16 v[200:203],v[64:67],v[132:135],v[200:203]
v_mfma_f32_16x16x32_bf16 v[208:211],v[64:67],v[196:199],v[208:211]
v_mfma_f32_16x16x32_bf16 v[208:211],v[68:71],v[212:215],v[208:211]
v_mfma_f32_16x16x32_bf16 v[208:211],v[72:75],v[228:231],v[208:211]
buffer_load_dwordx4 v7,s[48:51],s83 offen lds
v_mfma_f32_16x16x32_bf16 v[208:211],v[76:79],v[244:247],v[208:211]
s_add_u32 m0,s86,5120
s_add_u32 s83,s64,3
s_and_b32 s83,s83,3
v_mfma_f32_16x16x32_bf16 v[216:219],v[76:79],v[188:191],v[216:219]
s_lshl_b32 s83,s83,1
ds_read_b64_tr_b16 v[76:77],v1 offset:30784
ds_read_b64_tr_b16 v[78:79],v1 offset:35904
v_mfma_f32_16x16x32_bf16 v[216:219],v[72:75],v[172:175],v[216:219]
buffer_load_dwordx2 v[4:5],v3,s[44:47],s91 offen offset:1280 sc1
v_mfma_f32_16x16x32_bf16 v[216:219],v[68:71],v[156:159],v[216:219]
s_or_b32 s83,s83,1
ds_read_b64_tr_b16 v[68:69],v1 offset:10304
ds_read_b64_tr_b16 v[70:71],v1 offset:15424
v_mfma_f32_16x16x32_bf16 v[216:219],v[64:67],v[140:143],v[216:219]
s_mul_i32 s83,s83,163840
s_add_u32 s83,s83,s85
ds_read_b64_tr_b16 v[64:65],v1 offset:64
v_mfma_f32_16x16x32_bf16 v[224:227],v[80:83],v[32:35],v[224:227]
ds_read_b64_tr_b16 v[66:67],v1 offset:5184
v_mfma_f32_16x16x32_bf16 v[224:227],v[84:87],v[36:39],v[224:227]
ds_read_b64_tr_b16 v[72:73],v1 offset:20544
v_mfma_f32_16x16x32_bf16 v[224:227],v[88:91],v[40:43],v[224:227]
ds_read_b64_tr_b16 v[74:75],v1 offset:25664
v_mfma_f32_16x16x32_bf16 v[224:227],v[92:95],v[44:47],v[224:227]
v_mfma_f32_16x16x32_bf16 v[232:235],v[92:95],v[180:183],v[232:235]
buffer_load_dwordx4 v24,s[48:51],s84 offen lds
v_mfma_f32_16x16x32_bf16 v[232:235],v[88:91],v[164:167],v[232:235]
v_mfma_f32_16x16x32_bf16 v[232:235],v[84:87],v[148:151],v[232:235]
v_mfma_f32_16x16x32_bf16 v[232:235],v[80:83],v[132:135],v[232:235]
v_mfma_f32_16x16x32_bf16 v[240:243],v[80:83],v[196:199],v[240:243]
v_mfma_f32_16x16x32_bf16 v[240:243],v[84:87],v[212:215],v[240:243]
buffer_load_dwordx4 a[116:119],v6,s[28:31],s89 offen offset:1024 sc1
v_mfma_f32_16x16x32_bf16 v[240:243],v[88:91],v[228:231],v[240:243]
v_mfma_f32_16x16x32_bf16 v[240:243],v[92:95],v[244:247],v[240:243]
v_mfma_f32_16x16x32_bf16 v[248:251],v[92:95],v[188:191],v[248:251]
ds_read_b64_tr_b16 v[92:93],v2 offset:30816
ds_read_b64_tr_b16 v[94:95],v2 offset:35936
v_mfma_f32_16x16x32_bf16 v[248:251],v[88:91],v[172:175],v[248:251]
ds_read_b64_tr_b16 v[88:89],v2 offset:20576
v_mfma_f32_16x16x32_bf16 v[248:251],v[84:87],v[156:159],v[248:251]
ds_read_b64_tr_b16 v[84:85],v2 offset:10336
v_mfma_f32_16x16x32_bf16 v[248:251],v[80:83],v[140:143],v[248:251]
s_waitcnt lgkmcnt(4)
ds_read_b64_tr_b16 v[80:81],v2 offset:96
v_mfma_f32_16x16x32_bf16 a[192:195],v[96:99],a[128:131],a[192:195]
buffer_load_dwordx4 v25,s[48:51],s84 offen offset:1024 lds
v_mfma_f32_16x16x32_bf16 a[192:195],v[100:103],a[144:147],a[192:195]
ds_read_b64_tr_b16 v[82:83],v2 offset:5216
ds_read_b64_tr_b16 v[86:87],v2 offset:15456
v_mfma_f32_16x16x32_bf16 a[192:195],v[104:107],a[160:163],a[192:195]
ds_read_b64_tr_b16 v[90:91],v2 offset:25696
v_mfma_f32_16x16x32_bf16 a[192:195],v[108:111],a[176:179],a[192:195]
v_mfma_f32_16x16x32_bf16 a[196:199],v[108:111],a[180:183],a[196:199]
v_mfma_f32_16x16x32_bf16 a[196:199],v[104:107],a[164:167],a[196:199]
v_mfma_f32_16x16x32_bf16 a[196:199],v[100:103],a[148:151],a[196:199]
v_mfma_f32_16x16x32_bf16 a[196:199],v[96:99],a[132:135],a[196:199]
buffer_load_dwordx4 v26,s[48:51],s84 offen offset:2048 lds
v_mfma_f32_16x16x32_bf16 a[200:203],v[96:99],a[136:139],a[200:203]
v_mfma_f32_16x16x32_bf16 a[200:203],v[100:103],a[152:155],a[200:203]
v_mfma_f32_16x16x32_bf16 a[200:203],v[104:107],a[168:171],a[200:203]
v_mfma_f32_16x16x32_bf16 a[200:203],v[108:111],a[184:187],a[200:203]
s_waitcnt vmcnt(19)
v_mfma_f32_16x16x32_bf16 a[204:207],v[108:111],a[188:191],a[204:207]
ds_read_b64_tr_b16 v[108:109],v1 offset:30848
ds_read_b64_tr_b16 v[110:111],v1 offset:35968
v_mfma_f32_16x16x32_bf16 a[204:207],v[104:107],a[172:175],a[204:207]
s_waitcnt vmcnt(15)
ds_read_b64_tr_b16 v[104:105],v1 offset:20608
v_mfma_f32_16x16x32_bf16 a[204:207],v[100:103],a[156:159],a[204:207]
buffer_load_dwordx4 v27,s[48:51],s84 offen offset:3072 lds
v_mfma_f32_16x16x32_bf16 a[204:207],v[96:99],a[140:143],a[204:207]
s_add_u32 m0,s86,9216
ds_read_b64_tr_b16 v[96:97],v1 offset:128
ds_read_b64_tr_b16 v[98:99],v1 offset:5248
v_mfma_f32_16x16x32_bf16 a[208:211],v[112:115],a[128:131],a[208:211]
ds_read_b64_tr_b16 v[100:101],v1 offset:10368
v_mfma_f32_16x16x32_bf16 a[208:211],v[116:119],a[144:147],a[208:211]
ds_read_b64_tr_b16 v[102:103],v1 offset:15488
v_mfma_f32_16x16x32_bf16 a[208:211],v[120:123],a[160:163],a[208:211]
ds_read_b64_tr_b16 v[106:107],v1 offset:25728
v_mfma_f32_16x16x32_bf16 a[208:211],v[124:127],a[176:179],a[208:211]
v_mfma_f32_16x16x32_bf16 a[212:215],v[124:127],a[180:183],a[212:215]
v_mfma_f32_16x16x32_bf16 a[212:215],v[120:123],a[164:167],a[212:215]
buffer_load_dwordx4 v7,s[48:51],s84 offen lds
v_mfma_f32_16x16x32_bf16 a[212:215],v[116:119],a[148:151],a[212:215]
s_add_u32 m0,s86,122880
s_add_u32 s84,s83,20480
v_mfma_f32_16x16x32_bf16 a[212:215],v[112:115],a[132:135],a[212:215]
v_mfma_f32_16x16x32_bf16 a[216:219],v[112:115],a[136:139],a[216:219]
v_mfma_f32_16x16x32_bf16 a[216:219],v[116:119],a[152:155],a[216:219]
v_mfma_f32_16x16x32_bf16 a[216:219],v[120:123],a[168:171],a[216:219]
buffer_load_dwordx4 a[120:123],v28,s[28:31],s90 offen offset:3072 sc1
v_mfma_f32_16x16x32_bf16 a[216:219],v[124:127],a[184:187],a[216:219]
v_mfma_f32_16x16x32_bf16 a[220:223],v[124:127],a[188:191],a[220:223]
ds_read_b64_tr_b16 v[124:125],v2 offset:30880
ds_read_b64_tr_b16 v[126:127],v2 offset:36000
v_mfma_f32_16x16x32_bf16 a[220:223],v[120:123],a[172:175],a[220:223]
ds_read_b64_tr_b16 v[120:121],v2 offset:20640
v_mfma_f32_16x16x32_bf16 a[220:223],v[116:119],a[156:159],a[220:223]
ds_read_b64_tr_b16 v[116:117],v2 offset:10400
v_mfma_f32_16x16x32_bf16 a[220:223],v[112:115],a[140:143],a[220:223]
buffer_load_dwordx4 a[108:111],v6,s[28:31],s88 offen offset:1024 sc1
v_mfma_f32_16x16x32_bf16 a[224:227],v[64:67],a[128:131],a[224:227]
ds_read_b64_tr_b16 v[112:113],v2 offset:160
ds_read_b64_tr_b16 v[114:115],v2 offset:5280
v_mfma_f32_16x16x32_bf16 a[224:227],v[68:71],a[144:147],a[224:227]
ds_read_b64_tr_b16 v[118:119],v2 offset:15520
v_mfma_f32_16x16x32_bf16 a[224:227],v[72:75],a[160:163],a[224:227]
ds_read_b64_tr_b16 v[122:123],v2 offset:25760
v_mfma_f32_16x16x32_bf16 a[224:227],v[76:79],a[176:179],a[224:227]
v_mfma_f32_16x16x32_bf16 a[228:231],v[76:79],a[180:183],a[228:231]
buffer_load_dwordx4 a[112:115],v28,s[28:31],s89 offen offset:3072 sc1
v_mfma_f32_16x16x32_bf16 a[228:231],v[72:75],a[164:167],a[228:231]
v_mfma_f32_16x16x32_bf16 a[228:231],v[68:71],a[148:151],a[228:231]
v_mfma_f32_16x16x32_bf16 a[228:231],v[64:67],a[132:135],a[228:231]
v_mfma_f32_16x16x32_bf16 a[232:235],v[64:67],a[136:139],a[232:235]
v_mfma_f32_16x16x32_bf16 a[232:235],v[68:71],a[152:155],a[232:235]
buffer_load_dwordx4 a[104:107],v28,s[28:31],s88 offen offset:3072 sc1
v_mfma_f32_16x16x32_bf16 a[232:235],v[72:75],a[168:171],a[232:235]
s_waitcnt vmcnt(10)
v_cmp_ne_u32 s[56:57],s52,v4
v_cmp_ne_u32 s[58:59],s53,v5
v_mfma_f32_16x16x32_bf16 a[232:235],v[76:79],a[184:187],a[232:235]
s_or_b64 s[56:57],s[56:57],s[58:59]
s_cbranch_scc1 L_fr3_%=
L_fo3_%=:
s_lshl_b32 s91,s64,6
v_mfma_f32_16x16x32_bf16 a[236:239],v[76:79],a[188:191],a[236:239]
ds_read_b64_tr_b16 v[76:77],v1 offset:30912
ds_read_b64_tr_b16 v[78:79],v1 offset:36032
v_mfma_f32_16x16x32_bf16 a[236:239],v[72:75],a[172:175],a[236:239]
ds_read_b64_tr_b16 v[72:73],v1 offset:20672
v_bfe_u32 v3,v6,4,2
v_mfma_f32_16x16x32_bf16 a[236:239],v[68:71],a[156:159],a[236:239]
buffer_load_dwordx4 v[32:35],v6,s[28:31],s20 offen sc1
v_mfma_f32_16x16x32_bf16 a[236:239],v[64:67],a[140:143],a[236:239]
s_waitcnt lgkmcnt(3)
ds_read_b64_tr_b16 v[64:65],v1 offset:192
ds_read_b64_tr_b16 v[66:67],v1 offset:5312
v_mfma_f32_16x16x32_bf16 a[240:243],v[80:83],a[128:131],a[240:243]
ds_read_b64_tr_b16 v[68:69],v1 offset:10432
v_min_u32 v3,2,v3
v_mfma_f32_16x16x32_bf16 a[240:243],v[84:87],a[144:147],a[240:243]
ds_read_b64_tr_b16 v[70:71],v1 offset:15552
v_add3_u32 v3,v3,s63,1
v_mfma_f32_16x16x32_bf16 a[240:243],v[88:91],a[160:163],a[240:243]
ds_read_b64_tr_b16 v[74:75],v1 offset:25792
v_and_b32 v3,3,v3
v_mfma_f32_16x16x32_bf16 a[240:243],v[92:95],a[176:179],a[240:243]
v_lshlrev_b32 v3,8,v3
v_mfma_f32_16x16x32_bf16 a[244:247],v[92:95],a[180:183],a[244:247]
v_mfma_f32_16x16x32_bf16 a[244:247],v[88:91],a[164:167],a[244:247]
buffer_load_dwordx4 v24,s[48:51],s83 offen lds
v_mfma_f32_16x16x32_bf16 a[244:247],v[84:87],a[148:151],a[244:247]
v_mfma_f32_16x16x32_bf16 a[244:247],v[80:83],a[132:135],a[244:247]
v_mfma_f32_16x16x32_bf16 a[248:251],v[80:83],a[136:139],a[248:251]
v_mfma_f32_16x16x32_bf16 a[248:251],v[84:87],a[152:155],a[248:251]
v_mfma_f32_16x16x32_bf16 a[248:251],v[88:91],a[168:171],a[248:251]
buffer_load_dwordx4 v[36:39],v6,s[28:31],s21 offen sc1
v_mfma_f32_16x16x32_bf16 a[248:251],v[92:95],a[184:187],a[248:251]
v_mfma_f32_16x16x32_bf16 a[252:255],v[92:95],a[188:191],a[252:255]
ds_read_b64_tr_b16 v[92:93],v2 offset:30944
ds_read_b64_tr_b16 v[94:95],v2 offset:36064
)A"
R"A(v_mfma_f32_16x16x32_bf16 a[252:255],v[88:91],a[172:175],a[252:255]
ds_read_b64_tr_b16 v[88:89],v2 offset:20704
v_mfma_f32_16x16x32_bf16 a[252:255],v[84:87],a[156:159],a[252:255]
ds_read_b64_tr_b16 v[84:85],v2 offset:10464
v_mfma_f32_16x16x32_bf16 a[252:255],v[80:83],a[140:143],a[252:255]
ds_read_b64_tr_b16 v[80:81],v2 offset:224
v_mfma_f32_16x16x32_bf16 a[0:3],v[96:99],a[128:131],a[0:3]
ds_read_b64_tr_b16 v[82:83],v2 offset:5344
ds_read_b64_tr_b16 v[86:87],v2 offset:15584
v_mfma_f32_16x16x32_bf16 a[0:3],v[100:103],a[144:147],a[0:3]
buffer_load_dwordx4 v25,s[48:51],s83 offen offset:1024 lds
v_mfma_f32_16x16x32_bf16 a[0:3],v[104:107],a[160:163],a[0:3]
ds_read_b64_tr_b16 v[90:91],v2 offset:25824
v_mfma_f32_16x16x32_bf16 a[0:3],v[108:111],a[176:179],a[0:3]
v_mfma_f32_16x16x32_bf16 a[4:7],v[108:111],a[180:183],a[4:7]
v_mfma_f32_16x16x32_bf16 a[4:7],v[104:107],a[164:167],a[4:7]
v_mfma_f32_16x16x32_bf16 a[4:7],v[100:103],a[148:151],a[4:7]
buffer_load_dwordx4 v[40:43],v6,s[28:31],s22 offen sc1
v_mfma_f32_16x16x32_bf16 a[4:7],v[96:99],a[132:135],a[4:7]
v_mfma_f32_16x16x32_bf16 a[8:11],v[96:99],a[136:139],a[8:11]
v_mfma_f32_16x16x32_bf16 a[8:11],v[100:103],a[152:155],a[8:11]
v_mfma_f32_16x16x32_bf16 a[8:11],v[104:107],a[168:171],a[8:11]
v_mfma_f32_16x16x32_bf16 a[8:11],v[108:111],a[184:187],a[8:11]
buffer_load_dwordx4 a[96:99],v28,s[28:31],s87 offen offset:3072 sc1
v_mfma_f32_16x16x32_bf16 a[12:15],v[108:111],a[188:191],a[12:15]
s_add_u32 s87,s64,3
ds_read_b64_tr_b16 v[108:109],v1 offset:30976
ds_read_b64_tr_b16 v[110:111],v1 offset:36096
v_mfma_f32_16x16x32_bf16 a[12:15],v[104:107],a[172:175],a[12:15]
s_and_b32 s87,s87,3
s_lshl_b32 s87,s87,1
ds_read_b64_tr_b16 v[104:105],v1 offset:20736
v_mfma_f32_16x16x32_bf16 a[12:15],v[100:103],a[156:159],a[12:15]
s_or_b32 s87,s87,1
s_lshl_b32 s87,s87,16
ds_read_b64_tr_b16 v[100:101],v1 offset:10496
v_mfma_f32_16x16x32_bf16 a[12:15],v[96:99],a[140:143],a[12:15]
s_add_u32 s88,s87,4096
s_add_u32 s89,s87,8192
ds_read_b64_tr_b16 v[96:97],v1 offset:256
v_mfma_f32_16x16x32_bf16 a[16:19],v[112:115],a[128:131],a[16:19]
buffer_load_dwordx4 v[44:47],v6,s[28:31],s23 offen sc1
v_mfma_f32_16x16x32_bf16 a[16:19],v[116:119],a[144:147],a[16:19]
s_add_u32 s90,s87,12288
ds_read_b64_tr_b16 v[98:99],v1 offset:5376
ds_read_b64_tr_b16 v[102:103],v1 offset:15616
v_mfma_f32_16x16x32_bf16 a[16:19],v[120:123],a[160:163],a[16:19]
ds_read_b64_tr_b16 v[106:107],v1 offset:25856
v_add_u32 v1,0xffff6000,v1
v_mfma_f32_16x16x32_bf16 a[16:19],v[124:127],a[176:179],a[16:19]
v_mfma_f32_16x16x32_bf16 a[20:23],v[124:127],a[180:183],a[20:23]
v_mfma_f32_16x16x32_bf16 a[20:23],v[120:123],a[164:167],a[20:23]
v_mfma_f32_16x16x32_bf16 a[20:23],v[116:119],a[148:151],a[20:23]
v_mfma_f32_16x16x32_bf16 a[20:23],v[112:115],a[132:135],a[20:23]
buffer_load_dwordx4 v26,s[48:51],s83 offen offset:2048 lds
v_mfma_f32_16x16x32_bf16 a[24:27],v[112:115],a[136:139],a[24:27]
v_mfma_f32_16x16x32_bf16 a[24:27],v[116:119],a[152:155],a[24:27]
v_mfma_f32_16x16x32_bf16 a[24:27],v[120:123],a[168:171],a[24:27]
v_mfma_f32_16x16x32_bf16 a[24:27],v[124:127],a[184:187],a[24:27]
v_mfma_f32_16x16x32_bf16 a[28:31],v[124:127],a[188:191],a[28:31]
buffer_load_dwordx4 v[180:183],v6,s[28:31],s23 offen offset:1024 sc1
v_mfma_f32_16x16x32_bf16 a[28:31],v[120:123],a[172:175],a[28:31]
ds_read_b64_tr_b16 v[120:121],v2 offset:20768
ds_read_b64_tr_b16 v[122:123],v2 offset:25888
v_mfma_f32_16x16x32_bf16 a[28:31],v[116:119],a[156:159],a[28:31]
ds_read_b64_tr_b16 v[116:117],v2 offset:10528
v_mfma_f32_16x16x32_bf16 a[28:31],v[112:115],a[140:143],a[28:31]
s_waitcnt lgkmcnt(3)
ds_read_b64_tr_b16 v[112:113],v2 offset:288
v_mfma_f32_16x16x32_bf16 v[128:131],v[64:67],a[128:131],v[128:131]
ds_read_b64_tr_b16 v[114:115],v2 offset:5408
v_mfma_f32_16x16x32_bf16 v[128:131],v[68:71],a[144:147],v[128:131]
ds_read_b64_tr_b16 v[118:119],v2 offset:15648
ds_read_b64_tr_b16 v[124:125],v2 offset:31008
v_mfma_f32_16x16x32_bf16 v[128:131],v[72:75],a[160:163],v[128:131]
ds_read_b64_tr_b16 v[126:127],v2 offset:36128
v_add_u32 v2,0xffff6000,v2
v_mfma_f32_16x16x32_bf16 v[128:131],v[76:79],a[176:179],v[128:131]
buffer_load_dwordx4 v[164:167],v6,s[28:31],s22 offen offset:1024 sc1
v_mfma_f32_16x16x32_bf16 v[136:139],v[76:79],a[180:183],v[136:139]
v_mfma_f32_16x16x32_bf16 v[136:139],v[72:75],a[164:167],v[136:139]
v_mfma_f32_16x16x32_bf16 v[136:139],v[68:71],a[148:151],v[136:139]
v_mfma_f32_16x16x32_bf16 v[136:139],v[64:67],a[132:135],v[136:139]
v_mfma_f32_16x16x32_bf16 v[144:147],v[64:67],a[136:139],v[144:147]
v_mfma_f32_16x16x32_bf16 v[144:147],v[68:71],a[152:155],v[144:147]
v_mfma_f32_16x16x32_bf16 v[144:147],v[72:75],a[168:171],v[144:147]
buffer_load_dwordx4 v27,s[48:51],s83 offen offset:3072 lds
v_mfma_f32_16x16x32_bf16 v[144:147],v[76:79],a[184:187],v[144:147]
s_add_u32 m0,s86,126976
v_mfma_f32_16x16x32_bf16 v[152:155],v[76:79],a[188:191],v[152:155]
ds_read_b64_tr_b16 v[76:77],v1 offset:30720
ds_read_b64_tr_b16 v[78:79],v1 offset:35840
v_mfma_f32_16x16x32_bf16 v[152:155],v[72:75],a[172:175],v[152:155]
ds_read_b64_tr_b16 v[72:73],v1 offset:20480
v_mfma_f32_16x16x32_bf16 v[152:155],v[68:71],a[156:159],v[152:155]
ds_read_b64_tr_b16 v[68:69],v1 offset:10240
v_mfma_f32_16x16x32_bf16 v[152:155],v[64:67],a[140:143],v[152:155]
buffer_load_dwordx4 v[148:151],v6,s[28:31],s21 offen offset:1024 sc1
v_mfma_f32_16x16x32_bf16 v[160:163],v[80:83],a[128:131],v[160:163]
ds_read_b64_tr_b16 v[64:65],v1
ds_read_b64_tr_b16 v[66:67],v1 offset:5120
v_mfma_f32_16x16x32_bf16 v[160:163],v[84:87],a[144:147],v[160:163]
ds_read_b64_tr_b16 v[70:71],v1 offset:15360
v_mfma_f32_16x16x32_bf16 v[160:163],v[88:91],a[160:163],v[160:163]
ds_read_b64_tr_b16 v[74:75],v1 offset:25600
v_mfma_f32_16x16x32_bf16 v[160:163],v[92:95],a[176:179],v[160:163]
v_mfma_f32_16x16x32_bf16 v[168:171],v[92:95],a[180:183],v[168:171]
v_mfma_f32_16x16x32_bf16 v[168:171],v[88:91],a[164:167],v[168:171]
v_mfma_f32_16x16x32_bf16 v[168:171],v[84:87],a[148:151],v[168:171]
buffer_load_dwordx4 v7,s[48:51],s83 offen lds
v_mfma_f32_16x16x32_bf16 v[168:171],v[80:83],a[132:135],v[168:171]
s_add_u32 m0,s86,128000
s_mul_i32 s83,s63,20480
v_mfma_f32_16x16x32_bf16 v[176:179],v[80:83],a[136:139],v[176:179]
v_mfma_f32_16x16x32_bf16 v[176:179],v[84:87],a[152:155],v[176:179]
v_mfma_f32_16x16x32_bf16 v[176:179],v[88:91],a[168:171],v[176:179]
v_mfma_f32_16x16x32_bf16 v[176:179],v[92:95],a[184:187],v[176:179]
buffer_load_dwordx4 v[132:135],v6,s[28:31],s20 offen offset:1024 sc1
v_mfma_f32_16x16x32_bf16 v[184:187],v[92:95],a[188:191],v[184:187]
ds_read_b64_tr_b16 v[92:93],v2 offset:30752
ds_read_b64_tr_b16 v[94:95],v2 offset:35872
v_mfma_f32_16x16x32_bf16 v[184:187],v[88:91],a[172:175],v[184:187]
ds_read_b64_tr_b16 v[88:89],v2 offset:20512
v_mfma_f32_16x16x32_bf16 v[184:187],v[84:87],a[156:159],v[184:187]
ds_read_b64_tr_b16 v[84:85],v2 offset:10272
v_mfma_f32_16x16x32_bf16 v[184:187],v[80:83],a[140:143],v[184:187]
ds_read_b64_tr_b16 v[80:81],v2 offset:32
v_mfma_f32_16x16x32_bf16 v[192:195],v[96:99],a[128:131],v[192:195]
ds_read_b64_tr_b16 v[82:83],v2 offset:5152
ds_read_b64_tr_b16 v[86:87],v2 offset:15392
v_mfma_f32_16x16x32_bf16 v[192:195],v[100:103],a[144:147],v[192:195]
ds_read_b64_tr_b16 v[90:91],v2 offset:25632
v_mfma_f32_16x16x32_bf16 v[192:195],v[104:107],a[160:163],v[192:195]
buffer_load_dwordx4 v24,s[48:51],s84 offen lds
v_mfma_f32_16x16x32_bf16 v[192:195],v[108:111],a[176:179],v[192:195]
v_mfma_f32_16x16x32_bf16 v[200:203],v[108:111],a[180:183],v[200:203]
v_mfma_f32_16x16x32_bf16 v[200:203],v[104:107],a[164:167],v[200:203]
v_mfma_f32_16x16x32_bf16 v[200:203],v[100:103],a[148:151],v[200:203]
v_mfma_f32_16x16x32_bf16 v[200:203],v[96:99],a[132:135],v[200:203]
buffer_load_dwordx4 v[196:199],v6,s[28:31],s20 offen offset:2048 sc1
v_mfma_f32_16x16x32_bf16 v[208:211],v[96:99],a[136:139],v[208:211]
v_mfma_f32_16x16x32_bf16 v[208:211],v[100:103],a[152:155],v[208:211]
v_mfma_f32_16x16x32_bf16 v[208:211],v[104:107],a[168:171],v[208:211]
v_mfma_f32_16x16x32_bf16 v[208:211],v[108:111],a[184:187],v[208:211]
v_mfma_f32_16x16x32_bf16 v[216:219],v[108:111],a[188:191],v[216:219]
ds_read_b64_tr_b16 v[108:109],v1 offset:30784
ds_read_b64_tr_b16 v[110:111],v1 offset:35904
v_mfma_f32_16x16x32_bf16 v[216:219],v[104:107],a[172:175],v[216:219]
ds_read_b64_tr_b16 v[104:105],v1 offset:20544
v_mfma_f32_16x16x32_bf16 v[216:219],v[100:103],a[156:159],v[216:219]
buffer_load_dwordx4 v25,s[48:51],s84 offen offset:1024 lds
v_mfma_f32_16x16x32_bf16 v[216:219],v[96:99],a[140:143],v[216:219]
s_waitcnt lgkmcnt(3)
ds_read_b64_tr_b16 v[96:97],v1 offset:64
ds_read_b64_tr_b16 v[98:99],v1 offset:5184
v_mfma_f32_16x16x32_bf16 v[224:227],v[112:115],a[128:131],v[224:227]
ds_read_b64_tr_b16 v[100:101],v1 offset:10304
v_mfma_f32_16x16x32_bf16 v[224:227],v[116:119],a[144:147],v[224:227]
ds_read_b64_tr_b16 v[102:103],v1 offset:15424
v_mfma_f32_16x16x32_bf16 v[224:227],v[120:123],a[160:163],v[224:227]
ds_read_b64_tr_b16 v[106:107],v1 offset:25664
v_mfma_f32_16x16x32_bf16 v[224:227],v[124:127],a[176:179],v[224:227]
buffer_load_dwordx4 v[212:215],v6,s[28:31],s21 offen offset:2048 sc1
v_mfma_f32_16x16x32_bf16 v[232:235],v[124:127],a[180:183],v[232:235]
v_mfma_f32_16x16x32_bf16 v[232:235],v[120:123],a[164:167],v[232:235]
v_mfma_f32_16x16x32_bf16 v[232:235],v[116:119],a[148:151],v[232:235]
v_mfma_f32_16x16x32_bf16 v[232:235],v[112:115],a[132:135],v[232:235]
v_mfma_f32_16x16x32_bf16 v[240:243],v[112:115],a[136:139],v[240:243]
v_mfma_f32_16x16x32_bf16 v[240:243],v[116:119],a[152:155],v[240:243]
v_mfma_f32_16x16x32_bf16 v[240:243],v[120:123],a[168:171],v[240:243]
buffer_load_dwordx4 v[228:231],v6,s[28:31],s22 offen offset:2048 sc1
v_mfma_f32_16x16x32_bf16 v[240:243],v[124:127],a[184:187],v[240:243]
v_mfma_f32_16x16x32_bf16 v[248:251],v[124:127],a[188:191],v[248:251]
ds_read_b64_tr_b16 v[124:125],v2 offset:30816
ds_read_b64_tr_b16 v[126:127],v2 offset:35936
v_mfma_f32_16x16x32_bf16 v[248:251],v[120:123],a[172:175],v[248:251]
ds_read_b64_tr_b16 v[120:121],v2 offset:20576
v_mfma_f32_16x16x32_bf16 v[248:251],v[116:119],a[156:159],v[248:251]
ds_read_b64_tr_b16 v[116:117],v2 offset:10336
v_mfma_f32_16x16x32_bf16 v[248:251],v[112:115],a[140:143],v[248:251]
ds_read_b64_tr_b16 v[112:113],v2 offset:96
v_mfma_f32_16x16x32_bf16 a[192:195],v[64:67],a[68:71],a[192:195]
ds_read_b64_tr_b16 v[114:115],v2 offset:5216
ds_read_b64_tr_b16 v[118:119],v2 offset:15456
v_mfma_f32_16x16x32_bf16 a[192:195],v[68:71],a[76:79],a[192:195]
buffer_load_dwordx4 v26,s[48:51],s84 offen offset:2048 lds
v_mfma_f32_16x16x32_bf16 a[192:195],v[72:75],a[84:87],a[192:195]
ds_read_b64_tr_b16 v[122:123],v2 offset:25696
v_mfma_f32_16x16x32_bf16 a[192:195],v[76:79],a[92:95],a[192:195]
v_mfma_f32_16x16x32_bf16 a[196:199],v[76:79],a[124:127],a[196:199]
s_waitcnt vmcnt(28)
v_mfma_f32_16x16x32_bf16 a[196:199],v[72:75],a[116:119],a[196:199]
s_waitcnt vmcnt(19)
v_mfma_f32_16x16x32_bf16 a[196:199],v[68:71],a[108:111],a[196:199]
buffer_load_dwordx4 v[244:247],v6,s[28:31],s23 offen offset:2048 sc1
v_mfma_f32_16x16x32_bf16 a[196:199],v[64:67],a[100:103],a[196:199]
v_mfma_f32_16x16x32_bf16 a[200:203],v[64:67],a[64:67],a[200:203]
v_mfma_f32_16x16x32_bf16 a[200:203],v[68:71],a[72:75],a[200:203]
v_mfma_f32_16x16x32_bf16 a[200:203],v[72:75],a[80:83],a[200:203]
v_mfma_f32_16x16x32_bf16 a[200:203],v[76:79],a[88:91],a[200:203]
v_mfma_f32_16x16x32_bf16 a[204:207],v[76:79],a[120:123],a[204:207]
s_waitcnt vmcnt(15)
ds_read_b64_tr_b16 v[76:77],v1 offset:30848
ds_read_b64_tr_b16 v[78:79],v1 offset:35968
v_mfma_f32_16x16x32_bf16 a[204:207],v[72:75],a[112:115],a[204:207]
buffer_load_dwordx4 v27,s[48:51],s84 offen offset:3072 lds
v_mfma_f32_16x16x32_bf16 a[204:207],v[68:71],a[104:107],a[204:207]
s_add_u32 m0,s86,132096
ds_read_b64_tr_b16 v[68:69],v1 offset:10368
ds_read_b64_tr_b16 v[70:71],v1 offset:15488
v_mfma_f32_16x16x32_bf16 a[204:207],v[64:67],a[96:99],a[204:207]
ds_read_b64_tr_b16 v[64:65],v1 offset:128
v_mfma_f32_16x16x32_bf16 a[208:211],v[80:83],a[68:71],a[208:211]
ds_read_b64_tr_b16 v[66:67],v1 offset:5248
v_mfma_f32_16x16x32_bf16 a[208:211],v[84:87],a[76:79],a[208:211]
)A"
R"A(ds_read_b64_tr_b16 v[72:73],v1 offset:20608
v_mfma_f32_16x16x32_bf16 a[208:211],v[88:91],a[84:87],a[208:211]
buffer_load_dwordx4 v[188:191],v28,s[28:31],s23 offen offset:3072 sc1
v_mfma_f32_16x16x32_bf16 a[208:211],v[92:95],a[92:95],a[208:211]
ds_read_b64_tr_b16 v[74:75],v1 offset:25728
v_mfma_f32_16x16x32_bf16 a[212:215],v[92:95],a[124:127],a[212:215]
v_mfma_f32_16x16x32_bf16 a[212:215],v[88:91],a[116:119],a[212:215]
v_mfma_f32_16x16x32_bf16 a[212:215],v[84:87],a[108:111],a[212:215]
v_mfma_f32_16x16x32_bf16 a[212:215],v[80:83],a[100:103],a[212:215]
v_mfma_f32_16x16x32_bf16 a[216:219],v[80:83],a[64:67],a[216:219]
v_mfma_f32_16x16x32_bf16 a[216:219],v[84:87],a[72:75],a[216:219]
buffer_load_dwordx4 v7,s[48:51],s84 offen lds
v_mfma_f32_16x16x32_bf16 a[216:219],v[88:91],a[80:83],a[216:219]
v_mfma_f32_16x16x32_bf16 a[216:219],v[92:95],a[88:91],a[216:219]
v_mfma_f32_16x16x32_bf16 a[220:223],v[92:95],a[120:123],a[220:223]
ds_read_b64_tr_b16 v[92:93],v2 offset:30880
ds_read_b64_tr_b16 v[94:95],v2 offset:36000
v_mfma_f32_16x16x32_bf16 a[220:223],v[88:91],a[112:115],a[220:223]
ds_read_b64_tr_b16 v[88:89],v2 offset:20640
v_mfma_f32_16x16x32_bf16 a[220:223],v[84:87],a[104:107],a[220:223]
buffer_load_dwordx4 v[172:175],v28,s[28:31],s22 offen offset:3072 sc1
v_mfma_f32_16x16x32_bf16 a[220:223],v[80:83],a[96:99],a[220:223]
s_waitcnt lgkmcnt(3)
ds_read_b64_tr_b16 v[80:81],v2 offset:160
ds_read_b64_tr_b16 v[82:83],v2 offset:5280
v_mfma_f32_16x16x32_bf16 a[224:227],v[96:99],a[68:71],a[224:227]
ds_read_b64_tr_b16 v[84:85],v2 offset:10400
v_mfma_f32_16x16x32_bf16 a[224:227],v[100:103],a[76:79],a[224:227]
ds_read_b64_tr_b16 v[86:87],v2 offset:15520
v_mfma_f32_16x16x32_bf16 a[224:227],v[104:107],a[84:87],a[224:227]
ds_read_b64_tr_b16 v[90:91],v2 offset:25760
v_mfma_f32_16x16x32_bf16 a[224:227],v[108:111],a[92:95],a[224:227]
buffer_load_dwordx4 a[128:131],v6,s[28:31],s87 offen sc1
v_mfma_f32_16x16x32_bf16 a[228:231],v[108:111],a[124:127],a[228:231]
v_mfma_f32_16x16x32_bf16 a[228:231],v[104:107],a[116:119],a[228:231]
v_mfma_f32_16x16x32_bf16 a[228:231],v[100:103],a[108:111],a[228:231]
v_mfma_f32_16x16x32_bf16 a[228:231],v[96:99],a[100:103],a[228:231]
v_mfma_f32_16x16x32_bf16 a[232:235],v[96:99],a[64:67],a[232:235]
buffer_load_dwordx4 v[156:159],v28,s[28:31],s21 offen offset:3072 sc1
v_mfma_f32_16x16x32_bf16 a[232:235],v[100:103],a[72:75],a[232:235]
v_mfma_f32_16x16x32_bf16 a[232:235],v[104:107],a[80:83],a[232:235]
v_mfma_f32_16x16x32_bf16 a[232:235],v[108:111],a[88:91],a[232:235]
v_mfma_f32_16x16x32_bf16 a[236:239],v[108:111],a[120:123],a[236:239]
ds_read_b64_tr_b16 v[108:109],v1 offset:30912
ds_read_b64_tr_b16 v[110:111],v1 offset:36032
v_mfma_f32_16x16x32_bf16 a[236:239],v[104:107],a[112:115],a[236:239]
buffer_load_dwordx4 a[144:147],v6,s[28:31],s88 offen sc1
v_mfma_f32_16x16x32_bf16 a[236:239],v[100:103],a[104:107],a[236:239]
ds_read_b64_tr_b16 v[100:101],v1 offset:10432
ds_read_b64_tr_b16 v[102:103],v1 offset:15552
v_mfma_f32_16x16x32_bf16 a[236:239],v[96:99],a[96:99],a[236:239]
ds_read_b64_tr_b16 v[96:97],v1 offset:192
v_mfma_f32_16x16x32_bf16 a[240:243],v[112:115],a[68:71],a[240:243]
ds_read_b64_tr_b16 v[98:99],v1 offset:5312
v_mfma_f32_16x16x32_bf16 a[240:243],v[116:119],a[76:79],a[240:243]
ds_read_b64_tr_b16 v[104:105],v1 offset:20672
v_mfma_f32_16x16x32_bf16 a[240:243],v[120:123],a[84:87],a[240:243]
buffer_load_dwordx4 v[140:143],v28,s[28:31],s20 offen offset:3072 sc1
v_mfma_f32_16x16x32_bf16 a[240:243],v[124:127],a[92:95],a[240:243]
ds_read_b64_tr_b16 v[106:107],v1 offset:25792
v_add_u32 v1,0xffff6000,v1
v_mfma_f32_16x16x32_bf16 a[244:247],v[124:127],a[124:127],a[244:247]
v_mfma_f32_16x16x32_bf16 a[244:247],v[120:123],a[116:119],a[244:247]
v_mfma_f32_16x16x32_bf16 a[244:247],v[116:119],a[108:111],a[244:247]
v_mfma_f32_16x16x32_bf16 a[244:247],v[112:115],a[100:103],a[244:247]
buffer_load_dwordx4 a[160:163],v6,s[28:31],s89 offen sc1
v_mfma_f32_16x16x32_bf16 a[248:251],v[112:115],a[64:67],a[248:251]
v_mfma_f32_16x16x32_bf16 a[248:251],v[116:119],a[72:75],a[248:251]
v_mfma_f32_16x16x32_bf16 a[248:251],v[120:123],a[80:83],a[248:251]
v_mfma_f32_16x16x32_bf16 a[248:251],v[124:127],a[88:91],a[248:251]
v_mfma_f32_16x16x32_bf16 a[252:255],v[124:127],a[120:123],a[252:255]
buffer_load_dwordx4 a[176:179],v6,s[28:31],s90 offen sc1
v_mfma_f32_16x16x32_bf16 a[252:255],v[120:123],a[112:115],a[252:255]
ds_read_b64_tr_b16 v[120:121],v2 offset:20704
ds_read_b64_tr_b16 v[122:123],v2 offset:25824
v_mfma_f32_16x16x32_bf16 a[252:255],v[116:119],a[104:107],a[252:255]
ds_read_b64_tr_b16 v[116:117],v2 offset:10464
v_mfma_f32_16x16x32_bf16 a[252:255],v[112:115],a[96:99],a[252:255]
ds_read_b64_tr_b16 v[112:113],v2 offset:224
v_mfma_f32_16x16x32_bf16 a[0:3],v[64:67],a[68:71],a[0:3]
ds_read_b64_tr_b16 v[114:115],v2 offset:5344
v_mfma_f32_16x16x32_bf16 a[0:3],v[68:71],a[76:79],a[0:3]
buffer_load_dwordx4 a[180:183],v6,s[28:31],s90 offen offset:1024 sc1
v_mfma_f32_16x16x32_bf16 a[0:3],v[72:75],a[84:87],a[0:3]
ds_read_b64_tr_b16 v[118:119],v2 offset:15584
ds_read_b64_tr_b16 v[124:125],v2 offset:30944
v_mfma_f32_16x16x32_bf16 a[0:3],v[76:79],a[92:95],a[0:3]
ds_read_b64_tr_b16 v[126:127],v2 offset:36064
v_add_u32 v2,0xffff6000,v2
v_mfma_f32_16x16x32_bf16 a[4:7],v[76:79],a[124:127],a[4:7]
v_mfma_f32_16x16x32_bf16 a[4:7],v[72:75],a[116:119],a[4:7]
v_mfma_f32_16x16x32_bf16 a[4:7],v[68:71],a[108:111],a[4:7]
buffer_load_dwordx4 a[164:167],v6,s[28:31],s89 offen offset:1024 sc1
v_mfma_f32_16x16x32_bf16 a[4:7],v[64:67],a[100:103],a[4:7]
v_mfma_f32_16x16x32_bf16 a[8:11],v[64:67],a[64:67],a[8:11]
v_mfma_f32_16x16x32_bf16 a[8:11],v[68:71],a[72:75],a[8:11]
v_mfma_f32_16x16x32_bf16 a[8:11],v[72:75],a[80:83],a[8:11]
v_mfma_f32_16x16x32_bf16 a[8:11],v[76:79],a[88:91],a[8:11]
buffer_load_dwordx4 a[148:151],v6,s[28:31],s88 offen offset:1024 sc1
v_mfma_f32_16x16x32_bf16 a[12:15],v[76:79],a[120:123],a[12:15]
v_mfma_f32_16x16x32_bf16 a[12:15],v[72:75],a[112:115],a[12:15]
v_mfma_f32_16x16x32_bf16 a[12:15],v[68:71],a[104:107],a[12:15]
v_mfma_f32_16x16x32_bf16 a[12:15],v[64:67],a[96:99],a[12:15]
s_waitcnt vmcnt(10)
s_barrier
s_waitcnt lgkmcnt(0)
v_mfma_f32_16x16x32_bf16 a[16:19],v[80:83],a[68:71],a[16:19]
buffer_load_dwordx4 a[132:135],v6,s[28:31],s87 offen offset:1024 sc1
v_mfma_f32_16x16x32_bf16 a[16:19],v[84:87],a[76:79],a[16:19]
ds_read_b64_tr_b16 v[64:65],v1
ds_read_b64_tr_b16 v[66:67],v1 offset:5120
v_mfma_f32_16x16x32_bf16 a[16:19],v[88:91],a[84:87],a[16:19]
ds_read_b64_tr_b16 v[68:69],v1 offset:10240
v_mfma_f32_16x16x32_bf16 a[16:19],v[92:95],a[92:95],a[16:19]
s_nop 0
L_pd17_%=:
s_cbranch_execz L_trtg2F_%=
ds_read_b64_tr_b16 v[70:71],v1 offset:15360
v_mfma_f32_16x16x32_bf16 a[20:23],v[92:95],a[124:127],a[20:23]
ds_read_b64_tr_b16 v[72:73],v1 offset:20480
v_mfma_f32_16x16x32_bf16 a[20:23],v[88:91],a[116:119],a[20:23]
buffer_load_dwordx4 a[136:139],v6,s[28:31],s87 offen offset:2048 sc1
v_mfma_f32_16x16x32_bf16 a[20:23],v[84:87],a[108:111],a[20:23]
ds_read_b64_tr_b16 v[74:75],v1 offset:25600
ds_read_b64_tr_b16 v[76:77],v1 offset:30720
v_mfma_f32_16x16x32_bf16 a[20:23],v[80:83],a[100:103],a[20:23]
ds_read_b64_tr_b16 v[78:79],v1 offset:35840
v_add_u32 v1,0x1e000,v1
v_mfma_f32_16x16x32_bf16 a[24:27],v[80:83],a[64:67],a[24:27]
v_mfma_f32_16x16x32_bf16 a[24:27],v[84:87],a[72:75],a[24:27]
v_mfma_f32_16x16x32_bf16 a[24:27],v[88:91],a[80:83],a[24:27]
buffer_load_dwordx4 a[152:155],v6,s[28:31],s88 offen offset:2048 sc1
v_mfma_f32_16x16x32_bf16 a[24:27],v[92:95],a[88:91],a[24:27]
v_mfma_f32_16x16x32_bf16 a[28:31],v[92:95],a[120:123],a[28:31]
ds_read_b64_tr_b16 v[92:93],v2 offset:30752
ds_read_b64_tr_b16 v[94:95],v2 offset:35872
v_mfma_f32_16x16x32_bf16 a[28:31],v[88:91],a[112:115],a[28:31]
ds_read_b64_tr_b16 v[88:89],v2 offset:20512
v_mfma_f32_16x16x32_bf16 a[28:31],v[84:87],a[104:107],a[28:31]
ds_read_b64_tr_b16 v[84:85],v2 offset:10272
v_mfma_f32_16x16x32_bf16 a[28:31],v[80:83],a[96:99],a[28:31]
buffer_load_dwordx4 a[168:171],v6,s[28:31],s89 offen offset:2048 sc1
v_mfma_f32_16x16x32_bf16 v[128:131],v[96:99],a[68:71],v[128:131]
ds_read_b64_tr_b16 v[80:81],v2 offset:32
ds_read_b64_tr_b16 v[82:83],v2 offset:5152
v_mfma_f32_16x16x32_bf16 v[128:131],v[100:103],a[76:79],v[128:131]
ds_read_b64_tr_b16 v[86:87],v2 offset:15392
v_mfma_f32_16x16x32_bf16 v[128:131],v[104:107],a[84:87],v[128:131]
ds_read_b64_tr_b16 v[90:91],v2 offset:25632
v_add_u32 v2,0x1e000,v2
v_mfma_f32_16x16x32_bf16 v[128:131],v[108:111],a[92:95],v[128:131]
v_mfma_f32_16x16x32_bf16 v[136:139],v[108:111],a[124:127],v[136:139]
buffer_load_dwordx4 a[184:187],v6,s[28:31],s90 offen offset:2048 sc1
v_mfma_f32_16x16x32_bf16 v[136:139],v[104:107],a[116:119],v[136:139]
v_mfma_f32_16x16x32_bf16 v[136:139],v[100:103],a[108:111],v[136:139]
v_mfma_f32_16x16x32_bf16 v[136:139],v[96:99],a[100:103],v[136:139]
v_mfma_f32_16x16x32_bf16 v[144:147],v[96:99],a[64:67],v[144:147]
v_mfma_f32_16x16x32_bf16 v[144:147],v[100:103],a[72:75],v[144:147]
buffer_load_dwordx4 a[188:191],v28,s[28:31],s90 offen offset:3072 sc1
v_mfma_f32_16x16x32_bf16 v[144:147],v[104:107],a[80:83],v[144:147]
v_mfma_f32_16x16x32_bf16 v[144:147],v[108:111],a[88:91],v[144:147]
v_mfma_f32_16x16x32_bf16 v[152:155],v[108:111],a[120:123],v[152:155]
ds_read_b64_tr_b16 v[108:109],v1 offset:30720
ds_read_b64_tr_b16 v[110:111],v1 offset:35840
v_mfma_f32_16x16x32_bf16 v[152:155],v[104:107],a[112:115],v[152:155]
ds_read_b64_tr_b16 v[104:105],v1 offset:20480
v_mfma_f32_16x16x32_bf16 v[152:155],v[100:103],a[104:107],v[152:155]
buffer_load_dwordx4 a[172:175],v28,s[28:31],s89 offen offset:3072 sc1
v_mfma_f32_16x16x32_bf16 v[152:155],v[96:99],a[96:99],v[152:155]
ds_read_b64_tr_b16 v[96:97],v1
ds_read_b64_tr_b16 v[98:99],v1 offset:5120
v_mfma_f32_16x16x32_bf16 v[160:163],v[112:115],a[68:71],v[160:163]
ds_read_b64_tr_b16 v[100:101],v1 offset:10240
v_mfma_f32_16x16x32_bf16 v[160:163],v[116:119],a[76:79],v[160:163]
ds_read_b64_tr_b16 v[102:103],v1 offset:15360
v_mfma_f32_16x16x32_bf16 v[160:163],v[120:123],a[84:87],v[160:163]
ds_read_b64_tr_b16 v[106:107],v1 offset:25600
v_add_u32 v1,0xfffe2000,v1
v_mfma_f32_16x16x32_bf16 v[160:163],v[124:127],a[92:95],v[160:163]
buffer_load_dwordx4 a[156:159],v28,s[28:31],s88 offen offset:3072 sc1
v_mfma_f32_16x16x32_bf16 v[168:171],v[124:127],a[124:127],v[168:171]
v_mfma_f32_16x16x32_bf16 v[168:171],v[120:123],a[116:119],v[168:171]
v_mfma_f32_16x16x32_bf16 v[168:171],v[116:119],a[108:111],v[168:171]
v_mfma_f32_16x16x32_bf16 v[168:171],v[112:115],a[100:103],v[168:171]
v_mfma_f32_16x16x32_bf16 v[176:179],v[112:115],a[64:67],v[176:179]
buffer_load_dwordx4 a[140:143],v28,s[28:31],s87 offen offset:3072 sc1
v_mfma_f32_16x16x32_bf16 v[176:179],v[116:119],a[72:75],v[176:179]
s_mul_i32 s87,s60,81920
s_add_u32 s87,s87,s66
s_add_u32 s83,s83,s87
v_mfma_f32_16x16x32_bf16 v[176:179],v[120:123],a[80:83],v[176:179]
s_lshl_b32 s66,s63,6
s_add_u32 s87,s87,s66
s_add_u32 s88,s87,20480
v_mfma_f32_16x16x32_bf16 v[176:179],v[124:127],a[88:91],v[176:179]
s_add_u32 s89,s87,40960
s_add_u32 s90,s87,61440
s_add_u32 s66,s77,4096
v_mfma_f32_16x16x32_bf16 v[184:187],v[124:127],a[120:123],v[184:187]
ds_read_b64_tr_b16 v[124:125],v2 offset:30752
ds_read_b64_tr_b16 v[126:127],v2 offset:35872
v_mfma_f32_16x16x32_bf16 v[184:187],v[120:123],a[112:115],v[184:187]
ds_read_b64_tr_b16 v[120:121],v2 offset:20512
v_mfma_f32_16x16x32_bf16 v[184:187],v[116:119],a[104:107],v[184:187]
ds_read_b64_tr_b16 v[116:117],v2 offset:10272
v_mfma_f32_16x16x32_bf16 v[184:187],v[112:115],a[96:99],v[184:187]
s_waitcnt lgkmcnt(4)
ds_read_b64_tr_b16 v[112:113],v2 offset:32
v_mfma_f32_16x16x32_bf16 a[192:195],v[64:67],v[32:35],a[192:195]
ds_read_b64_tr_b16 v[114:115],v2 offset:5152
ds_read_b64_tr_b16 v[118:119],v2 offset:15392
v_mfma_f32_16x16x32_bf16 a[192:195],v[68:71],v[36:39],a[192:195]
ds_read_b64_tr_b16 v[122:123],v2 offset:25632
v_add_u32 v2,0xfffe2000,v2
v_mfma_f32_16x16x32_bf16 a[192:195],v[72:75],v[40:43],a[192:195]
v_mfma_f32_16x16x32_bf16 a[192:195],v[76:79],v[44:47],a[192:195]
v_mfma_f32_16x16x32_bf16 a[196:199],v[76:79],v[180:183],a[196:199]
v_mfma_f32_16x16x32_bf16 a[196:199],v[72:75],v[164:167],a[196:199]
)A"
R"A(v_mfma_f32_16x16x32_bf16 a[196:199],v[68:71],v[148:151],a[196:199]
v_mfma_f32_16x16x32_bf16 a[196:199],v[64:67],v[132:135],a[196:199]
v_mfma_f32_16x16x32_bf16 a[200:203],v[64:67],v[196:199],a[200:203]
v_mfma_f32_16x16x32_bf16 a[200:203],v[68:71],v[212:215],a[200:203]
v_mfma_f32_16x16x32_bf16 a[200:203],v[72:75],v[228:231],a[200:203]
v_mfma_f32_16x16x32_bf16 a[200:203],v[76:79],v[244:247],a[200:203]
v_mfma_f32_16x16x32_bf16 a[204:207],v[76:79],v[188:191],a[204:207]
s_waitcnt vmcnt(18)
ds_read_b64_tr_b16 v[76:77],v1 offset:30784
ds_read_b64_tr_b16 v[78:79],v1 offset:35904
v_mfma_f32_16x16x32_bf16 a[204:207],v[72:75],v[172:175],a[204:207]
s_waitcnt vmcnt(16)
ds_read_b64_tr_b16 v[72:73],v1 offset:20544
v_mfma_f32_16x16x32_bf16 a[204:207],v[68:71],v[156:159],a[204:207]
s_waitcnt vmcnt(14)
ds_read_b64_tr_b16 v[68:69],v1 offset:10304
v_mfma_f32_16x16x32_bf16 a[204:207],v[64:67],v[140:143],a[204:207]
ds_read_b64_tr_b16 v[64:65],v1 offset:64
v_mfma_f32_16x16x32_bf16 a[208:211],v[80:83],v[32:35],a[208:211]
ds_read_b64_tr_b16 v[66:67],v1 offset:5184
ds_read_b64_tr_b16 v[70:71],v1 offset:15424
v_mfma_f32_16x16x32_bf16 a[208:211],v[84:87],v[36:39],a[208:211]
ds_read_b64_tr_b16 v[74:75],v1 offset:25664
v_add_u32 v1,0x1e000,v1
v_mfma_f32_16x16x32_bf16 a[208:211],v[88:91],v[40:43],a[208:211]
v_mfma_f32_16x16x32_bf16 a[208:211],v[92:95],v[44:47],a[208:211]
v_mfma_f32_16x16x32_bf16 a[212:215],v[92:95],v[180:183],a[212:215]
v_mfma_f32_16x16x32_bf16 a[212:215],v[88:91],v[164:167],a[212:215]
v_mfma_f32_16x16x32_bf16 a[212:215],v[84:87],v[148:151],a[212:215]
v_mfma_f32_16x16x32_bf16 a[212:215],v[80:83],v[132:135],a[212:215]
v_mfma_f32_16x16x32_bf16 a[216:219],v[80:83],v[196:199],a[216:219]
v_mfma_f32_16x16x32_bf16 a[216:219],v[84:87],v[212:215],a[216:219]
v_mfma_f32_16x16x32_bf16 a[216:219],v[88:91],v[228:231],a[216:219]
v_mfma_f32_16x16x32_bf16 a[216:219],v[92:95],v[244:247],a[216:219]
v_mfma_f32_16x16x32_bf16 a[220:223],v[92:95],v[188:191],a[220:223]
ds_read_b64_tr_b16 v[92:93],v2 offset:30816
ds_read_b64_tr_b16 v[94:95],v2 offset:35936
v_mfma_f32_16x16x32_bf16 a[220:223],v[88:91],v[172:175],a[220:223]
ds_read_b64_tr_b16 v[88:89],v2 offset:20576
v_mfma_f32_16x16x32_bf16 a[220:223],v[84:87],v[156:159],a[220:223]
ds_read_b64_tr_b16 v[84:85],v2 offset:10336
v_mfma_f32_16x16x32_bf16 a[220:223],v[80:83],v[140:143],a[220:223]
ds_read_b64_tr_b16 v[80:81],v2 offset:96
v_mfma_f32_16x16x32_bf16 a[192:195],v[96:99],a[128:131],a[192:195]
ds_read_b64_tr_b16 v[82:83],v2 offset:5216
ds_read_b64_tr_b16 v[86:87],v2 offset:15456
v_mfma_f32_16x16x32_bf16 a[192:195],v[100:103],a[144:147],a[192:195]
s_waitcnt vmcnt(13)
ds_read_b64_tr_b16 v[90:91],v2 offset:25696
v_add_u32 v2,0x1e000,v2
v_mfma_f32_16x16x32_bf16 a[192:195],v[104:107],a[160:163],a[192:195]
s_waitcnt vmcnt(12)
v_mfma_f32_16x16x32_bf16 a[192:195],v[108:111],a[176:179],a[192:195]
s_waitcnt vmcnt(11)
v_mfma_f32_16x16x32_bf16 a[196:199],v[108:111],a[180:183],a[196:199]
s_waitcnt vmcnt(10)
v_mfma_f32_16x16x32_bf16 a[196:199],v[104:107],a[164:167],a[196:199]
s_waitcnt vmcnt(9)
v_mfma_f32_16x16x32_bf16 a[196:199],v[100:103],a[148:151],a[196:199]
s_waitcnt vmcnt(8)
v_mfma_f32_16x16x32_bf16 a[196:199],v[96:99],a[132:135],a[196:199]
s_waitcnt vmcnt(7)
v_mfma_f32_16x16x32_bf16 a[200:203],v[96:99],a[136:139],a[200:203]
s_waitcnt vmcnt(6)
v_mfma_f32_16x16x32_bf16 a[200:203],v[100:103],a[152:155],a[200:203]
s_waitcnt vmcnt(5)
v_mfma_f32_16x16x32_bf16 a[200:203],v[104:107],a[168:171],a[200:203]
s_waitcnt vmcnt(4)
v_mfma_f32_16x16x32_bf16 a[200:203],v[108:111],a[184:187],a[200:203]
s_waitcnt vmcnt(3)
v_accvgpr_read_b32 v16,a192
v_accvgpr_read_b32 v17,a193
v_mfma_f32_16x16x32_bf16 a[204:207],v[108:111],a[188:191],a[204:207]
s_waitcnt vmcnt(2)
v_accvgpr_read_b32 v18,a194
v_accvgpr_read_b32 v19,a195
v_mfma_f32_16x16x32_bf16 a[204:207],v[104:107],a[172:175],a[204:207]
s_waitcnt vmcnt(1)
v_cvt_pk_bf16_f32 v16,v16,v17
v_cvt_pk_bf16_f32 v17,v18,v19
v_mfma_f32_16x16x32_bf16 a[204:207],v[100:103],a[156:159],a[204:207]
s_waitcnt vmcnt(0)
v_accvgpr_read_b32 v8,a196
v_accvgpr_read_b32 v9,a197
v_mfma_f32_16x16x32_bf16 a[204:207],v[96:99],a[140:143],a[204:207]
s_waitcnt lgkmcnt(0)
v_accvgpr_read_b32 v10,a198
v_accvgpr_read_b32 v11,a199
v_mfma_f32_16x16x32_bf16 a[208:211],v[112:115],a[128:131],a[208:211]
v_cvt_pk_bf16_f32 v8,v8,v9
v_cvt_pk_bf16_f32 v9,v10,v11
v_mfma_f32_16x16x32_bf16 a[208:211],v[116:119],a[144:147],a[208:211]
ds_read_b64_tr_b16 v[96:97],v1 offset:64
ds_read_b64_tr_b16 v[98:99],v1 offset:5184
v_mfma_f32_16x16x32_bf16 a[208:211],v[120:123],a[160:163],a[208:211]
ds_read_b64_tr_b16 v[100:101],v1 offset:10304
v_mfma_f32_16x16x32_bf16 a[208:211],v[124:127],a[176:179],a[208:211]
ds_read_b64_tr_b16 v[102:103],v1 offset:15424
v_mfma_f32_16x16x32_bf16 a[212:215],v[124:127],a[180:183],a[212:215]
ds_read_b64_tr_b16 v[104:105],v1 offset:20544
v_mfma_f32_16x16x32_bf16 a[212:215],v[120:123],a[164:167],a[212:215]
ds_read_b64_tr_b16 v[106:107],v1 offset:25664
ds_read_b64_tr_b16 v[108:109],v1 offset:30784
v_mfma_f32_16x16x32_bf16 a[212:215],v[116:119],a[148:151],a[212:215]
ds_read_b64_tr_b16 v[110:111],v1 offset:35904
v_add_u32 v1,0xfffe2000,v1
v_mfma_f32_16x16x32_bf16 a[212:215],v[112:115],a[132:135],a[212:215]
v_accvgpr_read_b32 v20,a208
v_accvgpr_read_b32 v21,a209
v_mfma_f32_16x16x32_bf16 a[216:219],v[112:115],a[136:139],a[216:219]
v_accvgpr_read_b32 v22,a210
v_accvgpr_read_b32 v23,a211
v_mfma_f32_16x16x32_bf16 a[216:219],v[116:119],a[152:155],a[216:219]
v_cvt_pk_bf16_f32 v18,v20,v21
v_cvt_pk_bf16_f32 v19,v22,v23
v_mfma_f32_16x16x32_bf16 a[216:219],v[120:123],a[168:171],a[216:219]
buffer_store_dwordx4 v[16:19],v6,s[36:39],s74 offen sc1
v_mfma_f32_16x16x32_bf16 a[216:219],v[124:127],a[184:187],a[216:219]
v_accvgpr_read_b32 v12,a212
v_accvgpr_read_b32 v13,a213
v_mfma_f32_16x16x32_bf16 a[220:223],v[124:127],a[188:191],a[220:223]
v_accvgpr_read_b32 v14,a214
v_accvgpr_read_b32 v15,a215
v_mfma_f32_16x16x32_bf16 a[220:223],v[120:123],a[172:175],a[220:223]
v_cvt_pk_bf16_f32 v10,v12,v13
v_cvt_pk_bf16_f32 v11,v14,v15
v_mfma_f32_16x16x32_bf16 a[220:223],v[116:119],a[156:159],a[220:223]
v_accvgpr_read_b32 v16,a200
v_accvgpr_read_b32 v17,a201
v_mfma_f32_16x16x32_bf16 a[220:223],v[112:115],a[140:143],a[220:223]
v_accvgpr_read_b32 v18,a202
v_accvgpr_read_b32 v19,a203
v_mfma_f32_16x16x32_bf16 a[224:227],v[64:67],v[32:35],a[224:227]
v_accvgpr_read_b32 v20,a216
v_accvgpr_read_b32 v21,a217
v_mfma_f32_16x16x32_bf16 a[224:227],v[68:71],v[36:39],a[224:227]
v_accvgpr_read_b32 v22,a218
v_accvgpr_read_b32 v23,a219
v_mfma_f32_16x16x32_bf16 a[224:227],v[72:75],v[40:43],a[224:227]
v_cvt_pk_bf16_f32 v16,v16,v17
v_cvt_pk_bf16_f32 v17,v18,v19
v_mfma_f32_16x16x32_bf16 a[224:227],v[76:79],v[44:47],a[224:227]
v_cvt_pk_bf16_f32 v18,v20,v21
v_cvt_pk_bf16_f32 v19,v22,v23
v_mfma_f32_16x16x32_bf16 a[228:231],v[76:79],v[180:183],a[228:231]
ds_read_b64_tr_b16 v[112:113],v2 offset:96
ds_read_b64_tr_b16 v[114:115],v2 offset:5216
v_mfma_f32_16x16x32_bf16 a[228:231],v[72:75],v[164:167],a[228:231]
buffer_store_dwordx4 v[8:11],v6,s[36:39],s74 offen offset:1024 sc1
v_mfma_f32_16x16x32_bf16 a[228:231],v[68:71],v[148:151],a[228:231]
ds_read_b64_tr_b16 v[116:117],v2 offset:10336
ds_read_b64_tr_b16 v[118:119],v2 offset:15456
v_mfma_f32_16x16x32_bf16 a[228:231],v[64:67],v[132:135],a[228:231]
ds_read_b64_tr_b16 v[120:121],v2 offset:20576
v_accvgpr_read_b32 v8,a204
v_mfma_f32_16x16x32_bf16 a[232:235],v[64:67],v[196:199],a[232:235]
ds_read_b64_tr_b16 v[122:123],v2 offset:25696
v_accvgpr_read_b32 v9,a205
v_mfma_f32_16x16x32_bf16 a[232:235],v[68:71],v[212:215],a[232:235]
ds_read_b64_tr_b16 v[124:125],v2 offset:30816
v_accvgpr_read_b32 v10,a206
v_mfma_f32_16x16x32_bf16 a[232:235],v[72:75],v[228:231],a[232:235]
ds_read_b64_tr_b16 v[126:127],v2 offset:35936
v_accvgpr_read_b32 v11,a207
v_mfma_f32_16x16x32_bf16 a[232:235],v[76:79],v[244:247],a[232:235]
v_accvgpr_read_b32 v12,a220
v_accvgpr_read_b32 v13,a221
v_mfma_f32_16x16x32_bf16 a[236:239],v[76:79],v[188:191],a[236:239]
v_accvgpr_read_b32 v14,a222
v_accvgpr_read_b32 v15,a223
v_mfma_f32_16x16x32_bf16 a[236:239],v[72:75],v[172:175],a[236:239]
v_cvt_pk_bf16_f32 v8,v8,v9
v_cvt_pk_bf16_f32 v9,v10,v11
v_mfma_f32_16x16x32_bf16 a[236:239],v[68:71],v[156:159],a[236:239]
v_cvt_pk_bf16_f32 v10,v12,v13
v_cvt_pk_bf16_f32 v11,v14,v15
v_mfma_f32_16x16x32_bf16 a[236:239],v[64:67],v[140:143],a[236:239]
ds_read_b64_tr_b16 v[64:65],v1 offset:128
ds_read_b64_tr_b16 v[66:67],v1 offset:5248
v_mfma_f32_16x16x32_bf16 a[240:243],v[80:83],v[32:35],a[240:243]
buffer_store_dwordx4 v[16:19],v6,s[36:39],s74 offen offset:2048 sc1
v_mfma_f32_16x16x32_bf16 a[240:243],v[84:87],v[36:39],a[240:243]
ds_read_b64_tr_b16 v[68:69],v1 offset:10368
ds_read_b64_tr_b16 v[70:71],v1 offset:15488
v_mfma_f32_16x16x32_bf16 a[240:243],v[88:91],v[40:43],a[240:243]
ds_read_b64_tr_b16 v[72:73],v1 offset:20608
v_add_u32 v2,0xfffe2000,v2
v_mfma_f32_16x16x32_bf16 a[240:243],v[92:95],v[44:47],a[240:243]
ds_read_b64_tr_b16 v[74:75],v1 offset:25728
v_mfma_f32_16x16x32_bf16 a[244:247],v[92:95],v[180:183],a[244:247]
ds_read_b64_tr_b16 v[76:77],v1 offset:30848
v_mfma_f32_16x16x32_bf16 a[244:247],v[88:91],v[164:167],a[244:247]
ds_read_b64_tr_b16 v[78:79],v1 offset:35968
v_add_u32 v1,0x1e000,v1
v_mfma_f32_16x16x32_bf16 a[244:247],v[84:87],v[148:151],a[244:247]
v_mfma_f32_16x16x32_bf16 a[244:247],v[80:83],v[132:135],a[244:247]
v_mfma_f32_16x16x32_bf16 a[248:251],v[80:83],v[196:199],a[248:251]
v_mfma_f32_16x16x32_bf16 a[248:251],v[84:87],v[212:215],a[248:251]
v_mfma_f32_16x16x32_bf16 a[248:251],v[88:91],v[228:231],a[248:251]
v_mfma_f32_16x16x32_bf16 a[248:251],v[92:95],v[244:247],a[248:251]
buffer_store_dwordx4 v[8:11],v28,s[36:39],s74 offen offset:3072 sc1
v_mfma_f32_16x16x32_bf16 a[252:255],v[92:95],v[188:191],a[252:255]
ds_read_b64_tr_b16 v[92:93],v2 offset:30880
ds_read_b64_tr_b16 v[94:95],v2 offset:36000
v_mfma_f32_16x16x32_bf16 a[252:255],v[88:91],v[172:175],a[252:255]
ds_read_b64_tr_b16 v[88:89],v2 offset:20640
v_mfma_f32_16x16x32_bf16 a[252:255],v[84:87],v[156:159],a[252:255]
ds_read_b64_tr_b16 v[84:85],v2 offset:10400
v_mfma_f32_16x16x32_bf16 a[252:255],v[80:83],v[140:143],a[252:255]
s_waitcnt lgkmcnt(4)
ds_read_b64_tr_b16 v[80:81],v2 offset:160
v_mfma_f32_16x16x32_bf16 a[224:227],v[96:99],a[128:131],a[224:227]
ds_read_b64_tr_b16 v[82:83],v2 offset:5280
ds_read_b64_tr_b16 v[86:87],v2 offset:15520
v_mfma_f32_16x16x32_bf16 a[224:227],v[100:103],a[144:147],a[224:227]
ds_read_b64_tr_b16 v[90:91],v2 offset:25760
v_add_u32 v2,0x1e000,v2
v_mfma_f32_16x16x32_bf16 a[224:227],v[104:107],a[160:163],a[224:227]
v_mfma_f32_16x16x32_bf16 a[224:227],v[108:111],a[176:179],a[224:227]
v_mfma_f32_16x16x32_bf16 a[228:231],v[108:111],a[180:183],a[228:231]
v_mfma_f32_16x16x32_bf16 a[228:231],v[104:107],a[164:167],a[228:231]
v_mfma_f32_16x16x32_bf16 a[228:231],v[100:103],a[148:151],a[228:231]
v_mfma_f32_16x16x32_bf16 a[228:231],v[96:99],a[132:135],a[228:231]
v_mfma_f32_16x16x32_bf16 a[232:235],v[96:99],a[136:139],a[232:235]
v_mfma_f32_16x16x32_bf16 a[232:235],v[100:103],a[152:155],a[232:235]
v_mfma_f32_16x16x32_bf16 a[232:235],v[104:107],a[168:171],a[232:235]
v_mfma_f32_16x16x32_bf16 a[232:235],v[108:111],a[184:187],a[232:235]
v_accvgpr_read_b32 v16,a224
v_accvgpr_read_b32 v17,a225
v_mfma_f32_16x16x32_bf16 a[236:239],v[108:111],a[188:191],a[236:239]
v_accvgpr_read_b32 v18,a226
v_accvgpr_read_b32 v19,a227
v_mfma_f32_16x16x32_bf16 a[236:239],v[104:107],a[172:175],a[236:239]
v_cvt_pk_bf16_f32 v16,v16,v17
v_cvt_pk_bf16_f32 v17,v18,v19
v_mfma_f32_16x16x32_bf16 a[236:239],v[100:103],a[156:159],a[236:239]
v_accvgpr_read_b32 v8,a228
v_accvgpr_read_b32 v9,a229
v_mfma_f32_16x16x32_bf16 a[236:239],v[96:99],a[140:143],a[236:239]
v_accvgpr_read_b32 v10,a230
v_accvgpr_read_b32 v11,a231
v_mfma_f32_16x16x32_bf16 a[240:243],v[112:115],a[128:131],a[240:243]
v_cvt_pk_bf16_f32 v8,v8,v9
v_cvt_pk_bf16_f32 v9,v10,v11
v_mfma_f32_16x16x32_bf16 a[240:243],v[116:119],a[144:147],a[240:243]
ds_read_b64_tr_b16 v[96:97],v1 offset:128
ds_read_b64_tr_b16 v[98:99],v1 offset:5248
v_mfma_f32_16x16x32_bf16 a[240:243],v[120:123],a[160:163],a[240:243]
)A"
R"A(ds_read_b64_tr_b16 v[100:101],v1 offset:10368
v_mfma_f32_16x16x32_bf16 a[240:243],v[124:127],a[176:179],a[240:243]
ds_read_b64_tr_b16 v[102:103],v1 offset:15488
v_mfma_f32_16x16x32_bf16 a[244:247],v[124:127],a[180:183],a[244:247]
ds_read_b64_tr_b16 v[104:105],v1 offset:20608
v_mfma_f32_16x16x32_bf16 a[244:247],v[120:123],a[164:167],a[244:247]
ds_read_b64_tr_b16 v[106:107],v1 offset:25728
ds_read_b64_tr_b16 v[108:109],v1 offset:30848
v_mfma_f32_16x16x32_bf16 a[244:247],v[116:119],a[148:151],a[244:247]
ds_read_b64_tr_b16 v[110:111],v1 offset:35968
v_add_u32 v1,0xfffe2000,v1
v_mfma_f32_16x16x32_bf16 a[244:247],v[112:115],a[132:135],a[244:247]
v_accvgpr_read_b32 v20,a240
v_accvgpr_read_b32 v21,a241
v_mfma_f32_16x16x32_bf16 a[248:251],v[112:115],a[136:139],a[248:251]
v_accvgpr_read_b32 v22,a242
v_accvgpr_read_b32 v23,a243
v_mfma_f32_16x16x32_bf16 a[248:251],v[116:119],a[152:155],a[248:251]
v_cvt_pk_bf16_f32 v18,v20,v21
v_cvt_pk_bf16_f32 v19,v22,v23
v_mfma_f32_16x16x32_bf16 a[248:251],v[120:123],a[168:171],a[248:251]
buffer_store_dwordx4 v[16:19],v6,s[36:39],s75 offen sc1
v_mfma_f32_16x16x32_bf16 a[248:251],v[124:127],a[184:187],a[248:251]
v_accvgpr_read_b32 v12,a244
v_accvgpr_read_b32 v13,a245
v_mfma_f32_16x16x32_bf16 a[252:255],v[124:127],a[188:191],a[252:255]
v_accvgpr_read_b32 v14,a246
v_accvgpr_read_b32 v15,a247
v_mfma_f32_16x16x32_bf16 a[252:255],v[120:123],a[172:175],a[252:255]
v_cvt_pk_bf16_f32 v10,v12,v13
v_cvt_pk_bf16_f32 v11,v14,v15
v_mfma_f32_16x16x32_bf16 a[252:255],v[116:119],a[156:159],a[252:255]
v_accvgpr_read_b32 v16,a232
v_accvgpr_read_b32 v17,a233
v_mfma_f32_16x16x32_bf16 a[252:255],v[112:115],a[140:143],a[252:255]
v_accvgpr_read_b32 v18,a234
v_accvgpr_read_b32 v19,a235
v_mfma_f32_16x16x32_bf16 a[0:3],v[64:67],v[32:35],a[0:3]
v_accvgpr_read_b32 v20,a248
v_accvgpr_read_b32 v21,a249
v_mfma_f32_16x16x32_bf16 a[0:3],v[68:71],v[36:39],a[0:3]
v_accvgpr_read_b32 v22,a250
v_accvgpr_read_b32 v23,a251
v_mfma_f32_16x16x32_bf16 a[0:3],v[72:75],v[40:43],a[0:3]
v_cvt_pk_bf16_f32 v16,v16,v17
v_cvt_pk_bf16_f32 v17,v18,v19
v_mfma_f32_16x16x32_bf16 a[0:3],v[76:79],v[44:47],a[0:3]
v_cvt_pk_bf16_f32 v18,v20,v21
v_cvt_pk_bf16_f32 v19,v22,v23
v_mfma_f32_16x16x32_bf16 a[4:7],v[76:79],v[180:183],a[4:7]
ds_read_b64_tr_b16 v[112:113],v2 offset:160
ds_read_b64_tr_b16 v[114:115],v2 offset:5280
v_mfma_f32_16x16x32_bf16 a[4:7],v[72:75],v[164:167],a[4:7]
buffer_store_dwordx4 v[8:11],v6,s[36:39],s75 offen offset:1024 sc1
v_mfma_f32_16x16x32_bf16 a[4:7],v[68:71],v[148:151],a[4:7]
ds_read_b64_tr_b16 v[116:117],v2 offset:10400
ds_read_b64_tr_b16 v[118:119],v2 offset:15520
v_mfma_f32_16x16x32_bf16 a[4:7],v[64:67],v[132:135],a[4:7]
ds_read_b64_tr_b16 v[120:121],v2 offset:20640
v_accvgpr_read_b32 v8,a236
v_mfma_f32_16x16x32_bf16 a[8:11],v[64:67],v[196:199],a[8:11]
ds_read_b64_tr_b16 v[122:123],v2 offset:25760
v_accvgpr_read_b32 v9,a237
v_mfma_f32_16x16x32_bf16 a[8:11],v[68:71],v[212:215],a[8:11]
ds_read_b64_tr_b16 v[124:125],v2 offset:30880
v_accvgpr_read_b32 v10,a238
v_mfma_f32_16x16x32_bf16 a[8:11],v[72:75],v[228:231],a[8:11]
ds_read_b64_tr_b16 v[126:127],v2 offset:36000
v_accvgpr_read_b32 v11,a239
v_mfma_f32_16x16x32_bf16 a[8:11],v[76:79],v[244:247],a[8:11]
v_accvgpr_read_b32 v12,a252
v_accvgpr_read_b32 v13,a253
v_mfma_f32_16x16x32_bf16 a[12:15],v[76:79],v[188:191],a[12:15]
v_accvgpr_read_b32 v14,a254
v_accvgpr_read_b32 v15,a255
v_mfma_f32_16x16x32_bf16 a[12:15],v[72:75],v[172:175],a[12:15]
v_cvt_pk_bf16_f32 v8,v8,v9
v_cvt_pk_bf16_f32 v9,v10,v11
v_mfma_f32_16x16x32_bf16 a[12:15],v[68:71],v[156:159],a[12:15]
v_cvt_pk_bf16_f32 v10,v12,v13
v_cvt_pk_bf16_f32 v11,v14,v15
v_mfma_f32_16x16x32_bf16 a[12:15],v[64:67],v[140:143],a[12:15]
s_waitcnt lgkmcnt(3)
ds_read_b64_tr_b16 v[64:65],v1 offset:192
ds_read_b64_tr_b16 v[66:67],v1 offset:5312
v_mfma_f32_16x16x32_bf16 a[16:19],v[80:83],v[32:35],a[16:19]
buffer_store_dwordx4 v[16:19],v6,s[36:39],s75 offen offset:2048 sc1
v_mfma_f32_16x16x32_bf16 a[16:19],v[84:87],v[36:39],a[16:19]
ds_read_b64_tr_b16 v[68:69],v1 offset:10432
ds_read_b64_tr_b16 v[70:71],v1 offset:15552
v_mfma_f32_16x16x32_bf16 a[16:19],v[88:91],v[40:43],a[16:19]
ds_read_b64_tr_b16 v[72:73],v1 offset:20672
v_add_u32 v2,0xfffe2000,v2
v_mfma_f32_16x16x32_bf16 a[16:19],v[92:95],v[44:47],a[16:19]
ds_read_b64_tr_b16 v[74:75],v1 offset:25792
v_mfma_f32_16x16x32_bf16 a[20:23],v[92:95],v[180:183],a[20:23]
ds_read_b64_tr_b16 v[76:77],v1 offset:30912
v_mfma_f32_16x16x32_bf16 a[20:23],v[88:91],v[164:167],a[20:23]
ds_read_b64_tr_b16 v[78:79],v1 offset:36032
v_add_u32 v1,0x1e000,v1
v_mfma_f32_16x16x32_bf16 a[20:23],v[84:87],v[148:151],a[20:23]
v_mfma_f32_16x16x32_bf16 a[20:23],v[80:83],v[132:135],a[20:23]
v_mfma_f32_16x16x32_bf16 a[24:27],v[80:83],v[196:199],a[24:27]
v_mfma_f32_16x16x32_bf16 a[24:27],v[84:87],v[212:215],a[24:27]
v_mfma_f32_16x16x32_bf16 a[24:27],v[88:91],v[228:231],a[24:27]
v_mfma_f32_16x16x32_bf16 a[24:27],v[92:95],v[244:247],a[24:27]
buffer_store_dwordx4 v[8:11],v28,s[36:39],s75 offen offset:3072 sc1
v_mfma_f32_16x16x32_bf16 a[28:31],v[92:95],v[188:191],a[28:31]
ds_read_b64_tr_b16 v[92:93],v2 offset:30944
ds_read_b64_tr_b16 v[94:95],v2 offset:36064
v_mfma_f32_16x16x32_bf16 a[28:31],v[88:91],v[172:175],a[28:31]
ds_read_b64_tr_b16 v[88:89],v2 offset:20704
v_mfma_f32_16x16x32_bf16 a[28:31],v[84:87],v[156:159],a[28:31]
ds_read_b64_tr_b16 v[84:85],v2 offset:10464
v_mfma_f32_16x16x32_bf16 a[28:31],v[80:83],v[140:143],a[28:31]
ds_read_b64_tr_b16 v[80:81],v2 offset:224
v_mfma_f32_16x16x32_bf16 a[0:3],v[96:99],a[128:131],a[0:3]
ds_read_b64_tr_b16 v[82:83],v2 offset:5344
ds_read_b64_tr_b16 v[86:87],v2 offset:15584
v_mfma_f32_16x16x32_bf16 a[0:3],v[100:103],a[144:147],a[0:3]
ds_read_b64_tr_b16 v[90:91],v2 offset:25824
v_add_u32 v2,0x1e000,v2
v_mfma_f32_16x16x32_bf16 a[0:3],v[104:107],a[160:163],a[0:3]
v_mfma_f32_16x16x32_bf16 a[0:3],v[108:111],a[176:179],a[0:3]
v_mfma_f32_16x16x32_bf16 a[4:7],v[108:111],a[180:183],a[4:7]
v_mfma_f32_16x16x32_bf16 a[4:7],v[104:107],a[164:167],a[4:7]
v_mfma_f32_16x16x32_bf16 a[4:7],v[100:103],a[148:151],a[4:7]
v_mfma_f32_16x16x32_bf16 a[4:7],v[96:99],a[132:135],a[4:7]
v_mfma_f32_16x16x32_bf16 a[8:11],v[96:99],a[136:139],a[8:11]
v_mfma_f32_16x16x32_bf16 a[8:11],v[100:103],a[152:155],a[8:11]
v_mfma_f32_16x16x32_bf16 a[8:11],v[104:107],a[168:171],a[8:11]
v_mfma_f32_16x16x32_bf16 a[8:11],v[108:111],a[184:187],a[8:11]
v_accvgpr_read_b32 v16,a0
v_accvgpr_read_b32 v17,a1
v_mfma_f32_16x16x32_bf16 a[12:15],v[108:111],a[188:191],a[12:15]
v_accvgpr_read_b32 v18,a2
v_accvgpr_read_b32 v19,a3
v_mfma_f32_16x16x32_bf16 a[12:15],v[104:107],a[172:175],a[12:15]
v_cvt_pk_bf16_f32 v16,v16,v17
v_cvt_pk_bf16_f32 v17,v18,v19
v_mfma_f32_16x16x32_bf16 a[12:15],v[100:103],a[156:159],a[12:15]
v_accvgpr_read_b32 v8,a4
v_accvgpr_read_b32 v9,a5
v_mfma_f32_16x16x32_bf16 a[12:15],v[96:99],a[140:143],a[12:15]
v_accvgpr_read_b32 v10,a6
v_accvgpr_read_b32 v11,a7
v_mfma_f32_16x16x32_bf16 a[16:19],v[112:115],a[128:131],a[16:19]
v_cvt_pk_bf16_f32 v8,v8,v9
v_cvt_pk_bf16_f32 v9,v10,v11
v_mfma_f32_16x16x32_bf16 a[16:19],v[116:119],a[144:147],a[16:19]
s_waitcnt lgkmcnt(0)
ds_read_b64_tr_b16 v[96:97],v1 offset:192
ds_read_b64_tr_b16 v[98:99],v1 offset:5312
v_mfma_f32_16x16x32_bf16 a[16:19],v[120:123],a[160:163],a[16:19]
ds_read_b64_tr_b16 v[100:101],v1 offset:10432
v_mfma_f32_16x16x32_bf16 a[16:19],v[124:127],a[176:179],a[16:19]
ds_read_b64_tr_b16 v[102:103],v1 offset:15552
v_mfma_f32_16x16x32_bf16 a[20:23],v[124:127],a[180:183],a[20:23]
ds_read_b64_tr_b16 v[104:105],v1 offset:20672
v_mfma_f32_16x16x32_bf16 a[20:23],v[120:123],a[164:167],a[20:23]
ds_read_b64_tr_b16 v[106:107],v1 offset:25792
ds_read_b64_tr_b16 v[108:109],v1 offset:30912
v_mfma_f32_16x16x32_bf16 a[20:23],v[116:119],a[148:151],a[20:23]
ds_read_b64_tr_b16 v[110:111],v1 offset:36032
v_add_u32 v1,0xfffec000,v1
v_mfma_f32_16x16x32_bf16 a[20:23],v[112:115],a[132:135],a[20:23]
v_accvgpr_read_b32 v20,a16
v_accvgpr_read_b32 v21,a17
v_mfma_f32_16x16x32_bf16 a[24:27],v[112:115],a[136:139],a[24:27]
v_accvgpr_read_b32 v22,a18
v_accvgpr_read_b32 v23,a19
v_mfma_f32_16x16x32_bf16 a[24:27],v[116:119],a[152:155],a[24:27]
v_cvt_pk_bf16_f32 v18,v20,v21
v_cvt_pk_bf16_f32 v19,v22,v23
v_mfma_f32_16x16x32_bf16 a[24:27],v[120:123],a[168:171],a[24:27]
s_nop 0
L_pd18_%=:
s_cbranch_execz L_trtg2F_%=
buffer_store_dwordx4 v[16:19],v6,s[36:39],s76 offen sc1
v_mfma_f32_16x16x32_bf16 a[24:27],v[124:127],a[184:187],a[24:27]
v_accvgpr_read_b32 v12,a20
v_accvgpr_read_b32 v13,a21
v_mfma_f32_16x16x32_bf16 a[28:31],v[124:127],a[188:191],a[28:31]
v_accvgpr_read_b32 v14,a22
v_accvgpr_read_b32 v15,a23
v_mfma_f32_16x16x32_bf16 a[28:31],v[120:123],a[172:175],a[28:31]
v_cvt_pk_bf16_f32 v10,v12,v13
v_cvt_pk_bf16_f32 v11,v14,v15
v_mfma_f32_16x16x32_bf16 a[28:31],v[116:119],a[156:159],a[28:31]
v_accvgpr_read_b32 v16,a8
v_accvgpr_read_b32 v17,a9
v_mfma_f32_16x16x32_bf16 a[28:31],v[112:115],a[140:143],a[28:31]
v_accvgpr_read_b32 v18,a10
v_accvgpr_read_b32 v19,a11
v_mfma_f32_16x16x32_bf16 v[128:131],v[64:67],v[32:35],v[128:131]
v_accvgpr_read_b32 v20,a24
v_accvgpr_read_b32 v21,a25
v_mfma_f32_16x16x32_bf16 v[128:131],v[68:71],v[36:39],v[128:131]
v_accvgpr_read_b32 v22,a26
v_accvgpr_read_b32 v23,a27
v_mfma_f32_16x16x32_bf16 v[128:131],v[72:75],v[40:43],v[128:131]
v_cvt_pk_bf16_f32 v16,v16,v17
v_cvt_pk_bf16_f32 v17,v18,v19
v_mfma_f32_16x16x32_bf16 v[128:131],v[76:79],v[44:47],v[128:131]
v_cvt_pk_bf16_f32 v18,v20,v21
v_cvt_pk_bf16_f32 v19,v22,v23
v_mfma_f32_16x16x32_bf16 v[136:139],v[76:79],v[180:183],v[136:139]
ds_read_b64_tr_b16 v[112:113],v2 offset:224
ds_read_b64_tr_b16 v[114:115],v2 offset:5344
v_mfma_f32_16x16x32_bf16 v[136:139],v[72:75],v[164:167],v[136:139]
buffer_store_dwordx4 v[8:11],v6,s[36:39],s76 offen offset:1024 sc1
v_mfma_f32_16x16x32_bf16 v[136:139],v[68:71],v[148:151],v[136:139]
ds_read_b64_tr_b16 v[116:117],v2 offset:10464
ds_read_b64_tr_b16 v[118:119],v2 offset:15584
v_mfma_f32_16x16x32_bf16 v[136:139],v[64:67],v[132:135],v[136:139]
ds_read_b64_tr_b16 v[120:121],v2 offset:20704
v_accvgpr_read_b32 v8,a12
v_mfma_f32_16x16x32_bf16 v[144:147],v[64:67],v[196:199],v[144:147]
ds_read_b64_tr_b16 v[122:123],v2 offset:25824
v_accvgpr_read_b32 v9,a13
v_mfma_f32_16x16x32_bf16 v[144:147],v[68:71],v[212:215],v[144:147]
ds_read_b64_tr_b16 v[124:125],v2 offset:30944
v_accvgpr_read_b32 v10,a14
v_mfma_f32_16x16x32_bf16 v[144:147],v[72:75],v[228:231],v[144:147]
ds_read_b64_tr_b16 v[126:127],v2 offset:36064
v_accvgpr_read_b32 v11,a15
v_mfma_f32_16x16x32_bf16 v[144:147],v[76:79],v[244:247],v[144:147]
v_accvgpr_read_b32 v12,a28
v_accvgpr_read_b32 v13,a29
v_mfma_f32_16x16x32_bf16 v[152:155],v[76:79],v[188:191],v[152:155]
v_accvgpr_read_b32 v14,a30
v_accvgpr_read_b32 v15,a31
v_mfma_f32_16x16x32_bf16 v[152:155],v[72:75],v[172:175],v[152:155]
v_cvt_pk_bf16_f32 v8,v8,v9
v_cvt_pk_bf16_f32 v9,v10,v11
v_mfma_f32_16x16x32_bf16 v[152:155],v[68:71],v[156:159],v[152:155]
v_cvt_pk_bf16_f32 v10,v12,v13
v_cvt_pk_bf16_f32 v11,v14,v15
v_mfma_f32_16x16x32_bf16 v[152:155],v[64:67],v[140:143],v[152:155]
ds_read_b64_tr_b16 v[64:65],v1 offset:256
ds_read_b64_tr_b16 v[66:67],v1 offset:5376
v_mfma_f32_16x16x32_bf16 v[160:163],v[80:83],v[32:35],v[160:163]
buffer_store_dwordx4 v[16:19],v6,s[36:39],s76 offen offset:2048 sc1
v_mfma_f32_16x16x32_bf16 v[160:163],v[84:87],v[36:39],v[160:163]
ds_read_b64_tr_b16 v[68:69],v1 offset:10496
ds_read_b64_tr_b16 v[70:71],v1 offset:15616
v_mfma_f32_16x16x32_bf16 v[160:163],v[88:91],v[40:43],v[160:163]
ds_read_b64_tr_b16 v[72:73],v1 offset:20736
v_add_u32 v2,0xfffec000,v2
v_mfma_f32_16x16x32_bf16 v[160:163],v[92:95],v[44:47],v[160:163]
ds_read_b64_tr_b16 v[74:75],v1 offset:25856
v_mov_b32 v20,s52
v_mfma_f32_16x16x32_bf16 v[168:171],v[92:95],v[180:183],v[168:171]
ds_read_b64_tr_b16 v[76:77],v1 offset:30976
v_mov_b32 v21,s53
v_mfma_f32_16x16x32_bf16 v[168:171],v[88:91],v[164:167],v[168:171]
ds_read_b64_tr_b16 v[78:79],v1 offset:36096
v_add_u32 v1,0xffff6000,v1
v_mfma_f32_16x16x32_bf16 v[168:171],v[84:87],v[148:151],v[168:171]
v_lshlrev_b32 v22,20,v6
v_mfma_f32_16x16x32_bf16 v[168:171],v[80:83],v[132:135],v[168:171]
)A"
R"A(v_mfma_f32_16x16x32_bf16 v[176:179],v[80:83],v[196:199],v[176:179]
v_mfma_f32_16x16x32_bf16 v[176:179],v[84:87],v[212:215],v[176:179]
v_mfma_f32_16x16x32_bf16 v[176:179],v[88:91],v[228:231],v[176:179]
v_mfma_f32_16x16x32_bf16 v[176:179],v[92:95],v[244:247],v[176:179]
buffer_store_dwordx4 v[8:11],v28,s[36:39],s76 offen offset:3072 sc1
v_mfma_f32_16x16x32_bf16 v[184:187],v[92:95],v[188:191],v[184:187]
ds_read_b64_tr_b16 v[92:93],v2 offset:31008
ds_read_b64_tr_b16 v[94:95],v2 offset:36128
v_mfma_f32_16x16x32_bf16 v[184:187],v[88:91],v[172:175],v[184:187]
ds_read_b64_tr_b16 v[88:89],v2 offset:20768
v_mfma_f32_16x16x32_bf16 v[184:187],v[84:87],v[156:159],v[184:187]
ds_read_b64_tr_b16 v[84:85],v2 offset:10528
v_mfma_f32_16x16x32_bf16 v[184:187],v[80:83],v[140:143],v[184:187]
s_waitcnt lgkmcnt(4)
ds_read_b64_tr_b16 v[80:81],v2 offset:288
v_mfma_f32_16x16x32_bf16 v[128:131],v[96:99],a[128:131],v[128:131]
ds_read_b64_tr_b16 v[82:83],v2 offset:5408
ds_read_b64_tr_b16 v[86:87],v2 offset:15648
v_mfma_f32_16x16x32_bf16 v[128:131],v[100:103],a[144:147],v[128:131]
ds_read_b64_tr_b16 v[90:91],v2 offset:25888
v_add_u32 v2,0xffff6000,v2
v_mfma_f32_16x16x32_bf16 v[128:131],v[104:107],a[160:163],v[128:131]
v_mfma_f32_16x16x32_bf16 v[128:131],v[108:111],a[176:179],v[128:131]
v_mfma_f32_16x16x32_bf16 v[136:139],v[108:111],a[180:183],v[136:139]
v_mfma_f32_16x16x32_bf16 v[136:139],v[104:107],a[164:167],v[136:139]
v_mfma_f32_16x16x32_bf16 v[136:139],v[100:103],a[148:151],v[136:139]
v_mfma_f32_16x16x32_bf16 v[136:139],v[96:99],a[132:135],v[136:139]
v_mfma_f32_16x16x32_bf16 v[144:147],v[96:99],a[136:139],v[144:147]
v_mfma_f32_16x16x32_bf16 v[144:147],v[100:103],a[152:155],v[144:147]
v_mfma_f32_16x16x32_bf16 v[144:147],v[104:107],a[168:171],v[144:147]
v_mfma_f32_16x16x32_bf16 v[144:147],v[108:111],a[184:187],v[144:147]
v_cvt_pk_bf16_f32 v128,v128,v129
v_cvt_pk_bf16_f32 v129,v130,v131
v_mfma_f32_16x16x32_bf16 v[152:155],v[108:111],a[188:191],v[152:155]
ds_read_b64_tr_b16 v[108:109],v1 offset:30976
ds_read_b64_tr_b16 v[110:111],v1 offset:36096
v_mfma_f32_16x16x32_bf16 v[152:155],v[104:107],a[172:175],v[152:155]
v_cvt_pk_bf16_f32 v136,v136,v137
v_cvt_pk_bf16_f32 v137,v138,v139
v_mfma_f32_16x16x32_bf16 v[152:155],v[100:103],a[156:159],v[152:155]
v_cvt_pk_bf16_f32 v144,v144,v145
v_cvt_pk_bf16_f32 v145,v146,v147
v_mfma_f32_16x16x32_bf16 v[152:155],v[96:99],a[140:143],v[152:155]
ds_read_b64_tr_b16 v[96:97],v1 offset:256
ds_read_b64_tr_b16 v[98:99],v1 offset:5376
v_mfma_f32_16x16x32_bf16 v[160:163],v[112:115],a[128:131],v[160:163]
ds_read_b64_tr_b16 v[100:101],v1 offset:10496
v_mfma_f32_16x16x32_bf16 v[160:163],v[116:119],a[144:147],v[160:163]
ds_read_b64_tr_b16 v[102:103],v1 offset:15616
v_mfma_f32_16x16x32_bf16 v[160:163],v[120:123],a[160:163],v[160:163]
ds_read_b64_tr_b16 v[104:105],v1 offset:20736
v_mfma_f32_16x16x32_bf16 v[160:163],v[124:127],a[176:179],v[160:163]
v_cvt_pk_bf16_f32 v152,v152,v153
v_cvt_pk_bf16_f32 v153,v154,v155
v_mfma_f32_16x16x32_bf16 v[168:171],v[124:127],a[180:183],v[168:171]
ds_read_b64_tr_b16 v[106:107],v1 offset:25856
v_add_u32 v1,0x1e000,v1
v_mfma_f32_16x16x32_bf16 v[168:171],v[120:123],a[164:167],v[168:171]
v_mfma_f32_16x16x32_bf16 v[168:171],v[116:119],a[148:151],v[168:171]
v_mfma_f32_16x16x32_bf16 v[168:171],v[112:115],a[132:135],v[168:171]
v_cvt_pk_bf16_f32 v130,v160,v161
v_cvt_pk_bf16_f32 v131,v162,v163
v_mfma_f32_16x16x32_bf16 v[176:179],v[112:115],a[136:139],v[176:179]
buffer_store_dwordx4 v[128:131],v6,s[36:39],s79 offen sc1
v_mfma_f32_16x16x32_bf16 v[176:179],v[116:119],a[152:155],v[176:179]
v_mfma_f32_16x16x32_bf16 v[176:179],v[120:123],a[168:171],v[176:179]
v_mfma_f32_16x16x32_bf16 v[176:179],v[124:127],a[184:187],v[176:179]
v_mfma_f32_16x16x32_bf16 v[184:187],v[124:127],a[188:191],v[184:187]
v_cvt_pk_bf16_f32 v138,v168,v169
v_cvt_pk_bf16_f32 v139,v170,v171
v_mfma_f32_16x16x32_bf16 v[184:187],v[120:123],a[172:175],v[184:187]
buffer_store_dwordx4 v[136:139],v6,s[36:39],s80 offen sc1
v_mfma_f32_16x16x32_bf16 v[184:187],v[116:119],a[156:159],v[184:187]
ds_read_b64_tr_b16 v[116:117],v2 offset:10528
ds_read_b64_tr_b16 v[118:119],v2 offset:15648
v_mfma_f32_16x16x32_bf16 v[184:187],v[112:115],a[140:143],v[184:187]
v_cvt_pk_bf16_f32 v146,v176,v177
v_cvt_pk_bf16_f32 v147,v178,v179
s_waitcnt vmcnt(6)
s_barrier
v_mfma_f32_16x16x32_bf16 v[192:195],v[64:67],a[68:71],v[192:195]
buffer_store_dwordx4 v[144:147],v6,s[36:39],s81 offen sc1
v_mfma_f32_16x16x32_bf16 v[192:195],v[68:71],a[76:79],v[192:195]
ds_read_b64_tr_b16 v[112:113],v2 offset:288
ds_read_b64_tr_b16 v[114:115],v2 offset:5408
v_mfma_f32_16x16x32_bf16 v[192:195],v[72:75],a[84:87],v[192:195]
buffer_store_dwordx2 v[58:59],v60,s[44:47],s18 offen offset:2312 sc1
v_mfma_f32_16x16x32_bf16 v[192:195],v[76:79],a[92:95],v[192:195]
v_cvt_pk_bf16_f32 v154,v184,v185
v_cvt_pk_bf16_f32 v155,v186,v187
v_mfma_f32_16x16x32_bf16 v[200:203],v[76:79],a[124:127],v[200:203]
buffer_store_dwordx4 v[152:155],v28,s[36:39],s82 offen sc1
v_mfma_f32_16x16x32_bf16 v[200:203],v[72:75],a[116:119],v[200:203]
ds_read_b64_tr_b16 v[120:121],v2 offset:20768
ds_read_b64_tr_b16 v[122:123],v2 offset:25888
v_mfma_f32_16x16x32_bf16 v[200:203],v[68:71],a[108:111],v[200:203]
ds_read_b64_tr_b16 v[124:125],v2 offset:31008
v_mfma_f32_16x16x32_bf16 v[200:203],v[64:67],a[100:103],v[200:203]
ds_read_b64_tr_b16 v[126:127],v2 offset:36128
v_add_u32 v2,0x1e000,v2
v_mfma_f32_16x16x32_bf16 v[208:211],v[64:67],a[64:67],v[208:211]
v_mfma_f32_16x16x32_bf16 v[208:211],v[68:71],a[72:75],v[208:211]
v_mfma_f32_16x16x32_bf16 v[208:211],v[72:75],a[80:83],v[208:211]
v_mfma_f32_16x16x32_bf16 v[208:211],v[76:79],a[88:91],v[208:211]
v_mfma_f32_16x16x32_bf16 v[216:219],v[76:79],a[120:123],v[216:219]
ds_read_b64_tr_b16 v[76:77],v1 offset:30976
ds_read_b64_tr_b16 v[78:79],v1 offset:36096
v_mfma_f32_16x16x32_bf16 v[216:219],v[72:75],a[112:115],v[216:219]
ds_read_b64_tr_b16 v[72:73],v1 offset:20736
v_mfma_f32_16x16x32_bf16 v[216:219],v[68:71],a[104:107],v[216:219]
ds_read_b64_tr_b16 v[68:69],v1 offset:10496
v_mfma_f32_16x16x32_bf16 v[216:219],v[64:67],a[96:99],v[216:219]
s_waitcnt lgkmcnt(4)
ds_read_b64_tr_b16 v[64:65],v1 offset:256
v_mfma_f32_16x16x32_bf16 v[224:227],v[80:83],a[68:71],v[224:227]
ds_read_b64_tr_b16 v[66:67],v1 offset:5376
ds_read_b64_tr_b16 v[70:71],v1 offset:15616
v_mfma_f32_16x16x32_bf16 v[224:227],v[84:87],a[76:79],v[224:227]
ds_read_b64_tr_b16 v[74:75],v1 offset:25856
v_mfma_f32_16x16x32_bf16 v[224:227],v[88:91],a[84:87],v[224:227]
v_mfma_f32_16x16x32_bf16 v[224:227],v[92:95],a[92:95],v[224:227]
v_mfma_f32_16x16x32_bf16 v[232:235],v[92:95],a[124:127],v[232:235]
v_mfma_f32_16x16x32_bf16 v[232:235],v[88:91],a[116:119],v[232:235]
v_mfma_f32_16x16x32_bf16 v[232:235],v[84:87],a[108:111],v[232:235]
v_mfma_f32_16x16x32_bf16 v[232:235],v[80:83],a[100:103],v[232:235]
v_mfma_f32_16x16x32_bf16 v[240:243],v[80:83],a[64:67],v[240:243]
v_mfma_f32_16x16x32_bf16 v[240:243],v[84:87],a[72:75],v[240:243]
v_mfma_f32_16x16x32_bf16 v[240:243],v[88:91],a[80:83],v[240:243]
v_mfma_f32_16x16x32_bf16 v[240:243],v[92:95],a[88:91],v[240:243]
v_mfma_f32_16x16x32_bf16 v[248:251],v[92:95],a[120:123],v[248:251]
ds_read_b64_tr_b16 v[92:93],v2 offset:31008
ds_read_b64_tr_b16 v[94:95],v2 offset:36128
v_mfma_f32_16x16x32_bf16 v[248:251],v[88:91],a[112:115],v[248:251]
ds_read_b64_tr_b16 v[88:89],v2 offset:20768
v_mfma_f32_16x16x32_bf16 v[248:251],v[84:87],a[104:107],v[248:251]
ds_read_b64_tr_b16 v[84:85],v2 offset:10528
v_mfma_f32_16x16x32_bf16 v[248:251],v[80:83],a[96:99],v[248:251]
ds_read_b64_tr_b16 v[80:81],v2 offset:288
v_mfma_f32_16x16x32_bf16 v[192:195],v[96:99],v[32:35],v[192:195]
ds_read_b64_tr_b16 v[82:83],v2 offset:5408
ds_read_b64_tr_b16 v[86:87],v2 offset:15648
v_mfma_f32_16x16x32_bf16 v[192:195],v[100:103],v[36:39],v[192:195]
ds_read_b64_tr_b16 v[90:91],v2 offset:25888
v_mfma_f32_16x16x32_bf16 v[192:195],v[104:107],v[40:43],v[192:195]
v_mfma_f32_16x16x32_bf16 v[192:195],v[108:111],v[44:47],v[192:195]
v_mfma_f32_16x16x32_bf16 v[200:203],v[108:111],v[180:183],v[200:203]
v_mfma_f32_16x16x32_bf16 v[200:203],v[104:107],v[164:167],v[200:203]
v_mfma_f32_16x16x32_bf16 v[200:203],v[100:103],v[148:151],v[200:203]
v_mfma_f32_16x16x32_bf16 v[200:203],v[96:99],v[132:135],v[200:203]
v_mfma_f32_16x16x32_bf16 v[208:211],v[96:99],v[196:199],v[208:211]
v_mfma_f32_16x16x32_bf16 v[208:211],v[100:103],v[212:215],v[208:211]
v_mfma_f32_16x16x32_bf16 v[208:211],v[104:107],v[228:231],v[208:211]
v_mfma_f32_16x16x32_bf16 v[208:211],v[108:111],v[244:247],v[208:211]
v_mfma_f32_16x16x32_bf16 v[216:219],v[108:111],v[188:191],v[216:219]
v_mfma_f32_16x16x32_bf16 v[216:219],v[104:107],v[172:175],v[216:219]
v_mfma_f32_16x16x32_bf16 v[216:219],v[100:103],v[156:159],v[216:219]
v_mfma_f32_16x16x32_bf16 v[216:219],v[96:99],v[140:143],v[216:219]
v_mfma_f32_16x16x32_bf16 v[224:227],v[112:115],v[32:35],v[224:227]
v_mfma_f32_16x16x32_bf16 v[224:227],v[116:119],v[36:39],v[224:227]
v_mfma_f32_16x16x32_bf16 v[224:227],v[120:123],v[40:43],v[224:227]
v_mfma_f32_16x16x32_bf16 v[224:227],v[124:127],v[44:47],v[224:227]
v_mfma_f32_16x16x32_bf16 v[232:235],v[124:127],v[180:183],v[232:235]
v_mfma_f32_16x16x32_bf16 v[232:235],v[120:123],v[164:167],v[232:235]
v_mfma_f32_16x16x32_bf16 v[232:235],v[116:119],v[148:151],v[232:235]
v_mfma_f32_16x16x32_bf16 v[232:235],v[112:115],v[132:135],v[232:235]
v_mfma_f32_16x16x32_bf16 v[240:243],v[112:115],v[196:199],v[240:243]
v_mfma_f32_16x16x32_bf16 v[240:243],v[116:119],v[212:215],v[240:243]
v_mfma_f32_16x16x32_bf16 v[240:243],v[120:123],v[228:231],v[240:243]
v_mfma_f32_16x16x32_bf16 v[240:243],v[124:127],v[244:247],v[240:243]
v_mfma_f32_16x16x32_bf16 v[248:251],v[124:127],v[188:191],v[248:251]
v_mfma_f32_16x16x32_bf16 v[248:251],v[120:123],v[172:175],v[248:251]
v_mfma_f32_16x16x32_bf16 v[248:251],v[116:119],v[156:159],v[248:251]
v_mfma_f32_16x16x32_bf16 v[248:251],v[112:115],v[140:143],v[248:251]
s_waitcnt lgkmcnt(0)
v_mfma_f32_16x16x32_bf16 v[192:195],v[64:67],a[128:131],v[192:195]
v_mfma_f32_16x16x32_bf16 v[192:195],v[68:71],a[144:147],v[192:195]
v_mfma_f32_16x16x32_bf16 v[192:195],v[72:75],a[160:163],v[192:195]
v_mfma_f32_16x16x32_bf16 v[192:195],v[76:79],a[176:179],v[192:195]
v_mfma_f32_16x16x32_bf16 v[200:203],v[76:79],a[180:183],v[200:203]
v_mfma_f32_16x16x32_bf16 v[200:203],v[72:75],a[164:167],v[200:203]
v_mfma_f32_16x16x32_bf16 v[200:203],v[68:71],a[148:151],v[200:203]
v_mfma_f32_16x16x32_bf16 v[200:203],v[64:67],a[132:135],v[200:203]
v_mfma_f32_16x16x32_bf16 v[208:211],v[64:67],a[136:139],v[208:211]
v_mfma_f32_16x16x32_bf16 v[208:211],v[68:71],a[152:155],v[208:211]
v_mfma_f32_16x16x32_bf16 v[208:211],v[72:75],a[168:171],v[208:211]
v_mfma_f32_16x16x32_bf16 v[208:211],v[76:79],a[184:187],v[208:211]
v_mfma_f32_16x16x32_bf16 v[216:219],v[76:79],a[188:191],v[216:219]
v_mfma_f32_16x16x32_bf16 v[216:219],v[72:75],a[172:175],v[216:219]
v_mfma_f32_16x16x32_bf16 v[216:219],v[68:71],a[156:159],v[216:219]
v_mfma_f32_16x16x32_bf16 v[216:219],v[64:67],a[140:143],v[216:219]
v_mfma_f32_16x16x32_bf16 v[224:227],v[80:83],a[128:131],v[224:227]
buffer_load_dwordx2 v[62:63],v61,s[44:47],s91 offen offset:2312 sc1
v_mfma_f32_16x16x32_bf16 v[224:227],v[84:87],a[144:147],v[224:227]
v_mfma_f32_16x16x32_bf16 v[224:227],v[88:91],a[160:163],v[224:227]
v_mfma_f32_16x16x32_bf16 v[224:227],v[92:95],a[176:179],v[224:227]
v_mfma_f32_16x16x32_bf16 v[232:235],v[92:95],a[180:183],v[232:235]
v_mfma_f32_16x16x32_bf16 v[232:235],v[88:91],a[164:167],v[232:235]
v_mfma_f32_16x16x32_bf16 v[232:235],v[84:87],a[148:151],v[232:235]
v_mfma_f32_16x16x32_bf16 v[232:235],v[80:83],a[132:135],v[232:235]
v_mfma_f32_16x16x32_bf16 v[240:243],v[80:83],a[136:139],v[240:243]
v_mfma_f32_16x16x32_bf16 v[240:243],v[84:87],a[152:155],v[240:243]
v_mfma_f32_16x16x32_bf16 v[240:243],v[88:91],a[168:171],v[240:243]
v_mfma_f32_16x16x32_bf16 v[240:243],v[92:95],a[184:187],v[240:243]
v_mfma_f32_16x16x32_bf16 v[248:251],v[92:95],a[188:191],v[248:251]
v_mfma_f32_16x16x32_bf16 v[248:251],v[88:91],a[172:175],v[248:251]
v_mfma_f32_16x16x32_bf16 v[248:251],v[84:87],a[156:159],v[248:251]
)A"
R"A(v_mfma_f32_16x16x32_bf16 v[248:251],v[80:83],a[140:143],v[248:251]
s_waitcnt vmcnt(1)
s_barrier
buffer_store_dwordx2 v[20:21],v22,s[44:47],s18 offen offset:2304 sc1
s_waitcnt vmcnt(1)
v_cmp_ne_u32 s[56:57],s52,v62
v_cmp_ne_u32 s[58:59],s53,v63
s_nop 0
s_or_b64 s[56:57],s[56:57],s[58:59]
s_cbranch_scc1 L_fr4_%=
L_fo4_%=:
buffer_load_dwordx4 v[64:67],v6,s[36:39],s66 offen sc1
buffer_load_dwordx4 v[68:71],v6,s[36:39],s67 offen sc1
buffer_load_dwordx4 v[72:75],v6,s[36:39],s66 offen offset:1024 sc1
buffer_load_dwordx4 v[76:79],v6,s[36:39],s67 offen offset:1024 sc1
buffer_load_dwordx4 v[80:83],v6,s[36:39],s66 offen offset:2048 sc1
buffer_load_dwordx4 v[84:87],v6,s[36:39],s67 offen offset:2048 sc1
buffer_load_dwordx4 v[88:91],v28,s[36:39],s66 offen offset:3072 sc1
buffer_load_dwordx4 v[92:95],v28,s[36:39],s67 offen offset:3072 sc1
buffer_load_dwordx2 v[4:5],v3,s[44:47],s91 offen offset:2304 sc1
s_waitcnt vmcnt(8)
v_mfma_f32_16x16x32_bf16 v[192:195],v[48:51],v[64:67],v[192:195]
s_waitcnt vmcnt(7)
v_mfma_f32_16x16x32_bf16 v[192:195],v[48:51],v[68:71],v[192:195]
v_mfma_f32_16x16x32_bf16 v[224:227],v[52:55],v[64:67],v[224:227]
v_mfma_f32_16x16x32_bf16 v[224:227],v[52:55],v[68:71],v[224:227]
s_waitcnt vmcnt(6)
v_mfma_f32_16x16x32_bf16 v[200:203],v[48:51],v[72:75],v[200:203]
s_waitcnt vmcnt(5)
v_mfma_f32_16x16x32_bf16 v[200:203],v[48:51],v[76:79],v[200:203]
v_mfma_f32_16x16x32_bf16 v[232:235],v[52:55],v[72:75],v[232:235]
v_mfma_f32_16x16x32_bf16 v[232:235],v[52:55],v[76:79],v[232:235]
s_waitcnt vmcnt(4)
v_mfma_f32_16x16x32_bf16 v[208:211],v[48:51],v[80:83],v[208:211]
s_waitcnt vmcnt(3)
v_mfma_f32_16x16x32_bf16 v[208:211],v[48:51],v[84:87],v[208:211]
v_mfma_f32_16x16x32_bf16 v[240:243],v[52:55],v[80:83],v[240:243]
v_mfma_f32_16x16x32_bf16 v[240:243],v[52:55],v[84:87],v[240:243]
s_waitcnt vmcnt(2)
v_mfma_f32_16x16x32_bf16 v[216:219],v[48:51],v[88:91],v[216:219]
s_waitcnt vmcnt(1)
v_mfma_f32_16x16x32_bf16 v[216:219],v[48:51],v[92:95],v[216:219]
v_mfma_f32_16x16x32_bf16 v[248:251],v[52:55],v[88:91],v[248:251]
v_mfma_f32_16x16x32_bf16 v[248:251],v[52:55],v[92:95],v[248:251]
s_waitcnt vmcnt(0)
v_cmp_ne_u32 s[56:57],s52,v4
v_cmp_ne_u32 s[58:59],s53,v5
s_nop 0
s_or_b64 s[56:57],s[56:57],s[58:59]
s_cbranch_scc1 L_fr5_%=
L_fo5_%=:
buffer_load_dwordx4 v[96:99],v6,s[36:39],s68 offen sc1
buffer_load_dwordx4 v[100:103],v6,s[36:39],s68 offen offset:1024 sc1
buffer_load_dwordx4 v[104:107],v6,s[36:39],s68 offen offset:2048 sc1
buffer_load_dwordx4 v[108:111],v6,s[36:39],s68 offen offset:3072 sc1
buffer_load_dwordx4 v[112:115],v6,s[36:39],s65 offen sc1
buffer_load_dwordx4 v[116:119],v6,s[36:39],s65 offen offset:1024 sc1
buffer_load_dwordx4 v[120:123],v6,s[36:39],s65 offen offset:2048 sc1
buffer_load_dwordx4 v[124:127],v28,s[36:39],s65 offen offset:3072 sc1
s_waitcnt vmcnt(7)
v_mfma_f32_16x16x32_bf16 v[128:131],v[48:51],v[96:99],0
s_waitcnt vmcnt(6)
v_mfma_f32_16x16x32_bf16 v[128:131],v[48:51],v[100:103],v[128:131]
s_waitcnt vmcnt(5)
v_mfma_f32_16x16x32_bf16 v[128:131],v[48:51],v[104:107],v[128:131]
s_waitcnt vmcnt(4)
v_mfma_f32_16x16x32_bf16 v[128:131],v[48:51],v[108:111],v[128:131]
v_mfma_f32_16x16x32_bf16 v[160:163],v[52:55],v[96:99],0
v_mfma_f32_16x16x32_bf16 v[160:163],v[52:55],v[100:103],v[160:163]
v_mfma_f32_16x16x32_bf16 v[160:163],v[52:55],v[104:107],v[160:163]
v_mfma_f32_16x16x32_bf16 v[160:163],v[52:55],v[108:111],v[160:163]
s_waitcnt vmcnt(3)
v_mfma_f32_16x16x32_bf16 v[192:195],v[48:51],v[112:115],v[192:195]
v_mfma_f32_16x16x32_bf16 v[224:227],v[52:55],v[112:115],v[224:227]
s_waitcnt vmcnt(2)
v_mfma_f32_16x16x32_bf16 v[200:203],v[48:51],v[116:119],v[200:203]
v_mfma_f32_16x16x32_bf16 v[232:235],v[52:55],v[116:119],v[232:235]
s_waitcnt vmcnt(1)
v_cvt_pk_bf16_f32 v128,v128,v129
v_cvt_pk_bf16_f32 v129,v130,v131
v_mfma_f32_16x16x32_bf16 v[208:211],v[48:51],v[120:123],v[208:211]
v_mfma_f32_16x16x32_bf16 v[240:243],v[52:55],v[120:123],v[240:243]
s_waitcnt vmcnt(0)
v_cvt_pk_bf16_f32 v130,v160,v161
v_cvt_pk_bf16_f32 v131,v162,v163
v_mfma_f32_16x16x32_bf16 v[216:219],v[48:51],v[124:127],v[216:219]
v_cvt_pk_bf16_f32 v192,v192,v193
v_cvt_pk_bf16_f32 v193,v194,v195
v_cvt_pk_bf16_f32 v194,v224,v225
v_cvt_pk_bf16_f32 v195,v226,v227
v_cvt_pk_bf16_f32 v200,v200,v201
v_cvt_pk_bf16_f32 v201,v202,v203
v_mfma_f32_16x16x32_bf16 v[248:251],v[52:55],v[124:127],v[248:251]
v_permlane16_swap_b32 v128,v130
v_permlane16_swap_b32 v129,v131
v_cvt_pk_bf16_f32 v202,v232,v233
v_cvt_pk_bf16_f32 v203,v234,v235
v_cvt_pk_bf16_f32 v208,v208,v209
v_cvt_pk_bf16_f32 v209,v210,v211
v_cvt_pk_bf16_f32 v210,v240,v241
v_cvt_pk_bf16_f32 v211,v242,v243
buffer_store_dwordx4 v[128:131],v56,s[24:27],s83 offen offset:256 sc1 nt
v_permlane16_swap_b32 v192,v194
v_permlane16_swap_b32 v193,v195
buffer_store_dwordx4 v[192:195],v56,s[24:27],s87 offen sc1 nt
v_permlane16_swap_b32 v200,v202
v_permlane16_swap_b32 v201,v203
v_cvt_pk_bf16_f32 v216,v216,v217
v_cvt_pk_bf16_f32 v217,v218,v219
v_cvt_pk_bf16_f32 v218,v248,v249
v_cvt_pk_bf16_f32 v219,v250,v251
buffer_store_dwordx4 v[200:203],v56,s[24:27],s88 offen sc1 nt
v_permlane16_swap_b32 v208,v210
v_permlane16_swap_b32 v209,v211
buffer_store_dwordx4 v[208:211],v56,s[24:27],s89 offen sc1 nt
v_permlane16_swap_b32 v216,v218
v_permlane16_swap_b32 v217,v219
buffer_store_dwordx4 v[216:219],v56,s[24:27],s90 offen sc1 nt
s_lshl_b32 s69,s62,20
s_add_u32 s69,s69,s18
buffer_store_dword v20,v22,s[44:47],s69 offen sc1
L_geF_%=:
s_branch L_end_%=
.p2align 12
L_pd19_%=:
s_cbranch_execz L_trt1_%=
L_GS_%=:
L_gbS_%=:
s_waitcnt lgkmcnt(14)
v_mfma_f32_16x16x32_bf16 a[192:195],v[64:67],a[68:71],0
buffer_store_dwordx4 v[32:35],v6,s[40:43],s79 offen sc1
s_waitcnt lgkmcnt(12)
v_mfma_f32_16x16x32_bf16 a[192:195],v[68:71],a[76:79],a[192:195]
s_lshl_b32 s91,s63,8
v_bfe_u32 v3,v6,4,2
v_min_u32 v3,2,v3
s_waitcnt lgkmcnt(10)
v_mfma_f32_16x16x32_bf16 a[192:195],v[72:75],a[84:87],a[192:195]
s_add_u32 s87,s64,1
v_add3_u32 v3,v3,s64,1
v_and_b32 v3,3,v3
s_waitcnt lgkmcnt(8)
v_mfma_f32_16x16x32_bf16 a[192:195],v[76:79],a[92:95],a[192:195]
s_and_b32 s87,s87,3
ds_read_b64_tr_b16 v[96:97],v1 offset:64
v_lshlrev_b32 v3,6,v3
v_mfma_f32_16x16x32_bf16 a[196:199],v[76:79],a[124:127],0
s_lshl_b32 s87,s87,1
ds_read_b64_tr_b16 v[98:99],v1 offset:5184
ds_read_b64_tr_b16 v[100:101],v1 offset:10304
v_mfma_f32_16x16x32_bf16 a[196:199],v[72:75],a[116:119],a[196:199]
s_lshl_b32 s87,s87,16
ds_read_b64_tr_b16 v[102:103],v1 offset:15424
v_lshlrev_b32 v29,10,v6
v_mfma_f32_16x16x32_bf16 a[196:199],v[68:71],a[108:111],a[196:199]
s_add_u32 s88,s87,4096
ds_read_b64_tr_b16 v[104:105],v1 offset:20544
v_accvgpr_read_b32 v16,a24
v_mfma_f32_16x16x32_bf16 a[196:199],v[64:67],a[100:103],a[196:199]
s_add_u32 s89,s87,8192
ds_read_b64_tr_b16 v[106:107],v1 offset:25664
v_accvgpr_read_b32 v17,a25
v_mfma_f32_16x16x32_bf16 a[200:203],v[64:67],a[64:67],0
s_add_u32 s90,s87,12288
ds_read_b64_tr_b16 v[108:109],v1 offset:30784
ds_read_b64_tr_b16 v[110:111],v1 offset:35904
v_mfma_f32_16x16x32_bf16 a[200:203],v[68:71],a[72:75],a[200:203]
s_add_u32 s20,s64,2
ds_read_b64_tr_b16 v[112:113],v2 offset:96
v_accvgpr_read_b32 v18,a26
v_mfma_f32_16x16x32_bf16 a[200:203],v[72:75],a[80:83],a[200:203]
s_and_b32 s20,s20,3
ds_read_b64_tr_b16 v[114:115],v2 offset:5216
v_accvgpr_read_b32 v19,a27
v_mfma_f32_16x16x32_bf16 a[200:203],v[76:79],a[88:91],a[200:203]
s_lshl_b32 s20,s20,1
ds_read_b64_tr_b16 v[116:117],v2 offset:10336
v_accvgpr_read_b32 v20,a28
v_mfma_f32_16x16x32_bf16 a[204:207],v[76:79],a[120:123],0
s_lshl_b32 s20,s20,16
ds_read_b64_tr_b16 v[118:119],v2 offset:15456
ds_read_b64_tr_b16 v[120:121],v2 offset:20576
v_mfma_f32_16x16x32_bf16 a[204:207],v[72:75],a[112:115],a[204:207]
s_add_u32 s21,s20,4096
ds_read_b64_tr_b16 v[122:123],v2 offset:25696
v_accvgpr_read_b32 v21,a29
v_mfma_f32_16x16x32_bf16 a[204:207],v[68:71],a[104:107],a[204:207]
s_add_u32 s22,s20,8192
ds_read_b64_tr_b16 v[124:125],v2 offset:30816
v_accvgpr_read_b32 v22,a30
v_mfma_f32_16x16x32_bf16 a[204:207],v[64:67],a[96:99],a[204:207]
buffer_store_dwordx4 v[36:39],v6,s[40:43],s80 offen sc1
s_waitcnt lgkmcnt(9)
v_mfma_f32_16x16x32_bf16 a[208:211],v[80:83],a[68:71],0
s_add_u32 s23,s20,12288
ds_read_b64_tr_b16 v[126:127],v2 offset:35936
ds_read_b64_tr_b16 v[64:65],v1 offset:128
v_mfma_f32_16x16x32_bf16 a[208:211],v[84:87],a[76:79],a[208:211]
s_add_u32 m0,s86,122880
ds_read_b64_tr_b16 v[66:67],v1 offset:5248
v_accvgpr_read_b32 v23,a31
v_mfma_f32_16x16x32_bf16 a[208:211],v[88:91],a[84:87],a[208:211]
s_add_u32 s83,s64,3
ds_read_b64_tr_b16 v[68:69],v1 offset:10368
v_mul_f32 v16,v16,v49
v_mfma_f32_16x16x32_bf16 a[208:211],v[92:95],a[92:95],a[208:211]
s_and_b32 s83,s83,3
ds_read_b64_tr_b16 v[70:71],v1 offset:15488
v_mul_f32 v17,v17,v49
v_mfma_f32_16x16x32_bf16 a[212:215],v[92:95],a[124:127],0
s_waitcnt vmcnt(21)
buffer_load_dwordx2 v[4:5],v3,s[44:47],s91 offen offset:256 sc1
v_mfma_f32_16x16x32_bf16 a[212:215],v[88:91],a[116:119],a[212:215]
s_lshl_b32 s83,s83,1
ds_read_b64_tr_b16 v[72:73],v1 offset:20608
ds_read_b64_tr_b16 v[74:75],v1 offset:25728
v_mfma_f32_16x16x32_bf16 a[212:215],v[84:87],a[108:111],a[212:215]
s_mul_i32 s83,s83,163840
ds_read_b64_tr_b16 v[76:77],v1 offset:30848
v_mul_f32 v18,v18,v49
v_mfma_f32_16x16x32_bf16 a[212:215],v[80:83],a[100:103],a[212:215]
s_add_u32 s83,s83,s85
ds_read_b64_tr_b16 v[78:79],v1 offset:35968
v_mul_f32 v19,v19,v49
v_mfma_f32_16x16x32_bf16 a[216:219],v[80:83],a[64:67],0
s_add_u32 s84,s83,20480
v_mul_f32 v20,v20,v49
v_mul_f32 v21,v21,v49
v_mfma_f32_16x16x32_bf16 a[216:219],v[84:87],a[72:75],a[216:219]
s_lshl_b32 s66,s61,3
v_mul_f32 v22,v22,v49
v_mul_f32 v23,v23,v49
v_mfma_f32_16x16x32_bf16 a[216:219],v[88:91],a[80:83],a[216:219]
s_add_u32 s66,s66,s64
v_cvt_pk_bf16_f32 v44,v16,v17
v_cvt_pk_bf16_f32 v45,v18,v19
v_mfma_f32_16x16x32_bf16 a[216:219],v[92:95],a[88:91],a[216:219]
s_mul_i32 s66,s66,262144
v_cvt_pk_bf16_f32 v46,v20,v21
v_cvt_pk_bf16_f32 v47,v22,v23
v_mfma_f32_16x16x32_bf16 a[220:223],v[92:95],a[120:123],0
s_add_u32 s66,s66,100663296
ds_read_b64_tr_b16 v[92:93],v2 offset:30880
ds_read_b64_tr_b16 v[94:95],v2 offset:36000
v_mfma_f32_16x16x32_bf16 a[220:223],v[88:91],a[112:115],a[220:223]
s_mov_b32 s38,262144
ds_read_b64_tr_b16 v[88:89],v2 offset:20640
v_mul_f32 v132,v132,v53
v_mfma_f32_16x16x32_bf16 a[220:223],v[84:87],a[104:107],a[220:223]
buffer_store_dwordx4 v[40:43],v6,s[40:43],s81 offen sc1
v_mfma_f32_16x16x32_bf16 a[220:223],v[80:83],a[96:99],a[220:223]
s_mov_b32 s39,0x20000
ds_read_b64_tr_b16 v[80:81],v2 offset:160
ds_read_b64_tr_b16 v[82:83],v2 offset:5280
v_mfma_f32_16x16x32_bf16 a[224:227],v[96:99],a[68:71],0
s_add_u32 s74,s63,1
ds_read_b64_tr_b16 v[84:85],v2 offset:10400
v_mul_f32 v133,v133,v53
v_mfma_f32_16x16x32_bf16 a[224:227],v[100:103],a[76:79],a[224:227]
s_and_b32 s74,s74,3
ds_read_b64_tr_b16 v[86:87],v2 offset:15520
v_mul_f32 v134,v134,v53
v_mfma_f32_16x16x32_bf16 a[224:227],v[104:107],a[84:87],a[224:227]
s_lshl_b32 s74,s74,2
ds_read_b64_tr_b16 v[90:91],v2 offset:25760
v_mul_f32 v135,v135,v53
s_waitcnt lgkmcnt(8)
v_mfma_f32_16x16x32_bf16 a[224:227],v[108:111],a[92:95],a[224:227]
s_add_u32 s74,s74,s60
v_mul_f32 v140,v140,v53
v_mul_f32 v141,v141,v53
v_mfma_f32_16x16x32_bf16 a[228:231],v[108:111],a[124:127],0
s_mul_i32 s74,s74,16384
v_mul_f32 v142,v142,v53
v_mul_f32 v143,v143,v53
v_mfma_f32_16x16x32_bf16 a[228:231],v[104:107],a[116:119],a[228:231]
s_add_u32 s74,s74,8192
v_cvt_pk_bf16_f32 v132,v132,v133
v_cvt_pk_bf16_f32 v133,v134,v135
v_mfma_f32_16x16x32_bf16 a[228:231],v[100:103],a[108:111],a[228:231]
s_add_u32 s75,s63,2
v_cvt_pk_bf16_f32 v134,v140,v141
v_cvt_pk_bf16_f32 v135,v142,v143
v_mfma_f32_16x16x32_bf16 a[228:231],v[96:99],a[100:103],a[228:231]
s_and_b32 s75,s75,3
v_mul_f32 v148,v148,v53
v_mul_f32 v149,v149,v53
v_mfma_f32_16x16x32_bf16 a[232:235],v[96:99],a[64:67],0
s_lshl_b32 s75,s75,2
v_mul_f32 v150,v150,v53
v_mul_f32 v151,v151,v53
v_mfma_f32_16x16x32_bf16 a[232:235],v[100:103],a[72:75],a[232:235]
s_add_u32 s75,s75,s60
v_mul_f32 v156,v156,v53
v_mul_f32 v157,v157,v53
v_mfma_f32_16x16x32_bf16 a[232:235],v[104:107],a[80:83],a[232:235]
s_mul_i32 s75,s75,16384
v_mul_f32 v158,v158,v53
)A"
R"A(v_mul_f32 v159,v159,v53
v_mfma_f32_16x16x32_bf16 a[232:235],v[108:111],a[88:91],a[232:235]
s_add_u32 s75,s75,4096
v_cvt_pk_bf16_f32 v148,v148,v149
v_cvt_pk_bf16_f32 v149,v150,v151
v_mfma_f32_16x16x32_bf16 a[236:239],v[108:111],a[120:123],0
s_add_u32 s76,s63,3
ds_read_b64_tr_b16 v[108:109],v1 offset:30912
ds_read_b64_tr_b16 v[110:111],v1 offset:36032
v_mfma_f32_16x16x32_bf16 a[236:239],v[104:107],a[112:115],a[236:239]
buffer_store_dwordx4 v[44:47],v6,s[40:43],s82 offen sc1
v_mfma_f32_16x16x32_bf16 a[236:239],v[100:103],a[104:107],a[236:239]
s_and_b32 s76,s76,3
ds_read_b64_tr_b16 v[100:101],v1 offset:10432
ds_read_b64_tr_b16 v[102:103],v1 offset:15552
v_mfma_f32_16x16x32_bf16 a[236:239],v[96:99],a[96:99],a[236:239]
s_lshl_b32 s76,s76,2
ds_read_b64_tr_b16 v[96:97],v1 offset:192
v_cvt_pk_bf16_f32 v150,v156,v157
v_mfma_f32_16x16x32_bf16 a[240:243],v[112:115],a[68:71],0
s_add_u32 s76,s76,s60
ds_read_b64_tr_b16 v[98:99],v1 offset:5312
v_cvt_pk_bf16_f32 v151,v158,v159
v_mfma_f32_16x16x32_bf16 a[240:243],v[116:119],a[76:79],a[240:243]
s_mul_i32 s76,s76,16384
ds_read_b64_tr_b16 v[104:105],v1 offset:20672
v_mul_f32 v164,v164,v53
v_mfma_f32_16x16x32_bf16 a[240:243],v[120:123],a[84:87],a[240:243]
s_add_u32 s76,s76,0
ds_read_b64_tr_b16 v[106:107],v1 offset:25792
v_mul_f32 v165,v165,v53
v_mfma_f32_16x16x32_bf16 a[240:243],v[124:127],a[92:95],a[240:243]
s_lshl_b32 s67,s63,10
v_mul_f32 v166,v166,v53
v_mul_f32 v167,v167,v53
v_mfma_f32_16x16x32_bf16 a[244:247],v[124:127],a[124:127],0
s_mov_b32 s26,307200
v_mul_f32 v172,v172,v53
v_mul_f32 v173,v173,v53
v_mfma_f32_16x16x32_bf16 a[244:247],v[120:123],a[116:119],a[244:247]
s_mov_b32 s27,0x20000
v_mul_f32 v174,v174,v53
v_mul_f32 v175,v175,v53
v_mfma_f32_16x16x32_bf16 a[244:247],v[116:119],a[108:111],a[244:247]
s_lshl_b32 s77,s63,2
v_cvt_pk_bf16_f32 v164,v164,v165
v_cvt_pk_bf16_f32 v165,v166,v167
v_mfma_f32_16x16x32_bf16 a[244:247],v[112:115],a[100:103],a[244:247]
s_add_u32 s77,s77,s60
v_cvt_pk_bf16_f32 v166,v172,v173
v_cvt_pk_bf16_f32 v167,v174,v175
v_mfma_f32_16x16x32_bf16 a[248:251],v[112:115],a[64:67],0
s_mul_i32 s77,s77,16384
v_mul_f32 v180,v180,v53
v_mul_f32 v181,v181,v53
v_mfma_f32_16x16x32_bf16 a[248:251],v[116:119],a[72:75],a[248:251]
s_add_u32 s65,s77,0
v_mul_f32 v182,v182,v53
v_mul_f32 v183,v183,v53
v_mfma_f32_16x16x32_bf16 a[248:251],v[120:123],a[80:83],a[248:251]
s_add_u32 s68,s77,12288
v_mul_f32 v188,v188,v53
v_mul_f32 v189,v189,v53
v_mfma_f32_16x16x32_bf16 a[248:251],v[124:127],a[88:91],a[248:251]
v_mul_f32 v190,v190,v53
v_mul_f32 v191,v191,v53
v_mfma_f32_16x16x32_bf16 a[252:255],v[124:127],a[120:123],0
buffer_load_dword v30,v29,s[8:11],s7 offen
v_mfma_f32_16x16x32_bf16 a[252:255],v[120:123],a[112:115],a[252:255]
ds_read_b64_tr_b16 v[120:121],v2 offset:20704
ds_read_b64_tr_b16 v[122:123],v2 offset:25824
v_mfma_f32_16x16x32_bf16 a[252:255],v[116:119],a[104:107],a[252:255]
buffer_store_dwordx4 v[132:135],v6,s[40:43],s79 offen offset:1024 sc1
v_mfma_f32_16x16x32_bf16 a[252:255],v[112:115],a[96:99],a[252:255]
ds_read_b64_tr_b16 v[112:113],v2 offset:224
ds_read_b64_tr_b16 v[114:115],v2 offset:5344
v_mfma_f32_16x16x32_bf16 a[0:3],v[64:67],a[68:71],0
ds_read_b64_tr_b16 v[116:117],v2 offset:10464
v_cvt_pk_bf16_f32 v180,v180,v181
v_mfma_f32_16x16x32_bf16 a[0:3],v[68:71],a[76:79],a[0:3]
ds_read_b64_tr_b16 v[118:119],v2 offset:15584
v_cvt_pk_bf16_f32 v181,v182,v183
v_mfma_f32_16x16x32_bf16 a[0:3],v[72:75],a[84:87],a[0:3]
ds_read_b64_tr_b16 v[124:125],v2 offset:30944
v_cvt_pk_bf16_f32 v182,v188,v189
v_mfma_f32_16x16x32_bf16 a[0:3],v[76:79],a[92:95],a[0:3]
ds_read_b64_tr_b16 v[126:127],v2 offset:36064
v_cvt_pk_bf16_f32 v183,v190,v191
v_mfma_f32_16x16x32_bf16 a[4:7],v[76:79],a[124:127],0
v_mul_f32 v196,v196,v57
v_mul_f32 v197,v197,v57
v_mfma_f32_16x16x32_bf16 a[4:7],v[72:75],a[116:119],a[4:7]
v_mul_f32 v198,v198,v57
v_mul_f32 v199,v199,v57
v_mfma_f32_16x16x32_bf16 a[4:7],v[68:71],a[108:111],a[4:7]
v_mul_f32 v204,v204,v57
v_mul_f32 v205,v205,v57
v_mfma_f32_16x16x32_bf16 a[4:7],v[64:67],a[100:103],a[4:7]
v_mul_f32 v206,v206,v57
v_mul_f32 v207,v207,v57
v_mfma_f32_16x16x32_bf16 a[8:11],v[64:67],a[64:67],0
v_cvt_pk_bf16_f32 v196,v196,v197
v_cvt_pk_bf16_f32 v197,v198,v199
v_mfma_f32_16x16x32_bf16 a[8:11],v[68:71],a[72:75],a[8:11]
v_cvt_pk_bf16_f32 v198,v204,v205
v_cvt_pk_bf16_f32 v199,v206,v207
v_mfma_f32_16x16x32_bf16 a[8:11],v[72:75],a[80:83],a[8:11]
v_mul_f32 v212,v212,v57
v_mul_f32 v213,v213,v57
v_mfma_f32_16x16x32_bf16 a[8:11],v[76:79],a[88:91],a[8:11]
v_mul_f32 v214,v214,v57
v_mul_f32 v215,v215,v57
v_mfma_f32_16x16x32_bf16 a[12:15],v[76:79],a[120:123],0
ds_read_b64_tr_b16 v[76:77],v1 offset:30976
ds_read_b64_tr_b16 v[78:79],v1 offset:36096
v_mfma_f32_16x16x32_bf16 a[12:15],v[72:75],a[112:115],a[12:15]
buffer_store_dwordx4 v[148:151],v6,s[40:43],s80 offen offset:1024 sc1
v_mfma_f32_16x16x32_bf16 a[12:15],v[68:71],a[104:107],a[12:15]
ds_read_b64_tr_b16 v[68:69],v1 offset:10496
ds_read_b64_tr_b16 v[70:71],v1 offset:15616
v_mfma_f32_16x16x32_bf16 a[12:15],v[64:67],a[96:99],a[12:15]
s_waitcnt lgkmcnt(4)
ds_read_b64_tr_b16 v[64:65],v1 offset:256
v_mul_f32 v220,v220,v57
v_mfma_f32_16x16x32_bf16 a[16:19],v[80:83],a[68:71],0
ds_read_b64_tr_b16 v[66:67],v1 offset:5376
v_mul_f32 v221,v221,v57
v_mfma_f32_16x16x32_bf16 a[16:19],v[84:87],a[76:79],a[16:19]
ds_read_b64_tr_b16 v[72:73],v1 offset:20736
v_mul_f32 v222,v222,v57
v_mfma_f32_16x16x32_bf16 a[16:19],v[88:91],a[84:87],a[16:19]
ds_read_b64_tr_b16 v[74:75],v1 offset:25856
v_add_u32 v1,0xffff6000,v1
v_mfma_f32_16x16x32_bf16 a[16:19],v[92:95],a[92:95],a[16:19]
v_mul_f32 v223,v223,v57
v_cvt_pk_bf16_f32 v212,v212,v213
v_mfma_f32_16x16x32_bf16 a[20:23],v[92:95],a[124:127],0
v_cvt_pk_bf16_f32 v213,v214,v215
v_cvt_pk_bf16_f32 v214,v220,v221
v_mfma_f32_16x16x32_bf16 a[20:23],v[88:91],a[116:119],a[20:23]
v_cvt_pk_bf16_f32 v215,v222,v223
v_mul_f32 v228,v228,v57
v_mfma_f32_16x16x32_bf16 a[20:23],v[84:87],a[108:111],a[20:23]
v_mul_f32 v229,v229,v57
v_mul_f32 v230,v230,v57
v_mfma_f32_16x16x32_bf16 a[20:23],v[80:83],a[100:103],a[20:23]
v_mul_f32 v231,v231,v57
v_mul_f32 v236,v236,v57
v_mfma_f32_16x16x32_bf16 a[24:27],v[80:83],a[64:67],0
v_mul_f32 v237,v237,v57
v_mul_f32 v238,v238,v57
v_mfma_f32_16x16x32_bf16 a[24:27],v[84:87],a[72:75],a[24:27]
v_mul_f32 v239,v239,v57
v_cvt_pk_bf16_f32 v228,v228,v229
v_mfma_f32_16x16x32_bf16 a[24:27],v[88:91],a[80:83],a[24:27]
s_waitcnt vmcnt(5)
v_cmp_ne_u32 s[56:57],s52,v4
v_cmp_ne_u32 s[58:59],s53,v5
v_mfma_f32_16x16x32_bf16 a[24:27],v[92:95],a[88:91],a[24:27]
s_or_b64 s[56:57],s[56:57],s[58:59]
s_cbranch_scc1 L_fr6_%=
L_fo6_%=:
v_cvt_pk_bf16_f32 v229,v230,v231
v_mfma_f32_16x16x32_bf16 a[28:31],v[92:95],a[120:123],0
buffer_store_dwordx4 v[164:167],v6,s[40:43],s81 offen offset:1024 sc1
v_mfma_f32_16x16x32_bf16 a[28:31],v[88:91],a[112:115],a[28:31]
s_lshl_b32 s91,s63,8
ds_read_b64_tr_b16 v[88:89],v2 offset:20768
ds_read_b64_tr_b16 v[90:91],v2 offset:25888
v_mfma_f32_16x16x32_bf16 a[28:31],v[84:87],a[104:107],a[28:31]
buffer_load_dwordx4 a[128:131],v6,s[28:31],s87 offen sc1
v_mfma_f32_16x16x32_bf16 a[28:31],v[80:83],a[96:99],a[28:31]
ds_read_b64_tr_b16 v[80:81],v2 offset:288
ds_read_b64_tr_b16 v[82:83],v2 offset:5408
v_mfma_f32_16x16x32_bf16 v[128:131],v[96:99],a[68:71],0
ds_read_b64_tr_b16 v[84:85],v2 offset:10528
v_cvt_pk_bf16_f32 v230,v236,v237
v_mfma_f32_16x16x32_bf16 v[128:131],v[100:103],a[76:79],v[128:131]
ds_read_b64_tr_b16 v[86:87],v2 offset:15648
v_cvt_pk_bf16_f32 v231,v238,v239
v_mfma_f32_16x16x32_bf16 v[128:131],v[104:107],a[84:87],v[128:131]
ds_read_b64_tr_b16 v[92:93],v2 offset:31008
v_mul_f32 v244,v244,v57
v_mfma_f32_16x16x32_bf16 v[128:131],v[108:111],a[92:95],v[128:131]
ds_read_b64_tr_b16 v[94:95],v2 offset:36128
v_add_u32 v2,0xffff6000,v2
v_mfma_f32_16x16x32_bf16 v[136:139],v[108:111],a[124:127],0
v_mul_f32 v245,v245,v57
v_mul_f32 v246,v246,v57
v_mfma_f32_16x16x32_bf16 v[136:139],v[104:107],a[116:119],v[136:139]
v_mul_f32 v247,v247,v57
v_mul_f32 v252,v252,v57
v_mfma_f32_16x16x32_bf16 v[136:139],v[100:103],a[108:111],v[136:139]
v_mul_f32 v253,v253,v57
v_mul_f32 v254,v254,v57
v_mfma_f32_16x16x32_bf16 v[136:139],v[96:99],a[100:103],v[136:139]
v_mul_f32 v255,v255,v57
v_cvt_pk_bf16_f32 v244,v244,v245
v_mfma_f32_16x16x32_bf16 v[144:147],v[96:99],a[64:67],0
v_cvt_pk_bf16_f32 v245,v246,v247
v_cvt_pk_bf16_f32 v246,v252,v253
v_mfma_f32_16x16x32_bf16 v[144:147],v[100:103],a[72:75],v[144:147]
buffer_load_dwordx4 a[144:147],v6,s[28:31],s88 offen sc1
v_mfma_f32_16x16x32_bf16 v[144:147],v[104:107],a[80:83],v[144:147]
v_cvt_pk_bf16_f32 v247,v254,v255
v_accvgpr_read_b32 v16,a40
v_mfma_f32_16x16x32_bf16 v[144:147],v[108:111],a[88:91],v[144:147]
buffer_store_dwordx4 v[180:183],v6,s[40:43],s82 offen offset:1024 sc1
v_mfma_f32_16x16x32_bf16 v[152:155],v[108:111],a[120:123],0
ds_read_b64_tr_b16 v[108:109],v1 offset:30720
ds_read_b64_tr_b16 v[110:111],v1 offset:35840
v_mfma_f32_16x16x32_bf16 v[152:155],v[104:107],a[112:115],v[152:155]
ds_read_b64_tr_b16 v[104:105],v1 offset:20480
v_accvgpr_read_b32 v17,a41
v_mfma_f32_16x16x32_bf16 v[152:155],v[100:103],a[104:107],v[152:155]
ds_read_b64_tr_b16 v[100:101],v1 offset:10240
v_accvgpr_read_b32 v18,a42
v_mfma_f32_16x16x32_bf16 v[152:155],v[96:99],a[96:99],v[152:155]
ds_read_b64_tr_b16 v[96:97],v1
v_accvgpr_read_b32 v19,a43
v_mfma_f32_16x16x32_bf16 v[160:163],v[112:115],a[68:71],0
ds_read_b64_tr_b16 v[98:99],v1 offset:5120
ds_read_b64_tr_b16 v[102:103],v1 offset:15360
v_mfma_f32_16x16x32_bf16 v[160:163],v[116:119],a[76:79],v[160:163]
ds_read_b64_tr_b16 v[106:107],v1 offset:25600
v_accvgpr_read_b32 v20,a44
v_mfma_f32_16x16x32_bf16 v[160:163],v[120:123],a[84:87],v[160:163]
v_accvgpr_read_b32 v21,a45
v_accvgpr_read_b32 v22,a46
v_mfma_f32_16x16x32_bf16 v[160:163],v[124:127],a[92:95],v[160:163]
v_accvgpr_read_b32 v23,a47
v_mul_f32 v16,v16,v61
v_mfma_f32_16x16x32_bf16 v[168:171],v[124:127],a[124:127],0
v_mul_f32 v17,v17,v61
v_mul_f32 v18,v18,v61
v_mfma_f32_16x16x32_bf16 v[168:171],v[120:123],a[116:119],v[168:171]
v_mul_f32 v19,v19,v61
v_mul_f32 v20,v20,v61
v_mfma_f32_16x16x32_bf16 v[168:171],v[116:119],a[108:111],v[168:171]
buffer_load_dwordx4 a[160:163],v6,s[28:31],s89 offen sc1
v_mfma_f32_16x16x32_bf16 v[168:171],v[112:115],a[100:103],v[168:171]
v_mul_f32 v21,v21,v61
v_mul_f32 v22,v22,v61
v_mfma_f32_16x16x32_bf16 v[176:179],v[112:115],a[64:67],0
v_mul_f32 v23,v23,v61
v_cvt_pk_bf16_f32 v156,v16,v17
v_mfma_f32_16x16x32_bf16 v[176:179],v[116:119],a[72:75],v[176:179]
v_cvt_pk_bf16_f32 v157,v18,v19
v_cvt_pk_bf16_f32 v158,v20,v21
v_mfma_f32_16x16x32_bf16 v[176:179],v[120:123],a[80:83],v[176:179]
buffer_store_dwordx4 v[196:199],v6,s[40:43],s79 offen offset:2048 sc1
v_mfma_f32_16x16x32_bf16 v[176:179],v[124:127],a[88:91],v[176:179]
v_cvt_pk_bf16_f32 v159,v22,v23
v_accvgpr_read_b32 v16,a56
v_mfma_f32_16x16x32_bf16 v[184:187],v[124:127],a[120:123],0
v_accvgpr_read_b32 v17,a57
v_accvgpr_read_b32 v18,a58
v_mfma_f32_16x16x32_bf16 v[184:187],v[120:123],a[112:115],v[184:187]
ds_read_b64_tr_b16 v[120:121],v2 offset:20512
ds_read_b64_tr_b16 v[122:123],v2 offset:25632
v_mfma_f32_16x16x32_bf16 v[184:187],v[116:119],a[104:107],v[184:187]
ds_read_b64_tr_b16 v[116:117],v2 offset:10272
v_accvgpr_read_b32 v19,a59
v_mfma_f32_16x16x32_bf16 v[184:187],v[112:115],a[96:99],v[184:187]
s_waitcnt lgkmcnt(3)
ds_read_b64_tr_b16 v[112:113],v2 offset:32
v_accvgpr_read_b32 v20,a60
v_mfma_f32_16x16x32_bf16 v[192:195],v[64:67],a[68:71],0
ds_read_b64_tr_b16 v[114:115],v2 offset:5152
v_accvgpr_read_b32 v21,a61
v_mfma_f32_16x16x32_bf16 v[192:195],v[68:71],a[76:79],v[192:195]
ds_read_b64_tr_b16 v[118:119],v2 offset:15392
v_accvgpr_read_b32 v22,a62
v_mfma_f32_16x16x32_bf16 v[192:195],v[72:75],a[84:87],v[192:195]
v_accvgpr_read_b32 v23,a63
v_mul_f32 v16,v16,v61
v_mfma_f32_16x16x32_bf16 v[192:195],v[76:79],a[92:95],v[192:195]
buffer_load_dwordx4 a[176:179],v6,s[28:31],s90 offen sc1
v_mfma_f32_16x16x32_bf16 v[200:203],v[76:79],a[124:127],0
v_mul_f32 v17,v17,v61
v_mul_f32 v18,v18,v61
v_mfma_f32_16x16x32_bf16 v[200:203],v[72:75],a[116:119],v[200:203]
v_mul_f32 v19,v19,v61
v_mul_f32 v20,v20,v61
v_mfma_f32_16x16x32_bf16 v[200:203],v[68:71],a[108:111],v[200:203]
)A"
R"A(v_mul_f32 v21,v21,v61
v_mul_f32 v22,v22,v61
v_mfma_f32_16x16x32_bf16 v[200:203],v[64:67],a[100:103],v[200:203]
v_mul_f32 v23,v23,v61
v_cvt_pk_bf16_f32 v188,v16,v17
v_mfma_f32_16x16x32_bf16 v[208:211],v[64:67],a[64:67],0
v_cvt_pk_bf16_f32 v189,v18,v19
v_cvt_pk_bf16_f32 v190,v20,v21
v_mfma_f32_16x16x32_bf16 v[208:211],v[68:71],a[72:75],v[208:211]
buffer_store_dwordx4 v[212:215],v6,s[40:43],s80 offen offset:2048 sc1
v_mfma_f32_16x16x32_bf16 v[208:211],v[72:75],a[80:83],v[208:211]
ds_read_b64_tr_b16 v[124:125],v2 offset:30752
v_cvt_pk_bf16_f32 v191,v22,v23
v_mfma_f32_16x16x32_bf16 v[208:211],v[76:79],a[88:91],v[208:211]
ds_read_b64_tr_b16 v[126:127],v2 offset:35872
v_accvgpr_read_b32 v8,a32
v_mfma_f32_16x16x32_bf16 v[216:219],v[76:79],a[120:123],0
v_accvgpr_read_b32 v9,a33
v_accvgpr_read_b32 v10,a34
v_mfma_f32_16x16x32_bf16 v[216:219],v[72:75],a[112:115],v[216:219]
v_accvgpr_read_b32 v11,a35
v_accvgpr_read_b32 v12,a36
v_mfma_f32_16x16x32_bf16 v[216:219],v[68:71],a[104:107],v[216:219]
v_accvgpr_read_b32 v13,a37
v_accvgpr_read_b32 v14,a38
v_mfma_f32_16x16x32_bf16 v[216:219],v[64:67],a[96:99],v[216:219]
v_accvgpr_read_b32 v15,a39
v_mul_f32 v8,v8,v61
v_mfma_f32_16x16x32_bf16 v[224:227],v[80:83],a[68:71],0
buffer_load_dwordx4 a[180:183],v6,s[28:31],s90 offen offset:1024 sc1
v_mfma_f32_16x16x32_bf16 v[224:227],v[84:87],a[76:79],v[224:227]
v_mul_f32 v9,v9,v61
v_mul_f32 v10,v10,v61
v_mfma_f32_16x16x32_bf16 v[224:227],v[88:91],a[84:87],v[224:227]
v_mul_f32 v11,v11,v61
v_mul_f32 v12,v12,v61
v_mfma_f32_16x16x32_bf16 v[224:227],v[92:95],a[92:95],v[224:227]
v_mul_f32 v13,v13,v61
v_mul_f32 v14,v14,v61
v_mfma_f32_16x16x32_bf16 v[232:235],v[92:95],a[124:127],0
v_mul_f32 v15,v15,v61
v_cvt_pk_bf16_f32 v140,v8,v9
v_mfma_f32_16x16x32_bf16 v[232:235],v[88:91],a[116:119],v[232:235]
v_cvt_pk_bf16_f32 v141,v10,v11
v_cvt_pk_bf16_f32 v142,v12,v13
v_mfma_f32_16x16x32_bf16 v[232:235],v[84:87],a[108:111],v[232:235]
v_cvt_pk_bf16_f32 v143,v14,v15
v_accvgpr_read_b32 v8,a48
v_mfma_f32_16x16x32_bf16 v[232:235],v[80:83],a[100:103],v[232:235]
v_accvgpr_read_b32 v9,a49
v_accvgpr_read_b32 v10,a50
v_mfma_f32_16x16x32_bf16 v[240:243],v[80:83],a[64:67],0
buffer_store_dwordx4 v[228:231],v6,s[40:43],s81 offen offset:2048 sc1
v_mfma_f32_16x16x32_bf16 v[240:243],v[84:87],a[72:75],v[240:243]
v_accvgpr_read_b32 v11,a51
v_accvgpr_read_b32 v12,a52
v_mfma_f32_16x16x32_bf16 v[240:243],v[88:91],a[80:83],v[240:243]
v_accvgpr_read_b32 v13,a53
v_accvgpr_read_b32 v14,a54
v_mfma_f32_16x16x32_bf16 v[240:243],v[92:95],a[88:91],v[240:243]
v_accvgpr_read_b32 v15,a55
v_mul_f32 v8,v8,v61
v_mfma_f32_16x16x32_bf16 v[248:251],v[92:95],a[120:123],0
v_mul_f32 v9,v9,v61
v_mul_f32 v10,v10,v61
v_mfma_f32_16x16x32_bf16 v[248:251],v[88:91],a[112:115],v[248:251]
buffer_load_dwordx4 a[164:167],v6,s[28:31],s89 offen offset:1024 sc1
v_mfma_f32_16x16x32_bf16 v[248:251],v[84:87],a[104:107],v[248:251]
v_mul_f32 v11,v11,v61
v_mul_f32 v12,v12,v61
v_mfma_f32_16x16x32_bf16 v[248:251],v[80:83],a[96:99],v[248:251]
v_mul_f32 v13,v13,v61
v_mul_f32 v14,v14,v61
v_mfma_f32_16x16x32_bf16 a[192:195],v[96:99],v[32:35],a[192:195]
v_mul_f32 v15,v15,v61
v_cvt_pk_bf16_f32 v172,v8,v9
v_mfma_f32_16x16x32_bf16 a[192:195],v[100:103],v[36:39],a[192:195]
v_cvt_pk_bf16_f32 v173,v10,v11
v_cvt_pk_bf16_f32 v174,v12,v13
v_mfma_f32_16x16x32_bf16 a[192:195],v[104:107],v[40:43],a[192:195]
buffer_load_dwordx4 a[68:71],v6,s[28:31],s20 offen sc1
v_mfma_f32_16x16x32_bf16 a[192:195],v[108:111],v[44:47],a[192:195]
ds_read_b64_tr_b16 v[64:65],v1 offset:64
v_cvt_pk_bf16_f32 v175,v14,v15
v_mfma_f32_16x16x32_bf16 a[196:199],v[108:111],v[180:183],a[196:199]
ds_read_b64_tr_b16 v[66:67],v1 offset:5184
ds_read_b64_tr_b16 v[68:69],v1 offset:10304
v_mfma_f32_16x16x32_bf16 a[196:199],v[104:107],v[164:167],a[196:199]
s_cmp_eq_u32 s60,3
s_cselect_b32 s69,0x40000000,0
ds_read_b64_tr_b16 v[70:71],v1 offset:15424
v_mfma_f32_16x16x32_bf16 a[196:199],v[100:103],v[148:151],a[196:199]
ds_read_b64_tr_b16 v[72:73],v1 offset:20544
v_add_u32 v28,s69,v6
v_mfma_f32_16x16x32_bf16 a[196:199],v[96:99],v[132:135],a[196:199]
buffer_store_dwordx4 v[244:247],v6,s[40:43],s82 offen offset:2048 sc1
v_mfma_f32_16x16x32_bf16 a[200:203],v[96:99],v[196:199],a[200:203]
s_and_b32 s69,s63,s60
ds_read_b64_tr_b16 v[74:75],v1 offset:25664
ds_read_b64_tr_b16 v[76:77],v1 offset:30784
v_mfma_f32_16x16x32_bf16 a[200:203],v[100:103],v[212:215],a[200:203]
ds_read_b64_tr_b16 v[78:79],v1 offset:35904
v_mov_b32 v8,s52
v_mfma_f32_16x16x32_bf16 a[200:203],v[104:107],v[228:231],a[200:203]
buffer_load_dwordx4 a[148:151],v6,s[28:31],s88 offen offset:1024 sc1
v_mfma_f32_16x16x32_bf16 a[200:203],v[108:111],v[244:247],a[200:203]
ds_read_b64_tr_b16 v[80:81],v2 offset:96
ds_read_b64_tr_b16 v[82:83],v2 offset:5216
v_mfma_f32_16x16x32_bf16 a[204:207],v[108:111],v[188:191],a[204:207]
ds_read_b64_tr_b16 v[84:85],v2 offset:10336
v_mov_b32 v9,s53
v_mfma_f32_16x16x32_bf16 a[204:207],v[104:107],v[172:175],a[204:207]
ds_read_b64_tr_b16 v[86:87],v2 offset:15456
v_lshlrev_b32 v10,20,v6
v_mfma_f32_16x16x32_bf16 a[204:207],v[100:103],v[156:159],a[204:207]
s_waitcnt lgkmcnt(7)
ds_read_b64_tr_b16 v[88:89],v2 offset:20576
v_bfe_u32 v3,v6,4,2
v_mfma_f32_16x16x32_bf16 a[204:207],v[96:99],v[140:143],a[204:207]
buffer_load_dwordx4 a[76:79],v6,s[28:31],s21 offen sc1
v_mfma_f32_16x16x32_bf16 a[208:211],v[112:115],v[32:35],a[208:211]
ds_read_b64_tr_b16 v[90:91],v2 offset:25696
ds_read_b64_tr_b16 v[92:93],v2 offset:30816
v_mfma_f32_16x16x32_bf16 a[208:211],v[116:119],v[36:39],a[208:211]
ds_read_b64_tr_b16 v[94:95],v2 offset:35936
v_min_u32 v3,2,v3
v_mfma_f32_16x16x32_bf16 a[208:211],v[120:123],v[40:43],a[208:211]
ds_read_b64_tr_b16 v[96:97],v1 offset:128
v_add3_u32 v3,v3,s64,1
v_mfma_f32_16x16x32_bf16 a[208:211],v[124:127],v[44:47],a[208:211]
ds_read_b64_tr_b16 v[98:99],v1 offset:5248
v_and_b32 v3,3,v3
v_mfma_f32_16x16x32_bf16 a[212:215],v[124:127],v[180:183],a[212:215]
ds_read_b64_tr_b16 v[100:101],v1 offset:10368
ds_read_b64_tr_b16 v[102:103],v1 offset:15488
v_mfma_f32_16x16x32_bf16 a[212:215],v[120:123],v[164:167],a[212:215]
ds_read_b64_tr_b16 v[104:105],v1 offset:20608
v_lshlrev_b32 v3,6,v3
v_mfma_f32_16x16x32_bf16 a[212:215],v[116:119],v[148:151],a[212:215]
s_nop 0
L_pd20_%=:
s_cbranch_execz L_trt1_%=
buffer_store_dwordx4 v[140:143],v28,s[40:43],s79 offen offset:3072 sc1
v_mfma_f32_16x16x32_bf16 a[212:215],v[112:115],v[132:135],a[212:215]
ds_read_b64_tr_b16 v[106:107],v1 offset:25728
ds_read_b64_tr_b16 v[108:109],v1 offset:30848
v_mfma_f32_16x16x32_bf16 a[216:219],v[112:115],v[196:199],a[216:219]
buffer_load_dwordx4 a[132:135],v6,s[28:31],s87 offen offset:1024 sc1
v_mfma_f32_16x16x32_bf16 a[216:219],v[116:119],v[212:215],a[216:219]
ds_read_b64_tr_b16 v[110:111],v1 offset:35968
v_bfe_u32 v56,v6,6,2
v_mfma_f32_16x16x32_bf16 a[216:219],v[120:123],v[228:231],a[216:219]
v_lshrrev_b32 v57,8,v6
v_cmp_eq_u32 s[56:57],v56,v57
v_mfma_f32_16x16x32_bf16 a[216:219],v[124:127],v[244:247],a[216:219]
v_bfe_u32 v56,v6,4,1
v_lshlrev_b32 v56,4,v56
v_mfma_f32_16x16x32_bf16 a[220:223],v[124:127],v[188:191],a[220:223]
ds_read_b64_tr_b16 v[124:125],v2 offset:30880
ds_read_b64_tr_b16 v[126:127],v2 offset:36000
v_mfma_f32_16x16x32_bf16 a[220:223],v[120:123],v[172:175],a[220:223]
buffer_load_dwordx4 a[84:87],v6,s[28:31],s22 offen sc1
v_mfma_f32_16x16x32_bf16 a[220:223],v[116:119],v[156:159],a[220:223]
ds_read_b64_tr_b16 v[116:117],v2 offset:10400
ds_read_b64_tr_b16 v[118:119],v2 offset:15520
v_mfma_f32_16x16x32_bf16 a[220:223],v[112:115],v[140:143],a[220:223]
ds_read_b64_tr_b16 v[112:113],v2 offset:160
v_mov_b32 v57,0x3f80
v_mfma_f32_16x16x32_bf16 a[224:227],v[64:67],v[32:35],a[224:227]
ds_read_b64_tr_b16 v[114:115],v2 offset:5280
v_lshlrev_b32 v56,v56,v57
v_mfma_f32_16x16x32_bf16 a[224:227],v[68:71],v[36:39],a[224:227]
s_waitcnt lgkmcnt(6)
ds_read_b64_tr_b16 v[120:121],v2 offset:20640
v_cndmask_b32 v56,0,v56,s[56:57]
v_mfma_f32_16x16x32_bf16 a[224:227],v[72:75],v[40:43],a[224:227]
ds_read_b64_tr_b16 v[122:123],v2 offset:25760
v_bfe_u32 v57,v6,5,1
v_mfma_f32_16x16x32_bf16 a[224:227],v[76:79],v[44:47],a[224:227]
s_add_u32 s36,s12,s66
s_addc_u32 s37,s13,0
v_cmp_eq_u32 s[56:57],0,v57
v_mfma_f32_16x16x32_bf16 a[228:231],v[76:79],v[180:183],a[228:231]
buffer_load_dwordx4 a[136:139],v6,s[28:31],s87 offen offset:2048 sc1
v_mfma_f32_16x16x32_bf16 a[228:231],v[72:75],v[164:167],a[228:231]
s_and_b32 s37,s37,0xffff
v_cndmask_b32 v48,0,v56,s[56:57]
v_cndmask_b32 v49,v56,0,s[56:57]
v_mfma_f32_16x16x32_bf16 a[228:231],v[68:71],v[148:151],a[228:231]
buffer_store_dwordx4 v[156:159],v28,s[40:43],s80 offen offset:3072 sc1
v_mfma_f32_16x16x32_bf16 a[228:231],v[64:67],v[132:135],a[228:231]
s_mul_i32 s66,s61,307200
v_mov_b32 v58,s52
v_mov_b32 v59,s53
v_mfma_f32_16x16x32_bf16 a[232:235],v[64:67],v[196:199],a[232:235]
s_add_u32 s24,s16,s66
s_addc_u32 s25,s17,0
v_lshlrev_b32 v60,20,v6
v_mfma_f32_16x16x32_bf16 a[232:235],v[68:71],v[212:215],a[232:235]
buffer_load_dwordx4 a[92:95],v6,s[28:31],s23 offen sc1
v_mfma_f32_16x16x32_bf16 a[232:235],v[72:75],v[228:231],a[232:235]
s_mul_i32 s66,s60,16384
s_add_u32 s66,s66,s67
s_add_u32 s79,s66,12288
v_mfma_f32_16x16x32_bf16 a[232:235],v[76:79],v[244:247],a[232:235]
s_add_u32 s80,s66,77824
v_bfe_u32 v61,v6,4,1
v_add3_u32 v61,v61,s63,2
v_mfma_f32_16x16x32_bf16 a[236:239],v[76:79],v[188:191],a[236:239]
s_and_b32 s25,s25,0xffff
ds_read_b64_tr_b16 v[76:77],v1 offset:30912
ds_read_b64_tr_b16 v[78:79],v1 offset:36032
v_mfma_f32_16x16x32_bf16 a[236:239],v[72:75],v[172:175],a[236:239]
s_add_u32 s67,s77,8192
ds_read_b64_tr_b16 v[72:73],v1 offset:20672
v_and_b32 v61,3,v61
v_mfma_f32_16x16x32_bf16 a[236:239],v[68:71],v[156:159],a[236:239]
ds_read_b64_tr_b16 v[68:69],v1 offset:10432
v_lshlrev_b32 v61,8,v61
v_mfma_f32_16x16x32_bf16 a[236:239],v[64:67],v[140:143],a[236:239]
ds_read_b64_tr_b16 v[64:65],v1 offset:192
v_mov_b32 v50,0
v_mfma_f32_16x16x32_bf16 a[240:243],v[80:83],v[32:35],a[240:243]
ds_read_b64_tr_b16 v[66:67],v1 offset:5312
ds_read_b64_tr_b16 v[70:71],v1 offset:15552
v_mfma_f32_16x16x32_bf16 a[240:243],v[84:87],v[36:39],a[240:243]
buffer_load_dwordx4 a[152:155],v6,s[28:31],s88 offen offset:2048 sc1
v_mfma_f32_16x16x32_bf16 a[240:243],v[88:91],v[40:43],a[240:243]
ds_read_b64_tr_b16 v[74:75],v1 offset:25792
v_mov_b32 v51,0
v_mfma_f32_16x16x32_bf16 a[240:243],v[92:95],v[44:47],a[240:243]
v_mov_b32 v52,0
v_mov_b32 v53,0
v_mfma_f32_16x16x32_bf16 a[244:247],v[92:95],v[180:183],a[244:247]
v_mov_b32 v54,v48
v_mov_b32 v55,v49
v_mfma_f32_16x16x32_bf16 a[244:247],v[88:91],v[164:167],a[244:247]
buffer_store_dwordx4 v[172:175],v28,s[40:43],s81 offen offset:3072 sc1
v_mfma_f32_16x16x32_bf16 a[244:247],v[84:87],v[148:151],a[244:247]
s_add_u32 s81,s66,143360
v_bfe_u32 v56,v6,4,4
v_mul_u32_u24 v56,1280,v56
v_mfma_f32_16x16x32_bf16 a[244:247],v[80:83],v[132:135],a[244:247]
buffer_load_dwordx4 a[124:127],v6,s[28:31],s23 offen offset:1024 sc1
v_mfma_f32_16x16x32_bf16 a[248:251],v[80:83],v[196:199],a[248:251]
v_bfe_u32 v57,v6,8,1
v_lshl_add_u32 v56,v57,5,v56
v_mfma_f32_16x16x32_bf16 a[248:251],v[84:87],v[212:215],a[248:251]
v_lshrrev_b32 v57,9,v6
v_lshl_add_u32 v56,v57,4,v56
v_mfma_f32_16x16x32_bf16 a[248:251],v[88:91],v[228:231],a[248:251]
s_cmp_eq_u32 s69,3
s_cselect_b32 s69,0x40000000,0
s_add_u32 s68,s68,s69
v_mfma_f32_16x16x32_bf16 a[248:251],v[92:95],v[244:247],a[248:251]
v_mfma_f32_16x16x32_bf16 a[252:255],v[92:95],v[188:191],a[252:255]
ds_read_b64_tr_b16 v[92:93],v2 offset:30944
ds_read_b64_tr_b16 v[94:95],v2 offset:36064
v_mfma_f32_16x16x32_bf16 a[252:255],v[88:91],v[172:175],a[252:255]
ds_read_b64_tr_b16 v[88:89],v2 offset:20704
v_mfma_f32_16x16x32_bf16 a[252:255],v[84:87],v[156:159],a[252:255]
buffer_load_dwordx4 a[168:171],v6,s[28:31],s89 offen offset:2048 sc1
v_mfma_f32_16x16x32_bf16 a[252:255],v[80:83],v[140:143],a[252:255]
ds_read_b64_tr_b16 v[80:81],v2 offset:224
ds_read_b64_tr_b16 v[82:83],v2 offset:5344
v_mfma_f32_16x16x32_bf16 a[0:3],v[96:99],v[32:35],a[0:3]
ds_read_b64_tr_b16 v[84:85],v2 offset:10464
v_mfma_f32_16x16x32_bf16 a[0:3],v[100:103],v[36:39],a[0:3]
ds_read_b64_tr_b16 v[86:87],v2 offset:15584
)A"
R"A(v_mfma_f32_16x16x32_bf16 a[0:3],v[104:107],v[40:43],a[0:3]
ds_read_b64_tr_b16 v[90:91],v2 offset:25824
v_mfma_f32_16x16x32_bf16 a[0:3],v[108:111],v[44:47],a[0:3]
buffer_load_dwordx4 a[116:119],v6,s[28:31],s22 offen offset:1024 sc1
v_mfma_f32_16x16x32_bf16 a[4:7],v[108:111],v[180:183],a[4:7]
v_mfma_f32_16x16x32_bf16 a[4:7],v[104:107],v[164:167],a[4:7]
buffer_store_dwordx4 v[188:191],v28,s[40:43],s82 offen offset:3072 sc1
v_mfma_f32_16x16x32_bf16 a[4:7],v[100:103],v[148:151],a[4:7]
s_add_u32 s82,s66,208896
s_mul_i32 s66,s64,320
v_mfma_f32_16x16x32_bf16 a[4:7],v[96:99],v[132:135],a[4:7]
v_mfma_f32_16x16x32_bf16 a[8:11],v[96:99],v[196:199],a[8:11]
v_mfma_f32_16x16x32_bf16 a[8:11],v[100:103],v[212:215],a[8:11]
v_mfma_f32_16x16x32_bf16 a[8:11],v[104:107],v[228:231],a[8:11]
v_mfma_f32_16x16x32_bf16 a[8:11],v[108:111],v[244:247],a[8:11]
buffer_load_dwordx4 a[184:187],v6,s[28:31],s90 offen offset:2048 sc1
v_mfma_f32_16x16x32_bf16 a[12:15],v[108:111],v[188:191],a[12:15]
ds_read_b64_tr_b16 v[108:109],v1 offset:30976
ds_read_b64_tr_b16 v[110:111],v1 offset:36096
v_mfma_f32_16x16x32_bf16 a[12:15],v[104:107],v[172:175],a[12:15]
ds_read_b64_tr_b16 v[104:105],v1 offset:20736
v_mfma_f32_16x16x32_bf16 a[12:15],v[100:103],v[156:159],a[12:15]
ds_read_b64_tr_b16 v[100:101],v1 offset:10496
v_mfma_f32_16x16x32_bf16 a[12:15],v[96:99],v[140:143],a[12:15]
s_waitcnt lgkmcnt(4)
ds_read_b64_tr_b16 v[96:97],v1 offset:256
v_mfma_f32_16x16x32_bf16 a[16:19],v[112:115],v[32:35],a[16:19]
buffer_load_dwordx4 a[108:111],v6,s[28:31],s21 offen offset:1024 sc1
v_mfma_f32_16x16x32_bf16 a[16:19],v[116:119],v[36:39],a[16:19]
ds_read_b64_tr_b16 v[98:99],v1 offset:5376
ds_read_b64_tr_b16 v[102:103],v1 offset:15616
v_mfma_f32_16x16x32_bf16 a[16:19],v[120:123],v[40:43],a[16:19]
ds_read_b64_tr_b16 v[106:107],v1 offset:25856
v_add_u32 v1,0xffff6000,v1
v_mfma_f32_16x16x32_bf16 a[16:19],v[124:127],v[44:47],a[16:19]
v_mfma_f32_16x16x32_bf16 a[20:23],v[124:127],v[180:183],a[20:23]
v_mfma_f32_16x16x32_bf16 a[20:23],v[120:123],v[164:167],a[20:23]
v_mfma_f32_16x16x32_bf16 a[20:23],v[116:119],v[148:151],a[20:23]
v_mfma_f32_16x16x32_bf16 a[20:23],v[112:115],v[132:135],a[20:23]
v_mfma_f32_16x16x32_bf16 a[24:27],v[112:115],v[196:199],a[24:27]
buffer_load_dwordx4 a[188:191],v28,s[28:31],s90 offen offset:3072 sc1
v_mfma_f32_16x16x32_bf16 a[24:27],v[116:119],v[212:215],a[24:27]
v_mfma_f32_16x16x32_bf16 a[24:27],v[120:123],v[228:231],a[24:27]
v_mfma_f32_16x16x32_bf16 a[24:27],v[124:127],v[244:247],a[24:27]
v_mfma_f32_16x16x32_bf16 a[28:31],v[124:127],v[188:191],a[28:31]
ds_read_b64_tr_b16 v[124:125],v2 offset:31008
ds_read_b64_tr_b16 v[126:127],v2 offset:36128
v_mfma_f32_16x16x32_bf16 a[28:31],v[120:123],v[172:175],a[28:31]
buffer_load_dwordx4 a[100:103],v6,s[28:31],s20 offen offset:1024 sc1
v_mfma_f32_16x16x32_bf16 a[28:31],v[116:119],v[156:159],a[28:31]
ds_read_b64_tr_b16 v[116:117],v2 offset:10528
ds_read_b64_tr_b16 v[118:119],v2 offset:15648
v_mfma_f32_16x16x32_bf16 a[28:31],v[112:115],v[140:143],a[28:31]
ds_read_b64_tr_b16 v[112:113],v2 offset:288
v_mfma_f32_16x16x32_bf16 v[128:131],v[64:67],v[32:35],v[128:131]
ds_read_b64_tr_b16 v[114:115],v2 offset:5408
v_mfma_f32_16x16x32_bf16 v[128:131],v[68:71],v[36:39],v[128:131]
ds_read_b64_tr_b16 v[120:121],v2 offset:20768
v_mfma_f32_16x16x32_bf16 v[128:131],v[72:75],v[40:43],v[128:131]
ds_read_b64_tr_b16 v[122:123],v2 offset:25888
v_add_u32 v2,0xffff6000,v2
v_mfma_f32_16x16x32_bf16 v[128:131],v[76:79],v[44:47],v[128:131]
v_mfma_f32_16x16x32_bf16 v[136:139],v[76:79],v[180:183],v[136:139]
v_mfma_f32_16x16x32_bf16 v[136:139],v[72:75],v[164:167],v[136:139]
buffer_load_dwordx4 a[172:175],v28,s[28:31],s89 offen offset:3072 sc1
v_mfma_f32_16x16x32_bf16 v[136:139],v[68:71],v[148:151],v[136:139]
v_mfma_f32_16x16x32_bf16 v[136:139],v[64:67],v[132:135],v[136:139]
v_mfma_f32_16x16x32_bf16 v[144:147],v[64:67],v[196:199],v[144:147]
v_mfma_f32_16x16x32_bf16 v[144:147],v[68:71],v[212:215],v[144:147]
v_mfma_f32_16x16x32_bf16 v[144:147],v[72:75],v[228:231],v[144:147]
buffer_load_dwordx4 a[64:67],v6,s[28:31],s20 offen offset:2048 sc1
v_mfma_f32_16x16x32_bf16 v[144:147],v[76:79],v[244:247],v[144:147]
v_mfma_f32_16x16x32_bf16 v[152:155],v[76:79],v[188:191],v[152:155]
v_mfma_f32_16x16x32_bf16 v[152:155],v[72:75],v[172:175],v[152:155]
v_mfma_f32_16x16x32_bf16 v[152:155],v[68:71],v[156:159],v[152:155]
v_mfma_f32_16x16x32_bf16 v[152:155],v[64:67],v[140:143],v[152:155]
s_mov_b64 exec,0
s_cmp_eq_u32 s60,0
s_cbranch_scc1 L_pd21_%=
s_cmp_eq_u32 s60,1
s_cbranch_scc1 L_pd22_%=
s_cmp_eq_u32 s60,2
s_cbranch_scc1 L_pd23_%=
s_branch L_trtg2S_%=
L_trtg2S_%=:
s_mov_b64 exec,-1
s_waitcnt lgkmcnt(0)
s_barrier
v_mfma_f32_16x16x32_bf16 v[160:163],v[80:83],v[32:35],v[160:163]
buffer_load_dwordx4 v24,s[48:51],s83 offen lds
v_mfma_f32_16x16x32_bf16 v[160:163],v[84:87],v[36:39],v[160:163]
ds_read_b64_tr_b16 v[64:65],v1
ds_read_b64_tr_b16 v[66:67],v1 offset:5120
v_mfma_f32_16x16x32_bf16 v[160:163],v[88:91],v[40:43],v[160:163]
ds_read_b64_tr_b16 v[68:69],v1 offset:10240
v_mfma_f32_16x16x32_bf16 v[160:163],v[92:95],v[44:47],v[160:163]
ds_read_b64_tr_b16 v[70:71],v1 offset:15360
v_mfma_f32_16x16x32_bf16 v[168:171],v[92:95],v[180:183],v[168:171]
ds_read_b64_tr_b16 v[72:73],v1 offset:20480
v_mfma_f32_16x16x32_bf16 v[168:171],v[88:91],v[164:167],v[168:171]
buffer_load_dwordx4 a[156:159],v28,s[28:31],s88 offen offset:3072 sc1
v_mfma_f32_16x16x32_bf16 v[168:171],v[84:87],v[148:151],v[168:171]
ds_read_b64_tr_b16 v[74:75],v1 offset:25600
ds_read_b64_tr_b16 v[76:77],v1 offset:30720
v_mfma_f32_16x16x32_bf16 v[168:171],v[80:83],v[132:135],v[168:171]
ds_read_b64_tr_b16 v[78:79],v1 offset:35840
v_mfma_f32_16x16x32_bf16 v[176:179],v[80:83],v[196:199],v[176:179]
v_mfma_f32_16x16x32_bf16 v[176:179],v[84:87],v[212:215],v[176:179]
v_mfma_f32_16x16x32_bf16 v[176:179],v[88:91],v[228:231],v[176:179]
buffer_load_dwordx4 a[72:75],v6,s[28:31],s21 offen offset:2048 sc1
v_mfma_f32_16x16x32_bf16 v[176:179],v[92:95],v[244:247],v[176:179]
v_mfma_f32_16x16x32_bf16 v[184:187],v[92:95],v[188:191],v[184:187]
ds_read_b64_tr_b16 v[92:93],v2 offset:30752
ds_read_b64_tr_b16 v[94:95],v2 offset:35872
v_mfma_f32_16x16x32_bf16 v[184:187],v[88:91],v[172:175],v[184:187]
ds_read_b64_tr_b16 v[88:89],v2 offset:20512
v_mfma_f32_16x16x32_bf16 v[184:187],v[84:87],v[156:159],v[184:187]
ds_read_b64_tr_b16 v[84:85],v2 offset:10272
v_mfma_f32_16x16x32_bf16 v[184:187],v[80:83],v[140:143],v[184:187]
buffer_load_dwordx4 a[140:143],v28,s[28:31],s87 offen offset:3072 sc1
v_mfma_f32_16x16x32_bf16 v[192:195],v[96:99],v[32:35],v[192:195]
s_add_u32 s87,s64,3
ds_read_b64_tr_b16 v[80:81],v2 offset:32
ds_read_b64_tr_b16 v[82:83],v2 offset:5152
v_mfma_f32_16x16x32_bf16 v[192:195],v[100:103],v[36:39],v[192:195]
s_and_b32 s87,s87,3
s_lshl_b32 s87,s87,1
ds_read_b64_tr_b16 v[86:87],v2 offset:15392
v_mfma_f32_16x16x32_bf16 v[192:195],v[104:107],v[40:43],v[192:195]
s_lshl_b32 s87,s87,16
s_add_u32 s88,s87,4096
ds_read_b64_tr_b16 v[90:91],v2 offset:25632
v_mfma_f32_16x16x32_bf16 v[192:195],v[108:111],v[44:47],v[192:195]
s_add_u32 s89,s87,8192
s_add_u32 s90,s87,12288
v_mfma_f32_16x16x32_bf16 v[200:203],v[108:111],v[180:183],v[200:203]
buffer_load_dwordx4 a[80:83],v6,s[28:31],s22 offen offset:2048 sc1
v_mfma_f32_16x16x32_bf16 v[200:203],v[104:107],v[164:167],v[200:203]
v_mfma_f32_16x16x32_bf16 v[200:203],v[100:103],v[148:151],v[200:203]
v_mfma_f32_16x16x32_bf16 v[200:203],v[96:99],v[132:135],v[200:203]
v_mfma_f32_16x16x32_bf16 v[208:211],v[96:99],v[196:199],v[208:211]
v_mfma_f32_16x16x32_bf16 v[208:211],v[100:103],v[212:215],v[208:211]
v_mfma_f32_16x16x32_bf16 v[208:211],v[104:107],v[228:231],v[208:211]
v_mfma_f32_16x16x32_bf16 v[208:211],v[108:111],v[244:247],v[208:211]
buffer_load_dwordx4 v25,s[48:51],s83 offen offset:1024 lds
v_mfma_f32_16x16x32_bf16 v[216:219],v[108:111],v[188:191],v[216:219]
ds_read_b64_tr_b16 v[108:109],v1 offset:30784
ds_read_b64_tr_b16 v[110:111],v1 offset:35904
v_mfma_f32_16x16x32_bf16 v[216:219],v[104:107],v[172:175],v[216:219]
ds_read_b64_tr_b16 v[104:105],v1 offset:20544
v_mfma_f32_16x16x32_bf16 v[216:219],v[100:103],v[156:159],v[216:219]
ds_read_b64_tr_b16 v[100:101],v1 offset:10304
v_mfma_f32_16x16x32_bf16 v[216:219],v[96:99],v[140:143],v[216:219]
ds_read_b64_tr_b16 v[96:97],v1 offset:64
v_mfma_f32_16x16x32_bf16 v[224:227],v[112:115],v[32:35],v[224:227]
buffer_load_dwordx4 a[88:91],v6,s[28:31],s23 offen offset:2048 sc1
v_mfma_f32_16x16x32_bf16 v[224:227],v[116:119],v[36:39],v[224:227]
ds_read_b64_tr_b16 v[98:99],v1 offset:5184
ds_read_b64_tr_b16 v[102:103],v1 offset:15424
v_mfma_f32_16x16x32_bf16 v[224:227],v[120:123],v[40:43],v[224:227]
ds_read_b64_tr_b16 v[106:107],v1 offset:25664
v_mfma_f32_16x16x32_bf16 v[224:227],v[124:127],v[44:47],v[224:227]
v_mfma_f32_16x16x32_bf16 v[232:235],v[124:127],v[180:183],v[232:235]
v_mfma_f32_16x16x32_bf16 v[232:235],v[120:123],v[164:167],v[232:235]
v_mfma_f32_16x16x32_bf16 v[232:235],v[116:119],v[148:151],v[232:235]
v_mfma_f32_16x16x32_bf16 v[232:235],v[112:115],v[132:135],v[232:235]
buffer_load_dwordx4 v26,s[48:51],s83 offen offset:2048 lds
v_mfma_f32_16x16x32_bf16 v[240:243],v[112:115],v[196:199],v[240:243]
v_mfma_f32_16x16x32_bf16 v[240:243],v[116:119],v[212:215],v[240:243]
v_mfma_f32_16x16x32_bf16 v[240:243],v[120:123],v[228:231],v[240:243]
v_mfma_f32_16x16x32_bf16 v[240:243],v[124:127],v[244:247],v[240:243]
v_mfma_f32_16x16x32_bf16 v[248:251],v[124:127],v[188:191],v[248:251]
ds_read_b64_tr_b16 v[124:125],v2 offset:30816
ds_read_b64_tr_b16 v[126:127],v2 offset:35936
v_mfma_f32_16x16x32_bf16 v[248:251],v[120:123],v[172:175],v[248:251]
buffer_load_dwordx4 a[120:123],v28,s[28:31],s23 offen offset:3072 sc1
v_mfma_f32_16x16x32_bf16 v[248:251],v[116:119],v[156:159],v[248:251]
ds_read_b64_tr_b16 v[116:117],v2 offset:10336
ds_read_b64_tr_b16 v[118:119],v2 offset:15456
v_mfma_f32_16x16x32_bf16 v[248:251],v[112:115],v[140:143],v[248:251]
s_waitcnt vmcnt(39) lgkmcnt(4)
ds_read_b64_tr_b16 v[112:113],v2 offset:96
v_mfma_f32_16x16x32_bf16 a[192:195],v[64:67],a[128:131],a[192:195]
ds_read_b64_tr_b16 v[114:115],v2 offset:5216
v_mfma_f32_16x16x32_bf16 a[192:195],v[68:71],a[144:147],a[192:195]
s_waitcnt vmcnt(37)
ds_read_b64_tr_b16 v[120:121],v2 offset:20576
v_mfma_f32_16x16x32_bf16 a[192:195],v[72:75],a[160:163],a[192:195]
s_waitcnt vmcnt(35)
ds_read_b64_tr_b16 v[122:123],v2 offset:25696
v_mfma_f32_16x16x32_bf16 a[192:195],v[76:79],a[176:179],a[192:195]
s_waitcnt vmcnt(27)
v_mfma_f32_16x16x32_bf16 a[196:199],v[76:79],a[180:183],a[196:199]
buffer_load_dwordx4 v27,s[48:51],s83 offen offset:3072 lds
v_mfma_f32_16x16x32_bf16 a[196:199],v[72:75],a[164:167],a[196:199]
s_add_u32 m0,s86,126976
v_mfma_f32_16x16x32_bf16 a[196:199],v[68:71],a[148:151],a[196:199]
s_waitcnt vmcnt(24)
v_mfma_f32_16x16x32_bf16 a[196:199],v[64:67],a[132:135],a[196:199]
v_mfma_f32_16x16x32_bf16 a[200:203],v[64:67],a[136:139],a[200:203]
s_waitcnt vmcnt(21)
v_mfma_f32_16x16x32_bf16 a[200:203],v[68:71],a[152:155],a[200:203]
s_waitcnt vmcnt(15)
v_mfma_f32_16x16x32_bf16 a[200:203],v[72:75],a[168:171],a[200:203]
buffer_load_dwordx4 a[112:115],v28,s[28:31],s22 offen offset:3072 sc1
v_mfma_f32_16x16x32_bf16 a[200:203],v[76:79],a[184:187],a[200:203]
s_waitcnt vmcnt(14)
v_mfma_f32_16x16x32_bf16 a[204:207],v[76:79],a[188:191],a[204:207]
s_waitcnt vmcnt(12)
ds_read_b64_tr_b16 v[76:77],v1 offset:30848
ds_read_b64_tr_b16 v[78:79],v1 offset:35968
v_mfma_f32_16x16x32_bf16 a[204:207],v[72:75],a[172:175],a[204:207]
s_waitcnt vmcnt(9)
ds_read_b64_tr_b16 v[72:73],v1 offset:20608
v_mfma_f32_16x16x32_bf16 a[204:207],v[68:71],a[156:159],a[204:207]
s_waitcnt vmcnt(7)
ds_read_b64_tr_b16 v[68:69],v1 offset:10368
v_mfma_f32_16x16x32_bf16 a[204:207],v[64:67],a[140:143],a[204:207]
ds_read_b64_tr_b16 v[64:65],v1 offset:128
v_mfma_f32_16x16x32_bf16 a[208:211],v[80:83],a[128:131],a[208:211]
ds_read_b64_tr_b16 v[66:67],v1 offset:5248
ds_read_b64_tr_b16 v[70:71],v1 offset:15488
v_mfma_f32_16x16x32_bf16 a[208:211],v[84:87],a[144:147],a[208:211]
buffer_load_dwordx4 v7,s[48:51],s83 offen lds
v_mfma_f32_16x16x32_bf16 a[208:211],v[88:91],a[160:163],a[208:211]
)A"
R"A(s_add_u32 m0,s86,128000
s_add_u32 s83,s64,1
ds_read_b64_tr_b16 v[74:75],v1 offset:25728
v_mfma_f32_16x16x32_bf16 a[208:211],v[92:95],a[176:179],a[208:211]
s_and_b32 s83,s83,3
s_lshl_b32 s83,s83,1
s_or_b32 s83,s83,1
v_mfma_f32_16x16x32_bf16 a[212:215],v[92:95],a[180:183],a[212:215]
s_mul_i32 s83,s83,163840
s_add_u32 s83,s83,s85
v_mfma_f32_16x16x32_bf16 a[212:215],v[88:91],a[164:167],a[212:215]
v_mfma_f32_16x16x32_bf16 a[212:215],v[84:87],a[148:151],a[212:215]
v_mfma_f32_16x16x32_bf16 a[212:215],v[80:83],a[132:135],a[212:215]
buffer_load_dwordx4 a[104:107],v28,s[28:31],s21 offen offset:3072 sc1
v_mfma_f32_16x16x32_bf16 a[216:219],v[80:83],a[136:139],a[216:219]
v_mfma_f32_16x16x32_bf16 a[216:219],v[84:87],a[152:155],a[216:219]
v_mfma_f32_16x16x32_bf16 a[216:219],v[88:91],a[168:171],a[216:219]
v_mfma_f32_16x16x32_bf16 a[216:219],v[92:95],a[184:187],a[216:219]
v_mfma_f32_16x16x32_bf16 a[220:223],v[92:95],a[188:191],a[220:223]
ds_read_b64_tr_b16 v[92:93],v2 offset:30880
ds_read_b64_tr_b16 v[94:95],v2 offset:36000
v_mfma_f32_16x16x32_bf16 a[220:223],v[88:91],a[172:175],a[220:223]
ds_read_b64_tr_b16 v[88:89],v2 offset:20640
v_mfma_f32_16x16x32_bf16 a[220:223],v[84:87],a[156:159],a[220:223]
buffer_load_dwordx4 v24,s[48:51],s84 offen lds
v_mfma_f32_16x16x32_bf16 a[220:223],v[80:83],a[140:143],a[220:223]
ds_read_b64_tr_b16 v[80:81],v2 offset:160
ds_read_b64_tr_b16 v[82:83],v2 offset:5280
s_barrier
v_mfma_f32_16x16x32_bf16 a[224:227],v[96:99],a[128:131],a[224:227]
ds_read_b64_tr_b16 v[84:85],v2 offset:10400
v_mfma_f32_16x16x32_bf16 a[224:227],v[100:103],a[144:147],a[224:227]
ds_read_b64_tr_b16 v[86:87],v2 offset:15520
v_mfma_f32_16x16x32_bf16 a[224:227],v[104:107],a[160:163],a[224:227]
buffer_store_dwordx2 v[8:9],v10,s[44:47],s18 offen offset:1280 sc1
v_mfma_f32_16x16x32_bf16 a[224:227],v[108:111],a[176:179],a[224:227]
ds_read_b64_tr_b16 v[90:91],v2 offset:25760
v_mfma_f32_16x16x32_bf16 a[228:231],v[108:111],a[180:183],a[228:231]
v_mfma_f32_16x16x32_bf16 a[228:231],v[104:107],a[164:167],a[228:231]
v_mfma_f32_16x16x32_bf16 a[228:231],v[100:103],a[148:151],a[228:231]
v_mfma_f32_16x16x32_bf16 a[228:231],v[96:99],a[132:135],a[228:231]
buffer_load_dwordx4 a[96:99],v28,s[28:31],s20 offen offset:3072 sc1
v_mfma_f32_16x16x32_bf16 a[232:235],v[96:99],a[136:139],a[232:235]
s_add_u32 s20,s64,1
s_and_b32 s20,s20,3
s_lshl_b32 s20,s20,1
v_mfma_f32_16x16x32_bf16 a[232:235],v[100:103],a[152:155],a[232:235]
s_or_b32 s20,s20,1
s_lshl_b32 s20,s20,16
s_add_u32 s21,s20,4096
v_mfma_f32_16x16x32_bf16 a[232:235],v[104:107],a[168:171],a[232:235]
s_add_u32 s22,s20,8192
s_add_u32 s23,s20,12288
v_mfma_f32_16x16x32_bf16 a[232:235],v[108:111],a[184:187],a[232:235]
v_mfma_f32_16x16x32_bf16 a[236:239],v[108:111],a[188:191],a[236:239]
ds_read_b64_tr_b16 v[108:109],v1 offset:30912
ds_read_b64_tr_b16 v[110:111],v1 offset:36032
v_mfma_f32_16x16x32_bf16 a[236:239],v[104:107],a[172:175],a[236:239]
ds_read_b64_tr_b16 v[104:105],v1 offset:20672
v_mfma_f32_16x16x32_bf16 a[236:239],v[100:103],a[156:159],a[236:239]
buffer_load_dwordx4 v25,s[48:51],s84 offen offset:1024 lds
v_mfma_f32_16x16x32_bf16 a[236:239],v[96:99],a[140:143],a[236:239]
s_waitcnt lgkmcnt(3)
ds_read_b64_tr_b16 v[96:97],v1 offset:192
ds_read_b64_tr_b16 v[98:99],v1 offset:5312
v_mfma_f32_16x16x32_bf16 a[240:243],v[112:115],a[128:131],a[240:243]
ds_read_b64_tr_b16 v[100:101],v1 offset:10432
v_mfma_f32_16x16x32_bf16 a[240:243],v[116:119],a[144:147],a[240:243]
ds_read_b64_tr_b16 v[102:103],v1 offset:15552
v_mfma_f32_16x16x32_bf16 a[240:243],v[120:123],a[160:163],a[240:243]
ds_read_b64_tr_b16 v[106:107],v1 offset:25792
v_mfma_f32_16x16x32_bf16 a[240:243],v[124:127],a[176:179],a[240:243]
v_mfma_f32_16x16x32_bf16 a[244:247],v[124:127],a[180:183],a[244:247]
v_mfma_f32_16x16x32_bf16 a[244:247],v[120:123],a[164:167],a[244:247]
buffer_load_dwordx4 v26,s[48:51],s84 offen offset:2048 lds
v_mfma_f32_16x16x32_bf16 a[244:247],v[116:119],a[148:151],a[244:247]
v_mfma_f32_16x16x32_bf16 a[244:247],v[112:115],a[132:135],a[244:247]
v_mfma_f32_16x16x32_bf16 a[248:251],v[112:115],a[136:139],a[248:251]
v_mfma_f32_16x16x32_bf16 a[248:251],v[116:119],a[152:155],a[248:251]
v_mfma_f32_16x16x32_bf16 a[248:251],v[120:123],a[168:171],a[248:251]
v_mfma_f32_16x16x32_bf16 a[248:251],v[124:127],a[184:187],a[248:251]
v_mfma_f32_16x16x32_bf16 a[252:255],v[124:127],a[188:191],a[252:255]
buffer_load_dwordx4 v27,s[48:51],s84 offen offset:3072 lds
v_mfma_f32_16x16x32_bf16 a[252:255],v[120:123],a[172:175],a[252:255]
s_add_u32 m0,s86,132096
ds_read_b64_tr_b16 v[120:121],v2 offset:20704
ds_read_b64_tr_b16 v[122:123],v2 offset:25824
v_mfma_f32_16x16x32_bf16 a[252:255],v[116:119],a[156:159],a[252:255]
ds_read_b64_tr_b16 v[116:117],v2 offset:10464
v_mfma_f32_16x16x32_bf16 a[252:255],v[112:115],a[140:143],a[252:255]
ds_read_b64_tr_b16 v[112:113],v2 offset:224
v_mfma_f32_16x16x32_bf16 a[0:3],v[64:67],a[128:131],a[0:3]
ds_read_b64_tr_b16 v[114:115],v2 offset:5344
v_mfma_f32_16x16x32_bf16 a[0:3],v[68:71],a[144:147],a[0:3]
ds_read_b64_tr_b16 v[118:119],v2 offset:15584
ds_read_b64_tr_b16 v[124:125],v2 offset:30944
v_mfma_f32_16x16x32_bf16 a[0:3],v[72:75],a[160:163],a[0:3]
ds_read_b64_tr_b16 v[126:127],v2 offset:36064
v_mfma_f32_16x16x32_bf16 a[0:3],v[76:79],a[176:179],a[0:3]
buffer_load_dwordx4 v7,s[48:51],s84 offen lds
v_mfma_f32_16x16x32_bf16 a[4:7],v[76:79],a[180:183],a[4:7]
s_add_u32 m0,s86,81920
s_add_u32 s84,s83,20480
v_mfma_f32_16x16x32_bf16 a[4:7],v[72:75],a[164:167],a[4:7]
v_mfma_f32_16x16x32_bf16 a[4:7],v[68:71],a[148:151],a[4:7]
v_mfma_f32_16x16x32_bf16 a[4:7],v[64:67],a[132:135],a[4:7]
v_mfma_f32_16x16x32_bf16 a[8:11],v[64:67],a[136:139],a[8:11]
v_mfma_f32_16x16x32_bf16 a[8:11],v[68:71],a[152:155],a[8:11]
v_mfma_f32_16x16x32_bf16 a[8:11],v[72:75],a[168:171],a[8:11]
buffer_load_dwordx4 v24,s[48:51],s83 offen lds
v_mfma_f32_16x16x32_bf16 a[8:11],v[76:79],a[184:187],a[8:11]
v_mfma_f32_16x16x32_bf16 a[12:15],v[76:79],a[188:191],a[12:15]
ds_read_b64_tr_b16 v[76:77],v1 offset:30976
ds_read_b64_tr_b16 v[78:79],v1 offset:36096
v_mfma_f32_16x16x32_bf16 a[12:15],v[72:75],a[172:175],a[12:15]
ds_read_b64_tr_b16 v[72:73],v1 offset:20736
v_mfma_f32_16x16x32_bf16 a[12:15],v[68:71],a[156:159],a[12:15]
ds_read_b64_tr_b16 v[68:69],v1 offset:10496
v_mfma_f32_16x16x32_bf16 a[12:15],v[64:67],a[140:143],a[12:15]
ds_read_b64_tr_b16 v[64:65],v1 offset:256
v_mfma_f32_16x16x32_bf16 a[16:19],v[80:83],a[128:131],a[16:19]
ds_read_b64_tr_b16 v[66:67],v1 offset:5376
ds_read_b64_tr_b16 v[70:71],v1 offset:15616
v_mfma_f32_16x16x32_bf16 a[16:19],v[84:87],a[144:147],a[16:19]
buffer_load_dwordx4 v25,s[48:51],s83 offen offset:1024 lds
v_mfma_f32_16x16x32_bf16 a[16:19],v[88:91],a[160:163],a[16:19]
ds_read_b64_tr_b16 v[74:75],v1 offset:25856
v_add_u32 v1,0xffff6000,v1
v_mfma_f32_16x16x32_bf16 a[16:19],v[92:95],a[176:179],a[16:19]
v_mfma_f32_16x16x32_bf16 a[20:23],v[92:95],a[180:183],a[20:23]
v_mfma_f32_16x16x32_bf16 a[20:23],v[88:91],a[164:167],a[20:23]
v_mfma_f32_16x16x32_bf16 a[20:23],v[84:87],a[148:151],a[20:23]
v_mfma_f32_16x16x32_bf16 a[20:23],v[80:83],a[132:135],a[20:23]
v_mfma_f32_16x16x32_bf16 a[24:27],v[80:83],a[136:139],a[24:27]
buffer_load_dwordx4 v26,s[48:51],s83 offen offset:2048 lds
v_mfma_f32_16x16x32_bf16 a[24:27],v[84:87],a[152:155],a[24:27]
v_mfma_f32_16x16x32_bf16 a[24:27],v[88:91],a[168:171],a[24:27]
v_mfma_f32_16x16x32_bf16 a[24:27],v[92:95],a[184:187],a[24:27]
v_mfma_f32_16x16x32_bf16 a[28:31],v[92:95],a[188:191],a[28:31]
ds_read_b64_tr_b16 v[92:93],v2 offset:31008
ds_read_b64_tr_b16 v[94:95],v2 offset:36128
v_mfma_f32_16x16x32_bf16 a[28:31],v[88:91],a[172:175],a[28:31]
ds_read_b64_tr_b16 v[88:89],v2 offset:20768
v_mfma_f32_16x16x32_bf16 a[28:31],v[84:87],a[156:159],a[28:31]
s_waitcnt lgkmcnt(3)
ds_read_b64_tr_b16 v[84:85],v2 offset:10528
v_mfma_f32_16x16x32_bf16 a[28:31],v[80:83],a[140:143],a[28:31]
buffer_load_dwordx4 v27,s[48:51],s83 offen offset:3072 lds
v_mfma_f32_16x16x32_bf16 v[128:131],v[96:99],a[128:131],v[128:131]
s_add_u32 m0,s86,86016
ds_read_b64_tr_b16 v[80:81],v2 offset:288
ds_read_b64_tr_b16 v[82:83],v2 offset:5408
v_mfma_f32_16x16x32_bf16 v[128:131],v[100:103],a[144:147],v[128:131]
ds_read_b64_tr_b16 v[86:87],v2 offset:15648
v_mfma_f32_16x16x32_bf16 v[128:131],v[104:107],a[160:163],v[128:131]
ds_read_b64_tr_b16 v[90:91],v2 offset:25888
v_add_u32 v2,0xffff6000,v2
v_mfma_f32_16x16x32_bf16 v[128:131],v[108:111],a[176:179],v[128:131]
v_mfma_f32_16x16x32_bf16 v[136:139],v[108:111],a[180:183],v[136:139]
v_mfma_f32_16x16x32_bf16 v[136:139],v[104:107],a[164:167],v[136:139]
v_mfma_f32_16x16x32_bf16 v[136:139],v[100:103],a[148:151],v[136:139]
buffer_load_dwordx4 v7,s[48:51],s83 offen lds
v_mfma_f32_16x16x32_bf16 v[136:139],v[96:99],a[132:135],v[136:139]
s_add_u32 m0,s86,87040
s_add_u32 s83,s64,2
s_and_b32 s83,s83,3
v_mfma_f32_16x16x32_bf16 v[144:147],v[96:99],a[136:139],v[144:147]
s_lshl_b32 s83,s83,1
s_or_b32 s83,s83,1
s_mul_i32 s83,s83,163840
v_mfma_f32_16x16x32_bf16 v[144:147],v[100:103],a[152:155],v[144:147]
s_add_u32 s83,s83,s85
v_mfma_f32_16x16x32_bf16 v[144:147],v[104:107],a[168:171],v[144:147]
v_mfma_f32_16x16x32_bf16 v[144:147],v[108:111],a[184:187],v[144:147]
v_mfma_f32_16x16x32_bf16 v[152:155],v[108:111],a[188:191],v[152:155]
ds_read_b64_tr_b16 v[108:109],v1 offset:30720
ds_read_b64_tr_b16 v[110:111],v1 offset:35840
v_mfma_f32_16x16x32_bf16 v[152:155],v[104:107],a[172:175],v[152:155]
buffer_load_dwordx4 v24,s[48:51],s84 offen lds
v_mfma_f32_16x16x32_bf16 v[152:155],v[100:103],a[156:159],v[152:155]
ds_read_b64_tr_b16 v[100:101],v1 offset:10240
ds_read_b64_tr_b16 v[102:103],v1 offset:15360
v_mfma_f32_16x16x32_bf16 v[152:155],v[96:99],a[140:143],v[152:155]
ds_read_b64_tr_b16 v[96:97],v1
s_waitcnt lgkmcnt(5)
s_barrier
v_mfma_f32_16x16x32_bf16 v[160:163],v[112:115],a[128:131],v[160:163]
ds_read_b64_tr_b16 v[98:99],v1 offset:5120
v_mfma_f32_16x16x32_bf16 v[160:163],v[116:119],a[144:147],v[160:163]
ds_read_b64_tr_b16 v[104:105],v1 offset:20480
v_mfma_f32_16x16x32_bf16 v[160:163],v[120:123],a[160:163],v[160:163]
ds_read_b64_tr_b16 v[106:107],v1 offset:25600
v_mfma_f32_16x16x32_bf16 v[160:163],v[124:127],a[176:179],v[160:163]
buffer_load_dwordx4 v25,s[48:51],s84 offen offset:1024 lds
v_mfma_f32_16x16x32_bf16 v[168:171],v[124:127],a[180:183],v[168:171]
v_mfma_f32_16x16x32_bf16 v[168:171],v[120:123],a[164:167],v[168:171]
L_pd21_%=:
s_cbranch_execz L_trtg2S_%=
v_mfma_f32_16x16x32_bf16 v[168:171],v[116:119],a[148:151],v[168:171]
v_mfma_f32_16x16x32_bf16 v[168:171],v[112:115],a[132:135],v[168:171]
v_mfma_f32_16x16x32_bf16 v[176:179],v[112:115],a[136:139],v[176:179]
v_mfma_f32_16x16x32_bf16 v[176:179],v[116:119],a[152:155],v[176:179]
v_mfma_f32_16x16x32_bf16 v[176:179],v[120:123],a[168:171],v[176:179]
buffer_load_dwordx4 v26,s[48:51],s84 offen offset:2048 lds
v_mfma_f32_16x16x32_bf16 v[176:179],v[124:127],a[184:187],v[176:179]
v_mfma_f32_16x16x32_bf16 v[184:187],v[124:127],a[188:191],v[184:187]
ds_read_b64_tr_b16 v[124:125],v2 offset:30752
ds_read_b64_tr_b16 v[126:127],v2 offset:35872
v_mfma_f32_16x16x32_bf16 v[184:187],v[120:123],a[172:175],v[184:187]
ds_read_b64_tr_b16 v[120:121],v2 offset:20512
v_mfma_f32_16x16x32_bf16 v[184:187],v[116:119],a[156:159],v[184:187]
ds_read_b64_tr_b16 v[116:117],v2 offset:10272
v_mfma_f32_16x16x32_bf16 v[184:187],v[112:115],a[140:143],v[184:187]
ds_read_b64_tr_b16 v[112:113],v2 offset:32
v_mfma_f32_16x16x32_bf16 v[192:195],v[64:67],a[128:131],v[192:195]
ds_read_b64_tr_b16 v[114:115],v2 offset:5152
ds_read_b64_tr_b16 v[118:119],v2 offset:15392
v_mfma_f32_16x16x32_bf16 v[192:195],v[68:71],a[144:147],v[192:195]
buffer_load_dwordx4 v27,s[48:51],s84 offen offset:3072 lds
v_mfma_f32_16x16x32_bf16 v[192:195],v[72:75],a[160:163],v[192:195]
s_add_u32 m0,s86,91136
ds_read_b64_tr_b16 v[122:123],v2 offset:25632
v_mfma_f32_16x16x32_bf16 v[192:195],v[76:79],a[176:179],v[192:195]
v_mfma_f32_16x16x32_bf16 v[200:203],v[76:79],a[180:183],v[200:203]
v_mfma_f32_16x16x32_bf16 v[200:203],v[72:75],a[164:167],v[200:203]
v_mfma_f32_16x16x32_bf16 v[200:203],v[68:71],a[148:151],v[200:203]
v_mfma_f32_16x16x32_bf16 v[200:203],v[64:67],a[132:135],v[200:203]
)A"
R"A(v_mfma_f32_16x16x32_bf16 v[208:211],v[64:67],a[136:139],v[208:211]
buffer_load_dwordx4 v7,s[48:51],s84 offen lds
v_mfma_f32_16x16x32_bf16 v[208:211],v[68:71],a[152:155],v[208:211]
s_add_u32 m0,s86,40960
s_add_u32 s84,s83,20480
v_mfma_f32_16x16x32_bf16 v[208:211],v[72:75],a[168:171],v[208:211]
v_mfma_f32_16x16x32_bf16 v[208:211],v[76:79],a[184:187],v[208:211]
v_mfma_f32_16x16x32_bf16 v[216:219],v[76:79],a[188:191],v[216:219]
ds_read_b64_tr_b16 v[76:77],v1 offset:30784
ds_read_b64_tr_b16 v[78:79],v1 offset:35904
v_mfma_f32_16x16x32_bf16 v[216:219],v[72:75],a[172:175],v[216:219]
buffer_load_dwordx2 v[4:5],v3,s[44:47],s91 offen offset:1280 sc1
v_mfma_f32_16x16x32_bf16 v[216:219],v[68:71],a[156:159],v[216:219]
ds_read_b64_tr_b16 v[68:69],v1 offset:10304
ds_read_b64_tr_b16 v[70:71],v1 offset:15424
v_mfma_f32_16x16x32_bf16 v[216:219],v[64:67],a[140:143],v[216:219]
ds_read_b64_tr_b16 v[64:65],v1 offset:64
v_mfma_f32_16x16x32_bf16 v[224:227],v[80:83],a[128:131],v[224:227]
ds_read_b64_tr_b16 v[66:67],v1 offset:5184
v_mfma_f32_16x16x32_bf16 v[224:227],v[84:87],a[144:147],v[224:227]
ds_read_b64_tr_b16 v[72:73],v1 offset:20544
v_mfma_f32_16x16x32_bf16 v[224:227],v[88:91],a[160:163],v[224:227]
buffer_load_dwordx4 v[32:35],v6,s[28:31],s87 offen sc1
v_mfma_f32_16x16x32_bf16 v[224:227],v[92:95],a[176:179],v[224:227]
ds_read_b64_tr_b16 v[74:75],v1 offset:25664
v_mfma_f32_16x16x32_bf16 v[232:235],v[92:95],a[180:183],v[232:235]
v_mfma_f32_16x16x32_bf16 v[232:235],v[88:91],a[164:167],v[232:235]
v_mfma_f32_16x16x32_bf16 v[232:235],v[84:87],a[148:151],v[232:235]
v_mfma_f32_16x16x32_bf16 v[232:235],v[80:83],a[132:135],v[232:235]
buffer_load_dwordx4 v[36:39],v6,s[28:31],s88 offen sc1
v_mfma_f32_16x16x32_bf16 v[240:243],v[80:83],a[136:139],v[240:243]
v_mfma_f32_16x16x32_bf16 v[240:243],v[84:87],a[152:155],v[240:243]
v_mfma_f32_16x16x32_bf16 v[240:243],v[88:91],a[168:171],v[240:243]
v_mfma_f32_16x16x32_bf16 v[240:243],v[92:95],a[184:187],v[240:243]
v_mfma_f32_16x16x32_bf16 v[248:251],v[92:95],a[188:191],v[248:251]
buffer_load_dwordx4 v[40:43],v6,s[28:31],s89 offen sc1
v_mfma_f32_16x16x32_bf16 v[248:251],v[88:91],a[172:175],v[248:251]
ds_read_b64_tr_b16 v[88:89],v2 offset:20576
ds_read_b64_tr_b16 v[90:91],v2 offset:25696
v_mfma_f32_16x16x32_bf16 v[248:251],v[84:87],a[156:159],v[248:251]
ds_read_b64_tr_b16 v[84:85],v2 offset:10336
v_mfma_f32_16x16x32_bf16 v[248:251],v[80:83],a[140:143],v[248:251]
s_waitcnt lgkmcnt(3)
ds_read_b64_tr_b16 v[80:81],v2 offset:96
v_mfma_f32_16x16x32_bf16 a[192:195],v[96:99],a[68:71],a[192:195]
ds_read_b64_tr_b16 v[82:83],v2 offset:5216
v_mfma_f32_16x16x32_bf16 a[192:195],v[100:103],a[76:79],a[192:195]
buffer_load_dwordx4 v[196:199],v6,s[28:31],s87 offen offset:2048 sc1
v_mfma_f32_16x16x32_bf16 a[192:195],v[104:107],a[84:87],a[192:195]
ds_read_b64_tr_b16 v[86:87],v2 offset:15456
ds_read_b64_tr_b16 v[92:93],v2 offset:30816
v_mfma_f32_16x16x32_bf16 a[192:195],v[108:111],a[92:95],a[192:195]
ds_read_b64_tr_b16 v[94:95],v2 offset:35936
v_mfma_f32_16x16x32_bf16 a[196:199],v[108:111],a[124:127],a[196:199]
v_mfma_f32_16x16x32_bf16 a[196:199],v[104:107],a[116:119],a[196:199]
v_mfma_f32_16x16x32_bf16 a[196:199],v[100:103],a[108:111],a[196:199]
buffer_load_dwordx4 v[44:47],v6,s[28:31],s90 offen sc1
v_mfma_f32_16x16x32_bf16 a[196:199],v[96:99],a[100:103],a[196:199]
v_mfma_f32_16x16x32_bf16 a[200:203],v[96:99],a[64:67],a[200:203]
v_mfma_f32_16x16x32_bf16 a[200:203],v[100:103],a[72:75],a[200:203]
s_waitcnt vmcnt(31)
v_mfma_f32_16x16x32_bf16 a[200:203],v[104:107],a[80:83],a[200:203]
s_waitcnt vmcnt(27)
v_mfma_f32_16x16x32_bf16 a[200:203],v[108:111],a[88:91],a[200:203]
buffer_load_dwordx4 v[212:215],v6,s[28:31],s88 offen offset:2048 sc1
v_mfma_f32_16x16x32_bf16 a[204:207],v[108:111],a[120:123],a[204:207]
s_waitcnt vmcnt(26)
ds_read_b64_tr_b16 v[108:109],v1 offset:30848
ds_read_b64_tr_b16 v[110:111],v1 offset:35968
v_mfma_f32_16x16x32_bf16 a[204:207],v[104:107],a[112:115],a[204:207]
s_waitcnt vmcnt(24)
ds_read_b64_tr_b16 v[104:105],v1 offset:20608
v_mfma_f32_16x16x32_bf16 a[204:207],v[100:103],a[104:107],a[204:207]
s_waitcnt vmcnt(21)
ds_read_b64_tr_b16 v[100:101],v1 offset:10368
v_mfma_f32_16x16x32_bf16 a[204:207],v[96:99],a[96:99],a[204:207]
ds_read_b64_tr_b16 v[96:97],v1 offset:128
v_mfma_f32_16x16x32_bf16 a[208:211],v[112:115],a[68:71],a[208:211]
buffer_load_dwordx4 v[180:183],v6,s[28:31],s90 offen offset:1024 sc1
v_mfma_f32_16x16x32_bf16 a[208:211],v[116:119],a[76:79],a[208:211]
ds_read_b64_tr_b16 v[98:99],v1 offset:5248
ds_read_b64_tr_b16 v[102:103],v1 offset:15488
v_mfma_f32_16x16x32_bf16 a[208:211],v[120:123],a[84:87],a[208:211]
ds_read_b64_tr_b16 v[106:107],v1 offset:25728
v_mfma_f32_16x16x32_bf16 a[208:211],v[124:127],a[92:95],a[208:211]
v_mfma_f32_16x16x32_bf16 a[212:215],v[124:127],a[124:127],a[212:215]
v_mfma_f32_16x16x32_bf16 a[212:215],v[120:123],a[116:119],a[212:215]
buffer_load_dwordx4 v[228:231],v6,s[28:31],s89 offen offset:2048 sc1
v_mfma_f32_16x16x32_bf16 a[212:215],v[116:119],a[108:111],a[212:215]
v_mfma_f32_16x16x32_bf16 a[212:215],v[112:115],a[100:103],a[212:215]
v_mfma_f32_16x16x32_bf16 a[216:219],v[112:115],a[64:67],a[216:219]
v_mfma_f32_16x16x32_bf16 a[216:219],v[116:119],a[72:75],a[216:219]
v_mfma_f32_16x16x32_bf16 a[216:219],v[120:123],a[80:83],a[216:219]
buffer_load_dwordx4 v[164:167],v6,s[28:31],s89 offen offset:1024 sc1
v_mfma_f32_16x16x32_bf16 a[216:219],v[124:127],a[88:91],a[216:219]
v_mfma_f32_16x16x32_bf16 a[220:223],v[124:127],a[120:123],a[220:223]
ds_read_b64_tr_b16 v[124:125],v2 offset:30880
ds_read_b64_tr_b16 v[126:127],v2 offset:36000
v_mfma_f32_16x16x32_bf16 a[220:223],v[120:123],a[112:115],a[220:223]
ds_read_b64_tr_b16 v[120:121],v2 offset:20640
v_mfma_f32_16x16x32_bf16 a[220:223],v[116:119],a[104:107],a[220:223]
ds_read_b64_tr_b16 v[116:117],v2 offset:10400
v_mfma_f32_16x16x32_bf16 a[220:223],v[112:115],a[96:99],a[220:223]
buffer_load_dwordx4 v[148:151],v6,s[28:31],s88 offen offset:1024 sc1
v_mfma_f32_16x16x32_bf16 a[224:227],v[64:67],a[68:71],a[224:227]
ds_read_b64_tr_b16 v[112:113],v2 offset:160
ds_read_b64_tr_b16 v[114:115],v2 offset:5280
v_mfma_f32_16x16x32_bf16 a[224:227],v[68:71],a[76:79],a[224:227]
ds_read_b64_tr_b16 v[118:119],v2 offset:15520
v_mfma_f32_16x16x32_bf16 a[224:227],v[72:75],a[84:87],a[224:227]
ds_read_b64_tr_b16 v[122:123],v2 offset:25760
v_mfma_f32_16x16x32_bf16 a[224:227],v[76:79],a[92:95],a[224:227]
v_mfma_f32_16x16x32_bf16 a[228:231],v[76:79],a[124:127],a[228:231]
buffer_load_dwordx4 v[244:247],v6,s[28:31],s90 offen offset:2048 sc1
v_mfma_f32_16x16x32_bf16 a[228:231],v[72:75],a[116:119],a[228:231]
v_mfma_f32_16x16x32_bf16 a[228:231],v[68:71],a[108:111],a[228:231]
v_mfma_f32_16x16x32_bf16 a[228:231],v[64:67],a[100:103],a[228:231]
v_mfma_f32_16x16x32_bf16 a[232:235],v[64:67],a[64:67],a[232:235]
v_mfma_f32_16x16x32_bf16 a[232:235],v[68:71],a[72:75],a[232:235]
buffer_load_dwordx4 v[188:191],v28,s[28:31],s90 offen offset:3072 sc1
v_mfma_f32_16x16x32_bf16 a[232:235],v[72:75],a[80:83],a[232:235]
s_waitcnt vmcnt(12)
v_cmp_ne_u32 s[56:57],s52,v4
v_cmp_ne_u32 s[58:59],s53,v5
v_mfma_f32_16x16x32_bf16 a[232:235],v[76:79],a[88:91],a[232:235]
s_or_b64 s[56:57],s[56:57],s[58:59]
s_cbranch_scc1 L_fr7_%=
L_fo7_%=:
s_lshl_b32 s91,s64,6
v_mfma_f32_16x16x32_bf16 a[236:239],v[76:79],a[120:123],a[236:239]
ds_read_b64_tr_b16 v[76:77],v1 offset:30912
ds_read_b64_tr_b16 v[78:79],v1 offset:36032
v_mfma_f32_16x16x32_bf16 a[236:239],v[72:75],a[112:115],a[236:239]
ds_read_b64_tr_b16 v[72:73],v1 offset:20672
v_bfe_u32 v3,v6,4,2
v_mfma_f32_16x16x32_bf16 a[236:239],v[68:71],a[104:107],a[236:239]
buffer_load_dwordx4 a[128:131],v6,s[28:31],s20 offen sc1
v_mfma_f32_16x16x32_bf16 a[236:239],v[64:67],a[96:99],a[236:239]
s_waitcnt lgkmcnt(3)
ds_read_b64_tr_b16 v[64:65],v1 offset:192
ds_read_b64_tr_b16 v[66:67],v1 offset:5312
v_mfma_f32_16x16x32_bf16 a[240:243],v[80:83],a[68:71],a[240:243]
ds_read_b64_tr_b16 v[68:69],v1 offset:10432
v_min_u32 v3,2,v3
v_mfma_f32_16x16x32_bf16 a[240:243],v[84:87],a[76:79],a[240:243]
ds_read_b64_tr_b16 v[70:71],v1 offset:15552
v_add3_u32 v3,v3,s63,1
v_mfma_f32_16x16x32_bf16 a[240:243],v[88:91],a[84:87],a[240:243]
ds_read_b64_tr_b16 v[74:75],v1 offset:25792
v_and_b32 v3,3,v3
v_mfma_f32_16x16x32_bf16 a[240:243],v[92:95],a[92:95],a[240:243]
buffer_load_dwordx4 v[132:135],v6,s[28:31],s87 offen offset:1024 sc1
v_mfma_f32_16x16x32_bf16 a[244:247],v[92:95],a[124:127],a[244:247]
v_lshlrev_b32 v3,8,v3
v_mfma_f32_16x16x32_bf16 a[244:247],v[88:91],a[116:119],a[244:247]
v_mfma_f32_16x16x32_bf16 a[244:247],v[84:87],a[108:111],a[244:247]
v_mfma_f32_16x16x32_bf16 a[244:247],v[80:83],a[100:103],a[244:247]
v_mfma_f32_16x16x32_bf16 a[248:251],v[80:83],a[64:67],a[248:251]
buffer_load_dwordx4 v[172:175],v28,s[28:31],s89 offen offset:3072 sc1
v_mfma_f32_16x16x32_bf16 a[248:251],v[84:87],a[72:75],a[248:251]
v_mfma_f32_16x16x32_bf16 a[248:251],v[88:91],a[80:83],a[248:251]
v_mfma_f32_16x16x32_bf16 a[248:251],v[92:95],a[88:91],a[248:251]
v_mfma_f32_16x16x32_bf16 a[252:255],v[92:95],a[120:123],a[252:255]
ds_read_b64_tr_b16 v[92:93],v2 offset:30944
ds_read_b64_tr_b16 v[94:95],v2 offset:36064
v_mfma_f32_16x16x32_bf16 a[252:255],v[88:91],a[112:115],a[252:255]
buffer_load_dwordx4 a[144:147],v6,s[28:31],s21 offen sc1
v_mfma_f32_16x16x32_bf16 a[252:255],v[84:87],a[104:107],a[252:255]
ds_read_b64_tr_b16 v[84:85],v2 offset:10464
ds_read_b64_tr_b16 v[86:87],v2 offset:15584
v_mfma_f32_16x16x32_bf16 a[252:255],v[80:83],a[96:99],a[252:255]
ds_read_b64_tr_b16 v[80:81],v2 offset:224
v_mfma_f32_16x16x32_bf16 a[0:3],v[96:99],a[68:71],a[0:3]
ds_read_b64_tr_b16 v[82:83],v2 offset:5344
v_mfma_f32_16x16x32_bf16 a[0:3],v[100:103],a[76:79],a[0:3]
ds_read_b64_tr_b16 v[88:89],v2 offset:20704
v_mfma_f32_16x16x32_bf16 a[0:3],v[104:107],a[84:87],a[0:3]
buffer_load_dwordx4 v[156:159],v28,s[28:31],s88 offen offset:3072 sc1
v_mfma_f32_16x16x32_bf16 a[0:3],v[108:111],a[92:95],a[0:3]
ds_read_b64_tr_b16 v[90:91],v2 offset:25824
v_mfma_f32_16x16x32_bf16 a[4:7],v[108:111],a[124:127],a[4:7]
v_mfma_f32_16x16x32_bf16 a[4:7],v[104:107],a[116:119],a[4:7]
v_mfma_f32_16x16x32_bf16 a[4:7],v[100:103],a[108:111],a[4:7]
v_mfma_f32_16x16x32_bf16 a[4:7],v[96:99],a[100:103],a[4:7]
buffer_load_dwordx4 a[160:163],v6,s[28:31],s22 offen sc1
v_mfma_f32_16x16x32_bf16 a[8:11],v[96:99],a[64:67],a[8:11]
v_mfma_f32_16x16x32_bf16 a[8:11],v[100:103],a[72:75],a[8:11]
v_mfma_f32_16x16x32_bf16 a[8:11],v[104:107],a[80:83],a[8:11]
v_mfma_f32_16x16x32_bf16 a[8:11],v[108:111],a[88:91],a[8:11]
v_mfma_f32_16x16x32_bf16 a[12:15],v[108:111],a[120:123],a[12:15]
buffer_load_dwordx4 v[140:143],v28,s[28:31],s87 offen offset:3072 sc1
v_mfma_f32_16x16x32_bf16 a[12:15],v[104:107],a[112:115],a[12:15]
s_add_u32 s87,s64,2
ds_read_b64_tr_b16 v[104:105],v1 offset:20736
ds_read_b64_tr_b16 v[106:107],v1 offset:25856
v_mfma_f32_16x16x32_bf16 a[12:15],v[100:103],a[104:107],a[12:15]
s_and_b32 s87,s87,3
s_lshl_b32 s87,s87,1
ds_read_b64_tr_b16 v[100:101],v1 offset:10496
v_mfma_f32_16x16x32_bf16 a[12:15],v[96:99],a[96:99],a[12:15]
s_or_b32 s87,s87,1
s_lshl_b32 s87,s87,16
ds_read_b64_tr_b16 v[96:97],v1 offset:256
v_mfma_f32_16x16x32_bf16 a[16:19],v[112:115],a[68:71],a[16:19]
s_add_u32 s89,s87,8192
s_add_u32 s90,s87,12288
ds_read_b64_tr_b16 v[98:99],v1 offset:5376
v_mfma_f32_16x16x32_bf16 a[16:19],v[116:119],a[76:79],a[16:19]
buffer_load_dwordx4 a[176:179],v6,s[28:31],s23 offen sc1
v_mfma_f32_16x16x32_bf16 a[16:19],v[120:123],a[84:87],a[16:19]
s_add_u32 s88,s87,4096
ds_read_b64_tr_b16 v[102:103],v1 offset:15616
ds_read_b64_tr_b16 v[108:109],v1 offset:30976
v_mfma_f32_16x16x32_bf16 a[16:19],v[124:127],a[92:95],a[16:19]
ds_read_b64_tr_b16 v[110:111],v1 offset:36096
v_add_u32 v1,0x1e000,v1
v_mfma_f32_16x16x32_bf16 a[20:23],v[124:127],a[124:127],a[20:23]
v_mfma_f32_16x16x32_bf16 a[20:23],v[120:123],a[116:119],a[20:23]
v_mfma_f32_16x16x32_bf16 a[20:23],v[116:119],a[108:111],a[20:23]
v_mfma_f32_16x16x32_bf16 a[20:23],v[112:115],a[100:103],a[20:23]
v_mfma_f32_16x16x32_bf16 a[24:27],v[112:115],a[64:67],a[24:27]
buffer_load_dwordx4 v24,s[48:51],s83 offen lds
)A"
R"A(v_mfma_f32_16x16x32_bf16 a[24:27],v[116:119],a[72:75],a[24:27]
v_mfma_f32_16x16x32_bf16 a[24:27],v[120:123],a[80:83],a[24:27]
v_mfma_f32_16x16x32_bf16 a[24:27],v[124:127],a[88:91],a[24:27]
v_mfma_f32_16x16x32_bf16 a[28:31],v[124:127],a[120:123],a[28:31]
ds_read_b64_tr_b16 v[124:125],v2 offset:31008
ds_read_b64_tr_b16 v[126:127],v2 offset:36128
v_mfma_f32_16x16x32_bf16 a[28:31],v[120:123],a[112:115],a[28:31]
buffer_load_dwordx4 a[180:183],v6,s[28:31],s23 offen offset:1024 sc1
v_mfma_f32_16x16x32_bf16 a[28:31],v[116:119],a[104:107],a[28:31]
ds_read_b64_tr_b16 v[116:117],v2 offset:10528
ds_read_b64_tr_b16 v[118:119],v2 offset:15648
v_mfma_f32_16x16x32_bf16 a[28:31],v[112:115],a[96:99],a[28:31]
s_waitcnt lgkmcnt(4)
ds_read_b64_tr_b16 v[112:113],v2 offset:288
v_mfma_f32_16x16x32_bf16 v[128:131],v[64:67],a[68:71],v[128:131]
ds_read_b64_tr_b16 v[114:115],v2 offset:5408
v_mfma_f32_16x16x32_bf16 v[128:131],v[68:71],a[76:79],v[128:131]
ds_read_b64_tr_b16 v[120:121],v2 offset:20768
v_mfma_f32_16x16x32_bf16 v[128:131],v[72:75],a[84:87],v[128:131]
ds_read_b64_tr_b16 v[122:123],v2 offset:25888
v_add_u32 v2,0x1e000,v2
v_mfma_f32_16x16x32_bf16 v[128:131],v[76:79],a[92:95],v[128:131]
v_mfma_f32_16x16x32_bf16 v[136:139],v[76:79],a[124:127],v[136:139]
buffer_load_dwordx4 v25,s[48:51],s83 offen offset:1024 lds
v_mfma_f32_16x16x32_bf16 v[136:139],v[72:75],a[116:119],v[136:139]
v_mfma_f32_16x16x32_bf16 v[136:139],v[68:71],a[108:111],v[136:139]
v_mfma_f32_16x16x32_bf16 v[136:139],v[64:67],a[100:103],v[136:139]
v_mfma_f32_16x16x32_bf16 v[144:147],v[64:67],a[64:67],v[144:147]
v_mfma_f32_16x16x32_bf16 v[144:147],v[68:71],a[72:75],v[144:147]
buffer_load_dwordx4 a[164:167],v6,s[28:31],s22 offen offset:1024 sc1
v_mfma_f32_16x16x32_bf16 v[144:147],v[72:75],a[80:83],v[144:147]
v_mfma_f32_16x16x32_bf16 v[144:147],v[76:79],a[88:91],v[144:147]
v_mfma_f32_16x16x32_bf16 v[152:155],v[76:79],a[120:123],v[152:155]
v_mfma_f32_16x16x32_bf16 v[152:155],v[72:75],a[112:115],v[152:155]
v_mfma_f32_16x16x32_bf16 v[152:155],v[68:71],a[104:107],v[152:155]
v_mfma_f32_16x16x32_bf16 v[152:155],v[64:67],a[96:99],v[152:155]
s_barrier
v_mfma_f32_16x16x32_bf16 v[160:163],v[80:83],a[68:71],v[160:163]
buffer_load_dwordx4 v26,s[48:51],s83 offen offset:2048 lds
v_mfma_f32_16x16x32_bf16 v[160:163],v[84:87],a[76:79],v[160:163]
ds_read_b64_tr_b16 v[64:65],v1
ds_read_b64_tr_b16 v[66:67],v1 offset:5120
v_mfma_f32_16x16x32_bf16 v[160:163],v[88:91],a[84:87],v[160:163]
ds_read_b64_tr_b16 v[68:69],v1 offset:10240
v_mfma_f32_16x16x32_bf16 v[160:163],v[92:95],a[92:95],v[160:163]
ds_read_b64_tr_b16 v[70:71],v1 offset:15360
v_mfma_f32_16x16x32_bf16 v[168:171],v[92:95],a[124:127],v[168:171]
ds_read_b64_tr_b16 v[72:73],v1 offset:20480
v_mfma_f32_16x16x32_bf16 v[168:171],v[88:91],a[116:119],v[168:171]
buffer_load_dwordx4 a[148:151],v6,s[28:31],s21 offen offset:1024 sc1
v_mfma_f32_16x16x32_bf16 v[168:171],v[84:87],a[108:111],v[168:171]
ds_read_b64_tr_b16 v[74:75],v1 offset:25600
ds_read_b64_tr_b16 v[76:77],v1 offset:30720
v_mfma_f32_16x16x32_bf16 v[168:171],v[80:83],a[100:103],v[168:171]
ds_read_b64_tr_b16 v[78:79],v1 offset:35840
v_mfma_f32_16x16x32_bf16 v[176:179],v[80:83],a[64:67],v[176:179]
v_mfma_f32_16x16x32_bf16 v[176:179],v[84:87],a[72:75],v[176:179]
v_mfma_f32_16x16x32_bf16 v[176:179],v[88:91],a[80:83],v[176:179]
v_mfma_f32_16x16x32_bf16 v[176:179],v[92:95],a[88:91],v[176:179]
v_mfma_f32_16x16x32_bf16 v[184:187],v[92:95],a[120:123],v[184:187]
buffer_load_dwordx4 v27,s[48:51],s83 offen offset:3072 lds
v_mfma_f32_16x16x32_bf16 v[184:187],v[88:91],a[112:115],v[184:187]
s_add_u32 m0,s86,45056
ds_read_b64_tr_b16 v[88:89],v2 offset:20512
ds_read_b64_tr_b16 v[90:91],v2 offset:25632
v_mfma_f32_16x16x32_bf16 v[184:187],v[84:87],a[104:107],v[184:187]
ds_read_b64_tr_b16 v[84:85],v2 offset:10272
v_mfma_f32_16x16x32_bf16 v[184:187],v[80:83],a[96:99],v[184:187]
ds_read_b64_tr_b16 v[80:81],v2 offset:32
v_mfma_f32_16x16x32_bf16 v[192:195],v[96:99],a[68:71],v[192:195]
ds_read_b64_tr_b16 v[82:83],v2 offset:5152
v_mfma_f32_16x16x32_bf16 v[192:195],v[100:103],a[76:79],v[192:195]
buffer_load_dwordx4 a[132:135],v6,s[28:31],s20 offen offset:1024 sc1
v_mfma_f32_16x16x32_bf16 v[192:195],v[104:107],a[84:87],v[192:195]
ds_read_b64_tr_b16 v[86:87],v2 offset:15392
ds_read_b64_tr_b16 v[92:93],v2 offset:30752
v_mfma_f32_16x16x32_bf16 v[192:195],v[108:111],a[92:95],v[192:195]
ds_read_b64_tr_b16 v[94:95],v2 offset:35872
v_mfma_f32_16x16x32_bf16 v[200:203],v[108:111],a[124:127],v[200:203]
v_mfma_f32_16x16x32_bf16 v[200:203],v[104:107],a[116:119],v[200:203]
v_mfma_f32_16x16x32_bf16 v[200:203],v[100:103],a[108:111],v[200:203]
v_mfma_f32_16x16x32_bf16 v[200:203],v[96:99],a[100:103],v[200:203]
v_mfma_f32_16x16x32_bf16 v[208:211],v[96:99],a[64:67],v[208:211]
buffer_load_dwordx4 v7,s[48:51],s83 offen lds
v_mfma_f32_16x16x32_bf16 v[208:211],v[100:103],a[72:75],v[208:211]
s_add_u32 m0,s86,46080
s_mul_i32 s83,s63,20480
v_mfma_f32_16x16x32_bf16 v[208:211],v[104:107],a[80:83],v[208:211]
v_mfma_f32_16x16x32_bf16 v[208:211],v[108:111],a[88:91],v[208:211]
v_mfma_f32_16x16x32_bf16 v[216:219],v[108:111],a[120:123],v[216:219]
ds_read_b64_tr_b16 v[108:109],v1 offset:30784
ds_read_b64_tr_b16 v[110:111],v1 offset:35904
v_mfma_f32_16x16x32_bf16 v[216:219],v[104:107],a[112:115],v[216:219]
buffer_load_dwordx4 a[136:139],v6,s[28:31],s20 offen offset:2048 sc1
v_mfma_f32_16x16x32_bf16 v[216:219],v[100:103],a[104:107],v[216:219]
ds_read_b64_tr_b16 v[100:101],v1 offset:10304
ds_read_b64_tr_b16 v[102:103],v1 offset:15424
v_mfma_f32_16x16x32_bf16 v[216:219],v[96:99],a[96:99],v[216:219]
s_waitcnt lgkmcnt(4)
ds_read_b64_tr_b16 v[96:97],v1 offset:64
v_mfma_f32_16x16x32_bf16 v[224:227],v[112:115],a[68:71],v[224:227]
ds_read_b64_tr_b16 v[98:99],v1 offset:5184
v_mfma_f32_16x16x32_bf16 v[224:227],v[116:119],a[76:79],v[224:227]
ds_read_b64_tr_b16 v[104:105],v1 offset:20544
v_mfma_f32_16x16x32_bf16 v[224:227],v[120:123],a[84:87],v[224:227]
ds_read_b64_tr_b16 v[106:107],v1 offset:25664
v_mfma_f32_16x16x32_bf16 v[224:227],v[124:127],a[92:95],v[224:227]
v_mfma_f32_16x16x32_bf16 v[232:235],v[124:127],a[124:127],v[232:235]
buffer_load_dwordx4 v24,s[48:51],s84 offen lds
v_mfma_f32_16x16x32_bf16 v[232:235],v[120:123],a[116:119],v[232:235]
v_mfma_f32_16x16x32_bf16 v[232:235],v[116:119],a[108:111],v[232:235]
v_mfma_f32_16x16x32_bf16 v[232:235],v[112:115],a[100:103],v[232:235]
v_mfma_f32_16x16x32_bf16 v[240:243],v[112:115],a[64:67],v[240:243]
v_mfma_f32_16x16x32_bf16 v[240:243],v[116:119],a[72:75],v[240:243]
buffer_load_dwordx4 a[152:155],v6,s[28:31],s21 offen offset:2048 sc1
v_mfma_f32_16x16x32_bf16 v[240:243],v[120:123],a[80:83],v[240:243]
v_mfma_f32_16x16x32_bf16 v[240:243],v[124:127],a[88:91],v[240:243]
v_mfma_f32_16x16x32_bf16 v[248:251],v[124:127],a[120:123],v[248:251]
ds_read_b64_tr_b16 v[124:125],v2 offset:30816
ds_read_b64_tr_b16 v[126:127],v2 offset:35936
v_mfma_f32_16x16x32_bf16 v[248:251],v[120:123],a[112:115],v[248:251]
ds_read_b64_tr_b16 v[120:121],v2 offset:20576
v_mfma_f32_16x16x32_bf16 v[248:251],v[116:119],a[104:107],v[248:251]
ds_read_b64_tr_b16 v[116:117],v2 offset:10336
v_mfma_f32_16x16x32_bf16 v[248:251],v[112:115],a[96:99],v[248:251]
s_waitcnt vmcnt(19)
ds_read_b64_tr_b16 v[112:113],v2 offset:96
v_mfma_f32_16x16x32_bf16 a[192:195],v[64:67],v[32:35],a[192:195]
buffer_load_dwordx4 v25,s[48:51],s84 offen offset:1024 lds
v_mfma_f32_16x16x32_bf16 a[192:195],v[68:71],v[36:39],a[192:195]
ds_read_b64_tr_b16 v[114:115],v2 offset:5216
ds_read_b64_tr_b16 v[118:119],v2 offset:15456
v_mfma_f32_16x16x32_bf16 a[192:195],v[72:75],v[40:43],a[192:195]
ds_read_b64_tr_b16 v[122:123],v2 offset:25696
v_mfma_f32_16x16x32_bf16 a[192:195],v[76:79],v[44:47],a[192:195]
v_mfma_f32_16x16x32_bf16 a[196:199],v[76:79],v[180:183],a[196:199]
v_mfma_f32_16x16x32_bf16 a[196:199],v[72:75],v[164:167],a[196:199]
buffer_load_dwordx4 a[168:171],v6,s[28:31],s22 offen offset:2048 sc1
v_mfma_f32_16x16x32_bf16 a[196:199],v[68:71],v[148:151],a[196:199]
s_waitcnt vmcnt(19)
v_mfma_f32_16x16x32_bf16 a[196:199],v[64:67],v[132:135],a[196:199]
v_mfma_f32_16x16x32_bf16 a[200:203],v[64:67],v[196:199],a[200:203]
v_mfma_f32_16x16x32_bf16 a[200:203],v[68:71],v[212:215],a[200:203]
v_mfma_f32_16x16x32_bf16 a[200:203],v[72:75],v[228:231],a[200:203]
v_mfma_f32_16x16x32_bf16 a[200:203],v[76:79],v[244:247],a[200:203]
v_mfma_f32_16x16x32_bf16 a[204:207],v[76:79],v[188:191],a[204:207]
buffer_load_dwordx4 v26,s[48:51],s84 offen offset:2048 lds
v_mfma_f32_16x16x32_bf16 a[204:207],v[72:75],v[172:175],a[204:207]
s_waitcnt vmcnt(18)
ds_read_b64_tr_b16 v[72:73],v1 offset:20608
ds_read_b64_tr_b16 v[74:75],v1 offset:25728
v_mfma_f32_16x16x32_bf16 a[204:207],v[68:71],v[156:159],a[204:207]
s_waitcnt vmcnt(16)
ds_read_b64_tr_b16 v[68:69],v1 offset:10368
v_mfma_f32_16x16x32_bf16 a[204:207],v[64:67],v[140:143],a[204:207]
ds_read_b64_tr_b16 v[64:65],v1 offset:128
v_mfma_f32_16x16x32_bf16 a[208:211],v[80:83],v[32:35],a[208:211]
ds_read_b64_tr_b16 v[66:67],v1 offset:5248
v_mfma_f32_16x16x32_bf16 a[208:211],v[84:87],v[36:39],a[208:211]
buffer_load_dwordx4 a[184:187],v6,s[28:31],s23 offen offset:2048 sc1
v_mfma_f32_16x16x32_bf16 a[208:211],v[88:91],v[40:43],a[208:211]
ds_read_b64_tr_b16 v[70:71],v1 offset:15488
ds_read_b64_tr_b16 v[76:77],v1 offset:30848
v_mfma_f32_16x16x32_bf16 a[208:211],v[92:95],v[44:47],a[208:211]
ds_read_b64_tr_b16 v[78:79],v1 offset:35968
v_mfma_f32_16x16x32_bf16 a[212:215],v[92:95],v[180:183],a[212:215]
v_mfma_f32_16x16x32_bf16 a[212:215],v[88:91],v[164:167],a[212:215]
v_mfma_f32_16x16x32_bf16 a[212:215],v[84:87],v[148:151],a[212:215]
v_mfma_f32_16x16x32_bf16 a[212:215],v[80:83],v[132:135],a[212:215]
v_mfma_f32_16x16x32_bf16 a[216:219],v[80:83],v[196:199],a[216:219]
buffer_load_dwordx4 v27,s[48:51],s84 offen offset:3072 lds
v_mfma_f32_16x16x32_bf16 a[216:219],v[84:87],v[212:215],a[216:219]
s_add_u32 m0,s86,50176
v_mfma_f32_16x16x32_bf16 a[216:219],v[88:91],v[228:231],a[216:219]
v_mfma_f32_16x16x32_bf16 a[216:219],v[92:95],v[244:247],a[216:219]
v_mfma_f32_16x16x32_bf16 a[220:223],v[92:95],v[188:191],a[220:223]
ds_read_b64_tr_b16 v[92:93],v2 offset:30880
ds_read_b64_tr_b16 v[94:95],v2 offset:36000
v_mfma_f32_16x16x32_bf16 a[220:223],v[88:91],v[172:175],a[220:223]
buffer_load_dwordx4 a[188:191],v28,s[28:31],s23 offen offset:3072 sc1
v_mfma_f32_16x16x32_bf16 a[220:223],v[84:87],v[156:159],a[220:223]
ds_read_b64_tr_b16 v[84:85],v2 offset:10400
ds_read_b64_tr_b16 v[86:87],v2 offset:15520
v_mfma_f32_16x16x32_bf16 a[220:223],v[80:83],v[140:143],a[220:223]
s_waitcnt lgkmcnt(4)
ds_read_b64_tr_b16 v[80:81],v2 offset:160
v_mfma_f32_16x16x32_bf16 a[224:227],v[96:99],v[32:35],a[224:227]
ds_read_b64_tr_b16 v[82:83],v2 offset:5280
v_mfma_f32_16x16x32_bf16 a[224:227],v[100:103],v[36:39],a[224:227]
ds_read_b64_tr_b16 v[88:89],v2 offset:20640
v_mfma_f32_16x16x32_bf16 a[224:227],v[104:107],v[40:43],a[224:227]
ds_read_b64_tr_b16 v[90:91],v2 offset:25760
v_mfma_f32_16x16x32_bf16 a[224:227],v[108:111],v[44:47],a[224:227]
v_mfma_f32_16x16x32_bf16 a[228:231],v[108:111],v[180:183],a[228:231]
buffer_load_dwordx4 v7,s[48:51],s84 offen lds
v_mfma_f32_16x16x32_bf16 a[228:231],v[104:107],v[164:167],a[228:231]
v_mfma_f32_16x16x32_bf16 a[228:231],v[100:103],v[148:151],a[228:231]
v_mfma_f32_16x16x32_bf16 a[228:231],v[96:99],v[132:135],a[228:231]
v_mfma_f32_16x16x32_bf16 a[232:235],v[96:99],v[196:199],a[232:235]
v_mfma_f32_16x16x32_bf16 a[232:235],v[100:103],v[212:215],a[232:235]
buffer_load_dwordx4 a[172:175],v28,s[28:31],s22 offen offset:3072 sc1
v_mfma_f32_16x16x32_bf16 a[232:235],v[104:107],v[228:231],a[232:235]
v_mfma_f32_16x16x32_bf16 a[232:235],v[108:111],v[244:247],a[232:235]
v_mfma_f32_16x16x32_bf16 a[236:239],v[108:111],v[188:191],a[236:239]
ds_read_b64_tr_b16 v[108:109],v1 offset:30912
ds_read_b64_tr_b16 v[110:111],v1 offset:36032
v_mfma_f32_16x16x32_bf16 a[236:239],v[104:107],v[172:175],a[236:239]
ds_read_b64_tr_b16 v[104:105],v1 offset:20672
v_mfma_f32_16x16x32_bf16 a[236:239],v[100:103],v[156:159],a[236:239]
buffer_load_dwordx4 a[84:87],v6,s[28:31],s89 offen sc1
v_mfma_f32_16x16x32_bf16 a[236:239],v[96:99],v[140:143],a[236:239]
)A"
R"A(ds_read_b64_tr_b16 v[96:97],v1 offset:192
ds_read_b64_tr_b16 v[98:99],v1 offset:5312
v_mfma_f32_16x16x32_bf16 a[240:243],v[112:115],v[32:35],a[240:243]
ds_read_b64_tr_b16 v[100:101],v1 offset:10432
v_mfma_f32_16x16x32_bf16 a[240:243],v[116:119],v[36:39],a[240:243]
ds_read_b64_tr_b16 v[102:103],v1 offset:15552
v_mfma_f32_16x16x32_bf16 a[240:243],v[120:123],v[40:43],a[240:243]
ds_read_b64_tr_b16 v[106:107],v1 offset:25792
v_add_u32 v1,0xffff6000,v1
v_mfma_f32_16x16x32_bf16 a[240:243],v[124:127],v[44:47],a[240:243]
buffer_load_dwordx4 a[92:95],v6,s[28:31],s90 offen sc1
v_mfma_f32_16x16x32_bf16 a[244:247],v[124:127],v[180:183],a[244:247]
v_mfma_f32_16x16x32_bf16 a[244:247],v[120:123],v[164:167],a[244:247]
v_mfma_f32_16x16x32_bf16 a[244:247],v[116:119],v[148:151],a[244:247]
v_mfma_f32_16x16x32_bf16 a[244:247],v[112:115],v[132:135],a[244:247]
v_mfma_f32_16x16x32_bf16 a[248:251],v[112:115],v[196:199],a[248:251]
buffer_load_dwordx4 a[156:159],v28,s[28:31],s21 offen offset:3072 sc1
v_mfma_f32_16x16x32_bf16 a[248:251],v[116:119],v[212:215],a[248:251]
v_mfma_f32_16x16x32_bf16 a[248:251],v[120:123],v[228:231],a[248:251]
v_mfma_f32_16x16x32_bf16 a[248:251],v[124:127],v[244:247],a[248:251]
v_mfma_f32_16x16x32_bf16 a[252:255],v[124:127],v[188:191],a[252:255]
ds_read_b64_tr_b16 v[124:125],v2 offset:30944
ds_read_b64_tr_b16 v[126:127],v2 offset:36064
v_mfma_f32_16x16x32_bf16 a[252:255],v[120:123],v[172:175],a[252:255]
buffer_load_dwordx4 a[68:71],v6,s[28:31],s87 offen sc1
v_mfma_f32_16x16x32_bf16 a[252:255],v[116:119],v[156:159],a[252:255]
ds_read_b64_tr_b16 v[116:117],v2 offset:10464
ds_read_b64_tr_b16 v[118:119],v2 offset:15584
v_mfma_f32_16x16x32_bf16 a[252:255],v[112:115],v[140:143],a[252:255]
ds_read_b64_tr_b16 v[112:113],v2 offset:224
v_mfma_f32_16x16x32_bf16 a[0:3],v[64:67],v[32:35],a[0:3]
ds_read_b64_tr_b16 v[114:115],v2 offset:5344
v_mfma_f32_16x16x32_bf16 a[0:3],v[68:71],v[36:39],a[0:3]
ds_read_b64_tr_b16 v[120:121],v2 offset:20704
v_mfma_f32_16x16x32_bf16 a[0:3],v[72:75],v[40:43],a[0:3]
buffer_load_dwordx4 a[140:143],v28,s[28:31],s20 offen offset:3072 sc1
v_mfma_f32_16x16x32_bf16 a[0:3],v[76:79],v[44:47],a[0:3]
ds_read_b64_tr_b16 v[122:123],v2 offset:25824
v_add_u32 v2,0xffff6000,v2
v_mfma_f32_16x16x32_bf16 a[4:7],v[76:79],v[180:183],a[4:7]
v_mfma_f32_16x16x32_bf16 a[4:7],v[72:75],v[164:167],a[4:7]
v_mfma_f32_16x16x32_bf16 a[4:7],v[68:71],v[148:151],a[4:7]
v_mfma_f32_16x16x32_bf16 a[4:7],v[64:67],v[132:135],a[4:7]
buffer_load_dwordx4 a[124:127],v6,s[28:31],s90 offen offset:1024 sc1
v_mfma_f32_16x16x32_bf16 a[8:11],v[64:67],v[196:199],a[8:11]
v_mfma_f32_16x16x32_bf16 a[8:11],v[68:71],v[212:215],a[8:11]
v_mfma_f32_16x16x32_bf16 a[8:11],v[72:75],v[228:231],a[8:11]
v_mfma_f32_16x16x32_bf16 a[8:11],v[76:79],v[244:247],a[8:11]
v_mfma_f32_16x16x32_bf16 a[12:15],v[76:79],v[188:191],a[12:15]
buffer_load_dwordx4 a[116:119],v6,s[28:31],s89 offen offset:1024 sc1
v_mfma_f32_16x16x32_bf16 a[12:15],v[72:75],v[172:175],a[12:15]
ds_read_b64_tr_b16 v[72:73],v1 offset:20480
ds_read_b64_tr_b16 v[74:75],v1 offset:25600
v_mfma_f32_16x16x32_bf16 a[12:15],v[68:71],v[156:159],a[12:15]
ds_read_b64_tr_b16 v[68:69],v1 offset:10240
v_mfma_f32_16x16x32_bf16 a[12:15],v[64:67],v[140:143],a[12:15]
s_waitcnt lgkmcnt(3)
ds_read_b64_tr_b16 v[64:65],v1
v_mfma_f32_16x16x32_bf16 a[16:19],v[80:83],v[32:35],a[16:19]
ds_read_b64_tr_b16 v[66:67],v1 offset:5120
v_mfma_f32_16x16x32_bf16 a[16:19],v[84:87],v[36:39],a[16:19]
buffer_load_dwordx4 a[76:79],v6,s[28:31],s88 offen sc1
v_mfma_f32_16x16x32_bf16 a[16:19],v[88:91],v[40:43],a[16:19]
ds_read_b64_tr_b16 v[70:71],v1 offset:15360
s_nop 0
L_pd22_%=:
s_cbranch_execz L_trtg2S_%=
ds_read_b64_tr_b16 v[76:77],v1 offset:30720
v_mfma_f32_16x16x32_bf16 a[16:19],v[92:95],v[44:47],a[16:19]
ds_read_b64_tr_b16 v[78:79],v1 offset:35840
v_add_u32 v1,0xffff6000,v1
v_mfma_f32_16x16x32_bf16 a[20:23],v[92:95],v[180:183],a[20:23]
v_mfma_f32_16x16x32_bf16 a[20:23],v[88:91],v[164:167],a[20:23]
v_mfma_f32_16x16x32_bf16 a[20:23],v[84:87],v[148:151],a[20:23]
buffer_load_dwordx4 a[108:111],v6,s[28:31],s88 offen offset:1024 sc1
v_mfma_f32_16x16x32_bf16 a[20:23],v[80:83],v[132:135],a[20:23]
v_mfma_f32_16x16x32_bf16 a[24:27],v[80:83],v[196:199],a[24:27]
v_mfma_f32_16x16x32_bf16 a[24:27],v[84:87],v[212:215],a[24:27]
v_mfma_f32_16x16x32_bf16 a[24:27],v[88:91],v[228:231],a[24:27]
v_mfma_f32_16x16x32_bf16 a[24:27],v[92:95],v[244:247],a[24:27]
buffer_load_dwordx4 a[100:103],v6,s[28:31],s87 offen offset:1024 sc1
v_mfma_f32_16x16x32_bf16 a[28:31],v[92:95],v[188:191],a[28:31]
ds_read_b64_tr_b16 v[92:93],v2 offset:30752
ds_read_b64_tr_b16 v[94:95],v2 offset:35872
v_mfma_f32_16x16x32_bf16 a[28:31],v[88:91],v[172:175],a[28:31]
ds_read_b64_tr_b16 v[88:89],v2 offset:20512
v_mfma_f32_16x16x32_bf16 a[28:31],v[84:87],v[156:159],a[28:31]
ds_read_b64_tr_b16 v[84:85],v2 offset:10272
v_mfma_f32_16x16x32_bf16 a[28:31],v[80:83],v[140:143],a[28:31]
ds_read_b64_tr_b16 v[80:81],v2 offset:32
v_mfma_f32_16x16x32_bf16 v[128:131],v[96:99],v[32:35],v[128:131]
buffer_load_dwordx4 a[64:67],v6,s[28:31],s87 offen offset:2048 sc1
v_mfma_f32_16x16x32_bf16 v[128:131],v[100:103],v[36:39],v[128:131]
ds_read_b64_tr_b16 v[82:83],v2 offset:5152
ds_read_b64_tr_b16 v[86:87],v2 offset:15392
v_mfma_f32_16x16x32_bf16 v[128:131],v[104:107],v[40:43],v[128:131]
ds_read_b64_tr_b16 v[90:91],v2 offset:25632
v_add_u32 v2,0xffff6000,v2
v_mfma_f32_16x16x32_bf16 v[128:131],v[108:111],v[44:47],v[128:131]
v_mfma_f32_16x16x32_bf16 v[136:139],v[108:111],v[180:183],v[136:139]
v_mfma_f32_16x16x32_bf16 v[136:139],v[104:107],v[164:167],v[136:139]
buffer_load_dwordx4 a[72:75],v6,s[28:31],s88 offen offset:2048 sc1
v_mfma_f32_16x16x32_bf16 v[136:139],v[100:103],v[148:151],v[136:139]
v_mfma_f32_16x16x32_bf16 v[136:139],v[96:99],v[132:135],v[136:139]
v_mfma_f32_16x16x32_bf16 v[144:147],v[96:99],v[196:199],v[144:147]
v_mfma_f32_16x16x32_bf16 v[144:147],v[100:103],v[212:215],v[144:147]
v_mfma_f32_16x16x32_bf16 v[144:147],v[104:107],v[228:231],v[144:147]
buffer_load_dwordx4 a[80:83],v6,s[28:31],s89 offen offset:2048 sc1
v_mfma_f32_16x16x32_bf16 v[144:147],v[108:111],v[244:247],v[144:147]
v_mfma_f32_16x16x32_bf16 v[152:155],v[108:111],v[188:191],v[152:155]
v_mfma_f32_16x16x32_bf16 v[152:155],v[104:107],v[172:175],v[152:155]
v_mfma_f32_16x16x32_bf16 v[152:155],v[100:103],v[156:159],v[152:155]
v_mfma_f32_16x16x32_bf16 v[152:155],v[96:99],v[140:143],v[152:155]
buffer_load_dwordx4 a[88:91],v6,s[28:31],s90 offen offset:2048 sc1
s_waitcnt vmcnt(15)
s_barrier
v_mfma_f32_16x16x32_bf16 v[160:163],v[112:115],v[32:35],v[160:163]
ds_read_b64_tr_b16 v[96:97],v1
ds_read_b64_tr_b16 v[98:99],v1 offset:5120
v_mfma_f32_16x16x32_bf16 v[160:163],v[116:119],v[36:39],v[160:163]
ds_read_b64_tr_b16 v[100:101],v1 offset:10240
v_mfma_f32_16x16x32_bf16 v[160:163],v[120:123],v[40:43],v[160:163]
ds_read_b64_tr_b16 v[102:103],v1 offset:15360
v_mfma_f32_16x16x32_bf16 v[160:163],v[124:127],v[44:47],v[160:163]
buffer_load_dwordx4 a[120:123],v28,s[28:31],s90 offen offset:3072 sc1
v_mfma_f32_16x16x32_bf16 v[168:171],v[124:127],v[180:183],v[168:171]
ds_read_b64_tr_b16 v[104:105],v1 offset:20480
ds_read_b64_tr_b16 v[106:107],v1 offset:25600
v_mfma_f32_16x16x32_bf16 v[168:171],v[120:123],v[164:167],v[168:171]
ds_read_b64_tr_b16 v[108:109],v1 offset:30720
v_mfma_f32_16x16x32_bf16 v[168:171],v[116:119],v[148:151],v[168:171]
ds_read_b64_tr_b16 v[110:111],v1 offset:35840
v_add_u32 v1,0xa000,v1
v_mfma_f32_16x16x32_bf16 v[168:171],v[112:115],v[132:135],v[168:171]
v_mfma_f32_16x16x32_bf16 v[176:179],v[112:115],v[196:199],v[176:179]
buffer_load_dwordx4 a[112:115],v28,s[28:31],s89 offen offset:3072 sc1
v_mfma_f32_16x16x32_bf16 v[176:179],v[116:119],v[212:215],v[176:179]
v_mfma_f32_16x16x32_bf16 v[176:179],v[120:123],v[228:231],v[176:179]
v_mfma_f32_16x16x32_bf16 v[176:179],v[124:127],v[244:247],v[176:179]
v_mfma_f32_16x16x32_bf16 v[184:187],v[124:127],v[188:191],v[184:187]
ds_read_b64_tr_b16 v[124:125],v2 offset:30752
ds_read_b64_tr_b16 v[126:127],v2 offset:35872
v_mfma_f32_16x16x32_bf16 v[184:187],v[120:123],v[172:175],v[184:187]
buffer_load_dwordx4 a[104:107],v28,s[28:31],s88 offen offset:3072 sc1
v_mfma_f32_16x16x32_bf16 v[184:187],v[116:119],v[156:159],v[184:187]
ds_read_b64_tr_b16 v[116:117],v2 offset:10272
ds_read_b64_tr_b16 v[118:119],v2 offset:15392
v_mfma_f32_16x16x32_bf16 v[184:187],v[112:115],v[140:143],v[184:187]
s_waitcnt lgkmcnt(4)
ds_read_b64_tr_b16 v[112:113],v2 offset:32
v_mfma_f32_16x16x32_bf16 a[192:195],v[64:67],a[128:131],a[192:195]
ds_read_b64_tr_b16 v[114:115],v2 offset:5152
v_mfma_f32_16x16x32_bf16 a[192:195],v[68:71],a[144:147],a[192:195]
ds_read_b64_tr_b16 v[120:121],v2 offset:20512
v_mfma_f32_16x16x32_bf16 a[192:195],v[72:75],a[160:163],a[192:195]
ds_read_b64_tr_b16 v[122:123],v2 offset:25632
v_add_u32 v2,0xa000,v2
v_mfma_f32_16x16x32_bf16 a[192:195],v[76:79],a[176:179],a[192:195]
v_mfma_f32_16x16x32_bf16 a[196:199],v[76:79],a[180:183],a[196:199]
v_mfma_f32_16x16x32_bf16 a[196:199],v[72:75],a[164:167],a[196:199]
buffer_load_dwordx4 a[96:99],v28,s[28:31],s87 offen offset:3072 sc1
v_mfma_f32_16x16x32_bf16 a[196:199],v[68:71],a[148:151],a[196:199]
s_mul_i32 s87,s60,81920
s_add_u32 s87,s87,s66
s_add_u32 s83,s83,s87
v_mfma_f32_16x16x32_bf16 a[196:199],v[64:67],a[132:135],a[196:199]
s_lshl_b32 s66,s63,6
s_add_u32 s87,s87,s66
s_add_u32 s88,s87,20480
v_mfma_f32_16x16x32_bf16 a[200:203],v[64:67],a[136:139],a[200:203]
s_add_u32 s89,s87,40960
s_add_u32 s90,s87,61440
s_add_u32 s66,s77,4096
v_mfma_f32_16x16x32_bf16 a[200:203],v[68:71],a[152:155],a[200:203]
v_mfma_f32_16x16x32_bf16 a[200:203],v[72:75],a[168:171],a[200:203]
v_mfma_f32_16x16x32_bf16 a[200:203],v[76:79],a[184:187],a[200:203]
v_mfma_f32_16x16x32_bf16 a[204:207],v[76:79],a[188:191],a[204:207]
s_waitcnt vmcnt(18)
ds_read_b64_tr_b16 v[76:77],v1 offset:30784
ds_read_b64_tr_b16 v[78:79],v1 offset:35904
v_mfma_f32_16x16x32_bf16 a[204:207],v[72:75],a[172:175],a[204:207]
s_waitcnt vmcnt(15)
ds_read_b64_tr_b16 v[72:73],v1 offset:20544
v_mfma_f32_16x16x32_bf16 a[204:207],v[68:71],a[156:159],a[204:207]
s_waitcnt vmcnt(13)
ds_read_b64_tr_b16 v[68:69],v1 offset:10304
v_mfma_f32_16x16x32_bf16 a[204:207],v[64:67],a[140:143],a[204:207]
ds_read_b64_tr_b16 v[64:65],v1 offset:64
v_mfma_f32_16x16x32_bf16 a[208:211],v[80:83],a[128:131],a[208:211]
ds_read_b64_tr_b16 v[66:67],v1 offset:5184
ds_read_b64_tr_b16 v[70:71],v1 offset:15424
v_mfma_f32_16x16x32_bf16 a[208:211],v[84:87],a[144:147],a[208:211]
ds_read_b64_tr_b16 v[74:75],v1 offset:25664
v_add_u32 v1,0xffff6000,v1
v_mfma_f32_16x16x32_bf16 a[208:211],v[88:91],a[160:163],a[208:211]
v_mfma_f32_16x16x32_bf16 a[208:211],v[92:95],a[176:179],a[208:211]
v_mfma_f32_16x16x32_bf16 a[212:215],v[92:95],a[180:183],a[212:215]
v_mfma_f32_16x16x32_bf16 a[212:215],v[88:91],a[164:167],a[212:215]
v_mfma_f32_16x16x32_bf16 a[212:215],v[84:87],a[148:151],a[212:215]
v_mfma_f32_16x16x32_bf16 a[212:215],v[80:83],a[132:135],a[212:215]
v_mfma_f32_16x16x32_bf16 a[216:219],v[80:83],a[136:139],a[216:219]
v_mfma_f32_16x16x32_bf16 a[216:219],v[84:87],a[152:155],a[216:219]
v_mfma_f32_16x16x32_bf16 a[216:219],v[88:91],a[168:171],a[216:219]
v_mfma_f32_16x16x32_bf16 a[216:219],v[92:95],a[184:187],a[216:219]
v_mfma_f32_16x16x32_bf16 a[220:223],v[92:95],a[188:191],a[220:223]
ds_read_b64_tr_b16 v[92:93],v2 offset:30816
ds_read_b64_tr_b16 v[94:95],v2 offset:35936
v_mfma_f32_16x16x32_bf16 a[220:223],v[88:91],a[172:175],a[220:223]
ds_read_b64_tr_b16 v[88:89],v2 offset:20576
v_mfma_f32_16x16x32_bf16 a[220:223],v[84:87],a[156:159],a[220:223]
ds_read_b64_tr_b16 v[84:85],v2 offset:10336
v_mfma_f32_16x16x32_bf16 a[220:223],v[80:83],a[140:143],a[220:223]
ds_read_b64_tr_b16 v[80:81],v2 offset:96
v_mfma_f32_16x16x32_bf16 a[192:195],v[96:99],a[68:71],a[192:195]
s_waitcnt vmcnt(10)
ds_read_b64_tr_b16 v[82:83],v2 offset:5216
ds_read_b64_tr_b16 v[86:87],v2 offset:15456
v_mfma_f32_16x16x32_bf16 a[192:195],v[100:103],a[76:79],a[192:195]
ds_read_b64_tr_b16 v[90:91],v2 offset:25696
v_add_u32 v2,0xffff6000,v2
v_mfma_f32_16x16x32_bf16 a[192:195],v[104:107],a[84:87],a[192:195]
)A"
R"A(v_mfma_f32_16x16x32_bf16 a[192:195],v[108:111],a[92:95],a[192:195]
v_mfma_f32_16x16x32_bf16 a[196:199],v[108:111],a[124:127],a[196:199]
v_mfma_f32_16x16x32_bf16 a[196:199],v[104:107],a[116:119],a[196:199]
s_waitcnt vmcnt(9)
v_mfma_f32_16x16x32_bf16 a[196:199],v[100:103],a[108:111],a[196:199]
s_waitcnt vmcnt(8)
v_mfma_f32_16x16x32_bf16 a[196:199],v[96:99],a[100:103],a[196:199]
s_waitcnt vmcnt(7)
v_mfma_f32_16x16x32_bf16 a[200:203],v[96:99],a[64:67],a[200:203]
s_waitcnt vmcnt(6)
v_mfma_f32_16x16x32_bf16 a[200:203],v[100:103],a[72:75],a[200:203]
s_waitcnt vmcnt(5)
v_mfma_f32_16x16x32_bf16 a[200:203],v[104:107],a[80:83],a[200:203]
s_waitcnt vmcnt(4)
v_mfma_f32_16x16x32_bf16 a[200:203],v[108:111],a[88:91],a[200:203]
s_waitcnt vmcnt(3)
v_accvgpr_read_b32 v8,a192
v_accvgpr_read_b32 v9,a193
v_mfma_f32_16x16x32_bf16 a[204:207],v[108:111],a[120:123],a[204:207]
s_waitcnt vmcnt(2)
v_accvgpr_read_b32 v10,a194
v_accvgpr_read_b32 v11,a195
v_mfma_f32_16x16x32_bf16 a[204:207],v[104:107],a[112:115],a[204:207]
s_waitcnt vmcnt(1)
v_cvt_pk_bf16_f32 v8,v8,v9
v_cvt_pk_bf16_f32 v9,v10,v11
v_mfma_f32_16x16x32_bf16 a[204:207],v[100:103],a[104:107],a[204:207]
s_waitcnt vmcnt(0)
v_accvgpr_read_b32 v16,a196
v_accvgpr_read_b32 v17,a197
v_mfma_f32_16x16x32_bf16 a[204:207],v[96:99],a[96:99],a[204:207]
s_waitcnt lgkmcnt(0)
v_accvgpr_read_b32 v18,a198
v_accvgpr_read_b32 v19,a199
v_mfma_f32_16x16x32_bf16 a[208:211],v[112:115],a[68:71],a[208:211]
v_cvt_pk_bf16_f32 v16,v16,v17
v_cvt_pk_bf16_f32 v17,v18,v19
v_mfma_f32_16x16x32_bf16 a[208:211],v[116:119],a[76:79],a[208:211]
ds_read_b64_tr_b16 v[96:97],v1 offset:64
ds_read_b64_tr_b16 v[98:99],v1 offset:5184
v_mfma_f32_16x16x32_bf16 a[208:211],v[120:123],a[84:87],a[208:211]
ds_read_b64_tr_b16 v[100:101],v1 offset:10304
v_mfma_f32_16x16x32_bf16 a[208:211],v[124:127],a[92:95],a[208:211]
ds_read_b64_tr_b16 v[102:103],v1 offset:15424
v_mfma_f32_16x16x32_bf16 a[212:215],v[124:127],a[124:127],a[212:215]
ds_read_b64_tr_b16 v[104:105],v1 offset:20544
v_mfma_f32_16x16x32_bf16 a[212:215],v[120:123],a[116:119],a[212:215]
ds_read_b64_tr_b16 v[106:107],v1 offset:25664
ds_read_b64_tr_b16 v[108:109],v1 offset:30784
v_mfma_f32_16x16x32_bf16 a[212:215],v[116:119],a[108:111],a[212:215]
ds_read_b64_tr_b16 v[110:111],v1 offset:35904
v_add_u32 v1,0xa000,v1
v_mfma_f32_16x16x32_bf16 a[212:215],v[112:115],a[100:103],a[212:215]
v_accvgpr_read_b32 v12,a208
v_accvgpr_read_b32 v13,a209
v_mfma_f32_16x16x32_bf16 a[216:219],v[112:115],a[64:67],a[216:219]
v_accvgpr_read_b32 v14,a210
v_accvgpr_read_b32 v15,a211
v_mfma_f32_16x16x32_bf16 a[216:219],v[116:119],a[72:75],a[216:219]
v_cvt_pk_bf16_f32 v10,v12,v13
v_cvt_pk_bf16_f32 v11,v14,v15
v_mfma_f32_16x16x32_bf16 a[216:219],v[120:123],a[80:83],a[216:219]
buffer_store_dwordx4 v[8:11],v6,s[36:39],s74 offen sc1
v_mfma_f32_16x16x32_bf16 a[216:219],v[124:127],a[88:91],a[216:219]
v_accvgpr_read_b32 v20,a212
v_accvgpr_read_b32 v21,a213
v_mfma_f32_16x16x32_bf16 a[220:223],v[124:127],a[120:123],a[220:223]
v_accvgpr_read_b32 v22,a214
v_accvgpr_read_b32 v23,a215
v_mfma_f32_16x16x32_bf16 a[220:223],v[120:123],a[112:115],a[220:223]
v_cvt_pk_bf16_f32 v18,v20,v21
v_cvt_pk_bf16_f32 v19,v22,v23
v_mfma_f32_16x16x32_bf16 a[220:223],v[116:119],a[104:107],a[220:223]
v_accvgpr_read_b32 v8,a200
v_accvgpr_read_b32 v9,a201
v_mfma_f32_16x16x32_bf16 a[220:223],v[112:115],a[96:99],a[220:223]
v_accvgpr_read_b32 v10,a202
v_accvgpr_read_b32 v11,a203
v_mfma_f32_16x16x32_bf16 a[224:227],v[64:67],a[128:131],a[224:227]
v_accvgpr_read_b32 v12,a216
v_accvgpr_read_b32 v13,a217
v_mfma_f32_16x16x32_bf16 a[224:227],v[68:71],a[144:147],a[224:227]
v_accvgpr_read_b32 v14,a218
v_accvgpr_read_b32 v15,a219
v_mfma_f32_16x16x32_bf16 a[224:227],v[72:75],a[160:163],a[224:227]
v_cvt_pk_bf16_f32 v8,v8,v9
v_cvt_pk_bf16_f32 v9,v10,v11
v_mfma_f32_16x16x32_bf16 a[224:227],v[76:79],a[176:179],a[224:227]
v_cvt_pk_bf16_f32 v10,v12,v13
v_cvt_pk_bf16_f32 v11,v14,v15
v_mfma_f32_16x16x32_bf16 a[228:231],v[76:79],a[180:183],a[228:231]
ds_read_b64_tr_b16 v[112:113],v2 offset:96
ds_read_b64_tr_b16 v[114:115],v2 offset:5216
v_mfma_f32_16x16x32_bf16 a[228:231],v[72:75],a[164:167],a[228:231]
buffer_store_dwordx4 v[16:19],v6,s[36:39],s74 offen offset:1024 sc1
v_mfma_f32_16x16x32_bf16 a[228:231],v[68:71],a[148:151],a[228:231]
ds_read_b64_tr_b16 v[116:117],v2 offset:10336
ds_read_b64_tr_b16 v[118:119],v2 offset:15456
v_mfma_f32_16x16x32_bf16 a[228:231],v[64:67],a[132:135],a[228:231]
ds_read_b64_tr_b16 v[120:121],v2 offset:20576
v_accvgpr_read_b32 v16,a204
v_mfma_f32_16x16x32_bf16 a[232:235],v[64:67],a[136:139],a[232:235]
ds_read_b64_tr_b16 v[122:123],v2 offset:25696
v_accvgpr_read_b32 v17,a205
v_mfma_f32_16x16x32_bf16 a[232:235],v[68:71],a[152:155],a[232:235]
ds_read_b64_tr_b16 v[124:125],v2 offset:30816
v_accvgpr_read_b32 v18,a206
v_mfma_f32_16x16x32_bf16 a[232:235],v[72:75],a[168:171],a[232:235]
ds_read_b64_tr_b16 v[126:127],v2 offset:35936
v_accvgpr_read_b32 v19,a207
v_mfma_f32_16x16x32_bf16 a[232:235],v[76:79],a[184:187],a[232:235]
v_accvgpr_read_b32 v20,a220
v_accvgpr_read_b32 v21,a221
v_mfma_f32_16x16x32_bf16 a[236:239],v[76:79],a[188:191],a[236:239]
v_accvgpr_read_b32 v22,a222
v_accvgpr_read_b32 v23,a223
v_mfma_f32_16x16x32_bf16 a[236:239],v[72:75],a[172:175],a[236:239]
v_cvt_pk_bf16_f32 v16,v16,v17
v_cvt_pk_bf16_f32 v17,v18,v19
v_mfma_f32_16x16x32_bf16 a[236:239],v[68:71],a[156:159],a[236:239]
v_cvt_pk_bf16_f32 v18,v20,v21
v_cvt_pk_bf16_f32 v19,v22,v23
v_mfma_f32_16x16x32_bf16 a[236:239],v[64:67],a[140:143],a[236:239]
ds_read_b64_tr_b16 v[64:65],v1 offset:128
ds_read_b64_tr_b16 v[66:67],v1 offset:5248
v_mfma_f32_16x16x32_bf16 a[240:243],v[80:83],a[128:131],a[240:243]
buffer_store_dwordx4 v[8:11],v6,s[36:39],s74 offen offset:2048 sc1
v_mfma_f32_16x16x32_bf16 a[240:243],v[84:87],a[144:147],a[240:243]
ds_read_b64_tr_b16 v[68:69],v1 offset:10368
ds_read_b64_tr_b16 v[70:71],v1 offset:15488
v_mfma_f32_16x16x32_bf16 a[240:243],v[88:91],a[160:163],a[240:243]
ds_read_b64_tr_b16 v[72:73],v1 offset:20608
v_add_u32 v2,0xa000,v2
v_mfma_f32_16x16x32_bf16 a[240:243],v[92:95],a[176:179],a[240:243]
ds_read_b64_tr_b16 v[74:75],v1 offset:25728
v_mfma_f32_16x16x32_bf16 a[244:247],v[92:95],a[180:183],a[244:247]
ds_read_b64_tr_b16 v[76:77],v1 offset:30848
v_mfma_f32_16x16x32_bf16 a[244:247],v[88:91],a[164:167],a[244:247]
ds_read_b64_tr_b16 v[78:79],v1 offset:35968
v_add_u32 v1,0xffff6000,v1
v_mfma_f32_16x16x32_bf16 a[244:247],v[84:87],a[148:151],a[244:247]
v_mfma_f32_16x16x32_bf16 a[244:247],v[80:83],a[132:135],a[244:247]
v_mfma_f32_16x16x32_bf16 a[248:251],v[80:83],a[136:139],a[248:251]
v_mfma_f32_16x16x32_bf16 a[248:251],v[84:87],a[152:155],a[248:251]
v_mfma_f32_16x16x32_bf16 a[248:251],v[88:91],a[168:171],a[248:251]
v_mfma_f32_16x16x32_bf16 a[248:251],v[92:95],a[184:187],a[248:251]
buffer_store_dwordx4 v[16:19],v28,s[36:39],s74 offen offset:3072 sc1
v_mfma_f32_16x16x32_bf16 a[252:255],v[92:95],a[188:191],a[252:255]
ds_read_b64_tr_b16 v[92:93],v2 offset:30880
ds_read_b64_tr_b16 v[94:95],v2 offset:36000
v_mfma_f32_16x16x32_bf16 a[252:255],v[88:91],a[172:175],a[252:255]
ds_read_b64_tr_b16 v[88:89],v2 offset:20640
v_mfma_f32_16x16x32_bf16 a[252:255],v[84:87],a[156:159],a[252:255]
ds_read_b64_tr_b16 v[84:85],v2 offset:10400
v_mfma_f32_16x16x32_bf16 a[252:255],v[80:83],a[140:143],a[252:255]
s_waitcnt lgkmcnt(4)
ds_read_b64_tr_b16 v[80:81],v2 offset:160
v_mfma_f32_16x16x32_bf16 a[224:227],v[96:99],a[68:71],a[224:227]
ds_read_b64_tr_b16 v[82:83],v2 offset:5280
ds_read_b64_tr_b16 v[86:87],v2 offset:15520
v_mfma_f32_16x16x32_bf16 a[224:227],v[100:103],a[76:79],a[224:227]
ds_read_b64_tr_b16 v[90:91],v2 offset:25760
v_add_u32 v2,0xffff6000,v2
v_mfma_f32_16x16x32_bf16 a[224:227],v[104:107],a[84:87],a[224:227]
v_mfma_f32_16x16x32_bf16 a[224:227],v[108:111],a[92:95],a[224:227]
v_mfma_f32_16x16x32_bf16 a[228:231],v[108:111],a[124:127],a[228:231]
v_mfma_f32_16x16x32_bf16 a[228:231],v[104:107],a[116:119],a[228:231]
v_mfma_f32_16x16x32_bf16 a[228:231],v[100:103],a[108:111],a[228:231]
v_mfma_f32_16x16x32_bf16 a[228:231],v[96:99],a[100:103],a[228:231]
v_mfma_f32_16x16x32_bf16 a[232:235],v[96:99],a[64:67],a[232:235]
v_mfma_f32_16x16x32_bf16 a[232:235],v[100:103],a[72:75],a[232:235]
v_mfma_f32_16x16x32_bf16 a[232:235],v[104:107],a[80:83],a[232:235]
v_mfma_f32_16x16x32_bf16 a[232:235],v[108:111],a[88:91],a[232:235]
v_accvgpr_read_b32 v8,a224
v_accvgpr_read_b32 v9,a225
v_mfma_f32_16x16x32_bf16 a[236:239],v[108:111],a[120:123],a[236:239]
v_accvgpr_read_b32 v10,a226
v_accvgpr_read_b32 v11,a227
v_mfma_f32_16x16x32_bf16 a[236:239],v[104:107],a[112:115],a[236:239]
v_cvt_pk_bf16_f32 v8,v8,v9
v_cvt_pk_bf16_f32 v9,v10,v11
v_mfma_f32_16x16x32_bf16 a[236:239],v[100:103],a[104:107],a[236:239]
v_accvgpr_read_b32 v16,a228
v_accvgpr_read_b32 v17,a229
v_mfma_f32_16x16x32_bf16 a[236:239],v[96:99],a[96:99],a[236:239]
v_accvgpr_read_b32 v18,a230
v_accvgpr_read_b32 v19,a231
v_mfma_f32_16x16x32_bf16 a[240:243],v[112:115],a[68:71],a[240:243]
v_cvt_pk_bf16_f32 v16,v16,v17
v_cvt_pk_bf16_f32 v17,v18,v19
v_mfma_f32_16x16x32_bf16 a[240:243],v[116:119],a[76:79],a[240:243]
ds_read_b64_tr_b16 v[96:97],v1 offset:128
ds_read_b64_tr_b16 v[98:99],v1 offset:5248
v_mfma_f32_16x16x32_bf16 a[240:243],v[120:123],a[84:87],a[240:243]
ds_read_b64_tr_b16 v[100:101],v1 offset:10368
v_mfma_f32_16x16x32_bf16 a[240:243],v[124:127],a[92:95],a[240:243]
ds_read_b64_tr_b16 v[102:103],v1 offset:15488
v_mfma_f32_16x16x32_bf16 a[244:247],v[124:127],a[124:127],a[244:247]
ds_read_b64_tr_b16 v[104:105],v1 offset:20608
v_mfma_f32_16x16x32_bf16 a[244:247],v[120:123],a[116:119],a[244:247]
ds_read_b64_tr_b16 v[106:107],v1 offset:25728
ds_read_b64_tr_b16 v[108:109],v1 offset:30848
v_mfma_f32_16x16x32_bf16 a[244:247],v[116:119],a[108:111],a[244:247]
ds_read_b64_tr_b16 v[110:111],v1 offset:35968
v_add_u32 v1,0xa000,v1
v_mfma_f32_16x16x32_bf16 a[244:247],v[112:115],a[100:103],a[244:247]
v_accvgpr_read_b32 v12,a240
v_accvgpr_read_b32 v13,a241
v_mfma_f32_16x16x32_bf16 a[248:251],v[112:115],a[64:67],a[248:251]
v_accvgpr_read_b32 v14,a242
v_accvgpr_read_b32 v15,a243
v_mfma_f32_16x16x32_bf16 a[248:251],v[116:119],a[72:75],a[248:251]
v_cvt_pk_bf16_f32 v10,v12,v13
v_cvt_pk_bf16_f32 v11,v14,v15
v_mfma_f32_16x16x32_bf16 a[248:251],v[120:123],a[80:83],a[248:251]
buffer_store_dwordx4 v[8:11],v6,s[36:39],s75 offen sc1
v_mfma_f32_16x16x32_bf16 a[248:251],v[124:127],a[88:91],a[248:251]
v_accvgpr_read_b32 v20,a244
v_accvgpr_read_b32 v21,a245
v_mfma_f32_16x16x32_bf16 a[252:255],v[124:127],a[120:123],a[252:255]
v_accvgpr_read_b32 v22,a246
v_accvgpr_read_b32 v23,a247
v_mfma_f32_16x16x32_bf16 a[252:255],v[120:123],a[112:115],a[252:255]
v_cvt_pk_bf16_f32 v18,v20,v21
v_cvt_pk_bf16_f32 v19,v22,v23
v_mfma_f32_16x16x32_bf16 a[252:255],v[116:119],a[104:107],a[252:255]
v_accvgpr_read_b32 v8,a232
v_accvgpr_read_b32 v9,a233
v_mfma_f32_16x16x32_bf16 a[252:255],v[112:115],a[96:99],a[252:255]
v_accvgpr_read_b32 v10,a234
v_accvgpr_read_b32 v11,a235
v_mfma_f32_16x16x32_bf16 a[0:3],v[64:67],a[128:131],a[0:3]
v_accvgpr_read_b32 v12,a248
v_accvgpr_read_b32 v13,a249
v_mfma_f32_16x16x32_bf16 a[0:3],v[68:71],a[144:147],a[0:3]
v_accvgpr_read_b32 v14,a250
v_accvgpr_read_b32 v15,a251
v_mfma_f32_16x16x32_bf16 a[0:3],v[72:75],a[160:163],a[0:3]
v_cvt_pk_bf16_f32 v8,v8,v9
v_cvt_pk_bf16_f32 v9,v10,v11
v_mfma_f32_16x16x32_bf16 a[0:3],v[76:79],a[176:179],a[0:3]
v_cvt_pk_bf16_f32 v10,v12,v13
v_cvt_pk_bf16_f32 v11,v14,v15
v_mfma_f32_16x16x32_bf16 a[4:7],v[76:79],a[180:183],a[4:7]
ds_read_b64_tr_b16 v[112:113],v2 offset:160
ds_read_b64_tr_b16 v[114:115],v2 offset:5280
v_mfma_f32_16x16x32_bf16 a[4:7],v[72:75],a[164:167],a[4:7]
buffer_store_dwordx4 v[16:19],v6,s[36:39],s75 offen offset:1024 sc1
v_mfma_f32_16x16x32_bf16 a[4:7],v[68:71],a[148:151],a[4:7]
ds_read_b64_tr_b16 v[116:117],v2 offset:10400
ds_read_b64_tr_b16 v[118:119],v2 offset:15520
v_mfma_f32_16x16x32_bf16 a[4:7],v[64:67],a[132:135],a[4:7]
ds_read_b64_tr_b16 v[120:121],v2 offset:20640
v_accvgpr_read_b32 v16,a236
v_mfma_f32_16x16x32_bf16 a[8:11],v[64:67],a[136:139],a[8:11]
ds_read_b64_tr_b16 v[122:123],v2 offset:25760
)A"
R"A(v_accvgpr_read_b32 v17,a237
v_mfma_f32_16x16x32_bf16 a[8:11],v[68:71],a[152:155],a[8:11]
ds_read_b64_tr_b16 v[124:125],v2 offset:30880
v_accvgpr_read_b32 v18,a238
v_mfma_f32_16x16x32_bf16 a[8:11],v[72:75],a[168:171],a[8:11]
ds_read_b64_tr_b16 v[126:127],v2 offset:36000
v_accvgpr_read_b32 v19,a239
v_mfma_f32_16x16x32_bf16 a[8:11],v[76:79],a[184:187],a[8:11]
v_accvgpr_read_b32 v20,a252
v_accvgpr_read_b32 v21,a253
v_mfma_f32_16x16x32_bf16 a[12:15],v[76:79],a[188:191],a[12:15]
v_accvgpr_read_b32 v22,a254
v_accvgpr_read_b32 v23,a255
v_mfma_f32_16x16x32_bf16 a[12:15],v[72:75],a[172:175],a[12:15]
v_cvt_pk_bf16_f32 v16,v16,v17
v_cvt_pk_bf16_f32 v17,v18,v19
v_mfma_f32_16x16x32_bf16 a[12:15],v[68:71],a[156:159],a[12:15]
v_cvt_pk_bf16_f32 v18,v20,v21
v_cvt_pk_bf16_f32 v19,v22,v23
v_mfma_f32_16x16x32_bf16 a[12:15],v[64:67],a[140:143],a[12:15]
s_waitcnt lgkmcnt(3)
ds_read_b64_tr_b16 v[64:65],v1 offset:192
ds_read_b64_tr_b16 v[66:67],v1 offset:5312
v_mfma_f32_16x16x32_bf16 a[16:19],v[80:83],a[128:131],a[16:19]
buffer_store_dwordx4 v[8:11],v6,s[36:39],s75 offen offset:2048 sc1
v_mfma_f32_16x16x32_bf16 a[16:19],v[84:87],a[144:147],a[16:19]
ds_read_b64_tr_b16 v[68:69],v1 offset:10432
ds_read_b64_tr_b16 v[70:71],v1 offset:15552
v_mfma_f32_16x16x32_bf16 a[16:19],v[88:91],a[160:163],a[16:19]
ds_read_b64_tr_b16 v[72:73],v1 offset:20672
v_add_u32 v2,0xa000,v2
v_mfma_f32_16x16x32_bf16 a[16:19],v[92:95],a[176:179],a[16:19]
ds_read_b64_tr_b16 v[74:75],v1 offset:25792
v_mfma_f32_16x16x32_bf16 a[20:23],v[92:95],a[180:183],a[20:23]
ds_read_b64_tr_b16 v[76:77],v1 offset:30912
v_mfma_f32_16x16x32_bf16 a[20:23],v[88:91],a[164:167],a[20:23]
ds_read_b64_tr_b16 v[78:79],v1 offset:36032
v_add_u32 v1,0xffff6000,v1
v_mfma_f32_16x16x32_bf16 a[20:23],v[84:87],a[148:151],a[20:23]
v_mfma_f32_16x16x32_bf16 a[20:23],v[80:83],a[132:135],a[20:23]
v_mfma_f32_16x16x32_bf16 a[24:27],v[80:83],a[136:139],a[24:27]
v_mfma_f32_16x16x32_bf16 a[24:27],v[84:87],a[152:155],a[24:27]
v_mfma_f32_16x16x32_bf16 a[24:27],v[88:91],a[168:171],a[24:27]
v_mfma_f32_16x16x32_bf16 a[24:27],v[92:95],a[184:187],a[24:27]
buffer_store_dwordx4 v[16:19],v28,s[36:39],s75 offen offset:3072 sc1
v_mfma_f32_16x16x32_bf16 a[28:31],v[92:95],a[188:191],a[28:31]
ds_read_b64_tr_b16 v[92:93],v2 offset:30944
ds_read_b64_tr_b16 v[94:95],v2 offset:36064
v_mfma_f32_16x16x32_bf16 a[28:31],v[88:91],a[172:175],a[28:31]
ds_read_b64_tr_b16 v[88:89],v2 offset:20704
v_mfma_f32_16x16x32_bf16 a[28:31],v[84:87],a[156:159],a[28:31]
ds_read_b64_tr_b16 v[84:85],v2 offset:10464
v_mfma_f32_16x16x32_bf16 a[28:31],v[80:83],a[140:143],a[28:31]
ds_read_b64_tr_b16 v[80:81],v2 offset:224
v_mfma_f32_16x16x32_bf16 a[0:3],v[96:99],a[68:71],a[0:3]
ds_read_b64_tr_b16 v[82:83],v2 offset:5344
ds_read_b64_tr_b16 v[86:87],v2 offset:15584
v_mfma_f32_16x16x32_bf16 a[0:3],v[100:103],a[76:79],a[0:3]
ds_read_b64_tr_b16 v[90:91],v2 offset:25824
v_add_u32 v2,0xffff6000,v2
v_mfma_f32_16x16x32_bf16 a[0:3],v[104:107],a[84:87],a[0:3]
v_mfma_f32_16x16x32_bf16 a[0:3],v[108:111],a[92:95],a[0:3]
v_mfma_f32_16x16x32_bf16 a[4:7],v[108:111],a[124:127],a[4:7]
v_mfma_f32_16x16x32_bf16 a[4:7],v[104:107],a[116:119],a[4:7]
v_mfma_f32_16x16x32_bf16 a[4:7],v[100:103],a[108:111],a[4:7]
v_mfma_f32_16x16x32_bf16 a[4:7],v[96:99],a[100:103],a[4:7]
v_mfma_f32_16x16x32_bf16 a[8:11],v[96:99],a[64:67],a[8:11]
v_mfma_f32_16x16x32_bf16 a[8:11],v[100:103],a[72:75],a[8:11]
v_mfma_f32_16x16x32_bf16 a[8:11],v[104:107],a[80:83],a[8:11]
v_mfma_f32_16x16x32_bf16 a[8:11],v[108:111],a[88:91],a[8:11]
v_accvgpr_read_b32 v8,a0
v_accvgpr_read_b32 v9,a1
v_mfma_f32_16x16x32_bf16 a[12:15],v[108:111],a[120:123],a[12:15]
v_accvgpr_read_b32 v10,a2
v_accvgpr_read_b32 v11,a3
v_mfma_f32_16x16x32_bf16 a[12:15],v[104:107],a[112:115],a[12:15]
v_cvt_pk_bf16_f32 v8,v8,v9
v_cvt_pk_bf16_f32 v9,v10,v11
v_mfma_f32_16x16x32_bf16 a[12:15],v[100:103],a[104:107],a[12:15]
v_accvgpr_read_b32 v16,a4
v_accvgpr_read_b32 v17,a5
v_mfma_f32_16x16x32_bf16 a[12:15],v[96:99],a[96:99],a[12:15]
v_accvgpr_read_b32 v18,a6
v_accvgpr_read_b32 v19,a7
v_mfma_f32_16x16x32_bf16 a[16:19],v[112:115],a[68:71],a[16:19]
v_cvt_pk_bf16_f32 v16,v16,v17
v_cvt_pk_bf16_f32 v17,v18,v19
v_mfma_f32_16x16x32_bf16 a[16:19],v[116:119],a[76:79],a[16:19]
s_waitcnt lgkmcnt(0)
ds_read_b64_tr_b16 v[96:97],v1 offset:192
ds_read_b64_tr_b16 v[98:99],v1 offset:5312
v_mfma_f32_16x16x32_bf16 a[16:19],v[120:123],a[84:87],a[16:19]
ds_read_b64_tr_b16 v[100:101],v1 offset:10432
v_mfma_f32_16x16x32_bf16 a[16:19],v[124:127],a[92:95],a[16:19]
ds_read_b64_tr_b16 v[102:103],v1 offset:15552
v_mfma_f32_16x16x32_bf16 a[20:23],v[124:127],a[124:127],a[20:23]
ds_read_b64_tr_b16 v[104:105],v1 offset:20672
v_mfma_f32_16x16x32_bf16 a[20:23],v[120:123],a[116:119],a[20:23]
ds_read_b64_tr_b16 v[106:107],v1 offset:25792
ds_read_b64_tr_b16 v[108:109],v1 offset:30912
v_mfma_f32_16x16x32_bf16 a[20:23],v[116:119],a[108:111],a[20:23]
ds_read_b64_tr_b16 v[110:111],v1 offset:36032
v_add_u32 v1,0x14000,v1
v_mfma_f32_16x16x32_bf16 a[20:23],v[112:115],a[100:103],a[20:23]
v_accvgpr_read_b32 v12,a16
v_accvgpr_read_b32 v13,a17
v_mfma_f32_16x16x32_bf16 a[24:27],v[112:115],a[64:67],a[24:27]
v_accvgpr_read_b32 v14,a18
v_accvgpr_read_b32 v15,a19
v_mfma_f32_16x16x32_bf16 a[24:27],v[116:119],a[72:75],a[24:27]
v_cvt_pk_bf16_f32 v10,v12,v13
v_cvt_pk_bf16_f32 v11,v14,v15
v_mfma_f32_16x16x32_bf16 a[24:27],v[120:123],a[80:83],a[24:27]
buffer_store_dwordx4 v[8:11],v6,s[36:39],s76 offen sc1
L_pd23_%=:
s_cbranch_execz L_trtg2S_%=
v_mfma_f32_16x16x32_bf16 a[24:27],v[124:127],a[88:91],a[24:27]
v_accvgpr_read_b32 v20,a20
v_accvgpr_read_b32 v21,a21
v_mfma_f32_16x16x32_bf16 a[28:31],v[124:127],a[120:123],a[28:31]
v_accvgpr_read_b32 v22,a22
v_accvgpr_read_b32 v23,a23
v_mfma_f32_16x16x32_bf16 a[28:31],v[120:123],a[112:115],a[28:31]
v_cvt_pk_bf16_f32 v18,v20,v21
v_cvt_pk_bf16_f32 v19,v22,v23
v_mfma_f32_16x16x32_bf16 a[28:31],v[116:119],a[104:107],a[28:31]
v_accvgpr_read_b32 v8,a8
v_accvgpr_read_b32 v9,a9
v_mfma_f32_16x16x32_bf16 a[28:31],v[112:115],a[96:99],a[28:31]
v_accvgpr_read_b32 v10,a10
v_accvgpr_read_b32 v11,a11
v_mfma_f32_16x16x32_bf16 v[128:131],v[64:67],a[128:131],v[128:131]
v_accvgpr_read_b32 v12,a24
v_accvgpr_read_b32 v13,a25
v_mfma_f32_16x16x32_bf16 v[128:131],v[68:71],a[144:147],v[128:131]
v_accvgpr_read_b32 v14,a26
v_accvgpr_read_b32 v15,a27
v_mfma_f32_16x16x32_bf16 v[128:131],v[72:75],a[160:163],v[128:131]
v_cvt_pk_bf16_f32 v8,v8,v9
v_cvt_pk_bf16_f32 v9,v10,v11
v_mfma_f32_16x16x32_bf16 v[128:131],v[76:79],a[176:179],v[128:131]
v_cvt_pk_bf16_f32 v10,v12,v13
v_cvt_pk_bf16_f32 v11,v14,v15
v_mfma_f32_16x16x32_bf16 v[136:139],v[76:79],a[180:183],v[136:139]
ds_read_b64_tr_b16 v[112:113],v2 offset:224
ds_read_b64_tr_b16 v[114:115],v2 offset:5344
v_mfma_f32_16x16x32_bf16 v[136:139],v[72:75],a[164:167],v[136:139]
buffer_store_dwordx4 v[16:19],v6,s[36:39],s76 offen offset:1024 sc1
v_mfma_f32_16x16x32_bf16 v[136:139],v[68:71],a[148:151],v[136:139]
ds_read_b64_tr_b16 v[116:117],v2 offset:10464
ds_read_b64_tr_b16 v[118:119],v2 offset:15584
v_mfma_f32_16x16x32_bf16 v[136:139],v[64:67],a[132:135],v[136:139]
ds_read_b64_tr_b16 v[120:121],v2 offset:20704
v_accvgpr_read_b32 v16,a12
v_mfma_f32_16x16x32_bf16 v[144:147],v[64:67],a[136:139],v[144:147]
ds_read_b64_tr_b16 v[122:123],v2 offset:25824
v_accvgpr_read_b32 v17,a13
v_mfma_f32_16x16x32_bf16 v[144:147],v[68:71],a[152:155],v[144:147]
ds_read_b64_tr_b16 v[124:125],v2 offset:30944
v_accvgpr_read_b32 v18,a14
v_mfma_f32_16x16x32_bf16 v[144:147],v[72:75],a[168:171],v[144:147]
ds_read_b64_tr_b16 v[126:127],v2 offset:36064
v_accvgpr_read_b32 v19,a15
v_mfma_f32_16x16x32_bf16 v[144:147],v[76:79],a[184:187],v[144:147]
v_accvgpr_read_b32 v20,a28
v_accvgpr_read_b32 v21,a29
v_mfma_f32_16x16x32_bf16 v[152:155],v[76:79],a[188:191],v[152:155]
v_accvgpr_read_b32 v22,a30
v_accvgpr_read_b32 v23,a31
v_mfma_f32_16x16x32_bf16 v[152:155],v[72:75],a[172:175],v[152:155]
v_cvt_pk_bf16_f32 v16,v16,v17
v_cvt_pk_bf16_f32 v17,v18,v19
v_mfma_f32_16x16x32_bf16 v[152:155],v[68:71],a[156:159],v[152:155]
v_cvt_pk_bf16_f32 v18,v20,v21
v_cvt_pk_bf16_f32 v19,v22,v23
v_mfma_f32_16x16x32_bf16 v[152:155],v[64:67],a[140:143],v[152:155]
ds_read_b64_tr_b16 v[64:65],v1 offset:256
ds_read_b64_tr_b16 v[66:67],v1 offset:5376
v_mfma_f32_16x16x32_bf16 v[160:163],v[80:83],a[128:131],v[160:163]
buffer_store_dwordx4 v[8:11],v6,s[36:39],s76 offen offset:2048 sc1
v_mfma_f32_16x16x32_bf16 v[160:163],v[84:87],a[144:147],v[160:163]
ds_read_b64_tr_b16 v[68:69],v1 offset:10496
ds_read_b64_tr_b16 v[70:71],v1 offset:15616
v_mfma_f32_16x16x32_bf16 v[160:163],v[88:91],a[160:163],v[160:163]
ds_read_b64_tr_b16 v[72:73],v1 offset:20736
v_add_u32 v2,0x14000,v2
v_mfma_f32_16x16x32_bf16 v[160:163],v[92:95],a[176:179],v[160:163]
ds_read_b64_tr_b16 v[74:75],v1 offset:25856
v_mov_b32 v20,s52
v_mfma_f32_16x16x32_bf16 v[168:171],v[92:95],a[180:183],v[168:171]
ds_read_b64_tr_b16 v[76:77],v1 offset:30976
v_mov_b32 v21,s53
v_mfma_f32_16x16x32_bf16 v[168:171],v[88:91],a[164:167],v[168:171]
ds_read_b64_tr_b16 v[78:79],v1 offset:36096
v_add_u32 v1,0xffff6000,v1
v_mfma_f32_16x16x32_bf16 v[168:171],v[84:87],a[148:151],v[168:171]
v_lshlrev_b32 v22,20,v6
v_mfma_f32_16x16x32_bf16 v[168:171],v[80:83],a[132:135],v[168:171]
v_mfma_f32_16x16x32_bf16 v[176:179],v[80:83],a[136:139],v[176:179]
v_mfma_f32_16x16x32_bf16 v[176:179],v[84:87],a[152:155],v[176:179]
v_mfma_f32_16x16x32_bf16 v[176:179],v[88:91],a[168:171],v[176:179]
v_mfma_f32_16x16x32_bf16 v[176:179],v[92:95],a[184:187],v[176:179]
buffer_store_dwordx4 v[16:19],v28,s[36:39],s76 offen offset:3072 sc1
v_mfma_f32_16x16x32_bf16 v[184:187],v[92:95],a[188:191],v[184:187]
ds_read_b64_tr_b16 v[92:93],v2 offset:31008
ds_read_b64_tr_b16 v[94:95],v2 offset:36128
v_mfma_f32_16x16x32_bf16 v[184:187],v[88:91],a[172:175],v[184:187]
ds_read_b64_tr_b16 v[88:89],v2 offset:20768
v_mfma_f32_16x16x32_bf16 v[184:187],v[84:87],a[156:159],v[184:187]
ds_read_b64_tr_b16 v[84:85],v2 offset:10528
v_mfma_f32_16x16x32_bf16 v[184:187],v[80:83],a[140:143],v[184:187]
s_waitcnt lgkmcnt(4)
ds_read_b64_tr_b16 v[80:81],v2 offset:288
v_mfma_f32_16x16x32_bf16 v[128:131],v[96:99],a[68:71],v[128:131]
ds_read_b64_tr_b16 v[82:83],v2 offset:5408
ds_read_b64_tr_b16 v[86:87],v2 offset:15648
v_mfma_f32_16x16x32_bf16 v[128:131],v[100:103],a[76:79],v[128:131]
ds_read_b64_tr_b16 v[90:91],v2 offset:25888
v_add_u32 v2,0xffff6000,v2
v_mfma_f32_16x16x32_bf16 v[128:131],v[104:107],a[84:87],v[128:131]
v_mfma_f32_16x16x32_bf16 v[128:131],v[108:111],a[92:95],v[128:131]
v_mfma_f32_16x16x32_bf16 v[136:139],v[108:111],a[124:127],v[136:139]
v_mfma_f32_16x16x32_bf16 v[136:139],v[104:107],a[116:119],v[136:139]
v_mfma_f32_16x16x32_bf16 v[136:139],v[100:103],a[108:111],v[136:139]
v_mfma_f32_16x16x32_bf16 v[136:139],v[96:99],a[100:103],v[136:139]
v_mfma_f32_16x16x32_bf16 v[144:147],v[96:99],a[64:67],v[144:147]
v_mfma_f32_16x16x32_bf16 v[144:147],v[100:103],a[72:75],v[144:147]
v_mfma_f32_16x16x32_bf16 v[144:147],v[104:107],a[80:83],v[144:147]
v_mfma_f32_16x16x32_bf16 v[144:147],v[108:111],a[88:91],v[144:147]
v_cvt_pk_bf16_f32 v128,v128,v129
v_cvt_pk_bf16_f32 v129,v130,v131
v_mfma_f32_16x16x32_bf16 v[152:155],v[108:111],a[120:123],v[152:155]
ds_read_b64_tr_b16 v[108:109],v1 offset:30976
ds_read_b64_tr_b16 v[110:111],v1 offset:36096
v_mfma_f32_16x16x32_bf16 v[152:155],v[104:107],a[112:115],v[152:155]
v_cvt_pk_bf16_f32 v136,v136,v137
v_cvt_pk_bf16_f32 v137,v138,v139
v_mfma_f32_16x16x32_bf16 v[152:155],v[100:103],a[104:107],v[152:155]
v_cvt_pk_bf16_f32 v144,v144,v145
v_cvt_pk_bf16_f32 v145,v146,v147
v_mfma_f32_16x16x32_bf16 v[152:155],v[96:99],a[96:99],v[152:155]
ds_read_b64_tr_b16 v[96:97],v1 offset:256
ds_read_b64_tr_b16 v[98:99],v1 offset:5376
v_mfma_f32_16x16x32_bf16 v[160:163],v[112:115],a[68:71],v[160:163]
ds_read_b64_tr_b16 v[100:101],v1 offset:10496
v_mfma_f32_16x16x32_bf16 v[160:163],v[116:119],a[76:79],v[160:163]
ds_read_b64_tr_b16 v[102:103],v1 offset:15616
v_mfma_f32_16x16x32_bf16 v[160:163],v[120:123],a[84:87],v[160:163]
ds_read_b64_tr_b16 v[104:105],v1 offset:20736
v_mfma_f32_16x16x32_bf16 v[160:163],v[124:127],a[92:95],v[160:163]
)A"
R"A(v_cvt_pk_bf16_f32 v152,v152,v153
v_cvt_pk_bf16_f32 v153,v154,v155
v_mfma_f32_16x16x32_bf16 v[168:171],v[124:127],a[124:127],v[168:171]
ds_read_b64_tr_b16 v[106:107],v1 offset:25856
v_add_u32 v1,0xffff6000,v1
v_mfma_f32_16x16x32_bf16 v[168:171],v[120:123],a[116:119],v[168:171]
v_mfma_f32_16x16x32_bf16 v[168:171],v[116:119],a[108:111],v[168:171]
v_mfma_f32_16x16x32_bf16 v[168:171],v[112:115],a[100:103],v[168:171]
v_cvt_pk_bf16_f32 v130,v160,v161
v_cvt_pk_bf16_f32 v131,v162,v163
v_mfma_f32_16x16x32_bf16 v[176:179],v[112:115],a[64:67],v[176:179]
buffer_store_dwordx4 v[128:131],v6,s[36:39],s79 offen sc1
v_mfma_f32_16x16x32_bf16 v[176:179],v[116:119],a[72:75],v[176:179]
v_mfma_f32_16x16x32_bf16 v[176:179],v[120:123],a[80:83],v[176:179]
v_mfma_f32_16x16x32_bf16 v[176:179],v[124:127],a[88:91],v[176:179]
v_mfma_f32_16x16x32_bf16 v[184:187],v[124:127],a[120:123],v[184:187]
v_cvt_pk_bf16_f32 v138,v168,v169
v_cvt_pk_bf16_f32 v139,v170,v171
v_mfma_f32_16x16x32_bf16 v[184:187],v[120:123],a[112:115],v[184:187]
buffer_store_dwordx4 v[136:139],v6,s[36:39],s80 offen sc1
v_mfma_f32_16x16x32_bf16 v[184:187],v[116:119],a[104:107],v[184:187]
ds_read_b64_tr_b16 v[116:117],v2 offset:10528
ds_read_b64_tr_b16 v[118:119],v2 offset:15648
v_mfma_f32_16x16x32_bf16 v[184:187],v[112:115],a[96:99],v[184:187]
v_cvt_pk_bf16_f32 v146,v176,v177
v_cvt_pk_bf16_f32 v147,v178,v179
s_waitcnt vmcnt(6)
s_barrier
v_mfma_f32_16x16x32_bf16 v[192:195],v[64:67],v[32:35],v[192:195]
buffer_store_dwordx4 v[144:147],v6,s[36:39],s81 offen sc1
v_mfma_f32_16x16x32_bf16 v[192:195],v[68:71],v[36:39],v[192:195]
ds_read_b64_tr_b16 v[112:113],v2 offset:288
ds_read_b64_tr_b16 v[114:115],v2 offset:5408
v_mfma_f32_16x16x32_bf16 v[192:195],v[72:75],v[40:43],v[192:195]
buffer_store_dwordx2 v[58:59],v60,s[44:47],s18 offen offset:2312 sc1
v_mfma_f32_16x16x32_bf16 v[192:195],v[76:79],v[44:47],v[192:195]
v_cvt_pk_bf16_f32 v154,v184,v185
v_cvt_pk_bf16_f32 v155,v186,v187
v_mfma_f32_16x16x32_bf16 v[200:203],v[76:79],v[180:183],v[200:203]
buffer_store_dwordx4 v[152:155],v28,s[36:39],s82 offen sc1
v_mfma_f32_16x16x32_bf16 v[200:203],v[72:75],v[164:167],v[200:203]
ds_read_b64_tr_b16 v[120:121],v2 offset:20768
ds_read_b64_tr_b16 v[122:123],v2 offset:25888
v_mfma_f32_16x16x32_bf16 v[200:203],v[68:71],v[148:151],v[200:203]
ds_read_b64_tr_b16 v[124:125],v2 offset:31008
v_mfma_f32_16x16x32_bf16 v[200:203],v[64:67],v[132:135],v[200:203]
ds_read_b64_tr_b16 v[126:127],v2 offset:36128
v_add_u32 v2,0xffff6000,v2
v_mfma_f32_16x16x32_bf16 v[208:211],v[64:67],v[196:199],v[208:211]
v_mfma_f32_16x16x32_bf16 v[208:211],v[68:71],v[212:215],v[208:211]
v_mfma_f32_16x16x32_bf16 v[208:211],v[72:75],v[228:231],v[208:211]
v_mfma_f32_16x16x32_bf16 v[208:211],v[76:79],v[244:247],v[208:211]
v_mfma_f32_16x16x32_bf16 v[216:219],v[76:79],v[188:191],v[216:219]
ds_read_b64_tr_b16 v[76:77],v1 offset:30976
ds_read_b64_tr_b16 v[78:79],v1 offset:36096
v_mfma_f32_16x16x32_bf16 v[216:219],v[72:75],v[172:175],v[216:219]
ds_read_b64_tr_b16 v[72:73],v1 offset:20736
v_mfma_f32_16x16x32_bf16 v[216:219],v[68:71],v[156:159],v[216:219]
ds_read_b64_tr_b16 v[68:69],v1 offset:10496
v_mfma_f32_16x16x32_bf16 v[216:219],v[64:67],v[140:143],v[216:219]
s_waitcnt lgkmcnt(4)
ds_read_b64_tr_b16 v[64:65],v1 offset:256
v_mfma_f32_16x16x32_bf16 v[224:227],v[80:83],v[32:35],v[224:227]
ds_read_b64_tr_b16 v[66:67],v1 offset:5376
ds_read_b64_tr_b16 v[70:71],v1 offset:15616
v_mfma_f32_16x16x32_bf16 v[224:227],v[84:87],v[36:39],v[224:227]
ds_read_b64_tr_b16 v[74:75],v1 offset:25856
v_mfma_f32_16x16x32_bf16 v[224:227],v[88:91],v[40:43],v[224:227]
v_mfma_f32_16x16x32_bf16 v[224:227],v[92:95],v[44:47],v[224:227]
v_mfma_f32_16x16x32_bf16 v[232:235],v[92:95],v[180:183],v[232:235]
v_mfma_f32_16x16x32_bf16 v[232:235],v[88:91],v[164:167],v[232:235]
v_mfma_f32_16x16x32_bf16 v[232:235],v[84:87],v[148:151],v[232:235]
v_mfma_f32_16x16x32_bf16 v[232:235],v[80:83],v[132:135],v[232:235]
v_mfma_f32_16x16x32_bf16 v[240:243],v[80:83],v[196:199],v[240:243]
v_mfma_f32_16x16x32_bf16 v[240:243],v[84:87],v[212:215],v[240:243]
v_mfma_f32_16x16x32_bf16 v[240:243],v[88:91],v[228:231],v[240:243]
v_mfma_f32_16x16x32_bf16 v[240:243],v[92:95],v[244:247],v[240:243]
v_mfma_f32_16x16x32_bf16 v[248:251],v[92:95],v[188:191],v[248:251]
ds_read_b64_tr_b16 v[92:93],v2 offset:31008
ds_read_b64_tr_b16 v[94:95],v2 offset:36128
v_mfma_f32_16x16x32_bf16 v[248:251],v[88:91],v[172:175],v[248:251]
ds_read_b64_tr_b16 v[88:89],v2 offset:20768
v_mfma_f32_16x16x32_bf16 v[248:251],v[84:87],v[156:159],v[248:251]
ds_read_b64_tr_b16 v[84:85],v2 offset:10528
v_mfma_f32_16x16x32_bf16 v[248:251],v[80:83],v[140:143],v[248:251]
ds_read_b64_tr_b16 v[80:81],v2 offset:288
v_mfma_f32_16x16x32_bf16 v[192:195],v[96:99],a[128:131],v[192:195]
ds_read_b64_tr_b16 v[82:83],v2 offset:5408
ds_read_b64_tr_b16 v[86:87],v2 offset:15648
v_mfma_f32_16x16x32_bf16 v[192:195],v[100:103],a[144:147],v[192:195]
ds_read_b64_tr_b16 v[90:91],v2 offset:25888
v_mfma_f32_16x16x32_bf16 v[192:195],v[104:107],a[160:163],v[192:195]
v_mfma_f32_16x16x32_bf16 v[192:195],v[108:111],a[176:179],v[192:195]
v_mfma_f32_16x16x32_bf16 v[200:203],v[108:111],a[180:183],v[200:203]
v_mfma_f32_16x16x32_bf16 v[200:203],v[104:107],a[164:167],v[200:203]
v_mfma_f32_16x16x32_bf16 v[200:203],v[100:103],a[148:151],v[200:203]
v_mfma_f32_16x16x32_bf16 v[200:203],v[96:99],a[132:135],v[200:203]
v_mfma_f32_16x16x32_bf16 v[208:211],v[96:99],a[136:139],v[208:211]
v_mfma_f32_16x16x32_bf16 v[208:211],v[100:103],a[152:155],v[208:211]
v_mfma_f32_16x16x32_bf16 v[208:211],v[104:107],a[168:171],v[208:211]
v_mfma_f32_16x16x32_bf16 v[208:211],v[108:111],a[184:187],v[208:211]
v_mfma_f32_16x16x32_bf16 v[216:219],v[108:111],a[188:191],v[216:219]
v_mfma_f32_16x16x32_bf16 v[216:219],v[104:107],a[172:175],v[216:219]
v_mfma_f32_16x16x32_bf16 v[216:219],v[100:103],a[156:159],v[216:219]
v_mfma_f32_16x16x32_bf16 v[216:219],v[96:99],a[140:143],v[216:219]
v_mfma_f32_16x16x32_bf16 v[224:227],v[112:115],a[128:131],v[224:227]
v_mfma_f32_16x16x32_bf16 v[224:227],v[116:119],a[144:147],v[224:227]
v_mfma_f32_16x16x32_bf16 v[224:227],v[120:123],a[160:163],v[224:227]
v_mfma_f32_16x16x32_bf16 v[224:227],v[124:127],a[176:179],v[224:227]
v_mfma_f32_16x16x32_bf16 v[232:235],v[124:127],a[180:183],v[232:235]
v_mfma_f32_16x16x32_bf16 v[232:235],v[120:123],a[164:167],v[232:235]
v_mfma_f32_16x16x32_bf16 v[232:235],v[116:119],a[148:151],v[232:235]
v_mfma_f32_16x16x32_bf16 v[232:235],v[112:115],a[132:135],v[232:235]
v_mfma_f32_16x16x32_bf16 v[240:243],v[112:115],a[136:139],v[240:243]
v_mfma_f32_16x16x32_bf16 v[240:243],v[116:119],a[152:155],v[240:243]
v_mfma_f32_16x16x32_bf16 v[240:243],v[120:123],a[168:171],v[240:243]
v_mfma_f32_16x16x32_bf16 v[240:243],v[124:127],a[184:187],v[240:243]
v_mfma_f32_16x16x32_bf16 v[248:251],v[124:127],a[188:191],v[248:251]
v_mfma_f32_16x16x32_bf16 v[248:251],v[120:123],a[172:175],v[248:251]
v_mfma_f32_16x16x32_bf16 v[248:251],v[116:119],a[156:159],v[248:251]
v_mfma_f32_16x16x32_bf16 v[248:251],v[112:115],a[140:143],v[248:251]
s_waitcnt lgkmcnt(0)
v_mfma_f32_16x16x32_bf16 v[192:195],v[64:67],a[68:71],v[192:195]
v_mfma_f32_16x16x32_bf16 v[192:195],v[68:71],a[76:79],v[192:195]
v_mfma_f32_16x16x32_bf16 v[192:195],v[72:75],a[84:87],v[192:195]
v_mfma_f32_16x16x32_bf16 v[192:195],v[76:79],a[92:95],v[192:195]
v_mfma_f32_16x16x32_bf16 v[200:203],v[76:79],a[124:127],v[200:203]
v_mfma_f32_16x16x32_bf16 v[200:203],v[72:75],a[116:119],v[200:203]
v_mfma_f32_16x16x32_bf16 v[200:203],v[68:71],a[108:111],v[200:203]
v_mfma_f32_16x16x32_bf16 v[200:203],v[64:67],a[100:103],v[200:203]
v_mfma_f32_16x16x32_bf16 v[208:211],v[64:67],a[64:67],v[208:211]
v_mfma_f32_16x16x32_bf16 v[208:211],v[68:71],a[72:75],v[208:211]
v_mfma_f32_16x16x32_bf16 v[208:211],v[72:75],a[80:83],v[208:211]
v_mfma_f32_16x16x32_bf16 v[208:211],v[76:79],a[88:91],v[208:211]
v_mfma_f32_16x16x32_bf16 v[216:219],v[76:79],a[120:123],v[216:219]
v_mfma_f32_16x16x32_bf16 v[216:219],v[72:75],a[112:115],v[216:219]
v_mfma_f32_16x16x32_bf16 v[216:219],v[68:71],a[104:107],v[216:219]
v_mfma_f32_16x16x32_bf16 v[216:219],v[64:67],a[96:99],v[216:219]
v_mfma_f32_16x16x32_bf16 v[224:227],v[80:83],a[68:71],v[224:227]
buffer_load_dwordx2 v[62:63],v61,s[44:47],s91 offen offset:2312 sc1
v_mfma_f32_16x16x32_bf16 v[224:227],v[84:87],a[76:79],v[224:227]
v_mfma_f32_16x16x32_bf16 v[224:227],v[88:91],a[84:87],v[224:227]
v_mfma_f32_16x16x32_bf16 v[224:227],v[92:95],a[92:95],v[224:227]
v_mfma_f32_16x16x32_bf16 v[232:235],v[92:95],a[124:127],v[232:235]
v_mfma_f32_16x16x32_bf16 v[232:235],v[88:91],a[116:119],v[232:235]
v_mfma_f32_16x16x32_bf16 v[232:235],v[84:87],a[108:111],v[232:235]
v_mfma_f32_16x16x32_bf16 v[232:235],v[80:83],a[100:103],v[232:235]
v_mfma_f32_16x16x32_bf16 v[240:243],v[80:83],a[64:67],v[240:243]
v_mfma_f32_16x16x32_bf16 v[240:243],v[84:87],a[72:75],v[240:243]
v_mfma_f32_16x16x32_bf16 v[240:243],v[88:91],a[80:83],v[240:243]
v_mfma_f32_16x16x32_bf16 v[240:243],v[92:95],a[88:91],v[240:243]
v_mfma_f32_16x16x32_bf16 v[248:251],v[92:95],a[120:123],v[248:251]
v_mfma_f32_16x16x32_bf16 v[248:251],v[88:91],a[112:115],v[248:251]
v_mfma_f32_16x16x32_bf16 v[248:251],v[84:87],a[104:107],v[248:251]
v_mfma_f32_16x16x32_bf16 v[248:251],v[80:83],a[96:99],v[248:251]
s_waitcnt vmcnt(1)
s_barrier
buffer_store_dwordx2 v[20:21],v22,s[44:47],s18 offen offset:2304 sc1
s_waitcnt vmcnt(1)
v_cmp_ne_u32 s[56:57],s52,v62
v_cmp_ne_u32 s[58:59],s53,v63
s_nop 0
s_or_b64 s[56:57],s[56:57],s[58:59]
s_cbranch_scc1 L_fr8_%=
L_fo8_%=:
buffer_load_dwordx4 v[64:67],v6,s[36:39],s66 offen sc1
buffer_load_dwordx4 v[68:71],v6,s[36:39],s67 offen sc1
buffer_load_dwordx4 v[72:75],v6,s[36:39],s66 offen offset:1024 sc1
buffer_load_dwordx4 v[76:79],v6,s[36:39],s67 offen offset:1024 sc1
buffer_load_dwordx4 v[80:83],v6,s[36:39],s66 offen offset:2048 sc1
buffer_load_dwordx4 v[84:87],v6,s[36:39],s67 offen offset:2048 sc1
buffer_load_dwordx4 v[88:91],v28,s[36:39],s66 offen offset:3072 sc1
buffer_load_dwordx4 v[92:95],v28,s[36:39],s67 offen offset:3072 sc1
buffer_load_dwordx2 v[4:5],v3,s[44:47],s91 offen offset:2304 sc1
s_waitcnt vmcnt(8)
v_mfma_f32_16x16x32_bf16 v[192:195],v[48:51],v[64:67],v[192:195]
s_waitcnt vmcnt(7)
v_mfma_f32_16x16x32_bf16 v[192:195],v[48:51],v[68:71],v[192:195]
v_mfma_f32_16x16x32_bf16 v[224:227],v[52:55],v[64:67],v[224:227]
v_mfma_f32_16x16x32_bf16 v[224:227],v[52:55],v[68:71],v[224:227]
s_waitcnt vmcnt(6)
v_mfma_f32_16x16x32_bf16 v[200:203],v[48:51],v[72:75],v[200:203]
s_waitcnt vmcnt(5)
v_mfma_f32_16x16x32_bf16 v[200:203],v[48:51],v[76:79],v[200:203]
v_mfma_f32_16x16x32_bf16 v[232:235],v[52:55],v[72:75],v[232:235]
v_mfma_f32_16x16x32_bf16 v[232:235],v[52:55],v[76:79],v[232:235]
s_waitcnt vmcnt(4)
v_mfma_f32_16x16x32_bf16 v[208:211],v[48:51],v[80:83],v[208:211]
s_waitcnt vmcnt(3)
v_mfma_f32_16x16x32_bf16 v[208:211],v[48:51],v[84:87],v[208:211]
v_mfma_f32_16x16x32_bf16 v[240:243],v[52:55],v[80:83],v[240:243]
v_mfma_f32_16x16x32_bf16 v[240:243],v[52:55],v[84:87],v[240:243]
s_waitcnt vmcnt(2)
v_mfma_f32_16x16x32_bf16 v[216:219],v[48:51],v[88:91],v[216:219]
s_waitcnt vmcnt(1)
v_mfma_f32_16x16x32_bf16 v[216:219],v[48:51],v[92:95],v[216:219]
v_mfma_f32_16x16x32_bf16 v[248:251],v[52:55],v[88:91],v[248:251]
v_mfma_f32_16x16x32_bf16 v[248:251],v[52:55],v[92:95],v[248:251]
s_waitcnt vmcnt(0)
v_cmp_ne_u32 s[56:57],s52,v4
v_cmp_ne_u32 s[58:59],s53,v5
s_nop 0
s_or_b64 s[56:57],s[56:57],s[58:59]
s_cbranch_scc1 L_fr9_%=
L_fo9_%=:
buffer_load_dwordx4 v[96:99],v6,s[36:39],s68 offen sc1
buffer_load_dwordx4 v[100:103],v6,s[36:39],s68 offen offset:1024 sc1
buffer_load_dwordx4 v[104:107],v6,s[36:39],s68 offen offset:2048 sc1
buffer_load_dwordx4 v[108:111],v6,s[36:39],s68 offen offset:3072 sc1
buffer_load_dwordx4 v[112:115],v6,s[36:39],s65 offen sc1
buffer_load_dwordx4 v[116:119],v6,s[36:39],s65 offen offset:1024 sc1
buffer_load_dwordx4 v[120:123],v6,s[36:39],s65 offen offset:2048 sc1
buffer_load_dwordx4 v[124:127],v28,s[36:39],s65 offen offset:3072 sc1
s_waitcnt vmcnt(7)
v_mfma_f32_16x16x32_bf16 v[128:131],v[48:51],v[96:99],0
s_waitcnt vmcnt(6)
v_mfma_f32_16x16x32_bf16 v[128:131],v[48:51],v[100:103],v[128:131]
)A"
R"A(s_waitcnt vmcnt(5)
v_mfma_f32_16x16x32_bf16 v[128:131],v[48:51],v[104:107],v[128:131]
s_waitcnt vmcnt(4)
v_mfma_f32_16x16x32_bf16 v[128:131],v[48:51],v[108:111],v[128:131]
v_mfma_f32_16x16x32_bf16 v[160:163],v[52:55],v[96:99],0
v_mfma_f32_16x16x32_bf16 v[160:163],v[52:55],v[100:103],v[160:163]
v_mfma_f32_16x16x32_bf16 v[160:163],v[52:55],v[104:107],v[160:163]
v_mfma_f32_16x16x32_bf16 v[160:163],v[52:55],v[108:111],v[160:163]
s_waitcnt vmcnt(3)
v_mfma_f32_16x16x32_bf16 v[192:195],v[48:51],v[112:115],v[192:195]
v_mfma_f32_16x16x32_bf16 v[224:227],v[52:55],v[112:115],v[224:227]
s_waitcnt vmcnt(2)
v_mfma_f32_16x16x32_bf16 v[200:203],v[48:51],v[116:119],v[200:203]
v_mfma_f32_16x16x32_bf16 v[232:235],v[52:55],v[116:119],v[232:235]
s_waitcnt vmcnt(1)
v_cvt_pk_bf16_f32 v128,v128,v129
v_cvt_pk_bf16_f32 v129,v130,v131
v_mfma_f32_16x16x32_bf16 v[208:211],v[48:51],v[120:123],v[208:211]
v_mfma_f32_16x16x32_bf16 v[240:243],v[52:55],v[120:123],v[240:243]
s_waitcnt vmcnt(0)
v_cvt_pk_bf16_f32 v130,v160,v161
v_cvt_pk_bf16_f32 v131,v162,v163
v_mfma_f32_16x16x32_bf16 v[216:219],v[48:51],v[124:127],v[216:219]
v_cvt_pk_bf16_f32 v192,v192,v193
v_cvt_pk_bf16_f32 v193,v194,v195
v_cvt_pk_bf16_f32 v194,v224,v225
v_cvt_pk_bf16_f32 v195,v226,v227
v_cvt_pk_bf16_f32 v200,v200,v201
v_cvt_pk_bf16_f32 v201,v202,v203
v_mfma_f32_16x16x32_bf16 v[248:251],v[52:55],v[124:127],v[248:251]
v_permlane16_swap_b32 v128,v130
v_permlane16_swap_b32 v129,v131
v_cvt_pk_bf16_f32 v202,v232,v233
v_cvt_pk_bf16_f32 v203,v234,v235
v_cvt_pk_bf16_f32 v208,v208,v209
v_cvt_pk_bf16_f32 v209,v210,v211
v_cvt_pk_bf16_f32 v210,v240,v241
v_cvt_pk_bf16_f32 v211,v242,v243
buffer_store_dwordx4 v[128:131],v56,s[24:27],s83 offen offset:256 sc1 nt
v_permlane16_swap_b32 v192,v194
v_permlane16_swap_b32 v193,v195
buffer_store_dwordx4 v[192:195],v56,s[24:27],s87 offen sc1 nt
v_permlane16_swap_b32 v200,v202
v_permlane16_swap_b32 v201,v203
v_cvt_pk_bf16_f32 v216,v216,v217
v_cvt_pk_bf16_f32 v217,v218,v219
v_cvt_pk_bf16_f32 v218,v248,v249
v_cvt_pk_bf16_f32 v219,v250,v251
buffer_store_dwordx4 v[200:203],v56,s[24:27],s88 offen sc1 nt
v_permlane16_swap_b32 v208,v210
v_permlane16_swap_b32 v209,v211
buffer_store_dwordx4 v[208:211],v56,s[24:27],s89 offen sc1 nt
v_permlane16_swap_b32 v216,v218
v_permlane16_swap_b32 v217,v219
buffer_store_dwordx4 v[216:219],v56,s[24:27],s90 offen sc1 nt
s_lshl_b32 s69,s62,20
s_add_u32 s69,s69,s18
buffer_store_dword v20,v22,s[44:47],s69 offen sc1
L_geS_%=:
s_branch L_end_%=
L_hot_%=:
L_fr1_%=:
s_mov_b32 s19,65536
L_fl1_%=:
s_sleep 6
buffer_load_dwordx2 v[4:5],v3,s[44:47],s91 offen offset:256 sc1
s_waitcnt vmcnt(0)
s_sub_u32 s19,s19,1
s_cmp_eq_u32 s19,0
s_cbranch_scc1 L_fo1_%=
v_cmp_ne_u32 s[56:57],s52,v4
v_cmp_ne_u32 s[58:59],s53,v5
s_nop 1
s_or_b64 s[56:57],s[56:57],s[58:59]
s_cbranch_scc1 L_fl1_%=
s_branch L_fo1_%=
L_fr2_%=:
s_mov_b32 s19,65536
L_fl2_%=:
s_sleep 6
buffer_load_dwordx2 v[4:5],v3,s[44:47],s91 offen offset:1280 sc1
s_waitcnt vmcnt(0)
s_sub_u32 s19,s19,1
s_cmp_eq_u32 s19,0
s_cbranch_scc1 L_fo2_%=
v_cmp_ne_u32 s[56:57],s52,v4
v_cmp_ne_u32 s[58:59],s53,v5
s_nop 1
s_or_b64 s[56:57],s[56:57],s[58:59]
s_cbranch_scc1 L_fl2_%=
s_branch L_fo2_%=
L_fr3_%=:
s_mov_b32 s19,65536
L_fl3_%=:
s_sleep 6
buffer_load_dwordx2 v[4:5],v3,s[44:47],s91 offen offset:1280 sc1
s_waitcnt vmcnt(0)
s_sub_u32 s19,s19,1
s_cmp_eq_u32 s19,0
s_cbranch_scc1 L_fo3_%=
v_cmp_ne_u32 s[56:57],s52,v4
v_cmp_ne_u32 s[58:59],s53,v5
s_nop 1
s_or_b64 s[56:57],s[56:57],s[58:59]
s_cbranch_scc1 L_fl3_%=
s_branch L_fo3_%=
L_fr4_%=:
s_mov_b32 s19,65536
L_fl4_%=:
s_sleep 6
buffer_load_dwordx2 v[62:63],v61,s[44:47],s91 offen offset:2312 sc1
s_waitcnt vmcnt(0)
s_sub_u32 s19,s19,1
s_cmp_eq_u32 s19,0
s_cbranch_scc1 L_fo4_%=
v_cmp_ne_u32 s[56:57],s52,v62
v_cmp_ne_u32 s[58:59],s53,v63
s_nop 1
s_or_b64 s[56:57],s[56:57],s[58:59]
s_cbranch_scc1 L_fl4_%=
s_branch L_fo4_%=
L_fr5_%=:
s_mov_b32 s19,65536
L_fl5_%=:
s_sleep 6
buffer_load_dwordx2 v[4:5],v3,s[44:47],s91 offen offset:2304 sc1
s_waitcnt vmcnt(0)
s_sub_u32 s19,s19,1
s_cmp_eq_u32 s19,0
s_cbranch_scc1 L_fo5_%=
v_cmp_ne_u32 s[56:57],s52,v4
v_cmp_ne_u32 s[58:59],s53,v5
s_nop 1
s_or_b64 s[56:57],s[56:57],s[58:59]
s_cbranch_scc1 L_fl5_%=
s_branch L_fo5_%=
L_fr6_%=:
s_mov_b32 s19,65536
L_fl6_%=:
s_sleep 6
buffer_load_dwordx2 v[4:5],v3,s[44:47],s91 offen offset:256 sc1
s_waitcnt vmcnt(0)
s_sub_u32 s19,s19,1
s_cmp_eq_u32 s19,0
s_cbranch_scc1 L_fo6_%=
v_cmp_ne_u32 s[56:57],s52,v4
v_cmp_ne_u32 s[58:59],s53,v5
s_nop 1
s_or_b64 s[56:57],s[56:57],s[58:59]
s_cbranch_scc1 L_fl6_%=
s_branch L_fo6_%=
L_fr7_%=:
s_mov_b32 s19,65536
L_fl7_%=:
s_sleep 6
buffer_load_dwordx2 v[4:5],v3,s[44:47],s91 offen offset:1280 sc1
s_waitcnt vmcnt(0)
s_sub_u32 s19,s19,1
s_cmp_eq_u32 s19,0
s_cbranch_scc1 L_fo7_%=
v_cmp_ne_u32 s[56:57],s52,v4
v_cmp_ne_u32 s[58:59],s53,v5
s_nop 1
s_or_b64 s[56:57],s[56:57],s[58:59]
s_cbranch_scc1 L_fl7_%=
s_branch L_fo7_%=
L_fr8_%=:
s_mov_b32 s19,65536
L_fl8_%=:
s_sleep 6
buffer_load_dwordx2 v[62:63],v61,s[44:47],s91 offen offset:2312 sc1
s_waitcnt vmcnt(0)
s_sub_u32 s19,s19,1
s_cmp_eq_u32 s19,0
s_cbranch_scc1 L_fo8_%=
v_cmp_ne_u32 s[56:57],s52,v62
v_cmp_ne_u32 s[58:59],s53,v63
s_nop 1
s_or_b64 s[56:57],s[56:57],s[58:59]
s_cbranch_scc1 L_fl8_%=
s_branch L_fo8_%=
L_fr9_%=:
s_mov_b32 s19,65536
L_fl9_%=:
s_sleep 6
buffer_load_dwordx2 v[4:5],v3,s[44:47],s91 offen offset:2304 sc1
s_waitcnt vmcnt(0)
s_sub_u32 s19,s19,1
s_cmp_eq_u32 s19,0
s_cbranch_scc1 L_fo9_%=
v_cmp_ne_u32 s[56:57],s52,v4
v_cmp_ne_u32 s[58:59],s53,v5
s_nop 1
s_or_b64 s[56:57],s[56:57],s[58:59]
s_cbranch_scc1 L_fl9_%=
s_branch L_fo9_%=
L_slow0_%=:
s_waitcnt vmcnt(0) lgkmcnt(0)
s_nop 7
v_accvgpr_write_b32 a160,v32
v_accvgpr_write_b32 a161,v33
v_accvgpr_write_b32 a162,v34
v_accvgpr_write_b32 a163,v35
v_accvgpr_write_b32 a164,v36
v_accvgpr_write_b32 a165,v37
v_accvgpr_write_b32 a166,v38
v_accvgpr_write_b32 a167,v39
v_accvgpr_write_b32 a168,v40
v_accvgpr_write_b32 a169,v41
v_accvgpr_write_b32 a170,v42
v_accvgpr_write_b32 a171,v43
v_accvgpr_write_b32 a172,v44
v_accvgpr_write_b32 a173,v45
v_accvgpr_write_b32 a174,v46
v_accvgpr_write_b32 a175,v47
s_waitcnt vmcnt(0) lgkmcnt(0)
s_nop 7
v_bfe_u32 v11,v6,4,4
v_lshrrev_b32 v12,8,v6
v_lshlrev_b32 v8,8,v11
v_lshl_add_u32 v8,v12,4,v8
v_lshlrev_b32 v9,10,v12
v_lshl_add_u32 v9,v11,1,v9
v_add_u32 v10,4096,v9
v_mov_b32 v48,0xff800000
v_mov_b32 v52,0xff800000
v_mov_b32 v56,0xff800000
v_mov_b32 v60,0xff800000
s_mov_b32 s65,0
s_mov_b32 s66,64
L_s10_%=:
buffer_load_dwordx4 a[128:131],v8,s[20:23],s65 offen
buffer_load_dwordx4 a[132:135],v8,s[20:23],s65 offen offset:64
buffer_load_dwordx4 a[136:139],v8,s[20:23],s65 offen offset:128
buffer_load_dwordx4 a[140:143],v8,s[20:23],s65 offen offset:192
s_waitcnt vmcnt(0)
v_mfma_f32_16x16x32_bf16 v[64:67],a[128:131],v[132:135],0
v_mfma_f32_16x16x32_bf16 v[64:67],a[132:135],v[140:143],v[64:67]
v_mfma_f32_16x16x32_bf16 v[64:67],a[136:139],v[148:151],v[64:67]
v_mfma_f32_16x16x32_bf16 v[64:67],a[140:143],v[156:159],v[64:67]
v_mfma_f32_16x16x32_bf16 v[80:83],a[128:131],v[164:167],0
v_mfma_f32_16x16x32_bf16 v[80:83],a[132:135],v[172:175],v[80:83]
v_mfma_f32_16x16x32_bf16 v[80:83],a[136:139],v[180:183],v[80:83]
v_mfma_f32_16x16x32_bf16 v[80:83],a[140:143],v[188:191],v[80:83]
v_mfma_f32_16x16x32_bf16 v[96:99],a[128:131],v[196:199],0
v_mfma_f32_16x16x32_bf16 v[96:99],a[132:135],v[204:207],v[96:99]
v_mfma_f32_16x16x32_bf16 v[96:99],a[136:139],v[212:215],v[96:99]
v_mfma_f32_16x16x32_bf16 v[96:99],a[140:143],v[220:223],v[96:99]
v_mfma_f32_16x16x32_bf16 v[112:115],a[128:131],v[228:231],0
v_mfma_f32_16x16x32_bf16 v[112:115],a[132:135],v[236:239],v[112:115]
v_mfma_f32_16x16x32_bf16 v[112:115],a[136:139],v[244:247],v[112:115]
v_mfma_f32_16x16x32_bf16 v[112:115],a[140:143],v[252:255],v[112:115]
s_nop 7
v_max3_f32 v48,v48,v64,v65
v_max3_f32 v48,v48,v66,v67
v_max3_f32 v52,v52,v80,v81
v_max3_f32 v52,v52,v82,v83
v_max3_f32 v56,v56,v96,v97
v_max3_f32 v56,v56,v98,v99
v_max3_f32 v60,v60,v112,v113
v_max3_f32 v60,v60,v114,v115
s_add_u32 s65,s65,4096
s_sub_u32 s66,s66,1
s_cmp_lg_u32 s66,0
s_cbranch_scc1 L_s10_%=
v_mov_b32 v11,v48
s_nop 1
v_permlane32_swap_b32 v48,v11
v_max_f32 v48,v48,v11
v_mov_b32 v11,v48
s_nop 1
v_permlane16_swap_b32 v48,v11
v_max_f32 v48,v48,v11
v_sub_f32 v32,0,v48
v_sub_f32 v33,0,v48
v_sub_f32 v34,0,v48
v_sub_f32 v35,0,v48
v_mov_b32 v11,v52
s_nop 1
v_permlane32_swap_b32 v52,v11
v_max_f32 v52,v52,v11
v_mov_b32 v11,v52
s_nop 1
v_permlane16_swap_b32 v52,v11
v_max_f32 v52,v52,v11
v_sub_f32 v36,0,v52
v_sub_f32 v37,0,v52
v_sub_f32 v38,0,v52
v_sub_f32 v39,0,v52
v_mov_b32 v11,v56
s_nop 1
v_permlane32_swap_b32 v56,v11
v_max_f32 v56,v56,v11
v_mov_b32 v11,v56
s_nop 1
v_permlane16_swap_b32 v56,v11
v_max_f32 v56,v56,v11
v_sub_f32 v40,0,v56
v_sub_f32 v41,0,v56
v_sub_f32 v42,0,v56
v_sub_f32 v43,0,v56
v_mov_b32 v11,v60
s_nop 1
v_permlane32_swap_b32 v60,v11
v_max_f32 v60,v60,v11
v_mov_b32 v11,v60
s_nop 1
v_permlane16_swap_b32 v60,v11
v_max_f32 v60,v60,v11
v_max_f32 v60,s72,v60
v_sub_f32 v44,0,v60
v_sub_f32 v45,0,v60
v_sub_f32 v46,0,v60
v_sub_f32 v47,0,v60
v_mov_b32 v48,0
v_mov_b32 v49,0
v_mov_b32 v50,0
v_mov_b32 v51,0
v_mov_b32 v52,0
v_mov_b32 v53,0
v_mov_b32 v54,0
v_mov_b32 v55,0
v_mov_b32 v56,0
v_mov_b32 v57,0
v_mov_b32 v58,0
v_mov_b32 v59,0
v_mov_b32 v60,0
v_mov_b32 v61,0
v_mov_b32 v62,0
v_mov_b32 v63,0
v_accvgpr_write_b32 a0,0
v_accvgpr_write_b32 a1,0
v_accvgpr_write_b32 a2,0
v_accvgpr_write_b32 a3,0
v_accvgpr_write_b32 a4,0
v_accvgpr_write_b32 a5,0
v_accvgpr_write_b32 a6,0
v_accvgpr_write_b32 a7,0
v_accvgpr_write_b32 a8,0
v_accvgpr_write_b32 a9,0
v_accvgpr_write_b32 a10,0
v_accvgpr_write_b32 a11,0
v_accvgpr_write_b32 a12,0
v_accvgpr_write_b32 a13,0
v_accvgpr_write_b32 a14,0
v_accvgpr_write_b32 a15,0
v_accvgpr_write_b32 a16,0
v_accvgpr_write_b32 a17,0
v_accvgpr_write_b32 a18,0
v_accvgpr_write_b32 a19,0
v_accvgpr_write_b32 a20,0
v_accvgpr_write_b32 a21,0
v_accvgpr_write_b32 a22,0
v_accvgpr_write_b32 a23,0
v_accvgpr_write_b32 a24,0
v_accvgpr_write_b32 a25,0
v_accvgpr_write_b32 a26,0
v_accvgpr_write_b32 a27,0
v_accvgpr_write_b32 a28,0
v_accvgpr_write_b32 a29,0
v_accvgpr_write_b32 a30,0
v_accvgpr_write_b32 a31,0
v_accvgpr_write_b32 a32,0
v_accvgpr_write_b32 a33,0
v_accvgpr_write_b32 a34,0
v_accvgpr_write_b32 a35,0
v_accvgpr_write_b32 a36,0
v_accvgpr_write_b32 a37,0
v_accvgpr_write_b32 a38,0
v_accvgpr_write_b32 a39,0
v_accvgpr_write_b32 a40,0
v_accvgpr_write_b32 a41,0
v_accvgpr_write_b32 a42,0
v_accvgpr_write_b32 a43,0
v_accvgpr_write_b32 a44,0
v_accvgpr_write_b32 a45,0
v_accvgpr_write_b32 a46,0
v_accvgpr_write_b32 a47,0
v_accvgpr_write_b32 a48,0
v_accvgpr_write_b32 a49,0
v_accvgpr_write_b32 a50,0
v_accvgpr_write_b32 a51,0
v_accvgpr_write_b32 a52,0
v_accvgpr_write_b32 a53,0
v_accvgpr_write_b32 a54,0
v_accvgpr_write_b32 a55,0
v_accvgpr_write_b32 a56,0
v_accvgpr_write_b32 a57,0
v_accvgpr_write_b32 a58,0
v_accvgpr_write_b32 a59,0
v_accvgpr_write_b32 a60,0
v_accvgpr_write_b32 a61,0
v_accvgpr_write_b32 a62,0
v_accvgpr_write_b32 a63,0
v_accvgpr_write_b32 a64,0
v_accvgpr_write_b32 a65,0
v_accvgpr_write_b32 a66,0
v_accvgpr_write_b32 a67,0
v_accvgpr_write_b32 a68,0
v_accvgpr_write_b32 a69,0
v_accvgpr_write_b32 a70,0
v_accvgpr_write_b32 a71,0
v_accvgpr_write_b32 a72,0
v_accvgpr_write_b32 a73,0
v_accvgpr_write_b32 a74,0
v_accvgpr_write_b32 a75,0
v_accvgpr_write_b32 a76,0
v_accvgpr_write_b32 a77,0
v_accvgpr_write_b32 a78,0
v_accvgpr_write_b32 a79,0
v_accvgpr_write_b32 a80,0
v_accvgpr_write_b32 a81,0
v_accvgpr_write_b32 a82,0
v_accvgpr_write_b32 a83,0
v_accvgpr_write_b32 a84,0
v_accvgpr_write_b32 a85,0
v_accvgpr_write_b32 a86,0
v_accvgpr_write_b32 a87,0
v_accvgpr_write_b32 a88,0
v_accvgpr_write_b32 a89,0
v_accvgpr_write_b32 a90,0
v_accvgpr_write_b32 a91,0
v_accvgpr_write_b32 a92,0
v_accvgpr_write_b32 a93,0
v_accvgpr_write_b32 a94,0
v_accvgpr_write_b32 a95,0
v_accvgpr_write_b32 a96,0
v_accvgpr_write_b32 a97,0
v_accvgpr_write_b32 a98,0
v_accvgpr_write_b32 a99,0
v_accvgpr_write_b32 a100,0
v_accvgpr_write_b32 a101,0
v_accvgpr_write_b32 a102,0
v_accvgpr_write_b32 a103,0
v_accvgpr_write_b32 a104,0
v_accvgpr_write_b32 a105,0
v_accvgpr_write_b32 a106,0
)A"
R"A(v_accvgpr_write_b32 a107,0
v_accvgpr_write_b32 a108,0
v_accvgpr_write_b32 a109,0
v_accvgpr_write_b32 a110,0
v_accvgpr_write_b32 a111,0
v_accvgpr_write_b32 a112,0
v_accvgpr_write_b32 a113,0
v_accvgpr_write_b32 a114,0
v_accvgpr_write_b32 a115,0
v_accvgpr_write_b32 a116,0
v_accvgpr_write_b32 a117,0
v_accvgpr_write_b32 a118,0
v_accvgpr_write_b32 a119,0
v_accvgpr_write_b32 a120,0
v_accvgpr_write_b32 a121,0
v_accvgpr_write_b32 a122,0
v_accvgpr_write_b32 a123,0
v_accvgpr_write_b32 a124,0
v_accvgpr_write_b32 a125,0
v_accvgpr_write_b32 a126,0
v_accvgpr_write_b32 a127,0
s_mov_b32 s65,0
s_mov_b32 s66,32
L_s20_%=:
s_add_u32 s67,s65,4096
buffer_load_dwordx4 a[128:131],v8,s[20:23],s65 offen
buffer_load_dwordx4 a[132:135],v8,s[20:23],s65 offen offset:64
buffer_load_dwordx4 a[136:139],v8,s[20:23],s65 offen offset:128
buffer_load_dwordx4 a[140:143],v8,s[20:23],s65 offen offset:192
buffer_load_dwordx4 a[144:147],v8,s[20:23],s67 offen
buffer_load_dwordx4 a[148:151],v8,s[20:23],s67 offen offset:64
buffer_load_dwordx4 a[152:155],v8,s[20:23],s67 offen offset:128
buffer_load_dwordx4 a[156:159],v8,s[20:23],s67 offen offset:192
s_waitcnt vmcnt(0)
v_mfma_f32_16x16x32_bf16 v[64:67],a[128:131],v[132:135],v[32:35]
v_mfma_f32_16x16x32_bf16 v[64:67],a[132:135],v[140:143],v[64:67]
v_mfma_f32_16x16x32_bf16 v[64:67],a[136:139],v[148:151],v[64:67]
v_mfma_f32_16x16x32_bf16 v[64:67],a[140:143],v[156:159],v[64:67]
v_mfma_f32_16x16x32_bf16 v[68:71],a[144:147],v[132:135],v[32:35]
v_mfma_f32_16x16x32_bf16 v[68:71],a[148:151],v[140:143],v[68:71]
v_mfma_f32_16x16x32_bf16 v[68:71],a[152:155],v[148:151],v[68:71]
v_mfma_f32_16x16x32_bf16 v[68:71],a[156:159],v[156:159],v[68:71]
v_mfma_f32_16x16x32_bf16 v[80:83],a[128:131],v[164:167],v[36:39]
v_mfma_f32_16x16x32_bf16 v[80:83],a[132:135],v[172:175],v[80:83]
v_mfma_f32_16x16x32_bf16 v[80:83],a[136:139],v[180:183],v[80:83]
v_mfma_f32_16x16x32_bf16 v[80:83],a[140:143],v[188:191],v[80:83]
v_mfma_f32_16x16x32_bf16 v[84:87],a[144:147],v[164:167],v[36:39]
v_mfma_f32_16x16x32_bf16 v[84:87],a[148:151],v[172:175],v[84:87]
v_mfma_f32_16x16x32_bf16 v[84:87],a[152:155],v[180:183],v[84:87]
v_mfma_f32_16x16x32_bf16 v[84:87],a[156:159],v[188:191],v[84:87]
v_mfma_f32_16x16x32_bf16 v[96:99],a[128:131],v[196:199],v[40:43]
v_mfma_f32_16x16x32_bf16 v[96:99],a[132:135],v[204:207],v[96:99]
v_mfma_f32_16x16x32_bf16 v[96:99],a[136:139],v[212:215],v[96:99]
v_mfma_f32_16x16x32_bf16 v[96:99],a[140:143],v[220:223],v[96:99]
v_mfma_f32_16x16x32_bf16 v[100:103],a[144:147],v[196:199],v[40:43]
v_mfma_f32_16x16x32_bf16 v[100:103],a[148:151],v[204:207],v[100:103]
v_mfma_f32_16x16x32_bf16 v[100:103],a[152:155],v[212:215],v[100:103]
v_mfma_f32_16x16x32_bf16 v[100:103],a[156:159],v[220:223],v[100:103]
v_mfma_f32_16x16x32_bf16 v[112:115],a[128:131],v[228:231],v[44:47]
v_mfma_f32_16x16x32_bf16 v[112:115],a[132:135],v[236:239],v[112:115]
v_mfma_f32_16x16x32_bf16 v[112:115],a[136:139],v[244:247],v[112:115]
v_mfma_f32_16x16x32_bf16 v[112:115],a[140:143],v[252:255],v[112:115]
v_mfma_f32_16x16x32_bf16 v[116:119],a[144:147],v[228:231],v[44:47]
v_mfma_f32_16x16x32_bf16 v[116:119],a[148:151],v[236:239],v[116:119]
v_mfma_f32_16x16x32_bf16 v[116:119],a[152:155],v[244:247],v[116:119]
v_mfma_f32_16x16x32_bf16 v[116:119],a[156:159],v[252:255],v[116:119]
s_nop 7
v_exp_f32 v64,v64
v_exp_f32 v65,v65
v_exp_f32 v66,v66
v_exp_f32 v67,v67
v_exp_f32 v68,v68
v_exp_f32 v69,v69
v_exp_f32 v70,v70
v_exp_f32 v71,v71
s_nop 0
v_cvt_pk_bf16_f32 v64,v64,v65
v_cvt_pk_bf16_f32 v65,v66,v67
v_cvt_pk_bf16_f32 v66,v68,v69
v_cvt_pk_bf16_f32 v67,v70,v71
v_exp_f32 v80,v80
v_exp_f32 v81,v81
v_exp_f32 v82,v82
v_exp_f32 v83,v83
v_exp_f32 v84,v84
v_exp_f32 v85,v85
v_exp_f32 v86,v86
v_exp_f32 v87,v87
s_nop 0
v_cvt_pk_bf16_f32 v80,v80,v81
v_cvt_pk_bf16_f32 v81,v82,v83
v_cvt_pk_bf16_f32 v82,v84,v85
v_cvt_pk_bf16_f32 v83,v86,v87
v_exp_f32 v96,v96
v_exp_f32 v97,v97
v_exp_f32 v98,v98
v_exp_f32 v99,v99
v_exp_f32 v100,v100
v_exp_f32 v101,v101
v_exp_f32 v102,v102
v_exp_f32 v103,v103
s_nop 0
v_cvt_pk_bf16_f32 v96,v96,v97
v_cvt_pk_bf16_f32 v97,v98,v99
v_cvt_pk_bf16_f32 v98,v100,v101
v_cvt_pk_bf16_f32 v99,v102,v103
v_exp_f32 v112,v112
v_exp_f32 v113,v113
v_exp_f32 v114,v114
v_exp_f32 v115,v115
v_exp_f32 v116,v116
v_exp_f32 v117,v117
v_exp_f32 v118,v118
v_exp_f32 v119,v119
s_nop 0
v_cvt_pk_bf16_f32 v112,v112,v113
v_cvt_pk_bf16_f32 v113,v114,v115
v_cvt_pk_bf16_f32 v114,v116,v117
v_cvt_pk_bf16_f32 v115,v118,v119
buffer_load_ushort v72,v9,s[24:27],s65 offen
buffer_load_ushort v73,v9,s[24:27],s65 offen offset:256
buffer_load_ushort v74,v9,s[24:27],s65 offen offset:512
buffer_load_ushort v75,v9,s[24:27],s65 offen offset:768
buffer_load_ushort v76,v10,s[24:27],s65 offen
buffer_load_ushort v77,v10,s[24:27],s65 offen offset:256
buffer_load_ushort v78,v10,s[24:27],s65 offen offset:512
buffer_load_ushort v79,v10,s[24:27],s65 offen offset:768
s_waitcnt vmcnt(0)
v_lshl_or_b32 v88,v73,16,v72
v_lshl_or_b32 v89,v75,16,v74
v_lshl_or_b32 v90,v77,16,v76
v_lshl_or_b32 v91,v79,16,v78
s_nop 1
v_mfma_f32_16x16x32_bf16 a[0:3],v[88:91],v[64:67],a[0:3]
v_mfma_f32_16x16x32_bf16 a[32:35],v[88:91],v[80:83],a[32:35]
v_mfma_f32_16x16x32_bf16 a[64:67],v[88:91],v[96:99],a[64:67]
v_mfma_f32_16x16x32_bf16 a[96:99],v[88:91],v[112:115],a[96:99]
buffer_load_ushort v72,v9,s[24:27],s65 offen offset:32
buffer_load_ushort v73,v9,s[24:27],s65 offen offset:288
buffer_load_ushort v74,v9,s[24:27],s65 offen offset:544
buffer_load_ushort v75,v9,s[24:27],s65 offen offset:800
buffer_load_ushort v76,v10,s[24:27],s65 offen offset:32
buffer_load_ushort v77,v10,s[24:27],s65 offen offset:288
buffer_load_ushort v78,v10,s[24:27],s65 offen offset:544
buffer_load_ushort v79,v10,s[24:27],s65 offen offset:800
s_waitcnt vmcnt(0)
v_lshl_or_b32 v88,v73,16,v72
v_lshl_or_b32 v89,v75,16,v74
v_lshl_or_b32 v90,v77,16,v76
v_lshl_or_b32 v91,v79,16,v78
s_nop 1
v_mfma_f32_16x16x32_bf16 a[4:7],v[88:91],v[64:67],a[4:7]
v_mfma_f32_16x16x32_bf16 a[36:39],v[88:91],v[80:83],a[36:39]
v_mfma_f32_16x16x32_bf16 a[68:71],v[88:91],v[96:99],a[68:71]
v_mfma_f32_16x16x32_bf16 a[100:103],v[88:91],v[112:115],a[100:103]
buffer_load_ushort v72,v9,s[24:27],s65 offen offset:64
buffer_load_ushort v73,v9,s[24:27],s65 offen offset:320
buffer_load_ushort v74,v9,s[24:27],s65 offen offset:576
buffer_load_ushort v75,v9,s[24:27],s65 offen offset:832
buffer_load_ushort v76,v10,s[24:27],s65 offen offset:64
buffer_load_ushort v77,v10,s[24:27],s65 offen offset:320
buffer_load_ushort v78,v10,s[24:27],s65 offen offset:576
buffer_load_ushort v79,v10,s[24:27],s65 offen offset:832
s_waitcnt vmcnt(0)
v_lshl_or_b32 v88,v73,16,v72
v_lshl_or_b32 v89,v75,16,v74
v_lshl_or_b32 v90,v77,16,v76
v_lshl_or_b32 v91,v79,16,v78
s_nop 1
v_mfma_f32_16x16x32_bf16 a[8:11],v[88:91],v[64:67],a[8:11]
v_mfma_f32_16x16x32_bf16 a[40:43],v[88:91],v[80:83],a[40:43]
v_mfma_f32_16x16x32_bf16 a[72:75],v[88:91],v[96:99],a[72:75]
v_mfma_f32_16x16x32_bf16 a[104:107],v[88:91],v[112:115],a[104:107]
buffer_load_ushort v72,v9,s[24:27],s65 offen offset:96
buffer_load_ushort v73,v9,s[24:27],s65 offen offset:352
buffer_load_ushort v74,v9,s[24:27],s65 offen offset:608
buffer_load_ushort v75,v9,s[24:27],s65 offen offset:864
buffer_load_ushort v76,v10,s[24:27],s65 offen offset:96
buffer_load_ushort v77,v10,s[24:27],s65 offen offset:352
buffer_load_ushort v78,v10,s[24:27],s65 offen offset:608
buffer_load_ushort v79,v10,s[24:27],s65 offen offset:864
s_waitcnt vmcnt(0)
v_lshl_or_b32 v88,v73,16,v72
v_lshl_or_b32 v89,v75,16,v74
v_lshl_or_b32 v90,v77,16,v76
v_lshl_or_b32 v91,v79,16,v78
s_nop 1
v_mfma_f32_16x16x32_bf16 a[12:15],v[88:91],v[64:67],a[12:15]
v_mfma_f32_16x16x32_bf16 a[44:47],v[88:91],v[80:83],a[44:47]
v_mfma_f32_16x16x32_bf16 a[76:79],v[88:91],v[96:99],a[76:79]
v_mfma_f32_16x16x32_bf16 a[108:111],v[88:91],v[112:115],a[108:111]
buffer_load_ushort v72,v9,s[24:27],s65 offen offset:128
buffer_load_ushort v73,v9,s[24:27],s65 offen offset:384
buffer_load_ushort v74,v9,s[24:27],s65 offen offset:640
buffer_load_ushort v75,v9,s[24:27],s65 offen offset:896
buffer_load_ushort v76,v10,s[24:27],s65 offen offset:128
buffer_load_ushort v77,v10,s[24:27],s65 offen offset:384
buffer_load_ushort v78,v10,s[24:27],s65 offen offset:640
buffer_load_ushort v79,v10,s[24:27],s65 offen offset:896
s_waitcnt vmcnt(0)
v_lshl_or_b32 v88,v73,16,v72
v_lshl_or_b32 v89,v75,16,v74
v_lshl_or_b32 v90,v77,16,v76
v_lshl_or_b32 v91,v79,16,v78
s_nop 1
v_mfma_f32_16x16x32_bf16 a[16:19],v[88:91],v[64:67],a[16:19]
v_mfma_f32_16x16x32_bf16 a[48:51],v[88:91],v[80:83],a[48:51]
v_mfma_f32_16x16x32_bf16 a[80:83],v[88:91],v[96:99],a[80:83]
v_mfma_f32_16x16x32_bf16 a[112:115],v[88:91],v[112:115],a[112:115]
buffer_load_ushort v72,v9,s[24:27],s65 offen offset:160
buffer_load_ushort v73,v9,s[24:27],s65 offen offset:416
buffer_load_ushort v74,v9,s[24:27],s65 offen offset:672
buffer_load_ushort v75,v9,s[24:27],s65 offen offset:928
buffer_load_ushort v76,v10,s[24:27],s65 offen offset:160
buffer_load_ushort v77,v10,s[24:27],s65 offen offset:416
buffer_load_ushort v78,v10,s[24:27],s65 offen offset:672
buffer_load_ushort v79,v10,s[24:27],s65 offen offset:928
s_waitcnt vmcnt(0)
v_lshl_or_b32 v88,v73,16,v72
v_lshl_or_b32 v89,v75,16,v74
v_lshl_or_b32 v90,v77,16,v76
v_lshl_or_b32 v91,v79,16,v78
s_nop 1
v_mfma_f32_16x16x32_bf16 a[20:23],v[88:91],v[64:67],a[20:23]
v_mfma_f32_16x16x32_bf16 a[52:55],v[88:91],v[80:83],a[52:55]
v_mfma_f32_16x16x32_bf16 a[84:87],v[88:91],v[96:99],a[84:87]
v_mfma_f32_16x16x32_bf16 a[116:119],v[88:91],v[112:115],a[116:119]
buffer_load_ushort v72,v9,s[24:27],s65 offen offset:192
buffer_load_ushort v73,v9,s[24:27],s65 offen offset:448
buffer_load_ushort v74,v9,s[24:27],s65 offen offset:704
buffer_load_ushort v75,v9,s[24:27],s65 offen offset:960
buffer_load_ushort v76,v10,s[24:27],s65 offen offset:192
buffer_load_ushort v77,v10,s[24:27],s65 offen offset:448
buffer_load_ushort v78,v10,s[24:27],s65 offen offset:704
buffer_load_ushort v79,v10,s[24:27],s65 offen offset:960
s_waitcnt vmcnt(0)
v_lshl_or_b32 v88,v73,16,v72
v_lshl_or_b32 v89,v75,16,v74
v_lshl_or_b32 v90,v77,16,v76
v_lshl_or_b32 v91,v79,16,v78
s_nop 1
v_mfma_f32_16x16x32_bf16 a[24:27],v[88:91],v[64:67],a[24:27]
v_mfma_f32_16x16x32_bf16 a[56:59],v[88:91],v[80:83],a[56:59]
v_mfma_f32_16x16x32_bf16 a[88:91],v[88:91],v[96:99],a[88:91]
v_mfma_f32_16x16x32_bf16 a[120:123],v[88:91],v[112:115],a[120:123]
buffer_load_ushort v72,v9,s[24:27],s65 offen offset:224
buffer_load_ushort v73,v9,s[24:27],s65 offen offset:480
buffer_load_ushort v74,v9,s[24:27],s65 offen offset:736
buffer_load_ushort v75,v9,s[24:27],s65 offen offset:992
buffer_load_ushort v76,v10,s[24:27],s65 offen offset:224
buffer_load_ushort v77,v10,s[24:27],s65 offen offset:480
buffer_load_ushort v78,v10,s[24:27],s65 offen offset:736
buffer_load_ushort v79,v10,s[24:27],s65 offen offset:992
s_waitcnt vmcnt(0)
v_lshl_or_b32 v88,v73,16,v72
v_lshl_or_b32 v89,v75,16,v74
v_lshl_or_b32 v90,v77,16,v76
v_lshl_or_b32 v91,v79,16,v78
s_nop 1
v_mfma_f32_16x16x32_bf16 a[28:31],v[88:91],v[64:67],a[28:31]
v_mfma_f32_16x16x32_bf16 a[60:63],v[88:91],v[80:83],a[60:63]
v_mfma_f32_16x16x32_bf16 a[92:95],v[88:91],v[96:99],a[92:95]
v_mfma_f32_16x16x32_bf16 a[124:127],v[88:91],v[112:115],a[124:127]
v_mfma_f32_16x16x32_bf16 v[48:51],v[28:31],v[64:67],v[48:51]
v_mfma_f32_16x16x32_bf16 v[52:55],v[28:31],v[80:83],v[52:55]
v_mfma_f32_16x16x32_bf16 v[56:59],v[28:31],v[96:99],v[56:59]
v_mfma_f32_16x16x32_bf16 v[60:63],v[28:31],v[112:115],v[60:63]
s_add_u32 s65,s65,8192
s_sub_u32 s66,s66,1
s_cmp_lg_u32 s66,0
s_cbranch_scc1 L_s20_%=
s_nop 7
v_max_f32 v24,0x00800000,v48
v_rcp_f32 v24,v24
v_max_f32 v25,0x00800000,v52
v_rcp_f32 v25,v25
v_max_f32 v26,0x00800000,v56
v_rcp_f32 v26,v26
v_max_f32 v27,0x00800000,v60
v_rcp_f32 v27,v27
v_accvgpr_read_b32 v8,a0
v_accvgpr_read_b32 v9,a1
v_accvgpr_read_b32 v10,a2
v_accvgpr_read_b32 v11,a3
v_accvgpr_read_b32 v12,a4
v_accvgpr_read_b32 v13,a5
v_accvgpr_read_b32 v14,a6
v_accvgpr_read_b32 v15,a7
v_mul_f32 v8,v8,v24
v_mul_f32 v9,v9,v24
v_mul_f32 v10,v10,v24
v_mul_f32 v11,v11,v24
v_mul_f32 v12,v12,v24
v_mul_f32 v13,v13,v24
v_mul_f32 v14,v14,v24
v_mul_f32 v15,v15,v24
)A"
R"A(v_cvt_pk_bf16_f32 v8,v8,v9
v_cvt_pk_bf16_f32 v9,v10,v11
v_cvt_pk_bf16_f32 v10,v12,v13
v_cvt_pk_bf16_f32 v11,v14,v15
ds_write_b128 v7,v[8:11] offset:6144
v_accvgpr_read_b32 v16,a8
v_accvgpr_read_b32 v17,a9
v_accvgpr_read_b32 v18,a10
v_accvgpr_read_b32 v19,a11
v_accvgpr_read_b32 v20,a12
v_accvgpr_read_b32 v21,a13
v_accvgpr_read_b32 v22,a14
v_accvgpr_read_b32 v23,a15
v_mul_f32 v16,v16,v24
v_mul_f32 v17,v17,v24
v_mul_f32 v18,v18,v24
v_mul_f32 v19,v19,v24
v_mul_f32 v20,v20,v24
v_mul_f32 v21,v21,v24
v_mul_f32 v22,v22,v24
v_mul_f32 v23,v23,v24
v_cvt_pk_bf16_f32 v16,v16,v17
v_cvt_pk_bf16_f32 v17,v18,v19
v_cvt_pk_bf16_f32 v18,v20,v21
v_cvt_pk_bf16_f32 v19,v22,v23
ds_write_b128 v7,v[16:19] offset:7168
v_accvgpr_read_b32 v8,a16
v_accvgpr_read_b32 v9,a17
v_accvgpr_read_b32 v10,a18
v_accvgpr_read_b32 v11,a19
v_accvgpr_read_b32 v12,a20
v_accvgpr_read_b32 v13,a21
v_accvgpr_read_b32 v14,a22
v_accvgpr_read_b32 v15,a23
v_mul_f32 v8,v8,v24
v_mul_f32 v9,v9,v24
v_mul_f32 v10,v10,v24
v_mul_f32 v11,v11,v24
v_mul_f32 v12,v12,v24
v_mul_f32 v13,v13,v24
v_mul_f32 v14,v14,v24
v_mul_f32 v15,v15,v24
v_cvt_pk_bf16_f32 v8,v8,v9
v_cvt_pk_bf16_f32 v9,v10,v11
v_cvt_pk_bf16_f32 v10,v12,v13
v_cvt_pk_bf16_f32 v11,v14,v15
ds_write_b128 v7,v[8:11]
v_accvgpr_read_b32 v16,a24
v_accvgpr_read_b32 v17,a25
v_accvgpr_read_b32 v18,a26
v_accvgpr_read_b32 v19,a27
v_accvgpr_read_b32 v20,a28
v_accvgpr_read_b32 v21,a29
v_accvgpr_read_b32 v22,a30
v_accvgpr_read_b32 v23,a31
v_mul_f32 v16,v16,v24
v_mul_f32 v17,v17,v24
v_mul_f32 v18,v18,v24
v_mul_f32 v19,v19,v24
v_mul_f32 v20,v20,v24
v_mul_f32 v21,v21,v24
v_mul_f32 v22,v22,v24
v_mul_f32 v23,v23,v24
v_cvt_pk_bf16_f32 v16,v16,v17
v_cvt_pk_bf16_f32 v17,v18,v19
v_cvt_pk_bf16_f32 v18,v20,v21
v_cvt_pk_bf16_f32 v19,v22,v23
ds_write_b128 v7,v[16:19] offset:1024
v_accvgpr_read_b32 v32,a160
v_accvgpr_read_b32 v33,a161
v_accvgpr_read_b32 v34,a162
v_accvgpr_read_b32 v35,a163
v_accvgpr_read_b32 v36,a164
v_accvgpr_read_b32 v37,a165
v_accvgpr_read_b32 v38,a166
v_accvgpr_read_b32 v39,a167
v_accvgpr_read_b32 v40,a168
v_accvgpr_read_b32 v41,a169
v_accvgpr_read_b32 v42,a170
v_accvgpr_read_b32 v43,a171
v_accvgpr_read_b32 v44,a172
v_accvgpr_read_b32 v45,a173
v_accvgpr_read_b32 v46,a174
v_accvgpr_read_b32 v47,a175
s_waitcnt lgkmcnt(0)
v_subrev_u32 v8,s78,v1
ds_read_b128 a[128:131],v8
ds_read_b128 a[132:135],v8 offset:256
ds_read_b128 a[136:139],v8 offset:512
ds_read_b128 a[140:143],v8 offset:768
ds_read_b128 a[144:147],v8 offset:4096
ds_read_b128 a[148:151],v8 offset:4352
ds_read_b128 a[152:155],v8 offset:4608
ds_read_b128 a[156:159],v8 offset:4864
ds_read_b128 a[160:163],v8 offset:8192
ds_read_b128 a[164:167],v8 offset:8448
ds_read_b128 a[168:171],v8 offset:8704
ds_read_b128 a[172:175],v8 offset:8960
ds_read_b128 a[176:179],v8 offset:12288
ds_read_b128 a[180:183],v8 offset:12544
ds_read_b128 a[184:187],v8 offset:12800
ds_read_b128 a[188:191],v8 offset:13056
s_waitcnt lgkmcnt(15)
v_mfma_f32_16x16x32_bf16 v[64:67],a[128:131],v[128:131],v[32:35]
s_waitcnt lgkmcnt(14)
v_mfma_f32_16x16x32_bf16 v[64:67],a[132:135],v[136:139],v[64:67]
s_waitcnt lgkmcnt(13)
v_mfma_f32_16x16x32_bf16 v[64:67],a[136:139],v[144:147],v[64:67]
s_waitcnt lgkmcnt(12)
v_mfma_f32_16x16x32_bf16 v[64:67],a[140:143],v[152:155],v[64:67]
s_waitcnt lgkmcnt(8)
v_mfma_f32_16x16x32_bf16 v[68:71],a[156:159],v[152:155],v[32:35]
v_mfma_f32_16x16x32_bf16 v[68:71],a[152:155],v[144:147],v[68:71]
v_mfma_f32_16x16x32_bf16 v[68:71],a[148:151],v[136:139],v[68:71]
v_mfma_f32_16x16x32_bf16 v[68:71],a[144:147],v[128:131],v[68:71]
s_waitcnt lgkmcnt(0)
v_mfma_f32_16x16x32_bf16 v[72:75],a[160:163],v[128:131],v[32:35]
v_mfma_f32_16x16x32_bf16 v[72:75],a[164:167],v[136:139],v[72:75]
v_mfma_f32_16x16x32_bf16 v[72:75],a[168:171],v[144:147],v[72:75]
v_mfma_f32_16x16x32_bf16 v[72:75],a[172:175],v[152:155],v[72:75]
v_mfma_f32_16x16x32_bf16 v[76:79],a[188:191],v[152:155],v[32:35]
v_mfma_f32_16x16x32_bf16 v[76:79],a[184:187],v[144:147],v[76:79]
v_mfma_f32_16x16x32_bf16 v[76:79],a[180:183],v[136:139],v[76:79]
v_mfma_f32_16x16x32_bf16 v[76:79],a[176:179],v[128:131],v[76:79]
v_mfma_f32_16x16x32_bf16 v[80:83],a[128:131],v[160:163],v[36:39]
v_mfma_f32_16x16x32_bf16 v[80:83],a[132:135],v[168:171],v[80:83]
v_mfma_f32_16x16x32_bf16 v[80:83],a[136:139],v[176:179],v[80:83]
v_mfma_f32_16x16x32_bf16 v[80:83],a[140:143],v[184:187],v[80:83]
v_mfma_f32_16x16x32_bf16 v[84:87],a[156:159],v[184:187],v[36:39]
v_mfma_f32_16x16x32_bf16 v[84:87],a[152:155],v[176:179],v[84:87]
v_mfma_f32_16x16x32_bf16 v[84:87],a[148:151],v[168:171],v[84:87]
v_mfma_f32_16x16x32_bf16 v[84:87],a[144:147],v[160:163],v[84:87]
v_mfma_f32_16x16x32_bf16 v[88:91],a[160:163],v[160:163],v[36:39]
v_mfma_f32_16x16x32_bf16 v[88:91],a[164:167],v[168:171],v[88:91]
v_mfma_f32_16x16x32_bf16 v[88:91],a[168:171],v[176:179],v[88:91]
v_mfma_f32_16x16x32_bf16 v[88:91],a[172:175],v[184:187],v[88:91]
v_mfma_f32_16x16x32_bf16 v[92:95],a[188:191],v[184:187],v[36:39]
v_mfma_f32_16x16x32_bf16 v[92:95],a[184:187],v[176:179],v[92:95]
v_mfma_f32_16x16x32_bf16 v[92:95],a[180:183],v[168:171],v[92:95]
v_mfma_f32_16x16x32_bf16 v[92:95],a[176:179],v[160:163],v[92:95]
v_mfma_f32_16x16x32_bf16 v[96:99],a[128:131],v[192:195],v[40:43]
v_mfma_f32_16x16x32_bf16 v[96:99],a[132:135],v[200:203],v[96:99]
v_mfma_f32_16x16x32_bf16 v[96:99],a[136:139],v[208:211],v[96:99]
v_mfma_f32_16x16x32_bf16 v[96:99],a[140:143],v[216:219],v[96:99]
v_mfma_f32_16x16x32_bf16 v[100:103],a[156:159],v[216:219],v[40:43]
v_mfma_f32_16x16x32_bf16 v[100:103],a[152:155],v[208:211],v[100:103]
v_mfma_f32_16x16x32_bf16 v[100:103],a[148:151],v[200:203],v[100:103]
v_mfma_f32_16x16x32_bf16 v[100:103],a[144:147],v[192:195],v[100:103]
v_mfma_f32_16x16x32_bf16 v[104:107],a[160:163],v[192:195],v[40:43]
v_mfma_f32_16x16x32_bf16 v[104:107],a[164:167],v[200:203],v[104:107]
v_mfma_f32_16x16x32_bf16 v[104:107],a[168:171],v[208:211],v[104:107]
v_mfma_f32_16x16x32_bf16 v[104:107],a[172:175],v[216:219],v[104:107]
v_mfma_f32_16x16x32_bf16 v[108:111],a[188:191],v[216:219],v[40:43]
v_mfma_f32_16x16x32_bf16 v[108:111],a[184:187],v[208:211],v[108:111]
v_mfma_f32_16x16x32_bf16 v[108:111],a[180:183],v[200:203],v[108:111]
v_mfma_f32_16x16x32_bf16 v[108:111],a[176:179],v[192:195],v[108:111]
v_exp_f32 v64,v64
v_exp_f32 v65,v65
s_nop 0
v_cvt_pk_bf16_f32 v64,v64,v65
v_exp_f32 v66,v66
v_exp_f32 v67,v67
s_nop 0
v_cvt_pk_bf16_f32 v65,v66,v67
v_exp_f32 v68,v68
v_exp_f32 v69,v69
s_nop 0
v_cvt_pk_bf16_f32 v66,v68,v69
v_exp_f32 v70,v70
v_exp_f32 v71,v71
s_nop 0
v_cvt_pk_bf16_f32 v67,v70,v71
v_exp_f32 v72,v72
v_exp_f32 v73,v73
s_nop 0
v_cvt_pk_bf16_f32 v72,v72,v73
v_exp_f32 v74,v74
v_exp_f32 v75,v75
s_nop 0
v_cvt_pk_bf16_f32 v73,v74,v75
v_exp_f32 v76,v76
v_exp_f32 v77,v77
s_nop 0
v_cvt_pk_bf16_f32 v74,v76,v77
v_exp_f32 v78,v78
v_exp_f32 v79,v79
s_nop 0
v_cvt_pk_bf16_f32 v75,v78,v79
v_exp_f32 v80,v80
v_exp_f32 v81,v81
s_nop 0
v_cvt_pk_bf16_f32 v80,v80,v81
v_exp_f32 v82,v82
v_exp_f32 v83,v83
s_nop 0
v_cvt_pk_bf16_f32 v81,v82,v83
v_exp_f32 v84,v84
v_exp_f32 v85,v85
s_nop 0
v_cvt_pk_bf16_f32 v82,v84,v85
v_exp_f32 v86,v86
v_exp_f32 v87,v87
s_nop 0
v_cvt_pk_bf16_f32 v83,v86,v87
v_exp_f32 v88,v88
v_exp_f32 v89,v89
s_nop 0
v_cvt_pk_bf16_f32 v88,v88,v89
v_mfma_f32_16x16x32_bf16 a[0:3],a[192:195],v[64:67],0
v_mfma_f32_16x16x32_bf16 a[0:3],a[196:199],v[72:75],a[0:3]
v_mfma_f32_16x16x32_bf16 a[4:7],a[200:203],v[64:67],0
v_mfma_f32_16x16x32_bf16 a[4:7],a[204:207],v[72:75],a[4:7]
v_mfma_f32_16x16x32_bf16 a[8:11],a[208:211],v[64:67],0
v_mfma_f32_16x16x32_bf16 a[8:11],a[212:215],v[72:75],a[8:11]
v_mfma_f32_16x16x32_bf16 a[12:15],a[216:219],v[64:67],0
v_mfma_f32_16x16x32_bf16 a[12:15],a[220:223],v[72:75],a[12:15]
v_mfma_f32_16x16x32_bf16 a[16:19],a[224:227],v[64:67],0
v_mfma_f32_16x16x32_bf16 a[16:19],a[228:231],v[72:75],a[16:19]
v_mfma_f32_16x16x32_bf16 a[20:23],a[232:235],v[64:67],0
v_mfma_f32_16x16x32_bf16 a[20:23],a[236:239],v[72:75],a[20:23]
v_mfma_f32_16x16x32_bf16 a[24:27],a[240:243],v[64:67],0
v_mfma_f32_16x16x32_bf16 a[24:27],a[244:247],v[72:75],a[24:27]
v_mfma_f32_16x16x32_bf16 a[28:31],a[248:251],v[64:67],0
v_mfma_f32_16x16x32_bf16 a[28:31],a[252:255],v[72:75],a[28:31]
v_mfma_f32_16x16x32_bf16 v[48:51],v[28:31],v[64:67],0
v_mfma_f32_16x16x32_bf16 v[48:51],v[28:31],v[72:75],v[48:51]
s_waitcnt vmcnt(0) lgkmcnt(0)
s_nop 7
s_branch L_ok0_%=
L_slow1_%=:
s_waitcnt vmcnt(0) lgkmcnt(0)
s_nop 7
v_bfe_u32 v11,v6,4,4
v_lshrrev_b32 v12,8,v6
v_lshlrev_b32 v8,8,v11
v_lshl_add_u32 v8,v12,4,v8
v_lshlrev_b32 v9,10,v12
v_lshl_add_u32 v9,v11,1,v9
v_add_u32 v10,4096,v9
v_mov_b32 v48,0xff800000
v_mov_b32 v52,0xff800000
v_mov_b32 v56,0xff800000
v_mov_b32 v60,0xff800000
s_mov_b32 s65,262144
s_mov_b32 s66,64
L_s11_%=:
buffer_load_dwordx4 a[128:131],v8,s[20:23],s65 offen
buffer_load_dwordx4 a[132:135],v8,s[20:23],s65 offen offset:64
buffer_load_dwordx4 a[136:139],v8,s[20:23],s65 offen offset:128
buffer_load_dwordx4 a[140:143],v8,s[20:23],s65 offen offset:192
s_waitcnt vmcnt(0)
v_mfma_f32_16x16x32_bf16 v[64:67],a[128:131],v[128:131],0
v_mfma_f32_16x16x32_bf16 v[64:67],a[132:135],v[136:139],v[64:67]
v_mfma_f32_16x16x32_bf16 v[64:67],a[136:139],v[144:147],v[64:67]
v_mfma_f32_16x16x32_bf16 v[64:67],a[140:143],v[152:155],v[64:67]
v_mfma_f32_16x16x32_bf16 v[80:83],a[128:131],v[160:163],0
v_mfma_f32_16x16x32_bf16 v[80:83],a[132:135],v[168:171],v[80:83]
v_mfma_f32_16x16x32_bf16 v[80:83],a[136:139],v[176:179],v[80:83]
v_mfma_f32_16x16x32_bf16 v[80:83],a[140:143],v[184:187],v[80:83]
v_mfma_f32_16x16x32_bf16 v[96:99],a[128:131],v[192:195],0
v_mfma_f32_16x16x32_bf16 v[96:99],a[132:135],v[200:203],v[96:99]
v_mfma_f32_16x16x32_bf16 v[96:99],a[136:139],v[208:211],v[96:99]
v_mfma_f32_16x16x32_bf16 v[96:99],a[140:143],v[216:219],v[96:99]
v_mfma_f32_16x16x32_bf16 v[112:115],a[128:131],v[224:227],0
v_mfma_f32_16x16x32_bf16 v[112:115],a[132:135],v[232:235],v[112:115]
v_mfma_f32_16x16x32_bf16 v[112:115],a[136:139],v[240:243],v[112:115]
v_mfma_f32_16x16x32_bf16 v[112:115],a[140:143],v[248:251],v[112:115]
s_nop 7
v_max3_f32 v48,v48,v64,v65
v_max3_f32 v48,v48,v66,v67
v_max3_f32 v52,v52,v80,v81
v_max3_f32 v52,v52,v82,v83
v_max3_f32 v56,v56,v96,v97
v_max3_f32 v56,v56,v98,v99
v_max3_f32 v60,v60,v112,v113
v_max3_f32 v60,v60,v114,v115
s_add_u32 s65,s65,4096
s_sub_u32 s66,s66,1
s_cmp_lg_u32 s66,0
s_cbranch_scc1 L_s11_%=
v_mov_b32 v11,v48
s_nop 1
v_permlane32_swap_b32 v48,v11
v_max_f32 v48,v48,v11
v_mov_b32 v11,v48
s_nop 1
v_permlane16_swap_b32 v48,v11
v_max_f32 v48,v48,v11
v_sub_f32 v32,0,v48
v_sub_f32 v33,0,v48
v_sub_f32 v34,0,v48
v_sub_f32 v35,0,v48
v_mov_b32 v11,v52
s_nop 1
v_permlane32_swap_b32 v52,v11
v_max_f32 v52,v52,v11
v_mov_b32 v11,v52
s_nop 1
v_permlane16_swap_b32 v52,v11
v_max_f32 v52,v52,v11
v_sub_f32 v36,0,v52
v_sub_f32 v37,0,v52
v_sub_f32 v38,0,v52
v_sub_f32 v39,0,v52
v_mov_b32 v11,v56
s_nop 1
v_permlane32_swap_b32 v56,v11
v_max_f32 v56,v56,v11
v_mov_b32 v11,v56
s_nop 1
v_permlane16_swap_b32 v56,v11
v_max_f32 v56,v56,v11
v_sub_f32 v40,0,v56
v_sub_f32 v41,0,v56
v_sub_f32 v42,0,v56
v_sub_f32 v43,0,v56
v_mov_b32 v11,v60
s_nop 1
v_permlane32_swap_b32 v60,v11
v_max_f32 v60,v60,v11
v_mov_b32 v11,v60
s_nop 1
v_permlane16_swap_b32 v60,v11
v_max_f32 v60,v60,v11
v_max_f32 v60,s72,v60
v_sub_f32 v44,0,v60
v_sub_f32 v45,0,v60
v_sub_f32 v46,0,v60
v_sub_f32 v47,0,v60
v_mov_b32 v48,0
v_mov_b32 v49,0
v_mov_b32 v50,0
v_mov_b32 v51,0
v_mov_b32 v52,0
v_mov_b32 v53,0
v_mov_b32 v54,0
v_mov_b32 v55,0
v_mov_b32 v56,0
v_mov_b32 v57,0
v_mov_b32 v58,0
v_mov_b32 v59,0
v_mov_b32 v60,0
v_mov_b32 v61,0
v_mov_b32 v62,0
v_mov_b32 v63,0
v_accvgpr_write_b32 a0,0
v_accvgpr_write_b32 a1,0
v_accvgpr_write_b32 a2,0
v_accvgpr_write_b32 a3,0
v_accvgpr_write_b32 a4,0
v_accvgpr_write_b32 a5,0
v_accvgpr_write_b32 a6,0
v_accvgpr_write_b32 a7,0
v_accvgpr_write_b32 a8,0
v_accvgpr_write_b32 a9,0
v_accvgpr_write_b32 a10,0
v_accvgpr_write_b32 a11,0
v_accvgpr_write_b32 a12,0
v_accvgpr_write_b32 a13,0
v_accvgpr_write_b32 a14,0
v_accvgpr_write_b32 a15,0
v_accvgpr_write_b32 a16,0
v_accvgpr_write_b32 a17,0
v_accvgpr_write_b32 a18,0
v_accvgpr_write_b32 a19,0
v_accvgpr_write_b32 a20,0
v_accvgpr_write_b32 a21,0
v_accvgpr_write_b32 a22,0
)A"
R"A(v_accvgpr_write_b32 a23,0
v_accvgpr_write_b32 a24,0
v_accvgpr_write_b32 a25,0
v_accvgpr_write_b32 a26,0
v_accvgpr_write_b32 a27,0
v_accvgpr_write_b32 a28,0
v_accvgpr_write_b32 a29,0
v_accvgpr_write_b32 a30,0
v_accvgpr_write_b32 a31,0
v_mov_b32 v132,0
v_mov_b32 v133,0
v_mov_b32 v134,0
v_mov_b32 v135,0
v_mov_b32 v140,0
v_mov_b32 v141,0
v_mov_b32 v142,0
v_mov_b32 v143,0
v_mov_b32 v148,0
v_mov_b32 v149,0
v_mov_b32 v150,0
v_mov_b32 v151,0
v_mov_b32 v156,0
v_mov_b32 v157,0
v_mov_b32 v158,0
v_mov_b32 v159,0
v_mov_b32 v164,0
v_mov_b32 v165,0
v_mov_b32 v166,0
v_mov_b32 v167,0
v_mov_b32 v172,0
v_mov_b32 v173,0
v_mov_b32 v174,0
v_mov_b32 v175,0
v_mov_b32 v180,0
v_mov_b32 v181,0
v_mov_b32 v182,0
v_mov_b32 v183,0
v_mov_b32 v188,0
v_mov_b32 v189,0
v_mov_b32 v190,0
v_mov_b32 v191,0
v_mov_b32 v196,0
v_mov_b32 v197,0
v_mov_b32 v198,0
v_mov_b32 v199,0
v_mov_b32 v204,0
v_mov_b32 v205,0
v_mov_b32 v206,0
v_mov_b32 v207,0
v_mov_b32 v212,0
v_mov_b32 v213,0
v_mov_b32 v214,0
v_mov_b32 v215,0
v_mov_b32 v220,0
v_mov_b32 v221,0
v_mov_b32 v222,0
v_mov_b32 v223,0
v_mov_b32 v228,0
v_mov_b32 v229,0
v_mov_b32 v230,0
v_mov_b32 v231,0
v_mov_b32 v236,0
v_mov_b32 v237,0
v_mov_b32 v238,0
v_mov_b32 v239,0
v_mov_b32 v244,0
v_mov_b32 v245,0
v_mov_b32 v246,0
v_mov_b32 v247,0
v_mov_b32 v252,0
v_mov_b32 v253,0
v_mov_b32 v254,0
v_mov_b32 v255,0
v_accvgpr_write_b32 a32,0
v_accvgpr_write_b32 a33,0
v_accvgpr_write_b32 a34,0
v_accvgpr_write_b32 a35,0
v_accvgpr_write_b32 a36,0
v_accvgpr_write_b32 a37,0
v_accvgpr_write_b32 a38,0
v_accvgpr_write_b32 a39,0
v_accvgpr_write_b32 a40,0
v_accvgpr_write_b32 a41,0
v_accvgpr_write_b32 a42,0
v_accvgpr_write_b32 a43,0
v_accvgpr_write_b32 a44,0
v_accvgpr_write_b32 a45,0
v_accvgpr_write_b32 a46,0
v_accvgpr_write_b32 a47,0
v_accvgpr_write_b32 a48,0
v_accvgpr_write_b32 a49,0
v_accvgpr_write_b32 a50,0
v_accvgpr_write_b32 a51,0
v_accvgpr_write_b32 a52,0
v_accvgpr_write_b32 a53,0
v_accvgpr_write_b32 a54,0
v_accvgpr_write_b32 a55,0
v_accvgpr_write_b32 a56,0
v_accvgpr_write_b32 a57,0
v_accvgpr_write_b32 a58,0
v_accvgpr_write_b32 a59,0
v_accvgpr_write_b32 a60,0
v_accvgpr_write_b32 a61,0
v_accvgpr_write_b32 a62,0
v_accvgpr_write_b32 a63,0
s_mov_b32 s65,262144
s_mov_b32 s66,32
L_s21_%=:
s_add_u32 s67,s65,4096
buffer_load_dwordx4 a[128:131],v8,s[20:23],s65 offen
buffer_load_dwordx4 a[132:135],v8,s[20:23],s65 offen offset:64
buffer_load_dwordx4 a[136:139],v8,s[20:23],s65 offen offset:128
buffer_load_dwordx4 a[140:143],v8,s[20:23],s65 offen offset:192
buffer_load_dwordx4 a[144:147],v8,s[20:23],s67 offen
buffer_load_dwordx4 a[148:151],v8,s[20:23],s67 offen offset:64
buffer_load_dwordx4 a[152:155],v8,s[20:23],s67 offen offset:128
buffer_load_dwordx4 a[156:159],v8,s[20:23],s67 offen offset:192
s_waitcnt vmcnt(0)
v_mfma_f32_16x16x32_bf16 v[64:67],a[128:131],v[128:131],v[32:35]
v_mfma_f32_16x16x32_bf16 v[64:67],a[132:135],v[136:139],v[64:67]
v_mfma_f32_16x16x32_bf16 v[64:67],a[136:139],v[144:147],v[64:67]
v_mfma_f32_16x16x32_bf16 v[64:67],a[140:143],v[152:155],v[64:67]
v_mfma_f32_16x16x32_bf16 v[68:71],a[144:147],v[128:131],v[32:35]
v_mfma_f32_16x16x32_bf16 v[68:71],a[148:151],v[136:139],v[68:71]
v_mfma_f32_16x16x32_bf16 v[68:71],a[152:155],v[144:147],v[68:71]
v_mfma_f32_16x16x32_bf16 v[68:71],a[156:159],v[152:155],v[68:71]
v_mfma_f32_16x16x32_bf16 v[80:83],a[128:131],v[160:163],v[36:39]
v_mfma_f32_16x16x32_bf16 v[80:83],a[132:135],v[168:171],v[80:83]
v_mfma_f32_16x16x32_bf16 v[80:83],a[136:139],v[176:179],v[80:83]
v_mfma_f32_16x16x32_bf16 v[80:83],a[140:143],v[184:187],v[80:83]
v_mfma_f32_16x16x32_bf16 v[84:87],a[144:147],v[160:163],v[36:39]
v_mfma_f32_16x16x32_bf16 v[84:87],a[148:151],v[168:171],v[84:87]
v_mfma_f32_16x16x32_bf16 v[84:87],a[152:155],v[176:179],v[84:87]
v_mfma_f32_16x16x32_bf16 v[84:87],a[156:159],v[184:187],v[84:87]
v_mfma_f32_16x16x32_bf16 v[96:99],a[128:131],v[192:195],v[40:43]
v_mfma_f32_16x16x32_bf16 v[96:99],a[132:135],v[200:203],v[96:99]
v_mfma_f32_16x16x32_bf16 v[96:99],a[136:139],v[208:211],v[96:99]
v_mfma_f32_16x16x32_bf16 v[96:99],a[140:143],v[216:219],v[96:99]
v_mfma_f32_16x16x32_bf16 v[100:103],a[144:147],v[192:195],v[40:43]
v_mfma_f32_16x16x32_bf16 v[100:103],a[148:151],v[200:203],v[100:103]
v_mfma_f32_16x16x32_bf16 v[100:103],a[152:155],v[208:211],v[100:103]
v_mfma_f32_16x16x32_bf16 v[100:103],a[156:159],v[216:219],v[100:103]
v_mfma_f32_16x16x32_bf16 v[112:115],a[128:131],v[224:227],v[44:47]
v_mfma_f32_16x16x32_bf16 v[112:115],a[132:135],v[232:235],v[112:115]
v_mfma_f32_16x16x32_bf16 v[112:115],a[136:139],v[240:243],v[112:115]
v_mfma_f32_16x16x32_bf16 v[112:115],a[140:143],v[248:251],v[112:115]
v_mfma_f32_16x16x32_bf16 v[116:119],a[144:147],v[224:227],v[44:47]
v_mfma_f32_16x16x32_bf16 v[116:119],a[148:151],v[232:235],v[116:119]
v_mfma_f32_16x16x32_bf16 v[116:119],a[152:155],v[240:243],v[116:119]
v_mfma_f32_16x16x32_bf16 v[116:119],a[156:159],v[248:251],v[116:119]
s_nop 7
v_exp_f32 v64,v64
v_exp_f32 v65,v65
v_exp_f32 v66,v66
v_exp_f32 v67,v67
v_exp_f32 v68,v68
v_exp_f32 v69,v69
v_exp_f32 v70,v70
v_exp_f32 v71,v71
s_nop 0
v_cvt_pk_bf16_f32 v64,v64,v65
v_cvt_pk_bf16_f32 v65,v66,v67
v_cvt_pk_bf16_f32 v66,v68,v69
v_cvt_pk_bf16_f32 v67,v70,v71
v_exp_f32 v80,v80
v_exp_f32 v81,v81
v_exp_f32 v82,v82
v_exp_f32 v83,v83
v_exp_f32 v84,v84
v_exp_f32 v85,v85
v_exp_f32 v86,v86
v_exp_f32 v87,v87
s_nop 0
v_cvt_pk_bf16_f32 v80,v80,v81
v_cvt_pk_bf16_f32 v81,v82,v83
v_cvt_pk_bf16_f32 v82,v84,v85
v_cvt_pk_bf16_f32 v83,v86,v87
v_exp_f32 v96,v96
v_exp_f32 v97,v97
v_exp_f32 v98,v98
v_exp_f32 v99,v99
v_exp_f32 v100,v100
v_exp_f32 v101,v101
v_exp_f32 v102,v102
v_exp_f32 v103,v103
s_nop 0
v_cvt_pk_bf16_f32 v96,v96,v97
v_cvt_pk_bf16_f32 v97,v98,v99
v_cvt_pk_bf16_f32 v98,v100,v101
v_cvt_pk_bf16_f32 v99,v102,v103
v_exp_f32 v112,v112
v_exp_f32 v113,v113
v_exp_f32 v114,v114
v_exp_f32 v115,v115
v_exp_f32 v116,v116
v_exp_f32 v117,v117
v_exp_f32 v118,v118
v_exp_f32 v119,v119
s_nop 0
v_cvt_pk_bf16_f32 v112,v112,v113
v_cvt_pk_bf16_f32 v113,v114,v115
v_cvt_pk_bf16_f32 v114,v116,v117
v_cvt_pk_bf16_f32 v115,v118,v119
buffer_load_ushort v72,v9,s[24:27],s65 offen
buffer_load_ushort v73,v9,s[24:27],s65 offen offset:256
buffer_load_ushort v74,v9,s[24:27],s65 offen offset:512
buffer_load_ushort v75,v9,s[24:27],s65 offen offset:768
buffer_load_ushort v76,v10,s[24:27],s65 offen
buffer_load_ushort v77,v10,s[24:27],s65 offen offset:256
buffer_load_ushort v78,v10,s[24:27],s65 offen offset:512
buffer_load_ushort v79,v10,s[24:27],s65 offen offset:768
s_waitcnt vmcnt(0)
v_lshl_or_b32 v88,v73,16,v72
v_lshl_or_b32 v89,v75,16,v74
v_lshl_or_b32 v90,v77,16,v76
v_lshl_or_b32 v91,v79,16,v78
s_nop 1
v_mfma_f32_16x16x32_bf16 a[0:3],v[88:91],v[64:67],a[0:3]
v_mfma_f32_16x16x32_bf16 v[132:135],v[88:91],v[80:83],v[132:135]
v_mfma_f32_16x16x32_bf16 v[196:199],v[88:91],v[96:99],v[196:199]
v_mfma_f32_16x16x32_bf16 a[32:35],v[88:91],v[112:115],a[32:35]
buffer_load_ushort v72,v9,s[24:27],s65 offen offset:32
buffer_load_ushort v73,v9,s[24:27],s65 offen offset:288
buffer_load_ushort v74,v9,s[24:27],s65 offen offset:544
buffer_load_ushort v75,v9,s[24:27],s65 offen offset:800
buffer_load_ushort v76,v10,s[24:27],s65 offen offset:32
buffer_load_ushort v77,v10,s[24:27],s65 offen offset:288
buffer_load_ushort v78,v10,s[24:27],s65 offen offset:544
buffer_load_ushort v79,v10,s[24:27],s65 offen offset:800
s_waitcnt vmcnt(0)
v_lshl_or_b32 v88,v73,16,v72
v_lshl_or_b32 v89,v75,16,v74
v_lshl_or_b32 v90,v77,16,v76
v_lshl_or_b32 v91,v79,16,v78
s_nop 1
v_mfma_f32_16x16x32_bf16 a[4:7],v[88:91],v[64:67],a[4:7]
v_mfma_f32_16x16x32_bf16 v[140:143],v[88:91],v[80:83],v[140:143]
v_mfma_f32_16x16x32_bf16 v[204:207],v[88:91],v[96:99],v[204:207]
v_mfma_f32_16x16x32_bf16 a[36:39],v[88:91],v[112:115],a[36:39]
buffer_load_ushort v72,v9,s[24:27],s65 offen offset:64
buffer_load_ushort v73,v9,s[24:27],s65 offen offset:320
buffer_load_ushort v74,v9,s[24:27],s65 offen offset:576
buffer_load_ushort v75,v9,s[24:27],s65 offen offset:832
buffer_load_ushort v76,v10,s[24:27],s65 offen offset:64
buffer_load_ushort v77,v10,s[24:27],s65 offen offset:320
buffer_load_ushort v78,v10,s[24:27],s65 offen offset:576
buffer_load_ushort v79,v10,s[24:27],s65 offen offset:832
s_waitcnt vmcnt(0)
v_lshl_or_b32 v88,v73,16,v72
v_lshl_or_b32 v89,v75,16,v74
v_lshl_or_b32 v90,v77,16,v76
v_lshl_or_b32 v91,v79,16,v78
s_nop 1
v_mfma_f32_16x16x32_bf16 a[8:11],v[88:91],v[64:67],a[8:11]
v_mfma_f32_16x16x32_bf16 v[148:151],v[88:91],v[80:83],v[148:151]
v_mfma_f32_16x16x32_bf16 v[212:215],v[88:91],v[96:99],v[212:215]
v_mfma_f32_16x16x32_bf16 a[40:43],v[88:91],v[112:115],a[40:43]
buffer_load_ushort v72,v9,s[24:27],s65 offen offset:96
buffer_load_ushort v73,v9,s[24:27],s65 offen offset:352
buffer_load_ushort v74,v9,s[24:27],s65 offen offset:608
buffer_load_ushort v75,v9,s[24:27],s65 offen offset:864
buffer_load_ushort v76,v10,s[24:27],s65 offen offset:96
buffer_load_ushort v77,v10,s[24:27],s65 offen offset:352
buffer_load_ushort v78,v10,s[24:27],s65 offen offset:608
buffer_load_ushort v79,v10,s[24:27],s65 offen offset:864
s_waitcnt vmcnt(0)
v_lshl_or_b32 v88,v73,16,v72
v_lshl_or_b32 v89,v75,16,v74
v_lshl_or_b32 v90,v77,16,v76
v_lshl_or_b32 v91,v79,16,v78
s_nop 1
v_mfma_f32_16x16x32_bf16 a[12:15],v[88:91],v[64:67],a[12:15]
v_mfma_f32_16x16x32_bf16 v[156:159],v[88:91],v[80:83],v[156:159]
v_mfma_f32_16x16x32_bf16 v[220:223],v[88:91],v[96:99],v[220:223]
v_mfma_f32_16x16x32_bf16 a[44:47],v[88:91],v[112:115],a[44:47]
buffer_load_ushort v72,v9,s[24:27],s65 offen offset:128
buffer_load_ushort v73,v9,s[24:27],s65 offen offset:384
buffer_load_ushort v74,v9,s[24:27],s65 offen offset:640
buffer_load_ushort v75,v9,s[24:27],s65 offen offset:896
buffer_load_ushort v76,v10,s[24:27],s65 offen offset:128
buffer_load_ushort v77,v10,s[24:27],s65 offen offset:384
buffer_load_ushort v78,v10,s[24:27],s65 offen offset:640
buffer_load_ushort v79,v10,s[24:27],s65 offen offset:896
s_waitcnt vmcnt(0)
v_lshl_or_b32 v88,v73,16,v72
v_lshl_or_b32 v89,v75,16,v74
v_lshl_or_b32 v90,v77,16,v76
v_lshl_or_b32 v91,v79,16,v78
s_nop 1
v_mfma_f32_16x16x32_bf16 a[16:19],v[88:91],v[64:67],a[16:19]
v_mfma_f32_16x16x32_bf16 v[164:167],v[88:91],v[80:83],v[164:167]
v_mfma_f32_16x16x32_bf16 v[228:231],v[88:91],v[96:99],v[228:231]
v_mfma_f32_16x16x32_bf16 a[48:51],v[88:91],v[112:115],a[48:51]
buffer_load_ushort v72,v9,s[24:27],s65 offen offset:160
buffer_load_ushort v73,v9,s[24:27],s65 offen offset:416
buffer_load_ushort v74,v9,s[24:27],s65 offen offset:672
buffer_load_ushort v75,v9,s[24:27],s65 offen offset:928
buffer_load_ushort v76,v10,s[24:27],s65 offen offset:160
buffer_load_ushort v77,v10,s[24:27],s65 offen offset:416
buffer_load_ushort v78,v10,s[24:27],s65 offen offset:672
buffer_load_ushort v79,v10,s[24:27],s65 offen offset:928
s_waitcnt vmcnt(0)
v_lshl_or_b32 v88,v73,16,v72
v_lshl_or_b32 v89,v75,16,v74
v_lshl_or_b32 v90,v77,16,v76
v_lshl_or_b32 v91,v79,16,v78
s_nop 1
v_mfma_f32_16x16x32_bf16 a[20:23],v[88:91],v[64:67],a[20:23]
v_mfma_f32_16x16x32_bf16 v[172:175],v[88:91],v[80:83],v[172:175]
v_mfma_f32_16x16x32_bf16 v[236:239],v[88:91],v[96:99],v[236:239]
v_mfma_f32_16x16x32_bf16 a[52:55],v[88:91],v[112:115],a[52:55]
buffer_load_ushort v72,v9,s[24:27],s65 offen offset:192
buffer_load_ushort v73,v9,s[24:27],s65 offen offset:448
buffer_load_ushort v74,v9,s[24:27],s65 offen offset:704
buffer_load_ushort v75,v9,s[24:27],s65 offen offset:960
buffer_load_ushort v76,v10,s[24:27],s65 offen offset:192
buffer_load_ushort v77,v10,s[24:27],s65 offen offset:448
buffer_load_ushort v78,v10,s[24:27],s65 offen offset:704
buffer_load_ushort v79,v10,s[24:27],s65 offen offset:960
s_waitcnt vmcnt(0)
v_lshl_or_b32 v88,v73,16,v72
v_lshl_or_b32 v89,v75,16,v74
v_lshl_or_b32 v90,v77,16,v76
v_lshl_or_b32 v91,v79,16,v78
s_nop 1
v_mfma_f32_16x16x32_bf16 a[24:27],v[88:91],v[64:67],a[24:27]
v_mfma_f32_16x16x32_bf16 v[180:183],v[88:91],v[80:83],v[180:183]
v_mfma_f32_16x16x32_bf16 v[244:247],v[88:91],v[96:99],v[244:247]
v_mfma_f32_16x16x32_bf16 a[56:59],v[88:91],v[112:115],a[56:59]
buffer_load_ushort v72,v9,s[24:27],s65 offen offset:224
buffer_load_ushort v73,v9,s[24:27],s65 offen offset:480
)A"
R"A(buffer_load_ushort v74,v9,s[24:27],s65 offen offset:736
buffer_load_ushort v75,v9,s[24:27],s65 offen offset:992
buffer_load_ushort v76,v10,s[24:27],s65 offen offset:224
buffer_load_ushort v77,v10,s[24:27],s65 offen offset:480
buffer_load_ushort v78,v10,s[24:27],s65 offen offset:736
buffer_load_ushort v79,v10,s[24:27],s65 offen offset:992
s_waitcnt vmcnt(0)
v_lshl_or_b32 v88,v73,16,v72
v_lshl_or_b32 v89,v75,16,v74
v_lshl_or_b32 v90,v77,16,v76
v_lshl_or_b32 v91,v79,16,v78
s_nop 1
v_mfma_f32_16x16x32_bf16 a[28:31],v[88:91],v[64:67],a[28:31]
v_mfma_f32_16x16x32_bf16 v[188:191],v[88:91],v[80:83],v[188:191]
v_mfma_f32_16x16x32_bf16 v[252:255],v[88:91],v[96:99],v[252:255]
v_mfma_f32_16x16x32_bf16 a[60:63],v[88:91],v[112:115],a[60:63]
v_mfma_f32_16x16x32_bf16 v[48:51],v[28:31],v[64:67],v[48:51]
v_mfma_f32_16x16x32_bf16 v[52:55],v[28:31],v[80:83],v[52:55]
v_mfma_f32_16x16x32_bf16 v[56:59],v[28:31],v[96:99],v[56:59]
v_mfma_f32_16x16x32_bf16 v[60:63],v[28:31],v[112:115],v[60:63]
s_add_u32 s65,s65,8192
s_sub_u32 s66,s66,1
s_cmp_lg_u32 s66,0
s_cbranch_scc1 L_s21_%=
s_nop 7
v_max_f32 v49,0x00800000,v48
v_rcp_f32 v49,v49
v_max_f32 v53,0x00800000,v52
v_rcp_f32 v53,v53
v_max_f32 v57,0x00800000,v56
v_rcp_f32 v57,v57
v_max_f32 v61,0x00800000,v60
v_rcp_f32 v61,v61
v_accvgpr_read_b32 v16,a0
v_accvgpr_read_b32 v17,a1
v_accvgpr_read_b32 v18,a2
v_accvgpr_read_b32 v19,a3
v_accvgpr_read_b32 v20,a4
v_accvgpr_read_b32 v21,a5
v_accvgpr_read_b32 v22,a6
v_accvgpr_read_b32 v23,a7
v_mul_f32 v16,v16,v49
v_mul_f32 v17,v17,v49
v_mul_f32 v18,v18,v49
v_mul_f32 v19,v19,v49
v_mul_f32 v20,v20,v49
v_mul_f32 v21,v21,v49
v_mul_f32 v22,v22,v49
v_mul_f32 v23,v23,v49
v_cvt_pk_bf16_f32 v32,v16,v17
v_cvt_pk_bf16_f32 v33,v18,v19
v_cvt_pk_bf16_f32 v34,v20,v21
v_cvt_pk_bf16_f32 v35,v22,v23
v_accvgpr_read_b32 v8,a8
v_accvgpr_read_b32 v9,a9
v_accvgpr_read_b32 v10,a10
v_accvgpr_read_b32 v11,a11
v_accvgpr_read_b32 v12,a12
v_accvgpr_read_b32 v13,a13
v_accvgpr_read_b32 v14,a14
v_accvgpr_read_b32 v15,a15
v_mul_f32 v8,v8,v49
v_mul_f32 v9,v9,v49
v_mul_f32 v10,v10,v49
v_mul_f32 v11,v11,v49
v_mul_f32 v12,v12,v49
v_mul_f32 v13,v13,v49
v_mul_f32 v14,v14,v49
v_mul_f32 v15,v15,v49
v_cvt_pk_bf16_f32 v36,v8,v9
v_cvt_pk_bf16_f32 v37,v10,v11
v_cvt_pk_bf16_f32 v38,v12,v13
v_cvt_pk_bf16_f32 v39,v14,v15
v_accvgpr_read_b32 v16,a16
v_accvgpr_read_b32 v17,a17
v_accvgpr_read_b32 v18,a18
v_accvgpr_read_b32 v19,a19
v_accvgpr_read_b32 v20,a20
v_accvgpr_read_b32 v21,a21
v_accvgpr_read_b32 v22,a22
v_accvgpr_read_b32 v23,a23
v_mul_f32 v16,v16,v49
v_mul_f32 v17,v17,v49
v_mul_f32 v18,v18,v49
v_mul_f32 v19,v19,v49
v_mul_f32 v20,v20,v49
v_mul_f32 v21,v21,v49
v_mul_f32 v22,v22,v49
v_mul_f32 v23,v23,v49
v_cvt_pk_bf16_f32 v40,v16,v17
v_cvt_pk_bf16_f32 v41,v18,v19
v_cvt_pk_bf16_f32 v42,v20,v21
v_cvt_pk_bf16_f32 v43,v22,v23
ds_read_b64_tr_b16 v[64:65],v1
ds_read_b64_tr_b16 v[66:67],v1 offset:5120
ds_read_b64_tr_b16 v[68:69],v1 offset:10240
ds_read_b64_tr_b16 v[70:71],v1 offset:15360
ds_read_b64_tr_b16 v[72:73],v1 offset:20480
ds_read_b64_tr_b16 v[74:75],v1 offset:25600
ds_read_b64_tr_b16 v[76:77],v1 offset:30720
ds_read_b64_tr_b16 v[78:79],v1 offset:35840
ds_read_b64_tr_b16 v[80:81],v2 offset:32
ds_read_b64_tr_b16 v[82:83],v2 offset:5152
ds_read_b64_tr_b16 v[84:85],v2 offset:10272
ds_read_b64_tr_b16 v[86:87],v2 offset:15392
ds_read_b64_tr_b16 v[88:89],v2 offset:20512
ds_read_b64_tr_b16 v[90:91],v2 offset:25632
ds_read_b64_tr_b16 v[92:93],v2 offset:30752
ds_read_b64_tr_b16 v[94:95],v2 offset:35872
s_waitcnt vmcnt(0) lgkmcnt(0)
s_nop 7
s_branch L_ok1_%=
L_end_%=:
)A"
    :: "s"(ka), "s"(blk), "s"(wid)
    : "v0", "v1", "v2", "v3", "v4", "v5", "v6", "v7", "v8", "v9", "v10", "v11", "v12", "v13", "v14", "v15", "v16", "v17", "v18", "v19", "v20", "v21", "v22", "v23", "v24", "v25", "v26", "v27", "v28", "v29", "v30", "v31", "v32", "v33", "v34", "v35", "v36", "v37", "v38", "v39", "v40", "v41", "v42", "v43", "v44", "v45", "v46", "v47", "v48", "v49", "v50", "v51", "v52", "v53", "v54", "v55", "v56", "v57", "v58", "v59", "v60", "v61", "v62", "v63", "v64", "v65", "v66", "v67", "v68", "v69", "v70", "v71", "v72", "v73", "v74", "v75", "v76", "v77", "v78", "v79", "v80", "v81", "v82", "v83", "v84", "v85", "v86", "v87", "v88", "v89", "v90", "v91", "v92", "v93", "v94", "v95", "v96", "v97", "v98", "v99", "v100", "v101", "v102", "v103", "v104", "v105", "v106", "v107", "v108", "v109", "v110", "v111", "v112", "v113", "v114", "v115", "v116", "v117", "v118", "v119", "v120", "v121", "v122", "v123", "v124", "v125", "v126", "v127", "v128", "v129", "v130", "v131", "v132", "v133", "v134", "v135", "v136", "v137", "v138", "v139", "v140", "v141", "v142", "v143", "v144", "v145", "v146", "v147", "v148", "v149", "v150", "v151", "v152", "v153", "v154", "v155", "v156", "v157", "v158", "v159", "v160", "v161", "v162", "v163", "v164", "v165", "v166", "v167", "v168", "v169", "v170", "v171", "v172", "v173", "v174", "v175", "v176", "v177", "v178", "v179", "v180", "v181", "v182", "v183", "v184", "v185", "v186", "v187", "v188", "v189", "v190", "v191", "v192", "v193", "v194", "v195", "v196", "v197", "v198", "v199", "v200", "v201", "v202", "v203", "v204", "v205", "v206", "v207", "v208", "v209", "v210", "v211", "v212", "v213", "v214", "v215", "v216", "v217", "v218", "v219", "v220", "v221", "v222", "v223", "v224", "v225", "v226", "v227", "v228", "v229", "v230", "v231", "v232", "v233", "v234", "v235", "v236", "v237", "v238", "v239", "v240", "v241", "v242", "v243", "v244", "v245", "v246", "v247", "v248", "v249", "v250", "v251", "v252", "v253", "v254", "v255", "a0", "a1", "a2", "a3", "a4", "a5", "a6", "a7", "a8", "a9", "a10", "a11", "a12", "a13", "a14", "a15", "a16", "a17", "a18", "a19", "a20", "a21", "a22", "a23", "a24", "a25", "a26", "a27", "a28", "a29", "a30", "a31", "a32", "a33", "a34", "a35", "a36", "a37", "a38", "a39", "a40", "a41", "a42", "a43", "a44", "a45", "a46", "a47", "a48", "a49", "a50", "a51", "a52", "a53", "a54", "a55", "a56", "a57", "a58", "a59", "a60", "a61", "a62", "a63", "a64", "a65", "a66", "a67", "a68", "a69", "a70", "a71", "a72", "a73", "a74", "a75", "a76", "a77", "a78", "a79", "a80", "a81", "a82", "a83", "a84", "a85", "a86", "a87", "a88", "a89", "a90", "a91", "a92", "a93", "a94", "a95", "a96", "a97", "a98", "a99", "a100", "a101", "a102", "a103", "a104", "a105", "a106", "a107", "a108", "a109", "a110", "a111", "a112", "a113", "a114", "a115", "a116", "a117", "a118", "a119", "a120", "a121", "a122", "a123", "a124", "a125", "a126", "a127", "a128", "a129", "a130", "a131", "a132", "a133", "a134", "a135", "a136", "a137", "a138", "a139", "a140", "a141", "a142", "a143", "a144", "a145", "a146", "a147", "a148", "a149", "a150", "a151", "a152", "a153", "a154", "a155", "a156", "a157", "a158", "a159", "a160", "a161", "a162", "a163", "a164", "a165", "a166", "a167", "a168", "a169", "a170", "a171", "a172", "a173", "a174", "a175", "a176", "a177", "a178", "a179", "a180", "a181", "a182", "a183", "a184", "a185", "a186", "a187", "a188", "a189", "a190", "a191", "a192", "a193", "a194", "a195", "a196", "a197", "a198", "a199", "a200", "a201", "a202", "a203", "a204", "a205", "a206", "a207", "a208", "a209", "a210", "a211", "a212", "a213", "a214", "a215", "a216", "a217", "a218", "a219", "a220", "a221", "a222", "a223", "a224", "a225", "a226", "a227", "a228", "a229", "a230", "a231", "a232", "a233", "a234", "a235", "a236", "a237", "a238", "a239", "a240", "a241", "a242", "a243", "a244", "a245", "a246", "a247", "a248", "a249", "a250", "a251", "a252", "a253", "a254", "a255", "s4", "s5", "s6", "s7", "s8", "s9", "s10", "s11", "s12", "s13", "s14", "s15", "s16", "s17", "s18", "s19", "s20", "s21", "s22", "s23", "s24", "s25", "s26", "s27", "s28", "s29", "s30", "s31", "s36", "s37", "s38", "s39", "s40", "s41", "s42", "s43", "s44", "s45", "s46", "s47", "s48", "s49", "s50", "s51", "s52", "s53", "s54", "s55", "s56", "s57", "s58", "s59", "s60", "s61", "s62", "s63", "s64", "s65", "s66", "s67", "s68", "s69", "s70", "s71", "s72", "s73", "s74", "s75", "s76", "s77", "s78", "s79", "s80", "s81", "s82", "s83", "s84", "s85", "s86", "s87", "s88", "s89", "s90", "s91", "s92", "s93", "s94", "s95", "s96", "s97", "s98", "s99", "vcc", "scc", "memory");
}

extern "C" void kernel_launch(void* const* d_in, const int* in_sizes, int n_in,
                              void* d_out, int out_size, void* d_ws, size_t ws_size,
                              hipStream_t stream) {
  (void)hipFuncSetAttribute((const void*)qattn_kernel, hipFuncAttributeMaxDynamicSharedMemorySize, LDS_ATT);
  qattn_kernel<<<256, 256, LDS_ATT, stream>>>(d_in[0], d_in[1], d_in[2], d_in[3], d_ws, d_in[4], d_out);
}
```
